```python
import torch

def _softmax(x, axis=-1):
    return torch.softmax(x, dim=axis)
def _gelu(x):
    return torch.nn.functional.gelu(x, approximate="tanh")
def _silu(x):
    return torch.nn.functional.silu(x)
def _sigmoid(x):
    return torch.sigmoid(x)
def _layernorm(x, axis=-1, eps=1e-5):
    m = x.mean(dim=axis, keepdim=True)
    v = x.var(dim=axis, keepdim=True, unbiased=False)
    return (x - m) / torch.sqrt(v + eps)
def _scatter_add(tgt, idx, src, axis=0):
    return tgt.clone().index_add_(axis, idx, src)
def _segment_sum(src, seg, n):
    out = torch.zeros((n,) + src.shape[1:], dtype=src.dtype)
    return out.index_add_(0, seg, src)
def _pad(x, pw):
    flat = tuple(v for pair in reversed(pw) for v in pair)
    return torch.nn.functional.pad(x, flat)

kernel_name = 'parallel_sum__compressed_kv_cla__moe_arch_mod__prompt'

def setup_inputs():

    _w = (lambda a: (lambda t: t.to(torch.bfloat16) if t.is_floating_point() else t)(torch.as_tensor(a)))
    def _A_setup():
        B, H, Sq, Sk, D, M = 1, 32, 2048, 2048, 128, 640
        Dc = 4 * D
        return {
            "x": _w((lambda *s: torch.randn(*s, dtype=torch.bfloat16) * 0.05)(B, Sq, M)),
            "c_kv": _w((lambda *s: torch.randn(*s, dtype=torch.bfloat16) * 1.6)(B, Sk, Dc)),
            "w_q": _w((lambda *s: torch.randn(*s, dtype=torch.bfloat16) * 0.8)(M, H, D) * (M ** -0.5)),
            "w_uk": _w((lambda *s: torch.randn(*s, dtype=torch.bfloat16) * 0.8)(Dc, H, D) * (Dc ** -0.5)),
            "w_uv": _w((lambda *s: torch.randn(*s, dtype=torch.bfloat16) * 1.6)(Dc, H, D) * (Dc ** -0.5)),
            "w_o": _w((lambda *s: torch.randn(*s, dtype=torch.bfloat16) * 1.6)(H, D, M) * ((H * D) ** -0.5)),
        }
    _A_in = _A_setup()

    def _B_setup():
        B, S, D, F, C = 1, 2048, 640, 1728, 8
        return {
            "x": _w((lambda *s: torch.randn(*s, dtype=torch.bfloat16) * 0.05)(B, S, D)),
            "w_r": _w((lambda *s: torch.randn(*s, dtype=torch.bfloat16) * 0.05)(D)),
            "w_gate": _w((lambda *s: torch.randn(*s, dtype=torch.bfloat16) * 0.05)(D, F)),
            "w_up": _w((lambda *s: torch.randn(*s, dtype=torch.bfloat16) * 0.05)(D, F)),
            "w_down": _w((lambda *s: torch.randn(*s, dtype=torch.bfloat16) * 0.05)(F, D)),
        }
    _B_in = _B_setup()

    return {
        "A_x": _A_in["x"],
        "A_c_kv": _A_in["c_kv"],
        "A_w_q": _A_in["w_q"],
        "A_w_uk": _A_in["w_uk"],
        "A_w_uv": _A_in["w_uv"],
        "A_w_o": _A_in["w_o"],
        "B_x": _B_in["x"],
        "B_w_r": _B_in["w_r"],
        "B_w_gate": _B_in["w_gate"],
        "B_w_up": _B_in["w_up"],
        "B_w_down": _B_in["w_down"],
    }

def reference(A_x, A_c_kv, A_w_q, A_w_uk, A_w_uv, A_w_o, B_x, B_w_r, B_w_gate, B_w_up, B_w_down):
    def _A_ref(x, c_kv, w_q, w_uk, w_uv, w_o):
        B, Sq, M = x.shape[0], x.shape[1], x.shape[2]
        Sk = c_kv.shape[1]
        H, D = w_q.shape[1], w_q.shape[2]
        q = torch.einsum("bqm,mhd->bhqd", x, w_q)
        k = torch.einsum("bkc,chd->bhkd", c_kv, w_uk)
        v = torch.einsum("bkc,chd->bhkd", c_kv, w_uv)
        scores = torch.einsum("bhqd,bhkd->bhqk", q, k) * (D ** -0.5)
        off = Sk - Sq
        qi = torch.arange(Sq)[:, None]
        kj = torch.arange(Sk)[None, :]
        cmask = kj <= (qi + off)
        scores = torch.where(cmask[None, None], scores, -1e9)
        p = _softmax(scores, axis=-1)
        ctx = torch.einsum("bhqk,bhkd->bhqd", p, v)
        return torch.einsum("bhqd,hdm->bqm", ctx, w_o)

    def _B_ref(x, w_r, w_gate, w_up, w_down):
        B, S, D = x.shape
        C = 8
        r = x @ w_r
        idx = torch.argsort(-r, axis=-1)[:, :C]
        idx_e = (lambda x, n, axis: torch.repeat_interleave(x, n, dim=axis))(idx.reshape(B, C, 1), D, axis=2)
        x_sel = (lambda x, i, axis: torch.take_along_dim(x, i, dim=axis))(x, idx_e, axis=1)
        g = _silu(x_sel @ w_gate)
        u = x_sel @ w_up
        y = (g * u) @ w_down
        r_sel = _sigmoid((lambda x, i, axis: torch.take_along_dim(x, i, dim=axis))(r, idx, axis=1))
        y = y * r_sel.reshape(B, C, 1)
        b_off = (torch.arange(B) * S).reshape(B, 1)
        flat_idx = (idx + b_off).reshape(B * C)
        zero = (x * 0.0).reshape(B * S, D)
        upd = _scatter_add(zero, flat_idx, y.reshape(B * C, D), axis=0)
        return x + upd.reshape(B, S, D)

    x = A_x
    xn = _layernorm(x, axis=-1)
    return x + _A_ref(xn, A_c_kv, A_w_q, A_w_uk, A_w_uv, A_w_o) + _B_ref(xn, B_w_r, B_w_gate, B_w_up, B_w_down)
```

```cpp
#include <hip/hip_runtime.h>
#include <hip/hip_bf16.h>
typedef __hip_bfloat16 bf16;
constexpr int LDS_BYTES = 163840;
__global__ __launch_bounds__(256, 1) void mega_kernel(const bf16* x, const bf16* ckv, const bf16* wq, const bf16* wuk, const bf16* wuv, const bf16* wo,
    const bf16* w_r, const bf16* wg, const bf16* wu, const bf16* wd, char* ws, bf16* out) {
  asm volatile(
R"(.macro W n
s_waitcnt lgkmcnt(\n)
.endm
.macro D v,r,s,o
buffer_load_dwordx4 v\v, s[\r:\r+3], s\s offen offset:\o lds
.endm
.macro Ra d,a,o
ds_read_b128 a[\d:\d+3], v\a offset:\o
.endm
.macro Ta d,a,o
ds_read_b64_tr_b16 a[\d:\d+1], v\a offset:\o
.endm
.macro Zvaa d,a,b
v_mfma_f32_16x16x32_bf16 v[\d:\d+3], a[\a:\a+3], a[\b:\b+3], 0
.endm
.macro Zaaa d,a,b
v_mfma_f32_16x16x32_bf16 a[\d:\d+3], a[\a:\a+3], a[\b:\b+3], 0
.endm
.macro A d,a,b
v_add_f32 v\d, v\a, v\b
.endm
.macro U d,a,b
v_mul_f32 v\d, v\a, v\b
.endm
.macro Mvaa d,a,b
v_mfma_f32_16x16x32_bf16 v[\d:\d+3], a[\a:\a+3], a[\b:\b+3], v[\d:\d+3]
.endm
.macro Maaa d,a,b
v_mfma_f32_16x16x32_bf16 a[\d:\d+3], a[\a:\a+3], a[\b:\b+3], a[\d:\d+3]
.endm
.macro K d,a,b
v_cvt_pk_bf16_f32 v\d, v\a, v\b
.endm
.macro Rv d,a,o
ds_read_b128 v[\d:\d+3], v\a offset:\o
.endm
.macro Mvav d,a,b
v_mfma_f32_16x16x32_bf16 v[\d:\d+3], a[\a:\a+3], v[\b:\b+3], v[\d:\d+3]
.endm
.macro G d,a
v_accvgpr_read_b32 v\d, a\a
.endm
.macro Dc v,r,s,o
buffer_load_dwordx4 v\v, s[\r:\r+3], s\s offen offset:\o sc1 lds
.endm
.macro Cvaa d,a,b,c
v_mfma_f32_16x16x32_bf16 v[\d:\d+3], a[\a:\a+3], a[\b:\b+3], v[\c:\c+3]
.endm
.macro X d
v_exp_f32 v\d, v\d
.endm
.macro Zvav d,a,b
v_mfma_f32_16x16x32_bf16 v[\d:\d+3], a[\a:\a+3], v[\b:\b+3], 0
.endm
.macro Maav d,a,b
v_mfma_f32_16x16x32_bf16 a[\d:\d+3], a[\a:\a+3], v[\b:\b+3], a[\d:\d+3]
.endm
.macro Mvvv d,a,b
v_mfma_f32_16x16x32_bf16 v[\d:\d+3], v[\a:\a+3], v[\b:\b+3], v[\d:\d+3]
.endm
.macro Zvvv d,a,b
v_mfma_f32_16x16x32_bf16 v[\d:\d+3], v[\a:\a+3], v[\b:\b+3], 0
.endm
s_load_dwordx16 s[16:31], %[ka], 0
s_load_dwordx8 s[32:39], %[ka], 64
s_mov_b32 s15, %[bid]
v_lshrrev_b32 v2, 6, %[tid]
v_and_b32 v100, 63, %[tid]
v_readfirstlane_b32 s14, v2
v_lshlrev_b32 v1, 4, v100
v_lshrrev_b32 v101, 4, v100
v_and_b32 v102, 15, v100
s_and_b32 s59, s15, 7
s_bfe_u32 s57, s15, 0x30003
s_lshr_b32 s58, s15, 6
s_lshl_b32 s56, s59, 2
s_add_u32 s56, s56, s58
v_and_b32 v88, 0x1f0, v1
v_lshrrev_b32 v105, 1, v101
v_mul_u32_u24 v105, 1280, v105
v_add_u32 v28, v88, v105
s_lshl_b32 s85, s15, 2
s_add_u32 s85, s85, s14
s_mul_i32 s84, s85, 2560
v_and_b32 v105, 1, v101
v_lshl_or_b32 v29, v105, 31, v28
v_lshlrev_b32 v104, 1, v101
v_xor_b32 v104, v104, v102
v_lshlrev_b32 v104, 4, v104
v_lshl_add_u32 v10, v101, 13, v104
v_xor_b32 v105, 128, v10
v_add_u32 v11, 31744, v10
v_add_u32 v12, 63488, v105
v_add_u32 v13, 95232, v105
v_add_u32 v14, 131072, v10
v_add_u32 v15, 162816, v10
v_add_u32 v16, 194560, v105
v_add_u32 v17, 226304, v105
s_lshl_b32 s80, s14, 1
s_sub_u32 s81, 11, s80
s_cmp_lt_u32 s14, 2
s_cselect_b32 s80, s80, s81
s_lshl_b32 s80, s80, 3
s_add_u32 s80, s80, s57
s_lshl_b32 s80, s80, 15
s_xor_b32 s81, s80, 262144
v_and_b32 v104, 7, v100
v_xor_b32 v104, v104, v101
v_lshlrev_b32 v104, 4, v104
v_lshrrev_b32 v105, 3, v100
v_lshl_add_u32 v104, v105, 10, v104
v_add_u32 v2, s80, v104
v_xor_b32 v105, 64, v2
v_add_u32 v3, 7168, v105
v_add_u32 v4, 14336, v2
v_add_u32 v5, 21504, v105
v_xor_b32 v6, 262144, v2
v_xor_b32 v7, 262144, v3
v_xor_b32 v8, 262144, v4
v_xor_b32 v9, 262144, v5
s_lshl_b32 s63, s14, 13
s_add_u32 s61, s63, 65536
s_and_b32 s80, s14, 1
s_lshr_b32 s81, s14, 1
s_lshl_b32 s62, s80, 14
s_lshl_b32 s82, s81, 13
s_add_u32 s62, s62, s82
s_lshl_b32 s81, s81, 18
s_lshl_b32 s83, s56, 8
s_add_u32 s81, s81, s83
s_cmp_eq_u32 s80, 0
W 0
s_cselect_b32 s44, s22, s24
s_cselect_b32 s45, s23, s25
s_mov_b32 s40, s18
s_and_b32 s41, s19, 0xffff
s_mov_b32 s42, 0x200000
s_mov_b32 s43, 0x20000
s_add_u32 s44, s44, s81
s_addc_u32 s45, s45, 0
s_and_b32 s45, s45, 0xffff
s_mov_b32 s46, 0x400000
s_mov_b32 s47, 0x20000
s_sub_u32 s46, s46, s81
s_load_dwordx2 s[12:13], s[36:37], 0
s_mov_b32 s64, 0
s_add_u32 m0, s61, 0
s_nop 0
D 2,40,64,0
D 3,40,64,1024
D 4,40,64,2048
D 5,40,64,3072
s_add_u32 m0, s61, 4096
s_nop 0
D 6,40,64,0
D 7,40,64,1024
D 8,40,64,2048
D 9,40,64,3072
s_mov_b32 s65, 0
s_add_u32 m0, s62, 0
s_nop 0
D 10,44,65,0
D 11,44,65,1024
D 12,44,65,2048
D 13,44,65,3072
s_add_u32 m0, s62, 4096
s_nop 0
D 14,44,65,0
D 15,44,65,1024
D 16,44,65,2048
D 17,44,65,3072
s_mov_b32 s52, s16
s_and_b32 s53, s17, 0xffff
s_mov_b32 s54, 2621440
s_mov_b32 s55, 0x20000
buffer_load_dwordx4 v[32:35], v28, s[52:55], s84 offen
buffer_load_dwordx4 v[36:39], v28, s[52:55], s84 offen offset:512
buffer_load_dwordx4 v[40:43], v29, s[52:55], s84 offen offset:1024
s_mov_b32 s52, s28
s_and_b32 s53, s29, 0xffff
s_mov_b32 s54, 1280
s_mov_b32 s55, 0x20000
buffer_load_dwordx4 v[44:47], v88, s[52:55], 0 offen offset:0
buffer_load_dwordx4 v[48:51], v88, s[52:55], 0 offen offset:512
buffer_load_dwordx4 v[52:55], v88, s[52:55], 0 offen offset:1024
s_mov_b32 s69, 0
s_cmp_eq_u32 s14, 2
s_cbranch_scc1 .LL_1%=
s_cmp_eq_u32 s14, 3
s_cbranch_scc1 .LL_2%=
s_lshl_b32 s80, s58, 1
s_sub_u32 s81, 11, s80
s_cmp_lt_u32 s58, 2
s_cselect_b32 s80, s80, s81
s_lshl_b32 s80, s80, 3
s_add_u32 s80, s80, s57
s_lshl_b32 s80, s80, 15
s_xor_b32 s81, s80, 262144
s_lshl_b32 s81, s14, 7
s_add_u32 s80, s80, s81
s_add_u32 s80, s80, 256
v_lshl_add_u32 v104, v88, 6, s80
v_and_b32 v105, 512, v1
v_lshlrev_b32 v105, 9, v105
v_xor_b32 v104, v104, v105
v_mov_b32 v92, s18
v_mov_b32 v93, s19
s_mov_b32 s68, 256
s_branch .LL_3%=
.LL_1%=:
v_lshrrev_b32 v104, 6, v1
v_lshlrev_b32 v104, 13, v104
v_and_b32 v105, 16, v1
v_lshl_add_u32 v104, v105, 3, v104
s_lshl_b32 s80, s57, 17
s_add_u32 s80, s80, s83
s_add_u32 s80, s80, 1048576
v_add_u32 v104, s80, v104
v_and_b32 v105, 32, v1
v_cmp_eq_u32 vcc, 0, v105
v_mov_b32 v92, s24
v_mov_b32 v93, s25
v_mov_b32 v105, s22
v_mov_b32 v106, s23
v_cndmask_b32 v92, v92, v105, vcc
v_cndmask_b32 v93, v93, v106, vcc
s_mov_b32 s68, 1048576
s_branch .LL_3%=
.LL_2%=:
v_lshrrev_b32 v104, 5, v1
v_lshlrev_b32 v104, 13, v104
v_and_b32 v105, 16, v1
v_lshl_add_u32 v104, v105, 3, v104
s_mul_i32 s80, s57, 655360
s_add_u32 s80, s80, s83
v_add_u32 v104, s80, v104
v_mov_b32 v92, s20
v_mov_b32 v93, s21
s_mov_b32 s68, 196608
.LL_3%=:
v_mov_b32 v105, 0
v_lshl_add_u64 v[92:93], v[104:105], 0, v[92:93]
s_getpc_b64 s[6:7]
.Lbeg%=:
s_lshr_b32 s80, s15, 3
s_lshl_b32 s80, s80, 2
s_add_u32 s80, s80, s14
s_mul_i32 s80, s80, 1536
v_min_u32 v104, 176, v1
v_lshl_add_u32 v104, v104, 3, s80
v_min_u32 v104, .Lend%=-.Lbeg%=-4, v104
global_load_dword v94, v104, s[6:7]
s_mov_b32 s64, 128
s_add_u32 m0, s61, 32768
s_nop 0
D 2,40,64,0
D 3,40,64,1024
D 4,40,64,2048
D 5,40,64,3072
s_add_u32 m0, s61, 36864
s_nop 0
D 6,40,64,0
D 7,40,64,1024
D 8,40,64,2048
D 9,40,64,3072
s_mov_b32 s65, 524288
s_add_u32 m0, s62, 32768
s_nop 0
D 10,44,65,0
D 11,44,65,1024
D 12,44,65,2048
D 13,44,65,3072
s_add_u32 m0, s62, 36864
s_nop 0
D 14,44,65,0
D 15,44,65,1024
D 16,44,65,2048
D 17,44,65,3072
global_load_dword v94, v[92:93], off
v_lshl_add_u64 v[92:93], v[92:93], 0, s[68:69]
v_lshrrev_b32 v104, 1, v102
v_xor_b32 v104, v104, v101
v_lshlrev_b32 v104, 4, v104
v_lshl_add_u32 v104, v102, 7, v104
s_lshr_b32 s81, s14, 1
s_lshl_b32 s81, s81, 14
s_add_u32 s81, s81, 65536
v_add_u32 v18, s81, v104
v_xor_b32 v19, 64, v18
v_and_b32 v105, 1, v101
v_lshrrev_b32 v104, 2, v102
v_lshl_or_b32 v105, v105, 2, v104
v_lshl_add_u32 v104, v101, 3, v104
v_lshlrev_b32 v104, 8, v104
v_lshl_add_u32 v104, v105, 5, v104
v_and_b32 v105, 3, v102
v_lshl_add_u32 v20, v105, 3, v104
v_xor_b32 v21, 32, v20
v_xor_b32 v22, 64, v20
v_xor_b32 v23, 96, v20
v_xor_b32 v24, 128, v20
v_xor_b32 v25, 160, v20
v_xor_b32 v26, 192, v20
v_xor_b32 v27, 224, v20
s_mov_b32 s8, s36
s_and_b32 s9, s37, 0xffff
s_mov_b32 s10, 134217728
s_mov_b32 s11, 0x20000
s_lshl_b32 s81, s14, 18
s_add_u32 s81, s81, s83
s_add_u32 s48, s20, s81
s_addc_u32 s49, s21, 0
s_and_b32 s49, s49, 0xffff
s_mov_b32 s50, 5242880
s_mov_b32 s51, 0x20000
s_sub_u32 s50, s50, s81
s_lshl_b32 s80, s57, 1
s_and_b32 s81, s14, 1
s_add_u32 s80, s80, s81
s_sub_u32 s81, 31, s80
s_cmp_lt_u32 s14, 2
s_cselect_b32 s60, s80, s81
s_cselect_b32 s82, 0, 458752
s_cselect_b32 s70, 131072, 0
s_sub_u32 s70, s70, 69632
s_mul_i32 s66, s60, 81920
s_add_u32 s66, s66, 131072
s_and_b32 s80, s14, 1
s_mul_i32 s67, s80, 16910336
s_mul_i32 s80, s56, 528384
s_add_u32 s67, s67, s80
s_lshl_b32 s80, s57, 13
s_add_u32 s67, s67, s80
s_add_u32 s67, s67, s82
s_add_u32 s67, s67, 4194304
W 0
s_add_u32 s12, s12, 0x7f4a7c15
s_addc_u32 s13, s13, 0x9e3779b9
s_bitcmp1_b32 s14, 0
s_cbranch_scc1 .Lv_4%=
s_waitcnt vmcnt(24)
s_barrier
Ra 160,18,0
Ta 224,20,0
Ta 226,20,1024
Ra 164,18,2048
Ra 168,18,4096
Ra 172,18,6144
Ra 176,18,8192
Ra 180,18,10240
Ra 184,18,12288
Ra 188,18,14336
Ta 228,21,0
Ta 230,21,1024
W 6
Zvaa 160,224,160
Ta 232,22,0
Ta 234,22,1024
Zvaa 192,224,164
Ta 236,23,0
Ta 238,23,1024
Zvaa 224,224,168
s_waitcnt vmcnt(21)
Zaaa 0,224,172
v_lshlrev_b32 v56, 16, v32
W 6
Zaaa 32,224,176
v_and_b32 v57, 0xffff0000, v32
Zaaa 64,224,180
v_lshlrev_b32 v58, 16, v33
Zaaa 96,224,184
v_and_b32 v59, 0xffff0000, v33
Zaaa 128,224,188
v_lshlrev_b32 v60, 16, v34
W 4
Zaaa 132,228,188
v_and_b32 v61, 0xffff0000, v34
Ta 240,24,0
Zaaa 100,228,184
Ta 242,24,1024
v_lshlrev_b32 v62, 16, v35
Zaaa 68,228,180
v_and_b32 v63, 0xffff0000, v35
v_lshlrev_b32 v64, 16, v36
Zaaa 36,228,176
v_and_b32 v65, 0xffff0000, v36
Zaaa 4,228,172
v_lshlrev_b32 v66, 16, v37
Zvaa 228,228,168
v_and_b32 v67, 0xffff0000, v37
Zvaa 196,228,164
v_lshlrev_b32 v68, 16, v38
Zvaa 164,228,160
v_and_b32 v69, 0xffff0000, v38
W 4
Zvaa 168,232,160
v_lshlrev_b32 v70, 16, v39
v_and_b32 v71, 0xffff0000, v39
Zvaa 200,232,164
Ta 244,25,0
Ta 246,25,1024
Zvaa 232,232,168
v_lshlrev_b32 v72, 16, v40
v_and_b32 v73, 0xffff0000, v40
Zaaa 8,232,172
v_lshlrev_b32 v74, 16, v41
Zaaa 40,232,176
v_and_b32 v75, 0xffff0000, v41
Zaaa 72,232,180
v_lshlrev_b32 v76, 16, v42
v_and_b32 v77, 0xffff0000, v42
Zaaa 104,232,184
v_lshlrev_b32 v78, 16, v43
Zaaa 136,232,188
v_and_b32 v79, 0xffff0000, v43
W 4
Zaaa 140,236,188
A 80,56,57
Ta 248,26,0
Zaaa 108,236,184
Ta 250,26,1024
A 81,58,59
Zaaa 76,236,180
A 80,80,60
Zaaa 44,236,176
A 81,81,61
A 80,80,62
Zaaa 12,236,172
A 81,81,63
Ra 192,19,0
Zvaa 236,236,168
A 80,80,64
Zvaa 204,236,164
A 81,81,65
Zvaa 172,236,160
A 80,80,66
W 5
Zvaa 176,240,160
Ra 196,19,2048
A 81,81,67
Zvaa 208,240,164
Ta 252,27,0
)"
R"(Ta 254,27,1024
Zvaa 240,240,168
A 80,80,68
A 81,81,69
Zaaa 16,240,172
A 80,80,70
Ra 200,19,4096
Zaaa 48,240,176
A 81,81,71
A 80,80,72
Zaaa 80,240,180
A 81,81,73
Ra 204,19,6144
Zaaa 112,240,184
A 80,80,74
Zaaa 144,240,188
A 81,81,75
W 8
Zaaa 148,244,188
A 80,80,76
A 81,81,77
Zaaa 116,244,184
Ta 224,20,8192
Ta 226,20,9216
Zaaa 84,244,180
Ra 208,19,8192
A 80,80,78
Zaaa 52,244,176
A 81,81,79
A 80,80,81
Zaaa 20,244,172
Ra 212,19,10240
v_add_f32_dpp v80, v80, v80 quad_perm:[1,0,3,2] row_mask:0xf bank_mask:0xf
Zvaa 244,244,168
Zvaa 212,244,164
v_add_f32_dpp v80, v80, v80 quad_perm:[2,3,0,1] row_mask:0xf bank_mask:0xf
Ra 216,19,12288
Zvaa 180,244,160
v_add_f32_dpp v80, v80, v80 row_half_mirror row_mask:0xf bank_mask:0xf
W 11
Zvaa 184,248,160
v_add_f32_dpp v80, v80, v80 row_mirror row_mask:0xf bank_mask:0xf
v_mov_b32 v81, v80
Zvaa 216,248,164
Ta 228,21,8192
v_permlane16_swap_b32 v80, v81
Zvaa 248,248,168
Ta 230,21,9216
A 80,80,81
Zaaa 24,248,172
Ra 220,19,14336
v_mul_f32 v84, 0x3acccccd, v80
Zaaa 56,248,176
s_mov_b32 s86, 0xffff
s_mov_b32 s87, 0xffff
Zaaa 88,248,180
v_sub_f32 v82, v56, v84
U 80,82,82
Zaaa 120,248,184
v_sub_f32 v83, v57, v84
Zaaa 152,248,188
v_fmac_f32 v80, v83, v83
W 10
Zaaa 156,252,188
v_sub_f32 v82, v58, v84
Ta 232,22,8192
Zaaa 124,252,184
Ta 234,22,9216
v_fmac_f32 v80, v82, v82
Zaaa 92,252,180
v_sub_f32 v83, v59, v84
v_fmac_f32 v80, v83, v83
Zaaa 60,252,176
v_sub_f32 v82, v60, v84
Zaaa 28,252,172
v_fmac_f32 v80, v82, v82
Zvaa 252,252,168
v_sub_f32 v83, v61, v84
Zvaa 220,252,164
v_fmac_f32 v80, v83, v83
Zvaa 188,252,160
v_sub_f32 v82, v62, v84
W 8
Mvaa 160,224,192
v_fmac_f32 v80, v82, v82
v_sub_f32 v83, v63, v84
Mvaa 192,224,196
Ta 236,23,8192
Ta 238,23,9216
Mvaa 224,224,200
v_fmac_f32 v80, v83, v83
v_sub_f32 v82, v64, v84
Maaa 0,224,204
v_fmac_f32 v80, v82, v82
W 4
Maaa 32,224,208
v_sub_f32 v83, v65, v84
Maaa 64,224,212
v_fmac_f32 v80, v83, v83
Maaa 96,224,216
v_sub_f32 v82, v66, v84
Maaa 128,224,220
v_fmac_f32 v80, v82, v82
v_sub_f32 v83, v67, v84
Maaa 132,228,220
v_fmac_f32 v80, v83, v83
Ta 240,24,8192
Maaa 100,228,216
Ta 242,24,9216
v_sub_f32 v82, v68, v84
Maaa 68,228,212
v_fmac_f32 v80, v82, v82
Maaa 36,228,208
v_sub_f32 v83, v69, v84
Maaa 4,228,204
v_fmac_f32 v80, v83, v83
Ta 244,25,8192
Mvaa 228,228,200
Ta 246,25,9216
v_sub_f32 v82, v70, v84
Mvaa 196,228,196
v_fmac_f32 v80, v82, v82
v_sub_f32 v83, v71, v84
Mvaa 164,228,192
Ta 248,26,8192
W 7
Mvaa 168,232,192
Ta 250,26,9216
v_fmac_f32 v80, v83, v83
Mvaa 200,232,196
v_sub_f32 v82, v72, v84
Ta 252,27,8192
Mvaa 232,232,200
Ta 254,27,9216
U 81,82,82
Maaa 8,232,204
v_sub_f32 v83, v73, v84
v_fmac_f32 v81, v83, v83
Maaa 40,232,208
v_sub_f32 v82, v74, v84
v_fmac_f32 v81, v82, v82
Maaa 72,232,212
v_sub_f32 v83, v75, v84
Maaa 104,232,216
v_fmac_f32 v81, v83, v83
Maaa 136,232,220
v_sub_f32 v82, v76, v84
W 8
Maaa 140,236,220
v_fmac_f32 v81, v82, v82
Maaa 108,236,216
v_sub_f32 v83, v77, v84
v_fmac_f32 v81, v83, v83
Maaa 76,236,212
s_waitcnt vmcnt(1) lgkmcnt(0)
s_barrier
Maaa 44,236,208
v_sub_f32 v82, v78, v84
s_mov_b32 s64, 256
Maaa 12,236,204
v_fmac_f32 v81, v82, v82
v_sub_f32 v83, v79, v84
Mvaa 236,236,200
Ra 160,18,32768
v_fmac_f32 v81, v83, v83
Mvaa 204,236,196
s_add_u32 m0, s61, 0
v_cndmask_b32_e64 v81, 0, v81, s[86:87]
Mvaa 172,236,192
A 80,80,81
Ra 164,18,34816
Mvaa 176,240,192
v_add_f32_dpp v80, v80, v80 quad_perm:[1,0,3,2] row_mask:0xf bank_mask:0xf
D 2,40,64,0
Mvaa 208,240,196
v_add_f32_dpp v80, v80, v80 quad_perm:[2,3,0,1] row_mask:0xf bank_mask:0xf
Mvaa 240,240,200
Ra 168,18,36864
v_add_f32_dpp v80, v80, v80 row_half_mirror row_mask:0xf bank_mask:0xf
Maaa 16,240,204
Maaa 48,240,208
v_add_f32_dpp v80, v80, v80 row_mirror row_mask:0xf bank_mask:0xf
D 3,40,64,1024
Maaa 80,240,212
v_mov_b32 v81, v80
Ra 172,18,38912
Maaa 112,240,216
v_permlane16_swap_b32 v80, v81
A 80,80,81
Maaa 144,240,220
v_mul_f32 v80, 0x3acccccd, v80
K 80,80,80
Maaa 148,244,220
D 4,40,64,2048
v_lshlrev_b32 v80, 16, v80
Maaa 116,244,216
v_add_f32 v80, 0x3727c5ac, v80
Ta 224,20,32768
Maaa 84,244,212
Ta 226,20,33792
Ra 176,18,40960
Maaa 52,244,208
K 80,80,80
v_lshlrev_b32 v80, 16, v80
Maaa 20,244,204
D 5,40,64,3072
s_add_u32 m0, s61, 4096
Mvaa 244,244,200
v_sqrt_f32 v81, v80
Mvaa 212,244,196
Ra 180,18,43008
Ra 184,18,45056
Mvaa 180,244,192
v_rcp_f32 v82, v81
Mvaa 184,248,192
D 6,40,64,0
v_fma_f32 v83, -v81, v81, v80
Mvaa 216,248,196
v_mul_f32 v82, 0.5, v82
v_fma_f32 v81, v83, v82, v81
Mvaa 248,248,200
Ta 228,21,32768
Ta 230,21,33792
Maaa 24,248,204
K 86,81,81
Ra 188,18,47104
Maaa 56,248,208
D 7,40,64,1024
v_lshlrev_b32 v86, 16, v86
Maaa 88,248,212
v_rcp_f32 v87, v86
Maaa 120,248,216
K 85,84,84
v_lshlrev_b32 v85, 16, v85
Maaa 152,248,220
v_sub_f32 v56, v56, v85
Maaa 156,252,220
D 8,40,64,2048
v_sub_f32 v57, v57, v85
Maaa 124,252,216
K 80,56,57
Ta 232,22,32768
Maaa 92,252,212
Ta 234,22,33792
v_lshlrev_b32 v56, 16, v80
Maaa 60,252,208
v_and_b32 v57, 0xffff0000, v80
U 80,56,87
Maaa 28,252,204
D 9,40,64,3072
s_mov_b32 s65, 1048576
Mvaa 252,252,200
s_add_u32 m0, s62, 0
U 81,57,87
Mvaa 220,252,196
v_fma_f32 v82, -v80, v86, v56
v_fma_f32 v83, -v81, v86, v57
Mvaa 188,252,192
v_fmac_f32 v80, v82, v87
W 8
Mvaa 160,224,160
D 10,44,65,0
v_fmac_f32 v81, v83, v87
Mvaa 192,224,164
K 32,80,81
Ta 236,23,32768
Mvaa 224,224,168
Ta 238,23,33792
v_lshlrev_b32 v80, 16, v32
Maaa 0,224,172
v_and_b32 v81, 0xffff0000, v32
W 4
Maaa 32,224,176
D 11,44,65,1024
v_lshlrev_b32 v82, 16, v44
Maaa 64,224,180
v_and_b32 v83, 0xffff0000, v44
U 84,80,82
Maaa 96,224,184
v_fmac_f32 v84, v81, v83
v_sub_f32 v58, v58, v85
Maaa 128,224,188
v_sub_f32 v59, v59, v85
Maaa 132,228,188
D 12,44,65,2048
K 80,58,59
Maaa 100,228,184
Ta 240,24,32768
Ta 242,24,33792
Maaa 68,228,180
v_lshlrev_b32 v58, 16, v80
v_and_b32 v59, 0xffff0000, v80
Maaa 36,228,176
U 80,58,87
U 81,59,87
Maaa 4,228,172
D 13,44,65,3072
s_add_u32 m0, s62, 4096
Mvaa 228,228,168
v_fma_f32 v82, -v80, v86, v58
v_fma_f32 v83, -v81, v86, v59
Mvaa 196,228,164
v_fmac_f32 v80, v82, v87
Mvaa 164,228,160
v_fmac_f32 v81, v83, v87
W 4
Mvaa 168,232,160
D 14,44,65,0
K 33,80,81
Mvaa 200,232,164
Ta 244,25,32768
Ta 246,25,33792
Mvaa 232,232,168
v_lshlrev_b32 v80, 16, v33
v_and_b32 v81, 0xffff0000, v33
Maaa 8,232,172
v_lshlrev_b32 v82, 16, v45
v_and_b32 v83, 0xffff0000, v45
Maaa 40,232,176
D 15,44,65,1024
v_fmac_f32 v84, v80, v82
Maaa 72,232,180
v_fmac_f32 v84, v81, v83
Maaa 104,232,184
v_sub_f32 v60, v60, v85
Maaa 136,232,188
v_sub_f32 v61, v61, v85
W 4
Maaa 140,236,188
D 16,44,65,2048
K 80,60,61
Maaa 108,236,184
v_lshlrev_b32 v60, 16, v80
Ta 248,26,32768
Maaa 76,236,180
Ta 250,26,33792
v_and_b32 v61, 0xffff0000, v80
Maaa 44,236,176
U 80,60,87
U 81,61,87
Maaa 12,236,172
D 17,44,65,3072
v_fma_f32 v82, -v80, v86, v60
Mvaa 236,236,168
Ra 192,19,32768
v_fma_f32 v83, -v81, v86, v61
Mvaa 204,236,164
v_fmac_f32 v80, v82, v87
v_fmac_f32 v81, v83, v87
Mvaa 172,236,160
K 34,80,81
W 5
Mvaa 176,240,160
global_load_dword v94, v[92:93], off
v_lshl_add_u64 v[92:93], v[92:93], 0, s[68:69]
Mvaa 208,240,164
Ra 196,19,34816
v_lshlrev_b32 v80, 16, v34
Mvaa 240,240,168
Ta 252,27,32768
Ta 254,27,33792
Maaa 16,240,172
v_and_b32 v81, 0xffff0000, v34
v_lshlrev_b32 v82, 16, v46
Maaa 48,240,176
Ra 200,19,36864
v_and_b32 v83, 0xffff0000, v46
Maaa 80,240,180
v_fmac_f32 v84, v80, v82
v_fmac_f32 v84, v81, v83
Maaa 112,240,184
v_sub_f32 v62, v62, v85
Ra 204,19,38912
Maaa 144,240,188
v_sub_f32 v63, v63, v85
W 8
Maaa 148,244,188
K 80,62,63
v_lshlrev_b32 v62, 16, v80
Maaa 116,244,184
Ta 224,20,40960
Ta 226,20,41984
Maaa 84,244,180
Ra 208,19,40960
v_and_b32 v63, 0xffff0000, v80
Maaa 52,244,176
U 80,62,87
U 81,63,87
Maaa 20,244,172
v_fma_f32 v82, -v80, v86, v62
Ra 212,19,43008
Mvaa 244,244,168
v_fma_f32 v83, -v81, v86, v63
v_fmac_f32 v80, v82, v87
Mvaa 212,244,164
v_fmac_f32 v81, v83, v87
Ra 216,19,45056
Mvaa 180,244,160
K 35,80,81
W 11
)"
R"(Mvaa 184,248,160
v_lshlrev_b32 v80, 16, v35
v_and_b32 v81, 0xffff0000, v35
Mvaa 216,248,164
Ta 228,21,40960
Ta 230,21,41984
Mvaa 248,248,168
v_lshlrev_b32 v82, 16, v47
Ra 220,19,47104
Maaa 24,248,172
v_and_b32 v83, 0xffff0000, v47
v_fmac_f32 v84, v80, v82
Maaa 56,248,176
v_fmac_f32 v84, v81, v83
Maaa 88,248,180
v_sub_f32 v64, v64, v85
Maaa 120,248,184
v_sub_f32 v65, v65, v85
K 80,64,65
Maaa 152,248,188
v_lshlrev_b32 v64, 16, v80
W 10
Maaa 156,252,188
v_and_b32 v65, 0xffff0000, v80
Ta 232,22,40960
Maaa 124,252,184
Ta 234,22,41984
U 80,64,87
Maaa 92,252,180
U 81,65,87
Maaa 60,252,176
v_fma_f32 v82, -v80, v86, v64
Maaa 28,252,172
v_fma_f32 v83, -v81, v86, v65
v_fmac_f32 v80, v82, v87
Mvaa 252,252,168
v_fmac_f32 v81, v83, v87
Mvaa 220,252,164
K 36,80,81
Mvaa 188,252,160
v_lshlrev_b32 v80, 16, v36
W 8
Mvaa 160,224,192
v_and_b32 v81, 0xffff0000, v36
Ta 236,23,40960
Mvaa 192,224,196
Ta 238,23,41984
v_lshlrev_b32 v82, 16, v48
Mvaa 224,224,200
v_and_b32 v83, 0xffff0000, v48
v_fmac_f32 v84, v80, v82
Maaa 0,224,204
v_fmac_f32 v84, v81, v83
W 4
Maaa 32,224,208
v_sub_f32 v66, v66, v85
Maaa 64,224,212
v_sub_f32 v67, v67, v85
Maaa 96,224,216
K 80,66,67
Maaa 128,224,220
v_lshlrev_b32 v66, 16, v80
Maaa 132,228,220
v_and_b32 v67, 0xffff0000, v80
Ta 240,24,40960
Maaa 100,228,216
Ta 242,24,41984
U 80,66,87
Maaa 68,228,212
U 81,67,87
v_fma_f32 v82, -v80, v86, v66
Maaa 36,228,208
v_fma_f32 v83, -v81, v86, v67
Maaa 4,228,204
v_fmac_f32 v80, v82, v87
Ta 244,25,40960
Mvaa 228,228,200
Ta 246,25,41984
v_fmac_f32 v81, v83, v87
Mvaa 196,228,196
K 37,80,81
Ta 248,26,40960
Mvaa 164,228,192
Ta 250,26,41984
W 8
Mvaa 168,232,192
v_lshlrev_b32 v80, 16, v37
v_and_b32 v81, 0xffff0000, v37
Mvaa 200,232,196
v_lshlrev_b32 v82, 16, v49
Ta 252,27,40960
Mvaa 232,232,200
Ta 254,27,41984
v_and_b32 v83, 0xffff0000, v49
Maaa 8,232,204
v_fmac_f32 v84, v80, v82
v_fmac_f32 v84, v81, v83
Maaa 40,232,208
v_sub_f32 v68, v68, v85
Maaa 72,232,212
v_sub_f32 v69, v69, v85
K 80,68,69
Maaa 104,232,216
v_lshlrev_b32 v68, 16, v80
Maaa 136,232,220
v_and_b32 v69, 0xffff0000, v80
W 8
Maaa 140,236,220
U 80,68,87
Maaa 108,236,216
U 81,69,87
Maaa 76,236,212
s_waitcnt vmcnt(1) lgkmcnt(0)
s_barrier
Maaa 44,236,208
v_fma_f32 v82, -v80, v86, v68
s_mov_b32 s64, 384
Maaa 12,236,204
v_fma_f32 v83, -v81, v86, v69
v_fmac_f32 v80, v82, v87
Mvaa 236,236,200
v_fmac_f32 v81, v83, v87
Ra 160,18,0
Mvaa 204,236,196
K 38,80,81
s_add_u32 m0, s61, 32768
Mvaa 172,236,192
v_lshlrev_b32 v80, 16, v38
v_and_b32 v81, 0xffff0000, v38
Mvaa 176,240,192
Ra 164,18,2048
v_lshlrev_b32 v82, 16, v50
Mvaa 208,240,196
D 2,40,64,0
v_and_b32 v83, 0xffff0000, v50
Mvaa 240,240,200
v_fmac_f32 v84, v80, v82
v_fmac_f32 v84, v81, v83
Maaa 16,240,204
Ra 168,18,4096
v_sub_f32 v70, v70, v85
Maaa 48,240,208
v_sub_f32 v71, v71, v85
Maaa 80,240,212
D 3,40,64,1024
K 80,70,71
Maaa 112,240,216
Ra 172,18,6144
v_lshlrev_b32 v70, 16, v80
Maaa 144,240,220
v_and_b32 v71, 0xffff0000, v80
U 80,70,87
Maaa 148,244,220
U 81,71,87
Ta 224,20,0
Maaa 116,244,216
D 4,40,64,2048
Ta 226,20,1024
Maaa 84,244,212
Ra 176,18,8192
v_fma_f32 v82, -v80, v86, v70
Maaa 52,244,208
v_fma_f32 v83, -v81, v86, v71
v_fmac_f32 v80, v82, v87
Maaa 20,244,204
Ra 180,18,10240
v_fmac_f32 v81, v83, v87
Mvaa 244,244,200
D 5,40,64,3072
s_add_u32 m0, s61, 36864
Mvaa 212,244,196
K 39,80,81
v_lshlrev_b32 v80, 16, v39
Mvaa 180,244,192
v_and_b32 v81, 0xffff0000, v39
Ra 184,18,12288
Mvaa 184,248,192
v_lshlrev_b32 v82, 16, v51
v_and_b32 v83, 0xffff0000, v51
Mvaa 216,248,196
D 6,40,64,0
Ta 228,21,0
Mvaa 248,248,200
Ta 230,21,1024
v_fmac_f32 v84, v80, v82
Maaa 24,248,204
Ra 188,18,14336
v_fmac_f32 v84, v81, v83
Maaa 56,248,208
v_sub_f32 v72, v72, v85
v_sub_f32 v73, v73, v85
Maaa 88,248,212
D 7,40,64,1024
K 80,72,73
Maaa 120,248,216
v_lshlrev_b32 v72, 16, v80
v_and_b32 v73, 0xffff0000, v80
Maaa 152,248,220
U 80,72,87
Maaa 156,252,220
U 81,73,87
Ta 232,22,0
Maaa 124,252,216
D 8,40,64,2048
Ta 234,22,1024
Maaa 92,252,212
v_fma_f32 v82, -v80, v86, v72
v_fma_f32 v83, -v81, v86, v73
Maaa 60,252,208
v_fmac_f32 v80, v82, v87
v_fmac_f32 v81, v83, v87
Maaa 28,252,204
K 40,80,81
Mvaa 252,252,200
D 9,40,64,3072
s_mov_b32 s65, 1572864
Mvaa 220,252,196
v_lshlrev_b32 v80, 16, v40
s_add_u32 m0, s62, 32768
Mvaa 188,252,192
v_and_b32 v81, 0xffff0000, v40
W 8
Mvaa 160,224,160
v_lshlrev_b32 v82, 16, v52
v_and_b32 v83, 0xffff0000, v52
Mvaa 192,224,164
D 10,44,65,0
v_fmac_f32 v84, v80, v82
Mvaa 224,224,168
Ta 236,23,0
Ta 238,23,1024
Maaa 0,224,172
v_fmac_f32 v84, v81, v83
W 4
Maaa 32,224,176
v_sub_f32 v74, v74, v85
v_sub_f32 v75, v75, v85
Maaa 64,224,180
D 11,44,65,1024
K 80,74,75
Maaa 96,224,184
v_lshlrev_b32 v74, 16, v80
v_and_b32 v75, 0xffff0000, v80
Maaa 128,224,188
U 80,74,87
U 81,75,87
Maaa 132,228,188
v_fma_f32 v82, -v80, v86, v74
Ta 240,24,0
Maaa 100,228,184
D 12,44,65,2048
Ta 242,24,1024
Maaa 68,228,180
v_fma_f32 v83, -v81, v86, v75
v_fmac_f32 v80, v82, v87
Maaa 36,228,176
v_fmac_f32 v81, v83, v87
Maaa 4,228,172
K 41,80,81
Mvaa 228,228,168
D 13,44,65,3072
s_add_u32 m0, s62, 36864
Mvaa 196,228,164
v_lshlrev_b32 v80, 16, v41
v_and_b32 v81, 0xffff0000, v41
Mvaa 164,228,160
v_lshlrev_b32 v82, 16, v53
W 4
Mvaa 168,232,160
v_and_b32 v83, 0xffff0000, v53
v_fmac_f32 v84, v80, v82
Mvaa 200,232,164
D 14,44,65,0
Ta 244,25,0
Mvaa 232,232,168
Ta 246,25,1024
v_fmac_f32 v84, v81, v83
Maaa 8,232,172
v_sub_f32 v76, v76, v85
v_sub_f32 v77, v77, v85
Maaa 40,232,176
K 80,76,77
v_lshlrev_b32 v76, 16, v80
Maaa 72,232,180
D 15,44,65,1024
v_and_b32 v77, 0xffff0000, v80
Maaa 104,232,184
U 80,76,87
Maaa 136,232,188
U 81,77,87
W 4
Maaa 140,236,188
v_fma_f32 v82, -v80, v86, v76
Ta 248,26,0
Maaa 108,236,184
D 16,44,65,2048
Ta 250,26,1024
Maaa 76,236,180
v_fma_f32 v83, -v81, v86, v77
v_fmac_f32 v80, v82, v87
Maaa 44,236,176
v_fmac_f32 v81, v83, v87
K 42,80,81
Maaa 12,236,172
v_lshlrev_b32 v80, 16, v42
Ra 192,19,0
Mvaa 236,236,168
D 17,44,65,3072
v_and_b32 v81, 0xffff0000, v42
Mvaa 204,236,164
v_lshlrev_b32 v82, 16, v54
Mvaa 172,236,160
v_and_b32 v83, 0xffff0000, v54
W 5
Mvaa 176,240,160
v_fmac_f32 v84, v80, v82
Ra 196,19,2048
Mvaa 208,240,164
v_fmac_f32 v84, v81, v83
Ta 252,27,0
Mvaa 240,240,168
Ta 254,27,1024
v_sub_f32 v78, v78, v85
Maaa 16,240,172
v_sub_f32 v79, v79, v85
Ra 200,19,4096
Maaa 48,240,176
K 80,78,79
v_lshlrev_b32 v78, 16, v80
Maaa 80,240,180
v_and_b32 v79, 0xffff0000, v80
U 80,78,87
Maaa 112,240,184
Ra 204,19,6144
U 81,79,87
Maaa 144,240,188
v_fma_f32 v82, -v80, v86, v78
W 8
Maaa 148,244,188
v_fma_f32 v83, -v81, v86, v79
Ta 224,20,8192
Maaa 116,244,184
Ta 226,20,9216
Ra 208,19,8192
Maaa 84,244,180
v_fmac_f32 v80, v82, v87
v_fmac_f32 v81, v83, v87
Maaa 52,244,176
K 43,80,81
Ra 212,19,10240
Maaa 20,244,172
v_lshlrev_b32 v80, 16, v43
v_and_b32 v81, 0xffff0000, v43
Mvaa 244,244,168
v_lshlrev_b32 v82, 16, v55
Mvaa 212,244,164
v_and_b32 v83, 0xffff0000, v55
Ra 216,19,12288
Mvaa 180,244,160
v_fmac_f32 v84, v80, v82
W 11
Mvaa 184,248,160
v_fmac_f32 v84, v81, v83
Ta 228,21,8192
Mvaa 216,248,164
Ta 230,21,9216
v_lshrrev_b32 v82, 9, v1
Mvaa 248,248,168
Ra 220,19,14336
s_lshl_b32 s80, s14, 1
Maaa 24,248,172
v_add_u32 v82, s80, v82
v_mul_u32_u24 v83, 1552, v82
Maaa 56,248,176
v_add_u32 v83, v83, v88
Maaa 88,248,180
v_add_u32 v83, 131072, v83
Maaa 120,248,184
ds_write_b128 v83, v[32:35] offset:0
Maaa 152,248,188
ds_write_b128 v83, v[36:39] offset:512
W 12
Maaa 156,252,188
ds_write_b128 v83, v[40:43] offset:1024
v_add_f32_dpp v84, v84, v84 quad_perm:[1,0,3,2] row_mask:0xf bank_mask:0xf
Maaa 124,252,184
Ta 232,22,8192
Ta 234,22,9216
Maaa 92,252,180
v_add_f32_dpp v84, v84, v84 quad_perm:[2,3,0,1] row_mask:0xf bank_mask:0xf
Maaa 60,252,176
Maaa 28,252,172
v_add_f32_dpp v84, v84, v84 row_half_mirror row_mask:0xf bank_mask:0xf
Mvaa 252,252,168
Mvaa 220,252,164
v_add_f32_dpp v84, v84, v84 row_mirror row_mask:0xf bank_mask:0xf
v_mov_b32 v81, v84
Mvaa 188,252,160
W 11
Mvaa 160,224,192
)"
R"(v_permlane16_swap_b32 v84, v81
A 84,84,81
Mvaa 192,224,196
K 80,84,84
v_lshlrev_b32 v80, 16, v80
Mvaa 224,224,200
s_lshl_b32 s80, s15, 3
Ta 236,23,8192
Maaa 0,224,204
Ta 238,23,9216
W 7
Maaa 32,224,208
v_add_u32 v82, s80, v82
v_sub_u32 v83, 2047, v82
Maaa 64,224,212
v_ashrrev_i32 v81, 31, v80
v_or_b32 v81, 0x80000000, v81
Maaa 96,224,216
v_xor_b32 v80, v80, v81
v_and_b32 v80, 0xffff0000, v80
Maaa 128,224,220
v_or_b32 v80, v80, v83
v_lshlrev_b32 v81, 23, v88
Maaa 132,228,220
v_lshl_or_b32 v82, v82, 2, v81
Ta 240,24,8192
Maaa 100,228,216
Ta 242,24,9216
s_mov_b32 s82, 2752512
Maaa 68,228,212
buffer_store_dword v80, v82, s[8:11], s82 offen sc1
Maaa 36,228,208
Maaa 4,228,204
Ta 244,25,8192
Ta 246,25,9216
Mvaa 228,228,200
Mvaa 196,228,196
Ta 248,26,8192
Ta 250,26,9216
Mvaa 164,228,192
W 8
Mvaa 168,232,192
Ta 252,27,8192
Ta 254,27,9216
Mvaa 200,232,196
Mvaa 232,232,200
Maaa 8,232,204
Maaa 40,232,208
Maaa 72,232,212
Maaa 104,232,216
Maaa 136,232,220
W 8
Maaa 140,236,220
Maaa 108,236,216
Maaa 76,236,212
s_waitcnt vmcnt(1) lgkmcnt(0)
s_barrier
Maaa 44,236,208
s_mov_b32 s64, 512
v_bfe_u32 v80, v1, 4, 3
Maaa 12,236,204
v_mul_u32_u24 v81, 1552, v80
Ra 160,18,32768
Mvaa 236,236,200
v_lshrrev_b32 v82, 7, v1
s_add_u32 m0, s61, 0
Mvaa 204,236,196
v_lshl_add_u32 v81, v82, 4, v81
Mvaa 172,236,192
s_lshl_b32 s80, s14, 7
s_add_u32 s80, s80, 131072
Mvaa 176,240,192
Ra 164,18,34816
D 2,40,64,0
Mvaa 208,240,196
v_add_u32 v81, s80, v81
ds_read_b128 v[32:35], v81
Mvaa 240,240,200
Rv 36,81,512
Ra 168,18,36864
Maaa 16,240,204
s_and_b32 s80, s14, 2
s_lshl_b32 s80, s80, 7
Maaa 48,240,208
D 3,40,64,1024
v_subrev_u32 v81, s80, v81
Maaa 80,240,212
Rv 40,81,1024
Ra 172,18,38912
Maaa 112,240,216
v_lshlrev_b32 v80, 4, v80
v_and_b32 v81, 3, v82
Maaa 144,240,220
v_lshl_add_u32 v80, v81, 8, v80
Maaa 148,244,220
D 4,40,64,2048
v_lshrrev_b32 v82, 2, v82
Maaa 116,244,216
Ta 224,20,32768
Ta 226,20,33792
Maaa 84,244,212
Ra 176,18,40960
v_lshl_add_u32 v80, v82, 12, v80
Maaa 52,244,208
s_lshr_b32 s82, s15, 3
s_mul_i32 s82, s82, 81920
Maaa 20,244,204
D 5,40,64,3072
s_add_u32 m0, s61, 4096
Mvaa 244,244,200
s_bfe_u32 s80, s15, 0x20001
Ra 180,18,43008
Mvaa 212,244,196
s_lshl_b32 s80, s80, 10
s_add_u32 s82, s82, s80
Mvaa 180,244,192
s_and_b32 s80, s15, 1
s_lshl_b32 s80, s80, 7
Mvaa 184,248,192
D 6,40,64,0
Ra 184,18,45056
Mvaa 216,248,196
s_add_u32 s82, s82, s80
s_add_u32 s82, s82, 131072
Mvaa 248,248,200
Ta 228,21,32768
Ta 230,21,33792
Maaa 24,248,204
s_lshl_b32 s80, s14, 13
s_add_u32 s83, s82, s80
Maaa 56,248,208
D 7,40,64,1024
Ra 188,18,47104
Maaa 88,248,212
W 9
buffer_store_dwordx4 v[32:35], v80, s[8:11], s83 offen sc1
Maaa 120,248,216
s_add_u32 s83, s83, 32768
Maaa 152,248,220
Maaa 156,252,220
D 8,40,64,2048
Ta 232,22,32768
Maaa 124,252,216
Ta 234,22,33792
Maaa 92,252,212
Maaa 60,252,208
Maaa 28,252,204
buffer_store_dwordx4 v[36:39], v80, s[8:11], s83 offen sc1
s_and_b32 s80, s80, 8192
Mvaa 252,252,200
s_add_u32 s83, s82, s80
s_add_u32 s83, s83, 65536
Mvaa 220,252,196
Mvaa 188,252,192
W 8
Mvaa 160,224,160
D 9,40,64,3072
s_mov_b32 s65, 2097152
Mvaa 192,224,164
s_add_u32 m0, s62, 0
Ta 236,23,32768
Mvaa 224,224,168
Ta 238,23,33792
Maaa 0,224,172
W 4
Maaa 32,224,176
buffer_store_dwordx4 v[40:43], v80, s[8:11], s83 offen sc1
Maaa 64,224,180
Maaa 96,224,184
Maaa 128,224,188
Maaa 132,228,188
D 10,44,65,0
Ta 240,24,32768
Maaa 100,228,184
Ta 242,24,33792
Maaa 68,228,180
Maaa 36,228,176
Maaa 4,228,172
D 11,44,65,1024
Mvaa 228,228,168
Mvaa 196,228,164
Mvaa 164,228,160
W 4
Mvaa 168,232,160
D 12,44,65,2048
Ta 244,25,32768
Mvaa 200,232,164
Ta 246,25,33792
Mvaa 232,232,168
Maaa 8,232,172
Maaa 40,232,176
D 13,44,65,3072
s_add_u32 m0, s62, 4096
Maaa 72,232,180
Maaa 104,232,184
Maaa 136,232,188
W 4
Maaa 140,236,188
D 14,44,65,0
Ta 248,26,32768
Maaa 108,236,184
Ta 250,26,33792
Maaa 76,236,180
Maaa 44,236,176
Maaa 12,236,172
D 15,44,65,1024
Ra 192,19,32768
Mvaa 236,236,168
Mvaa 204,236,164
Mvaa 172,236,160
Ra 196,19,34816
W 6
Mvaa 176,240,160
D 16,44,65,2048
Ta 252,27,32768
Mvaa 208,240,164
Ta 254,27,33792
Mvaa 240,240,168
Ra 200,19,36864
Maaa 16,240,172
Maaa 48,240,176
D 17,44,65,3072
Maaa 80,240,180
Ra 204,19,38912
Maaa 112,240,184
Maaa 144,240,188
W 8
Maaa 148,244,188
global_load_dword v94, v[92:93], off
v_lshl_add_u64 v[92:93], v[92:93], 0, s[68:69]
Maaa 116,244,184
Ta 224,20,40960
Ta 226,20,41984
Maaa 84,244,180
Ra 208,19,40960
Maaa 52,244,176
Ra 212,19,43008
Maaa 20,244,172
Mvaa 244,244,168
Mvaa 212,244,164
Ra 216,19,45056
Mvaa 180,244,160
W 11
Mvaa 184,248,160
Ta 228,21,40960
Ta 230,21,41984
Mvaa 216,248,164
Ra 220,19,47104
Mvaa 248,248,168
Maaa 24,248,172
Maaa 56,248,176
Maaa 88,248,180
Maaa 120,248,184
Maaa 152,248,188
W 10
Maaa 156,252,188
Ta 232,22,40960
Ta 234,22,41984
Maaa 124,252,184
Maaa 92,252,180
Maaa 60,252,176
Maaa 28,252,172
Mvaa 252,252,168
Mvaa 220,252,164
Mvaa 188,252,160
W 8
Mvaa 160,224,192
Ta 236,23,40960
Ta 238,23,41984
Mvaa 192,224,196
Mvaa 224,224,200
Maaa 0,224,204
W 4
Maaa 32,224,208
Maaa 64,224,212
Maaa 96,224,216
Maaa 128,224,220
Maaa 132,228,220
Ta 240,24,40960
Ta 242,24,41984
Maaa 100,228,216
Maaa 68,228,212
Maaa 36,228,208
Maaa 4,228,204
Ta 244,25,40960
Ta 246,25,41984
Mvaa 228,228,200
Mvaa 196,228,196
Ta 248,26,40960
Ta 250,26,41984
Mvaa 164,228,192
W 8
Mvaa 168,232,192
Ta 252,27,40960
Ta 254,27,41984
Mvaa 200,232,196
Mvaa 232,232,200
Maaa 8,232,204
Maaa 40,232,208
Maaa 72,232,212
Maaa 104,232,216
Maaa 136,232,220
W 8
Maaa 140,236,220
Maaa 108,236,216
Maaa 76,236,212
s_waitcnt vmcnt(1) lgkmcnt(0)
s_barrier
s_cmp_eq_u32 s14, 0
s_cbranch_scc1 .Lpub_6%=
.Lpubr_7%=:
Maaa 44,236,208
s_mov_b32 s64, 640
Maaa 12,236,204
Ra 160,18,0
Mvaa 236,236,200
s_add_u32 m0, s61, 32768
Mvaa 204,236,196
Mvaa 172,236,192
Ra 164,18,2048
Mvaa 176,240,192
D 2,40,64,0
Mvaa 208,240,196
Mvaa 240,240,200
Ra 168,18,4096
Maaa 16,240,204
Maaa 48,240,208
D 3,40,64,1024
Maaa 80,240,212
Ra 172,18,6144
Maaa 112,240,216
Maaa 144,240,220
Maaa 148,244,220
D 4,40,64,2048
Ta 224,20,0
Maaa 116,244,216
Ta 226,20,1024
Ra 176,18,8192
Maaa 84,244,212
Maaa 52,244,208
Ra 180,18,10240
Maaa 20,244,204
D 5,40,64,3072
s_add_u32 m0, s61, 36864
Mvaa 244,244,200
Mvaa 212,244,196
Ra 184,18,12288
Mvaa 180,244,192
Mvaa 184,248,192
D 6,40,64,0
Ta 228,21,0
Mvaa 216,248,196
Ta 230,21,1024
Ra 188,18,14336
Mvaa 248,248,200
Maaa 24,248,204
Maaa 56,248,208
D 7,40,64,1024
Maaa 88,248,212
Maaa 120,248,216
Maaa 152,248,220
Maaa 156,252,220
D 8,40,64,2048
Ta 232,22,0
Maaa 124,252,216
Ta 234,22,1024
Maaa 92,252,212
Maaa 60,252,208
Maaa 28,252,204
D 9,40,64,3072
s_mov_b32 s65, 2621440
Mvaa 252,252,200
s_add_u32 m0, s62, 32768
Mvaa 220,252,196
Mvaa 188,252,192
W 8
Mvaa 160,224,160
D 10,44,65,0
Ta 236,23,0
Mvaa 192,224,164
Ta 238,23,1024
Mvaa 224,224,168
Maaa 0,224,172
W 4
Maaa 32,224,176
D 11,44,65,1024
Maaa 64,224,180
Maaa 96,224,184
Maaa 128,224,188
Maaa 132,228,188
D 12,44,65,2048
Ta 240,24,0
Maaa 100,228,184
Ta 242,24,1024
Maaa 68,228,180
Maaa 36,228,176
Maaa 4,228,172
D 13,44,65,3072
s_add_u32 m0, s62, 36864
Mvaa 228,228,168
)"
R"(Mvaa 196,228,164
Mvaa 164,228,160
W 4
Mvaa 168,232,160
D 14,44,65,0
Ta 244,25,0
Mvaa 200,232,164
Ta 246,25,1024
Mvaa 232,232,168
Maaa 8,232,172
Maaa 40,232,176
D 15,44,65,1024
Maaa 72,232,180
Maaa 104,232,184
Maaa 136,232,188
W 4
Maaa 140,236,188
D 16,44,65,2048
Ta 248,26,0
Maaa 108,236,184
Ta 250,26,1024
Maaa 76,236,180
Maaa 44,236,176
Maaa 12,236,172
D 17,44,65,3072
s_mov_b32 s65, 0
Mvaa 236,236,168
s_add_u32 m0, s63, 131072
Ra 192,19,0
Mvaa 204,236,164
Mvaa 172,236,160
Ra 196,19,2048
W 6
Mvaa 176,240,160
D 10,48,65,0
Ta 252,27,0
Mvaa 208,240,164
Ta 254,27,1024
Mvaa 240,240,168
Ra 200,19,4096
Maaa 16,240,172
Maaa 48,240,176
D 11,48,65,1024
Maaa 80,240,180
Ra 204,19,6144
Maaa 112,240,184
Maaa 144,240,188
W 8
Maaa 148,244,188
D 12,48,65,2048
Ta 224,20,8192
Maaa 116,244,184
Ta 226,20,9216
Ra 208,19,8192
Maaa 84,244,180
Maaa 52,244,176
Ra 212,19,10240
Maaa 20,244,172
D 13,48,65,3072
s_add_u32 m0, s63, 135168
Mvaa 244,244,168
Mvaa 212,244,164
Ra 216,19,12288
Mvaa 180,244,160
W 11
Mvaa 184,248,160
D 14,48,65,0
Ta 228,21,8192
Mvaa 216,248,164
Ta 230,21,9216
Ra 220,19,14336
Mvaa 248,248,168
Maaa 24,248,172
Maaa 56,248,176
D 15,48,65,1024
Maaa 88,248,180
Maaa 120,248,184
Maaa 152,248,188
W 10
Maaa 156,252,188
D 16,48,65,2048
Ta 232,22,8192
Maaa 124,252,184
Ta 234,22,9216
Maaa 92,252,180
Maaa 60,252,176
Maaa 28,252,172
D 17,48,65,3072
Mvaa 252,252,168
Mvaa 220,252,164
Mvaa 188,252,160
W 8
Mvaa 160,224,192
Ta 236,23,8192
Ta 238,23,9216
Mvaa 192,224,196
Mvaa 224,224,200
Maaa 0,224,204
W 4
Maaa 32,224,208
Maaa 64,224,212
Maaa 96,224,216
Maaa 128,224,220
Maaa 132,228,220
Ta 240,24,8192
Ta 242,24,9216
Maaa 100,228,216
Maaa 68,228,212
Maaa 36,228,208
Maaa 4,228,204
Ta 244,25,8192
Ta 246,25,9216
Mvaa 228,228,200
Mvaa 196,228,196
Ta 248,26,8192
Ta 250,26,9216
Mvaa 164,228,192
W 8
Mvaa 168,232,192
Ta 252,27,8192
Ta 254,27,9216
Mvaa 200,232,196
Mvaa 232,232,200
Maaa 8,232,204
Maaa 40,232,208
Maaa 72,232,212
Maaa 104,232,216
Maaa 136,232,220
W 8
Maaa 140,236,220
Maaa 108,236,216
Maaa 76,236,212
s_waitcnt vmcnt(0) lgkmcnt(0)
s_barrier
Maaa 44,236,208
s_mov_b32 s64, 768
Maaa 12,236,204
Ra 160,18,32768
Mvaa 236,236,200
s_add_u32 m0, s61, 0
Mvaa 204,236,196
Mvaa 172,236,192
Ra 164,18,34816
Mvaa 176,240,192
D 2,40,64,0
v_bfe_u32 v28, v1, 4, 3
Mvaa 208,240,196
v_lshlrev_b32 v28, 3, v28
Mvaa 240,240,200
s_lshl_b32 s80, s60, 6
Ra 168,18,36864
Maaa 16,240,204
s_add_u32 s80, s80, 256
Maaa 48,240,208
D 3,40,64,1024
Maaa 80,240,212
Ra 172,18,38912
Maaa 112,240,216
Maaa 144,240,220
Maaa 148,244,220
buffer_load_dwordx2 v[28:29], v28, s[8:11], s80 offen sc1
Ta 224,20,32768
Maaa 116,244,216
Ta 226,20,33792
Ra 176,18,40960
Maaa 84,244,212
Maaa 52,244,208
Ra 180,18,43008
Maaa 20,244,204
D 4,40,64,2048
Mvaa 244,244,200
Mvaa 212,244,196
Ra 184,18,45056
Mvaa 180,244,192
Mvaa 184,248,192
D 5,40,64,3072
s_add_u32 m0, s61, 4096
Mvaa 216,248,196
Ta 228,21,32768
Ta 230,21,33792
Mvaa 248,248,200
Ra 188,18,47104
Maaa 24,248,204
Maaa 56,248,208
D 6,40,64,0
Maaa 88,248,212
Maaa 120,248,216
Maaa 152,248,220
Maaa 156,252,220
D 7,40,64,1024
Ta 232,22,32768
Maaa 124,252,216
Ta 234,22,33792
Maaa 92,252,212
Maaa 60,252,208
Maaa 28,252,204
D 8,40,64,2048
Mvaa 252,252,200
Mvaa 220,252,196
Mvaa 188,252,192
W 8
Mvaa 160,224,160
D 9,40,64,3072
s_mov_b32 s65, 3145728
Mvaa 192,224,164
s_add_u32 m0, s62, 0
Ta 236,23,32768
Mvaa 224,224,168
Ta 238,23,33792
Maaa 0,224,172
W 4
Maaa 32,224,176
D 10,44,65,0
Maaa 64,224,180
Maaa 96,224,184
Maaa 128,224,188
Maaa 132,228,188
D 11,44,65,1024
Ta 240,24,32768
Maaa 100,228,184
Ta 242,24,33792
Maaa 68,228,180
Maaa 36,228,176
Maaa 4,228,172
D 12,44,65,2048
Mvaa 228,228,168
Mvaa 196,228,164
Mvaa 164,228,160
W 4
Mvaa 168,232,160
D 13,44,65,3072
s_add_u32 m0, s62, 4096
Mvaa 200,232,164
Ta 244,25,32768
Ta 246,25,33792
Mvaa 232,232,168
Maaa 8,232,172
Maaa 40,232,176
D 14,44,65,0
Maaa 72,232,180
Maaa 104,232,184
s_waitcnt vmcnt(11)
v_cmp_eq_u64 vcc, s[12:13], v[28:29]
s_cmp_eq_u64 vcc, -1
s_cbranch_scc0 .Lspin_8%=
.Lok_9%=:
Maaa 136,232,188
W 4
Maaa 140,236,188
D 15,44,65,1024
Ta 248,26,32768
Maaa 108,236,184
Ta 250,26,33792
Maaa 76,236,180
Maaa 44,236,176
Maaa 12,236,172
D 16,44,65,2048
Ra 192,19,32768
Mvaa 236,236,168
Mvaa 204,236,164
Mvaa 172,236,160
Ra 196,19,34816
W 6
Mvaa 176,240,160
D 17,44,65,3072
v_lshlrev_b32 v92, 3, v1
Mvaa 208,240,164
s_lshl_b32 s69, s58, 13
s_add_u32 s68, s66, s69
Mvaa 240,240,168
Ta 252,27,32768
Ta 254,27,33792
Maaa 16,240,172
Ra 200,19,36864
Maaa 48,240,176
buffer_load_dword v94, v92, s[8:11], s68 offen sc1
Maaa 80,240,180
Ra 204,19,38912
Maaa 112,240,184
Maaa 144,240,188
W 8
Maaa 148,244,188
Ta 224,20,40960
Ta 226,20,41984
Maaa 116,244,184
Ra 208,19,40960
Maaa 84,244,180
Maaa 52,244,176
Ra 212,19,43008
Maaa 20,244,172
Mvaa 244,244,168
Mvaa 212,244,164
Ra 216,19,45056
Mvaa 180,244,160
W 11
Mvaa 184,248,160
Ta 228,21,40960
Ta 230,21,41984
Mvaa 216,248,164
Ra 220,19,47104
Mvaa 248,248,168
Maaa 24,248,172
Maaa 56,248,176
Maaa 88,248,180
Maaa 120,248,184
Maaa 152,248,188
W 10
Maaa 156,252,188
Ta 232,22,40960
Ta 234,22,41984
Maaa 124,252,184
Maaa 92,252,180
Maaa 60,252,176
Maaa 28,252,172
Mvaa 252,252,168
Mvaa 220,252,164
Mvaa 188,252,160
W 8
Mvaa 160,224,192
Ta 236,23,40960
Ta 238,23,41984
Mvaa 192,224,196
Mvaa 224,224,200
Maaa 0,224,204
W 4
Maaa 32,224,208
Maaa 64,224,212
Maaa 96,224,216
Maaa 128,224,220
Maaa 132,228,220
Ta 240,24,40960
Ta 242,24,41984
Maaa 100,228,216
Maaa 68,228,212
Maaa 36,228,208
Maaa 4,228,204
Ta 244,25,40960
Ta 246,25,41984
Mvaa 228,228,200
Mvaa 196,228,196
Ta 248,26,40960
Ta 250,26,41984
Mvaa 164,228,192
W 8
Mvaa 168,232,192
Ta 252,27,40960
Ta 254,27,41984
Mvaa 200,232,196
Mvaa 232,232,200
Maaa 8,232,204
Maaa 40,232,208
Maaa 72,232,212
Maaa 104,232,216
Maaa 136,232,220
W 8
Maaa 140,236,220
Maaa 108,236,216
Maaa 76,236,212
s_waitcnt vmcnt(1) lgkmcnt(0)
s_barrier
Maaa 44,236,208
s_mov_b32 s64, 896
Maaa 12,236,204
Ra 160,18,0
Mvaa 236,236,200
s_add_u32 m0, s61, 32768
Mvaa 204,236,196
Mvaa 172,236,192
Ra 164,18,2048
Mvaa 176,240,192
D 2,40,64,0
Mvaa 208,240,196
Mvaa 240,240,200
Ra 168,18,4096
Maaa 16,240,204
Maaa 48,240,208
D 3,40,64,1024
Maaa 80,240,212
Ra 172,18,6144
Maaa 112,240,216
Maaa 144,240,220
Maaa 148,244,220
D 4,40,64,2048
Ta 224,20,0
Maaa 116,244,216
Ta 226,20,1024
Ra 176,18,8192
Maaa 84,244,212
Maaa 52,244,208
Ra 180,18,10240
Maaa 20,244,204
D 5,40,64,3072
s_add_u32 m0, s61, 36864
Mvaa 244,244,200
Mvaa 212,244,196
Ra 184,18,12288
Mvaa 180,244,192
Mvaa 184,248,192
D 6,40,64,0
Ta 228,21,0
)"
R"(Mvaa 216,248,196
Ta 230,21,1024
Ra 188,18,14336
Mvaa 248,248,200
Maaa 24,248,204
Maaa 56,248,208
D 7,40,64,1024
Maaa 88,248,212
Maaa 120,248,216
Maaa 152,248,220
Maaa 156,252,220
D 8,40,64,2048
Ta 232,22,0
Maaa 124,252,216
Ta 234,22,1024
Maaa 92,252,212
Maaa 60,252,208
Maaa 28,252,204
D 9,40,64,3072
s_mov_b32 s65, 3670016
Mvaa 252,252,200
s_add_u32 m0, s62, 32768
Mvaa 220,252,196
Mvaa 188,252,192
W 8
Mvaa 160,224,160
D 10,44,65,0
Ta 236,23,0
Mvaa 192,224,164
Ta 238,23,1024
Mvaa 224,224,168
Maaa 0,224,172
W 4
Maaa 32,224,176
D 11,44,65,1024
Maaa 64,224,180
Maaa 96,224,184
Maaa 128,224,188
Maaa 132,228,188
D 12,44,65,2048
Ta 240,24,0
Maaa 100,228,184
Ta 242,24,1024
Maaa 68,228,180
Maaa 36,228,176
Maaa 4,228,172
D 13,44,65,3072
s_add_u32 m0, s62, 36864
Mvaa 228,228,168
Mvaa 196,228,164
Mvaa 164,228,160
W 4
Mvaa 168,232,160
D 14,44,65,0
Ta 244,25,0
Mvaa 200,232,164
Ta 246,25,1024
Mvaa 232,232,168
Maaa 8,232,172
Maaa 40,232,176
D 15,44,65,1024
Maaa 72,232,180
Maaa 104,232,184
Maaa 136,232,188
W 4
Maaa 140,236,188
D 16,44,65,2048
Ta 248,26,0
Maaa 108,236,184
Ta 250,26,1024
Maaa 76,236,180
Maaa 44,236,176
Maaa 12,236,172
D 17,44,65,3072
s_add_u32 s68, s68, 32768
Mvaa 236,236,168
Ra 192,19,0
Mvaa 204,236,164
Mvaa 172,236,160
Ra 196,19,2048
W 6
Mvaa 176,240,160
buffer_load_dword v94, v92, s[8:11], s68 offen sc1
v_add_u32 v2, 131072, v20
Mvaa 208,240,164
Ta 252,27,0
Ta 254,27,1024
Mvaa 240,240,168
v_add_u32 v3, 131072, v21
Ra 200,19,4096
Maaa 16,240,172
v_add_u32 v4, 131072, v22
Maaa 48,240,176
v_add_u32 v5, 131072, v23
Maaa 80,240,180
Ra 204,19,6144
Maaa 112,240,184
v_add_u32 v6, 131072, v24
Maaa 144,240,188
W 8
Maaa 148,244,188
Ta 224,20,8192
Ta 226,20,9216
Maaa 116,244,184
Ra 208,19,8192
v_add_u32 v7, 131072, v25
Maaa 84,244,180
v_add_u32 v8, 131072, v26
Maaa 52,244,176
Ra 212,19,10240
Maaa 20,244,172
v_add_u32 v9, 131072, v27
Mvaa 244,244,168
Mvaa 212,244,164
Ra 216,19,12288
Mvaa 180,244,160
W 11
Mvaa 184,248,160
Ta 228,21,8192
Ta 230,21,9216
Mvaa 216,248,164
Ra 220,19,14336
Mvaa 248,248,168
Maaa 24,248,172
Maaa 56,248,176
Maaa 88,248,180
Maaa 120,248,184
Maaa 152,248,188
W 10
Maaa 156,252,188
Ta 232,22,8192
Ta 234,22,9216
Maaa 124,252,184
Maaa 92,252,180
Maaa 60,252,176
Maaa 28,252,172
Mvaa 252,252,168
Mvaa 220,252,164
Mvaa 188,252,160
W 8
Mvaa 160,224,192
Ta 236,23,8192
Ta 238,23,9216
Mvaa 192,224,196
Mvaa 224,224,200
Maaa 0,224,204
W 4
Maaa 32,224,208
Maaa 64,224,212
Maaa 96,224,216
Maaa 128,224,220
Maaa 132,228,220
Ta 240,24,8192
Ta 242,24,9216
Maaa 100,228,216
Maaa 68,228,212
Maaa 36,228,208
Maaa 4,228,204
Ta 244,25,8192
Ta 246,25,9216
Mvaa 228,228,200
Mvaa 196,228,196
Ta 248,26,8192
Ta 250,26,9216
Mvaa 164,228,192
W 8
Mvaa 168,232,192
Ta 252,27,8192
Ta 254,27,9216
Mvaa 200,232,196
Mvaa 232,232,200
Maaa 8,232,204
Maaa 40,232,208
Maaa 72,232,212
Maaa 104,232,216
Maaa 136,232,220
W 8
Maaa 140,236,220
Maaa 108,236,216
Maaa 76,236,212
s_waitcnt vmcnt(1) lgkmcnt(0)
s_barrier
Maaa 44,236,208
s_mov_b32 s65, 1048576
Maaa 12,236,204
Ra 160,18,32768
Mvaa 236,236,200
s_add_u32 m0, s63, 0
Mvaa 204,236,196
Mvaa 172,236,192
Ra 164,18,34816
Mvaa 176,240,192
D 10,48,65,0
Mvaa 208,240,196
Mvaa 240,240,200
Ra 168,18,36864
Maaa 16,240,204
Maaa 48,240,208
D 11,48,65,1024
Maaa 80,240,212
Ra 172,18,38912
Maaa 112,240,216
Maaa 144,240,220
Maaa 148,244,220
D 12,48,65,2048
Ta 224,20,32768
Maaa 116,244,216
Ta 226,20,33792
Ra 176,18,40960
Maaa 84,244,212
Maaa 52,244,208
Ra 180,18,43008
Maaa 20,244,204
D 13,48,65,3072
s_add_u32 m0, s63, 4096
Mvaa 244,244,200
Mvaa 212,244,196
Ra 184,18,45056
Mvaa 180,244,192
Mvaa 184,248,192
D 14,48,65,0
Ta 228,21,32768
Mvaa 216,248,196
Ta 230,21,33792
Ra 188,18,47104
Mvaa 248,248,200
Maaa 24,248,204
Maaa 56,248,208
D 15,48,65,1024
Maaa 88,248,212
Maaa 120,248,216
Maaa 152,248,220
Maaa 156,252,220
D 16,48,65,2048
Ta 232,22,32768
Maaa 124,252,216
Ta 234,22,33792
Maaa 92,252,212
Maaa 60,252,208
Maaa 28,252,204
D 17,48,65,3072
s_mov_b32 s65, 2097152
Mvaa 252,252,200
s_add_u32 m0, s63, 65536
Mvaa 220,252,196
Mvaa 188,252,192
W 8
Mvaa 160,224,160
D 10,48,65,0
Ta 236,23,32768
Mvaa 192,224,164
Ta 238,23,33792
Mvaa 224,224,168
Maaa 0,224,172
W 4
Maaa 32,224,176
D 11,48,65,1024
Maaa 64,224,180
Maaa 96,224,184
Maaa 128,224,188
Maaa 132,228,188
D 12,48,65,2048
Ta 240,24,32768
Maaa 100,228,184
Ta 242,24,33792
Maaa 68,228,180
Maaa 36,228,176
Maaa 4,228,172
D 13,48,65,3072
s_add_u32 m0, s63, 69632
Mvaa 228,228,168
Mvaa 196,228,164
Mvaa 164,228,160
W 4
Mvaa 168,232,160
D 14,48,65,0
Ta 244,25,32768
Mvaa 200,232,164
Ta 246,25,33792
Mvaa 232,232,168
Maaa 8,232,172
Maaa 40,232,176
D 15,48,65,1024
Maaa 72,232,180
Maaa 104,232,184
Maaa 136,232,188
W 4
Maaa 140,236,188
D 16,48,65,2048
Ta 248,26,32768
Maaa 108,236,184
Ta 250,26,33792
Maaa 76,236,180
Maaa 44,236,176
Maaa 12,236,172
D 17,48,65,3072
s_and_b32 s69, s69, 8192
Mvaa 236,236,168
s_add_u32 s68, s66, s69
Ra 192,19,32768
Mvaa 204,236,164
s_add_u32 s68, s68, 65536
Mvaa 172,236,160
Ra 196,19,34816
W 6
Mvaa 176,240,160
buffer_load_dword v94, v92, s[8:11], s68 offen sc1
Ta 252,27,32768
Mvaa 208,240,164
Ta 254,27,33792
Mvaa 240,240,168
Ra 200,19,36864
Maaa 16,240,172
Maaa 48,240,176
Maaa 80,240,180
Ra 204,19,38912
Maaa 112,240,184
Maaa 144,240,188
W 8
Maaa 148,244,188
Ta 224,20,40960
Ta 226,20,41984
Maaa 116,244,184
Ra 208,19,40960
Maaa 84,244,180
Maaa 52,244,176
Ra 212,19,43008
Maaa 20,244,172
Mvaa 244,244,168
Mvaa 212,244,164
Ra 216,19,45056
Mvaa 180,244,160
W 11
Mvaa 184,248,160
Ta 228,21,40960
Ta 230,21,41984
Mvaa 216,248,164
Ra 220,19,47104
Mvaa 248,248,168
Maaa 24,248,172
Maaa 56,248,176
Maaa 88,248,180
Maaa 120,248,184
Maaa 152,248,188
W 10
Maaa 156,252,188
Ta 232,22,40960
Ta 234,22,41984
Maaa 124,252,184
Maaa 92,252,180
Maaa 60,252,176
Maaa 28,252,172
Mvaa 252,252,168
Mvaa 220,252,164
Mvaa 188,252,160
s_and_b32 s80, s14, 2
W 8
Mvaa 160,224,192
Ta 236,23,40960
Ta 238,23,41984
Mvaa 192,224,196
Mvaa 224,224,200
s_lshl_b32 s80, s80, 10
Maaa 0,224,204
W 4
Maaa 32,224,208
v_add_u32 v18, s80, v1
Maaa 64,224,212
Maaa 96,224,216
Maaa 128,224,220
v_xor_b32 v19, 2048, v18
Maaa 132,228,220
Ta 240,24,40960
Ta 242,24,41984
Maaa 100,228,216
buffer_load_dwordx4 a[160:163], v18, s[8:11], s66 offen offset:0 sc1
Maaa 68,228,212
Maaa 36,228,208
Maaa 4,228,204
Ta 244,25,40960
Ta 246,25,41984
Mvaa 228,228,200
buffer_load_dwordx4 a[164:167], v18, s[8:11], s66 offen offset:1024 sc1
Mvaa 196,228,196
Ta 248,26,40960
Ta 250,26,41984
Mvaa 164,228,192
W 8
Mvaa 168,232,192
Ta 252,27,40960
Ta 254,27,41984
Mvaa 200,232,196
buffer_load_dwordx4 a[168:171], v19, s[8:11], s66 offen offset:0 sc1
Mvaa 232,232,200
Maaa 8,232,204
Maaa 40,232,208
Maaa 72,232,212
buffer_load_dwordx4 a[172:175], v19, s[8:11], s66 offen offset:1024 sc1
s_add_u32 s66, s66, 4096
Maaa 104,232,216
)"
R"(Maaa 136,232,220
W 8
Maaa 140,236,220
Maaa 108,236,216
buffer_load_dwordx4 a[176:179], v18, s[8:11], s66 offen offset:0 sc1
Maaa 76,236,212
s_waitcnt vmcnt(6) lgkmcnt(0)
s_barrier
Maaa 44,236,208
Maaa 12,236,204
Mvaa 236,236,200
buffer_load_dwordx4 a[180:183], v18, s[8:11], s66 offen offset:1024 sc1
Mvaa 204,236,196
Mvaa 172,236,192
Mvaa 176,240,192
Mvaa 208,240,196
buffer_load_dwordx4 a[184:187], v19, s[8:11], s66 offen offset:0 sc1
Mvaa 240,240,200
s_mov_b32 s65, 3145728
Maaa 16,240,204
Maaa 48,240,208
Maaa 80,240,212
buffer_load_dwordx4 a[188:191], v19, s[8:11], s66 offen offset:1024 sc1
Maaa 112,240,216
Maaa 144,240,220
Maaa 148,244,220
Maaa 116,244,216
Maaa 84,244,212
Ta 224,2,0
Ta 226,2,1024
Maaa 52,244,208
Maaa 20,244,204
s_add_u32 m0, s63, 32768
Mvaa 244,244,200
Mvaa 212,244,196
Ta 228,3,0
Ta 230,3,1024
Mvaa 180,244,192
Mvaa 184,248,192
Mvaa 216,248,196
Mvaa 248,248,200
Ta 232,4,0
Ta 234,4,1024
Maaa 24,248,204
Maaa 56,248,208
Maaa 88,248,212
D 10,48,65,0
Maaa 120,248,216
Ta 236,5,0
Ta 238,5,1024
Maaa 152,248,220
Maaa 156,252,220
Maaa 124,252,216
Maaa 92,252,212
Ta 240,6,0
Ta 242,6,1024
Maaa 60,252,208
Maaa 28,252,204
Mvaa 252,252,200
Mvaa 220,252,196
D 11,48,65,1024
Mvaa 188,252,192
s_branch .Lq_5%=
.Lv_4%=:
s_waitcnt vmcnt(24)
s_barrier
Ra 160,18,0
Ta 224,20,16384
Ta 226,20,17408
Ra 164,18,2048
Ra 168,18,4096
Ra 172,18,6144
Ra 176,18,8192
Ra 180,18,10240
Ra 184,18,12288
Ra 188,18,14336
Ta 228,21,16384
Ta 230,21,17408
W 6
Zvaa 160,160,224
Ta 232,22,16384
Ta 234,22,17408
Zvaa 164,164,224
Ta 236,23,16384
Ta 238,23,17408
Zvaa 224,168,224
s_waitcnt vmcnt(21)
Zvaa 228,172,224
v_lshlrev_b32 v56, 16, v32
W 6
Zaaa 32,176,224
v_and_b32 v57, 0xffff0000, v32
Zaaa 36,180,224
v_lshlrev_b32 v58, 16, v33
Zaaa 96,184,224
v_and_b32 v59, 0xffff0000, v33
Zaaa 100,188,224
v_lshlrev_b32 v60, 16, v34
W 4
Zaaa 108,188,228
v_and_b32 v61, 0xffff0000, v34
Ta 240,24,16384
Zaaa 104,184,228
Ta 242,24,17408
v_lshlrev_b32 v62, 16, v35
Zaaa 44,180,228
v_and_b32 v63, 0xffff0000, v35
v_lshlrev_b32 v64, 16, v36
Zaaa 40,176,228
v_and_b32 v65, 0xffff0000, v36
Zvaa 236,172,228
v_lshlrev_b32 v66, 16, v37
Zvaa 232,168,228
v_and_b32 v67, 0xffff0000, v37
Zvaa 172,164,228
v_lshlrev_b32 v68, 16, v38
Zvaa 168,160,228
v_and_b32 v69, 0xffff0000, v38
W 4
Zvaa 176,160,232
v_lshlrev_b32 v70, 16, v39
v_and_b32 v71, 0xffff0000, v39
Zvaa 180,164,232
Ta 244,25,16384
Ta 246,25,17408
Zvaa 240,168,232
v_lshlrev_b32 v72, 16, v40
v_and_b32 v73, 0xffff0000, v40
Zvaa 244,172,232
v_lshlrev_b32 v74, 16, v41
Zaaa 48,176,232
v_and_b32 v75, 0xffff0000, v41
Zaaa 52,180,232
v_lshlrev_b32 v76, 16, v42
v_and_b32 v77, 0xffff0000, v42
Zaaa 112,184,232
v_lshlrev_b32 v78, 16, v43
Zaaa 116,188,232
v_and_b32 v79, 0xffff0000, v43
W 4
Zaaa 124,188,236
A 80,56,57
Ta 248,26,16384
Zaaa 120,184,236
Ta 250,26,17408
A 81,58,59
Zaaa 60,180,236
A 80,80,60
Zaaa 56,176,236
A 81,81,61
A 80,80,62
Zvaa 252,172,236
A 81,81,63
Ra 192,19,0
Zvaa 248,168,236
A 80,80,64
Zvaa 188,164,236
A 81,81,65
Zvaa 184,160,236
A 80,80,66
W 5
Zvaa 192,160,240
Ra 196,19,2048
A 81,81,67
Zvaa 196,164,240
Ta 252,27,16384
Ta 254,27,17408
Zaaa 0,168,240
A 80,80,68
A 81,81,69
Zaaa 4,172,240
A 80,80,70
Ra 200,19,4096
Zaaa 64,176,240
A 81,81,71
A 80,80,72
Zaaa 68,180,240
A 81,81,73
Ra 204,19,6144
Zaaa 128,184,240
A 80,80,74
Zaaa 132,188,240
A 81,81,75
W 8
Zaaa 140,188,244
A 80,80,76
A 81,81,77
Zaaa 136,184,244
Ta 224,20,24576
Ta 226,20,25600
Zaaa 76,180,244
Ra 208,19,8192
A 80,80,78
Zaaa 72,176,244
A 81,81,79
A 80,80,81
Zaaa 12,172,244
Ra 212,19,10240
v_add_f32_dpp v80, v80, v80 quad_perm:[1,0,3,2] row_mask:0xf bank_mask:0xf
Zaaa 8,168,244
Zvaa 204,164,244
v_add_f32_dpp v80, v80, v80 quad_perm:[2,3,0,1] row_mask:0xf bank_mask:0xf
Ra 216,19,12288
Zvaa 200,160,244
v_add_f32_dpp v80, v80, v80 row_half_mirror row_mask:0xf bank_mask:0xf
W 11
Zvaa 208,160,248
v_add_f32_dpp v80, v80, v80 row_mirror row_mask:0xf bank_mask:0xf
v_mov_b32 v81, v80
Zvaa 212,164,248
Ta 228,21,24576
v_permlane16_swap_b32 v80, v81
Zaaa 16,168,248
Ta 230,21,25600
A 80,80,81
Zaaa 20,172,248
Ra 220,19,14336
v_mul_f32 v84, 0x3acccccd, v80
Zaaa 80,176,248
s_mov_b32 s86, 0xffff
s_mov_b32 s87, 0xffff
Zaaa 84,180,248
v_sub_f32 v82, v56, v84
U 80,82,82
Zaaa 144,184,248
v_sub_f32 v83, v57, v84
Zaaa 148,188,248
v_fmac_f32 v80, v83, v83
W 10
Zaaa 156,188,252
v_sub_f32 v82, v58, v84
Ta 232,22,24576
Zaaa 152,184,252
Ta 234,22,25600
v_fmac_f32 v80, v82, v82
Zaaa 92,180,252
v_sub_f32 v83, v59, v84
v_fmac_f32 v80, v83, v83
Zaaa 88,176,252
v_sub_f32 v82, v60, v84
Zaaa 28,172,252
v_fmac_f32 v80, v82, v82
Zaaa 24,168,252
v_sub_f32 v83, v61, v84
Zvaa 220,164,252
v_fmac_f32 v80, v83, v83
Zvaa 216,160,252
v_sub_f32 v82, v62, v84
W 8
Mvaa 160,192,224
v_fmac_f32 v80, v82, v82
v_sub_f32 v83, v63, v84
Mvaa 164,196,224
Ta 236,23,24576
Ta 238,23,25600
Mvaa 224,200,224
v_fmac_f32 v80, v83, v83
v_sub_f32 v82, v64, v84
Mvaa 228,204,224
v_fmac_f32 v80, v82, v82
W 4
Maaa 32,208,224
v_sub_f32 v83, v65, v84
Maaa 36,212,224
v_fmac_f32 v80, v83, v83
Maaa 96,216,224
v_sub_f32 v82, v66, v84
Maaa 100,220,224
v_fmac_f32 v80, v82, v82
v_sub_f32 v83, v67, v84
Maaa 108,220,228
v_fmac_f32 v80, v83, v83
Ta 240,24,24576
Maaa 104,216,228
Ta 242,24,25600
v_sub_f32 v82, v68, v84
Maaa 44,212,228
v_fmac_f32 v80, v82, v82
Maaa 40,208,228
v_sub_f32 v83, v69, v84
Mvaa 236,204,228
v_fmac_f32 v80, v83, v83
Ta 244,25,24576
Mvaa 232,200,228
Ta 246,25,25600
v_sub_f32 v82, v70, v84
Mvaa 172,196,228
v_fmac_f32 v80, v82, v82
v_sub_f32 v83, v71, v84
Mvaa 168,192,228
Ta 248,26,24576
W 7
Mvaa 176,192,232
Ta 250,26,25600
v_fmac_f32 v80, v83, v83
Mvaa 180,196,232
v_sub_f32 v82, v72, v84
Ta 252,27,24576
Mvaa 240,200,232
Ta 254,27,25600
U 81,82,82
Mvaa 244,204,232
v_sub_f32 v83, v73, v84
v_fmac_f32 v81, v83, v83
Maaa 48,208,232
v_sub_f32 v82, v74, v84
v_fmac_f32 v81, v82, v82
Maaa 52,212,232
v_sub_f32 v83, v75, v84
Maaa 112,216,232
v_fmac_f32 v81, v83, v83
Maaa 116,220,232
v_sub_f32 v82, v76, v84
W 8
Maaa 124,220,236
v_fmac_f32 v81, v82, v82
Maaa 120,216,236
v_sub_f32 v83, v77, v84
v_fmac_f32 v81, v83, v83
Maaa 60,212,236
s_waitcnt vmcnt(1) lgkmcnt(0)
s_barrier
Maaa 56,208,236
v_sub_f32 v82, v78, v84
s_mov_b32 s64, 256
Mvaa 252,204,236
v_fmac_f32 v81, v82, v82
v_sub_f32 v83, v79, v84
Mvaa 248,200,236
Ra 160,18,32768
v_fmac_f32 v81, v83, v83
Mvaa 188,196,236
s_add_u32 m0, s61, 0
v_cndmask_b32_e64 v81, 0, v81, s[86:87]
Mvaa 184,192,236
A 80,80,81
Ra 164,18,34816
Mvaa 192,192,240
v_add_f32_dpp v80, v80, v80 quad_perm:[1,0,3,2] row_mask:0xf bank_mask:0xf
D 2,40,64,0
Mvaa 196,196,240
v_add_f32_dpp v80, v80, v80 quad_perm:[2,3,0,1] row_mask:0xf bank_mask:0xf
Maaa 0,200,240
Ra 168,18,36864
v_add_f32_dpp v80, v80, v80 row_half_mirror row_mask:0xf bank_mask:0xf
Maaa 4,204,240
Maaa 64,208,240
v_add_f32_dpp v80, v80, v80 row_mirror row_mask:0xf bank_mask:0xf
D 3,40,64,1024
Maaa 68,212,240
v_mov_b32 v81, v80
Ra 172,18,38912
Maaa 128,216,240
v_permlane16_swap_b32 v80, v81
A 80,80,81
Maaa 132,220,240
v_mul_f32 v80, 0x3acccccd, v80
K 80,80,80
Maaa 140,220,244
D 4,40,64,2048
v_lshlrev_b32 v80, 16, v80
Maaa 136,216,244
v_add_f32 v80, 0x3727c5ac, v80
Ta 224,20,49152
Maaa 76,212,244
Ta 226,20,50176
Ra 176,18,40960
Maaa 72,208,244
K 80,80,80
v_lshlrev_b32 v80, 16, v80
Maaa 12,204,244
D 5,40,64,3072
s_add_u32 m0, s61, 4096
Maaa 8,200,244
v_sqrt_f32 v81, v80
Mvaa 204,196,244
Ra 180,18,43008
Ra 184,18,45056
Mvaa 200,192,244
v_rcp_f32 v82, v81
Mvaa 208,192,248
D 6,40,64,0
v_fma_f32 v83, -v81, v81, v80
Mvaa 212,196,248
v_mul_f32 v82, 0.5, v82
v_fma_f32 v81, v83, v82, v81
Maaa 16,200,248
Ta 228,21,49152
Ta 230,21,50176
Maaa 20,204,248
K 86,81,81
Ra 188,18,47104
Maaa 80,208,248
D 7,40,64,1024
v_lshlrev_b32 v86, 16, v86
Maaa 84,212,248
v_rcp_f32 v87, v86
Maaa 144,216,248
K 85,84,84
v_lshlrev_b32 v85, 16, v85
)"
R"(Maaa 148,220,248
v_sub_f32 v56, v56, v85
Maaa 156,220,252
D 8,40,64,2048
v_sub_f32 v57, v57, v85
Maaa 152,216,252
K 80,56,57
Ta 232,22,49152
Maaa 92,212,252
Ta 234,22,50176
v_lshlrev_b32 v56, 16, v80
Maaa 88,208,252
v_and_b32 v57, 0xffff0000, v80
U 80,56,87
Maaa 28,204,252
D 9,40,64,3072
s_mov_b32 s65, 1048576
Maaa 24,200,252
s_add_u32 m0, s62, 0
U 81,57,87
Mvaa 220,196,252
v_fma_f32 v82, -v80, v86, v56
v_fma_f32 v83, -v81, v86, v57
Mvaa 216,192,252
v_fmac_f32 v80, v82, v87
W 8
Mvaa 160,160,224
D 10,44,65,0
v_fmac_f32 v81, v83, v87
Mvaa 164,164,224
K 32,80,81
Ta 236,23,49152
Mvaa 224,168,224
Ta 238,23,50176
v_lshlrev_b32 v80, 16, v32
Mvaa 228,172,224
v_and_b32 v81, 0xffff0000, v32
W 4
Maaa 32,176,224
D 11,44,65,1024
v_lshlrev_b32 v82, 16, v44
Maaa 36,180,224
v_and_b32 v83, 0xffff0000, v44
U 84,80,82
Maaa 96,184,224
v_fmac_f32 v84, v81, v83
v_sub_f32 v58, v58, v85
Maaa 100,188,224
v_sub_f32 v59, v59, v85
Maaa 108,188,228
D 12,44,65,2048
K 80,58,59
Maaa 104,184,228
Ta 240,24,49152
Ta 242,24,50176
Maaa 44,180,228
v_lshlrev_b32 v58, 16, v80
v_and_b32 v59, 0xffff0000, v80
Maaa 40,176,228
U 80,58,87
U 81,59,87
Mvaa 236,172,228
D 13,44,65,3072
s_add_u32 m0, s62, 4096
Mvaa 232,168,228
v_fma_f32 v82, -v80, v86, v58
v_fma_f32 v83, -v81, v86, v59
Mvaa 172,164,228
v_fmac_f32 v80, v82, v87
Mvaa 168,160,228
v_fmac_f32 v81, v83, v87
W 4
Mvaa 176,160,232
D 14,44,65,0
K 33,80,81
Mvaa 180,164,232
Ta 244,25,49152
Ta 246,25,50176
Mvaa 240,168,232
v_lshlrev_b32 v80, 16, v33
v_and_b32 v81, 0xffff0000, v33
Mvaa 244,172,232
v_lshlrev_b32 v82, 16, v45
v_and_b32 v83, 0xffff0000, v45
Maaa 48,176,232
D 15,44,65,1024
v_fmac_f32 v84, v80, v82
Maaa 52,180,232
v_fmac_f32 v84, v81, v83
Maaa 112,184,232
v_sub_f32 v60, v60, v85
Maaa 116,188,232
v_sub_f32 v61, v61, v85
W 4
Maaa 124,188,236
D 16,44,65,2048
K 80,60,61
Maaa 120,184,236
v_lshlrev_b32 v60, 16, v80
Ta 248,26,49152
Maaa 60,180,236
Ta 250,26,50176
v_and_b32 v61, 0xffff0000, v80
Maaa 56,176,236
U 80,60,87
U 81,61,87
Mvaa 252,172,236
D 17,44,65,3072
v_fma_f32 v82, -v80, v86, v60
Mvaa 248,168,236
Ra 192,19,32768
v_fma_f32 v83, -v81, v86, v61
Mvaa 188,164,236
v_fmac_f32 v80, v82, v87
v_fmac_f32 v81, v83, v87
Mvaa 184,160,236
K 34,80,81
W 5
Mvaa 192,160,240
global_load_dword v94, v[92:93], off
v_lshl_add_u64 v[92:93], v[92:93], 0, s[68:69]
Mvaa 196,164,240
Ra 196,19,34816
v_lshlrev_b32 v80, 16, v34
Maaa 0,168,240
Ta 252,27,49152
Ta 254,27,50176
Maaa 4,172,240
v_and_b32 v81, 0xffff0000, v34
v_lshlrev_b32 v82, 16, v46
Maaa 64,176,240
Ra 200,19,36864
v_and_b32 v83, 0xffff0000, v46
Maaa 68,180,240
v_fmac_f32 v84, v80, v82
v_fmac_f32 v84, v81, v83
Maaa 128,184,240
v_sub_f32 v62, v62, v85
Ra 204,19,38912
Maaa 132,188,240
v_sub_f32 v63, v63, v85
W 8
Maaa 140,188,244
K 80,62,63
v_lshlrev_b32 v62, 16, v80
Maaa 136,184,244
Ta 224,20,57344
Ta 226,20,58368
Maaa 76,180,244
Ra 208,19,40960
v_and_b32 v63, 0xffff0000, v80
Maaa 72,176,244
U 80,62,87
U 81,63,87
Maaa 12,172,244
v_fma_f32 v82, -v80, v86, v62
Ra 212,19,43008
Maaa 8,168,244
v_fma_f32 v83, -v81, v86, v63
v_fmac_f32 v80, v82, v87
Mvaa 204,164,244
v_fmac_f32 v81, v83, v87
Ra 216,19,45056
Mvaa 200,160,244
K 35,80,81
W 11
Mvaa 208,160,248
v_lshlrev_b32 v80, 16, v35
v_and_b32 v81, 0xffff0000, v35
Mvaa 212,164,248
Ta 228,21,57344
Ta 230,21,58368
Maaa 16,168,248
v_lshlrev_b32 v82, 16, v47
Ra 220,19,47104
Maaa 20,172,248
v_and_b32 v83, 0xffff0000, v47
v_fmac_f32 v84, v80, v82
Maaa 80,176,248
v_fmac_f32 v84, v81, v83
Maaa 84,180,248
v_sub_f32 v64, v64, v85
Maaa 144,184,248
v_sub_f32 v65, v65, v85
K 80,64,65
Maaa 148,188,248
v_lshlrev_b32 v64, 16, v80
W 10
Maaa 156,188,252
v_and_b32 v65, 0xffff0000, v80
Ta 232,22,57344
Maaa 152,184,252
Ta 234,22,58368
U 80,64,87
Maaa 92,180,252
U 81,65,87
Maaa 88,176,252
v_fma_f32 v82, -v80, v86, v64
Maaa 28,172,252
v_fma_f32 v83, -v81, v86, v65
v_fmac_f32 v80, v82, v87
Maaa 24,168,252
v_fmac_f32 v81, v83, v87
Mvaa 220,164,252
K 36,80,81
Mvaa 216,160,252
v_lshlrev_b32 v80, 16, v36
W 8
Mvaa 160,192,224
v_and_b32 v81, 0xffff0000, v36
Ta 236,23,57344
Mvaa 164,196,224
Ta 238,23,58368
v_lshlrev_b32 v82, 16, v48
Mvaa 224,200,224
v_and_b32 v83, 0xffff0000, v48
v_fmac_f32 v84, v80, v82
Mvaa 228,204,224
v_fmac_f32 v84, v81, v83
W 4
Maaa 32,208,224
v_sub_f32 v66, v66, v85
Maaa 36,212,224
v_sub_f32 v67, v67, v85
Maaa 96,216,224
K 80,66,67
Maaa 100,220,224
v_lshlrev_b32 v66, 16, v80
Maaa 108,220,228
v_and_b32 v67, 0xffff0000, v80
Ta 240,24,57344
Maaa 104,216,228
Ta 242,24,58368
U 80,66,87
Maaa 44,212,228
U 81,67,87
v_fma_f32 v82, -v80, v86, v66
Maaa 40,208,228
v_fma_f32 v83, -v81, v86, v67
Mvaa 236,204,228
v_fmac_f32 v80, v82, v87
Ta 244,25,57344
Mvaa 232,200,228
Ta 246,25,58368
v_fmac_f32 v81, v83, v87
Mvaa 172,196,228
K 37,80,81
Ta 248,26,57344
Mvaa 168,192,228
Ta 250,26,58368
W 8
Mvaa 176,192,232
v_lshlrev_b32 v80, 16, v37
v_and_b32 v81, 0xffff0000, v37
Mvaa 180,196,232
v_lshlrev_b32 v82, 16, v49
Ta 252,27,57344
Mvaa 240,200,232
Ta 254,27,58368
v_and_b32 v83, 0xffff0000, v49
Mvaa 244,204,232
v_fmac_f32 v84, v80, v82
v_fmac_f32 v84, v81, v83
Maaa 48,208,232
v_sub_f32 v68, v68, v85
Maaa 52,212,232
v_sub_f32 v69, v69, v85
K 80,68,69
Maaa 112,216,232
v_lshlrev_b32 v68, 16, v80
Maaa 116,220,232
v_and_b32 v69, 0xffff0000, v80
W 8
Maaa 124,220,236
U 80,68,87
Maaa 120,216,236
U 81,69,87
Maaa 60,212,236
s_waitcnt vmcnt(1) lgkmcnt(0)
s_barrier
Maaa 56,208,236
v_fma_f32 v82, -v80, v86, v68
s_mov_b32 s64, 384
Mvaa 252,204,236
v_fma_f32 v83, -v81, v86, v69
v_fmac_f32 v80, v82, v87
Mvaa 248,200,236
v_fmac_f32 v81, v83, v87
Ra 160,18,0
Mvaa 188,196,236
K 38,80,81
s_add_u32 m0, s61, 32768
Mvaa 184,192,236
v_lshlrev_b32 v80, 16, v38
v_and_b32 v81, 0xffff0000, v38
Mvaa 192,192,240
Ra 164,18,2048
v_lshlrev_b32 v82, 16, v50
Mvaa 196,196,240
D 2,40,64,0
v_and_b32 v83, 0xffff0000, v50
Maaa 0,200,240
v_fmac_f32 v84, v80, v82
v_fmac_f32 v84, v81, v83
Maaa 4,204,240
Ra 168,18,4096
v_sub_f32 v70, v70, v85
Maaa 64,208,240
v_sub_f32 v71, v71, v85
Maaa 68,212,240
D 3,40,64,1024
K 80,70,71
Maaa 128,216,240
Ra 172,18,6144
v_lshlrev_b32 v70, 16, v80
Maaa 132,220,240
v_and_b32 v71, 0xffff0000, v80
U 80,70,87
Maaa 140,220,244
U 81,71,87
Ta 224,20,16384
Maaa 136,216,244
D 4,40,64,2048
Ta 226,20,17408
Maaa 76,212,244
Ra 176,18,8192
v_fma_f32 v82, -v80, v86, v70
Maaa 72,208,244
v_fma_f32 v83, -v81, v86, v71
v_fmac_f32 v80, v82, v87
Maaa 12,204,244
Ra 180,18,10240
v_fmac_f32 v81, v83, v87
Maaa 8,200,244
D 5,40,64,3072
s_add_u32 m0, s61, 36864
Mvaa 204,196,244
K 39,80,81
v_lshlrev_b32 v80, 16, v39
Mvaa 200,192,244
v_and_b32 v81, 0xffff0000, v39
Ra 184,18,12288
Mvaa 208,192,248
v_lshlrev_b32 v82, 16, v51
v_and_b32 v83, 0xffff0000, v51
Mvaa 212,196,248
D 6,40,64,0
Ta 228,21,16384
Maaa 16,200,248
Ta 230,21,17408
v_fmac_f32 v84, v80, v82
Maaa 20,204,248
Ra 188,18,14336
v_fmac_f32 v84, v81, v83
Maaa 80,208,248
v_sub_f32 v72, v72, v85
v_sub_f32 v73, v73, v85
Maaa 84,212,248
D 7,40,64,1024
K 80,72,73
Maaa 144,216,248
v_lshlrev_b32 v72, 16, v80
v_and_b32 v73, 0xffff0000, v80
Maaa 148,220,248
U 80,72,87
Maaa 156,220,252
U 81,73,87
Ta 232,22,16384
Maaa 152,216,252
D 8,40,64,2048
Ta 234,22,17408
Maaa 92,212,252
v_fma_f32 v82, -v80, v86, v72
v_fma_f32 v83, -v81, v86, v73
Maaa 88,208,252
v_fmac_f32 v80, v82, v87
v_fmac_f32 v81, v83, v87
Maaa 28,204,252
K 40,80,81
Maaa 24,200,252
D 9,40,64,3072
s_mov_b32 s65, 1572864
Mvaa 220,196,252
v_lshlrev_b32 v80, 16, v40
s_add_u32 m0, s62, 32768
Mvaa 216,192,252
v_and_b32 v81, 0xffff0000, v40
W 8
Mvaa 160,160,224
v_lshlrev_b32 v82, 16, v52
v_and_b32 v83, 0xffff0000, v52
Mvaa 164,164,224
D 10,44,65,0
v_fmac_f32 v84, v80, v82
Mvaa 224,168,224
Ta 236,23,16384
Ta 238,23,17408
Mvaa 228,172,224
v_fmac_f32 v84, v81, v83
W 4
)"
R"(Maaa 32,176,224
v_sub_f32 v74, v74, v85
v_sub_f32 v75, v75, v85
Maaa 36,180,224
D 11,44,65,1024
K 80,74,75
Maaa 96,184,224
v_lshlrev_b32 v74, 16, v80
v_and_b32 v75, 0xffff0000, v80
Maaa 100,188,224
U 80,74,87
U 81,75,87
Maaa 108,188,228
v_fma_f32 v82, -v80, v86, v74
Ta 240,24,16384
Maaa 104,184,228
D 12,44,65,2048
Ta 242,24,17408
Maaa 44,180,228
v_fma_f32 v83, -v81, v86, v75
v_fmac_f32 v80, v82, v87
Maaa 40,176,228
v_fmac_f32 v81, v83, v87
Mvaa 236,172,228
K 41,80,81
Mvaa 232,168,228
D 13,44,65,3072
s_add_u32 m0, s62, 36864
Mvaa 172,164,228
v_lshlrev_b32 v80, 16, v41
v_and_b32 v81, 0xffff0000, v41
Mvaa 168,160,228
v_lshlrev_b32 v82, 16, v53
W 4
Mvaa 176,160,232
v_and_b32 v83, 0xffff0000, v53
v_fmac_f32 v84, v80, v82
Mvaa 180,164,232
D 14,44,65,0
Ta 244,25,16384
Mvaa 240,168,232
Ta 246,25,17408
v_fmac_f32 v84, v81, v83
Mvaa 244,172,232
v_sub_f32 v76, v76, v85
v_sub_f32 v77, v77, v85
Maaa 48,176,232
K 80,76,77
v_lshlrev_b32 v76, 16, v80
Maaa 52,180,232
D 15,44,65,1024
v_and_b32 v77, 0xffff0000, v80
Maaa 112,184,232
U 80,76,87
Maaa 116,188,232
U 81,77,87
W 4
Maaa 124,188,236
v_fma_f32 v82, -v80, v86, v76
Ta 248,26,16384
Maaa 120,184,236
D 16,44,65,2048
Ta 250,26,17408
Maaa 60,180,236
v_fma_f32 v83, -v81, v86, v77
v_fmac_f32 v80, v82, v87
Maaa 56,176,236
v_fmac_f32 v81, v83, v87
K 42,80,81
Mvaa 252,172,236
v_lshlrev_b32 v80, 16, v42
Ra 192,19,0
Mvaa 248,168,236
D 17,44,65,3072
v_and_b32 v81, 0xffff0000, v42
Mvaa 188,164,236
v_lshlrev_b32 v82, 16, v54
Mvaa 184,160,236
v_and_b32 v83, 0xffff0000, v54
W 5
Mvaa 192,160,240
v_fmac_f32 v84, v80, v82
Ra 196,19,2048
Mvaa 196,164,240
v_fmac_f32 v84, v81, v83
Ta 252,27,16384
Maaa 0,168,240
Ta 254,27,17408
v_sub_f32 v78, v78, v85
Maaa 4,172,240
v_sub_f32 v79, v79, v85
Ra 200,19,4096
Maaa 64,176,240
K 80,78,79
v_lshlrev_b32 v78, 16, v80
Maaa 68,180,240
v_and_b32 v79, 0xffff0000, v80
U 80,78,87
Maaa 128,184,240
Ra 204,19,6144
U 81,79,87
Maaa 132,188,240
v_fma_f32 v82, -v80, v86, v78
W 8
Maaa 140,188,244
v_fma_f32 v83, -v81, v86, v79
Ta 224,20,24576
Maaa 136,184,244
Ta 226,20,25600
Ra 208,19,8192
Maaa 76,180,244
v_fmac_f32 v80, v82, v87
v_fmac_f32 v81, v83, v87
Maaa 72,176,244
K 43,80,81
Ra 212,19,10240
Maaa 12,172,244
v_lshlrev_b32 v80, 16, v43
v_and_b32 v81, 0xffff0000, v43
Maaa 8,168,244
v_lshlrev_b32 v82, 16, v55
Mvaa 204,164,244
v_and_b32 v83, 0xffff0000, v55
Ra 216,19,12288
Mvaa 200,160,244
v_fmac_f32 v84, v80, v82
W 11
Mvaa 208,160,248
v_fmac_f32 v84, v81, v83
Ta 228,21,24576
Mvaa 212,164,248
Ta 230,21,25600
v_lshrrev_b32 v82, 9, v1
Maaa 16,168,248
Ra 220,19,14336
s_lshl_b32 s80, s14, 1
Maaa 20,172,248
v_add_u32 v82, s80, v82
v_mul_u32_u24 v83, 1552, v82
Maaa 80,176,248
v_add_u32 v83, v83, v88
Maaa 84,180,248
v_add_u32 v83, 131072, v83
Maaa 144,184,248
ds_write_b128 v83, v[32:35] offset:0
Maaa 148,188,248
ds_write_b128 v83, v[36:39] offset:512
W 12
Maaa 156,188,252
ds_write_b128 v83, v[40:43] offset:1024
v_add_f32_dpp v84, v84, v84 quad_perm:[1,0,3,2] row_mask:0xf bank_mask:0xf
Maaa 152,184,252
Ta 232,22,24576
Ta 234,22,25600
Maaa 92,180,252
v_add_f32_dpp v84, v84, v84 quad_perm:[2,3,0,1] row_mask:0xf bank_mask:0xf
Maaa 88,176,252
Maaa 28,172,252
v_add_f32_dpp v84, v84, v84 row_half_mirror row_mask:0xf bank_mask:0xf
Maaa 24,168,252
Mvaa 220,164,252
v_add_f32_dpp v84, v84, v84 row_mirror row_mask:0xf bank_mask:0xf
v_mov_b32 v81, v84
Mvaa 216,160,252
W 11
Mvaa 160,192,224
v_permlane16_swap_b32 v84, v81
A 84,84,81
Mvaa 164,196,224
K 80,84,84
v_lshlrev_b32 v80, 16, v80
Mvaa 224,200,224
s_lshl_b32 s80, s15, 3
Ta 236,23,24576
Mvaa 228,204,224
Ta 238,23,25600
W 7
Maaa 32,208,224
v_add_u32 v82, s80, v82
v_sub_u32 v83, 2047, v82
Maaa 36,212,224
v_ashrrev_i32 v81, 31, v80
v_or_b32 v81, 0x80000000, v81
Maaa 96,216,224
v_xor_b32 v80, v80, v81
v_and_b32 v80, 0xffff0000, v80
Maaa 100,220,224
v_or_b32 v80, v80, v83
v_lshlrev_b32 v81, 23, v88
Maaa 108,220,228
v_lshl_or_b32 v82, v82, 2, v81
Ta 240,24,24576
Maaa 104,216,228
Ta 242,24,25600
s_mov_b32 s82, 2752512
Maaa 44,212,228
buffer_store_dword v80, v82, s[8:11], s82 offen sc1
Maaa 40,208,228
Mvaa 236,204,228
Ta 244,25,24576
Ta 246,25,25600
Mvaa 232,200,228
Mvaa 172,196,228
Ta 248,26,24576
Ta 250,26,25600
Mvaa 168,192,228
W 8
Mvaa 176,192,232
Ta 252,27,24576
Ta 254,27,25600
Mvaa 180,196,232
Mvaa 240,200,232
Mvaa 244,204,232
Maaa 48,208,232
Maaa 52,212,232
Maaa 112,216,232
Maaa 116,220,232
W 8
Maaa 124,220,236
Maaa 120,216,236
Maaa 60,212,236
s_waitcnt vmcnt(1) lgkmcnt(0)
s_barrier
Maaa 56,208,236
s_mov_b32 s64, 512
v_bfe_u32 v80, v1, 4, 3
Mvaa 252,204,236
v_mul_u32_u24 v81, 1552, v80
Ra 160,18,32768
Mvaa 248,200,236
v_lshrrev_b32 v82, 7, v1
s_add_u32 m0, s61, 0
Mvaa 188,196,236
v_lshl_add_u32 v81, v82, 4, v81
Mvaa 184,192,236
s_lshl_b32 s80, s14, 7
s_add_u32 s80, s80, 131072
Mvaa 192,192,240
Ra 164,18,34816
D 2,40,64,0
Mvaa 196,196,240
v_add_u32 v81, s80, v81
ds_read_b128 v[32:35], v81
Maaa 0,200,240
Rv 36,81,512
Ra 168,18,36864
Maaa 4,204,240
s_and_b32 s80, s14, 2
s_lshl_b32 s80, s80, 7
Maaa 64,208,240
D 3,40,64,1024
v_subrev_u32 v81, s80, v81
Maaa 68,212,240
Rv 40,81,1024
Ra 172,18,38912
Maaa 128,216,240
v_lshlrev_b32 v80, 4, v80
v_and_b32 v81, 3, v82
Maaa 132,220,240
v_lshl_add_u32 v80, v81, 8, v80
Maaa 140,220,244
D 4,40,64,2048
v_lshrrev_b32 v82, 2, v82
Maaa 136,216,244
Ta 224,20,49152
Ta 226,20,50176
Maaa 76,212,244
Ra 176,18,40960
v_lshl_add_u32 v80, v82, 12, v80
Maaa 72,208,244
s_lshr_b32 s82, s15, 3
s_mul_i32 s82, s82, 81920
Maaa 12,204,244
D 5,40,64,3072
s_add_u32 m0, s61, 4096
Maaa 8,200,244
s_bfe_u32 s80, s15, 0x20001
Ra 180,18,43008
Mvaa 204,196,244
s_lshl_b32 s80, s80, 10
s_add_u32 s82, s82, s80
Mvaa 200,192,244
s_and_b32 s80, s15, 1
s_lshl_b32 s80, s80, 7
Mvaa 208,192,248
D 6,40,64,0
Ra 184,18,45056
Mvaa 212,196,248
s_add_u32 s82, s82, s80
s_add_u32 s82, s82, 131072
Maaa 16,200,248
Ta 228,21,49152
Ta 230,21,50176
Maaa 20,204,248
s_lshl_b32 s80, s14, 13
s_add_u32 s83, s82, s80
Maaa 80,208,248
D 7,40,64,1024
Ra 188,18,47104
Maaa 84,212,248
W 9
buffer_store_dwordx4 v[32:35], v80, s[8:11], s83 offen sc1
Maaa 144,216,248
s_add_u32 s83, s83, 32768
Maaa 148,220,248
Maaa 156,220,252
D 8,40,64,2048
Ta 232,22,49152
Maaa 152,216,252
Ta 234,22,50176
Maaa 92,212,252
Maaa 88,208,252
Maaa 28,204,252
buffer_store_dwordx4 v[36:39], v80, s[8:11], s83 offen sc1
s_and_b32 s80, s80, 8192
Maaa 24,200,252
s_add_u32 s83, s82, s80
s_add_u32 s83, s83, 65536
Mvaa 220,196,252
Mvaa 216,192,252
W 8
Mvaa 160,160,224
D 9,40,64,3072
s_mov_b32 s65, 2097152
Mvaa 164,164,224
s_add_u32 m0, s62, 0
Ta 236,23,49152
Mvaa 224,168,224
Ta 238,23,50176
Mvaa 228,172,224
W 4
Maaa 32,176,224
buffer_store_dwordx4 v[40:43], v80, s[8:11], s83 offen sc1
Maaa 36,180,224
Maaa 96,184,224
Maaa 100,188,224
Maaa 108,188,228
D 10,44,65,0
Ta 240,24,49152
Maaa 104,184,228
Ta 242,24,50176
Maaa 44,180,228
Maaa 40,176,228
Mvaa 236,172,228
D 11,44,65,1024
Mvaa 232,168,228
Mvaa 172,164,228
Mvaa 168,160,228
W 4
Mvaa 176,160,232
D 12,44,65,2048
Ta 244,25,49152
Mvaa 180,164,232
Ta 246,25,50176
Mvaa 240,168,232
Mvaa 244,172,232
Maaa 48,176,232
D 13,44,65,3072
s_add_u32 m0, s62, 4096
Maaa 52,180,232
Maaa 112,184,232
Maaa 116,188,232
W 4
Maaa 124,188,236
D 14,44,65,0
Ta 248,26,49152
Maaa 120,184,236
Ta 250,26,50176
Maaa 60,180,236
Maaa 56,176,236
Mvaa 252,172,236
D 15,44,65,1024
Ra 192,19,32768
Mvaa 248,168,236
Mvaa 188,164,236
Mvaa 184,160,236
Ra 196,19,34816
W 6
Mvaa 192,160,240
D 16,44,65,2048
Ta 252,27,49152
Mvaa 196,164,240
Ta 254,27,50176
Maaa 0,168,240
Ra 200,19,36864
Maaa 4,172,240
Maaa 64,176,240
D 17,44,65,3072
Maaa 68,180,240
Ra 204,19,38912
Maaa 128,184,240
Maaa 132,188,240
W 8
Maaa 140,188,244
global_load_dword v94, v[92:93], off
v_lshl_add_u64 v[92:93], v[92:93], 0, s[68:69]
Maaa 136,184,244
Ta 224,20,57344
Ta 226,20,58368
Maaa 76,180,244
)"
R"(Ra 208,19,40960
Maaa 72,176,244
Ra 212,19,43008
Maaa 12,172,244
Maaa 8,168,244
Mvaa 204,164,244
Ra 216,19,45056
Mvaa 200,160,244
W 11
Mvaa 208,160,248
Ta 228,21,57344
Ta 230,21,58368
Mvaa 212,164,248
Ra 220,19,47104
Maaa 16,168,248
Maaa 20,172,248
Maaa 80,176,248
Maaa 84,180,248
Maaa 144,184,248
Maaa 148,188,248
W 10
Maaa 156,188,252
Ta 232,22,57344
Ta 234,22,58368
Maaa 152,184,252
Maaa 92,180,252
Maaa 88,176,252
Maaa 28,172,252
Maaa 24,168,252
Mvaa 220,164,252
Mvaa 216,160,252
W 8
Mvaa 160,192,224
Ta 236,23,57344
Ta 238,23,58368
Mvaa 164,196,224
Mvaa 224,200,224
Mvaa 228,204,224
W 4
Maaa 32,208,224
Maaa 36,212,224
Maaa 96,216,224
Maaa 100,220,224
Maaa 108,220,228
Ta 240,24,57344
Ta 242,24,58368
Maaa 104,216,228
Maaa 44,212,228
Maaa 40,208,228
Mvaa 236,204,228
Ta 244,25,57344
Ta 246,25,58368
Mvaa 232,200,228
Mvaa 172,196,228
Ta 248,26,57344
Ta 250,26,58368
Mvaa 168,192,228
W 8
Mvaa 176,192,232
Ta 252,27,57344
Ta 254,27,58368
Mvaa 180,196,232
Mvaa 240,200,232
Mvaa 244,204,232
Maaa 48,208,232
Maaa 52,212,232
Maaa 112,216,232
Maaa 116,220,232
W 8
Maaa 124,220,236
Maaa 120,216,236
Maaa 60,212,236
s_waitcnt vmcnt(1) lgkmcnt(0)
s_barrier
s_cmp_eq_u32 s14, 0
s_cbranch_scc1 .Lpub_11%=
.Lpubr_12%=:
Maaa 56,208,236
s_mov_b32 s64, 640
Mvaa 252,204,236
Ra 160,18,0
Mvaa 248,200,236
s_add_u32 m0, s61, 32768
Mvaa 188,196,236
Mvaa 184,192,236
Ra 164,18,2048
Mvaa 192,192,240
D 2,40,64,0
Mvaa 196,196,240
Maaa 0,200,240
Ra 168,18,4096
Maaa 4,204,240
Maaa 64,208,240
D 3,40,64,1024
Maaa 68,212,240
Ra 172,18,6144
Maaa 128,216,240
Maaa 132,220,240
Maaa 140,220,244
D 4,40,64,2048
Ta 224,20,16384
Maaa 136,216,244
Ta 226,20,17408
Ra 176,18,8192
Maaa 76,212,244
Maaa 72,208,244
Ra 180,18,10240
Maaa 12,204,244
D 5,40,64,3072
s_add_u32 m0, s61, 36864
Maaa 8,200,244
Mvaa 204,196,244
Ra 184,18,12288
Mvaa 200,192,244
Mvaa 208,192,248
D 6,40,64,0
Ta 228,21,16384
Mvaa 212,196,248
Ta 230,21,17408
Ra 188,18,14336
Maaa 16,200,248
Maaa 20,204,248
Maaa 80,208,248
D 7,40,64,1024
Maaa 84,212,248
Maaa 144,216,248
Maaa 148,220,248
Maaa 156,220,252
D 8,40,64,2048
Ta 232,22,16384
Maaa 152,216,252
Ta 234,22,17408
Maaa 92,212,252
Maaa 88,208,252
Maaa 28,204,252
D 9,40,64,3072
s_mov_b32 s65, 2621440
Maaa 24,200,252
s_add_u32 m0, s62, 32768
Mvaa 220,196,252
Mvaa 216,192,252
W 8
Mvaa 160,160,224
D 10,44,65,0
Ta 236,23,16384
Mvaa 164,164,224
Ta 238,23,17408
Mvaa 224,168,224
Mvaa 228,172,224
W 4
Maaa 32,176,224
D 11,44,65,1024
Maaa 36,180,224
Maaa 96,184,224
Maaa 100,188,224
Maaa 108,188,228
D 12,44,65,2048
Ta 240,24,16384
Maaa 104,184,228
Ta 242,24,17408
Maaa 44,180,228
Maaa 40,176,228
Mvaa 236,172,228
D 13,44,65,3072
s_add_u32 m0, s62, 36864
Mvaa 232,168,228
Mvaa 172,164,228
Mvaa 168,160,228
W 4
Mvaa 176,160,232
D 14,44,65,0
Ta 244,25,16384
Mvaa 180,164,232
Ta 246,25,17408
Mvaa 240,168,232
Mvaa 244,172,232
Maaa 48,176,232
D 15,44,65,1024
Maaa 52,180,232
Maaa 112,184,232
Maaa 116,188,232
W 4
Maaa 124,188,236
D 16,44,65,2048
Ta 248,26,16384
Maaa 120,184,236
Ta 250,26,17408
Maaa 60,180,236
Maaa 56,176,236
Mvaa 252,172,236
D 17,44,65,3072
s_mov_b32 s65, 0
Mvaa 248,168,236
s_add_u32 m0, s63, 131072
Ra 192,19,0
Mvaa 188,164,236
Mvaa 184,160,236
Ra 196,19,2048
W 6
Mvaa 192,160,240
D 10,48,65,0
Ta 252,27,16384
Mvaa 196,164,240
Ta 254,27,17408
Maaa 0,168,240
Ra 200,19,4096
Maaa 4,172,240
Maaa 64,176,240
D 11,48,65,1024
Maaa 68,180,240
Ra 204,19,6144
Maaa 128,184,240
Maaa 132,188,240
W 8
Maaa 140,188,244
D 12,48,65,2048
Ta 224,20,24576
Maaa 136,184,244
Ta 226,20,25600
Ra 208,19,8192
Maaa 76,180,244
Maaa 72,176,244
Ra 212,19,10240
Maaa 12,172,244
D 13,48,65,3072
s_add_u32 m0, s63, 135168
Maaa 8,168,244
Mvaa 204,164,244
Ra 216,19,12288
Mvaa 200,160,244
W 11
Mvaa 208,160,248
D 14,48,65,0
Ta 228,21,24576
Mvaa 212,164,248
Ta 230,21,25600
Ra 220,19,14336
Maaa 16,168,248
Maaa 20,172,248
Maaa 80,176,248
D 15,48,65,1024
Maaa 84,180,248
Maaa 144,184,248
Maaa 148,188,248
W 10
Maaa 156,188,252
D 16,48,65,2048
Ta 232,22,24576
Maaa 152,184,252
Ta 234,22,25600
Maaa 92,180,252
Maaa 88,176,252
Maaa 28,172,252
D 17,48,65,3072
Maaa 24,168,252
Mvaa 220,164,252
Mvaa 216,160,252
W 8
Mvaa 160,192,224
Ta 236,23,24576
Ta 238,23,25600
Mvaa 164,196,224
Mvaa 224,200,224
Mvaa 228,204,224
W 4
Maaa 32,208,224
Maaa 36,212,224
Maaa 96,216,224
Maaa 100,220,224
Maaa 108,220,228
Ta 240,24,24576
Ta 242,24,25600
Maaa 104,216,228
Maaa 44,212,228
Maaa 40,208,228
Mvaa 236,204,228
Ta 244,25,24576
Ta 246,25,25600
Mvaa 232,200,228
Mvaa 172,196,228
Ta 248,26,24576
Ta 250,26,25600
Mvaa 168,192,228
W 8
Mvaa 176,192,232
Ta 252,27,24576
Ta 254,27,25600
Mvaa 180,196,232
Mvaa 240,200,232
Mvaa 244,204,232
Maaa 48,208,232
Maaa 52,212,232
Maaa 112,216,232
Maaa 116,220,232
W 8
Maaa 124,220,236
Maaa 120,216,236
Maaa 60,212,236
s_waitcnt vmcnt(0) lgkmcnt(0)
s_barrier
Maaa 56,208,236
s_mov_b32 s64, 768
Mvaa 252,204,236
Ra 160,18,32768
Mvaa 248,200,236
s_add_u32 m0, s61, 0
Mvaa 188,196,236
Mvaa 184,192,236
Ra 164,18,34816
Mvaa 192,192,240
D 2,40,64,0
v_bfe_u32 v28, v1, 4, 3
Mvaa 196,196,240
v_lshlrev_b32 v28, 3, v28
Maaa 0,200,240
s_lshl_b32 s80, s60, 6
Ra 168,18,36864
Maaa 4,204,240
s_add_u32 s80, s80, 256
Maaa 64,208,240
D 3,40,64,1024
Maaa 68,212,240
Ra 172,18,38912
Maaa 128,216,240
Maaa 132,220,240
Maaa 140,220,244
buffer_load_dwordx2 v[28:29], v28, s[8:11], s80 offen sc1
Ta 224,20,49152
Maaa 136,216,244
Ta 226,20,50176
Ra 176,18,40960
Maaa 76,212,244
Maaa 72,208,244
Ra 180,18,43008
Maaa 12,204,244
D 4,40,64,2048
Maaa 8,200,244
Mvaa 204,196,244
Ra 184,18,45056
Mvaa 200,192,244
Mvaa 208,192,248
D 5,40,64,3072
s_add_u32 m0, s61, 4096
Mvaa 212,196,248
Ta 228,21,49152
Ta 230,21,50176
Maaa 16,200,248
Ra 188,18,47104
Maaa 20,204,248
Maaa 80,208,248
D 6,40,64,0
Maaa 84,212,248
Maaa 144,216,248
Maaa 148,220,248
Maaa 156,220,252
D 7,40,64,1024
Ta 232,22,49152
Maaa 152,216,252
Ta 234,22,50176
Maaa 92,212,252
Maaa 88,208,252
Maaa 28,204,252
D 8,40,64,2048
Maaa 24,200,252
Mvaa 220,196,252
Mvaa 216,192,252
W 8
Mvaa 160,160,224
D 9,40,64,3072
s_mov_b32 s65, 3145728
Mvaa 164,164,224
s_add_u32 m0, s62, 0
Ta 236,23,49152
Mvaa 224,168,224
Ta 238,23,50176
Mvaa 228,172,224
W 4
Maaa 32,176,224
D 10,44,65,0
Maaa 36,180,224
Maaa 96,184,224
Maaa 100,188,224
Maaa 108,188,228
D 11,44,65,1024
Ta 240,24,49152
Maaa 104,184,228
Ta 242,24,50176
Maaa 44,180,228
Maaa 40,176,228
Mvaa 236,172,228
D 12,44,65,2048
Mvaa 232,168,228
Mvaa 172,164,228
Mvaa 168,160,228
W 4
Mvaa 176,160,232
D 13,44,65,3072
s_add_u32 m0, s62, 4096
Mvaa 180,164,232
Ta 244,25,49152
)"
R"(Ta 246,25,50176
Mvaa 240,168,232
Mvaa 244,172,232
Maaa 48,176,232
D 14,44,65,0
Maaa 52,180,232
Maaa 112,184,232
s_waitcnt vmcnt(11)
v_cmp_eq_u64 vcc, s[12:13], v[28:29]
s_cmp_eq_u64 vcc, -1
s_cbranch_scc0 .Lspin_13%=
.Lok_14%=:
Maaa 116,188,232
W 4
Maaa 124,188,236
D 15,44,65,1024
Ta 248,26,49152
Maaa 120,184,236
Ta 250,26,50176
Maaa 60,180,236
Maaa 56,176,236
Mvaa 252,172,236
D 16,44,65,2048
Ra 192,19,32768
Mvaa 248,168,236
Mvaa 188,164,236
Mvaa 184,160,236
Ra 196,19,34816
W 6
Mvaa 192,160,240
D 17,44,65,3072
v_lshlrev_b32 v92, 3, v1
Mvaa 196,164,240
s_lshl_b32 s69, s58, 13
s_add_u32 s68, s66, s69
Maaa 0,168,240
Ta 252,27,49152
Ta 254,27,50176
Maaa 4,172,240
Ra 200,19,36864
Maaa 64,176,240
buffer_load_dword v94, v92, s[8:11], s68 offen sc1
Maaa 68,180,240
Ra 204,19,38912
Maaa 128,184,240
Maaa 132,188,240
W 8
Maaa 140,188,244
Ta 224,20,57344
Ta 226,20,58368
Maaa 136,184,244
Ra 208,19,40960
Maaa 76,180,244
Maaa 72,176,244
Ra 212,19,43008
Maaa 12,172,244
Maaa 8,168,244
Mvaa 204,164,244
Ra 216,19,45056
Mvaa 200,160,244
W 11
Mvaa 208,160,248
Ta 228,21,57344
Ta 230,21,58368
Mvaa 212,164,248
Ra 220,19,47104
Maaa 16,168,248
Maaa 20,172,248
Maaa 80,176,248
Maaa 84,180,248
Maaa 144,184,248
Maaa 148,188,248
W 10
Maaa 156,188,252
Ta 232,22,57344
Ta 234,22,58368
Maaa 152,184,252
Maaa 92,180,252
Maaa 88,176,252
Maaa 28,172,252
Maaa 24,168,252
Mvaa 220,164,252
Mvaa 216,160,252
W 8
Mvaa 160,192,224
Ta 236,23,57344
Ta 238,23,58368
Mvaa 164,196,224
Mvaa 224,200,224
Mvaa 228,204,224
W 4
Maaa 32,208,224
Maaa 36,212,224
Maaa 96,216,224
Maaa 100,220,224
Maaa 108,220,228
Ta 240,24,57344
Ta 242,24,58368
Maaa 104,216,228
Maaa 44,212,228
Maaa 40,208,228
Mvaa 236,204,228
Ta 244,25,57344
Ta 246,25,58368
Mvaa 232,200,228
Mvaa 172,196,228
Ta 248,26,57344
Ta 250,26,58368
Mvaa 168,192,228
W 8
Mvaa 176,192,232
Ta 252,27,57344
Ta 254,27,58368
Mvaa 180,196,232
Mvaa 240,200,232
Mvaa 244,204,232
Maaa 48,208,232
Maaa 52,212,232
Maaa 112,216,232
Maaa 116,220,232
W 8
Maaa 124,220,236
Maaa 120,216,236
Maaa 60,212,236
s_waitcnt vmcnt(1) lgkmcnt(0)
s_barrier
s_cmp_eq_u32 s85, 43
s_cbranch_scc1 .Lrt_16%=
.Lrtr_17%=:
Maaa 56,208,236
s_mov_b32 s64, 896
Mvaa 252,204,236
Ra 160,18,0
Mvaa 248,200,236
s_add_u32 m0, s61, 32768
Mvaa 188,196,236
Mvaa 184,192,236
Ra 164,18,2048
Mvaa 192,192,240
D 2,40,64,0
Mvaa 196,196,240
Maaa 0,200,240
Ra 168,18,4096
Maaa 4,204,240
Maaa 64,208,240
D 3,40,64,1024
Maaa 68,212,240
Ra 172,18,6144
Maaa 128,216,240
Maaa 132,220,240
Maaa 140,220,244
D 4,40,64,2048
Ta 224,20,16384
Maaa 136,216,244
Ta 226,20,17408
Ra 176,18,8192
Maaa 76,212,244
Maaa 72,208,244
Ra 180,18,10240
Maaa 12,204,244
D 5,40,64,3072
s_add_u32 m0, s61, 36864
Maaa 8,200,244
Mvaa 204,196,244
Ra 184,18,12288
Mvaa 200,192,244
Mvaa 208,192,248
D 6,40,64,0
Ta 228,21,16384
Mvaa 212,196,248
Ta 230,21,17408
Ra 188,18,14336
Maaa 16,200,248
Maaa 20,204,248
Maaa 80,208,248
D 7,40,64,1024
Maaa 84,212,248
Maaa 144,216,248
Maaa 148,220,248
Maaa 156,220,252
D 8,40,64,2048
Ta 232,22,16384
Maaa 152,216,252
Ta 234,22,17408
Maaa 92,212,252
Maaa 88,208,252
Maaa 28,204,252
D 9,40,64,3072
s_mov_b32 s65, 3670016
Maaa 24,200,252
s_add_u32 m0, s62, 32768
Mvaa 220,196,252
Mvaa 216,192,252
W 8
Mvaa 160,160,224
D 10,44,65,0
Ta 236,23,16384
Mvaa 164,164,224
Ta 238,23,17408
Mvaa 224,168,224
Mvaa 228,172,224
W 4
Maaa 32,176,224
D 11,44,65,1024
Maaa 36,180,224
Maaa 96,184,224
Maaa 100,188,224
Maaa 108,188,228
D 12,44,65,2048
Ta 240,24,16384
Maaa 104,184,228
Ta 242,24,17408
Maaa 44,180,228
Maaa 40,176,228
Mvaa 236,172,228
D 13,44,65,3072
s_add_u32 m0, s62, 36864
Mvaa 232,168,228
Mvaa 172,164,228
Mvaa 168,160,228
W 4
Mvaa 176,160,232
D 14,44,65,0
Ta 244,25,16384
Mvaa 180,164,232
Ta 246,25,17408
Mvaa 240,168,232
Mvaa 244,172,232
Maaa 48,176,232
D 15,44,65,1024
Maaa 52,180,232
Maaa 112,184,232
Maaa 116,188,232
W 4
Maaa 124,188,236
D 16,44,65,2048
Ta 248,26,16384
Maaa 120,184,236
Ta 250,26,17408
Maaa 60,180,236
Maaa 56,176,236
Mvaa 252,172,236
D 17,44,65,3072
s_add_u32 s68, s68, 32768
Mvaa 248,168,236
Ra 192,19,0
Mvaa 188,164,236
Mvaa 184,160,236
Ra 196,19,2048
W 6
Mvaa 192,160,240
buffer_load_dword v94, v92, s[8:11], s68 offen sc1
v_add_u32 v2, 131072, v20
Mvaa 196,164,240
Ta 252,27,16384
Ta 254,27,17408
Maaa 0,168,240
v_add_u32 v3, 131072, v21
Ra 200,19,4096
Maaa 4,172,240
v_add_u32 v4, 131072, v22
Maaa 64,176,240
v_add_u32 v5, 131072, v23
Maaa 68,180,240
Ra 204,19,6144
Maaa 128,184,240
v_add_u32 v6, 131072, v24
Maaa 132,188,240
W 8
Maaa 140,188,244
Ta 224,20,24576
Ta 226,20,25600
Maaa 136,184,244
Ra 208,19,8192
v_add_u32 v7, 131072, v25
Maaa 76,180,244
v_add_u32 v8, 131072, v26
Maaa 72,176,244
Ra 212,19,10240
Maaa 12,172,244
v_add_u32 v9, 131072, v27
Maaa 8,168,244
Mvaa 204,164,244
Ra 216,19,12288
Mvaa 200,160,244
W 11
Mvaa 208,160,248
Ta 228,21,24576
Ta 230,21,25600
Mvaa 212,164,248
Ra 220,19,14336
Maaa 16,168,248
Maaa 20,172,248
Maaa 80,176,248
Maaa 84,180,248
Maaa 144,184,248
Maaa 148,188,248
W 10
Maaa 156,188,252
Ta 232,22,24576
Ta 234,22,25600
Maaa 152,184,252
Maaa 92,180,252
Maaa 88,176,252
Maaa 28,172,252
Maaa 24,168,252
Mvaa 220,164,252
Mvaa 216,160,252
W 8
Mvaa 160,192,224
Ta 236,23,24576
Ta 238,23,25600
Mvaa 164,196,224
Mvaa 224,200,224
Mvaa 228,204,224
W 4
Maaa 32,208,224
Maaa 36,212,224
Maaa 96,216,224
Maaa 100,220,224
Maaa 108,220,228
Ta 240,24,24576
Ta 242,24,25600
Maaa 104,216,228
Maaa 44,212,228
Maaa 40,208,228
Mvaa 236,204,228
Ta 244,25,24576
Ta 246,25,25600
Mvaa 232,200,228
Mvaa 172,196,228
Ta 248,26,24576
Ta 250,26,25600
Mvaa 168,192,228
W 8
Mvaa 176,192,232
Ta 252,27,24576
Ta 254,27,25600
Mvaa 180,196,232
Mvaa 240,200,232
Mvaa 244,204,232
Maaa 48,208,232
Maaa 52,212,232
Maaa 112,216,232
Maaa 116,220,232
W 8
Maaa 124,220,236
Maaa 120,216,236
Maaa 60,212,236
s_waitcnt vmcnt(1) lgkmcnt(0)
s_barrier
s_cmp_eq_u32 s85, 43
s_cbranch_scc1 .Lrt_18%=
.Lrtr_19%=:
Maaa 56,208,236
s_mov_b32 s65, 1048576
Mvaa 252,204,236
Ra 160,18,32768
Mvaa 248,200,236
s_add_u32 m0, s63, 0
Mvaa 188,196,236
Mvaa 184,192,236
Ra 164,18,34816
Mvaa 192,192,240
D 10,48,65,0
Mvaa 196,196,240
Maaa 0,200,240
Ra 168,18,36864
Maaa 4,204,240
Maaa 64,208,240
D 11,48,65,1024
Maaa 68,212,240
Ra 172,18,38912
Maaa 128,216,240
Maaa 132,220,240
Maaa 140,220,244
D 12,48,65,2048
Ta 224,20,49152
Maaa 136,216,244
Ta 226,20,50176
Ra 176,18,40960
Maaa 76,212,244
Maaa 72,208,244
Ra 180,18,43008
Maaa 12,204,244
D 13,48,65,3072
s_add_u32 m0, s63, 4096
Maaa 8,200,244
Mvaa 204,196,244
Ra 184,18,45056
Mvaa 200,192,244
Mvaa 208,192,248
D 14,48,65,0
Ta 228,21,49152
Mvaa 212,196,248
Ta 230,21,50176
Ra 188,18,47104
Maaa 16,200,248
Maaa 20,204,248
Maaa 80,208,248
)"
R"(D 15,48,65,1024
Maaa 84,212,248
Maaa 144,216,248
Maaa 148,220,248
Maaa 156,220,252
D 16,48,65,2048
Ta 232,22,49152
Maaa 152,216,252
Ta 234,22,50176
Maaa 92,212,252
Maaa 88,208,252
Maaa 28,204,252
D 17,48,65,3072
s_mov_b32 s65, 2097152
Maaa 24,200,252
s_add_u32 m0, s63, 65536
Mvaa 220,196,252
Mvaa 216,192,252
W 8
Mvaa 160,160,224
D 10,48,65,0
Ta 236,23,49152
Mvaa 164,164,224
Ta 238,23,50176
Mvaa 224,168,224
Mvaa 228,172,224
W 4
Maaa 32,176,224
D 11,48,65,1024
Maaa 36,180,224
Maaa 96,184,224
Maaa 100,188,224
Maaa 108,188,228
D 12,48,65,2048
Ta 240,24,49152
Maaa 104,184,228
Ta 242,24,50176
Maaa 44,180,228
Maaa 40,176,228
Mvaa 236,172,228
D 13,48,65,3072
s_add_u32 m0, s63, 69632
Mvaa 232,168,228
Mvaa 172,164,228
Mvaa 168,160,228
W 4
Mvaa 176,160,232
D 14,48,65,0
Ta 244,25,49152
Mvaa 180,164,232
Ta 246,25,50176
Mvaa 240,168,232
Mvaa 244,172,232
Maaa 48,176,232
D 15,48,65,1024
Maaa 52,180,232
Maaa 112,184,232
Maaa 116,188,232
W 4
Maaa 124,188,236
D 16,48,65,2048
Ta 248,26,49152
Maaa 120,184,236
Ta 250,26,50176
Maaa 60,180,236
Maaa 56,176,236
Mvaa 252,172,236
D 17,48,65,3072
s_and_b32 s69, s69, 8192
Mvaa 248,168,236
s_add_u32 s68, s66, s69
Ra 192,19,32768
Mvaa 188,164,236
s_add_u32 s68, s68, 65536
Mvaa 184,160,236
Ra 196,19,34816
W 6
Mvaa 192,160,240
buffer_load_dword v94, v92, s[8:11], s68 offen sc1
Ta 252,27,49152
Mvaa 196,164,240
Ta 254,27,50176
Maaa 0,168,240
Ra 200,19,36864
Maaa 4,172,240
Maaa 64,176,240
Maaa 68,180,240
Ra 204,19,38912
Maaa 128,184,240
Maaa 132,188,240
W 8
Maaa 140,188,244
Ta 224,20,57344
Ta 226,20,58368
Maaa 136,184,244
Ra 208,19,40960
Maaa 76,180,244
Maaa 72,176,244
Ra 212,19,43008
Maaa 12,172,244
Maaa 8,168,244
Mvaa 204,164,244
Ra 216,19,45056
Mvaa 200,160,244
W 11
Mvaa 208,160,248
Ta 228,21,57344
Ta 230,21,58368
Mvaa 212,164,248
Ra 220,19,47104
Maaa 16,168,248
Maaa 20,172,248
Maaa 80,176,248
Maaa 84,180,248
Maaa 144,184,248
Maaa 148,188,248
W 10
Maaa 156,188,252
Ta 232,22,57344
Ta 234,22,58368
Maaa 152,184,252
Maaa 92,180,252
Maaa 88,176,252
Maaa 28,172,252
Maaa 24,168,252
Mvaa 220,164,252
Mvaa 216,160,252
s_and_b32 s80, s14, 2
W 8
Mvaa 160,192,224
Ta 236,23,57344
Ta 238,23,58368
Mvaa 164,196,224
Mvaa 224,200,224
s_lshl_b32 s80, s80, 10
Mvaa 228,204,224
W 4
Maaa 32,208,224
v_add_u32 v18, s80, v1
Maaa 36,212,224
Maaa 96,216,224
Maaa 100,220,224
v_xor_b32 v19, 2048, v18
Maaa 108,220,228
Ta 240,24,57344
Ta 242,24,58368
Maaa 104,216,228
buffer_load_dwordx4 a[160:163], v18, s[8:11], s66 offen offset:0 sc1
Maaa 44,212,228
Maaa 40,208,228
Mvaa 236,204,228
Ta 244,25,57344
Ta 246,25,58368
Mvaa 232,200,228
buffer_load_dwordx4 a[164:167], v18, s[8:11], s66 offen offset:1024 sc1
Mvaa 172,196,228
Ta 248,26,57344
Ta 250,26,58368
Mvaa 168,192,228
W 8
Mvaa 176,192,232
Ta 252,27,57344
Ta 254,27,58368
Mvaa 180,196,232
buffer_load_dwordx4 a[168:171], v19, s[8:11], s66 offen offset:0 sc1
Mvaa 240,200,232
Mvaa 244,204,232
Maaa 48,208,232
Maaa 52,212,232
buffer_load_dwordx4 a[172:175], v19, s[8:11], s66 offen offset:1024 sc1
s_add_u32 s66, s66, 4096
Maaa 112,216,232
Maaa 116,220,232
W 8
Maaa 124,220,236
Maaa 120,216,236
buffer_load_dwordx4 a[176:179], v18, s[8:11], s66 offen offset:0 sc1
Maaa 60,212,236
s_waitcnt vmcnt(6) lgkmcnt(0)
s_barrier
s_cmp_eq_u32 s85, 43
s_cbranch_scc1 .Lrt_22%=
.Lrtr_23%=:
Maaa 56,208,236
Mvaa 252,204,236
Mvaa 248,200,236
buffer_load_dwordx4 a[180:183], v18, s[8:11], s66 offen offset:1024 sc1
Mvaa 188,196,236
Mvaa 184,192,236
Mvaa 192,192,240
Mvaa 196,196,240
buffer_load_dwordx4 a[184:187], v19, s[8:11], s66 offen offset:0 sc1
Maaa 0,200,240
s_mov_b32 s65, 3145728
Maaa 4,204,240
Maaa 64,208,240
Maaa 68,212,240
buffer_load_dwordx4 a[188:191], v19, s[8:11], s66 offen offset:1024 sc1
Maaa 128,216,240
Maaa 132,220,240
Maaa 140,220,244
Maaa 136,216,244
Maaa 76,212,244
Ta 224,2,0
Ta 226,2,1024
Maaa 72,208,244
Maaa 12,204,244
s_add_u32 m0, s63, 32768
Maaa 8,200,244
Mvaa 204,196,244
Ta 228,3,0
Ta 230,3,1024
Mvaa 200,192,244
Mvaa 208,192,248
Mvaa 212,196,248
Maaa 16,200,248
Ta 232,4,0
Ta 234,4,1024
Maaa 20,204,248
Maaa 80,208,248
Maaa 84,212,248
D 10,48,65,0
Maaa 144,216,248
Ta 236,5,0
Ta 238,5,1024
Maaa 148,220,248
Maaa 156,220,252
Maaa 152,216,252
Maaa 92,212,252
Ta 240,6,0
Ta 242,6,1024
Maaa 88,208,252
Maaa 28,204,252
Maaa 24,200,252
Mvaa 220,196,252
D 11,48,65,1024
Mvaa 216,192,252
.Lq_5%=:
s_waitcnt vmcnt(6) lgkmcnt(6)
Zvaa 32,224,160
Ta 244,7,0
Ta 246,7,1024
Zvaa 64,224,164
Ta 248,8,0
Ta 250,8,1024
Zvaa 96,224,168
s_add_u32 s66, s66, 4096
buffer_load_dwordx4 a[192:195], v18, s[8:11], s66 offen offset:0 sc1
Zvaa 128,224,172
Ta 252,9,0
Ta 254,9,1024
Zvaa 132,228,172
Zvaa 100,228,168
Zvaa 68,228,164
buffer_load_dwordx4 a[196:199], v18, s[8:11], s66 offen offset:1024 sc1
Ta 224,2,8192
Zvaa 36,228,160
Ta 226,2,9216
W 10
Zvaa 40,232,160
K 160,160,161
Zvaa 72,232,164
Zvaa 104,232,168
buffer_load_dwordx4 a[200:203], v19, s[8:11], s66 offen offset:0 sc1
Ta 228,3,8192
Zvaa 136,232,172
Ta 230,3,9216
K 161,162,163
Zvaa 140,236,172
Zvaa 108,236,168
Zvaa 76,236,164
buffer_load_dwordx4 a[204:207], v19, s[8:11], s66 offen offset:1024 sc1
s_add_u32 s66, s66, 4096
Zvaa 44,236,160
K 162,164,165
W 8
Zvaa 48,240,160
Ta 232,4,8192
Ta 234,4,9216
Zvaa 80,240,164
K 163,166,167
Zvaa 112,240,168
buffer_load_dwordx4 a[208:211], v18, s[8:11], s66 offen offset:0 sc1
Ta 236,5,8192
Zvaa 144,240,172
Ta 238,5,9216
Zvaa 148,244,172
Zvaa 116,244,168
Zvaa 84,244,164
buffer_load_dwordx4 a[212:215], v18, s[8:11], s66 offen offset:1024 sc1
Ta 240,6,8192
Zvaa 52,244,160
Ta 242,6,9216
W 10
Zvaa 56,248,160
Zvaa 88,248,164
Zvaa 120,248,168
buffer_load_dwordx4 a[216:219], v19, s[8:11], s66 offen offset:0 sc1
Ta 244,7,8192
Zvaa 152,248,172
Ta 246,7,9216
Zvaa 156,252,172
Zvaa 124,252,168
Zvaa 92,252,164
buffer_store_dwordx4 v[160:163], v1, s[8:11], s67 offen offset:0 sc1
K 168,168,169
Zvaa 60,252,160
K 169,170,171
s_waitcnt vmcnt(10) lgkmcnt(8)
Mvaa 32,224,176
Ta 248,8,8192
Ta 250,8,9216
Mvaa 64,224,180
K 170,172,173
Mvaa 96,224,184
buffer_load_dwordx4 a[220:223], v19, s[8:11], s66 offen offset:1024 sc1
s_add_u32 s66, s66, 4096
Mvaa 128,224,188
Ta 252,9,8192
Ta 254,9,9216
Mvaa 132,228,188
K 171,174,175
Mvaa 100,228,184
Mvaa 68,228,180
D 12,48,65,2048
Ta 224,2,16384
Mvaa 36,228,176
Ta 226,2,17408
W 10
Mvaa 40,232,176
Mvaa 72,232,180
Mvaa 104,232,184
D 13,48,65,3072
s_add_u32 m0, s63, 36864
Mvaa 136,232,188
Ta 228,3,16384
Ta 230,3,17408
Mvaa 140,236,188
Mvaa 108,236,184
Mvaa 76,236,180
buffer_load_dwordx4 a[160:163], v18, s[8:11], s66 offen offset:0 sc1
Ta 232,4,16384
Mvaa 44,236,176
Ta 234,4,17408
W 10
Mvaa 48,240,176
Mvaa 80,240,180
buffer_store_dwordx4 v[168:171], v1, s[8:11], s67 offen offset:1024 sc1
K 176,176,177
Mvaa 112,240,184
K 177,178,179
Ta 236,5,16384
Mvaa 144,240,188
Ta 238,5,17408
K 178,180,181
Mvaa 148,244,188
Mvaa 116,244,184
buffer_load_dwordx4 a[164:167], v18, s[8:11], s66 offen offset:1024 sc1
Mvaa 84,244,180
K 179,182,183
Ta 240,6,16384
Mvaa 52,244,176
Ta 242,6,17408
W 10
Mvaa 56,248,176
Mvaa 88,248,180
buffer_load_dwordx4 a[168:171], v19, s[8:11], s66 offen offset:0 sc1
Mvaa 120,248,184
Ta 244,7,16384
Ta 246,7,17408
Mvaa 152,248,188
Mvaa 156,252,188
Mvaa 124,252,184
buffer_load_dwordx4 a[172:175], v19, s[8:11], s66 offen offset:1024 sc1
Mvaa 92,252,180
Ta 248,8,16384
Ta 250,8,17408
Mvaa 60,252,176
s_add_u32 s66, s66, 4096
s_waitcnt vmcnt(12) lgkmcnt(10)
Mvaa 32,224,192
Mvaa 64,224,196
D 14,48,65,0
Mvaa 96,224,200
Ta 252,9,16384
Ta 254,9,17408
Mvaa 128,224,204
buffer_store_dwordx4 v[176:179], v1, s[8:11], s67 offen offset:2048 sc1
K 184,184,185
Mvaa 132,228,204
K 185,186,187
Mvaa 100,228,200
Mvaa 68,228,196
K 186,188,189
Ta 224,2,24576
Mvaa 36,228,192
D 15,48,65,1024
W 9
Mvaa 40,232,192
Ta 226,2,25600
)"
R"(Mvaa 72,232,196
K 187,190,191
Mvaa 104,232,200
Ta 228,3,24576
Ta 230,3,25600
Mvaa 136,232,204
D 16,48,65,2048
Mvaa 140,236,204
Mvaa 108,236,200
Mvaa 76,236,196
Ta 232,4,24576
Ta 234,4,25600
Mvaa 44,236,192
D 17,48,65,3072
W 10
Mvaa 48,240,192
s_mov_b32 s65, 4194304
s_add_u32 m0, s63, 98304
Mvaa 80,240,196
Mvaa 112,240,200
Ta 236,5,24576
Ta 238,5,25600
Mvaa 144,240,204
buffer_load_dwordx4 a[176:179], v18, s[8:11], s66 offen offset:0 sc1
Mvaa 148,244,204
Mvaa 116,244,200
Mvaa 84,244,196
buffer_store_dwordx4 v[184:187], v1, s[8:11], s67 offen offset:3072 sc1
K 192,192,193
Mvaa 52,244,192
K 193,194,195
W 8
Mvaa 56,248,192
Ta 240,6,24576
Ta 242,6,25600
Mvaa 88,248,196
K 194,196,197
Mvaa 120,248,200
buffer_load_dwordx4 a[180:183], v18, s[8:11], s66 offen offset:1024 sc1
Ta 244,7,24576
Mvaa 152,248,204
Ta 246,7,25600
K 195,198,199
Mvaa 156,252,204
Mvaa 124,252,200
s_add_u32 s67, s67, 4096
Mvaa 92,252,196
buffer_load_dwordx4 a[184:187], v19, s[8:11], s66 offen offset:0 sc1
Ta 248,8,24576
Mvaa 60,252,192
Ta 250,8,25600
s_waitcnt vmcnt(16) lgkmcnt(10)
Mvaa 32,224,208
Mvaa 64,224,212
Mvaa 96,224,216
buffer_load_dwordx4 a[188:191], v19, s[8:11], s66 offen offset:1024 sc1
s_add_u32 s66, s66, 4096
Mvaa 128,224,220
Ta 252,9,24576
Ta 254,9,25600
Mvaa 132,228,220
Mvaa 100,228,216
Mvaa 68,228,212
buffer_load_dwordx4 v[184:187], v18, s[8:11], s66 offen offset:0 sc1
Ta 224,20,0
Mvaa 36,228,208
Ta 226,20,1024
W 10
Mvaa 40,232,208
Mvaa 72,232,212
buffer_store_dwordx4 v[192:195], v1, s[8:11], s67 offen offset:0 sc1
K 200,200,201
Mvaa 104,232,216
K 201,202,203
Ta 228,21,0
Mvaa 136,232,220
Ta 230,21,1024
K 202,204,205
Mvaa 140,236,220
Mvaa 108,236,216
buffer_load_dwordx4 v[188:191], v18, s[8:11], s66 offen offset:1024 sc1
Mvaa 76,236,212
K 203,206,207
Ta 232,22,0
Mvaa 44,236,208
Ta 234,22,1024
W 10
Mvaa 48,240,208
Mvaa 80,240,212
D 10,48,65,0
Mvaa 112,240,216
Ta 236,23,0
Ta 238,23,1024
Mvaa 144,240,220
Mvaa 148,244,220
Mvaa 116,244,216
buffer_load_dwordx4 a[192:195], v19, s[8:11], s66 offen offset:0 sc1
Mvaa 84,244,212
Ta 240,24,0
Ta 242,24,1024
Mvaa 52,244,208
W 10
Mvaa 56,248,208
Mvaa 88,248,212
buffer_load_dwordx4 a[196:199], v19, s[8:11], s66 offen offset:1024 sc1
s_add_u32 s66, s66, 4096
Mvaa 120,248,216
Ta 244,25,0
Ta 246,25,1024
Mvaa 152,248,220
buffer_store_dwordx4 v[200:203], v1, s[8:11], s67 offen offset:1024 sc1
K 208,208,209
Mvaa 156,252,220
K 209,210,211
Mvaa 124,252,216
Mvaa 92,252,212
K 210,212,213
Ta 248,26,0
Mvaa 60,252,208
buffer_load_dwordx4 a[200:203], v18, s[8:11], s66 offen offset:0 sc1
s_waitcnt vmcnt(18) lgkmcnt(9)
Mvaa 32,224,160
Ta 250,26,1024
Mvaa 64,224,164
K 211,214,215
Mvaa 96,224,168
Ta 252,27,0
Ta 254,27,1024
Mvaa 128,224,172
buffer_load_dwordx4 a[204:207], v18, s[8:11], s66 offen offset:1024 sc1
Mvaa 132,228,172
Mvaa 100,228,168
Mvaa 68,228,164
Ta 224,20,8192
Ta 226,20,9216
Mvaa 36,228,160
D 11,48,65,1024
W 10
Mvaa 40,232,160
v_add_u32 v2, 65536, v20
Mvaa 72,232,164
Mvaa 104,232,168
Ta 228,21,8192
Ta 230,21,9216
Mvaa 136,232,172
buffer_load_dwordx4 v[192:195], v19, s[8:11], s66 offen offset:0 sc1
v_add_u32 v3, 65536, v21
Mvaa 140,236,172
Mvaa 108,236,168
Mvaa 76,236,164
buffer_store_dwordx4 v[208:211], v1, s[8:11], s67 offen offset:2048 sc1
K 216,216,217
Mvaa 44,236,160
K 217,218,219
W 8
Mvaa 48,240,160
Ta 232,22,8192
Ta 234,22,9216
Mvaa 80,240,164
v_add_u32 v4, 65536, v22
K 218,220,221
Mvaa 112,240,168
buffer_load_dwordx4 v[196:199], v19, s[8:11], s66 offen offset:1024 sc1
s_add_u32 s66, s66, 4096
Mvaa 144,240,172
Ta 236,23,8192
Ta 238,23,9216
Mvaa 148,244,172
v_add_u32 v5, 65536, v23
K 219,222,223
Mvaa 116,244,168
Mvaa 84,244,164
D 12,48,65,2048
Ta 240,24,8192
Mvaa 52,244,160
Ta 242,24,9216
W 10
Mvaa 56,248,160
v_add_u32 v6, 65536, v24
Mvaa 88,248,164
Mvaa 120,248,168
D 13,48,65,3072
s_add_u32 m0, s63, 102400
Mvaa 152,248,172
Ta 244,25,8192
Ta 246,25,9216
Mvaa 156,252,172
v_add_u32 v7, 65536, v25
Mvaa 124,252,168
Mvaa 92,252,164
buffer_load_dwordx4 v[200:203], v18, s[8:11], s66 offen offset:0 sc1
Ta 248,26,8192
Mvaa 60,252,160
Ta 250,26,9216
s_waitcnt vmcnt(16) lgkmcnt(10)
Mvaa 32,224,176
v_add_u32 v8, 65536, v26
Mvaa 64,224,180
buffer_store_dwordx4 v[216:219], v1, s[8:11], s67 offen offset:3072 sc1
K 224,224,225
Mvaa 96,224,184
K 225,226,227
K 226,228,229
Mvaa 128,224,188
Ta 252,27,8192
Ta 254,27,9216
Mvaa 132,228,188
v_add_u32 v9, 65536, v27
K 227,230,231
Mvaa 100,228,184
buffer_load_dwordx4 v[204:207], v18, s[8:11], s66 offen offset:1024 sc1
Mvaa 68,228,180
Ta 224,20,16384
Ta 226,20,17408
Mvaa 36,228,176
s_add_u32 s67, s67, s70
W 10
Mvaa 40,232,176
Mvaa 72,232,180
buffer_load_dwordx4 a[208:211], v19, s[8:11], s66 offen offset:0 sc1
Mvaa 104,232,184
Ta 228,21,16384
Ta 230,21,17408
Mvaa 136,232,188
Mvaa 140,236,188
Mvaa 108,236,184
buffer_load_dwordx4 a[212:215], v19, s[8:11], s66 offen offset:1024 sc1
s_add_u32 s66, s66, 4096
Mvaa 76,236,180
Ta 232,22,16384
Ta 234,22,17408
Mvaa 44,236,176
W 10
Mvaa 48,240,176
Mvaa 80,240,180
buffer_load_dwordx4 a[216:219], v18, s[8:11], s66 offen offset:0 sc1
Mvaa 112,240,184
Ta 236,23,16384
Ta 238,23,17408
Mvaa 144,240,188
buffer_store_dwordx4 v[224:227], v1, s[8:11], s67 offen offset:0 sc1
K 232,232,233
Mvaa 148,244,188
K 233,234,235
Mvaa 116,244,184
Mvaa 84,244,180
K 234,236,237
Ta 240,24,16384
Mvaa 52,244,176
D 14,48,65,0
W 9
Mvaa 56,248,176
Ta 242,24,17408
Mvaa 88,248,180
K 235,238,239
Mvaa 120,248,184
Ta 244,25,16384
Ta 246,25,17408
Mvaa 152,248,188
buffer_load_dwordx4 a[220:223], v18, s[8:11], s66 offen offset:1024 sc1
Mvaa 156,252,188
Mvaa 124,252,184
Mvaa 92,252,180
Ta 248,26,16384
Ta 250,26,17408
Mvaa 60,252,176
D 15,48,65,1024
s_waitcnt vmcnt(19) lgkmcnt(10)
Mvav 32,224,184
Mvav 64,224,188
Mvaa 96,224,192
Ta 252,27,16384
Ta 254,27,17408
Mvaa 128,224,196
buffer_load_dwordx4 v[208:211], v19, s[8:11], s66 offen offset:0 sc1
Mvaa 132,228,196
Mvaa 100,228,192
Mvav 68,228,188
buffer_store_dwordx4 v[232:235], v1, s[8:11], s67 offen offset:1024 sc1
K 240,240,241
Mvav 36,228,184
K 241,242,243
W 8
Mvav 40,232,184
Ta 224,20,24576
Ta 226,20,25600
Mvav 72,232,188
K 242,244,245
Mvaa 104,232,192
D 16,48,65,2048
Ta 228,21,24576
Mvaa 136,232,196
Ta 230,21,25600
K 243,246,247
Mvaa 140,236,196
Mvaa 108,236,192
Mvav 76,236,188
buffer_load_dwordx4 v[212:215], v19, s[8:11], s66 offen offset:1024 sc1
s_add_u32 s66, s66, 4096
Mvav 44,236,184
Ta 232,22,24576
W 9
Mvav 48,240,184
Ta 234,22,25600
Mvav 80,240,188
Mvaa 112,240,192
buffer_load_dwordx4 a[160:163], v18, s[8:11], s66 offen offset:0 sc1
Ta 236,23,24576
Mvaa 144,240,196
Ta 238,23,25600
Mvaa 148,244,196
Mvaa 116,244,192
Mvav 84,244,188
D 17,48,65,3072
Ta 240,24,24576
Mvav 52,244,184
Ta 242,24,25600
W 10
Mvav 56,248,184
buffer_store_dwordx4 v[240:243], v1, s[8:11], s67 offen offset:2048 sc1
K 248,248,249
Mvav 88,248,188
K 249,250,251
Mvaa 120,248,192
Ta 244,25,24576
Ta 246,25,25600
Mvaa 152,248,196
K 250,252,253
Mvaa 156,252,196
buffer_load_dwordx4 a[164:167], v18, s[8:11], s66 offen offset:1024 sc1
Mvaa 124,252,192
Mvav 92,252,188
K 251,254,255
Ta 248,26,24576
Mvav 60,252,184
Ta 250,26,25600
s_waitcnt vmcnt(20) lgkmcnt(10)
Mvaa 32,224,200
buffer_load_dwordx4 a[168:171], v19, s[8:11], s66 offen offset:0 sc1
Mvaa 64,224,204
Mvav 96,224,192
Ta 252,27,24576
Ta 254,27,25600
Mvav 128,224,196
Mvav 132,228,196
buffer_load_dwordx4 a[172:175], v19, s[8:11], s66 offen offset:1024 sc1
s_add_u32 s66, s66, 4096
Mvav 100,228,192
Mvaa 68,228,204
Ta 224,2,0
Ta 226,2,1024
Mvaa 36,228,200
W 10
Mvaa 40,232,200
buffer_load_dwordx4 v[216:219], v18, s[8:11], s66 offen offset:0 sc1
Mvaa 72,232,204
Mvav 104,232,192
Ta 228,3,0
Ta 230,3,1024
Mvav 136,232,196
buffer_store_dwordx4 v[248:251], v1, s[8:11], s67 offen offset:3072 sc1
G 160,0
Mvav 140,236,196
G 161,1
G 162,2
Mvav 108,236,192
G 163,3
G 164,4
Mvaa 76,236,204
G 165,5
Ta 232,4,0
Mvaa 44,236,200
buffer_load_dwordx4 v[220:223], v18, s[8:11], s66 offen offset:1024 sc1
W 9
Mvaa 48,240,200
Ta 234,4,1024
G 166,6
Mvaa 80,240,204
G 167,7
K 160,160,161
Mvav 112,240,192
K 161,162,163
Ta 236,5,0
Mvav 144,240,196
buffer_load_dwordx4 v[224:227], v19, s[8:11], s66 offen offset:0 sc1
Ta 238,5,1024
Mvav 148,244,196
K 162,164,165
K 163,166,167
Mvav 116,244,192
s_add_u32 s67, s67, 4096
Mvaa 84,244,204
Ta 240,6,0
Ta 242,6,1024
Mvaa 52,244,200
buffer_load_dwordx4 v[228:231], v19, s[8:11], s66 offen offset:1024 sc1
W 10
Mvaa 56,248,200
s_add_u32 s66, s66, 4096
Mvaa 88,248,204
)"
R"(Mvav 120,248,192
Ta 244,7,0
Ta 246,7,1024
Mvav 152,248,196
buffer_load_dwordx4 a[176:179], v18, s[8:11], s66 offen offset:0 sc1
Mvav 156,252,196
Mvav 124,252,192
Mvaa 92,252,204
buffer_store_dwordx4 v[160:163], v1, s[8:11], s67 offen offset:0 sc1
G 168,8
Mvaa 60,252,200
G 169,9
s_waitcnt vmcnt(22) lgkmcnt(8)
Mvav 32,224,200
G 170,10
G 171,11
Mvav 64,224,204
Ta 248,8,0
Ta 250,8,1024
Mvaa 96,224,208
buffer_load_dwordx4 a[180:183], v18, s[8:11], s66 offen offset:1024 sc1
G 172,12
Mvaa 128,224,212
G 173,13
G 174,14
Mvaa 132,228,212
s_lshl_b32 s80, s56, 9
G 175,15
Mvaa 100,228,208
s_lshl_b32 s81, s57, 5
Ta 252,9,0
Mvav 68,228,204
buffer_load_dwordx4 a[184:187], v19, s[8:11], s66 offen offset:0 sc1
Ta 254,9,1024
Mvav 36,228,200
s_add_u32 s80, s80, s81
W 8
Mvav 40,232,200
K 168,168,169
s_lshl_b32 s81, s14, 3
Mvav 72,232,204
K 169,170,171
s_add_u32 s80, s80, s81
Mvaa 104,232,208
buffer_load_dwordx4 a[188:191], v19, s[8:11], s66 offen offset:1024 sc1
s_add_u32 s66, s66, 4096
Mvaa 136,232,212
s_add_u32 s71, s80, 4096
K 170,172,173
Mvaa 140,236,212
K 171,174,175
Ta 224,2,8192
Mvaa 108,236,208
Ta 226,2,9216
Ta 228,3,8192
Mvav 76,236,204
buffer_load_dwordx4 v[232:235], v18, s[8:11], s66 offen offset:0 sc1
Ta 230,3,9216
Mvav 44,236,200
Ta 232,4,8192
W 9
Mvav 48,240,200
buffer_store_dwordx4 v[168:171], v1, s[8:11], s67 offen offset:1024 sc1
G 176,16
Mvav 80,240,204
Ta 234,4,9216
G 177,17
Mvaa 112,240,208
G 178,18
G 179,19
Mvaa 144,240,212
G 180,20
Ta 236,5,8192
Mvaa 148,244,212
v_mov_b64 v[28:29], s[12:13]
s_mov_b64 exec, 1
buffer_store_dwordx2 v[28:29], off, s[8:11], s71 sc1
s_mov_b64 exec, -1
Mvaa 116,244,208
s_lshr_b32 s80, s15, 3
Ta 238,5,9216
Mvav 84,244,204
G 181,21
G 182,22
Mvav 52,244,200
s_lshl_b32 s80, s80, 2
W 8
Mvav 56,248,200
buffer_load_dwordx4 v[236:239], v18, s[8:11], s66 offen offset:1024 sc1
G 183,23
Mvav 88,248,204
K 176,176,177
Ta 240,6,8192
Mvaa 120,248,208
Ta 242,6,9216
s_add_u32 s80, s80, s14
Mvaa 152,248,212
K 177,178,179
K 178,180,181
Mvaa 156,252,212
buffer_load_dwordx4 v[240:243], v19, s[8:11], s66 offen offset:0 sc1
s_mul_i32 s80, s80, 1536
Mvaa 124,252,208
K 179,182,183
Ta 244,7,8192
Mvav 92,252,204
Ta 246,7,9216
v_min_u32 v28, 176, v1
Mvav 60,252,200
v_lshl_add_u32 v28, v28, 3, s80
s_waitcnt vmcnt(21) lgkmcnt(8)
Mvaa 32,224,216
buffer_load_dwordx4 v[244:247], v19, s[8:11], s66 offen offset:1024 sc1
Ta 248,8,8192
Mvaa 64,224,220
Ta 250,8,9216
s_add_u32 s66, s66, 4096
Mvav 96,224,208
v_min_u32 v28, .Lend%=-.Lbeg%=-4, v28
Ta 252,9,8192
Mvav 128,224,212
buffer_store_dwordx4 v[176:179], v1, s[8:11], s67 offen offset:2048 sc1
G 160,24
Mvav 132,228,212
G 161,25
Ta 254,9,9216
Mvav 100,228,208
G 162,26
G 163,27
Mvaa 68,228,220
G 164,28
G 165,29
Mvaa 36,228,216
buffer_load_dwordx4 v[184:187], v18, s[8:11], s66 offen offset:0 sc1
W 8
Mvaa 40,232,216
Ta 224,2,16384
Ta 226,2,17408
Mvaa 72,232,220
G 166,30
G 167,31
Mvav 104,232,208
K 160,160,161
K 161,162,163
Mvav 136,232,212
global_load_dword v29, v28, s[6:7]
Ta 228,3,16384
Mvav 140,236,212
Ta 230,3,17408
K 162,164,165
Mvav 108,236,208
K 163,166,167
Mvaa 76,236,220
Ta 232,4,16384
Ta 234,4,17408
Mvaa 44,236,216
buffer_load_dwordx4 v[188:191], v18, s[8:11], s66 offen offset:1024 sc1
W 10
Mvaa 48,240,216
Mvaa 80,240,220
Mvav 112,240,208
Ta 236,5,16384
Ta 238,5,17408
Mvav 144,240,212
buffer_store_dwordx4 v[160:163], v1, s[8:11], s67 offen offset:3072 sc1
G 168,32
Mvav 148,244,212
G 169,33
G 170,34
Mvav 116,244,208
G 171,35
Mvaa 84,244,220
Ta 240,6,16384
Ta 242,6,17408
Mvaa 52,244,216
buffer_load_dwordx4 a[192:195], v19, s[8:11], s66 offen offset:0 sc1
W 10
Mvaa 56,248,216
G 172,36
G 173,37
Mvaa 88,248,220
G 174,38
G 175,39
Mvav 120,248,208
Ta 244,7,16384
Ta 246,7,17408
Mvav 152,248,212
buffer_load_dwordx4 a[196:199], v19, s[8:11], s66 offen offset:1024 sc1
K 168,168,169
Mvav 156,252,212
K 169,170,171
K 170,172,173
Mvav 124,252,208
K 171,174,175
Mvaa 92,252,220
Ta 248,8,16384
Ta 250,8,17408
Mvaa 60,252,216
s_add_u32 s67, s67, s70
s_waitcnt vmcnt(23) lgkmcnt(10)
Mvaa 32,224,160
buffer_store_dwordx4 v[168:171], v1, s[8:11], s67 offen offset:0 sc1
s_add_u32 s66, s66, 4096
Mvaa 64,224,164
Mvaa 96,224,168
Ta 252,9,16384
Ta 254,9,17408
Mvaa 128,224,172
Mvaa 132,228,172
buffer_load_dwordx4 v[248:251], v18, s[8:11], s66 offen offset:0 sc1
Mvaa 100,228,168
G 176,40
Mvaa 68,228,164
Ta 224,2,24576
Ta 226,2,25600
Mvaa 36,228,160
G 177,41
W 10
Mvaa 40,232,160
G 178,42
buffer_load_dwordx4 v[252:255], v18, s[8:11], s66 offen offset:1024 sc1
Mvaa 72,232,164
G 179,43
Mvaa 104,232,168
G 180,44
Ta 228,3,24576
Mvaa 136,232,172
Ta 230,3,25600
G 181,45
Mvaa 140,236,172
G 182,46
Mvaa 108,236,168
G 183,47
Mvaa 76,236,164
K 176,176,177
Ta 232,4,24576
Mvaa 44,236,160
Ta 234,4,25600
W 10
Mvaa 48,240,160
K 177,178,179
K 178,180,181
Mvaa 80,240,164
buffer_load_dwordx4 a[0:3], v19, s[8:11], s66 offen offset:0 sc1
K 179,182,183
Mvaa 112,240,168
Ta 236,5,24576
Ta 238,5,25600
Mvaa 144,240,172
Mvaa 148,244,172
Mvaa 116,244,168
buffer_store_dwordx4 v[176:179], v1, s[8:11], s67 offen offset:1024 sc1
Mvaa 84,244,164
Ta 240,6,24576
Ta 242,6,25600
Mvaa 52,244,160
G 160,48
W 10
Mvaa 56,248,160
Mvaa 88,248,164
buffer_load_dwordx4 a[4:7], v19, s[8:11], s66 offen offset:1024 sc1
G 161,49
Mvaa 120,248,168
G 162,50
Ta 244,7,24576
Mvaa 152,248,172
Ta 246,7,25600
G 163,51
Mvaa 156,252,172
G 164,52
Mvaa 124,252,168
G 165,53
Mvaa 92,252,164
G 166,54
Ta 248,8,24576
Mvaa 60,252,160
Ta 250,8,25600
s_waitcnt vmcnt(24) lgkmcnt(10)
Mvav 32,224,216
G 167,55
K 160,160,161
Mvav 64,224,220
s_add_u32 s66, s66, 4096
buffer_load_dwordx4 a[200:203], v18, s[8:11], s66 offen offset:0 sc1
Mvav 96,224,224
K 161,162,163
K 162,164,165
Mvav 128,224,228
Ta 252,9,24576
Ta 254,9,25600
Mvav 132,228,228
K 163,166,167
Mvav 100,228,224
buffer_store_dwordx4 v[160:163], v1, s[8:11], s67 offen offset:2048 sc1
Mvav 68,228,220
Mvav 36,228,216
W 8
Mvav 40,232,216
Mvav 72,232,220
buffer_load_dwordx4 a[204:207], v18, s[8:11], s66 offen offset:1024 sc1
Mvav 104,232,224
G 168,56
Mvav 136,232,228
Mvav 140,236,228
W 0
s_barrier
Mvav 108,236,224
G 169,57
Ta 224,20,32768
Mvav 76,236,220
Ta 226,20,33792
Ta 228,21,32768
Mvav 44,236,216
Ta 230,21,33792
G 170,58
Mvav 48,240,216
G 171,59
Ta 232,22,32768
Mvav 80,240,220
Ta 234,22,33792
G 172,60
Mvav 112,240,224
G 173,61
buffer_load_dwordx4 v[192:195], v19, s[8:11], s66 offen offset:0 sc1
Mvav 144,240,228
G 174,62
G 175,63
Mvav 148,244,228
Ta 236,23,32768
Ta 238,23,33792
Mvav 116,244,224
K 168,168,169
K 169,170,171
Mvav 84,244,220
K 170,172,173
K 171,174,175
Mvav 52,244,216
Ta 240,24,32768
Ta 242,24,33792
Mvav 56,248,216
buffer_store_dwordx4 v[168:171], v1, s[8:11], s67 offen offset:3072 sc1
Mvav 88,248,220
Mvav 120,248,224
Ta 244,25,32768
Ta 246,25,33792
Mvav 152,248,228
Mvav 156,252,228
buffer_load_dwordx4 v[196:199], v19, s[8:11], s66 offen offset:1024 sc1
Mvav 124,252,224
G 176,64
Mvav 92,252,220
Ta 248,26,32768
Ta 250,26,33792
Mvav 60,252,216
G 177,65
s_waitcnt vmcnt(25) lgkmcnt(10)
Mvaa 32,224,176
G 178,66
s_add_u32 s66, s66, 4096
Mvaa 64,224,180
buffer_load_dwordx4 a[8:11], v18, s[8:11], s66 offen offset:0 sc1
G 179,67
Mvaa 96,224,184
G 180,68
Ta 252,27,32768
Mvaa 128,224,188
Ta 254,27,33792
G 181,69
Mvaa 132,228,188
G 182,70
G 183,71
Mvaa 100,228,184
K 176,176,177
Mvaa 68,228,180
Ta 224,20,40960
Ta 226,20,41984
Mvaa 36,228,176
K 177,178,179
W 10
Mvaa 40,232,176
K 178,180,181
buffer_load_dwordx4 a[12:15], v18, s[8:11], s66 offen offset:1024 sc1
Mvaa 72,232,180
K 179,182,183
s_add_u32 s67, s67, 4096
Mvaa 104,232,184
Ta 228,21,40960
Ta 230,21,41984
Mvaa 136,232,188
Mvaa 140,236,188
buffer_store_dwordx4 v[176:179], v1, s[8:11], s67 offen offset:0 sc1
Mvaa 108,236,184
Mvaa 76,236,180
Ta 232,22,40960
Ta 234,22,41984
Mvaa 44,236,176
G 160,72
W 10
Mvaa 48,240,176
buffer_load_dwordx4 v[200:203], v19, s[8:11], s66 offen offset:0 sc1
)"
R"(Mvaa 80,240,180
G 161,73
Mvaa 112,240,184
G 162,74
Ta 236,23,40960
Mvaa 144,240,188
Ta 238,23,41984
G 163,75
Mvaa 148,244,188
G 164,76
Mvaa 116,244,184
G 165,77
Mvaa 84,244,180
G 166,78
Ta 240,24,40960
Mvaa 52,244,176
Ta 242,24,41984
W 10
Mvaa 56,248,176
G 167,79
K 160,160,161
Mvaa 88,248,180
buffer_load_dwordx4 v[204:207], v19, s[8:11], s66 offen offset:1024 sc1
K 161,162,163
Mvaa 120,248,184
K 162,164,165
Ta 244,25,40960
Mvaa 152,248,188
Ta 246,25,41984
v_bfe_u32 v28, v1, 4, 5
Mvaa 156,252,188
K 163,166,167
v_lshlrev_b32 v28, 3, v28
Mvaa 124,252,184
buffer_store_dwordx4 v[160:163], v1, s[8:11], s67 offen offset:1024 sc1
s_lshl_b32 s80, s56, 9
Mvaa 92,252,180
s_add_u32 s80, s80, 4096
Ta 248,26,40960
Mvaa 60,252,176
Ta 250,26,41984
s_waitcnt vmcnt(25) lgkmcnt(10)
Mvav 32,224,232
s_add_u32 s66, s66, 4096
Mvav 64,224,236
buffer_load_dwordx2 v[28:29], v28, s[8:11], s80 offen sc1
Mvav 96,224,240
Ta 252,27,40960
Ta 254,27,41984
Mvav 128,224,244
G 168,80
Mvav 132,228,244
G 169,81
Mvav 100,228,240
buffer_load_dwordx4 a[208:211], v18, s[8:11], s66 offen offset:0 sc1
G 170,82
Mvav 68,228,236
G 171,83
Ta 224,20,49152
Mvav 36,228,232
Ta 226,20,50176
W 10
Mvav 40,232,232
G 172,84
G 173,85
Mvav 72,232,236
buffer_load_dwordx4 a[212:215], v18, s[8:11], s66 offen offset:1024 sc1
G 174,86
Mvav 104,232,240
G 175,87
Ta 228,21,49152
Mvav 136,232,244
Ta 230,21,50176
K 168,168,169
Mvav 140,236,244
K 169,170,171
Mvav 108,236,240
K 170,172,173
Mvav 76,236,236
K 171,174,175
Ta 232,22,49152
Mvav 44,236,232
Ta 234,22,50176
W 10
Mvav 48,240,232
s_and_b32 s81, s14, 1
buffer_store_dwordx4 v[168:171], v1, s[8:11], s67 offen offset:2048 sc1
Mvav 80,240,236
s_mul_i32 s91, s81, 16902144
Mvav 112,240,240
Ta 236,23,49152
Ta 238,23,50176
Mvav 144,240,244
s_mul_i32 s82, s56, 528384
Mvav 148,244,244
buffer_load_dwordx4 a[16:19], v19, s[8:11], s66 offen offset:0 sc1
Mvav 116,244,240
G 176,88
Mvav 84,244,236
s_add_u32 s91, s91, s82
Ta 240,24,49152
Mvav 52,244,232
Ta 242,24,50176
W 10
Mvav 56,248,232
G 177,89
G 178,90
Mvav 88,248,236
buffer_load_dwordx4 a[20:23], v19, s[8:11], s66 offen offset:1024 sc1
s_add_u32 s91, s91, 4194304
Mvav 120,248,240
G 179,91
G 180,92
Mvav 152,248,244
Ta 244,25,49152
Ta 246,25,50176
Mvav 156,252,244
G 181,93
s_lshr_b32 s82, s14, 1
Mvav 124,252,240
G 182,94
G 183,95
Mvav 92,252,236
s_lshl_b32 s83, s82, 14
K 176,176,177
Mvav 60,252,232
Ta 248,26,49152
s_waitcnt vmcnt(24) lgkmcnt(9)
Mvav 32,224,184
Ta 250,26,50176
K 177,178,179
Mvav 64,224,188
s_add_u32 s91, s91, s83
K 178,180,181
Mvaa 96,224,192
s_add_u32 s66, s66, 4096
buffer_load_dwordx4 a[24:27], v18, s[8:11], s66 offen offset:0 sc1
Mvaa 128,224,196
K 179,182,183
Ta 252,27,49152
Mvaa 132,228,196
Ta 254,27,50176
s_lshl_b32 s81, s81, 14
Mvaa 100,228,192
s_lshl_b32 s82, s82, 16
Mvav 68,228,188
buffer_store_dwordx4 v[176:179], v1, s[8:11], s67 offen offset:3072 sc1
Ta 224,20,57344
Mvav 36,228,184
Ta 226,20,58368
W 10
Mvav 40,232,184
G 160,96
s_or_b32 s92, s81, s82
Mvav 72,232,188
G 161,97
G 162,98
Mvaa 104,232,192
buffer_load_dwordx4 a[28:31], v18, s[8:11], s66 offen offset:1024 sc1
s_xor_b32 s92, s92, 0x10000
Mvaa 136,232,196
Ta 228,21,57344
Ta 230,21,58368
Mvaa 140,236,196
G 163,99
G 164,100
Mvaa 108,236,192
G 165,101
G 166,102
Mvav 76,236,188
Ta 232,22,57344
Ta 234,22,58368
Mvav 44,236,184
G 167,103
W 10
Mvav 48,240,184
K 160,160,161
buffer_load_dwordx4 a[216:219], v19, s[8:11], s66 offen offset:0 sc1
Mvav 80,240,188
K 161,162,163
K 162,164,165
Mvaa 112,240,192
Ta 236,23,57344
Ta 238,23,58368
Mvaa 144,240,196
K 163,166,167
s_add_u32 s67, s67, s70
Mvaa 148,244,196
buffer_store_dwordx4 v[160:163], v1, s[8:11], s67 offen offset:0 sc1
Mvaa 116,244,192
Mvav 84,244,188
Ta 240,24,57344
Ta 242,24,58368
Mvav 52,244,184
W 10
Mvav 56,248,184
s_waitcnt vmcnt(10)
v_cmp_eq_u64 vcc, s[12:13], v[28:29]
s_cmp_eq_u64 vcc, -1
s_cbranch_scc0 .Lspin_26%=
.Lok_27%=:
Mvav 88,248,188
s_add_u32 m0, s92, 0
buffer_load_dwordx4 a[220:223], v19, s[8:11], s66 offen offset:1024 sc1
Mvaa 120,248,192
Ta 244,25,57344
Ta 246,25,58368
Mvaa 152,248,196
G 168,104
s_add_u32 s81, s91, 0
Mvaa 156,252,196
G 169,105
Mvaa 124,252,192
G 170,106
Mvav 92,252,188
G 171,107
Ta 248,26,57344
Mvav 60,252,184
Ta 250,26,58368
W 10
Mvav 32,224,248
G 172,108
Dc 1,8,81,0
Mvav 64,224,252
G 173,109
G 174,110
Mvaa 96,224,0
G 175,111
Ta 252,27,57344
Mvaa 128,224,4
Ta 254,27,58368
K 168,168,169
Mvaa 132,228,4
Dc 1,8,81,1024
K 169,170,171
Mvaa 100,228,0
K 170,172,173
Mvav 68,228,252
K 171,174,175
Ta 224,2,32768
Mvav 36,228,248
Ta 226,2,33792
W 10
Mvav 40,232,248
Dc 1,8,81,2048
Mvav 72,232,252
Mvaa 104,232,0
Ta 228,3,32768
Ta 230,3,33792
Mvaa 136,232,4
Mvaa 140,236,4
buffer_store_dwordx4 v[168:171], v1, s[8:11], s67 offen offset:1024 sc1
G 176,112
Mvaa 108,236,0
Mvav 76,236,252
G 177,113
Ta 232,4,32768
Mvav 44,236,248
Ta 234,4,33792
W 10
Mvav 48,240,248
Dc 1,8,81,3072
s_add_u32 m0, s92, 4096
Mvav 80,240,252
G 178,114
G 179,115
Mvaa 112,240,0
G 180,116
s_add_u32 s81, s91, 4096
Mvaa 144,240,4
G 181,117
Ta 236,5,32768
Mvaa 148,244,4
Ta 238,5,33792
G 182,118
Mvaa 116,244,0
G 183,119
K 176,176,177
Mvav 84,244,252
Dc 1,8,81,0
K 177,178,179
Mvav 52,244,248
Ta 240,6,32768
W 9
Mvav 56,248,248
Ta 242,6,33792
K 178,180,181
Mvav 88,248,252
K 179,182,183
Mvaa 120,248,0
Dc 1,8,81,1024
Ta 244,7,32768
Mvaa 152,248,4
Ta 246,7,33792
Mvaa 156,252,4
Mvaa 124,252,0
Mvav 92,252,252
buffer_store_dwordx4 v[176:179], v1, s[8:11], s67 offen offset:2048 sc1
Ta 248,8,32768
Mvav 60,252,248
Ta 250,8,33792
W 10
Mvaa 32,224,200
G 160,120
Mvaa 64,224,204
G 161,121
Mvav 96,224,192
Dc 1,8,81,2048
G 162,122
Mvav 128,224,196
Ta 252,9,32768
Ta 254,9,33792
Mvav 132,228,196
G 163,123
G 164,124
Mvav 100,228,192
G 165,125
Mvaa 68,228,204
Dc 1,8,81,3072
s_add_u32 m0, s92, 8192
Mvaa 36,228,200
G 166,126
W 8
Mvaa 40,232,200
Ta 224,2,40960
Ta 226,2,41984
Mvaa 72,232,204
G 167,127
s_add_u32 s81, s91, 8192
Mvav 104,232,192
K 160,160,161
K 161,162,163
Mvav 136,232,196
K 162,164,165
Ta 228,3,40960
Mvav 140,236,196
Ta 230,3,41984
K 163,166,167
Mvav 108,236,192
Dc 1,8,81,0
Mvaa 76,236,204
Ta 232,4,40960
Ta 234,4,41984
Mvaa 44,236,200
W 10
Mvaa 48,240,200
Mvaa 80,240,204
buffer_store_dwordx4 v[160:163], v1, s[8:11], s67 offen offset:3072 sc1
Mvav 112,240,192
Ta 236,5,40960
Ta 238,5,41984
Mvav 144,240,196
G 168,128
G 169,129
Mvav 148,244,196
G 170,130
Mvav 116,244,192
Dc 1,8,81,1024
G 171,131
Mvaa 84,244,204
Ta 240,6,40960
Ta 242,6,41984
Mvaa 52,244,200
G 172,132
W 10
Mvaa 56,248,200
G 173,133
G 174,134
Mvaa 88,248,204
Dc 1,8,81,2048
G 175,135
Mvav 120,248,192
Ta 244,7,40960
Ta 246,7,41984
Mvav 152,248,196
K 168,168,169
K 169,170,171
Mvav 156,252,196
K 170,172,173
Mvav 124,252,192
Dc 1,8,81,3072
s_add_u32 m0, s92, 12288
Mvaa 92,252,204
K 171,174,175
s_add_u32 s81, s91, 12288
Mvaa 60,252,200
Ta 248,8,40960
W 9
Mvaa 32,224,8
Ta 250,8,41984
s_add_u32 s67, s67, 4096
Mvaa 64,224,12
buffer_store_dwordx4 v[168:171], v1, s[8:11], s67 offen offset:0 sc1
Mvav 96,224,200
Ta 252,9,40960
Ta 254,9,41984
Mvav 128,224,204
Mvav 132,228,204
G 176,136
Mvav 100,228,200
)"
R"(Dc 1,8,81,0
Mvaa 68,228,12
G 177,137
Ta 224,2,49152
Mvaa 36,228,8
Ta 226,2,50176
W 10
Mvaa 40,232,8
G 178,138
G 179,139
Mvaa 72,232,12
Dc 1,8,81,1024
G 180,140
Mvav 104,232,200
G 181,141
Ta 228,3,49152
Mvav 136,232,204
Ta 230,3,50176
G 182,142
Mvav 140,236,204
G 183,143
Mvav 108,236,200
Dc 1,8,81,2048
K 176,176,177
Mvaa 76,236,12
K 177,178,179
Ta 232,4,49152
Mvaa 44,236,8
Ta 234,4,50176
W 10
Mvaa 48,240,8
K 178,180,181
K 179,182,183
Mvaa 80,240,12
Dc 1,8,81,3072
Mvav 112,240,200
Ta 236,5,49152
Ta 238,5,50176
Mvav 144,240,204
Mvav 148,244,204
Mvav 116,244,200
buffer_store_dwordx4 v[176:179], v1, s[8:11], s67 offen offset:1024 sc1
Mvaa 84,244,12
Ta 240,6,49152
Ta 242,6,50176
Mvaa 52,244,8
G 160,144
W 10
Mvaa 56,248,8
Mvaa 88,248,12
G 161,145
Mvav 120,248,200
G 162,146
Ta 244,7,49152
Mvav 152,248,204
Ta 246,7,50176
G 163,147
Mvav 156,252,204
G 164,148
Mvav 124,252,200
G 165,149
Mvaa 92,252,12
G 166,150
Ta 248,8,49152
Mvaa 60,252,8
Ta 250,8,50176
s_waitcnt vmcnt(27) lgkmcnt(10)
Mvaa 32,224,208
G 167,151
K 160,160,161
Mvaa 64,224,212
K 161,162,163
Mvaa 96,224,16
K 162,164,165
Ta 252,9,49152
Mvaa 128,224,20
Ta 254,9,50176
K 163,166,167
Mvaa 132,228,20
buffer_store_dwordx4 v[160:163], v1, s[8:11], s67 offen offset:2048 sc1
Mvaa 100,228,16
Mvaa 68,228,212
Ta 224,2,57344
Ta 226,2,58368
Mvaa 36,228,208
W 10
Mvaa 40,232,208
Mvaa 72,232,212
Mvaa 104,232,16
Ta 228,3,57344
Ta 230,3,58368
Mvaa 136,232,20
G 168,152
Mvaa 140,236,20
G 169,153
Mvaa 108,236,16
G 170,154
Mvaa 76,236,212
G 171,155
Ta 232,4,57344
Mvaa 44,236,208
Ta 234,4,58368
W 10
Mvaa 48,240,208
G 172,156
G 173,157
Mvaa 80,240,212
G 174,158
Mvaa 112,240,16
G 175,159
Ta 236,5,57344
Mvaa 144,240,20
Ta 238,5,58368
K 168,168,169
Mvaa 148,244,20
K 169,170,171
Mvaa 116,244,16
K 170,172,173
Mvaa 84,244,212
K 171,174,175
Ta 240,6,57344
Mvaa 52,244,208
Ta 242,6,58368
W 10
Mvaa 56,248,208
buffer_store_dwordx4 v[168:171], v1, s[8:11], s67 offen offset:3072 sc1
Mvaa 88,248,212
Mvaa 120,248,16
Ta 244,7,57344
Ta 246,7,58368
Mvaa 152,248,20
Mvaa 156,252,20
Mvaa 124,252,16
Mvaa 92,252,212
Ta 248,8,57344
Ta 250,8,58368
Mvaa 60,252,208
s_waitcnt vmcnt(23) lgkmcnt(10)
Mvaa 32,224,24
Mvaa 64,224,28
Mvaa 96,224,216
Ta 252,9,57344
Ta 254,9,58368
Mvaa 128,224,220
Mvaa 132,228,220
Mvaa 100,228,216
Mvaa 68,228,28
Mvaa 36,228,24
W 8
Mvaa 40,232,24
Mvaa 72,232,28
Mvaa 104,232,216
Mvaa 136,232,220
Mvaa 140,236,220
Mvaa 108,236,216
Mvaa 76,236,28
Mvaa 44,236,24
W 4
Mvaa 48,240,24
Mvaa 80,240,28
Mvaa 112,240,216
Mvaa 144,240,220
Mvaa 148,244,220
Mvaa 116,244,216
Mvaa 84,244,28
Mvaa 52,244,24
W 0
Mvaa 56,248,24
Mvaa 88,248,28
Mvaa 120,248,216
Mvaa 152,248,220
Mvaa 156,252,220
Mvaa 124,252,216
Mvaa 92,252,28
Mvaa 60,252,24
v_and_b32 v28, 16, v1
v_cmp_eq_u32 vcc, 0, v28
v_mov_b32 v240, 0xeeeeeeee
v_mov_b32 v29, 0x44444444
v_cndmask_b32 v240, v240, v29, vcc
v_mov_b32 v241, 0xff800000
v_add_u32 v242, 0x10000, v1
v_add_u32 v243, 4096, v1
v_add_u32 v244, 8192, v1
v_add_u32 v245, 12288, v1
v_bfe_u32 v28, v1, 4, 4
v_lshrrev_b32 v29, 8, v1
v_lshlrev_b32 v29, 2, v29
v_sub_u32 v28, v28, v29
v_cmp_le_i32_e64 s[72:73], 0, v28
v_cmp_le_i32_e64 s[74:75], 1, v28
v_cmp_le_i32_e64 s[76:77], 2, v28
v_cmp_le_i32_e64 s[78:79], 3, v28
s_mov_b32 s80, 0x3f803f80
s_mov_b32 s81, 0x3f003f00
v_accvgpr_write_b32 a176, s80
v_accvgpr_write_b32 a180, s81
v_accvgpr_write_b32 a177, s80
v_accvgpr_write_b32 a181, s81
v_accvgpr_write_b32 a178, s80
v_accvgpr_write_b32 a182, s81
v_accvgpr_write_b32 a179, s80
v_accvgpr_write_b32 a183, s81
v_accvgpr_write_b32 a160, 0
v_accvgpr_write_b32 a161, 0
v_accvgpr_write_b32 a162, 0
v_accvgpr_write_b32 a163, 0
v_accvgpr_write_b32 a164, 0
v_accvgpr_write_b32 a165, 0
v_accvgpr_write_b32 a166, 0
v_accvgpr_write_b32 a167, 0
v_accvgpr_write_b32 a168, 0
v_accvgpr_write_b32 a169, 0
v_accvgpr_write_b32 a170, 0
v_accvgpr_write_b32 a171, 0
v_accvgpr_write_b32 a172, 0
v_accvgpr_write_b32 a173, 0
v_accvgpr_write_b32 a174, 0
v_accvgpr_write_b32 a175, 0
s_mov_b32 s90, 0x5d800000
s_mov_b64 s[88:89], 0
s_mov_b32 s64, 0
s_and_b32 s80, s14, 1
s_lshl_b32 s62, s80, 14
s_lshl_b32 s82, s57, 15
s_sub_u32 s82, 507904, s82
s_add_u32 s82, s82, s62
s_lshl_b32 s65, s57, 1
s_sub_u32 s65, s65, 1
s_max_i32 s65, s65, 0
s_sub_u32 s66, s60, 1
s_cmp_lt_u32 s14, 2
s_cselect_b32 s82, s82, 16384
s_cselect_b32 s66, s66, 15
s_cselect_b32 s83, 32768, 0
s_add_u32 s61, s91, s82
s_add_u32 s62, s62, s83
s_sub_u32 s63, 16384, s83
s_lshl_b32 s67, s60, 7
s_add_u32 s67, s67, s56
s_lshl_b32 s67, s67, 12
s_add_u32 s67, s67, 41943040
s_lshl_b32 s69, s60, 3
s_add_u32 s69, s69, s59
s_lshl_b32 s69, s69, 6
s_lshl_b32 s83, s58, 3
s_add_u32 s69, s69, s83
s_add_u32 s69, s69, 20480
s_mov_b32 s70, 0
s_mov_b32 s91, 0
v_mov_b32 v252, v241
v_mov_b32 v253, v241
v_mov_b32 v254, v241
v_mov_b32 v255, v241
s_sub_u32 s80, s57, 4
s_lshl_b32 s85, s80, 5
s_lshl_b32 s81, s58, 3
s_add_u32 s85, s85, s81
s_add_u32 s85, s85, s59
s_cmp_lt_u32 s85, 54
s_cselect_b32 s65, 1, s65
s_mov_b32 s68, 4102
s_mov_b64 s[24:25], -1
s_cmp_ge_u32 s14, 2
s_cbranch_scc1 .Lpb_29%=
v_mul_f32 v32, 0x3e0293ee, v32
v_mul_f32 v33, 0x3e0293ee, v33
v_mul_f32 v34, 0x3e0293ee, v34
v_mul_f32 v35, 0x3e0293ee, v35
v_mul_f32 v36, 0x3e0293ee, v36
v_mul_f32 v37, 0x3e0293ee, v37
v_mul_f32 v38, 0x3e0293ee, v38
v_mul_f32 v39, 0x3e0293ee, v39
K 32,32,33
K 33,34,35
K 34,36,37
K 35,38,39
v_accvgpr_write_b32 a0, v32
v_accvgpr_write_b32 a1, v33
v_accvgpr_write_b32 a2, v34
v_accvgpr_write_b32 a3, v35
v_mul_f32 v64, 0x3e0293ee, v64
v_mul_f32 v65, 0x3e0293ee, v65
v_mul_f32 v66, 0x3e0293ee, v66
v_mul_f32 v67, 0x3e0293ee, v67
v_mul_f32 v68, 0x3e0293ee, v68
v_mul_f32 v69, 0x3e0293ee, v69
v_mul_f32 v70, 0x3e0293ee, v70
v_mul_f32 v71, 0x3e0293ee, v71
K 64,64,65
K 65,66,67
K 66,68,69
K 67,70,71
v_accvgpr_write_b32 a16, v64
v_accvgpr_write_b32 a17, v65
v_accvgpr_write_b32 a18, v66
v_accvgpr_write_b32 a19, v67
v_mul_f32 v96, 0x3e0293ee, v96
v_mul_f32 v97, 0x3e0293ee, v97
v_mul_f32 v98, 0x3e0293ee, v98
v_mul_f32 v99, 0x3e0293ee, v99
v_mul_f32 v100, 0x3e0293ee, v100
v_mul_f32 v101, 0x3e0293ee, v101
v_mul_f32 v102, 0x3e0293ee, v102
v_mul_f32 v103, 0x3e0293ee, v103
K 96,96,97
K 97,98,99
K 98,100,101
K 99,102,103
v_accvgpr_write_b32 a32, v96
v_accvgpr_write_b32 a33, v97
v_accvgpr_write_b32 a34, v98
v_accvgpr_write_b32 a35, v99
v_mul_f32 v128, 0x3e0293ee, v128
v_mul_f32 v129, 0x3e0293ee, v129
v_mul_f32 v130, 0x3e0293ee, v130
v_mul_f32 v131, 0x3e0293ee, v131
v_mul_f32 v132, 0x3e0293ee, v132
v_mul_f32 v133, 0x3e0293ee, v133
v_mul_f32 v134, 0x3e0293ee, v134
v_mul_f32 v135, 0x3e0293ee, v135
K 128,128,129
K 129,130,131
K 130,132,133
K 131,134,135
v_accvgpr_write_b32 a48, v128
v_accvgpr_write_b32 a49, v129
v_accvgpr_write_b32 a50, v130
v_accvgpr_write_b32 a51, v131
v_mul_f32 v40, 0x3e0293ee, v40
v_mul_f32 v41, 0x3e0293ee, v41
v_mul_f32 v42, 0x3e0293ee, v42
v_mul_f32 v43, 0x3e0293ee, v43
v_mul_f32 v44, 0x3e0293ee, v44
v_mul_f32 v45, 0x3e0293ee, v45
v_mul_f32 v46, 0x3e0293ee, v46
v_mul_f32 v47, 0x3e0293ee, v47
K 40,40,41
K 41,42,43
K 42,44,45
K 43,46,47
v_accvgpr_write_b32 a4, v40
v_accvgpr_write_b32 a5, v41
v_accvgpr_write_b32 a6, v42
v_accvgpr_write_b32 a7, v43
v_mul_f32 v72, 0x3e0293ee, v72
v_mul_f32 v73, 0x3e0293ee, v73
v_mul_f32 v74, 0x3e0293ee, v74
v_mul_f32 v75, 0x3e0293ee, v75
v_mul_f32 v76, 0x3e0293ee, v76
v_mul_f32 v77, 0x3e0293ee, v77
v_mul_f32 v78, 0x3e0293ee, v78
v_mul_f32 v79, 0x3e0293ee, v79
K 72,72,73
K 73,74,75
K 74,76,77
K 75,78,79
v_accvgpr_write_b32 a20, v72
v_accvgpr_write_b32 a21, v73
v_accvgpr_write_b32 a22, v74
v_accvgpr_write_b32 a23, v75
v_mul_f32 v104, 0x3e0293ee, v104
v_mul_f32 v105, 0x3e0293ee, v105
v_mul_f32 v106, 0x3e0293ee, v106
v_mul_f32 v107, 0x3e0293ee, v107
v_mul_f32 v108, 0x3e0293ee, v108
v_mul_f32 v109, 0x3e0293ee, v109
v_mul_f32 v110, 0x3e0293ee, v110
v_mul_f32 v111, 0x3e0293ee, v111
K 104,104,105
K 105,106,107
K 106,108,109
K 107,110,111
v_accvgpr_write_b32 a36, v104
v_accvgpr_write_b32 a37, v105
v_accvgpr_write_b32 a38, v106
v_accvgpr_write_b32 a39, v107
v_mul_f32 v136, 0x3e0293ee, v136
v_mul_f32 v137, 0x3e0293ee, v137
v_mul_f32 v138, 0x3e0293ee, v138
v_mul_f32 v139, 0x3e0293ee, v139
v_mul_f32 v140, 0x3e0293ee, v140
v_mul_f32 v141, 0x3e0293ee, v141
v_mul_f32 v142, 0x3e0293ee, v142
v_mul_f32 v143, 0x3e0293ee, v143
K 136,136,137
K 137,138,139
K 138,140,141
K 139,142,143
v_accvgpr_write_b32 a52, v136
v_accvgpr_write_b32 a53, v137
v_accvgpr_write_b32 a54, v138
v_accvgpr_write_b32 a55, v139
v_mul_f32 v48, 0x3e0293ee, v48
v_mul_f32 v49, 0x3e0293ee, v49
v_mul_f32 v50, 0x3e0293ee, v50
v_mul_f32 v51, 0x3e0293ee, v51
v_mul_f32 v52, 0x3e0293ee, v52
v_mul_f32 v53, 0x3e0293ee, v53
v_mul_f32 v54, 0x3e0293ee, v54
)"
R"(v_mul_f32 v55, 0x3e0293ee, v55
K 48,48,49
K 49,50,51
K 50,52,53
K 51,54,55
v_accvgpr_write_b32 a8, v48
v_accvgpr_write_b32 a9, v49
v_accvgpr_write_b32 a10, v50
v_accvgpr_write_b32 a11, v51
v_mul_f32 v80, 0x3e0293ee, v80
v_mul_f32 v81, 0x3e0293ee, v81
v_mul_f32 v82, 0x3e0293ee, v82
v_mul_f32 v83, 0x3e0293ee, v83
v_mul_f32 v84, 0x3e0293ee, v84
v_mul_f32 v85, 0x3e0293ee, v85
v_mul_f32 v86, 0x3e0293ee, v86
v_mul_f32 v87, 0x3e0293ee, v87
K 80,80,81
K 81,82,83
K 82,84,85
K 83,86,87
v_accvgpr_write_b32 a24, v80
v_accvgpr_write_b32 a25, v81
v_accvgpr_write_b32 a26, v82
v_accvgpr_write_b32 a27, v83
v_mul_f32 v112, 0x3e0293ee, v112
v_mul_f32 v113, 0x3e0293ee, v113
v_mul_f32 v114, 0x3e0293ee, v114
v_mul_f32 v115, 0x3e0293ee, v115
v_mul_f32 v116, 0x3e0293ee, v116
v_mul_f32 v117, 0x3e0293ee, v117
v_mul_f32 v118, 0x3e0293ee, v118
v_mul_f32 v119, 0x3e0293ee, v119
K 112,112,113
K 113,114,115
K 114,116,117
K 115,118,119
v_accvgpr_write_b32 a40, v112
v_accvgpr_write_b32 a41, v113
v_accvgpr_write_b32 a42, v114
v_accvgpr_write_b32 a43, v115
v_mul_f32 v144, 0x3e0293ee, v144
v_mul_f32 v145, 0x3e0293ee, v145
v_mul_f32 v146, 0x3e0293ee, v146
v_mul_f32 v147, 0x3e0293ee, v147
v_mul_f32 v148, 0x3e0293ee, v148
v_mul_f32 v149, 0x3e0293ee, v149
v_mul_f32 v150, 0x3e0293ee, v150
v_mul_f32 v151, 0x3e0293ee, v151
K 144,144,145
K 145,146,147
K 146,148,149
K 147,150,151
v_accvgpr_write_b32 a56, v144
v_accvgpr_write_b32 a57, v145
v_accvgpr_write_b32 a58, v146
v_accvgpr_write_b32 a59, v147
v_mul_f32 v56, 0x3e0293ee, v56
v_mul_f32 v57, 0x3e0293ee, v57
v_mul_f32 v58, 0x3e0293ee, v58
v_mul_f32 v59, 0x3e0293ee, v59
v_mul_f32 v60, 0x3e0293ee, v60
v_mul_f32 v61, 0x3e0293ee, v61
v_mul_f32 v62, 0x3e0293ee, v62
v_mul_f32 v63, 0x3e0293ee, v63
K 56,56,57
K 57,58,59
K 58,60,61
K 59,62,63
v_accvgpr_write_b32 a12, v56
v_accvgpr_write_b32 a13, v57
v_accvgpr_write_b32 a14, v58
v_accvgpr_write_b32 a15, v59
v_mul_f32 v88, 0x3e0293ee, v88
v_mul_f32 v89, 0x3e0293ee, v89
v_mul_f32 v90, 0x3e0293ee, v90
v_mul_f32 v91, 0x3e0293ee, v91
v_mul_f32 v92, 0x3e0293ee, v92
v_mul_f32 v93, 0x3e0293ee, v93
v_mul_f32 v94, 0x3e0293ee, v94
v_mul_f32 v95, 0x3e0293ee, v95
K 88,88,89
K 89,90,91
K 90,92,93
K 91,94,95
v_accvgpr_write_b32 a28, v88
v_accvgpr_write_b32 a29, v89
v_accvgpr_write_b32 a30, v90
v_accvgpr_write_b32 a31, v91
v_mul_f32 v120, 0x3e0293ee, v120
v_mul_f32 v121, 0x3e0293ee, v121
v_mul_f32 v122, 0x3e0293ee, v122
v_mul_f32 v123, 0x3e0293ee, v123
v_mul_f32 v124, 0x3e0293ee, v124
v_mul_f32 v125, 0x3e0293ee, v125
v_mul_f32 v126, 0x3e0293ee, v126
v_mul_f32 v127, 0x3e0293ee, v127
K 120,120,121
K 121,122,123
K 122,124,125
K 123,126,127
v_accvgpr_write_b32 a44, v120
v_accvgpr_write_b32 a45, v121
v_accvgpr_write_b32 a46, v122
v_accvgpr_write_b32 a47, v123
v_mul_f32 v152, 0x3e0293ee, v152
v_mul_f32 v153, 0x3e0293ee, v153
v_mul_f32 v154, 0x3e0293ee, v154
v_mul_f32 v155, 0x3e0293ee, v155
v_mul_f32 v156, 0x3e0293ee, v156
v_mul_f32 v157, 0x3e0293ee, v157
v_mul_f32 v158, 0x3e0293ee, v158
v_mul_f32 v159, 0x3e0293ee, v159
K 152,152,153
K 153,154,155
K 154,156,157
K 155,158,159
v_accvgpr_write_b32 a60, v152
v_accvgpr_write_b32 a61, v153
v_accvgpr_write_b32 a62, v154
v_accvgpr_write_b32 a63, v155
s_waitcnt vmcnt(0)
s_barrier
s_add_u32 s71, s71, 256
v_mov_b64 v[28:29], s[12:13]
s_mov_b64 exec, 1
buffer_store_dwordx2 v[28:29], off, s[8:11], s71 sc1
s_mov_b64 exec, -1
.Lago%=:
s_cmp_eq_u32 s57, 0
s_cbranch_scc0 .LL_43%=
s_add_u32 m0, s62, 0
s_nop 0
Dc 1,8,61,0
Dc 1,8,61,1024
Dc 1,8,61,2048
Dc 1,8,61,3072
s_add_u32 m0, s62, 4096
s_nop 0
Dc 243,8,61,0
Dc 243,8,61,1024
Dc 243,8,61,2048
Dc 243,8,61,3072
s_add_u32 m0, s62, 8192
s_nop 0
Dc 244,8,61,0
Dc 244,8,61,1024
Dc 244,8,61,2048
Dc 244,8,61,3072
s_add_u32 m0, s62, 12288
s_nop 0
Dc 245,8,61,0
Dc 245,8,61,1024
Dc 245,8,61,2048
Dc 245,8,61,3072
s_add_u32 s61, s61, s63
.LL_43%=:
s_cmp_eq_u32 s60, 0
s_cbranch_scc1 .Lpd_30%=
Ra 128,242,0
Ra 132,242,1024
Ra 136,242,2048
Ra 140,242,3072
W 0
Zvaa 128,128,0
Ra 144,242,4096
Ra 148,242,5120
Ra 152,242,6144
Ra 156,242,7168
Zvaa 132,128,16
s_and_b32 s80, s14, 1
v_lshrrev_b32 v20, 4, v1
v_add_u32 v20, 0, v20
v_mul_u32_u24 v26, 205, v20
Zvaa 136,128,32
s_lshl_b32 s80, s80, 14
v_lshrrev_b32 v26, 12, v26
v_mul_u32_u24 v25, 20, v26
v_sub_u32 v20, v20, v25
Zvaa 140,128,48
s_add_u32 s80, s80, 131072
v_lshrrev_b32 v25, 1, v26
v_and_b32 v25, 2, v25
v_xor_b32 v20, v20, v25
Mvaa 128,132,4
v_add_u32 v12, s80, v1
v_mul_u32_u24 v26, 80, v26
v_add_lshl_u32 v16, v20, v26, 4
v_lshrrev_b32 v20, 4, v1
Mvaa 132,132,20
Ra 64,12,8192
v_add_u32 v20, 64, v20
v_mul_u32_u24 v26, 205, v20
v_lshrrev_b32 v26, 12, v26
Mvaa 136,132,36
Ra 128,242,8192
Ra 68,12,9216
v_mul_u32_u24 v25, 20, v26
v_sub_u32 v20, v20, v25
Mvaa 140,132,52
v_lshrrev_b32 v25, 1, v26
Ra 72,12,10240
v_and_b32 v25, 2, v25
v_xor_b32 v20, v20, v25
Mvaa 128,136,8
v_mul_u32_u24 v26, 80, v26
Ra 76,12,11264
v_add_lshl_u32 v17, v20, v26, 4
v_subrev_u32 v17, 1024, v17
Mvaa 132,136,24
v_lshrrev_b32 v20, 4, v1
Ra 80,12,12288
v_add_u32 v20, 128, v20
v_mul_u32_u24 v26, 205, v20
Mvaa 136,136,40
v_lshrrev_b32 v26, 12, v26
Ra 84,12,13312
v_mul_u32_u24 v25, 20, v26
v_sub_u32 v20, v20, v25
Mvaa 140,136,56
v_lshrrev_b32 v25, 1, v26
Ra 132,242,9216
Ra 88,12,14336
v_and_b32 v25, 2, v25
Mvaa 128,140,12
v_xor_b32 v20, v20, v25
v_mul_u32_u24 v26, 80, v26
Ra 92,12,15360
v_add_lshl_u32 v18, v20, v26, 4
Mvaa 132,140,28
v_subrev_u32 v18, 2048, v18
v_lshrrev_b32 v20, 4, v1
Ra 96,12,0
v_add_u32 v20, 192, v20
Mvaa 136,140,44
v_mul_u32_u24 v26, 205, v20
v_lshrrev_b32 v26, 12, v26
Ra 100,12,1024
v_mul_u32_u24 v25, 20, v26
Mvaa 140,140,60
v_sub_u32 v20, v20, v25
v_lshrrev_b32 v25, 1, v26
Ra 104,12,2048
W 13
Zvaa 144,144,0
v_and_b32 v25, 2, v25
v_xor_b32 v20, v20, v25
v_mul_u32_u24 v26, 80, v26
Ra 136,242,10240
Zvaa 148,144,16
Ra 108,12,3072
v_add_lshl_u32 v19, v20, v26, 4
v_subrev_u32 v19, 3072, v19
v_lshrrev_b32 v20, 4, v1
Zvaa 152,144,32
Ra 112,12,4096
v_add_u32 v20, 256, v20
v_mul_u32_u24 v26, 205, v20
v_lshrrev_b32 v26, 12, v26
Zvaa 156,144,48
Ra 116,12,5120
v_mul_u32_u24 v25, 20, v26
v_sub_u32 v20, v20, v25
v_lshrrev_b32 v25, 1, v26
Mvaa 144,148,4
Ra 120,12,6144
v_and_b32 v25, 2, v25
v_xor_b32 v20, v20, v25
v_mul_u32_u24 v26, 80, v26
Mvaa 148,148,20
Ra 124,12,7168
v_add_lshl_u32 v20, v20, v26, 4
v_lshrrev_b32 v26, 6, v1
v_mul_u32_u24 v26, 320, v26
Mvaa 152,148,36
Ra 140,242,11264
v_bfe_u32 v25, v1, 4, 2
v_lshl_add_u32 v26, v25, 3, v26
v_bfe_u32 v25, v1, 8, 1
Mvaa 156,148,52
v_lshlrev_b32 v25, 5, v25
v_max3_f32 v4, v128, v129, v130
v_max3_f32 v4, v4, v131, v252
v_max3_f32 v5, v132, v133, v134
Mvaa 144,152,8
v_max3_f32 v5, v5, v135, v253
v_max3_f32 v6, v136, v137, v138
v_max3_f32 v6, v6, v139, v254
v_max3_f32 v7, v140, v141, v142
Mvaa 148,152,24
v_max3_f32 v7, v7, v143, v255
v_mov_b32 v8, v4
v_mov_b32 v9, v5
v_mov_b32 v10, v6
Mvaa 152,152,40
v_mov_b32 v11, v7
v_permlane32_swap_b32 v4, v8
v_permlane32_swap_b32 v5, v9
v_permlane32_swap_b32 v6, v10
Mvaa 156,152,56
v_permlane32_swap_b32 v7, v11
v_max_f32 v4, v4, v8
v_max_f32 v5, v5, v9
v_max_f32 v6, v6, v10
Mvaa 144,156,12
v_max_f32 v7, v7, v11
v_mov_b32 v8, v4
v_mov_b32 v9, v5
v_mov_b32 v10, v6
Mvaa 148,156,28
v_mov_b32 v11, v7
v_permlane16_swap_b32 v4, v8
v_permlane16_swap_b32 v5, v9
v_permlane16_swap_b32 v6, v10
Mvaa 152,156,44
v_permlane16_swap_b32 v7, v11
v_max_f32 v4, v4, v8
v_max_f32 v5, v5, v9
v_max_f32 v6, v6, v10
v_max_f32 v7, v7, v11
v_sub_f32 v4, 0, v4
v_sub_f32 v5, 0, v5
v_sub_f32 v6, 0, v6
v_sub_f32 v7, 0, v7
v_mov_b32 v224, v4
v_mov_b32 v225, v4
v_mov_b32 v226, v4
v_mov_b32 v227, v4
v_mov_b32 v228, v5
v_mov_b32 v229, v5
v_mov_b32 v230, v5
v_mov_b32 v231, v5
v_mov_b32 v232, v6
v_mov_b32 v233, v6
v_mov_b32 v234, v6
v_mov_b32 v235, v6
v_mov_b32 v236, v7
v_mov_b32 v237, v7
v_mov_b32 v238, v7
v_mov_b32 v239, v7
Mvaa 156,156,60
v_add_u32 v21, v26, v25
v_sub_u32 v22, v26, v25
v_add_u32 v23, 61440, v21
W 0
Cvaa 160,128,0,224
v_add_u32 v24, 61440, v22
v_add_u32 v25, 122880, v21
v_add_u32 v26, 122880, v22
s_mul_i32 s94, s59, 655360
Cvaa 164,128,16,228
s_mul_i32 s95, s58, 320
s_add_u32 s94, s94, s95
s_add_u32 s40, s26, s94
s_addc_u32 s41, s27, 0
Cvaa 168,128,32,232
Ra 144,242,12288
s_and_b32 s41, s41, 0xffff
s_mov_b32 s42, 5242880
s_mov_b32 s43, 0x20000
Cvaa 172,128,48,236
s_sub_u32 s42, s42, s94
s_mov_b64 s[44:45], s[8:9]
s_mov_b32 s47, 0x20000
s_mul_i32 s48, s14, 20480
Mvaa 160,132,4
s_and_b32 s94, s14, 1
s_lshl_b32 s95, s57, 1
s_add_u32 s94, s94, s95
s_lshl_b32 s54, s94, 3
Mvaa 164,132,20
s_add_u32 s54, s54, s59
s_lshl_b32 s54, s54, 6
s_xor_b32 s55, s54, 15872
s_add_u32 s54, s54, 20480
Mvaa 168,132,36
Ra 148,242,13312
s_add_u32 s55, s55, 20480
s_and_b32 s95, s14, 2
s_lshl_b32 s94, s94, 2
Mvaa 172,132,52
s_add_u32 s94, s94, s95
s_lshl_b32 s95, s95, 4
s_add_u32 s55, s55, s95
s_lshl_b32 s49, s94, 17
Mvaa 160,136,8
s_lshl_b32 s95, s59, 14
s_add_u32 s49, s49, s95
s_xor_b32 s50, s49, 16252928
s_add_u32 s49, s49, 41943040
Mvaa 164,136,24
s_add_u32 s50, s50, 41943040
s_lshr_b32 s94, s15, 3
s_lshl_b32 s94, s94, 3
s_add_u32 s94, s94, s14
Mvaa 168,136,40
Ra 152,242,14336
Ra 156,242,15360
s_lshl_b32 s94, s94, 4
s_add_u32 s94, s94, s59
)"
R"(Mvaa 172,136,56
s_mul_i32 s52, s94, 5120
s_add_u32 s52, s52, 62914560
s_lshl_b32 s53, s94, 3
s_add_u32 s53, s53, 65536
Mvaa 160,140,12
s_xor_b32 s62, s62, 0x10000
A 128,128,224
A 129,129,224
A 130,130,224
Mvaa 164,140,28
s_and_b32 s80, s14, 1
A 131,131,224
X 128
Mvaa 168,140,44
X 129
s_lshl_b32 s80, s80, 14
s_add_u32 s80, s80, 98304
Mvaa 172,140,60
X 130
K 192,128,129
W 0
Cvaa 176,144,0,224
X 131
A 132,132,228
K 193,130,131
Cvaa 180,144,16,228
A 133,133,228
A 134,134,228
Ra 128,242,24576
v_add_u32 v8, s80, v1
Cvaa 184,144,32,232
A 135,135,228
X 132
Ra 132,242,25600
Cvaa 188,144,48,236
X 133
X 134
Mvaa 176,148,4
K 200,132,133
X 135
A 136,136,232
Mvaa 180,148,20
K 201,134,135
A 137,137,232
A 138,138,232
A 139,139,232
Mvaa 184,148,36
X 136
X 137
Mvaa 188,148,52
X 138
Ra 136,242,26624
K 208,136,137
Mvaa 176,152,8
X 139
A 140,140,236
K 209,138,139
Mvaa 180,152,24
A 141,141,236
A 142,142,236
A 143,143,236
Ra 140,242,27648
Mvaa 184,152,40
X 140
X 141
Mvaa 188,152,56
X 142
K 216,140,141
Ra 144,242,28672
Mvaa 176,156,12
X 143
A 144,144,224
K 217,142,143
Mvaa 180,156,28
A 145,145,224
A 146,146,224
A 147,147,224
Ra 148,242,29696
Mvaa 184,156,44
X 144
X 145
X 146
K 194,144,145
X 147
A 148,148,228
K 195,146,147
A 149,149,228
A 150,150,228
A 151,151,228
X 148
X 149
X 150
K 202,148,149
X 151
A 152,152,232
K 203,150,151
A 153,153,232
A 154,154,232
A 155,155,232
X 152
X 153
X 154
K 210,152,153
X 155
A 156,156,236
K 211,154,155
A 157,157,236
A 158,158,236
A 159,159,236
X 156
X 157
X 158
K 218,156,157
X 159
X 172
K 219,158,159
Mvaa 188,156,60
Ra 152,242,30720
W 3
Zvav 32,128,192
Ra 156,242,31744
Zvav 64,128,200
Zvav 96,128,208
Zvav 128,128,216
Zvav 132,132,216
Zvav 100,132,208
Zvav 68,132,200
Zvav 36,132,192
Zvav 40,136,192
Zvav 72,136,200
X 173
Zvav 104,136,208
X 174
K 220,172,173
Zvav 136,136,216
X 175
Zvav 140,140,216
X 168
K 221,174,175
Zvav 108,140,208
X 169
X 170
Zvav 76,140,200
s_waitcnt vmcnt(0) lgkmcnt(0)
s_barrier
v_xor_b32 v242, 0x10000, v242
ds_read_b128 v[248:251], v8
Zvav 44,140,192
K 212,168,169
X 171
Ra 128,242,0
Zvav 48,144,192
X 164
K 213,170,171
Zvav 80,144,200
X 165
X 166
Zvav 112,144,208
K 204,164,165
X 167
Ra 132,242,1024
Zvav 144,144,216
X 160
Zvav 148,148,216
K 205,166,167
X 161
Zvav 116,148,208
X 162
Ra 136,242,2048
Zvav 84,148,200
K 196,160,161
X 163
W 3
Zvav 52,148,192
X 176
K 197,162,163
Zvav 56,152,192
X 177
Ra 140,242,3072
Zvav 88,152,200
X 178
K 198,176,177
Zvav 120,152,208
X 179
X 180
Zvav 152,152,216
K 199,178,179
Zvav 156,156,216
X 181
X 182
Zvav 124,156,208
K 206,180,181
X 183
Zvav 92,156,200
K 207,182,183
Zvav 60,156,192
s_cmp_eq_u32 s64, s65
s_cbranch_scc1 .Ll1t_33%=
.Ll0_31%=:
W 0
Cvaa 160,128,0,224
Ra 144,242,4096
Ra 148,242,5120
Cvaa 164,128,16,228
Ra 152,242,6144
s_xor_b32 s62, s62, 0x10000
Cvaa 168,128,32,232
X 184
Cvaa 172,128,48,236
X 185
Mvaa 160,132,4
X 186
Mvaa 164,132,20
K 214,184,185
Ra 156,242,7168
Mvaa 168,132,36
X 187
Mvaa 172,132,52
X 188
Mvaa 160,136,8
Ra 128,242,16384
Mvaa 164,136,24
K 215,186,187
Mvaa 168,136,40
X 189
Mvaa 172,136,56
X 190
Mvaa 160,140,12
Ra 132,242,17408
Mvaa 164,140,28
K 222,188,189
Mvaa 168,140,44
X 191
Mvaa 172,140,60
K 223,190,191
v_smfmac_f32_16x16x64_bf16 a[160:163], a[176:179], v[192:199], v240
Ra 136,242,18432
v_smfmac_f32_16x16x64_bf16 a[164:167], a[176:179], v[200:207], v240
X 160
v_smfmac_f32_16x16x64_bf16 a[168:171], a[176:179], v[208:215], v240
X 161
v_smfmac_f32_16x16x64_bf16 a[172:175], a[176:179], v[216:223], v240
W 3
Cvaa 176,144,0,224
X 162
Cvaa 180,144,16,228
Ra 140,242,19456
K 192,160,161
Cvaa 184,144,32,232
X 163
Cvaa 188,144,48,236
X 164
Mvaa 176,148,4
Ra 144,242,20480
Mvaa 180,148,20
K 193,162,163
Mvaa 184,148,36
X 165
Mvaa 188,148,52
X 166
Mvaa 176,152,8
Ra 148,242,21504
Mvaa 180,152,24
K 200,164,165
Mvaa 184,152,40
X 167
Mvaa 188,152,56
X 168
Mvaa 176,156,12
Ra 152,242,22528
Mvaa 180,156,28
K 201,166,167
Mvaa 184,156,44
X 169
Mvaa 188,156,60
W 3
Mvav 32,128,196
X 170
Mvav 64,128,204
Ra 156,242,23552
K 208,168,169
Mvav 96,128,212
X 171
Mvav 128,128,220
Mvav 132,132,220
X 172
Mvav 100,132,212
Ra 128,242,8192
s_add_u32 s64, s64, 1
Mvav 68,132,204
K 209,170,171
Mvav 36,132,196
X 173
Mvav 40,136,196
X 174
Mvav 72,136,204
Ra 132,242,9216
K 216,172,173
Mvav 104,136,212
X 175
Mvav 136,136,220
Mvav 140,140,220
X 176
Mvav 108,140,212
Ra 136,242,10240
K 217,174,175
Mvav 76,140,204
X 177
Mvav 44,140,196
W 3
Mvav 48,144,196
X 178
Mvav 80,144,204
Ra 140,242,11264
Mvav 112,144,212
K 194,176,177
Mvav 144,144,220
X 179
Mvav 148,148,220
X 180
Mvav 116,148,212
Ra 144,242,12288
Mvav 84,148,204
K 195,178,179
Mvav 52,148,196
X 181
Mvav 56,152,196
X 182
Mvav 88,152,204
Ra 148,242,13312
Mvav 120,152,212
K 202,180,181
Mvav 152,152,220
X 183
Mvav 156,156,220
X 184
Mvav 124,156,212
Ra 152,242,14336
Mvav 92,156,204
K 203,182,183
Mvav 60,156,196
W 3
Cvaa 160,128,0,224
X 185
Cvaa 164,128,16,228
X 186
Cvaa 168,128,32,232
Ra 156,242,15360
K 210,184,185
Cvaa 172,128,48,236
X 187
Mvaa 160,132,4
X 188
Mvaa 164,132,20
Ra 128,242,24576
Mvaa 168,132,36
K 211,186,187
Mvaa 172,132,52
X 189
Mvaa 160,136,8
X 190
Mvaa 164,136,24
Ra 132,242,25600
Mvaa 168,136,40
K 218,188,189
Mvaa 172,136,56
X 191
Mvaa 160,140,12
Ra 136,242,26624
Mvaa 164,140,28
Mvaa 168,140,44
Mvaa 172,140,60
W 3
Cvaa 176,144,0,224
Ra 140,242,27648
Cvaa 180,144,16,228
X 160
Cvaa 184,144,32,232
K 219,190,191
Cvaa 188,144,48,236
X 161
Mvaa 176,148,4
X 162
Mvaa 180,148,20
K 196,160,161
Ra 144,242,28672
Mvaa 184,148,36
X 163
Mvaa 188,148,52
X 164
Mvaa 176,152,8
K 197,162,163
Ra 148,242,29696
Mvaa 180,152,24
X 165
Mvaa 184,152,40
X 166
Mvaa 188,152,56
K 204,164,165
)"
R"(Mvaa 176,156,12
X 167
Mvaa 180,156,28
Ra 152,242,30720
Mvaa 184,156,44
X 168
Mvaa 188,156,60
K 205,166,167
W 3
Mvav 32,128,192
X 169
Mvav 64,128,200
Ra 156,242,31744
Mvav 96,128,208
X 170
Mvav 128,128,216
K 212,168,169
Mvav 132,132,216
X 171
Mvav 100,132,208
X 172
Mvav 68,132,200
Mvav 36,132,192
K 213,170,171
Mvav 40,136,192
X 173
Mvav 72,136,200
X 174
Mvav 104,136,208
Mvav 136,136,216
K 220,172,173
Mvav 140,140,216
X 175
Mvav 108,140,208
X 176
Mvav 76,140,200
W 0
s_barrier
v_xor_b32 v242, 0x10000, v242
s_bitcmp1_b32 s68, s64
s_cbranch_scc1 .Lact_44%=
.Lactr_45%=:
Mvav 44,140,192
K 221,174,175
Ra 128,242,0
Mvav 48,144,192
X 177
Mvav 80,144,200
X 178
Mvav 112,144,208
Ra 132,242,1024
Mvav 144,144,216
K 198,176,177
Mvav 148,148,216
X 179
Mvav 116,148,208
X 180
Mvav 84,148,200
Ra 136,242,2048
Mvav 52,148,192
K 199,178,179
Mvav 56,152,192
X 181
Mvav 88,152,200
Ra 140,242,3072
Mvav 120,152,208
X 182
Mvav 152,152,216
K 206,180,181
Mvav 156,156,216
X 183
Mvav 124,156,208
Mvav 92,156,200
K 207,182,183
Mvav 60,156,192
s_cmp_lg_u32 s64, s65
s_cbranch_scc1 .Ll0_31%=
s_cmp_lt_u32 s85, 54
s_cbranch_scc1 .Lmoe_42%=
.Ll1t_33%=:
s_cmp_eq_u32 s64, s66
s_cbranch_scc1 .Lsp1_34%=
.Ll1_32%=:
W 0
Cvaa 160,128,0,224
Ra 144,242,4096
Ra 148,242,5120
Cvaa 164,128,16,228
Ra 152,242,6144
s_add_u32 m0, s62, 0
Cvaa 168,128,32,232
X 184
Cvaa 172,128,48,236
X 185
Mvaa 160,132,4
X 186
Mvaa 164,132,20
K 214,184,185
Ra 156,242,7168
Mvaa 168,132,36
Dc 1,8,61,0
Mvaa 172,132,52
X 187
Mvaa 160,136,8
X 188
Mvaa 164,136,24
Ra 128,242,16384
K 215,186,187
Mvaa 168,136,40
Dc 1,8,61,1024
Mvaa 172,136,56
X 189
Mvaa 160,140,12
X 190
Mvaa 164,140,28
Ra 132,242,17408
K 222,188,189
Mvaa 168,140,44
Dc 1,8,61,2048
Mvaa 172,140,60
X 191
v_smfmac_f32_16x16x64_bf16 a[160:163], a[176:179], v[192:199], v240
K 223,190,191
Ra 136,242,18432
v_smfmac_f32_16x16x64_bf16 a[164:167], a[176:179], v[200:207], v240
X 160
v_smfmac_f32_16x16x64_bf16 a[168:171], a[176:179], v[208:215], v240
Dc 1,8,61,3072
v_smfmac_f32_16x16x64_bf16 a[172:175], a[176:179], v[216:223], v240
s_add_u32 m0, s62, 4096
W 3
Cvaa 176,144,0,224
X 161
Cvaa 180,144,16,228
X 162
Cvaa 184,144,32,232
Ra 140,242,19456
K 192,160,161
Cvaa 188,144,48,236
X 163
Mvaa 176,148,4
Dc 243,8,61,0
Ra 144,242,20480
Mvaa 180,148,20
X 164
Mvaa 184,148,36
K 193,162,163
Mvaa 188,148,52
X 165
Mvaa 176,152,8
Dc 243,8,61,1024
Ra 148,242,21504
Mvaa 180,152,24
X 166
Mvaa 184,152,40
K 200,164,165
Mvaa 188,152,56
X 167
Mvaa 176,156,12
Dc 243,8,61,2048
Ra 152,242,22528
Mvaa 180,156,28
X 168
Mvaa 184,156,44
K 201,166,167
Mvaa 188,156,60
W 3
Mvav 32,128,196
Dc 243,8,61,3072
s_add_u32 m0, s62, 8192
Mvav 64,128,204
X 169
Mvav 96,128,212
X 170
Mvav 128,128,220
Ra 156,242,23552
K 208,168,169
Mvav 132,132,220
X 171
Mvav 100,132,212
Dc 244,8,61,0
Ra 128,242,8192
Mvav 68,132,204
X 172
Mvav 36,132,196
K 209,170,171
Mvav 40,136,196
X 173
Mvav 72,136,204
Dc 244,8,61,1024
Ra 132,242,9216
Mvav 104,136,212
X 174
Mvav 136,136,220
K 216,172,173
Mvav 140,140,220
X 175
Mvav 108,140,212
Dc 244,8,61,2048
Ra 136,242,10240
Mvav 76,140,204
X 176
Mvav 44,140,196
K 217,174,175
W 3
Mvav 48,144,196
X 177
Mvav 80,144,204
Dc 244,8,61,3072
Ra 140,242,11264
Mvav 112,144,212
X 178
Mvav 144,144,220
s_add_u32 m0, s62, 12288
K 194,176,177
Mvav 148,148,220
X 179
Mvav 116,148,212
X 180
Mvav 84,148,204
Dc 245,8,61,0
Ra 144,242,12288
Mvav 52,148,196
K 195,178,179
Mvav 56,152,196
X 181
Mvav 88,152,204
X 182
Mvav 120,152,212
Dc 245,8,61,1024
Ra 148,242,13312
Mvav 152,152,220
K 202,180,181
Mvav 156,156,220
X 183
Mvav 124,156,212
X 184
Mvav 92,156,204
Dc 245,8,61,2048
Ra 152,242,14336
Mvav 60,156,196
K 203,182,183
W 3
Cvaa 160,128,0,224
X 185
Cvaa 164,128,16,228
X 186
Cvaa 168,128,32,232
Dc 245,8,61,3072
Ra 156,242,15360
Cvaa 172,128,48,236
K 210,184,185
s_add_u32 s61, s61, s63
Mvaa 160,132,4
X 187
Mvaa 164,132,20
X 188
Mvaa 168,132,36
Ra 128,242,24576
s_xor_b32 s62, s62, 0x10000
Mvaa 172,132,52
K 211,186,187
Mvaa 160,136,8
X 189
Mvaa 164,136,24
X 190
Mvaa 168,136,40
Ra 132,242,25600
s_add_u32 s64, s64, 1
Mvaa 172,136,56
K 218,188,189
Mvaa 160,140,12
X 191
Mvaa 164,140,28
Ra 136,242,26624
Mvaa 168,140,44
Mvaa 172,140,60
W 3
Cvaa 176,144,0,224
Ra 140,242,27648
Cvaa 180,144,16,228
X 160
Cvaa 184,144,32,232
K 219,190,191
Cvaa 188,144,48,236
X 161
Mvaa 176,148,4
X 162
Mvaa 180,148,20
K 196,160,161
Ra 144,242,28672
Mvaa 184,148,36
X 163
Mvaa 188,148,52
X 164
Mvaa 176,152,8
K 197,162,163
Ra 148,242,29696
Mvaa 180,152,24
X 165
Mvaa 184,152,40
X 166
Mvaa 188,152,56
K 204,164,165
Mvaa 176,156,12
X 167
Mvaa 180,156,28
Ra 152,242,30720
Mvaa 184,156,44
X 168
Mvaa 188,156,60
K 205,166,167
W 3
Mvav 32,128,192
X 169
Mvav 64,128,200
Ra 156,242,31744
Mvav 96,128,208
X 170
Mvav 128,128,216
K 212,168,169
Mvav 132,132,216
X 171
Mvav 100,132,208
X 172
Mvav 68,132,200
Mvav 36,132,192
K 213,170,171
Mvav 40,136,192
X 173
Mvav 72,136,200
X 174
Mvav 104,136,208
Mvav 136,136,216
K 220,172,173
Mvav 140,140,216
X 175
Mvav 108,140,208
X 176
Mvav 76,140,200
s_waitcnt vmcnt(0) lgkmcnt(0)
s_barrier
v_xor_b32 v242, 0x10000, v242
s_bitcmp1_b32 s68, s64
s_cbranch_scc1 .Lact_46%=
.Lactr_47%=:
Mvav 44,140,192
K 221,174,175
Ra 128,242,0
Mvav 48,144,192
X 177
Mvav 80,144,200
X 178
Mvav 112,144,208
Ra 132,242,1024
Mvav 144,144,216
K 198,176,177
Mvav 148,148,216
X 179
Mvav 116,148,208
X 180
Mvav 84,148,200
Ra 136,242,2048
Mvav 52,148,192
K 199,178,179
Mvav 56,152,192
X 181
Mvav 88,152,200
Ra 140,242,3072
Mvav 120,152,208
X 182
Mvav 152,152,216
K 206,180,181
Mvav 156,156,216
X 183
Mvav 124,156,208
Mvav 92,156,200
K 207,182,183
Mvav 60,156,192
s_cmp_lg_u32 s64, s66
s_cbranch_scc1 .Ll1_32%=
s_cmp_ge_u32 s14, 2
s_cbranch_scc1 .Lf1_38%=
.Lsp1_34%=:
W 0
Cvaa 160,128,0,224
Ra 144,242,4096
Ra 148,242,5120
Cvaa 164,128,16,228
Ra 152,242,6144
Ra 156,242,7168
Cvaa 168,128,32,232
X 184
Cvaa 172,128,48,236
X 185
Mvaa 160,132,4
s_add_u32 m0, s62, 0
Ra 128,242,16384
Mvaa 164,132,20
X 186
)"
R"(Mvaa 168,132,36
K 214,184,185
Dc 1,8,61,0
Mvaa 172,132,52
X 187
Mvaa 160,136,8
X 188
Mvaa 164,136,24
K 215,186,187
Dc 1,8,61,1024
Mvaa 168,136,40
X 189
Mvaa 172,136,56
Ra 132,242,17408
Mvaa 160,140,12
X 190
Mvaa 164,140,28
Dc 1,8,61,2048
K 222,188,189
Mvaa 168,140,44
X 191
Mvaa 172,140,60
Ra 136,242,18432
K 223,190,191
v_smfmac_f32_16x16x64_bf16 a[160:163], a[176:179], v[192:199], v240
Dc 1,8,61,3072
v_cndmask_b32_e64 v160, v241, v160, s[72:73]
v_smfmac_f32_16x16x64_bf16 a[164:167], a[176:179], v[200:207], v240
v_cndmask_b32_e64 v161, v241, v161, s[74:75]
v_cndmask_b32_e64 v162, v241, v162, s[76:77]
v_smfmac_f32_16x16x64_bf16 a[168:171], a[176:179], v[208:215], v240
v_cndmask_b32_e64 v163, v241, v163, s[78:79]
s_add_u32 m0, s62, 4096
v_smfmac_f32_16x16x64_bf16 a[172:175], a[176:179], v[216:223], v240
Ra 140,242,19456
W 4
Cvaa 180,144,16,228
X 160
Cvaa 184,144,32,232
X 161
Cvaa 188,144,48,236
Dc 243,8,61,0
Ra 144,242,20480
Mvaa 180,148,20
X 162
Mvaa 184,148,36
K 192,160,161
Mvaa 188,148,52
X 163
Mvaa 180,152,24
Dc 243,8,61,1024
Ra 148,242,21504
Mvaa 184,152,40
X 164
Mvaa 188,152,56
K 193,162,163
Ra 152,242,22528
Mvaa 180,156,28
Dc 243,8,61,2048
Mvaa 184,156,44
X 165
Mvaa 188,156,60
W 3
Mvav 32,128,196
X 166
Mvav 64,128,204
K 200,164,165
Dc 243,8,61,3072
Mvav 96,128,212
X 167
Mvav 128,128,220
X 168
Mvav 132,132,220
s_add_u32 m0, s62, 8192
K 201,166,167
Mvav 100,132,212
Ra 156,242,23552
Dc 244,8,61,0
Mvav 68,132,204
X 169
Mvav 36,132,196
X 170
Mvav 40,136,196
K 208,168,169
Ra 128,242,8192
Mvav 72,136,204
X 171
Mvav 104,136,212
Dc 244,8,61,1024
Ra 132,242,9216
Mvav 136,136,220
X 172
Mvav 140,140,220
K 209,170,171
Ra 136,242,10240
Mvav 108,140,212
X 173
Mvav 76,140,204
Dc 244,8,61,2048
Mvav 44,140,196
Ra 140,242,11264
W 4
Mvav 48,144,196
X 174
Mvav 80,144,204
K 216,172,173
Dc 244,8,61,3072
Mvav 112,144,212
X 175
Mvav 144,144,220
v_mov_b32 v194, 0
K 217,174,175
Mvav 148,148,220
v_mov_b32 v195, 0
s_add_u32 m0, s62, 12288
Mvav 116,148,212
v_cndmask_b32_e64 v180, v241, v180, s[72:73]
v_cndmask_b32_e64 v181, v241, v181, s[74:75]
Mvav 84,148,204
v_cndmask_b32_e64 v182, v241, v182, s[76:77]
v_cndmask_b32_e64 v183, v241, v183, s[78:79]
Mvav 52,148,196
Dc 245,8,61,0
Ra 144,242,12288
Mvav 56,152,196
X 180
Mvav 88,152,204
X 181
Mvav 120,152,212
Dc 245,8,61,1024
Ra 148,242,13312
Mvav 152,152,220
X 182
Mvav 156,156,220
K 202,180,181
Ra 152,242,14336
Mvav 124,156,212
X 183
Mvav 92,156,204
Dc 245,8,61,2048
Mvav 60,156,196
Ra 156,242,15360
W 4
Cvaa 168,128,32,232
X 184
Cvaa 172,128,48,236
K 203,182,183
Dc 245,8,61,3072
Mvaa 168,132,36
X 185
Mvaa 172,132,52
X 186
Mvaa 168,136,40
s_add_u32 s61, s61, s63
K 210,184,185
Mvaa 172,136,56
X 187
Mvaa 168,140,44
Ra 128,242,24576
Ra 132,242,25600
Ra 136,242,26624
Mvaa 172,140,60
X 188
K 211,186,187
X 189
X 190
K 218,188,189
X 191
v_mov_b32 v196, 0
Ra 140,242,27648
K 219,190,191
W 4
Cvaa 188,144,48,236
s_xor_b32 s62, s62, 0x10000
s_add_u32 s64, s64, 1
Mvaa 188,148,52
Ra 144,242,28672
v_mov_b32 v197, 0
Mvaa 188,152,56
v_mov_b32 v204, 0
Ra 148,242,29696
Mvaa 188,156,60
v_mov_b32 v205, 0
W 2
Mvav 32,128,192
Ra 152,242,30720
v_cndmask_b32_e64 v168, v241, v168, s[72:73]
Mvav 64,128,200
v_cndmask_b32_e64 v169, v241, v169, s[74:75]
v_cndmask_b32_e64 v170, v241, v170, s[76:77]
Mvav 96,128,208
Ra 156,242,31744
v_cndmask_b32_e64 v171, v241, v171, s[78:79]
Mvav 128,128,216
X 168
Mvav 132,132,216
X 169
Mvav 100,132,208
X 170
Mvav 68,132,200
v_mov_b32 v224, v250
K 212,168,169
Mvav 36,132,192
v_mov_b32 v225, v250
v_mov_b32 v226, v250
Mvav 40,136,192
X 171
Mvav 72,136,200
X 172
Mvav 104,136,208
v_mov_b32 v227, v250
K 213,170,171
Mvav 136,136,216
X 173
Mvav 140,140,216
v_mov_b32 v228, v251
v_mov_b32 v229, v251
Mvav 108,140,208
X 174
Mvav 76,140,200
s_waitcnt vmcnt(0) lgkmcnt(0)
s_barrier
v_xor_b32 v242, 0x10000, v242
s_bitcmp1_b32 s68, s64
s_cbranch_scc1 .Lact_48%=
.Lactr_49%=:
Mvav 44,140,192
K 220,172,173
v_mov_b32 v230, v251
Mvav 48,144,192
X 175
Mvav 80,144,200
v_mov_b32 v231, v251
v_mov_b32 v198, 0
Mvav 112,144,208
Ra 128,242,40960
K 221,174,175
Mvav 144,144,216
v_mov_b32 v232, v248
v_mov_b32 v199, 0
Mvav 148,148,216
v_mov_b32 v206, 0
v_mov_b32 v233, v248
Mvav 116,148,208
v_mov_b32 v207, 0
Ra 132,242,41984
Mvav 84,148,200
v_mov_b32 v214, 0
v_mov_b32 v234, v248
Mvav 52,148,192
v_mov_b32 v215, 0
v_cndmask_b32_e64 v188, v241, v188, s[72:73]
Mvav 56,152,192
v_mov_b32 v235, v248
v_cndmask_b32_e64 v189, v241, v189, s[74:75]
Mvav 88,152,200
Ra 136,242,43008
v_cndmask_b32_e64 v190, v241, v190, s[76:77]
Mvav 120,152,208
v_mov_b32 v236, v249
v_cndmask_b32_e64 v191, v241, v191, s[78:79]
Mvav 152,152,216
X 188
Mvav 156,156,216
v_mov_b32 v237, v249
Ra 140,242,44032
Mvav 124,156,208
X 189
Mvav 92,156,200
v_mov_b32 v238, v249
v_mov_b32 v239, v249
Mvav 60,156,192
X 190
K 222,188,189
X 191
s_nop 0
K 223,190,191
s_cmp_eq_u32 s64, 15
s_cbranch_scc1 .Lfs_40%=
.Lsp2_35%=:
v_smfmac_f32_16x16x64_bf16 a[160:163], a[176:179], v[192:199], v240
Ra 144,242,45056
Ra 148,242,46080
Ra 152,242,47104
Ra 156,242,48128
s_add_u32 m0, s62, 0
v_smfmac_f32_16x16x64_bf16 a[164:167], a[176:179], v[200:207], v240
v_smfmac_f32_16x16x64_bf16 a[168:171], a[176:179], v[208:215], v240
Dc 1,8,61,0
v_smfmac_f32_16x16x64_bf16 a[172:175], a[176:179], v[216:223], v240
W 4
Cvaa 168,128,96,232
v_mov_b32 v192, 0
Dc 1,8,61,1024
G 12,160
Cvaa 172,128,112,236
G 4,161
A 12,12,4
v_cmp_nlt_f32_e64 s[86:87], v12, s90
v_mov_b32 v193, 0
Ra 128,242,16384
Mvaa 168,132,100
v_rcp_f32 v12, v12
s_or_b64 s[88:89], s[88:89], s[86:87]
v_mov_b32 v200, 0
v_accvgpr_write_b32 a160, 0
Dc 1,8,61,2048
Mvaa 172,132,116
v_accvgpr_write_b32 a161, 0
v_accvgpr_write_b32 a162, 0
v_mov_b32 v201, 0
v_accvgpr_write_b32 a163, 0
G 13,164
Ra 132,242,17408
Mvaa 168,136,104
G 5,165
A 13,13,5
v_cmp_nlt_f32_e64 s[86:87], v13, s90
v_rcp_f32 v13, v13
Dc 1,8,61,3072
Mvaa 172,136,120
s_or_b64 s[88:89], s[88:89], s[86:87]
v_accvgpr_write_b32 a164, 0
v_accvgpr_write_b32 a165, 0
Ra 136,242,18432
v_accvgpr_write_b32 a166, 0
Mvaa 168,140,108
v_accvgpr_write_b32 a167, 0
U 32,32,12
U 33,33,12
s_add_u32 m0, s62, 4096
U 34,34,12
Mvaa 172,140,124
Ra 140,242,19456
U 35,35,12
U 36,36,12
U 37,37,12
U 38,38,12
W 4
Cvaa 188,144,112,236
U 39,39,12
K 32,32,33
K 33,34,35
Ra 144,242,20480
Dc 243,8,61,0
K 34,36,37
Mvaa 188,148,116
K 35,38,39
s_add_u32 s93, s67, 0
Ra 148,242,21504
v_cndmask_b32_e64 v168, v241, v168, s[72:73]
v_cndmask_b32_e64 v169, v241, v169, s[74:75]
v_cndmask_b32_e64 v170, v241, v170, s[76:77]
Mvaa 188,152,120
buffer_store_dwordx4 v[32:35], v1, s[8:11], s93 offen offset:0 sc1
v_cndmask_b32_e64 v171, v241, v171, s[78:79]
X 168
X 169
Mvaa 188,156,124
U 40,40,12
U 41,41,12
U 42,42,12
X 170
W 2
Mvav 96,128,212
U 43,43,12
Ra 152,242,22528
Dc 243,8,61,1024
U 44,44,12
U 45,45,12
U 46,46,12
Mvav 128,128,220
U 47,47,12
Ra 156,242,23552
K 40,40,41
K 208,168,169
K 41,42,43
K 42,44,45
Mvav 132,132,220
K 43,46,47
X 171
Dc 243,8,61,2048
Ra 128,242,32768
Mvav 100,132,212
X 172
Ra 132,242,33792
Mvav 104,136,212
buffer_store_dwordx4 v[40:43], v1, s[8:11], s93 offen offset:1024 sc1
U 48,48,12
U 49,49,12
U 50,50,12
U 51,51,12
U 52,52,12
Mvav 136,136,220
U 53,53,12
U 54,54,12
U 55,55,12
K 48,48,49
K 49,50,51
K 50,52,53
)"
R"(Mvav 140,140,220
Dc 243,8,61,3072
K 51,54,55
K 209,170,171
X 173
s_add_u32 m0, s62, 8192
Mvav 108,140,212
Ra 136,242,34816
X 174
Ra 140,242,35840
W 4
Mvav 112,144,212
buffer_store_dwordx4 v[48:51], v1, s[8:11], s93 offen offset:2048 sc1
U 56,56,12
U 57,57,12
U 58,58,12
U 59,59,12
U 60,60,12
Mvav 144,144,220
U 61,61,12
U 62,62,12
U 63,63,12
K 56,56,57
K 57,58,59
K 58,60,61
Mvav 148,148,220
Dc 244,8,61,0
K 59,62,63
K 216,172,173
X 175
Ra 144,242,36864
Mvav 116,148,212
v_mov_b32 v194, 0
K 217,174,175
Ra 148,242,37888
Mvav 120,152,212
buffer_store_dwordx4 v[56:59], v1, s[8:11], s93 offen offset:3072 sc1
G 14,168
G 6,169
A 14,14,6
v_cmp_nlt_f32_e64 s[86:87], v14, s90
v_mov_b32 v195, 0
Mvav 152,152,220
v_rcp_f32 v14, v14
s_or_b64 s[88:89], s[88:89], s[86:87]
v_accvgpr_write_b32 a168, 0
v_accvgpr_write_b32 a169, 0
v_accvgpr_write_b32 a170, 0
Mvav 156,156,220
Dc 244,8,61,1024
v_accvgpr_write_b32 a171, 0
G 15,172
G 7,173
A 15,15,7
v_cmp_nlt_f32_e64 s[86:87], v15, s90
Mvav 124,156,212
v_rcp_f32 v15, v15
s_or_b64 s[88:89], s[88:89], s[86:87]
v_accvgpr_write_b32 a172, 0
v_accvgpr_write_b32 a173, 0
W 2
Cvaa 160,128,64,224
Dc 244,8,61,2048
v_accvgpr_write_b32 a174, 0
v_mov_b32 v202, 0
v_accvgpr_write_b32 a175, 0
U 64,64,13
U 65,65,13
Cvaa 164,128,80,228
v_mov_b32 v203, 0
U 66,66,13
Ra 152,242,38912
U 67,67,13
U 68,68,13
v_mov_b32 v210, 0
Cvaa 168,128,96,232
Dc 244,8,61,3072
U 69,69,13
U 70,70,13
s_add_u32 m0, s62, 12288
U 71,71,13
K 64,64,65
Cvaa 172,128,112,236
v_mov_b32 v211, 0
K 65,66,67
K 66,68,69
Ra 156,242,39936
K 67,70,71
v_cndmask_b32_e64 v188, v241, v188, s[72:73]
Mvaa 160,132,68
s_add_u32 s93, s67, 131072
buffer_store_dwordx4 v[64:67], v1, s[8:11], s93 offen offset:0 sc1
U 72,72,13
v_cndmask_b32_e64 v189, v241, v189, s[74:75]
U 73,73,13
U 74,74,13
Mvaa 164,132,84
U 75,75,13
v_cndmask_b32_e64 v190, v241, v190, s[76:77]
U 76,76,13
U 77,77,13
U 78,78,13
v_cndmask_b32_e64 v191, v241, v191, s[78:79]
Mvaa 168,132,100
Dc 245,8,61,0
U 79,79,13
K 72,72,73
K 73,74,75
K 74,76,77
K 75,78,79
Mvaa 172,132,116
X 188
Ra 128,242,49152
X 189
Ra 132,242,50176
Mvaa 160,136,72
Dc 245,8,61,1024
X 190
K 218,188,189
X 191
Mvaa 164,136,88
Mvaa 168,136,104
buffer_store_dwordx4 v[72:75], v1, s[8:11], s93 offen offset:1024 sc1
U 80,80,13
U 81,81,13
U 82,82,13
U 83,83,13
U 84,84,13
Mvaa 172,136,120
U 85,85,13
U 86,86,13
U 87,87,13
K 80,80,81
K 81,82,83
K 82,84,85
Mvaa 160,140,76
Dc 245,8,61,2048
K 83,86,87
Ra 136,242,51200
Mvaa 164,140,92
Mvaa 168,140,108
Dc 245,8,61,3072
s_add_u32 s61, s61, s63
s_xor_b32 s62, s62, 0x10000
s_add_u32 s64, s64, 1
s_add_u32 s70, s64, 1
Mvaa 172,140,124
Ra 140,242,52224
v_cndmask_b32_e64 v160, v241, v160, s[72:73]
K 219,190,191
v_cndmask_b32_e64 v161, v241, v161, s[74:75]
v_cndmask_b32_e64 v162, v241, v162, s[76:77]
W 4
Cvaa 180,144,80,228
buffer_store_dwordx4 v[80:83], v1, s[8:11], s93 offen offset:2048 sc1
U 88,88,13
U 89,89,13
U 90,90,13
U 91,91,13
U 92,92,13
Cvaa 184,144,96,232
U 93,93,13
U 94,94,13
U 95,95,13
K 88,88,89
K 89,90,91
K 90,92,93
Cvaa 188,144,112,236
K 91,94,95
buffer_store_dwordx4 v[88:91], v1, s[8:11], s93 offen offset:3072 sc1
U 96,96,14
U 97,97,14
U 98,98,14
U 99,99,14
Mvaa 180,148,84
U 100,100,14
U 101,101,14
U 102,102,14
U 103,103,14
K 96,96,97
K 97,98,99
Mvaa 184,148,100
K 98,100,101
K 99,102,103
s_add_u32 s93, s67, 262144
buffer_store_dwordx4 v[96:99], v1, s[8:11], s93 offen offset:0 sc1
U 104,104,14
U 105,105,14
Mvaa 188,148,116
U 106,106,14
U 107,107,14
U 108,108,14
U 109,109,14
U 110,110,14
U 111,111,14
Mvaa 180,152,88
v_cndmask_b32_e64 v163, v241, v163, s[78:79]
X 160
X 161
K 104,104,105
Mvaa 184,152,104
K 105,106,107
K 106,108,109
K 107,110,111
s_bitset1_b32 s68, s70
buffer_store_dwordx4 v[104:107], v1, s[8:11], s93 offen offset:1024 sc1
U 112,112,14
Mvaa 188,152,120
U 113,113,14
X 162
U 114,114,14
U 115,115,14
U 116,116,14
Mvaa 180,156,92
U 117,117,14
U 118,118,14
Ra 144,242,53248
U 119,119,14
K 196,160,161
K 112,112,113
Mvaa 184,156,108
K 113,114,115
K 114,116,117
X 163
K 115,118,119
buffer_store_dwordx4 v[112:115], v1, s[8:11], s93 offen offset:2048 sc1
Mvaa 188,156,124
U 120,120,14
U 121,121,14
U 122,122,14
U 123,123,14
U 124,124,14
W 1
Zvav 32,128,192
X 164
U 125,125,14
Ra 148,242,54272
U 126,126,14
U 127,127,14
Zvav 64,128,200
K 120,120,121
K 121,122,123
K 122,124,125
K 197,162,163
K 123,126,127
buffer_store_dwordx4 v[120:123], v1, s[8:11], s93 offen offset:3072 sc1
Zvav 96,128,208
U 128,128,15
U 129,129,15
U 130,130,15
U 131,131,15
U 132,132,15
U 133,133,15
U 134,134,15
U 135,135,15
K 128,128,129
K 129,130,131
K 130,132,133
K 131,134,135
s_add_u32 s93, s67, 393216
buffer_store_dwordx4 v[128:131], v1, s[8:11], s93 offen offset:0 sc1
s_nop 1
Zvav 128,128,216
X 165
Ra 152,242,55296
X 166
K 204,164,165
Zvav 132,132,216
U 136,136,15
U 137,137,15
U 138,138,15
X 167
U 139,139,15
Zvav 100,132,208
U 140,140,15
U 141,141,15
U 142,142,15
Ra 156,242,56320
U 143,143,15
K 136,136,137
Zvav 68,132,200
K 137,138,139
X 168
K 138,140,141
K 139,142,143
buffer_store_dwordx4 v[136:139], v1, s[8:11], s93 offen offset:1024 sc1
Zvav 36,132,192
U 144,144,15
U 145,145,15
U 146,146,15
K 205,166,167
U 147,147,15
U 148,148,15
Zvav 40,136,192
U 149,149,15
X 169
U 150,150,15
U 151,151,15
K 144,144,145
Zvav 72,136,200
K 145,146,147
K 146,148,149
K 147,150,151
buffer_store_dwordx4 v[144:147], v1, s[8:11], s93 offen offset:2048 sc1
X 170
Zvav 104,136,208
U 152,152,15
U 153,153,15
U 154,154,15
U 155,155,15
U 156,156,15
U 157,157,15
Zvav 136,136,216
K 212,168,169
U 158,158,15
U 159,159,15
K 152,152,153
X 171
Zvav 140,140,216
K 153,154,155
K 154,156,157
K 155,158,159
buffer_store_dwordx4 v[152:155], v1, s[8:11], s93 offen offset:3072 sc1
X 172
Zvav 108,140,208
K 213,170,171
X 173
X 174
Zvav 76,140,200
s_waitcnt vmcnt(10) lgkmcnt(0)
s_barrier
v_xor_b32 v242, 0x10000, v242
s_bitcmp1_b32 s68, s64
s_cbranch_scc1 .Lact_50%=
.Lactr_51%=:
Zvav 44,140,192
K 220,172,173
X 175
Ra 128,242,40960
Zvav 48,144,192
Zvav 80,144,200
v_mov_b32 v198, 0
Zvav 112,144,208
K 221,174,175
Ra 132,242,41984
Zvav 144,144,216
v_mov_b32 v199, 0
v_cndmask_b32_e64 v180, v241, v180, s[72:73]
Zvav 148,148,216
v_cndmask_b32_e64 v181, v241, v181, s[74:75]
Zvav 116,148,208
v_cndmask_b32_e64 v182, v241, v182, s[76:77]
Ra 136,242,43008
Zvav 84,148,200
v_cndmask_b32_e64 v183, v241, v183, s[78:79]
Zvav 52,148,192
X 180
Zvav 56,152,192
X 181
Ra 140,242,44032
Zvav 88,152,200
Zvav 120,152,208
X 182
Zvav 152,152,216
K 206,180,181
Zvav 156,156,216
X 183
Zvav 124,156,208
Zvav 92,156,200
K 207,182,183
Zvav 60,156,192
s_cmp_eq_u32 s64, 15
s_cbranch_scc1 .Lf2_39%=
.Ll2_36%=:
W 0
Cvaa 160,128,64,224
Ra 144,242,45056
Ra 148,242,46080
Cvaa 164,128,80,228
Ra 152,242,47104
s_add_u32 m0, s62, 0
Cvaa 168,128,96,232
X 184
Cvaa 172,128,112,236
X 185
Mvaa 160,132,68
X 186
Mvaa 164,132,84
K 214,184,185
Ra 156,242,48128
Mvaa 168,132,100
Dc 1,8,61,0
Mvaa 172,132,116
X 187
Mvaa 160,136,72
X 188
Mvaa 164,136,88
Ra 128,242,57344
K 215,186,187
Mvaa 168,136,104
)"
R"(Dc 1,8,61,1024
Mvaa 172,136,120
X 189
Mvaa 160,140,76
X 190
Mvaa 164,140,92
Ra 132,242,58368
K 222,188,189
Mvaa 168,140,108
Dc 1,8,61,2048
Mvaa 172,140,124
X 191
v_smfmac_f32_16x16x64_bf16 a[160:163], a[176:179], v[192:199], v240
K 223,190,191
Ra 136,242,59392
v_smfmac_f32_16x16x64_bf16 a[164:167], a[176:179], v[200:207], v240
X 160
v_smfmac_f32_16x16x64_bf16 a[168:171], a[176:179], v[208:215], v240
Dc 1,8,61,3072
v_smfmac_f32_16x16x64_bf16 a[172:175], a[176:179], v[216:223], v240
s_add_u32 m0, s62, 4096
W 3
Cvaa 176,144,64,224
X 161
Cvaa 180,144,80,228
X 162
Cvaa 184,144,96,232
Ra 140,242,60416
K 192,160,161
Cvaa 188,144,112,236
X 163
Mvaa 176,148,68
Dc 243,8,61,0
Ra 144,242,61440
Mvaa 180,148,84
X 164
Mvaa 184,148,100
K 193,162,163
Mvaa 188,148,116
X 165
Mvaa 176,152,72
Dc 243,8,61,1024
Ra 148,242,62464
Mvaa 180,152,88
X 166
Mvaa 184,152,104
K 200,164,165
Mvaa 188,152,120
X 167
Mvaa 176,156,76
Dc 243,8,61,2048
Ra 152,242,63488
Mvaa 180,156,92
X 168
Mvaa 184,156,108
K 201,166,167
Mvaa 188,156,124
W 3
Mvav 32,128,196
Dc 243,8,61,3072
s_add_u32 m0, s62, 8192
Mvav 64,128,204
X 169
Mvav 96,128,212
X 170
Mvav 128,128,220
Ra 156,242,64512
K 208,168,169
Mvav 132,132,220
X 171
Mvav 100,132,212
Dc 244,8,61,0
Ra 128,242,32768
Mvav 68,132,204
X 172
Mvav 36,132,196
K 209,170,171
Mvav 40,136,196
X 173
Mvav 72,136,204
Dc 244,8,61,1024
Ra 132,242,33792
Mvav 104,136,212
X 174
Mvav 136,136,220
K 216,172,173
Mvav 140,140,220
X 175
Mvav 108,140,212
Dc 244,8,61,2048
Ra 136,242,34816
Mvav 76,140,204
X 176
Mvav 44,140,196
K 217,174,175
W 3
Mvav 48,144,196
X 177
Mvav 80,144,204
Dc 244,8,61,3072
Ra 140,242,35840
Mvav 112,144,212
X 178
Mvav 144,144,220
s_add_u32 m0, s62, 12288
K 194,176,177
Mvav 148,148,220
X 179
Mvav 116,148,212
X 180
Mvav 84,148,204
Dc 245,8,61,0
Ra 144,242,36864
Mvav 52,148,196
K 195,178,179
Mvav 56,152,196
X 181
Mvav 88,152,204
X 182
Mvav 120,152,212
Dc 245,8,61,1024
Ra 148,242,37888
Mvav 152,152,220
K 202,180,181
Mvav 156,156,220
X 183
Mvav 124,156,212
X 184
Mvav 92,156,204
Dc 245,8,61,2048
Ra 152,242,38912
Mvav 60,156,196
K 203,182,183
W 3
Cvaa 160,128,64,224
X 185
Cvaa 164,128,80,228
X 186
Cvaa 168,128,96,232
Dc 245,8,61,3072
Ra 156,242,39936
Cvaa 172,128,112,236
K 210,184,185
s_add_u32 s61, s61, s63
Mvaa 160,132,68
X 187
Mvaa 164,132,84
X 188
Mvaa 168,132,100
Ra 128,242,49152
s_xor_b32 s62, s62, 0x10000
Mvaa 172,132,116
K 211,186,187
Mvaa 160,136,72
X 189
Mvaa 164,136,88
X 190
Mvaa 168,136,104
Ra 132,242,50176
s_add_u32 s64, s64, 1
Mvaa 172,136,120
K 218,188,189
Mvaa 160,140,76
X 191
Mvaa 164,140,92
Ra 136,242,51200
Mvaa 168,140,108
Mvaa 172,140,124
W 3
Cvaa 176,144,64,224
Ra 140,242,52224
Cvaa 180,144,80,228
X 160
Cvaa 184,144,96,232
K 219,190,191
Cvaa 188,144,112,236
X 161
Mvaa 176,148,68
X 162
Mvaa 180,148,84
K 196,160,161
Ra 144,242,53248
Mvaa 184,148,100
X 163
Mvaa 188,148,116
X 164
Mvaa 176,152,72
K 197,162,163
Ra 148,242,54272
Mvaa 180,152,88
X 165
Mvaa 184,152,104
X 166
Mvaa 188,152,120
K 204,164,165
Mvaa 176,156,76
X 167
Mvaa 180,156,92
Ra 152,242,55296
Mvaa 184,156,108
X 168
Mvaa 188,156,124
K 205,166,167
W 3
Mvav 32,128,192
X 169
Mvav 64,128,200
Ra 156,242,56320
Mvav 96,128,208
X 170
Mvav 128,128,216
K 212,168,169
Mvav 132,132,216
X 171
Mvav 100,132,208
X 172
Mvav 68,132,200
Mvav 36,132,192
K 213,170,171
Mvav 40,136,192
X 173
Mvav 72,136,200
X 174
Mvav 104,136,208
Mvav 136,136,216
K 220,172,173
Mvav 140,140,216
X 175
Mvav 108,140,208
X 176
Mvav 76,140,200
s_waitcnt vmcnt(0) lgkmcnt(0)
s_barrier
v_xor_b32 v242, 0x10000, v242
s_bitcmp1_b32 s68, s64
s_cbranch_scc1 .Lact_52%=
.Lactr_53%=:
Mvav 44,140,192
K 221,174,175
Ra 128,242,40960
Mvav 48,144,192
X 177
Mvav 80,144,200
X 178
Mvav 112,144,208
Ra 132,242,41984
Mvav 144,144,216
K 198,176,177
Mvav 148,148,216
X 179
Mvav 116,148,208
X 180
Mvav 84,148,200
Ra 136,242,43008
Mvav 52,148,192
K 199,178,179
Mvav 56,152,192
X 181
Mvav 88,152,200
Ra 140,242,44032
Mvav 120,152,208
X 182
Mvav 152,152,216
K 206,180,181
Mvav 156,156,216
X 183
Mvav 124,156,208
Mvav 92,156,200
K 207,182,183
Mvav 60,156,192
s_cmp_lg_u32 s64, 15
s_cbranch_scc1 .Ll2_36%=
.Lf2_39%=:
W 0
Cvaa 160,128,64,224
Ra 144,242,45056
Ra 148,242,46080
Cvaa 164,128,80,228
Ra 152,242,47104
s_lshr_b32 s92, s14, 1
Cvaa 168,128,96,232
X 184
Cvaa 172,128,112,236
X 185
Mvaa 160,132,68
s_and_b32 s71, s14, 1
s_mul_i32 s71, s71, 5120
Mvaa 164,132,84
X 186
Mvaa 168,132,100
K 214,184,185
Ra 156,242,48128
Mvaa 172,132,116
X 187
Mvaa 160,136,72
v_bfe_u32 v28, v1, 4, 2
v_lshlrev_b32 v28, 3, v28
Mvaa 164,136,88
X 188
Mvaa 168,136,104
K 215,186,187
buffer_load_dwordx2 v[28:29], v28, s[8:11], s54 offen sc1
Mvaa 172,136,120
X 189
Mvaa 160,140,76
Ra 128,242,57344
s_add_u32 s94, s58, 0
Mvaa 164,140,92
X 190
Mvaa 168,140,108
K 222,188,189
s_and_b32 s94, s94, 3
Mvaa 172,140,124
X 191
v_smfmac_f32_16x16x64_bf16 a[160:163], a[176:179], v[192:199], v240
s_mul_i32 s94, s94, 163840
K 223,190,191
v_smfmac_f32_16x16x64_bf16 a[164:167], a[176:179], v[200:207], v240
Ra 132,242,58368
s_add_u32 s94, s94, s48
v_smfmac_f32_16x16x64_bf16 a[168:171], a[176:179], v[208:215], v240
s_mul_i32 s95, s92, 10240
s_add_u32 s95, s95, s71
v_smfmac_f32_16x16x64_bf16 a[172:175], a[176:179], v[216:223], v240
Ra 136,242,59392
W 3
Cvaa 176,144,64,224
X 160
Cvaa 180,144,80,228
X 161
Cvaa 184,144,96,232
X 162
Cvaa 188,144,112,236
K 192,160,161
s_add_u32 m0, s95, 0
Mvaa 176,148,68
X 163
Mvaa 180,148,84
X 164
Mvaa 184,148,100
D 16,40,94,0
K 193,162,163
Mvaa 188,148,116
Ra 140,242,60416
Ra 144,242,61440
Mvaa 176,152,72
X 165
Mvaa 180,152,88
D 17,40,94,1024
Ra 148,242,62464
Mvaa 184,152,104
X 166
Mvaa 188,152,120
K 200,164,165
Mvaa 176,156,76
D 18,40,94,2048
Ra 152,242,63488
Mvaa 180,156,92
X 167
Mvaa 184,156,108
X 172
Mvaa 188,156,124
D 19,40,94,3072
W 3
Mvav 32,128,196
s_add_u32 m0, s95, 4096
K 201,166,167
Mvav 64,128,204
X 173
Mvav 96,128,212
buffer_load_dwordx4 v20, s[40:43], s94 offen lds
s_add_u32 s94, s94, 81920
Mvav 128,128,220
X 174
Mvav 132,132,220
s_mul_i32 s95, s92, 112640
K 216,172,173
Mvav 100,132,212
s_add_u32 s95, s95, s71
s_add_u32 m0, s95, 20480
Mvav 68,132,204
X 175
Mvav 36,132,196
X 168
Mvav 40,136,196
K 217,174,175
D 16,40,94,0
Mvav 72,136,204
X 169
Mvav 104,136,212
Ra 156,242,64512
Ra 128,242,49152
Mvav 136,136,220
D 17,40,94,1024
Mvav 140,140,220
X 170
Mvav 108,140,212
K 208,168,169
Ra 132,242,50176
Mvav 76,140,204
)"
R"(D 18,40,94,2048
Mvav 44,140,196
W 2
Mvav 48,144,196
X 171
Mvav 80,144,204
D 19,40,94,3072
s_add_u32 m0, s95, 24576
Mvav 112,144,212
X 176
Mvav 144,144,220
K 209,170,171
Ra 136,242,51200
Mvav 148,148,220
X 177
Mvav 116,148,212
buffer_load_dwordx4 v20, s[40:43], s94 offen lds
s_waitcnt vmcnt(10)
v_cmp_eq_u64 vcc, s[12:13], v[28:29]
s_cmp_eq_u64 vcc, -1
s_cselect_b32 s46, 134217728, 0
Mvav 84,148,204
X 178
Mvav 52,148,196
K 194,176,177
s_add_u32 s81, s58, 0
Mvav 56,152,196
X 179
Mvav 88,152,204
s_and_b32 s81, s81, 3
s_lshl_b32 s81, s81, 12
Mvav 120,152,212
X 180
Mvav 152,152,220
K 195,178,179
s_add_u32 s81, s81, s49
Mvav 156,156,220
X 181
Mvav 124,156,212
X 182
Mvav 92,156,204
Ra 140,242,52224
s_add_u32 s82, s81, 131072
Mvav 60,156,196
K 202,180,181
X 183
X 184
K 203,182,183
Maav 160,180,192
X 185
X 186
K 210,184,185
X 187
X 188
K 211,186,187
Maav 164,180,200
X 189
X 190
K 218,188,189
X 191
s_nop 0
K 219,190,191
Maav 168,180,208
buffer_load_dwordx4 a[184:187], v1, s[44:47], s81 offen offset:0 sc1
Ra 144,242,53248
Maav 172,180,216
Ra 148,242,54272
W 2
Mvav 32,128,192
Ra 152,242,55296
Ra 156,242,56320
Mvav 64,128,200
buffer_load_dwordx4 a[200:203], v1, s[44:47], s82 offen offset:0 sc1
Mvav 96,128,208
Mvav 128,128,216
Mvav 132,132,216
buffer_load_dwordx4 a[188:191], v1, s[44:47], s81 offen offset:1024 sc1
Mvav 100,132,208
Mvav 68,132,200
Mvav 36,132,192
buffer_load_dwordx4 a[204:207], v1, s[44:47], s82 offen offset:1024 sc1
Mvav 40,136,192
Mvav 72,136,200
Mvav 104,136,208
buffer_load_dwordx4 a[192:195], v1, s[44:47], s81 offen offset:2048 sc1
Mvav 136,136,216
Mvav 140,140,216
Mvav 108,140,208
buffer_load_dwordx4 a[208:211], v1, s[44:47], s82 offen offset:2048 sc1
Mvav 76,140,200
Mvav 44,140,192
W 0
Mvav 48,144,192
buffer_load_dwordx4 a[196:199], v1, s[44:47], s81 offen offset:3072 sc1
Mvav 80,144,200
Mvav 112,144,208
Mvav 144,144,216
buffer_load_dwordx4 a[212:215], v1, s[44:47], s82 offen offset:3072 sc1
s_add_u32 s94, s58, 1
Mvav 148,148,216
s_and_b32 s94, s94, 3
s_mul_i32 s94, s94, 163840
Mvav 116,148,208
s_add_u32 s94, s94, s48
s_mul_i32 s95, s92, 10240
Mvav 84,148,200
s_add_u32 s95, s95, s71
s_add_u32 m0, s95, 143360
Mvav 52,148,192
D 16,40,94,0
Mvav 56,152,192
Mvav 88,152,200
Mvav 120,152,208
D 17,40,94,1024
Mvav 152,152,216
Mvav 156,156,216
Mvav 124,156,208
D 18,40,94,2048
Mvav 92,156,200
Mvav 60,156,192
D 19,40,94,3072
s_add_u32 m0, s95, 147456
s_nop 0
buffer_load_dwordx4 v20, s[40:43], s94 offen lds
s_add_u32 s81, s58, 1
s_and_b32 s81, s81, 3
s_lshl_b32 s81, s81, 12
s_add_u32 s81, s81, s49
s_add_u32 s82, s81, 131072
buffer_load_dwordx4 a[216:219], v1, s[44:47], s81 offen offset:0 sc1
buffer_load_dwordx4 a[232:235], v1, s[44:47], s82 offen offset:0 sc1
buffer_load_dwordx4 a[220:223], v1, s[44:47], s81 offen offset:1024 sc1
buffer_load_dwordx4 a[236:239], v1, s[44:47], s82 offen offset:1024 sc1
buffer_load_dwordx4 a[224:227], v1, s[44:47], s81 offen offset:2048 sc1
buffer_load_dwordx4 a[240:243], v1, s[44:47], s82 offen offset:2048 sc1
buffer_load_dwordx4 a[228:231], v1, s[44:47], s81 offen offset:3072 sc1
buffer_load_dwordx4 a[244:247], v1, s[44:47], s82 offen offset:3072 sc1
s_branch .Lend_41%=
.Lfs_40%=:
v_smfmac_f32_16x16x64_bf16 a[160:163], a[176:179], v[192:199], v240
Ra 144,242,45056
Ra 148,242,46080
Ra 152,242,47104
Ra 156,242,48128
s_lshr_b32 s92, s14, 1
v_smfmac_f32_16x16x64_bf16 a[164:167], a[176:179], v[200:207], v240
s_and_b32 s71, s14, 1
v_smfmac_f32_16x16x64_bf16 a[168:171], a[176:179], v[208:215], v240
s_mul_i32 s71, s71, 5120
v_smfmac_f32_16x16x64_bf16 a[172:175], a[176:179], v[216:223], v240
v_bfe_u32 v28, v1, 4, 2
W 4
Cvaa 168,128,96,232
v_lshlrev_b32 v28, 3, v28
v_mov_b32 v192, 0
G 12,160
Cvaa 172,128,112,236
G 4,161
A 12,12,4
v_cmp_nlt_f32_e64 s[86:87], v12, s90
buffer_load_dwordx2 v[28:29], v28, s[8:11], s54 offen sc1
v_rcp_f32 v12, v12
Mvaa 168,132,100
s_or_b64 s[88:89], s[88:89], s[86:87]
Ra 128,242,16384
v_accvgpr_write_b32 a160, 0
v_accvgpr_write_b32 a161, 0
v_mov_b32 v193, 0
v_accvgpr_write_b32 a162, 0
Mvaa 172,132,116
s_add_u32 s94, s58, 0
v_accvgpr_write_b32 a163, 0
G 13,164
G 5,165
A 13,13,5
v_cmp_nlt_f32_e64 s[86:87], v13, s90
Mvaa 168,136,104
v_rcp_f32 v13, v13
s_and_b32 s94, s94, 3
v_mov_b32 v200, 0
s_or_b64 s[88:89], s[88:89], s[86:87]
v_accvgpr_write_b32 a164, 0
Mvaa 172,136,120
v_accvgpr_write_b32 a165, 0
v_accvgpr_write_b32 a166, 0
Ra 132,242,17408
v_accvgpr_write_b32 a167, 0
s_mul_i32 s94, s94, 163840
U 32,32,12
Mvaa 168,140,108
Ra 136,242,18432
U 33,33,12
U 34,34,12
U 35,35,12
v_mov_b32 v201, 0
U 36,36,12
Mvaa 172,140,124
Ra 140,242,19456
U 37,37,12
s_add_u32 s94, s94, s48
U 38,38,12
U 39,39,12
W 4
Cvaa 188,144,112,236
K 32,32,33
K 33,34,35
K 34,36,37
K 35,38,39
s_mul_i32 s95, s92, 10240
s_add_u32 s93, s67, 0
Mvaa 188,148,116
buffer_store_dwordx4 v[32:35], v1, s[8:11], s93 offen offset:0 sc1
U 40,40,12
U 41,41,12
U 42,42,12
U 43,43,12
s_add_u32 s95, s95, s71
Mvaa 188,152,120
U 44,44,12
U 45,45,12
U 46,46,12
U 47,47,12
K 40,40,41
K 41,42,43
Mvaa 188,156,124
s_add_u32 m0, s95, 0
K 42,44,45
K 43,46,47
buffer_store_dwordx4 v[40:43], v1, s[8:11], s93 offen offset:1024 sc1
U 48,48,12
W 0
Mvav 96,128,212
U 49,49,12
U 50,50,12
U 51,51,12
U 52,52,12
Ra 144,242,20480
U 53,53,12
Mvav 128,128,220
D 16,40,94,0
U 54,54,12
U 55,55,12
K 48,48,49
K 49,50,51
K 50,52,53
Mvav 132,132,220
Ra 148,242,21504
Ra 152,242,22528
Ra 156,242,23552
K 51,54,55
v_cndmask_b32_e64 v168, v241, v168, s[72:73]
v_cndmask_b32_e64 v169, v241, v169, s[74:75]
Mvav 100,132,212
D 17,40,94,1024
v_cndmask_b32_e64 v170, v241, v170, s[76:77]
v_cndmask_b32_e64 v171, v241, v171, s[78:79]
X 168
Ra 128,242,49152
Mvav 104,136,212
X 169
Ra 132,242,50176
X 170
Mvav 136,136,220
buffer_store_dwordx4 v[48:51], v1, s[8:11], s93 offen offset:2048 sc1
U 56,56,12
U 57,57,12
U 58,58,12
U 59,59,12
U 60,60,12
Mvav 140,140,220
U 61,61,12
U 62,62,12
U 63,63,12
K 56,56,57
K 57,58,59
K 58,60,61
Mvav 108,140,212
D 18,40,94,2048
K 59,62,63
Ra 136,242,51200
K 208,168,169
Ra 140,242,52224
W 4
Mvav 112,144,212
X 171
Mvav 144,144,220
D 19,40,94,3072
s_add_u32 m0, s95, 4096
Ra 144,242,53248
Mvav 148,148,220
X 172
Mvav 116,148,212
buffer_store_dwordx4 v[56:59], v1, s[8:11], s93 offen offset:3072 sc1
G 14,168
G 6,169
A 14,14,6
v_cmp_nlt_f32_e64 s[86:87], v14, s90
Ra 148,242,54272
Mvav 120,152,212
v_rcp_f32 v14, v14
s_or_b64 s[88:89], s[88:89], s[86:87]
v_accvgpr_write_b32 a168, 0
v_accvgpr_write_b32 a169, 0
v_accvgpr_write_b32 a170, 0
Mvav 152,152,220
buffer_load_dwordx4 v20, s[40:43], s94 offen lds
v_accvgpr_write_b32 a171, 0
G 15,172
G 7,173
A 15,15,7
s_add_u32 s94, s94, 81920
Mvav 156,156,220
v_cmp_nlt_f32_e64 s[86:87], v15, s90
v_rcp_f32 v15, v15
s_or_b64 s[88:89], s[88:89], s[86:87]
v_accvgpr_write_b32 a172, 0
v_accvgpr_write_b32 a173, 0
v_accvgpr_write_b32 a174, 0
v_accvgpr_write_b32 a175, 0
K 209,170,171
X 173
X 174
K 216,172,173
X 175
v_mov_b32 v194, 0
K 217,174,175
v_mov_b32 v195, 0
v_mov_b32 v202, 0
v_mov_b32 v203, 0
v_mov_b32 v210, 0
v_mov_b32 v211, 0
Mvav 124,156,212
v_cndmask_b32_e64 v188, v241, v188, s[72:73]
v_cndmask_b32_e64 v189, v241, v189, s[74:75]
v_cndmask_b32_e64 v190, v241, v190, s[76:77]
v_cndmask_b32_e64 v191, v241, v191, s[78:79]
X 188
X 189
X 190
K 218,188,189
X 191
s_nop 0
K 219,190,191
Maav 168,180,208
s_mul_i32 s95, s92, 112640
U 64,64,13
U 65,65,13
U 66,66,13
U 67,67,13
s_add_u32 s95, s95, s71
Maav 172,180,216
U 68,68,13
U 69,69,13
U 70,70,13
U 71,71,13
K 64,64,65
W 2
Zvav 32,128,192
K 65,66,67
K 66,68,69
K 67,70,71
s_add_u32 s93, s67, 131072
buffer_store_dwordx4 v[64:67], v1, s[8:11], s93 offen offset:0 sc1
s_add_u32 m0, s95, 20480
s_nop 0
Zvav 64,128,200
U 72,72,13
U 73,73,13
U 74,74,13
U 75,75,13
U 76,76,13
U 77,77,13
U 78,78,13
U 79,79,13
K 72,72,73
K 73,74,75
K 74,76,77
K 75,78,79
buffer_store_dwordx4 v[72:75], v1, s[8:11], s93 offen offset:1024 sc1
U 80,80,13
U 81,81,13
U 82,82,13
U 83,83,13
U 84,84,13
U 85,85,13
U 86,86,13
U 87,87,13
K 80,80,81
K 81,82,83
K 82,84,85
K 83,86,87
buffer_store_dwordx4 v[80:83], v1, s[8:11], s93 offen offset:2048 sc1
U 88,88,13
U 89,89,13
U 90,90,13
U 91,91,13
U 92,92,13
U 93,93,13
U 94,94,13
U 95,95,13
)"
R"(K 88,88,89
K 89,90,91
K 90,92,93
K 91,94,95
buffer_store_dwordx4 v[88:91], v1, s[8:11], s93 offen offset:3072 sc1
U 96,96,14
U 97,97,14
U 98,98,14
U 99,99,14
U 100,100,14
U 101,101,14
U 102,102,14
U 103,103,14
K 96,96,97
K 97,98,99
K 98,100,101
K 99,102,103
s_add_u32 s93, s67, 262144
buffer_store_dwordx4 v[96:99], v1, s[8:11], s93 offen offset:0 sc1
s_nop 1
Zvav 96,128,208
U 104,104,14
U 105,105,14
U 106,106,14
U 107,107,14
U 108,108,14
U 109,109,14
U 110,110,14
U 111,111,14
K 104,104,105
K 105,106,107
K 106,108,109
K 107,110,111
buffer_store_dwordx4 v[104:107], v1, s[8:11], s93 offen offset:1024 sc1
U 112,112,14
U 113,113,14
U 114,114,14
U 115,115,14
U 116,116,14
U 117,117,14
U 118,118,14
U 119,119,14
K 112,112,113
K 113,114,115
K 114,116,117
K 115,118,119
buffer_store_dwordx4 v[112:115], v1, s[8:11], s93 offen offset:2048 sc1
U 120,120,14
U 121,121,14
U 122,122,14
U 123,123,14
U 124,124,14
U 125,125,14
U 126,126,14
U 127,127,14
K 120,120,121
K 121,122,123
K 122,124,125
K 123,126,127
buffer_store_dwordx4 v[120:123], v1, s[8:11], s93 offen offset:3072 sc1
U 128,128,15
U 129,129,15
U 130,130,15
U 131,131,15
U 132,132,15
U 133,133,15
U 134,134,15
U 135,135,15
K 128,128,129
K 129,130,131
K 130,132,133
K 131,134,135
s_add_u32 s93, s67, 393216
buffer_store_dwordx4 v[128:131], v1, s[8:11], s93 offen offset:0 sc1
s_nop 1
Zvav 128,128,216
Ra 152,242,55296
Ra 156,242,56320
U 136,136,15
U 137,137,15
U 138,138,15
U 139,139,15
Zvav 132,132,216
D 16,40,94,0
U 140,140,15
U 141,141,15
U 142,142,15
U 143,143,15
K 136,136,137
Zvav 100,132,208
K 137,138,139
K 138,140,141
K 139,142,143
Zvav 68,132,200
D 17,40,94,1024
Zvav 36,132,192
Zvav 40,136,192
D 18,40,94,2048
Zvav 72,136,200
Zvav 104,136,208
buffer_store_dwordx4 v[136:139], v1, s[8:11], s93 offen offset:1024 sc1
U 144,144,15
U 145,145,15
U 146,146,15
U 147,147,15
U 148,148,15
Zvav 136,136,216
U 149,149,15
U 150,150,15
U 151,151,15
K 144,144,145
K 145,146,147
K 146,148,149
Zvav 140,140,216
D 19,40,94,3072
s_add_u32 m0, s95, 24576
K 147,150,151
Zvav 108,140,208
Zvav 76,140,200
buffer_load_dwordx4 v20, s[40:43], s94 offen lds
s_waitcnt vmcnt(24)
v_cmp_eq_u64 vcc, s[12:13], v[28:29]
s_cmp_eq_u64 vcc, -1
s_cselect_b32 s46, 134217728, 0
s_add_u32 s81, s58, 0
Zvav 44,140,192
s_and_b32 s81, s81, 3
s_lshl_b32 s81, s81, 12
s_add_u32 s81, s81, s49
s_add_u32 s82, s81, 131072
W 0
Zvav 48,144,192
buffer_load_dwordx4 a[184:187], v1, s[44:47], s81 offen offset:0 sc1
Zvav 80,144,200
Zvav 112,144,208
buffer_store_dwordx4 v[144:147], v1, s[8:11], s93 offen offset:2048 sc1
U 152,152,15
U 153,153,15
U 154,154,15
U 155,155,15
U 156,156,15
Zvav 144,144,216
U 157,157,15
U 158,158,15
U 159,159,15
K 152,152,153
K 153,154,155
K 154,156,157
Zvav 148,148,216
buffer_load_dwordx4 a[200:203], v1, s[44:47], s82 offen offset:0 sc1
K 155,158,159
Zvav 116,148,208
Zvav 84,148,200
buffer_load_dwordx4 a[188:191], v1, s[44:47], s81 offen offset:1024 sc1
Zvav 52,148,192
Zvav 56,152,192
buffer_load_dwordx4 a[204:207], v1, s[44:47], s82 offen offset:1024 sc1
Zvav 88,152,200
Zvav 120,152,208
buffer_store_dwordx4 v[152:155], v1, s[8:11], s93 offen offset:3072 sc1
s_nop 1
Zvav 152,152,216
Zvav 156,156,216
buffer_load_dwordx4 a[192:195], v1, s[44:47], s81 offen offset:2048 sc1
Zvav 124,156,208
Zvav 92,156,200
buffer_load_dwordx4 a[208:211], v1, s[44:47], s82 offen offset:2048 sc1
Zvav 60,156,192
buffer_load_dwordx4 a[196:199], v1, s[44:47], s81 offen offset:3072 sc1
buffer_load_dwordx4 a[212:215], v1, s[44:47], s82 offen offset:3072 sc1
s_add_u32 s94, s58, 1
s_and_b32 s94, s94, 3
s_mul_i32 s94, s94, 163840
s_add_u32 s94, s94, s48
s_mul_i32 s95, s92, 10240
s_add_u32 s95, s95, s71
s_add_u32 m0, s95, 143360
s_nop 0
D 16,40,94,0
D 17,40,94,1024
D 18,40,94,2048
D 19,40,94,3072
s_add_u32 m0, s95, 147456
s_nop 0
buffer_load_dwordx4 v20, s[40:43], s94 offen lds
s_add_u32 s81, s58, 1
s_and_b32 s81, s81, 3
s_lshl_b32 s81, s81, 12
s_add_u32 s81, s81, s49
s_add_u32 s82, s81, 131072
buffer_load_dwordx4 a[216:219], v1, s[44:47], s81 offen offset:0 sc1
buffer_load_dwordx4 a[232:235], v1, s[44:47], s82 offen offset:0 sc1
buffer_load_dwordx4 a[220:223], v1, s[44:47], s81 offen offset:1024 sc1
buffer_load_dwordx4 a[236:239], v1, s[44:47], s82 offen offset:1024 sc1
buffer_load_dwordx4 a[224:227], v1, s[44:47], s81 offen offset:2048 sc1
buffer_load_dwordx4 a[240:243], v1, s[44:47], s82 offen offset:2048 sc1
buffer_load_dwordx4 a[228:231], v1, s[44:47], s81 offen offset:3072 sc1
buffer_load_dwordx4 a[244:247], v1, s[44:47], s82 offen offset:3072 sc1
s_mov_b32 s70, 1
s_branch .Lend_41%=
.Lpd_30%=:
Ra 128,242,0
Ra 132,242,1024
Ra 136,242,2048
Ra 140,242,3072
W 0
Zvaa 128,128,0
Ra 144,242,4096
Ra 148,242,5120
Ra 152,242,6144
Ra 156,242,7168
Zvaa 132,128,16
s_and_b32 s80, s14, 1
v_lshrrev_b32 v20, 4, v1
v_add_u32 v20, 0, v20
v_mul_u32_u24 v26, 205, v20
Zvaa 136,128,32
s_lshl_b32 s80, s80, 14
v_lshrrev_b32 v26, 12, v26
v_mul_u32_u24 v25, 20, v26
v_sub_u32 v20, v20, v25
Zvaa 140,128,48
s_add_u32 s80, s80, 131072
v_lshrrev_b32 v25, 1, v26
v_and_b32 v25, 2, v25
v_xor_b32 v20, v20, v25
Mvaa 128,132,4
v_add_u32 v12, s80, v1
v_mul_u32_u24 v26, 80, v26
v_add_lshl_u32 v16, v20, v26, 4
v_lshrrev_b32 v20, 4, v1
Mvaa 132,132,20
Ra 128,242,8192
Ra 64,12,8192
v_add_u32 v20, 64, v20
v_mul_u32_u24 v26, 205, v20
Mvaa 136,132,36
v_lshrrev_b32 v26, 12, v26
Ra 68,12,9216
v_mul_u32_u24 v25, 20, v26
v_sub_u32 v20, v20, v25
Mvaa 140,132,52
v_lshrrev_b32 v25, 1, v26
Ra 72,12,10240
v_and_b32 v25, 2, v25
v_xor_b32 v20, v20, v25
Mvaa 128,136,8
v_mul_u32_u24 v26, 80, v26
Ra 76,12,11264
v_add_lshl_u32 v17, v20, v26, 4
v_subrev_u32 v17, 1024, v17
Mvaa 132,136,24
v_lshrrev_b32 v20, 4, v1
Ra 80,12,12288
v_add_u32 v20, 128, v20
v_mul_u32_u24 v26, 205, v20
Mvaa 136,136,40
v_lshrrev_b32 v26, 12, v26
Ra 132,242,9216
Ra 84,12,13312
v_mul_u32_u24 v25, 20, v26
Mvaa 140,136,56
v_sub_u32 v20, v20, v25
v_lshrrev_b32 v25, 1, v26
Ra 88,12,14336
v_and_b32 v25, 2, v25
Mvaa 128,140,12
v_xor_b32 v20, v20, v25
v_mul_u32_u24 v26, 80, v26
Ra 92,12,15360
v_add_lshl_u32 v18, v20, v26, 4
Mvaa 132,140,28
v_subrev_u32 v18, 2048, v18
v_lshrrev_b32 v20, 4, v1
Ra 96,12,0
v_add_u32 v20, 192, v20
Mvaa 136,140,44
v_mul_u32_u24 v26, 205, v20
v_lshrrev_b32 v26, 12, v26
Ra 100,12,1024
v_mul_u32_u24 v25, 20, v26
Mvaa 140,140,60
v_sub_u32 v20, v20, v25
v_lshrrev_b32 v25, 1, v26
Ra 136,242,10240
W 13
Zvaa 148,144,16
Ra 104,12,2048
v_and_b32 v25, 2, v25
v_xor_b32 v20, v20, v25
v_mul_u32_u24 v26, 80, v26
Zvaa 152,144,32
Ra 108,12,3072
v_add_lshl_u32 v19, v20, v26, 4
v_subrev_u32 v19, 3072, v19
v_lshrrev_b32 v20, 4, v1
Zvaa 156,144,48
Ra 112,12,4096
v_add_u32 v20, 256, v20
v_mul_u32_u24 v26, 205, v20
v_lshrrev_b32 v26, 12, v26
Mvaa 148,148,20
Ra 116,12,5120
v_mul_u32_u24 v25, 20, v26
v_sub_u32 v20, v20, v25
v_lshrrev_b32 v25, 1, v26
Mvaa 152,148,36
Ra 120,12,6144
v_and_b32 v25, 2, v25
v_xor_b32 v20, v20, v25
Ra 140,242,11264
Mvaa 156,148,52
v_mul_u32_u24 v26, 80, v26
Ra 124,12,7168
v_add_lshl_u32 v20, v20, v26, 4
v_lshrrev_b32 v26, 6, v1
Mvaa 148,152,24
v_mul_u32_u24 v26, 320, v26
v_bfe_u32 v25, v1, 4, 2
v_lshl_add_u32 v26, v25, 3, v26
v_bfe_u32 v25, v1, 8, 1
Mvaa 152,152,40
v_lshlrev_b32 v25, 5, v25
v_cndmask_b32_e64 v128, v241, v128, s[72:73]
v_cndmask_b32_e64 v129, v241, v129, s[74:75]
v_cndmask_b32_e64 v130, v241, v130, s[76:77]
Mvaa 156,152,56
v_cndmask_b32_e64 v131, v241, v131, s[78:79]
v_max3_f32 v4, v128, v129, v130
v_max3_f32 v4, v4, v131, v252
v_max3_f32 v5, v132, v133, v134
Mvaa 148,156,28
v_max3_f32 v5, v5, v135, v253
v_max3_f32 v6, v136, v137, v138
v_max3_f32 v6, v6, v139, v254
v_max3_f32 v7, v140, v141, v142
Mvaa 152,156,44
v_max3_f32 v7, v7, v143, v255
v_mov_b32 v8, v4
v_mov_b32 v9, v5
v_mov_b32 v10, v6
v_mov_b32 v11, v7
v_permlane32_swap_b32 v4, v8
v_permlane32_swap_b32 v5, v9
v_permlane32_swap_b32 v6, v10
v_permlane32_swap_b32 v7, v11
v_max_f32 v4, v4, v8
v_max_f32 v5, v5, v9
v_max_f32 v6, v6, v10
v_max_f32 v7, v7, v11
v_mov_b32 v8, v4
v_mov_b32 v9, v5
v_mov_b32 v10, v6
v_mov_b32 v11, v7
v_permlane16_swap_b32 v4, v8
v_permlane16_swap_b32 v5, v9
v_permlane16_swap_b32 v6, v10
v_permlane16_swap_b32 v7, v11
v_max_f32 v4, v4, v8
v_max_f32 v5, v5, v9
v_max_f32 v6, v6, v10
v_max_f32 v7, v7, v11
v_sub_f32 v4, 0, v4
v_sub_f32 v5, 0, v5
v_sub_f32 v6, 0, v6
v_sub_f32 v7, 0, v7
v_mov_b32 v224, v4
v_mov_b32 v225, v4
v_mov_b32 v226, v4
v_mov_b32 v227, v4
v_mov_b32 v228, v5
v_mov_b32 v229, v5
v_mov_b32 v230, v5
v_mov_b32 v231, v5
v_mov_b32 v232, v6
v_mov_b32 v233, v6
v_mov_b32 v234, v6
v_mov_b32 v235, v6
v_mov_b32 v236, v7
v_mov_b32 v237, v7
v_mov_b32 v238, v7
v_mov_b32 v239, v7
Mvaa 156,156,60
v_add_u32 v21, v26, v25
v_sub_u32 v22, v26, v25
v_add_u32 v23, 61440, v21
W 1
Cvaa 168,128,32,232
v_add_u32 v24, 61440, v22
Ra 144,242,12288
v_add_u32 v25, 122880, v21
v_add_u32 v26, 122880, v22
Cvaa 172,128,48,236
s_mul_i32 s94, s59, 655360
s_mul_i32 s95, s58, 320
s_add_u32 s94, s94, s95
Ra 148,242,13312
Mvaa 168,132,36
Ra 152,242,14336
Ra 156,242,15360
)"
R"(s_add_u32 s40, s26, s94
s_addc_u32 s41, s27, 0
Mvaa 172,132,52
s_and_b32 s41, s41, 0xffff
s_mov_b32 s42, 5242880
s_mov_b32 s43, 0x20000
s_sub_u32 s42, s42, s94
Mvaa 168,136,40
s_mov_b64 s[44:45], s[8:9]
s_mov_b32 s47, 0x20000
s_mul_i32 s48, s14, 20480
s_and_b32 s94, s14, 1
Mvaa 172,136,56
s_lshl_b32 s95, s57, 1
s_add_u32 s94, s94, s95
s_lshl_b32 s54, s94, 3
s_add_u32 s54, s54, s59
Mvaa 168,140,44
Ra 128,242,24576
Ra 132,242,25600
Ra 136,242,26624
s_lshl_b32 s54, s54, 6
Mvaa 172,140,60
Ra 140,242,27648
s_xor_b32 s55, s54, 15872
s_add_u32 s54, s54, 20480
W 4
Cvaa 188,144,48,236
s_add_u32 s55, s55, 20480
s_and_b32 s95, s14, 2
s_lshl_b32 s94, s94, 2
s_add_u32 s94, s94, s95
Mvaa 188,148,52
s_lshl_b32 s95, s95, 4
s_add_u32 s55, s55, s95
s_lshl_b32 s49, s94, 17
s_lshl_b32 s95, s59, 14
Mvaa 188,152,56
A 128,128,224
A 129,129,224
A 130,130,224
A 131,131,224
X 128
X 129
X 130
K 192,128,129
X 131
A 132,132,228
K 193,130,131
A 133,133,228
A 134,134,228
A 135,135,228
X 132
X 133
X 134
K 200,132,133
X 135
A 136,136,232
K 201,134,135
A 137,137,232
A 138,138,232
A 139,139,232
X 136
X 137
X 138
K 208,136,137
X 139
A 140,140,236
K 209,138,139
A 141,141,236
A 142,142,236
A 143,143,236
X 140
X 141
X 142
K 216,140,141
X 143
v_mov_b32 v194, 0
K 217,142,143
v_mov_b32 v195, 0
v_cndmask_b32_e64 v148, v241, v148, s[72:73]
v_cndmask_b32_e64 v149, v241, v149, s[74:75]
v_cndmask_b32_e64 v150, v241, v150, s[76:77]
v_cndmask_b32_e64 v151, v241, v151, s[78:79]
A 148,148,228
A 149,149,228
A 150,150,228
A 151,151,228
X 148
X 149
X 150
K 202,148,149
X 151
A 152,152,232
K 203,150,151
A 153,153,232
A 154,154,232
A 155,155,232
X 152
X 153
X 154
K 210,152,153
X 155
A 156,156,236
K 211,154,155
A 157,157,236
A 158,158,236
A 159,159,236
X 156
X 157
X 158
K 218,156,157
X 159
s_nop 0
K 219,158,159
Mvaa 188,156,60
s_add_u32 s49, s49, s95
s_xor_b32 s50, s49, 16252928
s_add_u32 s49, s49, 41943040
W 0
Zvav 32,128,192
s_and_b32 s80, s14, 1
s_add_u32 s50, s50, 41943040
s_lshr_b32 s94, s15, 3
s_lshl_b32 s94, s94, 3
Zvav 64,128,200
s_add_u32 s94, s94, s14
s_lshl_b32 s94, s94, 4
s_add_u32 s94, s94, s59
s_mul_i32 s52, s94, 5120
Zvav 96,128,208
s_lshl_b32 s80, s80, 14
s_add_u32 s52, s52, 62914560
s_lshl_b32 s53, s94, 3
s_add_u32 s53, s53, 65536
Zvav 128,128,216
s_xor_b32 s62, s62, 0x10000
s_add_u32 s80, s80, 98304
v_add_u32 v8, s80, v1
Ra 144,242,28672
Zvav 132,132,216
Ra 148,242,29696
Ra 152,242,30720
Ra 156,242,31744
Zvav 100,132,208
Zvav 68,132,200
Zvav 36,132,192
Zvav 40,136,192
Zvav 72,136,200
Zvav 104,136,208
Zvav 136,136,216
Zvav 140,140,216
Zvav 108,140,208
Zvav 76,140,200
s_waitcnt vmcnt(0) lgkmcnt(0)
s_barrier
v_xor_b32 v242, 0x10000, v242
ds_read_b128 v[248:251], v8
Zvav 44,140,192
Ra 128,242,40960
Zvav 48,144,192
Zvav 80,144,200
X 172
X 173
Zvav 112,144,208
X 174
K 220,172,173
Ra 132,242,41984
Zvav 144,144,216
X 175
v_cndmask_b32_e64 v168, v241, v168, s[72:73]
K 221,174,175
Zvav 148,148,216
v_cndmask_b32_e64 v169, v241, v169, s[74:75]
v_cndmask_b32_e64 v170, v241, v170, s[76:77]
v_cndmask_b32_e64 v171, v241, v171, s[78:79]
Zvav 116,148,208
X 168
X 169
Zvav 84,148,200
Ra 136,242,43008
X 170
K 212,168,169
Zvav 52,148,192
W 3
v_mov_b32 v224, v250
v_mov_b32 v225, v250
v_mov_b32 v226, v250
Zvav 56,152,192
v_mov_b32 v227, v250
v_mov_b32 v228, v251
v_mov_b32 v229, v251
v_mov_b32 v230, v251
Zvav 88,152,200
v_mov_b32 v231, v251
v_mov_b32 v232, v248
v_mov_b32 v233, v248
v_mov_b32 v234, v248
Zvav 120,152,208
v_mov_b32 v235, v248
v_mov_b32 v236, v249
v_mov_b32 v237, v249
v_mov_b32 v238, v249
Zvav 152,152,216
v_mov_b32 v239, v249
X 171
v_mov_b32 v204, 0
Zvav 156,156,216
K 213,170,171
v_mov_b32 v205, 0
v_mov_b32 v196, 0
v_mov_b32 v197, 0
Zvav 124,156,208
Ra 140,242,44032
v_mov_b32 v198, 0
v_mov_b32 v199, 0
v_mov_b32 v206, 0
Zvav 92,156,200
v_mov_b32 v207, 0
v_mov_b32 v214, 0
v_mov_b32 v215, 0
v_cndmask_b32_e64 v188, v241, v188, s[72:73]
Zvav 60,156,192
v_cndmask_b32_e64 v189, v241, v189, s[74:75]
v_cndmask_b32_e64 v190, v241, v190, s[76:77]
v_cndmask_b32_e64 v191, v241, v191, s[78:79]
X 188
X 189
X 190
K 222,188,189
X 191
s_nop 0
K 223,190,191
s_nop 1
s_branch .Lsp2_35%=
.Lmoe_42%=:
W 0
Cvaa 160,128,0,224
Ra 144,242,4096
Ra 148,242,5120
Cvaa 164,128,16,228
Ra 152,242,6144
v_bfe_u32 v11, v1, 4, 3
Cvaa 168,128,32,232
X 184
Cvaa 172,128,48,236
X 185
Mvaa 160,132,4
s_xor_b32 s62, s62, 0x10000
v_lshlrev_b32 v11, 3, v11
Mvaa 164,132,20
X 186
Mvaa 168,132,36
buffer_load_dwordx2 v[8:9], v11, s[8:11], 0 offen offset:3584 sc1
s_cmp_eq_u32 s14, 0
s_cselect_b32 s20, s30, s32
s_cselect_b32 s21, s31, s33
Mvaa 172,132,52
K 214,184,185
Ra 156,242,7168
Mvaa 160,136,8
s_and_b32 s21, s21, 0xffff
s_mov_b32 s22, 2211840
Mvaa 164,136,24
X 187
Mvaa 168,136,40
X 188
Mvaa 172,136,56
s_mov_b32 s23, 0x20000
v_lshrrev_b32 v4, 6, v1
Mvaa 160,140,12
Ra 128,242,16384
K 215,186,187
Mvaa 164,140,28
X 189
Mvaa 168,140,44
v_mul_u32_u24 v4, 3456, v4
v_lshrrev_b32 v5, 4, v1
Mvaa 172,140,60
X 190
v_smfmac_f32_16x16x64_bf16 a[160:163], a[176:179], v[192:199], v240
v_xor_b32 v5, v5, v1
Ra 132,242,17408
v_smfmac_f32_16x16x64_bf16 a[164:167], a[176:179], v[200:207], v240
K 222,188,189
X 191
s_nop 0
K 223,190,191
v_smfmac_f32_16x16x64_bf16 a[168:171], a[176:179], v[208:215], v240
v_and_b32 v5, 32, v5
v_and_b32 v6, 16, v1
v_smfmac_f32_16x16x64_bf16 a[172:175], a[176:179], v[216:223], v240
v_add3_u32 v4, v4, v5, v6
W 2
Cvaa 176,144,0,224
v_add_u32 v5, 54272, v4
Ra 136,242,18432
Cvaa 180,144,16,228
X 160
Cvaa 184,144,32,232
X 161
Cvaa 188,144,48,236
v_add_u32 v6, 108544, v4
v_add_u32 v7, 162816, v4
Mvaa 176,148,4
X 162
Mvaa 180,148,20
s_lshl_b32 s94, s85, 6
Ra 140,242,19456
Mvaa 184,148,36
K 192,160,161
s_mul_i32 s95, s14, 20480
Mvaa 188,148,52
X 163
Mvaa 176,152,8
s_add_u32 s95, s95, 98304
s_add_u32 m0, s95, 0
Mvaa 180,152,24
X 164
Mvaa 184,152,40
D 4,20,94,0
Ra 144,242,20480
Mvaa 188,152,56
K 193,162,163
Ra 148,242,21504
Mvaa 176,156,12
X 165
Mvaa 180,156,28
X 166
Mvaa 184,156,44
D 5,20,94,1024
K 200,164,165
Mvaa 188,156,60
Ra 152,242,22528
W 3
Mvav 32,128,196
X 167
Mvav 64,128,204
X 168
Mvav 96,128,212
D 6,20,94,2048
K 201,166,167
Mvav 128,128,220
X 169
Mvav 132,132,220
X 170
Mvav 100,132,212
Ra 156,242,23552
K 208,168,169
Mvav 68,132,204
D 7,20,94,3072
s_add_u32 s94, s94, 221184
Mvav 36,132,196
s_add_u32 m0, s95, 4096
Ra 128,242,8192
Mvav 40,136,196
X 171
Mvav 72,136,204
X 172
Mvav 104,136,212
D 4,20,94,0
s_add_u32 s64, s64, 1
Mvav 136,136,220
K 209,170,171
Ra 132,242,9216
Mvav 140,140,220
X 173
Mvav 108,140,212
X 174
Mvav 76,140,204
D 5,20,94,1024
K 216,172,173
Mvav 44,140,196
Ra 136,242,10240
W 3
Mvav 48,144,196
X 175
Mvav 80,144,204
X 176
Mvav 112,144,212
D 6,20,94,2048
K 217,174,175
Mvav 144,144,220
X 177
Mvav 148,148,220
X 178
Mvav 116,148,212
Ra 140,242,11264
K 194,176,177
Mvav 84,148,204
D 7,20,94,3072
s_add_u32 s94, s94, 221184
Mvav 52,148,196
s_waitcnt vmcnt(8)
v_xor_b32 v8, s12, v8
v_xor_b32 v9, s13, v9
v_lshrrev_b32 v10, 11, v8
v_or_b32 v10, v10, v9
v_cmp_eq_u32_e64 s[86:87], 0, v10
s_cmp_eq_u64 s[86:87], -1
)"
R"(s_cbranch_scc0 .Lspin_54%=
.Lok_55%=:
Mvav 56,152,196
v_and_b32 v8, 2047, v8
v_lshrrev_b32 v10, 6, v8
Mvav 88,152,204
v_mul_u32_u24 v10, 81920, v10
v_bfe_u32 v9, v8, 4, 2
Mvav 120,152,212
v_lshl_add_u32 v10, v9, 10, v10
v_and_b32 v8, 15, v8
Mvav 152,152,220
v_lshl_add_u32 v10, v8, 4, v10
v_bfe_u32 v9, v1, 7, 2
Mvav 156,156,220
v_lshl_add_u32 v10, v9, 8, v10
v_lshrrev_b32 v9, 9, v1
Mvav 124,156,212
v_lshl_add_u32 v10, v9, 12, v10
s_mul_i32 s81, s14, 40960
Mvav 92,156,204
s_add_u32 s81, s81, 131072
s_mul_i32 s82, s14, 5120
Mvav 60,156,196
s_add_u32 m0, s82, 53248
W 0
Cvaa 160,128,0,224
X 179
Cvaa 164,128,16,228
buffer_load_dwordx4 v10, s[8:11], s81 offen sc1 lds
s_add_u32 s81, s81, 8192
Cvaa 168,128,32,232
X 180
Cvaa 172,128,48,236
Ra 144,242,12288
s_add_u32 m0, s82, 54272
Mvaa 160,132,4
K 195,178,179
Ra 148,242,13312
Mvaa 164,132,20
X 181
Mvaa 168,132,36
buffer_load_dwordx4 v10, s[8:11], s81 offen sc1 lds
s_add_u32 s81, s81, 8192
Mvaa 172,132,52
X 182
Mvaa 160,136,8
s_add_u32 m0, s82, 55296
K 202,180,181
Mvaa 164,136,24
X 183
Mvaa 168,136,40
Ra 152,242,14336
Ra 156,242,15360
Mvaa 172,136,56
buffer_load_dwordx4 v10, s[8:11], s81 offen sc1 lds
s_add_u32 s81, s81, 8192
Mvaa 160,140,12
X 184
Mvaa 164,140,28
s_add_u32 m0, s82, 56320
K 203,182,183
Mvaa 168,140,44
X 185
Mvaa 172,140,60
buffer_load_dwordx4 v10, s[8:11], s81 offen sc1 lds
W 0
Cvaa 176,144,0,224
s_add_u32 s81, s81, 8192
s_add_u32 m0, s82, 57344
Cvaa 180,144,16,228
X 186
K 210,184,185
X 187
X 188
K 211,186,187
Cvaa 184,144,32,232
X 189
X 190
K 218,188,189
X 191
X 160
K 219,190,191
Cvaa 188,144,48,236
buffer_load_dwordx4 v10, s[8:11], s81 offen sc1 lds
s_add_u32 m0, s95, 8192
Mvaa 176,148,4
Ra 128,242,24576
Ra 132,242,25600
Mvaa 180,148,20
Ra 136,242,26624
Ra 140,242,27648
Mvaa 184,148,36
X 161
Mvaa 188,148,52
D 4,20,94,0
Ra 144,242,28672
Mvaa 176,152,8
X 162
Mvaa 180,152,24
K 196,160,161
Ra 148,242,29696
Mvaa 184,152,40
X 163
Mvaa 188,152,56
D 5,20,94,1024
Mvaa 176,156,12
X 164
Mvaa 180,156,28
K 197,162,163
Ra 152,242,30720
Mvaa 184,156,44
X 165
Mvaa 188,156,60
D 6,20,94,2048
W 3
Mvav 32,128,192
X 166
Mvav 64,128,200
K 204,164,165
Ra 156,242,31744
Mvav 96,128,208
X 167
Mvav 128,128,216
D 7,20,94,3072
s_add_u32 s94, s94, 221184
Mvav 132,132,216
s_add_u32 m0, s95, 12288
Mvav 100,132,208
X 168
Mvav 68,132,200
K 205,166,167
Mvav 36,132,192
D 4,20,94,0
Mvav 40,136,192
X 169
Mvav 72,136,200
X 170
Mvav 104,136,208
K 212,168,169
Mvav 136,136,216
D 5,20,94,1024
Mvav 140,140,216
D 6,20,94,2048
D 7,20,94,3072
s_add_u32 s94, s94, 221184
s_add_u32 m0, s95, 16384
s_nop 0
D 4,20,94,0
D 5,20,94,1024
D 6,20,94,2048
D 7,20,94,3072
s_add_u32 s94, s94, 221184
Mvav 108,140,208
X 171
Mvav 76,140,200
s_waitcnt vmcnt(0) lgkmcnt(0)
s_barrier
v_xor_b32 v242, 0x10000, v242
s_bitcmp1_b32 s68, s64
s_cbranch_scc1 .Lact_57%=
.Lactr_58%=:
Mvav 44,140,192
v_lshrrev_b32 v8, 8, v1
v_bfe_u32 v9, v1, 6, 2
Mvav 48,144,192
X 172
Mvav 80,144,200
K 213,170,171
v_lshl_add_u32 v9, v8, 3, v9
Mvav 112,144,208
X 173
Mvav 144,144,216
X 174
Mvav 148,148,216
v_lshlrev_b32 v11, 6, v9
K 220,172,173
Mvav 116,148,208
v_xor_b32 v9, s14, v8
v_and_b32 v9, 1, v9
Mvav 84,148,200
X 175
Mvav 52,148,192
X 176
Mvav 56,152,192
v_lshl_add_u32 v11, v9, 5, v11
K 221,174,175
Mvav 88,152,200
Ra 128,242,0
v_bfe_u32 v9, v1, 4, 2
Mvav 120,152,208
X 177
Mvav 152,152,216
X 178
Mvav 156,156,216
v_lshl_add_u32 v12, v9, 3, v11
Ra 132,242,1024
Mvav 124,156,208
K 198,176,177
v_add_u32 v11, 98304, v12
Mvav 92,156,200
X 179
Mvav 60,156,192
X 180
Ra 136,242,2048
K 199,178,179
X 181
Ra 140,242,3072
X 182
K 206,180,181
X 183
s_nop 0
K 207,182,183
v_and_b32 v13, 0x70, v1
v_lshl_add_u32 v13, v8, 7, v13
W 0
Cvaa 160,128,0,224
Ra 144,242,4096
Ra 148,242,5120
Cvaa 164,128,16,228
X 184
Cvaa 168,128,32,232
Ta 184,11,0
Ta 186,11,256
Cvaa 172,128,48,236
Ta 188,11,20480
Ta 190,11,20736
Mvaa 160,132,4
Ra 192,13,53248
Ta 196,11,2048
Mvaa 164,132,20
s_cmp_eq_u32 s14, 0
s_cselect_b32 s95, 106496, 0
Mvaa 168,132,36
X 185
Mvaa 172,132,52
s_xor_b32 s62, s62, 0x10000
W 1
Zaaa 232,184,192
Ta 198,11,2304
Ta 200,11,22528
Zaaa 236,188,192
X 186
Mvaa 160,136,8
s_add_u32 s95, s95, 32768
Ta 202,11,22784
Mvaa 164,136,24
K 214,184,185
Ra 204,13,53760
Mvaa 168,136,40
X 187
Mvaa 172,136,56
s_add_u32 m0, s95, 0
Ra 152,242,6144
Mvaa 160,140,12
Ta 208,11,4096
Ta 210,11,4352
Mvaa 164,140,28
X 188
Mvaa 168,140,44
D 4,20,94,0
K 215,186,187
Mvaa 172,140,60
Ra 156,242,7168
Ta 212,11,24576
v_smfmac_f32_16x16x64_bf16 a[160:163], a[176:179], v[192:199], v240
Ta 214,11,24832
W 6
Maaa 232,196,204
X 189
Maaa 236,200,204
D 5,20,94,1024
Ra 216,13,54272
v_smfmac_f32_16x16x64_bf16 a[164:167], a[176:179], v[200:207], v240
X 190
K 222,188,189
X 191
X 160
K 223,190,191
v_smfmac_f32_16x16x64_bf16 a[168:171], a[176:179], v[208:215], v240
Ra 128,242,16384
Ta 220,11,6144
v_smfmac_f32_16x16x64_bf16 a[172:175], a[176:179], v[216:223], v240
Ta 222,11,6400
W 6
Cvaa 176,144,0,224
D 6,20,94,2048
Ra 132,242,17408
Cvaa 180,144,16,228
X 161
Cvaa 184,144,32,232
Ta 224,11,26624
Ta 226,11,26880
Cvaa 188,144,48,236
X 162
Mvaa 176,148,4
D 7,20,94,3072
s_add_u32 s94, s94, 221184
Mvaa 180,148,20
s_add_u32 m0, s95, 4096
W 6
Maaa 232,208,216
K 192,160,161
Ra 228,13,54784
Maaa 236,212,216
X 163
Mvaa 184,148,36
Ra 136,242,18432
D 4,20,94,0
Mvaa 188,148,52
X 164
Mvaa 176,152,8
Ta 184,11,8192
K 193,162,163
Mvaa 180,152,24
X 165
Mvaa 184,152,40
D 5,20,94,1024
Ra 140,242,19456
Mvaa 188,152,56
Ta 186,11,8448
Ta 188,11,28672
Mvaa 176,156,12
X 166
Mvaa 180,156,28
K 200,164,165
Ra 144,242,20480
Mvaa 184,156,44
D 6,20,94,2048
W 6
Maaa 232,220,228
X 167
Maaa 236,224,228
Ta 190,11,28928
Ra 192,13,55296
Mvaa 188,156,60
Ta 196,11,10240
W 6
Mvav 32,128,196
D 7,20,94,3072
s_add_u32 s94, s94, 221184
Mvav 64,128,204
X 168
Mvav 96,128,212
K 201,166,167
Ra 148,242,21504
Mvav 128,128,220
X 169
Mvav 132,132,220
s_add_u32 s64, s64, 1
s_add_u32 m0, s95, 8192
Mvav 100,132,212
X 170
Mvav 68,132,204
Ta 198,11,10496
K 208,168,169
Mvav 36,132,196
Ta 200,11,30720
W 4
Maaa 232,184,192
X 171
Maaa 236,188,192
Ra 152,242,22528
D 4,20,94,0
Mvav 40,136,196
X 172
Mvav 72,136,204
Ta 202,11,30976
K 209,170,171
Mvav 104,136,212
Ra 156,242,23552
Ra 204,13,55808
Mvav 136,136,220
D 5,20,94,1024
Ta 208,11,12288
Mvav 140,140,220
X 173
Mvav 108,140,212
X 174
Mvav 76,140,204
K 216,172,173
Ta 210,11,12544
Mvav 44,140,196
D 6,20,94,2048
W 3
Mvav 48,144,196
Ra 128,242,8192
W 3
Maaa 232,196,204
X 175
Maaa 236,200,204
Ta 212,11,32768
Ta 214,11,33024
Mvav 80,144,204
D 7,20,94,3072
s_add_u32 s94, s94, 221184
Mvav 112,144,212
X 176
Mvav 144,144,220
K 217,174,175
)"
R"(s_add_u32 m0, s95, 12288
Mvav 148,148,220
X 177
Mvav 116,148,212
Ra 216,13,56320
Ra 132,242,9216
Mvav 84,148,204
X 178
Mvav 52,148,196
Ta 220,11,14336
K 194,176,177
Mvav 56,152,196
D 4,20,94,0
Ta 222,11,14592
Mvav 88,152,204
Ra 136,242,10240
W 4
Maaa 232,208,216
X 179
Maaa 236,212,216
X 180
Mvav 120,152,212
Ra 140,242,11264
D 5,20,94,1024
Mvav 152,152,220
Ta 224,11,34816
K 195,178,179
Mvav 156,156,220
X 181
Mvav 124,156,212
Ta 226,11,35072
Ra 228,13,56832
Mvav 92,156,204
D 6,20,94,2048
Ta 184,11,16384
Mvav 60,156,196
Ra 144,242,12288
W 5
Cvaa 160,128,0,224
X 182
Cvaa 164,128,16,228
K 202,180,181
Ta 186,11,16640
Cvaa 168,128,32,232
D 7,20,94,3072
W 3
Maaa 232,220,228
X 183
Maaa 236,224,228
s_add_u32 s94, s94, 221184
s_add_u32 m0, s95, 16384
Cvaa 172,128,48,236
X 184
Mvaa 160,132,4
K 203,182,183
Ta 188,11,36864
Mvaa 164,132,20
X 185
Mvaa 168,132,36
Ra 148,242,13312
Ta 190,11,37120
Mvaa 172,132,52
X 186
Mvaa 160,136,8
Ra 192,13,57344
D 4,20,94,0
Mvaa 164,136,24
K 210,184,185
Ta 196,11,18432
Mvaa 168,136,40
X 187
Mvaa 172,136,56
Ra 152,242,14336
W 2
Maaa 232,184,192
Ra 156,242,15360
D 5,20,94,1024
Maaa 236,188,192
X 188
Mvaa 160,140,12
K 211,186,187
Ta 198,11,18688
Mvaa 164,140,28
X 189
Mvaa 168,140,44
D 6,20,94,2048
Ta 200,11,38912
Mvaa 172,140,60
Ta 202,11,39168
W 3
Cvaa 176,144,0,224
X 190
Cvaa 180,144,16,228
Ra 204,13,57856
K 218,188,189
Cvaa 184,144,32,232
X 191
X 160
K 219,190,191
Cvaa 188,144,48,236
D 7,20,94,3072
s_add_u32 s94, s94, 221184
Mvaa 176,148,4
Ra 128,242,24576
W 1
Maaa 232,196,204
Ra 132,242,25600
Ra 136,242,26624
Maaa 236,200,204
X 161
Mvaa 180,148,20
X 162
Mvaa 184,148,36
K 196,160,161
Ra 140,242,27648
Mvaa 188,148,52
X 163
Mvaa 176,152,8
X 164
Mvaa 180,152,24
K 197,162,163
Ra 144,242,28672
Mvaa 184,152,40
X 165
Mvaa 188,152,56
X 166
Mvaa 176,156,12
K 204,164,165
Ra 148,242,29696
Mvaa 180,156,28
X 167
Mvaa 184,156,44
X 168
Mvaa 188,156,60
K 205,166,167
W 2
Mvav 32,128,192
X 169
Mvav 64,128,200
Ra 152,242,30720
Ra 156,242,31744
Mvav 96,128,208
X 170
Mvav 128,128,216
K 212,168,169
Mvav 132,132,216
X 171
Mvav 100,132,208
X 172
Mvav 68,132,200
K 213,170,171
Mvav 36,132,192
X 173
Mvav 40,136,192
X 174
Mvav 72,136,200
K 220,172,173
Mvav 104,136,208
X 175
Mvav 136,136,216
X 176
Mvav 140,140,216
Mvav 108,140,208
K 221,174,175
Mvav 76,140,200
s_waitcnt vmcnt(0) lgkmcnt(0)
s_barrier
v_xor_b32 v242, 0x10000, v242
s_bitcmp1_b32 s68, s64
s_cbranch_scc1 .Lact_59%=
.Lactr_60%=:
Mvav 44,140,192
X 177
Mvav 48,144,192
X 178
Mvav 80,144,200
Ra 128,242,0
K 198,176,177
Mvav 112,144,208
X 179
Mvav 144,144,216
Ra 132,242,1024
Mvav 148,148,216
X 180
Mvav 116,148,208
Ra 136,242,2048
Mvav 84,148,200
K 199,178,179
Mvav 52,148,192
X 181
Mvav 56,152,192
Ra 140,242,3072
Mvav 88,152,200
X 182
Mvav 120,152,208
Mvav 152,152,216
K 206,180,181
Mvav 156,156,216
X 183
Mvav 124,156,208
Mvav 92,156,200
K 207,182,183
Mvav 60,156,192
W 0
Cvaa 160,128,0,224
Ra 144,242,4096
Ra 148,242,5120
Cvaa 164,128,16,228
X 184
Cvaa 168,128,32,232
Ta 184,11,40960
Ta 186,11,41216
Cvaa 172,128,48,236
Ta 188,12,32768
Ta 190,12,33024
Mvaa 160,132,4
Ra 192,13,58368
Ta 196,11,43008
Mvaa 164,132,20
s_mov_b32 s20, s34
s_xor_b32 s62, s62, 0x10000
Mvaa 168,132,36
X 185
Mvaa 172,132,52
Ta 198,11,43264
W 2
Maaa 232,184,192
s_and_b32 s21, s35, 0xffff
Ta 200,12,34816
Maaa 236,188,192
X 186
Mvaa 160,136,8
s_mov_b32 s22, 2211840
Ta 202,12,35072
Mvaa 164,136,24
K 214,184,185
Ra 204,13,58880
Mvaa 168,136,40
s_mov_b32 s23, 0x20000
Ra 152,242,6144
Mvaa 172,136,56
X 187
Mvaa 160,140,12
Ta 208,11,45056
v_lshrrev_b32 v4, 8, v1
Mvaa 164,140,28
X 188
Mvaa 168,140,44
Ta 210,11,45312
v_lshlrev_b32 v5, 5, v4
Mvaa 172,140,60
K 215,186,187
Ra 156,242,7168
v_smfmac_f32_16x16x64_bf16 a[160:163], a[176:179], v[192:199], v240
v_xor_b32 v5, v5, v1
W 4
Maaa 232,196,204
X 189
Maaa 236,200,204
Ta 212,12,36864
v_and_b32 v5, 0xf0, v5
v_smfmac_f32_16x16x64_bf16 a[164:167], a[176:179], v[200:207], v240
X 190
K 222,188,189
X 191
X 160
K 223,190,191
v_smfmac_f32_16x16x64_bf16 a[168:171], a[176:179], v[208:215], v240
Ta 214,12,37120
v_mul_u32_u24 v4, 1280, v4
v_smfmac_f32_16x16x64_bf16 a[172:175], a[176:179], v[216:223], v240
Ra 216,13,59392
W 3
Cvaa 176,144,0,224
v_add_u32 v4, v4, v5
Ra 128,242,16384
Cvaa 180,144,16,228
Ta 220,11,47104
v_xor_b32 v5, 128, v4
Cvaa 184,144,32,232
v_add_u32 v6, 8192, v4
Ta 222,11,47360
Cvaa 188,144,48,236
X 161
Mvaa 176,148,4
Ra 132,242,17408
v_add_u32 v7, 12288, v5
Mvaa 180,148,20
Ta 224,12,38912
W 5
Maaa 232,208,216
X 162
Maaa 236,212,216
v_add_u32 v5, 4096, v5
K 192,160,161
Mvaa 184,148,36
Ta 226,12,39168
s_mul_i32 s94, s85, 40960
Mvaa 188,148,52
X 163
Mvaa 176,152,8
s_mul_i32 s81, s14, 20480
Ra 228,13,59904
Mvaa 180,152,24
Ra 136,242,18432
s_add_u32 s94, s94, s81
Mvaa 184,152,40
X 164
Mvaa 188,152,56
Ta 184,11,49152
K 193,162,163
Mvaa 176,156,12
Ra 140,242,19456
s_lshl_b32 s95, s14, 12
Mvaa 180,156,28
X 165
Mvaa 184,156,44
Ta 186,11,49408
W 4
Maaa 232,220,228
s_add_u32 s95, s95, 98304
Ta 188,12,40960
Maaa 236,224,228
X 166
Mvaa 188,156,60
s_add_u32 m0, s95, 0
W 2
Mvav 32,128,196
K 200,164,165
Ra 144,242,20480
Mvav 64,128,204
X 167
Mvav 96,128,212
D 4,20,94,0
Ta 190,12,41216
Mvav 128,128,220
X 168
Mvav 132,132,220
Ra 192,13,60416
K 201,166,167
Mvav 100,132,212
X 169
Mvav 68,132,204
D 5,20,94,1024
Ta 196,11,51200
Mvav 36,132,196
Ra 148,242,21504
W 2
Maaa 232,184,192
s_add_u32 s64, s64, 1
Ta 198,11,51456
Maaa 236,188,192
X 170
Mvav 40,136,196
D 6,20,94,2048
K 208,168,169
Mvav 72,136,204
X 171
Mvav 104,136,212
Ra 152,242,22528
Ra 156,242,23552
Mvav 136,136,220
Ta 200,12,43008
Ta 202,12,43264
Mvav 140,140,220
D 7,20,94,3072
s_add_u32 s94, s94, 256
Mvav 108,140,212
Ra 204,13,60928
s_add_u32 m0, s95, 8192
Mvav 76,140,204
X 172
Mvav 44,140,196
K 209,170,171
W 3
Mvav 48,144,196
D 4,20,94,0
W 0
Maaa 232,196,204
X 173
Maaa 236,200,204
X 174
Mvav 80,144,204
Ta 208,11,53248
K 216,172,173
Mvav 112,144,212
D 5,20,94,1024
Ta 210,11,53504
Mvav 144,144,220
X 175
Mvav 148,148,220
Ra 128,242,8192
Ta 212,12,45056
Mvav 116,148,212
X 176
Mvav 84,148,204
Ta 214,12,45312
Ra 216,13,61440
Mvav 52,148,196
D 6,20,94,2048
)"
R"(K 217,174,175
Mvav 56,152,196
X 177
Mvav 88,152,204
Ra 132,242,9216
W 1
Maaa 232,208,216
X 178
Maaa 236,212,216
D 7,20,94,3072
s_add_u32 s94, s94, 256
Mvav 120,152,212
Ra 136,242,10240
Ra 140,242,11264
Mvav 152,152,220
s_add_u32 m0, s95, 16384
Ta 220,11,55296
Mvav 156,156,220
K 194,176,177
Ta 222,11,55552
Mvav 124,156,212
D 4,20,94,0
Ta 224,12,47104
Mvav 92,156,204
X 179
Mvav 60,156,196
Ta 226,12,47360
Ra 228,13,61952
W 5
Cvaa 160,128,0,224
X 180
Cvaa 164,128,16,228
D 5,20,94,1024
K 195,178,179
Cvaa 168,128,32,232
Ta 184,11,57344
W 1
Maaa 232,220,228
X 181
Maaa 236,224,228
X 182
Cvaa 172,128,48,236
D 6,20,94,2048
K 202,180,181
Mvaa 160,132,4
X 183
Mvaa 164,132,20
X 184
Mvaa 168,132,36
Ra 144,242,12288
Ta 186,11,57600
Mvaa 172,132,52
D 7,20,94,3072
s_add_u32 s94, s94, 256
Mvaa 160,136,8
Ta 188,12,49152
Ta 190,12,49408
Ra 192,13,62464
Mvaa 164,136,24
s_add_u32 m0, s95, 24576
K 203,182,183
Mvaa 168,136,40
X 185
Mvaa 172,136,56
D 4,20,94,0
W 0
Maaa 232,184,192
Ra 148,242,13312
Ra 152,242,14336
Ra 156,242,15360
Maaa 236,188,192
X 186
Mvaa 160,140,12
K 210,184,185
Ta 196,11,59392
Mvaa 164,140,28
D 5,20,94,1024
Ta 198,11,59648
Mvaa 168,140,44
X 187
Mvaa 172,140,60
Ta 200,12,51200
W 3
Cvaa 176,144,0,224
X 188
Cvaa 180,144,16,228
K 211,186,187
Ta 202,12,51456
Ra 204,13,62976
Cvaa 184,144,32,232
X 189
X 190
K 218,188,189
X 191
X 160
K 219,190,191
Cvaa 188,144,48,236
D 6,20,94,2048
Ra 128,242,24576
Mvaa 176,148,4
Ra 132,242,25600
W 2
Maaa 232,196,204
Ra 136,242,26624
Ra 140,242,27648
Maaa 236,200,204
D 7,20,94,3072
s_add_u32 s94, s94, 256
s_add_u32 m0, s95, 32768
s_nop 0
D 4,20,94,0
D 5,20,94,1024
D 6,20,94,2048
D 7,20,94,3072
s_add_u32 s94, s94, 256
Mvaa 180,148,20
X 161
Mvaa 184,148,36
X 162
Mvaa 188,148,52
K 196,160,161
Ra 144,242,28672
Mvaa 176,152,8
X 163
Mvaa 180,152,24
X 164
Mvaa 184,152,40
K 197,162,163
G 252,232
Mvaa 188,152,56
X 165
Mvaa 176,156,12
X 166
Mvaa 180,156,28
K 204,164,165
G 253,233
Mvaa 184,156,44
X 167
Mvaa 188,156,60
G 254,234
W 1
Mvav 32,128,192
X 168
Mvav 64,128,200
G 255,235
Ra 148,242,29696
Mvav 96,128,208
G 8,236
G 9,237
Mvav 128,128,216
G 10,238
G 14,239
Mvav 132,132,216
K 252,252,8
K 205,166,167
Mvav 100,132,208
K 253,253,9
K 254,254,10
Mvav 68,132,200
K 255,255,14
v_and_b32 v8, 0xffff0000, v252
Mvav 36,132,192
X 169
Mvav 40,136,192
v_and_b32 v9, 0xffff0000, v253
v_and_b32 v10, 0xffff0000, v254
Mvav 72,136,200
v_and_b32 v14, 0xffff0000, v255
v_lshlrev_b32 v252, 16, v252
Mvav 104,136,208
Ra 152,242,30720
Ra 156,242,31744
Mvav 136,136,216
v_lshlrev_b32 v253, 16, v253
v_lshlrev_b32 v254, 16, v254
v_lshlrev_b32 v255, 16, v255
v_mul_f32 v15, 0xbfb8aa3b, v252
v_mul_f32 v2, 0xbfb8aa3b, v253
v_mul_f32 v3, 0xbfb8aa3b, v254
v_mul_f32 v27, 0xbfb8aa3b, v255
X 15
X 2
X 3
X 27
v_add_f32 v15, 1.0, v15
v_add_f32 v2, 1.0, v2
v_add_f32 v3, 1.0, v3
v_add_f32 v27, 1.0, v27
v_rcp_f32 v15, v15
v_rcp_f32 v2, v2
v_rcp_f32 v3, v3
v_rcp_f32 v27, v27
U 252,252,15
U 253,253,2
U 254,254,3
U 255,255,27
K 252,252,252
K 253,253,253
K 254,254,254
K 255,255,255
v_lshlrev_b32 v252, 16, v252
v_lshlrev_b32 v253, 16, v253
v_lshlrev_b32 v254, 16, v254
v_lshlrev_b32 v255, 16, v255
U 252,252,8
U 253,253,9
U 254,254,10
U 255,255,14
K 246,252,253
K 247,254,255
s_lshl_b32 s81, s14, 3
v_add_u32 v2, s81, v1
ds_write_b64 v2, v[246:247] offset:63488
Mvav 140,140,216
X 170
Mvav 108,140,208
K 212,168,169
Mvav 76,140,200
s_waitcnt vmcnt(0) lgkmcnt(0)
s_barrier
v_xor_b32 v242, 0x10000, v242
s_bitcmp1_b32 s68, s64
s_cbranch_scc1 .Lact_61%=
.Lactr_62%=:
Mvav 44,140,192
X 171
Mvav 48,144,192
X 172
Mvav 80,144,200
K 213,170,171
Ra 128,242,0
Mvav 112,144,208
X 173
Mvav 144,144,216
X 174
Mvav 148,148,216
K 220,172,173
Ra 132,242,1024
Mvav 116,148,208
X 175
Mvav 84,148,200
X 176
Mvav 52,148,192
K 221,174,175
Ra 136,242,2048
Mvav 56,152,192
X 177
Mvav 88,152,200
X 178
Mvav 120,152,208
K 198,176,177
Ra 140,242,3072
Mvav 152,152,216
X 179
Mvav 156,156,216
X 180
Mvav 124,156,208
K 199,178,179
Mvav 92,156,200
X 181
Mvav 60,156,192
X 182
K 206,180,181
X 183
s_nop 0
K 207,182,183
W 0
Cvaa 160,128,0,224
Ra 144,242,4096
Ra 148,242,5120
Cvaa 164,128,16,228
X 184
Cvaa 168,128,32,232
Ra 216,1,63488
v_lshrrev_b32 v8, 6, v1
Cvaa 172,128,48,236
X 185
Mvaa 160,132,4
v_and_b32 v9, 7, v8
s_xor_b32 s62, s62, 0x10000
Mvaa 164,132,20
s_lshl_b32 s81, s14, 2
v_xor_b32 v9, s81, v9
Mvaa 168,132,36
v_lshlrev_b32 v9, 5, v9
v_lshl_add_u32 v4, v8, 8, v9
Mvaa 172,132,52
X 186
Mvaa 160,136,8
Ra 152,242,6144
v_bfe_u32 v9, v1, 4, 2
Mvaa 164,136,24
v_lshl_add_u32 v4, v9, 3, v4
v_add_u32 v4, 98304, v4
Mvaa 168,136,40
v_xor_b32 v5, 32, v4
K 214,184,185
Mvaa 172,136,56
v_xor_b32 v6, 64, v4
v_xor_b32 v7, 96, v4
Mvaa 160,140,12
Ta 184,4,0
Ta 186,4,4096
Mvaa 164,140,28
X 187
Mvaa 168,140,44
v_bfe_u32 v10, v1, 7, 1
v_lshl_or_b32 v10, v10, 31, v13
Mvaa 172,140,60
Ra 156,242,7168
s_mul_i32 s94, s85, 20480
v_smfmac_f32_16x16x64_bf16 a[160:163], a[176:179], v[192:199], v240
X 188
K 215,186,187
v_smfmac_f32_16x16x64_bf16 a[164:167], a[176:179], v[200:207], v240
X 189
X 190
K 222,188,189
X 191
s_nop 0
K 223,190,191
v_smfmac_f32_16x16x64_bf16 a[168:171], a[176:179], v[208:215], v240
Ta 188,5,0
Ta 190,5,4096
v_smfmac_f32_16x16x64_bf16 a[172:175], a[176:179], v[216:223], v240
s_lshl_b32 s82, s14, 11
W 3
Zaaa 232,184,216
s_add_u32 s94, s94, s82
W 2
Cvaa 176,144,0,224
Ra 128,242,16384
s_add_u32 s94, s94, 2760704
Cvaa 180,144,16,228
Ta 192,6,0
s_lshl_b32 s71, s85, 4
Cvaa 184,144,32,232
Ta 194,6,4096
s_lshl_b32 s82, s14, 3
Cvaa 188,144,48,236
s_add_u32 s71, s71, s82
W 3
Zaaa 236,188,216
X 160
Mvaa 176,148,4
Ra 132,242,17408
s_add_u32 s71, s71, 2560
Mvaa 180,148,20
Ta 196,7,0
Ta 198,7,4096
Mvaa 184,148,36
X 161
Mvaa 188,148,52
Ra 136,242,18432
W 4
Zaaa 240,192,216
X 162
Mvaa 176,152,8
K 192,160,161
Ta 200,4,8192
Mvaa 180,152,24
X 163
Mvaa 184,152,40
Ta 202,4,12288
buffer_store_dwordx4 a[232:235], v10, s[8:11], s94 offen offset:0 sc1
Mvaa 188,152,56
Ra 140,242,19456
W 4
Zaaa 244,196,216
X 164
Mvaa 176,156,12
K 193,162,163
Ta 204,5,8192
Mvaa 180,156,28
X 165
Mvaa 184,156,44
Ta 206,5,12288
buffer_store_dwordx4 a[236:239], v10, s[8:11], s94 offen offset:512 sc1
Mvaa 188,156,60
Ra 144,242,20480
W 4
Zaaa 248,200,216
Ta 208,6,8192
W 4
Mvav 32,128,196
X 166
Mvav 64,128,204
K 200,164,165
Ta 210,6,12288
Mvav 96,128,212
X 167
Mvav 128,128,220
buffer_store_dwordx4 a[240:243], v10, s[8:11], s94 offen offset:1024 sc1
W 3
Zaaa 252,204,216
X 168
Mvav 132,132,220
K 201,166,167
Ra 148,242,21504
Mvav 100,132,212
)"
R"(X 169
Mvav 68,132,204
Ta 212,7,8192
Ta 214,7,12288
Mvav 36,132,196
s_add_u32 s64, s64, 1
W 3
Zaaa 232,208,216
X 170
Mvav 40,136,196
buffer_store_dwordx4 a[244:247], v10, s[8:11], s94 offen offset:1536 sc1
s_add_u32 s94, s94, 4096
Mvav 72,136,204
K 208,168,169
Ra 152,242,22528
Mvav 104,136,212
X 171
Mvav 136,136,220
Ta 184,4,16384
W 2
Zaaa 236,212,216
Ra 156,242,23552
Ta 186,4,20480
Mvav 140,140,220
X 172
Mvav 108,140,212
buffer_store_dwordx4 a[248:251], v10, s[8:11], s94 offen offset:0 sc1
K 209,170,171
Mvav 76,140,204
X 173
Mvav 44,140,196
Ta 188,5,16384
W 1
Zaaa 240,184,216
X 174
Mvav 48,144,196
Ta 190,5,20480
buffer_store_dwordx4 a[252:255], v10, s[8:11], s94 offen offset:512 sc1
Mvav 80,144,204
K 216,172,173
Ra 128,242,8192
Mvav 112,144,212
X 175
Mvav 144,144,220
Ta 192,6,16384
W 2
Zaaa 244,188,216
X 176
Mvav 148,148,220
Ta 194,6,20480
buffer_store_dwordx4 a[232:235], v10, s[8:11], s94 offen offset:1024 sc1
Mvav 116,148,212
K 217,174,175
Ra 132,242,9216
Mvav 84,148,204
X 177
Mvav 52,148,196
Ta 196,7,16384
W 2
Zaaa 248,192,216
X 178
Mvav 56,152,196
Ta 198,7,20480
buffer_store_dwordx4 a[236:239], v10, s[8:11], s94 offen offset:1536 sc1
Mvav 88,152,204
s_add_u32 s94, s94, 4096
K 194,176,177
Mvav 120,152,212
X 179
Mvav 152,152,220
Ra 136,242,10240
W 1
Zaaa 252,196,216
Ra 140,242,11264
Ta 200,4,24576
Mvav 156,156,220
Ta 202,4,28672
buffer_store_dwordx4 a[240:243], v10, s[8:11], s94 offen offset:0 sc1
Mvav 124,156,212
X 180
Mvav 92,156,204
K 195,178,179
Ta 204,5,24576
Mvav 60,156,196
Ta 206,5,28672
W 2
Zaaa 232,200,216
X 181
Cvaa 160,128,0,224
X 182
Cvaa 164,128,16,228
K 202,180,181
buffer_store_dwordx4 a[244:247], v10, s[8:11], s94 offen offset:512 sc1
Cvaa 168,128,32,232
X 183
Cvaa 172,128,48,236
Ra 144,242,12288
W 1
Zaaa 236,204,216
X 184
Mvaa 160,132,4
Ta 208,6,24576
Ta 210,6,28672
Mvaa 164,132,20
K 203,182,183
buffer_store_dwordx4 a[248:251], v10, s[8:11], s94 offen offset:1024 sc1
Mvaa 168,132,36
X 185
Mvaa 172,132,52
Ra 148,242,13312
W 1
Zaaa 240,208,216
X 186
Mvaa 160,136,8
Ta 212,7,24576
Ta 214,7,28672
Mvaa 164,136,24
K 210,184,185
buffer_store_dwordx4 a[252:255], v10, s[8:11], s94 offen offset:1536 sc1
Mvaa 168,136,40
s_add_u32 s94, s94, 4096
Ra 152,242,14336
Mvaa 172,136,56
Ta 184,4,32768
W 2
Zaaa 244,212,216
Ra 156,242,15360
Ta 186,4,36864
Mvaa 160,140,12
X 187
Mvaa 164,140,28
X 188
Mvaa 168,140,44
K 211,186,187
buffer_store_dwordx4 a[232:235], v10, s[8:11], s94 offen offset:0 sc1
Mvaa 172,140,60
Ta 188,5,32768
W 1
Zaaa 248,184,216
X 189
Cvaa 176,144,0,224
Ta 190,5,36864
Ra 128,242,24576
Cvaa 180,144,16,228
X 190
Cvaa 184,144,32,232
K 218,188,189
X 191
X 160
K 219,190,191
Cvaa 188,144,48,236
buffer_store_dwordx4 a[236:239], v10, s[8:11], s94 offen offset:512 sc1
W 1
Zaaa 252,188,216
Ta 192,6,32768
Ta 194,6,36864
Mvaa 176,148,4
Ra 132,242,25600
Ta 196,7,32768
Mvaa 180,148,20
X 161
Mvaa 184,148,36
buffer_store_dwordx4 a[240:243], v10, s[8:11], s94 offen offset:1024 sc1
Ra 136,242,26624
Mvaa 188,148,52
Ta 198,7,36864
W 4
Zaaa 232,192,216
X 162
Mvaa 176,152,8
K 196,160,161
Ra 140,242,27648
Mvaa 180,152,24
X 163
Mvaa 184,152,40
buffer_store_dwordx4 a[244:247], v10, s[8:11], s94 offen offset:1536 sc1
s_add_u32 s94, s94, 4096
Mvaa 188,152,56
Ra 144,242,28672
W 2
Zaaa 236,196,216
X 164
Mvaa 176,156,12
K 197,162,163
Ra 148,242,29696
Mvaa 180,156,28
X 165
Mvaa 184,156,44
buffer_store_dwordx4 a[248:251], v10, s[8:11], s94 offen offset:0 sc1
Ra 152,242,30720
Mvaa 188,156,60
W 3
Mvav 32,128,192
X 166
Mvav 64,128,200
K 204,164,165
Ra 156,242,31744
Mvav 96,128,208
X 167
Mvav 128,128,216
buffer_store_dwordx4 a[252:255], v10, s[8:11], s94 offen offset:512 sc1
Mvav 132,132,216
X 168
Mvav 100,132,208
K 205,166,167
Mvav 68,132,200
X 169
Mvav 36,132,192
buffer_store_dwordx4 a[232:235], v10, s[8:11], s94 offen offset:1024 sc1
Mvav 40,136,192
X 170
Mvav 72,136,200
K 212,168,169
Mvav 104,136,208
X 171
Mvav 136,136,216
X 172
Mvav 140,140,216
buffer_store_dwordx4 a[236:239], v10, s[8:11], s94 offen offset:1536 sc1
s_add_u32 s94, s94, 4096
Mvav 108,140,208
K 213,170,171
Mvav 76,140,200
W 0
s_barrier
v_xor_b32 v242, 0x10000, v242
s_bitcmp1_b32 s68, s64
s_cbranch_scc1 .Lact_63%=
.Lactr_64%=:
Mvav 44,140,192
X 173
Mvav 48,144,192
X 174
Mvav 80,144,200
K 220,172,173
Ra 128,242,0
Mvav 112,144,208
X 175
Mvav 144,144,216
X 176
Mvav 148,148,216
K 221,174,175
s_add_u32 s91, s64, 1
Mvav 116,148,208
X 177
Mvav 84,148,200
X 178
Mvav 52,148,192
K 198,176,177
s_bitset1_b32 s68, s91
Mvav 56,152,192
X 179
Mvav 88,152,200
Ra 132,242,1024
Ra 136,242,2048
Mvav 120,152,208
X 180
Mvav 152,152,216
K 199,178,179
Ra 140,242,3072
Mvav 156,156,216
X 181
Mvav 124,156,208
X 182
Mvav 92,156,200
K 206,180,181
Mvav 60,156,192
X 183
s_nop 0
K 207,182,183
s_mov_b32 s85, 99
s_lshl_b32 s65, s57, 1
s_sub_u32 s65, s65, 1
s_branch .Ll0_31%=
.Lpb_29%=:
s_add_u32 s80, s62, 131072
v_add_u32 v12, s80, v1
v_mul_f32 v32, 0x3e0293ee, v32
v_mul_f32 v33, 0x3e0293ee, v33
v_mul_f32 v34, 0x3e0293ee, v34
v_mul_f32 v35, 0x3e0293ee, v35
v_mul_f32 v36, 0x3e0293ee, v36
v_mul_f32 v37, 0x3e0293ee, v37
v_mul_f32 v38, 0x3e0293ee, v38
v_mul_f32 v39, 0x3e0293ee, v39
K 32,32,33
K 33,34,35
K 34,36,37
K 35,38,39
ds_write_b128 v12, v[32:35] offset:0
Ra 0,12,0
v_mul_f32 v64, 0x3e0293ee, v64
v_mul_f32 v65, 0x3e0293ee, v65
v_mul_f32 v66, 0x3e0293ee, v66
v_mul_f32 v67, 0x3e0293ee, v67
v_mul_f32 v68, 0x3e0293ee, v68
v_mul_f32 v69, 0x3e0293ee, v69
v_mul_f32 v70, 0x3e0293ee, v70
v_mul_f32 v71, 0x3e0293ee, v71
K 64,64,65
K 65,66,67
K 66,68,69
K 67,70,71
ds_write_b128 v12, v[64:67] offset:4096
Ra 16,12,4096
v_mul_f32 v96, 0x3e0293ee, v96
v_mul_f32 v97, 0x3e0293ee, v97
v_mul_f32 v98, 0x3e0293ee, v98
v_mul_f32 v99, 0x3e0293ee, v99
v_mul_f32 v100, 0x3e0293ee, v100
v_mul_f32 v101, 0x3e0293ee, v101
v_mul_f32 v102, 0x3e0293ee, v102
v_mul_f32 v103, 0x3e0293ee, v103
K 96,96,97
K 97,98,99
K 98,100,101
K 99,102,103
ds_write_b128 v12, v[96:99] offset:8192
Ra 32,12,8192
v_mul_f32 v128, 0x3e0293ee, v128
v_mul_f32 v129, 0x3e0293ee, v129
v_mul_f32 v130, 0x3e0293ee, v130
v_mul_f32 v131, 0x3e0293ee, v131
v_mul_f32 v132, 0x3e0293ee, v132
v_mul_f32 v133, 0x3e0293ee, v133
v_mul_f32 v134, 0x3e0293ee, v134
v_mul_f32 v135, 0x3e0293ee, v135
K 128,128,129
K 129,130,131
K 130,132,133
K 131,134,135
ds_write_b128 v12, v[128:131] offset:12288
Ra 48,12,12288
v_mul_f32 v40, 0x3e0293ee, v40
v_mul_f32 v41, 0x3e0293ee, v41
v_mul_f32 v42, 0x3e0293ee, v42
v_mul_f32 v43, 0x3e0293ee, v43
v_mul_f32 v44, 0x3e0293ee, v44
v_mul_f32 v45, 0x3e0293ee, v45
v_mul_f32 v46, 0x3e0293ee, v46
v_mul_f32 v47, 0x3e0293ee, v47
K 40,40,41
K 41,42,43
K 42,44,45
K 43,46,47
ds_write_b128 v12, v[40:43] offset:1024
Ra 4,12,1024
v_mul_f32 v72, 0x3e0293ee, v72
v_mul_f32 v73, 0x3e0293ee, v73
v_mul_f32 v74, 0x3e0293ee, v74
v_mul_f32 v75, 0x3e0293ee, v75
v_mul_f32 v76, 0x3e0293ee, v76
v_mul_f32 v77, 0x3e0293ee, v77
v_mul_f32 v78, 0x3e0293ee, v78
v_mul_f32 v79, 0x3e0293ee, v79
K 72,72,73
K 73,74,75
K 74,76,77
K 75,78,79
ds_write_b128 v12, v[72:75] offset:5120
Ra 20,12,5120
v_mul_f32 v104, 0x3e0293ee, v104
v_mul_f32 v105, 0x3e0293ee, v105
v_mul_f32 v106, 0x3e0293ee, v106
v_mul_f32 v107, 0x3e0293ee, v107
v_mul_f32 v108, 0x3e0293ee, v108
v_mul_f32 v109, 0x3e0293ee, v109
v_mul_f32 v110, 0x3e0293ee, v110
v_mul_f32 v111, 0x3e0293ee, v111
K 104,104,105
K 105,106,107
K 106,108,109
K 107,110,111
ds_write_b128 v12, v[104:107] offset:9216
Ra 36,12,9216
v_mul_f32 v136, 0x3e0293ee, v136
v_mul_f32 v137, 0x3e0293ee, v137
v_mul_f32 v138, 0x3e0293ee, v138
v_mul_f32 v139, 0x3e0293ee, v139
v_mul_f32 v140, 0x3e0293ee, v140
v_mul_f32 v141, 0x3e0293ee, v141
v_mul_f32 v142, 0x3e0293ee, v142
v_mul_f32 v143, 0x3e0293ee, v143
K 136,136,137
K 137,138,139
K 138,140,141
K 139,142,143
ds_write_b128 v12, v[136:139] offset:13312
Ra 52,12,13312
v_mul_f32 v48, 0x3e0293ee, v48
v_mul_f32 v49, 0x3e0293ee, v49
v_mul_f32 v50, 0x3e0293ee, v50
v_mul_f32 v51, 0x3e0293ee, v51
v_mul_f32 v52, 0x3e0293ee, v52
v_mul_f32 v53, 0x3e0293ee, v53
v_mul_f32 v54, 0x3e0293ee, v54
v_mul_f32 v55, 0x3e0293ee, v55
K 48,48,49
K 49,50,51
K 50,52,53
)"
R"(K 51,54,55
ds_write_b128 v12, v[48:51] offset:2048
Ra 8,12,2048
v_mul_f32 v80, 0x3e0293ee, v80
v_mul_f32 v81, 0x3e0293ee, v81
v_mul_f32 v82, 0x3e0293ee, v82
v_mul_f32 v83, 0x3e0293ee, v83
v_mul_f32 v84, 0x3e0293ee, v84
v_mul_f32 v85, 0x3e0293ee, v85
v_mul_f32 v86, 0x3e0293ee, v86
v_mul_f32 v87, 0x3e0293ee, v87
K 80,80,81
K 81,82,83
K 82,84,85
K 83,86,87
ds_write_b128 v12, v[80:83] offset:6144
Ra 24,12,6144
v_mul_f32 v112, 0x3e0293ee, v112
v_mul_f32 v113, 0x3e0293ee, v113
v_mul_f32 v114, 0x3e0293ee, v114
v_mul_f32 v115, 0x3e0293ee, v115
v_mul_f32 v116, 0x3e0293ee, v116
v_mul_f32 v117, 0x3e0293ee, v117
v_mul_f32 v118, 0x3e0293ee, v118
v_mul_f32 v119, 0x3e0293ee, v119
K 112,112,113
K 113,114,115
K 114,116,117
K 115,118,119
ds_write_b128 v12, v[112:115] offset:10240
Ra 40,12,10240
v_mul_f32 v144, 0x3e0293ee, v144
v_mul_f32 v145, 0x3e0293ee, v145
v_mul_f32 v146, 0x3e0293ee, v146
v_mul_f32 v147, 0x3e0293ee, v147
v_mul_f32 v148, 0x3e0293ee, v148
v_mul_f32 v149, 0x3e0293ee, v149
v_mul_f32 v150, 0x3e0293ee, v150
v_mul_f32 v151, 0x3e0293ee, v151
K 144,144,145
K 145,146,147
K 146,148,149
K 147,150,151
ds_write_b128 v12, v[144:147] offset:14336
Ra 56,12,14336
v_mul_f32 v56, 0x3e0293ee, v56
v_mul_f32 v57, 0x3e0293ee, v57
v_mul_f32 v58, 0x3e0293ee, v58
v_mul_f32 v59, 0x3e0293ee, v59
v_mul_f32 v60, 0x3e0293ee, v60
v_mul_f32 v61, 0x3e0293ee, v61
v_mul_f32 v62, 0x3e0293ee, v62
v_mul_f32 v63, 0x3e0293ee, v63
K 56,56,57
K 57,58,59
K 58,60,61
K 59,62,63
ds_write_b128 v12, v[56:59] offset:3072
Ra 12,12,3072
v_mul_f32 v88, 0x3e0293ee, v88
v_mul_f32 v89, 0x3e0293ee, v89
v_mul_f32 v90, 0x3e0293ee, v90
v_mul_f32 v91, 0x3e0293ee, v91
v_mul_f32 v92, 0x3e0293ee, v92
v_mul_f32 v93, 0x3e0293ee, v93
v_mul_f32 v94, 0x3e0293ee, v94
v_mul_f32 v95, 0x3e0293ee, v95
K 88,88,89
K 89,90,91
K 90,92,93
K 91,94,95
ds_write_b128 v12, v[88:91] offset:7168
Ra 28,12,7168
v_mul_f32 v120, 0x3e0293ee, v120
v_mul_f32 v121, 0x3e0293ee, v121
v_mul_f32 v122, 0x3e0293ee, v122
v_mul_f32 v123, 0x3e0293ee, v123
v_mul_f32 v124, 0x3e0293ee, v124
v_mul_f32 v125, 0x3e0293ee, v125
v_mul_f32 v126, 0x3e0293ee, v126
v_mul_f32 v127, 0x3e0293ee, v127
K 120,120,121
K 121,122,123
K 122,124,125
K 123,126,127
ds_write_b128 v12, v[120:123] offset:11264
Ra 44,12,11264
v_mul_f32 v152, 0x3e0293ee, v152
v_mul_f32 v153, 0x3e0293ee, v153
v_mul_f32 v154, 0x3e0293ee, v154
v_mul_f32 v155, 0x3e0293ee, v155
v_mul_f32 v156, 0x3e0293ee, v156
v_mul_f32 v157, 0x3e0293ee, v157
v_mul_f32 v158, 0x3e0293ee, v158
v_mul_f32 v159, 0x3e0293ee, v159
K 152,152,153
K 153,154,155
K 154,156,157
K 155,158,159
ds_write_b128 v12, v[152:155] offset:15360
Ra 60,12,15360
s_waitcnt vmcnt(0) lgkmcnt(0)
s_barrier
s_add_u32 s71, s71, 256
v_mov_b64 v[28:29], s[12:13]
s_mov_b64 exec, 1
buffer_store_dwordx2 v[28:29], off, s[8:11], s71 sc1
s_mov_b64 exec, -1
.Lbgo%=:
Ra 128,242,0
Ra 132,242,1024
Ra 136,242,2048
Ra 140,242,3072
W 0
Zvaa 128,128,0
Ra 144,242,4096
Ra 148,242,5120
Ra 152,242,6144
Ra 156,242,7168
Zvaa 132,128,16
v_lshrrev_b32 v20, 4, v1
v_add_u32 v20, 0, v20
v_mul_u32_u24 v26, 205, v20
v_lshrrev_b32 v26, 12, v26
Zvaa 136,128,32
v_mul_u32_u24 v25, 20, v26
v_sub_u32 v20, v20, v25
v_lshrrev_b32 v25, 1, v26
v_and_b32 v25, 2, v25
Zvaa 140,128,48
v_xor_b32 v20, v20, v25
v_mul_u32_u24 v26, 80, v26
v_add_lshl_u32 v16, v20, v26, 4
v_lshrrev_b32 v20, 4, v1
Mvaa 128,132,4
v_add_u32 v20, 64, v20
v_mul_u32_u24 v26, 205, v20
v_lshrrev_b32 v26, 12, v26
Ra 128,242,8192
Mvaa 132,132,20
v_mul_u32_u24 v25, 20, v26
v_sub_u32 v20, v20, v25
v_lshrrev_b32 v25, 1, v26
v_and_b32 v25, 2, v25
Mvaa 136,132,36
v_xor_b32 v20, v20, v25
v_mul_u32_u24 v26, 80, v26
v_add_lshl_u32 v17, v20, v26, 4
v_subrev_u32 v17, 1024, v17
Mvaa 140,132,52
v_lshrrev_b32 v20, 4, v1
v_add_u32 v20, 128, v20
v_mul_u32_u24 v26, 205, v20
v_lshrrev_b32 v26, 12, v26
Mvaa 128,136,8
v_mul_u32_u24 v25, 20, v26
v_sub_u32 v20, v20, v25
v_lshrrev_b32 v25, 1, v26
Ra 132,242,9216
Mvaa 132,136,24
v_and_b32 v25, 2, v25
v_xor_b32 v20, v20, v25
v_mul_u32_u24 v26, 80, v26
v_add_lshl_u32 v18, v20, v26, 4
Mvaa 136,136,40
v_subrev_u32 v18, 2048, v18
v_lshrrev_b32 v20, 4, v1
v_add_u32 v20, 192, v20
v_mul_u32_u24 v26, 205, v20
Mvaa 140,136,56
v_lshrrev_b32 v26, 12, v26
v_mul_u32_u24 v25, 20, v26
v_sub_u32 v20, v20, v25
v_lshrrev_b32 v25, 1, v26
Mvaa 128,140,12
v_and_b32 v25, 2, v25
v_xor_b32 v20, v20, v25
v_mul_u32_u24 v26, 80, v26
Ra 136,242,10240
Mvaa 132,140,28
v_add_lshl_u32 v19, v20, v26, 4
v_subrev_u32 v19, 3072, v19
v_lshrrev_b32 v20, 4, v1
v_add_u32 v20, 256, v20
Mvaa 136,140,44
v_mul_u32_u24 v26, 205, v20
v_lshrrev_b32 v26, 12, v26
v_mul_u32_u24 v25, 20, v26
v_sub_u32 v20, v20, v25
Mvaa 140,140,60
v_lshrrev_b32 v25, 1, v26
v_and_b32 v25, 2, v25
v_xor_b32 v20, v20, v25
W 3
Zvaa 144,144,0
v_mul_u32_u24 v26, 80, v26
v_add_lshl_u32 v20, v20, v26, 4
v_lshrrev_b32 v26, 6, v1
v_mul_u32_u24 v26, 320, v26
Zvaa 148,144,16
Ra 140,242,11264
v_bfe_u32 v25, v1, 4, 2
v_lshl_add_u32 v26, v25, 3, v26
v_bfe_u32 v25, v1, 8, 1
Zvaa 152,144,32
v_lshlrev_b32 v25, 5, v25
v_max3_f32 v4, v128, v129, v130
v_max3_f32 v4, v4, v131, v252
v_max3_f32 v5, v132, v133, v134
Zvaa 156,144,48
v_max3_f32 v5, v5, v135, v253
v_max3_f32 v6, v136, v137, v138
v_max3_f32 v6, v6, v139, v254
v_max3_f32 v7, v140, v141, v142
Mvaa 144,148,4
v_max3_f32 v7, v7, v143, v255
v_mov_b32 v8, v4
v_mov_b32 v9, v5
v_mov_b32 v10, v6
Mvaa 148,148,20
v_mov_b32 v11, v7
v_permlane32_swap_b32 v4, v8
v_permlane32_swap_b32 v5, v9
v_permlane32_swap_b32 v6, v10
Mvaa 152,148,36
v_permlane32_swap_b32 v7, v11
v_max_f32 v4, v4, v8
v_max_f32 v5, v5, v9
v_max_f32 v6, v6, v10
Mvaa 156,148,52
v_max_f32 v7, v7, v11
v_mov_b32 v8, v4
v_mov_b32 v9, v5
v_mov_b32 v10, v6
Mvaa 144,152,8
v_mov_b32 v11, v7
v_permlane16_swap_b32 v4, v8
v_permlane16_swap_b32 v5, v9
v_permlane16_swap_b32 v6, v10
Mvaa 148,152,24
v_permlane16_swap_b32 v7, v11
v_max_f32 v4, v4, v8
v_max_f32 v5, v5, v9
v_max_f32 v6, v6, v10
Mvaa 152,152,40
v_max_f32 v7, v7, v11
v_sub_f32 v4, 0, v4
v_sub_f32 v5, 0, v5
v_sub_f32 v6, 0, v6
Mvaa 156,152,56
v_sub_f32 v7, 0, v7
v_mov_b32 v224, v4
v_mov_b32 v225, v4
v_mov_b32 v226, v4
Mvaa 144,156,12
v_mov_b32 v227, v4
v_mov_b32 v228, v5
v_mov_b32 v229, v5
v_mov_b32 v230, v5
Mvaa 148,156,28
v_mov_b32 v231, v5
v_mov_b32 v232, v6
v_mov_b32 v233, v6
v_mov_b32 v234, v6
Mvaa 152,156,44
v_mov_b32 v235, v6
v_mov_b32 v236, v7
v_mov_b32 v237, v7
v_mov_b32 v238, v7
v_mov_b32 v239, v7
Mvaa 156,156,60
v_add_u32 v21, v26, v25
v_sub_u32 v22, v26, v25
v_add_u32 v23, 61440, v21
W 0
Cvaa 160,128,0,224
v_add_u32 v24, 61440, v22
v_add_u32 v25, 122880, v21
v_add_u32 v26, 122880, v22
s_mul_i32 s94, s59, 655360
Cvaa 164,128,16,228
s_mul_i32 s95, s58, 320
s_add_u32 s94, s94, s95
s_add_u32 s40, s26, s94
s_addc_u32 s41, s27, 0
Cvaa 168,128,32,232
Ra 144,242,12288
s_and_b32 s41, s41, 0xffff
s_mov_b32 s42, 5242880
s_mov_b32 s43, 0x20000
Cvaa 172,128,48,236
s_sub_u32 s42, s42, s94
s_mov_b64 s[44:45], s[8:9]
s_mov_b32 s47, 0x20000
s_mul_i32 s48, s14, 20480
Mvaa 160,132,4
s_and_b32 s94, s14, 1
s_lshl_b32 s95, s57, 1
s_add_u32 s94, s94, s95
s_lshl_b32 s54, s94, 3
Mvaa 164,132,20
s_add_u32 s54, s54, s59
s_lshl_b32 s54, s54, 6
s_xor_b32 s55, s54, 15872
s_add_u32 s54, s54, 20480
Mvaa 168,132,36
Ra 148,242,13312
s_add_u32 s55, s55, 20480
s_and_b32 s95, s14, 2
s_lshl_b32 s94, s94, 2
Mvaa 172,132,52
s_add_u32 s94, s94, s95
s_lshl_b32 s95, s95, 4
s_add_u32 s55, s55, s95
s_lshl_b32 s49, s94, 17
Mvaa 160,136,8
s_lshl_b32 s95, s59, 14
s_add_u32 s49, s49, s95
s_xor_b32 s50, s49, 16252928
s_add_u32 s49, s49, 41943040
Mvaa 164,136,24
s_add_u32 s50, s50, 41943040
s_lshr_b32 s94, s15, 3
s_lshl_b32 s94, s94, 3
s_add_u32 s94, s94, s14
Mvaa 168,136,40
Ra 152,242,14336
Ra 156,242,15360
s_lshl_b32 s94, s94, 4
s_add_u32 s94, s94, s59
Mvaa 172,136,56
s_mul_i32 s52, s94, 5120
s_add_u32 s52, s52, 62914560
s_lshl_b32 s53, s94, 3
s_add_u32 s53, s53, 65536
Mvaa 160,140,12
s_xor_b32 s62, s62, 0x10000
A 128,128,224
A 129,129,224
A 130,130,224
Mvaa 164,140,28
s_and_b32 s80, s14, 1
A 131,131,224
X 128
Mvaa 168,140,44
X 129
s_lshl_b32 s80, s80, 14
s_add_u32 s80, s80, 98304
Mvaa 172,140,60
X 130
K 192,128,129
W 0
Cvaa 176,144,0,224
X 131
A 132,132,228
v_add_u32 v8, s80, v1
Cvaa 180,144,16,228
K 193,130,131
A 133,133,228
A 134,134,228
Ra 128,242,24576
Cvaa 184,144,32,232
A 135,135,228
ds_write_b128 v8, v[4:7]
X 132
Cvaa 188,144,48,236
X 133
X 134
Mvaa 176,148,4
K 200,132,133
X 135
Ra 132,242,25600
Mvaa 180,148,20
A 136,136,232
K 201,134,135
A 137,137,232
A 138,138,232
Mvaa 184,148,36
A 139,139,232
X 136
Ra 136,242,26624
Mvaa 188,148,52
X 137
X 138
Mvaa 176,152,8
K 208,136,137
X 139
A 140,140,236
Mvaa 180,152,24
K 209,138,139
A 141,141,236
A 142,142,236
A 143,143,236
Mvaa 184,152,40
Ra 140,242,27648
X 140
Ra 144,242,28672
Mvaa 188,152,56
X 141
X 142
)"
R"(Mvaa 176,156,12
K 216,140,141
X 143
A 144,144,224
Mvaa 180,156,28
K 217,142,143
A 145,145,224
A 146,146,224
A 147,147,224
Mvaa 184,156,44
X 144
X 145
X 146
K 194,144,145
X 147
A 148,148,228
K 195,146,147
A 149,149,228
A 150,150,228
A 151,151,228
X 148
X 149
X 150
K 202,148,149
X 151
A 152,152,232
K 203,150,151
A 153,153,232
A 154,154,232
A 155,155,232
X 152
X 153
X 154
K 210,152,153
X 155
A 156,156,236
K 211,154,155
A 157,157,236
A 158,158,236
A 159,159,236
X 156
X 157
X 158
K 218,156,157
X 159
X 172
K 219,158,159
Mvaa 188,156,60
Ra 148,242,29696
Ra 152,242,30720
W 3
Zvav 32,128,192
Ra 156,242,31744
Zvav 64,128,200
Zvav 96,128,208
Zvav 128,128,216
Zvav 132,132,216
Zvav 100,132,208
Zvav 68,132,200
Zvav 36,132,192
Zvav 40,136,192
Zvav 72,136,200
X 173
Zvav 104,136,208
X 174
K 220,172,173
Zvav 136,136,216
X 175
Zvav 140,140,216
X 168
K 221,174,175
Zvav 108,140,208
X 169
X 170
Zvav 76,140,200
s_waitcnt vmcnt(0) lgkmcnt(0)
s_barrier
v_xor_b32 v242, 0x10000, v242
K 212,168,169
Zvav 44,140,192
X 171
X 164
Zvav 48,144,192
Ra 128,242,0
K 213,170,171
X 165
Zvav 80,144,200
X 166
Zvav 112,144,208
K 204,164,165
X 167
Ra 132,242,1024
Zvav 144,144,216
X 160
Zvav 148,148,216
K 205,166,167
X 161
Zvav 116,148,208
X 162
Ra 136,242,2048
Zvav 84,148,200
K 196,160,161
X 163
Zvav 52,148,192
X 176
K 197,162,163
Zvav 56,152,192
X 177
Ra 140,242,3072
Zvav 88,152,200
X 178
K 198,176,177
Zvav 120,152,208
X 179
X 180
Zvav 152,152,216
K 199,178,179
Zvav 156,156,216
X 181
X 182
Zvav 124,156,208
K 206,180,181
X 183
Zvav 92,156,200
K 207,182,183
Zvav 60,156,192
s_branch .Ll1_32%=
.Lf1_38%=:
W 0
Cvaa 160,128,0,224
Ra 144,242,4096
Ra 148,242,5120
Cvaa 164,128,16,228
Ra 152,242,6144
s_lshr_b32 s92, s14, 1
Cvaa 168,128,32,232
X 184
Cvaa 172,128,48,236
X 185
Mvaa 160,132,4
s_and_b32 s71, s14, 1
s_mul_i32 s71, s71, 5120
Mvaa 164,132,20
X 186
Mvaa 168,132,36
K 214,184,185
Ra 156,242,7168
Mvaa 172,132,52
X 187
Mvaa 160,136,8
v_bfe_u32 v28, v1, 4, 2
v_lshlrev_b32 v28, 3, v28
Mvaa 164,136,24
X 188
Mvaa 168,136,40
K 215,186,187
buffer_load_dwordx2 v[28:29], v28, s[8:11], s54 offen sc1
Mvaa 172,136,56
X 189
Mvaa 160,140,12
Ra 128,242,16384
s_add_u32 s94, s58, 0
Mvaa 164,140,28
X 190
Mvaa 168,140,44
K 222,188,189
s_and_b32 s94, s94, 3
Mvaa 172,140,60
X 191
v_smfmac_f32_16x16x64_bf16 a[160:163], a[176:179], v[192:199], v240
s_mul_i32 s94, s94, 163840
K 223,190,191
v_smfmac_f32_16x16x64_bf16 a[164:167], a[176:179], v[200:207], v240
Ra 132,242,17408
s_add_u32 s94, s94, s48
v_smfmac_f32_16x16x64_bf16 a[168:171], a[176:179], v[208:215], v240
s_mul_i32 s95, s92, 10240
s_add_u32 s95, s95, s71
v_smfmac_f32_16x16x64_bf16 a[172:175], a[176:179], v[216:223], v240
Ra 136,242,18432
W 3
Cvaa 176,144,0,224
X 160
Cvaa 180,144,16,228
X 161
Cvaa 184,144,32,232
X 162
Cvaa 188,144,48,236
K 192,160,161
s_add_u32 m0, s95, 0
Mvaa 176,148,4
X 163
Mvaa 180,148,20
X 164
Mvaa 184,148,36
D 16,40,94,0
K 193,162,163
Mvaa 188,148,52
Ra 140,242,19456
Ra 144,242,20480
Mvaa 176,152,8
X 165
Mvaa 180,152,24
D 17,40,94,1024
Ra 148,242,21504
Mvaa 184,152,40
X 166
Mvaa 188,152,56
K 200,164,165
Mvaa 176,156,12
D 18,40,94,2048
Ra 152,242,22528
Mvaa 180,156,28
X 167
Mvaa 184,156,44
X 172
Mvaa 188,156,60
s_cmp_eq_u32 s60, 16
s_cbranch_scc1 .Lmk_65%=
.Lmkr_66%=:
W 3
Mvav 32,128,196
D 19,40,94,3072
s_add_u32 m0, s95, 4096
Mvav 64,128,204
K 201,166,167
Ra 156,242,23552
Mvav 96,128,212
X 173
Mvav 128,128,220
buffer_load_dwordx4 v20, s[40:43], s94 offen lds
s_add_u32 s94, s94, 81920
Mvav 132,132,220
X 174
Mvav 100,132,212
s_mul_i32 s95, s92, 112640
K 216,172,173
Mvav 68,132,204
s_add_u32 s95, s95, s71
s_add_u32 m0, s95, 20480
Mvav 36,132,196
X 175
Mvav 40,136,196
D 16,40,94,0
Ra 128,242,24576
Mvav 72,136,204
X 168
Mvav 104,136,212
K 217,174,175
Mvav 136,136,220
D 17,40,94,1024
Mvav 140,140,220
X 169
Mvav 108,140,212
X 170
Mvav 76,140,204
D 18,40,94,2048
K 208,168,169
Mvav 44,140,196
Ra 132,242,25600
W 2
Mvav 48,144,196
X 171
Mvav 80,144,204
D 19,40,94,3072
s_add_u32 m0, s95, 24576
Mvav 112,144,212
X 176
Mvav 144,144,220
K 209,170,171
Ra 136,242,26624
Mvav 148,148,220
X 177
Mvav 116,148,212
buffer_load_dwordx4 v20, s[40:43], s94 offen lds
s_waitcnt vmcnt(10)
v_cmp_eq_u64 vcc, s[12:13], v[28:29]
s_cmp_eq_u64 vcc, -1
s_cselect_b32 s46, 134217728, 0
Mvav 84,148,204
X 178
Mvav 52,148,196
K 194,176,177
s_add_u32 s81, s58, 0
Mvav 56,152,196
X 179
Mvav 88,152,204
s_and_b32 s81, s81, 3
s_lshl_b32 s81, s81, 12
Mvav 120,152,212
X 180
Mvav 152,152,220
K 195,178,179
s_add_u32 s81, s81, s49
Mvav 156,156,220
X 181
Mvav 124,156,212
X 182
Mvav 92,156,204
Ra 140,242,27648
s_add_u32 s82, s81, 131072
Mvav 60,156,196
K 202,180,181
X 183
X 184
K 203,182,183
Maav 160,180,192
X 185
X 186
K 210,184,185
X 187
X 188
K 211,186,187
Maav 164,180,200
X 189
X 190
K 218,188,189
X 191
s_nop 0
K 219,190,191
Maav 168,180,208
buffer_load_dwordx4 a[184:187], v1, s[44:47], s81 offen offset:0 sc1
Ra 144,242,28672
Maav 172,180,216
Ra 148,242,29696
W 2
Mvav 32,128,192
Ra 152,242,30720
Ra 156,242,31744
Mvav 64,128,200
buffer_load_dwordx4 a[200:203], v1, s[44:47], s82 offen offset:0 sc1
Mvav 96,128,208
Mvav 128,128,216
Mvav 132,132,216
buffer_load_dwordx4 a[188:191], v1, s[44:47], s81 offen offset:1024 sc1
Mvav 100,132,208
Mvav 68,132,200
Mvav 36,132,192
buffer_load_dwordx4 a[204:207], v1, s[44:47], s82 offen offset:1024 sc1
Mvav 40,136,192
Mvav 72,136,200
Mvav 104,136,208
buffer_load_dwordx4 a[192:195], v1, s[44:47], s81 offen offset:2048 sc1
Mvav 136,136,216
Mvav 140,140,216
Mvav 108,140,208
buffer_load_dwordx4 a[208:211], v1, s[44:47], s82 offen offset:2048 sc1
Mvav 76,140,200
Mvav 44,140,192
W 0
Mvav 48,144,192
buffer_load_dwordx4 a[196:199], v1, s[44:47], s81 offen offset:3072 sc1
Mvav 80,144,200
Mvav 112,144,208
Mvav 144,144,216
buffer_load_dwordx4 a[212:215], v1, s[44:47], s82 offen offset:3072 sc1
s_add_u32 s94, s58, 1
Mvav 148,148,216
s_and_b32 s94, s94, 3
s_mul_i32 s94, s94, 163840
Mvav 116,148,208
s_add_u32 s94, s94, s48
s_mul_i32 s95, s92, 10240
Mvav 84,148,200
s_add_u32 s95, s95, s71
s_add_u32 m0, s95, 143360
Mvav 52,148,192
D 16,40,94,0
Mvav 56,152,192
Mvav 88,152,200
Mvav 120,152,208
D 17,40,94,1024
Mvav 152,152,216
Mvav 156,156,216
Mvav 124,156,208
D 18,40,94,2048
Mvav 92,156,200
Mvav 60,156,192
D 19,40,94,3072
s_add_u32 m0, s95, 147456
s_nop 0
buffer_load_dwordx4 v20, s[40:43], s94 offen lds
s_add_u32 s81, s58, 1
s_and_b32 s81, s81, 3
s_lshl_b32 s81, s81, 12
s_add_u32 s81, s81, s49
s_add_u32 s82, s81, 131072
buffer_load_dwordx4 a[216:219], v1, s[44:47], s81 offen offset:0 sc1
buffer_load_dwordx4 a[232:235], v1, s[44:47], s82 offen offset:0 sc1
buffer_load_dwordx4 a[220:223], v1, s[44:47], s81 offen offset:1024 sc1
buffer_load_dwordx4 a[236:239], v1, s[44:47], s82 offen offset:1024 sc1
buffer_load_dwordx4 a[224:227], v1, s[44:47], s81 offen offset:2048 sc1
buffer_load_dwordx4 a[240:243], v1, s[44:47], s82 offen offset:2048 sc1
buffer_load_dwordx4 a[228:231], v1, s[44:47], s81 offen offset:3072 sc1
buffer_load_dwordx4 a[244:247], v1, s[44:47], s82 offen offset:3072 sc1
.Lend_41%=:
s_mov_b32 s80, 40960
s_mov_b32 s81, 73728
s_bitcmp1_b32 s14, 0
s_cselect_b32 s80, 49152, s80
)"
R"(s_cselect_b32 s81, 98304, s81
s_cmp_lt_u32 s14, 2
s_cselect_b32 s82, s80, s81
s_cselect_b32 s83, s81, s80
v_add_u32 v2, s82, v1
v_add_u32 v3, s83, v1
s_lshl_b32 s80, s14, 9
v_lshrrev_b32 v29, 1, v1
v_add_u32 v29, s80, v29
G 4,160
G 8,161
G 5,164
G 9,165
G 6,168
G 10,169
G 7,172
G 11,173
A 4,4,8
A 5,5,9
A 6,6,10
A 7,7,11
ds_write_b64 v29, v[6:7] offset:57344
v_cmp_ngt_f32 vcc, s90, v4
s_or_b64 s[88:89], s[88:89], vcc
v_cmp_ngt_f32 vcc, s90, v5
s_or_b64 s[88:89], s[88:89], vcc
v_cmp_ngt_f32 vcc, s90, v6
s_or_b64 s[88:89], s[88:89], vcc
v_cmp_ngt_f32 vcc, s90, v7
s_or_b64 s[88:89], s[88:89], vcc
s_and_b64 s[88:89], s[88:89], s[24:25]
s_cselect_b32 s80, 1, 0
s_lshl_b32 s81, s14, 2
v_mov_b32 v8, s80
v_mov_b32 v9, s81
ds_write_b32 v9, v8 offset:59392
K 96,96,97
K 97,98,99
K 98,100,101
K 99,102,103
ds_write_b128 v2, v[96:99] offset:0
K 104,104,105
K 105,106,107
K 106,108,109
K 107,110,111
ds_write_b128 v2, v[104:107] offset:1024
K 112,112,113
K 113,114,115
K 114,116,117
K 115,118,119
ds_write_b128 v2, v[112:115] offset:2048
K 120,120,121
K 121,122,123
K 122,124,125
K 123,126,127
ds_write_b128 v2, v[120:123] offset:3072
K 128,128,129
K 129,130,131
K 130,132,133
K 131,134,135
ds_write_b128 v2, v[128:131] offset:4096
K 136,136,137
K 137,138,139
K 138,140,141
K 139,142,143
ds_write_b128 v2, v[136:139] offset:5120
K 144,144,145
K 145,146,147
K 146,148,149
K 147,150,151
ds_write_b128 v2, v[144:147] offset:6144
K 152,152,153
K 153,154,155
K 154,156,157
K 155,158,159
ds_write_b128 v2, v[152:155] offset:7168
v_xor_b32 v10, 1024, v29
v_bfe_u32 v11, v1, 2, 4
v_bfe_u32 v12, v1, 6, 2
v_lshrrev_b32 v13, 8, v1
v_bfe_u32 v14, v1, 4, 1
v_lshlrev_b32 v14, 4, v14
v_mov_b32 v15, 0x3f80
v_lshlrev_b32 v15, v14, v15
v_bfe_u32 v14, v1, 5, 1
v_xor_b32 v28, v12, v13
v_lshl_or_b32 v28, v28, 1, v14
v_cmp_eq_u32 vcc, 0, v28
s_nop 1
v_cndmask_b32 v208, 0, v15, vcc
v_mov_b32 v210, 0
v_mov_b32 v212, 0
v_mov_b32 v214, v208
v_cmp_eq_u32 vcc, 1, v28
s_nop 1
v_cndmask_b32 v209, 0, v15, vcc
v_mov_b32 v211, 0
v_mov_b32 v213, 0
v_mov_b32 v215, v209
s_waitcnt vmcnt(8) lgkmcnt(0)
s_barrier
s_cmp_eq_u32 s46, 0
s_cbranch_scc1 .Llate_78%=
ds_read_b32 v11, v11 offset:59392
ds_read_b64 v[8:9], v10 offset:57344
Rv 176,3,0
Rv 180,3,1024
Rv 184,3,2048
Rv 188,3,3072
Rv 192,3,4096
Rv 196,3,5120
Rv 200,3,6144
Rv 204,3,7168
W 6
Mvvv 32,208,176
v_cmp_ne_u32 vcc, 0, v11
s_cmp_lg_u64 vcc, 0
s_cbranch_scc1 .Lredo%=
Mvvv 36,212,176
s_add_u32 s94, s94, 81920
A 4,4,8
Mvvv 40,208,180
v_rcp_f32 v4, v4
Mvvv 44,212,180
s_mul_i32 s95, s92, 30720
W 4
Mvvv 48,208,184
A 5,5,9
Ta 128,21,0
Mvvv 52,212,184
Ta 130,21,5120
s_add_u32 s95, s95, s71
Mvvv 56,208,188
v_rcp_f32 v5, v5
Mvvv 60,212,188
s_lshl_b32 s93, s58, 12
W 4
Mvvv 64,208,192
s_add_u32 s93, s93, s50
Ta 132,22,32
Mvvv 68,212,192
Ta 134,22,5152
U 32,32,4
Mvvv 72,208,196
s_add_u32 m0, s95, 30720
U 33,33,4
Mvvv 76,212,196
U 34,34,4
W 4
Mvvv 80,208,200
Ta 136,21,64
Ta 138,21,5184
Mvvv 84,212,200
U 35,35,4
D 16,40,94,0
Mvvv 88,208,204
U 36,36,4
U 37,37,4
Mvvv 92,212,204
Ta 140,22,96
Ta 142,22,5216
W 4
Zvaa 96,128,184
U 38,38,4
D 17,40,94,1024
Zvaa 104,128,200
U 39,39,4
K 32,32,33
Zvaa 108,132,200
Ta 144,21,128
Ta 146,21,5248
Zvaa 100,132,184
Ta 148,22,160
Ta 150,22,5280
W 4
Zvaa 112,136,184
K 33,34,35
D 18,40,94,2048
Zvaa 120,136,200
K 34,36,37
K 35,38,39
Zvaa 124,140,200
Ta 152,21,192
Ta 154,21,5312
Zvaa 116,140,184
Ta 156,22,224
Ta 158,22,5344
W 4
Zvaa 128,144,184
buffer_store_dwordx4 v[32:35], v1, s[8:11], s93 offen offset:0 sc1
U 40,40,4
Zvaa 136,144,200
U 41,41,4
U 42,42,4
Zvaa 140,148,200
U 43,43,4
U 44,44,4
Zvaa 132,148,184
Ta 128,21,256
Ta 130,21,5376
Ta 132,22,288
Ta 134,22,5408
W 4
Zvaa 144,152,184
D 19,40,94,3072
s_add_u32 m0, s95, 34816
Zvaa 152,152,200
U 45,45,4
U 46,46,4
Zvaa 156,156,200
U 47,47,4
K 40,40,41
Zvaa 148,156,184
Ta 136,21,10240
Ta 138,21,15360
Ta 140,22,10272
Ta 142,22,15392
W 4
Zvaa 160,128,184
buffer_load_dwordx4 v20, s[40:43], s94 offen lds
K 41,42,43
Zvaa 168,128,200
s_add_u32 s94, s58, 2
K 42,44,45
Zvaa 172,132,200
K 43,46,47
s_and_b32 s94, s94, 3
Zvaa 164,132,184
Ta 144,21,10304
Ta 146,21,15424
Ta 148,22,10336
Ta 150,22,15456
W 4
Mvaa 96,136,188
buffer_store_dwordx4 v[40:43], v1, s[8:11], s93 offen offset:1024 sc1
U 48,48,4
Mvaa 104,136,204
U 49,49,4
U 50,50,4
Mvaa 108,140,204
s_mul_i32 s94, s94, 163840
U 51,51,4
Mvaa 100,140,188
Ta 152,21,10368
Ta 154,21,15488
Ta 156,22,10400
Ta 158,22,15520
W 4
Mvaa 112,144,188
U 52,52,4
U 53,53,4
Mvaa 120,144,204
s_add_u32 s94, s94, s48
U 54,54,4
Mvaa 124,148,204
U 55,55,4
K 48,48,49
Mvaa 116,148,188
Ta 128,21,10432
Ta 130,21,15552
Ta 132,22,10464
Ta 134,22,15584
W 4
Mvaa 128,152,188
s_mul_i32 s95, s92, 30720
K 49,50,51
Mvaa 136,152,204
K 50,52,53
K 51,54,55
Mvaa 140,156,204
s_add_u32 s95, s95, s71
buffer_store_dwordx4 v[48:51], v1, s[8:11], s93 offen offset:2048 sc1
Mvaa 132,156,188
Ta 136,21,10496
Ta 138,21,15616
Ta 140,22,10528
Ta 142,22,15648
W 4
Mvaa 144,128,188
U 56,56,4
U 57,57,4
Mvaa 152,128,204
s_add_u32 m0, s95, 81920
U 58,58,4
Mvaa 156,132,204
U 59,59,4
U 60,60,4
Mvaa 148,132,188
Ta 144,21,20480
Ta 146,21,25600
Ta 148,22,20512
Ta 150,22,25632
W 4
Mvaa 160,136,188
D 16,40,94,0
U 61,61,4
Mvaa 168,136,204
D 17,40,94,1024
D 18,40,94,2048
D 19,40,94,3072
s_add_u32 m0, s95, 86016
s_nop 0
buffer_load_dwordx4 v20, s[40:43], s94 offen lds
s_waitcnt vmcnt(13)
s_cmp_lg_u32 s70, 0
s_cbranch_scc1 .Lpd_80%=
.Lpdr_81%=:
Mvaa 172,140,204
U 62,62,4
U 63,63,4
Mvaa 164,140,188
Ta 152,21,20544
Ta 154,21,25664
Ta 156,22,20576
Ta 158,22,25696
W 4
Mvaa 96,144,192
K 56,56,57
K 57,58,59
Mvaa 104,144,208
K 58,60,61
K 59,62,63
Mvaa 108,148,208
buffer_store_dwordx4 v[56:59], v1, s[8:11], s93 offen offset:3072 sc1
U 64,64,5
Mvaa 100,148,192
Ta 128,21,20608
Ta 130,21,25728
Ta 132,22,20640
Ta 134,22,25760
W 4
Mvaa 112,152,192
U 65,65,5
U 66,66,5
Mvaa 120,152,208
U 67,67,5
U 68,68,5
Mvaa 124,156,208
U 69,69,5
U 70,70,5
Mvaa 116,156,192
Ta 136,21,20672
Ta 138,21,25792
Ta 140,22,20704
Ta 142,22,25824
W 4
Mvaa 128,128,192
U 71,71,5
K 64,64,65
Mvaa 136,128,208
K 65,66,67
K 66,68,69
Mvaa 140,132,208
K 67,70,71
s_add_u32 s67, s93, 131072
Mvaa 132,132,192
Ta 144,21,20736
Ta 146,21,25856
Ta 148,22,20768
Ta 150,22,25888
W 4
Mvaa 144,136,192
buffer_store_dwordx4 v[64:67], v1, s[8:11], s67 offen offset:0 sc1
U 72,72,5
Mvaa 152,136,208
U 73,73,5
U 74,74,5
Mvaa 156,140,208
U 75,75,5
U 76,76,5
Mvaa 148,140,192
Ta 152,25,10240
Ta 154,25,15360
Ta 156,26,10272
Ta 158,26,15392
W 4
Mvaa 160,144,192
U 77,77,5
U 78,78,5
Mvaa 168,144,208
s_add_u32 s81, s58, 2
U 79,79,5
Mvaa 172,148,208
K 72,72,73
K 73,74,75
Mvaa 164,148,192
Ta 128,25,10304
Ta 130,25,15424
Ta 132,26,10336
Ta 134,26,15456
W 4
Mvaa 96,152,196
K 74,76,77
K 75,78,79
Mvaa 104,152,212
buffer_store_dwordx4 v[72:75], v1, s[8:11], s67 offen offset:1024 sc1
s_and_b32 s81, s81, 3
Mvaa 108,156,212
U 80,80,5
U 81,81,5
)"
R"(Mvaa 100,156,196
Ta 136,25,10368
Ta 138,25,15488
Ta 140,26,10400
Ta 142,26,15520
W 4
Mvaa 112,128,196
U 82,82,5
U 83,83,5
Mvaa 120,128,212
U 84,84,5
U 85,85,5
Mvaa 124,132,212
s_lshl_b32 s81, s81, 12
U 86,86,5
Mvaa 116,132,196
Ta 144,25,10432
Ta 146,25,15552
Ta 148,26,10464
Ta 150,26,15584
W 4
Mvaa 128,136,196
U 87,87,5
K 80,80,81
Mvaa 136,136,212
K 81,82,83
K 82,84,85
Mvaa 140,140,212
K 83,86,87
buffer_store_dwordx4 v[80:83], v1, s[8:11], s67 offen offset:2048 sc1
Mvaa 132,140,196
Ta 152,25,10496
Ta 154,25,15616
Ta 156,26,10528
Ta 158,26,15648
W 4
Mvaa 144,144,196
s_add_u32 s81, s81, s49
U 88,88,5
Mvaa 152,144,212
U 89,89,5
U 90,90,5
Mvaa 156,148,212
U 91,91,5
U 92,92,5
Mvaa 148,148,196
Ta 128,25,20480
Ta 130,25,25600
Ta 132,26,20512
Ta 134,26,25632
W 4
Mvaa 160,152,196
U 93,93,5
s_add_u32 s82, s81, 131072
Mvaa 168,152,212
U 94,94,5
U 95,95,5
Mvaa 172,156,212
K 88,88,89
K 89,90,91
Mvaa 164,156,196
Ta 136,25,20544
Ta 138,25,25664
Ta 140,26,20576
Ta 142,26,25696
W 4
Mvaa 96,128,216
K 90,92,93
K 91,94,95
Mvaa 104,128,232
buffer_load_dwordx4 a[0:3], v1, s[8:11], s81 offen offset:0 sc1
Ta 144,25,20608
Mvaa 108,132,232
Ta 146,25,25728
Ta 148,26,20640
Mvaa 100,132,216
Ta 150,26,25760
W 4
Mvaa 112,136,216
buffer_store_dwordx4 v[88:91], v1, s[8:11], s67 offen offset:3072 sc1
Ta 152,25,20672
Mvaa 120,136,232
Ta 154,25,25792
Ta 156,26,20704
Mvaa 124,140,232
Ta 158,26,25824
Ta 128,25,20736
Mvaa 116,140,216
buffer_load_dwordx4 a[16:19], v1, s[8:11], s82 offen offset:0 sc1
W 5
Mvaa 128,144,216
Ta 130,25,25856
Ta 132,26,20768
Mvaa 136,144,232
Ta 134,26,25888
Ta 136,25,30720
Mvaa 140,148,232
buffer_load_dwordx4 a[4:7], v1, s[8:11], s81 offen offset:1024 sc1
Ta 138,25,35840
Mvaa 132,148,216
Ta 140,26,30752
W 7
Mvaa 144,152,216
Ta 142,26,35872
Ta 144,25,30784
Mvaa 152,152,232
buffer_load_dwordx4 a[20:23], v1, s[8:11], s82 offen offset:1024 sc1
Ta 146,25,35904
Mvaa 156,156,232
Ta 148,26,30816
Ta 150,26,35936
Mvaa 148,156,216
Ta 152,25,30848
W 9
Mvaa 160,128,216
buffer_load_dwordx4 a[8:11], v1, s[8:11], s81 offen offset:2048 sc1
Ta 154,25,35968
Mvaa 168,128,232
Ta 156,26,30880
Ta 158,26,36000
Mvaa 172,132,232
Mvaa 164,132,216
buffer_load_dwordx4 a[24:27], v1, s[8:11], s82 offen offset:2048 sc1
W 8
Mvaa 96,136,220
s_waitcnt vmcnt(11)
s_barrier
Mvaa 104,136,236
Ta 128,25,30912
Ta 130,25,36032
Mvaa 108,140,236
buffer_load_dwordx4 a[12:15], v1, s[8:11], s81 offen offset:3072 sc1
Ta 132,26,30944
Mvaa 100,140,220
Ta 134,26,36064
W 8
Mvaa 112,144,220
Ta 136,25,30976
Ta 138,25,36096
Mvaa 120,144,236
buffer_load_dwordx4 a[28:31], v1, s[8:11], s82 offen offset:3072 sc1
s_add_u32 s94, s94, 81920
Mvaa 124,148,236
s_mul_i32 s95, s92, -81920
s_add_u32 s95, s95, s71
Mvaa 116,148,220
Ta 140,26,31008
W 7
Mvaa 128,152,220
Ta 142,26,36128
s_add_u32 m0, s95, 122880
Mvaa 136,152,236
Ta 144,21,30720
Ta 146,21,35840
Mvaa 140,156,236
D 16,40,94,0
Ta 148,22,30752
Mvaa 132,156,220
Ta 150,22,35872
W 8
Mvaa 144,128,220
Ta 152,21,30784
Ta 154,21,35904
Mvaa 152,128,236
D 17,40,94,1024
Ta 156,22,30816
Mvaa 156,132,236
Ta 158,22,35936
Mvaa 148,132,220
Ta 128,21,30848
W 9
Mvaa 160,136,220
D 18,40,94,2048
Ta 130,21,35968
Mvaa 168,136,236
Ta 132,22,30880
Ta 134,22,36000
Mvaa 172,140,236
Mvaa 164,140,220
D 19,40,94,3072
W 8
Mvaa 96,144,224
s_add_u32 m0, s95, 126976
Ta 136,21,30912
Mvaa 104,144,240
Ta 138,21,36032
Ta 140,22,30944
Mvaa 108,148,240
buffer_load_dwordx4 v20, s[40:43], s94 offen lds
s_add_u32 s94, s58, 3
Mvaa 100,148,224
s_and_b32 s94, s94, 3
W 7
Mvaa 112,152,224
s_lshl_b32 s80, s58, 3
s_add_u32 s80, s80, s55
Mvaa 120,152,240
s_waitcnt vmcnt(12)
v_mov_b64 v[28:29], s[12:13]
s_mov_b64 exec, 1
buffer_store_dwordx2 v[28:29], off, s[8:11], s80 sc1
s_mov_b64 exec, -1
Mvaa 124,156,240
Ta 142,22,36064
s_mul_i32 s94, s94, 163840
Mvaa 116,156,224
Ta 144,21,30976
W 5
Mvaa 128,128,224
Ta 146,21,36096
s_add_u32 s94, s94, s48
Mvaa 136,128,240
Ta 148,22,31008
Ta 150,22,36128
Mvaa 140,132,240
s_mul_i32 s95, s92, 20480
Ta 152,23,0
Mvaa 132,132,224
Ta 154,23,5120
W 6
Mvaa 144,136,224
s_add_u32 s95, s95, s71
Ta 156,24,32
Mvaa 152,136,240
Ta 158,24,5152
s_add_u32 m0, s95, 51200
Mvaa 156,140,240
Ta 128,23,64
Ta 130,23,5184
Mvaa 148,140,224
D 16,40,94,0
W 6
Mvaa 160,144,224
Ta 132,24,96
Ta 134,24,5216
Mvaa 168,144,240
Ta 136,23,128
Ta 138,23,5248
Mvaa 172,148,240
D 17,40,94,1024
Ta 140,24,160
Mvaa 164,148,224
Ta 142,24,5280
W 8
Mvaa 96,152,228
Ta 144,23,192
Ta 146,23,5312
Mvaa 104,152,244
D 18,40,94,2048
Ta 148,24,224
Mvaa 108,156,244
Ta 150,24,5344
Mvaa 100,156,228
Ta 152,23,256
W 9
Mvaa 112,128,228
D 19,40,94,3072
s_add_u32 m0, s95, 55296
Mvaa 120,128,244
Ta 154,23,5376
Ta 156,24,288
Mvaa 124,132,244
Ta 158,24,5408
Mvaa 116,132,228
buffer_load_dwordx4 v20, s[40:43], s94 offen lds
W 8
Mvaa 128,136,228
s_add_u32 s94, s94, 81920
s_mul_i32 s95, s92, 10240
Mvaa 136,136,244
s_add_u32 s95, s95, s71
Ta 128,23,20480
Mvaa 140,140,244
Ta 130,23,25600
s_add_u32 m0, s95, 92160
Mvaa 132,140,228
Ta 132,24,20512
W 7
Mvaa 144,144,228
Ta 134,24,25632
D 16,40,94,0
Mvaa 152,144,244
Ta 136,23,20544
Ta 138,23,25664
Mvaa 156,148,244
Ta 140,24,20576
Ta 142,24,25696
Mvaa 148,148,228
D 17,40,94,1024
W 8
Mvaa 160,152,228
Ta 144,23,20608
Ta 146,23,25728
Mvaa 168,152,244
Ta 148,24,20640
Ta 150,24,25760
Mvaa 172,156,244
D 18,40,94,2048
Mvaa 164,156,228
Ta 152,23,20672
s_waitcnt vmcnt(20) lgkmcnt(9)
Mvaa 96,128,0
Ta 154,23,25792
Mvaa 104,128,16
D 19,40,94,3072
s_add_u32 m0, s95, 96256
Mvaa 108,132,16
Ta 156,24,20704
Ta 158,24,25824
Mvaa 100,132,0
Ta 128,23,20736
W 9
Mvaa 112,136,0
buffer_load_dwordx4 v20, s[40:43], s94 offen lds
s_add_u32 s81, s58, 3
Mvaa 120,136,16
Ta 130,23,25856
s_and_b32 s81, s81, 3
Mvaa 124,140,16
Ta 132,24,20768
Ta 134,24,25888
Mvaa 116,140,0
s_lshl_b32 s81, s81, 12
W 8
Mvaa 128,144,0
Ta 136,23,51200
Ta 138,23,56320
Mvaa 136,144,16
Ta 140,24,51232
Ta 142,24,56352
Mvaa 140,148,16
s_add_u32 s81, s81, s49
Mvaa 132,148,0
Ta 144,23,51264
W 9
Mvaa 144,152,0
Ta 146,23,56384
s_add_u32 s82, s81, 131072
Mvaa 152,152,16
Ta 148,24,51296
Ta 150,24,56416
Mvaa 156,156,16
Mvaa 148,156,0
buffer_load_dwordx4 a[32:35], v1, s[8:11], s81 offen offset:0 sc1
W 8
Mvaa 160,128,0
Ta 152,23,51328
Ta 154,23,56448
Mvaa 168,128,16
Ta 156,24,51360
Ta 158,24,56480
Mvaa 172,132,16
buffer_load_dwordx4 a[48:51], v1, s[8:11], s82 offen offset:0 sc1
Mvaa 164,132,0
Ta 128,23,51392
s_waitcnt vmcnt(22) lgkmcnt(9)
Mvaa 96,136,4
s_waitcnt vmcnt(2)
s_barrier
Mvaa 104,136,20
Ta 130,23,56512
buffer_load_dwordx4 a[36:39], v1, s[8:11], s81 offen offset:1024 sc1
Mvaa 108,140,20
Ta 132,24,51424
Ta 134,24,56544
Mvaa 100,140,4
Ta 136,23,51456
W 9
Mvaa 112,144,4
buffer_load_dwordx4 a[52:55], v1, s[8:11], s82 offen offset:1024 sc1
Ta 138,23,56576
Mvaa 120,144,20
Ta 140,24,51488
Ta 142,24,56608
Mvaa 124,148,20
Mvaa 116,148,4
buffer_load_dwordx4 a[40:43], v1, s[8:11], s81 offen offset:2048 sc1
W 8
Mvaa 128,152,4
Ta 144,25,0
Ta 146,25,5120
Mvaa 136,152,20
Ta 148,26,32
Ta 150,26,5152
Mvaa 140,156,20
buffer_load_dwordx4 a[56:59], v1, s[8:11], s82 offen offset:2048 sc1
Mvaa 132,156,4
Ta 152,25,64
W 9
Mvaa 144,128,4
Ta 154,25,5184
Mvaa 152,128,20
buffer_load_dwordx4 a[44:47], v1, s[8:11], s81 offen offset:3072 sc1
Ta 156,26,96
Mvaa 156,132,20
Ta 158,26,5216
Mvaa 148,132,4
Ta 128,25,128
W 9
Mvaa 160,136,4
)"
R"(buffer_load_dwordx4 a[60:63], v1, s[8:11], s82 offen offset:3072 sc1
Ta 130,25,5248
Mvaa 168,136,20
Ta 132,26,160
Ta 134,26,5280
Mvaa 172,140,20
Mvaa 164,140,4
Ta 136,25,192
W 9
Mvaa 96,144,8
Ta 138,25,5312
Mvaa 104,144,24
Ta 140,26,224
Ta 142,26,5344
Mvaa 108,148,24
Mvaa 100,148,8
Ta 144,25,256
W 9
Mvaa 112,152,8
Ta 146,25,5376
Mvaa 120,152,24
Ta 148,26,288
Ta 150,26,5408
Mvaa 124,156,24
Mvaa 116,156,8
Ta 152,21,40960
W 9
Mvaa 128,128,8
Ta 154,21,46080
Mvaa 136,128,24
Ta 156,22,40992
Ta 158,22,46112
Mvaa 140,132,24
Mvaa 132,132,8
Ta 128,21,41024
W 9
Mvaa 144,136,8
Ta 130,21,46144
Mvaa 152,136,24
Ta 132,22,41056
Ta 134,22,46176
Mvaa 156,140,24
Mvaa 148,140,8
Ta 136,21,41088
W 9
Mvaa 160,144,8
Ta 138,21,46208
Mvaa 168,144,24
Ta 140,22,41120
Ta 142,22,46240
Mvaa 172,148,24
Mvaa 164,148,8
Ta 144,21,41152
W 9
Mvaa 96,152,12
Ta 146,21,46272
Mvaa 104,152,28
Ta 148,22,41184
Ta 150,22,46304
Mvaa 108,156,28
Mvaa 100,156,12
Ta 152,21,41216
W 9
Mvaa 112,128,12
Ta 154,21,46336
Mvaa 120,128,28
Ta 156,22,41248
Ta 158,22,46368
Mvaa 124,132,28
Mvaa 116,132,12
Ta 128,21,51200
W 9
Mvaa 128,136,12
Ta 130,21,56320
Mvaa 136,136,28
Ta 132,22,51232
Ta 134,22,56352
Mvaa 140,140,28
Mvaa 132,140,12
Ta 136,21,51264
W 9
Mvaa 144,144,12
Ta 138,21,56384
Mvaa 152,144,28
Ta 140,22,51296
Ta 142,22,56416
Mvaa 156,148,28
Mvaa 148,148,12
Ta 144,21,51328
W 9
Mvaa 160,152,12
Ta 146,21,56448
Mvaa 168,152,28
Ta 148,22,51360
Ta 150,22,56480
Mvaa 172,156,28
Mvaa 164,156,12
Ta 152,21,51392
s_waitcnt vmcnt(6) lgkmcnt(9)
Mvaa 96,128,32
Ta 154,21,56512
Mvaa 104,128,48
Ta 156,22,51424
Ta 158,22,56544
Mvaa 108,132,48
v_bfe_u32 v28, v1, 4, 2
Mvaa 100,132,32
v_lshlrev_b32 v28, 3, v28
W 8
Mvaa 112,136,32
Ta 128,21,51456
Ta 130,21,56576
Mvaa 120,136,48
buffer_load_dwordx2 v[28:29], v28, s[8:11], s55 offen sc1
Ta 132,22,51488
Mvaa 124,140,48
Ta 134,22,56608
Mvaa 116,140,32
Ta 136,23,10240
W 9
Mvaa 128,144,32
Ta 138,23,15360
Mvaa 136,144,48
Ta 140,24,10272
Ta 142,24,15392
Mvaa 140,148,48
Mvaa 132,148,32
Ta 144,23,10304
W 9
Mvaa 144,152,32
Ta 146,23,15424
Mvaa 152,152,48
Ta 148,24,10336
Ta 150,24,15456
Mvaa 156,156,48
Mvaa 148,156,32
Ta 152,23,10368
W 9
Mvaa 160,128,32
Ta 154,23,15488
Mvaa 168,128,48
Ta 156,24,10400
Ta 158,24,15520
Mvaa 172,132,48
Mvaa 164,132,32
Ta 128,23,10432
s_waitcnt vmcnt(5) lgkmcnt(9)
Mvaa 96,136,36
Ta 130,23,15552
Mvaa 104,136,52
Ta 132,24,10464
Ta 134,24,15584
Mvaa 108,140,52
Mvaa 100,140,36
Ta 136,23,10496
W 9
Mvaa 112,144,36
Ta 138,23,15616
Mvaa 120,144,52
Ta 140,24,10528
Ta 142,24,15648
Mvaa 124,148,52
Mvaa 116,148,36
Ta 144,23,30720
W 9
Mvaa 128,152,36
Ta 146,23,35840
Mvaa 136,152,52
Ta 148,24,30752
Ta 150,24,35872
Mvaa 140,156,52
Mvaa 132,156,36
Ta 152,23,30784
W 9
Mvaa 144,128,36
Ta 154,23,35904
Mvaa 152,128,52
Ta 156,24,30816
Ta 158,24,35936
Mvaa 156,132,52
Mvaa 148,132,36
Ta 128,23,30848
W 9
Mvaa 160,136,36
Ta 130,23,35968
Mvaa 168,136,52
Ta 132,24,30880
Ta 134,24,36000
Mvaa 172,140,52
Mvaa 164,140,36
Ta 136,23,30912
s_waitcnt vmcnt(3) lgkmcnt(9)
Mvaa 96,144,40
Ta 138,23,36032
Mvaa 104,144,56
Ta 140,24,30944
Ta 142,24,36064
Mvaa 108,148,56
Mvaa 100,148,40
Ta 144,23,30976
W 9
Mvaa 112,152,40
Ta 146,23,36096
s_waitcnt vmcnt(0)
v_cmp_eq_u64 vcc, s[12:13], v[28:29]
s_cmp_eq_u64 vcc, -1
s_cbranch_scc0 .Lspin_82%=
.Lok_83%=:
Mvaa 120,152,56
s_add_u32 s81, s58, 1
Ta 148,24,31008
Mvaa 124,156,56
Ta 150,24,36128
Mvaa 116,156,40
Ta 152,23,40960
W 9
Mvaa 128,128,40
Ta 154,23,46080
s_and_b32 s81, s81, 3
Mvaa 136,128,56
Ta 156,24,40992
Ta 158,24,46112
Mvaa 140,132,56
Mvaa 132,132,40
s_lshl_b32 s81, s81, 12
W 8
Mvaa 144,136,40
Ta 128,23,41024
Ta 130,23,46144
Mvaa 152,136,56
Ta 132,24,41056
Ta 134,24,46176
Mvaa 156,140,56
s_add_u32 s81, s81, s50
Mvaa 148,140,40
Ta 136,23,41088
W 9
Mvaa 160,144,40
Ta 138,23,46208
Mvaa 168,144,56
s_add_u32 s82, s81, 131072
Ta 140,24,41120
Mvaa 172,148,56
Ta 142,24,46240
Mvaa 164,148,40
Ta 144,23,41152
W 9
Mvaa 96,152,44
Ta 146,23,46272
buffer_load_dwordx4 a[64:67], v1, s[8:11], s81 offen offset:0 sc1
Mvaa 104,152,60
Ta 148,24,41184
Ta 150,24,46304
Mvaa 108,156,60
Mvaa 100,156,44
buffer_load_dwordx4 a[80:83], v1, s[8:11], s82 offen offset:0 sc1
W 8
Mvaa 112,128,44
Ta 152,23,41216
Ta 154,23,46336
Mvaa 120,128,60
Ta 156,24,41248
Ta 158,24,46368
Mvaa 124,132,60
buffer_load_dwordx4 a[68:71], v1, s[8:11], s81 offen offset:1024 sc1
Mvaa 116,132,44
Ta 128,21,0
W 9
Mvaa 128,136,44
Ta 130,21,5120
Mvaa 136,136,60
buffer_load_dwordx4 a[84:87], v1, s[8:11], s82 offen offset:1024 sc1
Ta 132,22,32
Mvaa 140,140,60
Ta 134,22,5152
K 96,96,97
Mvaa 132,140,44
Ta 136,21,64
W 9
Mvaa 144,144,44
Ta 138,21,5184
buffer_load_dwordx4 a[72:75], v1, s[8:11], s81 offen offset:2048 sc1
Mvaa 152,144,60
K 97,98,99
Ta 140,22,96
Mvaa 156,148,60
Ta 142,22,5216
Mvaa 148,148,44
K 98,100,101
W 8
Mvaa 160,152,44
buffer_load_dwordx4 a[88:91], v1, s[8:11], s82 offen offset:2048 sc1
Ta 144,21,128
Mvaa 168,152,60
Ta 146,21,5248
Ta 148,22,160
Mvaa 172,156,60
Ta 150,22,5280
K 99,102,103
Mvaa 164,156,44
buffer_load_dwordx4 a[76:79], v1, s[8:11], s81 offen offset:3072 sc1
W 8
Zvav 176,128,32
Ta 152,21,192
Ta 154,21,5312
Zvav 184,128,64
s_add_u32 s83, s52, 0
Ta 156,22,224
Zvav 188,132,64
buffer_load_dwordx4 a[92:95], v1, s[8:11], s82 offen offset:3072 sc1
Ta 158,22,5344
Zvav 180,132,32
Ta 128,21,256
W 9
Zvav 192,136,32
Ta 130,21,5376
s_add_u32 s81, s58, 2
Zvav 200,136,64
buffer_store_dwordx4 v[96:99], v1, s[8:11], s83 offen sc1
K 104,104,105
Zvav 204,140,64
Ta 132,22,288
Ta 134,22,5408
Zvav 196,140,32
K 105,106,107
W 8
Zvav 208,144,32
Ta 136,21,10240
Ta 138,21,15360
Zvav 216,144,64
s_and_b32 s81, s81, 3
K 106,108,109
Zvav 220,148,64
Ta 140,22,10272
Ta 142,22,15392
Zvav 212,148,32
s_lshl_b32 s81, s81, 12
W 8
Zvav 224,152,32
Ta 144,21,10304
Ta 146,21,15424
Zvav 232,152,64
K 107,110,111
Ta 148,22,10336
Zvav 236,156,64
Ta 150,22,15456
s_add_u32 s81, s81, s50
Zvav 228,156,32
s_add_u32 s83, s52, 40960
W 8
Zvav 240,128,32
Ta 152,21,10368
Ta 154,21,15488
Zvav 248,128,64
buffer_store_dwordx4 v[104:107], v1, s[8:11], s83 offen sc1
s_add_u32 s82, s81, 131072
Zvav 252,132,64
Ta 156,22,10400
Ta 158,22,15520
Zvav 244,132,32
K 112,112,113
W 8
Mvav 176,136,40
Ta 128,21,10432
Ta 130,21,15552
Mvav 184,136,72
buffer_load_dwordx4 a[96:99], v1, s[8:11], s81 offen offset:0 sc1
K 113,114,115
Mvav 188,140,72
Ta 132,22,10464
Ta 134,22,15584
Mvav 180,140,40
K 114,116,117
W 8
Mvav 192,144,40
buffer_load_dwordx4 a[112:115], v1, s[8:11], s82 offen offset:0 sc1
Ta 136,21,10496
Mvav 200,144,72
Ta 138,21,15616
Ta 140,22,10528
Mvav 204,148,72
Ta 142,22,15648
K 115,118,119
Mvav 196,148,40
buffer_load_dwordx4 a[100:103], v1, s[8:11], s81 offen offset:1024 sc1
W 8
Mvav 208,152,40
Ta 144,21,20480
Ta 146,21,25600
Mvav 216,152,72
s_add_u32 s83, s52, 1024
Ta 148,22,20512
Mvav 220,156,72
buffer_load_dwordx4 a[116:119], v1, s[8:11], s82 offen offset:1024 sc1
Ta 150,22,25632
Mvav 212,156,40
Ta 152,21,20544
W 9
Mvav 224,128,40
Ta 154,21,25664
)"
R"(Mvav 232,128,72
buffer_store_dwordx4 v[112:115], v1, s[8:11], s83 offen sc1
K 120,120,121
Mvav 236,132,72
Ta 156,22,20576
Ta 158,22,25696
Mvav 228,132,40
K 121,122,123
W 8
Mvav 240,136,40
buffer_load_dwordx4 a[104:107], v1, s[8:11], s81 offen offset:2048 sc1
Ta 128,21,20608
Mvav 248,136,72
Ta 130,21,25728
K 122,124,125
Mvav 252,140,72
Ta 132,22,20640
Ta 134,22,25760
Mvav 244,140,40
buffer_load_dwordx4 a[120:123], v1, s[8:11], s82 offen offset:2048 sc1
W 8
Mvav 176,144,48
Ta 136,21,20672
Ta 138,21,25792
Mvav 184,144,80
K 123,126,127
Ta 140,22,20704
Mvav 188,148,80
buffer_load_dwordx4 a[108:111], v1, s[8:11], s81 offen offset:3072 sc1
Ta 142,22,25824
Mvav 180,148,48
s_add_u32 s83, s52, 41984
W 8
Mvav 192,152,48
Ta 144,21,20736
Ta 146,21,25856
Mvav 200,152,80
buffer_load_dwordx4 a[124:127], v1, s[8:11], s82 offen offset:3072 sc1
s_add_u32 s81, s58, 3
Mvav 204,156,80
Ta 148,22,20768
Ta 150,22,25888
Mvav 196,156,48
Ta 152,25,10240
W 9
Mvav 208,128,48
buffer_store_dwordx4 v[120:123], v1, s[8:11], s83 offen sc1
K 128,128,129
Mvav 216,128,80
Ta 154,25,15360
s_and_b32 s81, s81, 3
Mvav 220,132,80
K 129,130,131
Ta 156,26,10272
Mvav 212,132,48
Ta 158,26,15392
W 8
Mvav 224,136,48
s_lshl_b32 s81, s81, 12
K 130,132,133
Mvav 232,136,80
Ta 128,25,10304
Ta 130,25,15424
Mvav 236,140,80
Ta 132,26,10336
Ta 134,26,15456
Mvav 228,140,48
K 131,134,135
W 8
Mvav 240,144,48
s_add_u32 s81, s81, s50
Ta 136,25,10368
Mvav 248,144,80
Ta 138,25,15488
s_add_u32 s83, s52, 2048
Mvav 252,148,80
s_add_u32 s82, s81, 131072
Ta 140,26,10400
Mvav 244,148,48
Ta 142,26,15520
W 8
Mvav 176,152,56
buffer_store_dwordx4 v[128:131], v1, s[8:11], s83 offen sc1
Ta 144,25,10432
Mvav 184,152,88
Ta 146,25,15552
K 136,136,137
Mvav 188,156,88
Ta 148,26,10464
Ta 150,26,15584
Mvav 180,156,56
buffer_load_dwordx4 a[184:187], v1, s[8:11], s81 offen offset:0 sc1
W 8
Mvav 192,128,56
K 137,138,139
Ta 152,25,10496
Mvav 200,128,88
Ta 154,25,15616
K 138,140,141
Mvav 204,132,88
buffer_load_dwordx4 a[200:203], v1, s[8:11], s82 offen offset:0 sc1
Ta 156,26,10528
Mvav 196,132,56
Ta 158,26,15648
W 8
Mvav 208,136,56
Ta 128,25,20480
Ta 130,25,25600
Mvav 216,136,88
buffer_load_dwordx4 a[188:191], v1, s[8:11], s81 offen offset:1024 sc1
K 139,142,143
Mvav 220,140,88
Ta 132,26,20512
Ta 134,26,25632
Mvav 212,140,56
s_add_u32 s83, s52, 43008
W 8
Mvav 224,144,56
buffer_load_dwordx4 a[204:207], v1, s[8:11], s82 offen offset:1024 sc1
Ta 136,25,20544
Mvav 232,144,88
Ta 138,25,25664
Ta 140,26,20576
Mvav 236,148,88
Ta 142,26,25696
Mvav 228,148,56
buffer_store_dwordx4 v[136:139], v1, s[8:11], s83 offen sc1
W 8
Mvav 240,152,56
K 144,144,145
Ta 144,25,20608
Mvav 248,152,88
Ta 146,25,25728
K 145,146,147
Mvav 252,156,88
buffer_load_dwordx4 a[192:195], v1, s[8:11], s81 offen offset:2048 sc1
Ta 148,26,20640
Mvav 244,156,56
Ta 150,26,25760
s_waitcnt vmcnt(25) lgkmcnt(8)
Mvaa 176,128,64
K 146,148,149
Ta 152,25,20672
Mvaa 184,128,80
buffer_load_dwordx4 a[208:211], v1, s[8:11], s82 offen offset:2048 sc1
Ta 154,25,25792
Mvaa 188,132,80
v_and_b32 v27, 1, v12
Ta 156,26,20704
Mvaa 180,132,64
Ta 158,26,25824
W 8
Mvaa 192,136,64
buffer_load_dwordx4 a[196:199], v1, s[8:11], s81 offen offset:3072 sc1
K 147,150,151
Mvaa 200,136,80
v_lshlrev_b32 v27, 1, v27
Ta 128,25,20736
Mvaa 204,140,80
Ta 130,25,25856
s_add_u32 s83, s52, 3072
Mvaa 196,140,64
buffer_load_dwordx4 a[212:215], v1, s[8:11], s82 offen offset:3072 sc1
W 6
Mvaa 208,144,64
Ta 132,26,20768
Ta 134,26,25888
Mvaa 216,144,80
v_xor_b32 v27, v27, v13
Ta 136,25,30720
Mvaa 220,148,80
buffer_store_dwordx4 v[144:147], v1, s[8:11], s83 offen sc1
Ta 138,25,35840
Mvaa 212,148,64
K 152,152,153
W 6
Mvaa 224,152,64
v_and_b32 v12, 2, v12
Ta 140,26,30752
Mvaa 232,152,80
Ta 142,26,35872
K 153,154,155
Mvaa 236,156,80
v_or_b32 v12, v12, v14
Ta 144,25,30784
Mvaa 228,156,64
Ta 146,25,35904
W 6
Mvaa 240,128,64
K 154,156,157
Ta 148,26,30816
Mvaa 248,128,80
Ta 150,26,35936
v_lshl_or_b32 v12, v27, 2, v12
Mvaa 252,132,80
Ta 152,25,30848
Ta 154,25,35968
Mvaa 244,132,64
K 155,158,159
s_waitcnt vmcnt(27) lgkmcnt(6)
Mvaa 176,136,68
v_cmp_eq_u32_e64 s[86:87], 0, v12
Ta 156,26,30880
Mvaa 184,136,84
Ta 158,26,36000
s_add_u32 s83, s52, 44032
Mvaa 188,140,84
Ta 128,25,30912
Ta 130,25,36032
Mvaa 180,140,68
v_cndmask_b32_e64 v4, 0, v15, s[86:87]
W 6
Mvaa 192,144,68
buffer_store_dwordx4 v[152:155], v1, s[8:11], s83 offen sc1
Ta 132,26,30944
Mvaa 200,144,84
Ta 134,26,36064
v_cmp_eq_u32_e64 s[86:87], 1, v12
Mvaa 204,148,84
K 160,160,161
Ta 136,25,30976
Mvaa 196,148,68
Ta 138,25,36096
W 6
Mvaa 208,152,68
K 161,162,163
v_cndmask_b32_e64 v5, 0, v15, s[86:87]
Mvaa 216,152,84
Ta 140,26,31008
Ta 142,26,36128
Mvaa 220,156,84
K 162,164,165
Ta 144,21,30720
Mvaa 212,156,68
Ta 146,21,35840
W 6
Mvaa 224,128,68
v_cmp_eq_u32_e64 s[86:87], 2, v12
Ta 148,22,30752
Mvaa 232,128,84
Ta 150,22,35872
K 163,166,167
Mvaa 236,132,84
v_cndmask_b32_e64 v6, 0, v15, s[86:87]
Ta 152,21,30784
Mvaa 228,132,68
Ta 154,21,35904
W 6
Mvaa 240,136,68
s_add_u32 s83, s52, 4096
v_cmp_eq_u32_e64 s[86:87], 3, v12
Mvaa 248,136,84
Ta 156,22,30816
Ta 158,22,35936
Mvaa 252,140,84
buffer_store_dwordx4 v[160:163], v1, s[8:11], s83 offen sc1
Ta 128,21,30848
Mvaa 244,140,68
Ta 130,21,35968
s_waitcnt vmcnt(27) lgkmcnt(6)
Mvaa 176,144,72
v_cndmask_b32_e64 v7, 0, v15, s[86:87]
K 168,168,169
Mvaa 184,144,88
Ta 132,22,30880
Ta 134,22,36000
Mvaa 188,148,88
v_cmp_eq_u32_e64 s[86:87], 4, v12
K 169,170,171
Mvaa 180,148,72
Ta 136,21,30912
W 5
Mvaa 192,152,72
Ta 138,21,36032
K 170,172,173
Mvaa 200,152,88
v_cndmask_b32_e64 v8, 0, v15, s[86:87]
Ta 140,22,30944
Mvaa 204,156,88
Ta 142,22,36064
Ta 144,21,30976
Mvaa 196,156,72
Ta 146,21,36096
W 6
Mvaa 208,128,72
K 171,174,175
v_cmp_eq_u32_e64 s[86:87], 5, v12
Mvaa 216,128,88
Ta 148,22,31008
Ta 150,22,36128
Mvaa 220,132,88
s_add_u32 s83, s52, 45056
v_cndmask_b32_e64 v9, 0, v15, s[86:87]
Mvaa 212,132,72
Ta 152,23,0
W 5
Mvaa 224,136,72
Ta 154,23,5120
buffer_store_dwordx4 v[168:171], v1, s[8:11], s83 offen sc1
Mvaa 232,136,88
Ta 156,24,32
Ta 158,24,5152
Mvaa 236,140,88
v_cmp_eq_u32_e64 s[86:87], 6, v12
Ta 128,23,64
Mvaa 228,140,72
Ta 130,23,5184
W 6
Mvaa 240,144,72
v_cndmask_b32_e64 v10, 0, v15, s[86:87]
Ta 132,24,96
Mvaa 248,144,88
Ta 134,24,5216
v_cmp_eq_u32_e64 s[86:87], 7, v12
Mvaa 252,148,88
Ta 136,23,128
Ta 138,23,5248
Mvaa 244,148,72
Ta 140,24,160
s_waitcnt vmcnt(26) lgkmcnt(7)
Mvaa 176,152,76
Ta 142,24,5280
v_cndmask_b32_e64 v11, 0, v15, s[86:87]
Mvaa 184,152,92
Ta 144,23,192
Ta 146,23,5312
Mvaa 188,156,92
Ta 148,24,224
Ta 150,24,5344
Mvaa 180,156,76
Ta 152,23,256
W 9
Mvaa 192,128,76
Ta 154,23,5376
Mvaa 200,128,92
Ta 156,24,288
Ta 158,24,5408
Mvaa 204,132,92
Mvaa 196,132,76
Ta 128,23,20480
W 9
Mvaa 208,136,76
Ta 130,23,25600
Mvaa 216,136,92
Ta 132,24,20512
Ta 134,24,25632
Mvaa 220,140,92
Mvaa 212,140,76
Ta 136,23,51200
W 9
Mvaa 224,144,76
Ta 138,23,56320
Mvaa 232,144,92
Ta 140,24,51232
Ta 142,24,56352
Mvaa 236,148,92
Mvaa 228,148,76
Ta 144,25,0
W 9
Mvaa 240,152,76
Ta 146,25,5120
Mvaa 248,152,92
Ta 148,26,32
Ta 150,26,5152
Mvaa 252,156,92
Mvaa 244,156,76
Ta 152,21,40960
s_waitcnt vmcnt(22) lgkmcnt(9)
Mvaa 176,128,96
Ta 154,21,46080
Mvaa 184,128,112
Ta 156,22,40992
Ta 158,22,46112
Mvaa 188,132,112
Mvaa 180,132,96
Ta 128,21,51200
s_waitcnt vmcnt(20) lgkmcnt(9)
Mvaa 176,136,100
Ta 130,21,56320
Mvaa 184,136,116
Ta 132,22,51232
Ta 134,22,56352
Mvaa 188,140,116
Mvaa 180,140,100
Ta 136,23,10240
s_waitcnt vmcnt(17) lgkmcnt(9)
Mvaa 176,144,104
Ta 138,23,15360
Mvaa 184,144,120
Ta 140,24,10272
Ta 142,24,15392
Mvaa 188,148,120
Mvaa 180,148,104
Ta 144,23,30720
s_waitcnt vmcnt(15) lgkmcnt(9)
Mvaa 176,152,108
Ta 146,23,35840
Mvaa 184,152,124
Ta 148,24,30752
Ta 150,24,35872
)"
R"(Mvaa 188,156,124
Mvaa 180,156,108
Ta 152,23,40960
s_waitcnt vmcnt(11) lgkmcnt(9)
Mvaa 176,128,184
Ta 154,23,46080
Mvaa 184,128,200
Ta 156,24,40992
Ta 158,24,46112
Mvaa 188,132,200
Mvaa 180,132,184
Ta 128,23,20544
s_waitcnt vmcnt(9) lgkmcnt(9)
Mvaa 176,136,188
Ta 130,23,25664
Mvaa 184,136,204
Ta 132,24,20576
Ta 134,24,25696
Mvaa 188,140,204
Mvaa 180,140,188
Ta 136,23,51264
s_waitcnt vmcnt(6) lgkmcnt(9)
Mvaa 176,144,192
Ta 138,23,56384
Mvaa 184,144,208
Ta 140,24,51296
Ta 142,24,56416
Mvaa 188,148,208
Mvaa 180,148,192
Ta 144,25,64
s_waitcnt vmcnt(4) lgkmcnt(9)
Mvaa 176,152,196
Ta 146,25,5184
Mvaa 184,152,212
Ta 148,26,96
Ta 150,26,5216
Mvaa 188,156,212
s_waitcnt vmcnt(0)
v_lshlrev_b32 v2, 2, v1
v_mov_b64 v[28:29], s[12:13]
s_mov_b64 exec, 3
buffer_store_dwordx2 v[28:29], v2, s[8:11], s53 offen offset:0 sc1
s_mov_b64 exec, -1
Mvaa 180,156,196
Ta 152,21,41024
W 9
Mvaa 192,128,96
Ta 154,21,46144
Mvaa 200,128,112
Ta 156,22,41056
Ta 158,22,46176
Mvaa 204,132,112
K 176,176,177
Mvaa 196,132,96
Ta 128,21,51264
W 9
Mvaa 192,136,100
Ta 130,21,56384
K 177,178,179
Mvaa 200,136,116
K 178,180,181
Ta 132,22,51296
Mvaa 204,140,116
Ta 134,22,56416
K 179,182,183
Mvaa 196,140,100
Ta 136,23,10304
W 9
Mvaa 192,144,104
Ta 138,23,15424
s_add_u32 s83, s52, 327680
Mvaa 200,144,120
buffer_store_dwordx4 v[176:179], v1, s[8:11], s83 offen sc1
K 184,184,185
Mvaa 204,148,120
K 185,186,187
K 186,188,189
Mvaa 196,148,104
K 187,190,191
W 6
Mvaa 192,152,108
s_add_u32 s83, s52, 368640
buffer_store_dwordx4 v[184:187], v1, s[8:11], s83 offen sc1
Mvaa 200,152,124
Ta 140,24,10336
Ta 142,24,15456
Mvaa 204,156,124
Ta 144,23,30784
Ta 146,23,35904
Mvaa 196,156,108
Ta 148,24,30816
W 7
Mvaa 192,128,184
Ta 150,24,35936
Ta 152,23,41024
Mvaa 200,128,200
Ta 154,23,46144
Ta 156,24,41056
Mvaa 204,132,200
Ta 158,24,46176
Mvaa 196,132,184
Ta 128,23,20608
W 9
Mvaa 192,136,188
Ta 130,23,25728
Mvaa 200,136,204
Ta 132,24,20640
Ta 134,24,25760
Mvaa 204,140,204
Mvaa 196,140,188
Ta 136,23,51328
W 9
Mvaa 192,144,192
Ta 138,23,56448
Mvaa 200,144,208
Ta 140,24,51360
Ta 142,24,56480
Mvaa 204,148,208
Mvaa 196,148,192
Ta 144,25,128
W 9
Mvaa 192,152,196
Ta 146,25,5248
Mvaa 200,152,212
Ta 148,26,160
Ta 150,26,5280
Mvaa 204,156,212
Mvaa 196,156,196
Ta 152,21,41088
W 9
Mvaa 208,128,96
Ta 154,21,46208
Mvaa 216,128,112
Ta 156,22,41120
Ta 158,22,46240
Mvaa 220,132,112
K 192,192,193
Mvaa 212,132,96
Ta 128,21,51328
W 9
Mvaa 208,136,100
Ta 130,21,56448
K 193,194,195
Mvaa 216,136,116
K 194,196,197
Ta 132,22,51360
Mvaa 220,140,116
Ta 134,22,56480
K 195,198,199
Mvaa 212,140,100
Ta 136,23,10368
W 9
Mvaa 208,144,104
Ta 138,23,15488
s_add_u32 s83, s52, 328704
Mvaa 216,144,120
buffer_store_dwordx4 v[192:195], v1, s[8:11], s83 offen sc1
K 200,200,201
Mvaa 220,148,120
K 201,202,203
K 202,204,205
Mvaa 212,148,104
K 203,206,207
W 6
Mvaa 208,152,108
s_add_u32 s83, s52, 369664
buffer_store_dwordx4 v[200:203], v1, s[8:11], s83 offen sc1
Mvaa 216,152,124
Ta 140,24,10400
Ta 142,24,15520
Mvaa 220,156,124
Ta 144,23,30848
Ta 146,23,35968
Mvaa 212,156,108
Ta 148,24,30880
W 7
Mvaa 208,128,184
Ta 150,24,36000
Ta 152,23,41088
Mvaa 216,128,200
Ta 154,23,46208
Ta 156,24,41120
Mvaa 220,132,200
Ta 158,24,46240
Mvaa 212,132,184
Ta 128,23,20672
W 9
Mvaa 208,136,188
Ta 130,23,25792
Mvaa 216,136,204
Ta 132,24,20704
Ta 134,24,25824
Mvaa 220,140,204
Mvaa 212,140,188
Ta 136,23,51392
W 9
Mvaa 208,144,192
Ta 138,23,56512
Mvaa 216,144,208
Ta 140,24,51424
Ta 142,24,56544
Mvaa 220,148,208
Mvaa 212,148,192
Ta 144,25,192
W 9
Mvaa 208,152,196
Ta 146,25,5312
Mvaa 216,152,212
Ta 148,26,224
Ta 150,26,5344
Mvaa 220,156,212
Mvaa 212,156,196
Ta 152,21,41152
W 9
Mvaa 224,128,96
Ta 154,21,46272
Mvaa 232,128,112
Ta 156,22,41184
Ta 158,22,46304
Mvaa 236,132,112
K 208,208,209
Mvaa 228,132,96
Ta 128,21,51392
W 9
Mvaa 224,136,100
Ta 130,21,56512
K 209,210,211
Mvaa 232,136,116
K 210,212,213
Ta 132,22,51424
Mvaa 236,140,116
Ta 134,22,56544
K 211,214,215
Mvaa 228,140,100
Ta 136,23,10432
W 9
Mvaa 224,144,104
Ta 138,23,15552
s_add_u32 s83, s52, 329728
Mvaa 232,144,120
buffer_store_dwordx4 v[208:211], v1, s[8:11], s83 offen sc1
K 216,216,217
Mvaa 236,148,120
K 217,218,219
K 218,220,221
Mvaa 228,148,104
K 219,222,223
W 6
Mvaa 224,152,108
s_add_u32 s83, s52, 370688
buffer_store_dwordx4 v[216:219], v1, s[8:11], s83 offen sc1
Mvaa 232,152,124
Ta 140,24,10464
Ta 142,24,15584
Mvaa 236,156,124
Ta 144,23,30912
Ta 146,23,36032
Mvaa 228,156,108
Ta 148,24,30944
W 7
Mvaa 224,128,184
Ta 150,24,36064
Ta 152,23,41152
Mvaa 232,128,200
Ta 154,23,46272
Ta 156,24,41184
Mvaa 236,132,200
Ta 158,24,46304
Mvaa 228,132,184
Ta 128,23,20736
W 9
Mvaa 224,136,188
Ta 130,23,25856
Mvaa 232,136,204
Ta 132,24,20768
Ta 134,24,25888
Mvaa 236,140,204
Mvaa 228,140,188
Ta 136,23,51456
W 9
Mvaa 224,144,192
Ta 138,23,56576
Mvaa 232,144,208
Ta 140,24,51488
Ta 142,24,56608
Mvaa 236,148,208
Mvaa 228,148,192
Ta 144,25,256
W 9
Mvaa 224,152,196
Ta 146,25,5376
Mvaa 232,152,212
Ta 148,26,288
Ta 150,26,5408
Mvaa 236,156,212
Mvaa 228,156,196
Ta 152,21,41216
W 9
Mvaa 240,128,96
Ta 154,21,46336
Mvaa 248,128,112
Ta 156,22,41248
Ta 158,22,46368
Mvaa 252,132,112
K 224,224,225
Mvaa 244,132,96
Ta 128,21,51456
W 9
Mvaa 240,136,100
Ta 130,21,56576
K 225,226,227
Mvaa 248,136,116
K 226,228,229
Ta 132,22,51488
Mvaa 252,140,116
Ta 134,22,56608
K 227,230,231
Mvaa 244,140,100
Ta 136,23,10496
W 9
Mvaa 240,144,104
Ta 138,23,15616
s_add_u32 s83, s52, 330752
Mvaa 248,144,120
buffer_store_dwordx4 v[224:227], v1, s[8:11], s83 offen sc1
K 232,232,233
Mvaa 252,148,120
K 233,234,235
K 234,236,237
Mvaa 244,148,104
K 235,238,239
W 6
Mvaa 240,152,108
s_add_u32 s83, s52, 371712
buffer_store_dwordx4 v[232:235], v1, s[8:11], s83 offen sc1
Mvaa 248,152,124
Ta 140,24,10528
Ta 142,24,15648
Mvaa 252,156,124
Ta 144,23,30976
Ta 146,23,36096
Mvaa 244,156,108
Ta 148,24,31008
W 7
Mvaa 240,128,184
Ta 150,24,36128
Ta 152,23,41216
Mvaa 248,128,200
Ta 154,23,46336
Ta 156,24,41248
Mvaa 252,132,200
Ta 158,24,46368
Mvaa 244,132,184
W 8
Mvaa 240,136,188
Mvaa 248,136,204
Mvaa 252,140,204
Mvaa 244,140,188
W 4
Mvaa 240,144,192
Mvaa 248,144,208
Mvaa 252,148,208
Mvaa 244,148,192
W 0
Mvaa 240,152,196
Mvaa 248,152,212
Mvaa 252,156,212
Mvaa 244,156,196
s_nop 4
K 240,240,241
K 241,242,243
s_nop 0
K 242,244,245
K 243,246,247
s_add_u32 s83, s52, 331776
buffer_store_dwordx4 v[240:243], v1, s[8:11], s83 offen sc1
K 248,248,249
K 249,250,251
K 250,252,253
K 251,254,255
s_add_u32 s83, s52, 372736
buffer_store_dwordx4 v[248:251], v1, s[8:11], s83 offen sc1
s_waitcnt vmcnt(0)
v_lshlrev_b32 v2, 2, v1
v_mov_b64 v[28:29], s[12:13]
s_mov_b64 exec, 3
buffer_store_dwordx2 v[28:29], v2, s[8:11], s53 offen offset:512 sc1
s_mov_b64 exec, -1
s_branch .Ltend_79%=
.Llate_78%=:
ds_read_b32 v11, v11 offset:59392
ds_read_b64 v[8:9], v10 offset:57344
Rv 176,3,0
Rv 180,3,1024
Rv 184,3,2048
Rv 188,3,3072
Rv 192,3,4096
Rv 196,3,5120
Rv 200,3,6144
Rv 204,3,7168
)"
R"(W 6
Mvvv 32,208,176
v_cmp_ne_u32 vcc, 0, v11
s_cmp_lg_u64 vcc, 0
s_cbranch_scc1 .Lredo%=
Mvvv 36,212,176
s_add_u32 s94, s94, 81920
A 4,4,8
Mvvv 40,208,180
v_rcp_f32 v4, v4
Mvvv 44,212,180
s_mul_i32 s95, s92, 30720
W 4
Mvvv 48,208,184
A 5,5,9
Ta 128,21,0
Mvvv 52,212,184
Ta 130,21,5120
s_add_u32 s95, s95, s71
Mvvv 56,208,188
v_rcp_f32 v5, v5
Mvvv 60,212,188
s_lshl_b32 s93, s58, 12
W 4
Mvvv 64,208,192
s_add_u32 s93, s93, s50
Ta 132,22,32
Mvvv 68,212,192
Ta 134,22,5152
U 32,32,4
Mvvv 72,208,196
s_add_u32 m0, s95, 30720
U 33,33,4
Mvvv 76,212,196
U 34,34,4
W 4
Mvvv 80,208,200
Ta 136,21,64
Ta 138,21,5184
Mvvv 84,212,200
U 35,35,4
D 16,40,94,0
Mvvv 88,208,204
U 36,36,4
U 37,37,4
U 38,38,4
U 39,39,4
K 32,32,33
K 33,34,35
K 34,36,37
K 35,38,39
buffer_store_dwordx4 v[32:35], v1, s[8:11], s93 offen offset:0 sc1
U 64,64,5
U 65,65,5
U 66,66,5
U 67,67,5
U 68,68,5
U 69,69,5
U 70,70,5
U 71,71,5
K 64,64,65
K 65,66,67
K 66,68,69
K 67,70,71
s_add_u32 s67, s93, 131072
Mvvv 92,212,204
Ta 140,22,96
Ta 142,22,5216
W 4
Zvav 176,128,32
Ta 144,21,128
Ta 146,21,5248
Zvav 184,128,64
D 17,40,94,1024
Ta 148,22,160
Zvav 188,132,64
Ta 150,22,5280
Ta 152,21,192
Zvav 180,132,32
Ta 154,21,5312
W 6
Zvav 192,136,32
D 18,40,94,2048
Ta 156,22,224
Zvav 200,136,64
Ta 158,22,5344
Ta 128,21,256
Zvav 204,140,64
Ta 130,21,5376
Ta 132,22,288
Zvav 196,140,32
D 19,40,94,3072
W 7
Zvav 208,144,32
s_add_u32 m0, s95, 34816
Ta 134,22,5408
Zvav 216,144,64
Ta 136,21,10240
Ta 138,21,15360
Zvav 220,148,64
buffer_load_dwordx4 v20, s[40:43], s94 offen lds
s_add_u32 s94, s58, 2
Zvav 212,148,32
s_and_b32 s94, s94, 3
W 6
Zvav 224,152,32
s_mul_i32 s94, s94, 163840
s_add_u32 s94, s94, s48
Zvav 232,152,64
buffer_store_dwordx4 v[64:67], v1, s[8:11], s67 offen offset:0 sc1
U 40,40,4
Zvav 236,156,64
U 41,41,4
U 42,42,4
Zvav 228,156,32
Ta 140,22,10272
Ta 142,22,15392
W 4
Zvav 240,128,32
U 43,43,4
U 44,44,4
Zvav 248,128,64
U 45,45,4
U 46,46,4
Zvav 252,132,64
U 47,47,4
K 40,40,41
K 41,42,43
K 42,44,45
K 43,46,47
buffer_store_dwordx4 v[40:43], v1, s[8:11], s93 offen offset:1024 sc1
U 72,72,5
U 73,73,5
U 74,74,5
U 75,75,5
U 76,76,5
U 77,77,5
U 78,78,5
U 79,79,5
K 72,72,73
K 73,74,75
K 74,76,77
K 75,78,79
Zvav 244,132,32
Ta 144,21,10304
Ta 146,21,15424
Ta 148,22,10336
Ta 150,22,15456
W 4
Mvav 176,136,40
s_mul_i32 s95, s92, 30720
s_add_u32 s95, s95, s71
Mvav 184,136,72
s_add_u32 m0, s95, 81920
Ta 152,21,10368
Mvav 188,140,72
Ta 154,21,15488
D 16,40,94,0
Mvav 180,140,40
Ta 156,22,10400
Ta 158,22,15520
W 4
Mvav 192,144,40
Ta 128,21,10432
Ta 130,21,15552
Mvav 200,144,72
D 17,40,94,1024
Ta 132,22,10464
Mvav 204,148,72
Ta 134,22,15584
Ta 136,21,10496
Mvav 196,148,40
Ta 138,21,15616
W 6
Mvav 208,152,40
D 18,40,94,2048
Ta 140,22,10528
Mvav 216,152,72
Ta 142,22,15648
Ta 144,21,20480
Mvav 220,156,72
Ta 146,21,25600
Ta 148,22,20512
Mvav 212,156,40
buffer_store_dwordx4 v[72:75], v1, s[8:11], s67 offen offset:1024 sc1
W 7
Mvav 224,128,40
U 48,48,4
U 49,49,4
Mvav 232,128,72
U 50,50,4
U 51,51,4
Mvav 236,132,72
D 19,40,94,3072
s_add_u32 m0, s95, 86016
Mvav 228,132,40
Ta 150,22,25632
W 4
Mvav 240,136,40
U 52,52,4
U 53,53,4
Mvav 248,136,72
buffer_load_dwordx4 v20, s[40:43], s94 offen lds
s_waitcnt vmcnt(14)
s_cmp_lg_u32 s70, 0
s_cbranch_scc1 .Lpd_85%=
.Lpdr_86%=:
Mvav 252,140,72
U 54,54,4
U 55,55,4
K 48,48,49
K 49,50,51
K 50,52,53
K 51,54,55
buffer_store_dwordx4 v[48:51], v1, s[8:11], s93 offen offset:2048 sc1
U 80,80,5
U 81,81,5
U 82,82,5
U 83,83,5
U 84,84,5
U 85,85,5
U 86,86,5
U 87,87,5
K 80,80,81
K 81,82,83
K 82,84,85
K 83,86,87
Mvav 244,140,40
Ta 152,21,20544
Ta 154,21,25664
Ta 156,22,20576
Ta 158,22,25696
W 4
Mvav 176,144,48
Ta 128,21,20608
Ta 130,21,25728
Mvav 184,144,80
Ta 132,22,20640
Ta 134,22,25760
Mvav 188,148,80
buffer_store_dwordx4 v[80:83], v1, s[8:11], s67 offen offset:2048 sc1
U 56,56,4
Mvav 180,148,48
U 57,57,4
W 4
Mvav 192,152,48
Ta 136,21,20672
Ta 138,21,25792
Mvav 200,152,80
U 58,58,4
U 59,59,4
Mvav 204,156,80
v_bfe_u32 v28, v1, 4, 2
U 60,60,4
Mvav 196,156,48
Ta 140,22,20704
Ta 142,22,25824
W 4
Mvav 208,128,48
v_lshlrev_b32 v28, 3, v28
U 61,61,4
Mvav 216,128,80
buffer_load_dwordx2 v[28:29], v28, s[8:11], s54 offen sc1
U 62,62,4
Mvav 220,132,80
U 63,63,4
K 56,56,57
Mvav 212,132,48
Ta 144,21,20736
Ta 146,21,25856
Ta 148,22,20768
Ta 150,22,25888
W 4
Mvav 224,136,48
K 57,58,59
K 58,60,61
Mvav 232,136,80
K 59,62,63
buffer_store_dwordx4 v[56:59], v1, s[8:11], s93 offen offset:3072 sc1
Mvav 236,140,80
U 88,88,5
U 89,89,5
Mvav 228,140,48
Ta 152,25,10240
Ta 154,25,15360
Ta 156,26,10272
Ta 158,26,15392
W 4
Mvav 240,144,48
U 90,90,5
U 91,91,5
Mvav 248,144,80
U 92,92,5
U 93,93,5
Mvav 252,148,80
U 94,94,5
U 95,95,5
K 88,88,89
K 89,90,91
K 90,92,93
K 91,94,95
Mvav 244,148,48
Ta 128,25,10304
Ta 130,25,15424
Ta 132,26,10336
Ta 134,26,15456
W 4
Mvav 176,152,56
buffer_store_dwordx4 v[88:91], v1, s[8:11], s67 offen offset:3072 sc1
Ta 136,25,10368
Mvav 184,152,88
Ta 138,25,15488
Ta 140,26,10400
Mvav 188,156,88
Ta 142,26,15520
Ta 144,25,10432
Mvav 180,156,56
Ta 146,25,15552
W 6
Mvav 192,128,56
Ta 148,26,10464
Ta 150,26,15584
Mvav 200,128,88
Ta 152,25,10496
Ta 154,25,15616
Mvav 204,132,88
Ta 156,26,10528
Ta 158,26,15648
Mvav 196,132,56
Ta 128,21,0
W 9
Mvav 208,136,56
Ta 130,21,5120
Mvav 216,136,88
Ta 132,22,32
Ta 134,22,5152
Mvav 220,140,88
Mvav 212,140,56
Ta 136,21,64
W 9
Mvav 224,144,56
Ta 138,21,5184
s_waitcnt vmcnt(2)
v_cmp_eq_u64 vcc, s[12:13], v[28:29]
s_cmp_eq_u64 vcc, -1
s_cbranch_scc0 .Lspin_87%=
.Lok_88%=:
Mvav 232,144,88
s_add_u32 s81, s58, 0
s_and_b32 s81, s81, 3
Mvav 236,148,88
Ta 140,22,96
Ta 142,22,5216
Mvav 228,148,56
s_lshl_b32 s81, s81, 12
W 8
Mvav 240,152,56
s_add_u32 s81, s81, s49
Ta 144,21,128
Mvav 248,152,88
Ta 146,21,5248
s_add_u32 s82, s81, 131072
Mvav 252,156,88
buffer_load_dwordx4 a[184:187], v1, s[8:11], s81 offen offset:0 sc1
buffer_load_dwordx4 a[200:203], v1, s[8:11], s82 offen offset:0 sc1
Mvav 244,156,56
Ta 148,22,160
s_waitcnt vmcnt(0) lgkmcnt(7)
Zvaa 96,128,184
Ta 150,22,5280
Ta 152,21,192
Zvaa 104,128,200
buffer_load_dwordx4 a[188:191], v1, s[8:11], s81 offen offset:1024 sc1
Ta 154,21,5312
Zvaa 108,132,200
Ta 156,22,224
Ta 158,22,5344
Zvaa 100,132,184
Ta 128,21,256
W 9
Zvaa 112,136,184
buffer_load_dwordx4 a[204:207], v1, s[8:11], s82 offen offset:1024 sc1
Ta 130,21,5376
Zvaa 120,136,200
Ta 132,22,288
Ta 134,22,5408
Zvaa 124,140,200
Zvaa 116,140,184
buffer_load_dwordx4 a[192:195], v1, s[8:11], s81 offen offset:2048 sc1
W 8
Zvaa 128,144,184
Ta 136,21,10240
Ta 138,21,15360
Zvaa 136,144,200
Ta 140,22,10272
Ta 142,22,15392
Zvaa 140,148,200
buffer_load_dwordx4 a[208:211], v1, s[8:11], s82 offen offset:2048 sc1
Zvaa 132,148,184
Ta 144,21,10304
)"
R"(W 9
Zvaa 144,152,184
Ta 146,21,15424
Zvaa 152,152,200
buffer_load_dwordx4 a[196:199], v1, s[8:11], s81 offen offset:3072 sc1
Ta 148,22,10336
Zvaa 156,156,200
Ta 150,22,15456
Zvaa 148,156,184
Ta 152,21,10368
W 9
Zvaa 160,128,184
buffer_load_dwordx4 a[212:215], v1, s[8:11], s82 offen offset:3072 sc1
Ta 154,21,15488
Zvaa 168,128,200
s_lshl_b32 s80, s58, 3
s_add_u32 s80, s80, s55
Zvaa 172,132,200
Ta 156,22,10400
Ta 158,22,15520
Zvaa 164,132,184
v_mov_b64 v[28:29], s[12:13]
s_mov_b64 exec, 1
buffer_store_dwordx2 v[28:29], off, s[8:11], s80 sc1
s_mov_b64 exec, -1
s_waitcnt vmcnt(5) lgkmcnt(8)
Mvaa 96,136,188
s_barrier
Ta 128,21,10432
Mvaa 104,136,204
Ta 130,21,15552
s_add_u32 s94, s94, 81920
Mvaa 108,140,204
Ta 132,22,10464
Ta 134,22,15584
Mvaa 100,140,188
s_mul_i32 s95, s92, -81920
W 8
Mvaa 112,144,188
Ta 136,21,10496
Ta 138,21,15616
Mvaa 120,144,204
s_add_u32 s95, s95, s71
Ta 140,22,10528
Mvaa 124,148,204
Ta 142,22,15648
s_add_u32 m0, s95, 122880
Mvaa 116,148,188
Ta 144,21,20480
W 9
Mvaa 128,152,188
Ta 146,21,25600
D 16,40,94,0
Mvaa 136,152,204
Ta 148,22,20512
Ta 150,22,25632
Mvaa 140,156,204
Mvaa 132,156,188
D 17,40,94,1024
W 8
Mvaa 144,128,188
Ta 152,21,20544
Ta 154,21,25664
Mvaa 152,128,204
Ta 156,22,20576
Ta 158,22,25696
Mvaa 156,132,204
D 18,40,94,2048
Mvaa 148,132,188
Ta 128,21,20608
W 9
Mvaa 160,136,188
Ta 130,21,25728
Mvaa 168,136,204
D 19,40,94,3072
s_add_u32 m0, s95, 126976
Mvaa 172,140,204
Ta 132,22,20640
Ta 134,22,25760
Mvaa 164,140,188
Ta 136,21,20672
s_waitcnt vmcnt(7) lgkmcnt(9)
Mvaa 96,144,192
buffer_load_dwordx4 v20, s[40:43], s94 offen lds
s_add_u32 s94, s58, 3
Mvaa 104,144,208
Ta 138,21,25792
s_and_b32 s94, s94, 3
Mvaa 108,148,208
s_add_u32 s81, s58, 1
Ta 140,22,20704
Mvaa 100,148,192
Ta 142,22,25824
W 8
Mvaa 112,152,192
s_and_b32 s81, s81, 3
Ta 144,21,20736
Mvaa 120,152,208
Ta 146,21,25856
s_lshl_b32 s81, s81, 12
Mvaa 124,156,208
s_add_u32 s81, s81, s49
Ta 148,22,20768
Mvaa 116,156,192
Ta 150,22,25888
W 8
Mvaa 128,128,192
s_add_u32 s82, s81, 131072
Ta 152,25,10240
Mvaa 136,128,208
Ta 154,25,15360
buffer_load_dwordx4 a[216:219], v1, s[8:11], s81 offen offset:0 sc1
Mvaa 140,132,208
Ta 156,26,10272
Ta 158,26,15392
Mvaa 132,132,192
Ta 128,25,10304
W 9
Mvaa 144,136,192
buffer_load_dwordx4 a[232:235], v1, s[8:11], s82 offen offset:0 sc1
Ta 130,25,15424
Mvaa 152,136,208
Ta 132,26,10336
Ta 134,26,15456
Mvaa 156,140,208
Mvaa 148,140,192
buffer_load_dwordx4 a[220:223], v1, s[8:11], s81 offen offset:1024 sc1
W 8
Mvaa 160,144,192
Ta 136,25,10368
Ta 138,25,15488
Mvaa 168,144,208
Ta 140,26,10400
Ta 142,26,15520
Mvaa 172,148,208
buffer_load_dwordx4 a[236:239], v1, s[8:11], s82 offen offset:1024 sc1
Mvaa 164,148,192
Ta 144,25,10432
s_waitcnt vmcnt(10) lgkmcnt(9)
Mvaa 96,152,196
Ta 146,25,15552
Mvaa 104,152,212
buffer_load_dwordx4 a[224:227], v1, s[8:11], s81 offen offset:2048 sc1
Ta 148,26,10464
Mvaa 108,156,212
Ta 150,26,15584
Mvaa 100,156,196
Ta 152,25,10496
W 9
Mvaa 112,128,196
buffer_load_dwordx4 a[240:243], v1, s[8:11], s82 offen offset:2048 sc1
Ta 154,25,15616
Mvaa 120,128,212
Ta 156,26,10528
Ta 158,26,15648
Mvaa 124,132,212
Mvaa 116,132,196
buffer_load_dwordx4 a[228:231], v1, s[8:11], s81 offen offset:3072 sc1
W 8
Mvaa 128,136,196
Ta 128,25,20480
Ta 130,25,25600
Mvaa 136,136,212
Ta 132,26,20512
Ta 134,26,25632
Mvaa 140,140,212
buffer_load_dwordx4 a[244:247], v1, s[8:11], s82 offen offset:3072 sc1
s_mul_i32 s94, s94, 163840
Mvaa 132,140,196
s_add_u32 s94, s94, s48
W 8
Mvaa 144,144,196
s_mul_i32 s95, s92, 20480
s_add_u32 s95, s95, s71
Mvaa 152,144,212
s_add_u32 m0, s95, 51200
s_nop 0
D 16,40,94,0
Mvaa 156,148,212
Ta 136,25,20544
Ta 138,25,25664
Mvaa 148,148,196
Ta 140,26,20576
W 7
Mvaa 160,152,196
D 17,40,94,1024
Ta 142,26,25696
Mvaa 168,152,212
Ta 144,25,20608
Ta 146,25,25728
Mvaa 172,156,212
Ta 148,26,20640
Ta 150,26,25760
Mvaa 164,156,196
D 18,40,94,2048
s_waitcnt vmcnt(9) lgkmcnt(8)
Mvaa 96,128,216
Ta 152,25,20672
Ta 154,25,25792
Mvaa 104,128,232
Ta 156,26,20704
Ta 158,26,25824
Mvaa 108,132,232
D 19,40,94,3072
s_add_u32 m0, s95, 55296
Mvaa 100,132,216
Ta 128,25,20736
W 9
Mvaa 112,136,216
Ta 130,25,25856
Mvaa 120,136,232
buffer_load_dwordx4 v20, s[40:43], s94 offen lds
s_add_u32 s94, s94, 81920
Mvaa 124,140,232
s_mul_i32 s95, s92, 10240
s_add_u32 s95, s95, s71
Mvaa 116,140,216
s_add_u32 m0, s95, 92160
W 6
Mvaa 128,144,216
D 16,40,94,0
Ta 132,26,20768
Mvaa 136,144,232
Ta 134,26,25888
Ta 136,25,30720
Mvaa 140,148,232
Ta 138,25,35840
Ta 140,26,30752
Mvaa 132,148,216
D 17,40,94,1024
W 7
Mvaa 144,152,216
Ta 142,26,35872
Ta 144,25,30784
Mvaa 152,152,232
Ta 146,25,35904
Ta 148,26,30816
Mvaa 156,156,232
D 18,40,94,2048
D 19,40,94,3072
s_add_u32 m0, s95, 96256
s_nop 0
buffer_load_dwordx4 v20, s[40:43], s94 offen lds
Mvaa 148,156,216
Ta 150,26,35936
W 8
Mvaa 160,128,216
Ta 152,25,30848
Ta 154,25,35968
Mvaa 168,128,232
Ta 156,26,30880
Ta 158,26,36000
Mvaa 172,132,232
Mvaa 164,132,216
Ta 128,25,30912
s_waitcnt vmcnt(14) lgkmcnt(9)
Mvaa 96,136,220
s_waitcnt vmcnt(0)
s_barrier
Mvaa 104,136,236
Ta 130,25,36032
s_add_u32 s81, s58, 2
Mvaa 108,140,236
Ta 132,26,30944
Ta 134,26,36064
Mvaa 100,140,220
s_and_b32 s81, s81, 3
W 8
Mvaa 112,144,220
Ta 136,25,30976
Ta 138,25,36096
Mvaa 120,144,236
s_lshl_b32 s81, s81, 12
Ta 140,26,31008
Mvaa 124,148,236
Ta 142,26,36128
Mvaa 116,148,220
Ta 144,21,30720
W 9
Mvaa 128,152,220
Ta 146,21,35840
s_add_u32 s81, s81, s49
Mvaa 136,152,236
Ta 148,22,30752
Ta 150,22,35872
Mvaa 140,156,236
s_add_u32 s82, s81, 131072
Mvaa 132,156,220
Ta 152,21,30784
W 9
Mvaa 144,128,220
Ta 154,21,35904
Mvaa 152,128,236
buffer_load_dwordx4 a[0:3], v1, s[8:11], s81 offen offset:0 sc1
Ta 156,22,30816
Mvaa 156,132,236
Ta 158,22,35936
Mvaa 148,132,220
Ta 128,21,30848
W 9
Mvaa 160,136,220
buffer_load_dwordx4 a[16:19], v1, s[8:11], s82 offen offset:0 sc1
Ta 130,21,35968
Mvaa 168,136,236
Ta 132,22,30880
Ta 134,22,36000
Mvaa 172,140,236
Mvaa 164,140,220
buffer_load_dwordx4 a[4:7], v1, s[8:11], s81 offen offset:1024 sc1
W 8
Mvaa 96,144,224
Ta 136,21,30912
Ta 138,21,36032
Mvaa 104,144,240
Ta 140,22,30944
Ta 142,22,36064
Mvaa 108,148,240
buffer_load_dwordx4 a[20:23], v1, s[8:11], s82 offen offset:1024 sc1
Mvaa 100,148,224
Ta 144,21,30976
W 9
Mvaa 112,152,224
Ta 146,21,36096
Mvaa 120,152,240
buffer_load_dwordx4 a[8:11], v1, s[8:11], s81 offen offset:2048 sc1
Ta 148,22,31008
Mvaa 124,156,240
Ta 150,22,36128
Mvaa 116,156,224
Ta 152,23,0
W 9
Mvaa 128,128,224
buffer_load_dwordx4 a[24:27], v1, s[8:11], s82 offen offset:2048 sc1
Ta 154,23,5120
Mvaa 136,128,240
Ta 156,24,32
Ta 158,24,5152
Mvaa 140,132,240
Mvaa 132,132,224
buffer_load_dwordx4 a[12:15], v1, s[8:11], s81 offen offset:3072 sc1
W 8
Mvaa 144,136,224
Ta 128,23,64
Ta 130,23,5184
Mvaa 152,136,240
Ta 132,24,96
Ta 134,24,5216
Mvaa 156,140,240
buffer_load_dwordx4 a[28:31], v1, s[8:11], s82 offen offset:3072 sc1
s_add_u32 s81, s58, 3
Mvaa 148,140,224
s_and_b32 s81, s81, 3
W 8
Mvaa 160,144,224
Ta 136,23,128
Ta 138,23,5248
Mvaa 168,144,240
s_lshl_b32 s81, s81, 12
Ta 140,24,160
Mvaa 172,148,240
Ta 142,24,5280
Mvaa 164,148,224
Ta 144,23,192
W 9
Mvaa 96,152,228
Ta 146,23,5312
s_add_u32 s81, s81, s49
Mvaa 104,152,244
Ta 148,24,224
Ta 150,24,5344
Mvaa 108,156,244
s_add_u32 s82, s81, 131072
Mvaa 100,156,228
Ta 152,23,256
W 9
Mvaa 112,128,228
Ta 154,23,5376
Mvaa 120,128,244
buffer_load_dwordx4 a[32:35], v1, s[8:11], s81 offen offset:0 sc1
Ta 156,24,288
Mvaa 124,132,244
Ta 158,24,5408
Mvaa 116,132,228
Ta 128,23,20480
W 9
Mvaa 128,136,228
buffer_load_dwordx4 a[48:51], v1, s[8:11], s82 offen offset:0 sc1
Ta 130,23,25600
Mvaa 136,136,244
Ta 132,24,20512
Ta 134,24,25632
Mvaa 140,140,244
Mvaa 132,140,228
buffer_load_dwordx4 a[36:39], v1, s[8:11], s81 offen offset:1024 sc1
W 8
Mvaa 144,144,228
Ta 136,23,20544
Ta 138,23,25664
Mvaa 152,144,244
Ta 140,24,20576
)"
R"(Ta 142,24,25696
Mvaa 156,148,244
buffer_load_dwordx4 a[52:55], v1, s[8:11], s82 offen offset:1024 sc1
Mvaa 148,148,228
Ta 144,23,20608
W 9
Mvaa 160,152,228
Ta 146,23,25728
Mvaa 168,152,244
buffer_load_dwordx4 a[40:43], v1, s[8:11], s81 offen offset:2048 sc1
Ta 148,24,20640
Mvaa 172,156,244
Ta 150,24,25760
Mvaa 164,156,228
Ta 152,23,20672
s_waitcnt vmcnt(11) lgkmcnt(9)
Mvaa 96,128,0
buffer_load_dwordx4 a[56:59], v1, s[8:11], s82 offen offset:2048 sc1
Ta 154,23,25792
Mvaa 104,128,16
Ta 156,24,20704
Ta 158,24,25824
Mvaa 108,132,16
Mvaa 100,132,0
buffer_load_dwordx4 a[44:47], v1, s[8:11], s81 offen offset:3072 sc1
W 8
Mvaa 112,136,0
Ta 128,23,20736
Ta 130,23,25856
Mvaa 120,136,16
Ta 132,24,20768
Ta 134,24,25888
Mvaa 124,140,16
buffer_load_dwordx4 a[60:63], v1, s[8:11], s82 offen offset:3072 sc1
Mvaa 116,140,0
Ta 136,23,51200
W 9
Mvaa 128,144,0
Ta 138,23,56320
v_bfe_u32 v28, v1, 4, 2
Mvaa 136,144,16
v_lshlrev_b32 v28, 3, v28
Ta 140,24,51232
Mvaa 140,148,16
Ta 142,24,56352
buffer_load_dwordx2 v[28:29], v28, s[8:11], s55 offen sc1
Mvaa 132,148,0
Ta 144,23,51264
W 9
Mvaa 144,152,0
Ta 146,23,56384
Mvaa 152,152,16
Ta 148,24,51296
Ta 150,24,56416
Mvaa 156,156,16
Mvaa 148,156,0
Ta 152,23,51328
W 9
Mvaa 160,128,0
Ta 154,23,56448
Mvaa 168,128,16
Ta 156,24,51360
Ta 158,24,56480
Mvaa 172,132,16
Mvaa 164,132,0
Ta 128,23,51392
s_waitcnt vmcnt(13) lgkmcnt(9)
Mvaa 96,136,4
Ta 130,23,56512
Mvaa 104,136,20
Ta 132,24,51424
Ta 134,24,56544
Mvaa 108,140,20
Mvaa 100,140,4
Ta 136,23,51456
W 9
Mvaa 112,144,4
Ta 138,23,56576
Mvaa 120,144,20
Ta 140,24,51488
Ta 142,24,56608
Mvaa 124,148,20
Mvaa 116,148,4
Ta 144,25,0
W 9
Mvaa 128,152,4
Ta 146,25,5120
Mvaa 136,152,20
Ta 148,26,32
Ta 150,26,5152
Mvaa 140,156,20
Mvaa 132,156,4
Ta 152,25,64
W 9
Mvaa 144,128,4
Ta 154,25,5184
Mvaa 152,128,20
Ta 156,26,96
Ta 158,26,5216
Mvaa 156,132,20
Mvaa 148,132,4
Ta 128,25,128
W 9
Mvaa 160,136,4
Ta 130,25,5248
Mvaa 168,136,20
Ta 132,26,160
Ta 134,26,5280
Mvaa 172,140,20
Mvaa 164,140,4
Ta 136,25,192
s_waitcnt vmcnt(11) lgkmcnt(9)
Mvaa 96,144,8
Ta 138,25,5312
Mvaa 104,144,24
Ta 140,26,224
Ta 142,26,5344
Mvaa 108,148,24
Mvaa 100,148,8
Ta 144,25,256
W 9
Mvaa 112,152,8
Ta 146,25,5376
Mvaa 120,152,24
Ta 148,26,288
Ta 150,26,5408
Mvaa 124,156,24
Mvaa 116,156,8
Ta 152,21,40960
W 9
Mvaa 128,128,8
Ta 154,21,46080
Mvaa 136,128,24
Ta 156,22,40992
Ta 158,22,46112
Mvaa 140,132,24
s_waitcnt vmcnt(0)
v_cmp_eq_u64 vcc, s[12:13], v[28:29]
s_cmp_eq_u64 vcc, -1
s_cbranch_scc0 .Lspin_90%=
.Lok_91%=:
Mvaa 132,132,8
s_add_u32 s81, s58, 1
W 8
Mvaa 144,136,8
Ta 128,21,41024
Ta 130,21,46144
Mvaa 152,136,24
Ta 132,22,41056
Ta 134,22,46176
Mvaa 156,140,24
s_and_b32 s81, s81, 3
Mvaa 148,140,8
Ta 136,21,41088
W 9
Mvaa 160,144,8
Ta 138,21,46208
Mvaa 168,144,24
s_lshl_b32 s81, s81, 12
Ta 140,22,41120
Mvaa 172,148,24
Ta 142,22,46240
Mvaa 164,148,8
Ta 144,21,41152
W 9
Mvaa 96,152,12
Ta 146,21,46272
s_add_u32 s81, s81, s50
Mvaa 104,152,28
Ta 148,22,41184
Ta 150,22,46304
Mvaa 108,156,28
Mvaa 100,156,12
s_add_u32 s82, s81, 131072
W 8
Mvaa 112,128,12
Ta 152,21,41216
Ta 154,21,46336
Mvaa 120,128,28
Ta 156,22,41248
Ta 158,22,46368
Mvaa 124,132,28
buffer_load_dwordx4 a[64:67], v1, s[8:11], s81 offen offset:0 sc1
Mvaa 116,132,12
Ta 128,21,51200
W 9
Mvaa 128,136,12
Ta 130,21,56320
Mvaa 136,136,28
buffer_load_dwordx4 a[80:83], v1, s[8:11], s82 offen offset:0 sc1
Ta 132,22,51232
Mvaa 140,140,28
Ta 134,22,56352
Mvaa 132,140,12
Ta 136,21,51264
W 9
Mvaa 144,144,12
Ta 138,21,56384
buffer_load_dwordx4 a[68:71], v1, s[8:11], s81 offen offset:1024 sc1
Mvaa 152,144,28
Ta 140,22,51296
Ta 142,22,56416
Mvaa 156,148,28
Mvaa 148,148,12
buffer_load_dwordx4 a[84:87], v1, s[8:11], s82 offen offset:1024 sc1
W 8
Mvaa 160,152,12
Ta 144,21,51328
Ta 146,21,56448
Mvaa 168,152,28
Ta 148,22,51360
Ta 150,22,56480
Mvaa 172,156,28
buffer_load_dwordx4 a[72:75], v1, s[8:11], s81 offen offset:2048 sc1
Mvaa 164,156,12
Ta 152,21,51392
W 9
Mvaa 96,128,32
Ta 154,21,56512
Mvaa 104,128,48
buffer_load_dwordx4 a[88:91], v1, s[8:11], s82 offen offset:2048 sc1
Ta 156,22,51424
Mvaa 108,132,48
Ta 158,22,56544
Mvaa 100,132,32
Ta 128,21,51456
W 9
Mvaa 112,136,32
Ta 130,21,56576
buffer_load_dwordx4 a[76:79], v1, s[8:11], s81 offen offset:3072 sc1
Mvaa 120,136,48
Ta 132,22,51488
Ta 134,22,56608
Mvaa 124,140,48
Mvaa 116,140,32
buffer_load_dwordx4 a[92:95], v1, s[8:11], s82 offen offset:3072 sc1
W 8
Mvaa 128,144,32
Ta 136,23,10240
Ta 138,23,15360
Mvaa 136,144,48
Ta 140,24,10272
Ta 142,24,15392
Mvaa 140,148,48
s_add_u32 s81, s58, 2
Mvaa 132,148,32
Ta 144,23,10304
W 9
Mvaa 144,152,32
Ta 146,23,15424
Mvaa 152,152,48
Ta 148,24,10336
Ta 150,24,15456
Mvaa 156,156,48
s_and_b32 s81, s81, 3
Mvaa 148,156,32
Ta 152,23,10368
W 9
Mvaa 160,128,32
Ta 154,23,15488
Mvaa 168,128,48
s_lshl_b32 s81, s81, 12
Ta 156,24,10400
Mvaa 172,132,48
Ta 158,24,15520
Mvaa 164,132,32
Ta 128,23,10432
W 9
Mvaa 96,136,36
Ta 130,23,15552
s_add_u32 s81, s81, s50
Mvaa 104,136,52
Ta 132,24,10464
Ta 134,24,15584
Mvaa 108,140,52
Mvaa 100,140,36
s_add_u32 s82, s81, 131072
W 8
Mvaa 112,144,36
Ta 136,23,10496
Ta 138,23,15616
Mvaa 120,144,52
Ta 140,24,10528
Ta 142,24,15648
Mvaa 124,148,52
buffer_load_dwordx4 a[96:99], v1, s[8:11], s81 offen offset:0 sc1
Mvaa 116,148,36
Ta 144,23,30720
W 9
Mvaa 128,152,36
Ta 146,23,35840
Mvaa 136,152,52
buffer_load_dwordx4 a[112:115], v1, s[8:11], s82 offen offset:0 sc1
Ta 148,24,30752
Mvaa 140,156,52
Ta 150,24,35872
Mvaa 132,156,36
Ta 152,23,30784
W 9
Mvaa 144,128,36
Ta 154,23,35904
buffer_load_dwordx4 a[100:103], v1, s[8:11], s81 offen offset:1024 sc1
Mvaa 152,128,52
Ta 156,24,30816
Ta 158,24,35936
Mvaa 156,132,52
Mvaa 148,132,36
buffer_load_dwordx4 a[116:119], v1, s[8:11], s82 offen offset:1024 sc1
W 8
Mvaa 160,136,36
Ta 128,23,30848
Ta 130,23,35968
Mvaa 168,136,52
Ta 132,24,30880
Ta 134,24,36000
Mvaa 172,140,52
buffer_load_dwordx4 a[104:107], v1, s[8:11], s81 offen offset:2048 sc1
Mvaa 164,140,36
Ta 136,23,30912
W 9
Mvaa 96,144,40
Ta 138,23,36032
Mvaa 104,144,56
buffer_load_dwordx4 a[120:123], v1, s[8:11], s82 offen offset:2048 sc1
Ta 140,24,30944
Mvaa 108,148,56
Ta 142,24,36064
Mvaa 100,148,40
Ta 144,23,30976
W 9
Mvaa 112,152,40
Ta 146,23,36096
buffer_load_dwordx4 a[108:111], v1, s[8:11], s81 offen offset:3072 sc1
Mvaa 120,152,56
Ta 148,24,31008
Ta 150,24,36128
Mvaa 124,156,56
Mvaa 116,156,40
buffer_load_dwordx4 a[124:127], v1, s[8:11], s82 offen offset:3072 sc1
W 8
Mvaa 128,128,40
Ta 152,23,40960
Ta 154,23,46080
Mvaa 136,128,56
Ta 156,24,40992
Ta 158,24,46112
Mvaa 140,132,56
s_add_u32 s81, s58, 3
Mvaa 132,132,40
Ta 128,23,41024
W 9
Mvaa 144,136,40
Ta 130,23,46144
Mvaa 152,136,56
Ta 132,24,41056
Ta 134,24,46176
Mvaa 156,140,56
s_and_b32 s81, s81, 3
Mvaa 148,140,40
Ta 136,23,41088
W 9
Mvaa 160,144,40
Ta 138,23,46208
Mvaa 168,144,56
s_lshl_b32 s81, s81, 12
Ta 140,24,41120
Mvaa 172,148,56
Ta 142,24,46240
Mvaa 164,148,40
Ta 144,23,41152
W 9
Mvaa 96,152,44
Ta 146,23,46272
s_add_u32 s81, s81, s50
Mvaa 104,152,60
Ta 148,24,41184
Ta 150,24,46304
Mvaa 108,156,60
Mvaa 100,156,44
s_add_u32 s82, s81, 131072
W 8
Mvaa 112,128,44
Ta 152,23,41216
Ta 154,23,46336
Mvaa 120,128,60
Ta 156,24,41248
Ta 158,24,46368
Mvaa 124,132,60
buffer_load_dwordx4 a[184:187], v1, s[8:11], s81 offen offset:0 sc1
Mvaa 116,132,44
Ta 128,25,20480
W 9
Mvaa 128,136,44
Ta 130,25,25600
Mvaa 136,136,60
buffer_load_dwordx4 a[200:203], v1, s[8:11], s82 offen offset:0 sc1
Ta 132,26,20512
Mvaa 140,140,60
Ta 134,26,25632
K 96,96,97
)"
R"(Mvaa 132,140,44
Ta 136,25,20544
W 9
Mvaa 144,144,44
Ta 138,25,25664
buffer_load_dwordx4 a[188:191], v1, s[8:11], s81 offen offset:1024 sc1
Mvaa 152,144,60
K 97,98,99
Ta 140,26,20576
Mvaa 156,148,60
Ta 142,26,25696
Mvaa 148,148,44
buffer_load_dwordx4 a[204:207], v1, s[8:11], s82 offen offset:1024 sc1
W 8
Mvaa 160,152,44
K 98,100,101
Ta 144,25,20608
Mvaa 168,152,60
Ta 146,25,25728
Ta 148,26,20640
Mvaa 172,156,60
Ta 150,26,25760
K 99,102,103
Mvaa 164,156,44
buffer_load_dwordx4 a[192:195], v1, s[8:11], s81 offen offset:2048 sc1
s_waitcnt vmcnt(19) lgkmcnt(8)
Mvaa 176,128,64
Ta 152,25,20672
Ta 154,25,25792
Mvaa 184,128,80
s_add_u32 s83, s52, 0
v_and_b32 v27, 1, v12
Mvaa 188,132,80
buffer_load_dwordx4 a[208:211], v1, s[8:11], s82 offen offset:2048 sc1
Ta 156,26,20704
Mvaa 180,132,64
Ta 158,26,25824
W 8
Mvaa 192,136,64
v_lshlrev_b32 v27, 1, v27
Ta 128,25,20736
Mvaa 200,136,80
buffer_store_dwordx4 v[96:99], v1, s[8:11], s83 offen sc1
Ta 130,25,25856
Mvaa 204,140,80
K 104,104,105
Ta 132,26,20768
Mvaa 196,140,64
Ta 134,26,25888
W 8
Mvaa 208,144,64
buffer_load_dwordx4 a[196:199], v1, s[8:11], s81 offen offset:3072 sc1
v_xor_b32 v27, v27, v13
Mvaa 216,144,80
K 105,106,107
Ta 136,25,30720
Mvaa 220,148,80
Ta 138,25,35840
v_and_b32 v12, 2, v12
Mvaa 212,148,64
buffer_load_dwordx4 a[212:215], v1, s[8:11], s82 offen offset:3072 sc1
W 6
Mvaa 224,152,64
K 106,108,109
Ta 140,26,30752
Mvaa 232,152,80
Ta 142,26,35872
v_or_b32 v12, v12, v14
Mvaa 236,156,80
Ta 144,25,30784
Ta 146,25,35904
Mvaa 228,156,64
K 107,110,111
W 6
Mvaa 240,128,64
Ta 148,26,30816
Ta 150,26,35936
Mvaa 248,128,80
s_add_u32 s83, s52, 40960
v_lshl_or_b32 v12, v27, 2, v12
Mvaa 252,132,80
Ta 152,25,30848
Ta 154,25,35968
Mvaa 244,132,64
buffer_store_dwordx4 v[104:107], v1, s[8:11], s83 offen sc1
s_waitcnt vmcnt(22) lgkmcnt(6)
Mvaa 176,136,68
v_cmp_eq_u32_e64 s[86:87], 0, v12
Ta 156,26,30880
Mvaa 184,136,84
Ta 158,26,36000
K 112,112,113
Mvaa 188,140,84
Ta 128,25,30912
Ta 130,25,36032
Mvaa 180,140,68
v_cndmask_b32_e64 v4, 0, v15, s[86:87]
W 6
Mvaa 192,144,68
K 113,114,115
Ta 132,26,30944
Mvaa 200,144,84
Ta 134,26,36064
v_cmp_eq_u32_e64 s[86:87], 1, v12
Mvaa 204,148,84
K 114,116,117
Ta 136,25,30976
Mvaa 196,148,68
Ta 138,25,36096
W 6
Mvaa 208,152,68
v_cndmask_b32_e64 v5, 0, v15, s[86:87]
Ta 140,26,31008
Mvaa 216,152,84
Ta 142,26,36128
K 115,118,119
Mvaa 220,156,84
Ta 144,21,30720
Ta 146,21,35840
Mvaa 212,156,68
v_cmp_eq_u32_e64 s[86:87], 2, v12
W 6
Mvaa 224,128,68
s_add_u32 s83, s52, 1024
Ta 148,22,30752
Mvaa 232,128,84
Ta 150,22,35872
buffer_store_dwordx4 v[112:115], v1, s[8:11], s83 offen sc1
Mvaa 236,132,84
v_cndmask_b32_e64 v6, 0, v15, s[86:87]
Ta 152,21,30784
Mvaa 228,132,68
Ta 154,21,35904
W 6
Mvaa 240,136,68
K 120,120,121
v_cmp_eq_u32_e64 s[86:87], 3, v12
Mvaa 248,136,84
Ta 156,22,30816
Ta 158,22,35936
Mvaa 252,140,84
K 121,122,123
Ta 128,21,30848
Mvaa 244,140,68
Ta 130,21,35968
s_waitcnt vmcnt(21) lgkmcnt(6)
Mvaa 176,144,72
v_cndmask_b32_e64 v7, 0, v15, s[86:87]
K 122,124,125
Mvaa 184,144,88
Ta 132,22,30880
Ta 134,22,36000
Mvaa 188,148,88
v_cmp_eq_u32_e64 s[86:87], 4, v12
Ta 136,21,30912
Mvaa 180,148,72
Ta 138,21,36032
W 6
Mvaa 192,152,72
K 123,126,127
v_cndmask_b32_e64 v8, 0, v15, s[86:87]
Mvaa 200,152,88
Ta 140,22,30944
Ta 142,22,36064
Mvaa 204,156,88
s_add_u32 s83, s52, 41984
Ta 144,21,30976
Mvaa 196,156,72
Ta 146,21,36096
W 6
Mvaa 208,128,72
v_cmp_eq_u32_e64 s[86:87], 5, v12
buffer_store_dwordx4 v[120:123], v1, s[8:11], s83 offen sc1
Mvaa 216,128,88
Ta 148,22,31008
Ta 150,22,36128
Mvaa 220,132,88
K 128,128,129
v_cndmask_b32_e64 v9, 0, v15, s[86:87]
Mvaa 212,132,72
Ta 152,23,0
W 5
Mvaa 224,136,72
Ta 154,23,5120
K 129,130,131
Mvaa 232,136,88
Ta 156,24,32
Ta 158,24,5152
Mvaa 236,140,88
v_cmp_eq_u32_e64 s[86:87], 6, v12
K 130,132,133
Mvaa 228,140,72
Ta 128,23,64
W 5
Mvaa 240,144,72
Ta 130,23,5184
v_cndmask_b32_e64 v10, 0, v15, s[86:87]
Mvaa 248,144,88
Ta 132,24,96
Ta 134,24,5216
Mvaa 252,148,88
K 131,134,135
v_cmp_eq_u32_e64 s[86:87], 7, v12
Mvaa 244,148,72
Ta 136,23,128
s_waitcnt vmcnt(20) lgkmcnt(5)
Mvaa 176,152,76
Ta 138,23,5248
s_add_u32 s83, s52, 2048
Mvaa 184,152,92
Ta 140,24,160
Ta 142,24,5280
Mvaa 188,156,92
v_cndmask_b32_e64 v11, 0, v15, s[86:87]
buffer_store_dwordx4 v[128:131], v1, s[8:11], s83 offen sc1
Mvaa 180,156,76
Ta 144,23,192
W 5
Mvaa 192,128,76
Ta 146,23,5312
K 136,136,137
Mvaa 200,128,92
Ta 148,24,224
Ta 150,24,5344
Mvaa 204,132,92
K 137,138,139
Ta 152,23,256
Mvaa 196,132,76
Ta 154,23,5376
W 6
Mvaa 208,136,76
K 138,140,141
Ta 156,24,288
Mvaa 216,136,92
Ta 158,24,5408
Ta 128,23,20480
Mvaa 220,140,92
Ta 130,23,25600
K 139,142,143
Mvaa 212,140,76
Ta 132,24,20512
W 7
Mvaa 224,144,76
Ta 134,24,25632
s_add_u32 s83, s52, 43008
Mvaa 232,144,92
Ta 136,23,51200
Ta 138,23,56320
Mvaa 236,148,92
buffer_store_dwordx4 v[136:139], v1, s[8:11], s83 offen sc1
Ta 140,24,51232
Mvaa 228,148,76
Ta 142,24,56352
W 8
Mvaa 240,152,76
K 144,144,145
Ta 144,25,0
Mvaa 248,152,92
Ta 146,25,5120
K 145,146,147
Mvaa 252,156,92
Ta 148,26,32
Ta 150,26,5152
Mvaa 244,156,76
K 146,148,149
s_waitcnt vmcnt(20) lgkmcnt(8)
Mvaa 176,128,96
Ta 152,21,40960
Ta 154,21,46080
Mvaa 184,128,112
Ta 156,22,40992
Ta 158,22,46112
Mvaa 188,132,112
K 147,150,151
Mvaa 180,132,96
Ta 128,21,51200
s_waitcnt vmcnt(18) lgkmcnt(9)
Mvaa 176,136,100
Ta 130,21,56320
s_add_u32 s83, s52, 3072
Mvaa 184,136,116
Ta 132,22,51232
Ta 134,22,56352
Mvaa 188,140,116
buffer_store_dwordx4 v[144:147], v1, s[8:11], s83 offen sc1
Mvaa 180,140,100
Ta 136,23,10240
s_waitcnt vmcnt(17) lgkmcnt(9)
Mvaa 176,144,104
Ta 138,23,15360
K 152,152,153
Mvaa 184,144,120
Ta 140,24,10272
Ta 142,24,15392
Mvaa 188,148,120
Mvaa 180,148,104
K 153,154,155
s_waitcnt vmcnt(15) lgkmcnt(8)
Mvaa 176,152,108
Ta 144,23,30720
Ta 146,23,35840
Mvaa 184,152,124
K 154,156,157
Ta 148,24,30752
Mvaa 188,156,124
Ta 150,24,35872
Mvaa 180,156,108
Ta 152,23,40960
s_waitcnt vmcnt(13) lgkmcnt(9)
Mvaa 176,128,184
Ta 154,23,46080
K 155,158,159
Mvaa 184,128,200
Ta 156,24,40992
Ta 158,24,46112
Mvaa 188,132,200
s_add_u32 s83, s52, 44032
Mvaa 180,132,184
Ta 128,23,20544
s_waitcnt vmcnt(11) lgkmcnt(9)
Mvaa 176,136,188
Ta 130,23,25664
buffer_store_dwordx4 v[152:155], v1, s[8:11], s83 offen sc1
Mvaa 184,136,204
Ta 132,24,20576
Ta 134,24,25696
Mvaa 188,140,204
K 160,160,161
Mvaa 180,140,188
Ta 136,23,51264
s_waitcnt vmcnt(10) lgkmcnt(9)
Mvaa 176,144,192
Ta 138,23,56384
Mvaa 184,144,208
K 161,162,163
Ta 140,24,51296
Mvaa 188,148,208
Ta 142,24,56416
Mvaa 180,148,192
K 162,164,165
s_waitcnt vmcnt(7) lgkmcnt(8)
Mvaa 176,152,196
Ta 144,25,64
Ta 146,25,5184
Mvaa 184,152,212
Ta 148,26,96
Ta 150,26,5216
Mvaa 188,156,212
K 163,166,167
Mvaa 180,156,196
Ta 152,21,41024
W 9
Mvaa 192,128,96
Ta 154,21,46144
s_add_u32 s83, s52, 4096
Mvaa 200,128,112
Ta 156,22,41056
Ta 158,22,46176
Mvaa 204,132,112
buffer_store_dwordx4 v[160:163], v1, s[8:11], s83 offen sc1
Mvaa 196,132,96
Ta 128,21,51264
W 9
Mvaa 192,136,100
Ta 130,21,56384
K 168,168,169
Mvaa 200,136,116
Ta 132,22,51296
Ta 134,22,56416
Mvaa 204,140,116
Mvaa 196,140,100
K 169,170,171
W 8
Mvaa 192,144,104
Ta 136,23,10304
Ta 138,23,15424
Mvaa 200,144,120
K 170,172,173
Ta 140,24,10336
Mvaa 204,148,120
Ta 142,24,15456
Mvaa 196,148,104
Ta 144,23,30784
W 9
Mvaa 192,152,108
Ta 146,23,35904
K 171,174,175
Mvaa 200,152,124
Ta 148,24,30816
Ta 150,24,35936
Mvaa 204,156,124
s_add_u32 s83, s52, 45056
Mvaa 196,156,108
Ta 152,23,41024
W 9
Mvaa 192,128,184
Ta 154,23,46144
buffer_store_dwordx4 v[168:171], v1, s[8:11], s83 offen sc1
Mvaa 200,128,200
K 176,176,177
)"
R"(K 177,178,179
Mvaa 204,132,200
K 178,180,181
K 179,182,183
Mvaa 196,132,184
s_add_u32 s83, s52, 327680
W 6
Mvaa 192,136,188
buffer_store_dwordx4 v[176:179], v1, s[8:11], s83 offen sc1
K 184,184,185
Mvaa 200,136,204
K 185,186,187
K 186,188,189
Mvaa 204,140,204
K 187,190,191
s_add_u32 s83, s52, 368640
Mvaa 196,140,188
Ta 156,24,41056
Ta 158,24,46176
W 4
Mvaa 192,144,192
buffer_store_dwordx4 v[184:187], v1, s[8:11], s83 offen sc1
Ta 128,23,20608
Mvaa 200,144,208
Ta 130,23,25728
Ta 132,24,20640
Mvaa 204,148,208
Ta 134,24,25760
Ta 136,23,51328
Mvaa 196,148,192
Ta 138,23,56448
W 6
Mvaa 192,152,196
Ta 140,24,51360
Ta 142,24,56480
Mvaa 200,152,212
Ta 144,25,128
Ta 146,25,5248
Mvaa 204,156,212
Ta 148,26,160
Ta 150,26,5280
Mvaa 196,156,196
Ta 152,21,41088
W 9
Mvaa 208,128,96
Ta 154,21,46208
Mvaa 216,128,112
Ta 156,22,41120
Ta 158,22,46240
Mvaa 220,132,112
K 192,192,193
Mvaa 212,132,96
Ta 128,21,51328
W 9
Mvaa 208,136,100
Ta 130,21,56448
K 193,194,195
Mvaa 216,136,116
K 194,196,197
Ta 132,22,51360
Mvaa 220,140,116
Ta 134,22,56480
K 195,198,199
Mvaa 212,140,100
Ta 136,23,10368
W 9
Mvaa 208,144,104
Ta 138,23,15488
s_add_u32 s83, s52, 328704
Mvaa 216,144,120
buffer_store_dwordx4 v[192:195], v1, s[8:11], s83 offen sc1
K 200,200,201
Mvaa 220,148,120
K 201,202,203
K 202,204,205
Mvaa 212,148,104
K 203,206,207
W 6
Mvaa 208,152,108
s_add_u32 s83, s52, 369664
buffer_store_dwordx4 v[200:203], v1, s[8:11], s83 offen sc1
Mvaa 216,152,124
Ta 140,24,10400
Ta 142,24,15520
Mvaa 220,156,124
Ta 144,23,30848
Ta 146,23,35968
Mvaa 212,156,108
Ta 148,24,30880
W 7
Mvaa 208,128,184
Ta 150,24,36000
Ta 152,23,41088
Mvaa 216,128,200
Ta 154,23,46208
Ta 156,24,41120
Mvaa 220,132,200
Ta 158,24,46240
Mvaa 212,132,184
Ta 128,23,20672
W 9
Mvaa 208,136,188
Ta 130,23,25792
Mvaa 216,136,204
Ta 132,24,20704
Ta 134,24,25824
Mvaa 220,140,204
Mvaa 212,140,188
Ta 136,23,51392
W 9
Mvaa 208,144,192
Ta 138,23,56512
Mvaa 216,144,208
Ta 140,24,51424
Ta 142,24,56544
Mvaa 220,148,208
Mvaa 212,148,192
Ta 144,25,192
W 9
Mvaa 208,152,196
Ta 146,25,5312
Mvaa 216,152,212
Ta 148,26,224
Ta 150,26,5344
Mvaa 220,156,212
Mvaa 212,156,196
Ta 152,21,41152
W 9
Mvaa 224,128,96
Ta 154,21,46272
Mvaa 232,128,112
Ta 156,22,41184
Ta 158,22,46304
Mvaa 236,132,112
K 208,208,209
Mvaa 228,132,96
Ta 128,21,51392
W 9
Mvaa 224,136,100
Ta 130,21,56512
K 209,210,211
Mvaa 232,136,116
K 210,212,213
Ta 132,22,51424
Mvaa 236,140,116
Ta 134,22,56544
K 211,214,215
Mvaa 228,140,100
Ta 136,23,10432
W 9
Mvaa 224,144,104
Ta 138,23,15552
s_add_u32 s83, s52, 329728
Mvaa 232,144,120
buffer_store_dwordx4 v[208:211], v1, s[8:11], s83 offen sc1
K 216,216,217
Mvaa 236,148,120
K 217,218,219
K 218,220,221
Mvaa 228,148,104
K 219,222,223
W 6
Mvaa 224,152,108
s_add_u32 s83, s52, 370688
buffer_store_dwordx4 v[216:219], v1, s[8:11], s83 offen sc1
Mvaa 232,152,124
Ta 140,24,10464
Ta 142,24,15584
Mvaa 236,156,124
Ta 144,23,30912
Ta 146,23,36032
Mvaa 228,156,108
Ta 148,24,30944
W 7
Mvaa 224,128,184
Ta 150,24,36064
s_waitcnt vmcnt(6)
v_lshlrev_b32 v2, 2, v1
v_mov_b64 v[28:29], s[12:13]
s_mov_b64 exec, 3
buffer_store_dwordx2 v[28:29], v2, s[8:11], s53 offen offset:0 sc1
s_mov_b64 exec, -1
Mvaa 232,128,200
Ta 152,23,41152
Ta 154,23,46272
Mvaa 236,132,200
Ta 156,24,41184
Ta 158,24,46304
Mvaa 228,132,184
Ta 128,23,20736
W 9
Mvaa 224,136,188
Ta 130,23,25856
Mvaa 232,136,204
Ta 132,24,20768
Ta 134,24,25888
Mvaa 236,140,204
Mvaa 228,140,188
Ta 136,23,51456
W 9
Mvaa 224,144,192
Ta 138,23,56576
Mvaa 232,144,208
Ta 140,24,51488
Ta 142,24,56608
Mvaa 236,148,208
Mvaa 228,148,192
Ta 144,25,256
W 9
Mvaa 224,152,196
Ta 146,25,5376
Mvaa 232,152,212
Ta 148,26,288
Ta 150,26,5408
Mvaa 236,156,212
Mvaa 228,156,196
Ta 152,21,41216
W 9
Mvaa 240,128,96
Ta 154,21,46336
Mvaa 248,128,112
Ta 156,22,41248
Ta 158,22,46368
Mvaa 252,132,112
K 224,224,225
Mvaa 244,132,96
Ta 128,21,51456
W 9
Mvaa 240,136,100
Ta 130,21,56576
K 225,226,227
Mvaa 248,136,116
K 226,228,229
Ta 132,22,51488
Mvaa 252,140,116
Ta 134,22,56608
K 227,230,231
Mvaa 244,140,100
Ta 136,23,10496
W 9
Mvaa 240,144,104
Ta 138,23,15616
s_add_u32 s83, s52, 330752
Mvaa 248,144,120
buffer_store_dwordx4 v[224:227], v1, s[8:11], s83 offen sc1
K 232,232,233
Mvaa 252,148,120
K 233,234,235
K 234,236,237
Mvaa 244,148,104
K 235,238,239
W 6
Mvaa 240,152,108
s_add_u32 s83, s52, 371712
buffer_store_dwordx4 v[232:235], v1, s[8:11], s83 offen sc1
Mvaa 248,152,124
Ta 140,24,10528
Ta 142,24,15648
Mvaa 252,156,124
Ta 144,23,30976
Ta 146,23,36096
Mvaa 244,156,108
Ta 148,24,31008
W 7
Mvaa 240,128,184
Ta 150,24,36128
Ta 152,23,41216
Mvaa 248,128,200
Ta 154,23,46336
Ta 156,24,41248
Mvaa 252,132,200
Ta 158,24,46368
Mvaa 244,132,184
W 8
Mvaa 240,136,188
Mvaa 248,136,204
Mvaa 252,140,204
Mvaa 244,140,188
W 4
Mvaa 240,144,192
Mvaa 248,144,208
Mvaa 252,148,208
Mvaa 244,148,192
W 0
Mvaa 240,152,196
Mvaa 248,152,212
Mvaa 252,156,212
Mvaa 244,156,196
s_nop 4
K 240,240,241
K 241,242,243
s_nop 0
K 242,244,245
K 243,246,247
s_add_u32 s83, s52, 331776
buffer_store_dwordx4 v[240:243], v1, s[8:11], s83 offen sc1
K 248,248,249
K 249,250,251
K 250,252,253
K 251,254,255
s_add_u32 s83, s52, 372736
buffer_store_dwordx4 v[248:251], v1, s[8:11], s83 offen sc1
s_waitcnt vmcnt(0)
v_lshlrev_b32 v2, 2, v1
v_mov_b64 v[28:29], s[12:13]
s_mov_b64 exec, 3
buffer_store_dwordx2 v[28:29], v2, s[8:11], s53 offen offset:512 sc1
s_mov_b64 exec, -1
.Ltend_79%=:
s_lshr_b32 s80, s15, 3
s_lshl_b32 s80, s80, 4
s_add_u32 s80, s80, s59
s_lshl_b32 s53, s80, 6
s_add_u32 s53, s53, 65536
s_mul_i32 s52, s80, 40960
s_add_u32 s52, s52, 62914560
v_bfe_u32 v3, v1, 4, 3
v_lshlrev_b32 v3, 3, v3
v_bfe_u32 v2, v1, 4, 4
v_mul_u32_u24 v2, 1280, v2
v_lshrrev_b32 v28, 8, v1
v_lshl_add_u32 v2, v28, 4, v2
s_mov_b32 s40, s16
s_and_b32 s41, s17, 0xffff
s_mov_b32 s42, 2621440
s_mov_b32 s43, 0x20000
s_mov_b32 s44, s38
s_and_b32 s45, s39, 0xffff
s_mov_b32 s46, 2621440
s_mov_b32 s47, 0x20000
s_bfe_u32 s80, s59, 0x10001
s_lshl_b32 s81, s57, 1
s_add_u32 s80, s80, s81
s_lshl_b32 s61, s80, 2
s_lshr_b32 s80, s59, 2
s_lshl_b32 s80, s80, 1
s_add_u32 s61, s61, s80
s_and_b32 s80, s59, 1
s_add_u32 s61, s61, s80
s_mov_b32 s84, 0
.LL_93%=:
buffer_load_dwordx2 v[12:13], v3, s[8:11], 0 offen offset:3584 sc1
s_waitcnt vmcnt(0)
v_xor_b32 v12, s12, v12
v_xor_b32 v13, s13, v13
v_lshrrev_b32 v14, 11, v12
v_or_b32 v13, v13, v14
v_cmp_eq_u32 vcc, 0, v13
s_cmp_eq_u64 vcc, -1
s_cbranch_scc1 .LL_94%=
s_sleep 2
s_add_u32 s84, s84, 1
s_cmp_lt_u32 s84, 1048576
s_cbranch_scc1 .LL_93%=
.LL_94%=:
v_and_b32 v12, 2047, v12
v_lshrrev_b32 v13, 4, v12
buffer_load_dwordx2 v[28:29], v3, s[8:11], s53 offen offset:0 sc1
s_mul_i32 s63, s61, 20480
s_mul_i32 s80, s58, 320
s_add_u32 s63, s63, s80
s_lshr_b32 s80, s61, 2
s_mul_i32 s64, s80, 81920
s_mul_i32 s80, s58, 20480
s_add_u32 s64, s64, s80
s_and_b32 s80, s61, 3
s_lshl_b32 s80, s80, 10
s_add_u32 s64, s64, s80
s_add_u32 s64, s64, 131072
s_lshl_b32 s94, s14, 6
s_add_u32 s94, s94, s63
s_lshl_b32 s95, s14, 12
s_add_u32 s95, s95, s64
s_add_u32 s64, s64, 16384
buffer_load_dwordx4 v[32:35], v2, s[40:43], s94 offen
buffer_load_dwordx4 v[64:67], v1, s[8:11], s95 offen sc1
buffer_load_dwordx4 v[48:51], v2, s[40:43], s63 offen offset:256
buffer_load_dwordx4 v[80:83], v1, s[8:11], s64 offen sc1
s_lshl_b32 s62, s14, 10
s_add_u32 s62, s62, s52
s_waitcnt vmcnt(4)
v_cmp_eq_u64 vcc, s[12:13], v[28:29]
s_cmp_eq_u64 vcc, -1
s_cbranch_scc0 .Lspin_95%=
.Lok_96%=:
s_cmp_eq_u32 s14, 0
s_cbranch_scc0 .Lone_98%=
s_add_u32 s80, s62, 0
buffer_load_dwordx4 v[96:99], v1, s[8:11], s80 offen sc1
s_add_u32 s80, s52, 4096
buffer_load_dwordx4 v[128:131], v1, s[8:11], s80 offen sc1
s_add_u32 s80, s62, 5120
buffer_load_dwordx4 v[100:103], v1, s[8:11], s80 offen sc1
s_add_u32 s80, s52, 9216
buffer_load_dwordx4 v[132:135], v1, s[8:11], s80 offen sc1
s_add_u32 s80, s62, 10240
buffer_load_dwordx4 v[104:107], v1, s[8:11], s80 offen sc1
s_add_u32 s80, s52, 14336
buffer_load_dwordx4 v[136:139], v1, s[8:11], s80 offen sc1
s_add_u32 s80, s62, 15360
)"
R"(buffer_load_dwordx4 v[108:111], v1, s[8:11], s80 offen sc1
s_add_u32 s80, s52, 19456
buffer_load_dwordx4 v[140:143], v1, s[8:11], s80 offen sc1
s_add_u32 s80, s62, 20480
buffer_load_dwordx4 v[112:115], v1, s[8:11], s80 offen sc1
s_add_u32 s80, s52, 24576
buffer_load_dwordx4 v[144:147], v1, s[8:11], s80 offen sc1
s_add_u32 s80, s62, 25600
buffer_load_dwordx4 v[116:119], v1, s[8:11], s80 offen sc1
s_add_u32 s80, s52, 29696
buffer_load_dwordx4 v[148:151], v1, s[8:11], s80 offen sc1
s_add_u32 s80, s62, 30720
buffer_load_dwordx4 v[120:123], v1, s[8:11], s80 offen sc1
s_add_u32 s80, s52, 34816
buffer_load_dwordx4 v[152:155], v1, s[8:11], s80 offen sc1
s_add_u32 s80, s62, 35840
buffer_load_dwordx4 v[124:127], v1, s[8:11], s80 offen sc1
s_add_u32 s80, s52, 39936
buffer_load_dwordx4 v[156:159], v1, s[8:11], s80 offen sc1
s_waitcnt vmcnt(16)
v_lshlrev_b32 v36, 16, v32
v_and_b32 v37, 0xffff0000, v32
v_lshlrev_b32 v38, 16, v33
v_and_b32 v39, 0xffff0000, v33
v_lshlrev_b32 v40, 16, v34
v_and_b32 v41, 0xffff0000, v34
v_lshlrev_b32 v42, 16, v35
v_and_b32 v43, 0xffff0000, v35
v_lshlrev_b32 v68, 16, v64
v_and_b32 v69, 0xffff0000, v64
v_lshlrev_b32 v70, 16, v65
v_and_b32 v71, 0xffff0000, v65
v_lshlrev_b32 v72, 16, v66
v_and_b32 v73, 0xffff0000, v66
v_lshlrev_b32 v74, 16, v67
v_and_b32 v75, 0xffff0000, v67
v_lshlrev_b32 v52, 16, v48
v_and_b32 v53, 0xffff0000, v48
v_lshlrev_b32 v54, 16, v49
v_and_b32 v55, 0xffff0000, v49
v_lshlrev_b32 v56, 16, v50
v_and_b32 v57, 0xffff0000, v50
v_lshlrev_b32 v58, 16, v51
v_and_b32 v59, 0xffff0000, v51
v_lshlrev_b32 v84, 16, v80
v_and_b32 v85, 0xffff0000, v80
v_lshlrev_b32 v86, 16, v81
v_and_b32 v87, 0xffff0000, v81
v_lshlrev_b32 v88, 16, v82
v_and_b32 v89, 0xffff0000, v82
v_lshlrev_b32 v90, 16, v83
v_and_b32 v91, 0xffff0000, v83
v_cmp_eq_u32 vcc, s61, v13
s_and_b32 s65, vcc_lo, 0xff
s_cbranch_scc1 .Lup_100%=
.Lupr_101%=:
s_waitcnt vmcnt(14)
Zvvv 160,4,96
Zvvv 164,8,96
Zvvv 168,4,128
Zvvv 172,8,128
s_waitcnt vmcnt(12)
Mvvv 160,4,100
Mvvv 164,8,100
Mvvv 168,4,132
Mvvv 172,8,132
s_waitcnt vmcnt(10)
Mvvv 160,4,104
Mvvv 164,8,104
Mvvv 168,4,136
Mvvv 172,8,136
s_waitcnt vmcnt(8)
Mvvv 160,4,108
Mvvv 164,8,108
Mvvv 168,4,140
Mvvv 172,8,140
s_waitcnt vmcnt(6)
Mvvv 160,4,112
Mvvv 164,8,112
Mvvv 168,4,144
Mvvv 172,8,144
s_waitcnt vmcnt(4)
Mvvv 160,4,116
Mvvv 164,8,116
Mvvv 168,4,148
Mvvv 172,8,148
s_waitcnt vmcnt(2)
Mvvv 160,4,120
Mvvv 164,8,120
Mvvv 168,4,152
Mvvv 172,8,152
s_waitcnt vmcnt(0)
Mvvv 160,4,124
Mvvv 164,8,124
Mvvv 168,4,156
Mvvv 172,8,156
s_nop 4
K 176,160,161
K 177,162,163
K 178,164,165
K 179,166,167
v_lshlrev_b32 v160, 16, v176
v_and_b32 v161, 0xffff0000, v176
v_lshlrev_b32 v162, 16, v177
v_and_b32 v163, 0xffff0000, v177
v_lshlrev_b32 v164, 16, v178
v_and_b32 v165, 0xffff0000, v178
v_lshlrev_b32 v166, 16, v179
v_and_b32 v167, 0xffff0000, v179
A 160,36,160
A 161,37,161
A 162,38,162
A 163,39,163
A 164,40,164
A 165,41,165
A 166,42,166
A 167,43,167
K 176,160,161
K 177,162,163
K 178,164,165
K 179,166,167
v_lshlrev_b32 v160, 16, v176
v_and_b32 v161, 0xffff0000, v176
v_lshlrev_b32 v162, 16, v177
v_and_b32 v163, 0xffff0000, v177
v_lshlrev_b32 v164, 16, v178
v_and_b32 v165, 0xffff0000, v178
v_lshlrev_b32 v166, 16, v179
v_and_b32 v167, 0xffff0000, v179
A 160,68,160
A 161,69,161
A 162,70,162
A 163,71,163
A 164,72,164
A 165,73,165
A 166,74,166
A 167,75,167
K 160,160,161
K 161,162,163
K 162,164,165
K 163,166,167
buffer_store_dwordx4 v[160:163], v2, s[44:47], s94 offen
K 176,168,169
K 177,170,171
K 178,172,173
K 179,174,175
v_lshlrev_b32 v168, 16, v176
v_and_b32 v169, 0xffff0000, v176
v_lshlrev_b32 v170, 16, v177
v_and_b32 v171, 0xffff0000, v177
v_lshlrev_b32 v172, 16, v178
v_and_b32 v173, 0xffff0000, v178
v_lshlrev_b32 v174, 16, v179
v_and_b32 v175, 0xffff0000, v179
A 168,52,168
A 169,53,169
A 170,54,170
A 171,55,171
A 172,56,172
A 173,57,173
A 174,58,174
A 175,59,175
K 176,168,169
K 177,170,171
K 178,172,173
K 179,174,175
v_lshlrev_b32 v168, 16, v176
v_and_b32 v169, 0xffff0000, v176
v_lshlrev_b32 v170, 16, v177
v_and_b32 v171, 0xffff0000, v177
v_lshlrev_b32 v172, 16, v178
v_and_b32 v173, 0xffff0000, v178
v_lshlrev_b32 v174, 16, v179
v_and_b32 v175, 0xffff0000, v179
A 168,84,168
A 169,85,169
A 170,86,170
A 171,87,171
A 172,88,172
A 173,89,173
A 174,90,174
A 175,91,175
K 168,168,169
K 169,170,171
K 170,172,173
K 171,174,175
buffer_store_dwordx4 v[168:171], v2, s[44:47], s63 offen offset:256
s_branch .Ljoin_99%=
.Lone_98%=:
s_add_u32 s80, s62, 0
buffer_load_dwordx4 v[96:99], v1, s[8:11], s80 offen sc1
s_add_u32 s80, s62, 5120
buffer_load_dwordx4 v[100:103], v1, s[8:11], s80 offen sc1
s_add_u32 s80, s62, 10240
buffer_load_dwordx4 v[104:107], v1, s[8:11], s80 offen sc1
s_add_u32 s80, s62, 15360
buffer_load_dwordx4 v[108:111], v1, s[8:11], s80 offen sc1
s_add_u32 s80, s62, 20480
buffer_load_dwordx4 v[112:115], v1, s[8:11], s80 offen sc1
s_add_u32 s80, s62, 25600
buffer_load_dwordx4 v[116:119], v1, s[8:11], s80 offen sc1
s_add_u32 s80, s62, 30720
buffer_load_dwordx4 v[120:123], v1, s[8:11], s80 offen sc1
s_add_u32 s80, s62, 35840
buffer_load_dwordx4 v[124:127], v1, s[8:11], s80 offen sc1
s_waitcnt vmcnt(8)
v_lshlrev_b32 v36, 16, v32
v_and_b32 v37, 0xffff0000, v32
v_lshlrev_b32 v38, 16, v33
v_and_b32 v39, 0xffff0000, v33
v_lshlrev_b32 v40, 16, v34
v_and_b32 v41, 0xffff0000, v34
v_lshlrev_b32 v42, 16, v35
v_and_b32 v43, 0xffff0000, v35
v_lshlrev_b32 v68, 16, v64
v_and_b32 v69, 0xffff0000, v64
v_lshlrev_b32 v70, 16, v65
v_and_b32 v71, 0xffff0000, v65
v_lshlrev_b32 v72, 16, v66
v_and_b32 v73, 0xffff0000, v66
v_lshlrev_b32 v74, 16, v67
v_and_b32 v75, 0xffff0000, v67
v_cmp_eq_u32 vcc, s61, v13
s_and_b32 s65, vcc_lo, 0xff
s_cbranch_scc1 .Lup_105%=
.Lupr_106%=:
s_waitcnt vmcnt(7)
Zvvv 160,4,96
Zvvv 164,8,96
s_waitcnt vmcnt(6)
Mvvv 160,4,100
Mvvv 164,8,100
s_waitcnt vmcnt(5)
Mvvv 160,4,104
Mvvv 164,8,104
s_waitcnt vmcnt(4)
Mvvv 160,4,108
Mvvv 164,8,108
s_waitcnt vmcnt(3)
Mvvv 160,4,112
Mvvv 164,8,112
s_waitcnt vmcnt(2)
Mvvv 160,4,116
Mvvv 164,8,116
s_waitcnt vmcnt(1)
Mvvv 160,4,120
Mvvv 164,8,120
s_waitcnt vmcnt(0)
Mvvv 160,4,124
Mvvv 164,8,124
s_nop 6
K 176,160,161
K 177,162,163
K 178,164,165
K 179,166,167
v_lshlrev_b32 v160, 16, v176
v_and_b32 v161, 0xffff0000, v176
v_lshlrev_b32 v162, 16, v177
v_and_b32 v163, 0xffff0000, v177
v_lshlrev_b32 v164, 16, v178
v_and_b32 v165, 0xffff0000, v178
v_lshlrev_b32 v166, 16, v179
v_and_b32 v167, 0xffff0000, v179
A 160,36,160
A 161,37,161
A 162,38,162
A 163,39,163
A 164,40,164
A 165,41,165
A 166,42,166
A 167,43,167
K 176,160,161
K 177,162,163
K 178,164,165
K 179,166,167
v_lshlrev_b32 v160, 16, v176
v_and_b32 v161, 0xffff0000, v176
v_lshlrev_b32 v162, 16, v177
v_and_b32 v163, 0xffff0000, v177
v_lshlrev_b32 v164, 16, v178
v_and_b32 v165, 0xffff0000, v178
v_lshlrev_b32 v166, 16, v179
v_and_b32 v167, 0xffff0000, v179
A 160,68,160
A 161,69,161
A 162,70,162
A 163,71,163
A 164,72,164
A 165,73,165
A 166,74,166
A 167,75,167
K 160,160,161
K 161,162,163
K 162,164,165
K 163,166,167
buffer_store_dwordx4 v[160:163], v2, s[44:47], s94 offen
.Ljoin_99%=:
buffer_load_dwordx2 v[28:29], v3, s[8:11], s53 offen offset:512 sc1
s_xor_b32 s61, s61, 124
s_mul_i32 s63, s61, 20480
s_mul_i32 s80, s58, 320
s_add_u32 s63, s63, s80
s_lshr_b32 s80, s61, 2
s_mul_i32 s64, s80, 81920
s_mul_i32 s80, s58, 20480
s_add_u32 s64, s64, s80
s_and_b32 s80, s61, 3
s_lshl_b32 s80, s80, 10
s_add_u32 s64, s64, s80
s_add_u32 s64, s64, 131072
s_lshl_b32 s94, s14, 6
s_add_u32 s94, s94, s63
s_lshl_b32 s95, s14, 12
s_add_u32 s95, s95, s64
s_add_u32 s64, s64, 16384
buffer_load_dwordx4 v[32:35], v2, s[40:43], s94 offen
buffer_load_dwordx4 v[64:67], v1, s[8:11], s95 offen sc1
buffer_load_dwordx4 v[48:51], v2, s[40:43], s63 offen offset:256
buffer_load_dwordx4 v[80:83], v1, s[8:11], s64 offen sc1
s_lshl_b32 s62, s14, 10
s_add_u32 s62, s62, s52
s_waitcnt vmcnt(4)
v_cmp_eq_u64 vcc, s[12:13], v[28:29]
s_cmp_eq_u64 vcc, -1
s_cbranch_scc0 .Lspin_110%=
.Lok_111%=:
s_cmp_eq_u32 s14, 1
s_cbranch_scc0 .Lone_113%=
s_add_u32 s80, s62, 327680
buffer_load_dwordx4 v[96:99], v1, s[8:11], s80 offen sc1
s_add_u32 s80, s52, 331776
buffer_load_dwordx4 v[128:131], v1, s[8:11], s80 offen sc1
s_add_u32 s80, s62, 332800
buffer_load_dwordx4 v[100:103], v1, s[8:11], s80 offen sc1
s_add_u32 s80, s52, 336896
buffer_load_dwordx4 v[132:135], v1, s[8:11], s80 offen sc1
s_add_u32 s80, s62, 337920
buffer_load_dwordx4 v[104:107], v1, s[8:11], s80 offen sc1
s_add_u32 s80, s52, 342016
buffer_load_dwordx4 v[136:139], v1, s[8:11], s80 offen sc1
s_add_u32 s80, s62, 343040
buffer_load_dwordx4 v[108:111], v1, s[8:11], s80 offen sc1
s_add_u32 s80, s52, 347136
buffer_load_dwordx4 v[140:143], v1, s[8:11], s80 offen sc1
s_add_u32 s80, s62, 348160
buffer_load_dwordx4 v[112:115], v1, s[8:11], s80 offen sc1
s_add_u32 s80, s52, 352256
buffer_load_dwordx4 v[144:147], v1, s[8:11], s80 offen sc1
s_add_u32 s80, s62, 353280
buffer_load_dwordx4 v[116:119], v1, s[8:11], s80 offen sc1
s_add_u32 s80, s52, 357376
buffer_load_dwordx4 v[148:151], v1, s[8:11], s80 offen sc1
s_add_u32 s80, s62, 358400
buffer_load_dwordx4 v[120:123], v1, s[8:11], s80 offen sc1
s_add_u32 s80, s52, 362496
buffer_load_dwordx4 v[152:155], v1, s[8:11], s80 offen sc1
s_add_u32 s80, s62, 363520
buffer_load_dwordx4 v[124:127], v1, s[8:11], s80 offen sc1
s_add_u32 s80, s52, 367616
buffer_load_dwordx4 v[156:159], v1, s[8:11], s80 offen sc1
s_waitcnt vmcnt(16)
v_lshlrev_b32 v36, 16, v32
v_and_b32 v37, 0xffff0000, v32
v_lshlrev_b32 v38, 16, v33
v_and_b32 v39, 0xffff0000, v33
v_lshlrev_b32 v40, 16, v34
v_and_b32 v41, 0xffff0000, v34
v_lshlrev_b32 v42, 16, v35
v_and_b32 v43, 0xffff0000, v35
v_lshlrev_b32 v68, 16, v64
v_and_b32 v69, 0xffff0000, v64
v_lshlrev_b32 v70, 16, v65
v_and_b32 v71, 0xffff0000, v65
v_lshlrev_b32 v72, 16, v66
v_and_b32 v73, 0xffff0000, v66
v_lshlrev_b32 v74, 16, v67
v_and_b32 v75, 0xffff0000, v67
v_lshlrev_b32 v52, 16, v48
v_and_b32 v53, 0xffff0000, v48
v_lshlrev_b32 v54, 16, v49
v_and_b32 v55, 0xffff0000, v49
v_lshlrev_b32 v56, 16, v50
v_and_b32 v57, 0xffff0000, v50
v_lshlrev_b32 v58, 16, v51
v_and_b32 v59, 0xffff0000, v51
v_lshlrev_b32 v84, 16, v80
v_and_b32 v85, 0xffff0000, v80
v_lshlrev_b32 v86, 16, v81
v_and_b32 v87, 0xffff0000, v81
v_lshlrev_b32 v88, 16, v82
v_and_b32 v89, 0xffff0000, v82
v_lshlrev_b32 v90, 16, v83
v_and_b32 v91, 0xffff0000, v83
v_cmp_eq_u32 vcc, s61, v13
s_and_b32 s65, vcc_lo, 0xff
s_cbranch_scc1 .Lup_115%=
.Lupr_116%=:
s_waitcnt vmcnt(14)
Zvvv 160,4,96
Zvvv 164,8,96
)"
R"(Zvvv 168,4,128
Zvvv 172,8,128
s_waitcnt vmcnt(12)
Mvvv 160,4,100
Mvvv 164,8,100
Mvvv 168,4,132
Mvvv 172,8,132
s_waitcnt vmcnt(10)
Mvvv 160,4,104
Mvvv 164,8,104
Mvvv 168,4,136
Mvvv 172,8,136
s_waitcnt vmcnt(8)
Mvvv 160,4,108
Mvvv 164,8,108
Mvvv 168,4,140
Mvvv 172,8,140
s_waitcnt vmcnt(6)
Mvvv 160,4,112
Mvvv 164,8,112
Mvvv 168,4,144
Mvvv 172,8,144
s_waitcnt vmcnt(4)
Mvvv 160,4,116
Mvvv 164,8,116
Mvvv 168,4,148
Mvvv 172,8,148
s_waitcnt vmcnt(2)
Mvvv 160,4,120
Mvvv 164,8,120
Mvvv 168,4,152
Mvvv 172,8,152
s_waitcnt vmcnt(0)
Mvvv 160,4,124
Mvvv 164,8,124
Mvvv 168,4,156
Mvvv 172,8,156
s_nop 4
K 176,160,161
K 177,162,163
K 178,164,165
K 179,166,167
v_lshlrev_b32 v160, 16, v176
v_and_b32 v161, 0xffff0000, v176
v_lshlrev_b32 v162, 16, v177
v_and_b32 v163, 0xffff0000, v177
v_lshlrev_b32 v164, 16, v178
v_and_b32 v165, 0xffff0000, v178
v_lshlrev_b32 v166, 16, v179
v_and_b32 v167, 0xffff0000, v179
A 160,36,160
A 161,37,161
A 162,38,162
A 163,39,163
A 164,40,164
A 165,41,165
A 166,42,166
A 167,43,167
K 176,160,161
K 177,162,163
K 178,164,165
K 179,166,167
v_lshlrev_b32 v160, 16, v176
v_and_b32 v161, 0xffff0000, v176
v_lshlrev_b32 v162, 16, v177
v_and_b32 v163, 0xffff0000, v177
v_lshlrev_b32 v164, 16, v178
v_and_b32 v165, 0xffff0000, v178
v_lshlrev_b32 v166, 16, v179
v_and_b32 v167, 0xffff0000, v179
A 160,68,160
A 161,69,161
A 162,70,162
A 163,71,163
A 164,72,164
A 165,73,165
A 166,74,166
A 167,75,167
K 160,160,161
K 161,162,163
K 162,164,165
K 163,166,167
buffer_store_dwordx4 v[160:163], v2, s[44:47], s94 offen
K 176,168,169
K 177,170,171
K 178,172,173
K 179,174,175
v_lshlrev_b32 v168, 16, v176
v_and_b32 v169, 0xffff0000, v176
v_lshlrev_b32 v170, 16, v177
v_and_b32 v171, 0xffff0000, v177
v_lshlrev_b32 v172, 16, v178
v_and_b32 v173, 0xffff0000, v178
v_lshlrev_b32 v174, 16, v179
v_and_b32 v175, 0xffff0000, v179
A 168,52,168
A 169,53,169
A 170,54,170
A 171,55,171
A 172,56,172
A 173,57,173
A 174,58,174
A 175,59,175
K 176,168,169
K 177,170,171
K 178,172,173
K 179,174,175
v_lshlrev_b32 v168, 16, v176
v_and_b32 v169, 0xffff0000, v176
v_lshlrev_b32 v170, 16, v177
v_and_b32 v171, 0xffff0000, v177
v_lshlrev_b32 v172, 16, v178
v_and_b32 v173, 0xffff0000, v178
v_lshlrev_b32 v174, 16, v179
v_and_b32 v175, 0xffff0000, v179
A 168,84,168
A 169,85,169
A 170,86,170
A 171,87,171
A 172,88,172
A 173,89,173
A 174,90,174
A 175,91,175
K 168,168,169
K 169,170,171
K 170,172,173
K 171,174,175
buffer_store_dwordx4 v[168:171], v2, s[44:47], s63 offen offset:256
s_branch .Ljoin_114%=
.Lone_113%=:
s_add_u32 s80, s62, 327680
buffer_load_dwordx4 v[96:99], v1, s[8:11], s80 offen sc1
s_add_u32 s80, s62, 332800
buffer_load_dwordx4 v[100:103], v1, s[8:11], s80 offen sc1
s_add_u32 s80, s62, 337920
buffer_load_dwordx4 v[104:107], v1, s[8:11], s80 offen sc1
s_add_u32 s80, s62, 343040
buffer_load_dwordx4 v[108:111], v1, s[8:11], s80 offen sc1
s_add_u32 s80, s62, 348160
buffer_load_dwordx4 v[112:115], v1, s[8:11], s80 offen sc1
s_add_u32 s80, s62, 353280
buffer_load_dwordx4 v[116:119], v1, s[8:11], s80 offen sc1
s_add_u32 s80, s62, 358400
buffer_load_dwordx4 v[120:123], v1, s[8:11], s80 offen sc1
s_add_u32 s80, s62, 363520
buffer_load_dwordx4 v[124:127], v1, s[8:11], s80 offen sc1
s_waitcnt vmcnt(8)
v_lshlrev_b32 v36, 16, v32
v_and_b32 v37, 0xffff0000, v32
v_lshlrev_b32 v38, 16, v33
v_and_b32 v39, 0xffff0000, v33
v_lshlrev_b32 v40, 16, v34
v_and_b32 v41, 0xffff0000, v34
v_lshlrev_b32 v42, 16, v35
v_and_b32 v43, 0xffff0000, v35
v_lshlrev_b32 v68, 16, v64
v_and_b32 v69, 0xffff0000, v64
v_lshlrev_b32 v70, 16, v65
v_and_b32 v71, 0xffff0000, v65
v_lshlrev_b32 v72, 16, v66
v_and_b32 v73, 0xffff0000, v66
v_lshlrev_b32 v74, 16, v67
v_and_b32 v75, 0xffff0000, v67
v_cmp_eq_u32 vcc, s61, v13
s_and_b32 s65, vcc_lo, 0xff
s_cbranch_scc1 .Lup_120%=
.Lupr_121%=:
s_waitcnt vmcnt(7)
Zvvv 160,4,96
Zvvv 164,8,96
s_waitcnt vmcnt(6)
Mvvv 160,4,100
Mvvv 164,8,100
s_waitcnt vmcnt(5)
Mvvv 160,4,104
Mvvv 164,8,104
s_waitcnt vmcnt(4)
Mvvv 160,4,108
Mvvv 164,8,108
s_waitcnt vmcnt(3)
Mvvv 160,4,112
Mvvv 164,8,112
s_waitcnt vmcnt(2)
Mvvv 160,4,116
Mvvv 164,8,116
s_waitcnt vmcnt(1)
Mvvv 160,4,120
Mvvv 164,8,120
s_waitcnt vmcnt(0)
Mvvv 160,4,124
Mvvv 164,8,124
s_nop 6
K 176,160,161
K 177,162,163
K 178,164,165
K 179,166,167
v_lshlrev_b32 v160, 16, v176
v_and_b32 v161, 0xffff0000, v176
v_lshlrev_b32 v162, 16, v177
v_and_b32 v163, 0xffff0000, v177
v_lshlrev_b32 v164, 16, v178
v_and_b32 v165, 0xffff0000, v178
v_lshlrev_b32 v166, 16, v179
v_and_b32 v167, 0xffff0000, v179
A 160,36,160
A 161,37,161
A 162,38,162
A 163,39,163
A 164,40,164
A 165,41,165
A 166,42,166
A 167,43,167
K 176,160,161
K 177,162,163
K 178,164,165
K 179,166,167
v_lshlrev_b32 v160, 16, v176
v_and_b32 v161, 0xffff0000, v176
v_lshlrev_b32 v162, 16, v177
v_and_b32 v163, 0xffff0000, v177
v_lshlrev_b32 v164, 16, v178
v_and_b32 v165, 0xffff0000, v178
v_lshlrev_b32 v166, 16, v179
v_and_b32 v167, 0xffff0000, v179
A 160,68,160
A 161,69,161
A 162,70,162
A 163,71,163
A 164,72,164
A 165,73,165
A 166,74,166
A 167,75,167
K 160,160,161
K 161,162,163
K 162,164,165
K 163,166,167
buffer_store_dwordx4 v[160:163], v2, s[44:47], s94 offen
.Ljoin_114%=:
s_lshl_b32 s80, s15, 2
s_add_u32 s80, s80, s14
s_cmp_eq_u32 s80, 4
s_cbranch_scc0 .LL_125%=
v_mov_b64 v[28:29], s[12:13]
s_mov_b64 exec, 1
buffer_store_dwordx2 v[28:29], off, s[8:11], 0 sc1
s_mov_b64 exec, -1
.LL_125%=:
s_branch .Lend_126%=
.Lpub_6%=:
s_lshl_b32 s80, s15, 3
s_add_u32 s80, s80, 256
v_mov_b64 v[28:29], s[12:13]
s_mov_b64 exec, 1
buffer_store_dwordx2 v[28:29], off, s[8:11], s80 sc1
s_mov_b64 exec, -1
s_nop 0
s_branch .Lpubr_7%=
.Lspin_8%=:
s_mov_b32 s84, 0
.Llp_10%=:
s_sleep 2
v_bfe_u32 v28, v1, 4, 3
v_lshlrev_b32 v28, 3, v28
s_lshl_b32 s80, s60, 6
s_add_u32 s80, s80, 256
buffer_load_dwordx2 v[28:29], v28, s[8:11], s80 offen sc1
s_waitcnt vmcnt(0)
s_add_u32 s84, s84, 1
v_cmp_eq_u64 vcc, s[12:13], v[28:29]
s_cmp_eq_u64 vcc, -1
s_cbranch_scc1 .Lok_9%=
s_cmp_lt_u32 s84, 1048576
s_cbranch_scc1 .Llp_10%=
s_branch .Lok_9%=
.Lpub_11%=:
s_lshl_b32 s80, s15, 3
s_add_u32 s80, s80, 256
v_mov_b64 v[28:29], s[12:13]
s_mov_b64 exec, 1
buffer_store_dwordx2 v[28:29], off, s[8:11], s80 sc1
s_mov_b64 exec, -1
s_nop 0
s_branch .Lpubr_12%=
.Lspin_13%=:
s_mov_b32 s84, 0
.Llp_15%=:
s_sleep 2
v_bfe_u32 v28, v1, 4, 3
v_lshlrev_b32 v28, 3, v28
s_lshl_b32 s80, s60, 6
s_add_u32 s80, s80, 256
buffer_load_dwordx2 v[28:29], v28, s[8:11], s80 offen sc1
s_waitcnt vmcnt(0)
s_add_u32 s84, s84, 1
v_cmp_eq_u64 vcc, s[12:13], v[28:29]
s_cmp_eq_u64 vcc, -1
s_cbranch_scc1 .Lok_14%=
s_cmp_lt_u32 s84, 1048576
s_cbranch_scc1 .Llp_15%=
s_branch .Lok_14%=
.Lrt_16%=:
v_lshrrev_b32 v136, 1, v1
buffer_load_dwordx2 v[96:97], v136, s[8:11], 0 offen offset:256 sc1
buffer_load_dwordx2 v[98:99], v136, s[8:11], 0 offen offset:768 sc1
buffer_load_dwordx2 v[100:101], v136, s[8:11], 0 offen offset:1280 sc1
buffer_load_dwordx2 v[102:103], v136, s[8:11], 0 offen offset:1792 sc1
s_branch .Lrtr_17%=
.Lrt_18%=:
s_mov_b32 s84, 0
.LL_20%=:
s_waitcnt vmcnt(0)
s_mov_b64 s[86:87], -1
v_cmp_eq_u64 vcc, s[12:13], v[96:97]
s_and_b64 s[86:87], s[86:87], vcc
v_cmp_eq_u64 vcc, s[12:13], v[98:99]
s_and_b64 s[86:87], s[86:87], vcc
v_cmp_eq_u64 vcc, s[12:13], v[100:101]
s_and_b64 s[86:87], s[86:87], vcc
v_cmp_eq_u64 vcc, s[12:13], v[102:103]
s_and_b64 s[86:87], s[86:87], vcc
s_cmp_eq_u64 s[86:87], -1
s_cbranch_scc1 .LL_21%=
s_sleep 2
buffer_load_dwordx2 v[96:97], v136, s[8:11], 0 offen offset:256 sc1
buffer_load_dwordx2 v[98:99], v136, s[8:11], 0 offen offset:768 sc1
buffer_load_dwordx2 v[100:101], v136, s[8:11], 0 offen offset:1280 sc1
buffer_load_dwordx2 v[102:103], v136, s[8:11], 0 offen offset:1792 sc1
s_add_u32 s84, s84, 1
s_cmp_lt_u32 s84, 1048576
s_cbranch_scc1 .LL_20%=
.LL_21%=:
s_mov_b32 s84, 2752512
buffer_load_dwordx4 v[104:107], v1, s[8:11], s84 offen offset:0 sc1
buffer_load_dwordx4 v[108:111], v1, s[8:11], s84 offen offset:1024 sc1
buffer_load_dwordx4 v[112:115], v1, s[8:11], s84 offen offset:2048 sc1
buffer_load_dwordx4 v[116:119], v1, s[8:11], s84 offen offset:3072 sc1
s_mov_b32 s84, 2756608
buffer_load_dwordx4 v[120:123], v1, s[8:11], s84 offen offset:0 sc1
buffer_load_dwordx4 v[124:127], v1, s[8:11], s84 offen offset:1024 sc1
buffer_load_dwordx4 v[128:131], v1, s[8:11], s84 offen offset:2048 sc1
buffer_load_dwordx4 v[132:135], v1, s[8:11], s84 offen offset:3072 sc1
s_branch .Lrtr_19%=
.Lrt_22%=:
s_waitcnt vmcnt(0)
v_mov_b32 v96, v104
v_mov_b32 v97, 0
v_mov_b32 v98, 0
v_mov_b32 v100, 0
v_med3_u32 v98, v97, v98, v105
v_med3_u32 v97, v96, v97, v105
v_max_u32 v96, v96, v105
v_med3_u32 v98, v97, v98, v106
v_med3_u32 v97, v96, v97, v106
v_max_u32 v96, v96, v106
v_med3_u32 v98, v97, v98, v107
v_med3_u32 v97, v96, v97, v107
v_max_u32 v96, v96, v107
v_med3_u32 v98, v97, v98, v108
v_med3_u32 v97, v96, v97, v108
v_max_u32 v96, v96, v108
v_med3_u32 v98, v97, v98, v109
v_med3_u32 v97, v96, v97, v109
v_max_u32 v96, v96, v109
v_med3_u32 v98, v97, v98, v110
v_med3_u32 v97, v96, v97, v110
v_max_u32 v96, v96, v110
v_med3_u32 v98, v97, v98, v111
v_med3_u32 v97, v96, v97, v111
v_max_u32 v96, v96, v111
v_med3_u32 v98, v97, v98, v112
v_med3_u32 v97, v96, v97, v112
v_max_u32 v96, v96, v112
v_med3_u32 v98, v97, v98, v113
v_med3_u32 v97, v96, v97, v113
v_max_u32 v96, v96, v113
v_med3_u32 v98, v97, v98, v114
v_med3_u32 v97, v96, v97, v114
v_max_u32 v96, v96, v114
v_med3_u32 v98, v97, v98, v115
v_med3_u32 v97, v96, v97, v115
v_max_u32 v96, v96, v115
v_med3_u32 v98, v97, v98, v116
v_med3_u32 v97, v96, v97, v116
v_max_u32 v96, v96, v116
v_med3_u32 v98, v97, v98, v117
v_med3_u32 v97, v96, v97, v117
v_max_u32 v96, v96, v117
v_med3_u32 v98, v97, v98, v118
v_med3_u32 v97, v96, v97, v118
v_max_u32 v96, v96, v118
v_med3_u32 v98, v97, v98, v119
v_med3_u32 v97, v96, v97, v119
v_max_u32 v96, v96, v119
v_med3_u32 v98, v97, v98, v120
v_med3_u32 v97, v96, v97, v120
v_max_u32 v96, v96, v120
v_med3_u32 v98, v97, v98, v121
v_med3_u32 v97, v96, v97, v121
v_max_u32 v96, v96, v121
v_med3_u32 v98, v97, v98, v122
)"
R"(v_med3_u32 v97, v96, v97, v122
v_max_u32 v96, v96, v122
v_med3_u32 v98, v97, v98, v123
v_med3_u32 v97, v96, v97, v123
v_max_u32 v96, v96, v123
v_med3_u32 v98, v97, v98, v124
v_med3_u32 v97, v96, v97, v124
v_max_u32 v96, v96, v124
v_med3_u32 v98, v97, v98, v125
v_med3_u32 v97, v96, v97, v125
v_max_u32 v96, v96, v125
v_med3_u32 v98, v97, v98, v126
v_med3_u32 v97, v96, v97, v126
v_max_u32 v96, v96, v126
v_med3_u32 v98, v97, v98, v127
v_med3_u32 v97, v96, v97, v127
v_max_u32 v96, v96, v127
v_med3_u32 v98, v97, v98, v128
v_med3_u32 v97, v96, v97, v128
v_max_u32 v96, v96, v128
v_med3_u32 v98, v97, v98, v129
v_med3_u32 v97, v96, v97, v129
v_max_u32 v96, v96, v129
v_med3_u32 v98, v97, v98, v130
v_med3_u32 v97, v96, v97, v130
v_max_u32 v96, v96, v130
v_med3_u32 v98, v97, v98, v131
v_med3_u32 v97, v96, v97, v131
v_max_u32 v96, v96, v131
v_med3_u32 v98, v97, v98, v132
v_med3_u32 v97, v96, v97, v132
v_max_u32 v96, v96, v132
v_med3_u32 v98, v97, v98, v133
v_med3_u32 v97, v96, v97, v133
v_max_u32 v96, v96, v133
v_med3_u32 v98, v97, v98, v134
v_med3_u32 v97, v96, v97, v134
v_max_u32 v96, v96, v134
v_med3_u32 v98, v97, v98, v135
v_med3_u32 v97, v96, v97, v135
v_max_u32 v96, v96, v135
v_mov_b32 v99, v96
s_nop 1
v_max_u32_dpp v99, v99, v99 row_shr:1 row_mask:0xf bank_mask:0xf
s_nop 1
v_max_u32_dpp v99, v99, v99 row_shr:2 row_mask:0xf bank_mask:0xf
s_nop 1
v_max_u32_dpp v99, v99, v99 row_shr:4 row_mask:0xf bank_mask:0xf
s_nop 1
v_max_u32_dpp v99, v99, v99 row_shr:8 row_mask:0xf bank_mask:0xf
s_nop 1
v_max_u32_dpp v99, v99, v99 row_bcast:15 row_mask:0xa bank_mask:0xf
s_nop 1
v_max_u32_dpp v99, v99, v99 row_bcast:31 row_mask:0xc bank_mask:0xf
s_nop 0
v_readlane_b32 s86, v99, 63
s_nop 1
v_cmp_eq_u32 vcc, s86, v96
s_and_b32 s87, s86, 0xffff
s_sub_u32 s87, 2047, s87
v_cndmask_b32 v96, v96, v97, vcc
v_cndmask_b32_e64 v97, v97, 0, vcc
v_writelane_b32 v100, s87, 0
v_mov_b32 v99, v96
s_nop 1
v_max_u32_dpp v99, v99, v99 row_shr:1 row_mask:0xf bank_mask:0xf
s_nop 1
v_max_u32_dpp v99, v99, v99 row_shr:2 row_mask:0xf bank_mask:0xf
s_nop 1
v_max_u32_dpp v99, v99, v99 row_shr:4 row_mask:0xf bank_mask:0xf
s_nop 1
v_max_u32_dpp v99, v99, v99 row_shr:8 row_mask:0xf bank_mask:0xf
s_nop 1
v_max_u32_dpp v99, v99, v99 row_bcast:15 row_mask:0xa bank_mask:0xf
s_nop 1
v_max_u32_dpp v99, v99, v99 row_bcast:31 row_mask:0xc bank_mask:0xf
s_nop 0
v_readlane_b32 s86, v99, 63
s_nop 1
v_cmp_eq_u32 vcc, s86, v96
s_and_b32 s87, s86, 0xffff
s_sub_u32 s87, 2047, s87
v_cndmask_b32 v96, v96, v97, vcc
v_cndmask_b32_e64 v97, v97, 0, vcc
v_writelane_b32 v100, s87, 1
v_mov_b32 v99, v96
s_nop 1
v_max_u32_dpp v99, v99, v99 row_shr:1 row_mask:0xf bank_mask:0xf
s_nop 1
v_max_u32_dpp v99, v99, v99 row_shr:2 row_mask:0xf bank_mask:0xf
s_nop 1
v_max_u32_dpp v99, v99, v99 row_shr:4 row_mask:0xf bank_mask:0xf
s_nop 1
v_max_u32_dpp v99, v99, v99 row_shr:8 row_mask:0xf bank_mask:0xf
s_nop 1
v_max_u32_dpp v99, v99, v99 row_bcast:15 row_mask:0xa bank_mask:0xf
s_nop 1
v_max_u32_dpp v99, v99, v99 row_bcast:31 row_mask:0xc bank_mask:0xf
s_nop 0
v_readlane_b32 s86, v99, 63
s_nop 1
v_cmp_eq_u32 vcc, s86, v96
s_and_b32 s87, s86, 0xffff
s_sub_u32 s87, 2047, s87
v_cndmask_b32 v96, v96, v97, vcc
v_cndmask_b32_e64 v97, v97, 0, vcc
v_writelane_b32 v100, s87, 2
v_mov_b32 v99, v96
s_nop 1
v_max_u32_dpp v99, v99, v99 row_shr:1 row_mask:0xf bank_mask:0xf
s_nop 1
v_max_u32_dpp v99, v99, v99 row_shr:2 row_mask:0xf bank_mask:0xf
s_nop 1
v_max_u32_dpp v99, v99, v99 row_shr:4 row_mask:0xf bank_mask:0xf
s_nop 1
v_max_u32_dpp v99, v99, v99 row_shr:8 row_mask:0xf bank_mask:0xf
s_nop 1
v_max_u32_dpp v99, v99, v99 row_bcast:15 row_mask:0xa bank_mask:0xf
s_nop 1
v_max_u32_dpp v99, v99, v99 row_bcast:31 row_mask:0xc bank_mask:0xf
s_nop 0
v_readlane_b32 s86, v99, 63
s_nop 1
v_cmp_eq_u32 vcc, s86, v96
s_and_b32 s87, s86, 0xffff
s_sub_u32 s87, 2047, s87
v_cndmask_b32 v96, v96, v97, vcc
v_cndmask_b32_e64 v97, v97, 0, vcc
v_writelane_b32 v100, s87, 3
v_mov_b32 v99, v96
s_nop 1
v_max_u32_dpp v99, v99, v99 row_shr:1 row_mask:0xf bank_mask:0xf
s_nop 1
v_max_u32_dpp v99, v99, v99 row_shr:2 row_mask:0xf bank_mask:0xf
s_nop 1
v_max_u32_dpp v99, v99, v99 row_shr:4 row_mask:0xf bank_mask:0xf
s_nop 1
v_max_u32_dpp v99, v99, v99 row_shr:8 row_mask:0xf bank_mask:0xf
s_nop 1
v_max_u32_dpp v99, v99, v99 row_bcast:15 row_mask:0xa bank_mask:0xf
s_nop 1
v_max_u32_dpp v99, v99, v99 row_bcast:31 row_mask:0xc bank_mask:0xf
s_nop 0
v_readlane_b32 s86, v99, 63
s_nop 1
v_cmp_eq_u32 vcc, s86, v96
s_and_b32 s87, s86, 0xffff
s_sub_u32 s87, 2047, s87
v_cndmask_b32 v96, v96, v97, vcc
v_cndmask_b32_e64 v97, v97, 0, vcc
v_writelane_b32 v100, s87, 4
v_mov_b32 v99, v96
s_nop 1
v_max_u32_dpp v99, v99, v99 row_shr:1 row_mask:0xf bank_mask:0xf
s_nop 1
v_max_u32_dpp v99, v99, v99 row_shr:2 row_mask:0xf bank_mask:0xf
s_nop 1
v_max_u32_dpp v99, v99, v99 row_shr:4 row_mask:0xf bank_mask:0xf
s_nop 1
v_max_u32_dpp v99, v99, v99 row_shr:8 row_mask:0xf bank_mask:0xf
s_nop 1
v_max_u32_dpp v99, v99, v99 row_bcast:15 row_mask:0xa bank_mask:0xf
s_nop 1
v_max_u32_dpp v99, v99, v99 row_bcast:31 row_mask:0xc bank_mask:0xf
s_nop 0
v_readlane_b32 s86, v99, 63
s_nop 1
v_cmp_eq_u32 vcc, s86, v96
s_and_b32 s87, s86, 0xffff
s_sub_u32 s87, 2047, s87
v_cndmask_b32 v96, v96, v97, vcc
v_cndmask_b32_e64 v97, v97, 0, vcc
v_writelane_b32 v100, s87, 5
v_mov_b32 v99, v96
s_nop 1
v_max_u32_dpp v99, v99, v99 row_shr:1 row_mask:0xf bank_mask:0xf
s_nop 1
v_max_u32_dpp v99, v99, v99 row_shr:2 row_mask:0xf bank_mask:0xf
s_nop 1
v_max_u32_dpp v99, v99, v99 row_shr:4 row_mask:0xf bank_mask:0xf
s_nop 1
v_max_u32_dpp v99, v99, v99 row_shr:8 row_mask:0xf bank_mask:0xf
s_nop 1
v_max_u32_dpp v99, v99, v99 row_bcast:15 row_mask:0xa bank_mask:0xf
s_nop 1
v_max_u32_dpp v99, v99, v99 row_bcast:31 row_mask:0xc bank_mask:0xf
s_nop 0
v_readlane_b32 s86, v99, 63
s_nop 1
v_cmp_eq_u32 vcc, s86, v96
s_and_b32 s87, s86, 0xffff
s_sub_u32 s87, 2047, s87
v_cndmask_b32 v96, v96, v97, vcc
v_cndmask_b32_e64 v97, v97, 0, vcc
v_writelane_b32 v100, s87, 6
v_mov_b32 v99, v96
s_nop 1
v_max_u32_dpp v99, v99, v99 row_shr:1 row_mask:0xf bank_mask:0xf
s_nop 1
v_max_u32_dpp v99, v99, v99 row_shr:2 row_mask:0xf bank_mask:0xf
s_nop 1
v_max_u32_dpp v99, v99, v99 row_shr:4 row_mask:0xf bank_mask:0xf
s_nop 1
v_max_u32_dpp v99, v99, v99 row_shr:8 row_mask:0xf bank_mask:0xf
s_nop 1
v_max_u32_dpp v99, v99, v99 row_bcast:15 row_mask:0xa bank_mask:0xf
s_nop 1
v_max_u32_dpp v99, v99, v99 row_bcast:31 row_mask:0xc bank_mask:0xf
s_nop 0
v_readlane_b32 s86, v99, 63
s_nop 1
v_cmp_eq_u32 vcc, s86, v96
s_and_b32 s87, s86, 0xffff
s_sub_u32 s87, 2047, s87
v_cndmask_b32 v96, v96, v97, vcc
v_cndmask_b32_e64 v97, v97, 0, vcc
v_writelane_b32 v100, s87, 7
v_mov_b32 v99, v98
s_nop 1
v_max_u32_dpp v99, v99, v99 row_shr:1 row_mask:0xf bank_mask:0xf
s_nop 1
v_max_u32_dpp v99, v99, v99 row_shr:2 row_mask:0xf bank_mask:0xf
s_nop 1
v_max_u32_dpp v99, v99, v99 row_shr:4 row_mask:0xf bank_mask:0xf
s_nop 1
v_max_u32_dpp v99, v99, v99 row_shr:8 row_mask:0xf bank_mask:0xf
s_nop 1
v_max_u32_dpp v99, v99, v99 row_bcast:15 row_mask:0xa bank_mask:0xf
s_nop 1
v_max_u32_dpp v99, v99, v99 row_bcast:31 row_mask:0xc bank_mask:0xf
s_nop 0
v_readlane_b32 s87, v99, 63
s_nop 0
s_cmp_gt_u32 s87, s86
s_cbranch_scc0 .LL_24%=
s_mov_b32 m0, 0
.LL_25%=:
v_max3_u32 v96, v104, v105, v106
v_max3_u32 v96, v96, v107, v108
v_max3_u32 v96, v96, v109, v110
v_max3_u32 v96, v96, v111, v112
v_max3_u32 v96, v96, v113, v114
v_max3_u32 v96, v96, v115, v116
v_max3_u32 v96, v96, v117, v118
v_max3_u32 v96, v96, v119, v120
v_max3_u32 v96, v96, v121, v122
v_max3_u32 v96, v96, v123, v124
v_max3_u32 v96, v96, v125, v126
v_max3_u32 v96, v96, v127, v128
v_max3_u32 v96, v96, v129, v130
v_max3_u32 v96, v96, v131, v132
v_max3_u32 v96, v96, v133, v134
v_max_u32 v96, v96, v135
v_mov_b32 v99, v96
s_nop 1
v_max_u32_dpp v99, v99, v99 row_shr:1 row_mask:0xf bank_mask:0xf
s_nop 1
v_max_u32_dpp v99, v99, v99 row_shr:2 row_mask:0xf bank_mask:0xf
s_nop 1
v_max_u32_dpp v99, v99, v99 row_shr:4 row_mask:0xf bank_mask:0xf
s_nop 1
v_max_u32_dpp v99, v99, v99 row_shr:8 row_mask:0xf bank_mask:0xf
s_nop 1
v_max_u32_dpp v99, v99, v99 row_bcast:15 row_mask:0xa bank_mask:0xf
s_nop 1
v_max_u32_dpp v99, v99, v99 row_bcast:31 row_mask:0xc bank_mask:0xf
s_nop 0
v_readlane_b32 s86, v99, 63
s_nop 1
v_cmp_eq_u32 vcc, s86, v104
s_nop 1
v_cndmask_b32_e64 v104, v104, 0, vcc
v_cmp_eq_u32 vcc, s86, v105
s_nop 1
v_cndmask_b32_e64 v105, v105, 0, vcc
v_cmp_eq_u32 vcc, s86, v106
s_nop 1
v_cndmask_b32_e64 v106, v106, 0, vcc
v_cmp_eq_u32 vcc, s86, v107
s_nop 1
v_cndmask_b32_e64 v107, v107, 0, vcc
v_cmp_eq_u32 vcc, s86, v108
s_nop 1
v_cndmask_b32_e64 v108, v108, 0, vcc
v_cmp_eq_u32 vcc, s86, v109
s_nop 1
v_cndmask_b32_e64 v109, v109, 0, vcc
v_cmp_eq_u32 vcc, s86, v110
s_nop 1
v_cndmask_b32_e64 v110, v110, 0, vcc
v_cmp_eq_u32 vcc, s86, v111
s_nop 1
v_cndmask_b32_e64 v111, v111, 0, vcc
v_cmp_eq_u32 vcc, s86, v112
s_nop 1
v_cndmask_b32_e64 v112, v112, 0, vcc
v_cmp_eq_u32 vcc, s86, v113
s_nop 1
v_cndmask_b32_e64 v113, v113, 0, vcc
v_cmp_eq_u32 vcc, s86, v114
s_nop 1
v_cndmask_b32_e64 v114, v114, 0, vcc
v_cmp_eq_u32 vcc, s86, v115
s_nop 1
v_cndmask_b32_e64 v115, v115, 0, vcc
v_cmp_eq_u32 vcc, s86, v116
s_nop 1
v_cndmask_b32_e64 v116, v116, 0, vcc
v_cmp_eq_u32 vcc, s86, v117
s_nop 1
v_cndmask_b32_e64 v117, v117, 0, vcc
v_cmp_eq_u32 vcc, s86, v118
s_nop 1
v_cndmask_b32_e64 v118, v118, 0, vcc
v_cmp_eq_u32 vcc, s86, v119
s_nop 1
v_cndmask_b32_e64 v119, v119, 0, vcc
v_cmp_eq_u32 vcc, s86, v120
s_nop 1
v_cndmask_b32_e64 v120, v120, 0, vcc
v_cmp_eq_u32 vcc, s86, v121
s_nop 1
v_cndmask_b32_e64 v121, v121, 0, vcc
v_cmp_eq_u32 vcc, s86, v122
s_nop 1
v_cndmask_b32_e64 v122, v122, 0, vcc
v_cmp_eq_u32 vcc, s86, v123
s_nop 1
v_cndmask_b32_e64 v123, v123, 0, vcc
v_cmp_eq_u32 vcc, s86, v124
s_nop 1
v_cndmask_b32_e64 v124, v124, 0, vcc
v_cmp_eq_u32 vcc, s86, v125
s_nop 1
v_cndmask_b32_e64 v125, v125, 0, vcc
v_cmp_eq_u32 vcc, s86, v126
s_nop 1
v_cndmask_b32_e64 v126, v126, 0, vcc
v_cmp_eq_u32 vcc, s86, v127
s_nop 1
v_cndmask_b32_e64 v127, v127, 0, vcc
v_cmp_eq_u32 vcc, s86, v128
s_nop 1
v_cndmask_b32_e64 v128, v128, 0, vcc
v_cmp_eq_u32 vcc, s86, v129
s_nop 1
v_cndmask_b32_e64 v129, v129, 0, vcc
v_cmp_eq_u32 vcc, s86, v130
s_nop 1
v_cndmask_b32_e64 v130, v130, 0, vcc
v_cmp_eq_u32 vcc, s86, v131
s_nop 1
v_cndmask_b32_e64 v131, v131, 0, vcc
v_cmp_eq_u32 vcc, s86, v132
s_nop 1
v_cndmask_b32_e64 v132, v132, 0, vcc
v_cmp_eq_u32 vcc, s86, v133
s_nop 1
v_cndmask_b32_e64 v133, v133, 0, vcc
v_cmp_eq_u32 vcc, s86, v134
s_nop 1
v_cndmask_b32_e64 v134, v134, 0, vcc
v_cmp_eq_u32 vcc, s86, v135
s_nop 1
v_cndmask_b32_e64 v135, v135, 0, vcc
s_and_b32 s87, s86, 0xffff
s_sub_u32 s87, 2047, s87
s_nop 0
v_writelane_b32 v100, s87, m0
s_add_u32 m0, m0, 1
s_cmp_lt_u32 m0, 8
s_cbranch_scc1 .LL_25%=
.LL_24%=:
v_xor_b32 v102, s12, v100
v_mov_b32 v103, s13
s_mov_b64 exec, 0xff
buffer_store_dwordx2 v[102:103], v136, s[8:11], 0 offen offset:3584 sc1
s_mov_b64 exec, -1
s_branch .Lrtr_23%=
.Lspin_26%=:
s_mov_b32 s84, 0
.Llp_28%=:
s_sleep 2
v_bfe_u32 v28, v1, 4, 5
v_lshlrev_b32 v28, 3, v28
s_lshl_b32 s80, s56, 9
s_add_u32 s80, s80, 4096
buffer_load_dwordx2 v[28:29], v28, s[8:11], s80 offen sc1
s_waitcnt vmcnt(0)
s_add_u32 s84, s84, 1
v_cmp_eq_u64 vcc, s[12:13], v[28:29]
s_cmp_eq_u64 vcc, -1
s_cbranch_scc1 .Lok_27%=
s_cmp_lt_u32 s84, 1048576
s_cbranch_scc1 .Llp_28%=
s_branch .Lok_27%=
.Lspin_54%=:
s_mov_b32 s84, 0
.Llp_56%=:
s_sleep 2
)"
R"(buffer_load_dwordx2 v[8:9], v11, s[8:11], 0 offen offset:3584 sc1
s_waitcnt vmcnt(0)
s_add_u32 s84, s84, 1
v_xor_b32 v8, s12, v8
v_xor_b32 v9, s13, v9
v_lshrrev_b32 v10, 11, v8
v_or_b32 v10, v10, v9
v_cmp_eq_u32_e64 s[86:87], 0, v10
s_cmp_eq_u64 s[86:87], -1
s_cbranch_scc1 .Lok_55%=
s_cmp_lt_u32 s84, 1048576
s_cbranch_scc1 .Llp_56%=
s_branch .Lok_55%=
.Lmk_65%=:
s_nop 7
v_cndmask_b32_e64 v168, v241, v168, s[72:73]
v_cndmask_b32_e64 v169, v241, v169, s[74:75]
v_cndmask_b32_e64 v170, v241, v170, s[76:77]
v_cndmask_b32_e64 v171, v241, v171, s[78:79]
v_cndmask_b32_e64 v188, v241, v188, s[72:73]
v_cndmask_b32_e64 v189, v241, v189, s[74:75]
v_cndmask_b32_e64 v190, v241, v190, s[76:77]
v_cndmask_b32_e64 v191, v241, v191, s[78:79]
v_mov_b32 v184, v241
v_mov_b32 v185, v241
v_mov_b32 v186, v241
v_mov_b32 v187, v241
s_branch .Lmkr_66%=
.Lspin_68%=:
s_mov_b32 s84, 0
.Llp_70%=:
s_sleep 2
v_bfe_u32 v28, v1, 4, 5
v_lshlrev_b32 v28, 3, v28
s_lshl_b32 s80, s56, 9
s_add_u32 s80, s80, 4352
buffer_load_dwordx2 v[28:29], v28, s[8:11], s80 offen sc1
s_waitcnt vmcnt(0)
s_add_u32 s84, s84, 1
v_cmp_eq_u64 vcc, s[12:13], v[28:29]
s_cmp_eq_u64 vcc, -1
s_cbranch_scc1 .Lok_69%=
s_cmp_lt_u32 s84, 1048576
s_cbranch_scc1 .Llp_70%=
s_branch .Lok_69%=
.Lact_44%=:
s_call_b64 s[4:5], .Lhandler%=
s_branch .Lactr_45%=
.Lact_46%=:
s_call_b64 s[4:5], .Lhandler%=
s_branch .Lactr_47%=
.Lact_48%=:
s_call_b64 s[4:5], .Lhandler%=
s_branch .Lactr_49%=
.Lact_50%=:
s_call_b64 s[4:5], .Lhandler%=
s_branch .Lactr_51%=
.Lact_52%=:
s_call_b64 s[4:5], .Lhandler%=
s_branch .Lactr_53%=
.Lact_57%=:
s_call_b64 s[4:5], .Lhandler%=
s_branch .Lactr_58%=
.Lact_59%=:
s_call_b64 s[4:5], .Lhandler%=
s_branch .Lactr_60%=
.Lact_61%=:
s_call_b64 s[4:5], .Lhandler%=
s_branch .Lactr_62%=
.Lact_63%=:
s_call_b64 s[4:5], .Lhandler%=
s_branch .Lactr_64%=
.Lhandler%=:
s_cmp_eq_u32 s64, 2
s_cbranch_scc0 .LL_67%=
s_waitcnt vmcnt(0)
v_cmp_eq_u64 vcc, s[12:13], v[28:29]
s_cmp_eq_u64 vcc, -1
s_cbranch_scc0 .Lspin_68%=
.Lok_69%=:
.LL_67%=:
s_cmp_eq_u32 s64, s70
s_cbranch_scc0 .LL_71%=
s_mov_b32 s70, 0
s_and_b64 s[86:87], s[88:89], s[24:25]
s_cbranch_scc1 .LL_72%=
s_waitcnt vmcnt(0)
v_mov_b64 v[28:29], s[12:13]
s_mov_b64 exec, 1
buffer_store_dwordx2 v[28:29], off, s[8:11], s69 sc1
s_mov_b64 exec, -1
.LL_72%=:
.LL_71%=:
s_cmp_eq_u32 s64, s91
s_cbranch_scc0 .LL_73%=
s_waitcnt vmcnt(0)
v_mov_b64 v[28:29], s[12:13]
s_mov_b64 exec, 1
buffer_store_dwordx2 v[28:29], off, s[8:11], s71 sc1
s_mov_b64 exec, -1
.LL_73%=:
s_cmp_eq_u32 s64, 1
s_cbranch_scc0 .LL_74%=
v_bfe_u32 v28, v1, 4, 5
v_lshlrev_b32 v28, 3, v28
s_lshl_b32 s80, s56, 9
s_add_u32 s80, s80, 4352
buffer_load_dwordx2 v[28:29], v28, s[8:11], s80 offen sc1
.LL_74%=:
s_cmp_eq_u32 s64, 12
s_cbranch_scc0 .LL_75%=
s_lshr_b32 s80, s15, 3
s_lshl_b32 s81, s59, 5
s_add_u32 s80, s80, s81
s_lshl_b32 s80, s80, 2
s_add_u32 s80, s80, s14
s_mul_i32 s80, s80, 5120
v_min_u32 v28, 624, v1
v_lshl_add_u32 v28, v28, 3, s80
global_load_dword v29, v28, s[26:27]
s_lshr_b32 s80, s15, 3
s_lshl_b32 s80, s80, 2
s_add_u32 s80, s80, s14
s_mul_i32 s80, s80, 1536
v_min_u32 v28, 176, v1
v_lshl_add_u32 v28, v28, 3, s80
v_min_u32 v28, .Lend%=-.Lbeg%=-4, v28
global_load_dword v29, v28, s[6:7]
.LL_75%=:
s_setpc_b64 s[4:5]
.Lredo%=:
s_waitcnt vmcnt(0) lgkmcnt(0)
s_barrier
s_and_b32 s81, s14, 1
s_mul_i32 s91, s81, 16902144
s_mul_i32 s82, s56, 528384
s_add_u32 s91, s91, s82
s_add_u32 s91, s91, 4194304
s_lshr_b32 s82, s14, 1
s_lshl_b32 s83, s82, 14
s_add_u32 s91, s91, s83
s_lshl_b32 s81, s81, 14
s_lshl_b32 s82, s82, 16
s_or_b32 s92, s81, s82
s_xor_b32 s92, s92, 0x10000
s_add_u32 m0, s92, 0
s_add_u32 s81, s91, 0
Dc 1,8,81,0
Dc 1,8,81,1024
Dc 1,8,81,2048
Dc 1,8,81,3072
s_add_u32 m0, s92, 4096
s_add_u32 s81, s91, 4096
Dc 1,8,81,0
Dc 1,8,81,1024
Dc 1,8,81,2048
Dc 1,8,81,3072
s_add_u32 m0, s92, 8192
s_add_u32 s81, s91, 8192
Dc 1,8,81,0
Dc 1,8,81,1024
Dc 1,8,81,2048
Dc 1,8,81,3072
s_add_u32 m0, s92, 12288
s_add_u32 s81, s91, 12288
Dc 1,8,81,0
Dc 1,8,81,1024
Dc 1,8,81,2048
Dc 1,8,81,3072
v_and_b32 v28, 16, v1
v_cmp_eq_u32 vcc, 0, v28
v_mov_b32 v240, 0xeeeeeeee
v_mov_b32 v29, 0x44444444
v_cndmask_b32 v240, v240, v29, vcc
v_mov_b32 v241, 0xff800000
v_add_u32 v242, 0x10000, v1
v_add_u32 v243, 4096, v1
v_add_u32 v244, 8192, v1
v_add_u32 v245, 12288, v1
v_bfe_u32 v28, v1, 4, 4
v_lshrrev_b32 v29, 8, v1
v_lshlrev_b32 v29, 2, v29
v_sub_u32 v28, v28, v29
v_cmp_le_i32_e64 s[72:73], 0, v28
v_cmp_le_i32_e64 s[74:75], 1, v28
v_cmp_le_i32_e64 s[76:77], 2, v28
v_cmp_le_i32_e64 s[78:79], 3, v28
s_mov_b32 s80, 0x3f803f80
s_mov_b32 s81, 0x3f003f00
v_accvgpr_write_b32 a176, s80
v_accvgpr_write_b32 a180, s81
v_accvgpr_write_b32 a177, s80
v_accvgpr_write_b32 a181, s81
v_accvgpr_write_b32 a178, s80
v_accvgpr_write_b32 a182, s81
v_accvgpr_write_b32 a179, s80
v_accvgpr_write_b32 a183, s81
v_accvgpr_write_b32 a160, 0
v_accvgpr_write_b32 a161, 0
v_accvgpr_write_b32 a162, 0
v_accvgpr_write_b32 a163, 0
v_accvgpr_write_b32 a164, 0
v_accvgpr_write_b32 a165, 0
v_accvgpr_write_b32 a166, 0
v_accvgpr_write_b32 a167, 0
v_accvgpr_write_b32 a168, 0
v_accvgpr_write_b32 a169, 0
v_accvgpr_write_b32 a170, 0
v_accvgpr_write_b32 a171, 0
v_accvgpr_write_b32 a172, 0
v_accvgpr_write_b32 a173, 0
v_accvgpr_write_b32 a174, 0
v_accvgpr_write_b32 a175, 0
s_mov_b32 s90, 0x5d800000
s_mov_b64 s[88:89], 0
s_mov_b32 s64, 0
s_and_b32 s80, s14, 1
s_lshl_b32 s62, s80, 14
s_lshl_b32 s82, s57, 15
s_sub_u32 s82, 507904, s82
s_add_u32 s82, s82, s62
s_lshl_b32 s65, s57, 1
s_sub_u32 s65, s65, 1
s_max_i32 s65, s65, 0
s_sub_u32 s66, s60, 1
s_cmp_lt_u32 s14, 2
s_cselect_b32 s82, s82, 16384
s_cselect_b32 s66, s66, 15
s_cselect_b32 s83, 32768, 0
s_add_u32 s61, s91, s82
s_add_u32 s62, s62, s83
s_sub_u32 s63, 16384, s83
s_lshl_b32 s67, s60, 7
s_add_u32 s67, s67, s56
s_lshl_b32 s67, s67, 12
s_add_u32 s67, s67, 41943040
s_lshl_b32 s69, s60, 3
s_add_u32 s69, s69, s59
s_lshl_b32 s69, s69, 6
s_lshl_b32 s83, s58, 3
s_add_u32 s69, s69, s83
s_add_u32 s69, s69, 20480
s_mov_b32 s70, 0
s_mov_b32 s91, 0
s_mov_b64 s[24:25], 0
s_mov_b32 s68, 0
s_mov_b32 s85, 99
s_lshl_b32 s80, s60, 6
s_and_b32 s81, s14, 2
s_lshl_b32 s81, s81, 4
s_xor_b32 s82, s81, 0
s_add_u32 s82, s82, s80
v_add_u32 v4, s82, v28
v_mov_b32 v252, v241
s_xor_b32 s82, s81, 16
s_add_u32 s82, s82, s80
v_add_u32 v5, s82, v28
v_mov_b32 v253, v241
s_xor_b32 s82, s81, 32
s_add_u32 s82, s82, s80
v_add_u32 v6, s82, v28
v_mov_b32 v254, v241
s_xor_b32 s82, s81, 48
s_add_u32 s82, s82, s80
v_add_u32 v7, s82, v28
v_mov_b32 v255, v241
s_mul_i32 s82, s56, 528384
s_add_u32 s82, s82, 4194304
s_lshl_b32 s84, s60, 1
s_add_u32 s84, s84, 2
.LL_76%=:
buffer_load_dwordx4 a[128:131], v1, s[8:11], s82 offen offset:0 sc1
buffer_load_dwordx4 a[132:135], v1, s[8:11], s82 offen offset:1024 sc1
buffer_load_dwordx4 a[136:139], v1, s[8:11], s82 offen offset:2048 sc1
buffer_load_dwordx4 a[140:143], v1, s[8:11], s82 offen offset:3072 sc1
buffer_load_dwordx4 a[144:147], v243, s[8:11], s82 offen offset:0 sc1
buffer_load_dwordx4 a[148:151], v243, s[8:11], s82 offen offset:1024 sc1
buffer_load_dwordx4 a[152:155], v243, s[8:11], s82 offen offset:2048 sc1
buffer_load_dwordx4 a[156:159], v243, s[8:11], s82 offen offset:3072 sc1
s_waitcnt vmcnt(0)
Zvaa 160,128,0
Zvaa 164,128,16
Zvaa 168,128,32
Zvaa 172,128,48
Mvaa 160,132,4
Mvaa 164,132,20
Mvaa 168,132,36
Mvaa 172,132,52
Mvaa 160,136,8
Mvaa 164,136,24
Mvaa 168,136,40
Mvaa 172,136,56
Mvaa 160,140,12
Mvaa 164,140,28
Mvaa 168,140,44
Mvaa 172,140,60
Zvaa 176,144,0
Zvaa 180,144,16
Zvaa 184,144,32
Zvaa 188,144,48
Mvaa 176,148,4
Mvaa 180,148,20
Mvaa 184,148,36
Mvaa 188,148,52
Mvaa 176,152,8
Mvaa 180,152,24
Mvaa 184,152,40
Mvaa 188,152,56
Mvaa 176,156,12
Mvaa 180,156,28
Mvaa 184,156,44
Mvaa 188,156,60
v_cmp_le_i32 vcc, 0, v4
s_nop 1
v_cndmask_b32 v160, v241, v160, vcc
v_max_f32 v252, v252, v160
v_cmp_le_i32 vcc, 1, v4
s_nop 1
v_cndmask_b32 v161, v241, v161, vcc
v_max_f32 v252, v252, v161
v_cmp_le_i32 vcc, 2, v4
s_nop 1
v_cndmask_b32 v162, v241, v162, vcc
v_max_f32 v252, v252, v162
v_cmp_le_i32 vcc, 3, v4
s_nop 1
v_cndmask_b32 v163, v241, v163, vcc
v_max_f32 v252, v252, v163
v_cmp_le_i32 vcc, 0, v5
s_nop 1
v_cndmask_b32 v164, v241, v164, vcc
v_max_f32 v253, v253, v164
v_cmp_le_i32 vcc, 1, v5
s_nop 1
v_cndmask_b32 v165, v241, v165, vcc
v_max_f32 v253, v253, v165
v_cmp_le_i32 vcc, 2, v5
s_nop 1
v_cndmask_b32 v166, v241, v166, vcc
v_max_f32 v253, v253, v166
v_cmp_le_i32 vcc, 3, v5
s_nop 1
v_cndmask_b32 v167, v241, v167, vcc
v_max_f32 v253, v253, v167
v_cmp_le_i32 vcc, 0, v6
s_nop 1
v_cndmask_b32 v168, v241, v168, vcc
v_max_f32 v254, v254, v168
v_cmp_le_i32 vcc, 1, v6
s_nop 1
v_cndmask_b32 v169, v241, v169, vcc
v_max_f32 v254, v254, v169
v_cmp_le_i32 vcc, 2, v6
s_nop 1
v_cndmask_b32 v170, v241, v170, vcc
v_max_f32 v254, v254, v170
v_cmp_le_i32 vcc, 3, v6
s_nop 1
v_cndmask_b32 v171, v241, v171, vcc
v_max_f32 v254, v254, v171
v_cmp_le_i32 vcc, 0, v7
s_nop 1
v_cndmask_b32 v172, v241, v172, vcc
v_max_f32 v255, v255, v172
v_cmp_le_i32 vcc, 1, v7
s_nop 1
v_cndmask_b32 v173, v241, v173, vcc
v_max_f32 v255, v255, v173
v_cmp_le_i32 vcc, 2, v7
s_nop 1
v_cndmask_b32 v174, v241, v174, vcc
v_max_f32 v255, v255, v174
v_cmp_le_i32 vcc, 3, v7
s_nop 1
v_cndmask_b32 v175, v241, v175, vcc
v_max_f32 v255, v255, v175
v_cmp_le_i32 vcc, 16, v4
s_nop 1
v_cndmask_b32 v176, v241, v176, vcc
v_max_f32 v252, v252, v176
v_cmp_le_i32 vcc, 17, v4
s_nop 1
v_cndmask_b32 v177, v241, v177, vcc
v_max_f32 v252, v252, v177
v_cmp_le_i32 vcc, 18, v4
s_nop 1
v_cndmask_b32 v178, v241, v178, vcc
v_max_f32 v252, v252, v178
v_cmp_le_i32 vcc, 19, v4
s_nop 1
v_cndmask_b32 v179, v241, v179, vcc
v_max_f32 v252, v252, v179
v_cmp_le_i32 vcc, 16, v5
s_nop 1
v_cndmask_b32 v180, v241, v180, vcc
v_max_f32 v253, v253, v180
v_cmp_le_i32 vcc, 17, v5
s_nop 1
v_cndmask_b32 v181, v241, v181, vcc
v_max_f32 v253, v253, v181
)"
R"(v_cmp_le_i32 vcc, 18, v5
s_nop 1
v_cndmask_b32 v182, v241, v182, vcc
v_max_f32 v253, v253, v182
v_cmp_le_i32 vcc, 19, v5
s_nop 1
v_cndmask_b32 v183, v241, v183, vcc
v_max_f32 v253, v253, v183
v_cmp_le_i32 vcc, 16, v6
s_nop 1
v_cndmask_b32 v184, v241, v184, vcc
v_max_f32 v254, v254, v184
v_cmp_le_i32 vcc, 17, v6
s_nop 1
v_cndmask_b32 v185, v241, v185, vcc
v_max_f32 v254, v254, v185
v_cmp_le_i32 vcc, 18, v6
s_nop 1
v_cndmask_b32 v186, v241, v186, vcc
v_max_f32 v254, v254, v186
v_cmp_le_i32 vcc, 19, v6
s_nop 1
v_cndmask_b32 v187, v241, v187, vcc
v_max_f32 v254, v254, v187
v_cmp_le_i32 vcc, 16, v7
s_nop 1
v_cndmask_b32 v188, v241, v188, vcc
v_max_f32 v255, v255, v188
v_cmp_le_i32 vcc, 17, v7
s_nop 1
v_cndmask_b32 v189, v241, v189, vcc
v_max_f32 v255, v255, v189
v_cmp_le_i32 vcc, 18, v7
s_nop 1
v_cndmask_b32 v190, v241, v190, vcc
v_max_f32 v255, v255, v190
v_cmp_le_i32 vcc, 19, v7
s_nop 1
v_cndmask_b32 v191, v241, v191, vcc
v_max_f32 v255, v255, v191
v_subrev_u32 v4, 32, v4
v_subrev_u32 v5, 32, v5
v_subrev_u32 v6, 32, v6
v_subrev_u32 v7, 32, v7
s_add_u32 s82, s82, 8192
s_sub_u32 s84, s84, 1
s_cmp_lg_u32 s84, 0
s_cbranch_scc1 .LL_76%=
s_cmp_lt_u32 s14, 2
s_cbranch_scc1 .LL_77%=
s_add_u32 s80, s62, 131072
v_add_u32 v12, s80, v1
ds_write_b128 v12, a[0:3] offset:0
ds_write_b128 v12, a[4:7] offset:1024
ds_write_b128 v12, a[8:11] offset:2048
ds_write_b128 v12, a[12:15] offset:3072
ds_write_b128 v12, a[16:19] offset:4096
ds_write_b128 v12, a[20:23] offset:5120
ds_write_b128 v12, a[24:27] offset:6144
ds_write_b128 v12, a[28:31] offset:7168
ds_write_b128 v12, a[32:35] offset:8192
ds_write_b128 v12, a[36:39] offset:9216
ds_write_b128 v12, a[40:43] offset:10240
ds_write_b128 v12, a[44:47] offset:11264
ds_write_b128 v12, a[48:51] offset:12288
ds_write_b128 v12, a[52:55] offset:13312
ds_write_b128 v12, a[56:59] offset:14336
ds_write_b128 v12, a[60:63] offset:15360
W 0
s_barrier
s_branch .Lbgo%=
.LL_77%=:
s_barrier
s_branch .Lago%=
.Lpd_80%=:
v_mov_b64 v[28:29], s[12:13]
s_mov_b64 exec, 1
buffer_store_dwordx2 v[28:29], off, s[8:11], s69 sc1
s_mov_b64 exec, -1
s_branch .Lpdr_81%=
.Lspin_82%=:
s_mov_b32 s84, 0
.Llp_84%=:
s_sleep 2
v_bfe_u32 v28, v1, 4, 2
v_lshlrev_b32 v28, 3, v28
buffer_load_dwordx2 v[28:29], v28, s[8:11], s55 offen sc1
s_waitcnt vmcnt(0)
s_add_u32 s84, s84, 1
v_cmp_eq_u64 vcc, s[12:13], v[28:29]
s_cmp_eq_u64 vcc, -1
s_cbranch_scc1 .Lok_83%=
s_cmp_lt_u32 s84, 1048576
s_cbranch_scc1 .Llp_84%=
s_branch .Lok_83%=
.Lpd_85%=:
v_mov_b64 v[28:29], s[12:13]
s_mov_b64 exec, 1
buffer_store_dwordx2 v[28:29], off, s[8:11], s69 sc1
s_mov_b64 exec, -1
s_branch .Lpdr_86%=
.Lspin_87%=:
s_mov_b32 s84, 0
.Llp_89%=:
s_sleep 2
v_bfe_u32 v28, v1, 4, 2
v_lshlrev_b32 v28, 3, v28
buffer_load_dwordx2 v[28:29], v28, s[8:11], s54 offen sc1
s_waitcnt vmcnt(0)
s_add_u32 s84, s84, 1
v_cmp_eq_u64 vcc, s[12:13], v[28:29]
s_cmp_eq_u64 vcc, -1
s_cbranch_scc1 .Lok_88%=
s_cmp_lt_u32 s84, 1048576
s_cbranch_scc1 .Llp_89%=
s_branch .Lok_88%=
.Lspin_90%=:
s_mov_b32 s84, 0
.Llp_92%=:
s_sleep 2
v_bfe_u32 v28, v1, 4, 2
v_lshlrev_b32 v28, 3, v28
buffer_load_dwordx2 v[28:29], v28, s[8:11], s55 offen sc1
s_waitcnt vmcnt(0)
s_add_u32 s84, s84, 1
v_cmp_eq_u64 vcc, s[12:13], v[28:29]
s_cmp_eq_u64 vcc, -1
s_cbranch_scc1 .Lok_91%=
s_cmp_lt_u32 s84, 1048576
s_cbranch_scc1 .Llp_92%=
s_branch .Lok_91%=
.Lspin_95%=:
s_mov_b32 s84, 0
.Llp_97%=:
s_sleep 2
buffer_load_dwordx2 v[28:29], v3, s[8:11], s53 offen offset:0 sc1
s_waitcnt vmcnt(0)
s_add_u32 s84, s84, 1
v_cmp_eq_u64 vcc, s[12:13], v[28:29]
s_cmp_eq_u64 vcc, -1
s_cbranch_scc1 .Lok_96%=
s_cmp_lt_u32 s84, 1048576
s_cbranch_scc1 .Llp_97%=
s_branch .Lok_96%=
.Lup_100%=:
v_lshrrev_b32 v180, 1, v1
v_min_u32 v181, 344, v180
s_mov_b32 s84, 0
.LL_104%=:
buffer_load_dwordx2 v[184:185], v180, s[8:11], 0 offen offset:2560 sc1
buffer_load_dwordx2 v[186:187], v181, s[8:11], 0 offen offset:3072 sc1
s_waitcnt vmcnt(0)
v_cmp_eq_u64 vcc, s[12:13], v[184:185]
s_mov_b64 s[86:87], vcc
v_cmp_eq_u64 vcc, s[12:13], v[186:187]
s_and_b64 s[86:87], s[86:87], vcc
s_cmp_eq_u64 s[86:87], -1
s_cbranch_scc1 .LL_103%=
s_sleep 2
s_add_u32 s84, s84, 1
s_cmp_lt_u32 s84, 1048576
s_cbranch_scc1 .LL_104%=
.LL_103%=:
v_bfe_u32 v182, v1, 4, 4
v_mul_u32_u24 v180, 20480, v182
v_and_b32 v181, 0x300, v1
v_add_u32 v180, v180, v181
v_cmp_gt_u32 vcc, 6, v182
v_mov_b32 v181, 0x80000000
s_nop 0
v_cndmask_b32 v181, v181, v180, vcc
.LL_102%=:
s_ff1_i32_b32 s66, s65
s_bitset0_b32 s65, s66
s_nop 3
v_readlane_b32 s67, v12, s66
s_nop 3
s_lshl_b32 s80, s67, 2
s_add_u32 s80, s80, 2752512
buffer_load_dword v182, off, s[8:11], s80 sc1
s_mul_i32 s80, s58, 5120
s_lshl_b32 s81, s66, 4
s_add_u32 s80, s80, s81
s_lshl_b32 s81, s14, 10
s_add_u32 s80, s80, s81
s_add_u32 s80, s80, 2760704
buffer_load_dwordx4 v[184:187], v180, s[8:11], s80 offen offset:0 sc1
buffer_load_dwordx4 v[188:191], v180, s[8:11], s80 offen offset:128 sc1
s_add_u32 s80, s80, 327680
buffer_load_dwordx4 v[192:195], v180, s[8:11], s80 offen offset:0 sc1
buffer_load_dwordx4 v[196:199], v180, s[8:11], s80 offen offset:128 sc1
s_add_u32 s80, s80, 327680
buffer_load_dwordx4 v[200:203], v180, s[8:11], s80 offen offset:0 sc1
buffer_load_dwordx4 v[204:207], v180, s[8:11], s80 offen offset:128 sc1
s_add_u32 s80, s80, 327680
buffer_load_dwordx4 v[208:211], v181, s[8:11], s80 offen offset:0 sc1
buffer_load_dwordx4 v[212:215], v181, s[8:11], s80 offen offset:128 sc1
s_mul_i32 s80, s58, 5120
s_lshl_b32 s81, s66, 4
s_add_u32 s80, s80, s81
s_add_u32 s80, s80, 2764800
buffer_load_dwordx4 v[216:219], v180, s[8:11], s80 offen offset:0 sc1
buffer_load_dwordx4 v[220:223], v180, s[8:11], s80 offen offset:128 sc1
s_add_u32 s80, s80, 327680
buffer_load_dwordx4 v[224:227], v180, s[8:11], s80 offen offset:0 sc1
buffer_load_dwordx4 v[228:231], v180, s[8:11], s80 offen offset:128 sc1
s_add_u32 s80, s80, 327680
buffer_load_dwordx4 v[232:235], v180, s[8:11], s80 offen offset:0 sc1
buffer_load_dwordx4 v[236:239], v180, s[8:11], s80 offen offset:128 sc1
s_add_u32 s80, s80, 327680
buffer_load_dwordx4 v[240:243], v181, s[8:11], s80 offen offset:0 sc1
buffer_load_dwordx4 v[244:247], v181, s[8:11], s80 offen offset:128 sc1
s_waitcnt vmcnt(0)
v_and_b32 v182, 0xffff0000, v182
v_ashrrev_i32 v183, 31, v182
v_not_b32 v183, v183
v_or_b32 v183, 0x80000000, v183
v_xor_b32 v182, v182, v183
v_and_b32 v182, 0xffff0000, v182
v_mul_f32 v182, 0xbfb8aa3b, v182
X 182
s_nop 0
v_add_f32 v182, 1.0, v182
v_rcp_f32 v182, v182
s_nop 0
K 182,182,182
v_lshlrev_b32 v182, 16, v182
s_and_b32 s67, s67, 15
v_bfe_u32 v183, v1, 4, 4
v_cmp_eq_u32 vcc, s67, v183
A 184,184,192
A 185,185,193
A 186,186,194
A 187,187,195
A 188,188,196
A 189,189,197
A 190,190,198
A 191,191,199
A 184,184,200
A 185,185,201
A 186,186,202
A 187,187,203
A 188,188,204
A 189,189,205
A 190,190,206
A 191,191,207
A 184,184,208
A 185,185,209
A 186,186,210
A 187,187,211
A 188,188,212
A 189,189,213
A 190,190,214
A 191,191,215
v_add_f32_dpp v184, v184, v184 quad_perm:[1,0,3,2] row_mask:0xf bank_mask:0xf
v_add_f32_dpp v185, v185, v185 quad_perm:[1,0,3,2] row_mask:0xf bank_mask:0xf
v_add_f32_dpp v186, v186, v186 quad_perm:[1,0,3,2] row_mask:0xf bank_mask:0xf
v_add_f32_dpp v187, v187, v187 quad_perm:[1,0,3,2] row_mask:0xf bank_mask:0xf
v_add_f32_dpp v188, v188, v188 quad_perm:[1,0,3,2] row_mask:0xf bank_mask:0xf
v_add_f32_dpp v189, v189, v189 quad_perm:[1,0,3,2] row_mask:0xf bank_mask:0xf
v_add_f32_dpp v190, v190, v190 quad_perm:[1,0,3,2] row_mask:0xf bank_mask:0xf
v_add_f32_dpp v191, v191, v191 quad_perm:[1,0,3,2] row_mask:0xf bank_mask:0xf
v_add_f32_dpp v184, v184, v184 quad_perm:[2,3,0,1] row_mask:0xf bank_mask:0xf
v_add_f32_dpp v185, v185, v185 quad_perm:[2,3,0,1] row_mask:0xf bank_mask:0xf
v_add_f32_dpp v186, v186, v186 quad_perm:[2,3,0,1] row_mask:0xf bank_mask:0xf
v_add_f32_dpp v187, v187, v187 quad_perm:[2,3,0,1] row_mask:0xf bank_mask:0xf
v_add_f32_dpp v188, v188, v188 quad_perm:[2,3,0,1] row_mask:0xf bank_mask:0xf
v_add_f32_dpp v189, v189, v189 quad_perm:[2,3,0,1] row_mask:0xf bank_mask:0xf
v_add_f32_dpp v190, v190, v190 quad_perm:[2,3,0,1] row_mask:0xf bank_mask:0xf
v_add_f32_dpp v191, v191, v191 quad_perm:[2,3,0,1] row_mask:0xf bank_mask:0xf
v_add_f32_dpp v184, v184, v184 row_half_mirror row_mask:0xf bank_mask:0xf
v_add_f32_dpp v185, v185, v185 row_half_mirror row_mask:0xf bank_mask:0xf
v_add_f32_dpp v186, v186, v186 row_half_mirror row_mask:0xf bank_mask:0xf
v_add_f32_dpp v187, v187, v187 row_half_mirror row_mask:0xf bank_mask:0xf
v_add_f32_dpp v188, v188, v188 row_half_mirror row_mask:0xf bank_mask:0xf
v_add_f32_dpp v189, v189, v189 row_half_mirror row_mask:0xf bank_mask:0xf
v_add_f32_dpp v190, v190, v190 row_half_mirror row_mask:0xf bank_mask:0xf
v_add_f32_dpp v191, v191, v191 row_half_mirror row_mask:0xf bank_mask:0xf
v_add_f32_dpp v184, v184, v184 row_mirror row_mask:0xf bank_mask:0xf
v_add_f32_dpp v185, v185, v185 row_mirror row_mask:0xf bank_mask:0xf
v_add_f32_dpp v186, v186, v186 row_mirror row_mask:0xf bank_mask:0xf
v_add_f32_dpp v187, v187, v187 row_mirror row_mask:0xf bank_mask:0xf
v_add_f32_dpp v188, v188, v188 row_mirror row_mask:0xf bank_mask:0xf
v_add_f32_dpp v189, v189, v189 row_mirror row_mask:0xf bank_mask:0xf
v_add_f32_dpp v190, v190, v190 row_mirror row_mask:0xf bank_mask:0xf
v_add_f32_dpp v191, v191, v191 row_mirror row_mask:0xf bank_mask:0xf
K 184,184,184
v_lshlrev_b32 v184, 16, v184
U 184,184,182
K 184,184,184
v_lshlrev_b32 v184, 16, v184
A 184,184,68
K 184,184,184
v_lshlrev_b32 v184, 16, v184
v_cndmask_b32 v68, v68, v184, vcc
K 185,185,185
v_lshlrev_b32 v185, 16, v185
U 185,185,182
K 185,185,185
v_lshlrev_b32 v185, 16, v185
A 185,185,69
K 185,185,185
v_lshlrev_b32 v185, 16, v185
v_cndmask_b32 v69, v69, v185, vcc
K 186,186,186
v_lshlrev_b32 v186, 16, v186
U 186,186,182
K 186,186,186
v_lshlrev_b32 v186, 16, v186
A 186,186,70
K 186,186,186
v_lshlrev_b32 v186, 16, v186
v_cndmask_b32 v70, v70, v186, vcc
K 187,187,187
v_lshlrev_b32 v187, 16, v187
U 187,187,182
K 187,187,187
v_lshlrev_b32 v187, 16, v187
A 187,187,71
K 187,187,187
v_lshlrev_b32 v187, 16, v187
v_cndmask_b32 v71, v71, v187, vcc
K 188,188,188
v_lshlrev_b32 v188, 16, v188
U 188,188,182
K 188,188,188
v_lshlrev_b32 v188, 16, v188
A 188,188,72
K 188,188,188
v_lshlrev_b32 v188, 16, v188
v_cndmask_b32 v72, v72, v188, vcc
K 189,189,189
v_lshlrev_b32 v189, 16, v189
U 189,189,182
K 189,189,189
v_lshlrev_b32 v189, 16, v189
A 189,189,73
K 189,189,189
v_lshlrev_b32 v189, 16, v189
v_cndmask_b32 v73, v73, v189, vcc
K 190,190,190
v_lshlrev_b32 v190, 16, v190
U 190,190,182
K 190,190,190
v_lshlrev_b32 v190, 16, v190
A 190,190,74
K 190,190,190
v_lshlrev_b32 v190, 16, v190
v_cndmask_b32 v74, v74, v190, vcc
K 191,191,191
v_lshlrev_b32 v191, 16, v191
U 191,191,182
K 191,191,191
v_lshlrev_b32 v191, 16, v191
A 191,191,75
K 191,191,191
v_lshlrev_b32 v191, 16, v191
v_cndmask_b32 v75, v75, v191, vcc
A 216,216,224
A 217,217,225
A 218,218,226
A 219,219,227
A 220,220,228
A 221,221,229
A 222,222,230
A 223,223,231
A 216,216,232
A 217,217,233
A 218,218,234
A 219,219,235
A 220,220,236
A 221,221,237
A 222,222,238
A 223,223,239
A 216,216,240
A 217,217,241
A 218,218,242
A 219,219,243
A 220,220,244
A 221,221,245
A 222,222,246
A 223,223,247
v_add_f32_dpp v216, v216, v216 quad_perm:[1,0,3,2] row_mask:0xf bank_mask:0xf
v_add_f32_dpp v217, v217, v217 quad_perm:[1,0,3,2] row_mask:0xf bank_mask:0xf
v_add_f32_dpp v218, v218, v218 quad_perm:[1,0,3,2] row_mask:0xf bank_mask:0xf
v_add_f32_dpp v219, v219, v219 quad_perm:[1,0,3,2] row_mask:0xf bank_mask:0xf
v_add_f32_dpp v220, v220, v220 quad_perm:[1,0,3,2] row_mask:0xf bank_mask:0xf
v_add_f32_dpp v221, v221, v221 quad_perm:[1,0,3,2] row_mask:0xf bank_mask:0xf
v_add_f32_dpp v222, v222, v222 quad_perm:[1,0,3,2] row_mask:0xf bank_mask:0xf
v_add_f32_dpp v223, v223, v223 quad_perm:[1,0,3,2] row_mask:0xf bank_mask:0xf
v_add_f32_dpp v216, v216, v216 quad_perm:[2,3,0,1] row_mask:0xf bank_mask:0xf
v_add_f32_dpp v217, v217, v217 quad_perm:[2,3,0,1] row_mask:0xf bank_mask:0xf
v_add_f32_dpp v218, v218, v218 quad_perm:[2,3,0,1] row_mask:0xf bank_mask:0xf
v_add_f32_dpp v219, v219, v219 quad_perm:[2,3,0,1] row_mask:0xf bank_mask:0xf
v_add_f32_dpp v220, v220, v220 quad_perm:[2,3,0,1] row_mask:0xf bank_mask:0xf
v_add_f32_dpp v221, v221, v221 quad_perm:[2,3,0,1] row_mask:0xf bank_mask:0xf
v_add_f32_dpp v222, v222, v222 quad_perm:[2,3,0,1] row_mask:0xf bank_mask:0xf
v_add_f32_dpp v223, v223, v223 quad_perm:[2,3,0,1] row_mask:0xf bank_mask:0xf
v_add_f32_dpp v216, v216, v216 row_half_mirror row_mask:0xf bank_mask:0xf
v_add_f32_dpp v217, v217, v217 row_half_mirror row_mask:0xf bank_mask:0xf
)"
R"(v_add_f32_dpp v218, v218, v218 row_half_mirror row_mask:0xf bank_mask:0xf
v_add_f32_dpp v219, v219, v219 row_half_mirror row_mask:0xf bank_mask:0xf
v_add_f32_dpp v220, v220, v220 row_half_mirror row_mask:0xf bank_mask:0xf
v_add_f32_dpp v221, v221, v221 row_half_mirror row_mask:0xf bank_mask:0xf
v_add_f32_dpp v222, v222, v222 row_half_mirror row_mask:0xf bank_mask:0xf
v_add_f32_dpp v223, v223, v223 row_half_mirror row_mask:0xf bank_mask:0xf
v_add_f32_dpp v216, v216, v216 row_mirror row_mask:0xf bank_mask:0xf
v_add_f32_dpp v217, v217, v217 row_mirror row_mask:0xf bank_mask:0xf
v_add_f32_dpp v218, v218, v218 row_mirror row_mask:0xf bank_mask:0xf
v_add_f32_dpp v219, v219, v219 row_mirror row_mask:0xf bank_mask:0xf
v_add_f32_dpp v220, v220, v220 row_mirror row_mask:0xf bank_mask:0xf
v_add_f32_dpp v221, v221, v221 row_mirror row_mask:0xf bank_mask:0xf
v_add_f32_dpp v222, v222, v222 row_mirror row_mask:0xf bank_mask:0xf
v_add_f32_dpp v223, v223, v223 row_mirror row_mask:0xf bank_mask:0xf
K 216,216,216
v_lshlrev_b32 v216, 16, v216
U 216,216,182
K 216,216,216
v_lshlrev_b32 v216, 16, v216
A 216,216,84
K 216,216,216
v_lshlrev_b32 v216, 16, v216
v_cndmask_b32 v84, v84, v216, vcc
K 217,217,217
v_lshlrev_b32 v217, 16, v217
U 217,217,182
K 217,217,217
v_lshlrev_b32 v217, 16, v217
A 217,217,85
K 217,217,217
v_lshlrev_b32 v217, 16, v217
v_cndmask_b32 v85, v85, v217, vcc
K 218,218,218
v_lshlrev_b32 v218, 16, v218
U 218,218,182
K 218,218,218
v_lshlrev_b32 v218, 16, v218
A 218,218,86
K 218,218,218
v_lshlrev_b32 v218, 16, v218
v_cndmask_b32 v86, v86, v218, vcc
K 219,219,219
v_lshlrev_b32 v219, 16, v219
U 219,219,182
K 219,219,219
v_lshlrev_b32 v219, 16, v219
A 219,219,87
K 219,219,219
v_lshlrev_b32 v219, 16, v219
v_cndmask_b32 v87, v87, v219, vcc
K 220,220,220
v_lshlrev_b32 v220, 16, v220
U 220,220,182
K 220,220,220
v_lshlrev_b32 v220, 16, v220
A 220,220,88
K 220,220,220
v_lshlrev_b32 v220, 16, v220
v_cndmask_b32 v88, v88, v220, vcc
K 221,221,221
v_lshlrev_b32 v221, 16, v221
U 221,221,182
K 221,221,221
v_lshlrev_b32 v221, 16, v221
A 221,221,89
K 221,221,221
v_lshlrev_b32 v221, 16, v221
v_cndmask_b32 v89, v89, v221, vcc
K 222,222,222
v_lshlrev_b32 v222, 16, v222
U 222,222,182
K 222,222,222
v_lshlrev_b32 v222, 16, v222
A 222,222,90
K 222,222,222
v_lshlrev_b32 v222, 16, v222
v_cndmask_b32 v90, v90, v222, vcc
K 223,223,223
v_lshlrev_b32 v223, 16, v223
U 223,223,182
K 223,223,223
v_lshlrev_b32 v223, 16, v223
A 223,223,91
K 223,223,223
v_lshlrev_b32 v223, 16, v223
v_cndmask_b32 v91, v91, v223, vcc
s_cmp_lg_u32 s65, 0
s_cbranch_scc1 .LL_102%=
s_branch .Lupr_101%=
.Lup_105%=:
v_lshrrev_b32 v180, 1, v1
v_min_u32 v181, 344, v180
s_mov_b32 s84, 0
.LL_109%=:
buffer_load_dwordx2 v[184:185], v180, s[8:11], 0 offen offset:2560 sc1
buffer_load_dwordx2 v[186:187], v181, s[8:11], 0 offen offset:3072 sc1
s_waitcnt vmcnt(0)
v_cmp_eq_u64 vcc, s[12:13], v[184:185]
s_mov_b64 s[86:87], vcc
v_cmp_eq_u64 vcc, s[12:13], v[186:187]
s_and_b64 s[86:87], s[86:87], vcc
s_cmp_eq_u64 s[86:87], -1
s_cbranch_scc1 .LL_108%=
s_sleep 2
s_add_u32 s84, s84, 1
s_cmp_lt_u32 s84, 1048576
s_cbranch_scc1 .LL_109%=
.LL_108%=:
v_bfe_u32 v182, v1, 4, 4
v_mul_u32_u24 v180, 20480, v182
v_and_b32 v181, 0x300, v1
v_add_u32 v180, v180, v181
v_cmp_gt_u32 vcc, 6, v182
v_mov_b32 v181, 0x80000000
s_nop 0
v_cndmask_b32 v181, v181, v180, vcc
.LL_107%=:
s_ff1_i32_b32 s66, s65
s_bitset0_b32 s65, s66
s_nop 3
v_readlane_b32 s67, v12, s66
s_nop 3
s_lshl_b32 s80, s67, 2
s_add_u32 s80, s80, 2752512
buffer_load_dword v182, off, s[8:11], s80 sc1
s_mul_i32 s80, s58, 5120
s_lshl_b32 s81, s66, 4
s_add_u32 s80, s80, s81
s_lshl_b32 s81, s14, 10
s_add_u32 s80, s80, s81
s_add_u32 s80, s80, 2760704
buffer_load_dwordx4 v[184:187], v180, s[8:11], s80 offen offset:0 sc1
buffer_load_dwordx4 v[188:191], v180, s[8:11], s80 offen offset:128 sc1
s_add_u32 s80, s80, 327680
buffer_load_dwordx4 v[192:195], v180, s[8:11], s80 offen offset:0 sc1
buffer_load_dwordx4 v[196:199], v180, s[8:11], s80 offen offset:128 sc1
s_add_u32 s80, s80, 327680
buffer_load_dwordx4 v[200:203], v180, s[8:11], s80 offen offset:0 sc1
buffer_load_dwordx4 v[204:207], v180, s[8:11], s80 offen offset:128 sc1
s_add_u32 s80, s80, 327680
buffer_load_dwordx4 v[208:211], v181, s[8:11], s80 offen offset:0 sc1
buffer_load_dwordx4 v[212:215], v181, s[8:11], s80 offen offset:128 sc1
s_waitcnt vmcnt(0)
v_and_b32 v182, 0xffff0000, v182
v_ashrrev_i32 v183, 31, v182
v_not_b32 v183, v183
v_or_b32 v183, 0x80000000, v183
v_xor_b32 v182, v182, v183
v_and_b32 v182, 0xffff0000, v182
v_mul_f32 v182, 0xbfb8aa3b, v182
X 182
s_nop 0
v_add_f32 v182, 1.0, v182
v_rcp_f32 v182, v182
s_nop 0
K 182,182,182
v_lshlrev_b32 v182, 16, v182
s_and_b32 s67, s67, 15
v_bfe_u32 v183, v1, 4, 4
v_cmp_eq_u32 vcc, s67, v183
A 184,184,192
A 185,185,193
A 186,186,194
A 187,187,195
A 188,188,196
A 189,189,197
A 190,190,198
A 191,191,199
A 184,184,200
A 185,185,201
A 186,186,202
A 187,187,203
A 188,188,204
A 189,189,205
A 190,190,206
A 191,191,207
A 184,184,208
A 185,185,209
A 186,186,210
A 187,187,211
A 188,188,212
A 189,189,213
A 190,190,214
A 191,191,215
v_add_f32_dpp v184, v184, v184 quad_perm:[1,0,3,2] row_mask:0xf bank_mask:0xf
v_add_f32_dpp v185, v185, v185 quad_perm:[1,0,3,2] row_mask:0xf bank_mask:0xf
v_add_f32_dpp v186, v186, v186 quad_perm:[1,0,3,2] row_mask:0xf bank_mask:0xf
v_add_f32_dpp v187, v187, v187 quad_perm:[1,0,3,2] row_mask:0xf bank_mask:0xf
v_add_f32_dpp v188, v188, v188 quad_perm:[1,0,3,2] row_mask:0xf bank_mask:0xf
v_add_f32_dpp v189, v189, v189 quad_perm:[1,0,3,2] row_mask:0xf bank_mask:0xf
v_add_f32_dpp v190, v190, v190 quad_perm:[1,0,3,2] row_mask:0xf bank_mask:0xf
v_add_f32_dpp v191, v191, v191 quad_perm:[1,0,3,2] row_mask:0xf bank_mask:0xf
v_add_f32_dpp v184, v184, v184 quad_perm:[2,3,0,1] row_mask:0xf bank_mask:0xf
v_add_f32_dpp v185, v185, v185 quad_perm:[2,3,0,1] row_mask:0xf bank_mask:0xf
v_add_f32_dpp v186, v186, v186 quad_perm:[2,3,0,1] row_mask:0xf bank_mask:0xf
v_add_f32_dpp v187, v187, v187 quad_perm:[2,3,0,1] row_mask:0xf bank_mask:0xf
v_add_f32_dpp v188, v188, v188 quad_perm:[2,3,0,1] row_mask:0xf bank_mask:0xf
v_add_f32_dpp v189, v189, v189 quad_perm:[2,3,0,1] row_mask:0xf bank_mask:0xf
v_add_f32_dpp v190, v190, v190 quad_perm:[2,3,0,1] row_mask:0xf bank_mask:0xf
v_add_f32_dpp v191, v191, v191 quad_perm:[2,3,0,1] row_mask:0xf bank_mask:0xf
v_add_f32_dpp v184, v184, v184 row_half_mirror row_mask:0xf bank_mask:0xf
v_add_f32_dpp v185, v185, v185 row_half_mirror row_mask:0xf bank_mask:0xf
v_add_f32_dpp v186, v186, v186 row_half_mirror row_mask:0xf bank_mask:0xf
v_add_f32_dpp v187, v187, v187 row_half_mirror row_mask:0xf bank_mask:0xf
v_add_f32_dpp v188, v188, v188 row_half_mirror row_mask:0xf bank_mask:0xf
v_add_f32_dpp v189, v189, v189 row_half_mirror row_mask:0xf bank_mask:0xf
v_add_f32_dpp v190, v190, v190 row_half_mirror row_mask:0xf bank_mask:0xf
v_add_f32_dpp v191, v191, v191 row_half_mirror row_mask:0xf bank_mask:0xf
v_add_f32_dpp v184, v184, v184 row_mirror row_mask:0xf bank_mask:0xf
v_add_f32_dpp v185, v185, v185 row_mirror row_mask:0xf bank_mask:0xf
v_add_f32_dpp v186, v186, v186 row_mirror row_mask:0xf bank_mask:0xf
v_add_f32_dpp v187, v187, v187 row_mirror row_mask:0xf bank_mask:0xf
v_add_f32_dpp v188, v188, v188 row_mirror row_mask:0xf bank_mask:0xf
v_add_f32_dpp v189, v189, v189 row_mirror row_mask:0xf bank_mask:0xf
v_add_f32_dpp v190, v190, v190 row_mirror row_mask:0xf bank_mask:0xf
v_add_f32_dpp v191, v191, v191 row_mirror row_mask:0xf bank_mask:0xf
K 184,184,184
v_lshlrev_b32 v184, 16, v184
U 184,184,182
K 184,184,184
v_lshlrev_b32 v184, 16, v184
A 184,184,68
K 184,184,184
v_lshlrev_b32 v184, 16, v184
v_cndmask_b32 v68, v68, v184, vcc
K 185,185,185
v_lshlrev_b32 v185, 16, v185
U 185,185,182
K 185,185,185
v_lshlrev_b32 v185, 16, v185
A 185,185,69
K 185,185,185
v_lshlrev_b32 v185, 16, v185
v_cndmask_b32 v69, v69, v185, vcc
K 186,186,186
v_lshlrev_b32 v186, 16, v186
U 186,186,182
K 186,186,186
v_lshlrev_b32 v186, 16, v186
A 186,186,70
K 186,186,186
v_lshlrev_b32 v186, 16, v186
v_cndmask_b32 v70, v70, v186, vcc
K 187,187,187
v_lshlrev_b32 v187, 16, v187
U 187,187,182
K 187,187,187
v_lshlrev_b32 v187, 16, v187
A 187,187,71
K 187,187,187
v_lshlrev_b32 v187, 16, v187
v_cndmask_b32 v71, v71, v187, vcc
K 188,188,188
v_lshlrev_b32 v188, 16, v188
U 188,188,182
K 188,188,188
v_lshlrev_b32 v188, 16, v188
A 188,188,72
K 188,188,188
v_lshlrev_b32 v188, 16, v188
v_cndmask_b32 v72, v72, v188, vcc
K 189,189,189
v_lshlrev_b32 v189, 16, v189
U 189,189,182
K 189,189,189
v_lshlrev_b32 v189, 16, v189
A 189,189,73
K 189,189,189
v_lshlrev_b32 v189, 16, v189
v_cndmask_b32 v73, v73, v189, vcc
K 190,190,190
v_lshlrev_b32 v190, 16, v190
U 190,190,182
K 190,190,190
v_lshlrev_b32 v190, 16, v190
A 190,190,74
K 190,190,190
v_lshlrev_b32 v190, 16, v190
v_cndmask_b32 v74, v74, v190, vcc
K 191,191,191
v_lshlrev_b32 v191, 16, v191
U 191,191,182
K 191,191,191
v_lshlrev_b32 v191, 16, v191
A 191,191,75
K 191,191,191
v_lshlrev_b32 v191, 16, v191
v_cndmask_b32 v75, v75, v191, vcc
s_cmp_lg_u32 s65, 0
s_cbranch_scc1 .LL_107%=
s_branch .Lupr_106%=
.Lspin_110%=:
s_mov_b32 s84, 0
.Llp_112%=:
s_sleep 2
buffer_load_dwordx2 v[28:29], v3, s[8:11], s53 offen offset:512 sc1
s_waitcnt vmcnt(0)
s_add_u32 s84, s84, 1
v_cmp_eq_u64 vcc, s[12:13], v[28:29]
s_cmp_eq_u64 vcc, -1
s_cbranch_scc1 .Lok_111%=
s_cmp_lt_u32 s84, 1048576
s_cbranch_scc1 .Llp_112%=
s_branch .Lok_111%=
.Lup_115%=:
v_lshrrev_b32 v180, 1, v1
v_min_u32 v181, 344, v180
s_mov_b32 s84, 0
.LL_119%=:
buffer_load_dwordx2 v[184:185], v180, s[8:11], 0 offen offset:2560 sc1
buffer_load_dwordx2 v[186:187], v181, s[8:11], 0 offen offset:3072 sc1
s_waitcnt vmcnt(0)
v_cmp_eq_u64 vcc, s[12:13], v[184:185]
s_mov_b64 s[86:87], vcc
v_cmp_eq_u64 vcc, s[12:13], v[186:187]
s_and_b64 s[86:87], s[86:87], vcc
s_cmp_eq_u64 s[86:87], -1
s_cbranch_scc1 .LL_118%=
s_sleep 2
s_add_u32 s84, s84, 1
s_cmp_lt_u32 s84, 1048576
s_cbranch_scc1 .LL_119%=
.LL_118%=:
v_bfe_u32 v182, v1, 4, 4
v_mul_u32_u24 v180, 20480, v182
v_and_b32 v181, 0x300, v1
v_add_u32 v180, v180, v181
v_cmp_gt_u32 vcc, 6, v182
v_mov_b32 v181, 0x80000000
s_nop 0
v_cndmask_b32 v181, v181, v180, vcc
.LL_117%=:
s_ff1_i32_b32 s66, s65
s_bitset0_b32 s65, s66
s_nop 3
v_readlane_b32 s67, v12, s66
s_nop 3
s_lshl_b32 s80, s67, 2
s_add_u32 s80, s80, 2752512
buffer_load_dword v182, off, s[8:11], s80 sc1
s_mul_i32 s80, s58, 5120
s_lshl_b32 s81, s66, 4
s_add_u32 s80, s80, s81
s_lshl_b32 s81, s14, 10
s_add_u32 s80, s80, s81
s_add_u32 s80, s80, 2760704
buffer_load_dwordx4 v[184:187], v180, s[8:11], s80 offen offset:0 sc1
buffer_load_dwordx4 v[188:191], v180, s[8:11], s80 offen offset:128 sc1
s_add_u32 s80, s80, 327680
buffer_load_dwordx4 v[192:195], v180, s[8:11], s80 offen offset:0 sc1
buffer_load_dwordx4 v[196:199], v180, s[8:11], s80 offen offset:128 sc1
s_add_u32 s80, s80, 327680
buffer_load_dwordx4 v[200:203], v180, s[8:11], s80 offen offset:0 sc1
buffer_load_dwordx4 v[204:207], v180, s[8:11], s80 offen offset:128 sc1
s_add_u32 s80, s80, 327680
buffer_load_dwordx4 v[208:211], v181, s[8:11], s80 offen offset:0 sc1
buffer_load_dwordx4 v[212:215], v181, s[8:11], s80 offen offset:128 sc1
s_mul_i32 s80, s58, 5120
s_lshl_b32 s81, s66, 4
s_add_u32 s80, s80, s81
s_add_u32 s80, s80, 2764800
buffer_load_dwordx4 v[216:219], v180, s[8:11], s80 offen offset:0 sc1
buffer_load_dwordx4 v[220:223], v180, s[8:11], s80 offen offset:128 sc1
s_add_u32 s80, s80, 327680
buffer_load_dwordx4 v[224:227], v180, s[8:11], s80 offen offset:0 sc1
buffer_load_dwordx4 v[228:231], v180, s[8:11], s80 offen offset:128 sc1
s_add_u32 s80, s80, 327680
buffer_load_dwordx4 v[232:235], v180, s[8:11], s80 offen offset:0 sc1
buffer_load_dwordx4 v[236:239], v180, s[8:11], s80 offen offset:128 sc1
s_add_u32 s80, s80, 327680
buffer_load_dwordx4 v[240:243], v181, s[8:11], s80 offen offset:0 sc1
buffer_load_dwordx4 v[244:247], v181, s[8:11], s80 offen offset:128 sc1
s_waitcnt vmcnt(0)
v_and_b32 v182, 0xffff0000, v182
v_ashrrev_i32 v183, 31, v182
v_not_b32 v183, v183
v_or_b32 v183, 0x80000000, v183
v_xor_b32 v182, v182, v183
v_and_b32 v182, 0xffff0000, v182
v_mul_f32 v182, 0xbfb8aa3b, v182
X 182
s_nop 0
v_add_f32 v182, 1.0, v182
v_rcp_f32 v182, v182
s_nop 0
K 182,182,182
v_lshlrev_b32 v182, 16, v182
s_and_b32 s67, s67, 15
v_bfe_u32 v183, v1, 4, 4
v_cmp_eq_u32 vcc, s67, v183
A 184,184,192
A 185,185,193
A 186,186,194
A 187,187,195
A 188,188,196
A 189,189,197
A 190,190,198
A 191,191,199
A 184,184,200
A 185,185,201
)"
R"(A 186,186,202
A 187,187,203
A 188,188,204
A 189,189,205
A 190,190,206
A 191,191,207
A 184,184,208
A 185,185,209
A 186,186,210
A 187,187,211
A 188,188,212
A 189,189,213
A 190,190,214
A 191,191,215
v_add_f32_dpp v184, v184, v184 quad_perm:[1,0,3,2] row_mask:0xf bank_mask:0xf
v_add_f32_dpp v185, v185, v185 quad_perm:[1,0,3,2] row_mask:0xf bank_mask:0xf
v_add_f32_dpp v186, v186, v186 quad_perm:[1,0,3,2] row_mask:0xf bank_mask:0xf
v_add_f32_dpp v187, v187, v187 quad_perm:[1,0,3,2] row_mask:0xf bank_mask:0xf
v_add_f32_dpp v188, v188, v188 quad_perm:[1,0,3,2] row_mask:0xf bank_mask:0xf
v_add_f32_dpp v189, v189, v189 quad_perm:[1,0,3,2] row_mask:0xf bank_mask:0xf
v_add_f32_dpp v190, v190, v190 quad_perm:[1,0,3,2] row_mask:0xf bank_mask:0xf
v_add_f32_dpp v191, v191, v191 quad_perm:[1,0,3,2] row_mask:0xf bank_mask:0xf
v_add_f32_dpp v184, v184, v184 quad_perm:[2,3,0,1] row_mask:0xf bank_mask:0xf
v_add_f32_dpp v185, v185, v185 quad_perm:[2,3,0,1] row_mask:0xf bank_mask:0xf
v_add_f32_dpp v186, v186, v186 quad_perm:[2,3,0,1] row_mask:0xf bank_mask:0xf
v_add_f32_dpp v187, v187, v187 quad_perm:[2,3,0,1] row_mask:0xf bank_mask:0xf
v_add_f32_dpp v188, v188, v188 quad_perm:[2,3,0,1] row_mask:0xf bank_mask:0xf
v_add_f32_dpp v189, v189, v189 quad_perm:[2,3,0,1] row_mask:0xf bank_mask:0xf
v_add_f32_dpp v190, v190, v190 quad_perm:[2,3,0,1] row_mask:0xf bank_mask:0xf
v_add_f32_dpp v191, v191, v191 quad_perm:[2,3,0,1] row_mask:0xf bank_mask:0xf
v_add_f32_dpp v184, v184, v184 row_half_mirror row_mask:0xf bank_mask:0xf
v_add_f32_dpp v185, v185, v185 row_half_mirror row_mask:0xf bank_mask:0xf
v_add_f32_dpp v186, v186, v186 row_half_mirror row_mask:0xf bank_mask:0xf
v_add_f32_dpp v187, v187, v187 row_half_mirror row_mask:0xf bank_mask:0xf
v_add_f32_dpp v188, v188, v188 row_half_mirror row_mask:0xf bank_mask:0xf
v_add_f32_dpp v189, v189, v189 row_half_mirror row_mask:0xf bank_mask:0xf
v_add_f32_dpp v190, v190, v190 row_half_mirror row_mask:0xf bank_mask:0xf
v_add_f32_dpp v191, v191, v191 row_half_mirror row_mask:0xf bank_mask:0xf
v_add_f32_dpp v184, v184, v184 row_mirror row_mask:0xf bank_mask:0xf
v_add_f32_dpp v185, v185, v185 row_mirror row_mask:0xf bank_mask:0xf
v_add_f32_dpp v186, v186, v186 row_mirror row_mask:0xf bank_mask:0xf
v_add_f32_dpp v187, v187, v187 row_mirror row_mask:0xf bank_mask:0xf
v_add_f32_dpp v188, v188, v188 row_mirror row_mask:0xf bank_mask:0xf
v_add_f32_dpp v189, v189, v189 row_mirror row_mask:0xf bank_mask:0xf
v_add_f32_dpp v190, v190, v190 row_mirror row_mask:0xf bank_mask:0xf
v_add_f32_dpp v191, v191, v191 row_mirror row_mask:0xf bank_mask:0xf
K 184,184,184
v_lshlrev_b32 v184, 16, v184
U 184,184,182
K 184,184,184
v_lshlrev_b32 v184, 16, v184
A 184,184,68
K 184,184,184
v_lshlrev_b32 v184, 16, v184
v_cndmask_b32 v68, v68, v184, vcc
K 185,185,185
v_lshlrev_b32 v185, 16, v185
U 185,185,182
K 185,185,185
v_lshlrev_b32 v185, 16, v185
A 185,185,69
K 185,185,185
v_lshlrev_b32 v185, 16, v185
v_cndmask_b32 v69, v69, v185, vcc
K 186,186,186
v_lshlrev_b32 v186, 16, v186
U 186,186,182
K 186,186,186
v_lshlrev_b32 v186, 16, v186
A 186,186,70
K 186,186,186
v_lshlrev_b32 v186, 16, v186
v_cndmask_b32 v70, v70, v186, vcc
K 187,187,187
v_lshlrev_b32 v187, 16, v187
U 187,187,182
K 187,187,187
v_lshlrev_b32 v187, 16, v187
A 187,187,71
K 187,187,187
v_lshlrev_b32 v187, 16, v187
v_cndmask_b32 v71, v71, v187, vcc
K 188,188,188
v_lshlrev_b32 v188, 16, v188
U 188,188,182
K 188,188,188
v_lshlrev_b32 v188, 16, v188
A 188,188,72
K 188,188,188
v_lshlrev_b32 v188, 16, v188
v_cndmask_b32 v72, v72, v188, vcc
K 189,189,189
v_lshlrev_b32 v189, 16, v189
U 189,189,182
K 189,189,189
v_lshlrev_b32 v189, 16, v189
A 189,189,73
K 189,189,189
v_lshlrev_b32 v189, 16, v189
v_cndmask_b32 v73, v73, v189, vcc
K 190,190,190
v_lshlrev_b32 v190, 16, v190
U 190,190,182
K 190,190,190
v_lshlrev_b32 v190, 16, v190
A 190,190,74
K 190,190,190
v_lshlrev_b32 v190, 16, v190
v_cndmask_b32 v74, v74, v190, vcc
K 191,191,191
v_lshlrev_b32 v191, 16, v191
U 191,191,182
K 191,191,191
v_lshlrev_b32 v191, 16, v191
A 191,191,75
K 191,191,191
v_lshlrev_b32 v191, 16, v191
v_cndmask_b32 v75, v75, v191, vcc
A 216,216,224
A 217,217,225
A 218,218,226
A 219,219,227
A 220,220,228
A 221,221,229
A 222,222,230
A 223,223,231
A 216,216,232
A 217,217,233
A 218,218,234
A 219,219,235
A 220,220,236
A 221,221,237
A 222,222,238
A 223,223,239
A 216,216,240
A 217,217,241
A 218,218,242
A 219,219,243
A 220,220,244
A 221,221,245
A 222,222,246
A 223,223,247
v_add_f32_dpp v216, v216, v216 quad_perm:[1,0,3,2] row_mask:0xf bank_mask:0xf
v_add_f32_dpp v217, v217, v217 quad_perm:[1,0,3,2] row_mask:0xf bank_mask:0xf
v_add_f32_dpp v218, v218, v218 quad_perm:[1,0,3,2] row_mask:0xf bank_mask:0xf
v_add_f32_dpp v219, v219, v219 quad_perm:[1,0,3,2] row_mask:0xf bank_mask:0xf
v_add_f32_dpp v220, v220, v220 quad_perm:[1,0,3,2] row_mask:0xf bank_mask:0xf
v_add_f32_dpp v221, v221, v221 quad_perm:[1,0,3,2] row_mask:0xf bank_mask:0xf
v_add_f32_dpp v222, v222, v222 quad_perm:[1,0,3,2] row_mask:0xf bank_mask:0xf
v_add_f32_dpp v223, v223, v223 quad_perm:[1,0,3,2] row_mask:0xf bank_mask:0xf
v_add_f32_dpp v216, v216, v216 quad_perm:[2,3,0,1] row_mask:0xf bank_mask:0xf
v_add_f32_dpp v217, v217, v217 quad_perm:[2,3,0,1] row_mask:0xf bank_mask:0xf
v_add_f32_dpp v218, v218, v218 quad_perm:[2,3,0,1] row_mask:0xf bank_mask:0xf
v_add_f32_dpp v219, v219, v219 quad_perm:[2,3,0,1] row_mask:0xf bank_mask:0xf
v_add_f32_dpp v220, v220, v220 quad_perm:[2,3,0,1] row_mask:0xf bank_mask:0xf
v_add_f32_dpp v221, v221, v221 quad_perm:[2,3,0,1] row_mask:0xf bank_mask:0xf
v_add_f32_dpp v222, v222, v222 quad_perm:[2,3,0,1] row_mask:0xf bank_mask:0xf
v_add_f32_dpp v223, v223, v223 quad_perm:[2,3,0,1] row_mask:0xf bank_mask:0xf
v_add_f32_dpp v216, v216, v216 row_half_mirror row_mask:0xf bank_mask:0xf
v_add_f32_dpp v217, v217, v217 row_half_mirror row_mask:0xf bank_mask:0xf
v_add_f32_dpp v218, v218, v218 row_half_mirror row_mask:0xf bank_mask:0xf
v_add_f32_dpp v219, v219, v219 row_half_mirror row_mask:0xf bank_mask:0xf
v_add_f32_dpp v220, v220, v220 row_half_mirror row_mask:0xf bank_mask:0xf
v_add_f32_dpp v221, v221, v221 row_half_mirror row_mask:0xf bank_mask:0xf
v_add_f32_dpp v222, v222, v222 row_half_mirror row_mask:0xf bank_mask:0xf
v_add_f32_dpp v223, v223, v223 row_half_mirror row_mask:0xf bank_mask:0xf
v_add_f32_dpp v216, v216, v216 row_mirror row_mask:0xf bank_mask:0xf
v_add_f32_dpp v217, v217, v217 row_mirror row_mask:0xf bank_mask:0xf
v_add_f32_dpp v218, v218, v218 row_mirror row_mask:0xf bank_mask:0xf
v_add_f32_dpp v219, v219, v219 row_mirror row_mask:0xf bank_mask:0xf
v_add_f32_dpp v220, v220, v220 row_mirror row_mask:0xf bank_mask:0xf
v_add_f32_dpp v221, v221, v221 row_mirror row_mask:0xf bank_mask:0xf
v_add_f32_dpp v222, v222, v222 row_mirror row_mask:0xf bank_mask:0xf
v_add_f32_dpp v223, v223, v223 row_mirror row_mask:0xf bank_mask:0xf
K 216,216,216
v_lshlrev_b32 v216, 16, v216
U 216,216,182
K 216,216,216
v_lshlrev_b32 v216, 16, v216
A 216,216,84
K 216,216,216
v_lshlrev_b32 v216, 16, v216
v_cndmask_b32 v84, v84, v216, vcc
K 217,217,217
v_lshlrev_b32 v217, 16, v217
U 217,217,182
K 217,217,217
v_lshlrev_b32 v217, 16, v217
A 217,217,85
K 217,217,217
v_lshlrev_b32 v217, 16, v217
v_cndmask_b32 v85, v85, v217, vcc
K 218,218,218
v_lshlrev_b32 v218, 16, v218
U 218,218,182
K 218,218,218
v_lshlrev_b32 v218, 16, v218
A 218,218,86
K 218,218,218
v_lshlrev_b32 v218, 16, v218
v_cndmask_b32 v86, v86, v218, vcc
K 219,219,219
v_lshlrev_b32 v219, 16, v219
U 219,219,182
K 219,219,219
v_lshlrev_b32 v219, 16, v219
A 219,219,87
K 219,219,219
v_lshlrev_b32 v219, 16, v219
v_cndmask_b32 v87, v87, v219, vcc
K 220,220,220
v_lshlrev_b32 v220, 16, v220
U 220,220,182
K 220,220,220
v_lshlrev_b32 v220, 16, v220
A 220,220,88
K 220,220,220
v_lshlrev_b32 v220, 16, v220
v_cndmask_b32 v88, v88, v220, vcc
K 221,221,221
v_lshlrev_b32 v221, 16, v221
U 221,221,182
K 221,221,221
v_lshlrev_b32 v221, 16, v221
A 221,221,89
K 221,221,221
v_lshlrev_b32 v221, 16, v221
v_cndmask_b32 v89, v89, v221, vcc
K 222,222,222
v_lshlrev_b32 v222, 16, v222
U 222,222,182
K 222,222,222
v_lshlrev_b32 v222, 16, v222
A 222,222,90
K 222,222,222
v_lshlrev_b32 v222, 16, v222
v_cndmask_b32 v90, v90, v222, vcc
K 223,223,223
v_lshlrev_b32 v223, 16, v223
U 223,223,182
K 223,223,223
v_lshlrev_b32 v223, 16, v223
A 223,223,91
K 223,223,223
v_lshlrev_b32 v223, 16, v223
v_cndmask_b32 v91, v91, v223, vcc
s_cmp_lg_u32 s65, 0
s_cbranch_scc1 .LL_117%=
s_branch .Lupr_116%=
.Lup_120%=:
v_lshrrev_b32 v180, 1, v1
v_min_u32 v181, 344, v180
s_mov_b32 s84, 0
.LL_124%=:
buffer_load_dwordx2 v[184:185], v180, s[8:11], 0 offen offset:2560 sc1
buffer_load_dwordx2 v[186:187], v181, s[8:11], 0 offen offset:3072 sc1
s_waitcnt vmcnt(0)
v_cmp_eq_u64 vcc, s[12:13], v[184:185]
s_mov_b64 s[86:87], vcc
v_cmp_eq_u64 vcc, s[12:13], v[186:187]
s_and_b64 s[86:87], s[86:87], vcc
s_cmp_eq_u64 s[86:87], -1
s_cbranch_scc1 .LL_123%=
s_sleep 2
s_add_u32 s84, s84, 1
s_cmp_lt_u32 s84, 1048576
s_cbranch_scc1 .LL_124%=
.LL_123%=:
v_bfe_u32 v182, v1, 4, 4
v_mul_u32_u24 v180, 20480, v182
v_and_b32 v181, 0x300, v1
v_add_u32 v180, v180, v181
v_cmp_gt_u32 vcc, 6, v182
v_mov_b32 v181, 0x80000000
s_nop 0
v_cndmask_b32 v181, v181, v180, vcc
.LL_122%=:
s_ff1_i32_b32 s66, s65
s_bitset0_b32 s65, s66
s_nop 3
v_readlane_b32 s67, v12, s66
s_nop 3
s_lshl_b32 s80, s67, 2
s_add_u32 s80, s80, 2752512
buffer_load_dword v182, off, s[8:11], s80 sc1
s_mul_i32 s80, s58, 5120
s_lshl_b32 s81, s66, 4
s_add_u32 s80, s80, s81
s_lshl_b32 s81, s14, 10
s_add_u32 s80, s80, s81
s_add_u32 s80, s80, 2760704
buffer_load_dwordx4 v[184:187], v180, s[8:11], s80 offen offset:0 sc1
buffer_load_dwordx4 v[188:191], v180, s[8:11], s80 offen offset:128 sc1
s_add_u32 s80, s80, 327680
buffer_load_dwordx4 v[192:195], v180, s[8:11], s80 offen offset:0 sc1
buffer_load_dwordx4 v[196:199], v180, s[8:11], s80 offen offset:128 sc1
s_add_u32 s80, s80, 327680
buffer_load_dwordx4 v[200:203], v180, s[8:11], s80 offen offset:0 sc1
buffer_load_dwordx4 v[204:207], v180, s[8:11], s80 offen offset:128 sc1
s_add_u32 s80, s80, 327680
buffer_load_dwordx4 v[208:211], v181, s[8:11], s80 offen offset:0 sc1
buffer_load_dwordx4 v[212:215], v181, s[8:11], s80 offen offset:128 sc1
s_waitcnt vmcnt(0)
v_and_b32 v182, 0xffff0000, v182
v_ashrrev_i32 v183, 31, v182
v_not_b32 v183, v183
v_or_b32 v183, 0x80000000, v183
v_xor_b32 v182, v182, v183
v_and_b32 v182, 0xffff0000, v182
v_mul_f32 v182, 0xbfb8aa3b, v182
X 182
s_nop 0
v_add_f32 v182, 1.0, v182
v_rcp_f32 v182, v182
s_nop 0
K 182,182,182
v_lshlrev_b32 v182, 16, v182
s_and_b32 s67, s67, 15
v_bfe_u32 v183, v1, 4, 4
v_cmp_eq_u32 vcc, s67, v183
A 184,184,192
A 185,185,193
A 186,186,194
A 187,187,195
A 188,188,196
A 189,189,197
A 190,190,198
A 191,191,199
A 184,184,200
A 185,185,201
A 186,186,202
A 187,187,203
A 188,188,204
A 189,189,205
A 190,190,206
A 191,191,207
A 184,184,208
A 185,185,209
A 186,186,210
A 187,187,211
A 188,188,212
A 189,189,213
A 190,190,214
A 191,191,215
v_add_f32_dpp v184, v184, v184 quad_perm:[1,0,3,2] row_mask:0xf bank_mask:0xf
v_add_f32_dpp v185, v185, v185 quad_perm:[1,0,3,2] row_mask:0xf bank_mask:0xf
v_add_f32_dpp v186, v186, v186 quad_perm:[1,0,3,2] row_mask:0xf bank_mask:0xf
v_add_f32_dpp v187, v187, v187 quad_perm:[1,0,3,2] row_mask:0xf bank_mask:0xf
v_add_f32_dpp v188, v188, v188 quad_perm:[1,0,3,2] row_mask:0xf bank_mask:0xf
v_add_f32_dpp v189, v189, v189 quad_perm:[1,0,3,2] row_mask:0xf bank_mask:0xf
v_add_f32_dpp v190, v190, v190 quad_perm:[1,0,3,2] row_mask:0xf bank_mask:0xf
v_add_f32_dpp v191, v191, v191 quad_perm:[1,0,3,2] row_mask:0xf bank_mask:0xf
v_add_f32_dpp v184, v184, v184 quad_perm:[2,3,0,1] row_mask:0xf bank_mask:0xf
v_add_f32_dpp v185, v185, v185 quad_perm:[2,3,0,1] row_mask:0xf bank_mask:0xf
v_add_f32_dpp v186, v186, v186 quad_perm:[2,3,0,1] row_mask:0xf bank_mask:0xf
v_add_f32_dpp v187, v187, v187 quad_perm:[2,3,0,1] row_mask:0xf bank_mask:0xf
v_add_f32_dpp v188, v188, v188 quad_perm:[2,3,0,1] row_mask:0xf bank_mask:0xf
v_add_f32_dpp v189, v189, v189 quad_perm:[2,3,0,1] row_mask:0xf bank_mask:0xf
v_add_f32_dpp v190, v190, v190 quad_perm:[2,3,0,1] row_mask:0xf bank_mask:0xf
v_add_f32_dpp v191, v191, v191 quad_perm:[2,3,0,1] row_mask:0xf bank_mask:0xf
v_add_f32_dpp v184, v184, v184 row_half_mirror row_mask:0xf bank_mask:0xf
v_add_f32_dpp v185, v185, v185 row_half_mirror row_mask:0xf bank_mask:0xf
v_add_f32_dpp v186, v186, v186 row_half_mirror row_mask:0xf bank_mask:0xf
v_add_f32_dpp v187, v187, v187 row_half_mirror row_mask:0xf bank_mask:0xf
v_add_f32_dpp v188, v188, v188 row_half_mirror row_mask:0xf bank_mask:0xf
v_add_f32_dpp v189, v189, v189 row_half_mirror row_mask:0xf bank_mask:0xf
v_add_f32_dpp v190, v190, v190 row_half_mirror row_mask:0xf bank_mask:0xf
v_add_f32_dpp v191, v191, v191 row_half_mirror row_mask:0xf bank_mask:0xf
v_add_f32_dpp v184, v184, v184 row_mirror row_mask:0xf bank_mask:0xf
v_add_f32_dpp v185, v185, v185 row_mirror row_mask:0xf bank_mask:0xf
v_add_f32_dpp v186, v186, v186 row_mirror row_mask:0xf bank_mask:0xf
v_add_f32_dpp v187, v187, v187 row_mirror row_mask:0xf bank_mask:0xf
v_add_f32_dpp v188, v188, v188 row_mirror row_mask:0xf bank_mask:0xf
v_add_f32_dpp v189, v189, v189 row_mirror row_mask:0xf bank_mask:0xf
v_add_f32_dpp v190, v190, v190 row_mirror row_mask:0xf bank_mask:0xf
v_add_f32_dpp v191, v191, v191 row_mirror row_mask:0xf bank_mask:0xf
K 184,184,184
v_lshlrev_b32 v184, 16, v184
U 184,184,182
K 184,184,184
v_lshlrev_b32 v184, 16, v184
A 184,184,68
K 184,184,184
v_lshlrev_b32 v184, 16, v184
v_cndmask_b32 v68, v68, v184, vcc
K 185,185,185
v_lshlrev_b32 v185, 16, v185
U 185,185,182
K 185,185,185
v_lshlrev_b32 v185, 16, v185
A 185,185,69
K 185,185,185
v_lshlrev_b32 v185, 16, v185
v_cndmask_b32 v69, v69, v185, vcc
K 186,186,186
v_lshlrev_b32 v186, 16, v186
U 186,186,182
K 186,186,186
v_lshlrev_b32 v186, 16, v186
A 186,186,70
)"
R"(K 186,186,186
v_lshlrev_b32 v186, 16, v186
v_cndmask_b32 v70, v70, v186, vcc
K 187,187,187
v_lshlrev_b32 v187, 16, v187
U 187,187,182
K 187,187,187
v_lshlrev_b32 v187, 16, v187
A 187,187,71
K 187,187,187
v_lshlrev_b32 v187, 16, v187
v_cndmask_b32 v71, v71, v187, vcc
K 188,188,188
v_lshlrev_b32 v188, 16, v188
U 188,188,182
K 188,188,188
v_lshlrev_b32 v188, 16, v188
A 188,188,72
K 188,188,188
v_lshlrev_b32 v188, 16, v188
v_cndmask_b32 v72, v72, v188, vcc
K 189,189,189
v_lshlrev_b32 v189, 16, v189
U 189,189,182
K 189,189,189
v_lshlrev_b32 v189, 16, v189
A 189,189,73
K 189,189,189
v_lshlrev_b32 v189, 16, v189
v_cndmask_b32 v73, v73, v189, vcc
K 190,190,190
v_lshlrev_b32 v190, 16, v190
U 190,190,182
K 190,190,190
v_lshlrev_b32 v190, 16, v190
A 190,190,74
K 190,190,190
v_lshlrev_b32 v190, 16, v190
v_cndmask_b32 v74, v74, v190, vcc
K 191,191,191
v_lshlrev_b32 v191, 16, v191
U 191,191,182
K 191,191,191
v_lshlrev_b32 v191, 16, v191
A 191,191,75
K 191,191,191
v_lshlrev_b32 v191, 16, v191
v_cndmask_b32 v75, v75, v191, vcc
s_cmp_lg_u32 s65, 0
s_cbranch_scc1 .LL_122%=
s_branch .Lupr_121%=
.Lend_126%=:
.Lend%=:
.purgem W
.purgem D
.purgem Ra
.purgem Ta
.purgem Zvaa
.purgem Zaaa
.purgem A
.purgem U
.purgem Mvaa
.purgem Maaa
.purgem K
.purgem Rv
.purgem Mvav
.purgem G
.purgem Dc
.purgem Cvaa
.purgem X
.purgem Zvav
.purgem Maav
.purgem Mvvv
.purgem Zvvv
)"
    : : [ka] "s"(__builtin_amdgcn_kernarg_segment_ptr()), [bid] "s"(blockIdx.x), [tid] "v"(threadIdx.x)
    : "v1", "v2", "v3", "v4", "v5", "v6", "v7", "v8", "v9", "v10", "v11", "v12", "v13", "v14", "v15", "v16", "v17", "v18", "v19", "v20", "v21", "v22", "v23", "v24", "v25", "v26", "v27", "v28", "v29", "v30", "v31", "v32", "v33", "v34", "v35", "v36", "v37", "v38", "v39", "v40", "v41", "v42", "v43", "v44", "v45", "v46", "v47", "v48", "v49", "v50", "v51", "v52", "v53", "v54", "v55", "v56", "v57", "v58", "v59", "v60", "v61", "v62", "v63", "v64", "v65", "v66", "v67", "v68", "v69", "v70", "v71", "v72", "v73", "v74", "v75", "v76", "v77", "v78", "v79", "v80", "v81", "v82", "v83", "v84", "v85", "v86", "v87", "v88", "v89", "v90", "v91", "v92", "v93", "v94", "v95", "v96", "v97", "v98", "v99", "v100", "v101", "v102", "v103", "v104", "v105", "v106", "v107", "v108", "v109", "v110", "v111", "v112", "v113", "v114", "v115", "v116", "v117", "v118", "v119", "v120", "v121", "v122", "v123", "v124", "v125", "v126", "v127", "v128", "v129", "v130", "v131", "v132", "v133", "v134", "v135", "v136", "v137", "v138", "v139", "v140", "v141", "v142", "v143", "v144", "v145", "v146", "v147", "v148", "v149", "v150", "v151", "v152", "v153", "v154", "v155", "v156", "v157", "v158", "v159", "v160", "v161", "v162", "v163", "v164", "v165", "v166", "v167", "v168", "v169", "v170", "v171", "v172", "v173", "v174", "v175", "v176", "v177", "v178", "v179", "v180", "v181", "v182", "v183", "v184", "v185", "v186", "v187", "v188", "v189", "v190", "v191", "v192", "v193", "v194", "v195", "v196", "v197", "v198", "v199", "v200", "v201", "v202", "v203", "v204", "v205", "v206", "v207", "v208", "v209", "v210", "v211", "v212", "v213", "v214", "v215", "v216", "v217", "v218", "v219", "v220", "v221", "v222", "v223", "v224", "v225", "v226", "v227", "v228", "v229", "v230", "v231", "v232", "v233", "v234", "v235", "v236", "v237", "v238", "v239", "v240", "v241", "v242", "v243", "v244", "v245", "v246", "v247", "v248", "v249", "v250", "v251", "v252", "v253", "v254", "v255", "a0", "a1", "a2", "a3", "a4", "a5", "a6", "a7", "a8", "a9", "a10", "a11", "a12", "a13", "a14", "a15", "a16", "a17", "a18", "a19", "a20", "a21", "a22", "a23", "a24", "a25", "a26", "a27", "a28", "a29", "a30", "a31", "a32", "a33", "a34", "a35", "a36", "a37", "a38", "a39", "a40", "a41", "a42", "a43", "a44", "a45", "a46", "a47", "a48", "a49", "a50", "a51", "a52", "a53", "a54", "a55", "a56", "a57", "a58", "a59", "a60", "a61", "a62", "a63", "a64", "a65", "a66", "a67", "a68", "a69", "a70", "a71", "a72", "a73", "a74", "a75", "a76", "a77", "a78", "a79", "a80", "a81", "a82", "a83", "a84", "a85", "a86", "a87", "a88", "a89", "a90", "a91", "a92", "a93", "a94", "a95", "a96", "a97", "a98", "a99", "a100", "a101", "a102", "a103", "a104", "a105", "a106", "a107", "a108", "a109", "a110", "a111", "a112", "a113", "a114", "a115", "a116", "a117", "a118", "a119", "a120", "a121", "a122", "a123", "a124", "a125", "a126", "a127", "a128", "a129", "a130", "a131", "a132", "a133", "a134", "a135", "a136", "a137", "a138", "a139", "a140", "a141", "a142", "a143", "a144", "a145", "a146", "a147", "a148", "a149", "a150", "a151", "a152", "a153", "a154", "a155", "a156", "a157", "a158", "a159", "a160", "a161", "a162", "a163", "a164", "a165", "a166", "a167", "a168", "a169", "a170", "a171", "a172", "a173", "a174", "a175", "a176", "a177", "a178", "a179", "a180", "a181", "a182", "a183", "a184", "a185", "a186", "a187", "a188", "a189", "a190", "a191", "a192", "a193", "a194", "a195", "a196", "a197", "a198", "a199", "a200", "a201", "a202", "a203", "a204", "a205", "a206", "a207", "a208", "a209", "a210", "a211", "a212", "a213", "a214", "a215", "a216", "a217", "a218", "a219", "a220", "a221", "a222", "a223", "a224", "a225", "a226", "a227", "a228", "a229", "a230", "a231", "a232", "a233", "a234", "a235", "a236", "a237", "a238", "a239", "a240", "a241", "a242", "a243", "a244", "a245", "a246", "a247", "a248", "a249", "a250", "a251", "a252", "a253", "a254", "a255", "s4", "s5", "s6", "s7", "s8", "s9", "s10", "s11", "s12", "s13", "s14", "s15", "s16", "s17", "s18", "s19", "s20", "s21", "s22", "s23", "s24", "s25", "s26", "s27", "s28", "s29", "s30", "s31", "s32", "s33", "s34", "s35", "s36", "s37", "s38", "s39", "s40", "s41", "s42", "s43", "s44", "s45", "s46", "s47", "s48", "s49", "s50", "s51", "s52", "s53", "s54", "s55", "s56", "s57", "s58", "s59", "s60", "s61", "s62", "s63", "s64", "s65", "s66", "s67", "s68", "s69", "s70", "s71", "s72", "s73", "s74", "s75", "s76", "s77", "s78", "s79", "s80", "s81", "s82", "s83", "s84", "s85", "s86", "s87", "s88", "s89", "s90", "s91", "s92", "s93", "s94", "s95", "s96", "s97", "s98", "s99", "vcc", "scc", "memory"
  );
}
extern "C" void kernel_launch(void* const* d_in, const int* in_sizes, int n_in, void* d_out, int out_size, void* d_ws, size_t ws_size, hipStream_t stream) {
  (void)hipFuncSetAttribute((const void*)mega_kernel, hipFuncAttributeMaxDynamicSharedMemorySize, LDS_BYTES);
  mega_kernel<<<256, 256, LDS_BYTES, stream>>>((const bf16*)d_in[0], (const bf16*)d_in[1], (const bf16*)d_in[2], (const bf16*)d_in[3], (const bf16*)d_in[4], (const bf16*)d_in[5],
      (const bf16*)d_in[7], (const bf16*)d_in[8], (const bf16*)d_in[9], (const bf16*)d_in[10], (char*)d_ws, (bf16*)d_out);
}
```

```cpp
#include <hip/hip_runtime.h>
#include <hip/hip_bf16.h>
typedef __hip_bfloat16 bf16;
constexpr int LDS_BYTES = 163840;
__global__ __launch_bounds__(256, 1) void mega_kernel(const bf16* x, const bf16* ckv, const bf16* wq, const bf16* wuk, const bf16* wuv, const bf16* wo,
    const bf16* w_r, const bf16* wg, const bf16* wu, const bf16* wd, char* ws, bf16* out) {
  asm volatile(
R"(.macro W n
s_waitcnt lgkmcnt(\n)
.endm
.macro D v,r,s,o
buffer_load_dwordx4 v\v, s[\r:\r+3], s\s offen offset:\o lds
.endm
.macro Ra d,a,o
ds_read_b128 a[\d:\d+3], v\a offset:\o
.endm
.macro Ta d,a,o
ds_read_b64_tr_b16 a[\d:\d+1], v\a offset:\o
.endm
.macro Zvaa d,a,b
v_mfma_f32_16x16x32_bf16 v[\d:\d+3], a[\a:\a+3], a[\b:\b+3], 0
.endm
.macro Zaaa d,a,b
v_mfma_f32_16x16x32_bf16 a[\d:\d+3], a[\a:\a+3], a[\b:\b+3], 0
.endm
.macro A d,a,b
v_add_f32 v\d, v\a, v\b
.endm
.macro U d,a,b
v_mul_f32 v\d, v\a, v\b
.endm
.macro Mvaa d,a,b
v_mfma_f32_16x16x32_bf16 v[\d:\d+3], a[\a:\a+3], a[\b:\b+3], v[\d:\d+3]
.endm
.macro Maaa d,a,b
v_mfma_f32_16x16x32_bf16 a[\d:\d+3], a[\a:\a+3], a[\b:\b+3], a[\d:\d+3]
.endm
.macro K d,a,b
v_cvt_pk_bf16_f32 v\d, v\a, v\b
.endm
.macro Rv d,a,o
ds_read_b128 v[\d:\d+3], v\a offset:\o
.endm
.macro Mvav d,a,b
v_mfma_f32_16x16x32_bf16 v[\d:\d+3], a[\a:\a+3], v[\b:\b+3], v[\d:\d+3]
.endm
.macro G d,a
v_accvgpr_read_b32 v\d, a\a
.endm
.macro Dc v,r,s,o
buffer_load_dwordx4 v\v, s[\r:\r+3], s\s offen offset:\o sc1 lds
.endm
.macro Cvaa d,a,b,c
v_mfma_f32_16x16x32_bf16 v[\d:\d+3], a[\a:\a+3], a[\b:\b+3], v[\c:\c+3]
.endm
.macro X d
v_exp_f32 v\d, v\d
.endm
.macro Zvav d,a,b
v_mfma_f32_16x16x32_bf16 v[\d:\d+3], a[\a:\a+3], v[\b:\b+3], 0
.endm
.macro Maav d,a,b
v_mfma_f32_16x16x32_bf16 a[\d:\d+3], a[\a:\a+3], v[\b:\b+3], a[\d:\d+3]
.endm
.macro Mvvv d,a,b
v_mfma_f32_16x16x32_bf16 v[\d:\d+3], v[\a:\a+3], v[\b:\b+3], v[\d:\d+3]
.endm
.macro Zvvv d,a,b
v_mfma_f32_16x16x32_bf16 v[\d:\d+3], v[\a:\a+3], v[\b:\b+3], 0
.endm
s_load_dwordx16 s[16:31], %[ka], 0
s_load_dwordx8 s[32:39], %[ka], 64
s_mov_b32 s15, %[bid]
v_lshrrev_b32 v2, 6, %[tid]
v_and_b32 v100, 63, %[tid]
v_readfirstlane_b32 s14, v2
v_lshlrev_b32 v1, 4, v100
v_lshrrev_b32 v101, 4, v100
v_and_b32 v102, 15, v100
s_and_b32 s59, s15, 7
s_bfe_u32 s57, s15, 0x30003
s_lshr_b32 s58, s15, 6
s_lshl_b32 s56, s59, 2
s_add_u32 s56, s56, s58
v_and_b32 v88, 0x1f0, v1
v_lshrrev_b32 v105, 1, v101
v_mul_u32_u24 v105, 1280, v105
v_add_u32 v28, v88, v105
s_lshl_b32 s85, s15, 2
s_add_u32 s85, s85, s14
s_mul_i32 s84, s85, 2560
v_and_b32 v105, 1, v101
v_lshl_or_b32 v29, v105, 31, v28
v_lshlrev_b32 v104, 1, v101
v_xor_b32 v104, v104, v102
v_lshlrev_b32 v104, 4, v104
v_lshl_add_u32 v10, v101, 13, v104
v_xor_b32 v105, 128, v10
v_add_u32 v11, 31744, v10
v_add_u32 v12, 63488, v105
v_add_u32 v13, 95232, v105
v_add_u32 v14, 131072, v10
v_add_u32 v15, 162816, v10
v_add_u32 v16, 194560, v105
v_add_u32 v17, 226304, v105
s_lshl_b32 s80, s14, 1
s_sub_u32 s81, 11, s80
s_cmp_lt_u32 s14, 2
s_cselect_b32 s80, s80, s81
s_lshl_b32 s80, s80, 3
s_add_u32 s80, s80, s57
s_lshl_b32 s80, s80, 15
s_xor_b32 s81, s80, 262144
v_and_b32 v104, 7, v100
v_xor_b32 v104, v104, v101
v_lshlrev_b32 v104, 4, v104
v_lshrrev_b32 v105, 3, v100
v_lshl_add_u32 v104, v105, 10, v104
v_add_u32 v2, s80, v104
v_xor_b32 v105, 64, v2
v_add_u32 v3, 7168, v105
v_add_u32 v4, 14336, v2
v_add_u32 v5, 21504, v105
v_xor_b32 v6, 262144, v2
v_xor_b32 v7, 262144, v3
v_xor_b32 v8, 262144, v4
v_xor_b32 v9, 262144, v5
s_lshl_b32 s63, s14, 13
s_add_u32 s61, s63, 65536
s_and_b32 s80, s14, 1
s_lshr_b32 s81, s14, 1
s_lshl_b32 s62, s80, 14
s_lshl_b32 s82, s81, 13
s_add_u32 s62, s62, s82
s_lshl_b32 s81, s81, 18
s_lshl_b32 s83, s56, 8
s_add_u32 s81, s81, s83
s_cmp_eq_u32 s80, 0
W 0
s_cselect_b32 s44, s22, s24
s_cselect_b32 s45, s23, s25
s_mov_b32 s69, 0
s_cmp_eq_u32 s14, 2
s_cbranch_scc1 .LL_1%=
s_cmp_eq_u32 s14, 3
s_cbranch_scc1 .LL_2%=
s_lshl_b32 s80, s58, 1
s_sub_u32 s82, 11, s80
s_cmp_lt_u32 s58, 2
s_cselect_b32 s80, s80, s82
s_lshl_b32 s80, s80, 3
s_add_u32 s80, s80, s57
s_lshl_b32 s80, s80, 15
s_xor_b32 s82, s80, 262144
s_lshl_b32 s82, s14, 7
s_add_u32 s80, s80, s82
s_add_u32 s80, s80, 256
v_lshl_add_u32 v104, v88, 6, s80
v_and_b32 v105, 512, v1
v_lshlrev_b32 v105, 9, v105
v_xor_b32 v104, v104, v105
v_mov_b32 v92, s18
v_mov_b32 v93, s19
s_mov_b32 s68, 256
s_branch .LL_3%=
.LL_1%=:
v_lshrrev_b32 v104, 6, v1
v_lshlrev_b32 v104, 13, v104
v_and_b32 v105, 16, v1
v_lshl_add_u32 v104, v105, 3, v104
s_lshl_b32 s80, s57, 17
s_add_u32 s80, s80, s83
s_add_u32 s80, s80, 1048576
v_add_u32 v104, s80, v104
v_and_b32 v105, 32, v1
v_cmp_eq_u32 vcc, 0, v105
v_mov_b32 v92, s24
v_mov_b32 v93, s25
v_mov_b32 v105, s22
v_mov_b32 v106, s23
v_cndmask_b32 v92, v92, v105, vcc
v_cndmask_b32 v93, v93, v106, vcc
s_mov_b32 s68, 1048576
s_branch .LL_3%=
.LL_2%=:
v_lshrrev_b32 v104, 5, v1
v_lshlrev_b32 v104, 13, v104
v_and_b32 v105, 16, v1
v_lshl_add_u32 v104, v105, 3, v104
s_mul_i32 s80, s57, 655360
s_add_u32 s80, s80, s83
v_add_u32 v104, s80, v104
v_mov_b32 v92, s20
v_mov_b32 v93, s21
s_mov_b32 s68, 196608
.LL_3%=:
v_mov_b32 v105, 0
v_lshl_add_u64 v[92:93], v[104:105], 0, v[92:93]
s_mov_b32 s40, s18
s_and_b32 s41, s19, 0xffff
s_mov_b32 s42, 0x200000
s_mov_b32 s43, 0x20000
s_add_u32 s44, s44, s81
s_addc_u32 s45, s45, 0
s_and_b32 s45, s45, 0xffff
s_mov_b32 s46, 0x400000
s_mov_b32 s47, 0x20000
s_sub_u32 s46, s46, s81
s_load_dwordx2 s[12:13], s[36:37], 0
s_mov_b32 s64, 0
s_add_u32 m0, s61, 0
s_nop 0
D 2,40,64,0
D 3,40,64,1024
D 4,40,64,2048
D 5,40,64,3072
s_add_u32 m0, s61, 4096
s_nop 0
D 6,40,64,0
D 7,40,64,1024
D 8,40,64,2048
D 9,40,64,3072
s_mov_b32 s65, 0
s_add_u32 m0, s62, 0
s_nop 0
D 10,44,65,0
D 11,44,65,1024
D 12,44,65,2048
D 13,44,65,3072
s_add_u32 m0, s62, 4096
s_nop 0
D 14,44,65,0
D 15,44,65,1024
D 16,44,65,2048
D 17,44,65,3072
s_mov_b32 s52, s16
s_and_b32 s53, s17, 0xffff
s_mov_b32 s54, 2621440
s_mov_b32 s55, 0x20000
buffer_load_dwordx4 v[32:35], v28, s[52:55], s84 offen
buffer_load_dwordx4 v[36:39], v28, s[52:55], s84 offen offset:512
buffer_load_dwordx4 v[40:43], v29, s[52:55], s84 offen offset:1024
s_mov_b32 s52, s28
s_and_b32 s53, s29, 0xffff
s_mov_b32 s54, 1280
s_mov_b32 s55, 0x20000
buffer_load_dwordx4 v[44:47], v88, s[52:55], 0 offen offset:0
buffer_load_dwordx4 v[48:51], v88, s[52:55], 0 offen offset:512
buffer_load_dwordx4 v[52:55], v88, s[52:55], 0 offen offset:1024
s_getpc_b64 s[6:7]
.Lbeg%=:
s_lshr_b32 s80, s15, 3
s_lshl_b32 s80, s80, 2
s_add_u32 s80, s80, s14
s_mul_i32 s80, s80, 1536
v_min_u32 v104, 176, v1
v_lshl_add_u32 v104, v104, 3, s80
v_min_u32 v104, .Lend%=-.Lbeg%=-4, v104
global_load_dword v94, v104, s[6:7]
s_mov_b32 s64, 128
s_add_u32 m0, s61, 32768
s_nop 0
D 2,40,64,0
D 3,40,64,1024
D 4,40,64,2048
D 5,40,64,3072
s_add_u32 m0, s61, 36864
s_nop 0
D 6,40,64,0
D 7,40,64,1024
D 8,40,64,2048
D 9,40,64,3072
s_mov_b32 s65, 524288
s_add_u32 m0, s62, 32768
s_nop 0
D 10,44,65,0
D 11,44,65,1024
D 12,44,65,2048
D 13,44,65,3072
s_add_u32 m0, s62, 36864
s_nop 0
D 14,44,65,0
D 15,44,65,1024
D 16,44,65,2048
D 17,44,65,3072
global_load_dword v94, v[92:93], off
v_lshl_add_u64 v[92:93], v[92:93], 0, s[68:69]
v_lshrrev_b32 v104, 1, v102
v_xor_b32 v104, v104, v101
v_lshlrev_b32 v104, 4, v104
v_lshl_add_u32 v104, v102, 7, v104
s_lshr_b32 s81, s14, 1
s_lshl_b32 s81, s81, 14
s_add_u32 s81, s81, 65536
v_add_u32 v18, s81, v104
v_xor_b32 v19, 64, v18
v_and_b32 v105, 1, v101
v_lshrrev_b32 v104, 2, v102
v_lshl_or_b32 v105, v105, 2, v104
v_lshl_add_u32 v104, v101, 3, v104
v_lshlrev_b32 v104, 8, v104
v_lshl_add_u32 v104, v105, 5, v104
v_and_b32 v105, 3, v102
v_lshl_add_u32 v20, v105, 3, v104
v_xor_b32 v21, 32, v20
v_xor_b32 v22, 64, v20
v_xor_b32 v23, 96, v20
v_xor_b32 v24, 128, v20
v_xor_b32 v25, 160, v20
v_xor_b32 v26, 192, v20
v_xor_b32 v27, 224, v20
.LL_7%=:
s_lshr_b32 s81, s15, 3
s_cmp_eq_u32 s14, 2
s_cbranch_scc1 .LL_4%=
s_cmp_eq_u32 s14, 3
s_cbranch_scc1 .LL_5%=
s_cmp_eq_u32 s14, 0
s_cselect_b64 s[76:77], s[30:31], s[32:33]
s_mov_b32 s72, 3456
s_mov_b32 s73, 28
s_mul_i32 s74, s81, 69120
s_mul_i32 s82, s59, 448
s_andn2_b32 s82, s82, 127
s_add_u32 s74, s74, s82
s_mov_b32 s75, 2211836
s_branch .LL_6%=
.LL_4%=:
s_mov_b64 s[76:77], s[34:35]
s_mov_b32 s72, 512
s_mov_b32 s73, 24
s_mul_i32 s74, s81, 8960
s_mul_i32 s82, s59, 286720
s_add_u32 s74, s74, s82
s_mov_b32 s75, 2211836
s_branch .LL_6%=
.LL_5%=:
s_mov_b64 s[76:77], s[26:27]
s_mov_b32 s72, 512
s_mov_b32 s73, 56
s_mul_i32 s74, s81, 20480
s_mul_i32 s82, s59, 655360
s_add_u32 s74, s74, s82
s_mov_b32 s75, 5242876
s_branch .LL_6%=
.LL_6%=:
s_mov_b32 s8, s36
s_and_b32 s9, s37, 0xffff
s_mov_b32 s10, 79691776
s_mov_b32 s11, 0x20000
s_lshl_b32 s81, s14, 18
s_add_u32 s81, s81, s83
s_add_u32 s48, s20, s81
s_addc_u32 s49, s21, 0
s_and_b32 s49, s49, 0xffff
s_mov_b32 s50, 5242880
s_mov_b32 s51, 0x20000
s_sub_u32 s50, s50, s81
s_lshl_b32 s80, s57, 1
s_and_b32 s81, s14, 1
s_add_u32 s80, s80, s81
s_sub_u32 s81, 31, s80
s_cmp_lt_u32 s14, 2
s_cselect_b32 s60, s80, s81
s_cselect_b32 s82, 0, 458752
s_cselect_b32 s70, 131072, 0
s_sub_u32 s70, s70, 69632
s_mul_i32 s66, s60, 81920
s_add_u32 s66, s66, 131072
s_and_b32 s80, s14, 1
s_mul_i32 s67, s80, 16910336
s_mul_i32 s80, s56, 528384
s_add_u32 s67, s67, s80
s_lshl_b32 s80, s57, 13
s_add_u32 s67, s67, s80
s_add_u32 s67, s67, s82
s_add_u32 s67, s67, 4194304
W 0
s_add_u32 s12, s12, 0x7f4a7c15
s_addc_u32 s13, s13, 0x9e3779b9
s_bitcmp1_b32 s14, 0
s_cbranch_scc1 .Lv_8%=
s_waitcnt vmcnt(24)
s_barrier
Ra 160,18,0
Ta 224,20,0
Ta 226,20,1024
Ra 164,18,2048
Ra 168,18,4096
Ra 172,18,6144
Ra 176,18,8192
Ra 180,18,10240
Ra 184,18,12288
Ra 188,18,14336
Ta 228,21,0
Ta 230,21,1024
W 6
Zvaa 160,224,160
Ta 232,22,0
Ta 234,22,1024
Zvaa 192,224,164
Ta 236,23,0
Ta 238,23,1024
Zvaa 224,224,168
s_waitcnt vmcnt(21)
Zaaa 0,224,172
v_lshlrev_b32 v56, 16, v32
W 6
Zaaa 32,224,176
v_and_b32 v57, 0xffff0000, v32
Zaaa 64,224,180
v_lshlrev_b32 v58, 16, v33
Zaaa 96,224,184
v_and_b32 v59, 0xffff0000, v33
Zaaa 128,224,188
v_lshlrev_b32 v60, 16, v34
W 4
Zaaa 132,228,188
v_and_b32 v61, 0xffff0000, v34
Ta 240,24,0
Zaaa 100,228,184
Ta 242,24,1024
v_lshlrev_b32 v62, 16, v35
Zaaa 68,228,180
v_and_b32 v63, 0xffff0000, v35
v_lshlrev_b32 v64, 16, v36
Zaaa 36,228,176
v_and_b32 v65, 0xffff0000, v36
Zaaa 4,228,172
v_lshlrev_b32 v66, 16, v37
Zvaa 228,228,168
v_and_b32 v67, 0xffff0000, v37
Zvaa 196,228,164
v_lshlrev_b32 v68, 16, v38
Zvaa 164,228,160
v_and_b32 v69, 0xffff0000, v38
W 4
Zvaa 168,232,160
v_lshlrev_b32 v70, 16, v39
v_and_b32 v71, 0xffff0000, v39
Zvaa 200,232,164
Ta 244,25,0
Ta 246,25,1024
Zvaa 232,232,168
v_lshlrev_b32 v72, 16, v40
v_and_b32 v73, 0xffff0000, v40
Zaaa 8,232,172
v_lshlrev_b32 v74, 16, v41
Zaaa 40,232,176
)"
R"(v_and_b32 v75, 0xffff0000, v41
Zaaa 72,232,180
v_lshlrev_b32 v76, 16, v42
v_and_b32 v77, 0xffff0000, v42
Zaaa 104,232,184
v_lshlrev_b32 v78, 16, v43
Zaaa 136,232,188
v_and_b32 v79, 0xffff0000, v43
W 4
Zaaa 140,236,188
A 80,56,57
Ta 248,26,0
Zaaa 108,236,184
Ta 250,26,1024
A 81,58,59
Zaaa 76,236,180
A 80,80,60
Zaaa 44,236,176
A 81,81,61
A 80,80,62
Zaaa 12,236,172
A 81,81,63
Ra 192,19,0
Zvaa 236,236,168
A 80,80,64
Zvaa 204,236,164
A 81,81,65
Zvaa 172,236,160
A 80,80,66
W 5
Zvaa 176,240,160
Ra 196,19,2048
A 81,81,67
Zvaa 208,240,164
Ta 252,27,0
Ta 254,27,1024
Zvaa 240,240,168
A 80,80,68
A 81,81,69
Zaaa 16,240,172
A 80,80,70
Ra 200,19,4096
Zaaa 48,240,176
A 81,81,71
A 80,80,72
Zaaa 80,240,180
A 81,81,73
Ra 204,19,6144
Zaaa 112,240,184
A 80,80,74
Zaaa 144,240,188
A 81,81,75
W 8
Zaaa 148,244,188
A 80,80,76
A 81,81,77
Zaaa 116,244,184
Ta 224,20,8192
Ta 226,20,9216
Zaaa 84,244,180
Ra 208,19,8192
A 80,80,78
Zaaa 52,244,176
A 81,81,79
A 80,80,81
Zaaa 20,244,172
Ra 212,19,10240
v_add_f32_dpp v80, v80, v80 quad_perm:[1,0,3,2] row_mask:0xf bank_mask:0xf
Zvaa 244,244,168
Zvaa 212,244,164
v_add_f32_dpp v80, v80, v80 quad_perm:[2,3,0,1] row_mask:0xf bank_mask:0xf
Ra 216,19,12288
Zvaa 180,244,160
v_add_f32_dpp v80, v80, v80 row_half_mirror row_mask:0xf bank_mask:0xf
W 11
Zvaa 184,248,160
v_add_f32_dpp v80, v80, v80 row_mirror row_mask:0xf bank_mask:0xf
v_mov_b32 v81, v80
Zvaa 216,248,164
Ta 228,21,8192
v_permlane16_swap_b32 v80, v81
Zvaa 248,248,168
Ta 230,21,9216
A 80,80,81
Zaaa 24,248,172
Ra 220,19,14336
v_mul_f32 v84, 0x3acccccd, v80
Zaaa 56,248,176
s_mov_b32 s86, 0xffff
s_mov_b32 s87, 0xffff
Zaaa 88,248,180
v_sub_f32 v82, v56, v84
U 80,82,82
Zaaa 120,248,184
v_sub_f32 v83, v57, v84
Zaaa 152,248,188
v_fmac_f32 v80, v83, v83
W 10
Zaaa 156,252,188
v_sub_f32 v82, v58, v84
Ta 232,22,8192
Zaaa 124,252,184
Ta 234,22,9216
v_fmac_f32 v80, v82, v82
Zaaa 92,252,180
v_sub_f32 v83, v59, v84
v_fmac_f32 v80, v83, v83
Zaaa 60,252,176
v_sub_f32 v82, v60, v84
Zaaa 28,252,172
v_fmac_f32 v80, v82, v82
Zvaa 252,252,168
v_sub_f32 v83, v61, v84
Zvaa 220,252,164
v_fmac_f32 v80, v83, v83
Zvaa 188,252,160
v_sub_f32 v82, v62, v84
W 8
Mvaa 160,224,192
v_fmac_f32 v80, v82, v82
v_sub_f32 v83, v63, v84
Mvaa 192,224,196
Ta 236,23,8192
Ta 238,23,9216
Mvaa 224,224,200
v_fmac_f32 v80, v83, v83
v_sub_f32 v82, v64, v84
Maaa 0,224,204
v_fmac_f32 v80, v82, v82
W 4
Maaa 32,224,208
v_sub_f32 v83, v65, v84
Maaa 64,224,212
v_fmac_f32 v80, v83, v83
Maaa 96,224,216
v_sub_f32 v82, v66, v84
Maaa 128,224,220
v_fmac_f32 v80, v82, v82
v_sub_f32 v83, v67, v84
Maaa 132,228,220
v_fmac_f32 v80, v83, v83
Ta 240,24,8192
Maaa 100,228,216
Ta 242,24,9216
v_sub_f32 v82, v68, v84
Maaa 68,228,212
v_fmac_f32 v80, v82, v82
Maaa 36,228,208
v_sub_f32 v83, v69, v84
Maaa 4,228,204
v_fmac_f32 v80, v83, v83
Ta 244,25,8192
Mvaa 228,228,200
Ta 246,25,9216
v_sub_f32 v82, v70, v84
Mvaa 196,228,196
v_fmac_f32 v80, v82, v82
v_sub_f32 v83, v71, v84
Mvaa 164,228,192
Ta 248,26,8192
W 7
Mvaa 168,232,192
Ta 250,26,9216
v_fmac_f32 v80, v83, v83
Mvaa 200,232,196
v_sub_f32 v82, v72, v84
Ta 252,27,8192
Mvaa 232,232,200
Ta 254,27,9216
U 81,82,82
Maaa 8,232,204
v_sub_f32 v83, v73, v84
v_fmac_f32 v81, v83, v83
Maaa 40,232,208
v_sub_f32 v82, v74, v84
v_fmac_f32 v81, v82, v82
Maaa 72,232,212
v_sub_f32 v83, v75, v84
Maaa 104,232,216
v_fmac_f32 v81, v83, v83
Maaa 136,232,220
v_sub_f32 v82, v76, v84
W 8
Maaa 140,236,220
v_fmac_f32 v81, v82, v82
Maaa 108,236,216
v_sub_f32 v83, v77, v84
v_fmac_f32 v81, v83, v83
Maaa 76,236,212
s_waitcnt vmcnt(1) lgkmcnt(0)
s_barrier
Maaa 44,236,208
v_sub_f32 v82, v78, v84
s_mov_b32 s64, 256
Maaa 12,236,204
v_fmac_f32 v81, v82, v82
v_sub_f32 v83, v79, v84
Mvaa 236,236,200
Ra 160,18,32768
v_fmac_f32 v81, v83, v83
Mvaa 204,236,196
s_add_u32 m0, s61, 0
v_cndmask_b32_e64 v81, 0, v81, s[86:87]
Mvaa 172,236,192
A 80,80,81
Ra 164,18,34816
Mvaa 176,240,192
v_add_f32_dpp v80, v80, v80 quad_perm:[1,0,3,2] row_mask:0xf bank_mask:0xf
D 2,40,64,0
Mvaa 208,240,196
v_add_f32_dpp v80, v80, v80 quad_perm:[2,3,0,1] row_mask:0xf bank_mask:0xf
Mvaa 240,240,200
Ra 168,18,36864
v_add_f32_dpp v80, v80, v80 row_half_mirror row_mask:0xf bank_mask:0xf
Maaa 16,240,204
Maaa 48,240,208
v_add_f32_dpp v80, v80, v80 row_mirror row_mask:0xf bank_mask:0xf
D 3,40,64,1024
Maaa 80,240,212
v_mov_b32 v81, v80
Ra 172,18,38912
Maaa 112,240,216
v_permlane16_swap_b32 v80, v81
A 80,80,81
Maaa 144,240,220
v_mul_f32 v80, 0x3acccccd, v80
K 80,80,80
Maaa 148,244,220
D 4,40,64,2048
v_lshlrev_b32 v80, 16, v80
Maaa 116,244,216
v_add_f32 v80, 0x3727c5ac, v80
Ta 224,20,32768
Maaa 84,244,212
Ta 226,20,33792
Ra 176,18,40960
Maaa 52,244,208
K 80,80,80
v_lshlrev_b32 v80, 16, v80
Maaa 20,244,204
D 5,40,64,3072
s_add_u32 m0, s61, 4096
Mvaa 244,244,200
v_sqrt_f32 v81, v80
Mvaa 212,244,196
Ra 180,18,43008
Ra 184,18,45056
Mvaa 180,244,192
v_rcp_f32 v82, v81
Mvaa 184,248,192
D 6,40,64,0
v_fma_f32 v83, -v81, v81, v80
Mvaa 216,248,196
v_mul_f32 v82, 0.5, v82
v_fma_f32 v81, v83, v82, v81
Mvaa 248,248,200
Ta 228,21,32768
Ta 230,21,33792
Maaa 24,248,204
K 86,81,81
Ra 188,18,47104
Maaa 56,248,208
D 7,40,64,1024
v_lshlrev_b32 v86, 16, v86
Maaa 88,248,212
v_rcp_f32 v87, v86
Maaa 120,248,216
K 85,84,84
v_lshlrev_b32 v85, 16, v85
Maaa 152,248,220
v_sub_f32 v56, v56, v85
Maaa 156,252,220
D 8,40,64,2048
v_sub_f32 v57, v57, v85
Maaa 124,252,216
K 80,56,57
Ta 232,22,32768
Maaa 92,252,212
Ta 234,22,33792
v_lshlrev_b32 v56, 16, v80
Maaa 60,252,208
v_and_b32 v57, 0xffff0000, v80
U 80,56,87
Maaa 28,252,204
D 9,40,64,3072
s_mov_b32 s65, 1048576
Mvaa 252,252,200
s_add_u32 m0, s62, 0
U 81,57,87
Mvaa 220,252,196
v_fma_f32 v82, -v80, v86, v56
v_fma_f32 v83, -v81, v86, v57
Mvaa 188,252,192
v_fmac_f32 v80, v82, v87
W 8
Mvaa 160,224,160
D 10,44,65,0
v_fmac_f32 v81, v83, v87
Mvaa 192,224,164
K 32,80,81
Ta 236,23,32768
Mvaa 224,224,168
Ta 238,23,33792
v_lshlrev_b32 v80, 16, v32
Maaa 0,224,172
v_and_b32 v81, 0xffff0000, v32
W 4
Maaa 32,224,176
D 11,44,65,1024
v_lshlrev_b32 v82, 16, v44
Maaa 64,224,180
v_and_b32 v83, 0xffff0000, v44
U 84,80,82
Maaa 96,224,184
v_fmac_f32 v84, v81, v83
v_sub_f32 v58, v58, v85
Maaa 128,224,188
v_sub_f32 v59, v59, v85
Maaa 132,228,188
D 12,44,65,2048
K 80,58,59
Maaa 100,228,184
Ta 240,24,32768
Ta 242,24,33792
Maaa 68,228,180
v_lshlrev_b32 v58, 16, v80
v_and_b32 v59, 0xffff0000, v80
Maaa 36,228,176
U 80,58,87
U 81,59,87
Maaa 4,228,172
D 13,44,65,3072
s_add_u32 m0, s62, 4096
Mvaa 228,228,168
v_fma_f32 v82, -v80, v86, v58
v_fma_f32 v83, -v81, v86, v59
Mvaa 196,228,164
v_fmac_f32 v80, v82, v87
Mvaa 164,228,160
v_fmac_f32 v81, v83, v87
W 4
Mvaa 168,232,160
D 14,44,65,0
K 33,80,81
Mvaa 200,232,164
Ta 244,25,32768
Ta 246,25,33792
Mvaa 232,232,168
v_lshlrev_b32 v80, 16, v33
v_and_b32 v81, 0xffff0000, v33
Maaa 8,232,172
v_lshlrev_b32 v82, 16, v45
v_and_b32 v83, 0xffff0000, v45
Maaa 40,232,176
D 15,44,65,1024
v_fmac_f32 v84, v80, v82
Maaa 72,232,180
v_fmac_f32 v84, v81, v83
Maaa 104,232,184
v_sub_f32 v60, v60, v85
Maaa 136,232,188
v_sub_f32 v61, v61, v85
W 4
Maaa 140,236,188
D 16,44,65,2048
K 80,60,61
Maaa 108,236,184
v_lshlrev_b32 v60, 16, v80
Ta 248,26,32768
Maaa 76,236,180
Ta 250,26,33792
v_and_b32 v61, 0xffff0000, v80
Maaa 44,236,176
U 80,60,87
U 81,61,87
Maaa 12,236,172
D 17,44,65,3072
v_fma_f32 v82, -v80, v86, v60
Mvaa 236,236,168
Ra 192,19,32768
v_fma_f32 v83, -v81, v86, v61
Mvaa 204,236,164
v_fmac_f32 v80, v82, v87
v_fmac_f32 v81, v83, v87
Mvaa 172,236,160
K 34,80,81
W 5
Mvaa 176,240,160
global_load_dword v94, v[92:93], off
v_lshl_add_u64 v[92:93], v[92:93], 0, s[68:69]
Mvaa 208,240,164
Ra 196,19,34816
v_lshlrev_b32 v80, 16, v34
Mvaa 240,240,168
Ta 252,27,32768
Ta 254,27,33792
Maaa 16,240,172
v_and_b32 v81, 0xffff0000, v34
v_lshlrev_b32 v82, 16, v46
Maaa 48,240,176
)"
R"(Ra 200,19,36864
v_and_b32 v83, 0xffff0000, v46
Maaa 80,240,180
v_fmac_f32 v84, v80, v82
v_fmac_f32 v84, v81, v83
Maaa 112,240,184
v_sub_f32 v62, v62, v85
Ra 204,19,38912
Maaa 144,240,188
v_sub_f32 v63, v63, v85
W 8
Maaa 148,244,188
K 80,62,63
v_lshlrev_b32 v62, 16, v80
Maaa 116,244,184
Ta 224,20,40960
Ta 226,20,41984
Maaa 84,244,180
Ra 208,19,40960
v_and_b32 v63, 0xffff0000, v80
Maaa 52,244,176
U 80,62,87
U 81,63,87
Maaa 20,244,172
v_fma_f32 v82, -v80, v86, v62
Ra 212,19,43008
Mvaa 244,244,168
v_fma_f32 v83, -v81, v86, v63
v_fmac_f32 v80, v82, v87
Mvaa 212,244,164
v_fmac_f32 v81, v83, v87
Ra 216,19,45056
Mvaa 180,244,160
K 35,80,81
W 11
Mvaa 184,248,160
v_lshlrev_b32 v80, 16, v35
v_and_b32 v81, 0xffff0000, v35
Mvaa 216,248,164
Ta 228,21,40960
Ta 230,21,41984
Mvaa 248,248,168
v_lshlrev_b32 v82, 16, v47
Ra 220,19,47104
Maaa 24,248,172
v_and_b32 v83, 0xffff0000, v47
v_fmac_f32 v84, v80, v82
Maaa 56,248,176
v_fmac_f32 v84, v81, v83
Maaa 88,248,180
v_sub_f32 v64, v64, v85
Maaa 120,248,184
v_sub_f32 v65, v65, v85
K 80,64,65
Maaa 152,248,188
v_lshlrev_b32 v64, 16, v80
W 10
Maaa 156,252,188
v_and_b32 v65, 0xffff0000, v80
Ta 232,22,40960
Maaa 124,252,184
Ta 234,22,41984
U 80,64,87
Maaa 92,252,180
U 81,65,87
Maaa 60,252,176
v_fma_f32 v82, -v80, v86, v64
Maaa 28,252,172
v_fma_f32 v83, -v81, v86, v65
v_fmac_f32 v80, v82, v87
Mvaa 252,252,168
v_fmac_f32 v81, v83, v87
Mvaa 220,252,164
K 36,80,81
Mvaa 188,252,160
v_lshlrev_b32 v80, 16, v36
W 8
Mvaa 160,224,192
v_and_b32 v81, 0xffff0000, v36
Ta 236,23,40960
Mvaa 192,224,196
Ta 238,23,41984
v_lshlrev_b32 v82, 16, v48
Mvaa 224,224,200
v_and_b32 v83, 0xffff0000, v48
v_fmac_f32 v84, v80, v82
Maaa 0,224,204
v_fmac_f32 v84, v81, v83
W 4
Maaa 32,224,208
v_sub_f32 v66, v66, v85
Maaa 64,224,212
v_sub_f32 v67, v67, v85
Maaa 96,224,216
K 80,66,67
Maaa 128,224,220
v_lshlrev_b32 v66, 16, v80
Maaa 132,228,220
v_and_b32 v67, 0xffff0000, v80
Ta 240,24,40960
Maaa 100,228,216
Ta 242,24,41984
U 80,66,87
Maaa 68,228,212
U 81,67,87
v_fma_f32 v82, -v80, v86, v66
Maaa 36,228,208
v_fma_f32 v83, -v81, v86, v67
Maaa 4,228,204
v_fmac_f32 v80, v82, v87
Ta 244,25,40960
Mvaa 228,228,200
Ta 246,25,41984
v_fmac_f32 v81, v83, v87
Mvaa 196,228,196
K 37,80,81
Ta 248,26,40960
Mvaa 164,228,192
Ta 250,26,41984
W 8
Mvaa 168,232,192
v_lshlrev_b32 v80, 16, v37
v_and_b32 v81, 0xffff0000, v37
Mvaa 200,232,196
v_lshlrev_b32 v82, 16, v49
Ta 252,27,40960
Mvaa 232,232,200
Ta 254,27,41984
v_and_b32 v83, 0xffff0000, v49
Maaa 8,232,204
v_fmac_f32 v84, v80, v82
v_fmac_f32 v84, v81, v83
Maaa 40,232,208
v_sub_f32 v68, v68, v85
Maaa 72,232,212
v_sub_f32 v69, v69, v85
K 80,68,69
Maaa 104,232,216
v_lshlrev_b32 v68, 16, v80
Maaa 136,232,220
v_and_b32 v69, 0xffff0000, v80
W 8
Maaa 140,236,220
U 80,68,87
Maaa 108,236,216
U 81,69,87
Maaa 76,236,212
s_waitcnt vmcnt(1) lgkmcnt(0)
s_barrier
Maaa 44,236,208
v_fma_f32 v82, -v80, v86, v68
s_mov_b32 s64, 384
Maaa 12,236,204
v_fma_f32 v83, -v81, v86, v69
v_fmac_f32 v80, v82, v87
Mvaa 236,236,200
v_fmac_f32 v81, v83, v87
Ra 160,18,0
Mvaa 204,236,196
K 38,80,81
s_add_u32 m0, s61, 32768
Mvaa 172,236,192
v_lshlrev_b32 v80, 16, v38
v_and_b32 v81, 0xffff0000, v38
Mvaa 176,240,192
Ra 164,18,2048
v_lshlrev_b32 v82, 16, v50
Mvaa 208,240,196
D 2,40,64,0
v_and_b32 v83, 0xffff0000, v50
Mvaa 240,240,200
v_fmac_f32 v84, v80, v82
v_fmac_f32 v84, v81, v83
Maaa 16,240,204
Ra 168,18,4096
v_sub_f32 v70, v70, v85
Maaa 48,240,208
v_sub_f32 v71, v71, v85
Maaa 80,240,212
D 3,40,64,1024
K 80,70,71
Maaa 112,240,216
Ra 172,18,6144
v_lshlrev_b32 v70, 16, v80
Maaa 144,240,220
v_and_b32 v71, 0xffff0000, v80
U 80,70,87
Maaa 148,244,220
U 81,71,87
Ta 224,20,0
Maaa 116,244,216
D 4,40,64,2048
Ta 226,20,1024
Maaa 84,244,212
Ra 176,18,8192
v_fma_f32 v82, -v80, v86, v70
Maaa 52,244,208
v_fma_f32 v83, -v81, v86, v71
v_fmac_f32 v80, v82, v87
Maaa 20,244,204
Ra 180,18,10240
v_fmac_f32 v81, v83, v87
Mvaa 244,244,200
D 5,40,64,3072
s_add_u32 m0, s61, 36864
Mvaa 212,244,196
K 39,80,81
v_lshlrev_b32 v80, 16, v39
Mvaa 180,244,192
v_and_b32 v81, 0xffff0000, v39
Ra 184,18,12288
Mvaa 184,248,192
v_lshlrev_b32 v82, 16, v51
v_and_b32 v83, 0xffff0000, v51
Mvaa 216,248,196
D 6,40,64,0
Ta 228,21,0
Mvaa 248,248,200
Ta 230,21,1024
v_fmac_f32 v84, v80, v82
Maaa 24,248,204
Ra 188,18,14336
v_fmac_f32 v84, v81, v83
Maaa 56,248,208
v_sub_f32 v72, v72, v85
v_sub_f32 v73, v73, v85
Maaa 88,248,212
D 7,40,64,1024
K 80,72,73
Maaa 120,248,216
v_lshlrev_b32 v72, 16, v80
v_and_b32 v73, 0xffff0000, v80
Maaa 152,248,220
U 80,72,87
Maaa 156,252,220
U 81,73,87
Ta 232,22,0
Maaa 124,252,216
D 8,40,64,2048
Ta 234,22,1024
Maaa 92,252,212
v_fma_f32 v82, -v80, v86, v72
v_fma_f32 v83, -v81, v86, v73
Maaa 60,252,208
v_fmac_f32 v80, v82, v87
v_fmac_f32 v81, v83, v87
Maaa 28,252,204
K 40,80,81
Mvaa 252,252,200
D 9,40,64,3072
s_mov_b32 s65, 1572864
Mvaa 220,252,196
v_lshlrev_b32 v80, 16, v40
s_add_u32 m0, s62, 32768
Mvaa 188,252,192
v_and_b32 v81, 0xffff0000, v40
W 8
Mvaa 160,224,160
v_lshlrev_b32 v82, 16, v52
v_and_b32 v83, 0xffff0000, v52
Mvaa 192,224,164
D 10,44,65,0
v_fmac_f32 v84, v80, v82
Mvaa 224,224,168
Ta 236,23,0
Ta 238,23,1024
Maaa 0,224,172
v_fmac_f32 v84, v81, v83
W 4
Maaa 32,224,176
v_sub_f32 v74, v74, v85
v_sub_f32 v75, v75, v85
Maaa 64,224,180
D 11,44,65,1024
K 80,74,75
Maaa 96,224,184
v_lshlrev_b32 v74, 16, v80
v_and_b32 v75, 0xffff0000, v80
Maaa 128,224,188
U 80,74,87
U 81,75,87
Maaa 132,228,188
v_fma_f32 v82, -v80, v86, v74
Ta 240,24,0
Maaa 100,228,184
D 12,44,65,2048
Ta 242,24,1024
Maaa 68,228,180
v_fma_f32 v83, -v81, v86, v75
v_fmac_f32 v80, v82, v87
Maaa 36,228,176
v_fmac_f32 v81, v83, v87
Maaa 4,228,172
K 41,80,81
Mvaa 228,228,168
D 13,44,65,3072
s_add_u32 m0, s62, 36864
Mvaa 196,228,164
v_lshlrev_b32 v80, 16, v41
v_and_b32 v81, 0xffff0000, v41
Mvaa 164,228,160
v_lshlrev_b32 v82, 16, v53
W 4
Mvaa 168,232,160
v_and_b32 v83, 0xffff0000, v53
v_fmac_f32 v84, v80, v82
Mvaa 200,232,164
D 14,44,65,0
Ta 244,25,0
Mvaa 232,232,168
Ta 246,25,1024
v_fmac_f32 v84, v81, v83
Maaa 8,232,172
v_sub_f32 v76, v76, v85
v_sub_f32 v77, v77, v85
Maaa 40,232,176
K 80,76,77
v_lshlrev_b32 v76, 16, v80
Maaa 72,232,180
D 15,44,65,1024
v_and_b32 v77, 0xffff0000, v80
Maaa 104,232,184
U 80,76,87
Maaa 136,232,188
U 81,77,87
W 4
Maaa 140,236,188
v_fma_f32 v82, -v80, v86, v76
Ta 248,26,0
Maaa 108,236,184
D 16,44,65,2048
Ta 250,26,1024
Maaa 76,236,180
v_fma_f32 v83, -v81, v86, v77
v_fmac_f32 v80, v82, v87
Maaa 44,236,176
v_fmac_f32 v81, v83, v87
K 42,80,81
Maaa 12,236,172
v_lshlrev_b32 v80, 16, v42
Ra 192,19,0
Mvaa 236,236,168
D 17,44,65,3072
v_and_b32 v81, 0xffff0000, v42
Mvaa 204,236,164
v_lshlrev_b32 v82, 16, v54
Mvaa 172,236,160
v_and_b32 v83, 0xffff0000, v54
W 5
Mvaa 176,240,160
v_fmac_f32 v84, v80, v82
Ra 196,19,2048
Mvaa 208,240,164
v_fmac_f32 v84, v81, v83
Ta 252,27,0
Mvaa 240,240,168
Ta 254,27,1024
v_sub_f32 v78, v78, v85
Maaa 16,240,172
v_sub_f32 v79, v79, v85
Ra 200,19,4096
Maaa 48,240,176
K 80,78,79
v_lshlrev_b32 v78, 16, v80
Maaa 80,240,180
v_and_b32 v79, 0xffff0000, v80
U 80,78,87
Maaa 112,240,184
Ra 204,19,6144
U 81,79,87
Maaa 144,240,188
v_fma_f32 v82, -v80, v86, v78
W 8
Maaa 148,244,188
v_fma_f32 v83, -v81, v86, v79
Ta 224,20,8192
Maaa 116,244,184
Ta 226,20,9216
Ra 208,19,8192
Maaa 84,244,180
v_fmac_f32 v80, v82, v87
v_fmac_f32 v81, v83, v87
Maaa 52,244,176
K 43,80,81
Ra 212,19,10240
Maaa 20,244,172
v_lshlrev_b32 v80, 16, v43
v_and_b32 v81, 0xffff0000, v43
Mvaa 244,244,168
v_lshlrev_b32 v82, 16, v55
Mvaa 212,244,164
v_and_b32 v83, 0xffff0000, v55
Ra 216,19,12288
Mvaa 180,244,160
v_fmac_f32 v84, v80, v82
W 11
Mvaa 184,248,160
v_fmac_f32 v84, v81, v83
Ta 228,21,8192
Mvaa 216,248,164
)"
R"(Ta 230,21,9216
v_lshrrev_b32 v82, 9, v1
Mvaa 248,248,168
Ra 220,19,14336
s_lshl_b32 s80, s14, 1
Maaa 24,248,172
v_add_u32 v82, s80, v82
v_mul_u32_u24 v83, 1552, v82
Maaa 56,248,176
v_add_u32 v83, v83, v88
Maaa 88,248,180
v_add_u32 v83, 131072, v83
Maaa 120,248,184
ds_write_b128 v83, v[32:35] offset:0
Maaa 152,248,188
ds_write_b128 v83, v[36:39] offset:512
W 12
Maaa 156,252,188
ds_write_b128 v83, v[40:43] offset:1024
v_add_f32_dpp v84, v84, v84 quad_perm:[1,0,3,2] row_mask:0xf bank_mask:0xf
Maaa 124,252,184
Ta 232,22,8192
Ta 234,22,9216
Maaa 92,252,180
v_add_f32_dpp v84, v84, v84 quad_perm:[2,3,0,1] row_mask:0xf bank_mask:0xf
Maaa 60,252,176
Maaa 28,252,172
v_add_f32_dpp v84, v84, v84 row_half_mirror row_mask:0xf bank_mask:0xf
Mvaa 252,252,168
Mvaa 220,252,164
v_add_f32_dpp v84, v84, v84 row_mirror row_mask:0xf bank_mask:0xf
v_mov_b32 v81, v84
Mvaa 188,252,160
W 11
Mvaa 160,224,192
v_permlane16_swap_b32 v84, v81
A 84,84,81
Mvaa 192,224,196
K 80,84,84
v_lshlrev_b32 v80, 16, v80
Mvaa 224,224,200
s_lshl_b32 s80, s15, 3
Ta 236,23,8192
Maaa 0,224,204
Ta 238,23,9216
W 7
Maaa 32,224,208
v_add_u32 v82, s80, v82
v_sub_u32 v83, 2047, v82
Maaa 64,224,212
v_ashrrev_i32 v81, 31, v80
v_or_b32 v81, 0x80000000, v81
Maaa 96,224,216
v_xor_b32 v80, v80, v81
v_and_b32 v80, 0xffff0000, v80
Maaa 128,224,220
v_or_b32 v80, v80, v83
v_lshlrev_b32 v81, 23, v88
Maaa 132,228,220
v_lshl_or_b32 v82, v82, 2, v81
Ta 240,24,8192
Maaa 100,228,216
Ta 242,24,9216
s_mov_b32 s82, 2752512
Maaa 68,228,212
buffer_store_dword v80, v82, s[8:11], s82 offen sc1
Maaa 36,228,208
Maaa 4,228,204
Ta 244,25,8192
Ta 246,25,9216
Mvaa 228,228,200
Mvaa 196,228,196
Ta 248,26,8192
Ta 250,26,9216
Mvaa 164,228,192
W 8
Mvaa 168,232,192
Ta 252,27,8192
Ta 254,27,9216
Mvaa 200,232,196
Mvaa 232,232,200
Maaa 8,232,204
Maaa 40,232,208
Maaa 72,232,212
Maaa 104,232,216
Maaa 136,232,220
W 8
Maaa 140,236,220
Maaa 108,236,216
Maaa 76,236,212
s_waitcnt vmcnt(1) lgkmcnt(0)
s_barrier
Maaa 44,236,208
s_mov_b32 s64, 512
v_bfe_u32 v80, v1, 4, 3
Maaa 12,236,204
v_mul_u32_u24 v81, 1552, v80
Ra 160,18,32768
Mvaa 236,236,200
v_lshrrev_b32 v82, 7, v1
s_add_u32 m0, s61, 0
Mvaa 204,236,196
v_lshl_add_u32 v81, v82, 4, v81
Mvaa 172,236,192
s_lshl_b32 s80, s14, 7
s_add_u32 s80, s80, 131072
Mvaa 176,240,192
Ra 164,18,34816
D 2,40,64,0
Mvaa 208,240,196
v_add_u32 v81, s80, v81
ds_read_b128 v[32:35], v81
Mvaa 240,240,200
Rv 36,81,512
Ra 168,18,36864
Maaa 16,240,204
s_and_b32 s80, s14, 2
s_lshl_b32 s80, s80, 7
Maaa 48,240,208
D 3,40,64,1024
v_subrev_u32 v81, s80, v81
Maaa 80,240,212
Rv 40,81,1024
Ra 172,18,38912
Maaa 112,240,216
v_lshlrev_b32 v80, 4, v80
v_and_b32 v81, 3, v82
Maaa 144,240,220
v_lshl_add_u32 v80, v81, 8, v80
Maaa 148,244,220
D 4,40,64,2048
v_lshrrev_b32 v82, 2, v82
Maaa 116,244,216
Ta 224,20,32768
Ta 226,20,33792
Maaa 84,244,212
Ra 176,18,40960
v_lshl_add_u32 v80, v82, 12, v80
Maaa 52,244,208
s_lshr_b32 s82, s15, 3
s_mul_i32 s82, s82, 81920
Maaa 20,244,204
D 5,40,64,3072
s_add_u32 m0, s61, 4096
Mvaa 244,244,200
s_bfe_u32 s80, s15, 0x20001
Ra 180,18,43008
Mvaa 212,244,196
s_lshl_b32 s80, s80, 10
s_add_u32 s82, s82, s80
Mvaa 180,244,192
s_and_b32 s80, s15, 1
s_lshl_b32 s80, s80, 7
Mvaa 184,248,192
D 6,40,64,0
Ra 184,18,45056
Mvaa 216,248,196
s_add_u32 s82, s82, s80
s_add_u32 s82, s82, 131072
Mvaa 248,248,200
Ta 228,21,32768
Ta 230,21,33792
Maaa 24,248,204
s_lshl_b32 s80, s14, 13
s_add_u32 s83, s82, s80
Maaa 56,248,208
D 7,40,64,1024
Ra 188,18,47104
Maaa 88,248,212
W 9
buffer_store_dwordx4 v[32:35], v80, s[8:11], s83 offen sc1
Maaa 120,248,216
s_add_u32 s83, s83, 32768
Maaa 152,248,220
Maaa 156,252,220
D 8,40,64,2048
Ta 232,22,32768
Maaa 124,252,216
Ta 234,22,33792
Maaa 92,252,212
Maaa 60,252,208
Maaa 28,252,204
buffer_store_dwordx4 v[36:39], v80, s[8:11], s83 offen sc1
s_and_b32 s80, s80, 8192
Mvaa 252,252,200
s_add_u32 s83, s82, s80
s_add_u32 s83, s83, 65536
Mvaa 220,252,196
Mvaa 188,252,192
W 8
Mvaa 160,224,160
D 9,40,64,3072
s_mov_b32 s65, 2097152
Mvaa 192,224,164
s_add_u32 m0, s62, 0
Ta 236,23,32768
Mvaa 224,224,168
Ta 238,23,33792
Maaa 0,224,172
W 4
Maaa 32,224,176
buffer_store_dwordx4 v[40:43], v80, s[8:11], s83 offen sc1
Maaa 64,224,180
Maaa 96,224,184
Maaa 128,224,188
Maaa 132,228,188
D 10,44,65,0
Ta 240,24,32768
Maaa 100,228,184
Ta 242,24,33792
Maaa 68,228,180
Maaa 36,228,176
Maaa 4,228,172
D 11,44,65,1024
Mvaa 228,228,168
Mvaa 196,228,164
Mvaa 164,228,160
W 4
Mvaa 168,232,160
D 12,44,65,2048
Ta 244,25,32768
Mvaa 200,232,164
Ta 246,25,33792
Mvaa 232,232,168
Maaa 8,232,172
Maaa 40,232,176
D 13,44,65,3072
s_add_u32 m0, s62, 4096
Maaa 72,232,180
Maaa 104,232,184
Maaa 136,232,188
W 4
Maaa 140,236,188
D 14,44,65,0
Ta 248,26,32768
Maaa 108,236,184
Ta 250,26,33792
Maaa 76,236,180
Maaa 44,236,176
Maaa 12,236,172
D 15,44,65,1024
Ra 192,19,32768
Mvaa 236,236,168
Mvaa 204,236,164
Mvaa 172,236,160
Ra 196,19,34816
W 6
Mvaa 176,240,160
D 16,44,65,2048
Ta 252,27,32768
Mvaa 208,240,164
Ta 254,27,33792
Mvaa 240,240,168
Ra 200,19,36864
Maaa 16,240,172
Maaa 48,240,176
D 17,44,65,3072
Maaa 80,240,180
Ra 204,19,38912
Maaa 112,240,184
Maaa 144,240,188
W 8
Maaa 148,244,188
global_load_dword v94, v[92:93], off
v_lshl_add_u64 v[92:93], v[92:93], 0, s[68:69]
Maaa 116,244,184
Ta 224,20,40960
Ta 226,20,41984
Maaa 84,244,180
Ra 208,19,40960
Maaa 52,244,176
Ra 212,19,43008
Maaa 20,244,172
Mvaa 244,244,168
Mvaa 212,244,164
Ra 216,19,45056
Mvaa 180,244,160
W 11
Mvaa 184,248,160
Ta 228,21,40960
Ta 230,21,41984
Mvaa 216,248,164
Ra 220,19,47104
Mvaa 248,248,168
Maaa 24,248,172
Maaa 56,248,176
Maaa 88,248,180
Maaa 120,248,184
Maaa 152,248,188
W 10
Maaa 156,252,188
Ta 232,22,40960
Ta 234,22,41984
Maaa 124,252,184
Maaa 92,252,180
Maaa 60,252,176
Maaa 28,252,172
Mvaa 252,252,168
Mvaa 220,252,164
Mvaa 188,252,160
W 8
Mvaa 160,224,192
Ta 236,23,40960
Ta 238,23,41984
Mvaa 192,224,196
Mvaa 224,224,200
Maaa 0,224,204
W 4
Maaa 32,224,208
Maaa 64,224,212
Maaa 96,224,216
Maaa 128,224,220
Maaa 132,228,220
Ta 240,24,40960
Ta 242,24,41984
Maaa 100,228,216
Maaa 68,228,212
Maaa 36,228,208
Maaa 4,228,204
Ta 244,25,40960
Ta 246,25,41984
Mvaa 228,228,200
Mvaa 196,228,196
Ta 248,26,40960
Ta 250,26,41984
Mvaa 164,228,192
W 8
Mvaa 168,232,192
Ta 252,27,40960
Ta 254,27,41984
Mvaa 200,232,196
Mvaa 232,232,200
Maaa 8,232,204
Maaa 40,232,208
Maaa 72,232,212
Maaa 104,232,216
Maaa 136,232,220
W 8
Maaa 140,236,220
Maaa 108,236,216
Maaa 76,236,212
s_waitcnt vmcnt(1) lgkmcnt(0)
s_barrier
s_cmp_eq_u32 s14, 0
s_cbranch_scc1 .Lpub_10%=
.Lpubr_11%=:
Maaa 44,236,208
s_mov_b32 s64, 640
Maaa 12,236,204
Ra 160,18,0
Mvaa 236,236,200
s_add_u32 m0, s61, 32768
Mvaa 204,236,196
Mvaa 172,236,192
Ra 164,18,2048
Mvaa 176,240,192
D 2,40,64,0
Mvaa 208,240,196
Mvaa 240,240,200
Ra 168,18,4096
Maaa 16,240,204
Maaa 48,240,208
D 3,40,64,1024
Maaa 80,240,212
Ra 172,18,6144
Maaa 112,240,216
Maaa 144,240,220
Maaa 148,244,220
D 4,40,64,2048
Ta 224,20,0
Maaa 116,244,216
Ta 226,20,1024
Ra 176,18,8192
Maaa 84,244,212
Maaa 52,244,208
Ra 180,18,10240
Maaa 20,244,204
D 5,40,64,3072
s_add_u32 m0, s61, 36864
Mvaa 244,244,200
Mvaa 212,244,196
Ra 184,18,12288
Mvaa 180,244,192
Mvaa 184,248,192
D 6,40,64,0
Ta 228,21,0
Mvaa 216,248,196
Ta 230,21,1024
Ra 188,18,14336
Mvaa 248,248,200
Maaa 24,248,204
Maaa 56,248,208
D 7,40,64,1024
Maaa 88,248,212
Maaa 120,248,216
Maaa 152,248,220
Maaa 156,252,220
D 8,40,64,2048
Ta 232,22,0
Maaa 124,252,216
)"
R"(Ta 234,22,1024
Maaa 92,252,212
Maaa 60,252,208
Maaa 28,252,204
D 9,40,64,3072
s_mov_b32 s65, 2621440
Mvaa 252,252,200
s_add_u32 m0, s62, 32768
Mvaa 220,252,196
Mvaa 188,252,192
W 8
Mvaa 160,224,160
D 10,44,65,0
Ta 236,23,0
Mvaa 192,224,164
Ta 238,23,1024
Mvaa 224,224,168
Maaa 0,224,172
W 4
Maaa 32,224,176
D 11,44,65,1024
Maaa 64,224,180
Maaa 96,224,184
Maaa 128,224,188
Maaa 132,228,188
D 12,44,65,2048
Ta 240,24,0
Maaa 100,228,184
Ta 242,24,1024
Maaa 68,228,180
Maaa 36,228,176
Maaa 4,228,172
D 13,44,65,3072
s_add_u32 m0, s62, 36864
Mvaa 228,228,168
Mvaa 196,228,164
Mvaa 164,228,160
W 4
Mvaa 168,232,160
D 14,44,65,0
Ta 244,25,0
Mvaa 200,232,164
Ta 246,25,1024
Mvaa 232,232,168
Maaa 8,232,172
Maaa 40,232,176
D 15,44,65,1024
Maaa 72,232,180
Maaa 104,232,184
Maaa 136,232,188
W 4
Maaa 140,236,188
D 16,44,65,2048
Ta 248,26,0
Maaa 108,236,184
Ta 250,26,1024
Maaa 76,236,180
Maaa 44,236,176
Maaa 12,236,172
D 17,44,65,3072
s_mov_b32 s65, 0
Mvaa 236,236,168
s_add_u32 m0, s63, 131072
Ra 192,19,0
Mvaa 204,236,164
Mvaa 172,236,160
Ra 196,19,2048
W 6
Mvaa 176,240,160
D 10,48,65,0
Ta 252,27,0
Mvaa 208,240,164
Ta 254,27,1024
Mvaa 240,240,168
Ra 200,19,4096
Maaa 16,240,172
Maaa 48,240,176
D 11,48,65,1024
Maaa 80,240,180
Ra 204,19,6144
Maaa 112,240,184
Maaa 144,240,188
W 8
Maaa 148,244,188
D 12,48,65,2048
Ta 224,20,8192
Maaa 116,244,184
Ta 226,20,9216
Ra 208,19,8192
Maaa 84,244,180
Maaa 52,244,176
Ra 212,19,10240
Maaa 20,244,172
D 13,48,65,3072
s_add_u32 m0, s63, 135168
Mvaa 244,244,168
Mvaa 212,244,164
Ra 216,19,12288
Mvaa 180,244,160
W 11
Mvaa 184,248,160
D 14,48,65,0
Ta 228,21,8192
Mvaa 216,248,164
Ta 230,21,9216
Ra 220,19,14336
Mvaa 248,248,168
Maaa 24,248,172
Maaa 56,248,176
D 15,48,65,1024
Maaa 88,248,180
Maaa 120,248,184
Maaa 152,248,188
W 10
Maaa 156,252,188
D 16,48,65,2048
Ta 232,22,8192
Maaa 124,252,184
Ta 234,22,9216
Maaa 92,252,180
Maaa 60,252,176
Maaa 28,252,172
D 17,48,65,3072
v_lshrrev_b32 v138, 4, v1
Mvaa 252,252,168
s_sub_u32 s81, s73, 1
v_min_u32 v138, s81, v138
Mvaa 220,252,164
v_lshrrev_b32 v139, 2, v138
v_mul_u32_u24 v139, s72, v139
Mvaa 188,252,160
v_and_b32 v138, 3, v138
W 8
Mvaa 160,224,192
v_lshl_add_u32 v138, v138, 7, v139
v_add_u32 v138, s74, v138
Mvaa 192,224,196
s_lshr_b32 s73, s73, 2
s_mul_i32 s73, s73, s72
Mvaa 224,224,200
v_min_u32 v139, s75, v138
global_load_dword v94, v139, s[76:77]
Maaa 0,224,204
v_add_u32 v138, s73, v138
W 2
Maaa 32,224,208
Ta 236,23,8192
Ta 238,23,9216
Maaa 64,224,212
Maaa 96,224,216
Maaa 128,224,220
Maaa 132,228,220
Ta 240,24,8192
Ta 242,24,9216
Maaa 100,228,216
Maaa 68,228,212
Maaa 36,228,208
Maaa 4,228,204
Ta 244,25,8192
Ta 246,25,9216
Mvaa 228,228,200
Mvaa 196,228,196
Ta 248,26,8192
Ta 250,26,9216
Mvaa 164,228,192
W 8
Mvaa 168,232,192
Ta 252,27,8192
Ta 254,27,9216
Mvaa 200,232,196
Mvaa 232,232,200
Maaa 8,232,204
Maaa 40,232,208
Maaa 72,232,212
Maaa 104,232,216
Maaa 136,232,220
W 8
Maaa 140,236,220
Maaa 108,236,216
Maaa 76,236,212
s_waitcnt vmcnt(1) lgkmcnt(0)
s_barrier
Maaa 44,236,208
s_mov_b32 s64, 768
Maaa 12,236,204
Ra 160,18,32768
Mvaa 236,236,200
s_add_u32 m0, s61, 0
Mvaa 204,236,196
Mvaa 172,236,192
Ra 164,18,34816
Mvaa 176,240,192
D 2,40,64,0
Mvaa 208,240,196
Mvaa 240,240,200
Ra 168,18,36864
Maaa 16,240,204
Maaa 48,240,208
D 3,40,64,1024
Maaa 80,240,212
Ra 172,18,38912
Maaa 112,240,216
Maaa 144,240,220
Maaa 148,244,220
D 4,40,64,2048
Ta 224,20,32768
Maaa 116,244,216
Ta 226,20,33792
Ra 176,18,40960
Maaa 84,244,212
Maaa 52,244,208
Ra 180,18,43008
Maaa 20,244,204
D 5,40,64,3072
s_add_u32 m0, s61, 4096
Mvaa 244,244,200
Mvaa 212,244,196
Ra 184,18,45056
Mvaa 180,244,192
Mvaa 184,248,192
D 6,40,64,0
Ta 228,21,32768
Mvaa 216,248,196
Ta 230,21,33792
Ra 188,18,47104
Mvaa 248,248,200
Maaa 24,248,204
Maaa 56,248,208
D 7,40,64,1024
Maaa 88,248,212
Maaa 120,248,216
Maaa 152,248,220
Maaa 156,252,220
D 8,40,64,2048
Ta 232,22,32768
Maaa 124,252,216
Ta 234,22,33792
Maaa 92,252,212
Maaa 60,252,208
Maaa 28,252,204
D 9,40,64,3072
s_mov_b32 s65, 3145728
Mvaa 252,252,200
s_add_u32 m0, s62, 0
Mvaa 220,252,196
Mvaa 188,252,192
W 8
Mvaa 160,224,160
D 10,44,65,0
Ta 236,23,32768
Mvaa 192,224,164
Ta 238,23,33792
Mvaa 224,224,168
Maaa 0,224,172
W 4
Maaa 32,224,176
D 11,44,65,1024
Maaa 64,224,180
Maaa 96,224,184
Maaa 128,224,188
Maaa 132,228,188
D 12,44,65,2048
Ta 240,24,32768
Maaa 100,228,184
Ta 242,24,33792
Maaa 68,228,180
Maaa 36,228,176
Maaa 4,228,172
D 13,44,65,3072
s_add_u32 m0, s62, 4096
Mvaa 228,228,168
Mvaa 196,228,164
Mvaa 164,228,160
W 4
Mvaa 168,232,160
D 14,44,65,0
Ta 244,25,32768
Mvaa 200,232,164
Ta 246,25,33792
Mvaa 232,232,168
Maaa 8,232,172
Maaa 40,232,176
D 15,44,65,1024
Maaa 72,232,180
Maaa 104,232,184
Maaa 136,232,188
W 4
Maaa 140,236,188
D 16,44,65,2048
Ta 248,26,32768
Maaa 108,236,184
Ta 250,26,33792
Maaa 76,236,180
Maaa 44,236,176
Maaa 12,236,172
D 17,44,65,3072
Ra 192,19,32768
Mvaa 236,236,168
Mvaa 204,236,164
Mvaa 172,236,160
Ra 196,19,34816
W 6
Mvaa 176,240,160
Ta 252,27,32768
Ta 254,27,33792
Mvaa 208,240,164
v_min_u32 v139, s75, v138
Mvaa 240,240,168
Ra 200,19,36864
Maaa 16,240,172
Maaa 48,240,176
Maaa 80,240,180
global_load_dword v94, v139, s[76:77]
Ra 204,19,38912
Maaa 112,240,184
Maaa 144,240,188
W 8
Maaa 148,244,188
Ta 224,20,40960
Ta 226,20,41984
Maaa 116,244,184
Ra 208,19,40960
Maaa 84,244,180
v_add_u32 v138, s73, v138
Maaa 52,244,176
Ra 212,19,43008
Maaa 20,244,172
Mvaa 244,244,168
Mvaa 212,244,164
Ra 216,19,45056
Mvaa 180,244,160
W 11
Mvaa 184,248,160
Ta 228,21,40960
Ta 230,21,41984
Mvaa 216,248,164
Ra 220,19,47104
Mvaa 248,248,168
Maaa 24,248,172
Maaa 56,248,176
Maaa 88,248,180
Maaa 120,248,184
Maaa 152,248,188
W 10
Maaa 156,252,188
Ta 232,22,40960
Ta 234,22,41984
Maaa 124,252,184
Maaa 92,252,180
Maaa 60,252,176
Maaa 28,252,172
Mvaa 252,252,168
Mvaa 220,252,164
Mvaa 188,252,160
W 8
Mvaa 160,224,192
Ta 236,23,40960
Ta 238,23,41984
Mvaa 192,224,196
Mvaa 224,224,200
Maaa 0,224,204
W 4
Maaa 32,224,208
Maaa 64,224,212
Maaa 96,224,216
Maaa 128,224,220
Maaa 132,228,220
Ta 240,24,40960
Ta 242,24,41984
Maaa 100,228,216
Maaa 68,228,212
Maaa 36,228,208
Maaa 4,228,204
Ta 244,25,40960
Ta 246,25,41984
Mvaa 228,228,200
Mvaa 196,228,196
Ta 248,26,40960
Ta 250,26,41984
Mvaa 164,228,192
W 8
Mvaa 168,232,192
Ta 252,27,40960
Ta 254,27,41984
Mvaa 200,232,196
Mvaa 232,232,200
Maaa 8,232,204
Maaa 40,232,208
Maaa 72,232,212
Maaa 104,232,216
Maaa 136,232,220
W 8
Maaa 140,236,220
Maaa 108,236,216
Maaa 76,236,212
s_waitcnt vmcnt(1) lgkmcnt(0)
s_barrier
Maaa 44,236,208
s_mov_b32 s64, 896
Maaa 12,236,204
)"
R"(Ra 160,18,0
Mvaa 236,236,200
s_add_u32 m0, s61, 32768
Mvaa 204,236,196
Mvaa 172,236,192
Ra 164,18,2048
Mvaa 176,240,192
D 2,40,64,0
Mvaa 208,240,196
Mvaa 240,240,200
Ra 168,18,4096
Maaa 16,240,204
Maaa 48,240,208
D 3,40,64,1024
Maaa 80,240,212
Ra 172,18,6144
Maaa 112,240,216
Maaa 144,240,220
Maaa 148,244,220
D 4,40,64,2048
Ta 224,20,0
Maaa 116,244,216
Ta 226,20,1024
Ra 176,18,8192
Maaa 84,244,212
Maaa 52,244,208
Ra 180,18,10240
Maaa 20,244,204
D 5,40,64,3072
s_add_u32 m0, s61, 36864
Mvaa 244,244,200
Mvaa 212,244,196
Ra 184,18,12288
Mvaa 180,244,192
Mvaa 184,248,192
D 6,40,64,0
Ta 228,21,0
Mvaa 216,248,196
Ta 230,21,1024
Ra 188,18,14336
Mvaa 248,248,200
Maaa 24,248,204
Maaa 56,248,208
D 7,40,64,1024
Maaa 88,248,212
Maaa 120,248,216
Maaa 152,248,220
Maaa 156,252,220
D 8,40,64,2048
Ta 232,22,0
Maaa 124,252,216
Ta 234,22,1024
Maaa 92,252,212
Maaa 60,252,208
Maaa 28,252,204
D 9,40,64,3072
s_mov_b32 s65, 3670016
Mvaa 252,252,200
s_add_u32 m0, s62, 32768
Mvaa 220,252,196
Mvaa 188,252,192
W 8
Mvaa 160,224,160
D 10,44,65,0
Ta 236,23,0
Mvaa 192,224,164
Ta 238,23,1024
Mvaa 224,224,168
Maaa 0,224,172
W 4
Maaa 32,224,176
D 11,44,65,1024
Maaa 64,224,180
Maaa 96,224,184
Maaa 128,224,188
Maaa 132,228,188
D 12,44,65,2048
Ta 240,24,0
Maaa 100,228,184
Ta 242,24,1024
Maaa 68,228,180
Maaa 36,228,176
Maaa 4,228,172
D 13,44,65,3072
s_add_u32 m0, s62, 36864
Mvaa 228,228,168
Mvaa 196,228,164
Mvaa 164,228,160
W 4
Mvaa 168,232,160
D 14,44,65,0
Ta 244,25,0
Mvaa 200,232,164
Ta 246,25,1024
Mvaa 232,232,168
Maaa 8,232,172
Maaa 40,232,176
D 15,44,65,1024
Maaa 72,232,180
Maaa 104,232,184
Maaa 136,232,188
W 4
Maaa 140,236,188
D 16,44,65,2048
Ta 248,26,0
Maaa 108,236,184
Ta 250,26,1024
Maaa 76,236,180
Maaa 44,236,176
Maaa 12,236,172
D 17,44,65,3072
Ra 192,19,0
Mvaa 236,236,168
Mvaa 204,236,164
v_add_u32 v2, 131072, v20
Mvaa 172,236,160
Ra 196,19,2048
W 6
Mvaa 176,240,160
Ta 252,27,0
Ta 254,27,1024
Mvaa 208,240,164
v_add_u32 v3, 131072, v21
Mvaa 240,240,168
Ra 200,19,4096
v_add_u32 v4, 131072, v22
Maaa 16,240,172
Maaa 48,240,176
v_add_u32 v5, 131072, v23
Maaa 80,240,180
Ra 204,19,6144
Maaa 112,240,184
v_add_u32 v6, 131072, v24
Maaa 144,240,188
W 8
Maaa 148,244,188
Ta 224,20,8192
Ta 226,20,9216
Maaa 116,244,184
Ra 208,19,8192
v_add_u32 v7, 131072, v25
Maaa 84,244,180
v_add_u32 v8, 131072, v26
Maaa 52,244,176
Ra 212,19,10240
Maaa 20,244,172
v_add_u32 v9, 131072, v27
Mvaa 244,244,168
Mvaa 212,244,164
Ra 216,19,12288
Mvaa 180,244,160
W 11
Mvaa 184,248,160
Ta 228,21,8192
Ta 230,21,9216
Mvaa 216,248,164
Ra 220,19,14336
Mvaa 248,248,168
Maaa 24,248,172
Maaa 56,248,176
Maaa 88,248,180
Maaa 120,248,184
Maaa 152,248,188
W 10
Maaa 156,252,188
Ta 232,22,8192
Ta 234,22,9216
Maaa 124,252,184
Maaa 92,252,180
Maaa 60,252,176
Maaa 28,252,172
Mvaa 252,252,168
Mvaa 220,252,164
Mvaa 188,252,160
W 8
Mvaa 160,224,192
Ta 236,23,8192
Ta 238,23,9216
Mvaa 192,224,196
Mvaa 224,224,200
Maaa 0,224,204
W 4
Maaa 32,224,208
Maaa 64,224,212
Maaa 96,224,216
Maaa 128,224,220
Maaa 132,228,220
Ta 240,24,8192
Ta 242,24,9216
Maaa 100,228,216
Maaa 68,228,212
Maaa 36,228,208
Maaa 4,228,204
Ta 244,25,8192
Ta 246,25,9216
Mvaa 228,228,200
Mvaa 196,228,196
Ta 248,26,8192
Ta 250,26,9216
Mvaa 164,228,192
W 8
Mvaa 168,232,192
Ta 252,27,8192
Ta 254,27,9216
Mvaa 200,232,196
Mvaa 232,232,200
Maaa 8,232,204
Maaa 40,232,208
Maaa 72,232,212
Maaa 104,232,216
Maaa 136,232,220
W 8
Maaa 140,236,220
Maaa 108,236,216
Maaa 76,236,212
s_waitcnt vmcnt(0) lgkmcnt(0)
s_barrier
Maaa 44,236,208
s_mov_b32 s65, 1048576
Maaa 12,236,204
Ra 160,18,32768
Mvaa 236,236,200
s_add_u32 m0, s63, 0
Mvaa 204,236,196
Mvaa 172,236,192
Ra 164,18,34816
Mvaa 176,240,192
D 10,48,65,0
Mvaa 208,240,196
Mvaa 240,240,200
Ra 168,18,36864
Maaa 16,240,204
Maaa 48,240,208
D 11,48,65,1024
Maaa 80,240,212
Ra 172,18,38912
Maaa 112,240,216
Maaa 144,240,220
Maaa 148,244,220
D 12,48,65,2048
Ta 224,20,32768
Maaa 116,244,216
Ta 226,20,33792
Ra 176,18,40960
Maaa 84,244,212
Maaa 52,244,208
Ra 180,18,43008
Maaa 20,244,204
D 13,48,65,3072
s_add_u32 m0, s63, 4096
Mvaa 244,244,200
Mvaa 212,244,196
Ra 184,18,45056
Mvaa 180,244,192
Mvaa 184,248,192
D 14,48,65,0
Ta 228,21,32768
Mvaa 216,248,196
Ta 230,21,33792
Ra 188,18,47104
Mvaa 248,248,200
Maaa 24,248,204
Maaa 56,248,208
D 15,48,65,1024
Maaa 88,248,212
Maaa 120,248,216
v_bfe_u32 v28, v1, 4, 3
Maaa 152,248,220
v_lshlrev_b32 v28, 3, v28
Maaa 156,252,220
D 16,48,65,2048
s_lshl_b32 s80, s60, 6
Maaa 124,252,216
Ta 232,22,32768
Ta 234,22,33792
Maaa 92,252,212
s_add_u32 s80, s80, 256
Maaa 60,252,208
Maaa 28,252,204
D 17,48,65,3072
s_mov_b32 s65, 2097152
Mvaa 252,252,200
s_add_u32 m0, s63, 65536
Mvaa 220,252,196
Mvaa 188,252,192
W 8
Mvaa 160,224,160
buffer_load_dwordx2 v[28:29], v28, s[8:11], s80 offen sc1
Ta 236,23,32768
Mvaa 192,224,164
Ta 238,23,33792
Mvaa 224,224,168
Maaa 0,224,172
W 4
Maaa 32,224,176
D 10,48,65,0
Maaa 64,224,180
Maaa 96,224,184
Maaa 128,224,188
Maaa 132,228,188
D 11,48,65,1024
Ta 240,24,32768
Maaa 100,228,184
Ta 242,24,33792
Maaa 68,228,180
Maaa 36,228,176
Maaa 4,228,172
D 12,48,65,2048
Mvaa 228,228,168
Mvaa 196,228,164
Mvaa 164,228,160
W 4
Mvaa 168,232,160
D 13,48,65,3072
s_add_u32 m0, s63, 69632
Mvaa 200,232,164
Ta 244,25,32768
Ta 246,25,33792
Mvaa 232,232,168
Maaa 8,232,172
Maaa 40,232,176
D 14,48,65,0
Maaa 72,232,180
Maaa 104,232,184
Maaa 136,232,188
W 4
Maaa 140,236,188
D 15,48,65,1024
Ta 248,26,32768
Maaa 108,236,184
Ta 250,26,33792
Maaa 76,236,180
Maaa 44,236,176
Maaa 12,236,172
D 16,48,65,2048
Ra 192,19,32768
Mvaa 236,236,168
Mvaa 204,236,164
Mvaa 172,236,160
Ra 196,19,34816
W 6
Mvaa 176,240,160
D 17,48,65,3072
Ta 252,27,32768
Mvaa 208,240,164
Ta 254,27,33792
v_min_u32 v139, s75, v138
Mvaa 240,240,168
Ra 200,19,36864
Maaa 16,240,172
Maaa 48,240,176
Maaa 80,240,180
global_load_dword v94, v139, s[76:77]
Ra 204,19,38912
Maaa 112,240,184
Maaa 144,240,188
W 8
Maaa 148,244,188
Ta 224,20,40960
Ta 226,20,41984
Maaa 116,244,184
Ra 208,19,40960
Maaa 84,244,180
v_add_u32 v138, s73, v138
Maaa 52,244,176
Ra 212,19,43008
Maaa 20,244,172
s_waitcnt vmcnt(9)
v_cmp_eq_u64 vcc, s[12:13], v[28:29]
s_cmp_eq_u64 vcc, -1
s_cbranch_scc0 .Lspin_12%=
.Lok_13%=:
Mvaa 244,244,168
Mvaa 212,244,164
Ra 216,19,45056
Mvaa 180,244,160
W 11
Mvaa 184,248,160
Ta 228,21,40960
Ta 230,21,41984
Mvaa 216,248,164
Ra 220,19,47104
Mvaa 248,248,168
Maaa 24,248,172
Maaa 56,248,176
Maaa 88,248,180
Maaa 120,248,184
Maaa 152,248,188
W 10
Maaa 156,252,188
Ta 232,22,40960
Ta 234,22,41984
Maaa 124,252,184
Maaa 92,252,180
Maaa 60,252,176
Maaa 28,252,172
Mvaa 252,252,168
Mvaa 220,252,164
Mvaa 188,252,160
s_and_b32 s80, s14, 2
)"
R"(W 8
Mvaa 160,224,192
Ta 236,23,40960
Ta 238,23,41984
Mvaa 192,224,196
Mvaa 224,224,200
s_lshl_b32 s80, s80, 10
Maaa 0,224,204
W 4
Maaa 32,224,208
v_add_u32 v18, s80, v1
Maaa 64,224,212
Maaa 96,224,216
Maaa 128,224,220
v_xor_b32 v19, 2048, v18
Maaa 132,228,220
Ta 240,24,40960
Ta 242,24,41984
Maaa 100,228,216
buffer_load_dwordx4 a[160:163], v18, s[8:11], s66 offen offset:0 sc1
Maaa 68,228,212
Maaa 36,228,208
Maaa 4,228,204
Ta 244,25,40960
Ta 246,25,41984
Mvaa 228,228,200
buffer_load_dwordx4 a[164:167], v18, s[8:11], s66 offen offset:1024 sc1
Mvaa 196,228,196
Ta 248,26,40960
Ta 250,26,41984
Mvaa 164,228,192
W 8
Mvaa 168,232,192
Ta 252,27,40960
Ta 254,27,41984
Mvaa 200,232,196
buffer_load_dwordx4 a[168:171], v19, s[8:11], s66 offen offset:0 sc1
Mvaa 232,232,200
Maaa 8,232,204
Maaa 40,232,208
Maaa 72,232,212
buffer_load_dwordx4 a[172:175], v19, s[8:11], s66 offen offset:1024 sc1
s_add_u32 s66, s66, 4096
Maaa 104,232,216
Maaa 136,232,220
W 8
Maaa 140,236,220
Maaa 108,236,216
buffer_load_dwordx4 a[176:179], v18, s[8:11], s66 offen offset:0 sc1
Maaa 76,236,212
s_waitcnt vmcnt(6) lgkmcnt(0)
s_barrier
Maaa 44,236,208
Maaa 12,236,204
Mvaa 236,236,200
buffer_load_dwordx4 a[180:183], v18, s[8:11], s66 offen offset:1024 sc1
Mvaa 204,236,196
Mvaa 172,236,192
Mvaa 176,240,192
Mvaa 208,240,196
buffer_load_dwordx4 a[184:187], v19, s[8:11], s66 offen offset:0 sc1
Mvaa 240,240,200
s_mov_b32 s65, 3145728
Maaa 16,240,204
Maaa 48,240,208
Maaa 80,240,212
buffer_load_dwordx4 a[188:191], v19, s[8:11], s66 offen offset:1024 sc1
Maaa 112,240,216
Maaa 144,240,220
Maaa 148,244,220
Maaa 116,244,216
Maaa 84,244,212
Ta 224,2,0
Ta 226,2,1024
Maaa 52,244,208
Maaa 20,244,204
s_add_u32 m0, s63, 32768
Mvaa 244,244,200
Mvaa 212,244,196
Ta 228,3,0
Ta 230,3,1024
Mvaa 180,244,192
Mvaa 184,248,192
Mvaa 216,248,196
Mvaa 248,248,200
Ta 232,4,0
Ta 234,4,1024
Maaa 24,248,204
Maaa 56,248,208
Maaa 88,248,212
D 10,48,65,0
Maaa 120,248,216
Ta 236,5,0
Ta 238,5,1024
Maaa 152,248,220
Maaa 156,252,220
Maaa 124,252,216
Maaa 92,252,212
Ta 240,6,0
Ta 242,6,1024
Maaa 60,252,208
Maaa 28,252,204
Mvaa 252,252,200
Mvaa 220,252,196
D 11,48,65,1024
Mvaa 188,252,192
s_branch .Lq_9%=
.Lv_8%=:
s_waitcnt vmcnt(24)
s_barrier
Ra 160,18,0
Ta 224,20,16384
Ta 226,20,17408
Ra 164,18,2048
Ra 168,18,4096
Ra 172,18,6144
Ra 176,18,8192
Ra 180,18,10240
Ra 184,18,12288
Ra 188,18,14336
Ta 228,21,16384
Ta 230,21,17408
W 6
Zvaa 160,160,224
Ta 232,22,16384
Ta 234,22,17408
Zvaa 164,164,224
Ta 236,23,16384
Ta 238,23,17408
Zvaa 224,168,224
s_waitcnt vmcnt(21)
Zvaa 228,172,224
v_lshlrev_b32 v56, 16, v32
W 6
Zaaa 32,176,224
v_and_b32 v57, 0xffff0000, v32
Zaaa 36,180,224
v_lshlrev_b32 v58, 16, v33
Zaaa 96,184,224
v_and_b32 v59, 0xffff0000, v33
Zaaa 100,188,224
v_lshlrev_b32 v60, 16, v34
W 4
Zaaa 108,188,228
v_and_b32 v61, 0xffff0000, v34
Ta 240,24,16384
Zaaa 104,184,228
Ta 242,24,17408
v_lshlrev_b32 v62, 16, v35
Zaaa 44,180,228
v_and_b32 v63, 0xffff0000, v35
v_lshlrev_b32 v64, 16, v36
Zaaa 40,176,228
v_and_b32 v65, 0xffff0000, v36
Zvaa 236,172,228
v_lshlrev_b32 v66, 16, v37
Zvaa 232,168,228
v_and_b32 v67, 0xffff0000, v37
Zvaa 172,164,228
v_lshlrev_b32 v68, 16, v38
Zvaa 168,160,228
v_and_b32 v69, 0xffff0000, v38
W 4
Zvaa 176,160,232
v_lshlrev_b32 v70, 16, v39
v_and_b32 v71, 0xffff0000, v39
Zvaa 180,164,232
Ta 244,25,16384
Ta 246,25,17408
Zvaa 240,168,232
v_lshlrev_b32 v72, 16, v40
v_and_b32 v73, 0xffff0000, v40
Zvaa 244,172,232
v_lshlrev_b32 v74, 16, v41
Zaaa 48,176,232
v_and_b32 v75, 0xffff0000, v41
Zaaa 52,180,232
v_lshlrev_b32 v76, 16, v42
v_and_b32 v77, 0xffff0000, v42
Zaaa 112,184,232
v_lshlrev_b32 v78, 16, v43
Zaaa 116,188,232
v_and_b32 v79, 0xffff0000, v43
W 4
Zaaa 124,188,236
A 80,56,57
Ta 248,26,16384
Zaaa 120,184,236
Ta 250,26,17408
A 81,58,59
Zaaa 60,180,236
A 80,80,60
Zaaa 56,176,236
A 81,81,61
A 80,80,62
Zvaa 252,172,236
A 81,81,63
Ra 192,19,0
Zvaa 248,168,236
A 80,80,64
Zvaa 188,164,236
A 81,81,65
Zvaa 184,160,236
A 80,80,66
W 5
Zvaa 192,160,240
Ra 196,19,2048
A 81,81,67
Zvaa 196,164,240
Ta 252,27,16384
Ta 254,27,17408
Zaaa 0,168,240
A 80,80,68
A 81,81,69
Zaaa 4,172,240
A 80,80,70
Ra 200,19,4096
Zaaa 64,176,240
A 81,81,71
A 80,80,72
Zaaa 68,180,240
A 81,81,73
Ra 204,19,6144
Zaaa 128,184,240
A 80,80,74
Zaaa 132,188,240
A 81,81,75
W 8
Zaaa 140,188,244
A 80,80,76
A 81,81,77
Zaaa 136,184,244
Ta 224,20,24576
Ta 226,20,25600
Zaaa 76,180,244
Ra 208,19,8192
A 80,80,78
Zaaa 72,176,244
A 81,81,79
A 80,80,81
Zaaa 12,172,244
Ra 212,19,10240
v_add_f32_dpp v80, v80, v80 quad_perm:[1,0,3,2] row_mask:0xf bank_mask:0xf
Zaaa 8,168,244
Zvaa 204,164,244
v_add_f32_dpp v80, v80, v80 quad_perm:[2,3,0,1] row_mask:0xf bank_mask:0xf
Ra 216,19,12288
Zvaa 200,160,244
v_add_f32_dpp v80, v80, v80 row_half_mirror row_mask:0xf bank_mask:0xf
W 11
Zvaa 208,160,248
v_add_f32_dpp v80, v80, v80 row_mirror row_mask:0xf bank_mask:0xf
v_mov_b32 v81, v80
Zvaa 212,164,248
Ta 228,21,24576
v_permlane16_swap_b32 v80, v81
Zaaa 16,168,248
Ta 230,21,25600
A 80,80,81
Zaaa 20,172,248
Ra 220,19,14336
v_mul_f32 v84, 0x3acccccd, v80
Zaaa 80,176,248
s_mov_b32 s86, 0xffff
s_mov_b32 s87, 0xffff
Zaaa 84,180,248
v_sub_f32 v82, v56, v84
U 80,82,82
Zaaa 144,184,248
v_sub_f32 v83, v57, v84
Zaaa 148,188,248
v_fmac_f32 v80, v83, v83
W 10
Zaaa 156,188,252
v_sub_f32 v82, v58, v84
Ta 232,22,24576
Zaaa 152,184,252
Ta 234,22,25600
v_fmac_f32 v80, v82, v82
Zaaa 92,180,252
v_sub_f32 v83, v59, v84
v_fmac_f32 v80, v83, v83
Zaaa 88,176,252
v_sub_f32 v82, v60, v84
Zaaa 28,172,252
v_fmac_f32 v80, v82, v82
Zaaa 24,168,252
v_sub_f32 v83, v61, v84
Zvaa 220,164,252
v_fmac_f32 v80, v83, v83
Zvaa 216,160,252
v_sub_f32 v82, v62, v84
W 8
Mvaa 160,192,224
v_fmac_f32 v80, v82, v82
v_sub_f32 v83, v63, v84
Mvaa 164,196,224
Ta 236,23,24576
Ta 238,23,25600
Mvaa 224,200,224
v_fmac_f32 v80, v83, v83
v_sub_f32 v82, v64, v84
Mvaa 228,204,224
v_fmac_f32 v80, v82, v82
W 4
Maaa 32,208,224
v_sub_f32 v83, v65, v84
Maaa 36,212,224
v_fmac_f32 v80, v83, v83
Maaa 96,216,224
v_sub_f32 v82, v66, v84
Maaa 100,220,224
v_fmac_f32 v80, v82, v82
v_sub_f32 v83, v67, v84
Maaa 108,220,228
v_fmac_f32 v80, v83, v83
Ta 240,24,24576
Maaa 104,216,228
Ta 242,24,25600
v_sub_f32 v82, v68, v84
Maaa 44,212,228
v_fmac_f32 v80, v82, v82
Maaa 40,208,228
v_sub_f32 v83, v69, v84
Mvaa 236,204,228
v_fmac_f32 v80, v83, v83
Ta 244,25,24576
Mvaa 232,200,228
Ta 246,25,25600
v_sub_f32 v82, v70, v84
Mvaa 172,196,228
v_fmac_f32 v80, v82, v82
v_sub_f32 v83, v71, v84
Mvaa 168,192,228
Ta 248,26,24576
W 7
Mvaa 176,192,232
Ta 250,26,25600
v_fmac_f32 v80, v83, v83
Mvaa 180,196,232
v_sub_f32 v82, v72, v84
Ta 252,27,24576
Mvaa 240,200,232
Ta 254,27,25600
U 81,82,82
Mvaa 244,204,232
v_sub_f32 v83, v73, v84
v_fmac_f32 v81, v83, v83
Maaa 48,208,232
v_sub_f32 v82, v74, v84
v_fmac_f32 v81, v82, v82
Maaa 52,212,232
v_sub_f32 v83, v75, v84
Maaa 112,216,232
v_fmac_f32 v81, v83, v83
Maaa 116,220,232
v_sub_f32 v82, v76, v84
W 8
Maaa 124,220,236
v_fmac_f32 v81, v82, v82
Maaa 120,216,236
v_sub_f32 v83, v77, v84
v_fmac_f32 v81, v83, v83
Maaa 60,212,236
s_waitcnt vmcnt(1) lgkmcnt(0)
s_barrier
Maaa 56,208,236
v_sub_f32 v82, v78, v84
s_mov_b32 s64, 256
Mvaa 252,204,236
v_fmac_f32 v81, v82, v82
v_sub_f32 v83, v79, v84
Mvaa 248,200,236
Ra 160,18,32768
v_fmac_f32 v81, v83, v83
Mvaa 188,196,236
s_add_u32 m0, s61, 0
v_cndmask_b32_e64 v81, 0, v81, s[86:87]
Mvaa 184,192,236
A 80,80,81
Ra 164,18,34816
Mvaa 192,192,240
v_add_f32_dpp v80, v80, v80 quad_perm:[1,0,3,2] row_mask:0xf bank_mask:0xf
D 2,40,64,0
Mvaa 196,196,240
v_add_f32_dpp v80, v80, v80 quad_perm:[2,3,0,1] row_mask:0xf bank_mask:0xf
Maaa 0,200,240
Ra 168,18,36864
v_add_f32_dpp v80, v80, v80 row_half_mirror row_mask:0xf bank_mask:0xf
Maaa 4,204,240
Maaa 64,208,240
v_add_f32_dpp v80, v80, v80 row_mirror row_mask:0xf bank_mask:0xf
D 3,40,64,1024
Maaa 68,212,240
v_mov_b32 v81, v80
Ra 172,18,38912
Maaa 128,216,240
v_permlane16_swap_b32 v80, v81
A 80,80,81
Maaa 132,220,240
)"
R"(v_mul_f32 v80, 0x3acccccd, v80
K 80,80,80
Maaa 140,220,244
D 4,40,64,2048
v_lshlrev_b32 v80, 16, v80
Maaa 136,216,244
v_add_f32 v80, 0x3727c5ac, v80
Ta 224,20,49152
Maaa 76,212,244
Ta 226,20,50176
Ra 176,18,40960
Maaa 72,208,244
K 80,80,80
v_lshlrev_b32 v80, 16, v80
Maaa 12,204,244
D 5,40,64,3072
s_add_u32 m0, s61, 4096
Maaa 8,200,244
v_sqrt_f32 v81, v80
Mvaa 204,196,244
Ra 180,18,43008
Ra 184,18,45056
Mvaa 200,192,244
v_rcp_f32 v82, v81
Mvaa 208,192,248
D 6,40,64,0
v_fma_f32 v83, -v81, v81, v80
Mvaa 212,196,248
v_mul_f32 v82, 0.5, v82
v_fma_f32 v81, v83, v82, v81
Maaa 16,200,248
Ta 228,21,49152
Ta 230,21,50176
Maaa 20,204,248
K 86,81,81
Ra 188,18,47104
Maaa 80,208,248
D 7,40,64,1024
v_lshlrev_b32 v86, 16, v86
Maaa 84,212,248
v_rcp_f32 v87, v86
Maaa 144,216,248
K 85,84,84
v_lshlrev_b32 v85, 16, v85
Maaa 148,220,248
v_sub_f32 v56, v56, v85
Maaa 156,220,252
D 8,40,64,2048
v_sub_f32 v57, v57, v85
Maaa 152,216,252
K 80,56,57
Ta 232,22,49152
Maaa 92,212,252
Ta 234,22,50176
v_lshlrev_b32 v56, 16, v80
Maaa 88,208,252
v_and_b32 v57, 0xffff0000, v80
U 80,56,87
Maaa 28,204,252
D 9,40,64,3072
s_mov_b32 s65, 1048576
Maaa 24,200,252
s_add_u32 m0, s62, 0
U 81,57,87
Mvaa 220,196,252
v_fma_f32 v82, -v80, v86, v56
v_fma_f32 v83, -v81, v86, v57
Mvaa 216,192,252
v_fmac_f32 v80, v82, v87
W 8
Mvaa 160,160,224
D 10,44,65,0
v_fmac_f32 v81, v83, v87
Mvaa 164,164,224
K 32,80,81
Ta 236,23,49152
Mvaa 224,168,224
Ta 238,23,50176
v_lshlrev_b32 v80, 16, v32
Mvaa 228,172,224
v_and_b32 v81, 0xffff0000, v32
W 4
Maaa 32,176,224
D 11,44,65,1024
v_lshlrev_b32 v82, 16, v44
Maaa 36,180,224
v_and_b32 v83, 0xffff0000, v44
U 84,80,82
Maaa 96,184,224
v_fmac_f32 v84, v81, v83
v_sub_f32 v58, v58, v85
Maaa 100,188,224
v_sub_f32 v59, v59, v85
Maaa 108,188,228
D 12,44,65,2048
K 80,58,59
Maaa 104,184,228
Ta 240,24,49152
Ta 242,24,50176
Maaa 44,180,228
v_lshlrev_b32 v58, 16, v80
v_and_b32 v59, 0xffff0000, v80
Maaa 40,176,228
U 80,58,87
U 81,59,87
Mvaa 236,172,228
D 13,44,65,3072
s_add_u32 m0, s62, 4096
Mvaa 232,168,228
v_fma_f32 v82, -v80, v86, v58
v_fma_f32 v83, -v81, v86, v59
Mvaa 172,164,228
v_fmac_f32 v80, v82, v87
Mvaa 168,160,228
v_fmac_f32 v81, v83, v87
W 4
Mvaa 176,160,232
D 14,44,65,0
K 33,80,81
Mvaa 180,164,232
Ta 244,25,49152
Ta 246,25,50176
Mvaa 240,168,232
v_lshlrev_b32 v80, 16, v33
v_and_b32 v81, 0xffff0000, v33
Mvaa 244,172,232
v_lshlrev_b32 v82, 16, v45
v_and_b32 v83, 0xffff0000, v45
Maaa 48,176,232
D 15,44,65,1024
v_fmac_f32 v84, v80, v82
Maaa 52,180,232
v_fmac_f32 v84, v81, v83
Maaa 112,184,232
v_sub_f32 v60, v60, v85
Maaa 116,188,232
v_sub_f32 v61, v61, v85
W 4
Maaa 124,188,236
D 16,44,65,2048
K 80,60,61
Maaa 120,184,236
v_lshlrev_b32 v60, 16, v80
Ta 248,26,49152
Maaa 60,180,236
Ta 250,26,50176
v_and_b32 v61, 0xffff0000, v80
Maaa 56,176,236
U 80,60,87
U 81,61,87
Mvaa 252,172,236
D 17,44,65,3072
v_fma_f32 v82, -v80, v86, v60
Mvaa 248,168,236
Ra 192,19,32768
v_fma_f32 v83, -v81, v86, v61
Mvaa 188,164,236
v_fmac_f32 v80, v82, v87
v_fmac_f32 v81, v83, v87
Mvaa 184,160,236
K 34,80,81
W 5
Mvaa 192,160,240
global_load_dword v94, v[92:93], off
v_lshl_add_u64 v[92:93], v[92:93], 0, s[68:69]
Mvaa 196,164,240
Ra 196,19,34816
v_lshlrev_b32 v80, 16, v34
Maaa 0,168,240
Ta 252,27,49152
Ta 254,27,50176
Maaa 4,172,240
v_and_b32 v81, 0xffff0000, v34
v_lshlrev_b32 v82, 16, v46
Maaa 64,176,240
Ra 200,19,36864
v_and_b32 v83, 0xffff0000, v46
Maaa 68,180,240
v_fmac_f32 v84, v80, v82
v_fmac_f32 v84, v81, v83
Maaa 128,184,240
v_sub_f32 v62, v62, v85
Ra 204,19,38912
Maaa 132,188,240
v_sub_f32 v63, v63, v85
W 8
Maaa 140,188,244
K 80,62,63
v_lshlrev_b32 v62, 16, v80
Maaa 136,184,244
Ta 224,20,57344
Ta 226,20,58368
Maaa 76,180,244
Ra 208,19,40960
v_and_b32 v63, 0xffff0000, v80
Maaa 72,176,244
U 80,62,87
U 81,63,87
Maaa 12,172,244
v_fma_f32 v82, -v80, v86, v62
Ra 212,19,43008
Maaa 8,168,244
v_fma_f32 v83, -v81, v86, v63
v_fmac_f32 v80, v82, v87
Mvaa 204,164,244
v_fmac_f32 v81, v83, v87
Ra 216,19,45056
Mvaa 200,160,244
K 35,80,81
W 11
Mvaa 208,160,248
v_lshlrev_b32 v80, 16, v35
v_and_b32 v81, 0xffff0000, v35
Mvaa 212,164,248
Ta 228,21,57344
Ta 230,21,58368
Maaa 16,168,248
v_lshlrev_b32 v82, 16, v47
Ra 220,19,47104
Maaa 20,172,248
v_and_b32 v83, 0xffff0000, v47
v_fmac_f32 v84, v80, v82
Maaa 80,176,248
v_fmac_f32 v84, v81, v83
Maaa 84,180,248
v_sub_f32 v64, v64, v85
Maaa 144,184,248
v_sub_f32 v65, v65, v85
K 80,64,65
Maaa 148,188,248
v_lshlrev_b32 v64, 16, v80
W 10
Maaa 156,188,252
v_and_b32 v65, 0xffff0000, v80
Ta 232,22,57344
Maaa 152,184,252
Ta 234,22,58368
U 80,64,87
Maaa 92,180,252
U 81,65,87
Maaa 88,176,252
v_fma_f32 v82, -v80, v86, v64
Maaa 28,172,252
v_fma_f32 v83, -v81, v86, v65
v_fmac_f32 v80, v82, v87
Maaa 24,168,252
v_fmac_f32 v81, v83, v87
Mvaa 220,164,252
K 36,80,81
Mvaa 216,160,252
v_lshlrev_b32 v80, 16, v36
W 8
Mvaa 160,192,224
v_and_b32 v81, 0xffff0000, v36
Ta 236,23,57344
Mvaa 164,196,224
Ta 238,23,58368
v_lshlrev_b32 v82, 16, v48
Mvaa 224,200,224
v_and_b32 v83, 0xffff0000, v48
v_fmac_f32 v84, v80, v82
Mvaa 228,204,224
v_fmac_f32 v84, v81, v83
W 4
Maaa 32,208,224
v_sub_f32 v66, v66, v85
Maaa 36,212,224
v_sub_f32 v67, v67, v85
Maaa 96,216,224
K 80,66,67
Maaa 100,220,224
v_lshlrev_b32 v66, 16, v80
Maaa 108,220,228
v_and_b32 v67, 0xffff0000, v80
Ta 240,24,57344
Maaa 104,216,228
Ta 242,24,58368
U 80,66,87
Maaa 44,212,228
U 81,67,87
v_fma_f32 v82, -v80, v86, v66
Maaa 40,208,228
v_fma_f32 v83, -v81, v86, v67
Mvaa 236,204,228
v_fmac_f32 v80, v82, v87
Ta 244,25,57344
Mvaa 232,200,228
Ta 246,25,58368
v_fmac_f32 v81, v83, v87
Mvaa 172,196,228
K 37,80,81
Ta 248,26,57344
Mvaa 168,192,228
Ta 250,26,58368
W 8
Mvaa 176,192,232
v_lshlrev_b32 v80, 16, v37
v_and_b32 v81, 0xffff0000, v37
Mvaa 180,196,232
v_lshlrev_b32 v82, 16, v49
Ta 252,27,57344
Mvaa 240,200,232
Ta 254,27,58368
v_and_b32 v83, 0xffff0000, v49
Mvaa 244,204,232
v_fmac_f32 v84, v80, v82
v_fmac_f32 v84, v81, v83
Maaa 48,208,232
v_sub_f32 v68, v68, v85
Maaa 52,212,232
v_sub_f32 v69, v69, v85
K 80,68,69
Maaa 112,216,232
v_lshlrev_b32 v68, 16, v80
Maaa 116,220,232
v_and_b32 v69, 0xffff0000, v80
W 8
Maaa 124,220,236
U 80,68,87
Maaa 120,216,236
U 81,69,87
Maaa 60,212,236
s_waitcnt vmcnt(1) lgkmcnt(0)
s_barrier
Maaa 56,208,236
v_fma_f32 v82, -v80, v86, v68
s_mov_b32 s64, 384
Mvaa 252,204,236
v_fma_f32 v83, -v81, v86, v69
v_fmac_f32 v80, v82, v87
Mvaa 248,200,236
v_fmac_f32 v81, v83, v87
Ra 160,18,0
Mvaa 188,196,236
K 38,80,81
s_add_u32 m0, s61, 32768
Mvaa 184,192,236
v_lshlrev_b32 v80, 16, v38
v_and_b32 v81, 0xffff0000, v38
Mvaa 192,192,240
Ra 164,18,2048
v_lshlrev_b32 v82, 16, v50
Mvaa 196,196,240
D 2,40,64,0
v_and_b32 v83, 0xffff0000, v50
Maaa 0,200,240
v_fmac_f32 v84, v80, v82
v_fmac_f32 v84, v81, v83
Maaa 4,204,240
Ra 168,18,4096
v_sub_f32 v70, v70, v85
Maaa 64,208,240
v_sub_f32 v71, v71, v85
Maaa 68,212,240
D 3,40,64,1024
K 80,70,71
Maaa 128,216,240
Ra 172,18,6144
v_lshlrev_b32 v70, 16, v80
Maaa 132,220,240
v_and_b32 v71, 0xffff0000, v80
U 80,70,87
Maaa 140,220,244
U 81,71,87
Ta 224,20,16384
Maaa 136,216,244
D 4,40,64,2048
Ta 226,20,17408
Maaa 76,212,244
Ra 176,18,8192
v_fma_f32 v82, -v80, v86, v70
Maaa 72,208,244
v_fma_f32 v83, -v81, v86, v71
v_fmac_f32 v80, v82, v87
Maaa 12,204,244
Ra 180,18,10240
v_fmac_f32 v81, v83, v87
Maaa 8,200,244
D 5,40,64,3072
s_add_u32 m0, s61, 36864
Mvaa 204,196,244
K 39,80,81
v_lshlrev_b32 v80, 16, v39
Mvaa 200,192,244
v_and_b32 v81, 0xffff0000, v39
Ra 184,18,12288
Mvaa 208,192,248
v_lshlrev_b32 v82, 16, v51
v_and_b32 v83, 0xffff0000, v51
Mvaa 212,196,248
D 6,40,64,0
Ta 228,21,16384
Maaa 16,200,248
Ta 230,21,17408
v_fmac_f32 v84, v80, v82
Maaa 20,204,248
Ra 188,18,14336
v_fmac_f32 v84, v81, v83
Maaa 80,208,248
v_sub_f32 v72, v72, v85
)"
R"(v_sub_f32 v73, v73, v85
Maaa 84,212,248
D 7,40,64,1024
K 80,72,73
Maaa 144,216,248
v_lshlrev_b32 v72, 16, v80
v_and_b32 v73, 0xffff0000, v80
Maaa 148,220,248
U 80,72,87
Maaa 156,220,252
U 81,73,87
Ta 232,22,16384
Maaa 152,216,252
D 8,40,64,2048
Ta 234,22,17408
Maaa 92,212,252
v_fma_f32 v82, -v80, v86, v72
v_fma_f32 v83, -v81, v86, v73
Maaa 88,208,252
v_fmac_f32 v80, v82, v87
v_fmac_f32 v81, v83, v87
Maaa 28,204,252
K 40,80,81
Maaa 24,200,252
D 9,40,64,3072
s_mov_b32 s65, 1572864
Mvaa 220,196,252
v_lshlrev_b32 v80, 16, v40
s_add_u32 m0, s62, 32768
Mvaa 216,192,252
v_and_b32 v81, 0xffff0000, v40
W 8
Mvaa 160,160,224
v_lshlrev_b32 v82, 16, v52
v_and_b32 v83, 0xffff0000, v52
Mvaa 164,164,224
D 10,44,65,0
v_fmac_f32 v84, v80, v82
Mvaa 224,168,224
Ta 236,23,16384
Ta 238,23,17408
Mvaa 228,172,224
v_fmac_f32 v84, v81, v83
W 4
Maaa 32,176,224
v_sub_f32 v74, v74, v85
v_sub_f32 v75, v75, v85
Maaa 36,180,224
D 11,44,65,1024
K 80,74,75
Maaa 96,184,224
v_lshlrev_b32 v74, 16, v80
v_and_b32 v75, 0xffff0000, v80
Maaa 100,188,224
U 80,74,87
U 81,75,87
Maaa 108,188,228
v_fma_f32 v82, -v80, v86, v74
Ta 240,24,16384
Maaa 104,184,228
D 12,44,65,2048
Ta 242,24,17408
Maaa 44,180,228
v_fma_f32 v83, -v81, v86, v75
v_fmac_f32 v80, v82, v87
Maaa 40,176,228
v_fmac_f32 v81, v83, v87
Mvaa 236,172,228
K 41,80,81
Mvaa 232,168,228
D 13,44,65,3072
s_add_u32 m0, s62, 36864
Mvaa 172,164,228
v_lshlrev_b32 v80, 16, v41
v_and_b32 v81, 0xffff0000, v41
Mvaa 168,160,228
v_lshlrev_b32 v82, 16, v53
W 4
Mvaa 176,160,232
v_and_b32 v83, 0xffff0000, v53
v_fmac_f32 v84, v80, v82
Mvaa 180,164,232
D 14,44,65,0
Ta 244,25,16384
Mvaa 240,168,232
Ta 246,25,17408
v_fmac_f32 v84, v81, v83
Mvaa 244,172,232
v_sub_f32 v76, v76, v85
v_sub_f32 v77, v77, v85
Maaa 48,176,232
K 80,76,77
v_lshlrev_b32 v76, 16, v80
Maaa 52,180,232
D 15,44,65,1024
v_and_b32 v77, 0xffff0000, v80
Maaa 112,184,232
U 80,76,87
Maaa 116,188,232
U 81,77,87
W 4
Maaa 124,188,236
v_fma_f32 v82, -v80, v86, v76
Ta 248,26,16384
Maaa 120,184,236
D 16,44,65,2048
Ta 250,26,17408
Maaa 60,180,236
v_fma_f32 v83, -v81, v86, v77
v_fmac_f32 v80, v82, v87
Maaa 56,176,236
v_fmac_f32 v81, v83, v87
K 42,80,81
Mvaa 252,172,236
v_lshlrev_b32 v80, 16, v42
Ra 192,19,0
Mvaa 248,168,236
D 17,44,65,3072
v_and_b32 v81, 0xffff0000, v42
Mvaa 188,164,236
v_lshlrev_b32 v82, 16, v54
Mvaa 184,160,236
v_and_b32 v83, 0xffff0000, v54
W 5
Mvaa 192,160,240
v_fmac_f32 v84, v80, v82
Ra 196,19,2048
Mvaa 196,164,240
v_fmac_f32 v84, v81, v83
Ta 252,27,16384
Maaa 0,168,240
Ta 254,27,17408
v_sub_f32 v78, v78, v85
Maaa 4,172,240
v_sub_f32 v79, v79, v85
Ra 200,19,4096
Maaa 64,176,240
K 80,78,79
v_lshlrev_b32 v78, 16, v80
Maaa 68,180,240
v_and_b32 v79, 0xffff0000, v80
U 80,78,87
Maaa 128,184,240
Ra 204,19,6144
U 81,79,87
Maaa 132,188,240
v_fma_f32 v82, -v80, v86, v78
W 8
Maaa 140,188,244
v_fma_f32 v83, -v81, v86, v79
Ta 224,20,24576
Maaa 136,184,244
Ta 226,20,25600
Ra 208,19,8192
Maaa 76,180,244
v_fmac_f32 v80, v82, v87
v_fmac_f32 v81, v83, v87
Maaa 72,176,244
K 43,80,81
Ra 212,19,10240
Maaa 12,172,244
v_lshlrev_b32 v80, 16, v43
v_and_b32 v81, 0xffff0000, v43
Maaa 8,168,244
v_lshlrev_b32 v82, 16, v55
Mvaa 204,164,244
v_and_b32 v83, 0xffff0000, v55
Ra 216,19,12288
Mvaa 200,160,244
v_fmac_f32 v84, v80, v82
W 11
Mvaa 208,160,248
v_fmac_f32 v84, v81, v83
Ta 228,21,24576
Mvaa 212,164,248
Ta 230,21,25600
v_lshrrev_b32 v82, 9, v1
Maaa 16,168,248
Ra 220,19,14336
s_lshl_b32 s80, s14, 1
Maaa 20,172,248
v_add_u32 v82, s80, v82
v_mul_u32_u24 v83, 1552, v82
Maaa 80,176,248
v_add_u32 v83, v83, v88
Maaa 84,180,248
v_add_u32 v83, 131072, v83
Maaa 144,184,248
ds_write_b128 v83, v[32:35] offset:0
Maaa 148,188,248
ds_write_b128 v83, v[36:39] offset:512
W 12
Maaa 156,188,252
ds_write_b128 v83, v[40:43] offset:1024
v_add_f32_dpp v84, v84, v84 quad_perm:[1,0,3,2] row_mask:0xf bank_mask:0xf
Maaa 152,184,252
Ta 232,22,24576
Ta 234,22,25600
Maaa 92,180,252
v_add_f32_dpp v84, v84, v84 quad_perm:[2,3,0,1] row_mask:0xf bank_mask:0xf
Maaa 88,176,252
Maaa 28,172,252
v_add_f32_dpp v84, v84, v84 row_half_mirror row_mask:0xf bank_mask:0xf
Maaa 24,168,252
Mvaa 220,164,252
v_add_f32_dpp v84, v84, v84 row_mirror row_mask:0xf bank_mask:0xf
v_mov_b32 v81, v84
Mvaa 216,160,252
W 11
Mvaa 160,192,224
v_permlane16_swap_b32 v84, v81
A 84,84,81
Mvaa 164,196,224
K 80,84,84
v_lshlrev_b32 v80, 16, v80
Mvaa 224,200,224
s_lshl_b32 s80, s15, 3
Ta 236,23,24576
Mvaa 228,204,224
Ta 238,23,25600
W 7
Maaa 32,208,224
v_add_u32 v82, s80, v82
v_sub_u32 v83, 2047, v82
Maaa 36,212,224
v_ashrrev_i32 v81, 31, v80
v_or_b32 v81, 0x80000000, v81
Maaa 96,216,224
v_xor_b32 v80, v80, v81
v_and_b32 v80, 0xffff0000, v80
Maaa 100,220,224
v_or_b32 v80, v80, v83
v_lshlrev_b32 v81, 23, v88
Maaa 108,220,228
v_lshl_or_b32 v82, v82, 2, v81
Ta 240,24,24576
Maaa 104,216,228
Ta 242,24,25600
s_mov_b32 s82, 2752512
Maaa 44,212,228
buffer_store_dword v80, v82, s[8:11], s82 offen sc1
Maaa 40,208,228
Mvaa 236,204,228
Ta 244,25,24576
Ta 246,25,25600
Mvaa 232,200,228
Mvaa 172,196,228
Ta 248,26,24576
Ta 250,26,25600
Mvaa 168,192,228
W 8
Mvaa 176,192,232
Ta 252,27,24576
Ta 254,27,25600
Mvaa 180,196,232
Mvaa 240,200,232
Mvaa 244,204,232
Maaa 48,208,232
Maaa 52,212,232
Maaa 112,216,232
Maaa 116,220,232
W 8
Maaa 124,220,236
Maaa 120,216,236
Maaa 60,212,236
s_waitcnt vmcnt(1) lgkmcnt(0)
s_barrier
Maaa 56,208,236
s_mov_b32 s64, 512
v_bfe_u32 v80, v1, 4, 3
Mvaa 252,204,236
v_mul_u32_u24 v81, 1552, v80
Ra 160,18,32768
Mvaa 248,200,236
v_lshrrev_b32 v82, 7, v1
s_add_u32 m0, s61, 0
Mvaa 188,196,236
v_lshl_add_u32 v81, v82, 4, v81
Mvaa 184,192,236
s_lshl_b32 s80, s14, 7
s_add_u32 s80, s80, 131072
Mvaa 192,192,240
Ra 164,18,34816
D 2,40,64,0
Mvaa 196,196,240
v_add_u32 v81, s80, v81
ds_read_b128 v[32:35], v81
Maaa 0,200,240
Rv 36,81,512
Ra 168,18,36864
Maaa 4,204,240
s_and_b32 s80, s14, 2
s_lshl_b32 s80, s80, 7
Maaa 64,208,240
D 3,40,64,1024
v_subrev_u32 v81, s80, v81
Maaa 68,212,240
Rv 40,81,1024
Ra 172,18,38912
Maaa 128,216,240
v_lshlrev_b32 v80, 4, v80
v_and_b32 v81, 3, v82
Maaa 132,220,240
v_lshl_add_u32 v80, v81, 8, v80
Maaa 140,220,244
D 4,40,64,2048
v_lshrrev_b32 v82, 2, v82
Maaa 136,216,244
Ta 224,20,49152
Ta 226,20,50176
Maaa 76,212,244
Ra 176,18,40960
v_lshl_add_u32 v80, v82, 12, v80
Maaa 72,208,244
s_lshr_b32 s82, s15, 3
s_mul_i32 s82, s82, 81920
Maaa 12,204,244
D 5,40,64,3072
s_add_u32 m0, s61, 4096
Maaa 8,200,244
s_bfe_u32 s80, s15, 0x20001
Ra 180,18,43008
Mvaa 204,196,244
s_lshl_b32 s80, s80, 10
s_add_u32 s82, s82, s80
Mvaa 200,192,244
s_and_b32 s80, s15, 1
s_lshl_b32 s80, s80, 7
Mvaa 208,192,248
D 6,40,64,0
Ra 184,18,45056
Mvaa 212,196,248
s_add_u32 s82, s82, s80
s_add_u32 s82, s82, 131072
Maaa 16,200,248
Ta 228,21,49152
Ta 230,21,50176
Maaa 20,204,248
s_lshl_b32 s80, s14, 13
s_add_u32 s83, s82, s80
Maaa 80,208,248
D 7,40,64,1024
Ra 188,18,47104
Maaa 84,212,248
W 9
buffer_store_dwordx4 v[32:35], v80, s[8:11], s83 offen sc1
Maaa 144,216,248
s_add_u32 s83, s83, 32768
Maaa 148,220,248
Maaa 156,220,252
D 8,40,64,2048
Ta 232,22,49152
Maaa 152,216,252
Ta 234,22,50176
Maaa 92,212,252
Maaa 88,208,252
Maaa 28,204,252
buffer_store_dwordx4 v[36:39], v80, s[8:11], s83 offen sc1
s_and_b32 s80, s80, 8192
Maaa 24,200,252
s_add_u32 s83, s82, s80
s_add_u32 s83, s83, 65536
Mvaa 220,196,252
Mvaa 216,192,252
W 8
Mvaa 160,160,224
D 9,40,64,3072
s_mov_b32 s65, 2097152
Mvaa 164,164,224
s_add_u32 m0, s62, 0
Ta 236,23,49152
Mvaa 224,168,224
Ta 238,23,50176
Mvaa 228,172,224
W 4
Maaa 32,176,224
buffer_store_dwordx4 v[40:43], v80, s[8:11], s83 offen sc1
Maaa 36,180,224
Maaa 96,184,224
Maaa 100,188,224
Maaa 108,188,228
D 10,44,65,0
Ta 240,24,49152
Maaa 104,184,228
Ta 242,24,50176
Maaa 44,180,228
Maaa 40,176,228
Mvaa 236,172,228
D 11,44,65,1024
Mvaa 232,168,228
Mvaa 172,164,228
Mvaa 168,160,228
W 4
Mvaa 176,160,232
D 12,44,65,2048
Ta 244,25,49152
Mvaa 180,164,232
Ta 246,25,50176
Mvaa 240,168,232
Mvaa 244,172,232
)"
R"(Maaa 48,176,232
D 13,44,65,3072
s_add_u32 m0, s62, 4096
Maaa 52,180,232
Maaa 112,184,232
Maaa 116,188,232
W 4
Maaa 124,188,236
D 14,44,65,0
Ta 248,26,49152
Maaa 120,184,236
Ta 250,26,50176
Maaa 60,180,236
Maaa 56,176,236
Mvaa 252,172,236
D 15,44,65,1024
Ra 192,19,32768
Mvaa 248,168,236
Mvaa 188,164,236
Mvaa 184,160,236
Ra 196,19,34816
W 6
Mvaa 192,160,240
D 16,44,65,2048
Ta 252,27,49152
Mvaa 196,164,240
Ta 254,27,50176
Maaa 0,168,240
Ra 200,19,36864
Maaa 4,172,240
Maaa 64,176,240
D 17,44,65,3072
Maaa 68,180,240
Ra 204,19,38912
Maaa 128,184,240
Maaa 132,188,240
W 8
Maaa 140,188,244
global_load_dword v94, v[92:93], off
v_lshl_add_u64 v[92:93], v[92:93], 0, s[68:69]
Maaa 136,184,244
Ta 224,20,57344
Ta 226,20,58368
Maaa 76,180,244
Ra 208,19,40960
Maaa 72,176,244
Ra 212,19,43008
Maaa 12,172,244
Maaa 8,168,244
Mvaa 204,164,244
Ra 216,19,45056
Mvaa 200,160,244
W 11
Mvaa 208,160,248
Ta 228,21,57344
Ta 230,21,58368
Mvaa 212,164,248
Ra 220,19,47104
Maaa 16,168,248
Maaa 20,172,248
Maaa 80,176,248
Maaa 84,180,248
Maaa 144,184,248
Maaa 148,188,248
W 10
Maaa 156,188,252
Ta 232,22,57344
Ta 234,22,58368
Maaa 152,184,252
Maaa 92,180,252
Maaa 88,176,252
Maaa 28,172,252
Maaa 24,168,252
Mvaa 220,164,252
Mvaa 216,160,252
W 8
Mvaa 160,192,224
Ta 236,23,57344
Ta 238,23,58368
Mvaa 164,196,224
Mvaa 224,200,224
Mvaa 228,204,224
W 4
Maaa 32,208,224
Maaa 36,212,224
Maaa 96,216,224
Maaa 100,220,224
Maaa 108,220,228
Ta 240,24,57344
Ta 242,24,58368
Maaa 104,216,228
Maaa 44,212,228
Maaa 40,208,228
Mvaa 236,204,228
Ta 244,25,57344
Ta 246,25,58368
Mvaa 232,200,228
Mvaa 172,196,228
Ta 248,26,57344
Ta 250,26,58368
Mvaa 168,192,228
W 8
Mvaa 176,192,232
Ta 252,27,57344
Ta 254,27,58368
Mvaa 180,196,232
Mvaa 240,200,232
Mvaa 244,204,232
Maaa 48,208,232
Maaa 52,212,232
Maaa 112,216,232
Maaa 116,220,232
W 8
Maaa 124,220,236
Maaa 120,216,236
Maaa 60,212,236
s_waitcnt vmcnt(1) lgkmcnt(0)
s_barrier
s_cmp_eq_u32 s14, 0
s_cbranch_scc1 .Lpub_15%=
.Lpubr_16%=:
Maaa 56,208,236
s_mov_b32 s64, 640
Mvaa 252,204,236
Ra 160,18,0
Mvaa 248,200,236
s_add_u32 m0, s61, 32768
Mvaa 188,196,236
Mvaa 184,192,236
Ra 164,18,2048
Mvaa 192,192,240
D 2,40,64,0
Mvaa 196,196,240
Maaa 0,200,240
Ra 168,18,4096
Maaa 4,204,240
Maaa 64,208,240
D 3,40,64,1024
Maaa 68,212,240
Ra 172,18,6144
Maaa 128,216,240
Maaa 132,220,240
Maaa 140,220,244
D 4,40,64,2048
Ta 224,20,16384
Maaa 136,216,244
Ta 226,20,17408
Ra 176,18,8192
Maaa 76,212,244
Maaa 72,208,244
Ra 180,18,10240
Maaa 12,204,244
D 5,40,64,3072
s_add_u32 m0, s61, 36864
Maaa 8,200,244
Mvaa 204,196,244
Ra 184,18,12288
Mvaa 200,192,244
Mvaa 208,192,248
D 6,40,64,0
Ta 228,21,16384
Mvaa 212,196,248
Ta 230,21,17408
Ra 188,18,14336
Maaa 16,200,248
Maaa 20,204,248
Maaa 80,208,248
D 7,40,64,1024
Maaa 84,212,248
Maaa 144,216,248
Maaa 148,220,248
Maaa 156,220,252
D 8,40,64,2048
Ta 232,22,16384
Maaa 152,216,252
Ta 234,22,17408
Maaa 92,212,252
Maaa 88,208,252
Maaa 28,204,252
D 9,40,64,3072
s_mov_b32 s65, 2621440
Maaa 24,200,252
s_add_u32 m0, s62, 32768
Mvaa 220,196,252
Mvaa 216,192,252
W 8
Mvaa 160,160,224
D 10,44,65,0
Ta 236,23,16384
Mvaa 164,164,224
Ta 238,23,17408
Mvaa 224,168,224
Mvaa 228,172,224
W 4
Maaa 32,176,224
D 11,44,65,1024
Maaa 36,180,224
Maaa 96,184,224
Maaa 100,188,224
Maaa 108,188,228
D 12,44,65,2048
Ta 240,24,16384
Maaa 104,184,228
Ta 242,24,17408
Maaa 44,180,228
Maaa 40,176,228
Mvaa 236,172,228
D 13,44,65,3072
s_add_u32 m0, s62, 36864
Mvaa 232,168,228
Mvaa 172,164,228
Mvaa 168,160,228
W 4
Mvaa 176,160,232
D 14,44,65,0
Ta 244,25,16384
Mvaa 180,164,232
Ta 246,25,17408
Mvaa 240,168,232
Mvaa 244,172,232
Maaa 48,176,232
D 15,44,65,1024
Maaa 52,180,232
Maaa 112,184,232
Maaa 116,188,232
W 4
Maaa 124,188,236
D 16,44,65,2048
Ta 248,26,16384
Maaa 120,184,236
Ta 250,26,17408
Maaa 60,180,236
Maaa 56,176,236
Mvaa 252,172,236
D 17,44,65,3072
s_mov_b32 s65, 0
Mvaa 248,168,236
s_add_u32 m0, s63, 131072
Ra 192,19,0
Mvaa 188,164,236
Mvaa 184,160,236
Ra 196,19,2048
W 6
Mvaa 192,160,240
D 10,48,65,0
Ta 252,27,16384
Mvaa 196,164,240
Ta 254,27,17408
Maaa 0,168,240
Ra 200,19,4096
Maaa 4,172,240
Maaa 64,176,240
D 11,48,65,1024
Maaa 68,180,240
Ra 204,19,6144
Maaa 128,184,240
Maaa 132,188,240
W 8
Maaa 140,188,244
D 12,48,65,2048
Ta 224,20,24576
Maaa 136,184,244
Ta 226,20,25600
Ra 208,19,8192
Maaa 76,180,244
Maaa 72,176,244
Ra 212,19,10240
Maaa 12,172,244
D 13,48,65,3072
s_add_u32 m0, s63, 135168
Maaa 8,168,244
Mvaa 204,164,244
Ra 216,19,12288
Mvaa 200,160,244
W 11
Mvaa 208,160,248
D 14,48,65,0
Ta 228,21,24576
Mvaa 212,164,248
Ta 230,21,25600
Ra 220,19,14336
Maaa 16,168,248
Maaa 20,172,248
Maaa 80,176,248
D 15,48,65,1024
Maaa 84,180,248
Maaa 144,184,248
Maaa 148,188,248
W 10
Maaa 156,188,252
D 16,48,65,2048
Ta 232,22,24576
Maaa 152,184,252
Ta 234,22,25600
Maaa 92,180,252
Maaa 88,176,252
Maaa 28,172,252
D 17,48,65,3072
v_lshrrev_b32 v138, 4, v1
Maaa 24,168,252
s_sub_u32 s81, s73, 1
v_min_u32 v138, s81, v138
Mvaa 220,164,252
v_lshrrev_b32 v139, 2, v138
v_mul_u32_u24 v139, s72, v139
Mvaa 216,160,252
v_and_b32 v138, 3, v138
W 8
Mvaa 160,192,224
v_lshl_add_u32 v138, v138, 7, v139
v_add_u32 v138, s74, v138
Mvaa 164,196,224
s_lshr_b32 s73, s73, 2
s_mul_i32 s73, s73, s72
Mvaa 224,200,224
v_min_u32 v139, s75, v138
global_load_dword v94, v139, s[76:77]
Mvaa 228,204,224
v_add_u32 v138, s73, v138
W 2
Maaa 32,208,224
Ta 236,23,24576
Ta 238,23,25600
Maaa 36,212,224
Maaa 96,216,224
Maaa 100,220,224
Maaa 108,220,228
Ta 240,24,24576
Ta 242,24,25600
Maaa 104,216,228
Maaa 44,212,228
Maaa 40,208,228
Mvaa 236,204,228
Ta 244,25,24576
Ta 246,25,25600
Mvaa 232,200,228
Mvaa 172,196,228
Ta 248,26,24576
Ta 250,26,25600
Mvaa 168,192,228
W 8
Mvaa 176,192,232
Ta 252,27,24576
Ta 254,27,25600
Mvaa 180,196,232
Mvaa 240,200,232
Mvaa 244,204,232
Maaa 48,208,232
Maaa 52,212,232
Maaa 112,216,232
Maaa 116,220,232
W 8
Maaa 124,220,236
Maaa 120,216,236
Maaa 60,212,236
s_waitcnt vmcnt(1) lgkmcnt(0)
s_barrier
Maaa 56,208,236
s_mov_b32 s64, 768
Mvaa 252,204,236
Ra 160,18,32768
Mvaa 248,200,236
s_add_u32 m0, s61, 0
Mvaa 188,196,236
Mvaa 184,192,236
Ra 164,18,34816
Mvaa 192,192,240
D 2,40,64,0
Mvaa 196,196,240
Maaa 0,200,240
Ra 168,18,36864
Maaa 4,204,240
Maaa 64,208,240
D 3,40,64,1024
Maaa 68,212,240
Ra 172,18,38912
Maaa 128,216,240
Maaa 132,220,240
Maaa 140,220,244
D 4,40,64,2048
Ta 224,20,49152
Maaa 136,216,244
Ta 226,20,50176
Ra 176,18,40960
Maaa 76,212,244
Maaa 72,208,244
Ra 180,18,43008
Maaa 12,204,244
D 5,40,64,3072
s_add_u32 m0, s61, 4096
Maaa 8,200,244
Mvaa 204,196,244
Ra 184,18,45056
Mvaa 200,192,244
Mvaa 208,192,248
D 6,40,64,0
Ta 228,21,49152
Mvaa 212,196,248
Ta 230,21,50176
Ra 188,18,47104
)"
R"(Maaa 16,200,248
Maaa 20,204,248
Maaa 80,208,248
D 7,40,64,1024
Maaa 84,212,248
Maaa 144,216,248
Maaa 148,220,248
Maaa 156,220,252
D 8,40,64,2048
Ta 232,22,49152
Maaa 152,216,252
Ta 234,22,50176
Maaa 92,212,252
Maaa 88,208,252
Maaa 28,204,252
D 9,40,64,3072
s_mov_b32 s65, 3145728
Maaa 24,200,252
s_add_u32 m0, s62, 0
Mvaa 220,196,252
Mvaa 216,192,252
W 8
Mvaa 160,160,224
D 10,44,65,0
Ta 236,23,49152
Mvaa 164,164,224
Ta 238,23,50176
Mvaa 224,168,224
Mvaa 228,172,224
W 4
Maaa 32,176,224
D 11,44,65,1024
Maaa 36,180,224
Maaa 96,184,224
Maaa 100,188,224
Maaa 108,188,228
D 12,44,65,2048
Ta 240,24,49152
Maaa 104,184,228
Ta 242,24,50176
Maaa 44,180,228
Maaa 40,176,228
Mvaa 236,172,228
D 13,44,65,3072
s_add_u32 m0, s62, 4096
Mvaa 232,168,228
Mvaa 172,164,228
Mvaa 168,160,228
W 4
Mvaa 176,160,232
D 14,44,65,0
Ta 244,25,49152
Mvaa 180,164,232
Ta 246,25,50176
Mvaa 240,168,232
Mvaa 244,172,232
Maaa 48,176,232
D 15,44,65,1024
Maaa 52,180,232
Maaa 112,184,232
Maaa 116,188,232
W 4
Maaa 124,188,236
D 16,44,65,2048
Ta 248,26,49152
Maaa 120,184,236
Ta 250,26,50176
Maaa 60,180,236
Maaa 56,176,236
Mvaa 252,172,236
D 17,44,65,3072
Ra 192,19,32768
Mvaa 248,168,236
Mvaa 188,164,236
Mvaa 184,160,236
Ra 196,19,34816
W 6
Mvaa 192,160,240
Ta 252,27,49152
Ta 254,27,50176
Mvaa 196,164,240
v_min_u32 v139, s75, v138
Maaa 0,168,240
Ra 200,19,36864
Maaa 4,172,240
Maaa 64,176,240
Maaa 68,180,240
global_load_dword v94, v139, s[76:77]
Ra 204,19,38912
Maaa 128,184,240
Maaa 132,188,240
W 8
Maaa 140,188,244
Ta 224,20,57344
Ta 226,20,58368
Maaa 136,184,244
Ra 208,19,40960
Maaa 76,180,244
v_add_u32 v138, s73, v138
Maaa 72,176,244
Ra 212,19,43008
Maaa 12,172,244
Maaa 8,168,244
Mvaa 204,164,244
Ra 216,19,45056
Mvaa 200,160,244
W 11
Mvaa 208,160,248
Ta 228,21,57344
Ta 230,21,58368
Mvaa 212,164,248
Ra 220,19,47104
Maaa 16,168,248
Maaa 20,172,248
Maaa 80,176,248
Maaa 84,180,248
Maaa 144,184,248
Maaa 148,188,248
W 10
Maaa 156,188,252
Ta 232,22,57344
Ta 234,22,58368
Maaa 152,184,252
Maaa 92,180,252
Maaa 88,176,252
Maaa 28,172,252
Maaa 24,168,252
Mvaa 220,164,252
Mvaa 216,160,252
W 8
Mvaa 160,192,224
Ta 236,23,57344
Ta 238,23,58368
Mvaa 164,196,224
Mvaa 224,200,224
Mvaa 228,204,224
W 4
Maaa 32,208,224
Maaa 36,212,224
Maaa 96,216,224
Maaa 100,220,224
Maaa 108,220,228
Ta 240,24,57344
Ta 242,24,58368
Maaa 104,216,228
Maaa 44,212,228
Maaa 40,208,228
Mvaa 236,204,228
Ta 244,25,57344
Ta 246,25,58368
Mvaa 232,200,228
Mvaa 172,196,228
Ta 248,26,57344
Ta 250,26,58368
Mvaa 168,192,228
W 8
Mvaa 176,192,232
Ta 252,27,57344
Ta 254,27,58368
Mvaa 180,196,232
Mvaa 240,200,232
Mvaa 244,204,232
Maaa 48,208,232
Maaa 52,212,232
Maaa 112,216,232
Maaa 116,220,232
W 8
Maaa 124,220,236
Maaa 120,216,236
Maaa 60,212,236
s_waitcnt vmcnt(1) lgkmcnt(0)
s_barrier
s_cmp_eq_u32 s85, 43
s_cbranch_scc1 .Lrt_17%=
.Lrtr_18%=:
Maaa 56,208,236
s_mov_b32 s64, 896
Mvaa 252,204,236
Ra 160,18,0
Mvaa 248,200,236
s_add_u32 m0, s61, 32768
Mvaa 188,196,236
Mvaa 184,192,236
Ra 164,18,2048
Mvaa 192,192,240
D 2,40,64,0
Mvaa 196,196,240
Maaa 0,200,240
Ra 168,18,4096
Maaa 4,204,240
Maaa 64,208,240
D 3,40,64,1024
Maaa 68,212,240
Ra 172,18,6144
Maaa 128,216,240
Maaa 132,220,240
Maaa 140,220,244
D 4,40,64,2048
Ta 224,20,16384
Maaa 136,216,244
Ta 226,20,17408
Ra 176,18,8192
Maaa 76,212,244
Maaa 72,208,244
Ra 180,18,10240
Maaa 12,204,244
D 5,40,64,3072
s_add_u32 m0, s61, 36864
Maaa 8,200,244
Mvaa 204,196,244
Ra 184,18,12288
Mvaa 200,192,244
Mvaa 208,192,248
D 6,40,64,0
Ta 228,21,16384
Mvaa 212,196,248
Ta 230,21,17408
Ra 188,18,14336
Maaa 16,200,248
Maaa 20,204,248
Maaa 80,208,248
D 7,40,64,1024
Maaa 84,212,248
Maaa 144,216,248
Maaa 148,220,248
Maaa 156,220,252
D 8,40,64,2048
Ta 232,22,16384
Maaa 152,216,252
Ta 234,22,17408
Maaa 92,212,252
Maaa 88,208,252
Maaa 28,204,252
D 9,40,64,3072
s_mov_b32 s65, 3670016
Maaa 24,200,252
s_add_u32 m0, s62, 32768
Mvaa 220,196,252
Mvaa 216,192,252
W 8
Mvaa 160,160,224
D 10,44,65,0
Ta 236,23,16384
Mvaa 164,164,224
Ta 238,23,17408
Mvaa 224,168,224
Mvaa 228,172,224
W 4
Maaa 32,176,224
D 11,44,65,1024
Maaa 36,180,224
Maaa 96,184,224
Maaa 100,188,224
Maaa 108,188,228
D 12,44,65,2048
Ta 240,24,16384
Maaa 104,184,228
Ta 242,24,17408
Maaa 44,180,228
Maaa 40,176,228
Mvaa 236,172,228
D 13,44,65,3072
s_add_u32 m0, s62, 36864
Mvaa 232,168,228
Mvaa 172,164,228
Mvaa 168,160,228
W 4
Mvaa 176,160,232
D 14,44,65,0
Ta 244,25,16384
Mvaa 180,164,232
Ta 246,25,17408
Mvaa 240,168,232
Mvaa 244,172,232
Maaa 48,176,232
D 15,44,65,1024
Maaa 52,180,232
Maaa 112,184,232
Maaa 116,188,232
W 4
Maaa 124,188,236
D 16,44,65,2048
Ta 248,26,16384
Maaa 120,184,236
Ta 250,26,17408
Maaa 60,180,236
Maaa 56,176,236
Mvaa 252,172,236
D 17,44,65,3072
Ra 192,19,0
Mvaa 248,168,236
Mvaa 188,164,236
v_add_u32 v2, 131072, v20
Mvaa 184,160,236
Ra 196,19,2048
W 6
Mvaa 192,160,240
Ta 252,27,16384
Ta 254,27,17408
Mvaa 196,164,240
v_add_u32 v3, 131072, v21
Maaa 0,168,240
Ra 200,19,4096
v_add_u32 v4, 131072, v22
Maaa 4,172,240
Maaa 64,176,240
v_add_u32 v5, 131072, v23
Maaa 68,180,240
Ra 204,19,6144
Maaa 128,184,240
v_add_u32 v6, 131072, v24
Maaa 132,188,240
W 8
Maaa 140,188,244
Ta 224,20,24576
Ta 226,20,25600
Maaa 136,184,244
Ra 208,19,8192
v_add_u32 v7, 131072, v25
Maaa 76,180,244
v_add_u32 v8, 131072, v26
Maaa 72,176,244
Ra 212,19,10240
Maaa 12,172,244
v_add_u32 v9, 131072, v27
Maaa 8,168,244
Mvaa 204,164,244
Ra 216,19,12288
Mvaa 200,160,244
W 11
Mvaa 208,160,248
Ta 228,21,24576
Ta 230,21,25600
Mvaa 212,164,248
Ra 220,19,14336
Maaa 16,168,248
Maaa 20,172,248
Maaa 80,176,248
Maaa 84,180,248
Maaa 144,184,248
Maaa 148,188,248
W 10
Maaa 156,188,252
Ta 232,22,24576
Ta 234,22,25600
Maaa 152,184,252
Maaa 92,180,252
Maaa 88,176,252
Maaa 28,172,252
Maaa 24,168,252
Mvaa 220,164,252
Mvaa 216,160,252
W 8
Mvaa 160,192,224
Ta 236,23,24576
Ta 238,23,25600
Mvaa 164,196,224
Mvaa 224,200,224
Mvaa 228,204,224
W 4
Maaa 32,208,224
Maaa 36,212,224
Maaa 96,216,224
Maaa 100,220,224
Maaa 108,220,228
Ta 240,24,24576
Ta 242,24,25600
Maaa 104,216,228
Maaa 44,212,228
Maaa 40,208,228
Mvaa 236,204,228
Ta 244,25,24576
Ta 246,25,25600
Mvaa 232,200,228
Mvaa 172,196,228
Ta 248,26,24576
Ta 250,26,25600
Mvaa 168,192,228
W 8
Mvaa 176,192,232
Ta 252,27,24576
Ta 254,27,25600
Mvaa 180,196,232
Mvaa 240,200,232
Mvaa 244,204,232
Maaa 48,208,232
Maaa 52,212,232
Maaa 112,216,232
Maaa 116,220,232
W 8
Maaa 124,220,236
Maaa 120,216,236
Maaa 60,212,236
s_waitcnt vmcnt(0) lgkmcnt(0)
s_barrier
s_cmp_eq_u32 s85, 43
s_cbranch_scc1 .Lrt_19%=
.Lrtr_20%=:
Maaa 56,208,236
s_mov_b32 s65, 1048576
)"
R"(Mvaa 252,204,236
Ra 160,18,32768
Mvaa 248,200,236
s_add_u32 m0, s63, 0
Mvaa 188,196,236
Mvaa 184,192,236
Ra 164,18,34816
Mvaa 192,192,240
D 10,48,65,0
Mvaa 196,196,240
Maaa 0,200,240
Ra 168,18,36864
Maaa 4,204,240
Maaa 64,208,240
D 11,48,65,1024
Maaa 68,212,240
Ra 172,18,38912
Maaa 128,216,240
Maaa 132,220,240
Maaa 140,220,244
D 12,48,65,2048
Ta 224,20,49152
Maaa 136,216,244
Ta 226,20,50176
Ra 176,18,40960
Maaa 76,212,244
Maaa 72,208,244
Ra 180,18,43008
Maaa 12,204,244
D 13,48,65,3072
s_add_u32 m0, s63, 4096
Maaa 8,200,244
Mvaa 204,196,244
Ra 184,18,45056
Mvaa 200,192,244
Mvaa 208,192,248
D 14,48,65,0
Ta 228,21,49152
Mvaa 212,196,248
Ta 230,21,50176
Ra 188,18,47104
Maaa 16,200,248
Maaa 20,204,248
Maaa 80,208,248
D 15,48,65,1024
Maaa 84,212,248
Maaa 144,216,248
v_bfe_u32 v28, v1, 4, 3
Maaa 148,220,248
v_lshlrev_b32 v28, 3, v28
Maaa 156,220,252
D 16,48,65,2048
s_lshl_b32 s80, s60, 6
Maaa 152,216,252
Ta 232,22,49152
Ta 234,22,50176
Maaa 92,212,252
s_add_u32 s80, s80, 256
Maaa 88,208,252
Maaa 28,204,252
D 17,48,65,3072
s_mov_b32 s65, 2097152
Maaa 24,200,252
s_add_u32 m0, s63, 65536
Mvaa 220,196,252
Mvaa 216,192,252
W 8
Mvaa 160,160,224
buffer_load_dwordx2 v[28:29], v28, s[8:11], s80 offen sc1
Ta 236,23,49152
Mvaa 164,164,224
Ta 238,23,50176
Mvaa 224,168,224
Mvaa 228,172,224
W 4
Maaa 32,176,224
D 10,48,65,0
Maaa 36,180,224
Maaa 96,184,224
Maaa 100,188,224
Maaa 108,188,228
D 11,48,65,1024
Ta 240,24,49152
Maaa 104,184,228
Ta 242,24,50176
Maaa 44,180,228
Maaa 40,176,228
Mvaa 236,172,228
D 12,48,65,2048
Mvaa 232,168,228
Mvaa 172,164,228
Mvaa 168,160,228
W 4
Mvaa 176,160,232
D 13,48,65,3072
s_add_u32 m0, s63, 69632
Mvaa 180,164,232
Ta 244,25,49152
Ta 246,25,50176
Mvaa 240,168,232
Mvaa 244,172,232
Maaa 48,176,232
D 14,48,65,0
Maaa 52,180,232
Maaa 112,184,232
Maaa 116,188,232
W 4
Maaa 124,188,236
D 15,48,65,1024
Ta 248,26,49152
Maaa 120,184,236
Ta 250,26,50176
Maaa 60,180,236
Maaa 56,176,236
Mvaa 252,172,236
D 16,48,65,2048
Ra 192,19,32768
Mvaa 248,168,236
Mvaa 188,164,236
Mvaa 184,160,236
Ra 196,19,34816
W 6
Mvaa 192,160,240
D 17,48,65,3072
Ta 252,27,49152
Mvaa 196,164,240
Ta 254,27,50176
v_min_u32 v139, s75, v138
Maaa 0,168,240
Ra 200,19,36864
Maaa 4,172,240
Maaa 64,176,240
Maaa 68,180,240
global_load_dword v94, v139, s[76:77]
Ra 204,19,38912
Maaa 128,184,240
Maaa 132,188,240
W 8
Maaa 140,188,244
Ta 224,20,57344
Ta 226,20,58368
Maaa 136,184,244
Ra 208,19,40960
Maaa 76,180,244
v_add_u32 v138, s73, v138
Maaa 72,176,244
Ra 212,19,43008
Maaa 12,172,244
s_waitcnt vmcnt(9)
v_cmp_eq_u64 vcc, s[12:13], v[28:29]
s_cmp_eq_u64 vcc, -1
s_cbranch_scc0 .Lspin_23%=
.Lok_24%=:
Maaa 8,168,244
Mvaa 204,164,244
Ra 216,19,45056
Mvaa 200,160,244
W 11
Mvaa 208,160,248
Ta 228,21,57344
Ta 230,21,58368
Mvaa 212,164,248
Ra 220,19,47104
Maaa 16,168,248
Maaa 20,172,248
Maaa 80,176,248
Maaa 84,180,248
Maaa 144,184,248
Maaa 148,188,248
W 10
Maaa 156,188,252
Ta 232,22,57344
Ta 234,22,58368
Maaa 152,184,252
Maaa 92,180,252
Maaa 88,176,252
Maaa 28,172,252
Maaa 24,168,252
Mvaa 220,164,252
Mvaa 216,160,252
s_and_b32 s80, s14, 2
W 8
Mvaa 160,192,224
Ta 236,23,57344
Ta 238,23,58368
Mvaa 164,196,224
Mvaa 224,200,224
s_lshl_b32 s80, s80, 10
Mvaa 228,204,224
W 4
Maaa 32,208,224
v_add_u32 v18, s80, v1
Maaa 36,212,224
Maaa 96,216,224
Maaa 100,220,224
v_xor_b32 v19, 2048, v18
Maaa 108,220,228
Ta 240,24,57344
Ta 242,24,58368
Maaa 104,216,228
buffer_load_dwordx4 a[160:163], v18, s[8:11], s66 offen offset:0 sc1
Maaa 44,212,228
Maaa 40,208,228
Mvaa 236,204,228
Ta 244,25,57344
Ta 246,25,58368
Mvaa 232,200,228
buffer_load_dwordx4 a[164:167], v18, s[8:11], s66 offen offset:1024 sc1
Mvaa 172,196,228
Ta 248,26,57344
Ta 250,26,58368
Mvaa 168,192,228
W 8
Mvaa 176,192,232
Ta 252,27,57344
Ta 254,27,58368
Mvaa 180,196,232
buffer_load_dwordx4 a[168:171], v19, s[8:11], s66 offen offset:0 sc1
Mvaa 240,200,232
Mvaa 244,204,232
Maaa 48,208,232
Maaa 52,212,232
buffer_load_dwordx4 a[172:175], v19, s[8:11], s66 offen offset:1024 sc1
s_add_u32 s66, s66, 4096
Maaa 112,216,232
Maaa 116,220,232
W 8
Maaa 124,220,236
Maaa 120,216,236
buffer_load_dwordx4 a[176:179], v18, s[8:11], s66 offen offset:0 sc1
Maaa 60,212,236
s_waitcnt vmcnt(6) lgkmcnt(0)
s_barrier
s_cmp_eq_u32 s85, 43
s_cbranch_scc1 .Lrt_26%=
.Lrtr_27%=:
Maaa 56,208,236
Mvaa 252,204,236
Mvaa 248,200,236
buffer_load_dwordx4 a[180:183], v18, s[8:11], s66 offen offset:1024 sc1
Mvaa 188,196,236
Mvaa 184,192,236
Mvaa 192,192,240
Mvaa 196,196,240
buffer_load_dwordx4 a[184:187], v19, s[8:11], s66 offen offset:0 sc1
Maaa 0,200,240
s_mov_b32 s65, 3145728
Maaa 4,204,240
Maaa 64,208,240
Maaa 68,212,240
buffer_load_dwordx4 a[188:191], v19, s[8:11], s66 offen offset:1024 sc1
Maaa 128,216,240
Maaa 132,220,240
Maaa 140,220,244
Maaa 136,216,244
Maaa 76,212,244
Ta 224,2,0
Ta 226,2,1024
Maaa 72,208,244
Maaa 12,204,244
s_add_u32 m0, s63, 32768
Maaa 8,200,244
Mvaa 204,196,244
Ta 228,3,0
Ta 230,3,1024
Mvaa 200,192,244
Mvaa 208,192,248
Mvaa 212,196,248
Maaa 16,200,248
Ta 232,4,0
Ta 234,4,1024
Maaa 20,204,248
Maaa 80,208,248
Maaa 84,212,248
D 10,48,65,0
Maaa 144,216,248
Ta 236,5,0
Ta 238,5,1024
Maaa 148,220,248
Maaa 156,220,252
Maaa 152,216,252
Maaa 92,212,252
Ta 240,6,0
Ta 242,6,1024
Maaa 88,208,252
Maaa 28,204,252
Maaa 24,200,252
Mvaa 220,196,252
D 11,48,65,1024
Mvaa 216,192,252
.Lq_9%=:
s_waitcnt vmcnt(6) lgkmcnt(6)
Zvaa 32,224,160
Ta 244,7,0
Ta 246,7,1024
Zvaa 64,224,164
Ta 248,8,0
Ta 250,8,1024
Zvaa 96,224,168
s_add_u32 s66, s66, 4096
buffer_load_dwordx4 a[192:195], v18, s[8:11], s66 offen offset:0 sc1
Zvaa 128,224,172
Ta 252,9,0
Ta 254,9,1024
Zvaa 132,228,172
Zvaa 100,228,168
Zvaa 68,228,164
buffer_load_dwordx4 a[196:199], v18, s[8:11], s66 offen offset:1024 sc1
Ta 224,2,8192
Zvaa 36,228,160
Ta 226,2,9216
W 10
Zvaa 40,232,160
K 160,160,161
Zvaa 72,232,164
Zvaa 104,232,168
buffer_load_dwordx4 a[200:203], v19, s[8:11], s66 offen offset:0 sc1
Ta 228,3,8192
Zvaa 136,232,172
Ta 230,3,9216
K 161,162,163
Zvaa 140,236,172
Zvaa 108,236,168
Zvaa 76,236,164
buffer_load_dwordx4 a[204:207], v19, s[8:11], s66 offen offset:1024 sc1
s_add_u32 s66, s66, 4096
Zvaa 44,236,160
K 162,164,165
W 8
Zvaa 48,240,160
Ta 232,4,8192
Ta 234,4,9216
Zvaa 80,240,164
K 163,166,167
Zvaa 112,240,168
buffer_load_dwordx4 a[208:211], v18, s[8:11], s66 offen offset:0 sc1
Ta 236,5,8192
Zvaa 144,240,172
Ta 238,5,9216
Zvaa 148,244,172
Zvaa 116,244,168
Zvaa 84,244,164
buffer_load_dwordx4 a[212:215], v18, s[8:11], s66 offen offset:1024 sc1
Ta 240,6,8192
Zvaa 52,244,160
Ta 242,6,9216
W 10
Zvaa 56,248,160
Zvaa 88,248,164
Zvaa 120,248,168
buffer_load_dwordx4 a[216:219], v19, s[8:11], s66 offen offset:0 sc1
Ta 244,7,8192
Zvaa 152,248,172
Ta 246,7,9216
Zvaa 156,252,172
Zvaa 124,252,168
Zvaa 92,252,164
buffer_store_dwordx4 v[160:163], v1, s[8:11], s67 offen offset:0 sc1
K 168,168,169
Zvaa 60,252,160
K 169,170,171
s_waitcnt vmcnt(10) lgkmcnt(8)
Mvaa 32,224,176
Ta 248,8,8192
Ta 250,8,9216
Mvaa 64,224,180
K 170,172,173
Mvaa 96,224,184
buffer_load_dwordx4 a[220:223], v19, s[8:11], s66 offen offset:1024 sc1
s_add_u32 s66, s66, 4096
Mvaa 128,224,188
Ta 252,9,8192
Ta 254,9,9216
Mvaa 132,228,188
K 171,174,175
Mvaa 100,228,184
Mvaa 68,228,180
D 12,48,65,2048
Ta 224,2,16384
Mvaa 36,228,176
Ta 226,2,17408
W 10
Mvaa 40,232,176
Mvaa 72,232,180
Mvaa 104,232,184
D 13,48,65,3072
s_add_u32 m0, s63, 36864
Mvaa 136,232,188
Ta 228,3,16384
Ta 230,3,17408
Mvaa 140,236,188
Mvaa 108,236,184
Mvaa 76,236,180
buffer_load_dwordx4 a[160:163], v18, s[8:11], s66 offen offset:0 sc1
Ta 232,4,16384
Mvaa 44,236,176
Ta 234,4,17408
W 10
Mvaa 48,240,176
Mvaa 80,240,180
)"
R"(buffer_store_dwordx4 v[168:171], v1, s[8:11], s67 offen offset:1024 sc1
K 176,176,177
Mvaa 112,240,184
K 177,178,179
Ta 236,5,16384
Mvaa 144,240,188
Ta 238,5,17408
K 178,180,181
Mvaa 148,244,188
Mvaa 116,244,184
buffer_load_dwordx4 a[164:167], v18, s[8:11], s66 offen offset:1024 sc1
Mvaa 84,244,180
K 179,182,183
Ta 240,6,16384
Mvaa 52,244,176
Ta 242,6,17408
W 10
Mvaa 56,248,176
Mvaa 88,248,180
buffer_load_dwordx4 a[168:171], v19, s[8:11], s66 offen offset:0 sc1
Mvaa 120,248,184
Ta 244,7,16384
Ta 246,7,17408
Mvaa 152,248,188
Mvaa 156,252,188
Mvaa 124,252,184
buffer_load_dwordx4 a[172:175], v19, s[8:11], s66 offen offset:1024 sc1
Mvaa 92,252,180
Ta 248,8,16384
Ta 250,8,17408
Mvaa 60,252,176
s_add_u32 s66, s66, 4096
s_waitcnt vmcnt(12) lgkmcnt(10)
Mvaa 32,224,192
Mvaa 64,224,196
D 14,48,65,0
Mvaa 96,224,200
Ta 252,9,16384
Ta 254,9,17408
Mvaa 128,224,204
buffer_store_dwordx4 v[176:179], v1, s[8:11], s67 offen offset:2048 sc1
K 184,184,185
Mvaa 132,228,204
K 185,186,187
Mvaa 100,228,200
Mvaa 68,228,196
K 186,188,189
Ta 224,2,24576
Mvaa 36,228,192
D 15,48,65,1024
W 9
Mvaa 40,232,192
Ta 226,2,25600
Mvaa 72,232,196
K 187,190,191
Mvaa 104,232,200
Ta 228,3,24576
Ta 230,3,25600
Mvaa 136,232,204
D 16,48,65,2048
Mvaa 140,236,204
Mvaa 108,236,200
Mvaa 76,236,196
Ta 232,4,24576
Ta 234,4,25600
Mvaa 44,236,192
D 17,48,65,3072
W 10
Mvaa 48,240,192
s_mov_b32 s65, 4194304
s_add_u32 m0, s63, 98304
Mvaa 80,240,196
Mvaa 112,240,200
Ta 236,5,24576
Ta 238,5,25600
Mvaa 144,240,204
buffer_load_dwordx4 a[176:179], v18, s[8:11], s66 offen offset:0 sc1
Mvaa 148,244,204
Mvaa 116,244,200
Mvaa 84,244,196
buffer_store_dwordx4 v[184:187], v1, s[8:11], s67 offen offset:3072 sc1
K 192,192,193
Mvaa 52,244,192
K 193,194,195
W 8
Mvaa 56,248,192
Ta 240,6,24576
Ta 242,6,25600
Mvaa 88,248,196
K 194,196,197
Mvaa 120,248,200
buffer_load_dwordx4 a[180:183], v18, s[8:11], s66 offen offset:1024 sc1
Ta 244,7,24576
Mvaa 152,248,204
Ta 246,7,25600
K 195,198,199
Mvaa 156,252,204
Mvaa 124,252,200
s_add_u32 s67, s67, 4096
Mvaa 92,252,196
buffer_load_dwordx4 a[184:187], v19, s[8:11], s66 offen offset:0 sc1
Ta 248,8,24576
Mvaa 60,252,192
Ta 250,8,25600
s_waitcnt vmcnt(16) lgkmcnt(10)
Mvaa 32,224,208
Mvaa 64,224,212
Mvaa 96,224,216
buffer_load_dwordx4 a[188:191], v19, s[8:11], s66 offen offset:1024 sc1
s_add_u32 s66, s66, 4096
Mvaa 128,224,220
Ta 252,9,24576
Ta 254,9,25600
Mvaa 132,228,220
Mvaa 100,228,216
Mvaa 68,228,212
buffer_load_dwordx4 v[184:187], v18, s[8:11], s66 offen offset:0 sc1
Ta 224,20,0
Mvaa 36,228,208
Ta 226,20,1024
W 10
Mvaa 40,232,208
Mvaa 72,232,212
buffer_store_dwordx4 v[192:195], v1, s[8:11], s67 offen offset:0 sc1
K 200,200,201
Mvaa 104,232,216
K 201,202,203
Ta 228,21,0
Mvaa 136,232,220
Ta 230,21,1024
K 202,204,205
Mvaa 140,236,220
Mvaa 108,236,216
buffer_load_dwordx4 v[188:191], v18, s[8:11], s66 offen offset:1024 sc1
Mvaa 76,236,212
K 203,206,207
Ta 232,22,0
Mvaa 44,236,208
Ta 234,22,1024
W 10
Mvaa 48,240,208
Mvaa 80,240,212
D 10,48,65,0
Mvaa 112,240,216
Ta 236,23,0
Ta 238,23,1024
Mvaa 144,240,220
Mvaa 148,244,220
Mvaa 116,244,216
buffer_load_dwordx4 a[192:195], v19, s[8:11], s66 offen offset:0 sc1
Mvaa 84,244,212
Ta 240,24,0
Ta 242,24,1024
Mvaa 52,244,208
W 10
Mvaa 56,248,208
Mvaa 88,248,212
buffer_load_dwordx4 a[196:199], v19, s[8:11], s66 offen offset:1024 sc1
s_add_u32 s66, s66, 4096
Mvaa 120,248,216
Ta 244,25,0
Ta 246,25,1024
Mvaa 152,248,220
buffer_store_dwordx4 v[200:203], v1, s[8:11], s67 offen offset:1024 sc1
K 208,208,209
Mvaa 156,252,220
K 209,210,211
Mvaa 124,252,216
Mvaa 92,252,212
K 210,212,213
Ta 248,26,0
Mvaa 60,252,208
buffer_load_dwordx4 a[200:203], v18, s[8:11], s66 offen offset:0 sc1
s_waitcnt vmcnt(18) lgkmcnt(9)
Mvaa 32,224,160
Ta 250,26,1024
Mvaa 64,224,164
K 211,214,215
Mvaa 96,224,168
Ta 252,27,0
Ta 254,27,1024
Mvaa 128,224,172
buffer_load_dwordx4 a[204:207], v18, s[8:11], s66 offen offset:1024 sc1
Mvaa 132,228,172
Mvaa 100,228,168
Mvaa 68,228,164
Ta 224,20,8192
Ta 226,20,9216
Mvaa 36,228,160
D 11,48,65,1024
W 10
Mvaa 40,232,160
v_add_u32 v2, 65536, v20
Mvaa 72,232,164
Mvaa 104,232,168
Ta 228,21,8192
Ta 230,21,9216
Mvaa 136,232,172
buffer_load_dwordx4 v[192:195], v19, s[8:11], s66 offen offset:0 sc1
v_add_u32 v3, 65536, v21
Mvaa 140,236,172
Mvaa 108,236,168
Mvaa 76,236,164
buffer_store_dwordx4 v[208:211], v1, s[8:11], s67 offen offset:2048 sc1
K 216,216,217
Mvaa 44,236,160
K 217,218,219
W 8
Mvaa 48,240,160
Ta 232,22,8192
Ta 234,22,9216
Mvaa 80,240,164
v_add_u32 v4, 65536, v22
K 218,220,221
Mvaa 112,240,168
buffer_load_dwordx4 v[196:199], v19, s[8:11], s66 offen offset:1024 sc1
s_add_u32 s66, s66, 4096
Mvaa 144,240,172
Ta 236,23,8192
Ta 238,23,9216
Mvaa 148,244,172
v_add_u32 v5, 65536, v23
K 219,222,223
Mvaa 116,244,168
Mvaa 84,244,164
D 12,48,65,2048
Ta 240,24,8192
Mvaa 52,244,160
Ta 242,24,9216
W 10
Mvaa 56,248,160
v_add_u32 v6, 65536, v24
Mvaa 88,248,164
Mvaa 120,248,168
D 13,48,65,3072
s_add_u32 m0, s63, 102400
Mvaa 152,248,172
Ta 244,25,8192
Ta 246,25,9216
Mvaa 156,252,172
v_add_u32 v7, 65536, v25
Mvaa 124,252,168
Mvaa 92,252,164
buffer_load_dwordx4 v[200:203], v18, s[8:11], s66 offen offset:0 sc1
Ta 248,26,8192
Mvaa 60,252,160
Ta 250,26,9216
s_waitcnt vmcnt(16) lgkmcnt(10)
Mvaa 32,224,176
v_add_u32 v8, 65536, v26
Mvaa 64,224,180
buffer_store_dwordx4 v[216:219], v1, s[8:11], s67 offen offset:3072 sc1
K 224,224,225
Mvaa 96,224,184
K 225,226,227
K 226,228,229
Mvaa 128,224,188
Ta 252,27,8192
Ta 254,27,9216
Mvaa 132,228,188
v_add_u32 v9, 65536, v27
K 227,230,231
Mvaa 100,228,184
buffer_load_dwordx4 v[204:207], v18, s[8:11], s66 offen offset:1024 sc1
Mvaa 68,228,180
Ta 224,20,16384
Ta 226,20,17408
Mvaa 36,228,176
s_add_u32 s67, s67, s70
W 10
Mvaa 40,232,176
Mvaa 72,232,180
buffer_load_dwordx4 a[208:211], v19, s[8:11], s66 offen offset:0 sc1
Mvaa 104,232,184
Ta 228,21,16384
Ta 230,21,17408
Mvaa 136,232,188
Mvaa 140,236,188
Mvaa 108,236,184
buffer_load_dwordx4 a[212:215], v19, s[8:11], s66 offen offset:1024 sc1
s_add_u32 s66, s66, 4096
Mvaa 76,236,180
Ta 232,22,16384
Ta 234,22,17408
Mvaa 44,236,176
W 10
Mvaa 48,240,176
Mvaa 80,240,180
buffer_load_dwordx4 a[216:219], v18, s[8:11], s66 offen offset:0 sc1
Mvaa 112,240,184
Ta 236,23,16384
Ta 238,23,17408
Mvaa 144,240,188
buffer_store_dwordx4 v[224:227], v1, s[8:11], s67 offen offset:0 sc1
K 232,232,233
Mvaa 148,244,188
K 233,234,235
Mvaa 116,244,184
Mvaa 84,244,180
K 234,236,237
Ta 240,24,16384
Mvaa 52,244,176
D 14,48,65,0
W 9
Mvaa 56,248,176
Ta 242,24,17408
Mvaa 88,248,180
K 235,238,239
Mvaa 120,248,184
Ta 244,25,16384
Ta 246,25,17408
Mvaa 152,248,188
buffer_load_dwordx4 a[220:223], v18, s[8:11], s66 offen offset:1024 sc1
Mvaa 156,252,188
Mvaa 124,252,184
Mvaa 92,252,180
Ta 248,26,16384
Ta 250,26,17408
Mvaa 60,252,176
D 15,48,65,1024
s_waitcnt vmcnt(19) lgkmcnt(10)
Mvav 32,224,184
Mvav 64,224,188
Mvaa 96,224,192
Ta 252,27,16384
Ta 254,27,17408
Mvaa 128,224,196
buffer_load_dwordx4 v[208:211], v19, s[8:11], s66 offen offset:0 sc1
Mvaa 132,228,196
Mvaa 100,228,192
Mvav 68,228,188
buffer_store_dwordx4 v[232:235], v1, s[8:11], s67 offen offset:1024 sc1
K 240,240,241
Mvav 36,228,184
K 241,242,243
W 8
Mvav 40,232,184
Ta 224,20,24576
Ta 226,20,25600
Mvav 72,232,188
K 242,244,245
Mvaa 104,232,192
D 16,48,65,2048
Ta 228,21,24576
Mvaa 136,232,196
Ta 230,21,25600
K 243,246,247
Mvaa 140,236,196
Mvaa 108,236,192
Mvav 76,236,188
buffer_load_dwordx4 v[212:215], v19, s[8:11], s66 offen offset:1024 sc1
s_add_u32 s66, s66, 4096
Mvav 44,236,184
Ta 232,22,24576
W 9
Mvav 48,240,184
Ta 234,22,25600
Mvav 80,240,188
Mvaa 112,240,192
buffer_load_dwordx4 a[160:163], v18, s[8:11], s66 offen offset:0 sc1
Ta 236,23,24576
Mvaa 144,240,196
Ta 238,23,25600
Mvaa 148,244,196
Mvaa 116,244,192
Mvav 84,244,188
D 17,48,65,3072
Ta 240,24,24576
Mvav 52,244,184
Ta 242,24,25600
W 10
Mvav 56,248,184
buffer_store_dwordx4 v[240:243], v1, s[8:11], s67 offen offset:2048 sc1
K 248,248,249
Mvav 88,248,188
K 249,250,251
Mvaa 120,248,192
Ta 244,25,24576
Ta 246,25,25600
Mvaa 152,248,196
K 250,252,253
Mvaa 156,252,196
buffer_load_dwordx4 a[164:167], v18, s[8:11], s66 offen offset:1024 sc1
Mvaa 124,252,192
Mvav 92,252,188
K 251,254,255
Ta 248,26,24576
Mvav 60,252,184
Ta 250,26,25600
s_waitcnt vmcnt(20) lgkmcnt(10)
Mvaa 32,224,200
buffer_load_dwordx4 a[168:171], v19, s[8:11], s66 offen offset:0 sc1
Mvaa 64,224,204
Mvav 96,224,192
Ta 252,27,24576
Ta 254,27,25600
Mvav 128,224,196
Mvav 132,228,196
buffer_load_dwordx4 a[172:175], v19, s[8:11], s66 offen offset:1024 sc1
s_add_u32 s66, s66, 4096
)"
R"(Mvav 100,228,192
Mvaa 68,228,204
Ta 224,2,0
Ta 226,2,1024
Mvaa 36,228,200
W 10
Mvaa 40,232,200
buffer_load_dwordx4 v[216:219], v18, s[8:11], s66 offen offset:0 sc1
Mvaa 72,232,204
Mvav 104,232,192
Ta 228,3,0
Ta 230,3,1024
Mvav 136,232,196
buffer_store_dwordx4 v[248:251], v1, s[8:11], s67 offen offset:3072 sc1
G 160,0
Mvav 140,236,196
G 161,1
G 162,2
Mvav 108,236,192
G 163,3
G 164,4
Mvaa 76,236,204
G 165,5
Ta 232,4,0
Mvaa 44,236,200
buffer_load_dwordx4 v[220:223], v18, s[8:11], s66 offen offset:1024 sc1
W 9
Mvaa 48,240,200
Ta 234,4,1024
G 166,6
Mvaa 80,240,204
G 167,7
K 160,160,161
Mvav 112,240,192
K 161,162,163
Ta 236,5,0
Mvav 144,240,196
buffer_load_dwordx4 v[224:227], v19, s[8:11], s66 offen offset:0 sc1
Ta 238,5,1024
Mvav 148,244,196
K 162,164,165
K 163,166,167
Mvav 116,244,192
s_add_u32 s67, s67, 4096
Mvaa 84,244,204
Ta 240,6,0
Ta 242,6,1024
Mvaa 52,244,200
buffer_load_dwordx4 v[228:231], v19, s[8:11], s66 offen offset:1024 sc1
W 10
Mvaa 56,248,200
s_add_u32 s66, s66, 4096
Mvaa 88,248,204
Mvav 120,248,192
Ta 244,7,0
Ta 246,7,1024
Mvav 152,248,196
buffer_load_dwordx4 a[176:179], v18, s[8:11], s66 offen offset:0 sc1
Mvav 156,252,196
Mvav 124,252,192
Mvaa 92,252,204
buffer_store_dwordx4 v[160:163], v1, s[8:11], s67 offen offset:0 sc1
G 168,8
Mvaa 60,252,200
G 169,9
s_waitcnt vmcnt(22) lgkmcnt(8)
Mvav 32,224,200
G 170,10
G 171,11
Mvav 64,224,204
Ta 248,8,0
Ta 250,8,1024
Mvaa 96,224,208
buffer_load_dwordx4 a[180:183], v18, s[8:11], s66 offen offset:1024 sc1
G 172,12
Mvaa 128,224,212
G 173,13
G 174,14
Mvaa 132,228,212
s_lshl_b32 s80, s56, 9
G 175,15
Mvaa 100,228,208
s_lshl_b32 s81, s57, 5
Ta 252,9,0
Mvav 68,228,204
buffer_load_dwordx4 a[184:187], v19, s[8:11], s66 offen offset:0 sc1
Ta 254,9,1024
Mvav 36,228,200
s_add_u32 s80, s80, s81
W 8
Mvav 40,232,200
K 168,168,169
s_lshl_b32 s81, s14, 3
Mvav 72,232,204
K 169,170,171
s_add_u32 s80, s80, s81
Mvaa 104,232,208
buffer_load_dwordx4 a[188:191], v19, s[8:11], s66 offen offset:1024 sc1
s_add_u32 s66, s66, 4096
Mvaa 136,232,212
s_add_u32 s71, s80, 4096
K 170,172,173
Mvaa 140,236,212
K 171,174,175
Ta 224,2,8192
Mvaa 108,236,208
Ta 226,2,9216
Ta 228,3,8192
Mvav 76,236,204
buffer_load_dwordx4 v[232:235], v18, s[8:11], s66 offen offset:0 sc1
Ta 230,3,9216
Mvav 44,236,200
Ta 232,4,8192
W 9
Mvav 48,240,200
buffer_store_dwordx4 v[168:171], v1, s[8:11], s67 offen offset:1024 sc1
G 176,16
Mvav 80,240,204
Ta 234,4,9216
G 177,17
Mvaa 112,240,208
G 178,18
G 179,19
Mvaa 144,240,212
G 180,20
Ta 236,5,8192
Mvaa 148,244,212
v_mov_b64 v[28:29], s[12:13]
s_mov_b64 exec, 1
buffer_store_dwordx2 v[28:29], off, s[8:11], s71 sc1
s_mov_b64 exec, -1
Mvaa 116,244,208
s_lshr_b32 s80, s15, 3
Ta 238,5,9216
Mvav 84,244,204
G 181,21
G 182,22
Mvav 52,244,200
s_lshl_b32 s80, s80, 2
W 8
Mvav 56,248,200
buffer_load_dwordx4 v[236:239], v18, s[8:11], s66 offen offset:1024 sc1
G 183,23
Mvav 88,248,204
K 176,176,177
Ta 240,6,8192
Mvaa 120,248,208
Ta 242,6,9216
s_add_u32 s80, s80, s14
Mvaa 152,248,212
K 177,178,179
K 178,180,181
Mvaa 156,252,212
buffer_load_dwordx4 v[240:243], v19, s[8:11], s66 offen offset:0 sc1
s_mul_i32 s80, s80, 1536
Mvaa 124,252,208
K 179,182,183
Ta 244,7,8192
Mvav 92,252,204
Ta 246,7,9216
v_min_u32 v28, 176, v1
Mvav 60,252,200
v_lshl_add_u32 v28, v28, 3, s80
s_waitcnt vmcnt(21) lgkmcnt(8)
Mvaa 32,224,216
buffer_load_dwordx4 v[244:247], v19, s[8:11], s66 offen offset:1024 sc1
Ta 248,8,8192
Mvaa 64,224,220
Ta 250,8,9216
s_add_u32 s66, s66, 4096
Mvav 96,224,208
v_min_u32 v28, .Lend%=-.Lbeg%=-4, v28
Ta 252,9,8192
Mvav 128,224,212
buffer_store_dwordx4 v[176:179], v1, s[8:11], s67 offen offset:2048 sc1
G 160,24
Mvav 132,228,212
G 161,25
Ta 254,9,9216
Mvav 100,228,208
G 162,26
G 163,27
Mvaa 68,228,220
G 164,28
G 165,29
Mvaa 36,228,216
buffer_load_dwordx4 v[184:187], v18, s[8:11], s66 offen offset:0 sc1
W 8
Mvaa 40,232,216
Ta 224,2,16384
Ta 226,2,17408
Mvaa 72,232,220
G 166,30
G 167,31
Mvav 104,232,208
K 160,160,161
K 161,162,163
Mvav 136,232,212
global_load_dword v29, v28, s[6:7]
Ta 228,3,16384
Mvav 140,236,212
Ta 230,3,17408
K 162,164,165
Mvav 108,236,208
K 163,166,167
Mvaa 76,236,220
Ta 232,4,16384
Ta 234,4,17408
Mvaa 44,236,216
buffer_load_dwordx4 v[188:191], v18, s[8:11], s66 offen offset:1024 sc1
W 10
Mvaa 48,240,216
Mvaa 80,240,220
Mvav 112,240,208
Ta 236,5,16384
Ta 238,5,17408
Mvav 144,240,212
buffer_store_dwordx4 v[160:163], v1, s[8:11], s67 offen offset:3072 sc1
G 168,32
Mvav 148,244,212
G 169,33
G 170,34
Mvav 116,244,208
G 171,35
Mvaa 84,244,220
Ta 240,6,16384
Ta 242,6,17408
Mvaa 52,244,216
buffer_load_dwordx4 a[192:195], v19, s[8:11], s66 offen offset:0 sc1
W 10
Mvaa 56,248,216
G 172,36
G 173,37
Mvaa 88,248,220
G 174,38
G 175,39
Mvav 120,248,208
Ta 244,7,16384
Ta 246,7,17408
Mvav 152,248,212
buffer_load_dwordx4 a[196:199], v19, s[8:11], s66 offen offset:1024 sc1
K 168,168,169
Mvav 156,252,212
K 169,170,171
K 170,172,173
Mvav 124,252,208
K 171,174,175
Mvaa 92,252,220
Ta 248,8,16384
Ta 250,8,17408
Mvaa 60,252,216
s_add_u32 s67, s67, s70
s_waitcnt vmcnt(23) lgkmcnt(10)
Mvaa 32,224,160
buffer_store_dwordx4 v[168:171], v1, s[8:11], s67 offen offset:0 sc1
s_add_u32 s66, s66, 4096
Mvaa 64,224,164
Mvaa 96,224,168
Ta 252,9,16384
Ta 254,9,17408
Mvaa 128,224,172
Mvaa 132,228,172
buffer_load_dwordx4 v[248:251], v18, s[8:11], s66 offen offset:0 sc1
Mvaa 100,228,168
G 176,40
Mvaa 68,228,164
Ta 224,2,24576
Ta 226,2,25600
Mvaa 36,228,160
G 177,41
W 10
Mvaa 40,232,160
G 178,42
buffer_load_dwordx4 v[252:255], v18, s[8:11], s66 offen offset:1024 sc1
Mvaa 72,232,164
G 179,43
Mvaa 104,232,168
G 180,44
Ta 228,3,24576
Mvaa 136,232,172
Ta 230,3,25600
G 181,45
Mvaa 140,236,172
G 182,46
Mvaa 108,236,168
G 183,47
Mvaa 76,236,164
K 176,176,177
Ta 232,4,24576
Mvaa 44,236,160
Ta 234,4,25600
W 10
Mvaa 48,240,160
K 177,178,179
K 178,180,181
Mvaa 80,240,164
buffer_load_dwordx4 a[0:3], v19, s[8:11], s66 offen offset:0 sc1
K 179,182,183
Mvaa 112,240,168
Ta 236,5,24576
Ta 238,5,25600
Mvaa 144,240,172
Mvaa 148,244,172
Mvaa 116,244,168
buffer_store_dwordx4 v[176:179], v1, s[8:11], s67 offen offset:1024 sc1
Mvaa 84,244,164
Ta 240,6,24576
Ta 242,6,25600
Mvaa 52,244,160
G 160,48
W 10
Mvaa 56,248,160
Mvaa 88,248,164
buffer_load_dwordx4 a[4:7], v19, s[8:11], s66 offen offset:1024 sc1
G 161,49
Mvaa 120,248,168
G 162,50
Ta 244,7,24576
Mvaa 152,248,172
Ta 246,7,25600
G 163,51
Mvaa 156,252,172
G 164,52
Mvaa 124,252,168
G 165,53
Mvaa 92,252,164
G 166,54
Ta 248,8,24576
Mvaa 60,252,160
Ta 250,8,25600
s_waitcnt vmcnt(24) lgkmcnt(10)
Mvav 32,224,216
G 167,55
K 160,160,161
Mvav 64,224,220
s_add_u32 s66, s66, 4096
buffer_load_dwordx4 a[200:203], v18, s[8:11], s66 offen offset:0 sc1
Mvav 96,224,224
K 161,162,163
K 162,164,165
Mvav 128,224,228
Ta 252,9,24576
Ta 254,9,25600
Mvav 132,228,228
K 163,166,167
Mvav 100,228,224
buffer_store_dwordx4 v[160:163], v1, s[8:11], s67 offen offset:2048 sc1
Mvav 68,228,220
Mvav 36,228,216
W 8
Mvav 40,232,216
Mvav 72,232,220
buffer_load_dwordx4 a[204:207], v18, s[8:11], s66 offen offset:1024 sc1
Mvav 104,232,224
G 168,56
Mvav 136,232,228
Mvav 140,236,228
W 0
s_barrier
Mvav 108,236,224
G 169,57
Ta 224,20,32768
Mvav 76,236,220
Ta 226,20,33792
Ta 228,21,32768
Mvav 44,236,216
Ta 230,21,33792
G 170,58
Mvav 48,240,216
G 171,59
Ta 232,22,32768
Mvav 80,240,220
Ta 234,22,33792
G 172,60
Mvav 112,240,224
G 173,61
buffer_load_dwordx4 v[192:195], v19, s[8:11], s66 offen offset:0 sc1
Mvav 144,240,228
G 174,62
G 175,63
Mvav 148,244,228
Ta 236,23,32768
Ta 238,23,33792
Mvav 116,244,224
K 168,168,169
K 169,170,171
Mvav 84,244,220
K 170,172,173
K 171,174,175
Mvav 52,244,216
Ta 240,24,32768
Ta 242,24,33792
Mvav 56,248,216
buffer_store_dwordx4 v[168:171], v1, s[8:11], s67 offen offset:3072 sc1
Mvav 88,248,220
Mvav 120,248,224
Ta 244,25,32768
Ta 246,25,33792
Mvav 152,248,228
Mvav 156,252,228
)"
R"(buffer_load_dwordx4 v[196:199], v19, s[8:11], s66 offen offset:1024 sc1
Mvav 124,252,224
G 176,64
Mvav 92,252,220
Ta 248,26,32768
Ta 250,26,33792
Mvav 60,252,216
G 177,65
s_waitcnt vmcnt(25) lgkmcnt(10)
Mvaa 32,224,176
G 178,66
s_add_u32 s66, s66, 4096
Mvaa 64,224,180
buffer_load_dwordx4 a[8:11], v18, s[8:11], s66 offen offset:0 sc1
G 179,67
Mvaa 96,224,184
G 180,68
Ta 252,27,32768
Mvaa 128,224,188
Ta 254,27,33792
G 181,69
Mvaa 132,228,188
G 182,70
G 183,71
Mvaa 100,228,184
K 176,176,177
Mvaa 68,228,180
Ta 224,20,40960
Ta 226,20,41984
Mvaa 36,228,176
K 177,178,179
W 10
Mvaa 40,232,176
K 178,180,181
buffer_load_dwordx4 a[12:15], v18, s[8:11], s66 offen offset:1024 sc1
Mvaa 72,232,180
K 179,182,183
s_add_u32 s67, s67, 4096
Mvaa 104,232,184
Ta 228,21,40960
Ta 230,21,41984
Mvaa 136,232,188
Mvaa 140,236,188
buffer_store_dwordx4 v[176:179], v1, s[8:11], s67 offen offset:0 sc1
Mvaa 108,236,184
Mvaa 76,236,180
Ta 232,22,40960
Ta 234,22,41984
Mvaa 44,236,176
G 160,72
W 10
Mvaa 48,240,176
buffer_load_dwordx4 v[200:203], v19, s[8:11], s66 offen offset:0 sc1
Mvaa 80,240,180
G 161,73
Mvaa 112,240,184
G 162,74
Ta 236,23,40960
Mvaa 144,240,188
Ta 238,23,41984
G 163,75
Mvaa 148,244,188
G 164,76
Mvaa 116,244,184
G 165,77
Mvaa 84,244,180
G 166,78
Ta 240,24,40960
Mvaa 52,244,176
Ta 242,24,41984
W 10
Mvaa 56,248,176
G 167,79
K 160,160,161
Mvaa 88,248,180
buffer_load_dwordx4 v[204:207], v19, s[8:11], s66 offen offset:1024 sc1
K 161,162,163
Mvaa 120,248,184
K 162,164,165
Ta 244,25,40960
Mvaa 152,248,188
Ta 246,25,41984
v_bfe_u32 v28, v1, 4, 5
Mvaa 156,252,188
K 163,166,167
v_lshlrev_b32 v28, 3, v28
Mvaa 124,252,184
buffer_store_dwordx4 v[160:163], v1, s[8:11], s67 offen offset:1024 sc1
s_lshl_b32 s80, s56, 9
Mvaa 92,252,180
s_add_u32 s80, s80, 4096
Ta 248,26,40960
Mvaa 60,252,176
Ta 250,26,41984
s_waitcnt vmcnt(25) lgkmcnt(10)
Mvav 32,224,232
s_add_u32 s66, s66, 4096
Mvav 64,224,236
buffer_load_dwordx2 v[28:29], v28, s[8:11], s80 offen sc1
Mvav 96,224,240
Ta 252,27,40960
Ta 254,27,41984
Mvav 128,224,244
G 168,80
Mvav 132,228,244
G 169,81
Mvav 100,228,240
buffer_load_dwordx4 a[208:211], v18, s[8:11], s66 offen offset:0 sc1
G 170,82
Mvav 68,228,236
G 171,83
Ta 224,20,49152
Mvav 36,228,232
Ta 226,20,50176
W 10
Mvav 40,232,232
G 172,84
G 173,85
Mvav 72,232,236
buffer_load_dwordx4 a[212:215], v18, s[8:11], s66 offen offset:1024 sc1
G 174,86
Mvav 104,232,240
G 175,87
Ta 228,21,49152
Mvav 136,232,244
Ta 230,21,50176
K 168,168,169
Mvav 140,236,244
K 169,170,171
Mvav 108,236,240
K 170,172,173
Mvav 76,236,236
K 171,174,175
Ta 232,22,49152
Mvav 44,236,232
Ta 234,22,50176
W 10
Mvav 48,240,232
s_and_b32 s81, s14, 1
buffer_store_dwordx4 v[168:171], v1, s[8:11], s67 offen offset:2048 sc1
Mvav 80,240,236
s_mul_i32 s91, s81, 16902144
Mvav 112,240,240
Ta 236,23,49152
Ta 238,23,50176
Mvav 144,240,244
s_mul_i32 s82, s56, 528384
Mvav 148,244,244
buffer_load_dwordx4 a[16:19], v19, s[8:11], s66 offen offset:0 sc1
Mvav 116,244,240
G 176,88
Mvav 84,244,236
s_add_u32 s91, s91, s82
Ta 240,24,49152
Mvav 52,244,232
Ta 242,24,50176
W 10
Mvav 56,248,232
G 177,89
G 178,90
Mvav 88,248,236
buffer_load_dwordx4 a[20:23], v19, s[8:11], s66 offen offset:1024 sc1
s_add_u32 s91, s91, 4194304
Mvav 120,248,240
G 179,91
G 180,92
Mvav 152,248,244
Ta 244,25,49152
Ta 246,25,50176
Mvav 156,252,244
G 181,93
s_lshr_b32 s82, s14, 1
Mvav 124,252,240
G 182,94
G 183,95
Mvav 92,252,236
s_lshl_b32 s83, s82, 14
K 176,176,177
Mvav 60,252,232
Ta 248,26,49152
s_waitcnt vmcnt(24) lgkmcnt(9)
Mvav 32,224,184
Ta 250,26,50176
K 177,178,179
Mvav 64,224,188
s_add_u32 s91, s91, s83
K 178,180,181
Mvaa 96,224,192
s_add_u32 s66, s66, 4096
buffer_load_dwordx4 a[24:27], v18, s[8:11], s66 offen offset:0 sc1
Mvaa 128,224,196
K 179,182,183
Ta 252,27,49152
Mvaa 132,228,196
Ta 254,27,50176
s_lshl_b32 s81, s81, 14
Mvaa 100,228,192
s_lshl_b32 s82, s82, 16
Mvav 68,228,188
buffer_store_dwordx4 v[176:179], v1, s[8:11], s67 offen offset:3072 sc1
Ta 224,20,57344
Mvav 36,228,184
Ta 226,20,58368
W 10
Mvav 40,232,184
G 160,96
s_or_b32 s92, s81, s82
Mvav 72,232,188
G 161,97
G 162,98
Mvaa 104,232,192
buffer_load_dwordx4 a[28:31], v18, s[8:11], s66 offen offset:1024 sc1
s_xor_b32 s92, s92, 0x10000
Mvaa 136,232,196
Ta 228,21,57344
Ta 230,21,58368
Mvaa 140,236,196
G 163,99
G 164,100
Mvaa 108,236,192
G 165,101
G 166,102
Mvav 76,236,188
Ta 232,22,57344
Ta 234,22,58368
Mvav 44,236,184
G 167,103
W 10
Mvav 48,240,184
K 160,160,161
buffer_load_dwordx4 a[216:219], v19, s[8:11], s66 offen offset:0 sc1
Mvav 80,240,188
K 161,162,163
K 162,164,165
Mvaa 112,240,192
Ta 236,23,57344
Ta 238,23,58368
Mvaa 144,240,196
K 163,166,167
s_add_u32 s67, s67, s70
Mvaa 148,244,196
buffer_store_dwordx4 v[160:163], v1, s[8:11], s67 offen offset:0 sc1
Mvaa 116,244,192
Mvav 84,244,188
Ta 240,24,57344
Ta 242,24,58368
Mvav 52,244,184
W 10
Mvav 56,248,184
s_waitcnt vmcnt(10)
v_cmp_eq_u64 vcc, s[12:13], v[28:29]
s_cmp_eq_u64 vcc, -1
s_cbranch_scc0 .Lspin_30%=
.Lok_31%=:
Mvav 88,248,188
s_add_u32 m0, s92, 0
buffer_load_dwordx4 a[220:223], v19, s[8:11], s66 offen offset:1024 sc1
Mvaa 120,248,192
Ta 244,25,57344
Ta 246,25,58368
Mvaa 152,248,196
G 168,104
s_add_u32 s81, s91, 0
Mvaa 156,252,196
G 169,105
Mvaa 124,252,192
G 170,106
Mvav 92,252,188
G 171,107
Ta 248,26,57344
Mvav 60,252,184
Ta 250,26,58368
W 10
Mvav 32,224,248
G 172,108
Dc 1,8,81,0
Mvav 64,224,252
G 173,109
G 174,110
Mvaa 96,224,0
G 175,111
Ta 252,27,57344
Mvaa 128,224,4
Ta 254,27,58368
K 168,168,169
Mvaa 132,228,4
Dc 1,8,81,1024
K 169,170,171
Mvaa 100,228,0
K 170,172,173
Mvav 68,228,252
K 171,174,175
Ta 224,2,32768
Mvav 36,228,248
Ta 226,2,33792
W 10
Mvav 40,232,248
Dc 1,8,81,2048
Mvav 72,232,252
Mvaa 104,232,0
Ta 228,3,32768
Ta 230,3,33792
Mvaa 136,232,4
Mvaa 140,236,4
buffer_store_dwordx4 v[168:171], v1, s[8:11], s67 offen offset:1024 sc1
G 176,112
Mvaa 108,236,0
Mvav 76,236,252
G 177,113
Ta 232,4,32768
Mvav 44,236,248
Ta 234,4,33792
W 10
Mvav 48,240,248
Dc 1,8,81,3072
s_add_u32 m0, s92, 4096
Mvav 80,240,252
G 178,114
G 179,115
Mvaa 112,240,0
G 180,116
s_add_u32 s81, s91, 4096
Mvaa 144,240,4
G 181,117
Ta 236,5,32768
Mvaa 148,244,4
Ta 238,5,33792
G 182,118
Mvaa 116,244,0
G 183,119
K 176,176,177
Mvav 84,244,252
Dc 1,8,81,0
K 177,178,179
Mvav 52,244,248
Ta 240,6,32768
W 9
Mvav 56,248,248
Ta 242,6,33792
K 178,180,181
Mvav 88,248,252
K 179,182,183
Mvaa 120,248,0
Dc 1,8,81,1024
Ta 244,7,32768
Mvaa 152,248,4
Ta 246,7,33792
Mvaa 156,252,4
Mvaa 124,252,0
Mvav 92,252,252
buffer_store_dwordx4 v[176:179], v1, s[8:11], s67 offen offset:2048 sc1
Ta 248,8,32768
Mvav 60,252,248
Ta 250,8,33792
W 10
Mvaa 32,224,200
G 160,120
Mvaa 64,224,204
G 161,121
Mvav 96,224,192
Dc 1,8,81,2048
G 162,122
Mvav 128,224,196
Ta 252,9,32768
Ta 254,9,33792
Mvav 132,228,196
G 163,123
G 164,124
Mvav 100,228,192
G 165,125
Mvaa 68,228,204
Dc 1,8,81,3072
s_add_u32 m0, s92, 8192
Mvaa 36,228,200
G 166,126
W 8
Mvaa 40,232,200
Ta 224,2,40960
Ta 226,2,41984
Mvaa 72,232,204
G 167,127
s_add_u32 s81, s91, 8192
Mvav 104,232,192
K 160,160,161
K 161,162,163
Mvav 136,232,196
K 162,164,165
Ta 228,3,40960
Mvav 140,236,196
Ta 230,3,41984
K 163,166,167
Mvav 108,236,192
Dc 1,8,81,0
Mvaa 76,236,204
Ta 232,4,40960
Ta 234,4,41984
Mvaa 44,236,200
W 10
Mvaa 48,240,200
Mvaa 80,240,204
)"
R"(buffer_store_dwordx4 v[160:163], v1, s[8:11], s67 offen offset:3072 sc1
Mvav 112,240,192
Ta 236,5,40960
Ta 238,5,41984
Mvav 144,240,196
G 168,128
G 169,129
Mvav 148,244,196
G 170,130
Mvav 116,244,192
Dc 1,8,81,1024
G 171,131
Mvaa 84,244,204
Ta 240,6,40960
Ta 242,6,41984
Mvaa 52,244,200
G 172,132
W 10
Mvaa 56,248,200
G 173,133
G 174,134
Mvaa 88,248,204
Dc 1,8,81,2048
G 175,135
Mvav 120,248,192
Ta 244,7,40960
Ta 246,7,41984
Mvav 152,248,196
K 168,168,169
K 169,170,171
Mvav 156,252,196
K 170,172,173
Mvav 124,252,192
Dc 1,8,81,3072
s_add_u32 m0, s92, 12288
Mvaa 92,252,204
K 171,174,175
s_add_u32 s81, s91, 12288
Mvaa 60,252,200
Ta 248,8,40960
W 9
Mvaa 32,224,8
Ta 250,8,41984
s_add_u32 s67, s67, 4096
Mvaa 64,224,12
buffer_store_dwordx4 v[168:171], v1, s[8:11], s67 offen offset:0 sc1
Mvav 96,224,200
Ta 252,9,40960
Ta 254,9,41984
Mvav 128,224,204
Mvav 132,228,204
G 176,136
Mvav 100,228,200
Dc 1,8,81,0
Mvaa 68,228,12
G 177,137
Ta 224,2,49152
Mvaa 36,228,8
Ta 226,2,50176
W 10
Mvaa 40,232,8
G 178,138
G 179,139
Mvaa 72,232,12
Dc 1,8,81,1024
G 180,140
Mvav 104,232,200
G 181,141
Ta 228,3,49152
Mvav 136,232,204
Ta 230,3,50176
G 182,142
Mvav 140,236,204
G 183,143
Mvav 108,236,200
Dc 1,8,81,2048
K 176,176,177
Mvaa 76,236,12
K 177,178,179
Ta 232,4,49152
Mvaa 44,236,8
Ta 234,4,50176
W 10
Mvaa 48,240,8
K 178,180,181
K 179,182,183
Mvaa 80,240,12
Dc 1,8,81,3072
Mvav 112,240,200
Ta 236,5,49152
Ta 238,5,50176
Mvav 144,240,204
Mvav 148,244,204
Mvav 116,244,200
buffer_store_dwordx4 v[176:179], v1, s[8:11], s67 offen offset:1024 sc1
Mvaa 84,244,12
Ta 240,6,49152
Ta 242,6,50176
Mvaa 52,244,8
G 160,144
W 10
Mvaa 56,248,8
Mvaa 88,248,12
G 161,145
Mvav 120,248,200
G 162,146
Ta 244,7,49152
Mvav 152,248,204
Ta 246,7,50176
G 163,147
Mvav 156,252,204
G 164,148
Mvav 124,252,200
G 165,149
Mvaa 92,252,12
G 166,150
Ta 248,8,49152
Mvaa 60,252,8
Ta 250,8,50176
s_waitcnt vmcnt(27) lgkmcnt(10)
Mvaa 32,224,208
G 167,151
K 160,160,161
Mvaa 64,224,212
K 161,162,163
Mvaa 96,224,16
K 162,164,165
Ta 252,9,49152
Mvaa 128,224,20
Ta 254,9,50176
K 163,166,167
Mvaa 132,228,20
buffer_store_dwordx4 v[160:163], v1, s[8:11], s67 offen offset:2048 sc1
Mvaa 100,228,16
Mvaa 68,228,212
Ta 224,2,57344
Ta 226,2,58368
Mvaa 36,228,208
W 10
Mvaa 40,232,208
Mvaa 72,232,212
Mvaa 104,232,16
Ta 228,3,57344
Ta 230,3,58368
Mvaa 136,232,20
G 168,152
Mvaa 140,236,20
G 169,153
Mvaa 108,236,16
G 170,154
Mvaa 76,236,212
G 171,155
Ta 232,4,57344
Mvaa 44,236,208
Ta 234,4,58368
W 10
Mvaa 48,240,208
G 172,156
G 173,157
Mvaa 80,240,212
G 174,158
Mvaa 112,240,16
G 175,159
Ta 236,5,57344
Mvaa 144,240,20
Ta 238,5,58368
K 168,168,169
Mvaa 148,244,20
K 169,170,171
Mvaa 116,244,16
K 170,172,173
Mvaa 84,244,212
K 171,174,175
Ta 240,6,57344
Mvaa 52,244,208
Ta 242,6,58368
W 10
Mvaa 56,248,208
buffer_store_dwordx4 v[168:171], v1, s[8:11], s67 offen offset:3072 sc1
Mvaa 88,248,212
Mvaa 120,248,16
Ta 244,7,57344
Ta 246,7,58368
Mvaa 152,248,20
Mvaa 156,252,20
Mvaa 124,252,16
Mvaa 92,252,212
Ta 248,8,57344
Ta 250,8,58368
Mvaa 60,252,208
s_waitcnt vmcnt(23) lgkmcnt(10)
Mvaa 32,224,24
Mvaa 64,224,28
Mvaa 96,224,216
Ta 252,9,57344
Ta 254,9,58368
Mvaa 128,224,220
Mvaa 132,228,220
Mvaa 100,228,216
Mvaa 68,228,28
Mvaa 36,228,24
W 8
Mvaa 40,232,24
Mvaa 72,232,28
Mvaa 104,232,216
Mvaa 136,232,220
Mvaa 140,236,220
Mvaa 108,236,216
Mvaa 76,236,28
Mvaa 44,236,24
W 4
Mvaa 48,240,24
Mvaa 80,240,28
Mvaa 112,240,216
Mvaa 144,240,220
Mvaa 148,244,220
Mvaa 116,244,216
Mvaa 84,244,28
Mvaa 52,244,24
W 0
Mvaa 56,248,24
Mvaa 88,248,28
Mvaa 120,248,216
Mvaa 152,248,220
Mvaa 156,252,220
Mvaa 124,252,216
Mvaa 92,252,28
Mvaa 60,252,24
v_and_b32 v28, 16, v1
v_cmp_eq_u32 vcc, 0, v28
v_mov_b32 v240, 0xeeeeeeee
v_mov_b32 v29, 0x44444444
v_cndmask_b32 v240, v240, v29, vcc
v_mov_b32 v241, 0xff800000
v_add_u32 v242, 0x10000, v1
v_add_u32 v243, 4096, v1
v_add_u32 v244, 8192, v1
v_add_u32 v245, 12288, v1
v_bfe_u32 v28, v1, 4, 4
v_lshrrev_b32 v29, 8, v1
v_lshlrev_b32 v29, 2, v29
v_sub_u32 v28, v28, v29
v_cmp_le_i32_e64 s[72:73], 0, v28
v_cmp_le_i32_e64 s[74:75], 1, v28
v_cmp_le_i32_e64 s[76:77], 2, v28
v_cmp_le_i32_e64 s[78:79], 3, v28
s_mov_b32 s80, 0x3f803f80
s_mov_b32 s81, 0x3f003f00
v_accvgpr_write_b32 a176, s80
v_accvgpr_write_b32 a180, s81
v_accvgpr_write_b32 a177, s80
v_accvgpr_write_b32 a181, s81
v_accvgpr_write_b32 a178, s80
v_accvgpr_write_b32 a182, s81
v_accvgpr_write_b32 a179, s80
v_accvgpr_write_b32 a183, s81
v_accvgpr_write_b32 a160, 0
v_accvgpr_write_b32 a161, 0
v_accvgpr_write_b32 a162, 0
v_accvgpr_write_b32 a163, 0
v_accvgpr_write_b32 a164, 0
v_accvgpr_write_b32 a165, 0
v_accvgpr_write_b32 a166, 0
v_accvgpr_write_b32 a167, 0
v_accvgpr_write_b32 a168, 0
v_accvgpr_write_b32 a169, 0
v_accvgpr_write_b32 a170, 0
v_accvgpr_write_b32 a171, 0
v_accvgpr_write_b32 a172, 0
v_accvgpr_write_b32 a173, 0
v_accvgpr_write_b32 a174, 0
v_accvgpr_write_b32 a175, 0
s_mov_b32 s90, 0x5d800000
s_mov_b64 s[88:89], 0
s_mov_b32 s64, 0
s_and_b32 s80, s14, 1
s_lshl_b32 s62, s80, 14
s_lshl_b32 s82, s57, 15
s_sub_u32 s82, 507904, s82
s_add_u32 s82, s82, s62
s_lshl_b32 s65, s57, 1
s_sub_u32 s65, s65, 1
s_max_i32 s65, s65, 0
s_sub_u32 s66, s60, 1
s_cmp_lt_u32 s14, 2
s_cselect_b32 s82, s82, 16384
s_cselect_b32 s66, s66, 15
s_cselect_b32 s83, 32768, 0
s_add_u32 s61, s91, s82
s_add_u32 s62, s62, s83
s_sub_u32 s63, 16384, s83
s_lshl_b32 s67, s60, 7
s_add_u32 s67, s67, s56
s_lshl_b32 s67, s67, 12
s_add_u32 s67, s67, 39845888
s_lshl_b32 s69, s60, 3
s_add_u32 s69, s69, s59
s_lshl_b32 s69, s69, 6
s_lshl_b32 s83, s58, 3
s_add_u32 s69, s69, s83
s_add_u32 s69, s69, 20480
s_mov_b32 s70, 0
s_mov_b32 s91, 0
v_mov_b32 v252, v241
v_mov_b32 v253, v241
v_mov_b32 v254, v241
v_mov_b32 v255, v241
s_sub_u32 s80, s57, 4
s_lshl_b32 s80, s80, 2
s_add_u32 s80, s80, s58
s_mul_i32 s85, s59, 7
s_add_u32 s85, s85, s80
s_cmp_lt_u32 s80, 7
s_cselect_b32 s85, s85, 99
s_cmp_lt_u32 s85, 54
s_cselect_b32 s65, 1, s65
s_mov_b32 s68, 4102
s_mov_b64 s[24:25], -1
s_cmp_ge_u32 s14, 2
s_cbranch_scc1 .Lpb_33%=
v_mul_f32 v32, 0x3e0293ee, v32
v_mul_f32 v33, 0x3e0293ee, v33
v_mul_f32 v34, 0x3e0293ee, v34
v_mul_f32 v35, 0x3e0293ee, v35
v_mul_f32 v36, 0x3e0293ee, v36
v_mul_f32 v37, 0x3e0293ee, v37
v_mul_f32 v38, 0x3e0293ee, v38
v_mul_f32 v39, 0x3e0293ee, v39
K 32,32,33
K 33,34,35
K 34,36,37
K 35,38,39
v_accvgpr_write_b32 a0, v32
v_accvgpr_write_b32 a1, v33
v_accvgpr_write_b32 a2, v34
v_accvgpr_write_b32 a3, v35
v_mul_f32 v64, 0x3e0293ee, v64
v_mul_f32 v65, 0x3e0293ee, v65
v_mul_f32 v66, 0x3e0293ee, v66
v_mul_f32 v67, 0x3e0293ee, v67
v_mul_f32 v68, 0x3e0293ee, v68
v_mul_f32 v69, 0x3e0293ee, v69
v_mul_f32 v70, 0x3e0293ee, v70
v_mul_f32 v71, 0x3e0293ee, v71
K 64,64,65
K 65,66,67
K 66,68,69
K 67,70,71
v_accvgpr_write_b32 a16, v64
v_accvgpr_write_b32 a17, v65
v_accvgpr_write_b32 a18, v66
v_accvgpr_write_b32 a19, v67
v_mul_f32 v96, 0x3e0293ee, v96
v_mul_f32 v97, 0x3e0293ee, v97
v_mul_f32 v98, 0x3e0293ee, v98
v_mul_f32 v99, 0x3e0293ee, v99
v_mul_f32 v100, 0x3e0293ee, v100
v_mul_f32 v101, 0x3e0293ee, v101
v_mul_f32 v102, 0x3e0293ee, v102
v_mul_f32 v103, 0x3e0293ee, v103
K 96,96,97
K 97,98,99
K 98,100,101
K 99,102,103
v_accvgpr_write_b32 a32, v96
v_accvgpr_write_b32 a33, v97
v_accvgpr_write_b32 a34, v98
v_accvgpr_write_b32 a35, v99
v_mul_f32 v128, 0x3e0293ee, v128
v_mul_f32 v129, 0x3e0293ee, v129
v_mul_f32 v130, 0x3e0293ee, v130
v_mul_f32 v131, 0x3e0293ee, v131
v_mul_f32 v132, 0x3e0293ee, v132
v_mul_f32 v133, 0x3e0293ee, v133
v_mul_f32 v134, 0x3e0293ee, v134
v_mul_f32 v135, 0x3e0293ee, v135
K 128,128,129
K 129,130,131
K 130,132,133
K 131,134,135
v_accvgpr_write_b32 a48, v128
v_accvgpr_write_b32 a49, v129
v_accvgpr_write_b32 a50, v130
v_accvgpr_write_b32 a51, v131
v_mul_f32 v40, 0x3e0293ee, v40
v_mul_f32 v41, 0x3e0293ee, v41
v_mul_f32 v42, 0x3e0293ee, v42
v_mul_f32 v43, 0x3e0293ee, v43
v_mul_f32 v44, 0x3e0293ee, v44
v_mul_f32 v45, 0x3e0293ee, v45
v_mul_f32 v46, 0x3e0293ee, v46
v_mul_f32 v47, 0x3e0293ee, v47
K 40,40,41
K 41,42,43
K 42,44,45
K 43,46,47
v_accvgpr_write_b32 a4, v40
v_accvgpr_write_b32 a5, v41
v_accvgpr_write_b32 a6, v42
v_accvgpr_write_b32 a7, v43
)"
R"(v_mul_f32 v72, 0x3e0293ee, v72
v_mul_f32 v73, 0x3e0293ee, v73
v_mul_f32 v74, 0x3e0293ee, v74
v_mul_f32 v75, 0x3e0293ee, v75
v_mul_f32 v76, 0x3e0293ee, v76
v_mul_f32 v77, 0x3e0293ee, v77
v_mul_f32 v78, 0x3e0293ee, v78
v_mul_f32 v79, 0x3e0293ee, v79
K 72,72,73
K 73,74,75
K 74,76,77
K 75,78,79
v_accvgpr_write_b32 a20, v72
v_accvgpr_write_b32 a21, v73
v_accvgpr_write_b32 a22, v74
v_accvgpr_write_b32 a23, v75
v_mul_f32 v104, 0x3e0293ee, v104
v_mul_f32 v105, 0x3e0293ee, v105
v_mul_f32 v106, 0x3e0293ee, v106
v_mul_f32 v107, 0x3e0293ee, v107
v_mul_f32 v108, 0x3e0293ee, v108
v_mul_f32 v109, 0x3e0293ee, v109
v_mul_f32 v110, 0x3e0293ee, v110
v_mul_f32 v111, 0x3e0293ee, v111
K 104,104,105
K 105,106,107
K 106,108,109
K 107,110,111
v_accvgpr_write_b32 a36, v104
v_accvgpr_write_b32 a37, v105
v_accvgpr_write_b32 a38, v106
v_accvgpr_write_b32 a39, v107
v_mul_f32 v136, 0x3e0293ee, v136
v_mul_f32 v137, 0x3e0293ee, v137
v_mul_f32 v138, 0x3e0293ee, v138
v_mul_f32 v139, 0x3e0293ee, v139
v_mul_f32 v140, 0x3e0293ee, v140
v_mul_f32 v141, 0x3e0293ee, v141
v_mul_f32 v142, 0x3e0293ee, v142
v_mul_f32 v143, 0x3e0293ee, v143
K 136,136,137
K 137,138,139
K 138,140,141
K 139,142,143
v_accvgpr_write_b32 a52, v136
v_accvgpr_write_b32 a53, v137
v_accvgpr_write_b32 a54, v138
v_accvgpr_write_b32 a55, v139
v_mul_f32 v48, 0x3e0293ee, v48
v_mul_f32 v49, 0x3e0293ee, v49
v_mul_f32 v50, 0x3e0293ee, v50
v_mul_f32 v51, 0x3e0293ee, v51
v_mul_f32 v52, 0x3e0293ee, v52
v_mul_f32 v53, 0x3e0293ee, v53
v_mul_f32 v54, 0x3e0293ee, v54
v_mul_f32 v55, 0x3e0293ee, v55
K 48,48,49
K 49,50,51
K 50,52,53
K 51,54,55
v_accvgpr_write_b32 a8, v48
v_accvgpr_write_b32 a9, v49
v_accvgpr_write_b32 a10, v50
v_accvgpr_write_b32 a11, v51
v_mul_f32 v80, 0x3e0293ee, v80
v_mul_f32 v81, 0x3e0293ee, v81
v_mul_f32 v82, 0x3e0293ee, v82
v_mul_f32 v83, 0x3e0293ee, v83
v_mul_f32 v84, 0x3e0293ee, v84
v_mul_f32 v85, 0x3e0293ee, v85
v_mul_f32 v86, 0x3e0293ee, v86
v_mul_f32 v87, 0x3e0293ee, v87
K 80,80,81
K 81,82,83
K 82,84,85
K 83,86,87
v_accvgpr_write_b32 a24, v80
v_accvgpr_write_b32 a25, v81
v_accvgpr_write_b32 a26, v82
v_accvgpr_write_b32 a27, v83
v_mul_f32 v112, 0x3e0293ee, v112
v_mul_f32 v113, 0x3e0293ee, v113
v_mul_f32 v114, 0x3e0293ee, v114
v_mul_f32 v115, 0x3e0293ee, v115
v_mul_f32 v116, 0x3e0293ee, v116
v_mul_f32 v117, 0x3e0293ee, v117
v_mul_f32 v118, 0x3e0293ee, v118
v_mul_f32 v119, 0x3e0293ee, v119
K 112,112,113
K 113,114,115
K 114,116,117
K 115,118,119
v_accvgpr_write_b32 a40, v112
v_accvgpr_write_b32 a41, v113
v_accvgpr_write_b32 a42, v114
v_accvgpr_write_b32 a43, v115
v_mul_f32 v144, 0x3e0293ee, v144
v_mul_f32 v145, 0x3e0293ee, v145
v_mul_f32 v146, 0x3e0293ee, v146
v_mul_f32 v147, 0x3e0293ee, v147
v_mul_f32 v148, 0x3e0293ee, v148
v_mul_f32 v149, 0x3e0293ee, v149
v_mul_f32 v150, 0x3e0293ee, v150
v_mul_f32 v151, 0x3e0293ee, v151
K 144,144,145
K 145,146,147
K 146,148,149
K 147,150,151
v_accvgpr_write_b32 a56, v144
v_accvgpr_write_b32 a57, v145
v_accvgpr_write_b32 a58, v146
v_accvgpr_write_b32 a59, v147
v_mul_f32 v56, 0x3e0293ee, v56
v_mul_f32 v57, 0x3e0293ee, v57
v_mul_f32 v58, 0x3e0293ee, v58
v_mul_f32 v59, 0x3e0293ee, v59
v_mul_f32 v60, 0x3e0293ee, v60
v_mul_f32 v61, 0x3e0293ee, v61
v_mul_f32 v62, 0x3e0293ee, v62
v_mul_f32 v63, 0x3e0293ee, v63
K 56,56,57
K 57,58,59
K 58,60,61
K 59,62,63
v_accvgpr_write_b32 a12, v56
v_accvgpr_write_b32 a13, v57
v_accvgpr_write_b32 a14, v58
v_accvgpr_write_b32 a15, v59
v_mul_f32 v88, 0x3e0293ee, v88
v_mul_f32 v89, 0x3e0293ee, v89
v_mul_f32 v90, 0x3e0293ee, v90
v_mul_f32 v91, 0x3e0293ee, v91
v_mul_f32 v92, 0x3e0293ee, v92
v_mul_f32 v93, 0x3e0293ee, v93
v_mul_f32 v94, 0x3e0293ee, v94
v_mul_f32 v95, 0x3e0293ee, v95
K 88,88,89
K 89,90,91
K 90,92,93
K 91,94,95
v_accvgpr_write_b32 a28, v88
v_accvgpr_write_b32 a29, v89
v_accvgpr_write_b32 a30, v90
v_accvgpr_write_b32 a31, v91
v_mul_f32 v120, 0x3e0293ee, v120
v_mul_f32 v121, 0x3e0293ee, v121
v_mul_f32 v122, 0x3e0293ee, v122
v_mul_f32 v123, 0x3e0293ee, v123
v_mul_f32 v124, 0x3e0293ee, v124
v_mul_f32 v125, 0x3e0293ee, v125
v_mul_f32 v126, 0x3e0293ee, v126
v_mul_f32 v127, 0x3e0293ee, v127
K 120,120,121
K 121,122,123
K 122,124,125
K 123,126,127
v_accvgpr_write_b32 a44, v120
v_accvgpr_write_b32 a45, v121
v_accvgpr_write_b32 a46, v122
v_accvgpr_write_b32 a47, v123
v_mul_f32 v152, 0x3e0293ee, v152
v_mul_f32 v153, 0x3e0293ee, v153
v_mul_f32 v154, 0x3e0293ee, v154
v_mul_f32 v155, 0x3e0293ee, v155
v_mul_f32 v156, 0x3e0293ee, v156
v_mul_f32 v157, 0x3e0293ee, v157
v_mul_f32 v158, 0x3e0293ee, v158
v_mul_f32 v159, 0x3e0293ee, v159
K 152,152,153
K 153,154,155
K 154,156,157
K 155,158,159
v_accvgpr_write_b32 a60, v152
v_accvgpr_write_b32 a61, v153
v_accvgpr_write_b32 a62, v154
v_accvgpr_write_b32 a63, v155
s_waitcnt vmcnt(0)
s_barrier
s_add_u32 s71, s71, 256
v_mov_b64 v[28:29], s[12:13]
s_mov_b64 exec, 1
buffer_store_dwordx2 v[28:29], off, s[8:11], s71 sc1
s_mov_b64 exec, -1
.Lago%=:
s_cmp_eq_u32 s57, 0
s_cbranch_scc0 .LL_49%=
s_add_u32 m0, s62, 0
s_nop 0
Dc 1,8,61,0
Dc 1,8,61,1024
Dc 1,8,61,2048
Dc 1,8,61,3072
s_add_u32 m0, s62, 4096
s_nop 0
Dc 243,8,61,0
Dc 243,8,61,1024
Dc 243,8,61,2048
Dc 243,8,61,3072
s_add_u32 m0, s62, 8192
s_nop 0
Dc 244,8,61,0
Dc 244,8,61,1024
Dc 244,8,61,2048
Dc 244,8,61,3072
s_add_u32 m0, s62, 12288
s_nop 0
Dc 245,8,61,0
Dc 245,8,61,1024
Dc 245,8,61,2048
Dc 245,8,61,3072
s_add_u32 s61, s61, s63
.LL_49%=:
s_cmp_eq_u32 s60, 0
s_cbranch_scc1 .Lpd_34%=
Ra 128,242,0
Ra 132,242,1024
Ra 136,242,2048
Ra 140,242,3072
W 0
Zvaa 128,128,0
Ra 144,242,4096
Ra 148,242,5120
Ra 152,242,6144
Ra 156,242,7168
Zvaa 132,128,16
s_and_b32 s80, s14, 1
v_lshrrev_b32 v20, 4, v1
v_add_u32 v20, 0, v20
v_mul_u32_u24 v26, 205, v20
Zvaa 136,128,32
s_lshl_b32 s80, s80, 14
v_lshrrev_b32 v26, 12, v26
v_mul_u32_u24 v25, 20, v26
v_sub_u32 v20, v20, v25
Zvaa 140,128,48
s_add_u32 s80, s80, 131072
v_lshrrev_b32 v25, 1, v26
v_and_b32 v25, 2, v25
v_xor_b32 v20, v20, v25
Mvaa 128,132,4
v_add_u32 v12, s80, v1
v_mul_u32_u24 v26, 80, v26
v_add_lshl_u32 v16, v20, v26, 4
v_lshrrev_b32 v20, 4, v1
Mvaa 132,132,20
Ra 64,12,8192
v_add_u32 v20, 64, v20
v_mul_u32_u24 v26, 205, v20
v_lshrrev_b32 v26, 12, v26
Mvaa 136,132,36
Ra 128,242,8192
Ra 68,12,9216
v_mul_u32_u24 v25, 20, v26
v_sub_u32 v20, v20, v25
Mvaa 140,132,52
v_lshrrev_b32 v25, 1, v26
Ra 72,12,10240
v_and_b32 v25, 2, v25
v_xor_b32 v20, v20, v25
Mvaa 128,136,8
v_mul_u32_u24 v26, 80, v26
Ra 76,12,11264
v_add_lshl_u32 v17, v20, v26, 4
v_subrev_u32 v17, 1024, v17
Mvaa 132,136,24
v_lshrrev_b32 v20, 4, v1
Ra 80,12,12288
v_add_u32 v20, 128, v20
v_mul_u32_u24 v26, 205, v20
Mvaa 136,136,40
v_lshrrev_b32 v26, 12, v26
Ra 84,12,13312
v_mul_u32_u24 v25, 20, v26
v_sub_u32 v20, v20, v25
Mvaa 140,136,56
v_lshrrev_b32 v25, 1, v26
Ra 132,242,9216
Ra 88,12,14336
v_and_b32 v25, 2, v25
Mvaa 128,140,12
v_xor_b32 v20, v20, v25
v_mul_u32_u24 v26, 80, v26
Ra 92,12,15360
v_add_lshl_u32 v18, v20, v26, 4
Mvaa 132,140,28
v_subrev_u32 v18, 2048, v18
v_lshrrev_b32 v20, 4, v1
Ra 96,12,0
v_add_u32 v20, 192, v20
Mvaa 136,140,44
v_mul_u32_u24 v26, 205, v20
v_lshrrev_b32 v26, 12, v26
Ra 100,12,1024
v_mul_u32_u24 v25, 20, v26
Mvaa 140,140,60
v_sub_u32 v20, v20, v25
v_lshrrev_b32 v25, 1, v26
Ra 104,12,2048
W 13
Zvaa 144,144,0
v_and_b32 v25, 2, v25
v_xor_b32 v20, v20, v25
v_mul_u32_u24 v26, 80, v26
Ra 136,242,10240
Zvaa 148,144,16
Ra 108,12,3072
v_add_lshl_u32 v19, v20, v26, 4
v_subrev_u32 v19, 3072, v19
v_lshrrev_b32 v20, 4, v1
Zvaa 152,144,32
Ra 112,12,4096
v_add_u32 v20, 256, v20
v_mul_u32_u24 v26, 205, v20
v_lshrrev_b32 v26, 12, v26
Zvaa 156,144,48
Ra 116,12,5120
v_mul_u32_u24 v25, 20, v26
v_sub_u32 v20, v20, v25
v_lshrrev_b32 v25, 1, v26
Mvaa 144,148,4
Ra 120,12,6144
v_and_b32 v25, 2, v25
v_xor_b32 v20, v20, v25
v_mul_u32_u24 v26, 80, v26
Mvaa 148,148,20
Ra 124,12,7168
v_add_lshl_u32 v20, v20, v26, 4
v_lshrrev_b32 v26, 6, v1
v_mul_u32_u24 v26, 320, v26
Mvaa 152,148,36
Ra 140,242,11264
v_bfe_u32 v25, v1, 4, 2
v_lshl_add_u32 v26, v25, 3, v26
v_bfe_u32 v25, v1, 8, 1
Mvaa 156,148,52
v_lshlrev_b32 v25, 5, v25
v_max3_f32 v4, v128, v129, v130
v_max3_f32 v4, v4, v131, v252
v_max3_f32 v5, v132, v133, v134
Mvaa 144,152,8
v_max3_f32 v5, v5, v135, v253
v_max3_f32 v6, v136, v137, v138
v_max3_f32 v6, v6, v139, v254
v_max3_f32 v7, v140, v141, v142
Mvaa 148,152,24
v_max3_f32 v7, v7, v143, v255
v_mov_b32 v8, v4
v_mov_b32 v9, v5
v_mov_b32 v10, v6
Mvaa 152,152,40
v_mov_b32 v11, v7
v_permlane32_swap_b32 v4, v8
v_permlane32_swap_b32 v5, v9
v_permlane32_swap_b32 v6, v10
Mvaa 156,152,56
v_permlane32_swap_b32 v7, v11
v_max_f32 v4, v4, v8
v_max_f32 v5, v5, v9
v_max_f32 v6, v6, v10
Mvaa 144,156,12
v_max_f32 v7, v7, v11
v_mov_b32 v8, v4
v_mov_b32 v9, v5
v_mov_b32 v10, v6
Mvaa 148,156,28
v_mov_b32 v11, v7
v_permlane16_swap_b32 v4, v8
v_permlane16_swap_b32 v5, v9
v_permlane16_swap_b32 v6, v10
Mvaa 152,156,44
v_permlane16_swap_b32 v7, v11
v_max_f32 v4, v4, v8
v_max_f32 v5, v5, v9
v_max_f32 v6, v6, v10
v_max_f32 v7, v7, v11
v_sub_f32 v4, 0, v4
v_sub_f32 v5, 0, v5
v_sub_f32 v6, 0, v6
v_sub_f32 v7, 0, v7
v_mov_b32 v224, v4
v_mov_b32 v225, v4
v_mov_b32 v226, v4
v_mov_b32 v227, v4
v_mov_b32 v228, v5
v_mov_b32 v229, v5
v_mov_b32 v230, v5
v_mov_b32 v231, v5
v_mov_b32 v232, v6
v_mov_b32 v233, v6
v_mov_b32 v234, v6
v_mov_b32 v235, v6
v_mov_b32 v236, v7
v_mov_b32 v237, v7
v_mov_b32 v238, v7
v_mov_b32 v239, v7
Mvaa 156,156,60
v_add_u32 v21, v26, v25
v_sub_u32 v22, v26, v25
v_add_u32 v23, 61440, v21
W 0
)"
R"(Cvaa 160,128,0,224
v_add_u32 v24, 61440, v22
v_add_u32 v25, 122880, v21
v_add_u32 v26, 122880, v22
s_mul_i32 s94, s59, 655360
Cvaa 164,128,16,228
s_mul_i32 s95, s58, 320
s_add_u32 s94, s94, s95
s_add_u32 s40, s26, s94
s_addc_u32 s41, s27, 0
Cvaa 168,128,32,232
Ra 144,242,12288
s_and_b32 s41, s41, 0xffff
s_mov_b32 s42, 5242880
s_mov_b32 s43, 0x20000
Cvaa 172,128,48,236
s_sub_u32 s42, s42, s94
s_mov_b64 s[44:45], s[8:9]
s_mov_b32 s47, 0x20000
s_mul_i32 s48, s14, 20480
Mvaa 160,132,4
s_and_b32 s94, s14, 1
s_lshl_b32 s95, s57, 1
s_add_u32 s94, s94, s95
s_lshl_b32 s54, s94, 3
Mvaa 164,132,20
s_add_u32 s54, s54, s59
s_lshl_b32 s54, s54, 6
s_xor_b32 s55, s54, 15872
s_add_u32 s54, s54, 20480
Mvaa 168,132,36
Ra 148,242,13312
s_add_u32 s55, s55, 20480
s_and_b32 s95, s14, 2
s_lshl_b32 s94, s94, 2
Mvaa 172,132,52
s_add_u32 s94, s94, s95
s_lshl_b32 s95, s95, 4
s_add_u32 s55, s55, s95
s_lshl_b32 s49, s94, 17
Mvaa 160,136,8
s_lshl_b32 s95, s59, 14
s_add_u32 s49, s49, s95
s_xor_b32 s50, s49, 16252928
s_add_u32 s49, s49, 39845888
Mvaa 164,136,24
s_add_u32 s50, s50, 39845888
s_lshr_b32 s94, s15, 3
s_lshl_b32 s94, s94, 3
s_add_u32 s94, s94, s14
Mvaa 168,136,40
Ra 152,242,14336
Ra 156,242,15360
s_lshl_b32 s94, s94, 4
s_add_u32 s94, s94, s59
Mvaa 172,136,56
s_mul_i32 s52, s94, 5120
s_add_u32 s52, s52, 56623104
s_lshl_b32 s53, s94, 3
s_add_u32 s53, s53, 65536
Mvaa 160,140,12
s_xor_b32 s62, s62, 0x10000
A 128,128,224
A 129,129,224
A 130,130,224
Mvaa 164,140,28
s_and_b32 s80, s14, 1
A 131,131,224
X 128
Mvaa 168,140,44
X 129
s_lshl_b32 s80, s80, 14
s_add_u32 s80, s80, 98304
Mvaa 172,140,60
X 130
K 192,128,129
W 0
Cvaa 176,144,0,224
X 131
A 132,132,228
K 193,130,131
Cvaa 180,144,16,228
A 133,133,228
A 134,134,228
Ra 128,242,24576
v_add_u32 v8, s80, v1
Cvaa 184,144,32,232
A 135,135,228
X 132
Ra 132,242,25600
Cvaa 188,144,48,236
X 133
X 134
Mvaa 176,148,4
K 200,132,133
X 135
A 136,136,232
Mvaa 180,148,20
K 201,134,135
A 137,137,232
A 138,138,232
A 139,139,232
Mvaa 184,148,36
X 136
X 137
Mvaa 188,148,52
X 138
Ra 136,242,26624
K 208,136,137
Mvaa 176,152,8
X 139
A 140,140,236
K 209,138,139
Mvaa 180,152,24
A 141,141,236
A 142,142,236
A 143,143,236
Ra 140,242,27648
Mvaa 184,152,40
X 140
X 141
Mvaa 188,152,56
X 142
K 216,140,141
Ra 144,242,28672
Mvaa 176,156,12
X 143
A 144,144,224
K 217,142,143
Mvaa 180,156,28
A 145,145,224
A 146,146,224
A 147,147,224
Ra 148,242,29696
Mvaa 184,156,44
X 144
X 145
X 146
K 194,144,145
X 147
A 148,148,228
K 195,146,147
A 149,149,228
A 150,150,228
A 151,151,228
X 148
X 149
X 150
K 202,148,149
X 151
A 152,152,232
K 203,150,151
A 153,153,232
A 154,154,232
A 155,155,232
X 152
X 153
X 154
K 210,152,153
X 155
A 156,156,236
K 211,154,155
A 157,157,236
A 158,158,236
A 159,159,236
X 156
X 157
X 158
K 218,156,157
X 159
X 172
K 219,158,159
Mvaa 188,156,60
Ra 152,242,30720
W 3
Zvav 32,128,192
Ra 156,242,31744
Zvav 64,128,200
Zvav 96,128,208
Zvav 128,128,216
Zvav 132,132,216
Zvav 100,132,208
Zvav 68,132,200
Zvav 36,132,192
Zvav 40,136,192
Zvav 72,136,200
X 173
Zvav 104,136,208
X 174
K 220,172,173
Zvav 136,136,216
X 175
Zvav 140,140,216
X 168
K 221,174,175
Zvav 108,140,208
X 169
X 170
Zvav 76,140,200
s_waitcnt vmcnt(0) lgkmcnt(0)
s_barrier
v_xor_b32 v242, 0x10000, v242
ds_read_b128 v[248:251], v8
Zvav 44,140,192
K 212,168,169
X 171
Ra 128,242,0
Zvav 48,144,192
X 164
K 213,170,171
Zvav 80,144,200
X 165
X 166
Zvav 112,144,208
K 204,164,165
X 167
Ra 132,242,1024
Zvav 144,144,216
X 160
Zvav 148,148,216
K 205,166,167
X 161
Zvav 116,148,208
X 162
Ra 136,242,2048
Zvav 84,148,200
K 196,160,161
X 163
W 3
Zvav 52,148,192
X 176
K 197,162,163
Zvav 56,152,192
X 177
Ra 140,242,3072
Zvav 88,152,200
X 178
K 198,176,177
Zvav 120,152,208
X 179
X 180
Zvav 152,152,216
K 199,178,179
Zvav 156,156,216
X 181
X 182
Zvav 124,156,208
K 206,180,181
X 183
Zvav 92,156,200
K 207,182,183
Zvav 60,156,192
.Ll0t_48%=:
s_cmp_eq_u32 s64, s65
s_cbranch_scc1 .Ll1t_37%=
.Ll0_35%=:
W 0
Cvaa 160,128,0,224
Ra 144,242,4096
Ra 148,242,5120
Cvaa 164,128,16,228
Ra 152,242,6144
s_xor_b32 s62, s62, 0x10000
Cvaa 168,128,32,232
X 184
Cvaa 172,128,48,236
X 185
Mvaa 160,132,4
X 186
Mvaa 164,132,20
K 214,184,185
Ra 156,242,7168
Mvaa 168,132,36
X 187
Mvaa 172,132,52
X 188
Mvaa 160,136,8
Ra 128,242,16384
Mvaa 164,136,24
K 215,186,187
Mvaa 168,136,40
X 189
Mvaa 172,136,56
X 190
Mvaa 160,140,12
Ra 132,242,17408
Mvaa 164,140,28
K 222,188,189
Mvaa 168,140,44
X 191
Mvaa 172,140,60
K 223,190,191
v_smfmac_f32_16x16x64_bf16 a[160:163], a[176:179], v[192:199], v240
Ra 136,242,18432
v_smfmac_f32_16x16x64_bf16 a[164:167], a[176:179], v[200:207], v240
X 160
v_smfmac_f32_16x16x64_bf16 a[168:171], a[176:179], v[208:215], v240
X 161
v_smfmac_f32_16x16x64_bf16 a[172:175], a[176:179], v[216:223], v240
W 3
Cvaa 176,144,0,224
X 162
Cvaa 180,144,16,228
Ra 140,242,19456
K 192,160,161
Cvaa 184,144,32,232
X 163
Cvaa 188,144,48,236
X 164
Mvaa 176,148,4
Ra 144,242,20480
Mvaa 180,148,20
K 193,162,163
Mvaa 184,148,36
X 165
Mvaa 188,148,52
X 166
Mvaa 176,152,8
Ra 148,242,21504
Mvaa 180,152,24
K 200,164,165
Mvaa 184,152,40
X 167
Mvaa 188,152,56
X 168
Mvaa 176,156,12
Ra 152,242,22528
Mvaa 180,156,28
K 201,166,167
Mvaa 184,156,44
X 169
Mvaa 188,156,60
W 3
Mvav 32,128,196
X 170
Mvav 64,128,204
Ra 156,242,23552
K 208,168,169
Mvav 96,128,212
X 171
Mvav 128,128,220
Mvav 132,132,220
X 172
Mvav 100,132,212
Ra 128,242,8192
s_add_u32 s64, s64, 1
Mvav 68,132,204
K 209,170,171
Mvav 36,132,196
X 173
Mvav 40,136,196
X 174
Mvav 72,136,204
Ra 132,242,9216
K 216,172,173
Mvav 104,136,212
X 175
Mvav 136,136,220
Mvav 140,140,220
X 176
Mvav 108,140,212
Ra 136,242,10240
K 217,174,175
Mvav 76,140,204
X 177
Mvav 44,140,196
W 3
Mvav 48,144,196
X 178
Mvav 80,144,204
Ra 140,242,11264
Mvav 112,144,212
K 194,176,177
Mvav 144,144,220
X 179
Mvav 148,148,220
X 180
Mvav 116,148,212
Ra 144,242,12288
Mvav 84,148,204
K 195,178,179
Mvav 52,148,196
X 181
Mvav 56,152,196
X 182
Mvav 88,152,204
Ra 148,242,13312
Mvav 120,152,212
K 202,180,181
Mvav 152,152,220
X 183
Mvav 156,156,220
X 184
Mvav 124,156,212
Ra 152,242,14336
Mvav 92,156,204
K 203,182,183
Mvav 60,156,196
W 3
Cvaa 160,128,0,224
)"
R"(X 185
Cvaa 164,128,16,228
X 186
Cvaa 168,128,32,232
Ra 156,242,15360
K 210,184,185
Cvaa 172,128,48,236
X 187
Mvaa 160,132,4
X 188
Mvaa 164,132,20
Ra 128,242,24576
Mvaa 168,132,36
K 211,186,187
Mvaa 172,132,52
X 189
Mvaa 160,136,8
X 190
Mvaa 164,136,24
Ra 132,242,25600
Mvaa 168,136,40
K 218,188,189
Mvaa 172,136,56
X 191
Mvaa 160,140,12
Ra 136,242,26624
Mvaa 164,140,28
Mvaa 168,140,44
Mvaa 172,140,60
W 3
Cvaa 176,144,0,224
Ra 140,242,27648
Cvaa 180,144,16,228
X 160
Cvaa 184,144,32,232
K 219,190,191
Cvaa 188,144,48,236
X 161
Mvaa 176,148,4
X 162
Mvaa 180,148,20
K 196,160,161
Ra 144,242,28672
Mvaa 184,148,36
X 163
Mvaa 188,148,52
X 164
Mvaa 176,152,8
K 197,162,163
Ra 148,242,29696
Mvaa 180,152,24
X 165
Mvaa 184,152,40
X 166
Mvaa 188,152,56
K 204,164,165
Mvaa 176,156,12
X 167
Mvaa 180,156,28
Ra 152,242,30720
Mvaa 184,156,44
X 168
Mvaa 188,156,60
K 205,166,167
W 3
Mvav 32,128,192
X 169
Mvav 64,128,200
Ra 156,242,31744
Mvav 96,128,208
X 170
Mvav 128,128,216
K 212,168,169
Mvav 132,132,216
X 171
Mvav 100,132,208
X 172
Mvav 68,132,200
Mvav 36,132,192
K 213,170,171
Mvav 40,136,192
X 173
Mvav 72,136,200
X 174
Mvav 104,136,208
Mvav 136,136,216
K 220,172,173
Mvav 140,140,216
X 175
Mvav 108,140,208
X 176
Mvav 76,140,200
W 0
s_barrier
v_xor_b32 v242, 0x10000, v242
s_bitcmp1_b32 s68, s64
s_cbranch_scc1 .Lact_50%=
.Lactr_51%=:
Mvav 44,140,192
K 221,174,175
Ra 128,242,0
Mvav 48,144,192
X 177
Mvav 80,144,200
X 178
Mvav 112,144,208
Ra 132,242,1024
Mvav 144,144,216
K 198,176,177
Mvav 148,148,216
X 179
Mvav 116,148,208
X 180
Mvav 84,148,200
Ra 136,242,2048
Mvav 52,148,192
K 199,178,179
Mvav 56,152,192
X 181
Mvav 88,152,200
Ra 140,242,3072
Mvav 120,152,208
X 182
Mvav 152,152,216
K 206,180,181
Mvav 156,156,216
X 183
Mvav 124,156,208
Mvav 92,156,200
K 207,182,183
Mvav 60,156,192
s_cmp_lg_u32 s64, s65
s_cbranch_scc1 .Ll0_35%=
s_cmp_lt_u32 s85, 54
s_cbranch_scc1 .Lmoe_46%=
.Ll1t_37%=:
s_cmp_eq_u32 s64, s66
s_cbranch_scc1 .Lsp1_38%=
.Ll1_36%=:
W 0
Cvaa 160,128,0,224
Ra 144,242,4096
Ra 148,242,5120
Cvaa 164,128,16,228
Ra 152,242,6144
s_add_u32 m0, s62, 0
Cvaa 168,128,32,232
X 184
Cvaa 172,128,48,236
X 185
Mvaa 160,132,4
X 186
Mvaa 164,132,20
K 214,184,185
Ra 156,242,7168
Mvaa 168,132,36
Dc 1,8,61,0
Mvaa 172,132,52
X 187
Mvaa 160,136,8
X 188
Mvaa 164,136,24
Ra 128,242,16384
K 215,186,187
Mvaa 168,136,40
Dc 1,8,61,1024
Mvaa 172,136,56
X 189
Mvaa 160,140,12
X 190
Mvaa 164,140,28
Ra 132,242,17408
K 222,188,189
Mvaa 168,140,44
Dc 1,8,61,2048
Mvaa 172,140,60
X 191
v_smfmac_f32_16x16x64_bf16 a[160:163], a[176:179], v[192:199], v240
K 223,190,191
Ra 136,242,18432
v_smfmac_f32_16x16x64_bf16 a[164:167], a[176:179], v[200:207], v240
X 160
v_smfmac_f32_16x16x64_bf16 a[168:171], a[176:179], v[208:215], v240
Dc 1,8,61,3072
v_smfmac_f32_16x16x64_bf16 a[172:175], a[176:179], v[216:223], v240
s_add_u32 m0, s62, 4096
W 3
Cvaa 176,144,0,224
X 161
Cvaa 180,144,16,228
X 162
Cvaa 184,144,32,232
Ra 140,242,19456
K 192,160,161
Cvaa 188,144,48,236
X 163
Mvaa 176,148,4
Dc 243,8,61,0
Ra 144,242,20480
Mvaa 180,148,20
X 164
Mvaa 184,148,36
K 193,162,163
Mvaa 188,148,52
X 165
Mvaa 176,152,8
Dc 243,8,61,1024
Ra 148,242,21504
Mvaa 180,152,24
X 166
Mvaa 184,152,40
K 200,164,165
Mvaa 188,152,56
X 167
Mvaa 176,156,12
Dc 243,8,61,2048
Ra 152,242,22528
Mvaa 180,156,28
X 168
Mvaa 184,156,44
K 201,166,167
Mvaa 188,156,60
W 3
Mvav 32,128,196
Dc 243,8,61,3072
s_add_u32 m0, s62, 8192
Mvav 64,128,204
X 169
Mvav 96,128,212
X 170
Mvav 128,128,220
Ra 156,242,23552
K 208,168,169
Mvav 132,132,220
X 171
Mvav 100,132,212
Dc 244,8,61,0
Ra 128,242,8192
Mvav 68,132,204
X 172
Mvav 36,132,196
K 209,170,171
Mvav 40,136,196
X 173
Mvav 72,136,204
Dc 244,8,61,1024
Ra 132,242,9216
Mvav 104,136,212
X 174
Mvav 136,136,220
K 216,172,173
Mvav 140,140,220
X 175
Mvav 108,140,212
Dc 244,8,61,2048
Ra 136,242,10240
Mvav 76,140,204
X 176
Mvav 44,140,196
K 217,174,175
W 3
Mvav 48,144,196
X 177
Mvav 80,144,204
Dc 244,8,61,3072
Ra 140,242,11264
Mvav 112,144,212
X 178
Mvav 144,144,220
s_add_u32 m0, s62, 12288
K 194,176,177
Mvav 148,148,220
X 179
Mvav 116,148,212
X 180
Mvav 84,148,204
Dc 245,8,61,0
Ra 144,242,12288
Mvav 52,148,196
K 195,178,179
Mvav 56,152,196
X 181
Mvav 88,152,204
X 182
Mvav 120,152,212
Dc 245,8,61,1024
Ra 148,242,13312
Mvav 152,152,220
K 202,180,181
Mvav 156,156,220
X 183
Mvav 124,156,212
X 184
Mvav 92,156,204
Dc 245,8,61,2048
Ra 152,242,14336
Mvav 60,156,196
K 203,182,183
W 3
Cvaa 160,128,0,224
X 185
Cvaa 164,128,16,228
X 186
Cvaa 168,128,32,232
Dc 245,8,61,3072
Ra 156,242,15360
Cvaa 172,128,48,236
K 210,184,185
s_add_u32 s61, s61, s63
Mvaa 160,132,4
X 187
Mvaa 164,132,20
X 188
Mvaa 168,132,36
Ra 128,242,24576
s_xor_b32 s62, s62, 0x10000
Mvaa 172,132,52
K 211,186,187
Mvaa 160,136,8
X 189
Mvaa 164,136,24
X 190
Mvaa 168,136,40
Ra 132,242,25600
s_add_u32 s64, s64, 1
Mvaa 172,136,56
K 218,188,189
Mvaa 160,140,12
X 191
Mvaa 164,140,28
Ra 136,242,26624
Mvaa 168,140,44
Mvaa 172,140,60
W 3
Cvaa 176,144,0,224
Ra 140,242,27648
Cvaa 180,144,16,228
X 160
Cvaa 184,144,32,232
K 219,190,191
Cvaa 188,144,48,236
X 161
Mvaa 176,148,4
X 162
Mvaa 180,148,20
K 196,160,161
Ra 144,242,28672
Mvaa 184,148,36
X 163
Mvaa 188,148,52
X 164
Mvaa 176,152,8
K 197,162,163
Ra 148,242,29696
Mvaa 180,152,24
X 165
Mvaa 184,152,40
X 166
Mvaa 188,152,56
K 204,164,165
Mvaa 176,156,12
X 167
Mvaa 180,156,28
Ra 152,242,30720
Mvaa 184,156,44
X 168
Mvaa 188,156,60
K 205,166,167
W 3
Mvav 32,128,192
X 169
Mvav 64,128,200
Ra 156,242,31744
Mvav 96,128,208
X 170
Mvav 128,128,216
K 212,168,169
Mvav 132,132,216
X 171
Mvav 100,132,208
X 172
Mvav 68,132,200
Mvav 36,132,192
K 213,170,171
Mvav 40,136,192
X 173
Mvav 72,136,200
X 174
Mvav 104,136,208
Mvav 136,136,216
K 220,172,173
Mvav 140,140,216
X 175
Mvav 108,140,208
X 176
Mvav 76,140,200
s_waitcnt vmcnt(0) lgkmcnt(0)
s_barrier
v_xor_b32 v242, 0x10000, v242
s_bitcmp1_b32 s68, s64
)"
R"(s_cbranch_scc1 .Lact_52%=
.Lactr_53%=:
Mvav 44,140,192
K 221,174,175
Ra 128,242,0
Mvav 48,144,192
X 177
Mvav 80,144,200
X 178
Mvav 112,144,208
Ra 132,242,1024
Mvav 144,144,216
K 198,176,177
Mvav 148,148,216
X 179
Mvav 116,148,208
X 180
Mvav 84,148,200
Ra 136,242,2048
Mvav 52,148,192
K 199,178,179
Mvav 56,152,192
X 181
Mvav 88,152,200
Ra 140,242,3072
Mvav 120,152,208
X 182
Mvav 152,152,216
K 206,180,181
Mvav 156,156,216
X 183
Mvav 124,156,208
Mvav 92,156,200
K 207,182,183
Mvav 60,156,192
s_cmp_lg_u32 s64, s66
s_cbranch_scc1 .Ll1_36%=
s_cmp_ge_u32 s14, 2
s_cbranch_scc1 .Lf1_42%=
.Lsp1_38%=:
W 0
Cvaa 160,128,0,224
Ra 144,242,4096
Ra 148,242,5120
Cvaa 164,128,16,228
Ra 152,242,6144
Ra 156,242,7168
Cvaa 168,128,32,232
X 184
Cvaa 172,128,48,236
X 185
Mvaa 160,132,4
s_add_u32 m0, s62, 0
Ra 128,242,16384
Mvaa 164,132,20
X 186
Mvaa 168,132,36
K 214,184,185
Dc 1,8,61,0
Mvaa 172,132,52
X 187
Mvaa 160,136,8
X 188
Mvaa 164,136,24
K 215,186,187
Dc 1,8,61,1024
Mvaa 168,136,40
X 189
Mvaa 172,136,56
Ra 132,242,17408
Mvaa 160,140,12
X 190
Mvaa 164,140,28
Dc 1,8,61,2048
K 222,188,189
Mvaa 168,140,44
X 191
Mvaa 172,140,60
Ra 136,242,18432
K 223,190,191
v_smfmac_f32_16x16x64_bf16 a[160:163], a[176:179], v[192:199], v240
Dc 1,8,61,3072
v_cndmask_b32_e64 v160, v241, v160, s[72:73]
v_smfmac_f32_16x16x64_bf16 a[164:167], a[176:179], v[200:207], v240
v_cndmask_b32_e64 v161, v241, v161, s[74:75]
v_cndmask_b32_e64 v162, v241, v162, s[76:77]
v_smfmac_f32_16x16x64_bf16 a[168:171], a[176:179], v[208:215], v240
v_cndmask_b32_e64 v163, v241, v163, s[78:79]
s_add_u32 m0, s62, 4096
v_smfmac_f32_16x16x64_bf16 a[172:175], a[176:179], v[216:223], v240
Ra 140,242,19456
W 4
Cvaa 180,144,16,228
X 160
Cvaa 184,144,32,232
X 161
Cvaa 188,144,48,236
Dc 243,8,61,0
Ra 144,242,20480
Mvaa 180,148,20
X 162
Mvaa 184,148,36
K 192,160,161
Mvaa 188,148,52
X 163
Mvaa 180,152,24
Dc 243,8,61,1024
Ra 148,242,21504
Mvaa 184,152,40
X 164
Mvaa 188,152,56
K 193,162,163
Ra 152,242,22528
Mvaa 180,156,28
Dc 243,8,61,2048
Mvaa 184,156,44
X 165
Mvaa 188,156,60
W 3
Mvav 32,128,196
X 166
Mvav 64,128,204
K 200,164,165
Dc 243,8,61,3072
Mvav 96,128,212
X 167
Mvav 128,128,220
X 168
Mvav 132,132,220
s_add_u32 m0, s62, 8192
K 201,166,167
Mvav 100,132,212
Ra 156,242,23552
Dc 244,8,61,0
Mvav 68,132,204
X 169
Mvav 36,132,196
X 170
Mvav 40,136,196
K 208,168,169
Ra 128,242,8192
Mvav 72,136,204
X 171
Mvav 104,136,212
Dc 244,8,61,1024
Ra 132,242,9216
Mvav 136,136,220
X 172
Mvav 140,140,220
K 209,170,171
Ra 136,242,10240
Mvav 108,140,212
X 173
Mvav 76,140,204
Dc 244,8,61,2048
Mvav 44,140,196
Ra 140,242,11264
W 4
Mvav 48,144,196
X 174
Mvav 80,144,204
K 216,172,173
Dc 244,8,61,3072
Mvav 112,144,212
X 175
Mvav 144,144,220
v_mov_b32 v194, 0
K 217,174,175
Mvav 148,148,220
v_mov_b32 v195, 0
s_add_u32 m0, s62, 12288
Mvav 116,148,212
v_cndmask_b32_e64 v180, v241, v180, s[72:73]
v_cndmask_b32_e64 v181, v241, v181, s[74:75]
Mvav 84,148,204
v_cndmask_b32_e64 v182, v241, v182, s[76:77]
v_cndmask_b32_e64 v183, v241, v183, s[78:79]
Mvav 52,148,196
Dc 245,8,61,0
Ra 144,242,12288
Mvav 56,152,196
X 180
Mvav 88,152,204
X 181
Mvav 120,152,212
Dc 245,8,61,1024
Ra 148,242,13312
Mvav 152,152,220
X 182
Mvav 156,156,220
K 202,180,181
Ra 152,242,14336
Mvav 124,156,212
X 183
Mvav 92,156,204
Dc 245,8,61,2048
Mvav 60,156,196
Ra 156,242,15360
W 4
Cvaa 168,128,32,232
X 184
Cvaa 172,128,48,236
K 203,182,183
Dc 245,8,61,3072
Mvaa 168,132,36
X 185
Mvaa 172,132,52
X 186
Mvaa 168,136,40
s_add_u32 s61, s61, s63
K 210,184,185
Mvaa 172,136,56
X 187
Mvaa 168,140,44
Ra 128,242,24576
Ra 132,242,25600
Ra 136,242,26624
Mvaa 172,140,60
X 188
K 211,186,187
X 189
X 190
K 218,188,189
X 191
v_mov_b32 v196, 0
Ra 140,242,27648
K 219,190,191
W 4
Cvaa 188,144,48,236
s_xor_b32 s62, s62, 0x10000
s_add_u32 s64, s64, 1
Mvaa 188,148,52
Ra 144,242,28672
v_mov_b32 v197, 0
Mvaa 188,152,56
v_mov_b32 v204, 0
Ra 148,242,29696
Mvaa 188,156,60
v_mov_b32 v205, 0
W 2
Mvav 32,128,192
Ra 152,242,30720
v_cndmask_b32_e64 v168, v241, v168, s[72:73]
Mvav 64,128,200
v_cndmask_b32_e64 v169, v241, v169, s[74:75]
v_cndmask_b32_e64 v170, v241, v170, s[76:77]
Mvav 96,128,208
Ra 156,242,31744
v_cndmask_b32_e64 v171, v241, v171, s[78:79]
Mvav 128,128,216
X 168
Mvav 132,132,216
X 169
Mvav 100,132,208
X 170
Mvav 68,132,200
v_mov_b32 v224, v250
K 212,168,169
Mvav 36,132,192
v_mov_b32 v225, v250
v_mov_b32 v226, v250
Mvav 40,136,192
X 171
Mvav 72,136,200
X 172
Mvav 104,136,208
v_mov_b32 v227, v250
K 213,170,171
Mvav 136,136,216
X 173
Mvav 140,140,216
v_mov_b32 v228, v251
v_mov_b32 v229, v251
Mvav 108,140,208
X 174
Mvav 76,140,200
s_waitcnt vmcnt(0) lgkmcnt(0)
s_barrier
v_xor_b32 v242, 0x10000, v242
s_bitcmp1_b32 s68, s64
s_cbranch_scc1 .Lact_54%=
.Lactr_55%=:
Mvav 44,140,192
K 220,172,173
v_mov_b32 v230, v251
Mvav 48,144,192
X 175
Mvav 80,144,200
v_mov_b32 v231, v251
v_mov_b32 v198, 0
Mvav 112,144,208
Ra 128,242,40960
K 221,174,175
Mvav 144,144,216
v_mov_b32 v232, v248
v_mov_b32 v199, 0
Mvav 148,148,216
v_mov_b32 v206, 0
v_mov_b32 v233, v248
Mvav 116,148,208
v_mov_b32 v207, 0
Ra 132,242,41984
Mvav 84,148,200
v_mov_b32 v214, 0
v_mov_b32 v234, v248
Mvav 52,148,192
v_mov_b32 v215, 0
v_cndmask_b32_e64 v188, v241, v188, s[72:73]
Mvav 56,152,192
v_mov_b32 v235, v248
v_cndmask_b32_e64 v189, v241, v189, s[74:75]
Mvav 88,152,200
Ra 136,242,43008
v_cndmask_b32_e64 v190, v241, v190, s[76:77]
Mvav 120,152,208
v_mov_b32 v236, v249
v_cndmask_b32_e64 v191, v241, v191, s[78:79]
Mvav 152,152,216
X 188
Mvav 156,156,216
v_mov_b32 v237, v249
Ra 140,242,44032
Mvav 124,156,208
X 189
Mvav 92,156,200
v_mov_b32 v238, v249
v_mov_b32 v239, v249
Mvav 60,156,192
X 190
K 222,188,189
X 191
s_nop 0
K 223,190,191
s_cmp_eq_u32 s64, 15
s_cbranch_scc1 .Lfs_44%=
.Lsp2_39%=:
v_smfmac_f32_16x16x64_bf16 a[160:163], a[176:179], v[192:199], v240
Ra 144,242,45056
Ra 148,242,46080
Ra 152,242,47104
Ra 156,242,48128
s_add_u32 m0, s62, 0
v_smfmac_f32_16x16x64_bf16 a[164:167], a[176:179], v[200:207], v240
v_smfmac_f32_16x16x64_bf16 a[168:171], a[176:179], v[208:215], v240
Dc 1,8,61,0
v_smfmac_f32_16x16x64_bf16 a[172:175], a[176:179], v[216:223], v240
W 4
Cvaa 168,128,96,232
v_mov_b32 v192, 0
Dc 1,8,61,1024
G 12,160
Cvaa 172,128,112,236
G 4,161
A 12,12,4
v_cmp_nlt_f32_e64 s[86:87], v12, s90
v_mov_b32 v193, 0
Ra 128,242,16384
Mvaa 168,132,100
v_rcp_f32 v12, v12
s_or_b64 s[88:89], s[88:89], s[86:87]
v_mov_b32 v200, 0
v_accvgpr_write_b32 a160, 0
Dc 1,8,61,2048
Mvaa 172,132,116
v_accvgpr_write_b32 a161, 0
v_accvgpr_write_b32 a162, 0
v_mov_b32 v201, 0
v_accvgpr_write_b32 a163, 0
G 13,164
Ra 132,242,17408
Mvaa 168,136,104
G 5,165
A 13,13,5
v_cmp_nlt_f32_e64 s[86:87], v13, s90
v_rcp_f32 v13, v13
Dc 1,8,61,3072
Mvaa 172,136,120
s_or_b64 s[88:89], s[88:89], s[86:87]
v_accvgpr_write_b32 a164, 0
v_accvgpr_write_b32 a165, 0
Ra 136,242,18432
v_accvgpr_write_b32 a166, 0
Mvaa 168,140,108
v_accvgpr_write_b32 a167, 0
U 32,32,12
U 33,33,12
s_add_u32 m0, s62, 4096
U 34,34,12
Mvaa 172,140,124
Ra 140,242,19456
U 35,35,12
U 36,36,12
U 37,37,12
U 38,38,12
W 4
Cvaa 188,144,112,236
U 39,39,12
K 32,32,33
K 33,34,35
Ra 144,242,20480
)"
R"(Dc 243,8,61,0
K 34,36,37
Mvaa 188,148,116
K 35,38,39
s_add_u32 s93, s67, 0
Ra 148,242,21504
v_cndmask_b32_e64 v168, v241, v168, s[72:73]
v_cndmask_b32_e64 v169, v241, v169, s[74:75]
v_cndmask_b32_e64 v170, v241, v170, s[76:77]
Mvaa 188,152,120
buffer_store_dwordx4 v[32:35], v1, s[8:11], s93 offen offset:0 sc1
v_cndmask_b32_e64 v171, v241, v171, s[78:79]
X 168
X 169
Mvaa 188,156,124
U 40,40,12
U 41,41,12
U 42,42,12
X 170
W 2
Mvav 96,128,212
U 43,43,12
Ra 152,242,22528
Dc 243,8,61,1024
U 44,44,12
U 45,45,12
U 46,46,12
Mvav 128,128,220
U 47,47,12
Ra 156,242,23552
K 40,40,41
K 208,168,169
K 41,42,43
K 42,44,45
Mvav 132,132,220
K 43,46,47
X 171
Dc 243,8,61,2048
Ra 128,242,32768
Mvav 100,132,212
X 172
Ra 132,242,33792
Mvav 104,136,212
buffer_store_dwordx4 v[40:43], v1, s[8:11], s93 offen offset:1024 sc1
U 48,48,12
U 49,49,12
U 50,50,12
U 51,51,12
U 52,52,12
Mvav 136,136,220
U 53,53,12
U 54,54,12
U 55,55,12
K 48,48,49
K 49,50,51
K 50,52,53
Mvav 140,140,220
Dc 243,8,61,3072
K 51,54,55
K 209,170,171
X 173
s_add_u32 m0, s62, 8192
Mvav 108,140,212
Ra 136,242,34816
X 174
Ra 140,242,35840
W 4
Mvav 112,144,212
buffer_store_dwordx4 v[48:51], v1, s[8:11], s93 offen offset:2048 sc1
U 56,56,12
U 57,57,12
U 58,58,12
U 59,59,12
U 60,60,12
Mvav 144,144,220
U 61,61,12
U 62,62,12
U 63,63,12
K 56,56,57
K 57,58,59
K 58,60,61
Mvav 148,148,220
Dc 244,8,61,0
K 59,62,63
K 216,172,173
X 175
Ra 144,242,36864
Mvav 116,148,212
v_mov_b32 v194, 0
K 217,174,175
Ra 148,242,37888
Mvav 120,152,212
buffer_store_dwordx4 v[56:59], v1, s[8:11], s93 offen offset:3072 sc1
G 14,168
G 6,169
A 14,14,6
v_cmp_nlt_f32_e64 s[86:87], v14, s90
v_mov_b32 v195, 0
Mvav 152,152,220
v_rcp_f32 v14, v14
s_or_b64 s[88:89], s[88:89], s[86:87]
v_accvgpr_write_b32 a168, 0
v_accvgpr_write_b32 a169, 0
v_accvgpr_write_b32 a170, 0
Mvav 156,156,220
Dc 244,8,61,1024
v_accvgpr_write_b32 a171, 0
G 15,172
G 7,173
A 15,15,7
v_cmp_nlt_f32_e64 s[86:87], v15, s90
Mvav 124,156,212
v_rcp_f32 v15, v15
s_or_b64 s[88:89], s[88:89], s[86:87]
v_accvgpr_write_b32 a172, 0
v_accvgpr_write_b32 a173, 0
W 2
Cvaa 160,128,64,224
Dc 244,8,61,2048
v_accvgpr_write_b32 a174, 0
v_mov_b32 v202, 0
v_accvgpr_write_b32 a175, 0
U 64,64,13
U 65,65,13
Cvaa 164,128,80,228
v_mov_b32 v203, 0
U 66,66,13
Ra 152,242,38912
U 67,67,13
U 68,68,13
v_mov_b32 v210, 0
Cvaa 168,128,96,232
Dc 244,8,61,3072
U 69,69,13
U 70,70,13
s_add_u32 m0, s62, 12288
U 71,71,13
K 64,64,65
Cvaa 172,128,112,236
v_mov_b32 v211, 0
K 65,66,67
K 66,68,69
Ra 156,242,39936
K 67,70,71
v_cndmask_b32_e64 v188, v241, v188, s[72:73]
Mvaa 160,132,68
s_add_u32 s93, s67, 131072
buffer_store_dwordx4 v[64:67], v1, s[8:11], s93 offen offset:0 sc1
U 72,72,13
v_cndmask_b32_e64 v189, v241, v189, s[74:75]
U 73,73,13
U 74,74,13
Mvaa 164,132,84
U 75,75,13
v_cndmask_b32_e64 v190, v241, v190, s[76:77]
U 76,76,13
U 77,77,13
U 78,78,13
v_cndmask_b32_e64 v191, v241, v191, s[78:79]
Mvaa 168,132,100
Dc 245,8,61,0
U 79,79,13
K 72,72,73
K 73,74,75
K 74,76,77
K 75,78,79
Mvaa 172,132,116
X 188
Ra 128,242,49152
X 189
Ra 132,242,50176
Mvaa 160,136,72
Dc 245,8,61,1024
X 190
K 218,188,189
X 191
Mvaa 164,136,88
Mvaa 168,136,104
buffer_store_dwordx4 v[72:75], v1, s[8:11], s93 offen offset:1024 sc1
U 80,80,13
U 81,81,13
U 82,82,13
U 83,83,13
U 84,84,13
Mvaa 172,136,120
U 85,85,13
U 86,86,13
U 87,87,13
K 80,80,81
K 81,82,83
K 82,84,85
Mvaa 160,140,76
Dc 245,8,61,2048
K 83,86,87
Ra 136,242,51200
Mvaa 164,140,92
Mvaa 168,140,108
Dc 245,8,61,3072
s_add_u32 s61, s61, s63
s_xor_b32 s62, s62, 0x10000
s_add_u32 s64, s64, 1
s_add_u32 s70, s64, 1
Mvaa 172,140,124
Ra 140,242,52224
v_cndmask_b32_e64 v160, v241, v160, s[72:73]
K 219,190,191
v_cndmask_b32_e64 v161, v241, v161, s[74:75]
v_cndmask_b32_e64 v162, v241, v162, s[76:77]
W 4
Cvaa 180,144,80,228
buffer_store_dwordx4 v[80:83], v1, s[8:11], s93 offen offset:2048 sc1
U 88,88,13
U 89,89,13
U 90,90,13
U 91,91,13
U 92,92,13
Cvaa 184,144,96,232
U 93,93,13
U 94,94,13
U 95,95,13
K 88,88,89
K 89,90,91
K 90,92,93
Cvaa 188,144,112,236
K 91,94,95
buffer_store_dwordx4 v[88:91], v1, s[8:11], s93 offen offset:3072 sc1
U 96,96,14
U 97,97,14
U 98,98,14
U 99,99,14
Mvaa 180,148,84
U 100,100,14
U 101,101,14
U 102,102,14
U 103,103,14
K 96,96,97
K 97,98,99
Mvaa 184,148,100
K 98,100,101
K 99,102,103
s_add_u32 s93, s67, 262144
buffer_store_dwordx4 v[96:99], v1, s[8:11], s93 offen offset:0 sc1
U 104,104,14
U 105,105,14
Mvaa 188,148,116
U 106,106,14
U 107,107,14
U 108,108,14
U 109,109,14
U 110,110,14
U 111,111,14
Mvaa 180,152,88
v_cndmask_b32_e64 v163, v241, v163, s[78:79]
X 160
X 161
K 104,104,105
Mvaa 184,152,104
K 105,106,107
K 106,108,109
K 107,110,111
s_bitset1_b32 s68, s70
buffer_store_dwordx4 v[104:107], v1, s[8:11], s93 offen offset:1024 sc1
U 112,112,14
Mvaa 188,152,120
U 113,113,14
X 162
U 114,114,14
U 115,115,14
U 116,116,14
Mvaa 180,156,92
U 117,117,14
U 118,118,14
Ra 144,242,53248
U 119,119,14
K 196,160,161
K 112,112,113
Mvaa 184,156,108
K 113,114,115
K 114,116,117
X 163
K 115,118,119
buffer_store_dwordx4 v[112:115], v1, s[8:11], s93 offen offset:2048 sc1
Mvaa 188,156,124
U 120,120,14
U 121,121,14
U 122,122,14
U 123,123,14
U 124,124,14
W 1
Zvav 32,128,192
X 164
U 125,125,14
Ra 148,242,54272
U 126,126,14
U 127,127,14
Zvav 64,128,200
K 120,120,121
K 121,122,123
K 122,124,125
K 197,162,163
K 123,126,127
buffer_store_dwordx4 v[120:123], v1, s[8:11], s93 offen offset:3072 sc1
Zvav 96,128,208
U 128,128,15
U 129,129,15
U 130,130,15
U 131,131,15
U 132,132,15
U 133,133,15
U 134,134,15
U 135,135,15
K 128,128,129
K 129,130,131
K 130,132,133
K 131,134,135
s_add_u32 s93, s67, 393216
buffer_store_dwordx4 v[128:131], v1, s[8:11], s93 offen offset:0 sc1
s_nop 1
Zvav 128,128,216
X 165
Ra 152,242,55296
X 166
K 204,164,165
Zvav 132,132,216
U 136,136,15
U 137,137,15
U 138,138,15
X 167
U 139,139,15
Zvav 100,132,208
U 140,140,15
U 141,141,15
U 142,142,15
Ra 156,242,56320
U 143,143,15
K 136,136,137
Zvav 68,132,200
K 137,138,139
X 168
K 138,140,141
K 139,142,143
buffer_store_dwordx4 v[136:139], v1, s[8:11], s93 offen offset:1024 sc1
Zvav 36,132,192
U 144,144,15
U 145,145,15
U 146,146,15
K 205,166,167
U 147,147,15
U 148,148,15
Zvav 40,136,192
U 149,149,15
X 169
U 150,150,15
U 151,151,15
K 144,144,145
Zvav 72,136,200
K 145,146,147
K 146,148,149
K 147,150,151
buffer_store_dwordx4 v[144:147], v1, s[8:11], s93 offen offset:2048 sc1
X 170
Zvav 104,136,208
U 152,152,15
U 153,153,15
U 154,154,15
U 155,155,15
U 156,156,15
U 157,157,15
Zvav 136,136,216
K 212,168,169
U 158,158,15
U 159,159,15
K 152,152,153
X 171
Zvav 140,140,216
K 153,154,155
K 154,156,157
K 155,158,159
buffer_store_dwordx4 v[152:155], v1, s[8:11], s93 offen offset:3072 sc1
X 172
Zvav 108,140,208
K 213,170,171
X 173
X 174
Zvav 76,140,200
s_waitcnt vmcnt(10) lgkmcnt(0)
s_barrier
v_xor_b32 v242, 0x10000, v242
s_bitcmp1_b32 s68, s64
s_cbranch_scc1 .Lact_56%=
.Lactr_57%=:
Zvav 44,140,192
K 220,172,173
X 175
Ra 128,242,40960
Zvav 48,144,192
Zvav 80,144,200
v_mov_b32 v198, 0
Zvav 112,144,208
K 221,174,175
)"
R"(Ra 132,242,41984
Zvav 144,144,216
v_mov_b32 v199, 0
v_cndmask_b32_e64 v180, v241, v180, s[72:73]
Zvav 148,148,216
v_cndmask_b32_e64 v181, v241, v181, s[74:75]
Zvav 116,148,208
v_cndmask_b32_e64 v182, v241, v182, s[76:77]
Ra 136,242,43008
Zvav 84,148,200
v_cndmask_b32_e64 v183, v241, v183, s[78:79]
Zvav 52,148,192
X 180
Zvav 56,152,192
X 181
Ra 140,242,44032
Zvav 88,152,200
Zvav 120,152,208
X 182
Zvav 152,152,216
K 206,180,181
Zvav 156,156,216
X 183
Zvav 124,156,208
Zvav 92,156,200
K 207,182,183
Zvav 60,156,192
s_cmp_eq_u32 s64, 15
s_cbranch_scc1 .Lf2_43%=
.Ll2_40%=:
W 0
Cvaa 160,128,64,224
Ra 144,242,45056
Ra 148,242,46080
Cvaa 164,128,80,228
Ra 152,242,47104
s_add_u32 m0, s62, 0
Cvaa 168,128,96,232
X 184
Cvaa 172,128,112,236
X 185
Mvaa 160,132,68
X 186
Mvaa 164,132,84
K 214,184,185
Ra 156,242,48128
Mvaa 168,132,100
Dc 1,8,61,0
Mvaa 172,132,116
X 187
Mvaa 160,136,72
X 188
Mvaa 164,136,88
Ra 128,242,57344
K 215,186,187
Mvaa 168,136,104
Dc 1,8,61,1024
Mvaa 172,136,120
X 189
Mvaa 160,140,76
X 190
Mvaa 164,140,92
Ra 132,242,58368
K 222,188,189
Mvaa 168,140,108
Dc 1,8,61,2048
Mvaa 172,140,124
X 191
v_smfmac_f32_16x16x64_bf16 a[160:163], a[176:179], v[192:199], v240
K 223,190,191
Ra 136,242,59392
v_smfmac_f32_16x16x64_bf16 a[164:167], a[176:179], v[200:207], v240
X 160
v_smfmac_f32_16x16x64_bf16 a[168:171], a[176:179], v[208:215], v240
Dc 1,8,61,3072
v_smfmac_f32_16x16x64_bf16 a[172:175], a[176:179], v[216:223], v240
s_add_u32 m0, s62, 4096
W 3
Cvaa 176,144,64,224
X 161
Cvaa 180,144,80,228
X 162
Cvaa 184,144,96,232
Ra 140,242,60416
K 192,160,161
Cvaa 188,144,112,236
X 163
Mvaa 176,148,68
Dc 243,8,61,0
Ra 144,242,61440
Mvaa 180,148,84
X 164
Mvaa 184,148,100
K 193,162,163
Mvaa 188,148,116
X 165
Mvaa 176,152,72
Dc 243,8,61,1024
Ra 148,242,62464
Mvaa 180,152,88
X 166
Mvaa 184,152,104
K 200,164,165
Mvaa 188,152,120
X 167
Mvaa 176,156,76
Dc 243,8,61,2048
Ra 152,242,63488
Mvaa 180,156,92
X 168
Mvaa 184,156,108
K 201,166,167
Mvaa 188,156,124
W 3
Mvav 32,128,196
Dc 243,8,61,3072
s_add_u32 m0, s62, 8192
Mvav 64,128,204
X 169
Mvav 96,128,212
X 170
Mvav 128,128,220
Ra 156,242,64512
K 208,168,169
Mvav 132,132,220
X 171
Mvav 100,132,212
Dc 244,8,61,0
Ra 128,242,32768
Mvav 68,132,204
X 172
Mvav 36,132,196
K 209,170,171
Mvav 40,136,196
X 173
Mvav 72,136,204
Dc 244,8,61,1024
Ra 132,242,33792
Mvav 104,136,212
X 174
Mvav 136,136,220
K 216,172,173
Mvav 140,140,220
X 175
Mvav 108,140,212
Dc 244,8,61,2048
Ra 136,242,34816
Mvav 76,140,204
X 176
Mvav 44,140,196
K 217,174,175
W 3
Mvav 48,144,196
X 177
Mvav 80,144,204
Dc 244,8,61,3072
Ra 140,242,35840
Mvav 112,144,212
X 178
Mvav 144,144,220
s_add_u32 m0, s62, 12288
K 194,176,177
Mvav 148,148,220
X 179
Mvav 116,148,212
X 180
Mvav 84,148,204
Dc 245,8,61,0
Ra 144,242,36864
Mvav 52,148,196
K 195,178,179
Mvav 56,152,196
X 181
Mvav 88,152,204
X 182
Mvav 120,152,212
Dc 245,8,61,1024
Ra 148,242,37888
Mvav 152,152,220
K 202,180,181
Mvav 156,156,220
X 183
Mvav 124,156,212
X 184
Mvav 92,156,204
Dc 245,8,61,2048
Ra 152,242,38912
Mvav 60,156,196
K 203,182,183
W 3
Cvaa 160,128,64,224
X 185
Cvaa 164,128,80,228
X 186
Cvaa 168,128,96,232
Dc 245,8,61,3072
Ra 156,242,39936
Cvaa 172,128,112,236
K 210,184,185
s_add_u32 s61, s61, s63
Mvaa 160,132,68
X 187
Mvaa 164,132,84
X 188
Mvaa 168,132,100
Ra 128,242,49152
s_xor_b32 s62, s62, 0x10000
Mvaa 172,132,116
K 211,186,187
Mvaa 160,136,72
X 189
Mvaa 164,136,88
X 190
Mvaa 168,136,104
Ra 132,242,50176
s_add_u32 s64, s64, 1
Mvaa 172,136,120
K 218,188,189
Mvaa 160,140,76
X 191
Mvaa 164,140,92
Ra 136,242,51200
Mvaa 168,140,108
Mvaa 172,140,124
W 3
Cvaa 176,144,64,224
Ra 140,242,52224
Cvaa 180,144,80,228
X 160
Cvaa 184,144,96,232
K 219,190,191
Cvaa 188,144,112,236
X 161
Mvaa 176,148,68
X 162
Mvaa 180,148,84
K 196,160,161
Ra 144,242,53248
Mvaa 184,148,100
X 163
Mvaa 188,148,116
X 164
Mvaa 176,152,72
K 197,162,163
Ra 148,242,54272
Mvaa 180,152,88
X 165
Mvaa 184,152,104
X 166
Mvaa 188,152,120
K 204,164,165
Mvaa 176,156,76
X 167
Mvaa 180,156,92
Ra 152,242,55296
Mvaa 184,156,108
X 168
Mvaa 188,156,124
K 205,166,167
W 3
Mvav 32,128,192
X 169
Mvav 64,128,200
Ra 156,242,56320
Mvav 96,128,208
X 170
Mvav 128,128,216
K 212,168,169
Mvav 132,132,216
X 171
Mvav 100,132,208
X 172
Mvav 68,132,200
Mvav 36,132,192
K 213,170,171
Mvav 40,136,192
X 173
Mvav 72,136,200
X 174
Mvav 104,136,208
Mvav 136,136,216
K 220,172,173
Mvav 140,140,216
X 175
Mvav 108,140,208
X 176
Mvav 76,140,200
s_waitcnt vmcnt(0) lgkmcnt(0)
s_barrier
v_xor_b32 v242, 0x10000, v242
s_bitcmp1_b32 s68, s64
s_cbranch_scc1 .Lact_58%=
.Lactr_59%=:
Mvav 44,140,192
K 221,174,175
Ra 128,242,40960
Mvav 48,144,192
X 177
Mvav 80,144,200
X 178
Mvav 112,144,208
Ra 132,242,41984
Mvav 144,144,216
K 198,176,177
Mvav 148,148,216
X 179
Mvav 116,148,208
X 180
Mvav 84,148,200
Ra 136,242,43008
Mvav 52,148,192
K 199,178,179
Mvav 56,152,192
X 181
Mvav 88,152,200
Ra 140,242,44032
Mvav 120,152,208
X 182
Mvav 152,152,216
K 206,180,181
Mvav 156,156,216
X 183
Mvav 124,156,208
Mvav 92,156,200
K 207,182,183
Mvav 60,156,192
s_cmp_lg_u32 s64, 15
s_cbranch_scc1 .Ll2_40%=
.Lf2_43%=:
W 0
Cvaa 160,128,64,224
Ra 144,242,45056
Ra 148,242,46080
Cvaa 164,128,80,228
Ra 152,242,47104
s_lshr_b32 s92, s14, 1
Cvaa 168,128,96,232
X 184
Cvaa 172,128,112,236
X 185
Mvaa 160,132,68
s_and_b32 s71, s14, 1
s_mul_i32 s71, s71, 5120
Mvaa 164,132,84
X 186
Mvaa 168,132,100
K 214,184,185
Ra 156,242,48128
Mvaa 172,132,116
X 187
Mvaa 160,136,72
v_bfe_u32 v28, v1, 4, 2
v_lshlrev_b32 v28, 3, v28
Mvaa 164,136,88
X 188
Mvaa 168,136,104
K 215,186,187
buffer_load_dwordx2 v[28:29], v28, s[8:11], s54 offen sc1
Mvaa 172,136,120
X 189
Mvaa 160,140,76
Ra 128,242,57344
s_add_u32 s94, s58, 0
Mvaa 164,140,92
X 190
Mvaa 168,140,108
K 222,188,189
s_and_b32 s94, s94, 3
Mvaa 172,140,124
X 191
v_smfmac_f32_16x16x64_bf16 a[160:163], a[176:179], v[192:199], v240
s_mul_i32 s94, s94, 163840
K 223,190,191
v_smfmac_f32_16x16x64_bf16 a[164:167], a[176:179], v[200:207], v240
Ra 132,242,58368
s_add_u32 s94, s94, s48
v_smfmac_f32_16x16x64_bf16 a[168:171], a[176:179], v[208:215], v240
s_mul_i32 s95, s92, 10240
s_add_u32 s95, s95, s71
v_smfmac_f32_16x16x64_bf16 a[172:175], a[176:179], v[216:223], v240
Ra 136,242,59392
W 3
Cvaa 176,144,64,224
X 160
Cvaa 180,144,80,228
X 161
Cvaa 184,144,96,232
X 162
Cvaa 188,144,112,236
K 192,160,161
s_add_u32 m0, s95, 0
Mvaa 176,148,68
X 163
Mvaa 180,148,84
X 164
Mvaa 184,148,100
D 16,40,94,0
K 193,162,163
Mvaa 188,148,116
Ra 140,242,60416
)"
R"(Ra 144,242,61440
Mvaa 176,152,72
X 165
Mvaa 180,152,88
D 17,40,94,1024
Ra 148,242,62464
Mvaa 184,152,104
X 166
Mvaa 188,152,120
K 200,164,165
Mvaa 176,156,76
D 18,40,94,2048
Ra 152,242,63488
Mvaa 180,156,92
X 167
Mvaa 184,156,108
X 172
Mvaa 188,156,124
D 19,40,94,3072
W 3
Mvav 32,128,196
s_add_u32 m0, s95, 4096
K 201,166,167
Mvav 64,128,204
X 173
Mvav 96,128,212
buffer_load_dwordx4 v20, s[40:43], s94 offen lds
s_add_u32 s94, s94, 81920
Mvav 128,128,220
X 174
Mvav 132,132,220
s_mul_i32 s95, s92, 112640
K 216,172,173
Mvav 100,132,212
s_add_u32 s95, s95, s71
s_add_u32 m0, s95, 20480
Mvav 68,132,204
X 175
Mvav 36,132,196
X 168
Mvav 40,136,196
K 217,174,175
D 16,40,94,0
Mvav 72,136,204
X 169
Mvav 104,136,212
Ra 156,242,64512
Ra 128,242,49152
Mvav 136,136,220
D 17,40,94,1024
Mvav 140,140,220
X 170
Mvav 108,140,212
K 208,168,169
Ra 132,242,50176
Mvav 76,140,204
D 18,40,94,2048
Mvav 44,140,196
W 2
Mvav 48,144,196
X 171
Mvav 80,144,204
D 19,40,94,3072
s_add_u32 m0, s95, 24576
Mvav 112,144,212
X 176
Mvav 144,144,220
K 209,170,171
Ra 136,242,51200
Mvav 148,148,220
X 177
Mvav 116,148,212
buffer_load_dwordx4 v20, s[40:43], s94 offen lds
s_waitcnt vmcnt(10)
v_cmp_eq_u64 vcc, s[12:13], v[28:29]
s_cmp_eq_u64 vcc, -1
s_cselect_b32 s46, 79691776, 0
Mvav 84,148,204
X 178
Mvav 52,148,196
K 194,176,177
s_add_u32 s81, s58, 0
Mvav 56,152,196
X 179
Mvav 88,152,204
s_and_b32 s81, s81, 3
s_lshl_b32 s81, s81, 12
Mvav 120,152,212
X 180
Mvav 152,152,220
K 195,178,179
s_add_u32 s81, s81, s49
Mvav 156,156,220
X 181
Mvav 124,156,212
X 182
Mvav 92,156,204
Ra 140,242,52224
s_add_u32 s82, s81, 131072
Mvav 60,156,196
K 202,180,181
X 183
X 184
K 203,182,183
Maav 160,180,192
X 185
X 186
K 210,184,185
X 187
X 188
K 211,186,187
Maav 164,180,200
X 189
X 190
K 218,188,189
X 191
s_nop 0
K 219,190,191
Maav 168,180,208
buffer_load_dwordx4 a[184:187], v1, s[44:47], s81 offen offset:0 sc1
Ra 144,242,53248
Maav 172,180,216
Ra 148,242,54272
W 2
Mvav 32,128,192
Ra 152,242,55296
Ra 156,242,56320
Mvav 64,128,200
buffer_load_dwordx4 a[200:203], v1, s[44:47], s82 offen offset:0 sc1
Mvav 96,128,208
Mvav 128,128,216
Mvav 132,132,216
buffer_load_dwordx4 a[188:191], v1, s[44:47], s81 offen offset:1024 sc1
Mvav 100,132,208
Mvav 68,132,200
Mvav 36,132,192
buffer_load_dwordx4 a[204:207], v1, s[44:47], s82 offen offset:1024 sc1
Mvav 40,136,192
Mvav 72,136,200
Mvav 104,136,208
buffer_load_dwordx4 a[192:195], v1, s[44:47], s81 offen offset:2048 sc1
Mvav 136,136,216
Mvav 140,140,216
Mvav 108,140,208
buffer_load_dwordx4 a[208:211], v1, s[44:47], s82 offen offset:2048 sc1
Mvav 76,140,200
Mvav 44,140,192
W 0
Mvav 48,144,192
buffer_load_dwordx4 a[196:199], v1, s[44:47], s81 offen offset:3072 sc1
Mvav 80,144,200
Mvav 112,144,208
Mvav 144,144,216
buffer_load_dwordx4 a[212:215], v1, s[44:47], s82 offen offset:3072 sc1
s_add_u32 s94, s58, 1
Mvav 148,148,216
s_and_b32 s94, s94, 3
s_mul_i32 s94, s94, 163840
Mvav 116,148,208
s_add_u32 s94, s94, s48
s_mul_i32 s95, s92, 10240
Mvav 84,148,200
s_add_u32 s95, s95, s71
s_add_u32 m0, s95, 143360
Mvav 52,148,192
D 16,40,94,0
Mvav 56,152,192
Mvav 88,152,200
Mvav 120,152,208
D 17,40,94,1024
Mvav 152,152,216
Mvav 156,156,216
Mvav 124,156,208
D 18,40,94,2048
Mvav 92,156,200
Mvav 60,156,192
D 19,40,94,3072
s_add_u32 m0, s95, 147456
s_nop 0
buffer_load_dwordx4 v20, s[40:43], s94 offen lds
s_add_u32 s81, s58, 1
s_and_b32 s81, s81, 3
s_lshl_b32 s81, s81, 12
s_add_u32 s81, s81, s49
s_add_u32 s82, s81, 131072
buffer_load_dwordx4 a[216:219], v1, s[44:47], s81 offen offset:0 sc1
buffer_load_dwordx4 a[232:235], v1, s[44:47], s82 offen offset:0 sc1
buffer_load_dwordx4 a[220:223], v1, s[44:47], s81 offen offset:1024 sc1
buffer_load_dwordx4 a[236:239], v1, s[44:47], s82 offen offset:1024 sc1
buffer_load_dwordx4 a[224:227], v1, s[44:47], s81 offen offset:2048 sc1
buffer_load_dwordx4 a[240:243], v1, s[44:47], s82 offen offset:2048 sc1
buffer_load_dwordx4 a[228:231], v1, s[44:47], s81 offen offset:3072 sc1
buffer_load_dwordx4 a[244:247], v1, s[44:47], s82 offen offset:3072 sc1
s_branch .Lend_45%=
.Lfs_44%=:
v_smfmac_f32_16x16x64_bf16 a[160:163], a[176:179], v[192:199], v240
Ra 144,242,45056
Ra 148,242,46080
Ra 152,242,47104
Ra 156,242,48128
s_lshr_b32 s92, s14, 1
v_smfmac_f32_16x16x64_bf16 a[164:167], a[176:179], v[200:207], v240
s_and_b32 s71, s14, 1
v_smfmac_f32_16x16x64_bf16 a[168:171], a[176:179], v[208:215], v240
s_mul_i32 s71, s71, 5120
v_smfmac_f32_16x16x64_bf16 a[172:175], a[176:179], v[216:223], v240
v_bfe_u32 v28, v1, 4, 2
W 4
Cvaa 168,128,96,232
v_lshlrev_b32 v28, 3, v28
v_mov_b32 v192, 0
G 12,160
Cvaa 172,128,112,236
G 4,161
A 12,12,4
v_cmp_nlt_f32_e64 s[86:87], v12, s90
buffer_load_dwordx2 v[28:29], v28, s[8:11], s54 offen sc1
v_rcp_f32 v12, v12
Mvaa 168,132,100
s_or_b64 s[88:89], s[88:89], s[86:87]
Ra 128,242,16384
v_accvgpr_write_b32 a160, 0
v_accvgpr_write_b32 a161, 0
v_mov_b32 v193, 0
v_accvgpr_write_b32 a162, 0
Mvaa 172,132,116
s_add_u32 s94, s58, 0
v_accvgpr_write_b32 a163, 0
G 13,164
G 5,165
A 13,13,5
v_cmp_nlt_f32_e64 s[86:87], v13, s90
Mvaa 168,136,104
v_rcp_f32 v13, v13
s_and_b32 s94, s94, 3
v_mov_b32 v200, 0
s_or_b64 s[88:89], s[88:89], s[86:87]
v_accvgpr_write_b32 a164, 0
Mvaa 172,136,120
v_accvgpr_write_b32 a165, 0
v_accvgpr_write_b32 a166, 0
Ra 132,242,17408
v_accvgpr_write_b32 a167, 0
s_mul_i32 s94, s94, 163840
U 32,32,12
Mvaa 168,140,108
Ra 136,242,18432
U 33,33,12
U 34,34,12
U 35,35,12
v_mov_b32 v201, 0
U 36,36,12
Mvaa 172,140,124
Ra 140,242,19456
U 37,37,12
s_add_u32 s94, s94, s48
U 38,38,12
U 39,39,12
W 4
Cvaa 188,144,112,236
K 32,32,33
K 33,34,35
K 34,36,37
K 35,38,39
s_mul_i32 s95, s92, 10240
s_add_u32 s93, s67, 0
Mvaa 188,148,116
buffer_store_dwordx4 v[32:35], v1, s[8:11], s93 offen offset:0 sc1
U 40,40,12
U 41,41,12
U 42,42,12
U 43,43,12
s_add_u32 s95, s95, s71
Mvaa 188,152,120
U 44,44,12
U 45,45,12
U 46,46,12
U 47,47,12
K 40,40,41
K 41,42,43
Mvaa 188,156,124
s_add_u32 m0, s95, 0
K 42,44,45
K 43,46,47
buffer_store_dwordx4 v[40:43], v1, s[8:11], s93 offen offset:1024 sc1
U 48,48,12
W 0
Mvav 96,128,212
U 49,49,12
U 50,50,12
U 51,51,12
U 52,52,12
Ra 144,242,20480
U 53,53,12
Mvav 128,128,220
D 16,40,94,0
U 54,54,12
U 55,55,12
K 48,48,49
K 49,50,51
K 50,52,53
Mvav 132,132,220
Ra 148,242,21504
Ra 152,242,22528
Ra 156,242,23552
K 51,54,55
v_cndmask_b32_e64 v168, v241, v168, s[72:73]
v_cndmask_b32_e64 v169, v241, v169, s[74:75]
Mvav 100,132,212
D 17,40,94,1024
v_cndmask_b32_e64 v170, v241, v170, s[76:77]
v_cndmask_b32_e64 v171, v241, v171, s[78:79]
X 168
Ra 128,242,49152
Mvav 104,136,212
X 169
Ra 132,242,50176
X 170
Mvav 136,136,220
buffer_store_dwordx4 v[48:51], v1, s[8:11], s93 offen offset:2048 sc1
U 56,56,12
U 57,57,12
U 58,58,12
U 59,59,12
U 60,60,12
Mvav 140,140,220
U 61,61,12
U 62,62,12
U 63,63,12
K 56,56,57
K 57,58,59
K 58,60,61
Mvav 108,140,212
D 18,40,94,2048
K 59,62,63
Ra 136,242,51200
K 208,168,169
Ra 140,242,52224
W 4
Mvav 112,144,212
X 171
Mvav 144,144,220
D 19,40,94,3072
s_add_u32 m0, s95, 4096
Ra 144,242,53248
Mvav 148,148,220
X 172
Mvav 116,148,212
buffer_store_dwordx4 v[56:59], v1, s[8:11], s93 offen offset:3072 sc1
G 14,168
G 6,169
A 14,14,6
v_cmp_nlt_f32_e64 s[86:87], v14, s90
Ra 148,242,54272
Mvav 120,152,212
v_rcp_f32 v14, v14
s_or_b64 s[88:89], s[88:89], s[86:87]
v_accvgpr_write_b32 a168, 0
v_accvgpr_write_b32 a169, 0
v_accvgpr_write_b32 a170, 0
Mvav 152,152,220
buffer_load_dwordx4 v20, s[40:43], s94 offen lds
v_accvgpr_write_b32 a171, 0
G 15,172
G 7,173
A 15,15,7
s_add_u32 s94, s94, 81920
Mvav 156,156,220
v_cmp_nlt_f32_e64 s[86:87], v15, s90
v_rcp_f32 v15, v15
s_or_b64 s[88:89], s[88:89], s[86:87]
v_accvgpr_write_b32 a172, 0
v_accvgpr_write_b32 a173, 0
v_accvgpr_write_b32 a174, 0
v_accvgpr_write_b32 a175, 0
K 209,170,171
X 173
X 174
K 216,172,173
X 175
v_mov_b32 v194, 0
K 217,174,175
v_mov_b32 v195, 0
v_mov_b32 v202, 0
v_mov_b32 v203, 0
v_mov_b32 v210, 0
v_mov_b32 v211, 0
Mvav 124,156,212
v_cndmask_b32_e64 v188, v241, v188, s[72:73]
v_cndmask_b32_e64 v189, v241, v189, s[74:75]
v_cndmask_b32_e64 v190, v241, v190, s[76:77]
v_cndmask_b32_e64 v191, v241, v191, s[78:79]
X 188
X 189
X 190
K 218,188,189
X 191
s_nop 0
K 219,190,191
Maav 168,180,208
)"
R"(s_mul_i32 s95, s92, 112640
U 64,64,13
U 65,65,13
U 66,66,13
U 67,67,13
s_add_u32 s95, s95, s71
Maav 172,180,216
U 68,68,13
U 69,69,13
U 70,70,13
U 71,71,13
K 64,64,65
W 2
Zvav 32,128,192
K 65,66,67
K 66,68,69
K 67,70,71
s_add_u32 s93, s67, 131072
buffer_store_dwordx4 v[64:67], v1, s[8:11], s93 offen offset:0 sc1
s_add_u32 m0, s95, 20480
s_nop 0
Zvav 64,128,200
U 72,72,13
U 73,73,13
U 74,74,13
U 75,75,13
U 76,76,13
U 77,77,13
U 78,78,13
U 79,79,13
K 72,72,73
K 73,74,75
K 74,76,77
K 75,78,79
buffer_store_dwordx4 v[72:75], v1, s[8:11], s93 offen offset:1024 sc1
U 80,80,13
U 81,81,13
U 82,82,13
U 83,83,13
U 84,84,13
U 85,85,13
U 86,86,13
U 87,87,13
K 80,80,81
K 81,82,83
K 82,84,85
K 83,86,87
buffer_store_dwordx4 v[80:83], v1, s[8:11], s93 offen offset:2048 sc1
U 88,88,13
U 89,89,13
U 90,90,13
U 91,91,13
U 92,92,13
U 93,93,13
U 94,94,13
U 95,95,13
K 88,88,89
K 89,90,91
K 90,92,93
K 91,94,95
buffer_store_dwordx4 v[88:91], v1, s[8:11], s93 offen offset:3072 sc1
U 96,96,14
U 97,97,14
U 98,98,14
U 99,99,14
U 100,100,14
U 101,101,14
U 102,102,14
U 103,103,14
K 96,96,97
K 97,98,99
K 98,100,101
K 99,102,103
s_add_u32 s93, s67, 262144
buffer_store_dwordx4 v[96:99], v1, s[8:11], s93 offen offset:0 sc1
s_nop 1
Zvav 96,128,208
U 104,104,14
U 105,105,14
U 106,106,14
U 107,107,14
U 108,108,14
U 109,109,14
U 110,110,14
U 111,111,14
K 104,104,105
K 105,106,107
K 106,108,109
K 107,110,111
buffer_store_dwordx4 v[104:107], v1, s[8:11], s93 offen offset:1024 sc1
U 112,112,14
U 113,113,14
U 114,114,14
U 115,115,14
U 116,116,14
U 117,117,14
U 118,118,14
U 119,119,14
K 112,112,113
K 113,114,115
K 114,116,117
K 115,118,119
buffer_store_dwordx4 v[112:115], v1, s[8:11], s93 offen offset:2048 sc1
U 120,120,14
U 121,121,14
U 122,122,14
U 123,123,14
U 124,124,14
U 125,125,14
U 126,126,14
U 127,127,14
K 120,120,121
K 121,122,123
K 122,124,125
K 123,126,127
buffer_store_dwordx4 v[120:123], v1, s[8:11], s93 offen offset:3072 sc1
U 128,128,15
U 129,129,15
U 130,130,15
U 131,131,15
U 132,132,15
U 133,133,15
U 134,134,15
U 135,135,15
K 128,128,129
K 129,130,131
K 130,132,133
K 131,134,135
s_add_u32 s93, s67, 393216
buffer_store_dwordx4 v[128:131], v1, s[8:11], s93 offen offset:0 sc1
s_nop 1
Zvav 128,128,216
Ra 152,242,55296
Ra 156,242,56320
U 136,136,15
U 137,137,15
U 138,138,15
U 139,139,15
Zvav 132,132,216
D 16,40,94,0
U 140,140,15
U 141,141,15
U 142,142,15
U 143,143,15
K 136,136,137
Zvav 100,132,208
K 137,138,139
K 138,140,141
K 139,142,143
Zvav 68,132,200
D 17,40,94,1024
Zvav 36,132,192
Zvav 40,136,192
D 18,40,94,2048
Zvav 72,136,200
Zvav 104,136,208
buffer_store_dwordx4 v[136:139], v1, s[8:11], s93 offen offset:1024 sc1
U 144,144,15
U 145,145,15
U 146,146,15
U 147,147,15
U 148,148,15
Zvav 136,136,216
U 149,149,15
U 150,150,15
U 151,151,15
K 144,144,145
K 145,146,147
K 146,148,149
Zvav 140,140,216
D 19,40,94,3072
s_add_u32 m0, s95, 24576
K 147,150,151
Zvav 108,140,208
Zvav 76,140,200
buffer_load_dwordx4 v20, s[40:43], s94 offen lds
s_waitcnt vmcnt(24)
v_cmp_eq_u64 vcc, s[12:13], v[28:29]
s_cmp_eq_u64 vcc, -1
s_cselect_b32 s46, 79691776, 0
s_add_u32 s81, s58, 0
Zvav 44,140,192
s_and_b32 s81, s81, 3
s_lshl_b32 s81, s81, 12
s_add_u32 s81, s81, s49
s_add_u32 s82, s81, 131072
W 0
Zvav 48,144,192
buffer_load_dwordx4 a[184:187], v1, s[44:47], s81 offen offset:0 sc1
Zvav 80,144,200
Zvav 112,144,208
buffer_store_dwordx4 v[144:147], v1, s[8:11], s93 offen offset:2048 sc1
U 152,152,15
U 153,153,15
U 154,154,15
U 155,155,15
U 156,156,15
Zvav 144,144,216
U 157,157,15
U 158,158,15
U 159,159,15
K 152,152,153
K 153,154,155
K 154,156,157
Zvav 148,148,216
buffer_load_dwordx4 a[200:203], v1, s[44:47], s82 offen offset:0 sc1
K 155,158,159
Zvav 116,148,208
Zvav 84,148,200
buffer_load_dwordx4 a[188:191], v1, s[44:47], s81 offen offset:1024 sc1
Zvav 52,148,192
Zvav 56,152,192
buffer_load_dwordx4 a[204:207], v1, s[44:47], s82 offen offset:1024 sc1
Zvav 88,152,200
Zvav 120,152,208
buffer_store_dwordx4 v[152:155], v1, s[8:11], s93 offen offset:3072 sc1
s_nop 1
Zvav 152,152,216
Zvav 156,156,216
buffer_load_dwordx4 a[192:195], v1, s[44:47], s81 offen offset:2048 sc1
Zvav 124,156,208
Zvav 92,156,200
buffer_load_dwordx4 a[208:211], v1, s[44:47], s82 offen offset:2048 sc1
Zvav 60,156,192
buffer_load_dwordx4 a[196:199], v1, s[44:47], s81 offen offset:3072 sc1
buffer_load_dwordx4 a[212:215], v1, s[44:47], s82 offen offset:3072 sc1
s_add_u32 s94, s58, 1
s_and_b32 s94, s94, 3
s_mul_i32 s94, s94, 163840
s_add_u32 s94, s94, s48
s_mul_i32 s95, s92, 10240
s_add_u32 s95, s95, s71
s_add_u32 m0, s95, 143360
s_nop 0
D 16,40,94,0
D 17,40,94,1024
D 18,40,94,2048
D 19,40,94,3072
s_add_u32 m0, s95, 147456
s_nop 0
buffer_load_dwordx4 v20, s[40:43], s94 offen lds
s_add_u32 s81, s58, 1
s_and_b32 s81, s81, 3
s_lshl_b32 s81, s81, 12
s_add_u32 s81, s81, s49
s_add_u32 s82, s81, 131072
buffer_load_dwordx4 a[216:219], v1, s[44:47], s81 offen offset:0 sc1
buffer_load_dwordx4 a[232:235], v1, s[44:47], s82 offen offset:0 sc1
buffer_load_dwordx4 a[220:223], v1, s[44:47], s81 offen offset:1024 sc1
buffer_load_dwordx4 a[236:239], v1, s[44:47], s82 offen offset:1024 sc1
buffer_load_dwordx4 a[224:227], v1, s[44:47], s81 offen offset:2048 sc1
buffer_load_dwordx4 a[240:243], v1, s[44:47], s82 offen offset:2048 sc1
buffer_load_dwordx4 a[228:231], v1, s[44:47], s81 offen offset:3072 sc1
buffer_load_dwordx4 a[244:247], v1, s[44:47], s82 offen offset:3072 sc1
s_mov_b32 s70, 1
s_branch .Lend_45%=
.Lpd_34%=:
Ra 128,242,0
Ra 132,242,1024
Ra 136,242,2048
Ra 140,242,3072
W 0
Zvaa 128,128,0
Ra 144,242,4096
Ra 148,242,5120
Ra 152,242,6144
Ra 156,242,7168
Zvaa 132,128,16
s_and_b32 s80, s14, 1
v_lshrrev_b32 v20, 4, v1
v_add_u32 v20, 0, v20
v_mul_u32_u24 v26, 205, v20
Zvaa 136,128,32
s_lshl_b32 s80, s80, 14
v_lshrrev_b32 v26, 12, v26
v_mul_u32_u24 v25, 20, v26
v_sub_u32 v20, v20, v25
Zvaa 140,128,48
s_add_u32 s80, s80, 131072
v_lshrrev_b32 v25, 1, v26
v_and_b32 v25, 2, v25
v_xor_b32 v20, v20, v25
Mvaa 128,132,4
v_add_u32 v12, s80, v1
v_mul_u32_u24 v26, 80, v26
v_add_lshl_u32 v16, v20, v26, 4
v_lshrrev_b32 v20, 4, v1
Mvaa 132,132,20
Ra 128,242,8192
Ra 64,12,8192
v_add_u32 v20, 64, v20
v_mul_u32_u24 v26, 205, v20
Mvaa 136,132,36
v_lshrrev_b32 v26, 12, v26
Ra 68,12,9216
v_mul_u32_u24 v25, 20, v26
v_sub_u32 v20, v20, v25
Mvaa 140,132,52
v_lshrrev_b32 v25, 1, v26
Ra 72,12,10240
v_and_b32 v25, 2, v25
v_xor_b32 v20, v20, v25
Mvaa 128,136,8
v_mul_u32_u24 v26, 80, v26
Ra 76,12,11264
v_add_lshl_u32 v17, v20, v26, 4
v_subrev_u32 v17, 1024, v17
Mvaa 132,136,24
v_lshrrev_b32 v20, 4, v1
Ra 80,12,12288
v_add_u32 v20, 128, v20
v_mul_u32_u24 v26, 205, v20
Mvaa 136,136,40
v_lshrrev_b32 v26, 12, v26
Ra 132,242,9216
Ra 84,12,13312
v_mul_u32_u24 v25, 20, v26
Mvaa 140,136,56
v_sub_u32 v20, v20, v25
v_lshrrev_b32 v25, 1, v26
Ra 88,12,14336
v_and_b32 v25, 2, v25
Mvaa 128,140,12
v_xor_b32 v20, v20, v25
v_mul_u32_u24 v26, 80, v26
Ra 92,12,15360
v_add_lshl_u32 v18, v20, v26, 4
Mvaa 132,140,28
v_subrev_u32 v18, 2048, v18
v_lshrrev_b32 v20, 4, v1
Ra 96,12,0
v_add_u32 v20, 192, v20
Mvaa 136,140,44
v_mul_u32_u24 v26, 205, v20
v_lshrrev_b32 v26, 12, v26
Ra 100,12,1024
v_mul_u32_u24 v25, 20, v26
Mvaa 140,140,60
v_sub_u32 v20, v20, v25
v_lshrrev_b32 v25, 1, v26
Ra 136,242,10240
W 13
Zvaa 148,144,16
Ra 104,12,2048
v_and_b32 v25, 2, v25
v_xor_b32 v20, v20, v25
v_mul_u32_u24 v26, 80, v26
Zvaa 152,144,32
Ra 108,12,3072
v_add_lshl_u32 v19, v20, v26, 4
v_subrev_u32 v19, 3072, v19
v_lshrrev_b32 v20, 4, v1
Zvaa 156,144,48
Ra 112,12,4096
v_add_u32 v20, 256, v20
v_mul_u32_u24 v26, 205, v20
v_lshrrev_b32 v26, 12, v26
Mvaa 148,148,20
Ra 116,12,5120
v_mul_u32_u24 v25, 20, v26
v_sub_u32 v20, v20, v25
v_lshrrev_b32 v25, 1, v26
Mvaa 152,148,36
Ra 120,12,6144
v_and_b32 v25, 2, v25
v_xor_b32 v20, v20, v25
Ra 140,242,11264
Mvaa 156,148,52
v_mul_u32_u24 v26, 80, v26
Ra 124,12,7168
v_add_lshl_u32 v20, v20, v26, 4
v_lshrrev_b32 v26, 6, v1
Mvaa 148,152,24
v_mul_u32_u24 v26, 320, v26
v_bfe_u32 v25, v1, 4, 2
v_lshl_add_u32 v26, v25, 3, v26
v_bfe_u32 v25, v1, 8, 1
Mvaa 152,152,40
v_lshlrev_b32 v25, 5, v25
v_cndmask_b32_e64 v128, v241, v128, s[72:73]
v_cndmask_b32_e64 v129, v241, v129, s[74:75]
v_cndmask_b32_e64 v130, v241, v130, s[76:77]
Mvaa 156,152,56
v_cndmask_b32_e64 v131, v241, v131, s[78:79]
v_max3_f32 v4, v128, v129, v130
v_max3_f32 v4, v4, v131, v252
v_max3_f32 v5, v132, v133, v134
Mvaa 148,156,28
v_max3_f32 v5, v5, v135, v253
v_max3_f32 v6, v136, v137, v138
v_max3_f32 v6, v6, v139, v254
v_max3_f32 v7, v140, v141, v142
Mvaa 152,156,44
v_max3_f32 v7, v7, v143, v255
v_mov_b32 v8, v4
v_mov_b32 v9, v5
v_mov_b32 v10, v6
v_mov_b32 v11, v7
v_permlane32_swap_b32 v4, v8
v_permlane32_swap_b32 v5, v9
)"
R"(v_permlane32_swap_b32 v6, v10
v_permlane32_swap_b32 v7, v11
v_max_f32 v4, v4, v8
v_max_f32 v5, v5, v9
v_max_f32 v6, v6, v10
v_max_f32 v7, v7, v11
v_mov_b32 v8, v4
v_mov_b32 v9, v5
v_mov_b32 v10, v6
v_mov_b32 v11, v7
v_permlane16_swap_b32 v4, v8
v_permlane16_swap_b32 v5, v9
v_permlane16_swap_b32 v6, v10
v_permlane16_swap_b32 v7, v11
v_max_f32 v4, v4, v8
v_max_f32 v5, v5, v9
v_max_f32 v6, v6, v10
v_max_f32 v7, v7, v11
v_sub_f32 v4, 0, v4
v_sub_f32 v5, 0, v5
v_sub_f32 v6, 0, v6
v_sub_f32 v7, 0, v7
v_mov_b32 v224, v4
v_mov_b32 v225, v4
v_mov_b32 v226, v4
v_mov_b32 v227, v4
v_mov_b32 v228, v5
v_mov_b32 v229, v5
v_mov_b32 v230, v5
v_mov_b32 v231, v5
v_mov_b32 v232, v6
v_mov_b32 v233, v6
v_mov_b32 v234, v6
v_mov_b32 v235, v6
v_mov_b32 v236, v7
v_mov_b32 v237, v7
v_mov_b32 v238, v7
v_mov_b32 v239, v7
Mvaa 156,156,60
v_add_u32 v21, v26, v25
v_sub_u32 v22, v26, v25
v_add_u32 v23, 61440, v21
W 1
Cvaa 168,128,32,232
v_add_u32 v24, 61440, v22
Ra 144,242,12288
v_add_u32 v25, 122880, v21
v_add_u32 v26, 122880, v22
Cvaa 172,128,48,236
s_mul_i32 s94, s59, 655360
s_mul_i32 s95, s58, 320
s_add_u32 s94, s94, s95
Ra 148,242,13312
Mvaa 168,132,36
Ra 152,242,14336
Ra 156,242,15360
s_add_u32 s40, s26, s94
s_addc_u32 s41, s27, 0
Mvaa 172,132,52
s_and_b32 s41, s41, 0xffff
s_mov_b32 s42, 5242880
s_mov_b32 s43, 0x20000
s_sub_u32 s42, s42, s94
Mvaa 168,136,40
s_mov_b64 s[44:45], s[8:9]
s_mov_b32 s47, 0x20000
s_mul_i32 s48, s14, 20480
s_and_b32 s94, s14, 1
Mvaa 172,136,56
s_lshl_b32 s95, s57, 1
s_add_u32 s94, s94, s95
s_lshl_b32 s54, s94, 3
s_add_u32 s54, s54, s59
Mvaa 168,140,44
Ra 128,242,24576
Ra 132,242,25600
Ra 136,242,26624
s_lshl_b32 s54, s54, 6
Mvaa 172,140,60
Ra 140,242,27648
s_xor_b32 s55, s54, 15872
s_add_u32 s54, s54, 20480
W 4
Cvaa 188,144,48,236
s_add_u32 s55, s55, 20480
s_and_b32 s95, s14, 2
s_lshl_b32 s94, s94, 2
s_add_u32 s94, s94, s95
Mvaa 188,148,52
s_lshl_b32 s95, s95, 4
s_add_u32 s55, s55, s95
s_lshl_b32 s49, s94, 17
s_lshl_b32 s95, s59, 14
Mvaa 188,152,56
A 128,128,224
A 129,129,224
A 130,130,224
A 131,131,224
X 128
X 129
X 130
K 192,128,129
X 131
A 132,132,228
K 193,130,131
A 133,133,228
A 134,134,228
A 135,135,228
X 132
X 133
X 134
K 200,132,133
X 135
A 136,136,232
K 201,134,135
A 137,137,232
A 138,138,232
A 139,139,232
X 136
X 137
X 138
K 208,136,137
X 139
A 140,140,236
K 209,138,139
A 141,141,236
A 142,142,236
A 143,143,236
X 140
X 141
X 142
K 216,140,141
X 143
v_mov_b32 v194, 0
K 217,142,143
v_mov_b32 v195, 0
v_cndmask_b32_e64 v148, v241, v148, s[72:73]
v_cndmask_b32_e64 v149, v241, v149, s[74:75]
v_cndmask_b32_e64 v150, v241, v150, s[76:77]
v_cndmask_b32_e64 v151, v241, v151, s[78:79]
A 148,148,228
A 149,149,228
A 150,150,228
A 151,151,228
X 148
X 149
X 150
K 202,148,149
X 151
A 152,152,232
K 203,150,151
A 153,153,232
A 154,154,232
A 155,155,232
X 152
X 153
X 154
K 210,152,153
X 155
A 156,156,236
K 211,154,155
A 157,157,236
A 158,158,236
A 159,159,236
X 156
X 157
X 158
K 218,156,157
X 159
s_nop 0
K 219,158,159
Mvaa 188,156,60
s_add_u32 s49, s49, s95
s_xor_b32 s50, s49, 16252928
s_add_u32 s49, s49, 39845888
W 0
Zvav 32,128,192
s_and_b32 s80, s14, 1
s_add_u32 s50, s50, 39845888
s_lshr_b32 s94, s15, 3
s_lshl_b32 s94, s94, 3
Zvav 64,128,200
s_add_u32 s94, s94, s14
s_lshl_b32 s94, s94, 4
s_add_u32 s94, s94, s59
s_mul_i32 s52, s94, 5120
Zvav 96,128,208
s_lshl_b32 s80, s80, 14
s_add_u32 s52, s52, 56623104
s_lshl_b32 s53, s94, 3
s_add_u32 s53, s53, 65536
Zvav 128,128,216
s_xor_b32 s62, s62, 0x10000
s_add_u32 s80, s80, 98304
v_add_u32 v8, s80, v1
Ra 144,242,28672
Zvav 132,132,216
Ra 148,242,29696
Ra 152,242,30720
Ra 156,242,31744
Zvav 100,132,208
Zvav 68,132,200
Zvav 36,132,192
Zvav 40,136,192
Zvav 72,136,200
Zvav 104,136,208
Zvav 136,136,216
Zvav 140,140,216
Zvav 108,140,208
Zvav 76,140,200
s_waitcnt vmcnt(0) lgkmcnt(0)
s_barrier
v_xor_b32 v242, 0x10000, v242
ds_read_b128 v[248:251], v8
Zvav 44,140,192
Ra 128,242,40960
Zvav 48,144,192
Zvav 80,144,200
X 172
X 173
Zvav 112,144,208
X 174
K 220,172,173
Ra 132,242,41984
Zvav 144,144,216
X 175
v_cndmask_b32_e64 v168, v241, v168, s[72:73]
K 221,174,175
Zvav 148,148,216
v_cndmask_b32_e64 v169, v241, v169, s[74:75]
v_cndmask_b32_e64 v170, v241, v170, s[76:77]
v_cndmask_b32_e64 v171, v241, v171, s[78:79]
Zvav 116,148,208
X 168
X 169
Zvav 84,148,200
Ra 136,242,43008
X 170
K 212,168,169
Zvav 52,148,192
W 3
v_mov_b32 v224, v250
v_mov_b32 v225, v250
v_mov_b32 v226, v250
Zvav 56,152,192
v_mov_b32 v227, v250
v_mov_b32 v228, v251
v_mov_b32 v229, v251
v_mov_b32 v230, v251
Zvav 88,152,200
v_mov_b32 v231, v251
v_mov_b32 v232, v248
v_mov_b32 v233, v248
v_mov_b32 v234, v248
Zvav 120,152,208
v_mov_b32 v235, v248
v_mov_b32 v236, v249
v_mov_b32 v237, v249
v_mov_b32 v238, v249
Zvav 152,152,216
v_mov_b32 v239, v249
X 171
v_mov_b32 v204, 0
Zvav 156,156,216
K 213,170,171
v_mov_b32 v205, 0
v_mov_b32 v196, 0
v_mov_b32 v197, 0
Zvav 124,156,208
Ra 140,242,44032
v_mov_b32 v198, 0
v_mov_b32 v199, 0
v_mov_b32 v206, 0
Zvav 92,156,200
v_mov_b32 v207, 0
v_mov_b32 v214, 0
v_mov_b32 v215, 0
v_cndmask_b32_e64 v188, v241, v188, s[72:73]
Zvav 60,156,192
v_cndmask_b32_e64 v189, v241, v189, s[74:75]
v_cndmask_b32_e64 v190, v241, v190, s[76:77]
v_cndmask_b32_e64 v191, v241, v191, s[78:79]
X 188
X 189
X 190
K 222,188,189
X 191
s_nop 0
K 223,190,191
s_nop 1
s_branch .Lsp2_39%=
.Lmoe_46%=:
W 0
Cvaa 160,128,0,224
Ra 144,242,4096
Ra 148,242,5120
Cvaa 164,128,16,228
Ra 152,242,6144
v_bfe_u32 v11, v1, 4, 3
Cvaa 168,128,32,232
X 184
Cvaa 172,128,48,236
X 185
Mvaa 160,132,4
v_lshlrev_b32 v11, 3, v11
s_xor_b32 s62, s62, 0x10000
Mvaa 164,132,20
buffer_load_dwordx2 v[8:9], v11, s[8:11], 0 offen offset:3584 sc1
s_cmp_eq_u32 s14, 0
s_cselect_b32 s20, s30, s32
s_cselect_b32 s21, s31, s33
Mvaa 168,132,36
X 186
Mvaa 172,132,52
s_and_b32 s21, s21, 0xffff
K 214,184,185
Mvaa 160,136,8
Ra 156,242,7168
s_mov_b32 s22, 2211840
Mvaa 164,136,24
X 187
Mvaa 168,136,40
s_mov_b32 s23, 0x20000
v_lshrrev_b32 v4, 6, v1
Mvaa 172,136,56
X 188
Mvaa 160,140,12
v_mul_u32_u24 v4, 3456, v4
v_lshrrev_b32 v5, 4, v1
Mvaa 164,140,28
Ra 128,242,16384
K 215,186,187
Mvaa 168,140,44
v_xor_b32 v5, v5, v1
v_and_b32 v5, 32, v5
Mvaa 172,140,60
X 189
v_smfmac_f32_16x16x64_bf16 a[160:163], a[176:179], v[192:199], v240
v_and_b32 v6, 16, v1
v_add3_u32 v4, v4, v5, v6
v_smfmac_f32_16x16x64_bf16 a[164:167], a[176:179], v[200:207], v240
X 190
K 222,188,189
X 191
s_nop 0
K 223,190,191
v_smfmac_f32_16x16x64_bf16 a[168:171], a[176:179], v[208:215], v240
v_add_u32 v5, 54272, v4
Ra 132,242,17408
v_smfmac_f32_16x16x64_bf16 a[172:175], a[176:179], v[216:223], v240
v_add_u32 v6, 108544, v4
W 2
Cvaa 176,144,0,224
v_add_u32 v7, 162816, v4
s_lshl_b32 s94, s85, 6
Cvaa 180,144,16,228
s_mul_i32 s95, s14, 20480
s_add_u32 s95, s95, 98304
Cvaa 184,144,32,232
Ra 136,242,18432
s_add_u32 m0, s95, 0
Cvaa 188,144,48,236
X 160
Mvaa 176,148,4
X 161
Mvaa 180,148,20
D 4,20,94,0
Ra 140,242,19456
Mvaa 184,148,36
X 162
Mvaa 188,148,52
D 5,20,94,1024
K 192,160,161
Mvaa 176,152,8
X 163
Mvaa 180,152,24
D 6,20,94,2048
Ra 144,242,20480
Mvaa 184,152,40
X 164
Mvaa 188,152,56
D 7,20,94,3072
s_add_u32 s94, s94, 221184
Mvaa 176,156,12
s_add_u32 m0, s95, 4096
K 193,162,163
Mvaa 180,156,28
D 4,20,94,0
Ra 148,242,21504
Mvaa 184,156,44
X 165
Mvaa 188,156,60
D 5,20,94,1024
W 2
Mvav 32,128,196
X 166
Mvav 64,128,204
D 6,20,94,2048
K 200,164,165
Mvav 96,128,212
)"
R"(X 167
Mvav 128,128,220
D 7,20,94,3072
s_add_u32 s94, s94, 221184
Mvav 132,132,220
s_waitcnt vmcnt(8)
v_xor_b32 v8, s12, v8
v_xor_b32 v9, s13, v9
v_lshrrev_b32 v10, 11, v8
v_or_b32 v10, v10, v9
v_cmp_eq_u32_e64 s[86:87], 0, v10
s_cmp_eq_u64 s[86:87], -1
s_cbranch_scc0 .Lspin_60%=
.Lok_61%=:
Mvav 100,132,212
v_and_b32 v8, 2047, v8
v_lshrrev_b32 v10, 6, v8
Mvav 68,132,204
v_mul_u32_u24 v10, 81920, v10
v_bfe_u32 v9, v8, 4, 2
Mvav 36,132,196
v_lshl_add_u32 v10, v9, 10, v10
v_and_b32 v8, 15, v8
Mvav 40,136,196
X 168
Mvav 72,136,204
v_lshl_add_u32 v10, v8, 4, v10
Ra 152,242,22528
Mvav 104,136,212
Ra 156,242,23552
v_bfe_u32 v9, v1, 7, 2
Mvav 136,136,220
K 201,166,167
v_lshl_add_u32 v10, v9, 8, v10
Mvav 140,140,220
X 169
Mvav 108,140,212
v_lshrrev_b32 v9, 9, v1
v_lshl_add_u32 v10, v9, 12, v10
Mvav 76,140,204
X 170
Mvav 44,140,196
s_mul_i32 s81, s14, 40960
W 0
Mvav 48,144,196
s_add_u32 s81, s81, 131072
K 208,168,169
Mvav 80,144,204
s_mul_i32 s82, s14, 5120
s_add_u32 m0, s82, 53248
Mvav 112,144,212
X 171
Mvav 144,144,220
buffer_load_dwordx4 v10, s[8:11], s81 offen sc1 lds
s_add_u32 s81, s81, 8192
Mvav 148,148,220
X 172
Mvav 116,148,212
Ra 128,242,8192
s_add_u32 s64, s64, 1
Mvav 84,148,204
s_add_u32 m0, s82, 54272
K 209,170,171
Mvav 52,148,196
buffer_load_dwordx4 v10, s[8:11], s81 offen sc1 lds
s_add_u32 s81, s81, 8192
Mvav 56,152,196
X 173
Mvav 88,152,204
s_add_u32 m0, s82, 55296
s_nop 0
buffer_load_dwordx4 v10, s[8:11], s81 offen sc1 lds
Mvav 120,152,212
Ra 132,242,9216
Ra 136,242,10240
Ra 140,242,11264
Mvav 152,152,220
X 174
Mvav 156,156,220
s_add_u32 s81, s81, 8192
K 216,172,173
Mvav 124,156,212
s_add_u32 m0, s82, 56320
s_nop 0
buffer_load_dwordx4 v10, s[8:11], s81 offen sc1 lds
Mvav 92,156,204
X 175
Mvav 60,156,196
s_add_u32 s81, s81, 8192
W 0
Cvaa 160,128,0,224
X 176
Cvaa 164,128,16,228
s_add_u32 m0, s82, 57344
s_nop 0
buffer_load_dwordx4 v10, s[8:11], s81 offen sc1 lds
Cvaa 168,128,32,232
K 217,174,175
s_add_u32 m0, s95, 8192
Cvaa 172,128,48,236
X 177
Mvaa 160,132,4
D 4,20,94,0
Ra 144,242,12288
Mvaa 164,132,20
X 178
Mvaa 168,132,36
D 5,20,94,1024
K 194,176,177
Mvaa 172,132,52
X 179
Mvaa 160,136,8
D 6,20,94,2048
Ra 148,242,13312
Mvaa 164,136,24
X 180
Mvaa 168,136,40
Ra 152,242,14336
Ra 156,242,15360
Mvaa 172,136,56
D 7,20,94,3072
s_add_u32 s94, s94, 221184
Mvaa 160,140,12
s_add_u32 m0, s95, 12288
K 195,178,179
Mvaa 164,140,28
D 4,20,94,0
Ra 128,242,24576
Mvaa 168,140,44
X 181
Mvaa 172,140,60
D 5,20,94,1024
W 1
Cvaa 176,144,0,224
X 182
K 202,180,181
X 183
X 184
K 203,182,183
Cvaa 180,144,16,228
X 185
X 186
K 210,184,185
X 187
X 188
K 211,186,187
Cvaa 184,144,32,232
X 189
X 190
K 218,188,189
X 191
X 160
K 219,190,191
Cvaa 188,144,48,236
D 6,20,94,2048
Ra 132,242,25600
Mvaa 176,148,4
Ra 136,242,26624
Ra 140,242,27648
Mvaa 180,148,20
D 7,20,94,3072
s_add_u32 s94, s94, 221184
Mvaa 184,148,36
s_add_u32 m0, s95, 16384
Ra 144,242,28672
Mvaa 188,148,52
D 4,20,94,0
Mvaa 176,152,8
X 161
Mvaa 180,152,24
D 5,20,94,1024
Ra 148,242,29696
Mvaa 184,152,40
X 162
Mvaa 188,152,56
D 6,20,94,2048
K 196,160,161
Mvaa 176,156,12
X 163
Mvaa 180,156,28
D 7,20,94,3072
s_add_u32 s94, s94, 221184
Mvaa 184,156,44
X 164
Mvaa 188,156,60
K 197,162,163
W 2
Mvav 32,128,192
X 165
Mvav 64,128,200
X 166
Mvav 96,128,208
K 204,164,165
Ra 152,242,30720
Mvav 128,128,216
X 167
Mvav 132,132,216
X 168
Mvav 100,132,208
K 205,166,167
Ra 156,242,31744
Mvav 68,132,200
X 169
Mvav 36,132,192
X 170
Mvav 40,136,192
K 212,168,169
v_lshrrev_b32 v8, 8, v1
Mvav 72,136,200
X 171
Mvav 104,136,208
X 172
Mvav 136,136,216
v_bfe_u32 v9, v1, 6, 2
K 213,170,171
Mvav 140,140,216
X 173
Mvav 108,140,208
v_lshl_add_u32 v9, v8, 3, v9
v_lshlrev_b32 v11, 6, v9
Mvav 76,140,200
s_waitcnt vmcnt(0) lgkmcnt(0)
s_barrier
v_xor_b32 v242, 0x10000, v242
s_bitcmp1_b32 s68, s64
s_cbranch_scc1 .Lact_63%=
.Lactr_64%=:
Mvav 44,140,192
X 174
Mvav 48,144,192
K 220,172,173
v_xor_b32 v9, s14, v8
Mvav 80,144,200
X 175
Mvav 112,144,208
v_and_b32 v9, 1, v9
v_lshl_add_u32 v11, v9, 5, v11
Mvav 144,144,216
X 176
Mvav 148,148,216
K 221,174,175
Ra 128,242,0
Mvav 116,148,208
X 177
Mvav 84,148,200
v_bfe_u32 v9, v1, 4, 2
v_lshl_add_u32 v12, v9, 3, v11
Mvav 52,148,192
X 178
Mvav 56,152,192
Ra 132,242,1024
K 198,176,177
Mvav 88,152,200
v_add_u32 v11, 98304, v12
v_and_b32 v13, 0x70, v1
Mvav 120,152,208
X 179
Mvav 152,152,216
X 180
Mvav 156,156,216
Ra 136,242,2048
v_lshl_add_u32 v13, v8, 7, v13
Mvav 124,156,208
K 199,178,179
Ra 140,242,3072
Mvav 92,156,200
X 181
Mvav 60,156,192
X 182
K 206,180,181
X 183
s_nop 0
K 207,182,183
W 0
Cvaa 160,128,0,224
Ra 144,242,4096
Ra 148,242,5120
Cvaa 164,128,16,228
X 184
Cvaa 168,128,32,232
Ta 184,11,0
Ta 186,11,256
Cvaa 172,128,48,236
Ta 188,11,20480
Ta 190,11,20736
Mvaa 160,132,4
Ra 192,13,53248
Ta 196,11,2048
Mvaa 164,132,20
s_cmp_eq_u32 s14, 0
s_cselect_b32 s95, 106496, 0
Mvaa 168,132,36
X 185
Mvaa 172,132,52
s_xor_b32 s62, s62, 0x10000
W 1
Zaaa 232,184,192
Ta 198,11,2304
s_add_u32 s95, s95, 32768
Zaaa 236,188,192
Ta 200,11,22528
Ta 202,11,22784
Mvaa 160,136,8
X 186
Mvaa 164,136,24
s_add_u32 m0, s95, 0
K 214,184,185
Mvaa 168,136,40
Ra 204,13,53760
Ra 152,242,6144
Mvaa 172,136,56
X 187
Mvaa 160,140,12
Ta 208,11,4096
D 4,20,94,0
Mvaa 164,140,28
X 188
Mvaa 168,140,44
Ta 210,11,4352
D 5,20,94,1024
Mvaa 172,140,60
K 215,186,187
Ra 156,242,7168
v_smfmac_f32_16x16x64_bf16 a[160:163], a[176:179], v[192:199], v240
Ta 212,11,24576
W 5
Maaa 232,196,204
X 189
Maaa 236,200,204
D 6,20,94,2048
Ta 214,11,24832
v_smfmac_f32_16x16x64_bf16 a[164:167], a[176:179], v[200:207], v240
X 190
K 222,188,189
X 191
X 160
K 223,190,191
v_smfmac_f32_16x16x64_bf16 a[168:171], a[176:179], v[208:215], v240
D 7,20,94,3072
Ra 216,13,54272
v_smfmac_f32_16x16x64_bf16 a[172:175], a[176:179], v[216:223], v240
Ra 128,242,16384
W 4
Cvaa 176,144,0,224
s_add_u32 s94, s94, 221184
Ta 220,11,6144
Cvaa 180,144,16,228
s_add_u32 m0, s95, 4096
Ta 222,11,6400
Cvaa 184,144,32,232
X 161
Cvaa 188,144,48,236
Ra 132,242,17408
D 4,20,94,0
Mvaa 176,148,4
Ta 224,11,26624
Ta 226,11,26880
Mvaa 180,148,20
D 5,20,94,1024
W 6
Maaa 232,208,216
X 162
Maaa 236,212,216
K 192,160,161
D 6,20,94,2048
Mvaa 184,148,36
X 163
Mvaa 188,148,52
Ra 228,13,54784
Ra 136,242,18432
Mvaa 176,152,8
X 164
Mvaa 180,152,24
D 7,20,94,3072
Ta 184,11,8192
Mvaa 184,152,40
K 193,162,163
Ra 140,242,19456
Mvaa 188,152,56
X 165
Mvaa 176,156,12
s_add_u32 s94, s94, 221184
Ta 186,11,8448
Mvaa 180,156,28
X 166
Mvaa 184,156,44
s_add_u32 m0, s95, 8192
W 4
Maaa 232,220,228
Ta 188,11,28672
K 200,164,165
Maaa 236,224,228
X 167
Mvaa 188,156,60
Ra 144,242,20480
W 3
Mvav 32,128,196
Ta 190,11,28928
D 4,20,94,0
Mvav 64,128,204
)"
R"(X 168
Mvav 96,128,212
Ra 192,13,55296
D 5,20,94,1024
Mvav 128,128,220
K 201,166,167
Ta 196,11,10240
Mvav 132,132,220
X 169
Mvav 100,132,212
D 6,20,94,2048
Ra 148,242,21504
Mvav 68,132,204
s_add_u32 s64, s64, 1
Ta 198,11,10496
Mvav 36,132,196
D 7,20,94,3072
W 3
Maaa 232,184,192
X 170
Maaa 236,188,192
K 208,168,169
Ta 200,11,30720
Mvav 40,136,196
X 171
Mvav 72,136,204
s_add_u32 s94, s94, 221184
Ra 152,242,22528
Mvav 104,136,212
Ra 156,242,23552
Ta 202,11,30976
Mvav 136,136,220
X 172
Mvav 140,140,220
s_add_u32 m0, s95, 12288
K 209,170,171
Mvav 108,140,212
Ra 204,13,55808
D 4,20,94,0
Mvav 76,140,204
X 173
Mvav 44,140,196
Ta 208,11,12288
W 3
Mvav 48,144,196
D 5,20,94,1024
W 1
Maaa 232,196,204
X 174
Maaa 236,200,204
K 216,172,173
Ta 210,11,12544
Mvav 80,144,204
X 175
Mvav 112,144,212
D 6,20,94,2048
Ra 128,242,8192
Mvav 144,144,220
Ta 212,11,32768
Ta 214,11,33024
Mvav 148,148,220
X 176
Mvav 116,148,212
D 7,20,94,3072
K 217,174,175
Mvav 84,148,204
Ra 216,13,56320
s_add_u32 s94, s94, 221184
Mvav 52,148,196
X 177
Mvav 56,152,196
X 178
Mvav 88,152,204
Ra 132,242,9216
W 1
Maaa 232,208,216
s_add_u32 m0, s95, 16384
Ta 220,11,14336
Maaa 236,212,216
K 194,176,177
D 4,20,94,0
Mvav 120,152,212
Ra 136,242,10240
Ra 140,242,11264
Mvav 152,152,220
X 179
Mvav 156,156,220
Ta 222,11,14592
D 5,20,94,1024
Mvav 124,156,212
X 180
Mvav 92,156,204
Ta 224,11,34816
K 195,178,179
Mvav 60,156,196
Ta 226,11,35072
Ra 228,13,56832
W 4
Cvaa 160,128,0,224
X 181
Cvaa 164,128,16,228
D 6,20,94,2048
Ta 184,11,16384
Cvaa 168,128,32,232
Ra 144,242,12288
W 2
Maaa 232,220,228
X 182
Maaa 236,224,228
D 7,20,94,3072
K 202,180,181
Cvaa 172,128,48,236
X 183
Mvaa 160,132,4
s_add_u32 s94, s94, 221184
Ta 186,11,16640
Mvaa 164,132,20
X 184
Mvaa 168,132,36
K 203,182,183
Ta 188,11,36864
Mvaa 172,132,52
X 185
Mvaa 160,136,8
Ta 190,11,37120
Ra 192,13,57344
Mvaa 164,136,24
Ra 148,242,13312
Ta 196,11,18432
Mvaa 168,136,40
X 186
Mvaa 172,136,56
K 210,184,185
W 2
Maaa 232,184,192
Ra 152,242,14336
Ra 156,242,15360
Maaa 236,188,192
X 187
Mvaa 160,140,12
X 188
Mvaa 164,140,28
K 211,186,187
Ta 198,11,18688
Mvaa 168,140,44
X 189
Mvaa 172,140,60
Ta 200,11,38912
W 2
Cvaa 176,144,0,224
X 190
Cvaa 180,144,16,228
Ta 202,11,39168
Ra 204,13,57856
Cvaa 184,144,32,232
K 218,188,189
X 191
X 160
K 219,190,191
Cvaa 188,144,48,236
Ra 128,242,24576
Ra 132,242,25600
Mvaa 176,148,4
Ra 136,242,26624
W 3
Maaa 232,196,204
X 161
Maaa 236,200,204
X 162
Mvaa 180,148,20
K 196,160,161
Ra 140,242,27648
Mvaa 184,148,36
X 163
Mvaa 188,148,52
X 164
Mvaa 176,152,8
K 197,162,163
Ra 144,242,28672
Mvaa 180,152,24
X 165
Mvaa 184,152,40
X 166
Mvaa 188,152,56
K 204,164,165
Ra 148,242,29696
Mvaa 176,156,12
X 167
Mvaa 180,156,28
X 168
Mvaa 184,156,44
K 205,166,167
Ra 152,242,30720
Mvaa 188,156,60
W 3
Mvav 32,128,192
X 169
Mvav 64,128,200
X 170
Mvav 96,128,208
K 212,168,169
Ra 156,242,31744
Mvav 128,128,216
X 171
Mvav 132,132,216
X 172
Mvav 100,132,208
K 213,170,171
Mvav 68,132,200
X 173
Mvav 36,132,192
X 174
Mvav 40,136,192
K 220,172,173
Mvav 72,136,200
X 175
Mvav 104,136,208
Mvav 136,136,216
X 176
Mvav 140,140,216
Mvav 108,140,208
K 221,174,175
Mvav 76,140,200
s_waitcnt vmcnt(0) lgkmcnt(0)
s_barrier
v_xor_b32 v242, 0x10000, v242
s_bitcmp1_b32 s68, s64
s_cbranch_scc1 .Lact_65%=
.Lactr_66%=:
Mvav 44,140,192
X 177
Mvav 48,144,192
X 178
Mvav 80,144,200
Ra 128,242,0
K 198,176,177
Mvav 112,144,208
X 179
Mvav 144,144,216
Ra 132,242,1024
Mvav 148,148,216
X 180
Mvav 116,148,208
Ra 136,242,2048
Mvav 84,148,200
K 199,178,179
Mvav 52,148,192
X 181
Mvav 56,152,192
Ra 140,242,3072
Mvav 88,152,200
X 182
Mvav 120,152,208
Mvav 152,152,216
K 206,180,181
Mvav 156,156,216
X 183
Mvav 124,156,208
Mvav 92,156,200
K 207,182,183
Mvav 60,156,192
W 0
Cvaa 160,128,0,224
Ra 144,242,4096
Ra 148,242,5120
Cvaa 164,128,16,228
X 184
Cvaa 168,128,32,232
Ta 184,11,40960
Ta 186,11,41216
Cvaa 172,128,48,236
Ta 188,12,32768
Ta 190,12,33024
Mvaa 160,132,4
Ra 192,13,58368
Ta 196,11,43008
Mvaa 164,132,20
s_mov_b32 s20, s34
s_xor_b32 s62, s62, 0x10000
Mvaa 168,132,36
X 185
Mvaa 172,132,52
Ta 198,11,43264
W 2
Maaa 232,184,192
s_and_b32 s21, s35, 0xffff
Ta 200,12,34816
Maaa 236,188,192
X 186
Mvaa 160,136,8
s_mov_b32 s22, 2211840
Ta 202,12,35072
Mvaa 164,136,24
s_mov_b32 s23, 0x20000
K 214,184,185
Mvaa 168,136,40
Ra 204,13,58880
Ra 152,242,6144
Mvaa 172,136,56
X 187
Mvaa 160,140,12
Ta 208,11,45056
v_lshrrev_b32 v4, 8, v1
Mvaa 164,140,28
X 188
Mvaa 168,140,44
v_lshlrev_b32 v5, 5, v4
Ta 210,11,45312
Mvaa 172,140,60
v_xor_b32 v5, v5, v1
K 215,186,187
v_smfmac_f32_16x16x64_bf16 a[160:163], a[176:179], v[192:199], v240
Ra 156,242,7168
W 4
Maaa 232,196,204
X 189
Maaa 236,200,204
Ta 212,12,36864
v_and_b32 v5, 0xf0, v5
v_smfmac_f32_16x16x64_bf16 a[164:167], a[176:179], v[200:207], v240
X 190
K 222,188,189
X 191
X 160
K 223,190,191
v_smfmac_f32_16x16x64_bf16 a[168:171], a[176:179], v[208:215], v240
v_mul_u32_u24 v4, 1280, v4
Ta 214,12,37120
v_smfmac_f32_16x16x64_bf16 a[172:175], a[176:179], v[216:223], v240
v_add_u32 v4, v4, v5
W 2
Cvaa 176,144,0,224
Ra 216,13,59392
v_xor_b32 v5, 128, v4
Cvaa 180,144,16,228
Ra 128,242,16384
v_add_u32 v6, 8192, v4
Cvaa 184,144,32,232
Ta 220,11,47104
v_add_u32 v7, 12288, v5
Cvaa 188,144,48,236
Ta 222,11,47360
v_add_u32 v5, 4096, v5
Mvaa 176,148,4
X 161
Mvaa 180,148,20
Ra 132,242,17408
W 4
Maaa 232,208,216
s_mul_i32 s94, s85, 40960
Ta 224,12,38912
Maaa 236,212,216
X 162
Mvaa 184,148,36
s_mul_i32 s81, s14, 20480
K 192,160,161
Mvaa 188,148,52
Ta 226,12,39168
s_add_u32 s94, s94, s81
Mvaa 176,152,8
X 163
Mvaa 180,152,24
s_lshl_b32 s95, s14, 12
Ra 228,13,59904
Mvaa 184,152,40
Ra 136,242,18432
s_add_u32 s95, s95, 98304
Mvaa 188,152,56
X 164
Mvaa 176,156,12
Ra 140,242,19456
Ta 184,11,49152
Mvaa 180,156,28
K 193,162,163
s_add_u32 m0, s95, 0
Mvaa 184,156,44
Ta 186,11,49408
W 4
Maaa 232,220,228
X 165
Maaa 236,224,228
D 4,20,94,0
Ta 188,12,40960
Mvaa 188,156,60
Ra 144,242,20480
W 4
Mvav 32,128,196
X 166
Mvav 64,128,204
D 5,20,94,1024
K 200,164,165
Mvav 96,128,212
X 167
Mvav 128,128,220
D 6,20,94,2048
Ta 190,12,41216
Mvav 132,132,220
Ra 192,13,60416
Ta 196,11,51200
Mvav 100,132,212
)"
R"(D 7,20,94,3072
s_add_u32 s94, s94, 256
Mvav 68,132,204
X 168
Mvav 36,132,196
K 201,166,167
W 1
Maaa 232,184,192
s_add_u32 m0, s95, 8192
s_nop 0
D 4,20,94,0
Maaa 236,188,192
X 169
Mvav 40,136,196
Ra 148,242,21504
s_add_u32 s64, s64, 1
Mvav 72,136,204
X 170
Mvav 104,136,212
Ra 152,242,22528
Ra 156,242,23552
Mvav 136,136,220
D 5,20,94,1024
Ta 198,11,51456
Mvav 140,140,220
K 208,168,169
Ta 200,12,43008
Mvav 108,140,212
Ta 202,12,43264
Ra 204,13,60928
Mvav 76,140,204
D 6,20,94,2048
Ta 208,11,53248
Mvav 44,140,196
Ta 210,11,53504
W 6
Mvav 48,144,196
D 7,20,94,3072
W 2
Maaa 232,196,204
X 171
Maaa 236,200,204
X 172
Mvav 80,144,204
s_add_u32 s94, s94, 256
s_add_u32 m0, s95, 16384
Mvav 112,144,212
K 209,170,171
D 4,20,94,0
Mvav 144,144,220
X 173
Mvav 148,148,220
D 5,20,94,1024
Ra 128,242,8192
Mvav 116,148,212
X 174
Mvav 84,148,204
Ta 212,12,45056
Ta 214,12,45312
Ra 216,13,61440
Mvav 52,148,196
D 6,20,94,2048
K 216,172,173
Mvav 56,152,196
X 175
Mvav 88,152,204
D 7,20,94,3072
W 0
Maaa 232,208,216
s_add_u32 s94, s94, 256
s_add_u32 m0, s95, 24576
Maaa 236,212,216
X 176
Mvav 120,152,212
Ra 132,242,9216
Ra 136,242,10240
Ra 140,242,11264
Mvav 152,152,220
K 217,174,175
D 4,20,94,0
Mvav 156,156,220
X 177
Mvav 124,156,212
D 5,20,94,1024
Ta 220,11,55296
Mvav 92,156,204
X 178
Mvav 60,156,196
Ta 222,11,55552
Ta 224,12,47104
Ta 226,12,47360
Ra 228,13,61952
W 5
Cvaa 160,128,0,224
D 6,20,94,2048
K 194,176,177
Cvaa 164,128,16,228
X 179
Cvaa 168,128,32,232
D 7,20,94,3072
W 0
Maaa 232,220,228
s_add_u32 s94, s94, 256
s_add_u32 m0, s95, 32768
Maaa 236,224,228
X 180
Cvaa 172,128,48,236
K 195,178,179
D 4,20,94,0
Mvaa 160,132,4
X 181
Mvaa 164,132,20
D 5,20,94,1024
Ta 184,11,57344
Mvaa 168,132,36
X 182
Mvaa 172,132,52
D 6,20,94,2048
K 202,180,181
Mvaa 160,136,8
Ta 186,11,57600
Ta 188,12,49152
Ta 190,12,49408
Ra 192,13,62464
Mvaa 164,136,24
D 7,20,94,3072
s_add_u32 s94, s94, 256
Mvaa 168,136,40
X 183
Mvaa 172,136,56
Ra 144,242,12288
W 1
Maaa 232,184,192
Ra 148,242,13312
Ra 152,242,14336
Ra 156,242,15360
Maaa 236,188,192
X 184
Mvaa 160,140,12
K 203,182,183
Ta 196,11,59392
Mvaa 164,140,28
X 185
Mvaa 168,140,44
X 186
Mvaa 172,140,60
K 210,184,185
W 1
Cvaa 176,144,0,224
X 187
Cvaa 180,144,16,228
X 188
K 211,186,187
Ta 198,11,59648
Ta 200,12,51200
Ta 202,12,51456
Ra 204,13,62976
Cvaa 184,144,32,232
X 189
X 190
K 218,188,189
X 191
X 160
K 219,190,191
Cvaa 188,144,48,236
Ra 128,242,24576
Ra 132,242,25600
Mvaa 176,148,4
Ra 136,242,26624
W 3
Maaa 232,196,204
X 161
Maaa 236,200,204
X 162
Mvaa 180,148,20
K 196,160,161
Ra 140,242,27648
Mvaa 184,148,36
X 163
Mvaa 188,148,52
X 164
Mvaa 176,152,8
K 197,162,163
Ra 144,242,28672
Mvaa 180,152,24
X 165
Mvaa 184,152,40
X 166
Mvaa 188,152,56
K 204,164,165
G 252,232
Mvaa 176,156,12
X 167
Mvaa 180,156,28
G 253,233
G 254,234
Mvaa 184,156,44
X 168
Mvaa 188,156,60
G 255,235
W 1
Mvav 32,128,192
Ra 148,242,29696
G 8,236
Mvav 64,128,200
G 9,237
G 10,238
Mvav 96,128,208
G 14,239
K 252,252,8
Mvav 128,128,216
K 205,166,167
K 253,253,9
Mvav 132,132,216
K 254,254,10
K 255,255,14
Mvav 100,132,208
X 169
Mvav 68,132,200
v_and_b32 v8, 0xffff0000, v252
v_and_b32 v9, 0xffff0000, v253
Mvav 36,132,192
v_and_b32 v10, 0xffff0000, v254
v_and_b32 v14, 0xffff0000, v255
Mvav 40,136,192
v_lshlrev_b32 v252, 16, v252
v_lshlrev_b32 v253, 16, v253
Mvav 72,136,200
v_lshlrev_b32 v254, 16, v254
v_lshlrev_b32 v255, 16, v255
Mvav 104,136,208
Ra 152,242,30720
Ra 156,242,31744
Mvav 136,136,216
v_mul_f32 v15, 0xbfb8aa3b, v252
v_mul_f32 v2, 0xbfb8aa3b, v253
v_mul_f32 v3, 0xbfb8aa3b, v254
v_mul_f32 v27, 0xbfb8aa3b, v255
X 15
X 2
X 3
X 27
v_add_f32 v15, 1.0, v15
v_add_f32 v2, 1.0, v2
v_add_f32 v3, 1.0, v3
v_add_f32 v27, 1.0, v27
v_rcp_f32 v15, v15
v_rcp_f32 v2, v2
v_rcp_f32 v3, v3
v_rcp_f32 v27, v27
U 252,252,15
U 253,253,2
U 254,254,3
U 255,255,27
K 252,252,252
K 253,253,253
K 254,254,254
K 255,255,255
v_lshlrev_b32 v252, 16, v252
v_lshlrev_b32 v253, 16, v253
v_lshlrev_b32 v254, 16, v254
v_lshlrev_b32 v255, 16, v255
U 252,252,8
U 253,253,9
U 254,254,10
U 255,255,14
K 246,252,253
K 247,254,255
s_lshl_b32 s81, s14, 3
v_add_u32 v2, s81, v1
ds_write_b64 v2, v[246:247] offset:63488
Mvav 140,140,216
X 170
Mvav 108,140,208
K 212,168,169
Mvav 76,140,200
s_waitcnt vmcnt(0) lgkmcnt(0)
s_barrier
v_xor_b32 v242, 0x10000, v242
s_bitcmp1_b32 s68, s64
s_cbranch_scc1 .Lact_67%=
.Lactr_68%=:
Mvav 44,140,192
X 171
Mvav 48,144,192
X 172
Mvav 80,144,200
K 213,170,171
Ra 128,242,0
Mvav 112,144,208
X 173
Mvav 144,144,216
X 174
Mvav 148,148,216
K 220,172,173
Ra 132,242,1024
Mvav 116,148,208
X 175
Mvav 84,148,200
X 176
Mvav 52,148,192
K 221,174,175
Ra 136,242,2048
Mvav 56,152,192
X 177
Mvav 88,152,200
X 178
Mvav 120,152,208
K 198,176,177
Ra 140,242,3072
Mvav 152,152,216
X 179
Mvav 156,156,216
X 180
Mvav 124,156,208
K 199,178,179
Mvav 92,156,200
X 181
Mvav 60,156,192
X 182
K 206,180,181
X 183
s_nop 0
K 207,182,183
W 0
Cvaa 160,128,0,224
Ra 144,242,4096
Ra 148,242,5120
Cvaa 164,128,16,228
X 184
Cvaa 168,128,32,232
Ra 216,1,63488
v_lshrrev_b32 v8, 6, v1
Cvaa 172,128,48,236
X 185
Mvaa 160,132,4
v_and_b32 v9, 7, v8
s_xor_b32 s62, s62, 0x10000
Mvaa 164,132,20
s_lshl_b32 s81, s14, 2
v_xor_b32 v9, s81, v9
Mvaa 168,132,36
v_lshlrev_b32 v9, 5, v9
v_lshl_add_u32 v4, v8, 8, v9
Mvaa 172,132,52
X 186
Mvaa 160,136,8
Ra 152,242,6144
v_bfe_u32 v9, v1, 4, 2
Mvaa 164,136,24
v_lshl_add_u32 v4, v9, 3, v4
v_add_u32 v4, 98304, v4
Mvaa 168,136,40
v_xor_b32 v5, 32, v4
K 214,184,185
Mvaa 172,136,56
v_xor_b32 v6, 64, v4
v_xor_b32 v7, 96, v4
Mvaa 160,140,12
Ta 184,4,0
Ta 186,4,4096
Mvaa 164,140,28
X 187
Mvaa 168,140,44
v_and_b32 v11, 0x70, v1
v_mul_u32_u24 v11, 160, v11
Mvaa 172,140,60
Ra 156,242,7168
v_lshrrev_b32 v10, 8, v1
v_smfmac_f32_16x16x64_bf16 a[160:163], a[176:179], v[192:199], v240
X 188
K 215,186,187
v_smfmac_f32_16x16x64_bf16 a[164:167], a[176:179], v[200:207], v240
X 189
X 190
K 222,188,189
X 191
s_nop 0
K 223,190,191
v_smfmac_f32_16x16x64_bf16 a[168:171], a[176:179], v[208:215], v240
Ta 188,5,0
v_lshl_add_u32 v11, v10, 4, v11
v_smfmac_f32_16x16x64_bf16 a[172:175], a[176:179], v[216:223], v240
Ta 190,5,4096
W 3
Zaaa 232,184,216
v_bfe_u32 v10, v1, 7, 1
W 2
Cvaa 176,144,0,224
v_lshl_or_b32 v10, v10, 31, v11
s_mul_i32 s94, s85, 20480
Cvaa 180,144,16,228
Ra 128,242,16384
s_lshl_b32 s82, s14, 8
Cvaa 184,144,32,232
Ta 192,6,0
s_add_u32 s94, s94, s82
Cvaa 188,144,48,236
Ta 194,6,4096
W 3
)"
R"(Zaaa 236,188,216
s_add_u32 s94, s94, 2760704
s_lshl_b32 s71, s85, 4
Mvaa 176,148,4
s_lshl_b32 s82, s14, 3
Ra 132,242,17408
Mvaa 180,148,20
X 160
Mvaa 184,148,36
s_add_u32 s71, s71, s82
Ta 196,7,0
Mvaa 188,148,52
s_add_u32 s71, s71, 2560
W 2
Zaaa 240,192,216
X 161
Mvaa 176,152,8
Ta 198,7,4096
Ra 136,242,18432
Mvaa 180,152,24
X 162
Mvaa 184,152,40
K 192,160,161
Ta 200,4,8192
Mvaa 188,152,56
Ta 202,4,12288
W 3
Zaaa 244,196,216
Ra 140,242,19456
buffer_store_dwordx4 a[232:235], v10, s[8:11], s94 offen offset:0 sc1
Mvaa 176,156,12
X 163
Mvaa 180,156,28
X 164
Mvaa 184,156,44
K 193,162,163
Ta 204,5,8192
Mvaa 188,156,60
Ta 206,5,12288
W 3
Zaaa 248,200,216
buffer_store_dwordx4 a[236:239], v10, s[8:11], s94 offen offset:64 sc1
W 2
Mvav 32,128,196
X 165
Mvav 64,128,204
X 166
Mvav 96,128,212
K 200,164,165
Ra 144,242,20480
Mvav 128,128,220
Ta 208,6,8192
W 2
Zaaa 252,204,216
X 167
Mvav 132,132,220
Ta 210,6,12288
buffer_store_dwordx4 a[240:243], v10, s[8:11], s94 offen offset:128 sc1
Mvav 100,132,212
X 168
Mvav 68,132,204
K 201,166,167
Ra 148,242,21504
Mvav 36,132,196
Ta 212,7,8192
W 2
Zaaa 232,208,216
X 169
Mvav 40,136,196
Ta 214,7,12288
s_add_u32 s64, s64, 1
Mvav 72,136,204
X 170
Mvav 104,136,212
buffer_store_dwordx4 a[244:247], v10, s[8:11], s94 offen offset:192 sc1
K 208,168,169
Mvav 136,136,220
Ra 152,242,22528
W 1
Zaaa 236,212,216
Ra 156,242,23552
Ta 184,4,16384
Mvav 140,140,220
Ta 186,4,20480
buffer_store_dwordx4 a[248:251], v10, s[8:11], s94 offen offset:512 sc1
Mvav 108,140,212
X 171
Mvav 76,140,204
X 172
Mvav 44,140,196
K 209,170,171
W 0
Zaaa 240,184,216
X 173
Mvav 48,144,196
Ta 188,5,16384
Ta 190,5,20480
Mvav 80,144,204
X 174
Mvav 112,144,212
buffer_store_dwordx4 a[252:255], v10, s[8:11], s94 offen offset:576 sc1
K 216,172,173
Mvav 144,144,220
Ra 128,242,8192
W 1
Zaaa 244,188,216
X 175
Mvav 148,148,220
Ta 192,6,16384
Ta 194,6,20480
Mvav 116,148,212
X 176
Mvav 84,148,204
buffer_store_dwordx4 a[232:235], v10, s[8:11], s94 offen offset:640 sc1
K 217,174,175
Mvav 52,148,196
Ra 132,242,9216
W 1
Zaaa 248,192,216
X 177
Mvav 56,152,196
Ta 196,7,16384
Ta 198,7,20480
Mvav 88,152,204
X 178
Mvav 120,152,212
buffer_store_dwordx4 a[236:239], v10, s[8:11], s94 offen offset:704 sc1
K 194,176,177
Mvav 152,152,220
Ra 136,242,10240
W 1
Zaaa 252,196,216
Ra 140,242,11264
Ta 200,4,24576
Mvav 156,156,220
Ta 202,4,28672
buffer_store_dwordx4 a[240:243], v10, s[8:11], s94 offen offset:1024 sc1
Mvav 124,156,212
X 179
Mvav 92,156,204
X 180
Mvav 60,156,196
K 195,178,179
W 0
Zaaa 232,200,216
X 181
Cvaa 160,128,0,224
Ta 204,5,24576
Ta 206,5,28672
Cvaa 164,128,16,228
X 182
Cvaa 168,128,32,232
K 202,180,181
buffer_store_dwordx4 a[244:247], v10, s[8:11], s94 offen offset:1088 sc1
Cvaa 172,128,48,236
Ra 144,242,12288
W 1
Zaaa 236,204,216
X 183
Mvaa 160,132,4
Ta 208,6,24576
Ta 210,6,28672
Mvaa 164,132,20
X 184
Mvaa 168,132,36
K 203,182,183
buffer_store_dwordx4 a[248:251], v10, s[8:11], s94 offen offset:1152 sc1
Mvaa 172,132,52
Ra 148,242,13312
W 1
Zaaa 240,208,216
X 185
Mvaa 160,136,8
Ta 212,7,24576
Ta 214,7,28672
Mvaa 164,136,24
X 186
Mvaa 168,136,40
K 210,184,185
buffer_store_dwordx4 a[252:255], v10, s[8:11], s94 offen offset:1216 sc1
Mvaa 172,136,56
Ra 152,242,14336
W 1
Zaaa 244,212,216
Ra 156,242,15360
Ta 184,4,32768
Mvaa 160,140,12
Ta 186,4,36864
buffer_store_dwordx4 a[232:235], v10, s[8:11], s94 offen offset:1536 sc1
Mvaa 164,140,28
X 187
Mvaa 168,140,44
X 188
Mvaa 172,140,60
K 211,186,187
W 0
Zaaa 248,184,216
X 189
Cvaa 176,144,0,224
Ta 188,5,32768
Ta 190,5,36864
Cvaa 180,144,16,228
X 190
Cvaa 184,144,32,232
K 218,188,189
X 191
X 160
K 219,190,191
Cvaa 188,144,48,236
buffer_store_dwordx4 a[236:239], v10, s[8:11], s94 offen offset:1600 sc1
W 0
Zaaa 252,188,216
Ra 128,242,24576
Ta 192,6,32768
Mvaa 176,148,4
Ta 194,6,36864
Ra 132,242,25600
Mvaa 180,148,20
buffer_store_dwordx4 a[240:243], v10, s[8:11], s94 offen offset:1664 sc1
Ta 196,7,32768
Mvaa 184,148,36
X 161
Mvaa 188,148,52
Ra 136,242,26624
W 3
Zaaa 232,192,216
X 162
Mvaa 176,152,8
Ta 198,7,36864
K 196,160,161
Mvaa 180,152,24
X 163
Mvaa 184,152,40
buffer_store_dwordx4 a[244:247], v10, s[8:11], s94 offen offset:1728 sc1
Ra 140,242,27648
Mvaa 188,152,56
Ra 144,242,28672
W 2
Zaaa 236,196,216
X 164
Mvaa 176,156,12
K 197,162,163
buffer_store_dwordx4 a[248:251], v10, s[8:11], s94 offen offset:2048 sc1
Mvaa 180,156,28
X 165
Mvaa 184,156,44
X 166
Mvaa 188,156,60
K 204,164,165
W 1
Mvav 32,128,192
X 167
Mvav 64,128,200
buffer_store_dwordx4 a[252:255], v10, s[8:11], s94 offen offset:2112 sc1
Ra 148,242,29696
Mvav 96,128,208
X 168
Mvav 128,128,216
K 205,166,167
Ra 152,242,30720
Mvav 132,132,216
X 169
Mvav 100,132,208
buffer_store_dwordx4 a[232:235], v10, s[8:11], s94 offen offset:2176 sc1
Ra 156,242,31744
Mvav 68,132,200
X 170
Mvav 36,132,192
K 212,168,169
buffer_store_dwordx4 a[236:239], v10, s[8:11], s94 offen offset:2240 sc1
Mvav 40,136,192
X 171
Mvav 72,136,200
X 172
Mvav 104,136,208
K 213,170,171
Mvav 136,136,216
X 173
Mvav 140,140,216
X 174
Mvav 108,140,208
K 220,172,173
Mvav 76,140,200
W 0
s_barrier
v_xor_b32 v242, 0x10000, v242
s_bitcmp1_b32 s68, s64
s_cbranch_scc1 .Lact_69%=
.Lactr_70%=:
Mvav 44,140,192
X 175
Mvav 48,144,192
X 176
Mvav 80,144,200
K 221,174,175
Ra 128,242,0
Mvav 112,144,208
X 177
Mvav 144,144,216
X 178
Mvav 148,148,216
s_add_u32 s91, s64, 1
K 198,176,177
Mvav 116,148,208
s_bitset1_b32 s68, s91
Ra 132,242,1024
Mvav 84,148,200
X 179
Mvav 52,148,192
X 180
Mvav 56,152,192
Ra 136,242,2048
K 199,178,179
Mvav 88,152,200
X 181
Mvav 120,152,208
Ra 140,242,3072
Mvav 152,152,216
X 182
Mvav 156,156,216
K 206,180,181
Mvav 124,156,208
X 183
Mvav 92,156,200
K 207,182,183
Mvav 60,156,192
s_mov_b32 s85, 99
s_lshl_b32 s65, s57, 1
s_sub_u32 s65, s65, 1
s_branch .Ll0t_48%=
.Lpb_33%=:
s_add_u32 s80, s62, 131072
v_add_u32 v12, s80, v1
v_mul_f32 v32, 0x3e0293ee, v32
v_mul_f32 v33, 0x3e0293ee, v33
v_mul_f32 v34, 0x3e0293ee, v34
v_mul_f32 v35, 0x3e0293ee, v35
v_mul_f32 v36, 0x3e0293ee, v36
v_mul_f32 v37, 0x3e0293ee, v37
v_mul_f32 v38, 0x3e0293ee, v38
v_mul_f32 v39, 0x3e0293ee, v39
K 32,32,33
K 33,34,35
K 34,36,37
K 35,38,39
ds_write_b128 v12, v[32:35] offset:0
Ra 0,12,0
v_mul_f32 v64, 0x3e0293ee, v64
v_mul_f32 v65, 0x3e0293ee, v65
v_mul_f32 v66, 0x3e0293ee, v66
v_mul_f32 v67, 0x3e0293ee, v67
v_mul_f32 v68, 0x3e0293ee, v68
v_mul_f32 v69, 0x3e0293ee, v69
v_mul_f32 v70, 0x3e0293ee, v70
v_mul_f32 v71, 0x3e0293ee, v71
K 64,64,65
K 65,66,67
K 66,68,69
K 67,70,71
ds_write_b128 v12, v[64:67] offset:4096
Ra 16,12,4096
v_mul_f32 v96, 0x3e0293ee, v96
v_mul_f32 v97, 0x3e0293ee, v97
v_mul_f32 v98, 0x3e0293ee, v98
v_mul_f32 v99, 0x3e0293ee, v99
v_mul_f32 v100, 0x3e0293ee, v100
v_mul_f32 v101, 0x3e0293ee, v101
v_mul_f32 v102, 0x3e0293ee, v102
v_mul_f32 v103, 0x3e0293ee, v103
K 96,96,97
K 97,98,99
K 98,100,101
K 99,102,103
ds_write_b128 v12, v[96:99] offset:8192
Ra 32,12,8192
v_mul_f32 v128, 0x3e0293ee, v128
v_mul_f32 v129, 0x3e0293ee, v129
v_mul_f32 v130, 0x3e0293ee, v130
v_mul_f32 v131, 0x3e0293ee, v131
v_mul_f32 v132, 0x3e0293ee, v132
v_mul_f32 v133, 0x3e0293ee, v133
v_mul_f32 v134, 0x3e0293ee, v134
v_mul_f32 v135, 0x3e0293ee, v135
K 128,128,129
K 129,130,131
K 130,132,133
K 131,134,135
ds_write_b128 v12, v[128:131] offset:12288
Ra 48,12,12288
v_mul_f32 v40, 0x3e0293ee, v40
v_mul_f32 v41, 0x3e0293ee, v41
v_mul_f32 v42, 0x3e0293ee, v42
v_mul_f32 v43, 0x3e0293ee, v43
v_mul_f32 v44, 0x3e0293ee, v44
v_mul_f32 v45, 0x3e0293ee, v45
v_mul_f32 v46, 0x3e0293ee, v46
v_mul_f32 v47, 0x3e0293ee, v47
K 40,40,41
K 41,42,43
K 42,44,45
)"
R"(K 43,46,47
ds_write_b128 v12, v[40:43] offset:1024
Ra 4,12,1024
v_mul_f32 v72, 0x3e0293ee, v72
v_mul_f32 v73, 0x3e0293ee, v73
v_mul_f32 v74, 0x3e0293ee, v74
v_mul_f32 v75, 0x3e0293ee, v75
v_mul_f32 v76, 0x3e0293ee, v76
v_mul_f32 v77, 0x3e0293ee, v77
v_mul_f32 v78, 0x3e0293ee, v78
v_mul_f32 v79, 0x3e0293ee, v79
K 72,72,73
K 73,74,75
K 74,76,77
K 75,78,79
ds_write_b128 v12, v[72:75] offset:5120
Ra 20,12,5120
v_mul_f32 v104, 0x3e0293ee, v104
v_mul_f32 v105, 0x3e0293ee, v105
v_mul_f32 v106, 0x3e0293ee, v106
v_mul_f32 v107, 0x3e0293ee, v107
v_mul_f32 v108, 0x3e0293ee, v108
v_mul_f32 v109, 0x3e0293ee, v109
v_mul_f32 v110, 0x3e0293ee, v110
v_mul_f32 v111, 0x3e0293ee, v111
K 104,104,105
K 105,106,107
K 106,108,109
K 107,110,111
ds_write_b128 v12, v[104:107] offset:9216
Ra 36,12,9216
v_mul_f32 v136, 0x3e0293ee, v136
v_mul_f32 v137, 0x3e0293ee, v137
v_mul_f32 v138, 0x3e0293ee, v138
v_mul_f32 v139, 0x3e0293ee, v139
v_mul_f32 v140, 0x3e0293ee, v140
v_mul_f32 v141, 0x3e0293ee, v141
v_mul_f32 v142, 0x3e0293ee, v142
v_mul_f32 v143, 0x3e0293ee, v143
K 136,136,137
K 137,138,139
K 138,140,141
K 139,142,143
ds_write_b128 v12, v[136:139] offset:13312
Ra 52,12,13312
v_mul_f32 v48, 0x3e0293ee, v48
v_mul_f32 v49, 0x3e0293ee, v49
v_mul_f32 v50, 0x3e0293ee, v50
v_mul_f32 v51, 0x3e0293ee, v51
v_mul_f32 v52, 0x3e0293ee, v52
v_mul_f32 v53, 0x3e0293ee, v53
v_mul_f32 v54, 0x3e0293ee, v54
v_mul_f32 v55, 0x3e0293ee, v55
K 48,48,49
K 49,50,51
K 50,52,53
K 51,54,55
ds_write_b128 v12, v[48:51] offset:2048
Ra 8,12,2048
v_mul_f32 v80, 0x3e0293ee, v80
v_mul_f32 v81, 0x3e0293ee, v81
v_mul_f32 v82, 0x3e0293ee, v82
v_mul_f32 v83, 0x3e0293ee, v83
v_mul_f32 v84, 0x3e0293ee, v84
v_mul_f32 v85, 0x3e0293ee, v85
v_mul_f32 v86, 0x3e0293ee, v86
v_mul_f32 v87, 0x3e0293ee, v87
K 80,80,81
K 81,82,83
K 82,84,85
K 83,86,87
ds_write_b128 v12, v[80:83] offset:6144
Ra 24,12,6144
v_mul_f32 v112, 0x3e0293ee, v112
v_mul_f32 v113, 0x3e0293ee, v113
v_mul_f32 v114, 0x3e0293ee, v114
v_mul_f32 v115, 0x3e0293ee, v115
v_mul_f32 v116, 0x3e0293ee, v116
v_mul_f32 v117, 0x3e0293ee, v117
v_mul_f32 v118, 0x3e0293ee, v118
v_mul_f32 v119, 0x3e0293ee, v119
K 112,112,113
K 113,114,115
K 114,116,117
K 115,118,119
ds_write_b128 v12, v[112:115] offset:10240
Ra 40,12,10240
v_mul_f32 v144, 0x3e0293ee, v144
v_mul_f32 v145, 0x3e0293ee, v145
v_mul_f32 v146, 0x3e0293ee, v146
v_mul_f32 v147, 0x3e0293ee, v147
v_mul_f32 v148, 0x3e0293ee, v148
v_mul_f32 v149, 0x3e0293ee, v149
v_mul_f32 v150, 0x3e0293ee, v150
v_mul_f32 v151, 0x3e0293ee, v151
K 144,144,145
K 145,146,147
K 146,148,149
K 147,150,151
ds_write_b128 v12, v[144:147] offset:14336
Ra 56,12,14336
v_mul_f32 v56, 0x3e0293ee, v56
v_mul_f32 v57, 0x3e0293ee, v57
v_mul_f32 v58, 0x3e0293ee, v58
v_mul_f32 v59, 0x3e0293ee, v59
v_mul_f32 v60, 0x3e0293ee, v60
v_mul_f32 v61, 0x3e0293ee, v61
v_mul_f32 v62, 0x3e0293ee, v62
v_mul_f32 v63, 0x3e0293ee, v63
K 56,56,57
K 57,58,59
K 58,60,61
K 59,62,63
ds_write_b128 v12, v[56:59] offset:3072
Ra 12,12,3072
v_mul_f32 v88, 0x3e0293ee, v88
v_mul_f32 v89, 0x3e0293ee, v89
v_mul_f32 v90, 0x3e0293ee, v90
v_mul_f32 v91, 0x3e0293ee, v91
v_mul_f32 v92, 0x3e0293ee, v92
v_mul_f32 v93, 0x3e0293ee, v93
v_mul_f32 v94, 0x3e0293ee, v94
v_mul_f32 v95, 0x3e0293ee, v95
K 88,88,89
K 89,90,91
K 90,92,93
K 91,94,95
ds_write_b128 v12, v[88:91] offset:7168
Ra 28,12,7168
v_mul_f32 v120, 0x3e0293ee, v120
v_mul_f32 v121, 0x3e0293ee, v121
v_mul_f32 v122, 0x3e0293ee, v122
v_mul_f32 v123, 0x3e0293ee, v123
v_mul_f32 v124, 0x3e0293ee, v124
v_mul_f32 v125, 0x3e0293ee, v125
v_mul_f32 v126, 0x3e0293ee, v126
v_mul_f32 v127, 0x3e0293ee, v127
K 120,120,121
K 121,122,123
K 122,124,125
K 123,126,127
ds_write_b128 v12, v[120:123] offset:11264
Ra 44,12,11264
v_mul_f32 v152, 0x3e0293ee, v152
v_mul_f32 v153, 0x3e0293ee, v153
v_mul_f32 v154, 0x3e0293ee, v154
v_mul_f32 v155, 0x3e0293ee, v155
v_mul_f32 v156, 0x3e0293ee, v156
v_mul_f32 v157, 0x3e0293ee, v157
v_mul_f32 v158, 0x3e0293ee, v158
v_mul_f32 v159, 0x3e0293ee, v159
K 152,152,153
K 153,154,155
K 154,156,157
K 155,158,159
ds_write_b128 v12, v[152:155] offset:15360
Ra 60,12,15360
s_waitcnt vmcnt(0) lgkmcnt(0)
s_barrier
s_add_u32 s71, s71, 256
v_mov_b64 v[28:29], s[12:13]
s_mov_b64 exec, 1
buffer_store_dwordx2 v[28:29], off, s[8:11], s71 sc1
s_mov_b64 exec, -1
.Lbgo%=:
Ra 128,242,0
Ra 132,242,1024
Ra 136,242,2048
Ra 140,242,3072
W 0
Zvaa 128,128,0
Ra 144,242,4096
Ra 148,242,5120
Ra 152,242,6144
Ra 156,242,7168
Zvaa 132,128,16
v_lshrrev_b32 v20, 4, v1
v_add_u32 v20, 0, v20
v_mul_u32_u24 v26, 205, v20
v_lshrrev_b32 v26, 12, v26
Zvaa 136,128,32
v_mul_u32_u24 v25, 20, v26
v_sub_u32 v20, v20, v25
v_lshrrev_b32 v25, 1, v26
v_and_b32 v25, 2, v25
Zvaa 140,128,48
v_xor_b32 v20, v20, v25
v_mul_u32_u24 v26, 80, v26
v_add_lshl_u32 v16, v20, v26, 4
v_lshrrev_b32 v20, 4, v1
Mvaa 128,132,4
v_add_u32 v20, 64, v20
v_mul_u32_u24 v26, 205, v20
v_lshrrev_b32 v26, 12, v26
Ra 128,242,8192
Mvaa 132,132,20
v_mul_u32_u24 v25, 20, v26
v_sub_u32 v20, v20, v25
v_lshrrev_b32 v25, 1, v26
v_and_b32 v25, 2, v25
Mvaa 136,132,36
v_xor_b32 v20, v20, v25
v_mul_u32_u24 v26, 80, v26
v_add_lshl_u32 v17, v20, v26, 4
v_subrev_u32 v17, 1024, v17
Mvaa 140,132,52
v_lshrrev_b32 v20, 4, v1
v_add_u32 v20, 128, v20
v_mul_u32_u24 v26, 205, v20
v_lshrrev_b32 v26, 12, v26
Mvaa 128,136,8
v_mul_u32_u24 v25, 20, v26
v_sub_u32 v20, v20, v25
v_lshrrev_b32 v25, 1, v26
Ra 132,242,9216
Mvaa 132,136,24
v_and_b32 v25, 2, v25
v_xor_b32 v20, v20, v25
v_mul_u32_u24 v26, 80, v26
v_add_lshl_u32 v18, v20, v26, 4
Mvaa 136,136,40
v_subrev_u32 v18, 2048, v18
v_lshrrev_b32 v20, 4, v1
v_add_u32 v20, 192, v20
v_mul_u32_u24 v26, 205, v20
Mvaa 140,136,56
v_lshrrev_b32 v26, 12, v26
v_mul_u32_u24 v25, 20, v26
v_sub_u32 v20, v20, v25
v_lshrrev_b32 v25, 1, v26
Mvaa 128,140,12
v_and_b32 v25, 2, v25
v_xor_b32 v20, v20, v25
v_mul_u32_u24 v26, 80, v26
Ra 136,242,10240
Mvaa 132,140,28
v_add_lshl_u32 v19, v20, v26, 4
v_subrev_u32 v19, 3072, v19
v_lshrrev_b32 v20, 4, v1
v_add_u32 v20, 256, v20
Mvaa 136,140,44
v_mul_u32_u24 v26, 205, v20
v_lshrrev_b32 v26, 12, v26
v_mul_u32_u24 v25, 20, v26
v_sub_u32 v20, v20, v25
Mvaa 140,140,60
v_lshrrev_b32 v25, 1, v26
v_and_b32 v25, 2, v25
v_xor_b32 v20, v20, v25
W 3
Zvaa 144,144,0
v_mul_u32_u24 v26, 80, v26
v_add_lshl_u32 v20, v20, v26, 4
v_lshrrev_b32 v26, 6, v1
v_mul_u32_u24 v26, 320, v26
Zvaa 148,144,16
Ra 140,242,11264
v_bfe_u32 v25, v1, 4, 2
v_lshl_add_u32 v26, v25, 3, v26
v_bfe_u32 v25, v1, 8, 1
Zvaa 152,144,32
v_lshlrev_b32 v25, 5, v25
v_max3_f32 v4, v128, v129, v130
v_max3_f32 v4, v4, v131, v252
v_max3_f32 v5, v132, v133, v134
Zvaa 156,144,48
v_max3_f32 v5, v5, v135, v253
v_max3_f32 v6, v136, v137, v138
v_max3_f32 v6, v6, v139, v254
v_max3_f32 v7, v140, v141, v142
Mvaa 144,148,4
v_max3_f32 v7, v7, v143, v255
v_mov_b32 v8, v4
v_mov_b32 v9, v5
v_mov_b32 v10, v6
Mvaa 148,148,20
v_mov_b32 v11, v7
v_permlane32_swap_b32 v4, v8
v_permlane32_swap_b32 v5, v9
v_permlane32_swap_b32 v6, v10
Mvaa 152,148,36
v_permlane32_swap_b32 v7, v11
v_max_f32 v4, v4, v8
v_max_f32 v5, v5, v9
v_max_f32 v6, v6, v10
Mvaa 156,148,52
v_max_f32 v7, v7, v11
v_mov_b32 v8, v4
v_mov_b32 v9, v5
v_mov_b32 v10, v6
Mvaa 144,152,8
v_mov_b32 v11, v7
v_permlane16_swap_b32 v4, v8
v_permlane16_swap_b32 v5, v9
v_permlane16_swap_b32 v6, v10
Mvaa 148,152,24
v_permlane16_swap_b32 v7, v11
v_max_f32 v4, v4, v8
v_max_f32 v5, v5, v9
v_max_f32 v6, v6, v10
Mvaa 152,152,40
v_max_f32 v7, v7, v11
v_sub_f32 v4, 0, v4
v_sub_f32 v5, 0, v5
v_sub_f32 v6, 0, v6
Mvaa 156,152,56
v_sub_f32 v7, 0, v7
v_mov_b32 v224, v4
v_mov_b32 v225, v4
v_mov_b32 v226, v4
Mvaa 144,156,12
v_mov_b32 v227, v4
v_mov_b32 v228, v5
v_mov_b32 v229, v5
v_mov_b32 v230, v5
Mvaa 148,156,28
v_mov_b32 v231, v5
v_mov_b32 v232, v6
v_mov_b32 v233, v6
v_mov_b32 v234, v6
Mvaa 152,156,44
v_mov_b32 v235, v6
v_mov_b32 v236, v7
v_mov_b32 v237, v7
v_mov_b32 v238, v7
v_mov_b32 v239, v7
Mvaa 156,156,60
v_add_u32 v21, v26, v25
v_sub_u32 v22, v26, v25
v_add_u32 v23, 61440, v21
W 0
Cvaa 160,128,0,224
v_add_u32 v24, 61440, v22
v_add_u32 v25, 122880, v21
v_add_u32 v26, 122880, v22
s_mul_i32 s94, s59, 655360
Cvaa 164,128,16,228
s_mul_i32 s95, s58, 320
s_add_u32 s94, s94, s95
s_add_u32 s40, s26, s94
s_addc_u32 s41, s27, 0
Cvaa 168,128,32,232
Ra 144,242,12288
s_and_b32 s41, s41, 0xffff
s_mov_b32 s42, 5242880
s_mov_b32 s43, 0x20000
Cvaa 172,128,48,236
s_sub_u32 s42, s42, s94
s_mov_b64 s[44:45], s[8:9]
s_mov_b32 s47, 0x20000
s_mul_i32 s48, s14, 20480
Mvaa 160,132,4
s_and_b32 s94, s14, 1
s_lshl_b32 s95, s57, 1
s_add_u32 s94, s94, s95
s_lshl_b32 s54, s94, 3
Mvaa 164,132,20
s_add_u32 s54, s54, s59
s_lshl_b32 s54, s54, 6
s_xor_b32 s55, s54, 15872
s_add_u32 s54, s54, 20480
Mvaa 168,132,36
Ra 148,242,13312
s_add_u32 s55, s55, 20480
s_and_b32 s95, s14, 2
s_lshl_b32 s94, s94, 2
Mvaa 172,132,52
s_add_u32 s94, s94, s95
s_lshl_b32 s95, s95, 4
s_add_u32 s55, s55, s95
s_lshl_b32 s49, s94, 17
Mvaa 160,136,8
s_lshl_b32 s95, s59, 14
s_add_u32 s49, s49, s95
s_xor_b32 s50, s49, 16252928
s_add_u32 s49, s49, 39845888
Mvaa 164,136,24
s_add_u32 s50, s50, 39845888
s_lshr_b32 s94, s15, 3
s_lshl_b32 s94, s94, 3
s_add_u32 s94, s94, s14
Mvaa 168,136,40
Ra 152,242,14336
Ra 156,242,15360
s_lshl_b32 s94, s94, 4
s_add_u32 s94, s94, s59
Mvaa 172,136,56
s_mul_i32 s52, s94, 5120
s_add_u32 s52, s52, 56623104
s_lshl_b32 s53, s94, 3
s_add_u32 s53, s53, 65536
Mvaa 160,140,12
s_xor_b32 s62, s62, 0x10000
A 128,128,224
A 129,129,224
A 130,130,224
Mvaa 164,140,28
s_and_b32 s80, s14, 1
A 131,131,224
X 128
)"
R"(Mvaa 168,140,44
X 129
s_lshl_b32 s80, s80, 14
s_add_u32 s80, s80, 98304
Mvaa 172,140,60
X 130
K 192,128,129
W 0
Cvaa 176,144,0,224
X 131
A 132,132,228
v_add_u32 v8, s80, v1
Cvaa 180,144,16,228
K 193,130,131
A 133,133,228
A 134,134,228
Ra 128,242,24576
Cvaa 184,144,32,232
A 135,135,228
ds_write_b128 v8, v[4:7]
X 132
Cvaa 188,144,48,236
X 133
X 134
Mvaa 176,148,4
K 200,132,133
X 135
Ra 132,242,25600
Mvaa 180,148,20
A 136,136,232
K 201,134,135
A 137,137,232
A 138,138,232
Mvaa 184,148,36
A 139,139,232
X 136
Ra 136,242,26624
Mvaa 188,148,52
X 137
X 138
Mvaa 176,152,8
K 208,136,137
X 139
A 140,140,236
Mvaa 180,152,24
K 209,138,139
A 141,141,236
A 142,142,236
A 143,143,236
Mvaa 184,152,40
Ra 140,242,27648
X 140
Ra 144,242,28672
Mvaa 188,152,56
X 141
X 142
Mvaa 176,156,12
K 216,140,141
X 143
A 144,144,224
Mvaa 180,156,28
K 217,142,143
A 145,145,224
A 146,146,224
A 147,147,224
Mvaa 184,156,44
X 144
X 145
X 146
K 194,144,145
X 147
A 148,148,228
K 195,146,147
A 149,149,228
A 150,150,228
A 151,151,228
X 148
X 149
X 150
K 202,148,149
X 151
A 152,152,232
K 203,150,151
A 153,153,232
A 154,154,232
A 155,155,232
X 152
X 153
X 154
K 210,152,153
X 155
A 156,156,236
K 211,154,155
A 157,157,236
A 158,158,236
A 159,159,236
X 156
X 157
X 158
K 218,156,157
X 159
X 172
K 219,158,159
Mvaa 188,156,60
Ra 148,242,29696
Ra 152,242,30720
W 3
Zvav 32,128,192
Ra 156,242,31744
Zvav 64,128,200
Zvav 96,128,208
Zvav 128,128,216
Zvav 132,132,216
Zvav 100,132,208
Zvav 68,132,200
Zvav 36,132,192
Zvav 40,136,192
Zvav 72,136,200
X 173
Zvav 104,136,208
X 174
K 220,172,173
Zvav 136,136,216
X 175
Zvav 140,140,216
X 168
K 221,174,175
Zvav 108,140,208
X 169
X 170
Zvav 76,140,200
s_waitcnt vmcnt(0) lgkmcnt(0)
s_barrier
v_xor_b32 v242, 0x10000, v242
K 212,168,169
Zvav 44,140,192
X 171
X 164
Zvav 48,144,192
Ra 128,242,0
K 213,170,171
X 165
Zvav 80,144,200
X 166
Zvav 112,144,208
K 204,164,165
X 167
Ra 132,242,1024
Zvav 144,144,216
X 160
Zvav 148,148,216
K 205,166,167
X 161
Zvav 116,148,208
X 162
Ra 136,242,2048
Zvav 84,148,200
K 196,160,161
X 163
Zvav 52,148,192
X 176
K 197,162,163
Zvav 56,152,192
X 177
Ra 140,242,3072
Zvav 88,152,200
X 178
K 198,176,177
Zvav 120,152,208
X 179
X 180
Zvav 152,152,216
K 199,178,179
Zvav 156,156,216
X 181
X 182
Zvav 124,156,208
K 206,180,181
X 183
Zvav 92,156,200
K 207,182,183
Zvav 60,156,192
s_branch .Ll1_36%=
.Lf1_42%=:
W 0
Cvaa 160,128,0,224
Ra 144,242,4096
Ra 148,242,5120
Cvaa 164,128,16,228
Ra 152,242,6144
s_lshr_b32 s92, s14, 1
Cvaa 168,128,32,232
X 184
Cvaa 172,128,48,236
X 185
Mvaa 160,132,4
s_and_b32 s71, s14, 1
s_mul_i32 s71, s71, 5120
Mvaa 164,132,20
X 186
Mvaa 168,132,36
K 214,184,185
Ra 156,242,7168
Mvaa 172,132,52
X 187
Mvaa 160,136,8
v_bfe_u32 v28, v1, 4, 2
v_lshlrev_b32 v28, 3, v28
Mvaa 164,136,24
X 188
Mvaa 168,136,40
K 215,186,187
buffer_load_dwordx2 v[28:29], v28, s[8:11], s54 offen sc1
Mvaa 172,136,56
X 189
Mvaa 160,140,12
Ra 128,242,16384
s_add_u32 s94, s58, 0
Mvaa 164,140,28
X 190
Mvaa 168,140,44
K 222,188,189
s_and_b32 s94, s94, 3
Mvaa 172,140,60
X 191
v_smfmac_f32_16x16x64_bf16 a[160:163], a[176:179], v[192:199], v240
s_mul_i32 s94, s94, 163840
K 223,190,191
v_smfmac_f32_16x16x64_bf16 a[164:167], a[176:179], v[200:207], v240
Ra 132,242,17408
s_add_u32 s94, s94, s48
v_smfmac_f32_16x16x64_bf16 a[168:171], a[176:179], v[208:215], v240
s_mul_i32 s95, s92, 10240
s_add_u32 s95, s95, s71
v_smfmac_f32_16x16x64_bf16 a[172:175], a[176:179], v[216:223], v240
Ra 136,242,18432
W 3
Cvaa 176,144,0,224
X 160
Cvaa 180,144,16,228
X 161
Cvaa 184,144,32,232
X 162
Cvaa 188,144,48,236
K 192,160,161
s_add_u32 m0, s95, 0
Mvaa 176,148,4
X 163
Mvaa 180,148,20
X 164
Mvaa 184,148,36
D 16,40,94,0
K 193,162,163
Mvaa 188,148,52
Ra 140,242,19456
Ra 144,242,20480
Mvaa 176,152,8
X 165
Mvaa 180,152,24
D 17,40,94,1024
Ra 148,242,21504
Mvaa 184,152,40
X 166
Mvaa 188,152,56
K 200,164,165
Mvaa 176,156,12
D 18,40,94,2048
Ra 152,242,22528
Mvaa 180,156,28
X 167
Mvaa 184,156,44
X 172
Mvaa 188,156,60
s_cmp_eq_u32 s60, 16
s_cbranch_scc1 .Lmk_71%=
.Lmkr_72%=:
W 3
Mvav 32,128,196
D 19,40,94,3072
s_add_u32 m0, s95, 4096
Mvav 64,128,204
K 201,166,167
Ra 156,242,23552
Mvav 96,128,212
X 173
Mvav 128,128,220
buffer_load_dwordx4 v20, s[40:43], s94 offen lds
s_add_u32 s94, s94, 81920
Mvav 132,132,220
X 174
Mvav 100,132,212
s_mul_i32 s95, s92, 112640
K 216,172,173
Mvav 68,132,204
s_add_u32 s95, s95, s71
s_add_u32 m0, s95, 20480
Mvav 36,132,196
X 175
Mvav 40,136,196
D 16,40,94,0
Ra 128,242,24576
Mvav 72,136,204
X 168
Mvav 104,136,212
K 217,174,175
Mvav 136,136,220
D 17,40,94,1024
Mvav 140,140,220
X 169
Mvav 108,140,212
X 170
Mvav 76,140,204
D 18,40,94,2048
K 208,168,169
Mvav 44,140,196
Ra 132,242,25600
W 2
Mvav 48,144,196
X 171
Mvav 80,144,204
D 19,40,94,3072
s_add_u32 m0, s95, 24576
Mvav 112,144,212
X 176
Mvav 144,144,220
K 209,170,171
Ra 136,242,26624
Mvav 148,148,220
X 177
Mvav 116,148,212
buffer_load_dwordx4 v20, s[40:43], s94 offen lds
s_waitcnt vmcnt(10)
v_cmp_eq_u64 vcc, s[12:13], v[28:29]
s_cmp_eq_u64 vcc, -1
s_cselect_b32 s46, 79691776, 0
Mvav 84,148,204
X 178
Mvav 52,148,196
K 194,176,177
s_add_u32 s81, s58, 0
Mvav 56,152,196
X 179
Mvav 88,152,204
s_and_b32 s81, s81, 3
s_lshl_b32 s81, s81, 12
Mvav 120,152,212
X 180
Mvav 152,152,220
K 195,178,179
s_add_u32 s81, s81, s49
Mvav 156,156,220
X 181
Mvav 124,156,212
X 182
Mvav 92,156,204
Ra 140,242,27648
s_add_u32 s82, s81, 131072
Mvav 60,156,196
K 202,180,181
X 183
X 184
K 203,182,183
Maav 160,180,192
X 185
X 186
K 210,184,185
X 187
X 188
K 211,186,187
Maav 164,180,200
X 189
X 190
K 218,188,189
X 191
s_nop 0
K 219,190,191
Maav 168,180,208
buffer_load_dwordx4 a[184:187], v1, s[44:47], s81 offen offset:0 sc1
Ra 144,242,28672
Maav 172,180,216
Ra 148,242,29696
W 2
Mvav 32,128,192
Ra 152,242,30720
Ra 156,242,31744
Mvav 64,128,200
buffer_load_dwordx4 a[200:203], v1, s[44:47], s82 offen offset:0 sc1
Mvav 96,128,208
Mvav 128,128,216
Mvav 132,132,216
buffer_load_dwordx4 a[188:191], v1, s[44:47], s81 offen offset:1024 sc1
Mvav 100,132,208
Mvav 68,132,200
Mvav 36,132,192
buffer_load_dwordx4 a[204:207], v1, s[44:47], s82 offen offset:1024 sc1
Mvav 40,136,192
Mvav 72,136,200
Mvav 104,136,208
buffer_load_dwordx4 a[192:195], v1, s[44:47], s81 offen offset:2048 sc1
Mvav 136,136,216
)"
R"(Mvav 140,140,216
Mvav 108,140,208
buffer_load_dwordx4 a[208:211], v1, s[44:47], s82 offen offset:2048 sc1
Mvav 76,140,200
Mvav 44,140,192
W 0
Mvav 48,144,192
buffer_load_dwordx4 a[196:199], v1, s[44:47], s81 offen offset:3072 sc1
Mvav 80,144,200
Mvav 112,144,208
Mvav 144,144,216
buffer_load_dwordx4 a[212:215], v1, s[44:47], s82 offen offset:3072 sc1
s_add_u32 s94, s58, 1
Mvav 148,148,216
s_and_b32 s94, s94, 3
s_mul_i32 s94, s94, 163840
Mvav 116,148,208
s_add_u32 s94, s94, s48
s_mul_i32 s95, s92, 10240
Mvav 84,148,200
s_add_u32 s95, s95, s71
s_add_u32 m0, s95, 143360
Mvav 52,148,192
D 16,40,94,0
Mvav 56,152,192
Mvav 88,152,200
Mvav 120,152,208
D 17,40,94,1024
Mvav 152,152,216
Mvav 156,156,216
Mvav 124,156,208
D 18,40,94,2048
Mvav 92,156,200
Mvav 60,156,192
D 19,40,94,3072
s_add_u32 m0, s95, 147456
s_nop 0
buffer_load_dwordx4 v20, s[40:43], s94 offen lds
s_add_u32 s81, s58, 1
s_and_b32 s81, s81, 3
s_lshl_b32 s81, s81, 12
s_add_u32 s81, s81, s49
s_add_u32 s82, s81, 131072
buffer_load_dwordx4 a[216:219], v1, s[44:47], s81 offen offset:0 sc1
buffer_load_dwordx4 a[232:235], v1, s[44:47], s82 offen offset:0 sc1
buffer_load_dwordx4 a[220:223], v1, s[44:47], s81 offen offset:1024 sc1
buffer_load_dwordx4 a[236:239], v1, s[44:47], s82 offen offset:1024 sc1
buffer_load_dwordx4 a[224:227], v1, s[44:47], s81 offen offset:2048 sc1
buffer_load_dwordx4 a[240:243], v1, s[44:47], s82 offen offset:2048 sc1
buffer_load_dwordx4 a[228:231], v1, s[44:47], s81 offen offset:3072 sc1
buffer_load_dwordx4 a[244:247], v1, s[44:47], s82 offen offset:3072 sc1
.Lend_45%=:
s_mov_b32 s80, 40960
s_mov_b32 s81, 73728
s_bitcmp1_b32 s14, 0
s_cselect_b32 s80, 49152, s80
s_cselect_b32 s81, 98304, s81
s_cmp_lt_u32 s14, 2
s_cselect_b32 s82, s80, s81
s_cselect_b32 s83, s81, s80
v_add_u32 v2, s82, v1
v_add_u32 v3, s83, v1
s_lshl_b32 s80, s14, 9
v_lshrrev_b32 v29, 1, v1
v_add_u32 v29, s80, v29
G 4,160
G 8,161
G 5,164
G 9,165
G 6,168
G 10,169
G 7,172
G 11,173
A 4,4,8
A 5,5,9
A 6,6,10
A 7,7,11
ds_write_b64 v29, v[6:7] offset:57344
v_cmp_ngt_f32 vcc, s90, v4
s_or_b64 s[88:89], s[88:89], vcc
v_cmp_ngt_f32 vcc, s90, v5
s_or_b64 s[88:89], s[88:89], vcc
v_cmp_ngt_f32 vcc, s90, v6
s_or_b64 s[88:89], s[88:89], vcc
v_cmp_ngt_f32 vcc, s90, v7
s_or_b64 s[88:89], s[88:89], vcc
s_and_b64 s[88:89], s[88:89], s[24:25]
s_cselect_b32 s80, 1, 0
s_lshl_b32 s81, s14, 2
v_mov_b32 v8, s80
v_mov_b32 v9, s81
ds_write_b32 v9, v8 offset:59392
K 96,96,97
K 97,98,99
K 98,100,101
K 99,102,103
ds_write_b128 v2, v[96:99] offset:0
K 104,104,105
K 105,106,107
K 106,108,109
K 107,110,111
ds_write_b128 v2, v[104:107] offset:1024
K 112,112,113
K 113,114,115
K 114,116,117
K 115,118,119
ds_write_b128 v2, v[112:115] offset:2048
K 120,120,121
K 121,122,123
K 122,124,125
K 123,126,127
ds_write_b128 v2, v[120:123] offset:3072
K 128,128,129
K 129,130,131
K 130,132,133
K 131,134,135
ds_write_b128 v2, v[128:131] offset:4096
K 136,136,137
K 137,138,139
K 138,140,141
K 139,142,143
ds_write_b128 v2, v[136:139] offset:5120
K 144,144,145
K 145,146,147
K 146,148,149
K 147,150,151
ds_write_b128 v2, v[144:147] offset:6144
K 152,152,153
K 153,154,155
K 154,156,157
K 155,158,159
ds_write_b128 v2, v[152:155] offset:7168
v_xor_b32 v10, 1024, v29
v_bfe_u32 v11, v1, 2, 4
v_bfe_u32 v12, v1, 6, 2
v_lshrrev_b32 v13, 8, v1
v_bfe_u32 v14, v1, 4, 1
v_lshlrev_b32 v14, 4, v14
v_mov_b32 v15, 0x3f80
v_lshlrev_b32 v15, v14, v15
v_bfe_u32 v14, v1, 5, 1
v_xor_b32 v28, v12, v13
v_lshl_or_b32 v28, v28, 1, v14
v_cmp_eq_u32 vcc, 0, v28
s_nop 1
v_cndmask_b32 v208, 0, v15, vcc
v_mov_b32 v210, 0
v_mov_b32 v212, 0
v_mov_b32 v214, v208
v_cmp_eq_u32 vcc, 1, v28
s_nop 1
v_cndmask_b32 v209, 0, v15, vcc
v_mov_b32 v211, 0
v_mov_b32 v213, 0
v_mov_b32 v215, v209
s_waitcnt vmcnt(8) lgkmcnt(0)
s_barrier
s_cmp_eq_u32 s46, 0
s_cbranch_scc1 .Llate_84%=
ds_read_b32 v11, v11 offset:59392
ds_read_b64 v[8:9], v10 offset:57344
Rv 176,3,0
Rv 180,3,1024
Rv 184,3,2048
Rv 188,3,3072
Rv 192,3,4096
Rv 196,3,5120
Rv 200,3,6144
Rv 204,3,7168
W 6
Mvvv 32,208,176
v_cmp_ne_u32 vcc, 0, v11
s_cmp_lg_u64 vcc, 0
s_cbranch_scc1 .Lredo%=
Mvvv 36,212,176
s_add_u32 s94, s94, 81920
A 4,4,8
Mvvv 40,208,180
v_rcp_f32 v4, v4
Mvvv 44,212,180
s_mul_i32 s95, s92, 30720
W 4
Mvvv 48,208,184
A 5,5,9
Ta 128,21,0
Mvvv 52,212,184
Ta 130,21,5120
s_add_u32 s95, s95, s71
Mvvv 56,208,188
v_rcp_f32 v5, v5
Mvvv 60,212,188
s_lshl_b32 s93, s58, 12
W 4
Mvvv 64,208,192
s_add_u32 s93, s93, s50
Ta 132,22,32
Mvvv 68,212,192
Ta 134,22,5152
U 32,32,4
Mvvv 72,208,196
s_add_u32 m0, s95, 30720
U 33,33,4
Mvvv 76,212,196
U 34,34,4
W 4
Mvvv 80,208,200
Ta 136,21,64
Ta 138,21,5184
Mvvv 84,212,200
U 35,35,4
D 16,40,94,0
Mvvv 88,208,204
U 36,36,4
U 37,37,4
Mvvv 92,212,204
Ta 140,22,96
Ta 142,22,5216
W 4
Zvaa 96,128,184
U 38,38,4
D 17,40,94,1024
Zvaa 104,128,200
U 39,39,4
K 32,32,33
Zvaa 108,132,200
Ta 144,21,128
Ta 146,21,5248
Zvaa 100,132,184
Ta 148,22,160
Ta 150,22,5280
W 4
Zvaa 112,136,184
K 33,34,35
D 18,40,94,2048
Zvaa 120,136,200
K 34,36,37
K 35,38,39
Zvaa 124,140,200
Ta 152,21,192
Ta 154,21,5312
Zvaa 116,140,184
Ta 156,22,224
Ta 158,22,5344
W 4
Zvaa 128,144,184
buffer_store_dwordx4 v[32:35], v1, s[8:11], s93 offen offset:0 sc1
U 40,40,4
Zvaa 136,144,200
U 41,41,4
U 42,42,4
Zvaa 140,148,200
U 43,43,4
U 44,44,4
Zvaa 132,148,184
Ta 128,21,256
Ta 130,21,5376
Ta 132,22,288
Ta 134,22,5408
W 4
Zvaa 144,152,184
D 19,40,94,3072
s_add_u32 m0, s95, 34816
Zvaa 152,152,200
U 45,45,4
U 46,46,4
Zvaa 156,156,200
U 47,47,4
K 40,40,41
Zvaa 148,156,184
Ta 136,21,10240
Ta 138,21,15360
Ta 140,22,10272
Ta 142,22,15392
W 4
Zvaa 160,128,184
buffer_load_dwordx4 v20, s[40:43], s94 offen lds
K 41,42,43
Zvaa 168,128,200
s_add_u32 s94, s58, 2
K 42,44,45
Zvaa 172,132,200
K 43,46,47
s_and_b32 s94, s94, 3
Zvaa 164,132,184
Ta 144,21,10304
Ta 146,21,15424
Ta 148,22,10336
Ta 150,22,15456
W 4
Mvaa 96,136,188
buffer_store_dwordx4 v[40:43], v1, s[8:11], s93 offen offset:1024 sc1
U 48,48,4
Mvaa 104,136,204
U 49,49,4
U 50,50,4
Mvaa 108,140,204
s_mul_i32 s94, s94, 163840
U 51,51,4
Mvaa 100,140,188
Ta 152,21,10368
Ta 154,21,15488
Ta 156,22,10400
Ta 158,22,15520
W 4
Mvaa 112,144,188
U 52,52,4
U 53,53,4
Mvaa 120,144,204
s_add_u32 s94, s94, s48
U 54,54,4
Mvaa 124,148,204
U 55,55,4
K 48,48,49
Mvaa 116,148,188
Ta 128,21,10432
Ta 130,21,15552
Ta 132,22,10464
Ta 134,22,15584
W 4
Mvaa 128,152,188
s_mul_i32 s95, s92, 30720
K 49,50,51
Mvaa 136,152,204
K 50,52,53
K 51,54,55
Mvaa 140,156,204
s_add_u32 s95, s95, s71
buffer_store_dwordx4 v[48:51], v1, s[8:11], s93 offen offset:2048 sc1
Mvaa 132,156,188
Ta 136,21,10496
Ta 138,21,15616
Ta 140,22,10528
Ta 142,22,15648
W 4
Mvaa 144,128,188
U 56,56,4
U 57,57,4
Mvaa 152,128,204
s_add_u32 m0, s95, 81920
U 58,58,4
Mvaa 156,132,204
U 59,59,4
U 60,60,4
Mvaa 148,132,188
Ta 144,21,20480
Ta 146,21,25600
Ta 148,22,20512
Ta 150,22,25632
W 4
Mvaa 160,136,188
D 16,40,94,0
U 61,61,4
Mvaa 168,136,204
D 17,40,94,1024
D 18,40,94,2048
D 19,40,94,3072
s_add_u32 m0, s95, 86016
s_nop 0
buffer_load_dwordx4 v20, s[40:43], s94 offen lds
s_waitcnt vmcnt(13)
s_cmp_lg_u32 s70, 0
s_cbranch_scc1 .Lpd_86%=
.Lpdr_87%=:
Mvaa 172,140,204
U 62,62,4
U 63,63,4
Mvaa 164,140,188
Ta 152,21,20544
Ta 154,21,25664
Ta 156,22,20576
Ta 158,22,25696
W 4
Mvaa 96,144,192
K 56,56,57
K 57,58,59
Mvaa 104,144,208
K 58,60,61
K 59,62,63
Mvaa 108,148,208
buffer_store_dwordx4 v[56:59], v1, s[8:11], s93 offen offset:3072 sc1
U 64,64,5
Mvaa 100,148,192
Ta 128,21,20608
Ta 130,21,25728
Ta 132,22,20640
Ta 134,22,25760
W 4
Mvaa 112,152,192
U 65,65,5
U 66,66,5
Mvaa 120,152,208
U 67,67,5
U 68,68,5
Mvaa 124,156,208
U 69,69,5
U 70,70,5
Mvaa 116,156,192
Ta 136,21,20672
Ta 138,21,25792
Ta 140,22,20704
)"
R"(Ta 142,22,25824
W 4
Mvaa 128,128,192
U 71,71,5
K 64,64,65
Mvaa 136,128,208
K 65,66,67
K 66,68,69
Mvaa 140,132,208
K 67,70,71
s_add_u32 s67, s93, 131072
Mvaa 132,132,192
Ta 144,21,20736
Ta 146,21,25856
Ta 148,22,20768
Ta 150,22,25888
W 4
Mvaa 144,136,192
buffer_store_dwordx4 v[64:67], v1, s[8:11], s67 offen offset:0 sc1
U 72,72,5
Mvaa 152,136,208
U 73,73,5
U 74,74,5
Mvaa 156,140,208
U 75,75,5
U 76,76,5
Mvaa 148,140,192
Ta 152,25,10240
Ta 154,25,15360
Ta 156,26,10272
Ta 158,26,15392
W 4
Mvaa 160,144,192
U 77,77,5
U 78,78,5
Mvaa 168,144,208
s_add_u32 s81, s58, 2
U 79,79,5
Mvaa 172,148,208
K 72,72,73
K 73,74,75
Mvaa 164,148,192
Ta 128,25,10304
Ta 130,25,15424
Ta 132,26,10336
Ta 134,26,15456
W 4
Mvaa 96,152,196
K 74,76,77
K 75,78,79
Mvaa 104,152,212
buffer_store_dwordx4 v[72:75], v1, s[8:11], s67 offen offset:1024 sc1
s_and_b32 s81, s81, 3
Mvaa 108,156,212
U 80,80,5
U 81,81,5
Mvaa 100,156,196
Ta 136,25,10368
Ta 138,25,15488
Ta 140,26,10400
Ta 142,26,15520
W 4
Mvaa 112,128,196
U 82,82,5
U 83,83,5
Mvaa 120,128,212
U 84,84,5
U 85,85,5
Mvaa 124,132,212
s_lshl_b32 s81, s81, 12
U 86,86,5
Mvaa 116,132,196
Ta 144,25,10432
Ta 146,25,15552
Ta 148,26,10464
Ta 150,26,15584
W 4
Mvaa 128,136,196
U 87,87,5
K 80,80,81
Mvaa 136,136,212
K 81,82,83
K 82,84,85
Mvaa 140,140,212
K 83,86,87
buffer_store_dwordx4 v[80:83], v1, s[8:11], s67 offen offset:2048 sc1
Mvaa 132,140,196
Ta 152,25,10496
Ta 154,25,15616
Ta 156,26,10528
Ta 158,26,15648
W 4
Mvaa 144,144,196
s_add_u32 s81, s81, s49
U 88,88,5
Mvaa 152,144,212
U 89,89,5
U 90,90,5
Mvaa 156,148,212
U 91,91,5
U 92,92,5
Mvaa 148,148,196
Ta 128,25,20480
Ta 130,25,25600
Ta 132,26,20512
Ta 134,26,25632
W 4
Mvaa 160,152,196
U 93,93,5
s_add_u32 s82, s81, 131072
Mvaa 168,152,212
U 94,94,5
U 95,95,5
Mvaa 172,156,212
K 88,88,89
K 89,90,91
Mvaa 164,156,196
Ta 136,25,20544
Ta 138,25,25664
Ta 140,26,20576
Ta 142,26,25696
W 4
Mvaa 96,128,216
K 90,92,93
K 91,94,95
Mvaa 104,128,232
buffer_load_dwordx4 a[0:3], v1, s[8:11], s81 offen offset:0 sc1
Ta 144,25,20608
Mvaa 108,132,232
Ta 146,25,25728
Ta 148,26,20640
Mvaa 100,132,216
Ta 150,26,25760
W 4
Mvaa 112,136,216
buffer_store_dwordx4 v[88:91], v1, s[8:11], s67 offen offset:3072 sc1
Ta 152,25,20672
Mvaa 120,136,232
Ta 154,25,25792
Ta 156,26,20704
Mvaa 124,140,232
Ta 158,26,25824
Ta 128,25,20736
Mvaa 116,140,216
buffer_load_dwordx4 a[16:19], v1, s[8:11], s82 offen offset:0 sc1
W 5
Mvaa 128,144,216
Ta 130,25,25856
Ta 132,26,20768
Mvaa 136,144,232
Ta 134,26,25888
Ta 136,25,30720
Mvaa 140,148,232
buffer_load_dwordx4 a[4:7], v1, s[8:11], s81 offen offset:1024 sc1
Ta 138,25,35840
Mvaa 132,148,216
Ta 140,26,30752
W 7
Mvaa 144,152,216
Ta 142,26,35872
Ta 144,25,30784
Mvaa 152,152,232
buffer_load_dwordx4 a[20:23], v1, s[8:11], s82 offen offset:1024 sc1
Ta 146,25,35904
Mvaa 156,156,232
Ta 148,26,30816
Ta 150,26,35936
Mvaa 148,156,216
Ta 152,25,30848
W 9
Mvaa 160,128,216
buffer_load_dwordx4 a[8:11], v1, s[8:11], s81 offen offset:2048 sc1
Ta 154,25,35968
Mvaa 168,128,232
Ta 156,26,30880
Ta 158,26,36000
Mvaa 172,132,232
Mvaa 164,132,216
buffer_load_dwordx4 a[24:27], v1, s[8:11], s82 offen offset:2048 sc1
W 8
Mvaa 96,136,220
s_waitcnt vmcnt(11)
s_barrier
Mvaa 104,136,236
Ta 128,25,30912
Ta 130,25,36032
Mvaa 108,140,236
buffer_load_dwordx4 a[12:15], v1, s[8:11], s81 offen offset:3072 sc1
Ta 132,26,30944
Mvaa 100,140,220
Ta 134,26,36064
W 8
Mvaa 112,144,220
Ta 136,25,30976
Ta 138,25,36096
Mvaa 120,144,236
buffer_load_dwordx4 a[28:31], v1, s[8:11], s82 offen offset:3072 sc1
s_add_u32 s94, s94, 81920
Mvaa 124,148,236
s_mul_i32 s95, s92, -81920
s_add_u32 s95, s95, s71
Mvaa 116,148,220
Ta 140,26,31008
W 7
Mvaa 128,152,220
Ta 142,26,36128
s_add_u32 m0, s95, 122880
Mvaa 136,152,236
Ta 144,21,30720
Ta 146,21,35840
Mvaa 140,156,236
D 16,40,94,0
Ta 148,22,30752
Mvaa 132,156,220
Ta 150,22,35872
W 8
Mvaa 144,128,220
Ta 152,21,30784
Ta 154,21,35904
Mvaa 152,128,236
D 17,40,94,1024
Ta 156,22,30816
Mvaa 156,132,236
Ta 158,22,35936
Mvaa 148,132,220
Ta 128,21,30848
W 9
Mvaa 160,136,220
D 18,40,94,2048
Ta 130,21,35968
Mvaa 168,136,236
Ta 132,22,30880
Ta 134,22,36000
Mvaa 172,140,236
Mvaa 164,140,220
D 19,40,94,3072
W 8
Mvaa 96,144,224
s_add_u32 m0, s95, 126976
Ta 136,21,30912
Mvaa 104,144,240
Ta 138,21,36032
Ta 140,22,30944
Mvaa 108,148,240
buffer_load_dwordx4 v20, s[40:43], s94 offen lds
s_add_u32 s94, s58, 3
Mvaa 100,148,224
s_and_b32 s94, s94, 3
W 7
Mvaa 112,152,224
s_lshl_b32 s80, s58, 3
s_add_u32 s80, s80, s55
Mvaa 120,152,240
s_waitcnt vmcnt(12)
v_mov_b64 v[28:29], s[12:13]
s_mov_b64 exec, 1
buffer_store_dwordx2 v[28:29], off, s[8:11], s80 sc1
s_mov_b64 exec, -1
Mvaa 124,156,240
Ta 142,22,36064
s_mul_i32 s94, s94, 163840
Mvaa 116,156,224
Ta 144,21,30976
W 5
Mvaa 128,128,224
Ta 146,21,36096
s_add_u32 s94, s94, s48
Mvaa 136,128,240
Ta 148,22,31008
Ta 150,22,36128
Mvaa 140,132,240
s_mul_i32 s95, s92, 20480
Ta 152,23,0
Mvaa 132,132,224
Ta 154,23,5120
W 6
Mvaa 144,136,224
s_add_u32 s95, s95, s71
Ta 156,24,32
Mvaa 152,136,240
Ta 158,24,5152
s_add_u32 m0, s95, 51200
Mvaa 156,140,240
Ta 128,23,64
Ta 130,23,5184
Mvaa 148,140,224
D 16,40,94,0
W 6
Mvaa 160,144,224
Ta 132,24,96
Ta 134,24,5216
Mvaa 168,144,240
Ta 136,23,128
Ta 138,23,5248
Mvaa 172,148,240
D 17,40,94,1024
Ta 140,24,160
Mvaa 164,148,224
Ta 142,24,5280
W 8
Mvaa 96,152,228
Ta 144,23,192
Ta 146,23,5312
Mvaa 104,152,244
D 18,40,94,2048
Ta 148,24,224
Mvaa 108,156,244
Ta 150,24,5344
Mvaa 100,156,228
Ta 152,23,256
W 9
Mvaa 112,128,228
D 19,40,94,3072
s_add_u32 m0, s95, 55296
Mvaa 120,128,244
Ta 154,23,5376
Ta 156,24,288
Mvaa 124,132,244
Ta 158,24,5408
Mvaa 116,132,228
buffer_load_dwordx4 v20, s[40:43], s94 offen lds
W 8
Mvaa 128,136,228
s_add_u32 s94, s94, 81920
s_mul_i32 s95, s92, 10240
Mvaa 136,136,244
s_add_u32 s95, s95, s71
Ta 128,23,20480
Mvaa 140,140,244
Ta 130,23,25600
s_add_u32 m0, s95, 92160
Mvaa 132,140,228
Ta 132,24,20512
W 7
Mvaa 144,144,228
Ta 134,24,25632
D 16,40,94,0
Mvaa 152,144,244
Ta 136,23,20544
Ta 138,23,25664
Mvaa 156,148,244
Ta 140,24,20576
Ta 142,24,25696
Mvaa 148,148,228
D 17,40,94,1024
W 8
Mvaa 160,152,228
Ta 144,23,20608
Ta 146,23,25728
Mvaa 168,152,244
Ta 148,24,20640
Ta 150,24,25760
Mvaa 172,156,244
D 18,40,94,2048
Mvaa 164,156,228
Ta 152,23,20672
s_waitcnt vmcnt(20) lgkmcnt(9)
Mvaa 96,128,0
Ta 154,23,25792
Mvaa 104,128,16
D 19,40,94,3072
s_add_u32 m0, s95, 96256
Mvaa 108,132,16
Ta 156,24,20704
Ta 158,24,25824
Mvaa 100,132,0
Ta 128,23,20736
W 9
Mvaa 112,136,0
buffer_load_dwordx4 v20, s[40:43], s94 offen lds
s_add_u32 s81, s58, 3
Mvaa 120,136,16
Ta 130,23,25856
s_and_b32 s81, s81, 3
Mvaa 124,140,16
Ta 132,24,20768
Ta 134,24,25888
Mvaa 116,140,0
s_lshl_b32 s81, s81, 12
W 8
Mvaa 128,144,0
Ta 136,23,51200
Ta 138,23,56320
Mvaa 136,144,16
Ta 140,24,51232
Ta 142,24,56352
Mvaa 140,148,16
s_add_u32 s81, s81, s49
Mvaa 132,148,0
Ta 144,23,51264
W 9
Mvaa 144,152,0
Ta 146,23,56384
s_add_u32 s82, s81, 131072
Mvaa 152,152,16
Ta 148,24,51296
Ta 150,24,56416
Mvaa 156,156,16
Mvaa 148,156,0
buffer_load_dwordx4 a[32:35], v1, s[8:11], s81 offen offset:0 sc1
)"
R"(W 8
Mvaa 160,128,0
Ta 152,23,51328
Ta 154,23,56448
Mvaa 168,128,16
Ta 156,24,51360
Ta 158,24,56480
Mvaa 172,132,16
buffer_load_dwordx4 a[48:51], v1, s[8:11], s82 offen offset:0 sc1
Mvaa 164,132,0
Ta 128,23,51392
s_waitcnt vmcnt(22) lgkmcnt(9)
Mvaa 96,136,4
s_waitcnt vmcnt(2)
s_barrier
Mvaa 104,136,20
Ta 130,23,56512
buffer_load_dwordx4 a[36:39], v1, s[8:11], s81 offen offset:1024 sc1
Mvaa 108,140,20
Ta 132,24,51424
Ta 134,24,56544
Mvaa 100,140,4
Ta 136,23,51456
W 9
Mvaa 112,144,4
buffer_load_dwordx4 a[52:55], v1, s[8:11], s82 offen offset:1024 sc1
Ta 138,23,56576
Mvaa 120,144,20
Ta 140,24,51488
Ta 142,24,56608
Mvaa 124,148,20
Mvaa 116,148,4
buffer_load_dwordx4 a[40:43], v1, s[8:11], s81 offen offset:2048 sc1
W 8
Mvaa 128,152,4
Ta 144,25,0
Ta 146,25,5120
Mvaa 136,152,20
Ta 148,26,32
Ta 150,26,5152
Mvaa 140,156,20
buffer_load_dwordx4 a[56:59], v1, s[8:11], s82 offen offset:2048 sc1
v_and_b32 v27, 1, v12
Mvaa 132,156,4
Ta 152,25,64
W 9
Mvaa 144,128,4
Ta 154,25,5184
Mvaa 152,128,20
buffer_load_dwordx4 a[44:47], v1, s[8:11], s81 offen offset:3072 sc1
v_lshlrev_b32 v27, 1, v27
Mvaa 156,132,20
Ta 156,26,96
Ta 158,26,5216
Mvaa 148,132,4
v_xor_b32 v27, v27, v13
W 8
Mvaa 160,136,4
buffer_load_dwordx4 a[60:63], v1, s[8:11], s82 offen offset:3072 sc1
Ta 128,25,128
Mvaa 168,136,20
Ta 130,25,5248
v_and_b32 v12, 2, v12
Mvaa 172,140,20
Ta 132,26,160
Ta 134,26,5280
Mvaa 164,140,4
Ta 136,25,192
W 9
Mvaa 96,144,8
Ta 138,25,5312
v_or_b32 v12, v12, v14
Mvaa 104,144,24
Ta 140,26,224
Ta 142,26,5344
Mvaa 108,148,24
v_lshl_or_b32 v12, v27, 2, v12
Mvaa 100,148,8
Ta 144,25,256
W 9
Mvaa 112,152,8
Ta 146,25,5376
v_cmp_eq_u32_e64 s[86:87], 0, v12
Mvaa 120,152,24
Ta 148,26,288
Ta 150,26,5408
Mvaa 124,156,24
v_cndmask_b32_e64 v4, 0, v15, s[86:87]
Mvaa 116,156,8
Ta 152,21,40960
W 9
Mvaa 128,128,8
Ta 154,21,46080
Mvaa 136,128,24
v_cmp_eq_u32_e64 s[86:87], 1, v12
Ta 156,22,40992
Mvaa 140,132,24
Ta 158,22,46112
Mvaa 132,132,8
v_cndmask_b32_e64 v5, 0, v15, s[86:87]
W 8
Mvaa 144,136,8
Ta 128,21,41024
Ta 130,21,46144
Mvaa 152,136,24
v_cmp_eq_u32_e64 s[86:87], 2, v12
Ta 132,22,41056
Mvaa 156,140,24
Ta 134,22,46176
Mvaa 148,140,8
Ta 136,21,41088
W 9
Mvaa 160,144,8
Ta 138,21,46208
v_cndmask_b32_e64 v6, 0, v15, s[86:87]
Mvaa 168,144,24
Ta 140,22,41120
Ta 142,22,46240
Mvaa 172,148,24
v_cmp_eq_u32_e64 s[86:87], 3, v12
Mvaa 164,148,8
Ta 144,21,41152
W 9
Mvaa 96,152,12
Ta 146,21,46272
v_cndmask_b32_e64 v7, 0, v15, s[86:87]
Mvaa 104,152,28
Ta 148,22,41184
Ta 150,22,46304
Mvaa 108,156,28
v_cmp_eq_u32_e64 s[86:87], 4, v12
Mvaa 100,156,12
Ta 152,21,41216
W 9
Mvaa 112,128,12
Ta 154,21,46336
Mvaa 120,128,28
v_cndmask_b32_e64 v8, 0, v15, s[86:87]
Ta 156,22,41248
Mvaa 124,132,28
Ta 158,22,46368
Mvaa 116,132,12
v_cmp_eq_u32_e64 s[86:87], 5, v12
W 8
Mvaa 128,136,12
Ta 128,21,51200
Ta 130,21,56320
Mvaa 136,136,28
v_cndmask_b32_e64 v9, 0, v15, s[86:87]
Ta 132,22,51232
Mvaa 140,140,28
Ta 134,22,56352
Mvaa 132,140,12
Ta 136,21,51264
W 9
Mvaa 144,144,12
Ta 138,21,56384
v_cmp_eq_u32_e64 s[86:87], 6, v12
Mvaa 152,144,28
Ta 140,22,51296
Ta 142,22,56416
Mvaa 156,148,28
v_cndmask_b32_e64 v10, 0, v15, s[86:87]
Mvaa 148,148,12
Ta 144,21,51328
W 9
Mvaa 160,152,12
Ta 146,21,56448
v_cmp_eq_u32_e64 s[86:87], 7, v12
Mvaa 168,152,28
Ta 148,22,51360
Ta 150,22,56480
Mvaa 172,156,28
v_cndmask_b32_e64 v11, 0, v15, s[86:87]
Mvaa 164,156,12
Ta 152,21,51392
s_waitcnt vmcnt(6) lgkmcnt(9)
Mvaa 96,128,32
Ta 154,21,56512
Mvaa 104,128,48
s_mov_b32 s20, s38
Ta 156,22,51424
Mvaa 108,132,48
Ta 158,22,56544
v_bfe_u32 v28, v1, 4, 2
Mvaa 100,132,32
s_and_b32 s21, s39, 0xffff
W 8
Mvaa 112,136,32
v_lshlrev_b32 v28, 3, v28
Ta 128,21,51456
Mvaa 120,136,48
Ta 130,21,56576
buffer_load_dwordx2 v[28:29], v28, s[8:11], s55 offen sc1
Mvaa 124,140,48
s_mov_b32 s22, 2621440
Ta 132,22,51488
Mvaa 116,140,32
Ta 134,22,56608
W 8
Mvaa 128,144,32
Ta 136,23,10240
Ta 138,23,15360
Mvaa 136,144,48
s_mov_b32 s23, 0x20000
Ta 140,24,10272
Mvaa 140,148,48
Ta 142,24,15392
s_and_b32 s17, s17, 0xffff
Mvaa 132,148,32
Ta 144,23,10304
W 9
Mvaa 144,152,32
Ta 146,23,15424
s_mov_b32 s18, 2621440
Mvaa 152,152,48
Ta 148,24,10336
Ta 150,24,15456
Mvaa 156,156,48
s_mov_b32 s19, 0x20000
Mvaa 148,156,32
Ta 152,23,10368
W 9
Mvaa 160,128,32
Ta 154,23,15488
Mvaa 168,128,48
s_lshr_b32 s66, s15, 3
Ta 156,24,10400
Mvaa 172,132,48
Ta 158,24,15520
Mvaa 164,132,32
s_lshl_b32 s66, s66, 4
s_waitcnt vmcnt(5) lgkmcnt(8)
Mvaa 96,136,36
Ta 128,23,10432
Ta 130,23,15552
Mvaa 104,136,52
s_add_u32 s66, s66, s59
Ta 132,24,10464
Mvaa 108,140,52
Ta 134,24,15584
Mvaa 100,140,36
Ta 136,23,10496
W 9
Mvaa 112,144,36
Ta 138,23,15616
s_lshl_b32 s63, s66, 6
Mvaa 120,144,52
Ta 140,24,10528
Ta 142,24,15648
Mvaa 124,148,52
s_add_u32 s63, s63, 65536
Mvaa 116,148,36
Ta 144,23,30720
W 9
Mvaa 128,152,36
Ta 146,23,35840
s_mul_i32 s62, s66, 40960
Mvaa 136,152,52
Ta 148,24,30752
Ta 150,24,35872
Mvaa 140,156,52
s_add_u32 s62, s62, 56623104
Mvaa 132,156,36
Ta 152,23,30784
W 9
Mvaa 144,128,36
Ta 154,23,35904
Mvaa 152,128,52
s_lshl_b32 s66, s14, 10
Ta 156,24,30816
Mvaa 156,132,52
Ta 158,24,35936
Mvaa 148,132,36
s_add_u32 s78, s62, s66
W 8
Mvaa 160,136,36
Ta 128,23,30848
Ta 130,23,35968
Mvaa 168,136,52
s_add_u32 s79, s78, 327680
Ta 132,24,30880
Mvaa 172,140,52
Ta 134,24,36000
Mvaa 164,140,36
Ta 136,23,30912
s_waitcnt vmcnt(3) lgkmcnt(9)
Mvaa 96,144,40
Ta 138,23,36032
s_and_b32 s66, s14, 1
Mvaa 104,144,56
Ta 140,24,30944
Ta 142,24,36064
Mvaa 108,148,56
s_mul_i32 s66, s66, 327680
Mvaa 100,148,40
Ta 144,23,30976
W 9
Mvaa 112,152,40
Ta 146,23,36096
s_add_u32 s65, s62, s66
Mvaa 120,152,56
s_waitcnt vmcnt(0)
v_cmp_eq_u64 vcc, s[12:13], v[28:29]
s_cmp_eq_u64 vcc, -1
s_cbranch_scc0 .Lspin_88%=
.Lok_89%=:
Mvaa 124,156,56
s_add_u32 s81, s58, 1
Ta 148,24,31008
Mvaa 116,156,40
Ta 150,24,36128
W 8
Mvaa 128,128,40
s_add_u32 s65, s65, 4096
Ta 152,23,40960
Mvaa 136,128,56
Ta 154,23,46080
s_and_b32 s81, s81, 3
Mvaa 140,132,56
s_mov_b32 s64, 0
Ta 156,24,40992
Mvaa 132,132,40
Ta 158,24,46112
W 8
Mvaa 144,136,40
s_lshl_b32 s81, s81, 12
v_bfe_u32 v27, v1, 4, 3
Mvaa 152,136,56
Ta 128,23,41024
Ta 130,23,46144
Mvaa 156,140,56
v_lshlrev_b32 v27, 3, v27
Ta 132,24,41056
Mvaa 148,140,40
Ta 134,24,46176
W 8
Mvaa 160,144,40
s_add_u32 s81, s81, s50
Ta 136,23,41088
Mvaa 168,144,56
Ta 138,23,46208
v_bfe_u32 v20, v1, 4, 4
Mvaa 172,148,56
s_add_u32 s82, s81, 131072
Ta 140,24,41120
Mvaa 164,148,40
Ta 142,24,46240
W 8
Mvaa 96,152,44
v_mul_u32_u24 v20, 1280, v20
Ta 144,23,41152
Mvaa 104,152,60
Ta 146,23,46272
buffer_load_dwordx4 a[64:67], v1, s[8:11], s81 offen offset:0 sc1
Mvaa 108,156,60
v_lshrrev_b32 v12, 8, v1
Ta 148,24,41184
Mvaa 100,156,44
Ta 150,24,46304
W 8
Mvaa 112,128,44
v_lshl_add_u32 v20, v12, 4, v20
buffer_load_dwordx4 a[80:83], v1, s[8:11], s82 offen offset:0 sc1
Mvaa 120,128,60
Ta 152,23,41216
Ta 154,23,46336
Mvaa 124,132,60
s_bfe_u32 s66, s59, 0x10001
Ta 156,24,41248
Mvaa 116,132,44
Ta 158,24,46368
W 8
Mvaa 128,136,44
buffer_load_dwordx4 a[68:71], v1, s[8:11], s81 offen offset:1024 sc1
s_lshl_b32 s67, s57, 1
Mvaa 136,136,60
Ta 128,21,0
Ta 130,21,5120
Mvaa 140,140,60
s_add_u32 s66, s66, s67
Ta 132,22,32
Mvaa 132,140,44
buffer_load_dwordx4 a[84:87], v1, s[8:11], s82 offen offset:1024 sc1
W 7
Mvaa 144,144,44
Ta 134,22,5152
K 96,96,97
Mvaa 152,144,60
s_lshl_b32 s61, s66, 2
Ta 136,21,64
Mvaa 156,148,60
Ta 138,21,5184
buffer_load_dwordx4 a[72:75], v1, s[8:11], s81 offen offset:2048 sc1
Mvaa 148,148,44
K 97,98,99
W 6
Mvaa 160,152,44
Ta 140,22,96
Ta 142,22,5216
)"
R"(Mvaa 168,152,60
s_lshr_b32 s66, s59, 2
K 98,100,101
Mvaa 172,156,60
buffer_load_dwordx4 a[88:91], v1, s[8:11], s82 offen offset:2048 sc1
Ta 144,21,128
Mvaa 164,156,44
Ta 146,21,5248
W 6
Zvav 176,128,32
K 99,102,103
s_lshl_b32 s66, s66, 1
Zvav 184,128,64
Ta 148,22,160
Ta 150,22,5280
Zvav 188,132,64
s_add_u32 s83, s52, 0
s_add_u32 s61, s61, s66
Zvav 180,132,32
buffer_load_dwordx4 a[76:79], v1, s[8:11], s81 offen offset:3072 sc1
W 4
Zvav 192,136,32
Ta 152,21,192
Ta 154,21,5312
Zvav 200,136,64
s_and_b32 s66, s59, 1
Ta 156,22,224
Zvav 204,140,64
buffer_store_dwordx4 v[96:99], v1, s[8:11], s83 offen sc1
Ta 158,22,5344
Zvav 196,140,32
K 104,104,105
W 4
Zvav 208,144,32
s_add_u32 s61, s61, s66
K 105,106,107
Zvav 216,144,64
buffer_load_dwordx4 a[92:95], v1, s[8:11], s82 offen offset:3072 sc1
Ta 128,21,256
Zvav 220,148,64
Ta 130,21,5376
s_add_u32 s81, s58, 2
Zvav 212,148,32
Ta 132,22,288
Ta 134,22,5408
W 4
Zvav 224,152,32
K 106,108,109
s_xor_b32 s67, s61, 0
Zvav 232,152,64
K 107,110,111
s_mul_i32 s72, s67, 20480
Zvav 236,156,64
Ta 136,21,10240
Ta 138,21,15360
Zvav 228,156,32
Ta 140,22,10272
Ta 142,22,15392
W 4
Zvav 240,128,32
s_and_b32 s81, s81, 3
s_add_u32 s83, s52, 40960
Zvav 248,128,64
s_mul_i32 s66, s58, 320
s_lshl_b32 s81, s81, 12
Zvav 252,132,64
buffer_store_dwordx4 v[104:107], v1, s[8:11], s83 offen sc1
Ta 144,21,10304
Zvav 244,132,32
Ta 146,21,15424
Ta 148,22,10336
Ta 150,22,15456
W 4
Mvav 176,136,40
s_add_u32 s72, s72, s66
K 112,112,113
Mvav 184,136,72
s_add_u32 s81, s81, s50
s_lshr_b32 s66, s67, 2
Mvav 188,140,72
K 113,114,115
Ta 152,21,10368
Mvav 180,140,40
Ta 154,21,15488
Ta 156,22,10400
Ta 158,22,15520
W 4
Mvav 192,144,40
K 114,116,117
s_mul_i32 s75, s66, 81920
Mvav 200,144,72
s_add_u32 s82, s81, 131072
K 115,118,119
Mvav 204,148,72
s_mul_i32 s66, s58, 20480
Ta 128,21,10432
Mvav 196,148,40
Ta 130,21,15552
Ta 132,22,10464
Ta 134,22,15584
W 4
Mvav 208,152,40
s_add_u32 s83, s52, 1024
buffer_load_dwordx4 a[96:99], v1, s[8:11], s81 offen offset:0 sc1
Mvav 216,152,72
s_add_u32 s75, s75, s66
s_and_b32 s66, s67, 3
Mvav 220,156,72
Ta 136,21,10496
Ta 138,21,15616
Mvav 212,156,40
Ta 140,22,10528
Ta 142,22,15648
W 4
Mvav 224,128,40
buffer_store_dwordx4 v[112:115], v1, s[8:11], s83 offen sc1
K 120,120,121
Mvav 232,128,72
K 121,122,123
s_lshl_b32 s66, s66, 10
Mvav 236,132,72
K 122,124,125
Ta 144,21,20480
Mvav 228,132,40
Ta 146,21,25600
Ta 148,22,20512
Ta 150,22,25632
W 4
Mvav 240,136,40
buffer_load_dwordx4 a[112:115], v1, s[8:11], s82 offen offset:0 sc1
s_add_u32 s75, s75, s66
Mvav 248,136,72
K 123,126,127
s_add_u32 s75, s75, 131072
Mvav 252,140,72
s_add_u32 s83, s52, 41984
Ta 152,21,20544
Mvav 244,140,40
Ta 154,21,25664
Ta 156,22,20576
Ta 158,22,25696
W 4
Mvav 176,144,48
buffer_load_dwordx4 a[100:103], v1, s[8:11], s81 offen offset:1024 sc1
s_xor_b32 s67, s61, 124
Mvav 184,144,80
s_mul_i32 s73, s67, 20480
Ta 128,21,20608
Mvav 188,148,80
Ta 130,21,25728
s_mul_i32 s66, s58, 320
Mvav 180,148,48
Ta 132,22,20640
Ta 134,22,25760
W 4
Mvav 192,152,48
buffer_load_dwordx4 a[116:119], v1, s[8:11], s82 offen offset:1024 sc1
s_add_u32 s73, s73, s66
Mvav 200,152,80
Ta 136,21,20672
Ta 138,21,25792
Mvav 204,156,80
Ta 140,22,20704
Ta 142,22,25824
Mvav 196,156,48
buffer_load_dwordx4 a[104:107], v1, s[8:11], s81 offen offset:2048 sc1
W 4
Mvav 208,128,48
s_lshr_b32 s66, s67, 2
Ta 144,21,20736
Mvav 216,128,80
Ta 146,21,25856
s_mul_i32 s76, s66, 81920
Mvav 220,132,80
buffer_store_dwordx4 v[120:123], v1, s[8:11], s83 offen sc1
K 128,128,129
Mvav 212,132,48
Ta 148,22,20768
Ta 150,22,25888
W 4
Mvav 224,136,48
K 129,130,131
K 130,132,133
Mvav 232,136,80
buffer_load_dwordx4 a[120:123], v1, s[8:11], s82 offen offset:2048 sc1
K 131,134,135
Mvav 236,140,80
s_add_u32 s83, s52, 2048
s_mul_i32 s66, s58, 20480
Mvav 228,140,48
Ta 152,25,10240
Ta 154,25,15360
Ta 156,26,10272
Ta 158,26,15392
W 4
Mvav 240,144,48
buffer_load_dwordx4 a[108:111], v1, s[8:11], s81 offen offset:3072 sc1
s_add_u32 s76, s76, s66
Mvav 248,144,80
s_and_b32 s66, s67, 3
Ta 128,25,10304
Mvav 252,148,80
Ta 130,25,15424
s_lshl_b32 s66, s66, 10
Mvav 244,148,48
Ta 132,26,10336
Ta 134,26,15456
W 4
Mvav 176,152,56
buffer_load_dwordx4 a[124:127], v1, s[8:11], s82 offen offset:3072 sc1
s_add_u32 s81, s58, 3
Mvav 184,152,88
s_and_b32 s81, s81, 3
s_lshl_b32 s81, s81, 12
Mvav 188,156,88
s_add_u32 s81, s81, s50
s_add_u32 s76, s76, s66
Mvav 180,156,56
Ta 136,25,10368
Ta 138,25,15488
Ta 140,26,10400
Ta 142,26,15520
W 4
Mvav 192,128,56
buffer_store_dwordx4 v[128:131], v1, s[8:11], s83 offen sc1
K 136,136,137
Mvav 200,128,88
K 137,138,139
K 138,140,141
Mvav 204,132,88
K 139,142,143
s_add_u32 s83, s52, 43008
Mvav 196,132,56
Ta 144,25,10432
Ta 146,25,15552
Ta 148,26,10464
Ta 150,26,15584
W 4
Mvav 208,136,56
buffer_store_dwordx4 v[136:139], v1, s[8:11], s83 offen sc1
K 144,144,145
Mvav 216,136,88
s_add_u32 s82, s81, 131072
s_add_u32 s76, s76, 131072
Mvav 220,140,88
K 145,146,147
s_bitcmp1_b32 s14, 0
s_cselect_b32 s74, s73, s72
s_cselect_b32 s77, s76, s75
Mvav 212,140,56
Ta 152,25,10496
Ta 154,25,15616
Ta 156,26,10528
Ta 158,26,15648
W 4
Mvav 224,144,56
buffer_load_dwordx4 a[184:187], v1, s[8:11], s81 offen offset:0 sc1
K 146,148,149
Mvav 232,144,88
K 147,150,151
s_add_u32 s74, s74, 256
Mvav 236,148,88
s_add_u32 s83, s52, 3072
s_add_u32 s77, s77, 16384
Mvav 228,148,56
Ta 128,25,20480
Ta 130,25,25600
Ta 132,26,20512
Ta 134,26,25632
W 4
Mvav 240,152,56
buffer_load_dwordx4 a[200:203], v1, s[8:11], s82 offen offset:0 sc1
s_lshl_b32 s66, s14, 6
Mvav 248,152,88
s_lshl_b32 s67, s14, 12
s_add_u32 s72, s72, s66
Mvav 252,156,88
Ta 136,25,20544
Ta 138,25,25664
Mvav 244,156,56
Ta 140,26,20576
Ta 142,26,25696
s_waitcnt vmcnt(22) lgkmcnt(4)
Mvaa 176,128,64
buffer_store_dwordx4 v[144:147], v1, s[8:11], s83 offen sc1
K 152,152,153
Mvaa 184,128,80
K 153,154,155
K 154,156,157
Mvaa 188,132,80
K 155,158,159
s_add_u32 s75, s75, s67
Mvaa 180,132,64
Ta 144,25,20608
Ta 146,25,25728
Ta 148,26,20640
Ta 150,26,25760
W 4
Mvaa 192,136,64
buffer_load_dwordx4 a[188:191], v1, s[8:11], s81 offen offset:1024 sc1
s_add_u32 s83, s52, 44032
Mvaa 200,136,80
s_add_u32 s73, s73, s66
s_add_u32 s76, s76, s67
Mvaa 204,140,80
Ta 152,25,20672
Ta 154,25,25792
Mvaa 196,140,64
Ta 156,26,20704
Ta 158,26,25824
W 4
Mvaa 208,144,64
buffer_load_dwordx4 a[204:207], v1, s[8:11], s82 offen offset:1024 sc1
Ta 128,25,20736
Mvaa 216,144,80
Ta 130,25,25856
Ta 132,26,20768
Mvaa 220,148,80
Ta 134,26,25888
Ta 136,25,30720
Mvaa 212,148,64
buffer_load_dwordx4 a[192:195], v1, s[8:11], s81 offen offset:2048 sc1
W 5
Mvaa 224,152,64
Ta 138,25,35840
Ta 140,26,30752
Mvaa 232,152,80
Ta 142,26,35872
Ta 144,25,30784
Mvaa 236,156,80
buffer_load_dwordx4 a[208:211], v1, s[8:11], s82 offen offset:2048 sc1
Ta 146,25,35904
Mvaa 228,156,64
Ta 148,26,30816
W 7
Mvaa 240,128,64
Ta 150,26,35936
Ta 152,25,30848
Mvaa 248,128,80
buffer_store_dwordx4 v[152:155], v1, s[8:11], s83 offen sc1
K 160,160,161
Mvaa 252,132,80
K 161,162,163
K 162,164,165
Mvaa 244,132,64
K 163,166,167
s_waitcnt vmcnt(26) lgkmcnt(5)
Mvaa 176,136,68
buffer_load_dwordx4 a[196:199], v1, s[8:11], s81 offen offset:3072 sc1
s_add_u32 s83, s52, 4096
Mvaa 184,136,84
Ta 154,25,35968
Ta 156,26,30880
Mvaa 188,140,84
Ta 158,26,36000
Ta 128,25,30912
Mvaa 180,140,68
buffer_load_dwordx4 a[212:215], v1, s[8:11], s82 offen offset:3072 sc1
W 5
Mvaa 192,144,68
Ta 130,25,36032
Ta 132,26,30944
Mvaa 200,144,84
Ta 134,26,36064
Ta 136,25,30976
Mvaa 204,148,84
buffer_load_dwordx4 v[36:39], v20, s[16:19], s72 offen
Ta 138,25,36096
Mvaa 196,148,68
Ta 140,26,31008
W 7
Mvaa 208,152,68
Ta 142,26,36128
Ta 144,21,30720
Mvaa 216,152,84
buffer_store_dwordx4 v[160:163], v1, s[8:11], s83 offen sc1
K 168,168,169
Mvaa 220,156,84
K 169,170,171
K 170,172,173
Mvaa 212,156,68
K 171,174,175
W 5
Mvaa 224,128,68
buffer_load_dwordx4 v[44:47], v1, s[8:11], s75 offen sc1
s_add_u32 s83, s52, 45056
Mvaa 232,128,84
Ta 146,21,35840
Ta 148,22,30752
Mvaa 236,132,84
Ta 150,22,35872
Ta 152,21,30784
Mvaa 228,132,68
buffer_load_dwordx4 v[52:55], v20, s[16:19], s73 offen
W 5
Mvaa 240,136,68
Ta 154,21,35904
Ta 156,22,30816
Mvaa 248,136,84
Ta 158,22,35936
)"
R"(Ta 128,21,30848
Mvaa 252,140,84
buffer_store_dwordx4 v[168:171], v1, s[8:11], s83 offen sc1
Ta 130,21,35968
Mvaa 244,140,68
Ta 132,22,30880
s_waitcnt vmcnt(31) lgkmcnt(7)
Mvaa 176,144,72
Ta 134,22,36000
Ta 136,21,30912
Mvaa 184,144,88
buffer_load_dwordx4 v[60:63], v1, s[8:11], s76 offen sc1
Ta 138,21,36032
Mvaa 188,148,88
Ta 140,22,30944
Ta 142,22,36064
Mvaa 180,148,72
Ta 144,21,30976
W 9
Mvaa 192,152,72
buffer_load_dwordx4 v[68:71], v20, s[16:19], s74 offen
Ta 146,21,36096
Mvaa 200,152,88
Ta 148,22,31008
Ta 150,22,36128
Mvaa 204,156,88
Mvaa 196,156,72
buffer_load_dwordx4 v[76:79], v1, s[8:11], s77 offen sc1
W 8
Mvaa 208,128,72
Ta 152,23,0
Ta 154,23,5120
Mvaa 216,128,88
Ta 156,24,32
Ta 158,24,5152
Mvaa 220,132,88
buffer_load_dwordx2 v[12:13], v27, s[8:11], 0 offen offset:3584 sc1
v_lshrrev_b32 v14, 1, v1
Mvaa 212,132,72
v_min_u32 v15, 344, v14
W 8
Mvaa 224,136,72
Ta 128,23,64
Ta 130,23,5184
Mvaa 232,136,88
buffer_load_dwordx2 v[16:17], v14, s[8:11], 0 offen offset:2560 sc1
Ta 132,24,96
Mvaa 236,140,88
Ta 134,24,5216
Mvaa 228,140,72
Ta 136,23,128
W 9
Mvaa 240,144,72
buffer_load_dwordx2 v[18:19], v15, s[8:11], 0 offen offset:3072 sc1
s_waitcnt vmcnt(0)
v_xor_b32 v12, s12, v12
v_xor_b32 v13, s13, v13
v_lshrrev_b32 v94, 11, v12
v_or_b32 v13, v13, v94
v_cmp_eq_u32 vcc, 0, v13
s_cmp_eq_u64 vcc, -1
s_cbranch_scc0 .LL_93%=
.LL_94%=:
v_bfe_u32 v13, v12, 4, 7
s_xor_b32 s66, s61, 124
v_cmp_eq_u32 vcc, s61, v13
v_cmp_eq_u32_e64 s[86:87], s66, v13
s_or_b32 s68, vcc_lo, s86
s_and_b32 s68, s68, 0xff
s_cbranch_scc1 .Lup_91%=
.Lupr_92%=:
Mvaa 248,144,88
v_lshlrev_b32 v32, 16, v36
v_and_b32 v33, 0xffff0000, v36
Mvaa 252,148,88
v_lshlrev_b32 v34, 16, v37
v_and_b32 v35, 0xffff0000, v37
Mvaa 244,148,72
v_lshlrev_b32 v36, 16, v38
W 5
Mvaa 176,152,76
v_and_b32 v37, 0xffff0000, v38
v_lshlrev_b32 v38, 16, v39
Mvaa 184,152,92
v_and_b32 v39, 0xffff0000, v39
v_lshlrev_b32 v40, 16, v44
Mvaa 188,156,92
v_and_b32 v41, 0xffff0000, v44
v_lshlrev_b32 v42, 16, v45
Mvaa 180,156,76
Ta 138,23,5248
Ta 140,24,160
Ta 142,24,5280
v_lshlrev_b32 v2, 2, v1
v_mov_b64 v[28:29], s[12:13]
s_mov_b64 exec, 3
buffer_store_dwordx2 v[28:29], v2, s[8:11], s53 offen offset:0 sc1
s_mov_b64 exec, -1
W 4
Mvaa 192,128,76
v_and_b32 v43, 0xffff0000, v45
v_lshlrev_b32 v44, 16, v46
Mvaa 200,128,92
v_and_b32 v45, 0xffff0000, v46
v_lshlrev_b32 v46, 16, v47
Mvaa 204,132,92
v_and_b32 v47, 0xffff0000, v47
v_lshlrev_b32 v48, 16, v52
Mvaa 196,132,76
Ta 144,23,192
Ta 146,23,5312
Ta 148,24,224
Ta 150,24,5344
W 4
Mvaa 208,136,76
v_and_b32 v49, 0xffff0000, v52
v_lshlrev_b32 v50, 16, v53
Mvaa 216,136,92
v_and_b32 v51, 0xffff0000, v53
v_lshlrev_b32 v52, 16, v54
Mvaa 220,140,92
v_and_b32 v53, 0xffff0000, v54
Ta 152,23,256
Mvaa 212,140,76
Ta 154,23,5376
Ta 156,24,288
Ta 158,24,5408
W 4
Mvaa 224,144,76
v_lshlrev_b32 v54, 16, v55
v_and_b32 v55, 0xffff0000, v55
Mvaa 232,144,92
v_lshlrev_b32 v56, 16, v60
v_and_b32 v57, 0xffff0000, v60
Mvaa 236,148,92
Ta 128,23,20480
Ta 130,23,25600
Mvaa 228,148,76
Ta 132,24,20512
Ta 134,24,25632
W 4
Mvaa 240,152,76
v_lshlrev_b32 v58, 16, v61
v_and_b32 v59, 0xffff0000, v61
Mvaa 248,152,92
v_lshlrev_b32 v60, 16, v62
v_and_b32 v61, 0xffff0000, v62
Mvaa 252,156,92
Ta 136,23,51200
Ta 138,23,56320
Mvaa 244,156,76
Ta 140,24,51232
Ta 142,24,56352
W 4
Mvaa 176,128,96
v_lshlrev_b32 v62, 16, v63
v_and_b32 v63, 0xffff0000, v63
Mvaa 184,128,112
v_lshlrev_b32 v64, 16, v68
v_and_b32 v65, 0xffff0000, v68
Mvaa 188,132,112
Ta 144,25,0
Ta 146,25,5120
Mvaa 180,132,96
Ta 148,26,32
Ta 150,26,5152
W 4
Mvaa 176,136,100
v_lshlrev_b32 v66, 16, v69
v_and_b32 v67, 0xffff0000, v69
Mvaa 184,136,116
v_lshlrev_b32 v68, 16, v70
v_and_b32 v69, 0xffff0000, v70
Mvaa 188,140,116
Ta 152,21,40960
Ta 154,21,46080
Mvaa 180,140,100
Ta 156,22,40992
Ta 158,22,46112
W 4
Mvaa 176,144,104
v_lshlrev_b32 v70, 16, v71
v_and_b32 v71, 0xffff0000, v71
Mvaa 184,144,120
v_lshlrev_b32 v72, 16, v76
v_and_b32 v73, 0xffff0000, v76
Mvaa 188,148,120
Ta 128,21,51200
Ta 130,21,56320
Mvaa 180,148,104
Ta 132,22,51232
Ta 134,22,56352
W 4
Mvaa 176,152,108
v_lshlrev_b32 v74, 16, v77
v_and_b32 v75, 0xffff0000, v77
Mvaa 184,152,124
v_lshlrev_b32 v76, 16, v78
v_and_b32 v77, 0xffff0000, v78
v_lshlrev_b32 v78, 16, v79
v_and_b32 v79, 0xffff0000, v79
buffer_load_dwordx2 v[28:29], v27, s[8:11], s63 offen offset:0 sc1
Mvaa 188,156,124
s_mov_b64 s[40:41], s[8:9]
Ta 136,23,10240
Mvaa 180,156,108
Ta 138,23,15360
Ta 140,24,10272
Ta 142,24,15392
W 4
Mvaa 176,128,184
Ta 144,23,30720
Ta 146,23,35840
Mvaa 184,128,200
Ta 148,24,30752
Ta 150,24,35872
Mvaa 188,132,200
Ta 152,23,40960
Ta 154,23,46080
Mvaa 180,132,184
Ta 156,24,40992
W 7
Mvaa 176,136,188
Ta 158,24,46112
Ta 128,23,20544
Mvaa 184,136,204
Ta 130,23,25664
Ta 132,24,20576
Mvaa 188,140,204
Ta 134,24,25696
Mvaa 180,140,188
Ta 136,23,51264
W 9
Mvaa 176,144,192
Ta 138,23,56384
Mvaa 184,144,208
Ta 140,24,51296
Ta 142,24,56416
Mvaa 188,148,208
Mvaa 180,148,192
Ta 144,25,64
W 9
Mvaa 176,152,196
Ta 146,25,5184
Mvaa 184,152,212
Ta 148,26,96
Ta 150,26,5216
Mvaa 188,156,212
Mvaa 180,156,196
Ta 152,21,41024
W 9
Mvaa 192,128,96
Ta 154,21,46144
Mvaa 200,128,112
Ta 156,22,41056
Ta 158,22,46176
Mvaa 204,132,112
K 176,176,177
Mvaa 196,132,96
Ta 128,21,51264
W 9
Mvaa 192,136,100
Ta 130,21,56384
K 177,178,179
Mvaa 200,136,116
K 178,180,181
Ta 132,22,51296
Mvaa 204,140,116
Ta 134,22,56416
K 179,182,183
Mvaa 196,140,100
Ta 136,23,10304
W 9
Mvaa 192,144,104
Ta 138,23,15424
s_add_u32 s83, s52, 327680
Mvaa 200,144,120
buffer_store_dwordx4 v[176:179], v1, s[8:11], s83 offen sc1
K 184,184,185
Mvaa 204,148,120
K 185,186,187
K 186,188,189
Mvaa 196,148,104
K 187,190,191
W 6
Mvaa 192,152,108
s_add_u32 s83, s52, 368640
buffer_store_dwordx4 v[184:187], v1, s[8:11], s83 offen sc1
Mvaa 200,152,124
Ta 140,24,10336
Ta 142,24,15456
Mvaa 204,156,124
Ta 144,23,30784
Ta 146,23,35904
Mvaa 196,156,108
Ta 148,24,30816
W 7
Mvaa 192,128,184
Ta 150,24,35936
Ta 152,23,41024
Mvaa 200,128,200
Ta 154,23,46144
Ta 156,24,41056
Mvaa 204,132,200
Ta 158,24,46176
Mvaa 196,132,184
Ta 128,23,20608
W 9
Mvaa 192,136,188
Ta 130,23,25728
Mvaa 200,136,204
Ta 132,24,20640
Ta 134,24,25760
Mvaa 204,140,204
s_waitcnt vmcnt(2)
v_cmp_eq_u64 vcc, s[12:13], v[28:29]
s_cmp_eq_u64 vcc, -1
s_cselect_b32 s46, 79691776, 0
s_cmp_eq_u32 s14, 0
s_cselect_b32 s42, s46, 0
Mvaa 196,140,188
s_add_u32 s62, s78, 0
W 8
Mvaa 192,144,192
Ta 136,23,51328
Ta 138,23,56448
Mvaa 200,144,208
Ta 140,24,51360
Ta 142,24,56480
Mvaa 204,148,208
buffer_load_dwordx4 v[96:99], v1, s[44:47], s62 offen sc1
Mvaa 196,148,192
Ta 144,25,128
W 9
Mvaa 192,152,196
Ta 146,25,5248
Mvaa 200,152,212
s_add_u32 s62, s78, 5120
Ta 148,26,160
Mvaa 204,156,212
Ta 150,26,5280
Mvaa 196,156,196
Ta 152,21,41088
W 9
Mvaa 208,128,96
Ta 154,21,46208
buffer_load_dwordx4 v[100:103], v1, s[44:47], s62 offen sc1
Mvaa 216,128,112
Ta 156,22,41120
Ta 158,22,46240
Mvaa 220,132,112
K 192,192,193
Mvaa 212,132,96
s_add_u32 s62, s78, 10240
W 8
Mvaa 208,136,100
Ta 128,21,51328
Ta 130,21,56448
Mvaa 216,136,116
K 193,194,195
K 194,196,197
Mvaa 220,140,116
Ta 132,22,51360
Ta 134,22,56480
Mvaa 212,140,100
buffer_load_dwordx4 v[104:107], v1, s[44:47], s62 offen sc1
W 8
Mvaa 208,144,104
K 195,198,199
Ta 136,23,10368
Mvaa 216,144,120
Ta 138,23,15488
s_add_u32 s83, s52, 328704
Mvaa 220,148,120
buffer_store_dwordx4 v[192:195], v1, s[8:11], s83 offen sc1
K 200,200,201
Mvaa 212,148,104
K 201,202,203
W 6
Mvaa 208,152,108
K 202,204,205
K 203,206,207
Mvaa 216,152,124
s_add_u32 s83, s52, 369664
buffer_store_dwordx4 v[200:203], v1, s[8:11], s83 offen sc1
Mvaa 220,156,124
s_add_u32 s62, s78, 15360
Ta 140,24,10400
Mvaa 212,156,108
Ta 142,24,15520
W 4
Mvaa 208,128,184
Ta 144,23,30848
Ta 146,23,35968
Mvaa 216,128,200
buffer_load_dwordx4 v[108:111], v1, s[44:47], s62 offen sc1
Ta 148,24,30880
Mvaa 220,132,200
Ta 150,24,36000
Ta 152,23,41088
)"
R"(Mvaa 212,132,184
Ta 154,23,46208
W 6
Mvaa 208,136,188
s_add_u32 s62, s78, 20480
Ta 156,24,41120
Mvaa 216,136,204
Ta 158,24,46240
buffer_load_dwordx4 v[112:115], v1, s[44:47], s62 offen sc1
Mvaa 220,140,204
Ta 128,23,20672
Ta 130,23,25792
Mvaa 212,140,188
Ta 132,24,20704
W 7
Mvaa 208,144,192
Ta 134,24,25824
s_add_u32 s62, s78, 25600
Mvaa 216,144,208
Ta 136,23,51392
Ta 138,23,56512
Mvaa 220,148,208
buffer_load_dwordx4 v[116:119], v1, s[44:47], s62 offen sc1
Ta 140,24,51424
Mvaa 212,148,192
Ta 142,24,56544
W 8
Mvaa 208,152,196
Ta 144,25,192
Ta 146,25,5312
Mvaa 216,152,212
s_add_u32 s62, s78, 30720
Ta 148,26,224
Mvaa 220,156,212
Ta 150,26,5344
Mvaa 212,156,196
buffer_load_dwordx4 v[120:123], v1, s[44:47], s62 offen sc1
W 8
Mvaa 224,128,96
Ta 152,21,41152
Ta 154,21,46272
Mvaa 232,128,112
Ta 156,22,41184
Ta 158,22,46304
Mvaa 236,132,112
s_add_u32 s62, s78, 35840
K 208,208,209
Mvaa 228,132,96
Ta 128,21,51392
W 9
Mvaa 224,136,100
Ta 130,21,56512
K 209,210,211
Mvaa 232,136,116
K 210,212,213
buffer_load_dwordx4 v[124:127], v1, s[44:47], s62 offen sc1
Mvaa 236,140,116
Ta 132,22,51424
Ta 134,22,56544
Mvaa 228,140,100
K 211,214,215
W 8
Mvaa 224,144,104
Ta 136,23,10432
Ta 138,23,15552
Mvaa 232,144,120
s_add_u32 s83, s52, 329728
s_add_u32 s62, s65, 0
Mvaa 236,148,120
buffer_store_dwordx4 v[208:211], v1, s[8:11], s83 offen sc1
K 216,216,217
Mvaa 228,148,104
K 217,218,219
W 6
Mvaa 224,152,108
K 218,220,221
K 219,222,223
Mvaa 232,152,124
s_add_u32 s83, s52, 370688
buffer_store_dwordx4 v[216:219], v1, s[8:11], s83 offen sc1
Mvaa 236,156,124
Ta 140,24,10464
Ta 142,24,15584
Mvaa 228,156,108
Ta 144,23,30912
W 5
Mvaa 224,128,184
Ta 146,23,36032
buffer_load_dwordx4 v[128:131], v1, s[40:43], s62 offen sc1
Mvaa 232,128,200
Ta 148,24,30944
Ta 150,24,36064
Mvaa 236,132,200
s_add_u32 s62, s65, 5120
Ta 152,23,41152
Mvaa 228,132,184
Ta 154,23,46272
W 6
Mvaa 224,136,188
Ta 156,24,41184
Ta 158,24,46304
Mvaa 232,136,204
buffer_load_dwordx4 v[132:135], v1, s[40:43], s62 offen sc1
Ta 128,23,20736
Mvaa 236,140,204
Ta 130,23,25856
s_add_u32 s62, s65, 10240
Mvaa 228,140,188
Ta 132,24,20768
W 7
Mvaa 224,144,192
Ta 134,24,25888
Ta 136,23,51456
Mvaa 232,144,208
Ta 138,23,56576
buffer_load_dwordx4 v[136:139], v1, s[40:43], s62 offen sc1
Mvaa 236,148,208
Ta 140,24,51488
Ta 142,24,56608
Mvaa 228,148,192
s_add_u32 s62, s65, 15360
W 8
Mvaa 224,152,196
Ta 144,25,256
Ta 146,25,5376
Mvaa 232,152,212
Ta 148,26,288
Ta 150,26,5408
Mvaa 236,156,212
buffer_load_dwordx4 v[140:143], v1, s[40:43], s62 offen sc1
Mvaa 228,156,196
Ta 152,21,41216
W 9
Mvaa 240,128,96
Ta 154,21,46336
Mvaa 248,128,112
Ta 156,22,41248
Ta 158,22,46368
Mvaa 252,132,112
s_add_u32 s62, s65, 20480
K 224,224,225
Mvaa 244,132,96
Ta 128,21,51456
W 9
Mvaa 240,136,100
Ta 130,21,56576
K 225,226,227
Mvaa 248,136,116
buffer_load_dwordx4 v[144:147], v1, s[40:43], s62 offen sc1
K 226,228,229
Mvaa 252,140,116
Ta 132,22,51488
Ta 134,22,56608
Mvaa 244,140,100
K 227,230,231
W 8
Mvaa 240,144,104
Ta 136,23,10496
Ta 138,23,15616
Mvaa 248,144,120
s_add_u32 s62, s65, 25600
s_add_u32 s83, s52, 330752
Mvaa 252,148,120
buffer_store_dwordx4 v[224:227], v1, s[8:11], s83 offen sc1
K 232,232,233
Mvaa 244,148,104
K 233,234,235
W 6
Mvaa 240,152,108
K 234,236,237
K 235,238,239
Mvaa 248,152,124
s_add_u32 s83, s52, 371712
buffer_store_dwordx4 v[232:235], v1, s[8:11], s83 offen sc1
Mvaa 252,156,124
Ta 140,24,10528
Ta 142,24,15648
Mvaa 244,156,108
Ta 144,23,30976
W 5
Mvaa 240,128,184
buffer_load_dwordx4 v[148:151], v1, s[40:43], s62 offen sc1
Ta 146,23,36096
Mvaa 248,128,200
Ta 148,24,31008
Ta 150,24,36128
Mvaa 252,132,200
s_add_u32 s62, s65, 30720
Ta 152,23,41216
Mvaa 244,132,184
Ta 154,23,46336
W 6
Mvaa 240,136,188
buffer_load_dwordx4 v[152:155], v1, s[40:43], s62 offen sc1
Ta 156,24,41248
Mvaa 248,136,204
Ta 158,24,46368
s_add_u32 s62, s65, 35840
Mvaa 252,140,204
Mvaa 244,140,188
buffer_load_dwordx4 v[156:159], v1, s[40:43], s62 offen sc1
W 4
Mvaa 240,144,192
Mvaa 248,144,208
Mvaa 252,148,208
Mvaa 244,148,192
W 0
Mvaa 240,152,196
Mvaa 248,152,212
Mvaa 252,156,212
Mvaa 244,156,196
s_nop 4
K 240,240,241
K 241,242,243
s_nop 0
K 242,244,245
K 243,246,247
s_add_u32 s83, s52, 331776
buffer_store_dwordx4 v[240:243], v1, s[8:11], s83 offen sc1
K 248,248,249
K 249,250,251
K 250,252,253
K 251,254,255
s_add_u32 s83, s52, 372736
buffer_store_dwordx4 v[248:251], v1, s[8:11], s83 offen sc1
s_waitcnt vmcnt(2)
s_branch .Ltend_85%=
.Llate_84%=:
ds_read_b32 v11, v11 offset:59392
ds_read_b64 v[8:9], v10 offset:57344
Rv 176,3,0
Rv 180,3,1024
Rv 184,3,2048
Rv 188,3,3072
Rv 192,3,4096
Rv 196,3,5120
Rv 200,3,6144
Rv 204,3,7168
W 6
Mvvv 32,208,176
v_cmp_ne_u32 vcc, 0, v11
s_cmp_lg_u64 vcc, 0
s_cbranch_scc1 .Lredo%=
Mvvv 36,212,176
s_add_u32 s94, s94, 81920
A 4,4,8
Mvvv 40,208,180
v_rcp_f32 v4, v4
Mvvv 44,212,180
s_mul_i32 s95, s92, 30720
W 4
Mvvv 48,208,184
A 5,5,9
Ta 128,21,0
Mvvv 52,212,184
Ta 130,21,5120
s_add_u32 s95, s95, s71
Mvvv 56,208,188
v_rcp_f32 v5, v5
Mvvv 60,212,188
s_lshl_b32 s93, s58, 12
W 4
Mvvv 64,208,192
s_add_u32 s93, s93, s50
Ta 132,22,32
Mvvv 68,212,192
Ta 134,22,5152
U 32,32,4
Mvvv 72,208,196
s_add_u32 m0, s95, 30720
U 33,33,4
Mvvv 76,212,196
U 34,34,4
W 4
Mvvv 80,208,200
Ta 136,21,64
Ta 138,21,5184
Mvvv 84,212,200
U 35,35,4
D 16,40,94,0
Mvvv 88,208,204
U 36,36,4
U 37,37,4
U 38,38,4
U 39,39,4
K 32,32,33
K 33,34,35
K 34,36,37
K 35,38,39
buffer_store_dwordx4 v[32:35], v1, s[8:11], s93 offen offset:0 sc1
U 64,64,5
U 65,65,5
U 66,66,5
U 67,67,5
U 68,68,5
U 69,69,5
U 70,70,5
U 71,71,5
K 64,64,65
K 65,66,67
K 66,68,69
K 67,70,71
s_add_u32 s67, s93, 131072
Mvvv 92,212,204
Ta 140,22,96
Ta 142,22,5216
W 4
Zvav 176,128,32
Ta 144,21,128
Ta 146,21,5248
Zvav 184,128,64
D 17,40,94,1024
Ta 148,22,160
Zvav 188,132,64
Ta 150,22,5280
Ta 152,21,192
Zvav 180,132,32
Ta 154,21,5312
W 6
Zvav 192,136,32
D 18,40,94,2048
Ta 156,22,224
Zvav 200,136,64
Ta 158,22,5344
Ta 128,21,256
Zvav 204,140,64
Ta 130,21,5376
Ta 132,22,288
Zvav 196,140,32
D 19,40,94,3072
W 7
Zvav 208,144,32
s_add_u32 m0, s95, 34816
Ta 134,22,5408
Zvav 216,144,64
Ta 136,21,10240
Ta 138,21,15360
Zvav 220,148,64
buffer_load_dwordx4 v20, s[40:43], s94 offen lds
s_add_u32 s94, s58, 2
Zvav 212,148,32
s_and_b32 s94, s94, 3
W 6
Zvav 224,152,32
s_mul_i32 s94, s94, 163840
s_add_u32 s94, s94, s48
Zvav 232,152,64
buffer_store_dwordx4 v[64:67], v1, s[8:11], s67 offen offset:0 sc1
U 40,40,4
Zvav 236,156,64
U 41,41,4
U 42,42,4
Zvav 228,156,32
Ta 140,22,10272
Ta 142,22,15392
W 4
Zvav 240,128,32
U 43,43,4
U 44,44,4
Zvav 248,128,64
U 45,45,4
U 46,46,4
Zvav 252,132,64
U 47,47,4
K 40,40,41
K 41,42,43
K 42,44,45
K 43,46,47
buffer_store_dwordx4 v[40:43], v1, s[8:11], s93 offen offset:1024 sc1
U 72,72,5
U 73,73,5
U 74,74,5
U 75,75,5
U 76,76,5
U 77,77,5
U 78,78,5
U 79,79,5
K 72,72,73
K 73,74,75
K 74,76,77
K 75,78,79
Zvav 244,132,32
Ta 144,21,10304
Ta 146,21,15424
Ta 148,22,10336
Ta 150,22,15456
W 4
Mvav 176,136,40
s_mul_i32 s95, s92, 30720
s_add_u32 s95, s95, s71
Mvav 184,136,72
s_add_u32 m0, s95, 81920
Ta 152,21,10368
Mvav 188,140,72
Ta 154,21,15488
D 16,40,94,0
Mvav 180,140,40
Ta 156,22,10400
Ta 158,22,15520
W 4
)"
R"(Mvav 192,144,40
Ta 128,21,10432
Ta 130,21,15552
Mvav 200,144,72
D 17,40,94,1024
Ta 132,22,10464
Mvav 204,148,72
Ta 134,22,15584
Ta 136,21,10496
Mvav 196,148,40
Ta 138,21,15616
W 6
Mvav 208,152,40
D 18,40,94,2048
Ta 140,22,10528
Mvav 216,152,72
Ta 142,22,15648
Ta 144,21,20480
Mvav 220,156,72
Ta 146,21,25600
Ta 148,22,20512
Mvav 212,156,40
buffer_store_dwordx4 v[72:75], v1, s[8:11], s67 offen offset:1024 sc1
W 7
Mvav 224,128,40
U 48,48,4
U 49,49,4
Mvav 232,128,72
U 50,50,4
U 51,51,4
Mvav 236,132,72
D 19,40,94,3072
s_add_u32 m0, s95, 86016
Mvav 228,132,40
Ta 150,22,25632
W 4
Mvav 240,136,40
U 52,52,4
U 53,53,4
Mvav 248,136,72
buffer_load_dwordx4 v20, s[40:43], s94 offen lds
s_waitcnt vmcnt(14)
s_cmp_lg_u32 s70, 0
s_cbranch_scc1 .Lpd_99%=
.Lpdr_100%=:
Mvav 252,140,72
U 54,54,4
U 55,55,4
K 48,48,49
K 49,50,51
K 50,52,53
K 51,54,55
buffer_store_dwordx4 v[48:51], v1, s[8:11], s93 offen offset:2048 sc1
U 80,80,5
U 81,81,5
U 82,82,5
U 83,83,5
U 84,84,5
U 85,85,5
U 86,86,5
U 87,87,5
K 80,80,81
K 81,82,83
K 82,84,85
K 83,86,87
Mvav 244,140,40
Ta 152,21,20544
Ta 154,21,25664
Ta 156,22,20576
Ta 158,22,25696
W 4
Mvav 176,144,48
Ta 128,21,20608
Ta 130,21,25728
Mvav 184,144,80
Ta 132,22,20640
Ta 134,22,25760
Mvav 188,148,80
buffer_store_dwordx4 v[80:83], v1, s[8:11], s67 offen offset:2048 sc1
U 56,56,4
Mvav 180,148,48
U 57,57,4
W 4
Mvav 192,152,48
Ta 136,21,20672
Ta 138,21,25792
Mvav 200,152,80
U 58,58,4
U 59,59,4
Mvav 204,156,80
v_bfe_u32 v28, v1, 4, 2
U 60,60,4
Mvav 196,156,48
Ta 140,22,20704
Ta 142,22,25824
W 4
Mvav 208,128,48
v_lshlrev_b32 v28, 3, v28
U 61,61,4
Mvav 216,128,80
buffer_load_dwordx2 v[28:29], v28, s[8:11], s54 offen sc1
U 62,62,4
Mvav 220,132,80
U 63,63,4
K 56,56,57
Mvav 212,132,48
Ta 144,21,20736
Ta 146,21,25856
Ta 148,22,20768
Ta 150,22,25888
W 4
Mvav 224,136,48
K 57,58,59
K 58,60,61
Mvav 232,136,80
K 59,62,63
buffer_store_dwordx4 v[56:59], v1, s[8:11], s93 offen offset:3072 sc1
Mvav 236,140,80
U 88,88,5
U 89,89,5
Mvav 228,140,48
Ta 152,25,10240
Ta 154,25,15360
Ta 156,26,10272
Ta 158,26,15392
W 4
Mvav 240,144,48
U 90,90,5
U 91,91,5
Mvav 248,144,80
U 92,92,5
U 93,93,5
Mvav 252,148,80
U 94,94,5
U 95,95,5
K 88,88,89
K 89,90,91
K 90,92,93
K 91,94,95
Mvav 244,148,48
Ta 128,25,10304
Ta 130,25,15424
Ta 132,26,10336
Ta 134,26,15456
W 4
Mvav 176,152,56
buffer_store_dwordx4 v[88:91], v1, s[8:11], s67 offen offset:3072 sc1
Ta 136,25,10368
Mvav 184,152,88
Ta 138,25,15488
Ta 140,26,10400
Mvav 188,156,88
Ta 142,26,15520
Ta 144,25,10432
Mvav 180,156,56
Ta 146,25,15552
W 6
Mvav 192,128,56
Ta 148,26,10464
Ta 150,26,15584
Mvav 200,128,88
Ta 152,25,10496
Ta 154,25,15616
Mvav 204,132,88
Ta 156,26,10528
Ta 158,26,15648
Mvav 196,132,56
Ta 128,21,0
W 9
Mvav 208,136,56
Ta 130,21,5120
Mvav 216,136,88
Ta 132,22,32
Ta 134,22,5152
Mvav 220,140,88
Mvav 212,140,56
Ta 136,21,64
W 9
Mvav 224,144,56
Ta 138,21,5184
s_waitcnt vmcnt(2)
v_cmp_eq_u64 vcc, s[12:13], v[28:29]
s_cmp_eq_u64 vcc, -1
s_cbranch_scc0 .Lspin_101%=
.Lok_102%=:
Mvav 232,144,88
s_add_u32 s81, s58, 0
s_and_b32 s81, s81, 3
Mvav 236,148,88
Ta 140,22,96
Ta 142,22,5216
Mvav 228,148,56
s_lshl_b32 s81, s81, 12
W 8
Mvav 240,152,56
s_add_u32 s81, s81, s49
Ta 144,21,128
Mvav 248,152,88
Ta 146,21,5248
s_add_u32 s82, s81, 131072
Mvav 252,156,88
buffer_load_dwordx4 a[184:187], v1, s[8:11], s81 offen offset:0 sc1
buffer_load_dwordx4 a[200:203], v1, s[8:11], s82 offen offset:0 sc1
Mvav 244,156,56
Ta 148,22,160
s_waitcnt vmcnt(0) lgkmcnt(7)
Zvaa 96,128,184
Ta 150,22,5280
Ta 152,21,192
Zvaa 104,128,200
buffer_load_dwordx4 a[188:191], v1, s[8:11], s81 offen offset:1024 sc1
Ta 154,21,5312
Zvaa 108,132,200
Ta 156,22,224
Ta 158,22,5344
Zvaa 100,132,184
Ta 128,21,256
W 9
Zvaa 112,136,184
buffer_load_dwordx4 a[204:207], v1, s[8:11], s82 offen offset:1024 sc1
Ta 130,21,5376
Zvaa 120,136,200
Ta 132,22,288
Ta 134,22,5408
Zvaa 124,140,200
Zvaa 116,140,184
buffer_load_dwordx4 a[192:195], v1, s[8:11], s81 offen offset:2048 sc1
W 8
Zvaa 128,144,184
Ta 136,21,10240
Ta 138,21,15360
Zvaa 136,144,200
Ta 140,22,10272
Ta 142,22,15392
Zvaa 140,148,200
buffer_load_dwordx4 a[208:211], v1, s[8:11], s82 offen offset:2048 sc1
Zvaa 132,148,184
Ta 144,21,10304
W 9
Zvaa 144,152,184
Ta 146,21,15424
Zvaa 152,152,200
buffer_load_dwordx4 a[196:199], v1, s[8:11], s81 offen offset:3072 sc1
Ta 148,22,10336
Zvaa 156,156,200
Ta 150,22,15456
Zvaa 148,156,184
Ta 152,21,10368
W 9
Zvaa 160,128,184
buffer_load_dwordx4 a[212:215], v1, s[8:11], s82 offen offset:3072 sc1
Ta 154,21,15488
Zvaa 168,128,200
s_lshl_b32 s80, s58, 3
s_add_u32 s80, s80, s55
Zvaa 172,132,200
Ta 156,22,10400
Ta 158,22,15520
Zvaa 164,132,184
v_mov_b64 v[28:29], s[12:13]
s_mov_b64 exec, 1
buffer_store_dwordx2 v[28:29], off, s[8:11], s80 sc1
s_mov_b64 exec, -1
s_waitcnt vmcnt(5) lgkmcnt(8)
Mvaa 96,136,188
s_barrier
Ta 128,21,10432
Mvaa 104,136,204
Ta 130,21,15552
s_add_u32 s94, s94, 81920
Mvaa 108,140,204
Ta 132,22,10464
Ta 134,22,15584
Mvaa 100,140,188
s_mul_i32 s95, s92, -81920
W 8
Mvaa 112,144,188
Ta 136,21,10496
Ta 138,21,15616
Mvaa 120,144,204
s_add_u32 s95, s95, s71
Ta 140,22,10528
Mvaa 124,148,204
Ta 142,22,15648
s_add_u32 m0, s95, 122880
Mvaa 116,148,188
Ta 144,21,20480
W 9
Mvaa 128,152,188
Ta 146,21,25600
D 16,40,94,0
Mvaa 136,152,204
Ta 148,22,20512
Ta 150,22,25632
Mvaa 140,156,204
Mvaa 132,156,188
D 17,40,94,1024
W 8
Mvaa 144,128,188
Ta 152,21,20544
Ta 154,21,25664
Mvaa 152,128,204
Ta 156,22,20576
Ta 158,22,25696
Mvaa 156,132,204
D 18,40,94,2048
Mvaa 148,132,188
Ta 128,21,20608
W 9
Mvaa 160,136,188
Ta 130,21,25728
Mvaa 168,136,204
D 19,40,94,3072
s_add_u32 m0, s95, 126976
Mvaa 172,140,204
Ta 132,22,20640
Ta 134,22,25760
Mvaa 164,140,188
Ta 136,21,20672
s_waitcnt vmcnt(7) lgkmcnt(9)
Mvaa 96,144,192
buffer_load_dwordx4 v20, s[40:43], s94 offen lds
s_add_u32 s94, s58, 3
Mvaa 104,144,208
Ta 138,21,25792
s_and_b32 s94, s94, 3
Mvaa 108,148,208
s_add_u32 s81, s58, 1
Ta 140,22,20704
Mvaa 100,148,192
Ta 142,22,25824
W 8
Mvaa 112,152,192
s_and_b32 s81, s81, 3
Ta 144,21,20736
Mvaa 120,152,208
Ta 146,21,25856
s_lshl_b32 s81, s81, 12
Mvaa 124,156,208
s_add_u32 s81, s81, s49
Ta 148,22,20768
Mvaa 116,156,192
Ta 150,22,25888
W 8
Mvaa 128,128,192
s_add_u32 s82, s81, 131072
Ta 152,25,10240
Mvaa 136,128,208
Ta 154,25,15360
buffer_load_dwordx4 a[216:219], v1, s[8:11], s81 offen offset:0 sc1
Mvaa 140,132,208
Ta 156,26,10272
Ta 158,26,15392
Mvaa 132,132,192
Ta 128,25,10304
W 9
Mvaa 144,136,192
buffer_load_dwordx4 a[232:235], v1, s[8:11], s82 offen offset:0 sc1
Ta 130,25,15424
Mvaa 152,136,208
Ta 132,26,10336
Ta 134,26,15456
Mvaa 156,140,208
Mvaa 148,140,192
buffer_load_dwordx4 a[220:223], v1, s[8:11], s81 offen offset:1024 sc1
W 8
Mvaa 160,144,192
Ta 136,25,10368
Ta 138,25,15488
Mvaa 168,144,208
Ta 140,26,10400
Ta 142,26,15520
Mvaa 172,148,208
buffer_load_dwordx4 a[236:239], v1, s[8:11], s82 offen offset:1024 sc1
Mvaa 164,148,192
Ta 144,25,10432
s_waitcnt vmcnt(10) lgkmcnt(9)
Mvaa 96,152,196
Ta 146,25,15552
Mvaa 104,152,212
buffer_load_dwordx4 a[224:227], v1, s[8:11], s81 offen offset:2048 sc1
Ta 148,26,10464
Mvaa 108,156,212
Ta 150,26,15584
Mvaa 100,156,196
Ta 152,25,10496
W 9
Mvaa 112,128,196
buffer_load_dwordx4 a[240:243], v1, s[8:11], s82 offen offset:2048 sc1
Ta 154,25,15616
Mvaa 120,128,212
Ta 156,26,10528
Ta 158,26,15648
Mvaa 124,132,212
Mvaa 116,132,196
buffer_load_dwordx4 a[228:231], v1, s[8:11], s81 offen offset:3072 sc1
W 8
Mvaa 128,136,196
Ta 128,25,20480
Ta 130,25,25600
)"
R"(Mvaa 136,136,212
Ta 132,26,20512
Ta 134,26,25632
Mvaa 140,140,212
buffer_load_dwordx4 a[244:247], v1, s[8:11], s82 offen offset:3072 sc1
s_mul_i32 s94, s94, 163840
Mvaa 132,140,196
s_add_u32 s94, s94, s48
W 8
Mvaa 144,144,196
s_mul_i32 s95, s92, 20480
s_add_u32 s95, s95, s71
Mvaa 152,144,212
s_add_u32 m0, s95, 51200
s_nop 0
D 16,40,94,0
Mvaa 156,148,212
Ta 136,25,20544
Ta 138,25,25664
Mvaa 148,148,196
Ta 140,26,20576
W 7
Mvaa 160,152,196
D 17,40,94,1024
Ta 142,26,25696
Mvaa 168,152,212
Ta 144,25,20608
Ta 146,25,25728
Mvaa 172,156,212
Ta 148,26,20640
Ta 150,26,25760
Mvaa 164,156,196
D 18,40,94,2048
s_waitcnt vmcnt(9) lgkmcnt(8)
Mvaa 96,128,216
Ta 152,25,20672
Ta 154,25,25792
Mvaa 104,128,232
Ta 156,26,20704
Ta 158,26,25824
Mvaa 108,132,232
D 19,40,94,3072
s_add_u32 m0, s95, 55296
Mvaa 100,132,216
Ta 128,25,20736
W 9
Mvaa 112,136,216
Ta 130,25,25856
Mvaa 120,136,232
buffer_load_dwordx4 v20, s[40:43], s94 offen lds
s_add_u32 s94, s94, 81920
Mvaa 124,140,232
s_mul_i32 s95, s92, 10240
s_add_u32 s95, s95, s71
Mvaa 116,140,216
s_add_u32 m0, s95, 92160
W 6
Mvaa 128,144,216
D 16,40,94,0
Ta 132,26,20768
Mvaa 136,144,232
Ta 134,26,25888
Ta 136,25,30720
Mvaa 140,148,232
Ta 138,25,35840
Ta 140,26,30752
Mvaa 132,148,216
D 17,40,94,1024
W 7
Mvaa 144,152,216
Ta 142,26,35872
Ta 144,25,30784
Mvaa 152,152,232
Ta 146,25,35904
Ta 148,26,30816
Mvaa 156,156,232
D 18,40,94,2048
D 19,40,94,3072
s_add_u32 m0, s95, 96256
s_nop 0
buffer_load_dwordx4 v20, s[40:43], s94 offen lds
Mvaa 148,156,216
Ta 150,26,35936
W 8
Mvaa 160,128,216
Ta 152,25,30848
Ta 154,25,35968
Mvaa 168,128,232
Ta 156,26,30880
Ta 158,26,36000
Mvaa 172,132,232
Mvaa 164,132,216
Ta 128,25,30912
s_waitcnt vmcnt(14) lgkmcnt(9)
Mvaa 96,136,220
s_waitcnt vmcnt(0)
s_barrier
Mvaa 104,136,236
Ta 130,25,36032
s_add_u32 s81, s58, 2
Mvaa 108,140,236
Ta 132,26,30944
Ta 134,26,36064
Mvaa 100,140,220
s_and_b32 s81, s81, 3
W 8
Mvaa 112,144,220
Ta 136,25,30976
Ta 138,25,36096
Mvaa 120,144,236
s_lshl_b32 s81, s81, 12
Ta 140,26,31008
Mvaa 124,148,236
Ta 142,26,36128
Mvaa 116,148,220
Ta 144,21,30720
W 9
Mvaa 128,152,220
Ta 146,21,35840
s_add_u32 s81, s81, s49
Mvaa 136,152,236
Ta 148,22,30752
Ta 150,22,35872
Mvaa 140,156,236
s_add_u32 s82, s81, 131072
v_and_b32 v27, 1, v12
Mvaa 132,156,220
Ta 152,21,30784
W 9
Mvaa 144,128,220
Ta 154,21,35904
Mvaa 152,128,236
v_lshlrev_b32 v27, 1, v27
buffer_load_dwordx4 a[0:3], v1, s[8:11], s81 offen offset:0 sc1
Mvaa 156,132,236
Ta 156,22,30816
Ta 158,22,35936
Mvaa 148,132,220
v_xor_b32 v27, v27, v13
W 8
Mvaa 160,136,220
buffer_load_dwordx4 a[16:19], v1, s[8:11], s82 offen offset:0 sc1
Ta 128,21,30848
Mvaa 168,136,236
Ta 130,21,35968
v_and_b32 v12, 2, v12
Mvaa 172,140,236
Ta 132,22,30880
Ta 134,22,36000
Mvaa 164,140,220
buffer_load_dwordx4 a[4:7], v1, s[8:11], s81 offen offset:1024 sc1
W 8
Mvaa 96,144,224
Ta 136,21,30912
Ta 138,21,36032
Mvaa 104,144,240
v_or_b32 v12, v12, v14
Ta 140,22,30944
Mvaa 108,148,240
buffer_load_dwordx4 a[20:23], v1, s[8:11], s82 offen offset:1024 sc1
Ta 142,22,36064
Mvaa 100,148,224
v_lshl_or_b32 v12, v27, 2, v12
W 8
Mvaa 112,152,224
Ta 144,21,30976
Ta 146,21,36096
Mvaa 120,152,240
buffer_load_dwordx4 a[8:11], v1, s[8:11], s81 offen offset:2048 sc1
v_cmp_eq_u32_e64 s[86:87], 0, v12
Mvaa 124,156,240
Ta 148,22,31008
Ta 150,22,36128
Mvaa 116,156,224
v_cndmask_b32_e64 v4, 0, v15, s[86:87]
W 8
Mvaa 128,128,224
buffer_load_dwordx4 a[24:27], v1, s[8:11], s82 offen offset:2048 sc1
Ta 152,23,0
Mvaa 136,128,240
Ta 154,23,5120
v_cmp_eq_u32_e64 s[86:87], 1, v12
Mvaa 140,132,240
Ta 156,24,32
Ta 158,24,5152
Mvaa 132,132,224
buffer_load_dwordx4 a[12:15], v1, s[8:11], s81 offen offset:3072 sc1
W 8
Mvaa 144,136,224
v_cndmask_b32_e64 v5, 0, v15, s[86:87]
Ta 128,23,64
Mvaa 152,136,240
Ta 130,23,5184
v_cmp_eq_u32_e64 s[86:87], 2, v12
Mvaa 156,140,240
buffer_load_dwordx4 a[28:31], v1, s[8:11], s82 offen offset:3072 sc1
s_add_u32 s81, s58, 3
Mvaa 148,140,224
Ta 132,24,96
W 7
Mvaa 160,144,224
Ta 134,24,5216
s_and_b32 s81, s81, 3
Mvaa 168,144,240
Ta 136,23,128
Ta 138,23,5248
Mvaa 172,148,240
v_cndmask_b32_e64 v6, 0, v15, s[86:87]
s_lshl_b32 s81, s81, 12
Mvaa 164,148,224
Ta 140,24,160
W 7
Mvaa 96,152,228
Ta 142,24,5280
v_cmp_eq_u32_e64 s[86:87], 3, v12
Mvaa 104,152,244
Ta 144,23,192
Ta 146,23,5312
Mvaa 108,156,244
s_add_u32 s81, s81, s49
v_cndmask_b32_e64 v7, 0, v15, s[86:87]
Mvaa 100,156,228
Ta 148,24,224
W 7
Mvaa 112,128,228
Ta 150,24,5344
s_add_u32 s82, s81, 131072
Mvaa 120,128,244
v_cmp_eq_u32_e64 s[86:87], 4, v12
Ta 152,23,256
Mvaa 124,132,244
Ta 154,23,5376
v_cndmask_b32_e64 v8, 0, v15, s[86:87]
Mvaa 116,132,228
buffer_load_dwordx4 a[32:35], v1, s[8:11], s81 offen offset:0 sc1
W 6
Mvaa 128,136,228
Ta 156,24,288
Ta 158,24,5408
Mvaa 136,136,244
v_cmp_eq_u32_e64 s[86:87], 5, v12
Ta 128,23,20480
Mvaa 140,140,244
buffer_load_dwordx4 a[48:51], v1, s[8:11], s82 offen offset:0 sc1
Ta 130,23,25600
Mvaa 132,140,228
v_cndmask_b32_e64 v9, 0, v15, s[86:87]
W 6
Mvaa 144,144,228
Ta 132,24,20512
Ta 134,24,25632
Mvaa 152,144,244
buffer_load_dwordx4 a[36:39], v1, s[8:11], s81 offen offset:1024 sc1
Ta 136,23,20544
Mvaa 156,148,244
Ta 138,23,25664
v_cmp_eq_u32_e64 s[86:87], 6, v12
Mvaa 148,148,228
Ta 140,24,20576
W 7
Mvaa 160,152,228
buffer_load_dwordx4 a[52:55], v1, s[8:11], s82 offen offset:1024 sc1
Ta 142,24,25696
Mvaa 168,152,244
v_cndmask_b32_e64 v10, 0, v15, s[86:87]
Ta 144,23,20608
Mvaa 172,156,244
Ta 146,23,25728
v_cmp_eq_u32_e64 s[86:87], 7, v12
Mvaa 164,156,228
buffer_load_dwordx4 a[40:43], v1, s[8:11], s81 offen offset:2048 sc1
s_waitcnt vmcnt(11) lgkmcnt(6)
Mvaa 96,128,0
Ta 148,24,20640
Ta 150,24,25760
Mvaa 104,128,16
v_cndmask_b32_e64 v11, 0, v15, s[86:87]
Ta 152,23,20672
Mvaa 108,132,16
buffer_load_dwordx4 a[56:59], v1, s[8:11], s82 offen offset:2048 sc1
Ta 154,23,25792
Mvaa 100,132,0
s_mov_b32 s20, s38
W 6
Mvaa 112,136,0
Ta 156,24,20704
Ta 158,24,25824
Mvaa 120,136,16
buffer_load_dwordx4 a[44:47], v1, s[8:11], s81 offen offset:3072 sc1
s_and_b32 s21, s39, 0xffff
Mvaa 124,140,16
Ta 128,23,20736
Ta 130,23,25856
Mvaa 116,140,0
s_mov_b32 s22, 2621440
W 6
Mvaa 128,144,0
buffer_load_dwordx4 a[60:63], v1, s[8:11], s82 offen offset:3072 sc1
Ta 132,24,20768
Mvaa 136,144,16
Ta 134,24,25888
Ta 136,23,51200
Mvaa 140,148,16
Ta 138,23,56320
s_mov_b32 s23, 0x20000
Mvaa 132,148,0
v_bfe_u32 v28, v1, 4, 2
W 6
Mvaa 144,152,0
v_lshlrev_b32 v28, 3, v28
Ta 140,24,51232
Mvaa 152,152,16
Ta 142,24,56352
buffer_load_dwordx2 v[28:29], v28, s[8:11], s55 offen sc1
Mvaa 156,156,16
s_and_b32 s17, s17, 0xffff
Ta 144,23,51264
Mvaa 148,156,0
Ta 146,23,56384
W 6
Mvaa 160,128,0
s_mov_b32 s18, 2621440
Ta 148,24,51296
Mvaa 168,128,16
Ta 150,24,56416
s_mov_b32 s19, 0x20000
Mvaa 172,132,16
Ta 152,23,51328
Ta 154,23,56448
Mvaa 164,132,0
s_lshr_b32 s66, s15, 3
s_waitcnt vmcnt(13) lgkmcnt(6)
Mvaa 96,136,4
Ta 156,24,51360
Ta 158,24,56480
Mvaa 104,136,20
s_lshl_b32 s66, s66, 4
Ta 128,23,51392
Mvaa 108,140,20
Ta 130,23,56512
s_add_u32 s66, s66, s59
Mvaa 100,140,4
Ta 132,24,51424
W 7
Mvaa 112,144,4
Ta 134,24,56544
Ta 136,23,51456
Mvaa 120,144,20
Ta 138,23,56576
s_lshl_b32 s63, s66, 6
Mvaa 124,148,20
Ta 140,24,51488
Ta 142,24,56608
Mvaa 116,148,4
s_add_u32 s63, s63, 65536
W 8
Mvaa 128,152,4
Ta 144,25,0
Ta 146,25,5120
Mvaa 136,152,20
s_mul_i32 s62, s66, 40960
Ta 148,26,32
Mvaa 140,156,20
Ta 150,26,5152
s_add_u32 s62, s62, 56623104
Mvaa 132,156,4
Ta 152,25,64
W 9
Mvaa 144,128,4
Ta 154,25,5184
Mvaa 152,128,20
s_lshl_b32 s66, s14, 10
Ta 156,26,96
Mvaa 156,132,20
Ta 158,26,5216
Mvaa 148,132,4
s_add_u32 s78, s62, s66
W 8
Mvaa 160,136,4
Ta 128,25,128
Ta 130,25,5248
Mvaa 168,136,20
s_add_u32 s79, s78, 327680
Ta 132,26,160
Mvaa 172,140,20
Ta 134,26,5280
Mvaa 164,140,4
Ta 136,25,192
s_waitcnt vmcnt(11) lgkmcnt(9)
Mvaa 96,144,8
Ta 138,25,5312
s_and_b32 s66, s14, 1
Mvaa 104,144,24
Ta 140,26,224
Ta 142,26,5344
Mvaa 108,148,24
s_mul_i32 s66, s66, 327680
Mvaa 100,148,8
Ta 144,25,256
W 9
)"
R"(Mvaa 112,152,8
Ta 146,25,5376
s_add_u32 s65, s62, s66
Mvaa 120,152,24
Ta 148,26,288
Ta 150,26,5408
Mvaa 124,156,24
s_add_u32 s65, s65, 4096
Mvaa 116,156,8
Ta 152,21,40960
W 9
Mvaa 128,128,8
Ta 154,21,46080
Mvaa 136,128,24
s_mov_b32 s64, 0
Ta 156,22,40992
Mvaa 140,132,24
Ta 158,22,46112
s_waitcnt vmcnt(0)
v_cmp_eq_u64 vcc, s[12:13], v[28:29]
s_cmp_eq_u64 vcc, -1
s_cbranch_scc0 .Lspin_104%=
.Lok_105%=:
Mvaa 132,132,8
s_add_u32 s81, s58, 1
W 8
Mvaa 144,136,8
v_bfe_u32 v27, v1, 4, 3
Ta 128,21,41024
Mvaa 152,136,24
Ta 130,21,46144
v_lshlrev_b32 v27, 3, v27
Mvaa 156,140,24
Ta 132,22,41056
Ta 134,22,46176
Mvaa 148,140,8
s_and_b32 s81, s81, 3
W 8
Mvaa 160,144,8
Ta 136,21,41088
Ta 138,21,46208
Mvaa 168,144,24
v_bfe_u32 v20, v1, 4, 4
s_lshl_b32 s81, s81, 12
Mvaa 172,148,24
Ta 140,22,41120
Ta 142,22,46240
Mvaa 164,148,8
v_mul_u32_u24 v20, 1280, v20
W 8
Mvaa 96,152,12
Ta 144,21,41152
Ta 146,21,46272
Mvaa 104,152,28
s_add_u32 s81, s81, s50
v_lshrrev_b32 v12, 8, v1
Mvaa 108,156,28
Ta 148,22,41184
Ta 150,22,46304
Mvaa 100,156,12
v_lshl_add_u32 v20, v12, 4, v20
W 8
Mvaa 112,128,12
s_add_u32 s82, s81, 131072
Ta 152,21,41216
Mvaa 120,128,28
Ta 154,21,46336
s_bfe_u32 s66, s59, 0x10001
Mvaa 124,132,28
Ta 156,22,41248
Ta 158,22,46368
Mvaa 116,132,12
buffer_load_dwordx4 a[64:67], v1, s[8:11], s81 offen offset:0 sc1
W 8
Mvaa 128,136,12
s_lshl_b32 s67, s57, 1
Ta 128,21,51200
Mvaa 136,136,28
Ta 130,21,56320
s_add_u32 s66, s66, s67
Mvaa 140,140,28
buffer_load_dwordx4 a[80:83], v1, s[8:11], s82 offen offset:0 sc1
Ta 132,22,51232
Mvaa 132,140,12
Ta 134,22,56352
W 8
Mvaa 144,144,12
s_lshl_b32 s61, s66, 2
Ta 136,21,51264
Mvaa 152,144,28
Ta 138,21,56384
buffer_load_dwordx4 a[68:71], v1, s[8:11], s81 offen offset:1024 sc1
Mvaa 156,148,28
Ta 140,22,51296
Ta 142,22,56416
Mvaa 148,148,12
s_lshr_b32 s66, s59, 2
W 8
Mvaa 160,152,12
buffer_load_dwordx4 a[84:87], v1, s[8:11], s82 offen offset:1024 sc1
Ta 144,21,51328
Mvaa 168,152,28
Ta 146,21,56448
s_lshl_b32 s66, s66, 1
Mvaa 172,156,28
Ta 148,22,51360
Ta 150,22,56480
Mvaa 164,156,12
buffer_load_dwordx4 a[72:75], v1, s[8:11], s81 offen offset:2048 sc1
W 8
Mvaa 96,128,32
s_add_u32 s61, s61, s66
Ta 152,21,51392
Mvaa 104,128,48
Ta 154,21,56512
s_and_b32 s66, s59, 1
Mvaa 108,132,48
buffer_load_dwordx4 a[88:91], v1, s[8:11], s82 offen offset:2048 sc1
Ta 156,22,51424
Mvaa 100,132,32
Ta 158,22,56544
W 8
Mvaa 112,136,32
s_add_u32 s61, s61, s66
Ta 128,21,51456
Mvaa 120,136,48
Ta 130,21,56576
buffer_load_dwordx4 a[76:79], v1, s[8:11], s81 offen offset:3072 sc1
Mvaa 124,140,48
s_xor_b32 s67, s61, 0
Ta 132,22,51488
Mvaa 116,140,32
Ta 134,22,56608
W 8
Mvaa 128,144,32
buffer_load_dwordx4 a[92:95], v1, s[8:11], s82 offen offset:3072 sc1
s_mul_i32 s72, s67, 20480
Mvaa 136,144,48
Ta 136,23,10240
Ta 138,23,15360
Mvaa 140,148,48
Ta 140,24,10272
Ta 142,24,15392
Mvaa 132,148,32
s_mul_i32 s66, s58, 320
W 8
Mvaa 144,152,32
s_add_u32 s81, s58, 2
Ta 144,23,10304
Mvaa 152,152,48
Ta 146,23,15424
s_add_u32 s72, s72, s66
Mvaa 156,156,48
Ta 148,24,10336
Ta 150,24,15456
Mvaa 148,156,32
s_and_b32 s81, s81, 3
W 8
Mvaa 160,128,32
s_lshr_b32 s66, s67, 2
Ta 152,23,10368
Mvaa 168,128,48
Ta 154,23,15488
s_mul_i32 s75, s66, 81920
Mvaa 172,132,48
s_lshl_b32 s81, s81, 12
Ta 156,24,10400
Mvaa 164,132,32
Ta 158,24,15520
W 8
Mvaa 96,136,36
s_mul_i32 s66, s58, 20480
Ta 128,23,10432
Mvaa 104,136,52
Ta 130,23,15552
s_add_u32 s81, s81, s50
Mvaa 108,140,52
s_add_u32 s75, s75, s66
Ta 132,24,10464
Mvaa 100,140,36
Ta 134,24,15584
W 8
Mvaa 112,144,36
s_add_u32 s82, s81, 131072
s_and_b32 s66, s67, 3
Mvaa 120,144,52
Ta 136,23,10496
Ta 138,23,15616
Mvaa 124,148,52
Ta 140,24,10528
Ta 142,24,15648
Mvaa 116,148,36
s_lshl_b32 s66, s66, 10
W 8
Mvaa 128,152,36
buffer_load_dwordx4 a[96:99], v1, s[8:11], s81 offen offset:0 sc1
Ta 144,23,30720
Mvaa 136,152,52
Ta 146,23,35840
s_add_u32 s75, s75, s66
Mvaa 140,156,52
Ta 148,24,30752
Ta 150,24,35872
Mvaa 132,156,36
buffer_load_dwordx4 a[112:115], v1, s[8:11], s82 offen offset:0 sc1
W 8
Mvaa 144,128,36
s_add_u32 s75, s75, 131072
Ta 152,23,30784
Mvaa 152,128,52
Ta 154,23,35904
s_xor_b32 s67, s61, 124
Mvaa 156,132,52
buffer_load_dwordx4 a[100:103], v1, s[8:11], s81 offen offset:1024 sc1
Ta 156,24,30816
Mvaa 148,132,36
Ta 158,24,35936
W 8
Mvaa 160,136,36
s_mul_i32 s73, s67, 20480
Ta 128,23,30848
Mvaa 168,136,52
buffer_load_dwordx4 a[116:119], v1, s[8:11], s82 offen offset:1024 sc1
Ta 130,23,35968
Mvaa 172,140,52
s_mul_i32 s66, s58, 320
Ta 132,24,30880
Mvaa 164,140,36
Ta 134,24,36000
W 8
Mvaa 96,144,40
buffer_load_dwordx4 a[104:107], v1, s[8:11], s81 offen offset:2048 sc1
s_add_u32 s73, s73, s66
Mvaa 104,144,56
Ta 136,23,30912
Ta 138,23,36032
Mvaa 108,148,56
Ta 140,24,30944
Ta 142,24,36064
Mvaa 100,148,40
buffer_load_dwordx4 a[120:123], v1, s[8:11], s82 offen offset:2048 sc1
W 8
Mvaa 112,152,40
s_lshr_b32 s66, s67, 2
Ta 144,23,30976
Mvaa 120,152,56
Ta 146,23,36096
s_mul_i32 s76, s66, 81920
Mvaa 124,156,56
buffer_load_dwordx4 a[108:111], v1, s[8:11], s81 offen offset:3072 sc1
Ta 148,24,31008
Mvaa 116,156,40
Ta 150,24,36128
W 8
Mvaa 128,128,40
s_mul_i32 s66, s58, 20480
Ta 152,23,40960
Mvaa 136,128,56
buffer_load_dwordx4 a[124:127], v1, s[8:11], s82 offen offset:3072 sc1
Ta 154,23,46080
Mvaa 140,132,56
s_add_u32 s76, s76, s66
Ta 156,24,40992
Mvaa 132,132,40
Ta 158,24,46112
W 8
Mvaa 144,136,40
s_add_u32 s81, s58, 3
s_and_b32 s66, s67, 3
Mvaa 152,136,56
Ta 128,23,41024
Ta 130,23,46144
Mvaa 156,140,56
s_lshl_b32 s66, s66, 10
Ta 132,24,41056
Mvaa 148,140,40
Ta 134,24,46176
W 8
Mvaa 160,144,40
s_and_b32 s81, s81, 3
s_add_u32 s76, s76, s66
Mvaa 168,144,56
Ta 136,23,41088
Ta 138,23,46208
Mvaa 172,148,56
s_lshl_b32 s81, s81, 12
Ta 140,24,41120
Mvaa 164,148,40
Ta 142,24,46240
W 8
Mvaa 96,152,44
s_add_u32 s76, s76, 131072
Ta 144,23,41152
Mvaa 104,152,60
Ta 146,23,46272
s_add_u32 s81, s81, s50
Mvaa 108,156,60
s_bitcmp1_b32 s14, 0
s_cselect_b32 s74, s73, s72
s_cselect_b32 s77, s76, s75
Mvaa 100,156,44
Ta 148,24,41184
W 7
Mvaa 112,128,44
Ta 150,24,46304
s_add_u32 s74, s74, 256
Mvaa 120,128,60
s_add_u32 s82, s81, 131072
Ta 152,23,41216
Mvaa 124,132,60
Ta 154,23,46336
s_add_u32 s77, s77, 16384
Mvaa 116,132,44
Ta 156,24,41248
W 7
Mvaa 128,136,44
Ta 158,24,46368
buffer_load_dwordx4 a[184:187], v1, s[8:11], s81 offen offset:0 sc1
Mvaa 136,136,60
s_lshl_b32 s66, s14, 6
Ta 128,25,20480
Mvaa 140,140,60
Ta 130,25,25600
s_lshl_b32 s67, s14, 12
Mvaa 132,140,44
buffer_load_dwordx4 a[200:203], v1, s[8:11], s82 offen offset:0 sc1
W 6
Mvaa 144,144,44
Ta 132,26,20512
Ta 134,26,25632
Mvaa 152,144,60
K 96,96,97
s_add_u32 s72, s72, s66
Mvaa 156,148,60
Ta 136,25,20544
Ta 138,25,25664
Mvaa 148,148,44
K 97,98,99
W 6
Mvaa 160,152,44
buffer_load_dwordx4 a[188:191], v1, s[8:11], s81 offen offset:1024 sc1
K 98,100,101
Mvaa 168,152,60
Ta 140,26,20576
Ta 142,26,25696
Mvaa 172,156,60
K 99,102,103
s_add_u32 s75, s75, s67
Mvaa 164,156,44
s_add_u32 s83, s52, 0
s_waitcnt vmcnt(17) lgkmcnt(4)
Mvaa 176,128,64
buffer_load_dwordx4 a[204:207], v1, s[8:11], s82 offen offset:1024 sc1
Ta 144,25,20608
Mvaa 184,128,80
Ta 146,25,25728
s_add_u32 s73, s73, s66
Mvaa 188,132,80
Ta 148,26,20640
Ta 150,26,25760
Mvaa 180,132,64
buffer_store_dwordx4 v[96:99], v1, s[8:11], s83 offen sc1
W 4
Mvaa 192,136,64
K 104,104,105
K 105,106,107
Mvaa 200,136,80
s_add_u32 s76, s76, s67
K 106,108,109
Mvaa 204,140,80
buffer_load_dwordx4 a[192:195], v1, s[8:11], s81 offen offset:2048 sc1
Ta 152,25,20672
Mvaa 196,140,64
Ta 154,25,25792
Ta 156,26,20704
Ta 158,26,25824
W 4
Mvaa 208,144,64
K 107,110,111
s_add_u32 s83, s52, 40960
Mvaa 216,144,80
buffer_load_dwordx4 a[208:211], v1, s[8:11], s82 offen offset:2048 sc1
Ta 128,25,20736
Mvaa 220,148,80
Ta 130,25,25856
Ta 132,26,20768
Mvaa 212,148,64
Ta 134,26,25888
W 4
Mvaa 224,152,64
buffer_store_dwordx4 v[104:107], v1, s[8:11], s83 offen sc1
K 112,112,113
Mvaa 232,152,80
K 113,114,115
K 114,116,117
Mvaa 236,156,80
K 115,118,119
s_add_u32 s83, s52, 1024
Mvaa 228,156,64
Ta 136,25,30720
)"
R"(Ta 138,25,35840
Ta 140,26,30752
Ta 142,26,35872
W 4
Mvaa 240,128,64
buffer_load_dwordx4 a[196:199], v1, s[8:11], s81 offen offset:3072 sc1
Ta 144,25,30784
Mvaa 248,128,80
Ta 146,25,35904
Ta 148,26,30816
Mvaa 252,132,80
Ta 150,26,35936
Ta 152,25,30848
Mvaa 244,132,64
buffer_load_dwordx4 v[36:39], v20, s[16:19], s72 offen
s_waitcnt vmcnt(22) lgkmcnt(5)
Mvaa 176,136,68
Ta 154,25,35968
Ta 156,26,30880
Mvaa 184,136,84
Ta 158,26,36000
Ta 128,25,30912
Mvaa 188,140,84
buffer_load_dwordx4 a[212:215], v1, s[8:11], s82 offen offset:3072 sc1
Ta 130,25,36032
Mvaa 180,140,68
Ta 132,26,30944
W 7
Mvaa 192,144,68
Ta 134,26,36064
Ta 136,25,30976
Mvaa 200,144,84
buffer_store_dwordx4 v[112:115], v1, s[8:11], s83 offen sc1
K 120,120,121
Mvaa 204,148,84
K 121,122,123
K 122,124,125
Mvaa 196,148,68
K 123,126,127
W 5
Mvaa 208,152,68
buffer_load_dwordx4 v[44:47], v1, s[8:11], s75 offen sc1
s_add_u32 s83, s52, 41984
Mvaa 216,152,84
Ta 138,25,36096
Ta 140,26,31008
Mvaa 220,156,84
Ta 142,26,36128
Ta 144,21,30720
Mvaa 212,156,68
buffer_store_dwordx4 v[120:123], v1, s[8:11], s83 offen sc1
W 5
Mvaa 224,128,68
K 128,128,129
K 129,130,131
Mvaa 232,128,84
K 130,132,133
K 131,134,135
Mvaa 236,132,84
buffer_load_dwordx4 v[52:55], v20, s[16:19], s73 offen
s_add_u32 s83, s52, 2048
Mvaa 228,132,68
Ta 146,21,35840
Ta 148,22,30752
Ta 150,22,35872
W 4
Mvaa 240,136,68
Ta 152,21,30784
Ta 154,21,35904
Mvaa 248,136,84
buffer_store_dwordx4 v[128:131], v1, s[8:11], s83 offen sc1
K 136,136,137
Mvaa 252,140,84
K 137,138,139
K 138,140,141
Mvaa 244,140,68
Ta 156,22,30816
Ta 158,22,35936
s_waitcnt vmcnt(26) lgkmcnt(4)
Mvaa 176,144,72
buffer_load_dwordx4 v[60:63], v1, s[8:11], s76 offen sc1
K 139,142,143
Mvaa 184,144,88
s_add_u32 s83, s52, 43008
Ta 128,21,30848
Mvaa 188,148,88
Ta 130,21,35968
Ta 132,22,30880
Mvaa 180,148,72
Ta 134,22,36000
W 4
Mvaa 192,152,72
buffer_load_dwordx4 v[68:71], v20, s[16:19], s74 offen
Ta 136,21,30912
Mvaa 200,152,88
Ta 138,21,36032
Ta 140,22,30944
Mvaa 204,156,88
Ta 142,22,36064
Ta 144,21,30976
Mvaa 196,156,72
buffer_store_dwordx4 v[136:139], v1, s[8:11], s83 offen sc1
W 5
Mvaa 208,128,72
K 144,144,145
K 145,146,147
Mvaa 216,128,88
K 146,148,149
K 147,150,151
Mvaa 220,132,88
buffer_load_dwordx4 v[76:79], v1, s[8:11], s77 offen sc1
s_add_u32 s83, s52, 3072
Mvaa 212,132,72
Ta 146,21,36096
Ta 148,22,31008
Ta 150,22,36128
W 4
Mvaa 224,136,72
Ta 152,23,0
Ta 154,23,5120
Mvaa 232,136,88
buffer_store_dwordx4 v[144:147], v1, s[8:11], s83 offen sc1
K 152,152,153
Mvaa 236,140,88
K 153,154,155
K 154,156,157
Mvaa 228,140,72
Ta 156,24,32
Ta 158,24,5152
W 4
Mvaa 240,144,72
buffer_load_dwordx2 v[12:13], v27, s[8:11], 0 offen offset:3584 sc1
K 155,158,159
Mvaa 248,144,88
s_add_u32 s83, s52, 44032
v_lshrrev_b32 v14, 1, v1
Mvaa 252,148,88
v_min_u32 v15, 344, v14
Ta 128,23,64
Mvaa 244,148,72
Ta 130,23,5184
Ta 132,24,96
Ta 134,24,5216
s_waitcnt vmcnt(30) lgkmcnt(4)
Mvaa 176,152,76
buffer_store_dwordx4 v[152:155], v1, s[8:11], s83 offen sc1
K 160,160,161
Mvaa 184,152,92
K 161,162,163
K 162,164,165
Mvaa 188,156,92
K 163,166,167
s_add_u32 s83, s52, 4096
Mvaa 180,156,76
Ta 136,23,128
Ta 138,23,5248
Ta 140,24,160
Ta 142,24,5280
W 4
Mvaa 192,128,76
buffer_store_dwordx4 v[160:163], v1, s[8:11], s83 offen sc1
K 168,168,169
Mvaa 200,128,92
K 169,170,171
K 170,172,173
Mvaa 204,132,92
K 171,174,175
s_add_u32 s83, s52, 45056
Mvaa 196,132,76
Ta 144,23,192
Ta 146,23,5312
Ta 148,24,224
Ta 150,24,5344
W 4
Mvaa 208,136,76
buffer_store_dwordx4 v[168:171], v1, s[8:11], s83 offen sc1
Ta 152,23,256
Mvaa 216,136,92
Ta 154,23,5376
Ta 156,24,288
Mvaa 220,140,92
Ta 158,24,5408
Ta 128,23,20480
Mvaa 212,140,76
buffer_load_dwordx2 v[16:17], v14, s[8:11], 0 offen offset:2560 sc1
W 5
Mvaa 224,144,76
Ta 130,23,25600
Ta 132,24,20512
Mvaa 232,144,92
Ta 134,24,25632
Ta 136,23,51200
Mvaa 236,148,92
buffer_load_dwordx2 v[18:19], v15, s[8:11], 0 offen offset:3072 sc1
s_waitcnt vmcnt(0)
v_xor_b32 v12, s12, v12
v_xor_b32 v13, s13, v13
v_lshrrev_b32 v94, 11, v12
v_or_b32 v13, v13, v94
v_cmp_eq_u32 vcc, 0, v13
s_cmp_eq_u64 vcc, -1
s_cbranch_scc0 .LL_109%=
.LL_110%=:
v_bfe_u32 v13, v12, 4, 7
s_xor_b32 s66, s61, 124
v_cmp_eq_u32 vcc, s61, v13
v_cmp_eq_u32_e64 s[86:87], s66, v13
s_or_b32 s68, vcc_lo, s86
s_and_b32 s68, s68, 0xff
s_cbranch_scc1 .Lup_107%=
.Lupr_108%=:
Mvaa 228,148,76
Ta 138,23,56320
W 6
Mvaa 240,152,76
v_lshlrev_b32 v32, 16, v36
v_and_b32 v33, 0xffff0000, v36
Mvaa 248,152,92
Ta 140,24,51232
Ta 142,24,56352
Mvaa 252,156,92
v_lshlrev_b32 v34, 16, v37
v_and_b32 v35, 0xffff0000, v37
Mvaa 244,156,76
Ta 144,25,0
W 5
Mvaa 176,128,96
Ta 146,25,5120
v_lshlrev_b32 v36, 16, v38
Mvaa 184,128,112
v_and_b32 v37, 0xffff0000, v38
Ta 148,26,32
Mvaa 188,132,112
Ta 150,26,5152
v_lshlrev_b32 v38, 16, v39
Mvaa 180,132,96
v_and_b32 v39, 0xffff0000, v39
W 4
Mvaa 176,136,100
Ta 152,21,40960
Ta 154,21,46080
Mvaa 184,136,116
v_lshlrev_b32 v40, 16, v44
v_and_b32 v41, 0xffff0000, v44
Mvaa 188,140,116
Ta 156,22,40992
Ta 158,22,46112
Mvaa 180,140,100
v_lshlrev_b32 v42, 16, v45
W 4
Mvaa 176,144,104
v_and_b32 v43, 0xffff0000, v45
Ta 128,21,51200
Mvaa 184,144,120
Ta 130,21,56320
v_lshlrev_b32 v44, 16, v46
Mvaa 188,148,120
v_and_b32 v45, 0xffff0000, v46
Ta 132,22,51232
Mvaa 180,148,104
Ta 134,22,56352
W 4
Mvaa 176,152,108
v_lshlrev_b32 v46, 16, v47
v_and_b32 v47, 0xffff0000, v47
Mvaa 184,152,124
v_lshlrev_b32 v2, 2, v1
v_mov_b64 v[28:29], s[12:13]
s_mov_b64 exec, 3
buffer_store_dwordx2 v[28:29], v2, s[8:11], s53 offen offset:0 sc1
s_mov_b64 exec, -1
Mvaa 188,156,124
Ta 136,23,10240
Ta 138,23,15360
Mvaa 180,156,108
Ta 140,24,10272
Ta 142,24,15392
W 4
Mvaa 176,128,184
v_lshlrev_b32 v48, 16, v52
v_and_b32 v49, 0xffff0000, v52
Mvaa 184,128,200
v_lshlrev_b32 v50, 16, v53
v_and_b32 v51, 0xffff0000, v53
Mvaa 188,132,200
Ta 144,23,30720
Ta 146,23,35840
Mvaa 180,132,184
Ta 148,24,30752
Ta 150,24,35872
W 4
Mvaa 176,136,188
v_lshlrev_b32 v52, 16, v54
v_and_b32 v53, 0xffff0000, v54
Mvaa 184,136,204
v_lshlrev_b32 v54, 16, v55
v_and_b32 v55, 0xffff0000, v55
Mvaa 188,140,204
Ta 152,23,40960
Ta 154,23,46080
Mvaa 180,140,188
Ta 156,24,40992
Ta 158,24,46112
W 4
Mvaa 176,144,192
v_lshlrev_b32 v56, 16, v60
v_and_b32 v57, 0xffff0000, v60
Mvaa 184,144,208
v_lshlrev_b32 v58, 16, v61
v_and_b32 v59, 0xffff0000, v61
Mvaa 188,148,208
Ta 128,23,20544
Ta 130,23,25664
Mvaa 180,148,192
Ta 132,24,20576
Ta 134,24,25696
W 4
Mvaa 176,152,196
v_lshlrev_b32 v60, 16, v62
v_and_b32 v61, 0xffff0000, v62
Mvaa 184,152,212
v_lshlrev_b32 v62, 16, v63
v_and_b32 v63, 0xffff0000, v63
Mvaa 188,156,212
Ta 136,23,51264
Ta 138,23,56384
Mvaa 180,156,196
Ta 140,24,51296
Ta 142,24,56416
W 4
Mvaa 192,128,96
v_lshlrev_b32 v64, 16, v68
v_and_b32 v65, 0xffff0000, v68
Mvaa 200,128,112
v_lshlrev_b32 v66, 16, v69
v_and_b32 v67, 0xffff0000, v69
Mvaa 204,132,112
Ta 144,25,64
Ta 146,25,5184
Mvaa 196,132,96
Ta 148,26,96
Ta 150,26,5216
W 4
Mvaa 192,136,100
v_lshlrev_b32 v68, 16, v70
v_and_b32 v69, 0xffff0000, v70
Mvaa 200,136,116
v_lshlrev_b32 v70, 16, v71
v_and_b32 v71, 0xffff0000, v71
Mvaa 204,140,116
Ta 152,21,41024
Ta 154,21,46144
Mvaa 196,140,100
Ta 156,22,41056
Ta 158,22,46176
W 4
Mvaa 192,144,104
v_lshlrev_b32 v72, 16, v76
v_and_b32 v73, 0xffff0000, v76
Mvaa 200,144,120
v_lshlrev_b32 v74, 16, v77
K 176,176,177
Mvaa 204,148,120
v_and_b32 v75, 0xffff0000, v77
Ta 128,21,51264
Mvaa 196,148,104
Ta 130,21,56384
v_lshlrev_b32 v76, 16, v78
v_and_b32 v77, 0xffff0000, v78
Ta 132,22,51296
Ta 134,22,56416
v_lshlrev_b32 v78, 16, v79
v_and_b32 v79, 0xffff0000, v79
buffer_load_dwordx2 v[28:29], v27, s[8:11], s63 offen offset:0 sc1
W 4
Mvaa 192,152,108
s_mov_b64 s[40:41], s[8:9]
K 177,178,179
Mvaa 200,152,124
K 178,180,181
K 179,182,183
Mvaa 204,156,124
Ta 136,23,10304
Ta 138,23,15424
Mvaa 196,156,108
Ta 140,24,10336
Ta 142,24,15456
W 4
Mvaa 192,128,184
s_add_u32 s83, s52, 327680
buffer_store_dwordx4 v[176:179], v1, s[8:11], s83 offen sc1
Mvaa 200,128,200
K 184,184,185
K 185,186,187
Mvaa 204,132,200
K 186,188,189
K 187,190,191
Mvaa 196,132,184
Ta 144,23,30784
Ta 146,23,35904
)"
R"(Ta 148,24,30816
Ta 150,24,35936
W 4
Mvaa 192,136,188
s_add_u32 s83, s52, 368640
buffer_store_dwordx4 v[184:187], v1, s[8:11], s83 offen sc1
Mvaa 200,136,204
Ta 152,23,41024
Ta 154,23,46144
Mvaa 204,140,204
Ta 156,24,41056
Ta 158,24,46176
Mvaa 196,140,188
Ta 128,23,20608
W 5
Mvaa 192,144,192
Ta 130,23,25728
Ta 132,24,20640
Mvaa 200,144,208
Ta 134,24,25760
Ta 136,23,51328
Mvaa 204,148,208
Ta 138,23,56448
Ta 140,24,51360
Mvaa 196,148,192
Ta 142,24,56480
W 8
Mvaa 192,152,196
Ta 144,25,128
Ta 146,25,5248
Mvaa 200,152,212
Ta 148,26,160
Ta 150,26,5280
Mvaa 204,156,212
Mvaa 196,156,196
Ta 152,21,41088
W 9
Mvaa 208,128,96
Ta 154,21,46208
Mvaa 216,128,112
Ta 156,22,41120
Ta 158,22,46240
Mvaa 220,132,112
K 192,192,193
Mvaa 212,132,96
Ta 128,21,51328
W 9
Mvaa 208,136,100
Ta 130,21,56448
K 193,194,195
Mvaa 216,136,116
K 194,196,197
Ta 132,22,51360
Mvaa 220,140,116
Ta 134,22,56480
K 195,198,199
Mvaa 212,140,100
Ta 136,23,10368
W 9
Mvaa 208,144,104
Ta 138,23,15488
s_add_u32 s83, s52, 328704
Mvaa 216,144,120
buffer_store_dwordx4 v[192:195], v1, s[8:11], s83 offen sc1
K 200,200,201
Mvaa 220,148,120
K 201,202,203
K 202,204,205
Mvaa 212,148,104
K 203,206,207
W 6
Mvaa 208,152,108
s_add_u32 s83, s52, 369664
buffer_store_dwordx4 v[200:203], v1, s[8:11], s83 offen sc1
Mvaa 216,152,124
Ta 140,24,10400
Ta 142,24,15520
Mvaa 220,156,124
Ta 144,23,30848
Ta 146,23,35968
Mvaa 212,156,108
Ta 148,24,30880
W 7
Mvaa 208,128,184
Ta 150,24,36000
Ta 152,23,41088
Mvaa 216,128,200
Ta 154,23,46208
Ta 156,24,41120
Mvaa 220,132,200
Ta 158,24,46240
Mvaa 212,132,184
Ta 128,23,20672
W 9
Mvaa 208,136,188
Ta 130,23,25792
s_waitcnt vmcnt(4)
v_cmp_eq_u64 vcc, s[12:13], v[28:29]
s_cmp_eq_u64 vcc, -1
s_cselect_b32 s46, 79691776, 0
s_cmp_eq_u32 s14, 0
s_cselect_b32 s42, s46, 0
Mvaa 216,136,204
s_add_u32 s62, s78, 0
Ta 132,24,20704
Mvaa 220,140,204
Ta 134,24,25824
Mvaa 212,140,188
buffer_load_dwordx4 v[96:99], v1, s[44:47], s62 offen sc1
W 8
Mvaa 208,144,192
Ta 136,23,51392
Ta 138,23,56512
Mvaa 216,144,208
s_add_u32 s62, s78, 5120
Ta 140,24,51424
Mvaa 220,148,208
Ta 142,24,56544
Mvaa 212,148,192
buffer_load_dwordx4 v[100:103], v1, s[44:47], s62 offen sc1
W 8
Mvaa 208,152,196
Ta 144,25,192
Ta 146,25,5312
Mvaa 216,152,212
s_add_u32 s62, s78, 10240
Ta 148,26,224
Mvaa 220,156,212
Ta 150,26,5344
Mvaa 212,156,196
buffer_load_dwordx4 v[104:107], v1, s[44:47], s62 offen sc1
W 8
Mvaa 224,128,96
Ta 152,21,41152
Ta 154,21,46272
Mvaa 232,128,112
Ta 156,22,41184
Ta 158,22,46304
Mvaa 236,132,112
s_add_u32 s62, s78, 15360
K 208,208,209
Mvaa 228,132,96
Ta 128,21,51392
W 9
Mvaa 224,136,100
Ta 130,21,56512
buffer_load_dwordx4 v[108:111], v1, s[44:47], s62 offen sc1
Mvaa 232,136,116
K 209,210,211
K 210,212,213
Mvaa 236,140,116
Ta 132,22,51424
Ta 134,22,56544
Mvaa 228,140,100
s_add_u32 s62, s78, 20480
W 8
Mvaa 224,144,104
K 211,214,215
Ta 136,23,10432
Mvaa 232,144,120
Ta 138,23,15552
s_add_u32 s83, s52, 329728
Mvaa 236,148,120
buffer_load_dwordx4 v[112:115], v1, s[44:47], s62 offen sc1
Ta 140,24,10464
Mvaa 228,148,104
Ta 142,24,15584
W 8
Mvaa 224,152,108
s_add_u32 s62, s78, 25600
Ta 144,23,30912
Mvaa 232,152,124
buffer_store_dwordx4 v[208:211], v1, s[8:11], s83 offen sc1
K 216,216,217
Mvaa 236,156,124
K 217,218,219
K 218,220,221
Mvaa 228,156,108
K 219,222,223
W 5
Mvaa 224,128,184
s_add_u32 s83, s52, 370688
buffer_store_dwordx4 v[216:219], v1, s[8:11], s83 offen sc1
Mvaa 232,128,200
Ta 146,23,36032
Ta 148,24,30944
Mvaa 236,132,200
Ta 150,24,36064
Ta 152,23,41152
Mvaa 228,132,184
buffer_load_dwordx4 v[116:119], v1, s[44:47], s62 offen sc1
W 5
Mvaa 224,136,188
s_add_u32 s62, s78, 30720
Ta 154,23,46272
Mvaa 232,136,204
Ta 156,24,41184
Ta 158,24,46304
Mvaa 236,140,204
buffer_load_dwordx4 v[120:123], v1, s[44:47], s62 offen sc1
s_add_u32 s62, s78, 35840
Mvaa 228,140,188
Ta 128,23,20736
W 5
Mvaa 224,144,192
Ta 130,23,25856
Ta 132,24,20768
Mvaa 232,144,208
buffer_load_dwordx4 v[124:127], v1, s[44:47], s62 offen sc1
Ta 134,24,25888
Mvaa 236,148,208
Ta 136,23,51456
Ta 138,23,56576
Mvaa 228,148,192
s_add_u32 s62, s65, 0
W 6
Mvaa 224,152,196
Ta 140,24,51488
Ta 142,24,56608
Mvaa 232,152,212
buffer_load_dwordx4 v[128:131], v1, s[40:43], s62 offen sc1
Ta 144,25,256
Mvaa 236,156,212
Ta 146,25,5376
s_add_u32 s62, s65, 5120
Mvaa 228,156,196
Ta 148,26,288
W 7
Mvaa 240,128,96
Ta 150,26,5408
buffer_load_dwordx4 v[132:135], v1, s[40:43], s62 offen sc1
Mvaa 248,128,112
Ta 152,21,41216
Ta 154,21,46336
Mvaa 252,132,112
s_add_u32 s62, s65, 10240
Ta 156,22,41248
Mvaa 244,132,96
Ta 158,22,46368
W 8
Mvaa 240,136,100
buffer_load_dwordx4 v[136:139], v1, s[40:43], s62 offen sc1
K 224,224,225
Mvaa 248,136,116
Ta 128,21,51456
Ta 130,21,56576
Mvaa 252,140,116
K 225,226,227
s_add_u32 s62, s65, 15360
Mvaa 244,140,100
K 226,228,229
W 6
Mvaa 240,144,104
Ta 132,22,51488
Ta 134,22,56608
Mvaa 248,144,120
K 227,230,231
buffer_load_dwordx4 v[140:143], v1, s[40:43], s62 offen sc1
Mvaa 252,148,120
Ta 136,23,10496
Ta 138,23,15616
Mvaa 244,148,104
s_add_u32 s83, s52, 330752
W 6
Mvaa 240,152,108
s_add_u32 s62, s65, 20480
buffer_store_dwordx4 v[224:227], v1, s[8:11], s83 offen sc1
Mvaa 248,152,124
K 232,232,233
K 233,234,235
Mvaa 252,156,124
K 234,236,237
K 235,238,239
Mvaa 244,156,108
Ta 140,24,10528
Ta 142,24,15648
W 4
Mvaa 240,128,184
s_add_u32 s83, s52, 371712
buffer_store_dwordx4 v[232:235], v1, s[8:11], s83 offen sc1
Mvaa 248,128,200
Ta 144,23,30976
Ta 146,23,36096
Mvaa 252,132,200
Ta 148,24,31008
Ta 150,24,36128
Mvaa 244,132,184
buffer_load_dwordx4 v[144:147], v1, s[40:43], s62 offen sc1
W 4
Mvaa 240,136,188
s_add_u32 s62, s65, 25600
Ta 152,23,41216
Mvaa 248,136,204
Ta 154,23,46336
Ta 156,24,41248
Mvaa 252,140,204
buffer_load_dwordx4 v[148:151], v1, s[40:43], s62 offen sc1
Ta 158,24,46368
Mvaa 244,140,188
s_add_u32 s62, s65, 30720
W 4
Mvaa 240,144,192
Mvaa 248,144,208
buffer_load_dwordx4 v[152:155], v1, s[40:43], s62 offen sc1
s_add_u32 s62, s65, 35840
Mvaa 252,148,208
Mvaa 244,148,192
W 0
Mvaa 240,152,196
buffer_load_dwordx4 v[156:159], v1, s[40:43], s62 offen sc1
Mvaa 248,152,212
Mvaa 252,156,212
Mvaa 244,156,196
s_nop 3
K 240,240,241
K 241,242,243
s_nop 1
K 242,244,245
K 243,246,247
s_add_u32 s83, s52, 331776
buffer_store_dwordx4 v[240:243], v1, s[8:11], s83 offen sc1
K 248,248,249
K 249,250,251
K 250,252,253
K 251,254,255
s_add_u32 s83, s52, 372736
buffer_store_dwordx4 v[248:251], v1, s[8:11], s83 offen sc1
s_waitcnt vmcnt(2)
.Ltend_85%=:
s_cmp_lg_u32 s68, 0
s_cbranch_scc1 .Lup_115%=
.Lupr_116%=:
s_cmp_eq_u32 s46, 0
s_cbranch_scc1 .Lslow_125%=
s_cmp_eq_u32 s14, 0
s_cbranch_scc0 .Lone_127%=
Zvvv 80,4,96
Zvvv 84,8,96
Zvvv 88,4,128
Zvvv 92,8,128
Mvvv 80,4,100
Mvvv 84,8,100
Mvvv 88,4,132
Mvvv 92,8,132
Mvvv 80,4,104
Mvvv 84,8,104
Mvvv 88,4,136
Mvvv 92,8,136
Mvvv 80,4,108
Mvvv 84,8,108
Mvvv 88,4,140
Mvvv 92,8,140
Mvvv 80,4,112
Mvvv 84,8,112
Mvvv 88,4,144
Mvvv 92,8,144
Mvvv 80,4,116
Mvvv 84,8,116
Mvvv 88,4,148
Mvvv 92,8,148
Mvvv 80,4,120
Mvvv 84,8,120
Mvvv 88,4,152
Mvvv 92,8,152
Mvvv 80,4,124
Mvvv 84,8,124
Mvvv 88,4,156
Mvvv 92,8,156
s_nop 4
K 160,80,81
K 161,82,83
K 162,84,85
K 163,86,87
v_lshlrev_b32 v80, 16, v160
v_and_b32 v81, 0xffff0000, v160
v_lshlrev_b32 v82, 16, v161
v_and_b32 v83, 0xffff0000, v161
v_lshlrev_b32 v84, 16, v162
v_and_b32 v85, 0xffff0000, v162
v_lshlrev_b32 v86, 16, v163
v_and_b32 v87, 0xffff0000, v163
A 80,32,80
A 81,33,81
A 82,34,82
A 83,35,83
A 84,36,84
A 85,37,85
A 86,38,86
A 87,39,87
K 160,80,81
K 161,82,83
K 162,84,85
K 163,86,87
v_lshlrev_b32 v80, 16, v160
v_and_b32 v81, 0xffff0000, v160
v_lshlrev_b32 v82, 16, v161
v_and_b32 v83, 0xffff0000, v161
v_lshlrev_b32 v84, 16, v162
v_and_b32 v85, 0xffff0000, v162
v_lshlrev_b32 v86, 16, v163
)"
R"(v_and_b32 v87, 0xffff0000, v163
A 80,40,80
A 81,41,81
A 82,42,82
A 83,43,83
A 84,44,84
A 85,45,85
A 86,46,86
A 87,47,87
K 80,80,81
K 81,82,83
K 82,84,85
K 83,86,87
K 160,88,89
K 161,90,91
K 162,92,93
K 163,94,95
v_lshlrev_b32 v88, 16, v160
v_and_b32 v89, 0xffff0000, v160
v_lshlrev_b32 v90, 16, v161
v_and_b32 v91, 0xffff0000, v161
v_lshlrev_b32 v92, 16, v162
v_and_b32 v93, 0xffff0000, v162
v_lshlrev_b32 v94, 16, v163
v_and_b32 v95, 0xffff0000, v163
A 88,64,88
A 89,65,89
A 90,66,90
A 91,67,91
A 92,68,92
A 93,69,93
A 94,70,94
A 95,71,95
K 160,88,89
K 161,90,91
K 162,92,93
K 163,94,95
v_lshlrev_b32 v88, 16, v160
v_and_b32 v89, 0xffff0000, v160
v_lshlrev_b32 v90, 16, v161
v_and_b32 v91, 0xffff0000, v161
v_lshlrev_b32 v92, 16, v162
v_and_b32 v93, 0xffff0000, v162
v_lshlrev_b32 v94, 16, v163
v_and_b32 v95, 0xffff0000, v163
A 88,72,88
A 89,73,89
A 90,74,90
A 91,75,91
A 92,76,92
A 93,77,93
A 94,78,94
A 95,79,95
K 88,88,89
K 89,90,91
K 90,92,93
K 91,94,95
s_waitcnt vmcnt(0)
s_cmp_eq_u32 s64, 0
s_cbranch_scc0 .LL_129%=
v_lshlrev_b32 v2, 2, v1
v_mov_b64 v[28:29], s[12:13]
s_mov_b64 exec, 3
buffer_store_dwordx2 v[28:29], v2, s[8:11], s53 offen offset:512 sc1
s_mov_b64 exec, -1
.LL_129%=:
buffer_load_dwordx2 v[28:29], v27, s[8:11], s63 offen offset:512 sc1
buffer_store_dwordx4 v[80:83], v20, s[20:23], s72 offen
buffer_store_dwordx4 v[88:91], v20, s[20:23], s74 offen
s_waitcnt vmcnt(2)
s_branch .Ljoin_128%=
.Lone_127%=:
Zvvv 80,4,96
Zvvv 84,8,96
Mvvv 80,4,100
Mvvv 84,8,100
Mvvv 80,4,104
Mvvv 84,8,104
Mvvv 80,4,108
Mvvv 84,8,108
Mvvv 80,4,112
Mvvv 84,8,112
Mvvv 80,4,116
Mvvv 84,8,116
Mvvv 80,4,120
Mvvv 84,8,120
Mvvv 80,4,124
Mvvv 84,8,124
s_nop 6
K 160,80,81
K 161,82,83
K 162,84,85
K 163,86,87
v_lshlrev_b32 v80, 16, v160
v_and_b32 v81, 0xffff0000, v160
v_lshlrev_b32 v82, 16, v161
v_and_b32 v83, 0xffff0000, v161
v_lshlrev_b32 v84, 16, v162
v_and_b32 v85, 0xffff0000, v162
v_lshlrev_b32 v86, 16, v163
v_and_b32 v87, 0xffff0000, v163
A 80,32,80
A 81,33,81
A 82,34,82
A 83,35,83
A 84,36,84
A 85,37,85
A 86,38,86
A 87,39,87
K 160,80,81
K 161,82,83
K 162,84,85
K 163,86,87
v_lshlrev_b32 v80, 16, v160
v_and_b32 v81, 0xffff0000, v160
v_lshlrev_b32 v82, 16, v161
v_and_b32 v83, 0xffff0000, v161
v_lshlrev_b32 v84, 16, v162
v_and_b32 v85, 0xffff0000, v162
v_lshlrev_b32 v86, 16, v163
v_and_b32 v87, 0xffff0000, v163
A 80,40,80
A 81,41,81
A 82,42,82
A 83,43,83
A 84,44,84
A 85,45,85
A 86,46,86
A 87,47,87
K 80,80,81
K 81,82,83
K 82,84,85
K 83,86,87
s_waitcnt vmcnt(0)
s_cmp_eq_u32 s64, 0
s_cbranch_scc0 .LL_130%=
v_lshlrev_b32 v2, 2, v1
v_mov_b64 v[28:29], s[12:13]
s_mov_b64 exec, 3
buffer_store_dwordx2 v[28:29], v2, s[8:11], s53 offen offset:512 sc1
s_mov_b64 exec, -1
.LL_130%=:
buffer_load_dwordx2 v[28:29], v27, s[8:11], s63 offen offset:512 sc1
buffer_store_dwordx4 v[80:83], v20, s[20:23], s72 offen
s_waitcnt vmcnt(1)
.Ljoin_128%=:
.LL_126%=:
v_cmp_eq_u64 vcc, s[12:13], v[28:29]
s_cmp_eq_u64 vcc, -1
s_cbranch_scc0 .Lspin_131%=
.Lok_132%=:
s_cmp_eq_u32 s14, 1
s_cbranch_scc0 .Lone_134%=
s_add_u32 s62, s79, 0
buffer_load_dwordx4 v[96:99], v1, s[8:11], s62 offen sc1
s_add_u32 s62, s65, 0
buffer_load_dwordx4 v[128:131], v1, s[8:11], s62 offen sc1
s_add_u32 s62, s79, 5120
buffer_load_dwordx4 v[100:103], v1, s[8:11], s62 offen sc1
s_add_u32 s62, s65, 5120
buffer_load_dwordx4 v[132:135], v1, s[8:11], s62 offen sc1
s_add_u32 s62, s79, 10240
buffer_load_dwordx4 v[104:107], v1, s[8:11], s62 offen sc1
s_add_u32 s62, s65, 10240
buffer_load_dwordx4 v[136:139], v1, s[8:11], s62 offen sc1
s_add_u32 s62, s79, 15360
buffer_load_dwordx4 v[108:111], v1, s[8:11], s62 offen sc1
s_add_u32 s62, s65, 15360
buffer_load_dwordx4 v[140:143], v1, s[8:11], s62 offen sc1
s_add_u32 s62, s79, 20480
buffer_load_dwordx4 v[112:115], v1, s[8:11], s62 offen sc1
s_add_u32 s62, s65, 20480
buffer_load_dwordx4 v[144:147], v1, s[8:11], s62 offen sc1
s_add_u32 s62, s79, 25600
buffer_load_dwordx4 v[116:119], v1, s[8:11], s62 offen sc1
s_add_u32 s62, s65, 25600
buffer_load_dwordx4 v[148:151], v1, s[8:11], s62 offen sc1
s_add_u32 s62, s79, 30720
buffer_load_dwordx4 v[120:123], v1, s[8:11], s62 offen sc1
s_add_u32 s62, s65, 30720
buffer_load_dwordx4 v[152:155], v1, s[8:11], s62 offen sc1
s_add_u32 s62, s79, 35840
buffer_load_dwordx4 v[124:127], v1, s[8:11], s62 offen sc1
s_add_u32 s62, s65, 35840
buffer_load_dwordx4 v[156:159], v1, s[8:11], s62 offen sc1
s_waitcnt vmcnt(14)
Zvvv 80,4,96
Zvvv 84,8,96
Zvvv 88,4,128
Zvvv 92,8,128
s_waitcnt vmcnt(12)
Mvvv 80,4,100
Mvvv 84,8,100
Mvvv 88,4,132
Mvvv 92,8,132
s_waitcnt vmcnt(10)
Mvvv 80,4,104
Mvvv 84,8,104
Mvvv 88,4,136
Mvvv 92,8,136
s_waitcnt vmcnt(8)
Mvvv 80,4,108
Mvvv 84,8,108
Mvvv 88,4,140
Mvvv 92,8,140
s_waitcnt vmcnt(6)
Mvvv 80,4,112
Mvvv 84,8,112
Mvvv 88,4,144
Mvvv 92,8,144
s_waitcnt vmcnt(4)
Mvvv 80,4,116
Mvvv 84,8,116
Mvvv 88,4,148
Mvvv 92,8,148
s_waitcnt vmcnt(2)
Mvvv 80,4,120
Mvvv 84,8,120
Mvvv 88,4,152
Mvvv 92,8,152
s_waitcnt vmcnt(0)
Mvvv 80,4,124
Mvvv 84,8,124
Mvvv 88,4,156
Mvvv 92,8,156
s_nop 4
K 160,80,81
K 161,82,83
K 162,84,85
K 163,86,87
v_lshlrev_b32 v80, 16, v160
v_and_b32 v81, 0xffff0000, v160
v_lshlrev_b32 v82, 16, v161
v_and_b32 v83, 0xffff0000, v161
v_lshlrev_b32 v84, 16, v162
v_and_b32 v85, 0xffff0000, v162
v_lshlrev_b32 v86, 16, v163
v_and_b32 v87, 0xffff0000, v163
A 80,48,80
A 81,49,81
A 82,50,82
A 83,51,83
A 84,52,84
A 85,53,85
A 86,54,86
A 87,55,87
K 160,80,81
K 161,82,83
K 162,84,85
K 163,86,87
v_lshlrev_b32 v80, 16, v160
v_and_b32 v81, 0xffff0000, v160
v_lshlrev_b32 v82, 16, v161
v_and_b32 v83, 0xffff0000, v161
v_lshlrev_b32 v84, 16, v162
v_and_b32 v85, 0xffff0000, v162
v_lshlrev_b32 v86, 16, v163
v_and_b32 v87, 0xffff0000, v163
A 80,56,80
A 81,57,81
A 82,58,82
A 83,59,83
A 84,60,84
A 85,61,85
A 86,62,86
A 87,63,87
K 80,80,81
K 81,82,83
K 82,84,85
K 83,86,87
K 160,88,89
K 161,90,91
K 162,92,93
K 163,94,95
v_lshlrev_b32 v88, 16, v160
v_and_b32 v89, 0xffff0000, v160
v_lshlrev_b32 v90, 16, v161
v_and_b32 v91, 0xffff0000, v161
v_lshlrev_b32 v92, 16, v162
v_and_b32 v93, 0xffff0000, v162
v_lshlrev_b32 v94, 16, v163
v_and_b32 v95, 0xffff0000, v163
A 88,64,88
A 89,65,89
A 90,66,90
A 91,67,91
A 92,68,92
A 93,69,93
A 94,70,94
A 95,71,95
K 160,88,89
K 161,90,91
K 162,92,93
K 163,94,95
v_lshlrev_b32 v88, 16, v160
v_and_b32 v89, 0xffff0000, v160
v_lshlrev_b32 v90, 16, v161
v_and_b32 v91, 0xffff0000, v161
v_lshlrev_b32 v92, 16, v162
v_and_b32 v93, 0xffff0000, v162
v_lshlrev_b32 v94, 16, v163
v_and_b32 v95, 0xffff0000, v163
A 88,72,88
A 89,73,89
A 90,74,90
A 91,75,91
A 92,76,92
A 93,77,93
A 94,78,94
A 95,79,95
K 88,88,89
K 89,90,91
K 90,92,93
K 91,94,95
buffer_store_dwordx4 v[80:83], v20, s[20:23], s73 offen
buffer_store_dwordx4 v[88:91], v20, s[20:23], s74 offen
s_branch .Ljoin_135%=
.Lone_134%=:
s_add_u32 s62, s79, 0
buffer_load_dwordx4 v[96:99], v1, s[8:11], s62 offen sc1
s_add_u32 s62, s79, 5120
buffer_load_dwordx4 v[100:103], v1, s[8:11], s62 offen sc1
s_add_u32 s62, s79, 10240
buffer_load_dwordx4 v[104:107], v1, s[8:11], s62 offen sc1
s_add_u32 s62, s79, 15360
buffer_load_dwordx4 v[108:111], v1, s[8:11], s62 offen sc1
s_add_u32 s62, s79, 20480
buffer_load_dwordx4 v[112:115], v1, s[8:11], s62 offen sc1
s_add_u32 s62, s79, 25600
buffer_load_dwordx4 v[116:119], v1, s[8:11], s62 offen sc1
s_add_u32 s62, s79, 30720
buffer_load_dwordx4 v[120:123], v1, s[8:11], s62 offen sc1
s_add_u32 s62, s79, 35840
buffer_load_dwordx4 v[124:127], v1, s[8:11], s62 offen sc1
s_waitcnt vmcnt(7)
Zvvv 80,4,96
Zvvv 84,8,96
s_waitcnt vmcnt(6)
Mvvv 80,4,100
Mvvv 84,8,100
s_waitcnt vmcnt(5)
Mvvv 80,4,104
Mvvv 84,8,104
s_waitcnt vmcnt(4)
Mvvv 80,4,108
Mvvv 84,8,108
s_waitcnt vmcnt(3)
Mvvv 80,4,112
Mvvv 84,8,112
s_waitcnt vmcnt(2)
Mvvv 80,4,116
Mvvv 84,8,116
s_waitcnt vmcnt(1)
Mvvv 80,4,120
Mvvv 84,8,120
s_waitcnt vmcnt(0)
Mvvv 80,4,124
Mvvv 84,8,124
s_nop 6
K 160,80,81
K 161,82,83
K 162,84,85
K 163,86,87
v_lshlrev_b32 v80, 16, v160
v_and_b32 v81, 0xffff0000, v160
v_lshlrev_b32 v82, 16, v161
v_and_b32 v83, 0xffff0000, v161
v_lshlrev_b32 v84, 16, v162
v_and_b32 v85, 0xffff0000, v162
v_lshlrev_b32 v86, 16, v163
v_and_b32 v87, 0xffff0000, v163
A 80,48,80
A 81,49,81
A 82,50,82
A 83,51,83
A 84,52,84
A 85,53,85
A 86,54,86
A 87,55,87
K 160,80,81
K 161,82,83
K 162,84,85
K 163,86,87
v_lshlrev_b32 v80, 16, v160
v_and_b32 v81, 0xffff0000, v160
v_lshlrev_b32 v82, 16, v161
v_and_b32 v83, 0xffff0000, v161
v_lshlrev_b32 v84, 16, v162
v_and_b32 v85, 0xffff0000, v162
v_lshlrev_b32 v86, 16, v163
v_and_b32 v87, 0xffff0000, v163
A 80,56,80
A 81,57,81
A 82,58,82
A 83,59,83
A 84,60,84
A 85,61,85
A 86,62,86
A 87,63,87
K 80,80,81
)"
R"(K 81,82,83
K 82,84,85
K 83,86,87
buffer_store_dwordx4 v[80:83], v20, s[20:23], s73 offen
.Ljoin_135%=:
s_lshl_b32 s80, s15, 2
s_add_u32 s80, s80, s14
s_cmp_eq_u32 s80, 4
s_cbranch_scc0 .LL_136%=
v_mov_b64 v[28:29], s[12:13]
s_mov_b64 exec, 1
buffer_store_dwordx2 v[28:29], off, s[8:11], 0 sc1
s_mov_b64 exec, -1
.LL_136%=:
s_branch .Lend_143%=
.Lslow_125%=:
s_waitcnt vmcnt(0)
s_cmp_eq_u32 s64, 0
s_cbranch_scc0 .LL_137%=
v_lshlrev_b32 v2, 2, v1
v_mov_b64 v[28:29], s[12:13]
s_mov_b64 exec, 3
buffer_store_dwordx2 v[28:29], v2, s[8:11], s53 offen offset:512 sc1
s_mov_b64 exec, -1
.LL_137%=:
buffer_load_dwordx2 v[28:29], v27, s[8:11], s63 offen offset:0 sc1
s_waitcnt vmcnt(0)
v_cmp_eq_u64 vcc, s[12:13], v[28:29]
s_cmp_eq_u64 vcc, -1
s_cbranch_scc0 .Lspin_138%=
.Lok_139%=:
s_cmp_eq_u32 s14, 0
s_cbranch_scc0 .Lone_141%=
s_add_u32 s62, s78, 0
buffer_load_dwordx4 v[96:99], v1, s[8:11], s62 offen sc1
s_add_u32 s62, s65, 0
buffer_load_dwordx4 v[128:131], v1, s[8:11], s62 offen sc1
s_add_u32 s62, s78, 5120
buffer_load_dwordx4 v[100:103], v1, s[8:11], s62 offen sc1
s_add_u32 s62, s65, 5120
buffer_load_dwordx4 v[132:135], v1, s[8:11], s62 offen sc1
s_add_u32 s62, s78, 10240
buffer_load_dwordx4 v[104:107], v1, s[8:11], s62 offen sc1
s_add_u32 s62, s65, 10240
buffer_load_dwordx4 v[136:139], v1, s[8:11], s62 offen sc1
s_add_u32 s62, s78, 15360
buffer_load_dwordx4 v[108:111], v1, s[8:11], s62 offen sc1
s_add_u32 s62, s65, 15360
buffer_load_dwordx4 v[140:143], v1, s[8:11], s62 offen sc1
s_add_u32 s62, s78, 20480
buffer_load_dwordx4 v[112:115], v1, s[8:11], s62 offen sc1
s_add_u32 s62, s65, 20480
buffer_load_dwordx4 v[144:147], v1, s[8:11], s62 offen sc1
s_add_u32 s62, s78, 25600
buffer_load_dwordx4 v[116:119], v1, s[8:11], s62 offen sc1
s_add_u32 s62, s65, 25600
buffer_load_dwordx4 v[148:151], v1, s[8:11], s62 offen sc1
s_add_u32 s62, s78, 30720
buffer_load_dwordx4 v[120:123], v1, s[8:11], s62 offen sc1
s_add_u32 s62, s65, 30720
buffer_load_dwordx4 v[152:155], v1, s[8:11], s62 offen sc1
s_add_u32 s62, s78, 35840
buffer_load_dwordx4 v[124:127], v1, s[8:11], s62 offen sc1
s_add_u32 s62, s65, 35840
buffer_load_dwordx4 v[156:159], v1, s[8:11], s62 offen sc1
s_waitcnt vmcnt(14)
Zvvv 80,4,96
Zvvv 84,8,96
Zvvv 88,4,128
Zvvv 92,8,128
s_waitcnt vmcnt(12)
Mvvv 80,4,100
Mvvv 84,8,100
Mvvv 88,4,132
Mvvv 92,8,132
s_waitcnt vmcnt(10)
Mvvv 80,4,104
Mvvv 84,8,104
Mvvv 88,4,136
Mvvv 92,8,136
s_waitcnt vmcnt(8)
Mvvv 80,4,108
Mvvv 84,8,108
Mvvv 88,4,140
Mvvv 92,8,140
s_waitcnt vmcnt(6)
Mvvv 80,4,112
Mvvv 84,8,112
Mvvv 88,4,144
Mvvv 92,8,144
s_waitcnt vmcnt(4)
Mvvv 80,4,116
Mvvv 84,8,116
Mvvv 88,4,148
Mvvv 92,8,148
s_waitcnt vmcnt(2)
Mvvv 80,4,120
Mvvv 84,8,120
Mvvv 88,4,152
Mvvv 92,8,152
s_waitcnt vmcnt(0)
Mvvv 80,4,124
Mvvv 84,8,124
Mvvv 88,4,156
Mvvv 92,8,156
s_nop 4
K 160,80,81
K 161,82,83
K 162,84,85
K 163,86,87
v_lshlrev_b32 v80, 16, v160
v_and_b32 v81, 0xffff0000, v160
v_lshlrev_b32 v82, 16, v161
v_and_b32 v83, 0xffff0000, v161
v_lshlrev_b32 v84, 16, v162
v_and_b32 v85, 0xffff0000, v162
v_lshlrev_b32 v86, 16, v163
v_and_b32 v87, 0xffff0000, v163
A 80,32,80
A 81,33,81
A 82,34,82
A 83,35,83
A 84,36,84
A 85,37,85
A 86,38,86
A 87,39,87
K 160,80,81
K 161,82,83
K 162,84,85
K 163,86,87
v_lshlrev_b32 v80, 16, v160
v_and_b32 v81, 0xffff0000, v160
v_lshlrev_b32 v82, 16, v161
v_and_b32 v83, 0xffff0000, v161
v_lshlrev_b32 v84, 16, v162
v_and_b32 v85, 0xffff0000, v162
v_lshlrev_b32 v86, 16, v163
v_and_b32 v87, 0xffff0000, v163
A 80,40,80
A 81,41,81
A 82,42,82
A 83,43,83
A 84,44,84
A 85,45,85
A 86,46,86
A 87,47,87
K 80,80,81
K 81,82,83
K 82,84,85
K 83,86,87
K 160,88,89
K 161,90,91
K 162,92,93
K 163,94,95
v_lshlrev_b32 v88, 16, v160
v_and_b32 v89, 0xffff0000, v160
v_lshlrev_b32 v90, 16, v161
v_and_b32 v91, 0xffff0000, v161
v_lshlrev_b32 v92, 16, v162
v_and_b32 v93, 0xffff0000, v162
v_lshlrev_b32 v94, 16, v163
v_and_b32 v95, 0xffff0000, v163
A 88,64,88
A 89,65,89
A 90,66,90
A 91,67,91
A 92,68,92
A 93,69,93
A 94,70,94
A 95,71,95
K 160,88,89
K 161,90,91
K 162,92,93
K 163,94,95
v_lshlrev_b32 v88, 16, v160
v_and_b32 v89, 0xffff0000, v160
v_lshlrev_b32 v90, 16, v161
v_and_b32 v91, 0xffff0000, v161
v_lshlrev_b32 v92, 16, v162
v_and_b32 v93, 0xffff0000, v162
v_lshlrev_b32 v94, 16, v163
v_and_b32 v95, 0xffff0000, v163
A 88,72,88
A 89,73,89
A 90,74,90
A 91,75,91
A 92,76,92
A 93,77,93
A 94,78,94
A 95,79,95
K 88,88,89
K 89,90,91
K 90,92,93
K 91,94,95
buffer_store_dwordx4 v[80:83], v20, s[20:23], s72 offen
buffer_store_dwordx4 v[88:91], v20, s[20:23], s74 offen
s_branch .Ljoin_142%=
.Lone_141%=:
s_add_u32 s62, s78, 0
buffer_load_dwordx4 v[96:99], v1, s[8:11], s62 offen sc1
s_add_u32 s62, s78, 5120
buffer_load_dwordx4 v[100:103], v1, s[8:11], s62 offen sc1
s_add_u32 s62, s78, 10240
buffer_load_dwordx4 v[104:107], v1, s[8:11], s62 offen sc1
s_add_u32 s62, s78, 15360
buffer_load_dwordx4 v[108:111], v1, s[8:11], s62 offen sc1
s_add_u32 s62, s78, 20480
buffer_load_dwordx4 v[112:115], v1, s[8:11], s62 offen sc1
s_add_u32 s62, s78, 25600
buffer_load_dwordx4 v[116:119], v1, s[8:11], s62 offen sc1
s_add_u32 s62, s78, 30720
buffer_load_dwordx4 v[120:123], v1, s[8:11], s62 offen sc1
s_add_u32 s62, s78, 35840
buffer_load_dwordx4 v[124:127], v1, s[8:11], s62 offen sc1
s_waitcnt vmcnt(7)
Zvvv 80,4,96
Zvvv 84,8,96
s_waitcnt vmcnt(6)
Mvvv 80,4,100
Mvvv 84,8,100
s_waitcnt vmcnt(5)
Mvvv 80,4,104
Mvvv 84,8,104
s_waitcnt vmcnt(4)
Mvvv 80,4,108
Mvvv 84,8,108
s_waitcnt vmcnt(3)
Mvvv 80,4,112
Mvvv 84,8,112
s_waitcnt vmcnt(2)
Mvvv 80,4,116
Mvvv 84,8,116
s_waitcnt vmcnt(1)
Mvvv 80,4,120
Mvvv 84,8,120
s_waitcnt vmcnt(0)
Mvvv 80,4,124
Mvvv 84,8,124
s_nop 6
K 160,80,81
K 161,82,83
K 162,84,85
K 163,86,87
v_lshlrev_b32 v80, 16, v160
v_and_b32 v81, 0xffff0000, v160
v_lshlrev_b32 v82, 16, v161
v_and_b32 v83, 0xffff0000, v161
v_lshlrev_b32 v84, 16, v162
v_and_b32 v85, 0xffff0000, v162
v_lshlrev_b32 v86, 16, v163
v_and_b32 v87, 0xffff0000, v163
A 80,32,80
A 81,33,81
A 82,34,82
A 83,35,83
A 84,36,84
A 85,37,85
A 86,38,86
A 87,39,87
K 160,80,81
K 161,82,83
K 162,84,85
K 163,86,87
v_lshlrev_b32 v80, 16, v160
v_and_b32 v81, 0xffff0000, v160
v_lshlrev_b32 v82, 16, v161
v_and_b32 v83, 0xffff0000, v161
v_lshlrev_b32 v84, 16, v162
v_and_b32 v85, 0xffff0000, v162
v_lshlrev_b32 v86, 16, v163
v_and_b32 v87, 0xffff0000, v163
A 80,40,80
A 81,41,81
A 82,42,82
A 83,43,83
A 84,44,84
A 85,45,85
A 86,46,86
A 87,47,87
K 80,80,81
K 81,82,83
K 82,84,85
K 83,86,87
buffer_store_dwordx4 v[80:83], v20, s[20:23], s72 offen
.Ljoin_142%=:
buffer_load_dwordx2 v[28:29], v27, s[8:11], s63 offen offset:512 sc1
s_waitcnt vmcnt(0)
s_branch .LL_126%=
.Lup_115%=:
v_and_b32 v3, 0xffff0000, v3
v_ashrrev_i32 v2, 31, v3
v_not_b32 v2, v2
v_or_b32 v2, 0x80000000, v2
v_xor_b32 v3, v3, v2
v_and_b32 v3, 0xffff0000, v3
v_mul_f32 v3, 0xbfb8aa3b, v3
X 3
s_nop 0
v_add_f32 v3, 1.0, v3
v_rcp_f32 v3, v3
s_nop 0
K 3,3,3
v_lshlrev_b32 v3, 16, v3
s_lshr_b32 s80, s70, 4
s_and_b32 s70, s70, 15
v_bfe_u32 v2, v1, 4, 4
v_cmp_eq_u32 vcc, s70, v2
s_cmp_lg_u32 s80, s61
s_cselect_b32 s80, 1, 0
G 176,0
G 177,1
G 178,2
G 179,3
G 180,4
G 181,5
G 182,6
G 183,7
G 184,8
G 185,9
G 186,10
G 187,11
G 188,12
G 189,13
G 190,14
G 191,15
G 192,16
G 193,17
G 194,18
G 195,19
G 196,20
G 197,21
G 198,22
G 199,23
G 200,24
G 201,25
G 202,26
G 203,27
G 204,28
G 205,29
G 206,30
G 207,31
v_pk_add_f32 v[176:177], v[176:177], v[184:185]
v_pk_add_f32 v[192:193], v[192:193], v[200:201]
v_pk_add_f32 v[178:179], v[178:179], v[186:187]
v_pk_add_f32 v[194:195], v[194:195], v[202:203]
v_pk_add_f32 v[180:181], v[180:181], v[188:189]
v_pk_add_f32 v[196:197], v[196:197], v[204:205]
v_pk_add_f32 v[182:183], v[182:183], v[190:191]
v_pk_add_f32 v[198:199], v[198:199], v[206:207]
v_pk_add_f32 v[176:177], v[176:177], v[192:193]
v_pk_add_f32 v[178:179], v[178:179], v[194:195]
v_pk_add_f32 v[180:181], v[180:181], v[196:197]
v_pk_add_f32 v[182:183], v[182:183], v[198:199]
v_add_f32_dpp v176, v176, v176 quad_perm:[1,0,3,2] row_mask:0xf bank_mask:0xf
v_add_f32_dpp v177, v177, v177 quad_perm:[1,0,3,2] row_mask:0xf bank_mask:0xf
v_add_f32_dpp v178, v178, v178 quad_perm:[1,0,3,2] row_mask:0xf bank_mask:0xf
v_add_f32_dpp v179, v179, v179 quad_perm:[1,0,3,2] row_mask:0xf bank_mask:0xf
v_add_f32_dpp v180, v180, v180 quad_perm:[1,0,3,2] row_mask:0xf bank_mask:0xf
v_add_f32_dpp v181, v181, v181 quad_perm:[1,0,3,2] row_mask:0xf bank_mask:0xf
v_add_f32_dpp v182, v182, v182 quad_perm:[1,0,3,2] row_mask:0xf bank_mask:0xf
v_add_f32_dpp v183, v183, v183 quad_perm:[1,0,3,2] row_mask:0xf bank_mask:0xf
v_add_f32_dpp v176, v176, v176 quad_perm:[2,3,0,1] row_mask:0xf bank_mask:0xf
v_add_f32_dpp v177, v177, v177 quad_perm:[2,3,0,1] row_mask:0xf bank_mask:0xf
v_add_f32_dpp v178, v178, v178 quad_perm:[2,3,0,1] row_mask:0xf bank_mask:0xf
v_add_f32_dpp v179, v179, v179 quad_perm:[2,3,0,1] row_mask:0xf bank_mask:0xf
v_add_f32_dpp v180, v180, v180 quad_perm:[2,3,0,1] row_mask:0xf bank_mask:0xf
v_add_f32_dpp v181, v181, v181 quad_perm:[2,3,0,1] row_mask:0xf bank_mask:0xf
v_add_f32_dpp v182, v182, v182 quad_perm:[2,3,0,1] row_mask:0xf bank_mask:0xf
v_add_f32_dpp v183, v183, v183 quad_perm:[2,3,0,1] row_mask:0xf bank_mask:0xf
v_add_f32_dpp v176, v176, v176 row_half_mirror row_mask:0xf bank_mask:0xf
v_add_f32_dpp v177, v177, v177 row_half_mirror row_mask:0xf bank_mask:0xf
v_add_f32_dpp v178, v178, v178 row_half_mirror row_mask:0xf bank_mask:0xf
v_add_f32_dpp v179, v179, v179 row_half_mirror row_mask:0xf bank_mask:0xf
v_add_f32_dpp v180, v180, v180 row_half_mirror row_mask:0xf bank_mask:0xf
v_add_f32_dpp v181, v181, v181 row_half_mirror row_mask:0xf bank_mask:0xf
v_add_f32_dpp v182, v182, v182 row_half_mirror row_mask:0xf bank_mask:0xf
v_add_f32_dpp v183, v183, v183 row_half_mirror row_mask:0xf bank_mask:0xf
v_add_f32_dpp v176, v176, v176 row_mirror row_mask:0xf bank_mask:0xf
v_add_f32_dpp v177, v177, v177 row_mirror row_mask:0xf bank_mask:0xf
v_add_f32_dpp v178, v178, v178 row_mirror row_mask:0xf bank_mask:0xf
v_add_f32_dpp v179, v179, v179 row_mirror row_mask:0xf bank_mask:0xf
v_add_f32_dpp v180, v180, v180 row_mirror row_mask:0xf bank_mask:0xf
v_add_f32_dpp v181, v181, v181 row_mirror row_mask:0xf bank_mask:0xf
v_add_f32_dpp v182, v182, v182 row_mirror row_mask:0xf bank_mask:0xf
v_add_f32_dpp v183, v183, v183 row_mirror row_mask:0xf bank_mask:0xf
K 184,176,177
K 185,178,179
K 186,180,181
K 187,182,183
v_lshlrev_b32 v176, 16, v184
v_and_b32 v177, 0xffff0000, v184
v_lshlrev_b32 v178, 16, v185
v_and_b32 v179, 0xffff0000, v185
v_lshlrev_b32 v180, 16, v186
v_and_b32 v181, 0xffff0000, v186
v_lshlrev_b32 v182, 16, v187
v_and_b32 v183, 0xffff0000, v187
U 176,176,3
U 177,177,3
U 178,178,3
)"
R"(U 179,179,3
U 180,180,3
U 181,181,3
U 182,182,3
U 183,183,3
K 184,176,177
K 185,178,179
K 186,180,181
K 187,182,183
v_lshlrev_b32 v176, 16, v184
v_and_b32 v177, 0xffff0000, v184
v_lshlrev_b32 v178, 16, v185
v_and_b32 v179, 0xffff0000, v185
v_lshlrev_b32 v180, 16, v186
v_and_b32 v181, 0xffff0000, v186
v_lshlrev_b32 v182, 16, v187
v_and_b32 v183, 0xffff0000, v187
s_cmp_eq_u32 s80, 0
s_cbranch_scc0 .LL_118%=
A 176,176,40
A 177,177,41
A 178,178,42
A 179,179,43
A 180,180,44
A 181,181,45
A 182,182,46
A 183,183,47
K 184,176,177
K 185,178,179
K 186,180,181
K 187,182,183
v_lshlrev_b32 v176, 16, v184
v_and_b32 v177, 0xffff0000, v184
v_lshlrev_b32 v178, 16, v185
v_and_b32 v179, 0xffff0000, v185
v_lshlrev_b32 v180, 16, v186
v_and_b32 v181, 0xffff0000, v186
v_lshlrev_b32 v182, 16, v187
v_and_b32 v183, 0xffff0000, v187
v_cndmask_b32 v40, v40, v176, vcc
v_cndmask_b32 v41, v41, v177, vcc
v_cndmask_b32 v42, v42, v178, vcc
v_cndmask_b32 v43, v43, v179, vcc
v_cndmask_b32 v44, v44, v180, vcc
v_cndmask_b32 v45, v45, v181, vcc
v_cndmask_b32 v46, v46, v182, vcc
v_cndmask_b32 v47, v47, v183, vcc
s_branch .LL_117%=
.LL_118%=:
A 176,176,56
A 177,177,57
A 178,178,58
A 179,179,59
A 180,180,60
A 181,181,61
A 182,182,62
A 183,183,63
K 184,176,177
K 185,178,179
K 186,180,181
K 187,182,183
v_lshlrev_b32 v176, 16, v184
v_and_b32 v177, 0xffff0000, v184
v_lshlrev_b32 v178, 16, v185
v_and_b32 v179, 0xffff0000, v185
v_lshlrev_b32 v180, 16, v186
v_and_b32 v181, 0xffff0000, v186
v_lshlrev_b32 v182, 16, v187
v_and_b32 v183, 0xffff0000, v187
v_cndmask_b32 v56, v56, v176, vcc
v_cndmask_b32 v57, v57, v177, vcc
v_cndmask_b32 v58, v58, v178, vcc
v_cndmask_b32 v59, v59, v179, vcc
v_cndmask_b32 v60, v60, v180, vcc
v_cndmask_b32 v61, v61, v181, vcc
v_cndmask_b32 v62, v62, v182, vcc
v_cndmask_b32 v63, v63, v183, vcc
.LL_117%=:
s_cmp_eq_u32 s80, s14
s_cbranch_scc0 .LL_120%=
G 176,32
G 177,33
G 178,34
G 179,35
G 180,36
G 181,37
G 182,38
G 183,39
G 184,40
G 185,41
G 186,42
G 187,43
G 188,44
G 189,45
G 190,46
G 191,47
G 192,48
G 193,49
G 194,50
G 195,51
G 196,52
G 197,53
G 198,54
G 199,55
G 200,56
G 201,57
G 202,58
G 203,59
G 204,60
G 205,61
G 206,62
G 207,63
v_pk_add_f32 v[176:177], v[176:177], v[184:185]
v_pk_add_f32 v[192:193], v[192:193], v[200:201]
v_pk_add_f32 v[178:179], v[178:179], v[186:187]
v_pk_add_f32 v[194:195], v[194:195], v[202:203]
v_pk_add_f32 v[180:181], v[180:181], v[188:189]
v_pk_add_f32 v[196:197], v[196:197], v[204:205]
v_pk_add_f32 v[182:183], v[182:183], v[190:191]
v_pk_add_f32 v[198:199], v[198:199], v[206:207]
v_pk_add_f32 v[176:177], v[176:177], v[192:193]
v_pk_add_f32 v[178:179], v[178:179], v[194:195]
v_pk_add_f32 v[180:181], v[180:181], v[196:197]
v_pk_add_f32 v[182:183], v[182:183], v[198:199]
v_add_f32_dpp v176, v176, v176 quad_perm:[1,0,3,2] row_mask:0xf bank_mask:0xf
v_add_f32_dpp v177, v177, v177 quad_perm:[1,0,3,2] row_mask:0xf bank_mask:0xf
v_add_f32_dpp v178, v178, v178 quad_perm:[1,0,3,2] row_mask:0xf bank_mask:0xf
v_add_f32_dpp v179, v179, v179 quad_perm:[1,0,3,2] row_mask:0xf bank_mask:0xf
v_add_f32_dpp v180, v180, v180 quad_perm:[1,0,3,2] row_mask:0xf bank_mask:0xf
v_add_f32_dpp v181, v181, v181 quad_perm:[1,0,3,2] row_mask:0xf bank_mask:0xf
v_add_f32_dpp v182, v182, v182 quad_perm:[1,0,3,2] row_mask:0xf bank_mask:0xf
v_add_f32_dpp v183, v183, v183 quad_perm:[1,0,3,2] row_mask:0xf bank_mask:0xf
v_add_f32_dpp v176, v176, v176 quad_perm:[2,3,0,1] row_mask:0xf bank_mask:0xf
v_add_f32_dpp v177, v177, v177 quad_perm:[2,3,0,1] row_mask:0xf bank_mask:0xf
v_add_f32_dpp v178, v178, v178 quad_perm:[2,3,0,1] row_mask:0xf bank_mask:0xf
v_add_f32_dpp v179, v179, v179 quad_perm:[2,3,0,1] row_mask:0xf bank_mask:0xf
v_add_f32_dpp v180, v180, v180 quad_perm:[2,3,0,1] row_mask:0xf bank_mask:0xf
v_add_f32_dpp v181, v181, v181 quad_perm:[2,3,0,1] row_mask:0xf bank_mask:0xf
v_add_f32_dpp v182, v182, v182 quad_perm:[2,3,0,1] row_mask:0xf bank_mask:0xf
v_add_f32_dpp v183, v183, v183 quad_perm:[2,3,0,1] row_mask:0xf bank_mask:0xf
v_add_f32_dpp v176, v176, v176 row_half_mirror row_mask:0xf bank_mask:0xf
v_add_f32_dpp v177, v177, v177 row_half_mirror row_mask:0xf bank_mask:0xf
v_add_f32_dpp v178, v178, v178 row_half_mirror row_mask:0xf bank_mask:0xf
v_add_f32_dpp v179, v179, v179 row_half_mirror row_mask:0xf bank_mask:0xf
v_add_f32_dpp v180, v180, v180 row_half_mirror row_mask:0xf bank_mask:0xf
v_add_f32_dpp v181, v181, v181 row_half_mirror row_mask:0xf bank_mask:0xf
v_add_f32_dpp v182, v182, v182 row_half_mirror row_mask:0xf bank_mask:0xf
v_add_f32_dpp v183, v183, v183 row_half_mirror row_mask:0xf bank_mask:0xf
v_add_f32_dpp v176, v176, v176 row_mirror row_mask:0xf bank_mask:0xf
v_add_f32_dpp v177, v177, v177 row_mirror row_mask:0xf bank_mask:0xf
v_add_f32_dpp v178, v178, v178 row_mirror row_mask:0xf bank_mask:0xf
v_add_f32_dpp v179, v179, v179 row_mirror row_mask:0xf bank_mask:0xf
v_add_f32_dpp v180, v180, v180 row_mirror row_mask:0xf bank_mask:0xf
v_add_f32_dpp v181, v181, v181 row_mirror row_mask:0xf bank_mask:0xf
v_add_f32_dpp v182, v182, v182 row_mirror row_mask:0xf bank_mask:0xf
v_add_f32_dpp v183, v183, v183 row_mirror row_mask:0xf bank_mask:0xf
K 184,176,177
K 185,178,179
K 186,180,181
K 187,182,183
v_lshlrev_b32 v176, 16, v184
v_and_b32 v177, 0xffff0000, v184
v_lshlrev_b32 v178, 16, v185
v_and_b32 v179, 0xffff0000, v185
v_lshlrev_b32 v180, 16, v186
v_and_b32 v181, 0xffff0000, v186
v_lshlrev_b32 v182, 16, v187
v_and_b32 v183, 0xffff0000, v187
U 176,176,3
U 177,177,3
U 178,178,3
U 179,179,3
U 180,180,3
U 181,181,3
U 182,182,3
U 183,183,3
K 184,176,177
K 185,178,179
K 186,180,181
K 187,182,183
v_lshlrev_b32 v176, 16, v184
v_and_b32 v177, 0xffff0000, v184
v_lshlrev_b32 v178, 16, v185
v_and_b32 v179, 0xffff0000, v185
v_lshlrev_b32 v180, 16, v186
v_and_b32 v181, 0xffff0000, v186
v_lshlrev_b32 v182, 16, v187
v_and_b32 v183, 0xffff0000, v187
A 176,176,72
A 177,177,73
A 178,178,74
A 179,179,75
A 180,180,76
A 181,181,77
A 182,182,78
A 183,183,79
K 184,176,177
K 185,178,179
K 186,180,181
K 187,182,183
v_lshlrev_b32 v176, 16, v184
v_and_b32 v177, 0xffff0000, v184
v_lshlrev_b32 v178, 16, v185
v_and_b32 v179, 0xffff0000, v185
v_lshlrev_b32 v180, 16, v186
v_and_b32 v181, 0xffff0000, v186
v_lshlrev_b32 v182, 16, v187
v_and_b32 v183, 0xffff0000, v187
v_cndmask_b32 v72, v72, v176, vcc
v_cndmask_b32 v73, v73, v177, vcc
v_cndmask_b32 v74, v74, v178, vcc
v_cndmask_b32 v75, v75, v179, vcc
v_cndmask_b32 v76, v76, v180, vcc
v_cndmask_b32 v77, v77, v181, vcc
v_cndmask_b32 v78, v78, v182, vcc
v_cndmask_b32 v79, v79, v183, vcc
.LL_121%=:
.LL_120%=:
s_bitset0_b32 s68, s69
s_cmp_eq_u32 s68, 0
s_cbranch_scc1 .Lupr_116%=
s_cmp_eq_u32 s64, 0
s_cbranch_scc0 .LL_123%=
s_waitcnt vmcnt(0)
v_lshlrev_b32 v2, 2, v1
v_mov_b64 v[28:29], s[12:13]
s_mov_b64 exec, 3
buffer_store_dwordx2 v[28:29], v2, s[8:11], s53 offen offset:512 sc1
s_mov_b64 exec, -1
s_mov_b32 s64, 1
.LL_123%=:
s_ff1_i32_b32 s69, s68
s_nop 3
v_readlane_b32 s70, v12, s69
s_nop 3
s_lshl_b32 s66, s70, 2
s_add_u32 s66, s66, 2752512
buffer_load_dword v3, off, s[8:11], s66 sc1
s_mul_i32 s66, s58, 640
s_mul_i32 s67, s69, 2560
s_add_u32 s66, s66, s67
s_lshl_b32 s67, s14, 7
s_add_u32 s66, s66, s67
s_add_u32 s66, s66, 2760704
buffer_load_dwordx4 a[0:3], v94, s[8:11], s66 offen offset:0 sc1
buffer_load_dwordx4 a[4:7], v94, s[8:11], s66 offen offset:16 sc1
s_add_u32 s66, s66, 327680
buffer_load_dwordx4 a[8:11], v94, s[8:11], s66 offen offset:0 sc1
buffer_load_dwordx4 a[12:15], v94, s[8:11], s66 offen offset:16 sc1
s_add_u32 s66, s66, 327680
buffer_load_dwordx4 a[16:19], v94, s[8:11], s66 offen offset:0 sc1
buffer_load_dwordx4 a[20:23], v94, s[8:11], s66 offen offset:16 sc1
s_add_u32 s66, s66, 327680
buffer_load_dwordx4 a[24:27], v95, s[8:11], s66 offen offset:0 sc1
buffer_load_dwordx4 a[28:31], v95, s[8:11], s66 offen offset:16 sc1
s_mul_i32 s66, s58, 640
s_mul_i32 s67, s69, 2560
s_add_u32 s66, s66, s67
s_add_u32 s66, s66, 2761216
s_cmp_lt_u32 s14, 2
s_cbranch_scc0 .LL_124%=
buffer_load_dwordx4 a[32:35], v94, s[8:11], s66 offen offset:0 sc1
buffer_load_dwordx4 a[36:39], v94, s[8:11], s66 offen offset:16 sc1
s_add_u32 s66, s66, 327680
buffer_load_dwordx4 a[40:43], v94, s[8:11], s66 offen offset:0 sc1
buffer_load_dwordx4 a[44:47], v94, s[8:11], s66 offen offset:16 sc1
s_add_u32 s66, s66, 327680
buffer_load_dwordx4 a[48:51], v94, s[8:11], s66 offen offset:0 sc1
buffer_load_dwordx4 a[52:55], v94, s[8:11], s66 offen offset:16 sc1
s_add_u32 s66, s66, 327680
buffer_load_dwordx4 a[56:59], v95, s[8:11], s66 offen offset:0 sc1
buffer_load_dwordx4 a[60:63], v95, s[8:11], s66 offen offset:16 sc1
.LL_124%=:
s_waitcnt vmcnt(0)
s_branch .Lup_115%=
.Lpub_10%=:
s_lshl_b32 s80, s15, 3
s_add_u32 s80, s80, 256
v_mov_b64 v[28:29], s[12:13]
s_mov_b64 exec, 1
buffer_store_dwordx2 v[28:29], off, s[8:11], s80 sc1
s_mov_b64 exec, -1
s_nop 0
s_branch .Lpubr_11%=
.Lspin_12%=:
s_mov_b32 s84, 0
.Llp_14%=:
s_sleep 2
v_bfe_u32 v28, v1, 4, 3
v_lshlrev_b32 v28, 3, v28
s_lshl_b32 s80, s60, 6
s_add_u32 s80, s80, 256
buffer_load_dwordx2 v[28:29], v28, s[8:11], s80 offen sc1
s_waitcnt vmcnt(0)
s_add_u32 s84, s84, 1
v_cmp_eq_u64 vcc, s[12:13], v[28:29]
s_cmp_eq_u64 vcc, -1
s_cbranch_scc1 .Lok_13%=
s_cmp_lt_u32 s84, 1048576
s_cbranch_scc1 .Llp_14%=
s_branch .Lok_13%=
.Lpub_15%=:
s_lshl_b32 s80, s15, 3
s_add_u32 s80, s80, 256
v_mov_b64 v[28:29], s[12:13]
s_mov_b64 exec, 1
buffer_store_dwordx2 v[28:29], off, s[8:11], s80 sc1
s_mov_b64 exec, -1
s_nop 0
s_branch .Lpubr_16%=
.Lrt_17%=:
v_lshrrev_b32 v136, 1, v1
buffer_load_dwordx2 v[96:97], v136, s[8:11], 0 offen offset:256 sc1
buffer_load_dwordx2 v[98:99], v136, s[8:11], 0 offen offset:768 sc1
buffer_load_dwordx2 v[100:101], v136, s[8:11], 0 offen offset:1280 sc1
buffer_load_dwordx2 v[102:103], v136, s[8:11], 0 offen offset:1792 sc1
s_branch .Lrtr_18%=
.Lrt_19%=:
s_mov_b32 s84, 0
.LL_21%=:
s_waitcnt vmcnt(0)
s_mov_b64 s[86:87], -1
v_cmp_eq_u64 vcc, s[12:13], v[96:97]
s_and_b64 s[86:87], s[86:87], vcc
v_cmp_eq_u64 vcc, s[12:13], v[98:99]
s_and_b64 s[86:87], s[86:87], vcc
v_cmp_eq_u64 vcc, s[12:13], v[100:101]
s_and_b64 s[86:87], s[86:87], vcc
v_cmp_eq_u64 vcc, s[12:13], v[102:103]
s_and_b64 s[86:87], s[86:87], vcc
s_cmp_eq_u64 s[86:87], -1
s_cbranch_scc1 .LL_22%=
s_sleep 2
buffer_load_dwordx2 v[96:97], v136, s[8:11], 0 offen offset:256 sc1
buffer_load_dwordx2 v[98:99], v136, s[8:11], 0 offen offset:768 sc1
buffer_load_dwordx2 v[100:101], v136, s[8:11], 0 offen offset:1280 sc1
buffer_load_dwordx2 v[102:103], v136, s[8:11], 0 offen offset:1792 sc1
s_add_u32 s84, s84, 1
s_cmp_lt_u32 s84, 1048576
s_cbranch_scc1 .LL_21%=
.LL_22%=:
s_mov_b32 s84, 2752512
buffer_load_dwordx4 v[104:107], v1, s[8:11], s84 offen offset:0 sc1
buffer_load_dwordx4 v[108:111], v1, s[8:11], s84 offen offset:1024 sc1
buffer_load_dwordx4 v[112:115], v1, s[8:11], s84 offen offset:2048 sc1
buffer_load_dwordx4 v[116:119], v1, s[8:11], s84 offen offset:3072 sc1
s_mov_b32 s84, 2756608
buffer_load_dwordx4 v[120:123], v1, s[8:11], s84 offen offset:0 sc1
buffer_load_dwordx4 v[124:127], v1, s[8:11], s84 offen offset:1024 sc1
buffer_load_dwordx4 v[128:131], v1, s[8:11], s84 offen offset:2048 sc1
buffer_load_dwordx4 v[132:135], v1, s[8:11], s84 offen offset:3072 sc1
s_branch .Lrtr_20%=
.Lspin_23%=:
s_mov_b32 s84, 0
.Llp_25%=:
s_sleep 2
v_bfe_u32 v28, v1, 4, 3
v_lshlrev_b32 v28, 3, v28
s_lshl_b32 s80, s60, 6
s_add_u32 s80, s80, 256
buffer_load_dwordx2 v[28:29], v28, s[8:11], s80 offen sc1
s_waitcnt vmcnt(0)
s_add_u32 s84, s84, 1
v_cmp_eq_u64 vcc, s[12:13], v[28:29]
s_cmp_eq_u64 vcc, -1
s_cbranch_scc1 .Lok_24%=
s_cmp_lt_u32 s84, 1048576
s_cbranch_scc1 .Llp_25%=
s_branch .Lok_24%=
.Lrt_26%=:
s_waitcnt vmcnt(0)
v_mov_b32 v96, v104
v_mov_b32 v97, 0
v_mov_b32 v98, 0
v_mov_b32 v100, 0
v_med3_u32 v98, v97, v98, v105
v_med3_u32 v97, v96, v97, v105
v_max_u32 v96, v96, v105
v_med3_u32 v98, v97, v98, v106
v_med3_u32 v97, v96, v97, v106
v_max_u32 v96, v96, v106
v_med3_u32 v98, v97, v98, v107
v_med3_u32 v97, v96, v97, v107
v_max_u32 v96, v96, v107
v_med3_u32 v98, v97, v98, v108
v_med3_u32 v97, v96, v97, v108
v_max_u32 v96, v96, v108
v_med3_u32 v98, v97, v98, v109
v_med3_u32 v97, v96, v97, v109
v_max_u32 v96, v96, v109
v_med3_u32 v98, v97, v98, v110
v_med3_u32 v97, v96, v97, v110
v_max_u32 v96, v96, v110
v_med3_u32 v98, v97, v98, v111
v_med3_u32 v97, v96, v97, v111
v_max_u32 v96, v96, v111
v_med3_u32 v98, v97, v98, v112
v_med3_u32 v97, v96, v97, v112
v_max_u32 v96, v96, v112
v_med3_u32 v98, v97, v98, v113
)"
R"(v_med3_u32 v97, v96, v97, v113
v_max_u32 v96, v96, v113
v_med3_u32 v98, v97, v98, v114
v_med3_u32 v97, v96, v97, v114
v_max_u32 v96, v96, v114
v_med3_u32 v98, v97, v98, v115
v_med3_u32 v97, v96, v97, v115
v_max_u32 v96, v96, v115
v_med3_u32 v98, v97, v98, v116
v_med3_u32 v97, v96, v97, v116
v_max_u32 v96, v96, v116
v_med3_u32 v98, v97, v98, v117
v_med3_u32 v97, v96, v97, v117
v_max_u32 v96, v96, v117
v_med3_u32 v98, v97, v98, v118
v_med3_u32 v97, v96, v97, v118
v_max_u32 v96, v96, v118
v_med3_u32 v98, v97, v98, v119
v_med3_u32 v97, v96, v97, v119
v_max_u32 v96, v96, v119
v_med3_u32 v98, v97, v98, v120
v_med3_u32 v97, v96, v97, v120
v_max_u32 v96, v96, v120
v_med3_u32 v98, v97, v98, v121
v_med3_u32 v97, v96, v97, v121
v_max_u32 v96, v96, v121
v_med3_u32 v98, v97, v98, v122
v_med3_u32 v97, v96, v97, v122
v_max_u32 v96, v96, v122
v_med3_u32 v98, v97, v98, v123
v_med3_u32 v97, v96, v97, v123
v_max_u32 v96, v96, v123
v_med3_u32 v98, v97, v98, v124
v_med3_u32 v97, v96, v97, v124
v_max_u32 v96, v96, v124
v_med3_u32 v98, v97, v98, v125
v_med3_u32 v97, v96, v97, v125
v_max_u32 v96, v96, v125
v_med3_u32 v98, v97, v98, v126
v_med3_u32 v97, v96, v97, v126
v_max_u32 v96, v96, v126
v_med3_u32 v98, v97, v98, v127
v_med3_u32 v97, v96, v97, v127
v_max_u32 v96, v96, v127
v_med3_u32 v98, v97, v98, v128
v_med3_u32 v97, v96, v97, v128
v_max_u32 v96, v96, v128
v_med3_u32 v98, v97, v98, v129
v_med3_u32 v97, v96, v97, v129
v_max_u32 v96, v96, v129
v_med3_u32 v98, v97, v98, v130
v_med3_u32 v97, v96, v97, v130
v_max_u32 v96, v96, v130
v_med3_u32 v98, v97, v98, v131
v_med3_u32 v97, v96, v97, v131
v_max_u32 v96, v96, v131
v_med3_u32 v98, v97, v98, v132
v_med3_u32 v97, v96, v97, v132
v_max_u32 v96, v96, v132
v_med3_u32 v98, v97, v98, v133
v_med3_u32 v97, v96, v97, v133
v_max_u32 v96, v96, v133
v_med3_u32 v98, v97, v98, v134
v_med3_u32 v97, v96, v97, v134
v_max_u32 v96, v96, v134
v_med3_u32 v98, v97, v98, v135
v_med3_u32 v97, v96, v97, v135
v_max_u32 v96, v96, v135
v_mov_b32 v99, v96
s_nop 1
v_max_u32_dpp v99, v99, v99 row_shr:1 row_mask:0xf bank_mask:0xf
s_nop 1
v_max_u32_dpp v99, v99, v99 row_shr:2 row_mask:0xf bank_mask:0xf
s_nop 1
v_max_u32_dpp v99, v99, v99 row_shr:4 row_mask:0xf bank_mask:0xf
s_nop 1
v_max_u32_dpp v99, v99, v99 row_shr:8 row_mask:0xf bank_mask:0xf
s_nop 1
v_max_u32_dpp v99, v99, v99 row_bcast:15 row_mask:0xa bank_mask:0xf
s_nop 1
v_max_u32_dpp v99, v99, v99 row_bcast:31 row_mask:0xc bank_mask:0xf
s_nop 0
v_readlane_b32 s86, v99, 63
s_nop 1
v_cmp_eq_u32 vcc, s86, v96
s_and_b32 s87, s86, 0xffff
s_sub_u32 s87, 2047, s87
v_cndmask_b32 v96, v96, v97, vcc
v_cndmask_b32_e64 v97, v97, 0, vcc
v_writelane_b32 v100, s87, 0
v_mov_b32 v99, v96
s_nop 1
v_max_u32_dpp v99, v99, v99 row_shr:1 row_mask:0xf bank_mask:0xf
s_nop 1
v_max_u32_dpp v99, v99, v99 row_shr:2 row_mask:0xf bank_mask:0xf
s_nop 1
v_max_u32_dpp v99, v99, v99 row_shr:4 row_mask:0xf bank_mask:0xf
s_nop 1
v_max_u32_dpp v99, v99, v99 row_shr:8 row_mask:0xf bank_mask:0xf
s_nop 1
v_max_u32_dpp v99, v99, v99 row_bcast:15 row_mask:0xa bank_mask:0xf
s_nop 1
v_max_u32_dpp v99, v99, v99 row_bcast:31 row_mask:0xc bank_mask:0xf
s_nop 0
v_readlane_b32 s86, v99, 63
s_nop 1
v_cmp_eq_u32 vcc, s86, v96
s_and_b32 s87, s86, 0xffff
s_sub_u32 s87, 2047, s87
v_cndmask_b32 v96, v96, v97, vcc
v_cndmask_b32_e64 v97, v97, 0, vcc
v_writelane_b32 v100, s87, 1
v_mov_b32 v99, v96
s_nop 1
v_max_u32_dpp v99, v99, v99 row_shr:1 row_mask:0xf bank_mask:0xf
s_nop 1
v_max_u32_dpp v99, v99, v99 row_shr:2 row_mask:0xf bank_mask:0xf
s_nop 1
v_max_u32_dpp v99, v99, v99 row_shr:4 row_mask:0xf bank_mask:0xf
s_nop 1
v_max_u32_dpp v99, v99, v99 row_shr:8 row_mask:0xf bank_mask:0xf
s_nop 1
v_max_u32_dpp v99, v99, v99 row_bcast:15 row_mask:0xa bank_mask:0xf
s_nop 1
v_max_u32_dpp v99, v99, v99 row_bcast:31 row_mask:0xc bank_mask:0xf
s_nop 0
v_readlane_b32 s86, v99, 63
s_nop 1
v_cmp_eq_u32 vcc, s86, v96
s_and_b32 s87, s86, 0xffff
s_sub_u32 s87, 2047, s87
v_cndmask_b32 v96, v96, v97, vcc
v_cndmask_b32_e64 v97, v97, 0, vcc
v_writelane_b32 v100, s87, 2
v_mov_b32 v99, v96
s_nop 1
v_max_u32_dpp v99, v99, v99 row_shr:1 row_mask:0xf bank_mask:0xf
s_nop 1
v_max_u32_dpp v99, v99, v99 row_shr:2 row_mask:0xf bank_mask:0xf
s_nop 1
v_max_u32_dpp v99, v99, v99 row_shr:4 row_mask:0xf bank_mask:0xf
s_nop 1
v_max_u32_dpp v99, v99, v99 row_shr:8 row_mask:0xf bank_mask:0xf
s_nop 1
v_max_u32_dpp v99, v99, v99 row_bcast:15 row_mask:0xa bank_mask:0xf
s_nop 1
v_max_u32_dpp v99, v99, v99 row_bcast:31 row_mask:0xc bank_mask:0xf
s_nop 0
v_readlane_b32 s86, v99, 63
s_nop 1
v_cmp_eq_u32 vcc, s86, v96
s_and_b32 s87, s86, 0xffff
s_sub_u32 s87, 2047, s87
v_cndmask_b32 v96, v96, v97, vcc
v_cndmask_b32_e64 v97, v97, 0, vcc
v_writelane_b32 v100, s87, 3
v_mov_b32 v99, v96
s_nop 1
v_max_u32_dpp v99, v99, v99 row_shr:1 row_mask:0xf bank_mask:0xf
s_nop 1
v_max_u32_dpp v99, v99, v99 row_shr:2 row_mask:0xf bank_mask:0xf
s_nop 1
v_max_u32_dpp v99, v99, v99 row_shr:4 row_mask:0xf bank_mask:0xf
s_nop 1
v_max_u32_dpp v99, v99, v99 row_shr:8 row_mask:0xf bank_mask:0xf
s_nop 1
v_max_u32_dpp v99, v99, v99 row_bcast:15 row_mask:0xa bank_mask:0xf
s_nop 1
v_max_u32_dpp v99, v99, v99 row_bcast:31 row_mask:0xc bank_mask:0xf
s_nop 0
v_readlane_b32 s86, v99, 63
s_nop 1
v_cmp_eq_u32 vcc, s86, v96
s_and_b32 s87, s86, 0xffff
s_sub_u32 s87, 2047, s87
v_cndmask_b32 v96, v96, v97, vcc
v_cndmask_b32_e64 v97, v97, 0, vcc
v_writelane_b32 v100, s87, 4
v_mov_b32 v99, v96
s_nop 1
v_max_u32_dpp v99, v99, v99 row_shr:1 row_mask:0xf bank_mask:0xf
s_nop 1
v_max_u32_dpp v99, v99, v99 row_shr:2 row_mask:0xf bank_mask:0xf
s_nop 1
v_max_u32_dpp v99, v99, v99 row_shr:4 row_mask:0xf bank_mask:0xf
s_nop 1
v_max_u32_dpp v99, v99, v99 row_shr:8 row_mask:0xf bank_mask:0xf
s_nop 1
v_max_u32_dpp v99, v99, v99 row_bcast:15 row_mask:0xa bank_mask:0xf
s_nop 1
v_max_u32_dpp v99, v99, v99 row_bcast:31 row_mask:0xc bank_mask:0xf
s_nop 0
v_readlane_b32 s86, v99, 63
s_nop 1
v_cmp_eq_u32 vcc, s86, v96
s_and_b32 s87, s86, 0xffff
s_sub_u32 s87, 2047, s87
v_cndmask_b32 v96, v96, v97, vcc
v_cndmask_b32_e64 v97, v97, 0, vcc
v_writelane_b32 v100, s87, 5
v_mov_b32 v99, v96
s_nop 1
v_max_u32_dpp v99, v99, v99 row_shr:1 row_mask:0xf bank_mask:0xf
s_nop 1
v_max_u32_dpp v99, v99, v99 row_shr:2 row_mask:0xf bank_mask:0xf
s_nop 1
v_max_u32_dpp v99, v99, v99 row_shr:4 row_mask:0xf bank_mask:0xf
s_nop 1
v_max_u32_dpp v99, v99, v99 row_shr:8 row_mask:0xf bank_mask:0xf
s_nop 1
v_max_u32_dpp v99, v99, v99 row_bcast:15 row_mask:0xa bank_mask:0xf
s_nop 1
v_max_u32_dpp v99, v99, v99 row_bcast:31 row_mask:0xc bank_mask:0xf
s_nop 0
v_readlane_b32 s86, v99, 63
s_nop 1
v_cmp_eq_u32 vcc, s86, v96
s_and_b32 s87, s86, 0xffff
s_sub_u32 s87, 2047, s87
v_cndmask_b32 v96, v96, v97, vcc
v_cndmask_b32_e64 v97, v97, 0, vcc
v_writelane_b32 v100, s87, 6
v_mov_b32 v99, v96
s_nop 1
v_max_u32_dpp v99, v99, v99 row_shr:1 row_mask:0xf bank_mask:0xf
s_nop 1
v_max_u32_dpp v99, v99, v99 row_shr:2 row_mask:0xf bank_mask:0xf
s_nop 1
v_max_u32_dpp v99, v99, v99 row_shr:4 row_mask:0xf bank_mask:0xf
s_nop 1
v_max_u32_dpp v99, v99, v99 row_shr:8 row_mask:0xf bank_mask:0xf
s_nop 1
v_max_u32_dpp v99, v99, v99 row_bcast:15 row_mask:0xa bank_mask:0xf
s_nop 1
v_max_u32_dpp v99, v99, v99 row_bcast:31 row_mask:0xc bank_mask:0xf
s_nop 0
v_readlane_b32 s86, v99, 63
s_nop 1
v_cmp_eq_u32 vcc, s86, v96
s_and_b32 s87, s86, 0xffff
s_sub_u32 s87, 2047, s87
v_cndmask_b32 v96, v96, v97, vcc
v_cndmask_b32_e64 v97, v97, 0, vcc
v_writelane_b32 v100, s87, 7
v_mov_b32 v99, v98
s_nop 1
v_max_u32_dpp v99, v99, v99 row_shr:1 row_mask:0xf bank_mask:0xf
s_nop 1
v_max_u32_dpp v99, v99, v99 row_shr:2 row_mask:0xf bank_mask:0xf
s_nop 1
v_max_u32_dpp v99, v99, v99 row_shr:4 row_mask:0xf bank_mask:0xf
s_nop 1
v_max_u32_dpp v99, v99, v99 row_shr:8 row_mask:0xf bank_mask:0xf
s_nop 1
v_max_u32_dpp v99, v99, v99 row_bcast:15 row_mask:0xa bank_mask:0xf
s_nop 1
v_max_u32_dpp v99, v99, v99 row_bcast:31 row_mask:0xc bank_mask:0xf
s_nop 0
v_readlane_b32 s87, v99, 63
s_nop 0
s_cmp_gt_u32 s87, s86
s_cbranch_scc0 .LL_28%=
s_mov_b32 m0, 0
.LL_29%=:
v_max3_u32 v96, v104, v105, v106
v_max3_u32 v96, v96, v107, v108
v_max3_u32 v96, v96, v109, v110
v_max3_u32 v96, v96, v111, v112
v_max3_u32 v96, v96, v113, v114
v_max3_u32 v96, v96, v115, v116
v_max3_u32 v96, v96, v117, v118
v_max3_u32 v96, v96, v119, v120
v_max3_u32 v96, v96, v121, v122
v_max3_u32 v96, v96, v123, v124
v_max3_u32 v96, v96, v125, v126
v_max3_u32 v96, v96, v127, v128
v_max3_u32 v96, v96, v129, v130
v_max3_u32 v96, v96, v131, v132
v_max3_u32 v96, v96, v133, v134
v_max_u32 v96, v96, v135
v_mov_b32 v99, v96
s_nop 1
v_max_u32_dpp v99, v99, v99 row_shr:1 row_mask:0xf bank_mask:0xf
s_nop 1
v_max_u32_dpp v99, v99, v99 row_shr:2 row_mask:0xf bank_mask:0xf
s_nop 1
v_max_u32_dpp v99, v99, v99 row_shr:4 row_mask:0xf bank_mask:0xf
s_nop 1
v_max_u32_dpp v99, v99, v99 row_shr:8 row_mask:0xf bank_mask:0xf
s_nop 1
v_max_u32_dpp v99, v99, v99 row_bcast:15 row_mask:0xa bank_mask:0xf
s_nop 1
v_max_u32_dpp v99, v99, v99 row_bcast:31 row_mask:0xc bank_mask:0xf
s_nop 0
v_readlane_b32 s86, v99, 63
s_nop 1
v_cmp_eq_u32 vcc, s86, v104
s_nop 1
v_cndmask_b32_e64 v104, v104, 0, vcc
v_cmp_eq_u32 vcc, s86, v105
s_nop 1
v_cndmask_b32_e64 v105, v105, 0, vcc
v_cmp_eq_u32 vcc, s86, v106
s_nop 1
v_cndmask_b32_e64 v106, v106, 0, vcc
v_cmp_eq_u32 vcc, s86, v107
s_nop 1
v_cndmask_b32_e64 v107, v107, 0, vcc
v_cmp_eq_u32 vcc, s86, v108
s_nop 1
v_cndmask_b32_e64 v108, v108, 0, vcc
v_cmp_eq_u32 vcc, s86, v109
s_nop 1
v_cndmask_b32_e64 v109, v109, 0, vcc
v_cmp_eq_u32 vcc, s86, v110
s_nop 1
v_cndmask_b32_e64 v110, v110, 0, vcc
v_cmp_eq_u32 vcc, s86, v111
s_nop 1
v_cndmask_b32_e64 v111, v111, 0, vcc
v_cmp_eq_u32 vcc, s86, v112
s_nop 1
v_cndmask_b32_e64 v112, v112, 0, vcc
v_cmp_eq_u32 vcc, s86, v113
s_nop 1
v_cndmask_b32_e64 v113, v113, 0, vcc
v_cmp_eq_u32 vcc, s86, v114
s_nop 1
v_cndmask_b32_e64 v114, v114, 0, vcc
v_cmp_eq_u32 vcc, s86, v115
s_nop 1
v_cndmask_b32_e64 v115, v115, 0, vcc
v_cmp_eq_u32 vcc, s86, v116
s_nop 1
v_cndmask_b32_e64 v116, v116, 0, vcc
v_cmp_eq_u32 vcc, s86, v117
s_nop 1
v_cndmask_b32_e64 v117, v117, 0, vcc
v_cmp_eq_u32 vcc, s86, v118
s_nop 1
v_cndmask_b32_e64 v118, v118, 0, vcc
v_cmp_eq_u32 vcc, s86, v119
s_nop 1
v_cndmask_b32_e64 v119, v119, 0, vcc
v_cmp_eq_u32 vcc, s86, v120
s_nop 1
v_cndmask_b32_e64 v120, v120, 0, vcc
v_cmp_eq_u32 vcc, s86, v121
s_nop 1
v_cndmask_b32_e64 v121, v121, 0, vcc
v_cmp_eq_u32 vcc, s86, v122
s_nop 1
v_cndmask_b32_e64 v122, v122, 0, vcc
v_cmp_eq_u32 vcc, s86, v123
s_nop 1
v_cndmask_b32_e64 v123, v123, 0, vcc
v_cmp_eq_u32 vcc, s86, v124
s_nop 1
v_cndmask_b32_e64 v124, v124, 0, vcc
v_cmp_eq_u32 vcc, s86, v125
s_nop 1
v_cndmask_b32_e64 v125, v125, 0, vcc
v_cmp_eq_u32 vcc, s86, v126
s_nop 1
v_cndmask_b32_e64 v126, v126, 0, vcc
v_cmp_eq_u32 vcc, s86, v127
s_nop 1
v_cndmask_b32_e64 v127, v127, 0, vcc
v_cmp_eq_u32 vcc, s86, v128
s_nop 1
v_cndmask_b32_e64 v128, v128, 0, vcc
v_cmp_eq_u32 vcc, s86, v129
s_nop 1
v_cndmask_b32_e64 v129, v129, 0, vcc
v_cmp_eq_u32 vcc, s86, v130
s_nop 1
v_cndmask_b32_e64 v130, v130, 0, vcc
v_cmp_eq_u32 vcc, s86, v131
s_nop 1
v_cndmask_b32_e64 v131, v131, 0, vcc
v_cmp_eq_u32 vcc, s86, v132
s_nop 1
v_cndmask_b32_e64 v132, v132, 0, vcc
v_cmp_eq_u32 vcc, s86, v133
s_nop 1
v_cndmask_b32_e64 v133, v133, 0, vcc
v_cmp_eq_u32 vcc, s86, v134
s_nop 1
v_cndmask_b32_e64 v134, v134, 0, vcc
v_cmp_eq_u32 vcc, s86, v135
s_nop 1
v_cndmask_b32_e64 v135, v135, 0, vcc
s_and_b32 s87, s86, 0xffff
s_sub_u32 s87, 2047, s87
s_nop 0
v_writelane_b32 v100, s87, m0
s_add_u32 m0, m0, 1
s_cmp_lt_u32 m0, 8
s_cbranch_scc1 .LL_29%=
.LL_28%=:
)"
R"(v_xor_b32 v102, s12, v100
v_mov_b32 v103, s13
s_mov_b64 exec, 0xff
buffer_store_dwordx2 v[102:103], v136, s[8:11], 0 offen offset:3584 sc1
s_mov_b64 exec, -1
s_branch .Lrtr_27%=
.Lspin_30%=:
s_mov_b32 s84, 0
.Llp_32%=:
s_sleep 2
v_bfe_u32 v28, v1, 4, 5
v_lshlrev_b32 v28, 3, v28
s_lshl_b32 s80, s56, 9
s_add_u32 s80, s80, 4096
buffer_load_dwordx2 v[28:29], v28, s[8:11], s80 offen sc1
s_waitcnt vmcnt(0)
s_add_u32 s84, s84, 1
v_cmp_eq_u64 vcc, s[12:13], v[28:29]
s_cmp_eq_u64 vcc, -1
s_cbranch_scc1 .Lok_31%=
s_cmp_lt_u32 s84, 1048576
s_cbranch_scc1 .Llp_32%=
s_branch .Lok_31%=
.Lspin_60%=:
s_mov_b32 s84, 0
.Llp_62%=:
s_sleep 2
buffer_load_dwordx2 v[8:9], v11, s[8:11], 0 offen offset:3584 sc1
s_waitcnt vmcnt(0)
s_add_u32 s84, s84, 1
v_xor_b32 v8, s12, v8
v_xor_b32 v9, s13, v9
v_lshrrev_b32 v10, 11, v8
v_or_b32 v10, v10, v9
v_cmp_eq_u32_e64 s[86:87], 0, v10
s_cmp_eq_u64 s[86:87], -1
s_cbranch_scc1 .Lok_61%=
s_cmp_lt_u32 s84, 1048576
s_cbranch_scc1 .Llp_62%=
s_branch .Lok_61%=
.Lmk_71%=:
s_nop 7
v_cndmask_b32_e64 v168, v241, v168, s[72:73]
v_cndmask_b32_e64 v169, v241, v169, s[74:75]
v_cndmask_b32_e64 v170, v241, v170, s[76:77]
v_cndmask_b32_e64 v171, v241, v171, s[78:79]
v_cndmask_b32_e64 v188, v241, v188, s[72:73]
v_cndmask_b32_e64 v189, v241, v189, s[74:75]
v_cndmask_b32_e64 v190, v241, v190, s[76:77]
v_cndmask_b32_e64 v191, v241, v191, s[78:79]
v_mov_b32 v184, v241
v_mov_b32 v185, v241
v_mov_b32 v186, v241
v_mov_b32 v187, v241
s_branch .Lmkr_72%=
.Lspin_74%=:
s_mov_b32 s84, 0
.Llp_76%=:
s_sleep 2
v_bfe_u32 v28, v1, 4, 5
v_lshlrev_b32 v28, 3, v28
s_lshl_b32 s80, s56, 9
s_add_u32 s80, s80, 4352
buffer_load_dwordx2 v[28:29], v28, s[8:11], s80 offen sc1
s_waitcnt vmcnt(0)
s_add_u32 s84, s84, 1
v_cmp_eq_u64 vcc, s[12:13], v[28:29]
s_cmp_eq_u64 vcc, -1
s_cbranch_scc1 .Lok_75%=
s_cmp_lt_u32 s84, 1048576
s_cbranch_scc1 .Llp_76%=
s_branch .Lok_75%=
.Lact_50%=:
s_call_b64 s[4:5], .Lhandler%=
s_branch .Lactr_51%=
.Lact_52%=:
s_call_b64 s[4:5], .Lhandler%=
s_branch .Lactr_53%=
.Lact_54%=:
s_call_b64 s[4:5], .Lhandler%=
s_branch .Lactr_55%=
.Lact_56%=:
s_call_b64 s[4:5], .Lhandler%=
s_branch .Lactr_57%=
.Lact_58%=:
s_call_b64 s[4:5], .Lhandler%=
s_branch .Lactr_59%=
.Lact_63%=:
s_call_b64 s[4:5], .Lhandler%=
s_branch .Lactr_64%=
.Lact_65%=:
s_call_b64 s[4:5], .Lhandler%=
s_branch .Lactr_66%=
.Lact_67%=:
s_call_b64 s[4:5], .Lhandler%=
s_branch .Lactr_68%=
.Lact_69%=:
s_call_b64 s[4:5], .Lhandler%=
s_branch .Lactr_70%=
.Lhandler%=:
s_cmp_eq_u32 s64, 2
s_cbranch_scc0 .LL_73%=
s_waitcnt vmcnt(0)
v_cmp_eq_u64 vcc, s[12:13], v[28:29]
s_cmp_eq_u64 vcc, -1
s_cbranch_scc0 .Lspin_74%=
.Lok_75%=:
.LL_73%=:
s_cmp_eq_u32 s64, s70
s_cbranch_scc0 .LL_77%=
s_mov_b32 s70, 0
s_and_b64 s[86:87], s[88:89], s[24:25]
s_cbranch_scc1 .LL_78%=
s_waitcnt vmcnt(0)
v_mov_b64 v[28:29], s[12:13]
s_mov_b64 exec, 1
buffer_store_dwordx2 v[28:29], off, s[8:11], s69 sc1
s_mov_b64 exec, -1
.LL_78%=:
.LL_77%=:
s_cmp_eq_u32 s64, s91
s_cbranch_scc0 .LL_79%=
s_waitcnt vmcnt(0)
v_mov_b64 v[28:29], s[12:13]
s_mov_b64 exec, 1
buffer_store_dwordx2 v[28:29], off, s[8:11], s71 sc1
s_mov_b64 exec, -1
.LL_79%=:
s_cmp_eq_u32 s64, 1
s_cbranch_scc0 .LL_80%=
v_bfe_u32 v28, v1, 4, 5
v_lshlrev_b32 v28, 3, v28
s_lshl_b32 s80, s56, 9
s_add_u32 s80, s80, 4352
buffer_load_dwordx2 v[28:29], v28, s[8:11], s80 offen sc1
.LL_80%=:
s_cmp_eq_u32 s64, 12
s_cbranch_scc0 .LL_81%=
s_lshr_b32 s80, s15, 3
s_lshl_b32 s81, s59, 5
s_add_u32 s80, s80, s81
s_lshl_b32 s80, s80, 2
s_add_u32 s80, s80, s14
s_mul_i32 s80, s80, 5120
v_min_u32 v28, 624, v1
v_lshl_add_u32 v28, v28, 3, s80
global_load_dword v29, v28, s[26:27]
s_lshr_b32 s80, s15, 3
s_lshl_b32 s80, s80, 2
s_add_u32 s80, s80, s14
s_mul_i32 s80, s80, 1536
v_min_u32 v28, 176, v1
v_lshl_add_u32 v28, v28, 3, s80
v_min_u32 v28, .Lend%=-.Lbeg%=-4, v28
global_load_dword v29, v28, s[6:7]
.LL_81%=:
s_setpc_b64 s[4:5]
.Lredo%=:
s_waitcnt vmcnt(0) lgkmcnt(0)
s_barrier
s_and_b32 s81, s14, 1
s_mul_i32 s91, s81, 16902144
s_mul_i32 s82, s56, 528384
s_add_u32 s91, s91, s82
s_add_u32 s91, s91, 4194304
s_lshr_b32 s82, s14, 1
s_lshl_b32 s83, s82, 14
s_add_u32 s91, s91, s83
s_lshl_b32 s81, s81, 14
s_lshl_b32 s82, s82, 16
s_or_b32 s92, s81, s82
s_xor_b32 s92, s92, 0x10000
s_add_u32 m0, s92, 0
s_add_u32 s81, s91, 0
Dc 1,8,81,0
Dc 1,8,81,1024
Dc 1,8,81,2048
Dc 1,8,81,3072
s_add_u32 m0, s92, 4096
s_add_u32 s81, s91, 4096
Dc 1,8,81,0
Dc 1,8,81,1024
Dc 1,8,81,2048
Dc 1,8,81,3072
s_add_u32 m0, s92, 8192
s_add_u32 s81, s91, 8192
Dc 1,8,81,0
Dc 1,8,81,1024
Dc 1,8,81,2048
Dc 1,8,81,3072
s_add_u32 m0, s92, 12288
s_add_u32 s81, s91, 12288
Dc 1,8,81,0
Dc 1,8,81,1024
Dc 1,8,81,2048
Dc 1,8,81,3072
v_and_b32 v28, 16, v1
v_cmp_eq_u32 vcc, 0, v28
v_mov_b32 v240, 0xeeeeeeee
v_mov_b32 v29, 0x44444444
v_cndmask_b32 v240, v240, v29, vcc
v_mov_b32 v241, 0xff800000
v_add_u32 v242, 0x10000, v1
v_add_u32 v243, 4096, v1
v_add_u32 v244, 8192, v1
v_add_u32 v245, 12288, v1
v_bfe_u32 v28, v1, 4, 4
v_lshrrev_b32 v29, 8, v1
v_lshlrev_b32 v29, 2, v29
v_sub_u32 v28, v28, v29
v_cmp_le_i32_e64 s[72:73], 0, v28
v_cmp_le_i32_e64 s[74:75], 1, v28
v_cmp_le_i32_e64 s[76:77], 2, v28
v_cmp_le_i32_e64 s[78:79], 3, v28
s_mov_b32 s80, 0x3f803f80
s_mov_b32 s81, 0x3f003f00
v_accvgpr_write_b32 a176, s80
v_accvgpr_write_b32 a180, s81
v_accvgpr_write_b32 a177, s80
v_accvgpr_write_b32 a181, s81
v_accvgpr_write_b32 a178, s80
v_accvgpr_write_b32 a182, s81
v_accvgpr_write_b32 a179, s80
v_accvgpr_write_b32 a183, s81
v_accvgpr_write_b32 a160, 0
v_accvgpr_write_b32 a161, 0
v_accvgpr_write_b32 a162, 0
v_accvgpr_write_b32 a163, 0
v_accvgpr_write_b32 a164, 0
v_accvgpr_write_b32 a165, 0
v_accvgpr_write_b32 a166, 0
v_accvgpr_write_b32 a167, 0
v_accvgpr_write_b32 a168, 0
v_accvgpr_write_b32 a169, 0
v_accvgpr_write_b32 a170, 0
v_accvgpr_write_b32 a171, 0
v_accvgpr_write_b32 a172, 0
v_accvgpr_write_b32 a173, 0
v_accvgpr_write_b32 a174, 0
v_accvgpr_write_b32 a175, 0
s_mov_b32 s90, 0x5d800000
s_mov_b64 s[88:89], 0
s_mov_b32 s64, 0
s_and_b32 s80, s14, 1
s_lshl_b32 s62, s80, 14
s_lshl_b32 s82, s57, 15
s_sub_u32 s82, 507904, s82
s_add_u32 s82, s82, s62
s_lshl_b32 s65, s57, 1
s_sub_u32 s65, s65, 1
s_max_i32 s65, s65, 0
s_sub_u32 s66, s60, 1
s_cmp_lt_u32 s14, 2
s_cselect_b32 s82, s82, 16384
s_cselect_b32 s66, s66, 15
s_cselect_b32 s83, 32768, 0
s_add_u32 s61, s91, s82
s_add_u32 s62, s62, s83
s_sub_u32 s63, 16384, s83
s_lshl_b32 s67, s60, 7
s_add_u32 s67, s67, s56
s_lshl_b32 s67, s67, 12
s_add_u32 s67, s67, 39845888
s_lshl_b32 s69, s60, 3
s_add_u32 s69, s69, s59
s_lshl_b32 s69, s69, 6
s_lshl_b32 s83, s58, 3
s_add_u32 s69, s69, s83
s_add_u32 s69, s69, 20480
s_mov_b32 s70, 0
s_mov_b32 s91, 0
s_mov_b64 s[24:25], 0
s_mov_b32 s68, 0
s_mov_b32 s85, 99
s_lshl_b32 s80, s60, 6
s_and_b32 s81, s14, 2
s_lshl_b32 s81, s81, 4
s_xor_b32 s82, s81, 0
s_add_u32 s82, s82, s80
v_add_u32 v4, s82, v28
v_mov_b32 v252, v241
s_xor_b32 s82, s81, 16
s_add_u32 s82, s82, s80
v_add_u32 v5, s82, v28
v_mov_b32 v253, v241
s_xor_b32 s82, s81, 32
s_add_u32 s82, s82, s80
v_add_u32 v6, s82, v28
v_mov_b32 v254, v241
s_xor_b32 s82, s81, 48
s_add_u32 s82, s82, s80
v_add_u32 v7, s82, v28
v_mov_b32 v255, v241
s_mul_i32 s82, s56, 528384
s_add_u32 s82, s82, 4194304
s_lshl_b32 s84, s60, 1
s_add_u32 s84, s84, 2
.LL_82%=:
buffer_load_dwordx4 a[128:131], v1, s[8:11], s82 offen offset:0 sc1
buffer_load_dwordx4 a[132:135], v1, s[8:11], s82 offen offset:1024 sc1
buffer_load_dwordx4 a[136:139], v1, s[8:11], s82 offen offset:2048 sc1
buffer_load_dwordx4 a[140:143], v1, s[8:11], s82 offen offset:3072 sc1
buffer_load_dwordx4 a[144:147], v243, s[8:11], s82 offen offset:0 sc1
buffer_load_dwordx4 a[148:151], v243, s[8:11], s82 offen offset:1024 sc1
buffer_load_dwordx4 a[152:155], v243, s[8:11], s82 offen offset:2048 sc1
buffer_load_dwordx4 a[156:159], v243, s[8:11], s82 offen offset:3072 sc1
s_waitcnt vmcnt(0)
Zvaa 160,128,0
Zvaa 164,128,16
Zvaa 168,128,32
Zvaa 172,128,48
Mvaa 160,132,4
Mvaa 164,132,20
Mvaa 168,132,36
Mvaa 172,132,52
Mvaa 160,136,8
Mvaa 164,136,24
Mvaa 168,136,40
Mvaa 172,136,56
Mvaa 160,140,12
Mvaa 164,140,28
Mvaa 168,140,44
Mvaa 172,140,60
Zvaa 176,144,0
Zvaa 180,144,16
Zvaa 184,144,32
Zvaa 188,144,48
Mvaa 176,148,4
Mvaa 180,148,20
Mvaa 184,148,36
Mvaa 188,148,52
Mvaa 176,152,8
Mvaa 180,152,24
Mvaa 184,152,40
Mvaa 188,152,56
Mvaa 176,156,12
Mvaa 180,156,28
Mvaa 184,156,44
Mvaa 188,156,60
v_cmp_le_i32 vcc, 0, v4
s_nop 1
v_cndmask_b32 v160, v241, v160, vcc
v_max_f32 v252, v252, v160
v_cmp_le_i32 vcc, 1, v4
s_nop 1
v_cndmask_b32 v161, v241, v161, vcc
v_max_f32 v252, v252, v161
v_cmp_le_i32 vcc, 2, v4
s_nop 1
v_cndmask_b32 v162, v241, v162, vcc
v_max_f32 v252, v252, v162
v_cmp_le_i32 vcc, 3, v4
s_nop 1
v_cndmask_b32 v163, v241, v163, vcc
v_max_f32 v252, v252, v163
v_cmp_le_i32 vcc, 0, v5
s_nop 1
v_cndmask_b32 v164, v241, v164, vcc
v_max_f32 v253, v253, v164
v_cmp_le_i32 vcc, 1, v5
s_nop 1
v_cndmask_b32 v165, v241, v165, vcc
v_max_f32 v253, v253, v165
v_cmp_le_i32 vcc, 2, v5
s_nop 1
v_cndmask_b32 v166, v241, v166, vcc
v_max_f32 v253, v253, v166
v_cmp_le_i32 vcc, 3, v5
s_nop 1
v_cndmask_b32 v167, v241, v167, vcc
v_max_f32 v253, v253, v167
v_cmp_le_i32 vcc, 0, v6
s_nop 1
v_cndmask_b32 v168, v241, v168, vcc
v_max_f32 v254, v254, v168
v_cmp_le_i32 vcc, 1, v6
s_nop 1
v_cndmask_b32 v169, v241, v169, vcc
v_max_f32 v254, v254, v169
v_cmp_le_i32 vcc, 2, v6
s_nop 1
v_cndmask_b32 v170, v241, v170, vcc
v_max_f32 v254, v254, v170
v_cmp_le_i32 vcc, 3, v6
s_nop 1
v_cndmask_b32 v171, v241, v171, vcc
v_max_f32 v254, v254, v171
v_cmp_le_i32 vcc, 0, v7
s_nop 1
v_cndmask_b32 v172, v241, v172, vcc
v_max_f32 v255, v255, v172
v_cmp_le_i32 vcc, 1, v7
s_nop 1
v_cndmask_b32 v173, v241, v173, vcc
v_max_f32 v255, v255, v173
v_cmp_le_i32 vcc, 2, v7
s_nop 1
v_cndmask_b32 v174, v241, v174, vcc
v_max_f32 v255, v255, v174
v_cmp_le_i32 vcc, 3, v7
)"
R"(s_nop 1
v_cndmask_b32 v175, v241, v175, vcc
v_max_f32 v255, v255, v175
v_cmp_le_i32 vcc, 16, v4
s_nop 1
v_cndmask_b32 v176, v241, v176, vcc
v_max_f32 v252, v252, v176
v_cmp_le_i32 vcc, 17, v4
s_nop 1
v_cndmask_b32 v177, v241, v177, vcc
v_max_f32 v252, v252, v177
v_cmp_le_i32 vcc, 18, v4
s_nop 1
v_cndmask_b32 v178, v241, v178, vcc
v_max_f32 v252, v252, v178
v_cmp_le_i32 vcc, 19, v4
s_nop 1
v_cndmask_b32 v179, v241, v179, vcc
v_max_f32 v252, v252, v179
v_cmp_le_i32 vcc, 16, v5
s_nop 1
v_cndmask_b32 v180, v241, v180, vcc
v_max_f32 v253, v253, v180
v_cmp_le_i32 vcc, 17, v5
s_nop 1
v_cndmask_b32 v181, v241, v181, vcc
v_max_f32 v253, v253, v181
v_cmp_le_i32 vcc, 18, v5
s_nop 1
v_cndmask_b32 v182, v241, v182, vcc
v_max_f32 v253, v253, v182
v_cmp_le_i32 vcc, 19, v5
s_nop 1
v_cndmask_b32 v183, v241, v183, vcc
v_max_f32 v253, v253, v183
v_cmp_le_i32 vcc, 16, v6
s_nop 1
v_cndmask_b32 v184, v241, v184, vcc
v_max_f32 v254, v254, v184
v_cmp_le_i32 vcc, 17, v6
s_nop 1
v_cndmask_b32 v185, v241, v185, vcc
v_max_f32 v254, v254, v185
v_cmp_le_i32 vcc, 18, v6
s_nop 1
v_cndmask_b32 v186, v241, v186, vcc
v_max_f32 v254, v254, v186
v_cmp_le_i32 vcc, 19, v6
s_nop 1
v_cndmask_b32 v187, v241, v187, vcc
v_max_f32 v254, v254, v187
v_cmp_le_i32 vcc, 16, v7
s_nop 1
v_cndmask_b32 v188, v241, v188, vcc
v_max_f32 v255, v255, v188
v_cmp_le_i32 vcc, 17, v7
s_nop 1
v_cndmask_b32 v189, v241, v189, vcc
v_max_f32 v255, v255, v189
v_cmp_le_i32 vcc, 18, v7
s_nop 1
v_cndmask_b32 v190, v241, v190, vcc
v_max_f32 v255, v255, v190
v_cmp_le_i32 vcc, 19, v7
s_nop 1
v_cndmask_b32 v191, v241, v191, vcc
v_max_f32 v255, v255, v191
v_subrev_u32 v4, 32, v4
v_subrev_u32 v5, 32, v5
v_subrev_u32 v6, 32, v6
v_subrev_u32 v7, 32, v7
s_add_u32 s82, s82, 8192
s_sub_u32 s84, s84, 1
s_cmp_lg_u32 s84, 0
s_cbranch_scc1 .LL_82%=
s_cmp_lt_u32 s14, 2
s_cbranch_scc1 .LL_83%=
s_add_u32 s80, s62, 131072
v_add_u32 v12, s80, v1
ds_write_b128 v12, a[0:3] offset:0
ds_write_b128 v12, a[4:7] offset:1024
ds_write_b128 v12, a[8:11] offset:2048
ds_write_b128 v12, a[12:15] offset:3072
ds_write_b128 v12, a[16:19] offset:4096
ds_write_b128 v12, a[20:23] offset:5120
ds_write_b128 v12, a[24:27] offset:6144
ds_write_b128 v12, a[28:31] offset:7168
ds_write_b128 v12, a[32:35] offset:8192
ds_write_b128 v12, a[36:39] offset:9216
ds_write_b128 v12, a[40:43] offset:10240
ds_write_b128 v12, a[44:47] offset:11264
ds_write_b128 v12, a[48:51] offset:12288
ds_write_b128 v12, a[52:55] offset:13312
ds_write_b128 v12, a[56:59] offset:14336
ds_write_b128 v12, a[60:63] offset:15360
W 0
s_barrier
s_branch .Lbgo%=
.LL_83%=:
s_barrier
s_branch .Lago%=
.Lpd_86%=:
v_mov_b64 v[28:29], s[12:13]
s_mov_b64 exec, 1
buffer_store_dwordx2 v[28:29], off, s[8:11], s69 sc1
s_mov_b64 exec, -1
s_branch .Lpdr_87%=
.Lspin_88%=:
s_mov_b32 s84, 0
.Llp_90%=:
s_sleep 2
v_bfe_u32 v28, v1, 4, 2
v_lshlrev_b32 v28, 3, v28
buffer_load_dwordx2 v[28:29], v28, s[8:11], s55 offen sc1
s_waitcnt vmcnt(0)
s_add_u32 s84, s84, 1
v_cmp_eq_u64 vcc, s[12:13], v[28:29]
s_cmp_eq_u64 vcc, -1
s_cbranch_scc1 .Lok_89%=
s_cmp_lt_u32 s84, 1048576
s_cbranch_scc1 .Llp_90%=
s_branch .Lok_89%=
.LL_93%=:
s_mov_b32 s84, 0
.LL_95%=:
s_sleep 2
buffer_load_dwordx2 v[12:13], v27, s[8:11], 0 offen offset:3584 sc1
s_waitcnt vmcnt(0)
s_add_u32 s84, s84, 1
v_xor_b32 v12, s12, v12
v_xor_b32 v13, s13, v13
v_lshrrev_b32 v94, 11, v12
v_or_b32 v13, v13, v94
v_cmp_eq_u32 vcc, 0, v13
s_cmp_eq_u64 vcc, -1
s_cbranch_scc1 .LL_94%=
s_cmp_lt_u32 s84, 1048576
s_cbranch_scc1 .LL_95%=
s_branch .LL_94%=
.Lup_91%=:
s_mov_b32 s84, 0
.LL_97%=:
v_cmp_eq_u64 vcc, s[12:13], v[16:17]
s_mov_b64 s[86:87], vcc
v_cmp_eq_u64 vcc, s[12:13], v[18:19]
s_and_b64 s[86:87], s[86:87], vcc
s_cmp_eq_u64 s[86:87], -1
s_cbranch_scc1 .LL_96%=
s_sleep 2
buffer_load_dwordx2 v[16:17], v14, s[8:11], 0 offen offset:2560 sc1
buffer_load_dwordx2 v[18:19], v15, s[8:11], 0 offen offset:3072 sc1
s_waitcnt vmcnt(0)
s_add_u32 s84, s84, 1
s_cmp_lt_u32 s84, 1048576
s_cbranch_scc1 .LL_97%=
.LL_96%=:
v_bfe_u32 v3, v1, 4, 4
v_mul_u32_u24 v94, 20480, v3
v_lshrrev_b32 v95, 8, v1
v_lshl_add_u32 v94, v95, 5, v94
v_cmp_gt_u32 vcc, 6, v3
v_mov_b32 v95, 0x80000000
s_nop 0
v_cndmask_b32 v95, v95, v94, vcc
s_ff1_i32_b32 s69, s68
s_nop 3
v_readlane_b32 s70, v12, s69
s_nop 3
s_lshl_b32 s66, s70, 2
s_add_u32 s66, s66, 2752512
buffer_load_dword v3, off, s[8:11], s66 sc1
s_mul_i32 s66, s58, 640
s_mul_i32 s67, s69, 2560
s_add_u32 s66, s66, s67
s_lshl_b32 s67, s14, 7
s_add_u32 s66, s66, s67
s_add_u32 s66, s66, 2760704
buffer_load_dwordx4 a[0:3], v94, s[8:11], s66 offen offset:0 sc1
buffer_load_dwordx4 a[4:7], v94, s[8:11], s66 offen offset:16 sc1
s_add_u32 s66, s66, 327680
buffer_load_dwordx4 a[8:11], v94, s[8:11], s66 offen offset:0 sc1
buffer_load_dwordx4 a[12:15], v94, s[8:11], s66 offen offset:16 sc1
s_add_u32 s66, s66, 327680
buffer_load_dwordx4 a[16:19], v94, s[8:11], s66 offen offset:0 sc1
buffer_load_dwordx4 a[20:23], v94, s[8:11], s66 offen offset:16 sc1
s_add_u32 s66, s66, 327680
buffer_load_dwordx4 a[24:27], v95, s[8:11], s66 offen offset:0 sc1
buffer_load_dwordx4 a[28:31], v95, s[8:11], s66 offen offset:16 sc1
s_mul_i32 s66, s58, 640
s_mul_i32 s67, s69, 2560
s_add_u32 s66, s66, s67
s_add_u32 s66, s66, 2761216
s_cmp_lt_u32 s14, 2
s_cbranch_scc0 .LL_98%=
buffer_load_dwordx4 a[32:35], v94, s[8:11], s66 offen offset:0 sc1
buffer_load_dwordx4 a[36:39], v94, s[8:11], s66 offen offset:16 sc1
s_add_u32 s66, s66, 327680
buffer_load_dwordx4 a[40:43], v94, s[8:11], s66 offen offset:0 sc1
buffer_load_dwordx4 a[44:47], v94, s[8:11], s66 offen offset:16 sc1
s_add_u32 s66, s66, 327680
buffer_load_dwordx4 a[48:51], v94, s[8:11], s66 offen offset:0 sc1
buffer_load_dwordx4 a[52:55], v94, s[8:11], s66 offen offset:16 sc1
s_add_u32 s66, s66, 327680
buffer_load_dwordx4 a[56:59], v95, s[8:11], s66 offen offset:0 sc1
buffer_load_dwordx4 a[60:63], v95, s[8:11], s66 offen offset:16 sc1
.LL_98%=:
s_branch .Lupr_92%=
.Lpd_99%=:
v_mov_b64 v[28:29], s[12:13]
s_mov_b64 exec, 1
buffer_store_dwordx2 v[28:29], off, s[8:11], s69 sc1
s_mov_b64 exec, -1
s_branch .Lpdr_100%=
.Lspin_101%=:
s_mov_b32 s84, 0
.Llp_103%=:
s_sleep 2
v_bfe_u32 v28, v1, 4, 2
v_lshlrev_b32 v28, 3, v28
buffer_load_dwordx2 v[28:29], v28, s[8:11], s54 offen sc1
s_waitcnt vmcnt(0)
s_add_u32 s84, s84, 1
v_cmp_eq_u64 vcc, s[12:13], v[28:29]
s_cmp_eq_u64 vcc, -1
s_cbranch_scc1 .Lok_102%=
s_cmp_lt_u32 s84, 1048576
s_cbranch_scc1 .Llp_103%=
s_branch .Lok_102%=
.Lspin_104%=:
s_mov_b32 s84, 0
.Llp_106%=:
s_sleep 2
v_bfe_u32 v28, v1, 4, 2
v_lshlrev_b32 v28, 3, v28
buffer_load_dwordx2 v[28:29], v28, s[8:11], s55 offen sc1
s_waitcnt vmcnt(0)
s_add_u32 s84, s84, 1
v_cmp_eq_u64 vcc, s[12:13], v[28:29]
s_cmp_eq_u64 vcc, -1
s_cbranch_scc1 .Lok_105%=
s_cmp_lt_u32 s84, 1048576
s_cbranch_scc1 .Llp_106%=
s_branch .Lok_105%=
.LL_109%=:
s_mov_b32 s84, 0
.LL_111%=:
s_sleep 2
buffer_load_dwordx2 v[12:13], v27, s[8:11], 0 offen offset:3584 sc1
s_waitcnt vmcnt(0)
s_add_u32 s84, s84, 1
v_xor_b32 v12, s12, v12
v_xor_b32 v13, s13, v13
v_lshrrev_b32 v94, 11, v12
v_or_b32 v13, v13, v94
v_cmp_eq_u32 vcc, 0, v13
s_cmp_eq_u64 vcc, -1
s_cbranch_scc1 .LL_110%=
s_cmp_lt_u32 s84, 1048576
s_cbranch_scc1 .LL_111%=
s_branch .LL_110%=
.Lup_107%=:
s_mov_b32 s84, 0
.LL_113%=:
v_cmp_eq_u64 vcc, s[12:13], v[16:17]
s_mov_b64 s[86:87], vcc
v_cmp_eq_u64 vcc, s[12:13], v[18:19]
s_and_b64 s[86:87], s[86:87], vcc
s_cmp_eq_u64 s[86:87], -1
s_cbranch_scc1 .LL_112%=
s_sleep 2
buffer_load_dwordx2 v[16:17], v14, s[8:11], 0 offen offset:2560 sc1
buffer_load_dwordx2 v[18:19], v15, s[8:11], 0 offen offset:3072 sc1
s_waitcnt vmcnt(0)
s_add_u32 s84, s84, 1
s_cmp_lt_u32 s84, 1048576
s_cbranch_scc1 .LL_113%=
.LL_112%=:
v_bfe_u32 v3, v1, 4, 4
v_mul_u32_u24 v94, 20480, v3
v_lshrrev_b32 v95, 8, v1
v_lshl_add_u32 v94, v95, 5, v94
v_cmp_gt_u32 vcc, 6, v3
v_mov_b32 v95, 0x80000000
s_nop 0
v_cndmask_b32 v95, v95, v94, vcc
s_ff1_i32_b32 s69, s68
s_nop 3
v_readlane_b32 s70, v12, s69
s_nop 3
s_lshl_b32 s66, s70, 2
s_add_u32 s66, s66, 2752512
buffer_load_dword v3, off, s[8:11], s66 sc1
s_mul_i32 s66, s58, 640
s_mul_i32 s67, s69, 2560
s_add_u32 s66, s66, s67
s_lshl_b32 s67, s14, 7
s_add_u32 s66, s66, s67
s_add_u32 s66, s66, 2760704
buffer_load_dwordx4 a[0:3], v94, s[8:11], s66 offen offset:0 sc1
buffer_load_dwordx4 a[4:7], v94, s[8:11], s66 offen offset:16 sc1
s_add_u32 s66, s66, 327680
buffer_load_dwordx4 a[8:11], v94, s[8:11], s66 offen offset:0 sc1
buffer_load_dwordx4 a[12:15], v94, s[8:11], s66 offen offset:16 sc1
s_add_u32 s66, s66, 327680
buffer_load_dwordx4 a[16:19], v94, s[8:11], s66 offen offset:0 sc1
buffer_load_dwordx4 a[20:23], v94, s[8:11], s66 offen offset:16 sc1
s_add_u32 s66, s66, 327680
buffer_load_dwordx4 a[24:27], v95, s[8:11], s66 offen offset:0 sc1
buffer_load_dwordx4 a[28:31], v95, s[8:11], s66 offen offset:16 sc1
s_mul_i32 s66, s58, 640
s_mul_i32 s67, s69, 2560
s_add_u32 s66, s66, s67
s_add_u32 s66, s66, 2761216
s_cmp_lt_u32 s14, 2
s_cbranch_scc0 .LL_114%=
buffer_load_dwordx4 a[32:35], v94, s[8:11], s66 offen offset:0 sc1
buffer_load_dwordx4 a[36:39], v94, s[8:11], s66 offen offset:16 sc1
s_add_u32 s66, s66, 327680
buffer_load_dwordx4 a[40:43], v94, s[8:11], s66 offen offset:0 sc1
buffer_load_dwordx4 a[44:47], v94, s[8:11], s66 offen offset:16 sc1
s_add_u32 s66, s66, 327680
buffer_load_dwordx4 a[48:51], v94, s[8:11], s66 offen offset:0 sc1
buffer_load_dwordx4 a[52:55], v94, s[8:11], s66 offen offset:16 sc1
s_add_u32 s66, s66, 327680
buffer_load_dwordx4 a[56:59], v95, s[8:11], s66 offen offset:0 sc1
buffer_load_dwordx4 a[60:63], v95, s[8:11], s66 offen offset:16 sc1
.LL_114%=:
s_branch .Lupr_108%=
.Lspin_131%=:
s_mov_b32 s84, 0
.Llp_133%=:
s_sleep 1
buffer_load_dwordx2 v[28:29], v27, s[8:11], s63 offen offset:512 sc1
s_waitcnt vmcnt(0)
s_add_u32 s84, s84, 1
v_cmp_eq_u64 vcc, s[12:13], v[28:29]
s_cmp_eq_u64 vcc, -1
s_cbranch_scc1 .Lok_132%=
s_cmp_lt_u32 s84, 1048576
s_cbranch_scc1 .Llp_133%=
s_branch .Lok_132%=
.Lspin_138%=:
s_mov_b32 s84, 0
.Llp_140%=:
s_sleep 2
buffer_load_dwordx2 v[28:29], v27, s[8:11], s63 offen offset:0 sc1
s_waitcnt vmcnt(0)
s_add_u32 s84, s84, 1
v_cmp_eq_u64 vcc, s[12:13], v[28:29]
s_cmp_eq_u64 vcc, -1
s_cbranch_scc1 .Lok_139%=
s_cmp_lt_u32 s84, 1048576
s_cbranch_scc1 .Llp_140%=
s_branch .Lok_139%=
.Lend_143%=:
.Lend%=:
.purgem W
.purgem D
.purgem Ra
.purgem Ta
.purgem Zvaa
.purgem Zaaa
.purgem A
.purgem U
.purgem Mvaa
.purgem Maaa
.purgem K
.purgem Rv
.purgem Mvav
.purgem G
.purgem Dc
.purgem Cvaa
.purgem X
.purgem Zvav
.purgem Maav
.purgem Mvvv
.purgem Zvvv
)"
    : : [ka] "s"(__builtin_amdgcn_kernarg_segment_ptr()), [bid] "s"(blockIdx.x), [tid] "v"(threadIdx.x)
    : "v1", "v2", "v3", "v4", "v5", "v6", "v7", "v8", "v9", "v10", "v11", "v12", "v13", "v14", "v15", "v16", "v17", "v18", "v19", "v20", "v21", "v22", "v23", "v24", "v25", "v26", "v27", "v28", "v29", "v30", "v31", "v32", "v33", "v34", "v35", "v36", "v37", "v38", "v39", "v40", "v41", "v42", "v43", "v44", "v45", "v46", "v47", "v48", "v49", "v50", "v51", "v52", "v53", "v54", "v55", "v56", "v57", "v58", "v59", "v60", "v61", "v62", "v63", "v64", "v65", "v66", "v67", "v68", "v69", "v70", "v71", "v72", "v73", "v74", "v75", "v76", "v77", "v78", "v79", "v80", "v81", "v82", "v83", "v84", "v85", "v86", "v87", "v88", "v89", "v90", "v91", "v92", "v93", "v94", "v95", "v96", "v97", "v98", "v99", "v100", "v101", "v102", "v103", "v104", "v105", "v106", "v107", "v108", "v109", "v110", "v111", "v112", "v113", "v114", "v115", "v116", "v117", "v118", "v119", "v120", "v121", "v122", "v123", "v124", "v125", "v126", "v127", "v128", "v129", "v130", "v131", "v132", "v133", "v134", "v135", "v136", "v137", "v138", "v139", "v140", "v141", "v142", "v143", "v144", "v145", "v146", "v147", "v148", "v149", "v150", "v151", "v152", "v153", "v154", "v155", "v156", "v157", "v158", "v159", "v160", "v161", "v162", "v163", "v164", "v165", "v166", "v167", "v168", "v169", "v170", "v171", "v172", "v173", "v174", "v175", "v176", "v177", "v178", "v179", "v180", "v181", "v182", "v183", "v184", "v185", "v186", "v187", "v188", "v189", "v190", "v191", "v192", "v193", "v194", "v195", "v196", "v197", "v198", "v199", "v200", "v201", "v202", "v203", "v204", "v205", "v206", "v207", "v208", "v209", "v210", "v211", "v212", "v213", "v214", "v215", "v216", "v217", "v218", "v219", "v220", "v221", "v222", "v223", "v224", "v225", "v226", "v227", "v228", "v229", "v230", "v231", "v232", "v233", "v234", "v235", "v236", "v237", "v238", "v239", "v240", "v241", "v242", "v243", "v244", "v245", "v246", "v247", "v248", "v249", "v250", "v251", "v252", "v253", "v254", "v255", "a0", "a1", "a2", "a3", "a4", "a5", "a6", "a7", "a8", "a9", "a10", "a11", "a12", "a13", "a14", "a15", "a16", "a17", "a18", "a19", "a20", "a21", "a22", "a23", "a24", "a25", "a26", "a27", "a28", "a29", "a30", "a31", "a32", "a33", "a34", "a35", "a36", "a37", "a38", "a39", "a40", "a41", "a42", "a43", "a44", "a45", "a46", "a47", "a48", "a49", "a50", "a51", "a52", "a53", "a54", "a55", "a56", "a57", "a58", "a59", "a60", "a61", "a62", "a63", "a64", "a65", "a66", "a67", "a68", "a69", "a70", "a71", "a72", "a73", "a74", "a75", "a76", "a77", "a78", "a79", "a80", "a81", "a82", "a83", "a84", "a85", "a86", "a87", "a88", "a89", "a90", "a91", "a92", "a93", "a94", "a95", "a96", "a97", "a98", "a99", "a100", "a101", "a102", "a103", "a104", "a105", "a106", "a107", "a108", "a109", "a110", "a111", "a112", "a113", "a114", "a115", "a116", "a117", "a118", "a119", "a120", "a121", "a122", "a123", "a124", "a125", "a126", "a127", "a128", "a129", "a130", "a131", "a132", "a133", "a134", "a135", "a136", "a137", "a138", "a139", "a140", "a141", "a142", "a143", "a144", "a145", "a146", "a147", "a148", "a149", "a150", "a151", "a152", "a153", "a154", "a155", "a156", "a157", "a158", "a159", "a160", "a161", "a162", "a163", "a164", "a165", "a166", "a167", "a168", "a169", "a170", "a171", "a172", "a173", "a174", "a175", "a176", "a177", "a178", "a179", "a180", "a181", "a182", "a183", "a184", "a185", "a186", "a187", "a188", "a189", "a190", "a191", "a192", "a193", "a194", "a195", "a196", "a197", "a198", "a199", "a200", "a201", "a202", "a203", "a204", "a205", "a206", "a207", "a208", "a209", "a210", "a211", "a212", "a213", "a214", "a215", "a216", "a217", "a218", "a219", "a220", "a221", "a222", "a223", "a224", "a225", "a226", "a227", "a228", "a229", "a230", "a231", "a232", "a233", "a234", "a235", "a236", "a237", "a238", "a239", "a240", "a241", "a242", "a243", "a244", "a245", "a246", "a247", "a248", "a249", "a250", "a251", "a252", "a253", "a254", "a255", "s4", "s5", "s6", "s7", "s8", "s9", "s10", "s11", "s12", "s13", "s14", "s15", "s16", "s17", "s18", "s19", "s20", "s21", "s22", "s23", "s24", "s25", "s26", "s27", "s28", "s29", "s30", "s31", "s32", "s33", "s34", "s35", "s36", "s37", "s38", "s39", "s40", "s41", "s42", "s43", "s44", "s45", "s46", "s47", "s48", "s49", "s50", "s51", "s52", "s53", "s54", "s55", "s56", "s57", "s58", "s59", "s60", "s61", "s62", "s63", "s64", "s65", "s66", "s67", "s68", "s69", "s70", "s71", "s72", "s73", "s74", "s75", "s76", "s77", "s78", "s79", "s80", "s81", "s82", "s83", "s84", "s85", "s86", "s87", "s88", "s89", "s90", "s91", "s92", "s93", "s94", "s95", "s96", "s97", "s98", "s99", "vcc", "scc", "memory"
  );
}
extern "C" void kernel_launch(void* const* d_in, const int* in_sizes, int n_in, void* d_out, int out_size, void* d_ws, size_t ws_size, hipStream_t stream) {
  (void)hipFuncSetAttribute((const void*)mega_kernel, hipFuncAttributeMaxDynamicSharedMemorySize, LDS_BYTES);
  mega_kernel<<<256, 256, LDS_BYTES, stream>>>((const bf16*)d_in[0], (const bf16*)d_in[1], (const bf16*)d_in[2], (const bf16*)d_in[3], (const bf16*)d_in[4], (const bf16*)d_in[5],
      (const bf16*)d_in[7], (const bf16*)d_in[8], (const bf16*)d_in[9], (const bf16*)d_in[10], (char*)d_ws + 0, (bf16*)d_out);
}
```

```cpp
#include <hip/hip_runtime.h>
#include <hip/hip_bf16.h>
typedef __hip_bfloat16 bf16;
constexpr int LDS_BYTES = 163840;
__global__ __launch_bounds__(256, 1) void mega_kernel(const bf16* x, const bf16* ckv, const bf16* wq, const bf16* wuk, const bf16* wuv, const bf16* wo,
    const bf16* w_r, const bf16* wg, const bf16* wu, const bf16* wd, char* ws, bf16* out) {
  asm volatile(
R"(.macro W n
s_waitcnt lgkmcnt(\n)
.endm
.macro D v,r,s,o
buffer_load_dwordx4 v\v, s[\r:\r+3], s\s offen offset:\o lds
.endm
.macro Ra d,a,o
ds_read_b128 a[\d:\d+3], v\a offset:\o
.endm
.macro Ta d,a,o
ds_read_b64_tr_b16 a[\d:\d+1], v\a offset:\o
.endm
.macro Zvaa d,a,b
v_mfma_f32_16x16x32_bf16 v[\d:\d+3], a[\a:\a+3], a[\b:\b+3], 0
.endm
.macro Zaaa d,a,b
v_mfma_f32_16x16x32_bf16 a[\d:\d+3], a[\a:\a+3], a[\b:\b+3], 0
.endm
.macro A d,a,b
v_add_f32 v\d, v\a, v\b
.endm
.macro U d,a,b
v_mul_f32 v\d, v\a, v\b
.endm
.macro Mvaa d,a,b
v_mfma_f32_16x16x32_bf16 v[\d:\d+3], a[\a:\a+3], a[\b:\b+3], v[\d:\d+3]
.endm
.macro Maaa d,a,b
v_mfma_f32_16x16x32_bf16 a[\d:\d+3], a[\a:\a+3], a[\b:\b+3], a[\d:\d+3]
.endm
.macro K d,a,b
v_cvt_pk_bf16_f32 v\d, v\a, v\b
.endm
.macro Rv d,a,o
ds_read_b128 v[\d:\d+3], v\a offset:\o
.endm
.macro Mvav d,a,b
v_mfma_f32_16x16x32_bf16 v[\d:\d+3], a[\a:\a+3], v[\b:\b+3], v[\d:\d+3]
.endm
.macro G d,a
v_accvgpr_read_b32 v\d, a\a
.endm
.macro Dc v,r,s,o
buffer_load_dwordx4 v\v, s[\r:\r+3], s\s offen offset:\o sc1 lds
.endm
.macro Zvav d,a,b
v_mfma_f32_16x16x32_bf16 v[\d:\d+3], a[\a:\a+3], v[\b:\b+3], 0
.endm
.macro Cvav d,a,b,c
v_mfma_f32_16x16x32_bf16 v[\d:\d+3], a[\a:\a+3], v[\b:\b+3], v[\c:\c+3]
.endm
.macro X d
v_exp_f32 v\d, v\d
.endm
.macro Cvaa d,a,b,c
v_mfma_f32_16x16x32_bf16 v[\d:\d+3], a[\a:\a+3], a[\b:\b+3], v[\c:\c+3]
.endm
.macro Maav d,a,b
v_mfma_f32_16x16x32_bf16 a[\d:\d+3], a[\a:\a+3], v[\b:\b+3], a[\d:\d+3]
.endm
.macro Mvvv d,a,b
v_mfma_f32_16x16x32_bf16 v[\d:\d+3], v[\a:\a+3], v[\b:\b+3], v[\d:\d+3]
.endm
.macro Zvvv d,a,b
v_mfma_f32_16x16x32_bf16 v[\d:\d+3], v[\a:\a+3], v[\b:\b+3], 0
.endm
s_load_dwordx16 s[16:31], %[ka], 0
s_load_dwordx8 s[32:39], %[ka], 64
s_mov_b32 s15, %[bid]
v_lshrrev_b32 v2, 6, %[tid]
v_and_b32 v100, 63, %[tid]
v_readfirstlane_b32 s14, v2
v_lshlrev_b32 v1, 4, v100
v_lshrrev_b32 v101, 4, v100
v_and_b32 v102, 15, v100
s_and_b32 s59, s15, 7
s_bfe_u32 s57, s15, 0x30003
s_lshr_b32 s58, s15, 6
s_lshl_b32 s56, s59, 2
s_add_u32 s56, s56, s58
v_and_b32 v88, 0x1f0, v1
v_lshrrev_b32 v105, 1, v101
v_mul_u32_u24 v105, 1280, v105
v_add_u32 v28, v88, v105
s_lshl_b32 s85, s15, 2
s_add_u32 s85, s85, s14
s_mul_i32 s84, s85, 2560
v_and_b32 v105, 1, v101
v_lshl_or_b32 v29, v105, 31, v28
v_lshlrev_b32 v104, 1, v101
v_xor_b32 v104, v104, v102
v_lshlrev_b32 v104, 4, v104
v_lshl_add_u32 v16, v101, 13, v104
v_xor_b32 v105, 128, v16
v_add_u32 v17, 31744, v16
v_add_u32 v18, 63488, v105
v_add_u32 v19, 95232, v105
v_add_u32 v20, 131072, v16
v_add_u32 v21, 162816, v16
v_add_u32 v22, 194560, v105
v_add_u32 v23, 226304, v105
s_lshl_b32 s80, s14, 3
s_add_u32 s80, s80, s57
s_lshl_b32 s80, s80, 15
v_and_b32 v104, 7, v100
v_xor_b32 v104, v104, v101
v_lshlrev_b32 v104, 4, v104
v_lshrrev_b32 v105, 3, v100
v_lshl_add_u32 v104, v105, 10, v104
v_add_u32 v2, s80, v104
v_xor_b32 v105, 64, v2
v_add_u32 v3, 7168, v105
v_add_u32 v4, 14336, v2
v_add_u32 v5, 21504, v105
v_xor_b32 v6, 1835008, v2
v_xor_b32 v7, 1835008, v3
v_xor_b32 v8, 1835008, v4
v_xor_b32 v9, 1835008, v5
s_lshl_b32 s63, s14, 13
s_add_u32 s61, s63, 65536
s_and_b32 s80, s14, 1
s_lshr_b32 s81, s14, 1
s_lshl_b32 s62, s80, 14
s_lshl_b32 s82, s81, 13
s_add_u32 s62, s62, s82
s_lshl_b32 s81, s81, 18
s_lshl_b32 s83, s56, 8
s_add_u32 s81, s81, s83
s_mov_b32 s64, 0
s_cmp_eq_u32 s80, 0
W 0
s_cselect_b32 s44, s22, s24
s_cselect_b32 s45, s23, s25
s_mov_b32 s69, 0
s_cmp_eq_u32 s14, 2
s_cbranch_scc1 .LL_1%=
s_cmp_eq_u32 s14, 3
s_cbranch_scc1 .LL_2%=
s_lshl_b32 s80, s58, 3
s_add_u32 s80, s80, s57
s_lshl_b32 s80, s80, 15
s_lshl_b32 s82, s14, 7
s_add_u32 s80, s80, s82
s_add_u32 s80, s80, 256
v_lshl_add_u32 v104, v88, 6, s80
v_and_b32 v105, 512, v1
v_mul_u32_u24 v105, 3584, v105
v_xor_b32 v104, v104, v105
v_mov_b32 v140, s18
v_mov_b32 v141, s19
s_mov_b32 s68, 256
s_branch .LL_3%=
.LL_1%=:
v_lshrrev_b32 v104, 6, v1
v_lshlrev_b32 v104, 13, v104
v_and_b32 v105, 16, v1
v_lshl_add_u32 v104, v105, 3, v104
s_lshl_b32 s80, s57, 17
s_add_u32 s80, s80, s83
s_add_u32 s80, s80, 1048576
v_add_u32 v104, s80, v104
v_and_b32 v105, 32, v1
v_cmp_eq_u32 vcc, 0, v105
v_mov_b32 v140, s24
v_mov_b32 v141, s25
v_mov_b32 v105, s22
v_mov_b32 v106, s23
v_cndmask_b32 v140, v140, v105, vcc
v_cndmask_b32 v141, v141, v106, vcc
s_mov_b32 s68, 1048576
s_branch .LL_3%=
.LL_2%=:
v_lshrrev_b32 v104, 5, v1
v_lshlrev_b32 v104, 13, v104
v_and_b32 v105, 16, v1
v_lshl_add_u32 v104, v105, 3, v104
s_mul_i32 s80, s57, 655360
s_add_u32 s80, s80, s83
v_add_u32 v104, s80, v104
v_mov_b32 v140, s20
v_mov_b32 v141, s21
s_mov_b32 s68, 196608
.LL_3%=:
v_mov_b32 v105, 0
v_lshl_add_u64 v[140:141], v[104:105], 0, v[140:141]
s_mov_b32 s40, s18
s_and_b32 s41, s19, 0xffff
s_mov_b32 s42, 0x200000
s_mov_b32 s43, 0x20000
s_add_u32 s44, s44, s81
s_addc_u32 s45, s45, 0
s_and_b32 s45, s45, 0xffff
s_mov_b32 s46, 0x400000
s_mov_b32 s47, 0x20000
s_sub_u32 s46, s46, s81
s_load_dwordx2 s[12:13], s[36:37], 0
s_add_u32 m0, s61, 0
s_nop 0
D 2,40,64,0
D 3,40,64,1024
D 4,40,64,2048
D 5,40,64,3072
s_add_u32 m0, s61, 4096
s_nop 0
D 6,40,64,0
D 7,40,64,1024
D 8,40,64,2048
D 9,40,64,3072
s_lshl_b32 s65, s64, 12
s_add_u32 m0, s62, 0
s_nop 0
D 16,44,65,0
D 17,44,65,1024
D 18,44,65,2048
D 19,44,65,3072
s_add_u32 m0, s62, 4096
s_nop 0
D 20,44,65,0
D 21,44,65,1024
D 22,44,65,2048
D 23,44,65,3072
s_mov_b32 s52, s16
s_and_b32 s53, s17, 0xffff
s_mov_b32 s54, 2621440
s_mov_b32 s55, 0x20000
buffer_load_dwordx4 v[32:35], v28, s[52:55], s84 offen
buffer_load_dwordx4 v[36:39], v28, s[52:55], s84 offen offset:512
buffer_load_dwordx4 v[40:43], v29, s[52:55], s84 offen offset:1024
s_mov_b32 s52, s28
s_and_b32 s53, s29, 0xffff
s_mov_b32 s54, 1280
s_mov_b32 s55, 0x20000
buffer_load_dwordx4 v[44:47], v88, s[52:55], 0 offen offset:0
buffer_load_dwordx4 v[48:51], v88, s[52:55], 0 offen offset:512
buffer_load_dwordx4 v[52:55], v88, s[52:55], 0 offen offset:1024
s_getpc_b64 s[6:7]
.Lbeg%=:
s_lshr_b32 s80, s15, 3
s_lshl_b32 s80, s80, 2
s_add_u32 s80, s80, s14
s_lshl_b32 s80, s80, 11
v_min_u32 v104, 240, v1
v_lshl_add_u32 v104, v104, 3, s80
v_min_u32 v104, .Lend%=-.Lbeg%=-4, v104
global_load_dword v142, v104, s[6:7]
s_add_u32 s64, s64, 128
s_and_b32 s64, s64, 1023
s_add_u32 m0, s61, 32768
s_nop 0
D 2,40,64,0
D 3,40,64,1024
D 4,40,64,2048
D 5,40,64,3072
s_add_u32 m0, s61, 36864
s_nop 0
D 6,40,64,0
D 7,40,64,1024
D 8,40,64,2048
D 9,40,64,3072
s_lshl_b32 s65, s64, 12
s_add_u32 m0, s62, 32768
s_nop 0
D 16,44,65,0
D 17,44,65,1024
D 18,44,65,2048
D 19,44,65,3072
s_add_u32 m0, s62, 36864
s_nop 0
D 20,44,65,0
D 21,44,65,1024
D 22,44,65,2048
D 23,44,65,3072
global_load_dword v142, v[140:141], off
v_lshl_add_u64 v[140:141], v[140:141], 0, s[68:69]
s_cmp_eq_u32 s14, 3
s_cbranch_scc0 .LL_4%=
s_lshr_b32 s80, s15, 3
s_cmp_lt_u32 s80, 8
s_cbranch_scc0 .LL_4%=
v_lshlrev_b32 v104, 16, v1
s_cmp_eq_u32 s80, 0
s_cselect_b64 s[86:87], s[26:27], s[86:87]
s_cselect_b32 s81, 5242876, s81
s_cmp_eq_u32 s80, 1
s_cselect_b64 s[86:87], s[30:31], s[86:87]
s_cselect_b32 s81, 2211836, s81
s_cmp_eq_u32 s80, 2
s_cselect_b64 s[86:87], s[32:33], s[86:87]
s_cselect_b32 s81, 2211836, s81
s_cmp_eq_u32 s80, 3
s_cselect_b64 s[86:87], s[34:35], s[86:87]
s_cselect_b32 s81, 2211836, s81
s_cmp_eq_u32 s80, 4
s_cselect_b64 s[86:87], s[38:39], s[86:87]
s_cselect_b32 s81, 2621436, s81
s_cmp_eq_u32 s80, 5
s_cselect_b64 s[86:87], s[20:21], s[86:87]
s_cselect_b32 s81, 5242876, s81
s_cmp_lt_u32 s80, 6
s_cbranch_scc1 .LL_5%=
s_mov_b64 s[86:87], s[36:37]
s_mov_b32 s81, 39841792
v_mul_u32_u24 v104, 33536, v1
v_add_u32 v104, 4729856, v104
s_cmp_eq_u32 s80, 6
s_cbranch_scc1 .LL_5%=
v_min_u32 v104, 16, v1
v_mul_u32_u24 v104, 261888, v104
v_add_u32 v104, 2048, v104
.LL_5%=:
v_min_u32 v104, s81, v104
global_load_dword v142, v104, s[86:87]
.LL_4%=:
v_lshrrev_b32 v104, 1, v102
v_xor_b32 v104, v104, v101
v_lshlrev_b32 v104, 4, v104
v_lshl_add_u32 v104, v102, 7, v104
s_lshr_b32 s81, s14, 1
s_lshl_b32 s81, s81, 14
s_add_u32 s81, s81, 65536
v_add_u32 v26, s81, v104
v_xor_b32 v27, 64, v26
v_and_b32 v105, 1, v101
v_lshrrev_b32 v104, 2, v102
v_lshl_or_b32 v105, v105, 2, v104
v_lshl_add_u32 v104, v101, 3, v104
v_lshlrev_b32 v104, 8, v104
v_lshl_add_u32 v104, v105, 5, v104
v_and_b32 v105, 3, v102
v_lshl_add_u32 v10, v105, 3, v104
v_xor_b32 v11, 32, v10
v_xor_b32 v12, 64, v10
v_xor_b32 v13, 96, v10
v_xor_b32 v14, 128, v10
v_xor_b32 v15, 160, v10
v_xor_b32 v24, 192, v10
v_xor_b32 v25, 224, v10
.LL_9%=:
s_lshr_b32 s81, s15, 3
s_cmp_eq_u32 s14, 2
s_cbranch_scc1 .LL_6%=
s_cmp_eq_u32 s14, 3
s_cbranch_scc1 .LL_7%=
s_cmp_eq_u32 s14, 0
s_cselect_b64 s[76:77], s[30:31], s[32:33]
s_mov_b32 s72, 3456
s_mov_b32 s73, 28
s_mul_i32 s74, s81, 69120
s_mul_i32 s82, s59, 448
s_andn2_b32 s82, s82, 127
s_add_u32 s74, s74, s82
s_mov_b32 s75, 2211836
s_branch .LL_8%=
.LL_6%=:
s_mov_b64 s[76:77], s[34:35]
s_mov_b32 s72, 512
s_mov_b32 s73, 24
s_mul_i32 s74, s81, 8960
s_mul_i32 s82, s59, 286720
s_add_u32 s74, s74, s82
s_mov_b32 s75, 2211836
s_branch .LL_8%=
.LL_7%=:
s_mov_b64 s[76:77], s[26:27]
s_mov_b32 s72, 512
s_mov_b32 s73, 4
s_mul_i32 s74, s81, 20480
s_mul_i32 s82, s59, 655360
s_add_u32 s74, s74, s82
s_mov_b32 s75, 5242876
s_branch .LL_8%=
.LL_8%=:
s_mov_b32 s8, s36
s_and_b32 s9, s37, 0xffff
s_mov_b32 s10, 79691776
s_mov_b32 s11, 0x20000
s_add_u32 s48, s20, s83
s_addc_u32 s49, s21, 0
s_and_b32 s49, s49, 0xffff
s_mov_b32 s50, 5242880
s_mov_b32 s51, 0x20000
s_sub_u32 s50, s50, s83
s_lshl_b32 s80, s57, 1
s_and_b32 s81, s14, 1
s_add_u32 s80, s80, s81
s_sub_u32 s81, 31, s80
s_cmp_lt_u32 s14, 2
s_cselect_b32 s60, s80, s81
s_mul_i32 s28, s60, 81920
s_add_u32 s28, s28, 131072
s_add_u32 s4, s28, 10240
s_mov_b32 s80, 0
s_lshl_b32 s81, s80, 9
s_add_u32 s28, s28, s81
s_add_u32 s5, s80, s14
s_cmp_ge_u32 s5, 20
s_cselect_b32 s81, 20, 0
s_sub_u32 s5, s5, s81
s_and_b32 s80, s14, 1
s_mul_i32 s29, s80, 17172480
s_mul_i32 s80, s56, 536576
s_add_u32 s29, s29, s80
s_lshl_b32 s80, s57, 13
s_add_u32 s29, s29, s80
s_add_u32 s29, s29, 4194304
W 0
s_add_u32 s12, s12, 0x7f4a7c15
s_addc_u32 s13, s13, 0x9e3779b9
s_bitcmp1_b32 s14, 0
s_cbranch_scc1 .Lv_10%=
s_waitcnt vmcnt(24)
s_barrier
Ra 32,26,0
Ta 0,10,0
Ta 2,10,1024
Ra 36,26,2048
Ra 40,26,4096
Ra 44,26,6144
Ra 48,26,8192
Ra 52,26,10240
Ra 56,26,12288
Ra 60,26,14336
Ta 4,11,0
Ta 6,11,1024
W 6
Zvaa 160,0,32
Ta 8,12,0
Ta 10,12,1024
Zvaa 192,0,36
Ta 12,13,0
Ta 14,13,1024
Zvaa 224,0,40
s_waitcnt vmcnt(21)
Zaaa 160,0,44
W 6
v_lshlrev_b32 v56, 16, v32
Zaaa 192,0,48
v_and_b32 v57, 0xffff0000, v32
v_lshlrev_b32 v58, 16, v33
Zaaa 224,0,52
v_and_b32 v59, 0xffff0000, v33
v_lshlrev_b32 v60, 16, v34
Zaaa 64,0,56
v_and_b32 v61, 0xffff0000, v34
)"
R"(v_lshlrev_b32 v62, 16, v35
Zaaa 96,0,60
W 4
v_and_b32 v63, 0xffff0000, v35
v_lshlrev_b32 v64, 16, v36
Zaaa 100,4,60
Ta 16,14,0
v_and_b32 v65, 0xffff0000, v36
Zaaa 68,4,56
Ta 18,14,1024
v_lshlrev_b32 v66, 16, v37
Zaaa 228,4,52
v_and_b32 v67, 0xffff0000, v37
v_lshlrev_b32 v68, 16, v38
Zaaa 196,4,48
v_and_b32 v69, 0xffff0000, v38
v_lshlrev_b32 v70, 16, v39
Zaaa 164,4,44
v_and_b32 v71, 0xffff0000, v39
v_lshlrev_b32 v72, 16, v40
Zvaa 228,4,40
v_and_b32 v73, 0xffff0000, v40
v_lshlrev_b32 v74, 16, v41
Zvaa 196,4,36
v_and_b32 v75, 0xffff0000, v41
v_lshlrev_b32 v76, 16, v42
Zvaa 164,4,32
W 4
v_and_b32 v77, 0xffff0000, v42
v_lshlrev_b32 v78, 16, v43
Zvaa 168,8,32
v_and_b32 v79, 0xffff0000, v43
A 80,56,57
Zvaa 200,8,36
Ta 20,15,0
Ta 22,15,1024
Zvaa 232,8,40
A 81,58,59
A 80,80,60
Zaaa 168,8,44
A 81,81,61
A 80,80,62
Zaaa 200,8,48
A 81,81,63
A 80,80,64
Zaaa 232,8,52
A 81,81,65
A 80,80,66
Zaaa 72,8,56
A 81,81,67
A 80,80,68
Zaaa 104,8,60
W 4
A 81,81,69
A 80,80,70
Zaaa 108,12,60
A 81,81,71
A 80,80,72
Zaaa 76,12,56
Ta 24,24,0
A 81,81,73
Zaaa 236,12,52
Ta 26,24,1024
A 80,80,74
Zaaa 204,12,48
A 81,81,75
A 80,80,76
Zaaa 172,12,44
A 81,81,77
A 80,80,78
Zvaa 236,12,40
A 81,81,79
A 80,80,81
Zvaa 204,12,36
Ra 128,27,0
Zvaa 172,12,32
W 5
Ra 132,27,2048
v_add_f32_dpp v80, v80, v80 quad_perm:[1,0,3,2] row_mask:0xf bank_mask:0xf
Zvaa 176,16,32
Ta 28,25,0
Ta 30,25,1024
Zvaa 208,16,36
v_add_f32_dpp v80, v80, v80 quad_perm:[2,3,0,1] row_mask:0xf bank_mask:0xf
Zvaa 240,16,40
Ra 136,27,4096
Zaaa 176,16,44
v_add_f32_dpp v80, v80, v80 row_half_mirror row_mask:0xf bank_mask:0xf
Zaaa 208,16,48
Zaaa 240,16,52
v_add_f32_dpp v80, v80, v80 row_mirror row_mask:0xf bank_mask:0xf
v_mov_b32 v81, v80
Zaaa 80,16,56
Ra 140,27,6144
Zaaa 112,16,60
W 8
v_permlane16_swap_b32 v80, v81
A 80,80,81
Zaaa 116,20,60
Ta 0,10,8192
v_mul_f32 v84, 0x3acccccd, v80
Zaaa 84,20,56
s_mov_b32 s86, 0xffff
s_mov_b32 s87, 0xffff
v_sub_f32 v82, v56, v84
Zaaa 244,20,52
U 80,82,82
v_sub_f32 v83, v57, v84
Zaaa 212,20,48
v_fmac_f32 v80, v83, v83
v_sub_f32 v82, v58, v84
Zaaa 180,20,44
v_fmac_f32 v80, v82, v82
v_sub_f32 v83, v59, v84
Zvaa 244,20,40
v_fmac_f32 v80, v83, v83
v_sub_f32 v82, v60, v84
Zvaa 212,20,36
v_fmac_f32 v80, v82, v82
v_sub_f32 v83, v61, v84
Zvaa 180,20,32
W 7
Ta 2,10,9216
v_fmac_f32 v80, v83, v83
Zvaa 184,24,32
Ra 144,27,8192
v_sub_f32 v82, v62, v84
Zvaa 216,24,36
v_fmac_f32 v80, v82, v82
v_sub_f32 v83, v63, v84
Zvaa 248,24,40
v_fmac_f32 v80, v83, v83
v_sub_f32 v82, v64, v84
Zaaa 184,24,44
Ra 148,27,10240
v_fmac_f32 v80, v82, v82
Zaaa 216,24,48
v_sub_f32 v83, v65, v84
v_fmac_f32 v80, v83, v83
Zaaa 248,24,52
v_sub_f32 v82, v66, v84
v_fmac_f32 v80, v82, v82
Zaaa 88,24,56
Ra 152,27,12288
v_sub_f32 v83, v67, v84
Zaaa 120,24,60
W 7
v_fmac_f32 v80, v83, v83
v_sub_f32 v82, v68, v84
Zaaa 124,28,60
v_fmac_f32 v80, v82, v82
v_sub_f32 v83, v69, v84
Zaaa 92,28,56
Ta 4,11,8192
Ta 6,11,9216
Zaaa 252,28,52
v_fmac_f32 v80, v83, v83
v_sub_f32 v82, v70, v84
Zaaa 220,28,48
Ra 156,27,14336
v_fmac_f32 v80, v82, v82
Zaaa 188,28,44
v_sub_f32 v83, v71, v84
v_fmac_f32 v80, v83, v83
Zvaa 252,28,40
v_sub_f32 v82, v72, v84
U 81,82,82
Zvaa 220,28,36
v_sub_f32 v83, v73, v84
v_fmac_f32 v81, v83, v83
Zvaa 188,28,32
W 6
v_sub_f32 v82, v74, v84
v_fmac_f32 v81, v82, v82
Mvaa 160,0,128
v_sub_f32 v83, v75, v84
v_fmac_f32 v81, v83, v83
Mvaa 192,0,132
Ta 8,12,8192
v_sub_f32 v82, v76, v84
Mvaa 224,0,136
Ta 10,12,9216
v_fmac_f32 v81, v82, v82
Maaa 160,0,140
W 2
v_sub_f32 v83, v77, v84
v_fmac_f32 v81, v83, v83
Maaa 192,0,144
v_sub_f32 v82, v78, v84
v_fmac_f32 v81, v82, v82
Maaa 224,0,148
v_sub_f32 v83, v79, v84
v_fmac_f32 v81, v83, v83
Maaa 64,0,152
v_cndmask_b32_e64 v81, 0, v81, s[86:87]
A 80,80,81
Maaa 96,0,156
Ta 12,13,8192
Ta 14,13,9216
Maaa 100,4,156
Ta 16,14,8192
v_add_f32_dpp v80, v80, v80 quad_perm:[1,0,3,2] row_mask:0xf bank_mask:0xf
Maaa 68,4,152
Ta 18,14,9216
Maaa 228,4,148
v_add_f32_dpp v80, v80, v80 quad_perm:[2,3,0,1] row_mask:0xf bank_mask:0xf
Maaa 196,4,144
Maaa 164,4,140
Ta 20,15,8192
v_add_f32_dpp v80, v80, v80 row_half_mirror row_mask:0xf bank_mask:0xf
Mvaa 228,4,136
Ta 22,15,9216
Mvaa 196,4,132
v_add_f32_dpp v80, v80, v80 row_mirror row_mask:0xf bank_mask:0xf
v_mov_b32 v81, v80
Mvaa 164,4,128
W 6
v_permlane16_swap_b32 v80, v81
A 80,80,81
Mvaa 168,8,128
v_mul_f32 v80, 0x3acccccd, v80
K 80,80,80
Mvaa 200,8,132
v_lshlrev_b32 v80, 16, v80
v_add_f32 v80, 0x3727c5ac, v80
Mvaa 232,8,136
K 80,80,80
v_lshlrev_b32 v80, 16, v80
Maaa 168,8,140
v_sqrt_f32 v81, v80
Maaa 200,8,144
v_rcp_f32 v82, v81
Maaa 232,8,148
v_fma_f32 v83, -v81, v81, v80
v_mul_f32 v82, 0.5, v82
Maaa 72,8,152
v_fma_f32 v81, v83, v82, v81
K 86,81,81
Maaa 104,8,156
W 4
Ta 24,24,8192
v_lshlrev_b32 v86, 16, v86
Maaa 108,12,156
v_rcp_f32 v87, v86
Maaa 76,12,152
Ta 26,24,9216
Ta 28,25,8192
Ta 30,25,9216
Maaa 236,12,148
s_waitcnt vmcnt(1) lgkmcnt(0)
s_barrier
K 85,84,84
Maaa 204,12,144
s_add_u32 s64, s64, 128
v_lshlrev_b32 v85, 16, v85
Maaa 172,12,140
v_sub_f32 v56, v56, v85
v_sub_f32 v57, v57, v85
Mvaa 236,12,136
s_and_b32 s64, s64, 1023
K 80,56,57
v_lshlrev_b32 v56, 16, v80
Mvaa 204,12,132
v_and_b32 v57, 0xffff0000, v80
U 80,56,87
Mvaa 172,12,128
U 81,57,87
v_fma_f32 v82, -v80, v86, v56
Mvaa 176,16,128
s_add_u32 m0, s61, 0
v_fma_f32 v83, -v81, v86, v57
v_fmac_f32 v80, v82, v87
Mvaa 208,16,132
v_fmac_f32 v81, v83, v87
K 32,80,81
Mvaa 240,16,136
v_lshlrev_b32 v80, 16, v32
v_and_b32 v81, 0xffff0000, v32
Maaa 176,16,140
v_lshlrev_b32 v82, 16, v44
v_and_b32 v83, 0xffff0000, v44
Maaa 208,16,144
U 84,80,82
v_fmac_f32 v84, v81, v83
Maaa 240,16,148
v_sub_f32 v58, v58, v85
v_sub_f32 v59, v59, v85
Maaa 80,16,152
K 80,58,59
v_lshlrev_b32 v58, 16, v80
Maaa 112,16,156
v_and_b32 v59, 0xffff0000, v80
U 80,58,87
Maaa 116,20,156
U 81,59,87
v_fma_f32 v82, -v80, v86, v58
Maaa 84,20,152
v_fma_f32 v83, -v81, v86, v59
v_fmac_f32 v80, v82, v87
Maaa 244,20,148
v_fmac_f32 v81, v83, v87
K 33,80,81
Maaa 212,20,144
Ra 32,26,32768
v_lshlrev_b32 v80, 16, v33
Maaa 180,20,140
v_and_b32 v81, 0xffff0000, v33
v_lshlrev_b32 v82, 16, v45
Mvaa 244,20,136
v_and_b32 v83, 0xffff0000, v45
v_fmac_f32 v84, v80, v82
Mvaa 212,20,132
Ra 36,26,34816
v_fmac_f32 v84, v81, v83
Mvaa 180,20,128
v_sub_f32 v60, v60, v85
v_sub_f32 v61, v61, v85
Mvaa 184,24,128
K 80,60,61
v_lshlrev_b32 v60, 16, v80
Mvaa 216,24,132
D 2,40,64,0
v_and_b32 v61, 0xffff0000, v80
Mvaa 248,24,136
Ra 40,26,36864
U 80,60,87
Maaa 184,24,140
U 81,61,87
v_fma_f32 v82, -v80, v86, v60
Maaa 216,24,144
v_fma_f32 v83, -v81, v86, v61
v_fmac_f32 v80, v82, v87
Maaa 248,24,148
D 3,40,64,1024
v_fmac_f32 v81, v83, v87
Maaa 88,24,152
Ra 44,26,38912
K 34,80,81
Maaa 120,24,156
v_lshlrev_b32 v80, 16, v34
v_and_b32 v81, 0xffff0000, v34
Maaa 124,28,156
v_lshlrev_b32 v82, 16, v46
v_and_b32 v83, 0xffff0000, v46
Maaa 92,28,152
D 4,40,64,2048
Ta 0,10,32768
Maaa 252,28,148
Ta 2,10,33792
Ra 48,26,40960
Maaa 220,28,144
v_fmac_f32 v84, v80, v82
v_fmac_f32 v84, v81, v83
Maaa 188,28,140
v_sub_f32 v62, v62, v85
v_sub_f32 v63, v63, v85
Mvaa 252,28,136
D 5,40,64,3072
K 80,62,63
Mvaa 220,28,132
Ra 52,26,43008
Ra 56,26,45056
Ta 4,11,32768
Ta 6,11,33792
Ra 60,26,47104
Mvaa 188,28,128
W 6
v_lshlrev_b32 v62, 16, v80
v_and_b32 v63, 0xffff0000, v80
Mvaa 160,0,32
s_add_u32 m0, s61, 4096
U 80,62,87
U 81,63,87
Mvaa 192,0,36
v_fma_f32 v82, -v80, v86, v62
v_fma_f32 v83, -v81, v86, v63
Mvaa 224,0,40
D 6,40,64,0
v_fmac_f32 v80, v82, v87
Maaa 160,0,44
W 0
v_fmac_f32 v81, v83, v87
K 35,80,81
Maaa 192,0,48
v_lshlrev_b32 v80, 16, v35
v_and_b32 v81, 0xffff0000, v35
Maaa 224,0,52
v_lshlrev_b32 v82, 16, v47
v_and_b32 v83, 0xffff0000, v47
Maaa 64,0,56
D 7,40,64,1024
v_fmac_f32 v84, v80, v82
Maaa 96,0,60
v_fmac_f32 v84, v81, v83
v_sub_f32 v64, v64, v85
Maaa 100,4,60
v_sub_f32 v65, v65, v85
K 80,64,65
Maaa 68,4,56
v_lshlrev_b32 v64, 16, v80
)"
R"(v_and_b32 v65, 0xffff0000, v80
Maaa 228,4,52
D 8,40,64,2048
U 80,64,87
Maaa 196,4,48
U 81,65,87
v_fma_f32 v82, -v80, v86, v64
Maaa 164,4,44
Ta 8,12,32768
Ta 10,12,33792
Mvaa 228,4,40
v_fma_f32 v83, -v81, v86, v65
v_fmac_f32 v80, v82, v87
Mvaa 196,4,36
D 9,40,64,3072
v_fmac_f32 v81, v83, v87
Mvaa 164,4,32
W 0
s_lshl_b32 s65, s64, 12
K 36,80,81
Mvaa 168,8,32
s_add_u32 m0, s62, 0
v_lshlrev_b32 v80, 16, v36
v_and_b32 v81, 0xffff0000, v36
Mvaa 200,8,36
v_lshlrev_b32 v82, 16, v48
v_and_b32 v83, 0xffff0000, v48
Mvaa 232,8,40
v_fmac_f32 v84, v80, v82
v_fmac_f32 v84, v81, v83
Maaa 168,8,44
v_sub_f32 v66, v66, v85
v_sub_f32 v67, v67, v85
Maaa 200,8,48
Ta 12,13,32768
Ta 14,13,33792
Maaa 232,8,52
D 16,44,65,0
K 80,66,67
Maaa 72,8,56
v_lshlrev_b32 v66, 16, v80
v_and_b32 v67, 0xffff0000, v80
Maaa 104,8,60
W 0
U 80,66,87
U 81,67,87
Maaa 108,12,60
v_fma_f32 v82, -v80, v86, v66
v_fma_f32 v83, -v81, v86, v67
Maaa 76,12,56
D 17,44,65,1024
v_fmac_f32 v80, v82, v87
Maaa 236,12,52
v_fmac_f32 v81, v83, v87
K 37,80,81
Maaa 204,12,48
v_lshlrev_b32 v80, 16, v37
v_and_b32 v81, 0xffff0000, v37
Maaa 172,12,44
Ta 16,14,32768
Ta 18,14,33792
Mvaa 236,12,40
D 18,44,65,2048
v_lshlrev_b32 v82, 16, v49
Mvaa 204,12,36
v_and_b32 v83, 0xffff0000, v49
v_fmac_f32 v84, v80, v82
Mvaa 172,12,32
W 0
v_fmac_f32 v84, v81, v83
v_sub_f32 v68, v68, v85
Mvaa 176,16,32
v_sub_f32 v69, v69, v85
K 80,68,69
Mvaa 208,16,36
D 19,44,65,3072
v_lshlrev_b32 v68, 16, v80
Mvaa 240,16,40
s_add_u32 m0, s62, 4096
v_and_b32 v69, 0xffff0000, v80
U 80,68,87
Maaa 176,16,44
U 81,69,87
v_fma_f32 v82, -v80, v86, v68
Maaa 208,16,48
Ta 20,15,32768
Ta 22,15,33792
Maaa 240,16,52
D 20,44,65,0
v_fma_f32 v83, -v81, v86, v69
Maaa 80,16,56
v_fmac_f32 v80, v82, v87
v_fmac_f32 v81, v83, v87
Maaa 112,16,60
W 0
K 38,80,81
v_lshlrev_b32 v80, 16, v38
Maaa 116,20,60
v_and_b32 v81, 0xffff0000, v38
v_lshlrev_b32 v82, 16, v50
Maaa 84,20,56
D 21,44,65,1024
v_and_b32 v83, 0xffff0000, v50
Maaa 244,20,52
v_fmac_f32 v84, v80, v82
v_fmac_f32 v84, v81, v83
Maaa 212,20,48
v_sub_f32 v70, v70, v85
v_sub_f32 v71, v71, v85
Maaa 180,20,44
Ta 24,24,32768
Ta 26,24,33792
Mvaa 244,20,40
D 22,44,65,2048
K 80,70,71
Mvaa 212,20,36
v_lshlrev_b32 v70, 16, v80
v_and_b32 v71, 0xffff0000, v80
Mvaa 180,20,32
W 0
U 80,70,87
U 81,71,87
Mvaa 184,24,32
v_fma_f32 v82, -v80, v86, v70
v_fma_f32 v83, -v81, v86, v71
Mvaa 216,24,36
D 23,44,65,3072
v_fmac_f32 v80, v82, v87
Mvaa 248,24,40
v_fmac_f32 v81, v83, v87
K 39,80,81
Maaa 184,24,44
v_lshlrev_b32 v80, 16, v39
v_and_b32 v81, 0xffff0000, v39
Maaa 216,24,48
Ra 128,27,32768
Ra 132,27,34816
Ta 28,25,32768
Ta 30,25,33792
Maaa 248,24,52
global_load_dword v142, v[140:141], off
v_lshl_add_u64 v[140:141], v[140:141], 0, s[68:69]
v_lshlrev_b32 v82, 16, v51
Maaa 88,24,56
v_and_b32 v83, 0xffff0000, v51
v_fmac_f32 v84, v80, v82
Maaa 120,24,60
W 0
v_fmac_f32 v84, v81, v83
v_sub_f32 v72, v72, v85
Maaa 124,28,60
v_sub_f32 v73, v73, v85
K 80,72,73
Maaa 92,28,56
v_lshlrev_b32 v72, 16, v80
v_and_b32 v73, 0xffff0000, v80
Maaa 252,28,52
Ra 136,27,36864
Ra 140,27,38912
Maaa 220,28,48
U 80,72,87
U 81,73,87
Maaa 188,28,44
Ta 0,10,40960
Ta 2,10,41984
Mvaa 252,28,40
v_fma_f32 v82, -v80, v86, v72
v_fma_f32 v83, -v81, v86, v73
Mvaa 220,28,36
Ra 144,27,40960
Ra 148,27,43008
Ra 152,27,45056
Ta 4,11,40960
Ta 6,11,41984
Ra 156,27,47104
Mvaa 188,28,32
W 6
v_fmac_f32 v80, v82, v87
v_fmac_f32 v81, v83, v87
Mvaa 160,0,128
K 40,80,81
v_lshlrev_b32 v80, 16, v40
Mvaa 192,0,132
v_and_b32 v81, 0xffff0000, v40
v_lshlrev_b32 v82, 16, v52
Mvaa 224,0,136
v_and_b32 v83, 0xffff0000, v52
v_fmac_f32 v84, v80, v82
Maaa 160,0,140
W 0
v_fmac_f32 v84, v81, v83
v_sub_f32 v74, v74, v85
Maaa 192,0,144
v_sub_f32 v75, v75, v85
K 80,74,75
Maaa 224,0,148
v_lshlrev_b32 v74, 16, v80
v_and_b32 v75, 0xffff0000, v80
Maaa 64,0,152
U 80,74,87
U 81,75,87
Maaa 96,0,156
v_fma_f32 v82, -v80, v86, v74
v_fma_f32 v83, -v81, v86, v75
Maaa 100,4,156
v_fmac_f32 v80, v82, v87
v_fmac_f32 v81, v83, v87
Maaa 68,4,152
K 41,80,81
v_lshlrev_b32 v80, 16, v41
Maaa 228,4,148
v_and_b32 v81, 0xffff0000, v41
v_lshlrev_b32 v82, 16, v53
Maaa 196,4,144
v_and_b32 v83, 0xffff0000, v53
v_fmac_f32 v84, v80, v82
Maaa 164,4,140
Ta 8,12,40960
Ta 10,12,41984
Mvaa 228,4,136
v_fmac_f32 v84, v81, v83
v_sub_f32 v76, v76, v85
Mvaa 196,4,132
v_sub_f32 v77, v77, v85
K 80,76,77
Mvaa 164,4,128
W 0
v_lshlrev_b32 v76, 16, v80
v_and_b32 v77, 0xffff0000, v80
Mvaa 168,8,128
U 80,76,87
U 81,77,87
Mvaa 200,8,132
v_fma_f32 v82, -v80, v86, v76
v_fma_f32 v83, -v81, v86, v77
Mvaa 232,8,136
v_fmac_f32 v80, v82, v87
v_fmac_f32 v81, v83, v87
Maaa 168,8,140
K 42,80,81
v_lshlrev_b32 v80, 16, v42
Maaa 200,8,144
Ta 12,13,40960
Ta 14,13,41984
Maaa 232,8,148
v_and_b32 v81, 0xffff0000, v42
v_lshlrev_b32 v82, 16, v54
Maaa 72,8,152
v_and_b32 v83, 0xffff0000, v54
v_fmac_f32 v84, v80, v82
Maaa 104,8,156
W 0
v_fmac_f32 v84, v81, v83
v_sub_f32 v78, v78, v85
Maaa 108,12,156
v_sub_f32 v79, v79, v85
K 80,78,79
Maaa 76,12,152
Ta 16,14,40960
Ta 18,14,41984
Ta 20,15,40960
Ta 22,15,41984
Ta 24,24,40960
Ta 26,24,41984
Ta 28,25,40960
Ta 30,25,41984
v_lshlrev_b32 v78, 16, v80
v_and_b32 v79, 0xffff0000, v80
U 80,78,87
U 81,79,87
v_fma_f32 v82, -v80, v86, v78
v_fma_f32 v83, -v81, v86, v79
v_fmac_f32 v80, v82, v87
v_fmac_f32 v81, v83, v87
K 43,80,81
v_lshlrev_b32 v80, 16, v43
v_and_b32 v81, 0xffff0000, v43
v_lshlrev_b32 v82, 16, v55
v_and_b32 v83, 0xffff0000, v55
v_fmac_f32 v84, v80, v82
v_fmac_f32 v84, v81, v83
v_lshrrev_b32 v82, 9, v1
s_lshl_b32 s80, s14, 1
v_add_u32 v82, s80, v82
v_mul_u32_u24 v83, 1552, v82
v_add_u32 v83, v83, v88
v_add_u32 v83, 131072, v83
ds_write_b128 v83, v[32:35] offset:0
ds_write_b128 v83, v[36:39] offset:512
ds_write_b128 v83, v[40:43] offset:1024
v_add_f32_dpp v84, v84, v84 quad_perm:[1,0,3,2] row_mask:0xf bank_mask:0xf
s_nop 1
v_add_f32_dpp v84, v84, v84 quad_perm:[2,3,0,1] row_mask:0xf bank_mask:0xf
s_nop 1
v_add_f32_dpp v84, v84, v84 row_half_mirror row_mask:0xf bank_mask:0xf
s_nop 1
v_add_f32_dpp v84, v84, v84 row_mirror row_mask:0xf bank_mask:0xf
v_mov_b32 v81, v84
s_nop 1
v_permlane16_swap_b32 v84, v81
A 84,84,81
K 80,84,84
v_lshlrev_b32 v80, 16, v80
s_lshl_b32 s80, s15, 3
v_add_u32 v82, s80, v82
v_sub_u32 v83, 2047, v82
v_ashrrev_i32 v81, 31, v80
v_or_b32 v81, 0x80000000, v81
v_xor_b32 v80, v80, v81
v_and_b32 v80, 0xffff0000, v80
v_or_b32 v80, v80, v83
v_and_b32 v82, 7, v82
v_lshlrev_b32 v82, 2, v82
v_add_u32 v82, 143488, v82
ds_write_b32 v82, v80
Maaa 236,12,148
s_waitcnt vmcnt(1) lgkmcnt(0)
s_barrier
s_add_u32 s64, s64, 128
Maaa 204,12,144
v_bfe_u32 v80, v1, 4, 3
Maaa 172,12,140
Ra 32,26,0
v_mul_u32_u24 v81, 1552, v80
Mvaa 236,12,136
s_and_b32 s64, s64, 1023
v_lshrrev_b32 v82, 7, v1
Mvaa 204,12,132
v_lshl_add_u32 v81, v82, 4, v81
Mvaa 172,12,128
s_lshl_b32 s80, s14, 7
Ra 36,26,2048
Mvaa 176,16,128
s_add_u32 m0, s61, 32768
s_add_u32 s80, s80, 131072
Mvaa 208,16,132
v_add_u32 v81, s80, v81
Mvaa 240,16,136
ds_read_b128 v[32:35], v81
Ra 40,26,4096
Maaa 176,16,140
D 2,40,64,0
Rv 36,81,512
Maaa 208,16,144
s_and_b32 s80, s14, 2
Maaa 240,16,148
s_lshl_b32 s80, s80, 7
Ra 44,26,6144
Maaa 80,16,152
v_subrev_u32 v81, s80, v81
Maaa 112,16,156
D 3,40,64,1024
Rv 40,81,1024
Maaa 116,20,156
Ta 0,10,0
v_lshlrev_b32 v80, 2, v80
Maaa 84,20,152
Ta 2,10,1024
Ra 48,26,8192
Maaa 244,20,148
v_add_u32 v80, 143488, v80
Maaa 212,20,144
D 4,40,64,2048
ds_read_b32 v84, v80
Maaa 180,20,140
Ra 52,26,10240
v_mov_b32 v85, s13
Mvaa 244,20,136
v_lshrrev_b32 v83, 1, v1
Mvaa 212,20,132
s_mul_i32 s82, s15, 10240
s_add_u32 s82, s82, 131072
Ra 56,26,12288
Mvaa 180,20,128
s_lshl_b32 s80, s14, 10
D 5,40,64,3072
Mvaa 184,24,128
s_add_u32 m0, s61, 36864
s_add_u32 s83, s82, s80
Ta 4,11,0
Mvaa 216,24,132
Ta 6,11,1024
Ra 60,26,14336
Mvaa 248,24,136
Maaa 184,24,140
D 6,40,64,0
Maaa 216,24,144
Maaa 248,24,148
Maaa 88,24,152
Maaa 120,24,156
W 5
buffer_store_dwordx4 v[32:35], v1, s[8:11], s83 offen sc1
s_add_u32 s83, s83, 4096
Maaa 124,28,156
Ta 8,12,0
Ta 10,12,1024
Maaa 92,28,152
Maaa 252,28,148
Maaa 220,28,144
D 7,40,64,1024
)"
R"(Maaa 188,28,140
Mvaa 252,28,136
Mvaa 220,28,132
Mvaa 188,28,128
buffer_store_dwordx4 v[36:39], v1, s[8:11], s83 offen sc1
Mvaa 160,0,32
s_and_b32 s80, s80, 1024
s_add_u32 s83, s82, s80
s_add_u32 s83, s83, 8192
Mvaa 192,0,36
Ta 12,13,0
Ta 14,13,1024
Mvaa 224,0,40
Maaa 160,0,44
W 4
D 8,40,64,2048
Maaa 192,0,48
Maaa 224,0,52
Maaa 64,0,56
Maaa 96,0,60
buffer_store_dwordx4 v[40:43], v1, s[8:11], s83 offen sc1
Maaa 100,4,60
Ta 16,14,0
Ta 18,14,1024
Maaa 68,4,56
Maaa 228,4,52
Maaa 196,4,48
D 9,40,64,3072
Maaa 164,4,44
s_lshl_b32 s65, s64, 12
s_add_u32 m0, s62, 32768
Mvaa 228,4,40
Mvaa 196,4,36
Mvaa 164,4,32
W 4
D 16,44,65,0
Mvaa 168,8,32
Ta 20,15,0
Ta 22,15,1024
Mvaa 200,8,36
Mvaa 232,8,40
Maaa 168,8,44
D 17,44,65,1024
Maaa 200,8,48
Maaa 232,8,52
Maaa 72,8,56
Maaa 104,8,60
W 4
D 18,44,65,2048
Maaa 108,12,60
Ta 24,24,0
Ta 26,24,1024
Maaa 76,12,56
Maaa 236,12,52
Maaa 204,12,48
D 19,44,65,3072
Maaa 172,12,44
s_add_u32 m0, s62, 36864
Ra 128,27,0
Mvaa 236,12,40
Mvaa 204,12,36
Mvaa 172,12,32
W 5
D 20,44,65,0
Ra 132,27,2048
Mvaa 176,16,32
Ta 28,25,0
Ta 30,25,1024
Mvaa 208,16,36
Mvaa 240,16,40
Ra 136,27,4096
Maaa 176,16,44
D 21,44,65,1024
Maaa 208,16,48
Maaa 240,16,52
Ra 140,27,6144
Maaa 80,16,56
Maaa 112,16,60
W 8
D 22,44,65,2048
Maaa 116,20,60
Ta 0,10,8192
Ta 2,10,9216
Maaa 84,20,56
Ra 144,27,8192
Maaa 244,20,52
Maaa 212,20,48
D 23,44,65,3072
Ra 148,27,10240
Maaa 180,20,44
Mvaa 244,20,40
Mvaa 212,20,36
Ra 152,27,12288
Mvaa 180,20,32
W 11
Mvaa 184,24,32
Ta 4,11,8192
Ta 6,11,9216
Mvaa 216,24,36
Ra 156,27,14336
Mvaa 248,24,40
Maaa 184,24,44
Maaa 216,24,48
Maaa 248,24,52
Maaa 88,24,56
Maaa 120,24,60
W 10
Maaa 124,28,60
Ta 8,12,8192
Ta 10,12,9216
Maaa 92,28,56
Maaa 252,28,52
Maaa 220,28,48
Maaa 188,28,44
Mvaa 252,28,40
Mvaa 220,28,36
Mvaa 188,28,32
W 8
Mvaa 160,0,128
Ta 12,13,8192
Ta 14,13,9216
Mvaa 192,0,132
Mvaa 224,0,136
Maaa 160,0,140
W 4
Maaa 192,0,144
Maaa 224,0,148
Maaa 64,0,152
Maaa 96,0,156
Maaa 100,4,156
Ta 16,14,8192
Ta 18,14,9216
Maaa 68,4,152
Maaa 228,4,148
Maaa 196,4,144
Maaa 164,4,140
Ta 20,15,8192
Ta 22,15,9216
Mvaa 228,4,136
Mvaa 196,4,132
Ta 24,24,8192
Ta 26,24,9216
Mvaa 164,4,128
W 8
Mvaa 168,8,128
Ta 28,25,8192
Ta 30,25,9216
Mvaa 200,8,132
Mvaa 232,8,136
Maaa 168,8,140
Maaa 200,8,144
Maaa 232,8,148
Maaa 72,8,152
Maaa 104,8,156
W 8
Maaa 108,12,156
Maaa 76,12,152
Maaa 236,12,148
s_waitcnt vmcnt(0) lgkmcnt(0)
s_barrier
s_cmp_eq_u32 s14, 0
s_cbranch_scc0 .Lpubr_13%=
s_lshl_b32 s80, s15, 6
s_add_u32 s80, s80, 40960
s_mov_b64 exec, 0xff
buffer_store_dwordx2 v[84:85], v83, s[8:11], s80 offen sc1
s_mov_b64 exec, -1
.Lpubr_13%=:
Maaa 204,12,144
s_add_u32 s64, s64, 128
Maaa 172,12,140
Ra 32,26,32768
Mvaa 236,12,136
s_and_b32 s64, s64, 1023
Mvaa 204,12,132
Mvaa 172,12,128
s_add_u32 m0, s61, 0
Ra 36,26,34816
Mvaa 176,16,128
Mvaa 208,16,132
D 2,40,64,0
Mvaa 240,16,136
Ra 40,26,36864
Maaa 176,16,140
Maaa 208,16,144
Maaa 240,16,148
D 3,40,64,1024
Ra 44,26,38912
Maaa 80,16,152
Maaa 112,16,156
Maaa 116,20,156
Ta 0,10,32768
Ta 2,10,33792
Maaa 84,20,152
D 4,40,64,2048
Ra 48,26,40960
Maaa 244,20,148
Maaa 212,20,144
Ra 52,26,43008
Maaa 180,20,140
Mvaa 244,20,136
D 5,40,64,3072
Mvaa 212,20,132
s_add_u32 m0, s61, 4096
Ra 56,26,45056
Mvaa 180,20,128
Mvaa 184,24,128
Ta 4,11,32768
Ta 6,11,33792
Mvaa 216,24,132
D 6,40,64,0
Ra 60,26,47104
Mvaa 248,24,136
Maaa 184,24,140
Maaa 216,24,144
Maaa 248,24,148
D 7,40,64,1024
Maaa 88,24,152
Maaa 120,24,156
Maaa 124,28,156
Ta 8,12,32768
Ta 10,12,33792
Maaa 92,28,152
D 8,40,64,2048
Maaa 252,28,148
Maaa 220,28,144
Maaa 188,28,140
Mvaa 252,28,136
D 9,40,64,3072
Mvaa 220,28,132
s_lshl_b32 s65, s64, 12
s_add_u32 m0, s62, 0
Mvaa 188,28,128
W 8
Mvaa 160,0,32
Ta 12,13,32768
Ta 14,13,33792
Mvaa 192,0,36
D 16,44,65,0
Mvaa 224,0,40
Maaa 160,0,44
W 4
Maaa 192,0,48
Maaa 224,0,52
D 17,44,65,1024
Maaa 64,0,56
Maaa 96,0,60
Maaa 100,4,60
Ta 16,14,32768
Ta 18,14,33792
Maaa 68,4,56
D 18,44,65,2048
Maaa 228,4,52
Maaa 196,4,48
Maaa 164,4,44
Mvaa 228,4,40
D 19,44,65,3072
Mvaa 196,4,36
s_add_u32 m0, s62, 4096
Mvaa 164,4,32
W 4
Mvaa 168,8,32
Ta 20,15,32768
Ta 22,15,33792
Mvaa 200,8,36
D 20,44,65,0
Mvaa 232,8,40
Maaa 168,8,44
Maaa 200,8,48
Maaa 232,8,52
D 21,44,65,1024
Maaa 72,8,56
Maaa 104,8,60
W 4
Maaa 108,12,60
Ta 24,24,32768
Ta 26,24,33792
Maaa 76,12,56
D 22,44,65,2048
Maaa 236,12,52
Maaa 204,12,48
Maaa 172,12,44
Ra 128,27,32768
Mvaa 236,12,40
D 23,44,65,3072
Mvaa 204,12,36
s_add_u32 s65, s5, 0
s_cmp_ge_u32 s65, 20
s_cselect_b32 s68, 20, 0
s_sub_u32 s65, s65, s68
Mvaa 172,12,32
W 5
s_lshl_b32 s65, s65, 18
s_add_u32 m0, s63, 131072
Mvaa 176,16,32
Ra 132,27,34816
Ta 28,25,32768
Mvaa 208,16,36
D 16,48,65,0
Ta 30,25,33792
Mvaa 240,16,40
Ra 136,27,36864
Maaa 176,16,44
Maaa 208,16,48
Maaa 240,16,52
D 17,48,65,1024
Ra 140,27,38912
Maaa 80,16,56
Maaa 112,16,60
W 8
Maaa 116,20,60
Ta 0,10,40960
Ta 2,10,41984
Maaa 84,20,56
D 18,48,65,2048
Ra 144,27,40960
Maaa 244,20,52
Maaa 212,20,48
Ra 148,27,43008
Maaa 180,20,44
Mvaa 244,20,40
D 19,48,65,3072
Mvaa 212,20,36
s_add_u32 m0, s63, 135168
Ra 152,27,45056
Mvaa 180,20,32
W 11
Mvaa 184,24,32
Ta 4,11,40960
Ta 6,11,41984
Mvaa 216,24,36
D 20,48,65,0
Ra 156,27,47104
Mvaa 248,24,40
Maaa 184,24,44
Maaa 216,24,48
Maaa 248,24,52
D 21,48,65,1024
Maaa 88,24,56
Maaa 120,24,60
W 10
Maaa 124,28,60
Ta 8,12,40960
Ta 10,12,41984
Maaa 92,28,56
D 22,48,65,2048
Maaa 252,28,52
Maaa 220,28,48
Maaa 188,28,44
Mvaa 252,28,40
D 23,48,65,3072
Mvaa 220,28,36
Mvaa 188,28,32
W 8
Mvaa 160,0,128
Ta 12,13,40960
Ta 14,13,41984
Mvaa 192,0,132
global_load_dword v142, v[140:141], off
v_lshl_add_u64 v[140:141], v[140:141], 0, s[68:69]
Mvaa 224,0,136
Maaa 160,0,140
W 4
Maaa 192,0,144
Maaa 224,0,148
Maaa 64,0,152
Maaa 96,0,156
Maaa 100,4,156
Ta 16,14,40960
Ta 18,14,41984
Maaa 68,4,152
Maaa 228,4,148
Maaa 196,4,144
Maaa 164,4,140
Ta 20,15,40960
Ta 22,15,41984
Mvaa 228,4,136
Mvaa 196,4,132
Ta 24,24,40960
Ta 26,24,41984
Mvaa 164,4,128
W 8
Mvaa 168,8,128
Ta 28,25,40960
Ta 30,25,41984
Mvaa 200,8,132
Mvaa 232,8,136
Maaa 168,8,140
Maaa 200,8,144
Maaa 232,8,148
Maaa 72,8,152
Maaa 104,8,156
W 8
Maaa 108,12,156
Maaa 76,12,152
Maaa 236,12,148
s_waitcnt vmcnt(1) lgkmcnt(0)
s_barrier
s_add_u32 s64, s64, 128
Maaa 204,12,144
)"
R"(Maaa 172,12,140
Ra 32,26,0
Mvaa 236,12,136
s_and_b32 s64, s64, 1023
Mvaa 204,12,132
Mvaa 172,12,128
Ra 36,26,2048
Mvaa 176,16,128
s_add_u32 m0, s61, 32768
Mvaa 208,16,132
Mvaa 240,16,136
Ra 40,26,4096
Maaa 176,16,140
D 2,40,64,0
Maaa 208,16,144
Maaa 240,16,148
Ra 44,26,6144
Maaa 80,16,152
Maaa 112,16,156
D 3,40,64,1024
Maaa 116,20,156
Ta 0,10,0
Ta 2,10,1024
Maaa 84,20,152
Ra 48,26,8192
Maaa 244,20,148
Maaa 212,20,144
D 4,40,64,2048
Ra 52,26,10240
Maaa 180,20,140
Mvaa 244,20,136
Mvaa 212,20,132
Ra 56,26,12288
Mvaa 180,20,128
D 5,40,64,3072
Mvaa 184,24,128
s_add_u32 m0, s61, 36864
Ta 4,11,0
Ta 6,11,1024
Mvaa 216,24,132
Ra 60,26,14336
Mvaa 248,24,136
v_lshrrev_b32 v28, 1, v1
Maaa 184,24,140
s_lshl_b32 s80, s60, 9
D 6,40,64,0
Maaa 216,24,144
s_add_u32 s80, s80, 40960
Maaa 248,24,148
Maaa 88,24,152
Maaa 120,24,156
D 7,40,64,1024
Maaa 124,28,156
Ta 8,12,0
Ta 10,12,1024
Maaa 92,28,152
Maaa 252,28,148
Maaa 220,28,144
buffer_load_dwordx2 v[28:29], v28, s[8:11], s80 offen sc1
Maaa 188,28,140
Mvaa 252,28,136
Mvaa 220,28,132
Mvaa 188,28,128
W 8
D 8,40,64,2048
Mvaa 160,0,32
Ta 12,13,0
Ta 14,13,1024
Mvaa 192,0,36
Mvaa 224,0,40
Maaa 160,0,44
W 4
D 9,40,64,3072
Maaa 192,0,48
s_lshl_b32 s65, s64, 12
s_add_u32 m0, s62, 32768
Maaa 224,0,52
Maaa 64,0,56
Maaa 96,0,60
D 16,44,65,0
Maaa 100,4,60
Ta 16,14,0
Ta 18,14,1024
Maaa 68,4,56
Maaa 228,4,52
Maaa 196,4,48
D 17,44,65,1024
Maaa 164,4,44
Mvaa 228,4,40
Mvaa 196,4,36
Mvaa 164,4,32
W 4
D 18,44,65,2048
Mvaa 168,8,32
Ta 20,15,0
Ta 22,15,1024
Mvaa 200,8,36
Mvaa 232,8,40
Maaa 168,8,44
D 19,44,65,3072
Maaa 200,8,48
s_add_u32 m0, s62, 36864
Maaa 232,8,52
Maaa 72,8,56
Maaa 104,8,60
W 4
D 20,44,65,0
Maaa 108,12,60
Ta 24,24,0
Ta 26,24,1024
Maaa 76,12,56
Maaa 236,12,52
Maaa 204,12,48
D 21,44,65,1024
Maaa 172,12,44
Ra 128,27,0
Mvaa 236,12,40
Mvaa 204,12,36
Mvaa 172,12,32
W 5
D 22,44,65,2048
Ra 132,27,2048
Mvaa 176,16,32
Ta 28,25,0
Ta 30,25,1024
Mvaa 208,16,36
Mvaa 240,16,40
Ra 136,27,4096
Maaa 176,16,44
D 23,44,65,3072
v_lshrrev_b32 v138, 4, v1
Maaa 208,16,48
s_sub_u32 s81, s73, 1
v_min_u32 v138, s81, v138
v_lshrrev_b32 v139, 2, v138
Maaa 240,16,52
Ra 140,27,6144
v_mul_u32_u24 v139, s72, v139
Maaa 80,16,56
v_and_b32 v138, 3, v138
Maaa 112,16,60
W 8
v_lshl_add_u32 v138, v138, 7, v139
Maaa 116,20,60
s_waitcnt vmcnt(10)
v_cmp_eq_u32 vcc, s13, v29
s_cmp_eq_u64 vcc, -1
s_cbranch_scc0 .Lspin_14%=
.Lok_15%=:
v_add_u32 v138, s74, v138
Maaa 84,20,56
s_lshr_b32 s73, s73, 2
Ta 0,10,8192
Ta 2,10,9216
Maaa 244,20,52
s_mul_i32 s73, s73, s72
Ra 144,27,8192
v_bfe_u32 v144, v1, 8, 1
Maaa 212,20,48
v_min_u32 v139, s75, v138
v_mul_u32_u24 v144, 10240, v144
Maaa 180,20,44
Ra 148,27,10240
v_and_b32 v145, 0xf0, v1
Mvaa 244,20,40
global_load_dword v142, v139, s[76:77]
v_lshl_add_u32 v144, v145, 3, v144
Mvaa 212,20,36
s_mul_i32 s69, s58, 20480
Ra 152,27,12288
v_add_u32 v138, s73, v138
Mvaa 180,20,32
W 11
s_add_u32 s69, s69, s28
Mvaa 184,24,32
Ta 4,11,8192
Ta 6,11,9216
Mvaa 216,24,36
buffer_load_dword v142, v144, s[8:11], s69 offen offset:0 sc1
Ra 156,27,14336
Mvaa 248,24,40
Maaa 184,24,44
Maaa 216,24,48
Maaa 248,24,52
Maaa 88,24,56
Maaa 120,24,60
W 10
Maaa 124,28,60
Ta 8,12,8192
Ta 10,12,9216
Maaa 92,28,56
Maaa 252,28,52
Maaa 220,28,48
Maaa 188,28,44
Mvaa 252,28,40
Mvaa 220,28,36
Mvaa 188,28,32
W 8
Mvaa 160,0,128
Ta 12,13,8192
Ta 14,13,9216
Mvaa 192,0,132
Mvaa 224,0,136
Maaa 160,0,140
W 4
Maaa 192,0,144
Maaa 224,0,148
Maaa 64,0,152
Maaa 96,0,156
Maaa 100,4,156
Ta 16,14,8192
Ta 18,14,9216
Maaa 68,4,152
Maaa 228,4,148
Maaa 196,4,144
Maaa 164,4,140
Ta 20,15,8192
Ta 22,15,9216
Mvaa 228,4,136
Mvaa 196,4,132
Ta 24,24,8192
Ta 26,24,9216
Mvaa 164,4,128
W 8
Mvaa 168,8,128
Ta 28,25,8192
Ta 30,25,9216
Mvaa 200,8,132
Mvaa 232,8,136
Maaa 168,8,140
Maaa 200,8,144
Maaa 232,8,148
Maaa 72,8,152
Maaa 104,8,156
W 8
Maaa 108,12,156
Maaa 76,12,152
Maaa 236,12,148
s_waitcnt vmcnt(2) lgkmcnt(0)
s_barrier
s_add_u32 s64, s64, 128
Maaa 204,12,144
Maaa 172,12,140
Ra 32,26,32768
Mvaa 236,12,136
s_and_b32 s64, s64, 1023
Mvaa 204,12,132
Mvaa 172,12,128
Ra 36,26,34816
Mvaa 176,16,128
s_add_u32 m0, s61, 0
Mvaa 208,16,132
Mvaa 240,16,136
Ra 40,26,36864
Maaa 176,16,140
D 2,40,64,0
Maaa 208,16,144
Maaa 240,16,148
Ra 44,26,38912
Maaa 80,16,152
Maaa 112,16,156
D 3,40,64,1024
Maaa 116,20,156
Ta 0,10,32768
Ta 2,10,33792
Maaa 84,20,152
Ra 48,26,40960
Maaa 244,20,148
Maaa 212,20,144
D 4,40,64,2048
Ra 52,26,43008
Maaa 180,20,140
Mvaa 244,20,136
Mvaa 212,20,132
Ra 56,26,45056
Mvaa 180,20,128
D 5,40,64,3072
Mvaa 184,24,128
s_add_u32 m0, s61, 4096
Ta 4,11,32768
Ta 6,11,33792
Mvaa 216,24,132
Ra 60,26,47104
Mvaa 248,24,136
Maaa 184,24,140
D 6,40,64,0
Maaa 216,24,144
Maaa 248,24,148
Maaa 88,24,152
Maaa 120,24,156
D 7,40,64,1024
Maaa 124,28,156
Ta 8,12,32768
Ta 10,12,33792
Maaa 92,28,152
Maaa 252,28,148
Maaa 220,28,144
D 8,40,64,2048
Maaa 188,28,140
Mvaa 252,28,136
Mvaa 220,28,132
Mvaa 188,28,128
W 8
D 9,40,64,3072
Mvaa 160,0,32
s_lshl_b32 s65, s64, 12
s_add_u32 m0, s62, 0
Ta 12,13,32768
Mvaa 192,0,36
Ta 14,13,33792
Mvaa 224,0,40
Maaa 160,0,44
W 4
D 16,44,65,0
Maaa 192,0,48
Maaa 224,0,52
Maaa 64,0,56
Maaa 96,0,60
D 17,44,65,1024
Maaa 100,4,60
Ta 16,14,32768
Ta 18,14,33792
Maaa 68,4,56
Maaa 228,4,52
Maaa 196,4,48
D 18,44,65,2048
Maaa 164,4,44
Mvaa 228,4,40
Mvaa 196,4,36
Mvaa 164,4,32
W 4
D 19,44,65,3072
Mvaa 168,8,32
s_add_u32 m0, s62, 4096
Ta 20,15,32768
Ta 22,15,33792
Mvaa 200,8,36
Mvaa 232,8,40
Maaa 168,8,44
D 20,44,65,0
Maaa 200,8,48
Maaa 232,8,52
Maaa 72,8,56
Maaa 104,8,60
W 4
D 21,44,65,1024
Maaa 108,12,60
Ta 24,24,32768
Ta 26,24,33792
Maaa 76,12,56
Maaa 236,12,52
Maaa 204,12,48
D 22,44,65,2048
Maaa 172,12,44
Ra 128,27,32768
Mvaa 236,12,40
Mvaa 204,12,36
Mvaa 172,12,32
W 5
D 23,44,65,3072
Ra 132,27,34816
Mvaa 176,16,32
Ta 28,25,32768
Ta 30,25,33792
Mvaa 208,16,36
v_min_u32 v139, s75, v138
Mvaa 240,16,40
Ra 136,27,36864
Maaa 176,16,44
buffer_load_dword v142, v144, s[8:11], s69 offen offset:2048 sc1
Maaa 208,16,48
Maaa 240,16,52
s_add_u32 s69, s69, 4096
Ra 140,27,38912
Maaa 80,16,56
Maaa 112,16,60
W 8
global_load_dword v142, v139, s[76:77]
Maaa 116,20,60
Ta 0,10,40960
Ta 2,10,41984
Maaa 84,20,56
Ra 144,27,40960
Maaa 244,20,52
v_add_u32 v138, s73, v138
Maaa 212,20,48
Ra 148,27,43008
Maaa 180,20,44
Mvaa 244,20,40
Mvaa 212,20,36
Ra 152,27,45056
Mvaa 180,20,32
W 11
Mvaa 184,24,32
Ta 4,11,40960
Ta 6,11,41984
Mvaa 216,24,36
Ra 156,27,47104
Mvaa 248,24,40
Maaa 184,24,44
)"
R"(Maaa 216,24,48
Maaa 248,24,52
Maaa 88,24,56
Maaa 120,24,60
W 10
Maaa 124,28,60
Ta 8,12,40960
Ta 10,12,41984
Maaa 92,28,56
Maaa 252,28,52
Maaa 220,28,48
Maaa 188,28,44
Mvaa 252,28,40
Mvaa 220,28,36
Mvaa 188,28,32
W 8
Mvaa 160,0,128
Ta 12,13,40960
Ta 14,13,41984
Mvaa 192,0,132
Mvaa 224,0,136
Maaa 160,0,140
W 4
Maaa 192,0,144
Maaa 224,0,148
Maaa 64,0,152
Maaa 96,0,156
Maaa 100,4,156
Ta 16,14,40960
Ta 18,14,41984
Maaa 68,4,152
Maaa 228,4,148
Maaa 196,4,144
Maaa 164,4,140
Ta 20,15,40960
Ta 22,15,41984
Mvaa 228,4,136
Mvaa 196,4,132
Ta 24,24,40960
Ta 26,24,41984
Mvaa 164,4,128
W 8
Mvaa 168,8,128
Ta 28,25,40960
Ta 30,25,41984
Mvaa 200,8,132
Mvaa 232,8,136
Maaa 168,8,140
Maaa 200,8,144
Maaa 232,8,148
Maaa 72,8,152
Maaa 104,8,156
W 8
Maaa 108,12,156
Maaa 76,12,152
Maaa 236,12,148
s_waitcnt vmcnt(2) lgkmcnt(0)
s_barrier
s_add_u32 s64, s64, 128
Maaa 204,12,144
Maaa 172,12,140
Ra 32,26,0
Mvaa 236,12,136
s_and_b32 s64, s64, 1023
Mvaa 204,12,132
Mvaa 172,12,128
Ra 36,26,2048
Mvaa 176,16,128
s_add_u32 m0, s61, 32768
Mvaa 208,16,132
Mvaa 240,16,136
Ra 40,26,4096
Maaa 176,16,140
D 2,40,64,0
Maaa 208,16,144
Maaa 240,16,148
Ra 44,26,6144
Maaa 80,16,152
Maaa 112,16,156
D 3,40,64,1024
Maaa 116,20,156
Ta 0,10,0
Ta 2,10,1024
Maaa 84,20,152
Ra 48,26,8192
Maaa 244,20,148
Maaa 212,20,144
D 4,40,64,2048
Ra 52,26,10240
Maaa 180,20,140
Mvaa 244,20,136
Mvaa 212,20,132
Ra 56,26,12288
Mvaa 180,20,128
D 5,40,64,3072
Mvaa 184,24,128
s_add_u32 m0, s61, 36864
Ta 4,11,0
Ta 6,11,1024
Mvaa 216,24,132
Ra 60,26,14336
Mvaa 248,24,136
Maaa 184,24,140
D 6,40,64,0
Maaa 216,24,144
Maaa 248,24,148
Maaa 88,24,152
Maaa 120,24,156
D 7,40,64,1024
Maaa 124,28,156
Ta 8,12,0
Ta 10,12,1024
Maaa 92,28,152
Maaa 252,28,148
Maaa 220,28,144
D 8,40,64,2048
Maaa 188,28,140
Mvaa 252,28,136
Mvaa 220,28,132
Mvaa 188,28,128
W 8
D 9,40,64,3072
Mvaa 160,0,32
s_lshl_b32 s65, s64, 12
s_add_u32 m0, s62, 32768
Ta 12,13,0
Mvaa 192,0,36
Ta 14,13,1024
Mvaa 224,0,40
Maaa 160,0,44
W 4
D 16,44,65,0
Maaa 192,0,48
Maaa 224,0,52
Maaa 64,0,56
Maaa 96,0,60
D 17,44,65,1024
Maaa 100,4,60
Ta 16,14,0
Ta 18,14,1024
Maaa 68,4,56
Maaa 228,4,52
Maaa 196,4,48
D 18,44,65,2048
Maaa 164,4,44
Mvaa 228,4,40
Mvaa 196,4,36
Mvaa 164,4,32
W 4
D 19,44,65,3072
Mvaa 168,8,32
s_add_u32 m0, s62, 36864
Ta 20,15,0
Ta 22,15,1024
Mvaa 200,8,36
Mvaa 232,8,40
Maaa 168,8,44
D 20,44,65,0
Maaa 200,8,48
Maaa 232,8,52
Maaa 72,8,56
Maaa 104,8,60
W 4
D 21,44,65,1024
Maaa 108,12,60
Ta 24,24,0
Ta 26,24,1024
Maaa 76,12,56
Maaa 236,12,52
Maaa 204,12,48
D 22,44,65,2048
Maaa 172,12,44
Ra 128,27,0
Mvaa 236,12,40
Mvaa 204,12,36
Mvaa 172,12,32
W 5
D 23,44,65,3072
v_add_u32 v2, 131072, v10
Mvaa 176,16,32
Ra 132,27,2048
Ta 28,25,0
Mvaa 208,16,36
Ta 30,25,1024
v_add_u32 v3, 131072, v11
Mvaa 240,16,40
Ra 136,27,4096
v_add_u32 v4, 131072, v12
Maaa 176,16,44
buffer_load_dword v142, v144, s[8:11], s69 offen offset:0 sc1
Maaa 208,16,48
v_add_u32 v5, 131072, v13
Maaa 240,16,52
Ra 140,27,6144
Maaa 80,16,56
v_add_u32 v6, 131072, v14
Maaa 112,16,60
W 8
Maaa 116,20,60
Ta 0,10,8192
Ta 2,10,9216
Maaa 84,20,56
Ra 144,27,8192
v_add_u32 v7, 131072, v15
Maaa 244,20,52
v_add_u32 v8, 131072, v24
Maaa 212,20,48
Ra 148,27,10240
Maaa 180,20,44
v_add_u32 v9, 131072, v25
Mvaa 244,20,40
Mvaa 212,20,36
Ra 152,27,12288
Mvaa 180,20,32
W 11
Mvaa 184,24,32
Ta 4,11,8192
Ta 6,11,9216
Mvaa 216,24,36
Ra 156,27,14336
Mvaa 248,24,40
Maaa 184,24,44
Maaa 216,24,48
Maaa 248,24,52
Maaa 88,24,56
Maaa 120,24,60
W 10
Maaa 124,28,60
Ta 8,12,8192
Ta 10,12,9216
Maaa 92,28,56
Maaa 252,28,52
Maaa 220,28,48
Maaa 188,28,44
Mvaa 252,28,40
Mvaa 220,28,36
Mvaa 188,28,32
W 8
Mvaa 160,0,128
Ta 12,13,8192
Ta 14,13,9216
Mvaa 192,0,132
Mvaa 224,0,136
Maaa 160,0,140
W 4
Maaa 192,0,144
Maaa 224,0,148
Maaa 64,0,152
Maaa 96,0,156
Maaa 100,4,156
Ta 16,14,8192
Ta 18,14,9216
Maaa 68,4,152
Maaa 228,4,148
Maaa 196,4,144
Maaa 164,4,140
Ta 20,15,8192
Ta 22,15,9216
Mvaa 228,4,136
Mvaa 196,4,132
Ta 24,24,8192
Ta 26,24,9216
Mvaa 164,4,128
W 8
Mvaa 168,8,128
Ta 28,25,8192
Ta 30,25,9216
Mvaa 200,8,132
Mvaa 232,8,136
Maaa 168,8,140
Maaa 200,8,144
Maaa 232,8,148
Maaa 72,8,152
Maaa 104,8,156
W 8
Maaa 108,12,156
Maaa 76,12,152
Maaa 236,12,148
s_waitcnt vmcnt(1) lgkmcnt(0)
s_barrier
s_add_u32 s65, s5, 4
Maaa 204,12,144
Maaa 172,12,140
Ra 32,26,32768
Mvaa 236,12,136
s_cmp_ge_u32 s65, 20
s_cselect_b32 s68, 20, 0
Mvaa 204,12,132
Mvaa 172,12,128
s_sub_u32 s65, s65, s68
Ra 36,26,34816
Mvaa 176,16,128
Mvaa 208,16,132
s_lshl_b32 s65, s65, 18
Mvaa 240,16,136
Ra 40,26,36864
Maaa 176,16,140
Maaa 208,16,144
s_add_u32 m0, s63, 0
Maaa 240,16,148
Ra 44,26,38912
Maaa 80,16,152
D 16,48,65,0
Maaa 112,16,156
Maaa 116,20,156
Ta 0,10,32768
Ta 2,10,33792
Maaa 84,20,152
Ra 48,26,40960
Maaa 244,20,148
D 17,48,65,1024
Maaa 212,20,144
Ra 52,26,43008
Maaa 180,20,140
Mvaa 244,20,136
Mvaa 212,20,132
D 18,48,65,2048
Ra 56,26,45056
Mvaa 180,20,128
Mvaa 184,24,128
Ta 4,11,32768
Ta 6,11,33792
Mvaa 216,24,132
Ra 60,26,47104
Mvaa 248,24,136
D 19,48,65,3072
Maaa 184,24,140
s_add_u32 m0, s63, 4096
Maaa 216,24,144
Maaa 248,24,148
Maaa 88,24,152
D 20,48,65,0
Maaa 120,24,156
Maaa 124,28,156
Ta 8,12,32768
Ta 10,12,33792
Maaa 92,28,152
Maaa 252,28,148
D 21,48,65,1024
Maaa 220,28,144
Maaa 188,28,140
Mvaa 252,28,136
Mvaa 220,28,132
D 22,48,65,2048
Mvaa 188,28,128
W 8
Mvaa 160,0,32
Ta 12,13,32768
Ta 14,13,33792
Mvaa 192,0,36
Mvaa 224,0,40
D 23,48,65,3072
Maaa 160,0,44
W 4
s_add_u32 s65, s5, 8
s_cmp_ge_u32 s65, 20
s_cselect_b32 s68, 20, 0
Maaa 192,0,48
s_sub_u32 s65, s65, s68
s_lshl_b32 s65, s65, 18
s_add_u32 m0, s63, 65536
Maaa 224,0,52
Maaa 64,0,56
D 16,48,65,0
Maaa 96,0,60
Maaa 100,4,60
Ta 16,14,32768
Ta 18,14,33792
Maaa 68,4,56
Maaa 228,4,52
D 17,48,65,1024
Maaa 196,4,48
Maaa 164,4,44
Mvaa 228,4,40
Mvaa 196,4,36
D 18,48,65,2048
Mvaa 164,4,32
W 4
Mvaa 168,8,32
Ta 20,15,32768
Ta 22,15,33792
Mvaa 200,8,36
Mvaa 232,8,40
D 19,48,65,3072
Maaa 168,8,44
s_add_u32 m0, s63, 69632
Maaa 200,8,48
Maaa 232,8,52
Maaa 72,8,56
D 20,48,65,0
Maaa 104,8,60
W 4
Maaa 108,12,60
Ta 24,24,32768
Ta 26,24,33792
Maaa 76,12,56
Maaa 236,12,52
D 21,48,65,1024
Maaa 204,12,48
Maaa 172,12,44
)"
R"(Ra 128,27,32768
Mvaa 236,12,40
Mvaa 204,12,36
D 22,48,65,2048
Mvaa 172,12,32
W 5
Ra 132,27,34816
Mvaa 176,16,32
Ta 28,25,32768
Ta 30,25,33792
Mvaa 208,16,36
Mvaa 240,16,40
D 23,48,65,3072
v_min_u32 v139, s75, v138
Maaa 176,16,44
Ra 136,27,36864
Maaa 208,16,48
Maaa 240,16,52
Ra 140,27,38912
Maaa 80,16,56
buffer_load_dword v142, v144, s[8:11], s69 offen offset:2048 sc1
Maaa 112,16,60
W 8
s_add_u32 s69, s69, 4096
Maaa 116,20,60
Ta 0,10,40960
Ta 2,10,41984
Maaa 84,20,56
Ra 144,27,40960
Maaa 244,20,52
global_load_dword v142, v139, s[76:77]
v_add_u32 v138, s73, v138
Maaa 212,20,48
Ra 148,27,43008
Maaa 180,20,44
Mvaa 244,20,40
v_bfe_u32 v29, v1, 7, 1
Mvaa 212,20,36
Ra 152,27,45056
Mvaa 180,20,32
W 11
v_mul_u32_u24 v28, 10240, v29
Mvaa 184,24,32
Ta 4,11,40960
Ta 6,11,41984
Mvaa 216,24,36
Ra 156,27,47104
v_lshrrev_b32 v29, 8, v1
Mvaa 248,24,40
Maaa 184,24,44
v_lshl_add_u32 v28, v29, 7, v28
Maaa 216,24,48
v_and_b32 v29, 0x70, v1
Maaa 248,24,52
Maaa 88,24,56
v_add_u32 v28, v28, v29
Maaa 120,24,60
W 10
Maaa 124,28,60
s_and_b32 s80, s14, 2
Ta 8,12,40960
Ta 10,12,41984
Maaa 92,28,56
s_mul_i32 s80, s80, 20480
Maaa 252,28,52
Maaa 220,28,48
s_sub_u32 s81, 40960, s80
Maaa 188,28,44
Mvaa 252,28,40
s_add_u32 s93, s28, 20480
Mvaa 220,28,36
Mvaa 188,28,32
W 8
v_add_u32 v26, s80, v28
Mvaa 160,0,128
Ta 12,13,40960
Ta 14,13,41984
Mvaa 192,0,132
Mvaa 224,0,136
v_add_u32 v27, s81, v28
Maaa 160,0,140
W 4
Maaa 192,0,144
buffer_load_dwordx4 a[32:35], v26, s[8:11], s28 offen offset:0 sc1
Maaa 224,0,148
Maaa 64,0,152
Maaa 96,0,156
Maaa 100,4,156
buffer_load_dwordx4 a[36:39], v26, s[8:11], s93 offen offset:0 sc1
Ta 16,14,40960
Maaa 68,4,152
Ta 18,14,41984
Maaa 228,4,148
Maaa 196,4,144
Maaa 164,4,140
buffer_load_dwordx4 a[40:43], v27, s[8:11], s28 offen offset:0 sc1
Ta 20,15,40960
Mvaa 228,4,136
Ta 22,15,41984
Mvaa 196,4,132
Ta 24,24,40960
Ta 26,24,41984
Mvaa 164,4,128
W 8
Mvaa 168,8,128
buffer_load_dwordx4 a[44:47], v27, s[8:11], s93 offen offset:0 sc1
Ta 28,25,40960
Mvaa 200,8,132
s_add_u32 s28, s28, 512
s_cmp_eq_u32 s28, s4
s_cselect_b32 s93, 10240, 0
Ta 30,25,41984
Mvaa 232,8,136
s_sub_u32 s28, s28, s93
Maaa 168,8,140
s_add_u32 s93, s28, 20480
Maaa 200,8,144
Maaa 232,8,148
Maaa 72,8,152
buffer_load_dwordx4 a[48:51], v26, s[8:11], s28 offen offset:0 sc1
Maaa 104,8,156
W 8
Maaa 108,12,156
Maaa 76,12,152
Maaa 236,12,148
s_waitcnt vmcnt(7) lgkmcnt(0)
s_barrier
buffer_load_dwordx4 a[52:55], v26, s[8:11], s93 offen offset:0 sc1
Maaa 204,12,144
Maaa 172,12,140
Mvaa 236,12,136
Mvaa 204,12,132
buffer_load_dwordx4 a[56:59], v27, s[8:11], s28 offen offset:0 sc1
Mvaa 172,12,128
Mvaa 176,16,128
Mvaa 208,16,132
Mvaa 240,16,136
s_add_u32 s65, s5, 12
buffer_load_dwordx4 a[60:63], v27, s[8:11], s93 offen offset:0 sc1
Maaa 176,16,140
Maaa 208,16,144
Maaa 240,16,148
Maaa 80,16,152
Maaa 112,16,156
Maaa 116,20,156
Maaa 84,20,152
Maaa 244,20,148
s_cmp_ge_u32 s65, 20
s_cselect_b32 s68, 20, 0
Ta 0,2,0
Ta 2,2,1024
Maaa 212,20,144
Maaa 180,20,140
Mvaa 244,20,136
Mvaa 212,20,132
Ta 4,3,0
Ta 6,3,1024
Mvaa 180,20,128
Mvaa 184,24,128
Mvaa 216,24,132
s_sub_u32 s65, s65, s68
Mvaa 248,24,136
Ta 8,4,0
Ta 10,4,1024
Maaa 184,24,140
Maaa 216,24,144
Maaa 248,24,148
Maaa 88,24,152
Ta 12,5,0
Ta 14,5,1024
Maaa 120,24,156
Maaa 124,28,156
s_lshl_b32 s65, s65, 18
Maaa 92,28,152
Maaa 252,28,148
Ta 16,6,0
Ta 18,6,1024
Maaa 220,28,144
Maaa 188,28,140
Mvaa 252,28,136
Mvaa 220,28,132
Mvaa 188,28,128
s_add_u32 m0, s63, 32768
s_branch .Lq_11%=
.Lv_10%=:
s_waitcnt vmcnt(24)
s_barrier
Ra 32,26,0
Ta 0,10,16384
Ta 2,10,17408
Ra 36,26,2048
Ra 40,26,4096
Ra 44,26,6144
Ra 48,26,8192
Ra 52,26,10240
Ra 56,26,12288
Ra 60,26,14336
Ta 4,11,16384
Ta 6,11,17408
W 6
Zvaa 160,32,0
Ta 8,12,16384
Ta 10,12,17408
Zvaa 164,36,0
Ta 12,13,16384
Ta 14,13,17408
Zvaa 224,40,0
s_waitcnt vmcnt(21)
Zvaa 228,44,0
W 6
v_lshlrev_b32 v56, 16, v32
Zaaa 192,48,0
v_and_b32 v57, 0xffff0000, v32
v_lshlrev_b32 v58, 16, v33
Zaaa 196,52,0
v_and_b32 v59, 0xffff0000, v33
v_lshlrev_b32 v60, 16, v34
Zaaa 64,56,0
v_and_b32 v61, 0xffff0000, v34
v_lshlrev_b32 v62, 16, v35
Zaaa 68,60,0
W 4
v_and_b32 v63, 0xffff0000, v35
v_lshlrev_b32 v64, 16, v36
Zaaa 76,60,4
Ta 16,14,16384
v_and_b32 v65, 0xffff0000, v36
Zaaa 72,56,4
Ta 18,14,17408
v_lshlrev_b32 v66, 16, v37
Zaaa 204,52,4
v_and_b32 v67, 0xffff0000, v37
v_lshlrev_b32 v68, 16, v38
Zaaa 200,48,4
v_and_b32 v69, 0xffff0000, v38
v_lshlrev_b32 v70, 16, v39
Zvaa 236,44,4
v_and_b32 v71, 0xffff0000, v39
v_lshlrev_b32 v72, 16, v40
Zvaa 232,40,4
v_and_b32 v73, 0xffff0000, v40
v_lshlrev_b32 v74, 16, v41
Zvaa 172,36,4
v_and_b32 v75, 0xffff0000, v41
v_lshlrev_b32 v76, 16, v42
Zvaa 168,32,4
W 4
v_and_b32 v77, 0xffff0000, v42
v_lshlrev_b32 v78, 16, v43
Zvaa 176,32,8
v_and_b32 v79, 0xffff0000, v43
A 80,56,57
Zvaa 180,36,8
Ta 20,15,16384
Ta 22,15,17408
Zvaa 240,40,8
A 81,58,59
A 80,80,60
Zvaa 244,44,8
A 81,81,61
A 80,80,62
Zaaa 208,48,8
A 81,81,63
A 80,80,64
Zaaa 212,52,8
A 81,81,65
A 80,80,66
Zaaa 80,56,8
A 81,81,67
A 80,80,68
Zaaa 84,60,8
W 4
A 81,81,69
A 80,80,70
Zaaa 92,60,12
A 81,81,71
A 80,80,72
Zaaa 88,56,12
Ta 24,24,16384
A 81,81,73
Zaaa 220,52,12
Ta 26,24,17408
A 80,80,74
Zaaa 216,48,12
A 81,81,75
A 80,80,76
Zvaa 252,44,12
A 81,81,77
A 80,80,78
Zvaa 248,40,12
A 81,81,79
A 80,80,81
Zvaa 188,36,12
Ra 128,27,0
Zvaa 184,32,12
W 5
Ra 132,27,2048
v_add_f32_dpp v80, v80, v80 quad_perm:[1,0,3,2] row_mask:0xf bank_mask:0xf
Zvaa 192,32,16
Ta 28,25,16384
Ta 30,25,17408
Zvaa 196,36,16
v_add_f32_dpp v80, v80, v80 quad_perm:[2,3,0,1] row_mask:0xf bank_mask:0xf
Zaaa 160,40,16
Ra 136,27,4096
Zaaa 164,44,16
v_add_f32_dpp v80, v80, v80 row_half_mirror row_mask:0xf bank_mask:0xf
Zaaa 224,48,16
Zaaa 228,52,16
v_add_f32_dpp v80, v80, v80 row_mirror row_mask:0xf bank_mask:0xf
v_mov_b32 v81, v80
Zaaa 96,56,16
Ra 140,27,6144
Zaaa 100,60,16
W 8
v_permlane16_swap_b32 v80, v81
A 80,80,81
Zaaa 108,60,20
Ta 0,10,24576
v_mul_f32 v84, 0x3acccccd, v80
Zaaa 104,56,20
s_mov_b32 s86, 0xffff
s_mov_b32 s87, 0xffff
v_sub_f32 v82, v56, v84
Zaaa 236,52,20
U 80,82,82
v_sub_f32 v83, v57, v84
Zaaa 232,48,20
v_fmac_f32 v80, v83, v83
v_sub_f32 v82, v58, v84
Zaaa 172,44,20
v_fmac_f32 v80, v82, v82
v_sub_f32 v83, v59, v84
Zaaa 168,40,20
v_fmac_f32 v80, v83, v83
v_sub_f32 v82, v60, v84
Zvaa 204,36,20
v_fmac_f32 v80, v82, v82
v_sub_f32 v83, v61, v84
Zvaa 200,32,20
W 7
Ta 2,10,25600
v_fmac_f32 v80, v83, v83
Zvaa 208,32,24
Ra 144,27,8192
v_sub_f32 v82, v62, v84
Zvaa 212,36,24
v_fmac_f32 v80, v82, v82
v_sub_f32 v83, v63, v84
Zaaa 176,40,24
v_fmac_f32 v80, v83, v83
v_sub_f32 v82, v64, v84
Zaaa 180,44,24
Ra 148,27,10240
v_fmac_f32 v80, v82, v82
Zaaa 240,48,24
v_sub_f32 v83, v65, v84
v_fmac_f32 v80, v83, v83
Zaaa 244,52,24
v_sub_f32 v82, v66, v84
v_fmac_f32 v80, v82, v82
Zaaa 112,56,24
Ra 152,27,12288
v_sub_f32 v83, v67, v84
Zaaa 116,60,24
W 7
v_fmac_f32 v80, v83, v83
v_sub_f32 v82, v68, v84
Zaaa 124,60,28
v_fmac_f32 v80, v82, v82
v_sub_f32 v83, v69, v84
Zaaa 120,56,28
Ta 4,11,24576
Ta 6,11,25600
Zaaa 252,52,28
v_fmac_f32 v80, v83, v83
v_sub_f32 v82, v70, v84
Zaaa 248,48,28
Ra 156,27,14336
v_fmac_f32 v80, v82, v82
Zaaa 188,44,28
v_sub_f32 v83, v71, v84
v_fmac_f32 v80, v83, v83
Zaaa 184,40,28
v_sub_f32 v82, v72, v84
U 81,82,82
Zvaa 220,36,28
v_sub_f32 v83, v73, v84
v_fmac_f32 v81, v83, v83
Zvaa 216,32,28
W 6
v_sub_f32 v82, v74, v84
v_fmac_f32 v81, v82, v82
Mvaa 160,128,0
v_sub_f32 v83, v75, v84
v_fmac_f32 v81, v83, v83
Mvaa 164,132,0
Ta 8,12,24576
v_sub_f32 v82, v76, v84
Mvaa 224,136,0
)"
R"(Ta 10,12,25600
v_fmac_f32 v81, v82, v82
Mvaa 228,140,0
W 2
v_sub_f32 v83, v77, v84
v_fmac_f32 v81, v83, v83
Maaa 192,144,0
v_sub_f32 v82, v78, v84
v_fmac_f32 v81, v82, v82
Maaa 196,148,0
v_sub_f32 v83, v79, v84
v_fmac_f32 v81, v83, v83
Maaa 64,152,0
v_cndmask_b32_e64 v81, 0, v81, s[86:87]
A 80,80,81
Maaa 68,156,0
Ta 12,13,24576
Ta 14,13,25600
Maaa 76,156,4
Ta 16,14,24576
v_add_f32_dpp v80, v80, v80 quad_perm:[1,0,3,2] row_mask:0xf bank_mask:0xf
Maaa 72,152,4
Ta 18,14,25600
Maaa 204,148,4
v_add_f32_dpp v80, v80, v80 quad_perm:[2,3,0,1] row_mask:0xf bank_mask:0xf
Maaa 200,144,4
Mvaa 236,140,4
Ta 20,15,24576
v_add_f32_dpp v80, v80, v80 row_half_mirror row_mask:0xf bank_mask:0xf
Mvaa 232,136,4
Ta 22,15,25600
Mvaa 172,132,4
v_add_f32_dpp v80, v80, v80 row_mirror row_mask:0xf bank_mask:0xf
v_mov_b32 v81, v80
Mvaa 168,128,4
W 6
v_permlane16_swap_b32 v80, v81
A 80,80,81
Mvaa 176,128,8
v_mul_f32 v80, 0x3acccccd, v80
K 80,80,80
Mvaa 180,132,8
v_lshlrev_b32 v80, 16, v80
v_add_f32 v80, 0x3727c5ac, v80
Mvaa 240,136,8
K 80,80,80
v_lshlrev_b32 v80, 16, v80
Mvaa 244,140,8
v_sqrt_f32 v81, v80
Maaa 208,144,8
v_rcp_f32 v82, v81
Maaa 212,148,8
v_fma_f32 v83, -v81, v81, v80
v_mul_f32 v82, 0.5, v82
Maaa 80,152,8
v_fma_f32 v81, v83, v82, v81
K 86,81,81
Maaa 84,156,8
W 4
Ta 24,24,24576
v_lshlrev_b32 v86, 16, v86
Maaa 92,156,12
v_rcp_f32 v87, v86
Maaa 88,152,12
Ta 26,24,25600
Ta 28,25,24576
Ta 30,25,25600
Maaa 220,148,12
s_waitcnt vmcnt(1) lgkmcnt(0)
s_barrier
K 85,84,84
Maaa 216,144,12
s_add_u32 s64, s64, 128
v_lshlrev_b32 v85, 16, v85
Mvaa 252,140,12
v_sub_f32 v56, v56, v85
v_sub_f32 v57, v57, v85
Mvaa 248,136,12
s_and_b32 s64, s64, 1023
K 80,56,57
v_lshlrev_b32 v56, 16, v80
Mvaa 188,132,12
v_and_b32 v57, 0xffff0000, v80
U 80,56,87
Mvaa 184,128,12
U 81,57,87
v_fma_f32 v82, -v80, v86, v56
Mvaa 192,128,16
s_add_u32 m0, s61, 0
v_fma_f32 v83, -v81, v86, v57
v_fmac_f32 v80, v82, v87
Mvaa 196,132,16
v_fmac_f32 v81, v83, v87
K 32,80,81
Maaa 160,136,16
v_lshlrev_b32 v80, 16, v32
v_and_b32 v81, 0xffff0000, v32
Maaa 164,140,16
v_lshlrev_b32 v82, 16, v44
v_and_b32 v83, 0xffff0000, v44
Maaa 224,144,16
U 84,80,82
v_fmac_f32 v84, v81, v83
Maaa 228,148,16
v_sub_f32 v58, v58, v85
v_sub_f32 v59, v59, v85
Maaa 96,152,16
K 80,58,59
v_lshlrev_b32 v58, 16, v80
Maaa 100,156,16
v_and_b32 v59, 0xffff0000, v80
U 80,58,87
Maaa 108,156,20
U 81,59,87
v_fma_f32 v82, -v80, v86, v58
Maaa 104,152,20
v_fma_f32 v83, -v81, v86, v59
v_fmac_f32 v80, v82, v87
Maaa 236,148,20
v_fmac_f32 v81, v83, v87
K 33,80,81
Maaa 232,144,20
Ra 32,26,32768
v_lshlrev_b32 v80, 16, v33
Maaa 172,140,20
v_and_b32 v81, 0xffff0000, v33
v_lshlrev_b32 v82, 16, v45
Maaa 168,136,20
v_and_b32 v83, 0xffff0000, v45
v_fmac_f32 v84, v80, v82
Mvaa 204,132,20
Ra 36,26,34816
v_fmac_f32 v84, v81, v83
Mvaa 200,128,20
v_sub_f32 v60, v60, v85
v_sub_f32 v61, v61, v85
Mvaa 208,128,24
K 80,60,61
v_lshlrev_b32 v60, 16, v80
Mvaa 212,132,24
D 2,40,64,0
v_and_b32 v61, 0xffff0000, v80
Maaa 176,136,24
Ra 40,26,36864
U 80,60,87
Maaa 180,140,24
U 81,61,87
v_fma_f32 v82, -v80, v86, v60
Maaa 240,144,24
v_fma_f32 v83, -v81, v86, v61
v_fmac_f32 v80, v82, v87
Maaa 244,148,24
D 3,40,64,1024
v_fmac_f32 v81, v83, v87
Maaa 112,152,24
Ra 44,26,38912
K 34,80,81
Maaa 116,156,24
v_lshlrev_b32 v80, 16, v34
v_and_b32 v81, 0xffff0000, v34
Maaa 124,156,28
v_lshlrev_b32 v82, 16, v46
v_and_b32 v83, 0xffff0000, v46
Maaa 120,152,28
D 4,40,64,2048
Ta 0,10,49152
Maaa 252,148,28
Ta 2,10,50176
Ra 48,26,40960
Maaa 248,144,28
v_fmac_f32 v84, v80, v82
v_fmac_f32 v84, v81, v83
Maaa 188,140,28
v_sub_f32 v62, v62, v85
v_sub_f32 v63, v63, v85
Maaa 184,136,28
D 5,40,64,3072
K 80,62,63
Mvaa 220,132,28
Ra 52,26,43008
Ra 56,26,45056
Ta 4,11,49152
Ta 6,11,50176
Ra 60,26,47104
Mvaa 216,128,28
W 6
v_lshlrev_b32 v62, 16, v80
v_and_b32 v63, 0xffff0000, v80
Mvaa 160,32,0
s_add_u32 m0, s61, 4096
U 80,62,87
U 81,63,87
Mvaa 164,36,0
v_fma_f32 v82, -v80, v86, v62
v_fma_f32 v83, -v81, v86, v63
Mvaa 224,40,0
D 6,40,64,0
v_fmac_f32 v80, v82, v87
Mvaa 228,44,0
W 0
v_fmac_f32 v81, v83, v87
K 35,80,81
Maaa 192,48,0
v_lshlrev_b32 v80, 16, v35
v_and_b32 v81, 0xffff0000, v35
Maaa 196,52,0
v_lshlrev_b32 v82, 16, v47
v_and_b32 v83, 0xffff0000, v47
Maaa 64,56,0
D 7,40,64,1024
v_fmac_f32 v84, v80, v82
Maaa 68,60,0
v_fmac_f32 v84, v81, v83
v_sub_f32 v64, v64, v85
Maaa 76,60,4
v_sub_f32 v65, v65, v85
K 80,64,65
Maaa 72,56,4
v_lshlrev_b32 v64, 16, v80
v_and_b32 v65, 0xffff0000, v80
Maaa 204,52,4
D 8,40,64,2048
U 80,64,87
Maaa 200,48,4
U 81,65,87
v_fma_f32 v82, -v80, v86, v64
Mvaa 236,44,4
Ta 8,12,49152
Ta 10,12,50176
Mvaa 232,40,4
v_fma_f32 v83, -v81, v86, v65
v_fmac_f32 v80, v82, v87
Mvaa 172,36,4
D 9,40,64,3072
v_fmac_f32 v81, v83, v87
Mvaa 168,32,4
W 0
s_lshl_b32 s65, s64, 12
K 36,80,81
Mvaa 176,32,8
s_add_u32 m0, s62, 0
v_lshlrev_b32 v80, 16, v36
v_and_b32 v81, 0xffff0000, v36
Mvaa 180,36,8
v_lshlrev_b32 v82, 16, v48
v_and_b32 v83, 0xffff0000, v48
Mvaa 240,40,8
v_fmac_f32 v84, v80, v82
v_fmac_f32 v84, v81, v83
Mvaa 244,44,8
v_sub_f32 v66, v66, v85
v_sub_f32 v67, v67, v85
Maaa 208,48,8
Ta 12,13,49152
Ta 14,13,50176
Maaa 212,52,8
D 16,44,65,0
K 80,66,67
Maaa 80,56,8
v_lshlrev_b32 v66, 16, v80
v_and_b32 v67, 0xffff0000, v80
Maaa 84,60,8
W 0
U 80,66,87
U 81,67,87
Maaa 92,60,12
v_fma_f32 v82, -v80, v86, v66
v_fma_f32 v83, -v81, v86, v67
Maaa 88,56,12
D 17,44,65,1024
v_fmac_f32 v80, v82, v87
Maaa 220,52,12
v_fmac_f32 v81, v83, v87
K 37,80,81
Maaa 216,48,12
v_lshlrev_b32 v80, 16, v37
v_and_b32 v81, 0xffff0000, v37
Mvaa 252,44,12
Ta 16,14,49152
Ta 18,14,50176
Mvaa 248,40,12
D 18,44,65,2048
v_lshlrev_b32 v82, 16, v49
Mvaa 188,36,12
v_and_b32 v83, 0xffff0000, v49
v_fmac_f32 v84, v80, v82
Mvaa 184,32,12
W 0
v_fmac_f32 v84, v81, v83
v_sub_f32 v68, v68, v85
Mvaa 192,32,16
v_sub_f32 v69, v69, v85
K 80,68,69
Mvaa 196,36,16
D 19,44,65,3072
v_lshlrev_b32 v68, 16, v80
Maaa 160,40,16
s_add_u32 m0, s62, 4096
v_and_b32 v69, 0xffff0000, v80
U 80,68,87
Maaa 164,44,16
U 81,69,87
v_fma_f32 v82, -v80, v86, v68
Maaa 224,48,16
Ta 20,15,49152
Ta 22,15,50176
Maaa 228,52,16
D 20,44,65,0
v_fma_f32 v83, -v81, v86, v69
Maaa 96,56,16
v_fmac_f32 v80, v82, v87
v_fmac_f32 v81, v83, v87
Maaa 100,60,16
W 0
K 38,80,81
v_lshlrev_b32 v80, 16, v38
Maaa 108,60,20
v_and_b32 v81, 0xffff0000, v38
v_lshlrev_b32 v82, 16, v50
Maaa 104,56,20
D 21,44,65,1024
v_and_b32 v83, 0xffff0000, v50
Maaa 236,52,20
v_fmac_f32 v84, v80, v82
v_fmac_f32 v84, v81, v83
Maaa 232,48,20
v_sub_f32 v70, v70, v85
v_sub_f32 v71, v71, v85
Maaa 172,44,20
Ta 24,24,49152
Ta 26,24,50176
Maaa 168,40,20
D 22,44,65,2048
K 80,70,71
Mvaa 204,36,20
v_lshlrev_b32 v70, 16, v80
v_and_b32 v71, 0xffff0000, v80
Mvaa 200,32,20
W 0
U 80,70,87
U 81,71,87
Mvaa 208,32,24
v_fma_f32 v82, -v80, v86, v70
v_fma_f32 v83, -v81, v86, v71
Mvaa 212,36,24
D 23,44,65,3072
v_fmac_f32 v80, v82, v87
Maaa 176,40,24
v_fmac_f32 v81, v83, v87
K 39,80,81
Maaa 180,44,24
v_lshlrev_b32 v80, 16, v39
v_and_b32 v81, 0xffff0000, v39
Maaa 240,48,24
Ra 128,27,32768
Ra 132,27,34816
Ta 28,25,49152
Ta 30,25,50176
Maaa 244,52,24
global_load_dword v142, v[140:141], off
v_lshl_add_u64 v[140:141], v[140:141], 0, s[68:69]
v_lshlrev_b32 v82, 16, v51
Maaa 112,56,24
v_and_b32 v83, 0xffff0000, v51
v_fmac_f32 v84, v80, v82
Maaa 116,60,24
W 0
v_fmac_f32 v84, v81, v83
v_sub_f32 v72, v72, v85
Maaa 124,60,28
v_sub_f32 v73, v73, v85
K 80,72,73
Maaa 120,56,28
v_lshlrev_b32 v72, 16, v80
v_and_b32 v73, 0xffff0000, v80
Maaa 252,52,28
Ra 136,27,36864
Ra 140,27,38912
Maaa 248,48,28
U 80,72,87
U 81,73,87
Maaa 188,44,28
Ta 0,10,57344
Ta 2,10,58368
Maaa 184,40,28
v_fma_f32 v82, -v80, v86, v72
v_fma_f32 v83, -v81, v86, v73
Mvaa 220,36,28
Ra 144,27,40960
Ra 148,27,43008
Ra 152,27,45056
Ta 4,11,57344
Ta 6,11,58368
Ra 156,27,47104
Mvaa 216,32,28
W 6
v_fmac_f32 v80, v82, v87
v_fmac_f32 v81, v83, v87
Mvaa 160,128,0
K 40,80,81
)"
R"(v_lshlrev_b32 v80, 16, v40
Mvaa 164,132,0
v_and_b32 v81, 0xffff0000, v40
v_lshlrev_b32 v82, 16, v52
Mvaa 224,136,0
v_and_b32 v83, 0xffff0000, v52
v_fmac_f32 v84, v80, v82
Mvaa 228,140,0
W 0
v_fmac_f32 v84, v81, v83
v_sub_f32 v74, v74, v85
Maaa 192,144,0
v_sub_f32 v75, v75, v85
K 80,74,75
Maaa 196,148,0
v_lshlrev_b32 v74, 16, v80
v_and_b32 v75, 0xffff0000, v80
Maaa 64,152,0
U 80,74,87
U 81,75,87
Maaa 68,156,0
v_fma_f32 v82, -v80, v86, v74
v_fma_f32 v83, -v81, v86, v75
Maaa 76,156,4
v_fmac_f32 v80, v82, v87
v_fmac_f32 v81, v83, v87
Maaa 72,152,4
K 41,80,81
v_lshlrev_b32 v80, 16, v41
Maaa 204,148,4
v_and_b32 v81, 0xffff0000, v41
v_lshlrev_b32 v82, 16, v53
Maaa 200,144,4
v_and_b32 v83, 0xffff0000, v53
v_fmac_f32 v84, v80, v82
Mvaa 236,140,4
Ta 8,12,57344
Ta 10,12,58368
Mvaa 232,136,4
v_fmac_f32 v84, v81, v83
v_sub_f32 v76, v76, v85
Mvaa 172,132,4
v_sub_f32 v77, v77, v85
K 80,76,77
Mvaa 168,128,4
W 0
v_lshlrev_b32 v76, 16, v80
v_and_b32 v77, 0xffff0000, v80
Mvaa 176,128,8
U 80,76,87
U 81,77,87
Mvaa 180,132,8
v_fma_f32 v82, -v80, v86, v76
v_fma_f32 v83, -v81, v86, v77
Mvaa 240,136,8
v_fmac_f32 v80, v82, v87
v_fmac_f32 v81, v83, v87
Mvaa 244,140,8
K 42,80,81
v_lshlrev_b32 v80, 16, v42
Maaa 208,144,8
Ta 12,13,57344
Ta 14,13,58368
Maaa 212,148,8
v_and_b32 v81, 0xffff0000, v42
v_lshlrev_b32 v82, 16, v54
Maaa 80,152,8
v_and_b32 v83, 0xffff0000, v54
v_fmac_f32 v84, v80, v82
Maaa 84,156,8
W 0
v_fmac_f32 v84, v81, v83
v_sub_f32 v78, v78, v85
Maaa 92,156,12
v_sub_f32 v79, v79, v85
K 80,78,79
Maaa 88,152,12
Ta 16,14,57344
Ta 18,14,58368
Ta 20,15,57344
Ta 22,15,58368
Ta 24,24,57344
Ta 26,24,58368
Ta 28,25,57344
Ta 30,25,58368
v_lshlrev_b32 v78, 16, v80
v_and_b32 v79, 0xffff0000, v80
U 80,78,87
U 81,79,87
v_fma_f32 v82, -v80, v86, v78
v_fma_f32 v83, -v81, v86, v79
v_fmac_f32 v80, v82, v87
v_fmac_f32 v81, v83, v87
K 43,80,81
v_lshlrev_b32 v80, 16, v43
v_and_b32 v81, 0xffff0000, v43
v_lshlrev_b32 v82, 16, v55
v_and_b32 v83, 0xffff0000, v55
v_fmac_f32 v84, v80, v82
v_fmac_f32 v84, v81, v83
v_lshrrev_b32 v82, 9, v1
s_lshl_b32 s80, s14, 1
v_add_u32 v82, s80, v82
v_mul_u32_u24 v83, 1552, v82
v_add_u32 v83, v83, v88
v_add_u32 v83, 131072, v83
ds_write_b128 v83, v[32:35] offset:0
ds_write_b128 v83, v[36:39] offset:512
ds_write_b128 v83, v[40:43] offset:1024
v_add_f32_dpp v84, v84, v84 quad_perm:[1,0,3,2] row_mask:0xf bank_mask:0xf
s_nop 1
v_add_f32_dpp v84, v84, v84 quad_perm:[2,3,0,1] row_mask:0xf bank_mask:0xf
s_nop 1
v_add_f32_dpp v84, v84, v84 row_half_mirror row_mask:0xf bank_mask:0xf
s_nop 1
v_add_f32_dpp v84, v84, v84 row_mirror row_mask:0xf bank_mask:0xf
v_mov_b32 v81, v84
s_nop 1
v_permlane16_swap_b32 v84, v81
A 84,84,81
K 80,84,84
v_lshlrev_b32 v80, 16, v80
s_lshl_b32 s80, s15, 3
v_add_u32 v82, s80, v82
v_sub_u32 v83, 2047, v82
v_ashrrev_i32 v81, 31, v80
v_or_b32 v81, 0x80000000, v81
v_xor_b32 v80, v80, v81
v_and_b32 v80, 0xffff0000, v80
v_or_b32 v80, v80, v83
v_and_b32 v82, 7, v82
v_lshlrev_b32 v82, 2, v82
v_add_u32 v82, 143488, v82
ds_write_b32 v82, v80
Maaa 220,148,12
s_waitcnt vmcnt(1) lgkmcnt(0)
s_barrier
s_add_u32 s64, s64, 128
Maaa 216,144,12
v_bfe_u32 v80, v1, 4, 3
Mvaa 252,140,12
Ra 32,26,0
v_mul_u32_u24 v81, 1552, v80
Mvaa 248,136,12
s_and_b32 s64, s64, 1023
v_lshrrev_b32 v82, 7, v1
Mvaa 188,132,12
v_lshl_add_u32 v81, v82, 4, v81
Mvaa 184,128,12
s_lshl_b32 s80, s14, 7
Ra 36,26,2048
Mvaa 192,128,16
s_add_u32 m0, s61, 32768
s_add_u32 s80, s80, 131072
Mvaa 196,132,16
v_add_u32 v81, s80, v81
Maaa 160,136,16
ds_read_b128 v[32:35], v81
Ra 40,26,4096
Maaa 164,140,16
D 2,40,64,0
Rv 36,81,512
Maaa 224,144,16
s_and_b32 s80, s14, 2
Maaa 228,148,16
s_lshl_b32 s80, s80, 7
Ra 44,26,6144
Maaa 96,152,16
v_subrev_u32 v81, s80, v81
Maaa 100,156,16
D 3,40,64,1024
Rv 40,81,1024
Maaa 108,156,20
Ta 0,10,16384
v_lshlrev_b32 v80, 2, v80
Maaa 104,152,20
Ta 2,10,17408
Ra 48,26,8192
Maaa 236,148,20
v_add_u32 v80, 143488, v80
Maaa 232,144,20
D 4,40,64,2048
ds_read_b32 v84, v80
Maaa 172,140,20
Ra 52,26,10240
v_mov_b32 v85, s13
Maaa 168,136,20
v_lshrrev_b32 v83, 1, v1
Mvaa 204,132,20
s_mul_i32 s82, s15, 10240
s_add_u32 s82, s82, 131072
Ra 56,26,12288
Mvaa 200,128,20
s_lshl_b32 s80, s14, 10
D 5,40,64,3072
Mvaa 208,128,24
s_add_u32 m0, s61, 36864
s_add_u32 s83, s82, s80
Ta 4,11,16384
Mvaa 212,132,24
Ta 6,11,17408
Ra 60,26,14336
Maaa 176,136,24
Maaa 180,140,24
D 6,40,64,0
Maaa 240,144,24
Maaa 244,148,24
Maaa 112,152,24
Maaa 116,156,24
W 5
buffer_store_dwordx4 v[32:35], v1, s[8:11], s83 offen sc1
s_add_u32 s83, s83, 4096
Maaa 124,156,28
Ta 8,12,16384
Ta 10,12,17408
Maaa 120,152,28
Maaa 252,148,28
Maaa 248,144,28
D 7,40,64,1024
Maaa 188,140,28
Maaa 184,136,28
Mvaa 220,132,28
Mvaa 216,128,28
buffer_store_dwordx4 v[36:39], v1, s[8:11], s83 offen sc1
Mvaa 160,32,0
s_and_b32 s80, s80, 1024
s_add_u32 s83, s82, s80
s_add_u32 s83, s83, 8192
Mvaa 164,36,0
Ta 12,13,16384
Ta 14,13,17408
Mvaa 224,40,0
Mvaa 228,44,0
W 4
D 8,40,64,2048
Maaa 192,48,0
Maaa 196,52,0
Maaa 64,56,0
Maaa 68,60,0
buffer_store_dwordx4 v[40:43], v1, s[8:11], s83 offen sc1
Maaa 76,60,4
Ta 16,14,16384
Ta 18,14,17408
Maaa 72,56,4
Maaa 204,52,4
Maaa 200,48,4
D 9,40,64,3072
Mvaa 236,44,4
s_lshl_b32 s65, s64, 12
s_add_u32 m0, s62, 32768
Mvaa 232,40,4
Mvaa 172,36,4
Mvaa 168,32,4
W 4
D 16,44,65,0
Mvaa 176,32,8
Ta 20,15,16384
Ta 22,15,17408
Mvaa 180,36,8
Mvaa 240,40,8
Mvaa 244,44,8
D 17,44,65,1024
Maaa 208,48,8
Maaa 212,52,8
Maaa 80,56,8
Maaa 84,60,8
W 4
D 18,44,65,2048
Maaa 92,60,12
Ta 24,24,16384
Ta 26,24,17408
Maaa 88,56,12
Maaa 220,52,12
Maaa 216,48,12
D 19,44,65,3072
Mvaa 252,44,12
s_add_u32 m0, s62, 36864
Ra 128,27,0
Mvaa 248,40,12
Mvaa 188,36,12
Mvaa 184,32,12
W 5
D 20,44,65,0
Ra 132,27,2048
Mvaa 192,32,16
Ta 28,25,16384
Ta 30,25,17408
Mvaa 196,36,16
Maaa 160,40,16
Ra 136,27,4096
Maaa 164,44,16
D 21,44,65,1024
Maaa 224,48,16
Maaa 228,52,16
Ra 140,27,6144
Maaa 96,56,16
Maaa 100,60,16
W 8
D 22,44,65,2048
Maaa 108,60,20
Ta 0,10,24576
Ta 2,10,25600
Maaa 104,56,20
Ra 144,27,8192
Maaa 236,52,20
Maaa 232,48,20
D 23,44,65,3072
Ra 148,27,10240
Maaa 172,44,20
Maaa 168,40,20
Mvaa 204,36,20
Ra 152,27,12288
Mvaa 200,32,20
W 11
Mvaa 208,32,24
Ta 4,11,24576
Ta 6,11,25600
Mvaa 212,36,24
Ra 156,27,14336
Maaa 176,40,24
Maaa 180,44,24
Maaa 240,48,24
Maaa 244,52,24
Maaa 112,56,24
Maaa 116,60,24
W 10
Maaa 124,60,28
Ta 8,12,24576
Ta 10,12,25600
Maaa 120,56,28
Maaa 252,52,28
Maaa 248,48,28
Maaa 188,44,28
Maaa 184,40,28
Mvaa 220,36,28
Mvaa 216,32,28
W 8
Mvaa 160,128,0
Ta 12,13,24576
Ta 14,13,25600
Mvaa 164,132,0
Mvaa 224,136,0
Mvaa 228,140,0
W 4
Maaa 192,144,0
Maaa 196,148,0
Maaa 64,152,0
Maaa 68,156,0
Maaa 76,156,4
Ta 16,14,24576
Ta 18,14,25600
Maaa 72,152,4
Maaa 204,148,4
Maaa 200,144,4
Mvaa 236,140,4
Ta 20,15,24576
Ta 22,15,25600
Mvaa 232,136,4
Mvaa 172,132,4
Ta 24,24,24576
Ta 26,24,25600
Mvaa 168,128,4
W 8
Mvaa 176,128,8
Ta 28,25,24576
Ta 30,25,25600
Mvaa 180,132,8
Mvaa 240,136,8
Mvaa 244,140,8
Maaa 208,144,8
Maaa 212,148,8
Maaa 80,152,8
Maaa 84,156,8
W 8
Maaa 92,156,12
Maaa 88,152,12
Maaa 220,148,12
s_waitcnt vmcnt(0) lgkmcnt(0)
s_barrier
s_cmp_eq_u32 s14, 0
s_cbranch_scc0 .Lpubr_18%=
s_lshl_b32 s80, s15, 6
s_add_u32 s80, s80, 40960
s_mov_b64 exec, 0xff
buffer_store_dwordx2 v[84:85], v83, s[8:11], s80 offen sc1
s_mov_b64 exec, -1
.Lpubr_18%=:
Maaa 216,144,12
s_add_u32 s64, s64, 128
Mvaa 252,140,12
Ra 32,26,32768
Mvaa 248,136,12
s_and_b32 s64, s64, 1023
Mvaa 188,132,12
Mvaa 184,128,12
s_add_u32 m0, s61, 0
Ra 36,26,34816
Mvaa 192,128,16
Mvaa 196,132,16
D 2,40,64,0
)"
R"(Maaa 160,136,16
Ra 40,26,36864
Maaa 164,140,16
Maaa 224,144,16
Maaa 228,148,16
D 3,40,64,1024
Ra 44,26,38912
Maaa 96,152,16
Maaa 100,156,16
Maaa 108,156,20
Ta 0,10,49152
Ta 2,10,50176
Maaa 104,152,20
D 4,40,64,2048
Ra 48,26,40960
Maaa 236,148,20
Maaa 232,144,20
Ra 52,26,43008
Maaa 172,140,20
Maaa 168,136,20
D 5,40,64,3072
Mvaa 204,132,20
s_add_u32 m0, s61, 4096
Ra 56,26,45056
Mvaa 200,128,20
Mvaa 208,128,24
Ta 4,11,49152
Ta 6,11,50176
Mvaa 212,132,24
D 6,40,64,0
Ra 60,26,47104
Maaa 176,136,24
Maaa 180,140,24
Maaa 240,144,24
Maaa 244,148,24
D 7,40,64,1024
Maaa 112,152,24
Maaa 116,156,24
Maaa 124,156,28
Ta 8,12,49152
Ta 10,12,50176
Maaa 120,152,28
D 8,40,64,2048
Maaa 252,148,28
Maaa 248,144,28
Maaa 188,140,28
Maaa 184,136,28
D 9,40,64,3072
Mvaa 220,132,28
s_lshl_b32 s65, s64, 12
s_add_u32 m0, s62, 0
Mvaa 216,128,28
W 8
Mvaa 160,32,0
Ta 12,13,49152
Ta 14,13,50176
Mvaa 164,36,0
D 16,44,65,0
Mvaa 224,40,0
Mvaa 228,44,0
W 4
Maaa 192,48,0
Maaa 196,52,0
D 17,44,65,1024
Maaa 64,56,0
Maaa 68,60,0
Maaa 76,60,4
Ta 16,14,49152
Ta 18,14,50176
Maaa 72,56,4
D 18,44,65,2048
Maaa 204,52,4
Maaa 200,48,4
Mvaa 236,44,4
Mvaa 232,40,4
D 19,44,65,3072
Mvaa 172,36,4
s_add_u32 m0, s62, 4096
Mvaa 168,32,4
W 4
Mvaa 176,32,8
Ta 20,15,49152
Ta 22,15,50176
Mvaa 180,36,8
D 20,44,65,0
Mvaa 240,40,8
Mvaa 244,44,8
Maaa 208,48,8
Maaa 212,52,8
D 21,44,65,1024
Maaa 80,56,8
Maaa 84,60,8
W 4
Maaa 92,60,12
Ta 24,24,49152
Ta 26,24,50176
Maaa 88,56,12
D 22,44,65,2048
Maaa 220,52,12
Maaa 216,48,12
Mvaa 252,44,12
Ra 128,27,32768
Mvaa 248,40,12
D 23,44,65,3072
Mvaa 188,36,12
s_add_u32 s65, s5, 0
s_cmp_ge_u32 s65, 20
s_cselect_b32 s68, 20, 0
s_sub_u32 s65, s65, s68
Mvaa 184,32,12
W 5
s_lshl_b32 s65, s65, 18
s_add_u32 m0, s63, 131072
Mvaa 192,32,16
Ra 132,27,34816
Ta 28,25,49152
Mvaa 196,36,16
D 16,48,65,0
Ta 30,25,50176
Maaa 160,40,16
Ra 136,27,36864
Maaa 164,44,16
Maaa 224,48,16
Maaa 228,52,16
D 17,48,65,1024
Ra 140,27,38912
Maaa 96,56,16
Maaa 100,60,16
W 8
Maaa 108,60,20
Ta 0,10,57344
Ta 2,10,58368
Maaa 104,56,20
D 18,48,65,2048
Ra 144,27,40960
Maaa 236,52,20
Maaa 232,48,20
Ra 148,27,43008
Maaa 172,44,20
Maaa 168,40,20
D 19,48,65,3072
Mvaa 204,36,20
s_add_u32 m0, s63, 135168
Ra 152,27,45056
Mvaa 200,32,20
W 11
Mvaa 208,32,24
Ta 4,11,57344
Ta 6,11,58368
Mvaa 212,36,24
D 20,48,65,0
Ra 156,27,47104
Maaa 176,40,24
Maaa 180,44,24
Maaa 240,48,24
Maaa 244,52,24
D 21,48,65,1024
Maaa 112,56,24
Maaa 116,60,24
W 10
Maaa 124,60,28
Ta 8,12,57344
Ta 10,12,58368
Maaa 120,56,28
D 22,48,65,2048
Maaa 252,52,28
Maaa 248,48,28
Maaa 188,44,28
Maaa 184,40,28
D 23,48,65,3072
Mvaa 220,36,28
Mvaa 216,32,28
W 8
Mvaa 160,128,0
Ta 12,13,57344
Ta 14,13,58368
Mvaa 164,132,0
global_load_dword v142, v[140:141], off
v_lshl_add_u64 v[140:141], v[140:141], 0, s[68:69]
Mvaa 224,136,0
Mvaa 228,140,0
W 4
Maaa 192,144,0
Maaa 196,148,0
Maaa 64,152,0
Maaa 68,156,0
Maaa 76,156,4
Ta 16,14,57344
Ta 18,14,58368
Maaa 72,152,4
Maaa 204,148,4
Maaa 200,144,4
Mvaa 236,140,4
Ta 20,15,57344
Ta 22,15,58368
Mvaa 232,136,4
Mvaa 172,132,4
Ta 24,24,57344
Ta 26,24,58368
Mvaa 168,128,4
W 8
Mvaa 176,128,8
Ta 28,25,57344
Ta 30,25,58368
Mvaa 180,132,8
Mvaa 240,136,8
Mvaa 244,140,8
Maaa 208,144,8
Maaa 212,148,8
Maaa 80,152,8
Maaa 84,156,8
W 8
Maaa 92,156,12
Maaa 88,152,12
Maaa 220,148,12
s_waitcnt vmcnt(1) lgkmcnt(0)
s_barrier
s_add_u32 s64, s64, 128
Maaa 216,144,12
Mvaa 252,140,12
Ra 32,26,0
Mvaa 248,136,12
s_and_b32 s64, s64, 1023
Mvaa 188,132,12
Mvaa 184,128,12
Ra 36,26,2048
Mvaa 192,128,16
s_add_u32 m0, s61, 32768
Mvaa 196,132,16
Maaa 160,136,16
Ra 40,26,4096
Maaa 164,140,16
D 2,40,64,0
Maaa 224,144,16
Maaa 228,148,16
Ra 44,26,6144
Maaa 96,152,16
Maaa 100,156,16
D 3,40,64,1024
Maaa 108,156,20
Ta 0,10,16384
Ta 2,10,17408
Maaa 104,152,20
Ra 48,26,8192
Maaa 236,148,20
Maaa 232,144,20
D 4,40,64,2048
Ra 52,26,10240
Maaa 172,140,20
Maaa 168,136,20
Mvaa 204,132,20
Ra 56,26,12288
Mvaa 200,128,20
D 5,40,64,3072
Mvaa 208,128,24
s_add_u32 m0, s61, 36864
Ta 4,11,16384
Ta 6,11,17408
Mvaa 212,132,24
Ra 60,26,14336
Maaa 176,136,24
v_lshrrev_b32 v28, 1, v1
Maaa 180,140,24
s_lshl_b32 s80, s60, 9
D 6,40,64,0
Maaa 240,144,24
s_add_u32 s80, s80, 40960
Maaa 244,148,24
Maaa 112,152,24
Maaa 116,156,24
D 7,40,64,1024
Maaa 124,156,28
Ta 8,12,16384
Ta 10,12,17408
Maaa 120,152,28
Maaa 252,148,28
Maaa 248,144,28
buffer_load_dwordx2 v[28:29], v28, s[8:11], s80 offen sc1
Maaa 188,140,28
Maaa 184,136,28
Mvaa 220,132,28
Mvaa 216,128,28
W 8
D 8,40,64,2048
Mvaa 160,32,0
Ta 12,13,16384
Ta 14,13,17408
Mvaa 164,36,0
Mvaa 224,40,0
Mvaa 228,44,0
W 4
D 9,40,64,3072
Maaa 192,48,0
s_lshl_b32 s65, s64, 12
s_add_u32 m0, s62, 32768
Maaa 196,52,0
Maaa 64,56,0
Maaa 68,60,0
D 16,44,65,0
Maaa 76,60,4
Ta 16,14,16384
Ta 18,14,17408
Maaa 72,56,4
Maaa 204,52,4
Maaa 200,48,4
D 17,44,65,1024
Mvaa 236,44,4
Mvaa 232,40,4
Mvaa 172,36,4
Mvaa 168,32,4
W 4
D 18,44,65,2048
Mvaa 176,32,8
Ta 20,15,16384
Ta 22,15,17408
Mvaa 180,36,8
Mvaa 240,40,8
Mvaa 244,44,8
D 19,44,65,3072
Maaa 208,48,8
s_add_u32 m0, s62, 36864
Maaa 212,52,8
Maaa 80,56,8
Maaa 84,60,8
W 4
D 20,44,65,0
Maaa 92,60,12
Ta 24,24,16384
Ta 26,24,17408
Maaa 88,56,12
Maaa 220,52,12
Maaa 216,48,12
D 21,44,65,1024
Mvaa 252,44,12
Ra 128,27,0
Mvaa 248,40,12
Mvaa 188,36,12
Mvaa 184,32,12
W 5
D 22,44,65,2048
Ra 132,27,2048
Mvaa 192,32,16
Ta 28,25,16384
Ta 30,25,17408
Mvaa 196,36,16
Maaa 160,40,16
Ra 136,27,4096
Maaa 164,44,16
D 23,44,65,3072
v_lshrrev_b32 v138, 4, v1
Maaa 224,48,16
s_sub_u32 s81, s73, 1
v_min_u32 v138, s81, v138
v_lshrrev_b32 v139, 2, v138
Maaa 228,52,16
Ra 140,27,6144
v_mul_u32_u24 v139, s72, v139
Maaa 96,56,16
v_and_b32 v138, 3, v138
Maaa 100,60,16
W 8
v_lshl_add_u32 v138, v138, 7, v139
Maaa 108,60,20
s_waitcnt vmcnt(10)
v_cmp_eq_u32 vcc, s13, v29
s_cmp_eq_u64 vcc, -1
s_cbranch_scc0 .Lspin_19%=
.Lok_20%=:
v_add_u32 v138, s74, v138
Maaa 104,56,20
s_lshr_b32 s73, s73, 2
Ta 0,10,24576
Ta 2,10,25600
Maaa 236,52,20
s_mul_i32 s73, s73, s72
Ra 144,27,8192
v_bfe_u32 v144, v1, 8, 1
Maaa 232,48,20
v_min_u32 v139, s75, v138
v_mul_u32_u24 v144, 10240, v144
Maaa 172,44,20
Ra 148,27,10240
v_and_b32 v145, 0xf0, v1
Maaa 168,40,20
global_load_dword v142, v139, s[76:77]
v_lshl_add_u32 v144, v145, 3, v144
Mvaa 204,36,20
s_mul_i32 s69, s58, 20480
Ra 152,27,12288
v_add_u32 v138, s73, v138
Mvaa 200,32,20
W 11
s_add_u32 s69, s69, s28
Mvaa 208,32,24
Ta 4,11,24576
Ta 6,11,25600
Mvaa 212,36,24
buffer_load_dword v142, v144, s[8:11], s69 offen offset:0 sc1
Ra 156,27,14336
Maaa 176,40,24
)"
R"(Maaa 180,44,24
Maaa 240,48,24
Maaa 244,52,24
Maaa 112,56,24
Maaa 116,60,24
W 10
Maaa 124,60,28
Ta 8,12,24576
Ta 10,12,25600
Maaa 120,56,28
Maaa 252,52,28
Maaa 248,48,28
Maaa 188,44,28
Maaa 184,40,28
Mvaa 220,36,28
Mvaa 216,32,28
W 8
Mvaa 160,128,0
Ta 12,13,24576
Ta 14,13,25600
Mvaa 164,132,0
Mvaa 224,136,0
Mvaa 228,140,0
W 4
Maaa 192,144,0
Maaa 196,148,0
Maaa 64,152,0
Maaa 68,156,0
Maaa 76,156,4
Ta 16,14,24576
Ta 18,14,25600
Maaa 72,152,4
Maaa 204,148,4
Maaa 200,144,4
Mvaa 236,140,4
Ta 20,15,24576
Ta 22,15,25600
Mvaa 232,136,4
Mvaa 172,132,4
Ta 24,24,24576
Ta 26,24,25600
Mvaa 168,128,4
W 8
Mvaa 176,128,8
Ta 28,25,24576
Ta 30,25,25600
Mvaa 180,132,8
Mvaa 240,136,8
Mvaa 244,140,8
Maaa 208,144,8
Maaa 212,148,8
Maaa 80,152,8
Maaa 84,156,8
W 8
Maaa 92,156,12
Maaa 88,152,12
Maaa 220,148,12
s_waitcnt vmcnt(2) lgkmcnt(0)
s_barrier
s_add_u32 s64, s64, 128
Maaa 216,144,12
Mvaa 252,140,12
Ra 32,26,32768
Mvaa 248,136,12
s_and_b32 s64, s64, 1023
Mvaa 188,132,12
Mvaa 184,128,12
Ra 36,26,34816
Mvaa 192,128,16
s_add_u32 m0, s61, 0
Mvaa 196,132,16
Maaa 160,136,16
Ra 40,26,36864
Maaa 164,140,16
D 2,40,64,0
Maaa 224,144,16
Maaa 228,148,16
Ra 44,26,38912
Maaa 96,152,16
Maaa 100,156,16
D 3,40,64,1024
Maaa 108,156,20
Ta 0,10,49152
Ta 2,10,50176
Maaa 104,152,20
Ra 48,26,40960
Maaa 236,148,20
Maaa 232,144,20
D 4,40,64,2048
Ra 52,26,43008
Maaa 172,140,20
Maaa 168,136,20
Mvaa 204,132,20
Ra 56,26,45056
Mvaa 200,128,20
D 5,40,64,3072
Mvaa 208,128,24
s_add_u32 m0, s61, 4096
Ta 4,11,49152
Ta 6,11,50176
Mvaa 212,132,24
Ra 60,26,47104
Maaa 176,136,24
Maaa 180,140,24
D 6,40,64,0
Maaa 240,144,24
Maaa 244,148,24
Maaa 112,152,24
Maaa 116,156,24
D 7,40,64,1024
Maaa 124,156,28
Ta 8,12,49152
Ta 10,12,50176
Maaa 120,152,28
Maaa 252,148,28
Maaa 248,144,28
D 8,40,64,2048
Maaa 188,140,28
Maaa 184,136,28
Mvaa 220,132,28
Mvaa 216,128,28
W 8
D 9,40,64,3072
Mvaa 160,32,0
s_lshl_b32 s65, s64, 12
s_add_u32 m0, s62, 0
Ta 12,13,49152
Mvaa 164,36,0
Ta 14,13,50176
Mvaa 224,40,0
Mvaa 228,44,0
W 4
D 16,44,65,0
Maaa 192,48,0
Maaa 196,52,0
Maaa 64,56,0
Maaa 68,60,0
D 17,44,65,1024
Maaa 76,60,4
Ta 16,14,49152
Ta 18,14,50176
Maaa 72,56,4
Maaa 204,52,4
Maaa 200,48,4
D 18,44,65,2048
Mvaa 236,44,4
Mvaa 232,40,4
Mvaa 172,36,4
Mvaa 168,32,4
W 4
D 19,44,65,3072
Mvaa 176,32,8
s_add_u32 m0, s62, 4096
Ta 20,15,49152
Ta 22,15,50176
Mvaa 180,36,8
Mvaa 240,40,8
Mvaa 244,44,8
D 20,44,65,0
Maaa 208,48,8
Maaa 212,52,8
Maaa 80,56,8
Maaa 84,60,8
W 4
D 21,44,65,1024
Maaa 92,60,12
Ta 24,24,49152
Ta 26,24,50176
Maaa 88,56,12
Maaa 220,52,12
Maaa 216,48,12
D 22,44,65,2048
Mvaa 252,44,12
Ra 128,27,32768
Mvaa 248,40,12
Mvaa 188,36,12
Mvaa 184,32,12
W 5
D 23,44,65,3072
Ra 132,27,34816
Mvaa 192,32,16
Ta 28,25,49152
Ta 30,25,50176
Mvaa 196,36,16
v_min_u32 v139, s75, v138
Maaa 160,40,16
Ra 136,27,36864
Maaa 164,44,16
buffer_load_dword v142, v144, s[8:11], s69 offen offset:2048 sc1
Maaa 224,48,16
Maaa 228,52,16
s_add_u32 s69, s69, 4096
Ra 140,27,38912
Maaa 96,56,16
Maaa 100,60,16
W 8
global_load_dword v142, v139, s[76:77]
Maaa 108,60,20
Ta 0,10,57344
Ta 2,10,58368
Maaa 104,56,20
Ra 144,27,40960
Maaa 236,52,20
v_add_u32 v138, s73, v138
Maaa 232,48,20
Ra 148,27,43008
Maaa 172,44,20
Maaa 168,40,20
Mvaa 204,36,20
Ra 152,27,45056
Mvaa 200,32,20
W 11
Mvaa 208,32,24
Ta 4,11,57344
Ta 6,11,58368
Mvaa 212,36,24
Ra 156,27,47104
Maaa 176,40,24
Maaa 180,44,24
Maaa 240,48,24
Maaa 244,52,24
Maaa 112,56,24
Maaa 116,60,24
W 10
Maaa 124,60,28
Ta 8,12,57344
Ta 10,12,58368
Maaa 120,56,28
Maaa 252,52,28
Maaa 248,48,28
Maaa 188,44,28
Maaa 184,40,28
Mvaa 220,36,28
Mvaa 216,32,28
W 8
Mvaa 160,128,0
Ta 12,13,57344
Ta 14,13,58368
Mvaa 164,132,0
Mvaa 224,136,0
Mvaa 228,140,0
W 4
Maaa 192,144,0
Maaa 196,148,0
Maaa 64,152,0
Maaa 68,156,0
Maaa 76,156,4
Ta 16,14,57344
Ta 18,14,58368
Maaa 72,152,4
Maaa 204,148,4
Maaa 200,144,4
Mvaa 236,140,4
Ta 20,15,57344
Ta 22,15,58368
Mvaa 232,136,4
Mvaa 172,132,4
Ta 24,24,57344
Ta 26,24,58368
Mvaa 168,128,4
W 8
Mvaa 176,128,8
Ta 28,25,57344
Ta 30,25,58368
Mvaa 180,132,8
Mvaa 240,136,8
Mvaa 244,140,8
Maaa 208,144,8
Maaa 212,148,8
Maaa 80,152,8
Maaa 84,156,8
W 8
Maaa 92,156,12
Maaa 88,152,12
Maaa 220,148,12
s_waitcnt vmcnt(2) lgkmcnt(0)
s_barrier
s_cmp_eq_u32 s85, 43
s_cbranch_scc1 .Lrt_22%=
.Lrtr_23%=:
Maaa 216,144,12
s_add_u32 s64, s64, 128
Mvaa 252,140,12
Ra 32,26,0
Mvaa 248,136,12
s_and_b32 s64, s64, 1023
Mvaa 188,132,12
Mvaa 184,128,12
Ra 36,26,2048
Mvaa 192,128,16
s_add_u32 m0, s61, 32768
Mvaa 196,132,16
Maaa 160,136,16
Ra 40,26,4096
Maaa 164,140,16
D 2,40,64,0
Maaa 224,144,16
Maaa 228,148,16
Ra 44,26,6144
Maaa 96,152,16
Maaa 100,156,16
D 3,40,64,1024
Maaa 108,156,20
Ta 0,10,16384
Ta 2,10,17408
Maaa 104,152,20
Ra 48,26,8192
Maaa 236,148,20
Maaa 232,144,20
D 4,40,64,2048
Ra 52,26,10240
Maaa 172,140,20
Maaa 168,136,20
Mvaa 204,132,20
Ra 56,26,12288
Mvaa 200,128,20
D 5,40,64,3072
Mvaa 208,128,24
s_add_u32 m0, s61, 36864
Ta 4,11,16384
Ta 6,11,17408
Mvaa 212,132,24
Ra 60,26,14336
Maaa 176,136,24
Maaa 180,140,24
D 6,40,64,0
Maaa 240,144,24
Maaa 244,148,24
Maaa 112,152,24
Maaa 116,156,24
D 7,40,64,1024
Maaa 124,156,28
Ta 8,12,16384
Ta 10,12,17408
Maaa 120,152,28
Maaa 252,148,28
Maaa 248,144,28
D 8,40,64,2048
Maaa 188,140,28
Maaa 184,136,28
Mvaa 220,132,28
Mvaa 216,128,28
W 8
D 9,40,64,3072
Mvaa 160,32,0
s_lshl_b32 s65, s64, 12
s_add_u32 m0, s62, 32768
Ta 12,13,16384
Mvaa 164,36,0
Ta 14,13,17408
Mvaa 224,40,0
Mvaa 228,44,0
W 4
D 16,44,65,0
Maaa 192,48,0
Maaa 196,52,0
Maaa 64,56,0
Maaa 68,60,0
D 17,44,65,1024
Maaa 76,60,4
Ta 16,14,16384
Ta 18,14,17408
Maaa 72,56,4
Maaa 204,52,4
Maaa 200,48,4
D 18,44,65,2048
Mvaa 236,44,4
Mvaa 232,40,4
Mvaa 172,36,4
Mvaa 168,32,4
W 4
D 19,44,65,3072
Mvaa 176,32,8
s_add_u32 m0, s62, 36864
Ta 20,15,16384
Ta 22,15,17408
Mvaa 180,36,8
Mvaa 240,40,8
Mvaa 244,44,8
D 20,44,65,0
Maaa 208,48,8
Maaa 212,52,8
Maaa 80,56,8
Maaa 84,60,8
W 4
D 21,44,65,1024
Maaa 92,60,12
Ta 24,24,16384
Ta 26,24,17408
Maaa 88,56,12
Maaa 220,52,12
Maaa 216,48,12
D 22,44,65,2048
Mvaa 252,44,12
Ra 128,27,0
Mvaa 248,40,12
Mvaa 188,36,12
Mvaa 184,32,12
W 5
D 23,44,65,3072
)"
R"(v_add_u32 v2, 131072, v10
Mvaa 192,32,16
Ra 132,27,2048
Ta 28,25,16384
Mvaa 196,36,16
Ta 30,25,17408
v_add_u32 v3, 131072, v11
Maaa 160,40,16
Ra 136,27,4096
v_add_u32 v4, 131072, v12
Maaa 164,44,16
buffer_load_dword v142, v144, s[8:11], s69 offen offset:0 sc1
Maaa 224,48,16
v_add_u32 v5, 131072, v13
Maaa 228,52,16
Ra 140,27,6144
Maaa 96,56,16
v_add_u32 v6, 131072, v14
Maaa 100,60,16
W 8
Maaa 108,60,20
Ta 0,10,24576
Ta 2,10,25600
Maaa 104,56,20
Ra 144,27,8192
v_add_u32 v7, 131072, v15
Maaa 236,52,20
v_add_u32 v8, 131072, v24
Maaa 232,48,20
Ra 148,27,10240
Maaa 172,44,20
v_add_u32 v9, 131072, v25
Maaa 168,40,20
Mvaa 204,36,20
Ra 152,27,12288
Mvaa 200,32,20
W 11
Mvaa 208,32,24
Ta 4,11,24576
Ta 6,11,25600
Mvaa 212,36,24
Ra 156,27,14336
Maaa 176,40,24
Maaa 180,44,24
Maaa 240,48,24
Maaa 244,52,24
Maaa 112,56,24
Maaa 116,60,24
W 10
Maaa 124,60,28
Ta 8,12,24576
Ta 10,12,25600
Maaa 120,56,28
Maaa 252,52,28
Maaa 248,48,28
Maaa 188,44,28
Maaa 184,40,28
Mvaa 220,36,28
Mvaa 216,32,28
W 8
Mvaa 160,128,0
Ta 12,13,24576
Ta 14,13,25600
Mvaa 164,132,0
Mvaa 224,136,0
Mvaa 228,140,0
W 4
Maaa 192,144,0
Maaa 196,148,0
Maaa 64,152,0
Maaa 68,156,0
Maaa 76,156,4
Ta 16,14,24576
Ta 18,14,25600
Maaa 72,152,4
Maaa 204,148,4
Maaa 200,144,4
Mvaa 236,140,4
Ta 20,15,24576
Ta 22,15,25600
Mvaa 232,136,4
Mvaa 172,132,4
Ta 24,24,24576
Ta 26,24,25600
Mvaa 168,128,4
W 8
Mvaa 176,128,8
Ta 28,25,24576
Ta 30,25,25600
Mvaa 180,132,8
Mvaa 240,136,8
Mvaa 244,140,8
Maaa 208,144,8
Maaa 212,148,8
Maaa 80,152,8
Maaa 84,156,8
W 8
Maaa 92,156,12
Maaa 88,152,12
Maaa 220,148,12
s_waitcnt vmcnt(1) lgkmcnt(0)
s_barrier
s_cmp_eq_u32 s85, 43
s_cbranch_scc1 .Lrt_24%=
.Lrtr_25%=:
Maaa 216,144,12
s_add_u32 s65, s5, 4
Mvaa 252,140,12
Ra 32,26,32768
Mvaa 248,136,12
s_cmp_ge_u32 s65, 20
s_cselect_b32 s68, 20, 0
Mvaa 188,132,12
Mvaa 184,128,12
s_sub_u32 s65, s65, s68
Ra 36,26,34816
Mvaa 192,128,16
Mvaa 196,132,16
s_lshl_b32 s65, s65, 18
Maaa 160,136,16
Ra 40,26,36864
Maaa 164,140,16
Maaa 224,144,16
s_add_u32 m0, s63, 0
Maaa 228,148,16
Ra 44,26,38912
Maaa 96,152,16
D 16,48,65,0
Maaa 100,156,16
Maaa 108,156,20
Ta 0,10,49152
Ta 2,10,50176
Maaa 104,152,20
Ra 48,26,40960
Maaa 236,148,20
D 17,48,65,1024
Maaa 232,144,20
Ra 52,26,43008
Maaa 172,140,20
Maaa 168,136,20
Mvaa 204,132,20
D 18,48,65,2048
Ra 56,26,45056
Mvaa 200,128,20
Mvaa 208,128,24
Ta 4,11,49152
Ta 6,11,50176
Mvaa 212,132,24
Ra 60,26,47104
Maaa 176,136,24
D 19,48,65,3072
Maaa 180,140,24
s_add_u32 m0, s63, 4096
Maaa 240,144,24
Maaa 244,148,24
Maaa 112,152,24
D 20,48,65,0
Maaa 116,156,24
Maaa 124,156,28
Ta 8,12,49152
Ta 10,12,50176
Maaa 120,152,28
Maaa 252,148,28
D 21,48,65,1024
Maaa 248,144,28
Maaa 188,140,28
Maaa 184,136,28
Mvaa 220,132,28
D 22,48,65,2048
Mvaa 216,128,28
W 8
Mvaa 160,32,0
Ta 12,13,49152
Ta 14,13,50176
Mvaa 164,36,0
Mvaa 224,40,0
D 23,48,65,3072
Mvaa 228,44,0
W 4
s_add_u32 s65, s5, 8
s_cmp_ge_u32 s65, 20
s_cselect_b32 s68, 20, 0
Maaa 192,48,0
s_sub_u32 s65, s65, s68
s_lshl_b32 s65, s65, 18
s_add_u32 m0, s63, 65536
Maaa 196,52,0
Maaa 64,56,0
D 16,48,65,0
Maaa 68,60,0
Maaa 76,60,4
Ta 16,14,49152
Ta 18,14,50176
Maaa 72,56,4
Maaa 204,52,4
D 17,48,65,1024
Maaa 200,48,4
Mvaa 236,44,4
Mvaa 232,40,4
Mvaa 172,36,4
D 18,48,65,2048
Mvaa 168,32,4
W 4
Mvaa 176,32,8
Ta 20,15,49152
Ta 22,15,50176
Mvaa 180,36,8
Mvaa 240,40,8
D 19,48,65,3072
Mvaa 244,44,8
s_add_u32 m0, s63, 69632
Maaa 208,48,8
Maaa 212,52,8
Maaa 80,56,8
D 20,48,65,0
Maaa 84,60,8
W 4
Maaa 92,60,12
Ta 24,24,49152
Ta 26,24,50176
Maaa 88,56,12
Maaa 220,52,12
D 21,48,65,1024
Maaa 216,48,12
Mvaa 252,44,12
Ra 128,27,32768
Mvaa 248,40,12
Mvaa 188,36,12
D 22,48,65,2048
Mvaa 184,32,12
W 5
Ra 132,27,34816
Mvaa 192,32,16
Ta 28,25,49152
Ta 30,25,50176
Mvaa 196,36,16
Maaa 160,40,16
D 23,48,65,3072
v_min_u32 v139, s75, v138
Maaa 164,44,16
Ra 136,27,36864
Maaa 224,48,16
Maaa 228,52,16
Ra 140,27,38912
Maaa 96,56,16
buffer_load_dword v142, v144, s[8:11], s69 offen offset:2048 sc1
Maaa 100,60,16
W 8
s_add_u32 s69, s69, 4096
Maaa 108,60,20
Ta 0,10,57344
Ta 2,10,58368
Maaa 104,56,20
Ra 144,27,40960
Maaa 236,52,20
global_load_dword v142, v139, s[76:77]
v_add_u32 v138, s73, v138
Maaa 232,48,20
Ra 148,27,43008
Maaa 172,44,20
Maaa 168,40,20
v_bfe_u32 v29, v1, 7, 1
Mvaa 204,36,20
Ra 152,27,45056
Mvaa 200,32,20
W 11
v_mul_u32_u24 v28, 10240, v29
Mvaa 208,32,24
Ta 4,11,57344
Ta 6,11,58368
Mvaa 212,36,24
Ra 156,27,47104
v_lshrrev_b32 v29, 8, v1
Maaa 176,40,24
Maaa 180,44,24
v_lshl_add_u32 v28, v29, 7, v28
Maaa 240,48,24
v_and_b32 v29, 0x70, v1
Maaa 244,52,24
Maaa 112,56,24
v_add_u32 v28, v28, v29
Maaa 116,60,24
W 10
Maaa 124,60,28
s_and_b32 s80, s14, 2
Ta 8,12,57344
Ta 10,12,58368
Maaa 120,56,28
s_mul_i32 s80, s80, 20480
Maaa 252,52,28
Maaa 248,48,28
s_sub_u32 s81, 40960, s80
Maaa 188,44,28
Maaa 184,40,28
s_add_u32 s93, s28, 20480
Mvaa 220,36,28
Mvaa 216,32,28
W 8
v_add_u32 v26, s80, v28
Mvaa 160,128,0
Ta 12,13,57344
Ta 14,13,58368
Mvaa 164,132,0
Mvaa 224,136,0
v_add_u32 v27, s81, v28
Mvaa 228,140,0
W 4
Maaa 192,144,0
buffer_load_dwordx4 a[32:35], v26, s[8:11], s28 offen offset:0 sc1
Maaa 196,148,0
Maaa 64,152,0
Maaa 68,156,0
Maaa 76,156,4
buffer_load_dwordx4 a[36:39], v26, s[8:11], s93 offen offset:0 sc1
Ta 16,14,57344
Maaa 72,152,4
Ta 18,14,58368
Maaa 204,148,4
Maaa 200,144,4
Mvaa 236,140,4
buffer_load_dwordx4 a[40:43], v27, s[8:11], s28 offen offset:0 sc1
Ta 20,15,57344
Mvaa 232,136,4
Ta 22,15,58368
Mvaa 172,132,4
Ta 24,24,57344
Ta 26,24,58368
Mvaa 168,128,4
W 8
Mvaa 176,128,8
buffer_load_dwordx4 a[44:47], v27, s[8:11], s93 offen offset:0 sc1
Ta 28,25,57344
Mvaa 180,132,8
s_add_u32 s28, s28, 512
s_cmp_eq_u32 s28, s4
s_cselect_b32 s93, 10240, 0
Ta 30,25,58368
Mvaa 240,136,8
s_sub_u32 s28, s28, s93
Mvaa 244,140,8
s_add_u32 s93, s28, 20480
Maaa 208,144,8
Maaa 212,148,8
Maaa 80,152,8
buffer_load_dwordx4 a[48:51], v26, s[8:11], s28 offen offset:0 sc1
Maaa 84,156,8
W 8
Maaa 92,156,12
Maaa 88,152,12
Maaa 220,148,12
s_waitcnt vmcnt(7) lgkmcnt(0)
s_barrier
buffer_load_dwordx4 a[52:55], v26, s[8:11], s93 offen offset:0 sc1
Maaa 216,144,12
Mvaa 252,140,12
Mvaa 248,136,12
Mvaa 188,132,12
buffer_load_dwordx4 a[56:59], v27, s[8:11], s28 offen offset:0 sc1
Mvaa 184,128,12
Mvaa 192,128,16
Mvaa 196,132,16
Maaa 160,136,16
s_add_u32 s65, s5, 12
buffer_load_dwordx4 a[60:63], v27, s[8:11], s93 offen offset:0 sc1
Maaa 164,140,16
Maaa 224,144,16
Maaa 228,148,16
Maaa 96,152,16
Maaa 100,156,16
Maaa 108,156,20
Maaa 104,152,20
Maaa 236,148,20
s_cmp_ge_u32 s65, 20
s_cselect_b32 s68, 20, 0
Ta 0,2,0
Ta 2,2,1024
Maaa 232,144,20
Maaa 172,140,20
Maaa 168,136,20
Mvaa 204,132,20
Ta 4,3,0
Ta 6,3,1024
Mvaa 200,128,20
Mvaa 208,128,24
Mvaa 212,132,24
s_sub_u32 s65, s65, s68
Maaa 176,136,24
Ta 8,4,0
Ta 10,4,1024
Maaa 180,140,24
Maaa 240,144,24
Maaa 244,148,24
Maaa 112,152,24
Ta 12,5,0
Ta 14,5,1024
Maaa 116,156,24
Maaa 124,156,28
s_lshl_b32 s65, s65, 18
Maaa 120,152,28
Maaa 252,148,28
)"
R"(Ta 16,6,0
Ta 18,6,1024
Maaa 248,144,28
Maaa 188,140,28
Maaa 184,136,28
Mvaa 220,132,28
Mvaa 216,128,28
s_add_u32 m0, s63, 32768
.Lq_11%=:
s_cmp_ge_u32 s14, 2
s_cbranch_scc1 .Lpb_30%=
s_waitcnt vmcnt(4) lgkmcnt(6)
Zvaa 32,0,32
s_add_u32 s28, s28, 512
Ta 20,7,0
Ta 22,7,1024
Zvaa 64,0,36
s_cmp_eq_u32 s28, s4
s_cselect_b32 s93, 10240, 0
Ta 24,8,0
Ta 26,8,1024
Zvaa 96,0,40
s_sub_u32 s28, s28, s93
s_add_u32 s93, s28, 20480
buffer_load_dwordx4 a[128:131], v26, s[8:11], s28 offen offset:0 sc1
Zvaa 128,0,44
Ta 28,9,0
Ta 30,9,1024
Zvaa 132,4,44
Zvaa 100,4,40
Zvaa 68,4,36
buffer_load_dwordx4 a[132:135], v26, s[8:11], s93 offen offset:0 sc1
Ta 0,2,8192
Zvaa 36,4,32
W 9
Ta 2,2,9216
Zvaa 40,8,32
Zvaa 72,8,36
Zvaa 104,8,40
buffer_load_dwordx4 a[136:139], v27, s[8:11], s28 offen offset:0 sc1
Ta 4,3,8192
Zvaa 136,8,44
Ta 6,3,9216
K 160,160,161
Zvaa 140,12,44
K 161,162,163
Zvaa 108,12,40
Zvaa 76,12,36
buffer_load_dwordx4 a[140:143], v27, s[8:11], s93 offen offset:0 sc1
Ta 8,4,8192
Zvaa 44,12,32
W 9
s_add_u32 s28, s28, 512
s_cmp_eq_u32 s28, s4
s_cselect_b32 s93, 10240, 0
Zvaa 48,16,32
s_sub_u32 s28, s28, s93
s_add_u32 s93, s28, 20480
Ta 10,4,9216
Zvaa 80,16,36
K 162,164,165
K 163,166,167
Zvaa 112,16,40
s_add_u32 s36, s29, 0
buffer_load_dwordx4 a[144:147], v26, s[8:11], s28 offen offset:0 sc1
Ta 12,5,8192
Zvaa 144,16,44
Ta 14,5,9216
Zvaa 148,20,44
Zvaa 116,20,40
Zvaa 84,20,36
buffer_load_dwordx4 a[148:151], v26, s[8:11], s93 offen offset:0 sc1
Ta 16,6,8192
Zvaa 52,20,32
W 9
Ta 18,6,9216
Zvaa 56,24,32
Zvaa 88,24,36
Zvaa 120,24,40
buffer_load_dwordx4 a[152:155], v27, s[8:11], s28 offen offset:0 sc1
Ta 20,7,8192
Zvaa 152,24,44
Ta 22,7,9216
Zvaa 156,28,44
Zvaa 124,28,40
Zvaa 92,28,36
buffer_store_dwordx4 v[160:163], v1, s[8:11], s36 offen offset:0 sc1
K 168,168,169
Zvaa 60,28,32
s_waitcnt vmcnt(8) lgkmcnt(8)
Ta 24,8,8192
Ta 26,8,9216
Mvaa 32,0,48
K 169,170,171
Mvaa 64,0,52
Mvaa 96,0,56
buffer_load_dwordx4 a[156:159], v27, s[8:11], s93 offen offset:0 sc1
K 170,172,173
Mvaa 128,0,60
s_add_u32 s28, s28, 512
s_cmp_eq_u32 s28, s4
s_cselect_b32 s93, 10240, 0
s_sub_u32 s28, s28, s93
Mvaa 132,4,60
s_add_u32 s93, s28, 20480
Ta 28,9,8192
Ta 30,9,9216
Mvaa 100,4,56
K 171,174,175
Mvaa 68,4,52
D 16,48,65,0
Ta 0,2,16384
Mvaa 36,4,48
W 9
Ta 2,2,17408
Mvaa 40,8,48
Mvaa 72,8,52
Mvaa 104,8,56
D 17,48,65,1024
Ta 4,3,16384
Mvaa 136,8,60
Ta 6,3,17408
Mvaa 140,12,60
Mvaa 108,12,56
Mvaa 76,12,52
D 18,48,65,2048
Ta 8,4,16384
Mvaa 44,12,48
W 9
Ta 10,4,17408
Mvaa 48,16,48
Mvaa 80,16,52
buffer_store_dwordx4 v[168:171], v1, s[8:11], s36 offen offset:1024 sc1
K 176,176,177
Mvaa 112,16,56
Ta 12,5,16384
K 177,178,179
Mvaa 144,16,60
Ta 14,5,17408
Mvaa 148,20,60
K 178,180,181
Mvaa 116,20,56
D 19,48,65,3072
Mvaa 84,20,52
s_add_u32 m0, s63, 36864
Ta 16,6,16384
Ta 18,6,17408
Mvaa 52,20,48
W 10
K 179,182,183
Mvaa 56,24,48
Mvaa 88,24,52
buffer_load_dwordx4 a[32:35], v26, s[8:11], s28 offen offset:0 sc1
Mvaa 120,24,56
Ta 20,7,16384
Ta 22,7,17408
Mvaa 152,24,60
Mvaa 156,28,60
Mvaa 124,28,56
buffer_load_dwordx4 a[36:39], v26, s[8:11], s93 offen offset:0 sc1
Mvaa 92,28,52
Ta 24,8,16384
Ta 26,8,17408
Mvaa 60,28,48
s_waitcnt vmcnt(12) lgkmcnt(10)
Mvaa 32,0,128
Mvaa 64,0,132
buffer_load_dwordx4 a[40:43], v27, s[8:11], s28 offen offset:0 sc1
Mvaa 96,0,136
Ta 28,9,16384
Ta 30,9,17408
Mvaa 128,0,140
buffer_store_dwordx4 v[176:179], v1, s[8:11], s36 offen offset:2048 sc1
K 184,184,185
Mvaa 132,4,140
Mvaa 100,4,136
K 185,186,187
Mvaa 68,4,132
Ta 0,2,24576
Ta 2,2,25600
Mvaa 36,4,128
W 10
buffer_load_dwordx4 a[44:47], v27, s[8:11], s93 offen offset:0 sc1
K 186,188,189
Mvaa 40,8,128
s_add_u32 s28, s28, 512
s_cmp_eq_u32 s28, s4
s_cselect_b32 s93, 10240, 0
s_sub_u32 s28, s28, s93
Mvaa 72,8,132
s_add_u32 s93, s28, 20480
Mvaa 104,8,136
Ta 4,3,24576
K 187,190,191
Mvaa 136,8,140
D 20,48,65,0
Ta 6,3,25600
Mvaa 140,12,140
Mvaa 108,12,136
Mvaa 76,12,132
Ta 8,4,24576
Ta 10,4,25600
Mvaa 44,12,128
W 10
D 21,48,65,1024
Mvaa 48,16,128
Mvaa 80,16,132
Mvaa 112,16,136
Ta 12,5,24576
Ta 14,5,25600
Mvaa 144,16,140
D 22,48,65,2048
Mvaa 148,20,140
Mvaa 116,20,136
Mvaa 84,20,132
buffer_store_dwordx4 v[184:187], v1, s[8:11], s36 offen offset:3072 sc1
K 192,192,193
Mvaa 52,20,128
W 8
Ta 16,6,24576
Ta 18,6,25600
Mvaa 56,24,128
K 193,194,195
K 194,196,197
Mvaa 88,24,132
Mvaa 120,24,136
D 23,48,65,3072
Ta 20,7,24576
Mvaa 152,24,140
s_add_u32 s65, s5, 16
s_cmp_ge_u32 s65, 20
s_cselect_b32 s68, 20, 0
s_sub_u32 s65, s65, s68
Mvaa 156,28,140
s_lshl_b32 s65, s65, 18
Ta 22,7,25600
K 195,198,199
Mvaa 124,28,136
s_add_u32 s36, s29, 4096
Mvaa 92,28,132
buffer_load_dwordx4 a[48:51], v26, s[8:11], s28 offen offset:0 sc1
Ta 24,8,24576
Mvaa 60,28,128
s_waitcnt vmcnt(16) lgkmcnt(9)
Ta 26,8,25600
Mvaa 32,0,144
Mvaa 64,0,148
Mvaa 96,0,152
buffer_load_dwordx4 a[52:55], v26, s[8:11], s93 offen offset:0 sc1
Ta 28,9,24576
Mvaa 128,0,156
s_add_u32 m0, s63, 98304
Ta 30,9,25600
Mvaa 132,4,156
s_lshl_b32 s80, s56, 9
Mvaa 100,4,152
Mvaa 68,4,148
s_lshl_b32 s81, s57, 4
buffer_load_dwordx4 a[56:59], v27, s[8:11], s28 offen offset:0 sc1
Ta 0,10,0
Mvaa 36,4,144
W 9
s_add_u32 s80, s80, s81
Ta 2,10,1024
Mvaa 40,8,144
buffer_store_dwordx4 v[192:195], v1, s[8:11], s36 offen offset:0 sc1
K 200,200,201
Mvaa 72,8,148
s_and_b32 s81, s14, 1
Mvaa 104,8,152
s_lshl_b32 s81, s81, 3
Ta 4,11,0
K 201,202,203
Mvaa 136,8,156
Ta 6,11,1024
Mvaa 140,12,156
s_add_u32 s80, s80, s81
buffer_load_dwordx4 a[60:63], v27, s[8:11], s93 offen offset:0 sc1
K 202,204,205
Mvaa 108,12,152
s_add_u32 s28, s28, 512
s_cmp_eq_u32 s28, s4
s_cselect_b32 s93, 10240, 0
s_sub_u32 s28, s28, s93
Mvaa 76,12,148
s_add_u32 s93, s28, 20480
s_add_u32 s71, s80, 4096
Ta 8,12,0
Mvaa 44,12,144
W 9
Ta 10,12,1024
K 203,206,207
Mvaa 48,16,144
buffer_load_dwordx4 v[184:187], v26, s[8:11], s28 offen offset:0 sc1
Mvaa 80,16,148
Mvaa 112,16,152
Ta 12,13,0
Ta 14,13,1024
Mvaa 144,16,156
Mvaa 148,20,156
buffer_load_dwordx4 v[188:191], v26, s[8:11], s93 offen offset:0 sc1
Mvaa 116,20,152
Mvaa 84,20,148
Ta 16,14,0
Ta 18,14,1024
Mvaa 52,20,144
W 10
Mvaa 56,24,144
buffer_load_dwordx4 a[128:131], v27, s[8:11], s28 offen offset:0 sc1
Mvaa 88,24,148
Mvaa 120,24,152
Ta 20,15,0
Ta 22,15,1024
Mvaa 152,24,156
buffer_store_dwordx4 v[200:203], v1, s[8:11], s36 offen offset:1024 sc1
K 208,208,209
Mvaa 156,28,156
K 209,210,211
Mvaa 124,28,152
Mvaa 92,28,148
Ta 24,24,0
K 210,212,213
Mvaa 60,28,144
s_waitcnt vmcnt(14) lgkmcnt(9)
buffer_load_dwordx4 a[132:135], v27, s[8:11], s93 offen offset:0 sc1
Ta 26,24,1024
Mvaa 32,0,32
s_add_u32 s28, s28, 512
s_cmp_eq_u32 s28, s4
s_cselect_b32 s93, 10240, 0
s_sub_u32 s28, s28, s93
Mvaa 64,0,36
s_add_u32 s93, s28, 20480
K 211,214,215
Mvaa 96,0,40
Ta 28,25,0
Ta 30,25,1024
Mvaa 128,0,44
buffer_load_dwordx4 a[136:139], v26, s[8:11], s28 offen offset:0 sc1
Mvaa 132,4,44
Mvaa 100,4,40
Mvaa 68,4,36
Ta 0,10,8192
Ta 2,10,9216
Mvaa 36,4,32
W 10
buffer_load_dwordx4 a[140:143], v26, s[8:11], s93 offen offset:0 sc1
v_add_u32 v2, 65536, v10
Mvaa 40,8,32
Mvaa 72,8,36
Mvaa 104,8,40
Ta 4,11,8192
Ta 6,11,9216
Mvaa 136,8,44
buffer_load_dwordx4 v[192:195], v27, s[8:11], s28 offen offset:0 sc1
v_add_u32 v3, 65536, v11
Mvaa 140,12,44
Mvaa 108,12,40
buffer_store_dwordx4 v[208:211], v1, s[8:11], s36 offen offset:2048 sc1
K 216,216,217
Mvaa 76,12,36
Ta 8,12,8192
Ta 10,12,9216
Mvaa 44,12,32
W 10
v_add_u32 v4, 65536, v12
K 217,218,219
Mvaa 48,16,32
Mvaa 80,16,36
buffer_load_dwordx4 v[196:199], v27, s[8:11], s93 offen offset:0 sc1
K 218,220,221
Mvaa 112,16,40
s_add_u32 s28, s28, 512
s_cmp_eq_u32 s28, s4
s_cselect_b32 s93, 10240, 0
s_sub_u32 s28, s28, s93
Mvaa 144,16,44
s_add_u32 s93, s28, 20480
Ta 12,13,8192
Ta 14,13,9216
Mvaa 148,20,44
v_add_u32 v5, 65536, v13
K 219,222,223
Mvaa 116,20,40
D 16,48,65,0
Mvaa 84,20,36
Ta 16,14,8192
Ta 18,14,9216
Mvaa 52,20,32
W 10
v_add_u32 v6, 65536, v14
Mvaa 56,24,32
Mvaa 88,24,36
D 17,48,65,1024
Mvaa 120,24,40
Ta 20,15,8192
Ta 22,15,9216
Mvaa 152,24,44
v_add_u32 v7, 65536, v15
Mvaa 156,28,44
)"
R"(Mvaa 124,28,40
D 18,48,65,2048
Mvaa 92,28,36
Ta 24,24,8192
Ta 26,24,9216
Mvaa 60,28,32
s_waitcnt vmcnt(13) lgkmcnt(10)
v_add_u32 v8, 65536, v24
Mvaa 32,0,48
buffer_store_dwordx4 v[216:219], v1, s[8:11], s36 offen offset:3072 sc1
K 224,224,225
Mvaa 64,0,52
K 225,226,227
Mvaa 96,0,56
Ta 28,25,8192
Ta 30,25,9216
Mvaa 128,0,60
v_add_u32 v9, 65536, v25
K 226,228,229
Mvaa 132,4,60
buffer_load_dwordx4 v[200:203], v26, s[8:11], s28 offen offset:0 sc1
K 227,230,231
Mvaa 100,4,56
Mvaa 68,4,52
s_add_u32 s36, s29, 458752
Ta 0,10,16384
Ta 2,10,17408
Mvaa 36,4,48
W 10
Mvaa 40,8,48
buffer_load_dwordx4 v[204:207], v26, s[8:11], s93 offen offset:0 sc1
Mvaa 72,8,52
Mvaa 104,8,56
Ta 4,11,16384
Ta 6,11,17408
Mvaa 136,8,60
Mvaa 140,12,60
buffer_load_dwordx4 a[144:147], v27, s[8:11], s28 offen offset:0 sc1
Mvaa 108,12,56
Mvaa 76,12,52
Ta 8,12,16384
Ta 10,12,17408
Mvaa 44,12,48
W 10
Mvaa 48,16,48
buffer_load_dwordx4 a[148:151], v27, s[8:11], s93 offen offset:0 sc1
Mvaa 80,16,52
s_add_u32 s28, s28, 512
s_cmp_eq_u32 s28, s4
s_cselect_b32 s93, 10240, 0
s_sub_u32 s28, s28, s93
Mvaa 112,16,56
s_add_u32 s93, s28, 20480
buffer_store_dwordx4 v[224:227], v1, s[8:11], s36 offen offset:0 sc1
K 232,232,233
Mvaa 144,16,60
Ta 12,13,16384
Ta 14,13,17408
Mvaa 148,20,60
K 233,234,235
Mvaa 116,20,56
Mvaa 84,20,52
D 19,48,65,3072
K 234,236,237
Mvaa 52,20,48
W 8
s_add_u32 m0, s63, 102400
Ta 16,14,16384
Mvaa 56,24,48
Ta 18,14,17408
Mvaa 88,24,52
K 235,238,239
Mvaa 120,24,56
buffer_load_dwordx4 a[152:155], v26, s[8:11], s28 offen offset:0 sc1
Ta 20,15,16384
Mvaa 152,24,60
Ta 22,15,17408
Mvaa 156,28,60
Mvaa 124,28,56
Mvaa 92,28,52
buffer_load_dwordx4 a[156:159], v26, s[8:11], s93 offen offset:0 sc1
Ta 24,24,16384
Mvaa 60,28,48
s_waitcnt vmcnt(17) lgkmcnt(9)
Ta 26,24,17408
Mvav 32,0,184
Mvav 64,0,188
Mvaa 96,0,128
D 20,48,65,0
Ta 28,25,16384
Mvaa 128,0,132
Ta 30,25,17408
Mvaa 132,4,132
Mvaa 100,4,128
buffer_store_dwordx4 v[232:235], v1, s[8:11], s36 offen offset:1024 sc1
K 240,240,241
Mvav 68,4,188
Ta 0,10,24576
K 241,242,243
Mvav 36,4,184
W 9
Ta 2,10,25600
Mvav 40,8,184
K 242,244,245
Mvav 72,8,188
buffer_load_dwordx4 v[208:211], v27, s[8:11], s28 offen offset:0 sc1
Mvaa 104,8,128
Ta 4,11,24576
Ta 6,11,25600
Mvaa 136,8,132
K 243,246,247
Mvaa 140,12,132
Mvaa 108,12,128
D 21,48,65,1024
Mvav 76,12,188
Ta 8,12,24576
Ta 10,12,25600
Mvav 44,12,184
W 10
Mvav 48,16,184
Mvav 80,16,188
buffer_load_dwordx4 v[212:215], v27, s[8:11], s93 offen offset:0 sc1
Mvaa 112,16,128
s_add_u32 s28, s28, 512
s_cmp_eq_u32 s28, s4
s_cselect_b32 s93, 10240, 0
s_sub_u32 s28, s28, s93
Mvaa 144,16,132
s_add_u32 s93, s28, 20480
Ta 12,13,24576
Ta 14,13,25600
Mvaa 148,20,132
Mvaa 116,20,128
D 22,48,65,2048
Mvav 84,20,188
Ta 16,14,24576
Ta 18,14,25600
Mvav 52,20,184
buffer_store_dwordx4 v[240:243], v1, s[8:11], s36 offen offset:2048 sc1
W 10
Mvav 56,24,184
K 248,248,249
Mvav 88,24,188
K 249,250,251
Mvaa 120,24,128
Ta 20,15,24576
Ta 22,15,25600
Mvaa 152,24,132
buffer_load_dwordx4 a[32:35], v26, s[8:11], s28 offen offset:0 sc1
K 250,252,253
Mvaa 156,28,132
Mvaa 124,28,128
Mvav 92,28,188
Ta 24,24,24576
K 251,254,255
Mvav 60,28,184
s_waitcnt vmcnt(20) lgkmcnt(9)
D 23,48,65,3072
Ta 26,24,25600
Mvaa 32,0,136
Mvaa 64,0,140
Mvav 96,0,192
Ta 28,25,24576
Ta 30,25,25600
Mvav 128,0,196
buffer_load_dwordx4 a[36:39], v26, s[8:11], s93 offen offset:0 sc1
Mvav 132,4,196
Mvav 100,4,192
Mvaa 68,4,140
Ta 0,2,0
Ta 2,2,1024
Mvaa 36,4,136
W 10
buffer_load_dwordx4 a[40:43], v27, s[8:11], s28 offen offset:0 sc1
Mvaa 40,8,136
Mvaa 72,8,140
Mvav 104,8,192
buffer_store_dwordx4 v[248:251], v1, s[8:11], s36 offen offset:3072 sc1
G 160,160
Mvav 136,8,196
G 161,161
G 162,162
Mvav 140,12,196
Ta 4,3,0
Ta 6,3,1024
Mvav 108,12,192
G 163,163
G 164,164
Mvaa 76,12,140
buffer_load_dwordx4 a[44:47], v27, s[8:11], s93 offen offset:0 sc1
G 165,165
Mvaa 44,12,136
W 8
s_add_u32 s28, s28, 512
s_cmp_eq_u32 s28, s4
s_cselect_b32 s93, 10240, 0
Mvaa 48,16,136
s_sub_u32 s28, s28, s93
s_add_u32 s93, s28, 20480
G 166,166
Mvaa 80,16,140
Ta 8,4,0
Ta 10,4,1024
Mvav 112,16,192
buffer_load_dwordx4 v[216:219], v26, s[8:11], s28 offen offset:0 sc1
G 167,167
Mvav 144,16,196
K 160,160,161
K 161,162,163
Mvav 148,20,196
K 162,164,165
K 163,166,167
Mvav 116,20,192
s_add_u32 s36, s29, 462848
Ta 12,5,0
Ta 14,5,1024
Mvaa 84,20,140
buffer_load_dwordx4 v[220:223], v26, s[8:11], s93 offen offset:0 sc1
Ta 16,6,0
Mvaa 52,20,136
W 9
Ta 18,6,1024
Mvaa 56,24,136
Mvaa 88,24,140
Mvav 120,24,192
buffer_load_dwordx4 v[224:227], v27, s[8:11], s28 offen offset:0 sc1
Ta 20,7,0
Mvav 152,24,196
Ta 22,7,1024
Mvav 156,28,196
buffer_store_dwordx4 v[160:163], v1, s[8:11], s36 offen offset:0 sc1
G 168,168
Mvav 124,28,192
G 169,169
G 170,170
Mvaa 92,28,140
Ta 24,8,0
G 171,171
Mvaa 60,28,136
s_waitcnt vmcnt(21) lgkmcnt(9)
Ta 26,8,1024
G 172,172
Mvav 32,0,200
buffer_load_dwordx4 v[228:231], v27, s[8:11], s93 offen offset:0 sc1
G 173,173
Mvav 64,0,204
s_add_u32 s28, s28, 512
s_cmp_eq_u32 s28, s4
s_cselect_b32 s93, 10240, 0
s_sub_u32 s28, s28, s93
Mvaa 96,0,144
s_add_u32 s93, s28, 20480
G 174,174
G 175,175
Mvaa 128,0,148
Ta 28,9,0
Ta 30,9,1024
Mvaa 132,4,148
buffer_load_dwordx4 a[48:51], v26, s[8:11], s28 offen offset:0 sc1
K 168,168,169
Mvaa 100,4,144
K 169,170,171
K 170,172,173
Mvav 68,4,204
Ta 0,2,8192
K 171,174,175
Mvav 36,4,200
W 9
Ta 2,2,9216
Mvav 40,8,200
buffer_load_dwordx4 a[52:55], v26, s[8:11], s93 offen offset:0 sc1
Mvav 72,8,204
Mvaa 104,8,144
Ta 4,3,8192
Ta 6,3,9216
Mvaa 136,8,148
Mvaa 140,12,148
buffer_load_dwordx4 a[56:59], v27, s[8:11], s28 offen offset:0 sc1
Mvaa 108,12,144
Mvav 76,12,204
Ta 8,4,8192
Ta 10,4,9216
Mvav 44,12,200
buffer_store_dwordx4 v[168:171], v1, s[8:11], s36 offen offset:1024 sc1
W 10
Mvav 48,16,200
G 176,176
G 177,177
Mvav 80,16,204
G 178,178
G 179,179
Mvaa 112,16,144
G 180,180
G 181,181
Mvaa 144,16,148
buffer_load_dwordx4 a[60:63], v27, s[8:11], s93 offen offset:0 sc1
Ta 12,5,8192
Mvaa 148,20,148
s_add_u32 s28, s28, 512
s_cmp_eq_u32 s28, s4
s_cselect_b32 s93, 10240, 0
s_sub_u32 s28, s28, s93
Mvaa 116,20,144
s_add_u32 s93, s28, 20480
Ta 14,5,9216
v_lshrrev_b32 v20, 4, v1
Mvav 84,20,204
G 182,182
G 183,183
Mvav 52,20,200
W 8
buffer_load_dwordx4 v[232:235], v26, s[8:11], s28 offen offset:0 sc1
v_add_u32 v20, 0, v20
Mvav 56,24,200
K 176,176,177
K 177,178,179
Mvav 88,24,204
Ta 16,6,8192
v_mul_u32_u24 v22, 205, v20
Mvaa 120,24,144
Ta 18,6,9216
K 178,180,181
Mvaa 152,24,148
buffer_load_dwordx4 v[236:239], v26, s[8:11], s93 offen offset:0 sc1
K 179,182,183
Mvaa 156,28,148
v_lshrrev_b32 v22, 12, v22
v_mul_u32_u24 v23, 20, v22
Mvaa 124,28,144
Ta 20,7,8192
Ta 22,7,9216
Mvav 92,28,204
v_sub_u32 v20, v20, v23
v_lshrrev_b32 v23, 1, v22
Mvav 60,28,200
s_waitcnt vmcnt(20) lgkmcnt(8)
buffer_load_dwordx4 v[184:187], v27, s[8:11], s28 offen offset:0 sc1
Ta 24,8,8192
Mvaa 32,0,152
Ta 26,8,9216
v_and_b32 v23, 2, v23
Mvaa 64,0,156
buffer_store_dwordx4 v[176:179], v1, s[8:11], s36 offen offset:2048 sc1
G 160,184
Mvav 96,0,208
G 161,185
v_xor_b32 v20, v20, v23
Mvav 128,0,212
Ta 28,9,8192
G 162,186
Mvav 132,4,212
Ta 30,9,9216
G 163,187
Mvav 100,4,208
buffer_load_dwordx4 v[188:191], v27, s[8:11], s93 offen offset:0 sc1
v_mul_u32_u24 v22, 80, v22
Mvaa 68,4,156
s_add_u32 s28, s28, 512
s_cmp_eq_u32 s28, s4
s_cselect_b32 s93, 10240, 0
s_sub_u32 s28, s28, s93
Mvaa 36,4,152
W 8
s_add_u32 s93, s28, 20480
G 164,188
Mvaa 40,8,152
G 165,189
v_add_lshl_u32 v16, v20, v22, 4
Mvaa 72,8,156
buffer_load_dwordx4 a[128:131], v26, s[8:11], s28 offen offset:0 sc1
G 166,190
Mvav 104,8,208
Ta 0,2,16384
Ta 2,2,17408
Mvav 136,8,212
G 167,191
v_lshrrev_b32 v20, 4, v1
Mvav 140,12,212
K 160,160,161
K 161,162,163
Mvav 108,12,208
buffer_load_dwordx4 a[132:135], v26, s[8:11], s93 offen offset:0 sc1
v_add_u32 v20, 64, v20
Mvaa 76,12,156
Ta 4,3,16384
K 162,164,165
Mvaa 44,12,152
W 7
Ta 6,3,17408
K 163,166,167
Mvaa 48,16,152
v_mul_u32_u24 v22, 205, v20
v_lshrrev_b32 v22, 12, v22
Mvaa 80,16,156
buffer_store_dwordx4 v[160:163], v1, s[8:11], s36 offen offset:3072 sc1
Ta 8,4,16384
Mvav 112,16,208
Ta 10,4,17408
v_mul_u32_u24 v23, 20, v22
Mvav 144,16,212
)"
R"(Ta 12,5,16384
v_sub_u32 v20, v20, v23
Mvav 148,20,212
Ta 14,5,17408
G 168,192
Mvav 116,20,208
buffer_load_dwordx4 a[136:139], v27, s[8:11], s28 offen offset:0 sc1
v_lshrrev_b32 v23, 1, v22
Mvaa 84,20,156
G 169,193
G 170,194
Mvaa 52,20,152
W 8
v_and_b32 v23, 2, v23
G 171,195
Mvaa 56,24,152
G 172,196
v_xor_b32 v20, v20, v23
Mvaa 88,24,156
Ta 16,6,16384
Ta 18,6,17408
Mvav 120,24,208
G 173,197
G 174,198
Mvav 152,24,212
buffer_load_dwordx4 a[140:143], v27, s[8:11], s93 offen offset:0 sc1
v_mul_u32_u24 v22, 80, v22
Mvav 156,28,212
G 175,199
K 168,168,169
Mvav 124,28,208
v_add_lshl_u32 v17, v20, v22, 4
K 169,170,171
Mvaa 92,28,156
Ta 20,7,16384
Ta 22,7,17408
Mvaa 60,28,152
s_waitcnt vmcnt(20) lgkmcnt(8)
K 170,172,173
K 171,174,175
Mvaa 32,0,32
s_add_u32 s36, s29, 65536
buffer_store_dwordx4 v[168:171], v1, s[8:11], s36 offen offset:0 sc1
v_subrev_u32 v17, 1024, v17
Mvaa 64,0,36
s_add_u32 s28, s28, 512
Ta 24,8,16384
v_lshrrev_b32 v20, 4, v1
Mvaa 96,0,40
s_cmp_eq_u32 s28, s4
s_cselect_b32 s93, 10240, 0
Ta 26,8,17408
v_add_u32 v20, 128, v20
Mvaa 128,0,44
s_sub_u32 s28, s28, s93
s_add_u32 s93, s28, 20480
v_mul_u32_u24 v22, 205, v20
Mvaa 132,4,44
buffer_load_dwordx4 v[192:195], v26, s[8:11], s28 offen offset:0 sc1
Ta 28,9,16384
Mvaa 100,4,40
Ta 30,9,17408
v_lshrrev_b32 v22, 12, v22
Mvaa 68,4,36
G 176,200
v_mul_u32_u24 v23, 20, v22
Mvaa 36,4,32
W 8
Ta 0,2,24576
G 177,201
Mvaa 40,8,32
Ta 2,2,25600
G 178,202
Mvaa 72,8,36
v_sub_u32 v20, v20, v23
G 179,203
Mvaa 104,8,40
buffer_load_dwordx4 v[196:199], v26, s[8:11], s93 offen offset:0 sc1
G 180,204
Mvaa 136,8,44
v_lshrrev_b32 v23, 1, v22
G 181,205
Mvaa 140,12,44
Ta 4,3,24576
Ta 6,3,25600
Mvaa 108,12,40
G 182,206
v_and_b32 v23, 2, v23
Mvaa 76,12,36
G 183,207
K 176,176,177
Mvaa 44,12,32
W 8
v_xor_b32 v20, v20, v23
K 177,178,179
Mvaa 48,16,32
Ta 8,4,24576
Ta 10,4,25600
Mvaa 80,16,36
K 178,180,181
v_mul_u32_u24 v22, 80, v22
Mvaa 112,16,40
buffer_load_dwordx4 v[200:203], v27, s[8:11], s28 offen offset:0 sc1
K 179,182,183
Mvaa 144,16,44
Ta 12,5,24576
v_add_lshl_u32 v18, v20, v22, 4
Mvaa 148,20,44
Ta 14,5,25600
v_subrev_u32 v18, 2048, v18
Mvaa 116,20,40
v_lshrrev_b32 v20, 4, v1
Mvaa 84,20,36
buffer_store_dwordx4 v[176:179], v1, s[8:11], s36 offen offset:1024 sc1
Ta 16,6,24576
Mvaa 52,20,32
W 9
Ta 18,6,25600
v_add_u32 v20, 192, v20
Mvaa 56,24,32
v_mul_u32_u24 v22, 205, v20
G 160,208
Mvaa 88,24,36
G 161,209
Mvaa 120,24,40
buffer_load_dwordx4 v[204:207], v27, s[8:11], s93 offen offset:0 sc1
v_lshrrev_b32 v22, 12, v22
Mvaa 152,24,44
Ta 20,7,24576
G 162,210
Mvaa 156,28,44
Ta 22,7,25600
G 163,211
Mvaa 124,28,40
G 164,212
v_mul_u32_u24 v23, 20, v22
Mvaa 92,28,36
G 165,213
G 166,214
Mvaa 60,28,32
s_waitcnt vmcnt(21) lgkmcnt(8)
Ta 24,8,24576
v_sub_u32 v20, v20, v23
Mvav 32,0,216
s_add_u32 s28, s28, 512
Ta 26,8,25600
G 167,215
Mvav 64,0,220
s_cmp_eq_u32 s28, s4
s_cselect_b32 s93, 10240, 0
K 160,160,161
v_lshrrev_b32 v23, 1, v22
Mvav 96,0,224
s_sub_u32 s28, s28, s93
s_add_u32 s93, s28, 20480
buffer_load_dwordx4 a[144:147], v26, s[8:11], s28 offen offset:0 sc1
Mvav 128,0,228
K 161,162,163
K 162,164,165
Mvav 132,4,228
Ta 28,9,24576
v_and_b32 v23, 2, v23
Mvav 100,4,224
Ta 30,9,25600
K 163,166,167
Mvav 68,4,220
buffer_store_dwordx4 v[160:163], v1, s[8:11], s36 offen offset:2048 sc1
v_xor_b32 v20, v20, v23
Mvav 36,4,216
W 8
v_mul_u32_u24 v22, 80, v22
Mvav 40,8,216
v_add_lshl_u32 v19, v20, v22, 4
Mvav 72,8,220
v_subrev_u32 v19, 3072, v19
Mvav 104,8,224
buffer_load_dwordx4 a[148:151], v26, s[8:11], s93 offen offset:0 sc1
Mvav 136,8,228
v_lshrrev_b32 v20, 4, v1
G 168,216
Mvav 140,12,228
W 0
s_barrier
G 169,217
Mvav 108,12,224
Ta 0,10,32768
Ta 2,10,33792
Mvav 76,12,220
Ta 4,11,32768
Ta 6,11,33792
Mvav 44,12,216
v_add_u32 v20, 256, v20
G 170,218
Mvav 48,16,216
Ta 8,12,32768
G 171,219
Mvav 80,16,220
Ta 10,12,33792
v_mul_u32_u24 v22, 205, v20
Mvav 112,16,224
G 172,220
G 173,221
Mvav 144,16,228
buffer_load_dwordx4 a[184:187], v27, s[8:11], s28 offen offset:0 sc1
v_lshrrev_b32 v22, 12, v22
Mvav 148,20,228
G 174,222
G 175,223
Mvav 116,20,224
Ta 12,13,32768
Ta 14,13,33792
Mvav 84,20,220
K 168,168,169
v_mul_u32_u24 v23, 20, v22
Mvav 52,20,216
K 169,170,171
K 170,172,173
Mvav 56,24,216
v_sub_u32 v20, v20, v23
K 171,174,175
Mvav 88,24,220
Ta 16,14,32768
Ta 18,14,33792
Mvav 120,24,224
buffer_store_dwordx4 v[168:171], v1, s[8:11], s36 offen offset:3072 sc1
v_lshrrev_b32 v23, 1, v22
Mvav 152,24,228
v_and_b32 v23, 2, v23
v_xor_b32 v20, v20, v23
Mvav 156,28,228
Ta 20,15,32768
Ta 22,15,33792
Mvav 124,28,224
v_mul_u32_u24 v22, 80, v22
G 176,224
Mvav 92,28,220
buffer_load_dwordx4 a[188:191], v27, s[8:11], s93 offen offset:0 sc1
v_add_lshl_u32 v20, v20, v22, 4
Mvav 60,28,216
s_waitcnt vmcnt(14)
v_mov_b64 v[28:29], s[12:13]
s_mov_b64 exec, 1
buffer_store_dwordx2 v[28:29], off, s[8:11], s71 sc1
s_mov_b64 exec, -1
G 177,225
W 8
Mvaa 32,0,48
s_add_u32 s28, s28, 512
Ta 24,24,32768
Ta 26,24,33792
Mvaa 64,0,52
s_cmp_eq_u32 s28, s4
s_cselect_b32 s93, 10240, 0
G 178,226
G 179,227
Mvaa 96,0,56
s_sub_u32 s28, s28, s93
s_add_u32 s93, s28, 20480
v_lshrrev_b32 v22, 6, v1
Mvaa 128,0,60
buffer_load_dwordx4 a[152:155], v26, s[8:11], s28 offen offset:0 sc1
G 180,228
Mvaa 132,4,60
G 181,229
v_mul_u32_u24 v22, 320, v22
Mvaa 100,4,56
Ta 28,25,32768
Ta 30,25,33792
Mvaa 68,4,52
G 182,230
G 183,231
Mvaa 36,4,48
W 8
v_bfe_u32 v23, v1, 4, 2
K 176,176,177
Mvaa 40,8,48
K 177,178,179
v_lshl_add_u32 v22, v23, 3, v22
Mvaa 72,8,52
Ta 0,10,40960
K 178,180,181
Mvaa 104,8,56
Ta 2,10,41984
K 179,182,183
Mvaa 136,8,60
s_add_u32 s36, s29, 69632
buffer_load_dwordx4 a[156:159], v26, s[8:11], s93 offen offset:0 sc1
v_bfe_u32 v23, v1, 8, 1
Mvaa 140,12,60
Ta 4,11,40960
v_lshlrev_b32 v23, 5, v23
Mvaa 108,12,56
Ta 6,11,41984
v_add_u32 v21, v22, v23
Mvaa 76,12,52
Ta 8,12,40960
v_sub_u32 v22, v22, v23
Mvaa 44,12,48
W 9
buffer_store_dwordx4 v[176:179], v1, s[8:11], s36 offen offset:0 sc1
Ta 10,12,41984
Mvaa 48,16,48
s_mul_i32 s94, s59, 655360
G 160,232
Mvaa 80,16,52
s_mul_i32 s95, s58, 320
G 161,233
Mvaa 112,16,56
Ta 12,13,40960
G 162,234
Mvaa 144,16,60
s_add_u32 s94, s94, s95
buffer_load_dwordx4 v[208:211], v27, s[8:11], s28 offen offset:0 sc1
Ta 14,13,41984
Mvaa 148,20,60
G 163,235
G 164,236
Mvaa 116,20,56
s_add_u32 s40, s26, s94
s_addc_u32 s41, s27, 0
G 165,237
Mvaa 84,20,52
Ta 16,14,40960
G 166,238
Mvaa 52,20,48
W 9
Ta 18,14,41984
G 167,239
Mvaa 56,24,48
buffer_load_dwordx4 v[212:215], v27, s[8:11], s93 offen offset:0 sc1
K 160,160,161
Mvaa 88,24,52
s_and_b32 s41, s41, 0xffff
K 161,162,163
Mvaa 120,24,56
Ta 20,15,40960
K 162,164,165
Mvaa 152,24,60
s_mov_b32 s42, 5242880
Ta 22,15,41984
K 163,166,167
Mvaa 156,28,60
s_mov_b32 s43, 0x20000
buffer_store_dwordx4 v[160:163], v1, s[8:11], s36 offen offset:1024 sc1
Mvaa 124,28,56
Mvaa 92,28,52
s_sub_u32 s42, s42, s94
Ta 24,24,40960
Ta 26,24,41984
Mvaa 60,28,48
W 10
Mvav 32,0,232
s_mov_b64 s[44:45], s[8:9]
s_add_u32 s28, s28, 512
s_cmp_eq_u32 s28, s4
s_cselect_b32 s93, 10240, 0
Mvav 64,0,236
s_sub_u32 s28, s28, s93
s_add_u32 s93, s28, 20480
buffer_load_dwordx4 a[192:195], v26, s[8:11], s28 offen offset:0 sc1
Mvav 96,0,184
s_mov_b32 s47, 0x20000
Ta 28,25,40960
Ta 30,25,41984
Mvav 128,0,188
G 168,240
Mvav 132,4,188
s_and_b32 s48, s14, 1
G 169,241
Mvav 100,4,184
G 170,242
Mvav 68,4,236
s_mul_i32 s48, s48, 5120
Ta 0,10,49152
G 171,243
Mvav 36,4,232
W 9
Ta 2,10,50176
G 172,244
Mvav 40,8,232
s_lshr_b32 s51, s14, 1
buffer_load_dwordx4 a[196:199], v26, s[8:11], s93 offen offset:0 sc1
G 173,245
Mvav 72,8,236
G 174,246
Mvav 104,8,184
s_and_b32 s94, s14, 1
Ta 4,11,49152
G 175,247
Mvav 136,8,188
Ta 6,11,50176
K 168,168,169
Mvav 140,12,188
s_lshl_b32 s95, s57, 1
K 169,170,171
Mvav 108,12,184
s_add_u32 s94, s94, s95
K 170,172,173
Mvav 76,12,236
Ta 8,12,49152
)"
R"(K 171,174,175
Mvav 44,12,232
W 9
Ta 10,12,50176
buffer_store_dwordx4 v[168:171], v1, s[8:11], s36 offen offset:2048 sc1
Mvav 48,16,232
s_lshl_b32 s54, s94, 3
Mvav 80,16,236
s_add_u32 s54, s54, s59
Mvav 112,16,184
Ta 12,13,49152
Ta 14,13,50176
Mvav 144,16,188
s_lshl_b32 s54, s54, 6
buffer_load_dwordx4 a[200:203], v27, s[8:11], s28 offen offset:0 sc1
Mvav 148,20,188
Mvav 116,20,184
s_xor_b32 s55, s54, 15872
Mvav 84,20,236
Ta 16,14,49152
Ta 18,14,50176
Mvav 52,20,232
W 10
s_add_u32 s54, s54, 20480
G 176,248
Mvav 56,24,232
s_and_b32 s81, s14, 1
G 177,249
G 178,250
Mvav 88,24,236
s_add_u32 s55, s55, 20480
buffer_load_dwordx4 a[204:207], v27, s[8:11], s93 offen offset:0 sc1
G 179,251
Mvav 120,24,184
Ta 20,15,49152
G 180,252
Mvav 152,24,188
s_and_b32 s95, s14, 2
s_mul_i32 s91, s81, 17164288
Ta 22,15,50176
Mvav 156,28,188
G 181,253
G 182,254
Mvav 124,28,184
s_lshl_b32 s94, s94, 2
s_mul_i32 s82, s56, 536576
G 183,255
Mvav 92,28,236
s_add_u32 s94, s94, s95
Ta 24,24,49152
K 176,176,177
Mvav 60,28,232
s_waitcnt vmcnt(24) lgkmcnt(9)
Ta 26,24,50176
K 177,178,179
Mvaa 32,0,128
s_add_u32 s91, s91, s82
s_lshl_b32 s95, s95, 4
K 178,180,181
Mvaa 64,0,132
s_add_u32 s28, s28, 512
s_cmp_eq_u32 s28, s4
s_cselect_b32 s93, 10240, 0
s_sub_u32 s28, s28, s93
Mvaa 96,0,136
s_add_u32 s93, s28, 20480
buffer_load_dwordx4 a[32:35], v26, s[8:11], s28 offen offset:0 sc1
K 179,182,183
Mvaa 128,0,140
s_add_u32 s55, s55, s95
Ta 28,25,49152
Ta 30,25,50176
Mvaa 132,4,140
s_add_u32 s91, s91, 4194304
s_lshl_b32 s49, s94, 17
Mvaa 100,4,136
s_lshr_b32 s82, s14, 1
Mvaa 68,4,132
s_lshl_b32 s95, s59, 14
buffer_store_dwordx4 v[176:179], v1, s[8:11], s36 offen offset:3072 sc1
Ta 0,10,57344
Mvaa 36,4,128
W 9
s_lshl_b32 s83, s82, 14
Ta 2,10,58368
Mvaa 40,8,128
s_add_u32 s49, s49, s95
G 160,64
Mvaa 72,8,132
G 161,65
Mvaa 104,8,136
s_xor_b32 s50, s49, 16252928
s_add_u32 s91, s91, s83
buffer_load_dwordx4 a[36:39], v26, s[8:11], s93 offen offset:0 sc1
Mvaa 136,8,140
Ta 4,11,57344
G 162,66
Mvaa 140,12,140
s_add_u32 s49, s49, 39845888
Ta 6,11,58368
G 163,67
Mvaa 108,12,136
s_lshl_b32 s81, s81, 14
G 164,68
G 165,69
Mvaa 76,12,132
s_add_u32 s50, s50, 39845888
Ta 8,12,57344
G 166,70
Mvaa 44,12,128
W 9
Ta 10,12,58368
G 167,71
Mvaa 48,16,128
s_lshl_b32 s82, s82, 16
s_lshr_b32 s94, s15, 3
K 160,160,161
Mvaa 80,16,132
s_lshl_b32 s94, s94, 3
buffer_load_dwordx4 a[40:43], v27, s[8:11], s28 offen offset:0 sc1
K 161,162,163
Mvaa 112,16,136
s_or_b32 s92, s81, s82
K 162,164,165
K 163,166,167
Mvaa 144,16,140
s_add_u32 s36, s29, 393216
Ta 12,13,57344
Ta 14,13,58368
Mvaa 148,20,140
s_add_u32 s94, s94, s14
s_xor_b32 s92, s92, 0x10000
Mvaa 116,20,136
s_lshl_b32 s94, s94, 4
buffer_store_dwordx4 v[160:163], v1, s[8:11], s36 offen offset:0 sc1
Mvaa 84,20,132
Ta 16,14,57344
Ta 18,14,58368
Mvaa 52,20,128
W 10
s_add_u32 s94, s94, s59
Mvaa 56,24,128
Mvaa 88,24,132
s_mul_i32 s52, s94, 5120
buffer_load_dwordx4 a[44:47], v27, s[8:11], s93 offen offset:0 sc1
Mvaa 120,24,136
Ta 20,15,57344
Ta 22,15,58368
Mvaa 152,24,140
s_add_u32 s52, s52, 56623104
G 168,72
Mvaa 156,28,140
G 169,73
Mvaa 124,28,136
s_lshl_b32 s53, s94, 3
G 170,74
Mvaa 92,28,132
Ta 24,24,57344
G 171,75
Mvaa 60,28,128
s_waitcnt vmcnt(24) lgkmcnt(9)
s_add_u32 s53, s53, 65536
Ta 26,24,58368
Mvav 32,0,192
G 172,76
G 173,77
Mvav 64,0,196
v_bfe_u32 v244, v1, 4, 1
G 174,78
Mvav 96,0,200
G 175,79
v_mul_u32_u24 v244, 0xaaaa, v244
Mvav 128,0,204
Ta 28,25,57344
Ta 30,25,58368
Mvav 132,4,204
K 168,168,169
v_add_u32 v244, 0x4444, v244
Mvav 100,4,200
K 169,170,171
K 170,172,173
Mvav 68,4,196
v_lshl_or_b32 v240, v244, 16, v244
K 171,174,175
Mvav 36,4,192
W 8
Ta 0,2,32768
Ta 2,2,33792
Mvav 40,8,192
buffer_store_dwordx4 v[168:171], v1, s[8:11], s36 offen offset:1024 sc1
v_mov_b32 v241, 0xff800000
Mvav 72,8,196
v_add_u32 v242, 0x10000, v1
Mvav 104,8,200
Ta 4,3,32768
v_bfe_u32 v244, v1, 4, 4
Mvav 136,8,204
Ta 6,3,33792
Mvav 140,12,204
v_lshrrev_b32 v245, 8, v1
Mvav 108,12,200
v_lshlrev_b32 v245, 2, v245
Mvav 76,12,196
Ta 8,4,32768
Ta 10,4,33792
Mvav 44,12,192
W 10
G 176,80
v_sub_u32 v244, v244, v245
Mvav 48,16,192
G 177,81
G 178,82
Mvav 80,16,196
v_cmp_le_i32_e64 s[72:73], 0, v244
G 179,83
Mvav 112,16,200
v_cmp_le_i32_e64 s[74:75], 1, v244
G 180,84
Mvav 144,16,204
Ta 12,5,32768
Ta 14,5,33792
Mvav 148,20,204
G 181,85
v_cmp_le_i32_e64 s[76:77], 2, v244
Mvav 116,20,200
G 182,86
G 183,87
Mvav 84,20,196
v_cmp_le_i32_e64 s[78:79], 3, v244
K 176,176,177
Mvav 52,20,192
W 8
Ta 16,6,32768
Ta 18,6,33792
Mvav 56,24,192
K 177,178,179
v_add_u32 v243, 4096, v1
Mvav 88,24,196
K 178,180,181
v_add_u32 v244, 8192, v1
Mvav 120,24,200
K 179,182,183
v_add_u32 v245, 12288, v1
Mvav 152,24,204
buffer_store_dwordx4 v[176:179], v1, s[8:11], s36 offen offset:2048 sc1
Ta 20,7,32768
Mvav 156,28,204
s_mov_b32 s18, 0x3f803f80
Ta 22,7,33792
Mvav 124,28,200
s_mov_b32 s19, 0x3f003f00
Mvav 92,28,196
Ta 24,8,32768
Ta 26,8,33792
Mvav 60,28,192
s_waitcnt vmcnt(20) lgkmcnt(10)
v_accvgpr_write_b32 a176, s18
Mvaa 32,0,144
Mvaa 64,0,148
v_accvgpr_write_b32 a180, s19
G 160,88
Mvaa 96,0,184
v_mov_b32 v252, v241
G 161,89
Mvaa 128,0,188
Ta 28,9,32768
Ta 30,9,33792
Mvaa 132,4,188
G 162,90
v_accvgpr_write_b32 a177, s18
Mvaa 100,4,184
G 163,91
G 164,92
Mvaa 68,4,148
v_accvgpr_write_b32 a181, s19
G 165,93
Mvaa 36,4,144
W 8
Ta 0,2,40960
Ta 2,2,41984
Mvaa 40,8,144
v_mov_b32 v253, v241
G 166,94
Mvaa 72,8,148
G 167,95
v_accvgpr_write_b32 a178, s18
Mvaa 104,8,184
K 160,160,161
K 161,162,163
Mvaa 136,8,188
Ta 4,3,40960
v_accvgpr_write_b32 a182, s19
Mvaa 140,12,188
Ta 6,3,41984
K 162,164,165
Mvaa 108,12,184
K 163,166,167
v_mov_b32 v254, v241
Mvaa 76,12,148
buffer_store_dwordx4 v[160:163], v1, s[8:11], s36 offen offset:3072 sc1
v_accvgpr_write_b32 a179, s18
Mvaa 44,12,144
W 8
Ta 8,4,40960
Ta 10,4,41984
Mvaa 48,16,144
v_accvgpr_write_b32 a183, s19
Mvaa 80,16,148
v_mov_b32 v255, v241
Mvaa 112,16,184
Ta 12,5,40960
v_accvgpr_write_b32 a160, 0
Mvaa 144,16,188
Ta 14,5,41984
G 168,96
Mvaa 148,20,188
v_accvgpr_write_b32 a161, 0
G 169,97
Mvaa 116,20,184
G 170,98
v_accvgpr_write_b32 a162, 0
Mvaa 84,20,148
Ta 16,6,40960
G 171,99
Mvaa 52,20,144
W 9
Ta 18,6,41984
G 172,100
Mvaa 56,24,144
v_accvgpr_write_b32 a163, 0
G 173,101
Mvaa 88,24,148
G 174,102
v_accvgpr_write_b32 a164, 0
Mvaa 120,24,184
G 175,103
v_accvgpr_write_b32 a165, 0
Mvaa 152,24,188
Ta 20,7,40960
K 168,168,169
Mvaa 156,28,188
Ta 22,7,41984
K 169,170,171
Mvaa 124,28,184
v_accvgpr_write_b32 a166, 0
K 170,172,173
Mvaa 92,28,148
K 171,174,175
v_accvgpr_write_b32 a167, 0
Mvaa 60,28,144
s_waitcnt vmcnt(15) lgkmcnt(8)
s_add_u32 s36, s29, 397312
Ta 24,8,40960
Mvaa 32,0,152
Ta 26,8,41984
buffer_store_dwordx4 v[168:171], v1, s[8:11], s36 offen offset:0 sc1
Mvaa 64,0,156
v_accvgpr_write_b32 a168, 0
v_accvgpr_write_b32 a169, 0
Mvav 96,0,208
Ta 28,9,40960
v_accvgpr_write_b32 a170, 0
Mvav 128,0,212
Ta 30,9,41984
Mvav 132,4,212
v_accvgpr_write_b32 a171, 0
Mvav 100,4,208
v_accvgpr_write_b32 a172, 0
Mvaa 68,4,156
Ta 0,2,49152
Ta 2,2,50176
Mvaa 36,4,152
W 10
G 176,104
v_accvgpr_write_b32 a173, 0
Mvaa 40,8,152
G 177,105
G 178,106
Mvaa 72,8,156
v_accvgpr_write_b32 a174, 0
G 179,107
Mvav 104,8,208
G 180,108
v_accvgpr_write_b32 a175, 0
Mvav 136,8,212
Ta 4,3,49152
Ta 6,3,50176
Mvav 140,12,212
s_mov_b32 s90, 0x5d800000
G 181,109
G 182,110
Mvav 108,12,208
G 183,111
Mvaa 76,12,156
s_mov_b64 s[88:89], 0
Ta 8,4,49152
K 176,176,177
Mvaa 44,12,152
W 9
Ta 10,4,50176
)"
R"(K 177,178,179
Mvaa 48,16,152
s_mov_b32 s64, 0
K 178,180,181
Mvaa 80,16,156
s_and_b32 s18, s14, 1
K 179,182,183
Mvav 112,16,208
s_lshl_b32 s62, s18, 14
buffer_store_dwordx4 v[176:179], v1, s[8:11], s36 offen offset:1024 sc1
Ta 12,5,49152
Mvav 144,16,212
Ta 14,5,50176
Mvav 148,20,212
s_lshl_b32 s20, s57, 15
Mvav 116,20,208
Mvaa 84,20,156
s_sub_u32 s20, 507904, s20
Ta 16,6,49152
Ta 18,6,50176
Mvaa 52,20,152
W 10
s_add_u32 s20, s20, s62
Mvaa 56,24,152
Mvaa 88,24,156
s_lshl_b32 s65, s57, 1
G 160,112
Mvav 120,24,208
s_sub_u32 s65, s65, 1
Ta 20,7,49152
G 161,113
Mvav 152,24,212
Ta 22,7,50176
G 162,114
Mvav 156,28,212
s_max_i32 s65, s65, 0
G 163,115
Mvav 124,28,208
G 164,116
Mvaa 92,28,156
s_sub_u32 s66, s60, 1
Ta 24,8,49152
G 165,117
Mvaa 60,28,152
s_waitcnt vmcnt(11) lgkmcnt(9)
Ta 26,8,50176
G 166,118
Mvaa 32,0,192
s_cmp_lt_u32 s14, 2
s_cselect_b32 s20, s20, 16384
s_cselect_b32 s66, s66, 15
s_cselect_b32 s21, 32768, 0
G 167,119
Mvaa 64,0,196
s_add_u32 s61, s91, s20
K 160,160,161
Mvaa 96,0,200
s_add_u32 s62, s62, s21
Ta 28,9,49152
K 161,162,163
Mvaa 128,0,204
Ta 30,9,50176
K 162,164,165
Mvaa 132,4,204
s_sub_u32 s63, 16384, s21
K 163,166,167
Mvaa 100,4,200
buffer_store_dwordx4 v[160:163], v1, s[8:11], s36 offen offset:2048 sc1
Mvaa 68,4,196
s_lshl_b32 s67, s60, 7
Ta 0,2,57344
Ta 2,2,58368
Mvaa 36,4,192
W 10
s_add_u32 s67, s67, s56
Mvaa 40,8,192
Mvaa 72,8,196
s_lshl_b32 s67, s67, 12
Mvaa 104,8,200
s_add_u32 s67, s67, 39845888
Ta 4,3,57344
Ta 6,3,58368
Mvaa 136,8,204
Mvaa 140,12,204
s_lshl_b32 s69, s60, 3
G 168,120
Mvaa 108,12,200
G 169,121
Mvaa 76,12,196
s_add_u32 s69, s69, s59
Ta 8,4,57344
G 170,122
Mvaa 44,12,192
W 9
s_lshl_b32 s69, s69, 6
Ta 10,4,58368
Mvaa 48,16,192
G 171,123
G 172,124
Mvaa 80,16,196
s_lshl_b32 s21, s58, 3
G 173,125
Mvaa 112,16,200
s_add_u32 s69, s69, s21
Ta 12,5,57344
G 174,126
Mvaa 144,16,204
Ta 14,5,58368
G 175,127
Mvaa 148,20,204
s_add_u32 s69, s69, 20480
K 168,168,169
Mvaa 116,20,200
K 169,170,171
Mvaa 84,20,196
s_mov_b32 s70, 0
Ta 16,6,57344
K 170,172,173
Mvaa 52,20,192
W 9
Ta 18,6,58368
K 171,174,175
Mvaa 56,24,192
s_mov_b32 s37, 0
buffer_store_dwordx4 v[168:171], v1, s[8:11], s36 offen offset:3072 sc1
Mvaa 88,24,196
s_sub_u32 s18, s57, 4
Mvaa 120,24,200
s_lshl_b32 s18, s18, 2
Ta 20,7,57344
Ta 22,7,58368
Mvaa 152,24,204
Mvaa 156,28,204
s_add_u32 s18, s18, s58
Mvaa 124,28,200
Mvaa 92,28,196
s_mul_i32 s85, s59, 7
Ta 24,8,57344
Ta 26,8,58368
Mvaa 60,28,192
s_waitcnt vmcnt(7) lgkmcnt(10)
s_add_u32 s85, s85, s18
Mvaa 32,0,32
Mvaa 64,0,36
s_cmp_lt_u32 s18, 7
s_cselect_b32 s85, s85, 99
Mvaa 96,0,40
s_cmp_lt_u32 s85, 54
s_cselect_b32 s65, 1, s65
Ta 28,9,57344
Ta 30,9,58368
Mvaa 128,0,44
Mvaa 132,4,44
s_mov_b32 s68, 0
Mvaa 100,4,40
Mvaa 68,4,36
s_mov_b64 s[24:25], -1
Mvaa 36,4,32
W 8
s_mov_b32 s22, 0x3e0293ee
Mvaa 40,8,32
Mvaa 72,8,36
s_mov_b32 s23, 0x3e0293ee
Mvaa 104,8,40
Mvaa 136,8,44
Mvaa 140,12,44
Mvaa 108,12,40
Mvaa 76,12,36
Mvaa 44,12,32
W 4
Mvaa 48,16,32
Mvaa 80,16,36
Mvaa 112,16,40
Mvaa 144,16,44
Mvaa 148,20,44
W 0
s_barrier
Ra 128,242,0
Mvaa 116,20,40
Mvaa 84,20,36
Ra 132,242,1024
Mvaa 52,20,32
Mvaa 56,24,32
Ra 136,242,2048
Mvaa 88,24,36
Mvaa 120,24,40
Ra 140,242,3072
Mvaa 152,24,44
Mvaa 156,28,44
Mvaa 124,28,40
Mvaa 92,28,36
Mvaa 60,28,32
.Lago%=:
s_cmp_eq_u32 s57, 0
s_cbranch_scc0 .LL_47%=
s_add_u32 m0, s62, 0
s_nop 0
Dc 1,8,61,0
Dc 1,8,61,1024
Dc 1,8,61,2048
Dc 1,8,61,3072
s_add_u32 m0, s62, 4096
s_nop 0
Dc 243,8,61,0
Dc 243,8,61,1024
Dc 243,8,61,2048
Dc 243,8,61,3072
s_add_u32 m0, s62, 8192
s_nop 0
Dc 244,8,61,0
Dc 244,8,61,1024
Dc 244,8,61,2048
Dc 244,8,61,3072
s_add_u32 m0, s62, 12288
s_nop 0
Dc 245,8,61,0
Dc 245,8,61,1024
Dc 245,8,61,2048
Dc 245,8,61,3072
s_add_u32 s61, s61, s63
.LL_47%=:
s_cmp_eq_u32 s60, 0
s_cbranch_scc1 .Lpd_31%=
v_pk_mul_f32 v[32:33], v[32:33], s[22:23]
v_pk_mul_f32 v[34:35], v[34:35], s[22:23]
v_pk_mul_f32 v[36:37], v[36:37], s[22:23]
v_pk_mul_f32 v[38:39], v[38:39], s[22:23]
K 32,32,33
K 33,34,35
K 34,36,37
K 35,38,39
v_pk_mul_f32 v[64:65], v[64:65], s[22:23]
v_pk_mul_f32 v[66:67], v[66:67], s[22:23]
v_pk_mul_f32 v[68:69], v[68:69], s[22:23]
v_pk_mul_f32 v[70:71], v[70:71], s[22:23]
K 64,64,65
K 65,66,67
K 66,68,69
K 67,70,71
v_pk_mul_f32 v[96:97], v[96:97], s[22:23]
v_pk_mul_f32 v[98:99], v[98:99], s[22:23]
v_pk_mul_f32 v[100:101], v[100:101], s[22:23]
v_pk_mul_f32 v[102:103], v[102:103], s[22:23]
K 96,96,97
K 97,98,99
K 98,100,101
K 99,102,103
v_pk_mul_f32 v[128:129], v[128:129], s[22:23]
v_pk_mul_f32 v[130:131], v[130:131], s[22:23]
v_pk_mul_f32 v[132:133], v[132:133], s[22:23]
v_pk_mul_f32 v[134:135], v[134:135], s[22:23]
K 128,128,129
K 129,130,131
K 130,132,133
K 131,134,135
v_pk_mul_f32 v[40:41], v[40:41], s[22:23]
v_pk_mul_f32 v[42:43], v[42:43], s[22:23]
v_pk_mul_f32 v[44:45], v[44:45], s[22:23]
v_pk_mul_f32 v[46:47], v[46:47], s[22:23]
v_pk_mul_f32 v[72:73], v[72:73], s[22:23]
v_pk_mul_f32 v[74:75], v[74:75], s[22:23]
v_pk_mul_f32 v[76:77], v[76:77], s[22:23]
v_pk_mul_f32 v[78:79], v[78:79], s[22:23]
v_pk_mul_f32 v[104:105], v[104:105], s[22:23]
v_pk_mul_f32 v[106:107], v[106:107], s[22:23]
v_pk_mul_f32 v[108:109], v[108:109], s[22:23]
v_pk_mul_f32 v[110:111], v[110:111], s[22:23]
v_pk_mul_f32 v[136:137], v[136:137], s[22:23]
v_pk_mul_f32 v[138:139], v[138:139], s[22:23]
v_pk_mul_f32 v[140:141], v[140:141], s[22:23]
v_pk_mul_f32 v[142:143], v[142:143], s[22:23]
v_pk_mul_f32 v[48:49], v[48:49], s[22:23]
v_pk_mul_f32 v[50:51], v[50:51], s[22:23]
v_pk_mul_f32 v[52:53], v[52:53], s[22:23]
v_pk_mul_f32 v[54:55], v[54:55], s[22:23]
v_pk_mul_f32 v[80:81], v[80:81], s[22:23]
v_pk_mul_f32 v[82:83], v[82:83], s[22:23]
v_pk_mul_f32 v[84:85], v[84:85], s[22:23]
v_pk_mul_f32 v[86:87], v[86:87], s[22:23]
v_pk_mul_f32 v[112:113], v[112:113], s[22:23]
v_pk_mul_f32 v[114:115], v[114:115], s[22:23]
v_pk_mul_f32 v[116:117], v[116:117], s[22:23]
v_pk_mul_f32 v[118:119], v[118:119], s[22:23]
v_pk_mul_f32 v[144:145], v[144:145], s[22:23]
v_pk_mul_f32 v[146:147], v[146:147], s[22:23]
v_pk_mul_f32 v[148:149], v[148:149], s[22:23]
v_pk_mul_f32 v[150:151], v[150:151], s[22:23]
v_pk_mul_f32 v[56:57], v[56:57], s[22:23]
v_pk_mul_f32 v[58:59], v[58:59], s[22:23]
v_pk_mul_f32 v[60:61], v[60:61], s[22:23]
v_pk_mul_f32 v[62:63], v[62:63], s[22:23]
v_pk_mul_f32 v[88:89], v[88:89], s[22:23]
v_pk_mul_f32 v[90:91], v[90:91], s[22:23]
v_pk_mul_f32 v[92:93], v[92:93], s[22:23]
v_pk_mul_f32 v[94:95], v[94:95], s[22:23]
v_pk_mul_f32 v[120:121], v[120:121], s[22:23]
v_pk_mul_f32 v[122:123], v[122:123], s[22:23]
v_pk_mul_f32 v[124:125], v[124:125], s[22:23]
v_pk_mul_f32 v[126:127], v[126:127], s[22:23]
v_pk_mul_f32 v[152:153], v[152:153], s[22:23]
v_pk_mul_f32 v[154:155], v[154:155], s[22:23]
v_pk_mul_f32 v[156:157], v[156:157], s[22:23]
v_pk_mul_f32 v[158:159], v[158:159], s[22:23]
W 0
Zvav 196,128,32
Ra 144,242,4096
Ra 148,242,5120
Ra 152,242,6144
Ra 156,242,7168
Zvav 204,128,64
K 40,40,41
K 41,42,43
K 42,44,45
K 43,46,47
Zvav 212,128,96
K 72,72,73
K 73,74,75
K 74,76,77
K 75,78,79
Zvav 220,128,128
K 104,104,105
K 105,106,107
K 106,108,109
K 107,110,111
K 136,136,137
K 137,138,139
K 138,140,141
K 139,142,143
Mvav 196,132,40
Ra 128,242,8192
K 48,48,49
Mvav 204,132,72
K 49,50,51
K 50,52,53
K 51,54,55
K 80,80,81
Mvav 212,132,104
K 81,82,83
K 82,84,85
K 83,86,87
K 112,112,113
Mvav 220,132,136
K 113,114,115
K 114,116,117
K 115,118,119
K 144,144,145
K 145,146,147
K 146,148,149
K 147,150,151
Mvav 196,136,48
Ra 132,242,9216
K 56,56,57
Mvav 204,136,80
K 57,58,59
K 58,60,61
K 59,62,63
K 88,88,89
Mvav 212,136,112
K 89,90,91
K 90,92,93
K 91,94,95
K 120,120,121
Mvav 220,136,144
K 121,122,123
K 122,124,125
K 123,126,127
K 152,152,153
K 153,154,155
K 154,156,157
K 155,158,159
Mvav 196,140,56
Ra 136,242,10240
v_accvgpr_write_b32 a0, v32
Mvav 204,140,88
v_accvgpr_write_b32 a1, v33
Mvav 212,140,120
v_accvgpr_write_b32 a2, v34
Mvav 220,140,152
W 3
v_accvgpr_write_b32 a3, v35
v_accvgpr_write_b32 a4, v40
Zvav 36,144,32
Ra 140,242,11264
v_accvgpr_write_b32 a5, v41
Zvav 68,144,64
v_accvgpr_write_b32 a6, v42
v_accvgpr_write_b32 a7, v43
v_max3_f32 v4, v196, v197, v198
v_max3_f32 v4, v4, v199, v252
Zvav 100,144,96
v_max3_f32 v5, v204, v205, v206
v_max3_f32 v5, v5, v207, v253
v_max3_f32 v6, v212, v213, v214
v_max3_f32 v6, v6, v215, v254
Zvav 132,144,128
v_max3_f32 v7, v220, v221, v222
v_max3_f32 v7, v7, v223, v255
v_mov_b32 v8, v4
v_mov_b32 v9, v5
Mvav 36,148,40
)"
R"(v_mov_b32 v10, v6
v_mov_b32 v11, v7
v_permlane32_swap_b32 v4, v8
v_accvgpr_write_b32 a8, v48
Mvav 68,148,72
v_permlane32_swap_b32 v5, v9
v_permlane32_swap_b32 v6, v10
v_permlane32_swap_b32 v7, v11
v_max_f32 v4, v4, v8
Mvav 100,148,104
v_max_f32 v5, v5, v9
v_max_f32 v6, v6, v10
v_max_f32 v7, v7, v11
v_mov_b32 v8, v4
Mvav 132,148,136
v_mov_b32 v9, v5
v_mov_b32 v10, v6
v_mov_b32 v11, v7
v_accvgpr_write_b32 a9, v49
Mvav 36,152,48
v_permlane16_swap_b32 v4, v8
v_permlane16_swap_b32 v5, v9
v_permlane16_swap_b32 v6, v10
v_permlane16_swap_b32 v7, v11
Mvav 68,152,80
v_max_f32 v4, v4, v8
v_max_f32 v5, v5, v9
v_max_f32 v6, v6, v10
v_max_f32 v7, v7, v11
Mvav 100,152,112
v_sub_f32 v4, 0, v4
v_sub_f32 v5, 0, v5
v_sub_f32 v6, 0, v6
v_sub_f32 v7, 0, v7
Mvav 132,152,144
v_mov_b32 v224, v4
v_mov_b32 v225, v4
v_mov_b32 v226, v4
v_mov_b32 v227, v4
Mvav 36,156,56
v_mov_b32 v228, v5
v_mov_b32 v229, v5
v_mov_b32 v230, v5
v_mov_b32 v231, v5
Mvav 68,156,88
v_mov_b32 v232, v6
v_mov_b32 v233, v6
v_mov_b32 v234, v6
v_mov_b32 v235, v6
Mvav 100,156,120
v_mov_b32 v236, v7
v_mov_b32 v237, v7
v_mov_b32 v238, v7
v_mov_b32 v239, v7
Mvav 132,156,152
W 0
Ra 144,242,12288
v_accvgpr_write_b32 a10, v50
v_accvgpr_write_b32 a11, v51
v_accvgpr_write_b32 a12, v56
Cvav 160,128,32,224
s_and_b32 s80, s14, 1
v_accvgpr_write_b32 a13, v57
v_accvgpr_write_b32 a14, v58
v_accvgpr_write_b32 a15, v59
v_accvgpr_write_b32 a16, v64
Cvav 164,128,64,228
s_lshl_b32 s80, s80, 14
Ra 148,242,13312
v_accvgpr_write_b32 a17, v65
v_accvgpr_write_b32 a18, v66
v_accvgpr_write_b32 a19, v67
Cvav 168,128,96,232
v_accvgpr_write_b32 a20, v72
v_accvgpr_write_b32 a21, v73
v_accvgpr_write_b32 a22, v74
v_accvgpr_write_b32 a23, v75
Cvav 172,128,128,236
s_add_u32 s80, s80, 98304
Ra 152,242,14336
v_accvgpr_write_b32 a24, v80
v_accvgpr_write_b32 a25, v81
v_accvgpr_write_b32 a26, v82
Mvav 160,132,40
A 196,196,224
v_accvgpr_write_b32 a27, v83
A 197,197,224
A 198,198,224
Mvav 164,132,72
Ra 156,242,15360
v_accvgpr_write_b32 a28, v88
A 199,199,224
v_accvgpr_write_b32 a29, v89
Mvav 168,132,104
X 196
v_accvgpr_write_b32 a30, v90
v_accvgpr_write_b32 a31, v91
Mvav 172,132,136
X 197
X 198
Mvav 160,136,48
v_accvgpr_write_b32 a32, v96
K 192,196,197
X 199
Mvav 164,136,80
v_accvgpr_write_b32 a33, v97
A 204,204,228
v_accvgpr_write_b32 a34, v98
K 193,198,199
Mvav 168,136,112
Ra 128,242,24576
v_accvgpr_write_b32 a35, v99
A 205,205,228
A 206,206,228
Mvav 172,136,144
v_accvgpr_write_b32 a36, v104
v_add_u32 v8, s80, v1
A 207,207,228
v_accvgpr_write_b32 a37, v105
Mvav 160,140,56
X 204
X 205
Mvav 164,140,88
v_accvgpr_write_b32 a38, v106
X 206
v_accvgpr_write_b32 a39, v107
Mvav 168,140,120
v_accvgpr_write_b32 a40, v112
K 200,204,205
X 207
Mvav 172,140,152
W 1
Ra 132,242,25600
v_accvgpr_write_b32 a41, v113
A 212,212,232
v_accvgpr_write_b32 a42, v114
Cvav 176,144,32,224
K 201,206,207
A 213,213,232
v_accvgpr_write_b32 a43, v115
A 214,214,232
Cvav 180,144,64,228
A 215,215,232
v_accvgpr_write_b32 a44, v120
X 212
Cvav 184,144,96,232
v_accvgpr_write_b32 a45, v121
X 213
v_accvgpr_write_b32 a46, v122
Cvav 188,144,128,236
Ra 136,242,26624
X 214
v_accvgpr_write_b32 a47, v123
Mvav 176,148,40
K 208,212,213
v_accvgpr_write_b32 a48, v128
X 215
Mvav 180,148,72
v_accvgpr_write_b32 a49, v129
A 220,220,236
v_accvgpr_write_b32 a50, v130
K 209,214,215
Mvav 184,148,104
A 221,221,236
v_accvgpr_write_b32 a51, v131
A 222,222,236
A 223,223,236
Mvav 188,148,136
v_accvgpr_write_b32 a52, v136
X 220
v_accvgpr_write_b32 a53, v137
Mvav 176,152,48
Ra 140,242,27648
X 221
v_accvgpr_write_b32 a54, v138
Mvav 180,152,80
X 222
v_accvgpr_write_b32 a55, v139
K 216,220,221
Mvav 184,152,112
v_accvgpr_write_b32 a56, v144
X 223
v_accvgpr_write_b32 a57, v145
Mvav 188,152,144
A 36,36,224
K 217,222,223
v_accvgpr_write_b32 a58, v146
A 37,37,224
Mvav 176,156,56
v_accvgpr_write_b32 a59, v147
v_accvgpr_write_b32 a60, v152
v_accvgpr_write_b32 a61, v153
v_accvgpr_write_b32 a62, v154
v_accvgpr_write_b32 a63, v155
Mvav 180,156,88
s_barrier
Ra 144,242,28672
Ra 148,242,29696
A 38,38,224
A 39,39,224
Mvav 184,156,120
X 36
X 37
X 38
K 194,36,37
X 39
A 68,68,228
K 195,38,39
A 69,69,228
A 70,70,228
A 71,71,228
X 68
X 69
X 70
K 202,68,69
X 71
A 100,100,232
K 203,70,71
A 101,101,232
A 102,102,232
A 103,103,232
X 100
X 101
X 102
K 210,100,101
X 103
A 132,132,236
K 211,102,103
A 133,133,236
A 134,134,236
A 135,135,236
X 132
X 133
X 134
K 218,132,133
X 135
s_nop 0
K 219,134,135
Mvav 188,156,152
W 2
Ra 152,242,30720
Zvav 32,128,192
Ra 156,242,31744
Zvav 64,128,200
Zvav 96,128,208
s_and_b32 s80, s14, 1
s_xor_b32 s62, s62, 0x10000
Zvav 128,128,216
s_lshl_b32 s80, s80, 14
s_add_u32 s80, s80, 131072
v_add_u32 v23, 61440, v21
Zvav 132,132,216
v_add_u32 v12, s80, v1
Ra 64,12,8192
Ra 68,12,9216
v_add_u32 v24, 61440, v22
Zvav 100,132,208
Ra 72,12,10240
Ra 76,12,11264
Ra 80,12,12288
v_add_u32 v25, 122880, v21
Zvav 68,132,200
Ra 84,12,13312
Ra 88,12,14336
Ra 92,12,15360
v_add_u32 v26, 122880, v22
Zvav 36,132,192
Ra 96,12,0
Ra 100,12,1024
Ra 104,12,2048
Zvav 40,136,192
Ra 108,12,3072
Ra 112,12,4096
X 172
Zvav 72,136,200
Ra 116,12,5120
Ra 120,12,6144
X 173
Zvav 104,136,208
Ra 124,12,7168
X 174
K 220,172,173
Zvav 136,136,216
X 175
Zvav 140,140,216
X 168
K 221,174,175
Zvav 108,140,208
X 169
X 170
Zvav 76,140,200
s_waitcnt vmcnt(0) lgkmcnt(0)
s_barrier
v_xor_b32 v242, 0x10000, v242
ds_read_b128 v[248:251], v8
Zvav 44,140,192
s_add_u32 s80, s71, 128
v_mov_b64 v[28:29], s[12:13]
s_mov_b64 exec, 1
buffer_store_dwordx2 v[28:29], off, s[8:11], s80 sc1
s_mov_b64 exec, -1
Ra 128,242,0
K 212,168,169
X 171
Zvav 48,144,192
X 164
K 213,170,171
Zvav 80,144,200
X 165
X 166
Zvav 112,144,208
Ra 132,242,1024
K 204,164,165
X 167
Zvav 144,144,216
X 160
Zvav 148,148,216
K 205,166,167
X 161
Zvav 116,148,208
Ra 136,242,2048
X 162
Zvav 84,148,200
W 3
K 196,160,161
X 163
Zvav 52,148,192
X 176
K 197,162,163
Zvav 56,152,192
Ra 140,242,3072
X 177
Zvav 88,152,200
X 178
K 198,176,177
Zvav 120,152,208
X 179
X 180
Zvav 152,152,216
K 199,178,179
Zvav 156,156,216
X 181
X 182
Zvav 124,156,208
K 206,180,181
X 183
Zvav 92,156,200
K 207,182,183
Zvav 60,156,192
.Ll0t_45%=:
s_cmp_eq_u32 s64, s65
s_cbranch_scc1 .Ll1t_34%=
.Ll0_32%=:
W 0
Cvaa 160,128,0,224
Ra 144,242,4096
Ra 148,242,5120
Cvaa 164,128,16,228
s_xor_b32 s62, s62, 0x10000
Ra 152,242,6144
Cvaa 168,128,32,232
X 184
Cvaa 172,128,48,236
X 185
Mvaa 160,132,4
X 186
Mvaa 164,132,20
Ra 156,242,7168
K 214,184,185
Mvaa 168,132,36
X 187
Mvaa 172,132,52
X 188
Mvaa 160,136,8
Ra 128,242,16384
Mvaa 164,136,24
K 215,186,187
Mvaa 168,136,40
X 189
Mvaa 172,136,56
X 190
Mvaa 160,140,12
Ra 132,242,17408
Mvaa 164,140,28
K 222,188,189
Mvaa 168,140,44
X 191
Mvaa 172,140,60
K 223,190,191
v_smfmac_f32_16x16x64_bf16 a[160:163], a[176:179], v[192:199], v240
Ra 136,242,18432
v_smfmac_f32_16x16x64_bf16 a[164:167], a[176:179], v[200:207], v240
X 160
v_smfmac_f32_16x16x64_bf16 a[168:171], a[176:179], v[208:215], v240
X 161
v_smfmac_f32_16x16x64_bf16 a[172:175], a[176:179], v[216:223], v240
W 3
X 162
Cvaa 176,144,0,224
Ra 140,242,19456
)"
R"(Cvaa 180,144,16,228
K 192,160,161
Cvaa 184,144,32,232
X 163
Cvaa 188,144,48,236
X 164
Mvaa 176,148,4
Ra 144,242,20480
Mvaa 180,148,20
K 193,162,163
Mvaa 184,148,36
X 165
Mvaa 188,148,52
X 166
Mvaa 176,152,8
Ra 148,242,21504
Mvaa 180,152,24
K 200,164,165
Mvaa 184,152,40
X 167
Mvaa 188,152,56
X 168
Mvaa 176,156,12
Ra 152,242,22528
Mvaa 180,156,28
K 201,166,167
Mvaa 184,156,44
X 169
Mvaa 188,156,60
W 3
Mvav 32,128,196
X 170
Mvav 64,128,204
Ra 156,242,23552
K 208,168,169
Mvav 96,128,212
X 171
Mvav 128,128,220
Mvav 132,132,220
X 172
Mvav 100,132,212
s_add_u32 s64, s64, 1
Ra 128,242,8192
K 209,170,171
Mvav 68,132,204
X 173
Mvav 36,132,196
Mvav 40,136,196
X 174
Mvav 72,136,204
Ra 132,242,9216
K 216,172,173
Mvav 104,136,212
X 175
Mvav 136,136,220
Mvav 140,140,220
X 176
Mvav 108,140,212
Ra 136,242,10240
K 217,174,175
Mvav 76,140,204
X 177
Mvav 44,140,196
W 3
Mvav 48,144,196
X 178
Mvav 80,144,204
Ra 140,242,11264
Mvav 112,144,212
K 194,176,177
Mvav 144,144,220
X 179
Mvav 148,148,220
X 180
Mvav 116,148,212
Ra 144,242,12288
Mvav 84,148,204
K 195,178,179
Mvav 52,148,196
X 181
Mvav 56,152,196
X 182
Mvav 88,152,204
Ra 148,242,13312
Mvav 120,152,212
K 202,180,181
Mvav 152,152,220
X 183
Mvav 156,156,220
X 184
Mvav 124,156,212
Ra 152,242,14336
Mvav 92,156,204
K 203,182,183
Mvav 60,156,196
W 3
X 185
Cvaa 160,128,0,224
X 186
Cvaa 164,128,16,228
Ra 156,242,15360
Cvaa 168,128,32,232
K 210,184,185
Cvaa 172,128,48,236
X 187
Mvaa 160,132,4
X 188
Mvaa 164,132,20
Ra 128,242,24576
Mvaa 168,132,36
K 211,186,187
Mvaa 172,132,52
X 189
Mvaa 160,136,8
X 190
Mvaa 164,136,24
Ra 132,242,25600
Mvaa 168,136,40
K 218,188,189
Mvaa 172,136,56
X 191
Mvaa 160,140,12
Ra 136,242,26624
Mvaa 164,140,28
Mvaa 168,140,44
Mvaa 172,140,60
W 3
Cvaa 176,144,0,224
Ra 140,242,27648
Cvaa 180,144,16,228
X 160
Cvaa 184,144,32,232
K 219,190,191
Cvaa 188,144,48,236
X 161
Mvaa 176,148,4
X 162
Mvaa 180,148,20
Ra 144,242,28672
K 196,160,161
Mvaa 184,148,36
X 163
Mvaa 188,148,52
X 164
Mvaa 176,152,8
Ra 148,242,29696
K 197,162,163
Mvaa 180,152,24
X 165
Mvaa 184,152,40
X 166
Mvaa 188,152,56
K 204,164,165
Mvaa 176,156,12
X 167
Mvaa 180,156,28
Ra 152,242,30720
Mvaa 184,156,44
X 168
Mvaa 188,156,60
W 3
K 205,166,167
Mvav 32,128,192
X 169
Mvav 64,128,200
Ra 156,242,31744
Mvav 96,128,208
X 170
Mvav 128,128,216
K 212,168,169
Mvav 132,132,216
X 171
Mvav 100,132,208
X 172
Mvav 68,132,200
Mvav 36,132,192
K 213,170,171
Mvav 40,136,192
X 173
Mvav 72,136,200
X 174
Mvav 104,136,208
Mvav 136,136,216
K 220,172,173
Mvav 140,140,216
X 175
Mvav 108,140,208
X 176
Mvav 76,140,200
W 0
s_barrier
v_xor_b32 v242, 0x10000, v242
s_bitcmp1_b32 s68, s64
s_cbranch_scc1 .Lact_48%=
.Lactr_49%=:
Mvav 44,140,192
Ra 128,242,0
K 221,174,175
Mvav 48,144,192
X 177
Mvav 80,144,200
X 178
Mvav 112,144,208
Ra 132,242,1024
Mvav 144,144,216
K 198,176,177
Mvav 148,148,216
X 179
Mvav 116,148,208
X 180
Mvav 84,148,200
Ra 136,242,2048
Mvav 52,148,192
K 199,178,179
Mvav 56,152,192
X 181
Mvav 88,152,200
Ra 140,242,3072
Mvav 120,152,208
X 182
Mvav 152,152,216
K 206,180,181
Mvav 156,156,216
X 183
Mvav 124,156,208
Mvav 92,156,200
K 207,182,183
Mvav 60,156,192
s_cmp_lg_u32 s64, s65
s_cbranch_scc1 .Ll0_32%=
s_cmp_lt_u32 s85, 54
s_cbranch_scc1 .Lmoe_43%=
.Ll1t_34%=:
s_cmp_eq_u32 s64, s66
s_cbranch_scc1 .Lsp1_35%=
.Ll1_33%=:
W 0
Cvaa 160,128,0,224
Ra 144,242,4096
Ra 148,242,5120
Cvaa 164,128,16,228
s_add_u32 m0, s62, 0
Ra 152,242,6144
Cvaa 168,128,32,232
X 184
Cvaa 172,128,48,236
X 185
Mvaa 160,132,4
X 186
Mvaa 164,132,20
Ra 156,242,7168
K 214,184,185
Mvaa 168,132,36
Dc 1,8,61,0
Mvaa 172,132,52
X 187
Mvaa 160,136,8
X 188
Mvaa 164,136,24
Ra 128,242,16384
K 215,186,187
Mvaa 168,136,40
Dc 1,8,61,1024
Mvaa 172,136,56
X 189
Mvaa 160,140,12
X 190
Mvaa 164,140,28
Ra 132,242,17408
K 222,188,189
Mvaa 168,140,44
Dc 1,8,61,2048
Mvaa 172,140,60
X 191
v_smfmac_f32_16x16x64_bf16 a[160:163], a[176:179], v[192:199], v240
Ra 136,242,18432
K 223,190,191
v_smfmac_f32_16x16x64_bf16 a[164:167], a[176:179], v[200:207], v240
X 160
v_smfmac_f32_16x16x64_bf16 a[168:171], a[176:179], v[208:215], v240
Dc 1,8,61,3072
v_smfmac_f32_16x16x64_bf16 a[172:175], a[176:179], v[216:223], v240
W 3
X 161
Cvaa 176,144,0,224
s_add_u32 m0, s62, 4096
X 162
Cvaa 180,144,16,228
Ra 140,242,19456
K 192,160,161
Cvaa 184,144,32,232
X 163
Cvaa 188,144,48,236
Dc 243,8,61,0
Mvaa 176,148,4
X 164
Mvaa 180,148,20
Ra 144,242,20480
K 193,162,163
Mvaa 184,148,36
X 165
Mvaa 188,148,52
Dc 243,8,61,1024
Mvaa 176,152,8
X 166
Mvaa 180,152,24
Ra 148,242,21504
K 200,164,165
Mvaa 184,152,40
X 167
Mvaa 188,152,56
Dc 243,8,61,2048
Mvaa 176,156,12
X 168
Mvaa 180,156,28
Ra 152,242,22528
K 201,166,167
Mvaa 184,156,44
X 169
Mvaa 188,156,60
W 3
Dc 243,8,61,3072
Mvav 32,128,196
s_add_u32 m0, s62, 8192
X 170
Mvav 64,128,204
Ra 156,242,23552
K 208,168,169
Mvav 96,128,212
X 171
Mvav 128,128,220
Dc 244,8,61,0
Mvav 132,132,220
X 172
Mvav 100,132,212
Ra 128,242,8192
K 209,170,171
Mvav 68,132,204
X 173
Mvav 36,132,196
Dc 244,8,61,1024
Mvav 40,136,196
X 174
Mvav 72,136,204
Ra 132,242,9216
K 216,172,173
Mvav 104,136,212
X 175
Mvav 136,136,220
Dc 244,8,61,2048
Mvav 140,140,220
X 176
Mvav 108,140,212
Ra 136,242,10240
K 217,174,175
Mvav 76,140,204
X 177
Mvav 44,140,196
W 3
Dc 244,8,61,3072
Mvav 48,144,196
X 178
Mvav 80,144,204
s_add_u32 m0, s62, 12288
Ra 140,242,11264
Mvav 112,144,212
K 194,176,177
Mvav 144,144,220
X 179
Mvav 148,148,220
X 180
Mvav 116,148,212
Dc 245,8,61,0
Ra 144,242,12288
Mvav 84,148,204
K 195,178,179
Mvav 52,148,196
X 181
Mvav 56,152,196
X 182
Mvav 88,152,204
Dc 245,8,61,1024
Ra 148,242,13312
Mvav 120,152,212
K 202,180,181
Mvav 152,152,220
X 183
Mvav 156,156,220
X 184
Mvav 124,156,212
Dc 245,8,61,2048
Ra 152,242,14336
Mvav 92,156,204
K 203,182,183
Mvav 60,156,196
W 3
X 185
Cvaa 160,128,0,224
X 186
Cvaa 164,128,16,228
)"
R"(Dc 245,8,61,3072
Ra 156,242,15360
Cvaa 168,128,32,232
s_add_u32 s61, s61, s63
K 210,184,185
Cvaa 172,128,48,236
X 187
Mvaa 160,132,4
X 188
Mvaa 164,132,20
Ra 128,242,24576
Mvaa 168,132,36
s_xor_b32 s62, s62, 0x10000
K 211,186,187
Mvaa 172,132,52
X 189
Mvaa 160,136,8
X 190
Mvaa 164,136,24
s_add_u32 s64, s64, 1
Ra 132,242,25600
Mvaa 168,136,40
K 218,188,189
Mvaa 172,136,56
X 191
Mvaa 160,140,12
Ra 136,242,26624
Mvaa 164,140,28
Mvaa 168,140,44
Mvaa 172,140,60
W 3
Cvaa 176,144,0,224
Ra 140,242,27648
Cvaa 180,144,16,228
X 160
Cvaa 184,144,32,232
K 219,190,191
Cvaa 188,144,48,236
X 161
Mvaa 176,148,4
X 162
Mvaa 180,148,20
Ra 144,242,28672
K 196,160,161
Mvaa 184,148,36
X 163
Mvaa 188,148,52
X 164
Mvaa 176,152,8
Ra 148,242,29696
K 197,162,163
Mvaa 180,152,24
X 165
Mvaa 184,152,40
X 166
Mvaa 188,152,56
K 204,164,165
Mvaa 176,156,12
X 167
Mvaa 180,156,28
Ra 152,242,30720
Mvaa 184,156,44
X 168
Mvaa 188,156,60
W 3
K 205,166,167
Mvav 32,128,192
X 169
Mvav 64,128,200
Ra 156,242,31744
Mvav 96,128,208
X 170
Mvav 128,128,216
K 212,168,169
Mvav 132,132,216
X 171
Mvav 100,132,208
X 172
Mvav 68,132,200
Mvav 36,132,192
K 213,170,171
Mvav 40,136,192
X 173
Mvav 72,136,200
X 174
Mvav 104,136,208
Mvav 136,136,216
K 220,172,173
Mvav 140,140,216
X 175
Mvav 108,140,208
X 176
Mvav 76,140,200
s_waitcnt vmcnt(0) lgkmcnt(0)
s_barrier
v_xor_b32 v242, 0x10000, v242
s_bitcmp1_b32 s68, s64
s_cbranch_scc1 .Lact_50%=
.Lactr_51%=:
Mvav 44,140,192
Ra 128,242,0
K 221,174,175
Mvav 48,144,192
X 177
Mvav 80,144,200
X 178
Mvav 112,144,208
Ra 132,242,1024
Mvav 144,144,216
K 198,176,177
Mvav 148,148,216
X 179
Mvav 116,148,208
X 180
Mvav 84,148,200
Ra 136,242,2048
Mvav 52,148,192
K 199,178,179
Mvav 56,152,192
X 181
Mvav 88,152,200
Ra 140,242,3072
Mvav 120,152,208
X 182
Mvav 152,152,216
K 206,180,181
Mvav 156,156,216
X 183
Mvav 124,156,208
Mvav 92,156,200
K 207,182,183
Mvav 60,156,192
s_cmp_lg_u32 s64, s66
s_cbranch_scc1 .Ll1_33%=
s_cmp_ge_u32 s14, 2
s_cbranch_scc1 .Lf1r_46%=
.Lsp1_35%=:
W 0
Cvaa 160,128,0,224
Ra 144,242,4096
Ra 148,242,5120
Cvaa 164,128,16,228
s_add_u32 m0, s62, 0
Ra 152,242,6144
Ra 156,242,7168
Cvaa 168,128,32,232
X 184
Cvaa 172,128,48,236
X 185
Mvaa 160,132,4
X 186
Mvaa 164,132,20
Ra 128,242,16384
K 214,184,185
Mvaa 168,132,36
Dc 1,8,61,0
Mvaa 172,132,52
X 187
Mvaa 160,136,8
X 188
Mvaa 164,136,24
Dc 1,8,61,1024
K 215,186,187
Mvaa 168,136,40
X 189
Mvaa 172,136,56
Ra 132,242,17408
Mvaa 160,140,12
X 190
Mvaa 164,140,28
Dc 1,8,61,2048
K 222,188,189
Mvaa 168,140,44
X 191
Mvaa 172,140,60
Ra 136,242,18432
K 223,190,191
v_smfmac_f32_16x16x64_bf16 a[160:163], a[176:179], v[192:199], v240
Dc 1,8,61,3072
v_cndmask_b32_e64 v160, v241, v160, s[72:73]
v_smfmac_f32_16x16x64_bf16 a[164:167], a[176:179], v[200:207], v240
s_add_u32 m0, s62, 4096
v_cndmask_b32_e64 v161, v241, v161, s[74:75]
v_cndmask_b32_e64 v162, v241, v162, s[76:77]
v_smfmac_f32_16x16x64_bf16 a[168:171], a[176:179], v[208:215], v240
v_cndmask_b32_e64 v163, v241, v163, s[78:79]
v_smfmac_f32_16x16x64_bf16 a[172:175], a[176:179], v[216:223], v240
W 3
X 160
Cvaa 180,144,16,228
X 161
Cvaa 184,144,32,232
Ra 140,242,19456
Dc 243,8,61,0
Cvaa 188,144,48,236
X 162
Mvaa 180,148,20
Ra 144,242,20480
K 192,160,161
Mvaa 184,148,36
X 163
Mvaa 188,148,52
Dc 243,8,61,1024
Ra 148,242,21504
Mvaa 180,152,24
X 164
Mvaa 184,152,40
K 193,162,163
Mvaa 188,152,56
Dc 243,8,61,2048
Ra 152,242,22528
Mvaa 180,156,28
X 165
Mvaa 184,156,44
X 166
Mvaa 188,156,60
W 3
Dc 243,8,61,3072
K 200,164,165
Mvav 32,128,196
s_add_u32 m0, s62, 8192
X 167
Mvav 64,128,204
X 168
Mvav 96,128,212
Ra 156,242,23552
K 201,166,167
Mvav 128,128,220
X 169
Mvav 132,132,220
X 170
Mvav 100,132,212
Dc 244,8,61,0
K 208,168,169
Mvav 68,132,204
X 171
Mvav 36,132,196
Ra 128,242,8192
Ra 132,242,9216
Mvav 40,136,196
Dc 244,8,61,1024
Mvav 72,136,204
X 172
Mvav 104,136,212
K 209,170,171
Mvav 136,136,220
X 173
Mvav 140,140,220
Dc 244,8,61,2048
Ra 136,242,10240
Mvav 108,140,212
X 174
Mvav 76,140,204
K 216,172,173
Mvav 44,140,196
W 3
Dc 244,8,61,3072
Ra 140,242,11264
Mvav 48,144,196
s_add_u32 m0, s62, 12288
X 175
Mvav 80,144,204
v_mov_b32 v194, 0
K 217,174,175
Mvav 112,144,212
v_mov_b32 v195, 0
v_cndmask_b32_e64 v180, v241, v180, s[72:73]
Mvav 144,144,220
v_cndmask_b32_e64 v181, v241, v181, s[74:75]
v_cndmask_b32_e64 v182, v241, v182, s[76:77]
Mvav 148,148,220
Dc 245,8,61,0
v_cndmask_b32_e64 v183, v241, v183, s[78:79]
Mvav 116,148,212
X 180
Mvav 84,148,204
Ra 144,242,12288
Mvav 52,148,196
X 181
Mvav 56,152,196
Dc 245,8,61,1024
Ra 148,242,13312
Mvav 88,152,204
X 182
Mvav 120,152,212
K 202,180,181
Mvav 152,152,220
X 183
Mvav 156,156,220
Dc 245,8,61,2048
Ra 152,242,14336
Mvav 124,156,212
X 184
Mvav 92,156,204
K 203,182,183
Mvav 60,156,196
W 3
Dc 245,8,61,3072
Ra 156,242,15360
Cvaa 168,128,32,232
s_add_u32 s61, s61, s63
X 185
Cvaa 172,128,48,236
s_xor_b32 s62, s62, 0x10000
X 186
Mvaa 168,132,36
Ra 128,242,24576
K 210,184,185
Mvaa 172,132,52
s_add_u32 s64, s64, 1
X 187
Mvaa 168,136,40
X 188
Mvaa 172,136,56
Ra 132,242,25600
K 211,186,187
Mvaa 168,140,44
Ra 136,242,26624
Mvaa 172,140,60
Ra 140,242,27648
X 189
X 190
K 218,188,189
X 191
v_mov_b32 v196, 0
K 219,190,191
W 4
Cvaa 188,144,48,236
Ra 144,242,28672
Mvaa 188,148,52
v_mov_b32 v197, 0
v_mov_b32 v204, 0
Mvaa 188,152,56
Ra 148,242,29696
v_mov_b32 v205, 0
Mvaa 188,156,60
W 2
Ra 152,242,30720
v_cndmask_b32_e64 v168, v241, v168, s[72:73]
Mvav 32,128,192
v_cndmask_b32_e64 v169, v241, v169, s[74:75]
v_cndmask_b32_e64 v170, v241, v170, s[76:77]
Mvav 64,128,200
Ra 156,242,31744
v_cndmask_b32_e64 v171, v241, v171, s[78:79]
Mvav 96,128,208
X 168
Mvav 128,128,216
X 169
Mvav 132,132,216
X 170
Mvav 100,132,208
v_mov_b32 v224, v250
Mvav 68,132,200
K 212,168,169
v_mov_b32 v225, v250
Mvav 36,132,192
X 171
Mvav 40,136,192
v_mov_b32 v226, v250
Mvav 72,136,200
X 172
Mvav 104,136,208
v_mov_b32 v227, v250
K 213,170,171
Mvav 136,136,216
X 173
Mvav 140,140,216
v_mov_b32 v228, v251
v_mov_b32 v229, v251
Mvav 108,140,208
X 174
Mvav 76,140,200
K 220,172,173
v_mov_b32 v230, v251
Mvav 44,140,192
W 0
X 175
Mvav 48,144,192
v_mov_b32 v231, v251
v_mov_b32 v198, 0
Mvav 80,144,200
K 221,174,175
v_mov_b32 v232, v248
Mvav 112,144,208
v_mov_b32 v199, 0
v_mov_b32 v206, 0
Mvav 144,144,216
v_mov_b32 v233, v248
v_mov_b32 v207, 0
Mvav 148,148,216
v_mov_b32 v214, 0
v_mov_b32 v234, v248
Mvav 116,148,208
v_mov_b32 v215, 0
v_cndmask_b32_e64 v188, v241, v188, s[72:73]
Mvav 84,148,200
v_mov_b32 v235, v248
v_cndmask_b32_e64 v189, v241, v189, s[74:75]
Mvav 52,148,192
)"
R"(v_cndmask_b32_e64 v190, v241, v190, s[76:77]
v_mov_b32 v236, v249
Mvav 56,152,192
v_cndmask_b32_e64 v191, v241, v191, s[78:79]
v_mov_b32 v237, v249
Mvav 88,152,200
X 188
Mvav 120,152,208
X 189
Mvav 152,152,216
v_mov_b32 v238, v249
v_mov_b32 v239, v249
Mvav 156,156,216
X 190
Mvav 124,156,208
s_waitcnt vmcnt(0)
s_barrier
v_xor_b32 v242, 0x10000, v242
s_bitcmp1_b32 s68, s64
s_cbranch_scc1 .Lact_52%=
.Lactr_53%=:
Mvav 92,156,200
Ra 128,242,40960
K 222,188,189
Mvav 60,156,192
Ra 132,242,41984
Ra 136,242,43008
Ra 140,242,44032
X 191
s_nop 0
K 223,190,191
s_cmp_eq_u32 s64, 15
s_cbranch_scc1 .Lfs_41%=
.Lsp2_36%=:
v_smfmac_f32_16x16x64_bf16 a[160:163], a[176:179], v[192:199], v240
s_add_u32 m0, s62, 0
Ra 144,242,45056
Ra 148,242,46080
Ra 152,242,47104
Ra 156,242,48128
v_smfmac_f32_16x16x64_bf16 a[164:167], a[176:179], v[200:207], v240
v_smfmac_f32_16x16x64_bf16 a[168:171], a[176:179], v[208:215], v240
v_smfmac_f32_16x16x64_bf16 a[172:175], a[176:179], v[216:223], v240
W 4
Dc 1,8,61,0
Cvaa 168,128,96,232
v_mov_b32 v192, 0
G 12,160
Cvaa 172,128,112,236
Dc 1,8,61,1024
Ra 128,242,16384
G 4,161
A 12,12,4
v_cmp_nlt_f32_e64 s[86:87], v12, s90
v_mov_b32 v193, 0
Mvaa 168,132,100
v_rcp_f32 v12, v12
s_or_b64 s[88:89], s[88:89], s[86:87]
v_accvgpr_write_b32 a160, 0
v_mov_b32 v200, 0
v_accvgpr_write_b32 a161, 0
v_accvgpr_write_b32 a162, 0
Mvaa 172,132,116
Dc 1,8,61,2048
v_accvgpr_write_b32 a163, 0
G 13,164
v_mov_b32 v201, 0
G 5,165
A 13,13,5
Mvaa 168,136,104
Ra 132,242,17408
v_cmp_nlt_f32_e64 s[86:87], v13, s90
v_rcp_f32 v13, v13
s_or_b64 s[88:89], s[88:89], s[86:87]
v_accvgpr_write_b32 a164, 0
v_accvgpr_write_b32 a165, 0
Mvaa 172,136,120
Dc 1,8,61,3072
v_accvgpr_write_b32 a166, 0
v_accvgpr_write_b32 a167, 0
U 32,32,12
U 33,33,12
U 34,34,12
Mvaa 168,140,108
Ra 136,242,18432
U 35,35,12
U 36,36,12
U 37,37,12
U 38,38,12
U 39,39,12
Mvaa 172,140,124
s_add_u32 m0, s62, 4096
Ra 140,242,19456
K 32,32,33
K 33,34,35
K 34,36,37
K 35,38,39
W 4
Cvaa 188,144,112,236
s_add_u32 s93, s67, 0
buffer_store_dwordx4 v[32:35], v1, s[8:11], s93 offen offset:0 sc1
Ra 144,242,20480
U 40,40,12
U 41,41,12
U 42,42,12
U 43,43,12
Mvaa 188,148,116
Ra 148,242,21504
U 44,44,12
U 45,45,12
U 46,46,12
U 47,47,12
K 40,40,41
Mvaa 188,152,120
v_cndmask_b32_e64 v168, v241, v168, s[72:73]
v_cndmask_b32_e64 v169, v241, v169, s[74:75]
v_cndmask_b32_e64 v170, v241, v170, s[76:77]
v_cndmask_b32_e64 v171, v241, v171, s[78:79]
X 168
Mvaa 188,156,124
W 2
Dc 243,8,61,0
X 169
K 41,42,43
K 42,44,45
K 43,46,47
Mvav 96,128,212
Ra 152,242,22528
Ra 156,242,23552
X 170
K 208,168,169
Mvav 128,128,220
buffer_store_dwordx4 v[40:43], v1, s[8:11], s93 offen offset:1024 sc1
U 48,48,12
U 49,49,12
U 50,50,12
U 51,51,12
U 52,52,12
Mvav 132,132,220
U 53,53,12
U 54,54,12
U 55,55,12
K 48,48,49
K 49,50,51
K 50,52,53
Mvav 100,132,212
Dc 243,8,61,1024
Ra 128,242,32768
Ra 132,242,33792
X 171
K 51,54,55
Mvav 104,136,212
X 172
K 209,170,171
X 173
Mvav 136,136,220
buffer_store_dwordx4 v[48:51], v1, s[8:11], s93 offen offset:2048 sc1
U 56,56,12
U 57,57,12
U 58,58,12
U 59,59,12
U 60,60,12
Mvav 140,140,220
U 61,61,12
U 62,62,12
U 63,63,12
K 56,56,57
K 57,58,59
K 58,60,61
Mvav 108,140,212
W 2
Dc 243,8,61,2048
Ra 136,242,34816
Ra 140,242,35840
K 59,62,63
X 174
Mvav 112,144,212
K 216,172,173
X 175
Mvav 144,144,220
buffer_store_dwordx4 v[56:59], v1, s[8:11], s93 offen offset:3072 sc1
Ra 144,242,36864
G 14,168
G 6,169
A 14,14,6
v_cmp_nlt_f32_e64 s[86:87], v14, s90
Mvav 148,148,220
v_rcp_f32 v14, v14
s_or_b64 s[88:89], s[88:89], s[86:87]
v_accvgpr_write_b32 a168, 0
v_accvgpr_write_b32 a169, 0
v_accvgpr_write_b32 a170, 0
v_accvgpr_write_b32 a171, 0
Mvav 116,148,212
Dc 243,8,61,3072
s_add_u32 m0, s62, 8192
G 15,172
G 7,173
A 15,15,7
v_cmp_nlt_f32_e64 s[86:87], v15, s90
v_mov_b32 v194, 0
Mvav 120,152,212
v_rcp_f32 v15, v15
s_or_b64 s[88:89], s[88:89], s[86:87]
v_accvgpr_write_b32 a172, 0
v_accvgpr_write_b32 a173, 0
v_accvgpr_write_b32 a174, 0
v_accvgpr_write_b32 a175, 0
Mvav 152,152,220
Dc 244,8,61,0
U 64,64,13
U 65,65,13
U 66,66,13
U 67,67,13
U 68,68,13
Mvav 156,156,220
U 69,69,13
U 70,70,13
U 71,71,13
K 64,64,65
K 65,66,67
K 66,68,69
Mvav 124,156,212
W 1
Dc 244,8,61,1024
Ra 148,242,37888
K 67,70,71
s_add_u32 s93, s67, 131072
K 217,174,175
v_mov_b32 v195, 0
Cvaa 160,128,64,224
Ra 152,242,38912
Ra 156,242,39936
v_mov_b32 v202, 0
v_mov_b32 v203, 0
v_mov_b32 v210, 0
v_mov_b32 v211, 0
Cvaa 164,128,80,228
buffer_store_dwordx4 v[64:67], v1, s[8:11], s93 offen offset:0 sc1
U 72,72,13
U 73,73,13
U 74,74,13
U 75,75,13
U 76,76,13
Cvaa 168,128,96,232
U 77,77,13
U 78,78,13
U 79,79,13
K 72,72,73
K 73,74,75
K 74,76,77
Cvaa 172,128,112,236
Dc 244,8,61,2048
K 75,78,79
v_cndmask_b32_e64 v188, v241, v188, s[72:73]
v_cndmask_b32_e64 v189, v241, v189, s[74:75]
v_cndmask_b32_e64 v190, v241, v190, s[76:77]
v_cndmask_b32_e64 v191, v241, v191, s[78:79]
Mvaa 160,132,68
Ra 128,242,49152
X 188
X 189
Mvaa 164,132,84
buffer_store_dwordx4 v[72:75], v1, s[8:11], s93 offen offset:1024 sc1
U 80,80,13
U 81,81,13
U 82,82,13
U 83,83,13
U 84,84,13
Mvaa 168,132,100
U 85,85,13
U 86,86,13
U 87,87,13
K 80,80,81
K 81,82,83
K 82,84,85
Mvaa 172,132,116
Dc 244,8,61,3072
s_add_u32 m0, s62, 12288
Ra 132,242,50176
K 83,86,87
X 190
Mvaa 160,136,72
K 218,188,189
X 191
Mvaa 164,136,88
buffer_store_dwordx4 v[80:83], v1, s[8:11], s93 offen offset:2048 sc1
U 88,88,13
U 89,89,13
U 90,90,13
U 91,91,13
U 92,92,13
Mvaa 168,136,104
U 93,93,13
U 94,94,13
U 95,95,13
K 88,88,89
K 89,90,91
K 90,92,93
Mvaa 172,136,120
K 91,94,95
buffer_store_dwordx4 v[88:91], v1, s[8:11], s93 offen offset:3072 sc1
U 96,96,14
U 97,97,14
U 98,98,14
U 99,99,14
Mvaa 160,140,76
U 100,100,14
U 101,101,14
U 102,102,14
U 103,103,14
K 96,96,97
K 97,98,99
Mvaa 164,140,92
Dc 245,8,61,0
Ra 136,242,51200
K 98,100,101
K 99,102,103
s_add_u32 s93, s67, 262144
Mvaa 168,140,108
Mvaa 172,140,124
W 3
Dc 245,8,61,1024
Ra 140,242,52224
v_cndmask_b32_e64 v160, v241, v160, s[72:73]
K 219,190,191
v_cndmask_b32_e64 v161, v241, v161, s[74:75]
v_cndmask_b32_e64 v162, v241, v162, s[76:77]
Cvaa 180,144,80,228
v_cndmask_b32_e64 v163, v241, v163, s[78:79]
X 160
X 161
Cvaa 184,144,96,232
buffer_store_dwordx4 v[96:99], v1, s[8:11], s93 offen offset:0 sc1
U 104,104,14
U 105,105,14
U 106,106,14
U 107,107,14
U 108,108,14
Cvaa 188,144,112,236
U 109,109,14
U 110,110,14
U 111,111,14
K 104,104,105
K 105,106,107
K 106,108,109
Mvaa 180,148,84
Dc 245,8,61,2048
Ra 144,242,53248
K 107,110,111
X 162
K 196,160,161
Mvaa 184,148,100
X 163
Mvaa 188,148,116
buffer_store_dwordx4 v[104:107], v1, s[8:11], s93 offen offset:1024 sc1
U 112,112,14
U 113,113,14
U 114,114,14
U 115,115,14
U 116,116,14
Mvaa 180,152,88
U 117,117,14
U 118,118,14
U 119,119,14
K 112,112,113
K 113,114,115
K 114,116,117
Mvaa 184,152,104
Dc 245,8,61,3072
s_add_u32 s61, s61, s63
s_xor_b32 s62, s62, 0x10000
s_add_u32 s64, s64, 1
K 115,118,119
X 164
Mvaa 188,152,120
Ra 148,242,54272
Ra 152,242,55296
K 197,162,163
X 165
Mvaa 180,156,92
buffer_store_dwordx4 v[112:115], v1, s[8:11], s93 offen offset:2048 sc1
U 120,120,14
U 121,121,14
U 122,122,14
U 123,123,14
U 124,124,14
Mvaa 184,156,108
U 125,125,14
U 126,126,14
U 127,127,14
K 120,120,121
K 121,122,123
K 122,124,125
Mvaa 188,156,124
W 3
K 123,126,127
buffer_store_dwordx4 v[120:123], v1, s[8:11], s93 offen offset:3072 sc1
U 128,128,15
U 129,129,15
)"
R"(U 130,130,15
U 131,131,15
Zvav 32,128,192
U 132,132,15
U 133,133,15
U 134,134,15
U 135,135,15
K 128,128,129
K 129,130,131
Zvav 64,128,200
K 130,132,133
K 131,134,135
s_add_u32 s93, s67, 393216
buffer_store_dwordx4 v[128:131], v1, s[8:11], s93 offen offset:0 sc1
U 136,136,15
U 137,137,15
U 138,138,15
Zvav 96,128,208
U 139,139,15
U 140,140,15
U 141,141,15
U 142,142,15
U 143,143,15
K 136,136,137
Zvav 128,128,216
K 137,138,139
K 138,140,141
K 139,142,143
buffer_store_dwordx4 v[136:139], v1, s[8:11], s93 offen offset:1024 sc1
U 144,144,15
U 145,145,15
Zvav 132,132,216
U 146,146,15
U 147,147,15
U 148,148,15
U 149,149,15
U 150,150,15
U 151,151,15
Zvav 100,132,208
K 144,144,145
K 145,146,147
K 146,148,149
K 147,150,151
buffer_store_dwordx4 v[144:147], v1, s[8:11], s93 offen offset:2048 sc1
U 152,152,15
Zvav 68,132,200
U 153,153,15
U 154,154,15
U 155,155,15
U 156,156,15
U 157,157,15
U 158,158,15
Zvav 36,132,192
U 159,159,15
K 152,152,153
K 153,154,155
K 154,156,157
K 155,158,159
buffer_store_dwordx4 v[152:155], v1, s[8:11], s93 offen offset:3072 sc1
Zvav 40,136,192
Ra 156,242,56320
X 166
K 204,164,165
X 167
Zvav 72,136,200
X 168
K 205,166,167
X 169
Zvav 104,136,208
X 170
K 212,168,169
X 171
Zvav 136,136,216
X 172
K 213,170,171
X 173
Zvav 140,140,216
Zvav 108,140,208
X 174
Zvav 76,140,200
s_waitcnt vmcnt(6) lgkmcnt(0)
s_barrier
v_xor_b32 v242, 0x10000, v242
s_bitcmp1_b32 s68, s64
s_cbranch_scc1 .Lact_54%=
.Lactr_55%=:
Zvav 44,140,192
Ra 128,242,40960
K 220,172,173
X 175
Zvav 48,144,192
Zvav 80,144,200
v_mov_b32 v198, 0
Zvav 112,144,208
Ra 132,242,41984
K 221,174,175
Zvav 144,144,216
v_mov_b32 v199, 0
v_cndmask_b32_e64 v180, v241, v180, s[72:73]
Zvav 148,148,216
v_cndmask_b32_e64 v181, v241, v181, s[74:75]
Zvav 116,148,208
Ra 136,242,43008
v_cndmask_b32_e64 v182, v241, v182, s[76:77]
Zvav 84,148,200
v_cndmask_b32_e64 v183, v241, v183, s[78:79]
Zvav 52,148,192
X 180
Zvav 56,152,192
Ra 140,242,44032
X 181
Zvav 88,152,200
Zvav 120,152,208
X 182
Zvav 152,152,216
K 206,180,181
Zvav 156,156,216
X 183
Zvav 124,156,208
Zvav 92,156,200
s_waitcnt vmcnt(0)
s_and_b64 s[86:87], s[88:89], s[24:25]
s_cbranch_scc1 .LL_56%=
v_mov_b64 v[28:29], s[12:13]
s_mov_b64 exec, 1
buffer_store_dwordx2 v[28:29], off, s[8:11], s69 sc1
s_mov_b64 exec, -1
.LL_56%=:
K 207,182,183
Zvav 60,156,192
s_cmp_eq_u32 s64, 15
s_cbranch_scc1 .Lf2_40%=
.Ll2_37%=:
W 0
Cvaa 160,128,64,224
Ra 144,242,45056
Ra 148,242,46080
Cvaa 164,128,80,228
s_add_u32 m0, s62, 0
Ra 152,242,47104
Cvaa 168,128,96,232
X 184
Cvaa 172,128,112,236
X 185
Mvaa 160,132,68
X 186
Mvaa 164,132,84
Ra 156,242,48128
K 214,184,185
Mvaa 168,132,100
Dc 1,8,61,0
Mvaa 172,132,116
X 187
Mvaa 160,136,72
X 188
Mvaa 164,136,88
Ra 128,242,57344
K 215,186,187
Mvaa 168,136,104
Dc 1,8,61,1024
Mvaa 172,136,120
X 189
Mvaa 160,140,76
X 190
Mvaa 164,140,92
Ra 132,242,58368
K 222,188,189
Mvaa 168,140,108
Dc 1,8,61,2048
Mvaa 172,140,124
X 191
v_smfmac_f32_16x16x64_bf16 a[160:163], a[176:179], v[192:199], v240
Ra 136,242,59392
K 223,190,191
v_smfmac_f32_16x16x64_bf16 a[164:167], a[176:179], v[200:207], v240
X 160
v_smfmac_f32_16x16x64_bf16 a[168:171], a[176:179], v[208:215], v240
Dc 1,8,61,3072
v_smfmac_f32_16x16x64_bf16 a[172:175], a[176:179], v[216:223], v240
W 3
X 161
Cvaa 176,144,64,224
s_add_u32 m0, s62, 4096
X 162
Cvaa 180,144,80,228
Ra 140,242,60416
K 192,160,161
Cvaa 184,144,96,232
X 163
Cvaa 188,144,112,236
Dc 243,8,61,0
Mvaa 176,148,68
X 164
Mvaa 180,148,84
Ra 144,242,61440
K 193,162,163
Mvaa 184,148,100
X 165
Mvaa 188,148,116
Dc 243,8,61,1024
Mvaa 176,152,72
X 166
Mvaa 180,152,88
Ra 148,242,62464
K 200,164,165
Mvaa 184,152,104
X 167
Mvaa 188,152,120
Dc 243,8,61,2048
Mvaa 176,156,76
X 168
Mvaa 180,156,92
Ra 152,242,63488
K 201,166,167
Mvaa 184,156,108
X 169
Mvaa 188,156,124
W 3
Dc 243,8,61,3072
Mvav 32,128,196
s_add_u32 m0, s62, 8192
X 170
Mvav 64,128,204
Ra 156,242,64512
K 208,168,169
Mvav 96,128,212
X 171
Mvav 128,128,220
Dc 244,8,61,0
Mvav 132,132,220
X 172
Mvav 100,132,212
Ra 128,242,32768
K 209,170,171
Mvav 68,132,204
X 173
Mvav 36,132,196
Dc 244,8,61,1024
Mvav 40,136,196
X 174
Mvav 72,136,204
Ra 132,242,33792
K 216,172,173
Mvav 104,136,212
X 175
Mvav 136,136,220
Dc 244,8,61,2048
Mvav 140,140,220
X 176
Mvav 108,140,212
Ra 136,242,34816
K 217,174,175
Mvav 76,140,204
X 177
Mvav 44,140,196
W 3
Dc 244,8,61,3072
Mvav 48,144,196
X 178
Mvav 80,144,204
s_add_u32 m0, s62, 12288
Ra 140,242,35840
Mvav 112,144,212
K 194,176,177
Mvav 144,144,220
X 179
Mvav 148,148,220
X 180
Mvav 116,148,212
Dc 245,8,61,0
Ra 144,242,36864
Mvav 84,148,204
K 195,178,179
Mvav 52,148,196
X 181
Mvav 56,152,196
X 182
Mvav 88,152,204
Dc 245,8,61,1024
Ra 148,242,37888
Mvav 120,152,212
K 202,180,181
Mvav 152,152,220
X 183
Mvav 156,156,220
X 184
Mvav 124,156,212
Dc 245,8,61,2048
Ra 152,242,38912
Mvav 92,156,204
K 203,182,183
Mvav 60,156,196
W 3
X 185
Cvaa 160,128,64,224
X 186
Cvaa 164,128,80,228
Dc 245,8,61,3072
Ra 156,242,39936
Cvaa 168,128,96,232
s_add_u32 s61, s61, s63
K 210,184,185
Cvaa 172,128,112,236
X 187
Mvaa 160,132,68
X 188
Mvaa 164,132,84
Ra 128,242,49152
Mvaa 168,132,100
s_xor_b32 s62, s62, 0x10000
K 211,186,187
Mvaa 172,132,116
X 189
Mvaa 160,136,72
X 190
Mvaa 164,136,88
s_add_u32 s64, s64, 1
Ra 132,242,50176
Mvaa 168,136,104
K 218,188,189
Mvaa 172,136,120
X 191
Mvaa 160,140,76
Ra 136,242,51200
Mvaa 164,140,92
Mvaa 168,140,108
Mvaa 172,140,124
W 3
Cvaa 176,144,64,224
Ra 140,242,52224
Cvaa 180,144,80,228
X 160
Cvaa 184,144,96,232
K 219,190,191
Cvaa 188,144,112,236
X 161
Mvaa 176,148,68
X 162
Mvaa 180,148,84
Ra 144,242,53248
K 196,160,161
Mvaa 184,148,100
X 163
Mvaa 188,148,116
X 164
Mvaa 176,152,72
Ra 148,242,54272
K 197,162,163
Mvaa 180,152,88
X 165
Mvaa 184,152,104
X 166
Mvaa 188,152,120
K 204,164,165
Mvaa 176,156,76
X 167
Mvaa 180,156,92
Ra 152,242,55296
Mvaa 184,156,108
X 168
Mvaa 188,156,124
W 3
K 205,166,167
Mvav 32,128,192
X 169
Mvav 64,128,200
Ra 156,242,56320
Mvav 96,128,208
X 170
Mvav 128,128,216
K 212,168,169
Mvav 132,132,216
X 171
Mvav 100,132,208
X 172
Mvav 68,132,200
Mvav 36,132,192
K 213,170,171
Mvav 40,136,192
X 173
Mvav 72,136,200
X 174
Mvav 104,136,208
Mvav 136,136,216
K 220,172,173
Mvav 140,140,216
X 175
Mvav 108,140,208
X 176
Mvav 76,140,200
s_waitcnt vmcnt(0) lgkmcnt(0)
s_barrier
v_xor_b32 v242, 0x10000, v242
s_bitcmp1_b32 s68, s64
s_cbranch_scc1 .Lact_57%=
.Lactr_58%=:
Mvav 44,140,192
Ra 128,242,40960
K 221,174,175
Mvav 48,144,192
)"
R"(X 177
Mvav 80,144,200
X 178
Mvav 112,144,208
Ra 132,242,41984
Mvav 144,144,216
K 198,176,177
Mvav 148,148,216
X 179
Mvav 116,148,208
X 180
Mvav 84,148,200
Ra 136,242,43008
Mvav 52,148,192
K 199,178,179
Mvav 56,152,192
X 181
Mvav 88,152,200
Ra 140,242,44032
Mvav 120,152,208
X 182
Mvav 152,152,216
K 206,180,181
Mvav 156,156,216
X 183
Mvav 124,156,208
Mvav 92,156,200
K 207,182,183
Mvav 60,156,192
s_cmp_lg_u32 s64, 15
s_cbranch_scc1 .Ll2_37%=
.Lf2_40%=:
W 0
Cvaa 160,128,64,224
Ra 144,242,45056
Ra 148,242,46080
Cvaa 164,128,80,228
Ra 152,242,47104
v_bfe_u32 v28, v1, 4, 2
Cvaa 168,128,96,232
X 184
Cvaa 172,128,112,236
X 185
Mvaa 160,132,68
Ra 156,242,48128
v_lshlrev_b32 v28, 3, v28
Mvaa 164,132,84
X 186
Mvaa 168,132,100
buffer_load_dwordx2 v[28:29], v28, s[8:11], s54 offen sc1
K 214,184,185
Mvaa 172,132,116
s_lshl_b32 s94, s58, 2
X 187
Mvaa 160,136,72
s_add_u32 s94, s94, s51
X 188
Mvaa 164,136,88
s_add_u32 s94, s94, 3
Ra 128,242,57344
K 215,186,187
Mvaa 168,136,104
s_and_b32 s94, s94, 15
X 189
Mvaa 172,136,120
X 190
Mvaa 160,140,76
s_mul_i32 s94, s94, 10240
Ra 132,242,58368
K 222,188,189
Mvaa 164,140,92
s_add_u32 s94, s94, s48
X 191
Mvaa 168,140,108
K 223,190,191
Mvaa 172,140,124
s_lshl_b32 s94, s94, 2
v_smfmac_f32_16x16x64_bf16 a[160:163], a[176:179], v[192:199], v240
s_mul_i32 s95, s51, 10240
Ra 136,242,59392
v_smfmac_f32_16x16x64_bf16 a[164:167], a[176:179], v[200:207], v240
X 160
v_smfmac_f32_16x16x64_bf16 a[168:171], a[176:179], v[208:215], v240
s_add_u32 s95, s95, s48
X 161
v_smfmac_f32_16x16x64_bf16 a[172:175], a[176:179], v[216:223], v240
W 3
X 162
Cvaa 176,144,64,224
s_add_u32 m0, s95, 0
Ra 140,242,60416
K 192,160,161
Cvaa 180,144,80,228
X 163
Cvaa 184,144,96,232
X 164
Cvaa 188,144,112,236
D 16,40,94,0
K 193,162,163
Mvaa 176,148,68
X 165
Mvaa 180,148,84
X 166
Mvaa 184,148,100
D 17,40,94,1024
K 200,164,165
Mvaa 188,148,116
X 167
Mvaa 176,152,72
Ra 144,242,61440
Ra 148,242,62464
Mvaa 180,152,88
D 18,40,94,2048
Mvaa 184,152,104
X 172
Mvaa 188,152,120
K 201,166,167
Mvaa 176,156,76
D 19,40,94,3072
Ra 152,242,63488
Mvaa 180,156,92
s_add_u32 m0, s95, 4096
X 173
Mvaa 184,156,108
X 174
Mvaa 188,156,124
W 3
buffer_load_dwordx4 v20, s[40:43], s94 offen lds
K 216,172,173
Mvav 32,128,196
s_lshl_b32 s94, s58, 2
X 175
Mvav 64,128,204
s_add_u32 s94, s94, s51
X 168
Mvav 96,128,212
s_add_u32 s94, s94, 5
s_and_b32 s94, s94, 15
K 217,174,175
Mvav 128,128,220
s_mul_i32 s94, s94, 10240
X 169
Mvav 132,132,220
s_add_u32 s94, s94, s48
X 170
Mvav 100,132,212
s_lshl_b32 s94, s94, 2
Ra 156,242,64512
K 208,168,169
Mvav 68,132,204
s_mov_b32 s18, 40960
X 171
Mvav 36,132,196
s_mul_i32 s95, s51, 10240
X 176
Mvav 40,136,196
s_mov_b32 s19, 73728
s_bitcmp1_b32 s14, 0
s_cselect_b32 s18, 49152, s18
s_cselect_b32 s19, 98304, s19
K 209,170,171
Mvav 72,136,204
s_add_u32 s95, s95, s48
X 177
Mvav 104,136,212
s_cmp_lt_u32 s14, 2
s_cselect_b32 s20, s18, s19
s_cselect_b32 s21, s19, s18
X 178
Mvav 136,136,220
s_add_u32 m0, s95, 20480
Ra 128,242,49152
K 194,176,177
Mvav 140,140,220
X 179
Mvav 108,140,212
v_add_u32 v2, s20, v1
v_add_u32 v3, s21, v1
Mvav 76,140,204
s_lshl_b32 s18, s14, 9
X 180
Mvav 44,140,196
W 1
D 16,40,94,0
K 195,178,179
Mvav 48,144,196
X 181
Mvav 80,144,204
Ra 132,242,50176
v_lshrrev_b32 v232, 1, v1
Mvav 112,144,212
D 17,40,94,1024
v_add_u32 v232, s18, v232
Mvav 144,144,220
X 182
Mvav 148,148,220
K 202,180,181
v_bfe_u32 v12, v1, 6, 2
Mvav 116,148,212
D 18,40,94,2048
v_lshrrev_b32 v13, 8, v1
Mvav 84,148,204
X 183
Mvav 52,148,196
X 184
Mvav 56,152,196
D 19,40,94,3072
Ra 136,242,51200
Mvav 88,152,204
s_add_u32 m0, s95, 24576
v_bfe_u32 v14, v1, 4, 1
K 203,182,183
Mvav 120,152,212
v_lshlrev_b32 v14, 4, v14
v_mov_b32 v15, 0x3f80
Mvav 152,152,220
buffer_load_dwordx4 v20, s[40:43], s94 offen lds
Ra 140,242,52224
Mvav 156,156,220
s_waitcnt vmcnt(10)
v_cmp_eq_u64 vcc, s[12:13], v[28:29]
s_cmp_eq_u64 vcc, -1
s_cselect_b32 s46, 79691776, 0
X 185
Mvav 124,156,212
s_add_u32 s81, s58, 0
X 186
Mvav 92,156,204
s_and_b32 s81, s81, 3
v_lshlrev_b32 v15, v14, v15
K 210,184,185
Mvav 60,156,196
s_lshl_b32 s81, s81, 12
v_bfe_u32 v14, v1, 5, 1
v_xor_b32 v27, v12, v13
Maav 160,180,192
X 187
X 188
K 211,186,187
Maav 164,180,200
X 189
X 190
K 218,188,189
X 191
s_nop 0
K 219,190,191
Maav 168,180,208
s_add_u32 s81, s81, s49
Ra 144,242,53248
v_lshl_or_b32 v27, v27, 1, v14
Maav 172,180,216
W 1
v_cmp_eq_u32_e64 s[86:87], 0, v27
v_mov_b32 v226, 0
Mvav 32,128,192
s_add_u32 s82, s81, 131072
Ra 148,242,54272
v_cndmask_b32_e64 v224, 0, v15, s[86:87]
Mvav 64,128,200
buffer_load_dwordx4 a[184:187], v1, s[44:47], s81 offen offset:0 sc1
v_mov_b32 v228, 0
Mvav 96,128,208
v_mov_b32 v230, v224
v_cmp_eq_u32_e64 s[86:87], 1, v27
Mvav 128,128,216
Ra 152,242,55296
v_mov_b32 v227, 0
Mvav 132,132,216
buffer_load_dwordx4 a[200:203], v1, s[44:47], s82 offen offset:0 sc1
v_cndmask_b32_e64 v225, 0, v15, s[86:87]
Mvav 100,132,208
v_mov_b32 v229, 0
v_mov_b32 v231, v225
Mvav 68,132,200
Ra 156,242,56320
G 4,160
Mvav 36,132,192
buffer_load_dwordx4 a[188:191], v1, s[44:47], s81 offen offset:1024 sc1
G 8,161
Mvav 40,136,192
G 5,164
G 9,165
Mvav 72,136,200
G 6,168
G 10,169
Mvav 104,136,208
buffer_load_dwordx4 a[204:207], v1, s[44:47], s82 offen offset:1024 sc1
G 7,172
Mvav 136,136,216
G 11,173
A 4,4,8
Mvav 140,140,216
A 5,5,9
A 6,6,10
Mvav 108,140,208
buffer_load_dwordx4 a[192:195], v1, s[44:47], s81 offen offset:2048 sc1
A 7,7,11
Mvav 76,140,200
ds_write_b64 v232, v[6:7] offset:57344
A 8,4,5
Mvav 44,140,192
W 1
A 9,6,7
A 8,8,9
Mvav 48,144,192
buffer_load_dwordx4 a[208:211], v1, s[44:47], s82 offen offset:2048 sc1
v_cmp_ngt_f32_e64 s[86:87], s90, v8
Mvav 80,144,200
s_lshl_b32 s19, s14, 2
v_mov_b32 v9, s19
Mvav 112,144,208
s_or_b64 s[88:89], s[88:89], s[86:87]
s_and_b64 s[88:89], s[88:89], s[24:25]
s_cselect_b32 s18, 1, 0
v_mov_b32 v8, s18
Mvav 144,144,216
buffer_load_dwordx4 a[196:199], v1, s[44:47], s81 offen offset:3072 sc1
ds_write_b32 v9, v8 offset:59392
Mvav 148,148,216
v_xor_b32 v10, 1024, v232
v_bfe_u32 v11, v1, 2, 4
Mvav 116,148,208
K 96,96,97
K 97,98,99
Mvav 84,148,200
buffer_load_dwordx4 a[212:215], v1, s[44:47], s82 offen offset:3072 sc1
K 98,100,101
Mvav 52,148,192
s_add_u32 s81, s58, 1
s_and_b32 s81, s81, 3
s_lshl_b32 s81, s81, 12
Mvav 56,152,192
s_add_u32 s81, s81, s49
s_add_u32 s82, s81, 131072
K 99,102,103
Mvav 88,152,200
ds_write_b128 v2, v[96:99] offset:0
K 128,128,129
Mvav 120,152,208
K 129,130,131
K 130,132,133
Mvav 152,152,216
buffer_load_dwordx4 a[216:219], v1, s[44:47], s81 offen offset:0 sc1
K 131,134,135
Mvav 156,156,216
ds_write_b128 v2, v[128:131] offset:4096
K 104,104,105
Mvav 124,156,208
K 105,106,107
K 106,108,109
Mvav 92,156,200
buffer_load_dwordx4 a[232:235], v1, s[44:47], s82 offen offset:0 sc1
K 107,110,111
Mvav 60,156,192
ds_write_b128 v2, v[104:107] offset:1024
K 136,136,137
buffer_load_dwordx4 a[220:223], v1, s[44:47], s81 offen offset:1024 sc1
buffer_load_dwordx4 a[236:239], v1, s[44:47], s82 offen offset:1024 sc1
K 137,138,139
K 138,140,141
buffer_load_dwordx4 a[224:227], v1, s[44:47], s81 offen offset:2048 sc1
K 139,142,143
ds_write_b128 v2, v[136:139] offset:5120
buffer_load_dwordx4 a[240:243], v1, s[44:47], s82 offen offset:2048 sc1
buffer_load_dwordx4 a[228:231], v1, s[44:47], s81 offen offset:3072 sc1
buffer_load_dwordx4 a[244:247], v1, s[44:47], s82 offen offset:3072 sc1
K 112,112,113
K 113,114,115
K 114,116,117
K 115,118,119
ds_write_b128 v2, v[112:115] offset:2048
K 144,144,145
K 145,146,147
K 146,148,149
K 147,150,151
ds_write_b128 v2, v[144:147] offset:6144
K 120,120,121
K 121,122,123
K 122,124,125
K 123,126,127
ds_write_b128 v2, v[120:123] offset:3072
K 152,152,153
K 153,154,155
K 154,156,157
K 155,158,159
ds_write_b128 v2, v[152:155] offset:7168
s_branch .Lend_42%=
.Lfs_41%=:
v_smfmac_f32_16x16x64_bf16 a[160:163], a[176:179], v[192:199], v240
Ra 144,242,45056
Ra 148,242,46080
Ra 152,242,47104
Ra 156,242,48128
v_bfe_u32 v28, v1, 4, 2
v_smfmac_f32_16x16x64_bf16 a[164:167], a[176:179], v[200:207], v240
v_lshlrev_b32 v28, 3, v28
v_smfmac_f32_16x16x64_bf16 a[168:171], a[176:179], v[208:215], v240
buffer_load_dwordx2 v[28:29], v28, s[8:11], s54 offen sc1
v_smfmac_f32_16x16x64_bf16 a[172:175], a[176:179], v[216:223], v240
W 4
)"
R"(s_lshl_b32 s94, s58, 2
Cvaa 168,128,96,232
s_add_u32 s94, s94, s51
v_mov_b32 v192, 0
G 12,160
Cvaa 172,128,112,236
G 4,161
A 12,12,4
v_cmp_nlt_f32_e64 s[86:87], v12, s90
v_rcp_f32 v12, v12
s_or_b64 s[88:89], s[88:89], s[86:87]
v_accvgpr_write_b32 a160, 0
Mvaa 168,132,100
s_add_u32 s94, s94, 3
Ra 128,242,16384
v_accvgpr_write_b32 a161, 0
v_accvgpr_write_b32 a162, 0
v_accvgpr_write_b32 a163, 0
G 13,164
G 5,165
Mvaa 172,132,116
s_and_b32 s94, s94, 15
v_mov_b32 v193, 0
A 13,13,5
v_cmp_nlt_f32_e64 s[86:87], v13, s90
v_rcp_f32 v13, v13
s_or_b64 s[88:89], s[88:89], s[86:87]
Mvaa 168,136,104
s_mul_i32 s94, s94, 10240
v_accvgpr_write_b32 a164, 0
v_accvgpr_write_b32 a165, 0
v_accvgpr_write_b32 a166, 0
v_accvgpr_write_b32 a167, 0
U 32,32,12
U 33,33,12
Mvaa 172,136,120
s_add_u32 s94, s94, s48
U 34,34,12
v_mov_b32 v200, 0
U 35,35,12
U 36,36,12
U 37,37,12
U 38,38,12
Mvaa 168,140,108
s_lshl_b32 s94, s94, 2
Ra 132,242,17408
Ra 136,242,18432
U 39,39,12
K 32,32,33
K 33,34,35
K 34,36,37
Mvaa 172,140,124
Ra 140,242,19456
K 35,38,39
s_add_u32 s93, s67, 0
buffer_store_dwordx4 v[32:35], v1, s[8:11], s93 offen offset:0 sc1
U 40,40,12
v_mov_b32 v201, 0
W 4
Cvaa 188,144,112,236
s_mul_i32 s95, s51, 10240
U 41,41,12
U 42,42,12
U 43,43,12
U 44,44,12
U 45,45,12
U 46,46,12
Mvaa 188,148,116
s_add_u32 s95, s95, s48
U 47,47,12
K 40,40,41
K 41,42,43
K 42,44,45
K 43,46,47
buffer_store_dwordx4 v[40:43], v1, s[8:11], s93 offen offset:1024 sc1
Mvaa 188,152,120
s_add_u32 m0, s95, 0
U 48,48,12
U 49,49,12
U 50,50,12
U 51,51,12
U 52,52,12
U 53,53,12
Mvaa 188,156,124
W 0
U 54,54,12
U 55,55,12
K 48,48,49
K 49,50,51
K 50,52,53
K 51,54,55
Mvav 96,128,212
buffer_store_dwordx4 v[48:51], v1, s[8:11], s93 offen offset:2048 sc1
U 56,56,12
U 57,57,12
U 58,58,12
U 59,59,12
U 60,60,12
Mvav 128,128,220
U 61,61,12
U 62,62,12
U 63,63,12
K 56,56,57
K 57,58,59
K 58,60,61
Mvav 132,132,220
Ra 144,242,20480
Ra 148,242,21504
Ra 152,242,22528
Ra 156,242,23552
K 59,62,63
buffer_store_dwordx4 v[56:59], v1, s[8:11], s93 offen offset:3072 sc1
Mvav 100,132,212
G 14,168
G 6,169
A 14,14,6
v_cmp_nlt_f32_e64 s[86:87], v14, s90
v_rcp_f32 v14, v14
s_or_b64 s[88:89], s[88:89], s[86:87]
Mvav 104,136,212
D 16,40,94,0
v_accvgpr_write_b32 a168, 0
v_accvgpr_write_b32 a169, 0
v_accvgpr_write_b32 a170, 0
v_accvgpr_write_b32 a171, 0
G 15,172
Mvav 136,136,220
G 7,173
A 15,15,7
v_cndmask_b32_e64 v168, v241, v168, s[72:73]
v_cndmask_b32_e64 v169, v241, v169, s[74:75]
v_cndmask_b32_e64 v170, v241, v170, s[76:77]
v_cndmask_b32_e64 v171, v241, v171, s[78:79]
Mvav 140,140,220
D 17,40,94,1024
v_cmp_nlt_f32_e64 s[86:87], v15, s90
v_rcp_f32 v15, v15
s_or_b64 s[88:89], s[88:89], s[86:87]
v_accvgpr_write_b32 a172, 0
v_accvgpr_write_b32 a173, 0
Mvav 108,140,212
W 0
v_accvgpr_write_b32 a174, 0
v_accvgpr_write_b32 a175, 0
U 64,64,13
U 65,65,13
X 168
Mvav 112,144,212
D 18,40,94,2048
U 66,66,13
U 67,67,13
U 68,68,13
U 69,69,13
U 70,70,13
Mvav 144,144,220
Ra 128,242,49152
U 71,71,13
K 64,64,65
K 65,66,67
K 66,68,69
K 67,70,71
s_add_u32 s93, s67, 131072
Mvav 148,148,220
buffer_store_dwordx4 v[64:67], v1, s[8:11], s93 offen offset:0 sc1
U 72,72,13
U 73,73,13
U 74,74,13
U 75,75,13
U 76,76,13
Mvav 116,148,212
U 77,77,13
U 78,78,13
U 79,79,13
K 72,72,73
K 73,74,75
K 74,76,77
Mvav 120,152,212
Ra 132,242,50176
Ra 136,242,51200
Ra 140,242,52224
D 19,40,94,3072
s_add_u32 m0, s95, 4096
Ra 144,242,53248
K 75,78,79
Mvav 152,152,220
Ra 148,242,54272
X 169
X 170
K 208,168,169
Mvav 156,156,220
X 171
X 172
K 209,170,171
X 173
X 174
K 216,172,173
X 175
v_mov_b32 v194, 0
K 217,174,175
v_mov_b32 v195, 0
v_mov_b32 v202, 0
v_mov_b32 v203, 0
v_mov_b32 v210, 0
v_mov_b32 v211, 0
Mvav 124,156,212
v_cndmask_b32_e64 v188, v241, v188, s[72:73]
v_cndmask_b32_e64 v189, v241, v189, s[74:75]
v_cndmask_b32_e64 v190, v241, v190, s[76:77]
v_cndmask_b32_e64 v191, v241, v191, s[78:79]
X 188
X 189
X 190
K 218,188,189
X 191
s_nop 0
K 219,190,191
Maav 168,180,208
buffer_store_dwordx4 v[72:75], v1, s[8:11], s93 offen offset:1024 sc1
U 80,80,13
U 81,81,13
U 82,82,13
U 83,83,13
U 84,84,13
Maav 172,180,216
W 2
U 85,85,13
U 86,86,13
U 87,87,13
K 80,80,81
K 81,82,83
K 82,84,85
Zvav 32,128,192
buffer_load_dwordx4 v20, s[40:43], s94 offen lds
s_lshl_b32 s94, s58, 2
s_add_u32 s94, s94, s51
s_add_u32 s94, s94, 5
s_and_b32 s94, s94, 15
s_mul_i32 s94, s94, 10240
K 83,86,87
Zvav 64,128,200
buffer_store_dwordx4 v[80:83], v1, s[8:11], s93 offen offset:2048 sc1
U 88,88,13
U 89,89,13
U 90,90,13
U 91,91,13
U 92,92,13
U 93,93,13
U 94,94,13
U 95,95,13
K 88,88,89
K 89,90,91
K 90,92,93
K 91,94,95
buffer_store_dwordx4 v[88:91], v1, s[8:11], s93 offen offset:3072 sc1
U 96,96,14
U 97,97,14
U 98,98,14
U 99,99,14
U 100,100,14
U 101,101,14
U 102,102,14
U 103,103,14
K 96,96,97
K 97,98,99
K 98,100,101
K 99,102,103
s_add_u32 s93, s67, 262144
buffer_store_dwordx4 v[96:99], v1, s[8:11], s93 offen offset:0 sc1
s_nop 1
Zvav 96,128,208
U 104,104,14
U 105,105,14
U 106,106,14
U 107,107,14
U 108,108,14
U 109,109,14
U 110,110,14
U 111,111,14
K 104,104,105
K 105,106,107
K 106,108,109
K 107,110,111
buffer_store_dwordx4 v[104:107], v1, s[8:11], s93 offen offset:1024 sc1
U 112,112,14
U 113,113,14
U 114,114,14
U 115,115,14
U 116,116,14
U 117,117,14
U 118,118,14
U 119,119,14
K 112,112,113
K 113,114,115
K 114,116,117
K 115,118,119
buffer_store_dwordx4 v[112:115], v1, s[8:11], s93 offen offset:2048 sc1
U 120,120,14
U 121,121,14
U 122,122,14
U 123,123,14
U 124,124,14
U 125,125,14
U 126,126,14
U 127,127,14
K 120,120,121
K 121,122,123
K 122,124,125
K 123,126,127
buffer_store_dwordx4 v[120:123], v1, s[8:11], s93 offen offset:3072 sc1
U 128,128,15
U 129,129,15
U 130,130,15
U 131,131,15
U 132,132,15
U 133,133,15
U 134,134,15
U 135,135,15
K 128,128,129
K 129,130,131
K 130,132,133
K 131,134,135
s_add_u32 s93, s67, 393216
buffer_store_dwordx4 v[128:131], v1, s[8:11], s93 offen offset:0 sc1
s_nop 1
Zvav 128,128,216
s_add_u32 s94, s94, s48
s_lshl_b32 s94, s94, 2
s_mul_i32 s95, s51, 10240
s_add_u32 s95, s95, s48
U 136,136,15
U 137,137,15
U 138,138,15
Zvav 132,132,216
s_add_u32 m0, s95, 20480
U 139,139,15
U 140,140,15
U 141,141,15
U 142,142,15
U 143,143,15
K 136,136,137
Zvav 100,132,208
K 137,138,139
K 138,140,141
K 139,142,143
buffer_store_dwordx4 v[136:139], v1, s[8:11], s93 offen offset:1024 sc1
U 144,144,15
U 145,145,15
Zvav 68,132,200
U 146,146,15
U 147,147,15
U 148,148,15
U 149,149,15
U 150,150,15
U 151,151,15
Zvav 36,132,192
D 16,40,94,0
Ra 152,242,55296
K 144,144,145
K 145,146,147
K 146,148,149
K 147,150,151
Zvav 40,136,192
Ra 156,242,56320
Zvav 72,136,200
buffer_store_dwordx4 v[144:147], v1, s[8:11], s93 offen offset:2048 sc1
U 152,152,15
U 153,153,15
U 154,154,15
U 155,155,15
U 156,156,15
Zvav 104,136,208
U 157,157,15
U 158,158,15
U 159,159,15
K 152,152,153
K 153,154,155
K 154,156,157
Zvav 136,136,216
D 17,40,94,1024
K 155,158,159
Zvav 140,140,216
Zvav 108,140,208
D 18,40,94,2048
Zvav 76,140,200
Zvav 44,140,192
W 0
D 19,40,94,3072
s_add_u32 m0, s95, 24576
Zvav 48,144,192
Zvav 80,144,200
buffer_store_dwordx4 v[152:155], v1, s[8:11], s93 offen offset:3072 sc1
s_mov_b32 s18, 40960
s_mov_b32 s19, 73728
s_bitcmp1_b32 s14, 0
s_cselect_b32 s18, 49152, s18
s_cselect_b32 s19, 98304, s19
s_cmp_lt_u32 s14, 2
s_cselect_b32 s20, s18, s19
s_cselect_b32 s21, s19, s18
v_add_u32 v2, s20, v1
)"
R"(v_add_u32 v3, s21, v1
Zvav 112,144,208
s_lshl_b32 s18, s14, 9
v_lshrrev_b32 v232, 1, v1
v_add_u32 v232, s18, v232
v_bfe_u32 v12, v1, 6, 2
v_lshrrev_b32 v13, 8, v1
v_bfe_u32 v14, v1, 4, 1
v_lshlrev_b32 v14, 4, v14
Zvav 144,144,216
v_mov_b32 v15, 0x3f80
v_lshlrev_b32 v15, v14, v15
v_bfe_u32 v14, v1, 5, 1
v_xor_b32 v27, v12, v13
v_lshl_or_b32 v27, v27, 1, v14
v_cmp_eq_u32_e64 s[86:87], 0, v27
Zvav 148,148,216
buffer_load_dwordx4 v20, s[40:43], s94 offen lds
s_waitcnt vmcnt(26)
v_cmp_eq_u64 vcc, s[12:13], v[28:29]
s_cmp_eq_u64 vcc, -1
s_cselect_b32 s46, 79691776, 0
s_add_u32 s81, s58, 0
s_and_b32 s81, s81, 3
s_lshl_b32 s81, s81, 12
s_add_u32 s81, s81, s49
v_mov_b32 v226, 0
Zvav 116,148,208
s_add_u32 s82, s81, 131072
v_cndmask_b32_e64 v224, 0, v15, s[86:87]
v_mov_b32 v228, 0
v_mov_b32 v230, v224
v_cmp_eq_u32_e64 s[86:87], 1, v27
v_mov_b32 v227, 0
s_nop 0
v_cndmask_b32_e64 v225, 0, v15, s[86:87]
Zvav 84,148,200
v_mov_b32 v229, 0
v_mov_b32 v231, v225
G 4,160
G 8,161
G 5,164
G 9,165
Zvav 52,148,192
buffer_load_dwordx4 a[184:187], v1, s[44:47], s81 offen offset:0 sc1
G 6,168
G 10,169
G 7,172
G 11,173
A 4,4,8
Zvav 56,152,192
A 5,5,9
A 6,6,10
A 7,7,11
ds_write_b64 v232, v[6:7] offset:57344
A 8,4,5
A 9,6,7
Zvav 88,152,200
buffer_load_dwordx4 a[200:203], v1, s[44:47], s82 offen offset:0 sc1
A 8,8,9
v_cmp_ngt_f32_e64 s[86:87], s90, v8
s_lshl_b32 s19, s14, 2
v_mov_b32 v9, s19
s_or_b64 s[88:89], s[88:89], s[86:87]
s_and_b64 s[88:89], s[88:89], s[24:25]
s_cselect_b32 s18, 1, 0
Zvav 120,152,208
v_mov_b32 v8, s18
ds_write_b32 v9, v8 offset:59392
v_xor_b32 v10, 1024, v232
v_bfe_u32 v11, v1, 2, 4
K 96,96,97
K 97,98,99
Zvav 152,152,216
buffer_load_dwordx4 a[188:191], v1, s[44:47], s81 offen offset:1024 sc1
K 98,100,101
K 99,102,103
ds_write_b128 v2, v[96:99] offset:0
K 128,128,129
K 129,130,131
Zvav 156,156,216
K 130,132,133
K 131,134,135
ds_write_b128 v2, v[128:131] offset:4096
K 104,104,105
K 105,106,107
K 106,108,109
Zvav 124,156,208
buffer_load_dwordx4 a[204:207], v1, s[44:47], s82 offen offset:1024 sc1
K 107,110,111
ds_write_b128 v2, v[104:107] offset:1024
K 136,136,137
K 137,138,139
K 138,140,141
Zvav 92,156,200
K 139,142,143
ds_write_b128 v2, v[136:139] offset:5120
K 112,112,113
K 113,114,115
K 114,116,117
K 115,118,119
Zvav 60,156,192
buffer_load_dwordx4 a[192:195], v1, s[44:47], s81 offen offset:2048 sc1
ds_write_b128 v2, v[112:115] offset:2048
K 144,144,145
K 145,146,147
K 146,148,149
K 147,150,151
buffer_load_dwordx4 a[208:211], v1, s[44:47], s82 offen offset:2048 sc1
buffer_load_dwordx4 a[196:199], v1, s[44:47], s81 offen offset:3072 sc1
buffer_load_dwordx4 a[212:215], v1, s[44:47], s82 offen offset:3072 sc1
s_add_u32 s81, s58, 1
s_and_b32 s81, s81, 3
s_lshl_b32 s81, s81, 12
s_add_u32 s81, s81, s49
s_add_u32 s82, s81, 131072
buffer_load_dwordx4 a[216:219], v1, s[44:47], s81 offen offset:0 sc1
buffer_load_dwordx4 a[232:235], v1, s[44:47], s82 offen offset:0 sc1
buffer_load_dwordx4 a[220:223], v1, s[44:47], s81 offen offset:1024 sc1
buffer_load_dwordx4 a[236:239], v1, s[44:47], s82 offen offset:1024 sc1
buffer_load_dwordx4 a[224:227], v1, s[44:47], s81 offen offset:2048 sc1
buffer_load_dwordx4 a[240:243], v1, s[44:47], s82 offen offset:2048 sc1
buffer_load_dwordx4 a[228:231], v1, s[44:47], s81 offen offset:3072 sc1
buffer_load_dwordx4 a[244:247], v1, s[44:47], s82 offen offset:3072 sc1
ds_write_b128 v2, v[144:147] offset:6144
K 120,120,121
K 121,122,123
K 122,124,125
K 123,126,127
ds_write_b128 v2, v[120:123] offset:3072
K 152,152,153
K 153,154,155
K 154,156,157
K 155,158,159
ds_write_b128 v2, v[152:155] offset:7168
s_mov_b32 s70, 1
s_branch .Lend_42%=
.Lpd_31%=:
v_pk_mul_f32 v[32:33], v[32:33], s[22:23]
v_pk_mul_f32 v[34:35], v[34:35], s[22:23]
v_pk_mul_f32 v[36:37], v[36:37], s[22:23]
v_pk_mul_f32 v[38:39], v[38:39], s[22:23]
K 32,32,33
K 33,34,35
K 34,36,37
K 35,38,39
v_pk_mul_f32 v[64:65], v[64:65], s[22:23]
v_pk_mul_f32 v[66:67], v[66:67], s[22:23]
v_pk_mul_f32 v[68:69], v[68:69], s[22:23]
v_pk_mul_f32 v[70:71], v[70:71], s[22:23]
K 64,64,65
K 65,66,67
K 66,68,69
K 67,70,71
v_pk_mul_f32 v[96:97], v[96:97], s[22:23]
v_pk_mul_f32 v[98:99], v[98:99], s[22:23]
v_pk_mul_f32 v[100:101], v[100:101], s[22:23]
v_pk_mul_f32 v[102:103], v[102:103], s[22:23]
K 96,96,97
K 97,98,99
K 98,100,101
K 99,102,103
v_pk_mul_f32 v[128:129], v[128:129], s[22:23]
v_pk_mul_f32 v[130:131], v[130:131], s[22:23]
v_pk_mul_f32 v[132:133], v[132:133], s[22:23]
v_pk_mul_f32 v[134:135], v[134:135], s[22:23]
K 128,128,129
K 129,130,131
K 130,132,133
K 131,134,135
v_pk_mul_f32 v[40:41], v[40:41], s[22:23]
v_pk_mul_f32 v[42:43], v[42:43], s[22:23]
v_pk_mul_f32 v[44:45], v[44:45], s[22:23]
v_pk_mul_f32 v[46:47], v[46:47], s[22:23]
v_pk_mul_f32 v[72:73], v[72:73], s[22:23]
v_pk_mul_f32 v[74:75], v[74:75], s[22:23]
v_pk_mul_f32 v[76:77], v[76:77], s[22:23]
v_pk_mul_f32 v[78:79], v[78:79], s[22:23]
v_pk_mul_f32 v[104:105], v[104:105], s[22:23]
v_pk_mul_f32 v[106:107], v[106:107], s[22:23]
v_pk_mul_f32 v[108:109], v[108:109], s[22:23]
v_pk_mul_f32 v[110:111], v[110:111], s[22:23]
v_pk_mul_f32 v[136:137], v[136:137], s[22:23]
v_pk_mul_f32 v[138:139], v[138:139], s[22:23]
v_pk_mul_f32 v[140:141], v[140:141], s[22:23]
v_pk_mul_f32 v[142:143], v[142:143], s[22:23]
v_pk_mul_f32 v[48:49], v[48:49], s[22:23]
v_pk_mul_f32 v[50:51], v[50:51], s[22:23]
v_pk_mul_f32 v[52:53], v[52:53], s[22:23]
v_pk_mul_f32 v[54:55], v[54:55], s[22:23]
v_pk_mul_f32 v[80:81], v[80:81], s[22:23]
v_pk_mul_f32 v[82:83], v[82:83], s[22:23]
v_pk_mul_f32 v[84:85], v[84:85], s[22:23]
v_pk_mul_f32 v[86:87], v[86:87], s[22:23]
v_pk_mul_f32 v[112:113], v[112:113], s[22:23]
v_pk_mul_f32 v[114:115], v[114:115], s[22:23]
v_pk_mul_f32 v[116:117], v[116:117], s[22:23]
v_pk_mul_f32 v[118:119], v[118:119], s[22:23]
v_pk_mul_f32 v[144:145], v[144:145], s[22:23]
v_pk_mul_f32 v[146:147], v[146:147], s[22:23]
v_pk_mul_f32 v[148:149], v[148:149], s[22:23]
v_pk_mul_f32 v[150:151], v[150:151], s[22:23]
v_pk_mul_f32 v[56:57], v[56:57], s[22:23]
v_pk_mul_f32 v[58:59], v[58:59], s[22:23]
v_pk_mul_f32 v[60:61], v[60:61], s[22:23]
v_pk_mul_f32 v[62:63], v[62:63], s[22:23]
v_pk_mul_f32 v[88:89], v[88:89], s[22:23]
v_pk_mul_f32 v[90:91], v[90:91], s[22:23]
v_pk_mul_f32 v[92:93], v[92:93], s[22:23]
v_pk_mul_f32 v[94:95], v[94:95], s[22:23]
v_pk_mul_f32 v[120:121], v[120:121], s[22:23]
v_pk_mul_f32 v[122:123], v[122:123], s[22:23]
v_pk_mul_f32 v[124:125], v[124:125], s[22:23]
v_pk_mul_f32 v[126:127], v[126:127], s[22:23]
v_pk_mul_f32 v[152:153], v[152:153], s[22:23]
v_pk_mul_f32 v[154:155], v[154:155], s[22:23]
v_pk_mul_f32 v[156:157], v[156:157], s[22:23]
v_pk_mul_f32 v[158:159], v[158:159], s[22:23]
W 0
Zvav 196,128,32
Ra 144,242,4096
Ra 148,242,5120
Ra 152,242,6144
Ra 156,242,7168
Zvav 204,128,64
K 40,40,41
K 41,42,43
K 42,44,45
K 43,46,47
Zvav 212,128,96
K 72,72,73
K 73,74,75
K 74,76,77
K 75,78,79
Zvav 220,128,128
K 104,104,105
K 105,106,107
K 106,108,109
K 107,110,111
K 136,136,137
K 137,138,139
K 138,140,141
K 139,142,143
Mvav 196,132,40
Ra 128,242,8192
K 48,48,49
Mvav 204,132,72
K 49,50,51
K 50,52,53
K 51,54,55
K 80,80,81
Mvav 212,132,104
K 81,82,83
K 82,84,85
K 83,86,87
K 112,112,113
Mvav 220,132,136
K 113,114,115
K 114,116,117
K 115,118,119
K 144,144,145
K 145,146,147
K 146,148,149
K 147,150,151
Mvav 196,136,48
Ra 132,242,9216
K 56,56,57
Mvav 204,136,80
K 57,58,59
K 58,60,61
K 59,62,63
K 88,88,89
Mvav 212,136,112
K 89,90,91
K 90,92,93
K 91,94,95
K 120,120,121
Mvav 220,136,144
K 121,122,123
K 122,124,125
K 123,126,127
K 152,152,153
K 153,154,155
K 154,156,157
K 155,158,159
Mvav 196,140,56
Ra 136,242,10240
v_accvgpr_write_b32 a0, v32
Mvav 204,140,88
v_accvgpr_write_b32 a1, v33
v_accvgpr_write_b32 a2, v34
v_accvgpr_write_b32 a3, v35
Mvav 212,140,120
v_accvgpr_write_b32 a4, v40
v_accvgpr_write_b32 a5, v41
v_accvgpr_write_b32 a6, v42
Mvav 220,140,152
W 3
v_accvgpr_write_b32 a7, v43
v_accvgpr_write_b32 a8, v48
v_accvgpr_write_b32 a9, v49
v_accvgpr_write_b32 a10, v50
Zvav 68,144,64
Ra 140,242,11264
v_accvgpr_write_b32 a11, v51
v_accvgpr_write_b32 a12, v56
v_accvgpr_write_b32 a13, v57
Zvav 100,144,96
v_accvgpr_write_b32 a14, v58
v_accvgpr_write_b32 a15, v59
v_accvgpr_write_b32 a16, v64
v_cndmask_b32_e64 v196, v241, v196, s[72:73]
Zvav 132,144,128
v_cndmask_b32_e64 v197, v241, v197, s[74:75]
v_cndmask_b32_e64 v198, v241, v198, s[76:77]
v_cndmask_b32_e64 v199, v241, v199, s[78:79]
v_max3_f32 v4, v196, v197, v198
Mvav 68,148,72
v_max3_f32 v4, v4, v199, v252
v_max3_f32 v5, v204, v205, v206
v_max3_f32 v5, v5, v207, v253
v_max3_f32 v6, v212, v213, v214
Mvav 100,148,104
v_max3_f32 v6, v6, v215, v254
v_max3_f32 v7, v220, v221, v222
v_max3_f32 v7, v7, v223, v255
v_mov_b32 v8, v4
Mvav 132,148,136
v_mov_b32 v9, v5
v_mov_b32 v10, v6
v_mov_b32 v11, v7
v_accvgpr_write_b32 a17, v65
Mvav 68,152,80
v_permlane32_swap_b32 v4, v8
v_permlane32_swap_b32 v5, v9
v_permlane32_swap_b32 v6, v10
v_permlane32_swap_b32 v7, v11
Mvav 100,152,112
v_max_f32 v4, v4, v8
v_max_f32 v5, v5, v9
v_max_f32 v6, v6, v10
v_max_f32 v7, v7, v11
Mvav 132,152,144
v_mov_b32 v8, v4
v_mov_b32 v9, v5
v_mov_b32 v10, v6
v_mov_b32 v11, v7
Mvav 68,156,88
v_permlane16_swap_b32 v4, v8
v_permlane16_swap_b32 v5, v9
v_permlane16_swap_b32 v6, v10
v_accvgpr_write_b32 a18, v66
Mvav 100,156,120
v_permlane16_swap_b32 v7, v11
v_max_f32 v4, v4, v8
v_max_f32 v5, v5, v9
v_max_f32 v6, v6, v10
v_max_f32 v7, v7, v11
v_sub_f32 v4, 0, v4
v_sub_f32 v5, 0, v5
v_sub_f32 v6, 0, v6
v_sub_f32 v7, 0, v7
v_mov_b32 v224, v4
v_mov_b32 v225, v4
v_mov_b32 v226, v4
v_mov_b32 v227, v4
v_mov_b32 v228, v5
v_mov_b32 v229, v5
v_mov_b32 v230, v5
v_mov_b32 v231, v5
v_mov_b32 v232, v6
v_mov_b32 v233, v6
v_mov_b32 v234, v6
v_mov_b32 v235, v6
v_mov_b32 v236, v7
v_mov_b32 v237, v7
v_mov_b32 v238, v7
v_mov_b32 v239, v7
Mvav 132,156,152
W 0
Ra 144,242,12288
v_accvgpr_write_b32 a19, v67
v_accvgpr_write_b32 a20, v72
v_accvgpr_write_b32 a21, v73
Cvav 168,128,96,232
s_and_b32 s80, s14, 1
v_accvgpr_write_b32 a22, v74
v_accvgpr_write_b32 a23, v75
v_accvgpr_write_b32 a24, v80
v_accvgpr_write_b32 a25, v81
Cvav 172,128,128,236
s_lshl_b32 s80, s80, 14
v_accvgpr_write_b32 a26, v82
v_accvgpr_write_b32 a27, v83
v_accvgpr_write_b32 a28, v88
v_accvgpr_write_b32 a29, v89
Mvav 168,132,104
Ra 148,242,13312
Ra 152,242,14336
Ra 156,242,15360
)"
R"(v_accvgpr_write_b32 a30, v90
Mvav 172,132,136
s_add_u32 s80, s80, 98304
v_accvgpr_write_b32 a31, v91
v_accvgpr_write_b32 a32, v96
v_accvgpr_write_b32 a33, v97
v_accvgpr_write_b32 a34, v98
Mvav 168,136,112
v_accvgpr_write_b32 a35, v99
v_accvgpr_write_b32 a36, v104
v_accvgpr_write_b32 a37, v105
v_accvgpr_write_b32 a38, v106
Mvav 172,136,144
v_accvgpr_write_b32 a39, v107
v_accvgpr_write_b32 a40, v112
v_accvgpr_write_b32 a41, v113
v_accvgpr_write_b32 a42, v114
Mvav 168,140,120
Ra 128,242,24576
Ra 132,242,25600
Ra 136,242,26624
v_accvgpr_write_b32 a43, v115
Mvav 172,140,152
Ra 140,242,27648
v_accvgpr_write_b32 a44, v120
v_accvgpr_write_b32 a45, v121
W 4
Cvav 188,144,128,236
v_accvgpr_write_b32 a46, v122
v_accvgpr_write_b32 a47, v123
v_accvgpr_write_b32 a48, v128
v_accvgpr_write_b32 a49, v129
v_accvgpr_write_b32 a50, v130
v_accvgpr_write_b32 a51, v131
v_accvgpr_write_b32 a52, v136
v_accvgpr_write_b32 a53, v137
v_accvgpr_write_b32 a54, v138
v_accvgpr_write_b32 a55, v139
v_accvgpr_write_b32 a56, v144
v_accvgpr_write_b32 a57, v145
v_accvgpr_write_b32 a58, v146
v_accvgpr_write_b32 a59, v147
v_accvgpr_write_b32 a60, v152
v_accvgpr_write_b32 a61, v153
v_accvgpr_write_b32 a62, v154
v_accvgpr_write_b32 a63, v155
Mvav 188,148,136
s_barrier
v_add_u32 v8, s80, v1
A 196,196,224
A 197,197,224
A 198,198,224
Mvav 188,152,144
A 199,199,224
X 196
X 197
X 198
K 192,196,197
X 199
A 204,204,228
K 193,198,199
A 205,205,228
A 206,206,228
A 207,207,228
X 204
X 205
X 206
K 200,204,205
X 207
A 212,212,232
K 201,206,207
A 213,213,232
A 214,214,232
A 215,215,232
X 212
X 213
X 214
K 208,212,213
X 215
A 220,220,236
K 209,214,215
A 221,221,236
A 222,222,236
A 223,223,236
X 220
X 221
X 222
K 216,220,221
X 223
v_mov_b32 v194, 0
K 217,222,223
v_mov_b32 v195, 0
v_cndmask_b32_e64 v68, v241, v68, s[72:73]
v_cndmask_b32_e64 v69, v241, v69, s[74:75]
v_cndmask_b32_e64 v70, v241, v70, s[76:77]
v_cndmask_b32_e64 v71, v241, v71, s[78:79]
A 68,68,228
A 69,69,228
A 70,70,228
A 71,71,228
X 68
X 69
X 70
K 202,68,69
X 71
A 100,100,232
K 203,70,71
A 101,101,232
A 102,102,232
A 103,103,232
X 100
X 101
X 102
K 210,100,101
X 103
A 132,132,236
K 211,102,103
A 133,133,236
A 134,134,236
A 135,135,236
X 132
X 133
X 134
K 218,132,133
X 135
s_nop 0
K 219,134,135
Mvav 188,156,152
W 0
Ra 144,242,28672
Ra 148,242,29696
Ra 152,242,30720
Zvav 32,128,192
Ra 156,242,31744
Zvav 64,128,200
Zvav 96,128,208
s_and_b32 s80, s14, 1
s_xor_b32 s62, s62, 0x10000
Zvav 128,128,216
s_lshl_b32 s80, s80, 14
s_add_u32 s80, s80, 131072
v_add_u32 v23, 61440, v21
Zvav 132,132,216
v_add_u32 v12, s80, v1
Ra 64,12,8192
Ra 68,12,9216
v_add_u32 v24, 61440, v22
Zvav 100,132,208
Ra 72,12,10240
Ra 76,12,11264
Ra 80,12,12288
v_add_u32 v25, 122880, v21
Zvav 68,132,200
Ra 84,12,13312
Ra 88,12,14336
Ra 92,12,15360
v_add_u32 v26, 122880, v22
Zvav 36,132,192
Ra 96,12,0
Ra 100,12,1024
Ra 104,12,2048
Zvav 40,136,192
Ra 108,12,3072
Ra 112,12,4096
Ra 116,12,5120
Zvav 72,136,200
Ra 120,12,6144
Ra 124,12,7168
Zvav 104,136,208
Zvav 136,136,216
Zvav 140,140,216
Zvav 108,140,208
Zvav 76,140,200
s_waitcnt vmcnt(0) lgkmcnt(0)
s_barrier
v_xor_b32 v242, 0x10000, v242
ds_read_b128 v[248:251], v8
Zvav 44,140,192
s_add_u32 s80, s71, 128
v_mov_b64 v[28:29], s[12:13]
s_mov_b64 exec, 1
buffer_store_dwordx2 v[28:29], off, s[8:11], s80 sc1
s_mov_b64 exec, -1
Ra 128,242,40960
Zvav 48,144,192
Zvav 80,144,200
X 172
X 173
Zvav 112,144,208
Ra 132,242,41984
X 174
K 220,172,173
Zvav 144,144,216
X 175
v_cndmask_b32_e64 v168, v241, v168, s[72:73]
K 221,174,175
Zvav 148,148,216
v_cndmask_b32_e64 v169, v241, v169, s[74:75]
v_cndmask_b32_e64 v170, v241, v170, s[76:77]
v_cndmask_b32_e64 v171, v241, v171, s[78:79]
Zvav 116,148,208
X 168
X 169
Zvav 84,148,200
W 2
Ra 136,242,43008
X 170
K 212,168,169
Zvav 52,148,192
v_mov_b32 v224, v250
v_mov_b32 v225, v250
v_mov_b32 v226, v250
v_mov_b32 v227, v250
Zvav 56,152,192
v_mov_b32 v228, v251
v_mov_b32 v229, v251
v_mov_b32 v230, v251
v_mov_b32 v231, v251
Zvav 88,152,200
v_mov_b32 v232, v248
v_mov_b32 v233, v248
v_mov_b32 v234, v248
v_mov_b32 v235, v248
Zvav 120,152,208
v_mov_b32 v236, v249
v_mov_b32 v237, v249
v_mov_b32 v238, v249
v_mov_b32 v239, v249
Zvav 152,152,216
X 171
v_mov_b32 v204, 0
K 213,170,171
Zvav 156,156,216
Ra 140,242,44032
v_mov_b32 v205, 0
v_mov_b32 v196, 0
v_mov_b32 v197, 0
Zvav 124,156,208
v_mov_b32 v198, 0
v_mov_b32 v199, 0
v_mov_b32 v206, 0
v_mov_b32 v207, 0
Zvav 92,156,200
v_mov_b32 v214, 0
v_mov_b32 v215, 0
v_cndmask_b32_e64 v188, v241, v188, s[72:73]
v_cndmask_b32_e64 v189, v241, v189, s[74:75]
Zvav 60,156,192
v_cndmask_b32_e64 v190, v241, v190, s[76:77]
v_cndmask_b32_e64 v191, v241, v191, s[78:79]
X 188
X 189
X 190
K 222,188,189
X 191
s_nop 0
K 223,190,191
s_nop 1
s_branch .Lsp2_36%=
.Lmoe_43%=:
W 0
Cvaa 160,128,0,224
Ra 144,242,4096
Ra 148,242,5120
Cvaa 164,128,16,228
s_xor_b32 s62, s62, 0x10000
Ra 152,242,6144
v_bfe_u32 v11, v1, 4, 3
Cvaa 168,128,32,232
X 184
Cvaa 172,128,48,236
X 185
Mvaa 160,132,4
Ra 156,242,7168
v_lshlrev_b32 v11, 3, v11
Mvaa 164,132,20
buffer_load_dwordx2 v[8:9], v11, s[8:11], 0 offen offset:3584 sc1
Mvaa 168,132,36
s_cmp_eq_u32 s14, 0
s_cselect_b32 s20, s30, s32
s_cselect_b32 s21, s31, s33
X 186
Mvaa 172,132,52
s_and_b32 s21, s21, 0xffff
s_mov_b32 s22, 2211840
K 214,184,185
Mvaa 160,136,8
s_mov_b32 s23, 0x20000
X 187
Mvaa 164,136,24
X 188
Mvaa 168,136,40
v_lshrrev_b32 v4, 6, v1
v_mul_u32_u24 v4, 3456, v4
Mvaa 172,136,56
Ra 128,242,16384
K 215,186,187
Mvaa 160,140,12
v_lshrrev_b32 v5, 4, v1
v_xor_b32 v5, v5, v1
Mvaa 164,140,28
X 189
Mvaa 168,140,44
X 190
Mvaa 172,140,60
v_and_b32 v5, 32, v5
v_and_b32 v6, 16, v1
v_smfmac_f32_16x16x64_bf16 a[160:163], a[176:179], v[192:199], v240
Ra 132,242,17408
K 222,188,189
v_smfmac_f32_16x16x64_bf16 a[164:167], a[176:179], v[200:207], v240
X 191
s_nop 0
K 223,190,191
v_smfmac_f32_16x16x64_bf16 a[168:171], a[176:179], v[208:215], v240
v_add3_u32 v4, v4, v5, v6
v_add_u32 v5, 54272, v4
v_smfmac_f32_16x16x64_bf16 a[172:175], a[176:179], v[216:223], v240
W 2
v_add_u32 v6, 108544, v4
v_add_u32 v7, 162816, v4
Cvaa 176,144,0,224
s_lshl_b32 s94, s85, 6
s_mul_i32 s95, s14, 20480
Ra 136,242,18432
Cvaa 180,144,16,228
s_add_u32 s95, s95, 98304
X 160
Cvaa 184,144,32,232
s_add_u32 m0, s95, 0
X 161
Cvaa 188,144,48,236
X 162
Mvaa 176,148,4
Ra 140,242,19456
D 4,20,94,0
Mvaa 180,148,20
Ra 144,242,20480
K 192,160,161
Mvaa 184,148,36
D 5,20,94,1024
Mvaa 188,148,52
X 163
Mvaa 176,152,8
D 6,20,94,2048
Ra 148,242,21504
Mvaa 180,152,24
X 164
Mvaa 184,152,40
D 7,20,94,3072
K 193,162,163
Mvaa 188,152,56
s_add_u32 s94, s94, 221184
s_add_u32 m0, s95, 4096
Mvaa 176,156,12
X 165
Mvaa 180,156,28
D 4,20,94,0
Ra 152,242,22528
Mvaa 184,156,44
X 166
Mvaa 188,156,60
W 3
D 5,20,94,1024
K 200,164,165
Mvav 32,128,196
X 167
Mvav 64,128,204
D 6,20,94,2048
Ra 156,242,23552
Mvav 96,128,212
X 168
Mvav 128,128,220
D 7,20,94,3072
K 201,166,167
Mvav 132,132,220
s_add_u32 s94, s94, 221184
s_waitcnt vmcnt(8)
v_xor_b32 v8, s12, v8
v_xor_b32 v9, s13, v9
v_lshrrev_b32 v10, 11, v8
v_or_b32 v10, v10, v9
v_cmp_eq_u32_e64 s[86:87], 0, v10
s_cmp_eq_u64 s[86:87], -1
s_cbranch_scc0 .Lspin_59%=
.Lok_60%=:
v_bfe_u32 v10, v8, 3, 8
Mvav 100,132,212
s_add_u32 s64, s64, 1
v_mul_u32_u24 v10, 10240, v10
v_and_b32 v8, 7, v8
Mvav 68,132,204
X 169
Mvav 36,132,196
v_lshl_add_u32 v10, v8, 4, v10
v_and_b32 v9, 0x380, v1
Mvav 40,136,196
X 170
Mvav 72,136,204
v_add_u32 v10, v10, v9
)"
R"(K 208,168,169
Mvav 104,136,212
s_mul_i32 s82, s14, 5120
X 171
Mvav 136,136,220
s_add_u32 s81, s82, 131072
X 172
Mvav 140,140,220
s_add_u32 m0, s82, 53248
Ra 128,242,8192
Dc 10,8,81,0
Mvav 108,140,212
Ra 132,242,9216
K 209,170,171
Mvav 76,140,204
Dc 10,8,81,1024
Ra 136,242,10240
Mvav 44,140,196
W 3
X 173
Mvav 48,144,196
Dc 10,8,81,2048
Ra 140,242,11264
Mvav 80,144,204
X 174
Mvav 112,144,212
Dc 10,8,81,3072
K 216,172,173
Mvav 144,144,220
s_add_u32 m0, s82, 57344
s_add_u32 s81, s81, 4096
Mvav 148,148,220
X 175
Mvav 116,148,212
buffer_load_dwordx4 v10, s[8:11], s81 offen sc1 lds
Ra 144,242,12288
Mvav 84,148,204
s_add_u32 m0, s95, 8192
X 176
Mvav 52,148,196
D 4,20,94,0
K 217,174,175
Mvav 56,152,196
X 177
Mvav 88,152,204
D 5,20,94,1024
Ra 148,242,13312
Mvav 120,152,212
X 178
Mvav 152,152,220
D 6,20,94,2048
K 194,176,177
Mvav 156,156,220
X 179
Mvav 124,156,212
D 7,20,94,3072
Ra 152,242,14336
Mvav 92,156,204
s_add_u32 s94, s94, 221184
s_add_u32 m0, s95, 12288
Mvav 60,156,196
W 3
D 4,20,94,0
Cvaa 160,128,0,224
X 180
Cvaa 164,128,16,228
D 5,20,94,1024
K 195,178,179
Cvaa 168,128,32,232
X 181
Cvaa 172,128,48,236
D 6,20,94,2048
Ra 156,242,15360
Mvaa 160,132,4
X 182
Mvaa 164,132,20
D 7,20,94,3072
K 202,180,181
Mvaa 168,132,36
s_add_u32 s94, s94, 221184
s_add_u32 m0, s95, 16384
Ra 128,242,24576
Mvaa 172,132,52
D 4,20,94,0
Mvaa 160,136,8
X 183
Mvaa 164,136,24
D 5,20,94,1024
Ra 132,242,25600
Mvaa 168,136,40
X 184
Mvaa 172,136,56
D 6,20,94,2048
K 203,182,183
Mvaa 160,140,12
X 185
Mvaa 164,140,28
D 7,20,94,3072
Ra 136,242,26624
Mvaa 168,140,44
s_add_u32 s94, s94, 221184
X 186
Mvaa 172,140,60
W 3
K 210,184,185
Cvaa 176,144,0,224
X 187
Cvaa 180,144,16,228
X 188
K 211,186,187
Cvaa 184,144,32,232
X 189
X 190
K 218,188,189
X 191
X 160
K 219,190,191
Cvaa 188,144,48,236
X 161
Mvaa 176,148,4
Ra 140,242,27648
Ra 144,242,28672
Mvaa 180,148,20
X 162
Mvaa 184,148,36
K 196,160,161
Mvaa 188,148,52
X 163
Mvaa 176,152,8
X 164
Mvaa 180,152,24
Ra 148,242,29696
K 197,162,163
Mvaa 184,152,40
X 165
Mvaa 188,152,56
X 166
Mvaa 176,156,12
Ra 152,242,30720
K 204,164,165
Mvaa 180,156,28
X 167
Mvaa 184,156,44
X 168
Mvaa 188,156,60
W 3
K 205,166,167
Mvav 32,128,192
X 169
Mvav 64,128,200
Ra 156,242,31744
Mvav 96,128,208
X 170
Mvav 128,128,216
K 212,168,169
Mvav 132,132,216
X 171
Mvav 100,132,208
v_lshrrev_b32 v8, 8, v1
Mvav 68,132,200
X 172
Mvav 36,132,192
v_bfe_u32 v9, v1, 6, 2
K 213,170,171
Mvav 40,136,192
X 173
Mvav 72,136,200
v_lshl_add_u32 v9, v8, 3, v9
v_lshlrev_b32 v11, 6, v9
Mvav 104,136,208
X 174
Mvav 136,136,216
K 220,172,173
v_xor_b32 v9, s14, v8
Mvav 140,140,216
X 175
Mvav 108,140,208
v_and_b32 v9, 1, v9
Mvav 76,140,200
s_waitcnt vmcnt(0) lgkmcnt(0)
s_barrier
v_xor_b32 v242, 0x10000, v242
s_bitcmp1_b32 s68, s64
s_cbranch_scc1 .Lact_62%=
.Lactr_63%=:
Mvav 44,140,192
X 176
Mvav 48,144,192
v_lshl_add_u32 v11, v9, 5, v11
K 221,174,175
Mvav 80,144,200
Ra 128,242,0
v_bfe_u32 v9, v1, 4, 2
Mvav 112,144,208
X 177
Mvav 144,144,216
X 178
Mvav 148,148,216
Ra 132,242,1024
v_lshl_add_u32 v12, v9, 3, v11
Mvav 116,148,208
K 198,176,177
v_add_u32 v11, 98304, v12
Mvav 84,148,200
X 179
Mvav 52,148,192
Ra 136,242,2048
v_and_b32 v13, 0x70, v1
Mvav 56,152,192
X 180
Mvav 88,152,200
v_lshl_add_u32 v13, v8, 7, v13
K 199,178,179
Mvav 120,152,208
X 181
Mvav 152,152,216
Ra 140,242,3072
Mvav 156,156,216
X 182
Mvav 124,156,208
K 206,180,181
Mvav 92,156,200
X 183
Mvav 60,156,192
K 207,182,183
W 0
Cvaa 160,128,0,224
s_cmp_eq_u32 s14, 0
s_cselect_b32 s95, 106496, 0
Ra 144,242,4096
Ra 148,242,5120
Cvaa 164,128,16,228
s_xor_b32 s62, s62, 0x10000
X 184
Cvaa 168,128,32,232
Ta 184,11,0
Ta 186,11,256
Cvaa 172,128,48,236
s_add_u32 s95, s95, 32768
Ta 188,11,20480
Ta 190,11,20736
Mvaa 160,132,4
Ra 192,13,53248
Ta 196,11,2048
Mvaa 164,132,20
s_add_u32 m0, s95, 0
X 185
Mvaa 168,132,36
Ta 198,11,2304
Ta 200,11,22528
Mvaa 172,132,52
W 3
X 186
Zaaa 232,184,192
Ta 202,11,22784
K 214,184,185
Zaaa 236,188,192
Ra 204,13,53760
Ra 152,242,6144
Mvaa 160,136,8
X 187
Mvaa 164,136,24
Ta 208,11,4096
D 4,20,94,0
Mvaa 168,136,40
X 188
Mvaa 172,136,56
Ta 210,11,4352
D 5,20,94,1024
Mvaa 160,140,12
Ra 156,242,7168
K 215,186,187
Mvaa 164,140,28
X 189
Mvaa 168,140,44
Ta 212,11,24576
D 6,20,94,2048
Mvaa 172,140,60
X 190
v_smfmac_f32_16x16x64_bf16 a[160:163], a[176:179], v[192:199], v240
W 5
Ta 214,11,24832
D 7,20,94,3072
Maaa 232,196,204
s_add_u32 s94, s94, 221184
Ra 216,13,54272
K 222,188,189
Maaa 236,200,204
s_add_u32 m0, s95, 4096
X 191
v_smfmac_f32_16x16x64_bf16 a[164:167], a[176:179], v[200:207], v240
X 160
K 223,190,191
v_smfmac_f32_16x16x64_bf16 a[168:171], a[176:179], v[208:215], v240
Ra 128,242,16384
Ta 220,11,6144
v_smfmac_f32_16x16x64_bf16 a[172:175], a[176:179], v[216:223], v240
W 5
Ta 222,11,6400
Ra 132,242,17408
Cvaa 176,144,0,224
X 161
Cvaa 180,144,16,228
D 4,20,94,0
Ta 224,11,26624
Cvaa 184,144,32,232
X 162
Cvaa 188,144,48,236
D 5,20,94,1024
K 192,160,161
Mvaa 176,148,4
Ta 226,11,26880
Ra 228,13,54784
Mvaa 180,148,20
W 7
X 163
Maaa 232,208,216
D 6,20,94,2048
Ra 136,242,18432
Maaa 236,212,216
X 164
Mvaa 184,148,36
D 7,20,94,3072
Ta 184,11,8192
Mvaa 188,148,52
s_add_u32 s94, s94, 221184
Ra 140,242,19456
K 193,162,163
Mvaa 176,152,8
s_add_u32 m0, s95, 8192
X 165
Mvaa 180,152,24
Ta 186,11,8448
Ta 188,11,28672
Mvaa 184,152,40
X 166
Mvaa 188,152,56
Ra 144,242,20480
K 200,164,165
Mvaa 176,156,12
X 167
Mvaa 180,156,28
Ta 190,11,28928
D 4,20,94,0
Mvaa 184,156,44
W 7
X 168
Maaa 232,220,228
s_add_u32 s64, s64, 1
Ra 192,13,55296
D 5,20,94,1024
Maaa 236,224,228
Ta 196,11,10240
K 201,166,167
Mvaa 188,156,60
W 6
X 169
Mvav 32,128,196
D 6,20,94,2048
Ra 148,242,21504
Mvav 64,128,204
X 170
Mvav 96,128,212
Ta 198,11,10496
D 7,20,94,3072
Mvav 128,128,220
s_add_u32 s94, s94, 221184
Ta 200,11,30720
K 208,168,169
Mvav 132,132,220
s_add_u32 m0, s95, 12288
X 171
Mvav 100,132,212
Ra 152,242,22528
Ta 202,11,30976
Mvav 68,132,204
X 172
Mvav 36,132,196
W 6
Ra 204,13,55808
K 209,170,171
Maaa 232,184,192
X 173
Maaa 236,188,192
D 4,20,94,0
Ra 156,242,23552
Mvav 40,136,196
X 174
Mvav 72,136,204
Ta 208,11,12288
D 5,20,94,1024
Mvav 104,136,212
Ta 210,11,12544
K 216,172,173
Mvav 136,136,220
X 175
Mvav 140,140,220
D 6,20,94,2048
Ra 128,242,8192
Mvav 108,140,212
)"
R"(Ta 212,11,32768
Ta 214,11,33024
Mvav 76,140,204
X 176
Mvav 44,140,196
W 5
D 7,20,94,3072
K 217,174,175
Mvav 48,144,196
s_add_u32 s94, s94, 221184
X 177
Maaa 232,196,204
s_add_u32 m0, s95, 16384
Ra 216,13,56320
Ra 132,242,9216
Maaa 236,200,204
X 178
Mvav 80,144,204
Ta 220,11,14336
K 194,176,177
Mvav 112,144,212
X 179
Mvav 144,144,220
D 4,20,94,0
Ta 222,11,14592
Mvav 148,148,220
Ra 136,242,10240
Ta 224,11,34816
Mvav 116,148,212
X 180
Mvav 84,148,204
D 5,20,94,1024
K 195,178,179
Mvav 52,148,196
X 181
Mvav 56,152,196
D 6,20,94,2048
Ta 226,11,35072
Mvav 88,152,204
W 6
X 182
Maaa 232,208,216
D 7,20,94,3072
Ra 228,13,56832
Maaa 236,212,216
s_add_u32 s94, s94, 221184
Ra 140,242,11264
K 202,180,181
Mvav 120,152,212
X 183
Mvav 152,152,220
Ta 184,11,16384
Ra 144,242,12288
Mvav 156,156,220
X 184
Mvav 124,156,212
Ta 186,11,16640
K 203,182,183
Mvav 92,156,204
X 185
Mvav 60,156,196
W 3
Ta 188,11,36864
Ra 148,242,13312
Cvaa 160,128,0,224
X 186
Cvaa 164,128,16,228
Ta 190,11,37120
K 210,184,185
Cvaa 168,128,32,232
Ra 192,13,57344
Ta 196,11,18432
Maaa 232,220,228
X 187
Maaa 236,224,228
X 188
Cvaa 172,128,48,236
Ra 152,242,14336
K 211,186,187
Mvaa 160,132,4
Ta 198,11,18688
Ta 200,11,38912
Mvaa 164,132,20
X 189
Mvaa 168,132,36
X 190
Mvaa 172,132,52
Ra 156,242,15360
K 218,188,189
Mvaa 160,136,8
Ta 202,11,39168
Ra 204,13,57856
Mvaa 164,136,24
X 191
Mvaa 168,136,40
Ra 128,242,24576
Mvaa 172,136,56
W 8
Maaa 232,184,192
Ra 132,242,25600
Maaa 236,188,192
Mvaa 160,140,12
Mvaa 164,140,28
Mvaa 168,140,44
Ra 136,242,26624
Mvaa 172,140,60
W 5
Cvaa 176,144,0,224
Cvaa 180,144,16,228
Cvaa 184,144,32,232
X 160
K 219,190,191
Cvaa 188,144,48,236
X 161
Mvaa 176,148,4
W 3
X 162
Maaa 232,196,204
Ra 140,242,27648
K 196,160,161
Maaa 236,200,204
X 163
Mvaa 180,148,20
X 164
Mvaa 184,148,36
Ra 144,242,28672
K 197,162,163
Mvaa 188,148,52
X 165
Mvaa 176,152,8
X 166
Mvaa 180,152,24
Ra 148,242,29696
K 204,164,165
Mvaa 184,152,40
X 167
Mvaa 188,152,56
X 168
Mvaa 176,156,12
Ra 152,242,30720
K 205,166,167
Mvaa 180,156,28
X 169
Mvaa 184,156,44
X 170
Mvaa 188,156,60
W 3
K 212,168,169
Mvav 32,128,192
X 171
Mvav 64,128,200
Ra 156,242,31744
Mvav 96,128,208
X 172
Mvav 128,128,216
K 213,170,171
Mvav 132,132,216
X 173
Mvav 100,132,208
Mvav 68,132,200
X 174
Mvav 36,132,192
Mvav 40,136,192
K 220,172,173
Mvav 72,136,200
X 175
Mvav 104,136,208
Mvav 136,136,216
X 176
Mvav 140,140,216
Mvav 108,140,208
K 221,174,175
Mvav 76,140,200
s_waitcnt vmcnt(0) lgkmcnt(0)
s_barrier
v_xor_b32 v242, 0x10000, v242
s_bitcmp1_b32 s68, s64
s_cbranch_scc1 .Lact_64%=
.Lactr_65%=:
Mvav 44,140,192
X 177
Mvav 48,144,192
X 178
Mvav 80,144,200
Ra 128,242,0
K 198,176,177
Mvav 112,144,208
X 179
Mvav 144,144,216
Ra 132,242,1024
Mvav 148,148,216
X 180
Mvav 116,148,208
Ra 136,242,2048
Mvav 84,148,200
K 199,178,179
Mvav 52,148,192
X 181
Mvav 56,152,192
Ra 140,242,3072
Mvav 88,152,200
X 182
Mvav 120,152,208
Mvav 152,152,216
K 206,180,181
Mvav 156,156,216
X 183
Mvav 124,156,208
Mvav 92,156,200
K 207,182,183
Mvav 60,156,192
W 0
Cvaa 160,128,0,224
s_mov_b32 s20, s34
Ra 144,242,4096
Ra 148,242,5120
Cvaa 164,128,16,228
s_xor_b32 s62, s62, 0x10000
X 184
Cvaa 168,128,32,232
s_and_b32 s21, s35, 0xffff
Ta 184,11,40960
Ta 186,11,41216
Cvaa 172,128,48,236
Ta 188,12,32768
Ta 190,12,33024
Mvaa 160,132,4
s_mov_b32 s22, 2211840
Ra 192,13,58368
Ta 196,11,43008
Mvaa 164,132,20
s_mov_b32 s23, 0x20000
X 185
Mvaa 168,132,36
Ta 198,11,43264
Ta 200,12,34816
Mvaa 172,132,52
W 3
X 186
Maaa 232,184,192
Ta 202,12,35072
K 214,184,185
Maaa 236,188,192
Ra 204,13,58880
Ra 152,242,6144
Mvaa 160,136,8
X 187
Mvaa 164,136,24
Ta 208,11,45056
v_lshrrev_b32 v4, 8, v1
Mvaa 168,136,40
X 188
Mvaa 172,136,56
Ta 210,11,45312
v_lshlrev_b32 v5, 5, v4
Mvaa 160,140,12
v_xor_b32 v5, v5, v1
K 215,186,187
Mvaa 164,140,28
Ra 156,242,7168
Ta 212,12,36864
Mvaa 168,140,44
X 189
Mvaa 172,140,60
v_and_b32 v5, 0xf0, v5
v_mul_u32_u24 v4, 1280, v4
v_smfmac_f32_16x16x64_bf16 a[160:163], a[176:179], v[192:199], v240
W 5
X 190
Maaa 232,196,204
Ta 214,12,37120
v_add_u32 v4, v4, v5
Maaa 236,200,204
Ra 216,13,59392
K 222,188,189
v_smfmac_f32_16x16x64_bf16 a[164:167], a[176:179], v[200:207], v240
X 191
X 160
K 223,190,191
v_smfmac_f32_16x16x64_bf16 a[168:171], a[176:179], v[208:215], v240
Ra 128,242,16384
v_xor_b32 v5, 128, v4
v_smfmac_f32_16x16x64_bf16 a[172:175], a[176:179], v[216:223], v240
W 4
Ta 220,11,47104
v_add_u32 v6, 8192, v4
Cvaa 176,144,0,224
Ta 222,11,47360
v_add_u32 v7, 12288, v5
Cvaa 180,144,16,228
Ra 132,242,17408
v_add_u32 v5, 4096, v5
Cvaa 184,144,32,232
s_mul_i32 s94, s85, 40960
X 161
Cvaa 188,144,48,236
s_mul_i32 s81, s14, 20480
Ta 224,12,38912
Ta 226,12,39168
Mvaa 176,148,4
s_add_u32 s94, s94, s81
X 162
Mvaa 180,148,20
W 6
s_lshl_b32 s95, s14, 12
K 192,160,161
Maaa 232,208,216
s_add_u32 s95, s95, 98304
X 163
Maaa 236,212,216
s_add_u32 m0, s95, 0
Ra 228,13,59904
Ra 136,242,18432
Mvaa 184,148,36
X 164
Mvaa 188,148,52
Ta 184,11,49152
K 193,162,163
Mvaa 176,152,8
X 165
Mvaa 180,152,24
Ra 140,242,19456
Ta 186,11,49408
Mvaa 184,152,40
D 4,20,94,0
Ta 188,12,40960
Mvaa 188,152,56
X 166
Mvaa 176,156,12
D 5,20,94,1024
K 200,164,165
Mvaa 180,156,28
X 167
Mvaa 184,156,44
W 5
D 6,20,94,2048
Ra 144,242,20480
Maaa 232,220,228
s_add_u32 s64, s64, 1
Ta 190,12,41216
Ra 192,13,60416
Maaa 236,224,228
D 7,20,94,3072
Ta 196,11,51200
Mvaa 188,156,60
W 6
X 168
Mvav 32,128,196
s_add_u32 s94, s94, 256
s_add_u32 m0, s95, 8192
K 201,166,167
Mvav 64,128,204
X 169
Mvav 96,128,212
D 4,20,94,0
Ra 148,242,21504
Mvav 128,128,220
X 170
Mvav 132,132,220
D 5,20,94,1024
Ta 198,11,51456
Mvav 100,132,212
Ta 200,12,43008
K 208,168,169
Mvav 68,132,204
D 6,20,94,2048
Ra 152,242,22528
Mvav 36,132,196
W 5
X 171
Maaa 232,184,192
D 7,20,94,3072
Ta 202,12,43264
Maaa 236,188,192
s_add_u32 s94, s94, 256
X 172
Mvav 40,136,196
s_add_u32 m0, s95, 16384
Ra 204,13,60928
K 209,170,171
Mvav 72,136,204
X 173
Mvav 104,136,212
Ra 156,242,23552
D 4,20,94,0
Mvav 136,136,220
X 174
Mvav 140,140,220
D 5,20,94,1024
Ta 208,11,53248
Mvav 108,140,212
Ta 210,11,53504
K 216,172,173
Mvav 76,140,204
D 6,20,94,2048
Ra 128,242,8192
Mvav 44,140,196
W 3
X 175
Mvav 48,144,196
)"
R"(D 7,20,94,3072
Ta 212,12,45056
Maaa 232,196,204
s_add_u32 s94, s94, 256
X 176
Maaa 236,200,204
s_add_u32 m0, s95, 24576
Ta 214,12,45312
K 217,174,175
Mvav 80,144,204
D 4,20,94,0
Ra 216,13,61440
Mvav 112,144,212
X 177
Mvav 144,144,220
D 5,20,94,1024
Ra 132,242,9216
Mvav 148,148,220
X 178
Mvav 116,148,212
D 6,20,94,2048
Ta 220,11,55296
Mvav 84,148,204
Ta 222,11,55552
K 194,176,177
Mvav 52,148,196
D 7,20,94,3072
Ra 136,242,10240
Mvav 56,152,196
s_add_u32 s94, s94, 256
X 179
Mvav 88,152,204
W 4
X 180
Maaa 232,208,216
s_add_u32 m0, s95, 32768
Ta 224,12,47104
K 195,178,179
Maaa 236,212,216
D 4,20,94,0
Ta 226,12,47360
Mvav 120,152,212
Ra 140,242,11264
Ra 228,13,61952
Mvav 152,152,220
X 181
Mvav 156,156,220
D 5,20,94,1024
Ta 184,11,57344
Mvav 124,156,212
X 182
Mvav 92,156,204
D 6,20,94,2048
K 202,180,181
Mvav 60,156,196
W 2
X 183
Cvaa 160,128,0,224
D 7,20,94,3072
Ra 144,242,12288
Cvaa 164,128,16,228
s_add_u32 s94, s94, 256
X 184
Cvaa 168,128,32,232
W 2
Ta 186,11,57600
K 203,182,183
Maaa 232,220,228
X 185
Maaa 236,224,228
Ta 188,12,49152
Ra 148,242,13312
Cvaa 172,128,48,236
X 186
Mvaa 160,132,4
Ta 190,12,49408
K 210,184,185
Mvaa 164,132,20
Ra 192,13,62464
Ta 196,11,59392
Mvaa 168,132,36
X 187
Mvaa 172,132,52
X 188
Mvaa 160,136,8
Ra 152,242,14336
K 211,186,187
Mvaa 164,136,24
Ta 198,11,59648
Ta 200,12,51200
Mvaa 168,136,40
X 189
Mvaa 172,136,56
W 4
X 190
Maaa 232,184,192
Ra 156,242,15360
K 218,188,189
Maaa 236,188,192
Ta 202,12,51456
Ra 204,13,62976
Mvaa 160,140,12
X 191
Mvaa 164,140,28
Ra 128,242,24576
Ra 132,242,25600
Mvaa 168,140,44
Ra 136,242,26624
Mvaa 172,140,60
W 5
X 160
Cvaa 176,144,0,224
K 219,190,191
Cvaa 180,144,16,228
X 161
Cvaa 184,144,32,232
X 162
Cvaa 188,144,48,236
Ra 140,242,27648
K 196,160,161
Mvaa 176,148,4
W 4
X 163
Maaa 232,196,204
X 164
Maaa 236,200,204
Ra 144,242,28672
K 197,162,163
Mvaa 180,148,20
X 165
Mvaa 184,148,36
X 166
Mvaa 188,148,52
K 204,164,165
G 252,232
Mvaa 176,152,8
X 167
Mvaa 180,152,24
G 253,233
G 254,234
Mvaa 184,152,40
X 168
Mvaa 188,152,56
Ra 148,242,29696
G 255,235
Mvaa 176,156,12
G 8,236
G 9,237
Mvaa 180,156,28
G 10,238
G 14,239
Mvaa 184,156,44
K 252,252,8
K 205,166,167
Mvaa 188,156,60
W 2
K 253,253,9
K 254,254,10
Mvav 32,128,192
K 255,255,14
v_and_b32 v8, 0xffff0000, v252
Mvav 64,128,200
X 169
Mvav 96,128,208
v_and_b32 v9, 0xffff0000, v253
v_and_b32 v10, 0xffff0000, v254
Mvav 128,128,216
v_and_b32 v14, 0xffff0000, v255
v_lshlrev_b32 v252, 16, v252
Mvav 132,132,216
v_lshlrev_b32 v253, 16, v253
v_lshlrev_b32 v254, 16, v254
Mvav 100,132,208
Ra 152,242,30720
v_lshlrev_b32 v255, 16, v255
Mvav 68,132,200
X 170
Mvav 36,132,192
v_mul_f32 v15, 0xbfb8aa3b, v252
v_mul_f32 v2, 0xbfb8aa3b, v253
Mvav 40,136,192
v_mul_f32 v3, 0xbfb8aa3b, v254
v_mul_f32 v27, 0xbfb8aa3b, v255
Mvav 72,136,200
X 15
Mvav 104,136,208
Ra 156,242,31744
K 212,168,169
Mvav 136,136,216
X 2
X 3
X 27
v_add_f32 v15, 1.0, v15
v_add_f32 v2, 1.0, v2
v_add_f32 v3, 1.0, v3
v_add_f32 v27, 1.0, v27
v_rcp_f32 v15, v15
v_rcp_f32 v2, v2
v_rcp_f32 v3, v3
v_rcp_f32 v27, v27
U 252,252,15
U 253,253,2
U 254,254,3
U 255,255,27
K 252,252,252
K 253,253,253
K 254,254,254
K 255,255,255
v_lshlrev_b32 v252, 16, v252
v_lshlrev_b32 v253, 16, v253
v_lshlrev_b32 v254, 16, v254
v_lshlrev_b32 v255, 16, v255
U 252,252,8
U 253,253,9
U 254,254,10
U 255,255,14
K 246,252,253
K 247,254,255
s_lshl_b32 s81, s14, 3
v_add_u32 v2, s81, v1
ds_write_b64 v2, v[246:247] offset:63488
Mvav 140,140,216
X 171
Mvav 108,140,208
X 172
Mvav 76,140,200
s_waitcnt vmcnt(0) lgkmcnt(0)
s_barrier
v_xor_b32 v242, 0x10000, v242
s_bitcmp1_b32 s68, s64
s_cbranch_scc1 .Lact_66%=
.Lactr_67%=:
Mvav 44,140,192
Ra 128,242,0
K 213,170,171
Mvav 48,144,192
X 173
Mvav 80,144,200
X 174
Mvav 112,144,208
Ra 132,242,1024
K 220,172,173
Mvav 144,144,216
X 175
Mvav 148,148,216
X 176
Mvav 116,148,208
Ra 136,242,2048
K 221,174,175
Mvav 84,148,200
X 177
Mvav 52,148,192
X 178
Mvav 56,152,192
Ra 140,242,3072
K 198,176,177
Mvav 88,152,200
X 179
Mvav 120,152,208
X 180
Mvav 152,152,216
K 199,178,179
Mvav 156,156,216
X 181
Mvav 124,156,208
X 182
Mvav 92,156,200
K 206,180,181
Mvav 60,156,192
X 183
s_nop 0
K 207,182,183
W 0
Cvaa 160,128,0,224
Ra 144,242,4096
Ra 148,242,5120
Cvaa 164,128,16,228
s_xor_b32 s62, s62, 0x10000
X 184
Cvaa 168,128,32,232
Ra 216,1,63488
v_lshrrev_b32 v8, 6, v1
Cvaa 172,128,48,236
X 185
Mvaa 160,132,4
Ra 152,242,6144
v_and_b32 v9, 7, v8
Mvaa 164,132,20
s_lshl_b32 s81, s14, 2
v_xor_b32 v9, s81, v9
v_lshlrev_b32 v9, 5, v9
Mvaa 168,132,36
v_lshl_add_u32 v4, v8, 8, v9
v_bfe_u32 v9, v1, 4, 2
Mvaa 172,132,52
X 186
Mvaa 160,136,8
v_lshl_add_u32 v4, v9, 3, v4
v_add_u32 v4, 98304, v4
Mvaa 164,136,24
v_xor_b32 v5, 32, v4
K 214,184,185
Mvaa 168,136,40
v_xor_b32 v6, 64, v4
v_xor_b32 v7, 96, v4
Mvaa 172,136,56
Ta 184,4,0
Ta 186,4,4096
Mvaa 160,140,12
X 187
Mvaa 164,140,28
v_and_b32 v11, 0x70, v1
v_mul_u32_u24 v11, 160, v11
Mvaa 168,140,44
X 188
Mvaa 172,140,60
Ra 156,242,7168
v_lshrrev_b32 v10, 8, v1
v_smfmac_f32_16x16x64_bf16 a[160:163], a[176:179], v[192:199], v240
Ta 188,5,0
K 215,186,187
v_smfmac_f32_16x16x64_bf16 a[164:167], a[176:179], v[200:207], v240
X 189
X 190
K 222,188,189
X 191
s_nop 0
K 223,190,191
v_smfmac_f32_16x16x64_bf16 a[168:171], a[176:179], v[208:215], v240
Ta 190,5,4096
v_lshl_add_u32 v11, v10, 4, v11
v_smfmac_f32_16x16x64_bf16 a[172:175], a[176:179], v[216:223], v240
W 3
v_bfe_u32 v10, v1, 7, 1
v_lshl_or_b32 v10, v10, 31, v11
Zaaa 232,184,216
W 2
s_mul_i32 s94, s85, 20480
Ra 128,242,16384
Cvaa 176,144,0,224
s_lshl_b32 s82, s14, 8
s_add_u32 s94, s94, s82
Ta 192,6,0
Cvaa 180,144,16,228
s_add_u32 s94, s94, 2752512
s_lshl_b32 s71, s85, 4
Ta 194,6,4096
Cvaa 184,144,32,232
s_lshl_b32 s82, s14, 3
X 160
Cvaa 188,144,48,236
W 3
s_add_u32 s71, s71, s82
Ra 132,242,17408
Zaaa 236,188,216
s_add_u32 s71, s71, 2560
Ta 196,7,0
Ta 198,7,4096
Mvaa 176,148,4
X 161
Mvaa 180,148,20
X 162
Mvaa 184,148,36
Ra 136,242,18432
K 192,160,161
Mvaa 188,148,52
W 4
Ta 200,4,8192
Ta 202,4,12288
Zaaa 240,192,216
X 163
Mvaa 176,152,8
buffer_store_dwordx4 a[232:235], v10, s[8:11], s94 offen offset:0 sc1
Ra 140,242,19456
Mvaa 180,152,24
X 164
Mvaa 184,152,40
Ta 204,5,8192
K 193,162,163
Mvaa 188,152,56
W 5
X 165
Zaaa 244,196,216
Ta 206,5,12288
buffer_store_dwordx4 a[236:239], v10, s[8:11], s94 offen offset:64 sc1
Mvaa 176,156,12
X 166
Mvaa 180,156,28
Ra 144,242,20480
K 200,164,165
Mvaa 184,156,44
X 167
Mvaa 188,156,60
W 4
Ta 208,6,8192
Ta 210,6,12288
Zaaa 248,200,216
W 5
X 168
Mvav 32,128,196
)"
R"(s_add_u32 s64, s64, 1
buffer_store_dwordx4 a[240:243], v10, s[8:11], s94 offen offset:128 sc1
K 201,166,167
Mvav 64,128,204
Ra 148,242,21504
Ta 212,7,8192
Mvav 96,128,212
X 169
Mvav 128,128,220
W 5
Ta 214,7,12288
buffer_store_dwordx4 a[244:247], v10, s[8:11], s94 offen offset:192 sc1
Zaaa 252,204,216
X 170
Mvav 132,132,220
Ra 152,242,22528
K 208,168,169
Mvav 100,132,212
X 171
Mvav 68,132,204
Ta 184,4,16384
Ta 186,4,20480
Mvav 36,132,196
W 6
X 172
Zaaa 232,208,216
buffer_store_dwordx4 a[248:251], v10, s[8:11], s94 offen offset:512 sc1
K 209,170,171
Mvav 40,136,196
Ra 156,242,23552
Ta 188,5,16384
Mvav 72,136,204
X 173
Mvav 104,136,212
X 174
Mvav 136,136,220
W 5
Ta 190,5,20480
buffer_store_dwordx4 a[252:255], v10, s[8:11], s94 offen offset:576 sc1
Zaaa 236,212,216
Ra 128,242,8192
K 216,172,173
Mvav 140,140,220
X 175
Mvav 108,140,212
Ta 192,6,16384
Ta 194,6,20480
Mvav 76,140,204
X 176
Mvav 44,140,196
W 6
buffer_store_dwordx4 a[232:235], v10, s[8:11], s94 offen offset:640 sc1
K 217,174,175
Zaaa 240,184,216
W 5
X 177
Mvav 48,144,196
Ra 132,242,9216
Ta 196,7,16384
Mvav 80,144,204
X 178
Mvav 112,144,212
Ta 198,7,20480
buffer_store_dwordx4 a[236:239], v10, s[8:11], s94 offen offset:704 sc1
Mvav 144,144,220
W 6
Ra 136,242,10240
K 194,176,177
Zaaa 244,188,216
X 179
Mvav 148,148,220
Ta 200,4,24576
Ta 202,4,28672
Mvav 116,148,212
X 180
Mvav 84,148,204
buffer_store_dwordx4 a[240:243], v10, s[8:11], s94 offen offset:1024 sc1
K 195,178,179
Mvav 52,148,196
W 6
X 181
Zaaa 248,192,216
Ra 140,242,11264
Ta 204,5,24576
Mvav 56,152,196
X 182
Mvav 88,152,204
Ta 206,5,28672
K 202,180,181
Mvav 120,152,212
buffer_store_dwordx4 a[244:247], v10, s[8:11], s94 offen offset:1088 sc1
Ra 144,242,12288
Mvav 152,152,220
W 7
X 183
Zaaa 252,196,216
X 184
Mvav 156,156,220
Ta 208,6,24576
Ta 210,6,28672
Mvav 124,156,212
buffer_store_dwordx4 a[248:251], v10, s[8:11], s94 offen offset:1152 sc1
K 203,182,183
Mvav 92,156,204
X 185
Mvav 60,156,196
W 6
Ra 148,242,13312
Ta 212,7,24576
Zaaa 232,200,216
W 7
X 186
Cvaa 160,128,0,224
Ta 214,7,28672
K 210,184,185
Cvaa 164,128,16,228
buffer_store_dwordx4 a[252:255], v10, s[8:11], s94 offen offset:1216 sc1
Cvaa 168,128,32,232
X 187
Cvaa 172,128,48,236
W 6
X 188
Zaaa 236,204,216
Ra 152,242,14336
Ta 184,4,32768
Mvaa 160,132,4
Ta 186,4,36864
K 211,186,187
Mvaa 164,132,20
X 189
Mvaa 168,132,36
buffer_store_dwordx4 a[232:235], v10, s[8:11], s94 offen offset:1536 sc1
Ra 156,242,15360
Mvaa 172,132,52
W 7
X 190
Zaaa 240,208,216
Ta 188,5,32768
K 218,188,189
Mvaa 160,136,8
Ta 190,5,36864
buffer_store_dwordx4 a[236:239], v10, s[8:11], s94 offen offset:1600 sc1
Mvaa 164,136,24
X 191
Mvaa 168,136,40
Ra 128,242,24576
Mvaa 172,136,56
W 7
Ta 192,6,32768
Zaaa 244,212,216
Ta 194,6,36864
Mvaa 160,140,12
Ra 132,242,25600
buffer_store_dwordx4 a[240:243], v10, s[8:11], s94 offen offset:1664 sc1
Mvaa 164,140,28
Mvaa 168,140,44
Mvaa 172,140,60
W 7
Ta 196,7,32768
Zaaa 248,184,216
W 7
X 160
Cvaa 176,144,0,224
Ra 136,242,26624
K 219,190,191
Cvaa 180,144,16,228
X 161
Cvaa 184,144,32,232
X 162
Cvaa 188,144,48,236
W 6
Ta 198,7,36864
K 196,160,161
Zaaa 252,188,216
X 163
Mvaa 176,148,4
buffer_store_dwordx4 a[244:247], v10, s[8:11], s94 offen offset:1728 sc1
Ra 140,242,27648
Mvaa 180,148,20
X 164
Mvaa 184,148,36
buffer_store_dwordx4 a[248:251], v10, s[8:11], s94 offen offset:2048 sc1
K 197,162,163
Mvaa 188,148,52
W 5
X 165
Zaaa 232,192,216
X 166
Mvaa 176,152,8
Ra 144,242,28672
K 204,164,165
Mvaa 180,152,24
X 167
Mvaa 184,152,40
buffer_store_dwordx4 a[252:255], v10, s[8:11], s94 offen offset:2112 sc1
Ra 148,242,29696
Mvaa 188,152,56
W 3
X 168
Zaaa 236,196,216
K 205,166,167
Mvaa 176,156,12
X 169
Mvaa 180,156,28
Ra 152,242,30720
buffer_store_dwordx4 a[232:235], v10, s[8:11], s94 offen offset:2176 sc1
Mvaa 184,156,44
X 170
Mvaa 188,156,60
W 3
K 212,168,169
Mvav 32,128,192
X 171
Mvav 64,128,200
Ra 156,242,31744
buffer_store_dwordx4 a[236:239], v10, s[8:11], s94 offen offset:2240 sc1
Mvav 96,128,208
X 172
Mvav 128,128,216
K 213,170,171
Mvav 132,132,216
X 173
Mvav 100,132,208
Mvav 68,132,200
X 174
Mvav 36,132,192
Mvav 40,136,192
K 220,172,173
Mvav 72,136,200
X 175
Mvav 104,136,208
Mvav 136,136,216
X 176
Mvav 140,140,216
Mvav 108,140,208
K 221,174,175
Mvav 76,140,200
W 0
s_barrier
v_xor_b32 v242, 0x10000, v242
s_bitcmp1_b32 s68, s64
s_cbranch_scc1 .Lact_68%=
.Lactr_69%=:
Mvav 44,140,192
X 177
Mvav 48,144,192
s_add_u32 s37, s64, 1
X 178
Mvav 80,144,200
s_bitset1_b32 s68, s37
Ra 128,242,0
K 198,176,177
Mvav 112,144,208
X 179
Mvav 144,144,216
Ra 132,242,1024
Mvav 148,148,216
X 180
Mvav 116,148,208
Ra 136,242,2048
Mvav 84,148,200
K 199,178,179
Mvav 52,148,192
X 181
Mvav 56,152,192
Ra 140,242,3072
Mvav 88,152,200
X 182
Mvav 120,152,208
Mvav 152,152,216
K 206,180,181
Mvav 156,156,216
X 183
Mvav 124,156,208
Mvav 92,156,200
K 207,182,183
Mvav 60,156,192
s_mov_b32 s85, 99
s_lshl_b32 s65, s57, 1
s_sub_u32 s65, s65, 1
s_branch .Ll0t_45%=
.Lf1r_46%=:
s_lshl_b32 s94, s58, 2
s_add_u32 s94, s94, 0
s_and_b32 s94, s94, 15
s_mul_i32 s94, s94, 10240
s_add_u32 s94, s94, s48
s_lshl_b32 s94, s94, 2
s_mul_i32 s95, s51, 0
s_add_u32 s95, s95, s48
s_add_u32 m0, s95, 133120
s_nop 0
D 16,40,94,0
D 17,40,94,1024
D 18,40,94,2048
D 19,40,94,3072
s_add_u32 m0, s95, 137216
s_nop 0
buffer_load_dwordx4 v20, s[40:43], s94 offen lds
s_lshl_b32 s94, s58, 2
s_add_u32 s94, s94, 1
s_and_b32 s94, s94, 15
s_mul_i32 s94, s94, 10240
s_add_u32 s94, s94, s48
s_lshl_b32 s94, s94, 2
s_mul_i32 s95, s51, 0
s_add_u32 s95, s95, s48
s_add_u32 m0, s95, 143360
s_nop 0
D 16,40,94,0
D 17,40,94,1024
D 18,40,94,2048
D 19,40,94,3072
s_add_u32 m0, s95, 147456
s_nop 0
buffer_load_dwordx4 v20, s[40:43], s94 offen lds
s_lshl_b32 s94, s58, 2
s_add_u32 s94, s94, 2
s_and_b32 s94, s94, 15
s_mul_i32 s94, s94, 10240
s_add_u32 s94, s94, s48
s_lshl_b32 s94, s94, 2
s_mul_i32 s95, s51, 0
s_add_u32 s95, s95, s48
s_add_u32 m0, s95, 153600
s_nop 0
D 16,40,94,0
D 17,40,94,1024
D 18,40,94,2048
D 19,40,94,3072
s_add_u32 m0, s95, 157696
s_nop 0
buffer_load_dwordx4 v20, s[40:43], s94 offen lds
s_branch .Lf1_39%=
.Lspin_14%=:
s_mov_b32 s84, 0
.Llp_16%=:
s_sleep 2
v_lshrrev_b32 v28, 1, v1
s_lshl_b32 s80, s60, 9
s_add_u32 s80, s80, 40960
buffer_load_dwordx2 v[28:29], v28, s[8:11], s80 offen sc1
s_waitcnt vmcnt(0)
s_add_u32 s84, s84, 1
v_cmp_eq_u32 vcc, s13, v29
s_cmp_eq_u64 vcc, -1
s_cbranch_scc1 .Lok_15%=
s_cmp_lt_u32 s84, 1048576
s_cbranch_scc1 .Llp_16%=
s_branch .Lok_15%=
.Lspin_19%=:
s_mov_b32 s84, 0
.Llp_21%=:
s_sleep 2
v_lshrrev_b32 v28, 1, v1
s_lshl_b32 s80, s60, 9
s_add_u32 s80, s80, 40960
buffer_load_dwordx2 v[28:29], v28, s[8:11], s80 offen sc1
s_waitcnt vmcnt(0)
s_add_u32 s84, s84, 1
v_cmp_eq_u32 vcc, s13, v29
s_cmp_eq_u64 vcc, -1
s_cbranch_scc1 .Lok_20%=
s_cmp_lt_u32 s84, 1048576
s_cbranch_scc1 .Llp_21%=
s_branch .Lok_20%=
.Lrt_22%=:
s_waitcnt vmcnt(0)
s_mov_b32 s86, 40960
buffer_load_dwordx4 v[32:35], v1, s[8:11], s86 offen offset:0 sc1
buffer_load_dwordx4 v[36:39], v1, s[8:11], s86 offen offset:1024 sc1
buffer_load_dwordx4 v[40:43], v1, s[8:11], s86 offen offset:2048 sc1
buffer_load_dwordx4 v[44:47], v1, s[8:11], s86 offen offset:3072 sc1
s_mov_b32 s86, 45056
buffer_load_dwordx4 v[48:51], v1, s[8:11], s86 offen offset:0 sc1
buffer_load_dwordx4 v[52:55], v1, s[8:11], s86 offen offset:1024 sc1
buffer_load_dwordx4 v[56:59], v1, s[8:11], s86 offen offset:2048 sc1
buffer_load_dwordx4 v[60:63], v1, s[8:11], s86 offen offset:3072 sc1
s_mov_b32 s86, 49152
buffer_load_dwordx4 v[64:67], v1, s[8:11], s86 offen offset:0 sc1
buffer_load_dwordx4 v[68:71], v1, s[8:11], s86 offen offset:1024 sc1
buffer_load_dwordx4 v[72:75], v1, s[8:11], s86 offen offset:2048 sc1
buffer_load_dwordx4 v[76:79], v1, s[8:11], s86 offen offset:3072 sc1
s_mov_b32 s86, 53248
buffer_load_dwordx4 v[80:83], v1, s[8:11], s86 offen offset:0 sc1
buffer_load_dwordx4 v[84:87], v1, s[8:11], s86 offen offset:1024 sc1
buffer_load_dwordx4 v[88:91], v1, s[8:11], s86 offen offset:2048 sc1
buffer_load_dwordx4 v[92:95], v1, s[8:11], s86 offen offset:3072 sc1
s_branch .Lrtr_23%=
.Lrt_24%=:
v_lshrrev_b32 v136, 1, v1
s_mov_b32 s84, 0
.LL_26%=:
s_waitcnt vmcnt(0)
v_min3_u32 v96, v33, v35, v95
v_max3_u32 v97, v33, v35, v95
v_min3_u32 v96, v96, v37, v39
v_max3_u32 v97, v97, v37, v39
v_min3_u32 v96, v96, v41, v43
v_max3_u32 v97, v97, v41, v43
)"
R"(v_min3_u32 v96, v96, v45, v47
v_max3_u32 v97, v97, v45, v47
v_min3_u32 v96, v96, v49, v51
v_max3_u32 v97, v97, v49, v51
v_min3_u32 v96, v96, v53, v55
v_max3_u32 v97, v97, v53, v55
v_min3_u32 v96, v96, v57, v59
v_max3_u32 v97, v97, v57, v59
v_min3_u32 v96, v96, v61, v63
v_max3_u32 v97, v97, v61, v63
v_min3_u32 v96, v96, v65, v67
v_max3_u32 v97, v97, v65, v67
v_min3_u32 v96, v96, v69, v71
v_max3_u32 v97, v97, v69, v71
v_min3_u32 v96, v96, v73, v75
v_max3_u32 v97, v97, v73, v75
v_min3_u32 v96, v96, v77, v79
v_max3_u32 v97, v97, v77, v79
v_min3_u32 v96, v96, v81, v83
v_max3_u32 v97, v97, v81, v83
v_min3_u32 v96, v96, v85, v87
v_max3_u32 v97, v97, v85, v87
v_min3_u32 v96, v96, v89, v91
v_max3_u32 v97, v97, v89, v91
v_min_u32 v96, v96, v93
v_max_u32 v97, v97, v93
v_cmp_eq_u32 vcc, s13, v96
v_cmp_eq_u32_e64 s[86:87], s13, v97
s_and_b64 vcc, vcc, s[86:87]
s_cmp_eq_u64 vcc, -1
s_cbranch_scc1 .LL_27%=
s_sleep 2
s_mov_b32 s86, 40960
buffer_load_dwordx4 v[32:35], v1, s[8:11], s86 offen offset:0 sc1
buffer_load_dwordx4 v[36:39], v1, s[8:11], s86 offen offset:1024 sc1
buffer_load_dwordx4 v[40:43], v1, s[8:11], s86 offen offset:2048 sc1
buffer_load_dwordx4 v[44:47], v1, s[8:11], s86 offen offset:3072 sc1
s_mov_b32 s86, 45056
buffer_load_dwordx4 v[48:51], v1, s[8:11], s86 offen offset:0 sc1
buffer_load_dwordx4 v[52:55], v1, s[8:11], s86 offen offset:1024 sc1
buffer_load_dwordx4 v[56:59], v1, s[8:11], s86 offen offset:2048 sc1
buffer_load_dwordx4 v[60:63], v1, s[8:11], s86 offen offset:3072 sc1
s_mov_b32 s86, 49152
buffer_load_dwordx4 v[64:67], v1, s[8:11], s86 offen offset:0 sc1
buffer_load_dwordx4 v[68:71], v1, s[8:11], s86 offen offset:1024 sc1
buffer_load_dwordx4 v[72:75], v1, s[8:11], s86 offen offset:2048 sc1
buffer_load_dwordx4 v[76:79], v1, s[8:11], s86 offen offset:3072 sc1
s_mov_b32 s86, 53248
buffer_load_dwordx4 v[80:83], v1, s[8:11], s86 offen offset:0 sc1
buffer_load_dwordx4 v[84:87], v1, s[8:11], s86 offen offset:1024 sc1
buffer_load_dwordx4 v[88:91], v1, s[8:11], s86 offen offset:2048 sc1
buffer_load_dwordx4 v[92:95], v1, s[8:11], s86 offen offset:3072 sc1
s_add_u32 s84, s84, 1
s_cmp_lt_u32 s84, 1048576
s_cbranch_scc1 .LL_26%=
s_waitcnt vmcnt(0)
.LL_27%=:
v_mov_b32 v96, v32
v_mov_b32 v97, 0
v_mov_b32 v98, 0
v_mov_b32 v100, 0
v_med3_u32 v98, v97, v98, v34
v_med3_u32 v97, v96, v97, v34
v_max_u32 v96, v96, v34
v_med3_u32 v98, v97, v98, v36
v_med3_u32 v97, v96, v97, v36
v_max_u32 v96, v96, v36
v_med3_u32 v98, v97, v98, v38
v_med3_u32 v97, v96, v97, v38
v_max_u32 v96, v96, v38
v_med3_u32 v98, v97, v98, v40
v_med3_u32 v97, v96, v97, v40
v_max_u32 v96, v96, v40
v_med3_u32 v98, v97, v98, v42
v_med3_u32 v97, v96, v97, v42
v_max_u32 v96, v96, v42
v_med3_u32 v98, v97, v98, v44
v_med3_u32 v97, v96, v97, v44
v_max_u32 v96, v96, v44
v_med3_u32 v98, v97, v98, v46
v_med3_u32 v97, v96, v97, v46
v_max_u32 v96, v96, v46
v_med3_u32 v98, v97, v98, v48
v_med3_u32 v97, v96, v97, v48
v_max_u32 v96, v96, v48
v_med3_u32 v98, v97, v98, v50
v_med3_u32 v97, v96, v97, v50
v_max_u32 v96, v96, v50
v_med3_u32 v98, v97, v98, v52
v_med3_u32 v97, v96, v97, v52
v_max_u32 v96, v96, v52
v_med3_u32 v98, v97, v98, v54
v_med3_u32 v97, v96, v97, v54
v_max_u32 v96, v96, v54
v_med3_u32 v98, v97, v98, v56
v_med3_u32 v97, v96, v97, v56
v_max_u32 v96, v96, v56
v_med3_u32 v98, v97, v98, v58
v_med3_u32 v97, v96, v97, v58
v_max_u32 v96, v96, v58
v_med3_u32 v98, v97, v98, v60
v_med3_u32 v97, v96, v97, v60
v_max_u32 v96, v96, v60
v_med3_u32 v98, v97, v98, v62
v_med3_u32 v97, v96, v97, v62
v_max_u32 v96, v96, v62
v_med3_u32 v98, v97, v98, v64
v_med3_u32 v97, v96, v97, v64
v_max_u32 v96, v96, v64
v_med3_u32 v98, v97, v98, v66
v_med3_u32 v97, v96, v97, v66
v_max_u32 v96, v96, v66
v_med3_u32 v98, v97, v98, v68
v_med3_u32 v97, v96, v97, v68
v_max_u32 v96, v96, v68
v_med3_u32 v98, v97, v98, v70
v_med3_u32 v97, v96, v97, v70
v_max_u32 v96, v96, v70
v_med3_u32 v98, v97, v98, v72
v_med3_u32 v97, v96, v97, v72
v_max_u32 v96, v96, v72
v_med3_u32 v98, v97, v98, v74
v_med3_u32 v97, v96, v97, v74
v_max_u32 v96, v96, v74
v_med3_u32 v98, v97, v98, v76
v_med3_u32 v97, v96, v97, v76
v_max_u32 v96, v96, v76
v_med3_u32 v98, v97, v98, v78
v_med3_u32 v97, v96, v97, v78
v_max_u32 v96, v96, v78
v_med3_u32 v98, v97, v98, v80
v_med3_u32 v97, v96, v97, v80
v_max_u32 v96, v96, v80
v_med3_u32 v98, v97, v98, v82
v_med3_u32 v97, v96, v97, v82
v_max_u32 v96, v96, v82
v_med3_u32 v98, v97, v98, v84
v_med3_u32 v97, v96, v97, v84
v_max_u32 v96, v96, v84
v_med3_u32 v98, v97, v98, v86
v_med3_u32 v97, v96, v97, v86
v_max_u32 v96, v96, v86
v_med3_u32 v98, v97, v98, v88
v_med3_u32 v97, v96, v97, v88
v_max_u32 v96, v96, v88
v_med3_u32 v98, v97, v98, v90
v_med3_u32 v97, v96, v97, v90
v_max_u32 v96, v96, v90
v_med3_u32 v98, v97, v98, v92
v_med3_u32 v97, v96, v97, v92
v_max_u32 v96, v96, v92
v_med3_u32 v98, v97, v98, v94
v_med3_u32 v97, v96, v97, v94
v_max_u32 v96, v96, v94
v_mov_b32 v99, v96
s_nop 1
v_max_u32_dpp v99, v99, v99 row_shr:1 row_mask:0xf bank_mask:0xf
s_nop 1
v_max_u32_dpp v99, v99, v99 row_shr:2 row_mask:0xf bank_mask:0xf
s_nop 1
v_max_u32_dpp v99, v99, v99 row_shr:4 row_mask:0xf bank_mask:0xf
s_nop 1
v_max_u32_dpp v99, v99, v99 row_shr:8 row_mask:0xf bank_mask:0xf
s_nop 1
v_max_u32_dpp v99, v99, v99 row_bcast:15 row_mask:0xa bank_mask:0xf
s_nop 1
v_max_u32_dpp v99, v99, v99 row_bcast:31 row_mask:0xc bank_mask:0xf
s_nop 0
v_readlane_b32 s86, v99, 63
s_nop 1
v_cmp_eq_u32 vcc, s86, v96
s_and_b32 s87, s86, 0xffff
s_sub_u32 s87, 2047, s87
v_cndmask_b32 v96, v96, v97, vcc
v_cndmask_b32_e64 v97, v97, 0, vcc
v_writelane_b32 v100, s87, 0
v_mov_b32 v99, v96
s_nop 1
v_max_u32_dpp v99, v99, v99 row_shr:1 row_mask:0xf bank_mask:0xf
s_nop 1
v_max_u32_dpp v99, v99, v99 row_shr:2 row_mask:0xf bank_mask:0xf
s_nop 1
v_max_u32_dpp v99, v99, v99 row_shr:4 row_mask:0xf bank_mask:0xf
s_nop 1
v_max_u32_dpp v99, v99, v99 row_shr:8 row_mask:0xf bank_mask:0xf
s_nop 1
v_max_u32_dpp v99, v99, v99 row_bcast:15 row_mask:0xa bank_mask:0xf
s_nop 1
v_max_u32_dpp v99, v99, v99 row_bcast:31 row_mask:0xc bank_mask:0xf
s_nop 0
v_readlane_b32 s86, v99, 63
s_nop 1
v_cmp_eq_u32 vcc, s86, v96
s_and_b32 s87, s86, 0xffff
s_sub_u32 s87, 2047, s87
v_cndmask_b32 v96, v96, v97, vcc
v_cndmask_b32_e64 v97, v97, 0, vcc
v_writelane_b32 v100, s87, 1
v_mov_b32 v99, v96
s_nop 1
v_max_u32_dpp v99, v99, v99 row_shr:1 row_mask:0xf bank_mask:0xf
s_nop 1
v_max_u32_dpp v99, v99, v99 row_shr:2 row_mask:0xf bank_mask:0xf
s_nop 1
v_max_u32_dpp v99, v99, v99 row_shr:4 row_mask:0xf bank_mask:0xf
s_nop 1
v_max_u32_dpp v99, v99, v99 row_shr:8 row_mask:0xf bank_mask:0xf
s_nop 1
v_max_u32_dpp v99, v99, v99 row_bcast:15 row_mask:0xa bank_mask:0xf
s_nop 1
v_max_u32_dpp v99, v99, v99 row_bcast:31 row_mask:0xc bank_mask:0xf
s_nop 0
v_readlane_b32 s86, v99, 63
s_nop 1
v_cmp_eq_u32 vcc, s86, v96
s_and_b32 s87, s86, 0xffff
s_sub_u32 s87, 2047, s87
v_cndmask_b32 v96, v96, v97, vcc
v_cndmask_b32_e64 v97, v97, 0, vcc
v_writelane_b32 v100, s87, 2
v_mov_b32 v99, v96
s_nop 1
v_max_u32_dpp v99, v99, v99 row_shr:1 row_mask:0xf bank_mask:0xf
s_nop 1
v_max_u32_dpp v99, v99, v99 row_shr:2 row_mask:0xf bank_mask:0xf
s_nop 1
v_max_u32_dpp v99, v99, v99 row_shr:4 row_mask:0xf bank_mask:0xf
s_nop 1
v_max_u32_dpp v99, v99, v99 row_shr:8 row_mask:0xf bank_mask:0xf
s_nop 1
v_max_u32_dpp v99, v99, v99 row_bcast:15 row_mask:0xa bank_mask:0xf
s_nop 1
v_max_u32_dpp v99, v99, v99 row_bcast:31 row_mask:0xc bank_mask:0xf
s_nop 0
v_readlane_b32 s86, v99, 63
s_nop 1
v_cmp_eq_u32 vcc, s86, v96
s_and_b32 s87, s86, 0xffff
s_sub_u32 s87, 2047, s87
v_cndmask_b32 v96, v96, v97, vcc
v_cndmask_b32_e64 v97, v97, 0, vcc
v_writelane_b32 v100, s87, 3
v_mov_b32 v99, v96
s_nop 1
v_max_u32_dpp v99, v99, v99 row_shr:1 row_mask:0xf bank_mask:0xf
s_nop 1
v_max_u32_dpp v99, v99, v99 row_shr:2 row_mask:0xf bank_mask:0xf
s_nop 1
v_max_u32_dpp v99, v99, v99 row_shr:4 row_mask:0xf bank_mask:0xf
s_nop 1
v_max_u32_dpp v99, v99, v99 row_shr:8 row_mask:0xf bank_mask:0xf
s_nop 1
v_max_u32_dpp v99, v99, v99 row_bcast:15 row_mask:0xa bank_mask:0xf
s_nop 1
v_max_u32_dpp v99, v99, v99 row_bcast:31 row_mask:0xc bank_mask:0xf
s_nop 0
v_readlane_b32 s86, v99, 63
s_nop 1
v_cmp_eq_u32 vcc, s86, v96
s_and_b32 s87, s86, 0xffff
s_sub_u32 s87, 2047, s87
v_cndmask_b32 v96, v96, v97, vcc
v_cndmask_b32_e64 v97, v97, 0, vcc
v_writelane_b32 v100, s87, 4
v_mov_b32 v99, v96
s_nop 1
v_max_u32_dpp v99, v99, v99 row_shr:1 row_mask:0xf bank_mask:0xf
s_nop 1
v_max_u32_dpp v99, v99, v99 row_shr:2 row_mask:0xf bank_mask:0xf
s_nop 1
v_max_u32_dpp v99, v99, v99 row_shr:4 row_mask:0xf bank_mask:0xf
s_nop 1
v_max_u32_dpp v99, v99, v99 row_shr:8 row_mask:0xf bank_mask:0xf
s_nop 1
v_max_u32_dpp v99, v99, v99 row_bcast:15 row_mask:0xa bank_mask:0xf
s_nop 1
v_max_u32_dpp v99, v99, v99 row_bcast:31 row_mask:0xc bank_mask:0xf
s_nop 0
v_readlane_b32 s86, v99, 63
s_nop 1
v_cmp_eq_u32 vcc, s86, v96
s_and_b32 s87, s86, 0xffff
s_sub_u32 s87, 2047, s87
v_cndmask_b32 v96, v96, v97, vcc
v_cndmask_b32_e64 v97, v97, 0, vcc
v_writelane_b32 v100, s87, 5
v_mov_b32 v99, v96
s_nop 1
v_max_u32_dpp v99, v99, v99 row_shr:1 row_mask:0xf bank_mask:0xf
s_nop 1
v_max_u32_dpp v99, v99, v99 row_shr:2 row_mask:0xf bank_mask:0xf
s_nop 1
v_max_u32_dpp v99, v99, v99 row_shr:4 row_mask:0xf bank_mask:0xf
s_nop 1
v_max_u32_dpp v99, v99, v99 row_shr:8 row_mask:0xf bank_mask:0xf
s_nop 1
v_max_u32_dpp v99, v99, v99 row_bcast:15 row_mask:0xa bank_mask:0xf
s_nop 1
v_max_u32_dpp v99, v99, v99 row_bcast:31 row_mask:0xc bank_mask:0xf
s_nop 0
v_readlane_b32 s86, v99, 63
s_nop 1
v_cmp_eq_u32 vcc, s86, v96
s_and_b32 s87, s86, 0xffff
s_sub_u32 s87, 2047, s87
v_cndmask_b32 v96, v96, v97, vcc
v_cndmask_b32_e64 v97, v97, 0, vcc
v_writelane_b32 v100, s87, 6
v_mov_b32 v99, v96
s_nop 1
v_max_u32_dpp v99, v99, v99 row_shr:1 row_mask:0xf bank_mask:0xf
s_nop 1
v_max_u32_dpp v99, v99, v99 row_shr:2 row_mask:0xf bank_mask:0xf
s_nop 1
v_max_u32_dpp v99, v99, v99 row_shr:4 row_mask:0xf bank_mask:0xf
s_nop 1
v_max_u32_dpp v99, v99, v99 row_shr:8 row_mask:0xf bank_mask:0xf
s_nop 1
v_max_u32_dpp v99, v99, v99 row_bcast:15 row_mask:0xa bank_mask:0xf
s_nop 1
v_max_u32_dpp v99, v99, v99 row_bcast:31 row_mask:0xc bank_mask:0xf
s_nop 0
v_readlane_b32 s86, v99, 63
s_nop 1
v_cmp_eq_u32 vcc, s86, v96
s_and_b32 s87, s86, 0xffff
s_sub_u32 s87, 2047, s87
v_cndmask_b32 v96, v96, v97, vcc
v_cndmask_b32_e64 v97, v97, 0, vcc
v_writelane_b32 v100, s87, 7
v_mov_b32 v99, v98
s_nop 1
v_max_u32_dpp v99, v99, v99 row_shr:1 row_mask:0xf bank_mask:0xf
s_nop 1
v_max_u32_dpp v99, v99, v99 row_shr:2 row_mask:0xf bank_mask:0xf
s_nop 1
v_max_u32_dpp v99, v99, v99 row_shr:4 row_mask:0xf bank_mask:0xf
s_nop 1
v_max_u32_dpp v99, v99, v99 row_shr:8 row_mask:0xf bank_mask:0xf
s_nop 1
v_max_u32_dpp v99, v99, v99 row_bcast:15 row_mask:0xa bank_mask:0xf
s_nop 1
v_max_u32_dpp v99, v99, v99 row_bcast:31 row_mask:0xc bank_mask:0xf
s_nop 0
v_readlane_b32 s87, v99, 63
s_nop 0
s_cmp_gt_u32 s87, s86
s_cbranch_scc0 .LL_28%=
s_mov_b32 m0, 0
.LL_29%=:
v_max3_u32 v96, v32, v34, v36
v_max3_u32 v96, v96, v38, v40
v_max3_u32 v96, v96, v42, v44
v_max3_u32 v96, v96, v46, v48
v_max3_u32 v96, v96, v50, v52
v_max3_u32 v96, v96, v54, v56
v_max3_u32 v96, v96, v58, v60
v_max3_u32 v96, v96, v62, v64
v_max3_u32 v96, v96, v66, v68
v_max3_u32 v96, v96, v70, v72
v_max3_u32 v96, v96, v74, v76
v_max3_u32 v96, v96, v78, v80
v_max3_u32 v96, v96, v82, v84
v_max3_u32 v96, v96, v86, v88
v_max3_u32 v96, v96, v90, v92
v_max_u32 v96, v96, v94
v_mov_b32 v99, v96
s_nop 1
v_max_u32_dpp v99, v99, v99 row_shr:1 row_mask:0xf bank_mask:0xf
s_nop 1
v_max_u32_dpp v99, v99, v99 row_shr:2 row_mask:0xf bank_mask:0xf
s_nop 1
v_max_u32_dpp v99, v99, v99 row_shr:4 row_mask:0xf bank_mask:0xf
s_nop 1
v_max_u32_dpp v99, v99, v99 row_shr:8 row_mask:0xf bank_mask:0xf
s_nop 1
v_max_u32_dpp v99, v99, v99 row_bcast:15 row_mask:0xa bank_mask:0xf
s_nop 1
v_max_u32_dpp v99, v99, v99 row_bcast:31 row_mask:0xc bank_mask:0xf
s_nop 0
v_readlane_b32 s86, v99, 63
s_nop 1
v_cmp_eq_u32 vcc, s86, v32
s_nop 1
v_cndmask_b32_e64 v32, v32, 0, vcc
v_cmp_eq_u32 vcc, s86, v34
s_nop 1
v_cndmask_b32_e64 v34, v34, 0, vcc
v_cmp_eq_u32 vcc, s86, v36
s_nop 1
v_cndmask_b32_e64 v36, v36, 0, vcc
v_cmp_eq_u32 vcc, s86, v38
s_nop 1
v_cndmask_b32_e64 v38, v38, 0, vcc
v_cmp_eq_u32 vcc, s86, v40
s_nop 1
v_cndmask_b32_e64 v40, v40, 0, vcc
v_cmp_eq_u32 vcc, s86, v42
s_nop 1
v_cndmask_b32_e64 v42, v42, 0, vcc
)"
R"(v_cmp_eq_u32 vcc, s86, v44
s_nop 1
v_cndmask_b32_e64 v44, v44, 0, vcc
v_cmp_eq_u32 vcc, s86, v46
s_nop 1
v_cndmask_b32_e64 v46, v46, 0, vcc
v_cmp_eq_u32 vcc, s86, v48
s_nop 1
v_cndmask_b32_e64 v48, v48, 0, vcc
v_cmp_eq_u32 vcc, s86, v50
s_nop 1
v_cndmask_b32_e64 v50, v50, 0, vcc
v_cmp_eq_u32 vcc, s86, v52
s_nop 1
v_cndmask_b32_e64 v52, v52, 0, vcc
v_cmp_eq_u32 vcc, s86, v54
s_nop 1
v_cndmask_b32_e64 v54, v54, 0, vcc
v_cmp_eq_u32 vcc, s86, v56
s_nop 1
v_cndmask_b32_e64 v56, v56, 0, vcc
v_cmp_eq_u32 vcc, s86, v58
s_nop 1
v_cndmask_b32_e64 v58, v58, 0, vcc
v_cmp_eq_u32 vcc, s86, v60
s_nop 1
v_cndmask_b32_e64 v60, v60, 0, vcc
v_cmp_eq_u32 vcc, s86, v62
s_nop 1
v_cndmask_b32_e64 v62, v62, 0, vcc
v_cmp_eq_u32 vcc, s86, v64
s_nop 1
v_cndmask_b32_e64 v64, v64, 0, vcc
v_cmp_eq_u32 vcc, s86, v66
s_nop 1
v_cndmask_b32_e64 v66, v66, 0, vcc
v_cmp_eq_u32 vcc, s86, v68
s_nop 1
v_cndmask_b32_e64 v68, v68, 0, vcc
v_cmp_eq_u32 vcc, s86, v70
s_nop 1
v_cndmask_b32_e64 v70, v70, 0, vcc
v_cmp_eq_u32 vcc, s86, v72
s_nop 1
v_cndmask_b32_e64 v72, v72, 0, vcc
v_cmp_eq_u32 vcc, s86, v74
s_nop 1
v_cndmask_b32_e64 v74, v74, 0, vcc
v_cmp_eq_u32 vcc, s86, v76
s_nop 1
v_cndmask_b32_e64 v76, v76, 0, vcc
v_cmp_eq_u32 vcc, s86, v78
s_nop 1
v_cndmask_b32_e64 v78, v78, 0, vcc
v_cmp_eq_u32 vcc, s86, v80
s_nop 1
v_cndmask_b32_e64 v80, v80, 0, vcc
v_cmp_eq_u32 vcc, s86, v82
s_nop 1
v_cndmask_b32_e64 v82, v82, 0, vcc
v_cmp_eq_u32 vcc, s86, v84
s_nop 1
v_cndmask_b32_e64 v84, v84, 0, vcc
v_cmp_eq_u32 vcc, s86, v86
s_nop 1
v_cndmask_b32_e64 v86, v86, 0, vcc
v_cmp_eq_u32 vcc, s86, v88
s_nop 1
v_cndmask_b32_e64 v88, v88, 0, vcc
v_cmp_eq_u32 vcc, s86, v90
s_nop 1
v_cndmask_b32_e64 v90, v90, 0, vcc
v_cmp_eq_u32 vcc, s86, v92
s_nop 1
v_cndmask_b32_e64 v92, v92, 0, vcc
v_cmp_eq_u32 vcc, s86, v94
s_nop 1
v_cndmask_b32_e64 v94, v94, 0, vcc
s_and_b32 s87, s86, 0xffff
s_sub_u32 s87, 2047, s87
s_nop 0
v_writelane_b32 v100, s87, m0
s_add_u32 m0, m0, 1
s_cmp_lt_u32 m0, 8
s_cbranch_scc1 .LL_29%=
.LL_28%=:
v_xor_b32 v102, s12, v100
v_mov_b32 v103, s13
s_mov_b64 exec, 0xff
buffer_store_dwordx2 v[102:103], v136, s[8:11], 0 offen offset:3584 sc1
s_mov_b64 exec, -1
s_branch .Lrtr_25%=
.Lspin_59%=:
s_mov_b32 s84, 0
.Llp_61%=:
s_sleep 2
buffer_load_dwordx2 v[8:9], v11, s[8:11], 0 offen offset:3584 sc1
s_waitcnt vmcnt(0)
s_add_u32 s84, s84, 1
v_xor_b32 v8, s12, v8
v_xor_b32 v9, s13, v9
v_lshrrev_b32 v10, 11, v8
v_or_b32 v10, v10, v9
v_cmp_eq_u32_e64 s[86:87], 0, v10
s_cmp_eq_u64 s[86:87], -1
s_cbranch_scc1 .Lok_60%=
s_cmp_lt_u32 s84, 1048576
s_cbranch_scc1 .Llp_61%=
s_branch .Lok_60%=
.Lact_48%=:
s_call_b64 s[4:5], .Lhandler%=
s_branch .Lactr_49%=
.Lact_50%=:
s_call_b64 s[4:5], .Lhandler%=
s_branch .Lactr_51%=
.Lact_52%=:
s_call_b64 s[4:5], .Lhandler%=
s_branch .Lactr_53%=
.Lact_54%=:
s_call_b64 s[4:5], .Lhandler%=
s_branch .Lactr_55%=
.Lact_57%=:
s_call_b64 s[4:5], .Lhandler%=
s_branch .Lactr_58%=
.Lact_62%=:
s_call_b64 s[4:5], .Lhandler%=
s_branch .Lactr_63%=
.Lact_64%=:
s_call_b64 s[4:5], .Lhandler%=
s_branch .Lactr_65%=
.Lact_66%=:
s_call_b64 s[4:5], .Lhandler%=
s_branch .Lactr_67%=
.Lact_68%=:
s_call_b64 s[4:5], .Lhandler%=
s_branch .Lactr_69%=
.Lhandler%=:
s_cmp_eq_u32 s64, s70
s_cbranch_scc0 .LL_70%=
s_mov_b32 s70, 0
s_and_b64 s[86:87], s[88:89], s[24:25]
s_cbranch_scc1 .LL_71%=
s_waitcnt vmcnt(0)
v_mov_b64 v[28:29], s[12:13]
s_mov_b64 exec, 1
buffer_store_dwordx2 v[28:29], off, s[8:11], s69 sc1
s_mov_b64 exec, -1
.LL_71%=:
.LL_70%=:
s_cmp_eq_u32 s64, s37
s_cbranch_scc0 .LL_72%=
s_waitcnt vmcnt(0)
v_mov_b64 v[28:29], s[12:13]
s_mov_b64 exec, 1
buffer_store_dwordx2 v[28:29], off, s[8:11], s71 sc1
s_mov_b64 exec, -1
.LL_72%=:
s_setpc_b64 s[4:5]
.Lredo%=:
s_waitcnt vmcnt(0) lgkmcnt(0)
s_barrier
s_and_b32 s81, s14, 1
s_mul_i32 s91, s81, 17164288
s_mul_i32 s82, s56, 536576
s_add_u32 s91, s91, s82
s_add_u32 s91, s91, 4194304
s_lshr_b32 s82, s14, 1
s_lshl_b32 s83, s82, 14
s_add_u32 s91, s91, s83
s_lshl_b32 s81, s81, 14
s_lshl_b32 s82, s82, 16
s_or_b32 s92, s81, s82
s_xor_b32 s92, s92, 0x10000
s_add_u32 m0, s92, 0
s_add_u32 s81, s91, 0
Dc 1,8,81,0
Dc 1,8,81,1024
Dc 1,8,81,2048
Dc 1,8,81,3072
s_add_u32 m0, s92, 4096
s_add_u32 s81, s91, 4096
Dc 1,8,81,0
Dc 1,8,81,1024
Dc 1,8,81,2048
Dc 1,8,81,3072
s_add_u32 m0, s92, 8192
s_add_u32 s81, s91, 8192
Dc 1,8,81,0
Dc 1,8,81,1024
Dc 1,8,81,2048
Dc 1,8,81,3072
s_add_u32 m0, s92, 12288
s_add_u32 s81, s91, 12288
Dc 1,8,81,0
Dc 1,8,81,1024
Dc 1,8,81,2048
Dc 1,8,81,3072
v_bfe_u32 v244, v1, 4, 1
v_mul_u32_u24 v244, 0xaaaa, v244
v_add_u32 v244, 0x4444, v244
v_lshl_or_b32 v240, v244, 16, v244
v_mov_b32 v241, 0xff800000
v_add_u32 v242, 0x10000, v1
v_bfe_u32 v244, v1, 4, 4
v_lshrrev_b32 v245, 8, v1
v_lshlrev_b32 v245, 2, v245
v_sub_u32 v244, v244, v245
v_cmp_le_i32_e64 s[72:73], 0, v244
v_cmp_le_i32_e64 s[74:75], 1, v244
v_cmp_le_i32_e64 s[76:77], 2, v244
v_cmp_le_i32_e64 s[78:79], 3, v244
v_add_u32 v243, 4096, v1
v_add_u32 v244, 8192, v1
v_add_u32 v245, 12288, v1
s_mov_b32 s18, 0x3f803f80
s_mov_b32 s19, 0x3f003f00
v_accvgpr_write_b32 a176, s18
v_accvgpr_write_b32 a180, s19
v_mov_b32 v252, v241
v_accvgpr_write_b32 a177, s18
v_accvgpr_write_b32 a181, s19
v_mov_b32 v253, v241
v_accvgpr_write_b32 a178, s18
v_accvgpr_write_b32 a182, s19
v_mov_b32 v254, v241
v_accvgpr_write_b32 a179, s18
v_accvgpr_write_b32 a183, s19
v_mov_b32 v255, v241
v_accvgpr_write_b32 a160, 0
v_accvgpr_write_b32 a161, 0
v_accvgpr_write_b32 a162, 0
v_accvgpr_write_b32 a163, 0
v_accvgpr_write_b32 a164, 0
v_accvgpr_write_b32 a165, 0
v_accvgpr_write_b32 a166, 0
v_accvgpr_write_b32 a167, 0
v_accvgpr_write_b32 a168, 0
v_accvgpr_write_b32 a169, 0
v_accvgpr_write_b32 a170, 0
v_accvgpr_write_b32 a171, 0
v_accvgpr_write_b32 a172, 0
v_accvgpr_write_b32 a173, 0
v_accvgpr_write_b32 a174, 0
v_accvgpr_write_b32 a175, 0
s_mov_b32 s90, 0x5d800000
s_mov_b64 s[88:89], 0
s_mov_b32 s64, 0
s_and_b32 s18, s14, 1
s_lshl_b32 s62, s18, 14
s_lshl_b32 s20, s57, 15
s_sub_u32 s20, 507904, s20
s_add_u32 s20, s20, s62
s_lshl_b32 s65, s57, 1
s_sub_u32 s65, s65, 1
s_max_i32 s65, s65, 0
s_sub_u32 s66, s60, 1
s_cmp_lt_u32 s14, 2
s_cselect_b32 s20, s20, 16384
s_cselect_b32 s66, s66, 15
s_cselect_b32 s21, 32768, 0
s_add_u32 s61, s91, s20
s_add_u32 s62, s62, s21
s_sub_u32 s63, 16384, s21
s_lshl_b32 s67, s60, 7
s_add_u32 s67, s67, s56
s_lshl_b32 s67, s67, 12
s_add_u32 s67, s67, 39845888
s_lshl_b32 s69, s60, 3
s_add_u32 s69, s69, s59
s_lshl_b32 s69, s69, 6
s_lshl_b32 s21, s58, 3
s_add_u32 s69, s69, s21
s_add_u32 s69, s69, 20480
s_mov_b32 s70, 0
s_mov_b32 s37, 0
v_bfe_u32 v28, v1, 4, 4
v_lshrrev_b32 v29, 8, v1
v_lshlrev_b32 v29, 2, v29
v_sub_u32 v28, v28, v29
s_mov_b64 s[24:25], 0
s_mov_b32 s68, 0
s_mov_b32 s85, 99
s_lshl_b32 s80, s60, 6
s_and_b32 s81, s14, 2
s_lshl_b32 s81, s81, 4
s_xor_b32 s82, s81, 0
s_add_u32 s82, s82, s80
v_add_u32 v4, s82, v28
s_xor_b32 s82, s81, 16
s_add_u32 s82, s82, s80
v_add_u32 v5, s82, v28
s_xor_b32 s82, s81, 32
s_add_u32 s82, s82, s80
v_add_u32 v6, s82, v28
s_xor_b32 s82, s81, 48
s_add_u32 s82, s82, s80
v_add_u32 v7, s82, v28
s_mul_i32 s82, s56, 536576
s_add_u32 s82, s82, 4194304
s_lshl_b32 s84, s60, 1
s_add_u32 s84, s84, 2
.LL_73%=:
buffer_load_dwordx4 a[128:131], v1, s[8:11], s82 offen offset:0 sc1
buffer_load_dwordx4 a[132:135], v1, s[8:11], s82 offen offset:1024 sc1
buffer_load_dwordx4 a[136:139], v1, s[8:11], s82 offen offset:2048 sc1
buffer_load_dwordx4 a[140:143], v1, s[8:11], s82 offen offset:3072 sc1
buffer_load_dwordx4 a[144:147], v243, s[8:11], s82 offen offset:0 sc1
buffer_load_dwordx4 a[148:151], v243, s[8:11], s82 offen offset:1024 sc1
buffer_load_dwordx4 a[152:155], v243, s[8:11], s82 offen offset:2048 sc1
buffer_load_dwordx4 a[156:159], v243, s[8:11], s82 offen offset:3072 sc1
s_waitcnt vmcnt(0)
Zvaa 160,128,0
Zvaa 164,128,16
Zvaa 168,128,32
Zvaa 172,128,48
Mvaa 160,132,4
Mvaa 164,132,20
Mvaa 168,132,36
Mvaa 172,132,52
Mvaa 160,136,8
Mvaa 164,136,24
Mvaa 168,136,40
Mvaa 172,136,56
Mvaa 160,140,12
Mvaa 164,140,28
Mvaa 168,140,44
Mvaa 172,140,60
Zvaa 176,144,0
Zvaa 180,144,16
Zvaa 184,144,32
Zvaa 188,144,48
Mvaa 176,148,4
Mvaa 180,148,20
Mvaa 184,148,36
Mvaa 188,148,52
Mvaa 176,152,8
Mvaa 180,152,24
Mvaa 184,152,40
Mvaa 188,152,56
Mvaa 176,156,12
Mvaa 180,156,28
Mvaa 184,156,44
Mvaa 188,156,60
v_cmp_le_i32 vcc, 0, v4
s_nop 1
v_cndmask_b32 v160, v241, v160, vcc
v_max_f32 v252, v252, v160
v_cmp_le_i32 vcc, 1, v4
s_nop 1
v_cndmask_b32 v161, v241, v161, vcc
v_max_f32 v252, v252, v161
v_cmp_le_i32 vcc, 2, v4
s_nop 1
v_cndmask_b32 v162, v241, v162, vcc
v_max_f32 v252, v252, v162
v_cmp_le_i32 vcc, 3, v4
s_nop 1
v_cndmask_b32 v163, v241, v163, vcc
v_max_f32 v252, v252, v163
v_cmp_le_i32 vcc, 0, v5
s_nop 1
v_cndmask_b32 v164, v241, v164, vcc
v_max_f32 v253, v253, v164
v_cmp_le_i32 vcc, 1, v5
s_nop 1
v_cndmask_b32 v165, v241, v165, vcc
v_max_f32 v253, v253, v165
v_cmp_le_i32 vcc, 2, v5
s_nop 1
v_cndmask_b32 v166, v241, v166, vcc
v_max_f32 v253, v253, v166
v_cmp_le_i32 vcc, 3, v5
s_nop 1
v_cndmask_b32 v167, v241, v167, vcc
v_max_f32 v253, v253, v167
v_cmp_le_i32 vcc, 0, v6
s_nop 1
v_cndmask_b32 v168, v241, v168, vcc
v_max_f32 v254, v254, v168
v_cmp_le_i32 vcc, 1, v6
s_nop 1
v_cndmask_b32 v169, v241, v169, vcc
v_max_f32 v254, v254, v169
v_cmp_le_i32 vcc, 2, v6
s_nop 1
v_cndmask_b32 v170, v241, v170, vcc
v_max_f32 v254, v254, v170
v_cmp_le_i32 vcc, 3, v6
s_nop 1
v_cndmask_b32 v171, v241, v171, vcc
v_max_f32 v254, v254, v171
v_cmp_le_i32 vcc, 0, v7
s_nop 1
v_cndmask_b32 v172, v241, v172, vcc
v_max_f32 v255, v255, v172
v_cmp_le_i32 vcc, 1, v7
s_nop 1
v_cndmask_b32 v173, v241, v173, vcc
v_max_f32 v255, v255, v173
v_cmp_le_i32 vcc, 2, v7
)"
R"(s_nop 1
v_cndmask_b32 v174, v241, v174, vcc
v_max_f32 v255, v255, v174
v_cmp_le_i32 vcc, 3, v7
s_nop 1
v_cndmask_b32 v175, v241, v175, vcc
v_max_f32 v255, v255, v175
v_cmp_le_i32 vcc, 16, v4
s_nop 1
v_cndmask_b32 v176, v241, v176, vcc
v_max_f32 v252, v252, v176
v_cmp_le_i32 vcc, 17, v4
s_nop 1
v_cndmask_b32 v177, v241, v177, vcc
v_max_f32 v252, v252, v177
v_cmp_le_i32 vcc, 18, v4
s_nop 1
v_cndmask_b32 v178, v241, v178, vcc
v_max_f32 v252, v252, v178
v_cmp_le_i32 vcc, 19, v4
s_nop 1
v_cndmask_b32 v179, v241, v179, vcc
v_max_f32 v252, v252, v179
v_cmp_le_i32 vcc, 16, v5
s_nop 1
v_cndmask_b32 v180, v241, v180, vcc
v_max_f32 v253, v253, v180
v_cmp_le_i32 vcc, 17, v5
s_nop 1
v_cndmask_b32 v181, v241, v181, vcc
v_max_f32 v253, v253, v181
v_cmp_le_i32 vcc, 18, v5
s_nop 1
v_cndmask_b32 v182, v241, v182, vcc
v_max_f32 v253, v253, v182
v_cmp_le_i32 vcc, 19, v5
s_nop 1
v_cndmask_b32 v183, v241, v183, vcc
v_max_f32 v253, v253, v183
v_cmp_le_i32 vcc, 16, v6
s_nop 1
v_cndmask_b32 v184, v241, v184, vcc
v_max_f32 v254, v254, v184
v_cmp_le_i32 vcc, 17, v6
s_nop 1
v_cndmask_b32 v185, v241, v185, vcc
v_max_f32 v254, v254, v185
v_cmp_le_i32 vcc, 18, v6
s_nop 1
v_cndmask_b32 v186, v241, v186, vcc
v_max_f32 v254, v254, v186
v_cmp_le_i32 vcc, 19, v6
s_nop 1
v_cndmask_b32 v187, v241, v187, vcc
v_max_f32 v254, v254, v187
v_cmp_le_i32 vcc, 16, v7
s_nop 1
v_cndmask_b32 v188, v241, v188, vcc
v_max_f32 v255, v255, v188
v_cmp_le_i32 vcc, 17, v7
s_nop 1
v_cndmask_b32 v189, v241, v189, vcc
v_max_f32 v255, v255, v189
v_cmp_le_i32 vcc, 18, v7
s_nop 1
v_cndmask_b32 v190, v241, v190, vcc
v_max_f32 v255, v255, v190
v_cmp_le_i32 vcc, 19, v7
s_nop 1
v_cndmask_b32 v191, v241, v191, vcc
v_max_f32 v255, v255, v191
v_subrev_u32 v4, 32, v4
v_subrev_u32 v5, 32, v5
v_subrev_u32 v6, 32, v6
v_subrev_u32 v7, 32, v7
s_add_u32 s82, s82, 8192
s_sub_u32 s84, s84, 1
s_cmp_lg_u32 s84, 0
s_cbranch_scc1 .LL_73%=
G 28,0
v_lshlrev_b32 v32, 16, v28
v_and_b32 v33, 0xffff0000, v28
G 28,1
v_lshlrev_b32 v34, 16, v28
v_and_b32 v35, 0xffff0000, v28
G 28,2
v_lshlrev_b32 v36, 16, v28
v_and_b32 v37, 0xffff0000, v28
G 28,3
v_lshlrev_b32 v38, 16, v28
v_and_b32 v39, 0xffff0000, v28
G 28,4
v_lshlrev_b32 v40, 16, v28
v_and_b32 v41, 0xffff0000, v28
G 28,5
v_lshlrev_b32 v42, 16, v28
v_and_b32 v43, 0xffff0000, v28
G 28,6
v_lshlrev_b32 v44, 16, v28
v_and_b32 v45, 0xffff0000, v28
G 28,7
v_lshlrev_b32 v46, 16, v28
v_and_b32 v47, 0xffff0000, v28
G 28,8
v_lshlrev_b32 v48, 16, v28
v_and_b32 v49, 0xffff0000, v28
G 28,9
v_lshlrev_b32 v50, 16, v28
v_and_b32 v51, 0xffff0000, v28
G 28,10
v_lshlrev_b32 v52, 16, v28
v_and_b32 v53, 0xffff0000, v28
G 28,11
v_lshlrev_b32 v54, 16, v28
v_and_b32 v55, 0xffff0000, v28
G 28,12
v_lshlrev_b32 v56, 16, v28
v_and_b32 v57, 0xffff0000, v28
G 28,13
v_lshlrev_b32 v58, 16, v28
v_and_b32 v59, 0xffff0000, v28
G 28,14
v_lshlrev_b32 v60, 16, v28
v_and_b32 v61, 0xffff0000, v28
G 28,15
v_lshlrev_b32 v62, 16, v28
v_and_b32 v63, 0xffff0000, v28
G 28,16
v_lshlrev_b32 v64, 16, v28
v_and_b32 v65, 0xffff0000, v28
G 28,17
v_lshlrev_b32 v66, 16, v28
v_and_b32 v67, 0xffff0000, v28
G 28,18
v_lshlrev_b32 v68, 16, v28
v_and_b32 v69, 0xffff0000, v28
G 28,19
v_lshlrev_b32 v70, 16, v28
v_and_b32 v71, 0xffff0000, v28
G 28,20
v_lshlrev_b32 v72, 16, v28
v_and_b32 v73, 0xffff0000, v28
G 28,21
v_lshlrev_b32 v74, 16, v28
v_and_b32 v75, 0xffff0000, v28
G 28,22
v_lshlrev_b32 v76, 16, v28
v_and_b32 v77, 0xffff0000, v28
G 28,23
v_lshlrev_b32 v78, 16, v28
v_and_b32 v79, 0xffff0000, v28
G 28,24
v_lshlrev_b32 v80, 16, v28
v_and_b32 v81, 0xffff0000, v28
G 28,25
v_lshlrev_b32 v82, 16, v28
v_and_b32 v83, 0xffff0000, v28
G 28,26
v_lshlrev_b32 v84, 16, v28
v_and_b32 v85, 0xffff0000, v28
G 28,27
v_lshlrev_b32 v86, 16, v28
v_and_b32 v87, 0xffff0000, v28
G 28,28
v_lshlrev_b32 v88, 16, v28
v_and_b32 v89, 0xffff0000, v28
G 28,29
v_lshlrev_b32 v90, 16, v28
v_and_b32 v91, 0xffff0000, v28
G 28,30
v_lshlrev_b32 v92, 16, v28
v_and_b32 v93, 0xffff0000, v28
G 28,31
v_lshlrev_b32 v94, 16, v28
v_and_b32 v95, 0xffff0000, v28
G 28,32
v_lshlrev_b32 v96, 16, v28
v_and_b32 v97, 0xffff0000, v28
G 28,33
v_lshlrev_b32 v98, 16, v28
v_and_b32 v99, 0xffff0000, v28
G 28,34
v_lshlrev_b32 v100, 16, v28
v_and_b32 v101, 0xffff0000, v28
G 28,35
v_lshlrev_b32 v102, 16, v28
v_and_b32 v103, 0xffff0000, v28
G 28,36
v_lshlrev_b32 v104, 16, v28
v_and_b32 v105, 0xffff0000, v28
G 28,37
v_lshlrev_b32 v106, 16, v28
v_and_b32 v107, 0xffff0000, v28
G 28,38
v_lshlrev_b32 v108, 16, v28
v_and_b32 v109, 0xffff0000, v28
G 28,39
v_lshlrev_b32 v110, 16, v28
v_and_b32 v111, 0xffff0000, v28
G 28,40
v_lshlrev_b32 v112, 16, v28
v_and_b32 v113, 0xffff0000, v28
G 28,41
v_lshlrev_b32 v114, 16, v28
v_and_b32 v115, 0xffff0000, v28
G 28,42
v_lshlrev_b32 v116, 16, v28
v_and_b32 v117, 0xffff0000, v28
G 28,43
v_lshlrev_b32 v118, 16, v28
v_and_b32 v119, 0xffff0000, v28
G 28,44
v_lshlrev_b32 v120, 16, v28
v_and_b32 v121, 0xffff0000, v28
G 28,45
v_lshlrev_b32 v122, 16, v28
v_and_b32 v123, 0xffff0000, v28
G 28,46
v_lshlrev_b32 v124, 16, v28
v_and_b32 v125, 0xffff0000, v28
G 28,47
v_lshlrev_b32 v126, 16, v28
v_and_b32 v127, 0xffff0000, v28
G 28,48
v_lshlrev_b32 v128, 16, v28
v_and_b32 v129, 0xffff0000, v28
G 28,49
v_lshlrev_b32 v130, 16, v28
v_and_b32 v131, 0xffff0000, v28
G 28,50
v_lshlrev_b32 v132, 16, v28
v_and_b32 v133, 0xffff0000, v28
G 28,51
v_lshlrev_b32 v134, 16, v28
v_and_b32 v135, 0xffff0000, v28
G 28,52
v_lshlrev_b32 v136, 16, v28
v_and_b32 v137, 0xffff0000, v28
G 28,53
v_lshlrev_b32 v138, 16, v28
v_and_b32 v139, 0xffff0000, v28
G 28,54
v_lshlrev_b32 v140, 16, v28
v_and_b32 v141, 0xffff0000, v28
G 28,55
v_lshlrev_b32 v142, 16, v28
v_and_b32 v143, 0xffff0000, v28
G 28,56
v_lshlrev_b32 v144, 16, v28
v_and_b32 v145, 0xffff0000, v28
G 28,57
v_lshlrev_b32 v146, 16, v28
v_and_b32 v147, 0xffff0000, v28
G 28,58
v_lshlrev_b32 v148, 16, v28
v_and_b32 v149, 0xffff0000, v28
G 28,59
v_lshlrev_b32 v150, 16, v28
v_and_b32 v151, 0xffff0000, v28
G 28,60
v_lshlrev_b32 v152, 16, v28
v_and_b32 v153, 0xffff0000, v28
G 28,61
v_lshlrev_b32 v154, 16, v28
v_and_b32 v155, 0xffff0000, v28
G 28,62
v_lshlrev_b32 v156, 16, v28
v_and_b32 v157, 0xffff0000, v28
G 28,63
v_lshlrev_b32 v158, 16, v28
v_and_b32 v159, 0xffff0000, v28
s_mov_b32 s22, 1.0
s_mov_b32 s23, 1.0
s_mov_b32 s71, 3584
s_barrier
Ra 128,242,0
Ra 132,242,1024
Ra 136,242,2048
Ra 140,242,3072
s_cmp_ge_u32 s14, 2
s_cbranch_scc1 .Lbgo%=
s_branch .Lago%=
.Lpb_30%=:
s_waitcnt vmcnt(4) lgkmcnt(6)
Zvaa 32,0,32
s_add_u32 s28, s28, 512
Ta 20,7,0
Ta 22,7,1024
Zvaa 64,0,36
s_cmp_eq_u32 s28, s4
s_cselect_b32 s93, 10240, 0
Ta 24,8,0
Ta 26,8,1024
Zvaa 96,0,40
s_sub_u32 s28, s28, s93
s_add_u32 s93, s28, 20480
buffer_load_dwordx4 a[128:131], v26, s[8:11], s28 offen offset:0 sc1
Zvaa 128,0,44
Ta 28,9,0
Ta 30,9,1024
Zvaa 132,4,44
K 160,160,161
Zvaa 100,4,40
K 161,162,163
K 162,164,165
Zvaa 68,4,36
buffer_load_dwordx4 a[132:135], v26, s[8:11], s93 offen offset:0 sc1
K 163,166,167
Zvaa 36,4,32
W 8
Ta 0,2,8192
Ta 2,2,9216
Zvaa 40,8,32
K 168,168,169
K 169,170,171
Zvaa 72,8,36
K 170,172,173
K 171,174,175
Zvaa 104,8,40
buffer_load_dwordx4 a[136:139], v27, s[8:11], s28 offen offset:0 sc1
G 164,184
Zvaa 136,8,44
Ta 4,3,8192
G 165,185
Zvaa 140,12,44
Ta 6,3,9216
G 166,186
Zvaa 108,12,40
G 167,187
G 172,188
Zvaa 76,12,36
buffer_load_dwordx4 a[140:143], v27, s[8:11], s93 offen offset:0 sc1
G 173,189
Zvaa 44,12,32
W 8
s_add_u32 s28, s28, 512
s_cmp_eq_u32 s28, s4
s_cselect_b32 s93, 10240, 0
Zvaa 48,16,32
s_sub_u32 s28, s28, s93
s_add_u32 s93, s28, 20480
Ta 8,4,8192
Zvaa 80,16,36
Ta 10,4,9216
G 174,190
Zvaa 112,16,40
buffer_load_dwordx4 a[144:147], v26, s[8:11], s28 offen offset:0 sc1
G 175,191
Zvaa 144,16,44
Ta 12,5,8192
K 164,164,165
Zvaa 148,20,44
Ta 14,5,9216
K 165,166,167
Zvaa 116,20,40
K 166,172,173
K 167,174,175
Zvaa 84,20,36
buffer_load_dwordx4 a[148:151], v26, s[8:11], s93 offen offset:0 sc1
v_accvgpr_write_b32 a184, v164
Zvaa 52,20,32
W 8
Ta 16,6,8192
Ta 18,6,9216
Zvaa 56,24,32
v_accvgpr_write_b32 a185, v165
v_accvgpr_write_b32 a186, v166
Zvaa 88,24,36
v_accvgpr_write_b32 a187, v167
Zvaa 120,24,40
buffer_load_dwordx4 a[152:155], v27, s[8:11], s28 offen offset:0 sc1
Ta 20,7,8192
Zvaa 152,24,44
Ta 22,7,9216
K 184,184,185
Zvaa 156,28,44
K 185,186,187
Zvaa 124,28,40
K 186,188,189
Zvaa 92,28,36
buffer_load_dwordx4 a[156:159], v27, s[8:11], s93 offen offset:0 sc1
K 187,190,191
Zvaa 60,28,32
s_waitcnt vmcnt(8) lgkmcnt(8)
Ta 24,8,8192
Ta 26,8,9216
Mvaa 32,0,48
s_add_u32 s28, s28, 512
s_cmp_eq_u32 s28, s4
s_cselect_b32 s93, 10240, 0
v_accvgpr_write_b32 a188, v184
Mvaa 64,0,52
s_sub_u32 s28, s28, s93
s_add_u32 s93, s28, 20480
v_accvgpr_write_b32 a189, v185
Mvaa 96,0,56
D 16,48,65,0
v_accvgpr_write_b32 a190, v186
Mvaa 128,0,60
Ta 28,9,8192
)"
R"(v_accvgpr_write_b32 a191, v187
Mvaa 132,4,60
Ta 30,9,9216
G 164,192
Mvaa 100,4,56
G 165,193
Mvaa 68,4,52
D 17,48,65,1024
G 166,194
Mvaa 36,4,48
W 8
Ta 0,2,16384
Ta 2,2,17408
Mvaa 40,8,48
G 167,195
G 172,196
Mvaa 72,8,52
G 173,197
Mvaa 104,8,56
D 18,48,65,2048
G 174,198
Mvaa 136,8,60
Ta 4,3,16384
Ta 6,3,17408
Mvaa 140,12,60
G 175,199
K 164,164,165
Mvaa 108,12,56
Mvaa 76,12,52
D 19,48,65,3072
K 165,166,167
Mvaa 44,12,48
W 8
s_add_u32 m0, s63, 36864
Ta 8,4,16384
Mvaa 48,16,48
Ta 10,4,17408
K 166,172,173
Mvaa 80,16,52
K 167,174,175
v_accvgpr_write_b32 a192, v164
Mvaa 112,16,56
buffer_load_dwordx4 a[32:35], v26, s[8:11], s28 offen offset:0 sc1
v_accvgpr_write_b32 a193, v165
Mvaa 144,16,60
Ta 12,5,16384
Ta 14,5,17408
Mvaa 148,20,60
v_accvgpr_write_b32 a194, v166
v_accvgpr_write_b32 a195, v167
Mvaa 116,20,56
K 192,192,193
Mvaa 84,20,52
buffer_load_dwordx4 a[36:39], v26, s[8:11], s93 offen offset:0 sc1
Ta 16,6,16384
Mvaa 52,20,48
W 9
Ta 18,6,17408
K 193,194,195
Mvaa 56,24,48
K 194,196,197
Mvaa 88,24,52
K 195,198,199
Mvaa 120,24,56
buffer_load_dwordx4 a[40:43], v27, s[8:11], s28 offen offset:0 sc1
v_accvgpr_write_b32 a196, v192
Mvaa 152,24,60
Ta 20,7,16384
Ta 22,7,17408
Mvaa 156,28,60
v_accvgpr_write_b32 a197, v193
v_accvgpr_write_b32 a198, v194
Mvaa 124,28,56
v_accvgpr_write_b32 a199, v195
Mvaa 92,28,52
buffer_load_dwordx4 a[44:47], v27, s[8:11], s93 offen offset:0 sc1
Ta 24,8,16384
Mvaa 60,28,48
s_waitcnt vmcnt(12) lgkmcnt(9)
s_add_u32 s28, s28, 512
s_cmp_eq_u32 s28, s4
s_cselect_b32 s93, 10240, 0
Mvaa 32,0,128
s_sub_u32 s28, s28, s93
s_add_u32 s93, s28, 20480
Ta 26,8,17408
Mvaa 64,0,132
G 164,200
G 165,201
Mvaa 96,0,136
D 20,48,65,0
G 166,202
Mvaa 128,0,140
Ta 28,9,16384
G 167,203
Mvaa 132,4,140
Ta 30,9,17408
G 172,204
Mvaa 100,4,136
G 173,205
G 174,206
Mvaa 68,4,132
buffer_load_dwordx4 v[184:187], v26, s[8:11], s28 offen offset:0 sc1
G 175,207
Mvaa 36,4,128
W 8
Ta 0,2,24576
Ta 2,2,25600
Mvaa 40,8,128
K 164,164,165
Mvaa 72,8,132
K 165,166,167
Mvaa 104,8,136
buffer_load_dwordx4 v[188:191], v26, s[8:11], s93 offen offset:0 sc1
K 166,172,173
Mvaa 136,8,140
Ta 4,3,24576
Ta 6,3,25600
Mvaa 140,12,140
K 167,174,175
v_accvgpr_write_b32 a200, v164
Mvaa 108,12,136
v_accvgpr_write_b32 a201, v165
Mvaa 76,12,132
D 21,48,65,1024
v_accvgpr_write_b32 a202, v166
Mvaa 44,12,128
W 8
Ta 8,4,24576
Ta 10,4,25600
Mvaa 48,16,128
v_accvgpr_write_b32 a203, v167
K 240,240,241
Mvaa 80,16,132
Mvaa 112,16,136
D 22,48,65,2048
K 241,242,243
Mvaa 144,16,140
Ta 12,5,24576
Ta 14,5,25600
Mvaa 148,20,140
K 242,244,245
K 243,246,247
Mvaa 116,20,136
v_accvgpr_write_b32 a204, v240
Mvaa 84,20,132
D 23,48,65,3072
v_accvgpr_write_b32 a205, v241
Mvaa 52,20,128
W 8
s_add_u32 s65, s5, 16
s_cmp_ge_u32 s65, 20
s_cselect_b32 s68, 20, 0
Mvaa 56,24,128
s_sub_u32 s65, s65, s68
Ta 16,6,24576
Ta 18,6,25600
Mvaa 88,24,132
v_accvgpr_write_b32 a206, v242
v_accvgpr_write_b32 a207, v243
Mvaa 120,24,136
buffer_load_dwordx4 a[48:51], v27, s[8:11], s28 offen offset:0 sc1
G 164,208
Mvaa 152,24,140
Ta 20,7,24576
Ta 22,7,25600
Mvaa 156,28,140
s_lshl_b32 s65, s65, 18
G 165,209
G 166,210
Mvaa 124,28,136
G 167,211
Mvaa 92,28,132
buffer_load_dwordx4 a[52:55], v27, s[8:11], s93 offen offset:0 sc1
G 172,212
Mvaa 60,28,128
s_waitcnt vmcnt(16) lgkmcnt(8)
s_add_u32 s28, s28, 512
s_cmp_eq_u32 s28, s4
s_cselect_b32 s93, 10240, 0
Mvaa 32,0,144
s_sub_u32 s28, s28, s93
s_add_u32 s93, s28, 20480
Ta 24,8,24576
Mvaa 64,0,148
Ta 26,8,25600
G 173,213
Mvaa 96,0,152
buffer_load_dwordx4 a[56:59], v26, s[8:11], s28 offen offset:0 sc1
G 174,214
Mvaa 128,0,156
s_add_u32 m0, s63, 98304
Ta 28,9,24576
G 175,215
Mvaa 132,4,156
s_lshl_b32 s80, s56, 9
Ta 30,9,25600
K 164,164,165
Mvaa 100,4,152
K 165,166,167
K 166,172,173
Mvaa 68,4,148
s_lshl_b32 s81, s57, 4
buffer_load_dwordx4 a[60:63], v26, s[8:11], s93 offen offset:0 sc1
K 167,174,175
Mvaa 36,4,144
W 8
Ta 0,10,0
Ta 2,10,1024
Mvaa 40,8,144
s_add_u32 s80, s80, s81
v_accvgpr_write_b32 a208, v164
v_accvgpr_write_b32 a209, v165
Mvaa 72,8,148
s_and_b32 s81, s14, 1
v_accvgpr_write_b32 a210, v166
Mvaa 104,8,152
s_lshl_b32 s81, s81, 3
buffer_load_dwordx4 v[192:195], v27, s[8:11], s28 offen offset:0 sc1
v_accvgpr_write_b32 a211, v167
Mvaa 136,8,156
Ta 4,11,0
Ta 6,11,1024
Mvaa 140,12,156
s_add_u32 s80, s80, s81
K 248,248,249
Mvaa 108,12,152
s_add_u32 s71, s80, 4096
K 249,250,251
Mvaa 76,12,148
buffer_load_dwordx4 v[196:199], v27, s[8:11], s93 offen offset:0 sc1
K 250,252,253
Mvaa 44,12,144
W 8
s_add_u32 s28, s28, 512
s_cmp_eq_u32 s28, s4
s_cselect_b32 s93, 10240, 0
Mvaa 48,16,144
s_sub_u32 s28, s28, s93
s_add_u32 s93, s28, 20480
Ta 8,12,0
Mvaa 80,16,148
Ta 10,12,1024
K 251,254,255
Mvaa 112,16,152
buffer_load_dwordx4 a[128:131], v26, s[8:11], s28 offen offset:0 sc1
v_accvgpr_write_b32 a212, v248
Mvaa 144,16,156
v_accvgpr_write_b32 a213, v249
v_accvgpr_write_b32 a214, v250
Mvaa 148,20,156
Ta 12,13,0
Ta 14,13,1024
Mvaa 116,20,152
v_accvgpr_write_b32 a215, v251
G 164,216
Mvaa 84,20,148
buffer_load_dwordx4 a[132:135], v26, s[8:11], s93 offen offset:0 sc1
G 165,217
Mvaa 52,20,144
W 8
Ta 16,14,0
Ta 18,14,1024
Mvaa 56,24,144
G 166,218
G 167,219
Mvaa 88,24,148
G 172,220
Mvaa 120,24,152
buffer_load_dwordx4 a[136:139], v27, s[8:11], s28 offen offset:0 sc1
G 173,221
Mvaa 152,24,156
Ta 20,15,0
Ta 22,15,1024
Mvaa 156,28,156
G 174,222
G 175,223
Mvaa 124,28,152
K 164,164,165
Mvaa 92,28,148
buffer_load_dwordx4 a[140:143], v27, s[8:11], s93 offen offset:0 sc1
Ta 24,24,0
Mvaa 60,28,144
s_waitcnt vmcnt(16) lgkmcnt(9)
Ta 26,24,1024
K 165,166,167
Mvaa 32,0,32
s_add_u32 s28, s28, 512
s_cmp_eq_u32 s28, s4
s_cselect_b32 s93, 10240, 0
s_sub_u32 s28, s28, s93
Mvaa 64,0,36
s_add_u32 s93, s28, 20480
K 166,172,173
K 167,174,175
Mvaa 96,0,40
D 16,48,65,0
v_accvgpr_write_b32 a216, v164
Mvaa 128,0,44
Ta 28,25,0
Ta 30,25,1024
Mvaa 132,4,44
v_accvgpr_write_b32 a217, v165
v_accvgpr_write_b32 a218, v166
Mvaa 100,4,40
v_accvgpr_write_b32 a219, v167
Mvaa 68,4,36
D 17,48,65,1024
Ta 0,10,8192
Mvaa 36,4,32
W 9
Ta 2,10,9216
v_add_u32 v2, 65536, v10
Mvaa 40,8,32
K 200,200,201
K 201,202,203
Mvaa 72,8,36
K 202,204,205
Mvaa 104,8,40
D 18,48,65,2048
K 203,206,207
Mvaa 136,8,44
Ta 4,11,8192
Ta 6,11,9216
Mvaa 140,12,44
v_add_u32 v3, 65536, v11
v_accvgpr_write_b32 a220, v200
Mvaa 108,12,40
v_accvgpr_write_b32 a221, v201
v_accvgpr_write_b32 a222, v202
Mvaa 76,12,36
buffer_load_dwordx4 a[144:147], v26, s[8:11], s28 offen offset:0 sc1
v_accvgpr_write_b32 a223, v203
Mvaa 44,12,32
W 8
Ta 8,12,8192
Ta 10,12,9216
Mvaa 48,16,32
v_add_u32 v4, 65536, v12
G 164,224
Mvaa 80,16,36
G 165,225
Mvaa 112,16,40
buffer_load_dwordx4 a[148:151], v26, s[8:11], s93 offen offset:0 sc1
G 166,226
Mvaa 144,16,44
Ta 12,13,8192
Ta 14,13,9216
Mvaa 148,20,44
v_add_u32 v5, 65536, v13
G 167,227
Mvaa 116,20,40
G 172,228
G 173,229
Mvaa 84,20,36
buffer_load_dwordx4 a[152:155], v27, s[8:11], s28 offen offset:0 sc1
G 174,230
Mvaa 52,20,32
W 8
Ta 16,14,8192
Ta 18,14,9216
Mvaa 56,24,32
v_add_u32 v6, 65536, v14
G 175,231
Mvaa 88,24,36
K 164,164,165
Mvaa 120,24,40
buffer_load_dwordx4 a[156:159], v27, s[8:11], s93 offen offset:0 sc1
K 165,166,167
Mvaa 152,24,44
s_add_u32 s28, s28, 512
s_cmp_eq_u32 s28, s4
s_cselect_b32 s93, 10240, 0
s_sub_u32 s28, s28, s93
Mvaa 156,28,44
s_add_u32 s93, s28, 20480
Ta 20,15,8192
Ta 22,15,9216
Mvaa 124,28,40
v_add_u32 v7, 65536, v15
K 166,172,173
Mvaa 92,28,36
D 19,48,65,3072
K 167,174,175
Mvaa 60,28,32
s_waitcnt vmcnt(16) lgkmcnt(8)
s_add_u32 m0, s63, 102400
v_accvgpr_write_b32 a224, v164
Mvav 32,0,184
Ta 24,24,8192
v_accvgpr_write_b32 a225, v165
Mvav 64,0,188
Ta 26,24,9216
v_add_u32 v8, 65536, v24
Mvaa 96,0,48
D 20,48,65,0
v_accvgpr_write_b32 a226, v166
Mvaa 128,0,52
v_accvgpr_write_b32 a227, v167
K 208,208,209
)"
R"(Mvaa 132,4,52
Ta 28,25,8192
Ta 30,25,9216
Mvaa 100,4,48
v_add_u32 v9, 65536, v25
K 209,210,211
Mvav 68,4,188
D 21,48,65,1024
K 210,212,213
Mvav 36,4,184
W 8
K 211,214,215
v_accvgpr_write_b32 a228, v208
Mvav 40,8,184
Ta 0,10,16384
Ta 2,10,17408
Mvav 72,8,188
v_accvgpr_write_b32 a229, v209
v_accvgpr_write_b32 a230, v210
Mvaa 104,8,48
buffer_load_dwordx4 v[200:203], v26, s[8:11], s28 offen offset:0 sc1
v_accvgpr_write_b32 a231, v211
Mvaa 136,8,52
Ta 4,11,16384
Ta 6,11,17408
Mvaa 140,12,52
G 164,232
G 165,233
Mvaa 108,12,48
G 166,234
G 167,235
Mvav 76,12,188
buffer_load_dwordx4 v[204:207], v26, s[8:11], s93 offen offset:0 sc1
G 172,236
Mvav 44,12,184
W 8
Ta 8,12,16384
Ta 10,12,17408
Mvav 48,16,184
G 173,237
G 174,238
Mvav 80,16,188
G 175,239
K 164,164,165
Mvaa 112,16,48
D 22,48,65,2048
K 165,166,167
Mvaa 144,16,52
Ta 12,13,16384
Ta 14,13,17408
Mvaa 148,20,52
K 166,172,173
K 167,174,175
Mvaa 116,20,48
v_accvgpr_write_b32 a232, v164
v_accvgpr_write_b32 a233, v165
Mvav 84,20,188
buffer_load_dwordx4 a[32:35], v27, s[8:11], s28 offen offset:0 sc1
v_accvgpr_write_b32 a234, v166
Mvav 52,20,184
W 8
Ta 16,14,16384
Ta 18,14,17408
Mvav 56,24,184
v_accvgpr_write_b32 a235, v167
G 164,160
Mvav 88,24,188
G 165,161
Mvaa 120,24,48
buffer_load_dwordx4 a[36:39], v27, s[8:11], s93 offen offset:0 sc1
G 166,162
Mvaa 152,24,52
s_add_u32 s28, s28, 512
s_cmp_eq_u32 s28, s4
s_cselect_b32 s93, 10240, 0
s_sub_u32 s28, s28, s93
Mvaa 156,28,52
s_add_u32 s93, s28, 20480
Ta 20,15,16384
Ta 22,15,17408
Mvaa 124,28,48
G 167,163
G 172,164
Mvav 92,28,188
buffer_load_dwordx4 a[40:43], v26, s[8:11], s28 offen offset:0 sc1
G 173,165
Mvav 60,28,184
s_waitcnt vmcnt(20) lgkmcnt(8)
G 174,166
G 175,167
Mvaa 32,0,56
Ta 24,24,16384
Ta 26,24,17408
Mvaa 64,0,60
K 164,164,165
K 165,166,167
Mvav 96,0,192
D 23,48,65,3072
K 166,172,173
Mvav 128,0,196
K 167,174,175
v_accvgpr_write_b32 a236, v164
Mvav 132,4,196
Ta 28,25,16384
Ta 30,25,17408
Mvav 100,4,192
v_accvgpr_write_b32 a237, v165
v_accvgpr_write_b32 a238, v166
Mvaa 68,4,60
buffer_load_dwordx4 a[44:47], v26, s[8:11], s93 offen offset:0 sc1
v_accvgpr_write_b32 a239, v167
Mvaa 36,4,56
W 8
G 164,240
G 165,241
Mvaa 40,8,56
s_lshr_b32 s80, s15, 3
Ta 0,10,24576
Ta 2,10,25600
Mvaa 72,8,60
G 166,242
G 167,243
Mvav 104,8,192
s_lshl_b32 s80, s80, 1
buffer_load_dwordx4 v[208:211], v27, s[8:11], s28 offen offset:0 sc1
G 172,244
Mvav 136,8,196
s_add_u32 s80, s80, s14
Ta 4,11,24576
G 173,245
Mvav 140,12,196
s_sub_u32 s80, s80, 2
Ta 6,11,25600
G 174,246
Mvav 108,12,192
s_lshl_b32 s80, s80, 12
G 175,247
K 164,164,165
Mvaa 76,12,60
buffer_load_dwordx4 v[212:215], v27, s[8:11], s93 offen offset:0 sc1
K 165,166,167
Mvaa 44,12,56
W 8
s_add_u32 s28, s28, 512
s_cmp_eq_u32 s28, s4
s_cselect_b32 s93, 10240, 0
Mvaa 48,16,56
s_sub_u32 s28, s28, s93
s_add_u32 s93, s28, 20480
K 166,172,173
Mvaa 80,16,60
Ta 8,12,24576
v_min_u32 v28, 496, v1
Mvav 112,16,192
buffer_load_dwordx4 v[184:187], v26, s[8:11], s28 offen offset:0 sc1
Ta 10,12,25600
Mvav 144,16,196
K 167,174,175
v_lshl_add_u32 v28, v28, 3, s80
Mvav 148,20,196
v_accvgpr_write_b32 a240, v164
v_min_u32 v28, .Lend%=-.Lbeg%=-4, v28
Mvav 116,20,192
v_accvgpr_write_b32 a241, v165
v_accvgpr_write_b32 a242, v166
Mvaa 84,20,60
buffer_load_dwordx4 v[188:191], v26, s[8:11], s93 offen offset:0 sc1
v_accvgpr_write_b32 a243, v167
Mvaa 52,20,56
W 6
Ta 12,13,24576
Ta 14,13,25600
Mvaa 56,24,56
G 164,168
G 165,169
Mvaa 88,24,60
G 166,170
G 167,171
Mvav 120,24,192
buffer_load_dwordx4 a[48:51], v27, s[8:11], s28 offen offset:0 sc1
G 172,172
Mvav 152,24,196
Ta 16,14,24576
Ta 18,14,25600
Mvav 156,28,196
G 173,173
G 174,174
Mvav 124,28,192
G 175,175
K 164,164,165
Mvaa 92,28,60
global_load_dword v29, v28, s[6:7]
Ta 20,15,24576
Mvaa 60,28,56
s_waitcnt vmcnt(24) lgkmcnt(7)
Ta 22,15,25600
K 165,166,167
Mvaa 32,0,128
K 166,172,173
K 167,174,175
Mvaa 64,0,132
v_accvgpr_write_b32 a244, v164
v_accvgpr_write_b32 a245, v165
Mvaa 96,0,136
buffer_load_dwordx4 a[52:55], v27, s[8:11], s93 offen offset:0 sc1
Ta 24,24,24576
Mvaa 128,0,140
s_add_u32 s28, s28, 512
Ta 26,24,25600
v_accvgpr_write_b32 a246, v166
Mvaa 132,4,140
s_cmp_eq_u32 s28, s4
s_cselect_b32 s93, 10240, 0
s_sub_u32 s28, s28, s93
v_accvgpr_write_b32 a247, v167
Mvaa 100,4,136
s_add_u32 s93, s28, 20480
G 164,64
G 165,65
Mvaa 68,4,132
buffer_load_dwordx4 a[56:59], v26, s[8:11], s28 offen offset:0 sc1
G 166,66
Mvaa 36,4,128
W 6
Ta 28,25,24576
Ta 30,25,25600
Mvaa 40,8,128
G 167,67
G 172,68
Mvaa 72,8,132
G 173,69
G 174,70
Mvaa 104,8,136
Ta 0,2,0
G 175,71
Mvaa 136,8,140
Ta 2,2,1024
K 164,164,165
Mvaa 140,12,140
buffer_load_dwordx4 a[60:63], v26, s[8:11], s93 offen offset:0 sc1
K 165,166,167
Mvaa 108,12,136
K 166,172,173
K 167,174,175
Mvaa 76,12,132
Ta 4,3,0
Ta 6,3,1024
Mvaa 44,12,128
W 8
v_accvgpr_write_b32 a64, v164
v_accvgpr_write_b32 a65, v165
Mvaa 48,16,128
v_accvgpr_write_b32 a66, v166
v_accvgpr_write_b32 a67, v167
Mvaa 80,16,132
Ta 8,4,0
G 164,176
Mvaa 112,16,136
Ta 10,4,1024
G 165,177
Mvaa 144,16,140
buffer_load_dwordx4 v[192:195], v27, s[8:11], s28 offen offset:0 sc1
G 166,178
Mvaa 148,20,140
G 167,179
G 172,180
Mvaa 116,20,136
Ta 12,5,0
G 173,181
Mvaa 84,20,132
Ta 14,5,1024
G 174,182
Mvaa 52,20,128
W 8
G 175,183
K 164,164,165
Mvaa 56,24,128
K 165,166,167
K 166,172,173
Mvaa 88,24,132
Ta 16,6,0
Ta 18,6,1024
Mvaa 120,24,136
K 167,174,175
v_accvgpr_write_b32 a68, v164
Mvaa 152,24,140
buffer_load_dwordx4 v[196:199], v27, s[8:11], s93 offen offset:0 sc1
v_accvgpr_write_b32 a69, v165
Mvaa 156,28,140
Ta 20,7,0
v_accvgpr_write_b32 a70, v166
Mvaa 124,28,136
Ta 22,7,1024
v_accvgpr_write_b32 a71, v167
Mvaa 92,28,132
G 164,72
G 165,73
Mvaa 60,28,128
s_waitcnt vmcnt(22) lgkmcnt(8)
Ta 24,8,0
G 166,74
Mvaa 32,0,144
s_add_u32 s28, s28, 512
Ta 26,8,1024
G 167,75
Mvaa 64,0,148
s_cmp_eq_u32 s28, s4
s_cselect_b32 s93, 10240, 0
s_sub_u32 s28, s28, s93
G 172,76
Mvaa 96,0,152
s_add_u32 s93, s28, 20480
buffer_load_dwordx4 a[128:131], v26, s[8:11], s28 offen offset:0 sc1
G 173,77
Mvaa 128,0,156
Ta 28,9,0
G 174,78
Mvaa 132,4,156
Ta 30,9,1024
G 175,79
Mvaa 100,4,152
K 164,164,165
Mvaa 68,4,148
Ta 0,2,8192
K 165,166,167
Mvaa 36,4,144
W 9
Ta 2,2,9216
K 166,172,173
Mvaa 40,8,144
buffer_load_dwordx4 a[132:135], v26, s[8:11], s93 offen offset:0 sc1
K 167,174,175
Mvaa 72,8,148
v_accvgpr_write_b32 a72, v164
Mvaa 104,8,152
Ta 4,3,8192
v_accvgpr_write_b32 a73, v165
Mvaa 136,8,156
Ta 6,3,9216
v_accvgpr_write_b32 a74, v166
Mvaa 140,12,156
v_accvgpr_write_b32 a75, v167
Mvaa 108,12,152
K 216,216,217
Mvaa 76,12,148
Ta 8,4,8192
Ta 10,4,9216
Mvaa 44,12,144
W 10
K 217,218,219
Mvaa 48,16,144
buffer_load_dwordx4 a[136:139], v27, s[8:11], s28 offen offset:0 sc1
K 218,220,221
Mvaa 80,16,148
K 219,222,223
Mvaa 112,16,152
Ta 12,5,8192
v_accvgpr_write_b32 a76, v216
Mvaa 144,16,156
Ta 14,5,9216
v_lshrrev_b32 v20, 4, v1
Mvaa 148,20,156
v_accvgpr_write_b32 a77, v217
v_accvgpr_write_b32 a78, v218
Mvaa 116,20,152
v_add_u32 v20, 0, v20
v_accvgpr_write_b32 a79, v219
Mvaa 84,20,148
Ta 16,6,8192
v_mul_u32_u24 v22, 205, v20
Mvaa 52,20,144
W 9
Ta 18,6,9216
G 164,80
Mvaa 56,24,144
G 165,81
v_lshrrev_b32 v22, 12, v22
Mvaa 88,24,148
buffer_load_dwordx4 a[140:143], v27, s[8:11], s93 offen offset:0 sc1
G 166,82
Mvaa 120,24,152
G 167,83
v_mul_u32_u24 v23, 20, v22
Mvaa 152,24,156
Ta 20,7,8192
Ta 22,7,9216
Mvaa 156,28,156
G 172,84
v_sub_u32 v20, v20, v23
Mvaa 124,28,152
G 173,85
G 174,86
Mvaa 92,28,148
v_lshrrev_b32 v23, 1, v22
G 175,87
Mvaa 60,28,144
s_waitcnt vmcnt(18) lgkmcnt(8)
Ta 24,8,8192
Ta 26,8,9216
Mvav 32,0,200
)"
R"(s_add_u32 s28, s28, 512
K 164,164,165
v_and_b32 v23, 2, v23
Mvav 64,0,204
s_cmp_eq_u32 s28, s4
s_cselect_b32 s93, 10240, 0
s_sub_u32 s28, s28, s93
s_add_u32 s93, s28, 20480
Mvaa 96,0,32
buffer_load_dwordx4 v[216:219], v26, s[8:11], s28 offen offset:0 sc1
K 165,166,167
Mvaa 128,0,36
v_xor_b32 v20, v20, v23
K 166,172,173
Mvaa 132,4,36
Ta 28,9,8192
Ta 30,9,9216
Mvaa 100,4,32
K 167,174,175
v_mul_u32_u24 v22, 80, v22
Mvav 68,4,204
v_accvgpr_write_b32 a80, v164
v_accvgpr_write_b32 a81, v165
Mvav 36,4,200
W 8
v_add_lshl_u32 v16, v20, v22, 4
v_accvgpr_write_b32 a82, v166
Mvav 40,8,200
Ta 0,2,16384
Ta 2,2,17408
Mvav 72,8,204
v_lshrrev_b32 v20, 4, v1
v_accvgpr_write_b32 a83, v167
Mvaa 104,8,32
buffer_load_dwordx4 v[220:223], v26, s[8:11], s93 offen offset:0 sc1
K 224,224,225
Mvaa 136,8,36
v_add_u32 v20, 64, v20
K 225,226,227
Mvaa 140,12,36
Ta 4,3,16384
Ta 6,3,17408
Mvaa 108,12,32
K 226,228,229
v_mul_u32_u24 v22, 205, v20
Mvav 76,12,204
K 227,230,231
v_lshrrev_b32 v22, 12, v22
Mvav 44,12,200
W 8
v_accvgpr_write_b32 a84, v224
v_accvgpr_write_b32 a85, v225
Mvav 48,16,200
Ta 8,4,16384
Ta 10,4,17408
Mvav 80,16,204
v_mul_u32_u24 v23, 20, v22
v_accvgpr_write_b32 a86, v226
Mvaa 112,16,32
buffer_load_dwordx4 a[144:147], v27, s[8:11], s28 offen offset:0 sc1
v_accvgpr_write_b32 a87, v227
Mvaa 144,16,36
v_sub_u32 v20, v20, v23
G 164,88
Mvaa 148,20,36
Ta 12,5,16384
Ta 14,5,17408
Mvaa 116,20,32
G 165,89
v_lshrrev_b32 v23, 1, v22
Mvav 84,20,204
G 166,90
v_and_b32 v23, 2, v23
Mvav 52,20,200
W 8
G 167,91
G 172,92
Mvav 56,24,200
Ta 16,6,16384
v_xor_b32 v20, v20, v23
Mvav 88,24,204
Ta 18,6,17408
G 173,93
Mvaa 120,24,32
G 174,94
v_mul_u32_u24 v22, 80, v22
Mvaa 152,24,36
buffer_load_dwordx4 a[148:151], v27, s[8:11], s93 offen offset:0 sc1
G 175,95
Mvaa 156,28,36
Ta 20,7,16384
v_add_lshl_u32 v17, v20, v22, 4
Mvaa 124,28,32
Ta 22,7,17408
K 164,164,165
Mvav 92,28,204
K 165,166,167
v_subrev_u32 v17, 1024, v17
Mvav 60,28,200
s_waitcnt vmcnt(17) lgkmcnt(8)
K 166,172,173
K 167,174,175
Mvaa 32,0,40
s_add_u32 s28, s28, 512
Ta 24,8,16384
v_lshrrev_b32 v20, 4, v1
Mvaa 64,0,44
s_cmp_eq_u32 s28, s4
s_cselect_b32 s93, 10240, 0
Ta 26,8,17408
v_accvgpr_write_b32 a88, v164
Mvav 96,0,208
s_sub_u32 s28, s28, s93
v_add_u32 v20, 128, v20
v_accvgpr_write_b32 a89, v165
Mvav 128,0,212
s_add_u32 s93, s28, 20480
buffer_load_dwordx4 a[152:155], v26, s[8:11], s28 offen offset:0 sc1
v_accvgpr_write_b32 a90, v166
Mvav 132,4,212
v_mul_u32_u24 v22, 205, v20
v_accvgpr_write_b32 a91, v167
Mvav 100,4,208
Ta 28,9,16384
Ta 30,9,17408
Mvaa 68,4,44
K 232,232,233
v_lshrrev_b32 v22, 12, v22
Mvaa 36,4,40
W 8
K 233,234,235
v_mul_u32_u24 v23, 20, v22
Mvaa 40,8,40
Ta 0,2,24576
K 234,236,237
Mvaa 72,8,44
Ta 2,2,25600
K 235,238,239
Mvav 104,8,208
v_sub_u32 v20, v20, v23
v_accvgpr_write_b32 a92, v232
Mvav 136,8,212
buffer_load_dwordx4 a[156:159], v26, s[8:11], s93 offen offset:0 sc1
v_accvgpr_write_b32 a93, v233
Mvav 140,12,212
v_lshrrev_b32 v23, 1, v22
v_accvgpr_write_b32 a94, v234
Mvav 108,12,208
Ta 4,3,24576
Ta 6,3,25600
Mvaa 76,12,44
v_and_b32 v23, 2, v23
v_accvgpr_write_b32 a95, v235
Mvaa 44,12,40
W 8
G 164,96
v_xor_b32 v20, v20, v23
Mvaa 48,16,40
G 165,97
G 166,98
Mvaa 80,16,44
Ta 8,4,24576
Ta 10,4,25600
Mvav 112,16,208
v_mul_u32_u24 v22, 80, v22
G 167,99
Mvav 144,16,212
buffer_load_dwordx4 v[224:227], v27, s[8:11], s28 offen offset:0 sc1
G 172,100
Mvav 148,20,212
G 173,101
v_add_lshl_u32 v18, v20, v22, 4
Mvav 116,20,208
G 174,102
G 175,103
Mvaa 84,20,44
Ta 12,5,24576
Ta 14,5,25600
Mvaa 52,20,40
W 8
v_subrev_u32 v18, 2048, v18
K 164,164,165
Mvaa 56,24,40
K 165,166,167
v_lshrrev_b32 v20, 4, v1
Mvaa 88,24,44
K 166,172,173
K 167,174,175
Mvav 120,24,208
Ta 16,6,24576
Ta 18,6,25600
Mvav 152,24,212
v_add_u32 v20, 192, v20
v_accvgpr_write_b32 a96, v164
Mvav 156,28,212
v_accvgpr_write_b32 a97, v165
v_accvgpr_write_b32 a98, v166
Mvav 124,28,208
buffer_load_dwordx4 v[228:231], v27, s[8:11], s93 offen offset:0 sc1
v_mul_u32_u24 v22, 205, v20
Mvaa 92,28,44
v_accvgpr_write_b32 a99, v167
G 164,248
Mvaa 60,28,40
s_waitcnt vmcnt(16) lgkmcnt(6)
Ta 20,7,24576
v_lshrrev_b32 v22, 12, v22
Mvav 32,0,184
s_add_u32 s28, s28, 512
Ta 22,7,25600
G 165,249
Mvav 64,0,188
s_cmp_eq_u32 s28, s4
s_cselect_b32 s93, 10240, 0
G 166,250
v_mul_u32_u24 v23, 20, v22
Mvaa 96,0,48
s_sub_u32 s28, s28, s93
G 167,251
G 172,252
Mvaa 128,0,52
s_add_u32 s93, s28, 20480
v_sub_u32 v20, v20, v23
G 173,253
Mvaa 132,4,52
Ta 24,8,24576
Ta 26,8,25600
Mvaa 100,4,48
G 174,254
G 175,255
Mvav 68,4,188
buffer_load_dwordx4 v[200:203], v26, s[8:11], s28 offen offset:0 sc1
v_lshrrev_b32 v23, 1, v22
Mvav 36,4,184
W 6
K 164,164,165
K 165,166,167
Mvav 40,8,184
v_and_b32 v23, 2, v23
K 166,172,173
Mvav 72,8,188
Ta 28,9,24576
Ta 30,9,25600
Mvaa 104,8,48
K 167,174,175
v_xor_b32 v20, v20, v23
Mvaa 136,8,52
v_accvgpr_write_b32 a100, v164
v_accvgpr_write_b32 a101, v165
Mvaa 140,12,52
W 0
s_barrier
v_mul_u32_u24 v22, 80, v22
Mvaa 108,12,48
v_accvgpr_write_b32 a102, v166
v_accvgpr_write_b32 a103, v167
Mvav 76,12,188
G 164,104
v_add_lshl_u32 v19, v20, v22, 4
Mvav 44,12,184
buffer_load_dwordx4 v[204:207], v26, s[8:11], s93 offen offset:0 sc1
G 165,105
Mvav 48,16,184
v_subrev_u32 v19, 3072, v19
G 166,106
Mvav 80,16,188
G 167,107
v_lshrrev_b32 v20, 4, v1
Mvaa 112,16,48
Ta 0,10,32768
G 172,108
Mvaa 144,16,52
Ta 2,10,33792
Ta 4,11,32768
Mvaa 148,20,52
Ta 6,11,33792
G 173,109
Mvaa 116,20,48
v_add_u32 v20, 256, v20
G 174,110
Mvav 84,20,188
Ta 8,12,32768
Ta 10,12,33792
Mvav 52,20,184
v_mul_u32_u24 v22, 205, v20
G 175,111
Mvav 56,24,184
buffer_load_dwordx4 a[32:35], v27, s[8:11], s28 offen offset:0 sc1
K 164,164,165
Mvav 88,24,188
v_lshrrev_b32 v22, 12, v22
K 165,166,167
Mvaa 120,24,48
Ta 12,13,32768
Ta 14,13,33792
Mvaa 152,24,52
K 166,172,173
v_mul_u32_u24 v23, 20, v22
Mvaa 156,28,52
K 167,174,175
v_sub_u32 v20, v20, v23
Mvaa 124,28,48
v_accvgpr_write_b32 a104, v164
v_accvgpr_write_b32 a105, v165
Mvav 92,28,188
Ta 16,14,32768
Ta 18,14,33792
Mvav 60,28,184
s_waitcnt vmcnt(15) lgkmcnt(6)
v_lshrrev_b32 v23, 1, v22
v_accvgpr_write_b32 a106, v166
Mvaa 32,0,56
buffer_load_dwordx4 a[36:39], v27, s[8:11], s93 offen offset:0 sc1
v_accvgpr_write_b32 a107, v167
Mvaa 64,0,60
s_add_u32 s28, s28, 512
v_and_b32 v23, 2, v23
K 176,176,177
Mvav 96,0,192
s_cmp_eq_u32 s28, s4
s_cselect_b32 s93, 10240, 0
Ta 20,15,32768
v_xor_b32 v20, v20, v23
Mvav 128,0,196
s_sub_u32 s28, s28, s93
Ta 22,15,33792
K 177,178,179
Mvav 132,4,196
s_add_u32 s93, s28, 20480
K 178,180,181
v_mul_u32_u24 v22, 80, v22
Mvav 100,4,192
K 179,182,183
v_accvgpr_write_b32 a108, v176
Mvaa 68,4,60
Ta 24,24,32768
v_add_lshl_u32 v20, v20, v22, 4
Mvaa 36,4,56
W 5
Ta 26,24,33792
v_accvgpr_write_b32 a109, v177
Mvaa 40,8,56
v_lshrrev_b32 v22, 6, v1
v_accvgpr_write_b32 a110, v178
Mvaa 72,8,60
buffer_load_dwordx4 v[232:235], v26, s[8:11], s28 offen offset:0 sc1
v_accvgpr_write_b32 a111, v179
Mvav 104,8,192
Ta 28,25,32768
v_mul_u32_u24 v22, 320, v22
Mvav 136,8,196
Ta 30,25,33792
G 164,112
Mvav 140,12,196
G 165,113
v_bfe_u32 v23, v1, 4, 2
Mvav 108,12,192
G 166,114
v_lshl_add_u32 v22, v23, 3, v22
Mvaa 76,12,60
Ta 0,10,40960
Ta 2,10,41984
Mvaa 44,12,56
W 6
G 167,115
v_bfe_u32 v23, v1, 8, 1
Mvaa 48,16,56
buffer_load_dwordx4 v[236:239], v26, s[8:11], s93 offen offset:0 sc1
G 172,116
Mvaa 80,16,60
G 173,117
v_lshlrev_b32 v23, 5, v23
Mvav 112,16,192
Ta 4,11,40960
Ta 6,11,41984
Mvav 144,16,196
G 174,118
v_add_u32 v21, v22, v23
Mvav 148,20,196
G 175,119
K 164,164,165
Mvav 116,20,192
Ta 8,12,40960
v_sub_u32 v22, v22, v23
Mvaa 84,20,60
s_mul_i32 s94, s59, 655360
Ta 10,12,41984
K 165,166,167
Mvaa 52,20,56
W 6
buffer_load_dwordx4 a[40:43], v27, s[8:11], s28 offen offset:0 sc1
K 166,172,173
Mvaa 56,24,56
s_mul_i32 s95, s58, 320
Ta 12,13,40960
K 167,174,175
Mvaa 88,24,60
s_add_u32 s94, s94, s95
Ta 14,13,41984
v_accvgpr_write_b32 a112, v164
)"
R"(Mvav 120,24,192
s_add_u32 s40, s26, s94
s_addc_u32 s41, s27, 0
v_accvgpr_write_b32 a113, v165
Mvav 152,24,196
s_and_b32 s41, s41, 0xffff
Ta 16,14,40960
v_accvgpr_write_b32 a114, v166
Mvav 156,28,196
s_mov_b32 s42, 5242880
Ta 18,14,41984
v_accvgpr_write_b32 a115, v167
Mvav 124,28,192
s_mov_b32 s43, 0x20000
buffer_load_dwordx4 a[44:47], v27, s[8:11], s93 offen offset:0 sc1
v_accvgpr_write_b32 a116, v168
Mvaa 92,28,60
s_sub_u32 s42, s42, s94
Ta 20,15,40960
v_accvgpr_write_b32 a117, v169
Mvaa 60,28,56
s_waitcnt vmcnt(16) lgkmcnt(7)
Ta 22,15,41984
v_accvgpr_write_b32 a118, v170
Mvaa 32,0,128
s_mov_b64 s[44:45], s[8:9]
v_accvgpr_write_b32 a119, v171
G 164,120
Mvaa 64,0,132
s_add_u32 s28, s28, 512
Ta 24,24,40960
Ta 26,24,41984
Mvaa 96,0,136
s_cmp_eq_u32 s28, s4
s_cselect_b32 s93, 10240, 0
G 165,121
G 166,122
Mvaa 128,0,140
s_sub_u32 s28, s28, s93
s_add_u32 s93, s28, 20480
buffer_load_dwordx4 v[208:211], v26, s[8:11], s28 offen offset:0 sc1
Mvaa 132,4,140
s_mov_b32 s47, 0x20000
Ta 28,25,40960
G 167,123
Mvaa 100,4,136
s_and_b32 s48, s14, 1
Ta 30,25,41984
G 172,124
Mvaa 68,4,132
s_mul_i32 s48, s48, 5120
G 173,125
v_bfe_u32 v28, v1, 4, 4
Mvaa 36,4,128
W 8
G 174,126
v_lshlrev_b32 v28, 3, v28
Mvaa 40,8,128
s_lshl_b32 s80, s56, 9
Ta 0,10,49152
Ta 2,10,50176
Mvaa 72,8,132
s_lshr_b32 s51, s14, 1
s_add_u32 s80, s80, 4096
G 175,127
Mvaa 104,8,136
s_and_b32 s94, s14, 1
buffer_load_dwordx4 v[212:215], v26, s[8:11], s93 offen offset:0 sc1
K 164,164,165
Mvaa 136,8,140
Ta 4,11,49152
K 165,166,167
Mvaa 140,12,140
s_lshl_b32 s95, s57, 1
Ta 6,11,50176
K 166,172,173
Mvaa 108,12,136
s_add_u32 s94, s94, s95
K 167,174,175
Mvaa 76,12,132
buffer_load_dwordx2 v[28:29], v28, s[8:11], s80 offen sc1
v_accvgpr_write_b32 a120, v164
Mvaa 44,12,128
W 8
Ta 8,12,49152
Ta 10,12,50176
Mvaa 48,16,128
s_lshl_b32 s54, s94, 3
v_accvgpr_write_b32 a121, v165
v_accvgpr_write_b32 a122, v166
Mvaa 80,16,132
s_add_u32 s54, s54, s59
Mvaa 112,16,136
buffer_load_dwordx4 a[248:251], v27, s[8:11], s28 offen offset:0 sc1
v_accvgpr_write_b32 a123, v167
Mvaa 144,16,140
s_lshl_b32 s54, s54, 6
Ta 12,13,49152
Ta 14,13,50176
Mvaa 148,20,140
v_accvgpr_write_b32 a124, v160
v_accvgpr_write_b32 a125, v161
Mvaa 116,20,136
s_xor_b32 s55, s54, 15872
Mvaa 84,20,132
Ta 16,14,49152
v_accvgpr_write_b32 a126, v162
Mvaa 52,20,128
W 9
s_add_u32 s54, s54, 20480
Ta 18,14,50176
Mvaa 56,24,128
s_and_b32 s81, s14, 1
buffer_load_dwordx4 a[252:255], v27, s[8:11], s93 offen offset:0 sc1
v_accvgpr_write_b32 a127, v163
Mvaa 88,24,132
s_add_u32 s55, s55, 20480
Mvaa 120,24,136
Ta 20,15,49152
Ta 22,15,50176
Mvaa 152,24,140
s_and_b32 s95, s14, 2
s_mul_i32 s91, s81, 17164288
Mvaa 156,28,140
Mvaa 124,28,136
s_lshl_b32 s94, s94, 2
s_mul_i32 s82, s56, 536576
Mvaa 92,28,132
s_add_u32 s94, s94, s95
Ta 24,24,49152
Ta 26,24,50176
Mvaa 60,28,128
s_waitcnt vmcnt(17) lgkmcnt(10)
Mvav 32,0,216
s_add_u32 s91, s91, s82
s_lshl_b32 s95, s95, 4
s_add_u32 s28, s28, 512
Mvav 64,0,220
s_cmp_eq_u32 s28, s4
s_cselect_b32 s93, 10240, 0
s_sub_u32 s28, s28, s93
s_add_u32 s93, s28, 20480
Mvaa 96,0,144
s_add_u32 s55, s55, s95
buffer_load_dwordx4 v[184:187], v26, s[8:11], s28 offen offset:0 sc1
Ta 28,25,49152
Mvaa 128,0,148
s_add_u32 s91, s91, 4194304
Ta 30,25,50176
Mvaa 132,4,148
s_lshl_b32 s49, s94, 17
Mvaa 100,4,144
s_lshr_b32 s82, s14, 1
Mvav 68,4,220
s_lshl_b32 s95, s59, 14
Ta 0,10,57344
Ta 2,10,58368
Mvav 36,4,216
W 10
s_lshl_b32 s83, s82, 14
Mvav 40,8,216
s_add_u32 s49, s49, s95
buffer_load_dwordx4 v[188:191], v26, s[8:11], s93 offen offset:0 sc1
Mvav 72,8,220
Mvaa 104,8,144
s_xor_b32 s50, s49, 16252928
s_add_u32 s91, s91, s83
Ta 4,11,57344
Mvaa 136,8,148
Ta 6,11,58368
Mvaa 140,12,148
s_add_u32 s49, s49, 39845888
s_lshl_b32 s81, s81, 14
Mvaa 108,12,144
Mvav 76,12,220
s_add_u32 s50, s50, 39845888
Ta 8,12,57344
Ta 10,12,58368
Mvav 44,12,216
W 10
s_lshl_b32 s82, s82, 16
s_lshr_b32 s94, s15, 3
Mvav 48,16,216
buffer_load_dwordx4 a[48:51], v27, s[8:11], s28 offen offset:0 sc1
Mvav 80,16,220
s_lshl_b32 s94, s94, 3
s_or_b32 s92, s81, s82
Mvaa 112,16,144
Ta 12,13,57344
Ta 14,13,58368
Mvaa 144,16,148
s_add_u32 s94, s94, s14
Mvaa 148,20,148
s_xor_b32 s92, s92, 0x10000
Mvaa 116,20,144
s_lshl_b32 s94, s94, 4
Mvav 84,20,220
Ta 16,14,57344
Ta 18,14,58368
Mvav 52,20,216
W 10
s_add_u32 s94, s94, s59
Mvav 56,24,216
buffer_load_dwordx4 a[52:55], v27, s[8:11], s93 offen offset:0 sc1
Mvav 88,24,220
s_mul_i32 s52, s94, 5120
Mvaa 120,24,144
Ta 20,15,57344
Ta 22,15,58368
Mvaa 152,24,148
s_add_u32 s52, s52, 56623104
Mvaa 156,28,148
Mvaa 124,28,144
s_lshl_b32 s53, s94, 3
Mvav 92,28,220
Ta 24,24,57344
Ta 26,24,58368
Mvav 60,28,216
s_waitcnt vmcnt(17) lgkmcnt(10)
s_add_u32 s53, s53, 65536
Mvaa 32,0,152
s_waitcnt vmcnt(6)
v_cmp_eq_u64 vcc, s[12:13], v[28:29]
s_cmp_eq_u64 vcc, -1
s_cbranch_scc0 .Lspin_74%=
.Lok_75%=:
v_bfe_u32 v244, v1, 4, 1
Mvaa 64,0,156
s_sub_u32 s82, s91, 16384
Mvav 96,0,224
Ta 28,25,57344
v_mul_u32_u24 v244, 0xaaaa, v244
Mvav 128,0,228
Ta 30,25,58368
Mvav 132,4,228
s_xor_b32 s83, s92, 0x10000
v_add_u32 v244, 0x4444, v244
Mvav 100,4,224
v_lshl_or_b32 v240, v244, 16, v244
Mvaa 68,4,156
s_add_u32 m0, s83, 0
Ta 0,2,32768
Ta 2,2,33792
Mvaa 36,4,152
W 10
v_mov_b32 v241, 0xff800000
Mvaa 40,8,152
s_add_u32 s81, s82, 0
Mvaa 72,8,156
v_add_u32 v242, 0x10000, v1
Mvav 104,8,224
Dc 1,8,81,0
v_bfe_u32 v244, v1, 4, 4
Mvav 136,8,228
Ta 4,3,32768
Ta 6,3,33792
Mvav 140,12,228
v_lshrrev_b32 v245, 8, v1
Mvav 108,12,224
v_lshlrev_b32 v245, 2, v245
Mvaa 76,12,156
Dc 1,8,81,1024
Ta 8,4,32768
Mvaa 44,12,152
W 9
Ta 10,4,33792
v_sub_u32 v244, v244, v245
Mvaa 48,16,152
Mvaa 80,16,156
v_cmp_le_i32_e64 s[72:73], 0, v244
Mvav 112,16,224
Dc 1,8,81,2048
v_cmp_le_i32_e64 s[74:75], 1, v244
Mvav 144,16,228
Ta 12,5,32768
Ta 14,5,33792
Mvav 148,20,228
v_cmp_le_i32_e64 s[76:77], 2, v244
Mvav 116,20,224
v_cmp_le_i32_e64 s[78:79], 3, v244
Mvaa 84,20,156
Dc 1,8,81,3072
Ta 16,6,32768
Mvaa 52,20,152
W 9
s_add_u32 m0, s83, 4096
s_add_u32 s81, s82, 4096
Mvaa 56,24,152
Ta 18,6,33792
v_add_u32 v243, 4096, v1
Mvaa 88,24,156
v_add_u32 v244, 8192, v1
Mvav 120,24,224
Dc 1,8,81,0
v_add_u32 v245, 12288, v1
Mvav 152,24,228
Ta 20,7,32768
Ta 22,7,33792
Mvav 156,28,228
s_mov_b32 s18, 0x3f803f80
Mvav 124,28,224
s_mov_b32 s19, 0x3f003f00
Mvaa 92,28,156
Dc 1,8,81,1024
Ta 24,8,32768
Mvaa 60,28,152
W 9
Ta 26,8,33792
v_accvgpr_write_b32 a176, s18
Mvav 32,0,200
Mvav 64,0,204
v_accvgpr_write_b32 a180, s19
Mvaa 96,0,32
Dc 1,8,81,2048
v_mov_b32 v252, v241
Mvaa 128,0,36
Ta 28,9,32768
Ta 30,9,33792
Mvaa 132,4,36
v_accvgpr_write_b32 a177, s18
Mvaa 100,4,32
v_accvgpr_write_b32 a181, s19
Mvav 68,4,204
Dc 1,8,81,3072
Ta 0,2,40960
Mvav 36,4,200
W 9
s_add_u32 m0, s83, 8192
s_add_u32 s81, s82, 8192
Mvav 40,8,200
Ta 2,2,41984
v_mov_b32 v253, v241
Mvav 72,8,204
v_accvgpr_write_b32 a178, s18
Mvaa 104,8,32
Dc 1,8,81,0
v_accvgpr_write_b32 a182, s19
Mvaa 136,8,36
Ta 4,3,40960
Ta 6,3,41984
Mvaa 140,12,36
v_mov_b32 v254, v241
Mvaa 108,12,32
v_accvgpr_write_b32 a179, s18
Mvav 76,12,204
Dc 1,8,81,1024
Ta 8,4,40960
Mvav 44,12,200
W 9
Ta 10,4,41984
v_accvgpr_write_b32 a183, s19
Mvav 48,16,200
Mvav 80,16,204
v_mov_b32 v255, v241
Mvaa 112,16,32
Dc 1,8,81,2048
v_accvgpr_write_b32 a160, 0
Mvaa 144,16,36
Ta 12,5,40960
Ta 14,5,41984
Mvaa 148,20,36
v_accvgpr_write_b32 a161, 0
Mvaa 116,20,32
v_accvgpr_write_b32 a162, 0
Mvav 84,20,204
Dc 1,8,81,3072
Ta 16,6,40960
Mvav 52,20,200
W 9
s_add_u32 m0, s83, 12288
s_add_u32 s81, s82, 12288
Mvav 56,24,200
Ta 18,6,41984
v_accvgpr_write_b32 a163, 0
Mvav 88,24,204
v_accvgpr_write_b32 a164, 0
Mvaa 120,24,32
Dc 1,8,81,0
v_accvgpr_write_b32 a165, 0
Mvaa 152,24,36
Ta 20,7,40960
Ta 22,7,41984
Mvaa 156,28,36
v_accvgpr_write_b32 a166, 0
Mvaa 124,28,32
v_accvgpr_write_b32 a167, 0
Mvav 92,28,204
Dc 1,8,81,1024
Ta 24,8,40960
Mvav 60,28,200
W 9
Ta 26,8,41984
v_accvgpr_write_b32 a168, 0
Mvav 32,0,232
Mvav 64,0,236
v_accvgpr_write_b32 a169, 0
Mvaa 96,0,40
Dc 1,8,81,2048
v_accvgpr_write_b32 a170, 0
Mvaa 128,0,44
)"
R"(Ta 28,9,40960
Ta 30,9,41984
Mvaa 132,4,44
v_accvgpr_write_b32 a171, 0
Mvaa 100,4,40
v_accvgpr_write_b32 a172, 0
Mvav 68,4,236
Dc 1,8,81,3072
Ta 0,2,49152
Mvav 36,4,232
W 9
s_add_u32 m0, s92, 0
s_add_u32 s81, s91, 0
Mvav 40,8,232
Ta 2,2,50176
v_accvgpr_write_b32 a173, 0
Mvav 72,8,236
v_accvgpr_write_b32 a174, 0
Mvaa 104,8,40
Dc 1,8,81,0
v_accvgpr_write_b32 a175, 0
Mvaa 136,8,44
Ta 4,3,49152
Ta 6,3,50176
Mvaa 140,12,44
s_mov_b32 s90, 0x5d800000
Mvaa 108,12,40
Mvav 76,12,236
s_mov_b64 s[88:89], 0
Dc 1,8,81,1024
Ta 8,4,49152
Mvav 44,12,232
W 9
s_mov_b32 s64, 0
Ta 10,4,50176
Mvav 48,16,232
Mvav 80,16,236
s_and_b32 s18, s14, 1
Mvaa 112,16,40
s_lshl_b32 s62, s18, 14
Dc 1,8,81,2048
Ta 12,5,49152
Mvaa 144,16,44
Ta 14,5,50176
Mvaa 148,20,44
s_lshl_b32 s20, s57, 15
Mvaa 116,20,40
Mvav 84,20,236
s_sub_u32 s20, 507904, s20
Dc 1,8,81,3072
Ta 16,6,49152
Mvav 52,20,232
W 9
s_add_u32 m0, s92, 4096
s_add_u32 s81, s91, 4096
Mvav 56,24,232
s_add_u32 s20, s20, s62
Ta 18,6,50176
Mvav 88,24,236
s_lshl_b32 s65, s57, 1
Mvaa 120,24,40
s_sub_u32 s65, s65, 1
Dc 1,8,81,0
Ta 20,7,49152
Mvaa 152,24,44
Ta 22,7,50176
Mvaa 156,28,44
s_max_i32 s65, s65, 0
Mvaa 124,28,40
Mvav 92,28,236
s_sub_u32 s66, s60, 1
Dc 1,8,81,1024
Ta 24,8,49152
Mvav 60,28,232
s_waitcnt vmcnt(26) lgkmcnt(9)
s_cmp_lt_u32 s14, 2
s_cselect_b32 s20, s20, 16384
s_cselect_b32 s66, s66, 15
s_cselect_b32 s21, 32768, 0
Ta 26,8,50176
Mvav 32,0,208
Mvav 64,0,212
s_add_u32 s61, s91, s20
Mvaa 96,0,248
s_add_u32 s62, s62, s21
Dc 1,8,81,2048
Ta 28,9,49152
Mvaa 128,0,252
Ta 30,9,50176
Mvaa 132,4,252
s_sub_u32 s63, 16384, s21
Mvaa 100,4,248
Mvav 68,4,212
s_lshl_b32 s67, s60, 7
Dc 1,8,81,3072
Ta 0,2,57344
Mvav 36,4,208
W 9
s_add_u32 m0, s92, 8192
s_add_u32 s81, s91, 8192
Mvav 40,8,208
s_add_u32 s67, s67, s56
Ta 2,2,58368
Mvav 72,8,212
s_lshl_b32 s67, s67, 12
Mvaa 104,8,248
s_add_u32 s67, s67, 39845888
Dc 1,8,81,0
Ta 4,3,57344
Mvaa 136,8,252
Ta 6,3,58368
Mvaa 140,12,252
s_lshl_b32 s69, s60, 3
Mvaa 108,12,248
Mvav 76,12,212
s_add_u32 s69, s69, s59
Dc 1,8,81,1024
Ta 8,4,57344
Mvav 44,12,208
W 9
s_lshl_b32 s69, s69, 6
Ta 10,4,58368
Mvav 48,16,208
Mvav 80,16,212
s_lshl_b32 s21, s58, 3
Mvaa 112,16,248
s_add_u32 s69, s69, s21
Dc 1,8,81,2048
Ta 12,5,57344
Mvaa 144,16,252
Ta 14,5,58368
Mvaa 148,20,252
s_add_u32 s69, s69, 20480
Mvaa 116,20,248
Mvav 84,20,212
s_mov_b32 s70, 0
Dc 1,8,81,3072
Ta 16,6,57344
Mvav 52,20,208
W 9
s_add_u32 m0, s92, 12288
s_add_u32 s81, s91, 12288
Mvav 56,24,208
s_mov_b32 s37, 0
Ta 18,6,58368
Mvav 88,24,212
s_sub_u32 s18, s57, 4
Mvaa 120,24,248
s_lshl_b32 s18, s18, 2
Dc 1,8,81,0
Ta 20,7,57344
Mvaa 152,24,252
Ta 22,7,58368
Mvaa 156,28,252
s_add_u32 s18, s18, s58
Mvaa 124,28,248
Mvav 92,28,212
s_mul_i32 s85, s59, 7
Dc 1,8,81,1024
Ta 24,8,57344
Mvav 60,28,208
s_waitcnt vmcnt(30) lgkmcnt(9)
s_add_u32 s85, s85, s18
Ta 26,8,58368
Mvav 32,0,184
Mvav 64,0,188
s_cmp_lt_u32 s18, 7
s_cselect_b32 s85, s85, 99
Mvaa 96,0,48
s_cmp_lt_u32 s85, 54
s_cselect_b32 s65, 1, s65
Dc 1,8,81,2048
Ta 28,9,57344
Mvaa 128,0,52
Ta 30,9,58368
Mvaa 132,4,52
s_mov_b32 s68, 0
Mvaa 100,4,48
Mvav 68,4,188
s_mov_b64 s[24:25], -1
Dc 1,8,81,3072
Mvav 36,4,184
W 8
s_mov_b32 s22, 0x3e0293ee
Mvav 40,8,184
Mvav 72,8,188
s_mov_b32 s23, 0x3e0293ee
Mvaa 104,8,48
Mvaa 136,8,52
Mvaa 140,12,52
Mvaa 108,12,48
Mvav 76,12,188
Mvav 44,12,184
W 4
Mvav 48,16,184
Mvav 80,16,188
Mvaa 112,16,48
Mvaa 144,16,52
Mvaa 148,20,52
s_waitcnt vmcnt(0) lgkmcnt(0)
s_barrier
Ra 128,242,0
Mvaa 116,20,48
Mvav 84,20,188
Ra 132,242,1024
Mvav 52,20,184
Mvav 56,24,184
Ra 136,242,2048
Mvav 88,24,188
Mvaa 120,24,48
Ra 140,242,3072
Mvaa 152,24,52
Mvaa 156,28,52
Mvaa 124,28,48
Mvav 92,28,188
Mvav 60,28,184
.Lbgo%=:
v_pk_mul_f32 v[32:33], v[32:33], s[22:23]
v_pk_mul_f32 v[34:35], v[34:35], s[22:23]
v_pk_mul_f32 v[36:37], v[36:37], s[22:23]
v_pk_mul_f32 v[38:39], v[38:39], s[22:23]
K 32,32,33
K 33,34,35
K 34,36,37
K 35,38,39
v_pk_mul_f32 v[64:65], v[64:65], s[22:23]
v_pk_mul_f32 v[66:67], v[66:67], s[22:23]
v_pk_mul_f32 v[68:69], v[68:69], s[22:23]
v_pk_mul_f32 v[70:71], v[70:71], s[22:23]
K 64,64,65
K 65,66,67
K 66,68,69
K 67,70,71
v_pk_mul_f32 v[96:97], v[96:97], s[22:23]
v_pk_mul_f32 v[98:99], v[98:99], s[22:23]
v_pk_mul_f32 v[100:101], v[100:101], s[22:23]
v_pk_mul_f32 v[102:103], v[102:103], s[22:23]
K 96,96,97
K 97,98,99
K 98,100,101
K 99,102,103
v_pk_mul_f32 v[128:129], v[128:129], s[22:23]
v_pk_mul_f32 v[130:131], v[130:131], s[22:23]
v_pk_mul_f32 v[132:133], v[132:133], s[22:23]
v_pk_mul_f32 v[134:135], v[134:135], s[22:23]
K 128,128,129
K 129,130,131
K 130,132,133
K 131,134,135
v_pk_mul_f32 v[40:41], v[40:41], s[22:23]
v_pk_mul_f32 v[42:43], v[42:43], s[22:23]
v_pk_mul_f32 v[44:45], v[44:45], s[22:23]
v_pk_mul_f32 v[46:47], v[46:47], s[22:23]
v_pk_mul_f32 v[72:73], v[72:73], s[22:23]
v_pk_mul_f32 v[74:75], v[74:75], s[22:23]
v_pk_mul_f32 v[76:77], v[76:77], s[22:23]
v_pk_mul_f32 v[78:79], v[78:79], s[22:23]
v_pk_mul_f32 v[104:105], v[104:105], s[22:23]
v_pk_mul_f32 v[106:107], v[106:107], s[22:23]
v_pk_mul_f32 v[108:109], v[108:109], s[22:23]
v_pk_mul_f32 v[110:111], v[110:111], s[22:23]
v_pk_mul_f32 v[136:137], v[136:137], s[22:23]
v_pk_mul_f32 v[138:139], v[138:139], s[22:23]
v_pk_mul_f32 v[140:141], v[140:141], s[22:23]
v_pk_mul_f32 v[142:143], v[142:143], s[22:23]
v_pk_mul_f32 v[48:49], v[48:49], s[22:23]
v_pk_mul_f32 v[50:51], v[50:51], s[22:23]
v_pk_mul_f32 v[52:53], v[52:53], s[22:23]
v_pk_mul_f32 v[54:55], v[54:55], s[22:23]
v_pk_mul_f32 v[80:81], v[80:81], s[22:23]
v_pk_mul_f32 v[82:83], v[82:83], s[22:23]
v_pk_mul_f32 v[84:85], v[84:85], s[22:23]
v_pk_mul_f32 v[86:87], v[86:87], s[22:23]
v_pk_mul_f32 v[112:113], v[112:113], s[22:23]
v_pk_mul_f32 v[114:115], v[114:115], s[22:23]
v_pk_mul_f32 v[116:117], v[116:117], s[22:23]
v_pk_mul_f32 v[118:119], v[118:119], s[22:23]
v_pk_mul_f32 v[144:145], v[144:145], s[22:23]
v_pk_mul_f32 v[146:147], v[146:147], s[22:23]
v_pk_mul_f32 v[148:149], v[148:149], s[22:23]
v_pk_mul_f32 v[150:151], v[150:151], s[22:23]
v_pk_mul_f32 v[56:57], v[56:57], s[22:23]
v_pk_mul_f32 v[58:59], v[58:59], s[22:23]
v_pk_mul_f32 v[60:61], v[60:61], s[22:23]
v_pk_mul_f32 v[62:63], v[62:63], s[22:23]
v_pk_mul_f32 v[88:89], v[88:89], s[22:23]
v_pk_mul_f32 v[90:91], v[90:91], s[22:23]
v_pk_mul_f32 v[92:93], v[92:93], s[22:23]
v_pk_mul_f32 v[94:95], v[94:95], s[22:23]
v_pk_mul_f32 v[120:121], v[120:121], s[22:23]
v_pk_mul_f32 v[122:123], v[122:123], s[22:23]
v_pk_mul_f32 v[124:125], v[124:125], s[22:23]
v_pk_mul_f32 v[126:127], v[126:127], s[22:23]
v_pk_mul_f32 v[152:153], v[152:153], s[22:23]
v_pk_mul_f32 v[154:155], v[154:155], s[22:23]
v_pk_mul_f32 v[156:157], v[156:157], s[22:23]
v_pk_mul_f32 v[158:159], v[158:159], s[22:23]
W 0
Zvav 196,128,32
Ra 144,242,4096
Ra 148,242,5120
Ra 152,242,6144
Ra 156,242,7168
Zvav 204,128,64
K 40,40,41
K 41,42,43
K 42,44,45
K 43,46,47
Zvav 212,128,96
K 72,72,73
K 73,74,75
K 74,76,77
K 75,78,79
Zvav 220,128,128
K 104,104,105
K 105,106,107
K 106,108,109
K 107,110,111
K 136,136,137
K 137,138,139
K 138,140,141
K 139,142,143
Mvav 196,132,40
Ra 128,242,8192
K 48,48,49
Mvav 204,132,72
K 49,50,51
K 50,52,53
K 51,54,55
K 80,80,81
Mvav 212,132,104
K 81,82,83
K 82,84,85
K 83,86,87
K 112,112,113
Mvav 220,132,136
K 113,114,115
K 114,116,117
K 115,118,119
K 144,144,145
K 145,146,147
K 146,148,149
K 147,150,151
Mvav 196,136,48
Ra 132,242,9216
K 56,56,57
Mvav 204,136,80
K 57,58,59
K 58,60,61
K 59,62,63
K 88,88,89
Mvav 212,136,112
K 89,90,91
K 90,92,93
K 91,94,95
K 120,120,121
Mvav 220,136,144
K 121,122,123
K 122,124,125
K 123,126,127
K 152,152,153
K 153,154,155
K 154,156,157
K 155,158,159
Mvav 196,140,56
s_add_u32 s80, s62, 131072
Ra 136,242,10240
Mvav 204,140,88
Mvav 212,140,120
v_add_u32 v12, s80, v1
Mvav 220,140,152
W 3
ds_write_b128 v12, v[32:35] offset:0
Zvav 36,144,32
Ra 0,12,0
Ra 140,242,11264
Zvav 68,144,64
v_max3_f32 v4, v196, v197, v198
v_max3_f32 v4, v4, v199, v252
v_max3_f32 v5, v204, v205, v206
v_max3_f32 v5, v5, v207, v253
Zvav 100,144,96
v_max3_f32 v6, v212, v213, v214
v_max3_f32 v6, v6, v215, v254
v_max3_f32 v7, v220, v221, v222
v_max3_f32 v7, v7, v223, v255
Zvav 132,144,128
v_mov_b32 v8, v4
v_mov_b32 v9, v5
v_mov_b32 v10, v6
v_mov_b32 v11, v7
Mvav 36,148,40
ds_write_b128 v12, v[40:43] offset:1024
v_permlane32_swap_b32 v4, v8
v_permlane32_swap_b32 v5, v9
v_permlane32_swap_b32 v6, v10
Mvav 68,148,72
v_permlane32_swap_b32 v7, v11
v_max_f32 v4, v4, v8
v_max_f32 v5, v5, v9
)"
R"(v_max_f32 v6, v6, v10
Mvav 100,148,104
v_max_f32 v7, v7, v11
v_mov_b32 v8, v4
v_mov_b32 v9, v5
v_mov_b32 v10, v6
Mvav 132,148,136
Ra 4,12,1024
v_mov_b32 v11, v7
v_permlane16_swap_b32 v4, v8
v_permlane16_swap_b32 v5, v9
Mvav 36,152,48
v_permlane16_swap_b32 v6, v10
v_permlane16_swap_b32 v7, v11
v_max_f32 v4, v4, v8
v_max_f32 v5, v5, v9
Mvav 68,152,80
v_max_f32 v6, v6, v10
v_max_f32 v7, v7, v11
v_sub_f32 v4, 0, v4
v_sub_f32 v5, 0, v5
Mvav 100,152,112
v_sub_f32 v6, 0, v6
v_sub_f32 v7, 0, v7
v_mov_b32 v224, v4
v_mov_b32 v225, v4
Mvav 132,152,144
v_mov_b32 v226, v4
v_mov_b32 v227, v4
v_mov_b32 v228, v5
v_mov_b32 v229, v5
Mvav 36,156,56
v_mov_b32 v230, v5
v_mov_b32 v231, v5
v_mov_b32 v232, v6
v_mov_b32 v233, v6
Mvav 68,156,88
v_mov_b32 v234, v6
v_mov_b32 v235, v6
v_mov_b32 v236, v7
v_mov_b32 v237, v7
Mvav 100,156,120
ds_write_b128 v12, v[48:51] offset:2048
Ra 144,242,12288
v_mov_b32 v238, v7
v_mov_b32 v239, v7
Mvav 132,156,152
W 4
Ra 8,12,2048
ds_write_b128 v12, v[56:59] offset:3072
Ra 12,12,3072
Ra 148,242,13312
Cvav 160,128,32,224
s_and_b32 s80, s14, 1
ds_write_b128 v12, v[64:67] offset:4096
Ra 16,12,4096
ds_write_b128 v12, v[72:75] offset:5120
Ra 152,242,14336
Cvav 164,128,64,228
s_lshl_b32 s80, s80, 14
Ra 20,12,5120
ds_write_b128 v12, v[80:83] offset:6144
A 196,196,224
A 197,197,224
Cvav 168,128,96,232
s_add_u32 s80, s80, 98304
Ra 24,12,6144
Ra 156,242,15360
A 198,198,224
A 199,199,224
Cvav 172,128,128,236
X 196
X 197
Mvav 160,132,40
ds_write_b128 v12, v[88:91] offset:7168
Ra 28,12,7168
X 198
Mvav 164,132,72
ds_write_b128 v12, v[96:99] offset:8192
K 192,196,197
X 199
Mvav 168,132,104
A 204,204,228
v_add_u32 v8, s80, v1
K 193,198,199
A 205,205,228
Mvav 172,132,136
Ra 128,242,24576
Ra 32,12,8192
A 206,206,228
A 207,207,228
Mvav 160,136,48
ds_write_b128 v8, v[4:7]
ds_write_b128 v12, v[104:107] offset:9216
X 204
Mvav 164,136,80
X 205
X 206
Mvav 168,136,112
Ra 36,12,9216
K 200,204,205
X 207
Mvav 172,136,144
ds_write_b128 v12, v[112:115] offset:10240
Ra 132,242,25600
A 212,212,232
K 201,206,207
Mvav 160,140,56
Ra 40,12,10240
A 213,213,232
A 214,214,232
A 215,215,232
Mvav 164,140,88
ds_write_b128 v12, v[120:123] offset:11264
Ra 44,12,11264
X 212
Mvav 168,140,120
X 213
X 214
Mvav 172,140,152
W 13
Ra 136,242,26624
K 208,212,213
X 215
Cvav 176,144,32,224
ds_write_b128 v12, v[128:131] offset:12288
A 220,220,236
K 209,214,215
A 221,221,236
Cvav 180,144,64,228
Ra 48,12,12288
ds_write_b128 v12, v[136:139] offset:13312
A 222,222,236
A 223,223,236
Cvav 184,144,96,232
X 220
X 221
Cvav 188,144,128,236
Ra 140,242,27648
Ra 52,12,13312
X 222
Mvav 176,148,40
ds_write_b128 v12, v[144:147] offset:14336
K 216,220,221
X 223
Mvav 180,148,72
Ra 56,12,14336
A 36,36,224
K 217,222,223
A 37,37,224
Mvav 184,148,104
Ra 144,242,28672
ds_write_b128 v12, v[152:155] offset:15360
A 38,38,224
A 39,39,224
Mvav 188,148,136
X 36
X 37
Mvav 176,152,48
Ra 60,12,15360
X 38
K 194,36,37
Mvav 180,152,80
Ra 148,242,29696
X 39
A 68,68,228
Mvav 184,152,112
K 195,38,39
A 69,69,228
A 70,70,228
A 71,71,228
Mvav 188,152,144
X 68
X 69
Mvav 176,156,56
Ra 152,242,30720
X 70
K 202,68,69
Mvav 180,156,88
W 3
s_barrier
X 71
A 100,100,232
Mvav 184,156,120
K 203,70,71
A 101,101,232
A 102,102,232
A 103,103,232
X 100
X 101
X 102
K 210,100,101
X 103
A 132,132,236
K 211,102,103
A 133,133,236
A 134,134,236
A 135,135,236
X 132
X 133
X 134
K 218,132,133
X 135
s_nop 0
K 219,134,135
Mvav 188,156,152
Zvav 32,128,192
Ra 156,242,31744
Zvav 64,128,200
Zvav 96,128,208
s_xor_b32 s62, s62, 0x10000
Zvav 128,128,216
v_add_u32 v23, 61440, v21
Zvav 132,132,216
v_add_u32 v24, 61440, v22
Zvav 100,132,208
v_add_u32 v25, 122880, v21
Zvav 68,132,200
v_add_u32 v26, 122880, v22
Zvav 36,132,192
Zvav 40,136,192
X 172
Zvav 72,136,200
X 173
Zvav 104,136,208
X 174
K 220,172,173
Zvav 136,136,216
X 175
Zvav 140,140,216
X 168
K 221,174,175
Zvav 108,140,208
X 169
X 170
Zvav 76,140,200
s_waitcnt vmcnt(0) lgkmcnt(0)
s_barrier
v_xor_b32 v242, 0x10000, v242
K 212,168,169
Zvav 44,140,192
X 171
X 164
Zvav 48,144,192
Ra 128,242,0
K 213,170,171
X 165
Zvav 80,144,200
X 166
Zvav 112,144,208
Ra 132,242,1024
K 204,164,165
X 167
Zvav 144,144,216
X 160
Zvav 148,148,216
K 205,166,167
X 161
Zvav 116,148,208
Ra 136,242,2048
X 162
Zvav 84,148,200
K 196,160,161
X 163
Zvav 52,148,192
X 176
K 197,162,163
Zvav 56,152,192
Ra 140,242,3072
X 177
Zvav 88,152,200
X 178
K 198,176,177
Zvav 120,152,208
X 179
X 180
Zvav 152,152,216
K 199,178,179
Zvav 156,156,216
X 181
X 182
Zvav 124,156,208
K 206,180,181
X 183
Zvav 92,156,200
K 207,182,183
Zvav 60,156,192
s_cmp_eq_u64 s[24:25], 0
s_cbranch_scc1 .Ll1_33%=
W 0
Cvaa 160,128,0,224
Ra 144,242,4096
Ra 148,242,5120
Cvaa 164,128,16,228
s_add_u32 m0, s62, 0
Ra 152,242,6144
Cvaa 168,128,32,232
X 184
Cvaa 172,128,48,236
X 185
Mvaa 160,132,4
X 186
Mvaa 164,132,20
Ra 156,242,7168
K 214,184,185
Mvaa 168,132,36
Dc 1,8,61,0
Mvaa 172,132,52
X 187
Mvaa 160,136,8
X 188
Mvaa 164,136,24
Ra 128,242,16384
K 215,186,187
Mvaa 168,136,40
Dc 1,8,61,1024
Mvaa 172,136,56
X 189
Mvaa 160,140,12
X 190
Mvaa 164,140,28
Ra 132,242,17408
K 222,188,189
Mvaa 168,140,44
Dc 1,8,61,2048
Mvaa 172,140,60
X 191
v_smfmac_f32_16x16x64_bf16 a[160:163], a[176:179], v[192:199], v240
Ra 136,242,18432
K 223,190,191
v_smfmac_f32_16x16x64_bf16 a[164:167], a[176:179], v[200:207], v240
X 160
v_smfmac_f32_16x16x64_bf16 a[168:171], a[176:179], v[208:215], v240
Dc 1,8,61,3072
v_smfmac_f32_16x16x64_bf16 a[172:175], a[176:179], v[216:223], v240
W 3
X 161
Cvaa 176,144,0,224
s_add_u32 m0, s62, 4096
X 162
Cvaa 180,144,16,228
Ra 140,242,19456
K 192,160,161
Cvaa 184,144,32,232
X 163
Cvaa 188,144,48,236
Dc 243,8,61,0
Mvaa 176,148,4
X 164
Mvaa 180,148,20
Ra 144,242,20480
K 193,162,163
Mvaa 184,148,36
X 165
Mvaa 188,148,52
Dc 243,8,61,1024
Mvaa 176,152,8
X 166
Mvaa 180,152,24
Ra 148,242,21504
K 200,164,165
Mvaa 184,152,40
X 167
Mvaa 188,152,56
Dc 243,8,61,2048
Mvaa 176,156,12
X 168
Mvaa 180,156,28
Ra 152,242,22528
K 201,166,167
Mvaa 184,156,44
X 169
Mvaa 188,156,60
W 3
Dc 243,8,61,3072
Mvav 32,128,196
s_add_u32 m0, s62, 8192
X 170
Mvav 64,128,204
Ra 156,242,23552
K 208,168,169
Mvav 96,128,212
X 171
Mvav 128,128,220
Dc 244,8,61,0
Mvav 132,132,220
X 172
Mvav 100,132,212
Ra 128,242,8192
K 209,170,171
Mvav 68,132,204
X 173
Mvav 36,132,196
Dc 244,8,61,1024
Mvav 40,136,196
X 174
Mvav 72,136,204
Ra 132,242,9216
K 216,172,173
Mvav 104,136,212
)"
R"(X 175
Mvav 136,136,220
Dc 244,8,61,2048
Mvav 140,140,220
X 176
Mvav 108,140,212
Ra 136,242,10240
K 217,174,175
Mvav 76,140,204
X 177
Mvav 44,140,196
W 3
Dc 244,8,61,3072
Mvav 48,144,196
X 178
Mvav 80,144,204
s_add_u32 m0, s62, 12288
Ra 140,242,11264
Mvav 112,144,212
K 194,176,177
Mvav 144,144,220
X 179
Mvav 148,148,220
X 180
Mvav 116,148,212
Dc 245,8,61,0
Ra 144,242,12288
Mvav 84,148,204
K 195,178,179
Mvav 52,148,196
X 181
Mvav 56,152,196
X 182
Mvav 88,152,204
Dc 245,8,61,1024
Ra 148,242,13312
Mvav 120,152,212
K 202,180,181
Mvav 152,152,220
X 183
Mvav 156,156,220
X 184
Mvav 124,156,212
Dc 245,8,61,2048
Ra 152,242,14336
Mvav 92,156,204
K 203,182,183
Mvav 60,156,196
W 3
X 185
Cvaa 160,128,0,224
X 186
Cvaa 164,128,16,228
Dc 245,8,61,3072
Ra 156,242,15360
Cvaa 168,128,32,232
s_add_u32 s61, s61, s63
K 210,184,185
Cvaa 172,128,48,236
X 187
Mvaa 160,132,4
X 188
Mvaa 164,132,20
Ra 128,242,24576
Mvaa 168,132,36
s_xor_b32 s62, s62, 0x10000
K 211,186,187
Mvaa 172,132,52
X 189
Mvaa 160,136,8
X 190
Mvaa 164,136,24
s_add_u32 s64, s64, 1
Ra 132,242,25600
Mvaa 168,136,40
K 218,188,189
Mvaa 172,136,56
X 191
Mvaa 160,140,12
Ra 136,242,26624
Mvaa 164,140,28
Mvaa 168,140,44
s_add_u32 s36, s29, 131072
Mvaa 172,140,60
W 3
Cvaa 176,144,0,224
Ra 140,242,27648
Cvaa 180,144,16,228
X 160
Cvaa 184,144,32,232
K 219,190,191
Cvaa 188,144,48,236
X 161
Mvaa 176,148,4
X 162
Mvaa 180,148,20
buffer_store_dwordx4 a[124:127], v1, s[8:11], s36 offen offset:0 sc1
K 196,160,161
Mvaa 184,148,36
X 163
Mvaa 188,148,52
Ra 144,242,28672
Mvaa 176,152,8
X 164
Mvaa 180,152,24
Ra 148,242,29696
K 197,162,163
Mvaa 184,152,40
X 165
Mvaa 188,152,56
X 166
Mvaa 176,156,12
buffer_store_dwordx4 a[116:119], v1, s[8:11], s36 offen offset:1024 sc1
K 204,164,165
Mvaa 180,156,28
X 167
Mvaa 184,156,44
Ra 152,242,30720
Mvaa 188,156,60
W 3
X 168
Mvav 32,128,192
buffer_store_dwordx4 a[108:111], v1, s[8:11], s36 offen offset:2048 sc1
K 205,166,167
Mvav 64,128,200
X 169
Mvav 96,128,208
Ra 156,242,31744
Mvav 128,128,216
X 170
Mvav 132,132,216
K 212,168,169
Mvav 100,132,208
X 171
Mvav 68,132,200
X 172
Mvav 36,132,192
buffer_store_dwordx4 a[188:191], v1, s[8:11], s36 offen offset:3072 sc1
K 213,170,171
Mvav 40,136,192
X 173
Mvav 72,136,200
X 174
Mvav 104,136,208
Mvav 136,136,216
s_add_u32 s36, s29, 135168
K 220,172,173
Mvav 140,140,216
X 175
Mvav 108,140,208
X 176
Mvav 76,140,200
s_waitcnt vmcnt(4) lgkmcnt(0)
s_barrier
v_xor_b32 v242, 0x10000, v242
Mvav 44,140,192
Ra 128,242,0
K 221,174,175
Mvav 48,144,192
X 177
Mvav 80,144,200
buffer_store_dwordx4 a[196:199], v1, s[8:11], s36 offen offset:0 sc1
Mvav 112,144,208
X 178
Mvav 144,144,216
Ra 132,242,1024
K 198,176,177
Mvav 148,148,216
X 179
Mvav 116,148,208
X 180
Mvav 84,148,200
Ra 136,242,2048
Mvav 52,148,192
buffer_store_dwordx4 a[220:223], v1, s[8:11], s36 offen offset:1024 sc1
K 199,178,179
Mvav 56,152,192
X 181
Mvav 88,152,200
Ra 140,242,3072
Mvav 120,152,208
X 182
Mvav 152,152,216
K 206,180,181
Mvav 156,156,216
X 183
Mvav 124,156,208
Mvav 92,156,200
K 207,182,183
Mvav 60,156,192
W 0
Cvaa 160,128,0,224
Ra 144,242,4096
Ra 148,242,5120
Cvaa 164,128,16,228
s_add_u32 m0, s62, 0
Ra 152,242,6144
Cvaa 168,128,32,232
X 184
Cvaa 172,128,48,236
X 185
Mvaa 160,132,4
X 186
Mvaa 164,132,20
Ra 156,242,7168
K 214,184,185
Mvaa 168,132,36
Dc 1,8,61,0
Mvaa 172,132,52
X 187
Mvaa 160,136,8
X 188
Mvaa 164,136,24
Ra 128,242,16384
K 215,186,187
Mvaa 168,136,40
Dc 1,8,61,1024
Mvaa 172,136,56
X 189
Mvaa 160,140,12
X 190
Mvaa 164,140,28
Ra 132,242,17408
K 222,188,189
Mvaa 168,140,44
Dc 1,8,61,2048
Mvaa 172,140,60
X 191
v_smfmac_f32_16x16x64_bf16 a[160:163], a[176:179], v[192:199], v240
Ra 136,242,18432
K 223,190,191
v_smfmac_f32_16x16x64_bf16 a[164:167], a[176:179], v[200:207], v240
X 160
v_smfmac_f32_16x16x64_bf16 a[168:171], a[176:179], v[208:215], v240
Dc 1,8,61,3072
v_smfmac_f32_16x16x64_bf16 a[172:175], a[176:179], v[216:223], v240
W 3
X 161
Cvaa 176,144,0,224
s_add_u32 m0, s62, 4096
X 162
Cvaa 180,144,16,228
Ra 140,242,19456
K 192,160,161
Cvaa 184,144,32,232
X 163
Cvaa 188,144,48,236
Dc 243,8,61,0
Mvaa 176,148,4
X 164
Mvaa 180,148,20
Ra 144,242,20480
K 193,162,163
Mvaa 184,148,36
X 165
Mvaa 188,148,52
Dc 243,8,61,1024
Mvaa 176,152,8
X 166
Mvaa 180,152,24
Ra 148,242,21504
K 200,164,165
Mvaa 184,152,40
X 167
Mvaa 188,152,56
Dc 243,8,61,2048
Mvaa 176,156,12
X 168
Mvaa 180,156,28
Ra 152,242,22528
K 201,166,167
Mvaa 184,156,44
X 169
Mvaa 188,156,60
W 3
Dc 243,8,61,3072
Mvav 32,128,196
s_add_u32 m0, s62, 8192
X 170
Mvav 64,128,204
Ra 156,242,23552
K 208,168,169
Mvav 96,128,212
X 171
Mvav 128,128,220
Dc 244,8,61,0
Mvav 132,132,220
X 172
Mvav 100,132,212
Ra 128,242,8192
K 209,170,171
Mvav 68,132,204
X 173
Mvav 36,132,196
Dc 244,8,61,1024
Mvav 40,136,196
X 174
Mvav 72,136,204
Ra 132,242,9216
K 216,172,173
Mvav 104,136,212
X 175
Mvav 136,136,220
Dc 244,8,61,2048
Mvav 140,140,220
X 176
Mvav 108,140,212
Ra 136,242,10240
K 217,174,175
Mvav 76,140,204
X 177
Mvav 44,140,196
W 3
Dc 244,8,61,3072
Mvav 48,144,196
X 178
Mvav 80,144,204
s_add_u32 m0, s62, 12288
Ra 140,242,11264
Mvav 112,144,212
K 194,176,177
Mvav 144,144,220
X 179
Mvav 148,148,220
X 180
Mvav 116,148,212
Dc 245,8,61,0
Ra 144,242,12288
Mvav 84,148,204
K 195,178,179
Mvav 52,148,196
X 181
Mvav 56,152,196
X 182
Mvav 88,152,204
Dc 245,8,61,1024
Ra 148,242,13312
Mvav 120,152,212
K 202,180,181
Mvav 152,152,220
X 183
Mvav 156,156,220
X 184
Mvav 124,156,212
Dc 245,8,61,2048
Ra 152,242,14336
Mvav 92,156,204
K 203,182,183
v_bfe_u32 v28, v1, 4, 4
Mvav 60,156,196
W 3
X 185
Cvaa 160,128,0,224
Ra 156,242,15360
v_lshlrev_b32 v28, 3, v28
Cvaa 164,128,16,228
s_lshl_b32 s80, s56, 9
s_add_u32 s80, s80, 4224
Dc 245,8,61,3072
Cvaa 168,128,32,232
s_add_u32 s61, s61, s63
X 186
Cvaa 172,128,48,236
K 210,184,185
Mvaa 160,132,4
X 187
Mvaa 164,132,20
buffer_load_dwordx2 v[28:29], v28, s[8:11], s80 offen sc1
Ra 128,242,24576
Mvaa 168,132,36
s_xor_b32 s62, s62, 0x10000
X 188
Mvaa 172,132,52
K 211,186,187
Mvaa 160,136,8
X 189
Mvaa 164,136,24
s_add_u32 s64, s64, 1
X 190
Mvaa 168,136,40
Ra 132,242,25600
K 218,188,189
Mvaa 172,136,56
X 191
Mvaa 160,140,12
Ra 136,242,26624
Mvaa 164,140,28
Mvaa 168,140,44
buffer_store_dwordx4 a[228:231], v1, s[8:11], s36 offen offset:2048 sc1
Mvaa 172,140,60
W 3
Cvaa 176,144,0,224
Ra 140,242,27648
Cvaa 180,144,16,228
X 160
Cvaa 184,144,32,232
K 219,190,191
Cvaa 188,144,48,236
X 161
Mvaa 176,148,4
)"
R"(X 162
Mvaa 180,148,20
Ra 144,242,28672
K 196,160,161
Mvaa 184,148,36
X 163
Mvaa 188,148,52
X 164
Mvaa 176,152,8
buffer_store_dwordx4 a[76:79], v1, s[8:11], s36 offen offset:3072 sc1
K 197,162,163
Mvaa 180,152,24
X 165
Mvaa 184,152,40
Ra 148,242,29696
Mvaa 188,152,56
X 166
Mvaa 176,156,12
s_add_u32 s36, s29, 327680
Ra 152,242,30720
K 204,164,165
Mvaa 180,156,28
X 167
Mvaa 184,156,44
X 168
Mvaa 188,156,60
W 3
K 205,166,167
Mvav 32,128,192
X 169
Mvav 64,128,200
Ra 156,242,31744
Mvav 96,128,208
X 170
Mvav 128,128,216
buffer_store_dwordx4 a[84:87], v1, s[8:11], s36 offen offset:0 sc1
K 212,168,169
Mvav 132,132,216
X 171
Mvav 100,132,208
X 172
Mvav 68,132,200
Mvav 36,132,192
K 213,170,171
Mvav 40,136,192
X 173
Mvav 72,136,200
buffer_store_dwordx4 a[92:95], v1, s[8:11], s36 offen offset:1024 sc1
Mvav 104,136,208
X 174
Mvav 136,136,216
s_waitcnt vmcnt(4)
v_cmp_eq_u64 vcc, s[12:13], v[28:29]
s_cmp_eq_u64 vcc, -1
s_cbranch_scc0 .Lspin_77%=
.Lok_78%=:
K 220,172,173
Mvav 140,140,216
X 175
Mvav 108,140,208
X 176
Mvav 76,140,200
W 0
s_barrier
v_xor_b32 v242, 0x10000, v242
Mvav 44,140,192
buffer_store_dwordx4 a[204:207], v1, s[8:11], s36 offen offset:2048 sc1
K 221,174,175
Mvav 48,144,192
Ra 128,242,0
Mvav 80,144,200
X 177
Mvav 112,144,208
X 178
Mvav 144,144,216
Ra 132,242,1024
K 198,176,177
Mvav 148,148,216
X 179
Mvav 116,148,208
X 180
Mvav 84,148,200
Ra 136,242,2048
buffer_store_dwordx4 a[212:215], v1, s[8:11], s36 offen offset:3072 sc1
Mvav 52,148,192
K 199,178,179
Mvav 56,152,192
X 181
Mvav 88,152,200
Ra 140,242,3072
Mvav 120,152,208
X 182
Mvav 152,152,216
K 206,180,181
Mvav 156,156,216
X 183
Mvav 124,156,208
Mvav 92,156,200
K 207,182,183
Mvav 60,156,192
W 0
Cvaa 160,128,0,224
Ra 144,242,4096
Ra 148,242,5120
Cvaa 164,128,16,228
s_add_u32 m0, s62, 0
Ra 152,242,6144
Cvaa 168,128,32,232
X 184
Cvaa 172,128,48,236
X 185
Mvaa 160,132,4
X 186
Mvaa 164,132,20
Ra 156,242,7168
K 214,184,185
Mvaa 168,132,36
Dc 1,8,61,0
Mvaa 172,132,52
X 187
Mvaa 160,136,8
X 188
Mvaa 164,136,24
Ra 128,242,16384
K 215,186,187
Mvaa 168,136,40
Dc 1,8,61,1024
Mvaa 172,136,56
X 189
Mvaa 160,140,12
X 190
Mvaa 164,140,28
Ra 132,242,17408
K 222,188,189
Mvaa 168,140,44
Dc 1,8,61,2048
Mvaa 172,140,60
X 191
v_smfmac_f32_16x16x64_bf16 a[160:163], a[176:179], v[192:199], v240
Ra 136,242,18432
K 223,190,191
v_smfmac_f32_16x16x64_bf16 a[164:167], a[176:179], v[200:207], v240
X 160
v_smfmac_f32_16x16x64_bf16 a[168:171], a[176:179], v[208:215], v240
Dc 1,8,61,3072
v_smfmac_f32_16x16x64_bf16 a[172:175], a[176:179], v[216:223], v240
W 3
X 161
Cvaa 176,144,0,224
s_add_u32 m0, s62, 4096
X 162
Cvaa 180,144,16,228
Ra 140,242,19456
K 192,160,161
Cvaa 184,144,32,232
X 163
Cvaa 188,144,48,236
Dc 243,8,61,0
Mvaa 176,148,4
X 164
Mvaa 180,148,20
Ra 144,242,20480
K 193,162,163
Mvaa 184,148,36
X 165
Mvaa 188,148,52
Dc 243,8,61,1024
Mvaa 176,152,8
X 166
Mvaa 180,152,24
Ra 148,242,21504
K 200,164,165
Mvaa 184,152,40
X 167
Mvaa 188,152,56
Dc 243,8,61,2048
Mvaa 176,156,12
X 168
Mvaa 180,156,28
Ra 152,242,22528
K 201,166,167
Mvaa 184,156,44
X 169
Mvaa 188,156,60
W 3
Dc 243,8,61,3072
Mvav 32,128,196
s_add_u32 m0, s62, 8192
X 170
Mvav 64,128,204
Ra 156,242,23552
K 208,168,169
Mvav 96,128,212
X 171
Mvav 128,128,220
Dc 244,8,61,0
Mvav 132,132,220
X 172
Mvav 100,132,212
Ra 128,242,8192
K 209,170,171
Mvav 68,132,204
X 173
Mvav 36,132,196
Dc 244,8,61,1024
Mvav 40,136,196
X 174
Mvav 72,136,204
Ra 132,242,9216
K 216,172,173
Mvav 104,136,212
X 175
Mvav 136,136,220
Dc 244,8,61,2048
Mvav 140,140,220
X 176
Mvav 108,140,212
Ra 136,242,10240
K 217,174,175
Mvav 76,140,204
X 177
Mvav 44,140,196
W 3
Dc 244,8,61,3072
Mvav 48,144,196
X 178
Mvav 80,144,204
s_add_u32 m0, s62, 12288
Ra 140,242,11264
Mvav 112,144,212
K 194,176,177
Mvav 144,144,220
X 179
Mvav 148,148,220
X 180
Mvav 116,148,212
Dc 245,8,61,0
Ra 144,242,12288
Mvav 84,148,204
K 195,178,179
Mvav 52,148,196
X 181
Mvav 56,152,196
X 182
Mvav 88,152,204
Dc 245,8,61,1024
Ra 148,242,13312
Mvav 120,152,212
K 202,180,181
Mvav 152,152,220
X 183
Mvav 156,156,220
X 184
Mvav 124,156,212
Dc 245,8,61,2048
Ra 152,242,14336
Mvav 92,156,204
K 203,182,183
Mvav 60,156,196
W 3
X 185
Cvaa 160,128,0,224
X 186
Cvaa 164,128,16,228
Dc 245,8,61,3072
Ra 156,242,15360
Cvaa 168,128,32,232
s_add_u32 s61, s61, s63
K 210,184,185
Cvaa 172,128,48,236
X 187
Mvaa 160,132,4
X 188
Mvaa 164,132,20
Ra 128,242,24576
Mvaa 168,132,36
s_xor_b32 s62, s62, 0x10000
K 211,186,187
Mvaa 172,132,52
X 189
Mvaa 160,136,8
X 190
Mvaa 164,136,24
s_add_u32 s64, s64, 1
Ra 132,242,25600
Mvaa 168,136,40
K 218,188,189
Mvaa 172,136,56
X 191
Mvaa 160,140,12
Ra 136,242,26624
Mvaa 164,140,28
Mvaa 168,140,44
s_add_u32 s36, s29, 331776
Mvaa 172,140,60
W 3
Cvaa 176,144,0,224
Ra 140,242,27648
Cvaa 180,144,16,228
X 160
Cvaa 184,144,32,232
K 219,190,191
Cvaa 188,144,48,236
X 161
Mvaa 176,148,4
X 162
Mvaa 180,148,20
buffer_store_dwordx4 a[236:239], v1, s[8:11], s36 offen offset:0 sc1
K 196,160,161
Mvaa 184,148,36
X 163
Mvaa 188,148,52
Ra 144,242,28672
Mvaa 176,152,8
X 164
Mvaa 180,152,24
Ra 148,242,29696
K 197,162,163
Mvaa 184,152,40
X 165
Mvaa 188,152,56
X 166
Mvaa 176,156,12
buffer_store_dwordx4 a[244:247], v1, s[8:11], s36 offen offset:1024 sc1
K 204,164,165
Mvaa 180,156,28
X 167
Mvaa 184,156,44
Ra 152,242,30720
Mvaa 188,156,60
W 3
X 168
Mvav 32,128,192
buffer_store_dwordx4 a[68:71], v1, s[8:11], s36 offen offset:2048 sc1
K 205,166,167
Mvav 64,128,200
X 169
Mvav 96,128,208
Ra 156,242,31744
Mvav 128,128,216
X 170
Mvav 132,132,216
K 212,168,169
Mvav 100,132,208
X 171
Mvav 68,132,200
X 172
Mvav 36,132,192
buffer_store_dwordx4 a[184:187], v1, s[8:11], s36 offen offset:3072 sc1
K 213,170,171
Mvav 40,136,192
X 173
Mvav 72,136,200
X 174
Mvav 104,136,208
Mvav 136,136,216
s_add_u32 s36, s29, 196608
K 220,172,173
Mvav 140,140,216
X 175
Mvav 108,140,208
X 176
Mvav 76,140,200
s_waitcnt vmcnt(4) lgkmcnt(0)
s_barrier
v_xor_b32 v242, 0x10000, v242
Mvav 44,140,192
Ra 128,242,0
K 221,174,175
Mvav 48,144,192
X 177
Mvav 80,144,200
buffer_store_dwordx4 a[192:195], v1, s[8:11], s36 offen offset:0 sc1
Mvav 112,144,208
X 178
Mvav 144,144,216
Ra 132,242,1024
K 198,176,177
Mvav 148,148,216
X 179
Mvav 116,148,208
X 180
Mvav 84,148,200
Ra 136,242,2048
Mvav 52,148,192
buffer_store_dwordx4 a[200:203], v1, s[8:11], s36 offen offset:1024 sc1
K 199,178,179
Mvav 56,152,192
X 181
Mvav 88,152,200
Ra 140,242,3072
Mvav 120,152,208
X 182
Mvav 152,152,216
K 206,180,181
Mvav 156,156,216
X 183
Mvav 124,156,208
Mvav 92,156,200
K 207,182,183
)"
R"(Mvav 60,156,192
W 0
Cvaa 160,128,0,224
Ra 144,242,4096
Ra 148,242,5120
Cvaa 164,128,16,228
s_add_u32 m0, s62, 0
Ra 152,242,6144
Cvaa 168,128,32,232
X 184
Cvaa 172,128,48,236
X 185
Mvaa 160,132,4
X 186
Mvaa 164,132,20
Ra 156,242,7168
K 214,184,185
Mvaa 168,132,36
Dc 1,8,61,0
Mvaa 172,132,52
X 187
Mvaa 160,136,8
X 188
Mvaa 164,136,24
Ra 128,242,16384
K 215,186,187
Mvaa 168,136,40
Dc 1,8,61,1024
Mvaa 172,136,56
X 189
Mvaa 160,140,12
X 190
Mvaa 164,140,28
Ra 132,242,17408
K 222,188,189
Mvaa 168,140,44
Dc 1,8,61,2048
Mvaa 172,140,60
X 191
v_smfmac_f32_16x16x64_bf16 a[160:163], a[176:179], v[192:199], v240
Ra 136,242,18432
K 223,190,191
v_smfmac_f32_16x16x64_bf16 a[164:167], a[176:179], v[200:207], v240
X 160
v_smfmac_f32_16x16x64_bf16 a[168:171], a[176:179], v[208:215], v240
Dc 1,8,61,3072
v_smfmac_f32_16x16x64_bf16 a[172:175], a[176:179], v[216:223], v240
W 3
X 161
Cvaa 176,144,0,224
s_add_u32 m0, s62, 4096
X 162
Cvaa 180,144,16,228
Ra 140,242,19456
K 192,160,161
Cvaa 184,144,32,232
X 163
Cvaa 188,144,48,236
Dc 243,8,61,0
Mvaa 176,148,4
X 164
Mvaa 180,148,20
Ra 144,242,20480
K 193,162,163
Mvaa 184,148,36
X 165
Mvaa 188,148,52
Dc 243,8,61,1024
Mvaa 176,152,8
X 166
Mvaa 180,152,24
Ra 148,242,21504
K 200,164,165
Mvaa 184,152,40
X 167
Mvaa 188,152,56
Dc 243,8,61,2048
Mvaa 176,156,12
X 168
Mvaa 180,156,28
Ra 152,242,22528
K 201,166,167
Mvaa 184,156,44
X 169
Mvaa 188,156,60
W 3
Dc 243,8,61,3072
Mvav 32,128,196
s_add_u32 m0, s62, 8192
X 170
Mvav 64,128,204
Ra 156,242,23552
K 208,168,169
Mvav 96,128,212
X 171
Mvav 128,128,220
Dc 244,8,61,0
Mvav 132,132,220
X 172
Mvav 100,132,212
Ra 128,242,8192
K 209,170,171
Mvav 68,132,204
X 173
Mvav 36,132,196
Dc 244,8,61,1024
Mvav 40,136,196
X 174
Mvav 72,136,204
Ra 132,242,9216
K 216,172,173
Mvav 104,136,212
X 175
Mvav 136,136,220
Dc 244,8,61,2048
Mvav 140,140,220
X 176
Mvav 108,140,212
Ra 136,242,10240
K 217,174,175
Mvav 76,140,204
X 177
Mvav 44,140,196
W 3
Dc 244,8,61,3072
Mvav 48,144,196
X 178
Mvav 80,144,204
s_add_u32 m0, s62, 12288
Ra 140,242,11264
Mvav 112,144,212
K 194,176,177
Mvav 144,144,220
X 179
Mvav 148,148,220
X 180
Mvav 116,148,212
Dc 245,8,61,0
Ra 144,242,12288
Mvav 84,148,204
K 195,178,179
Mvav 52,148,196
X 181
Mvav 56,152,196
X 182
Mvav 88,152,204
Dc 245,8,61,1024
Ra 148,242,13312
Mvav 120,152,212
K 202,180,181
Mvav 152,152,220
X 183
Mvav 156,156,220
s_waitcnt vmcnt(16)
s_add_u32 s80, s71, 256
v_mov_b64 v[28:29], s[12:13]
s_mov_b64 exec, 1
buffer_store_dwordx2 v[28:29], off, s[8:11], s80 sc1
s_mov_b64 exec, -1
X 184
Mvav 124,156,212
Ra 152,242,14336
Mvav 92,156,204
K 203,182,183
Mvav 60,156,196
W 3
X 185
Cvaa 160,128,0,224
Dc 245,8,61,2048
Ra 156,242,15360
Cvaa 164,128,16,228
X 186
Cvaa 168,128,32,232
K 210,184,185
Cvaa 172,128,48,236
X 187
Mvaa 160,132,4
Dc 245,8,61,3072
Ra 128,242,24576
Mvaa 164,132,20
s_add_u32 s61, s61, s63
X 188
Mvaa 168,132,36
s_xor_b32 s62, s62, 0x10000
K 211,186,187
Mvaa 172,132,52
X 189
Mvaa 160,136,8
X 190
Mvaa 164,136,24
s_add_u32 s64, s64, 1
Ra 132,242,25600
Mvaa 168,136,40
K 218,188,189
Mvaa 172,136,56
X 191
Mvaa 160,140,12
Ra 136,242,26624
Mvaa 164,140,28
Mvaa 168,140,44
buffer_store_dwordx4 a[208:211], v1, s[8:11], s36 offen offset:2048 sc1
Mvaa 172,140,60
W 3
Cvaa 176,144,0,224
Ra 140,242,27648
Cvaa 180,144,16,228
X 160
Cvaa 184,144,32,232
buffer_store_dwordx4 a[216:219], v1, s[8:11], s36 offen offset:3072 sc1
K 219,190,191
Cvaa 188,144,48,236
X 161
Mvaa 176,148,4
s_add_u32 s36, s29, 200704
X 162
Mvaa 180,148,20
Ra 144,242,28672
K 196,160,161
Mvaa 184,148,36
X 163
Mvaa 188,148,52
X 164
Mvaa 176,152,8
buffer_store_dwordx4 a[224:227], v1, s[8:11], s36 offen offset:0 sc1
K 197,162,163
Mvaa 180,152,24
X 165
Mvaa 184,152,40
Ra 148,242,29696
Mvaa 188,152,56
X 166
Mvaa 176,156,12
buffer_store_dwordx4 a[232:235], v1, s[8:11], s36 offen offset:1024 sc1
K 204,164,165
Mvaa 180,156,28
X 167
Mvaa 184,156,44
Ra 152,242,30720
Mvaa 188,156,60
W 3
X 168
Mvav 32,128,192
buffer_store_dwordx4 a[240:243], v1, s[8:11], s36 offen offset:2048 sc1
K 205,166,167
Mvav 64,128,200
X 169
Mvav 96,128,208
Ra 156,242,31744
Mvav 128,128,216
X 170
Mvav 132,132,216
buffer_store_dwordx4 a[100:103], v1, s[8:11], s36 offen offset:3072 sc1
K 212,168,169
Mvav 100,132,208
s_lshr_b32 s81, s15, 3
s_lshl_b32 s82, s59, 5
s_add_u32 s81, s81, s82
Mvav 68,132,200
X 171
Mvav 36,132,192
X 172
Mvav 40,136,192
s_lshl_b32 s81, s81, 1
K 213,170,171
Mvav 72,136,200
X 173
Mvav 104,136,208
X 174
Mvav 136,136,216
s_add_u32 s81, s81, s14
K 220,172,173
Mvav 140,140,216
X 175
Mvav 108,140,208
s_sub_u32 s81, s81, 2
X 176
Mvav 76,140,200
s_waitcnt vmcnt(6) lgkmcnt(0)
s_barrier
v_xor_b32 v242, 0x10000, v242
Mvav 44,140,192
Ra 128,242,0
K 221,174,175
Mvav 48,144,192
s_mul_i32 s81, s81, 10240
X 177
Mvav 80,144,200
X 178
Mvav 112,144,208
Ra 132,242,1024
v_min_u32 v2, 144, v1
Mvav 144,144,216
K 198,176,177
Mvav 148,148,216
X 179
Mvav 116,148,208
Ra 136,242,2048
v_lshl_add_u32 v2, v2, 3, s81
Mvav 84,148,200
X 180
Mvav 52,148,192
global_load_dword v3, v2, s[26:27]
K 199,178,179
Mvav 56,152,192
X 181
Mvav 88,152,200
Ra 140,242,3072
Mvav 120,152,208
X 182
Mvav 152,152,216
v_add_u32 v2, 1280, v2
K 206,180,181
Mvav 156,156,216
X 183
Mvav 124,156,208
Mvav 92,156,200
K 207,182,183
Mvav 60,156,192
W 0
Cvaa 160,128,0,224
Ra 144,242,4096
Ra 148,242,5120
Cvaa 164,128,16,228
s_add_u32 m0, s62, 0
Ra 152,242,6144
Cvaa 168,128,32,232
X 184
Cvaa 172,128,48,236
X 185
Mvaa 160,132,4
X 186
Mvaa 164,132,20
Ra 156,242,7168
K 214,184,185
Mvaa 168,132,36
Dc 1,8,61,0
Mvaa 172,132,52
X 187
Mvaa 160,136,8
X 188
Mvaa 164,136,24
Ra 128,242,16384
K 215,186,187
Mvaa 168,136,40
Dc 1,8,61,1024
Mvaa 172,136,56
X 189
Mvaa 160,140,12
X 190
Mvaa 164,140,28
Ra 132,242,17408
K 222,188,189
Mvaa 168,140,44
Dc 1,8,61,2048
Mvaa 172,140,60
X 191
v_smfmac_f32_16x16x64_bf16 a[160:163], a[176:179], v[192:199], v240
Ra 136,242,18432
K 223,190,191
v_smfmac_f32_16x16x64_bf16 a[164:167], a[176:179], v[200:207], v240
X 160
v_smfmac_f32_16x16x64_bf16 a[168:171], a[176:179], v[208:215], v240
Dc 1,8,61,3072
v_smfmac_f32_16x16x64_bf16 a[172:175], a[176:179], v[216:223], v240
W 3
X 161
Cvaa 176,144,0,224
s_add_u32 m0, s62, 4096
X 162
Cvaa 180,144,16,228
Ra 140,242,19456
K 192,160,161
Cvaa 184,144,32,232
X 163
Cvaa 188,144,48,236
Dc 243,8,61,0
Mvaa 176,148,4
X 164
Mvaa 180,148,20
Ra 144,242,20480
K 193,162,163
Mvaa 184,148,36
X 165
Mvaa 188,148,52
Dc 243,8,61,1024
Mvaa 176,152,8
X 166
Mvaa 180,152,24
Ra 148,242,21504
K 200,164,165
Mvaa 184,152,40
X 167
Mvaa 188,152,56
Dc 243,8,61,2048
Mvaa 176,156,12
X 168
Mvaa 180,156,28
Ra 152,242,22528
K 201,166,167
Mvaa 184,156,44
)"
R"(X 169
Mvaa 188,156,60
W 3
Dc 243,8,61,3072
Mvav 32,128,196
s_add_u32 m0, s62, 8192
X 170
Mvav 64,128,204
Ra 156,242,23552
K 208,168,169
Mvav 96,128,212
X 171
Mvav 128,128,220
Dc 244,8,61,0
Mvav 132,132,220
X 172
Mvav 100,132,212
Ra 128,242,8192
K 209,170,171
Mvav 68,132,204
X 173
Mvav 36,132,196
Dc 244,8,61,1024
Mvav 40,136,196
X 174
Mvav 72,136,204
Ra 132,242,9216
K 216,172,173
Mvav 104,136,212
X 175
Mvav 136,136,220
Dc 244,8,61,2048
Mvav 140,140,220
X 176
Mvav 108,140,212
Ra 136,242,10240
K 217,174,175
Mvav 76,140,204
X 177
Mvav 44,140,196
W 3
Dc 244,8,61,3072
Mvav 48,144,196
X 178
Mvav 80,144,204
s_add_u32 m0, s62, 12288
Ra 140,242,11264
Mvav 112,144,212
K 194,176,177
Mvav 144,144,220
X 179
Mvav 148,148,220
X 180
Mvav 116,148,212
Dc 245,8,61,0
Ra 144,242,12288
Mvav 84,148,204
K 195,178,179
Mvav 52,148,196
X 181
Mvav 56,152,196
X 182
Mvav 88,152,204
Dc 245,8,61,1024
Ra 148,242,13312
Mvav 120,152,212
K 202,180,181
Mvav 152,152,220
X 183
Mvav 156,156,220
X 184
Mvav 124,156,212
Dc 245,8,61,2048
Ra 152,242,14336
Mvav 92,156,204
K 203,182,183
Mvav 60,156,196
W 3
X 185
Cvaa 160,128,0,224
X 186
Cvaa 164,128,16,228
Dc 245,8,61,3072
Ra 156,242,15360
Cvaa 168,128,32,232
s_add_u32 s61, s61, s63
K 210,184,185
Cvaa 172,128,48,236
X 187
Mvaa 160,132,4
X 188
Mvaa 164,132,20
Ra 128,242,24576
Mvaa 168,132,36
s_xor_b32 s62, s62, 0x10000
K 211,186,187
Mvaa 172,132,52
X 189
Mvaa 160,136,8
X 190
Mvaa 164,136,24
s_add_u32 s64, s64, 1
Ra 132,242,25600
Mvaa 168,136,40
K 218,188,189
Mvaa 172,136,56
X 191
Mvaa 160,140,12
Ra 136,242,26624
Mvaa 164,140,28
Mvaa 168,140,44
s_add_u32 s36, s29, 262144
Mvaa 172,140,60
W 3
Cvaa 176,144,0,224
Ra 140,242,27648
Cvaa 180,144,16,228
X 160
Cvaa 184,144,32,232
K 219,190,191
Cvaa 188,144,48,236
X 161
Mvaa 176,148,4
X 162
Mvaa 180,148,20
buffer_store_dwordx4 a[64:67], v1, s[8:11], s36 offen offset:0 sc1
K 196,160,161
Mvaa 184,148,36
X 163
Mvaa 188,148,52
Ra 144,242,28672
Mvaa 176,152,8
X 164
Mvaa 180,152,24
buffer_store_dwordx4 a[72:75], v1, s[8:11], s36 offen offset:1024 sc1
K 197,162,163
Mvaa 184,152,40
X 165
Mvaa 188,152,56
Ra 148,242,29696
Mvaa 176,156,12
X 166
Mvaa 180,156,28
buffer_store_dwordx4 a[80:83], v1, s[8:11], s36 offen offset:2048 sc1
K 204,164,165
Mvaa 184,156,44
X 167
Mvaa 188,156,60
W 2
Ra 152,242,30720
Mvav 32,128,192
X 168
Mvav 64,128,200
Ra 156,242,31744
K 205,166,167
Mvav 96,128,208
X 169
Mvav 128,128,216
buffer_store_dwordx4 a[88:91], v1, s[8:11], s36 offen offset:3072 sc1
Mvav 132,132,216
X 170
Mvav 100,132,208
s_add_u32 s36, s29, 266240
K 212,168,169
Mvav 68,132,200
X 171
Mvav 36,132,192
X 172
Mvav 40,136,192
K 213,170,171
Mvav 72,136,200
X 173
Mvav 104,136,208
X 174
Mvav 136,136,216
buffer_store_dwordx4 a[96:99], v1, s[8:11], s36 offen offset:0 sc1
K 220,172,173
Mvav 140,140,216
X 175
Mvav 108,140,208
X 176
Mvav 76,140,200
s_waitcnt vmcnt(5) lgkmcnt(0)
s_barrier
v_xor_b32 v242, 0x10000, v242
Mvav 44,140,192
buffer_store_dwordx4 a[104:107], v1, s[8:11], s36 offen offset:1024 sc1
K 221,174,175
Mvav 48,144,192
Ra 128,242,0
Mvav 80,144,200
X 177
Mvav 112,144,208
X 178
Mvav 144,144,216
Ra 132,242,1024
K 198,176,177
Mvav 148,148,216
global_load_dword v3, v2, s[26:27]
Mvav 116,148,208
X 179
Mvav 84,148,200
X 180
Mvav 52,148,192
Ra 136,242,2048
K 199,178,179
Mvav 56,152,192
Ra 140,242,3072
v_add_u32 v2, 1280, v2
Mvav 88,152,200
X 181
Mvav 120,152,208
X 182
Mvav 152,152,216
K 206,180,181
Mvav 156,156,216
X 183
Mvav 124,156,208
Mvav 92,156,200
K 207,182,183
Mvav 60,156,192
W 0
Cvaa 160,128,0,224
Ra 144,242,4096
Ra 148,242,5120
Cvaa 164,128,16,228
s_add_u32 m0, s62, 0
Ra 152,242,6144
Cvaa 168,128,32,232
X 184
Cvaa 172,128,48,236
X 185
Mvaa 160,132,4
X 186
Mvaa 164,132,20
Ra 156,242,7168
K 214,184,185
Mvaa 168,132,36
Dc 1,8,61,0
Mvaa 172,132,52
X 187
Mvaa 160,136,8
X 188
Mvaa 164,136,24
Ra 128,242,16384
K 215,186,187
Mvaa 168,136,40
Dc 1,8,61,1024
Mvaa 172,136,56
X 189
Mvaa 160,140,12
X 190
Mvaa 164,140,28
Ra 132,242,17408
K 222,188,189
Mvaa 168,140,44
Dc 1,8,61,2048
Mvaa 172,140,60
X 191
v_smfmac_f32_16x16x64_bf16 a[160:163], a[176:179], v[192:199], v240
Ra 136,242,18432
K 223,190,191
v_smfmac_f32_16x16x64_bf16 a[164:167], a[176:179], v[200:207], v240
X 160
v_smfmac_f32_16x16x64_bf16 a[168:171], a[176:179], v[208:215], v240
Dc 1,8,61,3072
v_smfmac_f32_16x16x64_bf16 a[172:175], a[176:179], v[216:223], v240
W 3
X 161
Cvaa 176,144,0,224
s_add_u32 m0, s62, 4096
X 162
Cvaa 180,144,16,228
Ra 140,242,19456
K 192,160,161
Cvaa 184,144,32,232
X 163
Cvaa 188,144,48,236
Dc 243,8,61,0
Mvaa 176,148,4
X 164
Mvaa 180,148,20
Ra 144,242,20480
K 193,162,163
Mvaa 184,148,36
X 165
Mvaa 188,148,52
Dc 243,8,61,1024
Mvaa 176,152,8
X 166
Mvaa 180,152,24
Ra 148,242,21504
K 200,164,165
Mvaa 184,152,40
X 167
Mvaa 188,152,56
Dc 243,8,61,2048
Mvaa 176,156,12
X 168
Mvaa 180,156,28
Ra 152,242,22528
K 201,166,167
Mvaa 184,156,44
X 169
Mvaa 188,156,60
W 3
Dc 243,8,61,3072
Mvav 32,128,196
s_add_u32 m0, s62, 8192
X 170
Mvav 64,128,204
Ra 156,242,23552
K 208,168,169
Mvav 96,128,212
X 171
Mvav 128,128,220
Dc 244,8,61,0
Mvav 132,132,220
X 172
Mvav 100,132,212
Ra 128,242,8192
K 209,170,171
Mvav 68,132,204
X 173
Mvav 36,132,196
Dc 244,8,61,1024
Mvav 40,136,196
X 174
Mvav 72,136,204
Ra 132,242,9216
K 216,172,173
Mvav 104,136,212
X 175
Mvav 136,136,220
Dc 244,8,61,2048
Mvav 140,140,220
X 176
Mvav 108,140,212
Ra 136,242,10240
K 217,174,175
Mvav 76,140,204
X 177
Mvav 44,140,196
W 3
Dc 244,8,61,3072
Mvav 48,144,196
X 178
Mvav 80,144,204
s_add_u32 m0, s62, 12288
Ra 140,242,11264
Mvav 112,144,212
K 194,176,177
Mvav 144,144,220
X 179
Mvav 148,148,220
X 180
Mvav 116,148,212
Dc 245,8,61,0
Ra 144,242,12288
Mvav 84,148,204
K 195,178,179
Mvav 52,148,196
X 181
Mvav 56,152,196
X 182
Mvav 88,152,204
Dc 245,8,61,1024
Ra 148,242,13312
Mvav 120,152,212
K 202,180,181
Mvav 152,152,220
X 183
Mvav 156,156,220
X 184
Mvav 124,156,212
Dc 245,8,61,2048
Ra 152,242,14336
Mvav 92,156,204
K 203,182,183
v_bfe_u32 v28, v1, 4, 4
Mvav 60,156,196
W 3
X 185
Cvaa 160,128,0,224
Ra 156,242,15360
v_lshlrev_b32 v28, 3, v28
Cvaa 164,128,16,228
s_lshl_b32 s80, s56, 9
s_add_u32 s80, s80, 4352
Dc 245,8,61,3072
Cvaa 168,128,32,232
s_add_u32 s61, s61, s63
X 186
Cvaa 172,128,48,236
K 210,184,185
Mvaa 160,132,4
X 187
Mvaa 164,132,20
buffer_load_dwordx2 v[28:29], v28, s[8:11], s80 offen sc1
Ra 128,242,24576
)"
R"(Mvaa 168,132,36
s_xor_b32 s62, s62, 0x10000
X 188
Mvaa 172,132,52
K 211,186,187
Mvaa 160,136,8
X 189
Mvaa 164,136,24
s_add_u32 s64, s64, 1
X 190
Mvaa 168,136,40
Ra 132,242,25600
K 218,188,189
Mvaa 172,136,56
X 191
Mvaa 160,140,12
Ra 136,242,26624
Mvaa 164,140,28
Mvaa 168,140,44
buffer_store_dwordx4 a[112:115], v1, s[8:11], s36 offen offset:2048 sc1
Mvaa 172,140,60
W 3
Cvaa 176,144,0,224
Ra 140,242,27648
Cvaa 180,144,16,228
X 160
Cvaa 184,144,32,232
K 219,190,191
Cvaa 188,144,48,236
X 161
Mvaa 176,148,4
X 162
Mvaa 180,148,20
Ra 144,242,28672
K 196,160,161
Mvaa 184,148,36
X 163
Mvaa 188,148,52
X 164
Mvaa 176,152,8
Ra 148,242,29696
K 197,162,163
Mvaa 180,152,24
X 165
Mvaa 184,152,40
X 166
Mvaa 188,152,56
buffer_store_dwordx4 a[120:123], v1, s[8:11], s36 offen offset:3072 sc1
K 204,164,165
Mvaa 176,156,12
X 167
Mvaa 180,156,28
Ra 152,242,30720
Mvaa 184,156,44
X 168
Mvaa 188,156,60
W 3
K 205,166,167
Mvav 32,128,192
X 169
Mvav 64,128,200
Ra 156,242,31744
Mvav 96,128,208
X 170
Mvav 128,128,216
K 212,168,169
Mvav 132,132,216
X 171
Mvav 100,132,208
X 172
Mvav 68,132,200
global_load_dword v3, v2, s[26:27]
Mvav 36,132,192
K 213,170,171
Mvav 40,136,192
X 173
Mvav 72,136,200
X 174
Mvav 104,136,208
Mvav 136,136,216
s_waitcnt vmcnt(3)
v_cmp_eq_u64 vcc, s[12:13], v[28:29]
s_cmp_eq_u64 vcc, -1
s_cbranch_scc0 .Lspin_80%=
.Lok_81%=:
K 220,172,173
Mvav 140,140,216
X 175
Mvav 108,140,208
X 176
Mvav 76,140,200
W 0
s_barrier
v_xor_b32 v242, 0x10000, v242
Mvav 44,140,192
Ra 128,242,0
K 221,174,175
Mvav 48,144,192
X 177
Mvav 80,144,200
v_add_u32 v2, 1280, v2
Mvav 112,144,208
X 178
Mvav 144,144,216
Ra 132,242,1024
K 198,176,177
Mvav 148,148,216
X 179
Mvav 116,148,208
X 180
Mvav 84,148,200
Ra 136,242,2048
Mvav 52,148,192
K 199,178,179
Mvav 56,152,192
X 181
Mvav 88,152,200
Ra 140,242,3072
Mvav 120,152,208
X 182
Mvav 152,152,216
K 206,180,181
Mvav 156,156,216
X 183
Mvav 124,156,208
Mvav 92,156,200
K 207,182,183
Mvav 60,156,192
W 0
Cvaa 160,128,0,224
Ra 144,242,4096
Ra 148,242,5120
Cvaa 164,128,16,228
s_add_u32 m0, s62, 0
Ra 152,242,6144
Cvaa 168,128,32,232
X 184
Cvaa 172,128,48,236
X 185
Mvaa 160,132,4
X 186
Mvaa 164,132,20
Ra 156,242,7168
K 214,184,185
Mvaa 168,132,36
Dc 1,8,61,0
Mvaa 172,132,52
X 187
Mvaa 160,136,8
X 188
Mvaa 164,136,24
Ra 128,242,16384
K 215,186,187
Mvaa 168,136,40
Dc 1,8,61,1024
Mvaa 172,136,56
X 189
Mvaa 160,140,12
X 190
Mvaa 164,140,28
Ra 132,242,17408
K 222,188,189
Mvaa 168,140,44
Dc 1,8,61,2048
Mvaa 172,140,60
X 191
v_smfmac_f32_16x16x64_bf16 a[160:163], a[176:179], v[192:199], v240
Ra 136,242,18432
K 223,190,191
v_smfmac_f32_16x16x64_bf16 a[164:167], a[176:179], v[200:207], v240
X 160
v_smfmac_f32_16x16x64_bf16 a[168:171], a[176:179], v[208:215], v240
Dc 1,8,61,3072
v_smfmac_f32_16x16x64_bf16 a[172:175], a[176:179], v[216:223], v240
W 3
X 161
Cvaa 176,144,0,224
s_add_u32 m0, s62, 4096
X 162
Cvaa 180,144,16,228
Ra 140,242,19456
K 192,160,161
Cvaa 184,144,32,232
X 163
Cvaa 188,144,48,236
Dc 243,8,61,0
Mvaa 176,148,4
X 164
Mvaa 180,148,20
Ra 144,242,20480
K 193,162,163
Mvaa 184,148,36
X 165
Mvaa 188,148,52
Dc 243,8,61,1024
Mvaa 176,152,8
X 166
Mvaa 180,152,24
Ra 148,242,21504
K 200,164,165
Mvaa 184,152,40
X 167
Mvaa 188,152,56
Dc 243,8,61,2048
Mvaa 176,156,12
X 168
Mvaa 180,156,28
Ra 152,242,22528
K 201,166,167
Mvaa 184,156,44
X 169
Mvaa 188,156,60
W 3
Dc 243,8,61,3072
Mvav 32,128,196
s_add_u32 m0, s62, 8192
X 170
Mvav 64,128,204
Ra 156,242,23552
K 208,168,169
Mvav 96,128,212
X 171
Mvav 128,128,220
Dc 244,8,61,0
Mvav 132,132,220
X 172
Mvav 100,132,212
Ra 128,242,8192
K 209,170,171
Mvav 68,132,204
X 173
Mvav 36,132,196
Dc 244,8,61,1024
Mvav 40,136,196
X 174
Mvav 72,136,204
Ra 132,242,9216
K 216,172,173
Mvav 104,136,212
X 175
Mvav 136,136,220
Dc 244,8,61,2048
Mvav 140,140,220
X 176
Mvav 108,140,212
Ra 136,242,10240
K 217,174,175
Mvav 76,140,204
X 177
Mvav 44,140,196
W 3
Dc 244,8,61,3072
Mvav 48,144,196
X 178
Mvav 80,144,204
s_add_u32 m0, s62, 12288
Ra 140,242,11264
Mvav 112,144,212
K 194,176,177
Mvav 144,144,220
X 179
Mvav 148,148,220
X 180
Mvav 116,148,212
Dc 245,8,61,0
Ra 144,242,12288
Mvav 84,148,204
K 195,178,179
Mvav 52,148,196
X 181
Mvav 56,152,196
X 182
Mvav 88,152,204
Dc 245,8,61,1024
Ra 148,242,13312
Mvav 120,152,212
K 202,180,181
Mvav 152,152,220
X 183
Mvav 156,156,220
s_waitcnt vmcnt(15)
s_add_u32 s80, s71, 384
v_mov_b64 v[28:29], s[12:13]
s_mov_b64 exec, 1
buffer_store_dwordx2 v[28:29], off, s[8:11], s80 sc1
s_mov_b64 exec, -1
X 184
Mvav 124,156,212
Ra 152,242,14336
Mvav 92,156,204
K 203,182,183
Mvav 60,156,196
W 3
X 185
Cvaa 160,128,0,224
Dc 245,8,61,2048
Ra 156,242,15360
Cvaa 164,128,16,228
X 186
Cvaa 168,128,32,232
K 210,184,185
Cvaa 172,128,48,236
X 187
Mvaa 160,132,4
Dc 245,8,61,3072
Ra 128,242,24576
Mvaa 164,132,20
s_add_u32 s61, s61, s63
X 188
Mvaa 168,132,36
s_xor_b32 s62, s62, 0x10000
K 211,186,187
Mvaa 172,132,52
X 189
Mvaa 160,136,8
X 190
Mvaa 164,136,24
s_add_u32 s64, s64, 1
Ra 132,242,25600
Mvaa 168,136,40
K 218,188,189
Mvaa 172,136,56
X 191
Mvaa 160,140,12
Ra 136,242,26624
Mvaa 164,140,28
Mvaa 168,140,44
global_load_dword v3, v2, s[26:27]
Mvaa 172,140,60
W 3
Cvaa 176,144,0,224
Ra 140,242,27648
Cvaa 180,144,16,228
X 160
Cvaa 184,144,32,232
K 219,190,191
Cvaa 188,144,48,236
X 161
Mvaa 176,148,4
X 162
Mvaa 180,148,20
Ra 144,242,28672
K 196,160,161
Mvaa 184,148,36
X 163
Mvaa 188,148,52
X 164
Mvaa 176,152,8
Ra 148,242,29696
K 197,162,163
Mvaa 180,152,24
X 165
Mvaa 184,152,40
X 166
Mvaa 188,152,56
K 204,164,165
Mvaa 176,156,12
X 167
Mvaa 180,156,28
Ra 152,242,30720
Mvaa 184,156,44
X 168
Mvaa 188,156,60
W 3
K 205,166,167
Mvav 32,128,192
X 169
Mvav 64,128,200
Ra 156,242,31744
Mvav 96,128,208
X 170
Mvav 128,128,216
K 212,168,169
Mvav 132,132,216
X 171
Mvav 100,132,208
X 172
Mvav 68,132,200
v_add_u32 v2, 1280, v2
Mvav 36,132,192
K 213,170,171
Mvav 40,136,192
X 173
Mvav 72,136,200
X 174
Mvav 104,136,208
Mvav 136,136,216
K 220,172,173
Mvav 140,140,216
X 175
Mvav 108,140,208
X 176
Mvav 76,140,200
s_waitcnt vmcnt(1) lgkmcnt(0)
s_barrier
v_xor_b32 v242, 0x10000, v242
Mvav 44,140,192
Ra 128,242,0
K 221,174,175
Mvav 48,144,192
)"
R"(X 177
Mvav 80,144,200
X 178
Mvav 112,144,208
Ra 132,242,1024
Mvav 144,144,216
K 198,176,177
Mvav 148,148,216
X 179
Mvav 116,148,208
X 180
Mvav 84,148,200
Ra 136,242,2048
Mvav 52,148,192
K 199,178,179
Mvav 56,152,192
X 181
Mvav 88,152,200
Ra 140,242,3072
Mvav 120,152,208
X 182
Mvav 152,152,216
K 206,180,181
Mvav 156,156,216
X 183
Mvav 124,156,208
Mvav 92,156,200
K 207,182,183
Mvav 60,156,192
W 0
Cvaa 160,128,0,224
Ra 144,242,4096
Ra 148,242,5120
Cvaa 164,128,16,228
s_add_u32 m0, s62, 0
Ra 152,242,6144
Cvaa 168,128,32,232
X 184
Cvaa 172,128,48,236
X 185
Mvaa 160,132,4
X 186
Mvaa 164,132,20
Ra 156,242,7168
K 214,184,185
Mvaa 168,132,36
Dc 1,8,61,0
Mvaa 172,132,52
X 187
Mvaa 160,136,8
X 188
Mvaa 164,136,24
Ra 128,242,16384
K 215,186,187
Mvaa 168,136,40
Dc 1,8,61,1024
Mvaa 172,136,56
X 189
Mvaa 160,140,12
X 190
Mvaa 164,140,28
Ra 132,242,17408
K 222,188,189
Mvaa 168,140,44
Dc 1,8,61,2048
Mvaa 172,140,60
X 191
v_smfmac_f32_16x16x64_bf16 a[160:163], a[176:179], v[192:199], v240
Ra 136,242,18432
K 223,190,191
v_smfmac_f32_16x16x64_bf16 a[164:167], a[176:179], v[200:207], v240
X 160
v_smfmac_f32_16x16x64_bf16 a[168:171], a[176:179], v[208:215], v240
Dc 1,8,61,3072
v_smfmac_f32_16x16x64_bf16 a[172:175], a[176:179], v[216:223], v240
W 3
X 161
Cvaa 176,144,0,224
s_add_u32 m0, s62, 4096
X 162
Cvaa 180,144,16,228
Ra 140,242,19456
K 192,160,161
Cvaa 184,144,32,232
X 163
Cvaa 188,144,48,236
Dc 243,8,61,0
Mvaa 176,148,4
X 164
Mvaa 180,148,20
Ra 144,242,20480
K 193,162,163
Mvaa 184,148,36
X 165
Mvaa 188,148,52
Dc 243,8,61,1024
Mvaa 176,152,8
X 166
Mvaa 180,152,24
Ra 148,242,21504
K 200,164,165
Mvaa 184,152,40
X 167
Mvaa 188,152,56
Dc 243,8,61,2048
Mvaa 176,156,12
X 168
Mvaa 180,156,28
Ra 152,242,22528
K 201,166,167
Mvaa 184,156,44
X 169
Mvaa 188,156,60
W 3
Dc 243,8,61,3072
Mvav 32,128,196
s_add_u32 m0, s62, 8192
X 170
Mvav 64,128,204
Ra 156,242,23552
K 208,168,169
Mvav 96,128,212
X 171
Mvav 128,128,220
Dc 244,8,61,0
Mvav 132,132,220
X 172
Mvav 100,132,212
Ra 128,242,8192
K 209,170,171
Mvav 68,132,204
X 173
Mvav 36,132,196
Dc 244,8,61,1024
Mvav 40,136,196
X 174
Mvav 72,136,204
Ra 132,242,9216
K 216,172,173
Mvav 104,136,212
X 175
Mvav 136,136,220
Dc 244,8,61,2048
Mvav 140,140,220
X 176
Mvav 108,140,212
Ra 136,242,10240
K 217,174,175
Mvav 76,140,204
X 177
Mvav 44,140,196
W 3
Dc 244,8,61,3072
Mvav 48,144,196
X 178
Mvav 80,144,204
s_add_u32 m0, s62, 12288
Ra 140,242,11264
Mvav 112,144,212
K 194,176,177
Mvav 144,144,220
X 179
Mvav 148,148,220
X 180
Mvav 116,148,212
Dc 245,8,61,0
Ra 144,242,12288
Mvav 84,148,204
K 195,178,179
Mvav 52,148,196
X 181
Mvav 56,152,196
X 182
Mvav 88,152,204
Dc 245,8,61,1024
Ra 148,242,13312
Mvav 120,152,212
K 202,180,181
Mvav 152,152,220
X 183
Mvav 156,156,220
X 184
Mvav 124,156,212
Dc 245,8,61,2048
Ra 152,242,14336
Mvav 92,156,204
K 203,182,183
Mvav 60,156,196
W 3
X 185
Cvaa 160,128,0,224
X 186
Cvaa 164,128,16,228
Dc 245,8,61,3072
Ra 156,242,15360
Cvaa 168,128,32,232
s_add_u32 s61, s61, s63
K 210,184,185
Cvaa 172,128,48,236
X 187
Mvaa 160,132,4
X 188
Mvaa 164,132,20
Ra 128,242,24576
Mvaa 168,132,36
s_xor_b32 s62, s62, 0x10000
K 211,186,187
Mvaa 172,132,52
X 189
Mvaa 160,136,8
X 190
Mvaa 164,136,24
s_add_u32 s64, s64, 1
Ra 132,242,25600
Mvaa 168,136,40
K 218,188,189
Mvaa 172,136,56
X 191
Mvaa 160,140,12
Ra 136,242,26624
Mvaa 164,140,28
Mvaa 168,140,44
global_load_dword v3, v2, s[26:27]
Mvaa 172,140,60
W 3
Cvaa 176,144,0,224
Ra 140,242,27648
Cvaa 180,144,16,228
X 160
Cvaa 184,144,32,232
K 219,190,191
Cvaa 188,144,48,236
X 161
Mvaa 176,148,4
X 162
Mvaa 180,148,20
Ra 144,242,28672
K 196,160,161
Mvaa 184,148,36
X 163
Mvaa 188,148,52
X 164
Mvaa 176,152,8
Ra 148,242,29696
K 197,162,163
Mvaa 180,152,24
X 165
Mvaa 184,152,40
X 166
Mvaa 188,152,56
K 204,164,165
Mvaa 176,156,12
X 167
Mvaa 180,156,28
Ra 152,242,30720
Mvaa 184,156,44
X 168
Mvaa 188,156,60
W 3
K 205,166,167
Mvav 32,128,192
X 169
Mvav 64,128,200
Ra 156,242,31744
Mvav 96,128,208
X 170
Mvav 128,128,216
K 212,168,169
Mvav 132,132,216
X 171
Mvav 100,132,208
X 172
Mvav 68,132,200
v_add_u32 v2, 1280, v2
Mvav 36,132,192
K 213,170,171
Mvav 40,136,192
X 173
Mvav 72,136,200
X 174
Mvav 104,136,208
Mvav 136,136,216
K 220,172,173
Mvav 140,140,216
X 175
Mvav 108,140,208
X 176
Mvav 76,140,200
s_waitcnt vmcnt(1) lgkmcnt(0)
s_barrier
v_xor_b32 v242, 0x10000, v242
Mvav 44,140,192
Ra 128,242,0
K 221,174,175
Mvav 48,144,192
X 177
Mvav 80,144,200
X 178
Mvav 112,144,208
Ra 132,242,1024
Mvav 144,144,216
K 198,176,177
Mvav 148,148,216
X 179
Mvav 116,148,208
X 180
Mvav 84,148,200
Ra 136,242,2048
Mvav 52,148,192
K 199,178,179
Mvav 56,152,192
X 181
Mvav 88,152,200
Ra 140,242,3072
Mvav 120,152,208
X 182
Mvav 152,152,216
K 206,180,181
Mvav 156,156,216
X 183
Mvav 124,156,208
Mvav 92,156,200
K 207,182,183
Mvav 60,156,192
W 0
Cvaa 160,128,0,224
Ra 144,242,4096
Ra 148,242,5120
Cvaa 164,128,16,228
s_add_u32 m0, s62, 0
Ra 152,242,6144
Cvaa 168,128,32,232
X 184
Cvaa 172,128,48,236
X 185
Mvaa 160,132,4
X 186
Mvaa 164,132,20
Ra 156,242,7168
K 214,184,185
Mvaa 168,132,36
Dc 1,8,61,0
Mvaa 172,132,52
X 187
Mvaa 160,136,8
X 188
Mvaa 164,136,24
Ra 128,242,16384
K 215,186,187
Mvaa 168,136,40
Dc 1,8,61,1024
Mvaa 172,136,56
X 189
Mvaa 160,140,12
X 190
Mvaa 164,140,28
Ra 132,242,17408
K 222,188,189
Mvaa 168,140,44
Dc 1,8,61,2048
Mvaa 172,140,60
X 191
v_smfmac_f32_16x16x64_bf16 a[160:163], a[176:179], v[192:199], v240
Ra 136,242,18432
K 223,190,191
v_smfmac_f32_16x16x64_bf16 a[164:167], a[176:179], v[200:207], v240
X 160
v_smfmac_f32_16x16x64_bf16 a[168:171], a[176:179], v[208:215], v240
Dc 1,8,61,3072
v_smfmac_f32_16x16x64_bf16 a[172:175], a[176:179], v[216:223], v240
W 3
X 161
Cvaa 176,144,0,224
s_add_u32 m0, s62, 4096
X 162
Cvaa 180,144,16,228
Ra 140,242,19456
K 192,160,161
Cvaa 184,144,32,232
X 163
Cvaa 188,144,48,236
Dc 243,8,61,0
Mvaa 176,148,4
X 164
Mvaa 180,148,20
Ra 144,242,20480
K 193,162,163
Mvaa 184,148,36
X 165
Mvaa 188,148,52
Dc 243,8,61,1024
Mvaa 176,152,8
X 166
Mvaa 180,152,24
Ra 148,242,21504
K 200,164,165
Mvaa 184,152,40
X 167
Mvaa 188,152,56
Dc 243,8,61,2048
)"
R"(Mvaa 176,156,12
X 168
Mvaa 180,156,28
Ra 152,242,22528
K 201,166,167
Mvaa 184,156,44
X 169
Mvaa 188,156,60
W 3
Dc 243,8,61,3072
Mvav 32,128,196
s_add_u32 m0, s62, 8192
X 170
Mvav 64,128,204
Ra 156,242,23552
K 208,168,169
Mvav 96,128,212
X 171
Mvav 128,128,220
Dc 244,8,61,0
Mvav 132,132,220
X 172
Mvav 100,132,212
Ra 128,242,8192
K 209,170,171
Mvav 68,132,204
X 173
Mvav 36,132,196
Dc 244,8,61,1024
Mvav 40,136,196
X 174
Mvav 72,136,204
Ra 132,242,9216
K 216,172,173
Mvav 104,136,212
X 175
Mvav 136,136,220
Dc 244,8,61,2048
Mvav 140,140,220
X 176
Mvav 108,140,212
Ra 136,242,10240
K 217,174,175
Mvav 76,140,204
X 177
Mvav 44,140,196
W 3
Dc 244,8,61,3072
Mvav 48,144,196
X 178
Mvav 80,144,204
s_add_u32 m0, s62, 12288
Ra 140,242,11264
Mvav 112,144,212
K 194,176,177
Mvav 144,144,220
X 179
Mvav 148,148,220
X 180
Mvav 116,148,212
Dc 245,8,61,0
Ra 144,242,12288
Mvav 84,148,204
K 195,178,179
Mvav 52,148,196
X 181
Mvav 56,152,196
X 182
Mvav 88,152,204
Dc 245,8,61,1024
Ra 148,242,13312
Mvav 120,152,212
K 202,180,181
Mvav 152,152,220
X 183
Mvav 156,156,220
X 184
Mvav 124,156,212
Dc 245,8,61,2048
Ra 152,242,14336
Mvav 92,156,204
K 203,182,183
Mvav 60,156,196
W 3
X 185
Cvaa 160,128,0,224
X 186
Cvaa 164,128,16,228
Dc 245,8,61,3072
Ra 156,242,15360
Cvaa 168,128,32,232
s_add_u32 s61, s61, s63
K 210,184,185
Cvaa 172,128,48,236
X 187
Mvaa 160,132,4
X 188
Mvaa 164,132,20
Ra 128,242,24576
Mvaa 168,132,36
s_xor_b32 s62, s62, 0x10000
K 211,186,187
Mvaa 172,132,52
X 189
Mvaa 160,136,8
X 190
Mvaa 164,136,24
s_add_u32 s64, s64, 1
Ra 132,242,25600
Mvaa 168,136,40
K 218,188,189
Mvaa 172,136,56
X 191
Mvaa 160,140,12
Ra 136,242,26624
Mvaa 164,140,28
Mvaa 168,140,44
global_load_dword v3, v2, s[26:27]
Mvaa 172,140,60
W 3
Cvaa 176,144,0,224
Ra 140,242,27648
Cvaa 180,144,16,228
X 160
Cvaa 184,144,32,232
K 219,190,191
Cvaa 188,144,48,236
X 161
Mvaa 176,148,4
X 162
Mvaa 180,148,20
K 196,160,161
v_add_u32 v2, 1280, v2
Mvaa 184,148,36
X 163
Mvaa 188,148,52
s_lshr_b32 s80, s15, 3
Ra 144,242,28672
Mvaa 176,152,8
X 164
Mvaa 180,152,24
Ra 148,242,29696
K 197,162,163
Mvaa 184,152,40
X 165
Mvaa 188,152,56
s_lshl_b32 s80, s80, 1
X 166
Mvaa 176,156,12
Ra 152,242,30720
K 204,164,165
Mvaa 180,156,28
X 167
Mvaa 184,156,44
X 168
Mvaa 188,156,60
W 3
s_add_u32 s80, s80, s14
K 205,166,167
Mvav 32,128,192
X 169
Mvav 64,128,200
Ra 156,242,31744
Mvav 96,128,208
X 170
Mvav 128,128,216
s_sub_u32 s80, s80, 2
K 212,168,169
Mvav 132,132,216
X 171
Mvav 100,132,208
X 172
Mvav 68,132,200
Mvav 36,132,192
K 213,170,171
Mvav 40,136,192
s_lshl_b32 s80, s80, 12
X 173
Mvav 72,136,200
X 174
Mvav 104,136,208
Mvav 136,136,216
K 220,172,173
Mvav 140,140,216
X 175
Mvav 108,140,208
v_min_u32 v246, 496, v1
Mvav 76,140,200
s_waitcnt vmcnt(1) lgkmcnt(0)
s_barrier
v_xor_b32 v242, 0x10000, v242
Mvav 44,140,192
X 176
Mvav 48,144,192
Ra 128,242,0
K 221,174,175
Mvav 80,144,200
X 177
Mvav 112,144,208
Ra 132,242,1024
v_lshl_add_u32 v246, v246, 3, s80
Mvav 144,144,216
X 178
Mvav 148,148,216
K 198,176,177
v_min_u32 v246, .Lend%=-.Lbeg%=-4, v246
Mvav 116,148,208
X 179
Mvav 84,148,200
X 180
Mvav 52,148,192
Ra 136,242,2048
K 199,178,179
Mvav 56,152,192
X 181
Mvav 88,152,200
global_load_dword v3, v246, s[6:7]
Ra 140,242,3072
Mvav 120,152,208
X 182
Mvav 152,152,216
K 206,180,181
Mvav 156,156,216
X 183
Mvav 124,156,208
Mvav 92,156,200
K 207,182,183
Mvav 60,156,192
W 0
Cvaa 160,128,0,224
Ra 144,242,4096
Ra 148,242,5120
Cvaa 164,128,16,228
s_add_u32 m0, s62, 0
Ra 152,242,6144
Cvaa 168,128,32,232
X 184
Cvaa 172,128,48,236
X 185
Mvaa 160,132,4
X 186
Mvaa 164,132,20
Ra 156,242,7168
K 214,184,185
Mvaa 168,132,36
Dc 1,8,61,0
Mvaa 172,132,52
X 187
Mvaa 160,136,8
X 188
Mvaa 164,136,24
Ra 128,242,16384
K 215,186,187
Mvaa 168,136,40
Dc 1,8,61,1024
Mvaa 172,136,56
X 189
Mvaa 160,140,12
X 190
Mvaa 164,140,28
Ra 132,242,17408
K 222,188,189
Mvaa 168,140,44
Dc 1,8,61,2048
Mvaa 172,140,60
X 191
v_smfmac_f32_16x16x64_bf16 a[160:163], a[176:179], v[192:199], v240
Ra 136,242,18432
K 223,190,191
v_smfmac_f32_16x16x64_bf16 a[164:167], a[176:179], v[200:207], v240
X 160
v_smfmac_f32_16x16x64_bf16 a[168:171], a[176:179], v[208:215], v240
Dc 1,8,61,3072
v_smfmac_f32_16x16x64_bf16 a[172:175], a[176:179], v[216:223], v240
W 3
X 161
Cvaa 176,144,0,224
s_add_u32 m0, s62, 4096
X 162
Cvaa 180,144,16,228
Ra 140,242,19456
K 192,160,161
Cvaa 184,144,32,232
X 163
Cvaa 188,144,48,236
Dc 243,8,61,0
Mvaa 176,148,4
X 164
Mvaa 180,148,20
Ra 144,242,20480
K 193,162,163
Mvaa 184,148,36
X 165
Mvaa 188,148,52
Dc 243,8,61,1024
Mvaa 176,152,8
X 166
Mvaa 180,152,24
Ra 148,242,21504
K 200,164,165
Mvaa 184,152,40
X 167
Mvaa 188,152,56
Dc 243,8,61,2048
Mvaa 176,156,12
X 168
Mvaa 180,156,28
Ra 152,242,22528
K 201,166,167
Mvaa 184,156,44
X 169
Mvaa 188,156,60
W 3
Dc 243,8,61,3072
Mvav 32,128,196
s_add_u32 m0, s62, 8192
X 170
Mvav 64,128,204
Ra 156,242,23552
K 208,168,169
Mvav 96,128,212
X 171
Mvav 128,128,220
Dc 244,8,61,0
Mvav 132,132,220
X 172
Mvav 100,132,212
Ra 128,242,8192
K 209,170,171
Mvav 68,132,204
X 173
Mvav 36,132,196
Dc 244,8,61,1024
Mvav 40,136,196
X 174
Mvav 72,136,204
Ra 132,242,9216
K 216,172,173
Mvav 104,136,212
X 175
Mvav 136,136,220
Dc 244,8,61,2048
Mvav 140,140,220
X 176
Mvav 108,140,212
Ra 136,242,10240
K 217,174,175
Mvav 76,140,204
X 177
Mvav 44,140,196
W 3
Dc 244,8,61,3072
Mvav 48,144,196
X 178
Mvav 80,144,204
s_add_u32 m0, s62, 12288
Ra 140,242,11264
Mvav 112,144,212
K 194,176,177
Mvav 144,144,220
X 179
Mvav 148,148,220
X 180
Mvav 116,148,212
Dc 245,8,61,0
Ra 144,242,12288
Mvav 84,148,204
K 195,178,179
Mvav 52,148,196
X 181
Mvav 56,152,196
X 182
Mvav 88,152,204
Dc 245,8,61,1024
Ra 148,242,13312
Mvav 120,152,212
K 202,180,181
Mvav 152,152,220
X 183
Mvav 156,156,220
X 184
Mvav 124,156,212
Dc 245,8,61,2048
Ra 152,242,14336
Mvav 92,156,204
K 203,182,183
v_bfe_u32 v28, v1, 4, 4
Mvav 60,156,196
W 3
X 185
Cvaa 160,128,0,224
Ra 156,242,15360
v_lshlrev_b32 v28, 3, v28
Cvaa 164,128,16,228
s_lshl_b32 s80, s56, 9
s_add_u32 s80, s80, 4480
Dc 245,8,61,3072
Cvaa 168,128,32,232
s_add_u32 s61, s61, s63
X 186
)"
R"(Cvaa 172,128,48,236
K 210,184,185
Mvaa 160,132,4
X 187
Mvaa 164,132,20
buffer_load_dwordx2 v[28:29], v28, s[8:11], s80 offen sc1
Ra 128,242,24576
Mvaa 168,132,36
s_xor_b32 s62, s62, 0x10000
X 188
Mvaa 172,132,52
K 211,186,187
Mvaa 160,136,8
X 189
Mvaa 164,136,24
s_add_u32 s64, s64, 1
X 190
Mvaa 168,136,40
Ra 132,242,25600
K 218,188,189
Mvaa 172,136,56
X 191
Mvaa 160,140,12
Ra 136,242,26624
Mvaa 164,140,28
Mvaa 168,140,44
global_load_dword v3, v2, s[26:27]
Mvaa 172,140,60
W 3
Cvaa 176,144,0,224
Ra 140,242,27648
Cvaa 180,144,16,228
X 160
Cvaa 184,144,32,232
K 219,190,191
Cvaa 188,144,48,236
X 161
Mvaa 176,148,4
X 162
Mvaa 180,148,20
Ra 144,242,28672
K 196,160,161
Mvaa 184,148,36
X 163
Mvaa 188,148,52
X 164
Mvaa 176,152,8
Ra 148,242,29696
K 197,162,163
Mvaa 180,152,24
X 165
Mvaa 184,152,40
X 166
Mvaa 188,152,56
K 204,164,165
Mvaa 176,156,12
X 167
Mvaa 180,156,28
Ra 152,242,30720
Mvaa 184,156,44
X 168
Mvaa 188,156,60
W 3
K 205,166,167
Mvav 32,128,192
X 169
Mvav 64,128,200
Ra 156,242,31744
Mvav 96,128,208
X 170
Mvav 128,128,216
K 212,168,169
Mvav 132,132,216
X 171
Mvav 100,132,208
X 172
Mvav 68,132,200
v_add_u32 v2, 1280, v2
Mvav 36,132,192
K 213,170,171
Mvav 40,136,192
X 173
Mvav 72,136,200
X 174
Mvav 104,136,208
Mvav 136,136,216
s_waitcnt vmcnt(1)
v_cmp_eq_u64 vcc, s[12:13], v[28:29]
s_cmp_eq_u64 vcc, -1
s_cbranch_scc0 .Lspin_83%=
.Lok_84%=:
K 220,172,173
Mvav 140,140,216
X 175
Mvav 108,140,208
X 176
Mvav 76,140,200
W 0
s_barrier
v_xor_b32 v242, 0x10000, v242
Mvav 44,140,192
Ra 128,242,0
K 221,174,175
Mvav 48,144,192
X 177
Mvav 80,144,200
X 178
Mvav 112,144,208
Ra 132,242,1024
Mvav 144,144,216
K 198,176,177
Mvav 148,148,216
X 179
Mvav 116,148,208
X 180
Mvav 84,148,200
Ra 136,242,2048
Mvav 52,148,192
K 199,178,179
Mvav 56,152,192
X 181
Mvav 88,152,200
Ra 140,242,3072
Mvav 120,152,208
X 182
Mvav 152,152,216
K 206,180,181
Mvav 156,156,216
X 183
Mvav 124,156,208
Mvav 92,156,200
K 207,182,183
Mvav 60,156,192
W 0
Cvaa 160,128,0,224
Ra 144,242,4096
Ra 148,242,5120
Cvaa 164,128,16,228
s_add_u32 m0, s62, 0
Ra 152,242,6144
Cvaa 168,128,32,232
X 184
Cvaa 172,128,48,236
X 185
Mvaa 160,132,4
X 186
Mvaa 164,132,20
Ra 156,242,7168
K 214,184,185
Mvaa 168,132,36
Dc 1,8,61,0
Mvaa 172,132,52
X 187
Mvaa 160,136,8
X 188
Mvaa 164,136,24
Ra 128,242,16384
K 215,186,187
Mvaa 168,136,40
Dc 1,8,61,1024
Mvaa 172,136,56
X 189
Mvaa 160,140,12
X 190
Mvaa 164,140,28
Ra 132,242,17408
K 222,188,189
Mvaa 168,140,44
Dc 1,8,61,2048
Mvaa 172,140,60
X 191
v_smfmac_f32_16x16x64_bf16 a[160:163], a[176:179], v[192:199], v240
Ra 136,242,18432
K 223,190,191
v_smfmac_f32_16x16x64_bf16 a[164:167], a[176:179], v[200:207], v240
X 160
v_smfmac_f32_16x16x64_bf16 a[168:171], a[176:179], v[208:215], v240
Dc 1,8,61,3072
v_smfmac_f32_16x16x64_bf16 a[172:175], a[176:179], v[216:223], v240
W 3
X 161
Cvaa 176,144,0,224
s_add_u32 m0, s62, 4096
X 162
Cvaa 180,144,16,228
s_sub_u32 s81, s57, 1
Ra 140,242,19456
K 192,160,161
Cvaa 184,144,32,232
X 163
Cvaa 188,144,48,236
s_lshl_b32 s85, s81, 5
Dc 243,8,61,0
Mvaa 176,148,4
X 164
Mvaa 180,148,20
s_add_u32 s85, s85, s56
Ra 144,242,20480
K 193,162,163
Mvaa 184,148,36
X 165
Mvaa 188,148,52
Dc 243,8,61,1024
Mvaa 176,152,8
s_lshl_b32 s85, s85, 1
X 166
Mvaa 180,152,24
Ra 148,242,21504
K 200,164,165
Mvaa 184,152,40
s_add_u32 s85, s85, s14
X 167
Mvaa 188,152,56
Dc 243,8,61,2048
Mvaa 176,156,12
s_sub_u32 s85, s85, 2
X 168
Mvaa 180,156,28
Ra 152,242,22528
K 201,166,167
Mvaa 184,156,44
X 169
Mvaa 188,156,60
W 3
s_cmp_lt_u32 s85, 160
s_cselect_b64 s[20:21], 0, -1
Dc 243,8,61,3072
Mvav 32,128,196
s_add_u32 m0, s62, 8192
X 170
Mvav 64,128,204
s_mul_i32 s18, s85, 205
Ra 156,242,23552
K 208,168,169
Mvav 96,128,212
X 171
Mvav 128,128,220
s_lshr_b32 s18, s18, 12
Dc 244,8,61,0
Mvav 132,132,220
X 172
Mvav 100,132,212
Ra 128,242,8192
K 209,170,171
Mvav 68,132,204
s_and_b32 s18, s18, 7
X 173
Mvav 36,132,196
Dc 244,8,61,1024
Mvav 40,136,196
X 174
Mvav 72,136,204
Ra 132,242,9216
v_lshrrev_b32 v4, 1, v1
Mvav 104,136,212
K 216,172,173
v_min_u32 v5, 344, v4
Mvav 136,136,220
Dc 244,8,61,2048
Mvav 140,140,220
X 175
Mvav 108,140,212
s_lshl_b32 s81, s18, 3
X 176
Mvav 76,140,204
Ra 136,242,10240
K 217,174,175
Mvav 44,140,196
W 3
Dc 244,8,61,3072
v_mov_b32 v6, s81
Mvav 48,144,196
X 177
Mvav 80,144,204
s_add_u32 m0, s62, 12288
X 178
Mvav 112,144,212
Ra 140,242,11264
K 194,176,177
Mvav 144,144,220
buffer_load_dwordx2 v[8:9], v4, s[8:11], 0 offen offset:2560 sc1
Mvav 148,148,220
X 179
Mvav 116,148,212
X 180
Mvav 84,148,204
Ra 144,242,12288
K 195,178,179
Mvav 52,148,196
buffer_load_dwordx2 v[10:11], v5, s[8:11], 0 offen offset:3072 sc1
Mvav 56,152,196
X 181
Mvav 88,152,204
X 182
Mvav 120,152,212
Ra 148,242,13312
K 202,180,181
Mvav 152,152,220
Dc 245,8,61,0
Mvav 156,156,220
X 183
Mvav 124,156,212
X 184
Mvav 92,156,204
Ra 152,242,14336
K 203,182,183
Mvav 60,156,196
W 3
buffer_load_dwordx2 v[12:13], v6, s[8:11], 0 offen offset:3584 sc1
Cvaa 160,128,0,224
X 185
Cvaa 164,128,16,228
X 186
Cvaa 168,128,32,232
Ra 156,242,15360
K 210,184,185
Cvaa 172,128,48,236
Dc 245,8,61,1024
Mvaa 160,132,4
X 187
Mvaa 164,132,20
X 188
Mvaa 168,132,36
Ra 128,242,24576
K 211,186,187
Mvaa 172,132,52
Dc 245,8,61,2048
Mvaa 160,136,8
X 189
Mvaa 164,136,24
X 190
Mvaa 168,136,40
Ra 132,242,25600
K 218,188,189
Mvaa 172,136,56
Dc 245,8,61,3072
Mvaa 160,140,12
s_add_u32 s61, s61, s63
s_xor_b32 s62, s62, 0x10000
s_add_u32 s64, s64, 1
Mvaa 164,140,28
X 191
Mvaa 168,140,44
Ra 136,242,26624
Mvaa 172,140,60
W 3
global_load_dword v3, v2, s[26:27]
Cvaa 176,144,0,224
X 160
Cvaa 180,144,16,228
Ra 140,242,27648
K 219,190,191
Cvaa 184,144,32,232
X 161
Cvaa 188,144,48,236
X 162
Mvaa 176,148,4
Ra 144,242,28672
K 196,160,161
Mvaa 180,148,20
X 163
Mvaa 184,148,36
X 164
Mvaa 188,148,52
K 197,162,163
Mvaa 176,152,8
X 165
Mvaa 180,152,24
Ra 148,242,29696
Mvaa 184,152,40
X 166
Mvaa 188,152,56
K 204,164,165
Mvaa 176,156,12
X 167
Mvaa 180,156,28
Ra 152,242,30720
Mvaa 184,156,44
X 168
Mvaa 188,156,60
W 3
K 205,166,167
Mvav 32,128,192
X 169
Mvav 64,128,200
Ra 156,242,31744
Mvav 96,128,208
X 170
Mvav 128,128,216
K 212,168,169
Mvav 132,132,216
X 171
Mvav 100,132,208
X 172
Mvav 68,132,200
v_add_u32 v2, 1280, v2
Mvav 36,132,192
K 213,170,171
Mvav 40,136,192
X 173
)"
R"(Mvav 72,136,200
X 174
Mvav 104,136,208
Mvav 136,136,216
s_waitcnt vmcnt(4)
v_cmp_eq_u64 vcc, s[12:13], v[8:9]
v_cmp_eq_u64_e64 s[86:87], s[12:13], v[10:11]
s_and_b64 vcc, vcc, s[86:87]
s_or_b64 vcc, vcc, s[20:21]
s_cmp_eq_u64 vcc, -1
s_cbranch_scc0 .Lspin_86%=
.Lok_87%=:
K 220,172,173
Mvav 140,140,216
X 175
Mvav 108,140,208
X 176
Mvav 76,140,200
s_waitcnt vmcnt(1) lgkmcnt(0)
s_barrier
v_xor_b32 v242, 0x10000, v242
Mvav 44,140,192
Ra 128,242,0
K 221,174,175
Mvav 48,144,192
X 177
Mvav 80,144,200
X 178
Mvav 112,144,208
Ra 132,242,1024
Mvav 144,144,216
K 198,176,177
Mvav 148,148,216
X 179
Mvav 116,148,208
X 180
Mvav 84,148,200
Ra 136,242,2048
Mvav 52,148,192
K 199,178,179
Mvav 56,152,192
X 181
Mvav 88,152,200
Ra 140,242,3072
Mvav 120,152,208
X 182
Mvav 152,152,216
K 206,180,181
Mvav 156,156,216
X 183
Mvav 124,156,208
Mvav 92,156,200
K 207,182,183
Mvav 60,156,192
W 0
Cvaa 160,128,0,224
Ra 144,242,4096
Ra 148,242,5120
Cvaa 164,128,16,228
s_add_u32 m0, s62, 0
Ra 152,242,6144
Cvaa 168,128,32,232
X 184
Cvaa 172,128,48,236
X 185
Mvaa 160,132,4
X 186
Mvaa 164,132,20
Ra 156,242,7168
K 214,184,185
Mvaa 168,132,36
Dc 1,8,61,0
Mvaa 172,132,52
X 187
Mvaa 160,136,8
X 188
Mvaa 164,136,24
Ra 128,242,16384
K 215,186,187
Mvaa 168,136,40
Dc 1,8,61,1024
Mvaa 172,136,56
X 189
Mvaa 160,140,12
X 190
Mvaa 164,140,28
Ra 132,242,17408
K 222,188,189
Mvaa 168,140,44
Dc 1,8,61,2048
Mvaa 172,140,60
X 191
v_smfmac_f32_16x16x64_bf16 a[160:163], a[176:179], v[192:199], v240
Ra 136,242,18432
K 223,190,191
v_smfmac_f32_16x16x64_bf16 a[164:167], a[176:179], v[200:207], v240
X 160
v_smfmac_f32_16x16x64_bf16 a[168:171], a[176:179], v[208:215], v240
Dc 1,8,61,3072
v_smfmac_f32_16x16x64_bf16 a[172:175], a[176:179], v[216:223], v240
W 3
X 161
Cvaa 176,144,0,224
s_add_u32 m0, s62, 4096
X 162
Cvaa 180,144,16,228
Ra 140,242,19456
K 192,160,161
Cvaa 184,144,32,232
X 163
Cvaa 188,144,48,236
Dc 243,8,61,0
Mvaa 176,148,4
X 164
Mvaa 180,148,20
Ra 144,242,20480
K 193,162,163
Mvaa 184,148,36
X 165
Mvaa 188,148,52
Dc 243,8,61,1024
Mvaa 176,152,8
X 166
Mvaa 180,152,24
Ra 148,242,21504
K 200,164,165
Mvaa 184,152,40
X 167
Mvaa 188,152,56
Dc 243,8,61,2048
Mvaa 176,156,12
X 168
Mvaa 180,156,28
Ra 152,242,22528
K 201,166,167
Mvaa 184,156,44
X 169
Mvaa 188,156,60
W 3
Dc 243,8,61,3072
Mvav 32,128,196
s_add_u32 m0, s62, 8192
X 170
Mvav 64,128,204
Ra 156,242,23552
K 208,168,169
Mvav 96,128,212
X 171
Mvav 128,128,220
Dc 244,8,61,0
Mvav 132,132,220
X 172
Mvav 100,132,212
Ra 128,242,8192
K 209,170,171
Mvav 68,132,204
X 173
Mvav 36,132,196
Dc 244,8,61,1024
Mvav 40,136,196
X 174
Mvav 72,136,204
Ra 132,242,9216
K 216,172,173
Mvav 104,136,212
X 175
Mvav 136,136,220
Dc 244,8,61,2048
Mvav 140,140,220
X 176
Mvav 108,140,212
Ra 136,242,10240
K 217,174,175
Mvav 76,140,204
X 177
Mvav 44,140,196
W 3
Dc 244,8,61,3072
Mvav 48,144,196
X 178
Mvav 80,144,204
s_add_u32 m0, s62, 12288
Ra 140,242,11264
Mvav 112,144,212
K 194,176,177
Mvav 144,144,220
X 179
Mvav 148,148,220
X 180
Mvav 116,148,212
Dc 245,8,61,0
Ra 144,242,12288
Mvav 84,148,204
K 195,178,179
Mvav 52,148,196
X 181
Mvav 56,152,196
X 182
Mvav 88,152,204
Dc 245,8,61,1024
Ra 148,242,13312
Mvav 120,152,212
v_xor_b32 v6, s12, v12
K 202,180,181
Mvav 152,152,220
X 183
Mvav 156,156,220
Ra 152,242,14336
v_and_b32 v6, 2047, v6
Mvav 124,156,212
Dc 245,8,61,2048
v_lshlrev_b32 v6, 3, v6
Mvav 92,156,204
X 184
Mvav 60,156,196
W 3
K 203,182,183
Cvaa 160,128,0,224
s_mov_b32 s81, 40960
X 185
Cvaa 164,128,16,228
Dc 245,8,61,3072
Ra 156,242,15360
Cvaa 168,128,32,232
s_add_u32 s61, s61, s63
X 186
Cvaa 172,128,48,236
K 210,184,185
Mvaa 160,132,4
X 187
Mvaa 164,132,20
buffer_load_dword v7, v6, s[8:11], s81 offen sc1
v_lshrrev_b32 v5, 7, v1
Mvaa 168,132,36
s_xor_b32 s62, s62, 0x10000
X 188
Mvaa 172,132,52
Ra 128,242,24576
K 211,186,187
Mvaa 160,136,8
Ra 132,242,25600
v_mul_u32_u24 v4, 20480, v5
Mvaa 164,136,24
s_add_u32 s64, s64, 1
X 189
Mvaa 168,136,40
X 190
Mvaa 172,136,56
v_and_b32 v6, 0x70, v1
K 218,188,189
Mvaa 160,140,12
X 191
Mvaa 164,140,28
Ra 136,242,26624
v_add_u32 v4, v4, v6
Mvaa 168,140,44
v_add_u32 v5, 2, v5
Mvaa 172,140,60
W 3
X 160
Cvaa 176,144,0,224
K 219,190,191
v_lshrrev_b32 v5, 3, v5
Cvaa 180,144,16,228
X 161
Cvaa 184,144,32,232
Ra 140,242,27648
v_lshl_or_b32 v5, v5, 31, v4
Cvaa 188,144,48,236
X 162
Mvaa 176,148,4
Ra 144,242,28672
K 196,160,161
Mvaa 180,148,20
s_lshl_b32 s19, s85, 7
X 163
Mvaa 184,148,36
X 164
Mvaa 188,148,52
s_add_u32 s19, s19, 2752512
K 197,162,163
Mvaa 176,152,8
X 165
Mvaa 180,152,24
s_and_b32 s81, s20, 134217728
Ra 148,242,29696
Mvaa 184,152,40
X 166
Mvaa 188,152,56
K 204,164,165
Mvaa 176,156,12
s_or_b32 s19, s19, s81
X 167
Mvaa 180,156,28
Ra 152,242,30720
Mvaa 184,156,44
X 168
Mvaa 188,156,60
W 3
buffer_load_dwordx4 a[64:67], v4, s[8:11], s19 offen sc1
K 205,166,167
Mvav 32,128,192
s_add_u32 s19, s19, 163840
X 169
Mvav 64,128,200
Ra 156,242,31744
Mvav 96,128,208
X 170
Mvav 128,128,216
buffer_load_dwordx4 a[68:71], v4, s[8:11], s19 offen sc1
K 212,168,169
Mvav 132,132,216
X 171
Mvav 100,132,208
s_add_u32 s19, s19, 163840
X 172
Mvav 68,132,200
Mvav 36,132,192
buffer_load_dwordx4 a[72:75], v4, s[8:11], s19 offen sc1
K 213,170,171
Mvav 40,136,192
X 173
Mvav 72,136,200
X 174
Mvav 104,136,208
s_add_u32 s19, s19, 163840
Mvav 136,136,216
K 220,172,173
Mvav 140,140,216
X 175
Mvav 108,140,208
buffer_load_dwordx4 a[76:79], v4, s[8:11], s19 offen sc1
Mvav 76,140,200
s_waitcnt vmcnt(5) lgkmcnt(0)
s_barrier
v_xor_b32 v242, 0x10000, v242
Mvav 44,140,192
s_add_u32 s19, s19, 163840
X 176
Mvav 48,144,192
Ra 128,242,0
K 221,174,175
Mvav 80,144,200
X 177
Mvav 112,144,208
buffer_load_dwordx4 a[80:83], v4, s[8:11], s19 offen sc1
Ra 132,242,1024
Mvav 144,144,216
s_add_u32 s19, s19, 163840
X 178
Mvav 148,148,216
K 198,176,177
Mvav 116,148,208
X 179
Mvav 84,148,200
buffer_load_dwordx4 a[84:87], v4, s[8:11], s19 offen sc1
Ra 136,242,2048
Mvav 52,148,192
X 180
Mvav 56,152,192
s_add_u32 s19, s19, 163840
Ra 140,242,3072
K 199,178,179
Mvav 88,152,200
X 181
Mvav 120,152,208
X 182
Mvav 152,152,216
buffer_load_dwordx4 a[88:91], v5, s[8:11], s19 offen sc1
K 206,180,181
Mvav 156,156,216
X 183
Mvav 124,156,208
Mvav 92,156,200
K 207,182,183
Mvav 60,156,192
W 0
Cvaa 160,128,0,224
Ra 144,242,4096
Ra 148,242,5120
Cvaa 164,128,16,228
s_add_u32 m0, s62, 0
Ra 152,242,6144
Cvaa 168,128,32,232
X 184
Cvaa 172,128,48,236
X 185
Mvaa 160,132,4
X 186
Mvaa 164,132,20
Ra 156,242,7168
K 214,184,185
Mvaa 168,132,36
Dc 1,8,61,0
Mvaa 172,132,52
X 187
Mvaa 160,136,8
X 188
Mvaa 164,136,24
Ra 128,242,16384
)"
R"(K 215,186,187
Mvaa 168,136,40
Dc 1,8,61,1024
Mvaa 172,136,56
X 189
Mvaa 160,140,12
X 190
Mvaa 164,140,28
Ra 132,242,17408
K 222,188,189
Mvaa 168,140,44
Dc 1,8,61,2048
Mvaa 172,140,60
X 191
v_smfmac_f32_16x16x64_bf16 a[160:163], a[176:179], v[192:199], v240
Ra 136,242,18432
K 223,190,191
v_smfmac_f32_16x16x64_bf16 a[164:167], a[176:179], v[200:207], v240
X 160
v_smfmac_f32_16x16x64_bf16 a[168:171], a[176:179], v[208:215], v240
Dc 1,8,61,3072
v_smfmac_f32_16x16x64_bf16 a[172:175], a[176:179], v[216:223], v240
W 3
X 161
Cvaa 176,144,0,224
s_add_u32 m0, s62, 4096
X 162
Cvaa 180,144,16,228
Ra 140,242,19456
K 192,160,161
Cvaa 184,144,32,232
X 163
Cvaa 188,144,48,236
Dc 243,8,61,0
Mvaa 176,148,4
X 164
Mvaa 180,148,20
Ra 144,242,20480
K 193,162,163
Mvaa 184,148,36
X 165
Mvaa 188,148,52
Dc 243,8,61,1024
Mvaa 176,152,8
X 166
Mvaa 180,152,24
Ra 148,242,21504
K 200,164,165
Mvaa 184,152,40
X 167
Mvaa 188,152,56
Dc 243,8,61,2048
Mvaa 176,156,12
X 168
Mvaa 180,156,28
Ra 152,242,22528
K 201,166,167
Mvaa 184,156,44
X 169
Mvaa 188,156,60
W 3
Dc 243,8,61,3072
Mvav 32,128,196
s_add_u32 m0, s62, 8192
X 170
Mvav 64,128,204
Ra 156,242,23552
K 208,168,169
Mvav 96,128,212
X 171
Mvav 128,128,220
Dc 244,8,61,0
Mvav 132,132,220
X 172
Mvav 100,132,212
Ra 128,242,8192
K 209,170,171
Mvav 68,132,204
X 173
Mvav 36,132,196
Dc 244,8,61,1024
Mvav 40,136,196
X 174
Mvav 72,136,204
Ra 132,242,9216
K 216,172,173
Mvav 104,136,212
X 175
Mvav 136,136,220
Dc 244,8,61,2048
Mvav 140,140,220
X 176
Mvav 108,140,212
Ra 136,242,10240
K 217,174,175
Mvav 76,140,204
X 177
Mvav 44,140,196
W 3
Dc 244,8,61,3072
Mvav 48,144,196
X 178
Mvav 80,144,204
s_add_u32 m0, s62, 12288
Ra 140,242,11264
Mvav 112,144,212
K 194,176,177
Mvav 144,144,220
X 179
Mvav 148,148,220
X 180
Mvav 116,148,212
Dc 245,8,61,0
Ra 144,242,12288
Mvav 84,148,204
K 195,178,179
Mvav 52,148,196
X 181
Mvav 56,152,196
X 182
Mvav 88,152,204
Dc 245,8,61,1024
Ra 148,242,13312
Mvav 120,152,212
K 202,180,181
Mvav 152,152,220
X 183
Mvav 156,156,220
X 184
Mvav 124,156,212
Dc 245,8,61,2048
Ra 152,242,14336
Mvav 92,156,204
K 203,182,183
Mvav 60,156,196
W 3
X 185
Cvaa 160,128,0,224
X 186
Cvaa 164,128,16,228
Dc 245,8,61,3072
Ra 156,242,15360
Cvaa 168,128,32,232
s_add_u32 s61, s61, s63
K 210,184,185
Cvaa 172,128,48,236
X 187
Mvaa 160,132,4
X 188
Mvaa 164,132,20
Ra 128,242,24576
Mvaa 168,132,36
s_xor_b32 s62, s62, 0x10000
K 211,186,187
Mvaa 172,132,52
X 189
Mvaa 160,136,8
X 190
Mvaa 164,136,24
s_add_u32 s64, s64, 1
Ra 132,242,25600
Mvaa 168,136,40
K 218,188,189
Mvaa 172,136,56
X 191
Mvaa 160,140,12
Ra 136,242,26624
Mvaa 164,140,28
Mvaa 168,140,44
s_bfe_u32 s81, s59, 0x10001
Mvaa 172,140,60
W 3
Cvaa 176,144,0,224
s_lshl_b32 s82, s57, 1
Ra 140,242,27648
Cvaa 180,144,16,228
s_add_u32 s81, s81, s82
X 160
Cvaa 184,144,32,232
s_lshl_b32 s83, s81, 2
K 219,190,191
Cvaa 188,144,48,236
s_lshr_b32 s81, s59, 2
X 161
Mvaa 176,148,4
s_lshl_b32 s81, s81, 1
X 162
Mvaa 180,148,20
s_add_u32 s83, s83, s81
Ra 144,242,28672
K 196,160,161
Mvaa 184,148,36
s_and_b32 s81, s59, 1
X 163
Mvaa 188,148,52
s_add_u32 s83, s83, s81
X 164
Mvaa 176,152,8
s_and_b32 s81, s14, 1
Ra 148,242,29696
K 197,162,163
Mvaa 180,152,24
s_mul_i32 s81, s81, 124
X 165
Mvaa 184,152,40
s_xor_b32 s83, s83, s81
X 166
Mvaa 188,152,56
s_mul_i32 s83, s83, 20480
K 204,164,165
Mvaa 176,156,12
s_mul_i32 s81, s58, 320
X 167
Mvaa 180,156,28
s_add_u32 s82, s83, s81
Ra 152,242,30720
Mvaa 184,156,44
X 168
Mvaa 188,156,60
W 3
v_bfe_u32 v246, v1, 4, 4
K 205,166,167
Mvav 32,128,192
v_mul_u32_u24 v246, 1280, v246
v_lshrrev_b32 v247, 8, v1
Mvav 64,128,200
X 169
Mvav 96,128,208
Ra 156,242,31744
v_mul_u32_u24 v247, 104, v247
Mvav 128,128,216
X 170
Mvav 132,132,216
v_add3_u32 v246, v246, v247, s82
K 212,168,169
Mvav 100,132,208
global_load_dword v3, v246, s[16:17]
Mvav 68,132,200
s_lshl_b32 s81, s81, 3
X 171
Mvav 36,132,192
s_add_u32 s83, s83, s81
X 172
Mvav 40,136,192
s_add_u32 s83, s83, 131072
K 213,170,171
v_lshrrev_b32 v246, 9, v1
Mvav 72,136,200
X 173
Mvav 104,136,208
v_mul_u32_u24 v246, 10240, v246
v_and_b32 v247, 0x1f0, v1
Mvav 136,136,216
X 174
Mvav 140,140,216
v_min_u32 v247, 304, v247
K 220,172,173
Mvav 108,140,208
v_lshl_add_u32 v246, v247, 3, v246
Mvav 76,140,200
s_waitcnt vmcnt(1) lgkmcnt(0)
s_barrier
v_xor_b32 v242, 0x10000, v242
Mvav 44,140,192
X 175
Mvav 48,144,192
buffer_load_dword v3, v246, s[8:11], s83 offen sc1
Ra 128,242,0
Mvav 80,144,200
s_lshl_b32 s94, s58, 2
X 176
Mvav 112,144,208
s_add_u32 s94, s94, 0
s_and_b32 s94, s94, 15
K 221,174,175
Mvav 144,144,216
s_mul_i32 s94, s94, 10240
X 177
Mvav 148,148,216
s_add_u32 s94, s94, s48
X 178
Mvav 116,148,208
s_lshl_b32 s94, s94, 2
Ra 132,242,1024
K 198,176,177
Mvav 84,148,200
s_mul_i32 s95, s51, 0
X 179
Mvav 52,148,192
s_add_u32 s95, s95, s48
s_add_u32 m0, s95, 133120
Ra 136,242,2048
Mvav 56,152,192
X 180
Mvav 88,152,200
D 16,40,94,0
K 199,178,179
Mvav 120,152,208
X 181
Mvav 152,152,216
Ra 140,242,3072
Mvav 156,156,216
X 182
Mvav 124,156,208
D 17,40,94,1024
K 206,180,181
Mvav 92,156,200
X 183
Mvav 60,156,192
D 18,40,94,2048
D 19,40,94,3072
s_add_u32 m0, s95, 137216
s_nop 0
buffer_load_dwordx4 v20, s[40:43], s94 offen lds
K 207,182,183
W 0
Cvaa 160,128,0,224
Ra 144,242,4096
Ra 148,242,5120
Cvaa 164,128,16,228
s_add_u32 m0, s62, 0
Ra 152,242,6144
G 8,64
Cvaa 168,128,32,232
X 184
Cvaa 172,128,48,236
X 185
Mvaa 160,132,4
G 9,65
G 10,66
Mvaa 164,132,20
X 186
Mvaa 168,132,36
Ra 156,242,7168
K 214,184,185
Mvaa 172,132,52
Dc 1,8,61,0
G 11,67
Mvaa 160,136,8
X 187
Mvaa 164,136,24
X 188
Mvaa 168,136,40
Ra 128,242,16384
G 12,68
Mvaa 172,136,56
Dc 1,8,61,1024
G 13,69
Mvaa 160,140,12
K 215,186,187
G 14,70
Mvaa 164,140,28
X 189
Mvaa 168,140,44
X 190
Mvaa 172,140,60
Dc 1,8,61,2048
G 15,71
v_smfmac_f32_16x16x64_bf16 a[160:163], a[176:179], v[192:199], v240
Ra 132,242,17408
K 222,188,189
v_smfmac_f32_16x16x64_bf16 a[164:167], a[176:179], v[200:207], v240
X 191
s_nop 0
K 223,190,191
v_smfmac_f32_16x16x64_bf16 a[168:171], a[176:179], v[208:215], v240
A 8,8,12
A 9,9,13
v_smfmac_f32_16x16x64_bf16 a[172:175], a[176:179], v[216:223], v240
W 2
Dc 1,8,61,3072
Ra 136,242,18432
Cvaa 176,144,0,224
s_add_u32 m0, s62, 4096
X 160
Cvaa 180,144,16,228
A 10,10,14
A 11,11,15
Cvaa 184,144,32,232
X 161
Cvaa 188,144,48,236
X 162
Mvaa 176,148,4
Ra 140,242,19456
G 12,72
Mvaa 180,148,20
K 192,160,161
G 13,73
Mvaa 184,148,36
X 163
Mvaa 188,148,52
Dc 243,8,61,0
G 14,74
Mvaa 176,152,8
X 164
Mvaa 180,152,24
Ra 144,242,20480
G 15,75
Mvaa 184,152,40
K 193,162,163
)"
R"(A 8,8,12
Mvaa 188,152,56
Dc 243,8,61,1024
A 9,9,13
Mvaa 176,156,12
X 165
Mvaa 180,156,28
X 166
Mvaa 184,156,44
Ra 148,242,21504
K 200,164,165
Mvaa 188,156,60
W 2
Dc 243,8,61,2048
A 10,10,14
Mvav 32,128,196
X 167
Mvav 64,128,204
Ra 152,242,22528
A 11,11,15
Mvav 96,128,212
X 168
Mvav 128,128,220
Dc 243,8,61,3072
G 12,76
Mvav 132,132,220
s_add_u32 m0, s62, 8192
K 201,166,167
G 13,77
Mvav 100,132,212
X 169
Mvav 68,132,204
X 170
Mvav 36,132,196
Ra 156,242,23552
G 14,78
Mvav 40,136,196
K 208,168,169
G 15,79
Mvav 72,136,204
X 171
Mvav 104,136,212
Dc 244,8,61,0
A 8,8,12
Mvav 136,136,220
X 172
Mvav 140,140,220
Ra 128,242,8192
A 9,9,13
Mvav 108,140,212
K 209,170,171
A 10,10,14
Mvav 76,140,204
X 173
Mvav 44,140,196
W 1
Dc 244,8,61,1024
A 11,11,15
Mvav 48,144,196
X 174
Mvav 80,144,204
Ra 132,242,9216
G 12,80
Mvav 112,144,212
K 216,172,173
G 13,81
Mvav 144,144,220
Dc 244,8,61,2048
Ra 136,242,10240
Mvav 148,148,220
X 175
Mvav 116,148,212
X 176
Mvav 84,148,204
G 14,82
K 217,174,175
Mvav 52,148,196
Dc 244,8,61,3072
G 15,83
Mvav 56,152,196
s_add_u32 m0, s62, 12288
X 177
Mvav 88,152,204
Ra 140,242,11264
A 8,8,12
Mvav 120,152,212
X 178
Mvav 152,152,220
A 9,9,13
K 194,176,177
Mvav 156,156,220
X 179
Mvav 124,156,212
Dc 245,8,61,0
A 10,10,14
Mvav 92,156,204
X 180
Mvav 60,156,196
W 0
Ra 144,242,12288
A 11,11,15
Cvaa 160,128,0,224
K 195,178,179
G 12,84
Cvaa 164,128,16,228
X 181
Cvaa 168,128,32,232
Dc 245,8,61,1024
G 13,85
Cvaa 172,128,48,236
X 182
Mvaa 160,132,4
Ra 148,242,13312
G 14,86
Mvaa 164,132,20
K 202,180,181
G 15,87
Mvaa 168,132,36
X 183
Mvaa 172,132,52
Dc 245,8,61,2048
Ra 152,242,14336
Mvaa 160,136,8
X 184
Mvaa 164,136,24
A 8,8,12
K 203,182,183
Mvaa 168,136,40
Ra 156,242,15360
A 9,9,13
Mvaa 172,136,56
X 185
Mvaa 160,140,12
Dc 245,8,61,3072
A 10,10,14
Mvaa 164,140,28
s_add_u32 s61, s61, s63
s_xor_b32 s62, s62, 0x10000
s_add_u32 s64, s64, 1
Mvaa 168,140,44
s_andn2_b32 s81, s49, 262144
X 186
Mvaa 172,140,60
W 0
A 11,11,15
K 210,184,185
Cvaa 176,144,0,224
s_lshl_b32 s82, s58, 12
X 187
Cvaa 180,144,16,228
X 188
K 211,186,187
Cvaa 184,144,32,232
X 189
X 190
K 218,188,189
X 191
X 160
K 219,190,191
Cvaa 188,144,48,236
s_add_u32 s81, s81, s82
Ra 128,242,24576
G 12,88
Mvaa 176,148,4
s_cmp_lt_u32 s57, 6
s_cselect_b32 s81, s81, 79691776
G 13,89
G 14,90
Mvaa 180,148,20
Ra 132,242,25600
G 15,91
Mvaa 184,148,36
A 8,8,12
A 9,9,13
Mvaa 188,148,52
Ra 136,242,26624
A 10,10,14
Mvaa 176,152,8
A 11,11,15
v_add_f32_dpp v8, v8, v8 row_ror:8 row_mask:0xf bank_mask:0xf
Mvaa 180,152,24
X 161
Mvaa 184,152,40
Ra 140,242,27648
v_add_f32_dpp v9, v9, v9 row_ror:8 row_mask:0xf bank_mask:0xf
Mvaa 188,152,56
X 162
Mvaa 176,156,12
K 196,160,161
v_add_f32_dpp v10, v10, v10 row_ror:8 row_mask:0xf bank_mask:0xf
Mvaa 180,156,28
X 163
Mvaa 184,156,44
Ra 144,242,28672
v_add_f32_dpp v11, v11, v11 row_ror:8 row_mask:0xf bank_mask:0xf
Mvaa 188,156,60
W 1
X 164
Mvav 32,128,192
v_lshrrev_b32 v246, 9, v1
v_mov_b32 v12, v8
Mvav 64,128,200
K 197,162,163
v_and_b32 v247, 0x1f0, v1
Mvav 96,128,208
X 165
Mvav 128,128,216
Ra 148,242,29696
v_mov_b32 v13, v9
Mvav 132,132,216
X 166
Mvav 100,132,208
v_lshlrev_b32 v247, 3, v247
v_mov_b32 v14, v10
Mvav 68,132,200
K 204,164,165
v_lshl_add_u32 v246, v246, 17, v247
Mvav 36,132,192
Ra 152,242,30720
v_mov_b32 v15, v11
Mvav 40,136,192
X 167
Mvav 72,136,200
buffer_load_dword v3, v246, s[8:11], s81 offen sc1
v_permlane16_swap_b32 v8, v12
Mvav 104,136,208
s_add_u32 s81, s81, 262144
Ra 156,242,31744
v_permlane16_swap_b32 v9, v13
Mvav 136,136,216
X 168
Mvav 140,140,216
K 205,166,167
v_permlane16_swap_b32 v10, v14
Mvav 108,140,208
X 169
Mvav 76,140,200
s_waitcnt vmcnt(1) lgkmcnt(0)
s_barrier
v_xor_b32 v242, 0x10000, v242
Mvav 44,140,192
buffer_load_dword v3, v246, s[8:11], s81 offen sc1
v_permlane16_swap_b32 v11, v15
Mvav 48,144,192
s_lshl_b32 s94, s58, 2
X 170
Mvav 80,144,200
s_add_u32 s94, s94, 1
K 212,168,169
A 8,8,12
Mvav 112,144,208
s_and_b32 s94, s94, 15
X 171
Mvav 144,144,216
s_mul_i32 s94, s94, 10240
A 9,9,13
A 10,10,14
Mvav 148,148,216
s_add_u32 s94, s94, s48
X 172
Mvav 116,148,208
s_lshl_b32 s94, s94, 2
K 213,170,171
A 11,11,15
Mvav 84,148,200
s_mul_i32 s95, s51, 0
X 173
Mvav 52,148,192
s_add_u32 s95, s95, s48
X 174
Mvav 56,152,192
s_add_u32 m0, s95, 143360
v_mov_b32 v12, v8
K 220,172,173
Mvav 88,152,200
v_mov_b32 v13, v9
v_mov_b32 v14, v10
Mvav 120,152,208
X 175
Mvav 152,152,216
X 176
Mvav 156,156,216
v_mov_b32 v15, v11
K 221,174,175
Mvav 124,156,208
Ra 128,242,0
v_permlane32_swap_b32 v8, v12
Mvav 92,156,200
X 177
Mvav 60,156,192
D 16,40,94,0
Ra 132,242,1024
D 17,40,94,1024
D 18,40,94,2048
Ra 136,242,2048
D 19,40,94,3072
s_add_u32 m0, s95, 147456
Ra 140,242,3072
buffer_load_dwordx4 v20, s[40:43], s94 offen lds
v_permlane32_swap_b32 v9, v13
X 178
v_permlane32_swap_b32 v10, v14
K 198,176,177
X 179
v_permlane32_swap_b32 v11, v15
X 180
A 8,8,12
K 199,178,179
A 9,9,13
X 181
A 10,10,14
X 182
A 11,11,15
K 206,180,181
X 183
s_nop 0
K 207,182,183
W 0
Cvaa 160,128,0,224
Ra 144,242,4096
Ra 148,242,5120
Cvaa 164,128,16,228
s_add_u32 m0, s62, 0
Ra 152,242,6144
v_and_b32 v7, 0xffff0000, v7
Cvaa 168,128,32,232
X 184
Cvaa 172,128,48,236
X 185
Mvaa 160,132,4
Ra 156,242,7168
v_ashrrev_i32 v6, 31, v7
Mvaa 164,132,20
X 186
Mvaa 168,132,36
K 214,184,185
v_not_b32 v6, v6
Mvaa 172,132,52
Dc 1,8,61,0
v_or_b32 v6, 0x80000000, v6
Mvaa 160,136,8
X 187
Mvaa 164,136,24
X 188
Mvaa 168,136,40
Ra 128,242,16384
v_xor_b32 v7, v7, v6
Mvaa 172,136,56
Dc 1,8,61,1024
K 215,186,187
Mvaa 160,140,12
X 189
Mvaa 164,140,28
Ra 132,242,17408
v_and_b32 v7, 0xffff0000, v7
Mvaa 168,140,44
X 190
Mvaa 172,140,60
Dc 1,8,61,2048
v_mul_f32 v7, 0xbfb8aa3b, v7
v_smfmac_f32_16x16x64_bf16 a[160:163], a[176:179], v[192:199], v240
Ra 136,242,18432
K 222,188,189
v_smfmac_f32_16x16x64_bf16 a[164:167], a[176:179], v[200:207], v240
X 191
s_nop 0
K 223,190,191
v_smfmac_f32_16x16x64_bf16 a[168:171], a[176:179], v[208:215], v240
X 7
v_smfmac_f32_16x16x64_bf16 a[172:175], a[176:179], v[216:223], v240
W 3
Dc 1,8,61,3072
v_add_f32 v7, 1.0, v7
Cvaa 176,144,0,224
s_add_u32 m0, s62, 4096
X 160
Cvaa 180,144,16,228
X 161
Cvaa 184,144,32,232
X 162
Cvaa 188,144,48,236
Ra 140,242,19456
K 192,160,161
Mvaa 176,148,4
v_rcp_f32 v7, v7
Mvaa 180,148,20
X 163
Mvaa 184,148,36
Dc 243,8,61,0
Ra 144,242,20480
Mvaa 188,148,52
X 164
Mvaa 176,152,8
K 7,7,7
K 193,162,163
Mvaa 180,152,24
X 165
Mvaa 184,152,40
Dc 243,8,61,1024
v_lshlrev_b32 v7, 16, v7
)"
R"(Mvaa 188,152,56
X 166
Mvaa 176,156,12
Ra 148,242,21504
K 12,8,9
Mvaa 180,156,28
K 200,164,165
K 13,10,11
Mvaa 184,156,44
Dc 243,8,61,2048
Ra 152,242,22528
Mvaa 188,156,60
W 3
X 167
Mvav 32,128,196
X 168
Mvav 64,128,204
v_lshlrev_b32 v8, 16, v12
K 201,166,167
Mvav 96,128,212
Dc 243,8,61,3072
v_and_b32 v9, 0xffff0000, v12
Mvav 128,128,220
s_add_u32 m0, s62, 8192
X 169
Mvav 132,132,220
X 170
Mvav 100,132,212
Ra 156,242,23552
v_lshlrev_b32 v10, 16, v13
Mvav 68,132,204
K 208,168,169
v_and_b32 v11, 0xffff0000, v13
Mvav 36,132,196
X 171
Mvav 40,136,196
Dc 244,8,61,0
Ra 128,242,8192
Mvav 72,136,204
X 172
Mvav 104,136,212
U 8,8,7
K 209,170,171
Mvav 136,136,220
X 173
Mvav 140,140,220
Dc 244,8,61,1024
U 9,9,7
Mvav 108,140,212
X 174
Mvav 76,140,204
Ra 132,242,9216
U 10,10,7
Mvav 44,140,196
W 2
K 216,172,173
U 11,11,7
Mvav 48,144,196
Dc 244,8,61,2048
Ra 136,242,10240
Mvav 80,144,204
X 175
Mvav 112,144,212
X 176
Mvav 144,144,220
K 12,8,9
K 217,174,175
Mvav 148,148,220
Dc 244,8,61,3072
K 14,10,11
Mvav 116,148,212
s_add_u32 m0, s62, 12288
X 177
Mvav 84,148,204
X 178
Mvav 52,148,196
Ra 140,242,11264
v_mov_b32 v13, s13
Mvav 56,152,196
K 194,176,177
v_mov_b32 v15, s13
Mvav 88,152,204
X 179
Mvav 120,152,212
X 180
Mvav 152,152,220
Dc 245,8,61,0
Ra 144,242,12288
Mvav 156,156,220
v_lshrrev_b32 v6, 7, v1
K 195,178,179
Mvav 124,156,212
X 181
Mvav 92,156,204
Ra 148,242,13312
v_lshl_or_b32 v6, v6, 28, v1
Mvav 60,156,196
W 2
s_lshl_b32 s19, s85, 7
Dc 245,8,61,1024
Cvaa 160,128,0,224
s_add_u32 s19, s19, 98304
X 182
Cvaa 164,128,16,228
s_and_b32 s81, s20, 134217728
Ra 152,242,14336
K 202,180,181
Cvaa 168,128,32,232
s_or_b32 s19, s19, s81
X 183
Cvaa 172,128,48,236
Dc 245,8,61,2048
Ra 156,242,15360
Mvaa 160,132,4
X 184
Mvaa 164,132,20
Ra 128,242,24576
K 203,182,183
Mvaa 168,132,36
X 185
Mvaa 172,132,52
Dc 245,8,61,3072
Mvaa 160,136,8
s_add_u32 s61, s61, s63
X 186
Mvaa 164,136,24
s_xor_b32 s62, s62, 0x10000
Ra 132,242,25600
K 210,184,185
Mvaa 168,136,40
s_add_u32 s64, s64, 1
buffer_store_dwordx4 v[12:15], v6, s[8:11], s19 offen sc1
Mvaa 172,136,56
X 187
Mvaa 160,140,12
X 188
Mvaa 164,140,28
Ra 136,242,26624
K 211,186,187
Mvaa 168,140,44
s_lshl_b32 s94, s58, 2
X 189
Mvaa 172,140,60
W 3
X 190
Cvaa 176,144,0,224
Ra 140,242,27648
K 218,188,189
Cvaa 180,144,16,228
X 191
Cvaa 184,144,32,232
X 160
K 219,190,191
Cvaa 188,144,48,236
s_add_u32 s94, s94, 2
X 161
Mvaa 176,148,4
X 162
Mvaa 180,148,20
s_and_b32 s94, s94, 15
Ra 144,242,28672
K 196,160,161
Mvaa 184,148,36
X 163
Mvaa 188,148,52
X 164
Mvaa 176,152,8
s_mul_i32 s94, s94, 10240
Ra 148,242,29696
K 197,162,163
Mvaa 180,152,24
X 165
Mvaa 184,152,40
X 166
Mvaa 188,152,56
s_add_u32 s94, s94, s48
K 204,164,165
Mvaa 176,156,12
X 167
Mvaa 180,156,28
Ra 152,242,30720
Mvaa 184,156,44
s_lshl_b32 s94, s94, 2
X 168
Mvaa 188,156,60
W 3
K 205,166,167
Mvav 32,128,192
X 169
Mvav 64,128,200
s_mul_i32 s95, s51, 0
Ra 156,242,31744
Mvav 96,128,208
X 170
Mvav 128,128,216
K 212,168,169
Mvav 132,132,216
s_add_u32 s95, s95, s48
X 171
Mvav 100,132,208
X 172
Mvav 68,132,200
Mvav 36,132,192
s_add_u32 m0, s95, 153600
K 213,170,171
Mvav 40,136,192
X 173
Mvav 72,136,200
X 174
Mvav 104,136,208
D 16,40,94,0
Mvav 136,136,216
K 220,172,173
Mvav 140,140,216
X 175
Mvav 108,140,208
X 176
Mvav 76,140,200
s_waitcnt vmcnt(2) lgkmcnt(0)
s_barrier
v_xor_b32 v242, 0x10000, v242
Mvav 44,140,192
D 17,40,94,1024
K 221,174,175
Mvav 48,144,192
Ra 128,242,0
Mvav 80,144,200
X 177
Mvav 112,144,208
X 178
Mvav 144,144,216
D 18,40,94,2048
Ra 132,242,1024
Mvav 148,148,216
K 198,176,177
Mvav 116,148,208
X 179
Mvav 84,148,200
X 180
Mvav 52,148,192
D 19,40,94,3072
Ra 136,242,2048
Mvav 56,152,192
s_add_u32 m0, s95, 157696
Ra 140,242,3072
K 199,178,179
Mvav 88,152,200
X 181
Mvav 120,152,208
X 182
Mvav 152,152,216
buffer_load_dwordx4 v20, s[40:43], s94 offen lds
K 206,180,181
Mvav 156,156,216
X 183
Mvav 124,156,208
Mvav 92,156,200
K 207,182,183
Mvav 60,156,192
.Lf1_39%=:
W 0
Cvaa 160,128,0,224
Ra 144,242,4096
Ra 148,242,5120
Cvaa 164,128,16,228
Ra 152,242,6144
v_bfe_u32 v28, v1, 4, 2
Cvaa 168,128,32,232
X 184
Cvaa 172,128,48,236
X 185
Mvaa 160,132,4
Ra 156,242,7168
v_lshlrev_b32 v28, 3, v28
Mvaa 164,132,20
X 186
Mvaa 168,132,36
buffer_load_dwordx2 v[28:29], v28, s[8:11], s54 offen sc1
K 214,184,185
Mvaa 172,132,52
s_lshl_b32 s94, s58, 2
X 187
Mvaa 160,136,8
s_add_u32 s94, s94, s51
X 188
Mvaa 164,136,24
s_add_u32 s94, s94, 3
Ra 128,242,16384
K 215,186,187
Mvaa 168,136,40
s_and_b32 s94, s94, 15
X 189
Mvaa 172,136,56
X 190
Mvaa 160,140,12
s_mul_i32 s94, s94, 10240
Ra 132,242,17408
K 222,188,189
Mvaa 164,140,28
s_add_u32 s94, s94, s48
X 191
Mvaa 168,140,44
K 223,190,191
Mvaa 172,140,60
s_lshl_b32 s94, s94, 2
v_smfmac_f32_16x16x64_bf16 a[160:163], a[176:179], v[192:199], v240
s_mul_i32 s95, s51, 10240
Ra 136,242,18432
v_smfmac_f32_16x16x64_bf16 a[164:167], a[176:179], v[200:207], v240
X 160
v_smfmac_f32_16x16x64_bf16 a[168:171], a[176:179], v[208:215], v240
s_add_u32 s95, s95, s48
X 161
v_smfmac_f32_16x16x64_bf16 a[172:175], a[176:179], v[216:223], v240
W 3
X 162
Cvaa 176,144,0,224
s_add_u32 m0, s95, 0
Ra 140,242,19456
K 192,160,161
Cvaa 180,144,16,228
X 163
Cvaa 184,144,32,232
X 164
Cvaa 188,144,48,236
D 16,40,94,0
K 193,162,163
Mvaa 176,148,4
X 165
Mvaa 180,148,20
X 166
Mvaa 184,148,36
D 17,40,94,1024
K 200,164,165
Mvaa 188,148,52
X 167
Mvaa 176,152,8
Ra 144,242,20480
Ra 148,242,21504
Mvaa 180,152,24
D 18,40,94,2048
Mvaa 184,152,40
X 172
Mvaa 188,152,56
K 201,166,167
Mvaa 176,156,12
D 19,40,94,3072
Ra 152,242,22528
Mvaa 180,156,28
s_add_u32 m0, s95, 4096
X 173
Mvaa 184,156,44
X 174
Mvaa 188,156,60
s_cmp_eq_u32 s60, 16
s_cbranch_scc1 .Lmk_89%=
.Lmkr_90%=:
W 3
Mvav 32,128,196
buffer_load_dwordx4 v20, s[40:43], s94 offen lds
K 216,172,173
Mvav 64,128,204
s_lshl_b32 s94, s58, 2
X 175
Mvav 96,128,212
s_add_u32 s94, s94, s51
s_add_u32 s94, s94, 5
s_and_b32 s94, s94, 15
Mvav 128,128,220
s_mul_i32 s94, s94, 10240
X 168
Mvav 132,132,220
s_add_u32 s94, s94, s48
Ra 156,242,23552
K 217,174,175
Mvav 100,132,212
s_lshl_b32 s94, s94, 2
X 169
Mvav 68,132,204
s_mov_b32 s18, 40960
X 170
Mvav 36,132,196
s_mul_i32 s95, s51, 10240
s_mov_b32 s19, 73728
K 208,168,169
Mvav 40,136,196
s_bitcmp1_b32 s14, 0
s_cselect_b32 s18, 49152, s18
s_cselect_b32 s19, 98304, s19
X 171
Mvav 72,136,204
s_add_u32 s95, s95, s48
X 176
Mvav 104,136,212
s_cmp_lt_u32 s14, 2
s_cselect_b32 s20, s18, s19
s_cselect_b32 s21, s19, s18
Ra 128,242,24576
K 209,170,171
Mvav 136,136,220
)"
R"(s_add_u32 m0, s95, 20480
X 177
Mvav 140,140,220
X 178
Mvav 108,140,212
K 194,176,177
v_add_u32 v2, s20, v1
Mvav 76,140,204
X 179
Mvav 44,140,196
W 1
X 180
Mvav 48,144,196
D 16,40,94,0
v_add_u32 v3, s21, v1
Mvav 80,144,204
s_lshl_b32 s18, s14, 9
Ra 132,242,25600
K 195,178,179
Mvav 112,144,212
X 181
Mvav 144,144,220
D 17,40,94,1024
v_lshrrev_b32 v232, 1, v1
Mvav 148,148,220
X 182
Mvav 116,148,212
v_add_u32 v232, s18, v232
K 202,180,181
Mvav 84,148,204
D 18,40,94,2048
v_bfe_u32 v12, v1, 6, 2
Mvav 52,148,196
X 183
Mvav 56,152,196
Ra 136,242,26624
v_lshrrev_b32 v13, 8, v1
Mvav 88,152,204
D 19,40,94,3072
v_bfe_u32 v14, v1, 4, 1
Mvav 120,152,212
s_add_u32 m0, s95, 24576
X 184
Mvav 152,152,220
K 203,182,183
v_lshlrev_b32 v14, 4, v14
Mvav 156,156,220
buffer_load_dwordx4 v20, s[40:43], s94 offen lds
v_mov_b32 v15, 0x3f80
Mvav 124,156,212
s_waitcnt vmcnt(10)
v_cmp_eq_u64 vcc, s[12:13], v[28:29]
s_cmp_eq_u64 vcc, -1
s_cselect_b32 s46, 79691776, 0
X 185
Mvav 92,156,204
s_add_u32 s81, s58, 0
Ra 140,242,27648
v_lshlrev_b32 v15, v14, v15
Mvav 60,156,196
s_and_b32 s81, s81, 3
X 186
Maav 160,180,192
K 210,184,185
X 187
X 188
K 211,186,187
Maav 164,180,200
X 189
X 190
K 218,188,189
X 191
s_nop 0
K 219,190,191
Maav 168,180,208
s_lshl_b32 s81, s81, 12
v_bfe_u32 v14, v1, 5, 1
v_xor_b32 v27, v12, v13
Maav 172,180,216
W 0
Ra 144,242,28672
v_lshl_or_b32 v27, v27, 1, v14
Mvav 32,128,192
s_add_u32 s81, s81, s49
v_cmp_eq_u32_e64 s[86:87], 0, v27
v_mov_b32 v226, 0
Mvav 64,128,200
s_add_u32 s82, s81, 131072
Ra 148,242,29696
v_cndmask_b32_e64 v224, 0, v15, s[86:87]
Mvav 96,128,208
buffer_load_dwordx4 a[184:187], v1, s[44:47], s81 offen offset:0 sc1
v_mov_b32 v228, 0
Mvav 128,128,216
v_mov_b32 v230, v224
v_cmp_eq_u32_e64 s[86:87], 1, v27
Mvav 132,132,216
Ra 152,242,30720
v_mov_b32 v227, 0
Mvav 100,132,208
buffer_load_dwordx4 a[200:203], v1, s[44:47], s82 offen offset:0 sc1
v_cndmask_b32_e64 v225, 0, v15, s[86:87]
Mvav 68,132,200
v_mov_b32 v229, 0
v_mov_b32 v231, v225
Mvav 36,132,192
Ra 156,242,31744
G 4,160
Mvav 40,136,192
buffer_load_dwordx4 a[188:191], v1, s[44:47], s81 offen offset:1024 sc1
G 8,161
Mvav 72,136,200
G 5,164
G 9,165
Mvav 104,136,208
G 6,168
G 10,169
Mvav 136,136,216
buffer_load_dwordx4 a[204:207], v1, s[44:47], s82 offen offset:1024 sc1
G 7,172
Mvav 140,140,216
G 11,173
A 4,4,8
Mvav 108,140,208
A 5,5,9
A 6,6,10
Mvav 76,140,200
buffer_load_dwordx4 a[192:195], v1, s[44:47], s81 offen offset:2048 sc1
A 7,7,11
Mvav 44,140,192
W 0
ds_write_b64 v232, v[6:7] offset:57344
A 8,4,5
Mvav 48,144,192
A 9,6,7
A 8,8,9
Mvav 80,144,200
buffer_load_dwordx4 a[208:211], v1, s[44:47], s82 offen offset:2048 sc1
v_cmp_ngt_f32_e64 s[86:87], s90, v8
Mvav 112,144,208
s_lshl_b32 s19, s14, 2
v_mov_b32 v9, s19
Mvav 144,144,216
s_or_b64 s[88:89], s[88:89], s[86:87]
s_and_b64 s[88:89], s[88:89], s[24:25]
s_cselect_b32 s18, 1, 0
v_mov_b32 v8, s18
Mvav 148,148,216
buffer_load_dwordx4 a[196:199], v1, s[44:47], s81 offen offset:3072 sc1
ds_write_b32 v9, v8 offset:59392
Mvav 116,148,208
v_xor_b32 v10, 1024, v232
v_bfe_u32 v11, v1, 2, 4
Mvav 84,148,200
K 96,96,97
K 97,98,99
Mvav 52,148,192
buffer_load_dwordx4 a[212:215], v1, s[44:47], s82 offen offset:3072 sc1
K 98,100,101
Mvav 56,152,192
s_add_u32 s81, s58, 1
s_and_b32 s81, s81, 3
s_lshl_b32 s81, s81, 12
Mvav 88,152,200
s_add_u32 s81, s81, s49
s_add_u32 s82, s81, 131072
K 99,102,103
Mvav 120,152,208
ds_write_b128 v2, v[96:99] offset:0
K 128,128,129
Mvav 152,152,216
K 129,130,131
K 130,132,133
Mvav 156,156,216
buffer_load_dwordx4 a[216:219], v1, s[44:47], s81 offen offset:0 sc1
K 131,134,135
Mvav 124,156,208
ds_write_b128 v2, v[128:131] offset:4096
K 104,104,105
Mvav 92,156,200
K 105,106,107
K 106,108,109
Mvav 60,156,192
buffer_load_dwordx4 a[232:235], v1, s[44:47], s82 offen offset:0 sc1
K 107,110,111
buffer_load_dwordx4 a[220:223], v1, s[44:47], s81 offen offset:1024 sc1
buffer_load_dwordx4 a[236:239], v1, s[44:47], s82 offen offset:1024 sc1
buffer_load_dwordx4 a[224:227], v1, s[44:47], s81 offen offset:2048 sc1
ds_write_b128 v2, v[104:107] offset:1024
K 136,136,137
K 137,138,139
K 138,140,141
K 139,142,143
ds_write_b128 v2, v[136:139] offset:5120
buffer_load_dwordx4 a[240:243], v1, s[44:47], s82 offen offset:2048 sc1
buffer_load_dwordx4 a[228:231], v1, s[44:47], s81 offen offset:3072 sc1
buffer_load_dwordx4 a[244:247], v1, s[44:47], s82 offen offset:3072 sc1
K 112,112,113
K 113,114,115
K 114,116,117
K 115,118,119
ds_write_b128 v2, v[112:115] offset:2048
K 144,144,145
K 145,146,147
K 146,148,149
K 147,150,151
ds_write_b128 v2, v[144:147] offset:6144
K 120,120,121
K 121,122,123
K 122,124,125
K 123,126,127
ds_write_b128 v2, v[120:123] offset:3072
K 152,152,153
K 153,154,155
K 154,156,157
K 155,158,159
ds_write_b128 v2, v[152:155] offset:7168
.Lend_42%=:
s_waitcnt vmcnt(8) lgkmcnt(0)
s_barrier
s_cmp_eq_u32 s46, 0
s_cbranch_scc1 .Llate_91%=
ds_read_b32 v11, v11 offset:59392
ds_read_b64 v[8:9], v10 offset:57344
Rv 176,3,0
Rv 180,3,1024
Rv 184,3,2048
Rv 188,3,3072
Rv 192,3,4096
Rv 196,3,5120
Rv 200,3,6144
Rv 204,3,7168
W 6
Mvvv 32,224,176
v_cmp_ne_u32 vcc, 0, v11
s_cmp_lg_u64 vcc, 0
s_cbranch_scc1 .Lredo%=
s_lshl_b32 s94, s58, 2
Mvvv 36,228,176
A 4,4,8
Mvvv 40,224,180
s_add_u32 s94, s94, s51
v_rcp_f32 v4, v4
Mvvv 44,228,180
W 4
s_add_u32 s94, s94, 7
A 5,5,9
Mvvv 48,224,184
Ta 128,25,10240
Ta 130,25,15360
Mvvv 52,228,184
s_and_b32 s94, s94, 15
v_rcp_f32 v5, v5
Mvvv 56,224,188
s_lshl_b32 s93, s58, 12
s_add_u32 s93, s93, s50
Ta 132,26,10272
Mvvv 60,228,188
W 5
s_mul_i32 s94, s94, 10240
Ta 134,26,15392
Mvvv 64,224,192
s_add_u32 s94, s94, s48
U 32,32,4
U 33,33,4
Mvvv 68,228,192
s_lshl_b32 s94, s94, 2
Ta 136,25,10304
U 34,34,4
Mvvv 72,224,196
s_mul_i32 s95, s51, 20480
Ta 138,25,15424
U 35,35,4
Mvvv 76,228,196
W 6
U 36,36,4
U 37,37,4
Mvvv 80,224,200
s_add_u32 s95, s95, s48
Ta 140,26,10336
Ta 142,26,15456
Mvvv 84,228,200
s_add_u32 m0, s95, 61440
U 38,38,4
U 39,39,4
Mvvv 88,224,204
Ta 144,25,10368
K 32,32,33
Mvvv 92,228,204
W 5
Ta 146,25,15488
K 33,34,35
Zvaa 96,128,184
K 34,36,37
K 35,38,39
Zvaa 104,128,200
buffer_store_dwordx4 v[32:35], v1, s[8:11], s93 offen offset:0 sc1
Ta 148,26,10400
Zvaa 108,132,200
Ta 150,26,15520
U 40,40,4
Zvaa 100,132,184
W 4
U 41,41,4
U 42,42,4
Zvaa 112,136,184
Ta 152,25,10432
Ta 154,25,15552
Zvaa 120,136,200
D 16,40,94,0
U 43,43,4
Zvaa 124,140,200
U 44,44,4
U 45,45,4
Zvaa 116,140,184
Ta 156,26,10464
Ta 158,26,15584
W 4
Zvaa 128,144,184
U 46,46,4
U 47,47,4
Zvaa 136,144,200
D 17,40,94,1024
K 40,40,41
Zvaa 140,148,200
Ta 128,25,10496
Ta 130,25,15616
Zvaa 132,148,184
Ta 132,26,10528
Ta 134,26,15648
W 4
Zvaa 144,152,184
K 41,42,43
K 42,44,45
Zvaa 152,152,200
D 18,40,94,2048
K 43,46,47
Zvaa 156,156,200
Ta 136,25,20480
Ta 138,25,25600
Zvaa 148,156,184
Ta 140,26,20512
Ta 142,26,25632
W 4
Zvaa 160,128,184
Ta 144,25,20544
Ta 146,25,25664
Zvaa 168,128,200
D 19,40,94,3072
Ta 148,26,20576
Zvaa 172,132,200
s_add_u32 m0, s95, 65536
Ta 150,26,25696
Ta 152,25,20608
Zvaa 164,132,184
W 5
Ta 154,25,25728
Ta 156,26,20640
Mvaa 96,136,188
Ta 158,26,25760
Ta 128,25,20672
Mvaa 104,136,204
buffer_load_dwordx4 v20, s[40:43], s94 offen lds
Ta 130,25,25792
Mvaa 108,140,204
s_lshl_b32 s94, s58, 2
s_add_u32 s94, s94, s51
s_add_u32 s94, s94, 9
Mvaa 100,140,188
W 6
s_and_b32 s94, s94, 15
s_mul_i32 s94, s94, 10240
Mvaa 112,144,188
s_add_u32 s94, s94, s48
s_lshl_b32 s94, s94, 2
s_mul_i32 s95, s51, 10240
Mvaa 120,144,204
s_add_u32 s95, s95, s48
buffer_store_dwordx4 v[40:43], v1, s[8:11], s93 offen offset:1024 sc1
U 48,48,4
Mvaa 124,148,204
s_add_u32 m0, s95, 112640
U 49,49,4
U 50,50,4
Mvaa 116,148,188
Ta 132,26,20704
Ta 134,26,25824
W 4
Mvaa 128,152,188
U 51,51,4
U 52,52,4
Mvaa 136,152,204
U 53,53,4
U 54,54,4
Mvaa 140,156,204
U 55,55,4
K 48,48,49
Mvaa 132,156,188
Ta 136,25,20736
Ta 138,25,25856
)"
R"(Ta 140,26,20768
Ta 142,26,25888
W 4
Mvaa 144,128,188
K 49,50,51
K 50,52,53
Mvaa 152,128,204
D 16,40,94,0
K 51,54,55
Mvaa 156,132,204
Ta 144,25,30720
Ta 146,25,35840
Mvaa 148,132,188
Ta 148,26,30752
Ta 150,26,35872
W 4
Mvaa 160,136,188
Ta 152,25,30784
Ta 154,25,35904
Mvaa 168,136,204
D 17,40,94,1024
D 18,40,94,2048
D 19,40,94,3072
s_add_u32 m0, s95, 116736
s_nop 0
buffer_load_dwordx4 v20, s[40:43], s94 offen lds
s_waitcnt vmcnt(12)
s_cmp_lg_u32 s70, 0
s_cbranch_scc1 .Lpd_93%=
.Lpdr_94%=:
Mvaa 172,140,204
s_add_u32 s81, s58, 2
Ta 156,26,30816
Ta 158,26,35936
Mvaa 164,140,188
W 4
Ta 128,25,30848
Ta 130,25,35968
Mvaa 96,144,192
s_and_b32 s81, s81, 3
Ta 132,26,30880
Ta 134,26,36000
Mvaa 104,144,208
buffer_store_dwordx4 v[48:51], v1, s[8:11], s93 offen offset:2048 sc1
U 56,56,4
Mvaa 108,148,208
s_lshl_b32 s81, s81, 12
U 57,57,4
U 58,58,4
Mvaa 100,148,192
W 4
U 59,59,4
U 60,60,4
Mvaa 112,152,192
U 61,61,4
U 62,62,4
Mvaa 120,152,208
U 63,63,4
K 56,56,57
Mvaa 124,156,208
s_add_u32 s81, s81, s49
K 57,58,59
K 58,60,61
Mvaa 116,156,192
Ta 136,25,30912
Ta 138,25,36032
Ta 140,26,30944
Ta 142,26,36064
W 4
Mvaa 128,128,192
Ta 144,25,30976
K 59,62,63
Mvaa 136,128,208
buffer_store_dwordx4 v[56:59], v1, s[8:11], s93 offen offset:3072 sc1
U 64,64,5
Mvaa 140,132,208
s_add_u32 s82, s81, 131072
U 65,65,5
U 66,66,5
Mvaa 132,132,192
Ta 146,25,36096
Ta 148,26,31008
Ta 150,26,36128
W 4
Mvaa 144,136,192
U 67,67,5
U 68,68,5
Mvaa 152,136,208
U 69,69,5
U 70,70,5
Mvaa 156,140,208
U 71,71,5
K 64,64,65
Mvaa 148,140,192
Ta 152,21,0
Ta 154,21,5120
Ta 156,22,32
Ta 158,22,5152
W 4
Mvaa 160,144,192
K 65,66,67
K 66,68,69
Mvaa 168,144,208
Ta 128,21,64
K 67,70,71
Mvaa 172,148,208
s_add_u32 s67, s93, 131072
buffer_store_dwordx4 v[64:67], v1, s[8:11], s67 offen offset:0 sc1
U 72,72,5
Mvaa 164,148,192
Ta 130,21,5184
Ta 132,22,96
Ta 134,22,5216
W 4
Mvaa 96,152,196
U 73,73,5
U 74,74,5
Mvaa 104,152,212
U 75,75,5
U 76,76,5
Mvaa 108,156,212
U 77,77,5
U 78,78,5
Mvaa 100,156,196
Ta 136,21,128
Ta 138,21,5248
Ta 140,22,160
Ta 142,22,5280
W 4
Mvaa 112,128,196
U 79,79,5
K 72,72,73
Mvaa 120,128,212
K 73,74,75
K 74,76,77
Mvaa 124,132,212
buffer_load_dwordx4 a[0:3], v1, s[8:11], s81 offen offset:0 sc1
K 75,78,79
Mvaa 116,132,196
Ta 144,21,192
Ta 146,21,5312
Ta 148,22,224
Ta 150,22,5344
W 4
Mvaa 128,136,196
Ta 152,21,256
Ta 154,21,5376
Mvaa 136,136,212
Ta 156,22,288
Ta 158,22,5408
Mvaa 140,140,212
buffer_store_dwordx4 v[72:75], v1, s[8:11], s67 offen offset:1024 sc1
U 80,80,5
Mvaa 132,140,196
W 4
U 81,81,5
U 82,82,5
Mvaa 144,144,196
U 83,83,5
U 84,84,5
Mvaa 152,144,212
U 85,85,5
U 86,86,5
Mvaa 156,148,212
U 87,87,5
K 80,80,81
Mvaa 148,148,196
Ta 128,21,10240
Ta 130,21,15360
Ta 132,22,10272
Ta 134,22,15392
W 4
Mvaa 160,152,196
K 81,82,83
K 82,84,85
Mvaa 168,152,212
buffer_load_dwordx4 a[16:19], v1, s[8:11], s82 offen offset:0 sc1
K 83,86,87
Mvaa 172,156,212
Ta 136,21,10304
Ta 138,21,15424
Mvaa 164,156,196
Ta 140,22,10336
Ta 142,22,15456
W 4
Mvaa 96,128,216
Ta 144,21,10368
Ta 146,21,15488
Mvaa 104,128,232
buffer_store_dwordx4 v[80:83], v1, s[8:11], s67 offen offset:2048 sc1
U 88,88,5
Mvaa 108,132,232
U 89,89,5
U 90,90,5
Mvaa 100,132,216
Ta 148,22,10400
Ta 150,22,15520
W 4
Mvaa 112,136,216
U 91,91,5
U 92,92,5
Mvaa 120,136,232
U 93,93,5
U 94,94,5
Mvaa 124,140,232
U 95,95,5
K 88,88,89
Mvaa 116,140,216
Ta 152,21,10432
Ta 154,21,15552
Ta 156,22,10464
Ta 158,22,15584
W 4
Mvaa 128,144,216
K 89,90,91
K 90,92,93
Mvaa 136,144,232
buffer_load_dwordx4 a[4:7], v1, s[8:11], s81 offen offset:1024 sc1
K 91,94,95
Mvaa 140,148,232
Ta 128,21,10496
Ta 130,21,15616
Mvaa 132,148,216
Ta 132,22,10528
Ta 134,22,15648
W 4
Mvaa 144,152,216
Ta 136,21,20480
Ta 138,21,25600
Mvaa 152,152,232
buffer_store_dwordx4 v[88:91], v1, s[8:11], s67 offen offset:3072 sc1
Ta 140,22,20512
Mvaa 156,156,232
Ta 142,22,25632
Ta 144,21,20544
Mvaa 148,156,216
W 5
Ta 146,21,25664
Ta 148,22,20576
Mvaa 160,128,216
Ta 150,22,25696
Ta 152,21,20608
Mvaa 168,128,232
buffer_load_dwordx4 a[20:23], v1, s[8:11], s82 offen offset:1024 sc1
Ta 154,21,25728
Mvaa 172,132,232
Ta 156,22,20640
Ta 158,22,25760
Mvaa 164,132,216
W 8
Ta 128,21,20672
Ta 130,21,25792
Mvaa 96,136,220
Mvaa 104,136,236
buffer_load_dwordx4 a[8:11], v1, s[8:11], s81 offen offset:2048 sc1
Ta 132,22,20704
Mvaa 108,140,236
Ta 134,22,25824
Mvaa 100,140,220
W 8
Ta 136,21,20736
Ta 138,21,25856
Mvaa 112,144,220
Mvaa 120,144,236
buffer_load_dwordx4 a[24:27], v1, s[8:11], s82 offen offset:2048 sc1
Ta 140,22,20768
Mvaa 124,148,236
Ta 142,22,25888
Mvaa 116,148,220
W 8
Ta 144,21,30720
Ta 146,21,35840
Mvaa 128,152,220
Mvaa 136,152,236
buffer_load_dwordx4 a[12:15], v1, s[8:11], s81 offen offset:3072 sc1
Ta 148,22,30752
Mvaa 140,156,236
Ta 150,22,35872
Mvaa 132,156,220
W 8
Ta 152,21,30784
Ta 154,21,35904
Mvaa 144,128,220
Mvaa 152,128,236
buffer_load_dwordx4 a[28:31], v1, s[8:11], s82 offen offset:3072 sc1
Ta 156,22,30816
Mvaa 156,132,236
s_waitcnt vmcnt(14)
s_barrier
Ta 158,22,35936
Mvaa 148,132,220
W 8
s_lshl_b32 s94, s58, 2
Ta 128,21,30848
Mvaa 160,136,220
s_add_u32 s94, s94, s51
Ta 130,21,35968
Mvaa 168,136,236
s_add_u32 s94, s94, 11
Ta 132,22,30880
Ta 134,22,36000
Mvaa 172,140,236
s_and_b32 s94, s94, 15
Mvaa 164,140,220
W 8
Ta 136,21,30912
Ta 138,21,36032
Mvaa 96,144,224
s_mul_i32 s94, s94, 10240
Mvaa 104,144,240
s_add_u32 s94, s94, s48
Ta 140,22,30944
Ta 142,22,36064
Mvaa 108,148,240
s_lshl_b32 s94, s94, 2
Mvaa 100,148,224
W 8
Ta 144,21,30976
Ta 146,21,36096
Mvaa 112,152,224
s_mul_i32 s95, s51, 10240
Mvaa 120,152,240
s_add_u32 s95, s95, s48
Ta 148,22,31008
Ta 150,22,36128
Mvaa 124,156,240
s_add_u32 m0, s95, 40960
Mvaa 116,156,224
W 8
Ta 152,23,0
Ta 154,23,5120
Mvaa 128,128,224
D 16,40,94,0
Mvaa 136,128,240
Ta 156,24,32
Ta 158,24,5152
Mvaa 140,132,240
Mvaa 132,132,224
W 8
Ta 128,23,64
Ta 130,23,5184
Mvaa 144,136,224
D 17,40,94,1024
Mvaa 152,136,240
Ta 132,24,96
Ta 134,24,5216
Mvaa 156,140,240
Mvaa 148,140,224
W 8
Ta 136,23,128
Ta 138,23,5248
Mvaa 160,144,224
D 18,40,94,2048
Mvaa 168,144,240
Ta 140,24,160
Ta 142,24,5280
Mvaa 172,148,240
Mvaa 164,148,224
W 8
Ta 144,23,192
Ta 146,23,5312
Mvaa 96,152,228
D 19,40,94,3072
Mvaa 104,152,244
s_add_u32 m0, s95, 45056
Ta 148,24,224
Ta 150,24,5344
Mvaa 108,156,244
Mvaa 100,156,228
W 8
Ta 152,23,256
Ta 154,23,5376
Mvaa 112,128,228
buffer_load_dwordx4 v20, s[40:43], s94 offen lds
Mvaa 120,128,244
s_lshl_b32 s80, s58, 3
s_add_u32 s80, s80, s55
Ta 156,24,288
Mvaa 124,132,244
Ta 158,24,5408
Mvaa 116,132,228
W 8
Ta 128,23,20480
Ta 130,23,25600
Mvaa 128,136,228
s_waitcnt vmcnt(10)
v_mov_b64 v[28:29], s[12:13]
s_mov_b64 exec, 1
buffer_store_dwordx2 v[28:29], off, s[8:11], s80 sc1
s_mov_b64 exec, -1
s_lshl_b32 s94, s58, 2
s_add_u32 s94, s94, s51
Mvaa 136,136,244
s_add_u32 s94, s94, 13
s_and_b32 s94, s94, 15
s_mul_i32 s94, s94, 10240
Mvaa 140,140,244
s_add_u32 s94, s94, s48
s_lshl_b32 s94, s94, 2
s_mul_i32 s95, s51, 20480
)"
R"(Mvaa 132,140,228
W 6
s_add_u32 s95, s95, s48
s_add_u32 m0, s95, 71680
Mvaa 144,144,228
D 16,40,94,0
Ta 132,24,20512
Mvaa 152,144,244
Ta 134,24,25632
Ta 136,23,20544
Mvaa 156,148,244
Ta 138,23,25664
Ta 140,24,20576
Mvaa 148,148,228
W 7
Ta 142,24,25696
Ta 144,23,20608
Mvaa 160,152,228
D 17,40,94,1024
Ta 146,23,25728
Mvaa 168,152,244
Ta 148,24,20640
Ta 150,24,25760
Mvaa 172,156,244
Mvaa 164,156,228
W 8
Ta 152,23,20672
Ta 154,23,25792
Mvaa 96,128,0
D 18,40,94,2048
Mvaa 104,128,16
Ta 156,24,20704
Ta 158,24,25824
Mvaa 108,132,16
Mvaa 100,132,0
W 8
Ta 128,23,20736
Ta 130,23,25856
Mvaa 112,136,0
D 19,40,94,3072
Mvaa 120,136,16
s_add_u32 m0, s95, 75776
Ta 132,24,20768
Ta 134,24,25888
Mvaa 124,140,16
Mvaa 116,140,0
W 8
Ta 136,23,51200
Ta 138,23,56320
Mvaa 128,144,0
buffer_load_dwordx4 v20, s[40:43], s94 offen lds
Mvaa 136,144,16
s_lshl_b32 s94, s58, 2
s_add_u32 s94, s94, 15
s_and_b32 s94, s94, 15
Mvaa 140,148,16
s_mul_i32 s94, s94, 10240
s_add_u32 s94, s94, s48
s_lshl_b32 s94, s94, 2
Mvaa 132,148,0
W 6
s_mul_i32 s95, s51, 0
s_add_u32 s95, s95, s48
Mvaa 144,152,0
s_add_u32 m0, s95, 102400
s_nop 0
D 16,40,94,0
Ta 140,24,51232
Mvaa 152,152,16
Ta 142,24,56352
Ta 144,23,51264
Mvaa 156,156,16
Ta 146,23,56384
Ta 148,24,51296
Mvaa 148,156,0
W 7
Ta 150,24,56416
Ta 152,23,51328
Mvaa 160,128,0
D 17,40,94,1024
Ta 154,23,56448
Mvaa 168,128,16
Ta 156,24,51360
Ta 158,24,56480
Mvaa 172,132,16
Mvaa 164,132,0
s_waitcnt vmcnt(17) lgkmcnt(8)
Ta 128,23,51392
Ta 130,23,56512
Mvaa 96,136,4
D 18,40,94,2048
Mvaa 104,136,20
Ta 132,24,51424
Ta 134,24,56544
Mvaa 108,140,20
Mvaa 100,140,4
W 8
Ta 136,23,51456
Ta 138,23,56576
Mvaa 112,144,4
D 19,40,94,3072
Mvaa 120,144,20
s_add_u32 m0, s95, 106496
Ta 140,24,51488
Ta 142,24,56608
Mvaa 124,148,20
Mvaa 116,148,4
W 8
Ta 144,25,0
Ta 146,25,5120
Mvaa 128,152,4
buffer_load_dwordx4 v20, s[40:43], s94 offen lds
Mvaa 136,152,20
s_add_u32 s81, s58, 3
s_and_b32 s81, s81, 3
s_lshl_b32 s81, s81, 12
Mvaa 140,156,20
s_add_u32 s81, s81, s49
s_add_u32 s82, s81, 131072
Ta 148,26,32
Mvaa 132,156,4
W 7
Ta 150,26,5152
Ta 152,25,64
Mvaa 144,128,4
buffer_load_dwordx4 a[32:35], v1, s[8:11], s81 offen offset:0 sc1
Ta 154,25,5184
Mvaa 152,128,20
Ta 156,26,96
Ta 158,26,5216
Mvaa 156,132,20
Mvaa 148,132,4
W 8
Ta 128,25,128
Ta 130,25,5248
Mvaa 160,136,4
buffer_load_dwordx4 a[48:51], v1, s[8:11], s82 offen offset:0 sc1
Mvaa 168,136,20
Ta 132,26,160
Ta 134,26,5280
Mvaa 172,140,20
Mvaa 164,140,4
s_waitcnt vmcnt(20) lgkmcnt(8)
Ta 136,25,192
Ta 138,25,5312
Mvaa 96,144,8
s_waitcnt vmcnt(2)
s_barrier
buffer_load_dwordx4 a[36:39], v1, s[8:11], s81 offen offset:1024 sc1
Mvaa 104,144,24
Ta 140,26,224
v_and_b32 v27, 1, v12
Mvaa 108,148,24
Ta 142,26,5344
Mvaa 100,148,8
W 8
Ta 144,25,256
v_lshlrev_b32 v27, 1, v27
Mvaa 112,152,8
buffer_load_dwordx4 a[52:55], v1, s[8:11], s82 offen offset:1024 sc1
Ta 146,25,5376
Mvaa 120,152,24
Ta 148,26,288
v_xor_b32 v27, v27, v13
Mvaa 124,156,24
Ta 150,26,5408
Mvaa 116,156,8
W 8
Ta 152,21,40960
Ta 154,21,46080
Mvaa 128,128,8
buffer_load_dwordx4 a[40:43], v1, s[8:11], s81 offen offset:2048 sc1
v_and_b32 v12, 2, v12
Mvaa 136,128,24
Ta 156,22,40992
Ta 158,22,46112
Mvaa 140,132,24
v_or_b32 v12, v12, v14
Mvaa 132,132,8
W 8
Ta 128,21,41024
Ta 130,21,46144
Mvaa 144,136,8
buffer_load_dwordx4 a[56:59], v1, s[8:11], s82 offen offset:2048 sc1
Mvaa 152,136,24
Ta 132,22,41056
v_lshl_or_b32 v12, v27, 2, v12
Mvaa 156,140,24
Ta 134,22,46176
Mvaa 148,140,8
W 8
Ta 136,21,41088
v_cmp_eq_u32_e64 s[86:87], 0, v12
Mvaa 160,144,8
buffer_load_dwordx4 a[44:47], v1, s[8:11], s81 offen offset:3072 sc1
Ta 138,21,46208
Mvaa 168,144,24
Ta 140,22,41120
Ta 142,22,46240
Mvaa 172,148,24
v_cndmask_b32_e64 v4, 0, v15, s[86:87]
Mvaa 164,148,8
W 8
Ta 144,21,41152
Ta 146,21,46272
Mvaa 96,152,12
buffer_load_dwordx4 a[60:63], v1, s[8:11], s82 offen offset:3072 sc1
v_cmp_eq_u32_e64 s[86:87], 1, v12
Mvaa 104,152,28
Ta 148,22,41184
Ta 150,22,46304
Mvaa 108,156,28
Mvaa 100,156,12
W 8
Ta 152,21,41216
v_cndmask_b32_e64 v5, 0, v15, s[86:87]
Mvaa 112,128,12
Ta 154,21,46336
Mvaa 120,128,28
Ta 156,22,41248
v_cmp_eq_u32_e64 s[86:87], 2, v12
Mvaa 124,132,28
Ta 158,22,46368
Mvaa 116,132,12
W 8
Ta 128,21,51200
Ta 130,21,56320
Mvaa 128,136,12
v_cndmask_b32_e64 v6, 0, v15, s[86:87]
Mvaa 136,136,28
Ta 132,22,51232
Ta 134,22,56352
Mvaa 140,140,28
v_cmp_eq_u32_e64 s[86:87], 3, v12
Mvaa 132,140,12
W 8
Ta 136,21,51264
Ta 138,21,56384
Mvaa 144,144,12
v_bfe_u32 v28, v1, 4, 2
Mvaa 152,144,28
v_cndmask_b32_e64 v7, 0, v15, s[86:87]
v_lshlrev_b32 v28, 3, v28
Mvaa 156,148,28
Ta 140,22,51296
Ta 142,22,56416
Mvaa 148,148,12
W 8
buffer_load_dwordx2 v[28:29], v28, s[8:11], s55 offen sc1
v_cmp_eq_u32_e64 s[86:87], 4, v12
Mvaa 160,152,12
Ta 144,21,51328
Ta 146,21,56448
Mvaa 168,152,28
Ta 148,22,51360
Ta 150,22,56480
Mvaa 172,156,28
v_cndmask_b32_e64 v8, 0, v15, s[86:87]
Mvaa 164,156,12
s_waitcnt vmcnt(7) lgkmcnt(8)
Ta 152,21,51392
Ta 154,21,56512
Mvaa 96,128,32
v_cmp_eq_u32_e64 s[86:87], 5, v12
Mvaa 104,128,48
Ta 156,22,51424
Ta 158,22,56544
Mvaa 108,132,48
Mvaa 100,132,32
W 8
Ta 128,21,51456
v_cndmask_b32_e64 v9, 0, v15, s[86:87]
Mvaa 112,136,32
Ta 130,21,56576
Mvaa 120,136,48
Ta 132,22,51488
v_cmp_eq_u32_e64 s[86:87], 6, v12
Mvaa 124,140,48
Ta 134,22,56608
Mvaa 116,140,32
W 8
Ta 136,23,10240
Ta 138,23,15360
Mvaa 128,144,32
v_cndmask_b32_e64 v10, 0, v15, s[86:87]
Mvaa 136,144,48
Ta 140,24,10272
Ta 142,24,15392
Mvaa 140,148,48
v_cmp_eq_u32_e64 s[86:87], 7, v12
Mvaa 132,148,32
W 8
Ta 144,23,10304
Ta 146,23,15424
Mvaa 144,152,32
Mvaa 152,152,48
Ta 148,24,10336
v_cndmask_b32_e64 v11, 0, v15, s[86:87]
Mvaa 156,156,48
Ta 150,24,15456
Mvaa 148,156,32
W 8
s_mov_b32 s20, s38
Ta 152,23,10368
Mvaa 160,128,32
Ta 154,23,15488
Mvaa 168,128,48
Ta 156,24,10400
Ta 158,24,15520
Mvaa 172,132,48
s_and_b32 s21, s39, 0xffff
Mvaa 164,132,32
s_waitcnt vmcnt(5) lgkmcnt(8)
Ta 128,23,10432
Ta 130,23,15552
Mvaa 96,136,36
s_mov_b32 s22, 2621440
Mvaa 104,136,52
Ta 132,24,10464
Ta 134,24,15584
Mvaa 108,140,52
Mvaa 100,140,36
W 8
s_mov_b32 s23, 0x20000
Ta 136,23,10496
Mvaa 112,144,36
Ta 138,23,15616
Mvaa 120,144,52
s_and_b32 s17, s17, 0xffff
Ta 140,24,10528
Ta 142,24,15648
Mvaa 124,148,52
Mvaa 116,148,36
W 8
Ta 144,23,30720
Ta 146,23,35840
Mvaa 128,152,36
s_mov_b32 s18, 2621440
Mvaa 136,152,52
Ta 148,24,30752
Ta 150,24,35872
Mvaa 140,156,52
s_mov_b32 s19, 0x20000
Mvaa 132,156,36
W 8
Ta 152,23,30784
Ta 154,23,35904
Mvaa 144,128,36
Mvaa 152,128,52
s_lshr_b32 s66, s15, 3
Ta 156,24,30816
Ta 158,24,35936
Mvaa 156,132,52
s_waitcnt vmcnt(0)
v_cmp_eq_u64 vcc, s[12:13], v[28:29]
s_cmp_eq_u64 vcc, -1
s_cbranch_scc0 .Lspin_95%=
.Lok_96%=:
s_add_u32 s81, s58, 1
Mvaa 148,132,36
W 8
s_lshl_b32 s66, s66, 4
Ta 128,23,30848
Mvaa 160,136,36
Ta 130,23,35968
Mvaa 168,136,52
Ta 132,24,30880
Ta 134,24,36000
Mvaa 172,140,52
s_and_b32 s81, s81, 3
s_add_u32 s66, s66, s59
Mvaa 164,140,36
W 8
Ta 136,23,30912
Ta 138,23,36032
Mvaa 96,144,40
s_lshl_b32 s63, s66, 6
Mvaa 104,144,56
s_lshl_b32 s81, s81, 12
Ta 140,24,30944
Ta 142,24,36064
Mvaa 108,148,56
Mvaa 100,148,40
W 8
s_add_u32 s63, s63, 65536
Ta 144,23,30976
Mvaa 112,152,40
s_add_u32 s81, s81, s50
Ta 146,23,36096
Mvaa 120,152,56
s_mul_i32 s62, s66, 40960
Ta 148,24,31008
Ta 150,24,36128
Mvaa 124,156,56
Mvaa 116,156,40
W 8
s_add_u32 s82, s81, 131072
Ta 152,23,40960
Mvaa 128,128,40
s_add_u32 s62, s62, 56623104
)"
R"(Ta 154,23,46080
Mvaa 136,128,56
Ta 156,24,40992
Ta 158,24,46112
Mvaa 140,132,56
s_lshl_b32 s66, s14, 10
buffer_load_dwordx4 a[64:67], v1, s[8:11], s81 offen offset:0 sc1
Mvaa 132,132,40
W 8
Ta 128,23,41024
Ta 130,23,46144
Mvaa 144,136,40
Mvaa 152,136,56
s_add_u32 s78, s62, s66
Ta 132,24,41056
Ta 134,24,46176
Mvaa 156,140,56
buffer_load_dwordx4 a[80:83], v1, s[8:11], s82 offen offset:0 sc1
Mvaa 148,140,40
W 8
s_add_u32 s79, s78, 327680
Ta 136,23,41088
Mvaa 160,144,40
Ta 138,23,46208
Mvaa 168,144,56
Ta 140,24,41120
Ta 142,24,46240
Mvaa 172,148,56
s_and_b32 s66, s14, 1
buffer_load_dwordx4 a[68:71], v1, s[8:11], s81 offen offset:1024 sc1
Mvaa 164,148,40
W 8
Ta 144,23,41152
Ta 146,23,46272
Mvaa 96,152,44
s_mul_i32 s66, s66, 327680
Mvaa 104,152,60
Ta 148,24,41184
Ta 150,24,46304
Mvaa 108,156,60
buffer_load_dwordx4 a[84:87], v1, s[8:11], s82 offen offset:1024 sc1
Mvaa 100,156,44
W 8
s_add_u32 s65, s62, s66
Ta 152,23,41216
Mvaa 112,128,44
Ta 154,23,46336
Mvaa 120,128,60
s_add_u32 s65, s65, 4096
Ta 156,24,41248
Ta 158,24,46368
Mvaa 124,132,60
buffer_load_dwordx4 a[72:75], v1, s[8:11], s81 offen offset:2048 sc1
Mvaa 116,132,44
W 8
Ta 128,25,10240
Ta 130,25,15360
Mvaa 128,136,44
s_mov_b32 s64, 0
Mvaa 136,136,60
Ta 132,26,10272
Ta 134,26,15392
Mvaa 140,140,60
buffer_load_dwordx4 a[88:91], v1, s[8:11], s82 offen offset:2048 sc1
v_bfe_u32 v27, v1, 4, 3
Mvaa 132,140,44
W 8
Ta 136,25,10304
K 96,96,97
Mvaa 144,144,44
Ta 138,25,15424
v_lshlrev_b32 v27, 3, v27
Mvaa 152,144,60
Ta 140,26,10336
K 97,98,99
Mvaa 156,148,60
buffer_load_dwordx4 a[76:79], v1, s[8:11], s81 offen offset:3072 sc1
Ta 142,26,15456
Mvaa 148,148,44
W 8
v_bfe_u32 v20, v1, 4, 4
K 98,100,101
Mvaa 160,152,44
Ta 144,25,10368
Ta 146,25,15488
Mvaa 168,152,60
Ta 148,26,10400
v_mul_u32_u24 v20, 1280, v20
Mvaa 172,156,60
buffer_load_dwordx4 a[92:95], v1, s[8:11], s82 offen offset:3072 sc1
Ta 150,26,15520
Mvaa 164,156,44
W 8
s_add_u32 s81, s58, 2
K 99,102,103
Zvav 176,128,32
s_and_b32 s81, s81, 3
Ta 152,25,10432
Ta 154,25,15552
Zvav 184,128,64
s_add_u32 s83, s52, 0
s_lshl_b32 s81, s81, 12
v_lshrrev_b32 v12, 8, v1
Zvav 188,132,64
Ta 156,26,10464
Ta 158,26,15584
Zvav 180,132,32
W 8
buffer_store_dwordx4 v[96:99], v1, s[8:11], s83 offen sc1
v_lshl_add_u32 v20, v12, 4, v20
Zvav 192,136,32
s_add_u32 s81, s81, s50
Ta 128,25,10496
Ta 130,25,15616
Zvav 200,136,64
s_bfe_u32 s66, s59, 0x10001
Ta 132,26,10528
K 104,104,105
Zvav 204,140,64
Ta 134,26,15648
Zvav 196,140,32
W 8
s_add_u32 s82, s81, 131072
K 105,106,107
Zvav 208,144,32
s_lshl_b32 s67, s57, 1
Ta 136,25,20480
Ta 138,25,25600
Zvav 216,144,64
Ta 140,26,20512
K 106,108,109
Zvav 220,148,64
s_add_u32 s66, s66, s67
Ta 142,26,25632
buffer_load_dwordx4 a[96:99], v1, s[8:11], s81 offen offset:0 sc1
Zvav 212,148,32
W 8
Ta 144,25,20544
Ta 146,25,25664
Zvav 224,152,32
s_lshl_b32 s61, s66, 2
K 107,110,111
Zvav 232,152,64
Ta 148,26,20576
Ta 150,26,25696
Zvav 236,156,64
s_add_u32 s83, s52, 40960
buffer_load_dwordx4 a[112:115], v1, s[8:11], s82 offen offset:0 sc1
Zvav 228,156,32
W 8
s_lshr_b32 s66, s59, 2
Ta 152,25,20608
Zvav 240,128,32
Ta 154,25,25728
Zvav 248,128,64
s_lshl_b32 s66, s66, 1
Ta 156,26,20640
Ta 158,26,25760
Zvav 252,132,64
buffer_load_dwordx4 a[100:103], v1, s[8:11], s81 offen offset:1024 sc1
Zvav 244,132,32
W 8
Ta 128,25,20672
Ta 130,25,25792
Mvav 176,136,40
s_add_u32 s61, s61, s66
Mvav 184,136,72
Ta 132,26,20704
Ta 134,26,25824
Mvav 188,140,72
s_and_b32 s66, s59, 1
buffer_store_dwordx4 v[104:107], v1, s[8:11], s83 offen sc1
K 112,112,113
Mvav 180,140,40
W 8
K 113,114,115
K 114,116,117
Mvav 192,144,40
Ta 136,25,20736
Ta 138,25,25856
Mvav 200,144,72
s_add_u32 s61, s61, s66
Ta 140,26,20768
Ta 142,26,25888
Mvav 204,148,72
buffer_load_dwordx4 a[116:119], v1, s[8:11], s82 offen offset:1024 sc1
K 115,118,119
Mvav 196,148,40
W 8
s_xor_b32 s67, s61, 0
Ta 144,25,30720
Mvav 208,152,40
s_add_u32 s83, s52, 1024
Ta 146,25,35840
Mvav 216,152,72
Ta 148,26,30752
Ta 150,26,35872
Mvav 220,156,72
s_mul_i32 s72, s67, 20480
buffer_load_dwordx4 a[104:107], v1, s[8:11], s81 offen offset:2048 sc1
Mvav 212,156,40
W 8
Ta 152,25,30784
Ta 154,25,35904
Mvav 224,128,40
s_mul_i32 s66, s58, 320
Mvav 232,128,72
Ta 156,26,30816
Ta 158,26,35936
Mvav 236,132,72
buffer_load_dwordx4 a[120:123], v1, s[8:11], s82 offen offset:2048 sc1
Mvav 228,132,40
W 8
s_add_u32 s72, s72, s66
Ta 128,25,30848
Mvav 240,136,40
Ta 130,25,35968
Mvav 248,136,72
s_mul_i32 s75, s67, 20480
Ta 132,26,30880
Ta 134,26,36000
Mvav 252,140,72
buffer_load_dwordx4 a[108:111], v1, s[8:11], s81 offen offset:3072 sc1
Mvav 244,140,40
W 8
Ta 136,25,30912
Ta 138,25,36032
Mvav 176,144,48
s_mul_i32 s66, s58, 2560
Mvav 184,144,80
Ta 140,26,30944
Ta 142,26,36064
Mvav 188,148,80
s_add_u32 s75, s75, s66
buffer_store_dwordx4 v[112:115], v1, s[8:11], s83 offen sc1
K 120,120,121
Mvav 180,148,48
W 8
K 121,122,123
K 122,124,125
Mvav 192,152,48
Ta 144,25,30976
K 123,126,127
Mvav 200,152,80
s_add_u32 s83, s52, 41984
s_add_u32 s75, s75, 131072
Ta 146,25,36096
Mvav 204,156,80
buffer_load_dwordx4 a[124:127], v1, s[8:11], s82 offen offset:3072 sc1
Ta 148,26,31008
Mvav 196,156,48
W 7
s_add_u32 s81, s58, 3
s_and_b32 s81, s81, 3
Mvav 208,128,48
s_lshl_b32 s81, s81, 12
s_add_u32 s81, s81, s50
Ta 150,26,36128
Mvav 216,128,80
s_add_u32 s82, s81, 131072
s_xor_b32 s67, s61, 124
Ta 152,21,0
Mvav 220,132,80
s_mul_i32 s73, s67, 20480
buffer_store_dwordx4 v[120:123], v1, s[8:11], s83 offen sc1
K 128,128,129
Mvav 212,132,48
W 5
Ta 154,21,5120
K 129,130,131
Mvav 224,136,48
s_mul_i32 s66, s58, 320
Ta 156,22,32
Ta 158,22,5152
Mvav 232,136,80
Ta 128,21,64
K 130,132,133
Mvav 236,140,80
buffer_load_dwordx4 a[184:187], v1, s[8:11], s81 offen offset:0 sc1
Ta 130,21,5184
Mvav 228,140,48
W 6
Ta 132,22,96
Ta 134,22,5216
Mvav 240,144,48
s_add_u32 s73, s73, s66
Ta 136,21,128
K 131,134,135
Mvav 248,144,80
s_add_u32 s83, s52, 2048
s_mul_i32 s76, s67, 20480
Ta 138,21,5248
Mvav 252,148,80
buffer_load_dwordx4 a[200:203], v1, s[8:11], s82 offen offset:0 sc1
Ta 140,22,160
Mvav 244,148,48
W 7
Ta 142,22,5280
Ta 144,21,192
Mvav 176,152,56
s_mul_i32 s66, s58, 2560
Ta 146,21,5312
Mvav 184,152,88
Ta 148,22,224
Ta 150,22,5344
Mvav 188,156,88
s_add_u32 s76, s76, s66
buffer_load_dwordx4 a[188:191], v1, s[8:11], s81 offen offset:1024 sc1
Mvav 180,156,56
W 8
Ta 152,21,256
Ta 154,21,5376
Mvav 192,128,56
Mvav 200,128,88
s_add_u32 s76, s76, 131072
Ta 156,22,288
Ta 158,22,5408
Mvav 204,132,88
buffer_store_dwordx4 v[128:131], v1, s[8:11], s83 offen sc1
K 136,136,137
Mvav 196,132,56
W 8
K 137,138,139
K 138,140,141
Mvav 208,136,56
Ta 128,21,10240
v_bfe_u32 v15, v1, 7, 1
Mvav 216,136,88
Ta 130,21,15360
K 139,142,143
Mvav 220,140,88
s_add_u32 s83, s52, 43008
buffer_load_dwordx4 a[204:207], v1, s[8:11], s82 offen offset:1024 sc1
Ta 132,22,10272
Mvav 212,140,56
W 7
Ta 134,22,15392
v_mul_u32_u24 v14, 10240, v15
Mvav 224,144,56
Ta 136,21,10304
Ta 138,21,15424
Mvav 232,144,88
Ta 140,22,10336
v_lshrrev_b32 v15, 8, v1
Mvav 236,148,88
buffer_load_dwordx4 a[192:195], v1, s[8:11], s81 offen offset:2048 sc1
Ta 142,22,15456
Mvav 228,148,56
W 8
Ta 144,21,10368
v_lshl_add_u32 v14, v15, 7, v14
Mvav 240,152,56
Ta 146,21,15488
Mvav 248,152,88
Ta 148,22,10400
v_and_b32 v15, 0x70, v1
Mvav 252,156,88
buffer_load_dwordx4 a[208:211], v1, s[8:11], s82 offen offset:2048 sc1
Ta 150,22,15520
Mvav 244,156,56
s_waitcnt vmcnt(25) lgkmcnt(8)
Ta 152,21,10432
Ta 154,21,15552
Mvaa 176,128,64
v_add_u32 v14, v14, v15
Mvaa 184,128,80
Ta 156,22,10464
Ta 158,22,15584
Mvaa 188,132,80
s_bitcmp1_b32 s14, 0
s_cselect_b32 s74, s73, s72
s_cselect_b32 s77, s76, s75
buffer_load_dwordx4 a[196:199], v1, s[8:11], s81 offen offset:3072 sc1
Mvaa 180,132,64
W 8
Ta 128,21,10496
Ta 130,21,15616
Mvaa 192,136,64
Mvaa 200,136,80
s_add_u32 s74, s74, 256
Ta 132,22,10528
Ta 134,22,15648
Mvaa 204,140,80
buffer_load_dwordx4 a[212:215], v1, s[8:11], s82 offen offset:3072 sc1
Mvaa 196,140,64
W 8
s_add_u32 s77, s77, 2048
Ta 136,21,20480
Mvaa 208,144,64
Ta 138,21,25600
Mvaa 216,144,80
Ta 140,22,20512
Ta 142,22,25632
Mvaa 220,148,80
s_lshl_b32 s66, s14, 6
buffer_store_dwordx4 v[136:139], v1, s[8:11], s83 offen sc1
K 144,144,145
Mvaa 212,148,64
W 8
K 145,146,147
)"
R"(K 146,148,149
Mvaa 224,152,64
s_lshl_b32 s67, s14, 9
Ta 144,21,20544
K 147,150,151
Mvaa 232,152,80
s_add_u32 s83, s52, 3072
Ta 146,21,25664
Ta 148,22,20576
Mvaa 236,156,80
buffer_store_dwordx4 v[144:147], v1, s[8:11], s83 offen sc1
K 152,152,153
Mvaa 228,156,64
W 7
K 153,154,155
K 154,156,157
Mvaa 240,128,64
s_add_u32 s72, s72, s66
Ta 150,22,25696
Ta 152,21,20608
Mvaa 248,128,80
s_add_u32 s75, s75, s67
Ta 154,21,25728
K 155,158,159
Mvaa 252,132,80
s_add_u32 s83, s52, 44032
Ta 156,22,20640
Ta 158,22,25760
Mvaa 244,132,64
s_waitcnt vmcnt(27) lgkmcnt(8)
Ta 128,21,20672
Ta 130,21,25792
Mvaa 176,136,68
s_add_u32 s73, s73, s66
buffer_store_dwordx4 v[152:155], v1, s[8:11], s83 offen sc1
Mvaa 184,136,84
Ta 132,22,20704
Ta 134,22,25824
Mvaa 188,140,84
s_add_u32 s76, s76, s67
K 160,160,161
Mvaa 180,140,68
W 8
Ta 136,21,20736
Ta 138,21,25856
Mvaa 192,144,68
buffer_load_dwordx4 v[36:39], v20, s[16:19], s72 offen
Mvaa 200,144,84
Ta 140,22,20768
K 161,162,163
Mvaa 204,148,84
Ta 142,22,25888
Mvaa 196,148,68
W 8
Ta 144,21,30720
K 162,164,165
Mvaa 208,152,68
Ta 146,21,35840
Mvaa 216,152,84
Ta 148,22,30752
Ta 150,22,35872
Mvaa 220,156,84
buffer_load_dwordx4 v[44:47], v14, s[8:11], s75 offen sc1
K 163,166,167
Mvaa 212,156,68
W 8
Ta 152,21,30784
Ta 154,21,35904
Mvaa 224,128,68
s_add_u32 s83, s52, 4096
Mvaa 232,128,84
Ta 156,22,30816
Ta 158,22,35936
Mvaa 236,132,84
buffer_store_dwordx4 v[160:163], v1, s[8:11], s83 offen sc1
Mvaa 228,132,68
W 8
Ta 128,21,30848
Ta 130,21,35968
Mvaa 240,136,68
K 168,168,169
Mvaa 248,136,84
Ta 132,22,30880
Ta 134,22,36000
Mvaa 252,140,84
buffer_load_dwordx4 v[52:55], v20, s[16:19], s73 offen
Mvaa 244,140,68
s_waitcnt vmcnt(30) lgkmcnt(8)
Ta 136,21,30912
K 169,170,171
Mvaa 176,144,72
Ta 138,21,36032
Mvaa 184,144,88
Ta 140,22,30944
K 170,172,173
Mvaa 188,148,88
Ta 142,22,36064
buffer_load_dwordx4 v[60:63], v14, s[8:11], s76 offen sc1
Mvaa 180,148,72
W 8
Ta 144,21,30976
Ta 146,21,36096
Mvaa 192,152,72
K 171,174,175
Mvaa 200,152,88
Ta 148,22,31008
Ta 150,22,36128
Mvaa 204,156,88
s_add_u32 s83, s52, 45056
Mvaa 196,156,72
W 8
buffer_load_dwordx4 v[68:71], v20, s[16:19], s74 offen
Ta 152,23,0
Mvaa 208,128,72
Ta 154,23,5120
Mvaa 216,128,88
Ta 156,24,32
Ta 158,24,5152
Mvaa 220,132,88
Mvaa 212,132,72
W 8
buffer_store_dwordx4 v[168:171], v1, s[8:11], s83 offen sc1
Ta 128,23,64
Mvaa 224,136,72
Ta 130,23,5184
Mvaa 232,136,88
Ta 132,24,96
Ta 134,24,5216
Mvaa 236,140,88
Mvaa 228,140,72
W 8
buffer_load_dwordx4 v[76:79], v14, s[8:11], s77 offen sc1
Ta 136,23,128
Mvaa 240,144,72
Ta 138,23,5248
Mvaa 248,144,88
Ta 140,24,160
Ta 142,24,5280
Mvaa 252,148,88
Mvaa 244,148,72
s_waitcnt vmcnt(32) lgkmcnt(8)
buffer_load_dwordx2 v[12:13], v27, s[8:11], 0 offen offset:3584 sc1
Ta 144,23,192
Mvaa 176,152,76
Ta 146,23,5312
Mvaa 184,152,92
Ta 148,24,224
Ta 150,24,5344
Mvaa 188,156,92
Mvaa 180,156,76
W 8
Ta 152,23,256
Ta 154,23,5376
Mvaa 192,128,76
Mvaa 200,128,92
Ta 156,24,288
Ta 158,24,5408
Mvaa 204,132,92
Mvaa 196,132,76
W 8
Ta 128,23,20480
Ta 130,23,25600
Mvaa 208,136,76
Mvaa 216,136,92
Ta 132,24,20512
Ta 134,24,25632
Mvaa 220,140,92
Mvaa 212,140,76
W 8
Ta 136,23,51200
Ta 138,23,56320
Mvaa 224,144,76
Mvaa 232,144,92
Ta 140,24,51232
Ta 142,24,56352
Mvaa 236,148,92
Mvaa 228,148,76
W 8
Ta 144,25,0
Ta 146,25,5120
Mvaa 240,152,76
Mvaa 248,152,92
Ta 148,26,32
Ta 150,26,5152
Mvaa 252,156,92
Mvaa 244,156,76
s_waitcnt vmcnt(30) lgkmcnt(8)
Ta 152,21,40960
Ta 154,21,46080
Mvaa 176,128,96
Mvaa 184,128,112
Ta 156,22,40992
Ta 158,22,46112
Mvaa 188,132,112
Mvaa 180,132,96
s_waitcnt vmcnt(27) lgkmcnt(8)
Ta 128,21,51200
Ta 130,21,56320
Mvaa 176,136,100
Mvaa 184,136,116
Ta 132,22,51232
Ta 134,22,56352
Mvaa 188,140,116
Mvaa 180,140,100
s_waitcnt vmcnt(25) lgkmcnt(8)
Ta 136,23,10240
Ta 138,23,15360
Mvaa 176,144,104
Mvaa 184,144,120
Ta 140,24,10272
Ta 142,24,15392
Mvaa 188,148,120
Mvaa 180,148,104
s_waitcnt vmcnt(22) lgkmcnt(8)
Ta 144,23,30720
Ta 146,23,35840
Mvaa 176,152,108
Mvaa 184,152,124
Ta 148,24,30752
Ta 150,24,35872
Mvaa 188,156,124
Mvaa 180,156,108
s_waitcnt vmcnt(19) lgkmcnt(8)
Ta 152,23,40960
Ta 154,23,46080
Mvaa 176,128,184
Mvaa 184,128,200
Ta 156,24,40992
Ta 158,24,46112
Mvaa 188,132,200
Mvaa 180,132,184
s_waitcnt vmcnt(16) lgkmcnt(8)
Ta 128,23,20544
Ta 130,23,25664
Mvaa 176,136,188
Mvaa 184,136,204
Ta 132,24,20576
Ta 134,24,25696
Mvaa 188,140,204
Mvaa 180,140,188
s_waitcnt vmcnt(14) lgkmcnt(8)
Ta 136,23,51264
Ta 138,23,56384
Mvaa 176,144,192
Mvaa 184,144,208
Ta 140,24,51296
Ta 142,24,56416
Mvaa 188,148,208
Mvaa 180,148,192
s_waitcnt vmcnt(12) lgkmcnt(8)
Ta 144,25,64
Ta 146,25,5184
Mvaa 176,152,196
Mvaa 184,152,212
Ta 148,26,96
Ta 150,26,5216
Mvaa 188,156,212
Mvaa 180,156,196
W 8
Ta 152,21,41024
Ta 154,21,46144
Mvaa 192,128,96
Mvaa 200,128,112
Ta 156,22,41056
Ta 158,22,46176
Mvaa 204,132,112
K 176,176,177
Mvaa 196,132,96
W 8
Ta 128,21,51264
Ta 130,21,56384
Mvaa 192,136,100
K 177,178,179
Mvaa 200,136,116
Ta 132,22,51296
K 178,180,181
Mvaa 204,140,116
Ta 134,22,56416
K 179,182,183
Mvaa 196,140,100
W 8
Ta 136,23,10304
Ta 138,23,15424
Mvaa 192,144,104
s_add_u32 s83, s52, 327680
Mvaa 200,144,120
buffer_store_dwordx4 v[176:179], v1, s[8:11], s83 offen sc1
Ta 140,24,10336
Mvaa 204,148,120
Ta 142,24,15456
Mvaa 196,148,104
s_waitcnt vmcnt(3)
v_lshlrev_b32 v2, 2, v1
v_mov_b64 v[28:29], s[12:13]
s_mov_b64 exec, 3
buffer_store_dwordx2 v[28:29], v2, s[8:11], s53 offen offset:0 sc1
s_mov_b64 exec, -1
K 184,184,185
W 8
Mvaa 192,152,108
K 185,186,187
K 186,188,189
Mvaa 200,152,124
s_waitcnt vmcnt(2)
v_xor_b32 v12, s12, v12
v_xor_b32 v13, s13, v13
v_lshrrev_b32 v3, 11, v12
v_or_b32 v13, v13, v3
v_cmp_eq_u32 vcc, 0, v13
s_cmp_eq_u64 vcc, -1
s_cbranch_scc0 .LL_100%=
.LL_101%=:
v_bfe_u32 v13, v12, 4, 7
s_xor_b32 s66, s61, 124
v_cmp_eq_u32 vcc, s61, v13
v_cmp_eq_u32_e64 s[86:87], s66, v13
s_or_b32 s68, vcc_lo, s86
s_and_b32 s68, s68, 0xff
s_cbranch_scc1 .Lup_98%=
.Lupr_99%=:
Ta 144,23,30784
K 187,190,191
Mvaa 204,156,124
s_add_u32 s83, s52, 368640
Ta 146,23,35904
Ta 148,24,30816
Mvaa 196,156,108
W 7
buffer_store_dwordx4 v[184:187], v1, s[8:11], s83 offen sc1
Ta 150,24,35936
Mvaa 192,128,184
Ta 152,23,41024
v_lshlrev_b32 v32, 16, v36
Mvaa 200,128,200
Ta 154,23,46144
v_and_b32 v33, 0xffff0000, v36
Mvaa 204,132,200
Ta 156,24,41056
v_lshlrev_b32 v34, 16, v37
Mvaa 196,132,184
W 7
Ta 158,24,46176
v_and_b32 v35, 0xffff0000, v37
Mvaa 192,136,188
Ta 128,23,20608
v_lshlrev_b32 v36, 16, v38
Mvaa 200,136,204
Ta 130,23,25728
v_and_b32 v37, 0xffff0000, v38
Mvaa 204,140,204
Ta 132,24,20640
v_lshlrev_b32 v38, 16, v39
Mvaa 196,140,188
W 7
Ta 134,24,25760
v_and_b32 v39, 0xffff0000, v39
Mvaa 192,144,192
Ta 136,23,51328
v_lshlrev_b32 v40, 16, v44
Mvaa 200,144,208
Ta 138,23,56448
v_and_b32 v41, 0xffff0000, v44
Mvaa 204,148,208
Ta 140,24,51360
v_lshlrev_b32 v42, 16, v45
Mvaa 196,148,192
W 7
Ta 142,24,56480
v_and_b32 v43, 0xffff0000, v45
Mvaa 192,152,196
Ta 144,25,128
v_lshlrev_b32 v44, 16, v46
Mvaa 200,152,212
Ta 146,25,5248
v_and_b32 v45, 0xffff0000, v46
Mvaa 204,156,212
Ta 148,26,160
v_lshlrev_b32 v46, 16, v47
Mvaa 196,156,196
W 7
Ta 150,26,5280
v_and_b32 v47, 0xffff0000, v47
Mvaa 208,128,96
Ta 152,21,41088
v_lshlrev_b32 v48, 16, v52
Mvaa 216,128,112
Ta 154,21,46208
v_and_b32 v49, 0xffff0000, v52
Mvaa 220,132,112
Ta 156,22,41120
v_lshlrev_b32 v50, 16, v53
Mvaa 212,132,96
W 7
Ta 158,22,46240
v_and_b32 v51, 0xffff0000, v53
Mvaa 208,136,100
K 192,192,193
v_lshlrev_b32 v52, 16, v54
Mvaa 216,136,116
Ta 128,21,51328
)"
R"(Ta 130,21,56448
Mvaa 220,140,116
K 193,194,195
v_and_b32 v53, 0xffff0000, v54
Mvaa 212,140,100
W 6
v_lshlrev_b32 v54, 16, v55
K 194,196,197
Mvaa 208,144,104
Ta 132,22,51360
Ta 134,22,56480
Mvaa 216,144,120
v_and_b32 v55, 0xffff0000, v55
K 195,198,199
Mvaa 220,148,120
s_add_u32 s83, s52, 328704
Ta 136,23,10368
v_lshlrev_b32 v56, 16, v60
Mvaa 212,148,104
W 5
Ta 138,23,15488
v_and_b32 v57, 0xffff0000, v60
Mvaa 208,152,108
buffer_store_dwordx4 v[192:195], v1, s[8:11], s83 offen sc1
v_lshlrev_b32 v58, 16, v61
Mvaa 216,152,124
K 200,200,201
K 201,202,203
Mvaa 220,156,124
K 202,204,205
K 203,206,207
Mvaa 212,156,108
Ta 140,24,10400
Ta 142,24,15520
W 4
Mvaa 208,128,184
s_add_u32 s83, s52, 369664
buffer_store_dwordx4 v[200:203], v1, s[8:11], s83 offen sc1
v_and_b32 v59, 0xffff0000, v61
Mvaa 216,128,200
Ta 144,23,30848
v_lshlrev_b32 v60, 16, v62
Mvaa 220,132,200
Ta 146,23,35968
v_and_b32 v61, 0xffff0000, v62
Mvaa 212,132,184
Ta 148,24,30880
Ta 150,24,36000
W 4
Mvaa 208,136,188
v_lshlrev_b32 v62, 16, v63
v_and_b32 v63, 0xffff0000, v63
Mvaa 216,136,204
Ta 152,23,41088
v_lshlrev_b32 v64, 16, v68
Mvaa 220,140,204
Ta 154,23,46208
v_and_b32 v65, 0xffff0000, v68
Mvaa 212,140,188
Ta 156,24,41120
Ta 158,24,46240
W 4
Mvaa 208,144,192
v_lshlrev_b32 v66, 16, v69
v_and_b32 v67, 0xffff0000, v69
Mvaa 216,144,208
Ta 128,23,20672
v_lshlrev_b32 v68, 16, v70
Mvaa 220,148,208
Ta 130,23,25792
v_and_b32 v69, 0xffff0000, v70
Mvaa 212,148,192
Ta 132,24,20704
Ta 134,24,25824
W 4
Mvaa 208,152,196
v_lshlrev_b32 v70, 16, v71
v_and_b32 v71, 0xffff0000, v71
Mvaa 216,152,212
Ta 136,23,51392
v_lshlrev_b32 v72, 16, v76
Mvaa 220,156,212
Ta 138,23,56512
v_and_b32 v73, 0xffff0000, v76
Mvaa 212,156,196
Ta 140,24,51424
Ta 142,24,56544
W 4
Mvaa 224,128,96
v_lshlrev_b32 v74, 16, v77
v_and_b32 v75, 0xffff0000, v77
Mvaa 232,128,112
Ta 144,25,192
v_lshlrev_b32 v76, 16, v78
Mvaa 236,132,112
Ta 146,25,5312
v_and_b32 v77, 0xffff0000, v78
Mvaa 228,132,96
Ta 148,26,224
Ta 150,26,5344
W 4
Mvaa 224,136,100
v_lshlrev_b32 v78, 16, v79
v_and_b32 v79, 0xffff0000, v79
Mvaa 232,136,116
Ta 152,21,41152
Ta 154,21,46272
Mvaa 236,140,116
Ta 156,22,41184
Ta 158,22,46304
Mvaa 228,140,100
W 4
Ta 128,21,51392
K 208,208,209
Mvaa 224,144,104
Ta 130,21,56512
K 209,210,211
Mvaa 232,144,120
Ta 132,22,51424
K 210,212,213
Mvaa 236,148,120
Ta 134,22,56544
K 211,214,215
Mvaa 228,148,104
W 4
Ta 136,23,10432
Ta 138,23,15552
Mvaa 224,152,108
s_add_u32 s83, s52, 329728
buffer_store_dwordx4 v[208:211], v1, s[8:11], s83 offen sc1
K 216,216,217
Mvaa 232,152,124
K 217,218,219
K 218,220,221
Mvaa 236,156,124
Ta 140,24,10464
K 219,222,223
Mvaa 228,156,108
s_add_u32 s83, s52, 370688
Ta 142,24,15584
W 4
Mvaa 224,128,184
buffer_store_dwordx4 v[216:219], v1, s[8:11], s83 offen sc1
Ta 144,23,30912
Mvaa 232,128,200
Ta 146,23,36032
Ta 148,24,30944
Mvaa 236,132,200
Ta 150,24,36064
Ta 152,23,41152
Mvaa 228,132,184
W 5
Ta 154,23,46272
Ta 156,24,41184
Mvaa 224,136,188
Ta 158,24,46304
Ta 128,23,20736
Mvaa 232,136,204
Ta 130,23,25856
Ta 132,24,20768
Mvaa 236,140,204
Ta 134,24,25888
Mvaa 228,140,188
W 8
Ta 136,23,51456
Ta 138,23,56576
Mvaa 224,144,192
Mvaa 232,144,208
Ta 140,24,51488
Ta 142,24,56608
Mvaa 236,148,208
Mvaa 228,148,192
W 8
Ta 144,25,256
Ta 146,25,5376
Mvaa 224,152,196
Mvaa 232,152,212
Ta 148,26,288
Ta 150,26,5408
Mvaa 236,156,212
Mvaa 228,156,196
W 8
Ta 152,21,41216
Ta 154,21,46336
Mvaa 240,128,96
Mvaa 248,128,112
Ta 156,22,41248
Ta 158,22,46368
Mvaa 252,132,112
K 224,224,225
Mvaa 244,132,96
W 8
Ta 128,21,51456
Ta 130,21,56576
Mvaa 240,136,100
K 225,226,227
Mvaa 248,136,116
Ta 132,22,51488
K 226,228,229
Mvaa 252,140,116
Ta 134,22,56608
K 227,230,231
Mvaa 244,140,100
W 8
Ta 136,23,10496
Ta 138,23,15616
Mvaa 240,144,104
s_add_u32 s83, s52, 330752
Mvaa 248,144,120
buffer_store_dwordx4 v[224:227], v1, s[8:11], s83 offen sc1
K 232,232,233
Mvaa 252,148,120
K 233,234,235
K 234,236,237
Mvaa 244,148,104
W 6
Ta 140,24,10528
K 235,238,239
Mvaa 240,152,108
s_add_u32 s83, s52, 371712
Ta 142,24,15648
Ta 144,23,30976
Mvaa 248,152,124
buffer_store_dwordx4 v[232:235], v1, s[8:11], s83 offen sc1
Ta 146,23,36096
Mvaa 252,156,124
Ta 148,24,31008
Ta 150,24,36128
Mvaa 244,156,108
W 8
Ta 152,23,41216
Ta 154,23,46336
Mvaa 240,128,184
Mvaa 248,128,200
Ta 156,24,41248
Ta 158,24,46368
Mvaa 252,132,200
Mvaa 244,132,184
W 8
Mvaa 240,136,188
Mvaa 248,136,204
Mvaa 252,140,204
Mvaa 244,140,188
W 4
Mvaa 240,144,192
Mvaa 248,144,208
Mvaa 252,148,208
Mvaa 244,148,192
W 0
Mvaa 240,152,196
Mvaa 248,152,212
Mvaa 252,156,212
Mvaa 244,156,196
s_nop 4
K 240,240,241
K 241,242,243
s_nop 0
K 242,244,245
K 243,246,247
s_add_u32 s83, s52, 331776
buffer_store_dwordx4 v[240:243], v1, s[8:11], s83 offen sc1
K 248,248,249
K 249,250,251
K 250,252,253
K 251,254,255
s_add_u32 s83, s52, 372736
buffer_store_dwordx4 v[248:251], v1, s[8:11], s83 offen sc1
s_branch .Ltend_92%=
.Llate_91%=:
ds_read_b32 v11, v11 offset:59392
ds_read_b64 v[8:9], v10 offset:57344
Rv 176,3,0
Rv 180,3,1024
Rv 184,3,2048
Rv 188,3,3072
Rv 192,3,4096
Rv 196,3,5120
Rv 200,3,6144
Rv 204,3,7168
W 6
Mvvv 32,224,176
v_cmp_ne_u32 vcc, 0, v11
s_cmp_lg_u64 vcc, 0
s_cbranch_scc1 .Lredo%=
s_lshl_b32 s94, s58, 2
Mvvv 36,228,176
A 4,4,8
Mvvv 40,224,180
s_add_u32 s94, s94, s51
v_rcp_f32 v4, v4
Mvvv 44,228,180
W 4
s_add_u32 s94, s94, 7
A 5,5,9
Mvvv 48,224,184
Ta 128,25,10240
Ta 130,25,15360
Mvvv 52,228,184
s_and_b32 s94, s94, 15
v_rcp_f32 v5, v5
Mvvv 56,224,188
s_lshl_b32 s93, s58, 12
s_add_u32 s93, s93, s50
Ta 132,26,10272
Mvvv 60,228,188
W 5
s_mul_i32 s94, s94, 10240
Ta 134,26,15392
Mvvv 64,224,192
s_add_u32 s94, s94, s48
U 32,32,4
U 33,33,4
Mvvv 68,228,192
s_lshl_b32 s94, s94, 2
Ta 136,25,10304
U 34,34,4
Mvvv 72,224,196
s_mul_i32 s95, s51, 20480
Ta 138,25,15424
U 35,35,4
Mvvv 76,228,196
W 6
U 36,36,4
U 37,37,4
Mvvv 80,224,200
s_add_u32 s95, s95, s48
Ta 140,26,10336
Ta 142,26,15456
Mvvv 84,228,200
s_add_u32 m0, s95, 61440
U 38,38,4
U 39,39,4
Mvvv 88,224,204
K 32,32,33
K 33,34,35
K 34,36,37
K 35,38,39
buffer_store_dwordx4 v[32:35], v1, s[8:11], s93 offen offset:0 sc1
U 64,64,5
U 65,65,5
U 66,66,5
U 67,67,5
U 68,68,5
U 69,69,5
U 70,70,5
U 71,71,5
K 64,64,65
K 65,66,67
K 66,68,69
K 67,70,71
s_add_u32 s67, s93, 131072
Mvvv 92,228,204
W 4
Ta 144,25,10368
Ta 146,25,15488
Zvav 176,128,32
Ta 148,26,10400
Ta 150,26,15520
Zvav 184,128,64
Ta 152,25,10432
Ta 154,25,15552
Zvav 188,132,64
D 16,40,94,0
Ta 156,26,10464
Zvav 180,132,32
W 7
Ta 158,26,15584
Ta 128,25,10496
Zvav 192,136,32
Ta 130,25,15616
Zvav 200,136,64
Ta 132,26,10528
Ta 134,26,15648
Zvav 204,140,64
D 17,40,94,1024
Zvav 196,140,32
W 8
Ta 136,25,20480
Ta 138,25,25600
Zvav 208,144,32
Zvav 216,144,64
Ta 140,26,20512
Ta 142,26,25632
Zvav 220,148,64
D 18,40,94,2048
Zvav 212,148,32
W 8
Ta 144,25,20544
Ta 146,25,25664
Zvav 224,152,32
Zvav 232,152,64
Ta 148,26,20576
Ta 150,26,25696
Zvav 236,156,64
D 19,40,94,3072
Zvav 228,156,32
W 8
s_add_u32 m0, s95, 65536
Ta 152,25,20608
Zvav 240,128,32
)"
R"(Ta 154,25,25728
Zvav 248,128,64
Ta 156,26,20640
Ta 158,26,25760
Zvav 252,132,64
buffer_store_dwordx4 v[64:67], v1, s[8:11], s67 offen offset:0 sc1
U 40,40,4
U 41,41,4
U 42,42,4
U 43,43,4
U 44,44,4
U 45,45,4
U 46,46,4
U 47,47,4
K 40,40,41
K 41,42,43
K 42,44,45
K 43,46,47
buffer_store_dwordx4 v[40:43], v1, s[8:11], s93 offen offset:1024 sc1
U 72,72,5
U 73,73,5
U 74,74,5
U 75,75,5
U 76,76,5
U 77,77,5
U 78,78,5
U 79,79,5
K 72,72,73
K 73,74,75
K 74,76,77
K 75,78,79
Zvav 244,132,32
W 8
Ta 128,25,20672
Ta 130,25,25792
Mvav 176,136,40
Mvav 184,136,72
Ta 132,26,20704
Ta 134,26,25824
Mvav 188,140,72
buffer_load_dwordx4 v20, s[40:43], s94 offen lds
Mvav 180,140,40
W 8
s_lshl_b32 s94, s58, 2
s_add_u32 s94, s94, s51
Mvav 192,144,40
s_add_u32 s94, s94, 9
s_and_b32 s94, s94, 15
s_mul_i32 s94, s94, 10240
Mvav 200,144,72
s_add_u32 s94, s94, s48
s_lshl_b32 s94, s94, 2
s_mul_i32 s95, s51, 10240
Mvav 204,148,72
s_add_u32 s95, s95, s48
s_add_u32 m0, s95, 112640
s_nop 0
D 16,40,94,0
Mvav 196,148,40
W 4
Ta 136,25,20736
Ta 138,25,25856
Mvav 208,152,40
Ta 140,26,20768
Ta 142,26,25888
Mvav 216,152,72
Ta 144,25,30720
Ta 146,25,35840
Mvav 220,156,72
D 17,40,94,1024
Ta 148,26,30752
Mvav 212,156,40
W 7
Ta 150,26,35872
Ta 152,25,30784
Mvav 224,128,40
Ta 154,25,35904
Mvav 232,128,72
Ta 156,26,30816
Ta 158,26,35936
Mvav 236,132,72
buffer_store_dwordx4 v[72:75], v1, s[8:11], s67 offen offset:1024 sc1
U 48,48,4
Mvav 228,132,40
W 8
U 49,49,4
U 50,50,4
Mvav 240,136,40
U 51,51,4
U 52,52,4
Mvav 248,136,72
D 18,40,94,2048
D 19,40,94,3072
s_add_u32 m0, s95, 116736
s_nop 0
buffer_load_dwordx4 v20, s[40:43], s94 offen lds
s_waitcnt vmcnt(14)
s_cmp_lg_u32 s70, 0
s_cbranch_scc1 .Lpd_103%=
.Lpdr_104%=:
Mvav 252,140,72
U 53,53,4
U 54,54,4
U 55,55,4
K 48,48,49
K 49,50,51
K 50,52,53
K 51,54,55
buffer_store_dwordx4 v[48:51], v1, s[8:11], s93 offen offset:2048 sc1
U 80,80,5
U 81,81,5
U 82,82,5
U 83,83,5
U 84,84,5
U 85,85,5
U 86,86,5
U 87,87,5
K 80,80,81
K 81,82,83
K 82,84,85
K 83,86,87
Mvav 244,140,40
W 4
Ta 128,25,30848
Ta 130,25,35968
Mvav 176,144,48
Ta 132,26,30880
Ta 134,26,36000
Mvav 184,144,80
Ta 136,25,30912
Ta 138,25,36032
Mvav 188,148,80
buffer_store_dwordx4 v[80:83], v1, s[8:11], s67 offen offset:2048 sc1
U 56,56,4
Mvav 180,148,48
W 6
U 57,57,4
U 58,58,4
Mvav 192,152,48
U 59,59,4
v_bfe_u32 v28, v1, 4, 2
Mvav 200,152,80
U 60,60,4
v_lshlrev_b32 v28, 3, v28
Mvav 204,156,80
Ta 140,26,30944
Ta 142,26,36064
Mvav 196,156,48
W 4
buffer_load_dwordx2 v[28:29], v28, s[8:11], s54 offen sc1
U 61,61,4
Mvav 208,128,48
U 62,62,4
U 63,63,4
Mvav 216,128,80
Ta 144,25,30976
K 56,56,57
Mvav 220,132,80
Ta 146,25,36096
K 57,58,59
Mvav 212,132,48
Ta 148,26,31008
Ta 150,26,36128
W 4
Mvav 224,136,48
K 58,60,61
K 59,62,63
Mvav 232,136,80
buffer_store_dwordx4 v[56:59], v1, s[8:11], s93 offen offset:3072 sc1
U 88,88,5
Mvav 236,140,80
Ta 152,21,0
U 89,89,5
Mvav 228,140,48
Ta 154,21,5120
Ta 156,22,32
Ta 158,22,5152
W 4
Mvav 240,144,48
U 90,90,5
U 91,91,5
Mvav 248,144,80
U 92,92,5
U 93,93,5
Mvav 252,148,80
U 94,94,5
U 95,95,5
K 88,88,89
K 89,90,91
K 90,92,93
K 91,94,95
Mvav 244,148,48
Ta 128,21,64
Ta 130,21,5184
Ta 132,22,96
Ta 134,22,5216
W 4
Mvav 176,152,56
buffer_store_dwordx4 v[88:91], v1, s[8:11], s67 offen offset:3072 sc1
Ta 136,21,128
Mvav 184,152,88
Ta 138,21,5248
Ta 140,22,160
Mvav 188,156,88
Ta 142,22,5280
Ta 144,21,192
Mvav 180,156,56
W 5
Ta 146,21,5312
Ta 148,22,224
Mvav 192,128,56
Ta 150,22,5344
Ta 152,21,256
Mvav 200,128,88
Ta 154,21,5376
Ta 156,22,288
Mvav 204,132,88
Ta 158,22,5408
Mvav 196,132,56
W 8
Ta 128,25,10240
Ta 130,25,15360
Mvav 208,136,56
Mvav 216,136,88
Ta 132,26,10272
Ta 134,26,15392
Mvav 220,140,88
Mvav 212,140,56
W 8
Ta 136,25,10304
Ta 138,25,15424
Mvav 224,144,56
s_waitcnt vmcnt(2)
v_cmp_eq_u64 vcc, s[12:13], v[28:29]
s_cmp_eq_u64 vcc, -1
s_cbranch_scc0 .Lspin_105%=
.Lok_106%=:
s_add_u32 s81, s58, 0
Mvav 232,144,88
s_and_b32 s81, s81, 3
Ta 140,26,10336
Ta 142,26,15456
Mvav 236,148,88
s_lshl_b32 s81, s81, 12
Mvav 228,148,56
W 8
s_add_u32 s81, s81, s49
Ta 144,25,10368
Mvav 240,152,56
s_add_u32 s82, s81, 131072
Ta 146,25,15488
Mvav 248,152,88
buffer_load_dwordx4 a[184:187], v1, s[8:11], s81 offen offset:0 sc1
Ta 148,26,10400
Mvav 252,156,88
buffer_load_dwordx4 a[200:203], v1, s[8:11], s82 offen offset:0 sc1
Ta 150,26,15520
Mvav 244,156,56
s_waitcnt vmcnt(0) lgkmcnt(8)
Ta 152,25,10432
Ta 154,25,15552
Zvaa 96,128,184
Zvaa 104,128,200
Ta 156,26,10464
Ta 158,26,15584
Zvaa 108,132,200
buffer_load_dwordx4 a[188:191], v1, s[8:11], s81 offen offset:1024 sc1
Zvaa 100,132,184
W 8
Ta 128,25,10496
Ta 130,25,15616
Zvaa 112,136,184
Zvaa 120,136,200
Ta 132,26,10528
Ta 134,26,15648
Zvaa 124,140,200
buffer_load_dwordx4 a[204:207], v1, s[8:11], s82 offen offset:1024 sc1
Zvaa 116,140,184
W 8
Ta 136,25,20480
Ta 138,25,25600
Zvaa 128,144,184
Zvaa 136,144,200
Ta 140,26,20512
Ta 142,26,25632
Zvaa 140,148,200
buffer_load_dwordx4 a[192:195], v1, s[8:11], s81 offen offset:2048 sc1
Zvaa 132,148,184
W 8
Ta 144,25,20544
Ta 146,25,25664
Zvaa 144,152,184
Zvaa 152,152,200
Ta 148,26,20576
Ta 150,26,25696
Zvaa 156,156,200
buffer_load_dwordx4 a[208:211], v1, s[8:11], s82 offen offset:2048 sc1
Zvaa 148,156,184
W 8
Ta 152,25,20608
Ta 154,25,25728
Zvaa 160,128,184
Zvaa 168,128,200
Ta 156,26,20640
Ta 158,26,25760
Zvaa 172,132,200
buffer_load_dwordx4 a[196:199], v1, s[8:11], s81 offen offset:3072 sc1
Zvaa 164,132,184
s_waitcnt vmcnt(3) lgkmcnt(8)
Ta 128,25,20672
Ta 130,25,25792
Mvaa 96,136,188
Mvaa 104,136,204
Ta 132,26,20704
Ta 134,26,25824
Mvaa 108,140,204
buffer_load_dwordx4 a[212:215], v1, s[8:11], s82 offen offset:3072 sc1
Mvaa 100,140,188
W 8
s_lshl_b32 s80, s58, 3
s_add_u32 s80, s80, s55
Mvaa 112,144,188
Ta 136,25,20736
Ta 138,25,25856
Mvaa 120,144,204
Ta 140,26,20768
Ta 142,26,25888
Mvaa 124,148,204
v_mov_b64 v[28:29], s[12:13]
s_mov_b64 exec, 1
buffer_store_dwordx2 v[28:29], off, s[8:11], s80 sc1
s_mov_b64 exec, -1
Mvaa 116,148,188
W 8
Ta 144,25,30720
Ta 146,25,35840
Mvaa 128,152,188
Mvaa 136,152,204
Ta 148,26,30752
Ta 150,26,35872
Mvaa 140,156,204
Mvaa 132,156,188
W 8
Ta 152,25,30784
Ta 154,25,35904
Mvaa 144,128,188
Mvaa 152,128,204
Ta 156,26,30816
Ta 158,26,35936
Mvaa 156,132,204
s_barrier
s_lshl_b32 s94, s58, 2
Mvaa 148,132,188
W 8
Ta 128,25,30848
Ta 130,25,35968
Mvaa 160,136,188
s_add_u32 s94, s94, s51
Mvaa 168,136,204
s_add_u32 s94, s94, 11
Ta 132,26,30880
Ta 134,26,36000
Mvaa 172,140,204
s_and_b32 s94, s94, 15
Mvaa 164,140,188
s_waitcnt vmcnt(3) lgkmcnt(8)
Ta 136,25,30912
Ta 138,25,36032
Mvaa 96,144,192
s_mul_i32 s94, s94, 10240
Mvaa 104,144,208
s_add_u32 s94, s94, s48
Ta 140,26,30944
Ta 142,26,36064
Mvaa 108,148,208
s_lshl_b32 s94, s94, 2
Mvaa 100,148,192
W 8
Ta 144,25,30976
Ta 146,25,36096
Mvaa 112,152,192
s_mul_i32 s95, s51, 10240
Mvaa 120,152,208
s_add_u32 s95, s95, s48
Ta 148,26,31008
Ta 150,26,36128
Mvaa 124,156,208
s_add_u32 m0, s95, 40960
Mvaa 116,156,192
W 8
Ta 152,21,0
Ta 154,21,5120
Mvaa 128,128,192
D 16,40,94,0
Mvaa 136,128,208
Ta 156,22,32
Ta 158,22,5152
Mvaa 140,132,208
Mvaa 132,132,192
W 8
)"
R"(Ta 128,21,64
Ta 130,21,5184
Mvaa 144,136,192
D 17,40,94,1024
Mvaa 152,136,208
Ta 132,22,96
Ta 134,22,5216
Mvaa 156,140,208
Mvaa 148,140,192
W 8
Ta 136,21,128
Ta 138,21,5248
Mvaa 160,144,192
D 18,40,94,2048
Mvaa 168,144,208
Ta 140,22,160
Ta 142,22,5280
Mvaa 172,148,208
Mvaa 164,148,192
s_waitcnt vmcnt(4) lgkmcnt(8)
Ta 144,21,192
Ta 146,21,5312
Mvaa 96,152,196
D 19,40,94,3072
Mvaa 104,152,212
s_add_u32 m0, s95, 45056
Ta 148,22,224
Ta 150,22,5344
Mvaa 108,156,212
Mvaa 100,156,196
W 8
Ta 152,21,256
Ta 154,21,5376
Mvaa 112,128,196
buffer_load_dwordx4 v20, s[40:43], s94 offen lds
Mvaa 120,128,212
s_add_u32 s81, s58, 1
s_and_b32 s81, s81, 3
s_lshl_b32 s81, s81, 12
Mvaa 124,132,212
s_add_u32 s81, s81, s49
s_add_u32 s82, s81, 131072
Ta 156,22,288
Mvaa 116,132,196
W 7
Ta 158,22,5408
Ta 128,21,10240
Mvaa 128,136,196
buffer_load_dwordx4 a[216:219], v1, s[8:11], s81 offen offset:0 sc1
Ta 130,21,15360
Mvaa 136,136,212
Ta 132,22,10272
Ta 134,22,15392
Mvaa 140,140,212
Mvaa 132,140,196
W 8
Ta 136,21,10304
Ta 138,21,15424
Mvaa 144,144,196
buffer_load_dwordx4 a[232:235], v1, s[8:11], s82 offen offset:0 sc1
Mvaa 152,144,212
Ta 140,22,10336
Ta 142,22,15456
Mvaa 156,148,212
Mvaa 148,148,196
W 8
Ta 144,21,10368
Ta 146,21,15488
Mvaa 160,152,196
buffer_load_dwordx4 a[220:223], v1, s[8:11], s81 offen offset:1024 sc1
Mvaa 168,152,212
Ta 148,22,10400
Ta 150,22,15520
Mvaa 172,156,212
Mvaa 164,156,196
s_waitcnt vmcnt(1) lgkmcnt(8)
Ta 152,21,10432
Ta 154,21,15552
Mvaa 96,128,216
buffer_load_dwordx4 a[236:239], v1, s[8:11], s82 offen offset:1024 sc1
Mvaa 104,128,232
Ta 156,22,10464
Ta 158,22,15584
Mvaa 108,132,232
Mvaa 100,132,216
W 8
Ta 128,21,10496
Ta 130,21,15616
Mvaa 112,136,216
buffer_load_dwordx4 a[224:227], v1, s[8:11], s81 offen offset:2048 sc1
Mvaa 120,136,232
Ta 132,22,10528
Ta 134,22,15648
Mvaa 124,140,232
Mvaa 116,140,216
W 8
Ta 136,21,20480
Ta 138,21,25600
Mvaa 128,144,216
buffer_load_dwordx4 a[240:243], v1, s[8:11], s82 offen offset:2048 sc1
Mvaa 136,144,232
Ta 140,22,20512
Ta 142,22,25632
Mvaa 140,148,232
Mvaa 132,148,216
W 8
Ta 144,21,20544
Ta 146,21,25664
Mvaa 144,152,216
buffer_load_dwordx4 a[228:231], v1, s[8:11], s81 offen offset:3072 sc1
Mvaa 152,152,232
Ta 148,22,20576
Ta 150,22,25696
Mvaa 156,156,232
Mvaa 148,156,216
W 8
Ta 152,21,20608
Ta 154,21,25728
Mvaa 160,128,216
buffer_load_dwordx4 a[244:247], v1, s[8:11], s82 offen offset:3072 sc1
Mvaa 168,128,232
s_lshl_b32 s94, s58, 2
s_add_u32 s94, s94, s51
s_add_u32 s94, s94, 13
Mvaa 172,132,232
s_and_b32 s94, s94, 15
s_mul_i32 s94, s94, 10240
s_add_u32 s94, s94, s48
Mvaa 164,132,216
s_waitcnt vmcnt(4) lgkmcnt(6)
s_lshl_b32 s94, s94, 2
s_mul_i32 s95, s51, 20480
Mvaa 96,136,220
s_add_u32 s95, s95, s48
s_add_u32 m0, s95, 71680
s_nop 0
D 16,40,94,0
Mvaa 104,136,236
Ta 156,22,20640
Ta 158,22,25760
Mvaa 108,140,236
Ta 128,21,20672
Ta 130,21,25792
Mvaa 100,140,220
W 6
Ta 132,22,20704
Ta 134,22,25824
Mvaa 112,144,220
D 17,40,94,1024
Ta 136,21,20736
Mvaa 120,144,236
Ta 138,21,25856
Ta 140,22,20768
Mvaa 124,148,236
Ta 142,22,25888
Mvaa 116,148,220
W 8
Ta 144,21,30720
Ta 146,21,35840
Mvaa 128,152,220
D 18,40,94,2048
Mvaa 136,152,236
Ta 148,22,30752
Ta 150,22,35872
Mvaa 140,156,236
Mvaa 132,156,220
W 8
Ta 152,21,30784
Ta 154,21,35904
Mvaa 144,128,220
D 19,40,94,3072
Mvaa 152,128,236
s_add_u32 m0, s95, 75776
Ta 156,22,30816
Ta 158,22,35936
Mvaa 156,132,236
buffer_load_dwordx4 v20, s[40:43], s94 offen lds
s_lshl_b32 s94, s58, 2
s_add_u32 s94, s94, 15
s_and_b32 s94, s94, 15
s_mul_i32 s94, s94, 10240
s_add_u32 s94, s94, s48
s_lshl_b32 s94, s94, 2
s_mul_i32 s95, s51, 0
s_add_u32 s95, s95, s48
s_add_u32 m0, s95, 102400
s_nop 0
D 16,40,94,0
D 17,40,94,1024
D 18,40,94,2048
D 19,40,94,3072
s_add_u32 m0, s95, 106496
s_nop 0
buffer_load_dwordx4 v20, s[40:43], s94 offen lds
Mvaa 148,132,220
W 8
Ta 128,21,30848
Ta 130,21,35968
Mvaa 160,136,220
Mvaa 168,136,236
Ta 132,22,30880
Ta 134,22,36000
Mvaa 172,140,236
Mvaa 164,140,220
s_waitcnt vmcnt(12) lgkmcnt(8)
Ta 136,21,30912
Ta 138,21,36032
Mvaa 96,144,224
s_waitcnt vmcnt(0)
s_barrier
s_add_u32 s81, s58, 2
Mvaa 104,144,240
Ta 140,22,30944
v_and_b32 v27, 1, v12
Mvaa 108,148,240
Ta 142,22,36064
Mvaa 100,148,224
W 8
s_and_b32 s81, s81, 3
v_lshlrev_b32 v27, 1, v27
Mvaa 112,152,224
Ta 144,21,30976
Ta 146,21,36096
Mvaa 120,152,240
s_lshl_b32 s81, s81, 12
Ta 148,22,31008
Ta 150,22,36128
Mvaa 124,156,240
v_xor_b32 v27, v27, v13
Mvaa 116,156,224
W 8
Ta 152,23,0
Ta 154,23,5120
Mvaa 128,128,224
s_add_u32 s81, s81, s49
Mvaa 136,128,240
Ta 156,24,32
v_and_b32 v12, 2, v12
Mvaa 140,132,240
s_add_u32 s82, s81, 131072
Ta 158,24,5152
Mvaa 132,132,224
W 8
Ta 128,23,64
Ta 130,23,5184
Mvaa 144,136,224
v_or_b32 v12, v12, v14
Mvaa 152,136,240
buffer_load_dwordx4 a[0:3], v1, s[8:11], s81 offen offset:0 sc1
Ta 132,24,96
Mvaa 156,140,240
Ta 134,24,5216
Mvaa 148,140,224
W 8
Ta 136,23,128
v_lshl_or_b32 v12, v27, 2, v12
Mvaa 160,144,224
Ta 138,23,5248
Mvaa 168,144,240
buffer_load_dwordx4 a[16:19], v1, s[8:11], s82 offen offset:0 sc1
Ta 140,24,160
Mvaa 172,148,240
Ta 142,24,5280
v_cmp_eq_u32_e64 s[86:87], 0, v12
Mvaa 164,148,224
W 8
Ta 144,23,192
Ta 146,23,5312
Mvaa 96,152,228
Mvaa 104,152,244
buffer_load_dwordx4 a[4:7], v1, s[8:11], s81 offen offset:1024 sc1
v_cndmask_b32_e64 v4, 0, v15, s[86:87]
Mvaa 108,156,244
Ta 148,24,224
Ta 150,24,5344
Mvaa 100,156,228
W 8
Ta 152,23,256
Ta 154,23,5376
Mvaa 112,128,228
v_cmp_eq_u32_e64 s[86:87], 1, v12
Mvaa 120,128,244
buffer_load_dwordx4 a[20:23], v1, s[8:11], s82 offen offset:1024 sc1
Ta 156,24,288
Mvaa 124,132,244
Ta 158,24,5408
v_cndmask_b32_e64 v5, 0, v15, s[86:87]
Mvaa 116,132,228
W 8
Ta 128,23,20480
Ta 130,23,25600
Mvaa 128,136,228
Mvaa 136,136,244
buffer_load_dwordx4 a[8:11], v1, s[8:11], s81 offen offset:2048 sc1
v_cmp_eq_u32_e64 s[86:87], 2, v12
Mvaa 140,140,244
Ta 132,24,20512
Ta 134,24,25632
Mvaa 132,140,228
W 8
Ta 136,23,20544
Ta 138,23,25664
Mvaa 144,144,228
v_cndmask_b32_e64 v6, 0, v15, s[86:87]
Mvaa 152,144,244
buffer_load_dwordx4 a[24:27], v1, s[8:11], s82 offen offset:2048 sc1
Ta 140,24,20576
Mvaa 156,148,244
Ta 142,24,25696
Mvaa 148,148,228
W 8
Ta 144,23,20608
v_cmp_eq_u32_e64 s[86:87], 3, v12
Mvaa 160,152,228
Ta 146,23,25728
Mvaa 168,152,244
buffer_load_dwordx4 a[12:15], v1, s[8:11], s81 offen offset:3072 sc1
Ta 148,24,20640
Mvaa 172,156,244
Ta 150,24,25760
v_cndmask_b32_e64 v7, 0, v15, s[86:87]
Mvaa 164,156,228
s_waitcnt vmcnt(5) lgkmcnt(8)
Ta 152,23,20672
Ta 154,23,25792
Mvaa 96,128,0
Mvaa 104,128,16
buffer_load_dwordx4 a[28:31], v1, s[8:11], s82 offen offset:3072 sc1
v_cmp_eq_u32_e64 s[86:87], 4, v12
Mvaa 108,132,16
s_add_u32 s81, s58, 3
s_and_b32 s81, s81, 3
s_lshl_b32 s81, s81, 12
Mvaa 100,132,0
W 6
s_add_u32 s81, s81, s49
Ta 156,24,20704
Mvaa 112,136,0
s_add_u32 s82, s81, 131072
Ta 158,24,25824
Ta 128,23,20736
Mvaa 120,136,16
Ta 130,23,25856
v_cndmask_b32_e64 v8, 0, v15, s[86:87]
Mvaa 124,140,16
buffer_load_dwordx4 a[32:35], v1, s[8:11], s81 offen offset:0 sc1
Ta 132,24,20768
Mvaa 116,140,0
W 7
Ta 134,24,25888
v_cmp_eq_u32_e64 s[86:87], 5, v12
Mvaa 128,144,0
Ta 136,23,51200
Ta 138,23,56320
Mvaa 136,144,16
Ta 140,24,51232
Ta 142,24,56352
Mvaa 140,148,16
buffer_load_dwordx4 a[48:51], v1, s[8:11], s82 offen offset:0 sc1
v_cndmask_b32_e64 v9, 0, v15, s[86:87]
Mvaa 132,148,0
W 8
Ta 144,23,51264
Ta 146,23,56384
Mvaa 144,152,0
v_cmp_eq_u32_e64 s[86:87], 6, v12
Mvaa 152,152,16
Ta 148,24,51296
Ta 150,24,56416
Mvaa 156,156,16
buffer_load_dwordx4 a[36:39], v1, s[8:11], s81 offen offset:1024 sc1
Mvaa 148,156,0
W 8
Ta 152,23,51328
v_cndmask_b32_e64 v10, 0, v15, s[86:87]
Mvaa 160,128,0
Ta 154,23,56448
Mvaa 168,128,16
Ta 156,24,51360
Ta 158,24,56480
Mvaa 172,132,16
buffer_load_dwordx4 a[52:55], v1, s[8:11], s82 offen offset:1024 sc1
v_cmp_eq_u32_e64 s[86:87], 7, v12
Mvaa 164,132,0
s_waitcnt vmcnt(8) lgkmcnt(8)
Ta 128,23,51392
Ta 130,23,56512
Mvaa 96,136,4
Mvaa 104,136,20
Ta 132,24,51424
v_cndmask_b32_e64 v11, 0, v15, s[86:87]
Mvaa 108,140,20
buffer_load_dwordx4 a[40:43], v1, s[8:11], s81 offen offset:2048 sc1
Ta 134,24,56544
Mvaa 100,140,4
W 8
Ta 136,23,51456
Ta 138,23,56576
Mvaa 112,144,4
s_mov_b32 s20, s38
)"
R"(Mvaa 120,144,20
Ta 140,24,51488
Ta 142,24,56608
Mvaa 124,148,20
buffer_load_dwordx4 a[56:59], v1, s[8:11], s82 offen offset:2048 sc1
Mvaa 116,148,4
W 8
s_and_b32 s21, s39, 0xffff
Ta 144,25,0
Mvaa 128,152,4
Ta 146,25,5120
Mvaa 136,152,20
Ta 148,26,32
Ta 150,26,5152
Mvaa 140,156,20
s_mov_b32 s22, 2621440
buffer_load_dwordx4 a[44:47], v1, s[8:11], s81 offen offset:3072 sc1
Mvaa 132,156,4
W 8
Ta 152,25,64
Ta 154,25,5184
Mvaa 144,128,4
Mvaa 152,128,20
s_mov_b32 s23, 0x20000
Ta 156,26,96
Ta 158,26,5216
Mvaa 156,132,20
buffer_load_dwordx4 a[60:63], v1, s[8:11], s82 offen offset:3072 sc1
v_bfe_u32 v28, v1, 4, 2
Mvaa 148,132,4
W 8
Ta 128,25,128
v_lshlrev_b32 v28, 3, v28
Mvaa 160,136,4
s_and_b32 s17, s17, 0xffff
Ta 130,25,5248
Mvaa 168,136,20
Ta 132,26,160
Ta 134,26,5280
Mvaa 172,140,20
s_mov_b32 s18, 2621440
buffer_load_dwordx2 v[28:29], v28, s[8:11], s55 offen sc1
Mvaa 164,140,4
s_waitcnt vmcnt(11) lgkmcnt(8)
Ta 136,25,192
Ta 138,25,5312
Mvaa 96,144,8
Mvaa 104,144,24
s_mov_b32 s19, 0x20000
Ta 140,26,224
Ta 142,26,5344
Mvaa 108,148,24
Mvaa 100,148,8
W 8
Ta 144,25,256
Ta 146,25,5376
Mvaa 112,152,8
s_lshr_b32 s66, s15, 3
Mvaa 120,152,24
Ta 148,26,288
Ta 150,26,5408
Mvaa 124,156,24
Mvaa 116,156,8
W 8
s_lshl_b32 s66, s66, 4
Ta 152,21,40960
Mvaa 128,128,8
Ta 154,21,46080
Mvaa 136,128,24
Ta 156,22,40992
Ta 158,22,46112
Mvaa 140,132,24
s_add_u32 s66, s66, s59
Mvaa 132,132,8
W 8
Ta 128,21,41024
Ta 130,21,46144
Mvaa 144,136,8
Mvaa 152,136,24
s_lshl_b32 s63, s66, 6
Ta 132,22,41056
Ta 134,22,46176
Mvaa 156,140,24
Mvaa 148,140,8
W 8
Ta 136,21,41088
Ta 138,21,46208
Mvaa 160,144,8
s_add_u32 s63, s63, 65536
Mvaa 168,144,24
Ta 140,22,41120
Ta 142,22,46240
Mvaa 172,148,24
Mvaa 164,148,8
s_waitcnt vmcnt(9) lgkmcnt(8)
s_mul_i32 s62, s66, 40960
Ta 144,21,41152
Mvaa 96,152,12
Ta 146,21,46272
Mvaa 104,152,28
Ta 148,22,41184
Ta 150,22,46304
Mvaa 108,156,28
s_add_u32 s62, s62, 56623104
Mvaa 100,156,12
W 8
Ta 152,21,41216
Ta 154,21,46336
Mvaa 112,128,12
s_lshl_b32 s66, s14, 10
Mvaa 120,128,28
Ta 156,22,41248
Ta 158,22,46368
Mvaa 124,132,28
Mvaa 116,132,12
W 8
s_add_u32 s78, s62, s66
Ta 128,21,51200
Mvaa 128,136,12
Ta 130,21,56320
Mvaa 136,136,28
Ta 132,22,51232
Ta 134,22,56352
Mvaa 140,140,28
s_add_u32 s79, s78, 327680
s_waitcnt vmcnt(0)
v_cmp_eq_u64 vcc, s[12:13], v[28:29]
s_cmp_eq_u64 vcc, -1
s_cbranch_scc0 .Lspin_108%=
.Lok_109%=:
s_add_u32 s81, s58, 1
Mvaa 132,140,12
W 8
Ta 136,21,51264
Ta 138,21,56384
Mvaa 144,144,12
Mvaa 152,144,28
s_and_b32 s66, s14, 1
Ta 140,22,51296
Ta 142,22,56416
Mvaa 156,148,28
s_and_b32 s81, s81, 3
Mvaa 148,148,12
W 8
Ta 144,21,51328
Ta 146,21,56448
Mvaa 160,152,12
s_mul_i32 s66, s66, 327680
Mvaa 168,152,28
s_lshl_b32 s81, s81, 12
Ta 148,22,51360
Ta 150,22,56480
Mvaa 172,156,28
Mvaa 164,156,12
W 8
s_add_u32 s65, s62, s66
Ta 152,21,51392
Mvaa 96,128,32
s_add_u32 s81, s81, s50
Ta 154,21,56512
Mvaa 104,128,48
Ta 156,22,51424
Ta 158,22,56544
Mvaa 108,132,48
s_add_u32 s65, s65, 4096
Mvaa 100,132,32
W 8
s_add_u32 s82, s81, 131072
Ta 128,21,51456
Mvaa 112,136,32
Ta 130,21,56576
Mvaa 120,136,48
s_mov_b32 s64, 0
Ta 132,22,51488
Ta 134,22,56608
Mvaa 124,140,48
buffer_load_dwordx4 a[64:67], v1, s[8:11], s81 offen offset:0 sc1
Mvaa 116,140,32
W 8
Ta 136,23,10240
Ta 138,23,15360
Mvaa 128,144,32
v_bfe_u32 v27, v1, 4, 3
Mvaa 136,144,48
Ta 140,24,10272
Ta 142,24,15392
Mvaa 140,148,48
buffer_load_dwordx4 a[80:83], v1, s[8:11], s82 offen offset:0 sc1
v_lshlrev_b32 v27, 3, v27
Mvaa 132,148,32
W 8
Ta 144,23,10304
Ta 146,23,15424
Mvaa 144,152,32
Mvaa 152,152,48
Ta 148,24,10336
v_bfe_u32 v20, v1, 4, 4
Mvaa 156,156,48
buffer_load_dwordx4 a[68:71], v1, s[8:11], s81 offen offset:1024 sc1
Ta 150,24,15456
Mvaa 148,156,32
W 8
Ta 152,23,10368
Ta 154,23,15488
Mvaa 160,128,32
v_mul_u32_u24 v20, 1280, v20
Mvaa 168,128,48
Ta 156,24,10400
Ta 158,24,15520
Mvaa 172,132,48
buffer_load_dwordx4 a[84:87], v1, s[8:11], s82 offen offset:1024 sc1
Mvaa 164,132,32
W 8
Ta 128,23,10432
v_lshrrev_b32 v12, 8, v1
Mvaa 96,136,36
Ta 130,23,15552
Mvaa 104,136,52
Ta 132,24,10464
Ta 134,24,15584
Mvaa 108,140,52
buffer_load_dwordx4 a[72:75], v1, s[8:11], s81 offen offset:2048 sc1
v_lshl_add_u32 v20, v12, 4, v20
Mvaa 100,140,36
W 8
Ta 136,23,10496
Ta 138,23,15616
Mvaa 112,144,36
Mvaa 120,144,52
s_bfe_u32 s66, s59, 0x10001
Ta 140,24,10528
Ta 142,24,15648
Mvaa 124,148,52
buffer_load_dwordx4 a[88:91], v1, s[8:11], s82 offen offset:2048 sc1
Mvaa 116,148,36
W 8
Ta 144,23,30720
Ta 146,23,35840
Mvaa 128,152,36
s_lshl_b32 s67, s57, 1
Mvaa 136,152,52
Ta 148,24,30752
Ta 150,24,35872
Mvaa 140,156,52
buffer_load_dwordx4 a[76:79], v1, s[8:11], s81 offen offset:3072 sc1
Mvaa 132,156,36
W 8
s_add_u32 s66, s66, s67
Ta 152,23,30784
Mvaa 144,128,36
Ta 154,23,35904
Mvaa 152,128,52
Ta 156,24,30816
Ta 158,24,35936
Mvaa 156,132,52
s_lshl_b32 s61, s66, 2
buffer_load_dwordx4 a[92:95], v1, s[8:11], s82 offen offset:3072 sc1
Mvaa 148,132,36
W 8
s_add_u32 s81, s58, 2
s_and_b32 s81, s81, 3
Mvaa 160,136,36
s_lshr_b32 s66, s59, 2
Ta 128,23,30848
Ta 130,23,35968
Mvaa 168,136,52
s_lshl_b32 s81, s81, 12
Ta 132,24,30880
Ta 134,24,36000
Mvaa 172,140,52
Mvaa 164,140,36
W 8
s_lshl_b32 s66, s66, 1
Ta 136,23,30912
Mvaa 96,144,40
s_add_u32 s81, s81, s50
Ta 138,23,36032
Mvaa 104,144,56
Ta 140,24,30944
Ta 142,24,36064
Mvaa 108,148,56
s_add_u32 s61, s61, s66
Mvaa 100,148,40
W 8
s_add_u32 s82, s81, 131072
Ta 144,23,30976
Mvaa 112,152,40
Ta 146,23,36096
Mvaa 120,152,56
s_and_b32 s66, s59, 1
Ta 148,24,31008
Ta 150,24,36128
Mvaa 124,156,56
buffer_load_dwordx4 a[96:99], v1, s[8:11], s81 offen offset:0 sc1
Mvaa 116,156,40
W 8
Ta 152,23,40960
Ta 154,23,46080
Mvaa 128,128,40
s_add_u32 s61, s61, s66
Mvaa 136,128,56
Ta 156,24,40992
Ta 158,24,46112
Mvaa 140,132,56
buffer_load_dwordx4 a[112:115], v1, s[8:11], s82 offen offset:0 sc1
Mvaa 132,132,40
W 8
s_xor_b32 s67, s61, 0
Ta 128,23,41024
Mvaa 144,136,40
Ta 130,23,46144
Mvaa 152,136,56
Ta 132,24,41056
Ta 134,24,46176
Mvaa 156,140,56
s_mul_i32 s72, s67, 20480
buffer_load_dwordx4 a[100:103], v1, s[8:11], s81 offen offset:1024 sc1
Mvaa 148,140,40
W 8
Ta 136,23,41088
Ta 138,23,46208
Mvaa 160,144,40
Mvaa 168,144,56
s_mul_i32 s66, s58, 320
Ta 140,24,41120
Ta 142,24,46240
Mvaa 172,148,56
buffer_load_dwordx4 a[116:119], v1, s[8:11], s82 offen offset:1024 sc1
Mvaa 164,148,40
W 8
Ta 144,23,41152
Ta 146,23,46272
Mvaa 96,152,44
s_add_u32 s72, s72, s66
Mvaa 104,152,60
Ta 148,24,41184
Ta 150,24,46304
Mvaa 108,156,60
s_mul_i32 s75, s67, 20480
buffer_load_dwordx4 a[104:107], v1, s[8:11], s81 offen offset:2048 sc1
Mvaa 100,156,44
W 8
Ta 152,23,41216
Ta 154,23,46336
Mvaa 112,128,44
Mvaa 120,128,60
s_mul_i32 s66, s58, 2560
Ta 156,24,41248
Ta 158,24,46368
Mvaa 124,132,60
buffer_load_dwordx4 a[120:123], v1, s[8:11], s82 offen offset:2048 sc1
Mvaa 116,132,44
W 8
Ta 128,21,10240
Ta 130,21,15360
Mvaa 128,136,44
s_add_u32 s75, s75, s66
Mvaa 136,136,60
Ta 132,22,10272
Ta 134,22,15392
Mvaa 140,140,60
buffer_load_dwordx4 a[108:111], v1, s[8:11], s81 offen offset:3072 sc1
K 96,96,97
Mvaa 132,140,44
W 8
s_add_u32 s75, s75, 131072
Ta 136,21,10304
Mvaa 144,144,44
Ta 138,21,15424
K 97,98,99
Mvaa 152,144,60
Ta 140,22,10336
Ta 142,22,15456
Mvaa 156,148,60
s_xor_b32 s67, s61, 124
buffer_load_dwordx4 a[124:127], v1, s[8:11], s82 offen offset:3072 sc1
K 98,100,101
Mvaa 148,148,44
W 8
s_add_u32 s81, s58, 3
s_and_b32 s81, s81, 3
Mvaa 160,152,44
Ta 144,21,10368
Ta 146,21,15488
Mvaa 168,152,60
s_lshl_b32 s81, s81, 12
s_mul_i32 s73, s67, 20480
K 99,102,103
Mvaa 172,156,60
s_add_u32 s83, s52, 0
Ta 148,22,10400
Ta 150,22,15520
Mvaa 164,156,44
s_waitcnt vmcnt(14) lgkmcnt(8)
buffer_store_dwordx4 v[96:99], v1, s[8:11], s83 offen sc1
Ta 152,21,10432
Mvaa 176,128,64
s_add_u32 s81, s81, s50
s_mul_i32 s66, s58, 320
Ta 154,21,15552
)"
R"(Mvaa 184,128,80
Ta 156,22,10464
K 104,104,105
Mvaa 188,132,80
Ta 158,22,15584
K 105,106,107
Mvaa 180,132,64
W 8
s_add_u32 s73, s73, s66
s_add_u32 s82, s81, 131072
Mvaa 192,136,64
Ta 128,21,10496
Ta 130,21,15616
Mvaa 200,136,80
Ta 132,22,10528
K 106,108,109
Mvaa 204,140,80
s_mul_i32 s76, s67, 20480
Ta 134,22,15648
K 107,110,111
Mvaa 196,140,64
W 8
s_add_u32 s83, s52, 40960
buffer_load_dwordx4 a[184:187], v1, s[8:11], s81 offen offset:0 sc1
Mvaa 208,144,64
s_mul_i32 s66, s58, 2560
Ta 136,21,20480
Ta 138,21,25600
Mvaa 216,144,80
Ta 140,22,20512
Ta 142,22,25632
Mvaa 220,148,80
Mvaa 212,148,64
W 8
buffer_store_dwordx4 v[104:107], v1, s[8:11], s83 offen sc1
K 112,112,113
Mvaa 224,152,64
s_add_u32 s76, s76, s66
Ta 144,21,20544
Ta 146,21,25664
Mvaa 232,152,80
Ta 148,22,20576
K 113,114,115
Mvaa 236,156,80
s_add_u32 s76, s76, 131072
Ta 150,22,25696
K 114,116,117
Mvaa 228,156,64
W 8
buffer_load_dwordx4 a[200:203], v1, s[8:11], s82 offen offset:0 sc1
Ta 152,21,20608
Mvaa 240,128,64
Ta 154,21,25728
K 115,118,119
Mvaa 248,128,80
s_add_u32 s83, s52, 1024
Ta 156,22,20640
v_bfe_u32 v15, v1, 7, 1
Mvaa 252,132,80
Ta 158,22,25760
Mvaa 244,132,64
s_waitcnt vmcnt(16) lgkmcnt(8)
buffer_load_dwordx4 a[188:191], v1, s[8:11], s81 offen offset:1024 sc1
Ta 128,21,20672
Mvaa 176,136,68
Ta 130,21,25792
v_mul_u32_u24 v14, 10240, v15
Mvaa 184,136,84
Ta 132,22,20704
Ta 134,22,25824
Mvaa 188,140,84
Mvaa 180,140,68
W 8
buffer_load_dwordx4 a[204:207], v1, s[8:11], s82 offen offset:1024 sc1
v_lshrrev_b32 v15, 8, v1
Mvaa 192,144,68
Ta 136,21,20736
Ta 138,21,25856
Mvaa 200,144,84
Ta 140,22,20768
Ta 142,22,25888
Mvaa 204,148,84
v_lshl_add_u32 v14, v15, 7, v14
Mvaa 196,148,68
W 8
buffer_load_dwordx4 a[192:195], v1, s[8:11], s81 offen offset:2048 sc1
Ta 144,21,30720
Mvaa 208,152,68
Ta 146,21,35840
Mvaa 216,152,84
Ta 148,22,30752
v_and_b32 v15, 0x70, v1
Mvaa 220,156,84
Ta 150,22,35872
Mvaa 212,156,68
W 8
buffer_store_dwordx4 v[112:115], v1, s[8:11], s83 offen sc1
K 120,120,121
Mvaa 224,128,68
K 121,122,123
K 122,124,125
Mvaa 232,128,84
Ta 152,21,30784
K 123,126,127
Mvaa 236,132,84
s_add_u32 s83, s52, 41984
Ta 154,21,35904
v_add_u32 v14, v14, v15
Mvaa 228,132,68
W 6
buffer_load_dwordx4 a[208:211], v1, s[8:11], s82 offen offset:2048 sc1
Ta 156,22,30816
Mvaa 240,136,68
s_bitcmp1_b32 s14, 0
s_cselect_b32 s74, s73, s72
s_cselect_b32 s77, s76, s75
Ta 158,22,35936
Ta 128,21,30848
Mvaa 248,136,84
s_add_u32 s74, s74, 256
Ta 130,21,35968
Ta 132,22,30880
Mvaa 252,140,84
Ta 134,22,36000
Mvaa 244,140,68
s_waitcnt vmcnt(19) lgkmcnt(8)
buffer_load_dwordx4 a[196:199], v1, s[8:11], s81 offen offset:3072 sc1
Ta 136,21,30912
Mvaa 176,144,72
s_add_u32 s77, s77, 2048
Ta 138,21,36032
Mvaa 184,144,88
Ta 140,22,30944
Ta 142,22,36064
Mvaa 188,148,88
Mvaa 180,148,72
W 8
s_lshl_b32 s66, s14, 6
buffer_load_dwordx4 a[212:215], v1, s[8:11], s82 offen offset:3072 sc1
Mvaa 192,152,72
Ta 144,21,30976
Ta 146,21,36096
Mvaa 200,152,88
Ta 148,22,31008
Ta 150,22,36128
Mvaa 204,156,88
s_lshl_b32 s67, s14, 9
Mvaa 196,156,72
W 8
buffer_store_dwordx4 v[120:123], v1, s[8:11], s83 offen sc1
K 128,128,129
Mvaa 208,128,72
K 129,130,131
K 130,132,133
Mvaa 216,128,88
s_add_u32 s72, s72, s66
Ta 152,23,0
K 131,134,135
Mvaa 220,132,88
s_add_u32 s83, s52, 2048
Ta 154,23,5120
Ta 156,24,32
Mvaa 212,132,72
W 7
buffer_store_dwordx4 v[128:131], v1, s[8:11], s83 offen sc1
K 136,136,137
Mvaa 224,136,72
s_add_u32 s75, s75, s67
K 137,138,139
K 138,140,141
Mvaa 232,136,88
Ta 158,24,5152
K 139,142,143
Mvaa 236,140,88
s_add_u32 s83, s52, 43008
Ta 128,23,64
Ta 130,23,5184
Mvaa 228,140,72
W 6
buffer_store_dwordx4 v[136:139], v1, s[8:11], s83 offen sc1
K 144,144,145
Mvaa 240,144,72
s_add_u32 s73, s73, s66
Ta 132,24,96
K 145,146,147
Mvaa 248,144,88
Ta 134,24,5216
K 146,148,149
Mvaa 252,148,88
s_add_u32 s76, s76, s67
Ta 136,23,128
Ta 138,23,5248
Mvaa 244,148,72
s_waitcnt vmcnt(22) lgkmcnt(6)
Ta 140,24,160
K 147,150,151
Mvaa 176,152,76
s_add_u32 s83, s52, 3072
Ta 142,24,5280
buffer_store_dwordx4 v[144:147], v1, s[8:11], s83 offen sc1
Mvaa 184,152,92
Ta 144,23,192
Ta 146,23,5312
Mvaa 188,156,92
Ta 148,24,224
K 152,152,153
Mvaa 180,156,76
W 7
Ta 150,24,5344
K 153,154,155
Mvaa 192,128,76
buffer_load_dwordx4 v[36:39], v20, s[16:19], s72 offen
Ta 152,23,256
Mvaa 200,128,92
Ta 154,23,5376
K 154,156,157
Mvaa 204,132,92
Ta 156,24,288
Ta 158,24,5408
Mvaa 196,132,76
W 8
Ta 128,23,20480
K 155,158,159
Mvaa 208,136,76
s_add_u32 s83, s52, 44032
buffer_load_dwordx4 v[44:47], v14, s[8:11], s75 offen sc1
Ta 130,23,25600
Mvaa 216,136,92
Ta 132,24,20512
Ta 134,24,25632
Mvaa 220,140,92
Mvaa 212,140,76
W 8
Ta 136,23,51200
Ta 138,23,56320
Mvaa 224,144,76
buffer_store_dwordx4 v[152:155], v1, s[8:11], s83 offen sc1
K 160,160,161
Mvaa 232,144,92
Ta 140,24,51232
K 161,162,163
Mvaa 236,148,92
Ta 142,24,56352
K 162,164,165
Mvaa 228,148,76
W 8
Ta 144,25,0
Ta 146,25,5120
Mvaa 240,152,76
buffer_load_dwordx4 v[52:55], v20, s[16:19], s73 offen
K 163,166,167
Mvaa 248,152,92
s_add_u32 s83, s52, 4096
Ta 148,26,32
Ta 150,26,5152
Mvaa 252,156,92
Mvaa 244,156,76
s_waitcnt vmcnt(25) lgkmcnt(8)
Ta 152,21,40960
Ta 154,21,46080
Mvaa 176,128,96
buffer_load_dwordx4 v[60:63], v14, s[8:11], s76 offen sc1
Mvaa 184,128,112
Ta 156,22,40992
Ta 158,22,46112
Mvaa 188,132,112
Mvaa 180,132,96
s_waitcnt vmcnt(24) lgkmcnt(8)
Ta 128,21,51200
Ta 130,21,56320
Mvaa 176,136,100
buffer_store_dwordx4 v[160:163], v1, s[8:11], s83 offen sc1
K 168,168,169
Mvaa 184,136,116
K 169,170,171
K 170,172,173
Mvaa 188,140,116
Ta 132,22,51232
Ta 134,22,56352
Mvaa 180,140,100
s_waitcnt vmcnt(23) lgkmcnt(8)
Ta 136,23,10240
K 171,174,175
Mvaa 176,144,104
s_add_u32 s83, s52, 45056
buffer_load_dwordx4 v[68:71], v20, s[16:19], s74 offen
Ta 138,23,15360
Mvaa 184,144,120
Ta 140,24,10272
Ta 142,24,15392
Mvaa 188,148,120
Mvaa 180,148,104
s_waitcnt vmcnt(22) lgkmcnt(8)
Ta 144,23,30720
Ta 146,23,35840
Mvaa 176,152,108
buffer_store_dwordx4 v[168:171], v1, s[8:11], s83 offen sc1
Mvaa 184,152,124
Ta 148,24,30752
Ta 150,24,35872
Mvaa 188,156,124
Mvaa 180,156,108
s_waitcnt vmcnt(19) lgkmcnt(8)
Ta 152,23,40960
Ta 154,23,46080
Mvaa 176,128,184
buffer_load_dwordx4 v[76:79], v14, s[8:11], s77 offen sc1
Mvaa 184,128,200
Ta 156,24,40992
Ta 158,24,46112
Mvaa 188,132,200
Mvaa 180,132,184
s_waitcnt vmcnt(18) lgkmcnt(8)
Ta 128,23,20544
Ta 130,23,25664
Mvaa 176,136,188
buffer_load_dwordx2 v[12:13], v27, s[8:11], 0 offen offset:3584 sc1
Mvaa 184,136,204
Ta 132,24,20576
Ta 134,24,25696
Mvaa 188,140,204
Mvaa 180,140,188
s_waitcnt vmcnt(16) lgkmcnt(8)
Ta 136,23,51264
Ta 138,23,56384
Mvaa 176,144,192
Mvaa 184,144,208
Ta 140,24,51296
Ta 142,24,56416
Mvaa 188,148,208
Mvaa 180,148,192
s_waitcnt vmcnt(14) lgkmcnt(8)
Ta 144,25,64
Ta 146,25,5184
Mvaa 176,152,196
Mvaa 184,152,212
Ta 148,26,96
Ta 150,26,5216
Mvaa 188,156,212
Mvaa 180,156,196
W 8
Ta 152,21,41024
Ta 154,21,46144
Mvaa 192,128,96
Mvaa 200,128,112
Ta 156,22,41056
Ta 158,22,46176
Mvaa 204,132,112
K 176,176,177
Mvaa 196,132,96
W 8
Ta 128,21,51264
Ta 130,21,56384
Mvaa 192,136,100
K 177,178,179
Mvaa 200,136,116
Ta 132,22,51296
K 178,180,181
Mvaa 204,140,116
Ta 134,22,56416
K 179,182,183
Mvaa 196,140,100
W 8
Ta 136,23,10304
Ta 138,23,15424
Mvaa 192,144,104
s_add_u32 s83, s52, 327680
Mvaa 200,144,120
buffer_store_dwordx4 v[176:179], v1, s[8:11], s83 offen sc1
K 184,184,185
Mvaa 204,148,120
K 185,186,187
K 186,188,189
Mvaa 196,148,104
W 6
Ta 140,24,10336
K 187,190,191
Mvaa 192,152,108
s_add_u32 s83, s52, 368640
Ta 142,24,15456
Ta 144,23,30784
Mvaa 200,152,124
buffer_store_dwordx4 v[184:187], v1, s[8:11], s83 offen sc1
Ta 146,23,35904
Mvaa 204,156,124
Ta 148,24,30816
Ta 150,24,35936
Mvaa 196,156,108
W 8
Ta 152,23,41024
Ta 154,23,46144
Mvaa 192,128,184
Mvaa 200,128,200
Ta 156,24,41056
Ta 158,24,46176
Mvaa 204,132,200
Mvaa 196,132,184
W 8
Ta 128,23,20608
Ta 130,23,25728
)"
R"(Mvaa 192,136,188
Mvaa 200,136,204
Ta 132,24,20640
Ta 134,24,25760
Mvaa 204,140,204
Mvaa 196,140,188
W 8
Ta 136,23,51328
Ta 138,23,56448
Mvaa 192,144,192
Mvaa 200,144,208
Ta 140,24,51360
Ta 142,24,56480
Mvaa 204,148,208
Mvaa 196,148,192
W 8
Ta 144,25,128
Ta 146,25,5248
Mvaa 192,152,196
Mvaa 200,152,212
Ta 148,26,160
Ta 150,26,5280
Mvaa 204,156,212
Mvaa 196,156,196
W 8
Ta 152,21,41088
Ta 154,21,46208
Mvaa 208,128,96
Mvaa 216,128,112
Ta 156,22,41120
Ta 158,22,46240
Mvaa 220,132,112
K 192,192,193
Mvaa 212,132,96
W 8
Ta 128,21,51328
Ta 130,21,56448
Mvaa 208,136,100
K 193,194,195
Mvaa 216,136,116
Ta 132,22,51360
K 194,196,197
Mvaa 220,140,116
Ta 134,22,56480
K 195,198,199
Mvaa 212,140,100
W 8
Ta 136,23,10368
Ta 138,23,15488
Mvaa 208,144,104
s_add_u32 s83, s52, 328704
Mvaa 216,144,120
buffer_store_dwordx4 v[192:195], v1, s[8:11], s83 offen sc1
K 200,200,201
Mvaa 220,148,120
K 201,202,203
K 202,204,205
Mvaa 212,148,104
W 6
Ta 140,24,10400
K 203,206,207
Mvaa 208,152,108
s_add_u32 s83, s52, 369664
Ta 142,24,15520
Ta 144,23,30848
Mvaa 216,152,124
buffer_store_dwordx4 v[200:203], v1, s[8:11], s83 offen sc1
Ta 146,23,35968
Mvaa 220,156,124
Ta 148,24,30880
Ta 150,24,36000
Mvaa 212,156,108
W 8
Ta 152,23,41088
Ta 154,23,46208
Mvaa 208,128,184
Mvaa 216,128,200
s_waitcnt vmcnt(6)
v_lshlrev_b32 v2, 2, v1
v_mov_b64 v[28:29], s[12:13]
s_mov_b64 exec, 3
buffer_store_dwordx2 v[28:29], v2, s[8:11], s53 offen offset:0 sc1
s_mov_b64 exec, -1
Ta 156,24,41120
Ta 158,24,46240
Mvaa 220,132,200
Mvaa 212,132,184
W 8
Ta 128,23,20672
Ta 130,23,25792
Mvaa 208,136,188
Mvaa 216,136,204
s_waitcnt vmcnt(5)
v_xor_b32 v12, s12, v12
v_xor_b32 v13, s13, v13
v_lshrrev_b32 v3, 11, v12
v_or_b32 v13, v13, v3
v_cmp_eq_u32 vcc, 0, v13
s_cmp_eq_u64 vcc, -1
s_cbranch_scc0 .LL_113%=
.LL_114%=:
v_bfe_u32 v13, v12, 4, 7
s_xor_b32 s66, s61, 124
v_cmp_eq_u32 vcc, s61, v13
v_cmp_eq_u32_e64 s[86:87], s66, v13
s_or_b32 s68, vcc_lo, s86
s_and_b32 s68, s68, 0xff
s_cbranch_scc1 .Lup_111%=
.Lupr_112%=:
Ta 132,24,20704
Ta 134,24,25824
Mvaa 220,140,204
Mvaa 212,140,188
W 8
Ta 136,23,51392
v_lshlrev_b32 v32, 16, v36
Mvaa 208,144,192
Ta 138,23,56512
v_and_b32 v33, 0xffff0000, v36
Mvaa 216,144,208
Ta 140,24,51424
v_lshlrev_b32 v34, 16, v37
Mvaa 220,148,208
Ta 142,24,56544
v_and_b32 v35, 0xffff0000, v37
Mvaa 212,148,192
W 8
Ta 144,25,192
v_lshlrev_b32 v36, 16, v38
Mvaa 208,152,196
Ta 146,25,5312
v_and_b32 v37, 0xffff0000, v38
Mvaa 216,152,212
Ta 148,26,224
v_lshlrev_b32 v38, 16, v39
Mvaa 220,156,212
Ta 150,26,5344
v_and_b32 v39, 0xffff0000, v39
Mvaa 212,156,196
W 8
Ta 152,21,41152
v_lshlrev_b32 v40, 16, v44
Mvaa 224,128,96
Ta 154,21,46272
v_and_b32 v41, 0xffff0000, v44
Mvaa 232,128,112
Ta 156,22,41184
v_lshlrev_b32 v42, 16, v45
Mvaa 236,132,112
Ta 158,22,46304
v_and_b32 v43, 0xffff0000, v45
Mvaa 228,132,96
W 8
K 208,208,209
v_lshlrev_b32 v44, 16, v46
Mvaa 224,136,100
Ta 128,21,51392
Ta 130,21,56512
Mvaa 232,136,116
v_and_b32 v45, 0xffff0000, v46
K 209,210,211
Mvaa 236,140,116
v_lshlrev_b32 v46, 16, v47
K 210,212,213
Mvaa 228,140,100
W 6
Ta 132,22,51424
Ta 134,22,56544
Mvaa 224,144,104
v_and_b32 v47, 0xffff0000, v47
K 211,214,215
Mvaa 232,144,120
s_add_u32 s83, s52, 329728
Ta 136,23,10432
v_lshlrev_b32 v48, 16, v52
Mvaa 236,148,120
Ta 138,23,15552
v_and_b32 v49, 0xffff0000, v52
Mvaa 228,148,104
W 6
buffer_store_dwordx4 v[208:211], v1, s[8:11], s83 offen sc1
v_lshlrev_b32 v50, 16, v53
Mvaa 224,152,108
K 216,216,217
K 217,218,219
Mvaa 232,152,124
K 218,220,221
K 219,222,223
Mvaa 236,156,124
s_add_u32 s83, s52, 370688
Ta 140,24,10464
Ta 142,24,15584
Mvaa 228,156,108
W 4
buffer_store_dwordx4 v[216:219], v1, s[8:11], s83 offen sc1
v_and_b32 v51, 0xffff0000, v53
Mvaa 224,128,184
Ta 144,23,30912
v_lshlrev_b32 v52, 16, v54
Mvaa 232,128,200
Ta 146,23,36032
v_and_b32 v53, 0xffff0000, v54
Mvaa 236,132,200
Ta 148,24,30944
v_lshlrev_b32 v54, 16, v55
Mvaa 228,132,184
Ta 150,24,36064
W 4
Mvaa 224,136,188
v_and_b32 v55, 0xffff0000, v55
v_lshlrev_b32 v56, 16, v60
Mvaa 232,136,204
Ta 152,23,41152
Ta 154,23,46272
Mvaa 236,140,204
v_and_b32 v57, 0xffff0000, v60
v_lshlrev_b32 v58, 16, v61
Mvaa 228,140,188
Ta 156,24,41184
Ta 158,24,46304
W 4
Mvaa 224,144,192
v_and_b32 v59, 0xffff0000, v61
v_lshlrev_b32 v60, 16, v62
Mvaa 232,144,208
Ta 128,23,20736
Ta 130,23,25856
Mvaa 236,148,208
v_and_b32 v61, 0xffff0000, v62
v_lshlrev_b32 v62, 16, v63
Mvaa 228,148,192
Ta 132,24,20768
Ta 134,24,25888
W 4
Mvaa 224,152,196
v_and_b32 v63, 0xffff0000, v63
v_lshlrev_b32 v64, 16, v68
Mvaa 232,152,212
Ta 136,23,51456
Ta 138,23,56576
Mvaa 236,156,212
v_and_b32 v65, 0xffff0000, v68
v_lshlrev_b32 v66, 16, v69
Mvaa 228,156,196
Ta 140,24,51488
Ta 142,24,56608
W 4
Mvaa 240,128,96
v_and_b32 v67, 0xffff0000, v69
v_lshlrev_b32 v68, 16, v70
Mvaa 248,128,112
Ta 144,25,256
Ta 146,25,5376
Mvaa 252,132,112
v_and_b32 v69, 0xffff0000, v70
v_lshlrev_b32 v70, 16, v71
Mvaa 244,132,96
Ta 148,26,288
Ta 150,26,5408
W 4
Mvaa 240,136,100
v_and_b32 v71, 0xffff0000, v71
v_lshlrev_b32 v72, 16, v76
Mvaa 248,136,116
Ta 152,21,41216
Ta 154,21,46336
Mvaa 252,140,116
v_and_b32 v73, 0xffff0000, v76
v_lshlrev_b32 v74, 16, v77
Mvaa 244,140,100
Ta 156,22,41248
Ta 158,22,46368
W 4
Mvaa 240,144,104
v_and_b32 v75, 0xffff0000, v77
K 224,224,225
Mvaa 248,144,120
Ta 128,21,51456
v_lshlrev_b32 v76, 16, v78
Mvaa 252,148,120
Ta 130,21,56576
K 225,226,227
Mvaa 244,148,104
Ta 132,22,51488
Ta 134,22,56608
W 4
Mvaa 240,152,108
v_and_b32 v77, 0xffff0000, v78
K 226,228,229
Mvaa 248,152,124
v_lshlrev_b32 v78, 16, v79
v_and_b32 v79, 0xffff0000, v79
Mvaa 252,156,124
Ta 136,23,10496
K 227,230,231
Mvaa 244,156,108
Ta 138,23,15616
Ta 140,24,10528
Ta 142,24,15648
W 4
Mvaa 240,128,184
s_add_u32 s83, s52, 330752
buffer_store_dwordx4 v[224:227], v1, s[8:11], s83 offen sc1
K 232,232,233
Mvaa 248,128,200
K 233,234,235
K 234,236,237
Mvaa 252,132,200
Ta 144,23,30976
K 235,238,239
Mvaa 244,132,184
Ta 146,23,36096
Ta 148,24,31008
Ta 150,24,36128
W 4
Mvaa 240,136,188
s_add_u32 s83, s52, 371712
buffer_store_dwordx4 v[232:235], v1, s[8:11], s83 offen sc1
Ta 152,23,41216
Mvaa 248,136,204
Ta 154,23,46336
Ta 156,24,41248
Mvaa 252,140,204
Ta 158,24,46368
Mvaa 244,140,188
W 4
Mvaa 240,144,192
Mvaa 248,144,208
Mvaa 252,148,208
Mvaa 244,148,192
W 0
Mvaa 240,152,196
Mvaa 248,152,212
Mvaa 252,156,212
Mvaa 244,156,196
s_nop 4
K 240,240,241
K 241,242,243
s_nop 0
K 242,244,245
K 243,246,247
s_add_u32 s83, s52, 331776
buffer_store_dwordx4 v[240:243], v1, s[8:11], s83 offen sc1
K 248,248,249
K 249,250,251
K 250,252,253
K 251,254,255
s_add_u32 s83, s52, 372736
buffer_store_dwordx4 v[248:251], v1, s[8:11], s83 offen sc1
.Ltend_92%=:
v_bfe_u32 v26, v1, 7, 1
v_lshl_add_u32 v26, v26, 9, v27
buffer_load_dwordx2 v[24:25], v26, s[8:11], s63 offen sc1
s_cmp_lg_u32 s68, 0
s_cbranch_scc1 .Lup_116%=
.Lupr_117%=:
s_lshl_b32 s66, 0x101, s59
s_waitcnt vmcnt(1)
s_cmp_eq_u32 s64, 0
s_cbranch_scc0 .LL_122%=
v_lshlrev_b32 v2, 2, v1
v_mov_b64 v[28:29], s[12:13]
s_mov_b64 exec, 3
buffer_store_dwordx2 v[28:29], v2, s[8:11], s53 offen offset:512 sc1
s_mov_b64 exec, -1
.LL_122%=:
s_barrier
s_waitcnt vmcnt(1)
v_cmp_eq_u64 vcc, s[12:13], v[24:25]
s_or_b32 s67, vcc_lo, s66
s_andn2_b32 s80, 0xffff, s67
s_cbranch_scc0 .LL_123%=
s_mov_b32 s84, 0
.LL_126%=:
s_andn2_b32 s80, 255, s67
s_cbranch_scc0 .LL_125%=
buffer_load_dwordx2 v[24:25], v26, s[8:11], s63 offen sc1
s_add_u32 s84, s84, 1
s_waitcnt vmcnt(0)
v_cmp_eq_u64 vcc, s[12:13], v[24:25]
s_or_b32 s67, vcc_lo, s66
s_cmp_lt_u32 s84, 4194304
s_cbranch_scc1 .LL_126%=
.LL_125%=:
s_add_u32 s62, s78, 0
buffer_load_dwordx4 v[96:99], v1, s[8:11], s62 offen sc1
s_add_u32 s62, s78, 5120
buffer_load_dwordx4 v[100:103], v1, s[8:11], s62 offen sc1
s_add_u32 s62, s78, 10240
buffer_load_dwordx4 v[104:107], v1, s[8:11], s62 offen sc1
s_add_u32 s62, s78, 15360
buffer_load_dwordx4 v[108:111], v1, s[8:11], s62 offen sc1
s_add_u32 s62, s78, 20480
buffer_load_dwordx4 v[112:115], v1, s[8:11], s62 offen sc1
s_add_u32 s62, s78, 25600
buffer_load_dwordx4 v[116:119], v1, s[8:11], s62 offen sc1
s_add_u32 s62, s78, 30720
buffer_load_dwordx4 v[120:123], v1, s[8:11], s62 offen sc1
s_add_u32 s62, s78, 35840
buffer_load_dwordx4 v[124:127], v1, s[8:11], s62 offen sc1
)"
R"(s_cmp_eq_u32 s14, 0
s_cbranch_scc0 .LL_127%=
s_add_u32 s62, s65, 0
buffer_load_dwordx4 v[128:131], v1, s[8:11], s62 offen sc1
s_add_u32 s62, s65, 5120
buffer_load_dwordx4 v[132:135], v1, s[8:11], s62 offen sc1
s_add_u32 s62, s65, 10240
buffer_load_dwordx4 v[136:139], v1, s[8:11], s62 offen sc1
s_add_u32 s62, s65, 15360
buffer_load_dwordx4 v[140:143], v1, s[8:11], s62 offen sc1
s_add_u32 s62, s65, 20480
buffer_load_dwordx4 v[144:147], v1, s[8:11], s62 offen sc1
s_add_u32 s62, s65, 25600
buffer_load_dwordx4 v[148:151], v1, s[8:11], s62 offen sc1
s_add_u32 s62, s65, 30720
buffer_load_dwordx4 v[152:155], v1, s[8:11], s62 offen sc1
s_add_u32 s62, s65, 35840
buffer_load_dwordx4 v[156:159], v1, s[8:11], s62 offen sc1
.LL_127%=:
buffer_load_dwordx2 v[24:25], v26, s[8:11], s63 offen sc1
s_waitcnt vmcnt(1)
Zvvv 80,4,96
Zvvv 84,8,96
Mvvv 80,4,100
Mvvv 84,8,100
Mvvv 80,4,104
Mvvv 84,8,104
Mvvv 80,4,108
Mvvv 84,8,108
Mvvv 80,4,112
Mvvv 84,8,112
Mvvv 80,4,116
Mvvv 84,8,116
Mvvv 80,4,120
Mvvv 84,8,120
Mvvv 80,4,124
Mvvv 84,8,124
s_nop 6
K 160,80,81
K 161,82,83
K 162,84,85
K 163,86,87
v_lshlrev_b32 v80, 16, v160
v_and_b32 v81, 0xffff0000, v160
v_lshlrev_b32 v82, 16, v161
v_and_b32 v83, 0xffff0000, v161
v_lshlrev_b32 v84, 16, v162
v_and_b32 v85, 0xffff0000, v162
v_lshlrev_b32 v86, 16, v163
v_and_b32 v87, 0xffff0000, v163
A 80,32,80
A 81,33,81
A 82,34,82
A 83,35,83
A 84,36,84
A 85,37,85
A 86,38,86
A 87,39,87
K 160,80,81
K 161,82,83
K 162,84,85
K 163,86,87
v_lshlrev_b32 v80, 16, v160
v_and_b32 v81, 0xffff0000, v160
v_lshlrev_b32 v82, 16, v161
v_and_b32 v83, 0xffff0000, v161
v_lshlrev_b32 v84, 16, v162
v_and_b32 v85, 0xffff0000, v162
v_lshlrev_b32 v86, 16, v163
v_and_b32 v87, 0xffff0000, v163
A 80,40,80
A 81,41,81
A 82,42,82
A 83,43,83
A 84,44,84
A 85,45,85
A 86,46,86
A 87,47,87
K 80,80,81
K 81,82,83
K 82,84,85
K 83,86,87
buffer_store_dwordx4 v[80:83], v20, s[20:23], s72 offen
s_cmp_eq_u32 s14, 0
s_cbranch_scc0 .LL_128%=
Zvvv 88,4,128
Zvvv 92,8,128
Mvvv 88,4,132
Mvvv 92,8,132
Mvvv 88,4,136
Mvvv 92,8,136
Mvvv 88,4,140
Mvvv 92,8,140
Mvvv 88,4,144
Mvvv 92,8,144
Mvvv 88,4,148
Mvvv 92,8,148
Mvvv 88,4,152
Mvvv 92,8,152
Mvvv 88,4,156
Mvvv 92,8,156
s_nop 6
K 160,88,89
K 161,90,91
K 162,92,93
K 163,94,95
v_lshlrev_b32 v88, 16, v160
v_and_b32 v89, 0xffff0000, v160
v_lshlrev_b32 v90, 16, v161
v_and_b32 v91, 0xffff0000, v161
v_lshlrev_b32 v92, 16, v162
v_and_b32 v93, 0xffff0000, v162
v_lshlrev_b32 v94, 16, v163
v_and_b32 v95, 0xffff0000, v163
A 88,64,88
A 89,65,89
A 90,66,90
A 91,67,91
A 92,68,92
A 93,69,93
A 94,70,94
A 95,71,95
K 160,88,89
K 161,90,91
K 162,92,93
K 163,94,95
v_lshlrev_b32 v88, 16, v160
v_and_b32 v89, 0xffff0000, v160
v_lshlrev_b32 v90, 16, v161
v_and_b32 v91, 0xffff0000, v161
v_lshlrev_b32 v92, 16, v162
v_and_b32 v93, 0xffff0000, v162
v_lshlrev_b32 v94, 16, v163
v_and_b32 v95, 0xffff0000, v163
A 88,72,88
A 89,73,89
A 90,74,90
A 91,75,91
A 92,76,92
A 93,77,93
A 94,78,94
A 95,79,95
K 88,88,89
K 89,90,91
K 90,92,93
K 91,94,95
buffer_store_dwordx4 v[88:91], v20, s[20:23], s74 offen
.LL_128%=:
s_nop 0
s_cmp_eq_u32 s14, 0
s_cbranch_scc1 .LL_122%=a
s_waitcnt vmcnt(1)
.LL_122%=a:
s_waitcnt vmcnt(2)
v_cmp_eq_u64 vcc, s[12:13], v[24:25]
s_or_b32 s67, vcc_lo, s66
s_mov_b32 s84, 0
.LL_130%=:
s_andn2_b32 s80, 65280, s67
s_cbranch_scc0 .LL_129%=
buffer_load_dwordx2 v[24:25], v26, s[8:11], s63 offen sc1
s_add_u32 s84, s84, 1
s_waitcnt vmcnt(0)
v_cmp_eq_u64 vcc, s[12:13], v[24:25]
s_or_b32 s67, vcc_lo, s66
s_cmp_lt_u32 s84, 4194304
s_cbranch_scc1 .LL_130%=
.LL_129%=:
s_add_u32 s62, s79, 0
buffer_load_dwordx4 v[208:211], v1, s[8:11], s62 offen sc1
s_add_u32 s62, s79, 5120
buffer_load_dwordx4 v[212:215], v1, s[8:11], s62 offen sc1
s_add_u32 s62, s79, 10240
buffer_load_dwordx4 v[216:219], v1, s[8:11], s62 offen sc1
s_add_u32 s62, s79, 15360
buffer_load_dwordx4 v[220:223], v1, s[8:11], s62 offen sc1
s_add_u32 s62, s79, 20480
buffer_load_dwordx4 v[224:227], v1, s[8:11], s62 offen sc1
s_add_u32 s62, s79, 25600
buffer_load_dwordx4 v[228:231], v1, s[8:11], s62 offen sc1
s_add_u32 s62, s79, 30720
buffer_load_dwordx4 v[232:235], v1, s[8:11], s62 offen sc1
s_add_u32 s62, s79, 35840
buffer_load_dwordx4 v[236:239], v1, s[8:11], s62 offen sc1
s_cmp_eq_u32 s14, 1
s_cbranch_scc0 .LL_131%=
s_add_u32 s62, s65, 0
buffer_load_dwordx4 v[128:131], v1, s[8:11], s62 offen sc1
s_add_u32 s62, s65, 5120
buffer_load_dwordx4 v[132:135], v1, s[8:11], s62 offen sc1
s_add_u32 s62, s65, 10240
buffer_load_dwordx4 v[136:139], v1, s[8:11], s62 offen sc1
s_add_u32 s62, s65, 15360
buffer_load_dwordx4 v[140:143], v1, s[8:11], s62 offen sc1
s_add_u32 s62, s65, 20480
buffer_load_dwordx4 v[144:147], v1, s[8:11], s62 offen sc1
s_add_u32 s62, s65, 25600
buffer_load_dwordx4 v[148:151], v1, s[8:11], s62 offen sc1
s_add_u32 s62, s65, 30720
buffer_load_dwordx4 v[152:155], v1, s[8:11], s62 offen sc1
s_add_u32 s62, s65, 35840
buffer_load_dwordx4 v[156:159], v1, s[8:11], s62 offen sc1
.LL_131%=:
s_waitcnt vmcnt(0)
Zvvv 80,4,208
Zvvv 84,8,208
Mvvv 80,4,212
Mvvv 84,8,212
Mvvv 80,4,216
Mvvv 84,8,216
Mvvv 80,4,220
Mvvv 84,8,220
Mvvv 80,4,224
Mvvv 84,8,224
Mvvv 80,4,228
Mvvv 84,8,228
Mvvv 80,4,232
Mvvv 84,8,232
Mvvv 80,4,236
Mvvv 84,8,236
s_nop 6
K 160,80,81
K 161,82,83
K 162,84,85
K 163,86,87
v_lshlrev_b32 v80, 16, v160
v_and_b32 v81, 0xffff0000, v160
v_lshlrev_b32 v82, 16, v161
v_and_b32 v83, 0xffff0000, v161
v_lshlrev_b32 v84, 16, v162
v_and_b32 v85, 0xffff0000, v162
v_lshlrev_b32 v86, 16, v163
v_and_b32 v87, 0xffff0000, v163
A 80,48,80
A 81,49,81
A 82,50,82
A 83,51,83
A 84,52,84
A 85,53,85
A 86,54,86
A 87,55,87
K 160,80,81
K 161,82,83
K 162,84,85
K 163,86,87
v_lshlrev_b32 v80, 16, v160
v_and_b32 v81, 0xffff0000, v160
v_lshlrev_b32 v82, 16, v161
v_and_b32 v83, 0xffff0000, v161
v_lshlrev_b32 v84, 16, v162
v_and_b32 v85, 0xffff0000, v162
v_lshlrev_b32 v86, 16, v163
v_and_b32 v87, 0xffff0000, v163
A 80,56,80
A 81,57,81
A 82,58,82
A 83,59,83
A 84,60,84
A 85,61,85
A 86,62,86
A 87,63,87
K 80,80,81
K 81,82,83
K 82,84,85
K 83,86,87
buffer_store_dwordx4 v[80:83], v20, s[20:23], s73 offen
s_cmp_eq_u32 s14, 1
s_cbranch_scc0 .LL_132%=
Zvvv 88,4,128
Zvvv 92,8,128
Mvvv 88,4,132
Mvvv 92,8,132
Mvvv 88,4,136
Mvvv 92,8,136
Mvvv 88,4,140
Mvvv 92,8,140
Mvvv 88,4,144
Mvvv 92,8,144
Mvvv 88,4,148
Mvvv 92,8,148
Mvvv 88,4,152
Mvvv 92,8,152
Mvvv 88,4,156
Mvvv 92,8,156
s_nop 6
K 160,88,89
K 161,90,91
K 162,92,93
K 163,94,95
v_lshlrev_b32 v88, 16, v160
v_and_b32 v89, 0xffff0000, v160
v_lshlrev_b32 v90, 16, v161
v_and_b32 v91, 0xffff0000, v161
v_lshlrev_b32 v92, 16, v162
v_and_b32 v93, 0xffff0000, v162
v_lshlrev_b32 v94, 16, v163
v_and_b32 v95, 0xffff0000, v163
A 88,64,88
A 89,65,89
A 90,66,90
A 91,67,91
A 92,68,92
A 93,69,93
A 94,70,94
A 95,71,95
K 160,88,89
K 161,90,91
K 162,92,93
K 163,94,95
v_lshlrev_b32 v88, 16, v160
v_and_b32 v89, 0xffff0000, v160
v_lshlrev_b32 v90, 16, v161
v_and_b32 v91, 0xffff0000, v161
v_lshlrev_b32 v92, 16, v162
v_and_b32 v93, 0xffff0000, v162
v_lshlrev_b32 v94, 16, v163
v_and_b32 v95, 0xffff0000, v163
A 88,72,88
A 89,73,89
A 90,74,90
A 91,75,91
A 92,76,92
A 93,77,93
A 94,78,94
A 95,79,95
K 88,88,89
K 89,90,91
K 90,92,93
K 91,94,95
buffer_store_dwordx4 v[88:91], v20, s[20:23], s74 offen
.LL_132%=:
s_branch .LL_124%=
.LL_123%=:
s_add_u32 s62, s78, 0
buffer_load_dwordx4 v[96:99], v1, s[8:11], s62 offen sc1
s_add_u32 s62, s78, 5120
buffer_load_dwordx4 v[100:103], v1, s[8:11], s62 offen sc1
s_add_u32 s62, s78, 10240
buffer_load_dwordx4 v[104:107], v1, s[8:11], s62 offen sc1
s_add_u32 s62, s78, 15360
buffer_load_dwordx4 v[108:111], v1, s[8:11], s62 offen sc1
s_add_u32 s62, s78, 20480
buffer_load_dwordx4 v[112:115], v1, s[8:11], s62 offen sc1
s_add_u32 s62, s78, 25600
buffer_load_dwordx4 v[116:119], v1, s[8:11], s62 offen sc1
s_add_u32 s62, s78, 30720
buffer_load_dwordx4 v[120:123], v1, s[8:11], s62 offen sc1
s_add_u32 s62, s78, 35840
buffer_load_dwordx4 v[124:127], v1, s[8:11], s62 offen sc1
s_cmp_eq_u32 s14, 0
s_cbranch_scc0 .LL_133%=
s_add_u32 s62, s65, 0
buffer_load_dwordx4 v[128:131], v1, s[8:11], s62 offen sc1
s_add_u32 s62, s65, 5120
buffer_load_dwordx4 v[132:135], v1, s[8:11], s62 offen sc1
s_add_u32 s62, s65, 10240
buffer_load_dwordx4 v[136:139], v1, s[8:11], s62 offen sc1
s_add_u32 s62, s65, 15360
buffer_load_dwordx4 v[140:143], v1, s[8:11], s62 offen sc1
s_add_u32 s62, s65, 20480
buffer_load_dwordx4 v[144:147], v1, s[8:11], s62 offen sc1
s_add_u32 s62, s65, 25600
buffer_load_dwordx4 v[148:151], v1, s[8:11], s62 offen sc1
s_add_u32 s62, s65, 30720
buffer_load_dwordx4 v[152:155], v1, s[8:11], s62 offen sc1
s_add_u32 s62, s65, 35840
buffer_load_dwordx4 v[156:159], v1, s[8:11], s62 offen sc1
.LL_133%=:
s_add_u32 s62, s79, 0
buffer_load_dwordx4 v[208:211], v1, s[8:11], s62 offen sc1
s_add_u32 s62, s79, 5120
buffer_load_dwordx4 v[212:215], v1, s[8:11], s62 offen sc1
s_add_u32 s62, s79, 10240
buffer_load_dwordx4 v[216:219], v1, s[8:11], s62 offen sc1
s_add_u32 s62, s79, 15360
buffer_load_dwordx4 v[220:223], v1, s[8:11], s62 offen sc1
s_add_u32 s62, s79, 20480
buffer_load_dwordx4 v[224:227], v1, s[8:11], s62 offen sc1
s_add_u32 s62, s79, 25600
buffer_load_dwordx4 v[228:231], v1, s[8:11], s62 offen sc1
s_add_u32 s62, s79, 30720
buffer_load_dwordx4 v[232:235], v1, s[8:11], s62 offen sc1
s_add_u32 s62, s79, 35840
buffer_load_dwordx4 v[236:239], v1, s[8:11], s62 offen sc1
s_cmp_eq_u32 s14, 1
s_cbranch_scc0 .LL_134%=
s_add_u32 s62, s65, 0
buffer_load_dwordx4 v[128:131], v1, s[8:11], s62 offen sc1
s_add_u32 s62, s65, 5120
buffer_load_dwordx4 v[132:135], v1, s[8:11], s62 offen sc1
s_add_u32 s62, s65, 10240
buffer_load_dwordx4 v[136:139], v1, s[8:11], s62 offen sc1
s_add_u32 s62, s65, 15360
buffer_load_dwordx4 v[140:143], v1, s[8:11], s62 offen sc1
s_add_u32 s62, s65, 20480
buffer_load_dwordx4 v[144:147], v1, s[8:11], s62 offen sc1
s_add_u32 s62, s65, 25600
buffer_load_dwordx4 v[148:151], v1, s[8:11], s62 offen sc1
s_add_u32 s62, s65, 30720
buffer_load_dwordx4 v[152:155], v1, s[8:11], s62 offen sc1
)"
R"(s_add_u32 s62, s65, 35840
buffer_load_dwordx4 v[156:159], v1, s[8:11], s62 offen sc1
.LL_134%=:
s_waitcnt vmcnt(8)
Zvvv 80,4,96
Zvvv 84,8,96
Mvvv 80,4,100
Mvvv 84,8,100
Mvvv 80,4,104
Mvvv 84,8,104
Mvvv 80,4,108
Mvvv 84,8,108
Mvvv 80,4,112
Mvvv 84,8,112
Mvvv 80,4,116
Mvvv 84,8,116
Mvvv 80,4,120
Mvvv 84,8,120
Mvvv 80,4,124
Mvvv 84,8,124
s_nop 6
K 160,80,81
K 161,82,83
K 162,84,85
K 163,86,87
v_lshlrev_b32 v80, 16, v160
v_and_b32 v81, 0xffff0000, v160
v_lshlrev_b32 v82, 16, v161
v_and_b32 v83, 0xffff0000, v161
v_lshlrev_b32 v84, 16, v162
v_and_b32 v85, 0xffff0000, v162
v_lshlrev_b32 v86, 16, v163
v_and_b32 v87, 0xffff0000, v163
A 80,32,80
A 81,33,81
A 82,34,82
A 83,35,83
A 84,36,84
A 85,37,85
A 86,38,86
A 87,39,87
K 160,80,81
K 161,82,83
K 162,84,85
K 163,86,87
v_lshlrev_b32 v80, 16, v160
v_and_b32 v81, 0xffff0000, v160
v_lshlrev_b32 v82, 16, v161
v_and_b32 v83, 0xffff0000, v161
v_lshlrev_b32 v84, 16, v162
v_and_b32 v85, 0xffff0000, v162
v_lshlrev_b32 v86, 16, v163
v_and_b32 v87, 0xffff0000, v163
A 80,40,80
A 81,41,81
A 82,42,82
A 83,43,83
A 84,44,84
A 85,45,85
A 86,46,86
A 87,47,87
K 80,80,81
K 81,82,83
K 82,84,85
K 83,86,87
buffer_store_dwordx4 v[80:83], v20, s[20:23], s72 offen
s_cmp_eq_u32 s14, 0
s_cbranch_scc0 .LL_135%=
Zvvv 88,4,128
Zvvv 92,8,128
Mvvv 88,4,132
Mvvv 92,8,132
Mvvv 88,4,136
Mvvv 92,8,136
Mvvv 88,4,140
Mvvv 92,8,140
Mvvv 88,4,144
Mvvv 92,8,144
Mvvv 88,4,148
Mvvv 92,8,148
Mvvv 88,4,152
Mvvv 92,8,152
Mvvv 88,4,156
Mvvv 92,8,156
s_nop 6
K 160,88,89
K 161,90,91
K 162,92,93
K 163,94,95
v_lshlrev_b32 v88, 16, v160
v_and_b32 v89, 0xffff0000, v160
v_lshlrev_b32 v90, 16, v161
v_and_b32 v91, 0xffff0000, v161
v_lshlrev_b32 v92, 16, v162
v_and_b32 v93, 0xffff0000, v162
v_lshlrev_b32 v94, 16, v163
v_and_b32 v95, 0xffff0000, v163
A 88,64,88
A 89,65,89
A 90,66,90
A 91,67,91
A 92,68,92
A 93,69,93
A 94,70,94
A 95,71,95
K 160,88,89
K 161,90,91
K 162,92,93
K 163,94,95
v_lshlrev_b32 v88, 16, v160
v_and_b32 v89, 0xffff0000, v160
v_lshlrev_b32 v90, 16, v161
v_and_b32 v91, 0xffff0000, v161
v_lshlrev_b32 v92, 16, v162
v_and_b32 v93, 0xffff0000, v162
v_lshlrev_b32 v94, 16, v163
v_and_b32 v95, 0xffff0000, v163
A 88,72,88
A 89,73,89
A 90,74,90
A 91,75,91
A 92,76,92
A 93,77,93
A 94,78,94
A 95,79,95
K 88,88,89
K 89,90,91
K 90,92,93
K 91,94,95
buffer_store_dwordx4 v[88:91], v20, s[20:23], s74 offen
.LL_135%=:
s_waitcnt vmcnt(1)
Zvvv 80,4,208
Zvvv 84,8,208
Mvvv 80,4,212
Mvvv 84,8,212
Mvvv 80,4,216
Mvvv 84,8,216
Mvvv 80,4,220
Mvvv 84,8,220
Mvvv 80,4,224
Mvvv 84,8,224
Mvvv 80,4,228
Mvvv 84,8,228
Mvvv 80,4,232
Mvvv 84,8,232
Mvvv 80,4,236
Mvvv 84,8,236
s_nop 6
K 160,80,81
K 161,82,83
K 162,84,85
K 163,86,87
v_lshlrev_b32 v80, 16, v160
v_and_b32 v81, 0xffff0000, v160
v_lshlrev_b32 v82, 16, v161
v_and_b32 v83, 0xffff0000, v161
v_lshlrev_b32 v84, 16, v162
v_and_b32 v85, 0xffff0000, v162
v_lshlrev_b32 v86, 16, v163
v_and_b32 v87, 0xffff0000, v163
A 80,48,80
A 81,49,81
A 82,50,82
A 83,51,83
A 84,52,84
A 85,53,85
A 86,54,86
A 87,55,87
K 160,80,81
K 161,82,83
K 162,84,85
K 163,86,87
v_lshlrev_b32 v80, 16, v160
v_and_b32 v81, 0xffff0000, v160
v_lshlrev_b32 v82, 16, v161
v_and_b32 v83, 0xffff0000, v161
v_lshlrev_b32 v84, 16, v162
v_and_b32 v85, 0xffff0000, v162
v_lshlrev_b32 v86, 16, v163
v_and_b32 v87, 0xffff0000, v163
A 80,56,80
A 81,57,81
A 82,58,82
A 83,59,83
A 84,60,84
A 85,61,85
A 86,62,86
A 87,63,87
K 80,80,81
K 81,82,83
K 82,84,85
K 83,86,87
buffer_store_dwordx4 v[80:83], v20, s[20:23], s73 offen
s_cmp_eq_u32 s14, 1
s_cbranch_scc0 .LL_136%=
Zvvv 88,4,128
Zvvv 92,8,128
Mvvv 88,4,132
Mvvv 92,8,132
Mvvv 88,4,136
Mvvv 92,8,136
Mvvv 88,4,140
Mvvv 92,8,140
Mvvv 88,4,144
Mvvv 92,8,144
Mvvv 88,4,148
Mvvv 92,8,148
Mvvv 88,4,152
Mvvv 92,8,152
Mvvv 88,4,156
Mvvv 92,8,156
s_nop 6
K 160,88,89
K 161,90,91
K 162,92,93
K 163,94,95
v_lshlrev_b32 v88, 16, v160
v_and_b32 v89, 0xffff0000, v160
v_lshlrev_b32 v90, 16, v161
v_and_b32 v91, 0xffff0000, v161
v_lshlrev_b32 v92, 16, v162
v_and_b32 v93, 0xffff0000, v162
v_lshlrev_b32 v94, 16, v163
v_and_b32 v95, 0xffff0000, v163
A 88,64,88
A 89,65,89
A 90,66,90
A 91,67,91
A 92,68,92
A 93,69,93
A 94,70,94
A 95,71,95
K 160,88,89
K 161,90,91
K 162,92,93
K 163,94,95
v_lshlrev_b32 v88, 16, v160
v_and_b32 v89, 0xffff0000, v160
v_lshlrev_b32 v90, 16, v161
v_and_b32 v91, 0xffff0000, v161
v_lshlrev_b32 v92, 16, v162
v_and_b32 v93, 0xffff0000, v162
v_lshlrev_b32 v94, 16, v163
v_and_b32 v95, 0xffff0000, v163
A 88,72,88
A 89,73,89
A 90,74,90
A 91,75,91
A 92,76,92
A 93,77,93
A 94,78,94
A 95,79,95
K 88,88,89
K 89,90,91
K 90,92,93
K 91,94,95
buffer_store_dwordx4 v[88:91], v20, s[20:23], s74 offen
.LL_136%=:
.LL_124%=:
s_lshl_b32 s80, s15, 2
s_add_u32 s80, s80, s14
s_cmp_eq_u32 s80, 4
s_cbranch_scc0 .LL_137%=
v_mov_b64 v[28:29], s[12:13]
s_mov_b64 exec, 1
buffer_store_dwordx2 v[28:29], off, s[8:11], 0 sc1
s_mov_b64 exec, -1
.LL_137%=:
s_branch .Lend_138%=
.Lup_116%=:
s_waitcnt vmcnt(5)
s_mov_b32 s84, 0
.LL_118%=:
v_min3_u32 v176, v161, v163, v165
v_max3_u32 v177, v161, v163, v165
v_min3_u32 v176, v176, v167, v169
v_max3_u32 v177, v177, v167, v169
v_min3_u32 v176, v176, v171, v173
v_max3_u32 v177, v177, v171, v173
v_min_u32 v176, v176, v175
v_max_u32 v177, v177, v175
v_cmp_eq_u32 vcc, s13, v176
v_cmp_eq_u32_e64 s[86:87], s13, v177
s_and_b64 vcc, vcc, s[86:87]
s_cmp_eq_u64 vcc, -1
s_cbranch_scc1 .LL_119%=
s_add_u32 s84, s84, 1
s_cmp_lt_u32 s84, 1048576
s_cbranch_scc1 .LL_120%=
.LL_119%=:
v_readlane_b32 s70, v12, s69
v_bfe_u32 v2, v1, 4, 4
s_nop 2
s_lshr_b32 s80, s70, 4
s_and_b32 s70, s70, 15
v_cmp_eq_u32 vcc, s70, v2
s_cmp_eq_u32 s80, s61
s_cselect_b64 s[86:87], vcc, 0
s_cselect_b64 s[88:89], 0, vcc
s_bitcmp1_b32 s14, 0
s_cselect_b64 vcc, s[88:89], s[86:87]
v_lshlrev_b32 v176, 16, v160
v_and_b32 v177, 0xffff0000, v160
v_lshlrev_b32 v178, 16, v162
v_and_b32 v179, 0xffff0000, v162
v_lshlrev_b32 v180, 16, v164
v_and_b32 v181, 0xffff0000, v164
v_lshlrev_b32 v182, 16, v166
v_and_b32 v183, 0xffff0000, v166
A 176,176,40
A 177,177,41
A 178,178,42
A 179,179,43
A 180,180,44
A 181,181,45
A 182,182,46
A 183,183,47
K 184,176,177
K 185,178,179
K 186,180,181
K 187,182,183
v_lshlrev_b32 v176, 16, v184
v_and_b32 v177, 0xffff0000, v184
v_lshlrev_b32 v178, 16, v185
v_and_b32 v179, 0xffff0000, v185
v_lshlrev_b32 v180, 16, v186
v_and_b32 v181, 0xffff0000, v186
v_lshlrev_b32 v182, 16, v187
v_and_b32 v183, 0xffff0000, v187
v_cndmask_b32_e64 v40, v40, v176, s[86:87]
v_cndmask_b32_e64 v41, v41, v177, s[86:87]
v_cndmask_b32_e64 v42, v42, v178, s[86:87]
v_cndmask_b32_e64 v43, v43, v179, s[86:87]
v_cndmask_b32_e64 v44, v44, v180, s[86:87]
v_cndmask_b32_e64 v45, v45, v181, s[86:87]
v_cndmask_b32_e64 v46, v46, v182, s[86:87]
v_cndmask_b32_e64 v47, v47, v183, s[86:87]
v_lshlrev_b32 v176, 16, v160
v_and_b32 v177, 0xffff0000, v160
v_lshlrev_b32 v178, 16, v162
v_and_b32 v179, 0xffff0000, v162
v_lshlrev_b32 v180, 16, v164
v_and_b32 v181, 0xffff0000, v164
v_lshlrev_b32 v182, 16, v166
v_and_b32 v183, 0xffff0000, v166
A 176,176,56
A 177,177,57
A 178,178,58
A 179,179,59
A 180,180,60
A 181,181,61
A 182,182,62
A 183,183,63
K 184,176,177
K 185,178,179
K 186,180,181
K 187,182,183
v_lshlrev_b32 v176, 16, v184
v_and_b32 v177, 0xffff0000, v184
v_lshlrev_b32 v178, 16, v185
v_and_b32 v179, 0xffff0000, v185
v_lshlrev_b32 v180, 16, v186
v_and_b32 v181, 0xffff0000, v186
v_lshlrev_b32 v182, 16, v187
v_and_b32 v183, 0xffff0000, v187
v_cndmask_b32_e64 v56, v56, v176, s[88:89]
v_cndmask_b32_e64 v57, v57, v177, s[88:89]
v_cndmask_b32_e64 v58, v58, v178, s[88:89]
v_cndmask_b32_e64 v59, v59, v179, s[88:89]
v_cndmask_b32_e64 v60, v60, v180, s[88:89]
v_cndmask_b32_e64 v61, v61, v181, s[88:89]
v_cndmask_b32_e64 v62, v62, v182, s[88:89]
v_cndmask_b32_e64 v63, v63, v183, s[88:89]
v_lshlrev_b32 v176, 16, v168
v_and_b32 v177, 0xffff0000, v168
v_lshlrev_b32 v178, 16, v170
v_and_b32 v179, 0xffff0000, v170
v_lshlrev_b32 v180, 16, v172
v_and_b32 v181, 0xffff0000, v172
v_lshlrev_b32 v182, 16, v174
v_and_b32 v183, 0xffff0000, v174
A 176,176,72
A 177,177,73
A 178,178,74
A 179,179,75
A 180,180,76
A 181,181,77
A 182,182,78
A 183,183,79
K 184,176,177
K 185,178,179
K 186,180,181
K 187,182,183
v_lshlrev_b32 v176, 16, v184
v_and_b32 v177, 0xffff0000, v184
v_lshlrev_b32 v178, 16, v185
v_and_b32 v179, 0xffff0000, v185
v_lshlrev_b32 v180, 16, v186
v_and_b32 v181, 0xffff0000, v186
)"
R"(v_lshlrev_b32 v182, 16, v187
v_and_b32 v183, 0xffff0000, v187
v_cndmask_b32_e64 v72, v72, v176, vcc
v_cndmask_b32_e64 v73, v73, v177, vcc
v_cndmask_b32_e64 v74, v74, v178, vcc
v_cndmask_b32_e64 v75, v75, v179, vcc
v_cndmask_b32_e64 v76, v76, v180, vcc
v_cndmask_b32_e64 v77, v77, v181, vcc
v_cndmask_b32_e64 v78, v78, v182, vcc
v_cndmask_b32_e64 v79, v79, v183, vcc
s_bitset0_b32 s68, s69
s_cmp_eq_u32 s68, 0
s_cbranch_scc1 .Lupr_117%=
s_ff1_i32_b32 s69, s68
.LL_120%=:
s_cmp_eq_u32 s64, 0
s_cbranch_scc0 .LL_121%=
s_waitcnt vmcnt(0)
v_lshlrev_b32 v2, 2, v1
v_mov_b64 v[28:29], s[12:13]
s_mov_b64 exec, 3
buffer_store_dwordx2 v[28:29], v2, s[8:11], s53 offen offset:512 sc1
s_mov_b64 exec, -1
s_mov_b32 s64, 1
.LL_121%=:
s_mul_i32 s66, s69, 20
s_mul_i32 s67, s58, 5
s_add_u32 s66, s66, s67
s_lshl_b32 s66, s66, 7
s_add_u32 s66, s66, 98304
s_lshl_b32 s67, s14, 7
s_add_u32 s67, s67, s66
buffer_load_dwordx4 v[160:163], v3, s[8:11], s67 offen offset:0 sc1
buffer_load_dwordx4 v[164:167], v3, s[8:11], s67 offen offset:16 sc1
buffer_load_dwordx4 v[168:171], v3, s[8:11], s66 offen offset:512 sc1
buffer_load_dwordx4 v[172:175], v3, s[8:11], s66 offen offset:528 sc1
s_waitcnt vmcnt(0)
s_branch .LL_118%=
.Lspin_74%=:
s_mov_b32 s84, 0
.Llp_76%=:
s_sleep 2
v_bfe_u32 v28, v1, 4, 4
v_lshlrev_b32 v28, 3, v28
s_lshl_b32 s80, s56, 9
s_add_u32 s80, s80, 4096
buffer_load_dwordx2 v[28:29], v28, s[8:11], s80 offen sc1
s_waitcnt vmcnt(0)
s_add_u32 s84, s84, 1
v_cmp_eq_u64 vcc, s[12:13], v[28:29]
s_cmp_eq_u64 vcc, -1
s_cbranch_scc1 .Lok_75%=
s_cmp_lt_u32 s84, 1048576
s_cbranch_scc1 .Llp_76%=
s_branch .Lok_75%=
.Lspin_77%=:
s_mov_b32 s84, 0
.Llp_79%=:
s_sleep 2
v_bfe_u32 v28, v1, 4, 4
v_lshlrev_b32 v28, 3, v28
s_lshl_b32 s80, s56, 9
s_add_u32 s80, s80, 4224
buffer_load_dwordx2 v[28:29], v28, s[8:11], s80 offen sc1
s_waitcnt vmcnt(0)
s_add_u32 s84, s84, 1
v_cmp_eq_u64 vcc, s[12:13], v[28:29]
s_cmp_eq_u64 vcc, -1
s_cbranch_scc1 .Lok_78%=
s_cmp_lt_u32 s84, 1048576
s_cbranch_scc1 .Llp_79%=
s_branch .Lok_78%=
.Lspin_80%=:
s_mov_b32 s84, 0
.Llp_82%=:
s_sleep 2
v_bfe_u32 v28, v1, 4, 4
v_lshlrev_b32 v28, 3, v28
s_lshl_b32 s80, s56, 9
s_add_u32 s80, s80, 4352
buffer_load_dwordx2 v[28:29], v28, s[8:11], s80 offen sc1
s_waitcnt vmcnt(0)
s_add_u32 s84, s84, 1
v_cmp_eq_u64 vcc, s[12:13], v[28:29]
s_cmp_eq_u64 vcc, -1
s_cbranch_scc1 .Lok_81%=
s_cmp_lt_u32 s84, 1048576
s_cbranch_scc1 .Llp_82%=
s_branch .Lok_81%=
.Lspin_83%=:
s_mov_b32 s84, 0
.Llp_85%=:
s_sleep 2
v_bfe_u32 v28, v1, 4, 4
v_lshlrev_b32 v28, 3, v28
s_lshl_b32 s80, s56, 9
s_add_u32 s80, s80, 4480
buffer_load_dwordx2 v[28:29], v28, s[8:11], s80 offen sc1
s_waitcnt vmcnt(0)
s_add_u32 s84, s84, 1
v_cmp_eq_u64 vcc, s[12:13], v[28:29]
s_cmp_eq_u64 vcc, -1
s_cbranch_scc1 .Lok_84%=
s_cmp_lt_u32 s84, 1048576
s_cbranch_scc1 .Llp_85%=
s_branch .Lok_84%=
.Lspin_86%=:
s_mov_b32 s84, 0
.Llp_88%=:
s_sleep 2
buffer_load_dwordx2 v[8:9], v4, s[8:11], 0 offen offset:2560 sc1
buffer_load_dwordx2 v[10:11], v5, s[8:11], 0 offen offset:3072 sc1
buffer_load_dwordx2 v[12:13], v6, s[8:11], 0 offen offset:3584 sc1
s_waitcnt vmcnt(0)
s_add_u32 s84, s84, 1
v_cmp_eq_u64 vcc, s[12:13], v[8:9]
v_cmp_eq_u64_e64 s[86:87], s[12:13], v[10:11]
s_and_b64 vcc, vcc, s[86:87]
s_or_b64 vcc, vcc, s[20:21]
s_cmp_eq_u64 vcc, -1
s_cbranch_scc1 .Lok_87%=
s_cmp_lt_u32 s84, 1048576
s_cbranch_scc1 .Llp_88%=
s_branch .Lok_87%=
.Lmk_89%=:
s_nop 7
v_cndmask_b32_e64 v168, v241, v168, s[72:73]
v_cndmask_b32_e64 v169, v241, v169, s[74:75]
v_cndmask_b32_e64 v170, v241, v170, s[76:77]
v_cndmask_b32_e64 v171, v241, v171, s[78:79]
v_cndmask_b32_e64 v188, v241, v188, s[72:73]
v_cndmask_b32_e64 v189, v241, v189, s[74:75]
v_cndmask_b32_e64 v190, v241, v190, s[76:77]
v_cndmask_b32_e64 v191, v241, v191, s[78:79]
v_mov_b32 v184, v241
v_mov_b32 v185, v241
v_mov_b32 v186, v241
v_mov_b32 v187, v241
s_branch .Lmkr_90%=
.Lpd_93%=:
v_mov_b64 v[28:29], s[12:13]
s_mov_b64 exec, 1
buffer_store_dwordx2 v[28:29], off, s[8:11], s69 sc1
s_mov_b64 exec, -1
s_branch .Lpdr_94%=
.Lspin_95%=:
s_mov_b32 s84, 0
.Llp_97%=:
s_sleep 2
v_bfe_u32 v28, v1, 4, 2
v_lshlrev_b32 v28, 3, v28
buffer_load_dwordx2 v[28:29], v28, s[8:11], s55 offen sc1
s_waitcnt vmcnt(0)
s_add_u32 s84, s84, 1
v_cmp_eq_u64 vcc, s[12:13], v[28:29]
s_cmp_eq_u64 vcc, -1
s_cbranch_scc1 .Lok_96%=
s_cmp_lt_u32 s84, 1048576
s_cbranch_scc1 .Llp_97%=
s_branch .Lok_96%=
.LL_100%=:
s_mov_b32 s84, 0
.LL_102%=:
s_sleep 2
buffer_load_dwordx2 v[12:13], v27, s[8:11], 0 offen offset:3584 sc1
s_waitcnt vmcnt(0)
s_add_u32 s84, s84, 1
v_xor_b32 v12, s12, v12
v_xor_b32 v13, s13, v13
v_lshrrev_b32 v3, 11, v12
v_or_b32 v13, v13, v3
v_cmp_eq_u32 vcc, 0, v13
s_cmp_eq_u64 vcc, -1
s_cbranch_scc1 .LL_101%=
s_cmp_lt_u32 s84, 1048576
s_cbranch_scc1 .LL_102%=
s_branch .LL_101%=
.Lup_98%=:
v_lshrrev_b32 v3, 8, v1
v_lshlrev_b32 v3, 5, v3
s_ff1_i32_b32 s69, s68
s_mul_i32 s66, s69, 20
s_mul_i32 s67, s58, 5
s_add_u32 s66, s66, s67
s_lshl_b32 s66, s66, 7
s_add_u32 s66, s66, 98304
s_lshl_b32 s67, s14, 7
s_add_u32 s67, s67, s66
buffer_load_dwordx4 v[160:163], v3, s[8:11], s67 offen offset:0 sc1
buffer_load_dwordx4 v[164:167], v3, s[8:11], s67 offen offset:16 sc1
buffer_load_dwordx4 v[168:171], v3, s[8:11], s66 offen offset:512 sc1
buffer_load_dwordx4 v[172:175], v3, s[8:11], s66 offen offset:528 sc1
s_branch .Lupr_99%=
.Lpd_103%=:
v_mov_b64 v[28:29], s[12:13]
s_mov_b64 exec, 1
buffer_store_dwordx2 v[28:29], off, s[8:11], s69 sc1
s_mov_b64 exec, -1
s_branch .Lpdr_104%=
.Lspin_105%=:
s_mov_b32 s84, 0
.Llp_107%=:
s_sleep 2
v_bfe_u32 v28, v1, 4, 2
v_lshlrev_b32 v28, 3, v28
buffer_load_dwordx2 v[28:29], v28, s[8:11], s54 offen sc1
s_waitcnt vmcnt(0)
s_add_u32 s84, s84, 1
v_cmp_eq_u64 vcc, s[12:13], v[28:29]
s_cmp_eq_u64 vcc, -1
s_cbranch_scc1 .Lok_106%=
s_cmp_lt_u32 s84, 1048576
s_cbranch_scc1 .Llp_107%=
s_branch .Lok_106%=
.Lspin_108%=:
s_mov_b32 s84, 0
.Llp_110%=:
s_sleep 2
v_bfe_u32 v28, v1, 4, 2
v_lshlrev_b32 v28, 3, v28
buffer_load_dwordx2 v[28:29], v28, s[8:11], s55 offen sc1
s_waitcnt vmcnt(0)
s_add_u32 s84, s84, 1
v_cmp_eq_u64 vcc, s[12:13], v[28:29]
s_cmp_eq_u64 vcc, -1
s_cbranch_scc1 .Lok_109%=
s_cmp_lt_u32 s84, 1048576
s_cbranch_scc1 .Llp_110%=
s_branch .Lok_109%=
.LL_113%=:
s_mov_b32 s84, 0
.LL_115%=:
s_sleep 2
buffer_load_dwordx2 v[12:13], v27, s[8:11], 0 offen offset:3584 sc1
s_waitcnt vmcnt(0)
s_add_u32 s84, s84, 1
v_xor_b32 v12, s12, v12
v_xor_b32 v13, s13, v13
v_lshrrev_b32 v3, 11, v12
v_or_b32 v13, v13, v3
v_cmp_eq_u32 vcc, 0, v13
s_cmp_eq_u64 vcc, -1
s_cbranch_scc1 .LL_114%=
s_cmp_lt_u32 s84, 1048576
s_cbranch_scc1 .LL_115%=
s_branch .LL_114%=
.Lup_111%=:
v_lshrrev_b32 v3, 8, v1
v_lshlrev_b32 v3, 5, v3
s_ff1_i32_b32 s69, s68
s_mul_i32 s66, s69, 20
s_mul_i32 s67, s58, 5
s_add_u32 s66, s66, s67
s_lshl_b32 s66, s66, 7
s_add_u32 s66, s66, 98304
s_lshl_b32 s67, s14, 7
s_add_u32 s67, s67, s66
buffer_load_dwordx4 v[160:163], v3, s[8:11], s67 offen offset:0 sc1
buffer_load_dwordx4 v[164:167], v3, s[8:11], s67 offen offset:16 sc1
buffer_load_dwordx4 v[168:171], v3, s[8:11], s66 offen offset:512 sc1
buffer_load_dwordx4 v[172:175], v3, s[8:11], s66 offen offset:528 sc1
s_branch .Lupr_112%=
.Lend_138%=:
.Lend%=:
.purgem W
.purgem D
.purgem Ra
.purgem Ta
.purgem Zvaa
.purgem Zaaa
.purgem A
.purgem U
.purgem Mvaa
.purgem Maaa
.purgem K
.purgem Rv
.purgem Mvav
.purgem G
.purgem Dc
.purgem Zvav
.purgem Cvav
.purgem X
.purgem Cvaa
.purgem Maav
.purgem Mvvv
.purgem Zvvv
)"
    : : [ka] "s"(__builtin_amdgcn_kernarg_segment_ptr()), [bid] "s"(blockIdx.x), [tid] "v"(threadIdx.x)
    : "v1", "v2", "v3", "v4", "v5", "v6", "v7", "v8", "v9", "v10", "v11", "v12", "v13", "v14", "v15", "v16", "v17", "v18", "v19", "v20", "v21", "v22", "v23", "v24", "v25", "v26", "v27", "v28", "v29", "v30", "v31", "v32", "v33", "v34", "v35", "v36", "v37", "v38", "v39", "v40", "v41", "v42", "v43", "v44", "v45", "v46", "v47", "v48", "v49", "v50", "v51", "v52", "v53", "v54", "v55", "v56", "v57", "v58", "v59", "v60", "v61", "v62", "v63", "v64", "v65", "v66", "v67", "v68", "v69", "v70", "v71", "v72", "v73", "v74", "v75", "v76", "v77", "v78", "v79", "v80", "v81", "v82", "v83", "v84", "v85", "v86", "v87", "v88", "v89", "v90", "v91", "v92", "v93", "v94", "v95", "v96", "v97", "v98", "v99", "v100", "v101", "v102", "v103", "v104", "v105", "v106", "v107", "v108", "v109", "v110", "v111", "v112", "v113", "v114", "v115", "v116", "v117", "v118", "v119", "v120", "v121", "v122", "v123", "v124", "v125", "v126", "v127", "v128", "v129", "v130", "v131", "v132", "v133", "v134", "v135", "v136", "v137", "v138", "v139", "v140", "v141", "v142", "v143", "v144", "v145", "v146", "v147", "v148", "v149", "v150", "v151", "v152", "v153", "v154", "v155", "v156", "v157", "v158", "v159", "v160", "v161", "v162", "v163", "v164", "v165", "v166", "v167", "v168", "v169", "v170", "v171", "v172", "v173", "v174", "v175", "v176", "v177", "v178", "v179", "v180", "v181", "v182", "v183", "v184", "v185", "v186", "v187", "v188", "v189", "v190", "v191", "v192", "v193", "v194", "v195", "v196", "v197", "v198", "v199", "v200", "v201", "v202", "v203", "v204", "v205", "v206", "v207", "v208", "v209", "v210", "v211", "v212", "v213", "v214", "v215", "v216", "v217", "v218", "v219", "v220", "v221", "v222", "v223", "v224", "v225", "v226", "v227", "v228", "v229", "v230", "v231", "v232", "v233", "v234", "v235", "v236", "v237", "v238", "v239", "v240", "v241", "v242", "v243", "v244", "v245", "v246", "v247", "v248", "v249", "v250", "v251", "v252", "v253", "v254", "v255", "a0", "a1", "a2", "a3", "a4", "a5", "a6", "a7", "a8", "a9", "a10", "a11", "a12", "a13", "a14", "a15", "a16", "a17", "a18", "a19", "a20", "a21", "a22", "a23", "a24", "a25", "a26", "a27", "a28", "a29", "a30", "a31", "a32", "a33", "a34", "a35", "a36", "a37", "a38", "a39", "a40", "a41", "a42", "a43", "a44", "a45", "a46", "a47", "a48", "a49", "a50", "a51", "a52", "a53", "a54", "a55", "a56", "a57", "a58", "a59", "a60", "a61", "a62", "a63", "a64", "a65", "a66", "a67", "a68", "a69", "a70", "a71", "a72", "a73", "a74", "a75", "a76", "a77", "a78", "a79", "a80", "a81", "a82", "a83", "a84", "a85", "a86", "a87", "a88", "a89", "a90", "a91", "a92", "a93", "a94", "a95", "a96", "a97", "a98", "a99", "a100", "a101", "a102", "a103", "a104", "a105", "a106", "a107", "a108", "a109", "a110", "a111", "a112", "a113", "a114", "a115", "a116", "a117", "a118", "a119", "a120", "a121", "a122", "a123", "a124", "a125", "a126", "a127", "a128", "a129", "a130", "a131", "a132", "a133", "a134", "a135", "a136", "a137", "a138", "a139", "a140", "a141", "a142", "a143", "a144", "a145", "a146", "a147", "a148", "a149", "a150", "a151", "a152", "a153", "a154", "a155", "a156", "a157", "a158", "a159", "a160", "a161", "a162", "a163", "a164", "a165", "a166", "a167", "a168", "a169", "a170", "a171", "a172", "a173", "a174", "a175", "a176", "a177", "a178", "a179", "a180", "a181", "a182", "a183", "a184", "a185", "a186", "a187", "a188", "a189", "a190", "a191", "a192", "a193", "a194", "a195", "a196", "a197", "a198", "a199", "a200", "a201", "a202", "a203", "a204", "a205", "a206", "a207", "a208", "a209", "a210", "a211", "a212", "a213", "a214", "a215", "a216", "a217", "a218", "a219", "a220", "a221", "a222", "a223", "a224", "a225", "a226", "a227", "a228", "a229", "a230", "a231", "a232", "a233", "a234", "a235", "a236", "a237", "a238", "a239", "a240", "a241", "a242", "a243", "a244", "a245", "a246", "a247", "a248", "a249", "a250", "a251", "a252", "a253", "a254", "a255", "s4", "s5", "s6", "s7", "s8", "s9", "s10", "s11", "s12", "s13", "s14", "s15", "s16", "s17", "s18", "s19", "s20", "s21", "s22", "s23", "s24", "s25", "s26", "s27", "s28", "s29", "s30", "s31", "s32", "s33", "s34", "s35", "s36", "s37", "s38", "s39", "s40", "s41", "s42", "s43", "s44", "s45", "s46", "s47", "s48", "s49", "s50", "s51", "s52", "s53", "s54", "s55", "s56", "s57", "s58", "s59", "s60", "s61", "s62", "s63", "s64", "s65", "s66", "s67", "s68", "s69", "s70", "s71", "s72", "s73", "s74", "s75", "s76", "s77", "s78", "s79", "s80", "s81", "s82", "s83", "s84", "s85", "s86", "s87", "s88", "s89", "s90", "s91", "s92", "s93", "s94", "s95", "s96", "s97", "s98", "s99", "vcc", "scc", "memory"
  );
}
extern "C" void kernel_launch(void* const* d_in, const int* in_sizes, int n_in, void* d_out, int out_size, void* d_ws, size_t ws_size, hipStream_t stream) {
  (void)hipFuncSetAttribute((const void*)mega_kernel, hipFuncAttributeMaxDynamicSharedMemorySize, LDS_BYTES);
  mega_kernel<<<256, 256, LDS_BYTES, stream>>>((const bf16*)d_in[0], (const bf16*)d_in[1], (const bf16*)d_in[2], (const bf16*)d_in[3], (const bf16*)d_in[4], (const bf16*)d_in[5],
      (const bf16*)d_in[7], (const bf16*)d_in[8], (const bf16*)d_in[9], (const bf16*)d_in[10], (char*)d_ws + 0, (bf16*)d_out);
}
```

```cpp
#include <hip/hip_runtime.h>
#include <hip/hip_bf16.h>
typedef __hip_bfloat16 bf16;
constexpr int LDS_BYTES = 163840;
__global__ __launch_bounds__(256, 1) void mega_kernel(const bf16* x, const bf16* ckv, const bf16* wq, const bf16* wuk, const bf16* wuv, const bf16* wo,
    const bf16* w_r, const bf16* wg, const bf16* wu, const bf16* wd, char* ws, bf16* out) {
  asm volatile(
R"(.macro W n
s_waitcnt lgkmcnt(\n)
.endm
.macro D v,r,s,o
buffer_load_dwordx4 v\v, s[\r:\r+3], s\s offen offset:\o lds
.endm
.macro Ra d,a,o
ds_read_b128 a[\d:\d+3], v\a offset:\o
.endm
.macro Ta d,a,o
ds_read_b64_tr_b16 a[\d:\d+1], v\a offset:\o
.endm
.macro Zvaa d,a,b
v_mfma_f32_16x16x32_bf16 v[\d:\d+3], a[\a:\a+3], a[\b:\b+3], 0
.endm
.macro Zaaa d,a,b
v_mfma_f32_16x16x32_bf16 a[\d:\d+3], a[\a:\a+3], a[\b:\b+3], 0
.endm
.macro A d,a,b
v_add_f32 v\d, v\a, v\b
.endm
.macro U d,a,b
v_mul_f32 v\d, v\a, v\b
.endm
.macro Mvaa d,a,b
v_mfma_f32_16x16x32_bf16 v[\d:\d+3], a[\a:\a+3], a[\b:\b+3], v[\d:\d+3]
.endm
.macro Maaa d,a,b
v_mfma_f32_16x16x32_bf16 a[\d:\d+3], a[\a:\a+3], a[\b:\b+3], a[\d:\d+3]
.endm
.macro K d,a,b
v_cvt_pk_bf16_f32 v\d, v\a, v\b
.endm
.macro Rv d,a,o
ds_read_b128 v[\d:\d+3], v\a offset:\o
.endm
.macro Mvav d,a,b
v_mfma_f32_16x16x32_bf16 v[\d:\d+3], a[\a:\a+3], v[\b:\b+3], v[\d:\d+3]
.endm
.macro G d,a
v_accvgpr_read_b32 v\d, a\a
.endm
.macro Dc v,r,s,o
buffer_load_dwordx4 v\v, s[\r:\r+3], s\s offen offset:\o sc1 lds
.endm
.macro Zvav d,a,b
v_mfma_f32_16x16x32_bf16 v[\d:\d+3], a[\a:\a+3], v[\b:\b+3], 0
.endm
.macro Cvav d,a,b,c
v_mfma_f32_16x16x32_bf16 v[\d:\d+3], a[\a:\a+3], v[\b:\b+3], v[\c:\c+3]
.endm
.macro X d
v_exp_f32 v\d, v\d
.endm
.macro Cvaa d,a,b,c
v_mfma_f32_16x16x32_bf16 v[\d:\d+3], a[\a:\a+3], a[\b:\b+3], v[\c:\c+3]
.endm
.macro Maav d,a,b
v_mfma_f32_16x16x32_bf16 a[\d:\d+3], a[\a:\a+3], v[\b:\b+3], a[\d:\d+3]
.endm
.macro Mvvv d,a,b
v_mfma_f32_16x16x32_bf16 v[\d:\d+3], v[\a:\a+3], v[\b:\b+3], v[\d:\d+3]
.endm
.macro Zvvv d,a,b
v_mfma_f32_16x16x32_bf16 v[\d:\d+3], v[\a:\a+3], v[\b:\b+3], 0
.endm
s_load_dwordx16 s[16:31], %[ka], 0
s_load_dwordx8 s[32:39], %[ka], 64
s_mov_b32 s15, %[bid]
v_lshrrev_b32 v2, 6, %[tid]
v_and_b32 v100, 63, %[tid]
v_readfirstlane_b32 s14, v2
v_lshlrev_b32 v1, 4, v100
v_lshrrev_b32 v101, 4, v100
v_and_b32 v102, 15, v100
s_and_b32 s59, s15, 7
s_bfe_u32 s57, s15, 0x30003
s_lshr_b32 s58, s15, 6
s_lshl_b32 s56, s59, 2
s_add_u32 s56, s56, s58
v_and_b32 v88, 0x1f0, v1
v_lshrrev_b32 v105, 1, v101
v_mul_u32_u24 v105, 1280, v105
v_add_u32 v28, v88, v105
s_lshl_b32 s85, s15, 2
s_add_u32 s85, s85, s14
s_mul_i32 s84, s85, 2560
v_and_b32 v105, 1, v101
v_lshl_or_b32 v29, v105, 31, v28
v_lshlrev_b32 v104, 1, v101
v_xor_b32 v104, v104, v102
v_lshlrev_b32 v104, 4, v104
v_lshl_add_u32 v16, v101, 13, v104
v_xor_b32 v105, 128, v16
v_add_u32 v17, 31744, v16
v_add_u32 v18, 63488, v105
v_add_u32 v19, 95232, v105
v_add_u32 v20, 131072, v16
v_add_u32 v21, 162816, v16
v_add_u32 v22, 194560, v105
v_add_u32 v23, 226304, v105
s_lshl_b32 s80, s14, 3
s_add_u32 s80, s80, s57
s_lshl_b32 s80, s80, 15
v_and_b32 v104, 7, v100
v_xor_b32 v104, v104, v101
v_lshlrev_b32 v104, 4, v104
v_lshrrev_b32 v105, 3, v100
v_lshl_add_u32 v104, v105, 10, v104
v_add_u32 v2, s80, v104
v_xor_b32 v105, 64, v2
v_add_u32 v3, 7168, v105
v_add_u32 v4, 14336, v2
v_add_u32 v5, 21504, v105
v_xor_b32 v6, 1835008, v2
v_xor_b32 v7, 1835008, v3
v_xor_b32 v8, 1835008, v4
v_xor_b32 v9, 1835008, v5
s_lshl_b32 s63, s14, 13
s_add_u32 s61, s63, 65536
s_and_b32 s80, s14, 1
s_lshr_b32 s81, s14, 1
s_lshl_b32 s62, s80, 14
s_lshl_b32 s82, s81, 13
s_add_u32 s62, s62, s82
s_lshl_b32 s81, s81, 18
s_lshl_b32 s83, s56, 8
s_add_u32 s81, s81, s83
s_mov_b32 s64, 0
s_cmp_eq_u32 s80, 0
W 0
s_cselect_b32 s44, s22, s24
s_cselect_b32 s45, s23, s25
s_mov_b32 s69, 0
s_cmp_eq_u32 s14, 2
s_cbranch_scc1 .LL_1%=
s_cmp_eq_u32 s14, 3
s_cbranch_scc1 .LL_2%=
s_lshl_b32 s80, s58, 3
s_add_u32 s80, s80, s57
s_lshl_b32 s80, s80, 15
s_lshl_b32 s82, s14, 7
s_add_u32 s80, s80, s82
s_add_u32 s80, s80, 256
v_lshl_add_u32 v104, v88, 6, s80
v_and_b32 v105, 512, v1
v_mul_u32_u24 v105, 3584, v105
v_xor_b32 v104, v104, v105
v_mov_b32 v140, s18
v_mov_b32 v141, s19
s_mov_b32 s68, 256
s_branch .LL_3%=
.LL_1%=:
v_lshrrev_b32 v104, 6, v1
v_lshlrev_b32 v104, 13, v104
v_and_b32 v105, 16, v1
v_lshl_add_u32 v104, v105, 3, v104
s_lshl_b32 s80, s57, 17
s_add_u32 s80, s80, s83
s_add_u32 s80, s80, 1048576
v_add_u32 v104, s80, v104
v_and_b32 v105, 32, v1
v_cmp_eq_u32 vcc, 0, v105
v_mov_b32 v140, s24
v_mov_b32 v141, s25
v_mov_b32 v105, s22
v_mov_b32 v106, s23
v_cndmask_b32 v140, v140, v105, vcc
v_cndmask_b32 v141, v141, v106, vcc
s_mov_b32 s68, 1048576
s_branch .LL_3%=
.LL_2%=:
v_lshrrev_b32 v104, 5, v1
v_lshlrev_b32 v104, 13, v104
v_and_b32 v105, 16, v1
v_lshl_add_u32 v104, v105, 3, v104
s_mul_i32 s80, s57, 655360
s_add_u32 s80, s80, s83
v_add_u32 v104, s80, v104
v_mov_b32 v140, s20
v_mov_b32 v141, s21
s_mov_b32 s68, 196608
.LL_3%=:
v_mov_b32 v105, 0
v_lshl_add_u64 v[140:141], v[104:105], 0, v[140:141]
s_mov_b32 s40, s18
s_and_b32 s41, s19, 0xffff
s_mov_b32 s42, 0x200000
s_mov_b32 s43, 0x20000
s_add_u32 s44, s44, s81
s_addc_u32 s45, s45, 0
s_and_b32 s45, s45, 0xffff
s_mov_b32 s46, 0x400000
s_mov_b32 s47, 0x20000
s_sub_u32 s46, s46, s81
s_load_dwordx2 s[12:13], s[36:37], 0
s_add_u32 m0, s61, 0
s_nop 0
D 2,40,64,0
D 3,40,64,1024
D 4,40,64,2048
D 5,40,64,3072
s_add_u32 m0, s61, 4096
s_nop 0
D 6,40,64,0
D 7,40,64,1024
D 8,40,64,2048
D 9,40,64,3072
s_lshl_b32 s65, s64, 12
s_add_u32 m0, s62, 0
s_nop 0
D 16,44,65,0
D 17,44,65,1024
D 18,44,65,2048
D 19,44,65,3072
s_add_u32 m0, s62, 4096
s_nop 0
D 20,44,65,0
D 21,44,65,1024
D 22,44,65,2048
D 23,44,65,3072
s_mov_b32 s52, s16
s_and_b32 s53, s17, 0xffff
s_mov_b32 s54, 2621440
s_mov_b32 s55, 0x20000
buffer_load_dwordx4 v[32:35], v28, s[52:55], s84 offen
buffer_load_dwordx4 v[36:39], v28, s[52:55], s84 offen offset:512
buffer_load_dwordx4 v[40:43], v29, s[52:55], s84 offen offset:1024
s_mov_b32 s52, s28
s_and_b32 s53, s29, 0xffff
s_mov_b32 s54, 1280
s_mov_b32 s55, 0x20000
buffer_load_dwordx4 v[44:47], v88, s[52:55], 0 offen offset:0
buffer_load_dwordx4 v[48:51], v88, s[52:55], 0 offen offset:512
buffer_load_dwordx4 v[52:55], v88, s[52:55], 0 offen offset:1024
s_getpc_b64 s[6:7]
.Lbeg%=:
s_lshr_b32 s80, s15, 3
s_lshl_b32 s80, s80, 2
s_add_u32 s80, s80, s14
s_lshl_b32 s80, s80, 11
v_min_u32 v104, 240, v1
v_lshl_add_u32 v104, v104, 3, s80
v_min_u32 v104, .Lend%=-.Lbeg%=-4, v104
global_load_dword v142, v104, s[6:7]
s_add_u32 s64, s64, 128
s_and_b32 s64, s64, 1023
s_add_u32 m0, s61, 32768
s_nop 0
D 2,40,64,0
D 3,40,64,1024
D 4,40,64,2048
D 5,40,64,3072
s_add_u32 m0, s61, 36864
s_nop 0
D 6,40,64,0
D 7,40,64,1024
D 8,40,64,2048
D 9,40,64,3072
s_lshl_b32 s65, s64, 12
s_add_u32 m0, s62, 32768
s_nop 0
D 16,44,65,0
D 17,44,65,1024
D 18,44,65,2048
D 19,44,65,3072
s_add_u32 m0, s62, 36864
s_nop 0
D 20,44,65,0
D 21,44,65,1024
D 22,44,65,2048
D 23,44,65,3072
global_load_dword v142, v[140:141], off
v_lshl_add_u64 v[140:141], v[140:141], 0, s[68:69]
s_cmp_eq_u32 s14, 3
s_cbranch_scc0 .LL_4%=
s_lshr_b32 s80, s15, 3
s_cmp_lt_u32 s80, 8
s_cbranch_scc0 .LL_4%=
v_lshlrev_b32 v104, 16, v1
s_cmp_eq_u32 s80, 0
s_cselect_b64 s[86:87], s[26:27], s[86:87]
s_cselect_b32 s81, 5242876, s81
s_cmp_eq_u32 s80, 1
s_cselect_b64 s[86:87], s[30:31], s[86:87]
s_cselect_b32 s81, 2211836, s81
s_cmp_eq_u32 s80, 2
s_cselect_b64 s[86:87], s[32:33], s[86:87]
s_cselect_b32 s81, 2211836, s81
s_cmp_eq_u32 s80, 3
s_cselect_b64 s[86:87], s[34:35], s[86:87]
s_cselect_b32 s81, 2211836, s81
s_cmp_eq_u32 s80, 4
s_cselect_b64 s[86:87], s[38:39], s[86:87]
s_cselect_b32 s81, 2621436, s81
s_cmp_eq_u32 s80, 5
s_cselect_b64 s[86:87], s[20:21], s[86:87]
s_cselect_b32 s81, 5242876, s81
s_cmp_lt_u32 s80, 6
s_cbranch_scc1 .LL_5%=
s_mov_b64 s[86:87], s[36:37]
s_sub_u32 s82, s80, 6
s_mul_i32 s82, s82, 39845888
s_add_u32 s81, s82, 38797312
v_add_u32 v104, s82, v104
.LL_5%=:
v_min_u32 v104, s81, v104
global_load_dword v142, v104, s[86:87] sc1
.LL_4%=:
v_lshrrev_b32 v104, 1, v102
v_xor_b32 v104, v104, v101
v_lshlrev_b32 v104, 4, v104
v_lshl_add_u32 v104, v102, 7, v104
s_lshr_b32 s81, s14, 1
s_lshl_b32 s81, s81, 14
s_add_u32 s81, s81, 65536
v_add_u32 v26, s81, v104
v_xor_b32 v27, 64, v26
v_and_b32 v105, 1, v101
v_lshrrev_b32 v104, 2, v102
v_lshl_or_b32 v105, v105, 2, v104
v_lshl_add_u32 v104, v101, 3, v104
v_lshlrev_b32 v104, 8, v104
v_lshl_add_u32 v104, v105, 5, v104
v_and_b32 v105, 3, v102
v_lshl_add_u32 v10, v105, 3, v104
v_xor_b32 v11, 32, v10
v_xor_b32 v12, 64, v10
v_xor_b32 v13, 96, v10
v_xor_b32 v14, 128, v10
v_xor_b32 v15, 160, v10
v_xor_b32 v24, 192, v10
v_xor_b32 v25, 224, v10
.LL_9%=:
s_lshr_b32 s81, s15, 3
s_cmp_eq_u32 s14, 2
s_cbranch_scc1 .LL_6%=
s_cmp_eq_u32 s14, 3
s_cbranch_scc1 .LL_7%=
s_cmp_eq_u32 s14, 0
s_cselect_b64 s[76:77], s[30:31], s[32:33]
s_mov_b32 s72, 3456
s_mov_b32 s73, 28
s_mul_i32 s74, s81, 69120
s_mul_i32 s82, s59, 448
s_andn2_b32 s82, s82, 127
s_add_u32 s74, s74, s82
s_mov_b32 s75, 2211836
s_branch .LL_8%=
.LL_6%=:
s_mov_b64 s[76:77], s[34:35]
s_mov_b32 s72, 512
s_mov_b32 s73, 24
s_mul_i32 s74, s81, 8960
s_mul_i32 s82, s59, 286720
s_add_u32 s74, s74, s82
s_mov_b32 s75, 2211836
s_branch .LL_8%=
.LL_7%=:
s_mov_b64 s[76:77], s[26:27]
s_mov_b32 s72, 512
s_mov_b32 s73, 4
s_mul_i32 s74, s81, 20480
s_mul_i32 s82, s59, 655360
s_add_u32 s74, s74, s82
s_mov_b32 s75, 5242876
s_branch .LL_8%=
.LL_8%=:
s_mov_b32 s8, s36
s_and_b32 s9, s37, 0xffff
s_mov_b32 s10, 79691776
s_mov_b32 s11, 0x20000
s_add_u32 s48, s20, s83
s_addc_u32 s49, s21, 0
s_and_b32 s49, s49, 0xffff
s_mov_b32 s50, 5242880
s_mov_b32 s51, 0x20000
s_sub_u32 s50, s50, s83
s_lshl_b32 s80, s57, 1
s_and_b32 s81, s14, 1
s_add_u32 s80, s80, s81
s_sub_u32 s81, 31, s80
s_cmp_lt_u32 s14, 2
s_cselect_b32 s60, s80, s81
s_mul_i32 s28, s60, 81920
s_add_u32 s28, s28, 131072
s_add_u32 s4, s28, 10240
s_mov_b32 s80, 0
s_lshl_b32 s81, s80, 9
s_add_u32 s28, s28, s81
s_add_u32 s5, s80, s14
s_cmp_ge_u32 s5, 20
s_cselect_b32 s81, 20, 0
s_sub_u32 s5, s5, s81
s_and_b32 s80, s14, 1
s_mul_i32 s29, s80, 17172480
s_mul_i32 s80, s56, 536576
s_add_u32 s29, s29, s80
s_lshl_b32 s80, s57, 13
s_add_u32 s29, s29, s80
s_add_u32 s29, s29, 4194304
W 0
s_add_u32 s12, s12, 0x7f4a7c15
s_addc_u32 s13, s13, 0x9e3779b9
s_bitcmp1_b32 s14, 0
s_cbranch_scc1 .Lv_10%=
s_waitcnt vmcnt(24)
s_barrier
Ra 32,26,0
Ta 0,10,0
Ta 2,10,1024
Ra 36,26,2048
Ra 40,26,4096
Ra 44,26,6144
Ra 48,26,8192
Ra 52,26,10240
Ra 56,26,12288
Ra 60,26,14336
Ta 4,11,0
Ta 6,11,1024
W 6
Zvaa 160,0,32
Ta 8,12,0
Ta 10,12,1024
Zvaa 192,0,36
Ta 12,13,0
Ta 14,13,1024
Zvaa 224,0,40
s_waitcnt vmcnt(21)
Zaaa 160,0,44
W 6
v_lshlrev_b32 v56, 16, v32
Zaaa 192,0,48
v_and_b32 v57, 0xffff0000, v32
v_lshlrev_b32 v58, 16, v33
Zaaa 224,0,52
v_and_b32 v59, 0xffff0000, v33
v_lshlrev_b32 v60, 16, v34
Zaaa 64,0,56
v_and_b32 v61, 0xffff0000, v34
v_lshlrev_b32 v62, 16, v35
Zaaa 96,0,60
W 4
v_and_b32 v63, 0xffff0000, v35
)"
R"(v_lshlrev_b32 v64, 16, v36
Zaaa 100,4,60
Ta 16,14,0
v_and_b32 v65, 0xffff0000, v36
Zaaa 68,4,56
Ta 18,14,1024
v_lshlrev_b32 v66, 16, v37
Zaaa 228,4,52
v_and_b32 v67, 0xffff0000, v37
v_lshlrev_b32 v68, 16, v38
Zaaa 196,4,48
v_and_b32 v69, 0xffff0000, v38
v_lshlrev_b32 v70, 16, v39
Zaaa 164,4,44
v_and_b32 v71, 0xffff0000, v39
v_lshlrev_b32 v72, 16, v40
Zvaa 228,4,40
v_and_b32 v73, 0xffff0000, v40
v_lshlrev_b32 v74, 16, v41
Zvaa 196,4,36
v_and_b32 v75, 0xffff0000, v41
v_lshlrev_b32 v76, 16, v42
Zvaa 164,4,32
W 4
v_and_b32 v77, 0xffff0000, v42
v_lshlrev_b32 v78, 16, v43
Zvaa 168,8,32
v_and_b32 v79, 0xffff0000, v43
A 80,56,57
Zvaa 200,8,36
Ta 20,15,0
Ta 22,15,1024
Zvaa 232,8,40
A 81,58,59
A 80,80,60
Zaaa 168,8,44
A 81,81,61
A 80,80,62
Zaaa 200,8,48
A 81,81,63
A 80,80,64
Zaaa 232,8,52
A 81,81,65
A 80,80,66
Zaaa 72,8,56
A 81,81,67
A 80,80,68
Zaaa 104,8,60
W 4
A 81,81,69
A 80,80,70
Zaaa 108,12,60
A 81,81,71
A 80,80,72
Zaaa 76,12,56
Ta 24,24,0
A 81,81,73
Zaaa 236,12,52
Ta 26,24,1024
A 80,80,74
Zaaa 204,12,48
A 81,81,75
A 80,80,76
Zaaa 172,12,44
A 81,81,77
A 80,80,78
Zvaa 236,12,40
A 81,81,79
A 80,80,81
Zvaa 204,12,36
Ra 128,27,0
Zvaa 172,12,32
W 5
Ra 132,27,2048
v_add_f32_dpp v80, v80, v80 quad_perm:[1,0,3,2] row_mask:0xf bank_mask:0xf
Zvaa 176,16,32
Ta 28,25,0
Ta 30,25,1024
Zvaa 208,16,36
v_add_f32_dpp v80, v80, v80 quad_perm:[2,3,0,1] row_mask:0xf bank_mask:0xf
Zvaa 240,16,40
Ra 136,27,4096
Zaaa 176,16,44
v_add_f32_dpp v80, v80, v80 row_half_mirror row_mask:0xf bank_mask:0xf
Zaaa 208,16,48
Zaaa 240,16,52
v_add_f32_dpp v80, v80, v80 row_mirror row_mask:0xf bank_mask:0xf
v_mov_b32 v81, v80
Zaaa 80,16,56
Ra 140,27,6144
Zaaa 112,16,60
W 8
v_permlane16_swap_b32 v80, v81
A 80,80,81
Zaaa 116,20,60
Ta 0,10,8192
v_mul_f32 v84, 0x3acccccd, v80
Zaaa 84,20,56
s_mov_b32 s86, 0xffff
s_mov_b32 s87, 0xffff
v_sub_f32 v82, v56, v84
Zaaa 244,20,52
U 80,82,82
v_sub_f32 v83, v57, v84
Zaaa 212,20,48
v_fmac_f32 v80, v83, v83
v_sub_f32 v82, v58, v84
Zaaa 180,20,44
v_fmac_f32 v80, v82, v82
v_sub_f32 v83, v59, v84
Zvaa 244,20,40
v_fmac_f32 v80, v83, v83
v_sub_f32 v82, v60, v84
Zvaa 212,20,36
v_fmac_f32 v80, v82, v82
v_sub_f32 v83, v61, v84
Zvaa 180,20,32
W 7
Ta 2,10,9216
v_fmac_f32 v80, v83, v83
Zvaa 184,24,32
Ra 144,27,8192
v_sub_f32 v82, v62, v84
Zvaa 216,24,36
v_fmac_f32 v80, v82, v82
v_sub_f32 v83, v63, v84
Zvaa 248,24,40
v_fmac_f32 v80, v83, v83
v_sub_f32 v82, v64, v84
Zaaa 184,24,44
Ra 148,27,10240
v_fmac_f32 v80, v82, v82
Zaaa 216,24,48
v_sub_f32 v83, v65, v84
v_fmac_f32 v80, v83, v83
Zaaa 248,24,52
v_sub_f32 v82, v66, v84
v_fmac_f32 v80, v82, v82
Zaaa 88,24,56
Ra 152,27,12288
v_sub_f32 v83, v67, v84
Zaaa 120,24,60
W 7
v_fmac_f32 v80, v83, v83
v_sub_f32 v82, v68, v84
Zaaa 124,28,60
v_fmac_f32 v80, v82, v82
v_sub_f32 v83, v69, v84
Zaaa 92,28,56
Ta 4,11,8192
Ta 6,11,9216
Zaaa 252,28,52
v_fmac_f32 v80, v83, v83
v_sub_f32 v82, v70, v84
Zaaa 220,28,48
Ra 156,27,14336
v_fmac_f32 v80, v82, v82
Zaaa 188,28,44
v_sub_f32 v83, v71, v84
v_fmac_f32 v80, v83, v83
Zvaa 252,28,40
v_sub_f32 v82, v72, v84
U 81,82,82
Zvaa 220,28,36
v_sub_f32 v83, v73, v84
v_fmac_f32 v81, v83, v83
Zvaa 188,28,32
W 6
v_sub_f32 v82, v74, v84
v_fmac_f32 v81, v82, v82
Mvaa 160,0,128
v_sub_f32 v83, v75, v84
v_fmac_f32 v81, v83, v83
Mvaa 192,0,132
Ta 8,12,8192
v_sub_f32 v82, v76, v84
Mvaa 224,0,136
Ta 10,12,9216
v_fmac_f32 v81, v82, v82
Maaa 160,0,140
W 2
v_sub_f32 v83, v77, v84
v_fmac_f32 v81, v83, v83
Maaa 192,0,144
v_sub_f32 v82, v78, v84
v_fmac_f32 v81, v82, v82
Maaa 224,0,148
v_sub_f32 v83, v79, v84
v_fmac_f32 v81, v83, v83
Maaa 64,0,152
v_cndmask_b32_e64 v81, 0, v81, s[86:87]
A 80,80,81
Maaa 96,0,156
Ta 12,13,8192
Ta 14,13,9216
Maaa 100,4,156
Ta 16,14,8192
v_add_f32_dpp v80, v80, v80 quad_perm:[1,0,3,2] row_mask:0xf bank_mask:0xf
Maaa 68,4,152
Ta 18,14,9216
Maaa 228,4,148
v_add_f32_dpp v80, v80, v80 quad_perm:[2,3,0,1] row_mask:0xf bank_mask:0xf
Maaa 196,4,144
Maaa 164,4,140
Ta 20,15,8192
v_add_f32_dpp v80, v80, v80 row_half_mirror row_mask:0xf bank_mask:0xf
Mvaa 228,4,136
Ta 22,15,9216
Mvaa 196,4,132
v_add_f32_dpp v80, v80, v80 row_mirror row_mask:0xf bank_mask:0xf
v_mov_b32 v81, v80
Mvaa 164,4,128
W 6
v_permlane16_swap_b32 v80, v81
A 80,80,81
Mvaa 168,8,128
v_mul_f32 v80, 0x3acccccd, v80
K 80,80,80
Mvaa 200,8,132
v_lshlrev_b32 v80, 16, v80
v_add_f32 v80, 0x3727c5ac, v80
Mvaa 232,8,136
K 80,80,80
v_lshlrev_b32 v80, 16, v80
Maaa 168,8,140
v_sqrt_f32 v81, v80
Maaa 200,8,144
v_rcp_f32 v82, v81
Maaa 232,8,148
v_fma_f32 v83, -v81, v81, v80
v_mul_f32 v82, 0.5, v82
Maaa 72,8,152
v_fma_f32 v81, v83, v82, v81
K 86,81,81
Maaa 104,8,156
W 4
Ta 24,24,8192
v_lshlrev_b32 v86, 16, v86
Maaa 108,12,156
v_rcp_f32 v87, v86
Maaa 76,12,152
Ta 26,24,9216
Ta 28,25,8192
Ta 30,25,9216
Maaa 236,12,148
s_waitcnt vmcnt(1) lgkmcnt(0)
s_barrier
K 85,84,84
Maaa 204,12,144
s_add_u32 s64, s64, 128
v_lshlrev_b32 v85, 16, v85
Maaa 172,12,140
v_sub_f32 v56, v56, v85
v_sub_f32 v57, v57, v85
Mvaa 236,12,136
s_and_b32 s64, s64, 1023
K 80,56,57
v_lshlrev_b32 v56, 16, v80
Mvaa 204,12,132
v_and_b32 v57, 0xffff0000, v80
U 80,56,87
Mvaa 172,12,128
U 81,57,87
v_fma_f32 v82, -v80, v86, v56
Mvaa 176,16,128
s_add_u32 m0, s61, 0
v_fma_f32 v83, -v81, v86, v57
v_fmac_f32 v80, v82, v87
Mvaa 208,16,132
v_fmac_f32 v81, v83, v87
K 32,80,81
Mvaa 240,16,136
v_lshlrev_b32 v80, 16, v32
v_and_b32 v81, 0xffff0000, v32
Maaa 176,16,140
v_lshlrev_b32 v82, 16, v44
v_and_b32 v83, 0xffff0000, v44
Maaa 208,16,144
U 84,80,82
v_fmac_f32 v84, v81, v83
Maaa 240,16,148
v_sub_f32 v58, v58, v85
v_sub_f32 v59, v59, v85
Maaa 80,16,152
K 80,58,59
v_lshlrev_b32 v58, 16, v80
Maaa 112,16,156
v_and_b32 v59, 0xffff0000, v80
U 80,58,87
Maaa 116,20,156
U 81,59,87
v_fma_f32 v82, -v80, v86, v58
Maaa 84,20,152
v_fma_f32 v83, -v81, v86, v59
v_fmac_f32 v80, v82, v87
Maaa 244,20,148
v_fmac_f32 v81, v83, v87
K 33,80,81
Maaa 212,20,144
Ra 32,26,32768
v_lshlrev_b32 v80, 16, v33
Maaa 180,20,140
v_and_b32 v81, 0xffff0000, v33
v_lshlrev_b32 v82, 16, v45
Mvaa 244,20,136
v_and_b32 v83, 0xffff0000, v45
v_fmac_f32 v84, v80, v82
Mvaa 212,20,132
Ra 36,26,34816
v_fmac_f32 v84, v81, v83
Mvaa 180,20,128
v_sub_f32 v60, v60, v85
v_sub_f32 v61, v61, v85
Mvaa 184,24,128
K 80,60,61
v_lshlrev_b32 v60, 16, v80
Mvaa 216,24,132
D 2,40,64,0
v_and_b32 v61, 0xffff0000, v80
Mvaa 248,24,136
Ra 40,26,36864
U 80,60,87
Maaa 184,24,140
U 81,61,87
v_fma_f32 v82, -v80, v86, v60
Maaa 216,24,144
v_fma_f32 v83, -v81, v86, v61
v_fmac_f32 v80, v82, v87
Maaa 248,24,148
D 3,40,64,1024
v_fmac_f32 v81, v83, v87
Maaa 88,24,152
Ra 44,26,38912
K 34,80,81
Maaa 120,24,156
v_lshlrev_b32 v80, 16, v34
v_and_b32 v81, 0xffff0000, v34
Maaa 124,28,156
v_lshlrev_b32 v82, 16, v46
v_and_b32 v83, 0xffff0000, v46
Maaa 92,28,152
D 4,40,64,2048
Ta 0,10,32768
Maaa 252,28,148
Ta 2,10,33792
Ra 48,26,40960
Maaa 220,28,144
v_fmac_f32 v84, v80, v82
v_fmac_f32 v84, v81, v83
Maaa 188,28,140
v_sub_f32 v62, v62, v85
v_sub_f32 v63, v63, v85
Mvaa 252,28,136
D 5,40,64,3072
K 80,62,63
Mvaa 220,28,132
Ra 52,26,43008
Ra 56,26,45056
Ta 4,11,32768
Ta 6,11,33792
Ra 60,26,47104
Mvaa 188,28,128
W 6
v_lshlrev_b32 v62, 16, v80
v_and_b32 v63, 0xffff0000, v80
Mvaa 160,0,32
s_add_u32 m0, s61, 4096
U 80,62,87
U 81,63,87
Mvaa 192,0,36
v_fma_f32 v82, -v80, v86, v62
v_fma_f32 v83, -v81, v86, v63
Mvaa 224,0,40
D 6,40,64,0
v_fmac_f32 v80, v82, v87
Maaa 160,0,44
W 0
v_fmac_f32 v81, v83, v87
K 35,80,81
Maaa 192,0,48
v_lshlrev_b32 v80, 16, v35
v_and_b32 v81, 0xffff0000, v35
Maaa 224,0,52
v_lshlrev_b32 v82, 16, v47
v_and_b32 v83, 0xffff0000, v47
Maaa 64,0,56
D 7,40,64,1024
v_fmac_f32 v84, v80, v82
Maaa 96,0,60
v_fmac_f32 v84, v81, v83
v_sub_f32 v64, v64, v85
Maaa 100,4,60
v_sub_f32 v65, v65, v85
K 80,64,65
Maaa 68,4,56
v_lshlrev_b32 v64, 16, v80
v_and_b32 v65, 0xffff0000, v80
Maaa 228,4,52
D 8,40,64,2048
U 80,64,87
)"
R"(Maaa 196,4,48
U 81,65,87
v_fma_f32 v82, -v80, v86, v64
Maaa 164,4,44
Ta 8,12,32768
Ta 10,12,33792
Mvaa 228,4,40
v_fma_f32 v83, -v81, v86, v65
v_fmac_f32 v80, v82, v87
Mvaa 196,4,36
D 9,40,64,3072
v_fmac_f32 v81, v83, v87
Mvaa 164,4,32
W 0
s_lshl_b32 s65, s64, 12
K 36,80,81
Mvaa 168,8,32
s_add_u32 m0, s62, 0
v_lshlrev_b32 v80, 16, v36
v_and_b32 v81, 0xffff0000, v36
Mvaa 200,8,36
v_lshlrev_b32 v82, 16, v48
v_and_b32 v83, 0xffff0000, v48
Mvaa 232,8,40
v_fmac_f32 v84, v80, v82
v_fmac_f32 v84, v81, v83
Maaa 168,8,44
v_sub_f32 v66, v66, v85
v_sub_f32 v67, v67, v85
Maaa 200,8,48
Ta 12,13,32768
Ta 14,13,33792
Maaa 232,8,52
D 16,44,65,0
K 80,66,67
Maaa 72,8,56
v_lshlrev_b32 v66, 16, v80
v_and_b32 v67, 0xffff0000, v80
Maaa 104,8,60
W 0
U 80,66,87
U 81,67,87
Maaa 108,12,60
v_fma_f32 v82, -v80, v86, v66
v_fma_f32 v83, -v81, v86, v67
Maaa 76,12,56
D 17,44,65,1024
v_fmac_f32 v80, v82, v87
Maaa 236,12,52
v_fmac_f32 v81, v83, v87
K 37,80,81
Maaa 204,12,48
v_lshlrev_b32 v80, 16, v37
v_and_b32 v81, 0xffff0000, v37
Maaa 172,12,44
Ta 16,14,32768
Ta 18,14,33792
Mvaa 236,12,40
D 18,44,65,2048
v_lshlrev_b32 v82, 16, v49
Mvaa 204,12,36
v_and_b32 v83, 0xffff0000, v49
v_fmac_f32 v84, v80, v82
Mvaa 172,12,32
W 0
v_fmac_f32 v84, v81, v83
v_sub_f32 v68, v68, v85
Mvaa 176,16,32
v_sub_f32 v69, v69, v85
K 80,68,69
Mvaa 208,16,36
D 19,44,65,3072
v_lshlrev_b32 v68, 16, v80
Mvaa 240,16,40
s_add_u32 m0, s62, 4096
v_and_b32 v69, 0xffff0000, v80
U 80,68,87
Maaa 176,16,44
U 81,69,87
v_fma_f32 v82, -v80, v86, v68
Maaa 208,16,48
Ta 20,15,32768
Ta 22,15,33792
Maaa 240,16,52
D 20,44,65,0
v_fma_f32 v83, -v81, v86, v69
Maaa 80,16,56
v_fmac_f32 v80, v82, v87
v_fmac_f32 v81, v83, v87
Maaa 112,16,60
W 0
K 38,80,81
v_lshlrev_b32 v80, 16, v38
Maaa 116,20,60
v_and_b32 v81, 0xffff0000, v38
v_lshlrev_b32 v82, 16, v50
Maaa 84,20,56
D 21,44,65,1024
v_and_b32 v83, 0xffff0000, v50
Maaa 244,20,52
v_fmac_f32 v84, v80, v82
v_fmac_f32 v84, v81, v83
Maaa 212,20,48
v_sub_f32 v70, v70, v85
v_sub_f32 v71, v71, v85
Maaa 180,20,44
Ta 24,24,32768
Ta 26,24,33792
Mvaa 244,20,40
D 22,44,65,2048
K 80,70,71
Mvaa 212,20,36
v_lshlrev_b32 v70, 16, v80
v_and_b32 v71, 0xffff0000, v80
Mvaa 180,20,32
W 0
U 80,70,87
U 81,71,87
Mvaa 184,24,32
v_fma_f32 v82, -v80, v86, v70
v_fma_f32 v83, -v81, v86, v71
Mvaa 216,24,36
D 23,44,65,3072
v_fmac_f32 v80, v82, v87
Mvaa 248,24,40
v_fmac_f32 v81, v83, v87
K 39,80,81
Maaa 184,24,44
v_lshlrev_b32 v80, 16, v39
v_and_b32 v81, 0xffff0000, v39
Maaa 216,24,48
Ra 128,27,32768
Ra 132,27,34816
Ta 28,25,32768
Ta 30,25,33792
Maaa 248,24,52
global_load_dword v142, v[140:141], off
v_lshl_add_u64 v[140:141], v[140:141], 0, s[68:69]
v_lshlrev_b32 v82, 16, v51
Maaa 88,24,56
v_and_b32 v83, 0xffff0000, v51
v_fmac_f32 v84, v80, v82
Maaa 120,24,60
W 0
v_fmac_f32 v84, v81, v83
v_sub_f32 v72, v72, v85
Maaa 124,28,60
v_sub_f32 v73, v73, v85
K 80,72,73
Maaa 92,28,56
v_lshlrev_b32 v72, 16, v80
v_and_b32 v73, 0xffff0000, v80
Maaa 252,28,52
Ra 136,27,36864
Ra 140,27,38912
Maaa 220,28,48
U 80,72,87
U 81,73,87
Maaa 188,28,44
Ta 0,10,40960
Ta 2,10,41984
Mvaa 252,28,40
v_fma_f32 v82, -v80, v86, v72
v_fma_f32 v83, -v81, v86, v73
Mvaa 220,28,36
Ra 144,27,40960
Ra 148,27,43008
Ra 152,27,45056
Ta 4,11,40960
Ta 6,11,41984
Ra 156,27,47104
Mvaa 188,28,32
W 6
v_fmac_f32 v80, v82, v87
v_fmac_f32 v81, v83, v87
Mvaa 160,0,128
K 40,80,81
v_lshlrev_b32 v80, 16, v40
Mvaa 192,0,132
v_and_b32 v81, 0xffff0000, v40
v_lshlrev_b32 v82, 16, v52
Mvaa 224,0,136
v_and_b32 v83, 0xffff0000, v52
v_fmac_f32 v84, v80, v82
Maaa 160,0,140
W 0
v_fmac_f32 v84, v81, v83
v_sub_f32 v74, v74, v85
Maaa 192,0,144
v_sub_f32 v75, v75, v85
K 80,74,75
Maaa 224,0,148
v_lshlrev_b32 v74, 16, v80
v_and_b32 v75, 0xffff0000, v80
Maaa 64,0,152
U 80,74,87
U 81,75,87
Maaa 96,0,156
v_fma_f32 v82, -v80, v86, v74
v_fma_f32 v83, -v81, v86, v75
Maaa 100,4,156
v_fmac_f32 v80, v82, v87
v_fmac_f32 v81, v83, v87
Maaa 68,4,152
K 41,80,81
v_lshlrev_b32 v80, 16, v41
Maaa 228,4,148
v_and_b32 v81, 0xffff0000, v41
v_lshlrev_b32 v82, 16, v53
Maaa 196,4,144
v_and_b32 v83, 0xffff0000, v53
v_fmac_f32 v84, v80, v82
Maaa 164,4,140
Ta 8,12,40960
Ta 10,12,41984
Mvaa 228,4,136
v_fmac_f32 v84, v81, v83
v_sub_f32 v76, v76, v85
Mvaa 196,4,132
v_sub_f32 v77, v77, v85
K 80,76,77
Mvaa 164,4,128
W 0
v_lshlrev_b32 v76, 16, v80
v_and_b32 v77, 0xffff0000, v80
Mvaa 168,8,128
U 80,76,87
U 81,77,87
Mvaa 200,8,132
v_fma_f32 v82, -v80, v86, v76
v_fma_f32 v83, -v81, v86, v77
Mvaa 232,8,136
v_fmac_f32 v80, v82, v87
v_fmac_f32 v81, v83, v87
Maaa 168,8,140
K 42,80,81
v_lshlrev_b32 v80, 16, v42
Maaa 200,8,144
Ta 12,13,40960
Ta 14,13,41984
Maaa 232,8,148
v_and_b32 v81, 0xffff0000, v42
v_lshlrev_b32 v82, 16, v54
Maaa 72,8,152
v_and_b32 v83, 0xffff0000, v54
v_fmac_f32 v84, v80, v82
Maaa 104,8,156
W 0
v_fmac_f32 v84, v81, v83
v_sub_f32 v78, v78, v85
Maaa 108,12,156
v_sub_f32 v79, v79, v85
K 80,78,79
Maaa 76,12,152
Ta 16,14,40960
Ta 18,14,41984
Ta 20,15,40960
Ta 22,15,41984
Ta 24,24,40960
Ta 26,24,41984
Ta 28,25,40960
Ta 30,25,41984
v_lshlrev_b32 v78, 16, v80
v_and_b32 v79, 0xffff0000, v80
U 80,78,87
U 81,79,87
v_fma_f32 v82, -v80, v86, v78
v_fma_f32 v83, -v81, v86, v79
v_fmac_f32 v80, v82, v87
v_fmac_f32 v81, v83, v87
K 43,80,81
v_lshlrev_b32 v80, 16, v43
v_and_b32 v81, 0xffff0000, v43
v_lshlrev_b32 v82, 16, v55
v_and_b32 v83, 0xffff0000, v55
v_fmac_f32 v84, v80, v82
v_fmac_f32 v84, v81, v83
v_lshrrev_b32 v82, 9, v1
s_lshl_b32 s80, s14, 1
v_add_u32 v82, s80, v82
v_mul_u32_u24 v83, 1552, v82
v_add_u32 v83, v83, v88
v_add_u32 v83, 131072, v83
ds_write_b128 v83, v[32:35] offset:0
ds_write_b128 v83, v[36:39] offset:512
ds_write_b128 v83, v[40:43] offset:1024
v_add_f32_dpp v84, v84, v84 quad_perm:[1,0,3,2] row_mask:0xf bank_mask:0xf
s_nop 1
v_add_f32_dpp v84, v84, v84 quad_perm:[2,3,0,1] row_mask:0xf bank_mask:0xf
s_nop 1
v_add_f32_dpp v84, v84, v84 row_half_mirror row_mask:0xf bank_mask:0xf
s_nop 1
v_add_f32_dpp v84, v84, v84 row_mirror row_mask:0xf bank_mask:0xf
v_mov_b32 v81, v84
s_nop 1
v_permlane16_swap_b32 v84, v81
A 84,84,81
K 80,84,84
v_lshlrev_b32 v80, 16, v80
s_lshl_b32 s80, s15, 3
v_add_u32 v82, s80, v82
v_sub_u32 v83, 2047, v82
v_ashrrev_i32 v81, 31, v80
v_or_b32 v81, 0x80000000, v81
v_xor_b32 v80, v80, v81
v_and_b32 v80, 0xffff0000, v80
v_or_b32 v80, v80, v83
v_and_b32 v82, 7, v82
v_lshlrev_b32 v82, 2, v82
v_add_u32 v82, 143488, v82
ds_write_b32 v82, v80
Maaa 236,12,148
s_waitcnt vmcnt(1) lgkmcnt(0)
s_barrier
s_add_u32 s64, s64, 128
Maaa 204,12,144
v_bfe_u32 v80, v1, 4, 3
Maaa 172,12,140
Ra 32,26,0
v_mul_u32_u24 v81, 1552, v80
Mvaa 236,12,136
s_and_b32 s64, s64, 1023
v_lshrrev_b32 v82, 7, v1
Mvaa 204,12,132
v_lshl_add_u32 v81, v82, 4, v81
Mvaa 172,12,128
s_lshl_b32 s80, s14, 7
Ra 36,26,2048
Mvaa 176,16,128
s_add_u32 m0, s61, 32768
s_add_u32 s80, s80, 131072
Mvaa 208,16,132
v_add_u32 v81, s80, v81
Mvaa 240,16,136
ds_read_b128 v[32:35], v81
Ra 40,26,4096
Maaa 176,16,140
D 2,40,64,0
Rv 36,81,512
Maaa 208,16,144
s_and_b32 s80, s14, 2
Maaa 240,16,148
s_lshl_b32 s80, s80, 7
Ra 44,26,6144
Maaa 80,16,152
v_subrev_u32 v81, s80, v81
Maaa 112,16,156
D 3,40,64,1024
Rv 40,81,1024
Maaa 116,20,156
Ta 0,10,0
v_lshlrev_b32 v80, 2, v80
Maaa 84,20,152
Ta 2,10,1024
Ra 48,26,8192
Maaa 244,20,148
v_add_u32 v80, 143488, v80
Maaa 212,20,144
D 4,40,64,2048
ds_read_b32 v84, v80
Maaa 180,20,140
Ra 52,26,10240
v_mov_b32 v85, s13
Mvaa 244,20,136
v_lshrrev_b32 v83, 1, v1
Mvaa 212,20,132
s_mul_i32 s82, s15, 10240
s_add_u32 s82, s82, 131072
Ra 56,26,12288
Mvaa 180,20,128
s_lshl_b32 s80, s14, 10
D 5,40,64,3072
Mvaa 184,24,128
s_add_u32 m0, s61, 36864
s_add_u32 s83, s82, s80
Ta 4,11,0
Mvaa 216,24,132
Ta 6,11,1024
Ra 60,26,14336
Mvaa 248,24,136
Maaa 184,24,140
D 6,40,64,0
Maaa 216,24,144
Maaa 248,24,148
Maaa 88,24,152
Maaa 120,24,156
W 5
buffer_store_dwordx4 v[32:35], v1, s[8:11], s83 offen sc1
s_add_u32 s83, s83, 4096
Maaa 124,28,156
Ta 8,12,0
Ta 10,12,1024
Maaa 92,28,152
Maaa 252,28,148
Maaa 220,28,144
D 7,40,64,1024
Maaa 188,28,140
Mvaa 252,28,136
Mvaa 220,28,132
Mvaa 188,28,128
)"
R"(buffer_store_dwordx4 v[36:39], v1, s[8:11], s83 offen sc1
Mvaa 160,0,32
s_and_b32 s80, s80, 1024
s_add_u32 s83, s82, s80
s_add_u32 s83, s83, 8192
Mvaa 192,0,36
Ta 12,13,0
Ta 14,13,1024
Mvaa 224,0,40
Maaa 160,0,44
W 4
D 8,40,64,2048
Maaa 192,0,48
Maaa 224,0,52
Maaa 64,0,56
Maaa 96,0,60
buffer_store_dwordx4 v[40:43], v1, s[8:11], s83 offen sc1
Maaa 100,4,60
Ta 16,14,0
Ta 18,14,1024
Maaa 68,4,56
Maaa 228,4,52
Maaa 196,4,48
D 9,40,64,3072
Maaa 164,4,44
s_lshl_b32 s65, s64, 12
s_add_u32 m0, s62, 32768
Mvaa 228,4,40
Mvaa 196,4,36
Mvaa 164,4,32
W 4
D 16,44,65,0
Mvaa 168,8,32
Ta 20,15,0
Ta 22,15,1024
Mvaa 200,8,36
Mvaa 232,8,40
Maaa 168,8,44
D 17,44,65,1024
Maaa 200,8,48
Maaa 232,8,52
Maaa 72,8,56
Maaa 104,8,60
W 4
D 18,44,65,2048
Maaa 108,12,60
Ta 24,24,0
Ta 26,24,1024
Maaa 76,12,56
Maaa 236,12,52
Maaa 204,12,48
D 19,44,65,3072
Maaa 172,12,44
s_add_u32 m0, s62, 36864
Ra 128,27,0
Mvaa 236,12,40
Mvaa 204,12,36
Mvaa 172,12,32
W 5
D 20,44,65,0
Ra 132,27,2048
Mvaa 176,16,32
Ta 28,25,0
Ta 30,25,1024
Mvaa 208,16,36
Mvaa 240,16,40
Ra 136,27,4096
Maaa 176,16,44
D 21,44,65,1024
Maaa 208,16,48
Maaa 240,16,52
Ra 140,27,6144
Maaa 80,16,56
Maaa 112,16,60
W 8
D 22,44,65,2048
Maaa 116,20,60
Ta 0,10,8192
Ta 2,10,9216
Maaa 84,20,56
Ra 144,27,8192
Maaa 244,20,52
Maaa 212,20,48
D 23,44,65,3072
Ra 148,27,10240
Maaa 180,20,44
Mvaa 244,20,40
Mvaa 212,20,36
Ra 152,27,12288
Mvaa 180,20,32
W 11
Mvaa 184,24,32
Ta 4,11,8192
Ta 6,11,9216
Mvaa 216,24,36
Ra 156,27,14336
Mvaa 248,24,40
Maaa 184,24,44
Maaa 216,24,48
Maaa 248,24,52
Maaa 88,24,56
Maaa 120,24,60
W 10
Maaa 124,28,60
Ta 8,12,8192
Ta 10,12,9216
Maaa 92,28,56
Maaa 252,28,52
Maaa 220,28,48
Maaa 188,28,44
Mvaa 252,28,40
Mvaa 220,28,36
Mvaa 188,28,32
W 8
Mvaa 160,0,128
Ta 12,13,8192
Ta 14,13,9216
Mvaa 192,0,132
Mvaa 224,0,136
Maaa 160,0,140
W 4
Maaa 192,0,144
Maaa 224,0,148
Maaa 64,0,152
Maaa 96,0,156
Maaa 100,4,156
Ta 16,14,8192
Ta 18,14,9216
Maaa 68,4,152
Maaa 228,4,148
Maaa 196,4,144
Maaa 164,4,140
Ta 20,15,8192
Ta 22,15,9216
Mvaa 228,4,136
Mvaa 196,4,132
Ta 24,24,8192
Ta 26,24,9216
Mvaa 164,4,128
W 8
Mvaa 168,8,128
Ta 28,25,8192
Ta 30,25,9216
Mvaa 200,8,132
Mvaa 232,8,136
Maaa 168,8,140
Maaa 200,8,144
Maaa 232,8,148
Maaa 72,8,152
Maaa 104,8,156
W 8
Maaa 108,12,156
Maaa 76,12,152
Maaa 236,12,148
s_waitcnt vmcnt(0) lgkmcnt(0)
s_barrier
s_cmp_eq_u32 s14, 0
s_cbranch_scc0 .Lpubr_13%=
s_lshl_b32 s80, s15, 6
s_add_u32 s80, s80, 40960
s_mov_b64 exec, 0xff
buffer_store_dwordx2 v[84:85], v83, s[8:11], s80 offen sc1
s_mov_b64 exec, -1
.Lpubr_13%=:
Maaa 204,12,144
s_add_u32 s64, s64, 128
Maaa 172,12,140
Ra 32,26,32768
Mvaa 236,12,136
s_and_b32 s64, s64, 1023
Mvaa 204,12,132
Mvaa 172,12,128
s_add_u32 m0, s61, 0
Ra 36,26,34816
Mvaa 176,16,128
Mvaa 208,16,132
D 2,40,64,0
Mvaa 240,16,136
Ra 40,26,36864
Maaa 176,16,140
Maaa 208,16,144
Maaa 240,16,148
D 3,40,64,1024
Ra 44,26,38912
Maaa 80,16,152
Maaa 112,16,156
Maaa 116,20,156
Ta 0,10,32768
Ta 2,10,33792
Maaa 84,20,152
D 4,40,64,2048
Ra 48,26,40960
Maaa 244,20,148
Maaa 212,20,144
Ra 52,26,43008
Maaa 180,20,140
Mvaa 244,20,136
D 5,40,64,3072
Mvaa 212,20,132
s_add_u32 m0, s61, 4096
Ra 56,26,45056
Mvaa 180,20,128
Mvaa 184,24,128
Ta 4,11,32768
Ta 6,11,33792
Mvaa 216,24,132
D 6,40,64,0
Ra 60,26,47104
Mvaa 248,24,136
Maaa 184,24,140
Maaa 216,24,144
Maaa 248,24,148
D 7,40,64,1024
Maaa 88,24,152
Maaa 120,24,156
Maaa 124,28,156
Ta 8,12,32768
Ta 10,12,33792
Maaa 92,28,152
D 8,40,64,2048
Maaa 252,28,148
Maaa 220,28,144
Maaa 188,28,140
Mvaa 252,28,136
D 9,40,64,3072
Mvaa 220,28,132
s_lshl_b32 s65, s64, 12
s_add_u32 m0, s62, 0
Mvaa 188,28,128
W 8
Mvaa 160,0,32
Ta 12,13,32768
Ta 14,13,33792
Mvaa 192,0,36
D 16,44,65,0
Mvaa 224,0,40
Maaa 160,0,44
W 4
Maaa 192,0,48
Maaa 224,0,52
D 17,44,65,1024
Maaa 64,0,56
Maaa 96,0,60
Maaa 100,4,60
Ta 16,14,32768
Ta 18,14,33792
Maaa 68,4,56
D 18,44,65,2048
Maaa 228,4,52
Maaa 196,4,48
Maaa 164,4,44
Mvaa 228,4,40
D 19,44,65,3072
Mvaa 196,4,36
s_add_u32 m0, s62, 4096
Mvaa 164,4,32
W 4
Mvaa 168,8,32
Ta 20,15,32768
Ta 22,15,33792
Mvaa 200,8,36
D 20,44,65,0
Mvaa 232,8,40
Maaa 168,8,44
Maaa 200,8,48
Maaa 232,8,52
D 21,44,65,1024
Maaa 72,8,56
Maaa 104,8,60
W 4
Maaa 108,12,60
Ta 24,24,32768
Ta 26,24,33792
Maaa 76,12,56
D 22,44,65,2048
Maaa 236,12,52
Maaa 204,12,48
Maaa 172,12,44
Ra 128,27,32768
Mvaa 236,12,40
D 23,44,65,3072
Mvaa 204,12,36
s_add_u32 s65, s5, 0
s_cmp_ge_u32 s65, 20
s_cselect_b32 s68, 20, 0
s_sub_u32 s65, s65, s68
Mvaa 172,12,32
W 5
s_lshl_b32 s65, s65, 18
s_add_u32 m0, s63, 131072
Mvaa 176,16,32
Ra 132,27,34816
Ta 28,25,32768
Mvaa 208,16,36
D 16,48,65,0
Ta 30,25,33792
Mvaa 240,16,40
Ra 136,27,36864
Maaa 176,16,44
Maaa 208,16,48
Maaa 240,16,52
D 17,48,65,1024
Ra 140,27,38912
Maaa 80,16,56
Maaa 112,16,60
W 8
Maaa 116,20,60
Ta 0,10,40960
Ta 2,10,41984
Maaa 84,20,56
D 18,48,65,2048
Ra 144,27,40960
Maaa 244,20,52
Maaa 212,20,48
Ra 148,27,43008
Maaa 180,20,44
Mvaa 244,20,40
D 19,48,65,3072
Mvaa 212,20,36
s_add_u32 m0, s63, 135168
Ra 152,27,45056
Mvaa 180,20,32
W 11
Mvaa 184,24,32
Ta 4,11,40960
Ta 6,11,41984
Mvaa 216,24,36
D 20,48,65,0
Ra 156,27,47104
Mvaa 248,24,40
Maaa 184,24,44
Maaa 216,24,48
Maaa 248,24,52
D 21,48,65,1024
Maaa 88,24,56
Maaa 120,24,60
W 10
Maaa 124,28,60
Ta 8,12,40960
Ta 10,12,41984
Maaa 92,28,56
D 22,48,65,2048
Maaa 252,28,52
Maaa 220,28,48
Maaa 188,28,44
Mvaa 252,28,40
D 23,48,65,3072
Mvaa 220,28,36
Mvaa 188,28,32
W 8
Mvaa 160,0,128
Ta 12,13,40960
Ta 14,13,41984
Mvaa 192,0,132
global_load_dword v142, v[140:141], off
v_lshl_add_u64 v[140:141], v[140:141], 0, s[68:69]
Mvaa 224,0,136
Maaa 160,0,140
W 4
Maaa 192,0,144
Maaa 224,0,148
Maaa 64,0,152
Maaa 96,0,156
Maaa 100,4,156
Ta 16,14,40960
Ta 18,14,41984
Maaa 68,4,152
Maaa 228,4,148
Maaa 196,4,144
Maaa 164,4,140
Ta 20,15,40960
Ta 22,15,41984
Mvaa 228,4,136
Mvaa 196,4,132
Ta 24,24,40960
Ta 26,24,41984
Mvaa 164,4,128
W 8
Mvaa 168,8,128
Ta 28,25,40960
Ta 30,25,41984
Mvaa 200,8,132
Mvaa 232,8,136
Maaa 168,8,140
Maaa 200,8,144
Maaa 232,8,148
Maaa 72,8,152
Maaa 104,8,156
W 8
Maaa 108,12,156
Maaa 76,12,152
Maaa 236,12,148
s_waitcnt vmcnt(1) lgkmcnt(0)
s_barrier
s_add_u32 s64, s64, 128
Maaa 204,12,144
Maaa 172,12,140
Ra 32,26,0
Mvaa 236,12,136
s_and_b32 s64, s64, 1023
)"
R"(Mvaa 204,12,132
Mvaa 172,12,128
Ra 36,26,2048
Mvaa 176,16,128
s_add_u32 m0, s61, 32768
Mvaa 208,16,132
Mvaa 240,16,136
Ra 40,26,4096
Maaa 176,16,140
D 2,40,64,0
Maaa 208,16,144
Maaa 240,16,148
Ra 44,26,6144
Maaa 80,16,152
Maaa 112,16,156
D 3,40,64,1024
Maaa 116,20,156
Ta 0,10,0
Ta 2,10,1024
Maaa 84,20,152
Ra 48,26,8192
Maaa 244,20,148
Maaa 212,20,144
D 4,40,64,2048
Ra 52,26,10240
Maaa 180,20,140
Mvaa 244,20,136
Mvaa 212,20,132
Ra 56,26,12288
Mvaa 180,20,128
D 5,40,64,3072
Mvaa 184,24,128
s_add_u32 m0, s61, 36864
Ta 4,11,0
Ta 6,11,1024
Mvaa 216,24,132
Ra 60,26,14336
Mvaa 248,24,136
v_lshrrev_b32 v28, 1, v1
Maaa 184,24,140
s_lshl_b32 s80, s60, 9
D 6,40,64,0
Maaa 216,24,144
s_add_u32 s80, s80, 40960
Maaa 248,24,148
Maaa 88,24,152
Maaa 120,24,156
D 7,40,64,1024
Maaa 124,28,156
Ta 8,12,0
Ta 10,12,1024
Maaa 92,28,152
Maaa 252,28,148
Maaa 220,28,144
buffer_load_dwordx2 v[28:29], v28, s[8:11], s80 offen sc1
Maaa 188,28,140
Mvaa 252,28,136
Mvaa 220,28,132
Mvaa 188,28,128
W 8
D 8,40,64,2048
Mvaa 160,0,32
Ta 12,13,0
Ta 14,13,1024
Mvaa 192,0,36
Mvaa 224,0,40
Maaa 160,0,44
W 4
D 9,40,64,3072
Maaa 192,0,48
s_lshl_b32 s65, s64, 12
s_add_u32 m0, s62, 32768
Maaa 224,0,52
Maaa 64,0,56
Maaa 96,0,60
D 16,44,65,0
Maaa 100,4,60
Ta 16,14,0
Ta 18,14,1024
Maaa 68,4,56
Maaa 228,4,52
Maaa 196,4,48
D 17,44,65,1024
Maaa 164,4,44
Mvaa 228,4,40
Mvaa 196,4,36
Mvaa 164,4,32
W 4
D 18,44,65,2048
Mvaa 168,8,32
Ta 20,15,0
Ta 22,15,1024
Mvaa 200,8,36
Mvaa 232,8,40
Maaa 168,8,44
D 19,44,65,3072
Maaa 200,8,48
s_add_u32 m0, s62, 36864
Maaa 232,8,52
Maaa 72,8,56
Maaa 104,8,60
W 4
D 20,44,65,0
Maaa 108,12,60
Ta 24,24,0
Ta 26,24,1024
Maaa 76,12,56
Maaa 236,12,52
Maaa 204,12,48
D 21,44,65,1024
Maaa 172,12,44
Ra 128,27,0
Mvaa 236,12,40
Mvaa 204,12,36
Mvaa 172,12,32
W 5
D 22,44,65,2048
Ra 132,27,2048
Mvaa 176,16,32
Ta 28,25,0
Ta 30,25,1024
Mvaa 208,16,36
Mvaa 240,16,40
Ra 136,27,4096
Maaa 176,16,44
D 23,44,65,3072
v_lshrrev_b32 v138, 4, v1
Maaa 208,16,48
s_sub_u32 s81, s73, 1
v_min_u32 v138, s81, v138
v_lshrrev_b32 v139, 2, v138
Maaa 240,16,52
Ra 140,27,6144
v_mul_u32_u24 v139, s72, v139
Maaa 80,16,56
v_and_b32 v138, 3, v138
Maaa 112,16,60
W 8
v_lshl_add_u32 v138, v138, 7, v139
Maaa 116,20,60
s_waitcnt vmcnt(10)
v_cmp_eq_u32 vcc, s13, v29
s_cmp_eq_u64 vcc, -1
s_cbranch_scc0 .Lspin_14%=
.Lok_15%=:
v_add_u32 v138, s74, v138
Maaa 84,20,56
s_lshr_b32 s73, s73, 2
Ta 0,10,8192
Ta 2,10,9216
Maaa 244,20,52
s_mul_i32 s73, s73, s72
Ra 144,27,8192
v_bfe_u32 v144, v1, 8, 1
Maaa 212,20,48
v_min_u32 v139, s75, v138
v_mul_u32_u24 v144, 10240, v144
Maaa 180,20,44
Ra 148,27,10240
v_and_b32 v145, 0xf0, v1
Mvaa 244,20,40
global_load_dword v142, v139, s[76:77]
v_lshl_add_u32 v144, v145, 3, v144
Mvaa 212,20,36
s_mul_i32 s69, s58, 20480
Ra 152,27,12288
v_add_u32 v138, s73, v138
Mvaa 180,20,32
W 11
s_add_u32 s69, s69, s28
Mvaa 184,24,32
Ta 4,11,8192
Ta 6,11,9216
Mvaa 216,24,36
buffer_load_dword v142, v144, s[8:11], s69 offen offset:0 sc1
Ra 156,27,14336
Mvaa 248,24,40
Maaa 184,24,44
Maaa 216,24,48
Maaa 248,24,52
Maaa 88,24,56
Maaa 120,24,60
W 10
Maaa 124,28,60
Ta 8,12,8192
Ta 10,12,9216
Maaa 92,28,56
Maaa 252,28,52
Maaa 220,28,48
Maaa 188,28,44
Mvaa 252,28,40
Mvaa 220,28,36
Mvaa 188,28,32
W 8
Mvaa 160,0,128
Ta 12,13,8192
Ta 14,13,9216
Mvaa 192,0,132
Mvaa 224,0,136
Maaa 160,0,140
W 4
Maaa 192,0,144
Maaa 224,0,148
Maaa 64,0,152
Maaa 96,0,156
Maaa 100,4,156
Ta 16,14,8192
Ta 18,14,9216
Maaa 68,4,152
Maaa 228,4,148
Maaa 196,4,144
Maaa 164,4,140
Ta 20,15,8192
Ta 22,15,9216
Mvaa 228,4,136
Mvaa 196,4,132
Ta 24,24,8192
Ta 26,24,9216
Mvaa 164,4,128
W 8
Mvaa 168,8,128
Ta 28,25,8192
Ta 30,25,9216
Mvaa 200,8,132
Mvaa 232,8,136
Maaa 168,8,140
Maaa 200,8,144
Maaa 232,8,148
Maaa 72,8,152
Maaa 104,8,156
W 8
Maaa 108,12,156
Maaa 76,12,152
Maaa 236,12,148
s_waitcnt vmcnt(2) lgkmcnt(0)
s_barrier
s_add_u32 s64, s64, 128
Maaa 204,12,144
Maaa 172,12,140
Ra 32,26,32768
Mvaa 236,12,136
s_and_b32 s64, s64, 1023
Mvaa 204,12,132
Mvaa 172,12,128
Ra 36,26,34816
Mvaa 176,16,128
s_add_u32 m0, s61, 0
Mvaa 208,16,132
Mvaa 240,16,136
Ra 40,26,36864
Maaa 176,16,140
D 2,40,64,0
Maaa 208,16,144
Maaa 240,16,148
Ra 44,26,38912
Maaa 80,16,152
Maaa 112,16,156
D 3,40,64,1024
Maaa 116,20,156
Ta 0,10,32768
Ta 2,10,33792
Maaa 84,20,152
Ra 48,26,40960
Maaa 244,20,148
Maaa 212,20,144
D 4,40,64,2048
Ra 52,26,43008
Maaa 180,20,140
Mvaa 244,20,136
Mvaa 212,20,132
Ra 56,26,45056
Mvaa 180,20,128
D 5,40,64,3072
Mvaa 184,24,128
s_add_u32 m0, s61, 4096
Ta 4,11,32768
Ta 6,11,33792
Mvaa 216,24,132
Ra 60,26,47104
Mvaa 248,24,136
Maaa 184,24,140
D 6,40,64,0
Maaa 216,24,144
Maaa 248,24,148
Maaa 88,24,152
Maaa 120,24,156
D 7,40,64,1024
Maaa 124,28,156
Ta 8,12,32768
Ta 10,12,33792
Maaa 92,28,152
Maaa 252,28,148
Maaa 220,28,144
D 8,40,64,2048
Maaa 188,28,140
Mvaa 252,28,136
Mvaa 220,28,132
Mvaa 188,28,128
W 8
D 9,40,64,3072
Mvaa 160,0,32
s_lshl_b32 s65, s64, 12
s_add_u32 m0, s62, 0
Ta 12,13,32768
Mvaa 192,0,36
Ta 14,13,33792
Mvaa 224,0,40
Maaa 160,0,44
W 4
D 16,44,65,0
Maaa 192,0,48
Maaa 224,0,52
Maaa 64,0,56
Maaa 96,0,60
D 17,44,65,1024
Maaa 100,4,60
Ta 16,14,32768
Ta 18,14,33792
Maaa 68,4,56
Maaa 228,4,52
Maaa 196,4,48
D 18,44,65,2048
Maaa 164,4,44
Mvaa 228,4,40
Mvaa 196,4,36
Mvaa 164,4,32
W 4
D 19,44,65,3072
Mvaa 168,8,32
s_add_u32 m0, s62, 4096
Ta 20,15,32768
Ta 22,15,33792
Mvaa 200,8,36
Mvaa 232,8,40
Maaa 168,8,44
D 20,44,65,0
Maaa 200,8,48
Maaa 232,8,52
Maaa 72,8,56
Maaa 104,8,60
W 4
D 21,44,65,1024
Maaa 108,12,60
Ta 24,24,32768
Ta 26,24,33792
Maaa 76,12,56
Maaa 236,12,52
Maaa 204,12,48
D 22,44,65,2048
Maaa 172,12,44
Ra 128,27,32768
Mvaa 236,12,40
Mvaa 204,12,36
Mvaa 172,12,32
W 5
D 23,44,65,3072
Ra 132,27,34816
Mvaa 176,16,32
Ta 28,25,32768
Ta 30,25,33792
Mvaa 208,16,36
v_min_u32 v139, s75, v138
Mvaa 240,16,40
Ra 136,27,36864
Maaa 176,16,44
buffer_load_dword v142, v144, s[8:11], s69 offen offset:2048 sc1
Maaa 208,16,48
Maaa 240,16,52
s_add_u32 s69, s69, 4096
Ra 140,27,38912
Maaa 80,16,56
Maaa 112,16,60
W 8
global_load_dword v142, v139, s[76:77]
Maaa 116,20,60
Ta 0,10,40960
Ta 2,10,41984
Maaa 84,20,56
Ra 144,27,40960
Maaa 244,20,52
v_add_u32 v138, s73, v138
Maaa 212,20,48
Ra 148,27,43008
Maaa 180,20,44
Mvaa 244,20,40
Mvaa 212,20,36
Ra 152,27,45056
Mvaa 180,20,32
W 11
Mvaa 184,24,32
Ta 4,11,40960
Ta 6,11,41984
Mvaa 216,24,36
Ra 156,27,47104
Mvaa 248,24,40
Maaa 184,24,44
Maaa 216,24,48
Maaa 248,24,52
Maaa 88,24,56
Maaa 120,24,60
)"
R"(W 10
Maaa 124,28,60
Ta 8,12,40960
Ta 10,12,41984
Maaa 92,28,56
Maaa 252,28,52
Maaa 220,28,48
Maaa 188,28,44
Mvaa 252,28,40
Mvaa 220,28,36
Mvaa 188,28,32
W 8
Mvaa 160,0,128
Ta 12,13,40960
Ta 14,13,41984
Mvaa 192,0,132
Mvaa 224,0,136
Maaa 160,0,140
W 4
Maaa 192,0,144
Maaa 224,0,148
Maaa 64,0,152
Maaa 96,0,156
Maaa 100,4,156
Ta 16,14,40960
Ta 18,14,41984
Maaa 68,4,152
Maaa 228,4,148
Maaa 196,4,144
Maaa 164,4,140
Ta 20,15,40960
Ta 22,15,41984
Mvaa 228,4,136
Mvaa 196,4,132
Ta 24,24,40960
Ta 26,24,41984
Mvaa 164,4,128
W 8
Mvaa 168,8,128
Ta 28,25,40960
Ta 30,25,41984
Mvaa 200,8,132
Mvaa 232,8,136
Maaa 168,8,140
Maaa 200,8,144
Maaa 232,8,148
Maaa 72,8,152
Maaa 104,8,156
W 8
Maaa 108,12,156
Maaa 76,12,152
Maaa 236,12,148
s_waitcnt vmcnt(2) lgkmcnt(0)
s_barrier
s_add_u32 s64, s64, 128
Maaa 204,12,144
Maaa 172,12,140
Ra 32,26,0
Mvaa 236,12,136
s_and_b32 s64, s64, 1023
Mvaa 204,12,132
Mvaa 172,12,128
Ra 36,26,2048
Mvaa 176,16,128
s_add_u32 m0, s61, 32768
Mvaa 208,16,132
Mvaa 240,16,136
Ra 40,26,4096
Maaa 176,16,140
D 2,40,64,0
Maaa 208,16,144
Maaa 240,16,148
Ra 44,26,6144
Maaa 80,16,152
Maaa 112,16,156
D 3,40,64,1024
Maaa 116,20,156
Ta 0,10,0
Ta 2,10,1024
Maaa 84,20,152
Ra 48,26,8192
Maaa 244,20,148
Maaa 212,20,144
D 4,40,64,2048
Ra 52,26,10240
Maaa 180,20,140
Mvaa 244,20,136
Mvaa 212,20,132
Ra 56,26,12288
Mvaa 180,20,128
D 5,40,64,3072
Mvaa 184,24,128
s_add_u32 m0, s61, 36864
Ta 4,11,0
Ta 6,11,1024
Mvaa 216,24,132
Ra 60,26,14336
Mvaa 248,24,136
Maaa 184,24,140
D 6,40,64,0
Maaa 216,24,144
Maaa 248,24,148
Maaa 88,24,152
Maaa 120,24,156
D 7,40,64,1024
Maaa 124,28,156
Ta 8,12,0
Ta 10,12,1024
Maaa 92,28,152
Maaa 252,28,148
Maaa 220,28,144
D 8,40,64,2048
Maaa 188,28,140
Mvaa 252,28,136
Mvaa 220,28,132
Mvaa 188,28,128
W 8
D 9,40,64,3072
Mvaa 160,0,32
s_lshl_b32 s65, s64, 12
s_add_u32 m0, s62, 32768
Ta 12,13,0
Mvaa 192,0,36
Ta 14,13,1024
Mvaa 224,0,40
Maaa 160,0,44
W 4
D 16,44,65,0
Maaa 192,0,48
Maaa 224,0,52
Maaa 64,0,56
Maaa 96,0,60
D 17,44,65,1024
Maaa 100,4,60
Ta 16,14,0
Ta 18,14,1024
Maaa 68,4,56
Maaa 228,4,52
Maaa 196,4,48
D 18,44,65,2048
Maaa 164,4,44
Mvaa 228,4,40
Mvaa 196,4,36
Mvaa 164,4,32
W 4
D 19,44,65,3072
Mvaa 168,8,32
s_add_u32 m0, s62, 36864
Ta 20,15,0
Ta 22,15,1024
Mvaa 200,8,36
Mvaa 232,8,40
Maaa 168,8,44
D 20,44,65,0
Maaa 200,8,48
Maaa 232,8,52
Maaa 72,8,56
Maaa 104,8,60
W 4
D 21,44,65,1024
Maaa 108,12,60
Ta 24,24,0
Ta 26,24,1024
Maaa 76,12,56
Maaa 236,12,52
Maaa 204,12,48
D 22,44,65,2048
Maaa 172,12,44
Ra 128,27,0
Mvaa 236,12,40
Mvaa 204,12,36
Mvaa 172,12,32
W 5
D 23,44,65,3072
v_add_u32 v2, 131072, v10
Mvaa 176,16,32
Ra 132,27,2048
Ta 28,25,0
Mvaa 208,16,36
Ta 30,25,1024
v_add_u32 v3, 131072, v11
Mvaa 240,16,40
Ra 136,27,4096
v_add_u32 v4, 131072, v12
Maaa 176,16,44
buffer_load_dword v142, v144, s[8:11], s69 offen offset:0 sc1
Maaa 208,16,48
v_add_u32 v5, 131072, v13
Maaa 240,16,52
Ra 140,27,6144
Maaa 80,16,56
v_add_u32 v6, 131072, v14
Maaa 112,16,60
W 8
Maaa 116,20,60
Ta 0,10,8192
Ta 2,10,9216
Maaa 84,20,56
Ra 144,27,8192
v_add_u32 v7, 131072, v15
Maaa 244,20,52
v_add_u32 v8, 131072, v24
Maaa 212,20,48
Ra 148,27,10240
Maaa 180,20,44
v_add_u32 v9, 131072, v25
Mvaa 244,20,40
Mvaa 212,20,36
Ra 152,27,12288
Mvaa 180,20,32
W 11
Mvaa 184,24,32
Ta 4,11,8192
Ta 6,11,9216
Mvaa 216,24,36
Ra 156,27,14336
Mvaa 248,24,40
Maaa 184,24,44
Maaa 216,24,48
Maaa 248,24,52
Maaa 88,24,56
Maaa 120,24,60
W 10
Maaa 124,28,60
Ta 8,12,8192
Ta 10,12,9216
Maaa 92,28,56
Maaa 252,28,52
Maaa 220,28,48
Maaa 188,28,44
Mvaa 252,28,40
Mvaa 220,28,36
Mvaa 188,28,32
W 8
Mvaa 160,0,128
Ta 12,13,8192
Ta 14,13,9216
Mvaa 192,0,132
Mvaa 224,0,136
Maaa 160,0,140
W 4
Maaa 192,0,144
Maaa 224,0,148
Maaa 64,0,152
Maaa 96,0,156
Maaa 100,4,156
Ta 16,14,8192
Ta 18,14,9216
Maaa 68,4,152
Maaa 228,4,148
Maaa 196,4,144
Maaa 164,4,140
Ta 20,15,8192
Ta 22,15,9216
Mvaa 228,4,136
Mvaa 196,4,132
Ta 24,24,8192
Ta 26,24,9216
Mvaa 164,4,128
W 8
Mvaa 168,8,128
Ta 28,25,8192
Ta 30,25,9216
Mvaa 200,8,132
Mvaa 232,8,136
Maaa 168,8,140
Maaa 200,8,144
Maaa 232,8,148
Maaa 72,8,152
Maaa 104,8,156
W 8
Maaa 108,12,156
Maaa 76,12,152
Maaa 236,12,148
s_waitcnt vmcnt(1) lgkmcnt(0)
s_barrier
s_add_u32 s65, s5, 4
Maaa 204,12,144
Maaa 172,12,140
Ra 32,26,32768
Mvaa 236,12,136
s_cmp_ge_u32 s65, 20
s_cselect_b32 s68, 20, 0
Mvaa 204,12,132
Mvaa 172,12,128
s_sub_u32 s65, s65, s68
Ra 36,26,34816
Mvaa 176,16,128
Mvaa 208,16,132
s_lshl_b32 s65, s65, 18
Mvaa 240,16,136
Ra 40,26,36864
Maaa 176,16,140
Maaa 208,16,144
s_add_u32 m0, s63, 0
Maaa 240,16,148
Ra 44,26,38912
Maaa 80,16,152
D 16,48,65,0
Maaa 112,16,156
Maaa 116,20,156
Ta 0,10,32768
Ta 2,10,33792
Maaa 84,20,152
Ra 48,26,40960
Maaa 244,20,148
D 17,48,65,1024
Maaa 212,20,144
Ra 52,26,43008
Maaa 180,20,140
Mvaa 244,20,136
Mvaa 212,20,132
D 18,48,65,2048
Ra 56,26,45056
Mvaa 180,20,128
Mvaa 184,24,128
Ta 4,11,32768
Ta 6,11,33792
Mvaa 216,24,132
Ra 60,26,47104
Mvaa 248,24,136
D 19,48,65,3072
Maaa 184,24,140
s_add_u32 m0, s63, 4096
Maaa 216,24,144
Maaa 248,24,148
Maaa 88,24,152
D 20,48,65,0
Maaa 120,24,156
Maaa 124,28,156
Ta 8,12,32768
Ta 10,12,33792
Maaa 92,28,152
Maaa 252,28,148
D 21,48,65,1024
Maaa 220,28,144
Maaa 188,28,140
Mvaa 252,28,136
Mvaa 220,28,132
D 22,48,65,2048
Mvaa 188,28,128
W 8
Mvaa 160,0,32
Ta 12,13,32768
Ta 14,13,33792
Mvaa 192,0,36
Mvaa 224,0,40
D 23,48,65,3072
Maaa 160,0,44
W 4
s_add_u32 s65, s5, 8
s_cmp_ge_u32 s65, 20
s_cselect_b32 s68, 20, 0
Maaa 192,0,48
s_sub_u32 s65, s65, s68
s_lshl_b32 s65, s65, 18
s_add_u32 m0, s63, 65536
Maaa 224,0,52
Maaa 64,0,56
D 16,48,65,0
Maaa 96,0,60
Maaa 100,4,60
Ta 16,14,32768
Ta 18,14,33792
Maaa 68,4,56
Maaa 228,4,52
D 17,48,65,1024
Maaa 196,4,48
Maaa 164,4,44
Mvaa 228,4,40
Mvaa 196,4,36
D 18,48,65,2048
Mvaa 164,4,32
W 4
Mvaa 168,8,32
Ta 20,15,32768
Ta 22,15,33792
Mvaa 200,8,36
Mvaa 232,8,40
D 19,48,65,3072
Maaa 168,8,44
s_add_u32 m0, s63, 69632
Maaa 200,8,48
Maaa 232,8,52
Maaa 72,8,56
D 20,48,65,0
Maaa 104,8,60
W 4
Maaa 108,12,60
Ta 24,24,32768
Ta 26,24,33792
Maaa 76,12,56
Maaa 236,12,52
D 21,48,65,1024
Maaa 204,12,48
Maaa 172,12,44
Ra 128,27,32768
Mvaa 236,12,40
Mvaa 204,12,36
D 22,48,65,2048
)"
R"(Mvaa 172,12,32
W 5
Ra 132,27,34816
Mvaa 176,16,32
Ta 28,25,32768
Ta 30,25,33792
Mvaa 208,16,36
Mvaa 240,16,40
D 23,48,65,3072
v_min_u32 v139, s75, v138
Maaa 176,16,44
Ra 136,27,36864
Maaa 208,16,48
Maaa 240,16,52
Ra 140,27,38912
Maaa 80,16,56
buffer_load_dword v142, v144, s[8:11], s69 offen offset:2048 sc1
Maaa 112,16,60
W 8
s_add_u32 s69, s69, 4096
Maaa 116,20,60
Ta 0,10,40960
Ta 2,10,41984
Maaa 84,20,56
Ra 144,27,40960
Maaa 244,20,52
global_load_dword v142, v139, s[76:77]
v_add_u32 v138, s73, v138
Maaa 212,20,48
Ra 148,27,43008
Maaa 180,20,44
Mvaa 244,20,40
v_bfe_u32 v29, v1, 7, 1
Mvaa 212,20,36
Ra 152,27,45056
Mvaa 180,20,32
W 11
v_mul_u32_u24 v28, 10240, v29
Mvaa 184,24,32
Ta 4,11,40960
Ta 6,11,41984
Mvaa 216,24,36
Ra 156,27,47104
v_lshrrev_b32 v29, 8, v1
Mvaa 248,24,40
Maaa 184,24,44
v_lshl_add_u32 v28, v29, 7, v28
Maaa 216,24,48
v_and_b32 v29, 0x70, v1
Maaa 248,24,52
Maaa 88,24,56
v_add_u32 v28, v28, v29
Maaa 120,24,60
W 10
Maaa 124,28,60
s_and_b32 s80, s14, 2
Ta 8,12,40960
Ta 10,12,41984
Maaa 92,28,56
s_mul_i32 s80, s80, 20480
Maaa 252,28,52
Maaa 220,28,48
s_sub_u32 s81, 40960, s80
Maaa 188,28,44
Mvaa 252,28,40
s_add_u32 s93, s28, 20480
Mvaa 220,28,36
Mvaa 188,28,32
W 8
v_add_u32 v26, s80, v28
Mvaa 160,0,128
Ta 12,13,40960
Ta 14,13,41984
Mvaa 192,0,132
Mvaa 224,0,136
v_add_u32 v27, s81, v28
Maaa 160,0,140
W 4
Maaa 192,0,144
buffer_load_dwordx4 a[32:35], v26, s[8:11], s28 offen offset:0 sc1
Maaa 224,0,148
Maaa 64,0,152
Maaa 96,0,156
Maaa 100,4,156
buffer_load_dwordx4 a[36:39], v26, s[8:11], s93 offen offset:0 sc1
Ta 16,14,40960
Maaa 68,4,152
Ta 18,14,41984
Maaa 228,4,148
Maaa 196,4,144
Maaa 164,4,140
buffer_load_dwordx4 a[40:43], v27, s[8:11], s28 offen offset:0 sc1
Ta 20,15,40960
Mvaa 228,4,136
Ta 22,15,41984
Mvaa 196,4,132
Ta 24,24,40960
Ta 26,24,41984
Mvaa 164,4,128
W 8
Mvaa 168,8,128
buffer_load_dwordx4 a[44:47], v27, s[8:11], s93 offen offset:0 sc1
Ta 28,25,40960
Mvaa 200,8,132
s_add_u32 s28, s28, 512
s_cmp_eq_u32 s28, s4
s_cselect_b32 s93, 10240, 0
Ta 30,25,41984
Mvaa 232,8,136
s_sub_u32 s28, s28, s93
Maaa 168,8,140
s_add_u32 s93, s28, 20480
Maaa 200,8,144
Maaa 232,8,148
Maaa 72,8,152
buffer_load_dwordx4 a[48:51], v26, s[8:11], s28 offen offset:0 sc1
Maaa 104,8,156
W 8
Maaa 108,12,156
Maaa 76,12,152
Maaa 236,12,148
s_waitcnt vmcnt(7) lgkmcnt(0)
s_barrier
buffer_load_dwordx4 a[52:55], v26, s[8:11], s93 offen offset:0 sc1
Maaa 204,12,144
Maaa 172,12,140
Mvaa 236,12,136
Mvaa 204,12,132
buffer_load_dwordx4 a[56:59], v27, s[8:11], s28 offen offset:0 sc1
Mvaa 172,12,128
Mvaa 176,16,128
Mvaa 208,16,132
Mvaa 240,16,136
s_add_u32 s65, s5, 12
buffer_load_dwordx4 a[60:63], v27, s[8:11], s93 offen offset:0 sc1
Maaa 176,16,140
Maaa 208,16,144
Maaa 240,16,148
Maaa 80,16,152
Maaa 112,16,156
Maaa 116,20,156
Maaa 84,20,152
Maaa 244,20,148
s_cmp_ge_u32 s65, 20
s_cselect_b32 s68, 20, 0
Ta 0,2,0
Ta 2,2,1024
Maaa 212,20,144
Maaa 180,20,140
Mvaa 244,20,136
Mvaa 212,20,132
Ta 4,3,0
Ta 6,3,1024
Mvaa 180,20,128
Mvaa 184,24,128
Mvaa 216,24,132
s_sub_u32 s65, s65, s68
Mvaa 248,24,136
Ta 8,4,0
Ta 10,4,1024
Maaa 184,24,140
Maaa 216,24,144
Maaa 248,24,148
Maaa 88,24,152
Ta 12,5,0
Ta 14,5,1024
Maaa 120,24,156
Maaa 124,28,156
s_lshl_b32 s65, s65, 18
Maaa 92,28,152
Maaa 252,28,148
Ta 16,6,0
Ta 18,6,1024
Maaa 220,28,144
Maaa 188,28,140
Mvaa 252,28,136
Mvaa 220,28,132
Mvaa 188,28,128
s_add_u32 m0, s63, 32768
s_branch .Lq_11%=
.Lv_10%=:
s_waitcnt vmcnt(24)
s_barrier
Ra 32,26,0
Ta 0,10,16384
Ta 2,10,17408
Ra 36,26,2048
Ra 40,26,4096
Ra 44,26,6144
Ra 48,26,8192
Ra 52,26,10240
Ra 56,26,12288
Ra 60,26,14336
Ta 4,11,16384
Ta 6,11,17408
W 6
Zvaa 160,32,0
Ta 8,12,16384
Ta 10,12,17408
Zvaa 164,36,0
Ta 12,13,16384
Ta 14,13,17408
Zvaa 224,40,0
s_waitcnt vmcnt(21)
Zvaa 228,44,0
W 6
v_lshlrev_b32 v56, 16, v32
Zaaa 192,48,0
v_and_b32 v57, 0xffff0000, v32
v_lshlrev_b32 v58, 16, v33
Zaaa 196,52,0
v_and_b32 v59, 0xffff0000, v33
v_lshlrev_b32 v60, 16, v34
Zaaa 64,56,0
v_and_b32 v61, 0xffff0000, v34
v_lshlrev_b32 v62, 16, v35
Zaaa 68,60,0
W 4
v_and_b32 v63, 0xffff0000, v35
v_lshlrev_b32 v64, 16, v36
Zaaa 76,60,4
Ta 16,14,16384
v_and_b32 v65, 0xffff0000, v36
Zaaa 72,56,4
Ta 18,14,17408
v_lshlrev_b32 v66, 16, v37
Zaaa 204,52,4
v_and_b32 v67, 0xffff0000, v37
v_lshlrev_b32 v68, 16, v38
Zaaa 200,48,4
v_and_b32 v69, 0xffff0000, v38
v_lshlrev_b32 v70, 16, v39
Zvaa 236,44,4
v_and_b32 v71, 0xffff0000, v39
v_lshlrev_b32 v72, 16, v40
Zvaa 232,40,4
v_and_b32 v73, 0xffff0000, v40
v_lshlrev_b32 v74, 16, v41
Zvaa 172,36,4
v_and_b32 v75, 0xffff0000, v41
v_lshlrev_b32 v76, 16, v42
Zvaa 168,32,4
W 4
v_and_b32 v77, 0xffff0000, v42
v_lshlrev_b32 v78, 16, v43
Zvaa 176,32,8
v_and_b32 v79, 0xffff0000, v43
A 80,56,57
Zvaa 180,36,8
Ta 20,15,16384
Ta 22,15,17408
Zvaa 240,40,8
A 81,58,59
A 80,80,60
Zvaa 244,44,8
A 81,81,61
A 80,80,62
Zaaa 208,48,8
A 81,81,63
A 80,80,64
Zaaa 212,52,8
A 81,81,65
A 80,80,66
Zaaa 80,56,8
A 81,81,67
A 80,80,68
Zaaa 84,60,8
W 4
A 81,81,69
A 80,80,70
Zaaa 92,60,12
A 81,81,71
A 80,80,72
Zaaa 88,56,12
Ta 24,24,16384
A 81,81,73
Zaaa 220,52,12
Ta 26,24,17408
A 80,80,74
Zaaa 216,48,12
A 81,81,75
A 80,80,76
Zvaa 252,44,12
A 81,81,77
A 80,80,78
Zvaa 248,40,12
A 81,81,79
A 80,80,81
Zvaa 188,36,12
Ra 128,27,0
Zvaa 184,32,12
W 5
Ra 132,27,2048
v_add_f32_dpp v80, v80, v80 quad_perm:[1,0,3,2] row_mask:0xf bank_mask:0xf
Zvaa 192,32,16
Ta 28,25,16384
Ta 30,25,17408
Zvaa 196,36,16
v_add_f32_dpp v80, v80, v80 quad_perm:[2,3,0,1] row_mask:0xf bank_mask:0xf
Zaaa 160,40,16
Ra 136,27,4096
Zaaa 164,44,16
v_add_f32_dpp v80, v80, v80 row_half_mirror row_mask:0xf bank_mask:0xf
Zaaa 224,48,16
Zaaa 228,52,16
v_add_f32_dpp v80, v80, v80 row_mirror row_mask:0xf bank_mask:0xf
v_mov_b32 v81, v80
Zaaa 96,56,16
Ra 140,27,6144
Zaaa 100,60,16
W 8
v_permlane16_swap_b32 v80, v81
A 80,80,81
Zaaa 108,60,20
Ta 0,10,24576
v_mul_f32 v84, 0x3acccccd, v80
Zaaa 104,56,20
s_mov_b32 s86, 0xffff
s_mov_b32 s87, 0xffff
v_sub_f32 v82, v56, v84
Zaaa 236,52,20
U 80,82,82
v_sub_f32 v83, v57, v84
Zaaa 232,48,20
v_fmac_f32 v80, v83, v83
v_sub_f32 v82, v58, v84
Zaaa 172,44,20
v_fmac_f32 v80, v82, v82
v_sub_f32 v83, v59, v84
Zaaa 168,40,20
v_fmac_f32 v80, v83, v83
v_sub_f32 v82, v60, v84
Zvaa 204,36,20
v_fmac_f32 v80, v82, v82
v_sub_f32 v83, v61, v84
Zvaa 200,32,20
W 7
Ta 2,10,25600
v_fmac_f32 v80, v83, v83
Zvaa 208,32,24
Ra 144,27,8192
v_sub_f32 v82, v62, v84
Zvaa 212,36,24
v_fmac_f32 v80, v82, v82
v_sub_f32 v83, v63, v84
Zaaa 176,40,24
v_fmac_f32 v80, v83, v83
v_sub_f32 v82, v64, v84
Zaaa 180,44,24
Ra 148,27,10240
v_fmac_f32 v80, v82, v82
Zaaa 240,48,24
v_sub_f32 v83, v65, v84
v_fmac_f32 v80, v83, v83
Zaaa 244,52,24
v_sub_f32 v82, v66, v84
v_fmac_f32 v80, v82, v82
Zaaa 112,56,24
Ra 152,27,12288
v_sub_f32 v83, v67, v84
Zaaa 116,60,24
W 7
v_fmac_f32 v80, v83, v83
v_sub_f32 v82, v68, v84
Zaaa 124,60,28
v_fmac_f32 v80, v82, v82
v_sub_f32 v83, v69, v84
Zaaa 120,56,28
Ta 4,11,24576
Ta 6,11,25600
Zaaa 252,52,28
v_fmac_f32 v80, v83, v83
v_sub_f32 v82, v70, v84
Zaaa 248,48,28
Ra 156,27,14336
v_fmac_f32 v80, v82, v82
Zaaa 188,44,28
v_sub_f32 v83, v71, v84
v_fmac_f32 v80, v83, v83
Zaaa 184,40,28
v_sub_f32 v82, v72, v84
U 81,82,82
Zvaa 220,36,28
v_sub_f32 v83, v73, v84
v_fmac_f32 v81, v83, v83
Zvaa 216,32,28
W 6
v_sub_f32 v82, v74, v84
v_fmac_f32 v81, v82, v82
Mvaa 160,128,0
v_sub_f32 v83, v75, v84
v_fmac_f32 v81, v83, v83
Mvaa 164,132,0
Ta 8,12,24576
v_sub_f32 v82, v76, v84
Mvaa 224,136,0
Ta 10,12,25600
v_fmac_f32 v81, v82, v82
Mvaa 228,140,0
W 2
)"
R"(v_sub_f32 v83, v77, v84
v_fmac_f32 v81, v83, v83
Maaa 192,144,0
v_sub_f32 v82, v78, v84
v_fmac_f32 v81, v82, v82
Maaa 196,148,0
v_sub_f32 v83, v79, v84
v_fmac_f32 v81, v83, v83
Maaa 64,152,0
v_cndmask_b32_e64 v81, 0, v81, s[86:87]
A 80,80,81
Maaa 68,156,0
Ta 12,13,24576
Ta 14,13,25600
Maaa 76,156,4
Ta 16,14,24576
v_add_f32_dpp v80, v80, v80 quad_perm:[1,0,3,2] row_mask:0xf bank_mask:0xf
Maaa 72,152,4
Ta 18,14,25600
Maaa 204,148,4
v_add_f32_dpp v80, v80, v80 quad_perm:[2,3,0,1] row_mask:0xf bank_mask:0xf
Maaa 200,144,4
Mvaa 236,140,4
Ta 20,15,24576
v_add_f32_dpp v80, v80, v80 row_half_mirror row_mask:0xf bank_mask:0xf
Mvaa 232,136,4
Ta 22,15,25600
Mvaa 172,132,4
v_add_f32_dpp v80, v80, v80 row_mirror row_mask:0xf bank_mask:0xf
v_mov_b32 v81, v80
Mvaa 168,128,4
W 6
v_permlane16_swap_b32 v80, v81
A 80,80,81
Mvaa 176,128,8
v_mul_f32 v80, 0x3acccccd, v80
K 80,80,80
Mvaa 180,132,8
v_lshlrev_b32 v80, 16, v80
v_add_f32 v80, 0x3727c5ac, v80
Mvaa 240,136,8
K 80,80,80
v_lshlrev_b32 v80, 16, v80
Mvaa 244,140,8
v_sqrt_f32 v81, v80
Maaa 208,144,8
v_rcp_f32 v82, v81
Maaa 212,148,8
v_fma_f32 v83, -v81, v81, v80
v_mul_f32 v82, 0.5, v82
Maaa 80,152,8
v_fma_f32 v81, v83, v82, v81
K 86,81,81
Maaa 84,156,8
W 4
Ta 24,24,24576
v_lshlrev_b32 v86, 16, v86
Maaa 92,156,12
v_rcp_f32 v87, v86
Maaa 88,152,12
Ta 26,24,25600
Ta 28,25,24576
Ta 30,25,25600
Maaa 220,148,12
s_waitcnt vmcnt(1) lgkmcnt(0)
s_barrier
K 85,84,84
Maaa 216,144,12
s_add_u32 s64, s64, 128
v_lshlrev_b32 v85, 16, v85
Mvaa 252,140,12
v_sub_f32 v56, v56, v85
v_sub_f32 v57, v57, v85
Mvaa 248,136,12
s_and_b32 s64, s64, 1023
K 80,56,57
v_lshlrev_b32 v56, 16, v80
Mvaa 188,132,12
v_and_b32 v57, 0xffff0000, v80
U 80,56,87
Mvaa 184,128,12
U 81,57,87
v_fma_f32 v82, -v80, v86, v56
Mvaa 192,128,16
s_add_u32 m0, s61, 0
v_fma_f32 v83, -v81, v86, v57
v_fmac_f32 v80, v82, v87
Mvaa 196,132,16
v_fmac_f32 v81, v83, v87
K 32,80,81
Maaa 160,136,16
v_lshlrev_b32 v80, 16, v32
v_and_b32 v81, 0xffff0000, v32
Maaa 164,140,16
v_lshlrev_b32 v82, 16, v44
v_and_b32 v83, 0xffff0000, v44
Maaa 224,144,16
U 84,80,82
v_fmac_f32 v84, v81, v83
Maaa 228,148,16
v_sub_f32 v58, v58, v85
v_sub_f32 v59, v59, v85
Maaa 96,152,16
K 80,58,59
v_lshlrev_b32 v58, 16, v80
Maaa 100,156,16
v_and_b32 v59, 0xffff0000, v80
U 80,58,87
Maaa 108,156,20
U 81,59,87
v_fma_f32 v82, -v80, v86, v58
Maaa 104,152,20
v_fma_f32 v83, -v81, v86, v59
v_fmac_f32 v80, v82, v87
Maaa 236,148,20
v_fmac_f32 v81, v83, v87
K 33,80,81
Maaa 232,144,20
Ra 32,26,32768
v_lshlrev_b32 v80, 16, v33
Maaa 172,140,20
v_and_b32 v81, 0xffff0000, v33
v_lshlrev_b32 v82, 16, v45
Maaa 168,136,20
v_and_b32 v83, 0xffff0000, v45
v_fmac_f32 v84, v80, v82
Mvaa 204,132,20
Ra 36,26,34816
v_fmac_f32 v84, v81, v83
Mvaa 200,128,20
v_sub_f32 v60, v60, v85
v_sub_f32 v61, v61, v85
Mvaa 208,128,24
K 80,60,61
v_lshlrev_b32 v60, 16, v80
Mvaa 212,132,24
D 2,40,64,0
v_and_b32 v61, 0xffff0000, v80
Maaa 176,136,24
Ra 40,26,36864
U 80,60,87
Maaa 180,140,24
U 81,61,87
v_fma_f32 v82, -v80, v86, v60
Maaa 240,144,24
v_fma_f32 v83, -v81, v86, v61
v_fmac_f32 v80, v82, v87
Maaa 244,148,24
D 3,40,64,1024
v_fmac_f32 v81, v83, v87
Maaa 112,152,24
Ra 44,26,38912
K 34,80,81
Maaa 116,156,24
v_lshlrev_b32 v80, 16, v34
v_and_b32 v81, 0xffff0000, v34
Maaa 124,156,28
v_lshlrev_b32 v82, 16, v46
v_and_b32 v83, 0xffff0000, v46
Maaa 120,152,28
D 4,40,64,2048
Ta 0,10,49152
Maaa 252,148,28
Ta 2,10,50176
Ra 48,26,40960
Maaa 248,144,28
v_fmac_f32 v84, v80, v82
v_fmac_f32 v84, v81, v83
Maaa 188,140,28
v_sub_f32 v62, v62, v85
v_sub_f32 v63, v63, v85
Maaa 184,136,28
D 5,40,64,3072
K 80,62,63
Mvaa 220,132,28
Ra 52,26,43008
Ra 56,26,45056
Ta 4,11,49152
Ta 6,11,50176
Ra 60,26,47104
Mvaa 216,128,28
W 6
v_lshlrev_b32 v62, 16, v80
v_and_b32 v63, 0xffff0000, v80
Mvaa 160,32,0
s_add_u32 m0, s61, 4096
U 80,62,87
U 81,63,87
Mvaa 164,36,0
v_fma_f32 v82, -v80, v86, v62
v_fma_f32 v83, -v81, v86, v63
Mvaa 224,40,0
D 6,40,64,0
v_fmac_f32 v80, v82, v87
Mvaa 228,44,0
W 0
v_fmac_f32 v81, v83, v87
K 35,80,81
Maaa 192,48,0
v_lshlrev_b32 v80, 16, v35
v_and_b32 v81, 0xffff0000, v35
Maaa 196,52,0
v_lshlrev_b32 v82, 16, v47
v_and_b32 v83, 0xffff0000, v47
Maaa 64,56,0
D 7,40,64,1024
v_fmac_f32 v84, v80, v82
Maaa 68,60,0
v_fmac_f32 v84, v81, v83
v_sub_f32 v64, v64, v85
Maaa 76,60,4
v_sub_f32 v65, v65, v85
K 80,64,65
Maaa 72,56,4
v_lshlrev_b32 v64, 16, v80
v_and_b32 v65, 0xffff0000, v80
Maaa 204,52,4
D 8,40,64,2048
U 80,64,87
Maaa 200,48,4
U 81,65,87
v_fma_f32 v82, -v80, v86, v64
Mvaa 236,44,4
Ta 8,12,49152
Ta 10,12,50176
Mvaa 232,40,4
v_fma_f32 v83, -v81, v86, v65
v_fmac_f32 v80, v82, v87
Mvaa 172,36,4
D 9,40,64,3072
v_fmac_f32 v81, v83, v87
Mvaa 168,32,4
W 0
s_lshl_b32 s65, s64, 12
K 36,80,81
Mvaa 176,32,8
s_add_u32 m0, s62, 0
v_lshlrev_b32 v80, 16, v36
v_and_b32 v81, 0xffff0000, v36
Mvaa 180,36,8
v_lshlrev_b32 v82, 16, v48
v_and_b32 v83, 0xffff0000, v48
Mvaa 240,40,8
v_fmac_f32 v84, v80, v82
v_fmac_f32 v84, v81, v83
Mvaa 244,44,8
v_sub_f32 v66, v66, v85
v_sub_f32 v67, v67, v85
Maaa 208,48,8
Ta 12,13,49152
Ta 14,13,50176
Maaa 212,52,8
D 16,44,65,0
K 80,66,67
Maaa 80,56,8
v_lshlrev_b32 v66, 16, v80
v_and_b32 v67, 0xffff0000, v80
Maaa 84,60,8
W 0
U 80,66,87
U 81,67,87
Maaa 92,60,12
v_fma_f32 v82, -v80, v86, v66
v_fma_f32 v83, -v81, v86, v67
Maaa 88,56,12
D 17,44,65,1024
v_fmac_f32 v80, v82, v87
Maaa 220,52,12
v_fmac_f32 v81, v83, v87
K 37,80,81
Maaa 216,48,12
v_lshlrev_b32 v80, 16, v37
v_and_b32 v81, 0xffff0000, v37
Mvaa 252,44,12
Ta 16,14,49152
Ta 18,14,50176
Mvaa 248,40,12
D 18,44,65,2048
v_lshlrev_b32 v82, 16, v49
Mvaa 188,36,12
v_and_b32 v83, 0xffff0000, v49
v_fmac_f32 v84, v80, v82
Mvaa 184,32,12
W 0
v_fmac_f32 v84, v81, v83
v_sub_f32 v68, v68, v85
Mvaa 192,32,16
v_sub_f32 v69, v69, v85
K 80,68,69
Mvaa 196,36,16
D 19,44,65,3072
v_lshlrev_b32 v68, 16, v80
Maaa 160,40,16
s_add_u32 m0, s62, 4096
v_and_b32 v69, 0xffff0000, v80
U 80,68,87
Maaa 164,44,16
U 81,69,87
v_fma_f32 v82, -v80, v86, v68
Maaa 224,48,16
Ta 20,15,49152
Ta 22,15,50176
Maaa 228,52,16
D 20,44,65,0
v_fma_f32 v83, -v81, v86, v69
Maaa 96,56,16
v_fmac_f32 v80, v82, v87
v_fmac_f32 v81, v83, v87
Maaa 100,60,16
W 0
K 38,80,81
v_lshlrev_b32 v80, 16, v38
Maaa 108,60,20
v_and_b32 v81, 0xffff0000, v38
v_lshlrev_b32 v82, 16, v50
Maaa 104,56,20
D 21,44,65,1024
v_and_b32 v83, 0xffff0000, v50
Maaa 236,52,20
v_fmac_f32 v84, v80, v82
v_fmac_f32 v84, v81, v83
Maaa 232,48,20
v_sub_f32 v70, v70, v85
v_sub_f32 v71, v71, v85
Maaa 172,44,20
Ta 24,24,49152
Ta 26,24,50176
Maaa 168,40,20
D 22,44,65,2048
K 80,70,71
Mvaa 204,36,20
v_lshlrev_b32 v70, 16, v80
v_and_b32 v71, 0xffff0000, v80
Mvaa 200,32,20
W 0
U 80,70,87
U 81,71,87
Mvaa 208,32,24
v_fma_f32 v82, -v80, v86, v70
v_fma_f32 v83, -v81, v86, v71
Mvaa 212,36,24
D 23,44,65,3072
v_fmac_f32 v80, v82, v87
Maaa 176,40,24
v_fmac_f32 v81, v83, v87
K 39,80,81
Maaa 180,44,24
v_lshlrev_b32 v80, 16, v39
v_and_b32 v81, 0xffff0000, v39
Maaa 240,48,24
Ra 128,27,32768
Ra 132,27,34816
Ta 28,25,49152
Ta 30,25,50176
Maaa 244,52,24
global_load_dword v142, v[140:141], off
v_lshl_add_u64 v[140:141], v[140:141], 0, s[68:69]
v_lshlrev_b32 v82, 16, v51
Maaa 112,56,24
v_and_b32 v83, 0xffff0000, v51
v_fmac_f32 v84, v80, v82
Maaa 116,60,24
W 0
v_fmac_f32 v84, v81, v83
v_sub_f32 v72, v72, v85
Maaa 124,60,28
v_sub_f32 v73, v73, v85
K 80,72,73
Maaa 120,56,28
v_lshlrev_b32 v72, 16, v80
v_and_b32 v73, 0xffff0000, v80
Maaa 252,52,28
Ra 136,27,36864
Ra 140,27,38912
Maaa 248,48,28
U 80,72,87
U 81,73,87
Maaa 188,44,28
Ta 0,10,57344
Ta 2,10,58368
Maaa 184,40,28
v_fma_f32 v82, -v80, v86, v72
v_fma_f32 v83, -v81, v86, v73
Mvaa 220,36,28
Ra 144,27,40960
Ra 148,27,43008
Ra 152,27,45056
Ta 4,11,57344
Ta 6,11,58368
Ra 156,27,47104
Mvaa 216,32,28
W 6
v_fmac_f32 v80, v82, v87
v_fmac_f32 v81, v83, v87
Mvaa 160,128,0
K 40,80,81
v_lshlrev_b32 v80, 16, v40
Mvaa 164,132,0
v_and_b32 v81, 0xffff0000, v40
v_lshlrev_b32 v82, 16, v52
)"
R"(Mvaa 224,136,0
v_and_b32 v83, 0xffff0000, v52
v_fmac_f32 v84, v80, v82
Mvaa 228,140,0
W 0
v_fmac_f32 v84, v81, v83
v_sub_f32 v74, v74, v85
Maaa 192,144,0
v_sub_f32 v75, v75, v85
K 80,74,75
Maaa 196,148,0
v_lshlrev_b32 v74, 16, v80
v_and_b32 v75, 0xffff0000, v80
Maaa 64,152,0
U 80,74,87
U 81,75,87
Maaa 68,156,0
v_fma_f32 v82, -v80, v86, v74
v_fma_f32 v83, -v81, v86, v75
Maaa 76,156,4
v_fmac_f32 v80, v82, v87
v_fmac_f32 v81, v83, v87
Maaa 72,152,4
K 41,80,81
v_lshlrev_b32 v80, 16, v41
Maaa 204,148,4
v_and_b32 v81, 0xffff0000, v41
v_lshlrev_b32 v82, 16, v53
Maaa 200,144,4
v_and_b32 v83, 0xffff0000, v53
v_fmac_f32 v84, v80, v82
Mvaa 236,140,4
Ta 8,12,57344
Ta 10,12,58368
Mvaa 232,136,4
v_fmac_f32 v84, v81, v83
v_sub_f32 v76, v76, v85
Mvaa 172,132,4
v_sub_f32 v77, v77, v85
K 80,76,77
Mvaa 168,128,4
W 0
v_lshlrev_b32 v76, 16, v80
v_and_b32 v77, 0xffff0000, v80
Mvaa 176,128,8
U 80,76,87
U 81,77,87
Mvaa 180,132,8
v_fma_f32 v82, -v80, v86, v76
v_fma_f32 v83, -v81, v86, v77
Mvaa 240,136,8
v_fmac_f32 v80, v82, v87
v_fmac_f32 v81, v83, v87
Mvaa 244,140,8
K 42,80,81
v_lshlrev_b32 v80, 16, v42
Maaa 208,144,8
Ta 12,13,57344
Ta 14,13,58368
Maaa 212,148,8
v_and_b32 v81, 0xffff0000, v42
v_lshlrev_b32 v82, 16, v54
Maaa 80,152,8
v_and_b32 v83, 0xffff0000, v54
v_fmac_f32 v84, v80, v82
Maaa 84,156,8
W 0
v_fmac_f32 v84, v81, v83
v_sub_f32 v78, v78, v85
Maaa 92,156,12
v_sub_f32 v79, v79, v85
K 80,78,79
Maaa 88,152,12
Ta 16,14,57344
Ta 18,14,58368
Ta 20,15,57344
Ta 22,15,58368
Ta 24,24,57344
Ta 26,24,58368
Ta 28,25,57344
Ta 30,25,58368
v_lshlrev_b32 v78, 16, v80
v_and_b32 v79, 0xffff0000, v80
U 80,78,87
U 81,79,87
v_fma_f32 v82, -v80, v86, v78
v_fma_f32 v83, -v81, v86, v79
v_fmac_f32 v80, v82, v87
v_fmac_f32 v81, v83, v87
K 43,80,81
v_lshlrev_b32 v80, 16, v43
v_and_b32 v81, 0xffff0000, v43
v_lshlrev_b32 v82, 16, v55
v_and_b32 v83, 0xffff0000, v55
v_fmac_f32 v84, v80, v82
v_fmac_f32 v84, v81, v83
v_lshrrev_b32 v82, 9, v1
s_lshl_b32 s80, s14, 1
v_add_u32 v82, s80, v82
v_mul_u32_u24 v83, 1552, v82
v_add_u32 v83, v83, v88
v_add_u32 v83, 131072, v83
ds_write_b128 v83, v[32:35] offset:0
ds_write_b128 v83, v[36:39] offset:512
ds_write_b128 v83, v[40:43] offset:1024
v_add_f32_dpp v84, v84, v84 quad_perm:[1,0,3,2] row_mask:0xf bank_mask:0xf
s_nop 1
v_add_f32_dpp v84, v84, v84 quad_perm:[2,3,0,1] row_mask:0xf bank_mask:0xf
s_nop 1
v_add_f32_dpp v84, v84, v84 row_half_mirror row_mask:0xf bank_mask:0xf
s_nop 1
v_add_f32_dpp v84, v84, v84 row_mirror row_mask:0xf bank_mask:0xf
v_mov_b32 v81, v84
s_nop 1
v_permlane16_swap_b32 v84, v81
A 84,84,81
K 80,84,84
v_lshlrev_b32 v80, 16, v80
s_lshl_b32 s80, s15, 3
v_add_u32 v82, s80, v82
v_sub_u32 v83, 2047, v82
v_ashrrev_i32 v81, 31, v80
v_or_b32 v81, 0x80000000, v81
v_xor_b32 v80, v80, v81
v_and_b32 v80, 0xffff0000, v80
v_or_b32 v80, v80, v83
v_and_b32 v82, 7, v82
v_lshlrev_b32 v82, 2, v82
v_add_u32 v82, 143488, v82
ds_write_b32 v82, v80
Maaa 220,148,12
s_waitcnt vmcnt(1) lgkmcnt(0)
s_barrier
s_add_u32 s64, s64, 128
Maaa 216,144,12
v_bfe_u32 v80, v1, 4, 3
Mvaa 252,140,12
Ra 32,26,0
v_mul_u32_u24 v81, 1552, v80
Mvaa 248,136,12
s_and_b32 s64, s64, 1023
v_lshrrev_b32 v82, 7, v1
Mvaa 188,132,12
v_lshl_add_u32 v81, v82, 4, v81
Mvaa 184,128,12
s_lshl_b32 s80, s14, 7
Ra 36,26,2048
Mvaa 192,128,16
s_add_u32 m0, s61, 32768
s_add_u32 s80, s80, 131072
Mvaa 196,132,16
v_add_u32 v81, s80, v81
Maaa 160,136,16
ds_read_b128 v[32:35], v81
Ra 40,26,4096
Maaa 164,140,16
D 2,40,64,0
Rv 36,81,512
Maaa 224,144,16
s_and_b32 s80, s14, 2
Maaa 228,148,16
s_lshl_b32 s80, s80, 7
Ra 44,26,6144
Maaa 96,152,16
v_subrev_u32 v81, s80, v81
Maaa 100,156,16
D 3,40,64,1024
Rv 40,81,1024
Maaa 108,156,20
Ta 0,10,16384
v_lshlrev_b32 v80, 2, v80
Maaa 104,152,20
Ta 2,10,17408
Ra 48,26,8192
Maaa 236,148,20
v_add_u32 v80, 143488, v80
Maaa 232,144,20
D 4,40,64,2048
ds_read_b32 v84, v80
Maaa 172,140,20
Ra 52,26,10240
v_mov_b32 v85, s13
Maaa 168,136,20
v_lshrrev_b32 v83, 1, v1
Mvaa 204,132,20
s_mul_i32 s82, s15, 10240
s_add_u32 s82, s82, 131072
Ra 56,26,12288
Mvaa 200,128,20
s_lshl_b32 s80, s14, 10
D 5,40,64,3072
Mvaa 208,128,24
s_add_u32 m0, s61, 36864
s_add_u32 s83, s82, s80
Ta 4,11,16384
Mvaa 212,132,24
Ta 6,11,17408
Ra 60,26,14336
Maaa 176,136,24
Maaa 180,140,24
D 6,40,64,0
Maaa 240,144,24
Maaa 244,148,24
Maaa 112,152,24
Maaa 116,156,24
W 5
buffer_store_dwordx4 v[32:35], v1, s[8:11], s83 offen sc1
s_add_u32 s83, s83, 4096
Maaa 124,156,28
Ta 8,12,16384
Ta 10,12,17408
Maaa 120,152,28
Maaa 252,148,28
Maaa 248,144,28
D 7,40,64,1024
Maaa 188,140,28
Maaa 184,136,28
Mvaa 220,132,28
Mvaa 216,128,28
buffer_store_dwordx4 v[36:39], v1, s[8:11], s83 offen sc1
Mvaa 160,32,0
s_and_b32 s80, s80, 1024
s_add_u32 s83, s82, s80
s_add_u32 s83, s83, 8192
Mvaa 164,36,0
Ta 12,13,16384
Ta 14,13,17408
Mvaa 224,40,0
Mvaa 228,44,0
W 4
D 8,40,64,2048
Maaa 192,48,0
Maaa 196,52,0
Maaa 64,56,0
Maaa 68,60,0
buffer_store_dwordx4 v[40:43], v1, s[8:11], s83 offen sc1
Maaa 76,60,4
Ta 16,14,16384
Ta 18,14,17408
Maaa 72,56,4
Maaa 204,52,4
Maaa 200,48,4
D 9,40,64,3072
Mvaa 236,44,4
s_lshl_b32 s65, s64, 12
s_add_u32 m0, s62, 32768
Mvaa 232,40,4
Mvaa 172,36,4
Mvaa 168,32,4
W 4
D 16,44,65,0
Mvaa 176,32,8
Ta 20,15,16384
Ta 22,15,17408
Mvaa 180,36,8
Mvaa 240,40,8
Mvaa 244,44,8
D 17,44,65,1024
Maaa 208,48,8
Maaa 212,52,8
Maaa 80,56,8
Maaa 84,60,8
W 4
D 18,44,65,2048
Maaa 92,60,12
Ta 24,24,16384
Ta 26,24,17408
Maaa 88,56,12
Maaa 220,52,12
Maaa 216,48,12
D 19,44,65,3072
Mvaa 252,44,12
s_add_u32 m0, s62, 36864
Ra 128,27,0
Mvaa 248,40,12
Mvaa 188,36,12
Mvaa 184,32,12
W 5
D 20,44,65,0
Ra 132,27,2048
Mvaa 192,32,16
Ta 28,25,16384
Ta 30,25,17408
Mvaa 196,36,16
Maaa 160,40,16
Ra 136,27,4096
Maaa 164,44,16
D 21,44,65,1024
Maaa 224,48,16
Maaa 228,52,16
Ra 140,27,6144
Maaa 96,56,16
Maaa 100,60,16
W 8
D 22,44,65,2048
Maaa 108,60,20
Ta 0,10,24576
Ta 2,10,25600
Maaa 104,56,20
Ra 144,27,8192
Maaa 236,52,20
Maaa 232,48,20
D 23,44,65,3072
Ra 148,27,10240
Maaa 172,44,20
Maaa 168,40,20
Mvaa 204,36,20
Ra 152,27,12288
Mvaa 200,32,20
W 11
Mvaa 208,32,24
Ta 4,11,24576
Ta 6,11,25600
Mvaa 212,36,24
Ra 156,27,14336
Maaa 176,40,24
Maaa 180,44,24
Maaa 240,48,24
Maaa 244,52,24
Maaa 112,56,24
Maaa 116,60,24
W 10
Maaa 124,60,28
Ta 8,12,24576
Ta 10,12,25600
Maaa 120,56,28
Maaa 252,52,28
Maaa 248,48,28
Maaa 188,44,28
Maaa 184,40,28
Mvaa 220,36,28
Mvaa 216,32,28
W 8
Mvaa 160,128,0
Ta 12,13,24576
Ta 14,13,25600
Mvaa 164,132,0
Mvaa 224,136,0
Mvaa 228,140,0
W 4
Maaa 192,144,0
Maaa 196,148,0
Maaa 64,152,0
Maaa 68,156,0
Maaa 76,156,4
Ta 16,14,24576
Ta 18,14,25600
Maaa 72,152,4
Maaa 204,148,4
Maaa 200,144,4
Mvaa 236,140,4
Ta 20,15,24576
Ta 22,15,25600
Mvaa 232,136,4
Mvaa 172,132,4
Ta 24,24,24576
Ta 26,24,25600
Mvaa 168,128,4
W 8
Mvaa 176,128,8
Ta 28,25,24576
Ta 30,25,25600
Mvaa 180,132,8
Mvaa 240,136,8
Mvaa 244,140,8
Maaa 208,144,8
Maaa 212,148,8
Maaa 80,152,8
Maaa 84,156,8
W 8
Maaa 92,156,12
Maaa 88,152,12
Maaa 220,148,12
s_waitcnt vmcnt(0) lgkmcnt(0)
s_barrier
s_cmp_eq_u32 s14, 0
s_cbranch_scc0 .Lpubr_18%=
s_lshl_b32 s80, s15, 6
s_add_u32 s80, s80, 40960
s_mov_b64 exec, 0xff
buffer_store_dwordx2 v[84:85], v83, s[8:11], s80 offen sc1
s_mov_b64 exec, -1
.Lpubr_18%=:
Maaa 216,144,12
s_add_u32 s64, s64, 128
Mvaa 252,140,12
Ra 32,26,32768
Mvaa 248,136,12
s_and_b32 s64, s64, 1023
Mvaa 188,132,12
Mvaa 184,128,12
s_add_u32 m0, s61, 0
Ra 36,26,34816
Mvaa 192,128,16
Mvaa 196,132,16
D 2,40,64,0
Maaa 160,136,16
Ra 40,26,36864
Maaa 164,140,16
Maaa 224,144,16
)"
R"(Maaa 228,148,16
D 3,40,64,1024
Ra 44,26,38912
Maaa 96,152,16
Maaa 100,156,16
Maaa 108,156,20
Ta 0,10,49152
Ta 2,10,50176
Maaa 104,152,20
D 4,40,64,2048
Ra 48,26,40960
Maaa 236,148,20
Maaa 232,144,20
Ra 52,26,43008
Maaa 172,140,20
Maaa 168,136,20
D 5,40,64,3072
Mvaa 204,132,20
s_add_u32 m0, s61, 4096
Ra 56,26,45056
Mvaa 200,128,20
Mvaa 208,128,24
Ta 4,11,49152
Ta 6,11,50176
Mvaa 212,132,24
D 6,40,64,0
Ra 60,26,47104
Maaa 176,136,24
Maaa 180,140,24
Maaa 240,144,24
Maaa 244,148,24
D 7,40,64,1024
Maaa 112,152,24
Maaa 116,156,24
Maaa 124,156,28
Ta 8,12,49152
Ta 10,12,50176
Maaa 120,152,28
D 8,40,64,2048
Maaa 252,148,28
Maaa 248,144,28
Maaa 188,140,28
Maaa 184,136,28
D 9,40,64,3072
Mvaa 220,132,28
s_lshl_b32 s65, s64, 12
s_add_u32 m0, s62, 0
Mvaa 216,128,28
W 8
Mvaa 160,32,0
Ta 12,13,49152
Ta 14,13,50176
Mvaa 164,36,0
D 16,44,65,0
Mvaa 224,40,0
Mvaa 228,44,0
W 4
Maaa 192,48,0
Maaa 196,52,0
D 17,44,65,1024
Maaa 64,56,0
Maaa 68,60,0
Maaa 76,60,4
Ta 16,14,49152
Ta 18,14,50176
Maaa 72,56,4
D 18,44,65,2048
Maaa 204,52,4
Maaa 200,48,4
Mvaa 236,44,4
Mvaa 232,40,4
D 19,44,65,3072
Mvaa 172,36,4
s_add_u32 m0, s62, 4096
Mvaa 168,32,4
W 4
Mvaa 176,32,8
Ta 20,15,49152
Ta 22,15,50176
Mvaa 180,36,8
D 20,44,65,0
Mvaa 240,40,8
Mvaa 244,44,8
Maaa 208,48,8
Maaa 212,52,8
D 21,44,65,1024
Maaa 80,56,8
Maaa 84,60,8
W 4
Maaa 92,60,12
Ta 24,24,49152
Ta 26,24,50176
Maaa 88,56,12
D 22,44,65,2048
Maaa 220,52,12
Maaa 216,48,12
Mvaa 252,44,12
Ra 128,27,32768
Mvaa 248,40,12
D 23,44,65,3072
Mvaa 188,36,12
s_add_u32 s65, s5, 0
s_cmp_ge_u32 s65, 20
s_cselect_b32 s68, 20, 0
s_sub_u32 s65, s65, s68
Mvaa 184,32,12
W 5
s_lshl_b32 s65, s65, 18
s_add_u32 m0, s63, 131072
Mvaa 192,32,16
Ra 132,27,34816
Ta 28,25,49152
Mvaa 196,36,16
D 16,48,65,0
Ta 30,25,50176
Maaa 160,40,16
Ra 136,27,36864
Maaa 164,44,16
Maaa 224,48,16
Maaa 228,52,16
D 17,48,65,1024
Ra 140,27,38912
Maaa 96,56,16
Maaa 100,60,16
W 8
Maaa 108,60,20
Ta 0,10,57344
Ta 2,10,58368
Maaa 104,56,20
D 18,48,65,2048
Ra 144,27,40960
Maaa 236,52,20
Maaa 232,48,20
Ra 148,27,43008
Maaa 172,44,20
Maaa 168,40,20
D 19,48,65,3072
Mvaa 204,36,20
s_add_u32 m0, s63, 135168
Ra 152,27,45056
Mvaa 200,32,20
W 11
Mvaa 208,32,24
Ta 4,11,57344
Ta 6,11,58368
Mvaa 212,36,24
D 20,48,65,0
Ra 156,27,47104
Maaa 176,40,24
Maaa 180,44,24
Maaa 240,48,24
Maaa 244,52,24
D 21,48,65,1024
Maaa 112,56,24
Maaa 116,60,24
W 10
Maaa 124,60,28
Ta 8,12,57344
Ta 10,12,58368
Maaa 120,56,28
D 22,48,65,2048
Maaa 252,52,28
Maaa 248,48,28
Maaa 188,44,28
Maaa 184,40,28
D 23,48,65,3072
Mvaa 220,36,28
Mvaa 216,32,28
W 8
Mvaa 160,128,0
Ta 12,13,57344
Ta 14,13,58368
Mvaa 164,132,0
global_load_dword v142, v[140:141], off
v_lshl_add_u64 v[140:141], v[140:141], 0, s[68:69]
Mvaa 224,136,0
Mvaa 228,140,0
W 4
Maaa 192,144,0
Maaa 196,148,0
Maaa 64,152,0
Maaa 68,156,0
Maaa 76,156,4
Ta 16,14,57344
Ta 18,14,58368
Maaa 72,152,4
Maaa 204,148,4
Maaa 200,144,4
Mvaa 236,140,4
Ta 20,15,57344
Ta 22,15,58368
Mvaa 232,136,4
Mvaa 172,132,4
Ta 24,24,57344
Ta 26,24,58368
Mvaa 168,128,4
W 8
Mvaa 176,128,8
Ta 28,25,57344
Ta 30,25,58368
Mvaa 180,132,8
Mvaa 240,136,8
Mvaa 244,140,8
Maaa 208,144,8
Maaa 212,148,8
Maaa 80,152,8
Maaa 84,156,8
W 8
Maaa 92,156,12
Maaa 88,152,12
Maaa 220,148,12
s_waitcnt vmcnt(1) lgkmcnt(0)
s_barrier
s_add_u32 s64, s64, 128
Maaa 216,144,12
Mvaa 252,140,12
Ra 32,26,0
Mvaa 248,136,12
s_and_b32 s64, s64, 1023
Mvaa 188,132,12
Mvaa 184,128,12
Ra 36,26,2048
Mvaa 192,128,16
s_add_u32 m0, s61, 32768
Mvaa 196,132,16
Maaa 160,136,16
Ra 40,26,4096
Maaa 164,140,16
D 2,40,64,0
Maaa 224,144,16
Maaa 228,148,16
Ra 44,26,6144
Maaa 96,152,16
Maaa 100,156,16
D 3,40,64,1024
Maaa 108,156,20
Ta 0,10,16384
Ta 2,10,17408
Maaa 104,152,20
Ra 48,26,8192
Maaa 236,148,20
Maaa 232,144,20
D 4,40,64,2048
Ra 52,26,10240
Maaa 172,140,20
Maaa 168,136,20
Mvaa 204,132,20
Ra 56,26,12288
Mvaa 200,128,20
D 5,40,64,3072
Mvaa 208,128,24
s_add_u32 m0, s61, 36864
Ta 4,11,16384
Ta 6,11,17408
Mvaa 212,132,24
Ra 60,26,14336
Maaa 176,136,24
v_lshrrev_b32 v28, 1, v1
Maaa 180,140,24
s_lshl_b32 s80, s60, 9
D 6,40,64,0
Maaa 240,144,24
s_add_u32 s80, s80, 40960
Maaa 244,148,24
Maaa 112,152,24
Maaa 116,156,24
D 7,40,64,1024
Maaa 124,156,28
Ta 8,12,16384
Ta 10,12,17408
Maaa 120,152,28
Maaa 252,148,28
Maaa 248,144,28
buffer_load_dwordx2 v[28:29], v28, s[8:11], s80 offen sc1
Maaa 188,140,28
Maaa 184,136,28
Mvaa 220,132,28
Mvaa 216,128,28
W 8
D 8,40,64,2048
Mvaa 160,32,0
Ta 12,13,16384
Ta 14,13,17408
Mvaa 164,36,0
Mvaa 224,40,0
Mvaa 228,44,0
W 4
D 9,40,64,3072
Maaa 192,48,0
s_lshl_b32 s65, s64, 12
s_add_u32 m0, s62, 32768
Maaa 196,52,0
Maaa 64,56,0
Maaa 68,60,0
D 16,44,65,0
Maaa 76,60,4
Ta 16,14,16384
Ta 18,14,17408
Maaa 72,56,4
Maaa 204,52,4
Maaa 200,48,4
D 17,44,65,1024
Mvaa 236,44,4
Mvaa 232,40,4
Mvaa 172,36,4
Mvaa 168,32,4
W 4
D 18,44,65,2048
Mvaa 176,32,8
Ta 20,15,16384
Ta 22,15,17408
Mvaa 180,36,8
Mvaa 240,40,8
Mvaa 244,44,8
D 19,44,65,3072
Maaa 208,48,8
s_add_u32 m0, s62, 36864
Maaa 212,52,8
Maaa 80,56,8
Maaa 84,60,8
W 4
D 20,44,65,0
Maaa 92,60,12
Ta 24,24,16384
Ta 26,24,17408
Maaa 88,56,12
Maaa 220,52,12
Maaa 216,48,12
D 21,44,65,1024
Mvaa 252,44,12
Ra 128,27,0
Mvaa 248,40,12
Mvaa 188,36,12
Mvaa 184,32,12
W 5
D 22,44,65,2048
Ra 132,27,2048
Mvaa 192,32,16
Ta 28,25,16384
Ta 30,25,17408
Mvaa 196,36,16
Maaa 160,40,16
Ra 136,27,4096
Maaa 164,44,16
D 23,44,65,3072
v_lshrrev_b32 v138, 4, v1
Maaa 224,48,16
s_sub_u32 s81, s73, 1
v_min_u32 v138, s81, v138
v_lshrrev_b32 v139, 2, v138
Maaa 228,52,16
Ra 140,27,6144
v_mul_u32_u24 v139, s72, v139
Maaa 96,56,16
v_and_b32 v138, 3, v138
Maaa 100,60,16
W 8
v_lshl_add_u32 v138, v138, 7, v139
Maaa 108,60,20
s_waitcnt vmcnt(10)
v_cmp_eq_u32 vcc, s13, v29
s_cmp_eq_u64 vcc, -1
s_cbranch_scc0 .Lspin_19%=
.Lok_20%=:
v_add_u32 v138, s74, v138
Maaa 104,56,20
s_lshr_b32 s73, s73, 2
Ta 0,10,24576
Ta 2,10,25600
Maaa 236,52,20
s_mul_i32 s73, s73, s72
Ra 144,27,8192
v_bfe_u32 v144, v1, 8, 1
Maaa 232,48,20
v_min_u32 v139, s75, v138
v_mul_u32_u24 v144, 10240, v144
Maaa 172,44,20
Ra 148,27,10240
v_and_b32 v145, 0xf0, v1
Maaa 168,40,20
global_load_dword v142, v139, s[76:77]
v_lshl_add_u32 v144, v145, 3, v144
Mvaa 204,36,20
s_mul_i32 s69, s58, 20480
Ra 152,27,12288
v_add_u32 v138, s73, v138
Mvaa 200,32,20
W 11
s_add_u32 s69, s69, s28
Mvaa 208,32,24
Ta 4,11,24576
Ta 6,11,25600
Mvaa 212,36,24
buffer_load_dword v142, v144, s[8:11], s69 offen offset:0 sc1
Ra 156,27,14336
Maaa 176,40,24
Maaa 180,44,24
Maaa 240,48,24
Maaa 244,52,24
Maaa 112,56,24
)"
R"(Maaa 116,60,24
W 10
Maaa 124,60,28
Ta 8,12,24576
Ta 10,12,25600
Maaa 120,56,28
Maaa 252,52,28
Maaa 248,48,28
Maaa 188,44,28
Maaa 184,40,28
Mvaa 220,36,28
Mvaa 216,32,28
W 8
Mvaa 160,128,0
Ta 12,13,24576
Ta 14,13,25600
Mvaa 164,132,0
Mvaa 224,136,0
Mvaa 228,140,0
W 4
Maaa 192,144,0
Maaa 196,148,0
Maaa 64,152,0
Maaa 68,156,0
Maaa 76,156,4
Ta 16,14,24576
Ta 18,14,25600
Maaa 72,152,4
Maaa 204,148,4
Maaa 200,144,4
Mvaa 236,140,4
Ta 20,15,24576
Ta 22,15,25600
Mvaa 232,136,4
Mvaa 172,132,4
Ta 24,24,24576
Ta 26,24,25600
Mvaa 168,128,4
W 8
Mvaa 176,128,8
Ta 28,25,24576
Ta 30,25,25600
Mvaa 180,132,8
Mvaa 240,136,8
Mvaa 244,140,8
Maaa 208,144,8
Maaa 212,148,8
Maaa 80,152,8
Maaa 84,156,8
W 8
Maaa 92,156,12
Maaa 88,152,12
Maaa 220,148,12
s_waitcnt vmcnt(2) lgkmcnt(0)
s_barrier
s_add_u32 s64, s64, 128
Maaa 216,144,12
Mvaa 252,140,12
Ra 32,26,32768
Mvaa 248,136,12
s_and_b32 s64, s64, 1023
Mvaa 188,132,12
Mvaa 184,128,12
Ra 36,26,34816
Mvaa 192,128,16
s_add_u32 m0, s61, 0
Mvaa 196,132,16
Maaa 160,136,16
Ra 40,26,36864
Maaa 164,140,16
D 2,40,64,0
Maaa 224,144,16
Maaa 228,148,16
Ra 44,26,38912
Maaa 96,152,16
Maaa 100,156,16
D 3,40,64,1024
Maaa 108,156,20
Ta 0,10,49152
Ta 2,10,50176
Maaa 104,152,20
Ra 48,26,40960
Maaa 236,148,20
Maaa 232,144,20
D 4,40,64,2048
Ra 52,26,43008
Maaa 172,140,20
Maaa 168,136,20
Mvaa 204,132,20
Ra 56,26,45056
Mvaa 200,128,20
D 5,40,64,3072
Mvaa 208,128,24
s_add_u32 m0, s61, 4096
Ta 4,11,49152
Ta 6,11,50176
Mvaa 212,132,24
Ra 60,26,47104
Maaa 176,136,24
Maaa 180,140,24
D 6,40,64,0
Maaa 240,144,24
Maaa 244,148,24
Maaa 112,152,24
Maaa 116,156,24
D 7,40,64,1024
Maaa 124,156,28
Ta 8,12,49152
Ta 10,12,50176
Maaa 120,152,28
Maaa 252,148,28
Maaa 248,144,28
D 8,40,64,2048
Maaa 188,140,28
Maaa 184,136,28
Mvaa 220,132,28
Mvaa 216,128,28
W 8
D 9,40,64,3072
Mvaa 160,32,0
s_lshl_b32 s65, s64, 12
s_add_u32 m0, s62, 0
Ta 12,13,49152
Mvaa 164,36,0
Ta 14,13,50176
Mvaa 224,40,0
Mvaa 228,44,0
W 4
D 16,44,65,0
Maaa 192,48,0
Maaa 196,52,0
Maaa 64,56,0
Maaa 68,60,0
D 17,44,65,1024
Maaa 76,60,4
Ta 16,14,49152
Ta 18,14,50176
Maaa 72,56,4
Maaa 204,52,4
Maaa 200,48,4
D 18,44,65,2048
Mvaa 236,44,4
Mvaa 232,40,4
Mvaa 172,36,4
Mvaa 168,32,4
W 4
D 19,44,65,3072
Mvaa 176,32,8
s_add_u32 m0, s62, 4096
Ta 20,15,49152
Ta 22,15,50176
Mvaa 180,36,8
Mvaa 240,40,8
Mvaa 244,44,8
D 20,44,65,0
Maaa 208,48,8
Maaa 212,52,8
Maaa 80,56,8
Maaa 84,60,8
W 4
D 21,44,65,1024
Maaa 92,60,12
Ta 24,24,49152
Ta 26,24,50176
Maaa 88,56,12
Maaa 220,52,12
Maaa 216,48,12
D 22,44,65,2048
Mvaa 252,44,12
Ra 128,27,32768
Mvaa 248,40,12
Mvaa 188,36,12
Mvaa 184,32,12
W 5
D 23,44,65,3072
Ra 132,27,34816
Mvaa 192,32,16
Ta 28,25,49152
Ta 30,25,50176
Mvaa 196,36,16
v_min_u32 v139, s75, v138
Maaa 160,40,16
Ra 136,27,36864
Maaa 164,44,16
buffer_load_dword v142, v144, s[8:11], s69 offen offset:2048 sc1
Maaa 224,48,16
Maaa 228,52,16
s_add_u32 s69, s69, 4096
Ra 140,27,38912
Maaa 96,56,16
Maaa 100,60,16
W 8
global_load_dword v142, v139, s[76:77]
Maaa 108,60,20
Ta 0,10,57344
Ta 2,10,58368
Maaa 104,56,20
Ra 144,27,40960
Maaa 236,52,20
v_add_u32 v138, s73, v138
Maaa 232,48,20
Ra 148,27,43008
Maaa 172,44,20
Maaa 168,40,20
Mvaa 204,36,20
Ra 152,27,45056
Mvaa 200,32,20
W 11
Mvaa 208,32,24
Ta 4,11,57344
Ta 6,11,58368
Mvaa 212,36,24
Ra 156,27,47104
Maaa 176,40,24
Maaa 180,44,24
Maaa 240,48,24
Maaa 244,52,24
Maaa 112,56,24
Maaa 116,60,24
W 10
Maaa 124,60,28
Ta 8,12,57344
Ta 10,12,58368
Maaa 120,56,28
Maaa 252,52,28
Maaa 248,48,28
Maaa 188,44,28
Maaa 184,40,28
Mvaa 220,36,28
Mvaa 216,32,28
W 8
Mvaa 160,128,0
Ta 12,13,57344
Ta 14,13,58368
Mvaa 164,132,0
Mvaa 224,136,0
Mvaa 228,140,0
W 4
Maaa 192,144,0
Maaa 196,148,0
Maaa 64,152,0
Maaa 68,156,0
Maaa 76,156,4
Ta 16,14,57344
Ta 18,14,58368
Maaa 72,152,4
Maaa 204,148,4
Maaa 200,144,4
Mvaa 236,140,4
Ta 20,15,57344
Ta 22,15,58368
Mvaa 232,136,4
Mvaa 172,132,4
Ta 24,24,57344
Ta 26,24,58368
Mvaa 168,128,4
W 8
Mvaa 176,128,8
Ta 28,25,57344
Ta 30,25,58368
Mvaa 180,132,8
Mvaa 240,136,8
Mvaa 244,140,8
Maaa 208,144,8
Maaa 212,148,8
Maaa 80,152,8
Maaa 84,156,8
W 8
Maaa 92,156,12
Maaa 88,152,12
Maaa 220,148,12
s_waitcnt vmcnt(2) lgkmcnt(0)
s_barrier
s_cmp_eq_u32 s85, 43
s_cbranch_scc1 .Lrt_22%=
.Lrtr_23%=:
Maaa 216,144,12
s_add_u32 s64, s64, 128
Mvaa 252,140,12
Ra 32,26,0
Mvaa 248,136,12
s_and_b32 s64, s64, 1023
Mvaa 188,132,12
Mvaa 184,128,12
Ra 36,26,2048
Mvaa 192,128,16
s_add_u32 m0, s61, 32768
Mvaa 196,132,16
Maaa 160,136,16
Ra 40,26,4096
Maaa 164,140,16
D 2,40,64,0
Maaa 224,144,16
Maaa 228,148,16
Ra 44,26,6144
Maaa 96,152,16
Maaa 100,156,16
D 3,40,64,1024
Maaa 108,156,20
Ta 0,10,16384
Ta 2,10,17408
Maaa 104,152,20
Ra 48,26,8192
Maaa 236,148,20
Maaa 232,144,20
D 4,40,64,2048
Ra 52,26,10240
Maaa 172,140,20
Maaa 168,136,20
Mvaa 204,132,20
Ra 56,26,12288
Mvaa 200,128,20
D 5,40,64,3072
Mvaa 208,128,24
s_add_u32 m0, s61, 36864
Ta 4,11,16384
Ta 6,11,17408
Mvaa 212,132,24
Ra 60,26,14336
Maaa 176,136,24
Maaa 180,140,24
D 6,40,64,0
Maaa 240,144,24
Maaa 244,148,24
Maaa 112,152,24
Maaa 116,156,24
D 7,40,64,1024
Maaa 124,156,28
Ta 8,12,16384
Ta 10,12,17408
Maaa 120,152,28
Maaa 252,148,28
Maaa 248,144,28
D 8,40,64,2048
Maaa 188,140,28
Maaa 184,136,28
Mvaa 220,132,28
Mvaa 216,128,28
W 8
D 9,40,64,3072
Mvaa 160,32,0
s_lshl_b32 s65, s64, 12
s_add_u32 m0, s62, 32768
Ta 12,13,16384
Mvaa 164,36,0
Ta 14,13,17408
Mvaa 224,40,0
Mvaa 228,44,0
W 4
D 16,44,65,0
Maaa 192,48,0
Maaa 196,52,0
Maaa 64,56,0
Maaa 68,60,0
D 17,44,65,1024
Maaa 76,60,4
Ta 16,14,16384
Ta 18,14,17408
Maaa 72,56,4
Maaa 204,52,4
Maaa 200,48,4
D 18,44,65,2048
Mvaa 236,44,4
Mvaa 232,40,4
Mvaa 172,36,4
Mvaa 168,32,4
W 4
D 19,44,65,3072
Mvaa 176,32,8
s_add_u32 m0, s62, 36864
Ta 20,15,16384
Ta 22,15,17408
Mvaa 180,36,8
Mvaa 240,40,8
Mvaa 244,44,8
D 20,44,65,0
Maaa 208,48,8
Maaa 212,52,8
Maaa 80,56,8
Maaa 84,60,8
W 4
D 21,44,65,1024
Maaa 92,60,12
Ta 24,24,16384
Ta 26,24,17408
Maaa 88,56,12
Maaa 220,52,12
Maaa 216,48,12
D 22,44,65,2048
Mvaa 252,44,12
Ra 128,27,0
Mvaa 248,40,12
Mvaa 188,36,12
Mvaa 184,32,12
W 5
D 23,44,65,3072
v_add_u32 v2, 131072, v10
Mvaa 192,32,16
Ra 132,27,2048
Ta 28,25,16384
)"
R"(Mvaa 196,36,16
Ta 30,25,17408
v_add_u32 v3, 131072, v11
Maaa 160,40,16
Ra 136,27,4096
v_add_u32 v4, 131072, v12
Maaa 164,44,16
buffer_load_dword v142, v144, s[8:11], s69 offen offset:0 sc1
Maaa 224,48,16
v_add_u32 v5, 131072, v13
Maaa 228,52,16
Ra 140,27,6144
Maaa 96,56,16
v_add_u32 v6, 131072, v14
Maaa 100,60,16
W 8
Maaa 108,60,20
Ta 0,10,24576
Ta 2,10,25600
Maaa 104,56,20
Ra 144,27,8192
v_add_u32 v7, 131072, v15
Maaa 236,52,20
v_add_u32 v8, 131072, v24
Maaa 232,48,20
Ra 148,27,10240
Maaa 172,44,20
v_add_u32 v9, 131072, v25
Maaa 168,40,20
Mvaa 204,36,20
Ra 152,27,12288
Mvaa 200,32,20
W 11
Mvaa 208,32,24
Ta 4,11,24576
Ta 6,11,25600
Mvaa 212,36,24
Ra 156,27,14336
Maaa 176,40,24
Maaa 180,44,24
Maaa 240,48,24
Maaa 244,52,24
Maaa 112,56,24
Maaa 116,60,24
W 10
Maaa 124,60,28
Ta 8,12,24576
Ta 10,12,25600
Maaa 120,56,28
Maaa 252,52,28
Maaa 248,48,28
Maaa 188,44,28
Maaa 184,40,28
Mvaa 220,36,28
Mvaa 216,32,28
W 8
Mvaa 160,128,0
Ta 12,13,24576
Ta 14,13,25600
Mvaa 164,132,0
Mvaa 224,136,0
Mvaa 228,140,0
W 4
Maaa 192,144,0
Maaa 196,148,0
Maaa 64,152,0
Maaa 68,156,0
Maaa 76,156,4
Ta 16,14,24576
Ta 18,14,25600
Maaa 72,152,4
Maaa 204,148,4
Maaa 200,144,4
Mvaa 236,140,4
Ta 20,15,24576
Ta 22,15,25600
Mvaa 232,136,4
Mvaa 172,132,4
Ta 24,24,24576
Ta 26,24,25600
Mvaa 168,128,4
W 8
Mvaa 176,128,8
Ta 28,25,24576
Ta 30,25,25600
Mvaa 180,132,8
Mvaa 240,136,8
Mvaa 244,140,8
Maaa 208,144,8
Maaa 212,148,8
Maaa 80,152,8
Maaa 84,156,8
W 8
Maaa 92,156,12
Maaa 88,152,12
Maaa 220,148,12
s_waitcnt vmcnt(1) lgkmcnt(0)
s_barrier
s_cmp_eq_u32 s85, 43
s_cbranch_scc1 .Lrt_24%=
.Lrtr_25%=:
Maaa 216,144,12
s_add_u32 s65, s5, 4
Mvaa 252,140,12
Ra 32,26,32768
Mvaa 248,136,12
s_cmp_ge_u32 s65, 20
s_cselect_b32 s68, 20, 0
Mvaa 188,132,12
Mvaa 184,128,12
s_sub_u32 s65, s65, s68
Ra 36,26,34816
Mvaa 192,128,16
Mvaa 196,132,16
s_lshl_b32 s65, s65, 18
Maaa 160,136,16
Ra 40,26,36864
Maaa 164,140,16
Maaa 224,144,16
s_add_u32 m0, s63, 0
Maaa 228,148,16
Ra 44,26,38912
Maaa 96,152,16
D 16,48,65,0
Maaa 100,156,16
Maaa 108,156,20
Ta 0,10,49152
Ta 2,10,50176
Maaa 104,152,20
Ra 48,26,40960
Maaa 236,148,20
D 17,48,65,1024
Maaa 232,144,20
Ra 52,26,43008
Maaa 172,140,20
Maaa 168,136,20
Mvaa 204,132,20
D 18,48,65,2048
Ra 56,26,45056
Mvaa 200,128,20
Mvaa 208,128,24
Ta 4,11,49152
Ta 6,11,50176
Mvaa 212,132,24
Ra 60,26,47104
Maaa 176,136,24
D 19,48,65,3072
Maaa 180,140,24
s_add_u32 m0, s63, 4096
Maaa 240,144,24
Maaa 244,148,24
Maaa 112,152,24
D 20,48,65,0
Maaa 116,156,24
Maaa 124,156,28
Ta 8,12,49152
Ta 10,12,50176
Maaa 120,152,28
Maaa 252,148,28
D 21,48,65,1024
Maaa 248,144,28
Maaa 188,140,28
Maaa 184,136,28
Mvaa 220,132,28
D 22,48,65,2048
Mvaa 216,128,28
W 8
Mvaa 160,32,0
Ta 12,13,49152
Ta 14,13,50176
Mvaa 164,36,0
Mvaa 224,40,0
D 23,48,65,3072
Mvaa 228,44,0
W 4
s_add_u32 s65, s5, 8
s_cmp_ge_u32 s65, 20
s_cselect_b32 s68, 20, 0
Maaa 192,48,0
s_sub_u32 s65, s65, s68
s_lshl_b32 s65, s65, 18
s_add_u32 m0, s63, 65536
Maaa 196,52,0
Maaa 64,56,0
D 16,48,65,0
Maaa 68,60,0
Maaa 76,60,4
Ta 16,14,49152
Ta 18,14,50176
Maaa 72,56,4
Maaa 204,52,4
D 17,48,65,1024
Maaa 200,48,4
Mvaa 236,44,4
Mvaa 232,40,4
Mvaa 172,36,4
D 18,48,65,2048
Mvaa 168,32,4
W 4
Mvaa 176,32,8
Ta 20,15,49152
Ta 22,15,50176
Mvaa 180,36,8
Mvaa 240,40,8
D 19,48,65,3072
Mvaa 244,44,8
s_add_u32 m0, s63, 69632
Maaa 208,48,8
Maaa 212,52,8
Maaa 80,56,8
D 20,48,65,0
Maaa 84,60,8
W 4
Maaa 92,60,12
Ta 24,24,49152
Ta 26,24,50176
Maaa 88,56,12
Maaa 220,52,12
D 21,48,65,1024
Maaa 216,48,12
Mvaa 252,44,12
Ra 128,27,32768
Mvaa 248,40,12
Mvaa 188,36,12
D 22,48,65,2048
Mvaa 184,32,12
W 5
Ra 132,27,34816
Mvaa 192,32,16
Ta 28,25,49152
Ta 30,25,50176
Mvaa 196,36,16
Maaa 160,40,16
D 23,48,65,3072
v_min_u32 v139, s75, v138
Maaa 164,44,16
Ra 136,27,36864
Maaa 224,48,16
Maaa 228,52,16
Ra 140,27,38912
Maaa 96,56,16
buffer_load_dword v142, v144, s[8:11], s69 offen offset:2048 sc1
Maaa 100,60,16
W 8
s_add_u32 s69, s69, 4096
Maaa 108,60,20
Ta 0,10,57344
Ta 2,10,58368
Maaa 104,56,20
Ra 144,27,40960
Maaa 236,52,20
global_load_dword v142, v139, s[76:77]
v_add_u32 v138, s73, v138
Maaa 232,48,20
Ra 148,27,43008
Maaa 172,44,20
Maaa 168,40,20
v_bfe_u32 v29, v1, 7, 1
Mvaa 204,36,20
Ra 152,27,45056
Mvaa 200,32,20
W 11
v_mul_u32_u24 v28, 10240, v29
Mvaa 208,32,24
Ta 4,11,57344
Ta 6,11,58368
Mvaa 212,36,24
Ra 156,27,47104
v_lshrrev_b32 v29, 8, v1
Maaa 176,40,24
Maaa 180,44,24
v_lshl_add_u32 v28, v29, 7, v28
Maaa 240,48,24
v_and_b32 v29, 0x70, v1
Maaa 244,52,24
Maaa 112,56,24
v_add_u32 v28, v28, v29
Maaa 116,60,24
W 10
Maaa 124,60,28
s_and_b32 s80, s14, 2
Ta 8,12,57344
Ta 10,12,58368
Maaa 120,56,28
s_mul_i32 s80, s80, 20480
Maaa 252,52,28
Maaa 248,48,28
s_sub_u32 s81, 40960, s80
Maaa 188,44,28
Maaa 184,40,28
s_add_u32 s93, s28, 20480
Mvaa 220,36,28
Mvaa 216,32,28
W 8
v_add_u32 v26, s80, v28
Mvaa 160,128,0
Ta 12,13,57344
Ta 14,13,58368
Mvaa 164,132,0
Mvaa 224,136,0
v_add_u32 v27, s81, v28
Mvaa 228,140,0
W 4
Maaa 192,144,0
buffer_load_dwordx4 a[32:35], v26, s[8:11], s28 offen offset:0 sc1
Maaa 196,148,0
Maaa 64,152,0
Maaa 68,156,0
Maaa 76,156,4
buffer_load_dwordx4 a[36:39], v26, s[8:11], s93 offen offset:0 sc1
Ta 16,14,57344
Maaa 72,152,4
Ta 18,14,58368
Maaa 204,148,4
Maaa 200,144,4
Mvaa 236,140,4
buffer_load_dwordx4 a[40:43], v27, s[8:11], s28 offen offset:0 sc1
Ta 20,15,57344
Mvaa 232,136,4
Ta 22,15,58368
Mvaa 172,132,4
Ta 24,24,57344
Ta 26,24,58368
Mvaa 168,128,4
W 8
Mvaa 176,128,8
buffer_load_dwordx4 a[44:47], v27, s[8:11], s93 offen offset:0 sc1
Ta 28,25,57344
Mvaa 180,132,8
s_add_u32 s28, s28, 512
s_cmp_eq_u32 s28, s4
s_cselect_b32 s93, 10240, 0
Ta 30,25,58368
Mvaa 240,136,8
s_sub_u32 s28, s28, s93
Mvaa 244,140,8
s_add_u32 s93, s28, 20480
Maaa 208,144,8
Maaa 212,148,8
Maaa 80,152,8
buffer_load_dwordx4 a[48:51], v26, s[8:11], s28 offen offset:0 sc1
Maaa 84,156,8
W 8
Maaa 92,156,12
Maaa 88,152,12
Maaa 220,148,12
s_waitcnt vmcnt(7) lgkmcnt(0)
s_barrier
buffer_load_dwordx4 a[52:55], v26, s[8:11], s93 offen offset:0 sc1
Maaa 216,144,12
Mvaa 252,140,12
Mvaa 248,136,12
Mvaa 188,132,12
buffer_load_dwordx4 a[56:59], v27, s[8:11], s28 offen offset:0 sc1
Mvaa 184,128,12
Mvaa 192,128,16
Mvaa 196,132,16
Maaa 160,136,16
s_add_u32 s65, s5, 12
buffer_load_dwordx4 a[60:63], v27, s[8:11], s93 offen offset:0 sc1
Maaa 164,140,16
Maaa 224,144,16
Maaa 228,148,16
Maaa 96,152,16
Maaa 100,156,16
Maaa 108,156,20
Maaa 104,152,20
Maaa 236,148,20
s_cmp_ge_u32 s65, 20
s_cselect_b32 s68, 20, 0
Ta 0,2,0
Ta 2,2,1024
Maaa 232,144,20
Maaa 172,140,20
Maaa 168,136,20
Mvaa 204,132,20
Ta 4,3,0
Ta 6,3,1024
Mvaa 200,128,20
Mvaa 208,128,24
Mvaa 212,132,24
s_sub_u32 s65, s65, s68
Maaa 176,136,24
Ta 8,4,0
Ta 10,4,1024
Maaa 180,140,24
Maaa 240,144,24
Maaa 244,148,24
Maaa 112,152,24
Ta 12,5,0
Ta 14,5,1024
Maaa 116,156,24
Maaa 124,156,28
s_lshl_b32 s65, s65, 18
Maaa 120,152,28
Maaa 252,148,28
Ta 16,6,0
Ta 18,6,1024
Maaa 248,144,28
Maaa 188,140,28
)"
R"(Maaa 184,136,28
Mvaa 220,132,28
Mvaa 216,128,28
s_add_u32 m0, s63, 32768
.Lq_11%=:
s_cmp_ge_u32 s14, 2
s_cbranch_scc1 .Lpb_30%=
s_waitcnt vmcnt(4) lgkmcnt(6)
Zvaa 32,0,32
s_add_u32 s28, s28, 512
Ta 20,7,0
Ta 22,7,1024
Zvaa 64,0,36
s_cmp_eq_u32 s28, s4
s_cselect_b32 s93, 10240, 0
Ta 24,8,0
Ta 26,8,1024
Zvaa 96,0,40
s_sub_u32 s28, s28, s93
s_add_u32 s93, s28, 20480
buffer_load_dwordx4 a[128:131], v26, s[8:11], s28 offen offset:0 sc1
Zvaa 128,0,44
Ta 28,9,0
Ta 30,9,1024
Zvaa 132,4,44
Zvaa 100,4,40
Zvaa 68,4,36
buffer_load_dwordx4 a[132:135], v26, s[8:11], s93 offen offset:0 sc1
Ta 0,2,8192
Zvaa 36,4,32
W 9
Ta 2,2,9216
Zvaa 40,8,32
Zvaa 72,8,36
Zvaa 104,8,40
buffer_load_dwordx4 a[136:139], v27, s[8:11], s28 offen offset:0 sc1
Ta 4,3,8192
Zvaa 136,8,44
Ta 6,3,9216
K 160,160,161
Zvaa 140,12,44
K 161,162,163
Zvaa 108,12,40
Zvaa 76,12,36
buffer_load_dwordx4 a[140:143], v27, s[8:11], s93 offen offset:0 sc1
Ta 8,4,8192
Zvaa 44,12,32
W 9
s_add_u32 s28, s28, 512
s_cmp_eq_u32 s28, s4
s_cselect_b32 s93, 10240, 0
Zvaa 48,16,32
s_sub_u32 s28, s28, s93
s_add_u32 s93, s28, 20480
Ta 10,4,9216
Zvaa 80,16,36
K 162,164,165
K 163,166,167
Zvaa 112,16,40
s_add_u32 s36, s29, 0
buffer_load_dwordx4 a[144:147], v26, s[8:11], s28 offen offset:0 sc1
Ta 12,5,8192
Zvaa 144,16,44
Ta 14,5,9216
Zvaa 148,20,44
Zvaa 116,20,40
Zvaa 84,20,36
buffer_load_dwordx4 a[148:151], v26, s[8:11], s93 offen offset:0 sc1
Ta 16,6,8192
Zvaa 52,20,32
W 9
Ta 18,6,9216
Zvaa 56,24,32
Zvaa 88,24,36
Zvaa 120,24,40
buffer_load_dwordx4 a[152:155], v27, s[8:11], s28 offen offset:0 sc1
Ta 20,7,8192
Zvaa 152,24,44
Ta 22,7,9216
Zvaa 156,28,44
Zvaa 124,28,40
Zvaa 92,28,36
buffer_store_dwordx4 v[160:163], v1, s[8:11], s36 offen offset:0 sc1
K 168,168,169
Zvaa 60,28,32
s_waitcnt vmcnt(8) lgkmcnt(8)
Ta 24,8,8192
Ta 26,8,9216
Mvaa 32,0,48
K 169,170,171
Mvaa 64,0,52
Mvaa 96,0,56
buffer_load_dwordx4 a[156:159], v27, s[8:11], s93 offen offset:0 sc1
K 170,172,173
Mvaa 128,0,60
s_add_u32 s28, s28, 512
s_cmp_eq_u32 s28, s4
s_cselect_b32 s93, 10240, 0
s_sub_u32 s28, s28, s93
Mvaa 132,4,60
s_add_u32 s93, s28, 20480
Ta 28,9,8192
Ta 30,9,9216
Mvaa 100,4,56
K 171,174,175
Mvaa 68,4,52
D 16,48,65,0
Ta 0,2,16384
Mvaa 36,4,48
W 9
Ta 2,2,17408
Mvaa 40,8,48
Mvaa 72,8,52
Mvaa 104,8,56
D 17,48,65,1024
Ta 4,3,16384
Mvaa 136,8,60
Ta 6,3,17408
Mvaa 140,12,60
Mvaa 108,12,56
Mvaa 76,12,52
D 18,48,65,2048
Ta 8,4,16384
Mvaa 44,12,48
W 9
Ta 10,4,17408
Mvaa 48,16,48
Mvaa 80,16,52
buffer_store_dwordx4 v[168:171], v1, s[8:11], s36 offen offset:1024 sc1
K 176,176,177
Mvaa 112,16,56
Ta 12,5,16384
K 177,178,179
Mvaa 144,16,60
Ta 14,5,17408
Mvaa 148,20,60
K 178,180,181
Mvaa 116,20,56
D 19,48,65,3072
Mvaa 84,20,52
s_add_u32 m0, s63, 36864
Ta 16,6,16384
Ta 18,6,17408
Mvaa 52,20,48
W 10
K 179,182,183
Mvaa 56,24,48
Mvaa 88,24,52
buffer_load_dwordx4 a[32:35], v26, s[8:11], s28 offen offset:0 sc1
Mvaa 120,24,56
Ta 20,7,16384
Ta 22,7,17408
Mvaa 152,24,60
Mvaa 156,28,60
Mvaa 124,28,56
buffer_load_dwordx4 a[36:39], v26, s[8:11], s93 offen offset:0 sc1
Mvaa 92,28,52
Ta 24,8,16384
Ta 26,8,17408
Mvaa 60,28,48
s_waitcnt vmcnt(12) lgkmcnt(10)
Mvaa 32,0,128
Mvaa 64,0,132
buffer_load_dwordx4 a[40:43], v27, s[8:11], s28 offen offset:0 sc1
Mvaa 96,0,136
Ta 28,9,16384
Ta 30,9,17408
Mvaa 128,0,140
buffer_store_dwordx4 v[176:179], v1, s[8:11], s36 offen offset:2048 sc1
K 184,184,185
Mvaa 132,4,140
Mvaa 100,4,136
K 185,186,187
Mvaa 68,4,132
Ta 0,2,24576
Ta 2,2,25600
Mvaa 36,4,128
W 10
buffer_load_dwordx4 a[44:47], v27, s[8:11], s93 offen offset:0 sc1
K 186,188,189
Mvaa 40,8,128
s_add_u32 s28, s28, 512
s_cmp_eq_u32 s28, s4
s_cselect_b32 s93, 10240, 0
s_sub_u32 s28, s28, s93
Mvaa 72,8,132
s_add_u32 s93, s28, 20480
Mvaa 104,8,136
Ta 4,3,24576
K 187,190,191
Mvaa 136,8,140
D 20,48,65,0
Ta 6,3,25600
Mvaa 140,12,140
Mvaa 108,12,136
Mvaa 76,12,132
Ta 8,4,24576
Ta 10,4,25600
Mvaa 44,12,128
W 10
D 21,48,65,1024
Mvaa 48,16,128
Mvaa 80,16,132
Mvaa 112,16,136
Ta 12,5,24576
Ta 14,5,25600
Mvaa 144,16,140
D 22,48,65,2048
Mvaa 148,20,140
Mvaa 116,20,136
Mvaa 84,20,132
buffer_store_dwordx4 v[184:187], v1, s[8:11], s36 offen offset:3072 sc1
K 192,192,193
Mvaa 52,20,128
W 8
Ta 16,6,24576
Ta 18,6,25600
Mvaa 56,24,128
K 193,194,195
K 194,196,197
Mvaa 88,24,132
Mvaa 120,24,136
D 23,48,65,3072
Ta 20,7,24576
Mvaa 152,24,140
s_add_u32 s65, s5, 16
s_cmp_ge_u32 s65, 20
s_cselect_b32 s68, 20, 0
s_sub_u32 s65, s65, s68
Mvaa 156,28,140
s_lshl_b32 s65, s65, 18
Ta 22,7,25600
K 195,198,199
Mvaa 124,28,136
s_add_u32 s36, s29, 4096
Mvaa 92,28,132
buffer_load_dwordx4 a[48:51], v26, s[8:11], s28 offen offset:0 sc1
Ta 24,8,24576
Mvaa 60,28,128
s_waitcnt vmcnt(16) lgkmcnt(9)
Ta 26,8,25600
Mvaa 32,0,144
Mvaa 64,0,148
Mvaa 96,0,152
buffer_load_dwordx4 a[52:55], v26, s[8:11], s93 offen offset:0 sc1
Ta 28,9,24576
Mvaa 128,0,156
s_add_u32 m0, s63, 98304
Ta 30,9,25600
Mvaa 132,4,156
s_lshl_b32 s80, s56, 9
Mvaa 100,4,152
Mvaa 68,4,148
s_lshl_b32 s81, s57, 4
buffer_load_dwordx4 a[56:59], v27, s[8:11], s28 offen offset:0 sc1
Ta 0,10,0
Mvaa 36,4,144
W 9
s_add_u32 s80, s80, s81
Ta 2,10,1024
Mvaa 40,8,144
buffer_store_dwordx4 v[192:195], v1, s[8:11], s36 offen offset:0 sc1
K 200,200,201
Mvaa 72,8,148
s_and_b32 s81, s14, 1
Mvaa 104,8,152
s_lshl_b32 s81, s81, 3
Ta 4,11,0
K 201,202,203
Mvaa 136,8,156
Ta 6,11,1024
Mvaa 140,12,156
s_add_u32 s80, s80, s81
buffer_load_dwordx4 a[60:63], v27, s[8:11], s93 offen offset:0 sc1
K 202,204,205
Mvaa 108,12,152
s_add_u32 s28, s28, 512
s_cmp_eq_u32 s28, s4
s_cselect_b32 s93, 10240, 0
s_sub_u32 s28, s28, s93
Mvaa 76,12,148
s_add_u32 s93, s28, 20480
s_add_u32 s71, s80, 4096
Ta 8,12,0
Mvaa 44,12,144
W 9
Ta 10,12,1024
K 203,206,207
Mvaa 48,16,144
buffer_load_dwordx4 v[184:187], v26, s[8:11], s28 offen offset:0 sc1
Mvaa 80,16,148
Mvaa 112,16,152
Ta 12,13,0
Ta 14,13,1024
Mvaa 144,16,156
Mvaa 148,20,156
buffer_load_dwordx4 v[188:191], v26, s[8:11], s93 offen offset:0 sc1
Mvaa 116,20,152
Mvaa 84,20,148
Ta 16,14,0
Ta 18,14,1024
Mvaa 52,20,144
W 10
Mvaa 56,24,144
buffer_load_dwordx4 a[128:131], v27, s[8:11], s28 offen offset:0 sc1
Mvaa 88,24,148
Mvaa 120,24,152
Ta 20,15,0
Ta 22,15,1024
Mvaa 152,24,156
buffer_store_dwordx4 v[200:203], v1, s[8:11], s36 offen offset:1024 sc1
K 208,208,209
Mvaa 156,28,156
K 209,210,211
Mvaa 124,28,152
Mvaa 92,28,148
Ta 24,24,0
K 210,212,213
Mvaa 60,28,144
s_waitcnt vmcnt(14) lgkmcnt(9)
buffer_load_dwordx4 a[132:135], v27, s[8:11], s93 offen offset:0 sc1
Ta 26,24,1024
Mvaa 32,0,32
s_add_u32 s28, s28, 512
s_cmp_eq_u32 s28, s4
s_cselect_b32 s93, 10240, 0
s_sub_u32 s28, s28, s93
Mvaa 64,0,36
s_add_u32 s93, s28, 20480
K 211,214,215
Mvaa 96,0,40
Ta 28,25,0
Ta 30,25,1024
Mvaa 128,0,44
buffer_load_dwordx4 a[136:139], v26, s[8:11], s28 offen offset:0 sc1
Mvaa 132,4,44
Mvaa 100,4,40
Mvaa 68,4,36
Ta 0,10,8192
Ta 2,10,9216
Mvaa 36,4,32
W 10
buffer_load_dwordx4 a[140:143], v26, s[8:11], s93 offen offset:0 sc1
v_add_u32 v2, 65536, v10
Mvaa 40,8,32
Mvaa 72,8,36
Mvaa 104,8,40
Ta 4,11,8192
Ta 6,11,9216
Mvaa 136,8,44
buffer_load_dwordx4 v[192:195], v27, s[8:11], s28 offen offset:0 sc1
v_add_u32 v3, 65536, v11
Mvaa 140,12,44
Mvaa 108,12,40
buffer_store_dwordx4 v[208:211], v1, s[8:11], s36 offen offset:2048 sc1
K 216,216,217
Mvaa 76,12,36
Ta 8,12,8192
Ta 10,12,9216
Mvaa 44,12,32
W 10
v_add_u32 v4, 65536, v12
K 217,218,219
Mvaa 48,16,32
Mvaa 80,16,36
buffer_load_dwordx4 v[196:199], v27, s[8:11], s93 offen offset:0 sc1
K 218,220,221
Mvaa 112,16,40
s_add_u32 s28, s28, 512
s_cmp_eq_u32 s28, s4
s_cselect_b32 s93, 10240, 0
s_sub_u32 s28, s28, s93
Mvaa 144,16,44
s_add_u32 s93, s28, 20480
Ta 12,13,8192
Ta 14,13,9216
Mvaa 148,20,44
v_add_u32 v5, 65536, v13
K 219,222,223
Mvaa 116,20,40
D 16,48,65,0
Mvaa 84,20,36
Ta 16,14,8192
Ta 18,14,9216
Mvaa 52,20,32
W 10
v_add_u32 v6, 65536, v14
Mvaa 56,24,32
Mvaa 88,24,36
D 17,48,65,1024
Mvaa 120,24,40
Ta 20,15,8192
Ta 22,15,9216
Mvaa 152,24,44
v_add_u32 v7, 65536, v15
Mvaa 156,28,44
Mvaa 124,28,40
D 18,48,65,2048
Mvaa 92,28,36
Ta 24,24,8192
)"
R"(Ta 26,24,9216
Mvaa 60,28,32
s_waitcnt vmcnt(13) lgkmcnt(10)
v_add_u32 v8, 65536, v24
Mvaa 32,0,48
buffer_store_dwordx4 v[216:219], v1, s[8:11], s36 offen offset:3072 sc1
K 224,224,225
Mvaa 64,0,52
K 225,226,227
Mvaa 96,0,56
Ta 28,25,8192
Ta 30,25,9216
Mvaa 128,0,60
v_add_u32 v9, 65536, v25
K 226,228,229
Mvaa 132,4,60
buffer_load_dwordx4 v[200:203], v26, s[8:11], s28 offen offset:0 sc1
K 227,230,231
Mvaa 100,4,56
Mvaa 68,4,52
s_add_u32 s36, s29, 458752
Ta 0,10,16384
Ta 2,10,17408
Mvaa 36,4,48
W 10
Mvaa 40,8,48
buffer_load_dwordx4 v[204:207], v26, s[8:11], s93 offen offset:0 sc1
Mvaa 72,8,52
Mvaa 104,8,56
Ta 4,11,16384
Ta 6,11,17408
Mvaa 136,8,60
Mvaa 140,12,60
buffer_load_dwordx4 a[144:147], v27, s[8:11], s28 offen offset:0 sc1
Mvaa 108,12,56
Mvaa 76,12,52
Ta 8,12,16384
Ta 10,12,17408
Mvaa 44,12,48
W 10
Mvaa 48,16,48
buffer_load_dwordx4 a[148:151], v27, s[8:11], s93 offen offset:0 sc1
Mvaa 80,16,52
s_add_u32 s28, s28, 512
s_cmp_eq_u32 s28, s4
s_cselect_b32 s93, 10240, 0
s_sub_u32 s28, s28, s93
Mvaa 112,16,56
s_add_u32 s93, s28, 20480
buffer_store_dwordx4 v[224:227], v1, s[8:11], s36 offen offset:0 sc1
K 232,232,233
Mvaa 144,16,60
Ta 12,13,16384
Ta 14,13,17408
Mvaa 148,20,60
K 233,234,235
Mvaa 116,20,56
Mvaa 84,20,52
D 19,48,65,3072
K 234,236,237
Mvaa 52,20,48
W 8
s_add_u32 m0, s63, 102400
Ta 16,14,16384
Mvaa 56,24,48
Ta 18,14,17408
Mvaa 88,24,52
K 235,238,239
Mvaa 120,24,56
buffer_load_dwordx4 a[152:155], v26, s[8:11], s28 offen offset:0 sc1
Ta 20,15,16384
Mvaa 152,24,60
Ta 22,15,17408
Mvaa 156,28,60
Mvaa 124,28,56
Mvaa 92,28,52
buffer_load_dwordx4 a[156:159], v26, s[8:11], s93 offen offset:0 sc1
Ta 24,24,16384
Mvaa 60,28,48
s_waitcnt vmcnt(17) lgkmcnt(9)
Ta 26,24,17408
Mvav 32,0,184
Mvav 64,0,188
Mvaa 96,0,128
D 20,48,65,0
Ta 28,25,16384
Mvaa 128,0,132
Ta 30,25,17408
Mvaa 132,4,132
Mvaa 100,4,128
buffer_store_dwordx4 v[232:235], v1, s[8:11], s36 offen offset:1024 sc1
K 240,240,241
Mvav 68,4,188
Ta 0,10,24576
K 241,242,243
Mvav 36,4,184
W 9
Ta 2,10,25600
Mvav 40,8,184
K 242,244,245
Mvav 72,8,188
buffer_load_dwordx4 v[208:211], v27, s[8:11], s28 offen offset:0 sc1
Mvaa 104,8,128
Ta 4,11,24576
Ta 6,11,25600
Mvaa 136,8,132
K 243,246,247
Mvaa 140,12,132
Mvaa 108,12,128
D 21,48,65,1024
Mvav 76,12,188
Ta 8,12,24576
Ta 10,12,25600
Mvav 44,12,184
W 10
Mvav 48,16,184
Mvav 80,16,188
buffer_load_dwordx4 v[212:215], v27, s[8:11], s93 offen offset:0 sc1
Mvaa 112,16,128
s_add_u32 s28, s28, 512
s_cmp_eq_u32 s28, s4
s_cselect_b32 s93, 10240, 0
s_sub_u32 s28, s28, s93
Mvaa 144,16,132
s_add_u32 s93, s28, 20480
Ta 12,13,24576
Ta 14,13,25600
Mvaa 148,20,132
Mvaa 116,20,128
D 22,48,65,2048
Mvav 84,20,188
Ta 16,14,24576
Ta 18,14,25600
Mvav 52,20,184
buffer_store_dwordx4 v[240:243], v1, s[8:11], s36 offen offset:2048 sc1
W 10
Mvav 56,24,184
K 248,248,249
Mvav 88,24,188
K 249,250,251
Mvaa 120,24,128
Ta 20,15,24576
Ta 22,15,25600
Mvaa 152,24,132
buffer_load_dwordx4 a[32:35], v26, s[8:11], s28 offen offset:0 sc1
K 250,252,253
Mvaa 156,28,132
Mvaa 124,28,128
Mvav 92,28,188
Ta 24,24,24576
K 251,254,255
Mvav 60,28,184
s_waitcnt vmcnt(20) lgkmcnt(9)
D 23,48,65,3072
Ta 26,24,25600
Mvaa 32,0,136
Mvaa 64,0,140
Mvav 96,0,192
Ta 28,25,24576
Ta 30,25,25600
Mvav 128,0,196
buffer_load_dwordx4 a[36:39], v26, s[8:11], s93 offen offset:0 sc1
Mvav 132,4,196
Mvav 100,4,192
Mvaa 68,4,140
Ta 0,2,0
Ta 2,2,1024
Mvaa 36,4,136
W 10
buffer_load_dwordx4 a[40:43], v27, s[8:11], s28 offen offset:0 sc1
Mvaa 40,8,136
Mvaa 72,8,140
Mvav 104,8,192
buffer_store_dwordx4 v[248:251], v1, s[8:11], s36 offen offset:3072 sc1
G 160,160
Mvav 136,8,196
G 161,161
G 162,162
Mvav 140,12,196
Ta 4,3,0
Ta 6,3,1024
Mvav 108,12,192
G 163,163
G 164,164
Mvaa 76,12,140
buffer_load_dwordx4 a[44:47], v27, s[8:11], s93 offen offset:0 sc1
G 165,165
Mvaa 44,12,136
W 8
s_add_u32 s28, s28, 512
s_cmp_eq_u32 s28, s4
s_cselect_b32 s93, 10240, 0
Mvaa 48,16,136
s_sub_u32 s28, s28, s93
s_add_u32 s93, s28, 20480
G 166,166
Mvaa 80,16,140
Ta 8,4,0
Ta 10,4,1024
Mvav 112,16,192
buffer_load_dwordx4 v[216:219], v26, s[8:11], s28 offen offset:0 sc1
G 167,167
Mvav 144,16,196
K 160,160,161
K 161,162,163
Mvav 148,20,196
K 162,164,165
K 163,166,167
Mvav 116,20,192
s_add_u32 s36, s29, 462848
Ta 12,5,0
Ta 14,5,1024
Mvaa 84,20,140
buffer_load_dwordx4 v[220:223], v26, s[8:11], s93 offen offset:0 sc1
Ta 16,6,0
Mvaa 52,20,136
W 9
Ta 18,6,1024
Mvaa 56,24,136
Mvaa 88,24,140
Mvav 120,24,192
buffer_load_dwordx4 v[224:227], v27, s[8:11], s28 offen offset:0 sc1
Ta 20,7,0
Mvav 152,24,196
Ta 22,7,1024
Mvav 156,28,196
buffer_store_dwordx4 v[160:163], v1, s[8:11], s36 offen offset:0 sc1
G 168,168
Mvav 124,28,192
G 169,169
G 170,170
Mvaa 92,28,140
Ta 24,8,0
G 171,171
Mvaa 60,28,136
s_waitcnt vmcnt(21) lgkmcnt(9)
Ta 26,8,1024
G 172,172
Mvav 32,0,200
buffer_load_dwordx4 v[228:231], v27, s[8:11], s93 offen offset:0 sc1
G 173,173
Mvav 64,0,204
s_add_u32 s28, s28, 512
s_cmp_eq_u32 s28, s4
s_cselect_b32 s93, 10240, 0
s_sub_u32 s28, s28, s93
Mvaa 96,0,144
s_add_u32 s93, s28, 20480
G 174,174
G 175,175
Mvaa 128,0,148
Ta 28,9,0
Ta 30,9,1024
Mvaa 132,4,148
buffer_load_dwordx4 a[48:51], v26, s[8:11], s28 offen offset:0 sc1
K 168,168,169
Mvaa 100,4,144
K 169,170,171
K 170,172,173
Mvav 68,4,204
Ta 0,2,8192
K 171,174,175
Mvav 36,4,200
W 9
Ta 2,2,9216
Mvav 40,8,200
buffer_load_dwordx4 a[52:55], v26, s[8:11], s93 offen offset:0 sc1
Mvav 72,8,204
Mvaa 104,8,144
Ta 4,3,8192
Ta 6,3,9216
Mvaa 136,8,148
Mvaa 140,12,148
buffer_load_dwordx4 a[56:59], v27, s[8:11], s28 offen offset:0 sc1
Mvaa 108,12,144
Mvav 76,12,204
Ta 8,4,8192
Ta 10,4,9216
Mvav 44,12,200
buffer_store_dwordx4 v[168:171], v1, s[8:11], s36 offen offset:1024 sc1
W 10
Mvav 48,16,200
G 176,176
G 177,177
Mvav 80,16,204
G 178,178
G 179,179
Mvaa 112,16,144
G 180,180
G 181,181
Mvaa 144,16,148
buffer_load_dwordx4 a[60:63], v27, s[8:11], s93 offen offset:0 sc1
Ta 12,5,8192
Mvaa 148,20,148
s_add_u32 s28, s28, 512
s_cmp_eq_u32 s28, s4
s_cselect_b32 s93, 10240, 0
s_sub_u32 s28, s28, s93
Mvaa 116,20,144
s_add_u32 s93, s28, 20480
Ta 14,5,9216
v_lshrrev_b32 v20, 4, v1
Mvav 84,20,204
G 182,182
G 183,183
Mvav 52,20,200
W 8
buffer_load_dwordx4 v[232:235], v26, s[8:11], s28 offen offset:0 sc1
v_add_u32 v20, 0, v20
Mvav 56,24,200
K 176,176,177
K 177,178,179
Mvav 88,24,204
Ta 16,6,8192
v_mul_u32_u24 v22, 205, v20
Mvaa 120,24,144
Ta 18,6,9216
K 178,180,181
Mvaa 152,24,148
buffer_load_dwordx4 v[236:239], v26, s[8:11], s93 offen offset:0 sc1
K 179,182,183
Mvaa 156,28,148
v_lshrrev_b32 v22, 12, v22
v_mul_u32_u24 v23, 20, v22
Mvaa 124,28,144
Ta 20,7,8192
Ta 22,7,9216
Mvav 92,28,204
v_sub_u32 v20, v20, v23
v_lshrrev_b32 v23, 1, v22
Mvav 60,28,200
s_waitcnt vmcnt(20) lgkmcnt(8)
buffer_load_dwordx4 v[184:187], v27, s[8:11], s28 offen offset:0 sc1
Ta 24,8,8192
Mvaa 32,0,152
Ta 26,8,9216
v_and_b32 v23, 2, v23
Mvaa 64,0,156
buffer_store_dwordx4 v[176:179], v1, s[8:11], s36 offen offset:2048 sc1
G 160,184
Mvav 96,0,208
G 161,185
v_xor_b32 v20, v20, v23
Mvav 128,0,212
Ta 28,9,8192
G 162,186
Mvav 132,4,212
Ta 30,9,9216
G 163,187
Mvav 100,4,208
buffer_load_dwordx4 v[188:191], v27, s[8:11], s93 offen offset:0 sc1
v_mul_u32_u24 v22, 80, v22
Mvaa 68,4,156
s_add_u32 s28, s28, 512
s_cmp_eq_u32 s28, s4
s_cselect_b32 s93, 10240, 0
s_sub_u32 s28, s28, s93
Mvaa 36,4,152
W 8
s_add_u32 s93, s28, 20480
G 164,188
Mvaa 40,8,152
G 165,189
v_add_lshl_u32 v16, v20, v22, 4
Mvaa 72,8,156
buffer_load_dwordx4 a[128:131], v26, s[8:11], s28 offen offset:0 sc1
G 166,190
Mvav 104,8,208
Ta 0,2,16384
Ta 2,2,17408
Mvav 136,8,212
G 167,191
v_lshrrev_b32 v20, 4, v1
Mvav 140,12,212
K 160,160,161
K 161,162,163
Mvav 108,12,208
buffer_load_dwordx4 a[132:135], v26, s[8:11], s93 offen offset:0 sc1
v_add_u32 v20, 64, v20
Mvaa 76,12,156
Ta 4,3,16384
K 162,164,165
Mvaa 44,12,152
W 7
Ta 6,3,17408
K 163,166,167
Mvaa 48,16,152
v_mul_u32_u24 v22, 205, v20
v_lshrrev_b32 v22, 12, v22
Mvaa 80,16,156
buffer_store_dwordx4 v[160:163], v1, s[8:11], s36 offen offset:3072 sc1
Ta 8,4,16384
Mvav 112,16,208
Ta 10,4,17408
v_mul_u32_u24 v23, 20, v22
Mvav 144,16,212
Ta 12,5,16384
v_sub_u32 v20, v20, v23
Mvav 148,20,212
Ta 14,5,17408
)"
R"(G 168,192
Mvav 116,20,208
buffer_load_dwordx4 a[136:139], v27, s[8:11], s28 offen offset:0 sc1
v_lshrrev_b32 v23, 1, v22
Mvaa 84,20,156
G 169,193
G 170,194
Mvaa 52,20,152
W 8
v_and_b32 v23, 2, v23
G 171,195
Mvaa 56,24,152
G 172,196
v_xor_b32 v20, v20, v23
Mvaa 88,24,156
Ta 16,6,16384
Ta 18,6,17408
Mvav 120,24,208
G 173,197
G 174,198
Mvav 152,24,212
buffer_load_dwordx4 a[140:143], v27, s[8:11], s93 offen offset:0 sc1
v_mul_u32_u24 v22, 80, v22
Mvav 156,28,212
G 175,199
K 168,168,169
Mvav 124,28,208
v_add_lshl_u32 v17, v20, v22, 4
K 169,170,171
Mvaa 92,28,156
Ta 20,7,16384
Ta 22,7,17408
Mvaa 60,28,152
s_waitcnt vmcnt(20) lgkmcnt(8)
K 170,172,173
K 171,174,175
Mvaa 32,0,32
s_add_u32 s36, s29, 65536
buffer_store_dwordx4 v[168:171], v1, s[8:11], s36 offen offset:0 sc1
v_subrev_u32 v17, 1024, v17
Mvaa 64,0,36
s_add_u32 s28, s28, 512
Ta 24,8,16384
v_lshrrev_b32 v20, 4, v1
Mvaa 96,0,40
s_cmp_eq_u32 s28, s4
s_cselect_b32 s93, 10240, 0
Ta 26,8,17408
v_add_u32 v20, 128, v20
Mvaa 128,0,44
s_sub_u32 s28, s28, s93
s_add_u32 s93, s28, 20480
v_mul_u32_u24 v22, 205, v20
Mvaa 132,4,44
buffer_load_dwordx4 v[192:195], v26, s[8:11], s28 offen offset:0 sc1
Ta 28,9,16384
Mvaa 100,4,40
Ta 30,9,17408
v_lshrrev_b32 v22, 12, v22
Mvaa 68,4,36
G 176,200
v_mul_u32_u24 v23, 20, v22
Mvaa 36,4,32
W 8
Ta 0,2,24576
G 177,201
Mvaa 40,8,32
Ta 2,2,25600
G 178,202
Mvaa 72,8,36
v_sub_u32 v20, v20, v23
G 179,203
Mvaa 104,8,40
buffer_load_dwordx4 v[196:199], v26, s[8:11], s93 offen offset:0 sc1
G 180,204
Mvaa 136,8,44
v_lshrrev_b32 v23, 1, v22
G 181,205
Mvaa 140,12,44
Ta 4,3,24576
Ta 6,3,25600
Mvaa 108,12,40
G 182,206
v_and_b32 v23, 2, v23
Mvaa 76,12,36
G 183,207
K 176,176,177
Mvaa 44,12,32
W 8
v_xor_b32 v20, v20, v23
K 177,178,179
Mvaa 48,16,32
Ta 8,4,24576
Ta 10,4,25600
Mvaa 80,16,36
K 178,180,181
v_mul_u32_u24 v22, 80, v22
Mvaa 112,16,40
buffer_load_dwordx4 v[200:203], v27, s[8:11], s28 offen offset:0 sc1
K 179,182,183
Mvaa 144,16,44
Ta 12,5,24576
v_add_lshl_u32 v18, v20, v22, 4
Mvaa 148,20,44
Ta 14,5,25600
v_subrev_u32 v18, 2048, v18
Mvaa 116,20,40
v_lshrrev_b32 v20, 4, v1
Mvaa 84,20,36
buffer_store_dwordx4 v[176:179], v1, s[8:11], s36 offen offset:1024 sc1
Ta 16,6,24576
Mvaa 52,20,32
W 9
Ta 18,6,25600
v_add_u32 v20, 192, v20
Mvaa 56,24,32
v_mul_u32_u24 v22, 205, v20
G 160,208
Mvaa 88,24,36
G 161,209
Mvaa 120,24,40
buffer_load_dwordx4 v[204:207], v27, s[8:11], s93 offen offset:0 sc1
v_lshrrev_b32 v22, 12, v22
Mvaa 152,24,44
Ta 20,7,24576
G 162,210
Mvaa 156,28,44
Ta 22,7,25600
G 163,211
Mvaa 124,28,40
G 164,212
v_mul_u32_u24 v23, 20, v22
Mvaa 92,28,36
G 165,213
G 166,214
Mvaa 60,28,32
s_waitcnt vmcnt(21) lgkmcnt(8)
Ta 24,8,24576
v_sub_u32 v20, v20, v23
Mvav 32,0,216
s_add_u32 s28, s28, 512
Ta 26,8,25600
G 167,215
Mvav 64,0,220
s_cmp_eq_u32 s28, s4
s_cselect_b32 s93, 10240, 0
K 160,160,161
v_lshrrev_b32 v23, 1, v22
Mvav 96,0,224
s_sub_u32 s28, s28, s93
s_add_u32 s93, s28, 20480
buffer_load_dwordx4 a[144:147], v26, s[8:11], s28 offen offset:0 sc1
Mvav 128,0,228
K 161,162,163
K 162,164,165
Mvav 132,4,228
Ta 28,9,24576
v_and_b32 v23, 2, v23
Mvav 100,4,224
Ta 30,9,25600
K 163,166,167
Mvav 68,4,220
buffer_store_dwordx4 v[160:163], v1, s[8:11], s36 offen offset:2048 sc1
v_xor_b32 v20, v20, v23
Mvav 36,4,216
W 8
v_mul_u32_u24 v22, 80, v22
Mvav 40,8,216
v_add_lshl_u32 v19, v20, v22, 4
Mvav 72,8,220
v_subrev_u32 v19, 3072, v19
Mvav 104,8,224
buffer_load_dwordx4 a[148:151], v26, s[8:11], s93 offen offset:0 sc1
Mvav 136,8,228
v_lshrrev_b32 v20, 4, v1
G 168,216
Mvav 140,12,228
W 0
s_barrier
G 169,217
Mvav 108,12,224
Ta 0,10,32768
Ta 2,10,33792
Mvav 76,12,220
Ta 4,11,32768
Ta 6,11,33792
Mvav 44,12,216
v_add_u32 v20, 256, v20
G 170,218
Mvav 48,16,216
Ta 8,12,32768
G 171,219
Mvav 80,16,220
Ta 10,12,33792
v_mul_u32_u24 v22, 205, v20
Mvav 112,16,224
G 172,220
G 173,221
Mvav 144,16,228
buffer_load_dwordx4 a[184:187], v27, s[8:11], s28 offen offset:0 sc1
v_lshrrev_b32 v22, 12, v22
Mvav 148,20,228
G 174,222
G 175,223
Mvav 116,20,224
Ta 12,13,32768
Ta 14,13,33792
Mvav 84,20,220
K 168,168,169
v_mul_u32_u24 v23, 20, v22
Mvav 52,20,216
K 169,170,171
K 170,172,173
Mvav 56,24,216
v_sub_u32 v20, v20, v23
K 171,174,175
Mvav 88,24,220
Ta 16,14,32768
Ta 18,14,33792
Mvav 120,24,224
buffer_store_dwordx4 v[168:171], v1, s[8:11], s36 offen offset:3072 sc1
v_lshrrev_b32 v23, 1, v22
Mvav 152,24,228
v_and_b32 v23, 2, v23
v_xor_b32 v20, v20, v23
Mvav 156,28,228
Ta 20,15,32768
Ta 22,15,33792
Mvav 124,28,224
v_mul_u32_u24 v22, 80, v22
G 176,224
Mvav 92,28,220
buffer_load_dwordx4 a[188:191], v27, s[8:11], s93 offen offset:0 sc1
v_add_lshl_u32 v20, v20, v22, 4
Mvav 60,28,216
s_waitcnt vmcnt(14)
v_mov_b64 v[28:29], s[12:13]
s_mov_b64 exec, 1
buffer_store_dwordx2 v[28:29], off, s[8:11], s71 sc1
s_mov_b64 exec, -1
G 177,225
W 8
Mvaa 32,0,48
s_add_u32 s28, s28, 512
Ta 24,24,32768
Ta 26,24,33792
Mvaa 64,0,52
s_cmp_eq_u32 s28, s4
s_cselect_b32 s93, 10240, 0
G 178,226
G 179,227
Mvaa 96,0,56
s_sub_u32 s28, s28, s93
s_add_u32 s93, s28, 20480
v_lshrrev_b32 v22, 6, v1
Mvaa 128,0,60
buffer_load_dwordx4 a[152:155], v26, s[8:11], s28 offen offset:0 sc1
G 180,228
Mvaa 132,4,60
G 181,229
v_mul_u32_u24 v22, 320, v22
Mvaa 100,4,56
Ta 28,25,32768
Ta 30,25,33792
Mvaa 68,4,52
G 182,230
G 183,231
Mvaa 36,4,48
W 8
v_bfe_u32 v23, v1, 4, 2
K 176,176,177
Mvaa 40,8,48
K 177,178,179
v_lshl_add_u32 v22, v23, 3, v22
Mvaa 72,8,52
Ta 0,10,40960
K 178,180,181
Mvaa 104,8,56
Ta 2,10,41984
K 179,182,183
Mvaa 136,8,60
s_add_u32 s36, s29, 69632
buffer_load_dwordx4 a[156:159], v26, s[8:11], s93 offen offset:0 sc1
v_bfe_u32 v23, v1, 8, 1
Mvaa 140,12,60
Ta 4,11,40960
v_lshlrev_b32 v23, 5, v23
Mvaa 108,12,56
Ta 6,11,41984
v_add_u32 v21, v22, v23
Mvaa 76,12,52
Ta 8,12,40960
v_sub_u32 v22, v22, v23
Mvaa 44,12,48
W 9
buffer_store_dwordx4 v[176:179], v1, s[8:11], s36 offen offset:0 sc1
Ta 10,12,41984
Mvaa 48,16,48
s_mul_i32 s94, s59, 655360
G 160,232
Mvaa 80,16,52
s_mul_i32 s95, s58, 320
G 161,233
Mvaa 112,16,56
Ta 12,13,40960
G 162,234
Mvaa 144,16,60
s_add_u32 s94, s94, s95
buffer_load_dwordx4 v[208:211], v27, s[8:11], s28 offen offset:0 sc1
Ta 14,13,41984
Mvaa 148,20,60
G 163,235
G 164,236
Mvaa 116,20,56
s_add_u32 s40, s26, s94
s_addc_u32 s41, s27, 0
G 165,237
Mvaa 84,20,52
Ta 16,14,40960
G 166,238
Mvaa 52,20,48
W 9
Ta 18,14,41984
G 167,239
Mvaa 56,24,48
buffer_load_dwordx4 v[212:215], v27, s[8:11], s93 offen offset:0 sc1
K 160,160,161
Mvaa 88,24,52
s_and_b32 s41, s41, 0xffff
K 161,162,163
Mvaa 120,24,56
Ta 20,15,40960
K 162,164,165
Mvaa 152,24,60
s_mov_b32 s42, 5242880
Ta 22,15,41984
K 163,166,167
Mvaa 156,28,60
s_mov_b32 s43, 0x20000
buffer_store_dwordx4 v[160:163], v1, s[8:11], s36 offen offset:1024 sc1
Mvaa 124,28,56
Mvaa 92,28,52
s_sub_u32 s42, s42, s94
Ta 24,24,40960
Ta 26,24,41984
Mvaa 60,28,48
W 10
Mvav 32,0,232
s_mov_b64 s[44:45], s[8:9]
s_add_u32 s28, s28, 512
s_cmp_eq_u32 s28, s4
s_cselect_b32 s93, 10240, 0
Mvav 64,0,236
s_sub_u32 s28, s28, s93
s_add_u32 s93, s28, 20480
buffer_load_dwordx4 a[192:195], v26, s[8:11], s28 offen offset:0 sc1
Mvav 96,0,184
s_mov_b32 s47, 0x20000
Ta 28,25,40960
Ta 30,25,41984
Mvav 128,0,188
G 168,240
Mvav 132,4,188
s_and_b32 s48, s14, 1
G 169,241
Mvav 100,4,184
G 170,242
Mvav 68,4,236
s_mul_i32 s48, s48, 5120
Ta 0,10,49152
G 171,243
Mvav 36,4,232
W 9
Ta 2,10,50176
G 172,244
Mvav 40,8,232
s_lshr_b32 s51, s14, 1
buffer_load_dwordx4 a[196:199], v26, s[8:11], s93 offen offset:0 sc1
G 173,245
Mvav 72,8,236
G 174,246
Mvav 104,8,184
s_and_b32 s94, s14, 1
Ta 4,11,49152
G 175,247
Mvav 136,8,188
Ta 6,11,50176
K 168,168,169
Mvav 140,12,188
s_lshl_b32 s95, s57, 1
K 169,170,171
Mvav 108,12,184
s_add_u32 s94, s94, s95
K 170,172,173
Mvav 76,12,236
Ta 8,12,49152
K 171,174,175
Mvav 44,12,232
W 9
Ta 10,12,50176
)"
R"(buffer_store_dwordx4 v[168:171], v1, s[8:11], s36 offen offset:2048 sc1
Mvav 48,16,232
s_lshl_b32 s54, s94, 3
Mvav 80,16,236
s_add_u32 s54, s54, s59
Mvav 112,16,184
Ta 12,13,49152
Ta 14,13,50176
Mvav 144,16,188
s_lshl_b32 s54, s54, 6
buffer_load_dwordx4 a[200:203], v27, s[8:11], s28 offen offset:0 sc1
Mvav 148,20,188
Mvav 116,20,184
s_xor_b32 s55, s54, 15872
Mvav 84,20,236
Ta 16,14,49152
Ta 18,14,50176
Mvav 52,20,232
W 10
s_add_u32 s54, s54, 20480
G 176,248
Mvav 56,24,232
s_and_b32 s81, s14, 1
G 177,249
G 178,250
Mvav 88,24,236
s_add_u32 s55, s55, 20480
buffer_load_dwordx4 a[204:207], v27, s[8:11], s93 offen offset:0 sc1
G 179,251
Mvav 120,24,184
Ta 20,15,49152
G 180,252
Mvav 152,24,188
s_and_b32 s95, s14, 2
s_mul_i32 s91, s81, 17164288
Ta 22,15,50176
Mvav 156,28,188
G 181,253
G 182,254
Mvav 124,28,184
s_lshl_b32 s94, s94, 2
s_mul_i32 s82, s56, 536576
G 183,255
Mvav 92,28,236
s_add_u32 s94, s94, s95
Ta 24,24,49152
K 176,176,177
Mvav 60,28,232
s_waitcnt vmcnt(24) lgkmcnt(9)
Ta 26,24,50176
K 177,178,179
Mvaa 32,0,128
s_add_u32 s91, s91, s82
s_lshl_b32 s95, s95, 4
K 178,180,181
Mvaa 64,0,132
s_add_u32 s28, s28, 512
s_cmp_eq_u32 s28, s4
s_cselect_b32 s93, 10240, 0
s_sub_u32 s28, s28, s93
Mvaa 96,0,136
s_add_u32 s93, s28, 20480
buffer_load_dwordx4 a[32:35], v26, s[8:11], s28 offen offset:0 sc1
K 179,182,183
Mvaa 128,0,140
s_add_u32 s55, s55, s95
Ta 28,25,49152
Ta 30,25,50176
Mvaa 132,4,140
s_add_u32 s91, s91, 4194304
s_lshl_b32 s49, s94, 17
Mvaa 100,4,136
s_lshr_b32 s82, s14, 1
Mvaa 68,4,132
s_lshl_b32 s95, s59, 14
buffer_store_dwordx4 v[176:179], v1, s[8:11], s36 offen offset:3072 sc1
Ta 0,10,57344
Mvaa 36,4,128
W 9
s_lshl_b32 s83, s82, 14
Ta 2,10,58368
Mvaa 40,8,128
s_add_u32 s49, s49, s95
G 160,64
Mvaa 72,8,132
G 161,65
Mvaa 104,8,136
s_xor_b32 s50, s49, 16252928
s_add_u32 s91, s91, s83
buffer_load_dwordx4 a[36:39], v26, s[8:11], s93 offen offset:0 sc1
Mvaa 136,8,140
Ta 4,11,57344
G 162,66
Mvaa 140,12,140
s_add_u32 s49, s49, 39845888
Ta 6,11,58368
G 163,67
Mvaa 108,12,136
s_lshl_b32 s81, s81, 14
G 164,68
G 165,69
Mvaa 76,12,132
s_add_u32 s50, s50, 39845888
Ta 8,12,57344
G 166,70
Mvaa 44,12,128
W 9
Ta 10,12,58368
G 167,71
Mvaa 48,16,128
s_lshl_b32 s82, s82, 16
s_lshr_b32 s94, s15, 3
K 160,160,161
Mvaa 80,16,132
s_lshl_b32 s94, s94, 3
buffer_load_dwordx4 a[40:43], v27, s[8:11], s28 offen offset:0 sc1
K 161,162,163
Mvaa 112,16,136
s_or_b32 s92, s81, s82
K 162,164,165
K 163,166,167
Mvaa 144,16,140
s_add_u32 s36, s29, 393216
Ta 12,13,57344
Ta 14,13,58368
Mvaa 148,20,140
s_add_u32 s94, s94, s14
s_xor_b32 s92, s92, 0x10000
Mvaa 116,20,136
s_lshl_b32 s94, s94, 4
buffer_store_dwordx4 v[160:163], v1, s[8:11], s36 offen offset:0 sc1
Mvaa 84,20,132
Ta 16,14,57344
Ta 18,14,58368
Mvaa 52,20,128
W 10
s_add_u32 s94, s94, s59
Mvaa 56,24,128
Mvaa 88,24,132
s_mul_i32 s52, s94, 5120
buffer_load_dwordx4 a[44:47], v27, s[8:11], s93 offen offset:0 sc1
Mvaa 120,24,136
Ta 20,15,57344
Ta 22,15,58368
Mvaa 152,24,140
s_add_u32 s52, s52, 56623104
G 168,72
Mvaa 156,28,140
G 169,73
Mvaa 124,28,136
s_lshl_b32 s53, s94, 3
G 170,74
Mvaa 92,28,132
Ta 24,24,57344
G 171,75
Mvaa 60,28,128
s_waitcnt vmcnt(24) lgkmcnt(9)
s_add_u32 s53, s53, 65536
Ta 26,24,58368
Mvav 32,0,192
G 172,76
G 173,77
Mvav 64,0,196
v_bfe_u32 v244, v1, 4, 1
G 174,78
Mvav 96,0,200
G 175,79
v_mul_u32_u24 v244, 0xaaaa, v244
Mvav 128,0,204
Ta 28,25,57344
Ta 30,25,58368
Mvav 132,4,204
K 168,168,169
v_add_u32 v244, 0x4444, v244
Mvav 100,4,200
K 169,170,171
K 170,172,173
Mvav 68,4,196
v_lshl_or_b32 v240, v244, 16, v244
K 171,174,175
Mvav 36,4,192
W 8
Ta 0,2,32768
Ta 2,2,33792
Mvav 40,8,192
buffer_store_dwordx4 v[168:171], v1, s[8:11], s36 offen offset:1024 sc1
v_mov_b32 v241, 0xff800000
Mvav 72,8,196
v_add_u32 v242, 0x10000, v1
Mvav 104,8,200
Ta 4,3,32768
v_bfe_u32 v244, v1, 4, 4
Mvav 136,8,204
Ta 6,3,33792
Mvav 140,12,204
v_lshrrev_b32 v245, 8, v1
Mvav 108,12,200
v_lshlrev_b32 v245, 2, v245
Mvav 76,12,196
Ta 8,4,32768
Ta 10,4,33792
Mvav 44,12,192
W 10
G 176,80
v_sub_u32 v244, v244, v245
Mvav 48,16,192
G 177,81
G 178,82
Mvav 80,16,196
v_cmp_le_i32_e64 s[72:73], 0, v244
G 179,83
Mvav 112,16,200
v_cmp_le_i32_e64 s[74:75], 1, v244
G 180,84
Mvav 144,16,204
Ta 12,5,32768
Ta 14,5,33792
Mvav 148,20,204
G 181,85
v_cmp_le_i32_e64 s[76:77], 2, v244
Mvav 116,20,200
G 182,86
G 183,87
Mvav 84,20,196
v_cmp_le_i32_e64 s[78:79], 3, v244
K 176,176,177
Mvav 52,20,192
W 8
Ta 16,6,32768
Ta 18,6,33792
Mvav 56,24,192
K 177,178,179
v_add_u32 v243, 4096, v1
Mvav 88,24,196
K 178,180,181
v_add_u32 v244, 8192, v1
Mvav 120,24,200
K 179,182,183
v_add_u32 v245, 12288, v1
Mvav 152,24,204
buffer_store_dwordx4 v[176:179], v1, s[8:11], s36 offen offset:2048 sc1
Ta 20,7,32768
Mvav 156,28,204
s_mov_b32 s18, 0x3f803f80
Ta 22,7,33792
Mvav 124,28,200
s_mov_b32 s19, 0x3f003f00
Mvav 92,28,196
Ta 24,8,32768
Ta 26,8,33792
Mvav 60,28,192
s_waitcnt vmcnt(20) lgkmcnt(10)
v_accvgpr_write_b32 a176, s18
Mvaa 32,0,144
Mvaa 64,0,148
v_accvgpr_write_b32 a180, s19
G 160,88
Mvaa 96,0,184
v_mov_b32 v252, v241
G 161,89
Mvaa 128,0,188
Ta 28,9,32768
Ta 30,9,33792
Mvaa 132,4,188
G 162,90
v_accvgpr_write_b32 a177, s18
Mvaa 100,4,184
G 163,91
G 164,92
Mvaa 68,4,148
v_accvgpr_write_b32 a181, s19
G 165,93
Mvaa 36,4,144
W 8
Ta 0,2,40960
Ta 2,2,41984
Mvaa 40,8,144
v_mov_b32 v253, v241
G 166,94
Mvaa 72,8,148
G 167,95
v_accvgpr_write_b32 a178, s18
Mvaa 104,8,184
K 160,160,161
K 161,162,163
Mvaa 136,8,188
Ta 4,3,40960
v_accvgpr_write_b32 a182, s19
Mvaa 140,12,188
Ta 6,3,41984
K 162,164,165
Mvaa 108,12,184
K 163,166,167
v_mov_b32 v254, v241
Mvaa 76,12,148
buffer_store_dwordx4 v[160:163], v1, s[8:11], s36 offen offset:3072 sc1
v_accvgpr_write_b32 a179, s18
Mvaa 44,12,144
W 8
Ta 8,4,40960
Ta 10,4,41984
Mvaa 48,16,144
v_accvgpr_write_b32 a183, s19
Mvaa 80,16,148
v_mov_b32 v255, v241
Mvaa 112,16,184
Ta 12,5,40960
v_accvgpr_write_b32 a160, 0
Mvaa 144,16,188
Ta 14,5,41984
G 168,96
Mvaa 148,20,188
v_accvgpr_write_b32 a161, 0
G 169,97
Mvaa 116,20,184
G 170,98
v_accvgpr_write_b32 a162, 0
Mvaa 84,20,148
Ta 16,6,40960
G 171,99
Mvaa 52,20,144
W 9
Ta 18,6,41984
G 172,100
Mvaa 56,24,144
v_accvgpr_write_b32 a163, 0
G 173,101
Mvaa 88,24,148
G 174,102
v_accvgpr_write_b32 a164, 0
Mvaa 120,24,184
G 175,103
v_accvgpr_write_b32 a165, 0
Mvaa 152,24,188
Ta 20,7,40960
K 168,168,169
Mvaa 156,28,188
Ta 22,7,41984
K 169,170,171
Mvaa 124,28,184
v_accvgpr_write_b32 a166, 0
K 170,172,173
Mvaa 92,28,148
K 171,174,175
v_accvgpr_write_b32 a167, 0
Mvaa 60,28,144
s_waitcnt vmcnt(15) lgkmcnt(8)
s_add_u32 s36, s29, 397312
Ta 24,8,40960
Mvaa 32,0,152
Ta 26,8,41984
buffer_store_dwordx4 v[168:171], v1, s[8:11], s36 offen offset:0 sc1
Mvaa 64,0,156
v_accvgpr_write_b32 a168, 0
v_accvgpr_write_b32 a169, 0
Mvav 96,0,208
Ta 28,9,40960
v_accvgpr_write_b32 a170, 0
Mvav 128,0,212
Ta 30,9,41984
Mvav 132,4,212
v_accvgpr_write_b32 a171, 0
Mvav 100,4,208
v_accvgpr_write_b32 a172, 0
Mvaa 68,4,156
Ta 0,2,49152
Ta 2,2,50176
Mvaa 36,4,152
W 10
G 176,104
v_accvgpr_write_b32 a173, 0
Mvaa 40,8,152
G 177,105
G 178,106
Mvaa 72,8,156
v_accvgpr_write_b32 a174, 0
G 179,107
Mvav 104,8,208
G 180,108
v_accvgpr_write_b32 a175, 0
Mvav 136,8,212
Ta 4,3,49152
Ta 6,3,50176
Mvav 140,12,212
s_mov_b32 s90, 0x5d800000
G 181,109
G 182,110
Mvav 108,12,208
G 183,111
Mvaa 76,12,156
s_mov_b64 s[88:89], 0
Ta 8,4,49152
K 176,176,177
Mvaa 44,12,152
W 9
Ta 10,4,50176
K 177,178,179
Mvaa 48,16,152
s_mov_b32 s64, 0
K 178,180,181
)"
R"(Mvaa 80,16,156
s_and_b32 s18, s14, 1
K 179,182,183
Mvav 112,16,208
s_lshl_b32 s62, s18, 14
buffer_store_dwordx4 v[176:179], v1, s[8:11], s36 offen offset:1024 sc1
Ta 12,5,49152
Mvav 144,16,212
Ta 14,5,50176
Mvav 148,20,212
s_lshl_b32 s20, s57, 15
Mvav 116,20,208
Mvaa 84,20,156
s_sub_u32 s20, 507904, s20
Ta 16,6,49152
Ta 18,6,50176
Mvaa 52,20,152
W 10
s_add_u32 s20, s20, s62
Mvaa 56,24,152
Mvaa 88,24,156
s_lshl_b32 s65, s57, 1
G 160,112
Mvav 120,24,208
s_sub_u32 s65, s65, 1
Ta 20,7,49152
G 161,113
Mvav 152,24,212
Ta 22,7,50176
G 162,114
Mvav 156,28,212
s_max_i32 s65, s65, 0
G 163,115
Mvav 124,28,208
G 164,116
Mvaa 92,28,156
s_sub_u32 s66, s60, 1
Ta 24,8,49152
G 165,117
Mvaa 60,28,152
s_waitcnt vmcnt(11) lgkmcnt(9)
Ta 26,8,50176
G 166,118
Mvaa 32,0,192
s_cmp_lt_u32 s14, 2
s_cselect_b32 s20, s20, 16384
s_cselect_b32 s66, s66, 15
s_cselect_b32 s21, 32768, 0
G 167,119
Mvaa 64,0,196
s_add_u32 s61, s91, s20
K 160,160,161
Mvaa 96,0,200
s_add_u32 s62, s62, s21
Ta 28,9,49152
K 161,162,163
Mvaa 128,0,204
Ta 30,9,50176
K 162,164,165
Mvaa 132,4,204
s_sub_u32 s63, 16384, s21
K 163,166,167
Mvaa 100,4,200
buffer_store_dwordx4 v[160:163], v1, s[8:11], s36 offen offset:2048 sc1
Mvaa 68,4,196
s_lshl_b32 s67, s60, 7
Ta 0,2,57344
Ta 2,2,58368
Mvaa 36,4,192
W 10
s_add_u32 s67, s67, s56
Mvaa 40,8,192
Mvaa 72,8,196
s_lshl_b32 s67, s67, 12
Mvaa 104,8,200
s_add_u32 s67, s67, 39845888
Ta 4,3,57344
Ta 6,3,58368
Mvaa 136,8,204
Mvaa 140,12,204
s_lshl_b32 s69, s60, 3
G 168,120
Mvaa 108,12,200
G 169,121
Mvaa 76,12,196
s_add_u32 s69, s69, s59
Ta 8,4,57344
G 170,122
Mvaa 44,12,192
W 9
s_lshl_b32 s69, s69, 6
Ta 10,4,58368
Mvaa 48,16,192
G 171,123
G 172,124
Mvaa 80,16,196
s_lshl_b32 s21, s58, 3
G 173,125
Mvaa 112,16,200
s_add_u32 s69, s69, s21
Ta 12,5,57344
G 174,126
Mvaa 144,16,204
Ta 14,5,58368
G 175,127
Mvaa 148,20,204
s_add_u32 s69, s69, 20480
K 168,168,169
Mvaa 116,20,200
K 169,170,171
Mvaa 84,20,196
s_mov_b32 s70, 0
Ta 16,6,57344
K 170,172,173
Mvaa 52,20,192
W 9
Ta 18,6,58368
K 171,174,175
Mvaa 56,24,192
s_mov_b32 s37, 0
buffer_store_dwordx4 v[168:171], v1, s[8:11], s36 offen offset:3072 sc1
Mvaa 88,24,196
s_sub_u32 s18, s57, 4
Mvaa 120,24,200
s_lshl_b32 s18, s18, 2
Ta 20,7,57344
Ta 22,7,58368
Mvaa 152,24,204
Mvaa 156,28,204
s_add_u32 s18, s18, s58
Mvaa 124,28,200
Mvaa 92,28,196
s_mul_i32 s85, s59, 7
Ta 24,8,57344
Ta 26,8,58368
Mvaa 60,28,192
s_waitcnt vmcnt(7) lgkmcnt(10)
s_add_u32 s85, s85, s18
Mvaa 32,0,32
Mvaa 64,0,36
s_cmp_lt_u32 s18, 7
s_cselect_b32 s85, s85, 99
Mvaa 96,0,40
s_cmp_lt_u32 s85, 54
s_cselect_b32 s65, 2, s65
Ta 28,9,57344
Ta 30,9,58368
Mvaa 128,0,44
Mvaa 132,4,44
s_mov_b32 s68, 0
Mvaa 100,4,40
Mvaa 68,4,36
s_mov_b64 s[24:25], -1
Mvaa 36,4,32
W 8
s_mov_b32 s22, 0x3e0293ee
Mvaa 40,8,32
Mvaa 72,8,36
s_mov_b32 s23, 0x3e0293ee
Mvaa 104,8,40
Mvaa 136,8,44
Mvaa 140,12,44
Mvaa 108,12,40
Mvaa 76,12,36
Mvaa 44,12,32
W 4
Mvaa 48,16,32
Mvaa 80,16,36
Mvaa 112,16,40
Mvaa 144,16,44
Mvaa 148,20,44
W 0
s_barrier
Ra 128,242,0
Mvaa 116,20,40
Mvaa 84,20,36
Ra 132,242,1024
Mvaa 52,20,32
Mvaa 56,24,32
Ra 136,242,2048
Mvaa 88,24,36
Mvaa 120,24,40
Ra 140,242,3072
Mvaa 152,24,44
Mvaa 156,28,44
Mvaa 124,28,40
Mvaa 92,28,36
Mvaa 60,28,32
.Lago%=:
s_cmp_eq_u32 s57, 0
s_cbranch_scc0 .LL_47%=
s_add_u32 m0, s62, 0
s_nop 0
Dc 1,8,61,0
Dc 1,8,61,1024
Dc 1,8,61,2048
Dc 1,8,61,3072
s_add_u32 m0, s62, 4096
s_nop 0
Dc 243,8,61,0
Dc 243,8,61,1024
Dc 243,8,61,2048
Dc 243,8,61,3072
s_add_u32 m0, s62, 8192
s_nop 0
Dc 244,8,61,0
Dc 244,8,61,1024
Dc 244,8,61,2048
Dc 244,8,61,3072
s_add_u32 m0, s62, 12288
s_nop 0
Dc 245,8,61,0
Dc 245,8,61,1024
Dc 245,8,61,2048
Dc 245,8,61,3072
s_add_u32 s61, s61, s63
.LL_47%=:
s_cmp_eq_u32 s60, 0
s_cbranch_scc1 .Lpd_31%=
v_pk_mul_f32 v[32:33], v[32:33], s[22:23]
v_pk_mul_f32 v[34:35], v[34:35], s[22:23]
v_pk_mul_f32 v[36:37], v[36:37], s[22:23]
v_pk_mul_f32 v[38:39], v[38:39], s[22:23]
K 32,32,33
K 33,34,35
K 34,36,37
K 35,38,39
v_pk_mul_f32 v[64:65], v[64:65], s[22:23]
v_pk_mul_f32 v[66:67], v[66:67], s[22:23]
v_pk_mul_f32 v[68:69], v[68:69], s[22:23]
v_pk_mul_f32 v[70:71], v[70:71], s[22:23]
K 64,64,65
K 65,66,67
K 66,68,69
K 67,70,71
v_pk_mul_f32 v[96:97], v[96:97], s[22:23]
v_pk_mul_f32 v[98:99], v[98:99], s[22:23]
v_pk_mul_f32 v[100:101], v[100:101], s[22:23]
v_pk_mul_f32 v[102:103], v[102:103], s[22:23]
K 96,96,97
K 97,98,99
K 98,100,101
K 99,102,103
v_pk_mul_f32 v[128:129], v[128:129], s[22:23]
v_pk_mul_f32 v[130:131], v[130:131], s[22:23]
v_pk_mul_f32 v[132:133], v[132:133], s[22:23]
v_pk_mul_f32 v[134:135], v[134:135], s[22:23]
K 128,128,129
K 129,130,131
K 130,132,133
K 131,134,135
v_pk_mul_f32 v[40:41], v[40:41], s[22:23]
v_pk_mul_f32 v[42:43], v[42:43], s[22:23]
v_pk_mul_f32 v[44:45], v[44:45], s[22:23]
v_pk_mul_f32 v[46:47], v[46:47], s[22:23]
v_pk_mul_f32 v[72:73], v[72:73], s[22:23]
v_pk_mul_f32 v[74:75], v[74:75], s[22:23]
v_pk_mul_f32 v[76:77], v[76:77], s[22:23]
v_pk_mul_f32 v[78:79], v[78:79], s[22:23]
v_pk_mul_f32 v[104:105], v[104:105], s[22:23]
v_pk_mul_f32 v[106:107], v[106:107], s[22:23]
v_pk_mul_f32 v[108:109], v[108:109], s[22:23]
v_pk_mul_f32 v[110:111], v[110:111], s[22:23]
v_pk_mul_f32 v[136:137], v[136:137], s[22:23]
v_pk_mul_f32 v[138:139], v[138:139], s[22:23]
v_pk_mul_f32 v[140:141], v[140:141], s[22:23]
v_pk_mul_f32 v[142:143], v[142:143], s[22:23]
v_pk_mul_f32 v[48:49], v[48:49], s[22:23]
v_pk_mul_f32 v[50:51], v[50:51], s[22:23]
v_pk_mul_f32 v[52:53], v[52:53], s[22:23]
v_pk_mul_f32 v[54:55], v[54:55], s[22:23]
v_pk_mul_f32 v[80:81], v[80:81], s[22:23]
v_pk_mul_f32 v[82:83], v[82:83], s[22:23]
v_pk_mul_f32 v[84:85], v[84:85], s[22:23]
v_pk_mul_f32 v[86:87], v[86:87], s[22:23]
v_pk_mul_f32 v[112:113], v[112:113], s[22:23]
v_pk_mul_f32 v[114:115], v[114:115], s[22:23]
v_pk_mul_f32 v[116:117], v[116:117], s[22:23]
v_pk_mul_f32 v[118:119], v[118:119], s[22:23]
v_pk_mul_f32 v[144:145], v[144:145], s[22:23]
v_pk_mul_f32 v[146:147], v[146:147], s[22:23]
v_pk_mul_f32 v[148:149], v[148:149], s[22:23]
v_pk_mul_f32 v[150:151], v[150:151], s[22:23]
v_pk_mul_f32 v[56:57], v[56:57], s[22:23]
v_pk_mul_f32 v[58:59], v[58:59], s[22:23]
v_pk_mul_f32 v[60:61], v[60:61], s[22:23]
v_pk_mul_f32 v[62:63], v[62:63], s[22:23]
v_pk_mul_f32 v[88:89], v[88:89], s[22:23]
v_pk_mul_f32 v[90:91], v[90:91], s[22:23]
v_pk_mul_f32 v[92:93], v[92:93], s[22:23]
v_pk_mul_f32 v[94:95], v[94:95], s[22:23]
v_pk_mul_f32 v[120:121], v[120:121], s[22:23]
v_pk_mul_f32 v[122:123], v[122:123], s[22:23]
v_pk_mul_f32 v[124:125], v[124:125], s[22:23]
v_pk_mul_f32 v[126:127], v[126:127], s[22:23]
v_pk_mul_f32 v[152:153], v[152:153], s[22:23]
v_pk_mul_f32 v[154:155], v[154:155], s[22:23]
v_pk_mul_f32 v[156:157], v[156:157], s[22:23]
v_pk_mul_f32 v[158:159], v[158:159], s[22:23]
W 0
Zvav 196,128,32
Ra 144,242,4096
Ra 148,242,5120
Ra 152,242,6144
Ra 156,242,7168
Zvav 204,128,64
K 40,40,41
K 41,42,43
K 42,44,45
K 43,46,47
Zvav 212,128,96
K 72,72,73
K 73,74,75
K 74,76,77
K 75,78,79
Zvav 220,128,128
K 104,104,105
K 105,106,107
K 106,108,109
K 107,110,111
K 136,136,137
K 137,138,139
K 138,140,141
K 139,142,143
Mvav 196,132,40
Ra 128,242,8192
K 48,48,49
Mvav 204,132,72
K 49,50,51
K 50,52,53
K 51,54,55
K 80,80,81
Mvav 212,132,104
K 81,82,83
K 82,84,85
K 83,86,87
K 112,112,113
Mvav 220,132,136
K 113,114,115
K 114,116,117
K 115,118,119
K 144,144,145
K 145,146,147
K 146,148,149
K 147,150,151
Mvav 196,136,48
Ra 132,242,9216
K 56,56,57
Mvav 204,136,80
K 57,58,59
K 58,60,61
K 59,62,63
K 88,88,89
Mvav 212,136,112
K 89,90,91
K 90,92,93
K 91,94,95
K 120,120,121
Mvav 220,136,144
K 121,122,123
K 122,124,125
K 123,126,127
K 152,152,153
K 153,154,155
K 154,156,157
K 155,158,159
Mvav 196,140,56
Ra 136,242,10240
v_accvgpr_write_b32 a0, v32
Mvav 204,140,88
v_accvgpr_write_b32 a1, v33
Mvav 212,140,120
v_accvgpr_write_b32 a2, v34
Mvav 220,140,152
W 3
v_accvgpr_write_b32 a3, v35
v_accvgpr_write_b32 a4, v40
Zvav 36,144,32
Ra 140,242,11264
v_accvgpr_write_b32 a5, v41
Zvav 68,144,64
v_accvgpr_write_b32 a6, v42
v_accvgpr_write_b32 a7, v43
v_max3_f32 v4, v196, v197, v198
v_max3_f32 v4, v4, v199, v252
Zvav 100,144,96
v_max3_f32 v5, v204, v205, v206
v_max3_f32 v5, v5, v207, v253
v_max3_f32 v6, v212, v213, v214
v_max3_f32 v6, v6, v215, v254
Zvav 132,144,128
v_max3_f32 v7, v220, v221, v222
v_max3_f32 v7, v7, v223, v255
v_mov_b32 v8, v4
v_mov_b32 v9, v5
Mvav 36,148,40
v_mov_b32 v10, v6
v_mov_b32 v11, v7
v_permlane32_swap_b32 v4, v8
v_accvgpr_write_b32 a8, v48
)"
R"(Mvav 68,148,72
v_permlane32_swap_b32 v5, v9
v_permlane32_swap_b32 v6, v10
v_permlane32_swap_b32 v7, v11
v_max_f32 v4, v4, v8
Mvav 100,148,104
v_max_f32 v5, v5, v9
v_max_f32 v6, v6, v10
v_max_f32 v7, v7, v11
v_mov_b32 v8, v4
Mvav 132,148,136
v_mov_b32 v9, v5
v_mov_b32 v10, v6
v_mov_b32 v11, v7
v_accvgpr_write_b32 a9, v49
Mvav 36,152,48
v_permlane16_swap_b32 v4, v8
v_permlane16_swap_b32 v5, v9
v_permlane16_swap_b32 v6, v10
v_permlane16_swap_b32 v7, v11
Mvav 68,152,80
v_max_f32 v4, v4, v8
v_max_f32 v5, v5, v9
v_max_f32 v6, v6, v10
v_max_f32 v7, v7, v11
Mvav 100,152,112
v_sub_f32 v4, 0, v4
v_sub_f32 v5, 0, v5
v_sub_f32 v6, 0, v6
v_sub_f32 v7, 0, v7
Mvav 132,152,144
v_mov_b32 v224, v4
v_mov_b32 v225, v4
v_mov_b32 v226, v4
v_mov_b32 v227, v4
Mvav 36,156,56
v_mov_b32 v228, v5
v_mov_b32 v229, v5
v_mov_b32 v230, v5
v_mov_b32 v231, v5
Mvav 68,156,88
v_mov_b32 v232, v6
v_mov_b32 v233, v6
v_mov_b32 v234, v6
v_mov_b32 v235, v6
Mvav 100,156,120
v_mov_b32 v236, v7
v_mov_b32 v237, v7
v_mov_b32 v238, v7
v_mov_b32 v239, v7
Mvav 132,156,152
W 0
Ra 144,242,12288
v_accvgpr_write_b32 a10, v50
v_accvgpr_write_b32 a11, v51
v_accvgpr_write_b32 a12, v56
Cvav 160,128,32,224
s_and_b32 s80, s14, 1
v_accvgpr_write_b32 a13, v57
v_accvgpr_write_b32 a14, v58
v_accvgpr_write_b32 a15, v59
v_accvgpr_write_b32 a16, v64
Cvav 164,128,64,228
s_lshl_b32 s80, s80, 14
Ra 148,242,13312
v_accvgpr_write_b32 a17, v65
v_accvgpr_write_b32 a18, v66
v_accvgpr_write_b32 a19, v67
Cvav 168,128,96,232
v_accvgpr_write_b32 a20, v72
v_accvgpr_write_b32 a21, v73
v_accvgpr_write_b32 a22, v74
v_accvgpr_write_b32 a23, v75
Cvav 172,128,128,236
s_add_u32 s80, s80, 98304
Ra 152,242,14336
v_accvgpr_write_b32 a24, v80
v_accvgpr_write_b32 a25, v81
v_accvgpr_write_b32 a26, v82
Mvav 160,132,40
A 196,196,224
v_accvgpr_write_b32 a27, v83
A 197,197,224
A 198,198,224
Mvav 164,132,72
Ra 156,242,15360
v_accvgpr_write_b32 a28, v88
A 199,199,224
v_accvgpr_write_b32 a29, v89
Mvav 168,132,104
X 196
v_accvgpr_write_b32 a30, v90
v_accvgpr_write_b32 a31, v91
Mvav 172,132,136
X 197
X 198
Mvav 160,136,48
v_accvgpr_write_b32 a32, v96
K 192,196,197
X 199
Mvav 164,136,80
v_accvgpr_write_b32 a33, v97
A 204,204,228
v_accvgpr_write_b32 a34, v98
K 193,198,199
Mvav 168,136,112
Ra 128,242,24576
v_accvgpr_write_b32 a35, v99
A 205,205,228
A 206,206,228
Mvav 172,136,144
v_accvgpr_write_b32 a36, v104
v_add_u32 v8, s80, v1
A 207,207,228
v_accvgpr_write_b32 a37, v105
Mvav 160,140,56
X 204
X 205
Mvav 164,140,88
v_accvgpr_write_b32 a38, v106
X 206
v_accvgpr_write_b32 a39, v107
Mvav 168,140,120
v_accvgpr_write_b32 a40, v112
K 200,204,205
X 207
Mvav 172,140,152
W 1
Ra 132,242,25600
v_accvgpr_write_b32 a41, v113
A 212,212,232
v_accvgpr_write_b32 a42, v114
Cvav 176,144,32,224
K 201,206,207
A 213,213,232
v_accvgpr_write_b32 a43, v115
A 214,214,232
Cvav 180,144,64,228
A 215,215,232
v_accvgpr_write_b32 a44, v120
X 212
Cvav 184,144,96,232
v_accvgpr_write_b32 a45, v121
X 213
v_accvgpr_write_b32 a46, v122
Cvav 188,144,128,236
Ra 136,242,26624
X 214
v_accvgpr_write_b32 a47, v123
Mvav 176,148,40
K 208,212,213
v_accvgpr_write_b32 a48, v128
X 215
Mvav 180,148,72
v_accvgpr_write_b32 a49, v129
A 220,220,236
v_accvgpr_write_b32 a50, v130
K 209,214,215
Mvav 184,148,104
A 221,221,236
v_accvgpr_write_b32 a51, v131
A 222,222,236
A 223,223,236
Mvav 188,148,136
v_accvgpr_write_b32 a52, v136
X 220
v_accvgpr_write_b32 a53, v137
Mvav 176,152,48
Ra 140,242,27648
X 221
v_accvgpr_write_b32 a54, v138
Mvav 180,152,80
X 222
v_accvgpr_write_b32 a55, v139
K 216,220,221
Mvav 184,152,112
v_accvgpr_write_b32 a56, v144
X 223
v_accvgpr_write_b32 a57, v145
Mvav 188,152,144
A 36,36,224
K 217,222,223
v_accvgpr_write_b32 a58, v146
A 37,37,224
Mvav 176,156,56
v_accvgpr_write_b32 a59, v147
v_accvgpr_write_b32 a60, v152
v_accvgpr_write_b32 a61, v153
v_accvgpr_write_b32 a62, v154
v_accvgpr_write_b32 a63, v155
Mvav 180,156,88
s_barrier
Ra 144,242,28672
Ra 148,242,29696
A 38,38,224
A 39,39,224
Mvav 184,156,120
X 36
X 37
X 38
K 194,36,37
X 39
A 68,68,228
K 195,38,39
A 69,69,228
A 70,70,228
A 71,71,228
X 68
X 69
X 70
K 202,68,69
X 71
A 100,100,232
K 203,70,71
A 101,101,232
A 102,102,232
A 103,103,232
X 100
X 101
X 102
K 210,100,101
X 103
A 132,132,236
K 211,102,103
A 133,133,236
A 134,134,236
A 135,135,236
X 132
X 133
X 134
K 218,132,133
X 135
s_nop 0
K 219,134,135
Mvav 188,156,152
W 2
Ra 152,242,30720
Zvav 32,128,192
Ra 156,242,31744
Zvav 64,128,200
Zvav 96,128,208
s_and_b32 s80, s14, 1
s_xor_b32 s62, s62, 0x10000
Zvav 128,128,216
s_lshl_b32 s80, s80, 14
s_add_u32 s80, s80, 131072
v_add_u32 v23, 61440, v21
Zvav 132,132,216
v_add_u32 v12, s80, v1
Ra 64,12,8192
Ra 68,12,9216
v_add_u32 v24, 61440, v22
Zvav 100,132,208
Ra 72,12,10240
Ra 76,12,11264
Ra 80,12,12288
v_add_u32 v25, 122880, v21
Zvav 68,132,200
Ra 84,12,13312
Ra 88,12,14336
Ra 92,12,15360
v_add_u32 v26, 122880, v22
Zvav 36,132,192
Ra 96,12,0
Ra 100,12,1024
Ra 104,12,2048
Zvav 40,136,192
Ra 108,12,3072
Ra 112,12,4096
X 172
Zvav 72,136,200
Ra 116,12,5120
Ra 120,12,6144
X 173
Zvav 104,136,208
Ra 124,12,7168
X 174
K 220,172,173
Zvav 136,136,216
X 175
Zvav 140,140,216
X 168
K 221,174,175
Zvav 108,140,208
X 169
X 170
Zvav 76,140,200
s_waitcnt vmcnt(0) lgkmcnt(0)
s_barrier
v_xor_b32 v242, 0x10000, v242
ds_read_b128 v[248:251], v8
Zvav 44,140,192
s_add_u32 s80, s71, 128
v_mov_b64 v[28:29], s[12:13]
s_mov_b64 exec, 1
buffer_store_dwordx2 v[28:29], off, s[8:11], s80 sc1
s_mov_b64 exec, -1
Ra 128,242,0
K 212,168,169
X 171
Zvav 48,144,192
X 164
K 213,170,171
Zvav 80,144,200
X 165
X 166
Zvav 112,144,208
Ra 132,242,1024
K 204,164,165
X 167
Zvav 144,144,216
X 160
Zvav 148,148,216
K 205,166,167
X 161
Zvav 116,148,208
Ra 136,242,2048
X 162
Zvav 84,148,200
W 3
K 196,160,161
X 163
Zvav 52,148,192
X 176
K 197,162,163
Zvav 56,152,192
Ra 140,242,3072
X 177
Zvav 88,152,200
X 178
K 198,176,177
Zvav 120,152,208
X 179
X 180
Zvav 152,152,216
K 199,178,179
Zvav 156,156,216
X 181
X 182
Zvav 124,156,208
K 206,180,181
X 183
Zvav 92,156,200
K 207,182,183
Zvav 60,156,192
.Ll0t_45%=:
s_cmp_eq_u32 s64, s65
s_cbranch_scc1 .Ll1t_34%=
.Ll0_32%=:
W 0
Cvaa 160,128,0,224
Ra 144,242,4096
Ra 148,242,5120
Cvaa 164,128,16,228
s_xor_b32 s62, s62, 0x10000
Ra 152,242,6144
Cvaa 168,128,32,232
X 184
Cvaa 172,128,48,236
X 185
Mvaa 160,132,4
X 186
Mvaa 164,132,20
Ra 156,242,7168
K 214,184,185
Mvaa 168,132,36
X 187
Mvaa 172,132,52
X 188
Mvaa 160,136,8
Ra 128,242,16384
Mvaa 164,136,24
K 215,186,187
Mvaa 168,136,40
X 189
Mvaa 172,136,56
X 190
Mvaa 160,140,12
Ra 132,242,17408
Mvaa 164,140,28
K 222,188,189
Mvaa 168,140,44
X 191
Mvaa 172,140,60
K 223,190,191
v_smfmac_f32_16x16x64_bf16 a[160:163], a[176:179], v[192:199], v240
Ra 136,242,18432
v_smfmac_f32_16x16x64_bf16 a[164:167], a[176:179], v[200:207], v240
X 160
v_smfmac_f32_16x16x64_bf16 a[168:171], a[176:179], v[208:215], v240
X 161
v_smfmac_f32_16x16x64_bf16 a[172:175], a[176:179], v[216:223], v240
W 3
X 162
Cvaa 176,144,0,224
Ra 140,242,19456
Cvaa 180,144,16,228
K 192,160,161
Cvaa 184,144,32,232
X 163
)"
R"(Cvaa 188,144,48,236
X 164
Mvaa 176,148,4
Ra 144,242,20480
Mvaa 180,148,20
K 193,162,163
Mvaa 184,148,36
X 165
Mvaa 188,148,52
X 166
Mvaa 176,152,8
Ra 148,242,21504
Mvaa 180,152,24
K 200,164,165
Mvaa 184,152,40
X 167
Mvaa 188,152,56
X 168
Mvaa 176,156,12
Ra 152,242,22528
Mvaa 180,156,28
K 201,166,167
Mvaa 184,156,44
X 169
Mvaa 188,156,60
W 3
Mvav 32,128,196
X 170
Mvav 64,128,204
Ra 156,242,23552
K 208,168,169
Mvav 96,128,212
X 171
Mvav 128,128,220
Mvav 132,132,220
X 172
Mvav 100,132,212
s_add_u32 s64, s64, 1
Ra 128,242,8192
K 209,170,171
Mvav 68,132,204
X 173
Mvav 36,132,196
Mvav 40,136,196
X 174
Mvav 72,136,204
Ra 132,242,9216
K 216,172,173
Mvav 104,136,212
X 175
Mvav 136,136,220
Mvav 140,140,220
X 176
Mvav 108,140,212
Ra 136,242,10240
K 217,174,175
Mvav 76,140,204
X 177
Mvav 44,140,196
W 3
Mvav 48,144,196
X 178
Mvav 80,144,204
Ra 140,242,11264
Mvav 112,144,212
K 194,176,177
Mvav 144,144,220
X 179
Mvav 148,148,220
X 180
Mvav 116,148,212
Ra 144,242,12288
Mvav 84,148,204
K 195,178,179
Mvav 52,148,196
X 181
Mvav 56,152,196
X 182
Mvav 88,152,204
Ra 148,242,13312
Mvav 120,152,212
K 202,180,181
Mvav 152,152,220
X 183
Mvav 156,156,220
X 184
Mvav 124,156,212
Ra 152,242,14336
Mvav 92,156,204
K 203,182,183
Mvav 60,156,196
W 3
X 185
Cvaa 160,128,0,224
X 186
Cvaa 164,128,16,228
Ra 156,242,15360
Cvaa 168,128,32,232
K 210,184,185
Cvaa 172,128,48,236
X 187
Mvaa 160,132,4
X 188
Mvaa 164,132,20
Ra 128,242,24576
Mvaa 168,132,36
K 211,186,187
Mvaa 172,132,52
X 189
Mvaa 160,136,8
X 190
Mvaa 164,136,24
Ra 132,242,25600
Mvaa 168,136,40
K 218,188,189
Mvaa 172,136,56
X 191
Mvaa 160,140,12
Ra 136,242,26624
Mvaa 164,140,28
Mvaa 168,140,44
Mvaa 172,140,60
W 3
Cvaa 176,144,0,224
Ra 140,242,27648
Cvaa 180,144,16,228
X 160
Cvaa 184,144,32,232
K 219,190,191
Cvaa 188,144,48,236
X 161
Mvaa 176,148,4
X 162
Mvaa 180,148,20
Ra 144,242,28672
K 196,160,161
Mvaa 184,148,36
X 163
Mvaa 188,148,52
X 164
Mvaa 176,152,8
Ra 148,242,29696
K 197,162,163
Mvaa 180,152,24
X 165
Mvaa 184,152,40
X 166
Mvaa 188,152,56
K 204,164,165
Mvaa 176,156,12
X 167
Mvaa 180,156,28
Ra 152,242,30720
Mvaa 184,156,44
X 168
Mvaa 188,156,60
W 3
K 205,166,167
Mvav 32,128,192
X 169
Mvav 64,128,200
Ra 156,242,31744
Mvav 96,128,208
X 170
Mvav 128,128,216
K 212,168,169
Mvav 132,132,216
X 171
Mvav 100,132,208
X 172
Mvav 68,132,200
Mvav 36,132,192
K 213,170,171
Mvav 40,136,192
X 173
Mvav 72,136,200
X 174
Mvav 104,136,208
Mvav 136,136,216
K 220,172,173
Mvav 140,140,216
X 175
Mvav 108,140,208
X 176
Mvav 76,140,200
W 0
s_barrier
v_xor_b32 v242, 0x10000, v242
Mvav 44,140,192
Ra 128,242,0
K 221,174,175
Mvav 48,144,192
X 177
Mvav 80,144,200
X 178
Mvav 112,144,208
Ra 132,242,1024
Mvav 144,144,216
K 198,176,177
Mvav 148,148,216
X 179
Mvav 116,148,208
X 180
Mvav 84,148,200
Ra 136,242,2048
Mvav 52,148,192
K 199,178,179
Mvav 56,152,192
X 181
Mvav 88,152,200
Ra 140,242,3072
Mvav 120,152,208
X 182
Mvav 152,152,216
K 206,180,181
Mvav 156,156,216
X 183
Mvav 124,156,208
Mvav 92,156,200
K 207,182,183
Mvav 60,156,192
s_cmp_lg_u32 s64, s65
s_cbranch_scc1 .Ll0_32%=
s_cmp_lt_u32 s85, 54
s_cbranch_scc1 .Lmoe_43%=
.Ll1t_34%=:
s_cmp_eq_u32 s64, s66
s_cbranch_scc1 .Lsp1_35%=
.Ll1_33%=:
W 0
Cvaa 160,128,0,224
Ra 144,242,4096
Ra 148,242,5120
Cvaa 164,128,16,228
s_add_u32 m0, s62, 0
Ra 152,242,6144
Cvaa 168,128,32,232
X 184
Cvaa 172,128,48,236
X 185
Mvaa 160,132,4
X 186
Mvaa 164,132,20
Ra 156,242,7168
K 214,184,185
Mvaa 168,132,36
Dc 1,8,61,0
Mvaa 172,132,52
X 187
Mvaa 160,136,8
X 188
Mvaa 164,136,24
Ra 128,242,16384
K 215,186,187
Mvaa 168,136,40
Dc 1,8,61,1024
Mvaa 172,136,56
X 189
Mvaa 160,140,12
X 190
Mvaa 164,140,28
Ra 132,242,17408
K 222,188,189
Mvaa 168,140,44
Dc 1,8,61,2048
Mvaa 172,140,60
X 191
v_smfmac_f32_16x16x64_bf16 a[160:163], a[176:179], v[192:199], v240
Ra 136,242,18432
K 223,190,191
v_smfmac_f32_16x16x64_bf16 a[164:167], a[176:179], v[200:207], v240
X 160
v_smfmac_f32_16x16x64_bf16 a[168:171], a[176:179], v[208:215], v240
Dc 1,8,61,3072
v_smfmac_f32_16x16x64_bf16 a[172:175], a[176:179], v[216:223], v240
W 3
X 161
Cvaa 176,144,0,224
s_add_u32 m0, s62, 4096
X 162
Cvaa 180,144,16,228
Ra 140,242,19456
K 192,160,161
Cvaa 184,144,32,232
X 163
Cvaa 188,144,48,236
Dc 243,8,61,0
Mvaa 176,148,4
X 164
Mvaa 180,148,20
Ra 144,242,20480
K 193,162,163
Mvaa 184,148,36
X 165
Mvaa 188,148,52
Dc 243,8,61,1024
Mvaa 176,152,8
X 166
Mvaa 180,152,24
Ra 148,242,21504
K 200,164,165
Mvaa 184,152,40
X 167
Mvaa 188,152,56
Dc 243,8,61,2048
Mvaa 176,156,12
X 168
Mvaa 180,156,28
Ra 152,242,22528
K 201,166,167
Mvaa 184,156,44
X 169
Mvaa 188,156,60
W 3
Dc 243,8,61,3072
Mvav 32,128,196
s_add_u32 m0, s62, 8192
X 170
Mvav 64,128,204
Ra 156,242,23552
K 208,168,169
Mvav 96,128,212
X 171
Mvav 128,128,220
Dc 244,8,61,0
Mvav 132,132,220
X 172
Mvav 100,132,212
Ra 128,242,8192
K 209,170,171
Mvav 68,132,204
X 173
Mvav 36,132,196
Dc 244,8,61,1024
Mvav 40,136,196
X 174
Mvav 72,136,204
Ra 132,242,9216
K 216,172,173
Mvav 104,136,212
X 175
Mvav 136,136,220
Dc 244,8,61,2048
Mvav 140,140,220
X 176
Mvav 108,140,212
Ra 136,242,10240
K 217,174,175
Mvav 76,140,204
X 177
Mvav 44,140,196
W 3
Dc 244,8,61,3072
Mvav 48,144,196
X 178
Mvav 80,144,204
s_add_u32 m0, s62, 12288
Ra 140,242,11264
Mvav 112,144,212
K 194,176,177
Mvav 144,144,220
X 179
Mvav 148,148,220
X 180
Mvav 116,148,212
Dc 245,8,61,0
Ra 144,242,12288
Mvav 84,148,204
K 195,178,179
Mvav 52,148,196
X 181
Mvav 56,152,196
X 182
Mvav 88,152,204
Dc 245,8,61,1024
Ra 148,242,13312
Mvav 120,152,212
K 202,180,181
Mvav 152,152,220
X 183
Mvav 156,156,220
X 184
Mvav 124,156,212
Dc 245,8,61,2048
Ra 152,242,14336
Mvav 92,156,204
K 203,182,183
Mvav 60,156,196
W 3
X 185
Cvaa 160,128,0,224
X 186
Cvaa 164,128,16,228
Dc 245,8,61,3072
Ra 156,242,15360
Cvaa 168,128,32,232
s_add_u32 s61, s61, s63
K 210,184,185
Cvaa 172,128,48,236
X 187
)"
R"(Mvaa 160,132,4
X 188
Mvaa 164,132,20
Ra 128,242,24576
Mvaa 168,132,36
s_xor_b32 s62, s62, 0x10000
K 211,186,187
Mvaa 172,132,52
X 189
Mvaa 160,136,8
X 190
Mvaa 164,136,24
s_add_u32 s64, s64, 1
Ra 132,242,25600
Mvaa 168,136,40
K 218,188,189
Mvaa 172,136,56
X 191
Mvaa 160,140,12
Ra 136,242,26624
Mvaa 164,140,28
Mvaa 168,140,44
Mvaa 172,140,60
W 3
Cvaa 176,144,0,224
Ra 140,242,27648
Cvaa 180,144,16,228
X 160
Cvaa 184,144,32,232
K 219,190,191
Cvaa 188,144,48,236
X 161
Mvaa 176,148,4
X 162
Mvaa 180,148,20
Ra 144,242,28672
K 196,160,161
Mvaa 184,148,36
X 163
Mvaa 188,148,52
X 164
Mvaa 176,152,8
Ra 148,242,29696
K 197,162,163
Mvaa 180,152,24
X 165
Mvaa 184,152,40
X 166
Mvaa 188,152,56
K 204,164,165
Mvaa 176,156,12
X 167
Mvaa 180,156,28
Ra 152,242,30720
Mvaa 184,156,44
X 168
Mvaa 188,156,60
W 3
K 205,166,167
Mvav 32,128,192
X 169
Mvav 64,128,200
Ra 156,242,31744
Mvav 96,128,208
X 170
Mvav 128,128,216
K 212,168,169
Mvav 132,132,216
X 171
Mvav 100,132,208
X 172
Mvav 68,132,200
Mvav 36,132,192
K 213,170,171
Mvav 40,136,192
X 173
Mvav 72,136,200
X 174
Mvav 104,136,208
Mvav 136,136,216
K 220,172,173
Mvav 140,140,216
X 175
Mvav 108,140,208
X 176
Mvav 76,140,200
s_waitcnt vmcnt(0) lgkmcnt(0)
s_barrier
v_xor_b32 v242, 0x10000, v242
Mvav 44,140,192
Ra 128,242,0
K 221,174,175
Mvav 48,144,192
X 177
Mvav 80,144,200
X 178
Mvav 112,144,208
Ra 132,242,1024
Mvav 144,144,216
K 198,176,177
Mvav 148,148,216
X 179
Mvav 116,148,208
X 180
Mvav 84,148,200
Ra 136,242,2048
Mvav 52,148,192
K 199,178,179
Mvav 56,152,192
X 181
Mvav 88,152,200
Ra 140,242,3072
Mvav 120,152,208
X 182
Mvav 152,152,216
K 206,180,181
Mvav 156,156,216
X 183
Mvav 124,156,208
Mvav 92,156,200
K 207,182,183
Mvav 60,156,192
s_cmp_lg_u32 s64, s66
s_cbranch_scc1 .Ll1_33%=
s_cmp_ge_u32 s14, 2
s_cbranch_scc1 .Lf1r_46%=
.Lsp1_35%=:
W 0
Cvaa 160,128,0,224
Ra 144,242,4096
Ra 148,242,5120
Cvaa 164,128,16,228
s_add_u32 m0, s62, 0
Ra 152,242,6144
Ra 156,242,7168
Cvaa 168,128,32,232
X 184
Cvaa 172,128,48,236
X 185
Mvaa 160,132,4
X 186
Mvaa 164,132,20
Ra 128,242,16384
K 214,184,185
Mvaa 168,132,36
Dc 1,8,61,0
Mvaa 172,132,52
X 187
Mvaa 160,136,8
X 188
Mvaa 164,136,24
Dc 1,8,61,1024
K 215,186,187
Mvaa 168,136,40
X 189
Mvaa 172,136,56
Ra 132,242,17408
Mvaa 160,140,12
X 190
Mvaa 164,140,28
Dc 1,8,61,2048
K 222,188,189
Mvaa 168,140,44
X 191
Mvaa 172,140,60
Ra 136,242,18432
K 223,190,191
v_smfmac_f32_16x16x64_bf16 a[160:163], a[176:179], v[192:199], v240
Dc 1,8,61,3072
v_cndmask_b32_e64 v160, v241, v160, s[72:73]
v_smfmac_f32_16x16x64_bf16 a[164:167], a[176:179], v[200:207], v240
s_add_u32 m0, s62, 4096
v_cndmask_b32_e64 v161, v241, v161, s[74:75]
v_cndmask_b32_e64 v162, v241, v162, s[76:77]
v_smfmac_f32_16x16x64_bf16 a[168:171], a[176:179], v[208:215], v240
v_cndmask_b32_e64 v163, v241, v163, s[78:79]
v_smfmac_f32_16x16x64_bf16 a[172:175], a[176:179], v[216:223], v240
W 3
X 160
Cvaa 180,144,16,228
X 161
Cvaa 184,144,32,232
Ra 140,242,19456
Dc 243,8,61,0
Cvaa 188,144,48,236
X 162
Mvaa 180,148,20
Ra 144,242,20480
K 192,160,161
Mvaa 184,148,36
X 163
Mvaa 188,148,52
Dc 243,8,61,1024
Ra 148,242,21504
Mvaa 180,152,24
X 164
Mvaa 184,152,40
K 193,162,163
Mvaa 188,152,56
Dc 243,8,61,2048
Ra 152,242,22528
Mvaa 180,156,28
X 165
Mvaa 184,156,44
X 166
Mvaa 188,156,60
W 3
Dc 243,8,61,3072
K 200,164,165
Mvav 32,128,196
s_add_u32 m0, s62, 8192
X 167
Mvav 64,128,204
X 168
Mvav 96,128,212
Ra 156,242,23552
K 201,166,167
Mvav 128,128,220
X 169
Mvav 132,132,220
X 170
Mvav 100,132,212
Dc 244,8,61,0
K 208,168,169
Mvav 68,132,204
X 171
Mvav 36,132,196
Ra 128,242,8192
Ra 132,242,9216
Mvav 40,136,196
Dc 244,8,61,1024
Mvav 72,136,204
X 172
Mvav 104,136,212
K 209,170,171
Mvav 136,136,220
X 173
Mvav 140,140,220
Dc 244,8,61,2048
Ra 136,242,10240
Mvav 108,140,212
X 174
Mvav 76,140,204
K 216,172,173
Mvav 44,140,196
W 3
Dc 244,8,61,3072
Ra 140,242,11264
Mvav 48,144,196
s_add_u32 m0, s62, 12288
X 175
Mvav 80,144,204
v_mov_b32 v194, 0
K 217,174,175
Mvav 112,144,212
v_mov_b32 v195, 0
v_cndmask_b32_e64 v180, v241, v180, s[72:73]
Mvav 144,144,220
v_cndmask_b32_e64 v181, v241, v181, s[74:75]
v_cndmask_b32_e64 v182, v241, v182, s[76:77]
Mvav 148,148,220
Dc 245,8,61,0
v_cndmask_b32_e64 v183, v241, v183, s[78:79]
Mvav 116,148,212
X 180
Mvav 84,148,204
Ra 144,242,12288
Mvav 52,148,196
X 181
Mvav 56,152,196
Dc 245,8,61,1024
Ra 148,242,13312
Mvav 88,152,204
X 182
Mvav 120,152,212
K 202,180,181
Mvav 152,152,220
X 183
Mvav 156,156,220
Dc 245,8,61,2048
Ra 152,242,14336
Mvav 124,156,212
X 184
Mvav 92,156,204
K 203,182,183
Mvav 60,156,196
W 3
Dc 245,8,61,3072
Ra 156,242,15360
Cvaa 168,128,32,232
s_add_u32 s61, s61, s63
X 185
Cvaa 172,128,48,236
s_xor_b32 s62, s62, 0x10000
X 186
Mvaa 168,132,36
Ra 128,242,24576
K 210,184,185
Mvaa 172,132,52
s_add_u32 s64, s64, 1
X 187
Mvaa 168,136,40
X 188
Mvaa 172,136,56
Ra 132,242,25600
K 211,186,187
Mvaa 168,140,44
Ra 136,242,26624
Mvaa 172,140,60
Ra 140,242,27648
X 189
X 190
K 218,188,189
X 191
v_mov_b32 v196, 0
K 219,190,191
W 4
Cvaa 188,144,48,236
Ra 144,242,28672
Mvaa 188,148,52
v_mov_b32 v197, 0
v_mov_b32 v204, 0
Mvaa 188,152,56
Ra 148,242,29696
v_mov_b32 v205, 0
Mvaa 188,156,60
W 2
Ra 152,242,30720
v_cndmask_b32_e64 v168, v241, v168, s[72:73]
Mvav 32,128,192
v_cndmask_b32_e64 v169, v241, v169, s[74:75]
v_cndmask_b32_e64 v170, v241, v170, s[76:77]
Mvav 64,128,200
Ra 156,242,31744
v_cndmask_b32_e64 v171, v241, v171, s[78:79]
Mvav 96,128,208
X 168
Mvav 128,128,216
X 169
Mvav 132,132,216
X 170
Mvav 100,132,208
v_mov_b32 v224, v250
Mvav 68,132,200
K 212,168,169
v_mov_b32 v225, v250
Mvav 36,132,192
X 171
Mvav 40,136,192
v_mov_b32 v226, v250
Mvav 72,136,200
X 172
Mvav 104,136,208
v_mov_b32 v227, v250
K 213,170,171
Mvav 136,136,216
X 173
Mvav 140,140,216
v_mov_b32 v228, v251
v_mov_b32 v229, v251
Mvav 108,140,208
X 174
Mvav 76,140,200
K 220,172,173
v_mov_b32 v230, v251
Mvav 44,140,192
W 0
X 175
Mvav 48,144,192
v_mov_b32 v231, v251
v_mov_b32 v198, 0
Mvav 80,144,200
K 221,174,175
v_mov_b32 v232, v248
Mvav 112,144,208
v_mov_b32 v199, 0
v_mov_b32 v206, 0
Mvav 144,144,216
v_mov_b32 v233, v248
v_mov_b32 v207, 0
Mvav 148,148,216
v_mov_b32 v214, 0
v_mov_b32 v234, v248
Mvav 116,148,208
v_mov_b32 v215, 0
v_cndmask_b32_e64 v188, v241, v188, s[72:73]
Mvav 84,148,200
v_mov_b32 v235, v248
v_cndmask_b32_e64 v189, v241, v189, s[74:75]
Mvav 52,148,192
v_cndmask_b32_e64 v190, v241, v190, s[76:77]
v_mov_b32 v236, v249
Mvav 56,152,192
v_cndmask_b32_e64 v191, v241, v191, s[78:79]
v_mov_b32 v237, v249
Mvav 88,152,200
X 188
Mvav 120,152,208
X 189
Mvav 152,152,216
)"
R"(v_mov_b32 v238, v249
v_mov_b32 v239, v249
Mvav 156,156,216
X 190
Mvav 124,156,208
s_waitcnt vmcnt(0)
s_barrier
v_xor_b32 v242, 0x10000, v242
Mvav 92,156,200
Ra 128,242,40960
K 222,188,189
Mvav 60,156,192
Ra 132,242,41984
Ra 136,242,43008
Ra 140,242,44032
X 191
s_nop 0
K 223,190,191
s_cmp_eq_u32 s64, 15
s_cbranch_scc1 .Lfs_41%=
.Lsp2_36%=:
v_smfmac_f32_16x16x64_bf16 a[160:163], a[176:179], v[192:199], v240
s_add_u32 m0, s62, 0
Ra 144,242,45056
Ra 148,242,46080
Ra 152,242,47104
Ra 156,242,48128
v_smfmac_f32_16x16x64_bf16 a[164:167], a[176:179], v[200:207], v240
v_smfmac_f32_16x16x64_bf16 a[168:171], a[176:179], v[208:215], v240
v_smfmac_f32_16x16x64_bf16 a[172:175], a[176:179], v[216:223], v240
W 4
Dc 1,8,61,0
Cvaa 168,128,96,232
v_mov_b32 v192, 0
G 12,160
Cvaa 172,128,112,236
Dc 1,8,61,1024
Ra 128,242,16384
G 4,161
A 12,12,4
v_cmp_nlt_f32_e64 s[86:87], v12, s90
v_mov_b32 v193, 0
Mvaa 168,132,100
v_rcp_f32 v12, v12
s_or_b64 s[88:89], s[88:89], s[86:87]
v_accvgpr_write_b32 a160, 0
v_mov_b32 v200, 0
v_accvgpr_write_b32 a161, 0
v_accvgpr_write_b32 a162, 0
Mvaa 172,132,116
Dc 1,8,61,2048
v_accvgpr_write_b32 a163, 0
G 13,164
v_mov_b32 v201, 0
G 5,165
A 13,13,5
Mvaa 168,136,104
Ra 132,242,17408
v_cmp_nlt_f32_e64 s[86:87], v13, s90
v_rcp_f32 v13, v13
s_or_b64 s[88:89], s[88:89], s[86:87]
v_accvgpr_write_b32 a164, 0
v_accvgpr_write_b32 a165, 0
Mvaa 172,136,120
Dc 1,8,61,3072
v_accvgpr_write_b32 a166, 0
v_accvgpr_write_b32 a167, 0
U 32,32,12
U 33,33,12
U 34,34,12
Mvaa 168,140,108
Ra 136,242,18432
U 35,35,12
U 36,36,12
U 37,37,12
U 38,38,12
U 39,39,12
Mvaa 172,140,124
s_add_u32 m0, s62, 4096
Ra 140,242,19456
K 32,32,33
K 33,34,35
K 34,36,37
K 35,38,39
W 4
Cvaa 188,144,112,236
s_add_u32 s93, s67, 0
buffer_store_dwordx4 v[32:35], v1, s[8:11], s93 offen offset:0 sc1
Ra 144,242,20480
U 40,40,12
U 41,41,12
U 42,42,12
U 43,43,12
Mvaa 188,148,116
Ra 148,242,21504
U 44,44,12
U 45,45,12
U 46,46,12
U 47,47,12
K 40,40,41
Mvaa 188,152,120
v_cndmask_b32_e64 v168, v241, v168, s[72:73]
v_cndmask_b32_e64 v169, v241, v169, s[74:75]
v_cndmask_b32_e64 v170, v241, v170, s[76:77]
v_cndmask_b32_e64 v171, v241, v171, s[78:79]
X 168
Mvaa 188,156,124
W 2
Dc 243,8,61,0
X 169
K 41,42,43
K 42,44,45
K 43,46,47
Mvav 96,128,212
Ra 152,242,22528
Ra 156,242,23552
X 170
K 208,168,169
Mvav 128,128,220
buffer_store_dwordx4 v[40:43], v1, s[8:11], s93 offen offset:1024 sc1
U 48,48,12
U 49,49,12
U 50,50,12
U 51,51,12
U 52,52,12
Mvav 132,132,220
U 53,53,12
U 54,54,12
U 55,55,12
K 48,48,49
K 49,50,51
K 50,52,53
Mvav 100,132,212
Dc 243,8,61,1024
Ra 128,242,32768
Ra 132,242,33792
X 171
K 51,54,55
Mvav 104,136,212
X 172
K 209,170,171
X 173
Mvav 136,136,220
buffer_store_dwordx4 v[48:51], v1, s[8:11], s93 offen offset:2048 sc1
U 56,56,12
U 57,57,12
U 58,58,12
U 59,59,12
U 60,60,12
Mvav 140,140,220
U 61,61,12
U 62,62,12
U 63,63,12
K 56,56,57
K 57,58,59
K 58,60,61
Mvav 108,140,212
W 2
Dc 243,8,61,2048
Ra 136,242,34816
Ra 140,242,35840
K 59,62,63
X 174
Mvav 112,144,212
K 216,172,173
X 175
Mvav 144,144,220
buffer_store_dwordx4 v[56:59], v1, s[8:11], s93 offen offset:3072 sc1
Ra 144,242,36864
G 14,168
G 6,169
A 14,14,6
v_cmp_nlt_f32_e64 s[86:87], v14, s90
Mvav 148,148,220
v_rcp_f32 v14, v14
s_or_b64 s[88:89], s[88:89], s[86:87]
v_accvgpr_write_b32 a168, 0
v_accvgpr_write_b32 a169, 0
v_accvgpr_write_b32 a170, 0
v_accvgpr_write_b32 a171, 0
Mvav 116,148,212
Dc 243,8,61,3072
s_add_u32 m0, s62, 8192
G 15,172
G 7,173
A 15,15,7
v_cmp_nlt_f32_e64 s[86:87], v15, s90
v_mov_b32 v194, 0
Mvav 120,152,212
v_rcp_f32 v15, v15
s_or_b64 s[88:89], s[88:89], s[86:87]
v_accvgpr_write_b32 a172, 0
v_accvgpr_write_b32 a173, 0
v_accvgpr_write_b32 a174, 0
v_accvgpr_write_b32 a175, 0
Mvav 152,152,220
Dc 244,8,61,0
U 64,64,13
U 65,65,13
U 66,66,13
U 67,67,13
U 68,68,13
Mvav 156,156,220
U 69,69,13
U 70,70,13
U 71,71,13
K 64,64,65
K 65,66,67
K 66,68,69
Mvav 124,156,212
W 1
Dc 244,8,61,1024
Ra 148,242,37888
K 67,70,71
s_add_u32 s93, s67, 131072
K 217,174,175
v_mov_b32 v195, 0
Cvaa 160,128,64,224
Ra 152,242,38912
Ra 156,242,39936
v_mov_b32 v202, 0
v_mov_b32 v203, 0
v_mov_b32 v210, 0
v_mov_b32 v211, 0
Cvaa 164,128,80,228
buffer_store_dwordx4 v[64:67], v1, s[8:11], s93 offen offset:0 sc1
U 72,72,13
U 73,73,13
U 74,74,13
U 75,75,13
U 76,76,13
Cvaa 168,128,96,232
U 77,77,13
U 78,78,13
U 79,79,13
K 72,72,73
K 73,74,75
K 74,76,77
Cvaa 172,128,112,236
Dc 244,8,61,2048
K 75,78,79
v_cndmask_b32_e64 v188, v241, v188, s[72:73]
v_cndmask_b32_e64 v189, v241, v189, s[74:75]
v_cndmask_b32_e64 v190, v241, v190, s[76:77]
v_cndmask_b32_e64 v191, v241, v191, s[78:79]
Mvaa 160,132,68
Ra 128,242,49152
X 188
X 189
Mvaa 164,132,84
buffer_store_dwordx4 v[72:75], v1, s[8:11], s93 offen offset:1024 sc1
U 80,80,13
U 81,81,13
U 82,82,13
U 83,83,13
U 84,84,13
Mvaa 168,132,100
U 85,85,13
U 86,86,13
U 87,87,13
K 80,80,81
K 81,82,83
K 82,84,85
Mvaa 172,132,116
Dc 244,8,61,3072
s_add_u32 m0, s62, 12288
Ra 132,242,50176
K 83,86,87
X 190
Mvaa 160,136,72
K 218,188,189
X 191
Mvaa 164,136,88
buffer_store_dwordx4 v[80:83], v1, s[8:11], s93 offen offset:2048 sc1
U 88,88,13
U 89,89,13
U 90,90,13
U 91,91,13
U 92,92,13
Mvaa 168,136,104
U 93,93,13
U 94,94,13
U 95,95,13
K 88,88,89
K 89,90,91
K 90,92,93
Mvaa 172,136,120
K 91,94,95
buffer_store_dwordx4 v[88:91], v1, s[8:11], s93 offen offset:3072 sc1
U 96,96,14
U 97,97,14
U 98,98,14
U 99,99,14
Mvaa 160,140,76
U 100,100,14
U 101,101,14
U 102,102,14
U 103,103,14
K 96,96,97
K 97,98,99
Mvaa 164,140,92
Dc 245,8,61,0
Ra 136,242,51200
K 98,100,101
K 99,102,103
s_add_u32 s93, s67, 262144
Mvaa 168,140,108
Mvaa 172,140,124
W 3
Dc 245,8,61,1024
Ra 140,242,52224
v_cndmask_b32_e64 v160, v241, v160, s[72:73]
K 219,190,191
v_cndmask_b32_e64 v161, v241, v161, s[74:75]
v_cndmask_b32_e64 v162, v241, v162, s[76:77]
Cvaa 180,144,80,228
v_cndmask_b32_e64 v163, v241, v163, s[78:79]
X 160
X 161
Cvaa 184,144,96,232
buffer_store_dwordx4 v[96:99], v1, s[8:11], s93 offen offset:0 sc1
U 104,104,14
U 105,105,14
U 106,106,14
U 107,107,14
U 108,108,14
Cvaa 188,144,112,236
U 109,109,14
U 110,110,14
U 111,111,14
K 104,104,105
K 105,106,107
K 106,108,109
Mvaa 180,148,84
Dc 245,8,61,2048
Ra 144,242,53248
K 107,110,111
X 162
K 196,160,161
Mvaa 184,148,100
X 163
Mvaa 188,148,116
buffer_store_dwordx4 v[104:107], v1, s[8:11], s93 offen offset:1024 sc1
U 112,112,14
U 113,113,14
U 114,114,14
U 115,115,14
U 116,116,14
Mvaa 180,152,88
U 117,117,14
U 118,118,14
U 119,119,14
K 112,112,113
K 113,114,115
K 114,116,117
Mvaa 184,152,104
Dc 245,8,61,3072
s_add_u32 s61, s61, s63
s_xor_b32 s62, s62, 0x10000
s_add_u32 s64, s64, 1
K 115,118,119
X 164
Mvaa 188,152,120
Ra 148,242,54272
Ra 152,242,55296
K 197,162,163
X 165
Mvaa 180,156,92
buffer_store_dwordx4 v[112:115], v1, s[8:11], s93 offen offset:2048 sc1
U 120,120,14
U 121,121,14
U 122,122,14
U 123,123,14
U 124,124,14
Mvaa 184,156,108
U 125,125,14
U 126,126,14
U 127,127,14
K 120,120,121
K 121,122,123
K 122,124,125
Mvaa 188,156,124
W 3
K 123,126,127
buffer_store_dwordx4 v[120:123], v1, s[8:11], s93 offen offset:3072 sc1
U 128,128,15
U 129,129,15
U 130,130,15
U 131,131,15
Zvav 32,128,192
U 132,132,15
U 133,133,15
U 134,134,15
U 135,135,15
K 128,128,129
K 129,130,131
Zvav 64,128,200
K 130,132,133
K 131,134,135
s_add_u32 s93, s67, 393216
)"
R"(buffer_store_dwordx4 v[128:131], v1, s[8:11], s93 offen offset:0 sc1
U 136,136,15
U 137,137,15
U 138,138,15
Zvav 96,128,208
U 139,139,15
U 140,140,15
U 141,141,15
U 142,142,15
U 143,143,15
K 136,136,137
Zvav 128,128,216
K 137,138,139
K 138,140,141
K 139,142,143
buffer_store_dwordx4 v[136:139], v1, s[8:11], s93 offen offset:1024 sc1
U 144,144,15
U 145,145,15
Zvav 132,132,216
U 146,146,15
U 147,147,15
U 148,148,15
U 149,149,15
U 150,150,15
U 151,151,15
Zvav 100,132,208
K 144,144,145
K 145,146,147
K 146,148,149
K 147,150,151
buffer_store_dwordx4 v[144:147], v1, s[8:11], s93 offen offset:2048 sc1
U 152,152,15
Zvav 68,132,200
U 153,153,15
U 154,154,15
U 155,155,15
U 156,156,15
U 157,157,15
U 158,158,15
Zvav 36,132,192
U 159,159,15
K 152,152,153
K 153,154,155
K 154,156,157
K 155,158,159
buffer_store_dwordx4 v[152:155], v1, s[8:11], s93 offen offset:3072 sc1
Zvav 40,136,192
Ra 156,242,56320
X 166
K 204,164,165
X 167
Zvav 72,136,200
X 168
K 205,166,167
X 169
Zvav 104,136,208
X 170
K 212,168,169
X 171
Zvav 136,136,216
X 172
K 213,170,171
X 173
Zvav 140,140,216
Zvav 108,140,208
X 174
Zvav 76,140,200
s_waitcnt vmcnt(6) lgkmcnt(0)
s_barrier
v_xor_b32 v242, 0x10000, v242
Zvav 44,140,192
Ra 128,242,40960
K 220,172,173
X 175
Zvav 48,144,192
Zvav 80,144,200
v_mov_b32 v198, 0
Zvav 112,144,208
Ra 132,242,41984
K 221,174,175
Zvav 144,144,216
v_mov_b32 v199, 0
v_cndmask_b32_e64 v180, v241, v180, s[72:73]
Zvav 148,148,216
v_cndmask_b32_e64 v181, v241, v181, s[74:75]
Zvav 116,148,208
Ra 136,242,43008
v_cndmask_b32_e64 v182, v241, v182, s[76:77]
Zvav 84,148,200
v_cndmask_b32_e64 v183, v241, v183, s[78:79]
Zvav 52,148,192
X 180
Zvav 56,152,192
Ra 140,242,44032
X 181
Zvav 88,152,200
Zvav 120,152,208
X 182
Zvav 152,152,216
K 206,180,181
Zvav 156,156,216
X 183
Zvav 124,156,208
Zvav 92,156,200
s_waitcnt vmcnt(0)
s_and_b64 s[86:87], s[88:89], s[24:25]
s_cbranch_scc1 .LL_48%=
v_mov_b64 v[28:29], s[12:13]
s_mov_b64 exec, 1
buffer_store_dwordx2 v[28:29], off, s[8:11], s69 sc1
s_mov_b64 exec, -1
.LL_48%=:
K 207,182,183
Zvav 60,156,192
s_cmp_eq_u32 s64, 15
s_cbranch_scc1 .Lf2_40%=
.Ll2_37%=:
W 0
Cvaa 160,128,64,224
Ra 144,242,45056
Ra 148,242,46080
Cvaa 164,128,80,228
s_add_u32 m0, s62, 0
Ra 152,242,47104
Cvaa 168,128,96,232
X 184
Cvaa 172,128,112,236
X 185
Mvaa 160,132,68
X 186
Mvaa 164,132,84
Ra 156,242,48128
K 214,184,185
Mvaa 168,132,100
Dc 1,8,61,0
Mvaa 172,132,116
X 187
Mvaa 160,136,72
X 188
Mvaa 164,136,88
Ra 128,242,57344
K 215,186,187
Mvaa 168,136,104
Dc 1,8,61,1024
Mvaa 172,136,120
X 189
Mvaa 160,140,76
X 190
Mvaa 164,140,92
Ra 132,242,58368
K 222,188,189
Mvaa 168,140,108
Dc 1,8,61,2048
Mvaa 172,140,124
X 191
v_smfmac_f32_16x16x64_bf16 a[160:163], a[176:179], v[192:199], v240
Ra 136,242,59392
K 223,190,191
v_smfmac_f32_16x16x64_bf16 a[164:167], a[176:179], v[200:207], v240
X 160
v_smfmac_f32_16x16x64_bf16 a[168:171], a[176:179], v[208:215], v240
Dc 1,8,61,3072
v_smfmac_f32_16x16x64_bf16 a[172:175], a[176:179], v[216:223], v240
W 3
X 161
Cvaa 176,144,64,224
s_add_u32 m0, s62, 4096
X 162
Cvaa 180,144,80,228
Ra 140,242,60416
K 192,160,161
Cvaa 184,144,96,232
X 163
Cvaa 188,144,112,236
Dc 243,8,61,0
Mvaa 176,148,68
X 164
Mvaa 180,148,84
Ra 144,242,61440
K 193,162,163
Mvaa 184,148,100
X 165
Mvaa 188,148,116
Dc 243,8,61,1024
Mvaa 176,152,72
X 166
Mvaa 180,152,88
Ra 148,242,62464
K 200,164,165
Mvaa 184,152,104
X 167
Mvaa 188,152,120
Dc 243,8,61,2048
Mvaa 176,156,76
X 168
Mvaa 180,156,92
Ra 152,242,63488
K 201,166,167
Mvaa 184,156,108
X 169
Mvaa 188,156,124
W 3
Dc 243,8,61,3072
Mvav 32,128,196
s_add_u32 m0, s62, 8192
X 170
Mvav 64,128,204
Ra 156,242,64512
K 208,168,169
Mvav 96,128,212
X 171
Mvav 128,128,220
Dc 244,8,61,0
Mvav 132,132,220
X 172
Mvav 100,132,212
Ra 128,242,32768
K 209,170,171
Mvav 68,132,204
X 173
Mvav 36,132,196
Dc 244,8,61,1024
Mvav 40,136,196
X 174
Mvav 72,136,204
Ra 132,242,33792
K 216,172,173
Mvav 104,136,212
X 175
Mvav 136,136,220
Dc 244,8,61,2048
Mvav 140,140,220
X 176
Mvav 108,140,212
Ra 136,242,34816
K 217,174,175
Mvav 76,140,204
X 177
Mvav 44,140,196
W 3
Dc 244,8,61,3072
Mvav 48,144,196
X 178
Mvav 80,144,204
s_add_u32 m0, s62, 12288
Ra 140,242,35840
Mvav 112,144,212
K 194,176,177
Mvav 144,144,220
X 179
Mvav 148,148,220
X 180
Mvav 116,148,212
Dc 245,8,61,0
Ra 144,242,36864
Mvav 84,148,204
K 195,178,179
Mvav 52,148,196
X 181
Mvav 56,152,196
X 182
Mvav 88,152,204
Dc 245,8,61,1024
Ra 148,242,37888
Mvav 120,152,212
K 202,180,181
Mvav 152,152,220
X 183
Mvav 156,156,220
X 184
Mvav 124,156,212
Dc 245,8,61,2048
Ra 152,242,38912
Mvav 92,156,204
K 203,182,183
Mvav 60,156,196
W 3
X 185
Cvaa 160,128,64,224
X 186
Cvaa 164,128,80,228
Dc 245,8,61,3072
Ra 156,242,39936
Cvaa 168,128,96,232
s_add_u32 s61, s61, s63
K 210,184,185
Cvaa 172,128,112,236
X 187
Mvaa 160,132,68
X 188
Mvaa 164,132,84
Ra 128,242,49152
Mvaa 168,132,100
s_xor_b32 s62, s62, 0x10000
K 211,186,187
Mvaa 172,132,116
X 189
Mvaa 160,136,72
X 190
Mvaa 164,136,88
s_add_u32 s64, s64, 1
Ra 132,242,50176
Mvaa 168,136,104
K 218,188,189
Mvaa 172,136,120
X 191
Mvaa 160,140,76
Ra 136,242,51200
Mvaa 164,140,92
Mvaa 168,140,108
Mvaa 172,140,124
W 3
Cvaa 176,144,64,224
Ra 140,242,52224
Cvaa 180,144,80,228
X 160
Cvaa 184,144,96,232
K 219,190,191
Cvaa 188,144,112,236
X 161
Mvaa 176,148,68
X 162
Mvaa 180,148,84
Ra 144,242,53248
K 196,160,161
Mvaa 184,148,100
X 163
Mvaa 188,148,116
X 164
Mvaa 176,152,72
Ra 148,242,54272
K 197,162,163
Mvaa 180,152,88
X 165
Mvaa 184,152,104
X 166
Mvaa 188,152,120
K 204,164,165
Mvaa 176,156,76
X 167
Mvaa 180,156,92
Ra 152,242,55296
Mvaa 184,156,108
X 168
Mvaa 188,156,124
W 3
K 205,166,167
Mvav 32,128,192
X 169
Mvav 64,128,200
Ra 156,242,56320
Mvav 96,128,208
X 170
Mvav 128,128,216
K 212,168,169
Mvav 132,132,216
X 171
Mvav 100,132,208
X 172
Mvav 68,132,200
Mvav 36,132,192
K 213,170,171
Mvav 40,136,192
X 173
Mvav 72,136,200
X 174
Mvav 104,136,208
Mvav 136,136,216
K 220,172,173
Mvav 140,140,216
X 175
Mvav 108,140,208
X 176
Mvav 76,140,200
s_waitcnt vmcnt(0) lgkmcnt(0)
s_barrier
v_xor_b32 v242, 0x10000, v242
Mvav 44,140,192
Ra 128,242,40960
K 221,174,175
Mvav 48,144,192
X 177
Mvav 80,144,200
X 178
Mvav 112,144,208
Ra 132,242,41984
Mvav 144,144,216
K 198,176,177
Mvav 148,148,216
X 179
Mvav 116,148,208
X 180
Mvav 84,148,200
Ra 136,242,43008
Mvav 52,148,192
K 199,178,179
Mvav 56,152,192
X 181
Mvav 88,152,200
Ra 140,242,44032
)"
R"(Mvav 120,152,208
X 182
Mvav 152,152,216
K 206,180,181
Mvav 156,156,216
X 183
Mvav 124,156,208
Mvav 92,156,200
K 207,182,183
Mvav 60,156,192
s_cmp_lg_u32 s64, 15
s_cbranch_scc1 .Ll2_37%=
.Lf2_40%=:
W 0
Cvaa 160,128,64,224
Ra 144,242,45056
Ra 148,242,46080
Cvaa 164,128,80,228
Ra 152,242,47104
v_bfe_u32 v28, v1, 4, 2
Cvaa 168,128,96,232
X 184
Cvaa 172,128,112,236
X 185
Mvaa 160,132,68
Ra 156,242,48128
v_lshlrev_b32 v28, 3, v28
Mvaa 164,132,84
X 186
Mvaa 168,132,100
buffer_load_dwordx2 v[28:29], v28, s[8:11], s54 offen sc1
K 214,184,185
Mvaa 172,132,116
s_lshl_b32 s94, s58, 2
X 187
Mvaa 160,136,72
s_add_u32 s94, s94, s51
X 188
Mvaa 164,136,88
s_add_u32 s94, s94, 3
Ra 128,242,57344
K 215,186,187
Mvaa 168,136,104
s_and_b32 s94, s94, 15
X 189
Mvaa 172,136,120
X 190
Mvaa 160,140,76
s_mul_i32 s94, s94, 10240
Ra 132,242,58368
K 222,188,189
Mvaa 164,140,92
s_add_u32 s94, s94, s48
X 191
Mvaa 168,140,108
K 223,190,191
Mvaa 172,140,124
s_lshl_b32 s94, s94, 2
v_smfmac_f32_16x16x64_bf16 a[160:163], a[176:179], v[192:199], v240
s_mul_i32 s95, s51, 10240
Ra 136,242,59392
v_smfmac_f32_16x16x64_bf16 a[164:167], a[176:179], v[200:207], v240
X 160
v_smfmac_f32_16x16x64_bf16 a[168:171], a[176:179], v[208:215], v240
s_add_u32 s95, s95, s48
X 161
v_smfmac_f32_16x16x64_bf16 a[172:175], a[176:179], v[216:223], v240
W 3
X 162
Cvaa 176,144,64,224
s_add_u32 m0, s95, 0
Ra 140,242,60416
K 192,160,161
Cvaa 180,144,80,228
X 163
Cvaa 184,144,96,232
X 164
Cvaa 188,144,112,236
D 16,40,94,0
K 193,162,163
Mvaa 176,148,68
X 165
Mvaa 180,148,84
X 166
Mvaa 184,148,100
D 17,40,94,1024
K 200,164,165
Mvaa 188,148,116
X 167
Mvaa 176,152,72
Ra 144,242,61440
Ra 148,242,62464
Mvaa 180,152,88
D 18,40,94,2048
Mvaa 184,152,104
X 172
Mvaa 188,152,120
K 201,166,167
Mvaa 176,156,76
D 19,40,94,3072
Ra 152,242,63488
Mvaa 180,156,92
s_add_u32 m0, s95, 4096
X 173
Mvaa 184,156,108
X 174
Mvaa 188,156,124
W 3
buffer_load_dwordx4 v20, s[40:43], s94 offen lds
K 216,172,173
Mvav 32,128,196
s_lshl_b32 s94, s58, 2
X 175
Mvav 64,128,204
s_add_u32 s94, s94, s51
X 168
Mvav 96,128,212
s_add_u32 s94, s94, 5
s_and_b32 s94, s94, 15
K 217,174,175
Mvav 128,128,220
s_mul_i32 s94, s94, 10240
X 169
Mvav 132,132,220
s_add_u32 s94, s94, s48
X 170
Mvav 100,132,212
s_lshl_b32 s94, s94, 2
Ra 156,242,64512
K 208,168,169
Mvav 68,132,204
s_mov_b32 s18, 40960
X 171
Mvav 36,132,196
s_mul_i32 s95, s51, 10240
X 176
Mvav 40,136,196
s_mov_b32 s19, 73728
s_bitcmp1_b32 s14, 0
s_cselect_b32 s18, 49152, s18
s_cselect_b32 s19, 98304, s19
K 209,170,171
Mvav 72,136,204
s_add_u32 s95, s95, s48
X 177
Mvav 104,136,212
s_cmp_lt_u32 s14, 2
s_cselect_b32 s20, s18, s19
s_cselect_b32 s21, s19, s18
X 178
Mvav 136,136,220
s_add_u32 m0, s95, 20480
Ra 128,242,49152
K 194,176,177
Mvav 140,140,220
X 179
Mvav 108,140,212
v_add_u32 v2, s20, v1
v_add_u32 v3, s21, v1
Mvav 76,140,204
s_lshl_b32 s18, s14, 9
X 180
Mvav 44,140,196
W 1
D 16,40,94,0
K 195,178,179
Mvav 48,144,196
X 181
Mvav 80,144,204
Ra 132,242,50176
v_lshrrev_b32 v232, 1, v1
Mvav 112,144,212
D 17,40,94,1024
v_add_u32 v232, s18, v232
Mvav 144,144,220
X 182
Mvav 148,148,220
K 202,180,181
v_bfe_u32 v12, v1, 6, 2
Mvav 116,148,212
D 18,40,94,2048
v_lshrrev_b32 v13, 8, v1
Mvav 84,148,204
X 183
Mvav 52,148,196
X 184
Mvav 56,152,196
D 19,40,94,3072
Ra 136,242,51200
Mvav 88,152,204
s_add_u32 m0, s95, 24576
v_bfe_u32 v14, v1, 4, 1
K 203,182,183
Mvav 120,152,212
v_lshlrev_b32 v14, 4, v14
v_mov_b32 v15, 0x3f80
Mvav 152,152,220
buffer_load_dwordx4 v20, s[40:43], s94 offen lds
Ra 140,242,52224
Mvav 156,156,220
s_waitcnt vmcnt(10)
v_cmp_eq_u64 vcc, s[12:13], v[28:29]
s_cmp_eq_u64 vcc, -1
s_cselect_b32 s46, 79691776, 0
X 185
Mvav 124,156,212
s_add_u32 s81, s58, 0
X 186
Mvav 92,156,204
s_and_b32 s81, s81, 3
v_lshlrev_b32 v15, v14, v15
K 210,184,185
Mvav 60,156,196
s_lshl_b32 s81, s81, 12
v_bfe_u32 v14, v1, 5, 1
v_xor_b32 v27, v12, v13
Maav 160,180,192
X 187
X 188
K 211,186,187
Maav 164,180,200
X 189
X 190
K 218,188,189
X 191
s_nop 0
K 219,190,191
Maav 168,180,208
s_add_u32 s81, s81, s49
Ra 144,242,53248
v_lshl_or_b32 v27, v27, 1, v14
Maav 172,180,216
W 1
v_cmp_eq_u32_e64 s[86:87], 0, v27
v_mov_b32 v226, 0
Mvav 32,128,192
s_add_u32 s82, s81, 131072
Ra 148,242,54272
v_cndmask_b32_e64 v224, 0, v15, s[86:87]
Mvav 64,128,200
buffer_load_dwordx4 a[184:187], v1, s[44:47], s81 offen offset:0 sc1
v_mov_b32 v228, 0
Mvav 96,128,208
v_mov_b32 v230, v224
v_cmp_eq_u32_e64 s[86:87], 1, v27
Mvav 128,128,216
Ra 152,242,55296
v_mov_b32 v227, 0
Mvav 132,132,216
buffer_load_dwordx4 a[200:203], v1, s[44:47], s82 offen offset:0 sc1
v_cndmask_b32_e64 v225, 0, v15, s[86:87]
Mvav 100,132,208
v_mov_b32 v229, 0
v_mov_b32 v231, v225
Mvav 68,132,200
Ra 156,242,56320
G 4,160
Mvav 36,132,192
buffer_load_dwordx4 a[188:191], v1, s[44:47], s81 offen offset:1024 sc1
G 8,161
Mvav 40,136,192
G 5,164
G 9,165
Mvav 72,136,200
G 6,168
G 10,169
Mvav 104,136,208
buffer_load_dwordx4 a[204:207], v1, s[44:47], s82 offen offset:1024 sc1
G 7,172
Mvav 136,136,216
G 11,173
A 4,4,8
Mvav 140,140,216
A 5,5,9
A 6,6,10
Mvav 108,140,208
buffer_load_dwordx4 a[192:195], v1, s[44:47], s81 offen offset:2048 sc1
A 7,7,11
Mvav 76,140,200
ds_write_b64 v232, v[6:7] offset:57344
A 8,4,5
Mvav 44,140,192
W 1
A 9,6,7
A 8,8,9
Mvav 48,144,192
buffer_load_dwordx4 a[208:211], v1, s[44:47], s82 offen offset:2048 sc1
v_cmp_ngt_f32_e64 s[86:87], s90, v8
Mvav 80,144,200
s_lshl_b32 s19, s14, 2
v_mov_b32 v9, s19
Mvav 112,144,208
s_or_b64 s[88:89], s[88:89], s[86:87]
s_and_b64 s[88:89], s[88:89], s[24:25]
s_cselect_b32 s18, 1, 0
v_mov_b32 v8, s18
Mvav 144,144,216
buffer_load_dwordx4 a[196:199], v1, s[44:47], s81 offen offset:3072 sc1
ds_write_b32 v9, v8 offset:59392
Mvav 148,148,216
v_xor_b32 v10, 1024, v232
v_bfe_u32 v11, v1, 2, 4
Mvav 116,148,208
K 96,96,97
K 97,98,99
Mvav 84,148,200
buffer_load_dwordx4 a[212:215], v1, s[44:47], s82 offen offset:3072 sc1
K 98,100,101
Mvav 52,148,192
s_add_u32 s81, s58, 1
s_and_b32 s81, s81, 3
s_lshl_b32 s81, s81, 12
Mvav 56,152,192
s_add_u32 s81, s81, s49
s_add_u32 s82, s81, 131072
K 99,102,103
Mvav 88,152,200
ds_write_b128 v2, v[96:99] offset:0
K 128,128,129
Mvav 120,152,208
K 129,130,131
K 130,132,133
Mvav 152,152,216
buffer_load_dwordx4 a[216:219], v1, s[44:47], s81 offen offset:0 sc1
K 131,134,135
Mvav 156,156,216
ds_write_b128 v2, v[128:131] offset:4096
K 104,104,105
Mvav 124,156,208
K 105,106,107
K 106,108,109
Mvav 92,156,200
buffer_load_dwordx4 a[232:235], v1, s[44:47], s82 offen offset:0 sc1
K 107,110,111
Mvav 60,156,192
ds_write_b128 v2, v[104:107] offset:1024
K 136,136,137
buffer_load_dwordx4 a[220:223], v1, s[44:47], s81 offen offset:1024 sc1
buffer_load_dwordx4 a[236:239], v1, s[44:47], s82 offen offset:1024 sc1
K 137,138,139
K 138,140,141
buffer_load_dwordx4 a[224:227], v1, s[44:47], s81 offen offset:2048 sc1
K 139,142,143
ds_write_b128 v2, v[136:139] offset:5120
buffer_load_dwordx4 a[240:243], v1, s[44:47], s82 offen offset:2048 sc1
buffer_load_dwordx4 a[228:231], v1, s[44:47], s81 offen offset:3072 sc1
buffer_load_dwordx4 a[244:247], v1, s[44:47], s82 offen offset:3072 sc1
K 112,112,113
K 113,114,115
K 114,116,117
K 115,118,119
ds_write_b128 v2, v[112:115] offset:2048
K 144,144,145
K 145,146,147
K 146,148,149
K 147,150,151
ds_write_b128 v2, v[144:147] offset:6144
K 120,120,121
K 121,122,123
K 122,124,125
K 123,126,127
ds_write_b128 v2, v[120:123] offset:3072
K 152,152,153
K 153,154,155
K 154,156,157
K 155,158,159
ds_write_b128 v2, v[152:155] offset:7168
s_branch .Lend_42%=
.Lfs_41%=:
v_smfmac_f32_16x16x64_bf16 a[160:163], a[176:179], v[192:199], v240
Ra 144,242,45056
Ra 148,242,46080
Ra 152,242,47104
Ra 156,242,48128
v_bfe_u32 v28, v1, 4, 2
v_smfmac_f32_16x16x64_bf16 a[164:167], a[176:179], v[200:207], v240
v_lshlrev_b32 v28, 3, v28
v_smfmac_f32_16x16x64_bf16 a[168:171], a[176:179], v[208:215], v240
buffer_load_dwordx2 v[28:29], v28, s[8:11], s54 offen sc1
v_smfmac_f32_16x16x64_bf16 a[172:175], a[176:179], v[216:223], v240
W 4
s_lshl_b32 s94, s58, 2
Cvaa 168,128,96,232
s_add_u32 s94, s94, s51
v_mov_b32 v192, 0
G 12,160
Cvaa 172,128,112,236
G 4,161
A 12,12,4
v_cmp_nlt_f32_e64 s[86:87], v12, s90
v_rcp_f32 v12, v12
s_or_b64 s[88:89], s[88:89], s[86:87]
v_accvgpr_write_b32 a160, 0
Mvaa 168,132,100
s_add_u32 s94, s94, 3
Ra 128,242,16384
v_accvgpr_write_b32 a161, 0
v_accvgpr_write_b32 a162, 0
v_accvgpr_write_b32 a163, 0
G 13,164
)"
R"(G 5,165
Mvaa 172,132,116
s_and_b32 s94, s94, 15
v_mov_b32 v193, 0
A 13,13,5
v_cmp_nlt_f32_e64 s[86:87], v13, s90
v_rcp_f32 v13, v13
s_or_b64 s[88:89], s[88:89], s[86:87]
Mvaa 168,136,104
s_mul_i32 s94, s94, 10240
v_accvgpr_write_b32 a164, 0
v_accvgpr_write_b32 a165, 0
v_accvgpr_write_b32 a166, 0
v_accvgpr_write_b32 a167, 0
U 32,32,12
U 33,33,12
Mvaa 172,136,120
s_add_u32 s94, s94, s48
U 34,34,12
v_mov_b32 v200, 0
U 35,35,12
U 36,36,12
U 37,37,12
U 38,38,12
Mvaa 168,140,108
s_lshl_b32 s94, s94, 2
Ra 132,242,17408
Ra 136,242,18432
U 39,39,12
K 32,32,33
K 33,34,35
K 34,36,37
Mvaa 172,140,124
Ra 140,242,19456
K 35,38,39
s_add_u32 s93, s67, 0
buffer_store_dwordx4 v[32:35], v1, s[8:11], s93 offen offset:0 sc1
U 40,40,12
v_mov_b32 v201, 0
W 4
Cvaa 188,144,112,236
s_mul_i32 s95, s51, 10240
U 41,41,12
U 42,42,12
U 43,43,12
U 44,44,12
U 45,45,12
U 46,46,12
Mvaa 188,148,116
s_add_u32 s95, s95, s48
U 47,47,12
K 40,40,41
K 41,42,43
K 42,44,45
K 43,46,47
buffer_store_dwordx4 v[40:43], v1, s[8:11], s93 offen offset:1024 sc1
Mvaa 188,152,120
s_add_u32 m0, s95, 0
U 48,48,12
U 49,49,12
U 50,50,12
U 51,51,12
U 52,52,12
U 53,53,12
Mvaa 188,156,124
W 0
U 54,54,12
U 55,55,12
K 48,48,49
K 49,50,51
K 50,52,53
K 51,54,55
Mvav 96,128,212
buffer_store_dwordx4 v[48:51], v1, s[8:11], s93 offen offset:2048 sc1
U 56,56,12
U 57,57,12
U 58,58,12
U 59,59,12
U 60,60,12
Mvav 128,128,220
U 61,61,12
U 62,62,12
U 63,63,12
K 56,56,57
K 57,58,59
K 58,60,61
Mvav 132,132,220
Ra 144,242,20480
Ra 148,242,21504
Ra 152,242,22528
Ra 156,242,23552
K 59,62,63
buffer_store_dwordx4 v[56:59], v1, s[8:11], s93 offen offset:3072 sc1
Mvav 100,132,212
G 14,168
G 6,169
A 14,14,6
v_cmp_nlt_f32_e64 s[86:87], v14, s90
v_rcp_f32 v14, v14
s_or_b64 s[88:89], s[88:89], s[86:87]
Mvav 104,136,212
D 16,40,94,0
v_accvgpr_write_b32 a168, 0
v_accvgpr_write_b32 a169, 0
v_accvgpr_write_b32 a170, 0
v_accvgpr_write_b32 a171, 0
G 15,172
Mvav 136,136,220
G 7,173
A 15,15,7
v_cndmask_b32_e64 v168, v241, v168, s[72:73]
v_cndmask_b32_e64 v169, v241, v169, s[74:75]
v_cndmask_b32_e64 v170, v241, v170, s[76:77]
v_cndmask_b32_e64 v171, v241, v171, s[78:79]
Mvav 140,140,220
D 17,40,94,1024
v_cmp_nlt_f32_e64 s[86:87], v15, s90
v_rcp_f32 v15, v15
s_or_b64 s[88:89], s[88:89], s[86:87]
v_accvgpr_write_b32 a172, 0
v_accvgpr_write_b32 a173, 0
Mvav 108,140,212
W 0
v_accvgpr_write_b32 a174, 0
v_accvgpr_write_b32 a175, 0
U 64,64,13
U 65,65,13
X 168
Mvav 112,144,212
D 18,40,94,2048
U 66,66,13
U 67,67,13
U 68,68,13
U 69,69,13
U 70,70,13
Mvav 144,144,220
Ra 128,242,49152
U 71,71,13
K 64,64,65
K 65,66,67
K 66,68,69
K 67,70,71
s_add_u32 s93, s67, 131072
Mvav 148,148,220
buffer_store_dwordx4 v[64:67], v1, s[8:11], s93 offen offset:0 sc1
U 72,72,13
U 73,73,13
U 74,74,13
U 75,75,13
U 76,76,13
Mvav 116,148,212
U 77,77,13
U 78,78,13
U 79,79,13
K 72,72,73
K 73,74,75
K 74,76,77
Mvav 120,152,212
Ra 132,242,50176
Ra 136,242,51200
Ra 140,242,52224
D 19,40,94,3072
s_add_u32 m0, s95, 4096
Ra 144,242,53248
K 75,78,79
Mvav 152,152,220
Ra 148,242,54272
X 169
X 170
K 208,168,169
Mvav 156,156,220
X 171
X 172
K 209,170,171
X 173
X 174
K 216,172,173
X 175
v_mov_b32 v194, 0
K 217,174,175
v_mov_b32 v195, 0
v_mov_b32 v202, 0
v_mov_b32 v203, 0
v_mov_b32 v210, 0
v_mov_b32 v211, 0
Mvav 124,156,212
v_cndmask_b32_e64 v188, v241, v188, s[72:73]
v_cndmask_b32_e64 v189, v241, v189, s[74:75]
v_cndmask_b32_e64 v190, v241, v190, s[76:77]
v_cndmask_b32_e64 v191, v241, v191, s[78:79]
X 188
X 189
X 190
K 218,188,189
X 191
s_nop 0
K 219,190,191
Maav 168,180,208
buffer_store_dwordx4 v[72:75], v1, s[8:11], s93 offen offset:1024 sc1
U 80,80,13
U 81,81,13
U 82,82,13
U 83,83,13
U 84,84,13
Maav 172,180,216
W 2
U 85,85,13
U 86,86,13
U 87,87,13
K 80,80,81
K 81,82,83
K 82,84,85
Zvav 32,128,192
buffer_load_dwordx4 v20, s[40:43], s94 offen lds
s_lshl_b32 s94, s58, 2
s_add_u32 s94, s94, s51
s_add_u32 s94, s94, 5
s_and_b32 s94, s94, 15
s_mul_i32 s94, s94, 10240
K 83,86,87
Zvav 64,128,200
buffer_store_dwordx4 v[80:83], v1, s[8:11], s93 offen offset:2048 sc1
U 88,88,13
U 89,89,13
U 90,90,13
U 91,91,13
U 92,92,13
U 93,93,13
U 94,94,13
U 95,95,13
K 88,88,89
K 89,90,91
K 90,92,93
K 91,94,95
buffer_store_dwordx4 v[88:91], v1, s[8:11], s93 offen offset:3072 sc1
U 96,96,14
U 97,97,14
U 98,98,14
U 99,99,14
U 100,100,14
U 101,101,14
U 102,102,14
U 103,103,14
K 96,96,97
K 97,98,99
K 98,100,101
K 99,102,103
s_add_u32 s93, s67, 262144
buffer_store_dwordx4 v[96:99], v1, s[8:11], s93 offen offset:0 sc1
s_nop 1
Zvav 96,128,208
U 104,104,14
U 105,105,14
U 106,106,14
U 107,107,14
U 108,108,14
U 109,109,14
U 110,110,14
U 111,111,14
K 104,104,105
K 105,106,107
K 106,108,109
K 107,110,111
buffer_store_dwordx4 v[104:107], v1, s[8:11], s93 offen offset:1024 sc1
U 112,112,14
U 113,113,14
U 114,114,14
U 115,115,14
U 116,116,14
U 117,117,14
U 118,118,14
U 119,119,14
K 112,112,113
K 113,114,115
K 114,116,117
K 115,118,119
buffer_store_dwordx4 v[112:115], v1, s[8:11], s93 offen offset:2048 sc1
U 120,120,14
U 121,121,14
U 122,122,14
U 123,123,14
U 124,124,14
U 125,125,14
U 126,126,14
U 127,127,14
K 120,120,121
K 121,122,123
K 122,124,125
K 123,126,127
buffer_store_dwordx4 v[120:123], v1, s[8:11], s93 offen offset:3072 sc1
U 128,128,15
U 129,129,15
U 130,130,15
U 131,131,15
U 132,132,15
U 133,133,15
U 134,134,15
U 135,135,15
K 128,128,129
K 129,130,131
K 130,132,133
K 131,134,135
s_add_u32 s93, s67, 393216
buffer_store_dwordx4 v[128:131], v1, s[8:11], s93 offen offset:0 sc1
s_nop 1
Zvav 128,128,216
s_add_u32 s94, s94, s48
s_lshl_b32 s94, s94, 2
s_mul_i32 s95, s51, 10240
s_add_u32 s95, s95, s48
U 136,136,15
U 137,137,15
U 138,138,15
Zvav 132,132,216
s_add_u32 m0, s95, 20480
U 139,139,15
U 140,140,15
U 141,141,15
U 142,142,15
U 143,143,15
K 136,136,137
Zvav 100,132,208
K 137,138,139
K 138,140,141
K 139,142,143
buffer_store_dwordx4 v[136:139], v1, s[8:11], s93 offen offset:1024 sc1
U 144,144,15
U 145,145,15
Zvav 68,132,200
U 146,146,15
U 147,147,15
U 148,148,15
U 149,149,15
U 150,150,15
U 151,151,15
Zvav 36,132,192
D 16,40,94,0
Ra 152,242,55296
K 144,144,145
K 145,146,147
K 146,148,149
K 147,150,151
Zvav 40,136,192
Ra 156,242,56320
Zvav 72,136,200
buffer_store_dwordx4 v[144:147], v1, s[8:11], s93 offen offset:2048 sc1
U 152,152,15
U 153,153,15
U 154,154,15
U 155,155,15
U 156,156,15
Zvav 104,136,208
U 157,157,15
U 158,158,15
U 159,159,15
K 152,152,153
K 153,154,155
K 154,156,157
Zvav 136,136,216
D 17,40,94,1024
K 155,158,159
Zvav 140,140,216
Zvav 108,140,208
D 18,40,94,2048
Zvav 76,140,200
Zvav 44,140,192
W 0
D 19,40,94,3072
s_add_u32 m0, s95, 24576
Zvav 48,144,192
Zvav 80,144,200
buffer_store_dwordx4 v[152:155], v1, s[8:11], s93 offen offset:3072 sc1
s_mov_b32 s18, 40960
s_mov_b32 s19, 73728
s_bitcmp1_b32 s14, 0
s_cselect_b32 s18, 49152, s18
s_cselect_b32 s19, 98304, s19
s_cmp_lt_u32 s14, 2
s_cselect_b32 s20, s18, s19
s_cselect_b32 s21, s19, s18
v_add_u32 v2, s20, v1
v_add_u32 v3, s21, v1
Zvav 112,144,208
s_lshl_b32 s18, s14, 9
v_lshrrev_b32 v232, 1, v1
v_add_u32 v232, s18, v232
v_bfe_u32 v12, v1, 6, 2
v_lshrrev_b32 v13, 8, v1
v_bfe_u32 v14, v1, 4, 1
v_lshlrev_b32 v14, 4, v14
Zvav 144,144,216
v_mov_b32 v15, 0x3f80
v_lshlrev_b32 v15, v14, v15
v_bfe_u32 v14, v1, 5, 1
v_xor_b32 v27, v12, v13
v_lshl_or_b32 v27, v27, 1, v14
v_cmp_eq_u32_e64 s[86:87], 0, v27
Zvav 148,148,216
buffer_load_dwordx4 v20, s[40:43], s94 offen lds
s_waitcnt vmcnt(26)
)"
R"(v_cmp_eq_u64 vcc, s[12:13], v[28:29]
s_cmp_eq_u64 vcc, -1
s_cselect_b32 s46, 79691776, 0
s_add_u32 s81, s58, 0
s_and_b32 s81, s81, 3
s_lshl_b32 s81, s81, 12
s_add_u32 s81, s81, s49
v_mov_b32 v226, 0
Zvav 116,148,208
s_add_u32 s82, s81, 131072
v_cndmask_b32_e64 v224, 0, v15, s[86:87]
v_mov_b32 v228, 0
v_mov_b32 v230, v224
v_cmp_eq_u32_e64 s[86:87], 1, v27
v_mov_b32 v227, 0
s_nop 0
v_cndmask_b32_e64 v225, 0, v15, s[86:87]
Zvav 84,148,200
v_mov_b32 v229, 0
v_mov_b32 v231, v225
G 4,160
G 8,161
G 5,164
G 9,165
Zvav 52,148,192
buffer_load_dwordx4 a[184:187], v1, s[44:47], s81 offen offset:0 sc1
G 6,168
G 10,169
G 7,172
G 11,173
A 4,4,8
Zvav 56,152,192
A 5,5,9
A 6,6,10
A 7,7,11
ds_write_b64 v232, v[6:7] offset:57344
A 8,4,5
A 9,6,7
Zvav 88,152,200
buffer_load_dwordx4 a[200:203], v1, s[44:47], s82 offen offset:0 sc1
A 8,8,9
v_cmp_ngt_f32_e64 s[86:87], s90, v8
s_lshl_b32 s19, s14, 2
v_mov_b32 v9, s19
s_or_b64 s[88:89], s[88:89], s[86:87]
s_and_b64 s[88:89], s[88:89], s[24:25]
s_cselect_b32 s18, 1, 0
Zvav 120,152,208
v_mov_b32 v8, s18
ds_write_b32 v9, v8 offset:59392
v_xor_b32 v10, 1024, v232
v_bfe_u32 v11, v1, 2, 4
K 96,96,97
K 97,98,99
Zvav 152,152,216
buffer_load_dwordx4 a[188:191], v1, s[44:47], s81 offen offset:1024 sc1
K 98,100,101
K 99,102,103
ds_write_b128 v2, v[96:99] offset:0
K 128,128,129
K 129,130,131
Zvav 156,156,216
K 130,132,133
K 131,134,135
ds_write_b128 v2, v[128:131] offset:4096
K 104,104,105
K 105,106,107
K 106,108,109
Zvav 124,156,208
buffer_load_dwordx4 a[204:207], v1, s[44:47], s82 offen offset:1024 sc1
K 107,110,111
ds_write_b128 v2, v[104:107] offset:1024
K 136,136,137
K 137,138,139
K 138,140,141
Zvav 92,156,200
K 139,142,143
ds_write_b128 v2, v[136:139] offset:5120
K 112,112,113
K 113,114,115
K 114,116,117
K 115,118,119
Zvav 60,156,192
buffer_load_dwordx4 a[192:195], v1, s[44:47], s81 offen offset:2048 sc1
ds_write_b128 v2, v[112:115] offset:2048
K 144,144,145
K 145,146,147
K 146,148,149
K 147,150,151
buffer_load_dwordx4 a[208:211], v1, s[44:47], s82 offen offset:2048 sc1
buffer_load_dwordx4 a[196:199], v1, s[44:47], s81 offen offset:3072 sc1
buffer_load_dwordx4 a[212:215], v1, s[44:47], s82 offen offset:3072 sc1
s_add_u32 s81, s58, 1
s_and_b32 s81, s81, 3
s_lshl_b32 s81, s81, 12
s_add_u32 s81, s81, s49
s_add_u32 s82, s81, 131072
buffer_load_dwordx4 a[216:219], v1, s[44:47], s81 offen offset:0 sc1
buffer_load_dwordx4 a[232:235], v1, s[44:47], s82 offen offset:0 sc1
buffer_load_dwordx4 a[220:223], v1, s[44:47], s81 offen offset:1024 sc1
buffer_load_dwordx4 a[236:239], v1, s[44:47], s82 offen offset:1024 sc1
buffer_load_dwordx4 a[224:227], v1, s[44:47], s81 offen offset:2048 sc1
buffer_load_dwordx4 a[240:243], v1, s[44:47], s82 offen offset:2048 sc1
buffer_load_dwordx4 a[228:231], v1, s[44:47], s81 offen offset:3072 sc1
buffer_load_dwordx4 a[244:247], v1, s[44:47], s82 offen offset:3072 sc1
ds_write_b128 v2, v[144:147] offset:6144
K 120,120,121
K 121,122,123
K 122,124,125
K 123,126,127
ds_write_b128 v2, v[120:123] offset:3072
K 152,152,153
K 153,154,155
K 154,156,157
K 155,158,159
ds_write_b128 v2, v[152:155] offset:7168
s_mov_b32 s70, 1
s_branch .Lend_42%=
.Lpd_31%=:
v_pk_mul_f32 v[32:33], v[32:33], s[22:23]
v_pk_mul_f32 v[34:35], v[34:35], s[22:23]
v_pk_mul_f32 v[36:37], v[36:37], s[22:23]
v_pk_mul_f32 v[38:39], v[38:39], s[22:23]
K 32,32,33
K 33,34,35
K 34,36,37
K 35,38,39
v_pk_mul_f32 v[64:65], v[64:65], s[22:23]
v_pk_mul_f32 v[66:67], v[66:67], s[22:23]
v_pk_mul_f32 v[68:69], v[68:69], s[22:23]
v_pk_mul_f32 v[70:71], v[70:71], s[22:23]
K 64,64,65
K 65,66,67
K 66,68,69
K 67,70,71
v_pk_mul_f32 v[96:97], v[96:97], s[22:23]
v_pk_mul_f32 v[98:99], v[98:99], s[22:23]
v_pk_mul_f32 v[100:101], v[100:101], s[22:23]
v_pk_mul_f32 v[102:103], v[102:103], s[22:23]
K 96,96,97
K 97,98,99
K 98,100,101
K 99,102,103
v_pk_mul_f32 v[128:129], v[128:129], s[22:23]
v_pk_mul_f32 v[130:131], v[130:131], s[22:23]
v_pk_mul_f32 v[132:133], v[132:133], s[22:23]
v_pk_mul_f32 v[134:135], v[134:135], s[22:23]
K 128,128,129
K 129,130,131
K 130,132,133
K 131,134,135
v_pk_mul_f32 v[40:41], v[40:41], s[22:23]
v_pk_mul_f32 v[42:43], v[42:43], s[22:23]
v_pk_mul_f32 v[44:45], v[44:45], s[22:23]
v_pk_mul_f32 v[46:47], v[46:47], s[22:23]
v_pk_mul_f32 v[72:73], v[72:73], s[22:23]
v_pk_mul_f32 v[74:75], v[74:75], s[22:23]
v_pk_mul_f32 v[76:77], v[76:77], s[22:23]
v_pk_mul_f32 v[78:79], v[78:79], s[22:23]
v_pk_mul_f32 v[104:105], v[104:105], s[22:23]
v_pk_mul_f32 v[106:107], v[106:107], s[22:23]
v_pk_mul_f32 v[108:109], v[108:109], s[22:23]
v_pk_mul_f32 v[110:111], v[110:111], s[22:23]
v_pk_mul_f32 v[136:137], v[136:137], s[22:23]
v_pk_mul_f32 v[138:139], v[138:139], s[22:23]
v_pk_mul_f32 v[140:141], v[140:141], s[22:23]
v_pk_mul_f32 v[142:143], v[142:143], s[22:23]
v_pk_mul_f32 v[48:49], v[48:49], s[22:23]
v_pk_mul_f32 v[50:51], v[50:51], s[22:23]
v_pk_mul_f32 v[52:53], v[52:53], s[22:23]
v_pk_mul_f32 v[54:55], v[54:55], s[22:23]
v_pk_mul_f32 v[80:81], v[80:81], s[22:23]
v_pk_mul_f32 v[82:83], v[82:83], s[22:23]
v_pk_mul_f32 v[84:85], v[84:85], s[22:23]
v_pk_mul_f32 v[86:87], v[86:87], s[22:23]
v_pk_mul_f32 v[112:113], v[112:113], s[22:23]
v_pk_mul_f32 v[114:115], v[114:115], s[22:23]
v_pk_mul_f32 v[116:117], v[116:117], s[22:23]
v_pk_mul_f32 v[118:119], v[118:119], s[22:23]
v_pk_mul_f32 v[144:145], v[144:145], s[22:23]
v_pk_mul_f32 v[146:147], v[146:147], s[22:23]
v_pk_mul_f32 v[148:149], v[148:149], s[22:23]
v_pk_mul_f32 v[150:151], v[150:151], s[22:23]
v_pk_mul_f32 v[56:57], v[56:57], s[22:23]
v_pk_mul_f32 v[58:59], v[58:59], s[22:23]
v_pk_mul_f32 v[60:61], v[60:61], s[22:23]
v_pk_mul_f32 v[62:63], v[62:63], s[22:23]
v_pk_mul_f32 v[88:89], v[88:89], s[22:23]
v_pk_mul_f32 v[90:91], v[90:91], s[22:23]
v_pk_mul_f32 v[92:93], v[92:93], s[22:23]
v_pk_mul_f32 v[94:95], v[94:95], s[22:23]
v_pk_mul_f32 v[120:121], v[120:121], s[22:23]
v_pk_mul_f32 v[122:123], v[122:123], s[22:23]
v_pk_mul_f32 v[124:125], v[124:125], s[22:23]
v_pk_mul_f32 v[126:127], v[126:127], s[22:23]
v_pk_mul_f32 v[152:153], v[152:153], s[22:23]
v_pk_mul_f32 v[154:155], v[154:155], s[22:23]
v_pk_mul_f32 v[156:157], v[156:157], s[22:23]
v_pk_mul_f32 v[158:159], v[158:159], s[22:23]
W 0
Zvav 196,128,32
Ra 144,242,4096
Ra 148,242,5120
Ra 152,242,6144
Ra 156,242,7168
Zvav 204,128,64
K 40,40,41
K 41,42,43
K 42,44,45
K 43,46,47
Zvav 212,128,96
K 72,72,73
K 73,74,75
K 74,76,77
K 75,78,79
Zvav 220,128,128
K 104,104,105
K 105,106,107
K 106,108,109
K 107,110,111
K 136,136,137
K 137,138,139
K 138,140,141
K 139,142,143
Mvav 196,132,40
Ra 128,242,8192
K 48,48,49
Mvav 204,132,72
K 49,50,51
K 50,52,53
K 51,54,55
K 80,80,81
Mvav 212,132,104
K 81,82,83
K 82,84,85
K 83,86,87
K 112,112,113
Mvav 220,132,136
K 113,114,115
K 114,116,117
K 115,118,119
K 144,144,145
K 145,146,147
K 146,148,149
K 147,150,151
Mvav 196,136,48
Ra 132,242,9216
K 56,56,57
Mvav 204,136,80
K 57,58,59
K 58,60,61
K 59,62,63
K 88,88,89
Mvav 212,136,112
K 89,90,91
K 90,92,93
K 91,94,95
K 120,120,121
Mvav 220,136,144
K 121,122,123
K 122,124,125
K 123,126,127
K 152,152,153
K 153,154,155
K 154,156,157
K 155,158,159
Mvav 196,140,56
Ra 136,242,10240
v_accvgpr_write_b32 a0, v32
Mvav 204,140,88
v_accvgpr_write_b32 a1, v33
v_accvgpr_write_b32 a2, v34
v_accvgpr_write_b32 a3, v35
Mvav 212,140,120
v_accvgpr_write_b32 a4, v40
v_accvgpr_write_b32 a5, v41
v_accvgpr_write_b32 a6, v42
Mvav 220,140,152
W 3
v_accvgpr_write_b32 a7, v43
v_accvgpr_write_b32 a8, v48
v_accvgpr_write_b32 a9, v49
v_accvgpr_write_b32 a10, v50
Zvav 68,144,64
Ra 140,242,11264
v_accvgpr_write_b32 a11, v51
v_accvgpr_write_b32 a12, v56
v_accvgpr_write_b32 a13, v57
Zvav 100,144,96
v_accvgpr_write_b32 a14, v58
v_accvgpr_write_b32 a15, v59
v_accvgpr_write_b32 a16, v64
v_cndmask_b32_e64 v196, v241, v196, s[72:73]
Zvav 132,144,128
v_cndmask_b32_e64 v197, v241, v197, s[74:75]
v_cndmask_b32_e64 v198, v241, v198, s[76:77]
v_cndmask_b32_e64 v199, v241, v199, s[78:79]
v_max3_f32 v4, v196, v197, v198
Mvav 68,148,72
v_max3_f32 v4, v4, v199, v252
v_max3_f32 v5, v204, v205, v206
v_max3_f32 v5, v5, v207, v253
v_max3_f32 v6, v212, v213, v214
Mvav 100,148,104
v_max3_f32 v6, v6, v215, v254
v_max3_f32 v7, v220, v221, v222
v_max3_f32 v7, v7, v223, v255
v_mov_b32 v8, v4
Mvav 132,148,136
v_mov_b32 v9, v5
v_mov_b32 v10, v6
v_mov_b32 v11, v7
v_accvgpr_write_b32 a17, v65
Mvav 68,152,80
v_permlane32_swap_b32 v4, v8
v_permlane32_swap_b32 v5, v9
v_permlane32_swap_b32 v6, v10
v_permlane32_swap_b32 v7, v11
Mvav 100,152,112
v_max_f32 v4, v4, v8
v_max_f32 v5, v5, v9
v_max_f32 v6, v6, v10
v_max_f32 v7, v7, v11
Mvav 132,152,144
v_mov_b32 v8, v4
v_mov_b32 v9, v5
v_mov_b32 v10, v6
v_mov_b32 v11, v7
Mvav 68,156,88
v_permlane16_swap_b32 v4, v8
v_permlane16_swap_b32 v5, v9
v_permlane16_swap_b32 v6, v10
v_accvgpr_write_b32 a18, v66
Mvav 100,156,120
v_permlane16_swap_b32 v7, v11
v_max_f32 v4, v4, v8
v_max_f32 v5, v5, v9
v_max_f32 v6, v6, v10
v_max_f32 v7, v7, v11
v_sub_f32 v4, 0, v4
v_sub_f32 v5, 0, v5
v_sub_f32 v6, 0, v6
v_sub_f32 v7, 0, v7
v_mov_b32 v224, v4
v_mov_b32 v225, v4
v_mov_b32 v226, v4
v_mov_b32 v227, v4
v_mov_b32 v228, v5
v_mov_b32 v229, v5
v_mov_b32 v230, v5
v_mov_b32 v231, v5
v_mov_b32 v232, v6
v_mov_b32 v233, v6
v_mov_b32 v234, v6
v_mov_b32 v235, v6
v_mov_b32 v236, v7
v_mov_b32 v237, v7
v_mov_b32 v238, v7
v_mov_b32 v239, v7
Mvav 132,156,152
W 0
Ra 144,242,12288
v_accvgpr_write_b32 a19, v67
v_accvgpr_write_b32 a20, v72
v_accvgpr_write_b32 a21, v73
Cvav 168,128,96,232
s_and_b32 s80, s14, 1
v_accvgpr_write_b32 a22, v74
v_accvgpr_write_b32 a23, v75
v_accvgpr_write_b32 a24, v80
v_accvgpr_write_b32 a25, v81
Cvav 172,128,128,236
s_lshl_b32 s80, s80, 14
v_accvgpr_write_b32 a26, v82
v_accvgpr_write_b32 a27, v83
v_accvgpr_write_b32 a28, v88
v_accvgpr_write_b32 a29, v89
Mvav 168,132,104
Ra 148,242,13312
Ra 152,242,14336
Ra 156,242,15360
v_accvgpr_write_b32 a30, v90
Mvav 172,132,136
s_add_u32 s80, s80, 98304
v_accvgpr_write_b32 a31, v91
v_accvgpr_write_b32 a32, v96
v_accvgpr_write_b32 a33, v97
v_accvgpr_write_b32 a34, v98
Mvav 168,136,112
v_accvgpr_write_b32 a35, v99
v_accvgpr_write_b32 a36, v104
v_accvgpr_write_b32 a37, v105
v_accvgpr_write_b32 a38, v106
Mvav 172,136,144
v_accvgpr_write_b32 a39, v107
v_accvgpr_write_b32 a40, v112
v_accvgpr_write_b32 a41, v113
v_accvgpr_write_b32 a42, v114
Mvav 168,140,120
Ra 128,242,24576
)"
R"(Ra 132,242,25600
Ra 136,242,26624
v_accvgpr_write_b32 a43, v115
Mvav 172,140,152
Ra 140,242,27648
v_accvgpr_write_b32 a44, v120
v_accvgpr_write_b32 a45, v121
W 4
Cvav 188,144,128,236
v_accvgpr_write_b32 a46, v122
v_accvgpr_write_b32 a47, v123
v_accvgpr_write_b32 a48, v128
v_accvgpr_write_b32 a49, v129
v_accvgpr_write_b32 a50, v130
v_accvgpr_write_b32 a51, v131
v_accvgpr_write_b32 a52, v136
v_accvgpr_write_b32 a53, v137
v_accvgpr_write_b32 a54, v138
v_accvgpr_write_b32 a55, v139
v_accvgpr_write_b32 a56, v144
v_accvgpr_write_b32 a57, v145
v_accvgpr_write_b32 a58, v146
v_accvgpr_write_b32 a59, v147
v_accvgpr_write_b32 a60, v152
v_accvgpr_write_b32 a61, v153
v_accvgpr_write_b32 a62, v154
v_accvgpr_write_b32 a63, v155
Mvav 188,148,136
s_barrier
v_add_u32 v8, s80, v1
A 196,196,224
A 197,197,224
A 198,198,224
Mvav 188,152,144
A 199,199,224
X 196
X 197
X 198
K 192,196,197
X 199
A 204,204,228
K 193,198,199
A 205,205,228
A 206,206,228
A 207,207,228
X 204
X 205
X 206
K 200,204,205
X 207
A 212,212,232
K 201,206,207
A 213,213,232
A 214,214,232
A 215,215,232
X 212
X 213
X 214
K 208,212,213
X 215
A 220,220,236
K 209,214,215
A 221,221,236
A 222,222,236
A 223,223,236
X 220
X 221
X 222
K 216,220,221
X 223
v_mov_b32 v194, 0
K 217,222,223
v_mov_b32 v195, 0
v_cndmask_b32_e64 v68, v241, v68, s[72:73]
v_cndmask_b32_e64 v69, v241, v69, s[74:75]
v_cndmask_b32_e64 v70, v241, v70, s[76:77]
v_cndmask_b32_e64 v71, v241, v71, s[78:79]
A 68,68,228
A 69,69,228
A 70,70,228
A 71,71,228
X 68
X 69
X 70
K 202,68,69
X 71
A 100,100,232
K 203,70,71
A 101,101,232
A 102,102,232
A 103,103,232
X 100
X 101
X 102
K 210,100,101
X 103
A 132,132,236
K 211,102,103
A 133,133,236
A 134,134,236
A 135,135,236
X 132
X 133
X 134
K 218,132,133
X 135
s_nop 0
K 219,134,135
Mvav 188,156,152
W 0
Ra 144,242,28672
Ra 148,242,29696
Ra 152,242,30720
Zvav 32,128,192
Ra 156,242,31744
Zvav 64,128,200
Zvav 96,128,208
s_and_b32 s80, s14, 1
s_xor_b32 s62, s62, 0x10000
Zvav 128,128,216
s_lshl_b32 s80, s80, 14
s_add_u32 s80, s80, 131072
v_add_u32 v23, 61440, v21
Zvav 132,132,216
v_add_u32 v12, s80, v1
Ra 64,12,8192
Ra 68,12,9216
v_add_u32 v24, 61440, v22
Zvav 100,132,208
Ra 72,12,10240
Ra 76,12,11264
Ra 80,12,12288
v_add_u32 v25, 122880, v21
Zvav 68,132,200
Ra 84,12,13312
Ra 88,12,14336
Ra 92,12,15360
v_add_u32 v26, 122880, v22
Zvav 36,132,192
Ra 96,12,0
Ra 100,12,1024
Ra 104,12,2048
Zvav 40,136,192
Ra 108,12,3072
Ra 112,12,4096
Ra 116,12,5120
Zvav 72,136,200
Ra 120,12,6144
Ra 124,12,7168
Zvav 104,136,208
Zvav 136,136,216
Zvav 140,140,216
Zvav 108,140,208
Zvav 76,140,200
s_waitcnt vmcnt(0) lgkmcnt(0)
s_barrier
v_xor_b32 v242, 0x10000, v242
ds_read_b128 v[248:251], v8
Zvav 44,140,192
s_add_u32 s80, s71, 128
v_mov_b64 v[28:29], s[12:13]
s_mov_b64 exec, 1
buffer_store_dwordx2 v[28:29], off, s[8:11], s80 sc1
s_mov_b64 exec, -1
Ra 128,242,40960
Zvav 48,144,192
Zvav 80,144,200
X 172
X 173
Zvav 112,144,208
Ra 132,242,41984
X 174
K 220,172,173
Zvav 144,144,216
X 175
v_cndmask_b32_e64 v168, v241, v168, s[72:73]
K 221,174,175
Zvav 148,148,216
v_cndmask_b32_e64 v169, v241, v169, s[74:75]
v_cndmask_b32_e64 v170, v241, v170, s[76:77]
v_cndmask_b32_e64 v171, v241, v171, s[78:79]
Zvav 116,148,208
X 168
X 169
Zvav 84,148,200
W 2
Ra 136,242,43008
X 170
K 212,168,169
Zvav 52,148,192
v_mov_b32 v224, v250
v_mov_b32 v225, v250
v_mov_b32 v226, v250
v_mov_b32 v227, v250
Zvav 56,152,192
v_mov_b32 v228, v251
v_mov_b32 v229, v251
v_mov_b32 v230, v251
v_mov_b32 v231, v251
Zvav 88,152,200
v_mov_b32 v232, v248
v_mov_b32 v233, v248
v_mov_b32 v234, v248
v_mov_b32 v235, v248
Zvav 120,152,208
v_mov_b32 v236, v249
v_mov_b32 v237, v249
v_mov_b32 v238, v249
v_mov_b32 v239, v249
Zvav 152,152,216
X 171
v_mov_b32 v204, 0
K 213,170,171
Zvav 156,156,216
Ra 140,242,44032
v_mov_b32 v205, 0
v_mov_b32 v196, 0
v_mov_b32 v197, 0
Zvav 124,156,208
v_mov_b32 v198, 0
v_mov_b32 v199, 0
v_mov_b32 v206, 0
v_mov_b32 v207, 0
Zvav 92,156,200
v_mov_b32 v214, 0
v_mov_b32 v215, 0
v_cndmask_b32_e64 v188, v241, v188, s[72:73]
v_cndmask_b32_e64 v189, v241, v189, s[74:75]
Zvav 60,156,192
v_cndmask_b32_e64 v190, v241, v190, s[76:77]
v_cndmask_b32_e64 v191, v241, v191, s[78:79]
X 188
X 189
X 190
K 222,188,189
X 191
s_nop 0
K 223,190,191
s_nop 1
s_branch .Lsp2_36%=
.Lmoe_43%=:
W 0
Cvaa 160,128,0,224
s_cmp_eq_u32 s14, 0
s_cselect_b32 s20, s30, s32
s_cselect_b32 s21, s31, s33
Ra 144,242,4096
Ra 148,242,5120
Cvaa 164,128,16,228
s_xor_b32 s62, s62, 0x10000
Ra 152,242,6144
Cvaa 168,128,32,232
X 184
Cvaa 172,128,48,236
s_and_b32 s21, s21, 0xffff
X 185
Mvaa 160,132,4
X 186
Mvaa 164,132,20
Ra 156,242,7168
K 214,184,185
Mvaa 168,132,36
s_mov_b32 s22, 2211840
X 187
Mvaa 172,132,52
X 188
Mvaa 160,136,8
Ra 128,242,16384
Mvaa 164,136,24
s_mov_b32 s23, 0x20000
K 215,186,187
Mvaa 168,136,40
X 189
Mvaa 172,136,56
X 190
Mvaa 160,140,12
Ra 132,242,17408
v_lshrrev_b32 v4, 6, v1
Mvaa 164,140,28
K 222,188,189
Mvaa 168,140,44
X 191
Mvaa 172,140,60
v_mul_u32_u24 v4, 3456, v4
K 223,190,191
v_smfmac_f32_16x16x64_bf16 a[160:163], a[176:179], v[192:199], v240
Ra 136,242,18432
v_smfmac_f32_16x16x64_bf16 a[164:167], a[176:179], v[200:207], v240
X 160
v_smfmac_f32_16x16x64_bf16 a[168:171], a[176:179], v[208:215], v240
v_lshrrev_b32 v5, 4, v1
v_smfmac_f32_16x16x64_bf16 a[172:175], a[176:179], v[216:223], v240
W 3
X 161
Cvaa 176,144,0,224
X 162
Cvaa 180,144,16,228
Ra 140,242,19456
v_xor_b32 v5, v5, v1
Cvaa 184,144,32,232
K 192,160,161
Cvaa 188,144,48,236
X 163
Mvaa 176,148,4
Ra 144,242,20480
v_and_b32 v5, 32, v5
Mvaa 180,148,20
X 164
Mvaa 184,148,36
K 193,162,163
v_and_b32 v6, 16, v1
Mvaa 188,148,52
X 165
Mvaa 176,152,8
X 166
Mvaa 180,152,24
Ra 148,242,21504
v_add3_u32 v4, v4, v5, v6
Mvaa 184,152,40
K 200,164,165
Mvaa 188,152,56
X 167
Mvaa 176,156,12
X 168
Mvaa 180,156,28
Ra 152,242,22528
v_add_u32 v5, 54272, v4
Mvaa 184,156,44
K 201,166,167
v_add_u32 v6, 108544, v4
Mvaa 188,156,60
W 3
X 169
Mvav 32,128,196
X 170
Mvav 64,128,204
Ra 156,242,23552
v_add_u32 v7, 162816, v4
Mvav 96,128,212
K 208,168,169
Mvav 128,128,220
X 171
Mvav 132,132,220
s_lshl_b32 s94, s85, 6
X 172
Mvav 100,132,212
s_add_u32 s64, s64, 1
Ra 128,242,8192
K 209,170,171
Mvav 68,132,204
X 173
Mvav 36,132,196
s_mul_i32 s95, s14, 20480
Mvav 40,136,196
X 174
Mvav 72,136,204
Ra 132,242,9216
K 216,172,173
Mvav 104,136,212
s_add_u32 s95, s95, 98304
X 175
Mvav 136,136,220
Mvav 140,140,220
s_add_u32 m0, s95, 0
X 176
Mvav 108,140,212
Ra 136,242,10240
K 217,174,175
Mvav 76,140,204
X 177
Mvav 44,140,196
W 3
D 4,20,94,0
Mvav 48,144,196
X 178
Mvav 80,144,204
Ra 140,242,11264
Mvav 112,144,212
D 5,20,94,1024
K 194,176,177
Mvav 144,144,220
X 179
Mvav 148,148,220
X 180
Mvav 116,148,212
Ra 144,242,12288
D 6,20,94,2048
Mvav 84,148,204
K 195,178,179
Mvav 52,148,196
X 181
Mvav 56,152,196
D 7,20,94,3072
Ra 148,242,13312
Mvav 88,152,204
X 182
Mvav 120,152,212
s_add_u32 s94, s94, 221184
K 202,180,181
Mvav 152,152,220
)"
R"(X 183
Mvav 156,156,220
X 184
Mvav 124,156,212
s_add_u32 m0, s95, 4096
Ra 152,242,14336
Mvav 92,156,204
K 203,182,183
Mvav 60,156,196
W 3
X 185
Cvaa 160,128,0,224
D 4,20,94,0
Ra 156,242,15360
Cvaa 164,128,16,228
X 186
Cvaa 168,128,32,232
K 210,184,185
Cvaa 172,128,48,236
X 187
Mvaa 160,132,4
D 5,20,94,1024
Ra 128,242,24576
Mvaa 164,132,20
X 188
Mvaa 168,132,36
D 6,20,94,2048
K 211,186,187
Mvaa 172,132,52
X 189
Mvaa 160,136,8
X 190
Mvaa 164,136,24
Ra 132,242,25600
D 7,20,94,3072
Mvaa 168,136,40
K 218,188,189
Mvaa 172,136,56
s_add_u32 s94, s94, 221184
X 191
Mvaa 160,140,12
Ra 136,242,26624
Mvaa 164,140,28
Mvaa 168,140,44
s_add_u32 m0, s95, 8192
Mvaa 172,140,60
W 3
Cvaa 176,144,0,224
Ra 140,242,27648
Cvaa 180,144,16,228
X 160
Cvaa 184,144,32,232
D 4,20,94,0
K 219,190,191
Cvaa 188,144,48,236
X 161
Mvaa 176,148,4
X 162
Mvaa 180,148,20
D 5,20,94,1024
K 196,160,161
Mvaa 184,148,36
X 163
Mvaa 188,148,52
Ra 144,242,28672
D 6,20,94,2048
Mvaa 176,152,8
X 164
Mvaa 180,152,24
Ra 148,242,29696
K 197,162,163
Mvaa 184,152,40
X 165
Mvaa 188,152,56
X 166
Mvaa 176,156,12
D 7,20,94,3072
K 204,164,165
Mvaa 180,156,28
s_add_u32 s94, s94, 221184
X 167
Mvaa 184,156,44
Ra 152,242,30720
Mvaa 188,156,60
W 3
X 168
Mvav 32,128,192
s_add_u32 m0, s95, 12288
Ra 156,242,31744
K 205,166,167
Mvav 64,128,200
X 169
Mvav 96,128,208
X 170
Mvav 128,128,216
D 4,20,94,0
K 212,168,169
Mvav 132,132,216
X 171
Mvav 100,132,208
D 5,20,94,1024
Mvav 68,132,200
X 172
Mvav 36,132,192
K 213,170,171
Mvav 40,136,192
X 173
Mvav 72,136,200
D 6,20,94,2048
Mvav 104,136,208
X 174
Mvav 136,136,216
D 7,20,94,3072
K 220,172,173
Mvav 140,140,216
X 175
Mvav 108,140,208
s_add_u32 s94, s94, 221184
X 176
Mvav 76,140,200
W 0
s_barrier
v_xor_b32 v242, 0x10000, v242
Mvav 44,140,192
Ra 128,242,0
K 221,174,175
Mvav 48,144,192
X 177
Mvav 80,144,200
v_bfe_u32 v11, v1, 4, 3
Mvav 112,144,208
X 178
Mvav 144,144,216
Ra 132,242,1024
v_lshlrev_b32 v11, 3, v11
Mvav 148,148,216
K 198,176,177
Mvav 116,148,208
X 179
Mvav 84,148,200
X 180
Mvav 52,148,192
Ra 136,242,2048
buffer_load_dwordx2 v[8:9], v11, s[8:11], 0 offen offset:3840 sc1
Mvav 56,152,192
Ra 140,242,3072
K 199,178,179
Mvav 88,152,200
X 181
Mvav 120,152,208
X 182
Mvav 152,152,216
K 206,180,181
Mvav 156,156,216
X 183
Mvav 124,156,208
Mvav 92,156,200
K 207,182,183
Mvav 60,156,192
W 0
Cvaa 160,128,0,224
s_add_u32 m0, s95, 16384
Ra 144,242,4096
Ra 148,242,5120
Cvaa 164,128,16,228
s_xor_b32 s62, s62, 0x10000
Ra 152,242,6144
Cvaa 168,128,32,232
X 184
Cvaa 172,128,48,236
X 185
Mvaa 160,132,4
X 186
Mvaa 164,132,20
D 4,20,94,0
K 214,184,185
Mvaa 168,132,36
Ra 156,242,7168
Mvaa 172,132,52
X 187
Mvaa 160,136,8
X 188
Mvaa 164,136,24
D 5,20,94,1024
Ra 128,242,16384
Mvaa 168,136,40
K 215,186,187
Mvaa 172,136,56
X 189
Mvaa 160,140,12
X 190
Mvaa 164,140,28
D 6,20,94,2048
Ra 132,242,17408
Mvaa 168,140,44
K 222,188,189
Mvaa 172,140,60
X 191
v_smfmac_f32_16x16x64_bf16 a[160:163], a[176:179], v[192:199], v240
Ra 136,242,18432
K 223,190,191
v_smfmac_f32_16x16x64_bf16 a[164:167], a[176:179], v[200:207], v240
D 7,20,94,3072
v_smfmac_f32_16x16x64_bf16 a[168:171], a[176:179], v[208:215], v240
s_add_u32 s94, s94, 221184
v_mul_u32_u24 v10, 160, v11
v_smfmac_f32_16x16x64_bf16 a[172:175], a[176:179], v[216:223], v240
W 3
X 160
Cvaa 176,144,0,224
X 161
Cvaa 180,144,16,228
Ra 140,242,19456
v_lshrrev_b32 v12, 7, v1
Cvaa 184,144,32,232
X 162
Cvaa 188,144,48,236
K 192,160,161
v_lshl_add_u32 v10, v12, 4, v10
Mvaa 176,148,4
s_mul_i32 s82, s14, 5120
X 163
Mvaa 180,148,20
X 164
Mvaa 184,148,36
Ra 144,242,20480
K 193,162,163
Mvaa 188,148,52
s_mul_i32 s81, s14, 640
X 165
Mvaa 176,152,8
X 166
Mvaa 180,152,24
s_add_u32 s81, s81, 118784
Ra 148,242,21504
K 200,164,165
Mvaa 184,152,40
X 167
Mvaa 188,152,56
X 168
Mvaa 176,156,12
s_waitcnt vmcnt(4)
v_cmp_eq_u64 vcc, s[12:13], v[8:9]
s_cmp_eq_u64 vcc, -1
s_cbranch_scc0 .Lspin_49%=
.Lok_50%=:
Ra 152,242,22528
Mvaa 180,156,28
K 201,166,167
Mvaa 184,156,44
s_add_u32 m0, s82, 53248
X 169
Mvaa 188,156,60
W 3
Mvav 32,128,196
X 170
Mvav 64,128,204
Ra 156,242,23552
Dc 10,8,81,0
Mvav 96,128,212
K 208,168,169
Mvav 128,128,220
X 171
Mvav 132,132,220
s_add_u32 m0, s82, 54144
X 172
Mvav 100,132,212
s_add_u32 s64, s64, 1
Ra 128,242,8192
K 209,170,171
Mvav 68,132,204
X 173
Mvav 36,132,196
Dc 10,8,81,128
Mvav 40,136,196
X 174
Mvav 72,136,204
s_add_u32 m0, s82, 55040
Ra 132,242,9216
K 216,172,173
Mvav 104,136,212
X 175
Mvav 136,136,220
Dc 10,8,81,256
Mvav 140,140,220
X 176
Mvav 108,140,212
Ra 136,242,10240
K 217,174,175
Mvav 76,140,204
s_add_u32 m0, s82, 55936
X 177
Mvav 44,140,196
W 3
Mvav 48,144,196
X 178
Mvav 80,144,204
Ra 140,242,11264
Dc 10,8,81,384
Mvav 112,144,212
K 194,176,177
Mvav 144,144,220
s_add_u32 m0, s82, 56832
X 179
Mvav 148,148,220
X 180
Mvav 116,148,212
Ra 144,242,12288
Mvav 84,148,204
Dc 10,8,81,512
K 195,178,179
Mvav 52,148,196
X 181
Mvav 56,152,196
X 182
Mvav 88,152,204
Ra 148,242,13312
Mvav 120,152,212
K 202,180,181
Mvav 152,152,220
X 183
Mvav 156,156,220
X 184
Mvav 124,156,212
Ra 152,242,14336
Mvav 92,156,204
K 203,182,183
Mvav 60,156,196
W 3
X 185
Cvaa 160,128,0,224
X 186
Cvaa 164,128,16,228
Ra 156,242,15360
Cvaa 168,128,32,232
K 210,184,185
Cvaa 172,128,48,236
X 187
Mvaa 160,132,4
X 188
Mvaa 164,132,20
Ra 128,242,24576
Mvaa 168,132,36
K 211,186,187
Mvaa 172,132,52
X 189
Mvaa 160,136,8
X 190
Mvaa 164,136,24
Ra 132,242,25600
Mvaa 168,136,40
K 218,188,189
Mvaa 172,136,56
X 191
Mvaa 160,140,12
Ra 136,242,26624
Mvaa 164,140,28
Mvaa 168,140,44
Mvaa 172,140,60
W 3
Cvaa 176,144,0,224
Ra 140,242,27648
v_lshrrev_b32 v8, 8, v1
Cvaa 180,144,16,228
X 160
Cvaa 184,144,32,232
K 219,190,191
Cvaa 188,144,48,236
X 161
Mvaa 176,148,4
X 162
Mvaa 180,148,20
K 196,160,161
v_bfe_u32 v9, v1, 6, 2
Mvaa 184,148,36
X 163
Mvaa 188,148,52
Ra 144,242,28672
v_lshl_add_u32 v9, v8, 3, v9
Mvaa 176,152,8
X 164
Mvaa 180,152,24
Ra 148,242,29696
K 197,162,163
Mvaa 184,152,40
X 165
Mvaa 188,152,56
X 166
Mvaa 176,156,12
K 204,164,165
v_lshlrev_b32 v11, 6, v9
Mvaa 180,156,28
X 167
Mvaa 184,156,44
Ra 152,242,30720
v_xor_b32 v9, s14, v8
Mvaa 188,156,60
W 3
X 168
Mvav 32,128,192
)"
R"(Ra 156,242,31744
K 205,166,167
Mvav 64,128,200
X 169
Mvav 96,128,208
X 170
Mvav 128,128,216
v_and_b32 v9, 1, v9
K 212,168,169
Mvav 132,132,216
X 171
Mvav 100,132,208
v_lshl_add_u32 v11, v9, 5, v11
Mvav 68,132,200
X 172
Mvav 36,132,192
K 213,170,171
Mvav 40,136,192
X 173
Mvav 72,136,200
v_bfe_u32 v9, v1, 4, 2
Mvav 104,136,208
X 174
Mvav 136,136,216
K 220,172,173
v_lshl_add_u32 v12, v9, 3, v11
Mvav 140,140,216
X 175
Mvav 108,140,208
X 176
Mvav 76,140,200
s_waitcnt vmcnt(0) lgkmcnt(0)
s_barrier
v_xor_b32 v242, 0x10000, v242
Mvav 44,140,192
v_add_u32 v11, 98304, v12
K 221,174,175
Mvav 48,144,192
Ra 128,242,0
Mvav 80,144,200
X 177
Mvav 112,144,208
X 178
Mvav 144,144,216
Ra 132,242,1024
v_and_b32 v13, 0x70, v1
Mvav 148,148,216
K 198,176,177
Mvav 116,148,208
X 179
Mvav 84,148,200
Ra 136,242,2048
v_lshl_add_u32 v13, v8, 7, v13
Mvav 52,148,192
X 180
Mvav 56,152,192
Ra 140,242,3072
K 199,178,179
Mvav 88,152,200
X 181
Mvav 120,152,208
X 182
Mvav 152,152,216
K 206,180,181
Mvav 156,156,216
X 183
Mvav 124,156,208
Mvav 92,156,200
K 207,182,183
Mvav 60,156,192
W 0
Cvaa 160,128,0,224
s_cmp_eq_u32 s14, 0
s_cselect_b32 s95, 106496, 0
Ra 144,242,4096
Ra 148,242,5120
Cvaa 164,128,16,228
s_xor_b32 s62, s62, 0x10000
X 184
Cvaa 168,128,32,232
Ta 184,11,0
Ta 186,11,256
Cvaa 172,128,48,236
s_add_u32 s95, s95, 32768
Ta 188,11,20480
Ta 190,11,20736
Mvaa 160,132,4
Ra 192,13,53248
Ta 196,11,2048
Mvaa 164,132,20
s_add_u32 m0, s95, 0
X 185
Mvaa 168,132,36
Ta 198,11,2304
Ta 200,11,22528
Mvaa 172,132,52
W 3
X 186
Zaaa 232,184,192
Ta 202,11,22784
K 214,184,185
Zaaa 236,188,192
Ra 204,13,53760
Ra 152,242,6144
Mvaa 160,136,8
X 187
Mvaa 164,136,24
Ta 208,11,4096
D 4,20,94,0
Mvaa 168,136,40
X 188
Mvaa 172,136,56
Ta 210,11,4352
D 5,20,94,1024
Mvaa 160,140,12
Ra 156,242,7168
K 215,186,187
Mvaa 164,140,28
X 189
Mvaa 168,140,44
Ta 212,11,24576
D 6,20,94,2048
Mvaa 172,140,60
X 190
v_smfmac_f32_16x16x64_bf16 a[160:163], a[176:179], v[192:199], v240
W 5
Ta 214,11,24832
D 7,20,94,3072
Maaa 232,196,204
s_add_u32 s94, s94, 221184
Ra 216,13,54272
K 222,188,189
Maaa 236,200,204
s_add_u32 m0, s95, 4096
X 191
v_smfmac_f32_16x16x64_bf16 a[164:167], a[176:179], v[200:207], v240
X 160
K 223,190,191
v_smfmac_f32_16x16x64_bf16 a[168:171], a[176:179], v[208:215], v240
Ra 128,242,16384
Ta 220,11,6144
v_smfmac_f32_16x16x64_bf16 a[172:175], a[176:179], v[216:223], v240
W 5
Ta 222,11,6400
Ra 132,242,17408
Cvaa 176,144,0,224
X 161
Cvaa 180,144,16,228
D 4,20,94,0
Ta 224,11,26624
Cvaa 184,144,32,232
X 162
Cvaa 188,144,48,236
D 5,20,94,1024
K 192,160,161
Mvaa 176,148,4
Ta 226,11,26880
Ra 228,13,54784
Mvaa 180,148,20
W 7
X 163
Maaa 232,208,216
D 6,20,94,2048
Ra 136,242,18432
Maaa 236,212,216
X 164
Mvaa 184,148,36
D 7,20,94,3072
Ta 184,11,8192
Mvaa 188,148,52
s_add_u32 s94, s94, 221184
Ra 140,242,19456
K 193,162,163
Mvaa 176,152,8
s_add_u32 m0, s95, 8192
X 165
Mvaa 180,152,24
Ta 186,11,8448
Ta 188,11,28672
Mvaa 184,152,40
X 166
Mvaa 188,152,56
Ra 144,242,20480
K 200,164,165
Mvaa 176,156,12
X 167
Mvaa 180,156,28
Ta 190,11,28928
D 4,20,94,0
Mvaa 184,156,44
W 7
X 168
Maaa 232,220,228
s_add_u32 s64, s64, 1
Ra 192,13,55296
D 5,20,94,1024
Maaa 236,224,228
Ta 196,11,10240
K 201,166,167
Mvaa 188,156,60
W 6
X 169
Mvav 32,128,196
D 6,20,94,2048
Ra 148,242,21504
Mvav 64,128,204
X 170
Mvav 96,128,212
Ta 198,11,10496
D 7,20,94,3072
Mvav 128,128,220
s_add_u32 s94, s94, 221184
Ta 200,11,30720
K 208,168,169
Mvav 132,132,220
s_add_u32 m0, s95, 12288
X 171
Mvav 100,132,212
Ra 152,242,22528
Ta 202,11,30976
Mvav 68,132,204
X 172
Mvav 36,132,196
W 6
Ra 204,13,55808
K 209,170,171
Maaa 232,184,192
X 173
Maaa 236,188,192
D 4,20,94,0
Ra 156,242,23552
Mvav 40,136,196
X 174
Mvav 72,136,204
Ta 208,11,12288
D 5,20,94,1024
Mvav 104,136,212
Ta 210,11,12544
K 216,172,173
Mvav 136,136,220
X 175
Mvav 140,140,220
D 6,20,94,2048
Ra 128,242,8192
Mvav 108,140,212
Ta 212,11,32768
Ta 214,11,33024
Mvav 76,140,204
X 176
Mvav 44,140,196
W 5
D 7,20,94,3072
K 217,174,175
Mvav 48,144,196
s_add_u32 s94, s94, 221184
X 177
Maaa 232,196,204
s_add_u32 m0, s95, 16384
Ra 216,13,56320
Ra 132,242,9216
Maaa 236,200,204
X 178
Mvav 80,144,204
Ta 220,11,14336
K 194,176,177
Mvav 112,144,212
X 179
Mvav 144,144,220
D 4,20,94,0
Ta 222,11,14592
Mvav 148,148,220
Ra 136,242,10240
Ta 224,11,34816
Mvav 116,148,212
X 180
Mvav 84,148,204
D 5,20,94,1024
K 195,178,179
Mvav 52,148,196
X 181
Mvav 56,152,196
D 6,20,94,2048
Ta 226,11,35072
Mvav 88,152,204
W 6
X 182
Maaa 232,208,216
D 7,20,94,3072
Ra 228,13,56832
Maaa 236,212,216
s_add_u32 s94, s94, 221184
Ra 140,242,11264
K 202,180,181
Mvav 120,152,212
X 183
Mvav 152,152,220
Ta 184,11,16384
Ra 144,242,12288
Mvav 156,156,220
X 184
Mvav 124,156,212
Ta 186,11,16640
K 203,182,183
Mvav 92,156,204
X 185
Mvav 60,156,196
W 3
Ta 188,11,36864
Ra 148,242,13312
Cvaa 160,128,0,224
X 186
Cvaa 164,128,16,228
Ta 190,11,37120
K 210,184,185
Cvaa 168,128,32,232
Ra 192,13,57344
Ta 196,11,18432
Maaa 232,220,228
X 187
Maaa 236,224,228
X 188
Cvaa 172,128,48,236
Ra 152,242,14336
K 211,186,187
Mvaa 160,132,4
Ta 198,11,18688
Ta 200,11,38912
Mvaa 164,132,20
X 189
Mvaa 168,132,36
X 190
Mvaa 172,132,52
Ra 156,242,15360
K 218,188,189
Mvaa 160,136,8
Ta 202,11,39168
Ra 204,13,57856
Mvaa 164,136,24
X 191
Mvaa 168,136,40
Ra 128,242,24576
Mvaa 172,136,56
W 8
Maaa 232,184,192
Ra 132,242,25600
Maaa 236,188,192
Mvaa 160,140,12
Mvaa 164,140,28
Mvaa 168,140,44
Ra 136,242,26624
Mvaa 172,140,60
W 5
Cvaa 176,144,0,224
Cvaa 180,144,16,228
Cvaa 184,144,32,232
X 160
K 219,190,191
Cvaa 188,144,48,236
X 161
Mvaa 176,148,4
W 3
X 162
Maaa 232,196,204
Ra 140,242,27648
K 196,160,161
Maaa 236,200,204
X 163
Mvaa 180,148,20
X 164
Mvaa 184,148,36
Ra 144,242,28672
K 197,162,163
Mvaa 188,148,52
X 165
Mvaa 176,152,8
X 166
Mvaa 180,152,24
Ra 148,242,29696
K 204,164,165
Mvaa 184,152,40
X 167
Mvaa 188,152,56
X 168
Mvaa 176,156,12
Ra 152,242,30720
K 205,166,167
Mvaa 180,156,28
X 169
Mvaa 184,156,44
X 170
Mvaa 188,156,60
W 3
K 212,168,169
Mvav 32,128,192
X 171
Mvav 64,128,200
Ra 156,242,31744
Mvav 96,128,208
X 172
Mvav 128,128,216
)"
R"(K 213,170,171
Mvav 132,132,216
X 173
Mvav 100,132,208
Mvav 68,132,200
X 174
Mvav 36,132,192
Mvav 40,136,192
K 220,172,173
Mvav 72,136,200
X 175
Mvav 104,136,208
Mvav 136,136,216
X 176
Mvav 140,140,216
Mvav 108,140,208
K 221,174,175
Mvav 76,140,200
s_waitcnt vmcnt(0) lgkmcnt(0)
s_barrier
v_xor_b32 v242, 0x10000, v242
Mvav 44,140,192
X 177
Mvav 48,144,192
X 178
Mvav 80,144,200
Ra 128,242,0
K 198,176,177
Mvav 112,144,208
X 179
Mvav 144,144,216
Ra 132,242,1024
Mvav 148,148,216
X 180
Mvav 116,148,208
Ra 136,242,2048
Mvav 84,148,200
K 199,178,179
Mvav 52,148,192
X 181
Mvav 56,152,192
Ra 140,242,3072
Mvav 88,152,200
X 182
Mvav 120,152,208
Mvav 152,152,216
K 206,180,181
Mvav 156,156,216
X 183
Mvav 124,156,208
Mvav 92,156,200
K 207,182,183
Mvav 60,156,192
W 0
Cvaa 160,128,0,224
s_mov_b32 s20, s34
Ra 144,242,4096
Ra 148,242,5120
Cvaa 164,128,16,228
s_xor_b32 s62, s62, 0x10000
X 184
Cvaa 168,128,32,232
s_and_b32 s21, s35, 0xffff
Ta 184,11,40960
Ta 186,11,41216
Cvaa 172,128,48,236
Ta 188,12,32768
Ta 190,12,33024
Mvaa 160,132,4
s_mov_b32 s22, 2211840
Ra 192,13,58368
Ta 196,11,43008
Mvaa 164,132,20
s_mov_b32 s23, 0x20000
X 185
Mvaa 168,132,36
Ta 198,11,43264
Ta 200,12,34816
Mvaa 172,132,52
W 3
X 186
Maaa 232,184,192
Ta 202,12,35072
K 214,184,185
Maaa 236,188,192
Ra 204,13,58880
Ra 152,242,6144
Mvaa 160,136,8
X 187
Mvaa 164,136,24
Ta 208,11,45056
v_lshrrev_b32 v4, 8, v1
Mvaa 168,136,40
X 188
Mvaa 172,136,56
Ta 210,11,45312
v_lshlrev_b32 v5, 5, v4
Mvaa 160,140,12
v_xor_b32 v5, v5, v1
K 215,186,187
Mvaa 164,140,28
Ra 156,242,7168
Ta 212,12,36864
Mvaa 168,140,44
X 189
Mvaa 172,140,60
v_and_b32 v5, 0xf0, v5
v_mul_u32_u24 v4, 1280, v4
v_smfmac_f32_16x16x64_bf16 a[160:163], a[176:179], v[192:199], v240
W 5
X 190
Maaa 232,196,204
Ta 214,12,37120
v_add_u32 v4, v4, v5
Maaa 236,200,204
Ra 216,13,59392
K 222,188,189
v_smfmac_f32_16x16x64_bf16 a[164:167], a[176:179], v[200:207], v240
X 191
X 160
K 223,190,191
v_smfmac_f32_16x16x64_bf16 a[168:171], a[176:179], v[208:215], v240
Ra 128,242,16384
v_xor_b32 v5, 128, v4
v_smfmac_f32_16x16x64_bf16 a[172:175], a[176:179], v[216:223], v240
W 4
Ta 220,11,47104
v_add_u32 v6, 8192, v4
Cvaa 176,144,0,224
Ta 222,11,47360
v_add_u32 v7, 12288, v5
Cvaa 180,144,16,228
Ra 132,242,17408
v_add_u32 v5, 4096, v5
Cvaa 184,144,32,232
s_mul_i32 s94, s85, 40960
X 161
Cvaa 188,144,48,236
s_mul_i32 s81, s14, 20480
Ta 224,12,38912
Ta 226,12,39168
Mvaa 176,148,4
s_add_u32 s94, s94, s81
X 162
Mvaa 180,148,20
W 6
s_lshl_b32 s95, s14, 12
K 192,160,161
Maaa 232,208,216
s_add_u32 s95, s95, 98304
X 163
Maaa 236,212,216
s_add_u32 m0, s95, 0
Ra 228,13,59904
Ra 136,242,18432
Mvaa 184,148,36
X 164
Mvaa 188,148,52
Ta 184,11,49152
K 193,162,163
Mvaa 176,152,8
X 165
Mvaa 180,152,24
Ra 140,242,19456
Ta 186,11,49408
Mvaa 184,152,40
D 4,20,94,0
Ta 188,12,40960
Mvaa 188,152,56
X 166
Mvaa 176,156,12
D 5,20,94,1024
K 200,164,165
Mvaa 180,156,28
X 167
Mvaa 184,156,44
W 5
D 6,20,94,2048
Ra 144,242,20480
Maaa 232,220,228
s_add_u32 s64, s64, 1
Ta 190,12,41216
Ra 192,13,60416
Maaa 236,224,228
D 7,20,94,3072
Ta 196,11,51200
Mvaa 188,156,60
W 6
X 168
Mvav 32,128,196
s_add_u32 s94, s94, 256
s_add_u32 m0, s95, 8192
K 201,166,167
Mvav 64,128,204
X 169
Mvav 96,128,212
D 4,20,94,0
Ra 148,242,21504
Mvav 128,128,220
X 170
Mvav 132,132,220
D 5,20,94,1024
Ta 198,11,51456
Mvav 100,132,212
Ta 200,12,43008
K 208,168,169
Mvav 68,132,204
D 6,20,94,2048
Ra 152,242,22528
Mvav 36,132,196
W 5
X 171
Maaa 232,184,192
D 7,20,94,3072
Ta 202,12,43264
Maaa 236,188,192
s_add_u32 s94, s94, 256
X 172
Mvav 40,136,196
s_add_u32 m0, s95, 16384
Ra 204,13,60928
K 209,170,171
Mvav 72,136,204
X 173
Mvav 104,136,212
Ra 156,242,23552
D 4,20,94,0
Mvav 136,136,220
X 174
Mvav 140,140,220
D 5,20,94,1024
Ta 208,11,53248
Mvav 108,140,212
Ta 210,11,53504
K 216,172,173
Mvav 76,140,204
D 6,20,94,2048
Ra 128,242,8192
Mvav 44,140,196
W 3
X 175
Mvav 48,144,196
D 7,20,94,3072
Ta 212,12,45056
Maaa 232,196,204
s_add_u32 s94, s94, 256
X 176
Maaa 236,200,204
s_add_u32 m0, s95, 24576
Ta 214,12,45312
K 217,174,175
Mvav 80,144,204
D 4,20,94,0
Ra 216,13,61440
Mvav 112,144,212
X 177
Mvav 144,144,220
D 5,20,94,1024
Ra 132,242,9216
Mvav 148,148,220
X 178
Mvav 116,148,212
D 6,20,94,2048
Ta 220,11,55296
Mvav 84,148,204
Ta 222,11,55552
K 194,176,177
Mvav 52,148,196
D 7,20,94,3072
Ra 136,242,10240
Mvav 56,152,196
s_add_u32 s94, s94, 256
X 179
Mvav 88,152,204
W 4
X 180
Maaa 232,208,216
s_add_u32 m0, s95, 32768
Ta 224,12,47104
K 195,178,179
Maaa 236,212,216
D 4,20,94,0
Ta 226,12,47360
Mvav 120,152,212
Ra 140,242,11264
Ra 228,13,61952
Mvav 152,152,220
X 181
Mvav 156,156,220
D 5,20,94,1024
Ta 184,11,57344
Mvav 124,156,212
X 182
Mvav 92,156,204
D 6,20,94,2048
K 202,180,181
Mvav 60,156,196
W 2
X 183
Cvaa 160,128,0,224
D 7,20,94,3072
Ra 144,242,12288
Cvaa 164,128,16,228
s_add_u32 s94, s94, 256
X 184
Cvaa 168,128,32,232
W 2
Ta 186,11,57600
K 203,182,183
Maaa 232,220,228
X 185
Maaa 236,224,228
Ta 188,12,49152
Ra 148,242,13312
Cvaa 172,128,48,236
X 186
Mvaa 160,132,4
Ta 190,12,49408
K 210,184,185
Mvaa 164,132,20
Ra 192,13,62464
Ta 196,11,59392
Mvaa 168,132,36
X 187
Mvaa 172,132,52
X 188
Mvaa 160,136,8
Ra 152,242,14336
K 211,186,187
Mvaa 164,136,24
Ta 198,11,59648
Ta 200,12,51200
Mvaa 168,136,40
X 189
Mvaa 172,136,56
W 4
X 190
Maaa 232,184,192
Ra 156,242,15360
K 218,188,189
Maaa 236,188,192
Ta 202,12,51456
Ra 204,13,62976
Mvaa 160,140,12
X 191
Mvaa 164,140,28
Ra 128,242,24576
Ra 132,242,25600
Mvaa 168,140,44
Ra 136,242,26624
Mvaa 172,140,60
W 5
X 160
Cvaa 176,144,0,224
K 219,190,191
Cvaa 180,144,16,228
X 161
Cvaa 184,144,32,232
X 162
Cvaa 188,144,48,236
Ra 140,242,27648
K 196,160,161
Mvaa 176,148,4
W 4
X 163
Maaa 232,196,204
X 164
Maaa 236,200,204
Ra 144,242,28672
K 197,162,163
Mvaa 180,148,20
X 165
Mvaa 184,148,36
X 166
Mvaa 188,148,52
K 204,164,165
G 252,232
Mvaa 176,152,8
X 167
Mvaa 180,152,24
G 253,233
G 254,234
Mvaa 184,152,40
X 168
Mvaa 188,152,56
G 255,235
G 8,236
Mvaa 176,156,12
Ra 148,242,29696
G 9,237
Mvaa 180,156,28
G 10,238
G 14,239
Mvaa 184,156,44
K 252,252,8
K 253,253,9
Mvaa 188,156,60
W 2
K 205,166,167
K 254,254,10
)"
R"(Mvav 32,128,192
K 255,255,14
v_and_b32 v8, 0xffff0000, v252
Mvav 64,128,200
v_and_b32 v9, 0xffff0000, v253
v_and_b32 v10, 0xffff0000, v254
Mvav 96,128,208
X 169
Mvav 128,128,216
v_and_b32 v14, 0xffff0000, v255
v_lshlrev_b32 v252, 16, v252
Mvav 132,132,216
v_lshlrev_b32 v253, 16, v253
v_lshlrev_b32 v254, 16, v254
Mvav 100,132,208
v_lshlrev_b32 v255, 16, v255
v_mul_f32 v15, 0xbfb8aa3b, v252
Mvav 68,132,200
Ra 152,242,30720
v_mul_f32 v2, 0xbfb8aa3b, v253
Mvav 36,132,192
X 170
Mvav 40,136,192
v_mul_f32 v3, 0xbfb8aa3b, v254
v_mul_f32 v27, 0xbfb8aa3b, v255
Mvav 72,136,200
X 15
Mvav 104,136,208
Ra 156,242,31744
K 212,168,169
Mvav 136,136,216
X 2
X 3
X 27
v_add_f32 v15, 1.0, v15
v_add_f32 v2, 1.0, v2
v_add_f32 v3, 1.0, v3
v_add_f32 v27, 1.0, v27
v_rcp_f32 v15, v15
v_rcp_f32 v2, v2
v_rcp_f32 v3, v3
v_rcp_f32 v27, v27
U 252,252,15
U 253,253,2
U 254,254,3
U 255,255,27
K 252,252,252
K 253,253,253
K 254,254,254
K 255,255,255
v_lshlrev_b32 v252, 16, v252
v_lshlrev_b32 v253, 16, v253
v_lshlrev_b32 v254, 16, v254
v_lshlrev_b32 v255, 16, v255
U 252,252,8
U 253,253,9
U 254,254,10
U 255,255,14
K 246,252,253
K 247,254,255
s_lshl_b32 s81, s14, 3
v_add_u32 v2, s81, v1
s_mov_b32 s86, 0x00ff00ff
s_mov_b32 s87, 0x00ff00ff
v_cndmask_b32_e64 v252, 0, v246, s[86:87]
v_cndmask_b32_e64 v253, 0, v247, s[86:87]
v_cndmask_b32_e64 v246, v246, 0, s[86:87]
v_cndmask_b32_e64 v247, v247, 0, s[86:87]
ds_write_b64 v2, v[252:253] offset:63488
ds_write_b64 v2, v[246:247] offset:64512
Mvav 140,140,216
X 171
Mvav 108,140,208
X 172
Mvav 76,140,200
s_waitcnt vmcnt(0) lgkmcnt(0)
s_barrier
v_xor_b32 v242, 0x10000, v242
Mvav 44,140,192
Ra 128,242,0
K 213,170,171
Mvav 48,144,192
X 173
Mvav 80,144,200
X 174
Mvav 112,144,208
Ra 132,242,1024
K 220,172,173
Mvav 144,144,216
X 175
Mvav 148,148,216
X 176
Mvav 116,148,208
Ra 136,242,2048
K 221,174,175
Mvav 84,148,200
X 177
Mvav 52,148,192
X 178
Mvav 56,152,192
Ra 140,242,3072
K 198,176,177
Mvav 88,152,200
X 179
Mvav 120,152,208
X 180
Mvav 152,152,216
K 199,178,179
Mvav 156,156,216
X 181
Mvav 124,156,208
X 182
Mvav 92,156,200
K 206,180,181
Mvav 60,156,192
X 183
s_nop 0
K 207,182,183
W 0
Cvaa 160,128,0,224
Ra 144,242,4096
Ra 148,242,5120
Cvaa 164,128,16,228
s_xor_b32 s62, s62, 0x10000
X 184
Cvaa 168,128,32,232
Ra 216,1,63488
Ra 220,1,64512
Cvaa 172,128,48,236
v_lshrrev_b32 v8, 6, v1
v_and_b32 v9, 7, v8
Mvaa 160,132,4
s_lshl_b32 s81, s14, 2
X 185
Mvaa 164,132,20
v_xor_b32 v9, s81, v9
v_lshlrev_b32 v9, 5, v9
Mvaa 168,132,36
X 186
Mvaa 172,132,52
v_lshl_add_u32 v4, v8, 8, v9
v_bfe_u32 v9, v1, 4, 2
Mvaa 160,136,8
v_lshl_add_u32 v4, v9, 3, v4
v_add_u32 v4, 98304, v4
Mvaa 164,136,24
K 214,184,185
v_xor_b32 v5, 32, v4
Mvaa 168,136,40
v_xor_b32 v6, 64, v4
v_xor_b32 v7, 96, v4
Ta 184,4,0
Ta 186,4,4096
Mvaa 172,136,56
Ta 188,5,0
Ta 190,5,4096
Mvaa 160,140,12
Ra 152,242,6144
Ta 192,6,0
Mvaa 164,140,28
W 4
X 187
Zaaa 232,184,216
Ta 194,6,4096
v_and_b32 v11, 0x70, v1
Mvaa 168,140,44
v_mul_u32_u24 v11, 160, v11
v_lshrrev_b32 v10, 8, v1
Mvaa 172,140,60
X 188
v_smfmac_f32_16x16x64_bf16 a[160:163], a[176:179], v[192:199], v240
Ra 156,242,7168
W 4
Maaa 232,188,220
K 215,186,187
v_lshl_add_u32 v11, v10, 4, v11
v_smfmac_f32_16x16x64_bf16 a[164:167], a[176:179], v[200:207], v240
X 189
X 190
K 222,188,189
X 191
X 160
K 223,190,191
v_smfmac_f32_16x16x64_bf16 a[168:171], a[176:179], v[208:215], v240
Ta 196,7,0
v_bfe_u32 v10, v1, 7, 1
v_smfmac_f32_16x16x64_bf16 a[172:175], a[176:179], v[216:223], v240
W 2
Ta 198,7,4096
v_lshl_add_u32 v10, v10, 6, v11
Zaaa 236,192,216
W 2
s_mul_i32 s94, s85, 20480
s_lshl_b32 s82, s14, 8
Cvaa 176,144,0,224
s_add_u32 s94, s94, s82
Ta 200,4,8192
Ta 202,4,12288
Cvaa 180,144,16,228
s_add_u32 s94, s94, 2752512
s_lshl_b32 s71, s85, 4
Ra 128,242,16384
Cvaa 184,144,32,232
W 3
s_lshl_b32 s82, s14, 3
Ta 204,5,8192
Maaa 236,196,220
s_add_u32 s71, s71, s82
s_add_u32 s71, s71, 2560
Ta 206,5,12288
Cvaa 188,144,48,236
X 161
Mvaa 176,148,4
Ra 132,242,17408
Ta 208,6,8192
Mvaa 180,148,20
W 5
X 162
Zaaa 240,200,216
Ta 210,6,12288
K 192,160,161
Mvaa 184,148,36
buffer_store_dwordx4 a[232:235], v10, s[8:11], s94 offen offset:0 sc1
Ta 212,7,8192
Mvaa 188,148,52
X 163
Mvaa 176,152,8
W 4
X 164
Maaa 240,204,220
Ra 136,242,18432
Ta 214,7,12288
Mvaa 180,152,24
Ta 184,4,16384
K 193,162,163
Mvaa 184,152,40
X 165
Mvaa 188,152,56
W 4
Ta 186,4,20480
Ra 140,242,19456
Zaaa 244,208,216
X 166
Mvaa 176,156,12
buffer_store_dwordx4 a[236:239], v10, s[8:11], s94 offen offset:128 sc1
K 200,164,165
Mvaa 180,156,28
X 167
Mvaa 184,156,44
W 3
Ta 188,5,16384
Ta 190,5,20480
Maaa 244,212,220
s_add_u32 s64, s64, 1
X 168
Mvaa 188,156,60
W 2
Ra 144,242,20480
K 201,166,167
Mvav 32,128,196
Ta 192,6,16384
Ta 194,6,20480
Mvav 64,128,204
X 169
Zaaa 248,184,216
buffer_store_dwordx4 a[240:243], v10, s[8:11], s94 offen offset:512 sc1
Ta 196,7,16384
Mvav 96,128,212
X 170
Mvav 128,128,220
Ra 148,242,21504
K 208,168,169
Mvav 132,132,220
W 5
Ta 198,7,20480
Ta 200,4,24576
Maaa 248,188,220
X 171
Mvav 100,132,212
X 172
Mvav 68,132,204
Ta 202,4,28672
K 209,170,171
Mvav 36,132,196
W 5
Ra 152,242,22528
buffer_store_dwordx4 a[244:247], v10, s[8:11], s94 offen offset:640 sc1
Zaaa 252,192,216
X 173
Mvav 40,136,196
X 174
Mvav 72,136,204
Ta 204,5,24576
Ta 206,5,28672
Mvav 104,136,212
W 5
Ra 156,242,23552
K 216,172,173
Maaa 252,196,220
X 175
Mvav 136,136,220
Ta 208,6,24576
Ta 210,6,28672
Mvav 140,140,220
X 176
Mvav 108,140,212
W 6
buffer_store_dwordx4 a[248:251], v10, s[8:11], s94 offen offset:1024 sc1
K 217,174,175
Zaaa 232,200,216
X 177
Mvav 76,140,204
Ta 212,7,24576
Ra 128,242,8192
Mvav 44,140,196
W 4
Ta 214,7,28672
Ta 184,4,32768
Mvav 48,144,196
X 178
Maaa 232,204,220
Ta 186,4,36864
K 194,176,177
Mvav 80,144,204
X 179
Mvav 112,144,212
Ra 132,242,9216
buffer_store_dwordx4 a[252:255], v10, s[8:11], s94 offen offset:1152 sc1
Mvav 144,144,220
W 6
X 180
Zaaa 236,208,216
Ta 188,5,32768
K 195,178,179
Mvav 148,148,220
X 181
Mvav 116,148,212
Ta 190,5,36864
Ra 136,242,10240
Mvav 84,148,204
W 6
X 182
Maaa 236,212,220
Ta 192,6,32768
K 202,180,181
Mvav 52,148,196
Ta 194,6,36864
Ra 140,242,11264
Mvav 56,152,196
X 183
Mvav 88,152,204
W 7
buffer_store_dwordx4 a[232:235], v10, s[8:11], s94 offen offset:1536 sc1
Ta 196,7,32768
Zaaa 240,184,216
X 184
Mvav 120,152,212
Ta 198,7,36864
K 203,182,183
Mvav 152,152,220
Ra 144,242,12288
Ra 148,242,13312
Mvav 156,156,220
W 8
X 185
Maaa 240,188,220
X 186
Mvav 124,156,212
buffer_store_dwordx4 a[236:239], v10, s[8:11], s94 offen offset:1664 sc1
K 210,184,185
Mvav 92,156,204
X 187
Mvav 60,156,196
W 5
X 188
Zaaa 244,192,216
W 4
Ra 152,242,14336
K 211,186,187
Cvaa 160,128,0,224
X 189
Cvaa 164,128,16,228
X 190
Cvaa 168,128,32,232
W 3
Ra 156,242,15360
buffer_store_dwordx4 a[240:243], v10, s[8:11], s94 offen offset:2048 sc1
Maaa 244,196,220
K 218,188,189
Cvaa 172,128,48,236
X 191
Mvaa 160,132,4
Ra 128,242,24576
Mvaa 164,132,20
Mvaa 168,132,36
Mvaa 172,132,52
Mvaa 160,136,8
Ra 132,242,25600
buffer_store_dwordx4 a[244:247], v10, s[8:11], s94 offen offset:2176 sc1
Mvaa 164,136,24
)"
R"(Mvaa 168,136,40
Mvaa 172,136,56
Mvaa 160,140,12
Ra 136,242,26624
Mvaa 164,140,28
Mvaa 168,140,44
Mvaa 172,140,60
W 3
Cvaa 176,144,0,224
Ra 140,242,27648
Cvaa 180,144,16,228
X 160
Cvaa 184,144,32,232
K 219,190,191
Cvaa 188,144,48,236
X 161
Mvaa 176,148,4
X 162
Mvaa 180,148,20
Ra 144,242,28672
K 196,160,161
Mvaa 184,148,36
X 163
Mvaa 188,148,52
X 164
Mvaa 176,152,8
Ra 148,242,29696
K 197,162,163
Mvaa 180,152,24
X 165
Mvaa 184,152,40
X 166
Mvaa 188,152,56
K 204,164,165
Mvaa 176,156,12
X 167
Mvaa 180,156,28
X 168
Mvaa 184,156,44
Ra 152,242,30720
K 205,166,167
Mvaa 188,156,60
W 3
X 169
Mvav 32,128,192
X 170
Mvav 64,128,200
Ra 156,242,31744
K 212,168,169
Mvav 96,128,208
X 171
Mvav 128,128,216
X 172
Mvav 132,132,216
K 213,170,171
Mvav 100,132,208
X 173
Mvav 68,132,200
X 174
Mvav 36,132,192
Mvav 40,136,192
K 220,172,173
Mvav 72,136,200
X 175
Mvav 104,136,208
Mvav 136,136,216
X 176
Mvav 140,140,216
Mvav 108,140,208
K 221,174,175
Mvav 76,140,200
W 0
s_barrier
v_xor_b32 v242, 0x10000, v242
Mvav 44,140,192
X 177
Mvav 48,144,192
X 178
Mvav 80,144,200
Ra 128,242,0
K 198,176,177
Mvav 112,144,208
X 179
Mvav 144,144,216
Ra 132,242,1024
Mvav 148,148,216
X 180
Mvav 116,148,208
Ra 136,242,2048
Mvav 84,148,200
K 199,178,179
Mvav 52,148,192
X 181
Mvav 56,152,192
Ra 140,242,3072
Mvav 88,152,200
X 182
Mvav 120,152,208
Mvav 152,152,216
K 206,180,181
Mvav 156,156,216
X 183
Mvav 124,156,208
Mvav 92,156,200
s_waitcnt vmcnt(0)
v_mov_b64 v[28:29], s[12:13]
s_mov_b64 exec, 1
buffer_store_dwordx2 v[28:29], off, s[8:11], s71 sc1
s_mov_b64 exec, -1
K 207,182,183
Mvav 60,156,192
s_mov_b32 s85, 99
s_lshl_b32 s65, s57, 1
s_sub_u32 s65, s65, 1
s_branch .Ll0t_45%=
.Lf1r_46%=:
s_lshl_b32 s94, s58, 2
s_add_u32 s94, s94, 0
s_and_b32 s94, s94, 15
s_mul_i32 s94, s94, 10240
s_add_u32 s94, s94, s48
s_lshl_b32 s94, s94, 2
s_mul_i32 s95, s51, 0
s_add_u32 s95, s95, s48
s_add_u32 m0, s95, 133120
s_nop 0
D 16,40,94,0
D 17,40,94,1024
D 18,40,94,2048
D 19,40,94,3072
s_add_u32 m0, s95, 137216
s_nop 0
buffer_load_dwordx4 v20, s[40:43], s94 offen lds
s_lshl_b32 s94, s58, 2
s_add_u32 s94, s94, 1
s_and_b32 s94, s94, 15
s_mul_i32 s94, s94, 10240
s_add_u32 s94, s94, s48
s_lshl_b32 s94, s94, 2
s_mul_i32 s95, s51, 0
s_add_u32 s95, s95, s48
s_add_u32 m0, s95, 143360
s_nop 0
D 16,40,94,0
D 17,40,94,1024
D 18,40,94,2048
D 19,40,94,3072
s_add_u32 m0, s95, 147456
s_nop 0
buffer_load_dwordx4 v20, s[40:43], s94 offen lds
s_lshl_b32 s94, s58, 2
s_add_u32 s94, s94, 2
s_and_b32 s94, s94, 15
s_mul_i32 s94, s94, 10240
s_add_u32 s94, s94, s48
s_lshl_b32 s94, s94, 2
s_mul_i32 s95, s51, 0
s_add_u32 s95, s95, s48
s_add_u32 m0, s95, 153600
s_nop 0
D 16,40,94,0
D 17,40,94,1024
D 18,40,94,2048
D 19,40,94,3072
s_add_u32 m0, s95, 157696
s_nop 0
buffer_load_dwordx4 v20, s[40:43], s94 offen lds
s_branch .Lf1_39%=
.Lspin_14%=:
s_mov_b32 s84, 0
.Llp_16%=:
s_sleep 2
v_lshrrev_b32 v28, 1, v1
s_lshl_b32 s80, s60, 9
s_add_u32 s80, s80, 40960
buffer_load_dwordx2 v[28:29], v28, s[8:11], s80 offen sc1
s_waitcnt vmcnt(0)
s_add_u32 s84, s84, 1
v_cmp_eq_u32 vcc, s13, v29
s_cmp_eq_u64 vcc, -1
s_cbranch_scc1 .Lok_15%=
s_cmp_lt_u32 s84, 1048576
s_cbranch_scc1 .Llp_16%=
s_branch .Lok_15%=
.Lspin_19%=:
s_mov_b32 s84, 0
.Llp_21%=:
s_sleep 2
v_lshrrev_b32 v28, 1, v1
s_lshl_b32 s80, s60, 9
s_add_u32 s80, s80, 40960
buffer_load_dwordx2 v[28:29], v28, s[8:11], s80 offen sc1
s_waitcnt vmcnt(0)
s_add_u32 s84, s84, 1
v_cmp_eq_u32 vcc, s13, v29
s_cmp_eq_u64 vcc, -1
s_cbranch_scc1 .Lok_20%=
s_cmp_lt_u32 s84, 1048576
s_cbranch_scc1 .Llp_21%=
s_branch .Lok_20%=
.Lrt_22%=:
s_waitcnt vmcnt(0)
s_mov_b32 s86, 40960
buffer_load_dwordx4 v[32:35], v1, s[8:11], s86 offen offset:0 sc1
buffer_load_dwordx4 v[36:39], v1, s[8:11], s86 offen offset:1024 sc1
buffer_load_dwordx4 v[40:43], v1, s[8:11], s86 offen offset:2048 sc1
buffer_load_dwordx4 v[44:47], v1, s[8:11], s86 offen offset:3072 sc1
s_mov_b32 s86, 45056
buffer_load_dwordx4 v[48:51], v1, s[8:11], s86 offen offset:0 sc1
buffer_load_dwordx4 v[52:55], v1, s[8:11], s86 offen offset:1024 sc1
buffer_load_dwordx4 v[56:59], v1, s[8:11], s86 offen offset:2048 sc1
buffer_load_dwordx4 v[60:63], v1, s[8:11], s86 offen offset:3072 sc1
s_mov_b32 s86, 49152
buffer_load_dwordx4 v[64:67], v1, s[8:11], s86 offen offset:0 sc1
buffer_load_dwordx4 v[68:71], v1, s[8:11], s86 offen offset:1024 sc1
buffer_load_dwordx4 v[72:75], v1, s[8:11], s86 offen offset:2048 sc1
buffer_load_dwordx4 v[76:79], v1, s[8:11], s86 offen offset:3072 sc1
s_mov_b32 s86, 53248
buffer_load_dwordx4 v[80:83], v1, s[8:11], s86 offen offset:0 sc1
buffer_load_dwordx4 v[84:87], v1, s[8:11], s86 offen offset:1024 sc1
buffer_load_dwordx4 v[88:91], v1, s[8:11], s86 offen offset:2048 sc1
buffer_load_dwordx4 v[92:95], v1, s[8:11], s86 offen offset:3072 sc1
s_branch .Lrtr_23%=
.Lrt_24%=:
v_lshrrev_b32 v136, 1, v1
s_mov_b32 s84, 0
.LL_26%=:
s_waitcnt vmcnt(0)
v_min3_u32 v96, v33, v35, v95
v_max3_u32 v97, v33, v35, v95
v_min3_u32 v96, v96, v37, v39
v_max3_u32 v97, v97, v37, v39
v_min3_u32 v96, v96, v41, v43
v_max3_u32 v97, v97, v41, v43
v_min3_u32 v96, v96, v45, v47
v_max3_u32 v97, v97, v45, v47
v_min3_u32 v96, v96, v49, v51
v_max3_u32 v97, v97, v49, v51
v_min3_u32 v96, v96, v53, v55
v_max3_u32 v97, v97, v53, v55
v_min3_u32 v96, v96, v57, v59
v_max3_u32 v97, v97, v57, v59
v_min3_u32 v96, v96, v61, v63
v_max3_u32 v97, v97, v61, v63
v_min3_u32 v96, v96, v65, v67
v_max3_u32 v97, v97, v65, v67
v_min3_u32 v96, v96, v69, v71
v_max3_u32 v97, v97, v69, v71
v_min3_u32 v96, v96, v73, v75
v_max3_u32 v97, v97, v73, v75
v_min3_u32 v96, v96, v77, v79
v_max3_u32 v97, v97, v77, v79
v_min3_u32 v96, v96, v81, v83
v_max3_u32 v97, v97, v81, v83
v_min3_u32 v96, v96, v85, v87
v_max3_u32 v97, v97, v85, v87
v_min3_u32 v96, v96, v89, v91
v_max3_u32 v97, v97, v89, v91
v_min_u32 v96, v96, v93
v_max_u32 v97, v97, v93
v_cmp_eq_u32 vcc, s13, v96
v_cmp_eq_u32_e64 s[86:87], s13, v97
s_and_b64 vcc, vcc, s[86:87]
s_cmp_eq_u64 vcc, -1
s_cbranch_scc1 .LL_27%=
s_sleep 2
s_mov_b32 s86, 40960
buffer_load_dwordx4 v[32:35], v1, s[8:11], s86 offen offset:0 sc1
buffer_load_dwordx4 v[36:39], v1, s[8:11], s86 offen offset:1024 sc1
buffer_load_dwordx4 v[40:43], v1, s[8:11], s86 offen offset:2048 sc1
buffer_load_dwordx4 v[44:47], v1, s[8:11], s86 offen offset:3072 sc1
s_mov_b32 s86, 45056
buffer_load_dwordx4 v[48:51], v1, s[8:11], s86 offen offset:0 sc1
buffer_load_dwordx4 v[52:55], v1, s[8:11], s86 offen offset:1024 sc1
buffer_load_dwordx4 v[56:59], v1, s[8:11], s86 offen offset:2048 sc1
buffer_load_dwordx4 v[60:63], v1, s[8:11], s86 offen offset:3072 sc1
s_mov_b32 s86, 49152
buffer_load_dwordx4 v[64:67], v1, s[8:11], s86 offen offset:0 sc1
buffer_load_dwordx4 v[68:71], v1, s[8:11], s86 offen offset:1024 sc1
buffer_load_dwordx4 v[72:75], v1, s[8:11], s86 offen offset:2048 sc1
buffer_load_dwordx4 v[76:79], v1, s[8:11], s86 offen offset:3072 sc1
s_mov_b32 s86, 53248
buffer_load_dwordx4 v[80:83], v1, s[8:11], s86 offen offset:0 sc1
buffer_load_dwordx4 v[84:87], v1, s[8:11], s86 offen offset:1024 sc1
buffer_load_dwordx4 v[88:91], v1, s[8:11], s86 offen offset:2048 sc1
buffer_load_dwordx4 v[92:95], v1, s[8:11], s86 offen offset:3072 sc1
s_add_u32 s84, s84, 1
s_cmp_lt_u32 s84, 1048576
s_cbranch_scc1 .LL_26%=
s_waitcnt vmcnt(0)
.LL_27%=:
v_mov_b32 v96, v32
v_mov_b32 v97, 0
v_mov_b32 v98, 0
v_mov_b32 v100, 0
v_med3_u32 v98, v97, v98, v34
v_med3_u32 v97, v96, v97, v34
v_max_u32 v96, v96, v34
v_med3_u32 v98, v97, v98, v36
v_med3_u32 v97, v96, v97, v36
v_max_u32 v96, v96, v36
v_med3_u32 v98, v97, v98, v38
v_med3_u32 v97, v96, v97, v38
v_max_u32 v96, v96, v38
v_med3_u32 v98, v97, v98, v40
v_med3_u32 v97, v96, v97, v40
v_max_u32 v96, v96, v40
v_med3_u32 v98, v97, v98, v42
v_med3_u32 v97, v96, v97, v42
v_max_u32 v96, v96, v42
v_med3_u32 v98, v97, v98, v44
v_med3_u32 v97, v96, v97, v44
v_max_u32 v96, v96, v44
v_med3_u32 v98, v97, v98, v46
v_med3_u32 v97, v96, v97, v46
v_max_u32 v96, v96, v46
v_med3_u32 v98, v97, v98, v48
v_med3_u32 v97, v96, v97, v48
v_max_u32 v96, v96, v48
v_med3_u32 v98, v97, v98, v50
v_med3_u32 v97, v96, v97, v50
v_max_u32 v96, v96, v50
v_med3_u32 v98, v97, v98, v52
v_med3_u32 v97, v96, v97, v52
v_max_u32 v96, v96, v52
v_med3_u32 v98, v97, v98, v54
v_med3_u32 v97, v96, v97, v54
v_max_u32 v96, v96, v54
v_med3_u32 v98, v97, v98, v56
v_med3_u32 v97, v96, v97, v56
v_max_u32 v96, v96, v56
v_med3_u32 v98, v97, v98, v58
v_med3_u32 v97, v96, v97, v58
v_max_u32 v96, v96, v58
v_med3_u32 v98, v97, v98, v60
v_med3_u32 v97, v96, v97, v60
v_max_u32 v96, v96, v60
v_med3_u32 v98, v97, v98, v62
v_med3_u32 v97, v96, v97, v62
v_max_u32 v96, v96, v62
v_med3_u32 v98, v97, v98, v64
v_med3_u32 v97, v96, v97, v64
v_max_u32 v96, v96, v64
v_med3_u32 v98, v97, v98, v66
v_med3_u32 v97, v96, v97, v66
v_max_u32 v96, v96, v66
v_med3_u32 v98, v97, v98, v68
v_med3_u32 v97, v96, v97, v68
v_max_u32 v96, v96, v68
v_med3_u32 v98, v97, v98, v70
v_med3_u32 v97, v96, v97, v70
v_max_u32 v96, v96, v70
v_med3_u32 v98, v97, v98, v72
v_med3_u32 v97, v96, v97, v72
v_max_u32 v96, v96, v72
v_med3_u32 v98, v97, v98, v74
v_med3_u32 v97, v96, v97, v74
v_max_u32 v96, v96, v74
v_med3_u32 v98, v97, v98, v76
v_med3_u32 v97, v96, v97, v76
v_max_u32 v96, v96, v76
v_med3_u32 v98, v97, v98, v78
v_med3_u32 v97, v96, v97, v78
v_max_u32 v96, v96, v78
v_med3_u32 v98, v97, v98, v80
v_med3_u32 v97, v96, v97, v80
v_max_u32 v96, v96, v80
v_med3_u32 v98, v97, v98, v82
v_med3_u32 v97, v96, v97, v82
v_max_u32 v96, v96, v82
v_med3_u32 v98, v97, v98, v84
v_med3_u32 v97, v96, v97, v84
v_max_u32 v96, v96, v84
v_med3_u32 v98, v97, v98, v86
v_med3_u32 v97, v96, v97, v86
v_max_u32 v96, v96, v86
v_med3_u32 v98, v97, v98, v88
v_med3_u32 v97, v96, v97, v88
v_max_u32 v96, v96, v88
v_med3_u32 v98, v97, v98, v90
v_med3_u32 v97, v96, v97, v90
v_max_u32 v96, v96, v90
v_med3_u32 v98, v97, v98, v92
v_med3_u32 v97, v96, v97, v92
v_max_u32 v96, v96, v92
v_med3_u32 v98, v97, v98, v94
v_med3_u32 v97, v96, v97, v94
v_max_u32 v96, v96, v94
v_mov_b32 v99, v96
s_nop 1
v_max_u32_dpp v99, v99, v99 row_shr:1 row_mask:0xf bank_mask:0xf
s_nop 1
v_max_u32_dpp v99, v99, v99 row_shr:2 row_mask:0xf bank_mask:0xf
s_nop 1
v_max_u32_dpp v99, v99, v99 row_shr:4 row_mask:0xf bank_mask:0xf
s_nop 1
v_max_u32_dpp v99, v99, v99 row_shr:8 row_mask:0xf bank_mask:0xf
s_nop 1
v_max_u32_dpp v99, v99, v99 row_bcast:15 row_mask:0xa bank_mask:0xf
s_nop 1
)"
R"(v_max_u32_dpp v99, v99, v99 row_bcast:31 row_mask:0xc bank_mask:0xf
s_nop 0
v_readlane_b32 s86, v99, 63
s_nop 1
v_cmp_eq_u32 vcc, s86, v96
s_and_b32 s87, s86, 0xffff
s_sub_u32 s87, 2047, s87
v_cndmask_b32 v96, v96, v97, vcc
v_cndmask_b32_e64 v97, v97, 0, vcc
v_writelane_b32 v100, s87, 0
v_mov_b32 v99, v96
s_nop 1
v_max_u32_dpp v99, v99, v99 row_shr:1 row_mask:0xf bank_mask:0xf
s_nop 1
v_max_u32_dpp v99, v99, v99 row_shr:2 row_mask:0xf bank_mask:0xf
s_nop 1
v_max_u32_dpp v99, v99, v99 row_shr:4 row_mask:0xf bank_mask:0xf
s_nop 1
v_max_u32_dpp v99, v99, v99 row_shr:8 row_mask:0xf bank_mask:0xf
s_nop 1
v_max_u32_dpp v99, v99, v99 row_bcast:15 row_mask:0xa bank_mask:0xf
s_nop 1
v_max_u32_dpp v99, v99, v99 row_bcast:31 row_mask:0xc bank_mask:0xf
s_nop 0
v_readlane_b32 s86, v99, 63
s_nop 1
v_cmp_eq_u32 vcc, s86, v96
s_and_b32 s87, s86, 0xffff
s_sub_u32 s87, 2047, s87
v_cndmask_b32 v96, v96, v97, vcc
v_cndmask_b32_e64 v97, v97, 0, vcc
v_writelane_b32 v100, s87, 1
v_mov_b32 v99, v96
s_nop 1
v_max_u32_dpp v99, v99, v99 row_shr:1 row_mask:0xf bank_mask:0xf
s_nop 1
v_max_u32_dpp v99, v99, v99 row_shr:2 row_mask:0xf bank_mask:0xf
s_nop 1
v_max_u32_dpp v99, v99, v99 row_shr:4 row_mask:0xf bank_mask:0xf
s_nop 1
v_max_u32_dpp v99, v99, v99 row_shr:8 row_mask:0xf bank_mask:0xf
s_nop 1
v_max_u32_dpp v99, v99, v99 row_bcast:15 row_mask:0xa bank_mask:0xf
s_nop 1
v_max_u32_dpp v99, v99, v99 row_bcast:31 row_mask:0xc bank_mask:0xf
s_nop 0
v_readlane_b32 s86, v99, 63
s_nop 1
v_cmp_eq_u32 vcc, s86, v96
s_and_b32 s87, s86, 0xffff
s_sub_u32 s87, 2047, s87
v_cndmask_b32 v96, v96, v97, vcc
v_cndmask_b32_e64 v97, v97, 0, vcc
v_writelane_b32 v100, s87, 2
v_mov_b32 v99, v96
s_nop 1
v_max_u32_dpp v99, v99, v99 row_shr:1 row_mask:0xf bank_mask:0xf
s_nop 1
v_max_u32_dpp v99, v99, v99 row_shr:2 row_mask:0xf bank_mask:0xf
s_nop 1
v_max_u32_dpp v99, v99, v99 row_shr:4 row_mask:0xf bank_mask:0xf
s_nop 1
v_max_u32_dpp v99, v99, v99 row_shr:8 row_mask:0xf bank_mask:0xf
s_nop 1
v_max_u32_dpp v99, v99, v99 row_bcast:15 row_mask:0xa bank_mask:0xf
s_nop 1
v_max_u32_dpp v99, v99, v99 row_bcast:31 row_mask:0xc bank_mask:0xf
s_nop 0
v_readlane_b32 s86, v99, 63
s_nop 1
v_cmp_eq_u32 vcc, s86, v96
s_and_b32 s87, s86, 0xffff
s_sub_u32 s87, 2047, s87
v_cndmask_b32 v96, v96, v97, vcc
v_cndmask_b32_e64 v97, v97, 0, vcc
v_writelane_b32 v100, s87, 3
v_mov_b32 v99, v96
s_nop 1
v_max_u32_dpp v99, v99, v99 row_shr:1 row_mask:0xf bank_mask:0xf
s_nop 1
v_max_u32_dpp v99, v99, v99 row_shr:2 row_mask:0xf bank_mask:0xf
s_nop 1
v_max_u32_dpp v99, v99, v99 row_shr:4 row_mask:0xf bank_mask:0xf
s_nop 1
v_max_u32_dpp v99, v99, v99 row_shr:8 row_mask:0xf bank_mask:0xf
s_nop 1
v_max_u32_dpp v99, v99, v99 row_bcast:15 row_mask:0xa bank_mask:0xf
s_nop 1
v_max_u32_dpp v99, v99, v99 row_bcast:31 row_mask:0xc bank_mask:0xf
s_nop 0
v_readlane_b32 s86, v99, 63
s_nop 1
v_cmp_eq_u32 vcc, s86, v96
s_and_b32 s87, s86, 0xffff
s_sub_u32 s87, 2047, s87
v_cndmask_b32 v96, v96, v97, vcc
v_cndmask_b32_e64 v97, v97, 0, vcc
v_writelane_b32 v100, s87, 4
v_mov_b32 v99, v96
s_nop 1
v_max_u32_dpp v99, v99, v99 row_shr:1 row_mask:0xf bank_mask:0xf
s_nop 1
v_max_u32_dpp v99, v99, v99 row_shr:2 row_mask:0xf bank_mask:0xf
s_nop 1
v_max_u32_dpp v99, v99, v99 row_shr:4 row_mask:0xf bank_mask:0xf
s_nop 1
v_max_u32_dpp v99, v99, v99 row_shr:8 row_mask:0xf bank_mask:0xf
s_nop 1
v_max_u32_dpp v99, v99, v99 row_bcast:15 row_mask:0xa bank_mask:0xf
s_nop 1
v_max_u32_dpp v99, v99, v99 row_bcast:31 row_mask:0xc bank_mask:0xf
s_nop 0
v_readlane_b32 s86, v99, 63
s_nop 1
v_cmp_eq_u32 vcc, s86, v96
s_and_b32 s87, s86, 0xffff
s_sub_u32 s87, 2047, s87
v_cndmask_b32 v96, v96, v97, vcc
v_cndmask_b32_e64 v97, v97, 0, vcc
v_writelane_b32 v100, s87, 5
v_mov_b32 v99, v96
s_nop 1
v_max_u32_dpp v99, v99, v99 row_shr:1 row_mask:0xf bank_mask:0xf
s_nop 1
v_max_u32_dpp v99, v99, v99 row_shr:2 row_mask:0xf bank_mask:0xf
s_nop 1
v_max_u32_dpp v99, v99, v99 row_shr:4 row_mask:0xf bank_mask:0xf
s_nop 1
v_max_u32_dpp v99, v99, v99 row_shr:8 row_mask:0xf bank_mask:0xf
s_nop 1
v_max_u32_dpp v99, v99, v99 row_bcast:15 row_mask:0xa bank_mask:0xf
s_nop 1
v_max_u32_dpp v99, v99, v99 row_bcast:31 row_mask:0xc bank_mask:0xf
s_nop 0
v_readlane_b32 s86, v99, 63
s_nop 1
v_cmp_eq_u32 vcc, s86, v96
s_and_b32 s87, s86, 0xffff
s_sub_u32 s87, 2047, s87
v_cndmask_b32 v96, v96, v97, vcc
v_cndmask_b32_e64 v97, v97, 0, vcc
v_writelane_b32 v100, s87, 6
v_mov_b32 v99, v96
s_nop 1
v_max_u32_dpp v99, v99, v99 row_shr:1 row_mask:0xf bank_mask:0xf
s_nop 1
v_max_u32_dpp v99, v99, v99 row_shr:2 row_mask:0xf bank_mask:0xf
s_nop 1
v_max_u32_dpp v99, v99, v99 row_shr:4 row_mask:0xf bank_mask:0xf
s_nop 1
v_max_u32_dpp v99, v99, v99 row_shr:8 row_mask:0xf bank_mask:0xf
s_nop 1
v_max_u32_dpp v99, v99, v99 row_bcast:15 row_mask:0xa bank_mask:0xf
s_nop 1
v_max_u32_dpp v99, v99, v99 row_bcast:31 row_mask:0xc bank_mask:0xf
s_nop 0
v_readlane_b32 s86, v99, 63
s_nop 1
v_cmp_eq_u32 vcc, s86, v96
s_and_b32 s87, s86, 0xffff
s_sub_u32 s87, 2047, s87
v_cndmask_b32 v96, v96, v97, vcc
v_cndmask_b32_e64 v97, v97, 0, vcc
v_writelane_b32 v100, s87, 7
v_mov_b32 v99, v98
s_nop 1
v_max_u32_dpp v99, v99, v99 row_shr:1 row_mask:0xf bank_mask:0xf
s_nop 1
v_max_u32_dpp v99, v99, v99 row_shr:2 row_mask:0xf bank_mask:0xf
s_nop 1
v_max_u32_dpp v99, v99, v99 row_shr:4 row_mask:0xf bank_mask:0xf
s_nop 1
v_max_u32_dpp v99, v99, v99 row_shr:8 row_mask:0xf bank_mask:0xf
s_nop 1
v_max_u32_dpp v99, v99, v99 row_bcast:15 row_mask:0xa bank_mask:0xf
s_nop 1
v_max_u32_dpp v99, v99, v99 row_bcast:31 row_mask:0xc bank_mask:0xf
s_nop 0
v_readlane_b32 s87, v99, 63
s_nop 0
s_cmp_gt_u32 s87, s86
s_cbranch_scc0 .LL_28%=
s_mov_b32 m0, 0
.LL_29%=:
v_max3_u32 v96, v32, v34, v36
v_max3_u32 v96, v96, v38, v40
v_max3_u32 v96, v96, v42, v44
v_max3_u32 v96, v96, v46, v48
v_max3_u32 v96, v96, v50, v52
v_max3_u32 v96, v96, v54, v56
v_max3_u32 v96, v96, v58, v60
v_max3_u32 v96, v96, v62, v64
v_max3_u32 v96, v96, v66, v68
v_max3_u32 v96, v96, v70, v72
v_max3_u32 v96, v96, v74, v76
v_max3_u32 v96, v96, v78, v80
v_max3_u32 v96, v96, v82, v84
v_max3_u32 v96, v96, v86, v88
v_max3_u32 v96, v96, v90, v92
v_max_u32 v96, v96, v94
v_mov_b32 v99, v96
s_nop 1
v_max_u32_dpp v99, v99, v99 row_shr:1 row_mask:0xf bank_mask:0xf
s_nop 1
v_max_u32_dpp v99, v99, v99 row_shr:2 row_mask:0xf bank_mask:0xf
s_nop 1
v_max_u32_dpp v99, v99, v99 row_shr:4 row_mask:0xf bank_mask:0xf
s_nop 1
v_max_u32_dpp v99, v99, v99 row_shr:8 row_mask:0xf bank_mask:0xf
s_nop 1
v_max_u32_dpp v99, v99, v99 row_bcast:15 row_mask:0xa bank_mask:0xf
s_nop 1
v_max_u32_dpp v99, v99, v99 row_bcast:31 row_mask:0xc bank_mask:0xf
s_nop 0
v_readlane_b32 s86, v99, 63
s_nop 1
v_cmp_eq_u32 vcc, s86, v32
s_nop 1
v_cndmask_b32_e64 v32, v32, 0, vcc
v_cmp_eq_u32 vcc, s86, v34
s_nop 1
v_cndmask_b32_e64 v34, v34, 0, vcc
v_cmp_eq_u32 vcc, s86, v36
s_nop 1
v_cndmask_b32_e64 v36, v36, 0, vcc
v_cmp_eq_u32 vcc, s86, v38
s_nop 1
v_cndmask_b32_e64 v38, v38, 0, vcc
v_cmp_eq_u32 vcc, s86, v40
s_nop 1
v_cndmask_b32_e64 v40, v40, 0, vcc
v_cmp_eq_u32 vcc, s86, v42
s_nop 1
v_cndmask_b32_e64 v42, v42, 0, vcc
v_cmp_eq_u32 vcc, s86, v44
s_nop 1
v_cndmask_b32_e64 v44, v44, 0, vcc
v_cmp_eq_u32 vcc, s86, v46
s_nop 1
v_cndmask_b32_e64 v46, v46, 0, vcc
v_cmp_eq_u32 vcc, s86, v48
s_nop 1
v_cndmask_b32_e64 v48, v48, 0, vcc
v_cmp_eq_u32 vcc, s86, v50
s_nop 1
v_cndmask_b32_e64 v50, v50, 0, vcc
v_cmp_eq_u32 vcc, s86, v52
s_nop 1
v_cndmask_b32_e64 v52, v52, 0, vcc
v_cmp_eq_u32 vcc, s86, v54
s_nop 1
v_cndmask_b32_e64 v54, v54, 0, vcc
v_cmp_eq_u32 vcc, s86, v56
s_nop 1
v_cndmask_b32_e64 v56, v56, 0, vcc
v_cmp_eq_u32 vcc, s86, v58
s_nop 1
v_cndmask_b32_e64 v58, v58, 0, vcc
v_cmp_eq_u32 vcc, s86, v60
s_nop 1
v_cndmask_b32_e64 v60, v60, 0, vcc
v_cmp_eq_u32 vcc, s86, v62
s_nop 1
v_cndmask_b32_e64 v62, v62, 0, vcc
v_cmp_eq_u32 vcc, s86, v64
s_nop 1
v_cndmask_b32_e64 v64, v64, 0, vcc
v_cmp_eq_u32 vcc, s86, v66
s_nop 1
v_cndmask_b32_e64 v66, v66, 0, vcc
v_cmp_eq_u32 vcc, s86, v68
s_nop 1
v_cndmask_b32_e64 v68, v68, 0, vcc
v_cmp_eq_u32 vcc, s86, v70
s_nop 1
v_cndmask_b32_e64 v70, v70, 0, vcc
v_cmp_eq_u32 vcc, s86, v72
s_nop 1
v_cndmask_b32_e64 v72, v72, 0, vcc
v_cmp_eq_u32 vcc, s86, v74
s_nop 1
v_cndmask_b32_e64 v74, v74, 0, vcc
v_cmp_eq_u32 vcc, s86, v76
s_nop 1
v_cndmask_b32_e64 v76, v76, 0, vcc
v_cmp_eq_u32 vcc, s86, v78
s_nop 1
v_cndmask_b32_e64 v78, v78, 0, vcc
v_cmp_eq_u32 vcc, s86, v80
s_nop 1
v_cndmask_b32_e64 v80, v80, 0, vcc
v_cmp_eq_u32 vcc, s86, v82
s_nop 1
v_cndmask_b32_e64 v82, v82, 0, vcc
v_cmp_eq_u32 vcc, s86, v84
s_nop 1
v_cndmask_b32_e64 v84, v84, 0, vcc
v_cmp_eq_u32 vcc, s86, v86
s_nop 1
v_cndmask_b32_e64 v86, v86, 0, vcc
v_cmp_eq_u32 vcc, s86, v88
s_nop 1
v_cndmask_b32_e64 v88, v88, 0, vcc
v_cmp_eq_u32 vcc, s86, v90
s_nop 1
v_cndmask_b32_e64 v90, v90, 0, vcc
v_cmp_eq_u32 vcc, s86, v92
s_nop 1
v_cndmask_b32_e64 v92, v92, 0, vcc
v_cmp_eq_u32 vcc, s86, v94
s_nop 1
v_cndmask_b32_e64 v94, v94, 0, vcc
s_and_b32 s87, s86, 0xffff
s_sub_u32 s87, 2047, s87
s_nop 0
v_writelane_b32 v100, s87, m0
s_add_u32 m0, m0, 1
s_cmp_lt_u32 m0, 8
s_cbranch_scc1 .LL_29%=
.LL_28%=:
v_xor_b32 v102, s12, v100
v_mov_b32 v103, s13
s_mov_b64 exec, 0xff
buffer_store_dwordx2 v[102:103], v136, s[8:11], 0 offen offset:3584 sc1
s_mov_b64 exec, -1
s_branch .Lrtr_25%=
.Lspin_49%=:
s_mov_b32 s84, 0
.Llp_51%=:
s_sleep 2
buffer_load_dwordx2 v[8:9], v11, s[8:11], 0 offen offset:3840 sc1
s_waitcnt vmcnt(0)
s_add_u32 s84, s84, 1
v_cmp_eq_u64 vcc, s[12:13], v[8:9]
s_cmp_eq_u64 vcc, -1
s_cbranch_scc1 .Lok_50%=
s_cmp_lt_u32 s84, 1048576
s_cbranch_scc1 .Llp_51%=
s_branch .Lok_50%=
.Lredo%=:
s_waitcnt vmcnt(0) lgkmcnt(0)
s_barrier
s_and_b32 s81, s14, 1
s_mul_i32 s91, s81, 17164288
s_mul_i32 s82, s56, 536576
s_add_u32 s91, s91, s82
s_add_u32 s91, s91, 4194304
s_lshr_b32 s82, s14, 1
s_lshl_b32 s83, s82, 14
s_add_u32 s91, s91, s83
s_lshl_b32 s81, s81, 14
s_lshl_b32 s82, s82, 16
s_or_b32 s92, s81, s82
s_xor_b32 s92, s92, 0x10000
s_add_u32 m0, s92, 0
s_add_u32 s81, s91, 0
Dc 1,8,81,0
Dc 1,8,81,1024
Dc 1,8,81,2048
Dc 1,8,81,3072
s_add_u32 m0, s92, 4096
s_add_u32 s81, s91, 4096
Dc 1,8,81,0
Dc 1,8,81,1024
Dc 1,8,81,2048
Dc 1,8,81,3072
s_add_u32 m0, s92, 8192
s_add_u32 s81, s91, 8192
Dc 1,8,81,0
Dc 1,8,81,1024
Dc 1,8,81,2048
Dc 1,8,81,3072
s_add_u32 m0, s92, 12288
s_add_u32 s81, s91, 12288
Dc 1,8,81,0
Dc 1,8,81,1024
Dc 1,8,81,2048
Dc 1,8,81,3072
v_bfe_u32 v244, v1, 4, 1
v_mul_u32_u24 v244, 0xaaaa, v244
v_add_u32 v244, 0x4444, v244
v_lshl_or_b32 v240, v244, 16, v244
v_mov_b32 v241, 0xff800000
v_add_u32 v242, 0x10000, v1
v_bfe_u32 v244, v1, 4, 4
v_lshrrev_b32 v245, 8, v1
v_lshlrev_b32 v245, 2, v245
v_sub_u32 v244, v244, v245
v_cmp_le_i32_e64 s[72:73], 0, v244
v_cmp_le_i32_e64 s[74:75], 1, v244
v_cmp_le_i32_e64 s[76:77], 2, v244
v_cmp_le_i32_e64 s[78:79], 3, v244
v_add_u32 v243, 4096, v1
v_add_u32 v244, 8192, v1
v_add_u32 v245, 12288, v1
s_mov_b32 s18, 0x3f803f80
s_mov_b32 s19, 0x3f003f00
v_accvgpr_write_b32 a176, s18
v_accvgpr_write_b32 a180, s19
v_mov_b32 v252, v241
)"
R"(v_accvgpr_write_b32 a177, s18
v_accvgpr_write_b32 a181, s19
v_mov_b32 v253, v241
v_accvgpr_write_b32 a178, s18
v_accvgpr_write_b32 a182, s19
v_mov_b32 v254, v241
v_accvgpr_write_b32 a179, s18
v_accvgpr_write_b32 a183, s19
v_mov_b32 v255, v241
v_accvgpr_write_b32 a160, 0
v_accvgpr_write_b32 a161, 0
v_accvgpr_write_b32 a162, 0
v_accvgpr_write_b32 a163, 0
v_accvgpr_write_b32 a164, 0
v_accvgpr_write_b32 a165, 0
v_accvgpr_write_b32 a166, 0
v_accvgpr_write_b32 a167, 0
v_accvgpr_write_b32 a168, 0
v_accvgpr_write_b32 a169, 0
v_accvgpr_write_b32 a170, 0
v_accvgpr_write_b32 a171, 0
v_accvgpr_write_b32 a172, 0
v_accvgpr_write_b32 a173, 0
v_accvgpr_write_b32 a174, 0
v_accvgpr_write_b32 a175, 0
s_mov_b32 s90, 0x5d800000
s_mov_b64 s[88:89], 0
s_mov_b32 s64, 0
s_and_b32 s18, s14, 1
s_lshl_b32 s62, s18, 14
s_lshl_b32 s20, s57, 15
s_sub_u32 s20, 507904, s20
s_add_u32 s20, s20, s62
s_lshl_b32 s65, s57, 1
s_sub_u32 s65, s65, 1
s_max_i32 s65, s65, 0
s_sub_u32 s66, s60, 1
s_cmp_lt_u32 s14, 2
s_cselect_b32 s20, s20, 16384
s_cselect_b32 s66, s66, 15
s_cselect_b32 s21, 32768, 0
s_add_u32 s61, s91, s20
s_add_u32 s62, s62, s21
s_sub_u32 s63, 16384, s21
s_lshl_b32 s67, s60, 7
s_add_u32 s67, s67, s56
s_lshl_b32 s67, s67, 12
s_add_u32 s67, s67, 39845888
s_lshl_b32 s69, s60, 3
s_add_u32 s69, s69, s59
s_lshl_b32 s69, s69, 6
s_lshl_b32 s21, s58, 3
s_add_u32 s69, s69, s21
s_add_u32 s69, s69, 20480
s_mov_b32 s70, 0
s_mov_b32 s37, 0
v_bfe_u32 v28, v1, 4, 4
v_lshrrev_b32 v29, 8, v1
v_lshlrev_b32 v29, 2, v29
v_sub_u32 v28, v28, v29
s_mov_b64 s[24:25], 0
s_mov_b32 s68, 0
s_mov_b32 s85, 99
s_lshl_b32 s80, s60, 6
s_and_b32 s81, s14, 2
s_lshl_b32 s81, s81, 4
s_xor_b32 s82, s81, 0
s_add_u32 s82, s82, s80
v_add_u32 v4, s82, v28
s_xor_b32 s82, s81, 16
s_add_u32 s82, s82, s80
v_add_u32 v5, s82, v28
s_xor_b32 s82, s81, 32
s_add_u32 s82, s82, s80
v_add_u32 v6, s82, v28
s_xor_b32 s82, s81, 48
s_add_u32 s82, s82, s80
v_add_u32 v7, s82, v28
s_mul_i32 s82, s56, 536576
s_add_u32 s82, s82, 4194304
s_lshl_b32 s84, s60, 1
s_add_u32 s84, s84, 2
.LL_52%=:
buffer_load_dwordx4 a[128:131], v1, s[8:11], s82 offen offset:0 sc1
buffer_load_dwordx4 a[132:135], v1, s[8:11], s82 offen offset:1024 sc1
buffer_load_dwordx4 a[136:139], v1, s[8:11], s82 offen offset:2048 sc1
buffer_load_dwordx4 a[140:143], v1, s[8:11], s82 offen offset:3072 sc1
buffer_load_dwordx4 a[144:147], v243, s[8:11], s82 offen offset:0 sc1
buffer_load_dwordx4 a[148:151], v243, s[8:11], s82 offen offset:1024 sc1
buffer_load_dwordx4 a[152:155], v243, s[8:11], s82 offen offset:2048 sc1
buffer_load_dwordx4 a[156:159], v243, s[8:11], s82 offen offset:3072 sc1
s_waitcnt vmcnt(0)
Zvaa 160,128,0
Zvaa 164,128,16
Zvaa 168,128,32
Zvaa 172,128,48
Mvaa 160,132,4
Mvaa 164,132,20
Mvaa 168,132,36
Mvaa 172,132,52
Mvaa 160,136,8
Mvaa 164,136,24
Mvaa 168,136,40
Mvaa 172,136,56
Mvaa 160,140,12
Mvaa 164,140,28
Mvaa 168,140,44
Mvaa 172,140,60
Zvaa 176,144,0
Zvaa 180,144,16
Zvaa 184,144,32
Zvaa 188,144,48
Mvaa 176,148,4
Mvaa 180,148,20
Mvaa 184,148,36
Mvaa 188,148,52
Mvaa 176,152,8
Mvaa 180,152,24
Mvaa 184,152,40
Mvaa 188,152,56
Mvaa 176,156,12
Mvaa 180,156,28
Mvaa 184,156,44
Mvaa 188,156,60
v_cmp_le_i32 vcc, 0, v4
s_nop 1
v_cndmask_b32 v160, v241, v160, vcc
v_max_f32 v252, v252, v160
v_cmp_le_i32 vcc, 1, v4
s_nop 1
v_cndmask_b32 v161, v241, v161, vcc
v_max_f32 v252, v252, v161
v_cmp_le_i32 vcc, 2, v4
s_nop 1
v_cndmask_b32 v162, v241, v162, vcc
v_max_f32 v252, v252, v162
v_cmp_le_i32 vcc, 3, v4
s_nop 1
v_cndmask_b32 v163, v241, v163, vcc
v_max_f32 v252, v252, v163
v_cmp_le_i32 vcc, 0, v5
s_nop 1
v_cndmask_b32 v164, v241, v164, vcc
v_max_f32 v253, v253, v164
v_cmp_le_i32 vcc, 1, v5
s_nop 1
v_cndmask_b32 v165, v241, v165, vcc
v_max_f32 v253, v253, v165
v_cmp_le_i32 vcc, 2, v5
s_nop 1
v_cndmask_b32 v166, v241, v166, vcc
v_max_f32 v253, v253, v166
v_cmp_le_i32 vcc, 3, v5
s_nop 1
v_cndmask_b32 v167, v241, v167, vcc
v_max_f32 v253, v253, v167
v_cmp_le_i32 vcc, 0, v6
s_nop 1
v_cndmask_b32 v168, v241, v168, vcc
v_max_f32 v254, v254, v168
v_cmp_le_i32 vcc, 1, v6
s_nop 1
v_cndmask_b32 v169, v241, v169, vcc
v_max_f32 v254, v254, v169
v_cmp_le_i32 vcc, 2, v6
s_nop 1
v_cndmask_b32 v170, v241, v170, vcc
v_max_f32 v254, v254, v170
v_cmp_le_i32 vcc, 3, v6
s_nop 1
v_cndmask_b32 v171, v241, v171, vcc
v_max_f32 v254, v254, v171
v_cmp_le_i32 vcc, 0, v7
s_nop 1
v_cndmask_b32 v172, v241, v172, vcc
v_max_f32 v255, v255, v172
v_cmp_le_i32 vcc, 1, v7
s_nop 1
v_cndmask_b32 v173, v241, v173, vcc
v_max_f32 v255, v255, v173
v_cmp_le_i32 vcc, 2, v7
s_nop 1
v_cndmask_b32 v174, v241, v174, vcc
v_max_f32 v255, v255, v174
v_cmp_le_i32 vcc, 3, v7
s_nop 1
v_cndmask_b32 v175, v241, v175, vcc
v_max_f32 v255, v255, v175
v_cmp_le_i32 vcc, 16, v4
s_nop 1
v_cndmask_b32 v176, v241, v176, vcc
v_max_f32 v252, v252, v176
v_cmp_le_i32 vcc, 17, v4
s_nop 1
v_cndmask_b32 v177, v241, v177, vcc
v_max_f32 v252, v252, v177
v_cmp_le_i32 vcc, 18, v4
s_nop 1
v_cndmask_b32 v178, v241, v178, vcc
v_max_f32 v252, v252, v178
v_cmp_le_i32 vcc, 19, v4
s_nop 1
v_cndmask_b32 v179, v241, v179, vcc
v_max_f32 v252, v252, v179
v_cmp_le_i32 vcc, 16, v5
s_nop 1
v_cndmask_b32 v180, v241, v180, vcc
v_max_f32 v253, v253, v180
v_cmp_le_i32 vcc, 17, v5
s_nop 1
v_cndmask_b32 v181, v241, v181, vcc
v_max_f32 v253, v253, v181
v_cmp_le_i32 vcc, 18, v5
s_nop 1
v_cndmask_b32 v182, v241, v182, vcc
v_max_f32 v253, v253, v182
v_cmp_le_i32 vcc, 19, v5
s_nop 1
v_cndmask_b32 v183, v241, v183, vcc
v_max_f32 v253, v253, v183
v_cmp_le_i32 vcc, 16, v6
s_nop 1
v_cndmask_b32 v184, v241, v184, vcc
v_max_f32 v254, v254, v184
v_cmp_le_i32 vcc, 17, v6
s_nop 1
v_cndmask_b32 v185, v241, v185, vcc
v_max_f32 v254, v254, v185
v_cmp_le_i32 vcc, 18, v6
s_nop 1
v_cndmask_b32 v186, v241, v186, vcc
v_max_f32 v254, v254, v186
v_cmp_le_i32 vcc, 19, v6
s_nop 1
v_cndmask_b32 v187, v241, v187, vcc
v_max_f32 v254, v254, v187
v_cmp_le_i32 vcc, 16, v7
s_nop 1
v_cndmask_b32 v188, v241, v188, vcc
v_max_f32 v255, v255, v188
v_cmp_le_i32 vcc, 17, v7
s_nop 1
v_cndmask_b32 v189, v241, v189, vcc
v_max_f32 v255, v255, v189
v_cmp_le_i32 vcc, 18, v7
s_nop 1
v_cndmask_b32 v190, v241, v190, vcc
v_max_f32 v255, v255, v190
v_cmp_le_i32 vcc, 19, v7
s_nop 1
v_cndmask_b32 v191, v241, v191, vcc
v_max_f32 v255, v255, v191
v_subrev_u32 v4, 32, v4
v_subrev_u32 v5, 32, v5
v_subrev_u32 v6, 32, v6
v_subrev_u32 v7, 32, v7
s_add_u32 s82, s82, 8192
s_sub_u32 s84, s84, 1
s_cmp_lg_u32 s84, 0
s_cbranch_scc1 .LL_52%=
G 28,0
v_lshlrev_b32 v32, 16, v28
v_and_b32 v33, 0xffff0000, v28
G 28,1
v_lshlrev_b32 v34, 16, v28
v_and_b32 v35, 0xffff0000, v28
G 28,2
v_lshlrev_b32 v36, 16, v28
v_and_b32 v37, 0xffff0000, v28
G 28,3
v_lshlrev_b32 v38, 16, v28
v_and_b32 v39, 0xffff0000, v28
G 28,4
v_lshlrev_b32 v40, 16, v28
v_and_b32 v41, 0xffff0000, v28
G 28,5
v_lshlrev_b32 v42, 16, v28
v_and_b32 v43, 0xffff0000, v28
G 28,6
v_lshlrev_b32 v44, 16, v28
v_and_b32 v45, 0xffff0000, v28
G 28,7
v_lshlrev_b32 v46, 16, v28
v_and_b32 v47, 0xffff0000, v28
G 28,8
v_lshlrev_b32 v48, 16, v28
v_and_b32 v49, 0xffff0000, v28
G 28,9
v_lshlrev_b32 v50, 16, v28
v_and_b32 v51, 0xffff0000, v28
G 28,10
v_lshlrev_b32 v52, 16, v28
v_and_b32 v53, 0xffff0000, v28
G 28,11
v_lshlrev_b32 v54, 16, v28
v_and_b32 v55, 0xffff0000, v28
G 28,12
v_lshlrev_b32 v56, 16, v28
v_and_b32 v57, 0xffff0000, v28
G 28,13
v_lshlrev_b32 v58, 16, v28
v_and_b32 v59, 0xffff0000, v28
G 28,14
v_lshlrev_b32 v60, 16, v28
v_and_b32 v61, 0xffff0000, v28
G 28,15
v_lshlrev_b32 v62, 16, v28
v_and_b32 v63, 0xffff0000, v28
G 28,16
v_lshlrev_b32 v64, 16, v28
v_and_b32 v65, 0xffff0000, v28
G 28,17
v_lshlrev_b32 v66, 16, v28
v_and_b32 v67, 0xffff0000, v28
G 28,18
v_lshlrev_b32 v68, 16, v28
v_and_b32 v69, 0xffff0000, v28
G 28,19
v_lshlrev_b32 v70, 16, v28
v_and_b32 v71, 0xffff0000, v28
G 28,20
v_lshlrev_b32 v72, 16, v28
v_and_b32 v73, 0xffff0000, v28
G 28,21
v_lshlrev_b32 v74, 16, v28
v_and_b32 v75, 0xffff0000, v28
G 28,22
v_lshlrev_b32 v76, 16, v28
v_and_b32 v77, 0xffff0000, v28
G 28,23
v_lshlrev_b32 v78, 16, v28
v_and_b32 v79, 0xffff0000, v28
G 28,24
v_lshlrev_b32 v80, 16, v28
v_and_b32 v81, 0xffff0000, v28
G 28,25
v_lshlrev_b32 v82, 16, v28
v_and_b32 v83, 0xffff0000, v28
G 28,26
v_lshlrev_b32 v84, 16, v28
v_and_b32 v85, 0xffff0000, v28
G 28,27
v_lshlrev_b32 v86, 16, v28
v_and_b32 v87, 0xffff0000, v28
G 28,28
v_lshlrev_b32 v88, 16, v28
v_and_b32 v89, 0xffff0000, v28
G 28,29
v_lshlrev_b32 v90, 16, v28
v_and_b32 v91, 0xffff0000, v28
G 28,30
v_lshlrev_b32 v92, 16, v28
v_and_b32 v93, 0xffff0000, v28
G 28,31
v_lshlrev_b32 v94, 16, v28
v_and_b32 v95, 0xffff0000, v28
G 28,32
v_lshlrev_b32 v96, 16, v28
v_and_b32 v97, 0xffff0000, v28
G 28,33
v_lshlrev_b32 v98, 16, v28
v_and_b32 v99, 0xffff0000, v28
G 28,34
v_lshlrev_b32 v100, 16, v28
v_and_b32 v101, 0xffff0000, v28
G 28,35
v_lshlrev_b32 v102, 16, v28
v_and_b32 v103, 0xffff0000, v28
G 28,36
v_lshlrev_b32 v104, 16, v28
v_and_b32 v105, 0xffff0000, v28
G 28,37
v_lshlrev_b32 v106, 16, v28
v_and_b32 v107, 0xffff0000, v28
G 28,38
v_lshlrev_b32 v108, 16, v28
v_and_b32 v109, 0xffff0000, v28
G 28,39
v_lshlrev_b32 v110, 16, v28
v_and_b32 v111, 0xffff0000, v28
G 28,40
v_lshlrev_b32 v112, 16, v28
v_and_b32 v113, 0xffff0000, v28
G 28,41
v_lshlrev_b32 v114, 16, v28
v_and_b32 v115, 0xffff0000, v28
G 28,42
v_lshlrev_b32 v116, 16, v28
v_and_b32 v117, 0xffff0000, v28
G 28,43
v_lshlrev_b32 v118, 16, v28
v_and_b32 v119, 0xffff0000, v28
G 28,44
v_lshlrev_b32 v120, 16, v28
v_and_b32 v121, 0xffff0000, v28
G 28,45
v_lshlrev_b32 v122, 16, v28
v_and_b32 v123, 0xffff0000, v28
G 28,46
v_lshlrev_b32 v124, 16, v28
)"
R"(v_and_b32 v125, 0xffff0000, v28
G 28,47
v_lshlrev_b32 v126, 16, v28
v_and_b32 v127, 0xffff0000, v28
G 28,48
v_lshlrev_b32 v128, 16, v28
v_and_b32 v129, 0xffff0000, v28
G 28,49
v_lshlrev_b32 v130, 16, v28
v_and_b32 v131, 0xffff0000, v28
G 28,50
v_lshlrev_b32 v132, 16, v28
v_and_b32 v133, 0xffff0000, v28
G 28,51
v_lshlrev_b32 v134, 16, v28
v_and_b32 v135, 0xffff0000, v28
G 28,52
v_lshlrev_b32 v136, 16, v28
v_and_b32 v137, 0xffff0000, v28
G 28,53
v_lshlrev_b32 v138, 16, v28
v_and_b32 v139, 0xffff0000, v28
G 28,54
v_lshlrev_b32 v140, 16, v28
v_and_b32 v141, 0xffff0000, v28
G 28,55
v_lshlrev_b32 v142, 16, v28
v_and_b32 v143, 0xffff0000, v28
G 28,56
v_lshlrev_b32 v144, 16, v28
v_and_b32 v145, 0xffff0000, v28
G 28,57
v_lshlrev_b32 v146, 16, v28
v_and_b32 v147, 0xffff0000, v28
G 28,58
v_lshlrev_b32 v148, 16, v28
v_and_b32 v149, 0xffff0000, v28
G 28,59
v_lshlrev_b32 v150, 16, v28
v_and_b32 v151, 0xffff0000, v28
G 28,60
v_lshlrev_b32 v152, 16, v28
v_and_b32 v153, 0xffff0000, v28
G 28,61
v_lshlrev_b32 v154, 16, v28
v_and_b32 v155, 0xffff0000, v28
G 28,62
v_lshlrev_b32 v156, 16, v28
v_and_b32 v157, 0xffff0000, v28
G 28,63
v_lshlrev_b32 v158, 16, v28
v_and_b32 v159, 0xffff0000, v28
s_mov_b32 s22, 1.0
s_mov_b32 s23, 1.0
s_mov_b32 s71, 3584
s_barrier
Ra 128,242,0
Ra 132,242,1024
Ra 136,242,2048
Ra 140,242,3072
s_cmp_ge_u32 s14, 2
s_cbranch_scc1 .Lbgo%=
s_branch .Lago%=
.Lpb_30%=:
s_waitcnt vmcnt(4) lgkmcnt(6)
Zvaa 32,0,32
s_add_u32 s28, s28, 512
Ta 20,7,0
Ta 22,7,1024
Zvaa 64,0,36
s_cmp_eq_u32 s28, s4
s_cselect_b32 s93, 10240, 0
Ta 24,8,0
Ta 26,8,1024
Zvaa 96,0,40
s_sub_u32 s28, s28, s93
s_add_u32 s93, s28, 20480
buffer_load_dwordx4 a[128:131], v26, s[8:11], s28 offen offset:0 sc1
Zvaa 128,0,44
Ta 28,9,0
Ta 30,9,1024
Zvaa 132,4,44
K 160,160,161
Zvaa 100,4,40
K 161,162,163
K 162,164,165
Zvaa 68,4,36
buffer_load_dwordx4 a[132:135], v26, s[8:11], s93 offen offset:0 sc1
K 163,166,167
Zvaa 36,4,32
W 8
Ta 0,2,8192
Ta 2,2,9216
Zvaa 40,8,32
K 168,168,169
K 169,170,171
Zvaa 72,8,36
K 170,172,173
K 171,174,175
Zvaa 104,8,40
buffer_load_dwordx4 a[136:139], v27, s[8:11], s28 offen offset:0 sc1
G 164,184
Zvaa 136,8,44
Ta 4,3,8192
G 165,185
Zvaa 140,12,44
Ta 6,3,9216
G 166,186
Zvaa 108,12,40
G 167,187
G 172,188
Zvaa 76,12,36
buffer_load_dwordx4 a[140:143], v27, s[8:11], s93 offen offset:0 sc1
G 173,189
Zvaa 44,12,32
W 8
s_add_u32 s28, s28, 512
s_cmp_eq_u32 s28, s4
s_cselect_b32 s93, 10240, 0
Zvaa 48,16,32
s_sub_u32 s28, s28, s93
s_add_u32 s93, s28, 20480
Ta 8,4,8192
Zvaa 80,16,36
Ta 10,4,9216
G 174,190
Zvaa 112,16,40
buffer_load_dwordx4 a[144:147], v26, s[8:11], s28 offen offset:0 sc1
G 175,191
Zvaa 144,16,44
Ta 12,5,8192
K 164,164,165
Zvaa 148,20,44
Ta 14,5,9216
K 165,166,167
Zvaa 116,20,40
K 166,172,173
K 167,174,175
Zvaa 84,20,36
buffer_load_dwordx4 a[148:151], v26, s[8:11], s93 offen offset:0 sc1
v_accvgpr_write_b32 a184, v164
Zvaa 52,20,32
W 8
Ta 16,6,8192
Ta 18,6,9216
Zvaa 56,24,32
v_accvgpr_write_b32 a185, v165
v_accvgpr_write_b32 a186, v166
Zvaa 88,24,36
v_accvgpr_write_b32 a187, v167
Zvaa 120,24,40
buffer_load_dwordx4 a[152:155], v27, s[8:11], s28 offen offset:0 sc1
Ta 20,7,8192
Zvaa 152,24,44
Ta 22,7,9216
K 184,184,185
Zvaa 156,28,44
K 185,186,187
Zvaa 124,28,40
K 186,188,189
Zvaa 92,28,36
buffer_load_dwordx4 a[156:159], v27, s[8:11], s93 offen offset:0 sc1
K 187,190,191
Zvaa 60,28,32
s_waitcnt vmcnt(8) lgkmcnt(8)
Ta 24,8,8192
Ta 26,8,9216
Mvaa 32,0,48
s_add_u32 s28, s28, 512
s_cmp_eq_u32 s28, s4
s_cselect_b32 s93, 10240, 0
v_accvgpr_write_b32 a188, v184
Mvaa 64,0,52
s_sub_u32 s28, s28, s93
s_add_u32 s93, s28, 20480
v_accvgpr_write_b32 a189, v185
Mvaa 96,0,56
D 16,48,65,0
v_accvgpr_write_b32 a190, v186
Mvaa 128,0,60
Ta 28,9,8192
v_accvgpr_write_b32 a191, v187
Mvaa 132,4,60
Ta 30,9,9216
G 164,192
Mvaa 100,4,56
G 165,193
Mvaa 68,4,52
D 17,48,65,1024
G 166,194
Mvaa 36,4,48
W 8
Ta 0,2,16384
Ta 2,2,17408
Mvaa 40,8,48
G 167,195
G 172,196
Mvaa 72,8,52
G 173,197
Mvaa 104,8,56
D 18,48,65,2048
G 174,198
Mvaa 136,8,60
Ta 4,3,16384
Ta 6,3,17408
Mvaa 140,12,60
G 175,199
K 164,164,165
Mvaa 108,12,56
Mvaa 76,12,52
D 19,48,65,3072
K 165,166,167
Mvaa 44,12,48
W 8
s_add_u32 m0, s63, 36864
Ta 8,4,16384
Mvaa 48,16,48
Ta 10,4,17408
K 166,172,173
Mvaa 80,16,52
K 167,174,175
v_accvgpr_write_b32 a192, v164
Mvaa 112,16,56
buffer_load_dwordx4 a[32:35], v26, s[8:11], s28 offen offset:0 sc1
v_accvgpr_write_b32 a193, v165
Mvaa 144,16,60
Ta 12,5,16384
Ta 14,5,17408
Mvaa 148,20,60
v_accvgpr_write_b32 a194, v166
v_accvgpr_write_b32 a195, v167
Mvaa 116,20,56
K 192,192,193
Mvaa 84,20,52
buffer_load_dwordx4 a[36:39], v26, s[8:11], s93 offen offset:0 sc1
Ta 16,6,16384
Mvaa 52,20,48
W 9
Ta 18,6,17408
K 193,194,195
Mvaa 56,24,48
K 194,196,197
Mvaa 88,24,52
K 195,198,199
Mvaa 120,24,56
buffer_load_dwordx4 a[40:43], v27, s[8:11], s28 offen offset:0 sc1
v_accvgpr_write_b32 a196, v192
Mvaa 152,24,60
Ta 20,7,16384
Ta 22,7,17408
Mvaa 156,28,60
v_accvgpr_write_b32 a197, v193
v_accvgpr_write_b32 a198, v194
Mvaa 124,28,56
v_accvgpr_write_b32 a199, v195
Mvaa 92,28,52
buffer_load_dwordx4 a[44:47], v27, s[8:11], s93 offen offset:0 sc1
Ta 24,8,16384
Mvaa 60,28,48
s_waitcnt vmcnt(12) lgkmcnt(9)
s_add_u32 s28, s28, 512
s_cmp_eq_u32 s28, s4
s_cselect_b32 s93, 10240, 0
Mvaa 32,0,128
s_sub_u32 s28, s28, s93
s_add_u32 s93, s28, 20480
Ta 26,8,17408
Mvaa 64,0,132
G 164,200
G 165,201
Mvaa 96,0,136
D 20,48,65,0
G 166,202
Mvaa 128,0,140
Ta 28,9,16384
G 167,203
Mvaa 132,4,140
Ta 30,9,17408
G 172,204
Mvaa 100,4,136
G 173,205
G 174,206
Mvaa 68,4,132
buffer_load_dwordx4 v[184:187], v26, s[8:11], s28 offen offset:0 sc1
G 175,207
Mvaa 36,4,128
W 8
Ta 0,2,24576
Ta 2,2,25600
Mvaa 40,8,128
K 164,164,165
Mvaa 72,8,132
K 165,166,167
Mvaa 104,8,136
buffer_load_dwordx4 v[188:191], v26, s[8:11], s93 offen offset:0 sc1
K 166,172,173
Mvaa 136,8,140
Ta 4,3,24576
Ta 6,3,25600
Mvaa 140,12,140
K 167,174,175
v_accvgpr_write_b32 a200, v164
Mvaa 108,12,136
v_accvgpr_write_b32 a201, v165
Mvaa 76,12,132
D 21,48,65,1024
v_accvgpr_write_b32 a202, v166
Mvaa 44,12,128
W 8
Ta 8,4,24576
Ta 10,4,25600
Mvaa 48,16,128
v_accvgpr_write_b32 a203, v167
K 240,240,241
Mvaa 80,16,132
Mvaa 112,16,136
D 22,48,65,2048
K 241,242,243
Mvaa 144,16,140
Ta 12,5,24576
Ta 14,5,25600
Mvaa 148,20,140
K 242,244,245
K 243,246,247
Mvaa 116,20,136
v_accvgpr_write_b32 a204, v240
Mvaa 84,20,132
D 23,48,65,3072
v_accvgpr_write_b32 a205, v241
Mvaa 52,20,128
W 8
s_add_u32 s65, s5, 16
s_cmp_ge_u32 s65, 20
s_cselect_b32 s68, 20, 0
Mvaa 56,24,128
s_sub_u32 s65, s65, s68
Ta 16,6,24576
Ta 18,6,25600
Mvaa 88,24,132
v_accvgpr_write_b32 a206, v242
v_accvgpr_write_b32 a207, v243
Mvaa 120,24,136
buffer_load_dwordx4 a[48:51], v27, s[8:11], s28 offen offset:0 sc1
G 164,208
Mvaa 152,24,140
Ta 20,7,24576
Ta 22,7,25600
Mvaa 156,28,140
s_lshl_b32 s65, s65, 18
G 165,209
G 166,210
Mvaa 124,28,136
G 167,211
Mvaa 92,28,132
buffer_load_dwordx4 a[52:55], v27, s[8:11], s93 offen offset:0 sc1
G 172,212
Mvaa 60,28,128
s_waitcnt vmcnt(16) lgkmcnt(8)
s_add_u32 s28, s28, 512
s_cmp_eq_u32 s28, s4
s_cselect_b32 s93, 10240, 0
Mvaa 32,0,144
s_sub_u32 s28, s28, s93
s_add_u32 s93, s28, 20480
Ta 24,8,24576
Mvaa 64,0,148
Ta 26,8,25600
G 173,213
Mvaa 96,0,152
buffer_load_dwordx4 a[56:59], v26, s[8:11], s28 offen offset:0 sc1
G 174,214
Mvaa 128,0,156
s_add_u32 m0, s63, 98304
Ta 28,9,24576
G 175,215
Mvaa 132,4,156
s_lshl_b32 s80, s56, 9
Ta 30,9,25600
K 164,164,165
Mvaa 100,4,152
K 165,166,167
K 166,172,173
Mvaa 68,4,148
s_lshl_b32 s81, s57, 4
buffer_load_dwordx4 a[60:63], v26, s[8:11], s93 offen offset:0 sc1
K 167,174,175
Mvaa 36,4,144
W 8
Ta 0,10,0
Ta 2,10,1024
Mvaa 40,8,144
s_add_u32 s80, s80, s81
v_accvgpr_write_b32 a208, v164
v_accvgpr_write_b32 a209, v165
Mvaa 72,8,148
s_and_b32 s81, s14, 1
v_accvgpr_write_b32 a210, v166
Mvaa 104,8,152
s_lshl_b32 s81, s81, 3
buffer_load_dwordx4 v[192:195], v27, s[8:11], s28 offen offset:0 sc1
)"
R"(v_accvgpr_write_b32 a211, v167
Mvaa 136,8,156
Ta 4,11,0
Ta 6,11,1024
Mvaa 140,12,156
s_add_u32 s80, s80, s81
K 248,248,249
Mvaa 108,12,152
s_add_u32 s71, s80, 4096
K 249,250,251
Mvaa 76,12,148
buffer_load_dwordx4 v[196:199], v27, s[8:11], s93 offen offset:0 sc1
K 250,252,253
Mvaa 44,12,144
W 8
s_add_u32 s28, s28, 512
s_cmp_eq_u32 s28, s4
s_cselect_b32 s93, 10240, 0
Mvaa 48,16,144
s_sub_u32 s28, s28, s93
s_add_u32 s93, s28, 20480
Ta 8,12,0
Mvaa 80,16,148
Ta 10,12,1024
K 251,254,255
Mvaa 112,16,152
buffer_load_dwordx4 a[128:131], v26, s[8:11], s28 offen offset:0 sc1
v_accvgpr_write_b32 a212, v248
Mvaa 144,16,156
v_accvgpr_write_b32 a213, v249
v_accvgpr_write_b32 a214, v250
Mvaa 148,20,156
Ta 12,13,0
Ta 14,13,1024
Mvaa 116,20,152
v_accvgpr_write_b32 a215, v251
G 164,216
Mvaa 84,20,148
buffer_load_dwordx4 a[132:135], v26, s[8:11], s93 offen offset:0 sc1
G 165,217
Mvaa 52,20,144
W 8
Ta 16,14,0
Ta 18,14,1024
Mvaa 56,24,144
s_lshr_b32 s80, s15, 3
G 166,218
G 167,219
Mvaa 88,24,148
G 172,220
Mvaa 120,24,152
s_lshl_b32 s80, s80, 1
buffer_load_dwordx4 a[136:139], v27, s[8:11], s28 offen offset:0 sc1
G 173,221
Mvaa 152,24,156
s_add_u32 s80, s80, s14
Ta 20,15,0
Ta 22,15,1024
Mvaa 156,28,156
s_sub_u32 s80, s80, 2
G 174,222
G 175,223
Mvaa 124,28,152
s_lshl_b32 s80, s80, 12
K 164,164,165
Mvaa 92,28,148
buffer_load_dwordx4 a[140:143], v27, s[8:11], s93 offen offset:0 sc1
v_min_u32 v28, 496, v1
Mvaa 60,28,144
s_waitcnt vmcnt(16) lgkmcnt(8)
Ta 24,24,0
Ta 26,24,1024
Mvaa 32,0,32
s_add_u32 s28, s28, 512
K 165,166,167
v_lshl_add_u32 v28, v28, 3, s80
Mvaa 64,0,36
s_cmp_eq_u32 s28, s4
s_cselect_b32 s93, 10240, 0
s_sub_u32 s28, s28, s93
s_add_u32 s93, s28, 20480
Mvaa 96,0,40
D 16,48,65,0
K 166,172,173
Mvaa 128,0,44
v_min_u32 v28, .Lend%=-.Lbeg%=-4, v28
K 167,174,175
Mvaa 132,4,44
Ta 28,25,0
v_accvgpr_write_b32 a216, v164
Mvaa 100,4,40
Ta 30,25,1024
v_accvgpr_write_b32 a217, v165
Mvaa 68,4,36
D 17,48,65,1024
v_accvgpr_write_b32 a218, v166
Mvaa 36,4,32
W 8
Ta 0,10,8192
v_accvgpr_write_b32 a219, v167
Mvaa 40,8,32
Ta 2,10,9216
v_add_u32 v2, 65536, v10
Mvaa 72,8,36
K 200,200,201
K 201,202,203
Mvaa 104,8,40
D 18,48,65,2048
K 202,204,205
Mvaa 136,8,44
Ta 4,11,8192
K 203,206,207
Mvaa 140,12,44
Ta 6,11,9216
v_add_u32 v3, 65536, v11
Mvaa 108,12,40
v_accvgpr_write_b32 a220, v200
v_accvgpr_write_b32 a221, v201
Mvaa 76,12,36
buffer_load_dwordx4 a[144:147], v26, s[8:11], s28 offen offset:0 sc1
v_accvgpr_write_b32 a222, v202
Mvaa 44,12,32
W 8
Ta 8,12,8192
v_accvgpr_write_b32 a223, v203
Mvaa 48,16,32
Ta 10,12,9216
v_add_u32 v4, 65536, v12
Mvaa 80,16,36
G 164,224
G 165,225
Mvaa 112,16,40
buffer_load_dwordx4 a[148:151], v26, s[8:11], s93 offen offset:0 sc1
G 166,226
Mvaa 144,16,44
Ta 12,13,8192
Ta 14,13,9216
Mvaa 148,20,44
v_add_u32 v5, 65536, v13
G 167,227
Mvaa 116,20,40
G 172,228
G 173,229
Mvaa 84,20,36
buffer_load_dwordx4 a[152:155], v27, s[8:11], s28 offen offset:0 sc1
G 174,230
Mvaa 52,20,32
W 8
Ta 16,14,8192
Ta 18,14,9216
Mvaa 56,24,32
v_add_u32 v6, 65536, v14
G 175,231
Mvaa 88,24,36
K 164,164,165
Mvaa 120,24,40
buffer_load_dwordx4 a[156:159], v27, s[8:11], s93 offen offset:0 sc1
K 165,166,167
Mvaa 152,24,44
s_add_u32 s28, s28, 512
s_cmp_eq_u32 s28, s4
s_cselect_b32 s93, 10240, 0
s_sub_u32 s28, s28, s93
Mvaa 156,28,44
s_add_u32 s93, s28, 20480
Ta 20,15,8192
Ta 22,15,9216
Mvaa 124,28,40
v_add_u32 v7, 65536, v15
K 166,172,173
Mvaa 92,28,36
D 19,48,65,3072
K 167,174,175
Mvaa 60,28,32
s_waitcnt vmcnt(16) lgkmcnt(8)
s_add_u32 m0, s63, 102400
v_accvgpr_write_b32 a224, v164
Mvav 32,0,184
Ta 24,24,8192
v_accvgpr_write_b32 a225, v165
Mvav 64,0,188
Ta 26,24,9216
v_add_u32 v8, 65536, v24
Mvaa 96,0,48
global_load_dword v29, v28, s[6:7]
v_accvgpr_write_b32 a226, v166
Mvaa 128,0,52
s_sub_u32 s74, s85, 66
s_and_b32 s75, s74, 0xffffffe3
s_cselect_b64 s[72:73], -1, 0
s_and_b32 s74, s74, 28
Mvaa 132,4,52
s_lshl_b32 s74, s74, 1
v_mov_b32 v28, s74
v_accvgpr_write_b32 a227, v167
Mvaa 100,4,48
Ta 28,25,8192
K 208,208,209
Mvav 68,4,188
D 20,48,65,0
Ta 30,25,9216
Mvav 36,4,184
W 8
v_add_u32 v9, 65536, v25
K 209,210,211
Mvav 40,8,184
K 210,212,213
K 211,214,215
Mvav 72,8,188
Ta 0,10,16384
v_accvgpr_write_b32 a228, v208
Mvaa 104,8,48
D 21,48,65,1024
Ta 2,10,17408
Mvaa 136,8,52
v_accvgpr_write_b32 a229, v209
v_accvgpr_write_b32 a230, v210
Mvaa 140,12,52
Ta 4,11,16384
v_accvgpr_write_b32 a231, v211
Mvaa 108,12,48
Ta 6,11,17408
G 164,232
Mvav 76,12,188
buffer_load_dwordx4 v[200:203], v26, s[8:11], s28 offen offset:0 sc1
G 165,233
Mvav 44,12,184
W 8
G 166,234
G 167,235
Mvav 48,16,184
Ta 8,12,16384
G 172,236
Mvav 80,16,188
Ta 10,12,17408
G 173,237
Mvaa 112,16,48
buffer_load_dwordx4 v[204:207], v26, s[8:11], s93 offen offset:0 sc1
G 174,238
Mvaa 144,16,52
G 175,239
K 164,164,165
Mvaa 148,20,52
Ta 12,13,16384
K 165,166,167
Mvaa 116,20,48
Ta 14,13,17408
K 166,172,173
Mvav 84,20,188
buffer_load_dwordx2 v[28:29], v28, s[8:11], 0 offen offset:3584 sc1
K 167,174,175
Mvav 52,20,184
W 8
v_accvgpr_write_b32 a232, v164
v_accvgpr_write_b32 a233, v165
Mvav 56,24,184
Ta 16,14,16384
v_accvgpr_write_b32 a234, v166
Mvav 88,24,188
Ta 18,14,17408
v_accvgpr_write_b32 a235, v167
Mvaa 120,24,48
D 22,48,65,2048
G 164,160
Mvaa 152,24,52
G 165,161
G 166,162
Mvaa 156,28,52
Ta 20,15,16384
Ta 22,15,17408
Mvaa 124,28,48
G 167,163
G 172,164
Mvav 92,28,188
buffer_load_dwordx4 a[32:35], v27, s[8:11], s28 offen offset:0 sc1
G 173,165
Mvav 60,28,184
s_waitcnt vmcnt(20) lgkmcnt(8)
G 174,166
G 175,167
Mvaa 32,0,56
Ta 24,24,16384
Ta 26,24,17408
Mvaa 64,0,60
K 164,164,165
K 165,166,167
Mvav 96,0,192
buffer_load_dwordx4 a[36:39], v27, s[8:11], s93 offen offset:0 sc1
K 166,172,173
Mvav 128,0,196
s_add_u32 s28, s28, 512
s_cmp_eq_u32 s28, s4
s_cselect_b32 s93, 10240, 0
s_sub_u32 s28, s28, s93
Mvav 132,4,196
s_add_u32 s93, s28, 20480
K 167,174,175
v_accvgpr_write_b32 a236, v164
Mvav 100,4,192
Ta 28,25,16384
Ta 30,25,17408
Mvaa 68,4,60
buffer_load_dwordx4 a[40:43], v26, s[8:11], s28 offen offset:0 sc1
v_accvgpr_write_b32 a237, v165
Mvaa 36,4,56
W 8
v_accvgpr_write_b32 a238, v166
v_accvgpr_write_b32 a239, v167
Mvaa 40,8,56
G 164,240
G 165,241
Mvaa 72,8,60
Ta 0,10,24576
Ta 2,10,25600
Mvav 104,8,192
D 23,48,65,3072
G 166,242
Mvav 136,8,196
G 167,243
G 172,244
Mvav 140,12,196
Ta 4,11,24576
G 173,245
Mvav 108,12,192
Ta 6,11,25600
G 174,246
Mvaa 76,12,60
buffer_load_dwordx4 a[44:47], v26, s[8:11], s93 offen offset:0 sc1
G 175,247
Mvaa 44,12,56
W 8
K 164,164,165
K 165,166,167
Mvaa 48,16,56
Ta 8,12,24576
K 166,172,173
Mvaa 80,16,60
Ta 10,12,25600
K 167,174,175
Mvav 112,16,192
s_cmp_lg_u32 s72, 0
s_cbranch_scc1 .LL_53%=
s_waitcnt vmcnt(6)
.LL_53%=:
v_xor_b32 v28, s12, v28
v_xor_b32 v29, s13, v29
v_lshrrev_b32 v248, 11, v28
v_or_b32 v29, v29, v248
v_cmp_eq_u32 vcc, 0, v29
s_or_b64 vcc, vcc, s[72:73]
s_cmp_eq_u64 vcc, -1
s_cbranch_scc0 .Lspin_54%=
.Lok_55%=:
buffer_load_dwordx4 v[208:211], v27, s[8:11], s28 offen offset:0 sc1
v_accvgpr_write_b32 a240, v164
Mvav 144,16,196
v_accvgpr_write_b32 a241, v165
v_accvgpr_write_b32 a242, v166
Mvav 148,20,196
Ta 12,13,24576
v_accvgpr_write_b32 a243, v167
Mvav 116,20,192
Ta 14,13,25600
G 164,168
Mvaa 84,20,60
buffer_load_dwordx4 v[212:215], v27, s[8:11], s93 offen offset:0 sc1
G 165,169
Mvaa 52,20,56
W 8
s_add_u32 s28, s28, 512
s_cmp_eq_u32 s28, s4
s_cselect_b32 s93, 10240, 0
Mvaa 56,24,56
s_sub_u32 s28, s28, s93
s_add_u32 s93, s28, 20480
G 166,170
Mvaa 88,24,60
G 167,171
G 172,172
Mvav 120,24,192
buffer_load_dwordx4 v[184:187], v26, s[8:11], s28 offen offset:0 sc1
Ta 16,14,24576
Mvav 152,24,196
Ta 18,14,25600
v_bfe_u32 v248, v28, 3, 8
Mvav 156,28,196
G 173,173
v_mul_u32_u24 v248, 10240, v248
Mvav 124,28,192
G 174,174
v_and_b32 v28, 7, v28
Mvaa 92,28,60
buffer_load_dwordx4 v[188:191], v26, s[8:11], s93 offen offset:0 sc1
G 175,175
Mvaa 60,28,56
s_waitcnt vmcnt(24) lgkmcnt(6)
K 164,164,165
v_lshl_add_u32 v248, v28, 4, v248
Mvaa 32,0,128
Ta 20,15,24576
)"
R"(Ta 22,15,25600
Mvaa 64,0,132
K 165,166,167
v_lshl_add_u32 v248, v1, 3, v248
Mvaa 96,0,136
buffer_load_dwordx4 a[48:51], v27, s[8:11], s28 offen offset:0 sc1
K 166,172,173
Mvaa 128,0,140
K 167,174,175
v_lshrrev_b32 v29, 8, v1
Mvaa 132,4,140
v_accvgpr_write_b32 a244, v164
v_lshlrev_b32 v29, 28, v29
Mvaa 100,4,136
Ta 24,24,24576
v_accvgpr_write_b32 a245, v165
Mvaa 68,4,132
buffer_load_dwordx4 a[52:55], v27, s[8:11], s93 offen offset:0 sc1
Ta 26,24,25600
Mvaa 36,4,128
W 6
v_or_b32 v249, v248, v29
v_accvgpr_write_b32 a246, v166
Mvaa 40,8,128
s_and_b32 s75, s72, 134217728
s_add_u32 s28, s28, 512
v_accvgpr_write_b32 a247, v167
Mvaa 72,8,132
s_cmp_eq_u32 s28, s4
s_cselect_b32 s93, 10240, 0
s_sub_u32 s28, s28, s93
s_add_u32 s93, s28, 20480
Mvaa 104,8,136
s_add_u32 s76, s75, 131072
buffer_load_dwordx4 a[56:59], v26, s[8:11], s28 offen offset:0 sc1
G 164,64
Mvaa 136,8,140
G 165,65
G 166,66
Mvaa 140,12,140
Ta 28,25,24576
Ta 30,25,25600
Mvaa 108,12,136
G 167,67
G 172,68
Mvaa 76,12,132
buffer_load_dwordx4 v[240:243], v248, s[8:11], s76 offen sc1
G 173,69
Mvaa 44,12,128
W 4
s_add_u32 s76, s76, 8192
G 174,70
Mvaa 48,16,128
Ta 0,2,0
G 175,71
Mvaa 80,16,132
Ta 2,2,1024
K 164,164,165
Mvaa 112,16,136
buffer_load_dwordx4 v[244:247], v249, s[8:11], s76 offen sc1
v_or_b32 v249, v1, v29
Mvaa 144,16,140
s_mul_i32 s76, s74, 160
s_add_u32 s76, s76, s75
K 165,166,167
Mvaa 148,20,140
s_add_u32 s76, s76, 118784
K 166,172,173
K 167,174,175
Mvaa 116,20,136
Ta 4,3,0
Ta 6,3,1024
Mvaa 84,20,132
buffer_load_dwordx4 a[60:63], v26, s[8:11], s93 offen offset:0 sc1
v_accvgpr_write_b32 a64, v164
Mvaa 52,20,128
W 4
v_accvgpr_write_b32 a65, v165
v_accvgpr_write_b32 a66, v166
Mvaa 56,24,128
v_accvgpr_write_b32 a67, v167
G 164,176
Mvaa 88,24,132
Ta 8,4,0
Ta 10,4,1024
Mvaa 120,24,136
G 165,177
G 166,178
Mvaa 152,24,140
buffer_load_dwordx4 v[192:195], v27, s[8:11], s28 offen offset:0 sc1
G 167,179
Mvaa 156,28,140
G 172,180
G 173,181
Mvaa 124,28,136
Ta 12,5,0
Ta 14,5,1024
Mvaa 92,28,132
G 174,182
G 175,183
Mvaa 60,28,128
s_waitcnt vmcnt(24) lgkmcnt(4)
K 164,164,165
K 165,166,167
Mvaa 32,0,144
Ta 16,6,0
K 166,172,173
Mvaa 64,0,148
Ta 18,6,1024
K 167,174,175
Mvaa 96,0,152
buffer_load_dwordx4 v[196:199], v27, s[8:11], s93 offen offset:0 sc1
v_accvgpr_write_b32 a68, v164
Mvaa 128,0,156
v_accvgpr_write_b32 a69, v165
v_accvgpr_write_b32 a70, v166
v_accvgpr_write_b32 a71, v167
Mvaa 132,4,156
s_add_u32 s28, s28, 512
Ta 20,7,0
Ta 22,7,1024
Mvaa 100,4,152
s_cmp_eq_u32 s28, s4
s_cselect_b32 s93, 10240, 0
G 164,72
G 165,73
Mvaa 68,4,148
s_sub_u32 s28, s28, s93
Ta 24,8,0
G 166,74
Mvaa 36,4,144
W 5
Ta 26,8,1024
G 167,75
Mvaa 40,8,144
s_add_u32 s93, s28, 20480
buffer_load_dwordx4 a[128:131], v26, s[8:11], s28 offen offset:0 sc1
G 172,76
Mvaa 72,8,148
G 173,77
G 174,78
Mvaa 104,8,152
Ta 28,9,0
Ta 30,9,1024
Mvaa 136,8,156
G 175,79
K 164,164,165
Mvaa 140,12,156
Ta 0,2,8192
K 165,166,167
Mvaa 108,12,152
Ta 2,2,9216
K 166,172,173
Mvaa 76,12,148
buffer_load_dwordx4 a[132:135], v26, s[8:11], s93 offen offset:0 sc1
K 167,174,175
Mvaa 44,12,144
W 6
v_accvgpr_write_b32 a72, v164
v_accvgpr_write_b32 a73, v165
Mvaa 48,16,144
Ta 4,3,8192
Ta 6,3,9216
Mvaa 80,16,148
v_accvgpr_write_b32 a74, v166
v_accvgpr_write_b32 a75, v167
Mvaa 112,16,152
Ta 8,4,8192
K 216,216,217
Mvaa 144,16,156
Ta 10,4,9216
K 217,218,219
Mvaa 148,20,156
buffer_load_dwordx4 a[136:139], v27, s[8:11], s28 offen offset:0 sc1
K 218,220,221
Mvaa 116,20,152
K 219,222,223
v_accvgpr_write_b32 a76, v216
Mvaa 84,20,148
Ta 12,5,8192
Ta 14,5,9216
Mvaa 52,20,144
W 8
v_lshrrev_b32 v20, 4, v1
v_accvgpr_write_b32 a77, v217
Mvaa 56,24,144
v_accvgpr_write_b32 a78, v218
v_add_u32 v20, 0, v20
Mvaa 88,24,148
v_accvgpr_write_b32 a79, v219
v_mul_u32_u24 v22, 205, v20
Mvaa 120,24,152
Ta 16,6,8192
Ta 18,6,9216
Mvaa 152,24,156
G 164,80
G 165,81
Mvaa 156,28,156
buffer_load_dwordx4 a[140:143], v27, s[8:11], s93 offen offset:0 sc1
v_lshrrev_b32 v22, 12, v22
Mvaa 124,28,152
G 166,82
G 167,83
Mvaa 92,28,148
Ta 20,7,8192
v_mul_u32_u24 v23, 20, v22
Mvaa 60,28,144
s_waitcnt vmcnt(19) lgkmcnt(7)
Ta 22,7,9216
G 172,84
Mvav 32,0,200
s_add_u32 s28, s28, 512
v_sub_u32 v20, v20, v23
G 173,85
Mvav 64,0,204
s_cmp_eq_u32 s28, s4
s_cselect_b32 s93, 10240, 0
G 174,86
v_lshrrev_b32 v23, 1, v22
Mvaa 96,0,32
s_sub_u32 s28, s28, s93
Ta 24,8,8192
G 175,87
Mvaa 128,0,36
s_add_u32 s93, s28, 20480
Ta 26,8,9216
K 164,164,165
Mvaa 132,4,36
buffer_load_dwordx4 v[216:219], v26, s[8:11], s28 offen offset:0 sc1
v_and_b32 v23, 2, v23
Mvaa 100,4,32
K 165,166,167
v_xor_b32 v20, v20, v23
Mvav 68,4,204
Ta 28,9,8192
K 166,172,173
Mvav 36,4,200
W 7
Ta 30,9,9216
K 167,174,175
Mvav 40,8,200
v_mul_u32_u24 v22, 80, v22
v_accvgpr_write_b32 a80, v164
Mvav 72,8,204
v_accvgpr_write_b32 a81, v165
v_add_lshl_u32 v16, v20, v22, 4
Mvaa 104,8,32
Ta 0,2,16384
v_accvgpr_write_b32 a82, v166
Mvaa 136,8,36
Ta 2,2,17408
v_lshrrev_b32 v20, 4, v1
Mvaa 140,12,36
buffer_load_dwordx4 v[220:223], v26, s[8:11], s93 offen offset:0 sc1
v_accvgpr_write_b32 a83, v167
Mvaa 108,12,32
K 224,224,225
v_add_u32 v20, 64, v20
Mvav 76,12,204
Ta 4,3,16384
K 225,226,227
Mvav 44,12,200
W 7
Ta 6,3,17408
K 226,228,229
Mvav 48,16,200
v_mul_u32_u24 v22, 205, v20
K 227,230,231
Mvav 80,16,204
v_lshrrev_b32 v22, 12, v22
v_accvgpr_write_b32 a84, v224
Mvaa 112,16,32
Ta 8,4,16384
v_accvgpr_write_b32 a85, v225
Mvaa 144,16,36
Ta 10,4,17408
v_mul_u32_u24 v23, 20, v22
Mvaa 148,20,36
v_accvgpr_write_b32 a86, v226
v_accvgpr_write_b32 a87, v227
Mvaa 116,20,32
buffer_load_dwordx4 a[144:147], v27, s[8:11], s28 offen offset:0 sc1
v_sub_u32 v20, v20, v23
Mvav 84,20,204
Ta 12,5,16384
G 164,88
Mvav 52,20,200
W 7
Ta 14,5,17408
G 165,89
Mvav 56,24,200
v_lshrrev_b32 v23, 1, v22
G 166,90
Mvav 88,24,204
v_and_b32 v23, 2, v23
G 167,91
Mvaa 120,24,32
G 172,92
v_xor_b32 v20, v20, v23
Mvaa 152,24,36
Ta 16,6,16384
Ta 18,6,17408
Mvaa 156,28,36
G 173,93
G 174,94
Mvaa 124,28,32
buffer_load_dwordx4 a[148:151], v27, s[8:11], s93 offen offset:0 sc1
v_mul_u32_u24 v22, 80, v22
Mvav 92,28,204
G 175,95
v_add_lshl_u32 v17, v20, v22, 4
Mvav 60,28,200
s_waitcnt vmcnt(18) lgkmcnt(6)
Ta 20,7,16384
Ta 22,7,17408
Mvaa 32,0,40
s_add_u32 s28, s28, 512
K 164,164,165
K 165,166,167
Mvaa 64,0,44
s_cmp_eq_u32 s28, s4
s_cselect_b32 s93, 10240, 0
v_subrev_u32 v17, 1024, v17
K 166,172,173
Mvav 96,0,208
s_sub_u32 s28, s28, s93
K 167,174,175
v_lshrrev_b32 v20, 4, v1
Mvav 128,0,212
s_add_u32 s93, s28, 20480
Ta 24,8,16384
Ta 26,8,17408
Mvav 132,4,212
s_cmp_lg_u32 s72, 0
s_cbranch_scc1 .LL_57%=
s_waitcnt vmcnt(11)
.LL_57%=:
buffer_store_dwordx4 v[240:243], v1, s[8:11], s76 offen sc1
v_accvgpr_write_b32 a88, v164
v_add_u32 v20, 128, v20
Mvav 100,4,208
v_accvgpr_write_b32 a89, v165
v_accvgpr_write_b32 a90, v166
Mvaa 68,4,44
v_mul_u32_u24 v22, 205, v20
v_accvgpr_write_b32 a91, v167
Mvaa 36,4,40
W 6
Ta 28,9,16384
Ta 30,9,17408
Mvaa 40,8,40
buffer_load_dwordx4 a[152:155], v26, s[8:11], s28 offen offset:0 sc1
K 232,232,233
Mvaa 72,8,44
v_lshrrev_b32 v22, 12, v22
K 233,234,235
Mvav 104,8,208
v_mul_u32_u24 v23, 20, v22
K 234,236,237
Mvav 136,8,212
Ta 0,2,24576
Ta 2,2,25600
Mvav 140,12,212
buffer_store_dwordx4 v[244:247], v249, s[8:11], s76 offen offset:1024 sc1
K 235,238,239
Mvav 108,12,208
s_add_u32 s76, s74, s75
s_add_u32 s76, s76, 3840
v_sub_u32 v20, v20, v23
Mvaa 76,12,44
v_accvgpr_write_b32 a92, v232
v_accvgpr_write_b32 a93, v233
Mvaa 44,12,40
W 6
v_lshrrev_b32 v23, 1, v22
v_accvgpr_write_b32 a94, v234
Mvaa 48,16,40
buffer_load_dwordx4 a[156:159], v26, s[8:11], s93 offen offset:0 sc1
Ta 4,3,24576
Mvaa 80,16,44
Ta 6,3,25600
v_and_b32 v23, 2, v23
Mvav 112,16,208
v_accvgpr_write_b32 a95, v235
G 164,96
Mvav 144,16,212
v_xor_b32 v20, v20, v23
G 165,97
Mvav 148,20,212
Ta 8,4,24576
G 166,98
Mvav 116,20,208
Ta 10,4,25600
v_mul_u32_u24 v22, 80, v22
Mvaa 84,20,44
G 167,99
G 172,100
Mvaa 52,20,40
W 6
buffer_load_dwordx4 v[224:227], v27, s[8:11], s28 offen offset:0 sc1
)"
R"(G 173,101
Mvaa 56,24,40
v_add_lshl_u32 v18, v20, v22, 4
G 174,102
Mvaa 88,24,44
Ta 12,5,24576
G 175,103
Mvav 120,24,208
Ta 14,5,25600
v_subrev_u32 v18, 2048, v18
Mvav 152,24,212
K 164,164,165
K 165,166,167
Mvav 156,28,212
v_lshrrev_b32 v20, 4, v1
K 166,172,173
Mvav 124,28,208
Ta 16,6,24576
K 167,174,175
Mvaa 92,28,44
Ta 18,6,25600
v_add_u32 v20, 192, v20
Mvaa 60,28,40
s_waitcnt vmcnt(19) lgkmcnt(6)
v_accvgpr_write_b32 a96, v164
v_accvgpr_write_b32 a97, v165
Mvav 32,0,184
v_accvgpr_write_b32 a98, v166
v_mul_u32_u24 v22, 205, v20
Mvav 64,0,188
buffer_load_dwordx4 v[228:231], v27, s[8:11], s93 offen offset:0 sc1
v_accvgpr_write_b32 a99, v167
Mvaa 96,0,48
s_add_u32 s28, s28, 512
G 164,248
v_lshrrev_b32 v22, 12, v22
Mvaa 128,0,52
s_cmp_eq_u32 s28, s4
s_cselect_b32 s93, 10240, 0
Ta 20,7,24576
Ta 22,7,25600
Mvaa 132,4,52
s_sub_u32 s28, s28, s93
G 165,249
G 166,250
Mvaa 100,4,48
s_add_u32 s93, s28, 20480
v_mul_u32_u24 v23, 20, v22
G 167,251
Mvav 68,4,188
G 172,252
v_sub_u32 v20, v20, v23
Mvav 36,4,184
W 4
Ta 24,8,24576
G 173,253
Mvav 40,8,184
Ta 26,8,25600
G 174,254
Mvav 72,8,188
Ta 28,9,24576
Ta 30,9,25600
Mvaa 104,8,48
G 175,255
v_lshrrev_b32 v23, 1, v22
Mvaa 136,8,52
buffer_load_dwordx4 v[200:203], v26, s[8:11], s28 offen offset:0 sc1
K 164,164,165
Mvaa 140,12,52
W 0
s_barrier
K 165,166,167
Mvaa 108,12,48
v_and_b32 v23, 2, v23
K 166,172,173
Mvav 76,12,188
K 167,174,175
v_xor_b32 v20, v20, v23
Mvav 44,12,184
v_accvgpr_write_b32 a100, v164
v_accvgpr_write_b32 a101, v165
v_accvgpr_write_b32 a102, v166
v_accvgpr_write_b32 a103, v167
Mvav 48,16,184
v_mul_u32_u24 v22, 80, v22
G 164,104
Mvav 80,16,188
buffer_load_dwordx4 v[204:207], v26, s[8:11], s93 offen offset:0 sc1
v_add_lshl_u32 v19, v20, v22, 4
Mvaa 112,16,48
G 165,105
v_subrev_u32 v19, 3072, v19
Mvaa 144,16,52
G 166,106
G 167,107
Mvaa 148,20,52
v_lshrrev_b32 v20, 4, v1
G 172,108
Mvaa 116,20,48
Ta 0,10,32768
Ta 2,10,33792
Mvav 84,20,188
Ta 4,11,32768
Ta 6,11,33792
Mvav 52,20,184
G 173,109
v_add_u32 v20, 256, v20
Mvav 56,24,184
Ta 8,12,32768
G 174,110
Mvav 88,24,188
Ta 10,12,33792
v_mul_u32_u24 v22, 205, v20
Mvaa 120,24,48
buffer_load_dwordx4 a[32:35], v27, s[8:11], s28 offen offset:0 sc1
G 175,111
Mvaa 152,24,52
K 164,164,165
v_lshrrev_b32 v22, 12, v22
Mvaa 156,28,52
Ta 12,13,32768
K 165,166,167
Mvaa 124,28,48
Ta 14,13,33792
K 166,172,173
Mvav 92,28,188
v_mul_u32_u24 v23, 20, v22
K 167,174,175
Mvav 60,28,184
s_waitcnt vmcnt(17) lgkmcnt(4)
v_sub_u32 v20, v20, v23
v_accvgpr_write_b32 a104, v164
Mvaa 32,0,56
Ta 16,14,32768
v_accvgpr_write_b32 a105, v165
Mvaa 64,0,60
Ta 18,14,33792
v_lshrrev_b32 v23, 1, v22
Mvav 96,0,192
v_accvgpr_write_b32 a106, v166
v_accvgpr_write_b32 a107, v167
Mvav 128,0,196
buffer_load_dwordx4 a[36:39], v27, s[8:11], s93 offen offset:0 sc1
v_and_b32 v23, 2, v23
Mvav 132,4,196
s_add_u32 s28, s28, 512
K 176,176,177
v_xor_b32 v20, v20, v23
Mvav 100,4,192
s_cmp_eq_u32 s28, s4
s_cselect_b32 s93, 10240, 0
Ta 20,15,32768
Ta 22,15,33792
Mvaa 68,4,60
s_sub_u32 s28, s28, s93
K 177,178,179
K 178,180,181
Mvaa 36,4,56
W 4
v_mul_u32_u24 v22, 80, v22
K 179,182,183
Mvaa 40,8,56
s_add_u32 s93, s28, 20480
v_accvgpr_write_b32 a108, v176
v_add_lshl_u32 v20, v20, v22, 4
Mvaa 72,8,60
Ta 24,24,32768
Ta 26,24,33792
Mvav 104,8,192
v_accvgpr_write_b32 a109, v177
v_accvgpr_write_b32 a110, v178
v_accvgpr_write_b32 a111, v179
Mvav 136,8,196
buffer_load_dwordx4 v[232:235], v26, s[8:11], s28 offen offset:0 sc1
v_lshrrev_b32 v22, 6, v1
Mvav 140,12,196
Ta 28,25,32768
v_mul_u32_u24 v22, 320, v22
Mvav 108,12,192
Ta 30,25,33792
G 164,112
Mvaa 76,12,60
G 165,113
v_bfe_u32 v23, v1, 4, 2
Mvaa 44,12,56
W 4
G 166,114
v_lshl_add_u32 v22, v23, 3, v22
Mvaa 48,16,56
s_cmp_lg_u32 s72, 0
s_cbranch_scc1 .LL_58%=
s_waitcnt vmcnt(8)
.LL_58%=:
v_mov_b64 v[28:29], s[12:13]
s_mov_b64 exec, 1
buffer_store_dwordx2 v[28:29], off, s[8:11], s76 sc1
s_mov_b64 exec, -1
Ta 0,10,40960
Ta 2,10,41984
Mvaa 80,16,60
G 167,115
v_bfe_u32 v23, v1, 8, 1
Mvav 112,16,192
G 172,116
G 173,117
Mvav 144,16,196
Ta 4,11,40960
v_lshlrev_b32 v23, 5, v23
Mvav 148,20,196
buffer_load_dwordx4 v[236:239], v26, s[8:11], s93 offen offset:0 sc1
Ta 6,11,41984
Mvav 116,20,192
G 174,118
v_add_u32 v21, v22, v23
Mvaa 84,20,60
G 175,119
K 164,164,165
Mvaa 52,20,56
W 4
Ta 8,12,40960
v_sub_u32 v22, v22, v23
Mvaa 56,24,56
s_mul_i32 s94, s59, 655360
Ta 10,12,41984
K 165,166,167
Mvaa 88,24,60
s_mul_i32 s95, s58, 320
buffer_load_dwordx4 a[40:43], v27, s[8:11], s28 offen offset:0 sc1
K 166,172,173
Mvav 120,24,192
s_add_u32 s94, s94, s95
Ta 12,13,40960
K 167,174,175
Mvav 152,24,196
Ta 14,13,41984
v_accvgpr_write_b32 a112, v164
Mvav 156,28,196
s_add_u32 s40, s26, s94
s_addc_u32 s41, s27, 0
v_accvgpr_write_b32 a113, v165
Mvav 124,28,192
s_and_b32 s41, s41, 0xffff
Ta 16,14,40960
v_accvgpr_write_b32 a114, v166
Mvaa 92,28,60
s_mov_b32 s42, 5242880
Ta 18,14,41984
v_accvgpr_write_b32 a115, v167
Mvaa 60,28,56
s_waitcnt vmcnt(18) lgkmcnt(6)
buffer_load_dwordx4 a[44:47], v27, s[8:11], s93 offen offset:0 sc1
v_accvgpr_write_b32 a116, v168
Mvaa 32,0,128
s_mov_b32 s43, 0x20000
Ta 20,15,40960
v_accvgpr_write_b32 a117, v169
Mvaa 64,0,132
s_sub_u32 s42, s42, s94
Ta 22,15,41984
v_accvgpr_write_b32 a118, v170
Mvaa 96,0,136
s_mov_b64 s[44:45], s[8:9]
v_accvgpr_write_b32 a119, v171
G 164,120
Mvaa 128,0,140
s_add_u32 s28, s28, 512
Ta 24,24,40960
Ta 26,24,41984
Mvaa 132,4,140
s_cmp_eq_u32 s28, s4
s_cselect_b32 s93, 10240, 0
G 165,121
G 166,122
Mvaa 100,4,136
s_sub_u32 s28, s28, s93
s_add_u32 s93, s28, 20480
buffer_load_dwordx4 v[208:211], v26, s[8:11], s28 offen offset:0 sc1
Mvaa 68,4,132
s_mov_b32 s47, 0x20000
Ta 28,25,40960
G 167,123
Mvaa 36,4,128
W 7
Ta 30,25,41984
G 172,124
Mvaa 40,8,128
s_and_b32 s48, s14, 1
G 173,125
v_bfe_u32 v28, v1, 4, 4
Mvaa 72,8,132
s_mul_i32 s48, s48, 5120
G 174,126
v_lshlrev_b32 v28, 3, v28
Mvaa 104,8,136
s_lshl_b32 s80, s56, 9
Ta 0,10,49152
Ta 2,10,50176
Mvaa 136,8,140
s_lshr_b32 s51, s14, 1
s_add_u32 s80, s80, 4096
G 175,127
Mvaa 140,12,140
s_and_b32 s94, s14, 1
buffer_load_dwordx4 v[212:215], v26, s[8:11], s93 offen offset:0 sc1
K 164,164,165
Mvaa 108,12,136
s_lshl_b32 s95, s57, 1
Ta 4,11,49152
K 165,166,167
Mvaa 76,12,132
s_add_u32 s94, s94, s95
Ta 6,11,50176
K 166,172,173
Mvaa 44,12,128
W 8
K 167,174,175
v_accvgpr_write_b32 a120, v164
Mvaa 48,16,128
s_lshl_b32 s54, s94, 3
buffer_load_dwordx2 v[28:29], v28, s[8:11], s80 offen sc1
Ta 8,12,49152
Mvaa 80,16,132
s_add_u32 s54, s54, s59
Ta 10,12,50176
v_accvgpr_write_b32 a121, v165
Mvaa 112,16,136
v_accvgpr_write_b32 a122, v166
v_accvgpr_write_b32 a123, v167
Mvaa 144,16,140
s_lshl_b32 s54, s54, 6
Ta 12,13,49152
Ta 14,13,50176
Mvaa 148,20,140
buffer_load_dwordx4 a[248:251], v27, s[8:11], s28 offen offset:0 sc1
v_accvgpr_write_b32 a124, v160
Mvaa 116,20,136
s_xor_b32 s55, s54, 15872
v_accvgpr_write_b32 a125, v161
Mvaa 84,20,132
Ta 16,14,49152
v_accvgpr_write_b32 a126, v162
Mvaa 52,20,128
W 9
s_add_u32 s54, s54, 20480
Ta 18,14,50176
Mvaa 56,24,128
s_and_b32 s81, s14, 1
buffer_load_dwordx4 a[252:255], v27, s[8:11], s93 offen offset:0 sc1
v_accvgpr_write_b32 a127, v163
Mvaa 88,24,132
s_add_u32 s55, s55, 20480
Mvaa 120,24,136
Ta 20,15,49152
Ta 22,15,50176
Mvaa 152,24,140
s_and_b32 s95, s14, 2
s_mul_i32 s91, s81, 17164288
Mvaa 156,28,140
Mvaa 124,28,136
s_lshl_b32 s94, s94, 2
s_mul_i32 s82, s56, 536576
Mvaa 92,28,132
s_add_u32 s94, s94, s95
Ta 24,24,49152
Ta 26,24,50176
Mvaa 60,28,128
s_waitcnt vmcnt(20) lgkmcnt(10)
Mvav 32,0,216
s_add_u32 s91, s91, s82
s_lshl_b32 s95, s95, 4
s_add_u32 s28, s28, 512
Mvav 64,0,220
s_cmp_eq_u32 s28, s4
s_cselect_b32 s93, 10240, 0
s_sub_u32 s28, s28, s93
s_add_u32 s93, s28, 20480
Mvaa 96,0,144
s_add_u32 s55, s55, s95
buffer_load_dwordx4 v[184:187], v26, s[8:11], s28 offen offset:0 sc1
Ta 28,25,49152
Mvaa 128,0,148
s_add_u32 s91, s91, 4194304
Ta 30,25,50176
Mvaa 132,4,148
s_lshl_b32 s49, s94, 17
Mvaa 100,4,144
s_lshr_b32 s82, s14, 1
Mvav 68,4,220
s_lshl_b32 s95, s59, 14
Ta 0,10,57344
Ta 2,10,58368
Mvav 36,4,216
W 10
s_lshl_b32 s83, s82, 14
Mvav 40,8,216
s_add_u32 s49, s49, s95
buffer_load_dwordx4 v[188:191], v26, s[8:11], s93 offen offset:0 sc1
Mvav 72,8,220
Mvaa 104,8,144
s_xor_b32 s50, s49, 16252928
)"
R"(s_add_u32 s91, s91, s83
Ta 4,11,57344
Mvaa 136,8,148
Ta 6,11,58368
Mvaa 140,12,148
s_add_u32 s49, s49, 39845888
s_lshl_b32 s81, s81, 14
Mvaa 108,12,144
Mvav 76,12,220
s_add_u32 s50, s50, 39845888
Ta 8,12,57344
Ta 10,12,58368
Mvav 44,12,216
W 10
s_lshl_b32 s82, s82, 16
s_lshr_b32 s94, s15, 3
Mvav 48,16,216
buffer_load_dwordx4 a[48:51], v27, s[8:11], s28 offen offset:0 sc1
Mvav 80,16,220
s_lshl_b32 s94, s94, 3
s_or_b32 s92, s81, s82
Mvaa 112,16,144
Ta 12,13,57344
Ta 14,13,58368
Mvaa 144,16,148
s_add_u32 s94, s94, s14
Mvaa 148,20,148
s_xor_b32 s92, s92, 0x10000
Mvaa 116,20,144
s_lshl_b32 s94, s94, 4
Mvav 84,20,220
Ta 16,14,57344
Ta 18,14,58368
Mvav 52,20,216
W 10
s_add_u32 s94, s94, s59
Mvav 56,24,216
buffer_load_dwordx4 a[52:55], v27, s[8:11], s93 offen offset:0 sc1
Mvav 88,24,220
s_mul_i32 s52, s94, 5120
Mvaa 120,24,144
Ta 20,15,57344
Ta 22,15,58368
Mvaa 152,24,148
s_add_u32 s52, s52, 56623104
Mvaa 156,28,148
Mvaa 124,28,144
s_lshl_b32 s53, s94, 3
Mvav 92,28,220
Ta 24,24,57344
Ta 26,24,58368
Mvav 60,28,216
s_waitcnt vmcnt(18) lgkmcnt(10)
s_add_u32 s53, s53, 65536
Mvaa 32,0,152
s_waitcnt vmcnt(6)
v_cmp_eq_u64 vcc, s[12:13], v[28:29]
s_cmp_eq_u64 vcc, -1
s_cbranch_scc0 .Lspin_59%=
.Lok_60%=:
v_bfe_u32 v244, v1, 4, 1
Mvaa 64,0,156
s_sub_u32 s82, s91, 16384
Mvav 96,0,224
Ta 28,25,57344
v_mul_u32_u24 v244, 0xaaaa, v244
Mvav 128,0,228
Ta 30,25,58368
Mvav 132,4,228
s_xor_b32 s83, s92, 0x10000
v_add_u32 v244, 0x4444, v244
Mvav 100,4,224
v_lshl_or_b32 v240, v244, 16, v244
Mvaa 68,4,156
s_add_u32 m0, s83, 0
Ta 0,2,32768
Ta 2,2,33792
Mvaa 36,4,152
W 10
v_mov_b32 v241, 0xff800000
Mvaa 40,8,152
s_add_u32 s81, s82, 0
Mvaa 72,8,156
v_add_u32 v242, 0x10000, v1
Mvav 104,8,224
Dc 1,8,81,0
v_bfe_u32 v244, v1, 4, 4
Mvav 136,8,228
Ta 4,3,32768
Ta 6,3,33792
Mvav 140,12,228
v_lshrrev_b32 v245, 8, v1
Mvav 108,12,224
v_lshlrev_b32 v245, 2, v245
Mvaa 76,12,156
Dc 1,8,81,1024
Ta 8,4,32768
Mvaa 44,12,152
W 9
Ta 10,4,33792
v_sub_u32 v244, v244, v245
Mvaa 48,16,152
Mvaa 80,16,156
v_cmp_le_i32_e64 s[72:73], 0, v244
Mvav 112,16,224
Dc 1,8,81,2048
v_cmp_le_i32_e64 s[74:75], 1, v244
Mvav 144,16,228
Ta 12,5,32768
Ta 14,5,33792
Mvav 148,20,228
v_cmp_le_i32_e64 s[76:77], 2, v244
Mvav 116,20,224
v_cmp_le_i32_e64 s[78:79], 3, v244
Mvaa 84,20,156
Dc 1,8,81,3072
Ta 16,6,32768
Mvaa 52,20,152
W 9
s_add_u32 m0, s83, 4096
s_add_u32 s81, s82, 4096
Mvaa 56,24,152
Ta 18,6,33792
v_add_u32 v243, 4096, v1
Mvaa 88,24,156
v_add_u32 v244, 8192, v1
Mvav 120,24,224
Dc 1,8,81,0
v_add_u32 v245, 12288, v1
Mvav 152,24,228
Ta 20,7,32768
Ta 22,7,33792
Mvav 156,28,228
s_mov_b32 s18, 0x3f803f80
Mvav 124,28,224
s_mov_b32 s19, 0x3f003f00
Mvaa 92,28,156
Dc 1,8,81,1024
Ta 24,8,32768
Mvaa 60,28,152
W 9
Ta 26,8,33792
v_accvgpr_write_b32 a176, s18
Mvav 32,0,200
Mvav 64,0,204
v_accvgpr_write_b32 a180, s19
Mvaa 96,0,32
Dc 1,8,81,2048
v_mov_b32 v252, v241
Mvaa 128,0,36
Ta 28,9,32768
Ta 30,9,33792
Mvaa 132,4,36
v_accvgpr_write_b32 a177, s18
Mvaa 100,4,32
v_accvgpr_write_b32 a181, s19
Mvav 68,4,204
Dc 1,8,81,3072
Ta 0,2,40960
Mvav 36,4,200
W 9
s_add_u32 m0, s83, 8192
s_add_u32 s81, s82, 8192
Mvav 40,8,200
Ta 2,2,41984
v_mov_b32 v253, v241
Mvav 72,8,204
v_accvgpr_write_b32 a178, s18
Mvaa 104,8,32
Dc 1,8,81,0
v_accvgpr_write_b32 a182, s19
Mvaa 136,8,36
Ta 4,3,40960
Ta 6,3,41984
Mvaa 140,12,36
v_mov_b32 v254, v241
Mvaa 108,12,32
v_accvgpr_write_b32 a179, s18
Mvav 76,12,204
Dc 1,8,81,1024
Ta 8,4,40960
Mvav 44,12,200
W 9
Ta 10,4,41984
v_accvgpr_write_b32 a183, s19
Mvav 48,16,200
Mvav 80,16,204
v_mov_b32 v255, v241
Mvaa 112,16,32
Dc 1,8,81,2048
v_accvgpr_write_b32 a160, 0
Mvaa 144,16,36
Ta 12,5,40960
Ta 14,5,41984
Mvaa 148,20,36
v_accvgpr_write_b32 a161, 0
Mvaa 116,20,32
v_accvgpr_write_b32 a162, 0
Mvav 84,20,204
Dc 1,8,81,3072
Ta 16,6,40960
Mvav 52,20,200
W 9
s_add_u32 m0, s83, 12288
s_add_u32 s81, s82, 12288
Mvav 56,24,200
Ta 18,6,41984
v_accvgpr_write_b32 a163, 0
Mvav 88,24,204
v_accvgpr_write_b32 a164, 0
Mvaa 120,24,32
Dc 1,8,81,0
v_accvgpr_write_b32 a165, 0
Mvaa 152,24,36
Ta 20,7,40960
Ta 22,7,41984
Mvaa 156,28,36
v_accvgpr_write_b32 a166, 0
Mvaa 124,28,32
v_accvgpr_write_b32 a167, 0
Mvav 92,28,204
Dc 1,8,81,1024
Ta 24,8,40960
Mvav 60,28,200
W 9
Ta 26,8,41984
v_accvgpr_write_b32 a168, 0
Mvav 32,0,232
Mvav 64,0,236
v_accvgpr_write_b32 a169, 0
Mvaa 96,0,40
Dc 1,8,81,2048
v_accvgpr_write_b32 a170, 0
Mvaa 128,0,44
Ta 28,9,40960
Ta 30,9,41984
Mvaa 132,4,44
v_accvgpr_write_b32 a171, 0
Mvaa 100,4,40
v_accvgpr_write_b32 a172, 0
Mvav 68,4,236
Dc 1,8,81,3072
Ta 0,2,49152
Mvav 36,4,232
W 9
s_add_u32 m0, s92, 0
s_add_u32 s81, s91, 0
Mvav 40,8,232
Ta 2,2,50176
v_accvgpr_write_b32 a173, 0
Mvav 72,8,236
v_accvgpr_write_b32 a174, 0
Mvaa 104,8,40
Dc 1,8,81,0
v_accvgpr_write_b32 a175, 0
Mvaa 136,8,44
Ta 4,3,49152
Ta 6,3,50176
Mvaa 140,12,44
s_mov_b32 s90, 0x5d800000
Mvaa 108,12,40
Mvav 76,12,236
s_mov_b64 s[88:89], 0
Dc 1,8,81,1024
Ta 8,4,49152
Mvav 44,12,232
W 9
s_mov_b32 s64, 0
Ta 10,4,50176
Mvav 48,16,232
Mvav 80,16,236
s_and_b32 s18, s14, 1
Mvaa 112,16,40
s_lshl_b32 s62, s18, 14
Dc 1,8,81,2048
Ta 12,5,49152
Mvaa 144,16,44
Ta 14,5,50176
Mvaa 148,20,44
s_lshl_b32 s20, s57, 15
Mvaa 116,20,40
Mvav 84,20,236
s_sub_u32 s20, 507904, s20
Dc 1,8,81,3072
Ta 16,6,49152
Mvav 52,20,232
W 9
s_add_u32 m0, s92, 4096
s_add_u32 s81, s91, 4096
Mvav 56,24,232
s_add_u32 s20, s20, s62
Ta 18,6,50176
Mvav 88,24,236
s_lshl_b32 s65, s57, 1
Mvaa 120,24,40
s_sub_u32 s65, s65, 1
Dc 1,8,81,0
Ta 20,7,49152
Mvaa 152,24,44
Ta 22,7,50176
Mvaa 156,28,44
s_max_i32 s65, s65, 0
Mvaa 124,28,40
Mvav 92,28,236
s_sub_u32 s66, s60, 1
Dc 1,8,81,1024
Ta 24,8,49152
Mvav 60,28,232
s_waitcnt vmcnt(26) lgkmcnt(9)
s_cmp_lt_u32 s14, 2
s_cselect_b32 s20, s20, 16384
s_cselect_b32 s66, s66, 15
s_cselect_b32 s21, 32768, 0
Ta 26,8,50176
Mvav 32,0,208
Mvav 64,0,212
s_add_u32 s61, s91, s20
Mvaa 96,0,248
s_add_u32 s62, s62, s21
Dc 1,8,81,2048
Ta 28,9,49152
Mvaa 128,0,252
Ta 30,9,50176
Mvaa 132,4,252
s_sub_u32 s63, 16384, s21
Mvaa 100,4,248
Mvav 68,4,212
s_lshl_b32 s67, s60, 7
Dc 1,8,81,3072
Ta 0,2,57344
Mvav 36,4,208
W 9
s_add_u32 m0, s92, 8192
s_add_u32 s81, s91, 8192
Mvav 40,8,208
s_add_u32 s67, s67, s56
Ta 2,2,58368
Mvav 72,8,212
s_lshl_b32 s67, s67, 12
Mvaa 104,8,248
s_add_u32 s67, s67, 39845888
Dc 1,8,81,0
Ta 4,3,57344
Mvaa 136,8,252
Ta 6,3,58368
Mvaa 140,12,252
s_lshl_b32 s69, s60, 3
Mvaa 108,12,248
Mvav 76,12,212
s_add_u32 s69, s69, s59
Dc 1,8,81,1024
Ta 8,4,57344
Mvav 44,12,208
W 9
s_lshl_b32 s69, s69, 6
Ta 10,4,58368
Mvav 48,16,208
Mvav 80,16,212
s_lshl_b32 s21, s58, 3
Mvaa 112,16,248
s_add_u32 s69, s69, s21
Dc 1,8,81,2048
Ta 12,5,57344
Mvaa 144,16,252
Ta 14,5,58368
Mvaa 148,20,252
s_add_u32 s69, s69, 20480
Mvaa 116,20,248
Mvav 84,20,212
s_mov_b32 s70, 0
Dc 1,8,81,3072
Ta 16,6,57344
Mvav 52,20,208
W 9
s_add_u32 m0, s92, 12288
s_add_u32 s81, s91, 12288
Mvav 56,24,208
s_mov_b32 s37, 0
Ta 18,6,58368
Mvav 88,24,212
s_sub_u32 s18, s57, 4
Mvaa 120,24,248
s_lshl_b32 s18, s18, 2
Dc 1,8,81,0
Ta 20,7,57344
Mvaa 152,24,252
Ta 22,7,58368
Mvaa 156,28,252
s_add_u32 s18, s18, s58
Mvaa 124,28,248
Mvav 92,28,212
s_mul_i32 s85, s59, 7
Dc 1,8,81,1024
Ta 24,8,57344
Mvav 60,28,208
s_waitcnt vmcnt(30) lgkmcnt(9)
s_add_u32 s85, s85, s18
Ta 26,8,58368
Mvav 32,0,184
Mvav 64,0,188
)"
R"(s_cmp_lt_u32 s18, 7
s_cselect_b32 s85, s85, 99
Mvaa 96,0,48
s_cmp_lt_u32 s85, 54
s_cselect_b32 s65, 2, s65
Dc 1,8,81,2048
Ta 28,9,57344
Mvaa 128,0,52
Ta 30,9,58368
Mvaa 132,4,52
s_mov_b32 s68, 0
Mvaa 100,4,48
Mvav 68,4,188
s_mov_b64 s[24:25], -1
Dc 1,8,81,3072
Mvav 36,4,184
W 8
s_mov_b32 s22, 0x3e0293ee
Mvav 40,8,184
Mvav 72,8,188
s_mov_b32 s23, 0x3e0293ee
Mvaa 104,8,48
Mvaa 136,8,52
Mvaa 140,12,52
Mvaa 108,12,48
Mvav 76,12,188
Mvav 44,12,184
W 4
Mvav 48,16,184
Mvav 80,16,188
Mvaa 112,16,48
Mvaa 144,16,52
Mvaa 148,20,52
s_waitcnt vmcnt(0) lgkmcnt(0)
s_barrier
Ra 128,242,0
Mvaa 116,20,48
Mvav 84,20,188
Ra 132,242,1024
Mvav 52,20,184
Mvav 56,24,184
Ra 136,242,2048
Mvav 88,24,188
Mvaa 120,24,48
Ra 140,242,3072
Mvaa 152,24,52
Mvaa 156,28,52
Mvaa 124,28,48
Mvav 92,28,188
Mvav 60,28,184
.Lbgo%=:
v_pk_mul_f32 v[32:33], v[32:33], s[22:23]
v_pk_mul_f32 v[34:35], v[34:35], s[22:23]
v_pk_mul_f32 v[36:37], v[36:37], s[22:23]
v_pk_mul_f32 v[38:39], v[38:39], s[22:23]
K 32,32,33
K 33,34,35
K 34,36,37
K 35,38,39
v_pk_mul_f32 v[64:65], v[64:65], s[22:23]
v_pk_mul_f32 v[66:67], v[66:67], s[22:23]
v_pk_mul_f32 v[68:69], v[68:69], s[22:23]
v_pk_mul_f32 v[70:71], v[70:71], s[22:23]
K 64,64,65
K 65,66,67
K 66,68,69
K 67,70,71
v_pk_mul_f32 v[96:97], v[96:97], s[22:23]
v_pk_mul_f32 v[98:99], v[98:99], s[22:23]
v_pk_mul_f32 v[100:101], v[100:101], s[22:23]
v_pk_mul_f32 v[102:103], v[102:103], s[22:23]
K 96,96,97
K 97,98,99
K 98,100,101
K 99,102,103
v_pk_mul_f32 v[128:129], v[128:129], s[22:23]
v_pk_mul_f32 v[130:131], v[130:131], s[22:23]
v_pk_mul_f32 v[132:133], v[132:133], s[22:23]
v_pk_mul_f32 v[134:135], v[134:135], s[22:23]
K 128,128,129
K 129,130,131
K 130,132,133
K 131,134,135
v_pk_mul_f32 v[40:41], v[40:41], s[22:23]
v_pk_mul_f32 v[42:43], v[42:43], s[22:23]
v_pk_mul_f32 v[44:45], v[44:45], s[22:23]
v_pk_mul_f32 v[46:47], v[46:47], s[22:23]
v_pk_mul_f32 v[72:73], v[72:73], s[22:23]
v_pk_mul_f32 v[74:75], v[74:75], s[22:23]
v_pk_mul_f32 v[76:77], v[76:77], s[22:23]
v_pk_mul_f32 v[78:79], v[78:79], s[22:23]
v_pk_mul_f32 v[104:105], v[104:105], s[22:23]
v_pk_mul_f32 v[106:107], v[106:107], s[22:23]
v_pk_mul_f32 v[108:109], v[108:109], s[22:23]
v_pk_mul_f32 v[110:111], v[110:111], s[22:23]
v_pk_mul_f32 v[136:137], v[136:137], s[22:23]
v_pk_mul_f32 v[138:139], v[138:139], s[22:23]
v_pk_mul_f32 v[140:141], v[140:141], s[22:23]
v_pk_mul_f32 v[142:143], v[142:143], s[22:23]
v_pk_mul_f32 v[48:49], v[48:49], s[22:23]
v_pk_mul_f32 v[50:51], v[50:51], s[22:23]
v_pk_mul_f32 v[52:53], v[52:53], s[22:23]
v_pk_mul_f32 v[54:55], v[54:55], s[22:23]
v_pk_mul_f32 v[80:81], v[80:81], s[22:23]
v_pk_mul_f32 v[82:83], v[82:83], s[22:23]
v_pk_mul_f32 v[84:85], v[84:85], s[22:23]
v_pk_mul_f32 v[86:87], v[86:87], s[22:23]
v_pk_mul_f32 v[112:113], v[112:113], s[22:23]
v_pk_mul_f32 v[114:115], v[114:115], s[22:23]
v_pk_mul_f32 v[116:117], v[116:117], s[22:23]
v_pk_mul_f32 v[118:119], v[118:119], s[22:23]
v_pk_mul_f32 v[144:145], v[144:145], s[22:23]
v_pk_mul_f32 v[146:147], v[146:147], s[22:23]
v_pk_mul_f32 v[148:149], v[148:149], s[22:23]
v_pk_mul_f32 v[150:151], v[150:151], s[22:23]
v_pk_mul_f32 v[56:57], v[56:57], s[22:23]
v_pk_mul_f32 v[58:59], v[58:59], s[22:23]
v_pk_mul_f32 v[60:61], v[60:61], s[22:23]
v_pk_mul_f32 v[62:63], v[62:63], s[22:23]
v_pk_mul_f32 v[88:89], v[88:89], s[22:23]
v_pk_mul_f32 v[90:91], v[90:91], s[22:23]
v_pk_mul_f32 v[92:93], v[92:93], s[22:23]
v_pk_mul_f32 v[94:95], v[94:95], s[22:23]
v_pk_mul_f32 v[120:121], v[120:121], s[22:23]
v_pk_mul_f32 v[122:123], v[122:123], s[22:23]
v_pk_mul_f32 v[124:125], v[124:125], s[22:23]
v_pk_mul_f32 v[126:127], v[126:127], s[22:23]
v_pk_mul_f32 v[152:153], v[152:153], s[22:23]
v_pk_mul_f32 v[154:155], v[154:155], s[22:23]
v_pk_mul_f32 v[156:157], v[156:157], s[22:23]
v_pk_mul_f32 v[158:159], v[158:159], s[22:23]
W 0
Zvav 196,128,32
Ra 144,242,4096
Ra 148,242,5120
Ra 152,242,6144
Ra 156,242,7168
Zvav 204,128,64
K 40,40,41
K 41,42,43
K 42,44,45
K 43,46,47
Zvav 212,128,96
K 72,72,73
K 73,74,75
K 74,76,77
K 75,78,79
Zvav 220,128,128
K 104,104,105
K 105,106,107
K 106,108,109
K 107,110,111
K 136,136,137
K 137,138,139
K 138,140,141
K 139,142,143
Mvav 196,132,40
Ra 128,242,8192
K 48,48,49
Mvav 204,132,72
K 49,50,51
K 50,52,53
K 51,54,55
K 80,80,81
Mvav 212,132,104
K 81,82,83
K 82,84,85
K 83,86,87
K 112,112,113
Mvav 220,132,136
K 113,114,115
K 114,116,117
K 115,118,119
K 144,144,145
K 145,146,147
K 146,148,149
K 147,150,151
Mvav 196,136,48
Ra 132,242,9216
K 56,56,57
Mvav 204,136,80
K 57,58,59
K 58,60,61
K 59,62,63
K 88,88,89
Mvav 212,136,112
K 89,90,91
K 90,92,93
K 91,94,95
K 120,120,121
Mvav 220,136,144
K 121,122,123
K 122,124,125
K 123,126,127
K 152,152,153
K 153,154,155
K 154,156,157
K 155,158,159
Mvav 196,140,56
s_add_u32 s80, s62, 131072
Ra 136,242,10240
Mvav 204,140,88
Mvav 212,140,120
v_add_u32 v12, s80, v1
Mvav 220,140,152
W 3
ds_write_b128 v12, v[32:35] offset:0
Zvav 36,144,32
Ra 0,12,0
Ra 140,242,11264
Zvav 68,144,64
v_max3_f32 v4, v196, v197, v198
v_max3_f32 v4, v4, v199, v252
v_max3_f32 v5, v204, v205, v206
v_max3_f32 v5, v5, v207, v253
Zvav 100,144,96
v_max3_f32 v6, v212, v213, v214
v_max3_f32 v6, v6, v215, v254
v_max3_f32 v7, v220, v221, v222
v_max3_f32 v7, v7, v223, v255
Zvav 132,144,128
v_mov_b32 v8, v4
v_mov_b32 v9, v5
v_mov_b32 v10, v6
v_mov_b32 v11, v7
Mvav 36,148,40
ds_write_b128 v12, v[40:43] offset:1024
v_permlane32_swap_b32 v4, v8
v_permlane32_swap_b32 v5, v9
v_permlane32_swap_b32 v6, v10
Mvav 68,148,72
v_permlane32_swap_b32 v7, v11
v_max_f32 v4, v4, v8
v_max_f32 v5, v5, v9
v_max_f32 v6, v6, v10
Mvav 100,148,104
v_max_f32 v7, v7, v11
v_mov_b32 v8, v4
v_mov_b32 v9, v5
v_mov_b32 v10, v6
Mvav 132,148,136
Ra 4,12,1024
v_mov_b32 v11, v7
v_permlane16_swap_b32 v4, v8
v_permlane16_swap_b32 v5, v9
Mvav 36,152,48
v_permlane16_swap_b32 v6, v10
v_permlane16_swap_b32 v7, v11
v_max_f32 v4, v4, v8
v_max_f32 v5, v5, v9
Mvav 68,152,80
v_max_f32 v6, v6, v10
v_max_f32 v7, v7, v11
v_sub_f32 v4, 0, v4
v_sub_f32 v5, 0, v5
Mvav 100,152,112
v_sub_f32 v6, 0, v6
v_sub_f32 v7, 0, v7
v_mov_b32 v224, v4
v_mov_b32 v225, v4
Mvav 132,152,144
v_mov_b32 v226, v4
v_mov_b32 v227, v4
v_mov_b32 v228, v5
v_mov_b32 v229, v5
Mvav 36,156,56
v_mov_b32 v230, v5
v_mov_b32 v231, v5
v_mov_b32 v232, v6
v_mov_b32 v233, v6
Mvav 68,156,88
v_mov_b32 v234, v6
v_mov_b32 v235, v6
v_mov_b32 v236, v7
v_mov_b32 v237, v7
Mvav 100,156,120
ds_write_b128 v12, v[48:51] offset:2048
Ra 144,242,12288
v_mov_b32 v238, v7
v_mov_b32 v239, v7
Mvav 132,156,152
W 4
Ra 8,12,2048
ds_write_b128 v12, v[56:59] offset:3072
Ra 12,12,3072
Ra 148,242,13312
Cvav 160,128,32,224
s_and_b32 s80, s14, 1
ds_write_b128 v12, v[64:67] offset:4096
Ra 16,12,4096
ds_write_b128 v12, v[72:75] offset:5120
Ra 152,242,14336
Cvav 164,128,64,228
s_lshl_b32 s80, s80, 14
Ra 20,12,5120
ds_write_b128 v12, v[80:83] offset:6144
A 196,196,224
A 197,197,224
Cvav 168,128,96,232
s_add_u32 s80, s80, 98304
Ra 24,12,6144
Ra 156,242,15360
A 198,198,224
A 199,199,224
Cvav 172,128,128,236
X 196
X 197
Mvav 160,132,40
ds_write_b128 v12, v[88:91] offset:7168
Ra 28,12,7168
X 198
Mvav 164,132,72
ds_write_b128 v12, v[96:99] offset:8192
K 192,196,197
X 199
Mvav 168,132,104
A 204,204,228
v_add_u32 v8, s80, v1
K 193,198,199
A 205,205,228
Mvav 172,132,136
Ra 128,242,24576
Ra 32,12,8192
A 206,206,228
A 207,207,228
Mvav 160,136,48
ds_write_b128 v8, v[4:7]
ds_write_b128 v12, v[104:107] offset:9216
X 204
Mvav 164,136,80
X 205
X 206
Mvav 168,136,112
Ra 36,12,9216
K 200,204,205
X 207
Mvav 172,136,144
ds_write_b128 v12, v[112:115] offset:10240
Ra 132,242,25600
A 212,212,232
K 201,206,207
Mvav 160,140,56
Ra 40,12,10240
A 213,213,232
A 214,214,232
A 215,215,232
Mvav 164,140,88
ds_write_b128 v12, v[120:123] offset:11264
Ra 44,12,11264
X 212
Mvav 168,140,120
X 213
X 214
Mvav 172,140,152
W 13
Ra 136,242,26624
K 208,212,213
X 215
Cvav 176,144,32,224
ds_write_b128 v12, v[128:131] offset:12288
A 220,220,236
K 209,214,215
A 221,221,236
Cvav 180,144,64,228
Ra 48,12,12288
ds_write_b128 v12, v[136:139] offset:13312
A 222,222,236
A 223,223,236
Cvav 184,144,96,232
X 220
X 221
Cvav 188,144,128,236
Ra 140,242,27648
Ra 52,12,13312
X 222
Mvav 176,148,40
ds_write_b128 v12, v[144:147] offset:14336
K 216,220,221
X 223
Mvav 180,148,72
Ra 56,12,14336
A 36,36,224
K 217,222,223
A 37,37,224
Mvav 184,148,104
Ra 144,242,28672
ds_write_b128 v12, v[152:155] offset:15360
A 38,38,224
A 39,39,224
Mvav 188,148,136
X 36
X 37
Mvav 176,152,48
Ra 60,12,15360
X 38
K 194,36,37
Mvav 180,152,80
Ra 148,242,29696
X 39
A 68,68,228
)"
R"(Mvav 184,152,112
K 195,38,39
A 69,69,228
A 70,70,228
A 71,71,228
Mvav 188,152,144
X 68
X 69
Mvav 176,156,56
Ra 152,242,30720
X 70
K 202,68,69
Mvav 180,156,88
W 3
s_barrier
X 71
A 100,100,232
Mvav 184,156,120
K 203,70,71
A 101,101,232
A 102,102,232
A 103,103,232
X 100
X 101
X 102
K 210,100,101
X 103
A 132,132,236
K 211,102,103
A 133,133,236
A 134,134,236
A 135,135,236
X 132
X 133
X 134
K 218,132,133
X 135
s_nop 0
K 219,134,135
Mvav 188,156,152
Zvav 32,128,192
Ra 156,242,31744
Zvav 64,128,200
Zvav 96,128,208
s_xor_b32 s62, s62, 0x10000
Zvav 128,128,216
v_add_u32 v23, 61440, v21
Zvav 132,132,216
v_add_u32 v24, 61440, v22
Zvav 100,132,208
v_add_u32 v25, 122880, v21
Zvav 68,132,200
v_add_u32 v26, 122880, v22
Zvav 36,132,192
Zvav 40,136,192
X 172
Zvav 72,136,200
X 173
Zvav 104,136,208
X 174
K 220,172,173
Zvav 136,136,216
X 175
Zvav 140,140,216
X 168
K 221,174,175
Zvav 108,140,208
X 169
X 170
Zvav 76,140,200
s_waitcnt vmcnt(0) lgkmcnt(0)
s_barrier
v_xor_b32 v242, 0x10000, v242
K 212,168,169
Zvav 44,140,192
X 171
X 164
Zvav 48,144,192
Ra 128,242,0
K 213,170,171
X 165
Zvav 80,144,200
X 166
Zvav 112,144,208
Ra 132,242,1024
K 204,164,165
X 167
Zvav 144,144,216
X 160
Zvav 148,148,216
K 205,166,167
X 161
Zvav 116,148,208
Ra 136,242,2048
X 162
Zvav 84,148,200
K 196,160,161
X 163
Zvav 52,148,192
X 176
K 197,162,163
Zvav 56,152,192
Ra 140,242,3072
X 177
Zvav 88,152,200
X 178
K 198,176,177
Zvav 120,152,208
X 179
X 180
Zvav 152,152,216
K 199,178,179
Zvav 156,156,216
X 181
X 182
Zvav 124,156,208
K 206,180,181
X 183
Zvav 92,156,200
K 207,182,183
Zvav 60,156,192
s_cmp_eq_u64 s[24:25], 0
s_cbranch_scc1 .Ll1_33%=
W 0
Cvaa 160,128,0,224
Ra 144,242,4096
Ra 148,242,5120
Cvaa 164,128,16,228
s_add_u32 m0, s62, 0
Ra 152,242,6144
Cvaa 168,128,32,232
X 184
Cvaa 172,128,48,236
X 185
Mvaa 160,132,4
X 186
Mvaa 164,132,20
Ra 156,242,7168
K 214,184,185
Mvaa 168,132,36
Dc 1,8,61,0
Mvaa 172,132,52
X 187
Mvaa 160,136,8
X 188
Mvaa 164,136,24
Ra 128,242,16384
K 215,186,187
Mvaa 168,136,40
Dc 1,8,61,1024
Mvaa 172,136,56
X 189
Mvaa 160,140,12
X 190
Mvaa 164,140,28
Ra 132,242,17408
K 222,188,189
Mvaa 168,140,44
Dc 1,8,61,2048
Mvaa 172,140,60
X 191
v_smfmac_f32_16x16x64_bf16 a[160:163], a[176:179], v[192:199], v240
Ra 136,242,18432
K 223,190,191
v_smfmac_f32_16x16x64_bf16 a[164:167], a[176:179], v[200:207], v240
X 160
v_smfmac_f32_16x16x64_bf16 a[168:171], a[176:179], v[208:215], v240
Dc 1,8,61,3072
v_smfmac_f32_16x16x64_bf16 a[172:175], a[176:179], v[216:223], v240
W 3
X 161
Cvaa 176,144,0,224
s_add_u32 m0, s62, 4096
X 162
Cvaa 180,144,16,228
Ra 140,242,19456
K 192,160,161
Cvaa 184,144,32,232
X 163
Cvaa 188,144,48,236
Dc 243,8,61,0
Mvaa 176,148,4
X 164
Mvaa 180,148,20
Ra 144,242,20480
K 193,162,163
Mvaa 184,148,36
X 165
Mvaa 188,148,52
Dc 243,8,61,1024
Mvaa 176,152,8
X 166
Mvaa 180,152,24
Ra 148,242,21504
K 200,164,165
Mvaa 184,152,40
X 167
Mvaa 188,152,56
Dc 243,8,61,2048
Mvaa 176,156,12
X 168
Mvaa 180,156,28
Ra 152,242,22528
K 201,166,167
Mvaa 184,156,44
X 169
Mvaa 188,156,60
W 3
Dc 243,8,61,3072
Mvav 32,128,196
s_add_u32 m0, s62, 8192
X 170
Mvav 64,128,204
Ra 156,242,23552
K 208,168,169
Mvav 96,128,212
X 171
Mvav 128,128,220
Dc 244,8,61,0
Mvav 132,132,220
X 172
Mvav 100,132,212
Ra 128,242,8192
K 209,170,171
Mvav 68,132,204
X 173
Mvav 36,132,196
Dc 244,8,61,1024
Mvav 40,136,196
X 174
Mvav 72,136,204
Ra 132,242,9216
K 216,172,173
Mvav 104,136,212
X 175
Mvav 136,136,220
Dc 244,8,61,2048
Mvav 140,140,220
X 176
Mvav 108,140,212
Ra 136,242,10240
K 217,174,175
Mvav 76,140,204
X 177
Mvav 44,140,196
W 3
Dc 244,8,61,3072
Mvav 48,144,196
X 178
Mvav 80,144,204
s_add_u32 m0, s62, 12288
Ra 140,242,11264
Mvav 112,144,212
K 194,176,177
Mvav 144,144,220
X 179
Mvav 148,148,220
X 180
Mvav 116,148,212
Dc 245,8,61,0
Ra 144,242,12288
Mvav 84,148,204
K 195,178,179
Mvav 52,148,196
X 181
Mvav 56,152,196
X 182
Mvav 88,152,204
Dc 245,8,61,1024
Ra 148,242,13312
Mvav 120,152,212
s_cmp_eq_u32 s14, 2
s_cselect_b64 s[18:19], s[30:31], s[32:33]
K 202,180,181
Mvav 152,152,220
s_lshr_b32 s81, s15, 3
X 183
Mvav 156,156,220
s_mul_i32 s82, s81, 69120
X 184
Mvav 124,156,212
s_mul_i32 s83, s59, 448
Dc 245,8,61,2048
Ra 152,242,14336
Mvav 92,156,204
s_andn2_b32 s83, s83, 127
K 203,182,183
Mvav 60,156,196
W 3
X 185
Cvaa 160,128,0,224
s_add_u32 s82, s82, s83
X 186
Cvaa 164,128,16,228
Dc 245,8,61,3072
Ra 156,242,15360
Cvaa 168,128,32,232
s_add_u32 s61, s61, s63
v_min_u32 v246, 624, v1
v_lshrrev_b32 v247, 6, v246
Cvaa 172,128,48,236
K 210,184,185
v_mul_u32_u24 v247, 3456, v247
Mvaa 160,132,4
X 187
Mvaa 164,132,20
Ra 128,242,24576
v_and_b32 v246, 48, v246
Mvaa 168,132,36
s_xor_b32 s62, s62, 0x10000
X 188
Mvaa 172,132,52
v_lshl_add_u32 v246, v246, 3, v247
v_add_u32 v246, s82, v246
Mvaa 160,136,8
global_load_dword v3, v246, s[18:19]
K 211,186,187
Mvaa 164,136,24
s_add_u32 s64, s64, 1
X 189
Mvaa 168,136,40
Ra 132,242,25600
v_add_u32 v246, 34560, v246
Mvaa 172,136,56
X 190
Mvaa 160,140,12
v_min_u32 v246, 2211836, v246
K 218,188,189
Mvaa 164,140,28
global_load_dword v3, v246, s[18:19]
Ra 136,242,26624
Mvaa 168,140,44
s_add_u32 s36, s29, 131072
X 191
Mvaa 172,140,60
W 3
X 160
Cvaa 176,144,0,224
Ra 140,242,27648
K 219,190,191
Cvaa 180,144,16,228
X 161
Cvaa 184,144,32,232
X 162
Cvaa 188,144,48,236
K 196,160,161
Mvaa 176,148,4
X 163
Mvaa 180,148,20
buffer_store_dwordx4 a[124:127], v1, s[8:11], s36 offen offset:0 sc1
Ra 144,242,28672
Mvaa 184,148,36
X 164
Mvaa 188,148,52
K 197,162,163
Mvaa 176,152,8
X 165
Mvaa 180,152,24
Ra 148,242,29696
Mvaa 184,152,40
X 166
Mvaa 188,152,56
buffer_store_dwordx4 a[116:119], v1, s[8:11], s36 offen offset:1024 sc1
K 204,164,165
Mvaa 176,156,12
X 167
Mvaa 180,156,28
Ra 152,242,30720
Mvaa 184,156,44
X 168
Mvaa 188,156,60
W 3
K 205,166,167
Mvav 32,128,192
X 169
Mvav 64,128,200
buffer_store_dwordx4 a[108:111], v1, s[8:11], s36 offen offset:2048 sc1
Ra 156,242,31744
Mvav 96,128,208
X 170
Mvav 128,128,216
K 212,168,169
Mvav 132,132,216
X 171
Mvav 100,132,208
X 172
Mvav 68,132,200
buffer_store_dwordx4 a[188:191], v1, s[8:11], s36 offen offset:3072 sc1
Mvav 36,132,192
K 213,170,171
Mvav 40,136,192
X 173
Mvav 72,136,200
X 174
Mvav 104,136,208
Mvav 136,136,216
s_add_u32 s36, s29, 135168
K 220,172,173
Mvav 140,140,216
X 175
)"
R"(Mvav 108,140,208
X 176
Mvav 76,140,200
s_waitcnt vmcnt(6) lgkmcnt(0)
s_barrier
v_xor_b32 v242, 0x10000, v242
Mvav 44,140,192
Ra 128,242,0
K 221,174,175
Mvav 48,144,192
X 177
Mvav 80,144,200
buffer_store_dwordx4 a[196:199], v1, s[8:11], s36 offen offset:0 sc1
Mvav 112,144,208
X 178
Mvav 144,144,216
Ra 132,242,1024
K 198,176,177
Mvav 148,148,216
X 179
Mvav 116,148,208
X 180
Mvav 84,148,200
Ra 136,242,2048
Mvav 52,148,192
buffer_store_dwordx4 a[220:223], v1, s[8:11], s36 offen offset:1024 sc1
K 199,178,179
Mvav 56,152,192
X 181
Mvav 88,152,200
Ra 140,242,3072
Mvav 120,152,208
X 182
Mvav 152,152,216
K 206,180,181
Mvav 156,156,216
X 183
Mvav 124,156,208
Mvav 92,156,200
K 207,182,183
Mvav 60,156,192
W 0
Cvaa 160,128,0,224
Ra 144,242,4096
Ra 148,242,5120
Cvaa 164,128,16,228
s_add_u32 m0, s62, 0
Ra 152,242,6144
Cvaa 168,128,32,232
X 184
Cvaa 172,128,48,236
X 185
Mvaa 160,132,4
X 186
Mvaa 164,132,20
Ra 156,242,7168
K 214,184,185
Mvaa 168,132,36
Dc 1,8,61,0
Mvaa 172,132,52
X 187
Mvaa 160,136,8
X 188
Mvaa 164,136,24
Ra 128,242,16384
K 215,186,187
Mvaa 168,136,40
Dc 1,8,61,1024
Mvaa 172,136,56
X 189
Mvaa 160,140,12
X 190
Mvaa 164,140,28
Ra 132,242,17408
K 222,188,189
Mvaa 168,140,44
Dc 1,8,61,2048
Mvaa 172,140,60
X 191
v_smfmac_f32_16x16x64_bf16 a[160:163], a[176:179], v[192:199], v240
Ra 136,242,18432
K 223,190,191
v_smfmac_f32_16x16x64_bf16 a[164:167], a[176:179], v[200:207], v240
X 160
v_smfmac_f32_16x16x64_bf16 a[168:171], a[176:179], v[208:215], v240
Dc 1,8,61,3072
v_smfmac_f32_16x16x64_bf16 a[172:175], a[176:179], v[216:223], v240
W 3
X 161
Cvaa 176,144,0,224
s_add_u32 m0, s62, 4096
X 162
Cvaa 180,144,16,228
Ra 140,242,19456
K 192,160,161
Cvaa 184,144,32,232
X 163
Cvaa 188,144,48,236
Dc 243,8,61,0
Mvaa 176,148,4
X 164
Mvaa 180,148,20
Ra 144,242,20480
K 193,162,163
Mvaa 184,148,36
X 165
Mvaa 188,148,52
Dc 243,8,61,1024
Mvaa 176,152,8
X 166
Mvaa 180,152,24
Ra 148,242,21504
K 200,164,165
Mvaa 184,152,40
X 167
Mvaa 188,152,56
Dc 243,8,61,2048
Mvaa 176,156,12
X 168
Mvaa 180,156,28
Ra 152,242,22528
K 201,166,167
Mvaa 184,156,44
X 169
Mvaa 188,156,60
W 3
Dc 243,8,61,3072
Mvav 32,128,196
s_add_u32 m0, s62, 8192
X 170
Mvav 64,128,204
Ra 156,242,23552
K 208,168,169
Mvav 96,128,212
X 171
Mvav 128,128,220
Dc 244,8,61,0
Mvav 132,132,220
X 172
Mvav 100,132,212
Ra 128,242,8192
K 209,170,171
Mvav 68,132,204
X 173
Mvav 36,132,196
Dc 244,8,61,1024
Mvav 40,136,196
X 174
Mvav 72,136,204
Ra 132,242,9216
K 216,172,173
Mvav 104,136,212
X 175
Mvav 136,136,220
Dc 244,8,61,2048
Mvav 140,140,220
X 176
Mvav 108,140,212
Ra 136,242,10240
K 217,174,175
Mvav 76,140,204
X 177
Mvav 44,140,196
W 3
Dc 244,8,61,3072
Mvav 48,144,196
X 178
Mvav 80,144,204
s_add_u32 m0, s62, 12288
Ra 140,242,11264
Mvav 112,144,212
K 194,176,177
Mvav 144,144,220
X 179
Mvav 148,148,220
X 180
Mvav 116,148,212
Dc 245,8,61,0
Ra 144,242,12288
Mvav 84,148,204
K 195,178,179
Mvav 52,148,196
X 181
Mvav 56,152,196
X 182
Mvav 88,152,204
Dc 245,8,61,1024
Ra 148,242,13312
Mvav 120,152,212
K 202,180,181
Mvav 152,152,220
X 183
Mvav 156,156,220
X 184
Mvav 124,156,212
Dc 245,8,61,2048
Ra 152,242,14336
Mvav 92,156,204
K 203,182,183
v_bfe_u32 v28, v1, 4, 4
Mvav 60,156,196
W 3
X 185
Cvaa 160,128,0,224
Ra 156,242,15360
v_lshlrev_b32 v28, 3, v28
Cvaa 164,128,16,228
s_lshl_b32 s80, s56, 9
s_add_u32 s80, s80, 4224
Dc 245,8,61,3072
Cvaa 168,128,32,232
s_add_u32 s61, s61, s63
X 186
Cvaa 172,128,48,236
K 210,184,185
Mvaa 160,132,4
X 187
Mvaa 164,132,20
buffer_load_dwordx2 v[28:29], v28, s[8:11], s80 offen sc1
Ra 128,242,24576
Mvaa 168,132,36
s_xor_b32 s62, s62, 0x10000
X 188
Mvaa 172,132,52
K 211,186,187
Mvaa 160,136,8
X 189
Mvaa 164,136,24
s_add_u32 s64, s64, 1
X 190
Mvaa 168,136,40
Ra 132,242,25600
K 218,188,189
Mvaa 172,136,56
X 191
Mvaa 160,140,12
Ra 136,242,26624
Mvaa 164,140,28
Mvaa 168,140,44
buffer_store_dwordx4 a[228:231], v1, s[8:11], s36 offen offset:2048 sc1
Mvaa 172,140,60
W 3
Cvaa 176,144,0,224
Ra 140,242,27648
Cvaa 180,144,16,228
X 160
Cvaa 184,144,32,232
K 219,190,191
Cvaa 188,144,48,236
X 161
Mvaa 176,148,4
X 162
Mvaa 180,148,20
Ra 144,242,28672
K 196,160,161
Mvaa 184,148,36
X 163
Mvaa 188,148,52
X 164
Mvaa 176,152,8
buffer_store_dwordx4 a[76:79], v1, s[8:11], s36 offen offset:3072 sc1
K 197,162,163
Mvaa 180,152,24
X 165
Mvaa 184,152,40
Ra 148,242,29696
Mvaa 188,152,56
X 166
Mvaa 176,156,12
s_add_u32 s36, s29, 327680
Ra 152,242,30720
K 204,164,165
Mvaa 180,156,28
X 167
Mvaa 184,156,44
X 168
Mvaa 188,156,60
W 3
K 205,166,167
Mvav 32,128,192
X 169
Mvav 64,128,200
Ra 156,242,31744
Mvav 96,128,208
X 170
Mvav 128,128,216
buffer_store_dwordx4 a[84:87], v1, s[8:11], s36 offen offset:0 sc1
K 212,168,169
Mvav 132,132,216
X 171
Mvav 100,132,208
X 172
Mvav 68,132,200
Mvav 36,132,192
K 213,170,171
Mvav 40,136,192
X 173
Mvav 72,136,200
buffer_store_dwordx4 a[92:95], v1, s[8:11], s36 offen offset:1024 sc1
Mvav 104,136,208
X 174
Mvav 136,136,216
s_waitcnt vmcnt(4)
v_cmp_eq_u64 vcc, s[12:13], v[28:29]
s_cmp_eq_u64 vcc, -1
s_cbranch_scc0 .Lspin_62%=
.Lok_63%=:
K 220,172,173
Mvav 140,140,216
X 175
Mvav 108,140,208
X 176
Mvav 76,140,200
W 0
s_barrier
v_xor_b32 v242, 0x10000, v242
Mvav 44,140,192
buffer_store_dwordx4 a[204:207], v1, s[8:11], s36 offen offset:2048 sc1
K 221,174,175
Mvav 48,144,192
Ra 128,242,0
Mvav 80,144,200
X 177
Mvav 112,144,208
X 178
Mvav 144,144,216
Ra 132,242,1024
K 198,176,177
Mvav 148,148,216
X 179
Mvav 116,148,208
X 180
Mvav 84,148,200
Ra 136,242,2048
buffer_store_dwordx4 a[212:215], v1, s[8:11], s36 offen offset:3072 sc1
Mvav 52,148,192
K 199,178,179
Mvav 56,152,192
X 181
Mvav 88,152,200
Ra 140,242,3072
Mvav 120,152,208
X 182
Mvav 152,152,216
K 206,180,181
Mvav 156,156,216
X 183
Mvav 124,156,208
Mvav 92,156,200
K 207,182,183
Mvav 60,156,192
W 0
Cvaa 160,128,0,224
Ra 144,242,4096
Ra 148,242,5120
Cvaa 164,128,16,228
s_add_u32 m0, s62, 0
Ra 152,242,6144
Cvaa 168,128,32,232
X 184
Cvaa 172,128,48,236
X 185
Mvaa 160,132,4
X 186
Mvaa 164,132,20
Ra 156,242,7168
K 214,184,185
Mvaa 168,132,36
Dc 1,8,61,0
Mvaa 172,132,52
X 187
Mvaa 160,136,8
X 188
Mvaa 164,136,24
Ra 128,242,16384
K 215,186,187
Mvaa 168,136,40
Dc 1,8,61,1024
Mvaa 172,136,56
X 189
Mvaa 160,140,12
X 190
Mvaa 164,140,28
Ra 132,242,17408
K 222,188,189
Mvaa 168,140,44
Dc 1,8,61,2048
Mvaa 172,140,60
X 191
v_smfmac_f32_16x16x64_bf16 a[160:163], a[176:179], v[192:199], v240
Ra 136,242,18432
K 223,190,191
v_smfmac_f32_16x16x64_bf16 a[164:167], a[176:179], v[200:207], v240
X 160
v_smfmac_f32_16x16x64_bf16 a[168:171], a[176:179], v[208:215], v240
Dc 1,8,61,3072
v_smfmac_f32_16x16x64_bf16 a[172:175], a[176:179], v[216:223], v240
W 3
X 161
Cvaa 176,144,0,224
)"
R"(s_add_u32 m0, s62, 4096
X 162
Cvaa 180,144,16,228
Ra 140,242,19456
K 192,160,161
Cvaa 184,144,32,232
X 163
Cvaa 188,144,48,236
Dc 243,8,61,0
Mvaa 176,148,4
X 164
Mvaa 180,148,20
Ra 144,242,20480
K 193,162,163
Mvaa 184,148,36
X 165
Mvaa 188,148,52
Dc 243,8,61,1024
Mvaa 176,152,8
X 166
Mvaa 180,152,24
Ra 148,242,21504
K 200,164,165
Mvaa 184,152,40
X 167
Mvaa 188,152,56
Dc 243,8,61,2048
Mvaa 176,156,12
X 168
Mvaa 180,156,28
Ra 152,242,22528
K 201,166,167
Mvaa 184,156,44
X 169
Mvaa 188,156,60
W 3
Dc 243,8,61,3072
Mvav 32,128,196
s_add_u32 m0, s62, 8192
X 170
Mvav 64,128,204
Ra 156,242,23552
K 208,168,169
Mvav 96,128,212
X 171
Mvav 128,128,220
Dc 244,8,61,0
Mvav 132,132,220
X 172
Mvav 100,132,212
Ra 128,242,8192
K 209,170,171
Mvav 68,132,204
X 173
Mvav 36,132,196
Dc 244,8,61,1024
Mvav 40,136,196
X 174
Mvav 72,136,204
Ra 132,242,9216
K 216,172,173
Mvav 104,136,212
X 175
Mvav 136,136,220
Dc 244,8,61,2048
Mvav 140,140,220
X 176
Mvav 108,140,212
Ra 136,242,10240
K 217,174,175
Mvav 76,140,204
X 177
Mvav 44,140,196
W 3
Dc 244,8,61,3072
Mvav 48,144,196
X 178
Mvav 80,144,204
s_add_u32 m0, s62, 12288
Ra 140,242,11264
Mvav 112,144,212
K 194,176,177
Mvav 144,144,220
X 179
Mvav 148,148,220
X 180
Mvav 116,148,212
Dc 245,8,61,0
Ra 144,242,12288
Mvav 84,148,204
K 195,178,179
Mvav 52,148,196
X 181
Mvav 56,152,196
X 182
Mvav 88,152,204
Dc 245,8,61,1024
Ra 148,242,13312
Mvav 120,152,212
K 202,180,181
Mvav 152,152,220
X 183
Mvav 156,156,220
X 184
Mvav 124,156,212
Dc 245,8,61,2048
Ra 152,242,14336
Mvav 92,156,204
s_mov_b64 s[18:19], s[34:35]
K 203,182,183
Mvav 60,156,196
W 3
X 185
Cvaa 160,128,0,224
s_lshr_b32 s81, s15, 3
X 186
Cvaa 164,128,16,228
s_lshl_b32 s82, s59, 5
Dc 245,8,61,3072
Ra 156,242,15360
Cvaa 168,128,32,232
s_add_u32 s81, s81, s82
s_add_u32 s61, s61, s63
K 210,184,185
Cvaa 172,128,48,236
s_lshl_b32 s81, s81, 1
X 187
Mvaa 160,132,4
s_add_u32 s81, s81, s14
X 188
Mvaa 164,132,20
s_sub_u32 s81, s81, 2
s_mul_i32 s81, s81, 4480
Ra 128,242,24576
Mvaa 168,132,36
s_xor_b32 s62, s62, 0x10000
K 211,186,187
v_min_u32 v246, 544, v1
Mvaa 172,132,52
X 189
Mvaa 160,136,8
Ra 132,242,25600
v_lshl_add_u32 v246, v246, 3, s81
Mvaa 164,136,24
s_add_u32 s64, s64, 1
X 190
Mvaa 168,136,40
v_min_u32 v246, 2211836, v246
K 218,188,189
Mvaa 172,136,56
global_load_dword v3, v246, s[18:19]
Mvaa 160,140,12
X 191
Mvaa 164,140,28
Ra 136,242,26624
Mvaa 168,140,44
s_add_u32 s36, s29, 331776
Mvaa 172,140,60
W 3
Cvaa 176,144,0,224
X 160
Cvaa 180,144,16,228
Ra 140,242,27648
K 219,190,191
Cvaa 184,144,32,232
X 161
Cvaa 188,144,48,236
X 162
Mvaa 176,148,4
buffer_store_dwordx4 a[236:239], v1, s[8:11], s36 offen offset:0 sc1
K 196,160,161
Mvaa 180,148,20
X 163
Mvaa 184,148,36
Ra 144,242,28672
Mvaa 188,148,52
X 164
Mvaa 176,152,8
Ra 148,242,29696
K 197,162,163
Mvaa 180,152,24
X 165
Mvaa 184,152,40
X 166
Mvaa 188,152,56
buffer_store_dwordx4 a[244:247], v1, s[8:11], s36 offen offset:1024 sc1
K 204,164,165
Mvaa 176,156,12
X 167
Mvaa 180,156,28
Ra 152,242,30720
Mvaa 184,156,44
X 168
Mvaa 188,156,60
W 3
K 205,166,167
Mvav 32,128,192
X 169
Mvav 64,128,200
buffer_store_dwordx4 a[68:71], v1, s[8:11], s36 offen offset:2048 sc1
Ra 156,242,31744
Mvav 96,128,208
X 170
Mvav 128,128,216
K 212,168,169
Mvav 132,132,216
X 171
Mvav 100,132,208
X 172
Mvav 68,132,200
buffer_store_dwordx4 a[184:187], v1, s[8:11], s36 offen offset:3072 sc1
Mvav 36,132,192
K 213,170,171
Mvav 40,136,192
X 173
Mvav 72,136,200
X 174
Mvav 104,136,208
Mvav 136,136,216
s_add_u32 s36, s29, 196608
K 220,172,173
Mvav 140,140,216
X 175
Mvav 108,140,208
X 176
Mvav 76,140,200
s_waitcnt vmcnt(5) lgkmcnt(0)
s_barrier
v_xor_b32 v242, 0x10000, v242
Mvav 44,140,192
Ra 128,242,0
K 221,174,175
Mvav 48,144,192
X 177
Mvav 80,144,200
buffer_store_dwordx4 a[192:195], v1, s[8:11], s36 offen offset:0 sc1
Mvav 112,144,208
X 178
Mvav 144,144,216
Ra 132,242,1024
K 198,176,177
Mvav 148,148,216
X 179
Mvav 116,148,208
X 180
Mvav 84,148,200
Ra 136,242,2048
Mvav 52,148,192
buffer_store_dwordx4 a[200:203], v1, s[8:11], s36 offen offset:1024 sc1
K 199,178,179
Mvav 56,152,192
X 181
Mvav 88,152,200
Ra 140,242,3072
Mvav 120,152,208
X 182
Mvav 152,152,216
K 206,180,181
Mvav 156,156,216
X 183
Mvav 124,156,208
Mvav 92,156,200
K 207,182,183
Mvav 60,156,192
W 0
Cvaa 160,128,0,224
Ra 144,242,4096
Ra 148,242,5120
Cvaa 164,128,16,228
s_add_u32 m0, s62, 0
Ra 152,242,6144
Cvaa 168,128,32,232
X 184
Cvaa 172,128,48,236
X 185
Mvaa 160,132,4
X 186
Mvaa 164,132,20
Ra 156,242,7168
K 214,184,185
Mvaa 168,132,36
Dc 1,8,61,0
Mvaa 172,132,52
X 187
Mvaa 160,136,8
X 188
Mvaa 164,136,24
Ra 128,242,16384
K 215,186,187
Mvaa 168,136,40
Dc 1,8,61,1024
Mvaa 172,136,56
X 189
Mvaa 160,140,12
X 190
Mvaa 164,140,28
Ra 132,242,17408
K 222,188,189
Mvaa 168,140,44
Dc 1,8,61,2048
Mvaa 172,140,60
X 191
v_smfmac_f32_16x16x64_bf16 a[160:163], a[176:179], v[192:199], v240
Ra 136,242,18432
K 223,190,191
v_smfmac_f32_16x16x64_bf16 a[164:167], a[176:179], v[200:207], v240
X 160
v_smfmac_f32_16x16x64_bf16 a[168:171], a[176:179], v[208:215], v240
Dc 1,8,61,3072
v_smfmac_f32_16x16x64_bf16 a[172:175], a[176:179], v[216:223], v240
W 3
X 161
Cvaa 176,144,0,224
s_add_u32 m0, s62, 4096
X 162
Cvaa 180,144,16,228
Ra 140,242,19456
K 192,160,161
Cvaa 184,144,32,232
X 163
Cvaa 188,144,48,236
Dc 243,8,61,0
Mvaa 176,148,4
X 164
Mvaa 180,148,20
Ra 144,242,20480
K 193,162,163
Mvaa 184,148,36
X 165
Mvaa 188,148,52
Dc 243,8,61,1024
Mvaa 176,152,8
X 166
Mvaa 180,152,24
Ra 148,242,21504
K 200,164,165
Mvaa 184,152,40
X 167
Mvaa 188,152,56
Dc 243,8,61,2048
Mvaa 176,156,12
X 168
Mvaa 180,156,28
Ra 152,242,22528
K 201,166,167
Mvaa 184,156,44
X 169
Mvaa 188,156,60
W 3
Dc 243,8,61,3072
Mvav 32,128,196
s_add_u32 m0, s62, 8192
X 170
Mvav 64,128,204
Ra 156,242,23552
K 208,168,169
Mvav 96,128,212
X 171
Mvav 128,128,220
Dc 244,8,61,0
Mvav 132,132,220
X 172
Mvav 100,132,212
Ra 128,242,8192
K 209,170,171
Mvav 68,132,204
X 173
Mvav 36,132,196
Dc 244,8,61,1024
Mvav 40,136,196
X 174
Mvav 72,136,204
Ra 132,242,9216
K 216,172,173
Mvav 104,136,212
X 175
Mvav 136,136,220
Dc 244,8,61,2048
Mvav 140,140,220
X 176
Mvav 108,140,212
Ra 136,242,10240
K 217,174,175
Mvav 76,140,204
X 177
Mvav 44,140,196
W 3
Dc 244,8,61,3072
Mvav 48,144,196
X 178
Mvav 80,144,204
s_add_u32 m0, s62, 12288
Ra 140,242,11264
Mvav 112,144,212
K 194,176,177
Mvav 144,144,220
X 179
Mvav 148,148,220
X 180
Mvav 116,148,212
)"
R"(Dc 245,8,61,0
Ra 144,242,12288
Mvav 84,148,204
K 195,178,179
Mvav 52,148,196
X 181
Mvav 56,152,196
X 182
Mvav 88,152,204
Dc 245,8,61,1024
Ra 148,242,13312
Mvav 120,152,212
K 202,180,181
Mvav 152,152,220
X 183
Mvav 156,156,220
s_waitcnt vmcnt(16)
s_add_u32 s80, s71, 256
v_mov_b64 v[28:29], s[12:13]
s_mov_b64 exec, 1
buffer_store_dwordx2 v[28:29], off, s[8:11], s80 sc1
s_mov_b64 exec, -1
X 184
Mvav 124,156,212
Ra 152,242,14336
Mvav 92,156,204
K 203,182,183
Mvav 60,156,196
W 3
X 185
Cvaa 160,128,0,224
Dc 245,8,61,2048
Ra 156,242,15360
Cvaa 164,128,16,228
X 186
Cvaa 168,128,32,232
K 210,184,185
Cvaa 172,128,48,236
X 187
Mvaa 160,132,4
Dc 245,8,61,3072
Ra 128,242,24576
Mvaa 164,132,20
s_add_u32 s61, s61, s63
X 188
Mvaa 168,132,36
s_xor_b32 s62, s62, 0x10000
K 211,186,187
Mvaa 172,132,52
X 189
Mvaa 160,136,8
X 190
Mvaa 164,136,24
s_add_u32 s64, s64, 1
Ra 132,242,25600
Mvaa 168,136,40
K 218,188,189
Mvaa 172,136,56
X 191
Mvaa 160,140,12
Ra 136,242,26624
Mvaa 164,140,28
Mvaa 168,140,44
buffer_store_dwordx4 a[208:211], v1, s[8:11], s36 offen offset:2048 sc1
Mvaa 172,140,60
W 3
Cvaa 176,144,0,224
Ra 140,242,27648
Cvaa 180,144,16,228
X 160
Cvaa 184,144,32,232
K 219,190,191
Cvaa 188,144,48,236
X 161
Mvaa 176,148,4
X 162
Mvaa 180,148,20
Ra 144,242,28672
K 196,160,161
Mvaa 184,148,36
X 163
Mvaa 188,148,52
X 164
Mvaa 176,152,8
buffer_store_dwordx4 a[216:219], v1, s[8:11], s36 offen offset:3072 sc1
K 197,162,163
Mvaa 180,152,24
X 165
Mvaa 184,152,40
Ra 148,242,29696
Mvaa 188,152,56
X 166
Mvaa 176,156,12
s_add_u32 s36, s29, 200704
Ra 152,242,30720
K 204,164,165
Mvaa 180,156,28
X 167
Mvaa 184,156,44
X 168
Mvaa 188,156,60
W 3
K 205,166,167
Mvav 32,128,192
X 169
Mvav 64,128,200
Ra 156,242,31744
Mvav 96,128,208
X 170
Mvav 128,128,216
buffer_store_dwordx4 a[224:227], v1, s[8:11], s36 offen offset:0 sc1
K 212,168,169
Mvav 132,132,216
X 171
Mvav 100,132,208
X 172
Mvav 68,132,200
Mvav 36,132,192
K 213,170,171
Mvav 40,136,192
X 173
Mvav 72,136,200
buffer_store_dwordx4 a[232:235], v1, s[8:11], s36 offen offset:1024 sc1
Mvav 104,136,208
X 174
Mvav 136,136,216
K 220,172,173
Mvav 140,140,216
X 175
Mvav 108,140,208
X 176
Mvav 76,140,200
s_waitcnt vmcnt(4) lgkmcnt(0)
s_barrier
v_xor_b32 v242, 0x10000, v242
Mvav 44,140,192
buffer_store_dwordx4 a[240:243], v1, s[8:11], s36 offen offset:2048 sc1
K 221,174,175
Mvav 48,144,192
Ra 128,242,0
Mvav 80,144,200
X 177
Mvav 112,144,208
X 178
Mvav 144,144,216
Ra 132,242,1024
K 198,176,177
Mvav 148,148,216
X 179
Mvav 116,148,208
X 180
Mvav 84,148,200
Ra 136,242,2048
buffer_store_dwordx4 a[100:103], v1, s[8:11], s36 offen offset:3072 sc1
Mvav 52,148,192
K 199,178,179
Mvav 56,152,192
X 181
Mvav 88,152,200
Ra 140,242,3072
Mvav 120,152,208
X 182
Mvav 152,152,216
K 206,180,181
Mvav 156,156,216
X 183
Mvav 124,156,208
Mvav 92,156,200
K 207,182,183
Mvav 60,156,192
W 0
Cvaa 160,128,0,224
Ra 144,242,4096
Ra 148,242,5120
Cvaa 164,128,16,228
s_add_u32 m0, s62, 0
Ra 152,242,6144
Cvaa 168,128,32,232
X 184
Cvaa 172,128,48,236
X 185
Mvaa 160,132,4
X 186
Mvaa 164,132,20
Ra 156,242,7168
K 214,184,185
Mvaa 168,132,36
Dc 1,8,61,0
Mvaa 172,132,52
X 187
Mvaa 160,136,8
X 188
Mvaa 164,136,24
Ra 128,242,16384
K 215,186,187
Mvaa 168,136,40
Dc 1,8,61,1024
Mvaa 172,136,56
X 189
Mvaa 160,140,12
X 190
Mvaa 164,140,28
Ra 132,242,17408
K 222,188,189
Mvaa 168,140,44
Dc 1,8,61,2048
Mvaa 172,140,60
X 191
v_smfmac_f32_16x16x64_bf16 a[160:163], a[176:179], v[192:199], v240
Ra 136,242,18432
K 223,190,191
v_smfmac_f32_16x16x64_bf16 a[164:167], a[176:179], v[200:207], v240
X 160
v_smfmac_f32_16x16x64_bf16 a[168:171], a[176:179], v[208:215], v240
Dc 1,8,61,3072
v_smfmac_f32_16x16x64_bf16 a[172:175], a[176:179], v[216:223], v240
W 3
X 161
Cvaa 176,144,0,224
s_add_u32 m0, s62, 4096
X 162
Cvaa 180,144,16,228
Ra 140,242,19456
K 192,160,161
Cvaa 184,144,32,232
X 163
Cvaa 188,144,48,236
Dc 243,8,61,0
Mvaa 176,148,4
X 164
Mvaa 180,148,20
Ra 144,242,20480
K 193,162,163
Mvaa 184,148,36
X 165
Mvaa 188,148,52
Dc 243,8,61,1024
Mvaa 176,152,8
X 166
Mvaa 180,152,24
Ra 148,242,21504
K 200,164,165
Mvaa 184,152,40
X 167
Mvaa 188,152,56
Dc 243,8,61,2048
Mvaa 176,156,12
X 168
Mvaa 180,156,28
Ra 152,242,22528
K 201,166,167
Mvaa 184,156,44
X 169
Mvaa 188,156,60
W 3
Dc 243,8,61,3072
Mvav 32,128,196
s_add_u32 m0, s62, 8192
X 170
Mvav 64,128,204
Ra 156,242,23552
K 208,168,169
Mvav 96,128,212
X 171
Mvav 128,128,220
Dc 244,8,61,0
Mvav 132,132,220
X 172
Mvav 100,132,212
Ra 128,242,8192
K 209,170,171
Mvav 68,132,204
X 173
Mvav 36,132,196
Dc 244,8,61,1024
Mvav 40,136,196
X 174
Mvav 72,136,204
Ra 132,242,9216
K 216,172,173
Mvav 104,136,212
X 175
Mvav 136,136,220
Dc 244,8,61,2048
Mvav 140,140,220
X 176
Mvav 108,140,212
Ra 136,242,10240
K 217,174,175
Mvav 76,140,204
X 177
Mvav 44,140,196
W 3
Dc 244,8,61,3072
Mvav 48,144,196
X 178
Mvav 80,144,204
s_add_u32 m0, s62, 12288
Ra 140,242,11264
Mvav 112,144,212
K 194,176,177
Mvav 144,144,220
X 179
Mvav 148,148,220
X 180
Mvav 116,148,212
Dc 245,8,61,0
Ra 144,242,12288
Mvav 84,148,204
K 195,178,179
Mvav 52,148,196
X 181
Mvav 56,152,196
X 182
Mvav 88,152,204
Dc 245,8,61,1024
Ra 148,242,13312
Mvav 120,152,212
K 202,180,181
Mvav 152,152,220
X 183
Mvav 156,156,220
X 184
Mvav 124,156,212
Dc 245,8,61,2048
Ra 152,242,14336
Mvav 92,156,204
K 203,182,183
Mvav 60,156,196
W 3
X 185
Cvaa 160,128,0,224
X 186
Cvaa 164,128,16,228
Dc 245,8,61,3072
Ra 156,242,15360
Cvaa 168,128,32,232
s_add_u32 s61, s61, s63
K 210,184,185
Cvaa 172,128,48,236
X 187
Mvaa 160,132,4
X 188
Mvaa 164,132,20
Ra 128,242,24576
Mvaa 168,132,36
s_xor_b32 s62, s62, 0x10000
K 211,186,187
Mvaa 172,132,52
X 189
Mvaa 160,136,8
X 190
Mvaa 164,136,24
s_add_u32 s64, s64, 1
Ra 132,242,25600
Mvaa 168,136,40
K 218,188,189
Mvaa 172,136,56
X 191
Mvaa 160,140,12
Ra 136,242,26624
Mvaa 164,140,28
Mvaa 168,140,44
s_add_u32 s36, s29, 262144
Mvaa 172,140,60
W 3
Cvaa 176,144,0,224
Ra 140,242,27648
Cvaa 180,144,16,228
X 160
Cvaa 184,144,32,232
K 219,190,191
Cvaa 188,144,48,236
X 161
Mvaa 176,148,4
X 162
Mvaa 180,148,20
buffer_store_dwordx4 a[64:67], v1, s[8:11], s36 offen offset:0 sc1
K 196,160,161
Mvaa 184,148,36
X 163
Mvaa 188,148,52
Ra 144,242,28672
Mvaa 176,152,8
X 164
Mvaa 180,152,24
Ra 148,242,29696
K 197,162,163
Mvaa 184,152,40
X 165
Mvaa 188,152,56
X 166
Mvaa 176,156,12
buffer_store_dwordx4 a[72:75], v1, s[8:11], s36 offen offset:1024 sc1
K 204,164,165
Mvaa 180,156,28
X 167
Mvaa 184,156,44
Ra 152,242,30720
Mvaa 188,156,60
)"
R"(W 3
X 168
Mvav 32,128,192
buffer_store_dwordx4 a[80:83], v1, s[8:11], s36 offen offset:2048 sc1
K 205,166,167
Mvav 64,128,200
X 169
Mvav 96,128,208
Ra 156,242,31744
Mvav 128,128,216
X 170
Mvav 132,132,216
K 212,168,169
Mvav 100,132,208
X 171
Mvav 68,132,200
X 172
Mvav 36,132,192
buffer_store_dwordx4 a[88:91], v1, s[8:11], s36 offen offset:3072 sc1
K 213,170,171
Mvav 40,136,192
X 173
Mvav 72,136,200
X 174
Mvav 104,136,208
Mvav 136,136,216
s_add_u32 s36, s29, 266240
K 220,172,173
Mvav 140,140,216
X 175
Mvav 108,140,208
X 176
Mvav 76,140,200
s_waitcnt vmcnt(4) lgkmcnt(0)
s_barrier
v_xor_b32 v242, 0x10000, v242
Mvav 44,140,192
Ra 128,242,0
K 221,174,175
Mvav 48,144,192
X 177
Mvav 80,144,200
buffer_store_dwordx4 a[96:99], v1, s[8:11], s36 offen offset:0 sc1
Mvav 112,144,208
X 178
Mvav 144,144,216
Ra 132,242,1024
K 198,176,177
Mvav 148,148,216
X 179
Mvav 116,148,208
X 180
Mvav 84,148,200
Ra 136,242,2048
Mvav 52,148,192
buffer_store_dwordx4 a[104:107], v1, s[8:11], s36 offen offset:1024 sc1
K 199,178,179
Mvav 56,152,192
X 181
Mvav 88,152,200
Ra 140,242,3072
Mvav 120,152,208
X 182
Mvav 152,152,216
K 206,180,181
Mvav 156,156,216
X 183
Mvav 124,156,208
Mvav 92,156,200
K 207,182,183
Mvav 60,156,192
W 0
Cvaa 160,128,0,224
Ra 144,242,4096
Ra 148,242,5120
Cvaa 164,128,16,228
s_add_u32 m0, s62, 0
Ra 152,242,6144
Cvaa 168,128,32,232
X 184
Cvaa 172,128,48,236
X 185
Mvaa 160,132,4
X 186
Mvaa 164,132,20
Ra 156,242,7168
K 214,184,185
Mvaa 168,132,36
Dc 1,8,61,0
Mvaa 172,132,52
X 187
Mvaa 160,136,8
X 188
Mvaa 164,136,24
Ra 128,242,16384
K 215,186,187
Mvaa 168,136,40
Dc 1,8,61,1024
Mvaa 172,136,56
X 189
Mvaa 160,140,12
X 190
Mvaa 164,140,28
Ra 132,242,17408
K 222,188,189
Mvaa 168,140,44
Dc 1,8,61,2048
Mvaa 172,140,60
X 191
v_smfmac_f32_16x16x64_bf16 a[160:163], a[176:179], v[192:199], v240
Ra 136,242,18432
K 223,190,191
v_smfmac_f32_16x16x64_bf16 a[164:167], a[176:179], v[200:207], v240
X 160
v_smfmac_f32_16x16x64_bf16 a[168:171], a[176:179], v[208:215], v240
Dc 1,8,61,3072
v_smfmac_f32_16x16x64_bf16 a[172:175], a[176:179], v[216:223], v240
W 3
X 161
Cvaa 176,144,0,224
s_add_u32 m0, s62, 4096
X 162
Cvaa 180,144,16,228
Ra 140,242,19456
K 192,160,161
Cvaa 184,144,32,232
X 163
Cvaa 188,144,48,236
Dc 243,8,61,0
Mvaa 176,148,4
X 164
Mvaa 180,148,20
Ra 144,242,20480
K 193,162,163
Mvaa 184,148,36
X 165
Mvaa 188,148,52
Dc 243,8,61,1024
Mvaa 176,152,8
X 166
Mvaa 180,152,24
Ra 148,242,21504
K 200,164,165
Mvaa 184,152,40
X 167
Mvaa 188,152,56
Dc 243,8,61,2048
Mvaa 176,156,12
X 168
Mvaa 180,156,28
Ra 152,242,22528
K 201,166,167
Mvaa 184,156,44
X 169
Mvaa 188,156,60
W 3
Dc 243,8,61,3072
Mvav 32,128,196
s_add_u32 m0, s62, 8192
X 170
Mvav 64,128,204
Ra 156,242,23552
K 208,168,169
Mvav 96,128,212
X 171
Mvav 128,128,220
Dc 244,8,61,0
Mvav 132,132,220
X 172
Mvav 100,132,212
Ra 128,242,8192
K 209,170,171
Mvav 68,132,204
X 173
Mvav 36,132,196
Dc 244,8,61,1024
Mvav 40,136,196
X 174
Mvav 72,136,204
Ra 132,242,9216
K 216,172,173
Mvav 104,136,212
X 175
Mvav 136,136,220
Dc 244,8,61,2048
Mvav 140,140,220
X 176
Mvav 108,140,212
Ra 136,242,10240
K 217,174,175
Mvav 76,140,204
X 177
Mvav 44,140,196
W 3
Dc 244,8,61,3072
Mvav 48,144,196
X 178
Mvav 80,144,204
s_add_u32 m0, s62, 12288
Ra 140,242,11264
Mvav 112,144,212
K 194,176,177
Mvav 144,144,220
X 179
Mvav 148,148,220
X 180
Mvav 116,148,212
Dc 245,8,61,0
Ra 144,242,12288
Mvav 84,148,204
K 195,178,179
Mvav 52,148,196
X 181
Mvav 56,152,196
X 182
Mvav 88,152,204
Dc 245,8,61,1024
Ra 148,242,13312
Mvav 120,152,212
K 202,180,181
Mvav 152,152,220
X 183
Mvav 156,156,220
X 184
Mvav 124,156,212
Dc 245,8,61,2048
Ra 152,242,14336
Mvav 92,156,204
K 203,182,183
v_bfe_u32 v28, v1, 4, 4
Mvav 60,156,196
W 3
X 185
Cvaa 160,128,0,224
Ra 156,242,15360
v_lshlrev_b32 v28, 3, v28
Cvaa 164,128,16,228
s_lshl_b32 s80, s56, 9
s_add_u32 s80, s80, 4352
Dc 245,8,61,3072
Cvaa 168,128,32,232
s_add_u32 s61, s61, s63
X 186
Cvaa 172,128,48,236
K 210,184,185
Mvaa 160,132,4
X 187
Mvaa 164,132,20
buffer_load_dwordx2 v[28:29], v28, s[8:11], s80 offen sc1
Ra 128,242,24576
Mvaa 168,132,36
s_xor_b32 s62, s62, 0x10000
X 188
Mvaa 172,132,52
K 211,186,187
Mvaa 160,136,8
X 189
Mvaa 164,136,24
s_add_u32 s64, s64, 1
X 190
Mvaa 168,136,40
Ra 132,242,25600
K 218,188,189
Mvaa 172,136,56
X 191
Mvaa 160,140,12
Ra 136,242,26624
Mvaa 164,140,28
Mvaa 168,140,44
buffer_store_dwordx4 a[112:115], v1, s[8:11], s36 offen offset:2048 sc1
Mvaa 172,140,60
W 3
Cvaa 176,144,0,224
Ra 140,242,27648
Cvaa 180,144,16,228
X 160
Cvaa 184,144,32,232
K 219,190,191
Cvaa 188,144,48,236
X 161
Mvaa 176,148,4
X 162
Mvaa 180,148,20
Ra 144,242,28672
K 196,160,161
Mvaa 184,148,36
X 163
Mvaa 188,148,52
X 164
Mvaa 176,152,8
Ra 148,242,29696
K 197,162,163
Mvaa 180,152,24
X 165
Mvaa 184,152,40
X 166
Mvaa 188,152,56
K 204,164,165
Mvaa 176,156,12
X 167
Mvaa 180,156,28
Ra 152,242,30720
Mvaa 184,156,44
X 168
Mvaa 188,156,60
W 3
K 205,166,167
Mvav 32,128,192
X 169
Mvav 64,128,200
Ra 156,242,31744
Mvav 96,128,208
X 170
Mvav 128,128,216
K 212,168,169
Mvav 132,132,216
X 171
Mvav 100,132,208
X 172
Mvav 68,132,200
buffer_store_dwordx4 a[120:123], v1, s[8:11], s36 offen offset:3072 sc1
Mvav 36,132,192
K 213,170,171
Mvav 40,136,192
X 173
Mvav 72,136,200
X 174
Mvav 104,136,208
Mvav 136,136,216
s_waitcnt vmcnt(2)
v_cmp_eq_u64 vcc, s[12:13], v[28:29]
s_cmp_eq_u64 vcc, -1
s_cbranch_scc0 .Lspin_65%=
.Lok_66%=:
K 220,172,173
Mvav 140,140,216
X 175
Mvav 108,140,208
X 176
Mvav 76,140,200
W 0
s_barrier
v_xor_b32 v242, 0x10000, v242
Mvav 44,140,192
Ra 128,242,0
K 221,174,175
Mvav 48,144,192
X 177
Mvav 80,144,200
X 178
Mvav 112,144,208
Ra 132,242,1024
Mvav 144,144,216
K 198,176,177
Mvav 148,148,216
X 179
Mvav 116,148,208
X 180
Mvav 84,148,200
Ra 136,242,2048
Mvav 52,148,192
K 199,178,179
Mvav 56,152,192
X 181
Mvav 88,152,200
Ra 140,242,3072
Mvav 120,152,208
X 182
Mvav 152,152,216
K 206,180,181
Mvav 156,156,216
X 183
Mvav 124,156,208
Mvav 92,156,200
K 207,182,183
Mvav 60,156,192
W 0
Cvaa 160,128,0,224
Ra 144,242,4096
Ra 148,242,5120
Cvaa 164,128,16,228
s_add_u32 m0, s62, 0
Ra 152,242,6144
Cvaa 168,128,32,232
X 184
Cvaa 172,128,48,236
X 185
Mvaa 160,132,4
X 186
Mvaa 164,132,20
Ra 156,242,7168
K 214,184,185
Mvaa 168,132,36
Dc 1,8,61,0
Mvaa 172,132,52
X 187
Mvaa 160,136,8
X 188
Mvaa 164,136,24
Ra 128,242,16384
)"
R"(K 215,186,187
Mvaa 168,136,40
Dc 1,8,61,1024
Mvaa 172,136,56
X 189
Mvaa 160,140,12
X 190
Mvaa 164,140,28
Ra 132,242,17408
K 222,188,189
Mvaa 168,140,44
Dc 1,8,61,2048
Mvaa 172,140,60
X 191
v_smfmac_f32_16x16x64_bf16 a[160:163], a[176:179], v[192:199], v240
Ra 136,242,18432
K 223,190,191
v_smfmac_f32_16x16x64_bf16 a[164:167], a[176:179], v[200:207], v240
X 160
v_smfmac_f32_16x16x64_bf16 a[168:171], a[176:179], v[208:215], v240
Dc 1,8,61,3072
v_smfmac_f32_16x16x64_bf16 a[172:175], a[176:179], v[216:223], v240
W 3
X 161
Cvaa 176,144,0,224
s_add_u32 m0, s62, 4096
X 162
Cvaa 180,144,16,228
Ra 140,242,19456
K 192,160,161
Cvaa 184,144,32,232
X 163
Cvaa 188,144,48,236
Dc 243,8,61,0
Mvaa 176,148,4
X 164
Mvaa 180,148,20
Ra 144,242,20480
K 193,162,163
Mvaa 184,148,36
X 165
Mvaa 188,148,52
Dc 243,8,61,1024
Mvaa 176,152,8
X 166
Mvaa 180,152,24
Ra 148,242,21504
K 200,164,165
Mvaa 184,152,40
X 167
Mvaa 188,152,56
Dc 243,8,61,2048
Mvaa 176,156,12
X 168
Mvaa 180,156,28
Ra 152,242,22528
K 201,166,167
Mvaa 184,156,44
X 169
Mvaa 188,156,60
W 3
Dc 243,8,61,3072
Mvav 32,128,196
s_add_u32 m0, s62, 8192
X 170
Mvav 64,128,204
Ra 156,242,23552
K 208,168,169
Mvav 96,128,212
X 171
Mvav 128,128,220
Dc 244,8,61,0
Mvav 132,132,220
X 172
Mvav 100,132,212
Ra 128,242,8192
K 209,170,171
Mvav 68,132,204
X 173
Mvav 36,132,196
Dc 244,8,61,1024
Mvav 40,136,196
X 174
Mvav 72,136,204
Ra 132,242,9216
K 216,172,173
Mvav 104,136,212
X 175
Mvav 136,136,220
Dc 244,8,61,2048
Mvav 140,140,220
X 176
Mvav 108,140,212
Ra 136,242,10240
K 217,174,175
Mvav 76,140,204
X 177
Mvav 44,140,196
W 3
Dc 244,8,61,3072
Mvav 48,144,196
X 178
Mvav 80,144,204
s_add_u32 m0, s62, 12288
Ra 140,242,11264
Mvav 112,144,212
K 194,176,177
Mvav 144,144,220
X 179
Mvav 148,148,220
X 180
Mvav 116,148,212
Dc 245,8,61,0
Ra 144,242,12288
Mvav 84,148,204
K 195,178,179
Mvav 52,148,196
X 181
Mvav 56,152,196
X 182
Mvav 88,152,204
Dc 245,8,61,1024
Ra 148,242,13312
Mvav 120,152,212
K 202,180,181
Mvav 152,152,220
s_add_u32 s81, s57, 2
X 183
Mvav 156,156,220
s_waitcnt vmcnt(14)
s_add_u32 s80, s71, 384
v_mov_b64 v[28:29], s[12:13]
s_mov_b64 exec, 1
buffer_store_dwordx2 v[28:29], off, s[8:11], s80 sc1
s_mov_b64 exec, -1
X 184
Mvav 124,156,212
s_bitcmp0_b32 s81, 2
s_cselect_b32 s22, -1, 0
s_and_b32 s81, s81, 3
Ra 152,242,14336
Mvav 92,156,204
s_lshl_b32 s82, s56, 2
K 203,182,183
Mvav 60,156,196
W 3
X 185
Cvaa 160,128,0,224
s_add_u32 s81, s81, s82
Dc 245,8,61,2048
Ra 156,242,15360
Cvaa 164,128,16,228
s_lshl_b32 s81, s81, 1
X 186
Cvaa 168,128,32,232
s_add_u32 s81, s81, s14
s_sub_u32 s81, s81, 2
K 210,184,185
Cvaa 172,128,48,236
s_mul_i32 s81, s81, 20480
X 187
Mvaa 160,132,4
Dc 245,8,61,3072
Ra 128,242,24576
Mvaa 164,132,20
s_add_u32 s61, s61, s63
X 188
Mvaa 168,132,36
s_xor_b32 s62, s62, 0x10000
v_min_u32 v2, 624, v1
v_lshl_add_u32 v2, v2, 3, s81
Mvaa 172,132,52
K 211,186,187
v_and_b32 v2, s22, v2
Mvaa 160,136,8
s_and_b32 s22, s22, 5120
X 189
Mvaa 164,136,24
s_add_u32 s64, s64, 1
global_load_dword v3, v2, s[26:27]
Ra 132,242,25600
Mvaa 168,136,40
X 190
Mvaa 172,136,56
v_add_u32 v2, s22, v2
K 218,188,189
Mvaa 160,140,12
X 191
Mvaa 164,140,28
Ra 136,242,26624
Mvaa 168,140,44
Mvaa 172,140,60
W 3
Cvaa 176,144,0,224
Ra 140,242,27648
Cvaa 180,144,16,228
X 160
Cvaa 184,144,32,232
K 219,190,191
Cvaa 188,144,48,236
X 161
Mvaa 176,148,4
X 162
Mvaa 180,148,20
Ra 144,242,28672
K 196,160,161
Mvaa 184,148,36
X 163
Mvaa 188,148,52
X 164
Mvaa 176,152,8
Ra 148,242,29696
K 197,162,163
Mvaa 180,152,24
X 165
Mvaa 184,152,40
X 166
Mvaa 188,152,56
K 204,164,165
Mvaa 176,156,12
X 167
Mvaa 180,156,28
Ra 152,242,30720
Mvaa 184,156,44
X 168
Mvaa 188,156,60
W 3
K 205,166,167
Mvav 32,128,192
X 169
Mvav 64,128,200
Ra 156,242,31744
Mvav 96,128,208
X 170
Mvav 128,128,216
K 212,168,169
Mvav 132,132,216
X 171
Mvav 100,132,208
X 172
Mvav 68,132,200
Mvav 36,132,192
K 213,170,171
Mvav 40,136,192
X 173
Mvav 72,136,200
X 174
Mvav 104,136,208
Mvav 136,136,216
K 220,172,173
Mvav 140,140,216
X 175
Mvav 108,140,208
X 176
Mvav 76,140,200
s_waitcnt vmcnt(1) lgkmcnt(0)
s_barrier
v_xor_b32 v242, 0x10000, v242
Mvav 44,140,192
Ra 128,242,0
K 221,174,175
Mvav 48,144,192
X 177
Mvav 80,144,200
X 178
Mvav 112,144,208
Ra 132,242,1024
Mvav 144,144,216
K 198,176,177
Mvav 148,148,216
X 179
Mvav 116,148,208
X 180
Mvav 84,148,200
Ra 136,242,2048
Mvav 52,148,192
K 199,178,179
Mvav 56,152,192
X 181
Mvav 88,152,200
Ra 140,242,3072
Mvav 120,152,208
X 182
Mvav 152,152,216
K 206,180,181
Mvav 156,156,216
X 183
Mvav 124,156,208
Mvav 92,156,200
K 207,182,183
Mvav 60,156,192
W 0
Cvaa 160,128,0,224
Ra 144,242,4096
Ra 148,242,5120
Cvaa 164,128,16,228
s_add_u32 m0, s62, 0
Ra 152,242,6144
Cvaa 168,128,32,232
X 184
Cvaa 172,128,48,236
X 185
Mvaa 160,132,4
X 186
Mvaa 164,132,20
Ra 156,242,7168
K 214,184,185
Mvaa 168,132,36
Dc 1,8,61,0
Mvaa 172,132,52
X 187
Mvaa 160,136,8
X 188
Mvaa 164,136,24
Ra 128,242,16384
K 215,186,187
Mvaa 168,136,40
Dc 1,8,61,1024
Mvaa 172,136,56
X 189
Mvaa 160,140,12
X 190
Mvaa 164,140,28
Ra 132,242,17408
K 222,188,189
Mvaa 168,140,44
Dc 1,8,61,2048
Mvaa 172,140,60
X 191
v_smfmac_f32_16x16x64_bf16 a[160:163], a[176:179], v[192:199], v240
Ra 136,242,18432
K 223,190,191
v_smfmac_f32_16x16x64_bf16 a[164:167], a[176:179], v[200:207], v240
X 160
v_smfmac_f32_16x16x64_bf16 a[168:171], a[176:179], v[208:215], v240
Dc 1,8,61,3072
v_smfmac_f32_16x16x64_bf16 a[172:175], a[176:179], v[216:223], v240
W 3
X 161
Cvaa 176,144,0,224
s_add_u32 m0, s62, 4096
X 162
Cvaa 180,144,16,228
Ra 140,242,19456
K 192,160,161
Cvaa 184,144,32,232
X 163
Cvaa 188,144,48,236
Dc 243,8,61,0
Mvaa 176,148,4
X 164
Mvaa 180,148,20
Ra 144,242,20480
K 193,162,163
Mvaa 184,148,36
X 165
Mvaa 188,148,52
Dc 243,8,61,1024
Mvaa 176,152,8
X 166
Mvaa 180,152,24
Ra 148,242,21504
K 200,164,165
Mvaa 184,152,40
X 167
Mvaa 188,152,56
Dc 243,8,61,2048
Mvaa 176,156,12
X 168
Mvaa 180,156,28
Ra 152,242,22528
K 201,166,167
Mvaa 184,156,44
X 169
Mvaa 188,156,60
W 3
Dc 243,8,61,3072
Mvav 32,128,196
s_add_u32 m0, s62, 8192
X 170
Mvav 64,128,204
Ra 156,242,23552
K 208,168,169
Mvav 96,128,212
X 171
Mvav 128,128,220
Dc 244,8,61,0
Mvav 132,132,220
X 172
Mvav 100,132,212
Ra 128,242,8192
K 209,170,171
Mvav 68,132,204
X 173
Mvav 36,132,196
Dc 244,8,61,1024
Mvav 40,136,196
X 174
Mvav 72,136,204
Ra 132,242,9216
)"
R"(K 216,172,173
Mvav 104,136,212
X 175
Mvav 136,136,220
Dc 244,8,61,2048
Mvav 140,140,220
X 176
Mvav 108,140,212
Ra 136,242,10240
K 217,174,175
Mvav 76,140,204
X 177
Mvav 44,140,196
W 3
Dc 244,8,61,3072
Mvav 48,144,196
X 178
Mvav 80,144,204
s_add_u32 m0, s62, 12288
Ra 140,242,11264
Mvav 112,144,212
K 194,176,177
Mvav 144,144,220
X 179
Mvav 148,148,220
X 180
Mvav 116,148,212
Dc 245,8,61,0
Ra 144,242,12288
Mvav 84,148,204
K 195,178,179
Mvav 52,148,196
X 181
Mvav 56,152,196
X 182
Mvav 88,152,204
Dc 245,8,61,1024
Ra 148,242,13312
Mvav 120,152,212
K 202,180,181
Mvav 152,152,220
X 183
Mvav 156,156,220
X 184
Mvav 124,156,212
Dc 245,8,61,2048
Ra 152,242,14336
Mvav 92,156,204
K 203,182,183
Mvav 60,156,196
W 3
X 185
Cvaa 160,128,0,224
X 186
Cvaa 164,128,16,228
Dc 245,8,61,3072
Ra 156,242,15360
Cvaa 168,128,32,232
s_add_u32 s61, s61, s63
K 210,184,185
Cvaa 172,128,48,236
X 187
Mvaa 160,132,4
X 188
Mvaa 164,132,20
Ra 128,242,24576
Mvaa 168,132,36
s_xor_b32 s62, s62, 0x10000
global_load_dword v3, v2, s[26:27]
K 211,186,187
Mvaa 172,132,52
X 189
Mvaa 160,136,8
X 190
Mvaa 164,136,24
s_add_u32 s64, s64, 1
Ra 132,242,25600
v_add_u32 v2, s22, v2
Mvaa 168,136,40
K 218,188,189
Mvaa 172,136,56
X 191
Mvaa 160,140,12
Ra 136,242,26624
Mvaa 164,140,28
Mvaa 168,140,44
Mvaa 172,140,60
W 3
Cvaa 176,144,0,224
Ra 140,242,27648
Cvaa 180,144,16,228
X 160
Cvaa 184,144,32,232
K 219,190,191
Cvaa 188,144,48,236
X 161
Mvaa 176,148,4
X 162
Mvaa 180,148,20
Ra 144,242,28672
K 196,160,161
Mvaa 184,148,36
X 163
Mvaa 188,148,52
X 164
Mvaa 176,152,8
Ra 148,242,29696
K 197,162,163
Mvaa 180,152,24
X 165
Mvaa 184,152,40
X 166
Mvaa 188,152,56
K 204,164,165
Mvaa 176,156,12
X 167
Mvaa 180,156,28
Ra 152,242,30720
Mvaa 184,156,44
X 168
Mvaa 188,156,60
W 3
K 205,166,167
Mvav 32,128,192
X 169
Mvav 64,128,200
Ra 156,242,31744
Mvav 96,128,208
X 170
Mvav 128,128,216
K 212,168,169
Mvav 132,132,216
X 171
Mvav 100,132,208
X 172
Mvav 68,132,200
Mvav 36,132,192
K 213,170,171
Mvav 40,136,192
X 173
Mvav 72,136,200
X 174
Mvav 104,136,208
Mvav 136,136,216
K 220,172,173
Mvav 140,140,216
X 175
Mvav 108,140,208
X 176
Mvav 76,140,200
s_waitcnt vmcnt(1) lgkmcnt(0)
s_barrier
v_xor_b32 v242, 0x10000, v242
Mvav 44,140,192
Ra 128,242,0
K 221,174,175
Mvav 48,144,192
X 177
Mvav 80,144,200
X 178
Mvav 112,144,208
Ra 132,242,1024
Mvav 144,144,216
K 198,176,177
Mvav 148,148,216
X 179
Mvav 116,148,208
X 180
Mvav 84,148,200
Ra 136,242,2048
Mvav 52,148,192
K 199,178,179
Mvav 56,152,192
X 181
Mvav 88,152,200
Ra 140,242,3072
Mvav 120,152,208
X 182
Mvav 152,152,216
K 206,180,181
Mvav 156,156,216
X 183
Mvav 124,156,208
Mvav 92,156,200
K 207,182,183
Mvav 60,156,192
W 0
Cvaa 160,128,0,224
Ra 144,242,4096
Ra 148,242,5120
Cvaa 164,128,16,228
s_add_u32 m0, s62, 0
Ra 152,242,6144
Cvaa 168,128,32,232
X 184
Cvaa 172,128,48,236
X 185
Mvaa 160,132,4
X 186
Mvaa 164,132,20
Ra 156,242,7168
K 214,184,185
Mvaa 168,132,36
Dc 1,8,61,0
Mvaa 172,132,52
X 187
Mvaa 160,136,8
X 188
Mvaa 164,136,24
Ra 128,242,16384
K 215,186,187
Mvaa 168,136,40
Dc 1,8,61,1024
Mvaa 172,136,56
X 189
Mvaa 160,140,12
X 190
Mvaa 164,140,28
Ra 132,242,17408
K 222,188,189
Mvaa 168,140,44
Dc 1,8,61,2048
Mvaa 172,140,60
X 191
v_smfmac_f32_16x16x64_bf16 a[160:163], a[176:179], v[192:199], v240
Ra 136,242,18432
K 223,190,191
v_smfmac_f32_16x16x64_bf16 a[164:167], a[176:179], v[200:207], v240
X 160
v_smfmac_f32_16x16x64_bf16 a[168:171], a[176:179], v[208:215], v240
Dc 1,8,61,3072
v_smfmac_f32_16x16x64_bf16 a[172:175], a[176:179], v[216:223], v240
W 3
X 161
Cvaa 176,144,0,224
s_add_u32 m0, s62, 4096
X 162
Cvaa 180,144,16,228
Ra 140,242,19456
K 192,160,161
Cvaa 184,144,32,232
X 163
Cvaa 188,144,48,236
Dc 243,8,61,0
Mvaa 176,148,4
X 164
Mvaa 180,148,20
Ra 144,242,20480
K 193,162,163
Mvaa 184,148,36
X 165
Mvaa 188,148,52
Dc 243,8,61,1024
Mvaa 176,152,8
X 166
Mvaa 180,152,24
Ra 148,242,21504
K 200,164,165
Mvaa 184,152,40
X 167
Mvaa 188,152,56
Dc 243,8,61,2048
Mvaa 176,156,12
X 168
Mvaa 180,156,28
Ra 152,242,22528
K 201,166,167
Mvaa 184,156,44
X 169
Mvaa 188,156,60
W 3
Dc 243,8,61,3072
Mvav 32,128,196
s_add_u32 m0, s62, 8192
X 170
Mvav 64,128,204
Ra 156,242,23552
K 208,168,169
Mvav 96,128,212
X 171
Mvav 128,128,220
Dc 244,8,61,0
Mvav 132,132,220
X 172
Mvav 100,132,212
Ra 128,242,8192
K 209,170,171
Mvav 68,132,204
X 173
Mvav 36,132,196
Dc 244,8,61,1024
Mvav 40,136,196
X 174
Mvav 72,136,204
Ra 132,242,9216
K 216,172,173
Mvav 104,136,212
X 175
Mvav 136,136,220
Dc 244,8,61,2048
Mvav 140,140,220
X 176
Mvav 108,140,212
Ra 136,242,10240
K 217,174,175
Mvav 76,140,204
X 177
Mvav 44,140,196
W 3
Dc 244,8,61,3072
Mvav 48,144,196
X 178
Mvav 80,144,204
s_add_u32 m0, s62, 12288
Ra 140,242,11264
Mvav 112,144,212
K 194,176,177
Mvav 144,144,220
X 179
Mvav 148,148,220
X 180
Mvav 116,148,212
Dc 245,8,61,0
Ra 144,242,12288
Mvav 84,148,204
K 195,178,179
Mvav 52,148,196
X 181
Mvav 56,152,196
X 182
Mvav 88,152,204
Dc 245,8,61,1024
Ra 148,242,13312
Mvav 120,152,212
K 202,180,181
Mvav 152,152,220
X 183
Mvav 156,156,220
X 184
Mvav 124,156,212
Dc 245,8,61,2048
Ra 152,242,14336
Mvav 92,156,204
K 203,182,183
Mvav 60,156,196
W 3
X 185
Cvaa 160,128,0,224
X 186
Cvaa 164,128,16,228
Dc 245,8,61,3072
Ra 156,242,15360
Cvaa 168,128,32,232
s_add_u32 s61, s61, s63
K 210,184,185
Cvaa 172,128,48,236
X 187
Mvaa 160,132,4
X 188
Mvaa 164,132,20
Ra 128,242,24576
Mvaa 168,132,36
s_xor_b32 s62, s62, 0x10000
global_load_dword v3, v2, s[26:27]
K 211,186,187
Mvaa 172,132,52
v_add_u32 v2, s22, v2
Mvaa 160,136,8
s_lshr_b32 s80, s15, 3
s_lshl_b32 s80, s80, 1
s_add_u32 s80, s80, s14
Mvaa 164,136,24
s_sub_u32 s80, s80, 2
s_lshl_b32 s80, s80, 12
v_min_u32 v247, 496, v1
Mvaa 168,136,40
s_add_u32 s64, s64, 1
X 189
Mvaa 172,136,56
Ra 132,242,25600
v_lshl_add_u32 v247, v247, 3, s80
Mvaa 160,140,12
X 190
Mvaa 164,140,28
v_min_u32 v247, .Lend%=-.Lbeg%=-4, v247
K 218,188,189
Mvaa 168,140,44
global_load_dword v3, v247, s[6:7]
Ra 136,242,26624
Mvaa 172,140,60
W 3
X 191
Cvaa 176,144,0,224
X 160
Cvaa 180,144,16,228
Ra 140,242,27648
K 219,190,191
Cvaa 184,144,32,232
X 161
Cvaa 188,144,48,236
)"
R"(X 162
Mvaa 176,148,4
Ra 144,242,28672
K 196,160,161
Mvaa 180,148,20
X 163
Mvaa 184,148,36
X 164
Mvaa 188,148,52
K 197,162,163
Mvaa 176,152,8
X 165
Mvaa 180,152,24
Ra 148,242,29696
Mvaa 184,152,40
X 166
Mvaa 188,152,56
K 204,164,165
Mvaa 176,156,12
X 167
Mvaa 180,156,28
Ra 152,242,30720
Mvaa 184,156,44
X 168
Mvaa 188,156,60
W 3
K 205,166,167
Mvav 32,128,192
X 169
Mvav 64,128,200
Ra 156,242,31744
Mvav 96,128,208
X 170
Mvav 128,128,216
K 212,168,169
Mvav 132,132,216
X 171
Mvav 100,132,208
X 172
Mvav 68,132,200
Mvav 36,132,192
K 213,170,171
Mvav 40,136,192
X 173
Mvav 72,136,200
X 174
Mvav 104,136,208
Mvav 136,136,216
K 220,172,173
Mvav 140,140,216
X 175
Mvav 108,140,208
X 176
Mvav 76,140,200
s_waitcnt vmcnt(2) lgkmcnt(0)
s_barrier
v_xor_b32 v242, 0x10000, v242
Mvav 44,140,192
Ra 128,242,0
K 221,174,175
Mvav 48,144,192
X 177
Mvav 80,144,200
X 178
Mvav 112,144,208
Ra 132,242,1024
Mvav 144,144,216
K 198,176,177
Mvav 148,148,216
X 179
Mvav 116,148,208
X 180
Mvav 84,148,200
Ra 136,242,2048
Mvav 52,148,192
K 199,178,179
Mvav 56,152,192
X 181
Mvav 88,152,200
Ra 140,242,3072
Mvav 120,152,208
X 182
Mvav 152,152,216
K 206,180,181
Mvav 156,156,216
X 183
Mvav 124,156,208
Mvav 92,156,200
K 207,182,183
Mvav 60,156,192
W 0
Cvaa 160,128,0,224
Ra 144,242,4096
Ra 148,242,5120
Cvaa 164,128,16,228
s_add_u32 m0, s62, 0
Ra 152,242,6144
Cvaa 168,128,32,232
X 184
Cvaa 172,128,48,236
X 185
Mvaa 160,132,4
X 186
Mvaa 164,132,20
Ra 156,242,7168
K 214,184,185
Mvaa 168,132,36
Dc 1,8,61,0
Mvaa 172,132,52
X 187
Mvaa 160,136,8
X 188
Mvaa 164,136,24
Ra 128,242,16384
K 215,186,187
Mvaa 168,136,40
Dc 1,8,61,1024
Mvaa 172,136,56
X 189
Mvaa 160,140,12
X 190
Mvaa 164,140,28
Ra 132,242,17408
K 222,188,189
Mvaa 168,140,44
Dc 1,8,61,2048
Mvaa 172,140,60
X 191
v_smfmac_f32_16x16x64_bf16 a[160:163], a[176:179], v[192:199], v240
Ra 136,242,18432
K 223,190,191
v_smfmac_f32_16x16x64_bf16 a[164:167], a[176:179], v[200:207], v240
X 160
v_smfmac_f32_16x16x64_bf16 a[168:171], a[176:179], v[208:215], v240
Dc 1,8,61,3072
v_smfmac_f32_16x16x64_bf16 a[172:175], a[176:179], v[216:223], v240
W 3
X 161
Cvaa 176,144,0,224
s_add_u32 m0, s62, 4096
X 162
Cvaa 180,144,16,228
Ra 140,242,19456
K 192,160,161
Cvaa 184,144,32,232
X 163
Cvaa 188,144,48,236
Dc 243,8,61,0
Mvaa 176,148,4
X 164
Mvaa 180,148,20
Ra 144,242,20480
K 193,162,163
Mvaa 184,148,36
X 165
Mvaa 188,148,52
Dc 243,8,61,1024
Mvaa 176,152,8
X 166
Mvaa 180,152,24
Ra 148,242,21504
K 200,164,165
Mvaa 184,152,40
X 167
Mvaa 188,152,56
Dc 243,8,61,2048
Mvaa 176,156,12
X 168
Mvaa 180,156,28
Ra 152,242,22528
K 201,166,167
Mvaa 184,156,44
X 169
Mvaa 188,156,60
W 3
Dc 243,8,61,3072
Mvav 32,128,196
s_add_u32 m0, s62, 8192
X 170
Mvav 64,128,204
Ra 156,242,23552
K 208,168,169
Mvav 96,128,212
X 171
Mvav 128,128,220
Dc 244,8,61,0
Mvav 132,132,220
X 172
Mvav 100,132,212
Ra 128,242,8192
K 209,170,171
Mvav 68,132,204
X 173
Mvav 36,132,196
Dc 244,8,61,1024
Mvav 40,136,196
X 174
Mvav 72,136,204
Ra 132,242,9216
K 216,172,173
Mvav 104,136,212
X 175
Mvav 136,136,220
Dc 244,8,61,2048
Mvav 140,140,220
X 176
Mvav 108,140,212
Ra 136,242,10240
K 217,174,175
Mvav 76,140,204
X 177
Mvav 44,140,196
W 3
Dc 244,8,61,3072
Mvav 48,144,196
X 178
Mvav 80,144,204
s_add_u32 m0, s62, 12288
Ra 140,242,11264
Mvav 112,144,212
K 194,176,177
Mvav 144,144,220
X 179
Mvav 148,148,220
X 180
Mvav 116,148,212
Dc 245,8,61,0
Ra 144,242,12288
Mvav 84,148,204
K 195,178,179
Mvav 52,148,196
X 181
Mvav 56,152,196
X 182
Mvav 88,152,204
Dc 245,8,61,1024
Ra 148,242,13312
Mvav 120,152,212
K 202,180,181
Mvav 152,152,220
X 183
Mvav 156,156,220
X 184
Mvav 124,156,212
Dc 245,8,61,2048
Ra 152,242,14336
Mvav 92,156,204
K 203,182,183
v_bfe_u32 v28, v1, 4, 4
Mvav 60,156,196
W 3
X 185
Cvaa 160,128,0,224
Ra 156,242,15360
v_lshlrev_b32 v28, 3, v28
Cvaa 164,128,16,228
s_lshl_b32 s80, s56, 9
s_add_u32 s80, s80, 4480
Dc 245,8,61,3072
Cvaa 168,128,32,232
s_add_u32 s61, s61, s63
X 186
Cvaa 172,128,48,236
K 210,184,185
Mvaa 160,132,4
X 187
Mvaa 164,132,20
buffer_load_dwordx2 v[28:29], v28, s[8:11], s80 offen sc1
Ra 128,242,24576
Mvaa 168,132,36
s_xor_b32 s62, s62, 0x10000
X 188
Mvaa 172,132,52
K 211,186,187
Mvaa 160,136,8
X 189
Mvaa 164,136,24
s_add_u32 s64, s64, 1
global_load_dword v3, v2, s[26:27]
Ra 132,242,25600
Mvaa 168,136,40
X 190
Mvaa 172,136,56
v_add_u32 v2, s22, v2
K 218,188,189
Mvaa 160,140,12
X 191
Mvaa 164,140,28
Ra 136,242,26624
Mvaa 168,140,44
Mvaa 172,140,60
W 3
Cvaa 176,144,0,224
Ra 140,242,27648
Cvaa 180,144,16,228
X 160
Cvaa 184,144,32,232
K 219,190,191
Cvaa 188,144,48,236
X 161
Mvaa 176,148,4
X 162
Mvaa 180,148,20
Ra 144,242,28672
K 196,160,161
Mvaa 184,148,36
X 163
Mvaa 188,148,52
X 164
Mvaa 176,152,8
Ra 148,242,29696
K 197,162,163
Mvaa 180,152,24
X 165
Mvaa 184,152,40
X 166
Mvaa 188,152,56
K 204,164,165
Mvaa 176,156,12
X 167
Mvaa 180,156,28
Ra 152,242,30720
Mvaa 184,156,44
X 168
Mvaa 188,156,60
W 3
K 205,166,167
Mvav 32,128,192
X 169
Mvav 64,128,200
Ra 156,242,31744
Mvav 96,128,208
X 170
Mvav 128,128,216
K 212,168,169
Mvav 132,132,216
X 171
Mvav 100,132,208
X 172
Mvav 68,132,200
Mvav 36,132,192
K 213,170,171
Mvav 40,136,192
X 173
Mvav 72,136,200
X 174
Mvav 104,136,208
Mvav 136,136,216
s_waitcnt vmcnt(1)
v_cmp_eq_u64 vcc, s[12:13], v[28:29]
s_cmp_eq_u64 vcc, -1
s_cbranch_scc0 .Lspin_68%=
.Lok_69%=:
K 220,172,173
Mvav 140,140,216
X 175
Mvav 108,140,208
X 176
Mvav 76,140,200
W 0
s_barrier
v_xor_b32 v242, 0x10000, v242
Mvav 44,140,192
Ra 128,242,0
K 221,174,175
Mvav 48,144,192
X 177
Mvav 80,144,200
X 178
Mvav 112,144,208
Ra 132,242,1024
Mvav 144,144,216
K 198,176,177
Mvav 148,148,216
X 179
Mvav 116,148,208
X 180
Mvav 84,148,200
Ra 136,242,2048
Mvav 52,148,192
K 199,178,179
Mvav 56,152,192
X 181
Mvav 88,152,200
Ra 140,242,3072
Mvav 120,152,208
X 182
Mvav 152,152,216
K 206,180,181
Mvav 156,156,216
X 183
Mvav 124,156,208
Mvav 92,156,200
K 207,182,183
Mvav 60,156,192
W 0
Cvaa 160,128,0,224
Ra 144,242,4096
Ra 148,242,5120
Cvaa 164,128,16,228
)"
R"(s_add_u32 m0, s62, 0
Ra 152,242,6144
Cvaa 168,128,32,232
X 184
Cvaa 172,128,48,236
X 185
Mvaa 160,132,4
X 186
Mvaa 164,132,20
Ra 156,242,7168
K 214,184,185
Mvaa 168,132,36
Dc 1,8,61,0
Mvaa 172,132,52
X 187
Mvaa 160,136,8
X 188
Mvaa 164,136,24
Ra 128,242,16384
K 215,186,187
Mvaa 168,136,40
Dc 1,8,61,1024
Mvaa 172,136,56
X 189
Mvaa 160,140,12
X 190
Mvaa 164,140,28
Ra 132,242,17408
K 222,188,189
Mvaa 168,140,44
Dc 1,8,61,2048
Mvaa 172,140,60
X 191
v_smfmac_f32_16x16x64_bf16 a[160:163], a[176:179], v[192:199], v240
Ra 136,242,18432
K 223,190,191
v_smfmac_f32_16x16x64_bf16 a[164:167], a[176:179], v[200:207], v240
X 160
v_smfmac_f32_16x16x64_bf16 a[168:171], a[176:179], v[208:215], v240
Dc 1,8,61,3072
v_smfmac_f32_16x16x64_bf16 a[172:175], a[176:179], v[216:223], v240
W 3
X 161
Cvaa 176,144,0,224
s_add_u32 m0, s62, 4096
X 162
Cvaa 180,144,16,228
s_sub_u32 s81, s57, 1
Ra 140,242,19456
K 192,160,161
Cvaa 184,144,32,232
X 163
Cvaa 188,144,48,236
s_lshl_b32 s85, s81, 5
Dc 243,8,61,0
Mvaa 176,148,4
X 164
Mvaa 180,148,20
s_add_u32 s85, s85, s56
Ra 144,242,20480
K 193,162,163
Mvaa 184,148,36
X 165
Mvaa 188,148,52
Dc 243,8,61,1024
Mvaa 176,152,8
s_lshl_b32 s85, s85, 1
X 166
Mvaa 180,152,24
Ra 148,242,21504
K 200,164,165
Mvaa 184,152,40
s_add_u32 s85, s85, s14
X 167
Mvaa 188,152,56
Dc 243,8,61,2048
Mvaa 176,156,12
s_sub_u32 s85, s85, 2
X 168
Mvaa 180,156,28
Ra 152,242,22528
K 201,166,167
Mvaa 184,156,44
X 169
Mvaa 188,156,60
W 3
s_cmp_lt_u32 s85, 160
s_cselect_b64 s[20:21], 0, -1
Dc 243,8,61,3072
Mvav 32,128,196
s_add_u32 m0, s62, 8192
X 170
Mvav 64,128,204
s_mul_i32 s18, s85, 205
Ra 156,242,23552
K 208,168,169
Mvav 96,128,212
X 171
Mvav 128,128,220
s_lshr_b32 s18, s18, 12
Dc 244,8,61,0
Mvav 132,132,220
X 172
Mvav 100,132,212
Ra 128,242,8192
K 209,170,171
Mvav 68,132,204
s_and_b32 s18, s18, 7
X 173
Mvav 36,132,196
Dc 244,8,61,1024
Mvav 40,136,196
X 174
Mvav 72,136,204
Ra 132,242,9216
v_lshrrev_b32 v4, 1, v1
Mvav 104,136,212
K 216,172,173
v_min_u32 v5, 344, v4
Mvav 136,136,220
Dc 244,8,61,2048
Mvav 140,140,220
X 175
Mvav 108,140,212
s_lshl_b32 s81, s18, 3
X 176
Mvav 76,140,204
Ra 136,242,10240
K 217,174,175
Mvav 44,140,196
W 3
Dc 244,8,61,3072
v_mov_b32 v6, s81
Mvav 48,144,196
X 177
Mvav 80,144,204
s_add_u32 m0, s62, 12288
X 178
Mvav 112,144,212
Ra 140,242,11264
K 194,176,177
Mvav 144,144,220
buffer_load_dwordx2 v[8:9], v4, s[8:11], 0 offen offset:2560 sc1
Mvav 148,148,220
X 179
Mvav 116,148,212
X 180
Mvav 84,148,204
Ra 144,242,12288
K 195,178,179
Mvav 52,148,196
buffer_load_dwordx2 v[10:11], v5, s[8:11], 0 offen offset:3072 sc1
Mvav 56,152,196
X 181
Mvav 88,152,204
X 182
Mvav 120,152,212
Ra 148,242,13312
K 202,180,181
Mvav 152,152,220
Dc 245,8,61,0
Mvav 156,156,220
X 183
Mvav 124,156,212
X 184
Mvav 92,156,204
Ra 152,242,14336
K 203,182,183
Mvav 60,156,196
W 3
buffer_load_dwordx2 v[12:13], v6, s[8:11], 0 offen offset:3584 sc1
Cvaa 160,128,0,224
X 185
Cvaa 164,128,16,228
X 186
Cvaa 168,128,32,232
Ra 156,242,15360
K 210,184,185
Cvaa 172,128,48,236
Dc 245,8,61,1024
Mvaa 160,132,4
X 187
Mvaa 164,132,20
X 188
Mvaa 168,132,36
Ra 128,242,24576
K 211,186,187
Mvaa 172,132,52
Dc 245,8,61,2048
Mvaa 160,136,8
X 189
Mvaa 164,136,24
X 190
Mvaa 168,136,40
Ra 132,242,25600
K 218,188,189
Mvaa 172,136,56
Dc 245,8,61,3072
Mvaa 160,140,12
s_add_u32 s61, s61, s63
s_xor_b32 s62, s62, 0x10000
s_add_u32 s64, s64, 1
Mvaa 164,140,28
X 191
Mvaa 168,140,44
Ra 136,242,26624
Mvaa 172,140,60
W 3
X 160
Cvaa 176,144,0,224
Ra 140,242,27648
K 219,190,191
Cvaa 180,144,16,228
X 161
Cvaa 184,144,32,232
X 162
Cvaa 188,144,48,236
K 196,160,161
Mvaa 176,148,4
X 163
Mvaa 180,148,20
Ra 144,242,28672
Mvaa 184,148,36
X 164
Mvaa 188,148,52
K 197,162,163
Mvaa 176,152,8
X 165
Mvaa 180,152,24
Ra 148,242,29696
Mvaa 184,152,40
X 166
Mvaa 188,152,56
K 204,164,165
Mvaa 176,156,12
X 167
Mvaa 180,156,28
Ra 152,242,30720
Mvaa 184,156,44
X 168
Mvaa 188,156,60
W 3
K 205,166,167
Mvav 32,128,192
X 169
Mvav 64,128,200
Ra 156,242,31744
Mvav 96,128,208
X 170
Mvav 128,128,216
K 212,168,169
Mvav 132,132,216
X 171
Mvav 100,132,208
X 172
Mvav 68,132,200
Mvav 36,132,192
K 213,170,171
Mvav 40,136,192
X 173
Mvav 72,136,200
X 174
Mvav 104,136,208
Mvav 136,136,216
s_waitcnt vmcnt(3)
v_cmp_eq_u64 vcc, s[12:13], v[8:9]
v_cmp_eq_u64_e64 s[86:87], s[12:13], v[10:11]
s_and_b64 vcc, vcc, s[86:87]
s_or_b64 vcc, vcc, s[20:21]
s_cmp_eq_u64 vcc, -1
s_cbranch_scc0 .Lspin_71%=
.Lok_72%=:
K 220,172,173
Mvav 140,140,216
X 175
Mvav 108,140,208
X 176
Mvav 76,140,200
s_waitcnt vmcnt(0) lgkmcnt(0)
s_barrier
v_xor_b32 v242, 0x10000, v242
Mvav 44,140,192
Ra 128,242,0
K 221,174,175
Mvav 48,144,192
X 177
Mvav 80,144,200
X 178
Mvav 112,144,208
Ra 132,242,1024
Mvav 144,144,216
K 198,176,177
Mvav 148,148,216
X 179
Mvav 116,148,208
X 180
Mvav 84,148,200
Ra 136,242,2048
Mvav 52,148,192
K 199,178,179
Mvav 56,152,192
X 181
Mvav 88,152,200
Ra 140,242,3072
Mvav 120,152,208
X 182
Mvav 152,152,216
K 206,180,181
Mvav 156,156,216
X 183
Mvav 124,156,208
Mvav 92,156,200
K 207,182,183
Mvav 60,156,192
W 0
Cvaa 160,128,0,224
Ra 144,242,4096
Ra 148,242,5120
Cvaa 164,128,16,228
s_add_u32 m0, s62, 0
Ra 152,242,6144
Cvaa 168,128,32,232
X 184
Cvaa 172,128,48,236
X 185
Mvaa 160,132,4
X 186
Mvaa 164,132,20
Ra 156,242,7168
K 214,184,185
Mvaa 168,132,36
Dc 1,8,61,0
Mvaa 172,132,52
X 187
Mvaa 160,136,8
X 188
Mvaa 164,136,24
Ra 128,242,16384
K 215,186,187
Mvaa 168,136,40
Dc 1,8,61,1024
Mvaa 172,136,56
X 189
Mvaa 160,140,12
X 190
Mvaa 164,140,28
Ra 132,242,17408
K 222,188,189
Mvaa 168,140,44
Dc 1,8,61,2048
Mvaa 172,140,60
X 191
v_smfmac_f32_16x16x64_bf16 a[160:163], a[176:179], v[192:199], v240
Ra 136,242,18432
K 223,190,191
v_smfmac_f32_16x16x64_bf16 a[164:167], a[176:179], v[200:207], v240
X 160
v_smfmac_f32_16x16x64_bf16 a[168:171], a[176:179], v[208:215], v240
Dc 1,8,61,3072
v_smfmac_f32_16x16x64_bf16 a[172:175], a[176:179], v[216:223], v240
W 3
X 161
Cvaa 176,144,0,224
s_add_u32 m0, s62, 4096
X 162
Cvaa 180,144,16,228
Ra 140,242,19456
K 192,160,161
Cvaa 184,144,32,232
X 163
Cvaa 188,144,48,236
Dc 243,8,61,0
Mvaa 176,148,4
X 164
Mvaa 180,148,20
Ra 144,242,20480
K 193,162,163
Mvaa 184,148,36
X 165
Mvaa 188,148,52
Dc 243,8,61,1024
Mvaa 176,152,8
X 166
Mvaa 180,152,24
Ra 148,242,21504
K 200,164,165
Mvaa 184,152,40
X 167
Mvaa 188,152,56
Dc 243,8,61,2048
Mvaa 176,156,12
X 168
Mvaa 180,156,28
Ra 152,242,22528
K 201,166,167
Mvaa 184,156,44
X 169
Mvaa 188,156,60
W 3
Dc 243,8,61,3072
)"
R"(Mvav 32,128,196
s_add_u32 m0, s62, 8192
X 170
Mvav 64,128,204
Ra 156,242,23552
K 208,168,169
Mvav 96,128,212
X 171
Mvav 128,128,220
Dc 244,8,61,0
Mvav 132,132,220
X 172
Mvav 100,132,212
Ra 128,242,8192
K 209,170,171
Mvav 68,132,204
X 173
Mvav 36,132,196
Dc 244,8,61,1024
Mvav 40,136,196
X 174
Mvav 72,136,204
Ra 132,242,9216
K 216,172,173
Mvav 104,136,212
X 175
Mvav 136,136,220
Dc 244,8,61,2048
Mvav 140,140,220
X 176
Mvav 108,140,212
Ra 136,242,10240
K 217,174,175
Mvav 76,140,204
X 177
Mvav 44,140,196
W 3
Dc 244,8,61,3072
Mvav 48,144,196
X 178
Mvav 80,144,204
s_add_u32 m0, s62, 12288
Ra 140,242,11264
Mvav 112,144,212
K 194,176,177
Mvav 144,144,220
X 179
Mvav 148,148,220
X 180
Mvav 116,148,212
Dc 245,8,61,0
Ra 144,242,12288
Mvav 84,148,204
K 195,178,179
Mvav 52,148,196
X 181
Mvav 56,152,196
X 182
Mvav 88,152,204
Dc 245,8,61,1024
Ra 148,242,13312
Mvav 120,152,212
v_xor_b32 v6, s12, v12
K 202,180,181
Mvav 152,152,220
X 183
Mvav 156,156,220
Ra 152,242,14336
v_and_b32 v6, 2047, v6
Mvav 124,156,212
Dc 245,8,61,2048
v_lshlrev_b32 v6, 3, v6
Mvav 92,156,204
X 184
Mvav 60,156,196
W 3
K 203,182,183
Cvaa 160,128,0,224
s_mov_b32 s81, 40960
X 185
Cvaa 164,128,16,228
Dc 245,8,61,3072
Ra 156,242,15360
Cvaa 168,128,32,232
s_add_u32 s61, s61, s63
X 186
Cvaa 172,128,48,236
K 210,184,185
Mvaa 160,132,4
X 187
Mvaa 164,132,20
buffer_load_dword v7, v6, s[8:11], s81 offen sc1
v_lshrrev_b32 v5, 7, v1
Mvaa 168,132,36
s_xor_b32 s62, s62, 0x10000
X 188
Mvaa 172,132,52
Ra 128,242,24576
K 211,186,187
Mvaa 160,136,8
Ra 132,242,25600
v_mul_u32_u24 v4, 20480, v5
Mvaa 164,136,24
s_add_u32 s64, s64, 1
X 189
Mvaa 168,136,40
X 190
Mvaa 172,136,56
v_and_b32 v6, 0x70, v1
K 218,188,189
Mvaa 160,140,12
X 191
Mvaa 164,140,28
Ra 136,242,26624
v_add_u32 v4, v4, v6
Mvaa 168,140,44
v_add_u32 v5, 2, v5
Mvaa 172,140,60
W 3
X 160
Cvaa 176,144,0,224
K 219,190,191
v_lshrrev_b32 v5, 3, v5
Cvaa 180,144,16,228
X 161
Cvaa 184,144,32,232
Ra 140,242,27648
v_lshl_or_b32 v5, v5, 31, v4
Cvaa 188,144,48,236
X 162
Mvaa 176,148,4
Ra 144,242,28672
K 196,160,161
Mvaa 180,148,20
s_lshl_b32 s19, s85, 7
X 163
Mvaa 184,148,36
X 164
Mvaa 188,148,52
s_add_u32 s19, s19, 2752512
K 197,162,163
Mvaa 176,152,8
X 165
Mvaa 180,152,24
s_and_b32 s81, s20, 134217728
Ra 148,242,29696
Mvaa 184,152,40
X 166
Mvaa 188,152,56
K 204,164,165
Mvaa 176,156,12
s_or_b32 s19, s19, s81
X 167
Mvaa 180,156,28
Ra 152,242,30720
Mvaa 184,156,44
X 168
Mvaa 188,156,60
W 3
buffer_load_dwordx4 a[64:67], v4, s[8:11], s19 offen sc1
K 205,166,167
Mvav 32,128,192
s_add_u32 s19, s19, 163840
X 169
Mvav 64,128,200
Ra 156,242,31744
Mvav 96,128,208
X 170
Mvav 128,128,216
buffer_load_dwordx4 a[68:71], v4, s[8:11], s19 offen sc1
K 212,168,169
Mvav 132,132,216
X 171
Mvav 100,132,208
s_add_u32 s19, s19, 163840
X 172
Mvav 68,132,200
Mvav 36,132,192
buffer_load_dwordx4 a[72:75], v4, s[8:11], s19 offen sc1
K 213,170,171
Mvav 40,136,192
X 173
Mvav 72,136,200
X 174
Mvav 104,136,208
s_add_u32 s19, s19, 163840
Mvav 136,136,216
K 220,172,173
Mvav 140,140,216
X 175
Mvav 108,140,208
buffer_load_dwordx4 a[76:79], v4, s[8:11], s19 offen sc1
Mvav 76,140,200
s_waitcnt vmcnt(5) lgkmcnt(0)
s_barrier
v_xor_b32 v242, 0x10000, v242
Mvav 44,140,192
s_add_u32 s19, s19, 163840
X 176
Mvav 48,144,192
Ra 128,242,0
K 221,174,175
Mvav 80,144,200
X 177
Mvav 112,144,208
buffer_load_dwordx4 a[80:83], v4, s[8:11], s19 offen sc1
Ra 132,242,1024
Mvav 144,144,216
s_add_u32 s19, s19, 163840
X 178
Mvav 148,148,216
K 198,176,177
Mvav 116,148,208
X 179
Mvav 84,148,200
buffer_load_dwordx4 a[84:87], v4, s[8:11], s19 offen sc1
Ra 136,242,2048
Mvav 52,148,192
X 180
Mvav 56,152,192
s_add_u32 s19, s19, 163840
Ra 140,242,3072
K 199,178,179
Mvav 88,152,200
X 181
Mvav 120,152,208
X 182
Mvav 152,152,216
buffer_load_dwordx4 a[88:91], v5, s[8:11], s19 offen sc1
K 206,180,181
Mvav 156,156,216
X 183
Mvav 124,156,208
Mvav 92,156,200
K 207,182,183
Mvav 60,156,192
W 0
Cvaa 160,128,0,224
Ra 144,242,4096
Ra 148,242,5120
Cvaa 164,128,16,228
s_add_u32 m0, s62, 0
Ra 152,242,6144
Cvaa 168,128,32,232
X 184
Cvaa 172,128,48,236
X 185
Mvaa 160,132,4
X 186
Mvaa 164,132,20
Ra 156,242,7168
K 214,184,185
Mvaa 168,132,36
Dc 1,8,61,0
Mvaa 172,132,52
X 187
Mvaa 160,136,8
X 188
Mvaa 164,136,24
Ra 128,242,16384
K 215,186,187
Mvaa 168,136,40
Dc 1,8,61,1024
Mvaa 172,136,56
X 189
Mvaa 160,140,12
X 190
Mvaa 164,140,28
Ra 132,242,17408
K 222,188,189
Mvaa 168,140,44
Dc 1,8,61,2048
Mvaa 172,140,60
X 191
v_smfmac_f32_16x16x64_bf16 a[160:163], a[176:179], v[192:199], v240
Ra 136,242,18432
K 223,190,191
v_smfmac_f32_16x16x64_bf16 a[164:167], a[176:179], v[200:207], v240
X 160
v_smfmac_f32_16x16x64_bf16 a[168:171], a[176:179], v[208:215], v240
Dc 1,8,61,3072
v_smfmac_f32_16x16x64_bf16 a[172:175], a[176:179], v[216:223], v240
W 3
X 161
Cvaa 176,144,0,224
s_add_u32 m0, s62, 4096
X 162
Cvaa 180,144,16,228
Ra 140,242,19456
K 192,160,161
Cvaa 184,144,32,232
X 163
Cvaa 188,144,48,236
Dc 243,8,61,0
Mvaa 176,148,4
X 164
Mvaa 180,148,20
Ra 144,242,20480
K 193,162,163
Mvaa 184,148,36
X 165
Mvaa 188,148,52
Dc 243,8,61,1024
Mvaa 176,152,8
X 166
Mvaa 180,152,24
Ra 148,242,21504
K 200,164,165
Mvaa 184,152,40
X 167
Mvaa 188,152,56
Dc 243,8,61,2048
Mvaa 176,156,12
X 168
Mvaa 180,156,28
Ra 152,242,22528
K 201,166,167
Mvaa 184,156,44
X 169
Mvaa 188,156,60
W 3
Dc 243,8,61,3072
Mvav 32,128,196
s_add_u32 m0, s62, 8192
X 170
Mvav 64,128,204
Ra 156,242,23552
K 208,168,169
Mvav 96,128,212
X 171
Mvav 128,128,220
Dc 244,8,61,0
Mvav 132,132,220
X 172
Mvav 100,132,212
Ra 128,242,8192
K 209,170,171
Mvav 68,132,204
X 173
Mvav 36,132,196
Dc 244,8,61,1024
Mvav 40,136,196
X 174
Mvav 72,136,204
Ra 132,242,9216
K 216,172,173
Mvav 104,136,212
X 175
Mvav 136,136,220
Dc 244,8,61,2048
Mvav 140,140,220
s_bfe_u32 s81, s59, 0x10001
X 176
Mvav 108,140,212
s_lshl_b32 s82, s57, 1
Ra 136,242,10240
K 217,174,175
Mvav 76,140,204
s_add_u32 s81, s81, s82
X 177
Mvav 44,140,196
W 3
s_lshl_b32 s83, s81, 2
Dc 244,8,61,3072
Mvav 48,144,196
s_lshr_b32 s81, s59, 2
X 178
Mvav 80,144,204
s_add_u32 m0, s62, 12288
s_lshl_b32 s81, s81, 1
Ra 140,242,11264
Mvav 112,144,212
s_add_u32 s83, s83, s81
K 194,176,177
Mvav 144,144,220
s_and_b32 s81, s59, 1
X 179
Mvav 148,148,220
s_add_u32 s83, s83, s81
X 180
Mvav 116,148,212
s_and_b32 s81, s14, 1
Dc 245,8,61,0
Ra 144,242,12288
Mvav 84,148,204
s_mul_i32 s81, s81, 124
K 195,178,179
Mvav 52,148,196
s_xor_b32 s83, s83, s81
X 181
Mvav 56,152,196
s_mul_i32 s83, s83, 20480
X 182
Mvav 88,152,204
s_mul_i32 s81, s58, 320
Dc 245,8,61,1024
Ra 148,242,13312
Mvav 120,152,212
s_add_u32 s82, s83, s81
K 202,180,181
)"
R"(Mvav 152,152,220
X 183
Mvav 156,156,220
Ra 152,242,14336
v_bfe_u32 v246, v1, 4, 4
Mvav 124,156,212
Dc 245,8,61,2048
v_mul_u32_u24 v246, 1280, v246
Mvav 92,156,204
X 184
Mvav 60,156,196
W 3
v_lshrrev_b32 v247, 8, v1
K 203,182,183
Cvaa 160,128,0,224
v_mul_u32_u24 v247, 104, v247
v_add3_u32 v246, v246, v247, s82
Cvaa 164,128,16,228
X 185
Cvaa 168,128,32,232
Dc 245,8,61,3072
Ra 156,242,15360
Cvaa 172,128,48,236
s_add_u32 s61, s61, s63
X 186
Mvaa 160,132,4
Ra 128,242,24576
K 210,184,185
Mvaa 164,132,20
X 187
Mvaa 168,132,36
s_xor_b32 s62, s62, 0x10000
X 188
Mvaa 172,132,52
global_load_dword v3, v246, s[16:17]
K 211,186,187
Mvaa 160,136,8
s_lshl_b32 s81, s81, 3
s_add_u32 s83, s83, s81
s_add_u32 s83, s83, 131072
Mvaa 164,136,24
s_add_u32 s64, s64, 1
v_lshrrev_b32 v246, 9, v1
v_mul_u32_u24 v246, 10240, v246
Mvaa 168,136,40
v_and_b32 v247, 0x1f0, v1
v_min_u32 v247, 304, v247
Mvaa 172,136,56
X 189
Mvaa 160,140,12
X 190
Mvaa 164,140,28
Ra 132,242,25600
v_lshl_add_u32 v246, v247, 3, v246
Mvaa 168,140,44
buffer_load_dword v3, v246, s[8:11], s83 offen sc1
K 218,188,189
Mvaa 172,140,60
W 2
X 191
Cvaa 176,144,0,224
s_lshl_b32 s94, s58, 2
Ra 136,242,26624
Ra 140,242,27648
Cvaa 180,144,16,228
X 160
Cvaa 184,144,32,232
s_add_u32 s94, s94, 0
K 219,190,191
Cvaa 188,144,48,236
X 161
Mvaa 176,148,4
X 162
Mvaa 180,148,20
s_and_b32 s94, s94, 15
Ra 144,242,28672
K 196,160,161
Mvaa 184,148,36
X 163
Mvaa 188,148,52
X 164
Mvaa 176,152,8
s_mul_i32 s94, s94, 10240
Ra 148,242,29696
K 197,162,163
Mvaa 180,152,24
X 165
Mvaa 184,152,40
X 166
Mvaa 188,152,56
s_add_u32 s94, s94, s48
K 204,164,165
Mvaa 176,156,12
X 167
Mvaa 180,156,28
Ra 152,242,30720
Mvaa 184,156,44
s_lshl_b32 s94, s94, 2
X 168
Mvaa 188,156,60
W 3
K 205,166,167
Mvav 32,128,192
X 169
Mvav 64,128,200
s_mul_i32 s95, s51, 0
Ra 156,242,31744
Mvav 96,128,208
X 170
Mvav 128,128,216
K 212,168,169
Mvav 132,132,216
s_add_u32 s95, s95, s48
X 171
Mvav 100,132,208
X 172
Mvav 68,132,200
Mvav 36,132,192
s_add_u32 m0, s95, 133120
K 213,170,171
Mvav 40,136,192
X 173
Mvav 72,136,200
X 174
Mvav 104,136,208
D 16,40,94,0
Mvav 136,136,216
K 220,172,173
Mvav 140,140,216
X 175
Mvav 108,140,208
X 176
Mvav 76,140,200
s_waitcnt vmcnt(3) lgkmcnt(0)
s_barrier
v_xor_b32 v242, 0x10000, v242
Mvav 44,140,192
D 17,40,94,1024
K 221,174,175
Mvav 48,144,192
Ra 128,242,0
Mvav 80,144,200
X 177
Mvav 112,144,208
X 178
Mvav 144,144,216
D 18,40,94,2048
Ra 132,242,1024
Mvav 148,148,216
K 198,176,177
Mvav 116,148,208
X 179
Mvav 84,148,200
X 180
Mvav 52,148,192
D 19,40,94,3072
Ra 136,242,2048
Mvav 56,152,192
s_add_u32 m0, s95, 137216
Ra 140,242,3072
K 199,178,179
Mvav 88,152,200
X 181
Mvav 120,152,208
X 182
Mvav 152,152,216
buffer_load_dwordx4 v20, s[40:43], s94 offen lds
K 206,180,181
Mvav 156,156,216
X 183
Mvav 124,156,208
Mvav 92,156,200
K 207,182,183
Mvav 60,156,192
W 0
Cvaa 160,128,0,224
Ra 144,242,4096
Ra 148,242,5120
Cvaa 164,128,16,228
s_add_u32 m0, s62, 0
Ra 152,242,6144
G 8,64
Cvaa 168,128,32,232
X 184
Cvaa 172,128,48,236
X 185
Mvaa 160,132,4
G 9,65
G 10,66
Mvaa 164,132,20
X 186
Mvaa 168,132,36
Ra 156,242,7168
K 214,184,185
Mvaa 172,132,52
Dc 1,8,61,0
G 11,67
Mvaa 160,136,8
X 187
Mvaa 164,136,24
X 188
Mvaa 168,136,40
Ra 128,242,16384
G 12,68
Mvaa 172,136,56
Dc 1,8,61,1024
G 13,69
Mvaa 160,140,12
K 215,186,187
G 14,70
Mvaa 164,140,28
X 189
Mvaa 168,140,44
X 190
Mvaa 172,140,60
Dc 1,8,61,2048
G 15,71
v_smfmac_f32_16x16x64_bf16 a[160:163], a[176:179], v[192:199], v240
Ra 132,242,17408
K 222,188,189
v_smfmac_f32_16x16x64_bf16 a[164:167], a[176:179], v[200:207], v240
X 191
s_nop 0
K 223,190,191
v_smfmac_f32_16x16x64_bf16 a[168:171], a[176:179], v[208:215], v240
A 8,8,12
A 9,9,13
v_smfmac_f32_16x16x64_bf16 a[172:175], a[176:179], v[216:223], v240
W 2
Dc 1,8,61,3072
Ra 136,242,18432
Cvaa 176,144,0,224
s_add_u32 m0, s62, 4096
X 160
Cvaa 180,144,16,228
A 10,10,14
A 11,11,15
Cvaa 184,144,32,232
X 161
Cvaa 188,144,48,236
X 162
Mvaa 176,148,4
Ra 140,242,19456
G 12,72
Mvaa 180,148,20
K 192,160,161
G 13,73
Mvaa 184,148,36
X 163
Mvaa 188,148,52
Dc 243,8,61,0
G 14,74
Mvaa 176,152,8
X 164
Mvaa 180,152,24
Ra 144,242,20480
G 15,75
Mvaa 184,152,40
K 193,162,163
A 8,8,12
Mvaa 188,152,56
Dc 243,8,61,1024
A 9,9,13
Mvaa 176,156,12
X 165
Mvaa 180,156,28
X 166
Mvaa 184,156,44
Ra 148,242,21504
K 200,164,165
Mvaa 188,156,60
W 2
Dc 243,8,61,2048
A 10,10,14
Mvav 32,128,196
X 167
Mvav 64,128,204
Ra 152,242,22528
A 11,11,15
Mvav 96,128,212
X 168
Mvav 128,128,220
Dc 243,8,61,3072
G 12,76
Mvav 132,132,220
s_add_u32 m0, s62, 8192
K 201,166,167
G 13,77
Mvav 100,132,212
X 169
Mvav 68,132,204
X 170
Mvav 36,132,196
Ra 156,242,23552
G 14,78
Mvav 40,136,196
K 208,168,169
G 15,79
Mvav 72,136,204
X 171
Mvav 104,136,212
Dc 244,8,61,0
A 8,8,12
Mvav 136,136,220
X 172
Mvav 140,140,220
Ra 128,242,8192
A 9,9,13
Mvav 108,140,212
K 209,170,171
A 10,10,14
Mvav 76,140,204
X 173
Mvav 44,140,196
W 1
Dc 244,8,61,1024
A 11,11,15
Mvav 48,144,196
X 174
Mvav 80,144,204
Ra 132,242,9216
G 12,80
Mvav 112,144,212
K 216,172,173
G 13,81
Mvav 144,144,220
Dc 244,8,61,2048
Ra 136,242,10240
Mvav 148,148,220
X 175
Mvav 116,148,212
X 176
Mvav 84,148,204
G 14,82
K 217,174,175
Mvav 52,148,196
Dc 244,8,61,3072
G 15,83
Mvav 56,152,196
s_add_u32 m0, s62, 12288
X 177
Mvav 88,152,204
Ra 140,242,11264
A 8,8,12
Mvav 120,152,212
X 178
Mvav 152,152,220
A 9,9,13
K 194,176,177
Mvav 156,156,220
X 179
Mvav 124,156,212
Dc 245,8,61,0
A 10,10,14
Mvav 92,156,204
X 180
Mvav 60,156,196
W 0
Ra 144,242,12288
A 11,11,15
Cvaa 160,128,0,224
s_andn2_b32 s81, s49, 262144
K 195,178,179
G 12,84
Cvaa 164,128,16,228
s_lshl_b32 s82, s58, 12
X 181
Cvaa 168,128,32,232
s_add_u32 s81, s81, s82
Dc 245,8,61,1024
G 13,85
Cvaa 172,128,48,236
s_cmp_lt_u32 s57, 6
s_cselect_b32 s81, s81, 79691776
X 182
Mvaa 160,132,4
Ra 148,242,13312
G 14,86
Mvaa 164,132,20
K 202,180,181
G 15,87
Mvaa 168,132,36
X 183
Mvaa 172,132,52
Dc 245,8,61,2048
Ra 152,242,14336
Mvaa 160,136,8
X 184
Mvaa 164,136,24
A 8,8,12
K 203,182,183
Mvaa 168,136,40
Ra 156,242,15360
A 9,9,13
Mvaa 172,136,56
X 185
Mvaa 160,140,12
Dc 245,8,61,3072
A 10,10,14
Mvaa 164,140,28
)"
R"(s_add_u32 s61, s61, s63
s_xor_b32 s62, s62, 0x10000
s_add_u32 s64, s64, 1
Mvaa 168,140,44
X 186
Mvaa 172,140,60
W 0
A 11,11,15
K 210,184,185
Cvaa 176,144,0,224
X 187
Cvaa 180,144,16,228
X 188
K 211,186,187
Cvaa 184,144,32,232
X 189
X 190
K 218,188,189
X 191
X 160
K 219,190,191
Cvaa 188,144,48,236
G 12,88
v_lshrrev_b32 v246, 9, v1
Mvaa 176,148,4
Ra 128,242,24576
v_and_b32 v247, 0x1f0, v1
Mvaa 180,148,20
G 13,89
v_lshlrev_b32 v247, 3, v247
Mvaa 184,148,36
G 14,90
v_lshl_add_u32 v246, v246, 17, v247
Mvaa 188,148,52
buffer_load_dword v3, v246, s[8:11], s81 offen sc1
G 15,91
Mvaa 176,152,8
s_add_u32 s81, s81, 262144
Ra 132,242,25600
A 8,8,12
Mvaa 180,152,24
Ra 136,242,26624
A 9,9,13
Mvaa 184,152,40
Ra 140,242,27648
A 10,10,14
Mvaa 188,152,56
buffer_load_dword v3, v246, s[8:11], s81 offen sc1
A 11,11,15
Mvaa 176,156,12
s_lshl_b32 s94, s58, 2
X 161
Mvaa 180,156,28
s_add_u32 s94, s94, 1
v_add_f32_dpp v8, v8, v8 row_ror:8 row_mask:0xf bank_mask:0xf
v_add_f32_dpp v9, v9, v9 row_ror:8 row_mask:0xf bank_mask:0xf
Mvaa 184,156,44
s_and_b32 s94, s94, 15
X 162
Mvaa 188,156,60
W 0
K 196,160,161
v_add_f32_dpp v10, v10, v10 row_ror:8 row_mask:0xf bank_mask:0xf
Mvav 32,128,192
s_mul_i32 s94, s94, 10240
X 163
Mvav 64,128,200
s_add_u32 s94, s94, s48
Ra 144,242,28672
v_add_f32_dpp v11, v11, v11 row_ror:8 row_mask:0xf bank_mask:0xf
Mvav 96,128,208
s_lshl_b32 s94, s94, 2
X 164
Mvav 128,128,216
s_mul_i32 s95, s51, 0
v_mov_b32 v12, v8
K 197,162,163
Mvav 132,132,216
s_add_u32 s95, s95, s48
X 165
Mvav 100,132,208
Ra 148,242,29696
v_mov_b32 v13, v9
Mvav 68,132,200
X 166
Mvav 36,132,192
s_add_u32 m0, s95, 143360
v_mov_b32 v14, v10
K 204,164,165
Mvav 40,136,192
Ra 152,242,30720
v_mov_b32 v15, v11
Mvav 72,136,200
X 167
Mvav 104,136,208
Ra 156,242,31744
v_permlane16_swap_b32 v8, v12
Mvav 136,136,216
X 168
Mvav 140,140,216
v_permlane16_swap_b32 v9, v13
K 205,166,167
Mvav 108,140,208
X 169
Mvav 76,140,200
s_waitcnt vmcnt(2) lgkmcnt(0)
s_barrier
v_xor_b32 v242, 0x10000, v242
Mvav 44,140,192
v_permlane16_swap_b32 v10, v14
v_permlane16_swap_b32 v11, v15
Mvav 48,144,192
X 170
Mvav 80,144,200
K 212,168,169
A 8,8,12
Mvav 112,144,208
X 171
Mvav 144,144,216
A 9,9,13
A 10,10,14
Mvav 148,148,216
X 172
Mvav 116,148,208
K 213,170,171
A 11,11,15
Mvav 84,148,200
X 173
Mvav 52,148,192
X 174
Mvav 56,152,192
D 16,40,94,0
v_mov_b32 v12, v8
Mvav 88,152,200
K 220,172,173
v_mov_b32 v13, v9
Mvav 120,152,208
X 175
Mvav 152,152,216
v_mov_b32 v14, v10
v_mov_b32 v15, v11
Mvav 156,156,216
D 17,40,94,1024
Ra 128,242,0
Mvav 124,156,208
X 176
Mvav 92,156,200
K 221,174,175
v_permlane32_swap_b32 v8, v12
Mvav 60,156,192
D 18,40,94,2048
Ra 132,242,1024
D 19,40,94,3072
s_add_u32 m0, s95, 147456
Ra 136,242,2048
Ra 140,242,3072
buffer_load_dwordx4 v20, s[40:43], s94 offen lds
X 177
v_permlane32_swap_b32 v9, v13
X 178
v_permlane32_swap_b32 v10, v14
K 198,176,177
X 179
v_permlane32_swap_b32 v11, v15
X 180
A 8,8,12
K 199,178,179
A 9,9,13
X 181
A 10,10,14
X 182
A 11,11,15
K 206,180,181
X 183
s_nop 0
K 207,182,183
W 0
Cvaa 160,128,0,224
Ra 144,242,4096
Ra 148,242,5120
Cvaa 164,128,16,228
s_add_u32 m0, s62, 0
Ra 152,242,6144
v_and_b32 v7, 0xffff0000, v7
Cvaa 168,128,32,232
X 184
Cvaa 172,128,48,236
X 185
Mvaa 160,132,4
Ra 156,242,7168
v_ashrrev_i32 v6, 31, v7
Mvaa 164,132,20
X 186
Mvaa 168,132,36
K 214,184,185
v_not_b32 v6, v6
Mvaa 172,132,52
Dc 1,8,61,0
v_or_b32 v6, 0x80000000, v6
Mvaa 160,136,8
X 187
Mvaa 164,136,24
X 188
Mvaa 168,136,40
Ra 128,242,16384
v_xor_b32 v7, v7, v6
Mvaa 172,136,56
Dc 1,8,61,1024
K 215,186,187
Mvaa 160,140,12
X 189
Mvaa 164,140,28
Ra 132,242,17408
v_and_b32 v7, 0xffff0000, v7
Mvaa 168,140,44
X 190
Mvaa 172,140,60
Dc 1,8,61,2048
v_mul_f32 v7, 0xbfb8aa3b, v7
v_smfmac_f32_16x16x64_bf16 a[160:163], a[176:179], v[192:199], v240
Ra 136,242,18432
K 222,188,189
v_smfmac_f32_16x16x64_bf16 a[164:167], a[176:179], v[200:207], v240
X 191
s_nop 0
K 223,190,191
v_smfmac_f32_16x16x64_bf16 a[168:171], a[176:179], v[208:215], v240
X 7
v_smfmac_f32_16x16x64_bf16 a[172:175], a[176:179], v[216:223], v240
W 3
Dc 1,8,61,3072
v_add_f32 v7, 1.0, v7
Cvaa 176,144,0,224
s_add_u32 m0, s62, 4096
X 160
Cvaa 180,144,16,228
X 161
Cvaa 184,144,32,232
X 162
Cvaa 188,144,48,236
Ra 140,242,19456
K 192,160,161
Mvaa 176,148,4
v_rcp_f32 v7, v7
Mvaa 180,148,20
X 163
Mvaa 184,148,36
Dc 243,8,61,0
Ra 144,242,20480
Mvaa 188,148,52
X 164
Mvaa 176,152,8
K 7,7,7
K 193,162,163
Mvaa 180,152,24
X 165
Mvaa 184,152,40
Dc 243,8,61,1024
v_lshlrev_b32 v7, 16, v7
Mvaa 188,152,56
X 166
Mvaa 176,156,12
Ra 148,242,21504
K 12,8,9
Mvaa 180,156,28
K 200,164,165
K 13,10,11
Mvaa 184,156,44
Dc 243,8,61,2048
Ra 152,242,22528
Mvaa 188,156,60
W 3
X 167
Mvav 32,128,196
X 168
Mvav 64,128,204
v_lshlrev_b32 v8, 16, v12
K 201,166,167
Mvav 96,128,212
Dc 243,8,61,3072
v_and_b32 v9, 0xffff0000, v12
Mvav 128,128,220
s_add_u32 m0, s62, 8192
X 169
Mvav 132,132,220
X 170
Mvav 100,132,212
Ra 156,242,23552
v_lshlrev_b32 v10, 16, v13
Mvav 68,132,204
K 208,168,169
v_and_b32 v11, 0xffff0000, v13
Mvav 36,132,196
X 171
Mvav 40,136,196
Dc 244,8,61,0
Ra 128,242,8192
Mvav 72,136,204
X 172
Mvav 104,136,212
U 8,8,7
K 209,170,171
Mvav 136,136,220
X 173
Mvav 140,140,220
Dc 244,8,61,1024
U 9,9,7
Mvav 108,140,212
X 174
Mvav 76,140,204
Ra 132,242,9216
U 10,10,7
Mvav 44,140,196
W 2
K 216,172,173
U 11,11,7
Mvav 48,144,196
Dc 244,8,61,2048
Ra 136,242,10240
Mvav 80,144,204
X 175
Mvav 112,144,212
X 176
Mvav 144,144,220
K 12,8,9
K 217,174,175
Mvav 148,148,220
Dc 244,8,61,3072
K 14,10,11
Mvav 116,148,212
s_add_u32 m0, s62, 12288
X 177
Mvav 84,148,204
X 178
Mvav 52,148,196
Ra 140,242,11264
v_mov_b32 v13, s13
Mvav 56,152,196
K 194,176,177
v_mov_b32 v15, s13
Mvav 88,152,204
X 179
Mvav 120,152,212
X 180
Mvav 152,152,220
Dc 245,8,61,0
Ra 144,242,12288
Mvav 156,156,220
v_lshrrev_b32 v6, 7, v1
K 195,178,179
Mvav 124,156,212
X 181
Mvav 92,156,204
Ra 148,242,13312
v_lshl_or_b32 v6, v6, 28, v1
Mvav 60,156,196
W 2
s_lshl_b32 s19, s85, 7
Dc 245,8,61,1024
Cvaa 160,128,0,224
s_add_u32 s19, s19, 98304
X 182
Cvaa 164,128,16,228
s_and_b32 s81, s20, 134217728
Ra 152,242,14336
K 202,180,181
Cvaa 168,128,32,232
s_or_b32 s19, s19, s81
X 183
Cvaa 172,128,48,236
Dc 245,8,61,2048
Ra 156,242,15360
Mvaa 160,132,4
X 184
Mvaa 164,132,20
Ra 128,242,24576
K 203,182,183
Mvaa 168,132,36
X 185
Mvaa 172,132,52
Dc 245,8,61,3072
Mvaa 160,136,8
s_add_u32 s61, s61, s63
X 186
Mvaa 164,136,24
s_xor_b32 s62, s62, 0x10000
Ra 132,242,25600
K 210,184,185
Mvaa 168,136,40
s_add_u32 s64, s64, 1
buffer_store_dwordx4 v[12:15], v6, s[8:11], s19 offen sc1
Mvaa 172,136,56
X 187
Mvaa 160,140,12
X 188
Mvaa 164,140,28
Ra 136,242,26624
K 211,186,187
Mvaa 168,140,44
s_lshl_b32 s94, s58, 2
)"
R"(X 189
Mvaa 172,140,60
W 3
X 190
Cvaa 176,144,0,224
Ra 140,242,27648
K 218,188,189
Cvaa 180,144,16,228
X 191
Cvaa 184,144,32,232
X 160
K 219,190,191
Cvaa 188,144,48,236
s_add_u32 s94, s94, 2
X 161
Mvaa 176,148,4
X 162
Mvaa 180,148,20
s_and_b32 s94, s94, 15
Ra 144,242,28672
K 196,160,161
Mvaa 184,148,36
X 163
Mvaa 188,148,52
X 164
Mvaa 176,152,8
s_mul_i32 s94, s94, 10240
Ra 148,242,29696
K 197,162,163
Mvaa 180,152,24
X 165
Mvaa 184,152,40
X 166
Mvaa 188,152,56
s_add_u32 s94, s94, s48
K 204,164,165
Mvaa 176,156,12
X 167
Mvaa 180,156,28
Ra 152,242,30720
Mvaa 184,156,44
s_lshl_b32 s94, s94, 2
X 168
Mvaa 188,156,60
W 3
K 205,166,167
Mvav 32,128,192
X 169
Mvav 64,128,200
s_mul_i32 s95, s51, 0
Ra 156,242,31744
Mvav 96,128,208
X 170
Mvav 128,128,216
K 212,168,169
Mvav 132,132,216
s_add_u32 s95, s95, s48
X 171
Mvav 100,132,208
X 172
Mvav 68,132,200
Mvav 36,132,192
s_add_u32 m0, s95, 153600
K 213,170,171
Mvav 40,136,192
X 173
Mvav 72,136,200
X 174
Mvav 104,136,208
D 16,40,94,0
Mvav 136,136,216
K 220,172,173
Mvav 140,140,216
X 175
Mvav 108,140,208
X 176
Mvav 76,140,200
s_waitcnt vmcnt(2) lgkmcnt(0)
s_barrier
v_xor_b32 v242, 0x10000, v242
Mvav 44,140,192
D 17,40,94,1024
K 221,174,175
Mvav 48,144,192
Ra 128,242,0
Mvav 80,144,200
X 177
Mvav 112,144,208
X 178
Mvav 144,144,216
D 18,40,94,2048
Ra 132,242,1024
Mvav 148,148,216
K 198,176,177
Mvav 116,148,208
X 179
Mvav 84,148,200
X 180
Mvav 52,148,192
D 19,40,94,3072
Ra 136,242,2048
Mvav 56,152,192
s_add_u32 m0, s95, 157696
Ra 140,242,3072
K 199,178,179
Mvav 88,152,200
X 181
Mvav 120,152,208
X 182
Mvav 152,152,216
buffer_load_dwordx4 v20, s[40:43], s94 offen lds
K 206,180,181
Mvav 156,156,216
X 183
Mvav 124,156,208
Mvav 92,156,200
K 207,182,183
Mvav 60,156,192
.Lf1_39%=:
W 0
Cvaa 160,128,0,224
Ra 144,242,4096
Ra 148,242,5120
Cvaa 164,128,16,228
Ra 152,242,6144
v_bfe_u32 v28, v1, 4, 2
Cvaa 168,128,32,232
X 184
Cvaa 172,128,48,236
X 185
Mvaa 160,132,4
Ra 156,242,7168
v_lshlrev_b32 v28, 3, v28
Mvaa 164,132,20
X 186
Mvaa 168,132,36
buffer_load_dwordx2 v[28:29], v28, s[8:11], s54 offen sc1
K 214,184,185
Mvaa 172,132,52
s_lshl_b32 s94, s58, 2
X 187
Mvaa 160,136,8
s_add_u32 s94, s94, s51
X 188
Mvaa 164,136,24
s_add_u32 s94, s94, 3
Ra 128,242,16384
K 215,186,187
Mvaa 168,136,40
s_and_b32 s94, s94, 15
X 189
Mvaa 172,136,56
X 190
Mvaa 160,140,12
s_mul_i32 s94, s94, 10240
Ra 132,242,17408
K 222,188,189
Mvaa 164,140,28
s_add_u32 s94, s94, s48
X 191
Mvaa 168,140,44
K 223,190,191
Mvaa 172,140,60
s_lshl_b32 s94, s94, 2
v_smfmac_f32_16x16x64_bf16 a[160:163], a[176:179], v[192:199], v240
s_mul_i32 s95, s51, 10240
Ra 136,242,18432
v_smfmac_f32_16x16x64_bf16 a[164:167], a[176:179], v[200:207], v240
X 160
v_smfmac_f32_16x16x64_bf16 a[168:171], a[176:179], v[208:215], v240
s_add_u32 s95, s95, s48
X 161
v_smfmac_f32_16x16x64_bf16 a[172:175], a[176:179], v[216:223], v240
W 3
X 162
Cvaa 176,144,0,224
s_add_u32 m0, s95, 0
Ra 140,242,19456
K 192,160,161
Cvaa 180,144,16,228
X 163
Cvaa 184,144,32,232
X 164
Cvaa 188,144,48,236
D 16,40,94,0
K 193,162,163
Mvaa 176,148,4
X 165
Mvaa 180,148,20
X 166
Mvaa 184,148,36
D 17,40,94,1024
K 200,164,165
Mvaa 188,148,52
X 167
Mvaa 176,152,8
Ra 144,242,20480
Ra 148,242,21504
Mvaa 180,152,24
D 18,40,94,2048
Mvaa 184,152,40
X 172
Mvaa 188,152,56
K 201,166,167
Mvaa 176,156,12
D 19,40,94,3072
Ra 152,242,22528
Mvaa 180,156,28
s_add_u32 m0, s95, 4096
X 173
Mvaa 184,156,44
X 174
Mvaa 188,156,60
s_cmp_eq_u32 s60, 16
s_cbranch_scc1 .Lmk_74%=
.Lmkr_75%=:
W 3
Mvav 32,128,196
buffer_load_dwordx4 v20, s[40:43], s94 offen lds
K 216,172,173
Mvav 64,128,204
s_lshl_b32 s94, s58, 2
X 175
Mvav 96,128,212
s_add_u32 s94, s94, s51
s_add_u32 s94, s94, 5
s_and_b32 s94, s94, 15
Mvav 128,128,220
s_mul_i32 s94, s94, 10240
X 168
Mvav 132,132,220
s_add_u32 s94, s94, s48
Ra 156,242,23552
K 217,174,175
Mvav 100,132,212
s_lshl_b32 s94, s94, 2
X 169
Mvav 68,132,204
s_mov_b32 s18, 40960
X 170
Mvav 36,132,196
s_mul_i32 s95, s51, 10240
s_mov_b32 s19, 73728
K 208,168,169
Mvav 40,136,196
s_bitcmp1_b32 s14, 0
s_cselect_b32 s18, 49152, s18
s_cselect_b32 s19, 98304, s19
X 171
Mvav 72,136,204
s_add_u32 s95, s95, s48
X 176
Mvav 104,136,212
s_cmp_lt_u32 s14, 2
s_cselect_b32 s20, s18, s19
s_cselect_b32 s21, s19, s18
Ra 128,242,24576
K 209,170,171
Mvav 136,136,220
s_add_u32 m0, s95, 20480
X 177
Mvav 140,140,220
X 178
Mvav 108,140,212
K 194,176,177
v_add_u32 v2, s20, v1
Mvav 76,140,204
X 179
Mvav 44,140,196
W 1
X 180
Mvav 48,144,196
D 16,40,94,0
v_add_u32 v3, s21, v1
Mvav 80,144,204
s_lshl_b32 s18, s14, 9
Ra 132,242,25600
K 195,178,179
Mvav 112,144,212
X 181
Mvav 144,144,220
D 17,40,94,1024
v_lshrrev_b32 v232, 1, v1
Mvav 148,148,220
X 182
Mvav 116,148,212
v_add_u32 v232, s18, v232
K 202,180,181
Mvav 84,148,204
D 18,40,94,2048
v_bfe_u32 v12, v1, 6, 2
Mvav 52,148,196
X 183
Mvav 56,152,196
Ra 136,242,26624
v_lshrrev_b32 v13, 8, v1
Mvav 88,152,204
D 19,40,94,3072
v_bfe_u32 v14, v1, 4, 1
Mvav 120,152,212
s_add_u32 m0, s95, 24576
X 184
Mvav 152,152,220
K 203,182,183
v_lshlrev_b32 v14, 4, v14
Mvav 156,156,220
buffer_load_dwordx4 v20, s[40:43], s94 offen lds
v_mov_b32 v15, 0x3f80
Mvav 124,156,212
s_waitcnt vmcnt(10)
v_cmp_eq_u64 vcc, s[12:13], v[28:29]
s_cmp_eq_u64 vcc, -1
s_cselect_b32 s46, 79691776, 0
X 185
Mvav 92,156,204
s_add_u32 s81, s58, 0
Ra 140,242,27648
v_lshlrev_b32 v15, v14, v15
Mvav 60,156,196
s_and_b32 s81, s81, 3
X 186
Maav 160,180,192
K 210,184,185
X 187
X 188
K 211,186,187
Maav 164,180,200
X 189
X 190
K 218,188,189
X 191
s_nop 0
K 219,190,191
Maav 168,180,208
s_lshl_b32 s81, s81, 12
v_bfe_u32 v14, v1, 5, 1
v_xor_b32 v27, v12, v13
Maav 172,180,216
W 0
Ra 144,242,28672
v_lshl_or_b32 v27, v27, 1, v14
Mvav 32,128,192
s_add_u32 s81, s81, s49
v_cmp_eq_u32_e64 s[86:87], 0, v27
v_mov_b32 v226, 0
Mvav 64,128,200
s_add_u32 s82, s81, 131072
Ra 148,242,29696
v_cndmask_b32_e64 v224, 0, v15, s[86:87]
Mvav 96,128,208
buffer_load_dwordx4 a[184:187], v1, s[44:47], s81 offen offset:0 sc1
v_mov_b32 v228, 0
Mvav 128,128,216
v_mov_b32 v230, v224
v_cmp_eq_u32_e64 s[86:87], 1, v27
Mvav 132,132,216
Ra 152,242,30720
v_mov_b32 v227, 0
Mvav 100,132,208
buffer_load_dwordx4 a[200:203], v1, s[44:47], s82 offen offset:0 sc1
v_cndmask_b32_e64 v225, 0, v15, s[86:87]
Mvav 68,132,200
v_mov_b32 v229, 0
v_mov_b32 v231, v225
Mvav 36,132,192
Ra 156,242,31744
G 4,160
Mvav 40,136,192
buffer_load_dwordx4 a[188:191], v1, s[44:47], s81 offen offset:1024 sc1
G 8,161
Mvav 72,136,200
G 5,164
G 9,165
Mvav 104,136,208
G 6,168
G 10,169
Mvav 136,136,216
buffer_load_dwordx4 a[204:207], v1, s[44:47], s82 offen offset:1024 sc1
G 7,172
Mvav 140,140,216
G 11,173
A 4,4,8
Mvav 108,140,208
A 5,5,9
A 6,6,10
Mvav 76,140,200
buffer_load_dwordx4 a[192:195], v1, s[44:47], s81 offen offset:2048 sc1
A 7,7,11
Mvav 44,140,192
W 0
ds_write_b64 v232, v[6:7] offset:57344
A 8,4,5
Mvav 48,144,192
A 9,6,7
A 8,8,9
Mvav 80,144,200
buffer_load_dwordx4 a[208:211], v1, s[44:47], s82 offen offset:2048 sc1
v_cmp_ngt_f32_e64 s[86:87], s90, v8
Mvav 112,144,208
s_lshl_b32 s19, s14, 2
)"
R"(v_mov_b32 v9, s19
Mvav 144,144,216
s_or_b64 s[88:89], s[88:89], s[86:87]
s_and_b64 s[88:89], s[88:89], s[24:25]
s_cselect_b32 s18, 1, 0
v_mov_b32 v8, s18
Mvav 148,148,216
buffer_load_dwordx4 a[196:199], v1, s[44:47], s81 offen offset:3072 sc1
ds_write_b32 v9, v8 offset:59392
Mvav 116,148,208
v_xor_b32 v10, 1024, v232
v_bfe_u32 v11, v1, 2, 4
Mvav 84,148,200
K 96,96,97
K 97,98,99
Mvav 52,148,192
buffer_load_dwordx4 a[212:215], v1, s[44:47], s82 offen offset:3072 sc1
K 98,100,101
Mvav 56,152,192
s_add_u32 s81, s58, 1
s_and_b32 s81, s81, 3
s_lshl_b32 s81, s81, 12
Mvav 88,152,200
s_add_u32 s81, s81, s49
s_add_u32 s82, s81, 131072
K 99,102,103
Mvav 120,152,208
ds_write_b128 v2, v[96:99] offset:0
K 128,128,129
Mvav 152,152,216
K 129,130,131
K 130,132,133
Mvav 156,156,216
buffer_load_dwordx4 a[216:219], v1, s[44:47], s81 offen offset:0 sc1
K 131,134,135
Mvav 124,156,208
ds_write_b128 v2, v[128:131] offset:4096
K 104,104,105
Mvav 92,156,200
K 105,106,107
K 106,108,109
Mvav 60,156,192
buffer_load_dwordx4 a[232:235], v1, s[44:47], s82 offen offset:0 sc1
K 107,110,111
buffer_load_dwordx4 a[220:223], v1, s[44:47], s81 offen offset:1024 sc1
buffer_load_dwordx4 a[236:239], v1, s[44:47], s82 offen offset:1024 sc1
buffer_load_dwordx4 a[224:227], v1, s[44:47], s81 offen offset:2048 sc1
ds_write_b128 v2, v[104:107] offset:1024
K 136,136,137
K 137,138,139
K 138,140,141
K 139,142,143
ds_write_b128 v2, v[136:139] offset:5120
buffer_load_dwordx4 a[240:243], v1, s[44:47], s82 offen offset:2048 sc1
buffer_load_dwordx4 a[228:231], v1, s[44:47], s81 offen offset:3072 sc1
buffer_load_dwordx4 a[244:247], v1, s[44:47], s82 offen offset:3072 sc1
K 112,112,113
K 113,114,115
K 114,116,117
K 115,118,119
ds_write_b128 v2, v[112:115] offset:2048
K 144,144,145
K 145,146,147
K 146,148,149
K 147,150,151
ds_write_b128 v2, v[144:147] offset:6144
K 120,120,121
K 121,122,123
K 122,124,125
K 123,126,127
ds_write_b128 v2, v[120:123] offset:3072
K 152,152,153
K 153,154,155
K 154,156,157
K 155,158,159
ds_write_b128 v2, v[152:155] offset:7168
.Lend_42%=:
s_waitcnt vmcnt(8) lgkmcnt(0)
s_barrier
s_cmp_eq_u32 s46, 0
s_cbranch_scc1 .Llate_76%=
ds_read_b32 v11, v11 offset:59392
ds_read_b64 v[8:9], v10 offset:57344
Rv 176,3,0
Rv 180,3,1024
Rv 184,3,2048
Rv 188,3,3072
Rv 192,3,4096
Rv 196,3,5120
Rv 200,3,6144
Rv 204,3,7168
W 6
Mvvv 32,224,176
v_cmp_ne_u32 vcc, 0, v11
s_cmp_lg_u64 vcc, 0
s_cbranch_scc1 .Lredo%=
s_lshl_b32 s94, s58, 2
Mvvv 36,228,176
A 4,4,8
Mvvv 40,224,180
s_add_u32 s94, s94, s51
v_rcp_f32 v4, v4
Mvvv 44,228,180
W 4
s_add_u32 s94, s94, 7
A 5,5,9
Mvvv 48,224,184
Ta 128,25,10240
Ta 130,25,15360
Mvvv 52,228,184
s_and_b32 s94, s94, 15
v_rcp_f32 v5, v5
Mvvv 56,224,188
s_lshl_b32 s93, s58, 12
s_add_u32 s93, s93, s50
Ta 132,26,10272
Mvvv 60,228,188
W 5
s_mul_i32 s94, s94, 10240
Ta 134,26,15392
Mvvv 64,224,192
s_add_u32 s94, s94, s48
U 32,32,4
U 33,33,4
Mvvv 68,228,192
s_lshl_b32 s94, s94, 2
Ta 136,25,10304
U 34,34,4
Mvvv 72,224,196
s_mul_i32 s95, s51, 20480
Ta 138,25,15424
U 35,35,4
Mvvv 76,228,196
W 6
U 36,36,4
U 37,37,4
Mvvv 80,224,200
s_add_u32 s95, s95, s48
Ta 140,26,10336
Ta 142,26,15456
Mvvv 84,228,200
s_add_u32 m0, s95, 61440
U 38,38,4
U 39,39,4
Mvvv 88,224,204
Ta 144,25,10368
K 32,32,33
Mvvv 92,228,204
W 5
Ta 146,25,15488
K 33,34,35
Zvaa 96,128,184
K 34,36,37
K 35,38,39
Zvaa 104,128,200
buffer_store_dwordx4 v[32:35], v1, s[8:11], s93 offen offset:0 sc1
Ta 148,26,10400
Zvaa 108,132,200
Ta 150,26,15520
U 40,40,4
Zvaa 100,132,184
W 4
U 41,41,4
U 42,42,4
Zvaa 112,136,184
Ta 152,25,10432
Ta 154,25,15552
Zvaa 120,136,200
D 16,40,94,0
U 43,43,4
Zvaa 124,140,200
U 44,44,4
U 45,45,4
Zvaa 116,140,184
Ta 156,26,10464
Ta 158,26,15584
W 4
Zvaa 128,144,184
U 46,46,4
U 47,47,4
Zvaa 136,144,200
D 17,40,94,1024
K 40,40,41
Zvaa 140,148,200
Ta 128,25,10496
Ta 130,25,15616
Zvaa 132,148,184
Ta 132,26,10528
Ta 134,26,15648
W 4
Zvaa 144,152,184
K 41,42,43
K 42,44,45
Zvaa 152,152,200
D 18,40,94,2048
K 43,46,47
Zvaa 156,156,200
Ta 136,25,20480
Ta 138,25,25600
Zvaa 148,156,184
Ta 140,26,20512
Ta 142,26,25632
W 4
Zvaa 160,128,184
Ta 144,25,20544
Ta 146,25,25664
Zvaa 168,128,200
D 19,40,94,3072
Ta 148,26,20576
Zvaa 172,132,200
s_add_u32 m0, s95, 65536
Ta 150,26,25696
Ta 152,25,20608
Zvaa 164,132,184
W 5
Ta 154,25,25728
Ta 156,26,20640
Mvaa 96,136,188
Ta 158,26,25760
Ta 128,25,20672
Mvaa 104,136,204
buffer_load_dwordx4 v20, s[40:43], s94 offen lds
Ta 130,25,25792
Mvaa 108,140,204
s_lshl_b32 s94, s58, 2
s_add_u32 s94, s94, s51
s_add_u32 s94, s94, 9
Mvaa 100,140,188
W 6
s_and_b32 s94, s94, 15
s_mul_i32 s94, s94, 10240
Mvaa 112,144,188
s_add_u32 s94, s94, s48
s_lshl_b32 s94, s94, 2
s_mul_i32 s95, s51, 10240
Mvaa 120,144,204
s_add_u32 s95, s95, s48
buffer_store_dwordx4 v[40:43], v1, s[8:11], s93 offen offset:1024 sc1
U 48,48,4
Mvaa 124,148,204
s_add_u32 m0, s95, 112640
U 49,49,4
U 50,50,4
Mvaa 116,148,188
Ta 132,26,20704
Ta 134,26,25824
W 4
Mvaa 128,152,188
U 51,51,4
U 52,52,4
Mvaa 136,152,204
U 53,53,4
U 54,54,4
Mvaa 140,156,204
U 55,55,4
K 48,48,49
Mvaa 132,156,188
Ta 136,25,20736
Ta 138,25,25856
Ta 140,26,20768
Ta 142,26,25888
W 4
Mvaa 144,128,188
K 49,50,51
K 50,52,53
Mvaa 152,128,204
D 16,40,94,0
K 51,54,55
Mvaa 156,132,204
Ta 144,25,30720
Ta 146,25,35840
Mvaa 148,132,188
Ta 148,26,30752
Ta 150,26,35872
W 4
Mvaa 160,136,188
Ta 152,25,30784
Ta 154,25,35904
Mvaa 168,136,204
D 17,40,94,1024
D 18,40,94,2048
D 19,40,94,3072
s_add_u32 m0, s95, 116736
s_nop 0
buffer_load_dwordx4 v20, s[40:43], s94 offen lds
s_waitcnt vmcnt(12)
s_cmp_lg_u32 s70, 0
s_cbranch_scc1 .Lpd_78%=
.Lpdr_79%=:
Mvaa 172,140,204
s_add_u32 s81, s58, 2
Ta 156,26,30816
Ta 158,26,35936
Mvaa 164,140,188
W 4
Ta 128,25,30848
Ta 130,25,35968
Mvaa 96,144,192
s_and_b32 s81, s81, 3
Ta 132,26,30880
Ta 134,26,36000
Mvaa 104,144,208
buffer_store_dwordx4 v[48:51], v1, s[8:11], s93 offen offset:2048 sc1
U 56,56,4
Mvaa 108,148,208
s_lshl_b32 s81, s81, 12
U 57,57,4
U 58,58,4
Mvaa 100,148,192
W 4
U 59,59,4
U 60,60,4
Mvaa 112,152,192
U 61,61,4
U 62,62,4
Mvaa 120,152,208
U 63,63,4
K 56,56,57
Mvaa 124,156,208
s_add_u32 s81, s81, s49
K 57,58,59
K 58,60,61
Mvaa 116,156,192
Ta 136,25,30912
Ta 138,25,36032
Ta 140,26,30944
Ta 142,26,36064
W 4
Mvaa 128,128,192
Ta 144,25,30976
K 59,62,63
Mvaa 136,128,208
buffer_store_dwordx4 v[56:59], v1, s[8:11], s93 offen offset:3072 sc1
U 64,64,5
Mvaa 140,132,208
s_add_u32 s82, s81, 131072
U 65,65,5
U 66,66,5
Mvaa 132,132,192
Ta 146,25,36096
Ta 148,26,31008
Ta 150,26,36128
W 4
Mvaa 144,136,192
U 67,67,5
U 68,68,5
Mvaa 152,136,208
U 69,69,5
U 70,70,5
Mvaa 156,140,208
U 71,71,5
K 64,64,65
Mvaa 148,140,192
Ta 152,21,0
Ta 154,21,5120
Ta 156,22,32
Ta 158,22,5152
W 4
Mvaa 160,144,192
K 65,66,67
K 66,68,69
Mvaa 168,144,208
Ta 128,21,64
K 67,70,71
Mvaa 172,148,208
s_add_u32 s67, s93, 131072
buffer_store_dwordx4 v[64:67], v1, s[8:11], s67 offen offset:0 sc1
U 72,72,5
Mvaa 164,148,192
Ta 130,21,5184
Ta 132,22,96
Ta 134,22,5216
W 4
Mvaa 96,152,196
U 73,73,5
U 74,74,5
Mvaa 104,152,212
U 75,75,5
U 76,76,5
Mvaa 108,156,212
U 77,77,5
U 78,78,5
Mvaa 100,156,196
Ta 136,21,128
Ta 138,21,5248
Ta 140,22,160
Ta 142,22,5280
W 4
Mvaa 112,128,196
U 79,79,5
K 72,72,73
Mvaa 120,128,212
K 73,74,75
K 74,76,77
Mvaa 124,132,212
buffer_load_dwordx4 a[0:3], v1, s[8:11], s81 offen offset:0 sc1
K 75,78,79
Mvaa 116,132,196
Ta 144,21,192
Ta 146,21,5312
)"
R"(Ta 148,22,224
Ta 150,22,5344
W 4
Mvaa 128,136,196
Ta 152,21,256
Ta 154,21,5376
Mvaa 136,136,212
Ta 156,22,288
Ta 158,22,5408
Mvaa 140,140,212
buffer_store_dwordx4 v[72:75], v1, s[8:11], s67 offen offset:1024 sc1
U 80,80,5
Mvaa 132,140,196
W 4
U 81,81,5
U 82,82,5
Mvaa 144,144,196
U 83,83,5
U 84,84,5
Mvaa 152,144,212
U 85,85,5
U 86,86,5
Mvaa 156,148,212
U 87,87,5
K 80,80,81
Mvaa 148,148,196
Ta 128,21,10240
Ta 130,21,15360
Ta 132,22,10272
Ta 134,22,15392
W 4
Mvaa 160,152,196
K 81,82,83
K 82,84,85
Mvaa 168,152,212
buffer_load_dwordx4 a[16:19], v1, s[8:11], s82 offen offset:0 sc1
K 83,86,87
Mvaa 172,156,212
Ta 136,21,10304
Ta 138,21,15424
Mvaa 164,156,196
Ta 140,22,10336
Ta 142,22,15456
W 4
Mvaa 96,128,216
Ta 144,21,10368
Ta 146,21,15488
Mvaa 104,128,232
buffer_store_dwordx4 v[80:83], v1, s[8:11], s67 offen offset:2048 sc1
U 88,88,5
Mvaa 108,132,232
U 89,89,5
U 90,90,5
Mvaa 100,132,216
Ta 148,22,10400
Ta 150,22,15520
W 4
Mvaa 112,136,216
U 91,91,5
U 92,92,5
Mvaa 120,136,232
U 93,93,5
U 94,94,5
Mvaa 124,140,232
U 95,95,5
K 88,88,89
Mvaa 116,140,216
Ta 152,21,10432
Ta 154,21,15552
Ta 156,22,10464
Ta 158,22,15584
W 4
Mvaa 128,144,216
K 89,90,91
K 90,92,93
Mvaa 136,144,232
buffer_load_dwordx4 a[4:7], v1, s[8:11], s81 offen offset:1024 sc1
K 91,94,95
Mvaa 140,148,232
Ta 128,21,10496
Ta 130,21,15616
Mvaa 132,148,216
Ta 132,22,10528
Ta 134,22,15648
W 4
Mvaa 144,152,216
Ta 136,21,20480
Ta 138,21,25600
Mvaa 152,152,232
buffer_store_dwordx4 v[88:91], v1, s[8:11], s67 offen offset:3072 sc1
Ta 140,22,20512
Mvaa 156,156,232
Ta 142,22,25632
Ta 144,21,20544
Mvaa 148,156,216
W 5
Ta 146,21,25664
Ta 148,22,20576
Mvaa 160,128,216
Ta 150,22,25696
Ta 152,21,20608
Mvaa 168,128,232
buffer_load_dwordx4 a[20:23], v1, s[8:11], s82 offen offset:1024 sc1
Ta 154,21,25728
Mvaa 172,132,232
Ta 156,22,20640
Ta 158,22,25760
Mvaa 164,132,216
W 8
Ta 128,21,20672
Ta 130,21,25792
Mvaa 96,136,220
Mvaa 104,136,236
buffer_load_dwordx4 a[8:11], v1, s[8:11], s81 offen offset:2048 sc1
Ta 132,22,20704
Mvaa 108,140,236
Ta 134,22,25824
Mvaa 100,140,220
W 8
Ta 136,21,20736
Ta 138,21,25856
Mvaa 112,144,220
Mvaa 120,144,236
buffer_load_dwordx4 a[24:27], v1, s[8:11], s82 offen offset:2048 sc1
Ta 140,22,20768
Mvaa 124,148,236
Ta 142,22,25888
Mvaa 116,148,220
W 8
Ta 144,21,30720
Ta 146,21,35840
Mvaa 128,152,220
Mvaa 136,152,236
buffer_load_dwordx4 a[12:15], v1, s[8:11], s81 offen offset:3072 sc1
Ta 148,22,30752
Mvaa 140,156,236
Ta 150,22,35872
Mvaa 132,156,220
W 8
Ta 152,21,30784
Ta 154,21,35904
Mvaa 144,128,220
Mvaa 152,128,236
buffer_load_dwordx4 a[28:31], v1, s[8:11], s82 offen offset:3072 sc1
Ta 156,22,30816
Mvaa 156,132,236
s_waitcnt vmcnt(14)
s_barrier
Ta 158,22,35936
Mvaa 148,132,220
W 8
s_lshl_b32 s94, s58, 2
Ta 128,21,30848
Mvaa 160,136,220
s_add_u32 s94, s94, s51
Ta 130,21,35968
Mvaa 168,136,236
s_add_u32 s94, s94, 11
Ta 132,22,30880
Ta 134,22,36000
Mvaa 172,140,236
s_and_b32 s94, s94, 15
Mvaa 164,140,220
W 8
Ta 136,21,30912
Ta 138,21,36032
Mvaa 96,144,224
s_mul_i32 s94, s94, 10240
Mvaa 104,144,240
s_add_u32 s94, s94, s48
Ta 140,22,30944
Ta 142,22,36064
Mvaa 108,148,240
s_lshl_b32 s94, s94, 2
Mvaa 100,148,224
W 8
Ta 144,21,30976
Ta 146,21,36096
Mvaa 112,152,224
s_mul_i32 s95, s51, 10240
Mvaa 120,152,240
s_add_u32 s95, s95, s48
Ta 148,22,31008
Ta 150,22,36128
Mvaa 124,156,240
s_add_u32 m0, s95, 40960
Mvaa 116,156,224
W 8
Ta 152,23,0
Ta 154,23,5120
Mvaa 128,128,224
D 16,40,94,0
Mvaa 136,128,240
Ta 156,24,32
Ta 158,24,5152
Mvaa 140,132,240
Mvaa 132,132,224
W 8
Ta 128,23,64
Ta 130,23,5184
Mvaa 144,136,224
D 17,40,94,1024
Mvaa 152,136,240
Ta 132,24,96
Ta 134,24,5216
Mvaa 156,140,240
Mvaa 148,140,224
W 8
Ta 136,23,128
Ta 138,23,5248
Mvaa 160,144,224
D 18,40,94,2048
Mvaa 168,144,240
Ta 140,24,160
Ta 142,24,5280
Mvaa 172,148,240
Mvaa 164,148,224
W 8
Ta 144,23,192
Ta 146,23,5312
Mvaa 96,152,228
D 19,40,94,3072
Mvaa 104,152,244
s_add_u32 m0, s95, 45056
Ta 148,24,224
Ta 150,24,5344
Mvaa 108,156,244
Mvaa 100,156,228
W 8
Ta 152,23,256
Ta 154,23,5376
Mvaa 112,128,228
buffer_load_dwordx4 v20, s[40:43], s94 offen lds
Mvaa 120,128,244
s_lshl_b32 s80, s58, 3
s_add_u32 s80, s80, s55
Ta 156,24,288
Mvaa 124,132,244
Ta 158,24,5408
Mvaa 116,132,228
W 8
Ta 128,23,20480
Ta 130,23,25600
Mvaa 128,136,228
s_waitcnt vmcnt(10)
v_mov_b64 v[28:29], s[12:13]
s_mov_b64 exec, 1
buffer_store_dwordx2 v[28:29], off, s[8:11], s80 sc1
s_mov_b64 exec, -1
s_lshl_b32 s94, s58, 2
s_add_u32 s94, s94, s51
Mvaa 136,136,244
s_add_u32 s94, s94, 13
s_and_b32 s94, s94, 15
s_mul_i32 s94, s94, 10240
Mvaa 140,140,244
s_add_u32 s94, s94, s48
s_lshl_b32 s94, s94, 2
s_mul_i32 s95, s51, 20480
Mvaa 132,140,228
W 6
s_add_u32 s95, s95, s48
s_add_u32 m0, s95, 71680
Mvaa 144,144,228
D 16,40,94,0
Ta 132,24,20512
Mvaa 152,144,244
Ta 134,24,25632
Ta 136,23,20544
Mvaa 156,148,244
Ta 138,23,25664
Ta 140,24,20576
Mvaa 148,148,228
W 7
Ta 142,24,25696
Ta 144,23,20608
Mvaa 160,152,228
D 17,40,94,1024
Ta 146,23,25728
Mvaa 168,152,244
Ta 148,24,20640
Ta 150,24,25760
Mvaa 172,156,244
Mvaa 164,156,228
W 8
Ta 152,23,20672
Ta 154,23,25792
Mvaa 96,128,0
D 18,40,94,2048
Mvaa 104,128,16
Ta 156,24,20704
Ta 158,24,25824
Mvaa 108,132,16
Mvaa 100,132,0
W 8
Ta 128,23,20736
Ta 130,23,25856
Mvaa 112,136,0
D 19,40,94,3072
Mvaa 120,136,16
s_add_u32 m0, s95, 75776
Ta 132,24,20768
Ta 134,24,25888
Mvaa 124,140,16
Mvaa 116,140,0
W 8
Ta 136,23,51200
Ta 138,23,56320
Mvaa 128,144,0
buffer_load_dwordx4 v20, s[40:43], s94 offen lds
Mvaa 136,144,16
s_lshl_b32 s94, s58, 2
s_add_u32 s94, s94, 15
s_and_b32 s94, s94, 15
Mvaa 140,148,16
s_mul_i32 s94, s94, 10240
s_add_u32 s94, s94, s48
s_lshl_b32 s94, s94, 2
Mvaa 132,148,0
W 6
s_mul_i32 s95, s51, 0
s_add_u32 s95, s95, s48
Mvaa 144,152,0
s_add_u32 m0, s95, 102400
s_nop 0
D 16,40,94,0
Ta 140,24,51232
Mvaa 152,152,16
Ta 142,24,56352
Ta 144,23,51264
Mvaa 156,156,16
Ta 146,23,56384
Ta 148,24,51296
Mvaa 148,156,0
W 7
Ta 150,24,56416
Ta 152,23,51328
Mvaa 160,128,0
D 17,40,94,1024
Ta 154,23,56448
Mvaa 168,128,16
Ta 156,24,51360
Ta 158,24,56480
Mvaa 172,132,16
Mvaa 164,132,0
s_waitcnt vmcnt(17) lgkmcnt(8)
Ta 128,23,51392
Ta 130,23,56512
Mvaa 96,136,4
D 18,40,94,2048
Mvaa 104,136,20
Ta 132,24,51424
Ta 134,24,56544
Mvaa 108,140,20
Mvaa 100,140,4
W 8
Ta 136,23,51456
Ta 138,23,56576
Mvaa 112,144,4
D 19,40,94,3072
Mvaa 120,144,20
s_add_u32 m0, s95, 106496
Ta 140,24,51488
Ta 142,24,56608
Mvaa 124,148,20
Mvaa 116,148,4
W 8
Ta 144,25,0
Ta 146,25,5120
Mvaa 128,152,4
buffer_load_dwordx4 v20, s[40:43], s94 offen lds
Mvaa 136,152,20
s_add_u32 s81, s58, 3
s_and_b32 s81, s81, 3
s_lshl_b32 s81, s81, 12
Mvaa 140,156,20
s_add_u32 s81, s81, s49
s_add_u32 s82, s81, 131072
Ta 148,26,32
Mvaa 132,156,4
W 7
Ta 150,26,5152
Ta 152,25,64
Mvaa 144,128,4
buffer_load_dwordx4 a[32:35], v1, s[8:11], s81 offen offset:0 sc1
Ta 154,25,5184
Mvaa 152,128,20
Ta 156,26,96
Ta 158,26,5216
Mvaa 156,132,20
Mvaa 148,132,4
W 8
Ta 128,25,128
Ta 130,25,5248
Mvaa 160,136,4
buffer_load_dwordx4 a[48:51], v1, s[8:11], s82 offen offset:0 sc1
Mvaa 168,136,20
Ta 132,26,160
Ta 134,26,5280
Mvaa 172,140,20
)"
R"(Mvaa 164,140,4
s_waitcnt vmcnt(20) lgkmcnt(8)
Ta 136,25,192
Ta 138,25,5312
Mvaa 96,144,8
s_waitcnt vmcnt(2)
s_barrier
buffer_load_dwordx4 a[36:39], v1, s[8:11], s81 offen offset:1024 sc1
Mvaa 104,144,24
Ta 140,26,224
v_and_b32 v27, 1, v12
Mvaa 108,148,24
Ta 142,26,5344
Mvaa 100,148,8
W 8
Ta 144,25,256
v_lshlrev_b32 v27, 1, v27
Mvaa 112,152,8
buffer_load_dwordx4 a[52:55], v1, s[8:11], s82 offen offset:1024 sc1
Ta 146,25,5376
Mvaa 120,152,24
Ta 148,26,288
v_xor_b32 v27, v27, v13
Mvaa 124,156,24
Ta 150,26,5408
Mvaa 116,156,8
W 8
Ta 152,21,40960
Ta 154,21,46080
Mvaa 128,128,8
buffer_load_dwordx4 a[40:43], v1, s[8:11], s81 offen offset:2048 sc1
v_and_b32 v12, 2, v12
Mvaa 136,128,24
Ta 156,22,40992
Ta 158,22,46112
Mvaa 140,132,24
v_or_b32 v12, v12, v14
Mvaa 132,132,8
W 8
Ta 128,21,41024
Ta 130,21,46144
Mvaa 144,136,8
buffer_load_dwordx4 a[56:59], v1, s[8:11], s82 offen offset:2048 sc1
Mvaa 152,136,24
Ta 132,22,41056
v_lshl_or_b32 v12, v27, 2, v12
Mvaa 156,140,24
Ta 134,22,46176
Mvaa 148,140,8
W 8
Ta 136,21,41088
v_cmp_eq_u32_e64 s[86:87], 0, v12
Mvaa 160,144,8
buffer_load_dwordx4 a[44:47], v1, s[8:11], s81 offen offset:3072 sc1
Ta 138,21,46208
Mvaa 168,144,24
Ta 140,22,41120
Ta 142,22,46240
Mvaa 172,148,24
v_cndmask_b32_e64 v4, 0, v15, s[86:87]
Mvaa 164,148,8
W 8
Ta 144,21,41152
Ta 146,21,46272
Mvaa 96,152,12
buffer_load_dwordx4 a[60:63], v1, s[8:11], s82 offen offset:3072 sc1
v_cmp_eq_u32_e64 s[86:87], 1, v12
Mvaa 104,152,28
Ta 148,22,41184
Ta 150,22,46304
Mvaa 108,156,28
Mvaa 100,156,12
W 8
Ta 152,21,41216
v_cndmask_b32_e64 v5, 0, v15, s[86:87]
Mvaa 112,128,12
Ta 154,21,46336
Mvaa 120,128,28
Ta 156,22,41248
v_cmp_eq_u32_e64 s[86:87], 2, v12
Mvaa 124,132,28
Ta 158,22,46368
Mvaa 116,132,12
W 8
Ta 128,21,51200
Ta 130,21,56320
Mvaa 128,136,12
v_cndmask_b32_e64 v6, 0, v15, s[86:87]
Mvaa 136,136,28
Ta 132,22,51232
Ta 134,22,56352
Mvaa 140,140,28
v_cmp_eq_u32_e64 s[86:87], 3, v12
Mvaa 132,140,12
W 8
Ta 136,21,51264
Ta 138,21,56384
Mvaa 144,144,12
v_bfe_u32 v28, v1, 4, 2
Mvaa 152,144,28
v_cndmask_b32_e64 v7, 0, v15, s[86:87]
v_lshlrev_b32 v28, 3, v28
Mvaa 156,148,28
Ta 140,22,51296
Ta 142,22,56416
Mvaa 148,148,12
W 8
buffer_load_dwordx2 v[28:29], v28, s[8:11], s55 offen sc1
v_cmp_eq_u32_e64 s[86:87], 4, v12
Mvaa 160,152,12
Ta 144,21,51328
Ta 146,21,56448
Mvaa 168,152,28
Ta 148,22,51360
Ta 150,22,56480
Mvaa 172,156,28
v_cndmask_b32_e64 v8, 0, v15, s[86:87]
Mvaa 164,156,12
s_waitcnt vmcnt(7) lgkmcnt(8)
Ta 152,21,51392
Ta 154,21,56512
Mvaa 96,128,32
v_cmp_eq_u32_e64 s[86:87], 5, v12
Mvaa 104,128,48
Ta 156,22,51424
Ta 158,22,56544
Mvaa 108,132,48
Mvaa 100,132,32
W 8
Ta 128,21,51456
v_cndmask_b32_e64 v9, 0, v15, s[86:87]
Mvaa 112,136,32
Ta 130,21,56576
Mvaa 120,136,48
Ta 132,22,51488
v_cmp_eq_u32_e64 s[86:87], 6, v12
Mvaa 124,140,48
Ta 134,22,56608
Mvaa 116,140,32
W 8
Ta 136,23,10240
Ta 138,23,15360
Mvaa 128,144,32
v_cndmask_b32_e64 v10, 0, v15, s[86:87]
Mvaa 136,144,48
Ta 140,24,10272
Ta 142,24,15392
Mvaa 140,148,48
v_cmp_eq_u32_e64 s[86:87], 7, v12
Mvaa 132,148,32
W 8
Ta 144,23,10304
Ta 146,23,15424
Mvaa 144,152,32
Mvaa 152,152,48
Ta 148,24,10336
v_cndmask_b32_e64 v11, 0, v15, s[86:87]
Mvaa 156,156,48
Ta 150,24,15456
Mvaa 148,156,32
W 8
s_mov_b32 s20, s38
Ta 152,23,10368
Mvaa 160,128,32
Ta 154,23,15488
Mvaa 168,128,48
Ta 156,24,10400
Ta 158,24,15520
Mvaa 172,132,48
s_and_b32 s21, s39, 0xffff
Mvaa 164,132,32
s_waitcnt vmcnt(5) lgkmcnt(8)
Ta 128,23,10432
Ta 130,23,15552
Mvaa 96,136,36
s_mov_b32 s22, 2621440
Mvaa 104,136,52
Ta 132,24,10464
Ta 134,24,15584
Mvaa 108,140,52
Mvaa 100,140,36
W 8
s_mov_b32 s23, 0x20000
Ta 136,23,10496
Mvaa 112,144,36
Ta 138,23,15616
Mvaa 120,144,52
s_and_b32 s17, s17, 0xffff
Ta 140,24,10528
Ta 142,24,15648
Mvaa 124,148,52
Mvaa 116,148,36
W 8
Ta 144,23,30720
Ta 146,23,35840
Mvaa 128,152,36
s_mov_b32 s18, 2621440
Mvaa 136,152,52
Ta 148,24,30752
Ta 150,24,35872
Mvaa 140,156,52
s_mov_b32 s19, 0x20000
Mvaa 132,156,36
W 8
Ta 152,23,30784
Ta 154,23,35904
Mvaa 144,128,36
Mvaa 152,128,52
s_lshr_b32 s66, s15, 3
Ta 156,24,30816
Ta 158,24,35936
Mvaa 156,132,52
s_waitcnt vmcnt(0)
v_cmp_eq_u64 vcc, s[12:13], v[28:29]
s_cmp_eq_u64 vcc, -1
s_cbranch_scc0 .Lspin_80%=
.Lok_81%=:
s_add_u32 s81, s58, 1
Mvaa 148,132,36
W 8
s_lshl_b32 s66, s66, 4
Ta 128,23,30848
Mvaa 160,136,36
Ta 130,23,35968
Mvaa 168,136,52
Ta 132,24,30880
Ta 134,24,36000
Mvaa 172,140,52
s_and_b32 s81, s81, 3
s_add_u32 s66, s66, s59
Mvaa 164,140,36
W 8
Ta 136,23,30912
Ta 138,23,36032
Mvaa 96,144,40
s_lshl_b32 s63, s66, 6
Mvaa 104,144,56
s_lshl_b32 s81, s81, 12
Ta 140,24,30944
Ta 142,24,36064
Mvaa 108,148,56
Mvaa 100,148,40
W 8
s_add_u32 s63, s63, 65536
Ta 144,23,30976
Mvaa 112,152,40
s_add_u32 s81, s81, s50
Ta 146,23,36096
Mvaa 120,152,56
s_mul_i32 s62, s66, 40960
Ta 148,24,31008
Ta 150,24,36128
Mvaa 124,156,56
Mvaa 116,156,40
W 8
s_add_u32 s82, s81, 131072
Ta 152,23,40960
Mvaa 128,128,40
s_add_u32 s62, s62, 56623104
Ta 154,23,46080
Mvaa 136,128,56
Ta 156,24,40992
Ta 158,24,46112
Mvaa 140,132,56
s_lshl_b32 s66, s14, 10
buffer_load_dwordx4 a[64:67], v1, s[8:11], s81 offen offset:0 sc1
Mvaa 132,132,40
W 8
Ta 128,23,41024
Ta 130,23,46144
Mvaa 144,136,40
Mvaa 152,136,56
s_add_u32 s78, s62, s66
Ta 132,24,41056
Ta 134,24,46176
Mvaa 156,140,56
buffer_load_dwordx4 a[80:83], v1, s[8:11], s82 offen offset:0 sc1
Mvaa 148,140,40
W 8
s_add_u32 s79, s78, 327680
Ta 136,23,41088
Mvaa 160,144,40
Ta 138,23,46208
Mvaa 168,144,56
Ta 140,24,41120
Ta 142,24,46240
Mvaa 172,148,56
s_and_b32 s66, s14, 1
buffer_load_dwordx4 a[68:71], v1, s[8:11], s81 offen offset:1024 sc1
Mvaa 164,148,40
W 8
Ta 144,23,41152
Ta 146,23,46272
Mvaa 96,152,44
s_mul_i32 s66, s66, 327680
Mvaa 104,152,60
Ta 148,24,41184
Ta 150,24,46304
Mvaa 108,156,60
buffer_load_dwordx4 a[84:87], v1, s[8:11], s82 offen offset:1024 sc1
Mvaa 100,156,44
W 8
s_add_u32 s65, s62, s66
Ta 152,23,41216
Mvaa 112,128,44
Ta 154,23,46336
Mvaa 120,128,60
s_add_u32 s65, s65, 4096
Ta 156,24,41248
Ta 158,24,46368
Mvaa 124,132,60
buffer_load_dwordx4 a[72:75], v1, s[8:11], s81 offen offset:2048 sc1
Mvaa 116,132,44
W 8
Ta 128,25,10240
Ta 130,25,15360
Mvaa 128,136,44
s_mov_b32 s64, 0
Mvaa 136,136,60
Ta 132,26,10272
Ta 134,26,15392
Mvaa 140,140,60
buffer_load_dwordx4 a[88:91], v1, s[8:11], s82 offen offset:2048 sc1
v_bfe_u32 v27, v1, 4, 3
Mvaa 132,140,44
W 8
Ta 136,25,10304
K 96,96,97
Mvaa 144,144,44
Ta 138,25,15424
v_lshlrev_b32 v27, 3, v27
Mvaa 152,144,60
Ta 140,26,10336
K 97,98,99
Mvaa 156,148,60
buffer_load_dwordx4 a[76:79], v1, s[8:11], s81 offen offset:3072 sc1
Ta 142,26,15456
Mvaa 148,148,44
W 8
v_bfe_u32 v20, v1, 4, 4
K 98,100,101
Mvaa 160,152,44
Ta 144,25,10368
Ta 146,25,15488
Mvaa 168,152,60
Ta 148,26,10400
v_mul_u32_u24 v20, 1280, v20
Mvaa 172,156,60
buffer_load_dwordx4 a[92:95], v1, s[8:11], s82 offen offset:3072 sc1
Ta 150,26,15520
Mvaa 164,156,44
W 8
s_add_u32 s81, s58, 2
K 99,102,103
Zvav 176,128,32
s_and_b32 s81, s81, 3
Ta 152,25,10432
Ta 154,25,15552
Zvav 184,128,64
s_add_u32 s83, s52, 0
s_lshl_b32 s81, s81, 12
v_lshrrev_b32 v12, 8, v1
Zvav 188,132,64
Ta 156,26,10464
Ta 158,26,15584
Zvav 180,132,32
W 8
buffer_store_dwordx4 v[96:99], v1, s[8:11], s83 offen sc1
v_lshl_add_u32 v20, v12, 4, v20
Zvav 192,136,32
s_add_u32 s81, s81, s50
Ta 128,25,10496
Ta 130,25,15616
Zvav 200,136,64
s_bfe_u32 s66, s59, 0x10001
Ta 132,26,10528
K 104,104,105
Zvav 204,140,64
Ta 134,26,15648
Zvav 196,140,32
W 8
s_add_u32 s82, s81, 131072
K 105,106,107
Zvav 208,144,32
s_lshl_b32 s67, s57, 1
Ta 136,25,20480
Ta 138,25,25600
Zvav 216,144,64
Ta 140,26,20512
K 106,108,109
Zvav 220,148,64
s_add_u32 s66, s66, s67
Ta 142,26,25632
buffer_load_dwordx4 a[96:99], v1, s[8:11], s81 offen offset:0 sc1
Zvav 212,148,32
W 8
Ta 144,25,20544
Ta 146,25,25664
Zvav 224,152,32
s_lshl_b32 s61, s66, 2
)"
R"(K 107,110,111
Zvav 232,152,64
Ta 148,26,20576
Ta 150,26,25696
Zvav 236,156,64
s_add_u32 s83, s52, 40960
buffer_load_dwordx4 a[112:115], v1, s[8:11], s82 offen offset:0 sc1
Zvav 228,156,32
W 8
s_lshr_b32 s66, s59, 2
Ta 152,25,20608
Zvav 240,128,32
Ta 154,25,25728
Zvav 248,128,64
s_lshl_b32 s66, s66, 1
Ta 156,26,20640
Ta 158,26,25760
Zvav 252,132,64
buffer_load_dwordx4 a[100:103], v1, s[8:11], s81 offen offset:1024 sc1
Zvav 244,132,32
W 8
Ta 128,25,20672
Ta 130,25,25792
Mvav 176,136,40
s_add_u32 s61, s61, s66
Mvav 184,136,72
Ta 132,26,20704
Ta 134,26,25824
Mvav 188,140,72
s_and_b32 s66, s59, 1
buffer_store_dwordx4 v[104:107], v1, s[8:11], s83 offen sc1
K 112,112,113
Mvav 180,140,40
W 8
K 113,114,115
K 114,116,117
Mvav 192,144,40
Ta 136,25,20736
Ta 138,25,25856
Mvav 200,144,72
s_add_u32 s61, s61, s66
Ta 140,26,20768
Ta 142,26,25888
Mvav 204,148,72
buffer_load_dwordx4 a[116:119], v1, s[8:11], s82 offen offset:1024 sc1
K 115,118,119
Mvav 196,148,40
W 8
s_xor_b32 s67, s61, 0
Ta 144,25,30720
Mvav 208,152,40
s_add_u32 s83, s52, 1024
Ta 146,25,35840
Mvav 216,152,72
Ta 148,26,30752
Ta 150,26,35872
Mvav 220,156,72
s_mul_i32 s72, s67, 20480
buffer_load_dwordx4 a[104:107], v1, s[8:11], s81 offen offset:2048 sc1
Mvav 212,156,40
W 8
Ta 152,25,30784
Ta 154,25,35904
Mvav 224,128,40
s_mul_i32 s66, s58, 320
Mvav 232,128,72
Ta 156,26,30816
Ta 158,26,35936
Mvav 236,132,72
buffer_load_dwordx4 a[120:123], v1, s[8:11], s82 offen offset:2048 sc1
Mvav 228,132,40
W 8
s_add_u32 s72, s72, s66
Ta 128,25,30848
Mvav 240,136,40
Ta 130,25,35968
Mvav 248,136,72
s_mul_i32 s75, s67, 20480
Ta 132,26,30880
Ta 134,26,36000
Mvav 252,140,72
buffer_load_dwordx4 a[108:111], v1, s[8:11], s81 offen offset:3072 sc1
Mvav 244,140,40
W 8
Ta 136,25,30912
Ta 138,25,36032
Mvav 176,144,48
s_mul_i32 s66, s58, 2560
Mvav 184,144,80
Ta 140,26,30944
Ta 142,26,36064
Mvav 188,148,80
s_add_u32 s75, s75, s66
buffer_store_dwordx4 v[112:115], v1, s[8:11], s83 offen sc1
K 120,120,121
Mvav 180,148,48
W 8
K 121,122,123
K 122,124,125
Mvav 192,152,48
Ta 144,25,30976
K 123,126,127
Mvav 200,152,80
s_add_u32 s83, s52, 41984
s_add_u32 s75, s75, 131072
Ta 146,25,36096
Mvav 204,156,80
buffer_load_dwordx4 a[124:127], v1, s[8:11], s82 offen offset:3072 sc1
Ta 148,26,31008
Mvav 196,156,48
W 7
s_add_u32 s81, s58, 3
s_and_b32 s81, s81, 3
Mvav 208,128,48
s_lshl_b32 s81, s81, 12
s_add_u32 s81, s81, s50
Ta 150,26,36128
Mvav 216,128,80
s_add_u32 s82, s81, 131072
s_xor_b32 s67, s61, 124
Ta 152,21,0
Mvav 220,132,80
s_mul_i32 s73, s67, 20480
buffer_store_dwordx4 v[120:123], v1, s[8:11], s83 offen sc1
K 128,128,129
Mvav 212,132,48
W 5
Ta 154,21,5120
K 129,130,131
Mvav 224,136,48
s_mul_i32 s66, s58, 320
Ta 156,22,32
Ta 158,22,5152
Mvav 232,136,80
Ta 128,21,64
K 130,132,133
Mvav 236,140,80
buffer_load_dwordx4 a[184:187], v1, s[8:11], s81 offen offset:0 sc1
Ta 130,21,5184
Mvav 228,140,48
W 6
Ta 132,22,96
Ta 134,22,5216
Mvav 240,144,48
s_add_u32 s73, s73, s66
Ta 136,21,128
K 131,134,135
Mvav 248,144,80
s_add_u32 s83, s52, 2048
s_mul_i32 s76, s67, 20480
Ta 138,21,5248
Mvav 252,148,80
buffer_load_dwordx4 a[200:203], v1, s[8:11], s82 offen offset:0 sc1
Ta 140,22,160
Mvav 244,148,48
W 7
Ta 142,22,5280
Ta 144,21,192
Mvav 176,152,56
s_mul_i32 s66, s58, 2560
Ta 146,21,5312
Mvav 184,152,88
Ta 148,22,224
Ta 150,22,5344
Mvav 188,156,88
s_add_u32 s76, s76, s66
buffer_load_dwordx4 a[188:191], v1, s[8:11], s81 offen offset:1024 sc1
Mvav 180,156,56
W 8
Ta 152,21,256
Ta 154,21,5376
Mvav 192,128,56
Mvav 200,128,88
s_add_u32 s76, s76, 131072
Ta 156,22,288
Ta 158,22,5408
Mvav 204,132,88
buffer_store_dwordx4 v[128:131], v1, s[8:11], s83 offen sc1
K 136,136,137
Mvav 196,132,56
W 8
K 137,138,139
K 138,140,141
Mvav 208,136,56
Ta 128,21,10240
v_bfe_u32 v15, v1, 7, 1
Mvav 216,136,88
Ta 130,21,15360
K 139,142,143
Mvav 220,140,88
s_add_u32 s83, s52, 43008
buffer_load_dwordx4 a[204:207], v1, s[8:11], s82 offen offset:1024 sc1
Ta 132,22,10272
Mvav 212,140,56
W 7
Ta 134,22,15392
v_mul_u32_u24 v14, 10240, v15
Mvav 224,144,56
Ta 136,21,10304
Ta 138,21,15424
Mvav 232,144,88
Ta 140,22,10336
v_lshrrev_b32 v15, 8, v1
Mvav 236,148,88
buffer_load_dwordx4 a[192:195], v1, s[8:11], s81 offen offset:2048 sc1
Ta 142,22,15456
Mvav 228,148,56
W 8
Ta 144,21,10368
v_lshl_add_u32 v14, v15, 7, v14
Mvav 240,152,56
Ta 146,21,15488
Mvav 248,152,88
Ta 148,22,10400
v_and_b32 v15, 0x70, v1
Mvav 252,156,88
buffer_load_dwordx4 a[208:211], v1, s[8:11], s82 offen offset:2048 sc1
Ta 150,22,15520
Mvav 244,156,56
s_waitcnt vmcnt(25) lgkmcnt(8)
Ta 152,21,10432
Ta 154,21,15552
Mvaa 176,128,64
v_add_u32 v14, v14, v15
Mvaa 184,128,80
Ta 156,22,10464
Ta 158,22,15584
Mvaa 188,132,80
s_bitcmp1_b32 s14, 0
s_cselect_b32 s74, s73, s72
s_cselect_b32 s77, s76, s75
buffer_load_dwordx4 a[196:199], v1, s[8:11], s81 offen offset:3072 sc1
Mvaa 180,132,64
W 8
Ta 128,21,10496
Ta 130,21,15616
Mvaa 192,136,64
Mvaa 200,136,80
s_add_u32 s74, s74, 256
Ta 132,22,10528
Ta 134,22,15648
Mvaa 204,140,80
buffer_load_dwordx4 a[212:215], v1, s[8:11], s82 offen offset:3072 sc1
Mvaa 196,140,64
W 8
s_add_u32 s77, s77, 2048
Ta 136,21,20480
Mvaa 208,144,64
Ta 138,21,25600
Mvaa 216,144,80
Ta 140,22,20512
Ta 142,22,25632
Mvaa 220,148,80
s_lshl_b32 s66, s14, 6
buffer_store_dwordx4 v[136:139], v1, s[8:11], s83 offen sc1
K 144,144,145
Mvaa 212,148,64
W 8
K 145,146,147
K 146,148,149
Mvaa 224,152,64
s_lshl_b32 s67, s14, 9
Ta 144,21,20544
K 147,150,151
Mvaa 232,152,80
s_add_u32 s83, s52, 3072
Ta 146,21,25664
Ta 148,22,20576
Mvaa 236,156,80
buffer_store_dwordx4 v[144:147], v1, s[8:11], s83 offen sc1
K 152,152,153
Mvaa 228,156,64
W 7
K 153,154,155
K 154,156,157
Mvaa 240,128,64
s_add_u32 s72, s72, s66
Ta 150,22,25696
Ta 152,21,20608
Mvaa 248,128,80
s_add_u32 s75, s75, s67
Ta 154,21,25728
K 155,158,159
Mvaa 252,132,80
s_add_u32 s83, s52, 44032
Ta 156,22,20640
Ta 158,22,25760
Mvaa 244,132,64
s_waitcnt vmcnt(27) lgkmcnt(8)
Ta 128,21,20672
Ta 130,21,25792
Mvaa 176,136,68
s_add_u32 s73, s73, s66
buffer_store_dwordx4 v[152:155], v1, s[8:11], s83 offen sc1
Mvaa 184,136,84
Ta 132,22,20704
Ta 134,22,25824
Mvaa 188,140,84
s_add_u32 s76, s76, s67
K 160,160,161
Mvaa 180,140,68
W 8
Ta 136,21,20736
Ta 138,21,25856
Mvaa 192,144,68
buffer_load_dwordx4 v[36:39], v20, s[16:19], s72 offen
Mvaa 200,144,84
Ta 140,22,20768
K 161,162,163
Mvaa 204,148,84
Ta 142,22,25888
Mvaa 196,148,68
W 8
Ta 144,21,30720
K 162,164,165
Mvaa 208,152,68
Ta 146,21,35840
Mvaa 216,152,84
Ta 148,22,30752
Ta 150,22,35872
Mvaa 220,156,84
buffer_load_dwordx4 v[44:47], v14, s[8:11], s75 offen sc1
K 163,166,167
Mvaa 212,156,68
W 8
Ta 152,21,30784
Ta 154,21,35904
Mvaa 224,128,68
s_add_u32 s83, s52, 4096
Mvaa 232,128,84
Ta 156,22,30816
Ta 158,22,35936
Mvaa 236,132,84
buffer_store_dwordx4 v[160:163], v1, s[8:11], s83 offen sc1
Mvaa 228,132,68
W 8
Ta 128,21,30848
Ta 130,21,35968
Mvaa 240,136,68
K 168,168,169
Mvaa 248,136,84
Ta 132,22,30880
Ta 134,22,36000
Mvaa 252,140,84
buffer_load_dwordx4 v[52:55], v20, s[16:19], s73 offen
Mvaa 244,140,68
s_waitcnt vmcnt(30) lgkmcnt(8)
Ta 136,21,30912
K 169,170,171
Mvaa 176,144,72
Ta 138,21,36032
Mvaa 184,144,88
Ta 140,22,30944
K 170,172,173
Mvaa 188,148,88
Ta 142,22,36064
buffer_load_dwordx4 v[60:63], v14, s[8:11], s76 offen sc1
Mvaa 180,148,72
W 8
Ta 144,21,30976
Ta 146,21,36096
Mvaa 192,152,72
K 171,174,175
Mvaa 200,152,88
Ta 148,22,31008
Ta 150,22,36128
Mvaa 204,156,88
s_add_u32 s83, s52, 45056
Mvaa 196,156,72
W 8
buffer_load_dwordx4 v[68:71], v20, s[16:19], s74 offen
Ta 152,23,0
Mvaa 208,128,72
Ta 154,23,5120
Mvaa 216,128,88
Ta 156,24,32
Ta 158,24,5152
Mvaa 220,132,88
Mvaa 212,132,72
W 8
buffer_store_dwordx4 v[168:171], v1, s[8:11], s83 offen sc1
Ta 128,23,64
Mvaa 224,136,72
Ta 130,23,5184
Mvaa 232,136,88
Ta 132,24,96
Ta 134,24,5216
Mvaa 236,140,88
Mvaa 228,140,72
W 8
buffer_load_dwordx4 v[76:79], v14, s[8:11], s77 offen sc1
Ta 136,23,128
Mvaa 240,144,72
Ta 138,23,5248
Mvaa 248,144,88
Ta 140,24,160
Ta 142,24,5280
Mvaa 252,148,88
Mvaa 244,148,72
s_waitcnt vmcnt(32) lgkmcnt(8)
)"
R"(buffer_load_dwordx2 v[12:13], v27, s[8:11], 0 offen offset:3584 sc1
Ta 144,23,192
Mvaa 176,152,76
Ta 146,23,5312
Mvaa 184,152,92
Ta 148,24,224
Ta 150,24,5344
Mvaa 188,156,92
Mvaa 180,156,76
W 8
Ta 152,23,256
Ta 154,23,5376
Mvaa 192,128,76
Mvaa 200,128,92
Ta 156,24,288
Ta 158,24,5408
Mvaa 204,132,92
Mvaa 196,132,76
W 8
Ta 128,23,20480
Ta 130,23,25600
Mvaa 208,136,76
Mvaa 216,136,92
Ta 132,24,20512
Ta 134,24,25632
Mvaa 220,140,92
Mvaa 212,140,76
W 8
Ta 136,23,51200
Ta 138,23,56320
Mvaa 224,144,76
Mvaa 232,144,92
Ta 140,24,51232
Ta 142,24,56352
Mvaa 236,148,92
Mvaa 228,148,76
W 8
Ta 144,25,0
Ta 146,25,5120
Mvaa 240,152,76
Mvaa 248,152,92
Ta 148,26,32
Ta 150,26,5152
Mvaa 252,156,92
Mvaa 244,156,76
s_waitcnt vmcnt(30) lgkmcnt(8)
Ta 152,21,40960
Ta 154,21,46080
Mvaa 176,128,96
Mvaa 184,128,112
Ta 156,22,40992
Ta 158,22,46112
Mvaa 188,132,112
Mvaa 180,132,96
s_waitcnt vmcnt(27) lgkmcnt(8)
Ta 128,21,51200
Ta 130,21,56320
Mvaa 176,136,100
Mvaa 184,136,116
Ta 132,22,51232
Ta 134,22,56352
Mvaa 188,140,116
Mvaa 180,140,100
s_waitcnt vmcnt(25) lgkmcnt(8)
Ta 136,23,10240
Ta 138,23,15360
Mvaa 176,144,104
Mvaa 184,144,120
Ta 140,24,10272
Ta 142,24,15392
Mvaa 188,148,120
Mvaa 180,148,104
s_waitcnt vmcnt(22) lgkmcnt(8)
Ta 144,23,30720
Ta 146,23,35840
Mvaa 176,152,108
Mvaa 184,152,124
Ta 148,24,30752
Ta 150,24,35872
Mvaa 188,156,124
Mvaa 180,156,108
s_waitcnt vmcnt(19) lgkmcnt(8)
Ta 152,23,40960
Ta 154,23,46080
Mvaa 176,128,184
Mvaa 184,128,200
Ta 156,24,40992
Ta 158,24,46112
Mvaa 188,132,200
Mvaa 180,132,184
s_waitcnt vmcnt(16) lgkmcnt(8)
Ta 128,23,20544
Ta 130,23,25664
Mvaa 176,136,188
Mvaa 184,136,204
Ta 132,24,20576
Ta 134,24,25696
Mvaa 188,140,204
Mvaa 180,140,188
s_waitcnt vmcnt(14) lgkmcnt(8)
Ta 136,23,51264
Ta 138,23,56384
Mvaa 176,144,192
Mvaa 184,144,208
Ta 140,24,51296
Ta 142,24,56416
Mvaa 188,148,208
Mvaa 180,148,192
s_waitcnt vmcnt(12) lgkmcnt(8)
Ta 144,25,64
Ta 146,25,5184
Mvaa 176,152,196
Mvaa 184,152,212
Ta 148,26,96
Ta 150,26,5216
Mvaa 188,156,212
Mvaa 180,156,196
W 8
Ta 152,21,41024
Ta 154,21,46144
Mvaa 192,128,96
Mvaa 200,128,112
Ta 156,22,41056
Ta 158,22,46176
Mvaa 204,132,112
K 176,176,177
Mvaa 196,132,96
W 8
Ta 128,21,51264
Ta 130,21,56384
Mvaa 192,136,100
K 177,178,179
Mvaa 200,136,116
Ta 132,22,51296
K 178,180,181
Mvaa 204,140,116
Ta 134,22,56416
K 179,182,183
Mvaa 196,140,100
W 8
Ta 136,23,10304
Ta 138,23,15424
Mvaa 192,144,104
s_add_u32 s83, s52, 327680
Mvaa 200,144,120
buffer_store_dwordx4 v[176:179], v1, s[8:11], s83 offen sc1
Ta 140,24,10336
Mvaa 204,148,120
Ta 142,24,15456
Mvaa 196,148,104
s_waitcnt vmcnt(3)
v_lshlrev_b32 v2, 2, v1
v_mov_b64 v[28:29], s[12:13]
s_mov_b64 exec, 3
buffer_store_dwordx2 v[28:29], v2, s[8:11], s53 offen offset:0 sc1
s_mov_b64 exec, -1
K 184,184,185
W 8
Mvaa 192,152,108
K 185,186,187
K 186,188,189
Mvaa 200,152,124
s_waitcnt vmcnt(2)
v_xor_b32 v12, s12, v12
v_xor_b32 v13, s13, v13
v_lshrrev_b32 v3, 11, v12
v_or_b32 v13, v13, v3
v_cmp_eq_u32 vcc, 0, v13
s_cmp_eq_u64 vcc, -1
s_cbranch_scc0 .LL_85%=
.LL_86%=:
v_bfe_u32 v13, v12, 4, 7
s_xor_b32 s66, s61, 124
v_cmp_eq_u32 vcc, s61, v13
v_cmp_eq_u32_e64 s[86:87], s66, v13
s_or_b32 s68, vcc_lo, s86
s_and_b32 s68, s68, 0xff
s_cbranch_scc1 .Lup_83%=
.Lupr_84%=:
Ta 144,23,30784
K 187,190,191
Mvaa 204,156,124
s_add_u32 s83, s52, 368640
Ta 146,23,35904
Ta 148,24,30816
Mvaa 196,156,108
W 7
buffer_store_dwordx4 v[184:187], v1, s[8:11], s83 offen sc1
Ta 150,24,35936
Mvaa 192,128,184
Ta 152,23,41024
v_lshlrev_b32 v32, 16, v36
Mvaa 200,128,200
Ta 154,23,46144
v_and_b32 v33, 0xffff0000, v36
Mvaa 204,132,200
Ta 156,24,41056
v_lshlrev_b32 v34, 16, v37
Mvaa 196,132,184
W 7
Ta 158,24,46176
v_and_b32 v35, 0xffff0000, v37
Mvaa 192,136,188
Ta 128,23,20608
v_lshlrev_b32 v36, 16, v38
Mvaa 200,136,204
Ta 130,23,25728
v_and_b32 v37, 0xffff0000, v38
Mvaa 204,140,204
Ta 132,24,20640
v_lshlrev_b32 v38, 16, v39
Mvaa 196,140,188
W 7
Ta 134,24,25760
v_and_b32 v39, 0xffff0000, v39
Mvaa 192,144,192
Ta 136,23,51328
v_lshlrev_b32 v40, 16, v44
Mvaa 200,144,208
Ta 138,23,56448
v_and_b32 v41, 0xffff0000, v44
Mvaa 204,148,208
Ta 140,24,51360
v_lshlrev_b32 v42, 16, v45
Mvaa 196,148,192
W 7
Ta 142,24,56480
v_and_b32 v43, 0xffff0000, v45
Mvaa 192,152,196
Ta 144,25,128
v_lshlrev_b32 v44, 16, v46
Mvaa 200,152,212
Ta 146,25,5248
v_and_b32 v45, 0xffff0000, v46
Mvaa 204,156,212
Ta 148,26,160
v_lshlrev_b32 v46, 16, v47
Mvaa 196,156,196
W 7
Ta 150,26,5280
v_and_b32 v47, 0xffff0000, v47
Mvaa 208,128,96
Ta 152,21,41088
v_lshlrev_b32 v48, 16, v52
Mvaa 216,128,112
Ta 154,21,46208
v_and_b32 v49, 0xffff0000, v52
Mvaa 220,132,112
Ta 156,22,41120
v_lshlrev_b32 v50, 16, v53
Mvaa 212,132,96
W 7
Ta 158,22,46240
v_and_b32 v51, 0xffff0000, v53
Mvaa 208,136,100
K 192,192,193
v_lshlrev_b32 v52, 16, v54
Mvaa 216,136,116
Ta 128,21,51328
Ta 130,21,56448
Mvaa 220,140,116
K 193,194,195
v_and_b32 v53, 0xffff0000, v54
Mvaa 212,140,100
W 6
v_lshlrev_b32 v54, 16, v55
K 194,196,197
Mvaa 208,144,104
Ta 132,22,51360
Ta 134,22,56480
Mvaa 216,144,120
v_and_b32 v55, 0xffff0000, v55
K 195,198,199
Mvaa 220,148,120
s_add_u32 s83, s52, 328704
Ta 136,23,10368
v_lshlrev_b32 v56, 16, v60
Mvaa 212,148,104
W 5
Ta 138,23,15488
v_and_b32 v57, 0xffff0000, v60
Mvaa 208,152,108
buffer_store_dwordx4 v[192:195], v1, s[8:11], s83 offen sc1
v_lshlrev_b32 v58, 16, v61
Mvaa 216,152,124
K 200,200,201
K 201,202,203
Mvaa 220,156,124
K 202,204,205
K 203,206,207
Mvaa 212,156,108
Ta 140,24,10400
Ta 142,24,15520
W 4
Mvaa 208,128,184
s_add_u32 s83, s52, 369664
buffer_store_dwordx4 v[200:203], v1, s[8:11], s83 offen sc1
v_and_b32 v59, 0xffff0000, v61
Mvaa 216,128,200
Ta 144,23,30848
v_lshlrev_b32 v60, 16, v62
Mvaa 220,132,200
Ta 146,23,35968
v_and_b32 v61, 0xffff0000, v62
Mvaa 212,132,184
Ta 148,24,30880
Ta 150,24,36000
W 4
Mvaa 208,136,188
v_lshlrev_b32 v62, 16, v63
v_and_b32 v63, 0xffff0000, v63
Mvaa 216,136,204
Ta 152,23,41088
v_lshlrev_b32 v64, 16, v68
Mvaa 220,140,204
Ta 154,23,46208
v_and_b32 v65, 0xffff0000, v68
Mvaa 212,140,188
Ta 156,24,41120
Ta 158,24,46240
W 4
Mvaa 208,144,192
v_lshlrev_b32 v66, 16, v69
v_and_b32 v67, 0xffff0000, v69
Mvaa 216,144,208
Ta 128,23,20672
v_lshlrev_b32 v68, 16, v70
Mvaa 220,148,208
Ta 130,23,25792
v_and_b32 v69, 0xffff0000, v70
Mvaa 212,148,192
Ta 132,24,20704
Ta 134,24,25824
W 4
Mvaa 208,152,196
v_lshlrev_b32 v70, 16, v71
v_and_b32 v71, 0xffff0000, v71
Mvaa 216,152,212
Ta 136,23,51392
v_lshlrev_b32 v72, 16, v76
Mvaa 220,156,212
Ta 138,23,56512
v_and_b32 v73, 0xffff0000, v76
Mvaa 212,156,196
Ta 140,24,51424
Ta 142,24,56544
W 4
Mvaa 224,128,96
v_lshlrev_b32 v74, 16, v77
v_and_b32 v75, 0xffff0000, v77
Mvaa 232,128,112
Ta 144,25,192
v_lshlrev_b32 v76, 16, v78
Mvaa 236,132,112
Ta 146,25,5312
v_and_b32 v77, 0xffff0000, v78
Mvaa 228,132,96
Ta 148,26,224
Ta 150,26,5344
W 4
Mvaa 224,136,100
v_lshlrev_b32 v78, 16, v79
v_and_b32 v79, 0xffff0000, v79
Mvaa 232,136,116
Ta 152,21,41152
Ta 154,21,46272
Mvaa 236,140,116
Ta 156,22,41184
Ta 158,22,46304
Mvaa 228,140,100
W 4
Ta 128,21,51392
K 208,208,209
Mvaa 224,144,104
Ta 130,21,56512
K 209,210,211
Mvaa 232,144,120
Ta 132,22,51424
K 210,212,213
Mvaa 236,148,120
Ta 134,22,56544
K 211,214,215
Mvaa 228,148,104
W 4
Ta 136,23,10432
Ta 138,23,15552
Mvaa 224,152,108
s_add_u32 s83, s52, 329728
buffer_store_dwordx4 v[208:211], v1, s[8:11], s83 offen sc1
K 216,216,217
Mvaa 232,152,124
K 217,218,219
K 218,220,221
Mvaa 236,156,124
Ta 140,24,10464
K 219,222,223
Mvaa 228,156,108
s_add_u32 s83, s52, 370688
Ta 142,24,15584
W 4
)"
R"(Mvaa 224,128,184
buffer_store_dwordx4 v[216:219], v1, s[8:11], s83 offen sc1
Ta 144,23,30912
Mvaa 232,128,200
Ta 146,23,36032
Ta 148,24,30944
Mvaa 236,132,200
Ta 150,24,36064
Ta 152,23,41152
Mvaa 228,132,184
W 5
Ta 154,23,46272
Ta 156,24,41184
Mvaa 224,136,188
Ta 158,24,46304
Ta 128,23,20736
Mvaa 232,136,204
Ta 130,23,25856
Ta 132,24,20768
Mvaa 236,140,204
Ta 134,24,25888
Mvaa 228,140,188
W 8
Ta 136,23,51456
Ta 138,23,56576
Mvaa 224,144,192
Mvaa 232,144,208
Ta 140,24,51488
Ta 142,24,56608
Mvaa 236,148,208
Mvaa 228,148,192
W 8
Ta 144,25,256
Ta 146,25,5376
Mvaa 224,152,196
Mvaa 232,152,212
Ta 148,26,288
Ta 150,26,5408
Mvaa 236,156,212
Mvaa 228,156,196
W 8
Ta 152,21,41216
Ta 154,21,46336
Mvaa 240,128,96
Mvaa 248,128,112
Ta 156,22,41248
Ta 158,22,46368
Mvaa 252,132,112
K 224,224,225
Mvaa 244,132,96
W 8
Ta 128,21,51456
Ta 130,21,56576
Mvaa 240,136,100
K 225,226,227
Mvaa 248,136,116
Ta 132,22,51488
K 226,228,229
Mvaa 252,140,116
Ta 134,22,56608
K 227,230,231
Mvaa 244,140,100
W 8
Ta 136,23,10496
Ta 138,23,15616
Mvaa 240,144,104
s_add_u32 s83, s52, 330752
Mvaa 248,144,120
buffer_store_dwordx4 v[224:227], v1, s[8:11], s83 offen sc1
K 232,232,233
Mvaa 252,148,120
K 233,234,235
K 234,236,237
Mvaa 244,148,104
W 6
Ta 140,24,10528
K 235,238,239
Mvaa 240,152,108
s_add_u32 s83, s52, 371712
Ta 142,24,15648
Ta 144,23,30976
Mvaa 248,152,124
buffer_store_dwordx4 v[232:235], v1, s[8:11], s83 offen sc1
Ta 146,23,36096
Mvaa 252,156,124
Ta 148,24,31008
Ta 150,24,36128
Mvaa 244,156,108
W 8
Ta 152,23,41216
Ta 154,23,46336
Mvaa 240,128,184
Mvaa 248,128,200
Ta 156,24,41248
Ta 158,24,46368
Mvaa 252,132,200
Mvaa 244,132,184
W 8
Mvaa 240,136,188
Mvaa 248,136,204
Mvaa 252,140,204
Mvaa 244,140,188
W 4
Mvaa 240,144,192
Mvaa 248,144,208
Mvaa 252,148,208
Mvaa 244,148,192
W 0
Mvaa 240,152,196
Mvaa 248,152,212
Mvaa 252,156,212
Mvaa 244,156,196
s_nop 4
K 240,240,241
K 241,242,243
s_nop 0
K 242,244,245
K 243,246,247
s_add_u32 s83, s52, 331776
buffer_store_dwordx4 v[240:243], v1, s[8:11], s83 offen sc1
K 248,248,249
K 249,250,251
K 250,252,253
K 251,254,255
s_add_u32 s83, s52, 372736
buffer_store_dwordx4 v[248:251], v1, s[8:11], s83 offen sc1
s_branch .Ltend_77%=
.Llate_76%=:
ds_read_b32 v11, v11 offset:59392
ds_read_b64 v[8:9], v10 offset:57344
Rv 176,3,0
Rv 180,3,1024
Rv 184,3,2048
Rv 188,3,3072
Rv 192,3,4096
Rv 196,3,5120
Rv 200,3,6144
Rv 204,3,7168
W 6
Mvvv 32,224,176
v_cmp_ne_u32 vcc, 0, v11
s_cmp_lg_u64 vcc, 0
s_cbranch_scc1 .Lredo%=
s_lshl_b32 s94, s58, 2
Mvvv 36,228,176
A 4,4,8
Mvvv 40,224,180
s_add_u32 s94, s94, s51
v_rcp_f32 v4, v4
Mvvv 44,228,180
W 4
s_add_u32 s94, s94, 7
A 5,5,9
Mvvv 48,224,184
Ta 128,25,10240
Ta 130,25,15360
Mvvv 52,228,184
s_and_b32 s94, s94, 15
v_rcp_f32 v5, v5
Mvvv 56,224,188
s_lshl_b32 s93, s58, 12
s_add_u32 s93, s93, s50
Ta 132,26,10272
Mvvv 60,228,188
W 5
s_mul_i32 s94, s94, 10240
Ta 134,26,15392
Mvvv 64,224,192
s_add_u32 s94, s94, s48
U 32,32,4
U 33,33,4
Mvvv 68,228,192
s_lshl_b32 s94, s94, 2
Ta 136,25,10304
U 34,34,4
Mvvv 72,224,196
s_mul_i32 s95, s51, 20480
Ta 138,25,15424
U 35,35,4
Mvvv 76,228,196
W 6
U 36,36,4
U 37,37,4
Mvvv 80,224,200
s_add_u32 s95, s95, s48
Ta 140,26,10336
Ta 142,26,15456
Mvvv 84,228,200
s_add_u32 m0, s95, 61440
U 38,38,4
U 39,39,4
Mvvv 88,224,204
K 32,32,33
K 33,34,35
K 34,36,37
K 35,38,39
buffer_store_dwordx4 v[32:35], v1, s[8:11], s93 offen offset:0 sc1
U 64,64,5
U 65,65,5
U 66,66,5
U 67,67,5
U 68,68,5
U 69,69,5
U 70,70,5
U 71,71,5
K 64,64,65
K 65,66,67
K 66,68,69
K 67,70,71
s_add_u32 s67, s93, 131072
Mvvv 92,228,204
W 4
Ta 144,25,10368
Ta 146,25,15488
Zvav 176,128,32
Ta 148,26,10400
Ta 150,26,15520
Zvav 184,128,64
Ta 152,25,10432
Ta 154,25,15552
Zvav 188,132,64
D 16,40,94,0
Ta 156,26,10464
Zvav 180,132,32
W 7
Ta 158,26,15584
Ta 128,25,10496
Zvav 192,136,32
Ta 130,25,15616
Zvav 200,136,64
Ta 132,26,10528
Ta 134,26,15648
Zvav 204,140,64
D 17,40,94,1024
Zvav 196,140,32
W 8
Ta 136,25,20480
Ta 138,25,25600
Zvav 208,144,32
Zvav 216,144,64
Ta 140,26,20512
Ta 142,26,25632
Zvav 220,148,64
D 18,40,94,2048
Zvav 212,148,32
W 8
Ta 144,25,20544
Ta 146,25,25664
Zvav 224,152,32
Zvav 232,152,64
Ta 148,26,20576
Ta 150,26,25696
Zvav 236,156,64
D 19,40,94,3072
Zvav 228,156,32
W 8
s_add_u32 m0, s95, 65536
Ta 152,25,20608
Zvav 240,128,32
Ta 154,25,25728
Zvav 248,128,64
Ta 156,26,20640
Ta 158,26,25760
Zvav 252,132,64
buffer_store_dwordx4 v[64:67], v1, s[8:11], s67 offen offset:0 sc1
U 40,40,4
U 41,41,4
U 42,42,4
U 43,43,4
U 44,44,4
U 45,45,4
U 46,46,4
U 47,47,4
K 40,40,41
K 41,42,43
K 42,44,45
K 43,46,47
buffer_store_dwordx4 v[40:43], v1, s[8:11], s93 offen offset:1024 sc1
U 72,72,5
U 73,73,5
U 74,74,5
U 75,75,5
U 76,76,5
U 77,77,5
U 78,78,5
U 79,79,5
K 72,72,73
K 73,74,75
K 74,76,77
K 75,78,79
Zvav 244,132,32
W 8
Ta 128,25,20672
Ta 130,25,25792
Mvav 176,136,40
Mvav 184,136,72
Ta 132,26,20704
Ta 134,26,25824
Mvav 188,140,72
buffer_load_dwordx4 v20, s[40:43], s94 offen lds
Mvav 180,140,40
W 8
s_lshl_b32 s94, s58, 2
s_add_u32 s94, s94, s51
Mvav 192,144,40
s_add_u32 s94, s94, 9
s_and_b32 s94, s94, 15
s_mul_i32 s94, s94, 10240
Mvav 200,144,72
s_add_u32 s94, s94, s48
s_lshl_b32 s94, s94, 2
s_mul_i32 s95, s51, 10240
Mvav 204,148,72
s_add_u32 s95, s95, s48
s_add_u32 m0, s95, 112640
s_nop 0
D 16,40,94,0
Mvav 196,148,40
W 4
Ta 136,25,20736
Ta 138,25,25856
Mvav 208,152,40
Ta 140,26,20768
Ta 142,26,25888
Mvav 216,152,72
Ta 144,25,30720
Ta 146,25,35840
Mvav 220,156,72
D 17,40,94,1024
Ta 148,26,30752
Mvav 212,156,40
W 7
Ta 150,26,35872
Ta 152,25,30784
Mvav 224,128,40
Ta 154,25,35904
Mvav 232,128,72
Ta 156,26,30816
Ta 158,26,35936
Mvav 236,132,72
buffer_store_dwordx4 v[72:75], v1, s[8:11], s67 offen offset:1024 sc1
U 48,48,4
Mvav 228,132,40
W 8
U 49,49,4
U 50,50,4
Mvav 240,136,40
U 51,51,4
U 52,52,4
Mvav 248,136,72
D 18,40,94,2048
D 19,40,94,3072
s_add_u32 m0, s95, 116736
s_nop 0
buffer_load_dwordx4 v20, s[40:43], s94 offen lds
s_waitcnt vmcnt(14)
s_cmp_lg_u32 s70, 0
s_cbranch_scc1 .Lpd_88%=
.Lpdr_89%=:
Mvav 252,140,72
U 53,53,4
U 54,54,4
U 55,55,4
K 48,48,49
K 49,50,51
K 50,52,53
K 51,54,55
buffer_store_dwordx4 v[48:51], v1, s[8:11], s93 offen offset:2048 sc1
U 80,80,5
U 81,81,5
U 82,82,5
U 83,83,5
U 84,84,5
U 85,85,5
U 86,86,5
U 87,87,5
K 80,80,81
K 81,82,83
K 82,84,85
K 83,86,87
Mvav 244,140,40
W 4
Ta 128,25,30848
Ta 130,25,35968
Mvav 176,144,48
Ta 132,26,30880
Ta 134,26,36000
Mvav 184,144,80
Ta 136,25,30912
Ta 138,25,36032
Mvav 188,148,80
buffer_store_dwordx4 v[80:83], v1, s[8:11], s67 offen offset:2048 sc1
U 56,56,4
Mvav 180,148,48
W 6
U 57,57,4
U 58,58,4
Mvav 192,152,48
U 59,59,4
v_bfe_u32 v28, v1, 4, 2
)"
R"(Mvav 200,152,80
U 60,60,4
v_lshlrev_b32 v28, 3, v28
Mvav 204,156,80
Ta 140,26,30944
Ta 142,26,36064
Mvav 196,156,48
W 4
buffer_load_dwordx2 v[28:29], v28, s[8:11], s54 offen sc1
U 61,61,4
Mvav 208,128,48
U 62,62,4
U 63,63,4
Mvav 216,128,80
Ta 144,25,30976
K 56,56,57
Mvav 220,132,80
Ta 146,25,36096
K 57,58,59
Mvav 212,132,48
Ta 148,26,31008
Ta 150,26,36128
W 4
Mvav 224,136,48
K 58,60,61
K 59,62,63
Mvav 232,136,80
buffer_store_dwordx4 v[56:59], v1, s[8:11], s93 offen offset:3072 sc1
U 88,88,5
Mvav 236,140,80
Ta 152,21,0
U 89,89,5
Mvav 228,140,48
Ta 154,21,5120
Ta 156,22,32
Ta 158,22,5152
W 4
Mvav 240,144,48
U 90,90,5
U 91,91,5
Mvav 248,144,80
U 92,92,5
U 93,93,5
Mvav 252,148,80
U 94,94,5
U 95,95,5
K 88,88,89
K 89,90,91
K 90,92,93
K 91,94,95
Mvav 244,148,48
Ta 128,21,64
Ta 130,21,5184
Ta 132,22,96
Ta 134,22,5216
W 4
Mvav 176,152,56
buffer_store_dwordx4 v[88:91], v1, s[8:11], s67 offen offset:3072 sc1
Ta 136,21,128
Mvav 184,152,88
Ta 138,21,5248
Ta 140,22,160
Mvav 188,156,88
Ta 142,22,5280
Ta 144,21,192
Mvav 180,156,56
W 5
Ta 146,21,5312
Ta 148,22,224
Mvav 192,128,56
Ta 150,22,5344
Ta 152,21,256
Mvav 200,128,88
Ta 154,21,5376
Ta 156,22,288
Mvav 204,132,88
Ta 158,22,5408
Mvav 196,132,56
W 8
Ta 128,25,10240
Ta 130,25,15360
Mvav 208,136,56
Mvav 216,136,88
Ta 132,26,10272
Ta 134,26,15392
Mvav 220,140,88
Mvav 212,140,56
W 8
Ta 136,25,10304
Ta 138,25,15424
Mvav 224,144,56
s_waitcnt vmcnt(2)
v_cmp_eq_u64 vcc, s[12:13], v[28:29]
s_cmp_eq_u64 vcc, -1
s_cbranch_scc0 .Lspin_90%=
.Lok_91%=:
s_add_u32 s81, s58, 0
Mvav 232,144,88
s_and_b32 s81, s81, 3
Ta 140,26,10336
Ta 142,26,15456
Mvav 236,148,88
s_lshl_b32 s81, s81, 12
Mvav 228,148,56
W 8
s_add_u32 s81, s81, s49
Ta 144,25,10368
Mvav 240,152,56
s_add_u32 s82, s81, 131072
Ta 146,25,15488
Mvav 248,152,88
buffer_load_dwordx4 a[184:187], v1, s[8:11], s81 offen offset:0 sc1
Ta 148,26,10400
Mvav 252,156,88
buffer_load_dwordx4 a[200:203], v1, s[8:11], s82 offen offset:0 sc1
Ta 150,26,15520
Mvav 244,156,56
s_waitcnt vmcnt(0) lgkmcnt(8)
Ta 152,25,10432
Ta 154,25,15552
Zvaa 96,128,184
Zvaa 104,128,200
Ta 156,26,10464
Ta 158,26,15584
Zvaa 108,132,200
buffer_load_dwordx4 a[188:191], v1, s[8:11], s81 offen offset:1024 sc1
Zvaa 100,132,184
W 8
Ta 128,25,10496
Ta 130,25,15616
Zvaa 112,136,184
Zvaa 120,136,200
Ta 132,26,10528
Ta 134,26,15648
Zvaa 124,140,200
buffer_load_dwordx4 a[204:207], v1, s[8:11], s82 offen offset:1024 sc1
Zvaa 116,140,184
W 8
Ta 136,25,20480
Ta 138,25,25600
Zvaa 128,144,184
Zvaa 136,144,200
Ta 140,26,20512
Ta 142,26,25632
Zvaa 140,148,200
buffer_load_dwordx4 a[192:195], v1, s[8:11], s81 offen offset:2048 sc1
Zvaa 132,148,184
W 8
Ta 144,25,20544
Ta 146,25,25664
Zvaa 144,152,184
Zvaa 152,152,200
Ta 148,26,20576
Ta 150,26,25696
Zvaa 156,156,200
buffer_load_dwordx4 a[208:211], v1, s[8:11], s82 offen offset:2048 sc1
Zvaa 148,156,184
W 8
Ta 152,25,20608
Ta 154,25,25728
Zvaa 160,128,184
Zvaa 168,128,200
Ta 156,26,20640
Ta 158,26,25760
Zvaa 172,132,200
buffer_load_dwordx4 a[196:199], v1, s[8:11], s81 offen offset:3072 sc1
Zvaa 164,132,184
s_waitcnt vmcnt(3) lgkmcnt(8)
Ta 128,25,20672
Ta 130,25,25792
Mvaa 96,136,188
Mvaa 104,136,204
Ta 132,26,20704
Ta 134,26,25824
Mvaa 108,140,204
buffer_load_dwordx4 a[212:215], v1, s[8:11], s82 offen offset:3072 sc1
Mvaa 100,140,188
W 8
s_lshl_b32 s80, s58, 3
s_add_u32 s80, s80, s55
Mvaa 112,144,188
Ta 136,25,20736
Ta 138,25,25856
Mvaa 120,144,204
Ta 140,26,20768
Ta 142,26,25888
Mvaa 124,148,204
v_mov_b64 v[28:29], s[12:13]
s_mov_b64 exec, 1
buffer_store_dwordx2 v[28:29], off, s[8:11], s80 sc1
s_mov_b64 exec, -1
Mvaa 116,148,188
W 8
Ta 144,25,30720
Ta 146,25,35840
Mvaa 128,152,188
Mvaa 136,152,204
Ta 148,26,30752
Ta 150,26,35872
Mvaa 140,156,204
Mvaa 132,156,188
W 8
Ta 152,25,30784
Ta 154,25,35904
Mvaa 144,128,188
Mvaa 152,128,204
Ta 156,26,30816
Ta 158,26,35936
Mvaa 156,132,204
s_barrier
s_lshl_b32 s94, s58, 2
Mvaa 148,132,188
W 8
Ta 128,25,30848
Ta 130,25,35968
Mvaa 160,136,188
s_add_u32 s94, s94, s51
Mvaa 168,136,204
s_add_u32 s94, s94, 11
Ta 132,26,30880
Ta 134,26,36000
Mvaa 172,140,204
s_and_b32 s94, s94, 15
Mvaa 164,140,188
s_waitcnt vmcnt(3) lgkmcnt(8)
Ta 136,25,30912
Ta 138,25,36032
Mvaa 96,144,192
s_mul_i32 s94, s94, 10240
Mvaa 104,144,208
s_add_u32 s94, s94, s48
Ta 140,26,30944
Ta 142,26,36064
Mvaa 108,148,208
s_lshl_b32 s94, s94, 2
Mvaa 100,148,192
W 8
Ta 144,25,30976
Ta 146,25,36096
Mvaa 112,152,192
s_mul_i32 s95, s51, 10240
Mvaa 120,152,208
s_add_u32 s95, s95, s48
Ta 148,26,31008
Ta 150,26,36128
Mvaa 124,156,208
s_add_u32 m0, s95, 40960
Mvaa 116,156,192
W 8
Ta 152,21,0
Ta 154,21,5120
Mvaa 128,128,192
D 16,40,94,0
Mvaa 136,128,208
Ta 156,22,32
Ta 158,22,5152
Mvaa 140,132,208
Mvaa 132,132,192
W 8
Ta 128,21,64
Ta 130,21,5184
Mvaa 144,136,192
D 17,40,94,1024
Mvaa 152,136,208
Ta 132,22,96
Ta 134,22,5216
Mvaa 156,140,208
Mvaa 148,140,192
W 8
Ta 136,21,128
Ta 138,21,5248
Mvaa 160,144,192
D 18,40,94,2048
Mvaa 168,144,208
Ta 140,22,160
Ta 142,22,5280
Mvaa 172,148,208
Mvaa 164,148,192
s_waitcnt vmcnt(4) lgkmcnt(8)
Ta 144,21,192
Ta 146,21,5312
Mvaa 96,152,196
D 19,40,94,3072
Mvaa 104,152,212
s_add_u32 m0, s95, 45056
Ta 148,22,224
Ta 150,22,5344
Mvaa 108,156,212
Mvaa 100,156,196
W 8
Ta 152,21,256
Ta 154,21,5376
Mvaa 112,128,196
buffer_load_dwordx4 v20, s[40:43], s94 offen lds
Mvaa 120,128,212
s_add_u32 s81, s58, 1
s_and_b32 s81, s81, 3
s_lshl_b32 s81, s81, 12
Mvaa 124,132,212
s_add_u32 s81, s81, s49
s_add_u32 s82, s81, 131072
Ta 156,22,288
Mvaa 116,132,196
W 7
Ta 158,22,5408
Ta 128,21,10240
Mvaa 128,136,196
buffer_load_dwordx4 a[216:219], v1, s[8:11], s81 offen offset:0 sc1
Ta 130,21,15360
Mvaa 136,136,212
Ta 132,22,10272
Ta 134,22,15392
Mvaa 140,140,212
Mvaa 132,140,196
W 8
Ta 136,21,10304
Ta 138,21,15424
Mvaa 144,144,196
buffer_load_dwordx4 a[232:235], v1, s[8:11], s82 offen offset:0 sc1
Mvaa 152,144,212
Ta 140,22,10336
Ta 142,22,15456
Mvaa 156,148,212
Mvaa 148,148,196
W 8
Ta 144,21,10368
Ta 146,21,15488
Mvaa 160,152,196
buffer_load_dwordx4 a[220:223], v1, s[8:11], s81 offen offset:1024 sc1
Mvaa 168,152,212
Ta 148,22,10400
Ta 150,22,15520
Mvaa 172,156,212
Mvaa 164,156,196
s_waitcnt vmcnt(1) lgkmcnt(8)
Ta 152,21,10432
Ta 154,21,15552
Mvaa 96,128,216
buffer_load_dwordx4 a[236:239], v1, s[8:11], s82 offen offset:1024 sc1
Mvaa 104,128,232
Ta 156,22,10464
Ta 158,22,15584
Mvaa 108,132,232
Mvaa 100,132,216
W 8
Ta 128,21,10496
Ta 130,21,15616
Mvaa 112,136,216
buffer_load_dwordx4 a[224:227], v1, s[8:11], s81 offen offset:2048 sc1
Mvaa 120,136,232
Ta 132,22,10528
Ta 134,22,15648
Mvaa 124,140,232
Mvaa 116,140,216
W 8
Ta 136,21,20480
Ta 138,21,25600
Mvaa 128,144,216
buffer_load_dwordx4 a[240:243], v1, s[8:11], s82 offen offset:2048 sc1
Mvaa 136,144,232
Ta 140,22,20512
Ta 142,22,25632
Mvaa 140,148,232
Mvaa 132,148,216
W 8
Ta 144,21,20544
Ta 146,21,25664
Mvaa 144,152,216
buffer_load_dwordx4 a[228:231], v1, s[8:11], s81 offen offset:3072 sc1
Mvaa 152,152,232
Ta 148,22,20576
Ta 150,22,25696
Mvaa 156,156,232
Mvaa 148,156,216
W 8
Ta 152,21,20608
Ta 154,21,25728
Mvaa 160,128,216
buffer_load_dwordx4 a[244:247], v1, s[8:11], s82 offen offset:3072 sc1
Mvaa 168,128,232
s_lshl_b32 s94, s58, 2
s_add_u32 s94, s94, s51
s_add_u32 s94, s94, 13
Mvaa 172,132,232
s_and_b32 s94, s94, 15
s_mul_i32 s94, s94, 10240
s_add_u32 s94, s94, s48
Mvaa 164,132,216
s_waitcnt vmcnt(4) lgkmcnt(6)
s_lshl_b32 s94, s94, 2
s_mul_i32 s95, s51, 20480
Mvaa 96,136,220
s_add_u32 s95, s95, s48
s_add_u32 m0, s95, 71680
s_nop 0
D 16,40,94,0
Mvaa 104,136,236
Ta 156,22,20640
Ta 158,22,25760
Mvaa 108,140,236
)"
R"(Ta 128,21,20672
Ta 130,21,25792
Mvaa 100,140,220
W 6
Ta 132,22,20704
Ta 134,22,25824
Mvaa 112,144,220
D 17,40,94,1024
Ta 136,21,20736
Mvaa 120,144,236
Ta 138,21,25856
Ta 140,22,20768
Mvaa 124,148,236
Ta 142,22,25888
Mvaa 116,148,220
W 8
Ta 144,21,30720
Ta 146,21,35840
Mvaa 128,152,220
D 18,40,94,2048
Mvaa 136,152,236
Ta 148,22,30752
Ta 150,22,35872
Mvaa 140,156,236
Mvaa 132,156,220
W 8
Ta 152,21,30784
Ta 154,21,35904
Mvaa 144,128,220
D 19,40,94,3072
Mvaa 152,128,236
s_add_u32 m0, s95, 75776
Ta 156,22,30816
Ta 158,22,35936
Mvaa 156,132,236
buffer_load_dwordx4 v20, s[40:43], s94 offen lds
s_lshl_b32 s94, s58, 2
s_add_u32 s94, s94, 15
s_and_b32 s94, s94, 15
s_mul_i32 s94, s94, 10240
s_add_u32 s94, s94, s48
s_lshl_b32 s94, s94, 2
s_mul_i32 s95, s51, 0
s_add_u32 s95, s95, s48
s_add_u32 m0, s95, 102400
s_nop 0
D 16,40,94,0
D 17,40,94,1024
D 18,40,94,2048
D 19,40,94,3072
s_add_u32 m0, s95, 106496
s_nop 0
buffer_load_dwordx4 v20, s[40:43], s94 offen lds
Mvaa 148,132,220
W 8
Ta 128,21,30848
Ta 130,21,35968
Mvaa 160,136,220
Mvaa 168,136,236
Ta 132,22,30880
Ta 134,22,36000
Mvaa 172,140,236
Mvaa 164,140,220
s_waitcnt vmcnt(12) lgkmcnt(8)
Ta 136,21,30912
Ta 138,21,36032
Mvaa 96,144,224
s_waitcnt vmcnt(0)
s_barrier
s_add_u32 s81, s58, 2
Mvaa 104,144,240
Ta 140,22,30944
v_and_b32 v27, 1, v12
Mvaa 108,148,240
Ta 142,22,36064
Mvaa 100,148,224
W 8
s_and_b32 s81, s81, 3
v_lshlrev_b32 v27, 1, v27
Mvaa 112,152,224
Ta 144,21,30976
Ta 146,21,36096
Mvaa 120,152,240
s_lshl_b32 s81, s81, 12
Ta 148,22,31008
Ta 150,22,36128
Mvaa 124,156,240
v_xor_b32 v27, v27, v13
Mvaa 116,156,224
W 8
Ta 152,23,0
Ta 154,23,5120
Mvaa 128,128,224
s_add_u32 s81, s81, s49
Mvaa 136,128,240
Ta 156,24,32
v_and_b32 v12, 2, v12
Mvaa 140,132,240
s_add_u32 s82, s81, 131072
Ta 158,24,5152
Mvaa 132,132,224
W 8
Ta 128,23,64
Ta 130,23,5184
Mvaa 144,136,224
v_or_b32 v12, v12, v14
Mvaa 152,136,240
buffer_load_dwordx4 a[0:3], v1, s[8:11], s81 offen offset:0 sc1
Ta 132,24,96
Mvaa 156,140,240
Ta 134,24,5216
Mvaa 148,140,224
W 8
Ta 136,23,128
v_lshl_or_b32 v12, v27, 2, v12
Mvaa 160,144,224
Ta 138,23,5248
Mvaa 168,144,240
buffer_load_dwordx4 a[16:19], v1, s[8:11], s82 offen offset:0 sc1
Ta 140,24,160
Mvaa 172,148,240
Ta 142,24,5280
v_cmp_eq_u32_e64 s[86:87], 0, v12
Mvaa 164,148,224
W 8
Ta 144,23,192
Ta 146,23,5312
Mvaa 96,152,228
Mvaa 104,152,244
buffer_load_dwordx4 a[4:7], v1, s[8:11], s81 offen offset:1024 sc1
v_cndmask_b32_e64 v4, 0, v15, s[86:87]
Mvaa 108,156,244
Ta 148,24,224
Ta 150,24,5344
Mvaa 100,156,228
W 8
Ta 152,23,256
Ta 154,23,5376
Mvaa 112,128,228
v_cmp_eq_u32_e64 s[86:87], 1, v12
Mvaa 120,128,244
buffer_load_dwordx4 a[20:23], v1, s[8:11], s82 offen offset:1024 sc1
Ta 156,24,288
Mvaa 124,132,244
Ta 158,24,5408
v_cndmask_b32_e64 v5, 0, v15, s[86:87]
Mvaa 116,132,228
W 8
Ta 128,23,20480
Ta 130,23,25600
Mvaa 128,136,228
Mvaa 136,136,244
buffer_load_dwordx4 a[8:11], v1, s[8:11], s81 offen offset:2048 sc1
v_cmp_eq_u32_e64 s[86:87], 2, v12
Mvaa 140,140,244
Ta 132,24,20512
Ta 134,24,25632
Mvaa 132,140,228
W 8
Ta 136,23,20544
Ta 138,23,25664
Mvaa 144,144,228
v_cndmask_b32_e64 v6, 0, v15, s[86:87]
Mvaa 152,144,244
buffer_load_dwordx4 a[24:27], v1, s[8:11], s82 offen offset:2048 sc1
Ta 140,24,20576
Mvaa 156,148,244
Ta 142,24,25696
Mvaa 148,148,228
W 8
Ta 144,23,20608
v_cmp_eq_u32_e64 s[86:87], 3, v12
Mvaa 160,152,228
Ta 146,23,25728
Mvaa 168,152,244
buffer_load_dwordx4 a[12:15], v1, s[8:11], s81 offen offset:3072 sc1
Ta 148,24,20640
Mvaa 172,156,244
Ta 150,24,25760
v_cndmask_b32_e64 v7, 0, v15, s[86:87]
Mvaa 164,156,228
s_waitcnt vmcnt(5) lgkmcnt(8)
Ta 152,23,20672
Ta 154,23,25792
Mvaa 96,128,0
Mvaa 104,128,16
buffer_load_dwordx4 a[28:31], v1, s[8:11], s82 offen offset:3072 sc1
v_cmp_eq_u32_e64 s[86:87], 4, v12
Mvaa 108,132,16
s_add_u32 s81, s58, 3
s_and_b32 s81, s81, 3
s_lshl_b32 s81, s81, 12
Mvaa 100,132,0
W 6
s_add_u32 s81, s81, s49
Ta 156,24,20704
Mvaa 112,136,0
s_add_u32 s82, s81, 131072
Ta 158,24,25824
Ta 128,23,20736
Mvaa 120,136,16
Ta 130,23,25856
v_cndmask_b32_e64 v8, 0, v15, s[86:87]
Mvaa 124,140,16
buffer_load_dwordx4 a[32:35], v1, s[8:11], s81 offen offset:0 sc1
Ta 132,24,20768
Mvaa 116,140,0
W 7
Ta 134,24,25888
v_cmp_eq_u32_e64 s[86:87], 5, v12
Mvaa 128,144,0
Ta 136,23,51200
Ta 138,23,56320
Mvaa 136,144,16
Ta 140,24,51232
Ta 142,24,56352
Mvaa 140,148,16
buffer_load_dwordx4 a[48:51], v1, s[8:11], s82 offen offset:0 sc1
v_cndmask_b32_e64 v9, 0, v15, s[86:87]
Mvaa 132,148,0
W 8
Ta 144,23,51264
Ta 146,23,56384
Mvaa 144,152,0
v_cmp_eq_u32_e64 s[86:87], 6, v12
Mvaa 152,152,16
Ta 148,24,51296
Ta 150,24,56416
Mvaa 156,156,16
buffer_load_dwordx4 a[36:39], v1, s[8:11], s81 offen offset:1024 sc1
Mvaa 148,156,0
W 8
Ta 152,23,51328
v_cndmask_b32_e64 v10, 0, v15, s[86:87]
Mvaa 160,128,0
Ta 154,23,56448
Mvaa 168,128,16
Ta 156,24,51360
Ta 158,24,56480
Mvaa 172,132,16
buffer_load_dwordx4 a[52:55], v1, s[8:11], s82 offen offset:1024 sc1
v_cmp_eq_u32_e64 s[86:87], 7, v12
Mvaa 164,132,0
s_waitcnt vmcnt(8) lgkmcnt(8)
Ta 128,23,51392
Ta 130,23,56512
Mvaa 96,136,4
Mvaa 104,136,20
Ta 132,24,51424
v_cndmask_b32_e64 v11, 0, v15, s[86:87]
Mvaa 108,140,20
buffer_load_dwordx4 a[40:43], v1, s[8:11], s81 offen offset:2048 sc1
Ta 134,24,56544
Mvaa 100,140,4
W 8
Ta 136,23,51456
Ta 138,23,56576
Mvaa 112,144,4
s_mov_b32 s20, s38
Mvaa 120,144,20
Ta 140,24,51488
Ta 142,24,56608
Mvaa 124,148,20
buffer_load_dwordx4 a[56:59], v1, s[8:11], s82 offen offset:2048 sc1
Mvaa 116,148,4
W 8
s_and_b32 s21, s39, 0xffff
Ta 144,25,0
Mvaa 128,152,4
Ta 146,25,5120
Mvaa 136,152,20
Ta 148,26,32
Ta 150,26,5152
Mvaa 140,156,20
s_mov_b32 s22, 2621440
buffer_load_dwordx4 a[44:47], v1, s[8:11], s81 offen offset:3072 sc1
Mvaa 132,156,4
W 8
Ta 152,25,64
Ta 154,25,5184
Mvaa 144,128,4
Mvaa 152,128,20
s_mov_b32 s23, 0x20000
Ta 156,26,96
Ta 158,26,5216
Mvaa 156,132,20
buffer_load_dwordx4 a[60:63], v1, s[8:11], s82 offen offset:3072 sc1
v_bfe_u32 v28, v1, 4, 2
Mvaa 148,132,4
W 8
Ta 128,25,128
v_lshlrev_b32 v28, 3, v28
Mvaa 160,136,4
s_and_b32 s17, s17, 0xffff
Ta 130,25,5248
Mvaa 168,136,20
Ta 132,26,160
Ta 134,26,5280
Mvaa 172,140,20
s_mov_b32 s18, 2621440
buffer_load_dwordx2 v[28:29], v28, s[8:11], s55 offen sc1
Mvaa 164,140,4
s_waitcnt vmcnt(11) lgkmcnt(8)
Ta 136,25,192
Ta 138,25,5312
Mvaa 96,144,8
Mvaa 104,144,24
s_mov_b32 s19, 0x20000
Ta 140,26,224
Ta 142,26,5344
Mvaa 108,148,24
Mvaa 100,148,8
W 8
Ta 144,25,256
Ta 146,25,5376
Mvaa 112,152,8
s_lshr_b32 s66, s15, 3
Mvaa 120,152,24
Ta 148,26,288
Ta 150,26,5408
Mvaa 124,156,24
Mvaa 116,156,8
W 8
s_lshl_b32 s66, s66, 4
Ta 152,21,40960
Mvaa 128,128,8
Ta 154,21,46080
Mvaa 136,128,24
Ta 156,22,40992
Ta 158,22,46112
Mvaa 140,132,24
s_add_u32 s66, s66, s59
Mvaa 132,132,8
W 8
Ta 128,21,41024
Ta 130,21,46144
Mvaa 144,136,8
Mvaa 152,136,24
s_lshl_b32 s63, s66, 6
Ta 132,22,41056
Ta 134,22,46176
Mvaa 156,140,24
Mvaa 148,140,8
W 8
Ta 136,21,41088
Ta 138,21,46208
Mvaa 160,144,8
s_add_u32 s63, s63, 65536
Mvaa 168,144,24
Ta 140,22,41120
Ta 142,22,46240
Mvaa 172,148,24
Mvaa 164,148,8
s_waitcnt vmcnt(9) lgkmcnt(8)
s_mul_i32 s62, s66, 40960
Ta 144,21,41152
Mvaa 96,152,12
Ta 146,21,46272
Mvaa 104,152,28
Ta 148,22,41184
Ta 150,22,46304
Mvaa 108,156,28
s_add_u32 s62, s62, 56623104
Mvaa 100,156,12
W 8
Ta 152,21,41216
Ta 154,21,46336
Mvaa 112,128,12
s_lshl_b32 s66, s14, 10
Mvaa 120,128,28
Ta 156,22,41248
Ta 158,22,46368
Mvaa 124,132,28
Mvaa 116,132,12
W 8
s_add_u32 s78, s62, s66
Ta 128,21,51200
Mvaa 128,136,12
Ta 130,21,56320
Mvaa 136,136,28
Ta 132,22,51232
Ta 134,22,56352
Mvaa 140,140,28
s_add_u32 s79, s78, 327680
s_waitcnt vmcnt(0)
v_cmp_eq_u64 vcc, s[12:13], v[28:29]
s_cmp_eq_u64 vcc, -1
s_cbranch_scc0 .Lspin_93%=
.Lok_94%=:
s_add_u32 s81, s58, 1
Mvaa 132,140,12
W 8
Ta 136,21,51264
Ta 138,21,56384
Mvaa 144,144,12
Mvaa 152,144,28
s_and_b32 s66, s14, 1
Ta 140,22,51296
Ta 142,22,56416
Mvaa 156,148,28
)"
R"(s_and_b32 s81, s81, 3
Mvaa 148,148,12
W 8
Ta 144,21,51328
Ta 146,21,56448
Mvaa 160,152,12
s_mul_i32 s66, s66, 327680
Mvaa 168,152,28
s_lshl_b32 s81, s81, 12
Ta 148,22,51360
Ta 150,22,56480
Mvaa 172,156,28
Mvaa 164,156,12
W 8
s_add_u32 s65, s62, s66
Ta 152,21,51392
Mvaa 96,128,32
s_add_u32 s81, s81, s50
Ta 154,21,56512
Mvaa 104,128,48
Ta 156,22,51424
Ta 158,22,56544
Mvaa 108,132,48
s_add_u32 s65, s65, 4096
Mvaa 100,132,32
W 8
s_add_u32 s82, s81, 131072
Ta 128,21,51456
Mvaa 112,136,32
Ta 130,21,56576
Mvaa 120,136,48
s_mov_b32 s64, 0
Ta 132,22,51488
Ta 134,22,56608
Mvaa 124,140,48
buffer_load_dwordx4 a[64:67], v1, s[8:11], s81 offen offset:0 sc1
Mvaa 116,140,32
W 8
Ta 136,23,10240
Ta 138,23,15360
Mvaa 128,144,32
v_bfe_u32 v27, v1, 4, 3
Mvaa 136,144,48
Ta 140,24,10272
Ta 142,24,15392
Mvaa 140,148,48
buffer_load_dwordx4 a[80:83], v1, s[8:11], s82 offen offset:0 sc1
v_lshlrev_b32 v27, 3, v27
Mvaa 132,148,32
W 8
Ta 144,23,10304
Ta 146,23,15424
Mvaa 144,152,32
Mvaa 152,152,48
Ta 148,24,10336
v_bfe_u32 v20, v1, 4, 4
Mvaa 156,156,48
buffer_load_dwordx4 a[68:71], v1, s[8:11], s81 offen offset:1024 sc1
Ta 150,24,15456
Mvaa 148,156,32
W 8
Ta 152,23,10368
Ta 154,23,15488
Mvaa 160,128,32
v_mul_u32_u24 v20, 1280, v20
Mvaa 168,128,48
Ta 156,24,10400
Ta 158,24,15520
Mvaa 172,132,48
buffer_load_dwordx4 a[84:87], v1, s[8:11], s82 offen offset:1024 sc1
Mvaa 164,132,32
W 8
Ta 128,23,10432
v_lshrrev_b32 v12, 8, v1
Mvaa 96,136,36
Ta 130,23,15552
Mvaa 104,136,52
Ta 132,24,10464
Ta 134,24,15584
Mvaa 108,140,52
buffer_load_dwordx4 a[72:75], v1, s[8:11], s81 offen offset:2048 sc1
v_lshl_add_u32 v20, v12, 4, v20
Mvaa 100,140,36
W 8
Ta 136,23,10496
Ta 138,23,15616
Mvaa 112,144,36
Mvaa 120,144,52
s_bfe_u32 s66, s59, 0x10001
Ta 140,24,10528
Ta 142,24,15648
Mvaa 124,148,52
buffer_load_dwordx4 a[88:91], v1, s[8:11], s82 offen offset:2048 sc1
Mvaa 116,148,36
W 8
Ta 144,23,30720
Ta 146,23,35840
Mvaa 128,152,36
s_lshl_b32 s67, s57, 1
Mvaa 136,152,52
Ta 148,24,30752
Ta 150,24,35872
Mvaa 140,156,52
buffer_load_dwordx4 a[76:79], v1, s[8:11], s81 offen offset:3072 sc1
Mvaa 132,156,36
W 8
s_add_u32 s66, s66, s67
Ta 152,23,30784
Mvaa 144,128,36
Ta 154,23,35904
Mvaa 152,128,52
Ta 156,24,30816
Ta 158,24,35936
Mvaa 156,132,52
s_lshl_b32 s61, s66, 2
buffer_load_dwordx4 a[92:95], v1, s[8:11], s82 offen offset:3072 sc1
Mvaa 148,132,36
W 8
s_add_u32 s81, s58, 2
s_and_b32 s81, s81, 3
Mvaa 160,136,36
s_lshr_b32 s66, s59, 2
Ta 128,23,30848
Ta 130,23,35968
Mvaa 168,136,52
s_lshl_b32 s81, s81, 12
Ta 132,24,30880
Ta 134,24,36000
Mvaa 172,140,52
Mvaa 164,140,36
W 8
s_lshl_b32 s66, s66, 1
Ta 136,23,30912
Mvaa 96,144,40
s_add_u32 s81, s81, s50
Ta 138,23,36032
Mvaa 104,144,56
Ta 140,24,30944
Ta 142,24,36064
Mvaa 108,148,56
s_add_u32 s61, s61, s66
Mvaa 100,148,40
W 8
s_add_u32 s82, s81, 131072
Ta 144,23,30976
Mvaa 112,152,40
Ta 146,23,36096
Mvaa 120,152,56
s_and_b32 s66, s59, 1
Ta 148,24,31008
Ta 150,24,36128
Mvaa 124,156,56
buffer_load_dwordx4 a[96:99], v1, s[8:11], s81 offen offset:0 sc1
Mvaa 116,156,40
W 8
Ta 152,23,40960
Ta 154,23,46080
Mvaa 128,128,40
s_add_u32 s61, s61, s66
Mvaa 136,128,56
Ta 156,24,40992
Ta 158,24,46112
Mvaa 140,132,56
buffer_load_dwordx4 a[112:115], v1, s[8:11], s82 offen offset:0 sc1
Mvaa 132,132,40
W 8
s_xor_b32 s67, s61, 0
Ta 128,23,41024
Mvaa 144,136,40
Ta 130,23,46144
Mvaa 152,136,56
Ta 132,24,41056
Ta 134,24,46176
Mvaa 156,140,56
s_mul_i32 s72, s67, 20480
buffer_load_dwordx4 a[100:103], v1, s[8:11], s81 offen offset:1024 sc1
Mvaa 148,140,40
W 8
Ta 136,23,41088
Ta 138,23,46208
Mvaa 160,144,40
Mvaa 168,144,56
s_mul_i32 s66, s58, 320
Ta 140,24,41120
Ta 142,24,46240
Mvaa 172,148,56
buffer_load_dwordx4 a[116:119], v1, s[8:11], s82 offen offset:1024 sc1
Mvaa 164,148,40
W 8
Ta 144,23,41152
Ta 146,23,46272
Mvaa 96,152,44
s_add_u32 s72, s72, s66
Mvaa 104,152,60
Ta 148,24,41184
Ta 150,24,46304
Mvaa 108,156,60
s_mul_i32 s75, s67, 20480
buffer_load_dwordx4 a[104:107], v1, s[8:11], s81 offen offset:2048 sc1
Mvaa 100,156,44
W 8
Ta 152,23,41216
Ta 154,23,46336
Mvaa 112,128,44
Mvaa 120,128,60
s_mul_i32 s66, s58, 2560
Ta 156,24,41248
Ta 158,24,46368
Mvaa 124,132,60
buffer_load_dwordx4 a[120:123], v1, s[8:11], s82 offen offset:2048 sc1
Mvaa 116,132,44
W 8
Ta 128,21,10240
Ta 130,21,15360
Mvaa 128,136,44
s_add_u32 s75, s75, s66
Mvaa 136,136,60
Ta 132,22,10272
Ta 134,22,15392
Mvaa 140,140,60
buffer_load_dwordx4 a[108:111], v1, s[8:11], s81 offen offset:3072 sc1
K 96,96,97
Mvaa 132,140,44
W 8
s_add_u32 s75, s75, 131072
Ta 136,21,10304
Mvaa 144,144,44
Ta 138,21,15424
K 97,98,99
Mvaa 152,144,60
Ta 140,22,10336
Ta 142,22,15456
Mvaa 156,148,60
s_xor_b32 s67, s61, 124
buffer_load_dwordx4 a[124:127], v1, s[8:11], s82 offen offset:3072 sc1
K 98,100,101
Mvaa 148,148,44
W 8
s_add_u32 s81, s58, 3
s_and_b32 s81, s81, 3
Mvaa 160,152,44
Ta 144,21,10368
Ta 146,21,15488
Mvaa 168,152,60
s_lshl_b32 s81, s81, 12
s_mul_i32 s73, s67, 20480
K 99,102,103
Mvaa 172,156,60
s_add_u32 s83, s52, 0
Ta 148,22,10400
Ta 150,22,15520
Mvaa 164,156,44
s_waitcnt vmcnt(14) lgkmcnt(8)
buffer_store_dwordx4 v[96:99], v1, s[8:11], s83 offen sc1
Ta 152,21,10432
Mvaa 176,128,64
s_add_u32 s81, s81, s50
s_mul_i32 s66, s58, 320
Ta 154,21,15552
Mvaa 184,128,80
Ta 156,22,10464
K 104,104,105
Mvaa 188,132,80
Ta 158,22,15584
K 105,106,107
Mvaa 180,132,64
W 8
s_add_u32 s73, s73, s66
s_add_u32 s82, s81, 131072
Mvaa 192,136,64
Ta 128,21,10496
Ta 130,21,15616
Mvaa 200,136,80
Ta 132,22,10528
K 106,108,109
Mvaa 204,140,80
s_mul_i32 s76, s67, 20480
Ta 134,22,15648
K 107,110,111
Mvaa 196,140,64
W 8
s_add_u32 s83, s52, 40960
buffer_load_dwordx4 a[184:187], v1, s[8:11], s81 offen offset:0 sc1
Mvaa 208,144,64
s_mul_i32 s66, s58, 2560
Ta 136,21,20480
Ta 138,21,25600
Mvaa 216,144,80
Ta 140,22,20512
Ta 142,22,25632
Mvaa 220,148,80
Mvaa 212,148,64
W 8
buffer_store_dwordx4 v[104:107], v1, s[8:11], s83 offen sc1
K 112,112,113
Mvaa 224,152,64
s_add_u32 s76, s76, s66
Ta 144,21,20544
Ta 146,21,25664
Mvaa 232,152,80
Ta 148,22,20576
K 113,114,115
Mvaa 236,156,80
s_add_u32 s76, s76, 131072
Ta 150,22,25696
K 114,116,117
Mvaa 228,156,64
W 8
buffer_load_dwordx4 a[200:203], v1, s[8:11], s82 offen offset:0 sc1
Ta 152,21,20608
Mvaa 240,128,64
Ta 154,21,25728
K 115,118,119
Mvaa 248,128,80
s_add_u32 s83, s52, 1024
Ta 156,22,20640
v_bfe_u32 v15, v1, 7, 1
Mvaa 252,132,80
Ta 158,22,25760
Mvaa 244,132,64
s_waitcnt vmcnt(16) lgkmcnt(8)
buffer_load_dwordx4 a[188:191], v1, s[8:11], s81 offen offset:1024 sc1
Ta 128,21,20672
Mvaa 176,136,68
Ta 130,21,25792
v_mul_u32_u24 v14, 10240, v15
Mvaa 184,136,84
Ta 132,22,20704
Ta 134,22,25824
Mvaa 188,140,84
Mvaa 180,140,68
W 8
buffer_load_dwordx4 a[204:207], v1, s[8:11], s82 offen offset:1024 sc1
v_lshrrev_b32 v15, 8, v1
Mvaa 192,144,68
Ta 136,21,20736
Ta 138,21,25856
Mvaa 200,144,84
Ta 140,22,20768
Ta 142,22,25888
Mvaa 204,148,84
v_lshl_add_u32 v14, v15, 7, v14
Mvaa 196,148,68
W 8
buffer_load_dwordx4 a[192:195], v1, s[8:11], s81 offen offset:2048 sc1
Ta 144,21,30720
Mvaa 208,152,68
Ta 146,21,35840
Mvaa 216,152,84
Ta 148,22,30752
v_and_b32 v15, 0x70, v1
Mvaa 220,156,84
Ta 150,22,35872
Mvaa 212,156,68
W 8
buffer_store_dwordx4 v[112:115], v1, s[8:11], s83 offen sc1
K 120,120,121
Mvaa 224,128,68
K 121,122,123
K 122,124,125
Mvaa 232,128,84
Ta 152,21,30784
K 123,126,127
Mvaa 236,132,84
s_add_u32 s83, s52, 41984
Ta 154,21,35904
v_add_u32 v14, v14, v15
Mvaa 228,132,68
W 6
buffer_load_dwordx4 a[208:211], v1, s[8:11], s82 offen offset:2048 sc1
Ta 156,22,30816
Mvaa 240,136,68
s_bitcmp1_b32 s14, 0
s_cselect_b32 s74, s73, s72
s_cselect_b32 s77, s76, s75
Ta 158,22,35936
Ta 128,21,30848
Mvaa 248,136,84
s_add_u32 s74, s74, 256
Ta 130,21,35968
Ta 132,22,30880
Mvaa 252,140,84
Ta 134,22,36000
Mvaa 244,140,68
s_waitcnt vmcnt(19) lgkmcnt(8)
buffer_load_dwordx4 a[196:199], v1, s[8:11], s81 offen offset:3072 sc1
Ta 136,21,30912
Mvaa 176,144,72
s_add_u32 s77, s77, 2048
Ta 138,21,36032
Mvaa 184,144,88
Ta 140,22,30944
Ta 142,22,36064
Mvaa 188,148,88
Mvaa 180,148,72
W 8
s_lshl_b32 s66, s14, 6
buffer_load_dwordx4 a[212:215], v1, s[8:11], s82 offen offset:3072 sc1
Mvaa 192,152,72
Ta 144,21,30976
)"
R"(Ta 146,21,36096
Mvaa 200,152,88
Ta 148,22,31008
Ta 150,22,36128
Mvaa 204,156,88
s_lshl_b32 s67, s14, 9
Mvaa 196,156,72
W 8
buffer_store_dwordx4 v[120:123], v1, s[8:11], s83 offen sc1
K 128,128,129
Mvaa 208,128,72
K 129,130,131
K 130,132,133
Mvaa 216,128,88
s_add_u32 s72, s72, s66
Ta 152,23,0
K 131,134,135
Mvaa 220,132,88
s_add_u32 s83, s52, 2048
Ta 154,23,5120
Ta 156,24,32
Mvaa 212,132,72
W 7
buffer_store_dwordx4 v[128:131], v1, s[8:11], s83 offen sc1
K 136,136,137
Mvaa 224,136,72
s_add_u32 s75, s75, s67
K 137,138,139
K 138,140,141
Mvaa 232,136,88
Ta 158,24,5152
K 139,142,143
Mvaa 236,140,88
s_add_u32 s83, s52, 43008
Ta 128,23,64
Ta 130,23,5184
Mvaa 228,140,72
W 6
buffer_store_dwordx4 v[136:139], v1, s[8:11], s83 offen sc1
K 144,144,145
Mvaa 240,144,72
s_add_u32 s73, s73, s66
Ta 132,24,96
K 145,146,147
Mvaa 248,144,88
Ta 134,24,5216
K 146,148,149
Mvaa 252,148,88
s_add_u32 s76, s76, s67
Ta 136,23,128
Ta 138,23,5248
Mvaa 244,148,72
s_waitcnt vmcnt(22) lgkmcnt(6)
Ta 140,24,160
K 147,150,151
Mvaa 176,152,76
s_add_u32 s83, s52, 3072
Ta 142,24,5280
buffer_store_dwordx4 v[144:147], v1, s[8:11], s83 offen sc1
Mvaa 184,152,92
Ta 144,23,192
Ta 146,23,5312
Mvaa 188,156,92
Ta 148,24,224
K 152,152,153
Mvaa 180,156,76
W 7
Ta 150,24,5344
K 153,154,155
Mvaa 192,128,76
buffer_load_dwordx4 v[36:39], v20, s[16:19], s72 offen
Ta 152,23,256
Mvaa 200,128,92
Ta 154,23,5376
K 154,156,157
Mvaa 204,132,92
Ta 156,24,288
Ta 158,24,5408
Mvaa 196,132,76
W 8
Ta 128,23,20480
K 155,158,159
Mvaa 208,136,76
s_add_u32 s83, s52, 44032
buffer_load_dwordx4 v[44:47], v14, s[8:11], s75 offen sc1
Ta 130,23,25600
Mvaa 216,136,92
Ta 132,24,20512
Ta 134,24,25632
Mvaa 220,140,92
Mvaa 212,140,76
W 8
Ta 136,23,51200
Ta 138,23,56320
Mvaa 224,144,76
buffer_store_dwordx4 v[152:155], v1, s[8:11], s83 offen sc1
K 160,160,161
Mvaa 232,144,92
Ta 140,24,51232
K 161,162,163
Mvaa 236,148,92
Ta 142,24,56352
K 162,164,165
Mvaa 228,148,76
W 8
Ta 144,25,0
Ta 146,25,5120
Mvaa 240,152,76
buffer_load_dwordx4 v[52:55], v20, s[16:19], s73 offen
K 163,166,167
Mvaa 248,152,92
s_add_u32 s83, s52, 4096
Ta 148,26,32
Ta 150,26,5152
Mvaa 252,156,92
Mvaa 244,156,76
s_waitcnt vmcnt(25) lgkmcnt(8)
Ta 152,21,40960
Ta 154,21,46080
Mvaa 176,128,96
buffer_load_dwordx4 v[60:63], v14, s[8:11], s76 offen sc1
Mvaa 184,128,112
Ta 156,22,40992
Ta 158,22,46112
Mvaa 188,132,112
Mvaa 180,132,96
s_waitcnt vmcnt(24) lgkmcnt(8)
Ta 128,21,51200
Ta 130,21,56320
Mvaa 176,136,100
buffer_store_dwordx4 v[160:163], v1, s[8:11], s83 offen sc1
K 168,168,169
Mvaa 184,136,116
K 169,170,171
K 170,172,173
Mvaa 188,140,116
Ta 132,22,51232
Ta 134,22,56352
Mvaa 180,140,100
s_waitcnt vmcnt(23) lgkmcnt(8)
Ta 136,23,10240
K 171,174,175
Mvaa 176,144,104
s_add_u32 s83, s52, 45056
buffer_load_dwordx4 v[68:71], v20, s[16:19], s74 offen
Ta 138,23,15360
Mvaa 184,144,120
Ta 140,24,10272
Ta 142,24,15392
Mvaa 188,148,120
Mvaa 180,148,104
s_waitcnt vmcnt(22) lgkmcnt(8)
Ta 144,23,30720
Ta 146,23,35840
Mvaa 176,152,108
buffer_store_dwordx4 v[168:171], v1, s[8:11], s83 offen sc1
Mvaa 184,152,124
Ta 148,24,30752
Ta 150,24,35872
Mvaa 188,156,124
Mvaa 180,156,108
s_waitcnt vmcnt(19) lgkmcnt(8)
Ta 152,23,40960
Ta 154,23,46080
Mvaa 176,128,184
buffer_load_dwordx4 v[76:79], v14, s[8:11], s77 offen sc1
Mvaa 184,128,200
Ta 156,24,40992
Ta 158,24,46112
Mvaa 188,132,200
Mvaa 180,132,184
s_waitcnt vmcnt(18) lgkmcnt(8)
Ta 128,23,20544
Ta 130,23,25664
Mvaa 176,136,188
buffer_load_dwordx2 v[12:13], v27, s[8:11], 0 offen offset:3584 sc1
Mvaa 184,136,204
Ta 132,24,20576
Ta 134,24,25696
Mvaa 188,140,204
Mvaa 180,140,188
s_waitcnt vmcnt(16) lgkmcnt(8)
Ta 136,23,51264
Ta 138,23,56384
Mvaa 176,144,192
Mvaa 184,144,208
Ta 140,24,51296
Ta 142,24,56416
Mvaa 188,148,208
Mvaa 180,148,192
s_waitcnt vmcnt(14) lgkmcnt(8)
Ta 144,25,64
Ta 146,25,5184
Mvaa 176,152,196
Mvaa 184,152,212
Ta 148,26,96
Ta 150,26,5216
Mvaa 188,156,212
Mvaa 180,156,196
W 8
Ta 152,21,41024
Ta 154,21,46144
Mvaa 192,128,96
Mvaa 200,128,112
Ta 156,22,41056
Ta 158,22,46176
Mvaa 204,132,112
K 176,176,177
Mvaa 196,132,96
W 8
Ta 128,21,51264
Ta 130,21,56384
Mvaa 192,136,100
K 177,178,179
Mvaa 200,136,116
Ta 132,22,51296
K 178,180,181
Mvaa 204,140,116
Ta 134,22,56416
K 179,182,183
Mvaa 196,140,100
W 8
Ta 136,23,10304
Ta 138,23,15424
Mvaa 192,144,104
s_add_u32 s83, s52, 327680
Mvaa 200,144,120
buffer_store_dwordx4 v[176:179], v1, s[8:11], s83 offen sc1
K 184,184,185
Mvaa 204,148,120
K 185,186,187
K 186,188,189
Mvaa 196,148,104
W 6
Ta 140,24,10336
K 187,190,191
Mvaa 192,152,108
s_add_u32 s83, s52, 368640
Ta 142,24,15456
Ta 144,23,30784
Mvaa 200,152,124
buffer_store_dwordx4 v[184:187], v1, s[8:11], s83 offen sc1
Ta 146,23,35904
Mvaa 204,156,124
Ta 148,24,30816
Ta 150,24,35936
Mvaa 196,156,108
W 8
Ta 152,23,41024
Ta 154,23,46144
Mvaa 192,128,184
Mvaa 200,128,200
Ta 156,24,41056
Ta 158,24,46176
Mvaa 204,132,200
Mvaa 196,132,184
W 8
Ta 128,23,20608
Ta 130,23,25728
Mvaa 192,136,188
Mvaa 200,136,204
Ta 132,24,20640
Ta 134,24,25760
Mvaa 204,140,204
Mvaa 196,140,188
W 8
Ta 136,23,51328
Ta 138,23,56448
Mvaa 192,144,192
Mvaa 200,144,208
Ta 140,24,51360
Ta 142,24,56480
Mvaa 204,148,208
Mvaa 196,148,192
W 8
Ta 144,25,128
Ta 146,25,5248
Mvaa 192,152,196
Mvaa 200,152,212
Ta 148,26,160
Ta 150,26,5280
Mvaa 204,156,212
Mvaa 196,156,196
W 8
Ta 152,21,41088
Ta 154,21,46208
Mvaa 208,128,96
Mvaa 216,128,112
Ta 156,22,41120
Ta 158,22,46240
Mvaa 220,132,112
K 192,192,193
Mvaa 212,132,96
W 8
Ta 128,21,51328
Ta 130,21,56448
Mvaa 208,136,100
K 193,194,195
Mvaa 216,136,116
Ta 132,22,51360
K 194,196,197
Mvaa 220,140,116
Ta 134,22,56480
K 195,198,199
Mvaa 212,140,100
W 8
Ta 136,23,10368
Ta 138,23,15488
Mvaa 208,144,104
s_add_u32 s83, s52, 328704
Mvaa 216,144,120
buffer_store_dwordx4 v[192:195], v1, s[8:11], s83 offen sc1
K 200,200,201
Mvaa 220,148,120
K 201,202,203
K 202,204,205
Mvaa 212,148,104
W 6
Ta 140,24,10400
K 203,206,207
Mvaa 208,152,108
s_add_u32 s83, s52, 369664
Ta 142,24,15520
Ta 144,23,30848
Mvaa 216,152,124
buffer_store_dwordx4 v[200:203], v1, s[8:11], s83 offen sc1
Ta 146,23,35968
Mvaa 220,156,124
Ta 148,24,30880
Ta 150,24,36000
Mvaa 212,156,108
W 8
Ta 152,23,41088
Ta 154,23,46208
Mvaa 208,128,184
Mvaa 216,128,200
s_waitcnt vmcnt(6)
v_lshlrev_b32 v2, 2, v1
v_mov_b64 v[28:29], s[12:13]
s_mov_b64 exec, 3
buffer_store_dwordx2 v[28:29], v2, s[8:11], s53 offen offset:0 sc1
s_mov_b64 exec, -1
Ta 156,24,41120
Ta 158,24,46240
Mvaa 220,132,200
Mvaa 212,132,184
W 8
Ta 128,23,20672
Ta 130,23,25792
Mvaa 208,136,188
Mvaa 216,136,204
s_waitcnt vmcnt(5)
v_xor_b32 v12, s12, v12
v_xor_b32 v13, s13, v13
v_lshrrev_b32 v3, 11, v12
v_or_b32 v13, v13, v3
v_cmp_eq_u32 vcc, 0, v13
s_cmp_eq_u64 vcc, -1
s_cbranch_scc0 .LL_98%=
.LL_99%=:
v_bfe_u32 v13, v12, 4, 7
s_xor_b32 s66, s61, 124
v_cmp_eq_u32 vcc, s61, v13
v_cmp_eq_u32_e64 s[86:87], s66, v13
s_or_b32 s68, vcc_lo, s86
s_and_b32 s68, s68, 0xff
s_cbranch_scc1 .Lup_96%=
.Lupr_97%=:
Ta 132,24,20704
Ta 134,24,25824
Mvaa 220,140,204
Mvaa 212,140,188
W 8
Ta 136,23,51392
v_lshlrev_b32 v32, 16, v36
Mvaa 208,144,192
Ta 138,23,56512
v_and_b32 v33, 0xffff0000, v36
Mvaa 216,144,208
Ta 140,24,51424
v_lshlrev_b32 v34, 16, v37
Mvaa 220,148,208
Ta 142,24,56544
v_and_b32 v35, 0xffff0000, v37
Mvaa 212,148,192
W 8
Ta 144,25,192
v_lshlrev_b32 v36, 16, v38
Mvaa 208,152,196
Ta 146,25,5312
v_and_b32 v37, 0xffff0000, v38
Mvaa 216,152,212
Ta 148,26,224
v_lshlrev_b32 v38, 16, v39
Mvaa 220,156,212
Ta 150,26,5344
v_and_b32 v39, 0xffff0000, v39
Mvaa 212,156,196
W 8
Ta 152,21,41152
)"
R"(v_lshlrev_b32 v40, 16, v44
Mvaa 224,128,96
Ta 154,21,46272
v_and_b32 v41, 0xffff0000, v44
Mvaa 232,128,112
Ta 156,22,41184
v_lshlrev_b32 v42, 16, v45
Mvaa 236,132,112
Ta 158,22,46304
v_and_b32 v43, 0xffff0000, v45
Mvaa 228,132,96
W 8
K 208,208,209
v_lshlrev_b32 v44, 16, v46
Mvaa 224,136,100
Ta 128,21,51392
Ta 130,21,56512
Mvaa 232,136,116
v_and_b32 v45, 0xffff0000, v46
K 209,210,211
Mvaa 236,140,116
v_lshlrev_b32 v46, 16, v47
K 210,212,213
Mvaa 228,140,100
W 6
Ta 132,22,51424
Ta 134,22,56544
Mvaa 224,144,104
v_and_b32 v47, 0xffff0000, v47
K 211,214,215
Mvaa 232,144,120
s_add_u32 s83, s52, 329728
Ta 136,23,10432
v_lshlrev_b32 v48, 16, v52
Mvaa 236,148,120
Ta 138,23,15552
v_and_b32 v49, 0xffff0000, v52
Mvaa 228,148,104
W 6
buffer_store_dwordx4 v[208:211], v1, s[8:11], s83 offen sc1
v_lshlrev_b32 v50, 16, v53
Mvaa 224,152,108
K 216,216,217
K 217,218,219
Mvaa 232,152,124
K 218,220,221
K 219,222,223
Mvaa 236,156,124
s_add_u32 s83, s52, 370688
Ta 140,24,10464
Ta 142,24,15584
Mvaa 228,156,108
W 4
buffer_store_dwordx4 v[216:219], v1, s[8:11], s83 offen sc1
v_and_b32 v51, 0xffff0000, v53
Mvaa 224,128,184
Ta 144,23,30912
v_lshlrev_b32 v52, 16, v54
Mvaa 232,128,200
Ta 146,23,36032
v_and_b32 v53, 0xffff0000, v54
Mvaa 236,132,200
Ta 148,24,30944
v_lshlrev_b32 v54, 16, v55
Mvaa 228,132,184
Ta 150,24,36064
W 4
Mvaa 224,136,188
v_and_b32 v55, 0xffff0000, v55
v_lshlrev_b32 v56, 16, v60
Mvaa 232,136,204
Ta 152,23,41152
Ta 154,23,46272
Mvaa 236,140,204
v_and_b32 v57, 0xffff0000, v60
v_lshlrev_b32 v58, 16, v61
Mvaa 228,140,188
Ta 156,24,41184
Ta 158,24,46304
W 4
Mvaa 224,144,192
v_and_b32 v59, 0xffff0000, v61
v_lshlrev_b32 v60, 16, v62
Mvaa 232,144,208
Ta 128,23,20736
Ta 130,23,25856
Mvaa 236,148,208
v_and_b32 v61, 0xffff0000, v62
v_lshlrev_b32 v62, 16, v63
Mvaa 228,148,192
Ta 132,24,20768
Ta 134,24,25888
W 4
Mvaa 224,152,196
v_and_b32 v63, 0xffff0000, v63
v_lshlrev_b32 v64, 16, v68
Mvaa 232,152,212
Ta 136,23,51456
Ta 138,23,56576
Mvaa 236,156,212
v_and_b32 v65, 0xffff0000, v68
v_lshlrev_b32 v66, 16, v69
Mvaa 228,156,196
Ta 140,24,51488
Ta 142,24,56608
W 4
Mvaa 240,128,96
v_and_b32 v67, 0xffff0000, v69
v_lshlrev_b32 v68, 16, v70
Mvaa 248,128,112
Ta 144,25,256
Ta 146,25,5376
Mvaa 252,132,112
v_and_b32 v69, 0xffff0000, v70
v_lshlrev_b32 v70, 16, v71
Mvaa 244,132,96
Ta 148,26,288
Ta 150,26,5408
W 4
Mvaa 240,136,100
v_and_b32 v71, 0xffff0000, v71
v_lshlrev_b32 v72, 16, v76
Mvaa 248,136,116
Ta 152,21,41216
Ta 154,21,46336
Mvaa 252,140,116
v_and_b32 v73, 0xffff0000, v76
v_lshlrev_b32 v74, 16, v77
Mvaa 244,140,100
Ta 156,22,41248
Ta 158,22,46368
W 4
Mvaa 240,144,104
v_and_b32 v75, 0xffff0000, v77
K 224,224,225
Mvaa 248,144,120
Ta 128,21,51456
v_lshlrev_b32 v76, 16, v78
Mvaa 252,148,120
Ta 130,21,56576
K 225,226,227
Mvaa 244,148,104
Ta 132,22,51488
Ta 134,22,56608
W 4
Mvaa 240,152,108
v_and_b32 v77, 0xffff0000, v78
K 226,228,229
Mvaa 248,152,124
v_lshlrev_b32 v78, 16, v79
v_and_b32 v79, 0xffff0000, v79
Mvaa 252,156,124
Ta 136,23,10496
K 227,230,231
Mvaa 244,156,108
Ta 138,23,15616
Ta 140,24,10528
Ta 142,24,15648
W 4
Mvaa 240,128,184
s_add_u32 s83, s52, 330752
buffer_store_dwordx4 v[224:227], v1, s[8:11], s83 offen sc1
K 232,232,233
Mvaa 248,128,200
K 233,234,235
K 234,236,237
Mvaa 252,132,200
Ta 144,23,30976
K 235,238,239
Mvaa 244,132,184
Ta 146,23,36096
Ta 148,24,31008
Ta 150,24,36128
W 4
Mvaa 240,136,188
s_add_u32 s83, s52, 371712
buffer_store_dwordx4 v[232:235], v1, s[8:11], s83 offen sc1
Ta 152,23,41216
Mvaa 248,136,204
Ta 154,23,46336
Ta 156,24,41248
Mvaa 252,140,204
Ta 158,24,46368
Mvaa 244,140,188
W 4
Mvaa 240,144,192
Mvaa 248,144,208
Mvaa 252,148,208
Mvaa 244,148,192
W 0
Mvaa 240,152,196
Mvaa 248,152,212
Mvaa 252,156,212
Mvaa 244,156,196
s_nop 4
K 240,240,241
K 241,242,243
s_nop 0
K 242,244,245
K 243,246,247
s_add_u32 s83, s52, 331776
buffer_store_dwordx4 v[240:243], v1, s[8:11], s83 offen sc1
K 248,248,249
K 249,250,251
K 250,252,253
K 251,254,255
s_add_u32 s83, s52, 372736
buffer_store_dwordx4 v[248:251], v1, s[8:11], s83 offen sc1
.Ltend_77%=:
v_bfe_u32 v26, v1, 7, 1
v_lshl_add_u32 v26, v26, 9, v27
buffer_load_dwordx2 v[24:25], v26, s[8:11], s63 offen sc1
s_cmp_lg_u32 s68, 0
s_cbranch_scc1 .Lup_101%=
.Lupr_102%=:
s_lshl_b32 s66, 0x101, s59
s_waitcnt vmcnt(1)
s_cmp_eq_u32 s64, 0
s_cbranch_scc0 .LL_107%=
v_lshlrev_b32 v2, 2, v1
v_mov_b64 v[28:29], s[12:13]
s_mov_b64 exec, 3
buffer_store_dwordx2 v[28:29], v2, s[8:11], s53 offen offset:512 sc1
s_mov_b64 exec, -1
.LL_107%=:
s_barrier
s_waitcnt vmcnt(1)
v_cmp_eq_u64 vcc, s[12:13], v[24:25]
s_or_b32 s67, vcc_lo, s66
s_andn2_b32 s80, 0xffff, s67
s_cbranch_scc0 .LL_108%=
s_mov_b32 s84, 0
.LL_111%=:
s_andn2_b32 s80, 255, s67
s_cbranch_scc0 .LL_110%=
s_sleep 2
buffer_load_dwordx2 v[24:25], v26, s[8:11], s63 offen sc1
s_add_u32 s84, s84, 1
s_waitcnt vmcnt(0)
v_cmp_eq_u64 vcc, s[12:13], v[24:25]
s_or_b32 s67, vcc_lo, s66
s_cmp_lt_u32 s84, 4194304
s_cbranch_scc1 .LL_111%=
.LL_110%=:
s_add_u32 s62, s78, 0
buffer_load_dwordx4 v[96:99], v1, s[8:11], s62 offen sc1
s_add_u32 s62, s78, 5120
buffer_load_dwordx4 v[100:103], v1, s[8:11], s62 offen sc1
s_add_u32 s62, s78, 10240
buffer_load_dwordx4 v[104:107], v1, s[8:11], s62 offen sc1
s_add_u32 s62, s78, 15360
buffer_load_dwordx4 v[108:111], v1, s[8:11], s62 offen sc1
s_add_u32 s62, s78, 20480
buffer_load_dwordx4 v[112:115], v1, s[8:11], s62 offen sc1
s_add_u32 s62, s78, 25600
buffer_load_dwordx4 v[116:119], v1, s[8:11], s62 offen sc1
s_add_u32 s62, s78, 30720
buffer_load_dwordx4 v[120:123], v1, s[8:11], s62 offen sc1
s_add_u32 s62, s78, 35840
buffer_load_dwordx4 v[124:127], v1, s[8:11], s62 offen sc1
s_cmp_eq_u32 s14, 0
s_cbranch_scc0 .LL_112%=
s_add_u32 s62, s65, 0
buffer_load_dwordx4 v[128:131], v1, s[8:11], s62 offen sc1
s_add_u32 s62, s65, 5120
buffer_load_dwordx4 v[132:135], v1, s[8:11], s62 offen sc1
s_add_u32 s62, s65, 10240
buffer_load_dwordx4 v[136:139], v1, s[8:11], s62 offen sc1
s_add_u32 s62, s65, 15360
buffer_load_dwordx4 v[140:143], v1, s[8:11], s62 offen sc1
s_add_u32 s62, s65, 20480
buffer_load_dwordx4 v[144:147], v1, s[8:11], s62 offen sc1
s_add_u32 s62, s65, 25600
buffer_load_dwordx4 v[148:151], v1, s[8:11], s62 offen sc1
s_add_u32 s62, s65, 30720
buffer_load_dwordx4 v[152:155], v1, s[8:11], s62 offen sc1
s_add_u32 s62, s65, 35840
buffer_load_dwordx4 v[156:159], v1, s[8:11], s62 offen sc1
.LL_112%=:
buffer_load_dwordx2 v[24:25], v26, s[8:11], s63 offen sc1
s_waitcnt vmcnt(1)
Zvvv 80,4,96
Zvvv 84,8,96
Mvvv 80,4,100
Mvvv 84,8,100
Mvvv 80,4,104
Mvvv 84,8,104
Mvvv 80,4,108
Mvvv 84,8,108
Mvvv 80,4,112
Mvvv 84,8,112
Mvvv 80,4,116
Mvvv 84,8,116
Mvvv 80,4,120
Mvvv 84,8,120
Mvvv 80,4,124
Mvvv 84,8,124
s_nop 6
K 160,80,81
K 161,82,83
K 162,84,85
K 163,86,87
v_lshlrev_b32 v80, 16, v160
v_and_b32 v81, 0xffff0000, v160
v_lshlrev_b32 v82, 16, v161
v_and_b32 v83, 0xffff0000, v161
v_lshlrev_b32 v84, 16, v162
v_and_b32 v85, 0xffff0000, v162
v_lshlrev_b32 v86, 16, v163
v_and_b32 v87, 0xffff0000, v163
A 80,32,80
A 81,33,81
A 82,34,82
A 83,35,83
A 84,36,84
A 85,37,85
A 86,38,86
A 87,39,87
K 160,80,81
K 161,82,83
K 162,84,85
K 163,86,87
v_lshlrev_b32 v80, 16, v160
v_and_b32 v81, 0xffff0000, v160
v_lshlrev_b32 v82, 16, v161
v_and_b32 v83, 0xffff0000, v161
v_lshlrev_b32 v84, 16, v162
v_and_b32 v85, 0xffff0000, v162
v_lshlrev_b32 v86, 16, v163
v_and_b32 v87, 0xffff0000, v163
A 80,40,80
A 81,41,81
A 82,42,82
A 83,43,83
A 84,44,84
A 85,45,85
A 86,46,86
A 87,47,87
K 80,80,81
K 81,82,83
K 82,84,85
K 83,86,87
buffer_store_dwordx4 v[80:83], v20, s[20:23], s72 offen nt
s_cmp_eq_u32 s14, 0
s_cbranch_scc0 .LL_113%=
Zvvv 88,4,128
Zvvv 92,8,128
Mvvv 88,4,132
Mvvv 92,8,132
Mvvv 88,4,136
Mvvv 92,8,136
Mvvv 88,4,140
Mvvv 92,8,140
Mvvv 88,4,144
Mvvv 92,8,144
Mvvv 88,4,148
Mvvv 92,8,148
Mvvv 88,4,152
Mvvv 92,8,152
Mvvv 88,4,156
Mvvv 92,8,156
s_nop 6
K 160,88,89
K 161,90,91
K 162,92,93
K 163,94,95
v_lshlrev_b32 v88, 16, v160
v_and_b32 v89, 0xffff0000, v160
v_lshlrev_b32 v90, 16, v161
v_and_b32 v91, 0xffff0000, v161
v_lshlrev_b32 v92, 16, v162
v_and_b32 v93, 0xffff0000, v162
v_lshlrev_b32 v94, 16, v163
v_and_b32 v95, 0xffff0000, v163
A 88,64,88
A 89,65,89
A 90,66,90
A 91,67,91
A 92,68,92
A 93,69,93
A 94,70,94
A 95,71,95
K 160,88,89
K 161,90,91
K 162,92,93
K 163,94,95
v_lshlrev_b32 v88, 16, v160
v_and_b32 v89, 0xffff0000, v160
v_lshlrev_b32 v90, 16, v161
v_and_b32 v91, 0xffff0000, v161
v_lshlrev_b32 v92, 16, v162
v_and_b32 v93, 0xffff0000, v162
v_lshlrev_b32 v94, 16, v163
v_and_b32 v95, 0xffff0000, v163
A 88,72,88
A 89,73,89
A 90,74,90
A 91,75,91
A 92,76,92
A 93,77,93
)"
R"(A 94,78,94
A 95,79,95
K 88,88,89
K 89,90,91
K 90,92,93
K 91,94,95
buffer_store_dwordx4 v[88:91], v20, s[20:23], s74 offen nt
.LL_113%=:
s_nop 0
s_cmp_eq_u32 s14, 0
s_cbranch_scc1 .LL_107%=a
s_waitcnt vmcnt(1)
.LL_107%=a:
s_waitcnt vmcnt(2)
v_cmp_eq_u64 vcc, s[12:13], v[24:25]
s_or_b32 s67, vcc_lo, s66
s_mov_b32 s84, 0
.LL_115%=:
s_andn2_b32 s80, 65280, s67
s_cbranch_scc0 .LL_114%=
s_sleep 2
buffer_load_dwordx2 v[24:25], v26, s[8:11], s63 offen sc1
s_add_u32 s84, s84, 1
s_waitcnt vmcnt(0)
v_cmp_eq_u64 vcc, s[12:13], v[24:25]
s_or_b32 s67, vcc_lo, s66
s_cmp_lt_u32 s84, 4194304
s_cbranch_scc1 .LL_115%=
.LL_114%=:
s_add_u32 s62, s79, 0
buffer_load_dwordx4 v[208:211], v1, s[8:11], s62 offen sc1
s_add_u32 s62, s79, 5120
buffer_load_dwordx4 v[212:215], v1, s[8:11], s62 offen sc1
s_add_u32 s62, s79, 10240
buffer_load_dwordx4 v[216:219], v1, s[8:11], s62 offen sc1
s_add_u32 s62, s79, 15360
buffer_load_dwordx4 v[220:223], v1, s[8:11], s62 offen sc1
s_add_u32 s62, s79, 20480
buffer_load_dwordx4 v[224:227], v1, s[8:11], s62 offen sc1
s_add_u32 s62, s79, 25600
buffer_load_dwordx4 v[228:231], v1, s[8:11], s62 offen sc1
s_add_u32 s62, s79, 30720
buffer_load_dwordx4 v[232:235], v1, s[8:11], s62 offen sc1
s_add_u32 s62, s79, 35840
buffer_load_dwordx4 v[236:239], v1, s[8:11], s62 offen sc1
s_cmp_eq_u32 s14, 1
s_cbranch_scc0 .LL_116%=
s_add_u32 s62, s65, 0
buffer_load_dwordx4 v[128:131], v1, s[8:11], s62 offen sc1
s_add_u32 s62, s65, 5120
buffer_load_dwordx4 v[132:135], v1, s[8:11], s62 offen sc1
s_add_u32 s62, s65, 10240
buffer_load_dwordx4 v[136:139], v1, s[8:11], s62 offen sc1
s_add_u32 s62, s65, 15360
buffer_load_dwordx4 v[140:143], v1, s[8:11], s62 offen sc1
s_add_u32 s62, s65, 20480
buffer_load_dwordx4 v[144:147], v1, s[8:11], s62 offen sc1
s_add_u32 s62, s65, 25600
buffer_load_dwordx4 v[148:151], v1, s[8:11], s62 offen sc1
s_add_u32 s62, s65, 30720
buffer_load_dwordx4 v[152:155], v1, s[8:11], s62 offen sc1
s_add_u32 s62, s65, 35840
buffer_load_dwordx4 v[156:159], v1, s[8:11], s62 offen sc1
.LL_116%=:
s_waitcnt vmcnt(0)
Zvvv 80,4,208
Zvvv 84,8,208
Mvvv 80,4,212
Mvvv 84,8,212
Mvvv 80,4,216
Mvvv 84,8,216
Mvvv 80,4,220
Mvvv 84,8,220
Mvvv 80,4,224
Mvvv 84,8,224
Mvvv 80,4,228
Mvvv 84,8,228
Mvvv 80,4,232
Mvvv 84,8,232
Mvvv 80,4,236
Mvvv 84,8,236
s_nop 6
K 160,80,81
K 161,82,83
K 162,84,85
K 163,86,87
v_lshlrev_b32 v80, 16, v160
v_and_b32 v81, 0xffff0000, v160
v_lshlrev_b32 v82, 16, v161
v_and_b32 v83, 0xffff0000, v161
v_lshlrev_b32 v84, 16, v162
v_and_b32 v85, 0xffff0000, v162
v_lshlrev_b32 v86, 16, v163
v_and_b32 v87, 0xffff0000, v163
A 80,48,80
A 81,49,81
A 82,50,82
A 83,51,83
A 84,52,84
A 85,53,85
A 86,54,86
A 87,55,87
K 160,80,81
K 161,82,83
K 162,84,85
K 163,86,87
v_lshlrev_b32 v80, 16, v160
v_and_b32 v81, 0xffff0000, v160
v_lshlrev_b32 v82, 16, v161
v_and_b32 v83, 0xffff0000, v161
v_lshlrev_b32 v84, 16, v162
v_and_b32 v85, 0xffff0000, v162
v_lshlrev_b32 v86, 16, v163
v_and_b32 v87, 0xffff0000, v163
A 80,56,80
A 81,57,81
A 82,58,82
A 83,59,83
A 84,60,84
A 85,61,85
A 86,62,86
A 87,63,87
K 80,80,81
K 81,82,83
K 82,84,85
K 83,86,87
buffer_store_dwordx4 v[80:83], v20, s[20:23], s73 offen nt
s_cmp_eq_u32 s14, 1
s_cbranch_scc0 .LL_117%=
Zvvv 88,4,128
Zvvv 92,8,128
Mvvv 88,4,132
Mvvv 92,8,132
Mvvv 88,4,136
Mvvv 92,8,136
Mvvv 88,4,140
Mvvv 92,8,140
Mvvv 88,4,144
Mvvv 92,8,144
Mvvv 88,4,148
Mvvv 92,8,148
Mvvv 88,4,152
Mvvv 92,8,152
Mvvv 88,4,156
Mvvv 92,8,156
s_nop 6
K 160,88,89
K 161,90,91
K 162,92,93
K 163,94,95
v_lshlrev_b32 v88, 16, v160
v_and_b32 v89, 0xffff0000, v160
v_lshlrev_b32 v90, 16, v161
v_and_b32 v91, 0xffff0000, v161
v_lshlrev_b32 v92, 16, v162
v_and_b32 v93, 0xffff0000, v162
v_lshlrev_b32 v94, 16, v163
v_and_b32 v95, 0xffff0000, v163
A 88,64,88
A 89,65,89
A 90,66,90
A 91,67,91
A 92,68,92
A 93,69,93
A 94,70,94
A 95,71,95
K 160,88,89
K 161,90,91
K 162,92,93
K 163,94,95
v_lshlrev_b32 v88, 16, v160
v_and_b32 v89, 0xffff0000, v160
v_lshlrev_b32 v90, 16, v161
v_and_b32 v91, 0xffff0000, v161
v_lshlrev_b32 v92, 16, v162
v_and_b32 v93, 0xffff0000, v162
v_lshlrev_b32 v94, 16, v163
v_and_b32 v95, 0xffff0000, v163
A 88,72,88
A 89,73,89
A 90,74,90
A 91,75,91
A 92,76,92
A 93,77,93
A 94,78,94
A 95,79,95
K 88,88,89
K 89,90,91
K 90,92,93
K 91,94,95
buffer_store_dwordx4 v[88:91], v20, s[20:23], s74 offen nt
.LL_117%=:
s_branch .LL_109%=
.LL_108%=:
s_add_u32 s62, s78, 0
buffer_load_dwordx4 v[96:99], v1, s[8:11], s62 offen sc1
s_add_u32 s62, s78, 5120
buffer_load_dwordx4 v[100:103], v1, s[8:11], s62 offen sc1
s_add_u32 s62, s78, 10240
buffer_load_dwordx4 v[104:107], v1, s[8:11], s62 offen sc1
s_add_u32 s62, s78, 15360
buffer_load_dwordx4 v[108:111], v1, s[8:11], s62 offen sc1
s_add_u32 s62, s78, 20480
buffer_load_dwordx4 v[112:115], v1, s[8:11], s62 offen sc1
s_add_u32 s62, s78, 25600
buffer_load_dwordx4 v[116:119], v1, s[8:11], s62 offen sc1
s_add_u32 s62, s78, 30720
buffer_load_dwordx4 v[120:123], v1, s[8:11], s62 offen sc1
s_add_u32 s62, s78, 35840
buffer_load_dwordx4 v[124:127], v1, s[8:11], s62 offen sc1
s_cmp_eq_u32 s14, 0
s_cbranch_scc0 .LL_118%=
s_add_u32 s62, s65, 0
buffer_load_dwordx4 v[128:131], v1, s[8:11], s62 offen sc1
s_add_u32 s62, s65, 5120
buffer_load_dwordx4 v[132:135], v1, s[8:11], s62 offen sc1
s_add_u32 s62, s65, 10240
buffer_load_dwordx4 v[136:139], v1, s[8:11], s62 offen sc1
s_add_u32 s62, s65, 15360
buffer_load_dwordx4 v[140:143], v1, s[8:11], s62 offen sc1
s_add_u32 s62, s65, 20480
buffer_load_dwordx4 v[144:147], v1, s[8:11], s62 offen sc1
s_add_u32 s62, s65, 25600
buffer_load_dwordx4 v[148:151], v1, s[8:11], s62 offen sc1
s_add_u32 s62, s65, 30720
buffer_load_dwordx4 v[152:155], v1, s[8:11], s62 offen sc1
s_add_u32 s62, s65, 35840
buffer_load_dwordx4 v[156:159], v1, s[8:11], s62 offen sc1
.LL_118%=:
s_add_u32 s62, s79, 0
buffer_load_dwordx4 v[208:211], v1, s[8:11], s62 offen sc1
s_add_u32 s62, s79, 5120
buffer_load_dwordx4 v[212:215], v1, s[8:11], s62 offen sc1
s_add_u32 s62, s79, 10240
buffer_load_dwordx4 v[216:219], v1, s[8:11], s62 offen sc1
s_add_u32 s62, s79, 15360
buffer_load_dwordx4 v[220:223], v1, s[8:11], s62 offen sc1
s_add_u32 s62, s79, 20480
buffer_load_dwordx4 v[224:227], v1, s[8:11], s62 offen sc1
s_add_u32 s62, s79, 25600
buffer_load_dwordx4 v[228:231], v1, s[8:11], s62 offen sc1
s_add_u32 s62, s79, 30720
buffer_load_dwordx4 v[232:235], v1, s[8:11], s62 offen sc1
s_add_u32 s62, s79, 35840
buffer_load_dwordx4 v[236:239], v1, s[8:11], s62 offen sc1
s_cmp_eq_u32 s14, 1
s_cbranch_scc0 .LL_119%=
s_add_u32 s62, s65, 0
buffer_load_dwordx4 v[128:131], v1, s[8:11], s62 offen sc1
s_add_u32 s62, s65, 5120
buffer_load_dwordx4 v[132:135], v1, s[8:11], s62 offen sc1
s_add_u32 s62, s65, 10240
buffer_load_dwordx4 v[136:139], v1, s[8:11], s62 offen sc1
s_add_u32 s62, s65, 15360
buffer_load_dwordx4 v[140:143], v1, s[8:11], s62 offen sc1
s_add_u32 s62, s65, 20480
buffer_load_dwordx4 v[144:147], v1, s[8:11], s62 offen sc1
s_add_u32 s62, s65, 25600
buffer_load_dwordx4 v[148:151], v1, s[8:11], s62 offen sc1
s_add_u32 s62, s65, 30720
buffer_load_dwordx4 v[152:155], v1, s[8:11], s62 offen sc1
s_add_u32 s62, s65, 35840
buffer_load_dwordx4 v[156:159], v1, s[8:11], s62 offen sc1
.LL_119%=:
s_waitcnt vmcnt(8)
Zvvv 80,4,96
Zvvv 84,8,96
Mvvv 80,4,100
Mvvv 84,8,100
Mvvv 80,4,104
Mvvv 84,8,104
Mvvv 80,4,108
Mvvv 84,8,108
Mvvv 80,4,112
Mvvv 84,8,112
Mvvv 80,4,116
Mvvv 84,8,116
Mvvv 80,4,120
Mvvv 84,8,120
Mvvv 80,4,124
Mvvv 84,8,124
s_nop 6
K 160,80,81
K 161,82,83
K 162,84,85
K 163,86,87
v_lshlrev_b32 v80, 16, v160
v_and_b32 v81, 0xffff0000, v160
v_lshlrev_b32 v82, 16, v161
v_and_b32 v83, 0xffff0000, v161
v_lshlrev_b32 v84, 16, v162
v_and_b32 v85, 0xffff0000, v162
v_lshlrev_b32 v86, 16, v163
v_and_b32 v87, 0xffff0000, v163
A 80,32,80
A 81,33,81
A 82,34,82
A 83,35,83
A 84,36,84
A 85,37,85
A 86,38,86
A 87,39,87
K 160,80,81
K 161,82,83
K 162,84,85
K 163,86,87
v_lshlrev_b32 v80, 16, v160
v_and_b32 v81, 0xffff0000, v160
v_lshlrev_b32 v82, 16, v161
v_and_b32 v83, 0xffff0000, v161
v_lshlrev_b32 v84, 16, v162
v_and_b32 v85, 0xffff0000, v162
v_lshlrev_b32 v86, 16, v163
v_and_b32 v87, 0xffff0000, v163
A 80,40,80
A 81,41,81
A 82,42,82
A 83,43,83
A 84,44,84
A 85,45,85
A 86,46,86
A 87,47,87
K 80,80,81
K 81,82,83
K 82,84,85
K 83,86,87
buffer_store_dwordx4 v[80:83], v20, s[20:23], s72 offen nt
s_cmp_eq_u32 s14, 0
s_cbranch_scc0 .LL_120%=
Zvvv 88,4,128
Zvvv 92,8,128
Mvvv 88,4,132
Mvvv 92,8,132
Mvvv 88,4,136
Mvvv 92,8,136
Mvvv 88,4,140
Mvvv 92,8,140
Mvvv 88,4,144
Mvvv 92,8,144
Mvvv 88,4,148
Mvvv 92,8,148
Mvvv 88,4,152
Mvvv 92,8,152
Mvvv 88,4,156
Mvvv 92,8,156
s_nop 6
K 160,88,89
K 161,90,91
K 162,92,93
K 163,94,95
v_lshlrev_b32 v88, 16, v160
v_and_b32 v89, 0xffff0000, v160
v_lshlrev_b32 v90, 16, v161
v_and_b32 v91, 0xffff0000, v161
v_lshlrev_b32 v92, 16, v162
v_and_b32 v93, 0xffff0000, v162
v_lshlrev_b32 v94, 16, v163
v_and_b32 v95, 0xffff0000, v163
A 88,64,88
A 89,65,89
A 90,66,90
A 91,67,91
A 92,68,92
A 93,69,93
A 94,70,94
A 95,71,95
K 160,88,89
K 161,90,91
K 162,92,93
K 163,94,95
v_lshlrev_b32 v88, 16, v160
v_and_b32 v89, 0xffff0000, v160
v_lshlrev_b32 v90, 16, v161
v_and_b32 v91, 0xffff0000, v161
v_lshlrev_b32 v92, 16, v162
v_and_b32 v93, 0xffff0000, v162
v_lshlrev_b32 v94, 16, v163
v_and_b32 v95, 0xffff0000, v163
A 88,72,88
A 89,73,89
A 90,74,90
A 91,75,91
A 92,76,92
A 93,77,93
A 94,78,94
A 95,79,95
K 88,88,89
K 89,90,91
K 90,92,93
K 91,94,95
buffer_store_dwordx4 v[88:91], v20, s[20:23], s74 offen nt
.LL_120%=:
s_waitcnt vmcnt(1)
Zvvv 80,4,208
Zvvv 84,8,208
Mvvv 80,4,212
Mvvv 84,8,212
Mvvv 80,4,216
Mvvv 84,8,216
Mvvv 80,4,220
)"
R"(Mvvv 84,8,220
Mvvv 80,4,224
Mvvv 84,8,224
Mvvv 80,4,228
Mvvv 84,8,228
Mvvv 80,4,232
Mvvv 84,8,232
Mvvv 80,4,236
Mvvv 84,8,236
s_nop 6
K 160,80,81
K 161,82,83
K 162,84,85
K 163,86,87
v_lshlrev_b32 v80, 16, v160
v_and_b32 v81, 0xffff0000, v160
v_lshlrev_b32 v82, 16, v161
v_and_b32 v83, 0xffff0000, v161
v_lshlrev_b32 v84, 16, v162
v_and_b32 v85, 0xffff0000, v162
v_lshlrev_b32 v86, 16, v163
v_and_b32 v87, 0xffff0000, v163
A 80,48,80
A 81,49,81
A 82,50,82
A 83,51,83
A 84,52,84
A 85,53,85
A 86,54,86
A 87,55,87
K 160,80,81
K 161,82,83
K 162,84,85
K 163,86,87
v_lshlrev_b32 v80, 16, v160
v_and_b32 v81, 0xffff0000, v160
v_lshlrev_b32 v82, 16, v161
v_and_b32 v83, 0xffff0000, v161
v_lshlrev_b32 v84, 16, v162
v_and_b32 v85, 0xffff0000, v162
v_lshlrev_b32 v86, 16, v163
v_and_b32 v87, 0xffff0000, v163
A 80,56,80
A 81,57,81
A 82,58,82
A 83,59,83
A 84,60,84
A 85,61,85
A 86,62,86
A 87,63,87
K 80,80,81
K 81,82,83
K 82,84,85
K 83,86,87
buffer_store_dwordx4 v[80:83], v20, s[20:23], s73 offen nt
s_cmp_eq_u32 s14, 1
s_cbranch_scc0 .LL_121%=
Zvvv 88,4,128
Zvvv 92,8,128
Mvvv 88,4,132
Mvvv 92,8,132
Mvvv 88,4,136
Mvvv 92,8,136
Mvvv 88,4,140
Mvvv 92,8,140
Mvvv 88,4,144
Mvvv 92,8,144
Mvvv 88,4,148
Mvvv 92,8,148
Mvvv 88,4,152
Mvvv 92,8,152
Mvvv 88,4,156
Mvvv 92,8,156
s_nop 6
K 160,88,89
K 161,90,91
K 162,92,93
K 163,94,95
v_lshlrev_b32 v88, 16, v160
v_and_b32 v89, 0xffff0000, v160
v_lshlrev_b32 v90, 16, v161
v_and_b32 v91, 0xffff0000, v161
v_lshlrev_b32 v92, 16, v162
v_and_b32 v93, 0xffff0000, v162
v_lshlrev_b32 v94, 16, v163
v_and_b32 v95, 0xffff0000, v163
A 88,64,88
A 89,65,89
A 90,66,90
A 91,67,91
A 92,68,92
A 93,69,93
A 94,70,94
A 95,71,95
K 160,88,89
K 161,90,91
K 162,92,93
K 163,94,95
v_lshlrev_b32 v88, 16, v160
v_and_b32 v89, 0xffff0000, v160
v_lshlrev_b32 v90, 16, v161
v_and_b32 v91, 0xffff0000, v161
v_lshlrev_b32 v92, 16, v162
v_and_b32 v93, 0xffff0000, v162
v_lshlrev_b32 v94, 16, v163
v_and_b32 v95, 0xffff0000, v163
A 88,72,88
A 89,73,89
A 90,74,90
A 91,75,91
A 92,76,92
A 93,77,93
A 94,78,94
A 95,79,95
K 88,88,89
K 89,90,91
K 90,92,93
K 91,94,95
buffer_store_dwordx4 v[88:91], v20, s[20:23], s74 offen nt
.LL_121%=:
.LL_109%=:
s_lshl_b32 s80, s15, 2
s_add_u32 s80, s80, s14
s_cmp_eq_u32 s80, 4
s_cbranch_scc0 .LL_122%=
v_mov_b64 v[28:29], s[12:13]
s_mov_b64 exec, 1
buffer_store_dwordx2 v[28:29], off, s[8:11], 0 sc1
s_mov_b64 exec, -1
.LL_122%=:
s_branch .Lend_123%=
.Lup_101%=:
s_waitcnt vmcnt(5)
s_mov_b32 s84, 0
.LL_103%=:
v_min3_u32 v176, v161, v163, v165
v_max3_u32 v177, v161, v163, v165
v_min3_u32 v176, v176, v167, v169
v_max3_u32 v177, v177, v167, v169
v_min3_u32 v176, v176, v171, v173
v_max3_u32 v177, v177, v171, v173
v_min_u32 v176, v176, v175
v_max_u32 v177, v177, v175
v_cmp_eq_u32 vcc, s13, v176
v_cmp_eq_u32_e64 s[86:87], s13, v177
s_and_b64 vcc, vcc, s[86:87]
s_cmp_eq_u64 vcc, -1
s_cbranch_scc1 .LL_104%=
s_add_u32 s84, s84, 1
s_cmp_lt_u32 s84, 1048576
s_cbranch_scc1 .LL_105%=
.LL_104%=:
v_readlane_b32 s70, v12, s69
v_bfe_u32 v2, v1, 4, 4
s_nop 2
s_lshr_b32 s80, s70, 4
s_and_b32 s70, s70, 15
v_cmp_eq_u32 vcc, s70, v2
s_cmp_eq_u32 s80, s61
s_cselect_b64 s[86:87], vcc, 0
s_cselect_b64 s[88:89], 0, vcc
s_bitcmp1_b32 s14, 0
s_cselect_b64 vcc, s[88:89], s[86:87]
v_lshlrev_b32 v176, 16, v160
v_and_b32 v177, 0xffff0000, v160
v_lshlrev_b32 v178, 16, v162
v_and_b32 v179, 0xffff0000, v162
v_lshlrev_b32 v180, 16, v164
v_and_b32 v181, 0xffff0000, v164
v_lshlrev_b32 v182, 16, v166
v_and_b32 v183, 0xffff0000, v166
A 176,176,40
A 177,177,41
A 178,178,42
A 179,179,43
A 180,180,44
A 181,181,45
A 182,182,46
A 183,183,47
K 184,176,177
K 185,178,179
K 186,180,181
K 187,182,183
v_lshlrev_b32 v176, 16, v184
v_and_b32 v177, 0xffff0000, v184
v_lshlrev_b32 v178, 16, v185
v_and_b32 v179, 0xffff0000, v185
v_lshlrev_b32 v180, 16, v186
v_and_b32 v181, 0xffff0000, v186
v_lshlrev_b32 v182, 16, v187
v_and_b32 v183, 0xffff0000, v187
v_cndmask_b32_e64 v40, v40, v176, s[86:87]
v_cndmask_b32_e64 v41, v41, v177, s[86:87]
v_cndmask_b32_e64 v42, v42, v178, s[86:87]
v_cndmask_b32_e64 v43, v43, v179, s[86:87]
v_cndmask_b32_e64 v44, v44, v180, s[86:87]
v_cndmask_b32_e64 v45, v45, v181, s[86:87]
v_cndmask_b32_e64 v46, v46, v182, s[86:87]
v_cndmask_b32_e64 v47, v47, v183, s[86:87]
v_lshlrev_b32 v176, 16, v160
v_and_b32 v177, 0xffff0000, v160
v_lshlrev_b32 v178, 16, v162
v_and_b32 v179, 0xffff0000, v162
v_lshlrev_b32 v180, 16, v164
v_and_b32 v181, 0xffff0000, v164
v_lshlrev_b32 v182, 16, v166
v_and_b32 v183, 0xffff0000, v166
A 176,176,56
A 177,177,57
A 178,178,58
A 179,179,59
A 180,180,60
A 181,181,61
A 182,182,62
A 183,183,63
K 184,176,177
K 185,178,179
K 186,180,181
K 187,182,183
v_lshlrev_b32 v176, 16, v184
v_and_b32 v177, 0xffff0000, v184
v_lshlrev_b32 v178, 16, v185
v_and_b32 v179, 0xffff0000, v185
v_lshlrev_b32 v180, 16, v186
v_and_b32 v181, 0xffff0000, v186
v_lshlrev_b32 v182, 16, v187
v_and_b32 v183, 0xffff0000, v187
v_cndmask_b32_e64 v56, v56, v176, s[88:89]
v_cndmask_b32_e64 v57, v57, v177, s[88:89]
v_cndmask_b32_e64 v58, v58, v178, s[88:89]
v_cndmask_b32_e64 v59, v59, v179, s[88:89]
v_cndmask_b32_e64 v60, v60, v180, s[88:89]
v_cndmask_b32_e64 v61, v61, v181, s[88:89]
v_cndmask_b32_e64 v62, v62, v182, s[88:89]
v_cndmask_b32_e64 v63, v63, v183, s[88:89]
v_lshlrev_b32 v176, 16, v168
v_and_b32 v177, 0xffff0000, v168
v_lshlrev_b32 v178, 16, v170
v_and_b32 v179, 0xffff0000, v170
v_lshlrev_b32 v180, 16, v172
v_and_b32 v181, 0xffff0000, v172
v_lshlrev_b32 v182, 16, v174
v_and_b32 v183, 0xffff0000, v174
A 176,176,72
A 177,177,73
A 178,178,74
A 179,179,75
A 180,180,76
A 181,181,77
A 182,182,78
A 183,183,79
K 184,176,177
K 185,178,179
K 186,180,181
K 187,182,183
v_lshlrev_b32 v176, 16, v184
v_and_b32 v177, 0xffff0000, v184
v_lshlrev_b32 v178, 16, v185
v_and_b32 v179, 0xffff0000, v185
v_lshlrev_b32 v180, 16, v186
v_and_b32 v181, 0xffff0000, v186
v_lshlrev_b32 v182, 16, v187
v_and_b32 v183, 0xffff0000, v187
v_cndmask_b32_e64 v72, v72, v176, vcc
v_cndmask_b32_e64 v73, v73, v177, vcc
v_cndmask_b32_e64 v74, v74, v178, vcc
v_cndmask_b32_e64 v75, v75, v179, vcc
v_cndmask_b32_e64 v76, v76, v180, vcc
v_cndmask_b32_e64 v77, v77, v181, vcc
v_cndmask_b32_e64 v78, v78, v182, vcc
v_cndmask_b32_e64 v79, v79, v183, vcc
s_bitset0_b32 s68, s69
s_cmp_eq_u32 s68, 0
s_cbranch_scc1 .Lupr_102%=
s_ff1_i32_b32 s69, s68
.LL_105%=:
s_cmp_eq_u32 s64, 0
s_cbranch_scc0 .LL_106%=
s_waitcnt vmcnt(0)
v_lshlrev_b32 v2, 2, v1
v_mov_b64 v[28:29], s[12:13]
s_mov_b64 exec, 3
buffer_store_dwordx2 v[28:29], v2, s[8:11], s53 offen offset:512 sc1
s_mov_b64 exec, -1
s_mov_b32 s64, 1
.LL_106%=:
s_mul_i32 s66, s69, 20
s_mul_i32 s67, s58, 5
s_add_u32 s66, s66, s67
s_lshl_b32 s66, s66, 7
s_add_u32 s66, s66, 98304
s_lshl_b32 s67, s14, 7
s_add_u32 s67, s67, s66
buffer_load_dwordx4 v[160:163], v3, s[8:11], s67 offen offset:0 sc1
buffer_load_dwordx4 v[164:167], v3, s[8:11], s67 offen offset:16 sc1
buffer_load_dwordx4 v[168:171], v3, s[8:11], s66 offen offset:512 sc1
buffer_load_dwordx4 v[172:175], v3, s[8:11], s66 offen offset:528 sc1
s_waitcnt vmcnt(0)
s_branch .LL_103%=
.Lspin_54%=:
s_mov_b32 s84, 0
.Llp_56%=:
s_sleep 2
v_mov_b32 v28, s74
buffer_load_dwordx2 v[28:29], v28, s[8:11], 0 offen offset:3584 sc1
s_waitcnt vmcnt(0)
s_add_u32 s84, s84, 1
v_xor_b32 v28, s12, v28
v_xor_b32 v29, s13, v29
v_lshrrev_b32 v248, 11, v28
v_or_b32 v29, v29, v248
v_cmp_eq_u32 vcc, 0, v29
s_or_b64 vcc, vcc, s[72:73]
s_cmp_eq_u64 vcc, -1
s_cbranch_scc1 .Lok_55%=
s_cmp_lt_u32 s84, 1048576
s_cbranch_scc1 .Llp_56%=
s_branch .Lok_55%=
.Lspin_59%=:
s_mov_b32 s84, 0
.Llp_61%=:
s_sleep 2
v_bfe_u32 v28, v1, 4, 4
v_lshlrev_b32 v28, 3, v28
s_lshl_b32 s80, s56, 9
s_add_u32 s80, s80, 4096
buffer_load_dwordx2 v[28:29], v28, s[8:11], s80 offen sc1
s_waitcnt vmcnt(0)
s_add_u32 s84, s84, 1
v_cmp_eq_u64 vcc, s[12:13], v[28:29]
s_cmp_eq_u64 vcc, -1
s_cbranch_scc1 .Lok_60%=
s_cmp_lt_u32 s84, 1048576
s_cbranch_scc1 .Llp_61%=
s_branch .Lok_60%=
.Lspin_62%=:
s_mov_b32 s84, 0
.Llp_64%=:
s_sleep 2
v_bfe_u32 v28, v1, 4, 4
v_lshlrev_b32 v28, 3, v28
s_lshl_b32 s80, s56, 9
s_add_u32 s80, s80, 4224
buffer_load_dwordx2 v[28:29], v28, s[8:11], s80 offen sc1
s_waitcnt vmcnt(0)
s_add_u32 s84, s84, 1
v_cmp_eq_u64 vcc, s[12:13], v[28:29]
s_cmp_eq_u64 vcc, -1
s_cbranch_scc1 .Lok_63%=
s_cmp_lt_u32 s84, 1048576
s_cbranch_scc1 .Llp_64%=
s_branch .Lok_63%=
.Lspin_65%=:
s_mov_b32 s84, 0
.Llp_67%=:
s_sleep 2
v_bfe_u32 v28, v1, 4, 4
v_lshlrev_b32 v28, 3, v28
s_lshl_b32 s80, s56, 9
s_add_u32 s80, s80, 4352
buffer_load_dwordx2 v[28:29], v28, s[8:11], s80 offen sc1
s_waitcnt vmcnt(0)
s_add_u32 s84, s84, 1
v_cmp_eq_u64 vcc, s[12:13], v[28:29]
s_cmp_eq_u64 vcc, -1
s_cbranch_scc1 .Lok_66%=
s_cmp_lt_u32 s84, 1048576
s_cbranch_scc1 .Llp_67%=
s_branch .Lok_66%=
.Lspin_68%=:
s_mov_b32 s84, 0
.Llp_70%=:
s_sleep 2
v_bfe_u32 v28, v1, 4, 4
v_lshlrev_b32 v28, 3, v28
s_lshl_b32 s80, s56, 9
s_add_u32 s80, s80, 4480
buffer_load_dwordx2 v[28:29], v28, s[8:11], s80 offen sc1
s_waitcnt vmcnt(0)
s_add_u32 s84, s84, 1
v_cmp_eq_u64 vcc, s[12:13], v[28:29]
s_cmp_eq_u64 vcc, -1
s_cbranch_scc1 .Lok_69%=
s_cmp_lt_u32 s84, 1048576
s_cbranch_scc1 .Llp_70%=
s_branch .Lok_69%=
.Lspin_71%=:
s_mov_b32 s84, 0
.Llp_73%=:
s_sleep 2
buffer_load_dwordx2 v[8:9], v4, s[8:11], 0 offen offset:2560 sc1
buffer_load_dwordx2 v[10:11], v5, s[8:11], 0 offen offset:3072 sc1
buffer_load_dwordx2 v[12:13], v6, s[8:11], 0 offen offset:3584 sc1
s_waitcnt vmcnt(0)
s_add_u32 s84, s84, 1
v_cmp_eq_u64 vcc, s[12:13], v[8:9]
v_cmp_eq_u64_e64 s[86:87], s[12:13], v[10:11]
s_and_b64 vcc, vcc, s[86:87]
s_or_b64 vcc, vcc, s[20:21]
s_cmp_eq_u64 vcc, -1
)"
R"(s_cbranch_scc1 .Lok_72%=
s_cmp_lt_u32 s84, 1048576
s_cbranch_scc1 .Llp_73%=
s_branch .Lok_72%=
.Lmk_74%=:
s_nop 7
v_cndmask_b32_e64 v168, v241, v168, s[72:73]
v_cndmask_b32_e64 v169, v241, v169, s[74:75]
v_cndmask_b32_e64 v170, v241, v170, s[76:77]
v_cndmask_b32_e64 v171, v241, v171, s[78:79]
v_cndmask_b32_e64 v188, v241, v188, s[72:73]
v_cndmask_b32_e64 v189, v241, v189, s[74:75]
v_cndmask_b32_e64 v190, v241, v190, s[76:77]
v_cndmask_b32_e64 v191, v241, v191, s[78:79]
v_mov_b32 v184, v241
v_mov_b32 v185, v241
v_mov_b32 v186, v241
v_mov_b32 v187, v241
s_branch .Lmkr_75%=
.Lpd_78%=:
v_mov_b64 v[28:29], s[12:13]
s_mov_b64 exec, 1
buffer_store_dwordx2 v[28:29], off, s[8:11], s69 sc1
s_mov_b64 exec, -1
s_branch .Lpdr_79%=
.Lspin_80%=:
s_mov_b32 s84, 0
.Llp_82%=:
s_sleep 2
v_bfe_u32 v28, v1, 4, 2
v_lshlrev_b32 v28, 3, v28
buffer_load_dwordx2 v[28:29], v28, s[8:11], s55 offen sc1
s_waitcnt vmcnt(0)
s_add_u32 s84, s84, 1
v_cmp_eq_u64 vcc, s[12:13], v[28:29]
s_cmp_eq_u64 vcc, -1
s_cbranch_scc1 .Lok_81%=
s_cmp_lt_u32 s84, 1048576
s_cbranch_scc1 .Llp_82%=
s_branch .Lok_81%=
.LL_85%=:
s_mov_b32 s84, 0
.LL_87%=:
s_sleep 2
buffer_load_dwordx2 v[12:13], v27, s[8:11], 0 offen offset:3584 sc1
s_waitcnt vmcnt(0)
s_add_u32 s84, s84, 1
v_xor_b32 v12, s12, v12
v_xor_b32 v13, s13, v13
v_lshrrev_b32 v3, 11, v12
v_or_b32 v13, v13, v3
v_cmp_eq_u32 vcc, 0, v13
s_cmp_eq_u64 vcc, -1
s_cbranch_scc1 .LL_86%=
s_cmp_lt_u32 s84, 1048576
s_cbranch_scc1 .LL_87%=
s_branch .LL_86%=
.Lup_83%=:
v_lshrrev_b32 v3, 8, v1
v_lshlrev_b32 v3, 5, v3
s_ff1_i32_b32 s69, s68
s_mul_i32 s66, s69, 20
s_mul_i32 s67, s58, 5
s_add_u32 s66, s66, s67
s_lshl_b32 s66, s66, 7
s_add_u32 s66, s66, 98304
s_lshl_b32 s67, s14, 7
s_add_u32 s67, s67, s66
buffer_load_dwordx4 v[160:163], v3, s[8:11], s67 offen offset:0 sc1
buffer_load_dwordx4 v[164:167], v3, s[8:11], s67 offen offset:16 sc1
buffer_load_dwordx4 v[168:171], v3, s[8:11], s66 offen offset:512 sc1
buffer_load_dwordx4 v[172:175], v3, s[8:11], s66 offen offset:528 sc1
s_branch .Lupr_84%=
.Lpd_88%=:
v_mov_b64 v[28:29], s[12:13]
s_mov_b64 exec, 1
buffer_store_dwordx2 v[28:29], off, s[8:11], s69 sc1
s_mov_b64 exec, -1
s_branch .Lpdr_89%=
.Lspin_90%=:
s_mov_b32 s84, 0
.Llp_92%=:
s_sleep 2
v_bfe_u32 v28, v1, 4, 2
v_lshlrev_b32 v28, 3, v28
buffer_load_dwordx2 v[28:29], v28, s[8:11], s54 offen sc1
s_waitcnt vmcnt(0)
s_add_u32 s84, s84, 1
v_cmp_eq_u64 vcc, s[12:13], v[28:29]
s_cmp_eq_u64 vcc, -1
s_cbranch_scc1 .Lok_91%=
s_cmp_lt_u32 s84, 1048576
s_cbranch_scc1 .Llp_92%=
s_branch .Lok_91%=
.Lspin_93%=:
s_mov_b32 s84, 0
.Llp_95%=:
s_sleep 2
v_bfe_u32 v28, v1, 4, 2
v_lshlrev_b32 v28, 3, v28
buffer_load_dwordx2 v[28:29], v28, s[8:11], s55 offen sc1
s_waitcnt vmcnt(0)
s_add_u32 s84, s84, 1
v_cmp_eq_u64 vcc, s[12:13], v[28:29]
s_cmp_eq_u64 vcc, -1
s_cbranch_scc1 .Lok_94%=
s_cmp_lt_u32 s84, 1048576
s_cbranch_scc1 .Llp_95%=
s_branch .Lok_94%=
.LL_98%=:
s_mov_b32 s84, 0
.LL_100%=:
s_sleep 2
buffer_load_dwordx2 v[12:13], v27, s[8:11], 0 offen offset:3584 sc1
s_waitcnt vmcnt(0)
s_add_u32 s84, s84, 1
v_xor_b32 v12, s12, v12
v_xor_b32 v13, s13, v13
v_lshrrev_b32 v3, 11, v12
v_or_b32 v13, v13, v3
v_cmp_eq_u32 vcc, 0, v13
s_cmp_eq_u64 vcc, -1
s_cbranch_scc1 .LL_99%=
s_cmp_lt_u32 s84, 1048576
s_cbranch_scc1 .LL_100%=
s_branch .LL_99%=
.Lup_96%=:
v_lshrrev_b32 v3, 8, v1
v_lshlrev_b32 v3, 5, v3
s_ff1_i32_b32 s69, s68
s_mul_i32 s66, s69, 20
s_mul_i32 s67, s58, 5
s_add_u32 s66, s66, s67
s_lshl_b32 s66, s66, 7
s_add_u32 s66, s66, 98304
s_lshl_b32 s67, s14, 7
s_add_u32 s67, s67, s66
buffer_load_dwordx4 v[160:163], v3, s[8:11], s67 offen offset:0 sc1
buffer_load_dwordx4 v[164:167], v3, s[8:11], s67 offen offset:16 sc1
buffer_load_dwordx4 v[168:171], v3, s[8:11], s66 offen offset:512 sc1
buffer_load_dwordx4 v[172:175], v3, s[8:11], s66 offen offset:528 sc1
s_branch .Lupr_97%=
.Lend_123%=:
.Lend%=:
.purgem W
.purgem D
.purgem Ra
.purgem Ta
.purgem Zvaa
.purgem Zaaa
.purgem A
.purgem U
.purgem Mvaa
.purgem Maaa
.purgem K
.purgem Rv
.purgem Mvav
.purgem G
.purgem Dc
.purgem Zvav
.purgem Cvav
.purgem X
.purgem Cvaa
.purgem Maav
.purgem Mvvv
.purgem Zvvv
)"
    : : [ka] "s"(__builtin_amdgcn_kernarg_segment_ptr()), [bid] "s"(blockIdx.x), [tid] "v"(threadIdx.x)
    : "v1", "v2", "v3", "v4", "v5", "v6", "v7", "v8", "v9", "v10", "v11", "v12", "v13", "v14", "v15", "v16", "v17", "v18", "v19", "v20", "v21", "v22", "v23", "v24", "v25", "v26", "v27", "v28", "v29", "v30", "v31", "v32", "v33", "v34", "v35", "v36", "v37", "v38", "v39", "v40", "v41", "v42", "v43", "v44", "v45", "v46", "v47", "v48", "v49", "v50", "v51", "v52", "v53", "v54", "v55", "v56", "v57", "v58", "v59", "v60", "v61", "v62", "v63", "v64", "v65", "v66", "v67", "v68", "v69", "v70", "v71", "v72", "v73", "v74", "v75", "v76", "v77", "v78", "v79", "v80", "v81", "v82", "v83", "v84", "v85", "v86", "v87", "v88", "v89", "v90", "v91", "v92", "v93", "v94", "v95", "v96", "v97", "v98", "v99", "v100", "v101", "v102", "v103", "v104", "v105", "v106", "v107", "v108", "v109", "v110", "v111", "v112", "v113", "v114", "v115", "v116", "v117", "v118", "v119", "v120", "v121", "v122", "v123", "v124", "v125", "v126", "v127", "v128", "v129", "v130", "v131", "v132", "v133", "v134", "v135", "v136", "v137", "v138", "v139", "v140", "v141", "v142", "v143", "v144", "v145", "v146", "v147", "v148", "v149", "v150", "v151", "v152", "v153", "v154", "v155", "v156", "v157", "v158", "v159", "v160", "v161", "v162", "v163", "v164", "v165", "v166", "v167", "v168", "v169", "v170", "v171", "v172", "v173", "v174", "v175", "v176", "v177", "v178", "v179", "v180", "v181", "v182", "v183", "v184", "v185", "v186", "v187", "v188", "v189", "v190", "v191", "v192", "v193", "v194", "v195", "v196", "v197", "v198", "v199", "v200", "v201", "v202", "v203", "v204", "v205", "v206", "v207", "v208", "v209", "v210", "v211", "v212", "v213", "v214", "v215", "v216", "v217", "v218", "v219", "v220", "v221", "v222", "v223", "v224", "v225", "v226", "v227", "v228", "v229", "v230", "v231", "v232", "v233", "v234", "v235", "v236", "v237", "v238", "v239", "v240", "v241", "v242", "v243", "v244", "v245", "v246", "v247", "v248", "v249", "v250", "v251", "v252", "v253", "v254", "v255", "a0", "a1", "a2", "a3", "a4", "a5", "a6", "a7", "a8", "a9", "a10", "a11", "a12", "a13", "a14", "a15", "a16", "a17", "a18", "a19", "a20", "a21", "a22", "a23", "a24", "a25", "a26", "a27", "a28", "a29", "a30", "a31", "a32", "a33", "a34", "a35", "a36", "a37", "a38", "a39", "a40", "a41", "a42", "a43", "a44", "a45", "a46", "a47", "a48", "a49", "a50", "a51", "a52", "a53", "a54", "a55", "a56", "a57", "a58", "a59", "a60", "a61", "a62", "a63", "a64", "a65", "a66", "a67", "a68", "a69", "a70", "a71", "a72", "a73", "a74", "a75", "a76", "a77", "a78", "a79", "a80", "a81", "a82", "a83", "a84", "a85", "a86", "a87", "a88", "a89", "a90", "a91", "a92", "a93", "a94", "a95", "a96", "a97", "a98", "a99", "a100", "a101", "a102", "a103", "a104", "a105", "a106", "a107", "a108", "a109", "a110", "a111", "a112", "a113", "a114", "a115", "a116", "a117", "a118", "a119", "a120", "a121", "a122", "a123", "a124", "a125", "a126", "a127", "a128", "a129", "a130", "a131", "a132", "a133", "a134", "a135", "a136", "a137", "a138", "a139", "a140", "a141", "a142", "a143", "a144", "a145", "a146", "a147", "a148", "a149", "a150", "a151", "a152", "a153", "a154", "a155", "a156", "a157", "a158", "a159", "a160", "a161", "a162", "a163", "a164", "a165", "a166", "a167", "a168", "a169", "a170", "a171", "a172", "a173", "a174", "a175", "a176", "a177", "a178", "a179", "a180", "a181", "a182", "a183", "a184", "a185", "a186", "a187", "a188", "a189", "a190", "a191", "a192", "a193", "a194", "a195", "a196", "a197", "a198", "a199", "a200", "a201", "a202", "a203", "a204", "a205", "a206", "a207", "a208", "a209", "a210", "a211", "a212", "a213", "a214", "a215", "a216", "a217", "a218", "a219", "a220", "a221", "a222", "a223", "a224", "a225", "a226", "a227", "a228", "a229", "a230", "a231", "a232", "a233", "a234", "a235", "a236", "a237", "a238", "a239", "a240", "a241", "a242", "a243", "a244", "a245", "a246", "a247", "a248", "a249", "a250", "a251", "a252", "a253", "a254", "a255", "s4", "s5", "s6", "s7", "s8", "s9", "s10", "s11", "s12", "s13", "s14", "s15", "s16", "s17", "s18", "s19", "s20", "s21", "s22", "s23", "s24", "s25", "s26", "s27", "s28", "s29", "s30", "s31", "s32", "s33", "s34", "s35", "s36", "s37", "s38", "s39", "s40", "s41", "s42", "s43", "s44", "s45", "s46", "s47", "s48", "s49", "s50", "s51", "s52", "s53", "s54", "s55", "s56", "s57", "s58", "s59", "s60", "s61", "s62", "s63", "s64", "s65", "s66", "s67", "s68", "s69", "s70", "s71", "s72", "s73", "s74", "s75", "s76", "s77", "s78", "s79", "s80", "s81", "s82", "s83", "s84", "s85", "s86", "s87", "s88", "s89", "s90", "s91", "s92", "s93", "s94", "s95", "s96", "s97", "s98", "s99", "vcc", "scc", "memory"
  );
}
extern "C" void kernel_launch(void* const* d_in, const int* in_sizes, int n_in, void* d_out, int out_size, void* d_ws, size_t ws_size, hipStream_t stream) {
  (void)hipFuncSetAttribute((const void*)mega_kernel, hipFuncAttributeMaxDynamicSharedMemorySize, LDS_BYTES);
  mega_kernel<<<256, 256, LDS_BYTES, stream>>>((const bf16*)d_in[0], (const bf16*)d_in[1], (const bf16*)d_in[2], (const bf16*)d_in[3], (const bf16*)d_in[4], (const bf16*)d_in[5],
      (const bf16*)d_in[7], (const bf16*)d_in[8], (const bf16*)d_in[9], (const bf16*)d_in[10], (char*)d_ws + 0, (bf16*)d_out);
}
```

```cpp
#include <hip/hip_runtime.h>
#include <hip/hip_bf16.h>
typedef __hip_bfloat16 bf16;
constexpr int LDS_BYTES = 163840;
__global__ __launch_bounds__(256, 1) void mega_kernel(const bf16* x, const bf16* ckv, const bf16* wq, const bf16* wuk, const bf16* wuv, const bf16* wo,
    const bf16* w_r, const bf16* wg, const bf16* wu, const bf16* wd, char* ws, bf16* out) {
  asm volatile(
R"(.macro W n
s_waitcnt lgkmcnt(\n)
.endm
.macro D v,r,s,o
buffer_load_dwordx4 v\v, s[\r:\r+3], s\s offen offset:\o lds
.endm
.macro Ra d,a,o
ds_read_b128 a[\d:\d+3], v\a offset:\o
.endm
.macro Ta d,a,o
ds_read_b64_tr_b16 a[\d:\d+1], v\a offset:\o
.endm
.macro Zvaa d,a,b
v_mfma_f32_16x16x32_bf16 v[\d:\d+3], a[\a:\a+3], a[\b:\b+3], 0
.endm
.macro Zaaa d,a,b
v_mfma_f32_16x16x32_bf16 a[\d:\d+3], a[\a:\a+3], a[\b:\b+3], 0
.endm
.macro A d,a,b
v_add_f32 v\d, v\a, v\b
.endm
.macro U d,a,b
v_mul_f32 v\d, v\a, v\b
.endm
.macro Mvaa d,a,b
v_mfma_f32_16x16x32_bf16 v[\d:\d+3], a[\a:\a+3], a[\b:\b+3], v[\d:\d+3]
.endm
.macro Maaa d,a,b
v_mfma_f32_16x16x32_bf16 a[\d:\d+3], a[\a:\a+3], a[\b:\b+3], a[\d:\d+3]
.endm
.macro K d,a,b
v_cvt_pk_bf16_f32 v\d, v\a, v\b
.endm
.macro Rv d,a,o
ds_read_b128 v[\d:\d+3], v\a offset:\o
.endm
.macro Mvav d,a,b
v_mfma_f32_16x16x32_bf16 v[\d:\d+3], a[\a:\a+3], v[\b:\b+3], v[\d:\d+3]
.endm
.macro G d,a
v_accvgpr_read_b32 v\d, a\a
.endm
.macro Dc v,r,s,o
buffer_load_dwordx4 v\v, s[\r:\r+3], s\s offen offset:\o sc1 lds
.endm
.macro Zvav d,a,b
v_mfma_f32_16x16x32_bf16 v[\d:\d+3], a[\a:\a+3], v[\b:\b+3], 0
.endm
.macro Cvav d,a,b,c
v_mfma_f32_16x16x32_bf16 v[\d:\d+3], a[\a:\a+3], v[\b:\b+3], v[\c:\c+3]
.endm
.macro X d
v_exp_f32 v\d, v\d
.endm
.macro Cvaa d,a,b,c
v_mfma_f32_16x16x32_bf16 v[\d:\d+3], a[\a:\a+3], a[\b:\b+3], v[\c:\c+3]
.endm
.macro Maav d,a,b
v_mfma_f32_16x16x32_bf16 a[\d:\d+3], a[\a:\a+3], v[\b:\b+3], a[\d:\d+3]
.endm
.macro Mvvv d,a,b
v_mfma_f32_16x16x32_bf16 v[\d:\d+3], v[\a:\a+3], v[\b:\b+3], v[\d:\d+3]
.endm
.macro Zvvv d,a,b
v_mfma_f32_16x16x32_bf16 v[\d:\d+3], v[\a:\a+3], v[\b:\b+3], 0
.endm
s_load_dwordx16 s[16:31], %[ka], 0
s_load_dwordx8 s[32:39], %[ka], 64
s_getpc_b64 s[6:7]
.Lbeg%=:
s_mov_b32 s15, %[bid]
v_lshrrev_b32 v2, 6, %[tid]
v_and_b32 v100, 63, %[tid]
v_readfirstlane_b32 s14, v2
v_lshlrev_b32 v1, 4, v100
v_lshrrev_b32 v101, 4, v100
v_and_b32 v102, 15, v100
s_and_b32 s59, s15, 7
s_bfe_u32 s57, s15, 0x30003
s_lshr_b32 s58, s15, 6
s_lshl_b32 s56, s59, 2
s_add_u32 s56, s56, s58
s_lshr_b32 s80, s15, 3
s_lshl_b32 s80, s80, 2
s_add_u32 s80, s80, s14
s_lshl_b32 s80, s80, 11
v_min_u32 v104, 240, v1
v_lshl_add_u32 v104, v104, 3, s80
v_min_u32 v104, .Lend%=-.Lbeg%=-4, v104
global_load_dword v142, v104, s[6:7]
v_and_b32 v88, 0x1f0, v1
v_lshrrev_b32 v105, 1, v101
v_mul_u32_u24 v105, 1280, v105
v_add_u32 v28, v88, v105
s_lshl_b32 s85, s15, 2
s_add_u32 s85, s85, s14
s_mul_i32 s84, s85, 2560
v_and_b32 v105, 1, v101
v_lshl_or_b32 v29, v105, 31, v28
v_lshlrev_b32 v104, 1, v101
v_xor_b32 v104, v104, v102
v_lshlrev_b32 v104, 4, v104
v_lshl_add_u32 v16, v101, 13, v104
v_xor_b32 v105, 128, v16
v_add_u32 v17, 31744, v16
v_add_u32 v18, 63488, v105
v_add_u32 v19, 95232, v105
v_add_u32 v20, 131072, v16
v_add_u32 v21, 162816, v16
v_add_u32 v22, 194560, v105
v_add_u32 v23, 226304, v105
s_lshl_b32 s80, s14, 3
s_add_u32 s80, s80, s57
s_lshl_b32 s80, s80, 15
v_and_b32 v104, 7, v100
v_xor_b32 v104, v104, v101
v_lshlrev_b32 v104, 4, v104
v_lshrrev_b32 v105, 3, v100
v_lshl_add_u32 v104, v105, 10, v104
v_add_u32 v2, s80, v104
v_xor_b32 v105, 64, v2
v_add_u32 v3, 7168, v105
v_add_u32 v4, 14336, v2
v_add_u32 v5, 21504, v105
v_xor_b32 v6, 1835008, v2
v_xor_b32 v7, 1835008, v3
v_xor_b32 v8, 1835008, v4
v_xor_b32 v9, 1835008, v5
s_lshl_b32 s63, s14, 13
s_add_u32 s61, s63, 65536
s_and_b32 s80, s14, 1
s_lshr_b32 s81, s14, 1
s_lshl_b32 s62, s80, 14
s_lshl_b32 s82, s81, 13
s_add_u32 s62, s62, s82
s_lshl_b32 s81, s81, 18
s_lshl_b32 s83, s56, 8
s_add_u32 s81, s81, s83
s_mov_b32 s64, 0
s_cmp_eq_u32 s80, 0
W 0
s_cselect_b32 s44, s22, s24
s_cselect_b32 s45, s23, s25
s_cmp_eq_u32 s14, 3
s_cbranch_scc0 .LL_1%=
s_lshr_b32 s88, s15, 3
s_cmp_lt_u32 s88, 14
s_cbranch_scc0 .LL_1%=
v_lshlrev_b32 v104, 16, v1
s_mov_b64 s[86:87], s[36:37]
s_mov_b32 s89, 38797312
s_cmp_eq_u32 s88, 1
s_cselect_b32 s90, 39845888, 0
s_add_u32 s86, s86, s90
s_addc_u32 s87, s87, 0
s_cmp_eq_u32 s88, 2
s_cselect_b64 s[86:87], s[26:27], s[86:87]
s_cselect_b32 s89, 5242876, s89
s_cmp_eq_u32 s88, 3
s_cselect_b64 s[86:87], s[30:31], s[86:87]
s_cselect_b32 s89, 2211836, s89
s_cmp_eq_u32 s88, 4
s_cselect_b64 s[86:87], s[32:33], s[86:87]
s_cselect_b32 s89, 2211836, s89
s_cmp_eq_u32 s88, 5
s_cselect_b64 s[86:87], s[34:35], s[86:87]
s_cselect_b32 s89, 2211836, s89
s_cmp_eq_u32 s88, 6
s_cselect_b64 s[86:87], s[38:39], s[86:87]
s_cselect_b32 s89, 2621436, s89
s_cmp_eq_u32 s88, 7
s_cselect_b64 s[86:87], s[20:21], s[86:87]
s_cselect_b32 s89, 5242876, s89
s_cmp_eq_u32 s88, 8
s_cselect_b64 s[86:87], s[18:19], s[86:87]
s_cselect_b32 s89, 2097148, s89
s_cmp_eq_u32 s88, 9
s_cselect_b64 s[86:87], s[22:23], s[86:87]
s_cselect_b32 s89, 4194300, s89
s_cmp_eq_u32 s88, 10
s_cselect_b64 s[86:87], s[24:25], s[86:87]
s_cselect_b32 s89, 4194300, s89
s_cmp_eq_u32 s88, 11
s_cselect_b64 s[86:87], s[16:17], s[86:87]
s_cselect_b32 s89, 2621436, s89
s_cmp_eq_u32 s88, 12
s_cselect_b64 s[86:87], s[28:29], s[86:87]
s_cselect_b32 s89, 1276, s89
s_cmp_eq_u32 s88, 13
s_cselect_b64 s[86:87], s[6:7], s[86:87]
s_cselect_b32 s89, 0, s89
v_min_u32 v104, s89, v104
global_load_dword v142, v104, s[86:87] sc1
.LL_1%=:
s_mov_b32 s40, s18
s_and_b32 s41, s19, 0xffff
s_mov_b32 s42, 0x200000
s_mov_b32 s43, 0x20000
s_add_u32 s44, s44, s81
s_addc_u32 s45, s45, 0
s_and_b32 s45, s45, 0xffff
s_mov_b32 s46, 0x400000
s_mov_b32 s47, 0x20000
s_sub_u32 s46, s46, s81
s_load_dwordx2 s[12:13], s[36:37], 0
s_add_u32 m0, s61, 0
s_nop 0
D 2,40,64,0
D 3,40,64,1024
D 4,40,64,2048
D 5,40,64,3072
s_add_u32 m0, s61, 4096
s_nop 0
D 6,40,64,0
D 7,40,64,1024
D 8,40,64,2048
D 9,40,64,3072
s_lshl_b32 s65, s64, 12
s_add_u32 m0, s62, 0
s_nop 0
D 16,44,65,0
D 17,44,65,1024
D 18,44,65,2048
D 19,44,65,3072
s_add_u32 m0, s62, 4096
s_nop 0
D 20,44,65,0
D 21,44,65,1024
D 22,44,65,2048
D 23,44,65,3072
s_mov_b32 s52, s16
s_and_b32 s53, s17, 0xffff
s_mov_b32 s54, 2621440
s_mov_b32 s55, 0x20000
buffer_load_dwordx4 v[32:35], v28, s[52:55], s84 offen
buffer_load_dwordx4 v[36:39], v28, s[52:55], s84 offen offset:512
buffer_load_dwordx4 v[40:43], v29, s[52:55], s84 offen offset:1024
s_mov_b32 s52, s28
s_and_b32 s53, s29, 0xffff
s_mov_b32 s54, 1280
s_mov_b32 s55, 0x20000
buffer_load_dwordx4 v[44:47], v88, s[52:55], 0 offen offset:0
buffer_load_dwordx4 v[48:51], v88, s[52:55], 0 offen offset:512
buffer_load_dwordx4 v[52:55], v88, s[52:55], 0 offen offset:1024
s_mov_b32 s69, 0
s_cmp_eq_u32 s14, 2
s_cbranch_scc1 .LL_2%=
s_cmp_eq_u32 s14, 3
s_cbranch_scc1 .LL_3%=
s_lshl_b32 s80, s58, 3
s_add_u32 s80, s80, s57
s_lshl_b32 s80, s80, 15
s_lshl_b32 s82, s14, 7
s_add_u32 s80, s80, s82
s_add_u32 s80, s80, 256
v_lshl_add_u32 v104, v88, 6, s80
v_and_b32 v105, 512, v1
v_mul_u32_u24 v105, 3584, v105
v_xor_b32 v104, v104, v105
v_mov_b32 v140, s18
v_mov_b32 v141, s19
s_mov_b32 s68, 256
s_branch .LL_4%=
.LL_2%=:
v_lshrrev_b32 v104, 6, v1
v_lshlrev_b32 v104, 13, v104
v_and_b32 v105, 16, v1
v_lshl_add_u32 v104, v105, 3, v104
s_lshl_b32 s80, s57, 17
s_add_u32 s80, s80, s83
s_add_u32 s80, s80, 1048576
v_add_u32 v104, s80, v104
v_and_b32 v105, 32, v1
v_cmp_eq_u32 vcc, 0, v105
v_mov_b32 v140, s24
v_mov_b32 v141, s25
v_mov_b32 v105, s22
v_mov_b32 v106, s23
v_cndmask_b32 v140, v140, v105, vcc
v_cndmask_b32 v141, v141, v106, vcc
s_mov_b32 s68, 1048576
s_branch .LL_4%=
.LL_3%=:
v_lshrrev_b32 v104, 5, v1
v_lshlrev_b32 v104, 13, v104
v_and_b32 v105, 16, v1
v_lshl_add_u32 v104, v105, 3, v104
s_mul_i32 s80, s57, 655360
s_add_u32 s80, s80, s83
v_add_u32 v104, s80, v104
v_mov_b32 v140, s20
v_mov_b32 v141, s21
s_mov_b32 s68, 196608
.LL_4%=:
v_mov_b32 v105, 0
v_lshl_add_u64 v[140:141], v[104:105], 0, v[140:141]
s_add_u32 s64, s64, 128
s_and_b32 s64, s64, 1023
s_add_u32 m0, s61, 32768
s_nop 0
D 2,40,64,0
D 3,40,64,1024
D 4,40,64,2048
D 5,40,64,3072
s_add_u32 m0, s61, 36864
s_nop 0
D 6,40,64,0
D 7,40,64,1024
D 8,40,64,2048
D 9,40,64,3072
s_lshl_b32 s65, s64, 12
v_lshrrev_b32 v104, 1, v102
v_xor_b32 v104, v104, v101
v_lshlrev_b32 v104, 4, v104
v_lshl_add_u32 v104, v102, 7, v104
s_lshr_b32 s81, s14, 1
s_lshl_b32 s81, s81, 14
s_add_u32 s81, s81, 65536
v_add_u32 v26, s81, v104
v_xor_b32 v27, 64, v26
v_and_b32 v105, 1, v101
v_lshrrev_b32 v104, 2, v102
v_lshl_or_b32 v105, v105, 2, v104
v_lshl_add_u32 v104, v101, 3, v104
v_lshlrev_b32 v104, 8, v104
v_lshl_add_u32 v104, v105, 5, v104
v_and_b32 v105, 3, v102
v_lshl_add_u32 v10, v105, 3, v104
v_xor_b32 v11, 32, v10
v_xor_b32 v12, 64, v10
v_xor_b32 v13, 96, v10
v_xor_b32 v14, 128, v10
v_xor_b32 v15, 160, v10
v_xor_b32 v24, 192, v10
v_xor_b32 v25, 224, v10
.LL_8%=:
s_lshr_b32 s81, s15, 3
s_cmp_eq_u32 s14, 2
s_cbranch_scc1 .LL_5%=
s_cmp_eq_u32 s14, 3
s_cbranch_scc1 .LL_6%=
s_cmp_eq_u32 s14, 0
s_cselect_b64 s[76:77], s[30:31], s[32:33]
s_mov_b32 s72, 3456
s_mov_b32 s73, 28
s_mul_i32 s74, s81, 69120
s_mul_i32 s82, s59, 448
s_andn2_b32 s82, s82, 127
s_add_u32 s74, s74, s82
s_mov_b32 s75, 2211836
s_branch .LL_7%=
.LL_5%=:
s_mov_b64 s[76:77], s[34:35]
s_mov_b32 s72, 512
s_mov_b32 s73, 24
s_mul_i32 s74, s81, 8960
s_mul_i32 s82, s59, 286720
s_add_u32 s74, s74, s82
s_mov_b32 s75, 2211836
s_branch .LL_7%=
.LL_6%=:
s_mov_b64 s[76:77], s[26:27]
s_mov_b32 s72, 512
s_mov_b32 s73, 4
s_mul_i32 s74, s81, 20480
s_mul_i32 s82, s59, 655360
s_add_u32 s74, s74, s82
s_mov_b32 s75, 5242876
s_branch .LL_7%=
.LL_7%=:
s_mov_b32 s8, s36
s_and_b32 s9, s37, 0xffff
s_mov_b32 s10, 79691776
s_mov_b32 s11, 0x20000
s_add_u32 s48, s20, s83
s_addc_u32 s49, s21, 0
s_and_b32 s49, s49, 0xffff
s_mov_b32 s50, 5242880
s_mov_b32 s51, 0x20000
s_sub_u32 s50, s50, s83
s_lshl_b32 s80, s57, 1
s_and_b32 s81, s14, 1
s_add_u32 s80, s80, s81
s_sub_u32 s81, 31, s80
s_cmp_lt_u32 s14, 2
s_cselect_b32 s60, s80, s81
s_mul_i32 s28, s60, 81920
s_add_u32 s28, s28, 131072
s_add_u32 s4, s28, 10240
s_mov_b32 s80, 0
s_lshl_b32 s81, s80, 9
s_add_u32 s28, s28, s81
s_add_u32 s5, s80, s14
s_cmp_ge_u32 s5, 20
s_cselect_b32 s81, 20, 0
s_sub_u32 s5, s5, s81
s_and_b32 s80, s14, 1
s_mul_i32 s29, s80, 17172480
s_mul_i32 s80, s56, 536576
s_add_u32 s29, s29, s80
s_lshl_b32 s80, s57, 13
s_add_u32 s29, s29, s80
s_add_u32 s29, s29, 4194304
W 0
s_add_u32 s12, s12, 0x7f4a7c15
s_addc_u32 s13, s13, 0x9e3779b9
s_bitcmp1_b32 s14, 0
s_cbranch_scc1 .Lv_9%=
s_waitcnt vmcnt(14)
s_barrier
Ra 32,26,0
Ta 0,10,0
Ta 2,10,1024
Ra 36,26,2048
Ra 40,26,4096
Ra 44,26,6144
Ra 48,26,8192
Ra 52,26,10240
Ra 56,26,12288
Ra 60,26,14336
Ta 4,11,0
Ta 6,11,1024
W 6
Zvaa 160,0,32
s_add_u32 m0, s62, 32768
Ta 8,12,0
Ta 10,12,1024
Zvaa 192,0,36
Ta 12,13,0
Ta 14,13,1024
Zvaa 224,0,40
s_waitcnt vmcnt(11)
Zaaa 160,0,44
W 6
v_lshlrev_b32 v56, 16, v32
Zaaa 192,0,48
D 16,44,65,0
v_and_b32 v57, 0xffff0000, v32
Zaaa 224,0,52
v_lshlrev_b32 v58, 16, v33
v_and_b32 v59, 0xffff0000, v33
Zaaa 64,0,56
v_lshlrev_b32 v60, 16, v34
v_and_b32 v61, 0xffff0000, v34
)"
R"(Zaaa 96,0,60
W 4
v_lshlrev_b32 v62, 16, v35
v_and_b32 v63, 0xffff0000, v35
Zaaa 100,4,60
D 17,44,65,1024
v_lshlrev_b32 v64, 16, v36
Zaaa 68,4,56
Ta 16,14,0
v_and_b32 v65, 0xffff0000, v36
Zaaa 228,4,52
Ta 18,14,1024
v_lshlrev_b32 v66, 16, v37
Zaaa 196,4,48
v_and_b32 v67, 0xffff0000, v37
v_lshlrev_b32 v68, 16, v38
Zaaa 164,4,44
D 18,44,65,2048
v_and_b32 v69, 0xffff0000, v38
Zvaa 228,4,40
v_lshlrev_b32 v70, 16, v39
v_and_b32 v71, 0xffff0000, v39
Zvaa 196,4,36
v_lshlrev_b32 v72, 16, v40
v_and_b32 v73, 0xffff0000, v40
Zvaa 164,4,32
W 4
v_lshlrev_b32 v74, 16, v41
v_and_b32 v75, 0xffff0000, v41
Zvaa 168,8,32
D 19,44,65,3072
v_lshlrev_b32 v76, 16, v42
Zvaa 200,8,36
s_add_u32 m0, s62, 36864
v_and_b32 v77, 0xffff0000, v42
v_lshlrev_b32 v78, 16, v43
Zvaa 232,8,40
v_and_b32 v79, 0xffff0000, v43
A 80,56,57
Zaaa 168,8,44
Ta 20,15,0
Ta 22,15,1024
Zaaa 200,8,48
A 81,58,59
A 80,80,60
Zaaa 232,8,52
A 81,81,61
A 80,80,62
Zaaa 72,8,56
A 81,81,63
A 80,80,64
Zaaa 104,8,60
W 4
D 20,44,65,0
A 81,81,65
Zaaa 108,12,60
A 80,80,66
A 81,81,67
Zaaa 76,12,56
A 80,80,68
A 81,81,69
Zaaa 236,12,52
A 80,80,70
A 81,81,71
Zaaa 204,12,48
D 21,44,65,1024
A 80,80,72
Zaaa 172,12,44
Ta 24,24,0
A 81,81,73
Zvaa 236,12,40
Ta 26,24,1024
A 80,80,74
Zvaa 204,12,36
A 81,81,75
A 80,80,76
Zvaa 172,12,32
W 4
A 81,81,77
A 80,80,78
Zvaa 176,16,32
D 22,44,65,2048
A 81,81,79
Zvaa 208,16,36
Ra 128,27,0
A 80,80,81
Zvaa 240,16,40
Ra 132,27,2048
Ta 28,25,0
Zaaa 176,16,44
Ta 30,25,1024
v_add_f32_dpp v80, v80, v80 quad_perm:[1,0,3,2] row_mask:0xf bank_mask:0xf
Zaaa 208,16,48
D 23,44,65,3072
Ra 136,27,4096
Zaaa 240,16,52
Ra 140,27,6144
v_add_f32_dpp v80, v80, v80 quad_perm:[2,3,0,1] row_mask:0xf bank_mask:0xf
Zaaa 80,16,56
Zaaa 112,16,60
W 8
v_add_f32_dpp v80, v80, v80 row_half_mirror row_mask:0xf bank_mask:0xf
Zaaa 116,20,60
global_load_dword v142, v[140:141], off
v_lshl_add_u64 v[140:141], v[140:141], 0, s[68:69]
Ta 0,10,8192
Zaaa 84,20,56
v_add_f32_dpp v80, v80, v80 row_mirror row_mask:0xf bank_mask:0xf
v_mov_b32 v81, v80
Zaaa 244,20,52
Ta 2,10,9216
Ra 144,27,8192
Zaaa 212,20,48
v_permlane16_swap_b32 v80, v81
A 80,80,81
Zaaa 180,20,44
Ra 148,27,10240
v_mul_f32 v84, 0x3acccccd, v80
Zvaa 244,20,40
s_mov_b32 s86, 0xffff
s_mov_b32 s87, 0xffff
v_sub_f32 v82, v56, v84
Zvaa 212,20,36
U 80,82,82
v_sub_f32 v83, v57, v84
Zvaa 180,20,32
W 10
v_fmac_f32 v80, v83, v83
v_sub_f32 v82, v58, v84
Zvaa 184,24,32
v_fmac_f32 v80, v82, v82
v_sub_f32 v83, v59, v84
Zvaa 216,24,36
v_fmac_f32 v80, v83, v83
v_sub_f32 v82, v60, v84
Zvaa 248,24,40
v_fmac_f32 v80, v82, v82
v_sub_f32 v83, v61, v84
Zaaa 184,24,44
v_fmac_f32 v80, v83, v83
v_sub_f32 v82, v62, v84
Zaaa 216,24,48
v_fmac_f32 v80, v82, v82
v_sub_f32 v83, v63, v84
Zaaa 248,24,52
v_fmac_f32 v80, v83, v83
v_sub_f32 v82, v64, v84
Zaaa 88,24,56
v_fmac_f32 v80, v82, v82
v_sub_f32 v83, v65, v84
Zaaa 120,24,60
W 6
v_fmac_f32 v80, v83, v83
v_sub_f32 v82, v66, v84
Zaaa 124,28,60
v_fmac_f32 v80, v82, v82
v_sub_f32 v83, v67, v84
Zaaa 92,28,56
Ra 152,27,12288
v_fmac_f32 v80, v83, v83
Zaaa 252,28,52
v_sub_f32 v82, v68, v84
v_fmac_f32 v80, v82, v82
Zaaa 220,28,48
Ta 4,11,8192
v_sub_f32 v83, v69, v84
Zaaa 188,28,44
Ta 6,11,9216
v_fmac_f32 v80, v83, v83
Zvaa 252,28,40
Ra 156,27,14336
v_sub_f32 v82, v70, v84
Zvaa 220,28,36
v_fmac_f32 v80, v82, v82
v_sub_f32 v83, v71, v84
Zvaa 188,28,32
W 6
v_fmac_f32 v80, v83, v83
v_sub_f32 v82, v72, v84
Mvaa 160,0,128
U 81,82,82
v_sub_f32 v83, v73, v84
Mvaa 192,0,132
v_fmac_f32 v81, v83, v83
v_sub_f32 v82, v74, v84
Mvaa 224,0,136
v_fmac_f32 v81, v82, v82
v_sub_f32 v83, v75, v84
Maaa 160,0,140
W 0
v_fmac_f32 v81, v83, v83
v_sub_f32 v82, v76, v84
Maaa 192,0,144
Ta 8,12,8192
Ta 10,12,9216
Maaa 224,0,148
v_fmac_f32 v81, v82, v82
v_sub_f32 v83, v77, v84
Maaa 64,0,152
v_fmac_f32 v81, v83, v83
v_sub_f32 v82, v78, v84
Maaa 96,0,156
v_fmac_f32 v81, v82, v82
v_sub_f32 v83, v79, v84
Maaa 100,4,156
v_fmac_f32 v81, v83, v83
v_cndmask_b32_e64 v81, 0, v81, s[86:87]
Maaa 68,4,152
Ta 12,13,8192
A 80,80,81
Maaa 228,4,148
Ta 14,13,9216
Ta 16,14,8192
Maaa 196,4,144
Ta 18,14,9216
v_add_f32_dpp v80, v80, v80 quad_perm:[1,0,3,2] row_mask:0xf bank_mask:0xf
Maaa 164,4,140
Ta 20,15,8192
Ta 22,15,9216
Mvaa 228,4,136
v_add_f32_dpp v80, v80, v80 quad_perm:[2,3,0,1] row_mask:0xf bank_mask:0xf
Mvaa 196,4,132
Ta 24,24,8192
Ta 26,24,9216
Mvaa 164,4,128
W 8
v_add_f32_dpp v80, v80, v80 row_half_mirror row_mask:0xf bank_mask:0xf
Mvaa 168,8,128
Ta 28,25,8192
Ta 30,25,9216
Mvaa 200,8,132
v_add_f32_dpp v80, v80, v80 row_mirror row_mask:0xf bank_mask:0xf
v_mov_b32 v81, v80
Mvaa 232,8,136
Maaa 168,8,140
v_permlane16_swap_b32 v80, v81
A 80,80,81
Maaa 200,8,144
v_mul_f32 v80, 0x3acccccd, v80
K 80,80,80
Maaa 232,8,148
v_lshlrev_b32 v80, 16, v80
v_add_f32 v80, 0x3727c5ac, v80
Maaa 72,8,152
K 80,80,80
v_lshlrev_b32 v80, 16, v80
Maaa 104,8,156
W 8
v_sqrt_f32 v81, v80
Maaa 108,12,156
v_rcp_f32 v82, v81
Maaa 76,12,152
v_fma_f32 v83, -v81, v81, v80
v_mul_f32 v82, 0.5, v82
Maaa 236,12,148
s_waitcnt vmcnt(1) lgkmcnt(0)
s_barrier
v_fma_f32 v81, v83, v82, v81
Maaa 204,12,144
s_add_u32 s64, s64, 128
K 86,81,81
v_lshlrev_b32 v86, 16, v86
Maaa 172,12,140
v_rcp_f32 v87, v86
Mvaa 236,12,136
s_and_b32 s64, s64, 1023
K 85,84,84
v_lshlrev_b32 v85, 16, v85
Mvaa 204,12,132
v_sub_f32 v56, v56, v85
v_sub_f32 v57, v57, v85
Mvaa 172,12,128
K 80,56,57
v_lshlrev_b32 v56, 16, v80
Mvaa 176,16,128
s_add_u32 m0, s61, 0
v_and_b32 v57, 0xffff0000, v80
U 80,56,87
Mvaa 208,16,132
U 81,57,87
v_fma_f32 v82, -v80, v86, v56
Mvaa 240,16,136
v_fma_f32 v83, -v81, v86, v57
v_fmac_f32 v80, v82, v87
Maaa 176,16,140
v_fmac_f32 v81, v83, v87
K 32,80,81
Maaa 208,16,144
v_lshlrev_b32 v80, 16, v32
v_and_b32 v81, 0xffff0000, v32
Maaa 240,16,148
v_lshlrev_b32 v82, 16, v44
v_and_b32 v83, 0xffff0000, v44
Maaa 80,16,152
U 84,80,82
v_fmac_f32 v84, v81, v83
Maaa 112,16,156
v_sub_f32 v58, v58, v85
v_sub_f32 v59, v59, v85
Maaa 116,20,156
K 80,58,59
v_lshlrev_b32 v58, 16, v80
Maaa 84,20,152
v_and_b32 v59, 0xffff0000, v80
U 80,58,87
Maaa 244,20,148
U 81,59,87
v_fma_f32 v82, -v80, v86, v58
Maaa 212,20,144
v_fma_f32 v83, -v81, v86, v59
v_fmac_f32 v80, v82, v87
Maaa 180,20,140
v_fmac_f32 v81, v83, v87
K 33,80,81
Mvaa 244,20,136
Ra 32,26,32768
v_lshlrev_b32 v80, 16, v33
Mvaa 212,20,132
v_and_b32 v81, 0xffff0000, v33
v_lshlrev_b32 v82, 16, v45
Mvaa 180,20,128
v_and_b32 v83, 0xffff0000, v45
v_fmac_f32 v84, v80, v82
Mvaa 184,24,128
Ra 36,26,34816
v_fmac_f32 v84, v81, v83
Mvaa 216,24,132
v_sub_f32 v60, v60, v85
v_sub_f32 v61, v61, v85
Mvaa 248,24,136
K 80,60,61
v_lshlrev_b32 v60, 16, v80
Maaa 184,24,140
D 2,40,64,0
v_and_b32 v61, 0xffff0000, v80
Maaa 216,24,144
Ra 40,26,36864
U 80,60,87
Maaa 248,24,148
U 81,61,87
v_fma_f32 v82, -v80, v86, v60
Maaa 88,24,152
v_fma_f32 v83, -v81, v86, v61
v_fmac_f32 v80, v82, v87
Maaa 120,24,156
D 3,40,64,1024
v_fmac_f32 v81, v83, v87
Maaa 124,28,156
Ra 44,26,38912
K 34,80,81
Maaa 92,28,152
v_lshlrev_b32 v80, 16, v34
v_and_b32 v81, 0xffff0000, v34
Maaa 252,28,148
v_lshlrev_b32 v82, 16, v46
v_and_b32 v83, 0xffff0000, v46
Maaa 220,28,144
D 4,40,64,2048
Ta 0,10,32768
Maaa 188,28,140
Ta 2,10,33792
Ra 48,26,40960
Mvaa 252,28,136
v_fmac_f32 v84, v80, v82
v_fmac_f32 v84, v81, v83
Mvaa 220,28,132
Ra 52,26,43008
Ra 56,26,45056
Ta 4,11,32768
Ta 6,11,33792
Ra 60,26,47104
Mvaa 188,28,128
W 6
D 5,40,64,3072
v_sub_f32 v62, v62, v85
Mvaa 160,0,32
s_add_u32 m0, s61, 4096
v_sub_f32 v63, v63, v85
K 80,62,63
Mvaa 192,0,36
v_lshlrev_b32 v62, 16, v80
v_and_b32 v63, 0xffff0000, v80
Mvaa 224,0,40
U 80,62,87
U 81,63,87
Maaa 160,0,44
W 0
v_fma_f32 v82, -v80, v86, v62
v_fma_f32 v83, -v81, v86, v63
Maaa 192,0,48
D 6,40,64,0
v_fmac_f32 v80, v82, v87
Maaa 224,0,52
v_fmac_f32 v81, v83, v87
K 35,80,81
Maaa 64,0,56
v_lshlrev_b32 v80, 16, v35
v_and_b32 v81, 0xffff0000, v35
Maaa 96,0,60
v_lshlrev_b32 v82, 16, v47
)"
R"(v_and_b32 v83, 0xffff0000, v47
Maaa 100,4,60
D 7,40,64,1024
v_fmac_f32 v84, v80, v82
Maaa 68,4,56
v_fmac_f32 v84, v81, v83
v_sub_f32 v64, v64, v85
Maaa 228,4,52
v_sub_f32 v65, v65, v85
K 80,64,65
Maaa 196,4,48
v_lshlrev_b32 v64, 16, v80
v_and_b32 v65, 0xffff0000, v80
Maaa 164,4,44
Ta 8,12,32768
Ta 10,12,33792
Mvaa 228,4,40
D 8,40,64,2048
U 80,64,87
Mvaa 196,4,36
U 81,65,87
v_fma_f32 v82, -v80, v86, v64
Mvaa 164,4,32
W 0
v_fma_f32 v83, -v81, v86, v65
v_fmac_f32 v80, v82, v87
Mvaa 168,8,32
v_fmac_f32 v81, v83, v87
K 36,80,81
Mvaa 200,8,36
D 9,40,64,3072
v_lshlrev_b32 v80, 16, v36
Mvaa 232,8,40
s_lshl_b32 s65, s64, 12
v_and_b32 v81, 0xffff0000, v36
v_lshlrev_b32 v82, 16, v48
Maaa 168,8,44
s_add_u32 m0, s62, 0
v_and_b32 v83, 0xffff0000, v48
v_fmac_f32 v84, v80, v82
Maaa 200,8,48
Ta 12,13,32768
Ta 14,13,33792
Maaa 232,8,52
v_fmac_f32 v84, v81, v83
v_sub_f32 v66, v66, v85
Maaa 72,8,56
D 16,44,65,0
v_sub_f32 v67, v67, v85
Maaa 104,8,60
W 0
K 80,66,67
v_lshlrev_b32 v66, 16, v80
Maaa 108,12,60
v_and_b32 v67, 0xffff0000, v80
U 80,66,87
Maaa 76,12,56
U 81,67,87
v_fma_f32 v82, -v80, v86, v66
Maaa 236,12,52
D 17,44,65,1024
v_fma_f32 v83, -v81, v86, v67
Maaa 204,12,48
v_fmac_f32 v80, v82, v87
v_fmac_f32 v81, v83, v87
Maaa 172,12,44
Ta 16,14,32768
Ta 18,14,33792
Mvaa 236,12,40
K 37,80,81
v_lshlrev_b32 v80, 16, v37
Mvaa 204,12,36
D 18,44,65,2048
v_and_b32 v81, 0xffff0000, v37
Mvaa 172,12,32
W 0
v_lshlrev_b32 v82, 16, v49
v_and_b32 v83, 0xffff0000, v49
Mvaa 176,16,32
v_fmac_f32 v84, v80, v82
v_fmac_f32 v84, v81, v83
Mvaa 208,16,36
v_sub_f32 v68, v68, v85
v_sub_f32 v69, v69, v85
Mvaa 240,16,40
D 19,44,65,3072
K 80,68,69
Maaa 176,16,44
s_add_u32 m0, s62, 4096
v_lshlrev_b32 v68, 16, v80
v_and_b32 v69, 0xffff0000, v80
Maaa 208,16,48
Ta 20,15,32768
Ta 22,15,33792
Maaa 240,16,52
U 80,68,87
U 81,69,87
Maaa 80,16,56
D 20,44,65,0
v_fma_f32 v82, -v80, v86, v68
Maaa 112,16,60
W 0
v_fma_f32 v83, -v81, v86, v69
v_fmac_f32 v80, v82, v87
Maaa 116,20,60
v_fmac_f32 v81, v83, v87
K 38,80,81
Maaa 84,20,56
v_lshlrev_b32 v80, 16, v38
v_and_b32 v81, 0xffff0000, v38
Maaa 244,20,52
D 21,44,65,1024
v_lshlrev_b32 v82, 16, v50
Maaa 212,20,48
v_and_b32 v83, 0xffff0000, v50
v_fmac_f32 v84, v80, v82
Maaa 180,20,44
Ta 24,24,32768
Ta 26,24,33792
Mvaa 244,20,40
v_fmac_f32 v84, v81, v83
v_sub_f32 v70, v70, v85
Mvaa 212,20,36
D 22,44,65,2048
v_sub_f32 v71, v71, v85
Mvaa 180,20,32
W 0
K 80,70,71
v_lshlrev_b32 v70, 16, v80
Mvaa 184,24,32
v_and_b32 v71, 0xffff0000, v80
U 80,70,87
Mvaa 216,24,36
U 81,71,87
v_fma_f32 v82, -v80, v86, v70
Mvaa 248,24,40
D 23,44,65,3072
v_fma_f32 v83, -v81, v86, v71
Maaa 184,24,44
v_fmac_f32 v80, v82, v87
v_fmac_f32 v81, v83, v87
Maaa 216,24,48
Ra 128,27,32768
Ra 132,27,34816
Ta 28,25,32768
Ta 30,25,33792
Maaa 248,24,52
K 39,80,81
v_lshlrev_b32 v80, 16, v39
Maaa 88,24,56
global_load_dword v142, v[140:141], off
v_lshl_add_u64 v[140:141], v[140:141], 0, s[68:69]
v_and_b32 v81, 0xffff0000, v39
Maaa 120,24,60
W 0
v_lshlrev_b32 v82, 16, v51
v_and_b32 v83, 0xffff0000, v51
Maaa 124,28,60
v_fmac_f32 v84, v80, v82
v_fmac_f32 v84, v81, v83
Maaa 92,28,56
v_sub_f32 v72, v72, v85
v_sub_f32 v73, v73, v85
Maaa 252,28,52
Ra 136,27,36864
Ra 140,27,38912
Maaa 220,28,48
K 80,72,73
v_lshlrev_b32 v72, 16, v80
Maaa 188,28,44
Ta 0,10,40960
Ta 2,10,41984
Mvaa 252,28,40
v_and_b32 v73, 0xffff0000, v80
U 80,72,87
Mvaa 220,28,36
Ra 144,27,40960
Ra 148,27,43008
Ra 152,27,45056
Ta 4,11,40960
Ta 6,11,41984
Ra 156,27,47104
Mvaa 188,28,32
W 6
U 81,73,87
v_fma_f32 v82, -v80, v86, v72
Mvaa 160,0,128
v_fma_f32 v83, -v81, v86, v73
v_fmac_f32 v80, v82, v87
Mvaa 192,0,132
v_fmac_f32 v81, v83, v87
K 40,80,81
Mvaa 224,0,136
v_lshlrev_b32 v80, 16, v40
v_and_b32 v81, 0xffff0000, v40
Maaa 160,0,140
W 0
v_lshlrev_b32 v82, 16, v52
v_and_b32 v83, 0xffff0000, v52
Maaa 192,0,144
v_fmac_f32 v84, v80, v82
v_fmac_f32 v84, v81, v83
Maaa 224,0,148
v_sub_f32 v74, v74, v85
v_sub_f32 v75, v75, v85
Maaa 64,0,152
K 80,74,75
v_lshlrev_b32 v74, 16, v80
Maaa 96,0,156
v_and_b32 v75, 0xffff0000, v80
U 80,74,87
Maaa 100,4,156
U 81,75,87
v_fma_f32 v82, -v80, v86, v74
Maaa 68,4,152
v_fma_f32 v83, -v81, v86, v75
v_fmac_f32 v80, v82, v87
Maaa 228,4,148
v_fmac_f32 v81, v83, v87
K 41,80,81
Maaa 196,4,144
v_lshlrev_b32 v80, 16, v41
v_and_b32 v81, 0xffff0000, v41
Maaa 164,4,140
Ta 8,12,40960
Ta 10,12,41984
Mvaa 228,4,136
v_lshlrev_b32 v82, 16, v53
v_and_b32 v83, 0xffff0000, v53
Mvaa 196,4,132
v_fmac_f32 v84, v80, v82
v_fmac_f32 v84, v81, v83
Mvaa 164,4,128
W 0
v_sub_f32 v76, v76, v85
v_sub_f32 v77, v77, v85
Mvaa 168,8,128
K 80,76,77
v_lshlrev_b32 v76, 16, v80
Mvaa 200,8,132
v_and_b32 v77, 0xffff0000, v80
U 80,76,87
Mvaa 232,8,136
U 81,77,87
v_fma_f32 v82, -v80, v86, v76
Maaa 168,8,140
v_fma_f32 v83, -v81, v86, v77
v_fmac_f32 v80, v82, v87
Maaa 200,8,144
Ta 12,13,40960
Ta 14,13,41984
Maaa 232,8,148
v_fmac_f32 v81, v83, v87
K 42,80,81
Maaa 72,8,152
v_lshlrev_b32 v80, 16, v42
v_and_b32 v81, 0xffff0000, v42
Maaa 104,8,156
W 0
v_lshlrev_b32 v82, 16, v54
v_and_b32 v83, 0xffff0000, v54
Maaa 108,12,156
v_fmac_f32 v84, v80, v82
v_fmac_f32 v84, v81, v83
Maaa 76,12,152
Ta 16,14,40960
Ta 18,14,41984
Ta 20,15,40960
Ta 22,15,41984
Ta 24,24,40960
Ta 26,24,41984
Ta 28,25,40960
Ta 30,25,41984
v_sub_f32 v78, v78, v85
v_sub_f32 v79, v79, v85
K 80,78,79
v_lshlrev_b32 v78, 16, v80
v_and_b32 v79, 0xffff0000, v80
U 80,78,87
U 81,79,87
v_fma_f32 v82, -v80, v86, v78
v_fma_f32 v83, -v81, v86, v79
v_fmac_f32 v80, v82, v87
v_fmac_f32 v81, v83, v87
K 43,80,81
v_lshlrev_b32 v80, 16, v43
v_and_b32 v81, 0xffff0000, v43
v_lshlrev_b32 v82, 16, v55
v_and_b32 v83, 0xffff0000, v55
v_fmac_f32 v84, v80, v82
v_fmac_f32 v84, v81, v83
v_lshrrev_b32 v82, 9, v1
s_lshl_b32 s80, s14, 1
v_add_u32 v82, s80, v82
v_mul_u32_u24 v83, 1552, v82
v_add_u32 v83, v83, v88
v_add_u32 v83, 131072, v83
ds_write_b128 v83, v[32:35] offset:0
ds_write_b128 v83, v[36:39] offset:512
ds_write_b128 v83, v[40:43] offset:1024
v_add_f32_dpp v84, v84, v84 quad_perm:[1,0,3,2] row_mask:0xf bank_mask:0xf
s_nop 1
v_add_f32_dpp v84, v84, v84 quad_perm:[2,3,0,1] row_mask:0xf bank_mask:0xf
s_nop 1
v_add_f32_dpp v84, v84, v84 row_half_mirror row_mask:0xf bank_mask:0xf
s_nop 1
v_add_f32_dpp v84, v84, v84 row_mirror row_mask:0xf bank_mask:0xf
v_mov_b32 v81, v84
s_nop 1
v_permlane16_swap_b32 v84, v81
A 84,84,81
K 80,84,84
v_lshlrev_b32 v80, 16, v80
s_lshl_b32 s80, s15, 3
v_add_u32 v82, s80, v82
v_sub_u32 v83, 2047, v82
v_ashrrev_i32 v81, 31, v80
v_or_b32 v81, 0x80000000, v81
v_xor_b32 v80, v80, v81
v_and_b32 v80, 0xffff0000, v80
v_or_b32 v80, v80, v83
v_and_b32 v82, 7, v82
v_lshlrev_b32 v82, 2, v82
v_add_u32 v82, 143488, v82
ds_write_b32 v82, v80
Maaa 236,12,148
s_waitcnt vmcnt(1) lgkmcnt(0)
s_barrier
s_add_u32 s64, s64, 128
Maaa 204,12,144
v_bfe_u32 v80, v1, 4, 3
Maaa 172,12,140
Ra 32,26,0
v_mul_u32_u24 v81, 1552, v80
Mvaa 236,12,136
s_and_b32 s64, s64, 1023
v_lshrrev_b32 v82, 7, v1
Mvaa 204,12,132
v_lshl_add_u32 v81, v82, 4, v81
Mvaa 172,12,128
s_lshl_b32 s80, s14, 7
Ra 36,26,2048
Mvaa 176,16,128
s_add_u32 m0, s61, 32768
s_add_u32 s80, s80, 131072
Mvaa 208,16,132
v_add_u32 v81, s80, v81
Mvaa 240,16,136
ds_read_b128 v[32:35], v81
Ra 40,26,4096
Maaa 176,16,140
D 2,40,64,0
Rv 36,81,512
Maaa 208,16,144
s_and_b32 s80, s14, 2
Maaa 240,16,148
s_lshl_b32 s80, s80, 7
Ra 44,26,6144
Maaa 80,16,152
v_subrev_u32 v81, s80, v81
Maaa 112,16,156
D 3,40,64,1024
Rv 40,81,1024
Maaa 116,20,156
Ta 0,10,0
v_lshlrev_b32 v80, 2, v80
Maaa 84,20,152
Ta 2,10,1024
Ra 48,26,8192
Maaa 244,20,148
v_add_u32 v80, 143488, v80
Maaa 212,20,144
D 4,40,64,2048
ds_read_b32 v84, v80
Maaa 180,20,140
Ra 52,26,10240
v_mov_b32 v85, s13
Mvaa 244,20,136
v_lshrrev_b32 v83, 1, v1
Mvaa 212,20,132
s_mul_i32 s82, s15, 10240
s_add_u32 s82, s82, 131072
Ra 56,26,12288
Mvaa 180,20,128
s_lshl_b32 s80, s14, 10
D 5,40,64,3072
Mvaa 184,24,128
s_add_u32 m0, s61, 36864
s_add_u32 s83, s82, s80
Ta 4,11,0
Mvaa 216,24,132
Ta 6,11,1024
Ra 60,26,14336
Mvaa 248,24,136
Maaa 184,24,140
D 6,40,64,0
Maaa 216,24,144
Maaa 248,24,148
Maaa 88,24,152
Maaa 120,24,156
)"
R"(W 5
buffer_store_dwordx4 v[32:35], v1, s[8:11], s83 offen sc1
s_add_u32 s83, s83, 4096
Maaa 124,28,156
Ta 8,12,0
Ta 10,12,1024
Maaa 92,28,152
Maaa 252,28,148
Maaa 220,28,144
D 7,40,64,1024
Maaa 188,28,140
Mvaa 252,28,136
Mvaa 220,28,132
Mvaa 188,28,128
buffer_store_dwordx4 v[36:39], v1, s[8:11], s83 offen sc1
Mvaa 160,0,32
s_and_b32 s80, s80, 1024
s_add_u32 s83, s82, s80
s_add_u32 s83, s83, 8192
Mvaa 192,0,36
Ta 12,13,0
Ta 14,13,1024
Mvaa 224,0,40
Maaa 160,0,44
W 4
D 8,40,64,2048
Maaa 192,0,48
Maaa 224,0,52
Maaa 64,0,56
Maaa 96,0,60
buffer_store_dwordx4 v[40:43], v1, s[8:11], s83 offen sc1
Maaa 100,4,60
Ta 16,14,0
Ta 18,14,1024
Maaa 68,4,56
Maaa 228,4,52
Maaa 196,4,48
D 9,40,64,3072
Maaa 164,4,44
s_lshl_b32 s65, s64, 12
s_add_u32 m0, s62, 32768
Mvaa 228,4,40
Mvaa 196,4,36
Mvaa 164,4,32
W 4
D 16,44,65,0
Mvaa 168,8,32
Ta 20,15,0
Ta 22,15,1024
Mvaa 200,8,36
Mvaa 232,8,40
Maaa 168,8,44
D 17,44,65,1024
Maaa 200,8,48
Maaa 232,8,52
Maaa 72,8,56
Maaa 104,8,60
W 4
D 18,44,65,2048
Maaa 108,12,60
Ta 24,24,0
Ta 26,24,1024
Maaa 76,12,56
Maaa 236,12,52
Maaa 204,12,48
D 19,44,65,3072
Maaa 172,12,44
s_add_u32 m0, s62, 36864
Ra 128,27,0
Mvaa 236,12,40
Mvaa 204,12,36
Mvaa 172,12,32
W 5
D 20,44,65,0
Ra 132,27,2048
Mvaa 176,16,32
Ta 28,25,0
Ta 30,25,1024
Mvaa 208,16,36
Mvaa 240,16,40
Ra 136,27,4096
Maaa 176,16,44
D 21,44,65,1024
Maaa 208,16,48
Maaa 240,16,52
Ra 140,27,6144
Maaa 80,16,56
Maaa 112,16,60
W 8
D 22,44,65,2048
Maaa 116,20,60
Ta 0,10,8192
Ta 2,10,9216
Maaa 84,20,56
Ra 144,27,8192
Maaa 244,20,52
Maaa 212,20,48
D 23,44,65,3072
Ra 148,27,10240
Maaa 180,20,44
Mvaa 244,20,40
Mvaa 212,20,36
Ra 152,27,12288
Mvaa 180,20,32
W 11
Mvaa 184,24,32
Ta 4,11,8192
Ta 6,11,9216
Mvaa 216,24,36
Ra 156,27,14336
Mvaa 248,24,40
Maaa 184,24,44
Maaa 216,24,48
Maaa 248,24,52
Maaa 88,24,56
Maaa 120,24,60
W 10
Maaa 124,28,60
Ta 8,12,8192
Ta 10,12,9216
Maaa 92,28,56
Maaa 252,28,52
Maaa 220,28,48
Maaa 188,28,44
Mvaa 252,28,40
Mvaa 220,28,36
Mvaa 188,28,32
W 8
Mvaa 160,0,128
Ta 12,13,8192
Ta 14,13,9216
Mvaa 192,0,132
Mvaa 224,0,136
Maaa 160,0,140
W 4
Maaa 192,0,144
Maaa 224,0,148
Maaa 64,0,152
Maaa 96,0,156
Maaa 100,4,156
Ta 16,14,8192
Ta 18,14,9216
Maaa 68,4,152
Maaa 228,4,148
Maaa 196,4,144
Maaa 164,4,140
Ta 20,15,8192
Ta 22,15,9216
Mvaa 228,4,136
Mvaa 196,4,132
Ta 24,24,8192
Ta 26,24,9216
Mvaa 164,4,128
W 8
Mvaa 168,8,128
Ta 28,25,8192
Ta 30,25,9216
Mvaa 200,8,132
Mvaa 232,8,136
Maaa 168,8,140
Maaa 200,8,144
Maaa 232,8,148
Maaa 72,8,152
Maaa 104,8,156
W 8
Maaa 108,12,156
Maaa 76,12,152
Maaa 236,12,148
s_waitcnt vmcnt(0) lgkmcnt(0)
s_barrier
s_cmp_eq_u32 s14, 0
s_cbranch_scc0 .Lpubr_12%=
s_lshl_b32 s80, s15, 6
s_add_u32 s80, s80, 40960
s_mov_b64 exec, 0xff
buffer_store_dwordx2 v[84:85], v83, s[8:11], s80 offen sc1
s_mov_b64 exec, -1
.Lpubr_12%=:
Maaa 204,12,144
s_add_u32 s64, s64, 128
Maaa 172,12,140
Ra 32,26,32768
Mvaa 236,12,136
s_and_b32 s64, s64, 1023
Mvaa 204,12,132
Mvaa 172,12,128
s_add_u32 m0, s61, 0
Ra 36,26,34816
Mvaa 176,16,128
Mvaa 208,16,132
D 2,40,64,0
Mvaa 240,16,136
Ra 40,26,36864
Maaa 176,16,140
Maaa 208,16,144
Maaa 240,16,148
D 3,40,64,1024
Ra 44,26,38912
Maaa 80,16,152
Maaa 112,16,156
Maaa 116,20,156
Ta 0,10,32768
Ta 2,10,33792
Maaa 84,20,152
D 4,40,64,2048
Ra 48,26,40960
Maaa 244,20,148
Maaa 212,20,144
Ra 52,26,43008
Maaa 180,20,140
Mvaa 244,20,136
D 5,40,64,3072
Mvaa 212,20,132
s_add_u32 m0, s61, 4096
Ra 56,26,45056
Mvaa 180,20,128
Mvaa 184,24,128
Ta 4,11,32768
Ta 6,11,33792
Mvaa 216,24,132
D 6,40,64,0
Ra 60,26,47104
Mvaa 248,24,136
Maaa 184,24,140
Maaa 216,24,144
Maaa 248,24,148
D 7,40,64,1024
Maaa 88,24,152
Maaa 120,24,156
Maaa 124,28,156
Ta 8,12,32768
Ta 10,12,33792
Maaa 92,28,152
D 8,40,64,2048
Maaa 252,28,148
Maaa 220,28,144
Maaa 188,28,140
Mvaa 252,28,136
D 9,40,64,3072
Mvaa 220,28,132
s_lshl_b32 s65, s64, 12
s_add_u32 m0, s62, 0
Mvaa 188,28,128
W 8
Mvaa 160,0,32
Ta 12,13,32768
Ta 14,13,33792
Mvaa 192,0,36
D 16,44,65,0
Mvaa 224,0,40
Maaa 160,0,44
W 4
Maaa 192,0,48
Maaa 224,0,52
D 17,44,65,1024
Maaa 64,0,56
Maaa 96,0,60
Maaa 100,4,60
Ta 16,14,32768
Ta 18,14,33792
Maaa 68,4,56
D 18,44,65,2048
Maaa 228,4,52
Maaa 196,4,48
Maaa 164,4,44
Mvaa 228,4,40
D 19,44,65,3072
Mvaa 196,4,36
s_add_u32 m0, s62, 4096
Mvaa 164,4,32
W 4
Mvaa 168,8,32
Ta 20,15,32768
Ta 22,15,33792
Mvaa 200,8,36
D 20,44,65,0
Mvaa 232,8,40
Maaa 168,8,44
Maaa 200,8,48
Maaa 232,8,52
D 21,44,65,1024
Maaa 72,8,56
Maaa 104,8,60
W 4
Maaa 108,12,60
Ta 24,24,32768
Ta 26,24,33792
Maaa 76,12,56
D 22,44,65,2048
Maaa 236,12,52
Maaa 204,12,48
Maaa 172,12,44
Ra 128,27,32768
Mvaa 236,12,40
D 23,44,65,3072
Mvaa 204,12,36
s_add_u32 s65, s5, 0
s_cmp_ge_u32 s65, 20
s_cselect_b32 s68, 20, 0
s_sub_u32 s65, s65, s68
Mvaa 172,12,32
W 5
s_lshl_b32 s65, s65, 18
s_add_u32 m0, s63, 131072
Mvaa 176,16,32
Ra 132,27,34816
Ta 28,25,32768
Mvaa 208,16,36
D 16,48,65,0
Ta 30,25,33792
Mvaa 240,16,40
Ra 136,27,36864
Maaa 176,16,44
Maaa 208,16,48
Maaa 240,16,52
D 17,48,65,1024
Ra 140,27,38912
Maaa 80,16,56
Maaa 112,16,60
W 8
Maaa 116,20,60
Ta 0,10,40960
Ta 2,10,41984
Maaa 84,20,56
D 18,48,65,2048
Ra 144,27,40960
Maaa 244,20,52
Maaa 212,20,48
Ra 148,27,43008
Maaa 180,20,44
Mvaa 244,20,40
D 19,48,65,3072
Mvaa 212,20,36
s_add_u32 m0, s63, 135168
Ra 152,27,45056
Mvaa 180,20,32
W 11
Mvaa 184,24,32
Ta 4,11,40960
Ta 6,11,41984
Mvaa 216,24,36
D 20,48,65,0
Ra 156,27,47104
Mvaa 248,24,40
Maaa 184,24,44
Maaa 216,24,48
Maaa 248,24,52
D 21,48,65,1024
Maaa 88,24,56
Maaa 120,24,60
W 10
Maaa 124,28,60
Ta 8,12,40960
Ta 10,12,41984
Maaa 92,28,56
D 22,48,65,2048
Maaa 252,28,52
Maaa 220,28,48
Maaa 188,28,44
Mvaa 252,28,40
D 23,48,65,3072
Mvaa 220,28,36
Mvaa 188,28,32
W 8
Mvaa 160,0,128
Ta 12,13,40960
Ta 14,13,41984
Mvaa 192,0,132
global_load_dword v142, v[140:141], off
v_lshl_add_u64 v[140:141], v[140:141], 0, s[68:69]
Mvaa 224,0,136
Maaa 160,0,140
W 4
Maaa 192,0,144
Maaa 224,0,148
Maaa 64,0,152
Maaa 96,0,156
Maaa 100,4,156
Ta 16,14,40960
Ta 18,14,41984
Maaa 68,4,152
Maaa 228,4,148
Maaa 196,4,144
Maaa 164,4,140
Ta 20,15,40960
Ta 22,15,41984
Mvaa 228,4,136
Mvaa 196,4,132
Ta 24,24,40960
Ta 26,24,41984
Mvaa 164,4,128
W 8
Mvaa 168,8,128
Ta 28,25,40960
Ta 30,25,41984
Mvaa 200,8,132
Mvaa 232,8,136
Maaa 168,8,140
Maaa 200,8,144
Maaa 232,8,148
)"
R"(Maaa 72,8,152
Maaa 104,8,156
W 8
Maaa 108,12,156
Maaa 76,12,152
Maaa 236,12,148
s_waitcnt vmcnt(1) lgkmcnt(0)
s_barrier
s_add_u32 s64, s64, 128
Maaa 204,12,144
Maaa 172,12,140
Ra 32,26,0
Mvaa 236,12,136
s_and_b32 s64, s64, 1023
Mvaa 204,12,132
Mvaa 172,12,128
Ra 36,26,2048
Mvaa 176,16,128
s_add_u32 m0, s61, 32768
Mvaa 208,16,132
Mvaa 240,16,136
Ra 40,26,4096
Maaa 176,16,140
D 2,40,64,0
Maaa 208,16,144
Maaa 240,16,148
Ra 44,26,6144
Maaa 80,16,152
Maaa 112,16,156
D 3,40,64,1024
Maaa 116,20,156
Ta 0,10,0
Ta 2,10,1024
Maaa 84,20,152
Ra 48,26,8192
Maaa 244,20,148
Maaa 212,20,144
D 4,40,64,2048
Ra 52,26,10240
Maaa 180,20,140
Mvaa 244,20,136
Mvaa 212,20,132
Ra 56,26,12288
Mvaa 180,20,128
D 5,40,64,3072
Mvaa 184,24,128
s_add_u32 m0, s61, 36864
Ta 4,11,0
Ta 6,11,1024
Mvaa 216,24,132
Ra 60,26,14336
Mvaa 248,24,136
v_lshrrev_b32 v28, 1, v1
Maaa 184,24,140
s_lshl_b32 s80, s60, 9
D 6,40,64,0
Maaa 216,24,144
s_add_u32 s80, s80, 40960
Maaa 248,24,148
Maaa 88,24,152
Maaa 120,24,156
D 7,40,64,1024
Maaa 124,28,156
Ta 8,12,0
Ta 10,12,1024
Maaa 92,28,152
Maaa 252,28,148
Maaa 220,28,144
buffer_load_dwordx2 v[28:29], v28, s[8:11], s80 offen sc1
Maaa 188,28,140
Mvaa 252,28,136
Mvaa 220,28,132
Mvaa 188,28,128
W 8
D 8,40,64,2048
Mvaa 160,0,32
Ta 12,13,0
Ta 14,13,1024
Mvaa 192,0,36
Mvaa 224,0,40
Maaa 160,0,44
W 4
D 9,40,64,3072
Maaa 192,0,48
s_lshl_b32 s65, s64, 12
s_add_u32 m0, s62, 32768
Maaa 224,0,52
Maaa 64,0,56
Maaa 96,0,60
D 16,44,65,0
Maaa 100,4,60
Ta 16,14,0
Ta 18,14,1024
Maaa 68,4,56
Maaa 228,4,52
Maaa 196,4,48
D 17,44,65,1024
Maaa 164,4,44
Mvaa 228,4,40
Mvaa 196,4,36
Mvaa 164,4,32
W 4
D 18,44,65,2048
Mvaa 168,8,32
Ta 20,15,0
Ta 22,15,1024
Mvaa 200,8,36
Mvaa 232,8,40
Maaa 168,8,44
D 19,44,65,3072
Maaa 200,8,48
s_add_u32 m0, s62, 36864
Maaa 232,8,52
Maaa 72,8,56
Maaa 104,8,60
W 4
D 20,44,65,0
Maaa 108,12,60
Ta 24,24,0
Ta 26,24,1024
Maaa 76,12,56
Maaa 236,12,52
Maaa 204,12,48
D 21,44,65,1024
Maaa 172,12,44
Ra 128,27,0
Mvaa 236,12,40
Mvaa 204,12,36
Mvaa 172,12,32
W 5
D 22,44,65,2048
Ra 132,27,2048
Mvaa 176,16,32
Ta 28,25,0
Ta 30,25,1024
Mvaa 208,16,36
Mvaa 240,16,40
Ra 136,27,4096
Maaa 176,16,44
D 23,44,65,3072
v_lshrrev_b32 v138, 4, v1
Maaa 208,16,48
s_sub_u32 s81, s73, 1
v_min_u32 v138, s81, v138
v_lshrrev_b32 v139, 2, v138
Maaa 240,16,52
Ra 140,27,6144
v_mul_u32_u24 v139, s72, v139
Maaa 80,16,56
v_and_b32 v138, 3, v138
Maaa 112,16,60
W 8
v_lshl_add_u32 v138, v138, 7, v139
Maaa 116,20,60
s_waitcnt vmcnt(10)
v_cmp_eq_u32 vcc, s13, v29
s_cmp_eq_u64 vcc, -1
s_cbranch_scc0 .Lspin_13%=
.Lok_14%=:
v_add_u32 v138, s74, v138
Maaa 84,20,56
s_lshr_b32 s73, s73, 2
Ta 0,10,8192
Ta 2,10,9216
Maaa 244,20,52
s_mul_i32 s73, s73, s72
Ra 144,27,8192
v_bfe_u32 v144, v1, 8, 1
Maaa 212,20,48
v_min_u32 v139, s75, v138
v_mul_u32_u24 v144, 10240, v144
Maaa 180,20,44
Ra 148,27,10240
v_and_b32 v145, 0xf0, v1
Mvaa 244,20,40
global_load_dword v142, v139, s[76:77]
v_lshl_add_u32 v144, v145, 3, v144
Mvaa 212,20,36
s_mul_i32 s69, s58, 20480
Ra 152,27,12288
v_add_u32 v138, s73, v138
Mvaa 180,20,32
W 11
s_add_u32 s69, s69, s28
Mvaa 184,24,32
Ta 4,11,8192
Ta 6,11,9216
Mvaa 216,24,36
buffer_load_dword v142, v144, s[8:11], s69 offen offset:0 sc1
Ra 156,27,14336
Mvaa 248,24,40
Maaa 184,24,44
Maaa 216,24,48
Maaa 248,24,52
Maaa 88,24,56
Maaa 120,24,60
W 10
Maaa 124,28,60
Ta 8,12,8192
Ta 10,12,9216
Maaa 92,28,56
Maaa 252,28,52
Maaa 220,28,48
Maaa 188,28,44
Mvaa 252,28,40
Mvaa 220,28,36
Mvaa 188,28,32
W 8
Mvaa 160,0,128
Ta 12,13,8192
Ta 14,13,9216
Mvaa 192,0,132
Mvaa 224,0,136
Maaa 160,0,140
W 4
Maaa 192,0,144
Maaa 224,0,148
Maaa 64,0,152
Maaa 96,0,156
Maaa 100,4,156
Ta 16,14,8192
Ta 18,14,9216
Maaa 68,4,152
Maaa 228,4,148
Maaa 196,4,144
Maaa 164,4,140
Ta 20,15,8192
Ta 22,15,9216
Mvaa 228,4,136
Mvaa 196,4,132
Ta 24,24,8192
Ta 26,24,9216
Mvaa 164,4,128
W 8
Mvaa 168,8,128
Ta 28,25,8192
Ta 30,25,9216
Mvaa 200,8,132
Mvaa 232,8,136
Maaa 168,8,140
Maaa 200,8,144
Maaa 232,8,148
Maaa 72,8,152
Maaa 104,8,156
W 8
Maaa 108,12,156
Maaa 76,12,152
Maaa 236,12,148
s_waitcnt vmcnt(2) lgkmcnt(0)
s_barrier
s_add_u32 s64, s64, 128
Maaa 204,12,144
Maaa 172,12,140
Ra 32,26,32768
Mvaa 236,12,136
s_and_b32 s64, s64, 1023
Mvaa 204,12,132
Mvaa 172,12,128
Ra 36,26,34816
Mvaa 176,16,128
s_add_u32 m0, s61, 0
Mvaa 208,16,132
Mvaa 240,16,136
Ra 40,26,36864
Maaa 176,16,140
D 2,40,64,0
Maaa 208,16,144
Maaa 240,16,148
Ra 44,26,38912
Maaa 80,16,152
Maaa 112,16,156
D 3,40,64,1024
Maaa 116,20,156
Ta 0,10,32768
Ta 2,10,33792
Maaa 84,20,152
Ra 48,26,40960
Maaa 244,20,148
Maaa 212,20,144
D 4,40,64,2048
Ra 52,26,43008
Maaa 180,20,140
Mvaa 244,20,136
Mvaa 212,20,132
Ra 56,26,45056
Mvaa 180,20,128
D 5,40,64,3072
Mvaa 184,24,128
s_add_u32 m0, s61, 4096
Ta 4,11,32768
Ta 6,11,33792
Mvaa 216,24,132
Ra 60,26,47104
Mvaa 248,24,136
Maaa 184,24,140
D 6,40,64,0
Maaa 216,24,144
Maaa 248,24,148
Maaa 88,24,152
Maaa 120,24,156
D 7,40,64,1024
Maaa 124,28,156
Ta 8,12,32768
Ta 10,12,33792
Maaa 92,28,152
Maaa 252,28,148
Maaa 220,28,144
D 8,40,64,2048
Maaa 188,28,140
Mvaa 252,28,136
Mvaa 220,28,132
Mvaa 188,28,128
W 8
D 9,40,64,3072
Mvaa 160,0,32
s_lshl_b32 s65, s64, 12
s_add_u32 m0, s62, 0
Ta 12,13,32768
Mvaa 192,0,36
Ta 14,13,33792
Mvaa 224,0,40
Maaa 160,0,44
W 4
D 16,44,65,0
Maaa 192,0,48
Maaa 224,0,52
Maaa 64,0,56
Maaa 96,0,60
D 17,44,65,1024
Maaa 100,4,60
Ta 16,14,32768
Ta 18,14,33792
Maaa 68,4,56
Maaa 228,4,52
Maaa 196,4,48
D 18,44,65,2048
Maaa 164,4,44
Mvaa 228,4,40
Mvaa 196,4,36
Mvaa 164,4,32
W 4
D 19,44,65,3072
Mvaa 168,8,32
s_add_u32 m0, s62, 4096
Ta 20,15,32768
Ta 22,15,33792
Mvaa 200,8,36
Mvaa 232,8,40
Maaa 168,8,44
D 20,44,65,0
Maaa 200,8,48
Maaa 232,8,52
Maaa 72,8,56
Maaa 104,8,60
W 4
D 21,44,65,1024
Maaa 108,12,60
Ta 24,24,32768
Ta 26,24,33792
Maaa 76,12,56
Maaa 236,12,52
Maaa 204,12,48
D 22,44,65,2048
Maaa 172,12,44
Ra 128,27,32768
Mvaa 236,12,40
Mvaa 204,12,36
Mvaa 172,12,32
W 5
D 23,44,65,3072
Ra 132,27,34816
Mvaa 176,16,32
Ta 28,25,32768
Ta 30,25,33792
Mvaa 208,16,36
v_min_u32 v139, s75, v138
Mvaa 240,16,40
Ra 136,27,36864
Maaa 176,16,44
buffer_load_dword v142, v144, s[8:11], s69 offen offset:2048 sc1
Maaa 208,16,48
Maaa 240,16,52
s_add_u32 s69, s69, 4096
Ra 140,27,38912
Maaa 80,16,56
Maaa 112,16,60
W 8
global_load_dword v142, v139, s[76:77]
Maaa 116,20,60
Ta 0,10,40960
Ta 2,10,41984
Maaa 84,20,56
Ra 144,27,40960
Maaa 244,20,52
v_add_u32 v138, s73, v138
Maaa 212,20,48
Ra 148,27,43008
Maaa 180,20,44
Mvaa 244,20,40
Mvaa 212,20,36
)"
R"(Ra 152,27,45056
Mvaa 180,20,32
W 11
Mvaa 184,24,32
Ta 4,11,40960
Ta 6,11,41984
Mvaa 216,24,36
Ra 156,27,47104
Mvaa 248,24,40
Maaa 184,24,44
Maaa 216,24,48
Maaa 248,24,52
Maaa 88,24,56
Maaa 120,24,60
W 10
Maaa 124,28,60
Ta 8,12,40960
Ta 10,12,41984
Maaa 92,28,56
Maaa 252,28,52
Maaa 220,28,48
Maaa 188,28,44
Mvaa 252,28,40
Mvaa 220,28,36
Mvaa 188,28,32
W 8
Mvaa 160,0,128
Ta 12,13,40960
Ta 14,13,41984
Mvaa 192,0,132
Mvaa 224,0,136
Maaa 160,0,140
W 4
Maaa 192,0,144
Maaa 224,0,148
Maaa 64,0,152
Maaa 96,0,156
Maaa 100,4,156
Ta 16,14,40960
Ta 18,14,41984
Maaa 68,4,152
Maaa 228,4,148
Maaa 196,4,144
Maaa 164,4,140
Ta 20,15,40960
Ta 22,15,41984
Mvaa 228,4,136
Mvaa 196,4,132
Ta 24,24,40960
Ta 26,24,41984
Mvaa 164,4,128
W 8
Mvaa 168,8,128
Ta 28,25,40960
Ta 30,25,41984
Mvaa 200,8,132
Mvaa 232,8,136
Maaa 168,8,140
Maaa 200,8,144
Maaa 232,8,148
Maaa 72,8,152
Maaa 104,8,156
W 8
Maaa 108,12,156
Maaa 76,12,152
Maaa 236,12,148
s_waitcnt vmcnt(2) lgkmcnt(0)
s_barrier
s_add_u32 s64, s64, 128
Maaa 204,12,144
Maaa 172,12,140
Ra 32,26,0
Mvaa 236,12,136
s_and_b32 s64, s64, 1023
Mvaa 204,12,132
Mvaa 172,12,128
Ra 36,26,2048
Mvaa 176,16,128
s_add_u32 m0, s61, 32768
Mvaa 208,16,132
Mvaa 240,16,136
Ra 40,26,4096
Maaa 176,16,140
D 2,40,64,0
Maaa 208,16,144
Maaa 240,16,148
Ra 44,26,6144
Maaa 80,16,152
Maaa 112,16,156
D 3,40,64,1024
Maaa 116,20,156
Ta 0,10,0
Ta 2,10,1024
Maaa 84,20,152
Ra 48,26,8192
Maaa 244,20,148
Maaa 212,20,144
D 4,40,64,2048
Ra 52,26,10240
Maaa 180,20,140
Mvaa 244,20,136
Mvaa 212,20,132
Ra 56,26,12288
Mvaa 180,20,128
D 5,40,64,3072
Mvaa 184,24,128
s_add_u32 m0, s61, 36864
Ta 4,11,0
Ta 6,11,1024
Mvaa 216,24,132
Ra 60,26,14336
Mvaa 248,24,136
Maaa 184,24,140
D 6,40,64,0
Maaa 216,24,144
Maaa 248,24,148
Maaa 88,24,152
Maaa 120,24,156
D 7,40,64,1024
Maaa 124,28,156
Ta 8,12,0
Ta 10,12,1024
Maaa 92,28,152
Maaa 252,28,148
Maaa 220,28,144
D 8,40,64,2048
Maaa 188,28,140
Mvaa 252,28,136
Mvaa 220,28,132
Mvaa 188,28,128
W 8
D 9,40,64,3072
Mvaa 160,0,32
s_lshl_b32 s65, s64, 12
s_add_u32 m0, s62, 32768
Ta 12,13,0
Mvaa 192,0,36
Ta 14,13,1024
Mvaa 224,0,40
Maaa 160,0,44
W 4
D 16,44,65,0
Maaa 192,0,48
Maaa 224,0,52
Maaa 64,0,56
Maaa 96,0,60
D 17,44,65,1024
Maaa 100,4,60
Ta 16,14,0
Ta 18,14,1024
Maaa 68,4,56
Maaa 228,4,52
Maaa 196,4,48
D 18,44,65,2048
Maaa 164,4,44
Mvaa 228,4,40
Mvaa 196,4,36
Mvaa 164,4,32
W 4
D 19,44,65,3072
Mvaa 168,8,32
s_add_u32 m0, s62, 36864
Ta 20,15,0
Ta 22,15,1024
Mvaa 200,8,36
Mvaa 232,8,40
Maaa 168,8,44
D 20,44,65,0
Maaa 200,8,48
Maaa 232,8,52
Maaa 72,8,56
Maaa 104,8,60
W 4
D 21,44,65,1024
Maaa 108,12,60
Ta 24,24,0
Ta 26,24,1024
Maaa 76,12,56
Maaa 236,12,52
Maaa 204,12,48
D 22,44,65,2048
Maaa 172,12,44
Ra 128,27,0
Mvaa 236,12,40
Mvaa 204,12,36
Mvaa 172,12,32
W 5
D 23,44,65,3072
v_add_u32 v2, 131072, v10
Mvaa 176,16,32
Ra 132,27,2048
Ta 28,25,0
Mvaa 208,16,36
Ta 30,25,1024
v_add_u32 v3, 131072, v11
Mvaa 240,16,40
Ra 136,27,4096
v_add_u32 v4, 131072, v12
Maaa 176,16,44
buffer_load_dword v142, v144, s[8:11], s69 offen offset:0 sc1
Maaa 208,16,48
v_add_u32 v5, 131072, v13
Maaa 240,16,52
Ra 140,27,6144
Maaa 80,16,56
v_add_u32 v6, 131072, v14
Maaa 112,16,60
W 8
Maaa 116,20,60
Ta 0,10,8192
Ta 2,10,9216
Maaa 84,20,56
Ra 144,27,8192
v_add_u32 v7, 131072, v15
Maaa 244,20,52
v_add_u32 v8, 131072, v24
Maaa 212,20,48
Ra 148,27,10240
Maaa 180,20,44
v_add_u32 v9, 131072, v25
Mvaa 244,20,40
Mvaa 212,20,36
Ra 152,27,12288
Mvaa 180,20,32
W 11
Mvaa 184,24,32
Ta 4,11,8192
Ta 6,11,9216
Mvaa 216,24,36
Ra 156,27,14336
Mvaa 248,24,40
Maaa 184,24,44
Maaa 216,24,48
Maaa 248,24,52
Maaa 88,24,56
Maaa 120,24,60
W 10
Maaa 124,28,60
Ta 8,12,8192
Ta 10,12,9216
Maaa 92,28,56
Maaa 252,28,52
Maaa 220,28,48
Maaa 188,28,44
Mvaa 252,28,40
Mvaa 220,28,36
Mvaa 188,28,32
W 8
Mvaa 160,0,128
Ta 12,13,8192
Ta 14,13,9216
Mvaa 192,0,132
Mvaa 224,0,136
Maaa 160,0,140
W 4
Maaa 192,0,144
Maaa 224,0,148
Maaa 64,0,152
Maaa 96,0,156
Maaa 100,4,156
Ta 16,14,8192
Ta 18,14,9216
Maaa 68,4,152
Maaa 228,4,148
Maaa 196,4,144
Maaa 164,4,140
Ta 20,15,8192
Ta 22,15,9216
Mvaa 228,4,136
Mvaa 196,4,132
Ta 24,24,8192
Ta 26,24,9216
Mvaa 164,4,128
W 8
Mvaa 168,8,128
Ta 28,25,8192
Ta 30,25,9216
Mvaa 200,8,132
Mvaa 232,8,136
Maaa 168,8,140
Maaa 200,8,144
Maaa 232,8,148
Maaa 72,8,152
Maaa 104,8,156
W 8
Maaa 108,12,156
Maaa 76,12,152
Maaa 236,12,148
s_waitcnt vmcnt(1) lgkmcnt(0)
s_barrier
s_add_u32 s65, s5, 4
Maaa 204,12,144
Maaa 172,12,140
Ra 32,26,32768
Mvaa 236,12,136
s_cmp_ge_u32 s65, 20
s_cselect_b32 s68, 20, 0
Mvaa 204,12,132
Mvaa 172,12,128
s_sub_u32 s65, s65, s68
Ra 36,26,34816
Mvaa 176,16,128
Mvaa 208,16,132
s_lshl_b32 s65, s65, 18
Mvaa 240,16,136
Ra 40,26,36864
Maaa 176,16,140
Maaa 208,16,144
s_add_u32 m0, s63, 0
Maaa 240,16,148
Ra 44,26,38912
Maaa 80,16,152
D 16,48,65,0
Maaa 112,16,156
Maaa 116,20,156
Ta 0,10,32768
Ta 2,10,33792
Maaa 84,20,152
Ra 48,26,40960
Maaa 244,20,148
D 17,48,65,1024
Maaa 212,20,144
Ra 52,26,43008
Maaa 180,20,140
Mvaa 244,20,136
Mvaa 212,20,132
D 18,48,65,2048
Ra 56,26,45056
Mvaa 180,20,128
Mvaa 184,24,128
Ta 4,11,32768
Ta 6,11,33792
Mvaa 216,24,132
Ra 60,26,47104
Mvaa 248,24,136
D 19,48,65,3072
Maaa 184,24,140
s_add_u32 m0, s63, 4096
Maaa 216,24,144
Maaa 248,24,148
Maaa 88,24,152
D 20,48,65,0
Maaa 120,24,156
Maaa 124,28,156
Ta 8,12,32768
Ta 10,12,33792
Maaa 92,28,152
Maaa 252,28,148
D 21,48,65,1024
Maaa 220,28,144
Maaa 188,28,140
Mvaa 252,28,136
Mvaa 220,28,132
D 22,48,65,2048
Mvaa 188,28,128
W 8
Mvaa 160,0,32
Ta 12,13,32768
Ta 14,13,33792
Mvaa 192,0,36
Mvaa 224,0,40
D 23,48,65,3072
Maaa 160,0,44
W 4
s_add_u32 s65, s5, 8
s_cmp_ge_u32 s65, 20
s_cselect_b32 s68, 20, 0
Maaa 192,0,48
s_sub_u32 s65, s65, s68
s_lshl_b32 s65, s65, 18
s_add_u32 m0, s63, 65536
Maaa 224,0,52
Maaa 64,0,56
D 16,48,65,0
Maaa 96,0,60
Maaa 100,4,60
Ta 16,14,32768
Ta 18,14,33792
Maaa 68,4,56
Maaa 228,4,52
D 17,48,65,1024
Maaa 196,4,48
Maaa 164,4,44
Mvaa 228,4,40
Mvaa 196,4,36
D 18,48,65,2048
Mvaa 164,4,32
W 4
Mvaa 168,8,32
Ta 20,15,32768
Ta 22,15,33792
Mvaa 200,8,36
Mvaa 232,8,40
D 19,48,65,3072
Maaa 168,8,44
s_add_u32 m0, s63, 69632
Maaa 200,8,48
Maaa 232,8,52
Maaa 72,8,56
D 20,48,65,0
)"
R"(Maaa 104,8,60
W 4
Maaa 108,12,60
Ta 24,24,32768
Ta 26,24,33792
Maaa 76,12,56
Maaa 236,12,52
D 21,48,65,1024
Maaa 204,12,48
Maaa 172,12,44
Ra 128,27,32768
Mvaa 236,12,40
Mvaa 204,12,36
D 22,48,65,2048
Mvaa 172,12,32
W 5
Ra 132,27,34816
Mvaa 176,16,32
Ta 28,25,32768
Ta 30,25,33792
Mvaa 208,16,36
Mvaa 240,16,40
D 23,48,65,3072
v_min_u32 v139, s75, v138
Maaa 176,16,44
Ra 136,27,36864
Maaa 208,16,48
Maaa 240,16,52
Ra 140,27,38912
Maaa 80,16,56
buffer_load_dword v142, v144, s[8:11], s69 offen offset:2048 sc1
Maaa 112,16,60
W 8
s_add_u32 s69, s69, 4096
Maaa 116,20,60
Ta 0,10,40960
Ta 2,10,41984
Maaa 84,20,56
Ra 144,27,40960
Maaa 244,20,52
global_load_dword v142, v139, s[76:77]
v_add_u32 v138, s73, v138
Maaa 212,20,48
Ra 148,27,43008
Maaa 180,20,44
Mvaa 244,20,40
v_bfe_u32 v29, v1, 7, 1
Mvaa 212,20,36
Ra 152,27,45056
Mvaa 180,20,32
W 11
v_mul_u32_u24 v28, 10240, v29
Mvaa 184,24,32
Ta 4,11,40960
Ta 6,11,41984
Mvaa 216,24,36
Ra 156,27,47104
v_lshrrev_b32 v29, 8, v1
Mvaa 248,24,40
Maaa 184,24,44
v_lshl_add_u32 v28, v29, 7, v28
Maaa 216,24,48
v_and_b32 v29, 0x70, v1
Maaa 248,24,52
Maaa 88,24,56
v_add_u32 v28, v28, v29
Maaa 120,24,60
W 10
Maaa 124,28,60
s_and_b32 s80, s14, 2
Ta 8,12,40960
Ta 10,12,41984
Maaa 92,28,56
s_mul_i32 s80, s80, 20480
Maaa 252,28,52
Maaa 220,28,48
s_sub_u32 s81, 40960, s80
Maaa 188,28,44
Mvaa 252,28,40
s_add_u32 s93, s28, 20480
Mvaa 220,28,36
Mvaa 188,28,32
W 8
v_add_u32 v26, s80, v28
Mvaa 160,0,128
Ta 12,13,40960
Ta 14,13,41984
Mvaa 192,0,132
Mvaa 224,0,136
v_add_u32 v27, s81, v28
Maaa 160,0,140
W 4
Maaa 192,0,144
buffer_load_dwordx4 a[32:35], v26, s[8:11], s28 offen offset:0 sc1
Maaa 224,0,148
Maaa 64,0,152
Maaa 96,0,156
Maaa 100,4,156
buffer_load_dwordx4 a[36:39], v26, s[8:11], s93 offen offset:0 sc1
Ta 16,14,40960
Maaa 68,4,152
Ta 18,14,41984
Maaa 228,4,148
Maaa 196,4,144
Maaa 164,4,140
buffer_load_dwordx4 a[40:43], v27, s[8:11], s28 offen offset:0 sc1
Ta 20,15,40960
Mvaa 228,4,136
Ta 22,15,41984
Mvaa 196,4,132
Ta 24,24,40960
Ta 26,24,41984
Mvaa 164,4,128
W 8
Mvaa 168,8,128
buffer_load_dwordx4 a[44:47], v27, s[8:11], s93 offen offset:0 sc1
Ta 28,25,40960
Mvaa 200,8,132
s_add_u32 s28, s28, 512
s_cmp_eq_u32 s28, s4
s_cselect_b32 s93, 10240, 0
Ta 30,25,41984
Mvaa 232,8,136
s_sub_u32 s28, s28, s93
Maaa 168,8,140
s_add_u32 s93, s28, 20480
Maaa 200,8,144
Maaa 232,8,148
Maaa 72,8,152
buffer_load_dwordx4 a[48:51], v26, s[8:11], s28 offen offset:0 sc1
Maaa 104,8,156
W 8
Maaa 108,12,156
Maaa 76,12,152
Maaa 236,12,148
s_waitcnt vmcnt(7) lgkmcnt(0)
s_barrier
buffer_load_dwordx4 a[52:55], v26, s[8:11], s93 offen offset:0 sc1
Maaa 204,12,144
Maaa 172,12,140
Mvaa 236,12,136
Mvaa 204,12,132
buffer_load_dwordx4 a[56:59], v27, s[8:11], s28 offen offset:0 sc1
Mvaa 172,12,128
Mvaa 176,16,128
Mvaa 208,16,132
Mvaa 240,16,136
s_add_u32 s65, s5, 12
buffer_load_dwordx4 a[60:63], v27, s[8:11], s93 offen offset:0 sc1
Maaa 176,16,140
Maaa 208,16,144
Maaa 240,16,148
Maaa 80,16,152
Maaa 112,16,156
Maaa 116,20,156
Maaa 84,20,152
Maaa 244,20,148
s_cmp_ge_u32 s65, 20
s_cselect_b32 s68, 20, 0
Ta 0,2,0
Ta 2,2,1024
Maaa 212,20,144
Maaa 180,20,140
Mvaa 244,20,136
Mvaa 212,20,132
Ta 4,3,0
Ta 6,3,1024
Mvaa 180,20,128
Mvaa 184,24,128
Mvaa 216,24,132
s_sub_u32 s65, s65, s68
Mvaa 248,24,136
Ta 8,4,0
Ta 10,4,1024
Maaa 184,24,140
Maaa 216,24,144
Maaa 248,24,148
Maaa 88,24,152
Ta 12,5,0
Ta 14,5,1024
Maaa 120,24,156
Maaa 124,28,156
s_lshl_b32 s65, s65, 18
Maaa 92,28,152
Maaa 252,28,148
Ta 16,6,0
Ta 18,6,1024
Maaa 220,28,144
Maaa 188,28,140
Mvaa 252,28,136
Mvaa 220,28,132
Mvaa 188,28,128
s_add_u32 m0, s63, 32768
s_branch .Lq_10%=
.Lv_9%=:
s_waitcnt vmcnt(14)
s_barrier
Ra 32,26,0
Ta 0,10,16384
Ta 2,10,17408
Ra 36,26,2048
Ra 40,26,4096
Ra 44,26,6144
Ra 48,26,8192
Ra 52,26,10240
Ra 56,26,12288
Ra 60,26,14336
Ta 4,11,16384
Ta 6,11,17408
W 6
Zvaa 160,32,0
s_add_u32 m0, s62, 32768
Ta 8,12,16384
Ta 10,12,17408
Zvaa 164,36,0
Ta 12,13,16384
Ta 14,13,17408
Zvaa 224,40,0
s_waitcnt vmcnt(11)
Zvaa 228,44,0
W 6
v_lshlrev_b32 v56, 16, v32
Zaaa 192,48,0
D 16,44,65,0
v_and_b32 v57, 0xffff0000, v32
Zaaa 196,52,0
v_lshlrev_b32 v58, 16, v33
v_and_b32 v59, 0xffff0000, v33
Zaaa 64,56,0
v_lshlrev_b32 v60, 16, v34
v_and_b32 v61, 0xffff0000, v34
Zaaa 68,60,0
W 4
v_lshlrev_b32 v62, 16, v35
v_and_b32 v63, 0xffff0000, v35
Zaaa 76,60,4
D 17,44,65,1024
v_lshlrev_b32 v64, 16, v36
Zaaa 72,56,4
Ta 16,14,16384
v_and_b32 v65, 0xffff0000, v36
Zaaa 204,52,4
Ta 18,14,17408
v_lshlrev_b32 v66, 16, v37
Zaaa 200,48,4
v_and_b32 v67, 0xffff0000, v37
v_lshlrev_b32 v68, 16, v38
Zvaa 236,44,4
D 18,44,65,2048
v_and_b32 v69, 0xffff0000, v38
Zvaa 232,40,4
v_lshlrev_b32 v70, 16, v39
v_and_b32 v71, 0xffff0000, v39
Zvaa 172,36,4
v_lshlrev_b32 v72, 16, v40
v_and_b32 v73, 0xffff0000, v40
Zvaa 168,32,4
W 4
v_lshlrev_b32 v74, 16, v41
v_and_b32 v75, 0xffff0000, v41
Zvaa 176,32,8
D 19,44,65,3072
v_lshlrev_b32 v76, 16, v42
Zvaa 180,36,8
s_add_u32 m0, s62, 36864
v_and_b32 v77, 0xffff0000, v42
v_lshlrev_b32 v78, 16, v43
Zvaa 240,40,8
v_and_b32 v79, 0xffff0000, v43
A 80,56,57
Zvaa 244,44,8
Ta 20,15,16384
Ta 22,15,17408
Zaaa 208,48,8
A 81,58,59
A 80,80,60
Zaaa 212,52,8
A 81,81,61
A 80,80,62
Zaaa 80,56,8
A 81,81,63
A 80,80,64
Zaaa 84,60,8
W 4
D 20,44,65,0
A 81,81,65
Zaaa 92,60,12
A 80,80,66
A 81,81,67
Zaaa 88,56,12
A 80,80,68
A 81,81,69
Zaaa 220,52,12
A 80,80,70
A 81,81,71
Zaaa 216,48,12
D 21,44,65,1024
A 80,80,72
Zvaa 252,44,12
Ta 24,24,16384
A 81,81,73
Zvaa 248,40,12
Ta 26,24,17408
A 80,80,74
Zvaa 188,36,12
A 81,81,75
A 80,80,76
Zvaa 184,32,12
W 4
A 81,81,77
A 80,80,78
Zvaa 192,32,16
D 22,44,65,2048
A 81,81,79
Zvaa 196,36,16
Ra 128,27,0
A 80,80,81
Zaaa 160,40,16
Ra 132,27,2048
Ta 28,25,16384
Zaaa 164,44,16
Ta 30,25,17408
v_add_f32_dpp v80, v80, v80 quad_perm:[1,0,3,2] row_mask:0xf bank_mask:0xf
Zaaa 224,48,16
D 23,44,65,3072
Ra 136,27,4096
Zaaa 228,52,16
Ra 140,27,6144
v_add_f32_dpp v80, v80, v80 quad_perm:[2,3,0,1] row_mask:0xf bank_mask:0xf
Zaaa 96,56,16
Zaaa 100,60,16
W 8
v_add_f32_dpp v80, v80, v80 row_half_mirror row_mask:0xf bank_mask:0xf
Zaaa 108,60,20
global_load_dword v142, v[140:141], off
v_lshl_add_u64 v[140:141], v[140:141], 0, s[68:69]
Ta 0,10,24576
Zaaa 104,56,20
v_add_f32_dpp v80, v80, v80 row_mirror row_mask:0xf bank_mask:0xf
v_mov_b32 v81, v80
Zaaa 236,52,20
Ta 2,10,25600
Ra 144,27,8192
Zaaa 232,48,20
v_permlane16_swap_b32 v80, v81
A 80,80,81
Zaaa 172,44,20
Ra 148,27,10240
v_mul_f32 v84, 0x3acccccd, v80
Zaaa 168,40,20
s_mov_b32 s86, 0xffff
s_mov_b32 s87, 0xffff
v_sub_f32 v82, v56, v84
Zvaa 204,36,20
U 80,82,82
v_sub_f32 v83, v57, v84
Zvaa 200,32,20
W 10
v_fmac_f32 v80, v83, v83
v_sub_f32 v82, v58, v84
Zvaa 208,32,24
v_fmac_f32 v80, v82, v82
v_sub_f32 v83, v59, v84
Zvaa 212,36,24
v_fmac_f32 v80, v83, v83
v_sub_f32 v82, v60, v84
Zaaa 176,40,24
v_fmac_f32 v80, v82, v82
v_sub_f32 v83, v61, v84
Zaaa 180,44,24
v_fmac_f32 v80, v83, v83
v_sub_f32 v82, v62, v84
Zaaa 240,48,24
v_fmac_f32 v80, v82, v82
v_sub_f32 v83, v63, v84
Zaaa 244,52,24
v_fmac_f32 v80, v83, v83
v_sub_f32 v82, v64, v84
Zaaa 112,56,24
v_fmac_f32 v80, v82, v82
v_sub_f32 v83, v65, v84
Zaaa 116,60,24
W 6
v_fmac_f32 v80, v83, v83
v_sub_f32 v82, v66, v84
Zaaa 124,60,28
v_fmac_f32 v80, v82, v82
v_sub_f32 v83, v67, v84
Zaaa 120,56,28
Ra 152,27,12288
v_fmac_f32 v80, v83, v83
Zaaa 252,52,28
v_sub_f32 v82, v68, v84
v_fmac_f32 v80, v82, v82
Zaaa 248,48,28
Ta 4,11,24576
v_sub_f32 v83, v69, v84
Zaaa 188,44,28
Ta 6,11,25600
v_fmac_f32 v80, v83, v83
)"
R"(Zaaa 184,40,28
Ra 156,27,14336
v_sub_f32 v82, v70, v84
Zvaa 220,36,28
v_fmac_f32 v80, v82, v82
v_sub_f32 v83, v71, v84
Zvaa 216,32,28
W 6
v_fmac_f32 v80, v83, v83
v_sub_f32 v82, v72, v84
Mvaa 160,128,0
U 81,82,82
v_sub_f32 v83, v73, v84
Mvaa 164,132,0
v_fmac_f32 v81, v83, v83
v_sub_f32 v82, v74, v84
Mvaa 224,136,0
v_fmac_f32 v81, v82, v82
v_sub_f32 v83, v75, v84
Mvaa 228,140,0
W 0
v_fmac_f32 v81, v83, v83
v_sub_f32 v82, v76, v84
Maaa 192,144,0
Ta 8,12,24576
Ta 10,12,25600
Maaa 196,148,0
v_fmac_f32 v81, v82, v82
v_sub_f32 v83, v77, v84
Maaa 64,152,0
v_fmac_f32 v81, v83, v83
v_sub_f32 v82, v78, v84
Maaa 68,156,0
v_fmac_f32 v81, v82, v82
v_sub_f32 v83, v79, v84
Maaa 76,156,4
v_fmac_f32 v81, v83, v83
v_cndmask_b32_e64 v81, 0, v81, s[86:87]
Maaa 72,152,4
Ta 12,13,24576
A 80,80,81
Maaa 204,148,4
Ta 14,13,25600
Ta 16,14,24576
Maaa 200,144,4
Ta 18,14,25600
v_add_f32_dpp v80, v80, v80 quad_perm:[1,0,3,2] row_mask:0xf bank_mask:0xf
Mvaa 236,140,4
Ta 20,15,24576
Ta 22,15,25600
Mvaa 232,136,4
v_add_f32_dpp v80, v80, v80 quad_perm:[2,3,0,1] row_mask:0xf bank_mask:0xf
Mvaa 172,132,4
Ta 24,24,24576
Ta 26,24,25600
Mvaa 168,128,4
W 8
v_add_f32_dpp v80, v80, v80 row_half_mirror row_mask:0xf bank_mask:0xf
Mvaa 176,128,8
Ta 28,25,24576
Ta 30,25,25600
Mvaa 180,132,8
v_add_f32_dpp v80, v80, v80 row_mirror row_mask:0xf bank_mask:0xf
v_mov_b32 v81, v80
Mvaa 240,136,8
Mvaa 244,140,8
v_permlane16_swap_b32 v80, v81
A 80,80,81
Maaa 208,144,8
v_mul_f32 v80, 0x3acccccd, v80
K 80,80,80
Maaa 212,148,8
v_lshlrev_b32 v80, 16, v80
v_add_f32 v80, 0x3727c5ac, v80
Maaa 80,152,8
K 80,80,80
v_lshlrev_b32 v80, 16, v80
Maaa 84,156,8
W 8
v_sqrt_f32 v81, v80
Maaa 92,156,12
v_rcp_f32 v82, v81
Maaa 88,152,12
v_fma_f32 v83, -v81, v81, v80
v_mul_f32 v82, 0.5, v82
Maaa 220,148,12
s_waitcnt vmcnt(1) lgkmcnt(0)
s_barrier
v_fma_f32 v81, v83, v82, v81
Maaa 216,144,12
s_add_u32 s64, s64, 128
K 86,81,81
v_lshlrev_b32 v86, 16, v86
Mvaa 252,140,12
v_rcp_f32 v87, v86
Mvaa 248,136,12
s_and_b32 s64, s64, 1023
K 85,84,84
v_lshlrev_b32 v85, 16, v85
Mvaa 188,132,12
v_sub_f32 v56, v56, v85
v_sub_f32 v57, v57, v85
Mvaa 184,128,12
K 80,56,57
v_lshlrev_b32 v56, 16, v80
Mvaa 192,128,16
s_add_u32 m0, s61, 0
v_and_b32 v57, 0xffff0000, v80
U 80,56,87
Mvaa 196,132,16
U 81,57,87
v_fma_f32 v82, -v80, v86, v56
Maaa 160,136,16
v_fma_f32 v83, -v81, v86, v57
v_fmac_f32 v80, v82, v87
Maaa 164,140,16
v_fmac_f32 v81, v83, v87
K 32,80,81
Maaa 224,144,16
v_lshlrev_b32 v80, 16, v32
v_and_b32 v81, 0xffff0000, v32
Maaa 228,148,16
v_lshlrev_b32 v82, 16, v44
v_and_b32 v83, 0xffff0000, v44
Maaa 96,152,16
U 84,80,82
v_fmac_f32 v84, v81, v83
Maaa 100,156,16
v_sub_f32 v58, v58, v85
v_sub_f32 v59, v59, v85
Maaa 108,156,20
K 80,58,59
v_lshlrev_b32 v58, 16, v80
Maaa 104,152,20
v_and_b32 v59, 0xffff0000, v80
U 80,58,87
Maaa 236,148,20
U 81,59,87
v_fma_f32 v82, -v80, v86, v58
Maaa 232,144,20
v_fma_f32 v83, -v81, v86, v59
v_fmac_f32 v80, v82, v87
Maaa 172,140,20
v_fmac_f32 v81, v83, v87
K 33,80,81
Maaa 168,136,20
Ra 32,26,32768
v_lshlrev_b32 v80, 16, v33
Mvaa 204,132,20
v_and_b32 v81, 0xffff0000, v33
v_lshlrev_b32 v82, 16, v45
Mvaa 200,128,20
v_and_b32 v83, 0xffff0000, v45
v_fmac_f32 v84, v80, v82
Mvaa 208,128,24
Ra 36,26,34816
v_fmac_f32 v84, v81, v83
Mvaa 212,132,24
v_sub_f32 v60, v60, v85
v_sub_f32 v61, v61, v85
Maaa 176,136,24
K 80,60,61
v_lshlrev_b32 v60, 16, v80
Maaa 180,140,24
D 2,40,64,0
v_and_b32 v61, 0xffff0000, v80
Maaa 240,144,24
Ra 40,26,36864
U 80,60,87
Maaa 244,148,24
U 81,61,87
v_fma_f32 v82, -v80, v86, v60
Maaa 112,152,24
v_fma_f32 v83, -v81, v86, v61
v_fmac_f32 v80, v82, v87
Maaa 116,156,24
D 3,40,64,1024
v_fmac_f32 v81, v83, v87
Maaa 124,156,28
Ra 44,26,38912
K 34,80,81
Maaa 120,152,28
v_lshlrev_b32 v80, 16, v34
v_and_b32 v81, 0xffff0000, v34
Maaa 252,148,28
v_lshlrev_b32 v82, 16, v46
v_and_b32 v83, 0xffff0000, v46
Maaa 248,144,28
D 4,40,64,2048
Ta 0,10,49152
Maaa 188,140,28
Ta 2,10,50176
Ra 48,26,40960
Maaa 184,136,28
v_fmac_f32 v84, v80, v82
v_fmac_f32 v84, v81, v83
Mvaa 220,132,28
Ra 52,26,43008
Ra 56,26,45056
Ta 4,11,49152
Ta 6,11,50176
Ra 60,26,47104
Mvaa 216,128,28
W 6
D 5,40,64,3072
v_sub_f32 v62, v62, v85
Mvaa 160,32,0
s_add_u32 m0, s61, 4096
v_sub_f32 v63, v63, v85
K 80,62,63
Mvaa 164,36,0
v_lshlrev_b32 v62, 16, v80
v_and_b32 v63, 0xffff0000, v80
Mvaa 224,40,0
U 80,62,87
U 81,63,87
Mvaa 228,44,0
W 0
v_fma_f32 v82, -v80, v86, v62
v_fma_f32 v83, -v81, v86, v63
Maaa 192,48,0
D 6,40,64,0
v_fmac_f32 v80, v82, v87
Maaa 196,52,0
v_fmac_f32 v81, v83, v87
K 35,80,81
Maaa 64,56,0
v_lshlrev_b32 v80, 16, v35
v_and_b32 v81, 0xffff0000, v35
Maaa 68,60,0
v_lshlrev_b32 v82, 16, v47
v_and_b32 v83, 0xffff0000, v47
Maaa 76,60,4
D 7,40,64,1024
v_fmac_f32 v84, v80, v82
Maaa 72,56,4
v_fmac_f32 v84, v81, v83
v_sub_f32 v64, v64, v85
Maaa 204,52,4
v_sub_f32 v65, v65, v85
K 80,64,65
Maaa 200,48,4
v_lshlrev_b32 v64, 16, v80
v_and_b32 v65, 0xffff0000, v80
Mvaa 236,44,4
Ta 8,12,49152
Ta 10,12,50176
Mvaa 232,40,4
D 8,40,64,2048
U 80,64,87
Mvaa 172,36,4
U 81,65,87
v_fma_f32 v82, -v80, v86, v64
Mvaa 168,32,4
W 0
v_fma_f32 v83, -v81, v86, v65
v_fmac_f32 v80, v82, v87
Mvaa 176,32,8
v_fmac_f32 v81, v83, v87
K 36,80,81
Mvaa 180,36,8
D 9,40,64,3072
v_lshlrev_b32 v80, 16, v36
Mvaa 240,40,8
s_lshl_b32 s65, s64, 12
v_and_b32 v81, 0xffff0000, v36
v_lshlrev_b32 v82, 16, v48
Mvaa 244,44,8
s_add_u32 m0, s62, 0
v_and_b32 v83, 0xffff0000, v48
v_fmac_f32 v84, v80, v82
Maaa 208,48,8
Ta 12,13,49152
Ta 14,13,50176
Maaa 212,52,8
v_fmac_f32 v84, v81, v83
v_sub_f32 v66, v66, v85
Maaa 80,56,8
D 16,44,65,0
v_sub_f32 v67, v67, v85
Maaa 84,60,8
W 0
K 80,66,67
v_lshlrev_b32 v66, 16, v80
Maaa 92,60,12
v_and_b32 v67, 0xffff0000, v80
U 80,66,87
Maaa 88,56,12
U 81,67,87
v_fma_f32 v82, -v80, v86, v66
Maaa 220,52,12
D 17,44,65,1024
v_fma_f32 v83, -v81, v86, v67
Maaa 216,48,12
v_fmac_f32 v80, v82, v87
v_fmac_f32 v81, v83, v87
Mvaa 252,44,12
Ta 16,14,49152
Ta 18,14,50176
Mvaa 248,40,12
K 37,80,81
v_lshlrev_b32 v80, 16, v37
Mvaa 188,36,12
D 18,44,65,2048
v_and_b32 v81, 0xffff0000, v37
Mvaa 184,32,12
W 0
v_lshlrev_b32 v82, 16, v49
v_and_b32 v83, 0xffff0000, v49
Mvaa 192,32,16
v_fmac_f32 v84, v80, v82
v_fmac_f32 v84, v81, v83
Mvaa 196,36,16
v_sub_f32 v68, v68, v85
v_sub_f32 v69, v69, v85
Maaa 160,40,16
D 19,44,65,3072
K 80,68,69
Maaa 164,44,16
s_add_u32 m0, s62, 4096
v_lshlrev_b32 v68, 16, v80
v_and_b32 v69, 0xffff0000, v80
Maaa 224,48,16
Ta 20,15,49152
Ta 22,15,50176
Maaa 228,52,16
U 80,68,87
U 81,69,87
Maaa 96,56,16
D 20,44,65,0
v_fma_f32 v82, -v80, v86, v68
Maaa 100,60,16
W 0
v_fma_f32 v83, -v81, v86, v69
v_fmac_f32 v80, v82, v87
Maaa 108,60,20
v_fmac_f32 v81, v83, v87
K 38,80,81
Maaa 104,56,20
v_lshlrev_b32 v80, 16, v38
v_and_b32 v81, 0xffff0000, v38
Maaa 236,52,20
D 21,44,65,1024
v_lshlrev_b32 v82, 16, v50
Maaa 232,48,20
v_and_b32 v83, 0xffff0000, v50
v_fmac_f32 v84, v80, v82
Maaa 172,44,20
Ta 24,24,49152
Ta 26,24,50176
Maaa 168,40,20
v_fmac_f32 v84, v81, v83
v_sub_f32 v70, v70, v85
Mvaa 204,36,20
D 22,44,65,2048
v_sub_f32 v71, v71, v85
Mvaa 200,32,20
W 0
K 80,70,71
v_lshlrev_b32 v70, 16, v80
Mvaa 208,32,24
v_and_b32 v71, 0xffff0000, v80
U 80,70,87
Mvaa 212,36,24
U 81,71,87
v_fma_f32 v82, -v80, v86, v70
Maaa 176,40,24
D 23,44,65,3072
v_fma_f32 v83, -v81, v86, v71
Maaa 180,44,24
v_fmac_f32 v80, v82, v87
v_fmac_f32 v81, v83, v87
Maaa 240,48,24
Ra 128,27,32768
Ra 132,27,34816
Ta 28,25,49152
Ta 30,25,50176
Maaa 244,52,24
K 39,80,81
v_lshlrev_b32 v80, 16, v39
Maaa 112,56,24
global_load_dword v142, v[140:141], off
v_lshl_add_u64 v[140:141], v[140:141], 0, s[68:69]
v_and_b32 v81, 0xffff0000, v39
Maaa 116,60,24
W 0
v_lshlrev_b32 v82, 16, v51
v_and_b32 v83, 0xffff0000, v51
Maaa 124,60,28
v_fmac_f32 v84, v80, v82
v_fmac_f32 v84, v81, v83
Maaa 120,56,28
v_sub_f32 v72, v72, v85
v_sub_f32 v73, v73, v85
Maaa 252,52,28
Ra 136,27,36864
Ra 140,27,38912
Maaa 248,48,28
K 80,72,73
v_lshlrev_b32 v72, 16, v80
)"
R"(Maaa 188,44,28
Ta 0,10,57344
Ta 2,10,58368
Maaa 184,40,28
v_and_b32 v73, 0xffff0000, v80
U 80,72,87
Mvaa 220,36,28
Ra 144,27,40960
Ra 148,27,43008
Ra 152,27,45056
Ta 4,11,57344
Ta 6,11,58368
Ra 156,27,47104
Mvaa 216,32,28
W 6
U 81,73,87
v_fma_f32 v82, -v80, v86, v72
Mvaa 160,128,0
v_fma_f32 v83, -v81, v86, v73
v_fmac_f32 v80, v82, v87
Mvaa 164,132,0
v_fmac_f32 v81, v83, v87
K 40,80,81
Mvaa 224,136,0
v_lshlrev_b32 v80, 16, v40
v_and_b32 v81, 0xffff0000, v40
Mvaa 228,140,0
W 0
v_lshlrev_b32 v82, 16, v52
v_and_b32 v83, 0xffff0000, v52
Maaa 192,144,0
v_fmac_f32 v84, v80, v82
v_fmac_f32 v84, v81, v83
Maaa 196,148,0
v_sub_f32 v74, v74, v85
v_sub_f32 v75, v75, v85
Maaa 64,152,0
K 80,74,75
v_lshlrev_b32 v74, 16, v80
Maaa 68,156,0
v_and_b32 v75, 0xffff0000, v80
U 80,74,87
Maaa 76,156,4
U 81,75,87
v_fma_f32 v82, -v80, v86, v74
Maaa 72,152,4
v_fma_f32 v83, -v81, v86, v75
v_fmac_f32 v80, v82, v87
Maaa 204,148,4
v_fmac_f32 v81, v83, v87
K 41,80,81
Maaa 200,144,4
v_lshlrev_b32 v80, 16, v41
v_and_b32 v81, 0xffff0000, v41
Mvaa 236,140,4
Ta 8,12,57344
Ta 10,12,58368
Mvaa 232,136,4
v_lshlrev_b32 v82, 16, v53
v_and_b32 v83, 0xffff0000, v53
Mvaa 172,132,4
v_fmac_f32 v84, v80, v82
v_fmac_f32 v84, v81, v83
Mvaa 168,128,4
W 0
v_sub_f32 v76, v76, v85
v_sub_f32 v77, v77, v85
Mvaa 176,128,8
K 80,76,77
v_lshlrev_b32 v76, 16, v80
Mvaa 180,132,8
v_and_b32 v77, 0xffff0000, v80
U 80,76,87
Mvaa 240,136,8
U 81,77,87
v_fma_f32 v82, -v80, v86, v76
Mvaa 244,140,8
v_fma_f32 v83, -v81, v86, v77
v_fmac_f32 v80, v82, v87
Maaa 208,144,8
Ta 12,13,57344
Ta 14,13,58368
Maaa 212,148,8
v_fmac_f32 v81, v83, v87
K 42,80,81
Maaa 80,152,8
v_lshlrev_b32 v80, 16, v42
v_and_b32 v81, 0xffff0000, v42
Maaa 84,156,8
W 0
v_lshlrev_b32 v82, 16, v54
v_and_b32 v83, 0xffff0000, v54
Maaa 92,156,12
v_fmac_f32 v84, v80, v82
v_fmac_f32 v84, v81, v83
Maaa 88,152,12
Ta 16,14,57344
Ta 18,14,58368
Ta 20,15,57344
Ta 22,15,58368
Ta 24,24,57344
Ta 26,24,58368
Ta 28,25,57344
Ta 30,25,58368
v_sub_f32 v78, v78, v85
v_sub_f32 v79, v79, v85
K 80,78,79
v_lshlrev_b32 v78, 16, v80
v_and_b32 v79, 0xffff0000, v80
U 80,78,87
U 81,79,87
v_fma_f32 v82, -v80, v86, v78
v_fma_f32 v83, -v81, v86, v79
v_fmac_f32 v80, v82, v87
v_fmac_f32 v81, v83, v87
K 43,80,81
v_lshlrev_b32 v80, 16, v43
v_and_b32 v81, 0xffff0000, v43
v_lshlrev_b32 v82, 16, v55
v_and_b32 v83, 0xffff0000, v55
v_fmac_f32 v84, v80, v82
v_fmac_f32 v84, v81, v83
v_lshrrev_b32 v82, 9, v1
s_lshl_b32 s80, s14, 1
v_add_u32 v82, s80, v82
v_mul_u32_u24 v83, 1552, v82
v_add_u32 v83, v83, v88
v_add_u32 v83, 131072, v83
ds_write_b128 v83, v[32:35] offset:0
ds_write_b128 v83, v[36:39] offset:512
ds_write_b128 v83, v[40:43] offset:1024
v_add_f32_dpp v84, v84, v84 quad_perm:[1,0,3,2] row_mask:0xf bank_mask:0xf
s_nop 1
v_add_f32_dpp v84, v84, v84 quad_perm:[2,3,0,1] row_mask:0xf bank_mask:0xf
s_nop 1
v_add_f32_dpp v84, v84, v84 row_half_mirror row_mask:0xf bank_mask:0xf
s_nop 1
v_add_f32_dpp v84, v84, v84 row_mirror row_mask:0xf bank_mask:0xf
v_mov_b32 v81, v84
s_nop 1
v_permlane16_swap_b32 v84, v81
A 84,84,81
K 80,84,84
v_lshlrev_b32 v80, 16, v80
s_lshl_b32 s80, s15, 3
v_add_u32 v82, s80, v82
v_sub_u32 v83, 2047, v82
v_ashrrev_i32 v81, 31, v80
v_or_b32 v81, 0x80000000, v81
v_xor_b32 v80, v80, v81
v_and_b32 v80, 0xffff0000, v80
v_or_b32 v80, v80, v83
v_and_b32 v82, 7, v82
v_lshlrev_b32 v82, 2, v82
v_add_u32 v82, 143488, v82
ds_write_b32 v82, v80
Maaa 220,148,12
s_waitcnt vmcnt(1) lgkmcnt(0)
s_barrier
s_add_u32 s64, s64, 128
Maaa 216,144,12
v_bfe_u32 v80, v1, 4, 3
Mvaa 252,140,12
Ra 32,26,0
v_mul_u32_u24 v81, 1552, v80
Mvaa 248,136,12
s_and_b32 s64, s64, 1023
v_lshrrev_b32 v82, 7, v1
Mvaa 188,132,12
v_lshl_add_u32 v81, v82, 4, v81
Mvaa 184,128,12
s_lshl_b32 s80, s14, 7
Ra 36,26,2048
Mvaa 192,128,16
s_add_u32 m0, s61, 32768
s_add_u32 s80, s80, 131072
Mvaa 196,132,16
v_add_u32 v81, s80, v81
Maaa 160,136,16
ds_read_b128 v[32:35], v81
Ra 40,26,4096
Maaa 164,140,16
D 2,40,64,0
Rv 36,81,512
Maaa 224,144,16
s_and_b32 s80, s14, 2
Maaa 228,148,16
s_lshl_b32 s80, s80, 7
Ra 44,26,6144
Maaa 96,152,16
v_subrev_u32 v81, s80, v81
Maaa 100,156,16
D 3,40,64,1024
Rv 40,81,1024
Maaa 108,156,20
Ta 0,10,16384
v_lshlrev_b32 v80, 2, v80
Maaa 104,152,20
Ta 2,10,17408
Ra 48,26,8192
Maaa 236,148,20
v_add_u32 v80, 143488, v80
Maaa 232,144,20
D 4,40,64,2048
ds_read_b32 v84, v80
Maaa 172,140,20
Ra 52,26,10240
v_mov_b32 v85, s13
Maaa 168,136,20
v_lshrrev_b32 v83, 1, v1
Mvaa 204,132,20
s_mul_i32 s82, s15, 10240
s_add_u32 s82, s82, 131072
Ra 56,26,12288
Mvaa 200,128,20
s_lshl_b32 s80, s14, 10
D 5,40,64,3072
Mvaa 208,128,24
s_add_u32 m0, s61, 36864
s_add_u32 s83, s82, s80
Ta 4,11,16384
Mvaa 212,132,24
Ta 6,11,17408
Ra 60,26,14336
Maaa 176,136,24
Maaa 180,140,24
D 6,40,64,0
Maaa 240,144,24
Maaa 244,148,24
Maaa 112,152,24
Maaa 116,156,24
W 5
buffer_store_dwordx4 v[32:35], v1, s[8:11], s83 offen sc1
s_add_u32 s83, s83, 4096
Maaa 124,156,28
Ta 8,12,16384
Ta 10,12,17408
Maaa 120,152,28
Maaa 252,148,28
Maaa 248,144,28
D 7,40,64,1024
Maaa 188,140,28
Maaa 184,136,28
Mvaa 220,132,28
Mvaa 216,128,28
buffer_store_dwordx4 v[36:39], v1, s[8:11], s83 offen sc1
Mvaa 160,32,0
s_and_b32 s80, s80, 1024
s_add_u32 s83, s82, s80
s_add_u32 s83, s83, 8192
Mvaa 164,36,0
Ta 12,13,16384
Ta 14,13,17408
Mvaa 224,40,0
Mvaa 228,44,0
W 4
D 8,40,64,2048
Maaa 192,48,0
Maaa 196,52,0
Maaa 64,56,0
Maaa 68,60,0
buffer_store_dwordx4 v[40:43], v1, s[8:11], s83 offen sc1
Maaa 76,60,4
Ta 16,14,16384
Ta 18,14,17408
Maaa 72,56,4
Maaa 204,52,4
Maaa 200,48,4
D 9,40,64,3072
Mvaa 236,44,4
s_lshl_b32 s65, s64, 12
s_add_u32 m0, s62, 32768
Mvaa 232,40,4
Mvaa 172,36,4
Mvaa 168,32,4
W 4
D 16,44,65,0
Mvaa 176,32,8
Ta 20,15,16384
Ta 22,15,17408
Mvaa 180,36,8
Mvaa 240,40,8
Mvaa 244,44,8
D 17,44,65,1024
Maaa 208,48,8
Maaa 212,52,8
Maaa 80,56,8
Maaa 84,60,8
W 4
D 18,44,65,2048
Maaa 92,60,12
Ta 24,24,16384
Ta 26,24,17408
Maaa 88,56,12
Maaa 220,52,12
Maaa 216,48,12
D 19,44,65,3072
Mvaa 252,44,12
s_add_u32 m0, s62, 36864
Ra 128,27,0
Mvaa 248,40,12
Mvaa 188,36,12
Mvaa 184,32,12
W 5
D 20,44,65,0
Ra 132,27,2048
Mvaa 192,32,16
Ta 28,25,16384
Ta 30,25,17408
Mvaa 196,36,16
Maaa 160,40,16
Ra 136,27,4096
Maaa 164,44,16
D 21,44,65,1024
Maaa 224,48,16
Maaa 228,52,16
Ra 140,27,6144
Maaa 96,56,16
Maaa 100,60,16
W 8
D 22,44,65,2048
Maaa 108,60,20
Ta 0,10,24576
Ta 2,10,25600
Maaa 104,56,20
Ra 144,27,8192
Maaa 236,52,20
Maaa 232,48,20
D 23,44,65,3072
Ra 148,27,10240
Maaa 172,44,20
Maaa 168,40,20
Mvaa 204,36,20
Ra 152,27,12288
Mvaa 200,32,20
W 11
Mvaa 208,32,24
Ta 4,11,24576
Ta 6,11,25600
Mvaa 212,36,24
Ra 156,27,14336
Maaa 176,40,24
Maaa 180,44,24
Maaa 240,48,24
Maaa 244,52,24
Maaa 112,56,24
Maaa 116,60,24
W 10
Maaa 124,60,28
Ta 8,12,24576
Ta 10,12,25600
Maaa 120,56,28
Maaa 252,52,28
Maaa 248,48,28
Maaa 188,44,28
Maaa 184,40,28
Mvaa 220,36,28
Mvaa 216,32,28
W 8
Mvaa 160,128,0
Ta 12,13,24576
Ta 14,13,25600
Mvaa 164,132,0
Mvaa 224,136,0
Mvaa 228,140,0
W 4
Maaa 192,144,0
Maaa 196,148,0
Maaa 64,152,0
Maaa 68,156,0
Maaa 76,156,4
Ta 16,14,24576
Ta 18,14,25600
Maaa 72,152,4
Maaa 204,148,4
Maaa 200,144,4
Mvaa 236,140,4
Ta 20,15,24576
Ta 22,15,25600
Mvaa 232,136,4
Mvaa 172,132,4
Ta 24,24,24576
Ta 26,24,25600
Mvaa 168,128,4
W 8
Mvaa 176,128,8
Ta 28,25,24576
Ta 30,25,25600
Mvaa 180,132,8
Mvaa 240,136,8
Mvaa 244,140,8
Maaa 208,144,8
Maaa 212,148,8
Maaa 80,152,8
Maaa 84,156,8
W 8
Maaa 92,156,12
Maaa 88,152,12
Maaa 220,148,12
s_waitcnt vmcnt(0) lgkmcnt(0)
)"
R"(s_barrier
s_cmp_eq_u32 s14, 0
s_cbranch_scc0 .Lpubr_17%=
s_lshl_b32 s80, s15, 6
s_add_u32 s80, s80, 40960
s_mov_b64 exec, 0xff
buffer_store_dwordx2 v[84:85], v83, s[8:11], s80 offen sc1
s_mov_b64 exec, -1
.Lpubr_17%=:
Maaa 216,144,12
s_add_u32 s64, s64, 128
Mvaa 252,140,12
Ra 32,26,32768
Mvaa 248,136,12
s_and_b32 s64, s64, 1023
Mvaa 188,132,12
Mvaa 184,128,12
s_add_u32 m0, s61, 0
Ra 36,26,34816
Mvaa 192,128,16
Mvaa 196,132,16
D 2,40,64,0
Maaa 160,136,16
Ra 40,26,36864
Maaa 164,140,16
Maaa 224,144,16
Maaa 228,148,16
D 3,40,64,1024
Ra 44,26,38912
Maaa 96,152,16
Maaa 100,156,16
Maaa 108,156,20
Ta 0,10,49152
Ta 2,10,50176
Maaa 104,152,20
D 4,40,64,2048
Ra 48,26,40960
Maaa 236,148,20
Maaa 232,144,20
Ra 52,26,43008
Maaa 172,140,20
Maaa 168,136,20
D 5,40,64,3072
Mvaa 204,132,20
s_add_u32 m0, s61, 4096
Ra 56,26,45056
Mvaa 200,128,20
Mvaa 208,128,24
Ta 4,11,49152
Ta 6,11,50176
Mvaa 212,132,24
D 6,40,64,0
Ra 60,26,47104
Maaa 176,136,24
Maaa 180,140,24
Maaa 240,144,24
Maaa 244,148,24
D 7,40,64,1024
Maaa 112,152,24
Maaa 116,156,24
Maaa 124,156,28
Ta 8,12,49152
Ta 10,12,50176
Maaa 120,152,28
D 8,40,64,2048
Maaa 252,148,28
Maaa 248,144,28
Maaa 188,140,28
Maaa 184,136,28
D 9,40,64,3072
Mvaa 220,132,28
s_lshl_b32 s65, s64, 12
s_add_u32 m0, s62, 0
Mvaa 216,128,28
W 8
Mvaa 160,32,0
Ta 12,13,49152
Ta 14,13,50176
Mvaa 164,36,0
D 16,44,65,0
Mvaa 224,40,0
Mvaa 228,44,0
W 4
Maaa 192,48,0
Maaa 196,52,0
D 17,44,65,1024
Maaa 64,56,0
Maaa 68,60,0
Maaa 76,60,4
Ta 16,14,49152
Ta 18,14,50176
Maaa 72,56,4
D 18,44,65,2048
Maaa 204,52,4
Maaa 200,48,4
Mvaa 236,44,4
Mvaa 232,40,4
D 19,44,65,3072
Mvaa 172,36,4
s_add_u32 m0, s62, 4096
Mvaa 168,32,4
W 4
Mvaa 176,32,8
Ta 20,15,49152
Ta 22,15,50176
Mvaa 180,36,8
D 20,44,65,0
Mvaa 240,40,8
Mvaa 244,44,8
Maaa 208,48,8
Maaa 212,52,8
D 21,44,65,1024
Maaa 80,56,8
Maaa 84,60,8
W 4
Maaa 92,60,12
Ta 24,24,49152
Ta 26,24,50176
Maaa 88,56,12
D 22,44,65,2048
Maaa 220,52,12
Maaa 216,48,12
Mvaa 252,44,12
Ra 128,27,32768
Mvaa 248,40,12
D 23,44,65,3072
Mvaa 188,36,12
s_add_u32 s65, s5, 0
s_cmp_ge_u32 s65, 20
s_cselect_b32 s68, 20, 0
s_sub_u32 s65, s65, s68
Mvaa 184,32,12
W 5
s_lshl_b32 s65, s65, 18
s_add_u32 m0, s63, 131072
Mvaa 192,32,16
Ra 132,27,34816
Ta 28,25,49152
Mvaa 196,36,16
D 16,48,65,0
Ta 30,25,50176
Maaa 160,40,16
Ra 136,27,36864
Maaa 164,44,16
Maaa 224,48,16
Maaa 228,52,16
D 17,48,65,1024
Ra 140,27,38912
Maaa 96,56,16
Maaa 100,60,16
W 8
Maaa 108,60,20
Ta 0,10,57344
Ta 2,10,58368
Maaa 104,56,20
D 18,48,65,2048
Ra 144,27,40960
Maaa 236,52,20
Maaa 232,48,20
Ra 148,27,43008
Maaa 172,44,20
Maaa 168,40,20
D 19,48,65,3072
Mvaa 204,36,20
s_add_u32 m0, s63, 135168
Ra 152,27,45056
Mvaa 200,32,20
W 11
Mvaa 208,32,24
Ta 4,11,57344
Ta 6,11,58368
Mvaa 212,36,24
D 20,48,65,0
Ra 156,27,47104
Maaa 176,40,24
Maaa 180,44,24
Maaa 240,48,24
Maaa 244,52,24
D 21,48,65,1024
Maaa 112,56,24
Maaa 116,60,24
W 10
Maaa 124,60,28
Ta 8,12,57344
Ta 10,12,58368
Maaa 120,56,28
D 22,48,65,2048
Maaa 252,52,28
Maaa 248,48,28
Maaa 188,44,28
Maaa 184,40,28
D 23,48,65,3072
Mvaa 220,36,28
Mvaa 216,32,28
W 8
Mvaa 160,128,0
Ta 12,13,57344
Ta 14,13,58368
Mvaa 164,132,0
global_load_dword v142, v[140:141], off
v_lshl_add_u64 v[140:141], v[140:141], 0, s[68:69]
Mvaa 224,136,0
Mvaa 228,140,0
W 4
Maaa 192,144,0
Maaa 196,148,0
Maaa 64,152,0
Maaa 68,156,0
Maaa 76,156,4
Ta 16,14,57344
Ta 18,14,58368
Maaa 72,152,4
Maaa 204,148,4
Maaa 200,144,4
Mvaa 236,140,4
Ta 20,15,57344
Ta 22,15,58368
Mvaa 232,136,4
Mvaa 172,132,4
Ta 24,24,57344
Ta 26,24,58368
Mvaa 168,128,4
W 8
Mvaa 176,128,8
Ta 28,25,57344
Ta 30,25,58368
Mvaa 180,132,8
Mvaa 240,136,8
Mvaa 244,140,8
Maaa 208,144,8
Maaa 212,148,8
Maaa 80,152,8
Maaa 84,156,8
W 8
Maaa 92,156,12
Maaa 88,152,12
Maaa 220,148,12
s_waitcnt vmcnt(1) lgkmcnt(0)
s_barrier
s_add_u32 s64, s64, 128
Maaa 216,144,12
Mvaa 252,140,12
Ra 32,26,0
Mvaa 248,136,12
s_and_b32 s64, s64, 1023
Mvaa 188,132,12
Mvaa 184,128,12
Ra 36,26,2048
Mvaa 192,128,16
s_add_u32 m0, s61, 32768
Mvaa 196,132,16
Maaa 160,136,16
Ra 40,26,4096
Maaa 164,140,16
D 2,40,64,0
Maaa 224,144,16
Maaa 228,148,16
Ra 44,26,6144
Maaa 96,152,16
Maaa 100,156,16
D 3,40,64,1024
Maaa 108,156,20
Ta 0,10,16384
Ta 2,10,17408
Maaa 104,152,20
Ra 48,26,8192
Maaa 236,148,20
Maaa 232,144,20
D 4,40,64,2048
Ra 52,26,10240
Maaa 172,140,20
Maaa 168,136,20
Mvaa 204,132,20
Ra 56,26,12288
Mvaa 200,128,20
D 5,40,64,3072
Mvaa 208,128,24
s_add_u32 m0, s61, 36864
Ta 4,11,16384
Ta 6,11,17408
Mvaa 212,132,24
Ra 60,26,14336
Maaa 176,136,24
v_lshrrev_b32 v28, 1, v1
Maaa 180,140,24
s_lshl_b32 s80, s60, 9
D 6,40,64,0
Maaa 240,144,24
s_add_u32 s80, s80, 40960
Maaa 244,148,24
Maaa 112,152,24
Maaa 116,156,24
D 7,40,64,1024
Maaa 124,156,28
Ta 8,12,16384
Ta 10,12,17408
Maaa 120,152,28
Maaa 252,148,28
Maaa 248,144,28
buffer_load_dwordx2 v[28:29], v28, s[8:11], s80 offen sc1
Maaa 188,140,28
Maaa 184,136,28
Mvaa 220,132,28
Mvaa 216,128,28
W 8
D 8,40,64,2048
Mvaa 160,32,0
Ta 12,13,16384
Ta 14,13,17408
Mvaa 164,36,0
Mvaa 224,40,0
Mvaa 228,44,0
W 4
D 9,40,64,3072
Maaa 192,48,0
s_lshl_b32 s65, s64, 12
s_add_u32 m0, s62, 32768
Maaa 196,52,0
Maaa 64,56,0
Maaa 68,60,0
D 16,44,65,0
Maaa 76,60,4
Ta 16,14,16384
Ta 18,14,17408
Maaa 72,56,4
Maaa 204,52,4
Maaa 200,48,4
D 17,44,65,1024
Mvaa 236,44,4
Mvaa 232,40,4
Mvaa 172,36,4
Mvaa 168,32,4
W 4
D 18,44,65,2048
Mvaa 176,32,8
Ta 20,15,16384
Ta 22,15,17408
Mvaa 180,36,8
Mvaa 240,40,8
Mvaa 244,44,8
D 19,44,65,3072
Maaa 208,48,8
s_add_u32 m0, s62, 36864
Maaa 212,52,8
Maaa 80,56,8
Maaa 84,60,8
W 4
D 20,44,65,0
Maaa 92,60,12
Ta 24,24,16384
Ta 26,24,17408
Maaa 88,56,12
Maaa 220,52,12
Maaa 216,48,12
D 21,44,65,1024
Mvaa 252,44,12
Ra 128,27,0
Mvaa 248,40,12
Mvaa 188,36,12
Mvaa 184,32,12
W 5
D 22,44,65,2048
Ra 132,27,2048
Mvaa 192,32,16
Ta 28,25,16384
Ta 30,25,17408
Mvaa 196,36,16
Maaa 160,40,16
Ra 136,27,4096
Maaa 164,44,16
D 23,44,65,3072
v_lshrrev_b32 v138, 4, v1
Maaa 224,48,16
s_sub_u32 s81, s73, 1
v_min_u32 v138, s81, v138
v_lshrrev_b32 v139, 2, v138
Maaa 228,52,16
Ra 140,27,6144
v_mul_u32_u24 v139, s72, v139
Maaa 96,56,16
v_and_b32 v138, 3, v138
Maaa 100,60,16
W 8
v_lshl_add_u32 v138, v138, 7, v139
Maaa 108,60,20
s_waitcnt vmcnt(10)
v_cmp_eq_u32 vcc, s13, v29
s_cmp_eq_u64 vcc, -1
s_cbranch_scc0 .Lspin_18%=
.Lok_19%=:
v_add_u32 v138, s74, v138
Maaa 104,56,20
s_lshr_b32 s73, s73, 2
Ta 0,10,24576
Ta 2,10,25600
Maaa 236,52,20
s_mul_i32 s73, s73, s72
Ra 144,27,8192
v_bfe_u32 v144, v1, 8, 1
Maaa 232,48,20
)"
R"(v_min_u32 v139, s75, v138
v_mul_u32_u24 v144, 10240, v144
Maaa 172,44,20
Ra 148,27,10240
v_and_b32 v145, 0xf0, v1
Maaa 168,40,20
global_load_dword v142, v139, s[76:77]
v_lshl_add_u32 v144, v145, 3, v144
Mvaa 204,36,20
s_mul_i32 s69, s58, 20480
Ra 152,27,12288
v_add_u32 v138, s73, v138
Mvaa 200,32,20
W 11
s_add_u32 s69, s69, s28
Mvaa 208,32,24
Ta 4,11,24576
Ta 6,11,25600
Mvaa 212,36,24
buffer_load_dword v142, v144, s[8:11], s69 offen offset:0 sc1
Ra 156,27,14336
Maaa 176,40,24
Maaa 180,44,24
Maaa 240,48,24
Maaa 244,52,24
Maaa 112,56,24
Maaa 116,60,24
W 10
Maaa 124,60,28
Ta 8,12,24576
Ta 10,12,25600
Maaa 120,56,28
Maaa 252,52,28
Maaa 248,48,28
Maaa 188,44,28
Maaa 184,40,28
Mvaa 220,36,28
Mvaa 216,32,28
W 8
Mvaa 160,128,0
Ta 12,13,24576
Ta 14,13,25600
Mvaa 164,132,0
Mvaa 224,136,0
Mvaa 228,140,0
W 4
Maaa 192,144,0
Maaa 196,148,0
Maaa 64,152,0
Maaa 68,156,0
Maaa 76,156,4
Ta 16,14,24576
Ta 18,14,25600
Maaa 72,152,4
Maaa 204,148,4
Maaa 200,144,4
Mvaa 236,140,4
Ta 20,15,24576
Ta 22,15,25600
Mvaa 232,136,4
Mvaa 172,132,4
Ta 24,24,24576
Ta 26,24,25600
Mvaa 168,128,4
W 8
Mvaa 176,128,8
Ta 28,25,24576
Ta 30,25,25600
Mvaa 180,132,8
Mvaa 240,136,8
Mvaa 244,140,8
Maaa 208,144,8
Maaa 212,148,8
Maaa 80,152,8
Maaa 84,156,8
W 8
Maaa 92,156,12
Maaa 88,152,12
Maaa 220,148,12
s_waitcnt vmcnt(2) lgkmcnt(0)
s_barrier
s_add_u32 s64, s64, 128
Maaa 216,144,12
Mvaa 252,140,12
Ra 32,26,32768
Mvaa 248,136,12
s_and_b32 s64, s64, 1023
Mvaa 188,132,12
Mvaa 184,128,12
Ra 36,26,34816
Mvaa 192,128,16
s_add_u32 m0, s61, 0
Mvaa 196,132,16
Maaa 160,136,16
Ra 40,26,36864
Maaa 164,140,16
D 2,40,64,0
Maaa 224,144,16
Maaa 228,148,16
Ra 44,26,38912
Maaa 96,152,16
Maaa 100,156,16
D 3,40,64,1024
Maaa 108,156,20
Ta 0,10,49152
Ta 2,10,50176
Maaa 104,152,20
Ra 48,26,40960
Maaa 236,148,20
Maaa 232,144,20
D 4,40,64,2048
Ra 52,26,43008
Maaa 172,140,20
Maaa 168,136,20
Mvaa 204,132,20
Ra 56,26,45056
Mvaa 200,128,20
D 5,40,64,3072
Mvaa 208,128,24
s_add_u32 m0, s61, 4096
Ta 4,11,49152
Ta 6,11,50176
Mvaa 212,132,24
Ra 60,26,47104
Maaa 176,136,24
Maaa 180,140,24
D 6,40,64,0
Maaa 240,144,24
Maaa 244,148,24
Maaa 112,152,24
Maaa 116,156,24
D 7,40,64,1024
Maaa 124,156,28
Ta 8,12,49152
Ta 10,12,50176
Maaa 120,152,28
Maaa 252,148,28
Maaa 248,144,28
D 8,40,64,2048
Maaa 188,140,28
Maaa 184,136,28
Mvaa 220,132,28
Mvaa 216,128,28
W 8
D 9,40,64,3072
Mvaa 160,32,0
s_lshl_b32 s65, s64, 12
s_add_u32 m0, s62, 0
Ta 12,13,49152
Mvaa 164,36,0
Ta 14,13,50176
Mvaa 224,40,0
Mvaa 228,44,0
W 4
D 16,44,65,0
Maaa 192,48,0
Maaa 196,52,0
Maaa 64,56,0
Maaa 68,60,0
D 17,44,65,1024
Maaa 76,60,4
Ta 16,14,49152
Ta 18,14,50176
Maaa 72,56,4
Maaa 204,52,4
Maaa 200,48,4
D 18,44,65,2048
Mvaa 236,44,4
Mvaa 232,40,4
Mvaa 172,36,4
Mvaa 168,32,4
W 4
D 19,44,65,3072
Mvaa 176,32,8
s_add_u32 m0, s62, 4096
Ta 20,15,49152
Ta 22,15,50176
Mvaa 180,36,8
Mvaa 240,40,8
Mvaa 244,44,8
D 20,44,65,0
Maaa 208,48,8
Maaa 212,52,8
Maaa 80,56,8
Maaa 84,60,8
W 4
D 21,44,65,1024
Maaa 92,60,12
Ta 24,24,49152
Ta 26,24,50176
Maaa 88,56,12
Maaa 220,52,12
Maaa 216,48,12
D 22,44,65,2048
Mvaa 252,44,12
Ra 128,27,32768
Mvaa 248,40,12
Mvaa 188,36,12
Mvaa 184,32,12
W 5
D 23,44,65,3072
Ra 132,27,34816
Mvaa 192,32,16
Ta 28,25,49152
Ta 30,25,50176
Mvaa 196,36,16
v_min_u32 v139, s75, v138
Maaa 160,40,16
Ra 136,27,36864
Maaa 164,44,16
buffer_load_dword v142, v144, s[8:11], s69 offen offset:2048 sc1
Maaa 224,48,16
Maaa 228,52,16
s_add_u32 s69, s69, 4096
Ra 140,27,38912
Maaa 96,56,16
Maaa 100,60,16
W 8
global_load_dword v142, v139, s[76:77]
Maaa 108,60,20
Ta 0,10,57344
Ta 2,10,58368
Maaa 104,56,20
Ra 144,27,40960
Maaa 236,52,20
v_add_u32 v138, s73, v138
Maaa 232,48,20
Ra 148,27,43008
Maaa 172,44,20
Maaa 168,40,20
Mvaa 204,36,20
Ra 152,27,45056
Mvaa 200,32,20
W 11
Mvaa 208,32,24
Ta 4,11,57344
Ta 6,11,58368
Mvaa 212,36,24
Ra 156,27,47104
Maaa 176,40,24
Maaa 180,44,24
Maaa 240,48,24
Maaa 244,52,24
Maaa 112,56,24
Maaa 116,60,24
W 10
Maaa 124,60,28
Ta 8,12,57344
Ta 10,12,58368
Maaa 120,56,28
Maaa 252,52,28
Maaa 248,48,28
Maaa 188,44,28
Maaa 184,40,28
Mvaa 220,36,28
Mvaa 216,32,28
W 8
Mvaa 160,128,0
Ta 12,13,57344
Ta 14,13,58368
Mvaa 164,132,0
Mvaa 224,136,0
Mvaa 228,140,0
W 4
Maaa 192,144,0
Maaa 196,148,0
Maaa 64,152,0
Maaa 68,156,0
Maaa 76,156,4
Ta 16,14,57344
Ta 18,14,58368
Maaa 72,152,4
Maaa 204,148,4
Maaa 200,144,4
Mvaa 236,140,4
Ta 20,15,57344
Ta 22,15,58368
Mvaa 232,136,4
Mvaa 172,132,4
Ta 24,24,57344
Ta 26,24,58368
Mvaa 168,128,4
W 8
Mvaa 176,128,8
Ta 28,25,57344
Ta 30,25,58368
Mvaa 180,132,8
Mvaa 240,136,8
Mvaa 244,140,8
Maaa 208,144,8
Maaa 212,148,8
Maaa 80,152,8
Maaa 84,156,8
W 8
Maaa 92,156,12
Maaa 88,152,12
Maaa 220,148,12
s_waitcnt vmcnt(2) lgkmcnt(0)
s_barrier
s_cmp_eq_u32 s85, 43
s_cbranch_scc1 .Lrt_21%=
.Lrtr_22%=:
Maaa 216,144,12
s_add_u32 s64, s64, 128
Mvaa 252,140,12
Ra 32,26,0
Mvaa 248,136,12
s_and_b32 s64, s64, 1023
Mvaa 188,132,12
Mvaa 184,128,12
Ra 36,26,2048
Mvaa 192,128,16
s_add_u32 m0, s61, 32768
Mvaa 196,132,16
Maaa 160,136,16
Ra 40,26,4096
Maaa 164,140,16
D 2,40,64,0
Maaa 224,144,16
Maaa 228,148,16
Ra 44,26,6144
Maaa 96,152,16
Maaa 100,156,16
D 3,40,64,1024
Maaa 108,156,20
Ta 0,10,16384
Ta 2,10,17408
Maaa 104,152,20
Ra 48,26,8192
Maaa 236,148,20
Maaa 232,144,20
D 4,40,64,2048
Ra 52,26,10240
Maaa 172,140,20
Maaa 168,136,20
Mvaa 204,132,20
Ra 56,26,12288
Mvaa 200,128,20
D 5,40,64,3072
Mvaa 208,128,24
s_add_u32 m0, s61, 36864
Ta 4,11,16384
Ta 6,11,17408
Mvaa 212,132,24
Ra 60,26,14336
Maaa 176,136,24
Maaa 180,140,24
D 6,40,64,0
Maaa 240,144,24
Maaa 244,148,24
Maaa 112,152,24
Maaa 116,156,24
D 7,40,64,1024
Maaa 124,156,28
Ta 8,12,16384
Ta 10,12,17408
Maaa 120,152,28
Maaa 252,148,28
Maaa 248,144,28
D 8,40,64,2048
Maaa 188,140,28
Maaa 184,136,28
Mvaa 220,132,28
Mvaa 216,128,28
W 8
D 9,40,64,3072
Mvaa 160,32,0
s_lshl_b32 s65, s64, 12
s_add_u32 m0, s62, 32768
Ta 12,13,16384
Mvaa 164,36,0
Ta 14,13,17408
Mvaa 224,40,0
Mvaa 228,44,0
W 4
D 16,44,65,0
Maaa 192,48,0
Maaa 196,52,0
Maaa 64,56,0
Maaa 68,60,0
D 17,44,65,1024
Maaa 76,60,4
Ta 16,14,16384
Ta 18,14,17408
Maaa 72,56,4
Maaa 204,52,4
Maaa 200,48,4
D 18,44,65,2048
Mvaa 236,44,4
Mvaa 232,40,4
Mvaa 172,36,4
Mvaa 168,32,4
W 4
D 19,44,65,3072
Mvaa 176,32,8
s_add_u32 m0, s62, 36864
Ta 20,15,16384
Ta 22,15,17408
Mvaa 180,36,8
Mvaa 240,40,8
)"
R"(Mvaa 244,44,8
D 20,44,65,0
Maaa 208,48,8
Maaa 212,52,8
Maaa 80,56,8
Maaa 84,60,8
W 4
D 21,44,65,1024
Maaa 92,60,12
Ta 24,24,16384
Ta 26,24,17408
Maaa 88,56,12
Maaa 220,52,12
Maaa 216,48,12
D 22,44,65,2048
Mvaa 252,44,12
Ra 128,27,0
Mvaa 248,40,12
Mvaa 188,36,12
Mvaa 184,32,12
W 5
D 23,44,65,3072
v_add_u32 v2, 131072, v10
Mvaa 192,32,16
Ra 132,27,2048
Ta 28,25,16384
Mvaa 196,36,16
Ta 30,25,17408
v_add_u32 v3, 131072, v11
Maaa 160,40,16
Ra 136,27,4096
v_add_u32 v4, 131072, v12
Maaa 164,44,16
buffer_load_dword v142, v144, s[8:11], s69 offen offset:0 sc1
Maaa 224,48,16
v_add_u32 v5, 131072, v13
Maaa 228,52,16
Ra 140,27,6144
Maaa 96,56,16
v_add_u32 v6, 131072, v14
Maaa 100,60,16
W 8
Maaa 108,60,20
Ta 0,10,24576
Ta 2,10,25600
Maaa 104,56,20
Ra 144,27,8192
v_add_u32 v7, 131072, v15
Maaa 236,52,20
v_add_u32 v8, 131072, v24
Maaa 232,48,20
Ra 148,27,10240
Maaa 172,44,20
v_add_u32 v9, 131072, v25
Maaa 168,40,20
Mvaa 204,36,20
Ra 152,27,12288
Mvaa 200,32,20
W 11
Mvaa 208,32,24
Ta 4,11,24576
Ta 6,11,25600
Mvaa 212,36,24
Ra 156,27,14336
Maaa 176,40,24
Maaa 180,44,24
Maaa 240,48,24
Maaa 244,52,24
Maaa 112,56,24
Maaa 116,60,24
W 10
Maaa 124,60,28
Ta 8,12,24576
Ta 10,12,25600
Maaa 120,56,28
Maaa 252,52,28
Maaa 248,48,28
Maaa 188,44,28
Maaa 184,40,28
Mvaa 220,36,28
Mvaa 216,32,28
W 8
Mvaa 160,128,0
Ta 12,13,24576
Ta 14,13,25600
Mvaa 164,132,0
Mvaa 224,136,0
Mvaa 228,140,0
W 4
Maaa 192,144,0
Maaa 196,148,0
Maaa 64,152,0
Maaa 68,156,0
Maaa 76,156,4
Ta 16,14,24576
Ta 18,14,25600
Maaa 72,152,4
Maaa 204,148,4
Maaa 200,144,4
Mvaa 236,140,4
Ta 20,15,24576
Ta 22,15,25600
Mvaa 232,136,4
Mvaa 172,132,4
Ta 24,24,24576
Ta 26,24,25600
Mvaa 168,128,4
W 8
Mvaa 176,128,8
Ta 28,25,24576
Ta 30,25,25600
Mvaa 180,132,8
Mvaa 240,136,8
Mvaa 244,140,8
Maaa 208,144,8
Maaa 212,148,8
Maaa 80,152,8
Maaa 84,156,8
W 8
Maaa 92,156,12
Maaa 88,152,12
Maaa 220,148,12
s_waitcnt vmcnt(1) lgkmcnt(0)
s_barrier
s_cmp_eq_u32 s85, 43
s_cbranch_scc1 .Lrt_23%=
.Lrtr_24%=:
Maaa 216,144,12
s_add_u32 s65, s5, 4
Mvaa 252,140,12
Ra 32,26,32768
Mvaa 248,136,12
s_cmp_ge_u32 s65, 20
s_cselect_b32 s68, 20, 0
Mvaa 188,132,12
Mvaa 184,128,12
s_sub_u32 s65, s65, s68
Ra 36,26,34816
Mvaa 192,128,16
Mvaa 196,132,16
s_lshl_b32 s65, s65, 18
Maaa 160,136,16
Ra 40,26,36864
Maaa 164,140,16
Maaa 224,144,16
s_add_u32 m0, s63, 0
Maaa 228,148,16
Ra 44,26,38912
Maaa 96,152,16
D 16,48,65,0
Maaa 100,156,16
Maaa 108,156,20
Ta 0,10,49152
Ta 2,10,50176
Maaa 104,152,20
Ra 48,26,40960
Maaa 236,148,20
D 17,48,65,1024
Maaa 232,144,20
Ra 52,26,43008
Maaa 172,140,20
Maaa 168,136,20
Mvaa 204,132,20
D 18,48,65,2048
Ra 56,26,45056
Mvaa 200,128,20
Mvaa 208,128,24
Ta 4,11,49152
Ta 6,11,50176
Mvaa 212,132,24
Ra 60,26,47104
Maaa 176,136,24
D 19,48,65,3072
Maaa 180,140,24
s_add_u32 m0, s63, 4096
Maaa 240,144,24
Maaa 244,148,24
Maaa 112,152,24
D 20,48,65,0
Maaa 116,156,24
Maaa 124,156,28
Ta 8,12,49152
Ta 10,12,50176
Maaa 120,152,28
Maaa 252,148,28
D 21,48,65,1024
Maaa 248,144,28
Maaa 188,140,28
Maaa 184,136,28
Mvaa 220,132,28
D 22,48,65,2048
Mvaa 216,128,28
W 8
Mvaa 160,32,0
Ta 12,13,49152
Ta 14,13,50176
Mvaa 164,36,0
Mvaa 224,40,0
D 23,48,65,3072
Mvaa 228,44,0
W 4
s_add_u32 s65, s5, 8
s_cmp_ge_u32 s65, 20
s_cselect_b32 s68, 20, 0
Maaa 192,48,0
s_sub_u32 s65, s65, s68
s_lshl_b32 s65, s65, 18
s_add_u32 m0, s63, 65536
Maaa 196,52,0
Maaa 64,56,0
D 16,48,65,0
Maaa 68,60,0
Maaa 76,60,4
Ta 16,14,49152
Ta 18,14,50176
Maaa 72,56,4
Maaa 204,52,4
D 17,48,65,1024
Maaa 200,48,4
Mvaa 236,44,4
Mvaa 232,40,4
Mvaa 172,36,4
D 18,48,65,2048
Mvaa 168,32,4
W 4
Mvaa 176,32,8
Ta 20,15,49152
Ta 22,15,50176
Mvaa 180,36,8
Mvaa 240,40,8
D 19,48,65,3072
Mvaa 244,44,8
s_add_u32 m0, s63, 69632
Maaa 208,48,8
Maaa 212,52,8
Maaa 80,56,8
D 20,48,65,0
Maaa 84,60,8
W 4
Maaa 92,60,12
Ta 24,24,49152
Ta 26,24,50176
Maaa 88,56,12
Maaa 220,52,12
D 21,48,65,1024
Maaa 216,48,12
Mvaa 252,44,12
Ra 128,27,32768
Mvaa 248,40,12
Mvaa 188,36,12
D 22,48,65,2048
Mvaa 184,32,12
W 5
Ra 132,27,34816
Mvaa 192,32,16
Ta 28,25,49152
Ta 30,25,50176
Mvaa 196,36,16
Maaa 160,40,16
D 23,48,65,3072
v_min_u32 v139, s75, v138
Maaa 164,44,16
Ra 136,27,36864
Maaa 224,48,16
Maaa 228,52,16
Ra 140,27,38912
Maaa 96,56,16
buffer_load_dword v142, v144, s[8:11], s69 offen offset:2048 sc1
Maaa 100,60,16
W 8
s_add_u32 s69, s69, 4096
Maaa 108,60,20
Ta 0,10,57344
Ta 2,10,58368
Maaa 104,56,20
Ra 144,27,40960
Maaa 236,52,20
global_load_dword v142, v139, s[76:77]
v_add_u32 v138, s73, v138
Maaa 232,48,20
Ra 148,27,43008
Maaa 172,44,20
Maaa 168,40,20
v_bfe_u32 v29, v1, 7, 1
Mvaa 204,36,20
Ra 152,27,45056
Mvaa 200,32,20
W 11
v_mul_u32_u24 v28, 10240, v29
Mvaa 208,32,24
Ta 4,11,57344
Ta 6,11,58368
Mvaa 212,36,24
Ra 156,27,47104
v_lshrrev_b32 v29, 8, v1
Maaa 176,40,24
Maaa 180,44,24
v_lshl_add_u32 v28, v29, 7, v28
Maaa 240,48,24
v_and_b32 v29, 0x70, v1
Maaa 244,52,24
Maaa 112,56,24
v_add_u32 v28, v28, v29
Maaa 116,60,24
W 10
Maaa 124,60,28
s_and_b32 s80, s14, 2
Ta 8,12,57344
Ta 10,12,58368
Maaa 120,56,28
s_mul_i32 s80, s80, 20480
Maaa 252,52,28
Maaa 248,48,28
s_sub_u32 s81, 40960, s80
Maaa 188,44,28
Maaa 184,40,28
s_add_u32 s93, s28, 20480
Mvaa 220,36,28
Mvaa 216,32,28
W 8
v_add_u32 v26, s80, v28
Mvaa 160,128,0
Ta 12,13,57344
Ta 14,13,58368
Mvaa 164,132,0
Mvaa 224,136,0
v_add_u32 v27, s81, v28
Mvaa 228,140,0
W 4
Maaa 192,144,0
buffer_load_dwordx4 a[32:35], v26, s[8:11], s28 offen offset:0 sc1
Maaa 196,148,0
Maaa 64,152,0
Maaa 68,156,0
Maaa 76,156,4
buffer_load_dwordx4 a[36:39], v26, s[8:11], s93 offen offset:0 sc1
Ta 16,14,57344
Maaa 72,152,4
Ta 18,14,58368
Maaa 204,148,4
Maaa 200,144,4
Mvaa 236,140,4
buffer_load_dwordx4 a[40:43], v27, s[8:11], s28 offen offset:0 sc1
Ta 20,15,57344
Mvaa 232,136,4
Ta 22,15,58368
Mvaa 172,132,4
Ta 24,24,57344
Ta 26,24,58368
Mvaa 168,128,4
W 8
Mvaa 176,128,8
buffer_load_dwordx4 a[44:47], v27, s[8:11], s93 offen offset:0 sc1
Ta 28,25,57344
Mvaa 180,132,8
s_add_u32 s28, s28, 512
s_cmp_eq_u32 s28, s4
s_cselect_b32 s93, 10240, 0
Ta 30,25,58368
Mvaa 240,136,8
s_sub_u32 s28, s28, s93
Mvaa 244,140,8
s_add_u32 s93, s28, 20480
Maaa 208,144,8
Maaa 212,148,8
Maaa 80,152,8
buffer_load_dwordx4 a[48:51], v26, s[8:11], s28 offen offset:0 sc1
Maaa 84,156,8
W 8
Maaa 92,156,12
Maaa 88,152,12
Maaa 220,148,12
s_waitcnt vmcnt(7) lgkmcnt(0)
s_barrier
buffer_load_dwordx4 a[52:55], v26, s[8:11], s93 offen offset:0 sc1
Maaa 216,144,12
Mvaa 252,140,12
Mvaa 248,136,12
Mvaa 188,132,12
buffer_load_dwordx4 a[56:59], v27, s[8:11], s28 offen offset:0 sc1
Mvaa 184,128,12
Mvaa 192,128,16
Mvaa 196,132,16
Maaa 160,136,16
s_add_u32 s65, s5, 12
buffer_load_dwordx4 a[60:63], v27, s[8:11], s93 offen offset:0 sc1
Maaa 164,140,16
Maaa 224,144,16
Maaa 228,148,16
Maaa 96,152,16
Maaa 100,156,16
Maaa 108,156,20
Maaa 104,152,20
Maaa 236,148,20
s_cmp_ge_u32 s65, 20
s_cselect_b32 s68, 20, 0
Ta 0,2,0
Ta 2,2,1024
Maaa 232,144,20
Maaa 172,140,20
)"
R"(Maaa 168,136,20
Mvaa 204,132,20
Ta 4,3,0
Ta 6,3,1024
Mvaa 200,128,20
Mvaa 208,128,24
Mvaa 212,132,24
s_sub_u32 s65, s65, s68
Maaa 176,136,24
Ta 8,4,0
Ta 10,4,1024
Maaa 180,140,24
Maaa 240,144,24
Maaa 244,148,24
Maaa 112,152,24
Ta 12,5,0
Ta 14,5,1024
Maaa 116,156,24
Maaa 124,156,28
s_lshl_b32 s65, s65, 18
Maaa 120,152,28
Maaa 252,148,28
Ta 16,6,0
Ta 18,6,1024
Maaa 248,144,28
Maaa 188,140,28
Maaa 184,136,28
Mvaa 220,132,28
Mvaa 216,128,28
s_add_u32 m0, s63, 32768
.Lq_10%=:
s_cmp_ge_u32 s14, 2
s_cbranch_scc1 .Lpb_29%=
s_waitcnt vmcnt(4) lgkmcnt(6)
Zvaa 32,0,32
s_add_u32 s28, s28, 512
Ta 20,7,0
Ta 22,7,1024
Zvaa 64,0,36
s_cmp_eq_u32 s28, s4
s_cselect_b32 s93, 10240, 0
Ta 24,8,0
Ta 26,8,1024
Zvaa 96,0,40
s_sub_u32 s28, s28, s93
s_add_u32 s93, s28, 20480
buffer_load_dwordx4 a[128:131], v26, s[8:11], s28 offen offset:0 sc1
Zvaa 128,0,44
Ta 28,9,0
Ta 30,9,1024
Zvaa 132,4,44
Zvaa 100,4,40
Zvaa 68,4,36
buffer_load_dwordx4 a[132:135], v26, s[8:11], s93 offen offset:0 sc1
Ta 0,2,8192
Zvaa 36,4,32
W 9
Ta 2,2,9216
Zvaa 40,8,32
Zvaa 72,8,36
Zvaa 104,8,40
buffer_load_dwordx4 a[136:139], v27, s[8:11], s28 offen offset:0 sc1
Ta 4,3,8192
Zvaa 136,8,44
Ta 6,3,9216
K 160,160,161
Zvaa 140,12,44
K 161,162,163
Zvaa 108,12,40
Zvaa 76,12,36
buffer_load_dwordx4 a[140:143], v27, s[8:11], s93 offen offset:0 sc1
Ta 8,4,8192
Zvaa 44,12,32
W 9
s_add_u32 s28, s28, 512
s_cmp_eq_u32 s28, s4
s_cselect_b32 s93, 10240, 0
Zvaa 48,16,32
s_sub_u32 s28, s28, s93
s_add_u32 s93, s28, 20480
Ta 10,4,9216
Zvaa 80,16,36
K 162,164,165
K 163,166,167
Zvaa 112,16,40
s_add_u32 s36, s29, 0
buffer_load_dwordx4 a[144:147], v26, s[8:11], s28 offen offset:0 sc1
Ta 12,5,8192
Zvaa 144,16,44
Ta 14,5,9216
Zvaa 148,20,44
Zvaa 116,20,40
Zvaa 84,20,36
buffer_load_dwordx4 a[148:151], v26, s[8:11], s93 offen offset:0 sc1
Ta 16,6,8192
Zvaa 52,20,32
W 9
Ta 18,6,9216
Zvaa 56,24,32
Zvaa 88,24,36
Zvaa 120,24,40
buffer_load_dwordx4 a[152:155], v27, s[8:11], s28 offen offset:0 sc1
Ta 20,7,8192
Zvaa 152,24,44
Ta 22,7,9216
Zvaa 156,28,44
Zvaa 124,28,40
Zvaa 92,28,36
buffer_store_dwordx4 v[160:163], v1, s[8:11], s36 offen offset:0 sc1
K 168,168,169
Zvaa 60,28,32
s_waitcnt vmcnt(8) lgkmcnt(8)
Ta 24,8,8192
Ta 26,8,9216
Mvaa 32,0,48
K 169,170,171
Mvaa 64,0,52
Mvaa 96,0,56
buffer_load_dwordx4 a[156:159], v27, s[8:11], s93 offen offset:0 sc1
K 170,172,173
Mvaa 128,0,60
s_add_u32 s28, s28, 512
s_cmp_eq_u32 s28, s4
s_cselect_b32 s93, 10240, 0
s_sub_u32 s28, s28, s93
Mvaa 132,4,60
s_add_u32 s93, s28, 20480
Ta 28,9,8192
Ta 30,9,9216
Mvaa 100,4,56
K 171,174,175
Mvaa 68,4,52
D 16,48,65,0
Ta 0,2,16384
Mvaa 36,4,48
W 9
Ta 2,2,17408
Mvaa 40,8,48
Mvaa 72,8,52
Mvaa 104,8,56
D 17,48,65,1024
Ta 4,3,16384
Mvaa 136,8,60
Ta 6,3,17408
Mvaa 140,12,60
Mvaa 108,12,56
Mvaa 76,12,52
D 18,48,65,2048
Ta 8,4,16384
Mvaa 44,12,48
W 9
Ta 10,4,17408
Mvaa 48,16,48
Mvaa 80,16,52
buffer_store_dwordx4 v[168:171], v1, s[8:11], s36 offen offset:1024 sc1
K 176,176,177
Mvaa 112,16,56
Ta 12,5,16384
K 177,178,179
Mvaa 144,16,60
Ta 14,5,17408
Mvaa 148,20,60
K 178,180,181
Mvaa 116,20,56
D 19,48,65,3072
Mvaa 84,20,52
s_add_u32 m0, s63, 36864
Ta 16,6,16384
Ta 18,6,17408
Mvaa 52,20,48
W 10
K 179,182,183
Mvaa 56,24,48
Mvaa 88,24,52
buffer_load_dwordx4 a[32:35], v26, s[8:11], s28 offen offset:0 sc1
Mvaa 120,24,56
Ta 20,7,16384
Ta 22,7,17408
Mvaa 152,24,60
Mvaa 156,28,60
Mvaa 124,28,56
buffer_load_dwordx4 a[36:39], v26, s[8:11], s93 offen offset:0 sc1
Mvaa 92,28,52
Ta 24,8,16384
Ta 26,8,17408
Mvaa 60,28,48
s_waitcnt vmcnt(12) lgkmcnt(10)
Mvaa 32,0,128
Mvaa 64,0,132
buffer_load_dwordx4 a[40:43], v27, s[8:11], s28 offen offset:0 sc1
Mvaa 96,0,136
Ta 28,9,16384
Ta 30,9,17408
Mvaa 128,0,140
buffer_store_dwordx4 v[176:179], v1, s[8:11], s36 offen offset:2048 sc1
K 184,184,185
Mvaa 132,4,140
Mvaa 100,4,136
K 185,186,187
Mvaa 68,4,132
Ta 0,2,24576
Ta 2,2,25600
Mvaa 36,4,128
W 10
buffer_load_dwordx4 a[44:47], v27, s[8:11], s93 offen offset:0 sc1
K 186,188,189
Mvaa 40,8,128
s_add_u32 s28, s28, 512
s_cmp_eq_u32 s28, s4
s_cselect_b32 s93, 10240, 0
s_sub_u32 s28, s28, s93
Mvaa 72,8,132
s_add_u32 s93, s28, 20480
Mvaa 104,8,136
Ta 4,3,24576
K 187,190,191
Mvaa 136,8,140
D 20,48,65,0
Ta 6,3,25600
Mvaa 140,12,140
Mvaa 108,12,136
Mvaa 76,12,132
Ta 8,4,24576
Ta 10,4,25600
Mvaa 44,12,128
W 10
D 21,48,65,1024
Mvaa 48,16,128
Mvaa 80,16,132
Mvaa 112,16,136
Ta 12,5,24576
Ta 14,5,25600
Mvaa 144,16,140
D 22,48,65,2048
Mvaa 148,20,140
Mvaa 116,20,136
Mvaa 84,20,132
buffer_store_dwordx4 v[184:187], v1, s[8:11], s36 offen offset:3072 sc1
K 192,192,193
Mvaa 52,20,128
W 8
Ta 16,6,24576
Ta 18,6,25600
Mvaa 56,24,128
K 193,194,195
K 194,196,197
Mvaa 88,24,132
Mvaa 120,24,136
D 23,48,65,3072
Ta 20,7,24576
Mvaa 152,24,140
s_add_u32 s65, s5, 16
s_cmp_ge_u32 s65, 20
s_cselect_b32 s68, 20, 0
s_sub_u32 s65, s65, s68
Mvaa 156,28,140
s_lshl_b32 s65, s65, 18
Ta 22,7,25600
K 195,198,199
Mvaa 124,28,136
s_add_u32 s36, s29, 4096
Mvaa 92,28,132
buffer_load_dwordx4 a[48:51], v26, s[8:11], s28 offen offset:0 sc1
Ta 24,8,24576
Mvaa 60,28,128
s_waitcnt vmcnt(16) lgkmcnt(9)
Ta 26,8,25600
Mvaa 32,0,144
Mvaa 64,0,148
Mvaa 96,0,152
buffer_load_dwordx4 a[52:55], v26, s[8:11], s93 offen offset:0 sc1
Ta 28,9,24576
Mvaa 128,0,156
s_add_u32 m0, s63, 98304
Ta 30,9,25600
Mvaa 132,4,156
s_lshl_b32 s80, s56, 9
Mvaa 100,4,152
Mvaa 68,4,148
s_lshl_b32 s81, s57, 4
buffer_load_dwordx4 a[56:59], v27, s[8:11], s28 offen offset:0 sc1
Ta 0,10,0
Mvaa 36,4,144
W 9
s_add_u32 s80, s80, s81
Ta 2,10,1024
Mvaa 40,8,144
buffer_store_dwordx4 v[192:195], v1, s[8:11], s36 offen offset:0 sc1
K 200,200,201
Mvaa 72,8,148
s_and_b32 s81, s14, 1
Mvaa 104,8,152
s_lshl_b32 s81, s81, 3
Ta 4,11,0
K 201,202,203
Mvaa 136,8,156
Ta 6,11,1024
Mvaa 140,12,156
s_add_u32 s80, s80, s81
buffer_load_dwordx4 a[60:63], v27, s[8:11], s93 offen offset:0 sc1
K 202,204,205
Mvaa 108,12,152
s_add_u32 s28, s28, 512
s_cmp_eq_u32 s28, s4
s_cselect_b32 s93, 10240, 0
s_sub_u32 s28, s28, s93
Mvaa 76,12,148
s_add_u32 s93, s28, 20480
s_add_u32 s71, s80, 4096
Ta 8,12,0
Mvaa 44,12,144
W 9
Ta 10,12,1024
K 203,206,207
Mvaa 48,16,144
buffer_load_dwordx4 v[184:187], v26, s[8:11], s28 offen offset:0 sc1
Mvaa 80,16,148
Mvaa 112,16,152
Ta 12,13,0
Ta 14,13,1024
Mvaa 144,16,156
Mvaa 148,20,156
buffer_load_dwordx4 v[188:191], v26, s[8:11], s93 offen offset:0 sc1
Mvaa 116,20,152
Mvaa 84,20,148
Ta 16,14,0
Ta 18,14,1024
Mvaa 52,20,144
W 10
Mvaa 56,24,144
buffer_load_dwordx4 a[128:131], v27, s[8:11], s28 offen offset:0 sc1
Mvaa 88,24,148
Mvaa 120,24,152
Ta 20,15,0
Ta 22,15,1024
Mvaa 152,24,156
buffer_store_dwordx4 v[200:203], v1, s[8:11], s36 offen offset:1024 sc1
K 208,208,209
Mvaa 156,28,156
K 209,210,211
Mvaa 124,28,152
Mvaa 92,28,148
Ta 24,24,0
K 210,212,213
Mvaa 60,28,144
s_waitcnt vmcnt(14) lgkmcnt(9)
buffer_load_dwordx4 a[132:135], v27, s[8:11], s93 offen offset:0 sc1
Ta 26,24,1024
Mvaa 32,0,32
s_add_u32 s28, s28, 512
s_cmp_eq_u32 s28, s4
s_cselect_b32 s93, 10240, 0
s_sub_u32 s28, s28, s93
Mvaa 64,0,36
s_add_u32 s93, s28, 20480
K 211,214,215
Mvaa 96,0,40
Ta 28,25,0
Ta 30,25,1024
Mvaa 128,0,44
buffer_load_dwordx4 a[136:139], v26, s[8:11], s28 offen offset:0 sc1
Mvaa 132,4,44
Mvaa 100,4,40
Mvaa 68,4,36
Ta 0,10,8192
Ta 2,10,9216
Mvaa 36,4,32
W 10
buffer_load_dwordx4 a[140:143], v26, s[8:11], s93 offen offset:0 sc1
v_add_u32 v2, 65536, v10
Mvaa 40,8,32
Mvaa 72,8,36
Mvaa 104,8,40
Ta 4,11,8192
Ta 6,11,9216
Mvaa 136,8,44
buffer_load_dwordx4 v[192:195], v27, s[8:11], s28 offen offset:0 sc1
v_add_u32 v3, 65536, v11
Mvaa 140,12,44
Mvaa 108,12,40
buffer_store_dwordx4 v[208:211], v1, s[8:11], s36 offen offset:2048 sc1
K 216,216,217
Mvaa 76,12,36
Ta 8,12,8192
Ta 10,12,9216
Mvaa 44,12,32
W 10
v_add_u32 v4, 65536, v12
K 217,218,219
Mvaa 48,16,32
Mvaa 80,16,36
buffer_load_dwordx4 v[196:199], v27, s[8:11], s93 offen offset:0 sc1
K 218,220,221
Mvaa 112,16,40
s_add_u32 s28, s28, 512
s_cmp_eq_u32 s28, s4
s_cselect_b32 s93, 10240, 0
s_sub_u32 s28, s28, s93
Mvaa 144,16,44
s_add_u32 s93, s28, 20480
)"
R"(Ta 12,13,8192
Ta 14,13,9216
Mvaa 148,20,44
v_add_u32 v5, 65536, v13
K 219,222,223
Mvaa 116,20,40
D 16,48,65,0
Mvaa 84,20,36
Ta 16,14,8192
Ta 18,14,9216
Mvaa 52,20,32
W 10
v_add_u32 v6, 65536, v14
Mvaa 56,24,32
Mvaa 88,24,36
D 17,48,65,1024
Mvaa 120,24,40
Ta 20,15,8192
Ta 22,15,9216
Mvaa 152,24,44
v_add_u32 v7, 65536, v15
Mvaa 156,28,44
Mvaa 124,28,40
D 18,48,65,2048
Mvaa 92,28,36
Ta 24,24,8192
Ta 26,24,9216
Mvaa 60,28,32
s_waitcnt vmcnt(13) lgkmcnt(10)
v_add_u32 v8, 65536, v24
Mvaa 32,0,48
buffer_store_dwordx4 v[216:219], v1, s[8:11], s36 offen offset:3072 sc1
K 224,224,225
Mvaa 64,0,52
K 225,226,227
Mvaa 96,0,56
Ta 28,25,8192
Ta 30,25,9216
Mvaa 128,0,60
v_add_u32 v9, 65536, v25
K 226,228,229
Mvaa 132,4,60
buffer_load_dwordx4 v[200:203], v26, s[8:11], s28 offen offset:0 sc1
K 227,230,231
Mvaa 100,4,56
Mvaa 68,4,52
s_add_u32 s36, s29, 458752
Ta 0,10,16384
Ta 2,10,17408
Mvaa 36,4,48
W 10
Mvaa 40,8,48
buffer_load_dwordx4 v[204:207], v26, s[8:11], s93 offen offset:0 sc1
Mvaa 72,8,52
Mvaa 104,8,56
Ta 4,11,16384
Ta 6,11,17408
Mvaa 136,8,60
Mvaa 140,12,60
buffer_load_dwordx4 a[144:147], v27, s[8:11], s28 offen offset:0 sc1
Mvaa 108,12,56
Mvaa 76,12,52
Ta 8,12,16384
Ta 10,12,17408
Mvaa 44,12,48
W 10
Mvaa 48,16,48
buffer_load_dwordx4 a[148:151], v27, s[8:11], s93 offen offset:0 sc1
Mvaa 80,16,52
s_add_u32 s28, s28, 512
s_cmp_eq_u32 s28, s4
s_cselect_b32 s93, 10240, 0
s_sub_u32 s28, s28, s93
Mvaa 112,16,56
s_add_u32 s93, s28, 20480
buffer_store_dwordx4 v[224:227], v1, s[8:11], s36 offen offset:0 sc1
K 232,232,233
Mvaa 144,16,60
Ta 12,13,16384
Ta 14,13,17408
Mvaa 148,20,60
K 233,234,235
Mvaa 116,20,56
Mvaa 84,20,52
D 19,48,65,3072
K 234,236,237
Mvaa 52,20,48
W 8
s_add_u32 m0, s63, 102400
Ta 16,14,16384
Mvaa 56,24,48
Ta 18,14,17408
Mvaa 88,24,52
K 235,238,239
Mvaa 120,24,56
buffer_load_dwordx4 a[152:155], v26, s[8:11], s28 offen offset:0 sc1
Ta 20,15,16384
Mvaa 152,24,60
Ta 22,15,17408
Mvaa 156,28,60
Mvaa 124,28,56
Mvaa 92,28,52
buffer_load_dwordx4 a[156:159], v26, s[8:11], s93 offen offset:0 sc1
Ta 24,24,16384
Mvaa 60,28,48
s_waitcnt vmcnt(17) lgkmcnt(9)
Ta 26,24,17408
Mvav 32,0,184
Mvav 64,0,188
Mvaa 96,0,128
D 20,48,65,0
Ta 28,25,16384
Mvaa 128,0,132
Ta 30,25,17408
Mvaa 132,4,132
Mvaa 100,4,128
buffer_store_dwordx4 v[232:235], v1, s[8:11], s36 offen offset:1024 sc1
K 240,240,241
Mvav 68,4,188
Ta 0,10,24576
K 241,242,243
Mvav 36,4,184
W 9
Ta 2,10,25600
Mvav 40,8,184
K 242,244,245
Mvav 72,8,188
buffer_load_dwordx4 v[208:211], v27, s[8:11], s28 offen offset:0 sc1
Mvaa 104,8,128
Ta 4,11,24576
Ta 6,11,25600
Mvaa 136,8,132
K 243,246,247
Mvaa 140,12,132
Mvaa 108,12,128
D 21,48,65,1024
Mvav 76,12,188
Ta 8,12,24576
Ta 10,12,25600
Mvav 44,12,184
W 10
Mvav 48,16,184
Mvav 80,16,188
buffer_load_dwordx4 v[212:215], v27, s[8:11], s93 offen offset:0 sc1
Mvaa 112,16,128
s_add_u32 s28, s28, 512
s_cmp_eq_u32 s28, s4
s_cselect_b32 s93, 10240, 0
s_sub_u32 s28, s28, s93
Mvaa 144,16,132
s_add_u32 s93, s28, 20480
Ta 12,13,24576
Ta 14,13,25600
Mvaa 148,20,132
Mvaa 116,20,128
D 22,48,65,2048
Mvav 84,20,188
Ta 16,14,24576
Ta 18,14,25600
Mvav 52,20,184
buffer_store_dwordx4 v[240:243], v1, s[8:11], s36 offen offset:2048 sc1
W 10
Mvav 56,24,184
K 248,248,249
Mvav 88,24,188
K 249,250,251
Mvaa 120,24,128
Ta 20,15,24576
Ta 22,15,25600
Mvaa 152,24,132
buffer_load_dwordx4 a[32:35], v26, s[8:11], s28 offen offset:0 sc1
K 250,252,253
Mvaa 156,28,132
Mvaa 124,28,128
Mvav 92,28,188
Ta 24,24,24576
K 251,254,255
Mvav 60,28,184
s_waitcnt vmcnt(20) lgkmcnt(9)
D 23,48,65,3072
Ta 26,24,25600
Mvaa 32,0,136
Mvaa 64,0,140
Mvav 96,0,192
Ta 28,25,24576
Ta 30,25,25600
Mvav 128,0,196
buffer_load_dwordx4 a[36:39], v26, s[8:11], s93 offen offset:0 sc1
Mvav 132,4,196
Mvav 100,4,192
Mvaa 68,4,140
Ta 0,2,0
Ta 2,2,1024
Mvaa 36,4,136
W 10
buffer_load_dwordx4 a[40:43], v27, s[8:11], s28 offen offset:0 sc1
Mvaa 40,8,136
Mvaa 72,8,140
Mvav 104,8,192
buffer_store_dwordx4 v[248:251], v1, s[8:11], s36 offen offset:3072 sc1
G 160,160
Mvav 136,8,196
G 161,161
G 162,162
Mvav 140,12,196
Ta 4,3,0
Ta 6,3,1024
Mvav 108,12,192
G 163,163
G 164,164
Mvaa 76,12,140
buffer_load_dwordx4 a[44:47], v27, s[8:11], s93 offen offset:0 sc1
G 165,165
Mvaa 44,12,136
W 8
s_add_u32 s28, s28, 512
s_cmp_eq_u32 s28, s4
s_cselect_b32 s93, 10240, 0
Mvaa 48,16,136
s_sub_u32 s28, s28, s93
s_add_u32 s93, s28, 20480
G 166,166
Mvaa 80,16,140
Ta 8,4,0
Ta 10,4,1024
Mvav 112,16,192
buffer_load_dwordx4 v[216:219], v26, s[8:11], s28 offen offset:0 sc1
G 167,167
Mvav 144,16,196
K 160,160,161
K 161,162,163
Mvav 148,20,196
K 162,164,165
K 163,166,167
Mvav 116,20,192
s_add_u32 s36, s29, 462848
Ta 12,5,0
Ta 14,5,1024
Mvaa 84,20,140
buffer_load_dwordx4 v[220:223], v26, s[8:11], s93 offen offset:0 sc1
Ta 16,6,0
Mvaa 52,20,136
W 9
Ta 18,6,1024
Mvaa 56,24,136
Mvaa 88,24,140
Mvav 120,24,192
buffer_load_dwordx4 v[224:227], v27, s[8:11], s28 offen offset:0 sc1
Ta 20,7,0
Mvav 152,24,196
Ta 22,7,1024
Mvav 156,28,196
buffer_store_dwordx4 v[160:163], v1, s[8:11], s36 offen offset:0 sc1
G 168,168
Mvav 124,28,192
G 169,169
G 170,170
Mvaa 92,28,140
Ta 24,8,0
G 171,171
Mvaa 60,28,136
s_waitcnt vmcnt(21) lgkmcnt(9)
Ta 26,8,1024
G 172,172
Mvav 32,0,200
buffer_load_dwordx4 v[228:231], v27, s[8:11], s93 offen offset:0 sc1
G 173,173
Mvav 64,0,204
s_add_u32 s28, s28, 512
s_cmp_eq_u32 s28, s4
s_cselect_b32 s93, 10240, 0
s_sub_u32 s28, s28, s93
Mvaa 96,0,144
s_add_u32 s93, s28, 20480
G 174,174
G 175,175
Mvaa 128,0,148
Ta 28,9,0
Ta 30,9,1024
Mvaa 132,4,148
buffer_load_dwordx4 a[48:51], v26, s[8:11], s28 offen offset:0 sc1
K 168,168,169
Mvaa 100,4,144
K 169,170,171
K 170,172,173
Mvav 68,4,204
Ta 0,2,8192
K 171,174,175
Mvav 36,4,200
W 9
Ta 2,2,9216
Mvav 40,8,200
buffer_load_dwordx4 a[52:55], v26, s[8:11], s93 offen offset:0 sc1
Mvav 72,8,204
Mvaa 104,8,144
Ta 4,3,8192
Ta 6,3,9216
Mvaa 136,8,148
Mvaa 140,12,148
buffer_load_dwordx4 a[56:59], v27, s[8:11], s28 offen offset:0 sc1
Mvaa 108,12,144
Mvav 76,12,204
Ta 8,4,8192
Ta 10,4,9216
Mvav 44,12,200
buffer_store_dwordx4 v[168:171], v1, s[8:11], s36 offen offset:1024 sc1
W 10
Mvav 48,16,200
G 176,176
G 177,177
Mvav 80,16,204
G 178,178
G 179,179
Mvaa 112,16,144
G 180,180
G 181,181
Mvaa 144,16,148
buffer_load_dwordx4 a[60:63], v27, s[8:11], s93 offen offset:0 sc1
Ta 12,5,8192
Mvaa 148,20,148
s_add_u32 s28, s28, 512
s_cmp_eq_u32 s28, s4
s_cselect_b32 s93, 10240, 0
s_sub_u32 s28, s28, s93
Mvaa 116,20,144
s_add_u32 s93, s28, 20480
Ta 14,5,9216
v_lshrrev_b32 v20, 4, v1
Mvav 84,20,204
G 182,182
G 183,183
Mvav 52,20,200
W 8
buffer_load_dwordx4 v[232:235], v26, s[8:11], s28 offen offset:0 sc1
v_add_u32 v20, 0, v20
Mvav 56,24,200
K 176,176,177
K 177,178,179
Mvav 88,24,204
Ta 16,6,8192
v_mul_u32_u24 v22, 205, v20
Mvaa 120,24,144
Ta 18,6,9216
K 178,180,181
Mvaa 152,24,148
buffer_load_dwordx4 v[236:239], v26, s[8:11], s93 offen offset:0 sc1
K 179,182,183
Mvaa 156,28,148
v_lshrrev_b32 v22, 12, v22
v_mul_u32_u24 v23, 20, v22
Mvaa 124,28,144
Ta 20,7,8192
Ta 22,7,9216
Mvav 92,28,204
v_sub_u32 v20, v20, v23
v_lshrrev_b32 v23, 1, v22
Mvav 60,28,200
s_waitcnt vmcnt(20) lgkmcnt(8)
buffer_load_dwordx4 v[184:187], v27, s[8:11], s28 offen offset:0 sc1
Ta 24,8,8192
Mvaa 32,0,152
Ta 26,8,9216
v_and_b32 v23, 2, v23
Mvaa 64,0,156
buffer_store_dwordx4 v[176:179], v1, s[8:11], s36 offen offset:2048 sc1
G 160,184
Mvav 96,0,208
G 161,185
v_xor_b32 v20, v20, v23
Mvav 128,0,212
Ta 28,9,8192
G 162,186
Mvav 132,4,212
Ta 30,9,9216
G 163,187
Mvav 100,4,208
buffer_load_dwordx4 v[188:191], v27, s[8:11], s93 offen offset:0 sc1
v_mul_u32_u24 v22, 80, v22
Mvaa 68,4,156
s_add_u32 s28, s28, 512
s_cmp_eq_u32 s28, s4
s_cselect_b32 s93, 10240, 0
s_sub_u32 s28, s28, s93
Mvaa 36,4,152
W 8
s_add_u32 s93, s28, 20480
G 164,188
Mvaa 40,8,152
G 165,189
v_add_lshl_u32 v16, v20, v22, 4
Mvaa 72,8,156
buffer_load_dwordx4 a[128:131], v26, s[8:11], s28 offen offset:0 sc1
G 166,190
Mvav 104,8,208
Ta 0,2,16384
Ta 2,2,17408
Mvav 136,8,212
G 167,191
v_lshrrev_b32 v20, 4, v1
Mvav 140,12,212
)"
R"(K 160,160,161
K 161,162,163
Mvav 108,12,208
buffer_load_dwordx4 a[132:135], v26, s[8:11], s93 offen offset:0 sc1
v_add_u32 v20, 64, v20
Mvaa 76,12,156
Ta 4,3,16384
K 162,164,165
Mvaa 44,12,152
W 7
Ta 6,3,17408
K 163,166,167
Mvaa 48,16,152
v_mul_u32_u24 v22, 205, v20
v_lshrrev_b32 v22, 12, v22
Mvaa 80,16,156
buffer_store_dwordx4 v[160:163], v1, s[8:11], s36 offen offset:3072 sc1
Ta 8,4,16384
Mvav 112,16,208
Ta 10,4,17408
v_mul_u32_u24 v23, 20, v22
Mvav 144,16,212
Ta 12,5,16384
v_sub_u32 v20, v20, v23
Mvav 148,20,212
Ta 14,5,17408
G 168,192
Mvav 116,20,208
buffer_load_dwordx4 a[136:139], v27, s[8:11], s28 offen offset:0 sc1
v_lshrrev_b32 v23, 1, v22
Mvaa 84,20,156
G 169,193
G 170,194
Mvaa 52,20,152
W 8
v_and_b32 v23, 2, v23
G 171,195
Mvaa 56,24,152
G 172,196
v_xor_b32 v20, v20, v23
Mvaa 88,24,156
Ta 16,6,16384
Ta 18,6,17408
Mvav 120,24,208
G 173,197
G 174,198
Mvav 152,24,212
buffer_load_dwordx4 a[140:143], v27, s[8:11], s93 offen offset:0 sc1
v_mul_u32_u24 v22, 80, v22
Mvav 156,28,212
G 175,199
K 168,168,169
Mvav 124,28,208
v_add_lshl_u32 v17, v20, v22, 4
K 169,170,171
Mvaa 92,28,156
Ta 20,7,16384
Ta 22,7,17408
Mvaa 60,28,152
s_waitcnt vmcnt(20) lgkmcnt(8)
K 170,172,173
K 171,174,175
Mvaa 32,0,32
s_add_u32 s36, s29, 65536
buffer_store_dwordx4 v[168:171], v1, s[8:11], s36 offen offset:0 sc1
v_subrev_u32 v17, 1024, v17
Mvaa 64,0,36
s_add_u32 s28, s28, 512
Ta 24,8,16384
v_lshrrev_b32 v20, 4, v1
Mvaa 96,0,40
s_cmp_eq_u32 s28, s4
s_cselect_b32 s93, 10240, 0
Ta 26,8,17408
v_add_u32 v20, 128, v20
Mvaa 128,0,44
s_sub_u32 s28, s28, s93
s_add_u32 s93, s28, 20480
v_mul_u32_u24 v22, 205, v20
Mvaa 132,4,44
buffer_load_dwordx4 v[192:195], v26, s[8:11], s28 offen offset:0 sc1
Ta 28,9,16384
Mvaa 100,4,40
Ta 30,9,17408
v_lshrrev_b32 v22, 12, v22
Mvaa 68,4,36
G 176,200
v_mul_u32_u24 v23, 20, v22
Mvaa 36,4,32
W 8
Ta 0,2,24576
G 177,201
Mvaa 40,8,32
Ta 2,2,25600
G 178,202
Mvaa 72,8,36
v_sub_u32 v20, v20, v23
G 179,203
Mvaa 104,8,40
buffer_load_dwordx4 v[196:199], v26, s[8:11], s93 offen offset:0 sc1
G 180,204
Mvaa 136,8,44
v_lshrrev_b32 v23, 1, v22
G 181,205
Mvaa 140,12,44
Ta 4,3,24576
Ta 6,3,25600
Mvaa 108,12,40
G 182,206
v_and_b32 v23, 2, v23
Mvaa 76,12,36
G 183,207
K 176,176,177
Mvaa 44,12,32
W 8
v_xor_b32 v20, v20, v23
K 177,178,179
Mvaa 48,16,32
Ta 8,4,24576
Ta 10,4,25600
Mvaa 80,16,36
K 178,180,181
v_mul_u32_u24 v22, 80, v22
Mvaa 112,16,40
buffer_load_dwordx4 v[200:203], v27, s[8:11], s28 offen offset:0 sc1
K 179,182,183
Mvaa 144,16,44
Ta 12,5,24576
v_add_lshl_u32 v18, v20, v22, 4
Mvaa 148,20,44
Ta 14,5,25600
v_subrev_u32 v18, 2048, v18
Mvaa 116,20,40
v_lshrrev_b32 v20, 4, v1
Mvaa 84,20,36
buffer_store_dwordx4 v[176:179], v1, s[8:11], s36 offen offset:1024 sc1
Ta 16,6,24576
Mvaa 52,20,32
W 9
Ta 18,6,25600
v_add_u32 v20, 192, v20
Mvaa 56,24,32
v_mul_u32_u24 v22, 205, v20
G 160,208
Mvaa 88,24,36
G 161,209
Mvaa 120,24,40
buffer_load_dwordx4 v[204:207], v27, s[8:11], s93 offen offset:0 sc1
v_lshrrev_b32 v22, 12, v22
Mvaa 152,24,44
Ta 20,7,24576
G 162,210
Mvaa 156,28,44
Ta 22,7,25600
G 163,211
Mvaa 124,28,40
G 164,212
v_mul_u32_u24 v23, 20, v22
Mvaa 92,28,36
G 165,213
G 166,214
Mvaa 60,28,32
s_waitcnt vmcnt(21) lgkmcnt(8)
Ta 24,8,24576
v_sub_u32 v20, v20, v23
Mvav 32,0,216
s_add_u32 s28, s28, 512
Ta 26,8,25600
G 167,215
Mvav 64,0,220
s_cmp_eq_u32 s28, s4
s_cselect_b32 s93, 10240, 0
K 160,160,161
v_lshrrev_b32 v23, 1, v22
Mvav 96,0,224
s_sub_u32 s28, s28, s93
s_add_u32 s93, s28, 20480
buffer_load_dwordx4 a[144:147], v26, s[8:11], s28 offen offset:0 sc1
Mvav 128,0,228
K 161,162,163
K 162,164,165
Mvav 132,4,228
Ta 28,9,24576
v_and_b32 v23, 2, v23
Mvav 100,4,224
Ta 30,9,25600
K 163,166,167
Mvav 68,4,220
buffer_store_dwordx4 v[160:163], v1, s[8:11], s36 offen offset:2048 sc1
v_xor_b32 v20, v20, v23
Mvav 36,4,216
W 8
v_mul_u32_u24 v22, 80, v22
Mvav 40,8,216
v_add_lshl_u32 v19, v20, v22, 4
Mvav 72,8,220
v_subrev_u32 v19, 3072, v19
Mvav 104,8,224
buffer_load_dwordx4 a[148:151], v26, s[8:11], s93 offen offset:0 sc1
Mvav 136,8,228
v_lshrrev_b32 v20, 4, v1
G 168,216
Mvav 140,12,228
W 0
s_barrier
G 169,217
Mvav 108,12,224
Ta 0,10,32768
Ta 2,10,33792
Mvav 76,12,220
Ta 4,11,32768
Ta 6,11,33792
Mvav 44,12,216
v_add_u32 v20, 256, v20
G 170,218
Mvav 48,16,216
Ta 8,12,32768
G 171,219
Mvav 80,16,220
Ta 10,12,33792
v_mul_u32_u24 v22, 205, v20
Mvav 112,16,224
G 172,220
G 173,221
Mvav 144,16,228
buffer_load_dwordx4 a[184:187], v27, s[8:11], s28 offen offset:0 sc1
v_lshrrev_b32 v22, 12, v22
Mvav 148,20,228
G 174,222
G 175,223
Mvav 116,20,224
Ta 12,13,32768
Ta 14,13,33792
Mvav 84,20,220
K 168,168,169
v_mul_u32_u24 v23, 20, v22
Mvav 52,20,216
K 169,170,171
K 170,172,173
Mvav 56,24,216
v_sub_u32 v20, v20, v23
K 171,174,175
Mvav 88,24,220
Ta 16,14,32768
Ta 18,14,33792
Mvav 120,24,224
buffer_store_dwordx4 v[168:171], v1, s[8:11], s36 offen offset:3072 sc1
v_lshrrev_b32 v23, 1, v22
Mvav 152,24,228
v_and_b32 v23, 2, v23
v_xor_b32 v20, v20, v23
Mvav 156,28,228
Ta 20,15,32768
Ta 22,15,33792
Mvav 124,28,224
v_mul_u32_u24 v22, 80, v22
G 176,224
Mvav 92,28,220
buffer_load_dwordx4 a[188:191], v27, s[8:11], s93 offen offset:0 sc1
v_add_lshl_u32 v20, v20, v22, 4
Mvav 60,28,216
s_waitcnt vmcnt(14)
v_mov_b64 v[28:29], s[12:13]
s_mov_b64 exec, 1
buffer_store_dwordx2 v[28:29], off, s[8:11], s71 sc1
s_mov_b64 exec, -1
G 177,225
W 8
Mvaa 32,0,48
s_add_u32 s28, s28, 512
Ta 24,24,32768
Ta 26,24,33792
Mvaa 64,0,52
s_cmp_eq_u32 s28, s4
s_cselect_b32 s93, 10240, 0
G 178,226
G 179,227
Mvaa 96,0,56
s_sub_u32 s28, s28, s93
s_add_u32 s93, s28, 20480
v_lshrrev_b32 v22, 6, v1
Mvaa 128,0,60
buffer_load_dwordx4 a[152:155], v26, s[8:11], s28 offen offset:0 sc1
G 180,228
Mvaa 132,4,60
G 181,229
v_mul_u32_u24 v22, 320, v22
Mvaa 100,4,56
Ta 28,25,32768
Ta 30,25,33792
Mvaa 68,4,52
G 182,230
G 183,231
Mvaa 36,4,48
W 8
v_bfe_u32 v23, v1, 4, 2
K 176,176,177
Mvaa 40,8,48
K 177,178,179
v_lshl_add_u32 v22, v23, 3, v22
Mvaa 72,8,52
Ta 0,10,40960
K 178,180,181
Mvaa 104,8,56
Ta 2,10,41984
K 179,182,183
Mvaa 136,8,60
s_add_u32 s36, s29, 69632
buffer_load_dwordx4 a[156:159], v26, s[8:11], s93 offen offset:0 sc1
v_bfe_u32 v23, v1, 8, 1
Mvaa 140,12,60
Ta 4,11,40960
v_lshlrev_b32 v23, 5, v23
Mvaa 108,12,56
Ta 6,11,41984
v_add_u32 v21, v22, v23
Mvaa 76,12,52
Ta 8,12,40960
v_sub_u32 v22, v22, v23
Mvaa 44,12,48
W 9
buffer_store_dwordx4 v[176:179], v1, s[8:11], s36 offen offset:0 sc1
Ta 10,12,41984
Mvaa 48,16,48
s_mul_i32 s94, s59, 655360
G 160,232
Mvaa 80,16,52
s_mul_i32 s95, s58, 320
G 161,233
Mvaa 112,16,56
Ta 12,13,40960
G 162,234
Mvaa 144,16,60
s_add_u32 s94, s94, s95
buffer_load_dwordx4 v[208:211], v27, s[8:11], s28 offen offset:0 sc1
Ta 14,13,41984
Mvaa 148,20,60
G 163,235
G 164,236
Mvaa 116,20,56
s_add_u32 s40, s26, s94
s_addc_u32 s41, s27, 0
G 165,237
Mvaa 84,20,52
Ta 16,14,40960
G 166,238
Mvaa 52,20,48
W 9
Ta 18,14,41984
G 167,239
Mvaa 56,24,48
buffer_load_dwordx4 v[212:215], v27, s[8:11], s93 offen offset:0 sc1
K 160,160,161
Mvaa 88,24,52
s_and_b32 s41, s41, 0xffff
K 161,162,163
Mvaa 120,24,56
Ta 20,15,40960
K 162,164,165
Mvaa 152,24,60
s_mov_b32 s42, 5242880
Ta 22,15,41984
K 163,166,167
Mvaa 156,28,60
s_mov_b32 s43, 0x20000
buffer_store_dwordx4 v[160:163], v1, s[8:11], s36 offen offset:1024 sc1
Mvaa 124,28,56
Mvaa 92,28,52
s_sub_u32 s42, s42, s94
Ta 24,24,40960
Ta 26,24,41984
Mvaa 60,28,48
W 10
Mvav 32,0,232
s_mov_b64 s[44:45], s[8:9]
s_add_u32 s28, s28, 512
s_cmp_eq_u32 s28, s4
s_cselect_b32 s93, 10240, 0
Mvav 64,0,236
s_sub_u32 s28, s28, s93
s_add_u32 s93, s28, 20480
buffer_load_dwordx4 a[192:195], v26, s[8:11], s28 offen offset:0 sc1
Mvav 96,0,184
s_mov_b32 s47, 0x20000
Ta 28,25,40960
Ta 30,25,41984
Mvav 128,0,188
G 168,240
Mvav 132,4,188
s_and_b32 s48, s14, 1
G 169,241
Mvav 100,4,184
G 170,242
Mvav 68,4,236
s_mul_i32 s48, s48, 5120
Ta 0,10,49152
G 171,243
Mvav 36,4,232
W 9
Ta 2,10,50176
)"
R"(G 172,244
Mvav 40,8,232
s_lshr_b32 s51, s14, 1
buffer_load_dwordx4 a[196:199], v26, s[8:11], s93 offen offset:0 sc1
G 173,245
Mvav 72,8,236
G 174,246
Mvav 104,8,184
s_and_b32 s94, s14, 1
Ta 4,11,49152
G 175,247
Mvav 136,8,188
Ta 6,11,50176
K 168,168,169
Mvav 140,12,188
s_lshl_b32 s95, s57, 1
K 169,170,171
Mvav 108,12,184
s_add_u32 s94, s94, s95
K 170,172,173
Mvav 76,12,236
Ta 8,12,49152
K 171,174,175
Mvav 44,12,232
W 9
Ta 10,12,50176
buffer_store_dwordx4 v[168:171], v1, s[8:11], s36 offen offset:2048 sc1
Mvav 48,16,232
s_lshl_b32 s54, s94, 3
Mvav 80,16,236
s_add_u32 s54, s54, s59
Mvav 112,16,184
Ta 12,13,49152
Ta 14,13,50176
Mvav 144,16,188
s_lshl_b32 s54, s54, 6
buffer_load_dwordx4 a[200:203], v27, s[8:11], s28 offen offset:0 sc1
Mvav 148,20,188
Mvav 116,20,184
s_xor_b32 s55, s54, 15872
Mvav 84,20,236
Ta 16,14,49152
Ta 18,14,50176
Mvav 52,20,232
W 10
s_add_u32 s54, s54, 20480
G 176,248
Mvav 56,24,232
s_and_b32 s81, s14, 1
G 177,249
G 178,250
Mvav 88,24,236
s_add_u32 s55, s55, 20480
buffer_load_dwordx4 a[204:207], v27, s[8:11], s93 offen offset:0 sc1
G 179,251
Mvav 120,24,184
Ta 20,15,49152
G 180,252
Mvav 152,24,188
s_and_b32 s95, s14, 2
s_mul_i32 s91, s81, 17164288
Ta 22,15,50176
Mvav 156,28,188
G 181,253
G 182,254
Mvav 124,28,184
s_lshl_b32 s94, s94, 2
s_mul_i32 s82, s56, 536576
G 183,255
Mvav 92,28,236
s_add_u32 s94, s94, s95
Ta 24,24,49152
K 176,176,177
Mvav 60,28,232
s_waitcnt vmcnt(24) lgkmcnt(9)
Ta 26,24,50176
K 177,178,179
Mvaa 32,0,128
s_add_u32 s91, s91, s82
s_lshl_b32 s95, s95, 4
K 178,180,181
Mvaa 64,0,132
s_add_u32 s28, s28, 512
s_cmp_eq_u32 s28, s4
s_cselect_b32 s93, 10240, 0
s_sub_u32 s28, s28, s93
Mvaa 96,0,136
s_add_u32 s93, s28, 20480
buffer_load_dwordx4 a[32:35], v26, s[8:11], s28 offen offset:0 sc1
K 179,182,183
Mvaa 128,0,140
s_add_u32 s55, s55, s95
Ta 28,25,49152
Ta 30,25,50176
Mvaa 132,4,140
s_add_u32 s91, s91, 4194304
s_lshl_b32 s49, s94, 17
Mvaa 100,4,136
s_lshr_b32 s82, s14, 1
Mvaa 68,4,132
s_lshl_b32 s95, s59, 14
buffer_store_dwordx4 v[176:179], v1, s[8:11], s36 offen offset:3072 sc1
Ta 0,10,57344
Mvaa 36,4,128
W 9
s_lshl_b32 s83, s82, 14
Ta 2,10,58368
Mvaa 40,8,128
s_add_u32 s49, s49, s95
G 160,64
Mvaa 72,8,132
G 161,65
Mvaa 104,8,136
s_xor_b32 s50, s49, 16252928
s_add_u32 s91, s91, s83
buffer_load_dwordx4 a[36:39], v26, s[8:11], s93 offen offset:0 sc1
Mvaa 136,8,140
Ta 4,11,57344
G 162,66
Mvaa 140,12,140
s_add_u32 s49, s49, 39845888
Ta 6,11,58368
G 163,67
Mvaa 108,12,136
s_lshl_b32 s81, s81, 14
G 164,68
G 165,69
Mvaa 76,12,132
s_add_u32 s50, s50, 39845888
Ta 8,12,57344
G 166,70
Mvaa 44,12,128
W 9
Ta 10,12,58368
G 167,71
Mvaa 48,16,128
s_lshl_b32 s82, s82, 16
s_lshr_b32 s94, s15, 3
K 160,160,161
Mvaa 80,16,132
s_lshl_b32 s94, s94, 3
buffer_load_dwordx4 a[40:43], v27, s[8:11], s28 offen offset:0 sc1
K 161,162,163
Mvaa 112,16,136
s_or_b32 s92, s81, s82
K 162,164,165
K 163,166,167
Mvaa 144,16,140
s_add_u32 s36, s29, 393216
Ta 12,13,57344
Ta 14,13,58368
Mvaa 148,20,140
s_add_u32 s94, s94, s14
s_xor_b32 s92, s92, 0x10000
Mvaa 116,20,136
s_lshl_b32 s94, s94, 4
buffer_store_dwordx4 v[160:163], v1, s[8:11], s36 offen offset:0 sc1
Mvaa 84,20,132
Ta 16,14,57344
Ta 18,14,58368
Mvaa 52,20,128
W 10
s_add_u32 s94, s94, s59
Mvaa 56,24,128
Mvaa 88,24,132
s_mul_i32 s52, s94, 5120
buffer_load_dwordx4 a[44:47], v27, s[8:11], s93 offen offset:0 sc1
Mvaa 120,24,136
Ta 20,15,57344
Ta 22,15,58368
Mvaa 152,24,140
s_add_u32 s52, s52, 56623104
G 168,72
Mvaa 156,28,140
G 169,73
Mvaa 124,28,136
s_lshl_b32 s53, s94, 3
G 170,74
Mvaa 92,28,132
Ta 24,24,57344
G 171,75
Mvaa 60,28,128
s_waitcnt vmcnt(24) lgkmcnt(9)
s_add_u32 s53, s53, 65536
Ta 26,24,58368
Mvav 32,0,192
G 172,76
G 173,77
Mvav 64,0,196
v_bfe_u32 v244, v1, 4, 1
G 174,78
Mvav 96,0,200
G 175,79
v_mul_u32_u24 v244, 0xaaaa, v244
Mvav 128,0,204
Ta 28,25,57344
Ta 30,25,58368
Mvav 132,4,204
K 168,168,169
v_add_u32 v244, 0x4444, v244
Mvav 100,4,200
K 169,170,171
K 170,172,173
Mvav 68,4,196
v_lshl_or_b32 v240, v244, 16, v244
K 171,174,175
Mvav 36,4,192
W 8
Ta 0,2,32768
Ta 2,2,33792
Mvav 40,8,192
buffer_store_dwordx4 v[168:171], v1, s[8:11], s36 offen offset:1024 sc1
v_mov_b32 v241, 0xff800000
Mvav 72,8,196
v_add_u32 v242, 0x10000, v1
Mvav 104,8,200
Ta 4,3,32768
v_bfe_u32 v244, v1, 4, 4
Mvav 136,8,204
Ta 6,3,33792
Mvav 140,12,204
v_lshrrev_b32 v245, 8, v1
Mvav 108,12,200
v_lshlrev_b32 v245, 2, v245
Mvav 76,12,196
Ta 8,4,32768
Ta 10,4,33792
Mvav 44,12,192
W 10
G 176,80
v_sub_u32 v244, v244, v245
Mvav 48,16,192
G 177,81
G 178,82
Mvav 80,16,196
v_cmp_le_i32_e64 s[72:73], 0, v244
G 179,83
Mvav 112,16,200
v_cmp_le_i32_e64 s[74:75], 1, v244
G 180,84
Mvav 144,16,204
Ta 12,5,32768
Ta 14,5,33792
Mvav 148,20,204
G 181,85
v_cmp_le_i32_e64 s[76:77], 2, v244
Mvav 116,20,200
G 182,86
G 183,87
Mvav 84,20,196
v_cmp_le_i32_e64 s[78:79], 3, v244
K 176,176,177
Mvav 52,20,192
W 8
Ta 16,6,32768
Ta 18,6,33792
Mvav 56,24,192
K 177,178,179
v_add_u32 v243, 4096, v1
Mvav 88,24,196
K 178,180,181
v_add_u32 v244, 8192, v1
Mvav 120,24,200
K 179,182,183
v_add_u32 v245, 12288, v1
Mvav 152,24,204
buffer_store_dwordx4 v[176:179], v1, s[8:11], s36 offen offset:2048 sc1
Ta 20,7,32768
Mvav 156,28,204
s_mov_b32 s18, 0x3f803f80
Ta 22,7,33792
Mvav 124,28,200
s_mov_b32 s19, 0x3f003f00
Mvav 92,28,196
Ta 24,8,32768
Ta 26,8,33792
Mvav 60,28,192
s_waitcnt vmcnt(20) lgkmcnt(10)
v_accvgpr_write_b32 a176, s18
Mvaa 32,0,144
Mvaa 64,0,148
v_accvgpr_write_b32 a180, s19
G 160,88
Mvaa 96,0,184
v_mov_b32 v252, v241
G 161,89
Mvaa 128,0,188
Ta 28,9,32768
Ta 30,9,33792
Mvaa 132,4,188
G 162,90
v_accvgpr_write_b32 a177, s18
Mvaa 100,4,184
G 163,91
G 164,92
Mvaa 68,4,148
v_accvgpr_write_b32 a181, s19
G 165,93
Mvaa 36,4,144
W 8
Ta 0,2,40960
Ta 2,2,41984
Mvaa 40,8,144
v_mov_b32 v253, v241
G 166,94
Mvaa 72,8,148
G 167,95
v_accvgpr_write_b32 a178, s18
Mvaa 104,8,184
K 160,160,161
K 161,162,163
Mvaa 136,8,188
Ta 4,3,40960
v_accvgpr_write_b32 a182, s19
Mvaa 140,12,188
Ta 6,3,41984
K 162,164,165
Mvaa 108,12,184
K 163,166,167
v_mov_b32 v254, v241
Mvaa 76,12,148
buffer_store_dwordx4 v[160:163], v1, s[8:11], s36 offen offset:3072 sc1
v_accvgpr_write_b32 a179, s18
Mvaa 44,12,144
W 8
Ta 8,4,40960
Ta 10,4,41984
Mvaa 48,16,144
v_accvgpr_write_b32 a183, s19
Mvaa 80,16,148
v_mov_b32 v255, v241
Mvaa 112,16,184
Ta 12,5,40960
v_accvgpr_write_b32 a160, 0
Mvaa 144,16,188
Ta 14,5,41984
G 168,96
Mvaa 148,20,188
v_accvgpr_write_b32 a161, 0
G 169,97
Mvaa 116,20,184
G 170,98
v_accvgpr_write_b32 a162, 0
Mvaa 84,20,148
Ta 16,6,40960
G 171,99
Mvaa 52,20,144
W 9
Ta 18,6,41984
G 172,100
Mvaa 56,24,144
v_accvgpr_write_b32 a163, 0
G 173,101
Mvaa 88,24,148
G 174,102
v_accvgpr_write_b32 a164, 0
Mvaa 120,24,184
G 175,103
v_accvgpr_write_b32 a165, 0
Mvaa 152,24,188
Ta 20,7,40960
K 168,168,169
Mvaa 156,28,188
Ta 22,7,41984
K 169,170,171
Mvaa 124,28,184
v_accvgpr_write_b32 a166, 0
K 170,172,173
Mvaa 92,28,148
K 171,174,175
v_accvgpr_write_b32 a167, 0
Mvaa 60,28,144
s_waitcnt vmcnt(15) lgkmcnt(8)
s_add_u32 s36, s29, 397312
Ta 24,8,40960
Mvaa 32,0,152
Ta 26,8,41984
buffer_store_dwordx4 v[168:171], v1, s[8:11], s36 offen offset:0 sc1
Mvaa 64,0,156
v_accvgpr_write_b32 a168, 0
v_accvgpr_write_b32 a169, 0
Mvav 96,0,208
Ta 28,9,40960
v_accvgpr_write_b32 a170, 0
Mvav 128,0,212
Ta 30,9,41984
Mvav 132,4,212
v_accvgpr_write_b32 a171, 0
Mvav 100,4,208
v_accvgpr_write_b32 a172, 0
Mvaa 68,4,156
Ta 0,2,49152
Ta 2,2,50176
Mvaa 36,4,152
W 10
G 176,104
v_accvgpr_write_b32 a173, 0
Mvaa 40,8,152
G 177,105
G 178,106
)"
R"(Mvaa 72,8,156
v_accvgpr_write_b32 a174, 0
G 179,107
Mvav 104,8,208
G 180,108
v_accvgpr_write_b32 a175, 0
Mvav 136,8,212
Ta 4,3,49152
Ta 6,3,50176
Mvav 140,12,212
s_mov_b32 s90, 0x5d800000
G 181,109
G 182,110
Mvav 108,12,208
G 183,111
Mvaa 76,12,156
s_mov_b64 s[88:89], 0
Ta 8,4,49152
K 176,176,177
Mvaa 44,12,152
W 9
Ta 10,4,50176
K 177,178,179
Mvaa 48,16,152
s_mov_b32 s64, 0
K 178,180,181
Mvaa 80,16,156
s_and_b32 s18, s14, 1
K 179,182,183
Mvav 112,16,208
s_lshl_b32 s62, s18, 14
buffer_store_dwordx4 v[176:179], v1, s[8:11], s36 offen offset:1024 sc1
Ta 12,5,49152
Mvav 144,16,212
Ta 14,5,50176
Mvav 148,20,212
s_lshl_b32 s20, s57, 15
Mvav 116,20,208
Mvaa 84,20,156
s_sub_u32 s20, 507904, s20
Ta 16,6,49152
Ta 18,6,50176
Mvaa 52,20,152
W 10
s_add_u32 s20, s20, s62
Mvaa 56,24,152
Mvaa 88,24,156
s_lshl_b32 s65, s57, 1
G 160,112
Mvav 120,24,208
s_sub_u32 s65, s65, 1
Ta 20,7,49152
G 161,113
Mvav 152,24,212
Ta 22,7,50176
G 162,114
Mvav 156,28,212
s_max_i32 s65, s65, 0
G 163,115
Mvav 124,28,208
G 164,116
Mvaa 92,28,156
s_sub_u32 s66, s60, 1
Ta 24,8,49152
G 165,117
Mvaa 60,28,152
s_waitcnt vmcnt(11) lgkmcnt(9)
Ta 26,8,50176
G 166,118
Mvaa 32,0,192
s_cmp_lt_u32 s14, 2
s_cselect_b32 s20, s20, 16384
s_cselect_b32 s66, s66, 15
s_cselect_b32 s21, 32768, 0
G 167,119
Mvaa 64,0,196
s_add_u32 s61, s91, s20
K 160,160,161
Mvaa 96,0,200
s_add_u32 s62, s62, s21
Ta 28,9,49152
K 161,162,163
Mvaa 128,0,204
Ta 30,9,50176
K 162,164,165
Mvaa 132,4,204
s_sub_u32 s63, 16384, s21
K 163,166,167
Mvaa 100,4,200
buffer_store_dwordx4 v[160:163], v1, s[8:11], s36 offen offset:2048 sc1
Mvaa 68,4,196
s_lshl_b32 s67, s60, 7
Ta 0,2,57344
Ta 2,2,58368
Mvaa 36,4,192
W 10
s_add_u32 s67, s67, s56
Mvaa 40,8,192
Mvaa 72,8,196
s_lshl_b32 s67, s67, 12
Mvaa 104,8,200
s_add_u32 s67, s67, 39845888
Ta 4,3,57344
Ta 6,3,58368
Mvaa 136,8,204
Mvaa 140,12,204
s_lshl_b32 s69, s60, 3
G 168,120
Mvaa 108,12,200
G 169,121
Mvaa 76,12,196
s_add_u32 s69, s69, s59
Ta 8,4,57344
G 170,122
Mvaa 44,12,192
W 9
s_lshl_b32 s69, s69, 6
Ta 10,4,58368
Mvaa 48,16,192
G 171,123
G 172,124
Mvaa 80,16,196
s_lshl_b32 s21, s58, 3
G 173,125
Mvaa 112,16,200
s_add_u32 s69, s69, s21
Ta 12,5,57344
G 174,126
Mvaa 144,16,204
Ta 14,5,58368
G 175,127
Mvaa 148,20,204
s_add_u32 s69, s69, 20480
K 168,168,169
Mvaa 116,20,200
K 169,170,171
Mvaa 84,20,196
s_mov_b32 s70, 0
Ta 16,6,57344
K 170,172,173
Mvaa 52,20,192
W 9
Ta 18,6,58368
K 171,174,175
Mvaa 56,24,192
s_mov_b32 s37, 0
buffer_store_dwordx4 v[168:171], v1, s[8:11], s36 offen offset:3072 sc1
Mvaa 88,24,196
s_sub_u32 s18, s57, 4
Mvaa 120,24,200
s_lshl_b32 s18, s18, 2
Ta 20,7,57344
Ta 22,7,58368
Mvaa 152,24,204
Mvaa 156,28,204
s_add_u32 s18, s18, s58
Mvaa 124,28,200
Mvaa 92,28,196
s_mul_i32 s85, s59, 7
Ta 24,8,57344
Ta 26,8,58368
Mvaa 60,28,192
s_waitcnt vmcnt(7) lgkmcnt(10)
s_add_u32 s85, s85, s18
Mvaa 32,0,32
Mvaa 64,0,36
s_cmp_lt_u32 s18, 7
s_cselect_b32 s85, s85, 99
Mvaa 96,0,40
s_cmp_lt_u32 s85, 54
s_cselect_b32 s65, 2, s65
Ta 28,9,57344
Ta 30,9,58368
Mvaa 128,0,44
Mvaa 132,4,44
s_mov_b32 s68, 0
Mvaa 100,4,40
Mvaa 68,4,36
s_mov_b64 s[24:25], -1
Mvaa 36,4,32
W 8
s_mov_b32 s22, 0x3e0293ee
Mvaa 40,8,32
Mvaa 72,8,36
s_mov_b32 s23, 0x3e0293ee
Mvaa 104,8,40
Mvaa 136,8,44
Mvaa 140,12,44
Mvaa 108,12,40
Mvaa 76,12,36
Mvaa 44,12,32
W 4
Mvaa 48,16,32
Mvaa 80,16,36
Mvaa 112,16,40
Mvaa 144,16,44
Mvaa 148,20,44
W 0
s_barrier
Ra 128,242,0
Mvaa 116,20,40
Mvaa 84,20,36
Ra 132,242,1024
Mvaa 52,20,32
Mvaa 56,24,32
Ra 136,242,2048
Mvaa 88,24,36
Mvaa 120,24,40
Ra 140,242,3072
Mvaa 152,24,44
Mvaa 156,28,44
Mvaa 124,28,40
Mvaa 92,28,36
Mvaa 60,28,32
.Lago%=:
s_cmp_eq_u32 s57, 0
s_cbranch_scc0 .LL_46%=
s_add_u32 m0, s62, 0
s_nop 0
Dc 1,8,61,0
Dc 1,8,61,1024
Dc 1,8,61,2048
Dc 1,8,61,3072
s_add_u32 m0, s62, 4096
s_nop 0
Dc 243,8,61,0
Dc 243,8,61,1024
Dc 243,8,61,2048
Dc 243,8,61,3072
s_add_u32 m0, s62, 8192
s_nop 0
Dc 244,8,61,0
Dc 244,8,61,1024
Dc 244,8,61,2048
Dc 244,8,61,3072
s_add_u32 m0, s62, 12288
s_nop 0
Dc 245,8,61,0
Dc 245,8,61,1024
Dc 245,8,61,2048
Dc 245,8,61,3072
s_add_u32 s61, s61, s63
.LL_46%=:
s_cmp_eq_u32 s60, 0
s_cbranch_scc1 .Lpd_30%=
v_pk_mul_f32 v[32:33], v[32:33], s[22:23]
v_pk_mul_f32 v[34:35], v[34:35], s[22:23]
v_pk_mul_f32 v[36:37], v[36:37], s[22:23]
v_pk_mul_f32 v[38:39], v[38:39], s[22:23]
K 32,32,33
K 33,34,35
K 34,36,37
K 35,38,39
v_pk_mul_f32 v[64:65], v[64:65], s[22:23]
v_pk_mul_f32 v[66:67], v[66:67], s[22:23]
v_pk_mul_f32 v[68:69], v[68:69], s[22:23]
v_pk_mul_f32 v[70:71], v[70:71], s[22:23]
K 64,64,65
K 65,66,67
K 66,68,69
K 67,70,71
v_pk_mul_f32 v[96:97], v[96:97], s[22:23]
v_pk_mul_f32 v[98:99], v[98:99], s[22:23]
v_pk_mul_f32 v[100:101], v[100:101], s[22:23]
v_pk_mul_f32 v[102:103], v[102:103], s[22:23]
K 96,96,97
K 97,98,99
K 98,100,101
K 99,102,103
v_pk_mul_f32 v[128:129], v[128:129], s[22:23]
v_pk_mul_f32 v[130:131], v[130:131], s[22:23]
v_pk_mul_f32 v[132:133], v[132:133], s[22:23]
v_pk_mul_f32 v[134:135], v[134:135], s[22:23]
K 128,128,129
K 129,130,131
K 130,132,133
K 131,134,135
v_pk_mul_f32 v[40:41], v[40:41], s[22:23]
v_pk_mul_f32 v[42:43], v[42:43], s[22:23]
v_pk_mul_f32 v[44:45], v[44:45], s[22:23]
v_pk_mul_f32 v[46:47], v[46:47], s[22:23]
v_pk_mul_f32 v[72:73], v[72:73], s[22:23]
v_pk_mul_f32 v[74:75], v[74:75], s[22:23]
v_pk_mul_f32 v[76:77], v[76:77], s[22:23]
v_pk_mul_f32 v[78:79], v[78:79], s[22:23]
v_pk_mul_f32 v[104:105], v[104:105], s[22:23]
v_pk_mul_f32 v[106:107], v[106:107], s[22:23]
v_pk_mul_f32 v[108:109], v[108:109], s[22:23]
v_pk_mul_f32 v[110:111], v[110:111], s[22:23]
v_pk_mul_f32 v[136:137], v[136:137], s[22:23]
v_pk_mul_f32 v[138:139], v[138:139], s[22:23]
v_pk_mul_f32 v[140:141], v[140:141], s[22:23]
v_pk_mul_f32 v[142:143], v[142:143], s[22:23]
v_pk_mul_f32 v[48:49], v[48:49], s[22:23]
v_pk_mul_f32 v[50:51], v[50:51], s[22:23]
v_pk_mul_f32 v[52:53], v[52:53], s[22:23]
v_pk_mul_f32 v[54:55], v[54:55], s[22:23]
v_pk_mul_f32 v[80:81], v[80:81], s[22:23]
v_pk_mul_f32 v[82:83], v[82:83], s[22:23]
v_pk_mul_f32 v[84:85], v[84:85], s[22:23]
v_pk_mul_f32 v[86:87], v[86:87], s[22:23]
v_pk_mul_f32 v[112:113], v[112:113], s[22:23]
v_pk_mul_f32 v[114:115], v[114:115], s[22:23]
v_pk_mul_f32 v[116:117], v[116:117], s[22:23]
v_pk_mul_f32 v[118:119], v[118:119], s[22:23]
v_pk_mul_f32 v[144:145], v[144:145], s[22:23]
v_pk_mul_f32 v[146:147], v[146:147], s[22:23]
v_pk_mul_f32 v[148:149], v[148:149], s[22:23]
v_pk_mul_f32 v[150:151], v[150:151], s[22:23]
v_pk_mul_f32 v[56:57], v[56:57], s[22:23]
v_pk_mul_f32 v[58:59], v[58:59], s[22:23]
v_pk_mul_f32 v[60:61], v[60:61], s[22:23]
v_pk_mul_f32 v[62:63], v[62:63], s[22:23]
v_pk_mul_f32 v[88:89], v[88:89], s[22:23]
v_pk_mul_f32 v[90:91], v[90:91], s[22:23]
v_pk_mul_f32 v[92:93], v[92:93], s[22:23]
v_pk_mul_f32 v[94:95], v[94:95], s[22:23]
v_pk_mul_f32 v[120:121], v[120:121], s[22:23]
v_pk_mul_f32 v[122:123], v[122:123], s[22:23]
v_pk_mul_f32 v[124:125], v[124:125], s[22:23]
v_pk_mul_f32 v[126:127], v[126:127], s[22:23]
v_pk_mul_f32 v[152:153], v[152:153], s[22:23]
v_pk_mul_f32 v[154:155], v[154:155], s[22:23]
v_pk_mul_f32 v[156:157], v[156:157], s[22:23]
v_pk_mul_f32 v[158:159], v[158:159], s[22:23]
W 0
Zvav 196,128,32
Ra 144,242,4096
Ra 148,242,5120
Ra 152,242,6144
Ra 156,242,7168
Zvav 204,128,64
K 40,40,41
K 41,42,43
K 42,44,45
K 43,46,47
Zvav 212,128,96
K 72,72,73
K 73,74,75
K 74,76,77
K 75,78,79
Zvav 220,128,128
K 104,104,105
K 105,106,107
K 106,108,109
K 107,110,111
K 136,136,137
K 137,138,139
K 138,140,141
K 139,142,143
Mvav 196,132,40
Ra 128,242,8192
K 48,48,49
Mvav 204,132,72
K 49,50,51
K 50,52,53
K 51,54,55
K 80,80,81
Mvav 212,132,104
K 81,82,83
K 82,84,85
K 83,86,87
K 112,112,113
Mvav 220,132,136
K 113,114,115
K 114,116,117
K 115,118,119
K 144,144,145
K 145,146,147
K 146,148,149
K 147,150,151
Mvav 196,136,48
Ra 132,242,9216
K 56,56,57
Mvav 204,136,80
K 57,58,59
K 58,60,61
K 59,62,63
K 88,88,89
Mvav 212,136,112
K 89,90,91
K 90,92,93
K 91,94,95
K 120,120,121
Mvav 220,136,144
K 121,122,123
K 122,124,125
K 123,126,127
K 152,152,153
K 153,154,155
K 154,156,157
K 155,158,159
Mvav 196,140,56
Ra 136,242,10240
v_accvgpr_write_b32 a0, v32
Mvav 204,140,88
v_accvgpr_write_b32 a1, v33
Mvav 212,140,120
v_accvgpr_write_b32 a2, v34
Mvav 220,140,152
)"
R"(W 3
v_accvgpr_write_b32 a3, v35
v_accvgpr_write_b32 a4, v40
Zvav 36,144,32
Ra 140,242,11264
v_accvgpr_write_b32 a5, v41
Zvav 68,144,64
v_accvgpr_write_b32 a6, v42
v_accvgpr_write_b32 a7, v43
v_max3_f32 v4, v196, v197, v198
v_max3_f32 v4, v4, v199, v252
Zvav 100,144,96
v_max3_f32 v5, v204, v205, v206
v_max3_f32 v5, v5, v207, v253
v_max3_f32 v6, v212, v213, v214
v_max3_f32 v6, v6, v215, v254
Zvav 132,144,128
v_max3_f32 v7, v220, v221, v222
v_max3_f32 v7, v7, v223, v255
v_mov_b32 v8, v4
v_mov_b32 v9, v5
Mvav 36,148,40
v_mov_b32 v10, v6
v_mov_b32 v11, v7
v_permlane32_swap_b32 v4, v8
v_accvgpr_write_b32 a8, v48
Mvav 68,148,72
v_permlane32_swap_b32 v5, v9
v_permlane32_swap_b32 v6, v10
v_permlane32_swap_b32 v7, v11
v_max_f32 v4, v4, v8
Mvav 100,148,104
v_max_f32 v5, v5, v9
v_max_f32 v6, v6, v10
v_max_f32 v7, v7, v11
v_mov_b32 v8, v4
Mvav 132,148,136
v_mov_b32 v9, v5
v_mov_b32 v10, v6
v_mov_b32 v11, v7
v_accvgpr_write_b32 a9, v49
Mvav 36,152,48
v_permlane16_swap_b32 v4, v8
v_permlane16_swap_b32 v5, v9
v_permlane16_swap_b32 v6, v10
v_permlane16_swap_b32 v7, v11
Mvav 68,152,80
v_max_f32 v4, v4, v8
v_max_f32 v5, v5, v9
v_max_f32 v6, v6, v10
v_max_f32 v7, v7, v11
Mvav 100,152,112
v_sub_f32 v4, 0, v4
v_sub_f32 v5, 0, v5
v_sub_f32 v6, 0, v6
v_sub_f32 v7, 0, v7
Mvav 132,152,144
v_mov_b32 v224, v4
v_mov_b32 v225, v4
v_mov_b32 v226, v4
v_mov_b32 v227, v4
Mvav 36,156,56
v_mov_b32 v228, v5
v_mov_b32 v229, v5
v_mov_b32 v230, v5
v_mov_b32 v231, v5
Mvav 68,156,88
v_mov_b32 v232, v6
v_mov_b32 v233, v6
v_mov_b32 v234, v6
v_mov_b32 v235, v6
Mvav 100,156,120
v_mov_b32 v236, v7
v_mov_b32 v237, v7
v_mov_b32 v238, v7
v_mov_b32 v239, v7
Mvav 132,156,152
W 0
Ra 144,242,12288
v_accvgpr_write_b32 a10, v50
v_accvgpr_write_b32 a11, v51
v_accvgpr_write_b32 a12, v56
Cvav 160,128,32,224
s_and_b32 s80, s14, 1
v_accvgpr_write_b32 a13, v57
v_accvgpr_write_b32 a14, v58
v_accvgpr_write_b32 a15, v59
v_accvgpr_write_b32 a16, v64
Cvav 164,128,64,228
s_lshl_b32 s80, s80, 14
Ra 148,242,13312
v_accvgpr_write_b32 a17, v65
v_accvgpr_write_b32 a18, v66
v_accvgpr_write_b32 a19, v67
Cvav 168,128,96,232
v_accvgpr_write_b32 a20, v72
v_accvgpr_write_b32 a21, v73
v_accvgpr_write_b32 a22, v74
v_accvgpr_write_b32 a23, v75
Cvav 172,128,128,236
s_add_u32 s80, s80, 98304
Ra 152,242,14336
v_accvgpr_write_b32 a24, v80
v_accvgpr_write_b32 a25, v81
v_accvgpr_write_b32 a26, v82
Mvav 160,132,40
A 196,196,224
v_accvgpr_write_b32 a27, v83
A 197,197,224
A 198,198,224
Mvav 164,132,72
Ra 156,242,15360
v_accvgpr_write_b32 a28, v88
A 199,199,224
v_accvgpr_write_b32 a29, v89
Mvav 168,132,104
X 196
v_accvgpr_write_b32 a30, v90
v_accvgpr_write_b32 a31, v91
Mvav 172,132,136
X 197
X 198
Mvav 160,136,48
v_accvgpr_write_b32 a32, v96
K 192,196,197
X 199
Mvav 164,136,80
v_accvgpr_write_b32 a33, v97
A 204,204,228
v_accvgpr_write_b32 a34, v98
K 193,198,199
Mvav 168,136,112
Ra 128,242,24576
v_accvgpr_write_b32 a35, v99
A 205,205,228
A 206,206,228
Mvav 172,136,144
v_accvgpr_write_b32 a36, v104
v_add_u32 v8, s80, v1
A 207,207,228
v_accvgpr_write_b32 a37, v105
Mvav 160,140,56
X 204
X 205
Mvav 164,140,88
v_accvgpr_write_b32 a38, v106
X 206
v_accvgpr_write_b32 a39, v107
Mvav 168,140,120
v_accvgpr_write_b32 a40, v112
K 200,204,205
X 207
Mvav 172,140,152
W 1
Ra 132,242,25600
v_accvgpr_write_b32 a41, v113
A 212,212,232
v_accvgpr_write_b32 a42, v114
Cvav 176,144,32,224
K 201,206,207
A 213,213,232
v_accvgpr_write_b32 a43, v115
A 214,214,232
Cvav 180,144,64,228
A 215,215,232
v_accvgpr_write_b32 a44, v120
X 212
Cvav 184,144,96,232
v_accvgpr_write_b32 a45, v121
X 213
v_accvgpr_write_b32 a46, v122
Cvav 188,144,128,236
Ra 136,242,26624
X 214
v_accvgpr_write_b32 a47, v123
Mvav 176,148,40
K 208,212,213
v_accvgpr_write_b32 a48, v128
X 215
Mvav 180,148,72
v_accvgpr_write_b32 a49, v129
A 220,220,236
v_accvgpr_write_b32 a50, v130
K 209,214,215
Mvav 184,148,104
A 221,221,236
v_accvgpr_write_b32 a51, v131
A 222,222,236
A 223,223,236
Mvav 188,148,136
v_accvgpr_write_b32 a52, v136
X 220
v_accvgpr_write_b32 a53, v137
Mvav 176,152,48
Ra 140,242,27648
X 221
v_accvgpr_write_b32 a54, v138
Mvav 180,152,80
X 222
v_accvgpr_write_b32 a55, v139
K 216,220,221
Mvav 184,152,112
v_accvgpr_write_b32 a56, v144
X 223
v_accvgpr_write_b32 a57, v145
Mvav 188,152,144
A 36,36,224
K 217,222,223
v_accvgpr_write_b32 a58, v146
A 37,37,224
Mvav 176,156,56
v_accvgpr_write_b32 a59, v147
v_accvgpr_write_b32 a60, v152
v_accvgpr_write_b32 a61, v153
v_accvgpr_write_b32 a62, v154
v_accvgpr_write_b32 a63, v155
Mvav 180,156,88
s_barrier
Ra 144,242,28672
Ra 148,242,29696
A 38,38,224
A 39,39,224
Mvav 184,156,120
X 36
X 37
X 38
K 194,36,37
X 39
A 68,68,228
K 195,38,39
A 69,69,228
A 70,70,228
A 71,71,228
X 68
X 69
X 70
K 202,68,69
X 71
A 100,100,232
K 203,70,71
A 101,101,232
A 102,102,232
A 103,103,232
X 100
X 101
X 102
K 210,100,101
X 103
A 132,132,236
K 211,102,103
A 133,133,236
A 134,134,236
A 135,135,236
X 132
X 133
X 134
K 218,132,133
X 135
s_nop 0
K 219,134,135
Mvav 188,156,152
W 2
Ra 152,242,30720
Zvav 32,128,192
Ra 156,242,31744
Zvav 64,128,200
Zvav 96,128,208
s_and_b32 s80, s14, 1
s_xor_b32 s62, s62, 0x10000
Zvav 128,128,216
s_lshl_b32 s80, s80, 14
s_add_u32 s80, s80, 131072
v_add_u32 v23, 61440, v21
Zvav 132,132,216
v_add_u32 v12, s80, v1
Ra 64,12,8192
Ra 68,12,9216
v_add_u32 v24, 61440, v22
Zvav 100,132,208
Ra 72,12,10240
Ra 76,12,11264
Ra 80,12,12288
v_add_u32 v25, 122880, v21
Zvav 68,132,200
Ra 84,12,13312
Ra 88,12,14336
Ra 92,12,15360
v_add_u32 v26, 122880, v22
Zvav 36,132,192
Ra 96,12,0
Ra 100,12,1024
Ra 104,12,2048
Zvav 40,136,192
Ra 108,12,3072
Ra 112,12,4096
X 172
Zvav 72,136,200
Ra 116,12,5120
Ra 120,12,6144
X 173
Zvav 104,136,208
Ra 124,12,7168
X 174
K 220,172,173
Zvav 136,136,216
X 175
Zvav 140,140,216
X 168
K 221,174,175
Zvav 108,140,208
X 169
X 170
Zvav 76,140,200
s_waitcnt vmcnt(0) lgkmcnt(0)
s_barrier
v_xor_b32 v242, 0x10000, v242
ds_read_b128 v[248:251], v8
Zvav 44,140,192
s_add_u32 s80, s71, 128
v_mov_b64 v[28:29], s[12:13]
s_mov_b64 exec, 1
buffer_store_dwordx2 v[28:29], off, s[8:11], s80 sc1
s_mov_b64 exec, -1
Ra 128,242,0
K 212,168,169
X 171
Zvav 48,144,192
X 164
K 213,170,171
Zvav 80,144,200
X 165
X 166
Zvav 112,144,208
Ra 132,242,1024
K 204,164,165
X 167
Zvav 144,144,216
X 160
Zvav 148,148,216
K 205,166,167
X 161
Zvav 116,148,208
Ra 136,242,2048
X 162
Zvav 84,148,200
W 3
K 196,160,161
X 163
Zvav 52,148,192
X 176
K 197,162,163
Zvav 56,152,192
Ra 140,242,3072
X 177
Zvav 88,152,200
X 178
K 198,176,177
Zvav 120,152,208
X 179
X 180
Zvav 152,152,216
K 199,178,179
Zvav 156,156,216
X 181
X 182
Zvav 124,156,208
K 206,180,181
X 183
Zvav 92,156,200
K 207,182,183
Zvav 60,156,192
.Ll0t_44%=:
s_cmp_eq_u32 s64, s65
s_cbranch_scc1 .Ll1t_33%=
.Ll0_31%=:
W 0
Cvaa 160,128,0,224
Ra 144,242,4096
Ra 148,242,5120
Cvaa 164,128,16,228
s_xor_b32 s62, s62, 0x10000
Ra 152,242,6144
Cvaa 168,128,32,232
X 184
Cvaa 172,128,48,236
X 185
Mvaa 160,132,4
X 186
Mvaa 164,132,20
Ra 156,242,7168
K 214,184,185
Mvaa 168,132,36
X 187
Mvaa 172,132,52
X 188
Mvaa 160,136,8
Ra 128,242,16384
Mvaa 164,136,24
K 215,186,187
Mvaa 168,136,40
)"
R"(X 189
Mvaa 172,136,56
X 190
Mvaa 160,140,12
Ra 132,242,17408
Mvaa 164,140,28
K 222,188,189
Mvaa 168,140,44
X 191
Mvaa 172,140,60
K 223,190,191
v_smfmac_f32_16x16x64_bf16 a[160:163], a[176:179], v[192:199], v240
Ra 136,242,18432
v_smfmac_f32_16x16x64_bf16 a[164:167], a[176:179], v[200:207], v240
X 160
v_smfmac_f32_16x16x64_bf16 a[168:171], a[176:179], v[208:215], v240
X 161
v_smfmac_f32_16x16x64_bf16 a[172:175], a[176:179], v[216:223], v240
W 3
X 162
Cvaa 176,144,0,224
Ra 140,242,19456
Cvaa 180,144,16,228
K 192,160,161
Cvaa 184,144,32,232
X 163
Cvaa 188,144,48,236
X 164
Mvaa 176,148,4
Ra 144,242,20480
Mvaa 180,148,20
K 193,162,163
Mvaa 184,148,36
X 165
Mvaa 188,148,52
X 166
Mvaa 176,152,8
Ra 148,242,21504
Mvaa 180,152,24
K 200,164,165
Mvaa 184,152,40
X 167
Mvaa 188,152,56
X 168
Mvaa 176,156,12
Ra 152,242,22528
Mvaa 180,156,28
K 201,166,167
Mvaa 184,156,44
X 169
Mvaa 188,156,60
W 3
Mvav 32,128,196
X 170
Mvav 64,128,204
Ra 156,242,23552
K 208,168,169
Mvav 96,128,212
X 171
Mvav 128,128,220
Mvav 132,132,220
X 172
Mvav 100,132,212
s_add_u32 s64, s64, 1
Ra 128,242,8192
K 209,170,171
Mvav 68,132,204
X 173
Mvav 36,132,196
Mvav 40,136,196
X 174
Mvav 72,136,204
Ra 132,242,9216
K 216,172,173
Mvav 104,136,212
X 175
Mvav 136,136,220
Mvav 140,140,220
X 176
Mvav 108,140,212
Ra 136,242,10240
K 217,174,175
Mvav 76,140,204
X 177
Mvav 44,140,196
W 3
Mvav 48,144,196
X 178
Mvav 80,144,204
Ra 140,242,11264
Mvav 112,144,212
K 194,176,177
Mvav 144,144,220
X 179
Mvav 148,148,220
X 180
Mvav 116,148,212
Ra 144,242,12288
Mvav 84,148,204
K 195,178,179
Mvav 52,148,196
X 181
Mvav 56,152,196
X 182
Mvav 88,152,204
Ra 148,242,13312
Mvav 120,152,212
K 202,180,181
Mvav 152,152,220
X 183
Mvav 156,156,220
X 184
Mvav 124,156,212
Ra 152,242,14336
Mvav 92,156,204
K 203,182,183
Mvav 60,156,196
W 3
X 185
Cvaa 160,128,0,224
X 186
Cvaa 164,128,16,228
Ra 156,242,15360
Cvaa 168,128,32,232
K 210,184,185
Cvaa 172,128,48,236
X 187
Mvaa 160,132,4
X 188
Mvaa 164,132,20
Ra 128,242,24576
Mvaa 168,132,36
K 211,186,187
Mvaa 172,132,52
X 189
Mvaa 160,136,8
X 190
Mvaa 164,136,24
Ra 132,242,25600
Mvaa 168,136,40
K 218,188,189
Mvaa 172,136,56
X 191
Mvaa 160,140,12
Ra 136,242,26624
Mvaa 164,140,28
Mvaa 168,140,44
Mvaa 172,140,60
W 3
Cvaa 176,144,0,224
Ra 140,242,27648
Cvaa 180,144,16,228
X 160
Cvaa 184,144,32,232
K 219,190,191
Cvaa 188,144,48,236
X 161
Mvaa 176,148,4
X 162
Mvaa 180,148,20
Ra 144,242,28672
K 196,160,161
Mvaa 184,148,36
X 163
Mvaa 188,148,52
X 164
Mvaa 176,152,8
Ra 148,242,29696
K 197,162,163
Mvaa 180,152,24
X 165
Mvaa 184,152,40
X 166
Mvaa 188,152,56
K 204,164,165
Mvaa 176,156,12
X 167
Mvaa 180,156,28
Ra 152,242,30720
Mvaa 184,156,44
X 168
Mvaa 188,156,60
W 3
K 205,166,167
Mvav 32,128,192
X 169
Mvav 64,128,200
Ra 156,242,31744
Mvav 96,128,208
X 170
Mvav 128,128,216
K 212,168,169
Mvav 132,132,216
X 171
Mvav 100,132,208
X 172
Mvav 68,132,200
Mvav 36,132,192
K 213,170,171
Mvav 40,136,192
X 173
Mvav 72,136,200
X 174
Mvav 104,136,208
Mvav 136,136,216
K 220,172,173
Mvav 140,140,216
X 175
Mvav 108,140,208
X 176
Mvav 76,140,200
W 0
s_barrier
v_xor_b32 v242, 0x10000, v242
Mvav 44,140,192
Ra 128,242,0
K 221,174,175
Mvav 48,144,192
X 177
Mvav 80,144,200
X 178
Mvav 112,144,208
Ra 132,242,1024
Mvav 144,144,216
K 198,176,177
Mvav 148,148,216
X 179
Mvav 116,148,208
X 180
Mvav 84,148,200
Ra 136,242,2048
Mvav 52,148,192
K 199,178,179
Mvav 56,152,192
X 181
Mvav 88,152,200
Ra 140,242,3072
Mvav 120,152,208
X 182
Mvav 152,152,216
K 206,180,181
Mvav 156,156,216
X 183
Mvav 124,156,208
Mvav 92,156,200
K 207,182,183
Mvav 60,156,192
s_cmp_lg_u32 s64, s65
s_cbranch_scc1 .Ll0_31%=
s_cmp_lt_u32 s85, 54
s_cbranch_scc1 .Lmoe_42%=
.Ll1t_33%=:
s_cmp_eq_u32 s64, s66
s_cbranch_scc1 .Lsp1_34%=
.Ll1_32%=:
W 0
Cvaa 160,128,0,224
Ra 144,242,4096
Ra 148,242,5120
Cvaa 164,128,16,228
s_add_u32 m0, s62, 0
Ra 152,242,6144
Cvaa 168,128,32,232
X 184
Cvaa 172,128,48,236
X 185
Mvaa 160,132,4
X 186
Mvaa 164,132,20
Ra 156,242,7168
K 214,184,185
Mvaa 168,132,36
Dc 1,8,61,0
Mvaa 172,132,52
X 187
Mvaa 160,136,8
X 188
Mvaa 164,136,24
Ra 128,242,16384
K 215,186,187
Mvaa 168,136,40
Dc 1,8,61,1024
Mvaa 172,136,56
X 189
Mvaa 160,140,12
X 190
Mvaa 164,140,28
Ra 132,242,17408
K 222,188,189
Mvaa 168,140,44
Dc 1,8,61,2048
Mvaa 172,140,60
X 191
v_smfmac_f32_16x16x64_bf16 a[160:163], a[176:179], v[192:199], v240
Ra 136,242,18432
K 223,190,191
v_smfmac_f32_16x16x64_bf16 a[164:167], a[176:179], v[200:207], v240
X 160
v_smfmac_f32_16x16x64_bf16 a[168:171], a[176:179], v[208:215], v240
Dc 1,8,61,3072
v_smfmac_f32_16x16x64_bf16 a[172:175], a[176:179], v[216:223], v240
W 3
X 161
Cvaa 176,144,0,224
s_add_u32 m0, s62, 4096
X 162
Cvaa 180,144,16,228
Ra 140,242,19456
K 192,160,161
Cvaa 184,144,32,232
X 163
Cvaa 188,144,48,236
Dc 243,8,61,0
Mvaa 176,148,4
X 164
Mvaa 180,148,20
Ra 144,242,20480
K 193,162,163
Mvaa 184,148,36
X 165
Mvaa 188,148,52
Dc 243,8,61,1024
Mvaa 176,152,8
X 166
Mvaa 180,152,24
Ra 148,242,21504
K 200,164,165
Mvaa 184,152,40
X 167
Mvaa 188,152,56
Dc 243,8,61,2048
Mvaa 176,156,12
X 168
Mvaa 180,156,28
Ra 152,242,22528
K 201,166,167
Mvaa 184,156,44
X 169
Mvaa 188,156,60
W 3
Dc 243,8,61,3072
Mvav 32,128,196
s_add_u32 m0, s62, 8192
X 170
Mvav 64,128,204
Ra 156,242,23552
K 208,168,169
Mvav 96,128,212
X 171
Mvav 128,128,220
Dc 244,8,61,0
Mvav 132,132,220
X 172
Mvav 100,132,212
Ra 128,242,8192
K 209,170,171
Mvav 68,132,204
X 173
Mvav 36,132,196
Dc 244,8,61,1024
Mvav 40,136,196
X 174
Mvav 72,136,204
Ra 132,242,9216
K 216,172,173
Mvav 104,136,212
X 175
Mvav 136,136,220
Dc 244,8,61,2048
Mvav 140,140,220
X 176
Mvav 108,140,212
Ra 136,242,10240
K 217,174,175
Mvav 76,140,204
X 177
Mvav 44,140,196
W 3
Dc 244,8,61,3072
Mvav 48,144,196
X 178
Mvav 80,144,204
s_add_u32 m0, s62, 12288
Ra 140,242,11264
Mvav 112,144,212
K 194,176,177
Mvav 144,144,220
X 179
Mvav 148,148,220
X 180
Mvav 116,148,212
Dc 245,8,61,0
Ra 144,242,12288
Mvav 84,148,204
K 195,178,179
Mvav 52,148,196
X 181
Mvav 56,152,196
X 182
Mvav 88,152,204
)"
R"(Dc 245,8,61,1024
Ra 148,242,13312
Mvav 120,152,212
K 202,180,181
Mvav 152,152,220
X 183
Mvav 156,156,220
X 184
Mvav 124,156,212
Dc 245,8,61,2048
Ra 152,242,14336
Mvav 92,156,204
K 203,182,183
Mvav 60,156,196
W 3
X 185
Cvaa 160,128,0,224
X 186
Cvaa 164,128,16,228
Dc 245,8,61,3072
Ra 156,242,15360
Cvaa 168,128,32,232
s_add_u32 s61, s61, s63
K 210,184,185
Cvaa 172,128,48,236
X 187
Mvaa 160,132,4
X 188
Mvaa 164,132,20
Ra 128,242,24576
Mvaa 168,132,36
s_xor_b32 s62, s62, 0x10000
K 211,186,187
Mvaa 172,132,52
X 189
Mvaa 160,136,8
X 190
Mvaa 164,136,24
s_add_u32 s64, s64, 1
Ra 132,242,25600
Mvaa 168,136,40
K 218,188,189
Mvaa 172,136,56
X 191
Mvaa 160,140,12
Ra 136,242,26624
Mvaa 164,140,28
Mvaa 168,140,44
Mvaa 172,140,60
W 3
Cvaa 176,144,0,224
Ra 140,242,27648
Cvaa 180,144,16,228
X 160
Cvaa 184,144,32,232
K 219,190,191
Cvaa 188,144,48,236
X 161
Mvaa 176,148,4
X 162
Mvaa 180,148,20
Ra 144,242,28672
K 196,160,161
Mvaa 184,148,36
X 163
Mvaa 188,148,52
X 164
Mvaa 176,152,8
Ra 148,242,29696
K 197,162,163
Mvaa 180,152,24
X 165
Mvaa 184,152,40
X 166
Mvaa 188,152,56
K 204,164,165
Mvaa 176,156,12
X 167
Mvaa 180,156,28
Ra 152,242,30720
Mvaa 184,156,44
X 168
Mvaa 188,156,60
W 3
K 205,166,167
Mvav 32,128,192
X 169
Mvav 64,128,200
Ra 156,242,31744
Mvav 96,128,208
X 170
Mvav 128,128,216
K 212,168,169
Mvav 132,132,216
X 171
Mvav 100,132,208
X 172
Mvav 68,132,200
Mvav 36,132,192
K 213,170,171
Mvav 40,136,192
X 173
Mvav 72,136,200
X 174
Mvav 104,136,208
Mvav 136,136,216
K 220,172,173
Mvav 140,140,216
X 175
Mvav 108,140,208
X 176
Mvav 76,140,200
s_waitcnt vmcnt(0) lgkmcnt(0)
s_barrier
v_xor_b32 v242, 0x10000, v242
Mvav 44,140,192
Ra 128,242,0
K 221,174,175
Mvav 48,144,192
X 177
Mvav 80,144,200
X 178
Mvav 112,144,208
Ra 132,242,1024
Mvav 144,144,216
K 198,176,177
Mvav 148,148,216
X 179
Mvav 116,148,208
X 180
Mvav 84,148,200
Ra 136,242,2048
Mvav 52,148,192
K 199,178,179
Mvav 56,152,192
X 181
Mvav 88,152,200
Ra 140,242,3072
Mvav 120,152,208
X 182
Mvav 152,152,216
K 206,180,181
Mvav 156,156,216
X 183
Mvav 124,156,208
Mvav 92,156,200
K 207,182,183
Mvav 60,156,192
s_cmp_lg_u32 s64, s66
s_cbranch_scc1 .Ll1_32%=
s_cmp_ge_u32 s14, 2
s_cbranch_scc1 .Lf1r_45%=
.Lsp1_34%=:
W 0
Cvaa 160,128,0,224
Ra 144,242,4096
Ra 148,242,5120
Cvaa 164,128,16,228
s_add_u32 m0, s62, 0
Ra 152,242,6144
Ra 156,242,7168
Cvaa 168,128,32,232
X 184
Cvaa 172,128,48,236
X 185
Mvaa 160,132,4
X 186
Mvaa 164,132,20
Ra 128,242,16384
K 214,184,185
Mvaa 168,132,36
Dc 1,8,61,0
Mvaa 172,132,52
X 187
Mvaa 160,136,8
X 188
Mvaa 164,136,24
Dc 1,8,61,1024
K 215,186,187
Mvaa 168,136,40
X 189
Mvaa 172,136,56
Ra 132,242,17408
Mvaa 160,140,12
X 190
Mvaa 164,140,28
Dc 1,8,61,2048
K 222,188,189
Mvaa 168,140,44
X 191
Mvaa 172,140,60
Ra 136,242,18432
K 223,190,191
v_smfmac_f32_16x16x64_bf16 a[160:163], a[176:179], v[192:199], v240
Dc 1,8,61,3072
v_cndmask_b32_e64 v160, v241, v160, s[72:73]
v_smfmac_f32_16x16x64_bf16 a[164:167], a[176:179], v[200:207], v240
s_add_u32 m0, s62, 4096
v_cndmask_b32_e64 v161, v241, v161, s[74:75]
v_cndmask_b32_e64 v162, v241, v162, s[76:77]
v_smfmac_f32_16x16x64_bf16 a[168:171], a[176:179], v[208:215], v240
v_cndmask_b32_e64 v163, v241, v163, s[78:79]
v_smfmac_f32_16x16x64_bf16 a[172:175], a[176:179], v[216:223], v240
W 3
X 160
Cvaa 180,144,16,228
X 161
Cvaa 184,144,32,232
Ra 140,242,19456
Dc 243,8,61,0
Cvaa 188,144,48,236
X 162
Mvaa 180,148,20
Ra 144,242,20480
K 192,160,161
Mvaa 184,148,36
X 163
Mvaa 188,148,52
Dc 243,8,61,1024
Ra 148,242,21504
Mvaa 180,152,24
X 164
Mvaa 184,152,40
K 193,162,163
Mvaa 188,152,56
Dc 243,8,61,2048
Ra 152,242,22528
Mvaa 180,156,28
X 165
Mvaa 184,156,44
X 166
Mvaa 188,156,60
W 3
Dc 243,8,61,3072
K 200,164,165
Mvav 32,128,196
s_add_u32 m0, s62, 8192
X 167
Mvav 64,128,204
X 168
Mvav 96,128,212
Ra 156,242,23552
K 201,166,167
Mvav 128,128,220
X 169
Mvav 132,132,220
X 170
Mvav 100,132,212
Dc 244,8,61,0
K 208,168,169
Mvav 68,132,204
X 171
Mvav 36,132,196
Ra 128,242,8192
Ra 132,242,9216
Mvav 40,136,196
Dc 244,8,61,1024
Mvav 72,136,204
X 172
Mvav 104,136,212
K 209,170,171
Mvav 136,136,220
X 173
Mvav 140,140,220
Dc 244,8,61,2048
Ra 136,242,10240
Mvav 108,140,212
X 174
Mvav 76,140,204
K 216,172,173
Mvav 44,140,196
W 3
Dc 244,8,61,3072
Ra 140,242,11264
Mvav 48,144,196
s_add_u32 m0, s62, 12288
X 175
Mvav 80,144,204
v_mov_b32 v194, 0
K 217,174,175
Mvav 112,144,212
v_mov_b32 v195, 0
v_cndmask_b32_e64 v180, v241, v180, s[72:73]
Mvav 144,144,220
v_cndmask_b32_e64 v181, v241, v181, s[74:75]
v_cndmask_b32_e64 v182, v241, v182, s[76:77]
Mvav 148,148,220
Dc 245,8,61,0
v_cndmask_b32_e64 v183, v241, v183, s[78:79]
Mvav 116,148,212
X 180
Mvav 84,148,204
Ra 144,242,12288
Mvav 52,148,196
X 181
Mvav 56,152,196
Dc 245,8,61,1024
Ra 148,242,13312
Mvav 88,152,204
X 182
Mvav 120,152,212
K 202,180,181
Mvav 152,152,220
X 183
Mvav 156,156,220
Dc 245,8,61,2048
Ra 152,242,14336
Mvav 124,156,212
X 184
Mvav 92,156,204
K 203,182,183
Mvav 60,156,196
W 3
Dc 245,8,61,3072
Ra 156,242,15360
Cvaa 168,128,32,232
s_add_u32 s61, s61, s63
X 185
Cvaa 172,128,48,236
s_xor_b32 s62, s62, 0x10000
X 186
Mvaa 168,132,36
Ra 128,242,24576
K 210,184,185
Mvaa 172,132,52
s_add_u32 s64, s64, 1
X 187
Mvaa 168,136,40
X 188
Mvaa 172,136,56
Ra 132,242,25600
K 211,186,187
Mvaa 168,140,44
Ra 136,242,26624
Mvaa 172,140,60
Ra 140,242,27648
X 189
X 190
K 218,188,189
X 191
v_mov_b32 v196, 0
K 219,190,191
W 4
Cvaa 188,144,48,236
Ra 144,242,28672
Mvaa 188,148,52
v_mov_b32 v197, 0
v_mov_b32 v204, 0
Mvaa 188,152,56
Ra 148,242,29696
v_mov_b32 v205, 0
Mvaa 188,156,60
W 2
Ra 152,242,30720
v_cndmask_b32_e64 v168, v241, v168, s[72:73]
Mvav 32,128,192
v_cndmask_b32_e64 v169, v241, v169, s[74:75]
v_cndmask_b32_e64 v170, v241, v170, s[76:77]
Mvav 64,128,200
Ra 156,242,31744
v_cndmask_b32_e64 v171, v241, v171, s[78:79]
Mvav 96,128,208
X 168
Mvav 128,128,216
X 169
Mvav 132,132,216
X 170
Mvav 100,132,208
v_mov_b32 v224, v250
Mvav 68,132,200
K 212,168,169
v_mov_b32 v225, v250
Mvav 36,132,192
X 171
Mvav 40,136,192
v_mov_b32 v226, v250
Mvav 72,136,200
X 172
Mvav 104,136,208
v_mov_b32 v227, v250
K 213,170,171
Mvav 136,136,216
X 173
Mvav 140,140,216
v_mov_b32 v228, v251
v_mov_b32 v229, v251
Mvav 108,140,208
X 174
Mvav 76,140,200
K 220,172,173
v_mov_b32 v230, v251
Mvav 44,140,192
W 0
X 175
Mvav 48,144,192
v_mov_b32 v231, v251
v_mov_b32 v198, 0
Mvav 80,144,200
K 221,174,175
v_mov_b32 v232, v248
)"
R"(Mvav 112,144,208
v_mov_b32 v199, 0
v_mov_b32 v206, 0
Mvav 144,144,216
v_mov_b32 v233, v248
v_mov_b32 v207, 0
Mvav 148,148,216
v_mov_b32 v214, 0
v_mov_b32 v234, v248
Mvav 116,148,208
v_mov_b32 v215, 0
v_cndmask_b32_e64 v188, v241, v188, s[72:73]
Mvav 84,148,200
v_mov_b32 v235, v248
v_cndmask_b32_e64 v189, v241, v189, s[74:75]
Mvav 52,148,192
v_cndmask_b32_e64 v190, v241, v190, s[76:77]
v_mov_b32 v236, v249
Mvav 56,152,192
v_cndmask_b32_e64 v191, v241, v191, s[78:79]
v_mov_b32 v237, v249
Mvav 88,152,200
X 188
Mvav 120,152,208
X 189
Mvav 152,152,216
v_mov_b32 v238, v249
v_mov_b32 v239, v249
Mvav 156,156,216
X 190
Mvav 124,156,208
s_waitcnt vmcnt(0)
s_barrier
v_xor_b32 v242, 0x10000, v242
Mvav 92,156,200
Ra 128,242,40960
K 222,188,189
Mvav 60,156,192
Ra 132,242,41984
Ra 136,242,43008
Ra 140,242,44032
X 191
s_nop 0
K 223,190,191
s_cmp_eq_u32 s64, 15
s_cbranch_scc1 .Lfs_40%=
.Lsp2_35%=:
v_smfmac_f32_16x16x64_bf16 a[160:163], a[176:179], v[192:199], v240
s_add_u32 m0, s62, 0
Ra 144,242,45056
Ra 148,242,46080
Ra 152,242,47104
Ra 156,242,48128
v_smfmac_f32_16x16x64_bf16 a[164:167], a[176:179], v[200:207], v240
v_smfmac_f32_16x16x64_bf16 a[168:171], a[176:179], v[208:215], v240
v_smfmac_f32_16x16x64_bf16 a[172:175], a[176:179], v[216:223], v240
W 4
Dc 1,8,61,0
Cvaa 168,128,96,232
v_mov_b32 v192, 0
G 12,160
Cvaa 172,128,112,236
Dc 1,8,61,1024
Ra 128,242,16384
G 4,161
A 12,12,4
v_cmp_nlt_f32_e64 s[86:87], v12, s90
v_mov_b32 v193, 0
Mvaa 168,132,100
v_rcp_f32 v12, v12
s_or_b64 s[88:89], s[88:89], s[86:87]
v_accvgpr_write_b32 a160, 0
v_mov_b32 v200, 0
v_accvgpr_write_b32 a161, 0
v_accvgpr_write_b32 a162, 0
Mvaa 172,132,116
Dc 1,8,61,2048
v_accvgpr_write_b32 a163, 0
G 13,164
v_mov_b32 v201, 0
G 5,165
A 13,13,5
Mvaa 168,136,104
Ra 132,242,17408
v_cmp_nlt_f32_e64 s[86:87], v13, s90
v_rcp_f32 v13, v13
s_or_b64 s[88:89], s[88:89], s[86:87]
v_accvgpr_write_b32 a164, 0
v_accvgpr_write_b32 a165, 0
Mvaa 172,136,120
Dc 1,8,61,3072
v_accvgpr_write_b32 a166, 0
v_accvgpr_write_b32 a167, 0
U 32,32,12
U 33,33,12
U 34,34,12
Mvaa 168,140,108
Ra 136,242,18432
U 35,35,12
U 36,36,12
U 37,37,12
U 38,38,12
U 39,39,12
Mvaa 172,140,124
s_add_u32 m0, s62, 4096
Ra 140,242,19456
K 32,32,33
K 33,34,35
K 34,36,37
K 35,38,39
W 4
Cvaa 188,144,112,236
s_add_u32 s93, s67, 0
buffer_store_dwordx4 v[32:35], v1, s[8:11], s93 offen offset:0 sc1
Ra 144,242,20480
U 40,40,12
U 41,41,12
U 42,42,12
U 43,43,12
Mvaa 188,148,116
Ra 148,242,21504
U 44,44,12
U 45,45,12
U 46,46,12
U 47,47,12
K 40,40,41
Mvaa 188,152,120
v_cndmask_b32_e64 v168, v241, v168, s[72:73]
v_cndmask_b32_e64 v169, v241, v169, s[74:75]
v_cndmask_b32_e64 v170, v241, v170, s[76:77]
v_cndmask_b32_e64 v171, v241, v171, s[78:79]
X 168
Mvaa 188,156,124
W 2
Dc 243,8,61,0
X 169
K 41,42,43
K 42,44,45
K 43,46,47
Mvav 96,128,212
Ra 152,242,22528
Ra 156,242,23552
X 170
K 208,168,169
Mvav 128,128,220
buffer_store_dwordx4 v[40:43], v1, s[8:11], s93 offen offset:1024 sc1
U 48,48,12
U 49,49,12
U 50,50,12
U 51,51,12
U 52,52,12
Mvav 132,132,220
U 53,53,12
U 54,54,12
U 55,55,12
K 48,48,49
K 49,50,51
K 50,52,53
Mvav 100,132,212
Dc 243,8,61,1024
Ra 128,242,32768
Ra 132,242,33792
X 171
K 51,54,55
Mvav 104,136,212
X 172
K 209,170,171
X 173
Mvav 136,136,220
buffer_store_dwordx4 v[48:51], v1, s[8:11], s93 offen offset:2048 sc1
U 56,56,12
U 57,57,12
U 58,58,12
U 59,59,12
U 60,60,12
Mvav 140,140,220
U 61,61,12
U 62,62,12
U 63,63,12
K 56,56,57
K 57,58,59
K 58,60,61
Mvav 108,140,212
W 2
Dc 243,8,61,2048
Ra 136,242,34816
Ra 140,242,35840
K 59,62,63
X 174
Mvav 112,144,212
K 216,172,173
X 175
Mvav 144,144,220
buffer_store_dwordx4 v[56:59], v1, s[8:11], s93 offen offset:3072 sc1
Ra 144,242,36864
G 14,168
G 6,169
A 14,14,6
v_cmp_nlt_f32_e64 s[86:87], v14, s90
Mvav 148,148,220
v_rcp_f32 v14, v14
s_or_b64 s[88:89], s[88:89], s[86:87]
v_accvgpr_write_b32 a168, 0
v_accvgpr_write_b32 a169, 0
v_accvgpr_write_b32 a170, 0
v_accvgpr_write_b32 a171, 0
Mvav 116,148,212
Dc 243,8,61,3072
s_add_u32 m0, s62, 8192
G 15,172
G 7,173
A 15,15,7
v_cmp_nlt_f32_e64 s[86:87], v15, s90
v_mov_b32 v194, 0
Mvav 120,152,212
v_rcp_f32 v15, v15
s_or_b64 s[88:89], s[88:89], s[86:87]
v_accvgpr_write_b32 a172, 0
v_accvgpr_write_b32 a173, 0
v_accvgpr_write_b32 a174, 0
v_accvgpr_write_b32 a175, 0
Mvav 152,152,220
Dc 244,8,61,0
U 64,64,13
U 65,65,13
U 66,66,13
U 67,67,13
U 68,68,13
Mvav 156,156,220
U 69,69,13
U 70,70,13
U 71,71,13
K 64,64,65
K 65,66,67
K 66,68,69
Mvav 124,156,212
W 1
Dc 244,8,61,1024
Ra 148,242,37888
K 67,70,71
s_add_u32 s93, s67, 131072
K 217,174,175
v_mov_b32 v195, 0
Cvaa 160,128,64,224
Ra 152,242,38912
Ra 156,242,39936
v_mov_b32 v202, 0
v_mov_b32 v203, 0
v_mov_b32 v210, 0
v_mov_b32 v211, 0
Cvaa 164,128,80,228
buffer_store_dwordx4 v[64:67], v1, s[8:11], s93 offen offset:0 sc1
U 72,72,13
U 73,73,13
U 74,74,13
U 75,75,13
U 76,76,13
Cvaa 168,128,96,232
U 77,77,13
U 78,78,13
U 79,79,13
K 72,72,73
K 73,74,75
K 74,76,77
Cvaa 172,128,112,236
Dc 244,8,61,2048
K 75,78,79
v_cndmask_b32_e64 v188, v241, v188, s[72:73]
v_cndmask_b32_e64 v189, v241, v189, s[74:75]
v_cndmask_b32_e64 v190, v241, v190, s[76:77]
v_cndmask_b32_e64 v191, v241, v191, s[78:79]
Mvaa 160,132,68
Ra 128,242,49152
X 188
X 189
Mvaa 164,132,84
buffer_store_dwordx4 v[72:75], v1, s[8:11], s93 offen offset:1024 sc1
U 80,80,13
U 81,81,13
U 82,82,13
U 83,83,13
U 84,84,13
Mvaa 168,132,100
U 85,85,13
U 86,86,13
U 87,87,13
K 80,80,81
K 81,82,83
K 82,84,85
Mvaa 172,132,116
Dc 244,8,61,3072
s_add_u32 m0, s62, 12288
Ra 132,242,50176
K 83,86,87
X 190
Mvaa 160,136,72
K 218,188,189
X 191
Mvaa 164,136,88
buffer_store_dwordx4 v[80:83], v1, s[8:11], s93 offen offset:2048 sc1
U 88,88,13
U 89,89,13
U 90,90,13
U 91,91,13
U 92,92,13
Mvaa 168,136,104
U 93,93,13
U 94,94,13
U 95,95,13
K 88,88,89
K 89,90,91
K 90,92,93
Mvaa 172,136,120
K 91,94,95
buffer_store_dwordx4 v[88:91], v1, s[8:11], s93 offen offset:3072 sc1
U 96,96,14
U 97,97,14
U 98,98,14
U 99,99,14
Mvaa 160,140,76
U 100,100,14
U 101,101,14
U 102,102,14
U 103,103,14
K 96,96,97
K 97,98,99
Mvaa 164,140,92
Dc 245,8,61,0
Ra 136,242,51200
K 98,100,101
K 99,102,103
s_add_u32 s93, s67, 262144
Mvaa 168,140,108
Mvaa 172,140,124
W 3
Dc 245,8,61,1024
Ra 140,242,52224
v_cndmask_b32_e64 v160, v241, v160, s[72:73]
K 219,190,191
v_cndmask_b32_e64 v161, v241, v161, s[74:75]
v_cndmask_b32_e64 v162, v241, v162, s[76:77]
Cvaa 180,144,80,228
v_cndmask_b32_e64 v163, v241, v163, s[78:79]
X 160
X 161
Cvaa 184,144,96,232
buffer_store_dwordx4 v[96:99], v1, s[8:11], s93 offen offset:0 sc1
U 104,104,14
U 105,105,14
U 106,106,14
U 107,107,14
U 108,108,14
Cvaa 188,144,112,236
U 109,109,14
U 110,110,14
U 111,111,14
K 104,104,105
K 105,106,107
K 106,108,109
Mvaa 180,148,84
Dc 245,8,61,2048
Ra 144,242,53248
K 107,110,111
X 162
K 196,160,161
Mvaa 184,148,100
X 163
Mvaa 188,148,116
buffer_store_dwordx4 v[104:107], v1, s[8:11], s93 offen offset:1024 sc1
U 112,112,14
U 113,113,14
U 114,114,14
U 115,115,14
U 116,116,14
Mvaa 180,152,88
U 117,117,14
U 118,118,14
U 119,119,14
K 112,112,113
K 113,114,115
K 114,116,117
Mvaa 184,152,104
Dc 245,8,61,3072
s_add_u32 s61, s61, s63
s_xor_b32 s62, s62, 0x10000
s_add_u32 s64, s64, 1
K 115,118,119
X 164
Mvaa 188,152,120
Ra 148,242,54272
Ra 152,242,55296
K 197,162,163
X 165
Mvaa 180,156,92
buffer_store_dwordx4 v[112:115], v1, s[8:11], s93 offen offset:2048 sc1
U 120,120,14
U 121,121,14
U 122,122,14
U 123,123,14
U 124,124,14
)"
R"(Mvaa 184,156,108
U 125,125,14
U 126,126,14
U 127,127,14
K 120,120,121
K 121,122,123
K 122,124,125
Mvaa 188,156,124
W 3
K 123,126,127
buffer_store_dwordx4 v[120:123], v1, s[8:11], s93 offen offset:3072 sc1
U 128,128,15
U 129,129,15
U 130,130,15
U 131,131,15
Zvav 32,128,192
U 132,132,15
U 133,133,15
U 134,134,15
U 135,135,15
K 128,128,129
K 129,130,131
Zvav 64,128,200
K 130,132,133
K 131,134,135
s_add_u32 s93, s67, 393216
buffer_store_dwordx4 v[128:131], v1, s[8:11], s93 offen offset:0 sc1
U 136,136,15
U 137,137,15
U 138,138,15
Zvav 96,128,208
U 139,139,15
U 140,140,15
U 141,141,15
U 142,142,15
U 143,143,15
K 136,136,137
Zvav 128,128,216
K 137,138,139
K 138,140,141
K 139,142,143
buffer_store_dwordx4 v[136:139], v1, s[8:11], s93 offen offset:1024 sc1
U 144,144,15
U 145,145,15
Zvav 132,132,216
U 146,146,15
U 147,147,15
U 148,148,15
U 149,149,15
U 150,150,15
U 151,151,15
Zvav 100,132,208
K 144,144,145
K 145,146,147
K 146,148,149
K 147,150,151
buffer_store_dwordx4 v[144:147], v1, s[8:11], s93 offen offset:2048 sc1
U 152,152,15
Zvav 68,132,200
U 153,153,15
U 154,154,15
U 155,155,15
U 156,156,15
U 157,157,15
U 158,158,15
Zvav 36,132,192
U 159,159,15
K 152,152,153
K 153,154,155
K 154,156,157
K 155,158,159
buffer_store_dwordx4 v[152:155], v1, s[8:11], s93 offen offset:3072 sc1
Zvav 40,136,192
Ra 156,242,56320
X 166
K 204,164,165
X 167
Zvav 72,136,200
X 168
K 205,166,167
X 169
Zvav 104,136,208
X 170
K 212,168,169
X 171
Zvav 136,136,216
X 172
K 213,170,171
X 173
Zvav 140,140,216
Zvav 108,140,208
X 174
Zvav 76,140,200
s_waitcnt vmcnt(6) lgkmcnt(0)
s_barrier
v_xor_b32 v242, 0x10000, v242
Zvav 44,140,192
Ra 128,242,40960
K 220,172,173
X 175
Zvav 48,144,192
Zvav 80,144,200
v_mov_b32 v198, 0
Zvav 112,144,208
Ra 132,242,41984
K 221,174,175
Zvav 144,144,216
v_mov_b32 v199, 0
v_cndmask_b32_e64 v180, v241, v180, s[72:73]
Zvav 148,148,216
v_cndmask_b32_e64 v181, v241, v181, s[74:75]
Zvav 116,148,208
Ra 136,242,43008
v_cndmask_b32_e64 v182, v241, v182, s[76:77]
Zvav 84,148,200
v_cndmask_b32_e64 v183, v241, v183, s[78:79]
Zvav 52,148,192
X 180
Zvav 56,152,192
Ra 140,242,44032
X 181
Zvav 88,152,200
Zvav 120,152,208
X 182
Zvav 152,152,216
K 206,180,181
Zvav 156,156,216
X 183
Zvav 124,156,208
Zvav 92,156,200
s_waitcnt vmcnt(0)
s_and_b64 s[86:87], s[88:89], s[24:25]
s_cbranch_scc1 .LL_47%=
v_mov_b64 v[28:29], s[12:13]
s_mov_b64 exec, 1
buffer_store_dwordx2 v[28:29], off, s[8:11], s69 sc1
s_mov_b64 exec, -1
.LL_47%=:
K 207,182,183
Zvav 60,156,192
s_cmp_eq_u32 s64, 15
s_cbranch_scc1 .Lf2_39%=
.Ll2_36%=:
W 0
Cvaa 160,128,64,224
Ra 144,242,45056
Ra 148,242,46080
Cvaa 164,128,80,228
s_add_u32 m0, s62, 0
Ra 152,242,47104
Cvaa 168,128,96,232
X 184
Cvaa 172,128,112,236
X 185
Mvaa 160,132,68
X 186
Mvaa 164,132,84
Ra 156,242,48128
K 214,184,185
Mvaa 168,132,100
Dc 1,8,61,0
Mvaa 172,132,116
X 187
Mvaa 160,136,72
X 188
Mvaa 164,136,88
Ra 128,242,57344
K 215,186,187
Mvaa 168,136,104
Dc 1,8,61,1024
Mvaa 172,136,120
X 189
Mvaa 160,140,76
X 190
Mvaa 164,140,92
Ra 132,242,58368
K 222,188,189
Mvaa 168,140,108
Dc 1,8,61,2048
Mvaa 172,140,124
X 191
v_smfmac_f32_16x16x64_bf16 a[160:163], a[176:179], v[192:199], v240
Ra 136,242,59392
K 223,190,191
v_smfmac_f32_16x16x64_bf16 a[164:167], a[176:179], v[200:207], v240
X 160
v_smfmac_f32_16x16x64_bf16 a[168:171], a[176:179], v[208:215], v240
Dc 1,8,61,3072
v_smfmac_f32_16x16x64_bf16 a[172:175], a[176:179], v[216:223], v240
W 3
X 161
Cvaa 176,144,64,224
s_add_u32 m0, s62, 4096
X 162
Cvaa 180,144,80,228
Ra 140,242,60416
K 192,160,161
Cvaa 184,144,96,232
X 163
Cvaa 188,144,112,236
Dc 243,8,61,0
Mvaa 176,148,68
X 164
Mvaa 180,148,84
Ra 144,242,61440
K 193,162,163
Mvaa 184,148,100
X 165
Mvaa 188,148,116
Dc 243,8,61,1024
Mvaa 176,152,72
X 166
Mvaa 180,152,88
Ra 148,242,62464
K 200,164,165
Mvaa 184,152,104
X 167
Mvaa 188,152,120
Dc 243,8,61,2048
Mvaa 176,156,76
X 168
Mvaa 180,156,92
Ra 152,242,63488
K 201,166,167
Mvaa 184,156,108
X 169
Mvaa 188,156,124
W 3
Dc 243,8,61,3072
Mvav 32,128,196
s_add_u32 m0, s62, 8192
X 170
Mvav 64,128,204
Ra 156,242,64512
K 208,168,169
Mvav 96,128,212
X 171
Mvav 128,128,220
Dc 244,8,61,0
Mvav 132,132,220
X 172
Mvav 100,132,212
Ra 128,242,32768
K 209,170,171
Mvav 68,132,204
X 173
Mvav 36,132,196
Dc 244,8,61,1024
Mvav 40,136,196
X 174
Mvav 72,136,204
Ra 132,242,33792
K 216,172,173
Mvav 104,136,212
X 175
Mvav 136,136,220
Dc 244,8,61,2048
Mvav 140,140,220
X 176
Mvav 108,140,212
Ra 136,242,34816
K 217,174,175
Mvav 76,140,204
X 177
Mvav 44,140,196
W 3
Dc 244,8,61,3072
Mvav 48,144,196
X 178
Mvav 80,144,204
s_add_u32 m0, s62, 12288
Ra 140,242,35840
Mvav 112,144,212
K 194,176,177
Mvav 144,144,220
X 179
Mvav 148,148,220
X 180
Mvav 116,148,212
Dc 245,8,61,0
Ra 144,242,36864
Mvav 84,148,204
K 195,178,179
Mvav 52,148,196
X 181
Mvav 56,152,196
X 182
Mvav 88,152,204
Dc 245,8,61,1024
Ra 148,242,37888
Mvav 120,152,212
K 202,180,181
Mvav 152,152,220
X 183
Mvav 156,156,220
X 184
Mvav 124,156,212
Dc 245,8,61,2048
Ra 152,242,38912
Mvav 92,156,204
K 203,182,183
Mvav 60,156,196
W 3
X 185
Cvaa 160,128,64,224
X 186
Cvaa 164,128,80,228
Dc 245,8,61,3072
Ra 156,242,39936
Cvaa 168,128,96,232
s_add_u32 s61, s61, s63
K 210,184,185
Cvaa 172,128,112,236
X 187
Mvaa 160,132,68
X 188
Mvaa 164,132,84
Ra 128,242,49152
Mvaa 168,132,100
s_xor_b32 s62, s62, 0x10000
K 211,186,187
Mvaa 172,132,116
X 189
Mvaa 160,136,72
X 190
Mvaa 164,136,88
s_add_u32 s64, s64, 1
Ra 132,242,50176
Mvaa 168,136,104
K 218,188,189
Mvaa 172,136,120
X 191
Mvaa 160,140,76
Ra 136,242,51200
Mvaa 164,140,92
Mvaa 168,140,108
Mvaa 172,140,124
W 3
Cvaa 176,144,64,224
Ra 140,242,52224
Cvaa 180,144,80,228
X 160
Cvaa 184,144,96,232
K 219,190,191
Cvaa 188,144,112,236
X 161
Mvaa 176,148,68
X 162
Mvaa 180,148,84
Ra 144,242,53248
K 196,160,161
Mvaa 184,148,100
X 163
Mvaa 188,148,116
X 164
Mvaa 176,152,72
Ra 148,242,54272
K 197,162,163
Mvaa 180,152,88
X 165
Mvaa 184,152,104
X 166
Mvaa 188,152,120
K 204,164,165
Mvaa 176,156,76
X 167
Mvaa 180,156,92
Ra 152,242,55296
Mvaa 184,156,108
X 168
Mvaa 188,156,124
W 3
K 205,166,167
Mvav 32,128,192
X 169
Mvav 64,128,200
Ra 156,242,56320
Mvav 96,128,208
X 170
Mvav 128,128,216
K 212,168,169
Mvav 132,132,216
X 171
Mvav 100,132,208
X 172
Mvav 68,132,200
Mvav 36,132,192
K 213,170,171
Mvav 40,136,192
X 173
Mvav 72,136,200
X 174
Mvav 104,136,208
Mvav 136,136,216
K 220,172,173
Mvav 140,140,216
X 175
Mvav 108,140,208
X 176
Mvav 76,140,200
)"
R"(s_waitcnt vmcnt(0) lgkmcnt(0)
s_barrier
v_xor_b32 v242, 0x10000, v242
Mvav 44,140,192
Ra 128,242,40960
K 221,174,175
Mvav 48,144,192
X 177
Mvav 80,144,200
X 178
Mvav 112,144,208
Ra 132,242,41984
Mvav 144,144,216
K 198,176,177
Mvav 148,148,216
X 179
Mvav 116,148,208
X 180
Mvav 84,148,200
Ra 136,242,43008
Mvav 52,148,192
K 199,178,179
Mvav 56,152,192
X 181
Mvav 88,152,200
Ra 140,242,44032
Mvav 120,152,208
X 182
Mvav 152,152,216
K 206,180,181
Mvav 156,156,216
X 183
Mvav 124,156,208
Mvav 92,156,200
K 207,182,183
Mvav 60,156,192
s_cmp_lg_u32 s64, 15
s_cbranch_scc1 .Ll2_36%=
.Lf2_39%=:
W 0
Cvaa 160,128,64,224
s_lshl_b32 s92, s58, 2
s_add_u32 s91, s92, s51
s_add_u32 s95, s91, 3
Cvaa 164,128,80,228
s_and_b32 s95, s95, 15
s_mul_i32 s95, s95, 40960
s_mul_i32 s71, s51, 10240
Cvaa 168,128,96,232
s_add_u32 s71, s71, 0
s_add_u32 m0, s71, s48
s_lshl2_add_u32 s94, s48, s95
Cvaa 172,128,112,236
Ra 144,242,45056
Ra 148,242,46080
Mvaa 160,132,68
Ra 152,242,47104
D 16,40,94,0
Mvaa 164,132,84
s_add_u32 s95, s91, 5
s_and_b32 s95, s95, 15
s_mul_i32 s95, s95, 40960
Mvaa 168,132,100
s_mul_i32 s71, s51, 10240
s_add_u32 s71, s71, 20480
Ra 156,242,48128
Mvaa 172,132,116
X 184
Mvaa 160,136,72
X 185
Mvaa 164,136,88
X 186
Mvaa 168,136,104
D 17,40,94,1024
K 214,184,185
Mvaa 172,136,120
X 187
Mvaa 160,140,76
X 188
Mvaa 164,140,92
Ra 128,242,57344
K 215,186,187
Mvaa 168,140,108
X 189
Mvaa 172,140,124
X 190
v_smfmac_f32_16x16x64_bf16 a[160:163], a[176:179], v[192:199], v240
D 18,40,94,2048
K 222,188,189
v_smfmac_f32_16x16x64_bf16 a[164:167], a[176:179], v[200:207], v240
X 191
s_nop 0
K 223,190,191
v_smfmac_f32_16x16x64_bf16 a[168:171], a[176:179], v[208:215], v240
Ra 132,242,58368
Ra 136,242,59392
v_smfmac_f32_16x16x64_bf16 a[172:175], a[176:179], v[216:223], v240
W 3
D 19,40,94,3072
Cvaa 176,144,64,224
s_add_u32 m0, m0, 4096
X 160
Cvaa 180,144,80,228
X 161
Cvaa 184,144,96,232
X 162
Cvaa 188,144,112,236
buffer_load_dwordx4 v20, s[40:43], s94 offen lds
v_bfe_u32 v28, v1, 4, 2
Mvaa 176,148,68
v_lshlrev_b32 v28, 3, v28
K 192,160,161
Mvaa 180,148,84
X 163
Mvaa 184,148,100
X 164
Mvaa 188,148,116
Ra 140,242,60416
K 193,162,163
Mvaa 176,152,72
buffer_load_dwordx2 v[28:29], v28, s[8:11], s54 offen sc1
Ra 144,242,61440
Mvaa 180,152,88
s_add_u32 m0, s71, s48
s_lshl2_add_u32 s94, s48, s95
Ra 148,242,62464
Mvaa 184,152,104
X 165
Mvaa 188,152,120
X 166
Mvaa 176,156,76
D 16,40,94,0
K 200,164,165
Mvaa 180,156,92
X 167
Mvaa 184,156,108
X 172
Mvaa 188,156,124
W 2
D 17,40,94,1024
K 201,166,167
Mvav 32,128,196
X 173
Mvav 64,128,204
X 174
Mvav 96,128,212
D 18,40,94,2048
K 216,172,173
Mvav 128,128,220
X 175
Mvav 132,132,220
s_mov_b32 s18, 40960
X 168
Mvav 100,132,212
Ra 152,242,63488
K 217,174,175
Mvav 68,132,204
s_mov_b32 s19, 73728
X 169
Mvav 36,132,196
s_bitcmp1_b32 s14, 0
s_cselect_b32 s18, 49152, s18
s_cselect_b32 s19, 98304, s19
X 170
Mvav 40,136,196
s_cmp_lt_u32 s14, 2
s_cselect_b32 s20, s18, s19
s_cselect_b32 s21, s19, s18
D 19,40,94,3072
Ra 156,242,64512
Mvav 72,136,204
s_add_u32 m0, m0, 4096
Ra 128,242,49152
K 208,168,169
Mvav 104,136,212
X 171
Mvav 136,136,220
X 176
Mvav 140,140,220
buffer_load_dwordx4 v20, s[40:43], s94 offen lds
K 209,170,171
Mvav 108,140,212
s_waitcnt vmcnt(5)
v_cmp_eq_u64 vcc, s[12:13], v[28:29]
s_cmp_eq_u64 vcc, -1
s_cselect_b32 s46, 79691776, 0
s_add_u32 s81, s58, 0
s_and_b32 s81, s81, 3
Mvav 76,140,204
s_lshl_b32 s81, s81, 12
s_add_u32 s81, s81, s49
v_add_u32 v2, s20, v1
Mvav 44,140,196
W 1
X 177
Mvav 48,144,196
X 178
Mvav 80,144,204
buffer_load_dwordx4 a[184:187], v1, s[44:47], s81 offen offset:0 sc1
K 194,176,177
Mvav 112,144,212
s_add_u32 s82, s81, 131072
X 179
Mvav 144,144,220
X 180
Mvav 148,148,220
v_add_u32 v3, s21, v1
K 195,178,179
Mvav 116,148,212
s_lshl_b32 s18, s14, 9
buffer_load_dwordx4 a[200:203], v1, s[44:47], s82 offen offset:0 sc1
Ra 132,242,50176
Mvav 84,148,204
X 181
Mvav 52,148,196
v_lshrrev_b32 v232, 1, v1
v_add_u32 v232, s18, v232
Mvav 56,152,196
X 182
Mvav 88,152,204
K 202,180,181
v_bfe_u32 v12, v1, 6, 2
Mvav 120,152,212
buffer_load_dwordx4 a[188:191], v1, s[44:47], s81 offen offset:1024 sc1
v_lshrrev_b32 v13, 8, v1
Mvav 152,152,220
X 183
Mvav 156,156,220
X 184
Mvav 124,156,212
Ra 136,242,51200
v_bfe_u32 v14, v1, 4, 1
Mvav 92,156,204
Ra 140,242,52224
K 203,182,183
Mvav 60,156,196
buffer_load_dwordx4 a[204:207], v1, s[44:47], s82 offen offset:1024 sc1
v_lshlrev_b32 v14, 4, v14
Maav 160,180,192
X 185
X 186
K 210,184,185
X 187
X 188
K 211,186,187
Maav 164,180,200
X 189
X 190
K 218,188,189
X 191
s_nop 0
K 219,190,191
Maav 168,180,208
v_mov_b32 v15, 0x3f80
v_lshlrev_b32 v15, v14, v15
Maav 172,180,216
W 0
buffer_load_dwordx4 a[192:195], v1, s[44:47], s81 offen offset:2048 sc1
v_bfe_u32 v14, v1, 5, 1
Mvav 32,128,192
v_xor_b32 v27, v12, v13
v_lshl_or_b32 v27, v27, 1, v14
Mvav 64,128,200
Ra 144,242,53248
v_cmp_eq_u32_e64 s[86:87], 0, v27
Mvav 96,128,208
buffer_load_dwordx4 a[208:211], v1, s[44:47], s82 offen offset:2048 sc1
v_mov_b32 v226, 0
Mvav 128,128,216
Ra 148,242,54272
v_cndmask_b32_e64 v224, 0, v15, s[86:87]
Mvav 132,132,216
v_mov_b32 v228, 0
v_mov_b32 v230, v224
Mvav 100,132,208
buffer_load_dwordx4 a[196:199], v1, s[44:47], s81 offen offset:3072 sc1
v_cmp_eq_u32_e64 s[86:87], 1, v27
Mvav 68,132,200
Ra 152,242,55296
v_mov_b32 v227, 0
Mvav 36,132,192
v_cndmask_b32_e64 v225, 0, v15, s[86:87]
v_mov_b32 v229, 0
Mvav 40,136,192
buffer_load_dwordx4 a[212:215], v1, s[44:47], s82 offen offset:3072 sc1
v_mov_b32 v231, v225
Mvav 72,136,200
s_add_u32 s81, s58, 1
s_and_b32 s81, s81, 3
s_lshl_b32 s81, s81, 12
Mvav 104,136,208
s_add_u32 s81, s81, s49
Ra 156,242,56320
G 4,160
Mvav 136,136,216
buffer_load_dwordx4 a[216:219], v1, s[44:47], s81 offen offset:0 sc1
G 8,161
Mvav 140,140,216
s_add_u32 s82, s81, 131072
G 5,164
G 9,165
Mvav 108,140,208
G 6,168
G 10,169
Mvav 76,140,200
buffer_load_dwordx4 a[232:235], v1, s[44:47], s82 offen offset:0 sc1
G 7,172
Mvav 44,140,192
W 0
G 11,173
A 4,4,8
Mvav 48,144,192
A 5,5,9
A 6,6,10
Mvav 80,144,200
buffer_load_dwordx4 a[220:223], v1, s[44:47], s81 offen offset:1024 sc1
A 7,7,11
Mvav 112,144,208
ds_write_b64 v232, v[6:7] offset:57344
A 8,4,5
Mvav 144,144,216
A 9,6,7
A 8,8,9
Mvav 148,148,216
buffer_load_dwordx4 a[236:239], v1, s[44:47], s82 offen offset:1024 sc1
v_cmp_ngt_f32_e64 s[86:87], s90, v8
Mvav 116,148,208
s_lshl_b32 s19, s14, 2
v_mov_b32 v9, s19
Mvav 84,148,200
s_or_b64 s[88:89], s[88:89], s[86:87]
s_and_b64 s[88:89], s[88:89], s[24:25]
s_cselect_b32 s18, 1, 0
v_mov_b32 v8, s18
Mvav 52,148,192
ds_write_b32 v9, v8 offset:59392
v_xor_b32 v10, 1024, v232
Mvav 56,152,192
v_bfe_u32 v11, v1, 2, 4
K 96,96,97
Mvav 88,152,200
K 97,98,99
K 98,100,101
Mvav 120,152,208
buffer_load_dwordx4 a[224:227], v1, s[44:47], s81 offen offset:2048 sc1
K 99,102,103
Mvav 152,152,216
ds_write_b128 v2, v[96:99] offset:0
K 128,128,129
Mvav 156,156,216
K 129,130,131
K 130,132,133
Mvav 124,156,208
buffer_load_dwordx4 a[240:243], v1, s[44:47], s82 offen offset:2048 sc1
K 131,134,135
Mvav 92,156,200
ds_write_b128 v2, v[128:131] offset:4096
K 104,104,105
Mvav 60,156,192
K 105,106,107
K 106,108,109
K 107,110,111
ds_write_b128 v2, v[104:107] offset:1024
K 136,136,137
K 137,138,139
K 138,140,141
K 139,142,143
ds_write_b128 v2, v[136:139] offset:5120
buffer_load_dwordx4 a[228:231], v1, s[44:47], s81 offen offset:3072 sc1
K 112,112,113
K 113,114,115
K 114,116,117
K 115,118,119
buffer_load_dwordx4 a[244:247], v1, s[44:47], s82 offen offset:3072 sc1
ds_write_b128 v2, v[112:115] offset:2048
K 144,144,145
K 145,146,147
K 146,148,149
K 147,150,151
ds_write_b128 v2, v[144:147] offset:6144
K 120,120,121
K 121,122,123
K 122,124,125
K 123,126,127
ds_write_b128 v2, v[120:123] offset:3072
K 152,152,153
K 153,154,155
K 154,156,157
K 155,158,159
ds_write_b128 v2, v[152:155] offset:7168
s_branch .Lend_41%=
.Lfs_40%=:
v_smfmac_f32_16x16x64_bf16 a[160:163], a[176:179], v[192:199], v240
s_lshl_b32 s92, s58, 2
s_add_u32 s91, s92, s51
s_add_u32 s95, s91, 3
s_and_b32 s95, s95, 15
s_mul_i32 s95, s95, 40960
s_mul_i32 s71, s51, 10240
s_add_u32 s71, s71, 0
)"
R"(v_smfmac_f32_16x16x64_bf16 a[164:167], a[176:179], v[200:207], v240
s_add_u32 m0, s71, s48
s_lshl2_add_u32 s94, s48, s95
Ra 144,242,45056
Ra 148,242,46080
Ra 152,242,47104
Ra 156,242,48128
D 16,40,94,0
v_smfmac_f32_16x16x64_bf16 a[168:171], a[176:179], v[208:215], v240
s_add_u32 s95, s91, 5
s_and_b32 s95, s95, 15
s_mul_i32 s95, s95, 40960
s_mul_i32 s71, s51, 10240
s_add_u32 s71, s71, 20480
v_smfmac_f32_16x16x64_bf16 a[172:175], a[176:179], v[216:223], v240
W 4
Cvaa 168,128,96,232
D 17,40,94,1024
v_mov_b32 v192, 0
G 12,160
Cvaa 172,128,112,236
G 4,161
A 12,12,4
v_cmp_nlt_f32_e64 s[86:87], v12, s90
v_rcp_f32 v12, v12
s_or_b64 s[88:89], s[88:89], s[86:87]
v_accvgpr_write_b32 a160, 0
Mvaa 168,132,100
Ra 128,242,16384
v_accvgpr_write_b32 a161, 0
v_accvgpr_write_b32 a162, 0
v_accvgpr_write_b32 a163, 0
G 13,164
G 5,165
Mvaa 172,132,116
D 18,40,94,2048
v_mov_b32 v193, 0
A 13,13,5
v_cmp_nlt_f32_e64 s[86:87], v13, s90
v_rcp_f32 v13, v13
s_or_b64 s[88:89], s[88:89], s[86:87]
Mvaa 168,136,104
v_accvgpr_write_b32 a164, 0
v_accvgpr_write_b32 a165, 0
v_accvgpr_write_b32 a166, 0
v_accvgpr_write_b32 a167, 0
U 32,32,12
U 33,33,12
Mvaa 172,136,120
U 34,34,12
v_mov_b32 v200, 0
U 35,35,12
U 36,36,12
U 37,37,12
U 38,38,12
Mvaa 168,140,108
Ra 132,242,17408
Ra 136,242,18432
U 39,39,12
K 32,32,33
K 33,34,35
K 34,36,37
Mvaa 172,140,124
Ra 140,242,19456
K 35,38,39
s_add_u32 s93, s67, 0
buffer_store_dwordx4 v[32:35], v1, s[8:11], s93 offen offset:0 sc1
U 40,40,12
v_mov_b32 v201, 0
W 4
Cvaa 188,144,112,236
U 41,41,12
U 42,42,12
U 43,43,12
U 44,44,12
U 45,45,12
U 46,46,12
Mvaa 188,148,116
D 19,40,94,3072
s_add_u32 m0, m0, 4096
U 47,47,12
K 40,40,41
K 41,42,43
K 42,44,45
K 43,46,47
Mvaa 188,152,120
Ra 144,242,20480
Ra 148,242,21504
v_cndmask_b32_e64 v168, v241, v168, s[72:73]
v_cndmask_b32_e64 v169, v241, v169, s[74:75]
v_cndmask_b32_e64 v170, v241, v170, s[76:77]
v_cndmask_b32_e64 v171, v241, v171, s[78:79]
Mvaa 188,156,124
W 2
buffer_store_dwordx4 v[40:43], v1, s[8:11], s93 offen offset:1024 sc1
U 48,48,12
U 49,49,12
U 50,50,12
U 51,51,12
U 52,52,12
Mvav 96,128,212
U 53,53,12
U 54,54,12
U 55,55,12
K 48,48,49
K 49,50,51
K 50,52,53
Mvav 128,128,220
K 51,54,55
buffer_store_dwordx4 v[48:51], v1, s[8:11], s93 offen offset:2048 sc1
U 56,56,12
U 57,57,12
U 58,58,12
U 59,59,12
Mvav 132,132,220
Ra 152,242,22528
Ra 156,242,23552
U 60,60,12
U 61,61,12
U 62,62,12
U 63,63,12
Mvav 100,132,212
buffer_load_dwordx4 v20, s[40:43], s94 offen lds
v_bfe_u32 v28, v1, 4, 2
v_lshlrev_b32 v28, 3, v28
K 56,56,57
K 57,58,59
K 58,60,61
Mvav 104,136,212
Ra 128,242,49152
K 59,62,63
X 168
X 169
Mvav 136,136,220
buffer_store_dwordx4 v[56:59], v1, s[8:11], s93 offen offset:3072 sc1
Ra 132,242,50176
G 14,168
G 6,169
A 14,14,6
v_cmp_nlt_f32_e64 s[86:87], v14, s90
Mvav 140,140,220
v_rcp_f32 v14, v14
s_or_b64 s[88:89], s[88:89], s[86:87]
v_accvgpr_write_b32 a168, 0
v_accvgpr_write_b32 a169, 0
v_accvgpr_write_b32 a170, 0
v_accvgpr_write_b32 a171, 0
Mvav 108,140,212
W 2
buffer_load_dwordx2 v[28:29], v28, s[8:11], s54 offen sc1
s_add_u32 m0, s71, s48
s_lshl2_add_u32 s94, s48, s95
G 15,172
G 7,173
A 15,15,7
Mvav 112,144,212
v_cmp_nlt_f32_e64 s[86:87], v15, s90
v_rcp_f32 v15, v15
s_or_b64 s[88:89], s[88:89], s[86:87]
v_accvgpr_write_b32 a172, 0
v_accvgpr_write_b32 a173, 0
v_accvgpr_write_b32 a174, 0
Mvav 144,144,220
v_accvgpr_write_b32 a175, 0
U 64,64,13
U 65,65,13
U 66,66,13
U 67,67,13
U 68,68,13
Mvav 148,148,220
U 69,69,13
U 70,70,13
U 71,71,13
K 64,64,65
K 65,66,67
K 66,68,69
Mvav 116,148,212
D 16,40,94,0
Ra 136,242,51200
K 67,70,71
s_add_u32 s93, s67, 131072
X 170
K 208,168,169
Mvav 120,152,212
Ra 140,242,52224
Ra 144,242,53248
X 171
X 172
Mvav 152,152,220
buffer_store_dwordx4 v[64:67], v1, s[8:11], s93 offen offset:0 sc1
U 72,72,13
U 73,73,13
U 74,74,13
U 75,75,13
U 76,76,13
Mvav 156,156,220
K 209,170,171
X 173
X 174
K 216,172,173
X 175
v_mov_b32 v194, 0
K 217,174,175
v_mov_b32 v195, 0
v_mov_b32 v202, 0
v_mov_b32 v203, 0
v_mov_b32 v210, 0
v_mov_b32 v211, 0
Mvav 124,156,212
v_cndmask_b32_e64 v188, v241, v188, s[72:73]
v_cndmask_b32_e64 v189, v241, v189, s[74:75]
v_cndmask_b32_e64 v190, v241, v190, s[76:77]
v_cndmask_b32_e64 v191, v241, v191, s[78:79]
X 188
X 189
X 190
K 218,188,189
X 191
s_nop 0
K 219,190,191
Maav 168,180,208
U 77,77,13
U 78,78,13
U 79,79,13
K 72,72,73
K 73,74,75
K 74,76,77
Maav 172,180,216
W 1
K 75,78,79
buffer_store_dwordx4 v[72:75], v1, s[8:11], s93 offen offset:1024 sc1
U 80,80,13
U 81,81,13
U 82,82,13
U 83,83,13
Zvav 32,128,192
U 84,84,13
U 85,85,13
U 86,86,13
U 87,87,13
K 80,80,81
K 81,82,83
Zvav 64,128,200
K 82,84,85
K 83,86,87
buffer_store_dwordx4 v[80:83], v1, s[8:11], s93 offen offset:2048 sc1
U 88,88,13
U 89,89,13
U 90,90,13
U 91,91,13
U 92,92,13
U 93,93,13
U 94,94,13
U 95,95,13
K 88,88,89
K 89,90,91
K 90,92,93
K 91,94,95
buffer_store_dwordx4 v[88:91], v1, s[8:11], s93 offen offset:3072 sc1
U 96,96,14
U 97,97,14
U 98,98,14
U 99,99,14
U 100,100,14
U 101,101,14
U 102,102,14
U 103,103,14
K 96,96,97
K 97,98,99
K 98,100,101
K 99,102,103
s_add_u32 s93, s67, 262144
buffer_store_dwordx4 v[96:99], v1, s[8:11], s93 offen offset:0 sc1
s_nop 1
Zvav 96,128,208
U 104,104,14
U 105,105,14
U 106,106,14
U 107,107,14
U 108,108,14
U 109,109,14
U 110,110,14
U 111,111,14
K 104,104,105
K 105,106,107
K 106,108,109
K 107,110,111
buffer_store_dwordx4 v[104:107], v1, s[8:11], s93 offen offset:1024 sc1
U 112,112,14
U 113,113,14
U 114,114,14
U 115,115,14
U 116,116,14
U 117,117,14
U 118,118,14
U 119,119,14
K 112,112,113
K 113,114,115
K 114,116,117
K 115,118,119
buffer_store_dwordx4 v[112:115], v1, s[8:11], s93 offen offset:2048 sc1
U 120,120,14
U 121,121,14
U 122,122,14
U 123,123,14
U 124,124,14
U 125,125,14
U 126,126,14
U 127,127,14
K 120,120,121
K 121,122,123
K 122,124,125
K 123,126,127
buffer_store_dwordx4 v[120:123], v1, s[8:11], s93 offen offset:3072 sc1
U 128,128,15
U 129,129,15
U 130,130,15
U 131,131,15
U 132,132,15
U 133,133,15
U 134,134,15
U 135,135,15
K 128,128,129
K 129,130,131
K 130,132,133
K 131,134,135
s_add_u32 s93, s67, 393216
buffer_store_dwordx4 v[128:131], v1, s[8:11], s93 offen offset:0 sc1
s_nop 1
Zvav 128,128,216
U 136,136,15
U 137,137,15
U 138,138,15
U 139,139,15
U 140,140,15
U 141,141,15
Zvav 132,132,216
D 17,40,94,1024
Ra 148,242,54272
U 142,142,15
U 143,143,15
K 136,136,137
K 137,138,139
Zvav 100,132,208
Ra 152,242,55296
Ra 156,242,56320
K 138,140,141
K 139,142,143
Zvav 68,132,200
D 18,40,94,2048
Zvav 36,132,192
Zvav 40,136,192
D 19,40,94,3072
s_add_u32 m0, m0, 4096
Zvav 72,136,200
Zvav 104,136,208
buffer_store_dwordx4 v[136:139], v1, s[8:11], s93 offen offset:1024 sc1
U 144,144,15
U 145,145,15
U 146,146,15
U 147,147,15
U 148,148,15
Zvav 136,136,216
U 149,149,15
U 150,150,15
U 151,151,15
K 144,144,145
K 145,146,147
K 146,148,149
Zvav 140,140,216
buffer_load_dwordx4 v20, s[40:43], s94 offen lds
s_waitcnt vmcnt(15)
v_cmp_eq_u64 vcc, s[12:13], v[28:29]
s_cmp_eq_u64 vcc, -1
s_cselect_b32 s46, 79691776, 0
s_add_u32 s81, s58, 0
s_and_b32 s81, s81, 3
s_lshl_b32 s81, s81, 12
s_add_u32 s81, s81, s49
K 147,150,151
Zvav 108,140,208
Zvav 76,140,200
buffer_load_dwordx4 a[184:187], v1, s[44:47], s81 offen offset:0 sc1
s_add_u32 s82, s81, 131072
Zvav 44,140,192
W 0
Zvav 48,144,192
buffer_store_dwordx4 v[144:147], v1, s[8:11], s93 offen offset:2048 sc1
U 152,152,15
U 153,153,15
U 154,154,15
U 155,155,15
U 156,156,15
Zvav 80,144,200
U 157,157,15
U 158,158,15
U 159,159,15
K 152,152,153
K 153,154,155
K 154,156,157
)"
R"(Zvav 112,144,208
buffer_load_dwordx4 a[200:203], v1, s[44:47], s82 offen offset:0 sc1
K 155,158,159
Zvav 144,144,216
Zvav 148,148,216
buffer_load_dwordx4 a[188:191], v1, s[44:47], s81 offen offset:1024 sc1
Zvav 116,148,208
Zvav 84,148,200
buffer_store_dwordx4 v[152:155], v1, s[8:11], s93 offen offset:3072 sc1
s_mov_b32 s18, 40960
s_mov_b32 s19, 73728
s_bitcmp1_b32 s14, 0
s_cselect_b32 s18, 49152, s18
s_cselect_b32 s19, 98304, s19
s_cmp_lt_u32 s14, 2
s_cselect_b32 s20, s18, s19
s_cselect_b32 s21, s19, s18
v_add_u32 v2, s20, v1
v_add_u32 v3, s21, v1
Zvav 52,148,192
s_lshl_b32 s18, s14, 9
v_lshrrev_b32 v232, 1, v1
v_add_u32 v232, s18, v232
v_bfe_u32 v12, v1, 6, 2
v_lshrrev_b32 v13, 8, v1
v_bfe_u32 v14, v1, 4, 1
v_lshlrev_b32 v14, 4, v14
Zvav 56,152,192
v_mov_b32 v15, 0x3f80
v_lshlrev_b32 v15, v14, v15
v_bfe_u32 v14, v1, 5, 1
v_xor_b32 v27, v12, v13
v_lshl_or_b32 v27, v27, 1, v14
v_cmp_eq_u32_e64 s[86:87], 0, v27
Zvav 88,152,200
buffer_load_dwordx4 a[204:207], v1, s[44:47], s82 offen offset:1024 sc1
v_mov_b32 v226, 0
Zvav 120,152,208
v_cndmask_b32_e64 v224, 0, v15, s[86:87]
v_mov_b32 v228, 0
v_mov_b32 v230, v224
v_cmp_eq_u32_e64 s[86:87], 1, v27
v_mov_b32 v227, 0
s_nop 0
v_cndmask_b32_e64 v225, 0, v15, s[86:87]
Zvav 152,152,216
buffer_load_dwordx4 a[192:195], v1, s[44:47], s81 offen offset:2048 sc1
v_mov_b32 v229, 0
v_mov_b32 v231, v225
G 4,160
G 8,161
G 5,164
Zvav 156,156,216
G 9,165
G 6,168
G 10,169
G 7,172
G 11,173
A 4,4,8
Zvav 124,156,208
buffer_load_dwordx4 a[208:211], v1, s[44:47], s82 offen offset:2048 sc1
A 5,5,9
A 6,6,10
A 7,7,11
ds_write_b64 v232, v[6:7] offset:57344
A 8,4,5
Zvav 92,156,200
A 9,6,7
A 8,8,9
v_cmp_ngt_f32_e64 s[86:87], s90, v8
s_lshl_b32 s19, s14, 2
v_mov_b32 v9, s19
s_or_b64 s[88:89], s[88:89], s[86:87]
s_and_b64 s[88:89], s[88:89], s[24:25]
s_cselect_b32 s18, 1, 0
Zvav 60,156,192
buffer_load_dwordx4 a[196:199], v1, s[44:47], s81 offen offset:3072 sc1
v_mov_b32 v8, s18
ds_write_b32 v9, v8 offset:59392
v_xor_b32 v10, 1024, v232
v_bfe_u32 v11, v1, 2, 4
K 96,96,97
buffer_load_dwordx4 a[212:215], v1, s[44:47], s82 offen offset:3072 sc1
s_add_u32 s81, s58, 1
s_and_b32 s81, s81, 3
s_lshl_b32 s81, s81, 12
s_add_u32 s81, s81, s49
K 97,98,99
K 98,100,101
buffer_load_dwordx4 a[216:219], v1, s[44:47], s81 offen offset:0 sc1
s_add_u32 s82, s81, 131072
K 99,102,103
ds_write_b128 v2, v[96:99] offset:0
K 128,128,129
K 129,130,131
K 130,132,133
K 131,134,135
ds_write_b128 v2, v[128:131] offset:4096
buffer_load_dwordx4 a[232:235], v1, s[44:47], s82 offen offset:0 sc1
buffer_load_dwordx4 a[220:223], v1, s[44:47], s81 offen offset:1024 sc1
K 104,104,105
K 105,106,107
buffer_load_dwordx4 a[236:239], v1, s[44:47], s82 offen offset:1024 sc1
K 106,108,109
K 107,110,111
ds_write_b128 v2, v[104:107] offset:1024
K 136,136,137
K 137,138,139
K 138,140,141
K 139,142,143
ds_write_b128 v2, v[136:139] offset:5120
buffer_load_dwordx4 a[224:227], v1, s[44:47], s81 offen offset:2048 sc1
buffer_load_dwordx4 a[240:243], v1, s[44:47], s82 offen offset:2048 sc1
buffer_load_dwordx4 a[228:231], v1, s[44:47], s81 offen offset:3072 sc1
K 112,112,113
K 113,114,115
K 114,116,117
K 115,118,119
buffer_load_dwordx4 a[244:247], v1, s[44:47], s82 offen offset:3072 sc1
ds_write_b128 v2, v[112:115] offset:2048
K 144,144,145
K 145,146,147
K 146,148,149
K 147,150,151
ds_write_b128 v2, v[144:147] offset:6144
K 120,120,121
K 121,122,123
K 122,124,125
K 123,126,127
ds_write_b128 v2, v[120:123] offset:3072
K 152,152,153
K 153,154,155
K 154,156,157
K 155,158,159
ds_write_b128 v2, v[152:155] offset:7168
s_waitcnt vmcnt(13)
s_and_b64 s[86:87], s[88:89], s[24:25]
s_cbranch_scc1 .LL_48%=
v_mov_b64 v[28:29], s[12:13]
s_mov_b64 exec, 1
buffer_store_dwordx2 v[28:29], off, s[8:11], s69 sc1
s_mov_b64 exec, -1
.LL_48%=:
s_mov_b32 s70, 0
s_branch .Lend_41%=
.Lpd_30%=:
v_pk_mul_f32 v[32:33], v[32:33], s[22:23]
v_pk_mul_f32 v[34:35], v[34:35], s[22:23]
v_pk_mul_f32 v[36:37], v[36:37], s[22:23]
v_pk_mul_f32 v[38:39], v[38:39], s[22:23]
K 32,32,33
K 33,34,35
K 34,36,37
K 35,38,39
v_pk_mul_f32 v[64:65], v[64:65], s[22:23]
v_pk_mul_f32 v[66:67], v[66:67], s[22:23]
v_pk_mul_f32 v[68:69], v[68:69], s[22:23]
v_pk_mul_f32 v[70:71], v[70:71], s[22:23]
K 64,64,65
K 65,66,67
K 66,68,69
K 67,70,71
v_pk_mul_f32 v[96:97], v[96:97], s[22:23]
v_pk_mul_f32 v[98:99], v[98:99], s[22:23]
v_pk_mul_f32 v[100:101], v[100:101], s[22:23]
v_pk_mul_f32 v[102:103], v[102:103], s[22:23]
K 96,96,97
K 97,98,99
K 98,100,101
K 99,102,103
v_pk_mul_f32 v[128:129], v[128:129], s[22:23]
v_pk_mul_f32 v[130:131], v[130:131], s[22:23]
v_pk_mul_f32 v[132:133], v[132:133], s[22:23]
v_pk_mul_f32 v[134:135], v[134:135], s[22:23]
K 128,128,129
K 129,130,131
K 130,132,133
K 131,134,135
v_pk_mul_f32 v[40:41], v[40:41], s[22:23]
v_pk_mul_f32 v[42:43], v[42:43], s[22:23]
v_pk_mul_f32 v[44:45], v[44:45], s[22:23]
v_pk_mul_f32 v[46:47], v[46:47], s[22:23]
v_pk_mul_f32 v[72:73], v[72:73], s[22:23]
v_pk_mul_f32 v[74:75], v[74:75], s[22:23]
v_pk_mul_f32 v[76:77], v[76:77], s[22:23]
v_pk_mul_f32 v[78:79], v[78:79], s[22:23]
v_pk_mul_f32 v[104:105], v[104:105], s[22:23]
v_pk_mul_f32 v[106:107], v[106:107], s[22:23]
v_pk_mul_f32 v[108:109], v[108:109], s[22:23]
v_pk_mul_f32 v[110:111], v[110:111], s[22:23]
v_pk_mul_f32 v[136:137], v[136:137], s[22:23]
v_pk_mul_f32 v[138:139], v[138:139], s[22:23]
v_pk_mul_f32 v[140:141], v[140:141], s[22:23]
v_pk_mul_f32 v[142:143], v[142:143], s[22:23]
v_pk_mul_f32 v[48:49], v[48:49], s[22:23]
v_pk_mul_f32 v[50:51], v[50:51], s[22:23]
v_pk_mul_f32 v[52:53], v[52:53], s[22:23]
v_pk_mul_f32 v[54:55], v[54:55], s[22:23]
v_pk_mul_f32 v[80:81], v[80:81], s[22:23]
v_pk_mul_f32 v[82:83], v[82:83], s[22:23]
v_pk_mul_f32 v[84:85], v[84:85], s[22:23]
v_pk_mul_f32 v[86:87], v[86:87], s[22:23]
v_pk_mul_f32 v[112:113], v[112:113], s[22:23]
v_pk_mul_f32 v[114:115], v[114:115], s[22:23]
v_pk_mul_f32 v[116:117], v[116:117], s[22:23]
v_pk_mul_f32 v[118:119], v[118:119], s[22:23]
v_pk_mul_f32 v[144:145], v[144:145], s[22:23]
v_pk_mul_f32 v[146:147], v[146:147], s[22:23]
v_pk_mul_f32 v[148:149], v[148:149], s[22:23]
v_pk_mul_f32 v[150:151], v[150:151], s[22:23]
v_pk_mul_f32 v[56:57], v[56:57], s[22:23]
v_pk_mul_f32 v[58:59], v[58:59], s[22:23]
v_pk_mul_f32 v[60:61], v[60:61], s[22:23]
v_pk_mul_f32 v[62:63], v[62:63], s[22:23]
v_pk_mul_f32 v[88:89], v[88:89], s[22:23]
v_pk_mul_f32 v[90:91], v[90:91], s[22:23]
v_pk_mul_f32 v[92:93], v[92:93], s[22:23]
v_pk_mul_f32 v[94:95], v[94:95], s[22:23]
v_pk_mul_f32 v[120:121], v[120:121], s[22:23]
v_pk_mul_f32 v[122:123], v[122:123], s[22:23]
v_pk_mul_f32 v[124:125], v[124:125], s[22:23]
v_pk_mul_f32 v[126:127], v[126:127], s[22:23]
v_pk_mul_f32 v[152:153], v[152:153], s[22:23]
v_pk_mul_f32 v[154:155], v[154:155], s[22:23]
v_pk_mul_f32 v[156:157], v[156:157], s[22:23]
v_pk_mul_f32 v[158:159], v[158:159], s[22:23]
W 0
Zvav 196,128,32
Ra 144,242,4096
Ra 148,242,5120
Ra 152,242,6144
Ra 156,242,7168
Zvav 204,128,64
K 40,40,41
K 41,42,43
K 42,44,45
K 43,46,47
Zvav 212,128,96
K 72,72,73
K 73,74,75
K 74,76,77
K 75,78,79
Zvav 220,128,128
K 104,104,105
K 105,106,107
K 106,108,109
K 107,110,111
K 136,136,137
K 137,138,139
K 138,140,141
K 139,142,143
Mvav 196,132,40
Ra 128,242,8192
K 48,48,49
Mvav 204,132,72
K 49,50,51
K 50,52,53
K 51,54,55
K 80,80,81
Mvav 212,132,104
K 81,82,83
K 82,84,85
K 83,86,87
K 112,112,113
Mvav 220,132,136
K 113,114,115
K 114,116,117
K 115,118,119
K 144,144,145
K 145,146,147
K 146,148,149
K 147,150,151
Mvav 196,136,48
Ra 132,242,9216
K 56,56,57
Mvav 204,136,80
K 57,58,59
K 58,60,61
K 59,62,63
K 88,88,89
Mvav 212,136,112
K 89,90,91
K 90,92,93
K 91,94,95
K 120,120,121
Mvav 220,136,144
K 121,122,123
K 122,124,125
K 123,126,127
K 152,152,153
K 153,154,155
K 154,156,157
K 155,158,159
Mvav 196,140,56
Ra 136,242,10240
v_accvgpr_write_b32 a0, v32
Mvav 204,140,88
v_accvgpr_write_b32 a1, v33
v_accvgpr_write_b32 a2, v34
v_accvgpr_write_b32 a3, v35
Mvav 212,140,120
v_accvgpr_write_b32 a4, v40
v_accvgpr_write_b32 a5, v41
v_accvgpr_write_b32 a6, v42
Mvav 220,140,152
W 3
v_accvgpr_write_b32 a7, v43
v_accvgpr_write_b32 a8, v48
v_accvgpr_write_b32 a9, v49
v_accvgpr_write_b32 a10, v50
Zvav 68,144,64
Ra 140,242,11264
v_accvgpr_write_b32 a11, v51
v_accvgpr_write_b32 a12, v56
v_accvgpr_write_b32 a13, v57
Zvav 100,144,96
v_accvgpr_write_b32 a14, v58
v_accvgpr_write_b32 a15, v59
v_accvgpr_write_b32 a16, v64
v_cndmask_b32_e64 v196, v241, v196, s[72:73]
Zvav 132,144,128
v_cndmask_b32_e64 v197, v241, v197, s[74:75]
v_cndmask_b32_e64 v198, v241, v198, s[76:77]
v_cndmask_b32_e64 v199, v241, v199, s[78:79]
v_max3_f32 v4, v196, v197, v198
Mvav 68,148,72
v_max3_f32 v4, v4, v199, v252
v_max3_f32 v5, v204, v205, v206
v_max3_f32 v5, v5, v207, v253
v_max3_f32 v6, v212, v213, v214
Mvav 100,148,104
v_max3_f32 v6, v6, v215, v254
v_max3_f32 v7, v220, v221, v222
v_max3_f32 v7, v7, v223, v255
v_mov_b32 v8, v4
Mvav 132,148,136
v_mov_b32 v9, v5
v_mov_b32 v10, v6
v_mov_b32 v11, v7
v_accvgpr_write_b32 a17, v65
Mvav 68,152,80
v_permlane32_swap_b32 v4, v8
v_permlane32_swap_b32 v5, v9
v_permlane32_swap_b32 v6, v10
v_permlane32_swap_b32 v7, v11
Mvav 100,152,112
v_max_f32 v4, v4, v8
v_max_f32 v5, v5, v9
v_max_f32 v6, v6, v10
v_max_f32 v7, v7, v11
Mvav 132,152,144
v_mov_b32 v8, v4
v_mov_b32 v9, v5
v_mov_b32 v10, v6
v_mov_b32 v11, v7
Mvav 68,156,88
v_permlane16_swap_b32 v4, v8
v_permlane16_swap_b32 v5, v9
v_permlane16_swap_b32 v6, v10
v_accvgpr_write_b32 a18, v66
Mvav 100,156,120
v_permlane16_swap_b32 v7, v11
v_max_f32 v4, v4, v8
v_max_f32 v5, v5, v9
v_max_f32 v6, v6, v10
v_max_f32 v7, v7, v11
v_sub_f32 v4, 0, v4
v_sub_f32 v5, 0, v5
v_sub_f32 v6, 0, v6
v_sub_f32 v7, 0, v7
v_mov_b32 v224, v4
v_mov_b32 v225, v4
v_mov_b32 v226, v4
v_mov_b32 v227, v4
v_mov_b32 v228, v5
v_mov_b32 v229, v5
v_mov_b32 v230, v5
v_mov_b32 v231, v5
v_mov_b32 v232, v6
v_mov_b32 v233, v6
v_mov_b32 v234, v6
v_mov_b32 v235, v6
v_mov_b32 v236, v7
v_mov_b32 v237, v7
v_mov_b32 v238, v7
v_mov_b32 v239, v7
Mvav 132,156,152
W 0
Ra 144,242,12288
v_accvgpr_write_b32 a19, v67
v_accvgpr_write_b32 a20, v72
v_accvgpr_write_b32 a21, v73
Cvav 168,128,96,232
s_and_b32 s80, s14, 1
v_accvgpr_write_b32 a22, v74
v_accvgpr_write_b32 a23, v75
v_accvgpr_write_b32 a24, v80
v_accvgpr_write_b32 a25, v81
Cvav 172,128,128,236
s_lshl_b32 s80, s80, 14
)"
R"(v_accvgpr_write_b32 a26, v82
v_accvgpr_write_b32 a27, v83
v_accvgpr_write_b32 a28, v88
v_accvgpr_write_b32 a29, v89
Mvav 168,132,104
Ra 148,242,13312
Ra 152,242,14336
Ra 156,242,15360
v_accvgpr_write_b32 a30, v90
Mvav 172,132,136
s_add_u32 s80, s80, 98304
v_accvgpr_write_b32 a31, v91
v_accvgpr_write_b32 a32, v96
v_accvgpr_write_b32 a33, v97
v_accvgpr_write_b32 a34, v98
Mvav 168,136,112
v_accvgpr_write_b32 a35, v99
v_accvgpr_write_b32 a36, v104
v_accvgpr_write_b32 a37, v105
v_accvgpr_write_b32 a38, v106
Mvav 172,136,144
v_accvgpr_write_b32 a39, v107
v_accvgpr_write_b32 a40, v112
v_accvgpr_write_b32 a41, v113
v_accvgpr_write_b32 a42, v114
Mvav 168,140,120
Ra 128,242,24576
Ra 132,242,25600
Ra 136,242,26624
v_accvgpr_write_b32 a43, v115
Mvav 172,140,152
Ra 140,242,27648
v_accvgpr_write_b32 a44, v120
v_accvgpr_write_b32 a45, v121
W 4
Cvav 188,144,128,236
v_accvgpr_write_b32 a46, v122
v_accvgpr_write_b32 a47, v123
v_accvgpr_write_b32 a48, v128
v_accvgpr_write_b32 a49, v129
v_accvgpr_write_b32 a50, v130
v_accvgpr_write_b32 a51, v131
v_accvgpr_write_b32 a52, v136
v_accvgpr_write_b32 a53, v137
v_accvgpr_write_b32 a54, v138
v_accvgpr_write_b32 a55, v139
v_accvgpr_write_b32 a56, v144
v_accvgpr_write_b32 a57, v145
v_accvgpr_write_b32 a58, v146
v_accvgpr_write_b32 a59, v147
v_accvgpr_write_b32 a60, v152
v_accvgpr_write_b32 a61, v153
v_accvgpr_write_b32 a62, v154
v_accvgpr_write_b32 a63, v155
Mvav 188,148,136
s_barrier
v_add_u32 v8, s80, v1
A 196,196,224
A 197,197,224
A 198,198,224
Mvav 188,152,144
A 199,199,224
X 196
X 197
X 198
K 192,196,197
X 199
A 204,204,228
K 193,198,199
A 205,205,228
A 206,206,228
A 207,207,228
X 204
X 205
X 206
K 200,204,205
X 207
A 212,212,232
K 201,206,207
A 213,213,232
A 214,214,232
A 215,215,232
X 212
X 213
X 214
K 208,212,213
X 215
A 220,220,236
K 209,214,215
A 221,221,236
A 222,222,236
A 223,223,236
X 220
X 221
X 222
K 216,220,221
X 223
v_mov_b32 v194, 0
K 217,222,223
v_mov_b32 v195, 0
v_cndmask_b32_e64 v68, v241, v68, s[72:73]
v_cndmask_b32_e64 v69, v241, v69, s[74:75]
v_cndmask_b32_e64 v70, v241, v70, s[76:77]
v_cndmask_b32_e64 v71, v241, v71, s[78:79]
A 68,68,228
A 69,69,228
A 70,70,228
A 71,71,228
X 68
X 69
X 70
K 202,68,69
X 71
A 100,100,232
K 203,70,71
A 101,101,232
A 102,102,232
A 103,103,232
X 100
X 101
X 102
K 210,100,101
X 103
A 132,132,236
K 211,102,103
A 133,133,236
A 134,134,236
A 135,135,236
X 132
X 133
X 134
K 218,132,133
X 135
s_nop 0
K 219,134,135
Mvav 188,156,152
W 0
Ra 144,242,28672
Ra 148,242,29696
Ra 152,242,30720
Zvav 32,128,192
Ra 156,242,31744
Zvav 64,128,200
Zvav 96,128,208
s_and_b32 s80, s14, 1
s_xor_b32 s62, s62, 0x10000
Zvav 128,128,216
s_lshl_b32 s80, s80, 14
s_add_u32 s80, s80, 131072
v_add_u32 v23, 61440, v21
Zvav 132,132,216
v_add_u32 v12, s80, v1
Ra 64,12,8192
Ra 68,12,9216
v_add_u32 v24, 61440, v22
Zvav 100,132,208
Ra 72,12,10240
Ra 76,12,11264
Ra 80,12,12288
v_add_u32 v25, 122880, v21
Zvav 68,132,200
Ra 84,12,13312
Ra 88,12,14336
Ra 92,12,15360
v_add_u32 v26, 122880, v22
Zvav 36,132,192
Ra 96,12,0
Ra 100,12,1024
Ra 104,12,2048
Zvav 40,136,192
Ra 108,12,3072
Ra 112,12,4096
Ra 116,12,5120
Zvav 72,136,200
Ra 120,12,6144
Ra 124,12,7168
Zvav 104,136,208
Zvav 136,136,216
Zvav 140,140,216
Zvav 108,140,208
Zvav 76,140,200
s_waitcnt vmcnt(0) lgkmcnt(0)
s_barrier
v_xor_b32 v242, 0x10000, v242
ds_read_b128 v[248:251], v8
Zvav 44,140,192
s_add_u32 s80, s71, 128
v_mov_b64 v[28:29], s[12:13]
s_mov_b64 exec, 1
buffer_store_dwordx2 v[28:29], off, s[8:11], s80 sc1
s_mov_b64 exec, -1
Ra 128,242,40960
Zvav 48,144,192
Zvav 80,144,200
X 172
X 173
Zvav 112,144,208
Ra 132,242,41984
X 174
K 220,172,173
Zvav 144,144,216
X 175
v_cndmask_b32_e64 v168, v241, v168, s[72:73]
K 221,174,175
Zvav 148,148,216
v_cndmask_b32_e64 v169, v241, v169, s[74:75]
v_cndmask_b32_e64 v170, v241, v170, s[76:77]
v_cndmask_b32_e64 v171, v241, v171, s[78:79]
Zvav 116,148,208
X 168
X 169
Zvav 84,148,200
W 2
Ra 136,242,43008
X 170
K 212,168,169
Zvav 52,148,192
v_mov_b32 v224, v250
v_mov_b32 v225, v250
v_mov_b32 v226, v250
v_mov_b32 v227, v250
Zvav 56,152,192
v_mov_b32 v228, v251
v_mov_b32 v229, v251
v_mov_b32 v230, v251
v_mov_b32 v231, v251
Zvav 88,152,200
v_mov_b32 v232, v248
v_mov_b32 v233, v248
v_mov_b32 v234, v248
v_mov_b32 v235, v248
Zvav 120,152,208
v_mov_b32 v236, v249
v_mov_b32 v237, v249
v_mov_b32 v238, v249
v_mov_b32 v239, v249
Zvav 152,152,216
X 171
v_mov_b32 v204, 0
K 213,170,171
Zvav 156,156,216
Ra 140,242,44032
v_mov_b32 v205, 0
v_mov_b32 v196, 0
v_mov_b32 v197, 0
Zvav 124,156,208
v_mov_b32 v198, 0
v_mov_b32 v199, 0
v_mov_b32 v206, 0
v_mov_b32 v207, 0
Zvav 92,156,200
v_mov_b32 v214, 0
v_mov_b32 v215, 0
v_cndmask_b32_e64 v188, v241, v188, s[72:73]
v_cndmask_b32_e64 v189, v241, v189, s[74:75]
Zvav 60,156,192
v_cndmask_b32_e64 v190, v241, v190, s[76:77]
v_cndmask_b32_e64 v191, v241, v191, s[78:79]
X 188
X 189
X 190
K 222,188,189
X 191
s_nop 0
K 223,190,191
s_nop 1
s_branch .Lsp2_35%=
.Lmoe_42%=:
W 0
Cvaa 160,128,0,224
s_cmp_eq_u32 s14, 0
s_cselect_b32 s20, s30, s32
s_cselect_b32 s21, s31, s33
Ra 144,242,4096
Ra 148,242,5120
Cvaa 164,128,16,228
s_xor_b32 s62, s62, 0x10000
Ra 152,242,6144
Cvaa 168,128,32,232
X 184
Cvaa 172,128,48,236
s_and_b32 s21, s21, 0xffff
X 185
Mvaa 160,132,4
X 186
Mvaa 164,132,20
Ra 156,242,7168
K 214,184,185
Mvaa 168,132,36
s_mov_b32 s22, 2211840
X 187
Mvaa 172,132,52
X 188
Mvaa 160,136,8
Ra 128,242,16384
Mvaa 164,136,24
s_mov_b32 s23, 0x20000
K 215,186,187
Mvaa 168,136,40
X 189
Mvaa 172,136,56
X 190
Mvaa 160,140,12
Ra 132,242,17408
v_lshrrev_b32 v4, 6, v1
Mvaa 164,140,28
K 222,188,189
Mvaa 168,140,44
X 191
Mvaa 172,140,60
v_mul_u32_u24 v4, 3456, v4
K 223,190,191
v_smfmac_f32_16x16x64_bf16 a[160:163], a[176:179], v[192:199], v240
Ra 136,242,18432
v_smfmac_f32_16x16x64_bf16 a[164:167], a[176:179], v[200:207], v240
X 160
v_smfmac_f32_16x16x64_bf16 a[168:171], a[176:179], v[208:215], v240
v_lshrrev_b32 v5, 4, v1
v_smfmac_f32_16x16x64_bf16 a[172:175], a[176:179], v[216:223], v240
W 3
X 161
Cvaa 176,144,0,224
X 162
Cvaa 180,144,16,228
Ra 140,242,19456
v_xor_b32 v5, v5, v1
Cvaa 184,144,32,232
K 192,160,161
Cvaa 188,144,48,236
X 163
Mvaa 176,148,4
Ra 144,242,20480
v_and_b32 v5, 32, v5
Mvaa 180,148,20
X 164
Mvaa 184,148,36
K 193,162,163
v_and_b32 v6, 16, v1
Mvaa 188,148,52
X 165
Mvaa 176,152,8
X 166
Mvaa 180,152,24
Ra 148,242,21504
v_add3_u32 v4, v4, v5, v6
Mvaa 184,152,40
K 200,164,165
Mvaa 188,152,56
X 167
Mvaa 176,156,12
X 168
Mvaa 180,156,28
Ra 152,242,22528
v_add_u32 v5, 54272, v4
Mvaa 184,156,44
K 201,166,167
v_add_u32 v6, 108544, v4
Mvaa 188,156,60
W 3
X 169
Mvav 32,128,196
X 170
Mvav 64,128,204
Ra 156,242,23552
v_add_u32 v7, 162816, v4
Mvav 96,128,212
K 208,168,169
Mvav 128,128,220
X 171
Mvav 132,132,220
s_lshl_b32 s94, s85, 6
X 172
Mvav 100,132,212
s_add_u32 s64, s64, 1
Ra 128,242,8192
K 209,170,171
Mvav 68,132,204
X 173
Mvav 36,132,196
s_mul_i32 s95, s14, 20480
Mvav 40,136,196
X 174
Mvav 72,136,204
Ra 132,242,9216
K 216,172,173
Mvav 104,136,212
s_add_u32 s95, s95, 98304
X 175
Mvav 136,136,220
Mvav 140,140,220
s_add_u32 m0, s95, 0
X 176
Mvav 108,140,212
Ra 136,242,10240
K 217,174,175
Mvav 76,140,204
X 177
Mvav 44,140,196
W 3
D 4,20,94,0
)"
R"(Mvav 48,144,196
X 178
Mvav 80,144,204
Ra 140,242,11264
Mvav 112,144,212
D 5,20,94,1024
K 194,176,177
Mvav 144,144,220
X 179
Mvav 148,148,220
X 180
Mvav 116,148,212
Ra 144,242,12288
D 6,20,94,2048
Mvav 84,148,204
K 195,178,179
Mvav 52,148,196
X 181
Mvav 56,152,196
D 7,20,94,3072
Ra 148,242,13312
Mvav 88,152,204
X 182
Mvav 120,152,212
s_add_u32 s94, s94, 221184
K 202,180,181
Mvav 152,152,220
X 183
Mvav 156,156,220
X 184
Mvav 124,156,212
s_add_u32 m0, s95, 4096
Ra 152,242,14336
Mvav 92,156,204
K 203,182,183
Mvav 60,156,196
W 3
X 185
Cvaa 160,128,0,224
D 4,20,94,0
Ra 156,242,15360
Cvaa 164,128,16,228
X 186
Cvaa 168,128,32,232
K 210,184,185
Cvaa 172,128,48,236
X 187
Mvaa 160,132,4
D 5,20,94,1024
Ra 128,242,24576
Mvaa 164,132,20
X 188
Mvaa 168,132,36
D 6,20,94,2048
K 211,186,187
Mvaa 172,132,52
X 189
Mvaa 160,136,8
X 190
Mvaa 164,136,24
Ra 132,242,25600
D 7,20,94,3072
Mvaa 168,136,40
K 218,188,189
Mvaa 172,136,56
s_add_u32 s94, s94, 221184
X 191
Mvaa 160,140,12
Ra 136,242,26624
Mvaa 164,140,28
Mvaa 168,140,44
s_add_u32 m0, s95, 8192
Mvaa 172,140,60
W 3
Cvaa 176,144,0,224
Ra 140,242,27648
Cvaa 180,144,16,228
X 160
Cvaa 184,144,32,232
D 4,20,94,0
K 219,190,191
Cvaa 188,144,48,236
X 161
Mvaa 176,148,4
X 162
Mvaa 180,148,20
D 5,20,94,1024
K 196,160,161
Mvaa 184,148,36
X 163
Mvaa 188,148,52
Ra 144,242,28672
D 6,20,94,2048
Mvaa 176,152,8
X 164
Mvaa 180,152,24
Ra 148,242,29696
K 197,162,163
Mvaa 184,152,40
X 165
Mvaa 188,152,56
X 166
Mvaa 176,156,12
D 7,20,94,3072
K 204,164,165
Mvaa 180,156,28
s_add_u32 s94, s94, 221184
X 167
Mvaa 184,156,44
Ra 152,242,30720
Mvaa 188,156,60
W 3
X 168
Mvav 32,128,192
s_add_u32 m0, s95, 12288
Ra 156,242,31744
K 205,166,167
Mvav 64,128,200
X 169
Mvav 96,128,208
X 170
Mvav 128,128,216
D 4,20,94,0
K 212,168,169
Mvav 132,132,216
X 171
Mvav 100,132,208
D 5,20,94,1024
Mvav 68,132,200
X 172
Mvav 36,132,192
K 213,170,171
Mvav 40,136,192
X 173
Mvav 72,136,200
D 6,20,94,2048
Mvav 104,136,208
X 174
Mvav 136,136,216
D 7,20,94,3072
K 220,172,173
Mvav 140,140,216
X 175
Mvav 108,140,208
s_add_u32 s94, s94, 221184
X 176
Mvav 76,140,200
W 0
s_barrier
v_xor_b32 v242, 0x10000, v242
Mvav 44,140,192
Ra 128,242,0
K 221,174,175
Mvav 48,144,192
X 177
Mvav 80,144,200
v_bfe_u32 v11, v1, 4, 3
Mvav 112,144,208
X 178
Mvav 144,144,216
Ra 132,242,1024
v_lshlrev_b32 v11, 3, v11
Mvav 148,148,216
K 198,176,177
Mvav 116,148,208
X 179
Mvav 84,148,200
X 180
Mvav 52,148,192
Ra 136,242,2048
buffer_load_dwordx2 v[8:9], v11, s[8:11], 0 offen offset:3840 sc1
Mvav 56,152,192
Ra 140,242,3072
K 199,178,179
Mvav 88,152,200
X 181
Mvav 120,152,208
X 182
Mvav 152,152,216
K 206,180,181
Mvav 156,156,216
X 183
Mvav 124,156,208
Mvav 92,156,200
K 207,182,183
Mvav 60,156,192
W 0
Cvaa 160,128,0,224
s_add_u32 m0, s95, 16384
Ra 144,242,4096
Ra 148,242,5120
Cvaa 164,128,16,228
s_xor_b32 s62, s62, 0x10000
Ra 152,242,6144
Cvaa 168,128,32,232
X 184
Cvaa 172,128,48,236
X 185
Mvaa 160,132,4
X 186
Mvaa 164,132,20
D 4,20,94,0
K 214,184,185
Mvaa 168,132,36
Ra 156,242,7168
Mvaa 172,132,52
X 187
Mvaa 160,136,8
X 188
Mvaa 164,136,24
D 5,20,94,1024
Ra 128,242,16384
Mvaa 168,136,40
K 215,186,187
Mvaa 172,136,56
X 189
Mvaa 160,140,12
X 190
Mvaa 164,140,28
D 6,20,94,2048
Ra 132,242,17408
Mvaa 168,140,44
K 222,188,189
Mvaa 172,140,60
X 191
v_smfmac_f32_16x16x64_bf16 a[160:163], a[176:179], v[192:199], v240
Ra 136,242,18432
K 223,190,191
v_smfmac_f32_16x16x64_bf16 a[164:167], a[176:179], v[200:207], v240
D 7,20,94,3072
v_smfmac_f32_16x16x64_bf16 a[168:171], a[176:179], v[208:215], v240
s_add_u32 s94, s94, 221184
v_mul_u32_u24 v10, 160, v11
v_smfmac_f32_16x16x64_bf16 a[172:175], a[176:179], v[216:223], v240
W 3
X 160
Cvaa 176,144,0,224
X 161
Cvaa 180,144,16,228
Ra 140,242,19456
v_lshrrev_b32 v12, 7, v1
Cvaa 184,144,32,232
X 162
Cvaa 188,144,48,236
K 192,160,161
v_lshl_add_u32 v10, v12, 4, v10
Mvaa 176,148,4
s_mul_i32 s82, s14, 5120
X 163
Mvaa 180,148,20
X 164
Mvaa 184,148,36
Ra 144,242,20480
K 193,162,163
Mvaa 188,148,52
s_mul_i32 s81, s14, 640
X 165
Mvaa 176,152,8
X 166
Mvaa 180,152,24
s_add_u32 s81, s81, 118784
Ra 148,242,21504
K 200,164,165
Mvaa 184,152,40
X 167
Mvaa 188,152,56
X 168
Mvaa 176,156,12
s_waitcnt vmcnt(4)
v_cmp_eq_u64 vcc, s[12:13], v[8:9]
s_cmp_eq_u64 vcc, -1
s_cbranch_scc0 .Lspin_49%=
.Lok_50%=:
Ra 152,242,22528
Mvaa 180,156,28
K 201,166,167
Mvaa 184,156,44
s_add_u32 m0, s82, 53248
X 169
Mvaa 188,156,60
W 3
Mvav 32,128,196
X 170
Mvav 64,128,204
Ra 156,242,23552
Dc 10,8,81,0
Mvav 96,128,212
K 208,168,169
Mvav 128,128,220
X 171
Mvav 132,132,220
s_add_u32 m0, s82, 54144
X 172
Mvav 100,132,212
s_add_u32 s64, s64, 1
Ra 128,242,8192
K 209,170,171
Mvav 68,132,204
X 173
Mvav 36,132,196
Dc 10,8,81,128
Mvav 40,136,196
X 174
Mvav 72,136,204
s_add_u32 m0, s82, 55040
Ra 132,242,9216
K 216,172,173
Mvav 104,136,212
X 175
Mvav 136,136,220
Dc 10,8,81,256
Mvav 140,140,220
X 176
Mvav 108,140,212
Ra 136,242,10240
K 217,174,175
Mvav 76,140,204
s_add_u32 m0, s82, 55936
X 177
Mvav 44,140,196
W 3
Mvav 48,144,196
X 178
Mvav 80,144,204
Ra 140,242,11264
Dc 10,8,81,384
Mvav 112,144,212
K 194,176,177
Mvav 144,144,220
s_add_u32 m0, s82, 56832
X 179
Mvav 148,148,220
X 180
Mvav 116,148,212
Ra 144,242,12288
Mvav 84,148,204
Dc 10,8,81,512
K 195,178,179
Mvav 52,148,196
X 181
Mvav 56,152,196
X 182
Mvav 88,152,204
Ra 148,242,13312
Mvav 120,152,212
K 202,180,181
Mvav 152,152,220
X 183
Mvav 156,156,220
X 184
Mvav 124,156,212
Ra 152,242,14336
Mvav 92,156,204
K 203,182,183
Mvav 60,156,196
W 3
X 185
Cvaa 160,128,0,224
X 186
Cvaa 164,128,16,228
Ra 156,242,15360
Cvaa 168,128,32,232
K 210,184,185
Cvaa 172,128,48,236
X 187
Mvaa 160,132,4
X 188
Mvaa 164,132,20
Ra 128,242,24576
Mvaa 168,132,36
K 211,186,187
Mvaa 172,132,52
X 189
Mvaa 160,136,8
X 190
Mvaa 164,136,24
Ra 132,242,25600
Mvaa 168,136,40
K 218,188,189
Mvaa 172,136,56
X 191
Mvaa 160,140,12
Ra 136,242,26624
Mvaa 164,140,28
Mvaa 168,140,44
Mvaa 172,140,60
W 3
Cvaa 176,144,0,224
Ra 140,242,27648
v_lshrrev_b32 v8, 8, v1
Cvaa 180,144,16,228
X 160
Cvaa 184,144,32,232
K 219,190,191
Cvaa 188,144,48,236
X 161
Mvaa 176,148,4
X 162
Mvaa 180,148,20
K 196,160,161
)"
R"(v_bfe_u32 v9, v1, 6, 2
Mvaa 184,148,36
X 163
Mvaa 188,148,52
Ra 144,242,28672
v_lshl_add_u32 v9, v8, 3, v9
Mvaa 176,152,8
X 164
Mvaa 180,152,24
Ra 148,242,29696
K 197,162,163
Mvaa 184,152,40
X 165
Mvaa 188,152,56
X 166
Mvaa 176,156,12
K 204,164,165
v_lshlrev_b32 v11, 6, v9
Mvaa 180,156,28
X 167
Mvaa 184,156,44
Ra 152,242,30720
v_xor_b32 v9, s14, v8
Mvaa 188,156,60
W 3
X 168
Mvav 32,128,192
Ra 156,242,31744
K 205,166,167
Mvav 64,128,200
X 169
Mvav 96,128,208
X 170
Mvav 128,128,216
v_and_b32 v9, 1, v9
K 212,168,169
Mvav 132,132,216
X 171
Mvav 100,132,208
v_lshl_add_u32 v11, v9, 5, v11
Mvav 68,132,200
X 172
Mvav 36,132,192
K 213,170,171
Mvav 40,136,192
X 173
Mvav 72,136,200
v_bfe_u32 v9, v1, 4, 2
Mvav 104,136,208
X 174
Mvav 136,136,216
K 220,172,173
v_lshl_add_u32 v12, v9, 3, v11
Mvav 140,140,216
X 175
Mvav 108,140,208
X 176
Mvav 76,140,200
s_waitcnt vmcnt(0) lgkmcnt(0)
s_barrier
v_xor_b32 v242, 0x10000, v242
Mvav 44,140,192
v_add_u32 v11, 98304, v12
K 221,174,175
Mvav 48,144,192
Ra 128,242,0
Mvav 80,144,200
X 177
Mvav 112,144,208
X 178
Mvav 144,144,216
Ra 132,242,1024
v_and_b32 v13, 0x70, v1
Mvav 148,148,216
K 198,176,177
Mvav 116,148,208
X 179
Mvav 84,148,200
Ra 136,242,2048
v_lshl_add_u32 v13, v8, 7, v13
Mvav 52,148,192
X 180
Mvav 56,152,192
Ra 140,242,3072
K 199,178,179
Mvav 88,152,200
X 181
Mvav 120,152,208
X 182
Mvav 152,152,216
K 206,180,181
Mvav 156,156,216
X 183
Mvav 124,156,208
Mvav 92,156,200
K 207,182,183
Mvav 60,156,192
W 0
Cvaa 160,128,0,224
s_cmp_eq_u32 s14, 0
s_cselect_b32 s95, 106496, 0
Ra 144,242,4096
Ra 148,242,5120
Cvaa 164,128,16,228
s_xor_b32 s62, s62, 0x10000
X 184
Cvaa 168,128,32,232
Ta 184,11,0
Ta 186,11,256
Cvaa 172,128,48,236
s_add_u32 s95, s95, 32768
Ta 188,11,20480
Ta 190,11,20736
Mvaa 160,132,4
Ra 192,13,53248
Ta 196,11,2048
Mvaa 164,132,20
s_add_u32 m0, s95, 0
X 185
Mvaa 168,132,36
Ta 198,11,2304
Ta 200,11,22528
Mvaa 172,132,52
W 3
X 186
Zaaa 232,184,192
Ta 202,11,22784
K 214,184,185
Zaaa 236,188,192
Ra 204,13,53760
Ra 152,242,6144
Mvaa 160,136,8
X 187
Mvaa 164,136,24
Ta 208,11,4096
D 4,20,94,0
Mvaa 168,136,40
X 188
Mvaa 172,136,56
Ta 210,11,4352
D 5,20,94,1024
Mvaa 160,140,12
Ra 156,242,7168
K 215,186,187
Mvaa 164,140,28
X 189
Mvaa 168,140,44
Ta 212,11,24576
D 6,20,94,2048
Mvaa 172,140,60
X 190
v_smfmac_f32_16x16x64_bf16 a[160:163], a[176:179], v[192:199], v240
W 5
Ta 214,11,24832
D 7,20,94,3072
Maaa 232,196,204
s_add_u32 s94, s94, 221184
Ra 216,13,54272
K 222,188,189
Maaa 236,200,204
s_add_u32 m0, s95, 4096
X 191
v_smfmac_f32_16x16x64_bf16 a[164:167], a[176:179], v[200:207], v240
X 160
K 223,190,191
v_smfmac_f32_16x16x64_bf16 a[168:171], a[176:179], v[208:215], v240
Ra 128,242,16384
Ta 220,11,6144
v_smfmac_f32_16x16x64_bf16 a[172:175], a[176:179], v[216:223], v240
W 5
Ta 222,11,6400
Ra 132,242,17408
Cvaa 176,144,0,224
X 161
Cvaa 180,144,16,228
D 4,20,94,0
Ta 224,11,26624
Cvaa 184,144,32,232
X 162
Cvaa 188,144,48,236
D 5,20,94,1024
K 192,160,161
Mvaa 176,148,4
Ta 226,11,26880
Ra 228,13,54784
Mvaa 180,148,20
W 7
X 163
Maaa 232,208,216
D 6,20,94,2048
Ra 136,242,18432
Maaa 236,212,216
X 164
Mvaa 184,148,36
D 7,20,94,3072
Ta 184,11,8192
Mvaa 188,148,52
s_add_u32 s94, s94, 221184
Ra 140,242,19456
K 193,162,163
Mvaa 176,152,8
s_add_u32 m0, s95, 8192
X 165
Mvaa 180,152,24
Ta 186,11,8448
Ta 188,11,28672
Mvaa 184,152,40
X 166
Mvaa 188,152,56
Ra 144,242,20480
K 200,164,165
Mvaa 176,156,12
X 167
Mvaa 180,156,28
Ta 190,11,28928
D 4,20,94,0
Mvaa 184,156,44
W 7
X 168
Maaa 232,220,228
s_add_u32 s64, s64, 1
Ra 192,13,55296
D 5,20,94,1024
Maaa 236,224,228
Ta 196,11,10240
K 201,166,167
Mvaa 188,156,60
W 6
X 169
Mvav 32,128,196
D 6,20,94,2048
Ra 148,242,21504
Mvav 64,128,204
X 170
Mvav 96,128,212
Ta 198,11,10496
D 7,20,94,3072
Mvav 128,128,220
s_add_u32 s94, s94, 221184
Ta 200,11,30720
K 208,168,169
Mvav 132,132,220
s_add_u32 m0, s95, 12288
X 171
Mvav 100,132,212
Ra 152,242,22528
Ta 202,11,30976
Mvav 68,132,204
X 172
Mvav 36,132,196
W 6
Ra 204,13,55808
K 209,170,171
Maaa 232,184,192
X 173
Maaa 236,188,192
D 4,20,94,0
Ra 156,242,23552
Mvav 40,136,196
X 174
Mvav 72,136,204
Ta 208,11,12288
D 5,20,94,1024
Mvav 104,136,212
Ta 210,11,12544
K 216,172,173
Mvav 136,136,220
X 175
Mvav 140,140,220
D 6,20,94,2048
Ra 128,242,8192
Mvav 108,140,212
Ta 212,11,32768
Ta 214,11,33024
Mvav 76,140,204
X 176
Mvav 44,140,196
W 5
D 7,20,94,3072
K 217,174,175
Mvav 48,144,196
s_add_u32 s94, s94, 221184
X 177
Maaa 232,196,204
s_add_u32 m0, s95, 16384
Ra 216,13,56320
Ra 132,242,9216
Maaa 236,200,204
X 178
Mvav 80,144,204
Ta 220,11,14336
K 194,176,177
Mvav 112,144,212
X 179
Mvav 144,144,220
D 4,20,94,0
Ta 222,11,14592
Mvav 148,148,220
Ra 136,242,10240
Ta 224,11,34816
Mvav 116,148,212
X 180
Mvav 84,148,204
D 5,20,94,1024
K 195,178,179
Mvav 52,148,196
X 181
Mvav 56,152,196
D 6,20,94,2048
Ta 226,11,35072
Mvav 88,152,204
W 6
X 182
Maaa 232,208,216
D 7,20,94,3072
Ra 228,13,56832
Maaa 236,212,216
s_add_u32 s94, s94, 221184
Ra 140,242,11264
K 202,180,181
Mvav 120,152,212
X 183
Mvav 152,152,220
Ta 184,11,16384
Ra 144,242,12288
Mvav 156,156,220
X 184
Mvav 124,156,212
Ta 186,11,16640
K 203,182,183
Mvav 92,156,204
X 185
Mvav 60,156,196
W 3
Ta 188,11,36864
Ra 148,242,13312
Cvaa 160,128,0,224
X 186
Cvaa 164,128,16,228
Ta 190,11,37120
K 210,184,185
Cvaa 168,128,32,232
Ra 192,13,57344
Ta 196,11,18432
Maaa 232,220,228
X 187
Maaa 236,224,228
X 188
Cvaa 172,128,48,236
Ra 152,242,14336
K 211,186,187
Mvaa 160,132,4
Ta 198,11,18688
Ta 200,11,38912
Mvaa 164,132,20
X 189
Mvaa 168,132,36
X 190
Mvaa 172,132,52
Ra 156,242,15360
K 218,188,189
Mvaa 160,136,8
Ta 202,11,39168
Ra 204,13,57856
Mvaa 164,136,24
X 191
Mvaa 168,136,40
Ra 128,242,24576
Mvaa 172,136,56
W 8
Maaa 232,184,192
Ra 132,242,25600
Maaa 236,188,192
Mvaa 160,140,12
Mvaa 164,140,28
Mvaa 168,140,44
Ra 136,242,26624
Mvaa 172,140,60
W 5
Cvaa 176,144,0,224
Cvaa 180,144,16,228
Cvaa 184,144,32,232
X 160
K 219,190,191
Cvaa 188,144,48,236
X 161
Mvaa 176,148,4
W 3
X 162
Maaa 232,196,204
Ra 140,242,27648
K 196,160,161
Maaa 236,200,204
X 163
Mvaa 180,148,20
X 164
Mvaa 184,148,36
Ra 144,242,28672
K 197,162,163
Mvaa 188,148,52
)"
R"(X 165
Mvaa 176,152,8
X 166
Mvaa 180,152,24
Ra 148,242,29696
K 204,164,165
Mvaa 184,152,40
X 167
Mvaa 188,152,56
X 168
Mvaa 176,156,12
Ra 152,242,30720
K 205,166,167
Mvaa 180,156,28
X 169
Mvaa 184,156,44
X 170
Mvaa 188,156,60
W 3
K 212,168,169
Mvav 32,128,192
X 171
Mvav 64,128,200
Ra 156,242,31744
Mvav 96,128,208
X 172
Mvav 128,128,216
K 213,170,171
Mvav 132,132,216
X 173
Mvav 100,132,208
Mvav 68,132,200
X 174
Mvav 36,132,192
Mvav 40,136,192
K 220,172,173
Mvav 72,136,200
X 175
Mvav 104,136,208
Mvav 136,136,216
X 176
Mvav 140,140,216
Mvav 108,140,208
K 221,174,175
Mvav 76,140,200
s_waitcnt vmcnt(0) lgkmcnt(0)
s_barrier
v_xor_b32 v242, 0x10000, v242
Mvav 44,140,192
X 177
Mvav 48,144,192
X 178
Mvav 80,144,200
Ra 128,242,0
K 198,176,177
Mvav 112,144,208
X 179
Mvav 144,144,216
Ra 132,242,1024
Mvav 148,148,216
X 180
Mvav 116,148,208
Ra 136,242,2048
Mvav 84,148,200
K 199,178,179
Mvav 52,148,192
X 181
Mvav 56,152,192
Ra 140,242,3072
Mvav 88,152,200
X 182
Mvav 120,152,208
Mvav 152,152,216
K 206,180,181
Mvav 156,156,216
X 183
Mvav 124,156,208
Mvav 92,156,200
K 207,182,183
Mvav 60,156,192
W 0
Cvaa 160,128,0,224
s_mov_b32 s20, s34
Ra 144,242,4096
Ra 148,242,5120
Cvaa 164,128,16,228
s_xor_b32 s62, s62, 0x10000
X 184
Cvaa 168,128,32,232
s_and_b32 s21, s35, 0xffff
Ta 184,11,40960
Ta 186,11,41216
Cvaa 172,128,48,236
Ta 188,12,32768
Ta 190,12,33024
Mvaa 160,132,4
s_mov_b32 s22, 2211840
Ra 192,13,58368
Ta 196,11,43008
Mvaa 164,132,20
s_mov_b32 s23, 0x20000
X 185
Mvaa 168,132,36
Ta 198,11,43264
Ta 200,12,34816
Mvaa 172,132,52
W 3
X 186
Maaa 232,184,192
Ta 202,12,35072
K 214,184,185
Maaa 236,188,192
Ra 204,13,58880
Ra 152,242,6144
Mvaa 160,136,8
X 187
Mvaa 164,136,24
Ta 208,11,45056
v_lshrrev_b32 v4, 8, v1
Mvaa 168,136,40
X 188
Mvaa 172,136,56
Ta 210,11,45312
v_lshlrev_b32 v5, 5, v4
Mvaa 160,140,12
v_xor_b32 v5, v5, v1
K 215,186,187
Mvaa 164,140,28
Ra 156,242,7168
Ta 212,12,36864
Mvaa 168,140,44
X 189
Mvaa 172,140,60
v_and_b32 v5, 0xf0, v5
v_mul_u32_u24 v4, 1280, v4
v_smfmac_f32_16x16x64_bf16 a[160:163], a[176:179], v[192:199], v240
W 5
X 190
Maaa 232,196,204
Ta 214,12,37120
v_add_u32 v4, v4, v5
Maaa 236,200,204
Ra 216,13,59392
K 222,188,189
v_smfmac_f32_16x16x64_bf16 a[164:167], a[176:179], v[200:207], v240
X 191
X 160
K 223,190,191
v_smfmac_f32_16x16x64_bf16 a[168:171], a[176:179], v[208:215], v240
Ra 128,242,16384
v_xor_b32 v5, 128, v4
v_smfmac_f32_16x16x64_bf16 a[172:175], a[176:179], v[216:223], v240
W 4
Ta 220,11,47104
v_add_u32 v6, 8192, v4
Cvaa 176,144,0,224
Ta 222,11,47360
v_add_u32 v7, 12288, v5
Cvaa 180,144,16,228
Ra 132,242,17408
v_add_u32 v5, 4096, v5
Cvaa 184,144,32,232
s_mul_i32 s94, s85, 40960
X 161
Cvaa 188,144,48,236
s_mul_i32 s81, s14, 20480
Ta 224,12,38912
Ta 226,12,39168
Mvaa 176,148,4
s_add_u32 s94, s94, s81
X 162
Mvaa 180,148,20
W 6
s_lshl_b32 s95, s14, 12
K 192,160,161
Maaa 232,208,216
s_add_u32 s95, s95, 98304
X 163
Maaa 236,212,216
s_add_u32 m0, s95, 0
Ra 228,13,59904
Ra 136,242,18432
Mvaa 184,148,36
X 164
Mvaa 188,148,52
Ta 184,11,49152
K 193,162,163
Mvaa 176,152,8
X 165
Mvaa 180,152,24
Ra 140,242,19456
Ta 186,11,49408
Mvaa 184,152,40
D 4,20,94,0
Ta 188,12,40960
Mvaa 188,152,56
X 166
Mvaa 176,156,12
D 5,20,94,1024
K 200,164,165
Mvaa 180,156,28
X 167
Mvaa 184,156,44
W 5
D 6,20,94,2048
Ra 144,242,20480
Maaa 232,220,228
s_add_u32 s64, s64, 1
Ta 190,12,41216
Ra 192,13,60416
Maaa 236,224,228
D 7,20,94,3072
Ta 196,11,51200
Mvaa 188,156,60
W 6
X 168
Mvav 32,128,196
s_add_u32 s94, s94, 256
s_add_u32 m0, s95, 8192
K 201,166,167
Mvav 64,128,204
X 169
Mvav 96,128,212
D 4,20,94,0
Ra 148,242,21504
Mvav 128,128,220
X 170
Mvav 132,132,220
D 5,20,94,1024
Ta 198,11,51456
Mvav 100,132,212
Ta 200,12,43008
K 208,168,169
Mvav 68,132,204
D 6,20,94,2048
Ra 152,242,22528
Mvav 36,132,196
W 5
X 171
Maaa 232,184,192
D 7,20,94,3072
Ta 202,12,43264
Maaa 236,188,192
s_add_u32 s94, s94, 256
X 172
Mvav 40,136,196
s_add_u32 m0, s95, 16384
Ra 204,13,60928
K 209,170,171
Mvav 72,136,204
X 173
Mvav 104,136,212
Ra 156,242,23552
D 4,20,94,0
Mvav 136,136,220
X 174
Mvav 140,140,220
D 5,20,94,1024
Ta 208,11,53248
Mvav 108,140,212
Ta 210,11,53504
K 216,172,173
Mvav 76,140,204
D 6,20,94,2048
Ra 128,242,8192
Mvav 44,140,196
W 3
X 175
Mvav 48,144,196
D 7,20,94,3072
Ta 212,12,45056
Maaa 232,196,204
s_add_u32 s94, s94, 256
X 176
Maaa 236,200,204
s_add_u32 m0, s95, 24576
Ta 214,12,45312
K 217,174,175
Mvav 80,144,204
D 4,20,94,0
Ra 216,13,61440
Mvav 112,144,212
X 177
Mvav 144,144,220
D 5,20,94,1024
Ra 132,242,9216
Mvav 148,148,220
X 178
Mvav 116,148,212
D 6,20,94,2048
Ta 220,11,55296
Mvav 84,148,204
Ta 222,11,55552
K 194,176,177
Mvav 52,148,196
D 7,20,94,3072
Ra 136,242,10240
Mvav 56,152,196
s_add_u32 s94, s94, 256
X 179
Mvav 88,152,204
W 4
X 180
Maaa 232,208,216
s_add_u32 m0, s95, 32768
Ta 224,12,47104
K 195,178,179
Maaa 236,212,216
D 4,20,94,0
Ta 226,12,47360
Mvav 120,152,212
Ra 140,242,11264
Ra 228,13,61952
Mvav 152,152,220
X 181
Mvav 156,156,220
D 5,20,94,1024
Ta 184,11,57344
Mvav 124,156,212
X 182
Mvav 92,156,204
D 6,20,94,2048
K 202,180,181
Mvav 60,156,196
W 2
X 183
Cvaa 160,128,0,224
D 7,20,94,3072
Ra 144,242,12288
Cvaa 164,128,16,228
s_add_u32 s94, s94, 256
X 184
Cvaa 168,128,32,232
W 2
Ta 186,11,57600
K 203,182,183
Maaa 232,220,228
X 185
Maaa 236,224,228
Ta 188,12,49152
Ra 148,242,13312
Cvaa 172,128,48,236
X 186
Mvaa 160,132,4
Ta 190,12,49408
K 210,184,185
Mvaa 164,132,20
Ra 192,13,62464
Ta 196,11,59392
Mvaa 168,132,36
X 187
Mvaa 172,132,52
X 188
Mvaa 160,136,8
Ra 152,242,14336
K 211,186,187
Mvaa 164,136,24
Ta 198,11,59648
Ta 200,12,51200
Mvaa 168,136,40
X 189
Mvaa 172,136,56
W 4
X 190
Maaa 232,184,192
Ra 156,242,15360
K 218,188,189
Maaa 236,188,192
Ta 202,12,51456
Ra 204,13,62976
Mvaa 160,140,12
X 191
Mvaa 164,140,28
Ra 128,242,24576
Ra 132,242,25600
Mvaa 168,140,44
Ra 136,242,26624
Mvaa 172,140,60
W 5
X 160
Cvaa 176,144,0,224
K 219,190,191
Cvaa 180,144,16,228
X 161
Cvaa 184,144,32,232
X 162
Cvaa 188,144,48,236
Ra 140,242,27648
K 196,160,161
Mvaa 176,148,4
W 4
X 163
Maaa 232,196,204
X 164
Maaa 236,200,204
Ra 144,242,28672
K 197,162,163
Mvaa 180,148,20
X 165
Mvaa 184,148,36
)"
R"(X 166
Mvaa 188,148,52
K 204,164,165
G 252,232
Mvaa 176,152,8
X 167
Mvaa 180,152,24
G 253,233
G 254,234
Mvaa 184,152,40
X 168
Mvaa 188,152,56
G 255,235
G 8,236
Mvaa 176,156,12
Ra 148,242,29696
G 9,237
Mvaa 180,156,28
G 10,238
G 14,239
Mvaa 184,156,44
K 252,252,8
K 253,253,9
Mvaa 188,156,60
W 2
K 205,166,167
K 254,254,10
Mvav 32,128,192
K 255,255,14
v_and_b32 v8, 0xffff0000, v252
Mvav 64,128,200
v_and_b32 v9, 0xffff0000, v253
v_and_b32 v10, 0xffff0000, v254
Mvav 96,128,208
X 169
Mvav 128,128,216
v_and_b32 v14, 0xffff0000, v255
v_lshlrev_b32 v252, 16, v252
Mvav 132,132,216
v_lshlrev_b32 v253, 16, v253
v_lshlrev_b32 v254, 16, v254
Mvav 100,132,208
v_lshlrev_b32 v255, 16, v255
v_mul_f32 v15, 0xbfb8aa3b, v252
Mvav 68,132,200
Ra 152,242,30720
v_mul_f32 v2, 0xbfb8aa3b, v253
Mvav 36,132,192
X 170
Mvav 40,136,192
v_mul_f32 v3, 0xbfb8aa3b, v254
v_mul_f32 v27, 0xbfb8aa3b, v255
Mvav 72,136,200
X 15
Mvav 104,136,208
Ra 156,242,31744
K 212,168,169
Mvav 136,136,216
X 2
X 3
X 27
v_add_f32 v15, 1.0, v15
v_add_f32 v2, 1.0, v2
v_add_f32 v3, 1.0, v3
v_add_f32 v27, 1.0, v27
v_rcp_f32 v15, v15
v_rcp_f32 v2, v2
v_rcp_f32 v3, v3
v_rcp_f32 v27, v27
U 252,252,15
U 253,253,2
U 254,254,3
U 255,255,27
K 252,252,252
K 253,253,253
K 254,254,254
K 255,255,255
v_lshlrev_b32 v252, 16, v252
v_lshlrev_b32 v253, 16, v253
v_lshlrev_b32 v254, 16, v254
v_lshlrev_b32 v255, 16, v255
U 252,252,8
U 253,253,9
U 254,254,10
U 255,255,14
K 246,252,253
K 247,254,255
s_lshl_b32 s81, s14, 3
v_add_u32 v2, s81, v1
s_mov_b32 s86, 0x00ff00ff
s_mov_b32 s87, 0x00ff00ff
v_cndmask_b32_e64 v252, 0, v246, s[86:87]
v_cndmask_b32_e64 v253, 0, v247, s[86:87]
v_cndmask_b32_e64 v246, v246, 0, s[86:87]
v_cndmask_b32_e64 v247, v247, 0, s[86:87]
ds_write_b64 v2, v[252:253] offset:63488
ds_write_b64 v2, v[246:247] offset:64512
Mvav 140,140,216
X 171
Mvav 108,140,208
X 172
Mvav 76,140,200
s_waitcnt vmcnt(0) lgkmcnt(0)
s_barrier
v_xor_b32 v242, 0x10000, v242
Mvav 44,140,192
Ra 128,242,0
K 213,170,171
Mvav 48,144,192
X 173
Mvav 80,144,200
X 174
Mvav 112,144,208
Ra 132,242,1024
K 220,172,173
Mvav 144,144,216
X 175
Mvav 148,148,216
X 176
Mvav 116,148,208
Ra 136,242,2048
K 221,174,175
Mvav 84,148,200
X 177
Mvav 52,148,192
X 178
Mvav 56,152,192
Ra 140,242,3072
K 198,176,177
Mvav 88,152,200
X 179
Mvav 120,152,208
X 180
Mvav 152,152,216
K 199,178,179
Mvav 156,156,216
X 181
Mvav 124,156,208
X 182
Mvav 92,156,200
K 206,180,181
Mvav 60,156,192
X 183
s_nop 0
K 207,182,183
W 0
Cvaa 160,128,0,224
Ra 144,242,4096
Ra 148,242,5120
Cvaa 164,128,16,228
s_xor_b32 s62, s62, 0x10000
X 184
Cvaa 168,128,32,232
Ra 216,1,63488
Ra 220,1,64512
Cvaa 172,128,48,236
v_lshrrev_b32 v8, 6, v1
v_and_b32 v9, 7, v8
Mvaa 160,132,4
s_lshl_b32 s81, s14, 2
X 185
Mvaa 164,132,20
v_xor_b32 v9, s81, v9
v_lshlrev_b32 v9, 5, v9
Mvaa 168,132,36
X 186
Mvaa 172,132,52
v_lshl_add_u32 v4, v8, 8, v9
v_bfe_u32 v9, v1, 4, 2
Mvaa 160,136,8
v_lshl_add_u32 v4, v9, 3, v4
v_add_u32 v4, 98304, v4
Mvaa 164,136,24
K 214,184,185
v_xor_b32 v5, 32, v4
Mvaa 168,136,40
v_xor_b32 v6, 64, v4
v_xor_b32 v7, 96, v4
Ta 184,4,0
Ta 186,4,4096
Mvaa 172,136,56
Ta 188,5,0
Ta 190,5,4096
Mvaa 160,140,12
Ra 152,242,6144
Ta 192,6,0
Mvaa 164,140,28
W 4
X 187
Zaaa 232,184,216
Ta 194,6,4096
v_and_b32 v11, 0x70, v1
Mvaa 168,140,44
v_mul_u32_u24 v11, 160, v11
v_lshrrev_b32 v10, 8, v1
Mvaa 172,140,60
X 188
v_smfmac_f32_16x16x64_bf16 a[160:163], a[176:179], v[192:199], v240
Ra 156,242,7168
W 4
Maaa 232,188,220
K 215,186,187
v_lshl_add_u32 v11, v10, 4, v11
v_smfmac_f32_16x16x64_bf16 a[164:167], a[176:179], v[200:207], v240
X 189
X 190
K 222,188,189
X 191
X 160
K 223,190,191
v_smfmac_f32_16x16x64_bf16 a[168:171], a[176:179], v[208:215], v240
Ta 196,7,0
v_bfe_u32 v10, v1, 7, 1
v_smfmac_f32_16x16x64_bf16 a[172:175], a[176:179], v[216:223], v240
W 2
Ta 198,7,4096
v_lshl_add_u32 v10, v10, 6, v11
Zaaa 236,192,216
W 2
s_mul_i32 s94, s85, 20480
s_lshl_b32 s82, s14, 8
Cvaa 176,144,0,224
s_add_u32 s94, s94, s82
Ta 200,4,8192
Ta 202,4,12288
Cvaa 180,144,16,228
s_add_u32 s94, s94, 2752512
s_lshl_b32 s71, s85, 4
Ra 128,242,16384
Cvaa 184,144,32,232
W 3
s_lshl_b32 s82, s14, 3
Ta 204,5,8192
Maaa 236,196,220
s_add_u32 s71, s71, s82
s_add_u32 s71, s71, 2560
Ta 206,5,12288
Cvaa 188,144,48,236
X 161
Mvaa 176,148,4
Ra 132,242,17408
Ta 208,6,8192
Mvaa 180,148,20
W 5
X 162
Zaaa 240,200,216
Ta 210,6,12288
K 192,160,161
Mvaa 184,148,36
buffer_store_dwordx4 a[232:235], v10, s[8:11], s94 offen offset:0 sc1
Ta 212,7,8192
Mvaa 188,148,52
X 163
Mvaa 176,152,8
W 4
X 164
Maaa 240,204,220
Ra 136,242,18432
Ta 214,7,12288
Mvaa 180,152,24
Ta 184,4,16384
K 193,162,163
Mvaa 184,152,40
X 165
Mvaa 188,152,56
W 4
Ta 186,4,20480
Ra 140,242,19456
Zaaa 244,208,216
X 166
Mvaa 176,156,12
buffer_store_dwordx4 a[236:239], v10, s[8:11], s94 offen offset:128 sc1
K 200,164,165
Mvaa 180,156,28
X 167
Mvaa 184,156,44
W 3
Ta 188,5,16384
Ta 190,5,20480
Maaa 244,212,220
s_add_u32 s64, s64, 1
X 168
Mvaa 188,156,60
W 2
Ra 144,242,20480
K 201,166,167
Mvav 32,128,196
Ta 192,6,16384
Ta 194,6,20480
Mvav 64,128,204
X 169
Zaaa 248,184,216
buffer_store_dwordx4 a[240:243], v10, s[8:11], s94 offen offset:512 sc1
Ta 196,7,16384
Mvav 96,128,212
X 170
Mvav 128,128,220
Ra 148,242,21504
K 208,168,169
Mvav 132,132,220
W 5
Ta 198,7,20480
Ta 200,4,24576
Maaa 248,188,220
X 171
Mvav 100,132,212
X 172
Mvav 68,132,204
Ta 202,4,28672
K 209,170,171
Mvav 36,132,196
W 5
Ra 152,242,22528
buffer_store_dwordx4 a[244:247], v10, s[8:11], s94 offen offset:640 sc1
Zaaa 252,192,216
X 173
Mvav 40,136,196
X 174
Mvav 72,136,204
Ta 204,5,24576
Ta 206,5,28672
Mvav 104,136,212
W 5
Ra 156,242,23552
K 216,172,173
Maaa 252,196,220
X 175
Mvav 136,136,220
Ta 208,6,24576
Ta 210,6,28672
Mvav 140,140,220
X 176
Mvav 108,140,212
W 6
buffer_store_dwordx4 a[248:251], v10, s[8:11], s94 offen offset:1024 sc1
K 217,174,175
Zaaa 232,200,216
X 177
Mvav 76,140,204
Ta 212,7,24576
Ra 128,242,8192
Mvav 44,140,196
W 4
Ta 214,7,28672
Ta 184,4,32768
Mvav 48,144,196
X 178
Maaa 232,204,220
Ta 186,4,36864
K 194,176,177
Mvav 80,144,204
X 179
Mvav 112,144,212
Ra 132,242,9216
buffer_store_dwordx4 a[252:255], v10, s[8:11], s94 offen offset:1152 sc1
Mvav 144,144,220
W 6
X 180
Zaaa 236,208,216
Ta 188,5,32768
K 195,178,179
Mvav 148,148,220
X 181
Mvav 116,148,212
Ta 190,5,36864
Ra 136,242,10240
Mvav 84,148,204
W 6
X 182
Maaa 236,212,220
Ta 192,6,32768
K 202,180,181
Mvav 52,148,196
Ta 194,6,36864
Ra 140,242,11264
Mvav 56,152,196
X 183
Mvav 88,152,204
W 7
buffer_store_dwordx4 a[232:235], v10, s[8:11], s94 offen offset:1536 sc1
Ta 196,7,32768
Zaaa 240,184,216
X 184
Mvav 120,152,212
Ta 198,7,36864
K 203,182,183
Mvav 152,152,220
Ra 144,242,12288
Ra 148,242,13312
Mvav 156,156,220
W 8
X 185
Maaa 240,188,220
X 186
Mvav 124,156,212
buffer_store_dwordx4 a[236:239], v10, s[8:11], s94 offen offset:1664 sc1
K 210,184,185
Mvav 92,156,204
X 187
Mvav 60,156,196
)"
R"(W 5
X 188
Zaaa 244,192,216
W 4
Ra 152,242,14336
K 211,186,187
Cvaa 160,128,0,224
X 189
Cvaa 164,128,16,228
X 190
Cvaa 168,128,32,232
W 3
Ra 156,242,15360
buffer_store_dwordx4 a[240:243], v10, s[8:11], s94 offen offset:2048 sc1
Maaa 244,196,220
K 218,188,189
Cvaa 172,128,48,236
X 191
Mvaa 160,132,4
Ra 128,242,24576
Mvaa 164,132,20
Mvaa 168,132,36
Mvaa 172,132,52
Mvaa 160,136,8
Ra 132,242,25600
buffer_store_dwordx4 a[244:247], v10, s[8:11], s94 offen offset:2176 sc1
Mvaa 164,136,24
Mvaa 168,136,40
Mvaa 172,136,56
Mvaa 160,140,12
Ra 136,242,26624
Mvaa 164,140,28
Mvaa 168,140,44
Mvaa 172,140,60
W 3
Cvaa 176,144,0,224
Ra 140,242,27648
Cvaa 180,144,16,228
X 160
Cvaa 184,144,32,232
K 219,190,191
Cvaa 188,144,48,236
X 161
Mvaa 176,148,4
X 162
Mvaa 180,148,20
Ra 144,242,28672
K 196,160,161
Mvaa 184,148,36
X 163
Mvaa 188,148,52
X 164
Mvaa 176,152,8
Ra 148,242,29696
K 197,162,163
Mvaa 180,152,24
X 165
Mvaa 184,152,40
X 166
Mvaa 188,152,56
K 204,164,165
Mvaa 176,156,12
X 167
Mvaa 180,156,28
X 168
Mvaa 184,156,44
Ra 152,242,30720
K 205,166,167
Mvaa 188,156,60
W 3
X 169
Mvav 32,128,192
X 170
Mvav 64,128,200
Ra 156,242,31744
K 212,168,169
Mvav 96,128,208
X 171
Mvav 128,128,216
X 172
Mvav 132,132,216
K 213,170,171
Mvav 100,132,208
X 173
Mvav 68,132,200
X 174
Mvav 36,132,192
Mvav 40,136,192
K 220,172,173
Mvav 72,136,200
X 175
Mvav 104,136,208
Mvav 136,136,216
X 176
Mvav 140,140,216
Mvav 108,140,208
K 221,174,175
Mvav 76,140,200
W 0
s_barrier
v_xor_b32 v242, 0x10000, v242
Mvav 44,140,192
X 177
Mvav 48,144,192
X 178
Mvav 80,144,200
Ra 128,242,0
K 198,176,177
Mvav 112,144,208
X 179
Mvav 144,144,216
Ra 132,242,1024
Mvav 148,148,216
X 180
Mvav 116,148,208
Ra 136,242,2048
Mvav 84,148,200
K 199,178,179
Mvav 52,148,192
X 181
Mvav 56,152,192
Ra 140,242,3072
Mvav 88,152,200
X 182
Mvav 120,152,208
Mvav 152,152,216
K 206,180,181
Mvav 156,156,216
X 183
Mvav 124,156,208
Mvav 92,156,200
s_waitcnt vmcnt(0)
v_mov_b64 v[28:29], s[12:13]
s_mov_b64 exec, 1
buffer_store_dwordx2 v[28:29], off, s[8:11], s71 sc1
s_mov_b64 exec, -1
K 207,182,183
Mvav 60,156,192
s_mov_b32 s85, 99
s_lshl_b32 s65, s57, 1
s_sub_u32 s65, s65, 1
s_branch .Ll0t_44%=
.Lf1r_45%=:
s_lshl_b32 s92, s58, 2
s_add_u32 s91, s92, s51
s_add_u32 s95, s92, 0
s_and_b32 s95, s95, 15
s_mul_i32 s95, s95, 40960
s_mul_i32 s71, s51, 0
s_add_u32 s71, s71, 133120
s_add_u32 m0, s71, s48
s_lshl2_add_u32 s94, s48, s95
D 16,40,94,0
s_add_u32 s95, s92, 1
s_and_b32 s95, s95, 15
s_mul_i32 s95, s95, 40960
s_mul_i32 s71, s51, 0
s_add_u32 s71, s71, 143360
D 17,40,94,1024
D 18,40,94,2048
D 19,40,94,3072
s_add_u32 m0, m0, 4096
s_nop 0
buffer_load_dwordx4 v20, s[40:43], s94 offen lds
s_add_u32 m0, s71, s48
s_lshl2_add_u32 s94, s48, s95
D 16,40,94,0
s_add_u32 s95, s92, 2
s_and_b32 s95, s95, 15
s_mul_i32 s95, s95, 40960
s_mul_i32 s71, s51, 0
s_add_u32 s71, s71, 153600
D 17,40,94,1024
D 18,40,94,2048
D 19,40,94,3072
s_add_u32 m0, m0, 4096
s_nop 0
buffer_load_dwordx4 v20, s[40:43], s94 offen lds
s_add_u32 m0, s71, s48
s_lshl2_add_u32 s94, s48, s95
D 16,40,94,0
D 17,40,94,1024
D 18,40,94,2048
D 19,40,94,3072
s_add_u32 m0, m0, 4096
s_nop 0
buffer_load_dwordx4 v20, s[40:43], s94 offen lds
s_branch .Lf1_38%=
.Lspin_13%=:
s_mov_b32 s84, 0
.Llp_15%=:
s_sleep 2
v_lshrrev_b32 v28, 1, v1
s_lshl_b32 s80, s60, 9
s_add_u32 s80, s80, 40960
buffer_load_dwordx2 v[28:29], v28, s[8:11], s80 offen sc1
s_waitcnt vmcnt(0)
s_add_u32 s84, s84, 1
v_cmp_eq_u32 vcc, s13, v29
s_cmp_eq_u64 vcc, -1
s_cbranch_scc1 .Lok_14%=
s_cmp_lt_u32 s84, 1048576
s_cbranch_scc1 .Llp_15%=
s_branch .Lok_14%=
.Lspin_18%=:
s_mov_b32 s84, 0
.Llp_20%=:
s_sleep 2
v_lshrrev_b32 v28, 1, v1
s_lshl_b32 s80, s60, 9
s_add_u32 s80, s80, 40960
buffer_load_dwordx2 v[28:29], v28, s[8:11], s80 offen sc1
s_waitcnt vmcnt(0)
s_add_u32 s84, s84, 1
v_cmp_eq_u32 vcc, s13, v29
s_cmp_eq_u64 vcc, -1
s_cbranch_scc1 .Lok_19%=
s_cmp_lt_u32 s84, 1048576
s_cbranch_scc1 .Llp_20%=
s_branch .Lok_19%=
.Lrt_21%=:
s_waitcnt vmcnt(0)
s_mov_b32 s86, 40960
buffer_load_dwordx4 v[32:35], v1, s[8:11], s86 offen offset:0 sc1
buffer_load_dwordx4 v[36:39], v1, s[8:11], s86 offen offset:1024 sc1
buffer_load_dwordx4 v[40:43], v1, s[8:11], s86 offen offset:2048 sc1
buffer_load_dwordx4 v[44:47], v1, s[8:11], s86 offen offset:3072 sc1
s_mov_b32 s86, 45056
buffer_load_dwordx4 v[48:51], v1, s[8:11], s86 offen offset:0 sc1
buffer_load_dwordx4 v[52:55], v1, s[8:11], s86 offen offset:1024 sc1
buffer_load_dwordx4 v[56:59], v1, s[8:11], s86 offen offset:2048 sc1
buffer_load_dwordx4 v[60:63], v1, s[8:11], s86 offen offset:3072 sc1
s_mov_b32 s86, 49152
buffer_load_dwordx4 v[64:67], v1, s[8:11], s86 offen offset:0 sc1
buffer_load_dwordx4 v[68:71], v1, s[8:11], s86 offen offset:1024 sc1
buffer_load_dwordx4 v[72:75], v1, s[8:11], s86 offen offset:2048 sc1
buffer_load_dwordx4 v[76:79], v1, s[8:11], s86 offen offset:3072 sc1
s_mov_b32 s86, 53248
buffer_load_dwordx4 v[80:83], v1, s[8:11], s86 offen offset:0 sc1
buffer_load_dwordx4 v[84:87], v1, s[8:11], s86 offen offset:1024 sc1
buffer_load_dwordx4 v[88:91], v1, s[8:11], s86 offen offset:2048 sc1
buffer_load_dwordx4 v[92:95], v1, s[8:11], s86 offen offset:3072 sc1
s_branch .Lrtr_22%=
.Lrt_23%=:
v_lshrrev_b32 v136, 1, v1
s_mov_b32 s84, 0
.LL_25%=:
s_waitcnt vmcnt(0)
v_min3_u32 v96, v33, v35, v95
v_max3_u32 v97, v33, v35, v95
v_min3_u32 v96, v96, v37, v39
v_max3_u32 v97, v97, v37, v39
v_min3_u32 v96, v96, v41, v43
v_max3_u32 v97, v97, v41, v43
v_min3_u32 v96, v96, v45, v47
v_max3_u32 v97, v97, v45, v47
v_min3_u32 v96, v96, v49, v51
v_max3_u32 v97, v97, v49, v51
v_min3_u32 v96, v96, v53, v55
v_max3_u32 v97, v97, v53, v55
v_min3_u32 v96, v96, v57, v59
v_max3_u32 v97, v97, v57, v59
v_min3_u32 v96, v96, v61, v63
v_max3_u32 v97, v97, v61, v63
v_min3_u32 v96, v96, v65, v67
v_max3_u32 v97, v97, v65, v67
v_min3_u32 v96, v96, v69, v71
v_max3_u32 v97, v97, v69, v71
v_min3_u32 v96, v96, v73, v75
v_max3_u32 v97, v97, v73, v75
v_min3_u32 v96, v96, v77, v79
v_max3_u32 v97, v97, v77, v79
v_min3_u32 v96, v96, v81, v83
v_max3_u32 v97, v97, v81, v83
v_min3_u32 v96, v96, v85, v87
v_max3_u32 v97, v97, v85, v87
v_min3_u32 v96, v96, v89, v91
v_max3_u32 v97, v97, v89, v91
v_min_u32 v96, v96, v93
v_max_u32 v97, v97, v93
v_cmp_eq_u32 vcc, s13, v96
v_cmp_eq_u32_e64 s[86:87], s13, v97
s_and_b64 vcc, vcc, s[86:87]
s_cmp_eq_u64 vcc, -1
s_cbranch_scc1 .LL_26%=
s_sleep 2
s_mov_b32 s86, 40960
buffer_load_dwordx4 v[32:35], v1, s[8:11], s86 offen offset:0 sc1
buffer_load_dwordx4 v[36:39], v1, s[8:11], s86 offen offset:1024 sc1
buffer_load_dwordx4 v[40:43], v1, s[8:11], s86 offen offset:2048 sc1
buffer_load_dwordx4 v[44:47], v1, s[8:11], s86 offen offset:3072 sc1
s_mov_b32 s86, 45056
buffer_load_dwordx4 v[48:51], v1, s[8:11], s86 offen offset:0 sc1
buffer_load_dwordx4 v[52:55], v1, s[8:11], s86 offen offset:1024 sc1
buffer_load_dwordx4 v[56:59], v1, s[8:11], s86 offen offset:2048 sc1
buffer_load_dwordx4 v[60:63], v1, s[8:11], s86 offen offset:3072 sc1
s_mov_b32 s86, 49152
buffer_load_dwordx4 v[64:67], v1, s[8:11], s86 offen offset:0 sc1
buffer_load_dwordx4 v[68:71], v1, s[8:11], s86 offen offset:1024 sc1
buffer_load_dwordx4 v[72:75], v1, s[8:11], s86 offen offset:2048 sc1
buffer_load_dwordx4 v[76:79], v1, s[8:11], s86 offen offset:3072 sc1
s_mov_b32 s86, 53248
buffer_load_dwordx4 v[80:83], v1, s[8:11], s86 offen offset:0 sc1
buffer_load_dwordx4 v[84:87], v1, s[8:11], s86 offen offset:1024 sc1
buffer_load_dwordx4 v[88:91], v1, s[8:11], s86 offen offset:2048 sc1
buffer_load_dwordx4 v[92:95], v1, s[8:11], s86 offen offset:3072 sc1
s_add_u32 s84, s84, 1
s_cmp_lt_u32 s84, 1048576
s_cbranch_scc1 .LL_25%=
s_waitcnt vmcnt(0)
.LL_26%=:
v_mov_b32 v96, v32
v_mov_b32 v97, 0
v_mov_b32 v98, 0
v_mov_b32 v100, 0
v_med3_u32 v98, v97, v98, v34
v_med3_u32 v97, v96, v97, v34
v_max_u32 v96, v96, v34
v_med3_u32 v98, v97, v98, v36
v_med3_u32 v97, v96, v97, v36
v_max_u32 v96, v96, v36
v_med3_u32 v98, v97, v98, v38
v_med3_u32 v97, v96, v97, v38
v_max_u32 v96, v96, v38
v_med3_u32 v98, v97, v98, v40
v_med3_u32 v97, v96, v97, v40
v_max_u32 v96, v96, v40
v_med3_u32 v98, v97, v98, v42
v_med3_u32 v97, v96, v97, v42
v_max_u32 v96, v96, v42
v_med3_u32 v98, v97, v98, v44
v_med3_u32 v97, v96, v97, v44
v_max_u32 v96, v96, v44
v_med3_u32 v98, v97, v98, v46
v_med3_u32 v97, v96, v97, v46
v_max_u32 v96, v96, v46
v_med3_u32 v98, v97, v98, v48
v_med3_u32 v97, v96, v97, v48
v_max_u32 v96, v96, v48
v_med3_u32 v98, v97, v98, v50
v_med3_u32 v97, v96, v97, v50
v_max_u32 v96, v96, v50
v_med3_u32 v98, v97, v98, v52
v_med3_u32 v97, v96, v97, v52
v_max_u32 v96, v96, v52
v_med3_u32 v98, v97, v98, v54
v_med3_u32 v97, v96, v97, v54
v_max_u32 v96, v96, v54
v_med3_u32 v98, v97, v98, v56
v_med3_u32 v97, v96, v97, v56
v_max_u32 v96, v96, v56
v_med3_u32 v98, v97, v98, v58
v_med3_u32 v97, v96, v97, v58
v_max_u32 v96, v96, v58
v_med3_u32 v98, v97, v98, v60
v_med3_u32 v97, v96, v97, v60
v_max_u32 v96, v96, v60
v_med3_u32 v98, v97, v98, v62
v_med3_u32 v97, v96, v97, v62
v_max_u32 v96, v96, v62
v_med3_u32 v98, v97, v98, v64
v_med3_u32 v97, v96, v97, v64
v_max_u32 v96, v96, v64
v_med3_u32 v98, v97, v98, v66
v_med3_u32 v97, v96, v97, v66
v_max_u32 v96, v96, v66
v_med3_u32 v98, v97, v98, v68
v_med3_u32 v97, v96, v97, v68
v_max_u32 v96, v96, v68
v_med3_u32 v98, v97, v98, v70
v_med3_u32 v97, v96, v97, v70
v_max_u32 v96, v96, v70
v_med3_u32 v98, v97, v98, v72
v_med3_u32 v97, v96, v97, v72
v_max_u32 v96, v96, v72
v_med3_u32 v98, v97, v98, v74
v_med3_u32 v97, v96, v97, v74
v_max_u32 v96, v96, v74
v_med3_u32 v98, v97, v98, v76
v_med3_u32 v97, v96, v97, v76
v_max_u32 v96, v96, v76
v_med3_u32 v98, v97, v98, v78
v_med3_u32 v97, v96, v97, v78
v_max_u32 v96, v96, v78
v_med3_u32 v98, v97, v98, v80
v_med3_u32 v97, v96, v97, v80
v_max_u32 v96, v96, v80
v_med3_u32 v98, v97, v98, v82
v_med3_u32 v97, v96, v97, v82
v_max_u32 v96, v96, v82
v_med3_u32 v98, v97, v98, v84
v_med3_u32 v97, v96, v97, v84
v_max_u32 v96, v96, v84
v_med3_u32 v98, v97, v98, v86
v_med3_u32 v97, v96, v97, v86
v_max_u32 v96, v96, v86
v_med3_u32 v98, v97, v98, v88
v_med3_u32 v97, v96, v97, v88
v_max_u32 v96, v96, v88
v_med3_u32 v98, v97, v98, v90
)"
R"(v_med3_u32 v97, v96, v97, v90
v_max_u32 v96, v96, v90
v_med3_u32 v98, v97, v98, v92
v_med3_u32 v97, v96, v97, v92
v_max_u32 v96, v96, v92
v_med3_u32 v98, v97, v98, v94
v_med3_u32 v97, v96, v97, v94
v_max_u32 v96, v96, v94
v_mov_b32 v99, v96
s_nop 1
v_max_u32_dpp v99, v99, v99 row_shr:1 row_mask:0xf bank_mask:0xf
s_nop 1
v_max_u32_dpp v99, v99, v99 row_shr:2 row_mask:0xf bank_mask:0xf
s_nop 1
v_max_u32_dpp v99, v99, v99 row_shr:4 row_mask:0xf bank_mask:0xf
s_nop 1
v_max_u32_dpp v99, v99, v99 row_shr:8 row_mask:0xf bank_mask:0xf
s_nop 1
v_max_u32_dpp v99, v99, v99 row_bcast:15 row_mask:0xa bank_mask:0xf
s_nop 1
v_max_u32_dpp v99, v99, v99 row_bcast:31 row_mask:0xc bank_mask:0xf
s_nop 0
v_readlane_b32 s86, v99, 63
s_nop 1
v_cmp_eq_u32 vcc, s86, v96
s_and_b32 s87, s86, 0xffff
s_sub_u32 s87, 2047, s87
v_cndmask_b32 v96, v96, v97, vcc
v_cndmask_b32_e64 v97, v97, 0, vcc
v_writelane_b32 v100, s87, 0
v_mov_b32 v99, v96
s_nop 1
v_max_u32_dpp v99, v99, v99 row_shr:1 row_mask:0xf bank_mask:0xf
s_nop 1
v_max_u32_dpp v99, v99, v99 row_shr:2 row_mask:0xf bank_mask:0xf
s_nop 1
v_max_u32_dpp v99, v99, v99 row_shr:4 row_mask:0xf bank_mask:0xf
s_nop 1
v_max_u32_dpp v99, v99, v99 row_shr:8 row_mask:0xf bank_mask:0xf
s_nop 1
v_max_u32_dpp v99, v99, v99 row_bcast:15 row_mask:0xa bank_mask:0xf
s_nop 1
v_max_u32_dpp v99, v99, v99 row_bcast:31 row_mask:0xc bank_mask:0xf
s_nop 0
v_readlane_b32 s86, v99, 63
s_nop 1
v_cmp_eq_u32 vcc, s86, v96
s_and_b32 s87, s86, 0xffff
s_sub_u32 s87, 2047, s87
v_cndmask_b32 v96, v96, v97, vcc
v_cndmask_b32_e64 v97, v97, 0, vcc
v_writelane_b32 v100, s87, 1
v_mov_b32 v99, v96
s_nop 1
v_max_u32_dpp v99, v99, v99 row_shr:1 row_mask:0xf bank_mask:0xf
s_nop 1
v_max_u32_dpp v99, v99, v99 row_shr:2 row_mask:0xf bank_mask:0xf
s_nop 1
v_max_u32_dpp v99, v99, v99 row_shr:4 row_mask:0xf bank_mask:0xf
s_nop 1
v_max_u32_dpp v99, v99, v99 row_shr:8 row_mask:0xf bank_mask:0xf
s_nop 1
v_max_u32_dpp v99, v99, v99 row_bcast:15 row_mask:0xa bank_mask:0xf
s_nop 1
v_max_u32_dpp v99, v99, v99 row_bcast:31 row_mask:0xc bank_mask:0xf
s_nop 0
v_readlane_b32 s86, v99, 63
s_nop 1
v_cmp_eq_u32 vcc, s86, v96
s_and_b32 s87, s86, 0xffff
s_sub_u32 s87, 2047, s87
v_cndmask_b32 v96, v96, v97, vcc
v_cndmask_b32_e64 v97, v97, 0, vcc
v_writelane_b32 v100, s87, 2
v_mov_b32 v99, v96
s_nop 1
v_max_u32_dpp v99, v99, v99 row_shr:1 row_mask:0xf bank_mask:0xf
s_nop 1
v_max_u32_dpp v99, v99, v99 row_shr:2 row_mask:0xf bank_mask:0xf
s_nop 1
v_max_u32_dpp v99, v99, v99 row_shr:4 row_mask:0xf bank_mask:0xf
s_nop 1
v_max_u32_dpp v99, v99, v99 row_shr:8 row_mask:0xf bank_mask:0xf
s_nop 1
v_max_u32_dpp v99, v99, v99 row_bcast:15 row_mask:0xa bank_mask:0xf
s_nop 1
v_max_u32_dpp v99, v99, v99 row_bcast:31 row_mask:0xc bank_mask:0xf
s_nop 0
v_readlane_b32 s86, v99, 63
s_nop 1
v_cmp_eq_u32 vcc, s86, v96
s_and_b32 s87, s86, 0xffff
s_sub_u32 s87, 2047, s87
v_cndmask_b32 v96, v96, v97, vcc
v_cndmask_b32_e64 v97, v97, 0, vcc
v_writelane_b32 v100, s87, 3
v_mov_b32 v99, v96
s_nop 1
v_max_u32_dpp v99, v99, v99 row_shr:1 row_mask:0xf bank_mask:0xf
s_nop 1
v_max_u32_dpp v99, v99, v99 row_shr:2 row_mask:0xf bank_mask:0xf
s_nop 1
v_max_u32_dpp v99, v99, v99 row_shr:4 row_mask:0xf bank_mask:0xf
s_nop 1
v_max_u32_dpp v99, v99, v99 row_shr:8 row_mask:0xf bank_mask:0xf
s_nop 1
v_max_u32_dpp v99, v99, v99 row_bcast:15 row_mask:0xa bank_mask:0xf
s_nop 1
v_max_u32_dpp v99, v99, v99 row_bcast:31 row_mask:0xc bank_mask:0xf
s_nop 0
v_readlane_b32 s86, v99, 63
s_nop 1
v_cmp_eq_u32 vcc, s86, v96
s_and_b32 s87, s86, 0xffff
s_sub_u32 s87, 2047, s87
v_cndmask_b32 v96, v96, v97, vcc
v_cndmask_b32_e64 v97, v97, 0, vcc
v_writelane_b32 v100, s87, 4
v_mov_b32 v99, v96
s_nop 1
v_max_u32_dpp v99, v99, v99 row_shr:1 row_mask:0xf bank_mask:0xf
s_nop 1
v_max_u32_dpp v99, v99, v99 row_shr:2 row_mask:0xf bank_mask:0xf
s_nop 1
v_max_u32_dpp v99, v99, v99 row_shr:4 row_mask:0xf bank_mask:0xf
s_nop 1
v_max_u32_dpp v99, v99, v99 row_shr:8 row_mask:0xf bank_mask:0xf
s_nop 1
v_max_u32_dpp v99, v99, v99 row_bcast:15 row_mask:0xa bank_mask:0xf
s_nop 1
v_max_u32_dpp v99, v99, v99 row_bcast:31 row_mask:0xc bank_mask:0xf
s_nop 0
v_readlane_b32 s86, v99, 63
s_nop 1
v_cmp_eq_u32 vcc, s86, v96
s_and_b32 s87, s86, 0xffff
s_sub_u32 s87, 2047, s87
v_cndmask_b32 v96, v96, v97, vcc
v_cndmask_b32_e64 v97, v97, 0, vcc
v_writelane_b32 v100, s87, 5
v_mov_b32 v99, v96
s_nop 1
v_max_u32_dpp v99, v99, v99 row_shr:1 row_mask:0xf bank_mask:0xf
s_nop 1
v_max_u32_dpp v99, v99, v99 row_shr:2 row_mask:0xf bank_mask:0xf
s_nop 1
v_max_u32_dpp v99, v99, v99 row_shr:4 row_mask:0xf bank_mask:0xf
s_nop 1
v_max_u32_dpp v99, v99, v99 row_shr:8 row_mask:0xf bank_mask:0xf
s_nop 1
v_max_u32_dpp v99, v99, v99 row_bcast:15 row_mask:0xa bank_mask:0xf
s_nop 1
v_max_u32_dpp v99, v99, v99 row_bcast:31 row_mask:0xc bank_mask:0xf
s_nop 0
v_readlane_b32 s86, v99, 63
s_nop 1
v_cmp_eq_u32 vcc, s86, v96
s_and_b32 s87, s86, 0xffff
s_sub_u32 s87, 2047, s87
v_cndmask_b32 v96, v96, v97, vcc
v_cndmask_b32_e64 v97, v97, 0, vcc
v_writelane_b32 v100, s87, 6
v_mov_b32 v99, v96
s_nop 1
v_max_u32_dpp v99, v99, v99 row_shr:1 row_mask:0xf bank_mask:0xf
s_nop 1
v_max_u32_dpp v99, v99, v99 row_shr:2 row_mask:0xf bank_mask:0xf
s_nop 1
v_max_u32_dpp v99, v99, v99 row_shr:4 row_mask:0xf bank_mask:0xf
s_nop 1
v_max_u32_dpp v99, v99, v99 row_shr:8 row_mask:0xf bank_mask:0xf
s_nop 1
v_max_u32_dpp v99, v99, v99 row_bcast:15 row_mask:0xa bank_mask:0xf
s_nop 1
v_max_u32_dpp v99, v99, v99 row_bcast:31 row_mask:0xc bank_mask:0xf
s_nop 0
v_readlane_b32 s86, v99, 63
s_nop 1
v_cmp_eq_u32 vcc, s86, v96
s_and_b32 s87, s86, 0xffff
s_sub_u32 s87, 2047, s87
v_cndmask_b32 v96, v96, v97, vcc
v_cndmask_b32_e64 v97, v97, 0, vcc
v_writelane_b32 v100, s87, 7
v_mov_b32 v99, v98
s_nop 1
v_max_u32_dpp v99, v99, v99 row_shr:1 row_mask:0xf bank_mask:0xf
s_nop 1
v_max_u32_dpp v99, v99, v99 row_shr:2 row_mask:0xf bank_mask:0xf
s_nop 1
v_max_u32_dpp v99, v99, v99 row_shr:4 row_mask:0xf bank_mask:0xf
s_nop 1
v_max_u32_dpp v99, v99, v99 row_shr:8 row_mask:0xf bank_mask:0xf
s_nop 1
v_max_u32_dpp v99, v99, v99 row_bcast:15 row_mask:0xa bank_mask:0xf
s_nop 1
v_max_u32_dpp v99, v99, v99 row_bcast:31 row_mask:0xc bank_mask:0xf
s_nop 0
v_readlane_b32 s87, v99, 63
s_nop 0
s_cmp_gt_u32 s87, s86
s_cbranch_scc0 .LL_27%=
s_mov_b32 m0, 0
.LL_28%=:
v_max3_u32 v96, v32, v34, v36
v_max3_u32 v96, v96, v38, v40
v_max3_u32 v96, v96, v42, v44
v_max3_u32 v96, v96, v46, v48
v_max3_u32 v96, v96, v50, v52
v_max3_u32 v96, v96, v54, v56
v_max3_u32 v96, v96, v58, v60
v_max3_u32 v96, v96, v62, v64
v_max3_u32 v96, v96, v66, v68
v_max3_u32 v96, v96, v70, v72
v_max3_u32 v96, v96, v74, v76
v_max3_u32 v96, v96, v78, v80
v_max3_u32 v96, v96, v82, v84
v_max3_u32 v96, v96, v86, v88
v_max3_u32 v96, v96, v90, v92
v_max_u32 v96, v96, v94
v_mov_b32 v99, v96
s_nop 1
v_max_u32_dpp v99, v99, v99 row_shr:1 row_mask:0xf bank_mask:0xf
s_nop 1
v_max_u32_dpp v99, v99, v99 row_shr:2 row_mask:0xf bank_mask:0xf
s_nop 1
v_max_u32_dpp v99, v99, v99 row_shr:4 row_mask:0xf bank_mask:0xf
s_nop 1
v_max_u32_dpp v99, v99, v99 row_shr:8 row_mask:0xf bank_mask:0xf
s_nop 1
v_max_u32_dpp v99, v99, v99 row_bcast:15 row_mask:0xa bank_mask:0xf
s_nop 1
v_max_u32_dpp v99, v99, v99 row_bcast:31 row_mask:0xc bank_mask:0xf
s_nop 0
v_readlane_b32 s86, v99, 63
s_nop 1
v_cmp_eq_u32 vcc, s86, v32
s_nop 1
v_cndmask_b32_e64 v32, v32, 0, vcc
v_cmp_eq_u32 vcc, s86, v34
s_nop 1
v_cndmask_b32_e64 v34, v34, 0, vcc
v_cmp_eq_u32 vcc, s86, v36
s_nop 1
v_cndmask_b32_e64 v36, v36, 0, vcc
v_cmp_eq_u32 vcc, s86, v38
s_nop 1
v_cndmask_b32_e64 v38, v38, 0, vcc
v_cmp_eq_u32 vcc, s86, v40
s_nop 1
v_cndmask_b32_e64 v40, v40, 0, vcc
v_cmp_eq_u32 vcc, s86, v42
s_nop 1
v_cndmask_b32_e64 v42, v42, 0, vcc
v_cmp_eq_u32 vcc, s86, v44
s_nop 1
v_cndmask_b32_e64 v44, v44, 0, vcc
v_cmp_eq_u32 vcc, s86, v46
s_nop 1
v_cndmask_b32_e64 v46, v46, 0, vcc
v_cmp_eq_u32 vcc, s86, v48
s_nop 1
v_cndmask_b32_e64 v48, v48, 0, vcc
v_cmp_eq_u32 vcc, s86, v50
s_nop 1
v_cndmask_b32_e64 v50, v50, 0, vcc
v_cmp_eq_u32 vcc, s86, v52
s_nop 1
v_cndmask_b32_e64 v52, v52, 0, vcc
v_cmp_eq_u32 vcc, s86, v54
s_nop 1
v_cndmask_b32_e64 v54, v54, 0, vcc
v_cmp_eq_u32 vcc, s86, v56
s_nop 1
v_cndmask_b32_e64 v56, v56, 0, vcc
v_cmp_eq_u32 vcc, s86, v58
s_nop 1
v_cndmask_b32_e64 v58, v58, 0, vcc
v_cmp_eq_u32 vcc, s86, v60
s_nop 1
v_cndmask_b32_e64 v60, v60, 0, vcc
v_cmp_eq_u32 vcc, s86, v62
s_nop 1
v_cndmask_b32_e64 v62, v62, 0, vcc
v_cmp_eq_u32 vcc, s86, v64
s_nop 1
v_cndmask_b32_e64 v64, v64, 0, vcc
v_cmp_eq_u32 vcc, s86, v66
s_nop 1
v_cndmask_b32_e64 v66, v66, 0, vcc
v_cmp_eq_u32 vcc, s86, v68
s_nop 1
v_cndmask_b32_e64 v68, v68, 0, vcc
v_cmp_eq_u32 vcc, s86, v70
s_nop 1
v_cndmask_b32_e64 v70, v70, 0, vcc
v_cmp_eq_u32 vcc, s86, v72
s_nop 1
v_cndmask_b32_e64 v72, v72, 0, vcc
v_cmp_eq_u32 vcc, s86, v74
s_nop 1
v_cndmask_b32_e64 v74, v74, 0, vcc
v_cmp_eq_u32 vcc, s86, v76
s_nop 1
v_cndmask_b32_e64 v76, v76, 0, vcc
v_cmp_eq_u32 vcc, s86, v78
s_nop 1
v_cndmask_b32_e64 v78, v78, 0, vcc
v_cmp_eq_u32 vcc, s86, v80
s_nop 1
v_cndmask_b32_e64 v80, v80, 0, vcc
v_cmp_eq_u32 vcc, s86, v82
s_nop 1
v_cndmask_b32_e64 v82, v82, 0, vcc
v_cmp_eq_u32 vcc, s86, v84
s_nop 1
v_cndmask_b32_e64 v84, v84, 0, vcc
v_cmp_eq_u32 vcc, s86, v86
s_nop 1
v_cndmask_b32_e64 v86, v86, 0, vcc
v_cmp_eq_u32 vcc, s86, v88
s_nop 1
v_cndmask_b32_e64 v88, v88, 0, vcc
v_cmp_eq_u32 vcc, s86, v90
s_nop 1
v_cndmask_b32_e64 v90, v90, 0, vcc
v_cmp_eq_u32 vcc, s86, v92
s_nop 1
v_cndmask_b32_e64 v92, v92, 0, vcc
v_cmp_eq_u32 vcc, s86, v94
s_nop 1
v_cndmask_b32_e64 v94, v94, 0, vcc
s_and_b32 s87, s86, 0xffff
s_sub_u32 s87, 2047, s87
s_nop 0
v_writelane_b32 v100, s87, m0
s_add_u32 m0, m0, 1
s_cmp_lt_u32 m0, 8
s_cbranch_scc1 .LL_28%=
.LL_27%=:
v_xor_b32 v102, s12, v100
v_mov_b32 v103, s13
s_mov_b64 exec, 0xff
buffer_store_dwordx2 v[102:103], v136, s[8:11], 0 offen offset:3584 sc1
s_mov_b64 exec, -1
s_branch .Lrtr_24%=
.Lspin_49%=:
s_mov_b32 s84, 0
.Llp_51%=:
s_sleep 2
buffer_load_dwordx2 v[8:9], v11, s[8:11], 0 offen offset:3840 sc1
s_waitcnt vmcnt(0)
s_add_u32 s84, s84, 1
v_cmp_eq_u64 vcc, s[12:13], v[8:9]
s_cmp_eq_u64 vcc, -1
s_cbranch_scc1 .Lok_50%=
s_cmp_lt_u32 s84, 1048576
s_cbranch_scc1 .Llp_51%=
s_branch .Lok_50%=
.Lredo%=:
s_waitcnt vmcnt(0) lgkmcnt(0)
s_barrier
s_and_b32 s81, s14, 1
s_mul_i32 s91, s81, 17164288
s_mul_i32 s82, s56, 536576
s_add_u32 s91, s91, s82
s_add_u32 s91, s91, 4194304
s_lshr_b32 s82, s14, 1
s_lshl_b32 s83, s82, 14
s_add_u32 s91, s91, s83
s_lshl_b32 s81, s81, 14
s_lshl_b32 s82, s82, 16
s_or_b32 s92, s81, s82
s_xor_b32 s92, s92, 0x10000
s_add_u32 m0, s92, 0
s_add_u32 s81, s91, 0
Dc 1,8,81,0
Dc 1,8,81,1024
Dc 1,8,81,2048
Dc 1,8,81,3072
s_add_u32 m0, s92, 4096
s_add_u32 s81, s91, 4096
Dc 1,8,81,0
Dc 1,8,81,1024
Dc 1,8,81,2048
Dc 1,8,81,3072
s_add_u32 m0, s92, 8192
s_add_u32 s81, s91, 8192
Dc 1,8,81,0
Dc 1,8,81,1024
Dc 1,8,81,2048
Dc 1,8,81,3072
s_add_u32 m0, s92, 12288
s_add_u32 s81, s91, 12288
Dc 1,8,81,0
Dc 1,8,81,1024
Dc 1,8,81,2048
Dc 1,8,81,3072
v_bfe_u32 v244, v1, 4, 1
v_mul_u32_u24 v244, 0xaaaa, v244
)"
R"(v_add_u32 v244, 0x4444, v244
v_lshl_or_b32 v240, v244, 16, v244
v_mov_b32 v241, 0xff800000
v_add_u32 v242, 0x10000, v1
v_bfe_u32 v244, v1, 4, 4
v_lshrrev_b32 v245, 8, v1
v_lshlrev_b32 v245, 2, v245
v_sub_u32 v244, v244, v245
v_cmp_le_i32_e64 s[72:73], 0, v244
v_cmp_le_i32_e64 s[74:75], 1, v244
v_cmp_le_i32_e64 s[76:77], 2, v244
v_cmp_le_i32_e64 s[78:79], 3, v244
v_add_u32 v243, 4096, v1
v_add_u32 v244, 8192, v1
v_add_u32 v245, 12288, v1
s_mov_b32 s18, 0x3f803f80
s_mov_b32 s19, 0x3f003f00
v_accvgpr_write_b32 a176, s18
v_accvgpr_write_b32 a180, s19
v_mov_b32 v252, v241
v_accvgpr_write_b32 a177, s18
v_accvgpr_write_b32 a181, s19
v_mov_b32 v253, v241
v_accvgpr_write_b32 a178, s18
v_accvgpr_write_b32 a182, s19
v_mov_b32 v254, v241
v_accvgpr_write_b32 a179, s18
v_accvgpr_write_b32 a183, s19
v_mov_b32 v255, v241
v_accvgpr_write_b32 a160, 0
v_accvgpr_write_b32 a161, 0
v_accvgpr_write_b32 a162, 0
v_accvgpr_write_b32 a163, 0
v_accvgpr_write_b32 a164, 0
v_accvgpr_write_b32 a165, 0
v_accvgpr_write_b32 a166, 0
v_accvgpr_write_b32 a167, 0
v_accvgpr_write_b32 a168, 0
v_accvgpr_write_b32 a169, 0
v_accvgpr_write_b32 a170, 0
v_accvgpr_write_b32 a171, 0
v_accvgpr_write_b32 a172, 0
v_accvgpr_write_b32 a173, 0
v_accvgpr_write_b32 a174, 0
v_accvgpr_write_b32 a175, 0
s_mov_b32 s90, 0x5d800000
s_mov_b64 s[88:89], 0
s_mov_b32 s64, 0
s_and_b32 s18, s14, 1
s_lshl_b32 s62, s18, 14
s_lshl_b32 s20, s57, 15
s_sub_u32 s20, 507904, s20
s_add_u32 s20, s20, s62
s_lshl_b32 s65, s57, 1
s_sub_u32 s65, s65, 1
s_max_i32 s65, s65, 0
s_sub_u32 s66, s60, 1
s_cmp_lt_u32 s14, 2
s_cselect_b32 s20, s20, 16384
s_cselect_b32 s66, s66, 15
s_cselect_b32 s21, 32768, 0
s_add_u32 s61, s91, s20
s_add_u32 s62, s62, s21
s_sub_u32 s63, 16384, s21
s_lshl_b32 s67, s60, 7
s_add_u32 s67, s67, s56
s_lshl_b32 s67, s67, 12
s_add_u32 s67, s67, 39845888
s_lshl_b32 s69, s60, 3
s_add_u32 s69, s69, s59
s_lshl_b32 s69, s69, 6
s_lshl_b32 s21, s58, 3
s_add_u32 s69, s69, s21
s_add_u32 s69, s69, 20480
s_mov_b32 s70, 0
s_mov_b32 s37, 0
v_bfe_u32 v28, v1, 4, 4
v_lshrrev_b32 v29, 8, v1
v_lshlrev_b32 v29, 2, v29
v_sub_u32 v28, v28, v29
s_mov_b64 s[24:25], 0
s_mov_b32 s68, 0
s_mov_b32 s85, 99
s_lshl_b32 s80, s60, 6
s_and_b32 s81, s14, 2
s_lshl_b32 s81, s81, 4
s_xor_b32 s82, s81, 0
s_add_u32 s82, s82, s80
v_add_u32 v4, s82, v28
s_xor_b32 s82, s81, 16
s_add_u32 s82, s82, s80
v_add_u32 v5, s82, v28
s_xor_b32 s82, s81, 32
s_add_u32 s82, s82, s80
v_add_u32 v6, s82, v28
s_xor_b32 s82, s81, 48
s_add_u32 s82, s82, s80
v_add_u32 v7, s82, v28
s_mul_i32 s82, s56, 536576
s_add_u32 s82, s82, 4194304
s_lshl_b32 s84, s60, 1
s_add_u32 s84, s84, 2
.LL_52%=:
buffer_load_dwordx4 a[128:131], v1, s[8:11], s82 offen offset:0 sc1
buffer_load_dwordx4 a[132:135], v1, s[8:11], s82 offen offset:1024 sc1
buffer_load_dwordx4 a[136:139], v1, s[8:11], s82 offen offset:2048 sc1
buffer_load_dwordx4 a[140:143], v1, s[8:11], s82 offen offset:3072 sc1
buffer_load_dwordx4 a[144:147], v243, s[8:11], s82 offen offset:0 sc1
buffer_load_dwordx4 a[148:151], v243, s[8:11], s82 offen offset:1024 sc1
buffer_load_dwordx4 a[152:155], v243, s[8:11], s82 offen offset:2048 sc1
buffer_load_dwordx4 a[156:159], v243, s[8:11], s82 offen offset:3072 sc1
s_waitcnt vmcnt(0)
Zvaa 160,128,0
Zvaa 164,128,16
Zvaa 168,128,32
Zvaa 172,128,48
Mvaa 160,132,4
Mvaa 164,132,20
Mvaa 168,132,36
Mvaa 172,132,52
Mvaa 160,136,8
Mvaa 164,136,24
Mvaa 168,136,40
Mvaa 172,136,56
Mvaa 160,140,12
Mvaa 164,140,28
Mvaa 168,140,44
Mvaa 172,140,60
Zvaa 176,144,0
Zvaa 180,144,16
Zvaa 184,144,32
Zvaa 188,144,48
Mvaa 176,148,4
Mvaa 180,148,20
Mvaa 184,148,36
Mvaa 188,148,52
Mvaa 176,152,8
Mvaa 180,152,24
Mvaa 184,152,40
Mvaa 188,152,56
Mvaa 176,156,12
Mvaa 180,156,28
Mvaa 184,156,44
Mvaa 188,156,60
v_cmp_le_i32 vcc, 0, v4
s_nop 1
v_cndmask_b32 v160, v241, v160, vcc
v_max_f32 v252, v252, v160
v_cmp_le_i32 vcc, 1, v4
s_nop 1
v_cndmask_b32 v161, v241, v161, vcc
v_max_f32 v252, v252, v161
v_cmp_le_i32 vcc, 2, v4
s_nop 1
v_cndmask_b32 v162, v241, v162, vcc
v_max_f32 v252, v252, v162
v_cmp_le_i32 vcc, 3, v4
s_nop 1
v_cndmask_b32 v163, v241, v163, vcc
v_max_f32 v252, v252, v163
v_cmp_le_i32 vcc, 0, v5
s_nop 1
v_cndmask_b32 v164, v241, v164, vcc
v_max_f32 v253, v253, v164
v_cmp_le_i32 vcc, 1, v5
s_nop 1
v_cndmask_b32 v165, v241, v165, vcc
v_max_f32 v253, v253, v165
v_cmp_le_i32 vcc, 2, v5
s_nop 1
v_cndmask_b32 v166, v241, v166, vcc
v_max_f32 v253, v253, v166
v_cmp_le_i32 vcc, 3, v5
s_nop 1
v_cndmask_b32 v167, v241, v167, vcc
v_max_f32 v253, v253, v167
v_cmp_le_i32 vcc, 0, v6
s_nop 1
v_cndmask_b32 v168, v241, v168, vcc
v_max_f32 v254, v254, v168
v_cmp_le_i32 vcc, 1, v6
s_nop 1
v_cndmask_b32 v169, v241, v169, vcc
v_max_f32 v254, v254, v169
v_cmp_le_i32 vcc, 2, v6
s_nop 1
v_cndmask_b32 v170, v241, v170, vcc
v_max_f32 v254, v254, v170
v_cmp_le_i32 vcc, 3, v6
s_nop 1
v_cndmask_b32 v171, v241, v171, vcc
v_max_f32 v254, v254, v171
v_cmp_le_i32 vcc, 0, v7
s_nop 1
v_cndmask_b32 v172, v241, v172, vcc
v_max_f32 v255, v255, v172
v_cmp_le_i32 vcc, 1, v7
s_nop 1
v_cndmask_b32 v173, v241, v173, vcc
v_max_f32 v255, v255, v173
v_cmp_le_i32 vcc, 2, v7
s_nop 1
v_cndmask_b32 v174, v241, v174, vcc
v_max_f32 v255, v255, v174
v_cmp_le_i32 vcc, 3, v7
s_nop 1
v_cndmask_b32 v175, v241, v175, vcc
v_max_f32 v255, v255, v175
v_cmp_le_i32 vcc, 16, v4
s_nop 1
v_cndmask_b32 v176, v241, v176, vcc
v_max_f32 v252, v252, v176
v_cmp_le_i32 vcc, 17, v4
s_nop 1
v_cndmask_b32 v177, v241, v177, vcc
v_max_f32 v252, v252, v177
v_cmp_le_i32 vcc, 18, v4
s_nop 1
v_cndmask_b32 v178, v241, v178, vcc
v_max_f32 v252, v252, v178
v_cmp_le_i32 vcc, 19, v4
s_nop 1
v_cndmask_b32 v179, v241, v179, vcc
v_max_f32 v252, v252, v179
v_cmp_le_i32 vcc, 16, v5
s_nop 1
v_cndmask_b32 v180, v241, v180, vcc
v_max_f32 v253, v253, v180
v_cmp_le_i32 vcc, 17, v5
s_nop 1
v_cndmask_b32 v181, v241, v181, vcc
v_max_f32 v253, v253, v181
v_cmp_le_i32 vcc, 18, v5
s_nop 1
v_cndmask_b32 v182, v241, v182, vcc
v_max_f32 v253, v253, v182
v_cmp_le_i32 vcc, 19, v5
s_nop 1
v_cndmask_b32 v183, v241, v183, vcc
v_max_f32 v253, v253, v183
v_cmp_le_i32 vcc, 16, v6
s_nop 1
v_cndmask_b32 v184, v241, v184, vcc
v_max_f32 v254, v254, v184
v_cmp_le_i32 vcc, 17, v6
s_nop 1
v_cndmask_b32 v185, v241, v185, vcc
v_max_f32 v254, v254, v185
v_cmp_le_i32 vcc, 18, v6
s_nop 1
v_cndmask_b32 v186, v241, v186, vcc
v_max_f32 v254, v254, v186
v_cmp_le_i32 vcc, 19, v6
s_nop 1
v_cndmask_b32 v187, v241, v187, vcc
v_max_f32 v254, v254, v187
v_cmp_le_i32 vcc, 16, v7
s_nop 1
v_cndmask_b32 v188, v241, v188, vcc
v_max_f32 v255, v255, v188
v_cmp_le_i32 vcc, 17, v7
s_nop 1
v_cndmask_b32 v189, v241, v189, vcc
v_max_f32 v255, v255, v189
v_cmp_le_i32 vcc, 18, v7
s_nop 1
v_cndmask_b32 v190, v241, v190, vcc
v_max_f32 v255, v255, v190
v_cmp_le_i32 vcc, 19, v7
s_nop 1
v_cndmask_b32 v191, v241, v191, vcc
v_max_f32 v255, v255, v191
v_subrev_u32 v4, 32, v4
v_subrev_u32 v5, 32, v5
v_subrev_u32 v6, 32, v6
v_subrev_u32 v7, 32, v7
s_add_u32 s82, s82, 8192
s_sub_u32 s84, s84, 1
s_cmp_lg_u32 s84, 0
s_cbranch_scc1 .LL_52%=
G 28,0
v_lshlrev_b32 v32, 16, v28
v_and_b32 v33, 0xffff0000, v28
G 28,1
v_lshlrev_b32 v34, 16, v28
v_and_b32 v35, 0xffff0000, v28
G 28,2
v_lshlrev_b32 v36, 16, v28
v_and_b32 v37, 0xffff0000, v28
G 28,3
v_lshlrev_b32 v38, 16, v28
v_and_b32 v39, 0xffff0000, v28
G 28,4
v_lshlrev_b32 v40, 16, v28
v_and_b32 v41, 0xffff0000, v28
G 28,5
v_lshlrev_b32 v42, 16, v28
v_and_b32 v43, 0xffff0000, v28
G 28,6
v_lshlrev_b32 v44, 16, v28
v_and_b32 v45, 0xffff0000, v28
G 28,7
v_lshlrev_b32 v46, 16, v28
v_and_b32 v47, 0xffff0000, v28
G 28,8
v_lshlrev_b32 v48, 16, v28
v_and_b32 v49, 0xffff0000, v28
G 28,9
v_lshlrev_b32 v50, 16, v28
v_and_b32 v51, 0xffff0000, v28
G 28,10
v_lshlrev_b32 v52, 16, v28
v_and_b32 v53, 0xffff0000, v28
G 28,11
v_lshlrev_b32 v54, 16, v28
v_and_b32 v55, 0xffff0000, v28
G 28,12
v_lshlrev_b32 v56, 16, v28
v_and_b32 v57, 0xffff0000, v28
G 28,13
v_lshlrev_b32 v58, 16, v28
v_and_b32 v59, 0xffff0000, v28
G 28,14
v_lshlrev_b32 v60, 16, v28
v_and_b32 v61, 0xffff0000, v28
G 28,15
v_lshlrev_b32 v62, 16, v28
v_and_b32 v63, 0xffff0000, v28
G 28,16
v_lshlrev_b32 v64, 16, v28
v_and_b32 v65, 0xffff0000, v28
G 28,17
v_lshlrev_b32 v66, 16, v28
v_and_b32 v67, 0xffff0000, v28
G 28,18
v_lshlrev_b32 v68, 16, v28
v_and_b32 v69, 0xffff0000, v28
G 28,19
v_lshlrev_b32 v70, 16, v28
v_and_b32 v71, 0xffff0000, v28
G 28,20
v_lshlrev_b32 v72, 16, v28
v_and_b32 v73, 0xffff0000, v28
G 28,21
v_lshlrev_b32 v74, 16, v28
v_and_b32 v75, 0xffff0000, v28
G 28,22
v_lshlrev_b32 v76, 16, v28
v_and_b32 v77, 0xffff0000, v28
G 28,23
v_lshlrev_b32 v78, 16, v28
v_and_b32 v79, 0xffff0000, v28
G 28,24
v_lshlrev_b32 v80, 16, v28
v_and_b32 v81, 0xffff0000, v28
G 28,25
v_lshlrev_b32 v82, 16, v28
v_and_b32 v83, 0xffff0000, v28
G 28,26
v_lshlrev_b32 v84, 16, v28
v_and_b32 v85, 0xffff0000, v28
G 28,27
v_lshlrev_b32 v86, 16, v28
v_and_b32 v87, 0xffff0000, v28
G 28,28
v_lshlrev_b32 v88, 16, v28
v_and_b32 v89, 0xffff0000, v28
G 28,29
v_lshlrev_b32 v90, 16, v28
v_and_b32 v91, 0xffff0000, v28
G 28,30
v_lshlrev_b32 v92, 16, v28
v_and_b32 v93, 0xffff0000, v28
G 28,31
v_lshlrev_b32 v94, 16, v28
v_and_b32 v95, 0xffff0000, v28
G 28,32
v_lshlrev_b32 v96, 16, v28
v_and_b32 v97, 0xffff0000, v28
G 28,33
v_lshlrev_b32 v98, 16, v28
v_and_b32 v99, 0xffff0000, v28
G 28,34
v_lshlrev_b32 v100, 16, v28
v_and_b32 v101, 0xffff0000, v28
G 28,35
v_lshlrev_b32 v102, 16, v28
v_and_b32 v103, 0xffff0000, v28
G 28,36
v_lshlrev_b32 v104, 16, v28
v_and_b32 v105, 0xffff0000, v28
G 28,37
v_lshlrev_b32 v106, 16, v28
v_and_b32 v107, 0xffff0000, v28
G 28,38
v_lshlrev_b32 v108, 16, v28
v_and_b32 v109, 0xffff0000, v28
G 28,39
v_lshlrev_b32 v110, 16, v28
v_and_b32 v111, 0xffff0000, v28
)"
R"(G 28,40
v_lshlrev_b32 v112, 16, v28
v_and_b32 v113, 0xffff0000, v28
G 28,41
v_lshlrev_b32 v114, 16, v28
v_and_b32 v115, 0xffff0000, v28
G 28,42
v_lshlrev_b32 v116, 16, v28
v_and_b32 v117, 0xffff0000, v28
G 28,43
v_lshlrev_b32 v118, 16, v28
v_and_b32 v119, 0xffff0000, v28
G 28,44
v_lshlrev_b32 v120, 16, v28
v_and_b32 v121, 0xffff0000, v28
G 28,45
v_lshlrev_b32 v122, 16, v28
v_and_b32 v123, 0xffff0000, v28
G 28,46
v_lshlrev_b32 v124, 16, v28
v_and_b32 v125, 0xffff0000, v28
G 28,47
v_lshlrev_b32 v126, 16, v28
v_and_b32 v127, 0xffff0000, v28
G 28,48
v_lshlrev_b32 v128, 16, v28
v_and_b32 v129, 0xffff0000, v28
G 28,49
v_lshlrev_b32 v130, 16, v28
v_and_b32 v131, 0xffff0000, v28
G 28,50
v_lshlrev_b32 v132, 16, v28
v_and_b32 v133, 0xffff0000, v28
G 28,51
v_lshlrev_b32 v134, 16, v28
v_and_b32 v135, 0xffff0000, v28
G 28,52
v_lshlrev_b32 v136, 16, v28
v_and_b32 v137, 0xffff0000, v28
G 28,53
v_lshlrev_b32 v138, 16, v28
v_and_b32 v139, 0xffff0000, v28
G 28,54
v_lshlrev_b32 v140, 16, v28
v_and_b32 v141, 0xffff0000, v28
G 28,55
v_lshlrev_b32 v142, 16, v28
v_and_b32 v143, 0xffff0000, v28
G 28,56
v_lshlrev_b32 v144, 16, v28
v_and_b32 v145, 0xffff0000, v28
G 28,57
v_lshlrev_b32 v146, 16, v28
v_and_b32 v147, 0xffff0000, v28
G 28,58
v_lshlrev_b32 v148, 16, v28
v_and_b32 v149, 0xffff0000, v28
G 28,59
v_lshlrev_b32 v150, 16, v28
v_and_b32 v151, 0xffff0000, v28
G 28,60
v_lshlrev_b32 v152, 16, v28
v_and_b32 v153, 0xffff0000, v28
G 28,61
v_lshlrev_b32 v154, 16, v28
v_and_b32 v155, 0xffff0000, v28
G 28,62
v_lshlrev_b32 v156, 16, v28
v_and_b32 v157, 0xffff0000, v28
G 28,63
v_lshlrev_b32 v158, 16, v28
v_and_b32 v159, 0xffff0000, v28
s_mov_b32 s22, 1.0
s_mov_b32 s23, 1.0
s_mov_b32 s71, 3584
s_barrier
Ra 128,242,0
Ra 132,242,1024
Ra 136,242,2048
Ra 140,242,3072
s_cmp_ge_u32 s14, 2
s_cbranch_scc1 .Lbgo%=
s_branch .Lago%=
.Lpb_29%=:
s_waitcnt vmcnt(4) lgkmcnt(6)
Zvaa 32,0,32
s_add_u32 s28, s28, 512
Ta 20,7,0
Ta 22,7,1024
Zvaa 64,0,36
s_cmp_eq_u32 s28, s4
s_cselect_b32 s93, 10240, 0
Ta 24,8,0
Ta 26,8,1024
Zvaa 96,0,40
s_sub_u32 s28, s28, s93
s_add_u32 s93, s28, 20480
buffer_load_dwordx4 a[128:131], v26, s[8:11], s28 offen offset:0 sc1
Zvaa 128,0,44
Ta 28,9,0
Ta 30,9,1024
Zvaa 132,4,44
K 160,160,161
Zvaa 100,4,40
K 161,162,163
K 162,164,165
Zvaa 68,4,36
buffer_load_dwordx4 a[132:135], v26, s[8:11], s93 offen offset:0 sc1
K 163,166,167
Zvaa 36,4,32
W 8
Ta 0,2,8192
Ta 2,2,9216
Zvaa 40,8,32
K 168,168,169
K 169,170,171
Zvaa 72,8,36
K 170,172,173
K 171,174,175
Zvaa 104,8,40
buffer_load_dwordx4 a[136:139], v27, s[8:11], s28 offen offset:0 sc1
G 164,184
Zvaa 136,8,44
Ta 4,3,8192
G 165,185
Zvaa 140,12,44
Ta 6,3,9216
G 166,186
Zvaa 108,12,40
G 167,187
G 172,188
Zvaa 76,12,36
buffer_load_dwordx4 a[140:143], v27, s[8:11], s93 offen offset:0 sc1
G 173,189
Zvaa 44,12,32
W 8
s_add_u32 s28, s28, 512
s_cmp_eq_u32 s28, s4
s_cselect_b32 s93, 10240, 0
Zvaa 48,16,32
s_sub_u32 s28, s28, s93
s_add_u32 s93, s28, 20480
Ta 8,4,8192
Zvaa 80,16,36
Ta 10,4,9216
G 174,190
Zvaa 112,16,40
buffer_load_dwordx4 a[144:147], v26, s[8:11], s28 offen offset:0 sc1
G 175,191
Zvaa 144,16,44
Ta 12,5,8192
K 164,164,165
Zvaa 148,20,44
Ta 14,5,9216
K 165,166,167
Zvaa 116,20,40
K 166,172,173
K 167,174,175
Zvaa 84,20,36
buffer_load_dwordx4 a[148:151], v26, s[8:11], s93 offen offset:0 sc1
v_accvgpr_write_b32 a184, v164
Zvaa 52,20,32
W 8
Ta 16,6,8192
Ta 18,6,9216
Zvaa 56,24,32
v_accvgpr_write_b32 a185, v165
v_accvgpr_write_b32 a186, v166
Zvaa 88,24,36
v_accvgpr_write_b32 a187, v167
Zvaa 120,24,40
buffer_load_dwordx4 a[152:155], v27, s[8:11], s28 offen offset:0 sc1
Ta 20,7,8192
Zvaa 152,24,44
Ta 22,7,9216
K 184,184,185
Zvaa 156,28,44
K 185,186,187
Zvaa 124,28,40
K 186,188,189
Zvaa 92,28,36
buffer_load_dwordx4 a[156:159], v27, s[8:11], s93 offen offset:0 sc1
K 187,190,191
Zvaa 60,28,32
s_waitcnt vmcnt(8) lgkmcnt(8)
Ta 24,8,8192
Ta 26,8,9216
Mvaa 32,0,48
s_add_u32 s28, s28, 512
s_cmp_eq_u32 s28, s4
s_cselect_b32 s93, 10240, 0
v_accvgpr_write_b32 a188, v184
Mvaa 64,0,52
s_sub_u32 s28, s28, s93
s_add_u32 s93, s28, 20480
v_accvgpr_write_b32 a189, v185
Mvaa 96,0,56
D 16,48,65,0
v_accvgpr_write_b32 a190, v186
Mvaa 128,0,60
Ta 28,9,8192
v_accvgpr_write_b32 a191, v187
Mvaa 132,4,60
Ta 30,9,9216
G 164,192
Mvaa 100,4,56
G 165,193
Mvaa 68,4,52
D 17,48,65,1024
G 166,194
Mvaa 36,4,48
W 8
Ta 0,2,16384
Ta 2,2,17408
Mvaa 40,8,48
G 167,195
G 172,196
Mvaa 72,8,52
G 173,197
Mvaa 104,8,56
D 18,48,65,2048
G 174,198
Mvaa 136,8,60
Ta 4,3,16384
Ta 6,3,17408
Mvaa 140,12,60
G 175,199
K 164,164,165
Mvaa 108,12,56
Mvaa 76,12,52
D 19,48,65,3072
K 165,166,167
Mvaa 44,12,48
W 8
s_add_u32 m0, s63, 36864
Ta 8,4,16384
Mvaa 48,16,48
Ta 10,4,17408
K 166,172,173
Mvaa 80,16,52
K 167,174,175
v_accvgpr_write_b32 a192, v164
Mvaa 112,16,56
buffer_load_dwordx4 a[32:35], v26, s[8:11], s28 offen offset:0 sc1
v_accvgpr_write_b32 a193, v165
Mvaa 144,16,60
Ta 12,5,16384
Ta 14,5,17408
Mvaa 148,20,60
v_accvgpr_write_b32 a194, v166
v_accvgpr_write_b32 a195, v167
Mvaa 116,20,56
K 192,192,193
Mvaa 84,20,52
buffer_load_dwordx4 a[36:39], v26, s[8:11], s93 offen offset:0 sc1
Ta 16,6,16384
Mvaa 52,20,48
W 9
Ta 18,6,17408
K 193,194,195
Mvaa 56,24,48
K 194,196,197
Mvaa 88,24,52
K 195,198,199
Mvaa 120,24,56
buffer_load_dwordx4 a[40:43], v27, s[8:11], s28 offen offset:0 sc1
v_accvgpr_write_b32 a196, v192
Mvaa 152,24,60
Ta 20,7,16384
Ta 22,7,17408
Mvaa 156,28,60
v_accvgpr_write_b32 a197, v193
v_accvgpr_write_b32 a198, v194
Mvaa 124,28,56
v_accvgpr_write_b32 a199, v195
Mvaa 92,28,52
buffer_load_dwordx4 a[44:47], v27, s[8:11], s93 offen offset:0 sc1
Ta 24,8,16384
Mvaa 60,28,48
s_waitcnt vmcnt(12) lgkmcnt(9)
s_add_u32 s28, s28, 512
s_cmp_eq_u32 s28, s4
s_cselect_b32 s93, 10240, 0
Mvaa 32,0,128
s_sub_u32 s28, s28, s93
s_add_u32 s93, s28, 20480
Ta 26,8,17408
Mvaa 64,0,132
G 164,200
G 165,201
Mvaa 96,0,136
D 20,48,65,0
G 166,202
Mvaa 128,0,140
Ta 28,9,16384
G 167,203
Mvaa 132,4,140
Ta 30,9,17408
G 172,204
Mvaa 100,4,136
G 173,205
G 174,206
Mvaa 68,4,132
buffer_load_dwordx4 v[184:187], v26, s[8:11], s28 offen offset:0 sc1
G 175,207
Mvaa 36,4,128
W 8
Ta 0,2,24576
Ta 2,2,25600
Mvaa 40,8,128
K 164,164,165
Mvaa 72,8,132
K 165,166,167
Mvaa 104,8,136
buffer_load_dwordx4 v[188:191], v26, s[8:11], s93 offen offset:0 sc1
K 166,172,173
Mvaa 136,8,140
Ta 4,3,24576
Ta 6,3,25600
Mvaa 140,12,140
K 167,174,175
v_accvgpr_write_b32 a200, v164
Mvaa 108,12,136
v_accvgpr_write_b32 a201, v165
Mvaa 76,12,132
D 21,48,65,1024
v_accvgpr_write_b32 a202, v166
Mvaa 44,12,128
W 8
Ta 8,4,24576
Ta 10,4,25600
Mvaa 48,16,128
v_accvgpr_write_b32 a203, v167
K 240,240,241
Mvaa 80,16,132
Mvaa 112,16,136
D 22,48,65,2048
K 241,242,243
Mvaa 144,16,140
Ta 12,5,24576
Ta 14,5,25600
Mvaa 148,20,140
K 242,244,245
K 243,246,247
Mvaa 116,20,136
v_accvgpr_write_b32 a204, v240
Mvaa 84,20,132
D 23,48,65,3072
v_accvgpr_write_b32 a205, v241
Mvaa 52,20,128
W 8
s_add_u32 s65, s5, 16
s_cmp_ge_u32 s65, 20
s_cselect_b32 s68, 20, 0
Mvaa 56,24,128
s_sub_u32 s65, s65, s68
Ta 16,6,24576
Ta 18,6,25600
Mvaa 88,24,132
v_accvgpr_write_b32 a206, v242
v_accvgpr_write_b32 a207, v243
Mvaa 120,24,136
buffer_load_dwordx4 a[48:51], v27, s[8:11], s28 offen offset:0 sc1
G 164,208
Mvaa 152,24,140
Ta 20,7,24576
Ta 22,7,25600
Mvaa 156,28,140
s_lshl_b32 s65, s65, 18
G 165,209
G 166,210
Mvaa 124,28,136
G 167,211
Mvaa 92,28,132
buffer_load_dwordx4 a[52:55], v27, s[8:11], s93 offen offset:0 sc1
G 172,212
Mvaa 60,28,128
s_waitcnt vmcnt(16) lgkmcnt(8)
s_add_u32 s28, s28, 512
s_cmp_eq_u32 s28, s4
s_cselect_b32 s93, 10240, 0
Mvaa 32,0,144
s_sub_u32 s28, s28, s93
s_add_u32 s93, s28, 20480
Ta 24,8,24576
Mvaa 64,0,148
Ta 26,8,25600
G 173,213
Mvaa 96,0,152
buffer_load_dwordx4 a[56:59], v26, s[8:11], s28 offen offset:0 sc1
G 174,214
Mvaa 128,0,156
s_add_u32 m0, s63, 98304
Ta 28,9,24576
G 175,215
Mvaa 132,4,156
s_lshl_b32 s80, s56, 9
Ta 30,9,25600
K 164,164,165
Mvaa 100,4,152
)"
R"(K 165,166,167
K 166,172,173
Mvaa 68,4,148
s_lshl_b32 s81, s57, 4
buffer_load_dwordx4 a[60:63], v26, s[8:11], s93 offen offset:0 sc1
K 167,174,175
Mvaa 36,4,144
W 8
Ta 0,10,0
Ta 2,10,1024
Mvaa 40,8,144
s_add_u32 s80, s80, s81
v_accvgpr_write_b32 a208, v164
v_accvgpr_write_b32 a209, v165
Mvaa 72,8,148
s_and_b32 s81, s14, 1
v_accvgpr_write_b32 a210, v166
Mvaa 104,8,152
s_lshl_b32 s81, s81, 3
buffer_load_dwordx4 v[192:195], v27, s[8:11], s28 offen offset:0 sc1
v_accvgpr_write_b32 a211, v167
Mvaa 136,8,156
Ta 4,11,0
Ta 6,11,1024
Mvaa 140,12,156
s_add_u32 s80, s80, s81
K 248,248,249
Mvaa 108,12,152
s_add_u32 s71, s80, 4096
K 249,250,251
Mvaa 76,12,148
buffer_load_dwordx4 v[196:199], v27, s[8:11], s93 offen offset:0 sc1
K 250,252,253
Mvaa 44,12,144
W 8
s_add_u32 s28, s28, 512
s_cmp_eq_u32 s28, s4
s_cselect_b32 s93, 10240, 0
Mvaa 48,16,144
s_sub_u32 s28, s28, s93
s_add_u32 s93, s28, 20480
Ta 8,12,0
Mvaa 80,16,148
Ta 10,12,1024
K 251,254,255
Mvaa 112,16,152
buffer_load_dwordx4 a[128:131], v26, s[8:11], s28 offen offset:0 sc1
v_accvgpr_write_b32 a212, v248
Mvaa 144,16,156
v_accvgpr_write_b32 a213, v249
v_accvgpr_write_b32 a214, v250
Mvaa 148,20,156
Ta 12,13,0
Ta 14,13,1024
Mvaa 116,20,152
v_accvgpr_write_b32 a215, v251
G 164,216
Mvaa 84,20,148
buffer_load_dwordx4 a[132:135], v26, s[8:11], s93 offen offset:0 sc1
G 165,217
Mvaa 52,20,144
W 8
Ta 16,14,0
Ta 18,14,1024
Mvaa 56,24,144
s_lshr_b32 s80, s15, 3
G 166,218
G 167,219
Mvaa 88,24,148
G 172,220
Mvaa 120,24,152
s_lshl_b32 s80, s80, 1
buffer_load_dwordx4 a[136:139], v27, s[8:11], s28 offen offset:0 sc1
G 173,221
Mvaa 152,24,156
s_add_u32 s80, s80, s14
Ta 20,15,0
Ta 22,15,1024
Mvaa 156,28,156
s_sub_u32 s80, s80, 2
G 174,222
G 175,223
Mvaa 124,28,152
s_lshl_b32 s80, s80, 12
K 164,164,165
Mvaa 92,28,148
buffer_load_dwordx4 a[140:143], v27, s[8:11], s93 offen offset:0 sc1
v_min_u32 v28, 496, v1
Mvaa 60,28,144
s_waitcnt vmcnt(16) lgkmcnt(8)
Ta 24,24,0
Ta 26,24,1024
Mvaa 32,0,32
s_add_u32 s28, s28, 512
K 165,166,167
v_lshl_add_u32 v28, v28, 3, s80
Mvaa 64,0,36
s_cmp_eq_u32 s28, s4
s_cselect_b32 s93, 10240, 0
s_sub_u32 s28, s28, s93
s_add_u32 s93, s28, 20480
Mvaa 96,0,40
D 16,48,65,0
K 166,172,173
Mvaa 128,0,44
v_min_u32 v28, .Lend%=-.Lbeg%=-4, v28
K 167,174,175
Mvaa 132,4,44
Ta 28,25,0
v_accvgpr_write_b32 a216, v164
Mvaa 100,4,40
Ta 30,25,1024
v_accvgpr_write_b32 a217, v165
Mvaa 68,4,36
D 17,48,65,1024
v_accvgpr_write_b32 a218, v166
Mvaa 36,4,32
W 8
Ta 0,10,8192
v_accvgpr_write_b32 a219, v167
Mvaa 40,8,32
Ta 2,10,9216
v_add_u32 v2, 65536, v10
Mvaa 72,8,36
K 200,200,201
K 201,202,203
Mvaa 104,8,40
D 18,48,65,2048
K 202,204,205
Mvaa 136,8,44
Ta 4,11,8192
K 203,206,207
Mvaa 140,12,44
Ta 6,11,9216
v_add_u32 v3, 65536, v11
Mvaa 108,12,40
v_accvgpr_write_b32 a220, v200
v_accvgpr_write_b32 a221, v201
Mvaa 76,12,36
buffer_load_dwordx4 a[144:147], v26, s[8:11], s28 offen offset:0 sc1
v_accvgpr_write_b32 a222, v202
Mvaa 44,12,32
W 8
Ta 8,12,8192
v_accvgpr_write_b32 a223, v203
Mvaa 48,16,32
Ta 10,12,9216
v_add_u32 v4, 65536, v12
Mvaa 80,16,36
G 164,224
G 165,225
Mvaa 112,16,40
buffer_load_dwordx4 a[148:151], v26, s[8:11], s93 offen offset:0 sc1
G 166,226
Mvaa 144,16,44
Ta 12,13,8192
Ta 14,13,9216
Mvaa 148,20,44
v_add_u32 v5, 65536, v13
G 167,227
Mvaa 116,20,40
G 172,228
G 173,229
Mvaa 84,20,36
buffer_load_dwordx4 a[152:155], v27, s[8:11], s28 offen offset:0 sc1
G 174,230
Mvaa 52,20,32
W 8
Ta 16,14,8192
Ta 18,14,9216
Mvaa 56,24,32
v_add_u32 v6, 65536, v14
G 175,231
Mvaa 88,24,36
K 164,164,165
Mvaa 120,24,40
buffer_load_dwordx4 a[156:159], v27, s[8:11], s93 offen offset:0 sc1
K 165,166,167
Mvaa 152,24,44
s_add_u32 s28, s28, 512
s_cmp_eq_u32 s28, s4
s_cselect_b32 s93, 10240, 0
s_sub_u32 s28, s28, s93
Mvaa 156,28,44
s_add_u32 s93, s28, 20480
Ta 20,15,8192
Ta 22,15,9216
Mvaa 124,28,40
v_add_u32 v7, 65536, v15
K 166,172,173
Mvaa 92,28,36
D 19,48,65,3072
K 167,174,175
Mvaa 60,28,32
s_waitcnt vmcnt(16) lgkmcnt(8)
s_add_u32 m0, s63, 102400
v_accvgpr_write_b32 a224, v164
Mvav 32,0,184
Ta 24,24,8192
v_accvgpr_write_b32 a225, v165
Mvav 64,0,188
Ta 26,24,9216
v_add_u32 v8, 65536, v24
Mvaa 96,0,48
global_load_dword v29, v28, s[6:7]
v_accvgpr_write_b32 a226, v166
Mvaa 128,0,52
s_sub_u32 s74, s85, 66
s_and_b32 s75, s74, 0xffffffe3
s_cselect_b64 s[72:73], -1, 0
s_and_b32 s74, s74, 28
Mvaa 132,4,52
s_lshl_b32 s74, s74, 1
v_mov_b32 v28, s74
v_accvgpr_write_b32 a227, v167
Mvaa 100,4,48
Ta 28,25,8192
K 208,208,209
Mvav 68,4,188
D 20,48,65,0
Ta 30,25,9216
Mvav 36,4,184
W 8
v_add_u32 v9, 65536, v25
K 209,210,211
Mvav 40,8,184
K 210,212,213
K 211,214,215
Mvav 72,8,188
Ta 0,10,16384
v_accvgpr_write_b32 a228, v208
Mvaa 104,8,48
D 21,48,65,1024
Ta 2,10,17408
Mvaa 136,8,52
v_accvgpr_write_b32 a229, v209
v_accvgpr_write_b32 a230, v210
Mvaa 140,12,52
Ta 4,11,16384
v_accvgpr_write_b32 a231, v211
Mvaa 108,12,48
Ta 6,11,17408
G 164,232
Mvav 76,12,188
buffer_load_dwordx4 v[200:203], v26, s[8:11], s28 offen offset:0 sc1
G 165,233
Mvav 44,12,184
W 8
G 166,234
G 167,235
Mvav 48,16,184
Ta 8,12,16384
G 172,236
Mvav 80,16,188
Ta 10,12,17408
G 173,237
Mvaa 112,16,48
buffer_load_dwordx4 v[204:207], v26, s[8:11], s93 offen offset:0 sc1
G 174,238
Mvaa 144,16,52
G 175,239
K 164,164,165
Mvaa 148,20,52
Ta 12,13,16384
K 165,166,167
Mvaa 116,20,48
Ta 14,13,17408
K 166,172,173
Mvav 84,20,188
buffer_load_dwordx2 v[28:29], v28, s[8:11], 0 offen offset:3584 sc1
K 167,174,175
Mvav 52,20,184
W 8
v_accvgpr_write_b32 a232, v164
v_accvgpr_write_b32 a233, v165
Mvav 56,24,184
Ta 16,14,16384
v_accvgpr_write_b32 a234, v166
Mvav 88,24,188
Ta 18,14,17408
v_accvgpr_write_b32 a235, v167
Mvaa 120,24,48
D 22,48,65,2048
G 164,160
Mvaa 152,24,52
G 165,161
G 166,162
Mvaa 156,28,52
Ta 20,15,16384
Ta 22,15,17408
Mvaa 124,28,48
G 167,163
G 172,164
Mvav 92,28,188
buffer_load_dwordx4 a[32:35], v27, s[8:11], s28 offen offset:0 sc1
G 173,165
Mvav 60,28,184
s_waitcnt vmcnt(20) lgkmcnt(8)
G 174,166
G 175,167
Mvaa 32,0,56
Ta 24,24,16384
Ta 26,24,17408
Mvaa 64,0,60
K 164,164,165
K 165,166,167
Mvav 96,0,192
buffer_load_dwordx4 a[36:39], v27, s[8:11], s93 offen offset:0 sc1
K 166,172,173
Mvav 128,0,196
s_add_u32 s28, s28, 512
s_cmp_eq_u32 s28, s4
s_cselect_b32 s93, 10240, 0
s_sub_u32 s28, s28, s93
Mvav 132,4,196
s_add_u32 s93, s28, 20480
K 167,174,175
v_accvgpr_write_b32 a236, v164
Mvav 100,4,192
Ta 28,25,16384
Ta 30,25,17408
Mvaa 68,4,60
buffer_load_dwordx4 a[40:43], v26, s[8:11], s28 offen offset:0 sc1
v_accvgpr_write_b32 a237, v165
Mvaa 36,4,56
W 8
v_accvgpr_write_b32 a238, v166
v_accvgpr_write_b32 a239, v167
Mvaa 40,8,56
G 164,240
G 165,241
Mvaa 72,8,60
Ta 0,10,24576
Ta 2,10,25600
Mvav 104,8,192
D 23,48,65,3072
G 166,242
Mvav 136,8,196
G 167,243
G 172,244
Mvav 140,12,196
Ta 4,11,24576
G 173,245
Mvav 108,12,192
Ta 6,11,25600
G 174,246
Mvaa 76,12,60
buffer_load_dwordx4 a[44:47], v26, s[8:11], s93 offen offset:0 sc1
G 175,247
Mvaa 44,12,56
W 8
K 164,164,165
K 165,166,167
Mvaa 48,16,56
Ta 8,12,24576
K 166,172,173
Mvaa 80,16,60
Ta 10,12,25600
K 167,174,175
Mvav 112,16,192
s_cmp_lg_u32 s72, 0
s_cbranch_scc1 .LL_53%=
s_waitcnt vmcnt(6)
.LL_53%=:
v_xor_b32 v28, s12, v28
v_xor_b32 v29, s13, v29
v_lshrrev_b32 v248, 11, v28
v_or_b32 v29, v29, v248
v_cmp_eq_u32 vcc, 0, v29
s_or_b64 vcc, vcc, s[72:73]
s_cmp_eq_u64 vcc, -1
s_cbranch_scc0 .Lspin_54%=
.Lok_55%=:
buffer_load_dwordx4 v[208:211], v27, s[8:11], s28 offen offset:0 sc1
v_accvgpr_write_b32 a240, v164
Mvav 144,16,196
v_accvgpr_write_b32 a241, v165
v_accvgpr_write_b32 a242, v166
Mvav 148,20,196
Ta 12,13,24576
v_accvgpr_write_b32 a243, v167
Mvav 116,20,192
Ta 14,13,25600
G 164,168
Mvaa 84,20,60
buffer_load_dwordx4 v[212:215], v27, s[8:11], s93 offen offset:0 sc1
G 165,169
Mvaa 52,20,56
W 8
s_add_u32 s28, s28, 512
s_cmp_eq_u32 s28, s4
s_cselect_b32 s93, 10240, 0
Mvaa 56,24,56
s_sub_u32 s28, s28, s93
s_add_u32 s93, s28, 20480
G 166,170
Mvaa 88,24,60
G 167,171
G 172,172
Mvav 120,24,192
)"
R"(buffer_load_dwordx4 v[184:187], v26, s[8:11], s28 offen offset:0 sc1
Ta 16,14,24576
Mvav 152,24,196
Ta 18,14,25600
v_bfe_u32 v248, v28, 3, 8
Mvav 156,28,196
G 173,173
v_mul_u32_u24 v248, 10240, v248
Mvav 124,28,192
G 174,174
v_and_b32 v28, 7, v28
Mvaa 92,28,60
buffer_load_dwordx4 v[188:191], v26, s[8:11], s93 offen offset:0 sc1
G 175,175
Mvaa 60,28,56
s_waitcnt vmcnt(24) lgkmcnt(6)
K 164,164,165
v_lshl_add_u32 v248, v28, 4, v248
Mvaa 32,0,128
Ta 20,15,24576
Ta 22,15,25600
Mvaa 64,0,132
K 165,166,167
v_lshl_add_u32 v248, v1, 3, v248
Mvaa 96,0,136
buffer_load_dwordx4 a[48:51], v27, s[8:11], s28 offen offset:0 sc1
K 166,172,173
Mvaa 128,0,140
K 167,174,175
v_lshrrev_b32 v29, 8, v1
Mvaa 132,4,140
v_accvgpr_write_b32 a244, v164
v_lshlrev_b32 v29, 28, v29
Mvaa 100,4,136
Ta 24,24,24576
v_accvgpr_write_b32 a245, v165
Mvaa 68,4,132
buffer_load_dwordx4 a[52:55], v27, s[8:11], s93 offen offset:0 sc1
Ta 26,24,25600
Mvaa 36,4,128
W 6
v_or_b32 v249, v248, v29
v_accvgpr_write_b32 a246, v166
Mvaa 40,8,128
s_and_b32 s75, s72, 134217728
s_add_u32 s28, s28, 512
v_accvgpr_write_b32 a247, v167
Mvaa 72,8,132
s_cmp_eq_u32 s28, s4
s_cselect_b32 s93, 10240, 0
s_sub_u32 s28, s28, s93
s_add_u32 s93, s28, 20480
Mvaa 104,8,136
s_add_u32 s76, s75, 131072
buffer_load_dwordx4 a[56:59], v26, s[8:11], s28 offen offset:0 sc1
G 164,64
Mvaa 136,8,140
G 165,65
G 166,66
Mvaa 140,12,140
Ta 28,25,24576
Ta 30,25,25600
Mvaa 108,12,136
G 167,67
G 172,68
Mvaa 76,12,132
buffer_load_dwordx4 v[240:243], v248, s[8:11], s76 offen sc1
G 173,69
Mvaa 44,12,128
W 4
s_add_u32 s76, s76, 8192
G 174,70
Mvaa 48,16,128
Ta 0,2,0
G 175,71
Mvaa 80,16,132
Ta 2,2,1024
K 164,164,165
Mvaa 112,16,136
buffer_load_dwordx4 v[244:247], v249, s[8:11], s76 offen sc1
v_or_b32 v249, v1, v29
Mvaa 144,16,140
s_mul_i32 s76, s74, 160
s_add_u32 s76, s76, s75
K 165,166,167
Mvaa 148,20,140
s_add_u32 s76, s76, 118784
K 166,172,173
K 167,174,175
Mvaa 116,20,136
Ta 4,3,0
Ta 6,3,1024
Mvaa 84,20,132
buffer_load_dwordx4 a[60:63], v26, s[8:11], s93 offen offset:0 sc1
v_accvgpr_write_b32 a64, v164
Mvaa 52,20,128
W 4
v_accvgpr_write_b32 a65, v165
v_accvgpr_write_b32 a66, v166
Mvaa 56,24,128
v_accvgpr_write_b32 a67, v167
G 164,176
Mvaa 88,24,132
Ta 8,4,0
Ta 10,4,1024
Mvaa 120,24,136
G 165,177
G 166,178
Mvaa 152,24,140
buffer_load_dwordx4 v[192:195], v27, s[8:11], s28 offen offset:0 sc1
G 167,179
Mvaa 156,28,140
G 172,180
G 173,181
Mvaa 124,28,136
Ta 12,5,0
Ta 14,5,1024
Mvaa 92,28,132
G 174,182
G 175,183
Mvaa 60,28,128
s_waitcnt vmcnt(24) lgkmcnt(4)
K 164,164,165
K 165,166,167
Mvaa 32,0,144
Ta 16,6,0
K 166,172,173
Mvaa 64,0,148
Ta 18,6,1024
K 167,174,175
Mvaa 96,0,152
buffer_load_dwordx4 v[196:199], v27, s[8:11], s93 offen offset:0 sc1
v_accvgpr_write_b32 a68, v164
Mvaa 128,0,156
v_accvgpr_write_b32 a69, v165
v_accvgpr_write_b32 a70, v166
v_accvgpr_write_b32 a71, v167
Mvaa 132,4,156
s_add_u32 s28, s28, 512
Ta 20,7,0
Ta 22,7,1024
Mvaa 100,4,152
s_cmp_eq_u32 s28, s4
s_cselect_b32 s93, 10240, 0
G 164,72
G 165,73
Mvaa 68,4,148
s_sub_u32 s28, s28, s93
Ta 24,8,0
G 166,74
Mvaa 36,4,144
W 5
Ta 26,8,1024
G 167,75
Mvaa 40,8,144
s_add_u32 s93, s28, 20480
buffer_load_dwordx4 a[128:131], v26, s[8:11], s28 offen offset:0 sc1
G 172,76
Mvaa 72,8,148
G 173,77
G 174,78
Mvaa 104,8,152
Ta 28,9,0
Ta 30,9,1024
Mvaa 136,8,156
G 175,79
K 164,164,165
Mvaa 140,12,156
Ta 0,2,8192
K 165,166,167
Mvaa 108,12,152
Ta 2,2,9216
K 166,172,173
Mvaa 76,12,148
buffer_load_dwordx4 a[132:135], v26, s[8:11], s93 offen offset:0 sc1
K 167,174,175
Mvaa 44,12,144
W 6
v_accvgpr_write_b32 a72, v164
v_accvgpr_write_b32 a73, v165
Mvaa 48,16,144
Ta 4,3,8192
Ta 6,3,9216
Mvaa 80,16,148
v_accvgpr_write_b32 a74, v166
v_accvgpr_write_b32 a75, v167
Mvaa 112,16,152
Ta 8,4,8192
K 216,216,217
Mvaa 144,16,156
Ta 10,4,9216
K 217,218,219
Mvaa 148,20,156
buffer_load_dwordx4 a[136:139], v27, s[8:11], s28 offen offset:0 sc1
K 218,220,221
Mvaa 116,20,152
K 219,222,223
v_accvgpr_write_b32 a76, v216
Mvaa 84,20,148
Ta 12,5,8192
Ta 14,5,9216
Mvaa 52,20,144
W 8
v_lshrrev_b32 v20, 4, v1
v_accvgpr_write_b32 a77, v217
Mvaa 56,24,144
v_accvgpr_write_b32 a78, v218
v_add_u32 v20, 0, v20
Mvaa 88,24,148
v_accvgpr_write_b32 a79, v219
v_mul_u32_u24 v22, 205, v20
Mvaa 120,24,152
Ta 16,6,8192
Ta 18,6,9216
Mvaa 152,24,156
G 164,80
G 165,81
Mvaa 156,28,156
buffer_load_dwordx4 a[140:143], v27, s[8:11], s93 offen offset:0 sc1
v_lshrrev_b32 v22, 12, v22
Mvaa 124,28,152
G 166,82
G 167,83
Mvaa 92,28,148
Ta 20,7,8192
v_mul_u32_u24 v23, 20, v22
Mvaa 60,28,144
s_waitcnt vmcnt(19) lgkmcnt(7)
Ta 22,7,9216
G 172,84
Mvav 32,0,200
s_add_u32 s28, s28, 512
v_sub_u32 v20, v20, v23
G 173,85
Mvav 64,0,204
s_cmp_eq_u32 s28, s4
s_cselect_b32 s93, 10240, 0
G 174,86
v_lshrrev_b32 v23, 1, v22
Mvaa 96,0,32
s_sub_u32 s28, s28, s93
Ta 24,8,8192
G 175,87
Mvaa 128,0,36
s_add_u32 s93, s28, 20480
Ta 26,8,9216
K 164,164,165
Mvaa 132,4,36
buffer_load_dwordx4 v[216:219], v26, s[8:11], s28 offen offset:0 sc1
v_and_b32 v23, 2, v23
Mvaa 100,4,32
K 165,166,167
v_xor_b32 v20, v20, v23
Mvav 68,4,204
Ta 28,9,8192
K 166,172,173
Mvav 36,4,200
W 7
Ta 30,9,9216
K 167,174,175
Mvav 40,8,200
v_mul_u32_u24 v22, 80, v22
v_accvgpr_write_b32 a80, v164
Mvav 72,8,204
v_accvgpr_write_b32 a81, v165
v_add_lshl_u32 v16, v20, v22, 4
Mvaa 104,8,32
Ta 0,2,16384
v_accvgpr_write_b32 a82, v166
Mvaa 136,8,36
Ta 2,2,17408
v_lshrrev_b32 v20, 4, v1
Mvaa 140,12,36
buffer_load_dwordx4 v[220:223], v26, s[8:11], s93 offen offset:0 sc1
v_accvgpr_write_b32 a83, v167
Mvaa 108,12,32
K 224,224,225
v_add_u32 v20, 64, v20
Mvav 76,12,204
Ta 4,3,16384
K 225,226,227
Mvav 44,12,200
W 7
Ta 6,3,17408
K 226,228,229
Mvav 48,16,200
v_mul_u32_u24 v22, 205, v20
K 227,230,231
Mvav 80,16,204
v_lshrrev_b32 v22, 12, v22
v_accvgpr_write_b32 a84, v224
Mvaa 112,16,32
Ta 8,4,16384
v_accvgpr_write_b32 a85, v225
Mvaa 144,16,36
Ta 10,4,17408
v_mul_u32_u24 v23, 20, v22
Mvaa 148,20,36
v_accvgpr_write_b32 a86, v226
v_accvgpr_write_b32 a87, v227
Mvaa 116,20,32
buffer_load_dwordx4 a[144:147], v27, s[8:11], s28 offen offset:0 sc1
v_sub_u32 v20, v20, v23
Mvav 84,20,204
Ta 12,5,16384
G 164,88
Mvav 52,20,200
W 7
Ta 14,5,17408
G 165,89
Mvav 56,24,200
v_lshrrev_b32 v23, 1, v22
G 166,90
Mvav 88,24,204
v_and_b32 v23, 2, v23
G 167,91
Mvaa 120,24,32
G 172,92
v_xor_b32 v20, v20, v23
Mvaa 152,24,36
Ta 16,6,16384
Ta 18,6,17408
Mvaa 156,28,36
G 173,93
G 174,94
Mvaa 124,28,32
buffer_load_dwordx4 a[148:151], v27, s[8:11], s93 offen offset:0 sc1
v_mul_u32_u24 v22, 80, v22
Mvav 92,28,204
G 175,95
v_add_lshl_u32 v17, v20, v22, 4
Mvav 60,28,200
s_waitcnt vmcnt(18) lgkmcnt(6)
Ta 20,7,16384
Ta 22,7,17408
Mvaa 32,0,40
s_add_u32 s28, s28, 512
K 164,164,165
K 165,166,167
Mvaa 64,0,44
s_cmp_eq_u32 s28, s4
s_cselect_b32 s93, 10240, 0
v_subrev_u32 v17, 1024, v17
K 166,172,173
Mvav 96,0,208
s_sub_u32 s28, s28, s93
K 167,174,175
v_lshrrev_b32 v20, 4, v1
Mvav 128,0,212
s_add_u32 s93, s28, 20480
Ta 24,8,16384
Ta 26,8,17408
Mvav 132,4,212
s_cmp_lg_u32 s72, 0
s_cbranch_scc1 .LL_57%=
s_waitcnt vmcnt(11)
.LL_57%=:
buffer_store_dwordx4 v[240:243], v1, s[8:11], s76 offen sc1
v_accvgpr_write_b32 a88, v164
v_add_u32 v20, 128, v20
Mvav 100,4,208
v_accvgpr_write_b32 a89, v165
v_accvgpr_write_b32 a90, v166
Mvaa 68,4,44
v_mul_u32_u24 v22, 205, v20
v_accvgpr_write_b32 a91, v167
Mvaa 36,4,40
W 6
Ta 28,9,16384
Ta 30,9,17408
Mvaa 40,8,40
buffer_load_dwordx4 a[152:155], v26, s[8:11], s28 offen offset:0 sc1
K 232,232,233
Mvaa 72,8,44
v_lshrrev_b32 v22, 12, v22
K 233,234,235
Mvav 104,8,208
v_mul_u32_u24 v23, 20, v22
K 234,236,237
Mvav 136,8,212
Ta 0,2,24576
Ta 2,2,25600
Mvav 140,12,212
buffer_store_dwordx4 v[244:247], v249, s[8:11], s76 offen offset:1024 sc1
K 235,238,239
Mvav 108,12,208
s_add_u32 s76, s74, s75
s_add_u32 s76, s76, 3840
v_sub_u32 v20, v20, v23
Mvaa 76,12,44
v_accvgpr_write_b32 a92, v232
v_accvgpr_write_b32 a93, v233
Mvaa 44,12,40
W 6
v_lshrrev_b32 v23, 1, v22
v_accvgpr_write_b32 a94, v234
Mvaa 48,16,40
buffer_load_dwordx4 a[156:159], v26, s[8:11], s93 offen offset:0 sc1
Ta 4,3,24576
Mvaa 80,16,44
)"
R"(Ta 6,3,25600
v_and_b32 v23, 2, v23
Mvav 112,16,208
v_accvgpr_write_b32 a95, v235
G 164,96
Mvav 144,16,212
v_xor_b32 v20, v20, v23
G 165,97
Mvav 148,20,212
Ta 8,4,24576
G 166,98
Mvav 116,20,208
Ta 10,4,25600
v_mul_u32_u24 v22, 80, v22
Mvaa 84,20,44
G 167,99
G 172,100
Mvaa 52,20,40
W 6
buffer_load_dwordx4 v[224:227], v27, s[8:11], s28 offen offset:0 sc1
G 173,101
Mvaa 56,24,40
v_add_lshl_u32 v18, v20, v22, 4
G 174,102
Mvaa 88,24,44
Ta 12,5,24576
G 175,103
Mvav 120,24,208
Ta 14,5,25600
v_subrev_u32 v18, 2048, v18
Mvav 152,24,212
K 164,164,165
K 165,166,167
Mvav 156,28,212
v_lshrrev_b32 v20, 4, v1
K 166,172,173
Mvav 124,28,208
Ta 16,6,24576
K 167,174,175
Mvaa 92,28,44
Ta 18,6,25600
v_add_u32 v20, 192, v20
Mvaa 60,28,40
s_waitcnt vmcnt(19) lgkmcnt(6)
v_accvgpr_write_b32 a96, v164
v_accvgpr_write_b32 a97, v165
Mvav 32,0,184
v_accvgpr_write_b32 a98, v166
v_mul_u32_u24 v22, 205, v20
Mvav 64,0,188
buffer_load_dwordx4 v[228:231], v27, s[8:11], s93 offen offset:0 sc1
v_accvgpr_write_b32 a99, v167
Mvaa 96,0,48
s_add_u32 s28, s28, 512
G 164,248
v_lshrrev_b32 v22, 12, v22
Mvaa 128,0,52
s_cmp_eq_u32 s28, s4
s_cselect_b32 s93, 10240, 0
Ta 20,7,24576
Ta 22,7,25600
Mvaa 132,4,52
s_sub_u32 s28, s28, s93
G 165,249
G 166,250
Mvaa 100,4,48
s_add_u32 s93, s28, 20480
v_mul_u32_u24 v23, 20, v22
G 167,251
Mvav 68,4,188
G 172,252
v_sub_u32 v20, v20, v23
Mvav 36,4,184
W 4
Ta 24,8,24576
G 173,253
Mvav 40,8,184
Ta 26,8,25600
G 174,254
Mvav 72,8,188
Ta 28,9,24576
Ta 30,9,25600
Mvaa 104,8,48
G 175,255
v_lshrrev_b32 v23, 1, v22
Mvaa 136,8,52
buffer_load_dwordx4 v[200:203], v26, s[8:11], s28 offen offset:0 sc1
K 164,164,165
Mvaa 140,12,52
W 0
s_barrier
K 165,166,167
Mvaa 108,12,48
v_and_b32 v23, 2, v23
K 166,172,173
Mvav 76,12,188
K 167,174,175
v_xor_b32 v20, v20, v23
Mvav 44,12,184
v_accvgpr_write_b32 a100, v164
v_accvgpr_write_b32 a101, v165
v_accvgpr_write_b32 a102, v166
v_accvgpr_write_b32 a103, v167
Mvav 48,16,184
v_mul_u32_u24 v22, 80, v22
G 164,104
Mvav 80,16,188
buffer_load_dwordx4 v[204:207], v26, s[8:11], s93 offen offset:0 sc1
v_add_lshl_u32 v19, v20, v22, 4
Mvaa 112,16,48
G 165,105
v_subrev_u32 v19, 3072, v19
Mvaa 144,16,52
G 166,106
G 167,107
Mvaa 148,20,52
v_lshrrev_b32 v20, 4, v1
G 172,108
Mvaa 116,20,48
Ta 0,10,32768
Ta 2,10,33792
Mvav 84,20,188
Ta 4,11,32768
Ta 6,11,33792
Mvav 52,20,184
G 173,109
v_add_u32 v20, 256, v20
Mvav 56,24,184
Ta 8,12,32768
G 174,110
Mvav 88,24,188
Ta 10,12,33792
v_mul_u32_u24 v22, 205, v20
Mvaa 120,24,48
buffer_load_dwordx4 a[32:35], v27, s[8:11], s28 offen offset:0 sc1
G 175,111
Mvaa 152,24,52
K 164,164,165
v_lshrrev_b32 v22, 12, v22
Mvaa 156,28,52
Ta 12,13,32768
K 165,166,167
Mvaa 124,28,48
Ta 14,13,33792
K 166,172,173
Mvav 92,28,188
v_mul_u32_u24 v23, 20, v22
K 167,174,175
Mvav 60,28,184
s_waitcnt vmcnt(17) lgkmcnt(4)
v_sub_u32 v20, v20, v23
v_accvgpr_write_b32 a104, v164
Mvaa 32,0,56
Ta 16,14,32768
v_accvgpr_write_b32 a105, v165
Mvaa 64,0,60
Ta 18,14,33792
v_lshrrev_b32 v23, 1, v22
Mvav 96,0,192
v_accvgpr_write_b32 a106, v166
v_accvgpr_write_b32 a107, v167
Mvav 128,0,196
buffer_load_dwordx4 a[36:39], v27, s[8:11], s93 offen offset:0 sc1
v_and_b32 v23, 2, v23
Mvav 132,4,196
s_add_u32 s28, s28, 512
K 176,176,177
v_xor_b32 v20, v20, v23
Mvav 100,4,192
s_cmp_eq_u32 s28, s4
s_cselect_b32 s93, 10240, 0
Ta 20,15,32768
Ta 22,15,33792
Mvaa 68,4,60
s_sub_u32 s28, s28, s93
K 177,178,179
K 178,180,181
Mvaa 36,4,56
W 4
v_mul_u32_u24 v22, 80, v22
K 179,182,183
Mvaa 40,8,56
s_add_u32 s93, s28, 20480
v_accvgpr_write_b32 a108, v176
v_add_lshl_u32 v20, v20, v22, 4
Mvaa 72,8,60
Ta 24,24,32768
Ta 26,24,33792
Mvav 104,8,192
v_accvgpr_write_b32 a109, v177
v_accvgpr_write_b32 a110, v178
v_accvgpr_write_b32 a111, v179
Mvav 136,8,196
buffer_load_dwordx4 v[232:235], v26, s[8:11], s28 offen offset:0 sc1
v_lshrrev_b32 v22, 6, v1
Mvav 140,12,196
Ta 28,25,32768
v_mul_u32_u24 v22, 320, v22
Mvav 108,12,192
Ta 30,25,33792
G 164,112
Mvaa 76,12,60
G 165,113
v_bfe_u32 v23, v1, 4, 2
Mvaa 44,12,56
W 4
G 166,114
v_lshl_add_u32 v22, v23, 3, v22
Mvaa 48,16,56
s_cmp_lg_u32 s72, 0
s_cbranch_scc1 .LL_58%=
s_waitcnt vmcnt(8)
.LL_58%=:
v_mov_b64 v[28:29], s[12:13]
s_mov_b64 exec, 1
buffer_store_dwordx2 v[28:29], off, s[8:11], s76 sc1
s_mov_b64 exec, -1
Ta 0,10,40960
Ta 2,10,41984
Mvaa 80,16,60
G 167,115
v_bfe_u32 v23, v1, 8, 1
Mvav 112,16,192
G 172,116
G 173,117
Mvav 144,16,196
Ta 4,11,40960
v_lshlrev_b32 v23, 5, v23
Mvav 148,20,196
buffer_load_dwordx4 v[236:239], v26, s[8:11], s93 offen offset:0 sc1
Ta 6,11,41984
Mvav 116,20,192
G 174,118
v_add_u32 v21, v22, v23
Mvaa 84,20,60
G 175,119
K 164,164,165
Mvaa 52,20,56
W 4
Ta 8,12,40960
v_sub_u32 v22, v22, v23
Mvaa 56,24,56
s_mul_i32 s94, s59, 655360
Ta 10,12,41984
K 165,166,167
Mvaa 88,24,60
s_mul_i32 s95, s58, 320
buffer_load_dwordx4 a[40:43], v27, s[8:11], s28 offen offset:0 sc1
K 166,172,173
Mvav 120,24,192
s_add_u32 s94, s94, s95
Ta 12,13,40960
K 167,174,175
Mvav 152,24,196
Ta 14,13,41984
v_accvgpr_write_b32 a112, v164
Mvav 156,28,196
s_add_u32 s40, s26, s94
s_addc_u32 s41, s27, 0
v_accvgpr_write_b32 a113, v165
Mvav 124,28,192
s_and_b32 s41, s41, 0xffff
Ta 16,14,40960
v_accvgpr_write_b32 a114, v166
Mvaa 92,28,60
s_mov_b32 s42, 5242880
Ta 18,14,41984
v_accvgpr_write_b32 a115, v167
Mvaa 60,28,56
s_waitcnt vmcnt(18) lgkmcnt(6)
buffer_load_dwordx4 a[44:47], v27, s[8:11], s93 offen offset:0 sc1
v_accvgpr_write_b32 a116, v168
Mvaa 32,0,128
s_mov_b32 s43, 0x20000
Ta 20,15,40960
v_accvgpr_write_b32 a117, v169
Mvaa 64,0,132
s_sub_u32 s42, s42, s94
Ta 22,15,41984
v_accvgpr_write_b32 a118, v170
Mvaa 96,0,136
s_mov_b64 s[44:45], s[8:9]
v_accvgpr_write_b32 a119, v171
G 164,120
Mvaa 128,0,140
s_add_u32 s28, s28, 512
Ta 24,24,40960
Ta 26,24,41984
Mvaa 132,4,140
s_cmp_eq_u32 s28, s4
s_cselect_b32 s93, 10240, 0
G 165,121
G 166,122
Mvaa 100,4,136
s_sub_u32 s28, s28, s93
s_add_u32 s93, s28, 20480
buffer_load_dwordx4 v[208:211], v26, s[8:11], s28 offen offset:0 sc1
Mvaa 68,4,132
s_mov_b32 s47, 0x20000
Ta 28,25,40960
G 167,123
Mvaa 36,4,128
W 7
Ta 30,25,41984
G 172,124
Mvaa 40,8,128
s_and_b32 s48, s14, 1
G 173,125
v_bfe_u32 v28, v1, 4, 4
Mvaa 72,8,132
s_mul_i32 s48, s48, 5120
G 174,126
v_lshlrev_b32 v28, 3, v28
Mvaa 104,8,136
s_lshl_b32 s80, s56, 9
Ta 0,10,49152
Ta 2,10,50176
Mvaa 136,8,140
s_lshr_b32 s51, s14, 1
s_add_u32 s80, s80, 4096
G 175,127
Mvaa 140,12,140
s_and_b32 s94, s14, 1
buffer_load_dwordx4 v[212:215], v26, s[8:11], s93 offen offset:0 sc1
K 164,164,165
Mvaa 108,12,136
s_lshl_b32 s95, s57, 1
Ta 4,11,49152
K 165,166,167
Mvaa 76,12,132
s_add_u32 s94, s94, s95
Ta 6,11,50176
K 166,172,173
Mvaa 44,12,128
W 8
K 167,174,175
v_accvgpr_write_b32 a120, v164
Mvaa 48,16,128
s_lshl_b32 s54, s94, 3
buffer_load_dwordx2 v[28:29], v28, s[8:11], s80 offen sc1
Ta 8,12,49152
Mvaa 80,16,132
s_add_u32 s54, s54, s59
Ta 10,12,50176
v_accvgpr_write_b32 a121, v165
Mvaa 112,16,136
v_accvgpr_write_b32 a122, v166
v_accvgpr_write_b32 a123, v167
Mvaa 144,16,140
s_lshl_b32 s54, s54, 6
Ta 12,13,49152
Ta 14,13,50176
Mvaa 148,20,140
buffer_load_dwordx4 a[248:251], v27, s[8:11], s28 offen offset:0 sc1
v_accvgpr_write_b32 a124, v160
Mvaa 116,20,136
s_xor_b32 s55, s54, 15872
v_accvgpr_write_b32 a125, v161
Mvaa 84,20,132
Ta 16,14,49152
v_accvgpr_write_b32 a126, v162
Mvaa 52,20,128
W 9
s_add_u32 s54, s54, 20480
Ta 18,14,50176
Mvaa 56,24,128
s_and_b32 s81, s14, 1
buffer_load_dwordx4 a[252:255], v27, s[8:11], s93 offen offset:0 sc1
v_accvgpr_write_b32 a127, v163
Mvaa 88,24,132
s_add_u32 s55, s55, 20480
Mvaa 120,24,136
Ta 20,15,49152
Ta 22,15,50176
Mvaa 152,24,140
s_and_b32 s95, s14, 2
s_mul_i32 s91, s81, 17164288
Mvaa 156,28,140
Mvaa 124,28,136
s_lshl_b32 s94, s94, 2
s_mul_i32 s82, s56, 536576
Mvaa 92,28,132
s_add_u32 s94, s94, s95
Ta 24,24,49152
Ta 26,24,50176
Mvaa 60,28,128
s_waitcnt vmcnt(20) lgkmcnt(10)
Mvav 32,0,216
s_add_u32 s91, s91, s82
s_lshl_b32 s95, s95, 4
s_add_u32 s28, s28, 512
Mvav 64,0,220
s_cmp_eq_u32 s28, s4
s_cselect_b32 s93, 10240, 0
s_sub_u32 s28, s28, s93
s_add_u32 s93, s28, 20480
Mvaa 96,0,144
s_add_u32 s55, s55, s95
buffer_load_dwordx4 v[184:187], v26, s[8:11], s28 offen offset:0 sc1
Ta 28,25,49152
)"
R"(Mvaa 128,0,148
s_add_u32 s91, s91, 4194304
Ta 30,25,50176
Mvaa 132,4,148
s_lshl_b32 s49, s94, 17
Mvaa 100,4,144
s_lshr_b32 s82, s14, 1
Mvav 68,4,220
s_lshl_b32 s95, s59, 14
Ta 0,10,57344
Ta 2,10,58368
Mvav 36,4,216
W 10
s_lshl_b32 s83, s82, 14
Mvav 40,8,216
s_add_u32 s49, s49, s95
buffer_load_dwordx4 v[188:191], v26, s[8:11], s93 offen offset:0 sc1
Mvav 72,8,220
Mvaa 104,8,144
s_xor_b32 s50, s49, 16252928
s_add_u32 s91, s91, s83
Ta 4,11,57344
Mvaa 136,8,148
Ta 6,11,58368
Mvaa 140,12,148
s_add_u32 s49, s49, 39845888
s_lshl_b32 s81, s81, 14
Mvaa 108,12,144
Mvav 76,12,220
s_add_u32 s50, s50, 39845888
Ta 8,12,57344
Ta 10,12,58368
Mvav 44,12,216
W 10
s_lshl_b32 s82, s82, 16
s_lshr_b32 s94, s15, 3
Mvav 48,16,216
buffer_load_dwordx4 a[48:51], v27, s[8:11], s28 offen offset:0 sc1
Mvav 80,16,220
s_lshl_b32 s94, s94, 3
s_or_b32 s92, s81, s82
Mvaa 112,16,144
Ta 12,13,57344
Ta 14,13,58368
Mvaa 144,16,148
s_add_u32 s94, s94, s14
Mvaa 148,20,148
s_xor_b32 s92, s92, 0x10000
Mvaa 116,20,144
s_lshl_b32 s94, s94, 4
Mvav 84,20,220
Ta 16,14,57344
Ta 18,14,58368
Mvav 52,20,216
W 10
s_add_u32 s94, s94, s59
Mvav 56,24,216
buffer_load_dwordx4 a[52:55], v27, s[8:11], s93 offen offset:0 sc1
Mvav 88,24,220
s_mul_i32 s52, s94, 5120
Mvaa 120,24,144
Ta 20,15,57344
Ta 22,15,58368
Mvaa 152,24,148
s_add_u32 s52, s52, 56623104
Mvaa 156,28,148
Mvaa 124,28,144
s_lshl_b32 s53, s94, 3
Mvav 92,28,220
Ta 24,24,57344
Ta 26,24,58368
Mvav 60,28,216
s_waitcnt vmcnt(18) lgkmcnt(10)
s_add_u32 s53, s53, 65536
Mvaa 32,0,152
s_waitcnt vmcnt(6)
v_cmp_eq_u64 vcc, s[12:13], v[28:29]
s_cmp_eq_u64 vcc, -1
s_cbranch_scc0 .Lspin_59%=
.Lok_60%=:
v_bfe_u32 v244, v1, 4, 1
Mvaa 64,0,156
s_sub_u32 s82, s91, 16384
Mvav 96,0,224
Ta 28,25,57344
v_mul_u32_u24 v244, 0xaaaa, v244
Mvav 128,0,228
Ta 30,25,58368
Mvav 132,4,228
s_xor_b32 s83, s92, 0x10000
v_add_u32 v244, 0x4444, v244
Mvav 100,4,224
v_lshl_or_b32 v240, v244, 16, v244
Mvaa 68,4,156
s_add_u32 m0, s83, 0
Ta 0,2,32768
Ta 2,2,33792
Mvaa 36,4,152
W 10
v_mov_b32 v241, 0xff800000
Mvaa 40,8,152
s_add_u32 s81, s82, 0
Mvaa 72,8,156
v_add_u32 v242, 0x10000, v1
Mvav 104,8,224
Dc 1,8,81,0
v_bfe_u32 v244, v1, 4, 4
Mvav 136,8,228
Ta 4,3,32768
Ta 6,3,33792
Mvav 140,12,228
v_lshrrev_b32 v245, 8, v1
Mvav 108,12,224
v_lshlrev_b32 v245, 2, v245
Mvaa 76,12,156
Dc 1,8,81,1024
Ta 8,4,32768
Mvaa 44,12,152
W 9
Ta 10,4,33792
v_sub_u32 v244, v244, v245
Mvaa 48,16,152
Mvaa 80,16,156
v_cmp_le_i32_e64 s[72:73], 0, v244
Mvav 112,16,224
Dc 1,8,81,2048
v_cmp_le_i32_e64 s[74:75], 1, v244
Mvav 144,16,228
Ta 12,5,32768
Ta 14,5,33792
Mvav 148,20,228
v_cmp_le_i32_e64 s[76:77], 2, v244
Mvav 116,20,224
v_cmp_le_i32_e64 s[78:79], 3, v244
Mvaa 84,20,156
Dc 1,8,81,3072
Ta 16,6,32768
Mvaa 52,20,152
W 9
s_add_u32 m0, s83, 4096
s_add_u32 s81, s82, 4096
Mvaa 56,24,152
Ta 18,6,33792
v_add_u32 v243, 4096, v1
Mvaa 88,24,156
v_add_u32 v244, 8192, v1
Mvav 120,24,224
Dc 1,8,81,0
v_add_u32 v245, 12288, v1
Mvav 152,24,228
Ta 20,7,32768
Ta 22,7,33792
Mvav 156,28,228
s_mov_b32 s18, 0x3f803f80
Mvav 124,28,224
s_mov_b32 s19, 0x3f003f00
Mvaa 92,28,156
Dc 1,8,81,1024
Ta 24,8,32768
Mvaa 60,28,152
W 9
Ta 26,8,33792
v_accvgpr_write_b32 a176, s18
Mvav 32,0,200
Mvav 64,0,204
v_accvgpr_write_b32 a180, s19
Mvaa 96,0,32
Dc 1,8,81,2048
v_mov_b32 v252, v241
Mvaa 128,0,36
Ta 28,9,32768
Ta 30,9,33792
Mvaa 132,4,36
v_accvgpr_write_b32 a177, s18
Mvaa 100,4,32
v_accvgpr_write_b32 a181, s19
Mvav 68,4,204
Dc 1,8,81,3072
Ta 0,2,40960
Mvav 36,4,200
W 9
s_add_u32 m0, s83, 8192
s_add_u32 s81, s82, 8192
Mvav 40,8,200
Ta 2,2,41984
v_mov_b32 v253, v241
Mvav 72,8,204
v_accvgpr_write_b32 a178, s18
Mvaa 104,8,32
Dc 1,8,81,0
v_accvgpr_write_b32 a182, s19
Mvaa 136,8,36
Ta 4,3,40960
Ta 6,3,41984
Mvaa 140,12,36
v_mov_b32 v254, v241
Mvaa 108,12,32
v_accvgpr_write_b32 a179, s18
Mvav 76,12,204
Dc 1,8,81,1024
Ta 8,4,40960
Mvav 44,12,200
W 9
Ta 10,4,41984
v_accvgpr_write_b32 a183, s19
Mvav 48,16,200
Mvav 80,16,204
v_mov_b32 v255, v241
Mvaa 112,16,32
Dc 1,8,81,2048
v_accvgpr_write_b32 a160, 0
Mvaa 144,16,36
Ta 12,5,40960
Ta 14,5,41984
Mvaa 148,20,36
v_accvgpr_write_b32 a161, 0
Mvaa 116,20,32
v_accvgpr_write_b32 a162, 0
Mvav 84,20,204
Dc 1,8,81,3072
Ta 16,6,40960
Mvav 52,20,200
W 9
s_add_u32 m0, s83, 12288
s_add_u32 s81, s82, 12288
Mvav 56,24,200
Ta 18,6,41984
v_accvgpr_write_b32 a163, 0
Mvav 88,24,204
v_accvgpr_write_b32 a164, 0
Mvaa 120,24,32
Dc 1,8,81,0
v_accvgpr_write_b32 a165, 0
Mvaa 152,24,36
Ta 20,7,40960
Ta 22,7,41984
Mvaa 156,28,36
v_accvgpr_write_b32 a166, 0
Mvaa 124,28,32
v_accvgpr_write_b32 a167, 0
Mvav 92,28,204
Dc 1,8,81,1024
Ta 24,8,40960
Mvav 60,28,200
W 9
Ta 26,8,41984
v_accvgpr_write_b32 a168, 0
Mvav 32,0,232
Mvav 64,0,236
v_accvgpr_write_b32 a169, 0
Mvaa 96,0,40
Dc 1,8,81,2048
v_accvgpr_write_b32 a170, 0
Mvaa 128,0,44
Ta 28,9,40960
Ta 30,9,41984
Mvaa 132,4,44
v_accvgpr_write_b32 a171, 0
Mvaa 100,4,40
v_accvgpr_write_b32 a172, 0
Mvav 68,4,236
Dc 1,8,81,3072
Ta 0,2,49152
Mvav 36,4,232
W 9
s_add_u32 m0, s92, 0
s_add_u32 s81, s91, 0
Mvav 40,8,232
Ta 2,2,50176
v_accvgpr_write_b32 a173, 0
Mvav 72,8,236
v_accvgpr_write_b32 a174, 0
Mvaa 104,8,40
Dc 1,8,81,0
v_accvgpr_write_b32 a175, 0
Mvaa 136,8,44
Ta 4,3,49152
Ta 6,3,50176
Mvaa 140,12,44
s_mov_b32 s90, 0x5d800000
Mvaa 108,12,40
Mvav 76,12,236
s_mov_b64 s[88:89], 0
Dc 1,8,81,1024
Ta 8,4,49152
Mvav 44,12,232
W 9
s_mov_b32 s64, 0
Ta 10,4,50176
Mvav 48,16,232
Mvav 80,16,236
s_and_b32 s18, s14, 1
Mvaa 112,16,40
s_lshl_b32 s62, s18, 14
Dc 1,8,81,2048
Ta 12,5,49152
Mvaa 144,16,44
Ta 14,5,50176
Mvaa 148,20,44
s_lshl_b32 s20, s57, 15
Mvaa 116,20,40
Mvav 84,20,236
s_sub_u32 s20, 507904, s20
Dc 1,8,81,3072
Ta 16,6,49152
Mvav 52,20,232
W 9
s_add_u32 m0, s92, 4096
s_add_u32 s81, s91, 4096
Mvav 56,24,232
s_add_u32 s20, s20, s62
Ta 18,6,50176
Mvav 88,24,236
s_lshl_b32 s65, s57, 1
Mvaa 120,24,40
s_sub_u32 s65, s65, 1
Dc 1,8,81,0
Ta 20,7,49152
Mvaa 152,24,44
Ta 22,7,50176
Mvaa 156,28,44
s_max_i32 s65, s65, 0
Mvaa 124,28,40
Mvav 92,28,236
s_sub_u32 s66, s60, 1
Dc 1,8,81,1024
Ta 24,8,49152
Mvav 60,28,232
s_waitcnt vmcnt(26) lgkmcnt(9)
s_cmp_lt_u32 s14, 2
s_cselect_b32 s20, s20, 16384
s_cselect_b32 s66, s66, 15
s_cselect_b32 s21, 32768, 0
Ta 26,8,50176
Mvav 32,0,208
Mvav 64,0,212
s_add_u32 s61, s91, s20
Mvaa 96,0,248
s_add_u32 s62, s62, s21
Dc 1,8,81,2048
Ta 28,9,49152
Mvaa 128,0,252
Ta 30,9,50176
Mvaa 132,4,252
s_sub_u32 s63, 16384, s21
Mvaa 100,4,248
Mvav 68,4,212
s_lshl_b32 s67, s60, 7
Dc 1,8,81,3072
Ta 0,2,57344
Mvav 36,4,208
W 9
s_add_u32 m0, s92, 8192
s_add_u32 s81, s91, 8192
Mvav 40,8,208
s_add_u32 s67, s67, s56
Ta 2,2,58368
Mvav 72,8,212
s_lshl_b32 s67, s67, 12
Mvaa 104,8,248
s_add_u32 s67, s67, 39845888
Dc 1,8,81,0
Ta 4,3,57344
Mvaa 136,8,252
Ta 6,3,58368
Mvaa 140,12,252
s_lshl_b32 s69, s60, 3
Mvaa 108,12,248
Mvav 76,12,212
s_add_u32 s69, s69, s59
Dc 1,8,81,1024
Ta 8,4,57344
Mvav 44,12,208
W 9
s_lshl_b32 s69, s69, 6
Ta 10,4,58368
Mvav 48,16,208
Mvav 80,16,212
s_lshl_b32 s21, s58, 3
Mvaa 112,16,248
s_add_u32 s69, s69, s21
Dc 1,8,81,2048
Ta 12,5,57344
Mvaa 144,16,252
Ta 14,5,58368
Mvaa 148,20,252
s_add_u32 s69, s69, 20480
Mvaa 116,20,248
Mvav 84,20,212
s_mov_b32 s70, 0
Dc 1,8,81,3072
Ta 16,6,57344
Mvav 52,20,208
W 9
s_add_u32 m0, s92, 12288
s_add_u32 s81, s91, 12288
Mvav 56,24,208
s_mov_b32 s37, 0
Ta 18,6,58368
Mvav 88,24,212
)"
R"(s_sub_u32 s18, s57, 4
Mvaa 120,24,248
s_lshl_b32 s18, s18, 2
Dc 1,8,81,0
Ta 20,7,57344
Mvaa 152,24,252
Ta 22,7,58368
Mvaa 156,28,252
s_add_u32 s18, s18, s58
Mvaa 124,28,248
Mvav 92,28,212
s_mul_i32 s85, s59, 7
Dc 1,8,81,1024
Ta 24,8,57344
Mvav 60,28,208
s_waitcnt vmcnt(30) lgkmcnt(9)
s_add_u32 s85, s85, s18
Ta 26,8,58368
Mvav 32,0,184
Mvav 64,0,188
s_cmp_lt_u32 s18, 7
s_cselect_b32 s85, s85, 99
Mvaa 96,0,48
s_cmp_lt_u32 s85, 54
s_cselect_b32 s65, 2, s65
Dc 1,8,81,2048
Ta 28,9,57344
Mvaa 128,0,52
Ta 30,9,58368
Mvaa 132,4,52
s_mov_b32 s68, 0
Mvaa 100,4,48
Mvav 68,4,188
s_mov_b64 s[24:25], -1
Dc 1,8,81,3072
Mvav 36,4,184
W 8
s_mov_b32 s22, 0x3e0293ee
Mvav 40,8,184
Mvav 72,8,188
s_mov_b32 s23, 0x3e0293ee
Mvaa 104,8,48
Mvaa 136,8,52
Mvaa 140,12,52
Mvaa 108,12,48
Mvav 76,12,188
Mvav 44,12,184
W 4
Mvav 48,16,184
Mvav 80,16,188
Mvaa 112,16,48
Mvaa 144,16,52
Mvaa 148,20,52
s_waitcnt vmcnt(0) lgkmcnt(0)
s_barrier
Ra 128,242,0
Mvaa 116,20,48
Mvav 84,20,188
Ra 132,242,1024
Mvav 52,20,184
Mvav 56,24,184
Ra 136,242,2048
Mvav 88,24,188
Mvaa 120,24,48
Ra 140,242,3072
Mvaa 152,24,52
Mvaa 156,28,52
Mvaa 124,28,48
Mvav 92,28,188
Mvav 60,28,184
.Lbgo%=:
v_pk_mul_f32 v[32:33], v[32:33], s[22:23]
v_pk_mul_f32 v[34:35], v[34:35], s[22:23]
v_pk_mul_f32 v[36:37], v[36:37], s[22:23]
v_pk_mul_f32 v[38:39], v[38:39], s[22:23]
K 32,32,33
K 33,34,35
K 34,36,37
K 35,38,39
v_pk_mul_f32 v[64:65], v[64:65], s[22:23]
v_pk_mul_f32 v[66:67], v[66:67], s[22:23]
v_pk_mul_f32 v[68:69], v[68:69], s[22:23]
v_pk_mul_f32 v[70:71], v[70:71], s[22:23]
K 64,64,65
K 65,66,67
K 66,68,69
K 67,70,71
v_pk_mul_f32 v[96:97], v[96:97], s[22:23]
v_pk_mul_f32 v[98:99], v[98:99], s[22:23]
v_pk_mul_f32 v[100:101], v[100:101], s[22:23]
v_pk_mul_f32 v[102:103], v[102:103], s[22:23]
K 96,96,97
K 97,98,99
K 98,100,101
K 99,102,103
v_pk_mul_f32 v[128:129], v[128:129], s[22:23]
v_pk_mul_f32 v[130:131], v[130:131], s[22:23]
v_pk_mul_f32 v[132:133], v[132:133], s[22:23]
v_pk_mul_f32 v[134:135], v[134:135], s[22:23]
K 128,128,129
K 129,130,131
K 130,132,133
K 131,134,135
v_pk_mul_f32 v[40:41], v[40:41], s[22:23]
v_pk_mul_f32 v[42:43], v[42:43], s[22:23]
v_pk_mul_f32 v[44:45], v[44:45], s[22:23]
v_pk_mul_f32 v[46:47], v[46:47], s[22:23]
v_pk_mul_f32 v[72:73], v[72:73], s[22:23]
v_pk_mul_f32 v[74:75], v[74:75], s[22:23]
v_pk_mul_f32 v[76:77], v[76:77], s[22:23]
v_pk_mul_f32 v[78:79], v[78:79], s[22:23]
v_pk_mul_f32 v[104:105], v[104:105], s[22:23]
v_pk_mul_f32 v[106:107], v[106:107], s[22:23]
v_pk_mul_f32 v[108:109], v[108:109], s[22:23]
v_pk_mul_f32 v[110:111], v[110:111], s[22:23]
v_pk_mul_f32 v[136:137], v[136:137], s[22:23]
v_pk_mul_f32 v[138:139], v[138:139], s[22:23]
v_pk_mul_f32 v[140:141], v[140:141], s[22:23]
v_pk_mul_f32 v[142:143], v[142:143], s[22:23]
v_pk_mul_f32 v[48:49], v[48:49], s[22:23]
v_pk_mul_f32 v[50:51], v[50:51], s[22:23]
v_pk_mul_f32 v[52:53], v[52:53], s[22:23]
v_pk_mul_f32 v[54:55], v[54:55], s[22:23]
v_pk_mul_f32 v[80:81], v[80:81], s[22:23]
v_pk_mul_f32 v[82:83], v[82:83], s[22:23]
v_pk_mul_f32 v[84:85], v[84:85], s[22:23]
v_pk_mul_f32 v[86:87], v[86:87], s[22:23]
v_pk_mul_f32 v[112:113], v[112:113], s[22:23]
v_pk_mul_f32 v[114:115], v[114:115], s[22:23]
v_pk_mul_f32 v[116:117], v[116:117], s[22:23]
v_pk_mul_f32 v[118:119], v[118:119], s[22:23]
v_pk_mul_f32 v[144:145], v[144:145], s[22:23]
v_pk_mul_f32 v[146:147], v[146:147], s[22:23]
v_pk_mul_f32 v[148:149], v[148:149], s[22:23]
v_pk_mul_f32 v[150:151], v[150:151], s[22:23]
v_pk_mul_f32 v[56:57], v[56:57], s[22:23]
v_pk_mul_f32 v[58:59], v[58:59], s[22:23]
v_pk_mul_f32 v[60:61], v[60:61], s[22:23]
v_pk_mul_f32 v[62:63], v[62:63], s[22:23]
v_pk_mul_f32 v[88:89], v[88:89], s[22:23]
v_pk_mul_f32 v[90:91], v[90:91], s[22:23]
v_pk_mul_f32 v[92:93], v[92:93], s[22:23]
v_pk_mul_f32 v[94:95], v[94:95], s[22:23]
v_pk_mul_f32 v[120:121], v[120:121], s[22:23]
v_pk_mul_f32 v[122:123], v[122:123], s[22:23]
v_pk_mul_f32 v[124:125], v[124:125], s[22:23]
v_pk_mul_f32 v[126:127], v[126:127], s[22:23]
v_pk_mul_f32 v[152:153], v[152:153], s[22:23]
v_pk_mul_f32 v[154:155], v[154:155], s[22:23]
v_pk_mul_f32 v[156:157], v[156:157], s[22:23]
v_pk_mul_f32 v[158:159], v[158:159], s[22:23]
W 0
Zvav 196,128,32
Ra 144,242,4096
Ra 148,242,5120
Ra 152,242,6144
Ra 156,242,7168
Zvav 204,128,64
K 40,40,41
K 41,42,43
K 42,44,45
K 43,46,47
Zvav 212,128,96
K 72,72,73
K 73,74,75
K 74,76,77
K 75,78,79
Zvav 220,128,128
K 104,104,105
K 105,106,107
K 106,108,109
K 107,110,111
K 136,136,137
K 137,138,139
K 138,140,141
K 139,142,143
Mvav 196,132,40
Ra 128,242,8192
K 48,48,49
Mvav 204,132,72
K 49,50,51
K 50,52,53
K 51,54,55
K 80,80,81
Mvav 212,132,104
K 81,82,83
K 82,84,85
K 83,86,87
K 112,112,113
Mvav 220,132,136
K 113,114,115
K 114,116,117
K 115,118,119
K 144,144,145
K 145,146,147
K 146,148,149
K 147,150,151
Mvav 196,136,48
Ra 132,242,9216
K 56,56,57
Mvav 204,136,80
K 57,58,59
K 58,60,61
K 59,62,63
K 88,88,89
Mvav 212,136,112
K 89,90,91
K 90,92,93
K 91,94,95
K 120,120,121
Mvav 220,136,144
K 121,122,123
K 122,124,125
K 123,126,127
K 152,152,153
K 153,154,155
K 154,156,157
K 155,158,159
Mvav 196,140,56
s_add_u32 s80, s62, 131072
Ra 136,242,10240
Mvav 204,140,88
Mvav 212,140,120
v_add_u32 v12, s80, v1
Mvav 220,140,152
W 3
ds_write_b128 v12, v[32:35] offset:0
Zvav 36,144,32
Ra 0,12,0
Ra 140,242,11264
Zvav 68,144,64
v_max3_f32 v4, v196, v197, v198
v_max3_f32 v4, v4, v199, v252
v_max3_f32 v5, v204, v205, v206
v_max3_f32 v5, v5, v207, v253
Zvav 100,144,96
v_max3_f32 v6, v212, v213, v214
v_max3_f32 v6, v6, v215, v254
v_max3_f32 v7, v220, v221, v222
v_max3_f32 v7, v7, v223, v255
Zvav 132,144,128
v_mov_b32 v8, v4
v_mov_b32 v9, v5
v_mov_b32 v10, v6
v_mov_b32 v11, v7
Mvav 36,148,40
ds_write_b128 v12, v[40:43] offset:1024
v_permlane32_swap_b32 v4, v8
v_permlane32_swap_b32 v5, v9
v_permlane32_swap_b32 v6, v10
Mvav 68,148,72
v_permlane32_swap_b32 v7, v11
v_max_f32 v4, v4, v8
v_max_f32 v5, v5, v9
v_max_f32 v6, v6, v10
Mvav 100,148,104
v_max_f32 v7, v7, v11
v_mov_b32 v8, v4
v_mov_b32 v9, v5
v_mov_b32 v10, v6
Mvav 132,148,136
Ra 4,12,1024
v_mov_b32 v11, v7
v_permlane16_swap_b32 v4, v8
v_permlane16_swap_b32 v5, v9
Mvav 36,152,48
v_permlane16_swap_b32 v6, v10
v_permlane16_swap_b32 v7, v11
v_max_f32 v4, v4, v8
v_max_f32 v5, v5, v9
Mvav 68,152,80
v_max_f32 v6, v6, v10
v_max_f32 v7, v7, v11
v_sub_f32 v4, 0, v4
v_sub_f32 v5, 0, v5
Mvav 100,152,112
v_sub_f32 v6, 0, v6
v_sub_f32 v7, 0, v7
v_mov_b32 v224, v4
v_mov_b32 v225, v4
Mvav 132,152,144
v_mov_b32 v226, v4
v_mov_b32 v227, v4
v_mov_b32 v228, v5
v_mov_b32 v229, v5
Mvav 36,156,56
v_mov_b32 v230, v5
v_mov_b32 v231, v5
v_mov_b32 v232, v6
v_mov_b32 v233, v6
Mvav 68,156,88
v_mov_b32 v234, v6
v_mov_b32 v235, v6
v_mov_b32 v236, v7
v_mov_b32 v237, v7
Mvav 100,156,120
ds_write_b128 v12, v[48:51] offset:2048
Ra 144,242,12288
v_mov_b32 v238, v7
v_mov_b32 v239, v7
Mvav 132,156,152
W 4
Ra 8,12,2048
ds_write_b128 v12, v[56:59] offset:3072
Ra 12,12,3072
Ra 148,242,13312
Cvav 160,128,32,224
s_and_b32 s80, s14, 1
ds_write_b128 v12, v[64:67] offset:4096
Ra 16,12,4096
ds_write_b128 v12, v[72:75] offset:5120
Ra 152,242,14336
Cvav 164,128,64,228
s_lshl_b32 s80, s80, 14
Ra 20,12,5120
ds_write_b128 v12, v[80:83] offset:6144
A 196,196,224
A 197,197,224
Cvav 168,128,96,232
s_add_u32 s80, s80, 98304
Ra 24,12,6144
Ra 156,242,15360
A 198,198,224
A 199,199,224
Cvav 172,128,128,236
X 196
X 197
Mvav 160,132,40
ds_write_b128 v12, v[88:91] offset:7168
Ra 28,12,7168
X 198
Mvav 164,132,72
ds_write_b128 v12, v[96:99] offset:8192
K 192,196,197
X 199
Mvav 168,132,104
A 204,204,228
v_add_u32 v8, s80, v1
K 193,198,199
A 205,205,228
Mvav 172,132,136
Ra 128,242,24576
Ra 32,12,8192
A 206,206,228
A 207,207,228
Mvav 160,136,48
ds_write_b128 v8, v[4:7]
ds_write_b128 v12, v[104:107] offset:9216
X 204
Mvav 164,136,80
X 205
X 206
Mvav 168,136,112
Ra 36,12,9216
K 200,204,205
X 207
Mvav 172,136,144
ds_write_b128 v12, v[112:115] offset:10240
Ra 132,242,25600
A 212,212,232
K 201,206,207
Mvav 160,140,56
Ra 40,12,10240
A 213,213,232
A 214,214,232
A 215,215,232
Mvav 164,140,88
ds_write_b128 v12, v[120:123] offset:11264
Ra 44,12,11264
X 212
Mvav 168,140,120
X 213
X 214
Mvav 172,140,152
W 13
Ra 136,242,26624
K 208,212,213
X 215
Cvav 176,144,32,224
ds_write_b128 v12, v[128:131] offset:12288
A 220,220,236
K 209,214,215
A 221,221,236
Cvav 180,144,64,228
Ra 48,12,12288
ds_write_b128 v12, v[136:139] offset:13312
A 222,222,236
A 223,223,236
Cvav 184,144,96,232
X 220
X 221
Cvav 188,144,128,236
Ra 140,242,27648
Ra 52,12,13312
X 222
Mvav 176,148,40
ds_write_b128 v12, v[144:147] offset:14336
K 216,220,221
X 223
Mvav 180,148,72
)"
R"(Ra 56,12,14336
A 36,36,224
K 217,222,223
A 37,37,224
Mvav 184,148,104
Ra 144,242,28672
ds_write_b128 v12, v[152:155] offset:15360
A 38,38,224
A 39,39,224
Mvav 188,148,136
X 36
X 37
Mvav 176,152,48
Ra 60,12,15360
X 38
K 194,36,37
Mvav 180,152,80
Ra 148,242,29696
X 39
A 68,68,228
Mvav 184,152,112
K 195,38,39
A 69,69,228
A 70,70,228
A 71,71,228
Mvav 188,152,144
X 68
X 69
Mvav 176,156,56
Ra 152,242,30720
X 70
K 202,68,69
Mvav 180,156,88
W 3
s_barrier
X 71
A 100,100,232
Mvav 184,156,120
K 203,70,71
A 101,101,232
A 102,102,232
A 103,103,232
X 100
X 101
X 102
K 210,100,101
X 103
A 132,132,236
K 211,102,103
A 133,133,236
A 134,134,236
A 135,135,236
X 132
X 133
X 134
K 218,132,133
X 135
s_nop 0
K 219,134,135
Mvav 188,156,152
Zvav 32,128,192
Ra 156,242,31744
Zvav 64,128,200
Zvav 96,128,208
s_xor_b32 s62, s62, 0x10000
Zvav 128,128,216
v_add_u32 v23, 61440, v21
Zvav 132,132,216
v_add_u32 v24, 61440, v22
Zvav 100,132,208
v_add_u32 v25, 122880, v21
Zvav 68,132,200
v_add_u32 v26, 122880, v22
Zvav 36,132,192
Zvav 40,136,192
X 172
Zvav 72,136,200
X 173
Zvav 104,136,208
X 174
K 220,172,173
Zvav 136,136,216
X 175
Zvav 140,140,216
X 168
K 221,174,175
Zvav 108,140,208
X 169
X 170
Zvav 76,140,200
s_waitcnt vmcnt(0) lgkmcnt(0)
s_barrier
v_xor_b32 v242, 0x10000, v242
K 212,168,169
Zvav 44,140,192
X 171
X 164
Zvav 48,144,192
Ra 128,242,0
K 213,170,171
X 165
Zvav 80,144,200
X 166
Zvav 112,144,208
Ra 132,242,1024
K 204,164,165
X 167
Zvav 144,144,216
X 160
Zvav 148,148,216
K 205,166,167
X 161
Zvav 116,148,208
Ra 136,242,2048
X 162
Zvav 84,148,200
K 196,160,161
X 163
Zvav 52,148,192
X 176
K 197,162,163
Zvav 56,152,192
Ra 140,242,3072
X 177
Zvav 88,152,200
X 178
K 198,176,177
Zvav 120,152,208
X 179
X 180
Zvav 152,152,216
K 199,178,179
Zvav 156,156,216
X 181
X 182
Zvav 124,156,208
K 206,180,181
X 183
Zvav 92,156,200
K 207,182,183
Zvav 60,156,192
s_cmp_eq_u64 s[24:25], 0
s_cbranch_scc1 .Ll1_32%=
W 0
Cvaa 160,128,0,224
Ra 144,242,4096
Ra 148,242,5120
Cvaa 164,128,16,228
s_add_u32 m0, s62, 0
Ra 152,242,6144
Cvaa 168,128,32,232
X 184
Cvaa 172,128,48,236
X 185
Mvaa 160,132,4
X 186
Mvaa 164,132,20
Ra 156,242,7168
K 214,184,185
Mvaa 168,132,36
Dc 1,8,61,0
Mvaa 172,132,52
X 187
Mvaa 160,136,8
X 188
Mvaa 164,136,24
Ra 128,242,16384
K 215,186,187
Mvaa 168,136,40
Dc 1,8,61,1024
Mvaa 172,136,56
X 189
Mvaa 160,140,12
X 190
Mvaa 164,140,28
Ra 132,242,17408
K 222,188,189
Mvaa 168,140,44
Dc 1,8,61,2048
Mvaa 172,140,60
X 191
v_smfmac_f32_16x16x64_bf16 a[160:163], a[176:179], v[192:199], v240
Ra 136,242,18432
K 223,190,191
v_smfmac_f32_16x16x64_bf16 a[164:167], a[176:179], v[200:207], v240
X 160
v_smfmac_f32_16x16x64_bf16 a[168:171], a[176:179], v[208:215], v240
Dc 1,8,61,3072
v_smfmac_f32_16x16x64_bf16 a[172:175], a[176:179], v[216:223], v240
W 3
X 161
Cvaa 176,144,0,224
s_add_u32 m0, s62, 4096
X 162
Cvaa 180,144,16,228
Ra 140,242,19456
K 192,160,161
Cvaa 184,144,32,232
X 163
Cvaa 188,144,48,236
Dc 243,8,61,0
Mvaa 176,148,4
X 164
Mvaa 180,148,20
Ra 144,242,20480
K 193,162,163
Mvaa 184,148,36
X 165
Mvaa 188,148,52
Dc 243,8,61,1024
Mvaa 176,152,8
X 166
Mvaa 180,152,24
Ra 148,242,21504
K 200,164,165
Mvaa 184,152,40
X 167
Mvaa 188,152,56
Dc 243,8,61,2048
Mvaa 176,156,12
X 168
Mvaa 180,156,28
Ra 152,242,22528
K 201,166,167
Mvaa 184,156,44
X 169
Mvaa 188,156,60
W 3
Dc 243,8,61,3072
Mvav 32,128,196
s_add_u32 m0, s62, 8192
X 170
Mvav 64,128,204
Ra 156,242,23552
K 208,168,169
Mvav 96,128,212
X 171
Mvav 128,128,220
Dc 244,8,61,0
Mvav 132,132,220
X 172
Mvav 100,132,212
Ra 128,242,8192
K 209,170,171
Mvav 68,132,204
X 173
Mvav 36,132,196
Dc 244,8,61,1024
Mvav 40,136,196
X 174
Mvav 72,136,204
Ra 132,242,9216
K 216,172,173
Mvav 104,136,212
X 175
Mvav 136,136,220
Dc 244,8,61,2048
Mvav 140,140,220
X 176
Mvav 108,140,212
Ra 136,242,10240
K 217,174,175
Mvav 76,140,204
X 177
Mvav 44,140,196
W 3
Dc 244,8,61,3072
Mvav 48,144,196
X 178
Mvav 80,144,204
s_add_u32 m0, s62, 12288
Ra 140,242,11264
Mvav 112,144,212
K 194,176,177
Mvav 144,144,220
X 179
Mvav 148,148,220
X 180
Mvav 116,148,212
Dc 245,8,61,0
Ra 144,242,12288
Mvav 84,148,204
K 195,178,179
Mvav 52,148,196
X 181
Mvav 56,152,196
X 182
Mvav 88,152,204
Dc 245,8,61,1024
Ra 148,242,13312
Mvav 120,152,212
s_cmp_eq_u32 s14, 2
s_cselect_b64 s[18:19], s[30:31], s[32:33]
K 202,180,181
Mvav 152,152,220
s_lshr_b32 s81, s15, 3
X 183
Mvav 156,156,220
s_mul_i32 s82, s81, 69120
X 184
Mvav 124,156,212
s_mul_i32 s83, s59, 448
Dc 245,8,61,2048
Ra 152,242,14336
Mvav 92,156,204
s_andn2_b32 s83, s83, 127
K 203,182,183
Mvav 60,156,196
W 3
X 185
Cvaa 160,128,0,224
s_add_u32 s82, s82, s83
X 186
Cvaa 164,128,16,228
Dc 245,8,61,3072
Ra 156,242,15360
Cvaa 168,128,32,232
s_add_u32 s61, s61, s63
v_min_u32 v246, 624, v1
v_lshrrev_b32 v247, 6, v246
Cvaa 172,128,48,236
K 210,184,185
v_mul_u32_u24 v247, 3456, v247
Mvaa 160,132,4
X 187
Mvaa 164,132,20
Ra 128,242,24576
v_and_b32 v246, 48, v246
Mvaa 168,132,36
s_xor_b32 s62, s62, 0x10000
X 188
Mvaa 172,132,52
v_lshl_add_u32 v246, v246, 3, v247
v_add_u32 v246, s82, v246
Mvaa 160,136,8
global_load_dword v3, v246, s[18:19]
K 211,186,187
Mvaa 164,136,24
s_add_u32 s64, s64, 1
X 189
Mvaa 168,136,40
Ra 132,242,25600
v_add_u32 v246, 34560, v246
Mvaa 172,136,56
X 190
Mvaa 160,140,12
v_min_u32 v246, 2211836, v246
K 218,188,189
Mvaa 164,140,28
global_load_dword v3, v246, s[18:19]
Ra 136,242,26624
Mvaa 168,140,44
s_add_u32 s36, s29, 131072
X 191
Mvaa 172,140,60
W 3
X 160
Cvaa 176,144,0,224
Ra 140,242,27648
K 219,190,191
Cvaa 180,144,16,228
X 161
Cvaa 184,144,32,232
X 162
Cvaa 188,144,48,236
K 196,160,161
Mvaa 176,148,4
X 163
Mvaa 180,148,20
buffer_store_dwordx4 a[124:127], v1, s[8:11], s36 offen offset:0 sc1
Ra 144,242,28672
Mvaa 184,148,36
X 164
Mvaa 188,148,52
K 197,162,163
Mvaa 176,152,8
X 165
Mvaa 180,152,24
Ra 148,242,29696
Mvaa 184,152,40
X 166
Mvaa 188,152,56
buffer_store_dwordx4 a[116:119], v1, s[8:11], s36 offen offset:1024 sc1
K 204,164,165
Mvaa 176,156,12
X 167
Mvaa 180,156,28
Ra 152,242,30720
Mvaa 184,156,44
X 168
Mvaa 188,156,60
W 3
K 205,166,167
Mvav 32,128,192
X 169
Mvav 64,128,200
buffer_store_dwordx4 a[108:111], v1, s[8:11], s36 offen offset:2048 sc1
Ra 156,242,31744
Mvav 96,128,208
X 170
)"
R"(Mvav 128,128,216
K 212,168,169
Mvav 132,132,216
X 171
Mvav 100,132,208
X 172
Mvav 68,132,200
buffer_store_dwordx4 a[188:191], v1, s[8:11], s36 offen offset:3072 sc1
Mvav 36,132,192
K 213,170,171
Mvav 40,136,192
X 173
Mvav 72,136,200
X 174
Mvav 104,136,208
Mvav 136,136,216
s_add_u32 s36, s29, 135168
K 220,172,173
Mvav 140,140,216
X 175
Mvav 108,140,208
X 176
Mvav 76,140,200
s_waitcnt vmcnt(6) lgkmcnt(0)
s_barrier
v_xor_b32 v242, 0x10000, v242
Mvav 44,140,192
Ra 128,242,0
K 221,174,175
Mvav 48,144,192
X 177
Mvav 80,144,200
buffer_store_dwordx4 a[196:199], v1, s[8:11], s36 offen offset:0 sc1
Mvav 112,144,208
X 178
Mvav 144,144,216
Ra 132,242,1024
K 198,176,177
Mvav 148,148,216
X 179
Mvav 116,148,208
X 180
Mvav 84,148,200
Ra 136,242,2048
Mvav 52,148,192
buffer_store_dwordx4 a[220:223], v1, s[8:11], s36 offen offset:1024 sc1
K 199,178,179
Mvav 56,152,192
X 181
Mvav 88,152,200
Ra 140,242,3072
Mvav 120,152,208
X 182
Mvav 152,152,216
K 206,180,181
Mvav 156,156,216
X 183
Mvav 124,156,208
Mvav 92,156,200
K 207,182,183
Mvav 60,156,192
W 0
Cvaa 160,128,0,224
Ra 144,242,4096
Ra 148,242,5120
Cvaa 164,128,16,228
s_add_u32 m0, s62, 0
Ra 152,242,6144
Cvaa 168,128,32,232
X 184
Cvaa 172,128,48,236
X 185
Mvaa 160,132,4
X 186
Mvaa 164,132,20
Ra 156,242,7168
K 214,184,185
Mvaa 168,132,36
Dc 1,8,61,0
Mvaa 172,132,52
X 187
Mvaa 160,136,8
X 188
Mvaa 164,136,24
Ra 128,242,16384
K 215,186,187
Mvaa 168,136,40
Dc 1,8,61,1024
Mvaa 172,136,56
X 189
Mvaa 160,140,12
X 190
Mvaa 164,140,28
Ra 132,242,17408
K 222,188,189
Mvaa 168,140,44
Dc 1,8,61,2048
Mvaa 172,140,60
X 191
v_smfmac_f32_16x16x64_bf16 a[160:163], a[176:179], v[192:199], v240
Ra 136,242,18432
K 223,190,191
v_smfmac_f32_16x16x64_bf16 a[164:167], a[176:179], v[200:207], v240
X 160
v_smfmac_f32_16x16x64_bf16 a[168:171], a[176:179], v[208:215], v240
Dc 1,8,61,3072
v_smfmac_f32_16x16x64_bf16 a[172:175], a[176:179], v[216:223], v240
W 3
X 161
Cvaa 176,144,0,224
s_add_u32 m0, s62, 4096
X 162
Cvaa 180,144,16,228
Ra 140,242,19456
K 192,160,161
Cvaa 184,144,32,232
X 163
Cvaa 188,144,48,236
Dc 243,8,61,0
Mvaa 176,148,4
X 164
Mvaa 180,148,20
Ra 144,242,20480
K 193,162,163
Mvaa 184,148,36
X 165
Mvaa 188,148,52
Dc 243,8,61,1024
Mvaa 176,152,8
X 166
Mvaa 180,152,24
Ra 148,242,21504
K 200,164,165
Mvaa 184,152,40
X 167
Mvaa 188,152,56
Dc 243,8,61,2048
Mvaa 176,156,12
X 168
Mvaa 180,156,28
Ra 152,242,22528
K 201,166,167
Mvaa 184,156,44
X 169
Mvaa 188,156,60
W 3
Dc 243,8,61,3072
Mvav 32,128,196
s_add_u32 m0, s62, 8192
X 170
Mvav 64,128,204
Ra 156,242,23552
K 208,168,169
Mvav 96,128,212
X 171
Mvav 128,128,220
Dc 244,8,61,0
Mvav 132,132,220
X 172
Mvav 100,132,212
Ra 128,242,8192
K 209,170,171
Mvav 68,132,204
X 173
Mvav 36,132,196
Dc 244,8,61,1024
Mvav 40,136,196
X 174
Mvav 72,136,204
Ra 132,242,9216
K 216,172,173
Mvav 104,136,212
X 175
Mvav 136,136,220
Dc 244,8,61,2048
Mvav 140,140,220
X 176
Mvav 108,140,212
Ra 136,242,10240
K 217,174,175
Mvav 76,140,204
X 177
Mvav 44,140,196
W 3
Dc 244,8,61,3072
Mvav 48,144,196
X 178
Mvav 80,144,204
s_add_u32 m0, s62, 12288
Ra 140,242,11264
Mvav 112,144,212
K 194,176,177
Mvav 144,144,220
X 179
Mvav 148,148,220
X 180
Mvav 116,148,212
Dc 245,8,61,0
Ra 144,242,12288
Mvav 84,148,204
K 195,178,179
Mvav 52,148,196
X 181
Mvav 56,152,196
X 182
Mvav 88,152,204
Dc 245,8,61,1024
Ra 148,242,13312
Mvav 120,152,212
K 202,180,181
Mvav 152,152,220
X 183
Mvav 156,156,220
X 184
Mvav 124,156,212
Dc 245,8,61,2048
Ra 152,242,14336
Mvav 92,156,204
K 203,182,183
v_bfe_u32 v28, v1, 4, 4
Mvav 60,156,196
W 3
X 185
Cvaa 160,128,0,224
Ra 156,242,15360
v_lshlrev_b32 v28, 3, v28
Cvaa 164,128,16,228
s_lshl_b32 s80, s56, 9
s_add_u32 s80, s80, 4224
Dc 245,8,61,3072
Cvaa 168,128,32,232
s_add_u32 s61, s61, s63
X 186
Cvaa 172,128,48,236
K 210,184,185
Mvaa 160,132,4
X 187
Mvaa 164,132,20
buffer_load_dwordx2 v[28:29], v28, s[8:11], s80 offen sc1
Ra 128,242,24576
Mvaa 168,132,36
s_xor_b32 s62, s62, 0x10000
X 188
Mvaa 172,132,52
K 211,186,187
Mvaa 160,136,8
X 189
Mvaa 164,136,24
s_add_u32 s64, s64, 1
X 190
Mvaa 168,136,40
Ra 132,242,25600
K 218,188,189
Mvaa 172,136,56
X 191
Mvaa 160,140,12
Ra 136,242,26624
Mvaa 164,140,28
Mvaa 168,140,44
buffer_store_dwordx4 a[228:231], v1, s[8:11], s36 offen offset:2048 sc1
Mvaa 172,140,60
W 3
Cvaa 176,144,0,224
Ra 140,242,27648
Cvaa 180,144,16,228
X 160
Cvaa 184,144,32,232
K 219,190,191
Cvaa 188,144,48,236
X 161
Mvaa 176,148,4
X 162
Mvaa 180,148,20
Ra 144,242,28672
K 196,160,161
Mvaa 184,148,36
X 163
Mvaa 188,148,52
X 164
Mvaa 176,152,8
buffer_store_dwordx4 a[76:79], v1, s[8:11], s36 offen offset:3072 sc1
K 197,162,163
Mvaa 180,152,24
X 165
Mvaa 184,152,40
Ra 148,242,29696
Mvaa 188,152,56
X 166
Mvaa 176,156,12
s_add_u32 s36, s29, 327680
Ra 152,242,30720
K 204,164,165
Mvaa 180,156,28
X 167
Mvaa 184,156,44
X 168
Mvaa 188,156,60
W 3
K 205,166,167
Mvav 32,128,192
X 169
Mvav 64,128,200
Ra 156,242,31744
Mvav 96,128,208
X 170
Mvav 128,128,216
buffer_store_dwordx4 a[84:87], v1, s[8:11], s36 offen offset:0 sc1
K 212,168,169
Mvav 132,132,216
X 171
Mvav 100,132,208
X 172
Mvav 68,132,200
Mvav 36,132,192
K 213,170,171
Mvav 40,136,192
X 173
Mvav 72,136,200
buffer_store_dwordx4 a[92:95], v1, s[8:11], s36 offen offset:1024 sc1
Mvav 104,136,208
X 174
Mvav 136,136,216
s_waitcnt vmcnt(4)
v_cmp_eq_u64 vcc, s[12:13], v[28:29]
s_cmp_eq_u64 vcc, -1
s_cbranch_scc0 .Lspin_62%=
.Lok_63%=:
K 220,172,173
Mvav 140,140,216
X 175
Mvav 108,140,208
X 176
Mvav 76,140,200
W 0
s_barrier
v_xor_b32 v242, 0x10000, v242
Mvav 44,140,192
buffer_store_dwordx4 a[204:207], v1, s[8:11], s36 offen offset:2048 sc1
K 221,174,175
Mvav 48,144,192
Ra 128,242,0
Mvav 80,144,200
X 177
Mvav 112,144,208
X 178
Mvav 144,144,216
Ra 132,242,1024
K 198,176,177
Mvav 148,148,216
X 179
Mvav 116,148,208
X 180
Mvav 84,148,200
Ra 136,242,2048
buffer_store_dwordx4 a[212:215], v1, s[8:11], s36 offen offset:3072 sc1
Mvav 52,148,192
K 199,178,179
Mvav 56,152,192
X 181
Mvav 88,152,200
Ra 140,242,3072
Mvav 120,152,208
X 182
Mvav 152,152,216
K 206,180,181
Mvav 156,156,216
X 183
Mvav 124,156,208
Mvav 92,156,200
K 207,182,183
Mvav 60,156,192
W 0
Cvaa 160,128,0,224
Ra 144,242,4096
Ra 148,242,5120
Cvaa 164,128,16,228
s_add_u32 m0, s62, 0
Ra 152,242,6144
Cvaa 168,128,32,232
X 184
Cvaa 172,128,48,236
X 185
Mvaa 160,132,4
X 186
Mvaa 164,132,20
Ra 156,242,7168
K 214,184,185
Mvaa 168,132,36
Dc 1,8,61,0
Mvaa 172,132,52
X 187
Mvaa 160,136,8
X 188
Mvaa 164,136,24
Ra 128,242,16384
K 215,186,187
Mvaa 168,136,40
Dc 1,8,61,1024
Mvaa 172,136,56
X 189
)"
R"(Mvaa 160,140,12
X 190
Mvaa 164,140,28
Ra 132,242,17408
K 222,188,189
Mvaa 168,140,44
Dc 1,8,61,2048
Mvaa 172,140,60
X 191
v_smfmac_f32_16x16x64_bf16 a[160:163], a[176:179], v[192:199], v240
Ra 136,242,18432
K 223,190,191
v_smfmac_f32_16x16x64_bf16 a[164:167], a[176:179], v[200:207], v240
X 160
v_smfmac_f32_16x16x64_bf16 a[168:171], a[176:179], v[208:215], v240
Dc 1,8,61,3072
v_smfmac_f32_16x16x64_bf16 a[172:175], a[176:179], v[216:223], v240
W 3
X 161
Cvaa 176,144,0,224
s_add_u32 m0, s62, 4096
X 162
Cvaa 180,144,16,228
Ra 140,242,19456
K 192,160,161
Cvaa 184,144,32,232
X 163
Cvaa 188,144,48,236
Dc 243,8,61,0
Mvaa 176,148,4
X 164
Mvaa 180,148,20
Ra 144,242,20480
K 193,162,163
Mvaa 184,148,36
X 165
Mvaa 188,148,52
Dc 243,8,61,1024
Mvaa 176,152,8
X 166
Mvaa 180,152,24
Ra 148,242,21504
K 200,164,165
Mvaa 184,152,40
X 167
Mvaa 188,152,56
Dc 243,8,61,2048
Mvaa 176,156,12
X 168
Mvaa 180,156,28
Ra 152,242,22528
K 201,166,167
Mvaa 184,156,44
X 169
Mvaa 188,156,60
W 3
Dc 243,8,61,3072
Mvav 32,128,196
s_add_u32 m0, s62, 8192
X 170
Mvav 64,128,204
Ra 156,242,23552
K 208,168,169
Mvav 96,128,212
X 171
Mvav 128,128,220
Dc 244,8,61,0
Mvav 132,132,220
X 172
Mvav 100,132,212
Ra 128,242,8192
K 209,170,171
Mvav 68,132,204
X 173
Mvav 36,132,196
Dc 244,8,61,1024
Mvav 40,136,196
X 174
Mvav 72,136,204
Ra 132,242,9216
K 216,172,173
Mvav 104,136,212
X 175
Mvav 136,136,220
Dc 244,8,61,2048
Mvav 140,140,220
X 176
Mvav 108,140,212
Ra 136,242,10240
K 217,174,175
Mvav 76,140,204
X 177
Mvav 44,140,196
W 3
Dc 244,8,61,3072
Mvav 48,144,196
X 178
Mvav 80,144,204
s_add_u32 m0, s62, 12288
Ra 140,242,11264
Mvav 112,144,212
K 194,176,177
Mvav 144,144,220
X 179
Mvav 148,148,220
X 180
Mvav 116,148,212
Dc 245,8,61,0
Ra 144,242,12288
Mvav 84,148,204
K 195,178,179
Mvav 52,148,196
X 181
Mvav 56,152,196
X 182
Mvav 88,152,204
Dc 245,8,61,1024
Ra 148,242,13312
Mvav 120,152,212
K 202,180,181
Mvav 152,152,220
X 183
Mvav 156,156,220
X 184
Mvav 124,156,212
Dc 245,8,61,2048
Ra 152,242,14336
Mvav 92,156,204
s_mov_b64 s[18:19], s[34:35]
K 203,182,183
Mvav 60,156,196
W 3
X 185
Cvaa 160,128,0,224
s_lshr_b32 s81, s15, 3
X 186
Cvaa 164,128,16,228
s_lshl_b32 s82, s59, 5
Dc 245,8,61,3072
Ra 156,242,15360
Cvaa 168,128,32,232
s_add_u32 s81, s81, s82
s_add_u32 s61, s61, s63
K 210,184,185
Cvaa 172,128,48,236
s_lshl_b32 s81, s81, 1
X 187
Mvaa 160,132,4
s_add_u32 s81, s81, s14
X 188
Mvaa 164,132,20
s_sub_u32 s81, s81, 2
s_mul_i32 s81, s81, 4480
Ra 128,242,24576
Mvaa 168,132,36
s_xor_b32 s62, s62, 0x10000
K 211,186,187
v_min_u32 v246, 544, v1
Mvaa 172,132,52
X 189
Mvaa 160,136,8
Ra 132,242,25600
v_lshl_add_u32 v246, v246, 3, s81
Mvaa 164,136,24
s_add_u32 s64, s64, 1
X 190
Mvaa 168,136,40
v_min_u32 v246, 2211836, v246
K 218,188,189
Mvaa 172,136,56
global_load_dword v3, v246, s[18:19]
Mvaa 160,140,12
X 191
Mvaa 164,140,28
Ra 136,242,26624
Mvaa 168,140,44
s_add_u32 s36, s29, 331776
Mvaa 172,140,60
W 3
Cvaa 176,144,0,224
X 160
Cvaa 180,144,16,228
Ra 140,242,27648
K 219,190,191
Cvaa 184,144,32,232
X 161
Cvaa 188,144,48,236
X 162
Mvaa 176,148,4
buffer_store_dwordx4 a[236:239], v1, s[8:11], s36 offen offset:0 sc1
K 196,160,161
Mvaa 180,148,20
X 163
Mvaa 184,148,36
Ra 144,242,28672
Mvaa 188,148,52
X 164
Mvaa 176,152,8
Ra 148,242,29696
K 197,162,163
Mvaa 180,152,24
X 165
Mvaa 184,152,40
X 166
Mvaa 188,152,56
buffer_store_dwordx4 a[244:247], v1, s[8:11], s36 offen offset:1024 sc1
K 204,164,165
Mvaa 176,156,12
X 167
Mvaa 180,156,28
Ra 152,242,30720
Mvaa 184,156,44
X 168
Mvaa 188,156,60
W 3
K 205,166,167
Mvav 32,128,192
X 169
Mvav 64,128,200
buffer_store_dwordx4 a[68:71], v1, s[8:11], s36 offen offset:2048 sc1
Ra 156,242,31744
Mvav 96,128,208
X 170
Mvav 128,128,216
K 212,168,169
Mvav 132,132,216
X 171
Mvav 100,132,208
X 172
Mvav 68,132,200
buffer_store_dwordx4 a[184:187], v1, s[8:11], s36 offen offset:3072 sc1
Mvav 36,132,192
K 213,170,171
Mvav 40,136,192
X 173
Mvav 72,136,200
X 174
Mvav 104,136,208
Mvav 136,136,216
s_add_u32 s36, s29, 196608
K 220,172,173
Mvav 140,140,216
X 175
Mvav 108,140,208
X 176
Mvav 76,140,200
s_waitcnt vmcnt(5) lgkmcnt(0)
s_barrier
v_xor_b32 v242, 0x10000, v242
Mvav 44,140,192
Ra 128,242,0
K 221,174,175
Mvav 48,144,192
X 177
Mvav 80,144,200
buffer_store_dwordx4 a[192:195], v1, s[8:11], s36 offen offset:0 sc1
Mvav 112,144,208
X 178
Mvav 144,144,216
Ra 132,242,1024
K 198,176,177
Mvav 148,148,216
X 179
Mvav 116,148,208
X 180
Mvav 84,148,200
Ra 136,242,2048
Mvav 52,148,192
buffer_store_dwordx4 a[200:203], v1, s[8:11], s36 offen offset:1024 sc1
K 199,178,179
Mvav 56,152,192
X 181
Mvav 88,152,200
Ra 140,242,3072
Mvav 120,152,208
X 182
Mvav 152,152,216
K 206,180,181
Mvav 156,156,216
X 183
Mvav 124,156,208
Mvav 92,156,200
K 207,182,183
Mvav 60,156,192
W 0
Cvaa 160,128,0,224
Ra 144,242,4096
Ra 148,242,5120
Cvaa 164,128,16,228
s_add_u32 m0, s62, 0
Ra 152,242,6144
Cvaa 168,128,32,232
X 184
Cvaa 172,128,48,236
X 185
Mvaa 160,132,4
X 186
Mvaa 164,132,20
Ra 156,242,7168
K 214,184,185
Mvaa 168,132,36
Dc 1,8,61,0
Mvaa 172,132,52
X 187
Mvaa 160,136,8
X 188
Mvaa 164,136,24
Ra 128,242,16384
K 215,186,187
Mvaa 168,136,40
Dc 1,8,61,1024
Mvaa 172,136,56
X 189
Mvaa 160,140,12
X 190
Mvaa 164,140,28
Ra 132,242,17408
K 222,188,189
Mvaa 168,140,44
Dc 1,8,61,2048
Mvaa 172,140,60
X 191
v_smfmac_f32_16x16x64_bf16 a[160:163], a[176:179], v[192:199], v240
Ra 136,242,18432
K 223,190,191
v_smfmac_f32_16x16x64_bf16 a[164:167], a[176:179], v[200:207], v240
X 160
v_smfmac_f32_16x16x64_bf16 a[168:171], a[176:179], v[208:215], v240
Dc 1,8,61,3072
v_smfmac_f32_16x16x64_bf16 a[172:175], a[176:179], v[216:223], v240
W 3
X 161
Cvaa 176,144,0,224
s_add_u32 m0, s62, 4096
X 162
Cvaa 180,144,16,228
Ra 140,242,19456
K 192,160,161
Cvaa 184,144,32,232
X 163
Cvaa 188,144,48,236
Dc 243,8,61,0
Mvaa 176,148,4
X 164
Mvaa 180,148,20
Ra 144,242,20480
K 193,162,163
Mvaa 184,148,36
X 165
Mvaa 188,148,52
Dc 243,8,61,1024
Mvaa 176,152,8
X 166
Mvaa 180,152,24
Ra 148,242,21504
K 200,164,165
Mvaa 184,152,40
X 167
Mvaa 188,152,56
Dc 243,8,61,2048
Mvaa 176,156,12
X 168
Mvaa 180,156,28
Ra 152,242,22528
K 201,166,167
Mvaa 184,156,44
X 169
Mvaa 188,156,60
W 3
Dc 243,8,61,3072
Mvav 32,128,196
s_add_u32 m0, s62, 8192
X 170
Mvav 64,128,204
Ra 156,242,23552
K 208,168,169
Mvav 96,128,212
X 171
Mvav 128,128,220
Dc 244,8,61,0
Mvav 132,132,220
X 172
Mvav 100,132,212
Ra 128,242,8192
K 209,170,171
Mvav 68,132,204
X 173
Mvav 36,132,196
Dc 244,8,61,1024
Mvav 40,136,196
X 174
Mvav 72,136,204
Ra 132,242,9216
K 216,172,173
Mvav 104,136,212
X 175
Mvav 136,136,220
Dc 244,8,61,2048
Mvav 140,140,220
X 176
)"
R"(Mvav 108,140,212
Ra 136,242,10240
K 217,174,175
Mvav 76,140,204
X 177
Mvav 44,140,196
W 3
Dc 244,8,61,3072
Mvav 48,144,196
X 178
Mvav 80,144,204
s_add_u32 m0, s62, 12288
Ra 140,242,11264
Mvav 112,144,212
K 194,176,177
Mvav 144,144,220
X 179
Mvav 148,148,220
X 180
Mvav 116,148,212
Dc 245,8,61,0
Ra 144,242,12288
Mvav 84,148,204
K 195,178,179
Mvav 52,148,196
X 181
Mvav 56,152,196
X 182
Mvav 88,152,204
Dc 245,8,61,1024
Ra 148,242,13312
Mvav 120,152,212
K 202,180,181
Mvav 152,152,220
X 183
Mvav 156,156,220
s_waitcnt vmcnt(16)
s_add_u32 s80, s71, 256
v_mov_b64 v[28:29], s[12:13]
s_mov_b64 exec, 1
buffer_store_dwordx2 v[28:29], off, s[8:11], s80 sc1
s_mov_b64 exec, -1
X 184
Mvav 124,156,212
Ra 152,242,14336
Mvav 92,156,204
K 203,182,183
Mvav 60,156,196
W 3
X 185
Cvaa 160,128,0,224
Dc 245,8,61,2048
Ra 156,242,15360
Cvaa 164,128,16,228
X 186
Cvaa 168,128,32,232
K 210,184,185
Cvaa 172,128,48,236
X 187
Mvaa 160,132,4
Dc 245,8,61,3072
Ra 128,242,24576
Mvaa 164,132,20
s_add_u32 s61, s61, s63
X 188
Mvaa 168,132,36
s_xor_b32 s62, s62, 0x10000
K 211,186,187
Mvaa 172,132,52
X 189
Mvaa 160,136,8
X 190
Mvaa 164,136,24
s_add_u32 s64, s64, 1
Ra 132,242,25600
Mvaa 168,136,40
K 218,188,189
Mvaa 172,136,56
X 191
Mvaa 160,140,12
Ra 136,242,26624
Mvaa 164,140,28
Mvaa 168,140,44
buffer_store_dwordx4 a[208:211], v1, s[8:11], s36 offen offset:2048 sc1
Mvaa 172,140,60
W 3
Cvaa 176,144,0,224
Ra 140,242,27648
Cvaa 180,144,16,228
X 160
Cvaa 184,144,32,232
K 219,190,191
Cvaa 188,144,48,236
X 161
Mvaa 176,148,4
X 162
Mvaa 180,148,20
Ra 144,242,28672
K 196,160,161
Mvaa 184,148,36
X 163
Mvaa 188,148,52
X 164
Mvaa 176,152,8
buffer_store_dwordx4 a[216:219], v1, s[8:11], s36 offen offset:3072 sc1
K 197,162,163
Mvaa 180,152,24
X 165
Mvaa 184,152,40
Ra 148,242,29696
Mvaa 188,152,56
X 166
Mvaa 176,156,12
s_add_u32 s36, s29, 200704
Ra 152,242,30720
K 204,164,165
Mvaa 180,156,28
X 167
Mvaa 184,156,44
X 168
Mvaa 188,156,60
W 3
K 205,166,167
Mvav 32,128,192
X 169
Mvav 64,128,200
Ra 156,242,31744
Mvav 96,128,208
X 170
Mvav 128,128,216
buffer_store_dwordx4 a[224:227], v1, s[8:11], s36 offen offset:0 sc1
K 212,168,169
Mvav 132,132,216
X 171
Mvav 100,132,208
X 172
Mvav 68,132,200
Mvav 36,132,192
K 213,170,171
Mvav 40,136,192
X 173
Mvav 72,136,200
buffer_store_dwordx4 a[232:235], v1, s[8:11], s36 offen offset:1024 sc1
Mvav 104,136,208
X 174
Mvav 136,136,216
K 220,172,173
Mvav 140,140,216
X 175
Mvav 108,140,208
X 176
Mvav 76,140,200
s_waitcnt vmcnt(4) lgkmcnt(0)
s_barrier
v_xor_b32 v242, 0x10000, v242
Mvav 44,140,192
buffer_store_dwordx4 a[240:243], v1, s[8:11], s36 offen offset:2048 sc1
K 221,174,175
Mvav 48,144,192
Ra 128,242,0
Mvav 80,144,200
X 177
Mvav 112,144,208
X 178
Mvav 144,144,216
Ra 132,242,1024
K 198,176,177
Mvav 148,148,216
X 179
Mvav 116,148,208
X 180
Mvav 84,148,200
Ra 136,242,2048
buffer_store_dwordx4 a[100:103], v1, s[8:11], s36 offen offset:3072 sc1
Mvav 52,148,192
K 199,178,179
Mvav 56,152,192
X 181
Mvav 88,152,200
Ra 140,242,3072
Mvav 120,152,208
X 182
Mvav 152,152,216
K 206,180,181
Mvav 156,156,216
X 183
Mvav 124,156,208
Mvav 92,156,200
K 207,182,183
Mvav 60,156,192
W 0
Cvaa 160,128,0,224
Ra 144,242,4096
Ra 148,242,5120
Cvaa 164,128,16,228
s_add_u32 m0, s62, 0
Ra 152,242,6144
Cvaa 168,128,32,232
X 184
Cvaa 172,128,48,236
X 185
Mvaa 160,132,4
X 186
Mvaa 164,132,20
Ra 156,242,7168
K 214,184,185
Mvaa 168,132,36
Dc 1,8,61,0
Mvaa 172,132,52
X 187
Mvaa 160,136,8
X 188
Mvaa 164,136,24
Ra 128,242,16384
K 215,186,187
Mvaa 168,136,40
Dc 1,8,61,1024
Mvaa 172,136,56
X 189
Mvaa 160,140,12
X 190
Mvaa 164,140,28
Ra 132,242,17408
K 222,188,189
Mvaa 168,140,44
Dc 1,8,61,2048
Mvaa 172,140,60
X 191
v_smfmac_f32_16x16x64_bf16 a[160:163], a[176:179], v[192:199], v240
Ra 136,242,18432
K 223,190,191
v_smfmac_f32_16x16x64_bf16 a[164:167], a[176:179], v[200:207], v240
X 160
v_smfmac_f32_16x16x64_bf16 a[168:171], a[176:179], v[208:215], v240
Dc 1,8,61,3072
v_smfmac_f32_16x16x64_bf16 a[172:175], a[176:179], v[216:223], v240
W 3
X 161
Cvaa 176,144,0,224
s_add_u32 m0, s62, 4096
X 162
Cvaa 180,144,16,228
Ra 140,242,19456
K 192,160,161
Cvaa 184,144,32,232
X 163
Cvaa 188,144,48,236
Dc 243,8,61,0
Mvaa 176,148,4
X 164
Mvaa 180,148,20
Ra 144,242,20480
K 193,162,163
Mvaa 184,148,36
X 165
Mvaa 188,148,52
Dc 243,8,61,1024
Mvaa 176,152,8
X 166
Mvaa 180,152,24
Ra 148,242,21504
K 200,164,165
Mvaa 184,152,40
X 167
Mvaa 188,152,56
Dc 243,8,61,2048
Mvaa 176,156,12
X 168
Mvaa 180,156,28
Ra 152,242,22528
K 201,166,167
Mvaa 184,156,44
X 169
Mvaa 188,156,60
W 3
Dc 243,8,61,3072
Mvav 32,128,196
s_add_u32 m0, s62, 8192
X 170
Mvav 64,128,204
Ra 156,242,23552
K 208,168,169
Mvav 96,128,212
X 171
Mvav 128,128,220
Dc 244,8,61,0
Mvav 132,132,220
X 172
Mvav 100,132,212
Ra 128,242,8192
K 209,170,171
Mvav 68,132,204
X 173
Mvav 36,132,196
Dc 244,8,61,1024
Mvav 40,136,196
X 174
Mvav 72,136,204
Ra 132,242,9216
K 216,172,173
Mvav 104,136,212
X 175
Mvav 136,136,220
Dc 244,8,61,2048
Mvav 140,140,220
X 176
Mvav 108,140,212
Ra 136,242,10240
K 217,174,175
Mvav 76,140,204
X 177
Mvav 44,140,196
W 3
Dc 244,8,61,3072
Mvav 48,144,196
X 178
Mvav 80,144,204
s_add_u32 m0, s62, 12288
Ra 140,242,11264
Mvav 112,144,212
K 194,176,177
Mvav 144,144,220
X 179
Mvav 148,148,220
X 180
Mvav 116,148,212
Dc 245,8,61,0
Ra 144,242,12288
Mvav 84,148,204
K 195,178,179
Mvav 52,148,196
X 181
Mvav 56,152,196
X 182
Mvav 88,152,204
Dc 245,8,61,1024
Ra 148,242,13312
Mvav 120,152,212
K 202,180,181
Mvav 152,152,220
X 183
Mvav 156,156,220
X 184
Mvav 124,156,212
Dc 245,8,61,2048
Ra 152,242,14336
Mvav 92,156,204
K 203,182,183
Mvav 60,156,196
W 3
X 185
Cvaa 160,128,0,224
X 186
Cvaa 164,128,16,228
Dc 245,8,61,3072
Ra 156,242,15360
Cvaa 168,128,32,232
s_add_u32 s61, s61, s63
K 210,184,185
Cvaa 172,128,48,236
X 187
Mvaa 160,132,4
X 188
Mvaa 164,132,20
Ra 128,242,24576
Mvaa 168,132,36
s_xor_b32 s62, s62, 0x10000
K 211,186,187
Mvaa 172,132,52
X 189
Mvaa 160,136,8
X 190
Mvaa 164,136,24
s_add_u32 s64, s64, 1
Ra 132,242,25600
Mvaa 168,136,40
K 218,188,189
Mvaa 172,136,56
X 191
Mvaa 160,140,12
Ra 136,242,26624
Mvaa 164,140,28
Mvaa 168,140,44
s_add_u32 s36, s29, 262144
Mvaa 172,140,60
W 3
Cvaa 176,144,0,224
Ra 140,242,27648
Cvaa 180,144,16,228
X 160
Cvaa 184,144,32,232
K 219,190,191
Cvaa 188,144,48,236
X 161
Mvaa 176,148,4
X 162
Mvaa 180,148,20
buffer_store_dwordx4 a[64:67], v1, s[8:11], s36 offen offset:0 sc1
K 196,160,161
Mvaa 184,148,36
)"
R"(X 163
Mvaa 188,148,52
Ra 144,242,28672
Mvaa 176,152,8
X 164
Mvaa 180,152,24
Ra 148,242,29696
K 197,162,163
Mvaa 184,152,40
X 165
Mvaa 188,152,56
X 166
Mvaa 176,156,12
buffer_store_dwordx4 a[72:75], v1, s[8:11], s36 offen offset:1024 sc1
K 204,164,165
Mvaa 180,156,28
X 167
Mvaa 184,156,44
Ra 152,242,30720
Mvaa 188,156,60
W 3
X 168
Mvav 32,128,192
buffer_store_dwordx4 a[80:83], v1, s[8:11], s36 offen offset:2048 sc1
K 205,166,167
Mvav 64,128,200
X 169
Mvav 96,128,208
Ra 156,242,31744
Mvav 128,128,216
X 170
Mvav 132,132,216
K 212,168,169
Mvav 100,132,208
X 171
Mvav 68,132,200
X 172
Mvav 36,132,192
buffer_store_dwordx4 a[88:91], v1, s[8:11], s36 offen offset:3072 sc1
K 213,170,171
Mvav 40,136,192
X 173
Mvav 72,136,200
X 174
Mvav 104,136,208
Mvav 136,136,216
s_add_u32 s36, s29, 266240
K 220,172,173
Mvav 140,140,216
X 175
Mvav 108,140,208
X 176
Mvav 76,140,200
s_waitcnt vmcnt(4) lgkmcnt(0)
s_barrier
v_xor_b32 v242, 0x10000, v242
Mvav 44,140,192
Ra 128,242,0
K 221,174,175
Mvav 48,144,192
X 177
Mvav 80,144,200
buffer_store_dwordx4 a[96:99], v1, s[8:11], s36 offen offset:0 sc1
Mvav 112,144,208
X 178
Mvav 144,144,216
Ra 132,242,1024
K 198,176,177
Mvav 148,148,216
X 179
Mvav 116,148,208
X 180
Mvav 84,148,200
Ra 136,242,2048
Mvav 52,148,192
buffer_store_dwordx4 a[104:107], v1, s[8:11], s36 offen offset:1024 sc1
K 199,178,179
Mvav 56,152,192
X 181
Mvav 88,152,200
Ra 140,242,3072
Mvav 120,152,208
X 182
Mvav 152,152,216
K 206,180,181
Mvav 156,156,216
X 183
Mvav 124,156,208
Mvav 92,156,200
K 207,182,183
Mvav 60,156,192
W 0
Cvaa 160,128,0,224
Ra 144,242,4096
Ra 148,242,5120
Cvaa 164,128,16,228
s_add_u32 m0, s62, 0
Ra 152,242,6144
Cvaa 168,128,32,232
X 184
Cvaa 172,128,48,236
X 185
Mvaa 160,132,4
X 186
Mvaa 164,132,20
Ra 156,242,7168
K 214,184,185
Mvaa 168,132,36
Dc 1,8,61,0
Mvaa 172,132,52
X 187
Mvaa 160,136,8
X 188
Mvaa 164,136,24
Ra 128,242,16384
K 215,186,187
Mvaa 168,136,40
Dc 1,8,61,1024
Mvaa 172,136,56
X 189
Mvaa 160,140,12
X 190
Mvaa 164,140,28
Ra 132,242,17408
K 222,188,189
Mvaa 168,140,44
Dc 1,8,61,2048
Mvaa 172,140,60
X 191
v_smfmac_f32_16x16x64_bf16 a[160:163], a[176:179], v[192:199], v240
Ra 136,242,18432
K 223,190,191
v_smfmac_f32_16x16x64_bf16 a[164:167], a[176:179], v[200:207], v240
X 160
v_smfmac_f32_16x16x64_bf16 a[168:171], a[176:179], v[208:215], v240
Dc 1,8,61,3072
v_smfmac_f32_16x16x64_bf16 a[172:175], a[176:179], v[216:223], v240
W 3
X 161
Cvaa 176,144,0,224
s_add_u32 m0, s62, 4096
X 162
Cvaa 180,144,16,228
Ra 140,242,19456
K 192,160,161
Cvaa 184,144,32,232
X 163
Cvaa 188,144,48,236
Dc 243,8,61,0
Mvaa 176,148,4
X 164
Mvaa 180,148,20
Ra 144,242,20480
K 193,162,163
Mvaa 184,148,36
X 165
Mvaa 188,148,52
Dc 243,8,61,1024
Mvaa 176,152,8
X 166
Mvaa 180,152,24
Ra 148,242,21504
K 200,164,165
Mvaa 184,152,40
X 167
Mvaa 188,152,56
Dc 243,8,61,2048
Mvaa 176,156,12
X 168
Mvaa 180,156,28
Ra 152,242,22528
K 201,166,167
Mvaa 184,156,44
X 169
Mvaa 188,156,60
W 3
Dc 243,8,61,3072
Mvav 32,128,196
s_add_u32 m0, s62, 8192
X 170
Mvav 64,128,204
Ra 156,242,23552
K 208,168,169
Mvav 96,128,212
X 171
Mvav 128,128,220
Dc 244,8,61,0
Mvav 132,132,220
X 172
Mvav 100,132,212
Ra 128,242,8192
K 209,170,171
Mvav 68,132,204
X 173
Mvav 36,132,196
Dc 244,8,61,1024
Mvav 40,136,196
X 174
Mvav 72,136,204
Ra 132,242,9216
K 216,172,173
Mvav 104,136,212
X 175
Mvav 136,136,220
Dc 244,8,61,2048
Mvav 140,140,220
X 176
Mvav 108,140,212
Ra 136,242,10240
K 217,174,175
Mvav 76,140,204
X 177
Mvav 44,140,196
W 3
Dc 244,8,61,3072
Mvav 48,144,196
X 178
Mvav 80,144,204
s_add_u32 m0, s62, 12288
Ra 140,242,11264
Mvav 112,144,212
K 194,176,177
Mvav 144,144,220
X 179
Mvav 148,148,220
X 180
Mvav 116,148,212
Dc 245,8,61,0
Ra 144,242,12288
Mvav 84,148,204
K 195,178,179
Mvav 52,148,196
X 181
Mvav 56,152,196
X 182
Mvav 88,152,204
Dc 245,8,61,1024
Ra 148,242,13312
Mvav 120,152,212
K 202,180,181
Mvav 152,152,220
X 183
Mvav 156,156,220
X 184
Mvav 124,156,212
Dc 245,8,61,2048
Ra 152,242,14336
Mvav 92,156,204
K 203,182,183
v_bfe_u32 v28, v1, 4, 4
Mvav 60,156,196
W 3
X 185
Cvaa 160,128,0,224
Ra 156,242,15360
v_lshlrev_b32 v28, 3, v28
Cvaa 164,128,16,228
s_lshl_b32 s80, s56, 9
s_add_u32 s80, s80, 4352
Dc 245,8,61,3072
Cvaa 168,128,32,232
s_add_u32 s61, s61, s63
X 186
Cvaa 172,128,48,236
K 210,184,185
Mvaa 160,132,4
X 187
Mvaa 164,132,20
buffer_load_dwordx2 v[28:29], v28, s[8:11], s80 offen sc1
Ra 128,242,24576
Mvaa 168,132,36
s_xor_b32 s62, s62, 0x10000
X 188
Mvaa 172,132,52
K 211,186,187
Mvaa 160,136,8
X 189
Mvaa 164,136,24
s_add_u32 s64, s64, 1
X 190
Mvaa 168,136,40
Ra 132,242,25600
K 218,188,189
Mvaa 172,136,56
X 191
Mvaa 160,140,12
Ra 136,242,26624
Mvaa 164,140,28
Mvaa 168,140,44
buffer_store_dwordx4 a[112:115], v1, s[8:11], s36 offen offset:2048 sc1
Mvaa 172,140,60
W 3
Cvaa 176,144,0,224
Ra 140,242,27648
Cvaa 180,144,16,228
X 160
Cvaa 184,144,32,232
K 219,190,191
Cvaa 188,144,48,236
X 161
Mvaa 176,148,4
X 162
Mvaa 180,148,20
Ra 144,242,28672
K 196,160,161
Mvaa 184,148,36
X 163
Mvaa 188,148,52
X 164
Mvaa 176,152,8
Ra 148,242,29696
K 197,162,163
Mvaa 180,152,24
X 165
Mvaa 184,152,40
X 166
Mvaa 188,152,56
K 204,164,165
Mvaa 176,156,12
X 167
Mvaa 180,156,28
Ra 152,242,30720
Mvaa 184,156,44
X 168
Mvaa 188,156,60
W 3
K 205,166,167
Mvav 32,128,192
X 169
Mvav 64,128,200
Ra 156,242,31744
Mvav 96,128,208
X 170
Mvav 128,128,216
K 212,168,169
Mvav 132,132,216
X 171
Mvav 100,132,208
X 172
Mvav 68,132,200
buffer_store_dwordx4 a[120:123], v1, s[8:11], s36 offen offset:3072 sc1
Mvav 36,132,192
K 213,170,171
Mvav 40,136,192
X 173
Mvav 72,136,200
X 174
Mvav 104,136,208
Mvav 136,136,216
s_waitcnt vmcnt(2)
v_cmp_eq_u64 vcc, s[12:13], v[28:29]
s_cmp_eq_u64 vcc, -1
s_cbranch_scc0 .Lspin_65%=
.Lok_66%=:
K 220,172,173
Mvav 140,140,216
X 175
Mvav 108,140,208
X 176
Mvav 76,140,200
W 0
s_barrier
v_xor_b32 v242, 0x10000, v242
Mvav 44,140,192
Ra 128,242,0
K 221,174,175
Mvav 48,144,192
X 177
Mvav 80,144,200
X 178
Mvav 112,144,208
Ra 132,242,1024
Mvav 144,144,216
K 198,176,177
Mvav 148,148,216
X 179
Mvav 116,148,208
X 180
Mvav 84,148,200
Ra 136,242,2048
Mvav 52,148,192
K 199,178,179
Mvav 56,152,192
X 181
Mvav 88,152,200
Ra 140,242,3072
Mvav 120,152,208
X 182
Mvav 152,152,216
K 206,180,181
Mvav 156,156,216
X 183
Mvav 124,156,208
Mvav 92,156,200
K 207,182,183
Mvav 60,156,192
W 0
Cvaa 160,128,0,224
Ra 144,242,4096
Ra 148,242,5120
)"
R"(Cvaa 164,128,16,228
s_add_u32 m0, s62, 0
Ra 152,242,6144
Cvaa 168,128,32,232
X 184
Cvaa 172,128,48,236
X 185
Mvaa 160,132,4
X 186
Mvaa 164,132,20
Ra 156,242,7168
K 214,184,185
Mvaa 168,132,36
Dc 1,8,61,0
Mvaa 172,132,52
X 187
Mvaa 160,136,8
X 188
Mvaa 164,136,24
Ra 128,242,16384
K 215,186,187
Mvaa 168,136,40
Dc 1,8,61,1024
Mvaa 172,136,56
X 189
Mvaa 160,140,12
X 190
Mvaa 164,140,28
Ra 132,242,17408
K 222,188,189
Mvaa 168,140,44
Dc 1,8,61,2048
Mvaa 172,140,60
X 191
v_smfmac_f32_16x16x64_bf16 a[160:163], a[176:179], v[192:199], v240
Ra 136,242,18432
K 223,190,191
v_smfmac_f32_16x16x64_bf16 a[164:167], a[176:179], v[200:207], v240
X 160
v_smfmac_f32_16x16x64_bf16 a[168:171], a[176:179], v[208:215], v240
Dc 1,8,61,3072
v_smfmac_f32_16x16x64_bf16 a[172:175], a[176:179], v[216:223], v240
W 3
X 161
Cvaa 176,144,0,224
s_add_u32 m0, s62, 4096
X 162
Cvaa 180,144,16,228
Ra 140,242,19456
K 192,160,161
Cvaa 184,144,32,232
X 163
Cvaa 188,144,48,236
Dc 243,8,61,0
Mvaa 176,148,4
X 164
Mvaa 180,148,20
Ra 144,242,20480
K 193,162,163
Mvaa 184,148,36
X 165
Mvaa 188,148,52
Dc 243,8,61,1024
Mvaa 176,152,8
X 166
Mvaa 180,152,24
Ra 148,242,21504
K 200,164,165
Mvaa 184,152,40
X 167
Mvaa 188,152,56
Dc 243,8,61,2048
Mvaa 176,156,12
X 168
Mvaa 180,156,28
Ra 152,242,22528
K 201,166,167
Mvaa 184,156,44
X 169
Mvaa 188,156,60
W 3
Dc 243,8,61,3072
Mvav 32,128,196
s_add_u32 m0, s62, 8192
X 170
Mvav 64,128,204
Ra 156,242,23552
K 208,168,169
Mvav 96,128,212
X 171
Mvav 128,128,220
Dc 244,8,61,0
Mvav 132,132,220
X 172
Mvav 100,132,212
Ra 128,242,8192
K 209,170,171
Mvav 68,132,204
X 173
Mvav 36,132,196
Dc 244,8,61,1024
Mvav 40,136,196
X 174
Mvav 72,136,204
Ra 132,242,9216
K 216,172,173
Mvav 104,136,212
X 175
Mvav 136,136,220
Dc 244,8,61,2048
Mvav 140,140,220
X 176
Mvav 108,140,212
Ra 136,242,10240
K 217,174,175
Mvav 76,140,204
X 177
Mvav 44,140,196
W 3
Dc 244,8,61,3072
Mvav 48,144,196
X 178
Mvav 80,144,204
s_add_u32 m0, s62, 12288
Ra 140,242,11264
Mvav 112,144,212
K 194,176,177
Mvav 144,144,220
X 179
Mvav 148,148,220
X 180
Mvav 116,148,212
Dc 245,8,61,0
Ra 144,242,12288
Mvav 84,148,204
K 195,178,179
Mvav 52,148,196
X 181
Mvav 56,152,196
X 182
Mvav 88,152,204
Dc 245,8,61,1024
Ra 148,242,13312
Mvav 120,152,212
K 202,180,181
Mvav 152,152,220
s_add_u32 s81, s57, 2
X 183
Mvav 156,156,220
s_waitcnt vmcnt(14)
s_add_u32 s80, s71, 384
v_mov_b64 v[28:29], s[12:13]
s_mov_b64 exec, 1
buffer_store_dwordx2 v[28:29], off, s[8:11], s80 sc1
s_mov_b64 exec, -1
X 184
Mvav 124,156,212
s_bitcmp0_b32 s81, 2
s_cselect_b32 s22, -1, 0
s_and_b32 s81, s81, 3
Ra 152,242,14336
Mvav 92,156,204
s_lshl_b32 s82, s56, 2
K 203,182,183
Mvav 60,156,196
W 3
X 185
Cvaa 160,128,0,224
s_add_u32 s81, s81, s82
Dc 245,8,61,2048
Ra 156,242,15360
Cvaa 164,128,16,228
s_lshl_b32 s81, s81, 1
X 186
Cvaa 168,128,32,232
s_add_u32 s81, s81, s14
s_sub_u32 s81, s81, 2
K 210,184,185
Cvaa 172,128,48,236
s_mul_i32 s81, s81, 20480
X 187
Mvaa 160,132,4
Dc 245,8,61,3072
Ra 128,242,24576
Mvaa 164,132,20
s_add_u32 s61, s61, s63
X 188
Mvaa 168,132,36
s_xor_b32 s62, s62, 0x10000
v_min_u32 v2, 624, v1
v_lshl_add_u32 v2, v2, 3, s81
Mvaa 172,132,52
K 211,186,187
v_and_b32 v2, s22, v2
Mvaa 160,136,8
s_and_b32 s22, s22, 5120
X 189
Mvaa 164,136,24
s_add_u32 s64, s64, 1
global_load_dword v3, v2, s[26:27]
Ra 132,242,25600
Mvaa 168,136,40
X 190
Mvaa 172,136,56
v_add_u32 v2, s22, v2
K 218,188,189
Mvaa 160,140,12
X 191
Mvaa 164,140,28
Ra 136,242,26624
Mvaa 168,140,44
Mvaa 172,140,60
W 3
Cvaa 176,144,0,224
Ra 140,242,27648
Cvaa 180,144,16,228
X 160
Cvaa 184,144,32,232
K 219,190,191
Cvaa 188,144,48,236
X 161
Mvaa 176,148,4
X 162
Mvaa 180,148,20
Ra 144,242,28672
K 196,160,161
Mvaa 184,148,36
X 163
Mvaa 188,148,52
X 164
Mvaa 176,152,8
Ra 148,242,29696
K 197,162,163
Mvaa 180,152,24
X 165
Mvaa 184,152,40
X 166
Mvaa 188,152,56
K 204,164,165
Mvaa 176,156,12
X 167
Mvaa 180,156,28
Ra 152,242,30720
Mvaa 184,156,44
X 168
Mvaa 188,156,60
W 3
K 205,166,167
Mvav 32,128,192
X 169
Mvav 64,128,200
Ra 156,242,31744
Mvav 96,128,208
X 170
Mvav 128,128,216
K 212,168,169
Mvav 132,132,216
X 171
Mvav 100,132,208
X 172
Mvav 68,132,200
Mvav 36,132,192
K 213,170,171
Mvav 40,136,192
X 173
Mvav 72,136,200
X 174
Mvav 104,136,208
Mvav 136,136,216
K 220,172,173
Mvav 140,140,216
X 175
Mvav 108,140,208
X 176
Mvav 76,140,200
s_waitcnt vmcnt(1) lgkmcnt(0)
s_barrier
v_xor_b32 v242, 0x10000, v242
Mvav 44,140,192
Ra 128,242,0
K 221,174,175
Mvav 48,144,192
X 177
Mvav 80,144,200
X 178
Mvav 112,144,208
Ra 132,242,1024
Mvav 144,144,216
K 198,176,177
Mvav 148,148,216
X 179
Mvav 116,148,208
X 180
Mvav 84,148,200
Ra 136,242,2048
Mvav 52,148,192
K 199,178,179
Mvav 56,152,192
X 181
Mvav 88,152,200
Ra 140,242,3072
Mvav 120,152,208
X 182
Mvav 152,152,216
K 206,180,181
Mvav 156,156,216
X 183
Mvav 124,156,208
Mvav 92,156,200
K 207,182,183
Mvav 60,156,192
W 0
Cvaa 160,128,0,224
Ra 144,242,4096
Ra 148,242,5120
Cvaa 164,128,16,228
s_add_u32 m0, s62, 0
Ra 152,242,6144
Cvaa 168,128,32,232
X 184
Cvaa 172,128,48,236
X 185
Mvaa 160,132,4
X 186
Mvaa 164,132,20
Ra 156,242,7168
K 214,184,185
Mvaa 168,132,36
Dc 1,8,61,0
Mvaa 172,132,52
X 187
Mvaa 160,136,8
X 188
Mvaa 164,136,24
Ra 128,242,16384
K 215,186,187
Mvaa 168,136,40
Dc 1,8,61,1024
Mvaa 172,136,56
X 189
Mvaa 160,140,12
X 190
Mvaa 164,140,28
Ra 132,242,17408
K 222,188,189
Mvaa 168,140,44
Dc 1,8,61,2048
Mvaa 172,140,60
X 191
v_smfmac_f32_16x16x64_bf16 a[160:163], a[176:179], v[192:199], v240
Ra 136,242,18432
K 223,190,191
v_smfmac_f32_16x16x64_bf16 a[164:167], a[176:179], v[200:207], v240
X 160
v_smfmac_f32_16x16x64_bf16 a[168:171], a[176:179], v[208:215], v240
Dc 1,8,61,3072
v_smfmac_f32_16x16x64_bf16 a[172:175], a[176:179], v[216:223], v240
W 3
X 161
Cvaa 176,144,0,224
s_add_u32 m0, s62, 4096
X 162
Cvaa 180,144,16,228
Ra 140,242,19456
K 192,160,161
Cvaa 184,144,32,232
X 163
Cvaa 188,144,48,236
Dc 243,8,61,0
Mvaa 176,148,4
X 164
Mvaa 180,148,20
Ra 144,242,20480
K 193,162,163
Mvaa 184,148,36
X 165
Mvaa 188,148,52
Dc 243,8,61,1024
Mvaa 176,152,8
X 166
Mvaa 180,152,24
Ra 148,242,21504
K 200,164,165
Mvaa 184,152,40
X 167
Mvaa 188,152,56
Dc 243,8,61,2048
Mvaa 176,156,12
X 168
Mvaa 180,156,28
Ra 152,242,22528
K 201,166,167
Mvaa 184,156,44
X 169
Mvaa 188,156,60
W 3
Dc 243,8,61,3072
Mvav 32,128,196
s_add_u32 m0, s62, 8192
X 170
)"
R"(Mvav 64,128,204
Ra 156,242,23552
K 208,168,169
Mvav 96,128,212
X 171
Mvav 128,128,220
Dc 244,8,61,0
Mvav 132,132,220
X 172
Mvav 100,132,212
Ra 128,242,8192
K 209,170,171
Mvav 68,132,204
X 173
Mvav 36,132,196
Dc 244,8,61,1024
Mvav 40,136,196
X 174
Mvav 72,136,204
Ra 132,242,9216
K 216,172,173
Mvav 104,136,212
X 175
Mvav 136,136,220
Dc 244,8,61,2048
Mvav 140,140,220
X 176
Mvav 108,140,212
Ra 136,242,10240
K 217,174,175
Mvav 76,140,204
X 177
Mvav 44,140,196
W 3
Dc 244,8,61,3072
Mvav 48,144,196
X 178
Mvav 80,144,204
s_add_u32 m0, s62, 12288
Ra 140,242,11264
Mvav 112,144,212
K 194,176,177
Mvav 144,144,220
X 179
Mvav 148,148,220
X 180
Mvav 116,148,212
Dc 245,8,61,0
Ra 144,242,12288
Mvav 84,148,204
K 195,178,179
Mvav 52,148,196
X 181
Mvav 56,152,196
X 182
Mvav 88,152,204
Dc 245,8,61,1024
Ra 148,242,13312
Mvav 120,152,212
K 202,180,181
Mvav 152,152,220
X 183
Mvav 156,156,220
X 184
Mvav 124,156,212
Dc 245,8,61,2048
Ra 152,242,14336
Mvav 92,156,204
K 203,182,183
Mvav 60,156,196
W 3
X 185
Cvaa 160,128,0,224
X 186
Cvaa 164,128,16,228
Dc 245,8,61,3072
Ra 156,242,15360
Cvaa 168,128,32,232
s_add_u32 s61, s61, s63
K 210,184,185
Cvaa 172,128,48,236
X 187
Mvaa 160,132,4
X 188
Mvaa 164,132,20
Ra 128,242,24576
Mvaa 168,132,36
s_xor_b32 s62, s62, 0x10000
global_load_dword v3, v2, s[26:27]
K 211,186,187
Mvaa 172,132,52
X 189
Mvaa 160,136,8
X 190
Mvaa 164,136,24
s_add_u32 s64, s64, 1
Ra 132,242,25600
v_add_u32 v2, s22, v2
Mvaa 168,136,40
K 218,188,189
Mvaa 172,136,56
X 191
Mvaa 160,140,12
Ra 136,242,26624
Mvaa 164,140,28
Mvaa 168,140,44
Mvaa 172,140,60
W 3
Cvaa 176,144,0,224
Ra 140,242,27648
Cvaa 180,144,16,228
X 160
Cvaa 184,144,32,232
K 219,190,191
Cvaa 188,144,48,236
X 161
Mvaa 176,148,4
X 162
Mvaa 180,148,20
Ra 144,242,28672
K 196,160,161
Mvaa 184,148,36
X 163
Mvaa 188,148,52
X 164
Mvaa 176,152,8
Ra 148,242,29696
K 197,162,163
Mvaa 180,152,24
X 165
Mvaa 184,152,40
X 166
Mvaa 188,152,56
K 204,164,165
Mvaa 176,156,12
X 167
Mvaa 180,156,28
Ra 152,242,30720
Mvaa 184,156,44
X 168
Mvaa 188,156,60
W 3
K 205,166,167
Mvav 32,128,192
X 169
Mvav 64,128,200
Ra 156,242,31744
Mvav 96,128,208
X 170
Mvav 128,128,216
K 212,168,169
Mvav 132,132,216
X 171
Mvav 100,132,208
X 172
Mvav 68,132,200
Mvav 36,132,192
K 213,170,171
Mvav 40,136,192
X 173
Mvav 72,136,200
X 174
Mvav 104,136,208
Mvav 136,136,216
K 220,172,173
Mvav 140,140,216
X 175
Mvav 108,140,208
X 176
Mvav 76,140,200
s_waitcnt vmcnt(1) lgkmcnt(0)
s_barrier
v_xor_b32 v242, 0x10000, v242
Mvav 44,140,192
Ra 128,242,0
K 221,174,175
Mvav 48,144,192
X 177
Mvav 80,144,200
X 178
Mvav 112,144,208
Ra 132,242,1024
Mvav 144,144,216
K 198,176,177
Mvav 148,148,216
X 179
Mvav 116,148,208
X 180
Mvav 84,148,200
Ra 136,242,2048
Mvav 52,148,192
K 199,178,179
Mvav 56,152,192
X 181
Mvav 88,152,200
Ra 140,242,3072
Mvav 120,152,208
X 182
Mvav 152,152,216
K 206,180,181
Mvav 156,156,216
X 183
Mvav 124,156,208
Mvav 92,156,200
K 207,182,183
Mvav 60,156,192
W 0
Cvaa 160,128,0,224
Ra 144,242,4096
Ra 148,242,5120
Cvaa 164,128,16,228
s_add_u32 m0, s62, 0
Ra 152,242,6144
Cvaa 168,128,32,232
X 184
Cvaa 172,128,48,236
X 185
Mvaa 160,132,4
X 186
Mvaa 164,132,20
Ra 156,242,7168
K 214,184,185
Mvaa 168,132,36
Dc 1,8,61,0
Mvaa 172,132,52
X 187
Mvaa 160,136,8
X 188
Mvaa 164,136,24
Ra 128,242,16384
K 215,186,187
Mvaa 168,136,40
Dc 1,8,61,1024
Mvaa 172,136,56
X 189
Mvaa 160,140,12
X 190
Mvaa 164,140,28
Ra 132,242,17408
K 222,188,189
Mvaa 168,140,44
Dc 1,8,61,2048
Mvaa 172,140,60
X 191
v_smfmac_f32_16x16x64_bf16 a[160:163], a[176:179], v[192:199], v240
Ra 136,242,18432
K 223,190,191
v_smfmac_f32_16x16x64_bf16 a[164:167], a[176:179], v[200:207], v240
X 160
v_smfmac_f32_16x16x64_bf16 a[168:171], a[176:179], v[208:215], v240
Dc 1,8,61,3072
v_smfmac_f32_16x16x64_bf16 a[172:175], a[176:179], v[216:223], v240
W 3
X 161
Cvaa 176,144,0,224
s_add_u32 m0, s62, 4096
X 162
Cvaa 180,144,16,228
Ra 140,242,19456
K 192,160,161
Cvaa 184,144,32,232
X 163
Cvaa 188,144,48,236
Dc 243,8,61,0
Mvaa 176,148,4
X 164
Mvaa 180,148,20
Ra 144,242,20480
K 193,162,163
Mvaa 184,148,36
X 165
Mvaa 188,148,52
Dc 243,8,61,1024
Mvaa 176,152,8
X 166
Mvaa 180,152,24
Ra 148,242,21504
K 200,164,165
Mvaa 184,152,40
X 167
Mvaa 188,152,56
Dc 243,8,61,2048
Mvaa 176,156,12
X 168
Mvaa 180,156,28
Ra 152,242,22528
K 201,166,167
Mvaa 184,156,44
X 169
Mvaa 188,156,60
W 3
Dc 243,8,61,3072
Mvav 32,128,196
s_add_u32 m0, s62, 8192
X 170
Mvav 64,128,204
Ra 156,242,23552
K 208,168,169
Mvav 96,128,212
X 171
Mvav 128,128,220
Dc 244,8,61,0
Mvav 132,132,220
X 172
Mvav 100,132,212
Ra 128,242,8192
K 209,170,171
Mvav 68,132,204
X 173
Mvav 36,132,196
Dc 244,8,61,1024
Mvav 40,136,196
X 174
Mvav 72,136,204
Ra 132,242,9216
K 216,172,173
Mvav 104,136,212
X 175
Mvav 136,136,220
Dc 244,8,61,2048
Mvav 140,140,220
X 176
Mvav 108,140,212
Ra 136,242,10240
K 217,174,175
Mvav 76,140,204
X 177
Mvav 44,140,196
W 3
Dc 244,8,61,3072
Mvav 48,144,196
X 178
Mvav 80,144,204
s_add_u32 m0, s62, 12288
Ra 140,242,11264
Mvav 112,144,212
K 194,176,177
Mvav 144,144,220
X 179
Mvav 148,148,220
X 180
Mvav 116,148,212
Dc 245,8,61,0
Ra 144,242,12288
Mvav 84,148,204
K 195,178,179
Mvav 52,148,196
X 181
Mvav 56,152,196
X 182
Mvav 88,152,204
Dc 245,8,61,1024
Ra 148,242,13312
Mvav 120,152,212
K 202,180,181
Mvav 152,152,220
X 183
Mvav 156,156,220
X 184
Mvav 124,156,212
Dc 245,8,61,2048
Ra 152,242,14336
Mvav 92,156,204
K 203,182,183
Mvav 60,156,196
W 3
X 185
Cvaa 160,128,0,224
X 186
Cvaa 164,128,16,228
Dc 245,8,61,3072
Ra 156,242,15360
Cvaa 168,128,32,232
s_add_u32 s61, s61, s63
K 210,184,185
Cvaa 172,128,48,236
X 187
Mvaa 160,132,4
X 188
Mvaa 164,132,20
Ra 128,242,24576
Mvaa 168,132,36
s_xor_b32 s62, s62, 0x10000
global_load_dword v3, v2, s[26:27]
K 211,186,187
Mvaa 172,132,52
v_add_u32 v2, s22, v2
Mvaa 160,136,8
s_lshr_b32 s80, s15, 3
s_lshl_b32 s80, s80, 1
s_add_u32 s80, s80, s14
Mvaa 164,136,24
s_sub_u32 s80, s80, 2
s_lshl_b32 s80, s80, 12
v_min_u32 v247, 496, v1
Mvaa 168,136,40
s_add_u32 s64, s64, 1
X 189
Mvaa 172,136,56
Ra 132,242,25600
)"
R"(v_lshl_add_u32 v247, v247, 3, s80
Mvaa 160,140,12
X 190
Mvaa 164,140,28
v_min_u32 v247, .Lend%=-.Lbeg%=-4, v247
K 218,188,189
Mvaa 168,140,44
global_load_dword v3, v247, s[6:7]
Ra 136,242,26624
Mvaa 172,140,60
W 3
X 191
Cvaa 176,144,0,224
X 160
Cvaa 180,144,16,228
Ra 140,242,27648
K 219,190,191
Cvaa 184,144,32,232
X 161
Cvaa 188,144,48,236
X 162
Mvaa 176,148,4
Ra 144,242,28672
K 196,160,161
Mvaa 180,148,20
X 163
Mvaa 184,148,36
X 164
Mvaa 188,148,52
K 197,162,163
Mvaa 176,152,8
X 165
Mvaa 180,152,24
Ra 148,242,29696
Mvaa 184,152,40
X 166
Mvaa 188,152,56
K 204,164,165
Mvaa 176,156,12
X 167
Mvaa 180,156,28
Ra 152,242,30720
Mvaa 184,156,44
X 168
Mvaa 188,156,60
W 3
K 205,166,167
Mvav 32,128,192
X 169
Mvav 64,128,200
Ra 156,242,31744
Mvav 96,128,208
X 170
Mvav 128,128,216
K 212,168,169
Mvav 132,132,216
X 171
Mvav 100,132,208
X 172
Mvav 68,132,200
Mvav 36,132,192
K 213,170,171
Mvav 40,136,192
X 173
Mvav 72,136,200
X 174
Mvav 104,136,208
Mvav 136,136,216
K 220,172,173
Mvav 140,140,216
X 175
Mvav 108,140,208
X 176
Mvav 76,140,200
s_waitcnt vmcnt(2) lgkmcnt(0)
s_barrier
v_xor_b32 v242, 0x10000, v242
Mvav 44,140,192
Ra 128,242,0
K 221,174,175
Mvav 48,144,192
X 177
Mvav 80,144,200
X 178
Mvav 112,144,208
Ra 132,242,1024
Mvav 144,144,216
K 198,176,177
Mvav 148,148,216
X 179
Mvav 116,148,208
X 180
Mvav 84,148,200
Ra 136,242,2048
Mvav 52,148,192
K 199,178,179
Mvav 56,152,192
X 181
Mvav 88,152,200
Ra 140,242,3072
Mvav 120,152,208
X 182
Mvav 152,152,216
K 206,180,181
Mvav 156,156,216
X 183
Mvav 124,156,208
Mvav 92,156,200
K 207,182,183
Mvav 60,156,192
W 0
Cvaa 160,128,0,224
Ra 144,242,4096
Ra 148,242,5120
Cvaa 164,128,16,228
s_add_u32 m0, s62, 0
Ra 152,242,6144
Cvaa 168,128,32,232
X 184
Cvaa 172,128,48,236
X 185
Mvaa 160,132,4
X 186
Mvaa 164,132,20
Ra 156,242,7168
K 214,184,185
Mvaa 168,132,36
Dc 1,8,61,0
Mvaa 172,132,52
X 187
Mvaa 160,136,8
X 188
Mvaa 164,136,24
Ra 128,242,16384
K 215,186,187
Mvaa 168,136,40
Dc 1,8,61,1024
Mvaa 172,136,56
X 189
Mvaa 160,140,12
X 190
Mvaa 164,140,28
Ra 132,242,17408
K 222,188,189
Mvaa 168,140,44
Dc 1,8,61,2048
Mvaa 172,140,60
X 191
v_smfmac_f32_16x16x64_bf16 a[160:163], a[176:179], v[192:199], v240
Ra 136,242,18432
K 223,190,191
v_smfmac_f32_16x16x64_bf16 a[164:167], a[176:179], v[200:207], v240
X 160
v_smfmac_f32_16x16x64_bf16 a[168:171], a[176:179], v[208:215], v240
Dc 1,8,61,3072
v_smfmac_f32_16x16x64_bf16 a[172:175], a[176:179], v[216:223], v240
W 3
X 161
Cvaa 176,144,0,224
s_add_u32 m0, s62, 4096
X 162
Cvaa 180,144,16,228
Ra 140,242,19456
K 192,160,161
Cvaa 184,144,32,232
X 163
Cvaa 188,144,48,236
Dc 243,8,61,0
Mvaa 176,148,4
X 164
Mvaa 180,148,20
Ra 144,242,20480
K 193,162,163
Mvaa 184,148,36
X 165
Mvaa 188,148,52
Dc 243,8,61,1024
Mvaa 176,152,8
X 166
Mvaa 180,152,24
Ra 148,242,21504
K 200,164,165
Mvaa 184,152,40
X 167
Mvaa 188,152,56
Dc 243,8,61,2048
Mvaa 176,156,12
X 168
Mvaa 180,156,28
Ra 152,242,22528
K 201,166,167
Mvaa 184,156,44
X 169
Mvaa 188,156,60
W 3
Dc 243,8,61,3072
Mvav 32,128,196
s_add_u32 m0, s62, 8192
X 170
Mvav 64,128,204
Ra 156,242,23552
K 208,168,169
Mvav 96,128,212
X 171
Mvav 128,128,220
Dc 244,8,61,0
Mvav 132,132,220
X 172
Mvav 100,132,212
Ra 128,242,8192
K 209,170,171
Mvav 68,132,204
X 173
Mvav 36,132,196
Dc 244,8,61,1024
Mvav 40,136,196
X 174
Mvav 72,136,204
Ra 132,242,9216
K 216,172,173
Mvav 104,136,212
X 175
Mvav 136,136,220
Dc 244,8,61,2048
Mvav 140,140,220
X 176
Mvav 108,140,212
Ra 136,242,10240
K 217,174,175
Mvav 76,140,204
X 177
Mvav 44,140,196
W 3
Dc 244,8,61,3072
Mvav 48,144,196
X 178
Mvav 80,144,204
s_add_u32 m0, s62, 12288
Ra 140,242,11264
Mvav 112,144,212
K 194,176,177
Mvav 144,144,220
X 179
Mvav 148,148,220
X 180
Mvav 116,148,212
Dc 245,8,61,0
Ra 144,242,12288
Mvav 84,148,204
K 195,178,179
Mvav 52,148,196
X 181
Mvav 56,152,196
X 182
Mvav 88,152,204
Dc 245,8,61,1024
Ra 148,242,13312
Mvav 120,152,212
K 202,180,181
Mvav 152,152,220
X 183
Mvav 156,156,220
X 184
Mvav 124,156,212
Dc 245,8,61,2048
Ra 152,242,14336
Mvav 92,156,204
K 203,182,183
v_bfe_u32 v28, v1, 4, 4
Mvav 60,156,196
W 3
X 185
Cvaa 160,128,0,224
Ra 156,242,15360
v_lshlrev_b32 v28, 3, v28
Cvaa 164,128,16,228
s_lshl_b32 s80, s56, 9
s_add_u32 s80, s80, 4480
Dc 245,8,61,3072
Cvaa 168,128,32,232
s_add_u32 s61, s61, s63
X 186
Cvaa 172,128,48,236
K 210,184,185
Mvaa 160,132,4
X 187
Mvaa 164,132,20
buffer_load_dwordx2 v[28:29], v28, s[8:11], s80 offen sc1
Ra 128,242,24576
Mvaa 168,132,36
s_xor_b32 s62, s62, 0x10000
X 188
Mvaa 172,132,52
K 211,186,187
Mvaa 160,136,8
X 189
Mvaa 164,136,24
s_add_u32 s64, s64, 1
global_load_dword v3, v2, s[26:27]
Ra 132,242,25600
Mvaa 168,136,40
X 190
Mvaa 172,136,56
v_add_u32 v2, s22, v2
K 218,188,189
Mvaa 160,140,12
X 191
Mvaa 164,140,28
Ra 136,242,26624
Mvaa 168,140,44
Mvaa 172,140,60
W 3
Cvaa 176,144,0,224
Ra 140,242,27648
Cvaa 180,144,16,228
X 160
Cvaa 184,144,32,232
K 219,190,191
Cvaa 188,144,48,236
X 161
Mvaa 176,148,4
X 162
Mvaa 180,148,20
Ra 144,242,28672
K 196,160,161
Mvaa 184,148,36
X 163
Mvaa 188,148,52
X 164
Mvaa 176,152,8
Ra 148,242,29696
K 197,162,163
Mvaa 180,152,24
X 165
Mvaa 184,152,40
X 166
Mvaa 188,152,56
K 204,164,165
Mvaa 176,156,12
X 167
Mvaa 180,156,28
Ra 152,242,30720
Mvaa 184,156,44
X 168
Mvaa 188,156,60
W 3
K 205,166,167
Mvav 32,128,192
X 169
Mvav 64,128,200
Ra 156,242,31744
Mvav 96,128,208
X 170
Mvav 128,128,216
K 212,168,169
Mvav 132,132,216
X 171
Mvav 100,132,208
X 172
Mvav 68,132,200
Mvav 36,132,192
K 213,170,171
Mvav 40,136,192
X 173
Mvav 72,136,200
X 174
Mvav 104,136,208
Mvav 136,136,216
s_waitcnt vmcnt(1)
v_cmp_eq_u64 vcc, s[12:13], v[28:29]
s_cmp_eq_u64 vcc, -1
s_cbranch_scc0 .Lspin_68%=
.Lok_69%=:
K 220,172,173
Mvav 140,140,216
X 175
Mvav 108,140,208
X 176
Mvav 76,140,200
W 0
s_barrier
v_xor_b32 v242, 0x10000, v242
Mvav 44,140,192
Ra 128,242,0
K 221,174,175
Mvav 48,144,192
X 177
Mvav 80,144,200
X 178
Mvav 112,144,208
Ra 132,242,1024
Mvav 144,144,216
K 198,176,177
Mvav 148,148,216
X 179
Mvav 116,148,208
X 180
Mvav 84,148,200
Ra 136,242,2048
Mvav 52,148,192
)"
R"(K 199,178,179
Mvav 56,152,192
X 181
Mvav 88,152,200
Ra 140,242,3072
Mvav 120,152,208
X 182
Mvav 152,152,216
K 206,180,181
Mvav 156,156,216
X 183
Mvav 124,156,208
Mvav 92,156,200
K 207,182,183
Mvav 60,156,192
W 0
Cvaa 160,128,0,224
Ra 144,242,4096
Ra 148,242,5120
Cvaa 164,128,16,228
s_add_u32 m0, s62, 0
Ra 152,242,6144
Cvaa 168,128,32,232
X 184
Cvaa 172,128,48,236
X 185
Mvaa 160,132,4
X 186
Mvaa 164,132,20
Ra 156,242,7168
K 214,184,185
Mvaa 168,132,36
Dc 1,8,61,0
Mvaa 172,132,52
X 187
Mvaa 160,136,8
X 188
Mvaa 164,136,24
Ra 128,242,16384
K 215,186,187
Mvaa 168,136,40
Dc 1,8,61,1024
Mvaa 172,136,56
X 189
Mvaa 160,140,12
X 190
Mvaa 164,140,28
Ra 132,242,17408
K 222,188,189
Mvaa 168,140,44
Dc 1,8,61,2048
Mvaa 172,140,60
X 191
v_smfmac_f32_16x16x64_bf16 a[160:163], a[176:179], v[192:199], v240
Ra 136,242,18432
K 223,190,191
v_smfmac_f32_16x16x64_bf16 a[164:167], a[176:179], v[200:207], v240
X 160
v_smfmac_f32_16x16x64_bf16 a[168:171], a[176:179], v[208:215], v240
Dc 1,8,61,3072
v_smfmac_f32_16x16x64_bf16 a[172:175], a[176:179], v[216:223], v240
W 3
X 161
Cvaa 176,144,0,224
s_add_u32 m0, s62, 4096
X 162
Cvaa 180,144,16,228
s_sub_u32 s81, s57, 1
Ra 140,242,19456
K 192,160,161
Cvaa 184,144,32,232
X 163
Cvaa 188,144,48,236
s_lshl_b32 s85, s81, 5
Dc 243,8,61,0
Mvaa 176,148,4
X 164
Mvaa 180,148,20
s_add_u32 s85, s85, s56
Ra 144,242,20480
K 193,162,163
Mvaa 184,148,36
X 165
Mvaa 188,148,52
Dc 243,8,61,1024
Mvaa 176,152,8
s_lshl_b32 s85, s85, 1
X 166
Mvaa 180,152,24
Ra 148,242,21504
K 200,164,165
Mvaa 184,152,40
s_add_u32 s85, s85, s14
X 167
Mvaa 188,152,56
Dc 243,8,61,2048
Mvaa 176,156,12
s_sub_u32 s85, s85, 2
X 168
Mvaa 180,156,28
Ra 152,242,22528
K 201,166,167
Mvaa 184,156,44
X 169
Mvaa 188,156,60
W 3
s_cmp_lt_u32 s85, 160
s_cselect_b64 s[20:21], 0, -1
Dc 243,8,61,3072
Mvav 32,128,196
s_add_u32 m0, s62, 8192
X 170
Mvav 64,128,204
s_mul_i32 s18, s85, 205
Ra 156,242,23552
K 208,168,169
Mvav 96,128,212
X 171
Mvav 128,128,220
s_lshr_b32 s18, s18, 12
Dc 244,8,61,0
Mvav 132,132,220
X 172
Mvav 100,132,212
Ra 128,242,8192
K 209,170,171
Mvav 68,132,204
s_and_b32 s18, s18, 7
X 173
Mvav 36,132,196
Dc 244,8,61,1024
Mvav 40,136,196
X 174
Mvav 72,136,204
Ra 132,242,9216
v_lshrrev_b32 v4, 1, v1
Mvav 104,136,212
K 216,172,173
v_min_u32 v5, 344, v4
Mvav 136,136,220
Dc 244,8,61,2048
Mvav 140,140,220
X 175
Mvav 108,140,212
s_lshl_b32 s81, s18, 3
X 176
Mvav 76,140,204
Ra 136,242,10240
K 217,174,175
Mvav 44,140,196
W 3
Dc 244,8,61,3072
v_mov_b32 v6, s81
Mvav 48,144,196
X 177
Mvav 80,144,204
s_add_u32 m0, s62, 12288
X 178
Mvav 112,144,212
Ra 140,242,11264
K 194,176,177
Mvav 144,144,220
buffer_load_dwordx2 v[8:9], v4, s[8:11], 0 offen offset:2560 sc1
Mvav 148,148,220
X 179
Mvav 116,148,212
X 180
Mvav 84,148,204
Ra 144,242,12288
K 195,178,179
Mvav 52,148,196
buffer_load_dwordx2 v[10:11], v5, s[8:11], 0 offen offset:3072 sc1
Mvav 56,152,196
X 181
Mvav 88,152,204
X 182
Mvav 120,152,212
Ra 148,242,13312
K 202,180,181
Mvav 152,152,220
Dc 245,8,61,0
Mvav 156,156,220
X 183
Mvav 124,156,212
X 184
Mvav 92,156,204
Ra 152,242,14336
K 203,182,183
Mvav 60,156,196
W 3
buffer_load_dwordx2 v[12:13], v6, s[8:11], 0 offen offset:3584 sc1
Cvaa 160,128,0,224
X 185
Cvaa 164,128,16,228
X 186
Cvaa 168,128,32,232
Ra 156,242,15360
K 210,184,185
Cvaa 172,128,48,236
Dc 245,8,61,1024
Mvaa 160,132,4
X 187
Mvaa 164,132,20
X 188
Mvaa 168,132,36
Ra 128,242,24576
K 211,186,187
Mvaa 172,132,52
Dc 245,8,61,2048
Mvaa 160,136,8
X 189
Mvaa 164,136,24
X 190
Mvaa 168,136,40
Ra 132,242,25600
K 218,188,189
Mvaa 172,136,56
Dc 245,8,61,3072
Mvaa 160,140,12
s_add_u32 s61, s61, s63
s_xor_b32 s62, s62, 0x10000
s_add_u32 s64, s64, 1
Mvaa 164,140,28
X 191
Mvaa 168,140,44
Ra 136,242,26624
Mvaa 172,140,60
W 3
X 160
Cvaa 176,144,0,224
Ra 140,242,27648
K 219,190,191
Cvaa 180,144,16,228
X 161
Cvaa 184,144,32,232
X 162
Cvaa 188,144,48,236
K 196,160,161
Mvaa 176,148,4
X 163
Mvaa 180,148,20
Ra 144,242,28672
Mvaa 184,148,36
X 164
Mvaa 188,148,52
K 197,162,163
Mvaa 176,152,8
X 165
Mvaa 180,152,24
Ra 148,242,29696
Mvaa 184,152,40
X 166
Mvaa 188,152,56
K 204,164,165
Mvaa 176,156,12
X 167
Mvaa 180,156,28
Ra 152,242,30720
Mvaa 184,156,44
X 168
Mvaa 188,156,60
W 3
K 205,166,167
Mvav 32,128,192
X 169
Mvav 64,128,200
Ra 156,242,31744
Mvav 96,128,208
X 170
Mvav 128,128,216
K 212,168,169
Mvav 132,132,216
X 171
Mvav 100,132,208
X 172
Mvav 68,132,200
Mvav 36,132,192
K 213,170,171
Mvav 40,136,192
X 173
Mvav 72,136,200
X 174
Mvav 104,136,208
Mvav 136,136,216
s_waitcnt vmcnt(3)
v_cmp_eq_u64 vcc, s[12:13], v[8:9]
v_cmp_eq_u64_e64 s[86:87], s[12:13], v[10:11]
s_and_b64 vcc, vcc, s[86:87]
s_or_b64 vcc, vcc, s[20:21]
s_cmp_eq_u64 vcc, -1
s_cbranch_scc0 .Lspin_71%=
.Lok_72%=:
K 220,172,173
Mvav 140,140,216
X 175
Mvav 108,140,208
X 176
Mvav 76,140,200
s_waitcnt vmcnt(0) lgkmcnt(0)
s_barrier
v_xor_b32 v242, 0x10000, v242
Mvav 44,140,192
Ra 128,242,0
K 221,174,175
Mvav 48,144,192
X 177
Mvav 80,144,200
X 178
Mvav 112,144,208
Ra 132,242,1024
Mvav 144,144,216
K 198,176,177
Mvav 148,148,216
X 179
Mvav 116,148,208
X 180
Mvav 84,148,200
Ra 136,242,2048
Mvav 52,148,192
K 199,178,179
Mvav 56,152,192
X 181
Mvav 88,152,200
Ra 140,242,3072
Mvav 120,152,208
X 182
Mvav 152,152,216
K 206,180,181
Mvav 156,156,216
X 183
Mvav 124,156,208
Mvav 92,156,200
K 207,182,183
Mvav 60,156,192
W 0
Cvaa 160,128,0,224
Ra 144,242,4096
Ra 148,242,5120
Cvaa 164,128,16,228
s_add_u32 m0, s62, 0
Ra 152,242,6144
Cvaa 168,128,32,232
X 184
Cvaa 172,128,48,236
X 185
Mvaa 160,132,4
X 186
Mvaa 164,132,20
Ra 156,242,7168
K 214,184,185
Mvaa 168,132,36
Dc 1,8,61,0
Mvaa 172,132,52
X 187
Mvaa 160,136,8
X 188
Mvaa 164,136,24
Ra 128,242,16384
K 215,186,187
Mvaa 168,136,40
Dc 1,8,61,1024
Mvaa 172,136,56
X 189
Mvaa 160,140,12
X 190
Mvaa 164,140,28
Ra 132,242,17408
K 222,188,189
Mvaa 168,140,44
Dc 1,8,61,2048
Mvaa 172,140,60
X 191
v_smfmac_f32_16x16x64_bf16 a[160:163], a[176:179], v[192:199], v240
Ra 136,242,18432
K 223,190,191
v_smfmac_f32_16x16x64_bf16 a[164:167], a[176:179], v[200:207], v240
X 160
v_smfmac_f32_16x16x64_bf16 a[168:171], a[176:179], v[208:215], v240
Dc 1,8,61,3072
v_smfmac_f32_16x16x64_bf16 a[172:175], a[176:179], v[216:223], v240
W 3
X 161
Cvaa 176,144,0,224
s_add_u32 m0, s62, 4096
X 162
Cvaa 180,144,16,228
Ra 140,242,19456
K 192,160,161
Cvaa 184,144,32,232
X 163
Cvaa 188,144,48,236
Dc 243,8,61,0
Mvaa 176,148,4
X 164
Mvaa 180,148,20
Ra 144,242,20480
K 193,162,163
Mvaa 184,148,36
X 165
Mvaa 188,148,52
)"
R"(Dc 243,8,61,1024
Mvaa 176,152,8
X 166
Mvaa 180,152,24
Ra 148,242,21504
K 200,164,165
Mvaa 184,152,40
X 167
Mvaa 188,152,56
Dc 243,8,61,2048
Mvaa 176,156,12
X 168
Mvaa 180,156,28
Ra 152,242,22528
K 201,166,167
Mvaa 184,156,44
X 169
Mvaa 188,156,60
W 3
Dc 243,8,61,3072
Mvav 32,128,196
s_add_u32 m0, s62, 8192
X 170
Mvav 64,128,204
Ra 156,242,23552
K 208,168,169
Mvav 96,128,212
X 171
Mvav 128,128,220
Dc 244,8,61,0
Mvav 132,132,220
X 172
Mvav 100,132,212
Ra 128,242,8192
K 209,170,171
Mvav 68,132,204
X 173
Mvav 36,132,196
Dc 244,8,61,1024
Mvav 40,136,196
X 174
Mvav 72,136,204
Ra 132,242,9216
K 216,172,173
Mvav 104,136,212
X 175
Mvav 136,136,220
Dc 244,8,61,2048
Mvav 140,140,220
X 176
Mvav 108,140,212
Ra 136,242,10240
K 217,174,175
Mvav 76,140,204
X 177
Mvav 44,140,196
W 3
Dc 244,8,61,3072
Mvav 48,144,196
X 178
Mvav 80,144,204
s_add_u32 m0, s62, 12288
Ra 140,242,11264
Mvav 112,144,212
K 194,176,177
Mvav 144,144,220
X 179
Mvav 148,148,220
X 180
Mvav 116,148,212
Dc 245,8,61,0
Ra 144,242,12288
Mvav 84,148,204
K 195,178,179
Mvav 52,148,196
X 181
Mvav 56,152,196
X 182
Mvav 88,152,204
Dc 245,8,61,1024
Ra 148,242,13312
Mvav 120,152,212
v_xor_b32 v6, s12, v12
K 202,180,181
Mvav 152,152,220
X 183
Mvav 156,156,220
Ra 152,242,14336
v_and_b32 v6, 2047, v6
Mvav 124,156,212
Dc 245,8,61,2048
v_lshlrev_b32 v6, 3, v6
Mvav 92,156,204
X 184
Mvav 60,156,196
W 3
K 203,182,183
Cvaa 160,128,0,224
s_mov_b32 s81, 40960
X 185
Cvaa 164,128,16,228
Dc 245,8,61,3072
Ra 156,242,15360
Cvaa 168,128,32,232
s_add_u32 s61, s61, s63
X 186
Cvaa 172,128,48,236
K 210,184,185
Mvaa 160,132,4
X 187
Mvaa 164,132,20
buffer_load_dword v7, v6, s[8:11], s81 offen sc1
v_lshrrev_b32 v5, 7, v1
Mvaa 168,132,36
s_xor_b32 s62, s62, 0x10000
X 188
Mvaa 172,132,52
Ra 128,242,24576
K 211,186,187
Mvaa 160,136,8
Ra 132,242,25600
v_mul_u32_u24 v4, 20480, v5
Mvaa 164,136,24
s_add_u32 s64, s64, 1
X 189
Mvaa 168,136,40
X 190
Mvaa 172,136,56
v_and_b32 v6, 0x70, v1
K 218,188,189
Mvaa 160,140,12
X 191
Mvaa 164,140,28
Ra 136,242,26624
v_add_u32 v4, v4, v6
Mvaa 168,140,44
v_add_u32 v5, 2, v5
Mvaa 172,140,60
W 3
X 160
Cvaa 176,144,0,224
K 219,190,191
v_lshrrev_b32 v5, 3, v5
Cvaa 180,144,16,228
X 161
Cvaa 184,144,32,232
Ra 140,242,27648
v_lshl_or_b32 v5, v5, 31, v4
Cvaa 188,144,48,236
X 162
Mvaa 176,148,4
Ra 144,242,28672
K 196,160,161
Mvaa 180,148,20
s_lshl_b32 s19, s85, 7
X 163
Mvaa 184,148,36
X 164
Mvaa 188,148,52
s_add_u32 s19, s19, 2752512
K 197,162,163
Mvaa 176,152,8
X 165
Mvaa 180,152,24
s_and_b32 s81, s20, 134217728
Ra 148,242,29696
Mvaa 184,152,40
X 166
Mvaa 188,152,56
K 204,164,165
Mvaa 176,156,12
s_or_b32 s19, s19, s81
X 167
Mvaa 180,156,28
Ra 152,242,30720
Mvaa 184,156,44
X 168
Mvaa 188,156,60
W 3
buffer_load_dwordx4 a[64:67], v4, s[8:11], s19 offen sc1
K 205,166,167
Mvav 32,128,192
s_add_u32 s19, s19, 163840
X 169
Mvav 64,128,200
Ra 156,242,31744
Mvav 96,128,208
X 170
Mvav 128,128,216
buffer_load_dwordx4 a[68:71], v4, s[8:11], s19 offen sc1
K 212,168,169
Mvav 132,132,216
X 171
Mvav 100,132,208
s_add_u32 s19, s19, 163840
X 172
Mvav 68,132,200
Mvav 36,132,192
buffer_load_dwordx4 a[72:75], v4, s[8:11], s19 offen sc1
K 213,170,171
Mvav 40,136,192
X 173
Mvav 72,136,200
X 174
Mvav 104,136,208
s_add_u32 s19, s19, 163840
Mvav 136,136,216
K 220,172,173
Mvav 140,140,216
X 175
Mvav 108,140,208
buffer_load_dwordx4 a[76:79], v4, s[8:11], s19 offen sc1
Mvav 76,140,200
s_waitcnt vmcnt(5) lgkmcnt(0)
s_barrier
v_xor_b32 v242, 0x10000, v242
Mvav 44,140,192
s_add_u32 s19, s19, 163840
X 176
Mvav 48,144,192
Ra 128,242,0
K 221,174,175
Mvav 80,144,200
X 177
Mvav 112,144,208
buffer_load_dwordx4 a[80:83], v4, s[8:11], s19 offen sc1
Ra 132,242,1024
Mvav 144,144,216
s_add_u32 s19, s19, 163840
X 178
Mvav 148,148,216
K 198,176,177
Mvav 116,148,208
X 179
Mvav 84,148,200
buffer_load_dwordx4 a[84:87], v4, s[8:11], s19 offen sc1
Ra 136,242,2048
Mvav 52,148,192
X 180
Mvav 56,152,192
s_add_u32 s19, s19, 163840
Ra 140,242,3072
K 199,178,179
Mvav 88,152,200
X 181
Mvav 120,152,208
X 182
Mvav 152,152,216
buffer_load_dwordx4 a[88:91], v5, s[8:11], s19 offen sc1
K 206,180,181
Mvav 156,156,216
X 183
Mvav 124,156,208
Mvav 92,156,200
K 207,182,183
Mvav 60,156,192
W 0
Cvaa 160,128,0,224
Ra 144,242,4096
Ra 148,242,5120
Cvaa 164,128,16,228
s_add_u32 m0, s62, 0
Ra 152,242,6144
Cvaa 168,128,32,232
X 184
Cvaa 172,128,48,236
X 185
Mvaa 160,132,4
X 186
Mvaa 164,132,20
Ra 156,242,7168
K 214,184,185
Mvaa 168,132,36
Dc 1,8,61,0
Mvaa 172,132,52
X 187
Mvaa 160,136,8
X 188
Mvaa 164,136,24
Ra 128,242,16384
K 215,186,187
Mvaa 168,136,40
Dc 1,8,61,1024
Mvaa 172,136,56
X 189
Mvaa 160,140,12
X 190
Mvaa 164,140,28
Ra 132,242,17408
K 222,188,189
Mvaa 168,140,44
Dc 1,8,61,2048
Mvaa 172,140,60
X 191
v_smfmac_f32_16x16x64_bf16 a[160:163], a[176:179], v[192:199], v240
Ra 136,242,18432
K 223,190,191
v_smfmac_f32_16x16x64_bf16 a[164:167], a[176:179], v[200:207], v240
X 160
v_smfmac_f32_16x16x64_bf16 a[168:171], a[176:179], v[208:215], v240
Dc 1,8,61,3072
v_smfmac_f32_16x16x64_bf16 a[172:175], a[176:179], v[216:223], v240
W 3
X 161
Cvaa 176,144,0,224
s_add_u32 m0, s62, 4096
X 162
Cvaa 180,144,16,228
Ra 140,242,19456
K 192,160,161
Cvaa 184,144,32,232
X 163
Cvaa 188,144,48,236
Dc 243,8,61,0
Mvaa 176,148,4
X 164
Mvaa 180,148,20
Ra 144,242,20480
K 193,162,163
Mvaa 184,148,36
X 165
Mvaa 188,148,52
Dc 243,8,61,1024
Mvaa 176,152,8
X 166
Mvaa 180,152,24
Ra 148,242,21504
K 200,164,165
Mvaa 184,152,40
X 167
Mvaa 188,152,56
Dc 243,8,61,2048
Mvaa 176,156,12
X 168
Mvaa 180,156,28
Ra 152,242,22528
K 201,166,167
Mvaa 184,156,44
X 169
Mvaa 188,156,60
W 3
Dc 243,8,61,3072
Mvav 32,128,196
s_add_u32 m0, s62, 8192
X 170
Mvav 64,128,204
Ra 156,242,23552
K 208,168,169
Mvav 96,128,212
X 171
Mvav 128,128,220
Dc 244,8,61,0
Mvav 132,132,220
X 172
Mvav 100,132,212
Ra 128,242,8192
K 209,170,171
Mvav 68,132,204
X 173
Mvav 36,132,196
Dc 244,8,61,1024
Mvav 40,136,196
X 174
Mvav 72,136,204
Ra 132,242,9216
K 216,172,173
Mvav 104,136,212
X 175
Mvav 136,136,220
Dc 244,8,61,2048
Mvav 140,140,220
s_bfe_u32 s81, s59, 0x10001
X 176
Mvav 108,140,212
s_lshl_b32 s82, s57, 1
Ra 136,242,10240
K 217,174,175
Mvav 76,140,204
s_add_u32 s81, s81, s82
X 177
Mvav 44,140,196
W 3
s_lshl_b32 s83, s81, 2
Dc 244,8,61,3072
Mvav 48,144,196
s_lshr_b32 s81, s59, 2
X 178
Mvav 80,144,204
s_add_u32 m0, s62, 12288
s_lshl_b32 s81, s81, 1
Ra 140,242,11264
Mvav 112,144,212
s_add_u32 s83, s83, s81
K 194,176,177
Mvav 144,144,220
s_and_b32 s81, s59, 1
X 179
Mvav 148,148,220
s_add_u32 s83, s83, s81
X 180
)"
R"(Mvav 116,148,212
s_and_b32 s81, s14, 1
Dc 245,8,61,0
Ra 144,242,12288
Mvav 84,148,204
s_mul_i32 s81, s81, 124
K 195,178,179
Mvav 52,148,196
s_xor_b32 s83, s83, s81
X 181
Mvav 56,152,196
s_mul_i32 s83, s83, 20480
X 182
Mvav 88,152,204
s_mul_i32 s81, s58, 320
Dc 245,8,61,1024
Ra 148,242,13312
Mvav 120,152,212
s_add_u32 s82, s83, s81
K 202,180,181
Mvav 152,152,220
X 183
Mvav 156,156,220
Ra 152,242,14336
v_bfe_u32 v246, v1, 4, 4
Mvav 124,156,212
Dc 245,8,61,2048
v_mul_u32_u24 v246, 1280, v246
Mvav 92,156,204
X 184
Mvav 60,156,196
W 3
v_lshrrev_b32 v247, 8, v1
K 203,182,183
Cvaa 160,128,0,224
v_mul_u32_u24 v247, 104, v247
v_add3_u32 v246, v246, v247, s82
Cvaa 164,128,16,228
X 185
Cvaa 168,128,32,232
Dc 245,8,61,3072
Ra 156,242,15360
Cvaa 172,128,48,236
s_add_u32 s61, s61, s63
X 186
Mvaa 160,132,4
Ra 128,242,24576
K 210,184,185
Mvaa 164,132,20
X 187
Mvaa 168,132,36
s_xor_b32 s62, s62, 0x10000
X 188
Mvaa 172,132,52
global_load_dword v3, v246, s[16:17]
K 211,186,187
Mvaa 160,136,8
s_lshl_b32 s81, s81, 3
s_add_u32 s83, s83, s81
s_add_u32 s83, s83, 131072
Mvaa 164,136,24
s_add_u32 s64, s64, 1
v_lshrrev_b32 v246, 9, v1
v_mul_u32_u24 v246, 10240, v246
Mvaa 168,136,40
v_and_b32 v247, 0x1f0, v1
v_min_u32 v247, 304, v247
Mvaa 172,136,56
X 189
Mvaa 160,140,12
X 190
Mvaa 164,140,28
Ra 132,242,25600
v_lshl_add_u32 v246, v247, 3, v246
Mvaa 168,140,44
buffer_load_dword v3, v246, s[8:11], s83 offen sc1
K 218,188,189
Mvaa 172,140,60
W 2
X 191
Cvaa 176,144,0,224
s_lshl_b32 s92, s58, 2
Ra 136,242,26624
Ra 140,242,27648
Cvaa 180,144,16,228
X 160
Cvaa 184,144,32,232
s_add_u32 s91, s92, s51
K 219,190,191
Cvaa 188,144,48,236
X 161
Mvaa 176,148,4
X 162
Mvaa 180,148,20
s_add_u32 s95, s92, 0
Ra 144,242,28672
K 196,160,161
Mvaa 184,148,36
X 163
Mvaa 188,148,52
X 164
Mvaa 176,152,8
s_and_b32 s95, s95, 15
Ra 148,242,29696
K 197,162,163
Mvaa 180,152,24
X 165
Mvaa 184,152,40
X 166
Mvaa 188,152,56
s_mul_i32 s95, s95, 40960
K 204,164,165
Mvaa 176,156,12
X 167
Mvaa 180,156,28
Ra 152,242,30720
Mvaa 184,156,44
s_mul_i32 s71, s51, 0
X 168
Mvaa 188,156,60
W 3
K 205,166,167
Mvav 32,128,192
X 169
Mvav 64,128,200
s_add_u32 s71, s71, 133120
Ra 156,242,31744
Mvav 96,128,208
X 170
Mvav 128,128,216
K 212,168,169
Mvav 132,132,216
s_add_u32 m0, s71, s48
X 171
Mvav 100,132,208
X 172
Mvav 68,132,200
Mvav 36,132,192
s_lshl2_add_u32 s94, s48, s95
K 213,170,171
Mvav 40,136,192
X 173
Mvav 72,136,200
X 174
Mvav 104,136,208
D 16,40,94,0
Mvav 136,136,216
K 220,172,173
Mvav 140,140,216
X 175
Mvav 108,140,208
X 176
Mvav 76,140,200
s_waitcnt vmcnt(3) lgkmcnt(0)
s_barrier
v_xor_b32 v242, 0x10000, v242
Mvav 44,140,192
D 17,40,94,1024
K 221,174,175
Mvav 48,144,192
Ra 128,242,0
Mvav 80,144,200
X 177
Mvav 112,144,208
X 178
Mvav 144,144,216
D 18,40,94,2048
Ra 132,242,1024
Mvav 148,148,216
K 198,176,177
Mvav 116,148,208
X 179
Mvav 84,148,200
X 180
Mvav 52,148,192
D 19,40,94,3072
Ra 136,242,2048
Mvav 56,152,192
s_add_u32 m0, m0, 4096
Ra 140,242,3072
K 199,178,179
Mvav 88,152,200
X 181
Mvav 120,152,208
X 182
Mvav 152,152,216
buffer_load_dwordx4 v20, s[40:43], s94 offen lds
K 206,180,181
Mvav 156,156,216
X 183
Mvav 124,156,208
Mvav 92,156,200
K 207,182,183
Mvav 60,156,192
W 0
Cvaa 160,128,0,224
Ra 144,242,4096
Ra 148,242,5120
Cvaa 164,128,16,228
s_add_u32 m0, s62, 0
Ra 152,242,6144
G 8,64
Cvaa 168,128,32,232
X 184
Cvaa 172,128,48,236
X 185
Mvaa 160,132,4
G 9,65
G 10,66
Mvaa 164,132,20
X 186
Mvaa 168,132,36
Ra 156,242,7168
K 214,184,185
Mvaa 172,132,52
Dc 1,8,61,0
G 11,67
Mvaa 160,136,8
X 187
Mvaa 164,136,24
X 188
Mvaa 168,136,40
Ra 128,242,16384
G 12,68
Mvaa 172,136,56
Dc 1,8,61,1024
G 13,69
Mvaa 160,140,12
K 215,186,187
G 14,70
Mvaa 164,140,28
X 189
Mvaa 168,140,44
X 190
Mvaa 172,140,60
Dc 1,8,61,2048
G 15,71
v_smfmac_f32_16x16x64_bf16 a[160:163], a[176:179], v[192:199], v240
Ra 132,242,17408
K 222,188,189
v_smfmac_f32_16x16x64_bf16 a[164:167], a[176:179], v[200:207], v240
X 191
s_nop 0
K 223,190,191
v_smfmac_f32_16x16x64_bf16 a[168:171], a[176:179], v[208:215], v240
A 8,8,12
A 9,9,13
v_smfmac_f32_16x16x64_bf16 a[172:175], a[176:179], v[216:223], v240
W 2
Dc 1,8,61,3072
Ra 136,242,18432
Cvaa 176,144,0,224
s_add_u32 m0, s62, 4096
X 160
Cvaa 180,144,16,228
A 10,10,14
A 11,11,15
Cvaa 184,144,32,232
X 161
Cvaa 188,144,48,236
X 162
Mvaa 176,148,4
Ra 140,242,19456
G 12,72
Mvaa 180,148,20
K 192,160,161
G 13,73
Mvaa 184,148,36
X 163
Mvaa 188,148,52
Dc 243,8,61,0
G 14,74
Mvaa 176,152,8
X 164
Mvaa 180,152,24
Ra 144,242,20480
G 15,75
Mvaa 184,152,40
K 193,162,163
A 8,8,12
Mvaa 188,152,56
Dc 243,8,61,1024
A 9,9,13
Mvaa 176,156,12
X 165
Mvaa 180,156,28
X 166
Mvaa 184,156,44
Ra 148,242,21504
K 200,164,165
Mvaa 188,156,60
W 2
Dc 243,8,61,2048
A 10,10,14
Mvav 32,128,196
X 167
Mvav 64,128,204
Ra 152,242,22528
A 11,11,15
Mvav 96,128,212
X 168
Mvav 128,128,220
Dc 243,8,61,3072
G 12,76
Mvav 132,132,220
s_add_u32 m0, s62, 8192
K 201,166,167
G 13,77
Mvav 100,132,212
X 169
Mvav 68,132,204
X 170
Mvav 36,132,196
Ra 156,242,23552
G 14,78
Mvav 40,136,196
K 208,168,169
G 15,79
Mvav 72,136,204
X 171
Mvav 104,136,212
Dc 244,8,61,0
A 8,8,12
Mvav 136,136,220
X 172
Mvav 140,140,220
Ra 128,242,8192
A 9,9,13
Mvav 108,140,212
K 209,170,171
A 10,10,14
Mvav 76,140,204
X 173
Mvav 44,140,196
W 1
Dc 244,8,61,1024
A 11,11,15
Mvav 48,144,196
X 174
Mvav 80,144,204
Ra 132,242,9216
G 12,80
Mvav 112,144,212
K 216,172,173
G 13,81
Mvav 144,144,220
Dc 244,8,61,2048
Ra 136,242,10240
Mvav 148,148,220
X 175
Mvav 116,148,212
X 176
Mvav 84,148,204
G 14,82
K 217,174,175
Mvav 52,148,196
Dc 244,8,61,3072
G 15,83
Mvav 56,152,196
s_add_u32 m0, s62, 12288
X 177
Mvav 88,152,204
Ra 140,242,11264
A 8,8,12
Mvav 120,152,212
X 178
Mvav 152,152,220
A 9,9,13
K 194,176,177
Mvav 156,156,220
X 179
Mvav 124,156,212
Dc 245,8,61,0
A 10,10,14
Mvav 92,156,204
X 180
Mvav 60,156,196
W 0
Ra 144,242,12288
A 11,11,15
Cvaa 160,128,0,224
s_andn2_b32 s81, s49, 262144
K 195,178,179
G 12,84
Cvaa 164,128,16,228
s_lshl_b32 s82, s58, 12
X 181
Cvaa 168,128,32,232
s_add_u32 s81, s81, s82
Dc 245,8,61,1024
G 13,85
Cvaa 172,128,48,236
s_cmp_lt_u32 s57, 6
s_cselect_b32 s81, s81, 79691776
X 182
Mvaa 160,132,4
Ra 148,242,13312
G 14,86
Mvaa 164,132,20
K 202,180,181
)"
R"(G 15,87
Mvaa 168,132,36
X 183
Mvaa 172,132,52
Dc 245,8,61,2048
Ra 152,242,14336
Mvaa 160,136,8
X 184
Mvaa 164,136,24
A 8,8,12
K 203,182,183
Mvaa 168,136,40
Ra 156,242,15360
A 9,9,13
Mvaa 172,136,56
X 185
Mvaa 160,140,12
Dc 245,8,61,3072
A 10,10,14
Mvaa 164,140,28
s_add_u32 s61, s61, s63
s_xor_b32 s62, s62, 0x10000
s_add_u32 s64, s64, 1
Mvaa 168,140,44
X 186
Mvaa 172,140,60
W 0
A 11,11,15
K 210,184,185
Cvaa 176,144,0,224
X 187
Cvaa 180,144,16,228
X 188
K 211,186,187
Cvaa 184,144,32,232
X 189
X 190
K 218,188,189
X 191
X 160
K 219,190,191
Cvaa 188,144,48,236
G 12,88
v_lshrrev_b32 v246, 9, v1
Mvaa 176,148,4
Ra 128,242,24576
v_and_b32 v247, 0x1f0, v1
Mvaa 180,148,20
G 13,89
v_lshlrev_b32 v247, 3, v247
Mvaa 184,148,36
G 14,90
v_lshl_add_u32 v246, v246, 17, v247
Mvaa 188,148,52
buffer_load_dword v3, v246, s[8:11], s81 offen sc1
G 15,91
Mvaa 176,152,8
s_add_u32 s81, s81, 262144
Ra 132,242,25600
A 8,8,12
Mvaa 180,152,24
Ra 136,242,26624
A 9,9,13
Mvaa 184,152,40
Ra 140,242,27648
A 10,10,14
Mvaa 188,152,56
buffer_load_dword v3, v246, s[8:11], s81 offen sc1
A 11,11,15
Mvaa 176,156,12
s_lshl_b32 s92, s58, 2
X 161
Mvaa 180,156,28
s_add_u32 s91, s92, s51
v_add_f32_dpp v8, v8, v8 row_ror:8 row_mask:0xf bank_mask:0xf
v_add_f32_dpp v9, v9, v9 row_ror:8 row_mask:0xf bank_mask:0xf
Mvaa 184,156,44
s_add_u32 s95, s92, 1
X 162
Mvaa 188,156,60
W 0
K 196,160,161
v_add_f32_dpp v10, v10, v10 row_ror:8 row_mask:0xf bank_mask:0xf
Mvav 32,128,192
s_and_b32 s95, s95, 15
X 163
Mvav 64,128,200
s_mul_i32 s95, s95, 40960
Ra 144,242,28672
v_add_f32_dpp v11, v11, v11 row_ror:8 row_mask:0xf bank_mask:0xf
Mvav 96,128,208
s_mul_i32 s71, s51, 0
X 164
Mvav 128,128,216
s_add_u32 s71, s71, 143360
v_mov_b32 v12, v8
K 197,162,163
Mvav 132,132,216
s_add_u32 m0, s71, s48
X 165
Mvav 100,132,208
Ra 148,242,29696
v_mov_b32 v13, v9
Mvav 68,132,200
X 166
Mvav 36,132,192
s_lshl2_add_u32 s94, s48, s95
v_mov_b32 v14, v10
K 204,164,165
Mvav 40,136,192
Ra 152,242,30720
v_mov_b32 v15, v11
Mvav 72,136,200
X 167
Mvav 104,136,208
Ra 156,242,31744
v_permlane16_swap_b32 v8, v12
Mvav 136,136,216
X 168
Mvav 140,140,216
v_permlane16_swap_b32 v9, v13
K 205,166,167
Mvav 108,140,208
X 169
Mvav 76,140,200
s_waitcnt vmcnt(2) lgkmcnt(0)
s_barrier
v_xor_b32 v242, 0x10000, v242
Mvav 44,140,192
v_permlane16_swap_b32 v10, v14
v_permlane16_swap_b32 v11, v15
Mvav 48,144,192
X 170
Mvav 80,144,200
K 212,168,169
A 8,8,12
Mvav 112,144,208
X 171
Mvav 144,144,216
A 9,9,13
A 10,10,14
Mvav 148,148,216
X 172
Mvav 116,148,208
K 213,170,171
A 11,11,15
Mvav 84,148,200
X 173
Mvav 52,148,192
X 174
Mvav 56,152,192
D 16,40,94,0
v_mov_b32 v12, v8
Mvav 88,152,200
K 220,172,173
v_mov_b32 v13, v9
Mvav 120,152,208
X 175
Mvav 152,152,216
v_mov_b32 v14, v10
v_mov_b32 v15, v11
Mvav 156,156,216
D 17,40,94,1024
Ra 128,242,0
Mvav 124,156,208
X 176
Mvav 92,156,200
K 221,174,175
v_permlane32_swap_b32 v8, v12
Mvav 60,156,192
D 18,40,94,2048
Ra 132,242,1024
D 19,40,94,3072
s_add_u32 m0, m0, 4096
Ra 136,242,2048
Ra 140,242,3072
buffer_load_dwordx4 v20, s[40:43], s94 offen lds
X 177
v_permlane32_swap_b32 v9, v13
X 178
v_permlane32_swap_b32 v10, v14
K 198,176,177
X 179
v_permlane32_swap_b32 v11, v15
X 180
A 8,8,12
K 199,178,179
A 9,9,13
X 181
A 10,10,14
X 182
A 11,11,15
K 206,180,181
X 183
s_nop 0
K 207,182,183
W 0
Cvaa 160,128,0,224
Ra 144,242,4096
Ra 148,242,5120
Cvaa 164,128,16,228
s_add_u32 m0, s62, 0
Ra 152,242,6144
v_and_b32 v7, 0xffff0000, v7
Cvaa 168,128,32,232
X 184
Cvaa 172,128,48,236
X 185
Mvaa 160,132,4
Ra 156,242,7168
v_ashrrev_i32 v6, 31, v7
Mvaa 164,132,20
X 186
Mvaa 168,132,36
K 214,184,185
v_not_b32 v6, v6
Mvaa 172,132,52
Dc 1,8,61,0
v_or_b32 v6, 0x80000000, v6
Mvaa 160,136,8
X 187
Mvaa 164,136,24
X 188
Mvaa 168,136,40
Ra 128,242,16384
v_xor_b32 v7, v7, v6
Mvaa 172,136,56
Dc 1,8,61,1024
K 215,186,187
Mvaa 160,140,12
X 189
Mvaa 164,140,28
Ra 132,242,17408
v_and_b32 v7, 0xffff0000, v7
Mvaa 168,140,44
X 190
Mvaa 172,140,60
Dc 1,8,61,2048
v_mul_f32 v7, 0xbfb8aa3b, v7
v_smfmac_f32_16x16x64_bf16 a[160:163], a[176:179], v[192:199], v240
Ra 136,242,18432
K 222,188,189
v_smfmac_f32_16x16x64_bf16 a[164:167], a[176:179], v[200:207], v240
X 191
s_nop 0
K 223,190,191
v_smfmac_f32_16x16x64_bf16 a[168:171], a[176:179], v[208:215], v240
X 7
v_smfmac_f32_16x16x64_bf16 a[172:175], a[176:179], v[216:223], v240
W 3
Dc 1,8,61,3072
v_add_f32 v7, 1.0, v7
Cvaa 176,144,0,224
s_add_u32 m0, s62, 4096
X 160
Cvaa 180,144,16,228
X 161
Cvaa 184,144,32,232
X 162
Cvaa 188,144,48,236
Ra 140,242,19456
K 192,160,161
Mvaa 176,148,4
v_rcp_f32 v7, v7
Mvaa 180,148,20
X 163
Mvaa 184,148,36
Dc 243,8,61,0
Ra 144,242,20480
Mvaa 188,148,52
X 164
Mvaa 176,152,8
K 7,7,7
K 193,162,163
Mvaa 180,152,24
X 165
Mvaa 184,152,40
Dc 243,8,61,1024
v_lshlrev_b32 v7, 16, v7
Mvaa 188,152,56
X 166
Mvaa 176,156,12
Ra 148,242,21504
K 12,8,9
Mvaa 180,156,28
K 200,164,165
K 13,10,11
Mvaa 184,156,44
Dc 243,8,61,2048
Ra 152,242,22528
Mvaa 188,156,60
W 3
X 167
Mvav 32,128,196
X 168
Mvav 64,128,204
v_lshlrev_b32 v8, 16, v12
K 201,166,167
Mvav 96,128,212
Dc 243,8,61,3072
v_and_b32 v9, 0xffff0000, v12
Mvav 128,128,220
s_add_u32 m0, s62, 8192
X 169
Mvav 132,132,220
X 170
Mvav 100,132,212
Ra 156,242,23552
v_lshlrev_b32 v10, 16, v13
Mvav 68,132,204
K 208,168,169
v_and_b32 v11, 0xffff0000, v13
Mvav 36,132,196
X 171
Mvav 40,136,196
Dc 244,8,61,0
Ra 128,242,8192
Mvav 72,136,204
X 172
Mvav 104,136,212
U 8,8,7
K 209,170,171
Mvav 136,136,220
X 173
Mvav 140,140,220
Dc 244,8,61,1024
U 9,9,7
Mvav 108,140,212
X 174
Mvav 76,140,204
Ra 132,242,9216
U 10,10,7
Mvav 44,140,196
W 2
K 216,172,173
U 11,11,7
Mvav 48,144,196
Dc 244,8,61,2048
Ra 136,242,10240
Mvav 80,144,204
X 175
Mvav 112,144,212
X 176
Mvav 144,144,220
K 12,8,9
K 217,174,175
Mvav 148,148,220
Dc 244,8,61,3072
K 14,10,11
Mvav 116,148,212
s_add_u32 m0, s62, 12288
X 177
Mvav 84,148,204
X 178
Mvav 52,148,196
Ra 140,242,11264
v_mov_b32 v13, s13
Mvav 56,152,196
K 194,176,177
v_mov_b32 v15, s13
Mvav 88,152,204
X 179
Mvav 120,152,212
X 180
Mvav 152,152,220
Dc 245,8,61,0
Ra 144,242,12288
Mvav 156,156,220
v_lshrrev_b32 v6, 7, v1
K 195,178,179
Mvav 124,156,212
X 181
Mvav 92,156,204
Ra 148,242,13312
v_lshl_or_b32 v6, v6, 28, v1
Mvav 60,156,196
W 2
s_lshl_b32 s19, s85, 7
Dc 245,8,61,1024
Cvaa 160,128,0,224
s_add_u32 s19, s19, 98304
X 182
Cvaa 164,128,16,228
s_and_b32 s81, s20, 134217728
Ra 152,242,14336
K 202,180,181
Cvaa 168,128,32,232
s_or_b32 s19, s19, s81
X 183
Cvaa 172,128,48,236
Dc 245,8,61,2048
Ra 156,242,15360
Mvaa 160,132,4
X 184
Mvaa 164,132,20
Ra 128,242,24576
K 203,182,183
Mvaa 168,132,36
X 185
Mvaa 172,132,52
)"
R"(Dc 245,8,61,3072
Mvaa 160,136,8
s_add_u32 s61, s61, s63
X 186
Mvaa 164,136,24
s_xor_b32 s62, s62, 0x10000
Ra 132,242,25600
K 210,184,185
Mvaa 168,136,40
s_add_u32 s64, s64, 1
buffer_store_dwordx4 v[12:15], v6, s[8:11], s19 offen sc1
Mvaa 172,136,56
X 187
Mvaa 160,140,12
X 188
Mvaa 164,140,28
Ra 136,242,26624
K 211,186,187
Mvaa 168,140,44
s_lshl_b32 s92, s58, 2
X 189
Mvaa 172,140,60
W 3
X 190
Cvaa 176,144,0,224
Ra 140,242,27648
K 218,188,189
Cvaa 180,144,16,228
X 191
Cvaa 184,144,32,232
X 160
K 219,190,191
Cvaa 188,144,48,236
s_add_u32 s91, s92, s51
X 161
Mvaa 176,148,4
X 162
Mvaa 180,148,20
s_add_u32 s95, s92, 2
Ra 144,242,28672
K 196,160,161
Mvaa 184,148,36
X 163
Mvaa 188,148,52
X 164
Mvaa 176,152,8
s_and_b32 s95, s95, 15
Ra 148,242,29696
K 197,162,163
Mvaa 180,152,24
X 165
Mvaa 184,152,40
X 166
Mvaa 188,152,56
s_mul_i32 s95, s95, 40960
K 204,164,165
Mvaa 176,156,12
X 167
Mvaa 180,156,28
Ra 152,242,30720
Mvaa 184,156,44
s_mul_i32 s71, s51, 0
X 168
Mvaa 188,156,60
W 3
K 205,166,167
Mvav 32,128,192
X 169
Mvav 64,128,200
s_add_u32 s71, s71, 153600
Ra 156,242,31744
Mvav 96,128,208
X 170
Mvav 128,128,216
K 212,168,169
Mvav 132,132,216
s_add_u32 m0, s71, s48
X 171
Mvav 100,132,208
X 172
Mvav 68,132,200
Mvav 36,132,192
s_lshl2_add_u32 s94, s48, s95
K 213,170,171
Mvav 40,136,192
X 173
Mvav 72,136,200
X 174
Mvav 104,136,208
D 16,40,94,0
Mvav 136,136,216
K 220,172,173
Mvav 140,140,216
X 175
Mvav 108,140,208
X 176
Mvav 76,140,200
s_waitcnt vmcnt(2) lgkmcnt(0)
s_barrier
v_xor_b32 v242, 0x10000, v242
Mvav 44,140,192
D 17,40,94,1024
K 221,174,175
Mvav 48,144,192
Ra 128,242,0
Mvav 80,144,200
X 177
Mvav 112,144,208
X 178
Mvav 144,144,216
D 18,40,94,2048
Ra 132,242,1024
Mvav 148,148,216
K 198,176,177
Mvav 116,148,208
X 179
Mvav 84,148,200
X 180
Mvav 52,148,192
D 19,40,94,3072
Ra 136,242,2048
Mvav 56,152,192
s_add_u32 m0, m0, 4096
Ra 140,242,3072
K 199,178,179
Mvav 88,152,200
X 181
Mvav 120,152,208
X 182
Mvav 152,152,216
buffer_load_dwordx4 v20, s[40:43], s94 offen lds
K 206,180,181
Mvav 156,156,216
X 183
Mvav 124,156,208
Mvav 92,156,200
K 207,182,183
Mvav 60,156,192
.Lf1_38%=:
W 0
Cvaa 160,128,0,224
s_lshl_b32 s92, s58, 2
s_add_u32 s91, s92, s51
s_add_u32 s95, s91, 3
Cvaa 164,128,16,228
s_and_b32 s95, s95, 15
s_mul_i32 s95, s95, 40960
s_mul_i32 s71, s51, 10240
Cvaa 168,128,32,232
s_add_u32 s71, s71, 0
s_add_u32 m0, s71, s48
s_lshl2_add_u32 s94, s48, s95
Cvaa 172,128,48,236
Ra 144,242,4096
Ra 148,242,5120
Mvaa 160,132,4
Ra 152,242,6144
D 16,40,94,0
Mvaa 164,132,20
s_add_u32 s95, s91, 5
s_and_b32 s95, s95, 15
s_mul_i32 s95, s95, 40960
Mvaa 168,132,36
s_mul_i32 s71, s51, 10240
s_add_u32 s71, s71, 20480
Ra 156,242,7168
Mvaa 172,132,52
X 184
Mvaa 160,136,8
X 185
Mvaa 164,136,24
X 186
Mvaa 168,136,40
D 17,40,94,1024
K 214,184,185
Mvaa 172,136,56
X 187
Mvaa 160,140,12
X 188
Mvaa 164,140,28
Ra 128,242,16384
K 215,186,187
Mvaa 168,140,44
X 189
Mvaa 172,140,60
X 190
v_smfmac_f32_16x16x64_bf16 a[160:163], a[176:179], v[192:199], v240
D 18,40,94,2048
K 222,188,189
v_smfmac_f32_16x16x64_bf16 a[164:167], a[176:179], v[200:207], v240
X 191
s_nop 0
K 223,190,191
v_smfmac_f32_16x16x64_bf16 a[168:171], a[176:179], v[208:215], v240
Ra 132,242,17408
Ra 136,242,18432
v_smfmac_f32_16x16x64_bf16 a[172:175], a[176:179], v[216:223], v240
W 3
D 19,40,94,3072
Cvaa 176,144,0,224
s_add_u32 m0, m0, 4096
X 160
Cvaa 180,144,16,228
X 161
Cvaa 184,144,32,232
X 162
Cvaa 188,144,48,236
buffer_load_dwordx4 v20, s[40:43], s94 offen lds
v_bfe_u32 v28, v1, 4, 2
Mvaa 176,148,4
v_lshlrev_b32 v28, 3, v28
K 192,160,161
Mvaa 180,148,20
X 163
Mvaa 184,148,36
X 164
Mvaa 188,148,52
Ra 140,242,19456
K 193,162,163
Mvaa 176,152,8
buffer_load_dwordx2 v[28:29], v28, s[8:11], s54 offen sc1
Ra 144,242,20480
Mvaa 180,152,24
s_add_u32 m0, s71, s48
s_lshl2_add_u32 s94, s48, s95
Ra 148,242,21504
Mvaa 184,152,40
X 165
Mvaa 188,152,56
X 166
Mvaa 176,156,12
D 16,40,94,0
K 200,164,165
Mvaa 180,156,28
X 167
Mvaa 184,156,44
X 172
Mvaa 188,156,60
s_cmp_eq_u32 s60, 16
s_cbranch_scc1 .Lmk_74%=
.Lmkr_75%=:
W 2
Mvav 32,128,196
D 17,40,94,1024
K 201,166,167
Mvav 64,128,204
X 173
Mvav 96,128,212
X 174
Mvav 128,128,220
D 18,40,94,2048
K 216,172,173
Mvav 132,132,220
s_mov_b32 s18, 40960
X 175
Mvav 100,132,212
Ra 152,242,22528
Ra 156,242,23552
Mvav 68,132,204
s_mov_b32 s19, 73728
X 168
Mvav 36,132,196
s_bitcmp1_b32 s14, 0
s_cselect_b32 s18, 49152, s18
s_cselect_b32 s19, 98304, s19
D 19,40,94,3072
K 217,174,175
Mvav 40,136,196
s_add_u32 m0, m0, 4096
X 169
Mvav 72,136,204
s_cmp_lt_u32 s14, 2
s_cselect_b32 s20, s18, s19
s_cselect_b32 s21, s19, s18
X 170
Mvav 104,136,212
buffer_load_dwordx4 v20, s[40:43], s94 offen lds
K 208,168,169
Mvav 136,136,220
s_waitcnt vmcnt(5)
v_cmp_eq_u64 vcc, s[12:13], v[28:29]
s_cmp_eq_u64 vcc, -1
s_cselect_b32 s46, 79691776, 0
s_add_u32 s81, s58, 0
s_and_b32 s81, s81, 3
Mvav 140,140,220
s_lshl_b32 s81, s81, 12
s_add_u32 s81, s81, s49
Ra 128,242,24576
Mvav 108,140,212
X 171
Mvav 76,140,204
X 176
Mvav 44,140,196
W 1
buffer_load_dwordx4 a[184:187], v1, s[44:47], s81 offen offset:0 sc1
K 209,170,171
Mvav 48,144,196
s_add_u32 s82, s81, 131072
X 177
Mvav 80,144,204
X 178
Mvav 112,144,212
K 194,176,177
v_add_u32 v2, s20, v1
Mvav 144,144,220
X 179
Mvav 148,148,220
X 180
Mvav 116,148,212
v_add_u32 v3, s21, v1
K 195,178,179
Mvav 84,148,204
s_lshl_b32 s18, s14, 9
buffer_load_dwordx4 a[200:203], v1, s[44:47], s82 offen offset:0 sc1
Ra 132,242,25600
Mvav 52,148,196
X 181
Mvav 56,152,196
v_lshrrev_b32 v232, 1, v1
v_add_u32 v232, s18, v232
Mvav 88,152,204
X 182
Mvav 120,152,212
K 202,180,181
v_bfe_u32 v12, v1, 6, 2
Mvav 152,152,220
buffer_load_dwordx4 a[188:191], v1, s[44:47], s81 offen offset:1024 sc1
v_lshrrev_b32 v13, 8, v1
Mvav 156,156,220
X 183
Mvav 124,156,212
X 184
Mvav 92,156,204
Ra 136,242,26624
Ra 140,242,27648
Mvav 60,156,196
K 203,182,183
v_bfe_u32 v14, v1, 4, 1
Maav 160,180,192
X 185
X 186
K 210,184,185
X 187
X 188
K 211,186,187
Maav 164,180,200
X 189
X 190
K 218,188,189
X 191
s_nop 0
K 219,190,191
Maav 168,180,208
buffer_load_dwordx4 a[204:207], v1, s[44:47], s82 offen offset:1024 sc1
v_lshlrev_b32 v14, 4, v14
Maav 172,180,216
W 0
v_mov_b32 v15, 0x3f80
v_lshlrev_b32 v15, v14, v15
Mvav 32,128,192
v_bfe_u32 v14, v1, 5, 1
v_xor_b32 v27, v12, v13
Mvav 64,128,200
buffer_load_dwordx4 a[192:195], v1, s[44:47], s81 offen offset:2048 sc1
v_lshl_or_b32 v27, v27, 1, v14
Mvav 96,128,208
Ra 144,242,28672
v_cmp_eq_u32_e64 s[86:87], 0, v27
Mvav 128,128,216
Ra 148,242,29696
v_mov_b32 v226, 0
Mvav 132,132,216
buffer_load_dwordx4 a[208:211], v1, s[44:47], s82 offen offset:2048 sc1
v_cndmask_b32_e64 v224, 0, v15, s[86:87]
Mvav 100,132,208
v_mov_b32 v228, 0
v_mov_b32 v230, v224
Mvav 68,132,200
v_cmp_eq_u32_e64 s[86:87], 1, v27
v_mov_b32 v227, 0
Mvav 36,132,192
buffer_load_dwordx4 a[196:199], v1, s[44:47], s81 offen offset:3072 sc1
Ra 152,242,30720
Mvav 40,136,192
v_cndmask_b32_e64 v225, 0, v15, s[86:87]
v_mov_b32 v229, 0
Mvav 72,136,200
Ra 156,242,31744
v_mov_b32 v231, v225
Mvav 104,136,208
buffer_load_dwordx4 a[212:215], v1, s[44:47], s82 offen offset:3072 sc1
G 4,160
Mvav 136,136,216
s_add_u32 s81, s58, 1
s_and_b32 s81, s81, 3
s_lshl_b32 s81, s81, 12
Mvav 140,140,216
s_add_u32 s81, s81, s49
G 8,161
)"
R"(G 5,164
Mvav 108,140,208
buffer_load_dwordx4 a[216:219], v1, s[44:47], s81 offen offset:0 sc1
G 9,165
Mvav 76,140,200
s_add_u32 s82, s81, 131072
G 6,168
G 10,169
Mvav 44,140,192
W 0
G 7,172
G 11,173
Mvav 48,144,192
buffer_load_dwordx4 a[232:235], v1, s[44:47], s82 offen offset:0 sc1
A 4,4,8
Mvav 80,144,200
A 5,5,9
A 6,6,10
Mvav 112,144,208
A 7,7,11
Mvav 144,144,216
ds_write_b64 v232, v[6:7] offset:57344
A 8,4,5
Mvav 148,148,216
A 9,6,7
A 8,8,9
Mvav 116,148,208
buffer_load_dwordx4 a[220:223], v1, s[44:47], s81 offen offset:1024 sc1
v_cmp_ngt_f32_e64 s[86:87], s90, v8
Mvav 84,148,200
s_lshl_b32 s19, s14, 2
v_mov_b32 v9, s19
Mvav 52,148,192
s_or_b64 s[88:89], s[88:89], s[86:87]
s_and_b64 s[88:89], s[88:89], s[24:25]
s_cselect_b32 s18, 1, 0
v_mov_b32 v8, s18
Mvav 56,152,192
ds_write_b32 v9, v8 offset:59392
v_xor_b32 v10, 1024, v232
Mvav 88,152,200
v_bfe_u32 v11, v1, 2, 4
K 96,96,97
Mvav 120,152,208
K 97,98,99
K 98,100,101
Mvav 152,152,216
buffer_load_dwordx4 a[236:239], v1, s[44:47], s82 offen offset:1024 sc1
K 99,102,103
Mvav 156,156,216
ds_write_b128 v2, v[96:99] offset:0
K 128,128,129
Mvav 124,156,208
K 129,130,131
K 130,132,133
Mvav 92,156,200
buffer_load_dwordx4 a[224:227], v1, s[44:47], s81 offen offset:2048 sc1
K 131,134,135
Mvav 60,156,192
ds_write_b128 v2, v[128:131] offset:4096
K 104,104,105
K 105,106,107
K 106,108,109
K 107,110,111
buffer_load_dwordx4 a[240:243], v1, s[44:47], s82 offen offset:2048 sc1
ds_write_b128 v2, v[104:107] offset:1024
K 136,136,137
K 137,138,139
K 138,140,141
K 139,142,143
ds_write_b128 v2, v[136:139] offset:5120
buffer_load_dwordx4 a[228:231], v1, s[44:47], s81 offen offset:3072 sc1
K 112,112,113
K 113,114,115
K 114,116,117
K 115,118,119
buffer_load_dwordx4 a[244:247], v1, s[44:47], s82 offen offset:3072 sc1
ds_write_b128 v2, v[112:115] offset:2048
K 144,144,145
K 145,146,147
K 146,148,149
K 147,150,151
ds_write_b128 v2, v[144:147] offset:6144
K 120,120,121
K 121,122,123
K 122,124,125
K 123,126,127
ds_write_b128 v2, v[120:123] offset:3072
K 152,152,153
K 153,154,155
K 154,156,157
K 155,158,159
ds_write_b128 v2, v[152:155] offset:7168
.Lend_41%=:
s_waitcnt vmcnt(8) lgkmcnt(0)
s_barrier
s_cmp_eq_u32 s46, 0
s_cbranch_scc1 .Llate_76%=
ds_read_b32 v11, v11 offset:59392
ds_read_b64 v[8:9], v10 offset:57344
Rv 176,3,0
Rv 180,3,1024
Rv 184,3,2048
Rv 188,3,3072
Rv 192,3,4096
Rv 196,3,5120
Rv 200,3,6144
Rv 204,3,7168
W 6
Mvvv 32,224,176
s_lshl_b32 s92, s58, 2
s_add_u32 s91, s92, s51
s_add_u32 s95, s91, 7
Mvvv 36,228,176
s_and_b32 s95, s95, 15
s_mul_i32 s95, s95, 40960
s_mul_i32 s71, s51, 20480
Mvvv 40,224,180
s_add_u32 s71, s71, 61440
s_add_u32 m0, s71, s48
s_lshl2_add_u32 s94, s48, s95
Mvvv 44,228,180
W 4
v_cmp_ne_u32 vcc, 0, v11
s_cmp_lg_u64 vcc, 0
s_cbranch_scc1 .Lredo%=
A 4,4,8
Mvvv 48,224,184
v_rcp_f32 v4, v4
Mvvv 52,228,184
Ta 128,25,10240
A 5,5,9
Mvvv 56,224,188
Ta 130,25,15360
D 16,40,94,0
Mvvv 60,228,188
W 4
s_add_u32 s95, s91, 9
s_and_b32 s95, s95, 15
Mvvv 64,224,192
s_mul_i32 s95, s95, 40960
s_mul_i32 s71, s51, 10240
s_add_u32 s71, s71, 112640
Mvvv 68,228,192
v_rcp_f32 v5, v5
Mvvv 72,224,196
s_lshl_b32 s93, s58, 12
s_add_u32 s93, s93, s50
Ta 132,26,10272
Mvvv 76,228,196
Ta 134,26,15392
W 4
Mvvv 80,224,200
U 32,32,4
U 33,33,4
Mvvv 84,228,200
Ta 136,25,10304
U 34,34,4
Mvvv 88,224,204
Ta 138,25,15424
U 35,35,4
Mvvv 92,228,204
Ta 140,26,10336
Ta 142,26,15456
W 4
Zvaa 96,128,184
U 36,36,4
U 37,37,4
Zvaa 104,128,200
D 17,40,94,1024
U 38,38,4
Zvaa 108,132,200
U 39,39,4
K 32,32,33
Zvaa 100,132,184
Ta 144,25,10368
Ta 146,25,15488
Ta 148,26,10400
Ta 150,26,15520
W 4
Zvaa 112,136,184
K 33,34,35
K 34,36,37
Zvaa 120,136,200
Ta 152,25,10432
K 35,38,39
Zvaa 124,140,200
buffer_store_dwordx4 v[32:35], v1, s[8:11], s93 offen offset:0 sc1
U 40,40,4
Zvaa 116,140,184
Ta 154,25,15552
Ta 156,26,10464
Ta 158,26,15584
W 4
Zvaa 128,144,184
U 41,41,4
U 42,42,4
Zvaa 136,144,200
U 43,43,4
U 44,44,4
Zvaa 140,148,200
D 18,40,94,2048
U 45,45,4
Zvaa 132,148,184
Ta 128,25,10496
Ta 130,25,15616
Ta 132,26,10528
Ta 134,26,15648
W 4
Zvaa 144,152,184
U 46,46,4
U 47,47,4
Zvaa 152,152,200
K 40,40,41
K 41,42,43
Zvaa 156,156,200
K 42,44,45
K 43,46,47
Zvaa 148,156,184
Ta 136,25,20480
Ta 138,25,25600
Ta 140,26,20512
Ta 142,26,25632
W 4
Zvaa 160,128,184
buffer_store_dwordx4 v[40:43], v1, s[8:11], s93 offen offset:1024 sc1
U 48,48,4
Zvaa 168,128,200
U 49,49,4
U 50,50,4
Zvaa 172,132,200
U 51,51,4
U 52,52,4
Zvaa 164,132,184
Ta 144,25,20544
Ta 146,25,25664
Ta 148,26,20576
Ta 150,26,25696
W 4
Mvaa 96,136,188
D 19,40,94,3072
U 53,53,4
Mvaa 104,136,204
s_add_u32 m0, m0, 4096
U 54,54,4
U 55,55,4
Mvaa 108,140,204
K 48,48,49
K 49,50,51
Mvaa 100,140,188
Ta 152,25,20608
Ta 154,25,25728
Ta 156,26,20640
Ta 158,26,25760
W 4
Mvaa 112,144,188
K 50,52,53
K 51,54,55
Mvaa 120,144,204
buffer_load_dwordx4 v20, s[40:43], s94 offen lds
Ta 128,25,20672
Mvaa 124,148,204
s_add_u32 m0, s71, s48
s_lshl2_add_u32 s94, s48, s95
Ta 130,25,25792
Mvaa 116,148,188
Ta 132,26,20704
Ta 134,26,25824
W 4
Mvaa 128,152,188
s_waitcnt vmcnt(7)
s_cmp_lg_u32 s70, 0
s_cbranch_scc1 .Lpd_78%=
.Lpdr_79%=:
Ta 136,25,20736
Ta 138,25,25856
Mvaa 136,152,204
buffer_store_dwordx4 v[48:51], v1, s[8:11], s93 offen offset:2048 sc1
U 56,56,4
Mvaa 140,156,204
U 57,57,4
U 58,58,4
Mvaa 132,156,188
Ta 140,26,20768
Ta 142,26,25888
W 4
Mvaa 144,128,188
U 59,59,4
U 60,60,4
Mvaa 152,128,204
U 61,61,4
U 62,62,4
Mvaa 156,132,204
U 63,63,4
K 56,56,57
Mvaa 148,132,188
Ta 144,25,30720
Ta 146,25,35840
Ta 148,26,30752
Ta 150,26,35872
W 4
Mvaa 160,136,188
D 16,40,94,0
K 57,58,59
Mvaa 168,136,204
K 58,60,61
K 59,62,63
Mvaa 172,140,204
Ta 152,25,30784
Ta 154,25,35904
Mvaa 164,140,188
Ta 156,26,30816
Ta 158,26,35936
W 4
Mvaa 96,144,192
buffer_store_dwordx4 v[56:59], v1, s[8:11], s93 offen offset:3072 sc1
U 64,64,5
Mvaa 104,144,208
U 65,65,5
U 66,66,5
Mvaa 108,148,208
U 67,67,5
U 68,68,5
Mvaa 100,148,192
Ta 128,25,30848
Ta 130,25,35968
Ta 132,26,30880
Ta 134,26,36000
W 4
Mvaa 112,152,192
D 17,40,94,1024
U 69,69,5
Mvaa 120,152,208
U 70,70,5
U 71,71,5
Mvaa 124,156,208
K 64,64,65
K 65,66,67
Mvaa 116,156,192
Ta 136,25,30912
Ta 138,25,36032
Ta 140,26,30944
Ta 142,26,36064
W 4
Mvaa 128,128,192
K 66,68,69
K 67,70,71
Mvaa 136,128,208
s_add_u32 s67, s93, 131072
buffer_store_dwordx4 v[64:67], v1, s[8:11], s67 offen offset:0 sc1
U 72,72,5
Mvaa 140,132,208
U 73,73,5
U 74,74,5
Mvaa 132,132,192
Ta 144,25,30976
Ta 146,25,36096
Ta 148,26,31008
Ta 150,26,36128
W 4
Mvaa 144,136,192
U 75,75,5
U 76,76,5
Mvaa 152,136,208
D 18,40,94,2048
U 77,77,5
Mvaa 156,140,208
U 78,78,5
U 79,79,5
Mvaa 148,140,192
Ta 152,21,0
Ta 154,21,5120
Ta 156,22,32
Ta 158,22,5152
W 4
Mvaa 160,144,192
K 72,72,73
K 73,74,75
Mvaa 168,144,208
K 74,76,77
K 75,78,79
Mvaa 172,148,208
buffer_store_dwordx4 v[72:75], v1, s[8:11], s67 offen offset:1024 sc1
U 80,80,5
Mvaa 164,148,192
Ta 128,21,64
Ta 130,21,5184
Ta 132,22,96
Ta 134,22,5216
W 4
Mvaa 96,152,196
U 81,81,5
U 82,82,5
Mvaa 104,152,212
U 83,83,5
U 84,84,5
Mvaa 108,156,212
D 19,40,94,3072
U 85,85,5
Mvaa 100,156,196
)"
R"(Ta 136,21,128
Ta 138,21,5248
Ta 140,22,160
Ta 142,22,5280
W 4
Mvaa 112,128,196
s_add_u32 m0, m0, 4096
U 86,86,5
U 87,87,5
Mvaa 120,128,212
K 80,80,81
K 81,82,83
Mvaa 124,132,212
buffer_load_dwordx4 v20, s[40:43], s94 offen lds
K 82,84,85
Mvaa 116,132,196
Ta 144,21,192
Ta 146,21,5312
Ta 148,22,224
Ta 150,22,5344
W 4
Mvaa 128,136,196
s_add_u32 s81, s58, 2
s_and_b32 s81, s81, 3
s_lshl_b32 s81, s81, 12
Mvaa 136,136,212
s_add_u32 s81, s81, s49
Ta 152,21,256
K 83,86,87
Mvaa 140,140,212
buffer_store_dwordx4 v[80:83], v1, s[8:11], s67 offen offset:2048 sc1
U 88,88,5
Mvaa 132,140,196
Ta 154,21,5376
Ta 156,22,288
Ta 158,22,5408
W 4
Mvaa 144,144,196
U 89,89,5
U 90,90,5
Mvaa 152,144,212
U 91,91,5
U 92,92,5
Mvaa 156,148,212
U 93,93,5
U 94,94,5
Mvaa 148,148,196
Ta 128,21,10240
Ta 130,21,15360
Ta 132,22,10272
Ta 134,22,15392
W 4
Mvaa 160,152,196
buffer_load_dwordx4 a[0:3], v1, s[8:11], s81 offen offset:0 sc1
U 95,95,5
Mvaa 168,152,212
s_add_u32 s82, s81, 131072
K 88,88,89
K 89,90,91
Mvaa 172,156,212
K 90,92,93
K 91,94,95
Mvaa 164,156,196
Ta 136,21,10304
Ta 138,21,15424
Ta 140,22,10336
Ta 142,22,15456
W 4
Mvaa 96,128,216
buffer_store_dwordx4 v[88:91], v1, s[8:11], s67 offen offset:3072 sc1
Ta 144,21,10368
Mvaa 104,128,232
Ta 146,21,15488
Ta 148,22,10400
Mvaa 108,132,232
Ta 150,22,15520
Ta 152,21,10432
Mvaa 100,132,216
W 5
Ta 154,21,15552
Ta 156,22,10464
Mvaa 112,136,216
buffer_load_dwordx4 a[16:19], v1, s[8:11], s82 offen offset:0 sc1
Ta 158,22,15584
Mvaa 120,136,232
Ta 128,21,10496
Ta 130,21,15616
Mvaa 124,140,232
Ta 132,22,10528
Ta 134,22,15648
Mvaa 116,140,216
W 8
Ta 136,21,20480
Ta 138,21,25600
Mvaa 128,144,216
buffer_load_dwordx4 a[4:7], v1, s[8:11], s81 offen offset:1024 sc1
Mvaa 136,144,232
Ta 140,22,20512
Ta 142,22,25632
Mvaa 140,148,232
Mvaa 132,148,216
W 8
Ta 144,21,20544
Ta 146,21,25664
Mvaa 144,152,216
buffer_load_dwordx4 a[20:23], v1, s[8:11], s82 offen offset:1024 sc1
Mvaa 152,152,232
Ta 148,22,20576
Ta 150,22,25696
Mvaa 156,156,232
Mvaa 148,156,216
W 8
Ta 152,21,20608
Ta 154,21,25728
Mvaa 160,128,216
buffer_load_dwordx4 a[8:11], v1, s[8:11], s81 offen offset:2048 sc1
Mvaa 168,128,232
Ta 156,22,20640
Ta 158,22,25760
Mvaa 172,132,232
Mvaa 164,132,216
W 8
Ta 128,21,20672
Ta 130,21,25792
Mvaa 96,136,220
buffer_load_dwordx4 a[24:27], v1, s[8:11], s82 offen offset:2048 sc1
Mvaa 104,136,236
Ta 132,22,20704
Ta 134,22,25824
Mvaa 108,140,236
Mvaa 100,140,220
W 8
Ta 136,21,20736
Ta 138,21,25856
Mvaa 112,144,220
buffer_load_dwordx4 a[12:15], v1, s[8:11], s81 offen offset:3072 sc1
Mvaa 120,144,236
Ta 140,22,20768
Ta 142,22,25888
Mvaa 124,148,236
Mvaa 116,148,220
W 8
Ta 144,21,30720
Ta 146,21,35840
Mvaa 128,152,220
Mvaa 136,152,236
Ta 148,22,30752
Ta 150,22,35872
Mvaa 140,156,236
buffer_load_dwordx4 a[28:31], v1, s[8:11], s82 offen offset:3072 sc1
Mvaa 132,156,220
W 8
s_lshl_b32 s92, s58, 2
s_add_u32 s91, s92, s51
Mvaa 144,128,220
s_add_u32 s95, s91, 11
s_and_b32 s95, s95, 15
s_mul_i32 s95, s95, 40960
Mvaa 152,128,236
s_mul_i32 s71, s51, 10240
s_add_u32 s71, s71, 40960
s_add_u32 m0, s71, s48
Mvaa 156,132,236
s_waitcnt vmcnt(10)
s_barrier
s_lshl2_add_u32 s94, s48, s95
Mvaa 148,132,220
W 4
Ta 152,21,30784
Ta 154,21,35904
Mvaa 160,136,220
Ta 156,22,30816
Ta 158,22,35936
Mvaa 168,136,236
D 16,40,94,0
Ta 128,21,30848
Mvaa 172,140,236
s_add_u32 s95, s91, 13
s_and_b32 s95, s95, 15
s_mul_i32 s95, s95, 40960
Mvaa 164,140,220
W 5
s_mul_i32 s71, s51, 20480
s_add_u32 s71, s71, 71680
Mvaa 96,144,224
Ta 130,21,35968
Ta 132,22,30880
Mvaa 104,144,240
Ta 134,22,36000
D 17,40,94,1024
Mvaa 108,148,240
Ta 136,21,30912
Ta 138,21,36032
Mvaa 100,148,224
W 6
Ta 140,22,30944
Ta 142,22,36064
Mvaa 112,152,224
Ta 144,21,30976
Ta 146,21,36096
Mvaa 120,152,240
D 18,40,94,2048
Ta 148,22,31008
Mvaa 124,156,240
Ta 150,22,36128
Mvaa 116,156,224
W 8
Ta 152,23,0
Ta 154,23,5120
Mvaa 128,128,224
Mvaa 136,128,240
D 19,40,94,3072
Ta 156,24,32
Mvaa 140,132,240
s_add_u32 m0, m0, 4096
Ta 158,24,5152
Mvaa 132,132,224
W 8
Ta 128,23,64
Ta 130,23,5184
Mvaa 144,136,224
Mvaa 152,136,240
buffer_load_dwordx4 v20, s[40:43], s94 offen lds
Ta 132,24,96
Mvaa 156,140,240
s_add_u32 m0, s71, s48
s_lshl2_add_u32 s94, s48, s95
Ta 134,24,5216
Mvaa 148,140,224
W 8
Ta 136,23,128
Ta 138,23,5248
Mvaa 160,144,224
Mvaa 168,144,240
D 16,40,94,0
Ta 140,24,160
Mvaa 172,148,240
s_add_u32 s95, s92, 15
s_and_b32 s95, s95, 15
s_mul_i32 s95, s95, 40960
Mvaa 164,148,224
W 7
s_mul_i32 s71, s51, 0
s_add_u32 s71, s71, 102400
Mvaa 96,152,228
Ta 142,24,5280
Ta 144,23,192
Mvaa 104,152,244
D 17,40,94,1024
Ta 146,23,5312
Mvaa 108,156,244
Ta 148,24,224
Ta 150,24,5344
Mvaa 100,156,228
W 8
Ta 152,23,256
Ta 154,23,5376
Mvaa 112,128,228
s_lshl_b32 s80, s58, 3
s_add_u32 s80, s80, s55
Mvaa 120,128,244
D 18,40,94,2048
Ta 156,24,288
Mvaa 124,132,244
Ta 158,24,5408
Mvaa 116,132,228
W 8
Ta 128,23,20480
Ta 130,23,25600
Mvaa 128,136,228
s_waitcnt vmcnt(15)
v_mov_b64 v[28:29], s[12:13]
s_mov_b64 exec, 1
buffer_store_dwordx2 v[28:29], off, s[8:11], s80 sc1
s_mov_b64 exec, -1
Mvaa 136,136,244
Ta 132,24,20512
Ta 134,24,25632
Mvaa 140,140,244
Mvaa 132,140,228
W 8
Ta 136,23,20544
Ta 138,23,25664
Mvaa 144,144,228
D 19,40,94,3072
Mvaa 152,144,244
s_add_u32 m0, m0, 4096
Ta 140,24,20576
Ta 142,24,25696
Mvaa 156,148,244
Mvaa 148,148,228
W 8
Ta 144,23,20608
Ta 146,23,25728
Mvaa 160,152,228
buffer_load_dwordx4 v20, s[40:43], s94 offen lds
Mvaa 168,152,244
s_add_u32 m0, s71, s48
s_lshl2_add_u32 s94, s48, s95
Ta 148,24,20640
Mvaa 172,156,244
Ta 150,24,25760
Mvaa 164,156,228
s_waitcnt vmcnt(17) lgkmcnt(8)
Ta 152,23,20672
Ta 154,23,25792
Mvaa 96,128,0
D 16,40,94,0
Mvaa 104,128,16
Ta 156,24,20704
Ta 158,24,25824
Mvaa 108,132,16
Mvaa 100,132,0
W 8
Ta 128,23,20736
Ta 130,23,25856
Mvaa 112,136,0
D 17,40,94,1024
Mvaa 120,136,16
Ta 132,24,20768
Ta 134,24,25888
Mvaa 124,140,16
Mvaa 116,140,0
W 8
Ta 136,23,51200
Ta 138,23,56320
Mvaa 128,144,0
D 18,40,94,2048
Mvaa 136,144,16
Ta 140,24,51232
Ta 142,24,56352
Mvaa 140,148,16
Mvaa 132,148,0
W 8
Ta 144,23,51264
Ta 146,23,56384
Mvaa 144,152,0
D 19,40,94,3072
Mvaa 152,152,16
s_add_u32 m0, m0, 4096
Ta 148,24,51296
Ta 150,24,56416
Mvaa 156,156,16
Mvaa 148,156,0
W 8
Ta 152,23,51328
Ta 154,23,56448
Mvaa 160,128,0
buffer_load_dwordx4 v20, s[40:43], s94 offen lds
Mvaa 168,128,16
s_add_u32 s81, s58, 3
s_and_b32 s81, s81, 3
s_lshl_b32 s81, s81, 12
Mvaa 172,132,16
s_add_u32 s81, s81, s49
Ta 156,24,51360
Ta 158,24,56480
Mvaa 164,132,0
s_waitcnt vmcnt(20) lgkmcnt(8)
Ta 128,23,51392
Ta 130,23,56512
Mvaa 96,136,4
buffer_load_dwordx4 a[32:35], v1, s[8:11], s81 offen offset:0 sc1
Mvaa 104,136,20
s_add_u32 s82, s81, 131072
Ta 132,24,51424
Ta 134,24,56544
Mvaa 108,140,20
Mvaa 100,140,4
W 8
Ta 136,23,51456
Ta 138,23,56576
Mvaa 112,144,4
buffer_load_dwordx4 a[48:51], v1, s[8:11], s82 offen offset:0 sc1
Mvaa 120,144,20
Ta 140,24,51488
Ta 142,24,56608
Mvaa 124,148,20
Mvaa 116,148,4
W 8
Ta 144,25,0
Ta 146,25,5120
Mvaa 128,152,4
buffer_load_dwordx4 a[36:39], v1, s[8:11], s81 offen offset:1024 sc1
Mvaa 136,152,20
Ta 148,26,32
Ta 150,26,5152
Mvaa 140,156,20
Mvaa 132,156,4
W 8
Ta 152,25,64
Ta 154,25,5184
Mvaa 144,128,4
buffer_load_dwordx4 a[52:55], v1, s[8:11], s82 offen offset:1024 sc1
Mvaa 152,128,20
Ta 156,26,96
Ta 158,26,5216
Mvaa 156,132,20
Mvaa 148,132,4
W 8
)"
R"(Ta 128,25,128
Ta 130,25,5248
Mvaa 160,136,4
buffer_load_dwordx4 a[40:43], v1, s[8:11], s81 offen offset:2048 sc1
Mvaa 168,136,20
Ta 132,26,160
Ta 134,26,5280
Mvaa 172,140,20
Mvaa 164,140,4
s_waitcnt vmcnt(23) lgkmcnt(8)
Ta 136,25,192
Ta 138,25,5312
Mvaa 96,144,8
s_waitcnt vmcnt(16)
s_barrier
buffer_load_dwordx4 a[56:59], v1, s[8:11], s82 offen offset:2048 sc1
Mvaa 104,144,24
Ta 140,26,224
v_and_b32 v27, 1, v12
Mvaa 108,148,24
Ta 142,26,5344
Mvaa 100,148,8
W 8
Ta 144,25,256
v_lshlrev_b32 v27, 1, v27
Mvaa 112,152,8
buffer_load_dwordx4 a[44:47], v1, s[8:11], s81 offen offset:3072 sc1
Ta 146,25,5376
Mvaa 120,152,24
Ta 148,26,288
v_xor_b32 v27, v27, v13
Mvaa 124,156,24
Ta 150,26,5408
Mvaa 116,156,8
W 8
Ta 152,21,40960
Ta 154,21,46080
Mvaa 128,128,8
buffer_load_dwordx4 a[60:63], v1, s[8:11], s82 offen offset:3072 sc1
v_and_b32 v12, 2, v12
Mvaa 136,128,24
Ta 156,22,40992
Ta 158,22,46112
Mvaa 140,132,24
v_or_b32 v12, v12, v14
Mvaa 132,132,8
W 8
Ta 128,21,41024
Ta 130,21,46144
Mvaa 144,136,8
Mvaa 152,136,24
Ta 132,22,41056
v_lshl_or_b32 v12, v27, 2, v12
Mvaa 156,140,24
Ta 134,22,46176
Mvaa 148,140,8
W 8
Ta 136,21,41088
v_cmp_eq_u32_e64 s[86:87], 0, v12
Mvaa 160,144,8
Ta 138,21,46208
Mvaa 168,144,24
Ta 140,22,41120
Ta 142,22,46240
Mvaa 172,148,24
v_cndmask_b32_e64 v4, 0, v15, s[86:87]
Mvaa 164,148,8
W 8
Ta 144,21,41152
Ta 146,21,46272
Mvaa 96,152,12
v_cmp_eq_u32_e64 s[86:87], 1, v12
Mvaa 104,152,28
Ta 148,22,41184
Ta 150,22,46304
Mvaa 108,156,28
Mvaa 100,156,12
W 8
Ta 152,21,41216
v_cndmask_b32_e64 v5, 0, v15, s[86:87]
Mvaa 112,128,12
Ta 154,21,46336
Mvaa 120,128,28
Ta 156,22,41248
v_cmp_eq_u32_e64 s[86:87], 2, v12
Mvaa 124,132,28
Ta 158,22,46368
Mvaa 116,132,12
W 8
Ta 128,21,51200
Ta 130,21,56320
Mvaa 128,136,12
v_cndmask_b32_e64 v6, 0, v15, s[86:87]
Mvaa 136,136,28
Ta 132,22,51232
Ta 134,22,56352
Mvaa 140,140,28
v_cmp_eq_u32_e64 s[86:87], 3, v12
Mvaa 132,140,12
W 8
Ta 136,21,51264
Ta 138,21,56384
Mvaa 144,144,12
v_bfe_u32 v28, v1, 4, 2
Mvaa 152,144,28
v_cndmask_b32_e64 v7, 0, v15, s[86:87]
v_lshlrev_b32 v28, 3, v28
Mvaa 156,148,28
Ta 140,22,51296
Ta 142,22,56416
Mvaa 148,148,12
W 8
buffer_load_dwordx2 v[28:29], v28, s[8:11], s55 offen sc1
v_cmp_eq_u32_e64 s[86:87], 4, v12
Mvaa 160,152,12
Ta 144,21,51328
Ta 146,21,56448
Mvaa 168,152,28
Ta 148,22,51360
Ta 150,22,56480
Mvaa 172,156,28
s_waitcnt vmcnt(9)
s_barrier
v_cndmask_b32_e64 v8, 0, v15, s[86:87]
Mvaa 164,156,12
s_waitcnt vmcnt(7) lgkmcnt(8)
Ta 152,21,51392
Ta 154,21,56512
Mvaa 96,128,32
v_cmp_eq_u32_e64 s[86:87], 5, v12
Mvaa 104,128,48
Ta 156,22,51424
Ta 158,22,56544
Mvaa 108,132,48
Mvaa 100,132,32
W 8
Ta 128,21,51456
v_cndmask_b32_e64 v9, 0, v15, s[86:87]
Mvaa 112,136,32
Ta 130,21,56576
Mvaa 120,136,48
Ta 132,22,51488
v_cmp_eq_u32_e64 s[86:87], 6, v12
Mvaa 124,140,48
Ta 134,22,56608
Mvaa 116,140,32
W 8
Ta 136,23,10240
Ta 138,23,15360
Mvaa 128,144,32
v_cndmask_b32_e64 v10, 0, v15, s[86:87]
Mvaa 136,144,48
Ta 140,24,10272
Ta 142,24,15392
Mvaa 140,148,48
v_cmp_eq_u32_e64 s[86:87], 7, v12
Mvaa 132,148,32
W 8
Ta 144,23,10304
Ta 146,23,15424
Mvaa 144,152,32
Mvaa 152,152,48
Ta 148,24,10336
v_cndmask_b32_e64 v11, 0, v15, s[86:87]
Mvaa 156,156,48
Ta 150,24,15456
Mvaa 148,156,32
W 8
s_mov_b32 s20, s38
Ta 152,23,10368
Mvaa 160,128,32
Ta 154,23,15488
Mvaa 168,128,48
Ta 156,24,10400
Ta 158,24,15520
Mvaa 172,132,48
s_and_b32 s21, s39, 0xffff
Mvaa 164,132,32
s_waitcnt vmcnt(5) lgkmcnt(8)
Ta 128,23,10432
Ta 130,23,15552
Mvaa 96,136,36
s_mov_b32 s22, 2621440
Mvaa 104,136,52
Ta 132,24,10464
Ta 134,24,15584
Mvaa 108,140,52
Mvaa 100,140,36
W 8
s_mov_b32 s23, 0x20000
Ta 136,23,10496
Mvaa 112,144,36
Ta 138,23,15616
Mvaa 120,144,52
s_and_b32 s17, s17, 0xffff
Ta 140,24,10528
Ta 142,24,15648
Mvaa 124,148,52
Mvaa 116,148,36
W 8
Ta 144,23,30720
Ta 146,23,35840
Mvaa 128,152,36
s_mov_b32 s18, 2621440
Mvaa 136,152,52
Ta 148,24,30752
Ta 150,24,35872
Mvaa 140,156,52
s_mov_b32 s19, 0x20000
Mvaa 132,156,36
W 8
Ta 152,23,30784
Ta 154,23,35904
Mvaa 144,128,36
Mvaa 152,128,52
s_lshr_b32 s66, s15, 3
Ta 156,24,30816
Ta 158,24,35936
Mvaa 156,132,52
s_waitcnt vmcnt(0)
v_cmp_eq_u64 vcc, s[12:13], v[28:29]
s_cmp_eq_u64 vcc, -1
s_cbranch_scc0 .Lspin_80%=
.Lok_81%=:
s_add_u32 s81, s58, 1
s_and_b32 s81, s81, 3
Mvaa 148,132,36
W 8
s_lshl_b32 s81, s81, 12
s_add_u32 s81, s81, s50
Mvaa 160,136,36
s_lshl_b32 s66, s66, 4
buffer_load_dwordx4 a[64:67], v1, s[8:11], s81 offen offset:0 sc1
Ta 128,23,30848
Mvaa 168,136,52
s_add_u32 s82, s81, 131072
Ta 130,23,35968
Ta 132,24,30880
Mvaa 172,140,52
s_add_u32 s66, s66, s59
Ta 134,24,36000
Mvaa 164,140,36
W 8
Ta 136,23,30912
Ta 138,23,36032
Mvaa 96,144,40
s_lshl_b32 s63, s66, 6
buffer_load_dwordx4 a[80:83], v1, s[8:11], s82 offen offset:0 sc1
Mvaa 104,144,56
Ta 140,24,30944
Ta 142,24,36064
Mvaa 108,148,56
Mvaa 100,148,40
W 8
s_add_u32 s63, s63, 65536
Ta 144,23,30976
Mvaa 112,152,40
Ta 146,23,36096
Mvaa 120,152,56
s_mul_i32 s62, s66, 40960
buffer_load_dwordx4 a[68:71], v1, s[8:11], s81 offen offset:1024 sc1
Ta 148,24,31008
Mvaa 124,156,56
Ta 150,24,36128
Mvaa 116,156,40
W 8
Ta 152,23,40960
Ta 154,23,46080
Mvaa 128,128,40
s_add_u32 s62, s62, 56623104
Mvaa 136,128,56
Ta 156,24,40992
Ta 158,24,46112
Mvaa 140,132,56
s_lshl_b32 s66, s14, 10
buffer_load_dwordx4 a[84:87], v1, s[8:11], s82 offen offset:1024 sc1
Mvaa 132,132,40
W 8
Ta 128,23,41024
Ta 130,23,46144
Mvaa 144,136,40
Mvaa 152,136,56
s_add_u32 s78, s62, s66
Ta 132,24,41056
Ta 134,24,46176
Mvaa 156,140,56
Mvaa 148,140,40
W 8
s_add_u32 s79, s78, 327680
buffer_load_dwordx4 a[72:75], v1, s[8:11], s81 offen offset:2048 sc1
Mvaa 160,144,40
Ta 136,23,41088
Ta 138,23,46208
Mvaa 168,144,56
Ta 140,24,41120
Ta 142,24,46240
Mvaa 172,148,56
s_and_b32 s66, s14, 1
Mvaa 164,148,40
W 8
Ta 144,23,41152
Ta 146,23,46272
Mvaa 96,152,44
s_mul_i32 s66, s66, 327680
buffer_load_dwordx4 a[88:91], v1, s[8:11], s82 offen offset:2048 sc1
Mvaa 104,152,60
Ta 148,24,41184
Ta 150,24,46304
Mvaa 108,156,60
Mvaa 100,156,44
W 8
s_add_u32 s65, s62, s66
Ta 152,23,41216
Mvaa 112,128,44
Ta 154,23,46336
Mvaa 120,128,60
s_add_u32 s65, s65, 4096
buffer_load_dwordx4 a[76:79], v1, s[8:11], s81 offen offset:3072 sc1
Ta 156,24,41248
Mvaa 124,132,60
Ta 158,24,46368
Mvaa 116,132,44
W 8
Ta 128,25,10240
Ta 130,25,15360
Mvaa 128,136,44
s_mov_b32 s64, 0
Mvaa 136,136,60
Ta 132,26,10272
Ta 134,26,15392
Mvaa 140,140,60
buffer_load_dwordx4 a[92:95], v1, s[8:11], s82 offen offset:3072 sc1
v_bfe_u32 v27, v1, 4, 3
Mvaa 132,140,44
W 8
s_add_u32 s81, s58, 2
s_and_b32 s81, s81, 3
Mvaa 144,144,44
s_lshl_b32 s81, s81, 12
s_add_u32 s81, s81, s50
K 96,96,97
Mvaa 152,144,60
Ta 136,25,10304
Ta 138,25,15424
Mvaa 156,148,60
v_lshlrev_b32 v27, 3, v27
K 97,98,99
Mvaa 148,148,44
W 6
Ta 140,26,10336
Ta 142,26,15456
Mvaa 160,152,44
buffer_load_dwordx4 a[96:99], v1, s[8:11], s81 offen offset:0 sc1
v_bfe_u32 v20, v1, 4, 4
Mvaa 168,152,60
s_add_u32 s82, s81, 131072
Ta 144,25,10368
K 98,100,101
Mvaa 172,156,60
Ta 146,25,15488
v_mul_u32_u24 v20, 1280, v20
Mvaa 164,156,44
W 6
Ta 148,26,10400
Ta 150,26,15520
Zvav 176,128,32
Ta 152,25,10432
K 99,102,103
Zvav 184,128,64
s_add_u32 s83, s52, 0
Ta 154,25,15552
buffer_load_dwordx4 a[112:115], v1, s[8:11], s82 offen offset:0 sc1
Zvav 188,132,64
Ta 156,26,10464
v_lshrrev_b32 v12, 8, v1
Zvav 180,132,32
W 7
Ta 158,26,15584
v_lshl_add_u32 v20, v12, 4, v20
Zvav 192,136,32
Ta 128,25,10496
Ta 130,25,15616
Zvav 200,136,64
s_bfe_u32 s66, s59, 0x10001
buffer_store_dwordx4 v[96:99], v1, s[8:11], s83 offen sc1
K 104,104,105
Zvav 204,140,64
Ta 132,26,10528
Ta 134,26,15648
Zvav 196,140,32
W 8
s_lshl_b32 s67, s57, 1
K 105,106,107
Zvav 208,144,32
Ta 136,25,20480
Ta 138,25,25600
Zvav 216,144,64
buffer_load_dwordx4 a[100:103], v1, s[8:11], s81 offen offset:1024 sc1
K 106,108,109
Zvav 220,148,64
)"
R"(s_add_u32 s66, s66, s67
Ta 140,26,20512
Ta 142,26,25632
Zvav 212,148,32
W 8
Ta 144,25,20544
Ta 146,25,25664
Zvav 224,152,32
s_lshl_b32 s61, s66, 2
K 107,110,111
Zvav 232,152,64
buffer_load_dwordx4 a[116:119], v1, s[8:11], s82 offen offset:1024 sc1
Ta 148,26,20576
Zvav 236,156,64
s_add_u32 s83, s52, 40960
Ta 150,26,25696
Zvav 228,156,32
W 8
s_lshr_b32 s66, s59, 2
Ta 152,25,20608
Zvav 240,128,32
Ta 154,25,25728
Zvav 248,128,64
s_lshl_b32 s66, s66, 1
buffer_load_dwordx4 a[104:107], v1, s[8:11], s81 offen offset:2048 sc1
Ta 156,26,20640
Zvav 252,132,64
Ta 158,26,25760
Zvav 244,132,32
W 8
Ta 128,25,20672
Ta 130,25,25792
Mvav 176,136,40
s_add_u32 s61, s61, s66
Mvav 184,136,72
buffer_store_dwordx4 v[104:107], v1, s[8:11], s83 offen sc1
K 112,112,113
Mvav 188,140,72
s_and_b32 s66, s59, 1
Ta 132,26,20704
K 113,114,115
Mvav 180,140,40
W 7
Ta 134,26,25824
K 114,116,117
Mvav 192,144,40
Ta 136,25,20736
Ta 138,25,25856
Mvav 200,144,72
s_add_u32 s61, s61, s66
buffer_load_dwordx4 a[120:123], v1, s[8:11], s82 offen offset:2048 sc1
Ta 140,26,20768
Mvav 204,148,72
Ta 142,26,25888
K 115,118,119
Mvav 196,148,40
W 8
s_xor_b32 s67, s61, 0
Ta 144,25,30720
Mvav 208,152,40
s_add_u32 s83, s52, 1024
Ta 146,25,35840
Mvav 216,152,72
buffer_load_dwordx4 a[108:111], v1, s[8:11], s81 offen offset:3072 sc1
Ta 148,26,30752
Mvav 220,156,72
s_mul_i32 s72, s67, 20480
Ta 150,26,35872
Mvav 212,156,40
W 8
Ta 152,25,30784
Ta 154,25,35904
Mvav 224,128,40
s_mul_i32 s66, s58, 320
Mvav 232,128,72
buffer_store_dwordx4 v[112:115], v1, s[8:11], s83 offen sc1
K 120,120,121
Mvav 236,132,72
Ta 156,26,30816
Ta 158,26,35936
Mvav 228,132,40
W 8
s_add_u32 s72, s72, s66
K 121,122,123
Mvav 240,136,40
Ta 128,25,30848
Ta 130,25,35968
Mvav 248,136,72
s_mul_i32 s75, s67, 20480
buffer_load_dwordx4 a[124:127], v1, s[8:11], s82 offen offset:3072 sc1
K 122,124,125
Mvav 252,140,72
s_add_u32 s81, s58, 3
s_and_b32 s81, s81, 3
s_lshl_b32 s81, s81, 12
Mvav 244,140,40
W 6
s_add_u32 s81, s81, s50
Ta 132,26,30880
Mvav 176,144,48
s_mul_i32 s66, s58, 2560
Ta 134,26,36000
Ta 136,25,30912
Mvav 184,144,80
buffer_load_dwordx4 a[184:187], v1, s[8:11], s81 offen offset:0 sc1
Ta 138,25,36032
Mvav 188,148,80
s_add_u32 s82, s81, 131072
Ta 140,26,30944
K 123,126,127
Mvav 180,148,48
W 7
s_add_u32 s83, s52, 41984
Ta 142,26,36064
Mvav 192,152,48
s_add_u32 s75, s75, s66
Ta 144,25,30976
Ta 146,25,36096
Mvav 200,152,80
s_add_u32 s75, s75, 131072
buffer_load_dwordx4 a[200:203], v1, s[8:11], s82 offen offset:0 sc1
Ta 148,26,31008
Mvav 204,156,80
Ta 150,26,36128
Mvav 196,156,48
W 8
s_xor_b32 s67, s61, 124
Ta 152,21,0
Mvav 208,128,48
Ta 154,21,5120
Mvav 216,128,80
buffer_store_dwordx4 v[120:123], v1, s[8:11], s83 offen sc1
K 128,128,129
Mvav 220,132,80
s_mul_i32 s73, s67, 20480
Ta 156,22,32
K 129,130,131
Mvav 212,132,48
W 7
Ta 158,22,5152
K 130,132,133
Mvav 224,136,48
s_mul_i32 s66, s58, 320
Ta 128,21,64
Ta 130,21,5184
Mvav 232,136,80
buffer_load_dwordx4 a[188:191], v1, s[8:11], s81 offen offset:1024 sc1
Ta 132,22,96
Mvav 236,140,80
Ta 134,22,5216
K 131,134,135
Mvav 228,140,48
W 8
s_add_u32 s73, s73, s66
Ta 136,21,128
Mvav 240,144,48
s_add_u32 s83, s52, 2048
Ta 138,21,5248
Mvav 248,144,80
s_mul_i32 s76, s67, 20480
buffer_load_dwordx4 a[204:207], v1, s[8:11], s82 offen offset:1024 sc1
Ta 140,22,160
Mvav 252,148,80
Ta 142,22,5280
Mvav 244,148,48
W 8
Ta 144,21,192
Ta 146,21,5312
Mvav 176,152,56
s_mul_i32 s66, s58, 2560
Mvav 184,152,88
buffer_load_dwordx4 a[192:195], v1, s[8:11], s81 offen offset:2048 sc1
Ta 148,22,224
Mvav 188,156,88
s_add_u32 s76, s76, s66
Ta 150,22,5344
Mvav 180,156,56
W 8
Ta 152,21,256
Ta 154,21,5376
Mvav 192,128,56
Mvav 200,128,88
s_add_u32 s76, s76, 131072
buffer_store_dwordx4 v[128:131], v1, s[8:11], s83 offen sc1
K 136,136,137
Mvav 204,132,88
K 137,138,139
K 138,140,141
Mvav 196,132,56
W 6
Ta 156,22,288
Ta 158,22,5408
Mvav 208,136,56
Ta 128,21,10240
v_bfe_u32 v15, v1, 7, 1
Mvav 216,136,88
buffer_load_dwordx4 a[208:211], v1, s[8:11], s82 offen offset:2048 sc1
Ta 130,21,15360
Mvav 220,140,88
Ta 132,22,10272
K 139,142,143
Mvav 212,140,56
W 7
Ta 134,22,15392
v_mul_u32_u24 v14, 10240, v15
Mvav 224,144,56
s_add_u32 s83, s52, 43008
Ta 136,21,10304
Ta 138,21,15424
Mvav 232,144,88
buffer_load_dwordx4 a[196:199], v1, s[8:11], s81 offen offset:3072 sc1
v_lshrrev_b32 v15, 8, v1
Mvav 236,148,88
Ta 140,22,10336
Ta 142,22,15456
Mvav 228,148,56
W 8
Ta 144,21,10368
v_lshl_add_u32 v14, v15, 7, v14
Mvav 240,152,56
Ta 146,21,15488
Mvav 248,152,88
buffer_load_dwordx4 a[212:215], v1, s[8:11], s82 offen offset:3072 sc1
v_and_b32 v15, 0x70, v1
Mvav 252,156,88
Ta 148,22,10400
Ta 150,22,15520
Mvav 244,156,56
s_waitcnt vmcnt(27) lgkmcnt(8)
Ta 152,21,10432
Ta 154,21,15552
Mvaa 176,128,64
v_add_u32 v14, v14, v15
Mvaa 184,128,80
buffer_store_dwordx4 v[136:139], v1, s[8:11], s83 offen sc1
K 144,144,145
Mvaa 188,132,80
s_bitcmp1_b32 s14, 0
s_cselect_b32 s74, s73, s72
s_cselect_b32 s77, s76, s75
K 145,146,147
K 146,148,149
Mvaa 180,132,64
W 6
Ta 156,22,10464
Ta 158,22,15584
Mvaa 192,136,64
Ta 128,21,10496
K 147,150,151
Mvaa 200,136,80
s_add_u32 s83, s52, 3072
s_add_u32 s74, s74, 256
Ta 130,21,15616
Mvaa 204,140,80
Ta 132,22,10528
Ta 134,22,15648
Mvaa 196,140,64
W 8
s_add_u32 s77, s77, 2048
buffer_store_dwordx4 v[144:147], v1, s[8:11], s83 offen sc1
Mvaa 208,144,64
Ta 136,21,20480
Ta 138,21,25600
Mvaa 216,144,80
Ta 140,22,20512
K 152,152,153
Mvaa 220,148,80
s_lshl_b32 s66, s14, 6
Ta 142,22,25632
Mvaa 212,148,64
W 8
Ta 144,21,20544
K 153,154,155
Mvaa 224,152,64
s_lshl_b32 s67, s14, 9
Ta 146,21,25664
Mvaa 232,152,80
Ta 148,22,20576
K 154,156,157
Mvaa 236,156,80
Ta 150,22,25696
Mvaa 228,156,64
W 8
s_add_u32 s72, s72, s66
Ta 152,21,20608
Mvaa 240,128,64
Ta 154,21,25728
K 155,158,159
Mvaa 248,128,80
s_add_u32 s75, s75, s67
Ta 156,22,20640
Ta 158,22,25760
Mvaa 252,132,80
s_add_u32 s83, s52, 44032
Mvaa 244,132,64
s_waitcnt vmcnt(27) lgkmcnt(8)
Ta 128,21,20672
Ta 130,21,25792
Mvaa 176,136,68
s_add_u32 s73, s73, s66
buffer_store_dwordx4 v[152:155], v1, s[8:11], s83 offen sc1
Mvaa 184,136,84
Ta 132,22,20704
Ta 134,22,25824
Mvaa 188,140,84
s_add_u32 s76, s76, s67
K 160,160,161
Mvaa 180,140,68
W 8
Ta 136,21,20736
Ta 138,21,25856
Mvaa 192,144,68
buffer_load_dwordx4 v[36:39], v20, s[16:19], s72 offen
Mvaa 200,144,84
Ta 140,22,20768
K 161,162,163
Mvaa 204,148,84
Ta 142,22,25888
Mvaa 196,148,68
W 8
Ta 144,21,30720
K 162,164,165
Mvaa 208,152,68
Ta 146,21,35840
Mvaa 216,152,84
Ta 148,22,30752
Ta 150,22,35872
Mvaa 220,156,84
K 163,166,167
Mvaa 212,156,68
W 8
buffer_load_dwordx4 v[44:47], v14, s[8:11], s75 offen sc1
Ta 152,21,30784
Mvaa 224,128,68
s_add_u32 s83, s52, 4096
Ta 154,21,35904
Mvaa 232,128,84
Ta 156,22,30816
Ta 158,22,35936
Mvaa 236,132,84
Mvaa 228,132,68
W 8
buffer_store_dwordx4 v[160:163], v1, s[8:11], s83 offen sc1
Ta 128,21,30848
Mvaa 240,136,68
Ta 130,21,35968
K 168,168,169
Mvaa 248,136,84
Ta 132,22,30880
Ta 134,22,36000
Mvaa 252,140,84
Mvaa 244,140,68
s_waitcnt vmcnt(29) lgkmcnt(8)
buffer_load_dwordx4 v[52:55], v20, s[16:19], s73 offen
K 169,170,171
Mvaa 176,144,72
Ta 136,21,30912
Ta 138,21,36032
Mvaa 184,144,88
Ta 140,22,30944
K 170,172,173
Mvaa 188,148,88
Ta 142,22,36064
Mvaa 180,148,72
W 8
Ta 144,21,30976
Ta 146,21,36096
Mvaa 192,152,72
buffer_load_dwordx4 v[60:63], v14, s[8:11], s76 offen sc1
K 171,174,175
Mvaa 200,152,88
Ta 148,22,31008
Ta 150,22,36128
Mvaa 204,156,88
s_add_u32 s83, s52, 45056
Mvaa 196,156,72
W 8
Ta 152,23,0
Ta 154,23,5120
Mvaa 208,128,72
buffer_store_dwordx4 v[168:171], v1, s[8:11], s83 offen sc1
Mvaa 216,128,88
Ta 156,24,32
Ta 158,24,5152
Mvaa 220,132,88
Mvaa 212,132,72
W 8
Ta 128,23,64
Ta 130,23,5184
Mvaa 224,136,72
buffer_load_dwordx4 v[68:71], v20, s[16:19], s74 offen
Mvaa 232,136,88
Ta 132,24,96
Ta 134,24,5216
Mvaa 236,140,88
Mvaa 228,140,72
W 8
Ta 136,23,128
Ta 138,23,5248
Mvaa 240,144,72
)"
R"(Mvaa 248,144,88
Ta 140,24,160
Ta 142,24,5280
Mvaa 252,148,88
buffer_load_dwordx4 v[76:79], v14, s[8:11], s77 offen sc1
Mvaa 244,148,72
s_waitcnt vmcnt(32) lgkmcnt(8)
Ta 144,23,192
Ta 146,23,5312
Mvaa 176,152,76
Mvaa 184,152,92
Ta 148,24,224
Ta 150,24,5344
Mvaa 188,156,92
Mvaa 180,156,76
W 8
Ta 152,23,256
Ta 154,23,5376
Mvaa 192,128,76
buffer_load_dwordx2 v[12:13], v27, s[8:11], 0 offen offset:3584 sc1
Mvaa 200,128,92
Ta 156,24,288
Ta 158,24,5408
Mvaa 204,132,92
Mvaa 196,132,76
W 8
Ta 128,23,20480
Ta 130,23,25600
Mvaa 208,136,76
Mvaa 216,136,92
Ta 132,24,20512
Ta 134,24,25632
Mvaa 220,140,92
Mvaa 212,140,76
W 8
Ta 136,23,51200
Ta 138,23,56320
Mvaa 224,144,76
Mvaa 232,144,92
Ta 140,24,51232
Ta 142,24,56352
Mvaa 236,148,92
Mvaa 228,148,76
W 8
Ta 144,25,0
Ta 146,25,5120
Mvaa 240,152,76
Mvaa 248,152,92
Ta 148,26,32
Ta 150,26,5152
Mvaa 252,156,92
Mvaa 244,156,76
s_waitcnt vmcnt(31) lgkmcnt(8)
Ta 152,21,40960
Ta 154,21,46080
Mvaa 176,128,96
Mvaa 184,128,112
Ta 156,22,40992
Ta 158,22,46112
Mvaa 188,132,112
Mvaa 180,132,96
s_waitcnt vmcnt(28) lgkmcnt(8)
Ta 128,21,51200
Ta 130,21,56320
Mvaa 176,136,100
Mvaa 184,136,116
Ta 132,22,51232
Ta 134,22,56352
Mvaa 188,140,116
Mvaa 180,140,100
s_waitcnt vmcnt(25) lgkmcnt(8)
Ta 136,23,10240
Ta 138,23,15360
Mvaa 176,144,104
Mvaa 184,144,120
Ta 140,24,10272
Ta 142,24,15392
Mvaa 188,148,120
Mvaa 180,148,104
s_waitcnt vmcnt(22) lgkmcnt(8)
Ta 144,23,30720
Ta 146,23,35840
Mvaa 176,152,108
Mvaa 184,152,124
Ta 148,24,30752
Ta 150,24,35872
Mvaa 188,156,124
Mvaa 180,156,108
s_waitcnt vmcnt(20) lgkmcnt(8)
Ta 152,23,40960
Ta 154,23,46080
Mvaa 176,128,184
Mvaa 184,128,200
Ta 156,24,40992
Ta 158,24,46112
Mvaa 188,132,200
Mvaa 180,132,184
s_waitcnt vmcnt(17) lgkmcnt(8)
Ta 128,23,20544
Ta 130,23,25664
Mvaa 176,136,188
Mvaa 184,136,204
Ta 132,24,20576
Ta 134,24,25696
Mvaa 188,140,204
Mvaa 180,140,188
s_waitcnt vmcnt(14) lgkmcnt(8)
Ta 136,23,51264
Ta 138,23,56384
Mvaa 176,144,192
Mvaa 184,144,208
Ta 140,24,51296
Ta 142,24,56416
Mvaa 188,148,208
Mvaa 180,148,192
s_waitcnt vmcnt(12) lgkmcnt(8)
Ta 144,25,64
Ta 146,25,5184
Mvaa 176,152,196
Mvaa 184,152,212
Ta 148,26,96
Ta 150,26,5216
Mvaa 188,156,212
Mvaa 180,156,196
W 8
Ta 152,21,41024
Ta 154,21,46144
Mvaa 192,128,96
Mvaa 200,128,112
Ta 156,22,41056
Ta 158,22,46176
Mvaa 204,132,112
K 176,176,177
Mvaa 196,132,96
W 8
Ta 128,21,51264
Ta 130,21,56384
Mvaa 192,136,100
K 177,178,179
Mvaa 200,136,116
Ta 132,22,51296
K 178,180,181
Mvaa 204,140,116
Ta 134,22,56416
K 179,182,183
Mvaa 196,140,100
W 8
Ta 136,23,10304
Ta 138,23,15424
Mvaa 192,144,104
s_add_u32 s83, s52, 327680
Mvaa 200,144,120
buffer_store_dwordx4 v[176:179], v1, s[8:11], s83 offen sc1
Ta 140,24,10336
Mvaa 204,148,120
Ta 142,24,15456
Mvaa 196,148,104
s_waitcnt vmcnt(4)
v_lshlrev_b32 v2, 2, v1
v_mov_b64 v[28:29], s[12:13]
s_mov_b64 exec, 3
buffer_store_dwordx2 v[28:29], v2, s[8:11], s53 offen offset:0 sc1
s_mov_b64 exec, -1
K 184,184,185
W 8
Mvaa 192,152,108
K 185,186,187
K 186,188,189
Mvaa 200,152,124
s_waitcnt vmcnt(2)
v_xor_b32 v12, s12, v12
v_xor_b32 v13, s13, v13
v_lshrrev_b32 v3, 11, v12
v_or_b32 v13, v13, v3
v_cmp_eq_u32 vcc, 0, v13
s_cmp_eq_u64 vcc, -1
s_cbranch_scc0 .LL_85%=
.LL_86%=:
v_bfe_u32 v13, v12, 4, 7
s_xor_b32 s66, s61, 124
v_cmp_eq_u32 vcc, s61, v13
v_cmp_eq_u32_e64 s[86:87], s66, v13
s_or_b32 s68, vcc_lo, s86
s_and_b32 s68, s68, 0xff
s_cbranch_scc1 .Lup_83%=
.Lupr_84%=:
Ta 144,23,30784
K 187,190,191
Mvaa 204,156,124
s_add_u32 s83, s52, 368640
Ta 146,23,35904
Ta 148,24,30816
Mvaa 196,156,108
W 7
buffer_store_dwordx4 v[184:187], v1, s[8:11], s83 offen sc1
Ta 150,24,35936
Mvaa 192,128,184
Ta 152,23,41024
v_lshlrev_b32 v32, 16, v36
Mvaa 200,128,200
Ta 154,23,46144
v_and_b32 v33, 0xffff0000, v36
Mvaa 204,132,200
Ta 156,24,41056
v_lshlrev_b32 v34, 16, v37
Mvaa 196,132,184
W 7
Ta 158,24,46176
v_and_b32 v35, 0xffff0000, v37
Mvaa 192,136,188
Ta 128,23,20608
v_lshlrev_b32 v36, 16, v38
Mvaa 200,136,204
Ta 130,23,25728
v_and_b32 v37, 0xffff0000, v38
Mvaa 204,140,204
Ta 132,24,20640
v_lshlrev_b32 v38, 16, v39
Mvaa 196,140,188
W 7
Ta 134,24,25760
v_and_b32 v39, 0xffff0000, v39
Mvaa 192,144,192
Ta 136,23,51328
v_lshlrev_b32 v40, 16, v44
Mvaa 200,144,208
Ta 138,23,56448
v_and_b32 v41, 0xffff0000, v44
Mvaa 204,148,208
Ta 140,24,51360
v_lshlrev_b32 v42, 16, v45
Mvaa 196,148,192
W 7
Ta 142,24,56480
v_and_b32 v43, 0xffff0000, v45
Mvaa 192,152,196
Ta 144,25,128
v_lshlrev_b32 v44, 16, v46
Mvaa 200,152,212
Ta 146,25,5248
v_and_b32 v45, 0xffff0000, v46
Mvaa 204,156,212
Ta 148,26,160
v_lshlrev_b32 v46, 16, v47
Mvaa 196,156,196
W 7
Ta 150,26,5280
v_and_b32 v47, 0xffff0000, v47
Mvaa 208,128,96
Ta 152,21,41088
v_lshlrev_b32 v48, 16, v52
Mvaa 216,128,112
Ta 154,21,46208
v_and_b32 v49, 0xffff0000, v52
Mvaa 220,132,112
Ta 156,22,41120
v_lshlrev_b32 v50, 16, v53
Mvaa 212,132,96
W 7
Ta 158,22,46240
v_and_b32 v51, 0xffff0000, v53
Mvaa 208,136,100
K 192,192,193
v_lshlrev_b32 v52, 16, v54
Mvaa 216,136,116
Ta 128,21,51328
Ta 130,21,56448
Mvaa 220,140,116
K 193,194,195
v_and_b32 v53, 0xffff0000, v54
Mvaa 212,140,100
W 6
v_lshlrev_b32 v54, 16, v55
K 194,196,197
Mvaa 208,144,104
Ta 132,22,51360
Ta 134,22,56480
Mvaa 216,144,120
v_and_b32 v55, 0xffff0000, v55
K 195,198,199
Mvaa 220,148,120
s_add_u32 s83, s52, 328704
Ta 136,23,10368
v_lshlrev_b32 v56, 16, v60
Mvaa 212,148,104
W 5
Ta 138,23,15488
v_and_b32 v57, 0xffff0000, v60
Mvaa 208,152,108
buffer_store_dwordx4 v[192:195], v1, s[8:11], s83 offen sc1
v_lshlrev_b32 v58, 16, v61
Mvaa 216,152,124
K 200,200,201
K 201,202,203
Mvaa 220,156,124
K 202,204,205
K 203,206,207
Mvaa 212,156,108
Ta 140,24,10400
Ta 142,24,15520
W 4
Mvaa 208,128,184
s_add_u32 s83, s52, 369664
buffer_store_dwordx4 v[200:203], v1, s[8:11], s83 offen sc1
v_and_b32 v59, 0xffff0000, v61
Mvaa 216,128,200
Ta 144,23,30848
v_lshlrev_b32 v60, 16, v62
Mvaa 220,132,200
Ta 146,23,35968
v_and_b32 v61, 0xffff0000, v62
Mvaa 212,132,184
Ta 148,24,30880
Ta 150,24,36000
W 4
Mvaa 208,136,188
v_lshlrev_b32 v62, 16, v63
v_and_b32 v63, 0xffff0000, v63
Mvaa 216,136,204
Ta 152,23,41088
v_lshlrev_b32 v64, 16, v68
Mvaa 220,140,204
Ta 154,23,46208
v_and_b32 v65, 0xffff0000, v68
Mvaa 212,140,188
Ta 156,24,41120
Ta 158,24,46240
W 4
Mvaa 208,144,192
v_lshlrev_b32 v66, 16, v69
v_and_b32 v67, 0xffff0000, v69
Mvaa 216,144,208
Ta 128,23,20672
v_lshlrev_b32 v68, 16, v70
Mvaa 220,148,208
Ta 130,23,25792
v_and_b32 v69, 0xffff0000, v70
Mvaa 212,148,192
Ta 132,24,20704
Ta 134,24,25824
W 4
Mvaa 208,152,196
v_lshlrev_b32 v70, 16, v71
v_and_b32 v71, 0xffff0000, v71
Mvaa 216,152,212
Ta 136,23,51392
v_lshlrev_b32 v72, 16, v76
Mvaa 220,156,212
Ta 138,23,56512
v_and_b32 v73, 0xffff0000, v76
Mvaa 212,156,196
Ta 140,24,51424
Ta 142,24,56544
W 4
Mvaa 224,128,96
v_lshlrev_b32 v74, 16, v77
v_and_b32 v75, 0xffff0000, v77
Mvaa 232,128,112
Ta 144,25,192
v_lshlrev_b32 v76, 16, v78
Mvaa 236,132,112
Ta 146,25,5312
v_and_b32 v77, 0xffff0000, v78
Mvaa 228,132,96
Ta 148,26,224
Ta 150,26,5344
W 4
Mvaa 224,136,100
v_lshlrev_b32 v78, 16, v79
v_and_b32 v79, 0xffff0000, v79
Mvaa 232,136,116
Ta 152,21,41152
Ta 154,21,46272
Mvaa 236,140,116
Ta 156,22,41184
Ta 158,22,46304
Mvaa 228,140,100
W 4
Ta 128,21,51392
K 208,208,209
Mvaa 224,144,104
Ta 130,21,56512
K 209,210,211
Mvaa 232,144,120
Ta 132,22,51424
K 210,212,213
Mvaa 236,148,120
Ta 134,22,56544
K 211,214,215
Mvaa 228,148,104
W 4
Ta 136,23,10432
Ta 138,23,15552
Mvaa 224,152,108
s_add_u32 s83, s52, 329728
buffer_store_dwordx4 v[208:211], v1, s[8:11], s83 offen sc1
K 216,216,217
Mvaa 232,152,124
K 217,218,219
K 218,220,221
)"
R"(Mvaa 236,156,124
Ta 140,24,10464
K 219,222,223
Mvaa 228,156,108
s_add_u32 s83, s52, 370688
Ta 142,24,15584
W 4
Mvaa 224,128,184
buffer_store_dwordx4 v[216:219], v1, s[8:11], s83 offen sc1
Ta 144,23,30912
Mvaa 232,128,200
Ta 146,23,36032
Ta 148,24,30944
Mvaa 236,132,200
Ta 150,24,36064
Ta 152,23,41152
Mvaa 228,132,184
W 5
Ta 154,23,46272
Ta 156,24,41184
Mvaa 224,136,188
Ta 158,24,46304
Ta 128,23,20736
Mvaa 232,136,204
Ta 130,23,25856
Ta 132,24,20768
Mvaa 236,140,204
Ta 134,24,25888
Mvaa 228,140,188
W 8
Ta 136,23,51456
Ta 138,23,56576
Mvaa 224,144,192
Mvaa 232,144,208
Ta 140,24,51488
Ta 142,24,56608
Mvaa 236,148,208
Mvaa 228,148,192
W 8
Ta 144,25,256
Ta 146,25,5376
Mvaa 224,152,196
Mvaa 232,152,212
Ta 148,26,288
Ta 150,26,5408
Mvaa 236,156,212
Mvaa 228,156,196
W 8
Ta 152,21,41216
Ta 154,21,46336
Mvaa 240,128,96
Mvaa 248,128,112
Ta 156,22,41248
Ta 158,22,46368
Mvaa 252,132,112
K 224,224,225
Mvaa 244,132,96
W 8
Ta 128,21,51456
Ta 130,21,56576
Mvaa 240,136,100
K 225,226,227
Mvaa 248,136,116
Ta 132,22,51488
K 226,228,229
Mvaa 252,140,116
Ta 134,22,56608
K 227,230,231
Mvaa 244,140,100
W 8
Ta 136,23,10496
Ta 138,23,15616
Mvaa 240,144,104
s_add_u32 s83, s52, 330752
Mvaa 248,144,120
buffer_store_dwordx4 v[224:227], v1, s[8:11], s83 offen sc1
K 232,232,233
Mvaa 252,148,120
K 233,234,235
K 234,236,237
Mvaa 244,148,104
W 6
Ta 140,24,10528
K 235,238,239
Mvaa 240,152,108
s_add_u32 s83, s52, 371712
Ta 142,24,15648
Ta 144,23,30976
Mvaa 248,152,124
buffer_store_dwordx4 v[232:235], v1, s[8:11], s83 offen sc1
Ta 146,23,36096
Mvaa 252,156,124
Ta 148,24,31008
Ta 150,24,36128
Mvaa 244,156,108
W 8
Ta 152,23,41216
Ta 154,23,46336
Mvaa 240,128,184
Mvaa 248,128,200
Ta 156,24,41248
Ta 158,24,46368
Mvaa 252,132,200
Mvaa 244,132,184
W 8
Mvaa 240,136,188
Mvaa 248,136,204
Mvaa 252,140,204
Mvaa 244,140,188
W 4
Mvaa 240,144,192
Mvaa 248,144,208
Mvaa 252,148,208
Mvaa 244,148,192
W 0
Mvaa 240,152,196
Mvaa 248,152,212
Mvaa 252,156,212
Mvaa 244,156,196
s_nop 4
K 240,240,241
K 241,242,243
s_nop 0
K 242,244,245
K 243,246,247
s_add_u32 s83, s52, 331776
buffer_store_dwordx4 v[240:243], v1, s[8:11], s83 offen sc1
K 248,248,249
K 249,250,251
K 250,252,253
K 251,254,255
s_add_u32 s83, s52, 372736
buffer_store_dwordx4 v[248:251], v1, s[8:11], s83 offen sc1
s_branch .Ltend_77%=
.Llate_76%=:
ds_read_b32 v11, v11 offset:59392
ds_read_b64 v[8:9], v10 offset:57344
Rv 176,3,0
Rv 180,3,1024
Rv 184,3,2048
Rv 188,3,3072
Rv 192,3,4096
Rv 196,3,5120
Rv 200,3,6144
Rv 204,3,7168
W 6
Mvvv 32,224,176
s_lshl_b32 s92, s58, 2
s_add_u32 s91, s92, s51
s_add_u32 s95, s91, 7
Mvvv 36,228,176
s_and_b32 s95, s95, 15
s_mul_i32 s95, s95, 40960
s_mul_i32 s71, s51, 20480
Mvvv 40,224,180
s_add_u32 s71, s71, 61440
s_add_u32 m0, s71, s48
s_lshl2_add_u32 s94, s48, s95
Mvvv 44,228,180
W 4
v_cmp_ne_u32 vcc, 0, v11
s_cmp_lg_u64 vcc, 0
s_cbranch_scc1 .Lredo%=
A 4,4,8
Mvvv 48,224,184
v_rcp_f32 v4, v4
Mvvv 52,228,184
Ta 128,25,10240
A 5,5,9
Mvvv 56,224,188
Ta 130,25,15360
D 16,40,94,0
Mvvv 60,228,188
W 4
s_add_u32 s95, s91, 9
s_and_b32 s95, s95, 15
Mvvv 64,224,192
s_mul_i32 s95, s95, 40960
s_mul_i32 s71, s51, 10240
s_add_u32 s71, s71, 112640
Mvvv 68,228,192
v_rcp_f32 v5, v5
Mvvv 72,224,196
s_lshl_b32 s93, s58, 12
s_add_u32 s93, s93, s50
Ta 132,26,10272
Mvvv 76,228,196
Ta 134,26,15392
W 4
Mvvv 80,224,200
U 32,32,4
U 33,33,4
Mvvv 84,228,200
Ta 136,25,10304
U 34,34,4
Mvvv 88,224,204
U 35,35,4
U 36,36,4
U 37,37,4
U 38,38,4
U 39,39,4
K 32,32,33
K 33,34,35
K 34,36,37
K 35,38,39
buffer_store_dwordx4 v[32:35], v1, s[8:11], s93 offen offset:0 sc1
U 64,64,5
U 65,65,5
U 66,66,5
U 67,67,5
U 68,68,5
U 69,69,5
U 70,70,5
U 71,71,5
K 64,64,65
K 65,66,67
K 66,68,69
K 67,70,71
s_add_u32 s67, s93, 131072
Mvvv 92,228,204
Ta 138,25,15424
Ta 140,26,10336
Ta 142,26,15456
W 4
Zvav 176,128,32
Ta 144,25,10368
Ta 146,25,15488
Zvav 184,128,64
Ta 148,26,10400
Ta 150,26,15520
Zvav 188,132,64
D 17,40,94,1024
Ta 152,25,10432
Zvav 180,132,32
W 5
Ta 154,25,15552
Ta 156,26,10464
Zvav 192,136,32
Ta 158,26,15584
Ta 128,25,10496
Zvav 200,136,64
Ta 130,25,15616
Ta 132,26,10528
Zvav 204,140,64
D 18,40,94,2048
Ta 134,26,15648
Zvav 196,140,32
W 8
Ta 136,25,20480
Ta 138,25,25600
Zvav 208,144,32
Zvav 216,144,64
Ta 140,26,20512
Ta 142,26,25632
Zvav 220,148,64
buffer_store_dwordx4 v[64:67], v1, s[8:11], s67 offen offset:0 sc1
U 40,40,4
Zvav 212,148,32
W 8
U 41,41,4
U 42,42,4
Zvav 224,152,32
U 43,43,4
U 44,44,4
Zvav 232,152,64
U 45,45,4
U 46,46,4
Zvav 236,156,64
D 19,40,94,3072
U 47,47,4
Zvav 228,156,32
W 4
s_add_u32 m0, m0, 4096
Ta 144,25,20544
Zvav 240,128,32
Ta 146,25,25664
K 40,40,41
Zvav 248,128,64
K 41,42,43
K 42,44,45
Zvav 252,132,64
K 43,46,47
buffer_store_dwordx4 v[40:43], v1, s[8:11], s93 offen offset:1024 sc1
U 72,72,5
U 73,73,5
U 74,74,5
U 75,75,5
U 76,76,5
U 77,77,5
U 78,78,5
U 79,79,5
K 72,72,73
K 73,74,75
K 74,76,77
K 75,78,79
Zvav 244,132,32
Ta 148,26,20576
Ta 150,26,25696
W 4
Mvav 176,136,40
Ta 152,25,20608
Ta 154,25,25728
Mvav 184,136,72
Ta 156,26,20640
Ta 158,26,25760
Mvav 188,140,72
buffer_load_dwordx4 v20, s[40:43], s94 offen lds
Ta 128,25,20672
Mvav 180,140,40
W 5
s_add_u32 m0, s71, s48
s_lshl2_add_u32 s94, s48, s95
Mvav 192,144,40
Ta 130,25,25792
Ta 132,26,20704
Mvav 200,144,72
Ta 134,26,25824
Ta 136,25,20736
Mvav 204,148,72
D 16,40,94,0
Ta 138,25,25856
Mvav 196,148,40
W 6
Ta 140,26,20768
Ta 142,26,25888
Mvav 208,152,40
s_waitcnt vmcnt(9)
s_cmp_lg_u32 s70, 0
s_cbranch_scc1 .Lpd_88%=
.Lpdr_89%=:
Ta 144,25,30720
Ta 146,25,35840
Mvav 216,152,72
Ta 148,26,30752
Ta 150,26,35872
Mvav 220,156,72
buffer_store_dwordx4 v[72:75], v1, s[8:11], s67 offen offset:1024 sc1
U 48,48,4
Mvav 212,156,40
W 8
U 49,49,4
U 50,50,4
Mvav 224,128,40
U 51,51,4
U 52,52,4
Mvav 232,128,72
U 53,53,4
U 54,54,4
Mvav 236,132,72
D 17,40,94,1024
U 55,55,4
Mvav 228,132,40
W 4
K 48,48,49
K 49,50,51
Mvav 240,136,40
K 50,52,53
K 51,54,55
Mvav 248,136,72
Ta 152,25,30784
Ta 154,25,35904
Mvav 252,140,72
buffer_store_dwordx4 v[48:51], v1, s[8:11], s93 offen offset:2048 sc1
U 80,80,5
U 81,81,5
U 82,82,5
U 83,83,5
U 84,84,5
U 85,85,5
U 86,86,5
U 87,87,5
K 80,80,81
K 81,82,83
K 82,84,85
K 83,86,87
Mvav 244,140,40
Ta 156,26,30816
Ta 158,26,35936
W 4
Mvav 176,144,48
Ta 128,25,30848
Ta 130,25,35968
Mvav 184,144,80
Ta 132,26,30880
Ta 134,26,36000
Mvav 188,148,80
D 18,40,94,2048
Ta 136,25,30912
Mvav 180,148,48
W 5
Ta 138,25,36032
Ta 140,26,30944
Mvav 192,152,48
Ta 142,26,36064
Ta 144,25,30976
Mvav 200,152,80
Ta 146,25,36096
Ta 148,26,31008
Mvav 204,156,80
buffer_store_dwordx4 v[80:83], v1, s[8:11], s67 offen offset:2048 sc1
)"
R"(U 56,56,4
Mvav 196,156,48
W 7
U 57,57,4
U 58,58,4
Mvav 208,128,48
U 59,59,4
U 60,60,4
Mvav 216,128,80
U 61,61,4
U 62,62,4
Mvav 220,132,80
D 19,40,94,3072
U 63,63,4
Mvav 212,132,48
s_add_u32 m0, m0, 4096
Ta 150,26,36128
W 4
Mvav 224,136,48
K 56,56,57
K 57,58,59
Mvav 232,136,80
K 58,60,61
K 59,62,63
Mvav 236,140,80
buffer_load_dwordx4 v20, s[40:43], s94 offen lds
v_bfe_u32 v28, v1, 4, 2
Mvav 228,140,48
Ta 152,21,0
Ta 154,21,5120
Ta 156,22,32
Ta 158,22,5152
W 4
Mvav 240,144,48
Ta 128,21,64
v_lshlrev_b32 v28, 3, v28
Mvav 248,144,80
buffer_load_dwordx2 v[28:29], v28, s[8:11], s54 offen sc1
s_waitcnt vmcnt(0)
v_cmp_eq_u64 vcc, s[12:13], v[28:29]
s_cmp_eq_u64 vcc, -1
s_cbranch_scc0 .Lspin_90%=
.Lok_91%=:
s_add_u32 s81, s58, 0
Mvav 252,148,80
buffer_store_dwordx4 v[56:59], v1, s[8:11], s93 offen offset:3072 sc1
U 88,88,5
U 89,89,5
U 90,90,5
U 91,91,5
U 92,92,5
U 93,93,5
U 94,94,5
U 95,95,5
K 88,88,89
K 89,90,91
K 90,92,93
K 91,94,95
Mvav 244,148,48
Ta 130,21,5184
Ta 132,22,96
Ta 134,22,5216
W 4
Mvav 176,152,56
s_and_b32 s81, s81, 3
s_lshl_b32 s81, s81, 12
s_add_u32 s81, s81, s49
Mvav 184,152,88
Ta 136,21,128
Ta 138,21,5248
Mvav 188,156,88
buffer_store_dwordx4 v[88:91], v1, s[8:11], s67 offen offset:3072 sc1
Ta 140,22,160
Mvav 180,156,56
Ta 142,22,5280
W 4
Mvav 192,128,56
Ta 144,21,192
Ta 146,21,5312
Mvav 200,128,88
Ta 148,22,224
Ta 150,22,5344
Mvav 204,132,88
buffer_load_dwordx4 a[184:187], v1, s[8:11], s81 offen offset:0 sc1
Ta 152,21,256
Mvav 196,132,56
W 5
s_add_u32 s82, s81, 131072
Ta 154,21,5376
Mvav 208,136,56
Ta 156,22,288
Ta 158,22,5408
Mvav 216,136,88
Ta 128,25,10240
Ta 130,25,15360
Mvav 220,140,88
buffer_load_dwordx4 a[200:203], v1, s[8:11], s82 offen offset:0 sc1
Ta 132,26,10272
Mvav 212,140,56
W 7
Ta 134,26,15392
Ta 136,25,10304
Mvav 224,144,56
Ta 138,25,15424
Mvav 232,144,88
Ta 140,26,10336
Ta 142,26,15456
Mvav 236,148,88
buffer_load_dwordx4 a[188:191], v1, s[8:11], s81 offen offset:1024 sc1
Mvav 228,148,56
W 8
Ta 144,25,10368
Ta 146,25,15488
Mvav 240,152,56
Mvav 248,152,88
Ta 148,26,10400
Ta 150,26,15520
Mvav 252,156,88
buffer_load_dwordx4 a[204:207], v1, s[8:11], s82 offen offset:1024 sc1
Mvav 244,156,56
s_waitcnt vmcnt(2) lgkmcnt(8)
Ta 152,25,10432
Ta 154,25,15552
Zvaa 96,128,184
Zvaa 104,128,200
Ta 156,26,10464
Ta 158,26,15584
Zvaa 108,132,200
buffer_load_dwordx4 a[192:195], v1, s[8:11], s81 offen offset:2048 sc1
Zvaa 100,132,184
W 8
Ta 128,25,10496
Ta 130,25,15616
Zvaa 112,136,184
Zvaa 120,136,200
Ta 132,26,10528
Ta 134,26,15648
Zvaa 124,140,200
buffer_load_dwordx4 a[208:211], v1, s[8:11], s82 offen offset:2048 sc1
Zvaa 116,140,184
W 8
Ta 136,25,20480
Ta 138,25,25600
Zvaa 128,144,184
Zvaa 136,144,200
Ta 140,26,20512
Ta 142,26,25632
Zvaa 140,148,200
buffer_load_dwordx4 a[196:199], v1, s[8:11], s81 offen offset:3072 sc1
Zvaa 132,148,184
W 8
Ta 144,25,20544
Ta 146,25,25664
Zvaa 144,152,184
Zvaa 152,152,200
Ta 148,26,20576
Ta 150,26,25696
Zvaa 156,156,200
buffer_load_dwordx4 a[212:215], v1, s[8:11], s82 offen offset:3072 sc1
Zvaa 148,156,184
W 8
Ta 152,25,20608
Ta 154,25,25728
Zvaa 160,128,184
s_lshl_b32 s80, s58, 3
s_add_u32 s80, s80, s55
Zvaa 168,128,200
Ta 156,26,20640
Ta 158,26,25760
Zvaa 172,132,200
v_mov_b64 v[28:29], s[12:13]
s_mov_b64 exec, 1
buffer_store_dwordx2 v[28:29], off, s[8:11], s80 sc1
s_mov_b64 exec, -1
Zvaa 164,132,184
s_waitcnt vmcnt(5) lgkmcnt(8)
Ta 128,25,20672
Ta 130,25,25792
Mvaa 96,136,188
s_lshl_b32 s92, s58, 2
s_add_u32 s91, s92, s51
s_add_u32 s95, s91, 11
Mvaa 104,136,204
s_and_b32 s95, s95, 15
s_mul_i32 s95, s95, 40960
s_mul_i32 s71, s51, 10240
Mvaa 108,140,204
s_add_u32 s71, s71, 40960
s_add_u32 m0, s71, s48
s_lshl2_add_u32 s94, s48, s95
Mvaa 100,140,188
W 6
Ta 132,26,20704
Ta 134,26,25824
Mvaa 112,144,188
Ta 136,25,20736
Ta 138,25,25856
Mvaa 120,144,204
Ta 140,26,20768
Ta 142,26,25888
Mvaa 124,148,204
Mvaa 116,148,188
W 8
Ta 144,25,30720
Ta 146,25,35840
Mvaa 128,152,188
Mvaa 136,152,204
Ta 148,26,30752
Ta 150,26,35872
Mvaa 140,156,204
Mvaa 132,156,188
W 8
Ta 152,25,30784
Ta 154,25,35904
Mvaa 144,128,188
Mvaa 152,128,204
Ta 156,26,30816
Ta 158,26,35936
Mvaa 156,132,204
s_barrier
D 16,40,94,0
Mvaa 148,132,188
W 8
s_add_u32 s95, s91, 13
s_and_b32 s95, s95, 15
Mvaa 160,136,188
s_mul_i32 s95, s95, 40960
s_mul_i32 s71, s51, 20480
s_add_u32 s71, s71, 71680
Mvaa 168,136,204
Ta 128,25,30848
Ta 130,25,35968
Mvaa 172,140,204
Ta 132,26,30880
Ta 134,26,36000
Mvaa 164,140,188
s_waitcnt vmcnt(4) lgkmcnt(8)
D 17,40,94,1024
Ta 136,25,30912
Mvaa 96,144,192
Ta 138,25,36032
Mvaa 104,144,208
Ta 140,26,30944
Ta 142,26,36064
Mvaa 108,148,208
Mvaa 100,148,192
W 8
D 18,40,94,2048
Ta 144,25,30976
Mvaa 112,152,192
Ta 146,25,36096
Mvaa 120,152,208
Ta 148,26,31008
Ta 150,26,36128
Mvaa 124,156,208
Mvaa 116,156,192
W 8
D 19,40,94,3072
Ta 152,21,0
Mvaa 128,128,192
s_add_u32 m0, m0, 4096
Ta 154,21,5120
Mvaa 136,128,208
Ta 156,22,32
Ta 158,22,5152
Mvaa 140,132,208
Mvaa 132,132,192
W 8
buffer_load_dwordx4 v20, s[40:43], s94 offen lds
Ta 128,21,64
Mvaa 144,136,192
s_add_u32 s81, s58, 1
s_and_b32 s81, s81, 3
s_lshl_b32 s81, s81, 12
Mvaa 152,136,208
s_add_u32 s81, s81, s49
Ta 130,21,5184
Ta 132,22,96
Mvaa 156,140,208
Ta 134,22,5216
Mvaa 148,140,192
W 8
buffer_load_dwordx4 a[216:219], v1, s[8:11], s81 offen offset:0 sc1
Ta 136,21,128
Mvaa 160,144,192
s_add_u32 s82, s81, 131072
Ta 138,21,5248
Mvaa 168,144,208
Ta 140,22,160
Ta 142,22,5280
Mvaa 172,148,208
Mvaa 164,148,192
s_waitcnt vmcnt(7) lgkmcnt(8)
buffer_load_dwordx4 a[232:235], v1, s[8:11], s82 offen offset:0 sc1
Ta 144,21,192
Mvaa 96,152,196
Ta 146,21,5312
Mvaa 104,152,212
Ta 148,22,224
Ta 150,22,5344
Mvaa 108,156,212
Mvaa 100,156,196
W 8
buffer_load_dwordx4 a[220:223], v1, s[8:11], s81 offen offset:1024 sc1
Ta 152,21,256
Mvaa 112,128,196
Ta 154,21,5376
Mvaa 120,128,212
Ta 156,22,288
Ta 158,22,5408
Mvaa 124,132,212
Mvaa 116,132,196
W 8
buffer_load_dwordx4 a[236:239], v1, s[8:11], s82 offen offset:1024 sc1
Ta 128,21,10240
Mvaa 128,136,196
Ta 130,21,15360
Mvaa 136,136,212
Ta 132,22,10272
Ta 134,22,15392
Mvaa 140,140,212
Mvaa 132,140,196
W 8
buffer_load_dwordx4 a[224:227], v1, s[8:11], s81 offen offset:2048 sc1
Ta 136,21,10304
Mvaa 144,144,196
Ta 138,21,15424
Mvaa 152,144,212
Ta 140,22,10336
Ta 142,22,15456
Mvaa 156,148,212
Mvaa 148,148,196
W 8
buffer_load_dwordx4 a[240:243], v1, s[8:11], s82 offen offset:2048 sc1
Ta 144,21,10368
Mvaa 160,152,196
Ta 146,21,15488
Mvaa 168,152,212
Ta 148,22,10400
Ta 150,22,15520
Mvaa 172,156,212
Mvaa 164,156,196
s_waitcnt vmcnt(4) lgkmcnt(8)
buffer_load_dwordx4 a[228:231], v1, s[8:11], s81 offen offset:3072 sc1
Ta 152,21,10432
Mvaa 96,128,216
Ta 154,21,15552
Mvaa 104,128,232
Ta 156,22,10464
Ta 158,22,15584
Mvaa 108,132,232
Mvaa 100,132,216
W 8
buffer_load_dwordx4 a[244:247], v1, s[8:11], s82 offen offset:3072 sc1
Ta 128,21,10496
Mvaa 112,136,216
s_add_u32 m0, s71, s48
s_lshl2_add_u32 s94, s48, s95
Ta 130,21,15616
Mvaa 120,136,232
Ta 132,22,10528
Ta 134,22,15648
Mvaa 124,140,232
Mvaa 116,140,216
W 8
D 16,40,94,0
Ta 136,21,20480
Mvaa 128,144,216
s_add_u32 s95, s92, 15
s_and_b32 s95, s95, 15
s_mul_i32 s95, s95, 40960
Mvaa 136,144,232
s_mul_i32 s71, s51, 0
s_add_u32 s71, s71, 102400
Ta 138,21,25600
Mvaa 140,148,232
Ta 140,22,20512
Ta 142,22,25632
Mvaa 132,148,216
W 8
D 17,40,94,1024
Ta 144,21,20544
Mvaa 144,152,216
Ta 146,21,25664
Mvaa 152,152,232
Ta 148,22,20576
Ta 150,22,25696
Mvaa 156,156,232
Mvaa 148,156,216
W 8
D 18,40,94,2048
Ta 152,21,20608
Mvaa 160,128,216
Ta 154,21,25728
Mvaa 168,128,232
Ta 156,22,20640
Ta 158,22,25760
Mvaa 172,132,232
Mvaa 164,132,216
s_waitcnt vmcnt(7) lgkmcnt(8)
)"
R"(D 19,40,94,3072
Ta 128,21,20672
Mvaa 96,136,220
s_add_u32 m0, m0, 4096
Ta 130,21,25792
Mvaa 104,136,236
Ta 132,22,20704
Ta 134,22,25824
Mvaa 108,140,236
Mvaa 100,140,220
W 8
buffer_load_dwordx4 v20, s[40:43], s94 offen lds
Ta 136,21,20736
Mvaa 112,144,220
s_add_u32 m0, s71, s48
s_lshl2_add_u32 s94, s48, s95
Ta 138,21,25856
Mvaa 120,144,236
Ta 140,22,20768
Ta 142,22,25888
Mvaa 124,148,236
Mvaa 116,148,220
W 8
D 16,40,94,0
Ta 144,21,30720
Mvaa 128,152,220
Ta 146,21,35840
Mvaa 136,152,236
Ta 148,22,30752
Ta 150,22,35872
Mvaa 140,156,236
Mvaa 132,156,220
W 8
D 17,40,94,1024
Ta 152,21,30784
Mvaa 144,128,220
Ta 154,21,35904
Mvaa 152,128,236
Ta 156,22,30816
Ta 158,22,35936
Mvaa 156,132,236
D 18,40,94,2048
D 19,40,94,3072
s_add_u32 m0, m0, 4096
s_nop 0
buffer_load_dwordx4 v20, s[40:43], s94 offen lds
Mvaa 148,132,220
W 8
s_add_u32 s81, s58, 2
s_and_b32 s81, s81, 3
Mvaa 160,136,220
s_lshl_b32 s81, s81, 12
s_add_u32 s81, s81, s49
Ta 128,21,30848
Mvaa 168,136,236
Ta 130,21,35968
Ta 132,22,30880
Mvaa 172,140,236
Ta 134,22,36000
Mvaa 164,140,220
s_waitcnt vmcnt(12) lgkmcnt(8)
Ta 136,21,30912
Ta 138,21,36032
Mvaa 96,144,224
s_barrier
buffer_load_dwordx4 a[0:3], v1, s[8:11], s81 offen offset:0 sc1
v_and_b32 v27, 1, v12
Mvaa 104,144,240
s_add_u32 s82, s81, 131072
Ta 140,22,30944
Ta 142,22,36064
Mvaa 108,148,240
Mvaa 100,148,224
W 8
Ta 144,21,30976
v_lshlrev_b32 v27, 1, v27
Mvaa 112,152,224
Ta 146,21,36096
buffer_load_dwordx4 a[16:19], v1, s[8:11], s82 offen offset:0 sc1
Mvaa 120,152,240
Ta 148,22,31008
Ta 150,22,36128
Mvaa 124,156,240
v_xor_b32 v27, v27, v13
Mvaa 116,156,224
W 8
Ta 152,23,0
Ta 154,23,5120
Mvaa 128,128,224
buffer_load_dwordx4 a[4:7], v1, s[8:11], s81 offen offset:1024 sc1
Mvaa 136,128,240
Ta 156,24,32
v_and_b32 v12, 2, v12
Mvaa 140,132,240
Ta 158,24,5152
Mvaa 132,132,224
W 8
Ta 128,23,64
Ta 130,23,5184
Mvaa 144,136,224
buffer_load_dwordx4 a[20:23], v1, s[8:11], s82 offen offset:1024 sc1
v_or_b32 v12, v12, v14
Mvaa 152,136,240
Ta 132,24,96
Ta 134,24,5216
Mvaa 156,140,240
Mvaa 148,140,224
W 8
Ta 136,23,128
v_lshl_or_b32 v12, v27, 2, v12
Mvaa 160,144,224
Ta 138,23,5248
buffer_load_dwordx4 a[8:11], v1, s[8:11], s81 offen offset:2048 sc1
Mvaa 168,144,240
Ta 140,24,160
Ta 142,24,5280
Mvaa 172,148,240
v_cmp_eq_u32_e64 s[86:87], 0, v12
Mvaa 164,148,224
s_waitcnt vmcnt(15) lgkmcnt(8)
Ta 144,23,192
Ta 146,23,5312
Mvaa 96,152,228
buffer_load_dwordx4 a[24:27], v1, s[8:11], s82 offen offset:2048 sc1
Mvaa 104,152,244
Ta 148,24,224
v_cndmask_b32_e64 v4, 0, v15, s[86:87]
Mvaa 108,156,244
Ta 150,24,5344
Mvaa 100,156,228
W 8
Ta 152,23,256
Ta 154,23,5376
Mvaa 112,128,228
buffer_load_dwordx4 a[12:15], v1, s[8:11], s81 offen offset:3072 sc1
v_cmp_eq_u32_e64 s[86:87], 1, v12
Mvaa 120,128,244
Ta 156,24,288
Ta 158,24,5408
Mvaa 124,132,244
v_cndmask_b32_e64 v5, 0, v15, s[86:87]
Mvaa 116,132,228
W 8
Ta 128,23,20480
Ta 130,23,25600
Mvaa 128,136,228
buffer_load_dwordx4 a[28:31], v1, s[8:11], s82 offen offset:3072 sc1
Mvaa 136,136,244
s_add_u32 s81, s58, 3
s_and_b32 s81, s81, 3
s_lshl_b32 s81, s81, 12
Mvaa 140,140,244
s_add_u32 s81, s81, s49
Ta 132,24,20512
v_cmp_eq_u32_e64 s[86:87], 2, v12
Mvaa 132,140,228
W 7
Ta 134,24,25632
Ta 136,23,20544
Mvaa 144,144,228
Ta 138,23,25664
v_cndmask_b32_e64 v6, 0, v15, s[86:87]
Mvaa 152,144,244
buffer_load_dwordx4 a[32:35], v1, s[8:11], s81 offen offset:0 sc1
Ta 140,24,20576
Mvaa 156,148,244
s_add_u32 s82, s81, 131072
Ta 142,24,25696
Mvaa 148,148,228
W 8
Ta 144,23,20608
v_cmp_eq_u32_e64 s[86:87], 3, v12
Mvaa 160,152,228
Ta 146,23,25728
Mvaa 168,152,244
buffer_load_dwordx4 a[48:51], v1, s[8:11], s82 offen offset:0 sc1
Ta 148,24,20640
Mvaa 172,156,244
s_waitcnt vmcnt(10)
s_barrier
Ta 150,24,25760
Mvaa 164,156,228
s_waitcnt vmcnt(8) lgkmcnt(8)
Ta 152,23,20672
v_cndmask_b32_e64 v7, 0, v15, s[86:87]
Mvaa 96,128,0
Ta 154,23,25792
Mvaa 104,128,16
buffer_load_dwordx4 a[36:39], v1, s[8:11], s81 offen offset:1024 sc1
v_cmp_eq_u32_e64 s[86:87], 4, v12
Mvaa 108,132,16
Ta 156,24,20704
Ta 158,24,25824
Mvaa 100,132,0
W 8
Ta 128,23,20736
Ta 130,23,25856
Mvaa 112,136,0
v_cndmask_b32_e64 v8, 0, v15, s[86:87]
Mvaa 120,136,16
buffer_load_dwordx4 a[52:55], v1, s[8:11], s82 offen offset:1024 sc1
Ta 132,24,20768
Mvaa 124,140,16
Ta 134,24,25888
Mvaa 116,140,0
W 8
Ta 136,23,51200
v_cmp_eq_u32_e64 s[86:87], 5, v12
Mvaa 128,144,0
Ta 138,23,56320
Mvaa 136,144,16
buffer_load_dwordx4 a[40:43], v1, s[8:11], s81 offen offset:2048 sc1
Ta 140,24,51232
Mvaa 140,148,16
Ta 142,24,56352
v_cndmask_b32_e64 v9, 0, v15, s[86:87]
Mvaa 132,148,0
W 8
Ta 144,23,51264
Ta 146,23,56384
Mvaa 144,152,0
v_cmp_eq_u32_e64 s[86:87], 6, v12
Mvaa 152,152,16
buffer_load_dwordx4 a[56:59], v1, s[8:11], s82 offen offset:2048 sc1
Ta 148,24,51296
Mvaa 156,156,16
Ta 150,24,56416
Mvaa 148,156,0
W 8
Ta 152,23,51328
v_cndmask_b32_e64 v10, 0, v15, s[86:87]
Mvaa 160,128,0
Ta 154,23,56448
Mvaa 168,128,16
buffer_load_dwordx4 a[44:47], v1, s[8:11], s81 offen offset:3072 sc1
Ta 156,24,51360
Mvaa 172,132,16
Ta 158,24,56480
v_cmp_eq_u32_e64 s[86:87], 7, v12
Mvaa 164,132,0
s_waitcnt vmcnt(11) lgkmcnt(8)
Ta 128,23,51392
Ta 130,23,56512
Mvaa 96,136,4
Mvaa 104,136,20
buffer_load_dwordx4 a[60:63], v1, s[8:11], s82 offen offset:3072 sc1
v_cndmask_b32_e64 v11, 0, v15, s[86:87]
Mvaa 108,140,20
Ta 132,24,51424
Ta 134,24,56544
Mvaa 100,140,4
W 8
Ta 136,23,51456
Ta 138,23,56576
Mvaa 112,144,4
s_mov_b32 s20, s38
Mvaa 120,144,20
Ta 140,24,51488
Ta 142,24,56608
Mvaa 124,148,20
Mvaa 116,148,4
W 8
s_and_b32 s21, s39, 0xffff
Ta 144,25,0
Mvaa 128,152,4
Ta 146,25,5120
v_bfe_u32 v28, v1, 4, 2
Mvaa 136,152,20
Ta 148,26,32
v_lshlrev_b32 v28, 3, v28
Mvaa 140,156,20
s_mov_b32 s22, 2621440
Ta 150,26,5152
buffer_load_dwordx2 v[28:29], v28, s[8:11], s55 offen sc1
Mvaa 132,156,4
W 8
Ta 152,25,64
Ta 154,25,5184
Mvaa 144,128,4
Mvaa 152,128,20
s_mov_b32 s23, 0x20000
Ta 156,26,96
Ta 158,26,5216
Mvaa 156,132,20
Mvaa 148,132,4
W 8
Ta 128,25,128
Ta 130,25,5248
Mvaa 160,136,4
s_and_b32 s17, s17, 0xffff
Mvaa 168,136,20
Ta 132,26,160
Ta 134,26,5280
Mvaa 172,140,20
s_mov_b32 s18, 2621440
Mvaa 164,140,4
s_waitcnt vmcnt(11) lgkmcnt(8)
Ta 136,25,192
Ta 138,25,5312
Mvaa 96,144,8
Mvaa 104,144,24
s_mov_b32 s19, 0x20000
Ta 140,26,224
Ta 142,26,5344
Mvaa 108,148,24
Mvaa 100,148,8
W 8
Ta 144,25,256
Ta 146,25,5376
Mvaa 112,152,8
s_lshr_b32 s66, s15, 3
Mvaa 120,152,24
Ta 148,26,288
Ta 150,26,5408
Mvaa 124,156,24
Mvaa 116,156,8
W 8
s_lshl_b32 s66, s66, 4
Ta 152,21,40960
Mvaa 128,128,8
Ta 154,21,46080
Mvaa 136,128,24
Ta 156,22,40992
Ta 158,22,46112
Mvaa 140,132,24
s_add_u32 s66, s66, s59
Mvaa 132,132,8
W 8
Ta 128,21,41024
Ta 130,21,46144
Mvaa 144,136,8
Mvaa 152,136,24
s_lshl_b32 s63, s66, 6
Ta 132,22,41056
Ta 134,22,46176
Mvaa 156,140,24
Mvaa 148,140,8
W 8
Ta 136,21,41088
Ta 138,21,46208
Mvaa 160,144,8
s_add_u32 s63, s63, 65536
Mvaa 168,144,24
Ta 140,22,41120
Ta 142,22,46240
Mvaa 172,148,24
Mvaa 164,148,8
s_waitcnt vmcnt(9) lgkmcnt(8)
s_mul_i32 s62, s66, 40960
Ta 144,21,41152
Mvaa 96,152,12
Ta 146,21,46272
Mvaa 104,152,28
Ta 148,22,41184
Ta 150,22,46304
Mvaa 108,156,28
s_add_u32 s62, s62, 56623104
Mvaa 100,156,12
W 8
Ta 152,21,41216
Ta 154,21,46336
Mvaa 112,128,12
s_lshl_b32 s66, s14, 10
Mvaa 120,128,28
Ta 156,22,41248
Ta 158,22,46368
Mvaa 124,132,28
Mvaa 116,132,12
W 8
s_add_u32 s78, s62, s66
Ta 128,21,51200
Mvaa 128,136,12
Ta 130,21,56320
Mvaa 136,136,28
Ta 132,22,51232
Ta 134,22,56352
Mvaa 140,140,28
s_add_u32 s79, s78, 327680
s_waitcnt vmcnt(0)
v_cmp_eq_u64 vcc, s[12:13], v[28:29]
s_cmp_eq_u64 vcc, -1
s_cbranch_scc0 .Lspin_93%=
.Lok_94%=:
s_add_u32 s81, s58, 1
Mvaa 132,140,12
W 8
s_and_b32 s81, s81, 3
s_lshl_b32 s81, s81, 12
Mvaa 144,144,12
s_add_u32 s81, s81, s50
buffer_load_dwordx4 a[64:67], v1, s[8:11], s81 offen offset:0 sc1
Ta 136,21,51264
Mvaa 152,144,28
s_add_u32 s82, s81, 131072
s_and_b32 s66, s14, 1
Ta 138,21,56384
Mvaa 156,148,28
Ta 140,22,51296
Ta 142,22,56416
Mvaa 148,148,12
)"
R"(W 8
Ta 144,21,51328
Ta 146,21,56448
Mvaa 160,152,12
s_mul_i32 s66, s66, 327680
buffer_load_dwordx4 a[80:83], v1, s[8:11], s82 offen offset:0 sc1
Mvaa 168,152,28
Ta 148,22,51360
Ta 150,22,56480
Mvaa 172,156,28
Mvaa 164,156,12
W 8
s_add_u32 s65, s62, s66
Ta 152,21,51392
Mvaa 96,128,32
Ta 154,21,56512
Mvaa 104,128,48
buffer_load_dwordx4 a[68:71], v1, s[8:11], s81 offen offset:1024 sc1
Ta 156,22,51424
Mvaa 108,132,48
s_add_u32 s65, s65, 4096
Ta 158,22,56544
Mvaa 100,132,32
W 8
Ta 128,21,51456
Ta 130,21,56576
Mvaa 112,136,32
Mvaa 120,136,48
s_mov_b32 s64, 0
Ta 132,22,51488
Ta 134,22,56608
Mvaa 124,140,48
buffer_load_dwordx4 a[84:87], v1, s[8:11], s82 offen offset:1024 sc1
Mvaa 116,140,32
W 8
Ta 136,23,10240
Ta 138,23,15360
Mvaa 128,144,32
v_bfe_u32 v27, v1, 4, 3
Mvaa 136,144,48
Ta 140,24,10272
Ta 142,24,15392
Mvaa 140,148,48
v_lshlrev_b32 v27, 3, v27
Mvaa 132,148,32
W 8
buffer_load_dwordx4 a[72:75], v1, s[8:11], s81 offen offset:2048 sc1
Ta 144,23,10304
Mvaa 144,152,32
Ta 146,23,15424
Mvaa 152,152,48
Ta 148,24,10336
v_bfe_u32 v20, v1, 4, 4
Mvaa 156,156,48
Ta 150,24,15456
Mvaa 148,156,32
W 8
Ta 152,23,10368
Ta 154,23,15488
Mvaa 160,128,32
buffer_load_dwordx4 a[88:91], v1, s[8:11], s82 offen offset:2048 sc1
v_mul_u32_u24 v20, 1280, v20
Mvaa 168,128,48
Ta 156,24,10400
Ta 158,24,15520
Mvaa 172,132,48
Mvaa 164,132,32
W 8
Ta 128,23,10432
v_lshrrev_b32 v12, 8, v1
Mvaa 96,136,36
Ta 130,23,15552
Mvaa 104,136,52
buffer_load_dwordx4 a[76:79], v1, s[8:11], s81 offen offset:3072 sc1
Ta 132,24,10464
Mvaa 108,140,52
Ta 134,24,15584
v_lshl_add_u32 v20, v12, 4, v20
Mvaa 100,140,36
W 8
Ta 136,23,10496
Ta 138,23,15616
Mvaa 112,144,36
Mvaa 120,144,52
s_bfe_u32 s66, s59, 0x10001
Ta 140,24,10528
Ta 142,24,15648
Mvaa 124,148,52
buffer_load_dwordx4 a[92:95], v1, s[8:11], s82 offen offset:3072 sc1
Mvaa 116,148,36
W 8
s_add_u32 s81, s58, 2
s_and_b32 s81, s81, 3
Mvaa 128,152,36
s_lshl_b32 s81, s81, 12
s_add_u32 s81, s81, s50
Ta 144,23,30720
Mvaa 136,152,52
s_lshl_b32 s67, s57, 1
Ta 146,23,35840
Ta 148,24,30752
Mvaa 140,156,52
Ta 150,24,35872
buffer_load_dwordx4 a[96:99], v1, s[8:11], s81 offen offset:0 sc1
Mvaa 132,156,36
W 8
s_add_u32 s82, s81, 131072
s_add_u32 s66, s66, s67
Mvaa 144,128,36
Ta 152,23,30784
Ta 154,23,35904
Mvaa 152,128,52
Ta 156,24,30816
Ta 158,24,35936
Mvaa 156,132,52
s_lshl_b32 s61, s66, 2
Mvaa 148,132,36
W 8
Ta 128,23,30848
Ta 130,23,35968
Mvaa 160,136,36
s_lshr_b32 s66, s59, 2
buffer_load_dwordx4 a[112:115], v1, s[8:11], s82 offen offset:0 sc1
Mvaa 168,136,52
Ta 132,24,30880
Ta 134,24,36000
Mvaa 172,140,52
Mvaa 164,140,36
W 8
s_lshl_b32 s66, s66, 1
Ta 136,23,30912
Mvaa 96,144,40
Ta 138,23,36032
Mvaa 104,144,56
buffer_load_dwordx4 a[100:103], v1, s[8:11], s81 offen offset:1024 sc1
Ta 140,24,30944
Mvaa 108,148,56
s_add_u32 s61, s61, s66
Ta 142,24,36064
Mvaa 100,148,40
W 8
Ta 144,23,30976
Ta 146,23,36096
Mvaa 112,152,40
Mvaa 120,152,56
s_and_b32 s66, s59, 1
Ta 148,24,31008
Ta 150,24,36128
Mvaa 124,156,56
buffer_load_dwordx4 a[116:119], v1, s[8:11], s82 offen offset:1024 sc1
Mvaa 116,156,40
W 8
Ta 152,23,40960
Ta 154,23,46080
Mvaa 128,128,40
s_add_u32 s61, s61, s66
Mvaa 136,128,56
Ta 156,24,40992
Ta 158,24,46112
Mvaa 140,132,56
Mvaa 132,132,40
W 8
s_xor_b32 s67, s61, 0
buffer_load_dwordx4 a[104:107], v1, s[8:11], s81 offen offset:2048 sc1
Mvaa 144,136,40
Ta 128,23,41024
Ta 130,23,46144
Mvaa 152,136,56
Ta 132,24,41056
Ta 134,24,46176
Mvaa 156,140,56
s_mul_i32 s72, s67, 20480
Mvaa 148,140,40
W 8
Ta 136,23,41088
Ta 138,23,46208
Mvaa 160,144,40
buffer_load_dwordx4 a[120:123], v1, s[8:11], s82 offen offset:2048 sc1
Mvaa 168,144,56
s_mul_i32 s66, s58, 320
Ta 140,24,41120
Ta 142,24,46240
Mvaa 172,148,56
Mvaa 164,148,40
W 8
Ta 144,23,41152
Ta 146,23,46272
Mvaa 96,152,44
s_add_u32 s72, s72, s66
Mvaa 104,152,60
buffer_load_dwordx4 a[108:111], v1, s[8:11], s81 offen offset:3072 sc1
Ta 148,24,41184
Mvaa 108,156,60
s_mul_i32 s75, s67, 20480
Ta 150,24,46304
Mvaa 100,156,44
W 8
Ta 152,23,41216
Ta 154,23,46336
Mvaa 112,128,44
Mvaa 120,128,60
s_mul_i32 s66, s58, 2560
Ta 156,24,41248
Ta 158,24,46368
Mvaa 124,132,60
buffer_load_dwordx4 a[124:127], v1, s[8:11], s82 offen offset:3072 sc1
Mvaa 116,132,44
W 8
s_add_u32 s81, s58, 3
s_and_b32 s81, s81, 3
Mvaa 128,136,44
s_lshl_b32 s81, s81, 12
s_add_u32 s81, s81, s50
Ta 128,21,10240
Mvaa 136,136,60
s_add_u32 s75, s75, s66
Ta 130,21,15360
Ta 132,22,10272
Mvaa 140,140,60
Ta 134,22,15392
buffer_load_dwordx4 a[184:187], v1, s[8:11], s81 offen offset:0 sc1
Mvaa 132,140,44
W 8
s_add_u32 s82, s81, 131072
K 96,96,97
Mvaa 144,144,44
s_add_u32 s75, s75, 131072
Ta 136,21,10304
Ta 138,21,15424
Mvaa 152,144,60
Ta 140,22,10336
K 97,98,99
Mvaa 156,148,60
s_xor_b32 s67, s61, 124
Ta 142,22,15456
K 98,100,101
Mvaa 148,148,44
W 8
Ta 144,21,10368
Ta 146,21,15488
Mvaa 160,152,44
buffer_load_dwordx4 a[200:203], v1, s[8:11], s82 offen offset:0 sc1
K 99,102,103
Mvaa 168,152,60
s_mul_i32 s73, s67, 20480
s_add_u32 s83, s52, 0
Ta 148,22,10400
Mvaa 172,156,60
Ta 150,22,15520
Mvaa 164,156,44
s_waitcnt vmcnt(16) lgkmcnt(8)
Ta 152,21,10432
Ta 154,21,15552
Mvaa 176,128,64
s_mul_i32 s66, s58, 320
buffer_store_dwordx4 v[96:99], v1, s[8:11], s83 offen sc1
K 104,104,105
Mvaa 184,128,80
Ta 156,22,10464
Ta 158,22,15584
Mvaa 188,132,80
K 105,106,107
Mvaa 180,132,64
W 8
s_add_u32 s73, s73, s66
Ta 128,21,10496
Mvaa 192,136,64
buffer_load_dwordx4 a[188:191], v1, s[8:11], s81 offen offset:1024 sc1
Ta 130,21,15616
Mvaa 200,136,80
Ta 132,22,10528
K 106,108,109
Mvaa 204,140,80
s_mul_i32 s76, s67, 20480
Ta 134,22,15648
K 107,110,111
Mvaa 196,140,64
W 8
s_add_u32 s83, s52, 40960
Ta 136,21,20480
Mvaa 208,144,64
s_mul_i32 s66, s58, 2560
buffer_load_dwordx4 a[204:207], v1, s[8:11], s82 offen offset:1024 sc1
Ta 138,21,25600
Mvaa 216,144,80
Ta 140,22,20512
Ta 142,22,25632
Mvaa 220,148,80
Mvaa 212,148,64
W 8
s_add_u32 s76, s76, s66
Ta 144,21,20544
Mvaa 224,152,64
buffer_store_dwordx4 v[104:107], v1, s[8:11], s83 offen sc1
K 112,112,113
Mvaa 232,152,80
Ta 146,21,25664
K 113,114,115
Mvaa 236,156,80
s_add_u32 s76, s76, 131072
Ta 148,22,20576
Ta 150,22,25696
Mvaa 228,156,64
W 8
Ta 152,21,20608
K 114,116,117
Mvaa 240,128,64
buffer_load_dwordx4 a[192:195], v1, s[8:11], s81 offen offset:2048 sc1
Ta 154,21,25728
Mvaa 248,128,80
K 115,118,119
v_bfe_u32 v15, v1, 7, 1
Mvaa 252,132,80
s_add_u32 s83, s52, 1024
Ta 156,22,20640
Ta 158,22,25760
Mvaa 244,132,64
s_waitcnt vmcnt(19) lgkmcnt(8)
Ta 128,21,20672
Ta 130,21,25792
Mvaa 176,136,68
buffer_load_dwordx4 a[208:211], v1, s[8:11], s82 offen offset:2048 sc1
v_mul_u32_u24 v14, 10240, v15
Mvaa 184,136,84
Ta 132,22,20704
Ta 134,22,25824
Mvaa 188,140,84
Mvaa 180,140,68
W 8
Ta 136,21,20736
v_lshrrev_b32 v15, 8, v1
Mvaa 192,144,68
buffer_store_dwordx4 v[112:115], v1, s[8:11], s83 offen sc1
K 120,120,121
Mvaa 200,144,84
Ta 138,21,25856
K 121,122,123
Mvaa 204,148,84
Ta 140,22,20768
K 122,124,125
Mvaa 196,148,68
W 7
Ta 142,22,25888
K 123,126,127
Mvaa 208,152,68
s_add_u32 s83, s52, 41984
buffer_load_dwordx4 a[196:199], v1, s[8:11], s81 offen offset:3072 sc1
v_lshl_add_u32 v14, v15, 7, v14
Mvaa 216,152,84
Ta 144,21,30720
Ta 146,21,35840
Mvaa 220,156,84
Ta 148,22,30752
v_and_b32 v15, 0x70, v1
Mvaa 212,156,68
W 7
Ta 150,22,35872
Ta 152,21,30784
Mvaa 224,128,68
buffer_load_dwordx4 a[212:215], v1, s[8:11], s82 offen offset:3072 sc1
Ta 154,21,35904
Mvaa 232,128,84
Ta 156,22,30816
v_add_u32 v14, v14, v15
Mvaa 236,132,84
s_bitcmp1_b32 s14, 0
s_cselect_b32 s74, s73, s72
s_cselect_b32 s77, s76, s75
Ta 158,22,35936
Mvaa 228,132,68
W 8
Ta 128,21,30848
Ta 130,21,35968
Mvaa 240,136,68
buffer_store_dwordx4 v[120:123], v1, s[8:11], s83 offen sc1
K 128,128,129
Mvaa 248,136,84
s_add_u32 s74, s74, 256
K 129,130,131
K 130,132,133
Mvaa 252,140,84
Ta 132,22,30880
K 131,134,135
Mvaa 244,140,68
s_waitcnt vmcnt(22) lgkmcnt(7)
s_add_u32 s83, s52, 2048
Ta 134,22,36000
Mvaa 176,144,72
s_add_u32 s77, s77, 2048
buffer_store_dwordx4 v[128:131], v1, s[8:11], s83 offen sc1
Ta 136,21,30912
Mvaa 184,144,88
Ta 138,21,36032
K 136,136,137
Mvaa 188,148,88
Ta 140,22,30944
Ta 142,22,36064
Mvaa 180,148,72
W 8
s_lshl_b32 s66, s14, 6
)"
R"(K 137,138,139
Mvaa 192,152,72
Ta 144,21,30976
Ta 146,21,36096
Mvaa 200,152,88
Ta 148,22,31008
K 138,140,141
Mvaa 204,156,88
s_lshl_b32 s67, s14, 9
Ta 150,22,36128
K 139,142,143
Mvaa 196,156,72
W 8
s_add_u32 s83, s52, 43008
Ta 152,23,0
Mvaa 208,128,72
Ta 154,23,5120
Mvaa 216,128,88
s_add_u32 s72, s72, s66
buffer_store_dwordx4 v[136:139], v1, s[8:11], s83 offen sc1
Ta 156,24,32
Mvaa 220,132,88
Ta 158,24,5152
K 144,144,145
Mvaa 212,132,72
W 8
Ta 128,23,64
Ta 130,23,5184
Mvaa 224,136,72
s_add_u32 s75, s75, s67
K 145,146,147
Mvaa 232,136,88
Ta 132,24,96
Ta 134,24,5216
Mvaa 236,140,88
K 146,148,149
Mvaa 228,140,72
W 8
s_add_u32 s73, s73, s66
Ta 136,23,128
Mvaa 240,144,72
Ta 138,23,5248
K 147,150,151
Mvaa 248,144,88
s_add_u32 s83, s52, 3072
Ta 140,24,160
Ta 142,24,5280
Mvaa 252,148,88
s_add_u32 s76, s76, s67
Mvaa 244,148,72
s_waitcnt vmcnt(22) lgkmcnt(8)
buffer_store_dwordx4 v[144:147], v1, s[8:11], s83 offen sc1
Ta 144,23,192
Mvaa 176,152,76
Ta 146,23,5312
K 152,152,153
Mvaa 184,152,92
Ta 148,24,224
Ta 150,24,5344
Mvaa 188,156,92
K 153,154,155
Mvaa 180,156,76
W 8
buffer_load_dwordx4 v[36:39], v20, s[16:19], s72 offen
Ta 152,23,256
Mvaa 192,128,76
Ta 154,23,5376
K 154,156,157
Mvaa 200,128,92
Ta 156,24,288
Ta 158,24,5408
Mvaa 204,132,92
K 155,158,159
Mvaa 196,132,76
W 8
s_add_u32 s83, s52, 44032
buffer_load_dwordx4 v[44:47], v14, s[8:11], s75 offen sc1
Mvaa 208,136,76
Ta 128,23,20480
Ta 130,23,25600
Mvaa 216,136,92
Ta 132,24,20512
Ta 134,24,25632
Mvaa 220,140,92
Mvaa 212,140,76
W 8
buffer_store_dwordx4 v[152:155], v1, s[8:11], s83 offen sc1
K 160,160,161
Mvaa 224,144,76
Ta 136,23,51200
Ta 138,23,56320
Mvaa 232,144,92
Ta 140,24,51232
K 161,162,163
Mvaa 236,148,92
Ta 142,24,56352
K 162,164,165
Mvaa 228,148,76
W 8
buffer_load_dwordx4 v[52:55], v20, s[16:19], s73 offen
Ta 144,25,0
Mvaa 240,152,76
Ta 146,25,5120
K 163,166,167
Mvaa 248,152,92
s_add_u32 s83, s52, 4096
Ta 148,26,32
Ta 150,26,5152
Mvaa 252,156,92
Mvaa 244,156,76
s_waitcnt vmcnt(25) lgkmcnt(8)
buffer_store_dwordx4 v[160:163], v1, s[8:11], s83 offen sc1
Ta 152,21,40960
Mvaa 176,128,96
Ta 154,21,46080
K 168,168,169
Mvaa 184,128,112
Ta 156,22,40992
Ta 158,22,46112
Mvaa 188,132,112
K 169,170,171
Mvaa 180,132,96
s_waitcnt vmcnt(24) lgkmcnt(8)
buffer_load_dwordx4 v[60:63], v14, s[8:11], s76 offen sc1
Ta 128,21,51200
Mvaa 176,136,100
Ta 130,21,56320
K 170,172,173
Mvaa 184,136,116
Ta 132,22,51232
Ta 134,22,56352
Mvaa 188,140,116
K 171,174,175
Mvaa 180,140,100
s_waitcnt vmcnt(23) lgkmcnt(8)
s_add_u32 s83, s52, 45056
buffer_load_dwordx4 v[68:71], v20, s[16:19], s74 offen
Mvaa 176,144,104
Ta 136,23,10240
Ta 138,23,15360
Mvaa 184,144,120
Ta 140,24,10272
Ta 142,24,15392
Mvaa 188,148,120
Mvaa 180,148,104
s_waitcnt vmcnt(22) lgkmcnt(8)
buffer_store_dwordx4 v[168:171], v1, s[8:11], s83 offen sc1
Ta 144,23,30720
Mvaa 176,152,108
Ta 146,23,35840
Mvaa 184,152,124
Ta 148,24,30752
Ta 150,24,35872
Mvaa 188,156,124
Mvaa 180,156,108
s_waitcnt vmcnt(21) lgkmcnt(8)
buffer_load_dwordx4 v[76:79], v14, s[8:11], s77 offen sc1
Ta 152,23,40960
Mvaa 176,128,184
Ta 154,23,46080
Mvaa 184,128,200
Ta 156,24,40992
Ta 158,24,46112
Mvaa 188,132,200
Mvaa 180,132,184
s_waitcnt vmcnt(19) lgkmcnt(8)
Ta 128,23,20544
Ta 130,23,25664
Mvaa 176,136,188
buffer_load_dwordx2 v[12:13], v27, s[8:11], 0 offen offset:3584 sc1
Mvaa 184,136,204
Ta 132,24,20576
Ta 134,24,25696
Mvaa 188,140,204
Mvaa 180,140,188
s_waitcnt vmcnt(17) lgkmcnt(8)
Ta 136,23,51264
Ta 138,23,56384
Mvaa 176,144,192
Mvaa 184,144,208
Ta 140,24,51296
Ta 142,24,56416
Mvaa 188,148,208
Mvaa 180,148,192
s_waitcnt vmcnt(14) lgkmcnt(8)
Ta 144,25,64
Ta 146,25,5184
Mvaa 176,152,196
Mvaa 184,152,212
Ta 148,26,96
Ta 150,26,5216
Mvaa 188,156,212
Mvaa 180,156,196
W 8
Ta 152,21,41024
Ta 154,21,46144
Mvaa 192,128,96
Mvaa 200,128,112
Ta 156,22,41056
Ta 158,22,46176
Mvaa 204,132,112
K 176,176,177
Mvaa 196,132,96
W 8
Ta 128,21,51264
Ta 130,21,56384
Mvaa 192,136,100
K 177,178,179
Mvaa 200,136,116
Ta 132,22,51296
K 178,180,181
Mvaa 204,140,116
Ta 134,22,56416
K 179,182,183
Mvaa 196,140,100
W 8
Ta 136,23,10304
Ta 138,23,15424
Mvaa 192,144,104
s_add_u32 s83, s52, 327680
Mvaa 200,144,120
buffer_store_dwordx4 v[176:179], v1, s[8:11], s83 offen sc1
K 184,184,185
Mvaa 204,148,120
K 185,186,187
K 186,188,189
Mvaa 196,148,104
W 6
Ta 140,24,10336
K 187,190,191
Mvaa 192,152,108
s_add_u32 s83, s52, 368640
Ta 142,24,15456
Ta 144,23,30784
Mvaa 200,152,124
buffer_store_dwordx4 v[184:187], v1, s[8:11], s83 offen sc1
Ta 146,23,35904
Mvaa 204,156,124
Ta 148,24,30816
Ta 150,24,35936
Mvaa 196,156,108
W 8
Ta 152,23,41024
Ta 154,23,46144
Mvaa 192,128,184
Mvaa 200,128,200
Ta 156,24,41056
Ta 158,24,46176
Mvaa 204,132,200
Mvaa 196,132,184
W 8
Ta 128,23,20608
Ta 130,23,25728
Mvaa 192,136,188
Mvaa 200,136,204
Ta 132,24,20640
Ta 134,24,25760
Mvaa 204,140,204
Mvaa 196,140,188
W 8
Ta 136,23,51328
Ta 138,23,56448
Mvaa 192,144,192
Mvaa 200,144,208
Ta 140,24,51360
Ta 142,24,56480
Mvaa 204,148,208
Mvaa 196,148,192
W 8
Ta 144,25,128
Ta 146,25,5248
Mvaa 192,152,196
Mvaa 200,152,212
Ta 148,26,160
Ta 150,26,5280
Mvaa 204,156,212
Mvaa 196,156,196
W 8
Ta 152,21,41088
Ta 154,21,46208
Mvaa 208,128,96
Mvaa 216,128,112
Ta 156,22,41120
Ta 158,22,46240
Mvaa 220,132,112
K 192,192,193
Mvaa 212,132,96
W 8
Ta 128,21,51328
Ta 130,21,56448
Mvaa 208,136,100
K 193,194,195
Mvaa 216,136,116
Ta 132,22,51360
K 194,196,197
Mvaa 220,140,116
Ta 134,22,56480
K 195,198,199
Mvaa 212,140,100
W 8
Ta 136,23,10368
Ta 138,23,15488
Mvaa 208,144,104
s_add_u32 s83, s52, 328704
Mvaa 216,144,120
buffer_store_dwordx4 v[192:195], v1, s[8:11], s83 offen sc1
K 200,200,201
Mvaa 220,148,120
K 201,202,203
K 202,204,205
Mvaa 212,148,104
W 6
Ta 140,24,10400
K 203,206,207
Mvaa 208,152,108
s_add_u32 s83, s52, 369664
Ta 142,24,15520
Ta 144,23,30848
Mvaa 216,152,124
buffer_store_dwordx4 v[200:203], v1, s[8:11], s83 offen sc1
Ta 146,23,35968
Mvaa 220,156,124
Ta 148,24,30880
Ta 150,24,36000
Mvaa 212,156,108
W 8
Ta 152,23,41088
Ta 154,23,46208
Mvaa 208,128,184
Mvaa 216,128,200
s_waitcnt vmcnt(6)
v_lshlrev_b32 v2, 2, v1
v_mov_b64 v[28:29], s[12:13]
s_mov_b64 exec, 3
buffer_store_dwordx2 v[28:29], v2, s[8:11], s53 offen offset:0 sc1
s_mov_b64 exec, -1
Ta 156,24,41120
Ta 158,24,46240
Mvaa 220,132,200
Mvaa 212,132,184
W 8
Ta 128,23,20672
Ta 130,23,25792
Mvaa 208,136,188
Mvaa 216,136,204
s_waitcnt vmcnt(5)
v_xor_b32 v12, s12, v12
v_xor_b32 v13, s13, v13
v_lshrrev_b32 v3, 11, v12
v_or_b32 v13, v13, v3
v_cmp_eq_u32 vcc, 0, v13
s_cmp_eq_u64 vcc, -1
s_cbranch_scc0 .LL_98%=
.LL_99%=:
v_bfe_u32 v13, v12, 4, 7
s_xor_b32 s66, s61, 124
v_cmp_eq_u32 vcc, s61, v13
v_cmp_eq_u32_e64 s[86:87], s66, v13
s_or_b32 s68, vcc_lo, s86
s_and_b32 s68, s68, 0xff
s_cbranch_scc1 .Lup_96%=
.Lupr_97%=:
Ta 132,24,20704
Ta 134,24,25824
Mvaa 220,140,204
Mvaa 212,140,188
W 8
Ta 136,23,51392
v_lshlrev_b32 v32, 16, v36
Mvaa 208,144,192
Ta 138,23,56512
v_and_b32 v33, 0xffff0000, v36
Mvaa 216,144,208
Ta 140,24,51424
v_lshlrev_b32 v34, 16, v37
Mvaa 220,148,208
Ta 142,24,56544
v_and_b32 v35, 0xffff0000, v37
Mvaa 212,148,192
W 8
Ta 144,25,192
v_lshlrev_b32 v36, 16, v38
Mvaa 208,152,196
Ta 146,25,5312
v_and_b32 v37, 0xffff0000, v38
Mvaa 216,152,212
Ta 148,26,224
v_lshlrev_b32 v38, 16, v39
Mvaa 220,156,212
Ta 150,26,5344
v_and_b32 v39, 0xffff0000, v39
Mvaa 212,156,196
W 8
Ta 152,21,41152
v_lshlrev_b32 v40, 16, v44
Mvaa 224,128,96
Ta 154,21,46272
v_and_b32 v41, 0xffff0000, v44
Mvaa 232,128,112
Ta 156,22,41184
)"
R"(v_lshlrev_b32 v42, 16, v45
Mvaa 236,132,112
Ta 158,22,46304
v_and_b32 v43, 0xffff0000, v45
Mvaa 228,132,96
W 8
K 208,208,209
v_lshlrev_b32 v44, 16, v46
Mvaa 224,136,100
Ta 128,21,51392
Ta 130,21,56512
Mvaa 232,136,116
v_and_b32 v45, 0xffff0000, v46
K 209,210,211
Mvaa 236,140,116
v_lshlrev_b32 v46, 16, v47
K 210,212,213
Mvaa 228,140,100
W 6
Ta 132,22,51424
Ta 134,22,56544
Mvaa 224,144,104
v_and_b32 v47, 0xffff0000, v47
K 211,214,215
Mvaa 232,144,120
s_add_u32 s83, s52, 329728
Ta 136,23,10432
v_lshlrev_b32 v48, 16, v52
Mvaa 236,148,120
Ta 138,23,15552
v_and_b32 v49, 0xffff0000, v52
Mvaa 228,148,104
W 6
buffer_store_dwordx4 v[208:211], v1, s[8:11], s83 offen sc1
v_lshlrev_b32 v50, 16, v53
Mvaa 224,152,108
K 216,216,217
K 217,218,219
Mvaa 232,152,124
K 218,220,221
K 219,222,223
Mvaa 236,156,124
s_add_u32 s83, s52, 370688
Ta 140,24,10464
Ta 142,24,15584
Mvaa 228,156,108
W 4
buffer_store_dwordx4 v[216:219], v1, s[8:11], s83 offen sc1
v_and_b32 v51, 0xffff0000, v53
Mvaa 224,128,184
Ta 144,23,30912
v_lshlrev_b32 v52, 16, v54
Mvaa 232,128,200
Ta 146,23,36032
v_and_b32 v53, 0xffff0000, v54
Mvaa 236,132,200
Ta 148,24,30944
v_lshlrev_b32 v54, 16, v55
Mvaa 228,132,184
Ta 150,24,36064
W 4
Mvaa 224,136,188
v_and_b32 v55, 0xffff0000, v55
v_lshlrev_b32 v56, 16, v60
Mvaa 232,136,204
Ta 152,23,41152
Ta 154,23,46272
Mvaa 236,140,204
v_and_b32 v57, 0xffff0000, v60
v_lshlrev_b32 v58, 16, v61
Mvaa 228,140,188
Ta 156,24,41184
Ta 158,24,46304
W 4
Mvaa 224,144,192
v_and_b32 v59, 0xffff0000, v61
v_lshlrev_b32 v60, 16, v62
Mvaa 232,144,208
Ta 128,23,20736
Ta 130,23,25856
Mvaa 236,148,208
v_and_b32 v61, 0xffff0000, v62
v_lshlrev_b32 v62, 16, v63
Mvaa 228,148,192
Ta 132,24,20768
Ta 134,24,25888
W 4
Mvaa 224,152,196
v_and_b32 v63, 0xffff0000, v63
v_lshlrev_b32 v64, 16, v68
Mvaa 232,152,212
Ta 136,23,51456
Ta 138,23,56576
Mvaa 236,156,212
v_and_b32 v65, 0xffff0000, v68
v_lshlrev_b32 v66, 16, v69
Mvaa 228,156,196
Ta 140,24,51488
Ta 142,24,56608
W 4
Mvaa 240,128,96
v_and_b32 v67, 0xffff0000, v69
v_lshlrev_b32 v68, 16, v70
Mvaa 248,128,112
Ta 144,25,256
Ta 146,25,5376
Mvaa 252,132,112
v_and_b32 v69, 0xffff0000, v70
v_lshlrev_b32 v70, 16, v71
Mvaa 244,132,96
Ta 148,26,288
Ta 150,26,5408
W 4
Mvaa 240,136,100
v_and_b32 v71, 0xffff0000, v71
v_lshlrev_b32 v72, 16, v76
Mvaa 248,136,116
Ta 152,21,41216
Ta 154,21,46336
Mvaa 252,140,116
v_and_b32 v73, 0xffff0000, v76
v_lshlrev_b32 v74, 16, v77
Mvaa 244,140,100
Ta 156,22,41248
Ta 158,22,46368
W 4
Mvaa 240,144,104
v_and_b32 v75, 0xffff0000, v77
K 224,224,225
Mvaa 248,144,120
Ta 128,21,51456
v_lshlrev_b32 v76, 16, v78
Mvaa 252,148,120
Ta 130,21,56576
K 225,226,227
Mvaa 244,148,104
Ta 132,22,51488
Ta 134,22,56608
W 4
Mvaa 240,152,108
v_and_b32 v77, 0xffff0000, v78
K 226,228,229
Mvaa 248,152,124
v_lshlrev_b32 v78, 16, v79
v_and_b32 v79, 0xffff0000, v79
Mvaa 252,156,124
Ta 136,23,10496
K 227,230,231
Mvaa 244,156,108
Ta 138,23,15616
Ta 140,24,10528
Ta 142,24,15648
W 4
Mvaa 240,128,184
s_add_u32 s83, s52, 330752
buffer_store_dwordx4 v[224:227], v1, s[8:11], s83 offen sc1
K 232,232,233
Mvaa 248,128,200
K 233,234,235
K 234,236,237
Mvaa 252,132,200
Ta 144,23,30976
K 235,238,239
Mvaa 244,132,184
Ta 146,23,36096
Ta 148,24,31008
Ta 150,24,36128
W 4
Mvaa 240,136,188
s_add_u32 s83, s52, 371712
buffer_store_dwordx4 v[232:235], v1, s[8:11], s83 offen sc1
Ta 152,23,41216
Mvaa 248,136,204
Ta 154,23,46336
Ta 156,24,41248
Mvaa 252,140,204
Ta 158,24,46368
Mvaa 244,140,188
W 4
Mvaa 240,144,192
Mvaa 248,144,208
Mvaa 252,148,208
Mvaa 244,148,192
W 0
Mvaa 240,152,196
Mvaa 248,152,212
Mvaa 252,156,212
Mvaa 244,156,196
s_nop 4
K 240,240,241
K 241,242,243
s_nop 0
K 242,244,245
K 243,246,247
s_add_u32 s83, s52, 331776
buffer_store_dwordx4 v[240:243], v1, s[8:11], s83 offen sc1
K 248,248,249
K 249,250,251
K 250,252,253
K 251,254,255
s_add_u32 s83, s52, 372736
buffer_store_dwordx4 v[248:251], v1, s[8:11], s83 offen sc1
.Ltend_77%=:
v_bfe_u32 v26, v1, 7, 1
v_lshl_add_u32 v26, v26, 9, v27
buffer_load_dwordx2 v[24:25], v26, s[8:11], s63 offen sc1
s_cmp_lg_u32 s68, 0
s_cbranch_scc1 .Lup_101%=
.Lupr_102%=:
s_lshl_b32 s66, 0x101, s59
s_waitcnt vmcnt(1)
s_cmp_eq_u32 s64, 0
s_cbranch_scc0 .LL_107%=
v_lshlrev_b32 v2, 2, v1
v_mov_b64 v[28:29], s[12:13]
s_mov_b64 exec, 3
buffer_store_dwordx2 v[28:29], v2, s[8:11], s53 offen offset:512 sc1
s_mov_b64 exec, -1
.LL_107%=:
s_barrier
s_waitcnt vmcnt(1)
v_cmp_eq_u64 vcc, s[12:13], v[24:25]
s_or_b32 s67, vcc_lo, s66
s_cmp_eq_u32 s14, 0
s_cbranch_scc1 .LL_109%=
s_cmp_eq_u32 s14, 1
s_cbranch_scc1 .LL_110%=
s_mov_b32 s71, 0
s_mov_b32 s84, 0
.LL_111%=:
s_mov_b32 s70, s71
s_andn2_b32 s69, s67, s71
s_and_b32 s69, s69, 0xffff
s_cbranch_scc0 .LL_114%=
s_and_b32 s80, s69, 255
s_cbranch_scc0 .LL_115%=
s_bitcmp1_b32 s69, 0
s_cbranch_scc0 .LL_116%=
s_add_u32 s62, s78, 0
buffer_load_dwordx4 v[96:99], v1, s[8:11], s62 offen sc1
.LL_116%=:
s_bitcmp1_b32 s69, 1
s_cbranch_scc0 .LL_117%=
s_add_u32 s62, s78, 5120
buffer_load_dwordx4 v[100:103], v1, s[8:11], s62 offen sc1
.LL_117%=:
s_bitcmp1_b32 s69, 2
s_cbranch_scc0 .LL_118%=
s_add_u32 s62, s78, 10240
buffer_load_dwordx4 v[104:107], v1, s[8:11], s62 offen sc1
.LL_118%=:
s_bitcmp1_b32 s69, 3
s_cbranch_scc0 .LL_119%=
s_add_u32 s62, s78, 15360
buffer_load_dwordx4 v[108:111], v1, s[8:11], s62 offen sc1
.LL_119%=:
s_bitcmp1_b32 s69, 4
s_cbranch_scc0 .LL_120%=
s_add_u32 s62, s78, 20480
buffer_load_dwordx4 v[112:115], v1, s[8:11], s62 offen sc1
.LL_120%=:
s_bitcmp1_b32 s69, 5
s_cbranch_scc0 .LL_121%=
s_add_u32 s62, s78, 25600
buffer_load_dwordx4 v[116:119], v1, s[8:11], s62 offen sc1
.LL_121%=:
s_bitcmp1_b32 s69, 6
s_cbranch_scc0 .LL_122%=
s_add_u32 s62, s78, 30720
buffer_load_dwordx4 v[120:123], v1, s[8:11], s62 offen sc1
.LL_122%=:
s_bitcmp1_b32 s69, 7
s_cbranch_scc0 .LL_123%=
s_add_u32 s62, s78, 35840
buffer_load_dwordx4 v[124:127], v1, s[8:11], s62 offen sc1
.LL_123%=:
.LL_115%=:
s_and_b32 s80, s69, 65280
s_cbranch_scc0 .LL_124%=
s_bitcmp1_b32 s69, 8
s_cbranch_scc0 .LL_125%=
s_add_u32 s62, s79, 0
buffer_load_dwordx4 v[208:211], v1, s[8:11], s62 offen sc1
.LL_125%=:
s_bitcmp1_b32 s69, 9
s_cbranch_scc0 .LL_126%=
s_add_u32 s62, s79, 5120
buffer_load_dwordx4 v[212:215], v1, s[8:11], s62 offen sc1
.LL_126%=:
s_bitcmp1_b32 s69, 10
s_cbranch_scc0 .LL_127%=
s_add_u32 s62, s79, 10240
buffer_load_dwordx4 v[216:219], v1, s[8:11], s62 offen sc1
.LL_127%=:
s_bitcmp1_b32 s69, 11
s_cbranch_scc0 .LL_128%=
s_add_u32 s62, s79, 15360
buffer_load_dwordx4 v[220:223], v1, s[8:11], s62 offen sc1
.LL_128%=:
s_bitcmp1_b32 s69, 12
s_cbranch_scc0 .LL_129%=
s_add_u32 s62, s79, 20480
buffer_load_dwordx4 v[224:227], v1, s[8:11], s62 offen sc1
.LL_129%=:
s_bitcmp1_b32 s69, 13
s_cbranch_scc0 .LL_130%=
s_add_u32 s62, s79, 25600
buffer_load_dwordx4 v[228:231], v1, s[8:11], s62 offen sc1
.LL_130%=:
s_bitcmp1_b32 s69, 14
s_cbranch_scc0 .LL_131%=
s_add_u32 s62, s79, 30720
buffer_load_dwordx4 v[232:235], v1, s[8:11], s62 offen sc1
.LL_131%=:
s_bitcmp1_b32 s69, 15
s_cbranch_scc0 .LL_132%=
s_add_u32 s62, s79, 35840
buffer_load_dwordx4 v[236:239], v1, s[8:11], s62 offen sc1
.LL_132%=:
.LL_124%=:
s_or_b32 s71, s71, s69
s_andn2_b32 s80, 0xffff, s71
s_cbranch_scc0 .LL_113%=
.LL_114%=:
s_sleep 4
buffer_load_dwordx2 v[24:25], v26, s[8:11], s63 offen sc1
s_and_b32 s80, s70, 0x100ff
s_cmp_eq_u32 s80, 0xff
s_cbranch_scc0 .LL_112%=
Zvvv 80,4,96
Zvvv 84,8,96
Mvvv 80,4,100
Mvvv 84,8,100
Mvvv 80,4,104
Mvvv 84,8,104
Mvvv 80,4,108
Mvvv 84,8,108
Mvvv 80,4,112
Mvvv 84,8,112
Mvvv 80,4,116
Mvvv 84,8,116
Mvvv 80,4,120
Mvvv 84,8,120
Mvvv 80,4,124
Mvvv 84,8,124
s_nop 6
K 160,80,81
K 161,82,83
K 162,84,85
K 163,86,87
v_lshlrev_b32 v80, 16, v160
v_and_b32 v81, 0xffff0000, v160
v_lshlrev_b32 v82, 16, v161
v_and_b32 v83, 0xffff0000, v161
v_lshlrev_b32 v84, 16, v162
v_and_b32 v85, 0xffff0000, v162
v_lshlrev_b32 v86, 16, v163
v_and_b32 v87, 0xffff0000, v163
v_pk_add_f32 v[80:81], v[32:33], v[80:81]
v_pk_add_f32 v[82:83], v[34:35], v[82:83]
v_pk_add_f32 v[84:85], v[36:37], v[84:85]
v_pk_add_f32 v[86:87], v[38:39], v[86:87]
K 160,80,81
K 161,82,83
K 162,84,85
K 163,86,87
v_lshlrev_b32 v80, 16, v160
v_and_b32 v81, 0xffff0000, v160
v_lshlrev_b32 v82, 16, v161
v_and_b32 v83, 0xffff0000, v161
v_lshlrev_b32 v84, 16, v162
v_and_b32 v85, 0xffff0000, v162
v_lshlrev_b32 v86, 16, v163
v_and_b32 v87, 0xffff0000, v163
v_pk_add_f32 v[80:81], v[40:41], v[80:81]
v_pk_add_f32 v[82:83], v[42:43], v[82:83]
v_pk_add_f32 v[84:85], v[44:45], v[84:85]
v_pk_add_f32 v[86:87], v[46:47], v[86:87]
K 80,80,81
K 81,82,83
K 82,84,85
K 83,86,87
buffer_store_dwordx4 v[80:83], v20, s[20:23], s72 offen nt
s_bitset1_b32 s71, 16
.LL_112%=:
s_add_u32 s84, s84, 1
s_waitcnt vmcnt(0)
v_cmp_eq_u64 vcc, s[12:13], v[24:25]
s_or_b32 s67, vcc_lo, s66
s_cmp_lt_u32 s84, 4194304
s_cbranch_scc1 .LL_111%=
.LL_113%=:
s_bitcmp1_b32 s71, 16
s_cbranch_scc1 .LL_133%=
s_and_b32 s80, s70, 0xff
s_cmp_eq_u32 s80, 0xff
s_cbranch_scc1 .LL_134%=
s_waitcnt vmcnt(0)
.LL_134%=:
Zvvv 80,4,96
)"
R"(Zvvv 84,8,96
Mvvv 80,4,100
Mvvv 84,8,100
Mvvv 80,4,104
Mvvv 84,8,104
Mvvv 80,4,108
Mvvv 84,8,108
Mvvv 80,4,112
Mvvv 84,8,112
Mvvv 80,4,116
Mvvv 84,8,116
Mvvv 80,4,120
Mvvv 84,8,120
Mvvv 80,4,124
Mvvv 84,8,124
s_nop 6
K 160,80,81
K 161,82,83
K 162,84,85
K 163,86,87
v_lshlrev_b32 v80, 16, v160
v_and_b32 v81, 0xffff0000, v160
v_lshlrev_b32 v82, 16, v161
v_and_b32 v83, 0xffff0000, v161
v_lshlrev_b32 v84, 16, v162
v_and_b32 v85, 0xffff0000, v162
v_lshlrev_b32 v86, 16, v163
v_and_b32 v87, 0xffff0000, v163
v_pk_add_f32 v[80:81], v[32:33], v[80:81]
v_pk_add_f32 v[82:83], v[34:35], v[82:83]
v_pk_add_f32 v[84:85], v[36:37], v[84:85]
v_pk_add_f32 v[86:87], v[38:39], v[86:87]
K 160,80,81
K 161,82,83
K 162,84,85
K 163,86,87
v_lshlrev_b32 v80, 16, v160
v_and_b32 v81, 0xffff0000, v160
v_lshlrev_b32 v82, 16, v161
v_and_b32 v83, 0xffff0000, v161
v_lshlrev_b32 v84, 16, v162
v_and_b32 v85, 0xffff0000, v162
v_lshlrev_b32 v86, 16, v163
v_and_b32 v87, 0xffff0000, v163
v_pk_add_f32 v[80:81], v[40:41], v[80:81]
v_pk_add_f32 v[82:83], v[42:43], v[82:83]
v_pk_add_f32 v[84:85], v[44:45], v[84:85]
v_pk_add_f32 v[86:87], v[46:47], v[86:87]
K 80,80,81
K 81,82,83
K 82,84,85
K 83,86,87
buffer_store_dwordx4 v[80:83], v20, s[20:23], s72 offen nt
s_waitcnt vmcnt(1)
s_nop 0
Zvvv 80,4,208
Zvvv 84,8,208
Mvvv 80,4,212
Mvvv 84,8,212
Mvvv 80,4,216
Mvvv 84,8,216
Mvvv 80,4,220
Mvvv 84,8,220
Mvvv 80,4,224
Mvvv 84,8,224
Mvvv 80,4,228
Mvvv 84,8,228
Mvvv 80,4,232
Mvvv 84,8,232
Mvvv 80,4,236
Mvvv 84,8,236
s_nop 6
K 160,80,81
K 161,82,83
K 162,84,85
K 163,86,87
v_lshlrev_b32 v80, 16, v160
v_and_b32 v81, 0xffff0000, v160
v_lshlrev_b32 v82, 16, v161
v_and_b32 v83, 0xffff0000, v161
v_lshlrev_b32 v84, 16, v162
v_and_b32 v85, 0xffff0000, v162
v_lshlrev_b32 v86, 16, v163
v_and_b32 v87, 0xffff0000, v163
v_pk_add_f32 v[80:81], v[48:49], v[80:81]
v_pk_add_f32 v[82:83], v[50:51], v[82:83]
v_pk_add_f32 v[84:85], v[52:53], v[84:85]
v_pk_add_f32 v[86:87], v[54:55], v[86:87]
K 160,80,81
K 161,82,83
K 162,84,85
K 163,86,87
v_lshlrev_b32 v80, 16, v160
v_and_b32 v81, 0xffff0000, v160
v_lshlrev_b32 v82, 16, v161
v_and_b32 v83, 0xffff0000, v161
v_lshlrev_b32 v84, 16, v162
v_and_b32 v85, 0xffff0000, v162
v_lshlrev_b32 v86, 16, v163
v_and_b32 v87, 0xffff0000, v163
v_pk_add_f32 v[80:81], v[56:57], v[80:81]
v_pk_add_f32 v[82:83], v[58:59], v[82:83]
v_pk_add_f32 v[84:85], v[60:61], v[84:85]
v_pk_add_f32 v[86:87], v[62:63], v[86:87]
K 80,80,81
K 81,82,83
K 82,84,85
K 83,86,87
buffer_store_dwordx4 v[80:83], v20, s[20:23], s73 offen nt
s_branch .LL_108%=
.LL_133%=:
s_waitcnt vmcnt(0)
Zvvv 80,4,208
Zvvv 84,8,208
Mvvv 80,4,212
Mvvv 84,8,212
Mvvv 80,4,216
Mvvv 84,8,216
Mvvv 80,4,220
Mvvv 84,8,220
Mvvv 80,4,224
Mvvv 84,8,224
Mvvv 80,4,228
Mvvv 84,8,228
Mvvv 80,4,232
Mvvv 84,8,232
Mvvv 80,4,236
Mvvv 84,8,236
s_nop 6
K 160,80,81
K 161,82,83
K 162,84,85
K 163,86,87
v_lshlrev_b32 v80, 16, v160
v_and_b32 v81, 0xffff0000, v160
v_lshlrev_b32 v82, 16, v161
v_and_b32 v83, 0xffff0000, v161
v_lshlrev_b32 v84, 16, v162
v_and_b32 v85, 0xffff0000, v162
v_lshlrev_b32 v86, 16, v163
v_and_b32 v87, 0xffff0000, v163
v_pk_add_f32 v[80:81], v[48:49], v[80:81]
v_pk_add_f32 v[82:83], v[50:51], v[82:83]
v_pk_add_f32 v[84:85], v[52:53], v[84:85]
v_pk_add_f32 v[86:87], v[54:55], v[86:87]
K 160,80,81
K 161,82,83
K 162,84,85
K 163,86,87
v_lshlrev_b32 v80, 16, v160
v_and_b32 v81, 0xffff0000, v160
v_lshlrev_b32 v82, 16, v161
v_and_b32 v83, 0xffff0000, v161
v_lshlrev_b32 v84, 16, v162
v_and_b32 v85, 0xffff0000, v162
v_lshlrev_b32 v86, 16, v163
v_and_b32 v87, 0xffff0000, v163
v_pk_add_f32 v[80:81], v[56:57], v[80:81]
v_pk_add_f32 v[82:83], v[58:59], v[82:83]
v_pk_add_f32 v[84:85], v[60:61], v[84:85]
v_pk_add_f32 v[86:87], v[62:63], v[86:87]
K 80,80,81
K 81,82,83
K 82,84,85
K 83,86,87
buffer_store_dwordx4 v[80:83], v20, s[20:23], s73 offen nt
s_branch .LL_108%=
.LL_109%=:
s_mov_b32 s71, 0
s_mov_b32 s84, 0
.LL_135%=:
s_mov_b32 s70, s71
s_andn2_b32 s69, s67, s71
s_and_b32 s69, s69, 0xffff
s_cbranch_scc0 .LL_138%=
s_and_b32 s80, s69, 255
s_cbranch_scc0 .LL_139%=
s_bitcmp1_b32 s69, 0
s_cbranch_scc0 .LL_140%=
s_add_u32 s62, s78, 0
buffer_load_dwordx4 v[96:99], v1, s[8:11], s62 offen sc1
s_add_u32 s62, s65, 0
buffer_load_dwordx4 v[128:131], v1, s[8:11], s62 offen sc1
.LL_140%=:
s_bitcmp1_b32 s69, 1
s_cbranch_scc0 .LL_141%=
s_add_u32 s62, s78, 5120
buffer_load_dwordx4 v[100:103], v1, s[8:11], s62 offen sc1
s_add_u32 s62, s65, 5120
buffer_load_dwordx4 v[132:135], v1, s[8:11], s62 offen sc1
.LL_141%=:
s_bitcmp1_b32 s69, 2
s_cbranch_scc0 .LL_142%=
s_add_u32 s62, s78, 10240
buffer_load_dwordx4 v[104:107], v1, s[8:11], s62 offen sc1
s_add_u32 s62, s65, 10240
buffer_load_dwordx4 v[136:139], v1, s[8:11], s62 offen sc1
.LL_142%=:
s_bitcmp1_b32 s69, 3
s_cbranch_scc0 .LL_143%=
s_add_u32 s62, s78, 15360
buffer_load_dwordx4 v[108:111], v1, s[8:11], s62 offen sc1
s_add_u32 s62, s65, 15360
buffer_load_dwordx4 v[140:143], v1, s[8:11], s62 offen sc1
.LL_143%=:
s_bitcmp1_b32 s69, 4
s_cbranch_scc0 .LL_144%=
s_add_u32 s62, s78, 20480
buffer_load_dwordx4 v[112:115], v1, s[8:11], s62 offen sc1
s_add_u32 s62, s65, 20480
buffer_load_dwordx4 v[144:147], v1, s[8:11], s62 offen sc1
.LL_144%=:
s_bitcmp1_b32 s69, 5
s_cbranch_scc0 .LL_145%=
s_add_u32 s62, s78, 25600
buffer_load_dwordx4 v[116:119], v1, s[8:11], s62 offen sc1
s_add_u32 s62, s65, 25600
buffer_load_dwordx4 v[148:151], v1, s[8:11], s62 offen sc1
.LL_145%=:
s_bitcmp1_b32 s69, 6
s_cbranch_scc0 .LL_146%=
s_add_u32 s62, s78, 30720
buffer_load_dwordx4 v[120:123], v1, s[8:11], s62 offen sc1
s_add_u32 s62, s65, 30720
buffer_load_dwordx4 v[152:155], v1, s[8:11], s62 offen sc1
.LL_146%=:
s_bitcmp1_b32 s69, 7
s_cbranch_scc0 .LL_147%=
s_add_u32 s62, s78, 35840
buffer_load_dwordx4 v[124:127], v1, s[8:11], s62 offen sc1
s_add_u32 s62, s65, 35840
buffer_load_dwordx4 v[156:159], v1, s[8:11], s62 offen sc1
.LL_147%=:
.LL_139%=:
s_and_b32 s80, s69, 65280
s_cbranch_scc0 .LL_148%=
s_bitcmp1_b32 s69, 8
s_cbranch_scc0 .LL_149%=
s_add_u32 s62, s79, 0
buffer_load_dwordx4 v[208:211], v1, s[8:11], s62 offen sc1
.LL_149%=:
s_bitcmp1_b32 s69, 9
s_cbranch_scc0 .LL_150%=
s_add_u32 s62, s79, 5120
buffer_load_dwordx4 v[212:215], v1, s[8:11], s62 offen sc1
.LL_150%=:
s_bitcmp1_b32 s69, 10
s_cbranch_scc0 .LL_151%=
s_add_u32 s62, s79, 10240
buffer_load_dwordx4 v[216:219], v1, s[8:11], s62 offen sc1
.LL_151%=:
s_bitcmp1_b32 s69, 11
s_cbranch_scc0 .LL_152%=
s_add_u32 s62, s79, 15360
buffer_load_dwordx4 v[220:223], v1, s[8:11], s62 offen sc1
.LL_152%=:
s_bitcmp1_b32 s69, 12
s_cbranch_scc0 .LL_153%=
s_add_u32 s62, s79, 20480
buffer_load_dwordx4 v[224:227], v1, s[8:11], s62 offen sc1
.LL_153%=:
s_bitcmp1_b32 s69, 13
s_cbranch_scc0 .LL_154%=
s_add_u32 s62, s79, 25600
buffer_load_dwordx4 v[228:231], v1, s[8:11], s62 offen sc1
.LL_154%=:
s_bitcmp1_b32 s69, 14
s_cbranch_scc0 .LL_155%=
s_add_u32 s62, s79, 30720
buffer_load_dwordx4 v[232:235], v1, s[8:11], s62 offen sc1
.LL_155%=:
s_bitcmp1_b32 s69, 15
s_cbranch_scc0 .LL_156%=
s_add_u32 s62, s79, 35840
buffer_load_dwordx4 v[236:239], v1, s[8:11], s62 offen sc1
.LL_156%=:
.LL_148%=:
s_or_b32 s71, s71, s69
s_andn2_b32 s80, 0xffff, s71
s_cbranch_scc0 .LL_137%=
.LL_138%=:
s_sleep 4
buffer_load_dwordx2 v[24:25], v26, s[8:11], s63 offen sc1
s_and_b32 s80, s70, 0x100ff
s_cmp_eq_u32 s80, 0xff
s_cbranch_scc0 .LL_136%=
Zvvv 80,4,96
Zvvv 84,8,96
Zvvv 88,4,128
Zvvv 92,8,128
Mvvv 80,4,100
Mvvv 84,8,100
Mvvv 88,4,132
Mvvv 92,8,132
Mvvv 80,4,104
Mvvv 84,8,104
Mvvv 88,4,136
Mvvv 92,8,136
Mvvv 80,4,108
Mvvv 84,8,108
Mvvv 88,4,140
Mvvv 92,8,140
Mvvv 80,4,112
Mvvv 84,8,112
Mvvv 88,4,144
Mvvv 92,8,144
Mvvv 80,4,116
Mvvv 84,8,116
Mvvv 88,4,148
Mvvv 92,8,148
Mvvv 80,4,120
Mvvv 84,8,120
Mvvv 88,4,152
Mvvv 92,8,152
Mvvv 80,4,124
Mvvv 84,8,124
Mvvv 88,4,156
Mvvv 92,8,156
s_nop 4
K 160,80,81
K 161,82,83
K 162,84,85
K 163,86,87
v_lshlrev_b32 v80, 16, v160
v_and_b32 v81, 0xffff0000, v160
v_lshlrev_b32 v82, 16, v161
v_and_b32 v83, 0xffff0000, v161
v_lshlrev_b32 v84, 16, v162
v_and_b32 v85, 0xffff0000, v162
v_lshlrev_b32 v86, 16, v163
v_and_b32 v87, 0xffff0000, v163
v_pk_add_f32 v[80:81], v[32:33], v[80:81]
v_pk_add_f32 v[82:83], v[34:35], v[82:83]
v_pk_add_f32 v[84:85], v[36:37], v[84:85]
v_pk_add_f32 v[86:87], v[38:39], v[86:87]
K 160,80,81
K 161,82,83
K 162,84,85
K 163,86,87
v_lshlrev_b32 v80, 16, v160
v_and_b32 v81, 0xffff0000, v160
v_lshlrev_b32 v82, 16, v161
v_and_b32 v83, 0xffff0000, v161
v_lshlrev_b32 v84, 16, v162
v_and_b32 v85, 0xffff0000, v162
v_lshlrev_b32 v86, 16, v163
v_and_b32 v87, 0xffff0000, v163
v_pk_add_f32 v[80:81], v[40:41], v[80:81]
v_pk_add_f32 v[82:83], v[42:43], v[82:83]
v_pk_add_f32 v[84:85], v[44:45], v[84:85]
v_pk_add_f32 v[86:87], v[46:47], v[86:87]
K 80,80,81
K 81,82,83
K 82,84,85
K 83,86,87
buffer_store_dwordx4 v[80:83], v20, s[20:23], s72 offen nt
K 160,88,89
K 161,90,91
K 162,92,93
K 163,94,95
v_lshlrev_b32 v88, 16, v160
v_and_b32 v89, 0xffff0000, v160
v_lshlrev_b32 v90, 16, v161
v_and_b32 v91, 0xffff0000, v161
v_lshlrev_b32 v92, 16, v162
v_and_b32 v93, 0xffff0000, v162
v_lshlrev_b32 v94, 16, v163
v_and_b32 v95, 0xffff0000, v163
v_pk_add_f32 v[88:89], v[64:65], v[88:89]
v_pk_add_f32 v[90:91], v[66:67], v[90:91]
v_pk_add_f32 v[92:93], v[68:69], v[92:93]
v_pk_add_f32 v[94:95], v[70:71], v[94:95]
K 160,88,89
K 161,90,91
K 162,92,93
K 163,94,95
v_lshlrev_b32 v88, 16, v160
v_and_b32 v89, 0xffff0000, v160
v_lshlrev_b32 v90, 16, v161
v_and_b32 v91, 0xffff0000, v161
v_lshlrev_b32 v92, 16, v162
v_and_b32 v93, 0xffff0000, v162
v_lshlrev_b32 v94, 16, v163
v_and_b32 v95, 0xffff0000, v163
v_pk_add_f32 v[88:89], v[72:73], v[88:89]
v_pk_add_f32 v[90:91], v[74:75], v[90:91]
v_pk_add_f32 v[92:93], v[76:77], v[92:93]
v_pk_add_f32 v[94:95], v[78:79], v[94:95]
K 88,88,89
K 89,90,91
K 90,92,93
K 91,94,95
buffer_store_dwordx4 v[88:91], v20, s[20:23], s74 offen nt
s_bitset1_b32 s71, 16
.LL_136%=:
s_add_u32 s84, s84, 1
s_waitcnt vmcnt(0)
v_cmp_eq_u64 vcc, s[12:13], v[24:25]
s_or_b32 s67, vcc_lo, s66
s_cmp_lt_u32 s84, 4194304
)"
R"(s_cbranch_scc1 .LL_135%=
.LL_137%=:
s_bitcmp1_b32 s71, 16
s_cbranch_scc1 .LL_157%=
s_and_b32 s80, s70, 0xff
s_cmp_eq_u32 s80, 0xff
s_cbranch_scc1 .LL_158%=
s_waitcnt vmcnt(0)
.LL_158%=:
Zvvv 80,4,96
Zvvv 84,8,96
Zvvv 88,4,128
Zvvv 92,8,128
Mvvv 80,4,100
Mvvv 84,8,100
Mvvv 88,4,132
Mvvv 92,8,132
Mvvv 80,4,104
Mvvv 84,8,104
Mvvv 88,4,136
Mvvv 92,8,136
Mvvv 80,4,108
Mvvv 84,8,108
Mvvv 88,4,140
Mvvv 92,8,140
Mvvv 80,4,112
Mvvv 84,8,112
Mvvv 88,4,144
Mvvv 92,8,144
Mvvv 80,4,116
Mvvv 84,8,116
Mvvv 88,4,148
Mvvv 92,8,148
Mvvv 80,4,120
Mvvv 84,8,120
Mvvv 88,4,152
Mvvv 92,8,152
Mvvv 80,4,124
Mvvv 84,8,124
Mvvv 88,4,156
Mvvv 92,8,156
s_nop 4
K 160,80,81
K 161,82,83
K 162,84,85
K 163,86,87
v_lshlrev_b32 v80, 16, v160
v_and_b32 v81, 0xffff0000, v160
v_lshlrev_b32 v82, 16, v161
v_and_b32 v83, 0xffff0000, v161
v_lshlrev_b32 v84, 16, v162
v_and_b32 v85, 0xffff0000, v162
v_lshlrev_b32 v86, 16, v163
v_and_b32 v87, 0xffff0000, v163
v_pk_add_f32 v[80:81], v[32:33], v[80:81]
v_pk_add_f32 v[82:83], v[34:35], v[82:83]
v_pk_add_f32 v[84:85], v[36:37], v[84:85]
v_pk_add_f32 v[86:87], v[38:39], v[86:87]
K 160,80,81
K 161,82,83
K 162,84,85
K 163,86,87
v_lshlrev_b32 v80, 16, v160
v_and_b32 v81, 0xffff0000, v160
v_lshlrev_b32 v82, 16, v161
v_and_b32 v83, 0xffff0000, v161
v_lshlrev_b32 v84, 16, v162
v_and_b32 v85, 0xffff0000, v162
v_lshlrev_b32 v86, 16, v163
v_and_b32 v87, 0xffff0000, v163
v_pk_add_f32 v[80:81], v[40:41], v[80:81]
v_pk_add_f32 v[82:83], v[42:43], v[82:83]
v_pk_add_f32 v[84:85], v[44:45], v[84:85]
v_pk_add_f32 v[86:87], v[46:47], v[86:87]
K 80,80,81
K 81,82,83
K 82,84,85
K 83,86,87
buffer_store_dwordx4 v[80:83], v20, s[20:23], s72 offen nt
K 160,88,89
K 161,90,91
K 162,92,93
K 163,94,95
v_lshlrev_b32 v88, 16, v160
v_and_b32 v89, 0xffff0000, v160
v_lshlrev_b32 v90, 16, v161
v_and_b32 v91, 0xffff0000, v161
v_lshlrev_b32 v92, 16, v162
v_and_b32 v93, 0xffff0000, v162
v_lshlrev_b32 v94, 16, v163
v_and_b32 v95, 0xffff0000, v163
v_pk_add_f32 v[88:89], v[64:65], v[88:89]
v_pk_add_f32 v[90:91], v[66:67], v[90:91]
v_pk_add_f32 v[92:93], v[68:69], v[92:93]
v_pk_add_f32 v[94:95], v[70:71], v[94:95]
K 160,88,89
K 161,90,91
K 162,92,93
K 163,94,95
v_lshlrev_b32 v88, 16, v160
v_and_b32 v89, 0xffff0000, v160
v_lshlrev_b32 v90, 16, v161
v_and_b32 v91, 0xffff0000, v161
v_lshlrev_b32 v92, 16, v162
v_and_b32 v93, 0xffff0000, v162
v_lshlrev_b32 v94, 16, v163
v_and_b32 v95, 0xffff0000, v163
v_pk_add_f32 v[88:89], v[72:73], v[88:89]
v_pk_add_f32 v[90:91], v[74:75], v[90:91]
v_pk_add_f32 v[92:93], v[76:77], v[92:93]
v_pk_add_f32 v[94:95], v[78:79], v[94:95]
K 88,88,89
K 89,90,91
K 90,92,93
K 91,94,95
buffer_store_dwordx4 v[88:91], v20, s[20:23], s74 offen nt
s_waitcnt vmcnt(2)
Zvvv 80,4,208
Zvvv 84,8,208
Mvvv 80,4,212
Mvvv 84,8,212
Mvvv 80,4,216
Mvvv 84,8,216
Mvvv 80,4,220
Mvvv 84,8,220
Mvvv 80,4,224
Mvvv 84,8,224
Mvvv 80,4,228
Mvvv 84,8,228
Mvvv 80,4,232
Mvvv 84,8,232
Mvvv 80,4,236
Mvvv 84,8,236
s_nop 6
K 160,80,81
K 161,82,83
K 162,84,85
K 163,86,87
v_lshlrev_b32 v80, 16, v160
v_and_b32 v81, 0xffff0000, v160
v_lshlrev_b32 v82, 16, v161
v_and_b32 v83, 0xffff0000, v161
v_lshlrev_b32 v84, 16, v162
v_and_b32 v85, 0xffff0000, v162
v_lshlrev_b32 v86, 16, v163
v_and_b32 v87, 0xffff0000, v163
v_pk_add_f32 v[80:81], v[48:49], v[80:81]
v_pk_add_f32 v[82:83], v[50:51], v[82:83]
v_pk_add_f32 v[84:85], v[52:53], v[84:85]
v_pk_add_f32 v[86:87], v[54:55], v[86:87]
K 160,80,81
K 161,82,83
K 162,84,85
K 163,86,87
v_lshlrev_b32 v80, 16, v160
v_and_b32 v81, 0xffff0000, v160
v_lshlrev_b32 v82, 16, v161
v_and_b32 v83, 0xffff0000, v161
v_lshlrev_b32 v84, 16, v162
v_and_b32 v85, 0xffff0000, v162
v_lshlrev_b32 v86, 16, v163
v_and_b32 v87, 0xffff0000, v163
v_pk_add_f32 v[80:81], v[56:57], v[80:81]
v_pk_add_f32 v[82:83], v[58:59], v[82:83]
v_pk_add_f32 v[84:85], v[60:61], v[84:85]
v_pk_add_f32 v[86:87], v[62:63], v[86:87]
K 80,80,81
K 81,82,83
K 82,84,85
K 83,86,87
buffer_store_dwordx4 v[80:83], v20, s[20:23], s73 offen nt
s_branch .LL_108%=
.LL_157%=:
s_waitcnt vmcnt(0)
Zvvv 80,4,208
Zvvv 84,8,208
Mvvv 80,4,212
Mvvv 84,8,212
Mvvv 80,4,216
Mvvv 84,8,216
Mvvv 80,4,220
Mvvv 84,8,220
Mvvv 80,4,224
Mvvv 84,8,224
Mvvv 80,4,228
Mvvv 84,8,228
Mvvv 80,4,232
Mvvv 84,8,232
Mvvv 80,4,236
Mvvv 84,8,236
s_nop 6
K 160,80,81
K 161,82,83
K 162,84,85
K 163,86,87
v_lshlrev_b32 v80, 16, v160
v_and_b32 v81, 0xffff0000, v160
v_lshlrev_b32 v82, 16, v161
v_and_b32 v83, 0xffff0000, v161
v_lshlrev_b32 v84, 16, v162
v_and_b32 v85, 0xffff0000, v162
v_lshlrev_b32 v86, 16, v163
v_and_b32 v87, 0xffff0000, v163
v_pk_add_f32 v[80:81], v[48:49], v[80:81]
v_pk_add_f32 v[82:83], v[50:51], v[82:83]
v_pk_add_f32 v[84:85], v[52:53], v[84:85]
v_pk_add_f32 v[86:87], v[54:55], v[86:87]
K 160,80,81
K 161,82,83
K 162,84,85
K 163,86,87
v_lshlrev_b32 v80, 16, v160
v_and_b32 v81, 0xffff0000, v160
v_lshlrev_b32 v82, 16, v161
v_and_b32 v83, 0xffff0000, v161
v_lshlrev_b32 v84, 16, v162
v_and_b32 v85, 0xffff0000, v162
v_lshlrev_b32 v86, 16, v163
v_and_b32 v87, 0xffff0000, v163
v_pk_add_f32 v[80:81], v[56:57], v[80:81]
v_pk_add_f32 v[82:83], v[58:59], v[82:83]
v_pk_add_f32 v[84:85], v[60:61], v[84:85]
v_pk_add_f32 v[86:87], v[62:63], v[86:87]
K 80,80,81
K 81,82,83
K 82,84,85
K 83,86,87
buffer_store_dwordx4 v[80:83], v20, s[20:23], s73 offen nt
s_branch .LL_108%=
.LL_110%=:
s_mov_b32 s71, 0
s_mov_b32 s84, 0
.LL_159%=:
s_mov_b32 s70, s71
s_andn2_b32 s69, s67, s71
s_and_b32 s69, s69, 0xffff
s_cbranch_scc0 .LL_162%=
s_and_b32 s80, s69, 255
s_cbranch_scc0 .LL_163%=
s_bitcmp1_b32 s69, 0
s_cbranch_scc0 .LL_164%=
s_add_u32 s62, s78, 0
buffer_load_dwordx4 v[96:99], v1, s[8:11], s62 offen sc1
.LL_164%=:
s_bitcmp1_b32 s69, 1
s_cbranch_scc0 .LL_165%=
s_add_u32 s62, s78, 5120
buffer_load_dwordx4 v[100:103], v1, s[8:11], s62 offen sc1
.LL_165%=:
s_bitcmp1_b32 s69, 2
s_cbranch_scc0 .LL_166%=
s_add_u32 s62, s78, 10240
buffer_load_dwordx4 v[104:107], v1, s[8:11], s62 offen sc1
.LL_166%=:
s_bitcmp1_b32 s69, 3
s_cbranch_scc0 .LL_167%=
s_add_u32 s62, s78, 15360
buffer_load_dwordx4 v[108:111], v1, s[8:11], s62 offen sc1
.LL_167%=:
s_bitcmp1_b32 s69, 4
s_cbranch_scc0 .LL_168%=
s_add_u32 s62, s78, 20480
buffer_load_dwordx4 v[112:115], v1, s[8:11], s62 offen sc1
.LL_168%=:
s_bitcmp1_b32 s69, 5
s_cbranch_scc0 .LL_169%=
s_add_u32 s62, s78, 25600
buffer_load_dwordx4 v[116:119], v1, s[8:11], s62 offen sc1
.LL_169%=:
s_bitcmp1_b32 s69, 6
s_cbranch_scc0 .LL_170%=
s_add_u32 s62, s78, 30720
buffer_load_dwordx4 v[120:123], v1, s[8:11], s62 offen sc1
.LL_170%=:
s_bitcmp1_b32 s69, 7
s_cbranch_scc0 .LL_171%=
s_add_u32 s62, s78, 35840
buffer_load_dwordx4 v[124:127], v1, s[8:11], s62 offen sc1
.LL_171%=:
.LL_163%=:
s_and_b32 s80, s69, 65280
s_cbranch_scc0 .LL_172%=
s_bitcmp1_b32 s69, 8
s_cbranch_scc0 .LL_173%=
s_add_u32 s62, s79, 0
buffer_load_dwordx4 v[208:211], v1, s[8:11], s62 offen sc1
s_add_u32 s62, s65, 0
buffer_load_dwordx4 v[128:131], v1, s[8:11], s62 offen sc1
.LL_173%=:
s_bitcmp1_b32 s69, 9
s_cbranch_scc0 .LL_174%=
s_add_u32 s62, s79, 5120
buffer_load_dwordx4 v[212:215], v1, s[8:11], s62 offen sc1
s_add_u32 s62, s65, 5120
buffer_load_dwordx4 v[132:135], v1, s[8:11], s62 offen sc1
.LL_174%=:
s_bitcmp1_b32 s69, 10
s_cbranch_scc0 .LL_175%=
s_add_u32 s62, s79, 10240
buffer_load_dwordx4 v[216:219], v1, s[8:11], s62 offen sc1
s_add_u32 s62, s65, 10240
buffer_load_dwordx4 v[136:139], v1, s[8:11], s62 offen sc1
.LL_175%=:
s_bitcmp1_b32 s69, 11
s_cbranch_scc0 .LL_176%=
s_add_u32 s62, s79, 15360
buffer_load_dwordx4 v[220:223], v1, s[8:11], s62 offen sc1
s_add_u32 s62, s65, 15360
buffer_load_dwordx4 v[140:143], v1, s[8:11], s62 offen sc1
.LL_176%=:
s_bitcmp1_b32 s69, 12
s_cbranch_scc0 .LL_177%=
s_add_u32 s62, s79, 20480
buffer_load_dwordx4 v[224:227], v1, s[8:11], s62 offen sc1
s_add_u32 s62, s65, 20480
buffer_load_dwordx4 v[144:147], v1, s[8:11], s62 offen sc1
.LL_177%=:
s_bitcmp1_b32 s69, 13
s_cbranch_scc0 .LL_178%=
s_add_u32 s62, s79, 25600
buffer_load_dwordx4 v[228:231], v1, s[8:11], s62 offen sc1
s_add_u32 s62, s65, 25600
buffer_load_dwordx4 v[148:151], v1, s[8:11], s62 offen sc1
.LL_178%=:
s_bitcmp1_b32 s69, 14
s_cbranch_scc0 .LL_179%=
s_add_u32 s62, s79, 30720
buffer_load_dwordx4 v[232:235], v1, s[8:11], s62 offen sc1
s_add_u32 s62, s65, 30720
buffer_load_dwordx4 v[152:155], v1, s[8:11], s62 offen sc1
.LL_179%=:
s_bitcmp1_b32 s69, 15
s_cbranch_scc0 .LL_180%=
s_add_u32 s62, s79, 35840
buffer_load_dwordx4 v[236:239], v1, s[8:11], s62 offen sc1
s_add_u32 s62, s65, 35840
buffer_load_dwordx4 v[156:159], v1, s[8:11], s62 offen sc1
.LL_180%=:
.LL_172%=:
s_or_b32 s71, s71, s69
s_andn2_b32 s80, 0xffff, s71
s_cbranch_scc0 .LL_161%=
.LL_162%=:
s_sleep 4
buffer_load_dwordx2 v[24:25], v26, s[8:11], s63 offen sc1
s_and_b32 s80, s70, 0x100ff
s_cmp_eq_u32 s80, 0xff
s_cbranch_scc0 .LL_160%=
Zvvv 80,4,96
Zvvv 84,8,96
Mvvv 80,4,100
Mvvv 84,8,100
Mvvv 80,4,104
Mvvv 84,8,104
Mvvv 80,4,108
Mvvv 84,8,108
Mvvv 80,4,112
Mvvv 84,8,112
Mvvv 80,4,116
Mvvv 84,8,116
Mvvv 80,4,120
Mvvv 84,8,120
Mvvv 80,4,124
Mvvv 84,8,124
s_nop 6
K 160,80,81
K 161,82,83
K 162,84,85
K 163,86,87
v_lshlrev_b32 v80, 16, v160
v_and_b32 v81, 0xffff0000, v160
v_lshlrev_b32 v82, 16, v161
v_and_b32 v83, 0xffff0000, v161
v_lshlrev_b32 v84, 16, v162
v_and_b32 v85, 0xffff0000, v162
v_lshlrev_b32 v86, 16, v163
v_and_b32 v87, 0xffff0000, v163
v_pk_add_f32 v[80:81], v[32:33], v[80:81]
v_pk_add_f32 v[82:83], v[34:35], v[82:83]
v_pk_add_f32 v[84:85], v[36:37], v[84:85]
v_pk_add_f32 v[86:87], v[38:39], v[86:87]
K 160,80,81
K 161,82,83
K 162,84,85
K 163,86,87
v_lshlrev_b32 v80, 16, v160
v_and_b32 v81, 0xffff0000, v160
v_lshlrev_b32 v82, 16, v161
v_and_b32 v83, 0xffff0000, v161
v_lshlrev_b32 v84, 16, v162
v_and_b32 v85, 0xffff0000, v162
v_lshlrev_b32 v86, 16, v163
v_and_b32 v87, 0xffff0000, v163
v_pk_add_f32 v[80:81], v[40:41], v[80:81]
v_pk_add_f32 v[82:83], v[42:43], v[82:83]
v_pk_add_f32 v[84:85], v[44:45], v[84:85]
v_pk_add_f32 v[86:87], v[46:47], v[86:87]
K 80,80,81
K 81,82,83
K 82,84,85
)"
R"(K 83,86,87
buffer_store_dwordx4 v[80:83], v20, s[20:23], s72 offen nt
s_bitset1_b32 s71, 16
.LL_160%=:
s_add_u32 s84, s84, 1
s_waitcnt vmcnt(0)
v_cmp_eq_u64 vcc, s[12:13], v[24:25]
s_or_b32 s67, vcc_lo, s66
s_cmp_lt_u32 s84, 4194304
s_cbranch_scc1 .LL_159%=
.LL_161%=:
s_bitcmp1_b32 s71, 16
s_cbranch_scc1 .LL_181%=
s_and_b32 s80, s70, 0xff
s_cmp_eq_u32 s80, 0xff
s_cbranch_scc1 .LL_182%=
s_waitcnt vmcnt(0)
.LL_182%=:
Zvvv 80,4,96
Zvvv 84,8,96
Mvvv 80,4,100
Mvvv 84,8,100
Mvvv 80,4,104
Mvvv 84,8,104
Mvvv 80,4,108
Mvvv 84,8,108
Mvvv 80,4,112
Mvvv 84,8,112
Mvvv 80,4,116
Mvvv 84,8,116
Mvvv 80,4,120
Mvvv 84,8,120
Mvvv 80,4,124
Mvvv 84,8,124
s_nop 6
K 160,80,81
K 161,82,83
K 162,84,85
K 163,86,87
v_lshlrev_b32 v80, 16, v160
v_and_b32 v81, 0xffff0000, v160
v_lshlrev_b32 v82, 16, v161
v_and_b32 v83, 0xffff0000, v161
v_lshlrev_b32 v84, 16, v162
v_and_b32 v85, 0xffff0000, v162
v_lshlrev_b32 v86, 16, v163
v_and_b32 v87, 0xffff0000, v163
v_pk_add_f32 v[80:81], v[32:33], v[80:81]
v_pk_add_f32 v[82:83], v[34:35], v[82:83]
v_pk_add_f32 v[84:85], v[36:37], v[84:85]
v_pk_add_f32 v[86:87], v[38:39], v[86:87]
K 160,80,81
K 161,82,83
K 162,84,85
K 163,86,87
v_lshlrev_b32 v80, 16, v160
v_and_b32 v81, 0xffff0000, v160
v_lshlrev_b32 v82, 16, v161
v_and_b32 v83, 0xffff0000, v161
v_lshlrev_b32 v84, 16, v162
v_and_b32 v85, 0xffff0000, v162
v_lshlrev_b32 v86, 16, v163
v_and_b32 v87, 0xffff0000, v163
v_pk_add_f32 v[80:81], v[40:41], v[80:81]
v_pk_add_f32 v[82:83], v[42:43], v[82:83]
v_pk_add_f32 v[84:85], v[44:45], v[84:85]
v_pk_add_f32 v[86:87], v[46:47], v[86:87]
K 80,80,81
K 81,82,83
K 82,84,85
K 83,86,87
buffer_store_dwordx4 v[80:83], v20, s[20:23], s72 offen nt
s_waitcnt vmcnt(1)
s_nop 0
Zvvv 80,4,208
Zvvv 84,8,208
Zvvv 88,4,128
Zvvv 92,8,128
Mvvv 80,4,212
Mvvv 84,8,212
Mvvv 88,4,132
Mvvv 92,8,132
Mvvv 80,4,216
Mvvv 84,8,216
Mvvv 88,4,136
Mvvv 92,8,136
Mvvv 80,4,220
Mvvv 84,8,220
Mvvv 88,4,140
Mvvv 92,8,140
Mvvv 80,4,224
Mvvv 84,8,224
Mvvv 88,4,144
Mvvv 92,8,144
Mvvv 80,4,228
Mvvv 84,8,228
Mvvv 88,4,148
Mvvv 92,8,148
Mvvv 80,4,232
Mvvv 84,8,232
Mvvv 88,4,152
Mvvv 92,8,152
Mvvv 80,4,236
Mvvv 84,8,236
Mvvv 88,4,156
Mvvv 92,8,156
s_nop 4
K 160,80,81
K 161,82,83
K 162,84,85
K 163,86,87
v_lshlrev_b32 v80, 16, v160
v_and_b32 v81, 0xffff0000, v160
v_lshlrev_b32 v82, 16, v161
v_and_b32 v83, 0xffff0000, v161
v_lshlrev_b32 v84, 16, v162
v_and_b32 v85, 0xffff0000, v162
v_lshlrev_b32 v86, 16, v163
v_and_b32 v87, 0xffff0000, v163
v_pk_add_f32 v[80:81], v[48:49], v[80:81]
v_pk_add_f32 v[82:83], v[50:51], v[82:83]
v_pk_add_f32 v[84:85], v[52:53], v[84:85]
v_pk_add_f32 v[86:87], v[54:55], v[86:87]
K 160,80,81
K 161,82,83
K 162,84,85
K 163,86,87
v_lshlrev_b32 v80, 16, v160
v_and_b32 v81, 0xffff0000, v160
v_lshlrev_b32 v82, 16, v161
v_and_b32 v83, 0xffff0000, v161
v_lshlrev_b32 v84, 16, v162
v_and_b32 v85, 0xffff0000, v162
v_lshlrev_b32 v86, 16, v163
v_and_b32 v87, 0xffff0000, v163
v_pk_add_f32 v[80:81], v[56:57], v[80:81]
v_pk_add_f32 v[82:83], v[58:59], v[82:83]
v_pk_add_f32 v[84:85], v[60:61], v[84:85]
v_pk_add_f32 v[86:87], v[62:63], v[86:87]
K 80,80,81
K 81,82,83
K 82,84,85
K 83,86,87
buffer_store_dwordx4 v[80:83], v20, s[20:23], s73 offen nt
K 160,88,89
K 161,90,91
K 162,92,93
K 163,94,95
v_lshlrev_b32 v88, 16, v160
v_and_b32 v89, 0xffff0000, v160
v_lshlrev_b32 v90, 16, v161
v_and_b32 v91, 0xffff0000, v161
v_lshlrev_b32 v92, 16, v162
v_and_b32 v93, 0xffff0000, v162
v_lshlrev_b32 v94, 16, v163
v_and_b32 v95, 0xffff0000, v163
v_pk_add_f32 v[88:89], v[64:65], v[88:89]
v_pk_add_f32 v[90:91], v[66:67], v[90:91]
v_pk_add_f32 v[92:93], v[68:69], v[92:93]
v_pk_add_f32 v[94:95], v[70:71], v[94:95]
K 160,88,89
K 161,90,91
K 162,92,93
K 163,94,95
v_lshlrev_b32 v88, 16, v160
v_and_b32 v89, 0xffff0000, v160
v_lshlrev_b32 v90, 16, v161
v_and_b32 v91, 0xffff0000, v161
v_lshlrev_b32 v92, 16, v162
v_and_b32 v93, 0xffff0000, v162
v_lshlrev_b32 v94, 16, v163
v_and_b32 v95, 0xffff0000, v163
v_pk_add_f32 v[88:89], v[72:73], v[88:89]
v_pk_add_f32 v[90:91], v[74:75], v[90:91]
v_pk_add_f32 v[92:93], v[76:77], v[92:93]
v_pk_add_f32 v[94:95], v[78:79], v[94:95]
K 88,88,89
K 89,90,91
K 90,92,93
K 91,94,95
buffer_store_dwordx4 v[88:91], v20, s[20:23], s74 offen nt
s_branch .LL_108%=
.LL_181%=:
s_waitcnt vmcnt(0)
Zvvv 80,4,208
Zvvv 84,8,208
Zvvv 88,4,128
Zvvv 92,8,128
Mvvv 80,4,212
Mvvv 84,8,212
Mvvv 88,4,132
Mvvv 92,8,132
Mvvv 80,4,216
Mvvv 84,8,216
Mvvv 88,4,136
Mvvv 92,8,136
Mvvv 80,4,220
Mvvv 84,8,220
Mvvv 88,4,140
Mvvv 92,8,140
Mvvv 80,4,224
Mvvv 84,8,224
Mvvv 88,4,144
Mvvv 92,8,144
Mvvv 80,4,228
Mvvv 84,8,228
Mvvv 88,4,148
Mvvv 92,8,148
Mvvv 80,4,232
Mvvv 84,8,232
Mvvv 88,4,152
Mvvv 92,8,152
Mvvv 80,4,236
Mvvv 84,8,236
Mvvv 88,4,156
Mvvv 92,8,156
s_nop 4
K 160,80,81
K 161,82,83
K 162,84,85
K 163,86,87
v_lshlrev_b32 v80, 16, v160
v_and_b32 v81, 0xffff0000, v160
v_lshlrev_b32 v82, 16, v161
v_and_b32 v83, 0xffff0000, v161
v_lshlrev_b32 v84, 16, v162
v_and_b32 v85, 0xffff0000, v162
v_lshlrev_b32 v86, 16, v163
v_and_b32 v87, 0xffff0000, v163
v_pk_add_f32 v[80:81], v[48:49], v[80:81]
v_pk_add_f32 v[82:83], v[50:51], v[82:83]
v_pk_add_f32 v[84:85], v[52:53], v[84:85]
v_pk_add_f32 v[86:87], v[54:55], v[86:87]
K 160,80,81
K 161,82,83
K 162,84,85
K 163,86,87
v_lshlrev_b32 v80, 16, v160
v_and_b32 v81, 0xffff0000, v160
v_lshlrev_b32 v82, 16, v161
v_and_b32 v83, 0xffff0000, v161
v_lshlrev_b32 v84, 16, v162
v_and_b32 v85, 0xffff0000, v162
v_lshlrev_b32 v86, 16, v163
v_and_b32 v87, 0xffff0000, v163
v_pk_add_f32 v[80:81], v[56:57], v[80:81]
v_pk_add_f32 v[82:83], v[58:59], v[82:83]
v_pk_add_f32 v[84:85], v[60:61], v[84:85]
v_pk_add_f32 v[86:87], v[62:63], v[86:87]
K 80,80,81
K 81,82,83
K 82,84,85
K 83,86,87
buffer_store_dwordx4 v[80:83], v20, s[20:23], s73 offen nt
K 160,88,89
K 161,90,91
K 162,92,93
K 163,94,95
v_lshlrev_b32 v88, 16, v160
v_and_b32 v89, 0xffff0000, v160
v_lshlrev_b32 v90, 16, v161
v_and_b32 v91, 0xffff0000, v161
v_lshlrev_b32 v92, 16, v162
v_and_b32 v93, 0xffff0000, v162
v_lshlrev_b32 v94, 16, v163
v_and_b32 v95, 0xffff0000, v163
v_pk_add_f32 v[88:89], v[64:65], v[88:89]
v_pk_add_f32 v[90:91], v[66:67], v[90:91]
v_pk_add_f32 v[92:93], v[68:69], v[92:93]
v_pk_add_f32 v[94:95], v[70:71], v[94:95]
K 160,88,89
K 161,90,91
K 162,92,93
K 163,94,95
v_lshlrev_b32 v88, 16, v160
v_and_b32 v89, 0xffff0000, v160
v_lshlrev_b32 v90, 16, v161
v_and_b32 v91, 0xffff0000, v161
v_lshlrev_b32 v92, 16, v162
v_and_b32 v93, 0xffff0000, v162
v_lshlrev_b32 v94, 16, v163
v_and_b32 v95, 0xffff0000, v163
v_pk_add_f32 v[88:89], v[72:73], v[88:89]
v_pk_add_f32 v[90:91], v[74:75], v[90:91]
v_pk_add_f32 v[92:93], v[76:77], v[92:93]
v_pk_add_f32 v[94:95], v[78:79], v[94:95]
K 88,88,89
K 89,90,91
K 90,92,93
K 91,94,95
buffer_store_dwordx4 v[88:91], v20, s[20:23], s74 offen nt
.LL_108%=:
s_lshl_b32 s80, s15, 2
s_add_u32 s80, s80, s14
s_cmp_eq_u32 s80, 4
s_cbranch_scc0 .LL_183%=
v_mov_b64 v[28:29], s[12:13]
s_mov_b64 exec, 1
buffer_store_dwordx2 v[28:29], off, s[8:11], 0 sc1
s_mov_b64 exec, -1
.LL_183%=:
s_branch .Lend_184%=
.Lup_101%=:
s_waitcnt vmcnt(5)
s_mov_b32 s84, 0
.LL_103%=:
v_min3_u32 v176, v161, v163, v165
v_max3_u32 v177, v161, v163, v165
v_min3_u32 v176, v176, v167, v169
v_max3_u32 v177, v177, v167, v169
v_min3_u32 v176, v176, v171, v173
v_max3_u32 v177, v177, v171, v173
v_min_u32 v176, v176, v175
v_max_u32 v177, v177, v175
v_cmp_eq_u32 vcc, s13, v176
v_cmp_eq_u32_e64 s[86:87], s13, v177
s_and_b64 vcc, vcc, s[86:87]
s_cmp_eq_u64 vcc, -1
s_cbranch_scc1 .LL_104%=
s_add_u32 s84, s84, 1
s_cmp_lt_u32 s84, 1048576
s_cbranch_scc1 .LL_105%=
.LL_104%=:
v_readlane_b32 s70, v12, s69
v_bfe_u32 v2, v1, 4, 4
s_nop 2
s_lshr_b32 s80, s70, 4
s_and_b32 s70, s70, 15
v_cmp_eq_u32 vcc, s70, v2
s_cmp_eq_u32 s80, s61
s_cselect_b64 s[86:87], vcc, 0
s_cselect_b64 s[88:89], 0, vcc
s_bitcmp1_b32 s14, 0
s_cselect_b64 vcc, s[88:89], s[86:87]
v_lshlrev_b32 v176, 16, v160
v_and_b32 v177, 0xffff0000, v160
v_lshlrev_b32 v178, 16, v162
v_and_b32 v179, 0xffff0000, v162
v_lshlrev_b32 v180, 16, v164
v_and_b32 v181, 0xffff0000, v164
v_lshlrev_b32 v182, 16, v166
v_and_b32 v183, 0xffff0000, v166
A 176,176,40
A 177,177,41
A 178,178,42
A 179,179,43
A 180,180,44
A 181,181,45
A 182,182,46
A 183,183,47
K 184,176,177
K 185,178,179
K 186,180,181
K 187,182,183
v_lshlrev_b32 v176, 16, v184
v_and_b32 v177, 0xffff0000, v184
v_lshlrev_b32 v178, 16, v185
v_and_b32 v179, 0xffff0000, v185
v_lshlrev_b32 v180, 16, v186
v_and_b32 v181, 0xffff0000, v186
v_lshlrev_b32 v182, 16, v187
v_and_b32 v183, 0xffff0000, v187
v_cndmask_b32_e64 v40, v40, v176, s[86:87]
v_cndmask_b32_e64 v41, v41, v177, s[86:87]
v_cndmask_b32_e64 v42, v42, v178, s[86:87]
v_cndmask_b32_e64 v43, v43, v179, s[86:87]
v_cndmask_b32_e64 v44, v44, v180, s[86:87]
v_cndmask_b32_e64 v45, v45, v181, s[86:87]
v_cndmask_b32_e64 v46, v46, v182, s[86:87]
v_cndmask_b32_e64 v47, v47, v183, s[86:87]
v_lshlrev_b32 v176, 16, v160
v_and_b32 v177, 0xffff0000, v160
v_lshlrev_b32 v178, 16, v162
v_and_b32 v179, 0xffff0000, v162
v_lshlrev_b32 v180, 16, v164
v_and_b32 v181, 0xffff0000, v164
v_lshlrev_b32 v182, 16, v166
v_and_b32 v183, 0xffff0000, v166
A 176,176,56
A 177,177,57
A 178,178,58
A 179,179,59
A 180,180,60
A 181,181,61
A 182,182,62
A 183,183,63
K 184,176,177
K 185,178,179
K 186,180,181
K 187,182,183
v_lshlrev_b32 v176, 16, v184
v_and_b32 v177, 0xffff0000, v184
v_lshlrev_b32 v178, 16, v185
v_and_b32 v179, 0xffff0000, v185
v_lshlrev_b32 v180, 16, v186
v_and_b32 v181, 0xffff0000, v186
v_lshlrev_b32 v182, 16, v187
v_and_b32 v183, 0xffff0000, v187
v_cndmask_b32_e64 v56, v56, v176, s[88:89]
v_cndmask_b32_e64 v57, v57, v177, s[88:89]
)"
R"(v_cndmask_b32_e64 v58, v58, v178, s[88:89]
v_cndmask_b32_e64 v59, v59, v179, s[88:89]
v_cndmask_b32_e64 v60, v60, v180, s[88:89]
v_cndmask_b32_e64 v61, v61, v181, s[88:89]
v_cndmask_b32_e64 v62, v62, v182, s[88:89]
v_cndmask_b32_e64 v63, v63, v183, s[88:89]
v_lshlrev_b32 v176, 16, v168
v_and_b32 v177, 0xffff0000, v168
v_lshlrev_b32 v178, 16, v170
v_and_b32 v179, 0xffff0000, v170
v_lshlrev_b32 v180, 16, v172
v_and_b32 v181, 0xffff0000, v172
v_lshlrev_b32 v182, 16, v174
v_and_b32 v183, 0xffff0000, v174
A 176,176,72
A 177,177,73
A 178,178,74
A 179,179,75
A 180,180,76
A 181,181,77
A 182,182,78
A 183,183,79
K 184,176,177
K 185,178,179
K 186,180,181
K 187,182,183
v_lshlrev_b32 v176, 16, v184
v_and_b32 v177, 0xffff0000, v184
v_lshlrev_b32 v178, 16, v185
v_and_b32 v179, 0xffff0000, v185
v_lshlrev_b32 v180, 16, v186
v_and_b32 v181, 0xffff0000, v186
v_lshlrev_b32 v182, 16, v187
v_and_b32 v183, 0xffff0000, v187
v_cndmask_b32_e64 v72, v72, v176, vcc
v_cndmask_b32_e64 v73, v73, v177, vcc
v_cndmask_b32_e64 v74, v74, v178, vcc
v_cndmask_b32_e64 v75, v75, v179, vcc
v_cndmask_b32_e64 v76, v76, v180, vcc
v_cndmask_b32_e64 v77, v77, v181, vcc
v_cndmask_b32_e64 v78, v78, v182, vcc
v_cndmask_b32_e64 v79, v79, v183, vcc
s_bitset0_b32 s68, s69
s_cmp_eq_u32 s68, 0
s_cbranch_scc1 .Lupr_102%=
s_ff1_i32_b32 s69, s68
.LL_105%=:
s_cmp_eq_u32 s64, 0
s_cbranch_scc0 .LL_106%=
s_waitcnt vmcnt(0)
v_lshlrev_b32 v2, 2, v1
v_mov_b64 v[28:29], s[12:13]
s_mov_b64 exec, 3
buffer_store_dwordx2 v[28:29], v2, s[8:11], s53 offen offset:512 sc1
s_mov_b64 exec, -1
s_mov_b32 s64, 1
.LL_106%=:
s_mul_i32 s66, s69, 20
s_mul_i32 s67, s58, 5
s_add_u32 s66, s66, s67
s_lshl_b32 s66, s66, 7
s_add_u32 s66, s66, 98304
s_lshl_b32 s67, s14, 7
s_add_u32 s67, s67, s66
buffer_load_dwordx4 v[160:163], v3, s[8:11], s67 offen offset:0 sc1
buffer_load_dwordx4 v[164:167], v3, s[8:11], s67 offen offset:16 sc1
buffer_load_dwordx4 v[168:171], v3, s[8:11], s66 offen offset:512 sc1
buffer_load_dwordx4 v[172:175], v3, s[8:11], s66 offen offset:528 sc1
s_waitcnt vmcnt(0)
s_branch .LL_103%=
.Lspin_54%=:
s_mov_b32 s84, 0
.Llp_56%=:
s_sleep 2
v_mov_b32 v28, s74
buffer_load_dwordx2 v[28:29], v28, s[8:11], 0 offen offset:3584 sc1
s_waitcnt vmcnt(0)
s_add_u32 s84, s84, 1
v_xor_b32 v28, s12, v28
v_xor_b32 v29, s13, v29
v_lshrrev_b32 v248, 11, v28
v_or_b32 v29, v29, v248
v_cmp_eq_u32 vcc, 0, v29
s_or_b64 vcc, vcc, s[72:73]
s_cmp_eq_u64 vcc, -1
s_cbranch_scc1 .Lok_55%=
s_cmp_lt_u32 s84, 1048576
s_cbranch_scc1 .Llp_56%=
s_branch .Lok_55%=
.Lspin_59%=:
s_mov_b32 s84, 0
.Llp_61%=:
s_sleep 2
v_bfe_u32 v28, v1, 4, 4
v_lshlrev_b32 v28, 3, v28
s_lshl_b32 s80, s56, 9
s_add_u32 s80, s80, 4096
buffer_load_dwordx2 v[28:29], v28, s[8:11], s80 offen sc1
s_waitcnt vmcnt(0)
s_add_u32 s84, s84, 1
v_cmp_eq_u64 vcc, s[12:13], v[28:29]
s_cmp_eq_u64 vcc, -1
s_cbranch_scc1 .Lok_60%=
s_cmp_lt_u32 s84, 1048576
s_cbranch_scc1 .Llp_61%=
s_branch .Lok_60%=
.Lspin_62%=:
s_mov_b32 s84, 0
.Llp_64%=:
s_sleep 2
v_bfe_u32 v28, v1, 4, 4
v_lshlrev_b32 v28, 3, v28
s_lshl_b32 s80, s56, 9
s_add_u32 s80, s80, 4224
buffer_load_dwordx2 v[28:29], v28, s[8:11], s80 offen sc1
s_waitcnt vmcnt(0)
s_add_u32 s84, s84, 1
v_cmp_eq_u64 vcc, s[12:13], v[28:29]
s_cmp_eq_u64 vcc, -1
s_cbranch_scc1 .Lok_63%=
s_cmp_lt_u32 s84, 1048576
s_cbranch_scc1 .Llp_64%=
s_branch .Lok_63%=
.Lspin_65%=:
s_mov_b32 s84, 0
.Llp_67%=:
s_sleep 2
v_bfe_u32 v28, v1, 4, 4
v_lshlrev_b32 v28, 3, v28
s_lshl_b32 s80, s56, 9
s_add_u32 s80, s80, 4352
buffer_load_dwordx2 v[28:29], v28, s[8:11], s80 offen sc1
s_waitcnt vmcnt(0)
s_add_u32 s84, s84, 1
v_cmp_eq_u64 vcc, s[12:13], v[28:29]
s_cmp_eq_u64 vcc, -1
s_cbranch_scc1 .Lok_66%=
s_cmp_lt_u32 s84, 1048576
s_cbranch_scc1 .Llp_67%=
s_branch .Lok_66%=
.Lspin_68%=:
s_mov_b32 s84, 0
.Llp_70%=:
s_sleep 2
v_bfe_u32 v28, v1, 4, 4
v_lshlrev_b32 v28, 3, v28
s_lshl_b32 s80, s56, 9
s_add_u32 s80, s80, 4480
buffer_load_dwordx2 v[28:29], v28, s[8:11], s80 offen sc1
s_waitcnt vmcnt(0)
s_add_u32 s84, s84, 1
v_cmp_eq_u64 vcc, s[12:13], v[28:29]
s_cmp_eq_u64 vcc, -1
s_cbranch_scc1 .Lok_69%=
s_cmp_lt_u32 s84, 1048576
s_cbranch_scc1 .Llp_70%=
s_branch .Lok_69%=
.Lspin_71%=:
s_mov_b32 s84, 0
.Llp_73%=:
s_sleep 2
buffer_load_dwordx2 v[8:9], v4, s[8:11], 0 offen offset:2560 sc1
buffer_load_dwordx2 v[10:11], v5, s[8:11], 0 offen offset:3072 sc1
buffer_load_dwordx2 v[12:13], v6, s[8:11], 0 offen offset:3584 sc1
s_waitcnt vmcnt(0)
s_add_u32 s84, s84, 1
v_cmp_eq_u64 vcc, s[12:13], v[8:9]
v_cmp_eq_u64_e64 s[86:87], s[12:13], v[10:11]
s_and_b64 vcc, vcc, s[86:87]
s_or_b64 vcc, vcc, s[20:21]
s_cmp_eq_u64 vcc, -1
s_cbranch_scc1 .Lok_72%=
s_cmp_lt_u32 s84, 1048576
s_cbranch_scc1 .Llp_73%=
s_branch .Lok_72%=
.Lmk_74%=:
s_nop 7
v_cndmask_b32_e64 v168, v241, v168, s[72:73]
v_cndmask_b32_e64 v169, v241, v169, s[74:75]
v_cndmask_b32_e64 v170, v241, v170, s[76:77]
v_cndmask_b32_e64 v171, v241, v171, s[78:79]
v_cndmask_b32_e64 v188, v241, v188, s[72:73]
v_cndmask_b32_e64 v189, v241, v189, s[74:75]
v_cndmask_b32_e64 v190, v241, v190, s[76:77]
v_cndmask_b32_e64 v191, v241, v191, s[78:79]
v_mov_b32 v184, v241
v_mov_b32 v185, v241
v_mov_b32 v186, v241
v_mov_b32 v187, v241
s_branch .Lmkr_75%=
.Lpd_78%=:
v_mov_b64 v[28:29], s[12:13]
s_mov_b64 exec, 1
buffer_store_dwordx2 v[28:29], off, s[8:11], s69 sc1
s_mov_b64 exec, -1
s_branch .Lpdr_79%=
.Lspin_80%=:
s_mov_b32 s84, 0
.Llp_82%=:
s_sleep 2
v_bfe_u32 v28, v1, 4, 2
v_lshlrev_b32 v28, 3, v28
buffer_load_dwordx2 v[28:29], v28, s[8:11], s55 offen sc1
s_waitcnt vmcnt(0)
s_add_u32 s84, s84, 1
v_cmp_eq_u64 vcc, s[12:13], v[28:29]
s_cmp_eq_u64 vcc, -1
s_cbranch_scc1 .Lok_81%=
s_cmp_lt_u32 s84, 1048576
s_cbranch_scc1 .Llp_82%=
s_branch .Lok_81%=
.LL_85%=:
s_mov_b32 s84, 0
.LL_87%=:
s_sleep 2
buffer_load_dwordx2 v[12:13], v27, s[8:11], 0 offen offset:3584 sc1
s_waitcnt vmcnt(0)
s_add_u32 s84, s84, 1
v_xor_b32 v12, s12, v12
v_xor_b32 v13, s13, v13
v_lshrrev_b32 v3, 11, v12
v_or_b32 v13, v13, v3
v_cmp_eq_u32 vcc, 0, v13
s_cmp_eq_u64 vcc, -1
s_cbranch_scc1 .LL_86%=
s_cmp_lt_u32 s84, 1048576
s_cbranch_scc1 .LL_87%=
s_branch .LL_86%=
.Lup_83%=:
v_lshrrev_b32 v3, 8, v1
v_lshlrev_b32 v3, 5, v3
s_ff1_i32_b32 s69, s68
s_mul_i32 s66, s69, 20
s_mul_i32 s67, s58, 5
s_add_u32 s66, s66, s67
s_lshl_b32 s66, s66, 7
s_add_u32 s66, s66, 98304
s_lshl_b32 s67, s14, 7
s_add_u32 s67, s67, s66
buffer_load_dwordx4 v[160:163], v3, s[8:11], s67 offen offset:0 sc1
buffer_load_dwordx4 v[164:167], v3, s[8:11], s67 offen offset:16 sc1
buffer_load_dwordx4 v[168:171], v3, s[8:11], s66 offen offset:512 sc1
buffer_load_dwordx4 v[172:175], v3, s[8:11], s66 offen offset:528 sc1
s_branch .Lupr_84%=
.Lpd_88%=:
v_mov_b64 v[28:29], s[12:13]
s_mov_b64 exec, 1
buffer_store_dwordx2 v[28:29], off, s[8:11], s69 sc1
s_mov_b64 exec, -1
s_branch .Lpdr_89%=
.Lspin_90%=:
s_mov_b32 s84, 0
.Llp_92%=:
s_sleep 2
v_bfe_u32 v28, v1, 4, 2
v_lshlrev_b32 v28, 3, v28
buffer_load_dwordx2 v[28:29], v28, s[8:11], s54 offen sc1
s_waitcnt vmcnt(0)
s_add_u32 s84, s84, 1
v_cmp_eq_u64 vcc, s[12:13], v[28:29]
s_cmp_eq_u64 vcc, -1
s_cbranch_scc1 .Lok_91%=
s_cmp_lt_u32 s84, 1048576
s_cbranch_scc1 .Llp_92%=
s_branch .Lok_91%=
.Lspin_93%=:
s_mov_b32 s84, 0
.Llp_95%=:
s_sleep 2
v_bfe_u32 v28, v1, 4, 2
v_lshlrev_b32 v28, 3, v28
buffer_load_dwordx2 v[28:29], v28, s[8:11], s55 offen sc1
s_waitcnt vmcnt(0)
s_add_u32 s84, s84, 1
v_cmp_eq_u64 vcc, s[12:13], v[28:29]
s_cmp_eq_u64 vcc, -1
s_cbranch_scc1 .Lok_94%=
s_cmp_lt_u32 s84, 1048576
s_cbranch_scc1 .Llp_95%=
s_branch .Lok_94%=
.LL_98%=:
s_mov_b32 s84, 0
.LL_100%=:
s_sleep 2
buffer_load_dwordx2 v[12:13], v27, s[8:11], 0 offen offset:3584 sc1
s_waitcnt vmcnt(0)
s_add_u32 s84, s84, 1
v_xor_b32 v12, s12, v12
v_xor_b32 v13, s13, v13
v_lshrrev_b32 v3, 11, v12
v_or_b32 v13, v13, v3
v_cmp_eq_u32 vcc, 0, v13
s_cmp_eq_u64 vcc, -1
s_cbranch_scc1 .LL_99%=
s_cmp_lt_u32 s84, 1048576
s_cbranch_scc1 .LL_100%=
s_branch .LL_99%=
.Lup_96%=:
v_lshrrev_b32 v3, 8, v1
v_lshlrev_b32 v3, 5, v3
s_ff1_i32_b32 s69, s68
s_mul_i32 s66, s69, 20
s_mul_i32 s67, s58, 5
s_add_u32 s66, s66, s67
s_lshl_b32 s66, s66, 7
s_add_u32 s66, s66, 98304
s_lshl_b32 s67, s14, 7
s_add_u32 s67, s67, s66
buffer_load_dwordx4 v[160:163], v3, s[8:11], s67 offen offset:0 sc1
buffer_load_dwordx4 v[164:167], v3, s[8:11], s67 offen offset:16 sc1
buffer_load_dwordx4 v[168:171], v3, s[8:11], s66 offen offset:512 sc1
buffer_load_dwordx4 v[172:175], v3, s[8:11], s66 offen offset:528 sc1
s_branch .Lupr_97%=
.Lend_184%=:
.Lend%=:
.purgem W
.purgem D
.purgem Ra
.purgem Ta
.purgem Zvaa
.purgem Zaaa
.purgem A
.purgem U
.purgem Mvaa
.purgem Maaa
.purgem K
.purgem Rv
.purgem Mvav
.purgem G
.purgem Dc
.purgem Zvav
.purgem Cvav
.purgem X
.purgem Cvaa
.purgem Maav
.purgem Mvvv
.purgem Zvvv
)"
    : : [ka] "s"(__builtin_amdgcn_kernarg_segment_ptr()), [bid] "s"(blockIdx.x), [tid] "v"(threadIdx.x)
    : "v1", "v2", "v3", "v4", "v5", "v6", "v7", "v8", "v9", "v10", "v11", "v12", "v13", "v14", "v15", "v16", "v17", "v18", "v19", "v20", "v21", "v22", "v23", "v24", "v25", "v26", "v27", "v28", "v29", "v30", "v31", "v32", "v33", "v34", "v35", "v36", "v37", "v38", "v39", "v40", "v41", "v42", "v43", "v44", "v45", "v46", "v47", "v48", "v49", "v50", "v51", "v52", "v53", "v54", "v55", "v56", "v57", "v58", "v59", "v60", "v61", "v62", "v63", "v64", "v65", "v66", "v67", "v68", "v69", "v70", "v71", "v72", "v73", "v74", "v75", "v76", "v77", "v78", "v79", "v80", "v81", "v82", "v83", "v84", "v85", "v86", "v87", "v88", "v89", "v90", "v91", "v92", "v93", "v94", "v95", "v96", "v97", "v98", "v99", "v100", "v101", "v102", "v103", "v104", "v105", "v106", "v107", "v108", "v109", "v110", "v111", "v112", "v113", "v114", "v115", "v116", "v117", "v118", "v119", "v120", "v121", "v122", "v123", "v124", "v125", "v126", "v127", "v128", "v129", "v130", "v131", "v132", "v133", "v134", "v135", "v136", "v137", "v138", "v139", "v140", "v141", "v142", "v143", "v144", "v145", "v146", "v147", "v148", "v149", "v150", "v151", "v152", "v153", "v154", "v155", "v156", "v157", "v158", "v159", "v160", "v161", "v162", "v163", "v164", "v165", "v166", "v167", "v168", "v169", "v170", "v171", "v172", "v173", "v174", "v175", "v176", "v177", "v178", "v179", "v180", "v181", "v182", "v183", "v184", "v185", "v186", "v187", "v188", "v189", "v190", "v191", "v192", "v193", "v194", "v195", "v196", "v197", "v198", "v199", "v200", "v201", "v202", "v203", "v204", "v205", "v206", "v207", "v208", "v209", "v210", "v211", "v212", "v213", "v214", "v215", "v216", "v217", "v218", "v219", "v220", "v221", "v222", "v223", "v224", "v225", "v226", "v227", "v228", "v229", "v230", "v231", "v232", "v233", "v234", "v235", "v236", "v237", "v238", "v239", "v240", "v241", "v242", "v243", "v244", "v245", "v246", "v247", "v248", "v249", "v250", "v251", "v252", "v253", "v254", "v255", "a0", "a1", "a2", "a3", "a4", "a5", "a6", "a7", "a8", "a9", "a10", "a11", "a12", "a13", "a14", "a15", "a16", "a17", "a18", "a19", "a20", "a21", "a22", "a23", "a24", "a25", "a26", "a27", "a28", "a29", "a30", "a31", "a32", "a33", "a34", "a35", "a36", "a37", "a38", "a39", "a40", "a41", "a42", "a43", "a44", "a45", "a46", "a47", "a48", "a49", "a50", "a51", "a52", "a53", "a54", "a55", "a56", "a57", "a58", "a59", "a60", "a61", "a62", "a63", "a64", "a65", "a66", "a67", "a68", "a69", "a70", "a71", "a72", "a73", "a74", "a75", "a76", "a77", "a78", "a79", "a80", "a81", "a82", "a83", "a84", "a85", "a86", "a87", "a88", "a89", "a90", "a91", "a92", "a93", "a94", "a95", "a96", "a97", "a98", "a99", "a100", "a101", "a102", "a103", "a104", "a105", "a106", "a107", "a108", "a109", "a110", "a111", "a112", "a113", "a114", "a115", "a116", "a117", "a118", "a119", "a120", "a121", "a122", "a123", "a124", "a125", "a126", "a127", "a128", "a129", "a130", "a131", "a132", "a133", "a134", "a135", "a136", "a137", "a138", "a139", "a140", "a141", "a142", "a143", "a144", "a145", "a146", "a147", "a148", "a149", "a150", "a151", "a152", "a153", "a154", "a155", "a156", "a157", "a158", "a159", "a160", "a161", "a162", "a163", "a164", "a165", "a166", "a167", "a168", "a169", "a170", "a171", "a172", "a173", "a174", "a175", "a176", "a177", "a178", "a179", "a180", "a181", "a182", "a183", "a184", "a185", "a186", "a187", "a188", "a189", "a190", "a191", "a192", "a193", "a194", "a195", "a196", "a197", "a198", "a199", "a200", "a201", "a202", "a203", "a204", "a205", "a206", "a207", "a208", "a209", "a210", "a211", "a212", "a213", "a214", "a215", "a216", "a217", "a218", "a219", "a220", "a221", "a222", "a223", "a224", "a225", "a226", "a227", "a228", "a229", "a230", "a231", "a232", "a233", "a234", "a235", "a236", "a237", "a238", "a239", "a240", "a241", "a242", "a243", "a244", "a245", "a246", "a247", "a248", "a249", "a250", "a251", "a252", "a253", "a254", "a255", "s4", "s5", "s6", "s7", "s8", "s9", "s10", "s11", "s12", "s13", "s14", "s15", "s16", "s17", "s18", "s19", "s20", "s21", "s22", "s23", "s24", "s25", "s26", "s27", "s28", "s29", "s30", "s31", "s32", "s33", "s34", "s35", "s36", "s37", "s38", "s39", "s40", "s41", "s42", "s43", "s44", "s45", "s46", "s47", "s48", "s49", "s50", "s51", "s52", "s53", "s54", "s55", "s56", "s57", "s58", "s59", "s60", "s61", "s62", "s63", "s64", "s65", "s66", "s67", "s68", "s69", "s70", "s71", "s72", "s73", "s74", "s75", "s76", "s77", "s78", "s79", "s80", "s81", "s82", "s83", "s84", "s85", "s86", "s87", "s88", "s89", "s90", "s91", "s92", "s93", "s94", "s95", "s96", "s97", "s98", "s99", "vcc", "scc", "memory"
  );
}
extern "C" void kernel_launch(void* const* d_in, const int* in_sizes, int n_in, void* d_out, int out_size, void* d_ws, size_t ws_size, hipStream_t stream) {
  (void)hipFuncSetAttribute((const void*)mega_kernel, hipFuncAttributeMaxDynamicSharedMemorySize, LDS_BYTES);
  mega_kernel<<<256, 256, LDS_BYTES, stream>>>((const bf16*)d_in[0], (const bf16*)d_in[1], (const bf16*)d_in[2], (const bf16*)d_in[3], (const bf16*)d_in[4], (const bf16*)d_in[5],
      (const bf16*)d_in[7], (const bf16*)d_in[8], (const bf16*)d_in[9], (const bf16*)d_in[10], (char*)d_ws + 0, (bf16*)d_out);
}
```

```cpp
#include <hip/hip_runtime.h>
#include <hip/hip_bf16.h>
typedef __hip_bfloat16 bf16;
constexpr int LDS_BYTES = 163840;
__global__ __launch_bounds__(256, 1) void mega_kernel(const bf16* x, const bf16* ckv, const bf16* wq, const bf16* wuk, const bf16* wuv, const bf16* wo,
    const bf16* w_r, const bf16* wg, const bf16* wu, const bf16* wd, char* ws, bf16* out) {
  asm volatile(
R"(.macro W n
s_waitcnt lgkmcnt(\n)
.endm
.macro D v,r,s,o
buffer_load_dwordx4 v\v, s[\r:\r+3], s\s offen offset:\o lds
.endm
.macro Ta d,a,o
ds_read_b64_tr_b16 a[\d:\d+1], v\a offset:\o
.endm
.macro Ra d,a,o
ds_read_b128 a[\d:\d+3], v\a offset:\o
.endm
.macro Zvaa d,a,b
v_mfma_f32_16x16x32_bf16 v[\d:\d+3], a[\a:\a+3], a[\b:\b+3], 0
.endm
.macro Mvaa d,a,b
v_mfma_f32_16x16x32_bf16 v[\d:\d+3], a[\a:\a+3], a[\b:\b+3], v[\d:\d+3]
.endm
.macro Zaaa d,a,b
v_mfma_f32_16x16x32_bf16 a[\d:\d+3], a[\a:\a+3], a[\b:\b+3], 0
.endm
.macro Maaa d,a,b
v_mfma_f32_16x16x32_bf16 a[\d:\d+3], a[\a:\a+3], a[\b:\b+3], a[\d:\d+3]
.endm
.macro A d,a,b
v_add_f32 v\d, v\a, v\b
.endm
.macro U d,a,b
v_mul_f32 v\d, v\a, v\b
.endm
.macro K d,a,b
v_cvt_pk_bf16_f32 v\d, v\a, v\b
.endm
.macro Rv d,a,o
ds_read_b128 v[\d:\d+3], v\a offset:\o
.endm
.macro Mvav d,a,b
v_mfma_f32_16x16x32_bf16 v[\d:\d+3], a[\a:\a+3], v[\b:\b+3], v[\d:\d+3]
.endm
.macro G d,a
v_accvgpr_read_b32 v\d, a\a
.endm
.macro Dc v,r,s,o
buffer_load_dwordx4 v\v, s[\r:\r+3], s\s offen offset:\o sc1 lds
.endm
.macro Zvav d,a,b
v_mfma_f32_16x16x32_bf16 v[\d:\d+3], a[\a:\a+3], v[\b:\b+3], 0
.endm
.macro Cvav d,a,b,c
v_mfma_f32_16x16x32_bf16 v[\d:\d+3], a[\a:\a+3], v[\b:\b+3], v[\c:\c+3]
.endm
.macro X d
v_exp_f32 v\d, v\d
.endm
.macro Cvaa d,a,b,c
v_mfma_f32_16x16x32_bf16 v[\d:\d+3], a[\a:\a+3], a[\b:\b+3], v[\c:\c+3]
.endm
.macro Maav d,a,b
v_mfma_f32_16x16x32_bf16 a[\d:\d+3], a[\a:\a+3], v[\b:\b+3], a[\d:\d+3]
.endm
.macro Mvvv d,a,b
v_mfma_f32_16x16x32_bf16 v[\d:\d+3], v[\a:\a+3], v[\b:\b+3], v[\d:\d+3]
.endm
.macro Zvvv d,a,b
v_mfma_f32_16x16x32_bf16 v[\d:\d+3], v[\a:\a+3], v[\b:\b+3], 0
.endm
s_load_dwordx16 s[16:31], %[ka], 0
s_load_dwordx8 s[32:39], %[ka], 64
s_getpc_b64 s[6:7]
.Lbeg%=:
s_mov_b32 s15, %[bid]
v_lshrrev_b32 v2, 6, %[tid]
v_and_b32 v100, 63, %[tid]
v_readfirstlane_b32 s14, v2
v_lshlrev_b32 v1, 4, v100
v_lshrrev_b32 v101, 4, v100
v_and_b32 v102, 15, v100
s_and_b32 s59, s15, 7
s_bfe_u32 s57, s15, 0x30003
s_lshr_b32 s58, s15, 6
s_lshl_b32 s56, s59, 2
s_add_u32 s56, s56, s58
s_lshr_b32 s80, s15, 3
s_lshl_b32 s80, s80, 2
s_add_u32 s80, s80, s14
s_lshl_b32 s80, s80, 11
v_min_u32 v104, 240, v1
v_lshl_add_u32 v104, v104, 3, s80
v_min_u32 v104, .Lend%=-.Lbeg%=-4, v104
global_load_dword v142, v104, s[6:7]
v_and_b32 v88, 0x1f0, v1
v_lshrrev_b32 v105, 1, v101
v_mul_u32_u24 v105, 1280, v105
v_add_u32 v28, v88, v105
s_lshl_b32 s85, s15, 2
s_add_u32 s85, s85, s14
s_mul_i32 s84, s85, 2560
v_and_b32 v105, 1, v101
v_lshl_or_b32 v29, v105, 31, v28
v_lshlrev_b32 v104, 1, v101
v_xor_b32 v104, v104, v102
v_lshlrev_b32 v104, 4, v104
v_lshl_add_u32 v16, v101, 13, v104
v_xor_b32 v105, 128, v16
v_add_u32 v17, 31744, v16
v_add_u32 v18, 63488, v105
v_add_u32 v19, 95232, v105
v_add_u32 v20, 131072, v16
v_add_u32 v21, 162816, v16
v_add_u32 v22, 194560, v105
v_add_u32 v23, 226304, v105
s_lshl_b32 s80, s14, 3
s_add_u32 s80, s80, s57
s_lshl_b32 s80, s80, 15
v_and_b32 v104, 7, v100
v_xor_b32 v104, v104, v101
v_lshlrev_b32 v104, 4, v104
v_lshrrev_b32 v105, 3, v100
v_lshl_add_u32 v104, v105, 10, v104
v_add_u32 v2, s80, v104
v_xor_b32 v105, 64, v2
v_add_u32 v3, 7168, v105
v_add_u32 v4, 14336, v2
v_add_u32 v5, 21504, v105
v_xor_b32 v6, 1835008, v2
v_xor_b32 v7, 1835008, v3
v_xor_b32 v8, 1835008, v4
v_xor_b32 v9, 1835008, v5
s_lshl_b32 s63, s14, 13
s_add_u32 s61, s63, 65536
s_and_b32 s80, s14, 1
s_lshr_b32 s81, s14, 1
s_lshl_b32 s62, s80, 14
s_lshl_b32 s82, s81, 13
s_add_u32 s62, s62, s82
s_lshl_b32 s81, s81, 18
s_lshl_b32 s83, s56, 8
s_add_u32 s81, s81, s83
s_mov_b32 s64, 0
s_cmp_eq_u32 s80, 0
W 0
s_cselect_b32 s44, s22, s24
s_cselect_b32 s45, s23, s25
s_cmp_eq_u32 s14, 3
s_cbranch_scc0 .LL_1%=
s_lshr_b32 s88, s15, 3
s_cmp_lt_u32 s88, 14
s_cbranch_scc0 .LL_1%=
v_lshlrev_b32 v104, 16, v1
s_mov_b64 s[86:87], s[36:37]
s_mov_b32 s89, 38797312
s_cmp_eq_u32 s88, 1
s_cselect_b32 s90, 39845888, 0
s_add_u32 s86, s86, s90
s_addc_u32 s87, s87, 0
s_cmp_eq_u32 s88, 2
s_cselect_b64 s[86:87], s[26:27], s[86:87]
s_cselect_b32 s89, 5242876, s89
s_cmp_eq_u32 s88, 3
s_cselect_b64 s[86:87], s[30:31], s[86:87]
s_cselect_b32 s89, 2211836, s89
s_cmp_eq_u32 s88, 4
s_cselect_b64 s[86:87], s[32:33], s[86:87]
s_cselect_b32 s89, 2211836, s89
s_cmp_eq_u32 s88, 5
s_cselect_b64 s[86:87], s[34:35], s[86:87]
s_cselect_b32 s89, 2211836, s89
s_cmp_eq_u32 s88, 6
s_cselect_b64 s[86:87], s[38:39], s[86:87]
s_cselect_b32 s89, 2621436, s89
s_cmp_eq_u32 s88, 7
s_cselect_b64 s[86:87], s[20:21], s[86:87]
s_cselect_b32 s89, 5242876, s89
s_cmp_eq_u32 s88, 8
s_cselect_b64 s[86:87], s[18:19], s[86:87]
s_cselect_b32 s89, 2097148, s89
s_cmp_eq_u32 s88, 9
s_cselect_b64 s[86:87], s[22:23], s[86:87]
s_cselect_b32 s89, 4194300, s89
s_cmp_eq_u32 s88, 10
s_cselect_b64 s[86:87], s[24:25], s[86:87]
s_cselect_b32 s89, 4194300, s89
s_cmp_eq_u32 s88, 11
s_cselect_b64 s[86:87], s[16:17], s[86:87]
s_cselect_b32 s89, 2621436, s89
s_cmp_eq_u32 s88, 12
s_cselect_b64 s[86:87], s[28:29], s[86:87]
s_cselect_b32 s89, 1276, s89
s_cmp_eq_u32 s88, 13
s_cselect_b64 s[86:87], s[6:7], s[86:87]
s_cselect_b32 s89, 0, s89
v_min_u32 v104, s89, v104
global_load_dword v142, v104, s[86:87] sc1
.LL_1%=:
s_mov_b32 s40, s18
s_and_b32 s41, s19, 0xffff
s_mov_b32 s42, 0x200000
s_mov_b32 s43, 0x20000
s_add_u32 s44, s44, s81
s_addc_u32 s45, s45, 0
s_and_b32 s45, s45, 0xffff
s_mov_b32 s46, 0x400000
s_mov_b32 s47, 0x20000
s_sub_u32 s46, s46, s81
s_load_dwordx2 s[12:13], s[36:37], 0
s_add_u32 m0, s61, 0
s_nop 0
D 2,40,64,0
D 3,40,64,1024
D 4,40,64,2048
D 5,40,64,3072
s_add_u32 m0, s61, 4096
s_nop 0
D 6,40,64,0
D 7,40,64,1024
D 8,40,64,2048
D 9,40,64,3072
s_lshl_b32 s65, s64, 12
s_add_u32 m0, s62, 0
s_nop 0
D 16,44,65,0
D 17,44,65,1024
D 18,44,65,2048
D 19,44,65,3072
s_add_u32 m0, s62, 4096
s_nop 0
D 20,44,65,0
D 21,44,65,1024
D 22,44,65,2048
D 23,44,65,3072
s_mov_b32 s52, s16
s_and_b32 s53, s17, 0xffff
s_mov_b32 s54, 2621440
s_mov_b32 s55, 0x20000
buffer_load_dwordx4 v[32:35], v28, s[52:55], s84 offen
buffer_load_dwordx4 v[36:39], v28, s[52:55], s84 offen offset:512
buffer_load_dwordx4 v[40:43], v29, s[52:55], s84 offen offset:1024
s_mov_b32 s52, s28
s_and_b32 s53, s29, 0xffff
s_mov_b32 s54, 1280
s_mov_b32 s55, 0x20000
buffer_load_dwordx4 v[44:47], v88, s[52:55], 0 offen offset:0
buffer_load_dwordx4 v[48:51], v88, s[52:55], 0 offen offset:512
buffer_load_dwordx4 v[52:55], v88, s[52:55], 0 offen offset:1024
s_mov_b32 s69, 0
s_cmp_eq_u32 s14, 2
s_cbranch_scc1 .LL_2%=
s_cmp_eq_u32 s14, 3
s_cbranch_scc1 .LL_3%=
s_lshl_b32 s80, s58, 3
s_add_u32 s80, s80, s57
s_lshl_b32 s80, s80, 15
s_lshl_b32 s82, s14, 7
s_add_u32 s80, s80, s82
s_add_u32 s80, s80, 256
v_lshl_add_u32 v104, v88, 6, s80
v_and_b32 v105, 512, v1
v_mul_u32_u24 v105, 3584, v105
v_xor_b32 v104, v104, v105
v_mov_b32 v140, s18
v_mov_b32 v141, s19
s_mov_b32 s68, 256
s_branch .LL_4%=
.LL_2%=:
v_lshrrev_b32 v104, 6, v1
v_lshlrev_b32 v104, 13, v104
v_and_b32 v105, 16, v1
v_lshl_add_u32 v104, v105, 3, v104
s_lshl_b32 s80, s57, 17
s_add_u32 s80, s80, s83
s_add_u32 s80, s80, 1048576
v_add_u32 v104, s80, v104
v_and_b32 v105, 32, v1
v_cmp_eq_u32 vcc, 0, v105
v_mov_b32 v140, s24
v_mov_b32 v141, s25
v_mov_b32 v105, s22
v_mov_b32 v106, s23
v_cndmask_b32 v140, v140, v105, vcc
v_cndmask_b32 v141, v141, v106, vcc
s_mov_b32 s68, 1048576
s_branch .LL_4%=
.LL_3%=:
v_lshrrev_b32 v104, 5, v1
v_lshlrev_b32 v104, 13, v104
v_and_b32 v105, 16, v1
v_lshl_add_u32 v104, v105, 3, v104
s_mul_i32 s80, s57, 655360
s_add_u32 s80, s80, s83
v_add_u32 v104, s80, v104
v_mov_b32 v140, s20
v_mov_b32 v141, s21
s_mov_b32 s68, 196608
.LL_4%=:
v_mov_b32 v105, 0
v_lshl_add_u64 v[140:141], v[104:105], 0, v[140:141]
s_add_u32 s64, s64, 128
s_and_b32 s64, s64, 1023
s_add_u32 m0, s61, 32768
s_nop 0
D 2,40,64,0
D 3,40,64,1024
D 4,40,64,2048
D 5,40,64,3072
s_add_u32 m0, s61, 36864
s_nop 0
D 6,40,64,0
D 7,40,64,1024
D 8,40,64,2048
D 9,40,64,3072
s_lshl_b32 s65, s64, 12
v_lshrrev_b32 v104, 1, v102
v_xor_b32 v104, v104, v101
v_lshlrev_b32 v104, 4, v104
v_lshl_add_u32 v104, v102, 7, v104
s_lshr_b32 s81, s14, 1
s_lshl_b32 s81, s81, 14
s_add_u32 s81, s81, 65536
v_add_u32 v26, s81, v104
v_xor_b32 v27, 64, v26
v_and_b32 v105, 1, v101
v_lshrrev_b32 v104, 2, v102
v_lshl_or_b32 v105, v105, 2, v104
v_lshl_add_u32 v104, v101, 3, v104
v_lshlrev_b32 v104, 8, v104
v_lshl_add_u32 v104, v105, 5, v104
v_and_b32 v105, 3, v102
v_lshl_add_u32 v10, v105, 3, v104
v_xor_b32 v11, 32, v10
v_xor_b32 v12, 64, v10
v_xor_b32 v13, 96, v10
v_xor_b32 v14, 128, v10
v_xor_b32 v15, 160, v10
v_xor_b32 v24, 192, v10
v_xor_b32 v25, 224, v10
.LL_8%=:
s_lshr_b32 s81, s15, 3
s_cmp_eq_u32 s14, 2
s_cbranch_scc1 .LL_5%=
s_cmp_eq_u32 s14, 3
s_cbranch_scc1 .LL_6%=
s_cmp_eq_u32 s14, 0
s_cselect_b64 s[76:77], s[30:31], s[32:33]
s_mov_b32 s72, 3456
s_mov_b32 s73, 28
s_mul_i32 s74, s81, 69120
s_mul_i32 s82, s59, 448
s_andn2_b32 s82, s82, 127
s_add_u32 s74, s74, s82
s_mov_b32 s75, 2211836
s_branch .LL_7%=
.LL_5%=:
s_mov_b64 s[76:77], s[34:35]
s_mov_b32 s72, 512
s_mov_b32 s73, 24
s_mul_i32 s74, s81, 8960
s_mul_i32 s82, s59, 286720
s_add_u32 s74, s74, s82
s_mov_b32 s75, 2211836
s_branch .LL_7%=
.LL_6%=:
s_mov_b64 s[76:77], s[26:27]
s_mov_b32 s72, 512
s_mov_b32 s73, 4
s_mul_i32 s74, s81, 20480
s_mul_i32 s82, s59, 655360
s_add_u32 s74, s74, s82
s_mov_b32 s75, 5242876
s_branch .LL_7%=
.LL_7%=:
s_mov_b32 s8, s36
s_and_b32 s9, s37, 0xffff
s_mov_b32 s10, 79691776
s_mov_b32 s11, 0x20000
s_add_u32 s48, s20, s83
s_addc_u32 s49, s21, 0
s_and_b32 s49, s49, 0xffff
s_mov_b32 s50, 5242880
s_mov_b32 s51, 0x20000
s_sub_u32 s50, s50, s83
s_lshl_b32 s80, s57, 1
s_and_b32 s81, s14, 1
s_add_u32 s80, s80, s81
s_sub_u32 s81, 31, s80
s_cmp_lt_u32 s14, 2
s_cselect_b32 s60, s80, s81
s_mul_i32 s28, s60, 81920
s_add_u32 s28, s28, 131072
s_add_u32 s4, s28, 10240
s_mov_b32 s80, 0
s_lshl_b32 s81, s80, 9
s_add_u32 s28, s28, s81
s_add_u32 s5, s80, s14
s_cmp_ge_u32 s5, 20
s_cselect_b32 s81, 20, 0
s_sub_u32 s5, s5, s81
s_and_b32 s80, s14, 1
s_mul_i32 s29, s80, 17172480
s_mul_i32 s80, s56, 536576
s_add_u32 s29, s29, s80
s_lshl_b32 s80, s57, 13
s_add_u32 s29, s29, s80
s_add_u32 s29, s29, 4194304
W 0
s_add_u32 s12, s12, 0x7f4a7c15
s_addc_u32 s13, s13, 0x9e3779b9
s_bitcmp1_b32 s14, 0
s_cbranch_scc1 .Lv_9%=
s_waitcnt vmcnt(14)
s_barrier
Ta 0,10,0
Ta 2,10,1024
Ra 32,26,0
Ta 4,10,8192
Ta 6,10,9216
Ra 128,27,0
Ra 132,27,2048
Ra 36,26,2048
Ra 40,26,4096
Ra 136,27,4096
Ra 140,27,6144
Ra 44,26,6144
Ra 48,26,8192
Ra 144,27,8192
W 6
Zvaa 160,0,32
s_add_u32 m0, s62, 32768
Ra 148,27,10240
Ra 52,26,10240
Ra 56,26,12288
Ra 152,27,12288
Ra 156,27,14336
Ra 60,26,14336
Mvaa 160,4,128
Ta 8,11,0
Ta 10,11,1024
Ta 12,11,8192
Ta 14,11,9216
Zvaa 192,4,132
Ta 16,12,0
Ta 18,12,1024
Mvaa 192,0,36
W 14
Ta 20,12,8192
)"
R"(Ta 22,12,9216
Zvaa 224,0,40
D 16,44,65,0
Mvaa 224,4,136
Zaaa 160,4,140
Maaa 160,0,44
W 12
Zaaa 192,0,48
D 17,44,65,1024
Maaa 192,4,144
Zaaa 224,4,148
s_waitcnt vmcnt(13)
Maaa 224,0,52
W 8
v_lshlrev_b32 v56, 16, v32
v_and_b32 v57, 0xffff0000, v32
v_lshlrev_b32 v58, 16, v33
v_and_b32 v59, 0xffff0000, v33
Zaaa 64,0,56
D 18,44,65,2048
v_lshlrev_b32 v60, 16, v34
v_and_b32 v61, 0xffff0000, v34
v_lshlrev_b32 v62, 16, v35
v_and_b32 v63, 0xffff0000, v35
Maaa 64,4,152
v_lshlrev_b32 v64, 16, v36
v_and_b32 v65, 0xffff0000, v36
v_lshlrev_b32 v66, 16, v37
v_and_b32 v67, 0xffff0000, v37
v_lshlrev_b32 v68, 16, v38
Zaaa 96,4,156
v_and_b32 v69, 0xffff0000, v38
v_lshlrev_b32 v70, 16, v39
v_and_b32 v71, 0xffff0000, v39
v_lshlrev_b32 v72, 16, v40
Maaa 96,0,60
W 4
v_and_b32 v73, 0xffff0000, v40
v_lshlrev_b32 v74, 16, v41
v_and_b32 v75, 0xffff0000, v41
v_lshlrev_b32 v76, 16, v42
v_and_b32 v77, 0xffff0000, v42
Zaaa 100,8,60
D 19,44,65,3072
v_lshlrev_b32 v78, 16, v43
v_and_b32 v79, 0xffff0000, v43
A 80,56,57
A 81,58,59
Maaa 100,12,156
s_add_u32 m0, s62, 36864
A 80,80,60
A 81,81,61
A 80,80,62
A 81,81,63
A 80,80,64
Zaaa 68,12,152
Ta 24,13,0
Ta 26,13,1024
A 81,81,65
A 80,80,66
A 81,81,67
A 80,80,68
Maaa 68,8,56
Ta 28,13,8192
Ta 30,13,9216
A 81,81,69
A 80,80,70
A 81,81,71
A 80,80,72
Zaaa 228,8,52
D 20,44,65,0
A 81,81,73
A 80,80,74
A 81,81,75
A 80,80,76
A 81,81,77
Maaa 228,12,148
A 80,80,78
A 81,81,79
A 80,80,81
s_nop 1
v_add_f32_dpp v80, v80, v80 quad_perm:[1,0,3,2] row_mask:0xf bank_mask:0xf
Zaaa 196,12,144
Maaa 196,8,48
v_add_f32_dpp v80, v80, v80 quad_perm:[2,3,0,1] row_mask:0xf bank_mask:0xf
s_nop 1
v_add_f32_dpp v80, v80, v80 row_half_mirror row_mask:0xf bank_mask:0xf
s_nop 1
v_add_f32_dpp v80, v80, v80 row_mirror row_mask:0xf bank_mask:0xf
v_mov_b32 v81, v80
s_nop 1
v_permlane16_swap_b32 v80, v81
A 80,80,81
Zaaa 164,8,44
D 21,44,65,1024
v_mul_f32 v84, 0x3acccccd, v80
s_mov_b32 s86, 0xffff
s_mov_b32 s87, 0xffff
v_sub_f32 v82, v56, v84
U 80,82,82
v_sub_f32 v83, v57, v84
Maaa 164,12,140
v_fmac_f32 v80, v83, v83
v_sub_f32 v82, v58, v84
v_fmac_f32 v80, v82, v82
v_sub_f32 v83, v59, v84
v_fmac_f32 v80, v83, v83
v_sub_f32 v82, v60, v84
Zvaa 228,12,136
v_fmac_f32 v80, v82, v82
v_sub_f32 v83, v61, v84
v_fmac_f32 v80, v83, v83
v_sub_f32 v82, v62, v84
v_fmac_f32 v80, v82, v82
Mvaa 228,8,40
v_sub_f32 v83, v63, v84
v_fmac_f32 v80, v83, v83
v_sub_f32 v82, v64, v84
v_fmac_f32 v80, v82, v82
Zvaa 196,8,36
D 22,44,65,2048
v_sub_f32 v83, v65, v84
v_fmac_f32 v80, v83, v83
v_sub_f32 v82, v66, v84
v_fmac_f32 v80, v82, v82
Mvaa 196,12,132
v_sub_f32 v83, v67, v84
v_fmac_f32 v80, v83, v83
v_sub_f32 v82, v68, v84
v_fmac_f32 v80, v82, v82
v_sub_f32 v83, v69, v84
Zvaa 164,12,128
v_fmac_f32 v80, v83, v83
v_sub_f32 v82, v70, v84
v_fmac_f32 v80, v82, v82
v_sub_f32 v83, v71, v84
Mvaa 164,8,32
W 4
v_fmac_f32 v80, v83, v83
v_sub_f32 v82, v72, v84
U 81,82,82
v_sub_f32 v83, v73, v84
v_fmac_f32 v81, v83, v83
Zvaa 168,16,32
D 23,44,65,3072
v_sub_f32 v82, v74, v84
v_fmac_f32 v81, v82, v82
v_sub_f32 v83, v75, v84
v_fmac_f32 v81, v83, v83
Mvaa 168,20,128
v_sub_f32 v82, v76, v84
v_fmac_f32 v81, v82, v82
v_sub_f32 v83, v77, v84
v_fmac_f32 v81, v83, v83
v_sub_f32 v82, v78, v84
Zvaa 200,20,132
Ta 0,14,0
Ta 2,14,1024
v_fmac_f32 v81, v82, v82
v_sub_f32 v83, v79, v84
v_fmac_f32 v81, v83, v83
v_cndmask_b32_e64 v81, 0, v81, s[86:87]
Mvaa 200,16,36
Ta 4,14,8192
Ta 6,14,9216
A 80,80,81
s_nop 1
v_add_f32_dpp v80, v80, v80 quad_perm:[1,0,3,2] row_mask:0xf bank_mask:0xf
s_nop 1
v_add_f32_dpp v80, v80, v80 quad_perm:[2,3,0,1] row_mask:0xf bank_mask:0xf
s_nop 1
v_add_f32_dpp v80, v80, v80 row_half_mirror row_mask:0xf bank_mask:0xf
Zvaa 232,16,40
global_load_dword v142, v[140:141], off
v_lshl_add_u64 v[140:141], v[140:141], 0, s[68:69]
Mvaa 232,20,136
v_add_f32_dpp v80, v80, v80 row_mirror row_mask:0xf bank_mask:0xf
v_mov_b32 v81, v80
s_nop 1
v_permlane16_swap_b32 v80, v81
A 80,80,81
v_mul_f32 v80, 0x3acccccd, v80
K 80,80,80
Zaaa 168,20,140
v_lshlrev_b32 v80, 16, v80
v_add_f32 v80, 0x3727c5ac, v80
K 80,80,80
v_lshlrev_b32 v80, 16, v80
v_sqrt_f32 v81, v80
Maaa 168,16,44
v_rcp_f32 v82, v81
v_fma_f32 v83, -v81, v81, v80
v_mul_f32 v82, 0.5, v82
v_fma_f32 v81, v83, v82, v81
K 86,81,81
Zaaa 200,16,48
v_lshlrev_b32 v86, 16, v86
v_rcp_f32 v87, v86
K 85,84,84
v_lshlrev_b32 v85, 16, v85
Maaa 200,20,144
s_waitcnt vmcnt(17)
v_sub_f32 v56, v56, v85
v_sub_f32 v57, v57, v85
K 80,56,57
Zaaa 232,20,148
v_lshlrev_b32 v56, 16, v80
v_and_b32 v57, 0xffff0000, v80
U 80,56,87
U 81,57,87
K 32,80,81
Maaa 232,16,52
v_lshlrev_b32 v80, 16, v32
v_and_b32 v81, 0xffff0000, v32
v_lshlrev_b32 v82, 16, v44
v_and_b32 v83, 0xffff0000, v44
Zaaa 72,16,56
U 84,80,82
v_fmac_f32 v84, v81, v83
v_sub_f32 v58, v58, v85
v_sub_f32 v59, v59, v85
Maaa 72,20,152
K 80,58,59
v_lshlrev_b32 v58, 16, v80
v_and_b32 v59, 0xffff0000, v80
U 80,58,87
U 81,59,87
Zaaa 104,20,156
K 33,80,81
v_lshlrev_b32 v80, 16, v33
v_and_b32 v81, 0xffff0000, v33
v_lshlrev_b32 v82, 16, v45
Maaa 104,16,60
W 4
v_and_b32 v83, 0xffff0000, v45
v_fmac_f32 v84, v80, v82
v_fmac_f32 v84, v81, v83
v_sub_f32 v60, v60, v85
v_sub_f32 v61, v61, v85
Zaaa 108,24,60
K 80,60,61
v_lshlrev_b32 v60, 16, v80
v_and_b32 v61, 0xffff0000, v80
U 80,60,87
Maaa 108,28,156
U 81,61,87
K 34,80,81
v_lshlrev_b32 v80, 16, v34
v_and_b32 v81, 0xffff0000, v34
v_lshlrev_b32 v82, 16, v46
Zaaa 76,28,152
Ta 8,15,0
Ta 10,15,1024
v_and_b32 v83, 0xffff0000, v46
v_fmac_f32 v84, v80, v82
v_fmac_f32 v84, v81, v83
v_sub_f32 v62, v62, v85
Maaa 76,24,56
Ta 12,15,8192
v_sub_f32 v63, v63, v85
K 80,62,63
v_lshlrev_b32 v62, 16, v80
v_and_b32 v63, 0xffff0000, v80
U 80,62,87
Zaaa 236,24,52
Ta 14,15,9216
U 81,63,87
K 35,80,81
v_lshlrev_b32 v80, 16, v35
v_and_b32 v81, 0xffff0000, v35
Maaa 236,28,148
v_lshlrev_b32 v82, 16, v47
v_and_b32 v83, 0xffff0000, v47
v_fmac_f32 v84, v80, v82
v_fmac_f32 v84, v81, v83
Zaaa 204,28,144
v_sub_f32 v64, v64, v85
v_sub_f32 v65, v65, v85
K 80,64,65
v_lshlrev_b32 v64, 16, v80
v_and_b32 v65, 0xffff0000, v80
Maaa 204,24,48
U 80,64,87
U 81,65,87
K 36,80,81
v_lshlrev_b32 v80, 16, v36
Zaaa 172,24,44
v_and_b32 v81, 0xffff0000, v36
v_lshlrev_b32 v82, 16, v48
v_and_b32 v83, 0xffff0000, v48
v_fmac_f32 v84, v80, v82
v_fmac_f32 v84, v81, v83
Maaa 172,28,140
v_sub_f32 v66, v66, v85
v_sub_f32 v67, v67, v85
K 80,66,67
v_lshlrev_b32 v66, 16, v80
Zvaa 236,28,136
v_and_b32 v67, 0xffff0000, v80
U 80,66,87
U 81,67,87
K 37,80,81
v_lshlrev_b32 v80, 16, v37
Mvaa 236,24,40
v_and_b32 v81, 0xffff0000, v37
v_lshlrev_b32 v82, 16, v49
v_and_b32 v83, 0xffff0000, v49
v_fmac_f32 v84, v80, v82
Zvaa 204,24,36
v_fmac_f32 v84, v81, v83
v_sub_f32 v68, v68, v85
v_sub_f32 v69, v69, v85
K 80,68,69
v_lshlrev_b32 v68, 16, v80
Mvaa 204,28,132
v_and_b32 v69, 0xffff0000, v80
U 80,68,87
U 81,69,87
K 38,80,81
Zvaa 172,28,128
v_lshlrev_b32 v80, 16, v38
v_and_b32 v81, 0xffff0000, v38
v_lshlrev_b32 v82, 16, v50
v_and_b32 v83, 0xffff0000, v50
Mvaa 172,24,32
W 4
v_fmac_f32 v84, v80, v82
v_fmac_f32 v84, v81, v83
v_sub_f32 v70, v70, v85
v_sub_f32 v71, v71, v85
K 80,70,71
Zvaa 176,0,32
v_lshlrev_b32 v70, 16, v80
v_and_b32 v71, 0xffff0000, v80
U 80,70,87
U 81,71,87
Mvaa 176,4,128
K 39,80,81
v_lshlrev_b32 v80, 16, v39
v_and_b32 v81, 0xffff0000, v39
v_lshlrev_b32 v82, 16, v51
v_and_b32 v83, 0xffff0000, v51
Zvaa 208,4,132
Ta 16,24,0
Ta 18,24,1024
v_fmac_f32 v84, v80, v82
v_fmac_f32 v84, v81, v83
v_sub_f32 v72, v72, v85
v_sub_f32 v73, v73, v85
Mvaa 208,0,36
Ta 20,24,8192
K 80,72,73
v_lshlrev_b32 v72, 16, v80
v_and_b32 v73, 0xffff0000, v80
U 80,72,87
U 81,73,87
Zvaa 240,0,40
Ta 22,24,9216
K 40,80,81
v_lshlrev_b32 v80, 16, v40
v_and_b32 v81, 0xffff0000, v40
v_lshlrev_b32 v82, 16, v52
Mvaa 240,4,136
v_and_b32 v83, 0xffff0000, v52
v_fmac_f32 v84, v80, v82
v_fmac_f32 v84, v81, v83
v_sub_f32 v74, v74, v85
Zaaa 176,4,140
v_sub_f32 v75, v75, v85
K 80,74,75
v_lshlrev_b32 v74, 16, v80
v_and_b32 v75, 0xffff0000, v80
U 80,74,87
Maaa 176,0,44
U 81,75,87
K 41,80,81
v_lshlrev_b32 v80, 16, v41
v_and_b32 v81, 0xffff0000, v41
Zaaa 208,0,48
v_lshlrev_b32 v82, 16, v53
v_and_b32 v83, 0xffff0000, v53
v_fmac_f32 v84, v80, v82
v_fmac_f32 v84, v81, v83
v_sub_f32 v76, v76, v85
Maaa 208,4,144
v_sub_f32 v77, v77, v85
K 80,76,77
v_lshlrev_b32 v76, 16, v80
v_and_b32 v77, 0xffff0000, v80
Zaaa 240,4,148
U 80,76,87
U 81,77,87
K 42,80,81
v_lshlrev_b32 v80, 16, v42
v_and_b32 v81, 0xffff0000, v42
Maaa 240,0,52
v_lshlrev_b32 v82, 16, v54
v_and_b32 v83, 0xffff0000, v54
v_fmac_f32 v84, v80, v82
v_fmac_f32 v84, v81, v83
)"
R"(Zaaa 80,0,56
v_sub_f32 v78, v78, v85
v_sub_f32 v79, v79, v85
K 80,78,79
v_lshlrev_b32 v78, 16, v80
v_and_b32 v79, 0xffff0000, v80
Maaa 80,4,152
U 80,78,87
U 81,79,87
K 43,80,81
v_lshlrev_b32 v80, 16, v43
Zaaa 112,4,156
v_and_b32 v81, 0xffff0000, v43
v_lshlrev_b32 v82, 16, v55
v_and_b32 v83, 0xffff0000, v55
v_fmac_f32 v84, v80, v82
Maaa 112,0,60
W 4
v_fmac_f32 v84, v81, v83
v_lshrrev_b32 v82, 9, v1
s_lshl_b32 s80, s14, 1
v_add_u32 v82, s80, v82
v_mul_u32_u24 v83, 1552, v82
Zaaa 116,8,60
Ta 24,25,0
Ta 26,25,1024
v_add_u32 v83, v83, v88
v_add_u32 v83, 131072, v83
ds_write_b128 v83, v[32:35] offset:0
ds_write_b128 v83, v[36:39] offset:512
Maaa 116,12,156
ds_write_b128 v83, v[40:43] offset:1024
v_add_f32_dpp v84, v84, v84 quad_perm:[1,0,3,2] row_mask:0xf bank_mask:0xf
s_nop 1
v_add_f32_dpp v84, v84, v84 quad_perm:[2,3,0,1] row_mask:0xf bank_mask:0xf
s_nop 1
v_add_f32_dpp v84, v84, v84 row_half_mirror row_mask:0xf bank_mask:0xf
s_nop 1
v_add_f32_dpp v84, v84, v84 row_mirror row_mask:0xf bank_mask:0xf
Zaaa 84,12,152
Ta 28,25,8192
Ta 30,25,9216
v_mov_b32 v81, v84
Maaa 84,8,56
Zaaa 244,8,52
v_permlane16_swap_b32 v84, v81
A 84,84,81
K 80,84,84
v_lshlrev_b32 v80, 16, v80
s_lshl_b32 s80, s15, 3
v_add_u32 v82, s80, v82
v_sub_u32 v83, 2047, v82
Maaa 244,12,148
v_ashrrev_i32 v81, 31, v80
v_or_b32 v81, 0x80000000, v81
v_xor_b32 v80, v80, v81
v_and_b32 v80, 0xffff0000, v80
v_or_b32 v80, v80, v83
v_and_b32 v82, 7, v82
Zaaa 212,12,144
v_lshlrev_b32 v82, 2, v82
v_add_u32 v82, 143488, v82
ds_write_b32 v82, v80
Maaa 212,8,48
Zaaa 180,8,44
Maaa 180,12,140
Zvaa 244,12,136
s_waitcnt vmcnt(1) lgkmcnt(0)
s_barrier
s_add_u32 s64, s64, 128
Mvaa 244,8,40
v_bfe_u32 v80, v1, 4, 3
Zvaa 212,8,36
v_mul_u32_u24 v81, 1552, v80
Mvaa 212,12,132
s_and_b32 s64, s64, 1023
v_lshrrev_b32 v82, 7, v1
Zvaa 180,12,128
v_lshl_add_u32 v81, v82, 4, v81
Mvaa 180,8,32
s_lshl_b32 s80, s14, 7
Zvaa 184,16,32
s_add_u32 m0, s61, 0
s_add_u32 s80, s80, 131072
Mvaa 184,20,128
v_add_u32 v81, s80, v81
Zvaa 216,20,132
D 2,40,64,0
ds_read_b128 v[32:35], v81
Mvaa 216,16,36
Ta 0,10,32768
Ta 2,10,33792
Zvaa 248,16,40
Rv 36,81,512
Ta 4,10,40960
Mvaa 248,20,136
s_and_b32 s80, s14, 2
s_lshl_b32 s80, s80, 7
Ta 6,10,41984
Zaaa 184,20,140
D 3,40,64,1024
v_subrev_u32 v81, s80, v81
Maaa 184,16,44
Rv 40,81,1024
Zaaa 216,16,48
v_lshlrev_b32 v80, 2, v80
Maaa 216,20,144
v_add_u32 v80, 143488, v80
Zaaa 248,20,148
D 4,40,64,2048
ds_read_b32 v84, v80
Maaa 248,16,52
v_mov_b32 v85, s13
Zaaa 88,16,56
v_lshrrev_b32 v83, 1, v1
Maaa 88,20,152
s_mul_i32 s82, s15, 10240
Zaaa 120,20,156
s_add_u32 s82, s82, 131072
D 5,40,64,3072
Maaa 120,16,60
s_add_u32 m0, s61, 4096
s_lshl_b32 s80, s14, 10
Zaaa 124,24,60
s_add_u32 s83, s82, s80
Ra 60,26,47104
Maaa 124,28,156
Ra 156,27,47104
Zaaa 92,28,152
D 6,40,64,0
Ra 152,27,45056
Maaa 92,24,56
Ta 8,11,32768
Ta 10,11,33792
Zaaa 252,24,52
Ra 56,26,45056
Ta 12,11,40960
Maaa 252,28,148
Ta 14,11,41984
Ra 52,26,43008
Zaaa 220,28,144
W 9
buffer_store_dwordx4 v[32:35], v1, s[8:11], s83 offen sc1
s_add_u32 s83, s83, 4096
Maaa 220,24,48
Ra 148,27,43008
Ra 144,27,40960
Zaaa 188,24,44
Ra 48,26,40960
Ra 44,26,38912
Maaa 188,28,140
Ra 140,27,38912
Zvaa 252,28,136
D 7,40,64,1024
Ra 136,27,36864
Mvaa 252,24,40
Ra 40,26,36864
Zvaa 220,24,36
Ra 36,26,34816
Mvaa 220,28,132
Ra 132,27,34816
Zvaa 188,28,128
buffer_store_dwordx4 v[36:39], v1, s[8:11], s83 offen sc1
Ra 128,27,32768
Mvaa 188,24,32
W 13
s_and_b32 s80, s80, 1024
s_add_u32 s83, s82, s80
Maaa 96,0,60
s_add_u32 s83, s83, 8192
Ra 32,26,32768
Maaa 96,4,156
Maaa 64,4,152
D 8,40,64,2048
Ta 16,12,32768
Maaa 64,0,56
W 9
Ta 18,12,33792
Ta 20,12,40960
Maaa 224,0,52
Ta 22,12,41984
Maaa 224,4,148
Maaa 192,4,144
D 9,40,64,3072
Maaa 192,0,48
W 8
s_lshl_b32 s65, s64, 12
s_add_u32 m0, s62, 0
Maaa 160,0,44
Maaa 160,4,140
Mvaa 224,4,136
buffer_store_dwordx4 v[40:43], v1, s[8:11], s83 offen sc1
Mvaa 224,0,40
W 4
Mvaa 192,0,36
Mvaa 192,4,132
Mvaa 160,4,128
D 16,44,65,0
Mvaa 160,0,32
Mvaa 164,8,32
Mvaa 164,12,128
Mvaa 196,12,132
D 17,44,65,1024
Ta 24,13,32768
Mvaa 196,8,36
Ta 26,13,33792
Ta 28,13,40960
Mvaa 228,8,40
Ta 30,13,41984
Mvaa 228,12,136
Maaa 164,12,140
D 18,44,65,2048
Maaa 164,8,44
Maaa 196,8,48
Maaa 196,12,144
Maaa 228,12,148
D 19,44,65,3072
Maaa 228,8,52
s_add_u32 m0, s62, 4096
Maaa 68,8,56
Maaa 68,12,152
Maaa 100,12,156
D 20,44,65,0
Maaa 100,8,60
W 4
Maaa 104,16,60
Maaa 104,20,156
Maaa 72,20,152
D 21,44,65,1024
Ta 0,14,32768
Maaa 72,16,56
Ta 2,14,33792
Ta 4,14,40960
Maaa 232,16,52
Ta 6,14,41984
Maaa 232,20,148
Maaa 200,20,144
D 22,44,65,2048
Maaa 200,16,48
Maaa 168,16,44
Maaa 168,20,140
Mvaa 232,20,136
D 23,44,65,3072
Mvaa 232,16,40
Mvaa 200,16,36
Mvaa 200,20,132
Mvaa 168,20,128
global_load_dword v142, v[140:141], off
v_lshl_add_u64 v[140:141], v[140:141], 0, s[68:69]
Mvaa 168,16,32
W 4
Mvaa 172,24,32
Mvaa 172,28,128
Mvaa 204,28,132
Ta 8,15,32768
Ta 10,15,33792
Mvaa 204,24,36
Ta 12,15,40960
Ta 14,15,41984
Mvaa 236,24,40
Mvaa 236,28,136
Maaa 172,28,140
Maaa 172,24,44
Maaa 204,24,48
Maaa 204,28,144
Maaa 236,28,148
Maaa 236,24,52
Maaa 76,24,56
Maaa 76,28,152
Maaa 108,28,156
Maaa 108,24,60
W 4
Maaa 112,0,60
Maaa 112,4,156
Maaa 80,4,152
Ta 16,24,32768
Ta 18,24,33792
Maaa 80,0,56
Ta 20,24,40960
Ta 22,24,41984
Maaa 240,0,52
Maaa 240,4,148
Maaa 208,4,144
Maaa 208,0,48
Maaa 176,0,44
Maaa 176,4,140
Mvaa 240,4,136
Mvaa 240,0,40
Mvaa 208,0,36
Mvaa 208,4,132
Mvaa 176,4,128
Mvaa 176,0,32
W 4
Mvaa 180,8,32
Ta 24,25,32768
Ta 26,25,33792
Mvaa 180,12,128
Mvaa 212,12,132
Ta 28,25,40960
Ta 30,25,41984
Mvaa 212,8,36
Mvaa 244,8,40
Mvaa 244,12,136
Maaa 180,12,140
Maaa 180,8,44
Maaa 212,8,48
Maaa 212,12,144
Maaa 244,12,148
s_waitcnt vmcnt(1) lgkmcnt(0)
s_barrier
s_cmp_eq_u32 s14, 0
s_cbranch_scc0 .Lpubr_12%=
s_lshl_b32 s80, s15, 6
s_add_u32 s80, s80, 40960
s_mov_b64 exec, 0xff
buffer_store_dwordx2 v[84:85], v83, s[8:11], s80 offen sc1
s_mov_b64 exec, -1
.Lpubr_12%=:
Maaa 244,8,52
s_add_u32 s64, s64, 128
Maaa 84,8,56
Maaa 84,12,152
s_and_b32 s64, s64, 1023
Maaa 116,12,156
Maaa 116,8,60
s_add_u32 m0, s61, 32768
Maaa 120,16,60
Maaa 120,20,156
Maaa 88,20,152
D 2,40,64,0
Ta 0,10,0
Maaa 88,16,56
Ta 2,10,1024
Ta 4,10,8192
Maaa 248,16,52
Ta 6,10,9216
Maaa 248,20,148
Maaa 216,20,144
D 3,40,64,1024
Maaa 216,16,48
Maaa 184,16,44
Maaa 184,20,140
Mvaa 248,20,136
D 4,40,64,2048
Mvaa 248,16,40
Mvaa 216,16,36
Mvaa 216,20,132
Mvaa 184,20,128
D 5,40,64,3072
Mvaa 184,16,32
s_add_u32 m0, s61, 36864
Mvaa 188,24,32
Ra 32,26,0
Mvaa 188,28,128
Ra 128,27,0
Mvaa 220,28,132
D 6,40,64,0
Ra 132,27,2048
Mvaa 220,24,36
Ta 8,11,0
Ta 10,11,1024
Mvaa 252,24,40
Ra 36,26,2048
Ta 12,11,8192
Mvaa 252,28,136
Ta 14,11,9216
Ra 40,26,4096
Maaa 188,28,140
D 7,40,64,1024
Ra 136,27,4096
Maaa 188,24,44
Ra 140,27,6144
Ra 44,26,6144
Maaa 220,24,48
Ra 48,26,8192
Maaa 220,28,144
Ra 144,27,8192
Maaa 252,28,148
D 8,40,64,2048
Ra 148,27,10240
Maaa 252,24,52
Ra 52,26,10240
Maaa 92,24,56
Ra 56,26,12288
Maaa 92,28,152
Ra 152,27,12288
Maaa 124,28,156
D 9,40,64,3072
Ra 156,27,14336
Maaa 124,24,60
W 13
s_lshl_b32 s65, s64, 12
s_add_u32 m0, s62, 32768
Mvaa 160,0,32
Ra 60,26,14336
Mvaa 160,4,128
Mvaa 192,4,132
D 16,44,65,0
Ta 16,12,0
Mvaa 192,0,36
)"
R"(W 9
Ta 18,12,1024
Ta 20,12,8192
Mvaa 224,0,40
Ta 22,12,9216
Mvaa 224,4,136
Maaa 160,4,140
D 17,44,65,1024
Maaa 160,0,44
W 8
Maaa 192,0,48
Maaa 192,4,144
Maaa 224,4,148
D 18,44,65,2048
Maaa 224,0,52
W 4
Maaa 64,0,56
Maaa 64,4,152
Maaa 96,4,156
D 19,44,65,3072
Maaa 96,0,60
s_add_u32 m0, s62, 36864
Maaa 100,8,60
Maaa 100,12,156
Maaa 68,12,152
D 20,44,65,0
Ta 24,13,0
Maaa 68,8,56
Ta 26,13,1024
Ta 28,13,8192
Maaa 228,8,52
Ta 30,13,9216
Maaa 228,12,148
Maaa 196,12,144
D 21,44,65,1024
Maaa 196,8,48
Maaa 164,8,44
Maaa 164,12,140
Mvaa 228,12,136
D 22,44,65,2048
Mvaa 228,8,40
Mvaa 196,8,36
Mvaa 196,12,132
Mvaa 164,12,128
D 23,44,65,3072
Mvaa 164,8,32
W 4
s_add_u32 s65, s5, 0
s_cmp_ge_u32 s65, 20
s_cselect_b32 s68, 20, 0
Mvaa 168,16,32
s_sub_u32 s65, s65, s68
s_lshl_b32 s65, s65, 18
s_add_u32 m0, s63, 131072
Mvaa 168,20,128
Mvaa 200,20,132
D 16,48,65,0
Ta 0,14,0
Mvaa 200,16,36
Ta 2,14,1024
Ta 4,14,8192
Mvaa 232,16,40
Ta 6,14,9216
Mvaa 232,20,136
Maaa 168,20,140
D 17,48,65,1024
Maaa 168,16,44
Maaa 200,16,48
Maaa 200,20,144
Maaa 232,20,148
D 18,48,65,2048
Maaa 232,16,52
Maaa 72,16,56
Maaa 72,20,152
Maaa 104,20,156
D 19,48,65,3072
Maaa 104,16,60
W 4
s_add_u32 m0, s63, 135168
Maaa 108,24,60
Maaa 108,28,156
Maaa 76,28,152
D 20,48,65,0
Ta 8,15,0
Maaa 76,24,56
Ta 10,15,1024
Ta 12,15,8192
Maaa 236,24,52
Ta 14,15,9216
Maaa 236,28,148
Maaa 204,28,144
D 21,48,65,1024
Maaa 204,24,48
Maaa 172,24,44
Maaa 172,28,140
Mvaa 236,28,136
D 22,48,65,2048
Mvaa 236,24,40
Mvaa 204,24,36
Mvaa 204,28,132
Mvaa 172,28,128
D 23,48,65,3072
Mvaa 172,24,32
W 4
Mvaa 176,0,32
Mvaa 176,4,128
Mvaa 208,4,132
Ta 16,24,0
Ta 18,24,1024
Mvaa 208,0,36
Ta 20,24,8192
Ta 22,24,9216
Mvaa 240,0,40
Mvaa 240,4,136
Maaa 176,4,140
Maaa 176,0,44
Maaa 208,0,48
Maaa 208,4,144
Maaa 240,4,148
Maaa 240,0,52
Maaa 80,0,56
Maaa 80,4,152
Maaa 112,4,156
Maaa 112,0,60
W 4
Maaa 116,8,60
Ta 24,25,0
Ta 26,25,1024
Maaa 116,12,156
Maaa 84,12,152
Ta 28,25,8192
Ta 30,25,9216
Maaa 84,8,56
Maaa 244,8,52
Maaa 244,12,148
Maaa 212,12,144
Maaa 212,8,48
Maaa 180,8,44
Maaa 180,12,140
Mvaa 244,12,136
s_waitcnt vmcnt(0) lgkmcnt(0)
s_barrier
s_add_u32 s64, s64, 128
Mvaa 244,8,40
Mvaa 212,8,36
Mvaa 212,12,132
s_and_b32 s64, s64, 1023
Mvaa 180,12,128
Mvaa 180,8,32
Mvaa 184,16,32
s_add_u32 m0, s61, 0
Mvaa 184,20,128
Mvaa 216,20,132
D 2,40,64,0
Ta 0,10,32768
Mvaa 216,16,36
Ta 2,10,33792
Ta 4,10,40960
Mvaa 248,16,40
Ta 6,10,41984
Mvaa 248,20,136
Maaa 184,20,140
D 3,40,64,1024
Maaa 184,16,44
Maaa 216,16,48
Maaa 216,20,144
Maaa 248,20,148
D 4,40,64,2048
Maaa 248,16,52
Maaa 88,16,56
Maaa 88,20,152
Maaa 120,20,156
D 5,40,64,3072
Maaa 120,16,60
s_add_u32 m0, s61, 4096
Maaa 124,24,60
Ra 60,26,47104
v_lshrrev_b32 v28, 1, v1
Maaa 124,28,156
s_lshl_b32 s80, s60, 9
Ra 156,27,47104
Maaa 92,28,152
s_add_u32 s80, s80, 40960
D 6,40,64,0
Ra 152,27,45056
Maaa 92,24,56
Ta 8,11,32768
Ta 10,11,33792
Maaa 252,24,52
Ra 56,26,45056
Ta 12,11,40960
Maaa 252,28,148
Ta 14,11,41984
Ra 52,26,43008
Maaa 220,28,144
D 7,40,64,1024
Ra 148,27,43008
Maaa 220,24,48
Ra 144,27,40960
Ra 48,26,40960
Maaa 188,24,44
Ra 44,26,38912
Maaa 188,28,140
Ra 140,27,38912
Mvaa 252,28,136
buffer_load_dwordx2 v[28:29], v28, s[8:11], s80 offen sc1
Ra 136,27,36864
Mvaa 252,24,40
Ra 40,26,36864
Mvaa 220,24,36
Ra 36,26,34816
Mvaa 220,28,132
Ra 132,27,34816
Mvaa 188,28,128
D 8,40,64,2048
Ra 128,27,32768
Mvaa 188,24,32
W 13
Ra 32,26,32768
Maaa 96,0,60
Maaa 96,4,156
Maaa 64,4,152
D 9,40,64,3072
Ta 16,12,32768
Maaa 64,0,56
W 9
s_lshl_b32 s65, s64, 12
s_add_u32 m0, s62, 0
Maaa 224,0,52
Ta 18,12,33792
Ta 20,12,40960
Maaa 224,4,148
Ta 22,12,41984
Maaa 192,4,144
D 16,44,65,0
Maaa 192,0,48
W 8
Maaa 160,0,44
Maaa 160,4,140
Mvaa 224,4,136
D 17,44,65,1024
Mvaa 224,0,40
W 4
Mvaa 192,0,36
Mvaa 192,4,132
Mvaa 160,4,128
D 18,44,65,2048
Mvaa 160,0,32
Mvaa 164,8,32
Mvaa 164,12,128
Mvaa 196,12,132
D 19,44,65,3072
Ta 24,13,32768
Mvaa 196,8,36
s_add_u32 m0, s62, 4096
Ta 26,13,33792
Ta 28,13,40960
Mvaa 228,8,40
Ta 30,13,41984
Mvaa 228,12,136
Maaa 164,12,140
D 20,44,65,0
Maaa 164,8,44
Maaa 196,8,48
Maaa 196,12,144
Maaa 228,12,148
D 21,44,65,1024
Maaa 228,8,52
Maaa 68,8,56
Maaa 68,12,152
Maaa 100,12,156
D 22,44,65,2048
Maaa 100,8,60
W 4
Maaa 104,16,60
Maaa 104,20,156
Maaa 72,20,152
D 23,44,65,3072
Ta 0,14,32768
Maaa 72,16,56
Ta 2,14,33792
Ta 4,14,40960
Maaa 232,16,52
Ta 6,14,41984
Maaa 232,20,148
Maaa 200,20,144
s_waitcnt vmcnt(10)
v_cmp_eq_u32 vcc, s13, v29
s_cmp_eq_u64 vcc, -1
s_cbranch_scc0 .Lspin_13%=
.Lok_14%=:
Maaa 200,16,48
global_load_dword v142, v[140:141], off
v_lshl_add_u64 v[140:141], v[140:141], 0, s[68:69]
Maaa 168,16,44
v_bfe_u32 v144, v1, 8, 1
Maaa 168,20,140
v_mul_u32_u24 v144, 10240, v144
Mvaa 232,20,136
v_and_b32 v145, 0xf0, v1
Mvaa 232,16,40
v_lshl_add_u32 v144, v145, 3, v144
Mvaa 200,16,36
s_mul_i32 s69, s58, 20480
Mvaa 200,20,132
s_add_u32 s69, s69, s28
Mvaa 168,20,128
buffer_load_dword v142, v144, s[8:11], s69 offen offset:0 sc1
Mvaa 168,16,32
W 4
Mvaa 172,24,32
Mvaa 172,28,128
Mvaa 204,28,132
Ta 8,15,32768
Ta 10,15,33792
Mvaa 204,24,36
Ta 12,15,40960
Ta 14,15,41984
Mvaa 236,24,40
Mvaa 236,28,136
Maaa 172,28,140
Maaa 172,24,44
Maaa 204,24,48
Maaa 204,28,144
Maaa 236,28,148
Maaa 236,24,52
Maaa 76,24,56
Maaa 76,28,152
Maaa 108,28,156
Maaa 108,24,60
W 4
Maaa 112,0,60
Maaa 112,4,156
Maaa 80,4,152
Ta 16,24,32768
Ta 18,24,33792
Maaa 80,0,56
Ta 20,24,40960
Ta 22,24,41984
Maaa 240,0,52
Maaa 240,4,148
Maaa 208,4,144
Maaa 208,0,48
Maaa 176,0,44
Maaa 176,4,140
Mvaa 240,4,136
Mvaa 240,0,40
Mvaa 208,0,36
Mvaa 208,4,132
Mvaa 176,4,128
Mvaa 176,0,32
W 4
Mvaa 180,8,32
Ta 24,25,32768
Ta 26,25,33792
Mvaa 180,12,128
Mvaa 212,12,132
Ta 28,25,40960
Ta 30,25,41984
Mvaa 212,8,36
Mvaa 244,8,40
Mvaa 244,12,136
Maaa 180,12,140
Maaa 180,8,44
Maaa 212,8,48
Maaa 212,12,144
Maaa 244,12,148
s_waitcnt vmcnt(2) lgkmcnt(0)
s_barrier
s_add_u32 s64, s64, 128
Maaa 244,8,52
Maaa 84,8,56
Maaa 84,12,152
s_and_b32 s64, s64, 1023
Maaa 116,12,156
Maaa 116,8,60
Maaa 120,16,60
s_add_u32 m0, s61, 32768
Maaa 120,20,156
Maaa 88,20,152
Ta 0,10,0
Ta 2,10,1024
Maaa 88,16,56
D 2,40,64,0
Ta 4,10,8192
Maaa 248,16,52
Ta 6,10,9216
Maaa 248,20,148
Maaa 216,20,144
Maaa 216,16,48
D 3,40,64,1024
Maaa 184,16,44
Maaa 184,20,140
Mvaa 248,20,136
Mvaa 248,16,40
D 4,40,64,2048
Mvaa 216,16,36
Mvaa 216,20,132
Mvaa 184,20,128
Mvaa 184,16,32
)"
R"(D 5,40,64,3072
Mvaa 188,24,32
s_add_u32 m0, s61, 36864
Ra 32,26,0
Mvaa 188,28,128
Ra 128,27,0
Mvaa 220,28,132
Ra 132,27,2048
Ta 8,11,0
Mvaa 220,24,36
D 6,40,64,0
Ta 10,11,1024
Mvaa 252,24,40
Ra 36,26,2048
Ta 12,11,8192
Mvaa 252,28,136
Ta 14,11,9216
Ra 40,26,4096
Maaa 188,28,140
Ra 136,27,4096
Ra 140,27,6144
Maaa 188,24,44
D 7,40,64,1024
Ra 44,26,6144
Maaa 220,24,48
Ra 48,26,8192
Maaa 220,28,144
Ra 144,27,8192
Maaa 252,28,148
Ra 148,27,10240
Maaa 252,24,52
D 8,40,64,2048
Ra 52,26,10240
Maaa 92,24,56
Ra 56,26,12288
Maaa 92,28,152
Ra 152,27,12288
Maaa 124,28,156
Ra 156,27,14336
Maaa 124,24,60
W 13
D 9,40,64,3072
Ra 60,26,14336
Mvaa 160,0,32
s_lshl_b32 s65, s64, 12
s_add_u32 m0, s62, 32768
Mvaa 160,4,128
Mvaa 192,4,132
Ta 16,12,0
Ta 18,12,1024
Mvaa 192,0,36
W 10
D 16,44,65,0
Ta 20,12,8192
Mvaa 224,0,40
Ta 22,12,9216
Mvaa 224,4,136
Maaa 160,4,140
Maaa 160,0,44
W 8
D 17,44,65,1024
Maaa 192,0,48
Maaa 192,4,144
Maaa 224,4,148
Maaa 224,0,52
W 4
D 18,44,65,2048
Maaa 64,0,56
Maaa 64,4,152
Maaa 96,4,156
Maaa 96,0,60
D 19,44,65,3072
Maaa 100,8,60
s_add_u32 m0, s62, 36864
Maaa 100,12,156
Maaa 68,12,152
Ta 24,13,0
Ta 26,13,1024
Maaa 68,8,56
D 20,44,65,0
Ta 28,13,8192
Maaa 228,8,52
Ta 30,13,9216
Maaa 228,12,148
Maaa 196,12,144
Maaa 196,8,48
D 21,44,65,1024
Maaa 164,8,44
Maaa 164,12,140
Mvaa 228,12,136
Mvaa 228,8,40
D 22,44,65,2048
Mvaa 196,8,36
Mvaa 196,12,132
Mvaa 164,12,128
Mvaa 164,8,32
W 4
D 23,44,65,3072
Mvaa 168,16,32
v_lshrrev_b32 v138, 4, v1
Mvaa 168,20,128
Mvaa 200,20,132
s_sub_u32 s81, s73, 1
Ta 0,14,0
Ta 2,14,1024
Mvaa 200,16,36
Ta 4,14,8192
v_min_u32 v138, s81, v138
Mvaa 232,16,40
Ta 6,14,9216
v_lshrrev_b32 v139, 2, v138
Mvaa 232,20,136
v_mul_u32_u24 v139, s72, v139
Maaa 168,20,140
v_and_b32 v138, 3, v138
Maaa 168,16,44
v_lshl_add_u32 v138, v138, 7, v139
Maaa 200,16,48
v_add_u32 v138, s74, v138
Maaa 200,20,144
s_lshr_b32 s73, s73, 2
Maaa 232,20,148
s_mul_i32 s73, s73, s72
buffer_load_dword v142, v144, s[8:11], s69 offen offset:2048 sc1
Maaa 232,16,52
v_min_u32 v139, s75, v138
Maaa 72,16,56
Maaa 72,20,152
s_add_u32 s69, s69, 4096
Maaa 104,20,156
global_load_dword v142, v139, s[76:77]
v_add_u32 v138, s73, v138
Maaa 104,16,60
W 4
Maaa 108,24,60
Maaa 108,28,156
Maaa 76,28,152
Ta 8,15,0
Ta 10,15,1024
Maaa 76,24,56
Ta 12,15,8192
Ta 14,15,9216
Maaa 236,24,52
Maaa 236,28,148
Maaa 204,28,144
Maaa 204,24,48
Maaa 172,24,44
Maaa 172,28,140
Mvaa 236,28,136
Mvaa 236,24,40
Mvaa 204,24,36
Mvaa 204,28,132
Mvaa 172,28,128
Mvaa 172,24,32
W 4
Mvaa 176,0,32
Mvaa 176,4,128
Mvaa 208,4,132
Ta 16,24,0
Ta 18,24,1024
Mvaa 208,0,36
Ta 20,24,8192
Ta 22,24,9216
Mvaa 240,0,40
Mvaa 240,4,136
Maaa 176,4,140
Maaa 176,0,44
Maaa 208,0,48
Maaa 208,4,144
Maaa 240,4,148
Maaa 240,0,52
Maaa 80,0,56
Maaa 80,4,152
Maaa 112,4,156
Maaa 112,0,60
W 4
Maaa 116,8,60
Ta 24,25,0
Ta 26,25,1024
Maaa 116,12,156
Maaa 84,12,152
Ta 28,25,8192
Ta 30,25,9216
Maaa 84,8,56
Maaa 244,8,52
Maaa 244,12,148
Maaa 212,12,144
Maaa 212,8,48
Maaa 180,8,44
Maaa 180,12,140
Mvaa 244,12,136
s_waitcnt vmcnt(2) lgkmcnt(0)
s_barrier
s_add_u32 s64, s64, 128
Mvaa 244,8,40
Mvaa 212,8,36
Mvaa 212,12,132
s_and_b32 s64, s64, 1023
Mvaa 180,12,128
Mvaa 180,8,32
Mvaa 184,16,32
s_add_u32 m0, s61, 0
Mvaa 184,20,128
Mvaa 216,20,132
Ta 0,10,32768
Ta 2,10,33792
Mvaa 216,16,36
D 2,40,64,0
Ta 4,10,40960
Mvaa 248,16,40
Ta 6,10,41984
Mvaa 248,20,136
Maaa 184,20,140
Maaa 184,16,44
D 3,40,64,1024
Maaa 216,16,48
Maaa 216,20,144
Maaa 248,20,148
Maaa 248,16,52
D 4,40,64,2048
Maaa 88,16,56
Maaa 88,20,152
Maaa 120,20,156
Maaa 120,16,60
D 5,40,64,3072
Maaa 124,24,60
s_add_u32 m0, s61, 4096
Ra 60,26,47104
Maaa 124,28,156
Ra 156,27,47104
Maaa 92,28,152
Ra 152,27,45056
Ta 8,11,32768
Maaa 92,24,56
D 6,40,64,0
Ta 10,11,33792
Maaa 252,24,52
Ra 56,26,45056
Ta 12,11,40960
Maaa 252,28,148
Ta 14,11,41984
Ra 52,26,43008
Maaa 220,28,144
Ra 148,27,43008
Ra 144,27,40960
Maaa 220,24,48
D 7,40,64,1024
Ra 48,26,40960
Maaa 188,24,44
Ra 44,26,38912
Maaa 188,28,140
Ra 140,27,38912
Mvaa 252,28,136
Ra 136,27,36864
Mvaa 252,24,40
D 8,40,64,2048
Ra 40,26,36864
Mvaa 220,24,36
Ra 36,26,34816
Mvaa 220,28,132
Ra 132,27,34816
Mvaa 188,28,128
Ra 128,27,32768
Mvaa 188,24,32
W 13
D 9,40,64,3072
Ra 32,26,32768
Maaa 96,0,60
s_lshl_b32 s65, s64, 12
s_add_u32 m0, s62, 0
Maaa 96,4,156
Maaa 64,4,152
Ta 16,12,32768
Ta 18,12,33792
Maaa 64,0,56
W 10
D 16,44,65,0
Ta 20,12,40960
Maaa 224,0,52
Ta 22,12,41984
Maaa 224,4,148
Maaa 192,4,144
Maaa 192,0,48
W 8
D 17,44,65,1024
Maaa 160,0,44
Maaa 160,4,140
Mvaa 224,4,136
Mvaa 224,0,40
W 4
D 18,44,65,2048
Mvaa 192,0,36
Mvaa 192,4,132
Mvaa 160,4,128
Mvaa 160,0,32
D 19,44,65,3072
Mvaa 164,8,32
s_add_u32 m0, s62, 4096
Mvaa 164,12,128
Mvaa 196,12,132
Ta 24,13,32768
Ta 26,13,33792
Mvaa 196,8,36
D 20,44,65,0
Ta 28,13,40960
Mvaa 228,8,40
Ta 30,13,41984
Mvaa 228,12,136
Maaa 164,12,140
Maaa 164,8,44
D 21,44,65,1024
Maaa 196,8,48
Maaa 196,12,144
Maaa 228,12,148
Maaa 228,8,52
D 22,44,65,2048
Maaa 68,8,56
Maaa 68,12,152
Maaa 100,12,156
Maaa 100,8,60
W 4
D 23,44,65,3072
Maaa 104,16,60
v_min_u32 v139, s75, v138
Maaa 104,20,156
Maaa 72,20,152
Ta 0,14,32768
Ta 2,14,33792
Maaa 72,16,56
buffer_load_dword v142, v144, s[8:11], s69 offen offset:0 sc1
Ta 4,14,40960
Maaa 232,16,52
Ta 6,14,41984
Maaa 232,20,148
Maaa 200,20,144
Maaa 200,16,48
global_load_dword v142, v139, s[76:77]
Maaa 168,16,44
Maaa 168,20,140
Mvaa 232,20,136
v_add_u32 v138, s73, v138
Mvaa 232,16,40
Mvaa 200,16,36
Mvaa 200,20,132
Mvaa 168,20,128
Mvaa 168,16,32
W 4
Mvaa 172,24,32
Mvaa 172,28,128
Mvaa 204,28,132
Ta 8,15,32768
Ta 10,15,33792
Mvaa 204,24,36
Ta 12,15,40960
Ta 14,15,41984
Mvaa 236,24,40
Mvaa 236,28,136
Maaa 172,28,140
Maaa 172,24,44
Maaa 204,24,48
Maaa 204,28,144
Maaa 236,28,148
Maaa 236,24,52
Maaa 76,24,56
Maaa 76,28,152
Maaa 108,28,156
Maaa 108,24,60
W 4
Maaa 112,0,60
Maaa 112,4,156
Maaa 80,4,152
Ta 16,24,32768
Ta 18,24,33792
Maaa 80,0,56
Ta 20,24,40960
Ta 22,24,41984
Maaa 240,0,52
Maaa 240,4,148
Maaa 208,4,144
Maaa 208,0,48
Maaa 176,0,44
Maaa 176,4,140
Mvaa 240,4,136
Mvaa 240,0,40
Mvaa 208,0,36
Mvaa 208,4,132
Mvaa 176,4,128
Mvaa 176,0,32
W 4
Mvaa 180,8,32
Ta 24,25,32768
Ta 26,25,33792
Mvaa 180,12,128
Mvaa 212,12,132
Ta 28,25,40960
Ta 30,25,41984
Mvaa 212,8,36
Mvaa 244,8,40
Mvaa 244,12,136
Maaa 180,12,140
)"
R"(Maaa 180,8,44
Maaa 212,8,48
Maaa 212,12,144
Maaa 244,12,148
s_waitcnt vmcnt(2) lgkmcnt(0)
s_barrier
s_add_u32 s64, s64, 128
Maaa 244,8,52
Maaa 84,8,56
Maaa 84,12,152
s_and_b32 s64, s64, 1023
Maaa 116,12,156
Maaa 116,8,60
Maaa 120,16,60
s_add_u32 m0, s61, 32768
Maaa 120,20,156
Maaa 88,20,152
Ta 0,10,0
Ta 2,10,1024
Maaa 88,16,56
D 2,40,64,0
Ta 4,10,8192
Maaa 248,16,52
Ta 6,10,9216
Maaa 248,20,148
Maaa 216,20,144
Maaa 216,16,48
D 3,40,64,1024
Maaa 184,16,44
Maaa 184,20,140
Mvaa 248,20,136
Mvaa 248,16,40
D 4,40,64,2048
Mvaa 216,16,36
Mvaa 216,20,132
Mvaa 184,20,128
Mvaa 184,16,32
D 5,40,64,3072
Mvaa 188,24,32
s_add_u32 m0, s61, 36864
Ra 32,26,0
Mvaa 188,28,128
Ra 128,27,0
Mvaa 220,28,132
Ra 132,27,2048
Ta 8,11,0
Mvaa 220,24,36
D 6,40,64,0
Ta 10,11,1024
Mvaa 252,24,40
Ra 36,26,2048
Ta 12,11,8192
Mvaa 252,28,136
Ta 14,11,9216
Ra 40,26,4096
Maaa 188,28,140
Ra 136,27,4096
Ra 140,27,6144
Maaa 188,24,44
D 7,40,64,1024
Ra 44,26,6144
Maaa 220,24,48
Ra 48,26,8192
Maaa 220,28,144
Ra 144,27,8192
Maaa 252,28,148
Ra 148,27,10240
Maaa 252,24,52
D 8,40,64,2048
Ra 52,26,10240
Maaa 92,24,56
Ra 56,26,12288
Maaa 92,28,152
Ra 152,27,12288
Maaa 124,28,156
Ra 156,27,14336
Maaa 124,24,60
W 13
D 9,40,64,3072
Ra 60,26,14336
Mvaa 160,0,32
s_lshl_b32 s65, s64, 12
s_add_u32 m0, s62, 32768
Mvaa 160,4,128
Mvaa 192,4,132
Ta 16,12,0
Ta 18,12,1024
Mvaa 192,0,36
W 10
D 16,44,65,0
Ta 20,12,8192
Mvaa 224,0,40
Ta 22,12,9216
Mvaa 224,4,136
Maaa 160,4,140
Maaa 160,0,44
W 8
D 17,44,65,1024
Maaa 192,0,48
Maaa 192,4,144
Maaa 224,4,148
Maaa 224,0,52
W 4
D 18,44,65,2048
Maaa 64,0,56
Maaa 64,4,152
Maaa 96,4,156
Maaa 96,0,60
D 19,44,65,3072
Maaa 100,8,60
s_add_u32 m0, s62, 36864
Maaa 100,12,156
Maaa 68,12,152
Ta 24,13,0
Ta 26,13,1024
Maaa 68,8,56
D 20,44,65,0
Ta 28,13,8192
Maaa 228,8,52
Ta 30,13,9216
Maaa 228,12,148
Maaa 196,12,144
Maaa 196,8,48
D 21,44,65,1024
Maaa 164,8,44
Maaa 164,12,140
Mvaa 228,12,136
Mvaa 228,8,40
D 22,44,65,2048
Mvaa 196,8,36
Mvaa 196,12,132
Mvaa 164,12,128
Mvaa 164,8,32
W 4
D 23,44,65,3072
v_add_u32 v2, 131072, v10
Mvaa 168,16,32
v_add_u32 v3, 131072, v11
Mvaa 168,20,128
Mvaa 200,20,132
Ta 0,14,0
Ta 2,14,1024
Mvaa 200,16,36
buffer_load_dword v142, v144, s[8:11], s69 offen offset:2048 sc1
v_add_u32 v4, 131072, v12
Mvaa 232,16,40
Ta 4,14,8192
Ta 6,14,9216
Mvaa 232,20,136
s_add_u32 s69, s69, 4096
v_add_u32 v5, 131072, v13
Maaa 168,20,140
v_add_u32 v6, 131072, v14
Maaa 168,16,44
Maaa 200,16,48
v_add_u32 v7, 131072, v15
Maaa 200,20,144
Maaa 232,20,148
v_add_u32 v8, 131072, v24
Maaa 232,16,52
Maaa 72,16,56
v_add_u32 v9, 131072, v25
Maaa 72,20,152
Maaa 104,20,156
Maaa 104,16,60
W 4
Maaa 108,24,60
Maaa 108,28,156
Maaa 76,28,152
Ta 8,15,0
Ta 10,15,1024
Maaa 76,24,56
Ta 12,15,8192
Ta 14,15,9216
Maaa 236,24,52
Maaa 236,28,148
Maaa 204,28,144
Maaa 204,24,48
Maaa 172,24,44
Maaa 172,28,140
Mvaa 236,28,136
Mvaa 236,24,40
Mvaa 204,24,36
Mvaa 204,28,132
Mvaa 172,28,128
Mvaa 172,24,32
W 4
Mvaa 176,0,32
Mvaa 176,4,128
Mvaa 208,4,132
Ta 16,24,0
Ta 18,24,1024
Mvaa 208,0,36
Ta 20,24,8192
Ta 22,24,9216
Mvaa 240,0,40
Mvaa 240,4,136
Maaa 176,4,140
Maaa 176,0,44
Maaa 208,0,48
Maaa 208,4,144
Maaa 240,4,148
Maaa 240,0,52
Maaa 80,0,56
Maaa 80,4,152
Maaa 112,4,156
Maaa 112,0,60
W 4
Maaa 116,8,60
Ta 24,25,0
Ta 26,25,1024
Maaa 116,12,156
Maaa 84,12,152
Ta 28,25,8192
Ta 30,25,9216
Maaa 84,8,56
Maaa 244,8,52
Maaa 244,12,148
Maaa 212,12,144
Maaa 212,8,48
Maaa 180,8,44
Maaa 180,12,140
Mvaa 244,12,136
s_waitcnt vmcnt(1) lgkmcnt(0)
s_barrier
s_add_u32 s65, s5, 4
Mvaa 244,8,40
Mvaa 212,8,36
Mvaa 212,12,132
s_cmp_ge_u32 s65, 20
s_cselect_b32 s68, 20, 0
Mvaa 180,12,128
Mvaa 180,8,32
s_sub_u32 s65, s65, s68
Mvaa 184,16,32
Mvaa 184,20,128
s_lshl_b32 s65, s65, 18
Mvaa 216,20,132
Ta 0,10,32768
Ta 2,10,33792
Mvaa 216,16,36
Mvaa 248,16,40
s_add_u32 m0, s63, 0
Mvaa 248,20,136
Maaa 184,20,140
D 16,48,65,0
Maaa 184,16,44
Maaa 216,16,48
Maaa 216,20,144
Maaa 248,20,148
D 17,48,65,1024
Ta 4,11,32768
Maaa 248,16,52
Ta 6,11,33792
Maaa 88,16,56
Maaa 88,20,152
Maaa 120,20,156
D 18,48,65,2048
Maaa 120,16,60
Maaa 124,24,60
Ra 60,26,47104
Maaa 124,28,156
Maaa 92,28,152
D 19,48,65,3072
Ta 8,12,32768
Maaa 92,24,56
s_add_u32 m0, s63, 4096
Ta 10,12,33792
Ra 56,26,45056
Maaa 252,24,52
Ra 52,26,43008
Maaa 252,28,148
Maaa 220,28,144
D 20,48,65,0
Maaa 220,24,48
Ra 48,26,40960
Maaa 188,24,44
Ra 44,26,38912
Maaa 188,28,140
Mvaa 252,28,136
D 21,48,65,1024
Ta 12,13,32768
Mvaa 252,24,40
Ta 14,13,33792
Ra 40,26,36864
Mvaa 220,24,36
Ra 36,26,34816
Mvaa 220,28,132
Mvaa 188,28,128
D 22,48,65,2048
Mvaa 188,24,32
Ra 32,26,32768
W 6
Maaa 96,0,60
Maaa 64,0,56
Maaa 224,0,52
D 23,48,65,3072
Ta 16,14,32768
Maaa 192,0,48
W 1
s_add_u32 s65, s5, 8
s_cmp_ge_u32 s65, 20
s_cselect_b32 s68, 20, 0
Maaa 160,0,44
s_sub_u32 s65, s65, s68
s_lshl_b32 s65, s65, 18
Ta 18,14,33792
Mvaa 224,0,40
s_add_u32 m0, s63, 65536
Mvaa 192,0,36
D 16,48,65,0
Mvaa 160,0,32
Mvaa 164,4,32
Mvaa 196,4,36
Mvaa 228,4,40
D 17,48,65,1024
Ta 20,15,32768
Maaa 164,4,44
Ta 22,15,33792
Maaa 196,4,48
Maaa 228,4,52
Maaa 68,4,56
D 18,48,65,2048
Maaa 100,4,60
Maaa 104,8,60
Maaa 72,8,56
Maaa 232,8,52
D 19,48,65,3072
Ta 24,24,32768
Maaa 200,8,48
s_add_u32 m0, s63, 69632
Ta 26,24,33792
Maaa 168,8,44
Mvaa 232,8,40
Mvaa 200,8,36
D 20,48,65,0
Mvaa 168,8,32
Mvaa 172,12,32
Mvaa 204,12,36
Mvaa 236,12,40
D 21,48,65,1024
Ta 28,25,32768
Maaa 172,12,44
Ta 30,25,33792
Maaa 204,12,48
Ra 156,27,47104
Maaa 236,12,52
Maaa 76,12,56
D 22,48,65,2048
Maaa 108,12,60
W 7
Ra 152,27,45056
Maaa 112,16,60
Maaa 80,16,56
Maaa 240,16,52
D 23,48,65,3072
v_min_u32 v139, s75, v138
Maaa 208,16,48
Ra 148,27,43008
Ta 0,10,40960
Maaa 176,16,44
Ta 2,10,41984
Mvaa 240,16,40
Ra 144,27,40960
Mvaa 208,16,36
buffer_load_dword v142, v144, s[8:11], s69 offen offset:0 sc1
Mvaa 176,16,32
W 10
Mvaa 180,20,32
Ra 140,27,38912
Mvaa 212,20,36
Mvaa 244,20,40
global_load_dword v142, v139, s[76:77]
v_add_u32 v138, s73, v138
Maaa 180,20,44
Ta 4,11,40960
Ta 6,11,41984
Maaa 212,20,48
Ra 136,27,36864
Maaa 244,20,52
v_bfe_u32 v29, v1, 7, 1
Maaa 84,20,56
Ra 132,27,34816
Maaa 116,20,60
W 13
v_mul_u32_u24 v28, 10240, v29
Maaa 120,24,60
Maaa 88,24,56
Ra 128,27,32768
v_lshrrev_b32 v29, 8, v1
Maaa 248,24,52
Ta 8,12,40960
Ta 10,12,41984
Maaa 216,24,48
v_lshl_add_u32 v28, v29, 7, v28
Maaa 184,24,44
v_and_b32 v29, 0x70, v1
Mvaa 248,24,40
Mvaa 216,24,36
v_add_u32 v28, v28, v29
)"
R"(Mvaa 184,24,32
W 14
Mvaa 188,28,32
s_and_b32 s80, s14, 2
Mvaa 220,28,36
s_mul_i32 s80, s80, 20480
Mvaa 252,28,40
Ta 12,13,40960
Ta 14,13,41984
Maaa 188,28,44
s_sub_u32 s81, 40960, s80
Maaa 220,28,48
Maaa 252,28,52
s_add_u32 s93, s28, 20480
Maaa 92,28,56
Maaa 124,28,60
W 10
v_add_u32 v26, s80, v28
Maaa 96,0,156
Maaa 64,0,152
Maaa 224,0,148
Ta 16,14,40960
v_add_u32 v27, s81, v28
Maaa 192,0,144
W 5
Ta 18,14,41984
Maaa 160,0,140
buffer_load_dwordx4 a[32:35], v26, s[8:11], s28 offen offset:0 sc1
Mvaa 224,0,136
Mvaa 192,0,132
Mvaa 160,0,128
Mvaa 164,4,128
buffer_load_dwordx4 a[36:39], v26, s[8:11], s93 offen offset:0 sc1
Mvaa 196,4,132
Mvaa 228,4,136
Ta 20,15,40960
Ta 22,15,41984
Maaa 164,4,140
Maaa 196,4,144
buffer_load_dwordx4 a[40:43], v27, s[8:11], s28 offen offset:0 sc1
Maaa 228,4,148
Maaa 68,4,152
Maaa 100,4,156
W 6
Maaa 104,8,156
buffer_load_dwordx4 a[44:47], v27, s[8:11], s93 offen offset:0 sc1
Ta 24,24,40960
Maaa 72,8,152
s_add_u32 s28, s28, 512
s_cmp_eq_u32 s28, s4
s_cselect_b32 s93, 10240, 0
Ta 26,24,41984
Maaa 232,8,148
s_sub_u32 s28, s28, s93
Ta 28,25,40960
Ta 30,25,41984
Maaa 200,8,144
s_add_u32 s93, s28, 20480
Maaa 168,8,140
Mvaa 232,8,136
Mvaa 200,8,132
s_add_u32 s65, s5, 12
s_cmp_ge_u32 s65, 20
s_cselect_b32 s68, 20, 0
buffer_load_dwordx4 a[48:51], v26, s[8:11], s28 offen offset:0 sc1
Mvaa 168,8,128
W 8
s_sub_u32 s65, s65, s68
s_lshl_b32 s65, s65, 18
Mvaa 172,12,128
s_add_u32 m0, s63, 32768
Mvaa 204,12,132
Mvaa 236,12,136
s_waitcnt vmcnt(7) lgkmcnt(0)
s_barrier
buffer_load_dwordx4 a[52:55], v26, s[8:11], s93 offen offset:0 sc1
Maaa 172,12,140
Maaa 204,12,144
Maaa 236,12,148
Maaa 76,12,152
buffer_load_dwordx4 a[56:59], v27, s[8:11], s28 offen offset:0 sc1
Maaa 108,12,156
Maaa 112,16,156
Maaa 80,16,152
Maaa 240,16,148
buffer_load_dwordx4 a[60:63], v27, s[8:11], s93 offen offset:0 sc1
Maaa 208,16,144
Maaa 176,16,140
Mvaa 240,16,136
Mvaa 208,16,132
D 16,48,65,0
Mvaa 176,16,128
Mvaa 180,20,128
Mvaa 212,20,132
Mvaa 244,20,136
D 17,48,65,1024
Ta 0,2,0
Maaa 180,20,140
Ta 2,2,1024
Maaa 212,20,144
Maaa 244,20,148
Maaa 84,20,152
D 18,48,65,2048
Ta 4,2,8192
Maaa 116,20,156
Ta 6,2,9216
Maaa 120,24,156
Maaa 88,24,152
Maaa 248,24,148
Ta 8,3,0
Ta 10,3,1024
Maaa 216,24,144
D 19,48,65,3072
Maaa 184,24,140
s_add_u32 m0, s63, 36864
Mvaa 248,24,136
Mvaa 216,24,132
Ta 12,3,8192
Ta 14,3,9216
Mvaa 184,24,128
D 20,48,65,0
Mvaa 188,28,128
Mvaa 220,28,132
Mvaa 252,28,136
Ta 16,4,0
Ta 18,4,1024
Maaa 188,28,140
Maaa 220,28,144
D 21,48,65,1024
Maaa 252,28,148
Maaa 92,28,152
Maaa 124,28,156
s_branch .Lq_10%=
.Lv_9%=:
s_waitcnt vmcnt(14)
s_barrier
Ta 0,10,16384
Ta 2,10,17408
Ra 32,26,0
Ta 4,10,24576
Ta 6,10,25600
Ra 128,27,0
Ra 132,27,2048
Ra 36,26,2048
Ra 40,26,4096
Ra 136,27,4096
Ra 140,27,6144
Ra 44,26,6144
Ra 48,26,8192
Ra 144,27,8192
W 6
Zvaa 160,32,0
s_add_u32 m0, s62, 32768
Ra 148,27,10240
Ra 52,26,10240
Ra 56,26,12288
Ra 152,27,12288
Ra 156,27,14336
Ra 60,26,14336
Mvaa 160,128,4
Ta 8,11,16384
Ta 10,11,17408
Ta 12,11,24576
Ta 14,11,25600
Zvaa 164,132,4
Ta 16,12,16384
Ta 18,12,17408
Mvaa 164,36,0
W 14
Ta 20,12,24576
Ta 22,12,25600
Zvaa 224,40,0
D 16,44,65,0
Mvaa 224,136,4
Zvaa 228,140,4
Mvaa 228,44,0
W 12
Zaaa 192,48,0
D 17,44,65,1024
Maaa 192,144,4
Zaaa 196,148,4
s_waitcnt vmcnt(13)
Maaa 196,52,0
W 8
v_lshlrev_b32 v56, 16, v32
v_and_b32 v57, 0xffff0000, v32
v_lshlrev_b32 v58, 16, v33
v_and_b32 v59, 0xffff0000, v33
Zaaa 64,56,0
D 18,44,65,2048
v_lshlrev_b32 v60, 16, v34
v_and_b32 v61, 0xffff0000, v34
v_lshlrev_b32 v62, 16, v35
v_and_b32 v63, 0xffff0000, v35
Maaa 64,152,4
v_lshlrev_b32 v64, 16, v36
v_and_b32 v65, 0xffff0000, v36
v_lshlrev_b32 v66, 16, v37
v_and_b32 v67, 0xffff0000, v37
v_lshlrev_b32 v68, 16, v38
Zaaa 68,156,4
v_and_b32 v69, 0xffff0000, v38
v_lshlrev_b32 v70, 16, v39
v_and_b32 v71, 0xffff0000, v39
v_lshlrev_b32 v72, 16, v40
Maaa 68,60,0
W 4
v_and_b32 v73, 0xffff0000, v40
v_lshlrev_b32 v74, 16, v41
v_and_b32 v75, 0xffff0000, v41
v_lshlrev_b32 v76, 16, v42
v_and_b32 v77, 0xffff0000, v42
Zaaa 76,60,8
D 19,44,65,3072
v_lshlrev_b32 v78, 16, v43
v_and_b32 v79, 0xffff0000, v43
A 80,56,57
A 81,58,59
Maaa 76,156,12
s_add_u32 m0, s62, 36864
A 80,80,60
A 81,81,61
A 80,80,62
A 81,81,63
A 80,80,64
Zaaa 72,152,12
Ta 24,13,16384
Ta 26,13,17408
A 81,81,65
A 80,80,66
A 81,81,67
A 80,80,68
Maaa 72,56,8
Ta 28,13,24576
Ta 30,13,25600
A 81,81,69
A 80,80,70
A 81,81,71
A 80,80,72
Zaaa 204,52,8
D 20,44,65,0
A 81,81,73
A 80,80,74
A 81,81,75
A 80,80,76
A 81,81,77
Maaa 204,148,12
A 80,80,78
A 81,81,79
A 80,80,81
s_nop 1
v_add_f32_dpp v80, v80, v80 quad_perm:[1,0,3,2] row_mask:0xf bank_mask:0xf
Zaaa 200,144,12
Maaa 200,48,8
v_add_f32_dpp v80, v80, v80 quad_perm:[2,3,0,1] row_mask:0xf bank_mask:0xf
s_nop 1
v_add_f32_dpp v80, v80, v80 row_half_mirror row_mask:0xf bank_mask:0xf
s_nop 1
v_add_f32_dpp v80, v80, v80 row_mirror row_mask:0xf bank_mask:0xf
v_mov_b32 v81, v80
s_nop 1
v_permlane16_swap_b32 v80, v81
A 80,80,81
Zvaa 236,44,8
D 21,44,65,1024
v_mul_f32 v84, 0x3acccccd, v80
s_mov_b32 s86, 0xffff
s_mov_b32 s87, 0xffff
v_sub_f32 v82, v56, v84
U 80,82,82
v_sub_f32 v83, v57, v84
Mvaa 236,140,12
v_fmac_f32 v80, v83, v83
v_sub_f32 v82, v58, v84
v_fmac_f32 v80, v82, v82
v_sub_f32 v83, v59, v84
v_fmac_f32 v80, v83, v83
v_sub_f32 v82, v60, v84
Zvaa 232,136,12
v_fmac_f32 v80, v82, v82
v_sub_f32 v83, v61, v84
v_fmac_f32 v80, v83, v83
v_sub_f32 v82, v62, v84
v_fmac_f32 v80, v82, v82
Mvaa 232,40,8
v_sub_f32 v83, v63, v84
v_fmac_f32 v80, v83, v83
v_sub_f32 v82, v64, v84
v_fmac_f32 v80, v82, v82
Zvaa 172,36,8
D 22,44,65,2048
v_sub_f32 v83, v65, v84
v_fmac_f32 v80, v83, v83
v_sub_f32 v82, v66, v84
v_fmac_f32 v80, v82, v82
Mvaa 172,132,12
v_sub_f32 v83, v67, v84
v_fmac_f32 v80, v83, v83
v_sub_f32 v82, v68, v84
v_fmac_f32 v80, v82, v82
v_sub_f32 v83, v69, v84
Zvaa 168,128,12
v_fmac_f32 v80, v83, v83
v_sub_f32 v82, v70, v84
v_fmac_f32 v80, v82, v82
v_sub_f32 v83, v71, v84
Mvaa 168,32,8
W 4
v_fmac_f32 v80, v83, v83
v_sub_f32 v82, v72, v84
U 81,82,82
v_sub_f32 v83, v73, v84
v_fmac_f32 v81, v83, v83
Zvaa 176,32,16
D 23,44,65,3072
v_sub_f32 v82, v74, v84
v_fmac_f32 v81, v82, v82
v_sub_f32 v83, v75, v84
v_fmac_f32 v81, v83, v83
Mvaa 176,128,20
v_sub_f32 v82, v76, v84
v_fmac_f32 v81, v82, v82
v_sub_f32 v83, v77, v84
v_fmac_f32 v81, v83, v83
v_sub_f32 v82, v78, v84
Zvaa 180,132,20
Ta 0,14,16384
Ta 2,14,17408
v_fmac_f32 v81, v82, v82
v_sub_f32 v83, v79, v84
v_fmac_f32 v81, v83, v83
v_cndmask_b32_e64 v81, 0, v81, s[86:87]
Mvaa 180,36,16
Ta 4,14,24576
Ta 6,14,25600
A 80,80,81
s_nop 1
v_add_f32_dpp v80, v80, v80 quad_perm:[1,0,3,2] row_mask:0xf bank_mask:0xf
s_nop 1
v_add_f32_dpp v80, v80, v80 quad_perm:[2,3,0,1] row_mask:0xf bank_mask:0xf
s_nop 1
v_add_f32_dpp v80, v80, v80 row_half_mirror row_mask:0xf bank_mask:0xf
Zvaa 240,40,16
global_load_dword v142, v[140:141], off
v_lshl_add_u64 v[140:141], v[140:141], 0, s[68:69]
Mvaa 240,136,20
v_add_f32_dpp v80, v80, v80 row_mirror row_mask:0xf bank_mask:0xf
v_mov_b32 v81, v80
s_nop 1
v_permlane16_swap_b32 v80, v81
A 80,80,81
v_mul_f32 v80, 0x3acccccd, v80
K 80,80,80
Zvaa 244,140,20
v_lshlrev_b32 v80, 16, v80
v_add_f32 v80, 0x3727c5ac, v80
K 80,80,80
v_lshlrev_b32 v80, 16, v80
v_sqrt_f32 v81, v80
Mvaa 244,44,16
v_rcp_f32 v82, v81
v_fma_f32 v83, -v81, v81, v80
v_mul_f32 v82, 0.5, v82
v_fma_f32 v81, v83, v82, v81
K 86,81,81
Zaaa 208,48,16
v_lshlrev_b32 v86, 16, v86
v_rcp_f32 v87, v86
K 85,84,84
v_lshlrev_b32 v85, 16, v85
Maaa 208,144,20
s_waitcnt vmcnt(17)
v_sub_f32 v56, v56, v85
v_sub_f32 v57, v57, v85
K 80,56,57
Zaaa 212,148,20
v_lshlrev_b32 v56, 16, v80
v_and_b32 v57, 0xffff0000, v80
U 80,56,87
U 81,57,87
K 32,80,81
Maaa 212,52,16
v_lshlrev_b32 v80, 16, v32
v_and_b32 v81, 0xffff0000, v32
v_lshlrev_b32 v82, 16, v44
v_and_b32 v83, 0xffff0000, v44
Zaaa 80,56,16
U 84,80,82
v_fmac_f32 v84, v81, v83
v_sub_f32 v58, v58, v85
v_sub_f32 v59, v59, v85
Maaa 80,152,20
K 80,58,59
v_lshlrev_b32 v58, 16, v80
v_and_b32 v59, 0xffff0000, v80
U 80,58,87
U 81,59,87
Zaaa 84,156,20
K 33,80,81
v_lshlrev_b32 v80, 16, v33
)"
R"(v_and_b32 v81, 0xffff0000, v33
v_lshlrev_b32 v82, 16, v45
Maaa 84,60,16
W 4
v_and_b32 v83, 0xffff0000, v45
v_fmac_f32 v84, v80, v82
v_fmac_f32 v84, v81, v83
v_sub_f32 v60, v60, v85
v_sub_f32 v61, v61, v85
Zaaa 92,60,24
K 80,60,61
v_lshlrev_b32 v60, 16, v80
v_and_b32 v61, 0xffff0000, v80
U 80,60,87
Maaa 92,156,28
U 81,61,87
K 34,80,81
v_lshlrev_b32 v80, 16, v34
v_and_b32 v81, 0xffff0000, v34
v_lshlrev_b32 v82, 16, v46
Zaaa 88,152,28
Ta 8,15,16384
Ta 10,15,17408
v_and_b32 v83, 0xffff0000, v46
v_fmac_f32 v84, v80, v82
v_fmac_f32 v84, v81, v83
v_sub_f32 v62, v62, v85
Maaa 88,56,24
Ta 12,15,24576
v_sub_f32 v63, v63, v85
K 80,62,63
v_lshlrev_b32 v62, 16, v80
v_and_b32 v63, 0xffff0000, v80
U 80,62,87
Zaaa 220,52,24
Ta 14,15,25600
U 81,63,87
K 35,80,81
v_lshlrev_b32 v80, 16, v35
v_and_b32 v81, 0xffff0000, v35
Maaa 220,148,28
v_lshlrev_b32 v82, 16, v47
v_and_b32 v83, 0xffff0000, v47
v_fmac_f32 v84, v80, v82
v_fmac_f32 v84, v81, v83
Zaaa 216,144,28
v_sub_f32 v64, v64, v85
v_sub_f32 v65, v65, v85
K 80,64,65
v_lshlrev_b32 v64, 16, v80
v_and_b32 v65, 0xffff0000, v80
Maaa 216,48,24
U 80,64,87
U 81,65,87
K 36,80,81
v_lshlrev_b32 v80, 16, v36
Zvaa 252,44,24
v_and_b32 v81, 0xffff0000, v36
v_lshlrev_b32 v82, 16, v48
v_and_b32 v83, 0xffff0000, v48
v_fmac_f32 v84, v80, v82
v_fmac_f32 v84, v81, v83
Mvaa 252,140,28
v_sub_f32 v66, v66, v85
v_sub_f32 v67, v67, v85
K 80,66,67
v_lshlrev_b32 v66, 16, v80
Zvaa 248,136,28
v_and_b32 v67, 0xffff0000, v80
U 80,66,87
U 81,67,87
K 37,80,81
v_lshlrev_b32 v80, 16, v37
Mvaa 248,40,24
v_and_b32 v81, 0xffff0000, v37
v_lshlrev_b32 v82, 16, v49
v_and_b32 v83, 0xffff0000, v49
v_fmac_f32 v84, v80, v82
Zvaa 188,36,24
v_fmac_f32 v84, v81, v83
v_sub_f32 v68, v68, v85
v_sub_f32 v69, v69, v85
K 80,68,69
v_lshlrev_b32 v68, 16, v80
Mvaa 188,132,28
v_and_b32 v69, 0xffff0000, v80
U 80,68,87
U 81,69,87
K 38,80,81
Zvaa 184,128,28
v_lshlrev_b32 v80, 16, v38
v_and_b32 v81, 0xffff0000, v38
v_lshlrev_b32 v82, 16, v50
v_and_b32 v83, 0xffff0000, v50
Mvaa 184,32,24
W 4
v_fmac_f32 v84, v80, v82
v_fmac_f32 v84, v81, v83
v_sub_f32 v70, v70, v85
v_sub_f32 v71, v71, v85
K 80,70,71
Zvaa 192,32,0
v_lshlrev_b32 v70, 16, v80
v_and_b32 v71, 0xffff0000, v80
U 80,70,87
U 81,71,87
Mvaa 192,128,4
K 39,80,81
v_lshlrev_b32 v80, 16, v39
v_and_b32 v81, 0xffff0000, v39
v_lshlrev_b32 v82, 16, v51
v_and_b32 v83, 0xffff0000, v51
Zvaa 196,132,4
Ta 16,24,16384
Ta 18,24,17408
v_fmac_f32 v84, v80, v82
v_fmac_f32 v84, v81, v83
v_sub_f32 v72, v72, v85
v_sub_f32 v73, v73, v85
Mvaa 196,36,0
Ta 20,24,24576
K 80,72,73
v_lshlrev_b32 v72, 16, v80
v_and_b32 v73, 0xffff0000, v80
U 80,72,87
U 81,73,87
Zaaa 160,40,0
Ta 22,24,25600
K 40,80,81
v_lshlrev_b32 v80, 16, v40
v_and_b32 v81, 0xffff0000, v40
v_lshlrev_b32 v82, 16, v52
Maaa 160,136,4
v_and_b32 v83, 0xffff0000, v52
v_fmac_f32 v84, v80, v82
v_fmac_f32 v84, v81, v83
v_sub_f32 v74, v74, v85
Zaaa 164,140,4
v_sub_f32 v75, v75, v85
K 80,74,75
v_lshlrev_b32 v74, 16, v80
v_and_b32 v75, 0xffff0000, v80
U 80,74,87
Maaa 164,44,0
U 81,75,87
K 41,80,81
v_lshlrev_b32 v80, 16, v41
v_and_b32 v81, 0xffff0000, v41
Zaaa 224,48,0
v_lshlrev_b32 v82, 16, v53
v_and_b32 v83, 0xffff0000, v53
v_fmac_f32 v84, v80, v82
v_fmac_f32 v84, v81, v83
v_sub_f32 v76, v76, v85
Maaa 224,144,4
v_sub_f32 v77, v77, v85
K 80,76,77
v_lshlrev_b32 v76, 16, v80
v_and_b32 v77, 0xffff0000, v80
Zaaa 228,148,4
U 80,76,87
U 81,77,87
K 42,80,81
v_lshlrev_b32 v80, 16, v42
v_and_b32 v81, 0xffff0000, v42
Maaa 228,52,0
v_lshlrev_b32 v82, 16, v54
v_and_b32 v83, 0xffff0000, v54
v_fmac_f32 v84, v80, v82
v_fmac_f32 v84, v81, v83
Zaaa 96,56,0
v_sub_f32 v78, v78, v85
v_sub_f32 v79, v79, v85
K 80,78,79
v_lshlrev_b32 v78, 16, v80
v_and_b32 v79, 0xffff0000, v80
Maaa 96,152,4
U 80,78,87
U 81,79,87
K 43,80,81
v_lshlrev_b32 v80, 16, v43
Zaaa 100,156,4
v_and_b32 v81, 0xffff0000, v43
v_lshlrev_b32 v82, 16, v55
v_and_b32 v83, 0xffff0000, v55
v_fmac_f32 v84, v80, v82
Maaa 100,60,0
W 4
v_fmac_f32 v84, v81, v83
v_lshrrev_b32 v82, 9, v1
s_lshl_b32 s80, s14, 1
v_add_u32 v82, s80, v82
v_mul_u32_u24 v83, 1552, v82
Zaaa 108,60,8
Ta 24,25,16384
Ta 26,25,17408
v_add_u32 v83, v83, v88
v_add_u32 v83, 131072, v83
ds_write_b128 v83, v[32:35] offset:0
ds_write_b128 v83, v[36:39] offset:512
Maaa 108,156,12
ds_write_b128 v83, v[40:43] offset:1024
v_add_f32_dpp v84, v84, v84 quad_perm:[1,0,3,2] row_mask:0xf bank_mask:0xf
s_nop 1
v_add_f32_dpp v84, v84, v84 quad_perm:[2,3,0,1] row_mask:0xf bank_mask:0xf
s_nop 1
v_add_f32_dpp v84, v84, v84 row_half_mirror row_mask:0xf bank_mask:0xf
s_nop 1
v_add_f32_dpp v84, v84, v84 row_mirror row_mask:0xf bank_mask:0xf
Zaaa 104,152,12
Ta 28,25,24576
Ta 30,25,25600
v_mov_b32 v81, v84
Maaa 104,56,8
Zaaa 236,52,8
v_permlane16_swap_b32 v84, v81
A 84,84,81
K 80,84,84
v_lshlrev_b32 v80, 16, v80
s_lshl_b32 s80, s15, 3
v_add_u32 v82, s80, v82
v_sub_u32 v83, 2047, v82
Maaa 236,148,12
v_ashrrev_i32 v81, 31, v80
v_or_b32 v81, 0x80000000, v81
v_xor_b32 v80, v80, v81
v_and_b32 v80, 0xffff0000, v80
v_or_b32 v80, v80, v83
v_and_b32 v82, 7, v82
Zaaa 232,144,12
v_lshlrev_b32 v82, 2, v82
v_add_u32 v82, 143488, v82
ds_write_b32 v82, v80
Maaa 232,48,8
Zaaa 172,44,8
Maaa 172,140,12
Zaaa 168,136,12
s_waitcnt vmcnt(1) lgkmcnt(0)
s_barrier
s_add_u32 s64, s64, 128
Maaa 168,40,8
v_bfe_u32 v80, v1, 4, 3
Zvaa 204,36,8
v_mul_u32_u24 v81, 1552, v80
Mvaa 204,132,12
s_and_b32 s64, s64, 1023
v_lshrrev_b32 v82, 7, v1
Zvaa 200,128,12
v_lshl_add_u32 v81, v82, 4, v81
Mvaa 200,32,8
s_lshl_b32 s80, s14, 7
Zvaa 208,32,16
s_add_u32 m0, s61, 0
s_add_u32 s80, s80, 131072
Mvaa 208,128,20
v_add_u32 v81, s80, v81
Zvaa 212,132,20
D 2,40,64,0
ds_read_b128 v[32:35], v81
Mvaa 212,36,16
Ta 0,10,49152
Ta 2,10,50176
Zaaa 176,40,16
Rv 36,81,512
Ta 4,10,57344
Maaa 176,136,20
s_and_b32 s80, s14, 2
s_lshl_b32 s80, s80, 7
Ta 6,10,58368
Zaaa 180,140,20
D 3,40,64,1024
v_subrev_u32 v81, s80, v81
Maaa 180,44,16
Rv 40,81,1024
Zaaa 240,48,16
v_lshlrev_b32 v80, 2, v80
Maaa 240,144,20
v_add_u32 v80, 143488, v80
Zaaa 244,148,20
D 4,40,64,2048
ds_read_b32 v84, v80
Maaa 244,52,16
v_mov_b32 v85, s13
Zaaa 112,56,16
v_lshrrev_b32 v83, 1, v1
Maaa 112,152,20
s_mul_i32 s82, s15, 10240
Zaaa 116,156,20
s_add_u32 s82, s82, 131072
D 5,40,64,3072
Maaa 116,60,16
s_add_u32 m0, s61, 4096
s_lshl_b32 s80, s14, 10
Zaaa 124,60,24
s_add_u32 s83, s82, s80
Ra 60,26,47104
Maaa 124,156,28
Ra 156,27,47104
Zaaa 120,152,28
D 6,40,64,0
Ra 152,27,45056
Maaa 120,56,24
Ta 8,11,49152
Ta 10,11,50176
Zaaa 252,52,24
Ra 56,26,45056
Ta 12,11,57344
Maaa 252,148,28
Ta 14,11,58368
Ra 52,26,43008
Zaaa 248,144,28
W 9
buffer_store_dwordx4 v[32:35], v1, s[8:11], s83 offen sc1
s_add_u32 s83, s83, 4096
Maaa 248,48,24
Ra 148,27,43008
Ra 144,27,40960
Zaaa 188,44,24
Ra 48,26,40960
Ra 44,26,38912
Maaa 188,140,28
Ra 140,27,38912
Zaaa 184,136,28
D 7,40,64,1024
Ra 136,27,36864
Maaa 184,40,24
Ra 40,26,36864
Zvaa 220,36,24
Ra 36,26,34816
Mvaa 220,132,28
Ra 132,27,34816
Zvaa 216,128,28
buffer_store_dwordx4 v[36:39], v1, s[8:11], s83 offen sc1
Ra 128,27,32768
Mvaa 216,32,24
W 13
s_and_b32 s80, s80, 1024
s_add_u32 s83, s82, s80
Maaa 68,60,0
s_add_u32 s83, s83, 8192
Ra 32,26,32768
Maaa 68,156,4
Maaa 64,152,4
D 8,40,64,2048
Ta 16,12,49152
Maaa 64,56,0
W 9
Ta 18,12,50176
Ta 20,12,57344
Maaa 196,52,0
Ta 22,12,58368
Maaa 196,148,4
Maaa 192,144,4
D 9,40,64,3072
Maaa 192,48,0
W 8
s_lshl_b32 s65, s64, 12
s_add_u32 m0, s62, 0
Mvaa 228,44,0
Mvaa 228,140,4
Mvaa 224,136,4
buffer_store_dwordx4 v[40:43], v1, s[8:11], s83 offen sc1
Mvaa 224,40,0
W 4
Mvaa 164,36,0
Mvaa 164,132,4
Mvaa 160,128,4
D 16,44,65,0
Mvaa 160,32,0
Mvaa 168,32,8
Mvaa 168,128,12
Mvaa 172,132,12
D 17,44,65,1024
Ta 24,13,49152
Mvaa 172,36,8
Ta 26,13,50176
Ta 28,13,57344
Mvaa 232,40,8
Ta 30,13,58368
Mvaa 232,136,12
Mvaa 236,140,12
D 18,44,65,2048
Mvaa 236,44,8
Maaa 200,48,8
Maaa 200,144,12
Maaa 204,148,12
D 19,44,65,3072
Maaa 204,52,8
s_add_u32 m0, s62, 4096
Maaa 72,56,8
Maaa 72,152,12
Maaa 76,156,12
D 20,44,65,0
Maaa 76,60,8
W 4
Maaa 84,60,16
Maaa 84,156,20
Maaa 80,152,20
D 21,44,65,1024
Ta 0,14,49152
)"
R"(Maaa 80,56,16
Ta 2,14,50176
Ta 4,14,57344
Maaa 212,52,16
Ta 6,14,58368
Maaa 212,148,20
Maaa 208,144,20
D 22,44,65,2048
Maaa 208,48,16
Mvaa 244,44,16
Mvaa 244,140,20
Mvaa 240,136,20
D 23,44,65,3072
Mvaa 240,40,16
Mvaa 180,36,16
Mvaa 180,132,20
Mvaa 176,128,20
global_load_dword v142, v[140:141], off
v_lshl_add_u64 v[140:141], v[140:141], 0, s[68:69]
Mvaa 176,32,16
W 4
Mvaa 184,32,24
Mvaa 184,128,28
Mvaa 188,132,28
Ta 8,15,49152
Ta 10,15,50176
Mvaa 188,36,24
Ta 12,15,57344
Ta 14,15,58368
Mvaa 248,40,24
Mvaa 248,136,28
Mvaa 252,140,28
Mvaa 252,44,24
Maaa 216,48,24
Maaa 216,144,28
Maaa 220,148,28
Maaa 220,52,24
Maaa 88,56,24
Maaa 88,152,28
Maaa 92,156,28
Maaa 92,60,24
W 4
Maaa 100,60,0
Maaa 100,156,4
Maaa 96,152,4
Ta 16,24,49152
Ta 18,24,50176
Maaa 96,56,0
Ta 20,24,57344
Ta 22,24,58368
Maaa 228,52,0
Maaa 228,148,4
Maaa 224,144,4
Maaa 224,48,0
Maaa 164,44,0
Maaa 164,140,4
Maaa 160,136,4
Maaa 160,40,0
Mvaa 196,36,0
Mvaa 196,132,4
Mvaa 192,128,4
Mvaa 192,32,0
W 4
Mvaa 200,32,8
Ta 24,25,49152
Ta 26,25,50176
Mvaa 200,128,12
Mvaa 204,132,12
Ta 28,25,57344
Ta 30,25,58368
Mvaa 204,36,8
Maaa 168,40,8
Maaa 168,136,12
Maaa 172,140,12
Maaa 172,44,8
Maaa 232,48,8
Maaa 232,144,12
Maaa 236,148,12
s_waitcnt vmcnt(1) lgkmcnt(0)
s_barrier
s_cmp_eq_u32 s14, 0
s_cbranch_scc0 .Lpubr_17%=
s_lshl_b32 s80, s15, 6
s_add_u32 s80, s80, 40960
s_mov_b64 exec, 0xff
buffer_store_dwordx2 v[84:85], v83, s[8:11], s80 offen sc1
s_mov_b64 exec, -1
.Lpubr_17%=:
Maaa 236,52,8
s_add_u32 s64, s64, 128
Maaa 104,56,8
Maaa 104,152,12
s_and_b32 s64, s64, 1023
Maaa 108,156,12
Maaa 108,60,8
s_add_u32 m0, s61, 32768
Maaa 116,60,16
Maaa 116,156,20
Maaa 112,152,20
D 2,40,64,0
Ta 0,10,16384
Maaa 112,56,16
Ta 2,10,17408
Ta 4,10,24576
Maaa 244,52,16
Ta 6,10,25600
Maaa 244,148,20
Maaa 240,144,20
D 3,40,64,1024
Maaa 240,48,16
Maaa 180,44,16
Maaa 180,140,20
Maaa 176,136,20
D 4,40,64,2048
Maaa 176,40,16
Mvaa 212,36,16
Mvaa 212,132,20
Mvaa 208,128,20
D 5,40,64,3072
Mvaa 208,32,16
s_add_u32 m0, s61, 36864
Mvaa 216,32,24
Ra 32,26,0
Mvaa 216,128,28
Ra 128,27,0
Mvaa 220,132,28
D 6,40,64,0
Ra 132,27,2048
Mvaa 220,36,24
Ta 8,11,16384
Ta 10,11,17408
Maaa 184,40,24
Ra 36,26,2048
Ta 12,11,24576
Maaa 184,136,28
Ta 14,11,25600
Ra 40,26,4096
Maaa 188,140,28
D 7,40,64,1024
Ra 136,27,4096
Maaa 188,44,24
Ra 140,27,6144
Ra 44,26,6144
Maaa 248,48,24
Ra 48,26,8192
Maaa 248,144,28
Ra 144,27,8192
Maaa 252,148,28
D 8,40,64,2048
Ra 148,27,10240
Maaa 252,52,24
Ra 52,26,10240
Maaa 120,56,24
Ra 56,26,12288
Maaa 120,152,28
Ra 152,27,12288
Maaa 124,156,28
D 9,40,64,3072
Ra 156,27,14336
Maaa 124,60,24
W 13
s_lshl_b32 s65, s64, 12
s_add_u32 m0, s62, 32768
Mvaa 160,32,0
Ra 60,26,14336
Mvaa 160,128,4
Mvaa 164,132,4
D 16,44,65,0
Ta 16,12,16384
Mvaa 164,36,0
W 9
Ta 18,12,17408
Ta 20,12,24576
Mvaa 224,40,0
Ta 22,12,25600
Mvaa 224,136,4
Mvaa 228,140,4
D 17,44,65,1024
Mvaa 228,44,0
W 8
Maaa 192,48,0
Maaa 192,144,4
Maaa 196,148,4
D 18,44,65,2048
Maaa 196,52,0
W 4
Maaa 64,56,0
Maaa 64,152,4
Maaa 68,156,4
D 19,44,65,3072
Maaa 68,60,0
s_add_u32 m0, s62, 36864
Maaa 76,60,8
Maaa 76,156,12
Maaa 72,152,12
D 20,44,65,0
Ta 24,13,16384
Maaa 72,56,8
Ta 26,13,17408
Ta 28,13,24576
Maaa 204,52,8
Ta 30,13,25600
Maaa 204,148,12
Maaa 200,144,12
D 21,44,65,1024
Maaa 200,48,8
Mvaa 236,44,8
Mvaa 236,140,12
Mvaa 232,136,12
D 22,44,65,2048
Mvaa 232,40,8
Mvaa 172,36,8
Mvaa 172,132,12
Mvaa 168,128,12
D 23,44,65,3072
Mvaa 168,32,8
W 4
s_add_u32 s65, s5, 0
s_cmp_ge_u32 s65, 20
s_cselect_b32 s68, 20, 0
Mvaa 176,32,16
s_sub_u32 s65, s65, s68
s_lshl_b32 s65, s65, 18
s_add_u32 m0, s63, 131072
Mvaa 176,128,20
Mvaa 180,132,20
D 16,48,65,0
Ta 0,14,16384
Mvaa 180,36,16
Ta 2,14,17408
Ta 4,14,24576
Mvaa 240,40,16
Ta 6,14,25600
Mvaa 240,136,20
Mvaa 244,140,20
D 17,48,65,1024
Mvaa 244,44,16
Maaa 208,48,16
Maaa 208,144,20
Maaa 212,148,20
D 18,48,65,2048
Maaa 212,52,16
Maaa 80,56,16
Maaa 80,152,20
Maaa 84,156,20
D 19,48,65,3072
Maaa 84,60,16
W 4
s_add_u32 m0, s63, 135168
Maaa 92,60,24
Maaa 92,156,28
Maaa 88,152,28
D 20,48,65,0
Ta 8,15,16384
Maaa 88,56,24
Ta 10,15,17408
Ta 12,15,24576
Maaa 220,52,24
Ta 14,15,25600
Maaa 220,148,28
Maaa 216,144,28
D 21,48,65,1024
Maaa 216,48,24
Mvaa 252,44,24
Mvaa 252,140,28
Mvaa 248,136,28
D 22,48,65,2048
Mvaa 248,40,24
Mvaa 188,36,24
Mvaa 188,132,28
Mvaa 184,128,28
D 23,48,65,3072
Mvaa 184,32,24
W 4
Mvaa 192,32,0
Mvaa 192,128,4
Mvaa 196,132,4
Ta 16,24,16384
Ta 18,24,17408
Mvaa 196,36,0
Ta 20,24,24576
Ta 22,24,25600
Maaa 160,40,0
Maaa 160,136,4
Maaa 164,140,4
Maaa 164,44,0
Maaa 224,48,0
Maaa 224,144,4
Maaa 228,148,4
Maaa 228,52,0
Maaa 96,56,0
Maaa 96,152,4
Maaa 100,156,4
Maaa 100,60,0
W 4
Maaa 108,60,8
Ta 24,25,16384
Ta 26,25,17408
Maaa 108,156,12
Maaa 104,152,12
Ta 28,25,24576
Ta 30,25,25600
Maaa 104,56,8
Maaa 236,52,8
Maaa 236,148,12
Maaa 232,144,12
Maaa 232,48,8
Maaa 172,44,8
Maaa 172,140,12
Maaa 168,136,12
s_waitcnt vmcnt(0) lgkmcnt(0)
s_barrier
s_add_u32 s64, s64, 128
Maaa 168,40,8
Mvaa 204,36,8
Mvaa 204,132,12
s_and_b32 s64, s64, 1023
Mvaa 200,128,12
Mvaa 200,32,8
Mvaa 208,32,16
s_add_u32 m0, s61, 0
Mvaa 208,128,20
Mvaa 212,132,20
D 2,40,64,0
Ta 0,10,49152
Mvaa 212,36,16
Ta 2,10,50176
Ta 4,10,57344
Maaa 176,40,16
Ta 6,10,58368
Maaa 176,136,20
Maaa 180,140,20
D 3,40,64,1024
Maaa 180,44,16
Maaa 240,48,16
Maaa 240,144,20
Maaa 244,148,20
D 4,40,64,2048
Maaa 244,52,16
Maaa 112,56,16
Maaa 112,152,20
Maaa 116,156,20
D 5,40,64,3072
Maaa 116,60,16
s_add_u32 m0, s61, 4096
Maaa 124,60,24
Ra 60,26,47104
v_lshrrev_b32 v28, 1, v1
Maaa 124,156,28
s_lshl_b32 s80, s60, 9
Ra 156,27,47104
Maaa 120,152,28
s_add_u32 s80, s80, 40960
D 6,40,64,0
Ra 152,27,45056
Maaa 120,56,24
Ta 8,11,49152
Ta 10,11,50176
Maaa 252,52,24
Ra 56,26,45056
Ta 12,11,57344
Maaa 252,148,28
Ta 14,11,58368
Ra 52,26,43008
Maaa 248,144,28
D 7,40,64,1024
Ra 148,27,43008
Maaa 248,48,24
Ra 144,27,40960
Ra 48,26,40960
Maaa 188,44,24
Ra 44,26,38912
Maaa 188,140,28
Ra 140,27,38912
Maaa 184,136,28
buffer_load_dwordx2 v[28:29], v28, s[8:11], s80 offen sc1
Ra 136,27,36864
Maaa 184,40,24
Ra 40,26,36864
Mvaa 220,36,24
Ra 36,26,34816
Mvaa 220,132,28
Ra 132,27,34816
Mvaa 216,128,28
D 8,40,64,2048
Ra 128,27,32768
Mvaa 216,32,24
W 13
Ra 32,26,32768
Maaa 68,60,0
Maaa 68,156,4
Maaa 64,152,4
D 9,40,64,3072
Ta 16,12,49152
Maaa 64,56,0
W 9
s_lshl_b32 s65, s64, 12
s_add_u32 m0, s62, 0
Maaa 196,52,0
Ta 18,12,50176
)"
R"(Ta 20,12,57344
Maaa 196,148,4
Ta 22,12,58368
Maaa 192,144,4
D 16,44,65,0
Maaa 192,48,0
W 8
Mvaa 228,44,0
Mvaa 228,140,4
Mvaa 224,136,4
D 17,44,65,1024
Mvaa 224,40,0
W 4
Mvaa 164,36,0
Mvaa 164,132,4
Mvaa 160,128,4
D 18,44,65,2048
Mvaa 160,32,0
Mvaa 168,32,8
Mvaa 168,128,12
Mvaa 172,132,12
D 19,44,65,3072
Ta 24,13,49152
Mvaa 172,36,8
s_add_u32 m0, s62, 4096
Ta 26,13,50176
Ta 28,13,57344
Mvaa 232,40,8
Ta 30,13,58368
Mvaa 232,136,12
Mvaa 236,140,12
D 20,44,65,0
Mvaa 236,44,8
Maaa 200,48,8
Maaa 200,144,12
Maaa 204,148,12
D 21,44,65,1024
Maaa 204,52,8
Maaa 72,56,8
Maaa 72,152,12
Maaa 76,156,12
D 22,44,65,2048
Maaa 76,60,8
W 4
Maaa 84,60,16
Maaa 84,156,20
Maaa 80,152,20
D 23,44,65,3072
Ta 0,14,49152
Maaa 80,56,16
Ta 2,14,50176
Ta 4,14,57344
Maaa 212,52,16
Ta 6,14,58368
Maaa 212,148,20
Maaa 208,144,20
s_waitcnt vmcnt(10)
v_cmp_eq_u32 vcc, s13, v29
s_cmp_eq_u64 vcc, -1
s_cbranch_scc0 .Lspin_18%=
.Lok_19%=:
Maaa 208,48,16
global_load_dword v142, v[140:141], off
v_lshl_add_u64 v[140:141], v[140:141], 0, s[68:69]
Mvaa 244,44,16
v_bfe_u32 v144, v1, 8, 1
Mvaa 244,140,20
v_mul_u32_u24 v144, 10240, v144
Mvaa 240,136,20
v_and_b32 v145, 0xf0, v1
Mvaa 240,40,16
v_lshl_add_u32 v144, v145, 3, v144
Mvaa 180,36,16
s_mul_i32 s69, s58, 20480
Mvaa 180,132,20
s_add_u32 s69, s69, s28
Mvaa 176,128,20
buffer_load_dword v142, v144, s[8:11], s69 offen offset:0 sc1
Mvaa 176,32,16
W 4
Mvaa 184,32,24
Mvaa 184,128,28
Mvaa 188,132,28
Ta 8,15,49152
Ta 10,15,50176
Mvaa 188,36,24
Ta 12,15,57344
Ta 14,15,58368
Mvaa 248,40,24
Mvaa 248,136,28
Mvaa 252,140,28
Mvaa 252,44,24
Maaa 216,48,24
Maaa 216,144,28
Maaa 220,148,28
Maaa 220,52,24
Maaa 88,56,24
Maaa 88,152,28
Maaa 92,156,28
Maaa 92,60,24
W 4
Maaa 100,60,0
Maaa 100,156,4
Maaa 96,152,4
Ta 16,24,49152
Ta 18,24,50176
Maaa 96,56,0
Ta 20,24,57344
Ta 22,24,58368
Maaa 228,52,0
Maaa 228,148,4
Maaa 224,144,4
Maaa 224,48,0
Maaa 164,44,0
Maaa 164,140,4
Maaa 160,136,4
Maaa 160,40,0
Mvaa 196,36,0
Mvaa 196,132,4
Mvaa 192,128,4
Mvaa 192,32,0
W 4
Mvaa 200,32,8
Ta 24,25,49152
Ta 26,25,50176
Mvaa 200,128,12
Mvaa 204,132,12
Ta 28,25,57344
Ta 30,25,58368
Mvaa 204,36,8
Maaa 168,40,8
Maaa 168,136,12
Maaa 172,140,12
Maaa 172,44,8
Maaa 232,48,8
Maaa 232,144,12
Maaa 236,148,12
s_waitcnt vmcnt(2) lgkmcnt(0)
s_barrier
s_add_u32 s64, s64, 128
Maaa 236,52,8
Maaa 104,56,8
Maaa 104,152,12
s_and_b32 s64, s64, 1023
Maaa 108,156,12
Maaa 108,60,8
Maaa 116,60,16
s_add_u32 m0, s61, 32768
Maaa 116,156,20
Maaa 112,152,20
Ta 0,10,16384
Ta 2,10,17408
Maaa 112,56,16
D 2,40,64,0
Ta 4,10,24576
Maaa 244,52,16
Ta 6,10,25600
Maaa 244,148,20
Maaa 240,144,20
Maaa 240,48,16
D 3,40,64,1024
Maaa 180,44,16
Maaa 180,140,20
Maaa 176,136,20
Maaa 176,40,16
D 4,40,64,2048
Mvaa 212,36,16
Mvaa 212,132,20
Mvaa 208,128,20
Mvaa 208,32,16
D 5,40,64,3072
Mvaa 216,32,24
s_add_u32 m0, s61, 36864
Ra 32,26,0
Mvaa 216,128,28
Ra 128,27,0
Mvaa 220,132,28
Ra 132,27,2048
Ta 8,11,16384
Mvaa 220,36,24
D 6,40,64,0
Ta 10,11,17408
Maaa 184,40,24
Ra 36,26,2048
Ta 12,11,24576
Maaa 184,136,28
Ta 14,11,25600
Ra 40,26,4096
Maaa 188,140,28
Ra 136,27,4096
Ra 140,27,6144
Maaa 188,44,24
D 7,40,64,1024
Ra 44,26,6144
Maaa 248,48,24
Ra 48,26,8192
Maaa 248,144,28
Ra 144,27,8192
Maaa 252,148,28
Ra 148,27,10240
Maaa 252,52,24
D 8,40,64,2048
Ra 52,26,10240
Maaa 120,56,24
Ra 56,26,12288
Maaa 120,152,28
Ra 152,27,12288
Maaa 124,156,28
Ra 156,27,14336
Maaa 124,60,24
W 13
D 9,40,64,3072
Ra 60,26,14336
Mvaa 160,32,0
s_lshl_b32 s65, s64, 12
s_add_u32 m0, s62, 32768
Mvaa 160,128,4
Mvaa 164,132,4
Ta 16,12,16384
Ta 18,12,17408
Mvaa 164,36,0
W 10
D 16,44,65,0
Ta 20,12,24576
Mvaa 224,40,0
Ta 22,12,25600
Mvaa 224,136,4
Mvaa 228,140,4
Mvaa 228,44,0
W 8
D 17,44,65,1024
Maaa 192,48,0
Maaa 192,144,4
Maaa 196,148,4
Maaa 196,52,0
W 4
D 18,44,65,2048
Maaa 64,56,0
Maaa 64,152,4
Maaa 68,156,4
Maaa 68,60,0
D 19,44,65,3072
Maaa 76,60,8
s_add_u32 m0, s62, 36864
Maaa 76,156,12
Maaa 72,152,12
Ta 24,13,16384
Ta 26,13,17408
Maaa 72,56,8
D 20,44,65,0
Ta 28,13,24576
Maaa 204,52,8
Ta 30,13,25600
Maaa 204,148,12
Maaa 200,144,12
Maaa 200,48,8
D 21,44,65,1024
Mvaa 236,44,8
Mvaa 236,140,12
Mvaa 232,136,12
Mvaa 232,40,8
D 22,44,65,2048
Mvaa 172,36,8
Mvaa 172,132,12
Mvaa 168,128,12
Mvaa 168,32,8
W 4
D 23,44,65,3072
Mvaa 176,32,16
v_lshrrev_b32 v138, 4, v1
Mvaa 176,128,20
Mvaa 180,132,20
s_sub_u32 s81, s73, 1
Ta 0,14,16384
Ta 2,14,17408
Mvaa 180,36,16
Ta 4,14,24576
v_min_u32 v138, s81, v138
Mvaa 240,40,16
Ta 6,14,25600
v_lshrrev_b32 v139, 2, v138
Mvaa 240,136,20
v_mul_u32_u24 v139, s72, v139
Mvaa 244,140,20
v_and_b32 v138, 3, v138
Mvaa 244,44,16
v_lshl_add_u32 v138, v138, 7, v139
Maaa 208,48,16
v_add_u32 v138, s74, v138
Maaa 208,144,20
s_lshr_b32 s73, s73, 2
Maaa 212,148,20
s_mul_i32 s73, s73, s72
buffer_load_dword v142, v144, s[8:11], s69 offen offset:2048 sc1
Maaa 212,52,16
v_min_u32 v139, s75, v138
Maaa 80,56,16
Maaa 80,152,20
s_add_u32 s69, s69, 4096
Maaa 84,156,20
global_load_dword v142, v139, s[76:77]
v_add_u32 v138, s73, v138
Maaa 84,60,16
W 4
Maaa 92,60,24
Maaa 92,156,28
Maaa 88,152,28
Ta 8,15,16384
Ta 10,15,17408
Maaa 88,56,24
Ta 12,15,24576
Ta 14,15,25600
Maaa 220,52,24
Maaa 220,148,28
Maaa 216,144,28
Maaa 216,48,24
Mvaa 252,44,24
Mvaa 252,140,28
Mvaa 248,136,28
Mvaa 248,40,24
Mvaa 188,36,24
Mvaa 188,132,28
Mvaa 184,128,28
Mvaa 184,32,24
W 4
Mvaa 192,32,0
Mvaa 192,128,4
Mvaa 196,132,4
Ta 16,24,16384
Ta 18,24,17408
Mvaa 196,36,0
Ta 20,24,24576
Ta 22,24,25600
Maaa 160,40,0
Maaa 160,136,4
Maaa 164,140,4
Maaa 164,44,0
Maaa 224,48,0
Maaa 224,144,4
Maaa 228,148,4
Maaa 228,52,0
Maaa 96,56,0
Maaa 96,152,4
Maaa 100,156,4
Maaa 100,60,0
W 4
Maaa 108,60,8
Ta 24,25,16384
Ta 26,25,17408
Maaa 108,156,12
Maaa 104,152,12
Ta 28,25,24576
Ta 30,25,25600
Maaa 104,56,8
Maaa 236,52,8
Maaa 236,148,12
Maaa 232,144,12
Maaa 232,48,8
Maaa 172,44,8
Maaa 172,140,12
Maaa 168,136,12
s_waitcnt vmcnt(2) lgkmcnt(0)
s_barrier
s_add_u32 s64, s64, 128
Maaa 168,40,8
Mvaa 204,36,8
Mvaa 204,132,12
s_and_b32 s64, s64, 1023
Mvaa 200,128,12
Mvaa 200,32,8
Mvaa 208,32,16
s_add_u32 m0, s61, 0
Mvaa 208,128,20
Mvaa 212,132,20
Ta 0,10,49152
Ta 2,10,50176
Mvaa 212,36,16
D 2,40,64,0
Ta 4,10,57344
Maaa 176,40,16
Ta 6,10,58368
Maaa 176,136,20
Maaa 180,140,20
Maaa 180,44,16
D 3,40,64,1024
Maaa 240,48,16
Maaa 240,144,20
Maaa 244,148,20
Maaa 244,52,16
D 4,40,64,2048
Maaa 112,56,16
Maaa 112,152,20
Maaa 116,156,20
Maaa 116,60,16
D 5,40,64,3072
Maaa 124,60,24
s_add_u32 m0, s61, 4096
Ra 60,26,47104
Maaa 124,156,28
Ra 156,27,47104
)"
R"(Maaa 120,152,28
Ra 152,27,45056
Ta 8,11,49152
Maaa 120,56,24
D 6,40,64,0
Ta 10,11,50176
Maaa 252,52,24
Ra 56,26,45056
Ta 12,11,57344
Maaa 252,148,28
Ta 14,11,58368
Ra 52,26,43008
Maaa 248,144,28
Ra 148,27,43008
Ra 144,27,40960
Maaa 248,48,24
D 7,40,64,1024
Ra 48,26,40960
Maaa 188,44,24
Ra 44,26,38912
Maaa 188,140,28
Ra 140,27,38912
Maaa 184,136,28
Ra 136,27,36864
Maaa 184,40,24
D 8,40,64,2048
Ra 40,26,36864
Mvaa 220,36,24
Ra 36,26,34816
Mvaa 220,132,28
Ra 132,27,34816
Mvaa 216,128,28
Ra 128,27,32768
Mvaa 216,32,24
W 13
D 9,40,64,3072
Ra 32,26,32768
Maaa 68,60,0
s_lshl_b32 s65, s64, 12
s_add_u32 m0, s62, 0
Maaa 68,156,4
Maaa 64,152,4
Ta 16,12,49152
Ta 18,12,50176
Maaa 64,56,0
W 10
D 16,44,65,0
Ta 20,12,57344
Maaa 196,52,0
Ta 22,12,58368
Maaa 196,148,4
Maaa 192,144,4
Maaa 192,48,0
W 8
D 17,44,65,1024
Mvaa 228,44,0
Mvaa 228,140,4
Mvaa 224,136,4
Mvaa 224,40,0
W 4
D 18,44,65,2048
Mvaa 164,36,0
Mvaa 164,132,4
Mvaa 160,128,4
Mvaa 160,32,0
D 19,44,65,3072
Mvaa 168,32,8
s_add_u32 m0, s62, 4096
Mvaa 168,128,12
Mvaa 172,132,12
Ta 24,13,49152
Ta 26,13,50176
Mvaa 172,36,8
D 20,44,65,0
Ta 28,13,57344
Mvaa 232,40,8
Ta 30,13,58368
Mvaa 232,136,12
Mvaa 236,140,12
Mvaa 236,44,8
D 21,44,65,1024
Maaa 200,48,8
Maaa 200,144,12
Maaa 204,148,12
Maaa 204,52,8
D 22,44,65,2048
Maaa 72,56,8
Maaa 72,152,12
Maaa 76,156,12
Maaa 76,60,8
W 4
D 23,44,65,3072
Maaa 84,60,16
v_min_u32 v139, s75, v138
Maaa 84,156,20
Maaa 80,152,20
Ta 0,14,49152
Ta 2,14,50176
Maaa 80,56,16
buffer_load_dword v142, v144, s[8:11], s69 offen offset:0 sc1
Ta 4,14,57344
Maaa 212,52,16
Ta 6,14,58368
Maaa 212,148,20
Maaa 208,144,20
Maaa 208,48,16
global_load_dword v142, v139, s[76:77]
Mvaa 244,44,16
Mvaa 244,140,20
Mvaa 240,136,20
v_add_u32 v138, s73, v138
Mvaa 240,40,16
Mvaa 180,36,16
Mvaa 180,132,20
Mvaa 176,128,20
Mvaa 176,32,16
W 4
Mvaa 184,32,24
Mvaa 184,128,28
Mvaa 188,132,28
Ta 8,15,49152
Ta 10,15,50176
Mvaa 188,36,24
Ta 12,15,57344
Ta 14,15,58368
Mvaa 248,40,24
Mvaa 248,136,28
Mvaa 252,140,28
Mvaa 252,44,24
Maaa 216,48,24
Maaa 216,144,28
Maaa 220,148,28
Maaa 220,52,24
Maaa 88,56,24
Maaa 88,152,28
Maaa 92,156,28
Maaa 92,60,24
W 4
Maaa 100,60,0
Maaa 100,156,4
Maaa 96,152,4
Ta 16,24,49152
Ta 18,24,50176
Maaa 96,56,0
Ta 20,24,57344
Ta 22,24,58368
Maaa 228,52,0
Maaa 228,148,4
Maaa 224,144,4
Maaa 224,48,0
Maaa 164,44,0
Maaa 164,140,4
Maaa 160,136,4
Maaa 160,40,0
Mvaa 196,36,0
Mvaa 196,132,4
Mvaa 192,128,4
Mvaa 192,32,0
W 4
Mvaa 200,32,8
Ta 24,25,49152
Ta 26,25,50176
Mvaa 200,128,12
Mvaa 204,132,12
Ta 28,25,57344
Ta 30,25,58368
Mvaa 204,36,8
Maaa 168,40,8
Maaa 168,136,12
Maaa 172,140,12
Maaa 172,44,8
Maaa 232,48,8
Maaa 232,144,12
Maaa 236,148,12
s_waitcnt vmcnt(2) lgkmcnt(0)
s_barrier
s_cmp_eq_u32 s85, 107
s_cbranch_scc1 .Lrt_21%=
.Lrtr_22%=:
Maaa 236,52,8
s_add_u32 s64, s64, 128
Maaa 104,56,8
Maaa 104,152,12
s_and_b32 s64, s64, 1023
Maaa 108,156,12
Maaa 108,60,8
Maaa 116,60,16
s_add_u32 m0, s61, 32768
Maaa 116,156,20
Maaa 112,152,20
Ta 0,10,16384
Ta 2,10,17408
Maaa 112,56,16
D 2,40,64,0
Ta 4,10,24576
Maaa 244,52,16
Ta 6,10,25600
Maaa 244,148,20
Maaa 240,144,20
Maaa 240,48,16
D 3,40,64,1024
Maaa 180,44,16
Maaa 180,140,20
Maaa 176,136,20
Maaa 176,40,16
D 4,40,64,2048
Mvaa 212,36,16
Mvaa 212,132,20
Mvaa 208,128,20
Mvaa 208,32,16
D 5,40,64,3072
Mvaa 216,32,24
s_add_u32 m0, s61, 36864
Ra 32,26,0
Mvaa 216,128,28
Ra 128,27,0
Mvaa 220,132,28
Ra 132,27,2048
Ta 8,11,16384
Mvaa 220,36,24
D 6,40,64,0
Ta 10,11,17408
Maaa 184,40,24
Ra 36,26,2048
Ta 12,11,24576
Maaa 184,136,28
Ta 14,11,25600
Ra 40,26,4096
Maaa 188,140,28
Ra 136,27,4096
Ra 140,27,6144
Maaa 188,44,24
D 7,40,64,1024
Ra 44,26,6144
Maaa 248,48,24
Ra 48,26,8192
Maaa 248,144,28
Ra 144,27,8192
Maaa 252,148,28
Ra 148,27,10240
Maaa 252,52,24
D 8,40,64,2048
Ra 52,26,10240
Maaa 120,56,24
Ra 56,26,12288
Maaa 120,152,28
Ra 152,27,12288
Maaa 124,156,28
Ra 156,27,14336
Maaa 124,60,24
W 13
D 9,40,64,3072
Ra 60,26,14336
Mvaa 160,32,0
s_lshl_b32 s65, s64, 12
s_add_u32 m0, s62, 32768
Mvaa 160,128,4
Mvaa 164,132,4
Ta 16,12,16384
Ta 18,12,17408
Mvaa 164,36,0
W 10
D 16,44,65,0
Ta 20,12,24576
Mvaa 224,40,0
Ta 22,12,25600
Mvaa 224,136,4
Mvaa 228,140,4
Mvaa 228,44,0
W 8
D 17,44,65,1024
Maaa 192,48,0
Maaa 192,144,4
Maaa 196,148,4
Maaa 196,52,0
W 4
D 18,44,65,2048
Maaa 64,56,0
Maaa 64,152,4
Maaa 68,156,4
Maaa 68,60,0
D 19,44,65,3072
Maaa 76,60,8
s_add_u32 m0, s62, 36864
Maaa 76,156,12
Maaa 72,152,12
Ta 24,13,16384
Ta 26,13,17408
Maaa 72,56,8
D 20,44,65,0
Ta 28,13,24576
Maaa 204,52,8
Ta 30,13,25600
Maaa 204,148,12
Maaa 200,144,12
Maaa 200,48,8
D 21,44,65,1024
Mvaa 236,44,8
Mvaa 236,140,12
Mvaa 232,136,12
Mvaa 232,40,8
D 22,44,65,2048
Mvaa 172,36,8
Mvaa 172,132,12
Mvaa 168,128,12
Mvaa 168,32,8
W 4
D 23,44,65,3072
v_add_u32 v2, 131072, v10
Mvaa 176,32,16
v_add_u32 v3, 131072, v11
Mvaa 176,128,20
Mvaa 180,132,20
Ta 0,14,16384
Ta 2,14,17408
Mvaa 180,36,16
buffer_load_dword v142, v144, s[8:11], s69 offen offset:2048 sc1
v_add_u32 v4, 131072, v12
Mvaa 240,40,16
Ta 4,14,24576
Ta 6,14,25600
Mvaa 240,136,20
s_add_u32 s69, s69, 4096
v_add_u32 v5, 131072, v13
Mvaa 244,140,20
v_add_u32 v6, 131072, v14
Mvaa 244,44,16
Maaa 208,48,16
v_add_u32 v7, 131072, v15
Maaa 208,144,20
Maaa 212,148,20
v_add_u32 v8, 131072, v24
Maaa 212,52,16
Maaa 80,56,16
v_add_u32 v9, 131072, v25
Maaa 80,152,20
Maaa 84,156,20
Maaa 84,60,16
W 4
Maaa 92,60,24
Maaa 92,156,28
Maaa 88,152,28
Ta 8,15,16384
Ta 10,15,17408
Maaa 88,56,24
Ta 12,15,24576
Ta 14,15,25600
Maaa 220,52,24
Maaa 220,148,28
Maaa 216,144,28
Maaa 216,48,24
Mvaa 252,44,24
Mvaa 252,140,28
Mvaa 248,136,28
Mvaa 248,40,24
Mvaa 188,36,24
Mvaa 188,132,28
Mvaa 184,128,28
Mvaa 184,32,24
W 4
Mvaa 192,32,0
Mvaa 192,128,4
Mvaa 196,132,4
Ta 16,24,16384
Ta 18,24,17408
Mvaa 196,36,0
Ta 20,24,24576
Ta 22,24,25600
Maaa 160,40,0
Maaa 160,136,4
Maaa 164,140,4
Maaa 164,44,0
Maaa 224,48,0
Maaa 224,144,4
Maaa 228,148,4
Maaa 228,52,0
Maaa 96,56,0
Maaa 96,152,4
Maaa 100,156,4
Maaa 100,60,0
W 4
Maaa 108,60,8
Ta 24,25,16384
Ta 26,25,17408
Maaa 108,156,12
Maaa 104,152,12
Ta 28,25,24576
Ta 30,25,25600
Maaa 104,56,8
Maaa 236,52,8
Maaa 236,148,12
Maaa 232,144,12
Maaa 232,48,8
Maaa 172,44,8
Maaa 172,140,12
Maaa 168,136,12
s_waitcnt vmcnt(1) lgkmcnt(0)
s_barrier
s_cmp_eq_u32 s85, 107
s_cbranch_scc1 .Lrt_23%=
)"
R"(.Lrtr_24%=:
Maaa 168,40,8
s_add_u32 s65, s5, 4
Mvaa 204,36,8
Mvaa 204,132,12
s_cmp_ge_u32 s65, 20
s_cselect_b32 s68, 20, 0
Mvaa 200,128,12
Mvaa 200,32,8
s_sub_u32 s65, s65, s68
Mvaa 208,32,16
Mvaa 208,128,20
s_lshl_b32 s65, s65, 18
Mvaa 212,132,20
Ta 0,10,49152
Ta 2,10,50176
Mvaa 212,36,16
Maaa 176,40,16
s_add_u32 m0, s63, 0
Maaa 176,136,20
Maaa 180,140,20
D 16,48,65,0
Maaa 180,44,16
Maaa 240,48,16
Maaa 240,144,20
Maaa 244,148,20
D 17,48,65,1024
Ta 4,11,49152
Maaa 244,52,16
Ta 6,11,50176
Maaa 112,56,16
Maaa 112,152,20
Maaa 116,156,20
D 18,48,65,2048
Maaa 116,60,16
Maaa 124,60,24
Ra 60,26,47104
Maaa 124,156,28
Maaa 120,152,28
D 19,48,65,3072
Ta 8,12,49152
Maaa 120,56,24
s_add_u32 m0, s63, 4096
Ta 10,12,50176
Ra 56,26,45056
Maaa 252,52,24
Ra 52,26,43008
Maaa 252,148,28
Maaa 248,144,28
D 20,48,65,0
Maaa 248,48,24
Ra 48,26,40960
Maaa 188,44,24
Ra 44,26,38912
Maaa 188,140,28
Maaa 184,136,28
D 21,48,65,1024
Ta 12,13,49152
Maaa 184,40,24
Ta 14,13,50176
Ra 40,26,36864
Mvaa 220,36,24
Ra 36,26,34816
Mvaa 220,132,28
Mvaa 216,128,28
D 22,48,65,2048
Mvaa 216,32,24
Ra 32,26,32768
W 6
Maaa 68,60,0
Maaa 64,56,0
Maaa 196,52,0
D 23,48,65,3072
Ta 16,14,49152
Maaa 192,48,0
W 1
s_add_u32 s65, s5, 8
s_cmp_ge_u32 s65, 20
s_cselect_b32 s68, 20, 0
Mvaa 228,44,0
s_sub_u32 s65, s65, s68
s_lshl_b32 s65, s65, 18
Ta 18,14,50176
Mvaa 224,40,0
s_add_u32 m0, s63, 65536
Mvaa 164,36,0
D 16,48,65,0
Mvaa 160,32,0
Mvaa 168,32,4
Mvaa 172,36,4
Mvaa 232,40,4
D 17,48,65,1024
Ta 20,15,49152
Mvaa 236,44,4
Ta 22,15,50176
Maaa 200,48,4
Maaa 204,52,4
Maaa 72,56,4
D 18,48,65,2048
Maaa 76,60,4
Maaa 84,60,8
Maaa 80,56,8
Maaa 212,52,8
D 19,48,65,3072
Ta 24,24,49152
Maaa 208,48,8
s_add_u32 m0, s63, 69632
Ta 26,24,50176
Mvaa 244,44,8
Mvaa 240,40,8
Mvaa 180,36,8
D 20,48,65,0
Mvaa 176,32,8
Mvaa 184,32,12
Mvaa 188,36,12
Mvaa 248,40,12
D 21,48,65,1024
Ta 28,25,49152
Mvaa 252,44,12
Ta 30,25,50176
Maaa 216,48,12
Ra 156,27,47104
Maaa 220,52,12
Maaa 88,56,12
D 22,48,65,2048
Maaa 92,60,12
W 7
Ra 152,27,45056
Maaa 100,60,16
Maaa 96,56,16
Maaa 228,52,16
D 23,48,65,3072
v_min_u32 v139, s75, v138
Maaa 224,48,16
Ra 148,27,43008
Ta 0,10,57344
Maaa 164,44,16
Ta 2,10,58368
Maaa 160,40,16
Ra 144,27,40960
Mvaa 196,36,16
buffer_load_dword v142, v144, s[8:11], s69 offen offset:0 sc1
Mvaa 192,32,16
W 10
Mvaa 200,32,20
Ra 140,27,38912
Mvaa 204,36,20
Maaa 168,40,20
global_load_dword v142, v139, s[76:77]
v_add_u32 v138, s73, v138
Maaa 172,44,20
Ta 4,11,57344
Ta 6,11,58368
Maaa 232,48,20
Ra 136,27,36864
Maaa 236,52,20
v_bfe_u32 v29, v1, 7, 1
Maaa 104,56,20
Ra 132,27,34816
Maaa 108,60,20
W 13
v_mul_u32_u24 v28, 10240, v29
Maaa 116,60,24
Maaa 112,56,24
Ra 128,27,32768
v_lshrrev_b32 v29, 8, v1
Maaa 244,52,24
Ta 8,12,57344
Ta 10,12,58368
Maaa 240,48,24
v_lshl_add_u32 v28, v29, 7, v28
Maaa 180,44,24
v_and_b32 v29, 0x70, v1
Maaa 176,40,24
Mvaa 212,36,24
v_add_u32 v28, v28, v29
Mvaa 208,32,24
W 14
Mvaa 216,32,28
s_and_b32 s80, s14, 2
Mvaa 220,36,28
s_mul_i32 s80, s80, 20480
Maaa 184,40,28
Ta 12,13,57344
Ta 14,13,58368
Maaa 188,44,28
s_sub_u32 s81, 40960, s80
Maaa 248,48,28
Maaa 252,52,28
s_add_u32 s93, s28, 20480
Maaa 120,56,28
Maaa 124,60,28
W 10
v_add_u32 v26, s80, v28
Maaa 68,156,0
Maaa 64,152,0
Maaa 196,148,0
Ta 16,14,57344
v_add_u32 v27, s81, v28
Maaa 192,144,0
W 5
Ta 18,14,58368
Mvaa 228,140,0
buffer_load_dwordx4 a[32:35], v26, s[8:11], s28 offen offset:0 sc1
Mvaa 224,136,0
Mvaa 164,132,0
Mvaa 160,128,0
Mvaa 168,128,4
buffer_load_dwordx4 a[36:39], v26, s[8:11], s93 offen offset:0 sc1
Mvaa 172,132,4
Mvaa 232,136,4
Ta 20,15,57344
Ta 22,15,58368
Mvaa 236,140,4
Maaa 200,144,4
buffer_load_dwordx4 a[40:43], v27, s[8:11], s28 offen offset:0 sc1
Maaa 204,148,4
Maaa 72,152,4
Maaa 76,156,4
W 6
Maaa 84,156,8
buffer_load_dwordx4 a[44:47], v27, s[8:11], s93 offen offset:0 sc1
Ta 24,24,57344
Maaa 80,152,8
s_add_u32 s28, s28, 512
s_cmp_eq_u32 s28, s4
s_cselect_b32 s93, 10240, 0
Ta 26,24,58368
Maaa 212,148,8
s_sub_u32 s28, s28, s93
Ta 28,25,57344
Ta 30,25,58368
Maaa 208,144,8
s_add_u32 s93, s28, 20480
Mvaa 244,140,8
Mvaa 240,136,8
Mvaa 180,132,8
s_add_u32 s65, s5, 12
s_cmp_ge_u32 s65, 20
s_cselect_b32 s68, 20, 0
buffer_load_dwordx4 a[48:51], v26, s[8:11], s28 offen offset:0 sc1
Mvaa 176,128,8
W 8
s_sub_u32 s65, s65, s68
s_lshl_b32 s65, s65, 18
Mvaa 184,128,12
s_add_u32 m0, s63, 32768
Mvaa 188,132,12
Mvaa 248,136,12
s_waitcnt vmcnt(7) lgkmcnt(0)
s_barrier
buffer_load_dwordx4 a[52:55], v26, s[8:11], s93 offen offset:0 sc1
Mvaa 252,140,12
Maaa 216,144,12
Maaa 220,148,12
Maaa 88,152,12
buffer_load_dwordx4 a[56:59], v27, s[8:11], s28 offen offset:0 sc1
Maaa 92,156,12
Maaa 100,156,16
Maaa 96,152,16
Maaa 228,148,16
buffer_load_dwordx4 a[60:63], v27, s[8:11], s93 offen offset:0 sc1
Maaa 224,144,16
Maaa 164,140,16
Maaa 160,136,16
Mvaa 196,132,16
D 16,48,65,0
Mvaa 192,128,16
Mvaa 200,128,20
Mvaa 204,132,20
Maaa 168,136,20
D 17,48,65,1024
Ta 0,2,0
Maaa 172,140,20
Ta 2,2,1024
Maaa 232,144,20
Maaa 236,148,20
Maaa 104,152,20
D 18,48,65,2048
Ta 4,2,8192
Maaa 108,156,20
Ta 6,2,9216
Maaa 116,156,24
Maaa 112,152,24
Maaa 244,148,24
Ta 8,3,0
Ta 10,3,1024
Maaa 240,144,24
D 19,48,65,3072
Maaa 180,140,24
s_add_u32 m0, s63, 36864
Maaa 176,136,24
Mvaa 212,132,24
Ta 12,3,8192
Ta 14,3,9216
Mvaa 208,128,24
D 20,48,65,0
Mvaa 216,128,28
Mvaa 220,132,28
Maaa 184,136,28
Ta 16,4,0
Ta 18,4,1024
Maaa 188,140,28
Maaa 248,144,28
D 21,48,65,1024
Maaa 252,148,28
Maaa 120,152,28
Maaa 124,156,28
.Lq_10%=:
s_cmp_ge_u32 s14, 2
s_cbranch_scc1 .Lpb_29%=
s_waitcnt vmcnt(6) lgkmcnt(6)
Zvaa 32,0,32
s_add_u32 s28, s28, 512
Ta 20,4,8192
Ta 22,4,9216
Mvaa 32,4,48
s_cmp_eq_u32 s28, s4
s_cselect_b32 s93, 10240, 0
Ta 24,5,0
Ta 26,5,1024
Zvaa 64,4,52
s_sub_u32 s28, s28, s93
s_add_u32 s93, s28, 20480
buffer_load_dwordx4 a[128:131], v26, s[8:11], s28 offen offset:0 sc1
Mvaa 64,0,36
Ta 28,5,8192
Ta 30,5,9216
Zvaa 96,0,40
K 184,184,185
K 185,186,187
Mvaa 96,4,56
K 186,188,189
K 187,190,191
Zvaa 128,4,60
buffer_load_dwordx4 a[132:135], v26, s[8:11], s93 offen offset:0 sc1
K 192,192,193
Mvaa 128,0,44
W 8
Ta 0,6,0
K 193,194,195
Zvaa 132,8,44
Ta 2,6,1024
K 194,196,197
Mvaa 132,12,60
K 195,198,199
K 200,200,201
Zvaa 100,12,56
buffer_load_dwordx4 a[136:139], v27, s[8:11], s28 offen offset:0 sc1
K 201,202,203
Mvaa 100,8,40
Ta 4,6,8192
Ta 6,6,9216
Zvaa 68,8,36
K 202,204,205
K 203,206,207
Mvaa 68,12,52
K 208,208,209
K 209,210,211
Zvaa 36,12,48
buffer_load_dwordx4 a[140:143], v27, s[8:11], s93 offen offset:0 sc1
K 210,212,213
Mvaa 36,8,32
W 8
s_add_u32 s28, s28, 512
s_cmp_eq_u32 s28, s4
s_cselect_b32 s93, 10240, 0
Zvaa 40,16,32
s_sub_u32 s28, s28, s93
s_add_u32 s93, s28, 20480
K 211,214,215
Mvaa 40,20,48
Ta 8,7,0
Ta 10,7,1024
Zvaa 72,20,52
buffer_load_dwordx4 a[144:147], v26, s[8:11], s28 offen offset:0 sc1
K 216,216,217
Mvaa 72,16,36
K 217,218,219
K 218,220,221
K 219,222,223
Zvaa 104,16,40
Ta 12,7,8192
Ta 14,7,9216
Mvaa 104,20,56
K 224,224,225
K 225,226,227
Zvaa 136,20,60
K 226,228,229
K 227,230,231
Mvaa 136,16,44
W 8
buffer_load_dwordx4 a[148:151], v26, s[8:11], s93 offen offset:0 sc1
K 232,232,233
Zvaa 140,24,44
K 233,234,235
K 234,236,237
Mvaa 140,28,60
Ta 16,8,0
K 235,238,239
)"
R"(Zvaa 108,28,56
Ta 18,8,1024
K 240,240,241
Mvaa 108,24,40
buffer_load_dwordx4 a[152:155], v27, s[8:11], s28 offen offset:0 sc1
K 241,242,243
Zvaa 76,24,36
K 242,244,245
K 243,246,247
Mvaa 76,28,52
Ta 20,8,8192
Ta 22,8,9216
Zvaa 44,28,48
K 248,248,249
K 249,250,251
Mvaa 44,24,32
K 250,252,253
K 251,254,255
W 8
Zvaa 48,0,32
buffer_load_dwordx4 a[156:159], v27, s[8:11], s93 offen offset:0 sc1
Ta 24,9,0
Mvaa 48,4,48
s_add_u32 s28, s28, 512
s_cmp_eq_u32 s28, s4
s_cselect_b32 s93, 10240, 0
s_sub_u32 s28, s28, s93
Zvaa 80,4,52
s_add_u32 s93, s28, 20480
Ta 26,9,1024
K 160,160,161
Mvaa 80,0,36
Ta 28,9,8192
Ta 30,9,9216
Zvaa 112,0,40
buffer_load_dwordx4 v[188:191], v26, s[8:11], s28 offen offset:0 sc1
K 161,162,163
Mvaa 112,4,56
K 162,164,165
Zvaa 144,4,60
K 163,166,167
Mvaa 144,0,44
W 8
Ta 0,2,16384
Ta 2,2,17408
Zvaa 148,8,44
s_add_u32 s36, s29, 0
buffer_load_dwordx4 v[196:199], v26, s[8:11], s93 offen offset:0 sc1
Mvaa 148,12,60
Zvaa 116,12,56
Ta 4,2,24576
Ta 6,2,25600
Mvaa 116,8,40
Zvaa 84,8,36
buffer_load_dwordx4 v[204:207], v27, s[8:11], s28 offen offset:0 sc1
Mvaa 84,12,52
Zvaa 52,12,48
Mvaa 52,8,32
W 8
Ta 8,3,16384
Ta 10,3,17408
Zvaa 56,16,32
buffer_load_dwordx4 v[212:215], v27, s[8:11], s93 offen offset:0 sc1
Mvaa 56,20,48
s_add_u32 s28, s28, 512
s_cmp_eq_u32 s28, s4
s_cselect_b32 s93, 10240, 0
s_sub_u32 s28, s28, s93
Zvaa 88,20,52
s_add_u32 s93, s28, 20480
Ta 12,3,24576
Ta 14,3,25600
Mvaa 88,16,36
Zvaa 120,16,40
buffer_store_dwordx4 v[160:163], v1, s[8:11], s36 offen offset:0 sc1
K 168,168,169
Mvaa 120,20,56
K 169,170,171
K 170,172,173
Zvaa 152,20,60
Mvaa 152,16,44
W 8
Ta 16,4,16384
K 171,174,175
Zvaa 156,24,44
buffer_load_dwordx4 v[220:223], v26, s[8:11], s28 offen offset:0 sc1
Ta 18,4,17408
Mvaa 156,28,60
Zvaa 124,28,56
Ta 20,4,24576
Ta 22,4,25600
Mvaa 124,24,40
Zvaa 92,24,36
buffer_load_dwordx4 v[228:231], v26, s[8:11], s93 offen offset:0 sc1
Mvaa 92,28,52
Zvaa 60,28,48
Mvaa 60,24,32
s_waitcnt vmcnt(7) lgkmcnt(8)
Ta 24,5,16384
Ta 26,5,17408
Mvaa 32,0,128
buffer_load_dwordx4 v[236:239], v27, s[8:11], s28 offen offset:0 sc1
Mvaa 32,4,144
Mvaa 64,4,148
Ta 28,5,24576
Ta 30,5,25600
Mvaa 64,0,132
buffer_store_dwordx4 v[168:171], v1, s[8:11], s36 offen offset:1024 sc1
K 176,176,177
Mvaa 96,0,136
K 177,178,179
K 178,180,181
Mvaa 96,4,152
Mvaa 128,4,156
Mvaa 128,0,140
W 8
buffer_load_dwordx4 v[244:247], v27, s[8:11], s93 offen offset:0 sc1
K 179,182,183
Mvaa 132,8,140
s_add_u32 s28, s28, 512
s_cmp_eq_u32 s28, s4
s_cselect_b32 s93, 10240, 0
s_sub_u32 s28, s28, s93
Mvaa 132,12,156
s_add_u32 s93, s28, 20480
Ta 0,6,16384
Ta 2,6,17408
Mvaa 100,12,152
Ta 4,6,24576
Ta 6,6,25600
Mvaa 100,8,136
buffer_load_dwordx4 v[252:255], v26, s[8:11], s28 offen offset:0 sc1
Mvaa 68,8,132
Mvaa 68,12,148
Mvaa 36,12,144
Mvaa 36,8,128
W 8
D 22,48,65,2048
Ta 8,7,16384
Mvaa 40,16,128
Ta 10,7,17408
Mvaa 40,20,144
Mvaa 72,20,148
Ta 12,7,24576
Ta 14,7,25600
Mvaa 72,16,132
buffer_store_dwordx4 v[176:179], v1, s[8:11], s36 offen offset:2048 sc1
Mvaa 104,16,136
Mvaa 104,20,152
Mvaa 136,20,156
Mvaa 136,16,140
W 8
buffer_load_dwordx4 a[32:35], v26, s[8:11], s93 offen offset:0 sc1
Ta 16,8,16384
Mvaa 140,24,140
Ta 18,8,17408
Mvaa 140,28,156
Mvaa 108,28,152
Ta 20,8,24576
Ta 22,8,25600
Mvaa 108,24,136
buffer_load_dwordx4 a[36:39], v27, s[8:11], s28 offen offset:0 sc1
Mvaa 76,24,132
Mvaa 76,28,148
Mvaa 44,28,144
Mvaa 44,24,128
W 8
buffer_load_dwordx4 a[40:43], v27, s[8:11], s93 offen offset:0 sc1
Ta 24,9,16384
Mvaa 48,0,128
s_add_u32 s28, s28, 512
s_cmp_eq_u32 s28, s4
s_cselect_b32 s93, 10240, 0
s_sub_u32 s28, s28, s93
Mvaa 48,4,144
s_add_u32 s93, s28, 20480
Ta 26,9,17408
Mvaa 80,4,148
Ta 28,9,24576
Ta 30,9,25600
Mvaa 80,0,132
buffer_store_dwordx4 v[184:187], v1, s[8:11], s36 offen offset:3072 sc1
Mvaa 112,0,136
s_add_u32 s36, s29, 4096
s_lshl_b32 s80, s56, 9
Mvaa 112,4,152
Mvaa 144,4,156
s_lshl_b32 s81, s57, 4
Mvaa 144,0,140
W 8
s_add_u32 s80, s80, s81
buffer_load_dwordx4 a[44:47], v26, s[8:11], s28 offen offset:0 sc1
Mvaa 148,8,140
Ta 0,10,0
Ta 2,10,1024
Mvaa 148,12,156
s_and_b32 s81, s14, 1
Mvaa 116,12,152
s_lshl_b32 s81, s81, 3
Ta 4,10,8192
Ta 6,10,9216
Mvaa 116,8,136
buffer_load_dwordx4 a[48:51], v26, s[8:11], s93 offen offset:0 sc1
Mvaa 84,8,132
s_add_u32 s80, s80, s81
Mvaa 84,12,148
s_add_u32 s71, s80, 4096
Mvaa 52,12,144
Mvaa 52,8,128
W 8
buffer_load_dwordx4 a[52:55], v27, s[8:11], s28 offen offset:0 sc1
Ta 8,11,0
Mvaa 56,16,128
Ta 10,11,1024
Mvaa 56,20,144
Mvaa 88,20,148
buffer_store_dwordx4 v[192:195], v1, s[8:11], s36 offen offset:0 sc1
Ta 12,11,8192
Mvaa 88,16,132
Ta 14,11,9216
Mvaa 120,16,136
Mvaa 120,20,152
Mvaa 152,20,156
buffer_load_dwordx4 a[56:59], v27, s[8:11], s93 offen offset:0 sc1
Mvaa 152,16,140
W 8
s_add_u32 s28, s28, 512
s_cmp_eq_u32 s28, s4
s_cselect_b32 s93, 10240, 0
Mvaa 156,24,140
s_sub_u32 s28, s28, s93
s_add_u32 s93, s28, 20480
Ta 16,12,0
Mvaa 156,28,156
Ta 18,12,1024
Mvaa 124,28,152
buffer_load_dwordx4 a[60:63], v26, s[8:11], s28 offen offset:0 sc1
Ta 20,12,8192
Mvaa 124,24,136
Ta 22,12,9216
Mvaa 92,24,132
Mvaa 92,28,148
Mvaa 60,28,144
D 23,48,65,3072
Mvaa 60,24,128
s_waitcnt vmcnt(14) lgkmcnt(8)
s_add_u32 s65, s5, 16
s_cmp_ge_u32 s65, 20
s_cselect_b32 s68, 20, 0
Mvav 32,0,188
s_sub_u32 s65, s65, s68
s_lshl_b32 s65, s65, 18
s_add_u32 m0, s63, 98304
Mvav 32,4,220
Ta 24,13,0
Ta 26,13,1024
Mvav 64,4,228
buffer_store_dwordx4 v[200:203], v1, s[8:11], s36 offen offset:1024 sc1
Ta 28,13,8192
Mvav 64,0,196
Ta 30,13,9216
Mvav 96,0,204
Mvav 96,4,236
Mvav 128,4,244
D 16,48,65,0
Mvav 128,0,212
W 8
Ta 0,14,0
Ta 2,14,1024
Mvav 132,8,212
v_add_u32 v2, 65536, v10
Mvav 132,12,244
Mvav 100,12,236
buffer_load_dwordx4 v[184:187], v26, s[8:11], s93 offen offset:0 sc1
Ta 4,14,8192
Mvav 100,8,204
Ta 6,14,9216
v_add_u32 v3, 65536, v11
Mvav 68,8,196
Mvav 68,12,228
Mvav 36,12,220
buffer_load_dwordx4 v[192:195], v27, s[8:11], s28 offen offset:0 sc1
Mvav 36,8,188
W 8
Ta 8,15,0
Ta 10,15,1024
Mvav 40,16,188
v_add_u32 v4, 65536, v12
Mvav 40,20,220
Mvav 72,20,228
buffer_store_dwordx4 v[208:211], v1, s[8:11], s36 offen offset:2048 sc1
Ta 12,15,8192
Mvav 72,16,196
Ta 14,15,9216
v_add_u32 v5, 65536, v13
Mvav 104,16,204
Mvav 104,20,236
Mvav 136,20,244
D 17,48,65,1024
Mvav 136,16,212
W 8
Ta 16,24,0
Ta 18,24,1024
Mvav 140,24,212
v_add_u32 v6, 65536, v14
Mvav 140,28,244
Mvav 108,28,236
buffer_load_dwordx4 a[128:131], v27, s[8:11], s93 offen offset:0 sc1
Ta 20,24,8192
Mvav 108,24,204
s_add_u32 s28, s28, 512
s_cmp_eq_u32 s28, s4
s_cselect_b32 s93, 10240, 0
s_sub_u32 s28, s28, s93
Mvav 76,24,196
s_add_u32 s93, s28, 20480
Ta 22,24,9216
v_add_u32 v7, 65536, v15
Mvav 76,28,228
Mvav 44,28,220
buffer_load_dwordx4 a[132:135], v26, s[8:11], s28 offen offset:0 sc1
Mvav 44,24,188
W 8
Ta 24,25,0
Ta 26,25,1024
Mvav 48,0,188
v_add_u32 v8, 65536, v24
Mvav 48,4,220
Mvav 80,4,228
buffer_store_dwordx4 v[216:219], v1, s[8:11], s36 offen offset:3072 sc1
Ta 28,25,8192
Mvav 80,0,196
s_add_u32 s36, s29, 458752
Ta 30,25,9216
v_add_u32 v9, 65536, v25
Mvav 112,0,204
Mvav 112,4,236
Mvav 144,4,244
buffer_load_dwordx4 a[136:139], v26, s[8:11], s93 offen offset:0 sc1
Mvav 144,0,212
W 8
Ta 0,10,16384
Ta 2,10,17408
Mvav 148,8,212
Mvav 148,12,244
Mvav 116,12,236
buffer_load_dwordx4 a[140:143], v27, s[8:11], s28 offen offset:0 sc1
Ta 4,10,24576
Mvav 116,8,204
Ta 6,10,25600
Mvav 84,8,196
Mvav 84,12,228
Mvav 52,12,220
buffer_load_dwordx4 a[144:147], v27, s[8:11], s93 offen offset:0 sc1
Mvav 52,8,188
W 8
s_add_u32 s28, s28, 512
s_cmp_eq_u32 s28, s4
s_cselect_b32 s93, 10240, 0
Mvav 56,16,188
s_sub_u32 s28, s28, s93
s_add_u32 s93, s28, 20480
Ta 8,11,16384
Mvav 56,20,220
buffer_store_dwordx4 v[224:227], v1, s[8:11], s36 offen offset:0 sc1
Ta 10,11,17408
Mvav 88,20,228
Ta 12,11,24576
Ta 14,11,25600
Mvav 88,16,196
Mvav 120,16,204
Mvav 120,20,236
buffer_load_dwordx4 a[148:151], v26, s[8:11], s28 offen offset:0 sc1
Mvav 152,20,244
Mvav 152,16,212
W 8
Ta 16,12,16384
Ta 18,12,17408
Mvav 156,24,212
Mvav 156,28,244
buffer_load_dwordx4 a[152:155], v26, s[8:11], s93 offen offset:0 sc1
Mvav 124,28,236
Ta 20,12,24576
Ta 22,12,25600
Mvav 124,24,204
Mvav 92,24,196
Mvav 92,28,228
buffer_load_dwordx4 a[156:159], v27, s[8:11], s28 offen offset:0 sc1
Mvav 60,28,220
Mvav 60,24,188
s_waitcnt vmcnt(18) lgkmcnt(8)
Ta 24,13,16384
Ta 26,13,17408
Mvav 32,0,252
Mvaa 32,4,44
buffer_store_dwordx4 v[232:235], v1, s[8:11], s36 offen offset:1024 sc1
Mvaa 64,4,48
Ta 28,13,24576
Ta 30,13,25600
Mvaa 64,0,32
)"
R"(Mvaa 96,0,36
Mvaa 96,4,52
D 18,48,65,2048
Mvaa 128,4,56
Mvaa 128,0,40
W 8
Ta 0,14,16384
Ta 2,14,17408
Mvaa 132,8,40
Mvaa 132,12,56
buffer_load_dwordx4 v[200:203], v27, s[8:11], s93 offen offset:0 sc1
Mvaa 100,12,52
s_add_u32 s28, s28, 512
s_cmp_eq_u32 s28, s4
s_cselect_b32 s93, 10240, 0
s_sub_u32 s28, s28, s93
Mvaa 100,8,36
s_add_u32 s93, s28, 20480
Ta 4,14,24576
Ta 6,14,25600
Mvaa 68,8,32
Mvaa 68,12,48
buffer_load_dwordx4 v[208:211], v26, s[8:11], s28 offen offset:0 sc1
Mvaa 36,12,44
Mvav 36,8,252
W 8
Ta 8,15,16384
Ta 10,15,17408
Mvav 40,16,252
Mvaa 40,20,44
buffer_store_dwordx4 v[240:243], v1, s[8:11], s36 offen offset:2048 sc1
Mvaa 72,20,48
Ta 12,15,24576
Ta 14,15,25600
Mvaa 72,16,32
Mvaa 104,16,36
Mvaa 104,20,52
buffer_load_dwordx4 v[216:219], v26, s[8:11], s93 offen offset:0 sc1
Mvaa 136,20,56
Mvaa 136,16,40
W 8
Ta 16,24,16384
Ta 18,24,17408
Mvaa 140,24,40
Mvaa 140,28,56
buffer_load_dwordx4 v[224:227], v27, s[8:11], s28 offen offset:0 sc1
Mvaa 108,28,52
Ta 20,24,24576
Ta 22,24,25600
Mvaa 108,24,36
Mvaa 76,24,32
Mvaa 76,28,48
D 19,48,65,3072
Mvaa 44,28,44
s_add_u32 m0, s63, 102400
Mvav 44,24,252
W 8
Ta 24,25,16384
Ta 26,25,17408
Mvav 48,0,252
Mvaa 48,4,44
buffer_store_dwordx4 v[248:251], v1, s[8:11], s36 offen offset:3072 sc1
G 160,160
Mvaa 80,4,48
G 161,161
G 162,162
Mvaa 80,0,32
G 163,163
G 164,164
Mvaa 112,0,36
G 165,165
G 166,166
Mvaa 112,4,52
buffer_load_dwordx4 v[188:191], v27, s[8:11], s93 offen offset:0 sc1
Ta 28,25,24576
Mvaa 144,4,56
s_add_u32 s28, s28, 512
s_cmp_eq_u32 s28, s4
s_cselect_b32 s93, 10240, 0
s_sub_u32 s28, s28, s93
Mvaa 144,0,40
W 7
s_add_u32 s93, s28, 20480
Ta 30,25,25600
Mvaa 148,8,40
G 167,167
K 160,160,161
Mvaa 148,12,56
buffer_load_dwordx4 v[196:199], v26, s[8:11], s28 offen offset:0 sc1
K 161,162,163
Mvaa 116,12,52
K 162,164,165
K 163,166,167
Mvaa 116,8,36
s_add_u32 s36, s29, 462848
Ta 0,2,0
Ta 2,2,1024
Mvaa 84,8,32
Ta 4,2,8192
Ta 6,2,9216
Mvaa 84,12,48
buffer_load_dwordx4 v[204:207], v26, s[8:11], s93 offen offset:0 sc1
Mvaa 52,12,44
Mvav 52,8,252
W 8
Ta 8,3,0
Ta 10,3,1024
Mvav 56,16,252
buffer_store_dwordx4 v[160:163], v1, s[8:11], s36 offen offset:0 sc1
G 168,168
Mvaa 56,20,44
G 169,169
G 170,170
Mvaa 88,20,48
G 171,171
G 172,172
Mvaa 88,16,32
G 173,173
G 174,174
Mvaa 120,16,36
D 20,48,65,0
Ta 12,3,8192
Mvaa 120,20,52
Ta 14,3,9216
G 175,175
Mvaa 152,20,56
K 168,168,169
K 169,170,171
Mvaa 152,16,40
W 8
K 170,172,173
K 171,174,175
Mvaa 156,24,40
buffer_load_dwordx4 v[212:215], v27, s[8:11], s28 offen offset:0 sc1
Ta 16,4,0
Mvaa 156,28,56
Ta 18,4,1024
Mvaa 124,28,52
Ta 20,4,8192
Ta 22,4,9216
Mvaa 124,24,36
Mvaa 92,24,32
buffer_load_dwordx4 v[220:223], v27, s[8:11], s93 offen offset:0 sc1
Mvaa 92,28,48
s_add_u32 s28, s28, 512
s_cmp_eq_u32 s28, s4
s_cselect_b32 s93, 10240, 0
s_sub_u32 s28, s28, s93
Mvaa 60,28,44
s_add_u32 s93, s28, 20480
Mvav 60,24,252
s_waitcnt vmcnt(20) lgkmcnt(8)
Ta 24,5,0
Ta 26,5,1024
Mvaa 32,0,60
buffer_store_dwordx4 v[168:171], v1, s[8:11], s36 offen offset:1024 sc1
G 176,176
Mvaa 32,4,132
G 177,177
G 178,178
Mvaa 64,4,136
G 179,179
G 180,180
Mvav 64,0,184
G 181,181
G 182,182
Mvav 96,0,192
buffer_load_dwordx4 v[228:231], v26, s[8:11], s28 offen offset:0 sc1
Ta 28,5,8192
Mvaa 96,4,140
Ta 30,5,9216
G 183,183
Mvaa 128,4,144
K 176,176,177
K 177,178,179
Mvaa 128,0,128
W 8
K 178,180,181
K 179,182,183
Mvaa 132,8,128
buffer_load_dwordx4 v[236:239], v26, s[8:11], s93 offen offset:0 sc1
Ta 0,6,0
Mvaa 132,12,144
Ta 2,6,1024
Mvaa 100,12,140
Ta 4,6,8192
Ta 6,6,9216
Mvav 100,8,192
Mvav 68,8,184
D 21,48,65,1024
Mvaa 68,12,136
Mvaa 36,12,132
Mvaa 36,8,60
W 8
Ta 8,7,0
Ta 10,7,1024
Mvaa 40,16,60
buffer_store_dwordx4 v[176:179], v1, s[8:11], s36 offen offset:2048 sc1
G 160,184
Mvaa 40,20,132
G 161,185
G 162,186
Mvaa 72,20,136
G 163,187
G 164,188
Mvav 72,16,184
G 165,189
G 166,190
Mvav 104,16,192
buffer_load_dwordx4 v[232:235], v27, s[8:11], s28 offen offset:0 sc1
Ta 12,7,8192
Mvaa 104,20,140
Ta 14,7,9216
G 167,191
Mvaa 136,20,144
K 160,160,161
K 161,162,163
Mvaa 136,16,128
W 8
K 162,164,165
K 163,166,167
Mvaa 140,24,128
buffer_load_dwordx4 a[32:35], v27, s[8:11], s93 offen offset:0 sc1
Ta 16,8,0
Mvaa 140,28,144
s_add_u32 s28, s28, 512
s_cmp_eq_u32 s28, s4
s_cselect_b32 s93, 10240, 0
s_sub_u32 s28, s28, s93
Mvaa 108,28,140
s_add_u32 s93, s28, 20480
Ta 18,8,1024
Ta 20,8,8192
Mvav 108,24,192
Ta 22,8,9216
Mvav 76,24,184
buffer_load_dwordx4 a[36:39], v26, s[8:11], s28 offen offset:0 sc1
Mvaa 76,28,136
Mvaa 44,28,132
Mvaa 44,24,60
W 8
Ta 24,9,0
Ta 26,9,1024
Mvaa 48,0,60
buffer_store_dwordx4 v[160:163], v1, s[8:11], s36 offen offset:3072 sc1
G 168,192
Mvaa 48,4,132
G 169,193
G 170,194
Mvaa 80,4,136
G 171,195
G 172,196
Mvav 80,0,184
G 173,197
G 174,198
Mvav 112,0,192
D 22,48,65,2048
G 175,199
Mvaa 112,4,140
Ta 28,9,8192
Ta 30,9,9216
Mvaa 144,4,144
K 168,168,169
K 169,170,171
Mvaa 144,0,128
W 8
K 170,172,173
K 171,174,175
Mvaa 148,8,128
buffer_load_dwordx4 a[40:43], v26, s[8:11], s93 offen offset:0 sc1
Ta 0,2,16384
Mvaa 148,12,144
Ta 2,2,17408
v_accvgpr_write_b32 a192, v168
Mvaa 116,12,140
v_accvgpr_write_b32 a193, v169
v_accvgpr_write_b32 a194, v170
Mvav 116,8,192
Ta 4,2,24576
Ta 6,2,25600
Mvav 84,8,184
buffer_load_dwordx4 a[44:47], v27, s[8:11], s28 offen offset:0 sc1
v_accvgpr_write_b32 a195, v171
Mvaa 84,12,136
G 176,64
Mvaa 52,12,132
G 177,65
Mvaa 52,8,60
W 8
Ta 8,3,16384
G 178,66
Mvaa 56,16,60
D 23,48,65,3072
Ta 10,3,17408
Mvaa 56,20,132
G 179,67
G 180,68
Mvaa 88,20,136
Ta 12,3,24576
Ta 14,3,25600
Mvav 88,16,184
G 181,69
Mvav 120,16,192
buffer_load_dwordx4 a[48:51], v27, s[8:11], s93 offen offset:0 sc1
G 182,70
Mvaa 120,20,140
s_add_u32 s28, s28, 512
s_cmp_eq_u32 s28, s4
s_cselect_b32 s93, 10240, 0
s_sub_u32 s28, s28, s93
Mvaa 152,20,144
s_add_u32 s93, s28, 20480
G 183,71
v_lshrrev_b32 v20, 4, v1
Mvaa 152,16,128
W 8
K 176,176,177
v_add_u32 v20, 0, v20
Mvaa 156,24,128
buffer_load_dwordx4 a[52:55], v26, s[8:11], s28 offen offset:0 sc1
Ta 16,4,16384
Mvaa 156,28,144
Ta 18,4,17408
K 177,178,179
Mvaa 124,28,140
K 178,180,181
v_mul_u32_u24 v22, 205, v20
Mvav 124,24,192
Ta 20,4,24576
K 179,182,183
Mvav 92,24,184
buffer_load_dwordx4 a[56:59], v26, s[8:11], s93 offen offset:0 sc1
Ta 22,4,25600
Mvaa 92,28,136
v_lshrrev_b32 v22, 12, v22
v_accvgpr_write_b32 a196, v176
Mvaa 60,28,132
v_mul_u32_u24 v23, 20, v22
v_accvgpr_write_b32 a197, v177
Mvaa 60,24,60
s_waitcnt vmcnt(22) lgkmcnt(8)
v_accvgpr_write_b32 a198, v178
v_sub_u32 v20, v20, v23
Mvaa 32,0,148
buffer_load_dwordx4 a[184:187], v27, s[8:11], s28 offen offset:0 sc1
v_accvgpr_write_b32 a199, v179
Mvav 32,4,208
Ta 24,5,16384
Ta 26,5,17408
Mvav 64,4,216
v_lshrrev_b32 v23, 1, v22
G 168,200
Mvaa 64,0,152
G 169,201
v_and_b32 v23, 2, v23
Mvaa 96,0,156
buffer_load_dwordx4 a[188:191], v27, s[8:11], s93 offen offset:0 sc1
Ta 28,5,24576
Mvav 96,4,224
s_add_u32 s28, s28, 512
s_cmp_eq_u32 s28, s4
s_cselect_b32 s93, 10240, 0
s_sub_u32 s28, s28, s93
Mvav 128,4,188
s_add_u32 s93, s28, 20480
Ta 30,5,25600
G 170,202
Mvav 128,0,200
W 8
v_xor_b32 v20, v20, v23
G 171,203
Mvav 132,8,200
buffer_load_dwordx4 a[60:63], v26, s[8:11], s28 offen offset:0 sc1
v_mul_u32_u24 v22, 80, v22
Mvav 132,12,188
G 172,204
G 173,205
Mvav 100,12,224
Ta 0,6,16384
v_add_lshl_u32 v16, v20, v22, 4
Mvaa 100,8,156
Ta 2,6,17408
G 174,206
Mvaa 68,8,152
buffer_load_dwordx4 v[184:187], v26, s[8:11], s93 offen offset:0 sc1
v_lshrrev_b32 v20, 4, v1
Mvav 68,12,216
Ta 4,6,24576
G 175,207
Mvav 36,12,208
Ta 6,6,25600
K 168,168,169
Mvaa 36,8,148
W 8
v_add_u32 v20, 64, v20
K 169,170,171
Mvaa 40,16,148
v_mul_u32_u24 v22, 205, v20
K 170,172,173
Mvav 40,20,208
)"
R"(v_lshrrev_b32 v22, 12, v22
K 171,174,175
Mvav 72,20,216
Ta 8,7,16384
Ta 10,7,17408
Mvaa 72,16,152
v_accvgpr_write_b32 a200, v168
v_mul_u32_u24 v23, 20, v22
Mvaa 104,16,156
buffer_load_dwordx4 v[192:195], v27, s[8:11], s28 offen offset:0 sc1
v_accvgpr_write_b32 a201, v169
Mvav 104,20,224
v_sub_u32 v20, v20, v23
v_accvgpr_write_b32 a202, v170
Mvav 136,20,188
Ta 12,7,24576
Ta 14,7,25600
Mvav 136,16,200
W 8
v_accvgpr_write_b32 a203, v171
v_lshrrev_b32 v23, 1, v22
Mvav 140,24,200
G 176,72
v_and_b32 v23, 2, v23
Mvav 140,28,188
G 177,73
v_xor_b32 v20, v20, v23
Mvav 108,28,224
Ta 16,8,16384
G 178,74
Mvaa 108,24,156
Ta 18,8,17408
G 179,75
Mvaa 76,24,152
buffer_load_dwordx4 a[128:131], v27, s[8:11], s93 offen offset:0 sc1
v_mul_u32_u24 v22, 80, v22
Mvav 76,28,216
G 180,76
v_add_lshl_u32 v17, v20, v22, 4
Mvav 44,28,208
Ta 20,8,24576
Ta 22,8,25600
Mvaa 44,24,148
W 8
G 181,77
G 182,78
Mvaa 48,0,148
s_add_u32 s28, s28, 512
v_subrev_u32 v17, 1024, v17
G 183,79
Mvav 48,4,208
s_cmp_eq_u32 s28, s4
s_cselect_b32 s93, 10240, 0
v_lshrrev_b32 v20, 4, v1
K 176,176,177
Mvav 80,4,216
s_sub_u32 s28, s28, s93
Ta 24,9,16384
Ta 26,9,17408
Mvaa 80,0,152
s_add_u32 s93, s28, 20480
v_add_u32 v20, 128, v20
K 177,178,179
Mvaa 112,0,156
buffer_load_dwordx4 a[132:135], v26, s[8:11], s28 offen offset:0 sc1
K 178,180,181
Mvav 112,4,224
v_mul_u32_u24 v22, 205, v20
K 179,182,183
Mvav 144,4,188
Ta 28,9,24576
Ta 30,9,25600
Mvav 144,0,200
W 8
v_lshrrev_b32 v22, 12, v22
v_accvgpr_write_b32 a204, v176
Mvav 148,8,200
s_waitcnt vmcnt(14)
v_mov_b64 v[28:29], s[12:13]
s_mov_b64 exec, 1
buffer_store_dwordx2 v[28:29], off, s[8:11], s71 sc1
s_mov_b64 exec, -1
v_accvgpr_write_b32 a205, v177
v_mul_u32_u24 v23, 20, v22
Mvav 148,12,188
v_accvgpr_write_b32 a206, v178
v_sub_u32 v20, v20, v23
Mvav 116,12,224
v_accvgpr_write_b32 a207, v179
v_lshrrev_b32 v23, 1, v22
Mvaa 116,8,156
G 168,208
G 169,209
Mvaa 84,8,152
s_waitcnt vmcnt(11) lgkmcnt(0)
s_barrier
buffer_load_dwordx4 a[136:139], v26, s[8:11], s93 offen offset:0 sc1
Mvav 84,12,216
v_and_b32 v23, 2, v23
G 170,210
Mvav 52,12,208
v_xor_b32 v20, v20, v23
G 171,211
Mvaa 52,8,148
Ta 0,10,32768
Ta 2,10,33792
Mvaa 56,16,148
Ta 4,10,40960
Ta 6,10,41984
Mvav 56,20,208
G 172,212
v_mul_u32_u24 v22, 80, v22
Mvav 88,20,216
G 173,213
v_add_lshl_u32 v18, v20, v22, 4
Mvaa 88,16,152
Ta 8,11,32768
Ta 10,11,33792
Mvaa 120,16,156
buffer_load_dwordx4 a[140:143], v27, s[8:11], s28 offen offset:0 sc1
G 174,214
Mvav 120,20,224
v_subrev_u32 v18, 2048, v18
G 175,215
Mvav 152,20,188
Ta 12,11,40960
Ta 14,11,41984
Mvav 152,16,200
K 168,168,169
v_lshrrev_b32 v20, 4, v1
Mvav 156,24,200
K 169,170,171
v_add_u32 v20, 192, v20
Mvav 156,28,188
K 170,172,173
K 171,174,175
Mvav 124,28,224
Ta 16,12,32768
v_mul_u32_u24 v22, 205, v20
Mvaa 124,24,156
Ta 18,12,33792
v_accvgpr_write_b32 a208, v168
Mvaa 92,24,152
buffer_load_dwordx4 a[144:147], v27, s[8:11], s93 offen offset:0 sc1
v_lshrrev_b32 v22, 12, v22
Mvav 92,28,216
v_accvgpr_write_b32 a209, v169
v_mul_u32_u24 v23, 20, v22
Mvav 60,28,208
Ta 20,12,40960
v_accvgpr_write_b32 a210, v170
Mvaa 60,24,148
W 7
Ta 22,12,41984
v_accvgpr_write_b32 a211, v171
Mvav 32,0,196
s_add_u32 s28, s28, 512
v_sub_u32 v20, v20, v23
G 176,80
Mvav 32,4,228
s_cmp_eq_u32 s28, s4
s_cselect_b32 s93, 10240, 0
v_lshrrev_b32 v23, 1, v22
G 177,81
Mvav 64,4,236
s_sub_u32 s28, s28, s93
G 178,82
v_and_b32 v23, 2, v23
Mvav 64,0,204
s_add_u32 s93, s28, 20480
Ta 24,13,32768
Ta 26,13,33792
Mvav 96,0,212
buffer_load_dwordx4 v[200:203], v26, s[8:11], s28 offen offset:0 sc1
G 179,83
Mvav 96,4,232
v_xor_b32 v20, v20, v23
G 180,84
Mvaa 128,4,32
Ta 28,13,40960
Ta 30,13,41984
Mvav 128,0,220
W 8
v_mul_u32_u24 v22, 80, v22
G 181,85
Mvav 132,8,220
G 182,86
v_add_lshl_u32 v19, v20, v22, 4
Mvaa 132,12,32
G 183,87
v_subrev_u32 v19, 3072, v19
Mvav 100,12,232
Ta 0,14,32768
K 176,176,177
Mvav 100,8,212
Ta 2,14,33792
K 177,178,179
Mvav 68,8,204
buffer_load_dwordx4 v[208:211], v26, s[8:11], s93 offen offset:0 sc1
v_lshrrev_b32 v20, 4, v1
Mvav 68,12,236
K 178,180,181
v_add_u32 v20, 256, v20
Mvav 36,12,228
Ta 4,14,40960
K 179,182,183
Mvav 36,8,196
W 7
Ta 6,14,41984
v_mul_u32_u24 v22, 205, v20
Mvav 40,16,196
v_accvgpr_write_b32 a212, v176
v_accvgpr_write_b32 a213, v177
Mvav 40,20,228
v_lshrrev_b32 v22, 12, v22
v_accvgpr_write_b32 a214, v178
Mvav 72,20,236
v_mul_u32_u24 v23, 20, v22
v_accvgpr_write_b32 a215, v179
Mvav 72,16,204
Ta 8,15,32768
Ta 10,15,33792
Mvav 104,16,212
buffer_load_dwordx4 v[216:219], v27, s[8:11], s28 offen offset:0 sc1
G 168,216
Mvav 104,20,232
v_sub_u32 v20, v20, v23
G 169,217
Mvaa 136,20,32
Ta 12,15,40960
v_lshrrev_b32 v23, 1, v22
Mvav 136,16,220
W 7
Ta 14,15,41984
G 170,218
Mvav 140,24,220
v_and_b32 v23, 2, v23
G 171,219
Mvaa 140,28,32
G 172,220
v_xor_b32 v20, v20, v23
Mvav 108,28,232
Ta 16,24,32768
G 173,221
Mvav 108,24,212
Ta 18,24,33792
v_mul_u32_u24 v22, 80, v22
Mvav 76,24,204
buffer_load_dwordx4 v[224:227], v27, s[8:11], s93 offen offset:0 sc1
G 174,222
Mvav 76,28,236
G 175,223
v_add_lshl_u32 v20, v20, v22, 4
Mvav 44,28,228
Ta 20,24,40960
Ta 22,24,41984
Mvav 44,24,196
W 8
K 168,168,169
v_lshrrev_b32 v22, 6, v1
Mvav 48,0,196
s_add_u32 s28, s28, 512
K 169,170,171
v_mul_u32_u24 v22, 320, v22
Mvav 48,4,228
s_cmp_eq_u32 s28, s4
s_cselect_b32 s93, 10240, 0
K 170,172,173
K 171,174,175
Mvav 80,4,236
s_sub_u32 s28, s28, s93
Ta 24,25,32768
v_bfe_u32 v23, v1, 4, 2
Mvav 80,0,204
s_add_u32 s93, s28, 20480
Ta 26,25,33792
v_accvgpr_write_b32 a216, v168
Mvav 112,0,212
buffer_load_dwordx4 v[188:191], v26, s[8:11], s28 offen offset:0 sc1
v_lshl_add_u32 v22, v23, 3, v22
Mvav 112,4,232
v_accvgpr_write_b32 a217, v169
v_accvgpr_write_b32 a218, v170
Mvaa 144,4,32
Ta 28,25,40960
v_bfe_u32 v23, v1, 8, 1
Mvav 144,0,220
W 7
Ta 30,25,41984
v_accvgpr_write_b32 a219, v171
Mvav 148,8,220
v_lshlrev_b32 v23, 5, v23
G 176,88
Mvaa 148,12,32
v_add_u32 v21, v22, v23
G 177,89
Mvav 116,12,232
G 178,90
v_sub_u32 v22, v22, v23
Mvav 116,8,212
s_mul_i32 s94, s59, 655360
Ta 0,10,49152
Ta 2,10,50176
Mvav 84,8,204
s_mul_i32 s95, s58, 320
G 179,91
G 180,92
Mvav 84,12,236
s_add_u32 s94, s94, s95
Ta 4,10,57344
Ta 6,10,58368
Mvav 52,12,228
G 181,93
G 182,94
Mvav 52,8,196
W 8
G 183,95
K 176,176,177
Mvav 56,16,196
s_add_u32 s40, s26, s94
s_addc_u32 s41, s27, 0
Ta 8,11,49152
Mvav 56,20,228
Ta 10,11,50176
K 177,178,179
Mvav 88,20,236
s_and_b32 s41, s41, 0xffff
K 178,180,181
K 179,182,183
Mvav 88,16,204
Ta 12,11,57344
Ta 14,11,58368
Mvav 120,16,212
s_mov_b32 s42, 5242880
v_accvgpr_write_b32 a220, v176
Mvav 120,20,232
s_mov_b32 s43, 0x20000
v_accvgpr_write_b32 a221, v177
Mvaa 152,20,32
v_accvgpr_write_b32 a222, v178
Mvav 152,16,220
W 8
s_sub_u32 s42, s42, s94
v_accvgpr_write_b32 a223, v179
Mvav 156,24,220
Ta 16,12,49152
Ta 18,12,50176
Mvaa 156,28,32
s_mov_b64 s[44:45], s[8:9]
G 168,224
Mvav 124,28,232
Ta 20,12,57344
G 169,225
Mvav 124,24,212
s_mov_b32 s47, 0x20000
Ta 22,12,58368
G 170,226
Mvav 92,24,204
s_and_b32 s48, s14, 1
G 171,227
Mvav 92,28,236
G 172,228
Mvav 60,28,228
s_mul_i32 s48, s48, 5120
Mvav 60,24,196
s_waitcnt vmcnt(14) lgkmcnt(8)
Ta 24,13,49152
G 173,229
Mvaa 32,0,36
s_lshr_b32 s51, s14, 1
Ta 26,13,50176
buffer_load_dwordx4 v[196:199], v26, s[8:11], s93 offen offset:0 sc1
Mvaa 32,4,52
G 174,230
G 175,231
Mvaa 64,4,56
s_and_b32 s94, s14, 1
Ta 28,13,57344
Ta 30,13,58368
Mvaa 64,0,40
s_lshl_b32 s95, s57, 1
K 168,168,169
Mvaa 96,0,44
buffer_load_dwordx4 v[204:207], v27, s[8:11], s28 offen offset:0 sc1
K 169,170,171
Mvaa 96,4,184
s_add_u32 s94, s94, s95
K 170,172,173
Mvaa 128,4,188
K 171,174,175
Mvaa 128,0,48
W 8
s_lshl_b32 s54, s94, 3
Ta 0,14,49152
Mvaa 132,8,48
buffer_load_dwordx4 v[212:215], v27, s[8:11], s93 offen offset:0 sc1
Ta 2,14,50176
Mvaa 132,12,188
s_add_u32 s54, s54, s59
v_accvgpr_write_b32 a224, v168
)"
R"(v_accvgpr_write_b32 a225, v169
Mvaa 100,12,184
s_lshl_b32 s54, s54, 6
Ta 4,14,57344
v_accvgpr_write_b32 a226, v170
Mvaa 100,8,44
Ta 6,14,58368
v_accvgpr_write_b32 a227, v171
Mvaa 68,8,40
s_xor_b32 s55, s54, 15872
s_and_b32 s81, s14, 1
G 176,96
Mvaa 68,12,56
Mvaa 36,12,52
s_add_u32 s54, s54, 20480
G 177,97
Mvaa 36,8,36
W 8
s_mul_i32 s91, s81, 17164288
G 178,98
Mvaa 40,16,36
s_add_u32 s55, s55, 20480
Ta 8,15,49152
Ta 10,15,50176
Mvaa 40,20,52
s_and_b32 s95, s14, 2
G 179,99
G 180,100
Mvaa 72,20,56
s_mul_i32 s82, s56, 536576
Ta 12,15,57344
Ta 14,15,58368
Mvaa 72,16,40
s_lshl_b32 s94, s94, 2
G 181,101
Mvaa 104,16,44
s_add_u32 s91, s91, s82
G 182,102
Mvaa 104,20,184
s_add_u32 s94, s94, s95
G 183,103
Mvaa 136,20,188
s_add_u32 s91, s91, 4194304
K 176,176,177
Mvaa 136,16,48
W 8
s_lshl_b32 s95, s95, 4
Ta 16,24,49152
Mvaa 140,24,48
s_add_u32 s55, s55, s95
Ta 18,24,50176
K 177,178,179
Mvaa 140,28,188
s_lshr_b32 s82, s14, 1
K 178,180,181
Mvaa 108,28,184
s_lshl_b32 s49, s94, 17
Ta 20,24,57344
K 179,182,183
Mvaa 108,24,44
s_lshl_b32 s83, s82, 14
Ta 22,24,58368
v_accvgpr_write_b32 a228, v176
Mvaa 76,24,40
s_lshl_b32 s95, s59, 14
Mvaa 76,28,56
v_accvgpr_write_b32 a229, v177
Mvaa 44,28,52
s_add_u32 s49, s49, s95
s_add_u32 s91, s91, s83
v_accvgpr_write_b32 a230, v178
Mvaa 44,24,36
W 8
s_xor_b32 s50, s49, 16252928
v_accvgpr_write_b32 a231, v179
Mvaa 48,0,36
s_lshl_b32 s81, s81, 14
Ta 24,25,49152
Ta 26,25,50176
Mvaa 48,4,52
s_add_u32 s49, s49, 39845888
G 168,232
Mvaa 80,4,56
Ta 28,25,57344
G 169,233
Mvaa 80,0,40
s_add_u32 s50, s50, 39845888
Ta 30,25,58368
G 170,234
Mvaa 112,0,44
s_lshl_b32 s82, s82, 16
G 171,235
Mvaa 112,4,184
s_lshr_b32 s94, s15, 3
s_or_b32 s92, s81, s82
G 172,236
Mvaa 144,4,188
s_lshl_b32 s94, s94, 3
Mvaa 144,0,48
W 8
Ta 0,2,32768
G 173,237
Mvaa 148,8,48
s_xor_b32 s92, s92, 0x10000
Ta 2,2,33792
G 174,238
Mvaa 148,12,188
s_add_u32 s94, s94, s14
G 175,239
Mvaa 116,12,184
s_lshl_b32 s94, s94, 4
Ta 4,2,40960
Ta 6,2,41984
Mvaa 116,8,44
K 168,168,169
Mvaa 84,8,40
s_add_u32 s94, s94, s59
K 169,170,171
Mvaa 84,12,56
s_mul_i32 s52, s94, 5120
K 170,172,173
Mvaa 52,12,52
K 171,174,175
Mvaa 52,8,36
W 8
s_add_u32 s52, s52, 56623104
Ta 8,3,32768
Mvaa 56,16,36
Ta 10,3,33792
v_accvgpr_write_b32 a232, v168
Mvaa 56,20,52
s_lshl_b32 s53, s94, 3
v_accvgpr_write_b32 a233, v169
Mvaa 88,20,56
Ta 12,3,40960
v_accvgpr_write_b32 a234, v170
Mvaa 88,16,40
s_add_u32 s53, s53, 65536
Ta 14,3,41984
v_accvgpr_write_b32 a235, v171
Mvaa 120,16,44
G 176,104
v_bfe_u32 v244, v1, 4, 1
Mvaa 120,20,184
Mvaa 152,20,188
G 177,105
v_mul_u32_u24 v244, 0xaaaa, v244
Mvaa 152,16,48
W 8
G 178,106
v_add_u32 v244, 0x4444, v244
Mvaa 156,24,48
Ta 16,4,32768
Ta 18,4,33792
Mvaa 156,28,188
G 179,107
v_lshl_or_b32 v240, v244, 16, v244
Mvaa 124,28,184
G 180,108
v_mov_b32 v241, 0xff800000
Mvaa 124,24,44
Ta 20,4,40960
Ta 22,4,41984
Mvaa 92,24,40
G 181,109
v_add_u32 v242, 0x10000, v1
Mvaa 92,28,56
G 182,110
v_bfe_u32 v244, v1, 4, 4
Mvaa 60,28,52
G 183,111
v_lshrrev_b32 v245, 8, v1
Mvaa 60,24,36
s_waitcnt vmcnt(8) lgkmcnt(8)
K 176,176,177
v_lshlrev_b32 v245, 2, v245
Mvaa 32,0,60
Ta 24,5,32768
Ta 26,5,33792
Mvaa 32,4,132
K 177,178,179
v_sub_u32 v244, v244, v245
Mvaa 64,4,136
K 178,180,181
v_cmp_le_i32_e64 s[72:73], 0, v244
Mvav 64,0,184
Ta 28,5,40960
K 179,182,183
Mvav 96,0,192
Ta 30,5,41984
v_accvgpr_write_b32 a236, v176
Mvaa 96,4,140
v_cmp_le_i32_e64 s[74:75], 1, v244
v_accvgpr_write_b32 a237, v177
Mvaa 128,4,144
v_cmp_le_i32_e64 s[76:77], 2, v244
v_accvgpr_write_b32 a238, v178
Mvaa 128,0,128
W 8
v_cmp_le_i32_e64 s[78:79], 3, v244
v_accvgpr_write_b32 a239, v179
Mvaa 132,8,128
Ta 0,6,32768
Ta 2,6,33792
Mvaa 132,12,144
v_add_u32 v243, 4096, v1
G 168,240
Mvaa 100,12,140
v_add_u32 v244, 8192, v1
G 169,241
Mvav 100,8,192
Ta 4,6,40960
v_add_u32 v245, 12288, v1
Mvav 68,8,184
s_mov_b32 s18, 0x3f803f80
Ta 6,6,41984
G 170,242
Mvaa 68,12,136
s_mov_b32 s19, 0x3f003f00
G 171,243
G 172,244
Mvaa 36,12,132
Mvaa 36,8,60
W 8
v_accvgpr_write_b32 a176, s18
G 173,245
Mvaa 40,16,60
Ta 8,7,32768
Ta 10,7,33792
Mvaa 40,20,132
v_accvgpr_write_b32 a180, s19
G 174,246
Mvaa 72,20,136
G 175,247
v_mov_b32 v252, v241
Mvav 72,16,184
Ta 12,7,40960
Ta 14,7,41984
Mvav 104,16,192
K 168,168,169
v_accvgpr_write_b32 a177, s18
Mvaa 104,20,140
K 169,170,171
v_accvgpr_write_b32 a181, s19
Mvaa 136,20,144
K 170,172,173
v_mov_b32 v253, v241
Mvaa 136,16,128
W 8
K 171,174,175
v_accvgpr_write_b32 a178, s18
Mvaa 140,24,128
Ta 16,8,32768
Ta 18,8,33792
Mvaa 140,28,144
v_accvgpr_write_b32 a240, v168
v_accvgpr_write_b32 a182, s19
Mvaa 108,28,140
v_accvgpr_write_b32 a241, v169
v_mov_b32 v254, v241
Mvav 108,24,192
Ta 20,8,40960
v_accvgpr_write_b32 a242, v170
Mvav 76,24,184
Ta 22,8,41984
v_accvgpr_write_b32 a179, s18
Mvaa 76,28,136
v_accvgpr_write_b32 a243, v171
v_accvgpr_write_b32 a183, s19
Mvaa 44,28,132
G 176,112
v_mov_b32 v255, v241
Mvaa 44,24,60
W 8
G 177,113
G 178,114
Mvaa 48,0,60
Ta 24,9,32768
v_accvgpr_write_b32 a160, 0
Mvaa 48,4,132
Ta 26,9,33792
G 179,115
Mvaa 80,4,136
v_accvgpr_write_b32 a161, 0
G 180,116
Mvav 80,0,184
Ta 28,9,40960
v_accvgpr_write_b32 a162, 0
Mvav 112,0,192
Ta 30,9,41984
G 181,117
Mvaa 112,4,140
v_accvgpr_write_b32 a163, 0
G 182,118
Mvaa 144,4,144
v_accvgpr_write_b32 a164, 0
G 183,119
Mvaa 144,0,128
W 8
v_accvgpr_write_b32 a165, 0
K 176,176,177
Mvaa 148,8,128
Ta 0,2,49152
v_accvgpr_write_b32 a166, 0
Mvaa 148,12,144
Ta 2,2,50176
K 177,178,179
Mvaa 116,12,140
v_accvgpr_write_b32 a167, 0
K 178,180,181
Mvav 116,8,192
v_accvgpr_write_b32 a168, 0
K 179,182,183
Mvav 84,8,184
Ta 4,2,57344
Ta 6,2,58368
Mvaa 84,12,136
v_accvgpr_write_b32 a169, 0
v_accvgpr_write_b32 a244, v176
Mvaa 52,12,132
v_accvgpr_write_b32 a245, v177
v_accvgpr_write_b32 a170, 0
Mvaa 52,8,60
W 8
v_accvgpr_write_b32 a246, v178
v_accvgpr_write_b32 a171, 0
Mvaa 56,16,60
Ta 8,3,49152
v_accvgpr_write_b32 a247, v179
Mvaa 56,20,132
Ta 10,3,50176
v_accvgpr_write_b32 a172, 0
Mvaa 88,20,136
G 168,248
v_accvgpr_write_b32 a173, 0
Mvav 88,16,184
G 169,249
v_accvgpr_write_b32 a174, 0
Mvav 120,16,192
Ta 12,3,57344
Ta 14,3,58368
Mvaa 120,20,140
G 170,250
v_accvgpr_write_b32 a175, 0
Mvaa 152,20,144
s_mov_b32 s90, 0x5d800000
G 171,251
G 172,252
Mvaa 152,16,128
W 8
s_mov_b64 s[88:89], 0
G 173,253
Mvaa 156,24,128
s_mov_b32 s64, 0
Ta 16,4,49152
Ta 18,4,50176
Mvaa 156,28,144
s_and_b32 s18, s14, 1
G 174,254
G 175,255
Mvaa 124,28,140
s_lshl_b32 s62, s18, 14
Ta 20,4,57344
Ta 22,4,58368
Mvav 124,24,192
K 168,168,169
Mvav 92,24,184
s_lshl_b32 s20, s57, 15
K 169,170,171
Mvaa 92,28,136
s_sub_u32 s20, 507904, s20
K 170,172,173
Mvaa 60,28,132
K 171,174,175
Mvaa 60,24,60
s_waitcnt vmcnt(0) lgkmcnt(8)
s_add_u32 s20, s20, s62
Ta 24,5,49152
Mvav 32,0,200
s_lshl_b32 s65, s57, 1
Ta 26,5,50176
v_accvgpr_write_b32 a248, v168
Mvav 32,4,188
s_sub_u32 s65, s65, 1
v_accvgpr_write_b32 a249, v169
Mvav 64,4,196
Ta 28,5,57344
v_accvgpr_write_b32 a250, v170
Mvav 64,0,208
s_max_i32 s65, s65, 0
Ta 30,5,58368
v_accvgpr_write_b32 a251, v171
Mvav 96,0,216
s_sub_u32 s66, s60, 1
G 176,120
Mvav 96,4,204
s_cmp_lt_u32 s14, 2
s_cselect_b32 s20, s20, 0
s_cselect_b32 s66, s66, 15
s_cselect_b32 s21, 32768, 0
)"
R"(Mvav 128,4,212
G 177,121
Mvav 128,0,224
W 8
s_add_u32 s61, s91, s20
G 178,122
Mvav 132,8,224
s_add_u32 s62, s62, s21
Ta 0,6,49152
Ta 2,6,50176
Mvav 132,12,212
G 179,123
G 180,124
Mvav 100,12,204
s_sub_u32 s63, 16384, s21
Ta 4,6,57344
Ta 6,6,58368
Mvav 100,8,216
s_lshl_b32 s67, s60, 7
G 181,125
Mvav 68,8,208
s_add_u32 s67, s67, s56
G 182,126
Mvav 68,12,196
G 183,127
Mvav 36,12,188
s_lshl_b32 s67, s67, 12
K 176,176,177
Mvav 36,8,200
W 8
s_add_u32 s67, s67, 39845888
Ta 8,7,49152
Mvav 40,16,200
s_lshl_b32 s69, s60, 3
Ta 10,7,50176
K 177,178,179
Mvav 40,20,188
K 178,180,181
Mvav 72,20,196
s_add_u32 s69, s69, s59
Ta 12,7,57344
K 179,182,183
Mvav 72,16,208
s_lshl_b32 s69, s69, 6
Ta 14,7,58368
v_accvgpr_write_b32 a252, v176
Mvav 104,16,216
Mvav 104,20,204
s_lshl_b32 s21, s58, 3
v_accvgpr_write_b32 a253, v177
Mvav 136,20,212
s_add_u32 s69, s69, s21
v_accvgpr_write_b32 a254, v178
Mvav 136,16,224
W 8
s_add_u32 s69, s69, 20480
v_accvgpr_write_b32 a255, v179
Mvav 140,24,224
Ta 16,8,49152
Ta 18,8,50176
Mvav 140,28,212
s_mov_b32 s70, 0
Mvav 108,28,204
s_mov_b32 s37, 0
Ta 20,8,57344
Ta 22,8,58368
Mvav 108,24,216
s_sub_u32 s18, s57, 4
Mvav 76,24,208
Mvav 76,28,196
s_lshl_b32 s18, s18, 2
Mvav 44,28,188
s_add_u32 s18, s18, s58
Mvav 44,24,200
W 8
Ta 24,9,49152
Ta 26,9,50176
Mvav 48,0,200
s_mul_i32 s85, s59, 7
Mvav 48,4,188
s_add_u32 s85, s85, s18
Mvav 80,4,196
s_cmp_lt_u32 s18, 7
s_cselect_b32 s85, s85, 99
Ta 28,9,57344
Ta 30,9,58368
Mvav 80,0,208
Mvav 112,0,216
s_cmp_lt_u32 s85, 54
s_cselect_b32 s65, 2, s65
Mvav 112,4,204
s_mov_b32 s68, 0
Mvav 144,4,212
s_mov_b64 s[24:25], -1
Mvav 144,0,224
W 8
Mvav 148,8,224
s_mov_b32 s22, 0x3e0293ee
Mvav 148,12,212
s_mov_b32 s23, 0x3e0293ee
Mvav 116,12,204
Mvav 116,8,216
Mvav 84,8,208
Mvav 84,12,196
Mvav 52,12,188
Mvav 52,8,200
W 4
Mvav 56,16,200
Mvav 56,20,188
Mvav 88,20,196
Mvav 88,16,208
Mvav 120,16,216
W 0
s_barrier
Ra 128,242,0
Mvav 120,20,204
Mvav 152,20,212
Ra 132,242,1024
Mvav 152,16,224
Mvav 156,24,224
Ra 136,242,2048
Mvav 156,28,212
Mvav 124,28,204
Ra 140,242,3072
Mvav 124,24,216
Mvav 92,24,208
Mvav 92,28,196
Mvav 60,28,188
Mvav 60,24,200
.Lago%=:
s_cmp_eq_u32 s57, 0
s_cbranch_scc0 .LL_46%=
s_add_u32 m0, s62, 0
s_nop 0
Dc 1,8,61,0
Dc 1,8,61,1024
Dc 1,8,61,2048
Dc 1,8,61,3072
s_add_u32 m0, s62, 4096
s_nop 0
Dc 243,8,61,0
Dc 243,8,61,1024
Dc 243,8,61,2048
Dc 243,8,61,3072
s_add_u32 m0, s62, 8192
s_nop 0
Dc 244,8,61,0
Dc 244,8,61,1024
Dc 244,8,61,2048
Dc 244,8,61,3072
s_add_u32 m0, s62, 12288
s_nop 0
Dc 245,8,61,0
Dc 245,8,61,1024
Dc 245,8,61,2048
Dc 245,8,61,3072
s_add_u32 s61, s61, s63
.LL_46%=:
s_cmp_eq_u32 s60, 0
s_cbranch_scc1 .Lpd_30%=
v_pk_mul_f32 v[32:33], v[32:33], s[22:23]
v_pk_mul_f32 v[34:35], v[34:35], s[22:23]
v_pk_mul_f32 v[36:37], v[36:37], s[22:23]
v_pk_mul_f32 v[38:39], v[38:39], s[22:23]
v_pk_mul_f32 v[64:65], v[64:65], s[22:23]
v_pk_mul_f32 v[66:67], v[66:67], s[22:23]
v_pk_mul_f32 v[68:69], v[68:69], s[22:23]
v_pk_mul_f32 v[70:71], v[70:71], s[22:23]
v_pk_mul_f32 v[96:97], v[96:97], s[22:23]
v_pk_mul_f32 v[98:99], v[98:99], s[22:23]
v_pk_mul_f32 v[100:101], v[100:101], s[22:23]
v_pk_mul_f32 v[102:103], v[102:103], s[22:23]
v_pk_mul_f32 v[128:129], v[128:129], s[22:23]
v_pk_mul_f32 v[130:131], v[130:131], s[22:23]
v_pk_mul_f32 v[132:133], v[132:133], s[22:23]
v_pk_mul_f32 v[134:135], v[134:135], s[22:23]
v_pk_mul_f32 v[40:41], v[40:41], s[22:23]
v_pk_mul_f32 v[42:43], v[42:43], s[22:23]
v_pk_mul_f32 v[44:45], v[44:45], s[22:23]
v_pk_mul_f32 v[46:47], v[46:47], s[22:23]
v_pk_mul_f32 v[72:73], v[72:73], s[22:23]
v_pk_mul_f32 v[74:75], v[74:75], s[22:23]
v_pk_mul_f32 v[76:77], v[76:77], s[22:23]
v_pk_mul_f32 v[78:79], v[78:79], s[22:23]
v_pk_mul_f32 v[104:105], v[104:105], s[22:23]
v_pk_mul_f32 v[106:107], v[106:107], s[22:23]
v_pk_mul_f32 v[108:109], v[108:109], s[22:23]
v_pk_mul_f32 v[110:111], v[110:111], s[22:23]
v_pk_mul_f32 v[136:137], v[136:137], s[22:23]
v_pk_mul_f32 v[138:139], v[138:139], s[22:23]
v_pk_mul_f32 v[140:141], v[140:141], s[22:23]
v_pk_mul_f32 v[142:143], v[142:143], s[22:23]
v_pk_mul_f32 v[48:49], v[48:49], s[22:23]
v_pk_mul_f32 v[50:51], v[50:51], s[22:23]
v_pk_mul_f32 v[52:53], v[52:53], s[22:23]
v_pk_mul_f32 v[54:55], v[54:55], s[22:23]
v_pk_mul_f32 v[80:81], v[80:81], s[22:23]
v_pk_mul_f32 v[82:83], v[82:83], s[22:23]
v_pk_mul_f32 v[84:85], v[84:85], s[22:23]
v_pk_mul_f32 v[86:87], v[86:87], s[22:23]
v_pk_mul_f32 v[112:113], v[112:113], s[22:23]
v_pk_mul_f32 v[114:115], v[114:115], s[22:23]
v_pk_mul_f32 v[116:117], v[116:117], s[22:23]
v_pk_mul_f32 v[118:119], v[118:119], s[22:23]
v_pk_mul_f32 v[144:145], v[144:145], s[22:23]
v_pk_mul_f32 v[146:147], v[146:147], s[22:23]
v_pk_mul_f32 v[148:149], v[148:149], s[22:23]
v_pk_mul_f32 v[150:151], v[150:151], s[22:23]
v_pk_mul_f32 v[56:57], v[56:57], s[22:23]
v_pk_mul_f32 v[58:59], v[58:59], s[22:23]
v_pk_mul_f32 v[60:61], v[60:61], s[22:23]
v_pk_mul_f32 v[62:63], v[62:63], s[22:23]
v_pk_mul_f32 v[88:89], v[88:89], s[22:23]
v_pk_mul_f32 v[90:91], v[90:91], s[22:23]
v_pk_mul_f32 v[92:93], v[92:93], s[22:23]
v_pk_mul_f32 v[94:95], v[94:95], s[22:23]
v_pk_mul_f32 v[120:121], v[120:121], s[22:23]
v_pk_mul_f32 v[122:123], v[122:123], s[22:23]
v_pk_mul_f32 v[124:125], v[124:125], s[22:23]
v_pk_mul_f32 v[126:127], v[126:127], s[22:23]
v_pk_mul_f32 v[152:153], v[152:153], s[22:23]
v_pk_mul_f32 v[154:155], v[154:155], s[22:23]
v_pk_mul_f32 v[156:157], v[156:157], s[22:23]
v_pk_mul_f32 v[158:159], v[158:159], s[22:23]
K 32,32,33
K 33,34,35
K 34,36,37
K 35,38,39
K 40,40,41
K 41,42,43
K 42,44,45
K 43,46,47
W 0
Zvav 196,128,32
s_add_u32 s36, s29, 65536
Ra 144,242,4096
Ra 148,242,5120
Ra 152,242,6144
Ra 156,242,7168
Mvav 196,132,40
K 64,64,65
K 65,66,67
K 66,68,69
K 67,70,71
K 72,72,73
K 73,74,75
K 74,76,77
K 75,78,79
s_nop 1
Zvav 204,132,72
buffer_store_dwordx4 a[192:195], v1, s[8:11], s36 offen offset:0 sc1
K 96,96,97
K 97,98,99
K 98,100,101
Mvav 204,128,64
K 99,102,103
K 104,104,105
K 105,106,107
K 106,108,109
K 107,110,111
Zvav 212,128,96
buffer_store_dwordx4 a[200:203], v1, s[8:11], s36 offen offset:1024 sc1
K 128,128,129
K 129,130,131
K 130,132,133
Mvav 212,132,104
K 131,134,135
K 136,136,137
K 137,138,139
K 138,140,141
K 139,142,143
s_nop 1
Zvav 220,132,136
K 48,48,49
K 49,50,51
K 50,52,53
K 51,54,55
Mvav 220,128,128
K 56,56,57
K 57,58,59
K 58,60,61
K 59,62,63
Mvav 196,136,48
buffer_store_dwordx4 a[208:211], v1, s[8:11], s36 offen offset:2048 sc1
Ra 128,242,8192
Ra 132,242,9216
K 80,80,81
Mvav 196,140,56
K 81,82,83
K 82,84,85
K 83,86,87
K 88,88,89
K 89,90,91
K 90,92,93
K 91,94,95
s_nop 1
Mvav 204,140,88
buffer_store_dwordx4 a[216:219], v1, s[8:11], s36 offen offset:3072 sc1
s_add_u32 s36, s29, 69632
K 112,112,113
K 113,114,115
K 114,116,117
Mvav 204,136,80
K 115,118,119
K 120,120,121
K 121,122,123
K 122,124,125
K 123,126,127
Mvav 212,136,112
K 144,144,145
K 145,146,147
K 146,148,149
K 147,150,151
Mvav 212,140,120
K 152,152,153
K 153,154,155
K 154,156,157
K 155,158,159
s_nop 1
Mvav 220,140,152
buffer_store_dwordx4 a[224:227], v1, s[8:11], s36 offen offset:0 sc1
v_accvgpr_write_b32 a0, v32
v_accvgpr_write_b32 a1, v33
v_accvgpr_write_b32 a2, v34
Mvav 220,136,144
W 2
Ra 136,242,10240
v_accvgpr_write_b32 a3, v35
v_accvgpr_write_b32 a4, v40
Zvav 36,144,32
Ra 140,242,11264
buffer_store_dwordx4 a[232:235], v1, s[8:11], s36 offen offset:1024 sc1
v_accvgpr_write_b32 a5, v41
Mvav 36,148,40
v_accvgpr_write_b32 a6, v42
v_accvgpr_write_b32 a7, v43
v_max3_f32 v4, v196, v197, v198
v_max3_f32 v4, v4, v199, v252
Zvav 68,148,72
v_max3_f32 v5, v204, v205, v206
v_max3_f32 v5, v5, v207, v253
v_max3_f32 v6, v212, v213, v214
v_max3_f32 v6, v6, v215, v254
Mvav 68,144,64
v_max3_f32 v7, v220, v221, v222
v_max3_f32 v7, v7, v223, v255
v_mov_b32 v8, v4
v_mov_b32 v9, v5
Zvav 100,144,96
v_mov_b32 v10, v6
v_mov_b32 v11, v7
v_permlane32_swap_b32 v4, v8
v_accvgpr_write_b32 a8, v48
Mvav 100,148,104
v_permlane32_swap_b32 v5, v9
v_permlane32_swap_b32 v6, v10
v_permlane32_swap_b32 v7, v11
v_max_f32 v4, v4, v8
Zvav 132,148,136
v_max_f32 v5, v5, v9
v_max_f32 v6, v6, v10
v_max_f32 v7, v7, v11
v_mov_b32 v8, v4
Mvav 132,144,128
v_mov_b32 v9, v5
v_mov_b32 v10, v6
v_mov_b32 v11, v7
v_accvgpr_write_b32 a9, v49
Mvav 36,152,48
v_permlane16_swap_b32 v4, v8
v_permlane16_swap_b32 v5, v9
v_permlane16_swap_b32 v6, v10
v_permlane16_swap_b32 v7, v11
Mvav 36,156,56
v_max_f32 v4, v4, v8
v_max_f32 v5, v5, v9
v_max_f32 v6, v6, v10
v_max_f32 v7, v7, v11
Mvav 68,156,88
v_sub_f32 v4, 0, v4
v_sub_f32 v5, 0, v5
v_sub_f32 v6, 0, v6
v_sub_f32 v7, 0, v7
Mvav 68,152,80
v_mov_b32 v224, v4
v_mov_b32 v225, v4
v_mov_b32 v226, v4
v_mov_b32 v227, v4
Mvav 100,152,112
v_mov_b32 v228, v5
v_mov_b32 v229, v5
v_mov_b32 v230, v5
v_mov_b32 v231, v5
Mvav 100,156,120
v_mov_b32 v232, v6
v_mov_b32 v233, v6
v_mov_b32 v234, v6
v_mov_b32 v235, v6
Mvav 132,156,152
v_mov_b32 v236, v7
)"
R"(v_mov_b32 v237, v7
v_mov_b32 v238, v7
v_mov_b32 v239, v7
Mvav 132,152,144
W 0
buffer_store_dwordx4 a[240:243], v1, s[8:11], s36 offen offset:2048 sc1
v_accvgpr_write_b32 a10, v50
v_accvgpr_write_b32 a11, v51
v_accvgpr_write_b32 a12, v56
Cvav 160,128,32,224
s_and_b32 s80, s14, 1
v_accvgpr_write_b32 a13, v57
v_accvgpr_write_b32 a14, v58
v_accvgpr_write_b32 a15, v59
v_accvgpr_write_b32 a16, v64
Mvav 160,132,40
buffer_store_dwordx4 a[248:251], v1, s[8:11], s36 offen offset:3072 sc1
s_add_u32 s36, s29, 393216
Ra 144,242,12288
Ra 148,242,13312
v_accvgpr_write_b32 a17, v65
Cvav 164,132,72,228
s_lshl_b32 s80, s80, 14
v_accvgpr_write_b32 a18, v66
v_accvgpr_write_b32 a19, v67
v_accvgpr_write_b32 a20, v72
v_accvgpr_write_b32 a21, v73
Mvav 164,128,64
s_add_u32 s80, s80, 98304
buffer_store_dwordx4 a[196:199], v1, s[8:11], s36 offen offset:0 sc1
v_accvgpr_write_b32 a22, v74
v_accvgpr_write_b32 a23, v75
v_accvgpr_write_b32 a24, v80
Cvav 168,128,96,232
v_accvgpr_write_b32 a25, v81
v_accvgpr_write_b32 a26, v82
A 196,196,224
v_accvgpr_write_b32 a27, v83
Mvav 168,132,104
buffer_store_dwordx4 a[204:207], v1, s[8:11], s36 offen offset:1024 sc1
A 197,197,224
A 198,198,224
v_accvgpr_write_b32 a28, v88
Cvav 172,132,136,236
Ra 152,242,14336
Ra 156,242,15360
A 199,199,224
v_accvgpr_write_b32 a29, v89
Mvav 172,128,128
buffer_store_dwordx4 a[212:215], v1, s[8:11], s36 offen offset:2048 sc1
X 196
v_accvgpr_write_b32 a30, v90
Mvav 160,136,48
X 197
v_accvgpr_write_b32 a31, v91
v_accvgpr_write_b32 a32, v96
Mvav 160,140,56
X 198
K 192,196,197
v_accvgpr_write_b32 a33, v97
Mvav 164,140,88
X 199
A 204,204,228
v_accvgpr_write_b32 a34, v98
Mvav 164,136,80
buffer_store_dwordx4 a[220:223], v1, s[8:11], s36 offen offset:3072 sc1
s_add_u32 s36, s29, 397312
K 193,198,199
v_accvgpr_write_b32 a35, v99
A 205,205,228
Mvav 168,136,112
A 206,206,228
v_accvgpr_write_b32 a36, v104
v_add_u32 v8, s80, v1
A 207,207,228
Mvav 168,140,120
X 204
v_accvgpr_write_b32 a37, v105
v_accvgpr_write_b32 a38, v106
Mvav 172,140,152
X 205
X 206
Mvav 172,136,144
W 0
buffer_store_dwordx4 a[228:231], v1, s[8:11], s36 offen offset:0 sc1
v_accvgpr_write_b32 a39, v107
v_accvgpr_write_b32 a40, v112
K 200,204,205
Cvav 176,144,32,224
Ra 128,242,24576
X 207
v_accvgpr_write_b32 a41, v113
Mvav 176,148,40
Ra 132,242,25600
A 212,212,232
v_accvgpr_write_b32 a42, v114
K 201,206,207
Cvav 180,148,72,228
A 213,213,232
v_accvgpr_write_b32 a43, v115
A 214,214,232
A 215,215,232
Mvav 180,144,64
buffer_store_dwordx4 a[236:239], v1, s[8:11], s36 offen offset:1024 sc1
v_accvgpr_write_b32 a44, v120
X 212
Cvav 184,144,96,232
v_accvgpr_write_b32 a45, v121
X 213
v_accvgpr_write_b32 a46, v122
Mvav 184,148,104
X 214
v_accvgpr_write_b32 a47, v123
K 208,212,213
Cvav 188,148,136,236
buffer_store_dwordx4 a[244:247], v1, s[8:11], s36 offen offset:2048 sc1
v_accvgpr_write_b32 a48, v128
X 215
Mvav 188,144,128
v_accvgpr_write_b32 a49, v129
A 220,220,236
v_accvgpr_write_b32 a50, v130
K 209,214,215
Mvav 176,152,48
A 221,221,236
v_accvgpr_write_b32 a51, v131
A 222,222,236
A 223,223,236
Mvav 176,156,56
Ra 136,242,26624
v_accvgpr_write_b32 a52, v136
X 220
Mvav 180,156,88
Ra 140,242,27648
v_accvgpr_write_b32 a53, v137
X 221
Mvav 180,152,80
buffer_store_dwordx4 a[252:255], v1, s[8:11], s36 offen offset:3072 sc1
v_accvgpr_write_b32 a54, v138
X 222
Mvav 184,152,112
v_accvgpr_write_b32 a55, v139
v_accvgpr_write_b32 a56, v144
v_accvgpr_write_b32 a57, v145
v_accvgpr_write_b32 a58, v146
v_accvgpr_write_b32 a59, v147
v_accvgpr_write_b32 a60, v152
v_accvgpr_write_b32 a61, v153
v_accvgpr_write_b32 a62, v154
v_accvgpr_write_b32 a63, v155
Mvav 184,156,120
s_barrier
K 216,220,221
X 223
A 36,36,224
Mvav 188,156,152
K 217,222,223
A 37,37,224
A 38,38,224
A 39,39,224
X 36
X 37
X 38
K 194,36,37
X 39
A 68,68,228
K 195,38,39
A 69,69,228
A 70,70,228
A 71,71,228
X 68
X 69
X 70
K 202,68,69
X 71
A 100,100,232
K 203,70,71
A 101,101,232
A 102,102,232
A 103,103,232
X 100
X 101
X 102
K 210,100,101
X 103
A 132,132,236
K 211,102,103
A 133,133,236
A 134,134,236
A 135,135,236
X 132
X 133
X 134
K 218,132,133
X 135
s_nop 0
K 219,134,135
Mvav 188,152,144
W 0
Ra 144,242,28672
Ra 148,242,29696
Ra 152,242,30720
Zvav 32,128,192
Ra 156,242,31744
Zvav 64,128,200
Zvav 96,128,208
s_and_b32 s80, s14, 1
s_xor_b32 s62, s62, 0x10000
Zvav 128,128,216
s_lshl_b32 s80, s80, 14
s_add_u32 s80, s80, 131072
v_add_u32 v23, 61440, v21
Zvav 132,132,216
v_add_u32 v12, s80, v1
Ra 64,12,8192
Ra 68,12,9216
v_add_u32 v24, 61440, v22
Zvav 100,132,208
Ra 72,12,10240
Ra 76,12,11264
Ra 80,12,12288
v_add_u32 v25, 122880, v21
Zvav 68,132,200
Ra 84,12,13312
Ra 88,12,14336
Ra 92,12,15360
v_add_u32 v26, 122880, v22
Zvav 36,132,192
Ra 96,12,0
Ra 100,12,1024
Ra 104,12,2048
Zvav 40,136,192
Ra 108,12,3072
Ra 112,12,4096
X 172
Zvav 72,136,200
Ra 116,12,5120
Ra 120,12,6144
X 173
Zvav 104,136,208
Ra 124,12,7168
X 174
K 220,172,173
Zvav 136,136,216
X 175
Zvav 140,140,216
X 168
K 221,174,175
Zvav 108,140,208
X 169
X 170
Zvav 76,140,200
s_waitcnt vmcnt(0) lgkmcnt(0)
s_barrier
v_xor_b32 v242, 0x10000, v242
ds_read_b128 v[248:251], v8
Zvav 44,140,192
s_add_u32 s80, s71, 128
v_mov_b64 v[28:29], s[12:13]
s_mov_b64 exec, 1
buffer_store_dwordx2 v[28:29], off, s[8:11], s80 sc1
s_mov_b64 exec, -1
Ra 128,242,0
K 212,168,169
X 171
Zvav 48,144,192
X 164
K 213,170,171
Zvav 80,144,200
X 165
X 166
Zvav 112,144,208
Ra 132,242,1024
K 204,164,165
X 167
Zvav 144,144,216
X 160
Zvav 148,148,216
K 205,166,167
X 161
Zvav 116,148,208
Ra 136,242,2048
X 162
Zvav 84,148,200
W 3
K 196,160,161
X 163
Zvav 52,148,192
X 176
K 197,162,163
Zvav 56,152,192
Ra 140,242,3072
X 177
Zvav 88,152,200
X 178
K 198,176,177
Zvav 120,152,208
X 179
X 180
Zvav 152,152,216
K 199,178,179
Zvav 156,156,216
X 181
X 182
Zvav 124,156,208
K 206,180,181
X 183
Zvav 92,156,200
K 207,182,183
Zvav 60,156,192
.Ll0t_44%=:
s_cmp_eq_u32 s64, s65
s_cbranch_scc1 .Ll1t_33%=
.Ll0_31%=:
W 0
Cvaa 160,128,0,224
Ra 144,242,4096
Ra 148,242,5120
Mvaa 160,132,4
s_xor_b32 s62, s62, 0x10000
Ra 152,242,6144
Cvaa 164,132,20,228
X 184
Mvaa 164,128,16
X 185
Cvaa 168,128,32,232
X 186
Mvaa 168,132,36
Ra 156,242,7168
K 214,184,185
Cvaa 172,132,52,236
X 187
Mvaa 172,128,48
X 188
Mvaa 160,136,8
Ra 128,242,16384
Mvaa 160,140,12
K 215,186,187
Mvaa 164,140,28
X 189
Mvaa 164,136,24
X 190
Mvaa 168,136,40
Ra 132,242,17408
Mvaa 168,140,44
K 222,188,189
Mvaa 172,140,60
X 191
Mvaa 172,136,56
K 223,190,191
v_smfmac_f32_16x16x64_bf16 a[160:163], a[176:179], v[192:199], v240
Ra 136,242,18432
v_smfmac_f32_16x16x64_bf16 a[164:167], a[176:179], v[200:207], v240
X 160
v_smfmac_f32_16x16x64_bf16 a[168:171], a[176:179], v[208:215], v240
X 161
v_smfmac_f32_16x16x64_bf16 a[172:175], a[176:179], v[216:223], v240
W 3
X 162
Cvaa 176,144,0,224
Ra 140,242,19456
Mvaa 176,148,4
K 192,160,161
Cvaa 180,148,20,228
X 163
Mvaa 180,144,16
X 164
Cvaa 184,144,32,232
Mvaa 184,148,36
K 193,162,163
Cvaa 188,148,52,236
X 165
Mvaa 188,144,48
X 166
Mvaa 176,152,8
Ra 144,242,20480
Ra 148,242,21504
Mvaa 176,156,12
K 200,164,165
Mvaa 180,156,28
X 167
Mvaa 180,152,24
X 168
Mvaa 184,152,40
Mvaa 184,156,44
K 201,166,167
Mvaa 188,156,60
X 169
Mvaa 188,152,56
W 2
Ra 152,242,22528
Mvav 32,128,196
X 170
Mvav 64,128,204
Ra 156,242,23552
K 208,168,169
Mvav 96,128,212
X 171
Mvav 128,128,220
Mvav 132,132,220
)"
R"(X 172
Mvav 100,132,212
s_add_u32 s64, s64, 1
Ra 128,242,8192
K 209,170,171
Mvav 68,132,204
X 173
Mvav 36,132,196
Mvav 40,136,196
X 174
Mvav 72,136,204
Ra 132,242,9216
K 216,172,173
Mvav 104,136,212
X 175
Mvav 136,136,220
Mvav 140,140,220
X 176
Mvav 108,140,212
Ra 136,242,10240
K 217,174,175
Mvav 76,140,204
X 177
Mvav 44,140,196
W 3
Mvav 48,144,196
X 178
Mvav 80,144,204
Ra 140,242,11264
Mvav 112,144,212
K 194,176,177
Mvav 144,144,220
X 179
Mvav 148,148,220
X 180
Mvav 116,148,212
Ra 144,242,12288
Mvav 84,148,204
K 195,178,179
Mvav 52,148,196
X 181
Mvav 56,152,196
X 182
Mvav 88,152,204
Ra 148,242,13312
Mvav 120,152,212
K 202,180,181
Mvav 152,152,220
X 183
Mvav 156,156,220
X 184
Mvav 124,156,212
Ra 152,242,14336
Mvav 92,156,204
K 203,182,183
Mvav 60,156,196
W 3
X 185
Cvaa 160,128,0,224
X 186
Mvaa 160,132,4
Ra 156,242,15360
Cvaa 164,132,20,228
K 210,184,185
Mvaa 164,128,16
X 187
Cvaa 168,128,32,232
X 188
Mvaa 168,132,36
Cvaa 172,132,52,236
K 211,186,187
Mvaa 172,128,48
X 189
Mvaa 160,136,8
Ra 128,242,24576
Ra 132,242,25600
Mvaa 160,140,12
X 190
Mvaa 164,140,28
K 218,188,189
Mvaa 164,136,24
X 191
Mvaa 168,136,40
Mvaa 168,140,44
Mvaa 172,140,60
X 160
Mvaa 172,136,56
W 2
Ra 136,242,26624
K 219,190,191
Cvaa 176,144,0,224
X 161
Mvaa 176,148,4
Ra 140,242,27648
Cvaa 180,148,20,228
X 162
Mvaa 180,144,16
K 196,160,161
Cvaa 184,144,32,232
X 163
Mvaa 184,148,36
X 164
Cvaa 188,148,52,236
K 197,162,163
Mvaa 188,144,48
X 165
Mvaa 176,152,8
Ra 144,242,28672
Ra 148,242,29696
Mvaa 176,156,12
X 166
Mvaa 180,156,28
K 204,164,165
Mvaa 180,152,24
X 167
Mvaa 184,152,40
Mvaa 184,156,44
X 168
Mvaa 188,156,60
Mvaa 188,152,56
W 2
Ra 152,242,30720
K 205,166,167
Mvav 32,128,192
X 169
Mvav 64,128,200
Ra 156,242,31744
Mvav 96,128,208
X 170
Mvav 128,128,216
K 212,168,169
Mvav 132,132,216
X 171
Mvav 100,132,208
X 172
Mvav 68,132,200
Mvav 36,132,192
K 213,170,171
Mvav 40,136,192
X 173
Mvav 72,136,200
X 174
Mvav 104,136,208
Mvav 136,136,216
K 220,172,173
Mvav 140,140,216
X 175
Mvav 108,140,208
X 176
Mvav 76,140,200
W 0
s_barrier
v_xor_b32 v242, 0x10000, v242
Mvav 44,140,192
Ra 128,242,0
K 221,174,175
Mvav 48,144,192
X 177
Mvav 80,144,200
X 178
Mvav 112,144,208
Ra 132,242,1024
Mvav 144,144,216
K 198,176,177
Mvav 148,148,216
X 179
Mvav 116,148,208
X 180
Mvav 84,148,200
Ra 136,242,2048
Mvav 52,148,192
K 199,178,179
Mvav 56,152,192
X 181
Mvav 88,152,200
Ra 140,242,3072
Mvav 120,152,208
X 182
Mvav 152,152,216
K 206,180,181
Mvav 156,156,216
X 183
Mvav 124,156,208
Mvav 92,156,200
K 207,182,183
Mvav 60,156,192
s_cmp_lg_u32 s64, s65
s_cbranch_scc1 .Ll0_31%=
s_cmp_lt_u32 s85, 54
s_cbranch_scc1 .Lmoe_42%=
.Ll1t_33%=:
s_cmp_eq_u32 s64, s66
s_cbranch_scc1 .Lsp1_34%=
.Ll1_32%=:
W 0
Cvaa 160,128,0,224
Ra 144,242,4096
Ra 148,242,5120
Mvaa 160,132,4
s_add_u32 m0, s62, 0
Ra 152,242,6144
Cvaa 164,132,20,228
X 184
Mvaa 164,128,16
X 185
Cvaa 168,128,32,232
X 186
Mvaa 168,132,36
Ra 156,242,7168
K 214,184,185
Cvaa 172,132,52,236
Dc 1,8,61,0
Mvaa 172,128,48
X 187
Mvaa 160,136,8
X 188
Mvaa 160,140,12
Ra 128,242,16384
K 215,186,187
Mvaa 164,140,28
Dc 1,8,61,1024
Mvaa 164,136,24
X 189
Mvaa 168,136,40
X 190
Mvaa 168,140,44
Ra 132,242,17408
K 222,188,189
Mvaa 172,140,60
Dc 1,8,61,2048
Mvaa 172,136,56
X 191
v_smfmac_f32_16x16x64_bf16 a[160:163], a[176:179], v[192:199], v240
Ra 136,242,18432
K 223,190,191
v_smfmac_f32_16x16x64_bf16 a[164:167], a[176:179], v[200:207], v240
X 160
v_smfmac_f32_16x16x64_bf16 a[168:171], a[176:179], v[208:215], v240
Dc 1,8,61,3072
v_smfmac_f32_16x16x64_bf16 a[172:175], a[176:179], v[216:223], v240
W 3
X 161
Cvaa 176,144,0,224
s_add_u32 m0, s62, 4096
X 162
Mvaa 176,148,4
Ra 140,242,19456
K 192,160,161
Cvaa 180,148,20,228
X 163
Mvaa 180,144,16
Dc 243,8,61,0
Cvaa 184,144,32,232
X 164
Mvaa 184,148,36
K 193,162,163
Cvaa 188,148,52,236
X 165
Mvaa 188,144,48
Dc 243,8,61,1024
Ra 144,242,20480
Mvaa 176,152,8
X 166
Mvaa 176,156,12
Ra 148,242,21504
K 200,164,165
Mvaa 180,156,28
X 167
Mvaa 180,152,24
Dc 243,8,61,2048
Mvaa 184,152,40
X 168
Mvaa 184,156,44
K 201,166,167
Mvaa 188,156,60
X 169
Mvaa 188,152,56
W 2
Dc 243,8,61,3072
Ra 152,242,22528
Mvav 32,128,196
s_add_u32 m0, s62, 8192
X 170
Mvav 64,128,204
Ra 156,242,23552
K 208,168,169
Mvav 96,128,212
X 171
Mvav 128,128,220
Dc 244,8,61,0
Mvav 132,132,220
X 172
Mvav 100,132,212
Ra 128,242,8192
K 209,170,171
Mvav 68,132,204
X 173
Mvav 36,132,196
Dc 244,8,61,1024
Mvav 40,136,196
X 174
Mvav 72,136,204
Ra 132,242,9216
K 216,172,173
Mvav 104,136,212
X 175
Mvav 136,136,220
Dc 244,8,61,2048
Mvav 140,140,220
X 176
Mvav 108,140,212
Ra 136,242,10240
K 217,174,175
Mvav 76,140,204
X 177
Mvav 44,140,196
W 3
Dc 244,8,61,3072
Mvav 48,144,196
X 178
Mvav 80,144,204
s_add_u32 m0, s62, 12288
Ra 140,242,11264
Mvav 112,144,212
K 194,176,177
Mvav 144,144,220
X 179
Mvav 148,148,220
X 180
Mvav 116,148,212
Dc 245,8,61,0
Ra 144,242,12288
Mvav 84,148,204
K 195,178,179
Mvav 52,148,196
X 181
Mvav 56,152,196
X 182
Mvav 88,152,204
Dc 245,8,61,1024
Ra 148,242,13312
Mvav 120,152,212
K 202,180,181
Mvav 152,152,220
X 183
Mvav 156,156,220
X 184
Mvav 124,156,212
Dc 245,8,61,2048
Ra 152,242,14336
Mvav 92,156,204
K 203,182,183
Mvav 60,156,196
W 3
X 185
Cvaa 160,128,0,224
X 186
Mvaa 160,132,4
Dc 245,8,61,3072
Ra 156,242,15360
Cvaa 164,132,20,228
s_add_u32 s61, s61, s63
K 210,184,185
Mvaa 164,128,16
X 187
Cvaa 168,128,32,232
X 188
Mvaa 168,132,36
Cvaa 172,132,52,236
s_xor_b32 s62, s62, 0x10000
K 211,186,187
Mvaa 172,128,48
X 189
Mvaa 160,136,8
Ra 128,242,24576
Ra 132,242,25600
Mvaa 160,140,12
s_add_u32 s64, s64, 1
X 190
Mvaa 164,140,28
K 218,188,189
Mvaa 164,136,24
X 191
Mvaa 168,136,40
Mvaa 168,140,44
X 160
Mvaa 172,140,60
K 219,190,191
Mvaa 172,136,56
W 2
X 161
Cvaa 176,144,0,224
Ra 136,242,26624
Ra 140,242,27648
Mvaa 176,148,4
X 162
Cvaa 180,148,20,228
K 196,160,161
Mvaa 180,144,16
X 163
)"
R"(Cvaa 184,144,32,232
Mvaa 184,148,36
X 164
Cvaa 188,148,52,236
K 197,162,163
Mvaa 188,144,48
X 165
Mvaa 176,152,8
Ra 144,242,28672
Ra 148,242,29696
Mvaa 176,156,12
X 166
Mvaa 180,156,28
K 204,164,165
Mvaa 180,152,24
X 167
Mvaa 184,152,40
Mvaa 184,156,44
X 168
Mvaa 188,156,60
Mvaa 188,152,56
W 2
Ra 152,242,30720
K 205,166,167
Mvav 32,128,192
X 169
Mvav 64,128,200
Ra 156,242,31744
Mvav 96,128,208
X 170
Mvav 128,128,216
K 212,168,169
Mvav 132,132,216
X 171
Mvav 100,132,208
X 172
Mvav 68,132,200
Mvav 36,132,192
K 213,170,171
Mvav 40,136,192
X 173
Mvav 72,136,200
X 174
Mvav 104,136,208
Mvav 136,136,216
K 220,172,173
Mvav 140,140,216
X 175
Mvav 108,140,208
X 176
Mvav 76,140,200
s_waitcnt vmcnt(0) lgkmcnt(0)
s_barrier
v_xor_b32 v242, 0x10000, v242
Mvav 44,140,192
Ra 128,242,0
K 221,174,175
Mvav 48,144,192
X 177
Mvav 80,144,200
X 178
Mvav 112,144,208
Ra 132,242,1024
Mvav 144,144,216
K 198,176,177
Mvav 148,148,216
X 179
Mvav 116,148,208
X 180
Mvav 84,148,200
Ra 136,242,2048
Mvav 52,148,192
K 199,178,179
Mvav 56,152,192
X 181
Mvav 88,152,200
Ra 140,242,3072
Mvav 120,152,208
X 182
Mvav 152,152,216
K 206,180,181
Mvav 156,156,216
X 183
Mvav 124,156,208
Mvav 92,156,200
K 207,182,183
Mvav 60,156,192
s_cmp_lg_u32 s64, s66
s_cbranch_scc1 .Ll1_32%=
s_cmp_ge_u32 s14, 2
s_cbranch_scc1 .Lf1r_45%=
.Lsp1_34%=:
W 0
Cvaa 160,128,0,224
Ra 144,242,4096
Ra 148,242,5120
Mvaa 160,132,4
s_add_u32 m0, s62, 0
Ra 152,242,6144
Cvaa 164,132,20,228
X 184
Mvaa 164,128,16
X 185
Cvaa 168,128,32,232
X 186
Mvaa 168,132,36
Ra 156,242,7168
K 214,184,185
Cvaa 172,132,52,236
Dc 1,8,61,0
Mvaa 172,128,48
X 187
Mvaa 160,136,8
X 188
Mvaa 160,140,12
Ra 128,242,16384
K 215,186,187
Mvaa 164,140,28
Dc 1,8,61,1024
Ra 132,242,17408
Mvaa 164,136,24
X 189
Mvaa 168,136,40
X 190
Mvaa 168,140,44
Dc 1,8,61,2048
K 222,188,189
Mvaa 172,140,60
X 191
Mvaa 172,136,56
Ra 136,242,18432
K 223,190,191
v_smfmac_f32_16x16x64_bf16 a[160:163], a[176:179], v[192:199], v240
Dc 1,8,61,3072
Ra 140,242,19456
v_smfmac_f32_16x16x64_bf16 a[164:167], a[176:179], v[200:207], v240
s_add_u32 m0, s62, 4096
v_cndmask_b32_e64 v160, v241, v160, s[72:73]
v_cndmask_b32_e64 v161, v241, v161, s[74:75]
v_smfmac_f32_16x16x64_bf16 a[168:171], a[176:179], v[208:215], v240
v_cndmask_b32_e64 v162, v241, v162, s[76:77]
v_cndmask_b32_e64 v163, v241, v163, s[78:79]
v_smfmac_f32_16x16x64_bf16 a[172:175], a[176:179], v[216:223], v240
W 4
X 160
Cvaa 180,144,16,228
X 161
Mvaa 180,148,20
Dc 243,8,61,0
Cvaa 184,148,36,232
X 162
Mvaa 184,144,32
K 192,160,161
Cvaa 188,144,48,236
X 163
Mvaa 188,148,52
Dc 243,8,61,1024
Ra 144,242,20480
Mvaa 180,152,24
X 164
Mvaa 180,156,28
Ra 148,242,21504
K 193,162,163
Mvaa 184,156,44
Dc 243,8,61,2048
Mvaa 184,152,40
X 165
Mvaa 188,152,56
X 166
Mvaa 188,156,60
W 2
Dc 243,8,61,3072
K 200,164,165
Mvav 32,128,196
s_add_u32 m0, s62, 8192
X 167
Mvav 64,128,204
Ra 152,242,22528
Ra 156,242,23552
Mvav 96,128,212
X 168
Mvav 128,128,220
Dc 244,8,61,0
K 201,166,167
Mvav 132,132,220
X 169
Mvav 100,132,212
X 170
Mvav 68,132,204
Ra 128,242,8192
K 208,168,169
Mvav 36,132,196
X 171
Mvav 40,136,196
Dc 244,8,61,1024
Ra 132,242,9216
Mvav 72,136,204
X 172
Mvav 104,136,212
K 209,170,171
Mvav 136,136,220
X 173
Mvav 140,140,220
Dc 244,8,61,2048
Ra 136,242,10240
Mvav 108,140,212
X 174
Mvav 76,140,204
K 216,172,173
Mvav 44,140,196
W 3
Dc 244,8,61,3072
Ra 140,242,11264
Mvav 48,144,196
s_add_u32 m0, s62, 12288
X 175
Mvav 80,144,204
v_mov_b32 v194, 0
K 217,174,175
Mvav 112,144,212
v_mov_b32 v195, 0
v_cndmask_b32_e64 v180, v241, v180, s[72:73]
Mvav 144,144,220
v_cndmask_b32_e64 v181, v241, v181, s[74:75]
v_cndmask_b32_e64 v182, v241, v182, s[76:77]
Mvav 148,148,220
Dc 245,8,61,0
v_cndmask_b32_e64 v183, v241, v183, s[78:79]
Mvav 116,148,212
X 180
Mvav 84,148,204
Ra 144,242,12288
Mvav 52,148,196
X 181
Mvav 56,152,196
Dc 245,8,61,1024
Ra 148,242,13312
Mvav 88,152,204
X 182
Mvav 120,152,212
K 202,180,181
Mvav 152,152,220
X 183
Mvav 156,156,220
Dc 245,8,61,2048
Ra 152,242,14336
Mvav 124,156,212
X 184
Mvav 92,156,204
K 203,182,183
Mvav 60,156,196
W 3
Dc 245,8,61,3072
Ra 156,242,15360
Cvaa 168,128,32,232
s_add_u32 s61, s61, s63
X 185
Mvaa 168,132,36
s_xor_b32 s62, s62, 0x10000
X 186
Cvaa 172,132,52,236
K 210,184,185
Mvaa 172,128,48
s_add_u32 s64, s64, 1
X 187
Mvaa 168,136,40
X 188
Mvaa 168,140,44
Ra 128,242,24576
K 211,186,187
Mvaa 172,140,60
Ra 132,242,25600
Mvaa 172,136,56
Ra 136,242,26624
Ra 140,242,27648
X 189
X 190
K 218,188,189
X 191
v_mov_b32 v196, 0
K 219,190,191
W 4
Cvaa 188,144,48,236
Ra 144,242,28672
Mvaa 188,148,52
v_mov_b32 v197, 0
v_mov_b32 v204, 0
Mvaa 188,152,56
Ra 148,242,29696
v_mov_b32 v205, 0
Mvaa 188,156,60
W 2
Ra 152,242,30720
v_cndmask_b32_e64 v168, v241, v168, s[72:73]
Mvav 32,128,192
v_cndmask_b32_e64 v169, v241, v169, s[74:75]
v_cndmask_b32_e64 v170, v241, v170, s[76:77]
Mvav 64,128,200
Ra 156,242,31744
v_cndmask_b32_e64 v171, v241, v171, s[78:79]
Mvav 96,128,208
X 168
Mvav 128,128,216
X 169
Mvav 132,132,216
X 170
Mvav 100,132,208
v_mov_b32 v224, v250
Mvav 68,132,200
K 212,168,169
v_mov_b32 v225, v250
Mvav 36,132,192
X 171
Mvav 40,136,192
v_mov_b32 v226, v250
Mvav 72,136,200
X 172
Mvav 104,136,208
v_mov_b32 v227, v250
K 213,170,171
Mvav 136,136,216
X 173
Mvav 140,140,216
v_mov_b32 v228, v251
v_mov_b32 v229, v251
Mvav 108,140,208
X 174
Mvav 76,140,200
K 220,172,173
v_mov_b32 v230, v251
Mvav 44,140,192
W 0
X 175
Mvav 48,144,192
v_mov_b32 v231, v251
v_mov_b32 v198, 0
Mvav 80,144,200
K 221,174,175
v_mov_b32 v232, v248
Mvav 112,144,208
v_mov_b32 v199, 0
v_mov_b32 v206, 0
Mvav 144,144,216
v_mov_b32 v233, v248
v_mov_b32 v207, 0
Mvav 148,148,216
v_mov_b32 v214, 0
v_mov_b32 v234, v248
Mvav 116,148,208
v_mov_b32 v215, 0
v_cndmask_b32_e64 v188, v241, v188, s[72:73]
Mvav 84,148,200
v_mov_b32 v235, v248
v_cndmask_b32_e64 v189, v241, v189, s[74:75]
Mvav 52,148,192
v_cndmask_b32_e64 v190, v241, v190, s[76:77]
v_mov_b32 v236, v249
Mvav 56,152,192
v_cndmask_b32_e64 v191, v241, v191, s[78:79]
v_mov_b32 v237, v249
Mvav 88,152,200
X 188
Mvav 120,152,208
X 189
Mvav 152,152,216
v_mov_b32 v238, v249
v_mov_b32 v239, v249
Mvav 156,156,216
X 190
Mvav 124,156,208
s_waitcnt vmcnt(0)
s_barrier
v_xor_b32 v242, 0x10000, v242
Mvav 92,156,200
Ra 128,242,40960
K 222,188,189
Mvav 60,156,192
Ra 132,242,41984
Ra 136,242,43008
Ra 140,242,44032
X 191
s_nop 0
K 223,190,191
s_cmp_eq_u32 s64, 15
s_cbranch_scc1 .Lfs_40%=
.Lsp2_35%=:
v_smfmac_f32_16x16x64_bf16 a[160:163], a[176:179], v[192:199], v240
s_add_u32 m0, s62, 0
Ra 144,242,45056
Ra 148,242,46080
Ra 152,242,47104
Ra 156,242,48128
v_smfmac_f32_16x16x64_bf16 a[164:167], a[176:179], v[200:207], v240
v_smfmac_f32_16x16x64_bf16 a[168:171], a[176:179], v[208:215], v240
v_smfmac_f32_16x16x64_bf16 a[172:175], a[176:179], v[216:223], v240
W 4
Dc 1,8,61,0
Cvaa 168,128,96,232
v_mov_b32 v192, 0
G 12,160
)"
R"(Mvaa 168,132,100
Dc 1,8,61,1024
G 4,161
A 12,12,4
v_cmp_nlt_f32_e64 s[86:87], v12, s90
v_mov_b32 v193, 0
Cvaa 172,132,116,236
v_rcp_f32 v12, v12
s_or_b64 s[88:89], s[88:89], s[86:87]
v_accvgpr_write_b32 a160, 0
v_mov_b32 v200, 0
v_accvgpr_write_b32 a161, 0
v_accvgpr_write_b32 a162, 0
Mvaa 172,128,112
Dc 1,8,61,2048
v_accvgpr_write_b32 a163, 0
G 13,164
v_mov_b32 v201, 0
G 5,165
A 13,13,5
Mvaa 168,136,104
Ra 128,242,16384
Ra 132,242,17408
v_cmp_nlt_f32_e64 s[86:87], v13, s90
v_rcp_f32 v13, v13
s_or_b64 s[88:89], s[88:89], s[86:87]
v_accvgpr_write_b32 a164, 0
Mvaa 168,140,108
Dc 1,8,61,3072
v_accvgpr_write_b32 a165, 0
v_accvgpr_write_b32 a166, 0
v_accvgpr_write_b32 a167, 0
U 32,32,12
U 33,33,12
Mvaa 172,140,124
U 34,34,12
U 35,35,12
U 36,36,12
U 37,37,12
U 38,38,12
U 39,39,12
Mvaa 172,136,120
s_add_u32 m0, s62, 4096
Ra 136,242,18432
Ra 140,242,19456
K 32,32,33
K 33,34,35
K 34,36,37
W 4
Cvaa 188,144,112,236
K 35,38,39
s_add_u32 s93, s67, 0
buffer_store_dwordx4 v[32:35], v1, s[8:11], s93 offen offset:0 sc1
U 40,40,12
U 41,41,12
U 42,42,12
U 43,43,12
Mvaa 188,148,116
Ra 144,242,20480
v_cndmask_b32_e64 v168, v241, v168, s[72:73]
v_cndmask_b32_e64 v169, v241, v169, s[74:75]
v_cndmask_b32_e64 v170, v241, v170, s[76:77]
v_cndmask_b32_e64 v171, v241, v171, s[78:79]
U 44,44,12
Mvaa 188,152,120
X 168
X 169
U 45,45,12
U 46,46,12
Mvaa 188,156,124
W 1
Ra 148,242,21504
Dc 243,8,61,0
U 47,47,12
K 40,40,41
K 41,42,43
K 42,44,45
Mvav 96,128,212
Ra 152,242,22528
Ra 156,242,23552
K 43,46,47
X 170
K 208,168,169
Mvav 128,128,220
buffer_store_dwordx4 v[40:43], v1, s[8:11], s93 offen offset:1024 sc1
U 48,48,12
U 49,49,12
U 50,50,12
U 51,51,12
U 52,52,12
Mvav 132,132,220
U 53,53,12
U 54,54,12
U 55,55,12
K 48,48,49
K 49,50,51
K 50,52,53
Mvav 100,132,212
Dc 243,8,61,1024
Ra 128,242,32768
Ra 132,242,33792
X 171
K 51,54,55
Mvav 104,136,212
X 172
K 209,170,171
X 173
Mvav 136,136,220
buffer_store_dwordx4 v[48:51], v1, s[8:11], s93 offen offset:2048 sc1
U 56,56,12
U 57,57,12
U 58,58,12
U 59,59,12
U 60,60,12
Mvav 140,140,220
U 61,61,12
U 62,62,12
U 63,63,12
K 56,56,57
K 57,58,59
K 58,60,61
Mvav 108,140,212
W 2
Dc 243,8,61,2048
Ra 136,242,34816
Ra 140,242,35840
K 59,62,63
X 174
Mvav 112,144,212
K 216,172,173
X 175
Mvav 144,144,220
buffer_store_dwordx4 v[56:59], v1, s[8:11], s93 offen offset:3072 sc1
Ra 144,242,36864
G 14,168
G 6,169
A 14,14,6
v_cmp_nlt_f32_e64 s[86:87], v14, s90
Mvav 148,148,220
v_rcp_f32 v14, v14
s_or_b64 s[88:89], s[88:89], s[86:87]
v_accvgpr_write_b32 a168, 0
v_accvgpr_write_b32 a169, 0
v_accvgpr_write_b32 a170, 0
v_accvgpr_write_b32 a171, 0
Mvav 116,148,212
Dc 243,8,61,3072
s_add_u32 m0, s62, 8192
G 15,172
G 7,173
A 15,15,7
v_cmp_nlt_f32_e64 s[86:87], v15, s90
v_mov_b32 v194, 0
Mvav 120,152,212
v_rcp_f32 v15, v15
s_or_b64 s[88:89], s[88:89], s[86:87]
v_accvgpr_write_b32 a172, 0
v_accvgpr_write_b32 a173, 0
v_accvgpr_write_b32 a174, 0
v_accvgpr_write_b32 a175, 0
Mvav 152,152,220
Dc 244,8,61,0
U 64,64,13
U 65,65,13
U 66,66,13
U 67,67,13
U 68,68,13
Mvav 156,156,220
U 69,69,13
U 70,70,13
U 71,71,13
K 64,64,65
K 65,66,67
K 66,68,69
Mvav 124,156,212
W 1
Dc 244,8,61,1024
Ra 148,242,37888
K 67,70,71
s_add_u32 s93, s67, 131072
K 217,174,175
v_mov_b32 v195, 0
Cvaa 160,128,64,224
Ra 152,242,38912
Ra 156,242,39936
v_mov_b32 v202, 0
v_mov_b32 v203, 0
v_mov_b32 v210, 0
v_mov_b32 v211, 0
Mvaa 160,132,68
buffer_store_dwordx4 v[64:67], v1, s[8:11], s93 offen offset:0 sc1
U 72,72,13
U 73,73,13
U 74,74,13
U 75,75,13
U 76,76,13
Cvaa 164,132,84,228
U 77,77,13
U 78,78,13
U 79,79,13
K 72,72,73
K 73,74,75
K 74,76,77
Mvaa 164,128,80
Dc 244,8,61,2048
K 75,78,79
v_cndmask_b32_e64 v188, v241, v188, s[72:73]
v_cndmask_b32_e64 v189, v241, v189, s[74:75]
v_cndmask_b32_e64 v190, v241, v190, s[76:77]
v_cndmask_b32_e64 v191, v241, v191, s[78:79]
Cvaa 168,128,96,232
X 188
X 189
Mvaa 168,132,100
buffer_store_dwordx4 v[72:75], v1, s[8:11], s93 offen offset:1024 sc1
U 80,80,13
U 81,81,13
U 82,82,13
U 83,83,13
U 84,84,13
Cvaa 172,132,116,236
U 85,85,13
U 86,86,13
U 87,87,13
K 80,80,81
K 81,82,83
K 82,84,85
Mvaa 172,128,112
Dc 244,8,61,3072
s_add_u32 m0, s62, 12288
Ra 128,242,49152
Ra 132,242,50176
K 83,86,87
X 190
Mvaa 160,136,72
K 218,188,189
X 191
Mvaa 160,140,76
buffer_store_dwordx4 v[80:83], v1, s[8:11], s93 offen offset:2048 sc1
U 88,88,13
U 89,89,13
U 90,90,13
U 91,91,13
U 92,92,13
Mvaa 164,140,92
U 93,93,13
U 94,94,13
U 95,95,13
K 88,88,89
K 89,90,91
K 90,92,93
Mvaa 164,136,88
K 91,94,95
buffer_store_dwordx4 v[88:91], v1, s[8:11], s93 offen offset:3072 sc1
U 96,96,14
U 97,97,14
U 98,98,14
U 99,99,14
Mvaa 168,136,104
U 100,100,14
U 101,101,14
U 102,102,14
U 103,103,14
K 96,96,97
K 97,98,99
Mvaa 168,140,108
Dc 245,8,61,0
K 98,100,101
K 99,102,103
s_add_u32 s93, s67, 262144
v_cndmask_b32_e64 v160, v241, v160, s[72:73]
K 219,190,191
v_cndmask_b32_e64 v161, v241, v161, s[74:75]
Mvaa 172,140,124
v_cndmask_b32_e64 v162, v241, v162, s[76:77]
v_cndmask_b32_e64 v163, v241, v163, s[78:79]
X 160
Mvaa 172,136,120
W 2
Dc 245,8,61,1024
Ra 136,242,51200
Ra 140,242,52224
X 161
Cvaa 180,144,80,228
Mvaa 180,148,84
buffer_store_dwordx4 v[96:99], v1, s[8:11], s93 offen offset:0 sc1
U 104,104,14
U 105,105,14
U 106,106,14
U 107,107,14
U 108,108,14
Cvaa 184,148,100,232
U 109,109,14
U 110,110,14
U 111,111,14
K 104,104,105
K 105,106,107
K 106,108,109
Mvaa 184,144,96
Dc 245,8,61,2048
K 107,110,111
X 162
K 196,160,161
Cvaa 188,144,112,236
Ra 144,242,53248
X 163
Mvaa 188,148,116
buffer_store_dwordx4 v[104:107], v1, s[8:11], s93 offen offset:1024 sc1
U 112,112,14
U 113,113,14
U 114,114,14
U 115,115,14
U 116,116,14
Mvaa 180,152,88
U 117,117,14
U 118,118,14
U 119,119,14
K 112,112,113
K 113,114,115
K 114,116,117
Mvaa 180,156,92
Dc 245,8,61,3072
s_add_u32 s61, s61, s63
s_xor_b32 s62, s62, 0x10000
s_add_u32 s64, s64, 1
K 115,118,119
X 164
Mvaa 184,156,108
Ra 148,242,54272
K 197,162,163
X 165
Mvaa 184,152,104
buffer_store_dwordx4 v[112:115], v1, s[8:11], s93 offen offset:2048 sc1
U 120,120,14
U 121,121,14
U 122,122,14
U 123,123,14
U 124,124,14
Mvaa 188,152,120
U 125,125,14
U 126,126,14
U 127,127,14
K 120,120,121
K 121,122,123
K 122,124,125
Mvaa 188,156,124
W 2
K 123,126,127
buffer_store_dwordx4 v[120:123], v1, s[8:11], s93 offen offset:3072 sc1
U 128,128,15
U 129,129,15
U 130,130,15
U 131,131,15
Zvav 32,128,192
U 132,132,15
U 133,133,15
U 134,134,15
U 135,135,15
K 128,128,129
K 129,130,131
Zvav 64,128,200
K 130,132,133
K 131,134,135
s_add_u32 s93, s67, 393216
buffer_store_dwordx4 v[128:131], v1, s[8:11], s93 offen offset:0 sc1
U 136,136,15
U 137,137,15
U 138,138,15
Zvav 96,128,208
U 139,139,15
U 140,140,15
U 141,141,15
U 142,142,15
U 143,143,15
K 136,136,137
Zvav 128,128,216
K 137,138,139
K 138,140,141
K 139,142,143
buffer_store_dwordx4 v[136:139], v1, s[8:11], s93 offen offset:1024 sc1
U 144,144,15
U 145,145,15
Zvav 132,132,216
U 146,146,15
U 147,147,15
U 148,148,15
U 149,149,15
U 150,150,15
U 151,151,15
Zvav 100,132,208
K 144,144,145
K 145,146,147
K 146,148,149
K 147,150,151
buffer_store_dwordx4 v[144:147], v1, s[8:11], s93 offen offset:2048 sc1
U 152,152,15
Zvav 68,132,200
U 153,153,15
U 154,154,15
U 155,155,15
)"
R"(U 156,156,15
U 157,157,15
U 158,158,15
Zvav 36,132,192
U 159,159,15
K 152,152,153
K 153,154,155
K 154,156,157
K 155,158,159
buffer_store_dwordx4 v[152:155], v1, s[8:11], s93 offen offset:3072 sc1
Zvav 40,136,192
Ra 152,242,55296
X 166
K 204,164,165
X 167
Zvav 72,136,200
Ra 156,242,56320
X 168
K 205,166,167
X 169
Zvav 104,136,208
X 170
K 212,168,169
X 171
Zvav 136,136,216
X 172
K 213,170,171
X 173
Zvav 140,140,216
Zvav 108,140,208
X 174
Zvav 76,140,200
s_waitcnt vmcnt(6) lgkmcnt(0)
s_barrier
v_xor_b32 v242, 0x10000, v242
Zvav 44,140,192
Ra 128,242,40960
K 220,172,173
X 175
Zvav 48,144,192
Zvav 80,144,200
v_mov_b32 v198, 0
Zvav 112,144,208
Ra 132,242,41984
K 221,174,175
Zvav 144,144,216
v_mov_b32 v199, 0
v_cndmask_b32_e64 v180, v241, v180, s[72:73]
Zvav 148,148,216
v_cndmask_b32_e64 v181, v241, v181, s[74:75]
Zvav 116,148,208
Ra 136,242,43008
v_cndmask_b32_e64 v182, v241, v182, s[76:77]
Zvav 84,148,200
v_cndmask_b32_e64 v183, v241, v183, s[78:79]
Zvav 52,148,192
X 180
Zvav 56,152,192
Ra 140,242,44032
X 181
Zvav 88,152,200
Zvav 120,152,208
X 182
Zvav 152,152,216
K 206,180,181
Zvav 156,156,216
X 183
Zvav 124,156,208
Zvav 92,156,200
s_waitcnt vmcnt(0)
s_and_b64 s[86:87], s[88:89], s[24:25]
s_cbranch_scc1 .LL_47%=
v_mov_b64 v[28:29], s[12:13]
s_mov_b64 exec, 1
buffer_store_dwordx2 v[28:29], off, s[8:11], s69 sc1
s_mov_b64 exec, -1
.LL_47%=:
K 207,182,183
Zvav 60,156,192
s_cmp_eq_u32 s64, 15
s_cbranch_scc1 .Lf2_39%=
.Ll2_36%=:
W 0
Cvaa 160,128,64,224
Ra 144,242,45056
Ra 148,242,46080
Mvaa 160,132,68
s_add_u32 m0, s62, 0
Ra 152,242,47104
Cvaa 164,132,84,228
X 184
Mvaa 164,128,80
X 185
Cvaa 168,128,96,232
X 186
Mvaa 168,132,100
Ra 156,242,48128
K 214,184,185
Cvaa 172,132,116,236
Dc 1,8,61,0
Mvaa 172,128,112
X 187
Mvaa 160,136,72
X 188
Mvaa 160,140,76
Ra 128,242,57344
K 215,186,187
Mvaa 164,140,92
Dc 1,8,61,1024
Mvaa 164,136,88
X 189
Mvaa 168,136,104
X 190
Mvaa 168,140,108
Ra 132,242,58368
K 222,188,189
Mvaa 172,140,124
Dc 1,8,61,2048
Mvaa 172,136,120
X 191
v_smfmac_f32_16x16x64_bf16 a[160:163], a[176:179], v[192:199], v240
Ra 136,242,59392
K 223,190,191
v_smfmac_f32_16x16x64_bf16 a[164:167], a[176:179], v[200:207], v240
X 160
v_smfmac_f32_16x16x64_bf16 a[168:171], a[176:179], v[208:215], v240
Dc 1,8,61,3072
v_smfmac_f32_16x16x64_bf16 a[172:175], a[176:179], v[216:223], v240
W 3
X 161
Cvaa 176,144,64,224
s_add_u32 m0, s62, 4096
X 162
Mvaa 176,148,68
Ra 140,242,60416
K 192,160,161
Cvaa 180,148,84,228
X 163
Mvaa 180,144,80
Dc 243,8,61,0
Cvaa 184,144,96,232
X 164
Mvaa 184,148,100
K 193,162,163
Cvaa 188,148,116,236
X 165
Mvaa 188,144,112
Dc 243,8,61,1024
Ra 144,242,61440
Mvaa 176,152,72
X 166
Mvaa 176,156,76
Ra 148,242,62464
K 200,164,165
Mvaa 180,156,92
X 167
Mvaa 180,152,88
Dc 243,8,61,2048
Mvaa 184,152,104
X 168
Mvaa 184,156,108
K 201,166,167
Mvaa 188,156,124
X 169
Mvaa 188,152,120
W 2
Dc 243,8,61,3072
Ra 152,242,63488
Mvav 32,128,196
s_add_u32 m0, s62, 8192
X 170
Mvav 64,128,204
Ra 156,242,64512
K 208,168,169
Mvav 96,128,212
X 171
Mvav 128,128,220
Dc 244,8,61,0
Mvav 132,132,220
X 172
Mvav 100,132,212
Ra 128,242,32768
K 209,170,171
Mvav 68,132,204
X 173
Mvav 36,132,196
Dc 244,8,61,1024
Mvav 40,136,196
X 174
Mvav 72,136,204
Ra 132,242,33792
K 216,172,173
Mvav 104,136,212
X 175
Mvav 136,136,220
Dc 244,8,61,2048
Mvav 140,140,220
X 176
Mvav 108,140,212
Ra 136,242,34816
K 217,174,175
Mvav 76,140,204
X 177
Mvav 44,140,196
W 3
Dc 244,8,61,3072
Mvav 48,144,196
X 178
Mvav 80,144,204
s_add_u32 m0, s62, 12288
Ra 140,242,35840
Mvav 112,144,212
K 194,176,177
Mvav 144,144,220
X 179
Mvav 148,148,220
X 180
Mvav 116,148,212
Dc 245,8,61,0
Ra 144,242,36864
Mvav 84,148,204
K 195,178,179
Mvav 52,148,196
X 181
Mvav 56,152,196
X 182
Mvav 88,152,204
Dc 245,8,61,1024
Ra 148,242,37888
Mvav 120,152,212
K 202,180,181
Mvav 152,152,220
X 183
Mvav 156,156,220
X 184
Mvav 124,156,212
Dc 245,8,61,2048
Ra 152,242,38912
Mvav 92,156,204
K 203,182,183
Mvav 60,156,196
W 3
X 185
Cvaa 160,128,64,224
X 186
Mvaa 160,132,68
Dc 245,8,61,3072
Ra 156,242,39936
Cvaa 164,132,84,228
s_add_u32 s61, s61, s63
K 210,184,185
Mvaa 164,128,80
X 187
Cvaa 168,128,96,232
X 188
Mvaa 168,132,100
Cvaa 172,132,116,236
s_xor_b32 s62, s62, 0x10000
K 211,186,187
Mvaa 172,128,112
X 189
Mvaa 160,136,72
Ra 128,242,49152
Ra 132,242,50176
Mvaa 160,140,76
s_add_u32 s64, s64, 1
X 190
Mvaa 164,140,92
K 218,188,189
Mvaa 164,136,88
X 191
Mvaa 168,136,104
Mvaa 168,140,108
X 160
Mvaa 172,140,124
K 219,190,191
Mvaa 172,136,120
W 2
X 161
Cvaa 176,144,64,224
Ra 136,242,51200
Ra 140,242,52224
Mvaa 176,148,68
X 162
Cvaa 180,148,84,228
K 196,160,161
Mvaa 180,144,80
X 163
Cvaa 184,144,96,232
Mvaa 184,148,100
X 164
Cvaa 188,148,116,236
K 197,162,163
Mvaa 188,144,112
X 165
Mvaa 176,152,72
Ra 144,242,53248
Ra 148,242,54272
Mvaa 176,156,76
X 166
Mvaa 180,156,92
K 204,164,165
Mvaa 180,152,88
X 167
Mvaa 184,152,104
Mvaa 184,156,108
X 168
Mvaa 188,156,124
Mvaa 188,152,120
W 2
Ra 152,242,55296
K 205,166,167
Mvav 32,128,192
X 169
Mvav 64,128,200
Ra 156,242,56320
Mvav 96,128,208
X 170
Mvav 128,128,216
K 212,168,169
Mvav 132,132,216
X 171
Mvav 100,132,208
X 172
Mvav 68,132,200
Mvav 36,132,192
K 213,170,171
Mvav 40,136,192
X 173
Mvav 72,136,200
X 174
Mvav 104,136,208
Mvav 136,136,216
K 220,172,173
Mvav 140,140,216
X 175
Mvav 108,140,208
X 176
Mvav 76,140,200
s_waitcnt vmcnt(0) lgkmcnt(0)
s_barrier
v_xor_b32 v242, 0x10000, v242
Mvav 44,140,192
Ra 128,242,40960
K 221,174,175
Mvav 48,144,192
X 177
Mvav 80,144,200
X 178
Mvav 112,144,208
Ra 132,242,41984
Mvav 144,144,216
K 198,176,177
Mvav 148,148,216
X 179
Mvav 116,148,208
X 180
Mvav 84,148,200
Ra 136,242,43008
Mvav 52,148,192
K 199,178,179
Mvav 56,152,192
X 181
Mvav 88,152,200
Ra 140,242,44032
Mvav 120,152,208
X 182
Mvav 152,152,216
K 206,180,181
Mvav 156,156,216
X 183
Mvav 124,156,208
Mvav 92,156,200
K 207,182,183
Mvav 60,156,192
s_cmp_lg_u32 s64, 15
s_cbranch_scc1 .Ll2_36%=
.Lf2_39%=:
W 0
Cvaa 160,128,64,224
s_lshl_b32 s92, s58, 2
s_add_u32 s91, s92, s51
s_add_u32 s95, s91, 3
Mvaa 160,132,68
s_and_b32 s95, s95, 15
s_mul_i32 s95, s95, 40960
s_mul_i32 s71, s51, 10240
Cvaa 164,132,84,228
s_add_u32 s71, s71, 0
s_add_u32 m0, s71, s48
s_lshl2_add_u32 s94, s48, s95
Mvaa 164,128,80
Ra 144,242,45056
Ra 148,242,46080
Cvaa 168,128,96,232
Ra 152,242,47104
D 16,40,94,0
Mvaa 168,132,100
s_add_u32 s95, s91, 5
s_and_b32 s95, s95, 15
)"
R"(s_mul_i32 s95, s95, 40960
Cvaa 172,132,116,236
s_mul_i32 s71, s51, 10240
s_add_u32 s71, s71, 20480
Ra 156,242,48128
Mvaa 172,128,112
X 184
Mvaa 160,136,72
X 185
Mvaa 160,140,76
X 186
Mvaa 164,140,92
D 17,40,94,1024
K 214,184,185
Mvaa 164,136,88
X 187
Mvaa 168,136,104
X 188
Mvaa 168,140,108
Ra 128,242,57344
K 215,186,187
Mvaa 172,140,124
X 189
Mvaa 172,136,120
X 190
v_smfmac_f32_16x16x64_bf16 a[160:163], a[176:179], v[192:199], v240
D 18,40,94,2048
K 222,188,189
v_smfmac_f32_16x16x64_bf16 a[164:167], a[176:179], v[200:207], v240
X 191
s_nop 0
K 223,190,191
v_smfmac_f32_16x16x64_bf16 a[168:171], a[176:179], v[208:215], v240
Ra 132,242,58368
Ra 136,242,59392
v_smfmac_f32_16x16x64_bf16 a[172:175], a[176:179], v[216:223], v240
W 3
D 19,40,94,3072
Cvaa 176,144,64,224
s_add_u32 m0, m0, 4096
X 160
Mvaa 176,148,68
X 161
Cvaa 180,148,84,228
X 162
Mvaa 180,144,80
buffer_load_dwordx4 v20, s[40:43], s94 offen lds
v_bfe_u32 v28, v1, 4, 2
Cvaa 184,144,96,232
v_lshlrev_b32 v28, 3, v28
K 192,160,161
Mvaa 184,148,100
X 163
Cvaa 188,148,116,236
X 164
Mvaa 188,144,112
Ra 140,242,60416
K 193,162,163
Mvaa 176,152,72
buffer_load_dwordx2 v[28:29], v28, s[8:11], s54 offen sc1
Ra 144,242,61440
Mvaa 176,156,76
s_add_u32 m0, s71, s48
s_lshl2_add_u32 s94, s48, s95
Ra 148,242,62464
Mvaa 180,156,92
X 165
Mvaa 180,152,88
X 166
Mvaa 184,152,104
D 16,40,94,0
K 200,164,165
Mvaa 184,156,108
X 167
Mvaa 188,156,124
X 172
Mvaa 188,152,120
W 2
D 17,40,94,1024
K 201,166,167
Mvav 32,128,196
X 173
Mvav 64,128,204
X 174
Mvav 96,128,212
D 18,40,94,2048
K 216,172,173
Mvav 128,128,220
X 175
Mvav 132,132,220
s_mov_b32 s18, 40960
X 168
Mvav 100,132,212
Ra 152,242,63488
K 217,174,175
Mvav 68,132,204
s_mov_b32 s19, 73728
X 169
Mvav 36,132,196
s_bitcmp1_b32 s14, 0
s_cselect_b32 s18, 49152, s18
s_cselect_b32 s19, 98304, s19
X 170
Mvav 40,136,196
s_cmp_lt_u32 s14, 2
s_cselect_b32 s20, s18, s19
s_cselect_b32 s21, s19, s18
D 19,40,94,3072
Ra 156,242,64512
Mvav 72,136,204
s_add_u32 m0, m0, 4096
Ra 128,242,49152
K 208,168,169
Mvav 104,136,212
X 171
Mvav 136,136,220
X 176
Mvav 140,140,220
buffer_load_dwordx4 v20, s[40:43], s94 offen lds
K 209,170,171
Mvav 108,140,212
s_waitcnt vmcnt(5)
v_cmp_eq_u64 vcc, s[12:13], v[28:29]
s_cmp_eq_u64 vcc, -1
s_cselect_b32 s46, 79691776, 0
s_add_u32 s81, s58, 0
s_and_b32 s81, s81, 3
Mvav 76,140,204
s_lshl_b32 s81, s81, 12
s_add_u32 s81, s81, s49
v_add_u32 v2, s20, v1
Mvav 44,140,196
W 1
X 177
Mvav 48,144,196
X 178
Mvav 80,144,204
buffer_load_dwordx4 a[184:187], v1, s[44:47], s81 offen offset:0 sc1
K 194,176,177
Mvav 112,144,212
s_add_u32 s82, s81, 131072
X 179
Mvav 144,144,220
X 180
Mvav 148,148,220
v_add_u32 v3, s21, v1
K 195,178,179
Mvav 116,148,212
s_lshl_b32 s18, s14, 9
buffer_load_dwordx4 a[200:203], v1, s[44:47], s82 offen offset:0 sc1
Ra 132,242,50176
Mvav 84,148,204
X 181
Mvav 52,148,196
v_lshrrev_b32 v232, 1, v1
v_add_u32 v232, s18, v232
Mvav 56,152,196
X 182
Mvav 88,152,204
K 202,180,181
v_bfe_u32 v12, v1, 6, 2
Mvav 120,152,212
buffer_load_dwordx4 a[188:191], v1, s[44:47], s81 offen offset:1024 sc1
v_lshrrev_b32 v13, 8, v1
Mvav 152,152,220
X 183
Mvav 156,156,220
X 184
Mvav 124,156,212
Ra 136,242,51200
v_bfe_u32 v14, v1, 4, 1
Mvav 92,156,204
Ra 140,242,52224
K 203,182,183
Mvav 60,156,196
buffer_load_dwordx4 a[204:207], v1, s[44:47], s82 offen offset:1024 sc1
v_lshlrev_b32 v14, 4, v14
Maav 160,180,192
X 185
X 186
K 210,184,185
X 187
X 188
K 211,186,187
Maav 164,180,200
X 189
X 190
K 218,188,189
X 191
s_nop 0
K 219,190,191
Maav 168,180,208
v_mov_b32 v15, 0x3f80
v_lshlrev_b32 v15, v14, v15
Maav 172,180,216
W 0
buffer_load_dwordx4 a[192:195], v1, s[44:47], s81 offen offset:2048 sc1
v_bfe_u32 v14, v1, 5, 1
Mvav 32,128,192
v_xor_b32 v27, v12, v13
v_lshl_or_b32 v27, v27, 1, v14
Mvav 64,128,200
Ra 144,242,53248
v_cmp_eq_u32_e64 s[86:87], 0, v27
Mvav 96,128,208
buffer_load_dwordx4 a[208:211], v1, s[44:47], s82 offen offset:2048 sc1
v_mov_b32 v226, 0
Mvav 128,128,216
Ra 148,242,54272
v_cndmask_b32_e64 v224, 0, v15, s[86:87]
Mvav 132,132,216
v_mov_b32 v228, 0
v_mov_b32 v230, v224
Mvav 100,132,208
buffer_load_dwordx4 a[196:199], v1, s[44:47], s81 offen offset:3072 sc1
v_cmp_eq_u32_e64 s[86:87], 1, v27
Mvav 68,132,200
Ra 152,242,55296
v_mov_b32 v227, 0
Mvav 36,132,192
v_cndmask_b32_e64 v225, 0, v15, s[86:87]
v_mov_b32 v229, 0
Mvav 40,136,192
buffer_load_dwordx4 a[212:215], v1, s[44:47], s82 offen offset:3072 sc1
v_mov_b32 v231, v225
Mvav 72,136,200
s_add_u32 s81, s58, 1
s_and_b32 s81, s81, 3
s_lshl_b32 s81, s81, 12
Mvav 104,136,208
s_add_u32 s81, s81, s49
Ra 156,242,56320
G 4,160
Mvav 136,136,216
buffer_load_dwordx4 a[216:219], v1, s[44:47], s81 offen offset:0 sc1
G 8,161
Mvav 140,140,216
s_add_u32 s82, s81, 131072
G 5,164
G 9,165
Mvav 108,140,208
G 6,168
G 10,169
Mvav 76,140,200
buffer_load_dwordx4 a[232:235], v1, s[44:47], s82 offen offset:0 sc1
G 7,172
Mvav 44,140,192
W 0
G 11,173
A 4,4,8
Mvav 48,144,192
A 5,5,9
A 6,6,10
Mvav 80,144,200
buffer_load_dwordx4 a[220:223], v1, s[44:47], s81 offen offset:1024 sc1
A 7,7,11
Mvav 112,144,208
ds_write_b64 v232, v[6:7] offset:57344
A 8,4,5
Mvav 144,144,216
A 9,6,7
A 8,8,9
Mvav 148,148,216
buffer_load_dwordx4 a[236:239], v1, s[44:47], s82 offen offset:1024 sc1
v_cmp_ngt_f32_e64 s[86:87], s90, v8
Mvav 116,148,208
s_lshl_b32 s19, s14, 2
v_mov_b32 v9, s19
Mvav 84,148,200
s_or_b64 s[88:89], s[88:89], s[86:87]
s_and_b64 s[88:89], s[88:89], s[24:25]
s_cselect_b32 s18, 1, 0
v_mov_b32 v8, s18
Mvav 52,148,192
ds_write_b32 v9, v8 offset:59392
v_xor_b32 v10, 1024, v232
Mvav 56,152,192
v_bfe_u32 v11, v1, 2, 4
K 96,96,97
Mvav 88,152,200
K 97,98,99
K 98,100,101
Mvav 120,152,208
buffer_load_dwordx4 a[224:227], v1, s[44:47], s81 offen offset:2048 sc1
K 99,102,103
Mvav 152,152,216
ds_write_b128 v2, v[96:99] offset:0
K 128,128,129
Mvav 156,156,216
K 129,130,131
K 130,132,133
Mvav 124,156,208
buffer_load_dwordx4 a[240:243], v1, s[44:47], s82 offen offset:2048 sc1
K 131,134,135
Mvav 92,156,200
ds_write_b128 v2, v[128:131] offset:4096
K 104,104,105
Mvav 60,156,192
K 105,106,107
K 106,108,109
K 107,110,111
ds_write_b128 v2, v[104:107] offset:1024
K 136,136,137
K 137,138,139
K 138,140,141
K 139,142,143
ds_write_b128 v2, v[136:139] offset:5120
buffer_load_dwordx4 a[228:231], v1, s[44:47], s81 offen offset:3072 sc1
K 112,112,113
K 113,114,115
K 114,116,117
K 115,118,119
buffer_load_dwordx4 a[244:247], v1, s[44:47], s82 offen offset:3072 sc1
ds_write_b128 v2, v[112:115] offset:2048
K 144,144,145
K 145,146,147
K 146,148,149
K 147,150,151
ds_write_b128 v2, v[144:147] offset:6144
K 120,120,121
K 121,122,123
K 122,124,125
K 123,126,127
ds_write_b128 v2, v[120:123] offset:3072
K 152,152,153
K 153,154,155
K 154,156,157
K 155,158,159
ds_write_b128 v2, v[152:155] offset:7168
s_branch .Lend_41%=
.Lfs_40%=:
v_smfmac_f32_16x16x64_bf16 a[160:163], a[176:179], v[192:199], v240
s_lshl_b32 s92, s58, 2
s_add_u32 s91, s92, s51
s_add_u32 s95, s91, 3
s_and_b32 s95, s95, 15
s_mul_i32 s95, s95, 40960
s_mul_i32 s71, s51, 10240
s_add_u32 s71, s71, 0
v_smfmac_f32_16x16x64_bf16 a[164:167], a[176:179], v[200:207], v240
s_add_u32 m0, s71, s48
s_lshl2_add_u32 s94, s48, s95
Ra 144,242,45056
Ra 148,242,46080
Ra 152,242,47104
Ra 156,242,48128
D 16,40,94,0
v_smfmac_f32_16x16x64_bf16 a[168:171], a[176:179], v[208:215], v240
s_add_u32 s95, s91, 5
s_and_b32 s95, s95, 15
s_mul_i32 s95, s95, 40960
s_mul_i32 s71, s51, 10240
s_add_u32 s71, s71, 20480
v_smfmac_f32_16x16x64_bf16 a[172:175], a[176:179], v[216:223], v240
W 4
Cvaa 168,128,96,232
D 17,40,94,1024
v_mov_b32 v192, 0
G 12,160
Mvaa 168,132,100
G 4,161
A 12,12,4
v_cmp_nlt_f32_e64 s[86:87], v12, s90
v_rcp_f32 v12, v12
s_or_b64 s[88:89], s[88:89], s[86:87]
v_accvgpr_write_b32 a160, 0
Cvaa 172,132,116,236
v_accvgpr_write_b32 a161, 0
v_accvgpr_write_b32 a162, 0
v_accvgpr_write_b32 a163, 0
G 13,164
G 5,165
v_mov_b32 v193, 0
Mvaa 172,128,112
D 18,40,94,2048
A 13,13,5
v_cmp_nlt_f32_e64 s[86:87], v13, s90
v_rcp_f32 v13, v13
s_or_b64 s[88:89], s[88:89], s[86:87]
v_accvgpr_write_b32 a164, 0
Mvaa 168,136,104
v_accvgpr_write_b32 a165, 0
v_accvgpr_write_b32 a166, 0
v_accvgpr_write_b32 a167, 0
U 32,32,12
U 33,33,12
U 34,34,12
Mvaa 168,140,108
v_mov_b32 v200, 0
U 35,35,12
U 36,36,12
U 37,37,12
U 38,38,12
U 39,39,12
Mvaa 172,140,124
Ra 128,242,16384
Ra 132,242,17408
K 32,32,33
K 33,34,35
K 34,36,37
)"
R"(K 35,38,39
s_add_u32 s93, s67, 0
Mvaa 172,136,120
Ra 136,242,18432
Ra 140,242,19456
buffer_store_dwordx4 v[32:35], v1, s[8:11], s93 offen offset:0 sc1
U 40,40,12
v_mov_b32 v201, 0
W 4
Cvaa 188,144,112,236
U 41,41,12
U 42,42,12
U 43,43,12
U 44,44,12
U 45,45,12
U 46,46,12
Mvaa 188,148,116
D 19,40,94,3072
s_add_u32 m0, m0, 4096
U 47,47,12
K 40,40,41
K 41,42,43
K 42,44,45
K 43,46,47
Mvaa 188,152,120
Ra 144,242,20480
Ra 148,242,21504
v_cndmask_b32_e64 v168, v241, v168, s[72:73]
v_cndmask_b32_e64 v169, v241, v169, s[74:75]
v_cndmask_b32_e64 v170, v241, v170, s[76:77]
v_cndmask_b32_e64 v171, v241, v171, s[78:79]
Mvaa 188,156,124
W 2
buffer_store_dwordx4 v[40:43], v1, s[8:11], s93 offen offset:1024 sc1
U 48,48,12
U 49,49,12
U 50,50,12
U 51,51,12
U 52,52,12
Mvav 96,128,212
U 53,53,12
U 54,54,12
U 55,55,12
K 48,48,49
K 49,50,51
K 50,52,53
Mvav 128,128,220
K 51,54,55
buffer_store_dwordx4 v[48:51], v1, s[8:11], s93 offen offset:2048 sc1
U 56,56,12
U 57,57,12
U 58,58,12
U 59,59,12
Mvav 132,132,220
Ra 152,242,22528
Ra 156,242,23552
U 60,60,12
U 61,61,12
U 62,62,12
U 63,63,12
Mvav 100,132,212
buffer_load_dwordx4 v20, s[40:43], s94 offen lds
v_bfe_u32 v28, v1, 4, 2
v_lshlrev_b32 v28, 3, v28
K 56,56,57
K 57,58,59
K 58,60,61
Mvav 104,136,212
Ra 128,242,49152
K 59,62,63
X 168
X 169
Mvav 136,136,220
buffer_store_dwordx4 v[56:59], v1, s[8:11], s93 offen offset:3072 sc1
Ra 132,242,50176
G 14,168
G 6,169
A 14,14,6
v_cmp_nlt_f32_e64 s[86:87], v14, s90
Mvav 140,140,220
v_rcp_f32 v14, v14
s_or_b64 s[88:89], s[88:89], s[86:87]
v_accvgpr_write_b32 a168, 0
v_accvgpr_write_b32 a169, 0
v_accvgpr_write_b32 a170, 0
v_accvgpr_write_b32 a171, 0
Mvav 108,140,212
W 2
buffer_load_dwordx2 v[28:29], v28, s[8:11], s54 offen sc1
s_add_u32 m0, s71, s48
s_lshl2_add_u32 s94, s48, s95
G 15,172
G 7,173
A 15,15,7
Mvav 112,144,212
v_cmp_nlt_f32_e64 s[86:87], v15, s90
v_rcp_f32 v15, v15
s_or_b64 s[88:89], s[88:89], s[86:87]
v_accvgpr_write_b32 a172, 0
v_accvgpr_write_b32 a173, 0
v_accvgpr_write_b32 a174, 0
Mvav 144,144,220
v_accvgpr_write_b32 a175, 0
U 64,64,13
U 65,65,13
U 66,66,13
U 67,67,13
U 68,68,13
Mvav 148,148,220
U 69,69,13
U 70,70,13
U 71,71,13
K 64,64,65
K 65,66,67
K 66,68,69
Mvav 116,148,212
D 16,40,94,0
Ra 136,242,51200
K 67,70,71
s_add_u32 s93, s67, 131072
X 170
K 208,168,169
Mvav 120,152,212
Ra 140,242,52224
Ra 144,242,53248
X 171
X 172
Mvav 152,152,220
buffer_store_dwordx4 v[64:67], v1, s[8:11], s93 offen offset:0 sc1
U 72,72,13
U 73,73,13
U 74,74,13
U 75,75,13
U 76,76,13
Mvav 156,156,220
K 209,170,171
X 173
X 174
K 216,172,173
X 175
v_mov_b32 v194, 0
K 217,174,175
v_mov_b32 v195, 0
v_mov_b32 v202, 0
v_mov_b32 v203, 0
v_mov_b32 v210, 0
v_mov_b32 v211, 0
Mvav 124,156,212
v_cndmask_b32_e64 v188, v241, v188, s[72:73]
v_cndmask_b32_e64 v189, v241, v189, s[74:75]
v_cndmask_b32_e64 v190, v241, v190, s[76:77]
v_cndmask_b32_e64 v191, v241, v191, s[78:79]
X 188
X 189
X 190
K 218,188,189
X 191
s_nop 0
K 219,190,191
Maav 168,180,208
U 77,77,13
U 78,78,13
U 79,79,13
K 72,72,73
K 73,74,75
K 74,76,77
Maav 172,180,216
W 1
K 75,78,79
buffer_store_dwordx4 v[72:75], v1, s[8:11], s93 offen offset:1024 sc1
U 80,80,13
U 81,81,13
U 82,82,13
U 83,83,13
Zvav 32,128,192
U 84,84,13
U 85,85,13
U 86,86,13
U 87,87,13
K 80,80,81
K 81,82,83
Zvav 64,128,200
K 82,84,85
K 83,86,87
buffer_store_dwordx4 v[80:83], v1, s[8:11], s93 offen offset:2048 sc1
U 88,88,13
U 89,89,13
U 90,90,13
U 91,91,13
U 92,92,13
U 93,93,13
U 94,94,13
U 95,95,13
K 88,88,89
K 89,90,91
K 90,92,93
K 91,94,95
buffer_store_dwordx4 v[88:91], v1, s[8:11], s93 offen offset:3072 sc1
U 96,96,14
U 97,97,14
U 98,98,14
U 99,99,14
U 100,100,14
U 101,101,14
U 102,102,14
U 103,103,14
K 96,96,97
K 97,98,99
K 98,100,101
K 99,102,103
s_add_u32 s93, s67, 262144
buffer_store_dwordx4 v[96:99], v1, s[8:11], s93 offen offset:0 sc1
s_nop 1
Zvav 96,128,208
U 104,104,14
U 105,105,14
U 106,106,14
U 107,107,14
U 108,108,14
U 109,109,14
U 110,110,14
U 111,111,14
K 104,104,105
K 105,106,107
K 106,108,109
K 107,110,111
buffer_store_dwordx4 v[104:107], v1, s[8:11], s93 offen offset:1024 sc1
U 112,112,14
U 113,113,14
U 114,114,14
U 115,115,14
U 116,116,14
U 117,117,14
U 118,118,14
U 119,119,14
K 112,112,113
K 113,114,115
K 114,116,117
K 115,118,119
buffer_store_dwordx4 v[112:115], v1, s[8:11], s93 offen offset:2048 sc1
U 120,120,14
U 121,121,14
U 122,122,14
U 123,123,14
U 124,124,14
U 125,125,14
U 126,126,14
U 127,127,14
K 120,120,121
K 121,122,123
K 122,124,125
K 123,126,127
buffer_store_dwordx4 v[120:123], v1, s[8:11], s93 offen offset:3072 sc1
U 128,128,15
U 129,129,15
U 130,130,15
U 131,131,15
U 132,132,15
U 133,133,15
U 134,134,15
U 135,135,15
K 128,128,129
K 129,130,131
K 130,132,133
K 131,134,135
s_add_u32 s93, s67, 393216
buffer_store_dwordx4 v[128:131], v1, s[8:11], s93 offen offset:0 sc1
s_nop 1
Zvav 128,128,216
U 136,136,15
U 137,137,15
U 138,138,15
U 139,139,15
U 140,140,15
U 141,141,15
Zvav 132,132,216
D 17,40,94,1024
Ra 148,242,54272
U 142,142,15
U 143,143,15
K 136,136,137
K 137,138,139
Zvav 100,132,208
Ra 152,242,55296
Ra 156,242,56320
K 138,140,141
K 139,142,143
Zvav 68,132,200
D 18,40,94,2048
Zvav 36,132,192
Zvav 40,136,192
D 19,40,94,3072
s_add_u32 m0, m0, 4096
Zvav 72,136,200
Zvav 104,136,208
buffer_store_dwordx4 v[136:139], v1, s[8:11], s93 offen offset:1024 sc1
U 144,144,15
U 145,145,15
U 146,146,15
U 147,147,15
U 148,148,15
Zvav 136,136,216
U 149,149,15
U 150,150,15
U 151,151,15
K 144,144,145
K 145,146,147
K 146,148,149
Zvav 140,140,216
buffer_load_dwordx4 v20, s[40:43], s94 offen lds
s_waitcnt vmcnt(15)
v_cmp_eq_u64 vcc, s[12:13], v[28:29]
s_cmp_eq_u64 vcc, -1
s_cselect_b32 s46, 79691776, 0
s_add_u32 s81, s58, 0
s_and_b32 s81, s81, 3
s_lshl_b32 s81, s81, 12
s_add_u32 s81, s81, s49
K 147,150,151
Zvav 108,140,208
Zvav 76,140,200
buffer_load_dwordx4 a[184:187], v1, s[44:47], s81 offen offset:0 sc1
s_add_u32 s82, s81, 131072
Zvav 44,140,192
W 0
Zvav 48,144,192
buffer_store_dwordx4 v[144:147], v1, s[8:11], s93 offen offset:2048 sc1
U 152,152,15
U 153,153,15
U 154,154,15
U 155,155,15
U 156,156,15
Zvav 80,144,200
U 157,157,15
U 158,158,15
U 159,159,15
K 152,152,153
K 153,154,155
K 154,156,157
Zvav 112,144,208
buffer_load_dwordx4 a[200:203], v1, s[44:47], s82 offen offset:0 sc1
K 155,158,159
Zvav 144,144,216
Zvav 148,148,216
buffer_load_dwordx4 a[188:191], v1, s[44:47], s81 offen offset:1024 sc1
Zvav 116,148,208
Zvav 84,148,200
buffer_store_dwordx4 v[152:155], v1, s[8:11], s93 offen offset:3072 sc1
s_mov_b32 s18, 40960
s_mov_b32 s19, 73728
s_bitcmp1_b32 s14, 0
s_cselect_b32 s18, 49152, s18
s_cselect_b32 s19, 98304, s19
s_cmp_lt_u32 s14, 2
s_cselect_b32 s20, s18, s19
s_cselect_b32 s21, s19, s18
v_add_u32 v2, s20, v1
v_add_u32 v3, s21, v1
Zvav 52,148,192
s_lshl_b32 s18, s14, 9
v_lshrrev_b32 v232, 1, v1
v_add_u32 v232, s18, v232
v_bfe_u32 v12, v1, 6, 2
v_lshrrev_b32 v13, 8, v1
v_bfe_u32 v14, v1, 4, 1
v_lshlrev_b32 v14, 4, v14
Zvav 56,152,192
v_mov_b32 v15, 0x3f80
v_lshlrev_b32 v15, v14, v15
v_bfe_u32 v14, v1, 5, 1
v_xor_b32 v27, v12, v13
v_lshl_or_b32 v27, v27, 1, v14
v_cmp_eq_u32_e64 s[86:87], 0, v27
Zvav 88,152,200
buffer_load_dwordx4 a[204:207], v1, s[44:47], s82 offen offset:1024 sc1
v_mov_b32 v226, 0
Zvav 120,152,208
v_cndmask_b32_e64 v224, 0, v15, s[86:87]
v_mov_b32 v228, 0
v_mov_b32 v230, v224
v_cmp_eq_u32_e64 s[86:87], 1, v27
v_mov_b32 v227, 0
s_nop 0
v_cndmask_b32_e64 v225, 0, v15, s[86:87]
Zvav 152,152,216
buffer_load_dwordx4 a[192:195], v1, s[44:47], s81 offen offset:2048 sc1
v_mov_b32 v229, 0
v_mov_b32 v231, v225
G 4,160
G 8,161
G 5,164
Zvav 156,156,216
G 9,165
G 6,168
G 10,169
G 7,172
G 11,173
A 4,4,8
Zvav 124,156,208
buffer_load_dwordx4 a[208:211], v1, s[44:47], s82 offen offset:2048 sc1
)"
R"(A 5,5,9
A 6,6,10
A 7,7,11
ds_write_b64 v232, v[6:7] offset:57344
A 8,4,5
Zvav 92,156,200
A 9,6,7
A 8,8,9
v_cmp_ngt_f32_e64 s[86:87], s90, v8
s_lshl_b32 s19, s14, 2
v_mov_b32 v9, s19
s_or_b64 s[88:89], s[88:89], s[86:87]
s_and_b64 s[88:89], s[88:89], s[24:25]
s_cselect_b32 s18, 1, 0
Zvav 60,156,192
buffer_load_dwordx4 a[196:199], v1, s[44:47], s81 offen offset:3072 sc1
v_mov_b32 v8, s18
ds_write_b32 v9, v8 offset:59392
v_xor_b32 v10, 1024, v232
v_bfe_u32 v11, v1, 2, 4
K 96,96,97
buffer_load_dwordx4 a[212:215], v1, s[44:47], s82 offen offset:3072 sc1
s_add_u32 s81, s58, 1
s_and_b32 s81, s81, 3
s_lshl_b32 s81, s81, 12
s_add_u32 s81, s81, s49
K 97,98,99
K 98,100,101
buffer_load_dwordx4 a[216:219], v1, s[44:47], s81 offen offset:0 sc1
s_add_u32 s82, s81, 131072
K 99,102,103
ds_write_b128 v2, v[96:99] offset:0
K 128,128,129
K 129,130,131
K 130,132,133
K 131,134,135
ds_write_b128 v2, v[128:131] offset:4096
buffer_load_dwordx4 a[232:235], v1, s[44:47], s82 offen offset:0 sc1
buffer_load_dwordx4 a[220:223], v1, s[44:47], s81 offen offset:1024 sc1
K 104,104,105
K 105,106,107
buffer_load_dwordx4 a[236:239], v1, s[44:47], s82 offen offset:1024 sc1
K 106,108,109
K 107,110,111
ds_write_b128 v2, v[104:107] offset:1024
K 136,136,137
K 137,138,139
K 138,140,141
K 139,142,143
ds_write_b128 v2, v[136:139] offset:5120
buffer_load_dwordx4 a[224:227], v1, s[44:47], s81 offen offset:2048 sc1
buffer_load_dwordx4 a[240:243], v1, s[44:47], s82 offen offset:2048 sc1
buffer_load_dwordx4 a[228:231], v1, s[44:47], s81 offen offset:3072 sc1
K 112,112,113
K 113,114,115
K 114,116,117
K 115,118,119
buffer_load_dwordx4 a[244:247], v1, s[44:47], s82 offen offset:3072 sc1
ds_write_b128 v2, v[112:115] offset:2048
K 144,144,145
K 145,146,147
K 146,148,149
K 147,150,151
ds_write_b128 v2, v[144:147] offset:6144
K 120,120,121
K 121,122,123
K 122,124,125
K 123,126,127
ds_write_b128 v2, v[120:123] offset:3072
K 152,152,153
K 153,154,155
K 154,156,157
K 155,158,159
ds_write_b128 v2, v[152:155] offset:7168
s_waitcnt vmcnt(13)
s_and_b64 s[86:87], s[88:89], s[24:25]
s_cbranch_scc1 .LL_48%=
v_mov_b64 v[28:29], s[12:13]
s_mov_b64 exec, 1
buffer_store_dwordx2 v[28:29], off, s[8:11], s69 sc1
s_mov_b64 exec, -1
.LL_48%=:
s_mov_b32 s70, 0
s_branch .Lend_41%=
.Lpd_30%=:
v_pk_mul_f32 v[32:33], v[32:33], s[22:23]
v_pk_mul_f32 v[34:35], v[34:35], s[22:23]
v_pk_mul_f32 v[36:37], v[36:37], s[22:23]
v_pk_mul_f32 v[38:39], v[38:39], s[22:23]
v_pk_mul_f32 v[64:65], v[64:65], s[22:23]
v_pk_mul_f32 v[66:67], v[66:67], s[22:23]
v_pk_mul_f32 v[68:69], v[68:69], s[22:23]
v_pk_mul_f32 v[70:71], v[70:71], s[22:23]
v_pk_mul_f32 v[96:97], v[96:97], s[22:23]
v_pk_mul_f32 v[98:99], v[98:99], s[22:23]
v_pk_mul_f32 v[100:101], v[100:101], s[22:23]
v_pk_mul_f32 v[102:103], v[102:103], s[22:23]
v_pk_mul_f32 v[128:129], v[128:129], s[22:23]
v_pk_mul_f32 v[130:131], v[130:131], s[22:23]
v_pk_mul_f32 v[132:133], v[132:133], s[22:23]
v_pk_mul_f32 v[134:135], v[134:135], s[22:23]
v_pk_mul_f32 v[40:41], v[40:41], s[22:23]
v_pk_mul_f32 v[42:43], v[42:43], s[22:23]
v_pk_mul_f32 v[44:45], v[44:45], s[22:23]
v_pk_mul_f32 v[46:47], v[46:47], s[22:23]
v_pk_mul_f32 v[72:73], v[72:73], s[22:23]
v_pk_mul_f32 v[74:75], v[74:75], s[22:23]
v_pk_mul_f32 v[76:77], v[76:77], s[22:23]
v_pk_mul_f32 v[78:79], v[78:79], s[22:23]
v_pk_mul_f32 v[104:105], v[104:105], s[22:23]
v_pk_mul_f32 v[106:107], v[106:107], s[22:23]
v_pk_mul_f32 v[108:109], v[108:109], s[22:23]
v_pk_mul_f32 v[110:111], v[110:111], s[22:23]
v_pk_mul_f32 v[136:137], v[136:137], s[22:23]
v_pk_mul_f32 v[138:139], v[138:139], s[22:23]
v_pk_mul_f32 v[140:141], v[140:141], s[22:23]
v_pk_mul_f32 v[142:143], v[142:143], s[22:23]
v_pk_mul_f32 v[48:49], v[48:49], s[22:23]
v_pk_mul_f32 v[50:51], v[50:51], s[22:23]
v_pk_mul_f32 v[52:53], v[52:53], s[22:23]
v_pk_mul_f32 v[54:55], v[54:55], s[22:23]
v_pk_mul_f32 v[80:81], v[80:81], s[22:23]
v_pk_mul_f32 v[82:83], v[82:83], s[22:23]
v_pk_mul_f32 v[84:85], v[84:85], s[22:23]
v_pk_mul_f32 v[86:87], v[86:87], s[22:23]
v_pk_mul_f32 v[112:113], v[112:113], s[22:23]
v_pk_mul_f32 v[114:115], v[114:115], s[22:23]
v_pk_mul_f32 v[116:117], v[116:117], s[22:23]
v_pk_mul_f32 v[118:119], v[118:119], s[22:23]
v_pk_mul_f32 v[144:145], v[144:145], s[22:23]
v_pk_mul_f32 v[146:147], v[146:147], s[22:23]
v_pk_mul_f32 v[148:149], v[148:149], s[22:23]
v_pk_mul_f32 v[150:151], v[150:151], s[22:23]
v_pk_mul_f32 v[56:57], v[56:57], s[22:23]
v_pk_mul_f32 v[58:59], v[58:59], s[22:23]
v_pk_mul_f32 v[60:61], v[60:61], s[22:23]
v_pk_mul_f32 v[62:63], v[62:63], s[22:23]
v_pk_mul_f32 v[88:89], v[88:89], s[22:23]
v_pk_mul_f32 v[90:91], v[90:91], s[22:23]
v_pk_mul_f32 v[92:93], v[92:93], s[22:23]
v_pk_mul_f32 v[94:95], v[94:95], s[22:23]
v_pk_mul_f32 v[120:121], v[120:121], s[22:23]
v_pk_mul_f32 v[122:123], v[122:123], s[22:23]
v_pk_mul_f32 v[124:125], v[124:125], s[22:23]
v_pk_mul_f32 v[126:127], v[126:127], s[22:23]
v_pk_mul_f32 v[152:153], v[152:153], s[22:23]
v_pk_mul_f32 v[154:155], v[154:155], s[22:23]
v_pk_mul_f32 v[156:157], v[156:157], s[22:23]
v_pk_mul_f32 v[158:159], v[158:159], s[22:23]
K 32,32,33
K 33,34,35
K 34,36,37
K 35,38,39
K 40,40,41
K 41,42,43
K 42,44,45
K 43,46,47
W 0
Zvav 196,128,32
s_add_u32 s36, s29, 65536
Ra 144,242,4096
Ra 148,242,5120
Ra 152,242,6144
Ra 156,242,7168
Mvav 196,132,40
K 64,64,65
K 65,66,67
K 66,68,69
K 67,70,71
K 72,72,73
K 73,74,75
K 74,76,77
K 75,78,79
s_nop 1
Zvav 204,132,72
buffer_store_dwordx4 a[192:195], v1, s[8:11], s36 offen offset:0 sc1
K 96,96,97
K 97,98,99
K 98,100,101
Mvav 204,128,64
K 99,102,103
K 104,104,105
K 105,106,107
K 106,108,109
K 107,110,111
Zvav 212,128,96
buffer_store_dwordx4 a[200:203], v1, s[8:11], s36 offen offset:1024 sc1
K 128,128,129
K 129,130,131
K 130,132,133
Mvav 212,132,104
K 131,134,135
K 136,136,137
K 137,138,139
K 138,140,141
K 139,142,143
s_nop 1
Zvav 220,132,136
buffer_store_dwordx4 a[208:211], v1, s[8:11], s36 offen offset:2048 sc1
K 48,48,49
K 49,50,51
K 50,52,53
Mvav 220,128,128
K 51,54,55
K 56,56,57
K 57,58,59
K 58,60,61
K 59,62,63
Mvav 196,136,48
Ra 128,242,8192
Ra 132,242,9216
buffer_store_dwordx4 a[216:219], v1, s[8:11], s36 offen offset:3072 sc1
s_add_u32 s36, s29, 69632
K 80,80,81
Mvav 196,140,56
K 81,82,83
K 82,84,85
K 83,86,87
K 88,88,89
K 89,90,91
K 90,92,93
K 91,94,95
s_nop 1
Mvav 204,140,88
buffer_store_dwordx4 a[224:227], v1, s[8:11], s36 offen offset:0 sc1
K 112,112,113
K 113,114,115
K 114,116,117
Mvav 204,136,80
K 115,118,119
K 120,120,121
K 121,122,123
K 122,124,125
K 123,126,127
Mvav 212,136,112
K 144,144,145
K 145,146,147
K 146,148,149
K 147,150,151
Mvav 212,140,120
K 152,152,153
K 153,154,155
K 154,156,157
K 155,158,159
s_nop 1
Mvav 220,140,152
buffer_store_dwordx4 a[232:235], v1, s[8:11], s36 offen offset:1024 sc1
v_accvgpr_write_b32 a0, v32
v_accvgpr_write_b32 a1, v33
v_accvgpr_write_b32 a2, v34
Mvav 220,136,144
W 2
v_accvgpr_write_b32 a3, v35
v_accvgpr_write_b32 a4, v40
v_accvgpr_write_b32 a5, v41
v_accvgpr_write_b32 a6, v42
Zvav 68,144,64
buffer_store_dwordx4 a[240:243], v1, s[8:11], s36 offen offset:2048 sc1
v_accvgpr_write_b32 a7, v43
v_accvgpr_write_b32 a8, v48
v_accvgpr_write_b32 a9, v49
Mvav 68,148,72
Ra 136,242,10240
Ra 140,242,11264
v_accvgpr_write_b32 a10, v50
v_accvgpr_write_b32 a11, v51
Zvav 100,148,104
v_accvgpr_write_b32 a12, v56
v_accvgpr_write_b32 a13, v57
v_accvgpr_write_b32 a14, v58
v_accvgpr_write_b32 a15, v59
Mvav 100,144,96
buffer_store_dwordx4 a[248:251], v1, s[8:11], s36 offen offset:3072 sc1
s_add_u32 s36, s29, 393216
v_accvgpr_write_b32 a16, v64
v_cndmask_b32_e64 v196, v241, v196, s[72:73]
v_cndmask_b32_e64 v197, v241, v197, s[74:75]
Zvav 132,144,128
v_cndmask_b32_e64 v198, v241, v198, s[76:77]
v_cndmask_b32_e64 v199, v241, v199, s[78:79]
v_max3_f32 v4, v196, v197, v198
v_max3_f32 v4, v4, v199, v252
Mvav 132,148,136
v_max3_f32 v5, v204, v205, v206
v_max3_f32 v5, v5, v207, v253
v_max3_f32 v6, v212, v213, v214
v_max3_f32 v6, v6, v215, v254
Mvav 68,152,80
v_max3_f32 v7, v220, v221, v222
v_max3_f32 v7, v7, v223, v255
v_mov_b32 v8, v4
v_mov_b32 v9, v5
Mvav 68,156,88
v_mov_b32 v10, v6
v_mov_b32 v11, v7
v_permlane32_swap_b32 v4, v8
v_accvgpr_write_b32 a17, v65
Mvav 100,156,120
v_permlane32_swap_b32 v5, v9
v_permlane32_swap_b32 v6, v10
v_permlane32_swap_b32 v7, v11
v_max_f32 v4, v4, v8
Mvav 100,152,112
v_max_f32 v5, v5, v9
v_max_f32 v6, v6, v10
v_max_f32 v7, v7, v11
v_mov_b32 v8, v4
Mvav 132,152,144
v_mov_b32 v9, v5
v_mov_b32 v10, v6
v_mov_b32 v11, v7
v_permlane16_swap_b32 v4, v8
v_permlane16_swap_b32 v5, v9
v_permlane16_swap_b32 v6, v10
v_permlane16_swap_b32 v7, v11
v_max_f32 v4, v4, v8
v_max_f32 v5, v5, v9
v_max_f32 v6, v6, v10
v_max_f32 v7, v7, v11
v_sub_f32 v4, 0, v4
v_sub_f32 v5, 0, v5
v_sub_f32 v6, 0, v6
v_sub_f32 v7, 0, v7
v_mov_b32 v224, v4
v_mov_b32 v225, v4
v_mov_b32 v226, v4
v_mov_b32 v227, v4
v_mov_b32 v228, v5
v_mov_b32 v229, v5
v_mov_b32 v230, v5
v_mov_b32 v231, v5
v_mov_b32 v232, v6
v_mov_b32 v233, v6
v_mov_b32 v234, v6
v_mov_b32 v235, v6
v_mov_b32 v236, v7
v_mov_b32 v237, v7
v_mov_b32 v238, v7
v_mov_b32 v239, v7
Mvav 132,156,152
W 0
v_accvgpr_write_b32 a18, v66
v_accvgpr_write_b32 a19, v67
v_accvgpr_write_b32 a20, v72
v_accvgpr_write_b32 a21, v73
Cvav 168,128,96,232
s_and_b32 s80, s14, 1
buffer_store_dwordx4 a[196:199], v1, s[8:11], s36 offen offset:0 sc1
v_accvgpr_write_b32 a22, v74
v_accvgpr_write_b32 a23, v75
v_accvgpr_write_b32 a24, v80
Mvav 168,132,104
s_lshl_b32 s80, s80, 14
Ra 144,242,12288
v_accvgpr_write_b32 a25, v81
v_accvgpr_write_b32 a26, v82
v_accvgpr_write_b32 a27, v83
Cvav 172,132,136,236
Ra 148,242,13312
Ra 152,242,14336
Ra 156,242,15360
v_accvgpr_write_b32 a28, v88
Mvav 172,128,128
s_add_u32 s80, s80, 98304
v_accvgpr_write_b32 a29, v89
v_accvgpr_write_b32 a30, v90
v_accvgpr_write_b32 a31, v91
v_accvgpr_write_b32 a32, v96
Mvav 168,136,112
buffer_store_dwordx4 a[204:207], v1, s[8:11], s36 offen offset:1024 sc1
v_accvgpr_write_b32 a33, v97
v_accvgpr_write_b32 a34, v98
v_accvgpr_write_b32 a35, v99
Mvav 168,140,120
v_accvgpr_write_b32 a36, v104
v_accvgpr_write_b32 a37, v105
v_accvgpr_write_b32 a38, v106
v_accvgpr_write_b32 a39, v107
Mvav 172,140,152
Ra 128,242,24576
Ra 132,242,25600
buffer_store_dwordx4 a[212:215], v1, s[8:11], s36 offen offset:2048 sc1
v_accvgpr_write_b32 a40, v112
Mvav 172,136,144
Ra 136,242,26624
Ra 140,242,27648
v_accvgpr_write_b32 a41, v113
W 4
Cvav 188,144,128,236
v_accvgpr_write_b32 a42, v114
v_accvgpr_write_b32 a43, v115
v_accvgpr_write_b32 a44, v120
v_accvgpr_write_b32 a45, v121
v_accvgpr_write_b32 a46, v122
v_accvgpr_write_b32 a47, v123
v_accvgpr_write_b32 a48, v128
v_accvgpr_write_b32 a49, v129
v_accvgpr_write_b32 a50, v130
v_accvgpr_write_b32 a51, v131
v_accvgpr_write_b32 a52, v136
)"
R"(v_accvgpr_write_b32 a53, v137
v_accvgpr_write_b32 a54, v138
v_accvgpr_write_b32 a55, v139
v_accvgpr_write_b32 a56, v144
v_accvgpr_write_b32 a57, v145
v_accvgpr_write_b32 a58, v146
v_accvgpr_write_b32 a59, v147
v_accvgpr_write_b32 a60, v152
v_accvgpr_write_b32 a61, v153
v_accvgpr_write_b32 a62, v154
v_accvgpr_write_b32 a63, v155
Mvav 188,148,136
s_barrier
buffer_store_dwordx4 a[220:223], v1, s[8:11], s36 offen offset:3072 sc1
s_add_u32 s36, s29, 397312
buffer_store_dwordx4 a[228:231], v1, s[8:11], s36 offen offset:0 sc1
buffer_store_dwordx4 a[236:239], v1, s[8:11], s36 offen offset:1024 sc1
buffer_store_dwordx4 a[244:247], v1, s[8:11], s36 offen offset:2048 sc1
buffer_store_dwordx4 a[252:255], v1, s[8:11], s36 offen offset:3072 sc1
v_add_u32 v8, s80, v1
Mvav 188,152,144
A 196,196,224
A 197,197,224
A 198,198,224
A 199,199,224
X 196
X 197
X 198
K 192,196,197
X 199
A 204,204,228
K 193,198,199
A 205,205,228
A 206,206,228
A 207,207,228
X 204
X 205
X 206
K 200,204,205
X 207
A 212,212,232
K 201,206,207
A 213,213,232
A 214,214,232
A 215,215,232
X 212
X 213
X 214
K 208,212,213
X 215
A 220,220,236
K 209,214,215
A 221,221,236
A 222,222,236
A 223,223,236
X 220
X 221
X 222
K 216,220,221
X 223
v_mov_b32 v194, 0
K 217,222,223
v_mov_b32 v195, 0
v_cndmask_b32_e64 v68, v241, v68, s[72:73]
v_cndmask_b32_e64 v69, v241, v69, s[74:75]
v_cndmask_b32_e64 v70, v241, v70, s[76:77]
v_cndmask_b32_e64 v71, v241, v71, s[78:79]
A 68,68,228
A 69,69,228
A 70,70,228
A 71,71,228
X 68
X 69
X 70
K 202,68,69
X 71
A 100,100,232
K 203,70,71
A 101,101,232
A 102,102,232
A 103,103,232
X 100
X 101
X 102
K 210,100,101
X 103
A 132,132,236
K 211,102,103
A 133,133,236
A 134,134,236
A 135,135,236
X 132
X 133
X 134
K 218,132,133
X 135
s_nop 0
K 219,134,135
Mvav 188,156,152
W 0
Ra 144,242,28672
Ra 148,242,29696
Ra 152,242,30720
Zvav 32,128,192
Ra 156,242,31744
Zvav 64,128,200
Zvav 96,128,208
s_and_b32 s80, s14, 1
s_xor_b32 s62, s62, 0x10000
Zvav 128,128,216
s_lshl_b32 s80, s80, 14
s_add_u32 s80, s80, 131072
v_add_u32 v23, 61440, v21
Zvav 132,132,216
v_add_u32 v12, s80, v1
Ra 64,12,8192
Ra 68,12,9216
v_add_u32 v24, 61440, v22
Zvav 100,132,208
Ra 72,12,10240
Ra 76,12,11264
Ra 80,12,12288
v_add_u32 v25, 122880, v21
Zvav 68,132,200
Ra 84,12,13312
Ra 88,12,14336
Ra 92,12,15360
v_add_u32 v26, 122880, v22
Zvav 36,132,192
Ra 96,12,0
Ra 100,12,1024
Ra 104,12,2048
Zvav 40,136,192
Ra 108,12,3072
Ra 112,12,4096
Ra 116,12,5120
Zvav 72,136,200
Ra 120,12,6144
Ra 124,12,7168
Zvav 104,136,208
Zvav 136,136,216
Zvav 140,140,216
Zvav 108,140,208
Zvav 76,140,200
s_waitcnt vmcnt(0) lgkmcnt(0)
s_barrier
v_xor_b32 v242, 0x10000, v242
ds_read_b128 v[248:251], v8
Zvav 44,140,192
s_add_u32 s80, s71, 128
v_mov_b64 v[28:29], s[12:13]
s_mov_b64 exec, 1
buffer_store_dwordx2 v[28:29], off, s[8:11], s80 sc1
s_mov_b64 exec, -1
Ra 128,242,40960
Zvav 48,144,192
Zvav 80,144,200
X 172
X 173
Zvav 112,144,208
Ra 132,242,41984
X 174
K 220,172,173
Zvav 144,144,216
X 175
v_cndmask_b32_e64 v168, v241, v168, s[72:73]
K 221,174,175
Zvav 148,148,216
v_cndmask_b32_e64 v169, v241, v169, s[74:75]
v_cndmask_b32_e64 v170, v241, v170, s[76:77]
v_cndmask_b32_e64 v171, v241, v171, s[78:79]
Zvav 116,148,208
X 168
X 169
Zvav 84,148,200
W 2
Ra 136,242,43008
X 170
K 212,168,169
Zvav 52,148,192
v_mov_b32 v224, v250
v_mov_b32 v225, v250
v_mov_b32 v226, v250
v_mov_b32 v227, v250
Zvav 56,152,192
v_mov_b32 v228, v251
v_mov_b32 v229, v251
v_mov_b32 v230, v251
v_mov_b32 v231, v251
Zvav 88,152,200
v_mov_b32 v232, v248
v_mov_b32 v233, v248
v_mov_b32 v234, v248
v_mov_b32 v235, v248
Zvav 120,152,208
v_mov_b32 v236, v249
v_mov_b32 v237, v249
v_mov_b32 v238, v249
v_mov_b32 v239, v249
Zvav 152,152,216
X 171
v_mov_b32 v204, 0
K 213,170,171
Zvav 156,156,216
Ra 140,242,44032
v_mov_b32 v205, 0
v_mov_b32 v196, 0
v_mov_b32 v197, 0
Zvav 124,156,208
v_mov_b32 v198, 0
v_mov_b32 v199, 0
v_mov_b32 v206, 0
v_mov_b32 v207, 0
Zvav 92,156,200
v_mov_b32 v214, 0
v_mov_b32 v215, 0
v_cndmask_b32_e64 v188, v241, v188, s[72:73]
v_cndmask_b32_e64 v189, v241, v189, s[74:75]
Zvav 60,156,192
v_cndmask_b32_e64 v190, v241, v190, s[76:77]
v_cndmask_b32_e64 v191, v241, v191, s[78:79]
X 188
X 189
X 190
K 222,188,189
X 191
s_nop 0
K 223,190,191
s_nop 1
s_branch .Lsp2_35%=
.Lmoe_42%=:
W 0
Cvaa 160,128,0,224
s_cmp_eq_u32 s14, 0
s_cselect_b32 s20, s30, s32
s_cselect_b32 s21, s31, s33
Ra 144,242,4096
Ra 148,242,5120
Mvaa 160,132,4
s_xor_b32 s62, s62, 0x10000
Ra 152,242,6144
Cvaa 164,132,20,228
X 184
Mvaa 164,128,16
s_and_b32 s21, s21, 0xffff
X 185
Cvaa 168,128,32,232
X 186
Mvaa 168,132,36
Ra 156,242,7168
K 214,184,185
Cvaa 172,132,52,236
s_mov_b32 s22, 2211840
X 187
Mvaa 172,128,48
X 188
Mvaa 160,136,8
Ra 128,242,16384
Mvaa 160,140,12
s_mov_b32 s23, 0x20000
K 215,186,187
Mvaa 164,140,28
X 189
Mvaa 164,136,24
X 190
Mvaa 168,136,40
Ra 132,242,17408
v_lshrrev_b32 v4, 6, v1
Mvaa 168,140,44
K 222,188,189
Mvaa 172,140,60
X 191
Mvaa 172,136,56
v_mul_u32_u24 v4, 3456, v4
K 223,190,191
v_smfmac_f32_16x16x64_bf16 a[160:163], a[176:179], v[192:199], v240
Ra 136,242,18432
v_smfmac_f32_16x16x64_bf16 a[164:167], a[176:179], v[200:207], v240
X 160
v_smfmac_f32_16x16x64_bf16 a[168:171], a[176:179], v[208:215], v240
v_lshrrev_b32 v5, 4, v1
v_smfmac_f32_16x16x64_bf16 a[172:175], a[176:179], v[216:223], v240
W 3
X 161
Cvaa 176,144,0,224
X 162
Mvaa 176,148,4
Ra 140,242,19456
v_xor_b32 v5, v5, v1
Cvaa 180,148,20,228
K 192,160,161
Mvaa 180,144,16
X 163
Cvaa 184,144,32,232
v_and_b32 v5, 32, v5
Mvaa 184,148,36
X 164
Cvaa 188,148,52,236
K 193,162,163
v_and_b32 v6, 16, v1
Mvaa 188,144,48
X 165
Mvaa 176,152,8
X 166
Mvaa 176,156,12
Ra 144,242,20480
Ra 148,242,21504
Mvaa 180,156,28
v_add3_u32 v4, v4, v5, v6
K 200,164,165
Mvaa 180,152,24
X 167
Mvaa 184,152,40
X 168
Mvaa 184,156,44
v_add_u32 v5, 54272, v4
K 201,166,167
Mvaa 188,156,60
X 169
Mvaa 188,152,56
W 2
Ra 152,242,22528
v_add_u32 v6, 108544, v4
Mvav 32,128,196
X 170
Mvav 64,128,204
Ra 156,242,23552
v_add_u32 v7, 162816, v4
Mvav 96,128,212
K 208,168,169
Mvav 128,128,220
X 171
Mvav 132,132,220
s_lshl_b32 s94, s85, 6
X 172
Mvav 100,132,212
s_add_u32 s64, s64, 1
Ra 128,242,8192
K 209,170,171
Mvav 68,132,204
X 173
Mvav 36,132,196
s_mul_i32 s95, s14, 20480
Mvav 40,136,196
X 174
Mvav 72,136,204
Ra 132,242,9216
K 216,172,173
Mvav 104,136,212
s_add_u32 s95, s95, 98304
X 175
Mvav 136,136,220
Mvav 140,140,220
s_add_u32 m0, s95, 0
X 176
Mvav 108,140,212
Ra 136,242,10240
K 217,174,175
Mvav 76,140,204
X 177
Mvav 44,140,196
W 3
D 4,20,94,0
Mvav 48,144,196
X 178
Mvav 80,144,204
Ra 140,242,11264
Mvav 112,144,212
D 5,20,94,1024
K 194,176,177
Mvav 144,144,220
X 179
Mvav 148,148,220
X 180
Mvav 116,148,212
Ra 144,242,12288
D 6,20,94,2048
Mvav 84,148,204
K 195,178,179
Mvav 52,148,196
X 181
Mvav 56,152,196
D 7,20,94,3072
Ra 148,242,13312
Mvav 88,152,204
X 182
Mvav 120,152,212
s_add_u32 s94, s94, 221184
K 202,180,181
Mvav 152,152,220
X 183
Mvav 156,156,220
X 184
Mvav 124,156,212
s_add_u32 m0, s95, 4096
Ra 152,242,14336
Mvav 92,156,204
K 203,182,183
Mvav 60,156,196
W 3
)"
R"(X 185
Cvaa 160,128,0,224
D 4,20,94,0
Ra 156,242,15360
Mvaa 160,132,4
X 186
Cvaa 164,132,20,228
K 210,184,185
Mvaa 164,128,16
X 187
Cvaa 168,128,32,232
D 5,20,94,1024
Mvaa 168,132,36
X 188
Cvaa 172,132,52,236
D 6,20,94,2048
K 211,186,187
Mvaa 172,128,48
X 189
Mvaa 160,136,8
Ra 128,242,24576
Ra 132,242,25600
Mvaa 160,140,12
X 190
Mvaa 164,140,28
D 7,20,94,3072
K 218,188,189
Mvaa 164,136,24
s_add_u32 s94, s94, 221184
X 191
Mvaa 168,136,40
Mvaa 168,140,44
X 160
Mvaa 172,140,60
s_add_u32 m0, s95, 8192
K 219,190,191
Mvaa 172,136,56
W 2
X 161
Cvaa 176,144,0,224
Ra 136,242,26624
Ra 140,242,27648
Mvaa 176,148,4
X 162
Cvaa 180,148,20,228
D 4,20,94,0
K 196,160,161
Mvaa 180,144,16
X 163
Cvaa 184,144,32,232
D 5,20,94,1024
Mvaa 184,148,36
X 164
Cvaa 188,148,52,236
K 197,162,163
Mvaa 188,144,48
D 6,20,94,2048
Ra 144,242,28672
Mvaa 176,152,8
X 165
Mvaa 176,156,12
Ra 148,242,29696
D 7,20,94,3072
Mvaa 180,156,28
X 166
Mvaa 180,152,24
K 204,164,165
Mvaa 184,152,40
s_add_u32 s94, s94, 221184
X 167
Mvaa 184,156,44
X 168
Mvaa 188,156,60
Mvaa 188,152,56
W 2
s_add_u32 m0, s95, 12288
K 205,166,167
Mvav 32,128,192
X 169
Mvav 64,128,200
Ra 152,242,30720
Ra 156,242,31744
Mvav 96,128,208
X 170
Mvav 128,128,216
D 4,20,94,0
K 212,168,169
Mvav 132,132,216
X 171
Mvav 100,132,208
D 5,20,94,1024
Mvav 68,132,200
X 172
Mvav 36,132,192
K 213,170,171
Mvav 40,136,192
X 173
Mvav 72,136,200
D 6,20,94,2048
Mvav 104,136,208
X 174
Mvav 136,136,216
D 7,20,94,3072
K 220,172,173
Mvav 140,140,216
X 175
Mvav 108,140,208
s_add_u32 s94, s94, 221184
X 176
Mvav 76,140,200
W 0
s_barrier
v_xor_b32 v242, 0x10000, v242
Mvav 44,140,192
Ra 128,242,0
K 221,174,175
Mvav 48,144,192
X 177
Mvav 80,144,200
v_bfe_u32 v11, v1, 4, 3
Mvav 112,144,208
X 178
Mvav 144,144,216
Ra 132,242,1024
v_lshlrev_b32 v11, 3, v11
Mvav 148,148,216
K 198,176,177
Mvav 116,148,208
X 179
Mvav 84,148,200
X 180
Mvav 52,148,192
Ra 136,242,2048
buffer_load_dwordx2 v[8:9], v11, s[8:11], 0 offen offset:3840 sc1
Mvav 56,152,192
Ra 140,242,3072
K 199,178,179
Mvav 88,152,200
X 181
Mvav 120,152,208
X 182
Mvav 152,152,216
K 206,180,181
Mvav 156,156,216
X 183
Mvav 124,156,208
Mvav 92,156,200
K 207,182,183
Mvav 60,156,192
W 0
Cvaa 160,128,0,224
s_add_u32 m0, s95, 16384
Ra 144,242,4096
Ra 148,242,5120
Mvaa 160,132,4
s_xor_b32 s62, s62, 0x10000
Ra 152,242,6144
Cvaa 164,132,20,228
X 184
Mvaa 164,128,16
X 185
Cvaa 168,128,32,232
X 186
Mvaa 168,132,36
D 4,20,94,0
K 214,184,185
Cvaa 172,132,52,236
Ra 156,242,7168
Mvaa 172,128,48
X 187
Mvaa 160,136,8
X 188
Mvaa 160,140,12
D 5,20,94,1024
Ra 128,242,16384
Mvaa 164,140,28
K 215,186,187
Mvaa 164,136,24
X 189
Mvaa 168,136,40
X 190
Mvaa 168,140,44
D 6,20,94,2048
Ra 132,242,17408
Mvaa 172,140,60
K 222,188,189
Mvaa 172,136,56
X 191
v_smfmac_f32_16x16x64_bf16 a[160:163], a[176:179], v[192:199], v240
Ra 136,242,18432
K 223,190,191
v_smfmac_f32_16x16x64_bf16 a[164:167], a[176:179], v[200:207], v240
D 7,20,94,3072
v_smfmac_f32_16x16x64_bf16 a[168:171], a[176:179], v[208:215], v240
s_add_u32 s94, s94, 221184
v_mul_u32_u24 v10, 160, v11
v_smfmac_f32_16x16x64_bf16 a[172:175], a[176:179], v[216:223], v240
W 3
X 160
Cvaa 176,144,0,224
X 161
Mvaa 176,148,4
Ra 140,242,19456
v_lshrrev_b32 v12, 7, v1
Cvaa 180,148,20,228
X 162
Mvaa 180,144,16
K 192,160,161
v_lshl_add_u32 v10, v12, 4, v10
Cvaa 184,144,32,232
s_mul_i32 s82, s14, 5120
X 163
Mvaa 184,148,36
X 164
Cvaa 188,148,52,236
K 193,162,163
Mvaa 188,144,48
s_mul_i32 s81, s14, 640
X 165
Mvaa 176,152,8
X 166
Mvaa 176,156,12
s_add_u32 s81, s81, 118784
Ra 144,242,20480
Ra 148,242,21504
Mvaa 180,156,28
K 200,164,165
Mvaa 180,152,24
X 167
Mvaa 184,152,40
s_waitcnt vmcnt(4)
v_cmp_eq_u64 vcc, s[12:13], v[8:9]
s_cmp_eq_u64 vcc, -1
s_cbranch_scc0 .Lspin_49%=
.Lok_50%=:
X 168
Mvaa 184,156,44
K 201,166,167
Mvaa 188,156,60
s_add_u32 m0, s82, 53248
X 169
Mvaa 188,152,56
W 2
Ra 152,242,22528
Mvav 32,128,196
X 170
Mvav 64,128,204
Ra 156,242,23552
Dc 10,8,81,0
Mvav 96,128,212
K 208,168,169
Mvav 128,128,220
X 171
Mvav 132,132,220
s_add_u32 m0, s82, 54144
X 172
Mvav 100,132,212
s_add_u32 s64, s64, 1
Ra 128,242,8192
K 209,170,171
Mvav 68,132,204
X 173
Mvav 36,132,196
Dc 10,8,81,128
Mvav 40,136,196
X 174
Mvav 72,136,204
s_add_u32 m0, s82, 55040
Ra 132,242,9216
K 216,172,173
Mvav 104,136,212
X 175
Mvav 136,136,220
Dc 10,8,81,256
Mvav 140,140,220
X 176
Mvav 108,140,212
Ra 136,242,10240
K 217,174,175
Mvav 76,140,204
s_add_u32 m0, s82, 55936
X 177
Mvav 44,140,196
W 3
Mvav 48,144,196
X 178
Mvav 80,144,204
Ra 140,242,11264
Dc 10,8,81,384
Mvav 112,144,212
K 194,176,177
Mvav 144,144,220
s_add_u32 m0, s82, 56832
X 179
Mvav 148,148,220
X 180
Mvav 116,148,212
Ra 144,242,12288
Mvav 84,148,204
Dc 10,8,81,512
K 195,178,179
Mvav 52,148,196
X 181
Mvav 56,152,196
X 182
Mvav 88,152,204
Ra 148,242,13312
Mvav 120,152,212
K 202,180,181
Mvav 152,152,220
X 183
Mvav 156,156,220
X 184
Mvav 124,156,212
Ra 152,242,14336
Mvav 92,156,204
K 203,182,183
Mvav 60,156,196
W 3
X 185
Cvaa 160,128,0,224
X 186
Mvaa 160,132,4
Ra 156,242,15360
Cvaa 164,132,20,228
K 210,184,185
Mvaa 164,128,16
X 187
Cvaa 168,128,32,232
X 188
Mvaa 168,132,36
Cvaa 172,132,52,236
K 211,186,187
Mvaa 172,128,48
X 189
Mvaa 160,136,8
Ra 128,242,24576
Ra 132,242,25600
Mvaa 160,140,12
X 190
Mvaa 164,140,28
K 218,188,189
Mvaa 164,136,24
X 191
Mvaa 168,136,40
Mvaa 168,140,44
Mvaa 172,140,60
X 160
Mvaa 172,136,56
W 2
Ra 136,242,26624
K 219,190,191
Cvaa 176,144,0,224
X 161
Mvaa 176,148,4
Ra 140,242,27648
v_lshrrev_b32 v8, 8, v1
Cvaa 180,148,20,228
X 162
Mvaa 180,144,16
K 196,160,161
Cvaa 184,144,32,232
X 163
Mvaa 184,148,36
v_bfe_u32 v9, v1, 6, 2
Cvaa 188,148,52,236
X 164
Mvaa 188,144,48
K 197,162,163
v_lshl_add_u32 v9, v8, 3, v9
Mvaa 176,152,8
X 165
Mvaa 176,156,12
Ra 144,242,28672
Ra 148,242,29696
Mvaa 180,156,28
X 166
Mvaa 180,152,24
K 204,164,165
v_lshlrev_b32 v11, 6, v9
Mvaa 184,152,40
X 167
Mvaa 184,156,44
X 168
Mvaa 188,156,60
v_xor_b32 v9, s14, v8
Mvaa 188,152,56
W 2
Ra 152,242,30720
K 205,166,167
Mvav 32,128,192
X 169
Mvav 64,128,200
Ra 156,242,31744
v_and_b32 v9, 1, v9
Mvav 96,128,208
X 170
Mvav 128,128,216
K 212,168,169
Mvav 132,132,216
)"
R"(X 171
Mvav 100,132,208
v_lshl_add_u32 v11, v9, 5, v11
Mvav 68,132,200
X 172
Mvav 36,132,192
K 213,170,171
Mvav 40,136,192
X 173
Mvav 72,136,200
v_bfe_u32 v9, v1, 4, 2
Mvav 104,136,208
X 174
Mvav 136,136,216
K 220,172,173
v_lshl_add_u32 v12, v9, 3, v11
Mvav 140,140,216
X 175
Mvav 108,140,208
X 176
Mvav 76,140,200
s_waitcnt vmcnt(0) lgkmcnt(0)
s_barrier
v_xor_b32 v242, 0x10000, v242
Mvav 44,140,192
v_add_u32 v11, 98304, v12
K 221,174,175
Mvav 48,144,192
Ra 128,242,0
Mvav 80,144,200
X 177
Mvav 112,144,208
X 178
Mvav 144,144,216
Ra 132,242,1024
v_and_b32 v13, 0x70, v1
Mvav 148,148,216
K 198,176,177
Mvav 116,148,208
X 179
Mvav 84,148,200
Ra 136,242,2048
v_lshl_add_u32 v13, v8, 7, v13
Mvav 52,148,192
X 180
Mvav 56,152,192
Ra 140,242,3072
K 199,178,179
Mvav 88,152,200
X 181
Mvav 120,152,208
X 182
Mvav 152,152,216
K 206,180,181
Mvav 156,156,216
X 183
Mvav 124,156,208
Mvav 92,156,200
K 207,182,183
Mvav 60,156,192
W 0
Cvaa 160,128,0,224
s_cmp_eq_u32 s14, 0
s_cselect_b32 s95, 106496, 0
Ra 144,242,4096
Ra 148,242,5120
Mvaa 160,132,4
s_xor_b32 s62, s62, 0x10000
X 184
Cvaa 164,132,20,228
Ta 184,11,0
Ta 186,11,256
Mvaa 164,128,16
s_add_u32 s95, s95, 32768
Ta 188,11,20480
Ta 190,11,20736
Cvaa 168,128,32,232
Ra 192,13,53248
Ta 196,11,2048
Mvaa 168,132,36
s_add_u32 m0, s95, 0
X 185
Cvaa 172,132,52,236
Ta 198,11,2304
Ta 200,11,22528
Mvaa 172,128,48
W 3
X 186
Zaaa 232,184,192
Ta 202,11,22784
K 214,184,185
Zaaa 236,188,192
Ra 204,13,53760
Ra 152,242,6144
Mvaa 160,136,8
X 187
Mvaa 160,140,12
Ta 208,11,4096
D 4,20,94,0
Mvaa 164,140,28
X 188
Mvaa 164,136,24
Ta 210,11,4352
D 5,20,94,1024
Mvaa 168,136,40
Ra 156,242,7168
K 215,186,187
Mvaa 168,140,44
X 189
Mvaa 172,140,60
Ta 212,11,24576
D 6,20,94,2048
Mvaa 172,136,56
X 190
v_smfmac_f32_16x16x64_bf16 a[160:163], a[176:179], v[192:199], v240
W 5
Ta 214,11,24832
D 7,20,94,3072
Maaa 232,196,204
s_add_u32 s94, s94, 221184
Ra 216,13,54272
K 222,188,189
Maaa 236,200,204
s_add_u32 m0, s95, 4096
X 191
v_smfmac_f32_16x16x64_bf16 a[164:167], a[176:179], v[200:207], v240
X 160
K 223,190,191
v_smfmac_f32_16x16x64_bf16 a[168:171], a[176:179], v[208:215], v240
Ra 128,242,16384
Ta 220,11,6144
v_smfmac_f32_16x16x64_bf16 a[172:175], a[176:179], v[216:223], v240
W 5
Ta 222,11,6400
Ra 132,242,17408
Cvaa 176,144,0,224
X 161
Mvaa 176,148,4
D 4,20,94,0
Ta 224,11,26624
Cvaa 180,148,20,228
X 162
Mvaa 180,144,16
D 5,20,94,1024
K 192,160,161
Cvaa 184,144,32,232
Ta 226,11,26880
Ra 228,13,54784
Mvaa 184,148,36
W 7
X 163
Maaa 232,208,216
D 6,20,94,2048
Ra 136,242,18432
Maaa 236,212,216
X 164
Cvaa 188,148,52,236
D 7,20,94,3072
Ta 184,11,8192
Mvaa 188,144,48
s_add_u32 s94, s94, 221184
Ra 140,242,19456
K 193,162,163
Mvaa 176,152,8
s_add_u32 m0, s95, 8192
X 165
Mvaa 176,156,12
Ta 186,11,8448
Ta 188,11,28672
Mvaa 180,156,28
X 166
Mvaa 180,152,24
Ra 144,242,20480
K 200,164,165
Mvaa 184,152,40
X 167
Mvaa 184,156,44
Ta 190,11,28928
D 4,20,94,0
Mvaa 188,156,60
W 7
X 168
Maaa 232,220,228
s_add_u32 s64, s64, 1
Ra 192,13,55296
D 5,20,94,1024
Maaa 236,224,228
Ta 196,11,10240
K 201,166,167
Mvaa 188,152,56
W 6
X 169
Mvav 32,128,196
D 6,20,94,2048
Ra 148,242,21504
Mvav 64,128,204
X 170
Mvav 96,128,212
Ta 198,11,10496
D 7,20,94,3072
Mvav 128,128,220
s_add_u32 s94, s94, 221184
Ta 200,11,30720
K 208,168,169
Mvav 132,132,220
s_add_u32 m0, s95, 12288
X 171
Mvav 100,132,212
Ra 152,242,22528
Ta 202,11,30976
Mvav 68,132,204
X 172
Mvav 36,132,196
W 6
Ra 204,13,55808
K 209,170,171
Maaa 232,184,192
X 173
Maaa 236,188,192
D 4,20,94,0
Ra 156,242,23552
Mvav 40,136,196
X 174
Mvav 72,136,204
Ta 208,11,12288
D 5,20,94,1024
Mvav 104,136,212
Ta 210,11,12544
K 216,172,173
Mvav 136,136,220
X 175
Mvav 140,140,220
D 6,20,94,2048
Ra 128,242,8192
Mvav 108,140,212
Ta 212,11,32768
Ta 214,11,33024
Mvav 76,140,204
X 176
Mvav 44,140,196
W 5
D 7,20,94,3072
K 217,174,175
Mvav 48,144,196
s_add_u32 s94, s94, 221184
X 177
Maaa 232,196,204
s_add_u32 m0, s95, 16384
Ra 132,242,9216
Ra 216,13,56320
Maaa 236,200,204
X 178
Mvav 80,144,204
Ta 220,11,14336
K 194,176,177
Mvav 112,144,212
X 179
Mvav 144,144,220
D 4,20,94,0
Ta 222,11,14592
Mvav 148,148,220
Ra 136,242,10240
Ta 224,11,34816
Mvav 116,148,212
X 180
Mvav 84,148,204
D 5,20,94,1024
K 195,178,179
Mvav 52,148,196
X 181
Mvav 56,152,196
D 6,20,94,2048
Ta 226,11,35072
Mvav 88,152,204
W 5
Ra 140,242,11264
Ra 228,13,56832
Maaa 232,208,216
X 182
Maaa 236,212,216
D 7,20,94,3072
K 202,180,181
Mvav 120,152,212
s_add_u32 s94, s94, 221184
X 183
Mvav 152,152,220
Ta 184,11,16384
Ra 144,242,12288
Mvav 156,156,220
X 184
Mvav 124,156,212
Ta 186,11,16640
K 203,182,183
Mvav 92,156,204
X 185
Mvav 60,156,196
W 4
Ta 188,11,36864
Ra 148,242,13312
Cvaa 160,128,0,224
X 186
Mvaa 160,132,4
Ta 190,11,37120
K 210,184,185
Cvaa 164,132,20,228
W 6
Ra 192,13,57344
Ta 196,11,18432
Maaa 232,220,228
X 187
Maaa 236,224,228
X 188
Mvaa 164,128,16
Ra 152,242,14336
K 211,186,187
Cvaa 168,128,32,232
Ta 198,11,18688
Ta 200,11,38912
Mvaa 168,132,36
X 189
Cvaa 172,132,52,236
X 190
Mvaa 172,128,48
Ra 156,242,15360
K 218,188,189
Mvaa 160,136,8
Ta 202,11,39168
Ra 204,13,57856
Mvaa 160,140,12
X 191
Mvaa 164,140,28
Ra 128,242,24576
Mvaa 164,136,24
W 8
Maaa 232,184,192
Ra 132,242,25600
Maaa 236,188,192
X 160
Mvaa 168,136,40
K 219,190,191
Mvaa 168,140,44
X 161
Mvaa 172,140,60
X 162
Mvaa 172,136,56
W 4
Ra 136,242,26624
K 196,160,161
Cvaa 176,144,0,224
X 163
Mvaa 176,148,4
X 164
Cvaa 180,148,20,228
Ra 140,242,27648
K 197,162,163
Mvaa 180,144,16
X 165
Cvaa 184,144,32,232
W 4
Maaa 232,196,204
X 166
Maaa 236,200,204
Mvaa 184,148,36
K 204,164,165
Cvaa 188,148,52,236
X 167
Mvaa 188,144,48
Ra 144,242,28672
Mvaa 176,152,8
X 168
Mvaa 176,156,12
Ra 148,242,29696
Mvaa 180,156,28
K 205,166,167
Mvaa 180,152,24
X 169
Mvaa 184,152,40
Mvaa 184,156,44
X 170
Mvaa 188,156,60
K 212,168,169
Mvaa 188,152,56
W 2
X 171
Mvav 32,128,192
Ra 152,242,30720
Ra 156,242,31744
Mvav 64,128,200
X 172
Mvav 96,128,208
Mvav 128,128,216
K 213,170,171
Mvav 132,132,216
X 173
Mvav 100,132,208
Mvav 68,132,200
X 174
Mvav 36,132,192
Mvav 40,136,192
K 220,172,173
)"
R"(Mvav 72,136,200
X 175
Mvav 104,136,208
Mvav 136,136,216
X 176
Mvav 140,140,216
Mvav 108,140,208
K 221,174,175
Mvav 76,140,200
s_waitcnt vmcnt(0) lgkmcnt(0)
s_barrier
v_xor_b32 v242, 0x10000, v242
Mvav 44,140,192
X 177
Mvav 48,144,192
X 178
Mvav 80,144,200
Ra 128,242,0
K 198,176,177
Mvav 112,144,208
X 179
Mvav 144,144,216
Ra 132,242,1024
Mvav 148,148,216
X 180
Mvav 116,148,208
Ra 136,242,2048
Mvav 84,148,200
K 199,178,179
Mvav 52,148,192
X 181
Mvav 56,152,192
Ra 140,242,3072
Mvav 88,152,200
X 182
Mvav 120,152,208
Mvav 152,152,216
K 206,180,181
Mvav 156,156,216
X 183
Mvav 124,156,208
Mvav 92,156,200
K 207,182,183
Mvav 60,156,192
W 0
Cvaa 160,128,0,224
s_mov_b32 s20, s34
Ra 144,242,4096
Ra 148,242,5120
Mvaa 160,132,4
s_xor_b32 s62, s62, 0x10000
X 184
Cvaa 164,132,20,228
s_and_b32 s21, s35, 0xffff
Ta 184,11,40960
Ta 186,11,41216
Mvaa 164,128,16
Ta 188,12,32768
Ta 190,12,33024
Cvaa 168,128,32,232
s_mov_b32 s22, 2211840
Ra 192,13,58368
Ta 196,11,43008
Mvaa 168,132,36
s_mov_b32 s23, 0x20000
X 185
Cvaa 172,132,52,236
Ta 198,11,43264
Ta 200,12,34816
Mvaa 172,128,48
W 3
X 186
Maaa 232,184,192
Ta 202,12,35072
K 214,184,185
Maaa 236,188,192
Ra 204,13,58880
Ra 152,242,6144
Mvaa 160,136,8
X 187
Mvaa 160,140,12
Ta 208,11,45056
v_lshrrev_b32 v4, 8, v1
Mvaa 164,140,28
X 188
Mvaa 164,136,24
Ta 210,11,45312
v_lshlrev_b32 v5, 5, v4
Mvaa 168,136,40
v_xor_b32 v5, v5, v1
K 215,186,187
Mvaa 168,140,44
Ra 156,242,7168
Ta 212,12,36864
Mvaa 172,140,60
X 189
Mvaa 172,136,56
v_and_b32 v5, 0xf0, v5
v_mul_u32_u24 v4, 1280, v4
v_smfmac_f32_16x16x64_bf16 a[160:163], a[176:179], v[192:199], v240
W 5
X 190
Maaa 232,196,204
Ta 214,12,37120
v_add_u32 v4, v4, v5
Maaa 236,200,204
Ra 216,13,59392
K 222,188,189
v_smfmac_f32_16x16x64_bf16 a[164:167], a[176:179], v[200:207], v240
X 191
X 160
K 223,190,191
v_smfmac_f32_16x16x64_bf16 a[168:171], a[176:179], v[208:215], v240
Ra 128,242,16384
v_xor_b32 v5, 128, v4
v_smfmac_f32_16x16x64_bf16 a[172:175], a[176:179], v[216:223], v240
W 4
Ta 220,11,47104
v_add_u32 v6, 8192, v4
Cvaa 176,144,0,224
Ta 222,11,47360
v_add_u32 v7, 12288, v5
Mvaa 176,148,4
Ra 132,242,17408
v_add_u32 v5, 4096, v5
Cvaa 180,148,20,228
s_mul_i32 s94, s85, 40960
X 161
Mvaa 180,144,16
s_mul_i32 s81, s14, 20480
Ta 224,12,38912
Ta 226,12,39168
Cvaa 184,144,32,232
s_add_u32 s94, s94, s81
X 162
Mvaa 184,148,36
W 6
s_lshl_b32 s95, s14, 12
K 192,160,161
Maaa 232,208,216
s_add_u32 s95, s95, 98304
X 163
Maaa 236,212,216
s_add_u32 m0, s95, 0
Ra 228,13,59904
Ra 136,242,18432
Cvaa 188,148,52,236
X 164
Mvaa 188,144,48
Ta 184,11,49152
K 193,162,163
Mvaa 176,152,8
X 165
Mvaa 176,156,12
Ra 140,242,19456
Ta 186,11,49408
Mvaa 180,156,28
D 4,20,94,0
Ta 188,12,40960
Mvaa 180,152,24
X 166
Mvaa 184,152,40
D 5,20,94,1024
K 200,164,165
Mvaa 184,156,44
X 167
Mvaa 188,156,60
W 5
D 6,20,94,2048
Ra 144,242,20480
Maaa 232,220,228
s_add_u32 s64, s64, 1
Ta 190,12,41216
Ra 192,13,60416
Maaa 236,224,228
D 7,20,94,3072
Ta 196,11,51200
Mvaa 188,152,56
W 6
X 168
Mvav 32,128,196
s_add_u32 s94, s94, 256
s_add_u32 m0, s95, 8192
K 201,166,167
Mvav 64,128,204
X 169
Mvav 96,128,212
D 4,20,94,0
Ra 148,242,21504
Mvav 128,128,220
X 170
Mvav 132,132,220
D 5,20,94,1024
Ta 198,11,51456
Mvav 100,132,212
Ta 200,12,43008
K 208,168,169
Mvav 68,132,204
D 6,20,94,2048
Ra 152,242,22528
Mvav 36,132,196
W 5
X 171
Maaa 232,184,192
D 7,20,94,3072
Ta 202,12,43264
Maaa 236,188,192
s_add_u32 s94, s94, 256
X 172
Mvav 40,136,196
s_add_u32 m0, s95, 16384
Ra 204,13,60928
K 209,170,171
Mvav 72,136,204
X 173
Mvav 104,136,212
Ra 156,242,23552
D 4,20,94,0
Mvav 136,136,220
X 174
Mvav 140,140,220
D 5,20,94,1024
Ta 208,11,53248
Mvav 108,140,212
Ta 210,11,53504
K 216,172,173
Mvav 76,140,204
D 6,20,94,2048
Ra 128,242,8192
Mvav 44,140,196
W 3
X 175
Mvav 48,144,196
D 7,20,94,3072
Ta 212,12,45056
Maaa 232,196,204
s_add_u32 s94, s94, 256
X 176
Maaa 236,200,204
s_add_u32 m0, s95, 24576
Ta 214,12,45312
K 217,174,175
Mvav 80,144,204
D 4,20,94,0
Ra 132,242,9216
Mvav 112,144,212
X 177
Mvav 144,144,220
Ra 216,13,61440
D 5,20,94,1024
Mvav 148,148,220
X 178
Mvav 116,148,212
D 6,20,94,2048
Ta 220,11,55296
Mvav 84,148,204
Ta 222,11,55552
K 194,176,177
Mvav 52,148,196
D 7,20,94,3072
Ra 136,242,10240
Mvav 56,152,196
s_add_u32 s94, s94, 256
X 179
Mvav 88,152,204
W 3
X 180
Maaa 232,208,216
s_add_u32 m0, s95, 32768
Ta 224,12,47104
K 195,178,179
Maaa 236,212,216
D 4,20,94,0
Ta 226,12,47360
Mvav 120,152,212
Ra 140,242,11264
Ra 228,13,61952
Mvav 152,152,220
X 181
Mvav 156,156,220
D 5,20,94,1024
Ta 184,11,57344
Mvav 124,156,212
X 182
Mvav 92,156,204
D 6,20,94,2048
K 202,180,181
Mvav 60,156,196
W 2
X 183
Cvaa 160,128,0,224
D 7,20,94,3072
Ra 144,242,12288
Mvaa 160,132,4
s_add_u32 s94, s94, 256
X 184
Cvaa 164,132,20,228
W 2
Ta 186,11,57600
K 203,182,183
Maaa 232,220,228
X 185
Maaa 236,224,228
Ta 188,12,49152
Ra 148,242,13312
Mvaa 164,128,16
X 186
Cvaa 168,128,32,232
Ta 190,12,49408
K 210,184,185
Mvaa 168,132,36
Ra 192,13,62464
Ta 196,11,59392
Cvaa 172,132,52,236
X 187
Mvaa 172,128,48
X 188
Mvaa 160,136,8
Ra 152,242,14336
K 211,186,187
Mvaa 160,140,12
Ta 198,11,59648
Ta 200,12,51200
Mvaa 164,140,28
X 189
Mvaa 164,136,24
W 4
X 190
Maaa 232,184,192
Ra 156,242,15360
K 218,188,189
Maaa 236,188,192
Ta 202,12,51456
Ra 204,13,62976
Mvaa 168,136,40
X 191
Mvaa 168,140,44
Ra 128,242,24576
Ra 132,242,25600
Mvaa 172,140,60
X 160
Mvaa 172,136,56
W 4
Ra 136,242,26624
K 219,190,191
Cvaa 176,144,0,224
X 161
Mvaa 176,148,4
X 162
Cvaa 180,148,20,228
Ra 140,242,27648
K 196,160,161
Mvaa 180,144,16
X 163
Cvaa 184,144,32,232
W 4
X 164
Maaa 232,196,204
K 197,162,163
Maaa 236,200,204
X 165
Mvaa 184,148,36
X 166
Cvaa 188,148,52,236
K 204,164,165
Mvaa 188,144,48
X 167
Mvaa 176,152,8
Ra 144,242,28672
G 252,232
Mvaa 176,156,12
G 253,233
G 254,234
Mvaa 180,156,28
X 168
Mvaa 180,152,24
G 255,235
G 8,236
Mvaa 184,152,40
Ra 148,242,29696
G 9,237
Mvaa 184,156,44
G 10,238
G 14,239
Mvaa 188,156,60
K 252,252,8
K 253,253,9
Mvaa 188,152,56
W 2
K 205,166,167
K 254,254,10
Mvav 32,128,192
K 255,255,14
v_and_b32 v8, 0xffff0000, v252
Mvav 64,128,200
v_and_b32 v9, 0xffff0000, v253
v_and_b32 v10, 0xffff0000, v254
Mvav 96,128,208
X 169
Mvav 128,128,216
)"
R"(v_and_b32 v14, 0xffff0000, v255
v_lshlrev_b32 v252, 16, v252
Mvav 132,132,216
v_lshlrev_b32 v253, 16, v253
v_lshlrev_b32 v254, 16, v254
Mvav 100,132,208
v_lshlrev_b32 v255, 16, v255
v_mul_f32 v15, 0xbfb8aa3b, v252
Mvav 68,132,200
v_mul_f32 v2, 0xbfb8aa3b, v253
v_mul_f32 v3, 0xbfb8aa3b, v254
Mvav 36,132,192
X 170
Mvav 40,136,192
v_mul_f32 v27, 0xbfb8aa3b, v255
K 212,168,169
Mvav 72,136,200
X 15
Mvav 104,136,208
Ra 152,242,30720
Ra 156,242,31744
Mvav 136,136,216
X 2
X 3
X 27
v_add_f32 v15, 1.0, v15
v_add_f32 v2, 1.0, v2
v_add_f32 v3, 1.0, v3
v_add_f32 v27, 1.0, v27
v_rcp_f32 v15, v15
v_rcp_f32 v2, v2
v_rcp_f32 v3, v3
v_rcp_f32 v27, v27
U 252,252,15
U 253,253,2
U 254,254,3
U 255,255,27
K 252,252,252
K 253,253,253
K 254,254,254
K 255,255,255
v_lshlrev_b32 v252, 16, v252
v_lshlrev_b32 v253, 16, v253
v_lshlrev_b32 v254, 16, v254
v_lshlrev_b32 v255, 16, v255
U 252,252,8
U 253,253,9
U 254,254,10
U 255,255,14
K 246,252,253
K 247,254,255
s_lshl_b32 s81, s14, 3
v_add_u32 v2, s81, v1
s_mov_b32 s86, 0x00ff00ff
s_mov_b32 s87, 0x00ff00ff
v_cndmask_b32_e64 v252, 0, v246, s[86:87]
v_cndmask_b32_e64 v253, 0, v247, s[86:87]
v_cndmask_b32_e64 v246, v246, 0, s[86:87]
v_cndmask_b32_e64 v247, v247, 0, s[86:87]
ds_write_b64 v2, v[252:253] offset:63488
ds_write_b64 v2, v[246:247] offset:64512
Mvav 140,140,216
X 171
Mvav 108,140,208
X 172
Mvav 76,140,200
s_waitcnt vmcnt(0) lgkmcnt(0)
s_barrier
v_xor_b32 v242, 0x10000, v242
Mvav 44,140,192
Ra 128,242,0
K 213,170,171
Mvav 48,144,192
X 173
Mvav 80,144,200
X 174
Mvav 112,144,208
Ra 132,242,1024
K 220,172,173
Mvav 144,144,216
X 175
Mvav 148,148,216
X 176
Mvav 116,148,208
Ra 136,242,2048
K 221,174,175
Mvav 84,148,200
X 177
Mvav 52,148,192
X 178
Mvav 56,152,192
Ra 140,242,3072
K 198,176,177
Mvav 88,152,200
X 179
Mvav 120,152,208
X 180
Mvav 152,152,216
K 199,178,179
Mvav 156,156,216
X 181
Mvav 124,156,208
X 182
Mvav 92,156,200
K 206,180,181
Mvav 60,156,192
X 183
s_nop 0
K 207,182,183
W 0
Cvaa 160,128,0,224
Ra 144,242,4096
Ra 148,242,5120
Mvaa 160,132,4
s_xor_b32 s62, s62, 0x10000
X 184
Cvaa 164,132,20,228
Ra 216,1,63488
Ra 220,1,64512
Mvaa 164,128,16
v_lshrrev_b32 v8, 6, v1
v_and_b32 v9, 7, v8
Cvaa 168,128,32,232
s_lshl_b32 s81, s14, 2
X 185
Mvaa 168,132,36
v_xor_b32 v9, s81, v9
v_lshlrev_b32 v9, 5, v9
Cvaa 172,132,52,236
X 186
Mvaa 172,128,48
v_lshl_add_u32 v4, v8, 8, v9
v_bfe_u32 v9, v1, 4, 2
Mvaa 160,136,8
v_lshl_add_u32 v4, v9, 3, v4
v_add_u32 v4, 98304, v4
Mvaa 160,140,12
K 214,184,185
v_xor_b32 v5, 32, v4
Mvaa 164,140,28
v_xor_b32 v6, 64, v4
v_xor_b32 v7, 96, v4
Ta 184,4,0
Ta 186,4,4096
Mvaa 164,136,24
Ta 188,5,0
Ta 190,5,4096
Mvaa 168,136,40
Ra 152,242,6144
Ta 192,6,0
Mvaa 168,140,44
W 4
X 187
Zaaa 232,184,216
Ta 194,6,4096
v_and_b32 v11, 0x70, v1
Mvaa 172,140,60
v_mul_u32_u24 v11, 160, v11
v_lshrrev_b32 v10, 8, v1
Mvaa 172,136,56
X 188
v_smfmac_f32_16x16x64_bf16 a[160:163], a[176:179], v[192:199], v240
Ra 156,242,7168
W 4
Maaa 232,188,220
K 215,186,187
v_lshl_add_u32 v11, v10, 4, v11
v_smfmac_f32_16x16x64_bf16 a[164:167], a[176:179], v[200:207], v240
X 189
X 190
K 222,188,189
X 191
X 160
K 223,190,191
v_smfmac_f32_16x16x64_bf16 a[168:171], a[176:179], v[208:215], v240
Ta 196,7,0
v_bfe_u32 v10, v1, 7, 1
v_smfmac_f32_16x16x64_bf16 a[172:175], a[176:179], v[216:223], v240
W 2
Ta 198,7,4096
v_lshl_add_u32 v10, v10, 6, v11
Zaaa 236,192,216
W 2
s_mul_i32 s94, s85, 20480
s_lshl_b32 s82, s14, 8
Cvaa 176,144,0,224
s_add_u32 s94, s94, s82
Ta 200,4,8192
Ta 202,4,12288
Mvaa 176,148,4
s_add_u32 s94, s94, 2752512
s_lshl_b32 s71, s85, 4
Ra 128,242,16384
Cvaa 180,148,20,228
W 3
s_lshl_b32 s82, s14, 3
Ta 204,5,8192
Maaa 236,196,220
s_add_u32 s71, s71, s82
s_add_u32 s71, s71, 2560
Ta 206,5,12288
Mvaa 180,144,16
X 161
Cvaa 184,144,32,232
Ra 132,242,17408
Ta 208,6,8192
Mvaa 184,148,36
W 5
X 162
Zaaa 240,200,216
Ta 210,6,12288
K 192,160,161
Cvaa 188,148,52,236
buffer_store_dwordx4 a[232:235], v10, s[8:11], s94 offen offset:0 sc1
Ta 212,7,8192
Mvaa 188,144,48
X 163
Mvaa 176,152,8
W 4
X 164
Maaa 240,204,220
Ra 136,242,18432
Ta 214,7,12288
Mvaa 176,156,12
Ta 184,4,16384
K 193,162,163
Mvaa 180,156,28
X 165
Mvaa 180,152,24
W 4
Ta 186,4,20480
Ra 140,242,19456
Zaaa 244,208,216
X 166
Mvaa 184,152,40
buffer_store_dwordx4 a[236:239], v10, s[8:11], s94 offen offset:128 sc1
K 200,164,165
Mvaa 184,156,44
X 167
Mvaa 188,156,60
W 3
Ta 188,5,16384
Ta 190,5,20480
Maaa 244,212,220
s_add_u32 s64, s64, 1
X 168
Mvaa 188,152,56
W 2
Ra 144,242,20480
K 201,166,167
Mvav 32,128,196
Ta 192,6,16384
Ta 194,6,20480
Mvav 64,128,204
X 169
Zaaa 248,184,216
buffer_store_dwordx4 a[240:243], v10, s[8:11], s94 offen offset:512 sc1
Ta 196,7,16384
Mvav 96,128,212
X 170
Mvav 128,128,220
Ra 148,242,21504
K 208,168,169
Mvav 132,132,220
W 5
Ta 198,7,20480
Ta 200,4,24576
Maaa 248,188,220
X 171
Mvav 100,132,212
X 172
Mvav 68,132,204
Ta 202,4,28672
K 209,170,171
Mvav 36,132,196
W 5
Ra 152,242,22528
buffer_store_dwordx4 a[244:247], v10, s[8:11], s94 offen offset:640 sc1
Zaaa 252,192,216
X 173
Mvav 40,136,196
X 174
Mvav 72,136,204
Ta 204,5,24576
Ta 206,5,28672
Mvav 104,136,212
W 5
Ra 156,242,23552
K 216,172,173
Maaa 252,196,220
X 175
Mvav 136,136,220
Ta 208,6,24576
Ta 210,6,28672
Mvav 140,140,220
X 176
Mvav 108,140,212
W 6
buffer_store_dwordx4 a[248:251], v10, s[8:11], s94 offen offset:1024 sc1
K 217,174,175
Zaaa 232,200,216
X 177
Mvav 76,140,204
Ta 212,7,24576
Ra 128,242,8192
Mvav 44,140,196
W 4
Ta 214,7,28672
Ta 184,4,32768
Mvav 48,144,196
X 178
Maaa 232,204,220
Ra 132,242,9216
K 194,176,177
Mvav 80,144,204
X 179
Mvav 112,144,212
Ta 186,4,36864
buffer_store_dwordx4 a[252:255], v10, s[8:11], s94 offen offset:1152 sc1
Mvav 144,144,220
W 6
X 180
Zaaa 236,208,216
Ta 188,5,32768
K 195,178,179
Mvav 148,148,220
X 181
Mvav 116,148,212
Ta 190,5,36864
Ra 136,242,10240
Mvav 84,148,204
W 6
X 182
Maaa 236,212,220
Ta 192,6,32768
K 202,180,181
Mvav 52,148,196
Ta 194,6,36864
Ra 140,242,11264
Mvav 56,152,196
X 183
Mvav 88,152,204
W 6
buffer_store_dwordx4 a[232:235], v10, s[8:11], s94 offen offset:1536 sc1
Ta 196,7,32768
Zaaa 240,184,216
X 184
Mvav 120,152,212
Ta 198,7,36864
K 203,182,183
Mvav 152,152,220
Ra 144,242,12288
Ra 148,242,13312
Mvav 156,156,220
W 8
X 185
Maaa 240,188,220
X 186
Mvav 124,156,212
buffer_store_dwordx4 a[236:239], v10, s[8:11], s94 offen offset:1664 sc1
K 210,184,185
Mvav 92,156,204
X 187
Mvav 60,156,196
W 5
X 188
Zaaa 244,192,216
W 4
Ra 152,242,14336
K 211,186,187
Cvaa 160,128,0,224
X 189
Mvaa 160,132,4
X 190
Cvaa 164,132,20,228
W 3
Ra 156,242,15360
buffer_store_dwordx4 a[240:243], v10, s[8:11], s94 offen offset:2048 sc1
Maaa 244,196,220
K 218,188,189
Mvaa 164,128,16
X 191
Cvaa 168,128,32,232
Mvaa 168,132,36
Cvaa 172,132,52,236
Mvaa 172,128,48
Ra 128,242,24576
Mvaa 160,136,8
Ra 132,242,25600
buffer_store_dwordx4 a[244:247], v10, s[8:11], s94 offen offset:2176 sc1
Mvaa 160,140,12
Mvaa 164,140,28
Mvaa 164,136,24
Mvaa 168,136,40
Mvaa 168,140,44
Mvaa 172,140,60
Mvaa 172,136,56
W 2
Ra 136,242,26624
Cvaa 176,144,0,224
)"
R"(X 160
Mvaa 176,148,4
Ra 140,242,27648
K 219,190,191
Cvaa 180,148,20,228
X 161
Mvaa 180,144,16
X 162
Cvaa 184,144,32,232
K 196,160,161
Mvaa 184,148,36
X 163
Cvaa 188,148,52,236
X 164
Mvaa 188,144,48
Ra 144,242,28672
K 197,162,163
Mvaa 176,152,8
X 165
Mvaa 176,156,12
X 166
Mvaa 180,156,28
Ra 148,242,29696
K 204,164,165
Mvaa 180,152,24
X 167
Mvaa 184,152,40
X 168
Mvaa 184,156,44
K 205,166,167
Mvaa 188,156,60
X 169
Mvaa 188,152,56
W 2
X 170
Mvav 32,128,192
Ra 152,242,30720
K 212,168,169
Mvav 64,128,200
X 171
Mvav 96,128,208
Ra 156,242,31744
Mvav 128,128,216
X 172
Mvav 132,132,216
K 213,170,171
Mvav 100,132,208
X 173
Mvav 68,132,200
X 174
Mvav 36,132,192
Mvav 40,136,192
K 220,172,173
Mvav 72,136,200
X 175
Mvav 104,136,208
Mvav 136,136,216
X 176
Mvav 140,140,216
Mvav 108,140,208
K 221,174,175
Mvav 76,140,200
W 0
s_barrier
v_xor_b32 v242, 0x10000, v242
Mvav 44,140,192
X 177
Mvav 48,144,192
X 178
Mvav 80,144,200
Ra 128,242,0
K 198,176,177
Mvav 112,144,208
X 179
Mvav 144,144,216
Ra 132,242,1024
Mvav 148,148,216
X 180
Mvav 116,148,208
Ra 136,242,2048
Mvav 84,148,200
K 199,178,179
Mvav 52,148,192
X 181
Mvav 56,152,192
Ra 140,242,3072
Mvav 88,152,200
X 182
Mvav 120,152,208
Mvav 152,152,216
K 206,180,181
Mvav 156,156,216
X 183
Mvav 124,156,208
Mvav 92,156,200
s_waitcnt vmcnt(0)
v_mov_b64 v[28:29], s[12:13]
s_mov_b64 exec, 1
buffer_store_dwordx2 v[28:29], off, s[8:11], s71 sc1
s_mov_b64 exec, -1
K 207,182,183
Mvav 60,156,192
s_mov_b32 s85, 99
s_lshl_b32 s65, s57, 1
s_sub_u32 s65, s65, 1
s_branch .Ll0t_44%=
.Lf1r_45%=:
s_lshl_b32 s92, s58, 2
s_add_u32 s91, s92, s51
s_add_u32 s95, s92, 0
s_and_b32 s95, s95, 15
s_mul_i32 s95, s95, 40960
s_mul_i32 s71, s51, 0
s_add_u32 s71, s71, 133120
s_add_u32 m0, s71, s48
s_lshl2_add_u32 s94, s48, s95
D 16,40,94,0
s_add_u32 s95, s92, 1
s_and_b32 s95, s95, 15
s_mul_i32 s95, s95, 40960
s_mul_i32 s71, s51, 0
s_add_u32 s71, s71, 143360
D 17,40,94,1024
D 18,40,94,2048
D 19,40,94,3072
s_add_u32 m0, m0, 4096
s_nop 0
buffer_load_dwordx4 v20, s[40:43], s94 offen lds
s_add_u32 m0, s71, s48
s_lshl2_add_u32 s94, s48, s95
D 16,40,94,0
s_add_u32 s95, s92, 2
s_and_b32 s95, s95, 15
s_mul_i32 s95, s95, 40960
s_mul_i32 s71, s51, 0
s_add_u32 s71, s71, 153600
D 17,40,94,1024
D 18,40,94,2048
D 19,40,94,3072
s_add_u32 m0, m0, 4096
s_nop 0
buffer_load_dwordx4 v20, s[40:43], s94 offen lds
s_add_u32 m0, s71, s48
s_lshl2_add_u32 s94, s48, s95
D 16,40,94,0
D 17,40,94,1024
D 18,40,94,2048
D 19,40,94,3072
s_add_u32 m0, m0, 4096
s_nop 0
buffer_load_dwordx4 v20, s[40:43], s94 offen lds
s_branch .Lf1_38%=
.Lspin_13%=:
s_mov_b32 s84, 0
.Llp_15%=:
s_sleep 2
v_lshrrev_b32 v28, 1, v1
s_lshl_b32 s80, s60, 9
s_add_u32 s80, s80, 40960
buffer_load_dwordx2 v[28:29], v28, s[8:11], s80 offen sc1
s_waitcnt vmcnt(0)
s_add_u32 s84, s84, 1
v_cmp_eq_u32 vcc, s13, v29
s_cmp_eq_u64 vcc, -1
s_cbranch_scc1 .Lok_14%=
s_cmp_lt_u32 s84, 1048576
s_cbranch_scc1 .Llp_15%=
s_branch .Lok_14%=
.Lspin_18%=:
s_mov_b32 s84, 0
.Llp_20%=:
s_sleep 2
v_lshrrev_b32 v28, 1, v1
s_lshl_b32 s80, s60, 9
s_add_u32 s80, s80, 40960
buffer_load_dwordx2 v[28:29], v28, s[8:11], s80 offen sc1
s_waitcnt vmcnt(0)
s_add_u32 s84, s84, 1
v_cmp_eq_u32 vcc, s13, v29
s_cmp_eq_u64 vcc, -1
s_cbranch_scc1 .Lok_19%=
s_cmp_lt_u32 s84, 1048576
s_cbranch_scc1 .Llp_20%=
s_branch .Lok_19%=
.Lrt_21%=:
s_waitcnt vmcnt(0)
s_mov_b32 s86, 40960
buffer_load_dwordx4 v[32:35], v1, s[8:11], s86 offen offset:0 sc1
buffer_load_dwordx4 v[36:39], v1, s[8:11], s86 offen offset:1024 sc1
buffer_load_dwordx4 v[40:43], v1, s[8:11], s86 offen offset:2048 sc1
buffer_load_dwordx4 v[44:47], v1, s[8:11], s86 offen offset:3072 sc1
s_mov_b32 s86, 45056
buffer_load_dwordx4 v[48:51], v1, s[8:11], s86 offen offset:0 sc1
buffer_load_dwordx4 v[52:55], v1, s[8:11], s86 offen offset:1024 sc1
buffer_load_dwordx4 v[56:59], v1, s[8:11], s86 offen offset:2048 sc1
buffer_load_dwordx4 v[60:63], v1, s[8:11], s86 offen offset:3072 sc1
s_mov_b32 s86, 49152
buffer_load_dwordx4 v[64:67], v1, s[8:11], s86 offen offset:0 sc1
buffer_load_dwordx4 v[68:71], v1, s[8:11], s86 offen offset:1024 sc1
buffer_load_dwordx4 v[72:75], v1, s[8:11], s86 offen offset:2048 sc1
buffer_load_dwordx4 v[76:79], v1, s[8:11], s86 offen offset:3072 sc1
s_mov_b32 s86, 53248
buffer_load_dwordx4 v[80:83], v1, s[8:11], s86 offen offset:0 sc1
buffer_load_dwordx4 v[84:87], v1, s[8:11], s86 offen offset:1024 sc1
buffer_load_dwordx4 v[88:91], v1, s[8:11], s86 offen offset:2048 sc1
buffer_load_dwordx4 v[92:95], v1, s[8:11], s86 offen offset:3072 sc1
s_branch .Lrtr_22%=
.Lrt_23%=:
v_lshrrev_b32 v136, 1, v1
s_mov_b32 s84, 0
.LL_25%=:
s_waitcnt vmcnt(0)
v_min3_u32 v96, v33, v35, v95
v_max3_u32 v97, v33, v35, v95
v_min3_u32 v96, v96, v37, v39
v_max3_u32 v97, v97, v37, v39
v_min3_u32 v96, v96, v41, v43
v_max3_u32 v97, v97, v41, v43
v_min3_u32 v96, v96, v45, v47
v_max3_u32 v97, v97, v45, v47
v_min3_u32 v96, v96, v49, v51
v_max3_u32 v97, v97, v49, v51
v_min3_u32 v96, v96, v53, v55
v_max3_u32 v97, v97, v53, v55
v_min3_u32 v96, v96, v57, v59
v_max3_u32 v97, v97, v57, v59
v_min3_u32 v96, v96, v61, v63
v_max3_u32 v97, v97, v61, v63
v_min3_u32 v96, v96, v65, v67
v_max3_u32 v97, v97, v65, v67
v_min3_u32 v96, v96, v69, v71
v_max3_u32 v97, v97, v69, v71
v_min3_u32 v96, v96, v73, v75
v_max3_u32 v97, v97, v73, v75
v_min3_u32 v96, v96, v77, v79
v_max3_u32 v97, v97, v77, v79
v_min3_u32 v96, v96, v81, v83
v_max3_u32 v97, v97, v81, v83
v_min3_u32 v96, v96, v85, v87
v_max3_u32 v97, v97, v85, v87
v_min3_u32 v96, v96, v89, v91
v_max3_u32 v97, v97, v89, v91
v_min_u32 v96, v96, v93
v_max_u32 v97, v97, v93
v_cmp_eq_u32 vcc, s13, v96
v_cmp_eq_u32_e64 s[86:87], s13, v97
s_and_b64 vcc, vcc, s[86:87]
s_cmp_eq_u64 vcc, -1
s_cbranch_scc1 .LL_26%=
s_sleep 2
s_mov_b32 s86, 40960
buffer_load_dwordx4 v[32:35], v1, s[8:11], s86 offen offset:0 sc1
buffer_load_dwordx4 v[36:39], v1, s[8:11], s86 offen offset:1024 sc1
buffer_load_dwordx4 v[40:43], v1, s[8:11], s86 offen offset:2048 sc1
buffer_load_dwordx4 v[44:47], v1, s[8:11], s86 offen offset:3072 sc1
s_mov_b32 s86, 45056
buffer_load_dwordx4 v[48:51], v1, s[8:11], s86 offen offset:0 sc1
buffer_load_dwordx4 v[52:55], v1, s[8:11], s86 offen offset:1024 sc1
buffer_load_dwordx4 v[56:59], v1, s[8:11], s86 offen offset:2048 sc1
buffer_load_dwordx4 v[60:63], v1, s[8:11], s86 offen offset:3072 sc1
s_mov_b32 s86, 49152
buffer_load_dwordx4 v[64:67], v1, s[8:11], s86 offen offset:0 sc1
buffer_load_dwordx4 v[68:71], v1, s[8:11], s86 offen offset:1024 sc1
buffer_load_dwordx4 v[72:75], v1, s[8:11], s86 offen offset:2048 sc1
buffer_load_dwordx4 v[76:79], v1, s[8:11], s86 offen offset:3072 sc1
s_mov_b32 s86, 53248
buffer_load_dwordx4 v[80:83], v1, s[8:11], s86 offen offset:0 sc1
buffer_load_dwordx4 v[84:87], v1, s[8:11], s86 offen offset:1024 sc1
buffer_load_dwordx4 v[88:91], v1, s[8:11], s86 offen offset:2048 sc1
buffer_load_dwordx4 v[92:95], v1, s[8:11], s86 offen offset:3072 sc1
s_add_u32 s84, s84, 1
s_cmp_lt_u32 s84, 1048576
s_cbranch_scc1 .LL_25%=
s_waitcnt vmcnt(0)
.LL_26%=:
v_mov_b32 v96, v32
v_mov_b32 v97, 0
v_mov_b32 v98, 0
v_mov_b32 v100, 0
v_med3_u32 v98, v97, v98, v34
v_med3_u32 v97, v96, v97, v34
v_max_u32 v96, v96, v34
v_med3_u32 v98, v97, v98, v36
v_med3_u32 v97, v96, v97, v36
v_max_u32 v96, v96, v36
v_med3_u32 v98, v97, v98, v38
v_med3_u32 v97, v96, v97, v38
v_max_u32 v96, v96, v38
v_med3_u32 v98, v97, v98, v40
v_med3_u32 v97, v96, v97, v40
v_max_u32 v96, v96, v40
v_med3_u32 v98, v97, v98, v42
v_med3_u32 v97, v96, v97, v42
v_max_u32 v96, v96, v42
v_med3_u32 v98, v97, v98, v44
v_med3_u32 v97, v96, v97, v44
v_max_u32 v96, v96, v44
v_med3_u32 v98, v97, v98, v46
v_med3_u32 v97, v96, v97, v46
v_max_u32 v96, v96, v46
v_med3_u32 v98, v97, v98, v48
v_med3_u32 v97, v96, v97, v48
v_max_u32 v96, v96, v48
v_med3_u32 v98, v97, v98, v50
v_med3_u32 v97, v96, v97, v50
v_max_u32 v96, v96, v50
v_med3_u32 v98, v97, v98, v52
v_med3_u32 v97, v96, v97, v52
v_max_u32 v96, v96, v52
v_med3_u32 v98, v97, v98, v54
v_med3_u32 v97, v96, v97, v54
v_max_u32 v96, v96, v54
v_med3_u32 v98, v97, v98, v56
v_med3_u32 v97, v96, v97, v56
v_max_u32 v96, v96, v56
v_med3_u32 v98, v97, v98, v58
v_med3_u32 v97, v96, v97, v58
v_max_u32 v96, v96, v58
v_med3_u32 v98, v97, v98, v60
v_med3_u32 v97, v96, v97, v60
v_max_u32 v96, v96, v60
v_med3_u32 v98, v97, v98, v62
v_med3_u32 v97, v96, v97, v62
v_max_u32 v96, v96, v62
v_med3_u32 v98, v97, v98, v64
v_med3_u32 v97, v96, v97, v64
v_max_u32 v96, v96, v64
v_med3_u32 v98, v97, v98, v66
v_med3_u32 v97, v96, v97, v66
v_max_u32 v96, v96, v66
v_med3_u32 v98, v97, v98, v68
v_med3_u32 v97, v96, v97, v68
v_max_u32 v96, v96, v68
v_med3_u32 v98, v97, v98, v70
v_med3_u32 v97, v96, v97, v70
v_max_u32 v96, v96, v70
v_med3_u32 v98, v97, v98, v72
v_med3_u32 v97, v96, v97, v72
v_max_u32 v96, v96, v72
v_med3_u32 v98, v97, v98, v74
v_med3_u32 v97, v96, v97, v74
v_max_u32 v96, v96, v74
v_med3_u32 v98, v97, v98, v76
v_med3_u32 v97, v96, v97, v76
v_max_u32 v96, v96, v76
v_med3_u32 v98, v97, v98, v78
v_med3_u32 v97, v96, v97, v78
v_max_u32 v96, v96, v78
v_med3_u32 v98, v97, v98, v80
v_med3_u32 v97, v96, v97, v80
v_max_u32 v96, v96, v80
v_med3_u32 v98, v97, v98, v82
v_med3_u32 v97, v96, v97, v82
v_max_u32 v96, v96, v82
v_med3_u32 v98, v97, v98, v84
v_med3_u32 v97, v96, v97, v84
v_max_u32 v96, v96, v84
v_med3_u32 v98, v97, v98, v86
v_med3_u32 v97, v96, v97, v86
v_max_u32 v96, v96, v86
v_med3_u32 v98, v97, v98, v88
v_med3_u32 v97, v96, v97, v88
v_max_u32 v96, v96, v88
v_med3_u32 v98, v97, v98, v90
v_med3_u32 v97, v96, v97, v90
v_max_u32 v96, v96, v90
v_med3_u32 v98, v97, v98, v92
v_med3_u32 v97, v96, v97, v92
v_max_u32 v96, v96, v92
v_med3_u32 v98, v97, v98, v94
v_med3_u32 v97, v96, v97, v94
v_max_u32 v96, v96, v94
v_mov_b32 v99, v96
s_nop 1
v_max_u32_dpp v99, v99, v99 row_shr:1 row_mask:0xf bank_mask:0xf
s_nop 1
v_max_u32_dpp v99, v99, v99 row_shr:2 row_mask:0xf bank_mask:0xf
s_nop 1
v_max_u32_dpp v99, v99, v99 row_shr:4 row_mask:0xf bank_mask:0xf
s_nop 1
v_max_u32_dpp v99, v99, v99 row_shr:8 row_mask:0xf bank_mask:0xf
s_nop 1
v_max_u32_dpp v99, v99, v99 row_bcast:15 row_mask:0xa bank_mask:0xf
s_nop 1
v_max_u32_dpp v99, v99, v99 row_bcast:31 row_mask:0xc bank_mask:0xf
s_nop 0
v_readlane_b32 s86, v99, 63
s_nop 1
v_cmp_eq_u32 vcc, s86, v96
s_and_b32 s87, s86, 0xffff
s_sub_u32 s87, 2047, s87
v_cndmask_b32 v96, v96, v97, vcc
v_cndmask_b32_e64 v97, v97, 0, vcc
v_writelane_b32 v100, s87, 0
v_mov_b32 v99, v96
s_nop 1
v_max_u32_dpp v99, v99, v99 row_shr:1 row_mask:0xf bank_mask:0xf
s_nop 1
v_max_u32_dpp v99, v99, v99 row_shr:2 row_mask:0xf bank_mask:0xf
s_nop 1
)"
R"(v_max_u32_dpp v99, v99, v99 row_shr:4 row_mask:0xf bank_mask:0xf
s_nop 1
v_max_u32_dpp v99, v99, v99 row_shr:8 row_mask:0xf bank_mask:0xf
s_nop 1
v_max_u32_dpp v99, v99, v99 row_bcast:15 row_mask:0xa bank_mask:0xf
s_nop 1
v_max_u32_dpp v99, v99, v99 row_bcast:31 row_mask:0xc bank_mask:0xf
s_nop 0
v_readlane_b32 s86, v99, 63
s_nop 1
v_cmp_eq_u32 vcc, s86, v96
s_and_b32 s87, s86, 0xffff
s_sub_u32 s87, 2047, s87
v_cndmask_b32 v96, v96, v97, vcc
v_cndmask_b32_e64 v97, v97, 0, vcc
v_writelane_b32 v100, s87, 1
v_mov_b32 v99, v96
s_nop 1
v_max_u32_dpp v99, v99, v99 row_shr:1 row_mask:0xf bank_mask:0xf
s_nop 1
v_max_u32_dpp v99, v99, v99 row_shr:2 row_mask:0xf bank_mask:0xf
s_nop 1
v_max_u32_dpp v99, v99, v99 row_shr:4 row_mask:0xf bank_mask:0xf
s_nop 1
v_max_u32_dpp v99, v99, v99 row_shr:8 row_mask:0xf bank_mask:0xf
s_nop 1
v_max_u32_dpp v99, v99, v99 row_bcast:15 row_mask:0xa bank_mask:0xf
s_nop 1
v_max_u32_dpp v99, v99, v99 row_bcast:31 row_mask:0xc bank_mask:0xf
s_nop 0
v_readlane_b32 s86, v99, 63
s_nop 1
v_cmp_eq_u32 vcc, s86, v96
s_and_b32 s87, s86, 0xffff
s_sub_u32 s87, 2047, s87
v_cndmask_b32 v96, v96, v97, vcc
v_cndmask_b32_e64 v97, v97, 0, vcc
v_writelane_b32 v100, s87, 2
v_mov_b32 v99, v96
s_nop 1
v_max_u32_dpp v99, v99, v99 row_shr:1 row_mask:0xf bank_mask:0xf
s_nop 1
v_max_u32_dpp v99, v99, v99 row_shr:2 row_mask:0xf bank_mask:0xf
s_nop 1
v_max_u32_dpp v99, v99, v99 row_shr:4 row_mask:0xf bank_mask:0xf
s_nop 1
v_max_u32_dpp v99, v99, v99 row_shr:8 row_mask:0xf bank_mask:0xf
s_nop 1
v_max_u32_dpp v99, v99, v99 row_bcast:15 row_mask:0xa bank_mask:0xf
s_nop 1
v_max_u32_dpp v99, v99, v99 row_bcast:31 row_mask:0xc bank_mask:0xf
s_nop 0
v_readlane_b32 s86, v99, 63
s_nop 1
v_cmp_eq_u32 vcc, s86, v96
s_and_b32 s87, s86, 0xffff
s_sub_u32 s87, 2047, s87
v_cndmask_b32 v96, v96, v97, vcc
v_cndmask_b32_e64 v97, v97, 0, vcc
v_writelane_b32 v100, s87, 3
v_mov_b32 v99, v96
s_nop 1
v_max_u32_dpp v99, v99, v99 row_shr:1 row_mask:0xf bank_mask:0xf
s_nop 1
v_max_u32_dpp v99, v99, v99 row_shr:2 row_mask:0xf bank_mask:0xf
s_nop 1
v_max_u32_dpp v99, v99, v99 row_shr:4 row_mask:0xf bank_mask:0xf
s_nop 1
v_max_u32_dpp v99, v99, v99 row_shr:8 row_mask:0xf bank_mask:0xf
s_nop 1
v_max_u32_dpp v99, v99, v99 row_bcast:15 row_mask:0xa bank_mask:0xf
s_nop 1
v_max_u32_dpp v99, v99, v99 row_bcast:31 row_mask:0xc bank_mask:0xf
s_nop 0
v_readlane_b32 s86, v99, 63
s_nop 1
v_cmp_eq_u32 vcc, s86, v96
s_and_b32 s87, s86, 0xffff
s_sub_u32 s87, 2047, s87
v_cndmask_b32 v96, v96, v97, vcc
v_cndmask_b32_e64 v97, v97, 0, vcc
v_writelane_b32 v100, s87, 4
v_mov_b32 v99, v96
s_nop 1
v_max_u32_dpp v99, v99, v99 row_shr:1 row_mask:0xf bank_mask:0xf
s_nop 1
v_max_u32_dpp v99, v99, v99 row_shr:2 row_mask:0xf bank_mask:0xf
s_nop 1
v_max_u32_dpp v99, v99, v99 row_shr:4 row_mask:0xf bank_mask:0xf
s_nop 1
v_max_u32_dpp v99, v99, v99 row_shr:8 row_mask:0xf bank_mask:0xf
s_nop 1
v_max_u32_dpp v99, v99, v99 row_bcast:15 row_mask:0xa bank_mask:0xf
s_nop 1
v_max_u32_dpp v99, v99, v99 row_bcast:31 row_mask:0xc bank_mask:0xf
s_nop 0
v_readlane_b32 s86, v99, 63
s_nop 1
v_cmp_eq_u32 vcc, s86, v96
s_and_b32 s87, s86, 0xffff
s_sub_u32 s87, 2047, s87
v_cndmask_b32 v96, v96, v97, vcc
v_cndmask_b32_e64 v97, v97, 0, vcc
v_writelane_b32 v100, s87, 5
v_mov_b32 v99, v96
s_nop 1
v_max_u32_dpp v99, v99, v99 row_shr:1 row_mask:0xf bank_mask:0xf
s_nop 1
v_max_u32_dpp v99, v99, v99 row_shr:2 row_mask:0xf bank_mask:0xf
s_nop 1
v_max_u32_dpp v99, v99, v99 row_shr:4 row_mask:0xf bank_mask:0xf
s_nop 1
v_max_u32_dpp v99, v99, v99 row_shr:8 row_mask:0xf bank_mask:0xf
s_nop 1
v_max_u32_dpp v99, v99, v99 row_bcast:15 row_mask:0xa bank_mask:0xf
s_nop 1
v_max_u32_dpp v99, v99, v99 row_bcast:31 row_mask:0xc bank_mask:0xf
s_nop 0
v_readlane_b32 s86, v99, 63
s_nop 1
v_cmp_eq_u32 vcc, s86, v96
s_and_b32 s87, s86, 0xffff
s_sub_u32 s87, 2047, s87
v_cndmask_b32 v96, v96, v97, vcc
v_cndmask_b32_e64 v97, v97, 0, vcc
v_writelane_b32 v100, s87, 6
v_mov_b32 v99, v96
s_nop 1
v_max_u32_dpp v99, v99, v99 row_shr:1 row_mask:0xf bank_mask:0xf
s_nop 1
v_max_u32_dpp v99, v99, v99 row_shr:2 row_mask:0xf bank_mask:0xf
s_nop 1
v_max_u32_dpp v99, v99, v99 row_shr:4 row_mask:0xf bank_mask:0xf
s_nop 1
v_max_u32_dpp v99, v99, v99 row_shr:8 row_mask:0xf bank_mask:0xf
s_nop 1
v_max_u32_dpp v99, v99, v99 row_bcast:15 row_mask:0xa bank_mask:0xf
s_nop 1
v_max_u32_dpp v99, v99, v99 row_bcast:31 row_mask:0xc bank_mask:0xf
s_nop 0
v_readlane_b32 s86, v99, 63
s_nop 1
v_cmp_eq_u32 vcc, s86, v96
s_and_b32 s87, s86, 0xffff
s_sub_u32 s87, 2047, s87
v_cndmask_b32 v96, v96, v97, vcc
v_cndmask_b32_e64 v97, v97, 0, vcc
v_writelane_b32 v100, s87, 7
v_mov_b32 v99, v98
s_nop 1
v_max_u32_dpp v99, v99, v99 row_shr:1 row_mask:0xf bank_mask:0xf
s_nop 1
v_max_u32_dpp v99, v99, v99 row_shr:2 row_mask:0xf bank_mask:0xf
s_nop 1
v_max_u32_dpp v99, v99, v99 row_shr:4 row_mask:0xf bank_mask:0xf
s_nop 1
v_max_u32_dpp v99, v99, v99 row_shr:8 row_mask:0xf bank_mask:0xf
s_nop 1
v_max_u32_dpp v99, v99, v99 row_bcast:15 row_mask:0xa bank_mask:0xf
s_nop 1
v_max_u32_dpp v99, v99, v99 row_bcast:31 row_mask:0xc bank_mask:0xf
s_nop 0
v_readlane_b32 s87, v99, 63
s_nop 0
s_cmp_gt_u32 s87, s86
s_cbranch_scc0 .LL_27%=
s_mov_b32 m0, 0
.LL_28%=:
v_max3_u32 v96, v32, v34, v36
v_max3_u32 v96, v96, v38, v40
v_max3_u32 v96, v96, v42, v44
v_max3_u32 v96, v96, v46, v48
v_max3_u32 v96, v96, v50, v52
v_max3_u32 v96, v96, v54, v56
v_max3_u32 v96, v96, v58, v60
v_max3_u32 v96, v96, v62, v64
v_max3_u32 v96, v96, v66, v68
v_max3_u32 v96, v96, v70, v72
v_max3_u32 v96, v96, v74, v76
v_max3_u32 v96, v96, v78, v80
v_max3_u32 v96, v96, v82, v84
v_max3_u32 v96, v96, v86, v88
v_max3_u32 v96, v96, v90, v92
v_max_u32 v96, v96, v94
v_mov_b32 v99, v96
s_nop 1
v_max_u32_dpp v99, v99, v99 row_shr:1 row_mask:0xf bank_mask:0xf
s_nop 1
v_max_u32_dpp v99, v99, v99 row_shr:2 row_mask:0xf bank_mask:0xf
s_nop 1
v_max_u32_dpp v99, v99, v99 row_shr:4 row_mask:0xf bank_mask:0xf
s_nop 1
v_max_u32_dpp v99, v99, v99 row_shr:8 row_mask:0xf bank_mask:0xf
s_nop 1
v_max_u32_dpp v99, v99, v99 row_bcast:15 row_mask:0xa bank_mask:0xf
s_nop 1
v_max_u32_dpp v99, v99, v99 row_bcast:31 row_mask:0xc bank_mask:0xf
s_nop 0
v_readlane_b32 s86, v99, 63
s_nop 1
v_cmp_eq_u32 vcc, s86, v32
s_nop 1
v_cndmask_b32_e64 v32, v32, 0, vcc
v_cmp_eq_u32 vcc, s86, v34
s_nop 1
v_cndmask_b32_e64 v34, v34, 0, vcc
v_cmp_eq_u32 vcc, s86, v36
s_nop 1
v_cndmask_b32_e64 v36, v36, 0, vcc
v_cmp_eq_u32 vcc, s86, v38
s_nop 1
v_cndmask_b32_e64 v38, v38, 0, vcc
v_cmp_eq_u32 vcc, s86, v40
s_nop 1
v_cndmask_b32_e64 v40, v40, 0, vcc
v_cmp_eq_u32 vcc, s86, v42
s_nop 1
v_cndmask_b32_e64 v42, v42, 0, vcc
v_cmp_eq_u32 vcc, s86, v44
s_nop 1
v_cndmask_b32_e64 v44, v44, 0, vcc
v_cmp_eq_u32 vcc, s86, v46
s_nop 1
v_cndmask_b32_e64 v46, v46, 0, vcc
v_cmp_eq_u32 vcc, s86, v48
s_nop 1
v_cndmask_b32_e64 v48, v48, 0, vcc
v_cmp_eq_u32 vcc, s86, v50
s_nop 1
v_cndmask_b32_e64 v50, v50, 0, vcc
v_cmp_eq_u32 vcc, s86, v52
s_nop 1
v_cndmask_b32_e64 v52, v52, 0, vcc
v_cmp_eq_u32 vcc, s86, v54
s_nop 1
v_cndmask_b32_e64 v54, v54, 0, vcc
v_cmp_eq_u32 vcc, s86, v56
s_nop 1
v_cndmask_b32_e64 v56, v56, 0, vcc
v_cmp_eq_u32 vcc, s86, v58
s_nop 1
v_cndmask_b32_e64 v58, v58, 0, vcc
v_cmp_eq_u32 vcc, s86, v60
s_nop 1
v_cndmask_b32_e64 v60, v60, 0, vcc
v_cmp_eq_u32 vcc, s86, v62
s_nop 1
v_cndmask_b32_e64 v62, v62, 0, vcc
v_cmp_eq_u32 vcc, s86, v64
s_nop 1
v_cndmask_b32_e64 v64, v64, 0, vcc
v_cmp_eq_u32 vcc, s86, v66
s_nop 1
v_cndmask_b32_e64 v66, v66, 0, vcc
v_cmp_eq_u32 vcc, s86, v68
s_nop 1
v_cndmask_b32_e64 v68, v68, 0, vcc
v_cmp_eq_u32 vcc, s86, v70
s_nop 1
v_cndmask_b32_e64 v70, v70, 0, vcc
v_cmp_eq_u32 vcc, s86, v72
s_nop 1
v_cndmask_b32_e64 v72, v72, 0, vcc
v_cmp_eq_u32 vcc, s86, v74
s_nop 1
v_cndmask_b32_e64 v74, v74, 0, vcc
v_cmp_eq_u32 vcc, s86, v76
s_nop 1
v_cndmask_b32_e64 v76, v76, 0, vcc
v_cmp_eq_u32 vcc, s86, v78
s_nop 1
v_cndmask_b32_e64 v78, v78, 0, vcc
v_cmp_eq_u32 vcc, s86, v80
s_nop 1
v_cndmask_b32_e64 v80, v80, 0, vcc
v_cmp_eq_u32 vcc, s86, v82
s_nop 1
v_cndmask_b32_e64 v82, v82, 0, vcc
v_cmp_eq_u32 vcc, s86, v84
s_nop 1
v_cndmask_b32_e64 v84, v84, 0, vcc
v_cmp_eq_u32 vcc, s86, v86
s_nop 1
v_cndmask_b32_e64 v86, v86, 0, vcc
v_cmp_eq_u32 vcc, s86, v88
s_nop 1
v_cndmask_b32_e64 v88, v88, 0, vcc
v_cmp_eq_u32 vcc, s86, v90
s_nop 1
v_cndmask_b32_e64 v90, v90, 0, vcc
v_cmp_eq_u32 vcc, s86, v92
s_nop 1
v_cndmask_b32_e64 v92, v92, 0, vcc
v_cmp_eq_u32 vcc, s86, v94
s_nop 1
v_cndmask_b32_e64 v94, v94, 0, vcc
s_and_b32 s87, s86, 0xffff
s_sub_u32 s87, 2047, s87
s_nop 0
v_writelane_b32 v100, s87, m0
s_add_u32 m0, m0, 1
s_cmp_lt_u32 m0, 8
s_cbranch_scc1 .LL_28%=
.LL_27%=:
v_xor_b32 v102, s12, v100
v_mov_b32 v103, s13
s_mov_b64 exec, 0xff
buffer_store_dwordx2 v[102:103], v136, s[8:11], 0 offen offset:3584 sc1
s_mov_b64 exec, -1
s_branch .Lrtr_24%=
.Lspin_49%=:
s_mov_b32 s84, 0
.Llp_51%=:
s_sleep 2
buffer_load_dwordx2 v[8:9], v11, s[8:11], 0 offen offset:3840 sc1
s_waitcnt vmcnt(0)
s_add_u32 s84, s84, 1
v_cmp_eq_u64 vcc, s[12:13], v[8:9]
s_cmp_eq_u64 vcc, -1
s_cbranch_scc1 .Lok_50%=
s_cmp_lt_u32 s84, 1048576
s_cbranch_scc1 .Llp_51%=
s_branch .Lok_50%=
.Lredo%=:
s_waitcnt vmcnt(0) lgkmcnt(0)
s_barrier
s_and_b32 s81, s14, 1
s_mul_i32 s91, s81, 17164288
s_mul_i32 s82, s56, 536576
s_add_u32 s91, s91, s82
s_add_u32 s91, s91, 4194304
s_lshr_b32 s82, s14, 1
s_lshl_b32 s83, s82, 14
s_add_u32 s91, s91, s83
s_lshl_b32 s81, s81, 14
s_lshl_b32 s82, s82, 16
s_or_b32 s92, s81, s82
s_xor_b32 s92, s92, 0x10000
s_add_u32 m0, s92, 0
s_add_u32 s81, s91, 0
Dc 1,8,81,0
Dc 1,8,81,1024
Dc 1,8,81,2048
Dc 1,8,81,3072
s_add_u32 m0, s92, 4096
s_add_u32 s81, s91, 4096
Dc 1,8,81,0
Dc 1,8,81,1024
Dc 1,8,81,2048
Dc 1,8,81,3072
s_add_u32 m0, s92, 8192
s_add_u32 s81, s91, 8192
Dc 1,8,81,0
Dc 1,8,81,1024
Dc 1,8,81,2048
Dc 1,8,81,3072
s_add_u32 m0, s92, 12288
s_add_u32 s81, s91, 12288
Dc 1,8,81,0
Dc 1,8,81,1024
Dc 1,8,81,2048
Dc 1,8,81,3072
v_bfe_u32 v244, v1, 4, 1
v_mul_u32_u24 v244, 0xaaaa, v244
v_add_u32 v244, 0x4444, v244
v_lshl_or_b32 v240, v244, 16, v244
v_mov_b32 v241, 0xff800000
v_add_u32 v242, 0x10000, v1
v_bfe_u32 v244, v1, 4, 4
v_lshrrev_b32 v245, 8, v1
v_lshlrev_b32 v245, 2, v245
v_sub_u32 v244, v244, v245
v_cmp_le_i32_e64 s[72:73], 0, v244
v_cmp_le_i32_e64 s[74:75], 1, v244
v_cmp_le_i32_e64 s[76:77], 2, v244
v_cmp_le_i32_e64 s[78:79], 3, v244
v_add_u32 v243, 4096, v1
v_add_u32 v244, 8192, v1
v_add_u32 v245, 12288, v1
s_mov_b32 s18, 0x3f803f80
s_mov_b32 s19, 0x3f003f00
v_accvgpr_write_b32 a176, s18
v_accvgpr_write_b32 a180, s19
v_mov_b32 v252, v241
v_accvgpr_write_b32 a177, s18
v_accvgpr_write_b32 a181, s19
v_mov_b32 v253, v241
v_accvgpr_write_b32 a178, s18
v_accvgpr_write_b32 a182, s19
v_mov_b32 v254, v241
v_accvgpr_write_b32 a179, s18
v_accvgpr_write_b32 a183, s19
v_mov_b32 v255, v241
v_accvgpr_write_b32 a160, 0
v_accvgpr_write_b32 a161, 0
v_accvgpr_write_b32 a162, 0
v_accvgpr_write_b32 a163, 0
v_accvgpr_write_b32 a164, 0
v_accvgpr_write_b32 a165, 0
v_accvgpr_write_b32 a166, 0
)"
R"(v_accvgpr_write_b32 a167, 0
v_accvgpr_write_b32 a168, 0
v_accvgpr_write_b32 a169, 0
v_accvgpr_write_b32 a170, 0
v_accvgpr_write_b32 a171, 0
v_accvgpr_write_b32 a172, 0
v_accvgpr_write_b32 a173, 0
v_accvgpr_write_b32 a174, 0
v_accvgpr_write_b32 a175, 0
s_mov_b32 s90, 0x5d800000
s_mov_b64 s[88:89], 0
s_mov_b32 s64, 0
s_and_b32 s18, s14, 1
s_lshl_b32 s62, s18, 14
s_lshl_b32 s20, s57, 15
s_sub_u32 s20, 507904, s20
s_add_u32 s20, s20, s62
s_lshl_b32 s65, s57, 1
s_sub_u32 s65, s65, 1
s_max_i32 s65, s65, 0
s_sub_u32 s66, s60, 1
s_cmp_lt_u32 s14, 2
s_cselect_b32 s20, s20, 0
s_cselect_b32 s66, s66, 15
s_cselect_b32 s21, 32768, 0
s_add_u32 s61, s91, s20
s_add_u32 s62, s62, s21
s_sub_u32 s63, 16384, s21
s_lshl_b32 s67, s60, 7
s_add_u32 s67, s67, s56
s_lshl_b32 s67, s67, 12
s_add_u32 s67, s67, 39845888
s_lshl_b32 s69, s60, 3
s_add_u32 s69, s69, s59
s_lshl_b32 s69, s69, 6
s_lshl_b32 s21, s58, 3
s_add_u32 s69, s69, s21
s_add_u32 s69, s69, 20480
s_mov_b32 s70, 0
s_mov_b32 s37, 0
v_bfe_u32 v28, v1, 4, 4
v_lshrrev_b32 v29, 8, v1
v_lshlrev_b32 v29, 2, v29
v_sub_u32 v28, v28, v29
s_mov_b64 s[24:25], 0
s_mov_b32 s68, 0
s_mov_b32 s85, 99
s_lshl_b32 s80, s60, 6
s_and_b32 s81, s14, 2
s_lshl_b32 s81, s81, 4
s_xor_b32 s82, s81, 0
s_add_u32 s82, s82, s80
v_add_u32 v4, s82, v28
s_xor_b32 s82, s81, 16
s_add_u32 s82, s82, s80
v_add_u32 v5, s82, v28
s_xor_b32 s82, s81, 32
s_add_u32 s82, s82, s80
v_add_u32 v6, s82, v28
s_xor_b32 s82, s81, 48
s_add_u32 s82, s82, s80
v_add_u32 v7, s82, v28
s_mul_i32 s82, s56, 536576
s_add_u32 s82, s82, 4194304
s_lshl_b32 s84, s60, 1
s_add_u32 s84, s84, 2
.LL_52%=:
buffer_load_dwordx4 a[128:131], v1, s[8:11], s82 offen offset:0 sc1
buffer_load_dwordx4 a[132:135], v1, s[8:11], s82 offen offset:1024 sc1
buffer_load_dwordx4 a[136:139], v1, s[8:11], s82 offen offset:2048 sc1
buffer_load_dwordx4 a[140:143], v1, s[8:11], s82 offen offset:3072 sc1
buffer_load_dwordx4 a[144:147], v243, s[8:11], s82 offen offset:0 sc1
buffer_load_dwordx4 a[148:151], v243, s[8:11], s82 offen offset:1024 sc1
buffer_load_dwordx4 a[152:155], v243, s[8:11], s82 offen offset:2048 sc1
buffer_load_dwordx4 a[156:159], v243, s[8:11], s82 offen offset:3072 sc1
s_waitcnt vmcnt(0)
Zvaa 160,128,0
Zvaa 164,128,16
Zvaa 168,128,32
Zvaa 172,128,48
Mvaa 160,132,4
Mvaa 164,132,20
Mvaa 168,132,36
Mvaa 172,132,52
Mvaa 160,136,8
Mvaa 164,136,24
Mvaa 168,136,40
Mvaa 172,136,56
Mvaa 160,140,12
Mvaa 164,140,28
Mvaa 168,140,44
Mvaa 172,140,60
Zvaa 176,144,0
Zvaa 180,144,16
Zvaa 184,144,32
Zvaa 188,144,48
Mvaa 176,148,4
Mvaa 180,148,20
Mvaa 184,148,36
Mvaa 188,148,52
Mvaa 176,152,8
Mvaa 180,152,24
Mvaa 184,152,40
Mvaa 188,152,56
Mvaa 176,156,12
Mvaa 180,156,28
Mvaa 184,156,44
Mvaa 188,156,60
v_cmp_le_i32 vcc, 0, v4
s_nop 1
v_cndmask_b32 v160, v241, v160, vcc
v_max_f32 v252, v252, v160
v_cmp_le_i32 vcc, 1, v4
s_nop 1
v_cndmask_b32 v161, v241, v161, vcc
v_max_f32 v252, v252, v161
v_cmp_le_i32 vcc, 2, v4
s_nop 1
v_cndmask_b32 v162, v241, v162, vcc
v_max_f32 v252, v252, v162
v_cmp_le_i32 vcc, 3, v4
s_nop 1
v_cndmask_b32 v163, v241, v163, vcc
v_max_f32 v252, v252, v163
v_cmp_le_i32 vcc, 0, v5
s_nop 1
v_cndmask_b32 v164, v241, v164, vcc
v_max_f32 v253, v253, v164
v_cmp_le_i32 vcc, 1, v5
s_nop 1
v_cndmask_b32 v165, v241, v165, vcc
v_max_f32 v253, v253, v165
v_cmp_le_i32 vcc, 2, v5
s_nop 1
v_cndmask_b32 v166, v241, v166, vcc
v_max_f32 v253, v253, v166
v_cmp_le_i32 vcc, 3, v5
s_nop 1
v_cndmask_b32 v167, v241, v167, vcc
v_max_f32 v253, v253, v167
v_cmp_le_i32 vcc, 0, v6
s_nop 1
v_cndmask_b32 v168, v241, v168, vcc
v_max_f32 v254, v254, v168
v_cmp_le_i32 vcc, 1, v6
s_nop 1
v_cndmask_b32 v169, v241, v169, vcc
v_max_f32 v254, v254, v169
v_cmp_le_i32 vcc, 2, v6
s_nop 1
v_cndmask_b32 v170, v241, v170, vcc
v_max_f32 v254, v254, v170
v_cmp_le_i32 vcc, 3, v6
s_nop 1
v_cndmask_b32 v171, v241, v171, vcc
v_max_f32 v254, v254, v171
v_cmp_le_i32 vcc, 0, v7
s_nop 1
v_cndmask_b32 v172, v241, v172, vcc
v_max_f32 v255, v255, v172
v_cmp_le_i32 vcc, 1, v7
s_nop 1
v_cndmask_b32 v173, v241, v173, vcc
v_max_f32 v255, v255, v173
v_cmp_le_i32 vcc, 2, v7
s_nop 1
v_cndmask_b32 v174, v241, v174, vcc
v_max_f32 v255, v255, v174
v_cmp_le_i32 vcc, 3, v7
s_nop 1
v_cndmask_b32 v175, v241, v175, vcc
v_max_f32 v255, v255, v175
v_cmp_le_i32 vcc, 16, v4
s_nop 1
v_cndmask_b32 v176, v241, v176, vcc
v_max_f32 v252, v252, v176
v_cmp_le_i32 vcc, 17, v4
s_nop 1
v_cndmask_b32 v177, v241, v177, vcc
v_max_f32 v252, v252, v177
v_cmp_le_i32 vcc, 18, v4
s_nop 1
v_cndmask_b32 v178, v241, v178, vcc
v_max_f32 v252, v252, v178
v_cmp_le_i32 vcc, 19, v4
s_nop 1
v_cndmask_b32 v179, v241, v179, vcc
v_max_f32 v252, v252, v179
v_cmp_le_i32 vcc, 16, v5
s_nop 1
v_cndmask_b32 v180, v241, v180, vcc
v_max_f32 v253, v253, v180
v_cmp_le_i32 vcc, 17, v5
s_nop 1
v_cndmask_b32 v181, v241, v181, vcc
v_max_f32 v253, v253, v181
v_cmp_le_i32 vcc, 18, v5
s_nop 1
v_cndmask_b32 v182, v241, v182, vcc
v_max_f32 v253, v253, v182
v_cmp_le_i32 vcc, 19, v5
s_nop 1
v_cndmask_b32 v183, v241, v183, vcc
v_max_f32 v253, v253, v183
v_cmp_le_i32 vcc, 16, v6
s_nop 1
v_cndmask_b32 v184, v241, v184, vcc
v_max_f32 v254, v254, v184
v_cmp_le_i32 vcc, 17, v6
s_nop 1
v_cndmask_b32 v185, v241, v185, vcc
v_max_f32 v254, v254, v185
v_cmp_le_i32 vcc, 18, v6
s_nop 1
v_cndmask_b32 v186, v241, v186, vcc
v_max_f32 v254, v254, v186
v_cmp_le_i32 vcc, 19, v6
s_nop 1
v_cndmask_b32 v187, v241, v187, vcc
v_max_f32 v254, v254, v187
v_cmp_le_i32 vcc, 16, v7
s_nop 1
v_cndmask_b32 v188, v241, v188, vcc
v_max_f32 v255, v255, v188
v_cmp_le_i32 vcc, 17, v7
s_nop 1
v_cndmask_b32 v189, v241, v189, vcc
v_max_f32 v255, v255, v189
v_cmp_le_i32 vcc, 18, v7
s_nop 1
v_cndmask_b32 v190, v241, v190, vcc
v_max_f32 v255, v255, v190
v_cmp_le_i32 vcc, 19, v7
s_nop 1
v_cndmask_b32 v191, v241, v191, vcc
v_max_f32 v255, v255, v191
v_subrev_u32 v4, 32, v4
v_subrev_u32 v5, 32, v5
v_subrev_u32 v6, 32, v6
v_subrev_u32 v7, 32, v7
s_add_u32 s82, s82, 8192
s_sub_u32 s84, s84, 1
s_cmp_lg_u32 s84, 0
s_cbranch_scc1 .LL_52%=
G 28,0
v_lshlrev_b32 v32, 16, v28
v_and_b32 v33, 0xffff0000, v28
G 28,1
v_lshlrev_b32 v34, 16, v28
v_and_b32 v35, 0xffff0000, v28
G 28,2
v_lshlrev_b32 v36, 16, v28
v_and_b32 v37, 0xffff0000, v28
G 28,3
v_lshlrev_b32 v38, 16, v28
v_and_b32 v39, 0xffff0000, v28
G 28,4
v_lshlrev_b32 v40, 16, v28
v_and_b32 v41, 0xffff0000, v28
G 28,5
v_lshlrev_b32 v42, 16, v28
v_and_b32 v43, 0xffff0000, v28
G 28,6
v_lshlrev_b32 v44, 16, v28
v_and_b32 v45, 0xffff0000, v28
G 28,7
v_lshlrev_b32 v46, 16, v28
v_and_b32 v47, 0xffff0000, v28
G 28,8
v_lshlrev_b32 v48, 16, v28
v_and_b32 v49, 0xffff0000, v28
G 28,9
v_lshlrev_b32 v50, 16, v28
v_and_b32 v51, 0xffff0000, v28
G 28,10
v_lshlrev_b32 v52, 16, v28
v_and_b32 v53, 0xffff0000, v28
G 28,11
v_lshlrev_b32 v54, 16, v28
v_and_b32 v55, 0xffff0000, v28
G 28,12
v_lshlrev_b32 v56, 16, v28
v_and_b32 v57, 0xffff0000, v28
G 28,13
v_lshlrev_b32 v58, 16, v28
v_and_b32 v59, 0xffff0000, v28
G 28,14
v_lshlrev_b32 v60, 16, v28
v_and_b32 v61, 0xffff0000, v28
G 28,15
v_lshlrev_b32 v62, 16, v28
v_and_b32 v63, 0xffff0000, v28
G 28,16
v_lshlrev_b32 v64, 16, v28
v_and_b32 v65, 0xffff0000, v28
G 28,17
v_lshlrev_b32 v66, 16, v28
v_and_b32 v67, 0xffff0000, v28
G 28,18
v_lshlrev_b32 v68, 16, v28
v_and_b32 v69, 0xffff0000, v28
G 28,19
v_lshlrev_b32 v70, 16, v28
v_and_b32 v71, 0xffff0000, v28
G 28,20
v_lshlrev_b32 v72, 16, v28
v_and_b32 v73, 0xffff0000, v28
G 28,21
v_lshlrev_b32 v74, 16, v28
v_and_b32 v75, 0xffff0000, v28
G 28,22
v_lshlrev_b32 v76, 16, v28
v_and_b32 v77, 0xffff0000, v28
G 28,23
v_lshlrev_b32 v78, 16, v28
v_and_b32 v79, 0xffff0000, v28
G 28,24
v_lshlrev_b32 v80, 16, v28
v_and_b32 v81, 0xffff0000, v28
G 28,25
v_lshlrev_b32 v82, 16, v28
v_and_b32 v83, 0xffff0000, v28
G 28,26
v_lshlrev_b32 v84, 16, v28
v_and_b32 v85, 0xffff0000, v28
G 28,27
v_lshlrev_b32 v86, 16, v28
v_and_b32 v87, 0xffff0000, v28
G 28,28
v_lshlrev_b32 v88, 16, v28
v_and_b32 v89, 0xffff0000, v28
G 28,29
v_lshlrev_b32 v90, 16, v28
v_and_b32 v91, 0xffff0000, v28
G 28,30
v_lshlrev_b32 v92, 16, v28
v_and_b32 v93, 0xffff0000, v28
G 28,31
v_lshlrev_b32 v94, 16, v28
v_and_b32 v95, 0xffff0000, v28
G 28,32
v_lshlrev_b32 v96, 16, v28
v_and_b32 v97, 0xffff0000, v28
G 28,33
v_lshlrev_b32 v98, 16, v28
v_and_b32 v99, 0xffff0000, v28
G 28,34
v_lshlrev_b32 v100, 16, v28
v_and_b32 v101, 0xffff0000, v28
G 28,35
v_lshlrev_b32 v102, 16, v28
v_and_b32 v103, 0xffff0000, v28
G 28,36
v_lshlrev_b32 v104, 16, v28
v_and_b32 v105, 0xffff0000, v28
G 28,37
v_lshlrev_b32 v106, 16, v28
v_and_b32 v107, 0xffff0000, v28
G 28,38
v_lshlrev_b32 v108, 16, v28
v_and_b32 v109, 0xffff0000, v28
G 28,39
v_lshlrev_b32 v110, 16, v28
v_and_b32 v111, 0xffff0000, v28
G 28,40
v_lshlrev_b32 v112, 16, v28
v_and_b32 v113, 0xffff0000, v28
G 28,41
v_lshlrev_b32 v114, 16, v28
v_and_b32 v115, 0xffff0000, v28
G 28,42
v_lshlrev_b32 v116, 16, v28
v_and_b32 v117, 0xffff0000, v28
G 28,43
v_lshlrev_b32 v118, 16, v28
v_and_b32 v119, 0xffff0000, v28
G 28,44
v_lshlrev_b32 v120, 16, v28
v_and_b32 v121, 0xffff0000, v28
G 28,45
v_lshlrev_b32 v122, 16, v28
v_and_b32 v123, 0xffff0000, v28
G 28,46
v_lshlrev_b32 v124, 16, v28
v_and_b32 v125, 0xffff0000, v28
G 28,47
v_lshlrev_b32 v126, 16, v28
v_and_b32 v127, 0xffff0000, v28
G 28,48
v_lshlrev_b32 v128, 16, v28
v_and_b32 v129, 0xffff0000, v28
G 28,49
v_lshlrev_b32 v130, 16, v28
v_and_b32 v131, 0xffff0000, v28
G 28,50
v_lshlrev_b32 v132, 16, v28
v_and_b32 v133, 0xffff0000, v28
G 28,51
v_lshlrev_b32 v134, 16, v28
v_and_b32 v135, 0xffff0000, v28
)"
R"(G 28,52
v_lshlrev_b32 v136, 16, v28
v_and_b32 v137, 0xffff0000, v28
G 28,53
v_lshlrev_b32 v138, 16, v28
v_and_b32 v139, 0xffff0000, v28
G 28,54
v_lshlrev_b32 v140, 16, v28
v_and_b32 v141, 0xffff0000, v28
G 28,55
v_lshlrev_b32 v142, 16, v28
v_and_b32 v143, 0xffff0000, v28
G 28,56
v_lshlrev_b32 v144, 16, v28
v_and_b32 v145, 0xffff0000, v28
G 28,57
v_lshlrev_b32 v146, 16, v28
v_and_b32 v147, 0xffff0000, v28
G 28,58
v_lshlrev_b32 v148, 16, v28
v_and_b32 v149, 0xffff0000, v28
G 28,59
v_lshlrev_b32 v150, 16, v28
v_and_b32 v151, 0xffff0000, v28
G 28,60
v_lshlrev_b32 v152, 16, v28
v_and_b32 v153, 0xffff0000, v28
G 28,61
v_lshlrev_b32 v154, 16, v28
v_and_b32 v155, 0xffff0000, v28
G 28,62
v_lshlrev_b32 v156, 16, v28
v_and_b32 v157, 0xffff0000, v28
G 28,63
v_lshlrev_b32 v158, 16, v28
v_and_b32 v159, 0xffff0000, v28
s_mov_b32 s22, 1.0
s_mov_b32 s23, 1.0
s_mov_b32 s29, 134217728
s_mov_b32 s71, 3584
s_barrier
Ra 128,242,0
Ra 132,242,1024
Ra 136,242,2048
Ra 140,242,3072
s_cmp_ge_u32 s14, 2
s_cbranch_scc1 .Lbgo%=
s_branch .Lago%=
.Lpb_29%=:
s_waitcnt vmcnt(6) lgkmcnt(6)
Zvaa 32,0,32
s_add_u32 s28, s28, 512
Ta 20,4,8192
Ta 22,4,9216
Mvaa 32,4,48
s_cmp_eq_u32 s28, s4
s_cselect_b32 s93, 10240, 0
Ta 24,5,0
Ta 26,5,1024
Zvaa 64,4,52
s_sub_u32 s28, s28, s93
s_add_u32 s93, s28, 20480
buffer_load_dwordx4 a[128:131], v26, s[8:11], s28 offen offset:0 sc1
Mvaa 64,0,36
Ta 28,5,8192
Ta 30,5,9216
Zvaa 96,0,40
K 184,184,185
K 185,186,187
Mvaa 96,4,56
K 186,188,189
K 187,190,191
Zvaa 128,4,60
buffer_load_dwordx4 a[132:135], v26, s[8:11], s93 offen offset:0 sc1
K 192,192,193
Mvaa 128,0,44
W 8
Ta 0,6,0
K 193,194,195
Zvaa 132,8,44
Ta 2,6,1024
K 194,196,197
Mvaa 132,12,60
K 195,198,199
K 200,200,201
Zvaa 100,12,56
buffer_load_dwordx4 a[136:139], v27, s[8:11], s28 offen offset:0 sc1
K 201,202,203
Mvaa 100,8,40
Ta 4,6,8192
Ta 6,6,9216
Zvaa 68,8,36
K 202,204,205
K 203,206,207
Mvaa 68,12,52
K 208,208,209
K 209,210,211
Zvaa 36,12,48
buffer_load_dwordx4 a[140:143], v27, s[8:11], s93 offen offset:0 sc1
K 210,212,213
Mvaa 36,8,32
W 8
s_add_u32 s28, s28, 512
s_cmp_eq_u32 s28, s4
s_cselect_b32 s93, 10240, 0
Zvaa 40,16,32
s_sub_u32 s28, s28, s93
s_add_u32 s93, s28, 20480
K 211,214,215
Mvaa 40,20,48
Ta 8,7,0
Ta 10,7,1024
Zvaa 72,20,52
buffer_load_dwordx4 a[144:147], v26, s[8:11], s28 offen offset:0 sc1
K 216,216,217
Mvaa 72,16,36
K 217,218,219
K 218,220,221
K 219,222,223
Zvaa 104,16,40
Ta 12,7,8192
Ta 14,7,9216
Mvaa 104,20,56
K 224,224,225
K 225,226,227
Zvaa 136,20,60
K 226,228,229
K 227,230,231
Mvaa 136,16,44
W 8
buffer_load_dwordx4 a[148:151], v26, s[8:11], s93 offen offset:0 sc1
K 232,232,233
Zvaa 140,24,44
K 233,234,235
K 234,236,237
Mvaa 140,28,60
Ta 16,8,0
K 235,238,239
Zvaa 108,28,56
Ta 18,8,1024
K 240,240,241
Mvaa 108,24,40
buffer_load_dwordx4 a[152:155], v27, s[8:11], s28 offen offset:0 sc1
K 241,242,243
Zvaa 76,24,36
K 242,244,245
K 243,246,247
Mvaa 76,28,52
Ta 20,8,8192
Ta 22,8,9216
Zvaa 44,28,48
K 248,248,249
K 249,250,251
Mvaa 44,24,32
K 250,252,253
K 251,254,255
W 8
Zvaa 48,0,32
buffer_load_dwordx4 a[156:159], v27, s[8:11], s93 offen offset:0 sc1
Ta 24,9,0
Mvaa 48,4,48
s_add_u32 s28, s28, 512
s_cmp_eq_u32 s28, s4
s_cselect_b32 s93, 10240, 0
s_sub_u32 s28, s28, s93
Zvaa 80,4,52
s_add_u32 s93, s28, 20480
Ta 26,9,1024
K 160,160,161
Mvaa 80,0,36
K 161,162,163
K 162,164,165
Zvaa 112,0,40
buffer_load_dwordx4 v[188:191], v26, s[8:11], s28 offen offset:0 sc1
K 163,166,167
Mvaa 112,4,56
Ta 28,9,8192
Ta 30,9,9216
Zvaa 144,4,60
K 168,168,169
K 169,170,171
Mvaa 144,0,44
W 8
K 170,172,173
K 171,174,175
Zvaa 148,8,44
buffer_load_dwordx4 v[196:199], v26, s[8:11], s93 offen offset:0 sc1
G 164,184
Mvaa 148,12,60
G 165,185
G 166,186
Zvaa 116,12,56
Ta 0,2,16384
Ta 2,2,17408
Mvaa 116,8,40
G 167,187
G 172,188
Zvaa 84,8,36
buffer_load_dwordx4 v[204:207], v27, s[8:11], s28 offen offset:0 sc1
Ta 4,2,24576
Mvaa 84,12,52
Ta 6,2,25600
G 173,189
Zvaa 52,12,48
G 174,190
G 175,191
Mvaa 52,8,32
W 8
Ta 8,3,16384
K 164,164,165
Zvaa 56,16,32
buffer_load_dwordx4 v[212:215], v27, s[8:11], s93 offen offset:0 sc1
Ta 10,3,17408
Mvaa 56,20,48
s_add_u32 s28, s28, 512
s_cmp_eq_u32 s28, s4
s_cselect_b32 s93, 10240, 0
s_sub_u32 s28, s28, s93
Zvaa 88,20,52
s_add_u32 s93, s28, 20480
K 165,166,167
K 166,172,173
Mvaa 88,16,36
Ta 12,3,24576
K 167,174,175
Zvaa 120,16,40
buffer_load_dwordx4 v[220:223], v26, s[8:11], s28 offen offset:0 sc1
Ta 14,3,25600
Mvaa 120,20,56
v_accvgpr_write_b32 a184, v164
v_accvgpr_write_b32 a185, v165
Zvaa 152,20,60
v_accvgpr_write_b32 a186, v166
Mvaa 152,16,44
W 8
Ta 16,4,16384
v_accvgpr_write_b32 a187, v167
Zvaa 156,24,44
buffer_load_dwordx4 v[228:231], v26, s[8:11], s93 offen offset:0 sc1
Ta 18,4,17408
Mvaa 156,28,60
v_accvgpr_write_b32 a188, v240
Zvaa 124,28,56
Ta 20,4,24576
v_accvgpr_write_b32 a189, v241
Mvaa 124,24,40
Ta 22,4,25600
v_accvgpr_write_b32 a190, v242
Zvaa 92,24,36
buffer_load_dwordx4 v[236:239], v27, s[8:11], s28 offen offset:0 sc1
v_accvgpr_write_b32 a191, v243
Mvaa 92,28,52
G 164,192
Zvaa 60,28,48
Mvaa 60,24,32
s_waitcnt vmcnt(7) lgkmcnt(8)
Ta 24,5,16384
G 165,193
Mvaa 32,0,128
buffer_load_dwordx4 v[244:247], v27, s[8:11], s93 offen offset:0 sc1
Ta 26,5,17408
Mvaa 32,4,144
s_add_u32 s28, s28, 512
s_cmp_eq_u32 s28, s4
s_cselect_b32 s93, 10240, 0
s_sub_u32 s28, s28, s93
Mvaa 64,4,148
s_add_u32 s93, s28, 20480
G 166,194
G 167,195
Mvaa 64,0,132
Ta 28,5,24576
Ta 30,5,25600
Mvaa 96,0,136
D 22,48,65,2048
G 172,196
Mvaa 96,4,152
G 173,197
G 174,198
Mvaa 128,4,156
G 175,199
Mvaa 128,0,140
W 8
Ta 0,6,16384
Ta 2,6,17408
Mvaa 132,8,140
buffer_load_dwordx4 a[32:35], v26, s[8:11], s28 offen offset:0 sc1
K 164,164,165
Mvaa 132,12,156
K 165,166,167
Mvaa 100,12,152
Ta 4,6,24576
K 166,172,173
Mvaa 100,8,136
Ta 6,6,25600
K 167,174,175
Mvaa 68,8,132
buffer_load_dwordx4 a[36:39], v26, s[8:11], s93 offen offset:0 sc1
v_accvgpr_write_b32 a192, v164
Mvaa 68,12,148
Mvaa 36,12,144
v_accvgpr_write_b32 a193, v165
Mvaa 36,8,128
W 8
Ta 8,7,16384
v_accvgpr_write_b32 a194, v166
Mvaa 40,16,128
buffer_load_dwordx4 a[40:43], v27, s[8:11], s28 offen offset:0 sc1
Ta 10,7,17408
Mvaa 40,20,144
v_accvgpr_write_b32 a195, v167
v_accvgpr_write_b32 a196, v248
Mvaa 72,20,148
Ta 12,7,24576
Ta 14,7,25600
Mvaa 72,16,132
v_accvgpr_write_b32 a197, v249
Mvaa 104,16,136
buffer_load_dwordx4 a[44:47], v27, s[8:11], s93 offen offset:0 sc1
v_accvgpr_write_b32 a198, v250
Mvaa 104,20,152
s_add_u32 s28, s28, 512
s_cmp_eq_u32 s28, s4
s_cselect_b32 s93, 10240, 0
s_sub_u32 s28, s28, s93
Mvaa 136,20,156
s_add_u32 s93, s28, 20480
v_accvgpr_write_b32 a199, v251
G 164,200
Mvaa 136,16,140
W 8
Ta 16,8,16384
Ta 18,8,17408
Mvaa 140,24,140
buffer_load_dwordx4 a[48:51], v26, s[8:11], s28 offen offset:0 sc1
G 165,201
Mvaa 140,28,156
G 166,202
Mvaa 108,28,152
Ta 20,8,24576
G 167,203
Mvaa 108,24,136
Ta 22,8,25600
G 172,204
Mvaa 76,24,132
buffer_load_dwordx4 a[52:55], v26, s[8:11], s93 offen offset:0 sc1
Mvaa 76,28,148
G 173,205
Mvaa 44,28,144
G 174,206
Mvaa 44,24,128
W 8
Ta 24,9,16384
G 175,207
Mvaa 48,0,128
buffer_load_dwordx4 a[56:59], v27, s[8:11], s28 offen offset:0 sc1
Ta 26,9,17408
Mvaa 48,4,144
K 164,164,165
Mvaa 80,4,148
Ta 28,9,24576
K 165,166,167
Mvaa 80,0,132
Ta 30,9,25600
K 166,172,173
Mvaa 112,0,136
s_lshl_b32 s80, s56, 9
buffer_load_dwordx4 a[60:63], v27, s[8:11], s93 offen offset:0 sc1
K 167,174,175
Mvaa 112,4,152
v_accvgpr_write_b32 a200, v164
Mvaa 144,4,156
s_lshl_b32 s81, s57, 4
Mvaa 144,0,140
W 8
s_add_u32 s80, s80, s81
v_accvgpr_write_b32 a201, v165
Mvaa 148,8,140
D 23,48,65,3072
Ta 0,10,0
Mvaa 148,12,156
s_add_u32 s65, s5, 16
s_cmp_ge_u32 s65, 20
s_cselect_b32 s68, 20, 0
s_sub_u32 s65, s65, s68
Mvaa 116,12,152
s_lshl_b32 s65, s65, 18
s_add_u32 m0, s63, 98304
Ta 2,10,1024
Mvaa 116,8,136
s_and_b32 s81, s14, 1
v_accvgpr_write_b32 a202, v166
v_accvgpr_write_b32 a203, v167
Mvaa 84,8,132
s_lshl_b32 s81, s81, 3
D 16,48,65,0
Ta 4,10,8192
Mvaa 84,12,148
s_add_u32 s80, s80, s81
Ta 6,10,9216
)"
R"(v_accvgpr_write_b32 a204, v184
Mvaa 52,12,144
s_add_u32 s71, s80, 4096
v_accvgpr_write_b32 a205, v185
v_accvgpr_write_b32 a206, v186
Mvaa 52,8,128
W 8
Ta 8,11,0
v_accvgpr_write_b32 a207, v187
Mvaa 56,16,128
Ta 10,11,1024
G 164,208
Mvaa 56,20,144
G 165,209
Mvaa 88,20,148
Ta 12,11,8192
G 166,210
Mvaa 88,16,132
Ta 14,11,9216
G 167,211
Mvaa 120,16,136
Mvaa 120,20,152
G 172,212
Mvaa 152,20,156
G 173,213
Mvaa 152,16,140
W 8
Ta 16,12,0
G 174,214
Mvaa 156,24,140
s_add_u32 s28, s28, 512
s_cmp_eq_u32 s28, s4
s_cselect_b32 s93, 10240, 0
Ta 18,12,1024
Mvaa 156,28,156
s_sub_u32 s28, s28, s93
s_add_u32 s93, s28, 20480
buffer_load_dwordx4 v[184:187], v26, s[8:11], s28 offen offset:0 sc1
Mvaa 124,28,152
s_lshr_b32 s80, s15, 3
s_lshl_b32 s80, s80, 1
G 175,215
Mvaa 124,24,136
s_add_u32 s80, s80, s14
Ta 20,12,8192
K 164,164,165
Mvaa 92,24,132
s_sub_u32 s80, s80, 2
Ta 22,12,9216
K 165,166,167
Mvaa 92,28,148
s_lshl_b32 s80, s80, 12
D 17,48,65,1024
K 166,172,173
Mvaa 60,28,144
K 167,174,175
v_min_u32 v28, 496, v1
Mvaa 60,24,128
s_waitcnt vmcnt(13) lgkmcnt(8)
v_accvgpr_write_b32 a208, v164
v_lshl_add_u32 v28, v28, 3, s80
Mvav 32,0,188
Ta 24,13,0
Ta 26,13,1024
Mvav 32,4,220
buffer_load_dwordx4 a[128:131], v26, s[8:11], s93 offen offset:0 sc1
v_min_u32 v28, .Lend%=-.Lbeg%=-4, v28
Mvav 64,4,228
v_accvgpr_write_b32 a209, v165
v_accvgpr_write_b32 a210, v166
Mvav 64,0,196
Ta 28,13,8192
Ta 30,13,9216
Mvav 96,0,204
v_accvgpr_write_b32 a211, v167
v_accvgpr_write_b32 a212, v192
Mvav 96,4,236
buffer_load_dwordx4 a[132:135], v27, s[8:11], s28 offen offset:0 sc1
Mvav 128,4,244
v_accvgpr_write_b32 a213, v193
Mvav 128,0,212
W 8
Ta 0,14,0
v_accvgpr_write_b32 a214, v194
Mvav 132,8,212
Ta 2,14,1024
v_add_u32 v2, 65536, v10
Mvav 132,12,244
buffer_load_dwordx4 a[136:139], v27, s[8:11], s93 offen offset:0 sc1
v_accvgpr_write_b32 a215, v195
Mvav 100,12,236
s_add_u32 s28, s28, 512
s_cmp_eq_u32 s28, s4
s_cselect_b32 s93, 10240, 0
s_sub_u32 s28, s28, s93
Mvav 100,8,204
s_add_u32 s93, s28, 20480
Ta 4,14,8192
G 164,216
Mvav 68,8,196
Ta 6,14,9216
v_add_u32 v3, 65536, v11
Mvav 68,12,228
buffer_load_dwordx4 a[140:143], v26, s[8:11], s28 offen offset:0 sc1
G 165,217
Mvav 36,12,220
G 166,218
G 167,219
Mvav 36,8,188
W 8
Ta 8,15,0
G 172,220
Mvav 40,16,188
Ta 10,15,1024
v_add_u32 v4, 65536, v12
Mvav 40,20,220
buffer_load_dwordx4 a[144:147], v26, s[8:11], s93 offen offset:0 sc1
G 173,221
Mvav 72,20,228
G 174,222
G 175,223
Mvav 72,16,196
Ta 12,15,8192
Ta 14,15,9216
Mvav 104,16,204
v_add_u32 v5, 65536, v13
K 164,164,165
Mvav 104,20,236
buffer_load_dwordx4 a[148:151], v27, s[8:11], s28 offen offset:0 sc1
K 165,166,167
Mvav 136,20,244
K 166,172,173
Mvav 136,16,212
W 8
Ta 16,24,0
K 167,174,175
Mvav 140,24,212
Ta 18,24,1024
v_add_u32 v6, 65536, v14
Mvav 140,28,244
buffer_load_dwordx4 a[152:155], v27, s[8:11], s93 offen offset:0 sc1
v_accvgpr_write_b32 a216, v164
Mvav 108,28,236
s_add_u32 s28, s28, 512
s_cmp_eq_u32 s28, s4
s_cselect_b32 s93, 10240, 0
s_sub_u32 s28, s28, s93
Mvav 108,24,204
s_add_u32 s93, s28, 20480
Ta 20,24,8192
v_accvgpr_write_b32 a217, v165
Mvav 76,24,196
Ta 22,24,9216
v_add_u32 v7, 65536, v15
Mvav 76,28,228
buffer_load_dwordx4 a[156:159], v26, s[8:11], s28 offen offset:0 sc1
v_accvgpr_write_b32 a218, v166
Mvav 44,28,220
v_accvgpr_write_b32 a219, v167
v_accvgpr_write_b32 a220, v200
Mvav 44,24,188
W 8
Ta 24,25,0
v_accvgpr_write_b32 a221, v201
Mvav 48,0,188
Ta 26,25,1024
v_add_u32 v8, 65536, v24
Mvav 48,4,220
global_load_dword v29, v28, s[6:7]
v_accvgpr_write_b32 a222, v202
Mvav 80,4,228
s_sub_u32 s74, s85, 66
s_and_b32 s75, s74, 0xffffffe3
s_cselect_b64 s[72:73], -1, 0
s_and_b32 s74, s74, 28
Mvav 80,0,196
s_lshl_b32 s74, s74, 1
v_mov_b32 v28, s74
v_accvgpr_write_b32 a223, v203
Mvav 112,0,204
Ta 28,25,8192
Ta 30,25,9216
Mvav 112,4,236
buffer_load_dwordx4 v[192:195], v26, s[8:11], s93 offen offset:0 sc1
v_add_u32 v9, 65536, v25
Mvav 144,4,244
G 164,224
G 165,225
Mvav 144,0,212
W 8
G 166,226
G 167,227
Mvav 148,8,212
Ta 0,10,16384
Ta 2,10,17408
Mvav 148,12,244
D 18,48,65,2048
G 172,228
Mvav 116,12,236
G 173,229
G 174,230
Mvav 116,8,204
Ta 4,10,24576
Ta 6,10,25600
Mvav 84,8,196
G 175,231
K 164,164,165
Mvav 84,12,228
buffer_load_dwordx2 v[28:29], v28, s[8:11], 0 offen offset:3584 sc1
Mvav 52,12,220
K 165,166,167
Mvav 52,8,188
W 8
Ta 8,11,16384
K 166,172,173
Mvav 56,16,188
Ta 10,11,17408
K 167,174,175
Mvav 56,20,220
buffer_load_dwordx4 v[200:203], v27, s[8:11], s28 offen offset:0 sc1
v_accvgpr_write_b32 a224, v164
Mvav 88,20,228
Ta 12,11,24576
Ta 14,11,25600
Mvav 88,16,196
v_accvgpr_write_b32 a225, v165
Mvav 120,16,204
v_accvgpr_write_b32 a226, v166
Mvav 120,20,236
D 19,48,65,3072
v_accvgpr_write_b32 a227, v167
Mvav 152,20,244
s_add_u32 m0, s63, 102400
v_accvgpr_write_b32 a228, v208
Mvav 152,16,212
W 8
Ta 16,12,16384
Ta 18,12,17408
Mvav 156,24,212
v_accvgpr_write_b32 a229, v209
Mvav 156,28,244
v_accvgpr_write_b32 a230, v210
Mvav 124,28,236
Ta 20,12,24576
v_accvgpr_write_b32 a231, v211
Mvav 124,24,204
Ta 22,12,25600
G 164,232
Mvav 92,24,196
G 165,233
Mvav 92,28,228
G 166,234
Mvav 60,28,220
G 167,235
Mvav 60,24,188
s_waitcnt vmcnt(18) lgkmcnt(8)
G 172,236
G 173,237
Mvaa 32,0,32
Ta 24,13,16384
Ta 26,13,17408
Mvaa 32,4,48
buffer_load_dwordx4 v[188:191], v27, s[8:11], s93 offen offset:0 sc1
G 174,238
Mvaa 64,4,52
s_add_u32 s28, s28, 512
s_cmp_eq_u32 s28, s4
s_cselect_b32 s93, 10240, 0
s_sub_u32 s28, s28, s93
Mvaa 64,0,36
s_add_u32 s93, s28, 20480
G 175,239
K 164,164,165
Mvaa 96,0,40
Ta 28,13,24576
Ta 30,13,25600
Mvaa 96,4,56
buffer_load_dwordx4 v[196:199], v26, s[8:11], s28 offen offset:0 sc1
K 165,166,167
Mvaa 128,4,60
K 166,172,173
K 167,174,175
Mvaa 128,0,44
W 8
v_accvgpr_write_b32 a232, v164
v_accvgpr_write_b32 a233, v165
Mvaa 132,8,44
v_accvgpr_write_b32 a234, v166
v_accvgpr_write_b32 a235, v167
Mvaa 132,12,60
buffer_load_dwordx4 v[204:207], v26, s[8:11], s93 offen offset:0 sc1
Ta 0,14,16384
Mvaa 100,12,56
Ta 2,14,17408
G 164,160
Mvaa 100,8,40
G 165,161
G 166,162
Mvaa 68,8,36
Ta 4,14,24576
G 167,163
Mvaa 68,12,52
buffer_load_dwordx4 v[212:215], v27, s[8:11], s28 offen offset:0 sc1
Ta 6,14,25600
Mvaa 36,12,48
G 172,164
G 173,165
Mvaa 36,8,32
W 8
G 174,166
G 175,167
Mvaa 40,16,32
K 164,164,165
K 165,166,167
Mvaa 40,20,48
buffer_load_dwordx4 v[220:223], v27, s[8:11], s93 offen offset:0 sc1
Ta 8,15,16384
Mvaa 72,20,52
s_add_u32 s28, s28, 512
s_cmp_eq_u32 s28, s4
s_cselect_b32 s93, 10240, 0
s_sub_u32 s28, s28, s93
Mvaa 72,16,36
s_add_u32 s93, s28, 20480
Ta 10,15,17408
K 166,172,173
Mvaa 104,16,40
s_cmp_lg_u32 s72, 0
s_cbranch_scc1 .LL_53%=
s_waitcnt vmcnt(7)
.LL_53%=:
v_xor_b32 v28, s12, v28
v_xor_b32 v29, s13, v29
v_lshrrev_b32 v248, 11, v28
v_or_b32 v29, v29, v248
v_cmp_eq_u32 vcc, 0, v29
s_or_b64 vcc, vcc, s[72:73]
s_cmp_eq_u64 vcc, -1
s_cbranch_scc0 .Lspin_54%=
.Lok_55%=:
K 167,174,175
v_accvgpr_write_b32 a236, v164
Mvaa 104,20,56
buffer_load_dwordx4 v[228:231], v26, s[8:11], s28 offen offset:0 sc1
v_accvgpr_write_b32 a237, v165
Mvaa 136,20,60
Ta 12,15,24576
Ta 14,15,25600
Mvaa 136,16,44
W 8
v_accvgpr_write_b32 a238, v166
v_accvgpr_write_b32 a239, v167
Mvaa 140,24,44
G 164,240
G 165,241
Mvaa 140,28,60
buffer_load_dwordx4 v[236:239], v26, s[8:11], s93 offen offset:0 sc1
G 166,242
Mvaa 108,28,56
v_bfe_u32 v248, v28, 3, 8
G 167,243
Mvaa 108,24,40
Ta 16,24,16384
Ta 18,24,17408
Mvaa 76,24,36
v_mul_u32_u24 v248, 10240, v248
G 172,244
Mvaa 76,28,52
D 20,48,65,0
G 173,245
Mvaa 44,28,48
v_and_b32 v28, 7, v28
G 174,246
Mvaa 44,24,32
W 6
v_lshl_add_u32 v248, v28, 4, v248
G 175,247
Mvaa 48,0,32
Ta 20,24,24576
Ta 22,24,25600
Mvaa 48,4,48
buffer_load_dwordx4 v[208:211], v27, s[8:11], s28 offen offset:0 sc1
K 164,164,165
Mvaa 80,4,52
v_lshl_add_u32 v248, v1, 3, v248
K 165,166,167
Mvaa 80,0,36
v_lshrrev_b32 v29, 8, v1
K 166,172,173
Mvaa 112,0,40
K 167,174,175
v_lshlrev_b32 v29, 28, v29
Mvaa 112,4,56
v_accvgpr_write_b32 a240, v164
v_or_b32 v249, v248, v29
Mvaa 144,4,60
s_and_b32 s75, s72, 134217728
Ta 24,25,16384
v_accvgpr_write_b32 a241, v165
Mvaa 144,0,44
)"
R"(W 5
Ta 26,25,17408
v_accvgpr_write_b32 a242, v166
Mvaa 148,8,44
s_add_u32 s76, s75, 131072
v_accvgpr_write_b32 a243, v167
G 164,168
Mvaa 148,12,60
buffer_load_dwordx4 v[240:243], v248, s[8:11], s76 offen sc1
G 165,169
Mvaa 116,12,56
s_add_u32 s76, s76, 8192
Ta 28,25,24576
Ta 30,25,25600
Mvaa 116,8,40
G 166,170
G 167,171
Mvaa 84,8,36
G 172,172
G 173,173
Mvaa 84,12,52
buffer_load_dwordx4 v[244:247], v249, s[8:11], s76 offen sc1
v_or_b32 v249, v1, v29
Mvaa 52,12,48
s_mul_i32 s76, s74, 160
G 174,174
G 175,175
Mvaa 52,8,32
W 4
Ta 0,2,0
Ta 2,2,1024
Mvaa 56,16,32
s_add_u32 s76, s76, s75
K 164,164,165
K 165,166,167
Mvaa 56,20,48
s_add_u32 s76, s76, 118784
K 166,172,173
K 167,174,175
Mvaa 88,20,52
Ta 4,2,8192
Ta 6,2,9216
Mvaa 88,16,36
D 21,48,65,1024
v_accvgpr_write_b32 a244, v164
Mvaa 120,16,40
v_accvgpr_write_b32 a245, v165
v_accvgpr_write_b32 a246, v166
Mvaa 120,20,56
v_accvgpr_write_b32 a247, v167
G 164,64
Mvaa 152,20,60
Ta 8,3,0
G 165,65
Mvaa 152,16,44
W 5
Ta 10,3,1024
G 166,66
Mvaa 156,24,44
G 167,67
G 172,68
Mvaa 156,28,60
Ta 12,3,8192
G 173,69
Mvaa 124,28,56
Ta 14,3,9216
G 174,70
Mvaa 124,24,40
G 175,71
K 164,164,165
Mvaa 92,24,36
K 165,166,167
K 166,172,173
Mvaa 92,28,52
Ta 16,4,0
K 167,174,175
Mvaa 60,28,48
Ta 18,4,1024
v_accvgpr_write_b32 a64, v164
Mvaa 60,24,32
s_waitcnt vmcnt(19) lgkmcnt(6)
v_accvgpr_write_b32 a65, v165
v_accvgpr_write_b32 a66, v166
Mvav 32,0,184
Ta 20,4,8192
v_accvgpr_write_b32 a67, v167
Mvaa 32,4,140
Ta 22,4,9216
G 164,176
Mvaa 64,4,144
G 165,177
G 166,178
Mvaa 64,0,128
G 167,179
G 172,180
Mvaa 96,0,132
Ta 24,5,0
G 173,181
Mvaa 96,4,148
Ta 26,5,1024
buffer_load_dwordx4 a[32:35], v27, s[8:11], s93 offen offset:0 sc1
Mvaa 128,4,152
s_add_u32 s28, s28, 512
s_cmp_eq_u32 s28, s4
s_cselect_b32 s93, 10240, 0
s_sub_u32 s28, s28, s93
Mvaa 128,0,136
W 6
s_add_u32 s93, s28, 20480
G 174,182
Mvaa 132,8,136
G 175,183
K 164,164,165
Mvaa 132,12,152
buffer_load_dwordx4 a[36:39], v26, s[8:11], s28 offen offset:0 sc1
K 165,166,167
Mvaa 100,12,148
Ta 28,5,8192
Ta 30,5,9216
Mvaa 100,8,132
K 166,172,173
K 167,174,175
Mvaa 68,8,128
v_accvgpr_write_b32 a68, v164
v_accvgpr_write_b32 a69, v165
Mvaa 68,12,144
buffer_load_dwordx4 a[40:43], v26, s[8:11], s93 offen offset:0 sc1
v_accvgpr_write_b32 a70, v166
Mvaa 36,12,140
Ta 0,6,0
v_accvgpr_write_b32 a71, v167
Mvav 36,8,184
W 5
Ta 2,6,1024
G 164,72
Mvav 40,16,184
G 165,73
G 166,74
Mvaa 40,20,140
buffer_load_dwordx4 a[44:47], v27, s[8:11], s28 offen offset:0 sc1
Ta 4,6,8192
Mvaa 72,20,144
Ta 6,6,9216
G 167,75
Mvaa 72,16,128
G 172,76
G 173,77
Mvaa 104,16,132
Ta 8,7,0
G 174,78
Mvaa 104,20,148
buffer_load_dwordx4 a[48:51], v27, s[8:11], s93 offen offset:0 sc1
Ta 10,7,1024
Mvaa 136,20,152
s_add_u32 s28, s28, 512
s_cmp_eq_u32 s28, s4
s_cselect_b32 s93, 10240, 0
s_sub_u32 s28, s28, s93
Mvaa 136,16,136
W 6
s_add_u32 s93, s28, 20480
G 175,79
Mvaa 140,24,136
Ta 12,7,8192
K 164,164,165
Mvaa 140,28,152
buffer_load_dwordx4 a[52:55], v26, s[8:11], s28 offen offset:0 sc1
Ta 14,7,9216
Mvaa 108,28,148
K 165,166,167
K 166,172,173
Mvaa 108,24,132
K 167,174,175
v_accvgpr_write_b32 a72, v164
Mvaa 76,24,128
Ta 16,8,0
Ta 18,8,1024
Mvaa 76,28,144
D 22,48,65,2048
v_accvgpr_write_b32 a73, v165
Mvaa 44,28,140
v_accvgpr_write_b32 a74, v166
v_accvgpr_write_b32 a75, v167
Mvav 44,24,184
W 6
Ta 20,8,8192
Ta 22,8,9216
Mvav 48,0,184
v_accvgpr_write_b32 a76, v216
v_accvgpr_write_b32 a77, v217
Mvaa 48,4,140
buffer_load_dwordx4 a[56:59], v26, s[8:11], s93 offen offset:0 sc1
v_accvgpr_write_b32 a78, v218
Mvaa 80,4,144
Ta 24,9,0
v_accvgpr_write_b32 a79, v219
Mvaa 80,0,128
Ta 26,9,1024
G 164,80
Mvaa 112,0,132
Ta 28,9,8192
G 165,81
Mvaa 112,4,148
buffer_load_dwordx4 a[60:63], v27, s[8:11], s28 offen offset:0 sc1
Ta 30,9,9216
Mvaa 144,4,152
G 166,82
G 167,83
Mvaa 144,0,136
W 8
G 172,84
G 173,85
Mvaa 148,8,136
Ta 0,2,16384
Ta 2,2,17408
Mvaa 148,12,152
D 23,48,65,3072
G 174,86
Mvaa 116,12,148
Ta 4,2,24576
G 175,87
Mvaa 116,8,132
Ta 6,2,25600
K 164,164,165
Mvaa 84,8,128
K 165,166,167
Mvaa 84,12,144
buffer_load_dwordx4 v[216:219], v27, s[8:11], s93 offen offset:0 sc1
K 166,172,173
Mvaa 52,12,140
K 167,174,175
Mvav 52,8,184
W 8
Ta 8,3,16384
Ta 10,3,17408
Mvav 56,16,184
v_accvgpr_write_b32 a80, v164
Mvaa 56,20,140
v_accvgpr_write_b32 a81, v165
Mvaa 88,20,144
Ta 12,3,24576
v_accvgpr_write_b32 a82, v166
Mvaa 88,16,128
Ta 14,3,25600
v_accvgpr_write_b32 a83, v167
Mvaa 120,16,132
v_accvgpr_write_b32 a84, v224
Mvaa 120,20,148
v_lshrrev_b32 v20, 4, v1
Mvaa 152,20,152
v_accvgpr_write_b32 a85, v225
Mvaa 152,16,136
W 8
v_accvgpr_write_b32 a86, v226
v_add_u32 v20, 0, v20
Mvaa 156,24,136
Ta 16,4,16384
Ta 18,4,17408
Mvaa 156,28,152
v_accvgpr_write_b32 a87, v227
v_mul_u32_u24 v22, 205, v20
Mvaa 124,28,148
Ta 20,4,24576
G 164,88
Mvaa 124,24,132
Ta 22,4,25600
v_lshrrev_b32 v22, 12, v22
Mvaa 92,24,128
G 165,89
G 166,90
Mvaa 92,28,144
v_mul_u32_u24 v23, 20, v22
G 167,91
Mvaa 60,28,140
v_sub_u32 v20, v20, v23
Mvav 60,24,184
s_waitcnt vmcnt(18) lgkmcnt(8)
Ta 24,5,16384
G 172,92
Mvaa 32,0,156
s_add_u32 s28, s28, 512
s_cmp_eq_u32 s28, s4
s_cselect_b32 s93, 10240, 0
Ta 26,5,17408
Mvav 32,4,196
s_sub_u32 s28, s28, s93
s_add_u32 s93, s28, 20480
buffer_load_dwordx4 v[184:187], v26, s[8:11], s28 offen offset:0 sc1
Mvav 64,4,204
G 173,93
v_lshrrev_b32 v23, 1, v22
Mvav 64,0,192
G 174,94
v_and_b32 v23, 2, v23
Mvav 96,0,200
Ta 28,5,24576
Ta 30,5,25600
Mvav 96,4,212
buffer_load_dwordx4 a[128:131], v26, s[8:11], s93 offen offset:0 sc1
G 175,95
Mvav 128,4,220
v_xor_b32 v20, v20, v23
K 164,164,165
Mvav 128,0,188
W 8
K 165,166,167
v_mul_u32_u24 v22, 80, v22
Mvav 132,8,188
K 166,172,173
v_add_lshl_u32 v16, v20, v22, 4
Mvav 132,12,220
buffer_load_dwordx4 a[132:135], v27, s[8:11], s28 offen offset:0 sc1
Ta 0,6,16384
Mvav 100,12,212
Ta 2,6,17408
K 167,174,175
Mvav 100,8,200
v_lshrrev_b32 v20, 4, v1
v_accvgpr_write_b32 a88, v164
Mvav 68,8,192
Ta 4,6,24576
v_accvgpr_write_b32 a89, v165
Mvav 68,12,204
buffer_load_dwordx4 a[136:139], v27, s[8:11], s93 offen offset:0 sc1
Ta 6,6,25600
Mvav 36,12,196
s_add_u32 s28, s28, 512
s_cmp_eq_u32 s28, s4
s_cselect_b32 s93, 10240, 0
s_sub_u32 s28, s28, s93
Mvaa 36,8,156
W 8
s_add_u32 s93, s28, 20480
v_add_u32 v20, 64, v20
Mvaa 40,16,156
v_accvgpr_write_b32 a90, v166
v_mul_u32_u24 v22, 205, v20
Mvav 40,20,196
buffer_load_dwordx4 a[140:143], v26, s[8:11], s28 offen offset:0 sc1
v_accvgpr_write_b32 a91, v167
Mvav 72,20,204
v_lshrrev_b32 v22, 12, v22
v_accvgpr_write_b32 a92, v232
Mvav 72,16,192
Ta 8,7,16384
Ta 10,7,17408
Mvav 104,16,200
v_accvgpr_write_b32 a93, v233
v_mul_u32_u24 v23, 20, v22
Mvav 104,20,212
s_cmp_lg_u32 s72, 0
s_cbranch_scc1 .LL_57%=
s_waitcnt vmcnt(17)
.LL_57%=:
buffer_store_dwordx4 v[240:243], v1, s[8:11], s76 offen sc1
v_accvgpr_write_b32 a94, v234
v_sub_u32 v20, v20, v23
Mvav 136,20,220
Ta 12,7,24576
v_accvgpr_write_b32 a95, v235
Mvav 136,16,188
W 7
Ta 14,7,25600
v_lshrrev_b32 v23, 1, v22
Mvav 140,24,188
G 164,96
G 165,97
Mvav 140,28,220
buffer_store_dwordx4 v[244:247], v249, s[8:11], s76 offen offset:1024 sc1
v_and_b32 v23, 2, v23
Mvav 108,28,212
s_add_u32 s76, s74, s75
s_add_u32 s76, s76, 3840
G 166,98
Mvav 108,24,200
G 167,99
G 172,100
Mvav 76,24,192
v_xor_b32 v20, v20, v23
G 173,101
Mvav 76,28,204
buffer_load_dwordx4 a[144:147], v26, s[8:11], s93 offen offset:0 sc1
Ta 16,8,16384
Mvav 44,28,196
Ta 18,8,17408
G 174,102
Mvaa 44,24,156
W 6
v_mul_u32_u24 v22, 80, v22
G 175,103
Mvaa 48,0,156
K 164,164,165
v_add_lshl_u32 v17, v20, v22, 4
Mvav 48,4,196
buffer_load_dwordx4 a[148:151], v27, s[8:11], s28 offen offset:0 sc1
Ta 20,8,24576
Mvav 80,4,204
Ta 22,8,25600
K 165,166,167
Mvav 80,0,192
)"
R"(K 166,172,173
v_subrev_u32 v17, 1024, v17
Mvav 112,0,200
K 167,174,175
v_accvgpr_write_b32 a96, v164
Mvav 112,4,212
buffer_load_dwordx4 a[152:155], v27, s[8:11], s93 offen offset:0 sc1
v_accvgpr_write_b32 a97, v165
Mvav 144,4,220
s_add_u32 s28, s28, 512
v_lshrrev_b32 v20, 4, v1
v_accvgpr_write_b32 a98, v166
Mvav 144,0,188
W 4
Ta 24,9,16384
v_accvgpr_write_b32 a99, v167
Mvav 148,8,188
s_cmp_eq_u32 s28, s4
s_cselect_b32 s93, 10240, 0
Ta 26,9,17408
v_add_u32 v20, 128, v20
Mvav 148,12,220
s_sub_u32 s28, s28, s93
Ta 28,9,24576
Ta 30,9,25600
Mvav 116,12,212
s_add_u32 s93, s28, 20480
buffer_load_dwordx4 v[224:227], v26, s[8:11], s28 offen offset:0 sc1
G 164,248
Mvav 116,8,200
G 165,249
v_mul_u32_u24 v22, 205, v20
Mvav 84,8,192
s_waitcnt vmcnt(12) lgkmcnt(0)
s_barrier
G 166,250
Mvav 84,12,204
G 167,251
v_lshrrev_b32 v22, 12, v22
Mvav 52,12,196
G 172,252
G 173,253
Mvaa 52,8,156
G 174,254
v_mul_u32_u24 v23, 20, v22
Mvaa 56,16,156
G 175,255
K 164,164,165
Mvav 56,20,196
v_sub_u32 v20, v20, v23
K 165,166,167
Mvav 88,20,204
K 166,172,173
v_lshrrev_b32 v23, 1, v22
Mvav 88,16,192
buffer_load_dwordx4 v[232:235], v26, s[8:11], s93 offen offset:0 sc1
K 167,174,175
Mvav 120,16,200
v_accvgpr_write_b32 a100, v164
v_accvgpr_write_b32 a101, v165
v_accvgpr_write_b32 a102, v166
v_accvgpr_write_b32 a103, v167
Mvav 120,20,212
v_and_b32 v23, 2, v23
v_xor_b32 v20, v20, v23
Mvav 152,20,220
Ta 0,10,32768
Ta 2,10,33792
Mvav 152,16,188
Ta 4,10,40960
Ta 6,10,41984
Mvav 156,24,188
G 164,104
G 165,105
Mvav 156,28,220
v_mul_u32_u24 v22, 80, v22
G 166,106
Mvav 124,28,212
Ta 8,11,32768
v_add_lshl_u32 v18, v20, v22, 4
Mvav 124,24,200
Ta 10,11,33792
G 167,107
Mvav 92,24,192
G 172,108
v_subrev_u32 v18, 2048, v18
Mvav 92,28,204
Ta 12,11,40960
G 173,109
Mvav 60,28,196
Ta 14,11,41984
G 174,110
Mvaa 60,24,156
W 4
v_lshrrev_b32 v20, 4, v1
G 175,111
Mvav 32,0,228
v_add_u32 v20, 192, v20
K 164,164,165
Mvaa 32,4,36
buffer_load_dwordx4 a[248:251], v27, s[8:11], s28 offen offset:0 sc1
K 165,166,167
Mvaa 64,4,40
v_mul_u32_u24 v22, 205, v20
K 166,172,173
Mvav 64,0,236
Ta 16,12,32768
Ta 18,12,33792
Mvav 96,0,208
K 167,174,175
v_lshrrev_b32 v22, 12, v22
Mvaa 96,4,44
buffer_load_dwordx4 a[252:255], v27, s[8:11], s93 offen offset:0 sc1
v_accvgpr_write_b32 a104, v164
Mvaa 128,4,48
s_add_u32 s28, s28, 512
v_mul_u32_u24 v23, 20, v22
v_accvgpr_write_b32 a105, v165
Mvaa 128,0,32
W 2
Ta 20,12,40960
Ta 22,12,41984
Mvaa 132,8,32
s_cmp_eq_u32 s28, s4
s_cselect_b32 s93, 10240, 0
v_accvgpr_write_b32 a106, v166
v_sub_u32 v20, v20, v23
Mvaa 132,12,48
s_sub_u32 s28, s28, s93
v_accvgpr_write_b32 a107, v167
K 176,176,177
Mvaa 100,12,44
s_add_u32 s93, s28, 20480
v_lshrrev_b32 v23, 1, v22
K 177,178,179
Mvav 100,8,208
v_and_b32 v23, 2, v23
K 178,180,181
Mvav 68,8,236
Ta 24,13,32768
Ta 26,13,33792
Mvaa 68,12,40
K 179,182,183
v_accvgpr_write_b32 a108, v176
v_accvgpr_write_b32 a109, v177
v_accvgpr_write_b32 a110, v178
v_accvgpr_write_b32 a111, v179
Mvaa 36,12,36
buffer_load_dwordx4 v[192:195], v26, s[8:11], s28 offen offset:0 sc1
v_xor_b32 v20, v20, v23
Mvav 36,8,228
W 2
Ta 28,13,40960
Ta 30,13,41984
Mvav 40,16,228
v_mul_u32_u24 v22, 80, v22
v_add_lshl_u32 v19, v20, v22, 4
Mvaa 40,20,36
G 164,112
v_subrev_u32 v19, 3072, v19
Mvaa 72,20,40
s_cmp_lg_u32 s72, 0
s_cbranch_scc1 .LL_58%=
s_waitcnt vmcnt(8)
.LL_58%=:
v_mov_b64 v[28:29], s[12:13]
s_mov_b64 exec, 1
buffer_store_dwordx2 v[28:29], off, s[8:11], s76 sc1
s_mov_b64 exec, -1
Ta 0,14,32768
G 165,113
Mvav 72,16,236
Ta 2,14,33792
v_lshrrev_b32 v20, 4, v1
Mvav 104,16,208
G 166,114
G 167,115
Mvaa 104,20,44
Ta 4,14,40960
v_add_u32 v20, 256, v20
Mvaa 136,20,48
buffer_load_dwordx4 v[200:203], v26, s[8:11], s93 offen offset:0 sc1
Ta 6,14,41984
Mvaa 136,16,32
W 4
G 172,116
v_mul_u32_u24 v22, 205, v20
Mvaa 140,24,32
G 173,117
G 174,118
Mvaa 140,28,48
v_lshrrev_b32 v22, 12, v22
G 175,119
Mvaa 108,28,44
Ta 8,15,32768
v_mul_u32_u24 v23, 20, v22
Mvav 108,24,208
Ta 10,15,33792
K 164,164,165
Mvav 76,24,236
buffer_load_dwordx4 v[188:191], v27, s[8:11], s28 offen offset:0 sc1
v_sub_u32 v20, v20, v23
Mvaa 76,28,40
K 165,166,167
K 166,172,173
Mvaa 44,28,36
Ta 12,15,40960
v_lshrrev_b32 v23, 1, v22
Mvav 44,24,228
W 3
Ta 14,15,41984
K 167,174,175
Mvav 48,0,228
v_and_b32 v23, 2, v23
v_accvgpr_write_b32 a112, v164
Mvaa 48,4,36
v_accvgpr_write_b32 a113, v165
v_xor_b32 v20, v20, v23
Mvaa 80,4,40
Ta 16,24,32768
v_accvgpr_write_b32 a114, v166
Mvav 80,0,236
Ta 18,24,33792
v_mul_u32_u24 v22, 80, v22
Mvav 112,0,208
buffer_load_dwordx4 v[196:199], v27, s[8:11], s93 offen offset:0 sc1
v_accvgpr_write_b32 a115, v167
Mvaa 112,4,44
s_add_u32 s28, s28, 512
v_add_lshl_u32 v20, v20, v22, 4
v_accvgpr_write_b32 a116, v168
Mvaa 144,4,48
v_accvgpr_write_b32 a117, v169
v_accvgpr_write_b32 a118, v170
v_accvgpr_write_b32 a119, v171
Mvaa 144,0,32
W 2
Ta 20,24,40960
Ta 22,24,41984
Mvaa 148,8,32
s_cmp_eq_u32 s28, s4
s_cselect_b32 s93, 10240, 0
v_lshrrev_b32 v22, 6, v1
v_mul_u32_u24 v22, 320, v22
Mvaa 148,12,48
s_sub_u32 s28, s28, s93
G 164,120
v_bfe_u32 v23, v1, 4, 2
Mvaa 116,12,44
s_add_u32 s93, s28, 20480
Ta 24,25,32768
Ta 26,25,33792
Mvav 116,8,208
G 165,121
v_lshl_add_u32 v22, v23, 3, v22
Mvav 84,8,236
buffer_load_dwordx4 v[204:207], v26, s[8:11], s28 offen offset:0 sc1
G 166,122
Mvaa 84,12,40
v_bfe_u32 v23, v1, 8, 1
G 167,123
Mvaa 52,12,36
Ta 28,25,40960
Ta 30,25,41984
Mvav 52,8,228
W 4
G 172,124
v_lshlrev_b32 v23, 5, v23
Mvav 56,16,228
G 173,125
v_add_u32 v21, v22, v23
Mvaa 56,20,36
G 174,126
v_sub_u32 v22, v22, v23
Mvaa 88,20,40
s_mul_i32 s94, s59, 655360
Ta 0,10,49152
Ta 2,10,50176
Mvav 88,16,236
s_mul_i32 s95, s58, 320
buffer_load_dwordx4 v[212:215], v26, s[8:11], s93 offen offset:0 sc1
G 175,127
Mvav 120,16,208
s_add_u32 s94, s94, s95
K 164,164,165
K 165,166,167
Mvaa 120,20,44
Ta 4,10,57344
Ta 6,10,58368
Mvaa 152,20,48
K 166,172,173
K 167,174,175
Mvaa 152,16,32
W 4
v_accvgpr_write_b32 a120, v164
v_accvgpr_write_b32 a121, v165
Mvaa 156,24,32
s_add_u32 s40, s26, s94
s_addc_u32 s41, s27, 0
Ta 8,11,49152
Mvaa 156,28,48
s_and_b32 s41, s41, 0xffff
Ta 10,11,50176
buffer_load_dwordx4 v[220:223], v27, s[8:11], s28 offen offset:0 sc1
Mvaa 124,28,44
s_mov_b32 s42, 5242880
v_accvgpr_write_b32 a122, v166
v_accvgpr_write_b32 a123, v167
Mvav 124,24,208
s_mov_b32 s43, 0x20000
Ta 12,11,57344
Ta 14,11,58368
Mvav 92,24,236
s_sub_u32 s42, s42, s94
v_accvgpr_write_b32 a124, v160
v_accvgpr_write_b32 a125, v161
Mvaa 92,28,40
s_mov_b64 s[44:45], s[8:9]
v_accvgpr_write_b32 a126, v162
v_accvgpr_write_b32 a127, v163
Mvaa 60,28,36
s_mov_b32 s47, 0x20000
Ta 16,12,49152
Ta 18,12,50176
Mvav 60,24,228
s_waitcnt vmcnt(18) lgkmcnt(6)
buffer_load_dwordx4 v[176:179], v27, s[8:11], s93 offen offset:0 sc1
v_bfe_u32 v28, v1, 4, 2
Mvaa 32,0,52
s_and_b32 s48, s14, 1
Ta 20,12,57344
v_lshlrev_b32 v28, 3, v28
Mvav 32,4,184
s_lshl_b32 s80, s56, 9
s_add_u32 s80, s80, 4096
Ta 22,12,58368
Mvaa 64,4,128
s_mul_i32 s48, s48, 5120
Ta 24,13,49152
Ta 26,13,50176
Mvaa 64,0,56
s_lshr_b32 s51, s14, 1
s_and_b32 s94, s14, 1
buffer_load_dwordx2 v[28:29], v28, s[8:11], s80 offen sc1
Mvaa 96,0,60
s_lshl_b32 s95, s57, 1
Ta 28,13,57344
Ta 30,13,58368
Mvaa 96,4,132
s_add_u32 s94, s94, s95
Mvaa 128,4,136
Mvav 128,0,216
W 8
s_lshl_b32 s54, s94, 3
Ta 0,14,49152
Mvav 132,8,216
Ta 2,14,50176
Mvaa 132,12,136
s_add_u32 s54, s54, s59
Mvaa 100,12,132
s_lshl_b32 s54, s54, 6
Ta 4,14,57344
Ta 6,14,58368
Mvaa 100,8,60
Mvaa 68,8,56
s_xor_b32 s55, s54, 15872
s_and_b32 s81, s14, 1
Mvaa 68,12,128
Mvav 36,12,184
s_add_u32 s54, s54, 20480
Mvaa 36,8,52
W 8
s_mul_i32 s91, s81, 17164288
Ta 8,15,49152
Mvaa 40,16,52
s_add_u32 s55, s55, 20480
Ta 10,15,50176
Mvav 40,20,184
s_and_b32 s95, s14, 2
s_mul_i32 s82, s56, 536576
Mvaa 72,20,128
Ta 12,15,57344
Ta 14,15,58368
Mvaa 72,16,56
s_lshl_b32 s94, s94, 2
Mvaa 104,16,60
s_add_u32 s91, s91, s82
Mvaa 104,20,132
s_add_u32 s94, s94, s95
Mvaa 136,20,136
s_add_u32 s91, s91, 4194304
Mvav 136,16,216
W 8
s_lshl_b32 s95, s95, 4
Ta 16,24,49152
Mvav 140,24,216
s_add_u32 s55, s55, s95
Ta 18,24,50176
Mvaa 140,28,136
s_lshr_b32 s82, s14, 1
)"
R"(Mvaa 108,28,132
s_lshl_b32 s49, s94, 17
Ta 20,24,57344
Ta 22,24,58368
Mvaa 108,24,60
s_lshl_b32 s83, s82, 14
Mvaa 76,24,56
s_lshl_b32 s95, s59, 14
Mvaa 76,28,128
Mvav 44,28,184
s_add_u32 s49, s49, s95
s_add_u32 s91, s91, s83
Mvaa 44,24,52
W 8
s_xor_b32 s50, s49, 16252928
Ta 24,25,49152
Mvaa 48,0,52
s_lshl_b32 s81, s81, 14
Ta 26,25,50176
Mvav 48,4,184
s_add_u32 s49, s49, 39845888
Mvaa 80,4,128
Ta 28,25,57344
Ta 30,25,58368
Mvaa 80,0,56
s_add_u32 s50, s50, 39845888
s_lshl_b32 s82, s82, 16
Mvaa 112,0,60
Mvaa 112,4,132
s_lshr_b32 s94, s15, 3
s_or_b32 s92, s81, s82
Mvaa 144,4,136
s_lshl_b32 s94, s94, 3
Mvav 144,0,216
W 8
Ta 0,2,32768
Ta 2,2,33792
Mvav 148,8,216
s_xor_b32 s92, s92, 0x10000
s_add_u32 s94, s94, s14
Mvaa 148,12,136
Mvaa 116,12,132
s_lshl_b32 s94, s94, 4
Ta 4,2,40960
Ta 6,2,41984
Mvaa 116,8,60
Mvaa 84,8,56
s_add_u32 s94, s94, s59
Mvaa 84,12,128
s_mul_i32 s52, s94, 5120
Mvav 52,12,184
Mvaa 52,8,52
W 8
s_add_u32 s52, s52, 56623104
Ta 8,3,32768
Mvaa 56,16,52
Ta 10,3,33792
Mvav 56,20,184
s_lshl_b32 s53, s94, 3
Mvaa 88,20,128
Ta 12,3,40960
Ta 14,3,41984
Mvaa 88,16,56
s_add_u32 s53, s53, 65536
Mvaa 120,16,60
s_waitcnt vmcnt(0)
v_cmp_eq_u64 vcc, s[12:13], v[28:29]
s_cmp_eq_u64 vcc, -1
s_cbranch_scc0 .Lspin_59%=
.Lok_60%=:
s_sub_u32 s82, s91, 16384
Mvaa 120,20,132
s_xor_b32 s83, s92, 0x10000
s_add_u32 m0, s83, 0
s_add_u32 s81, s82, 0
Mvaa 152,20,136
v_bfe_u32 v244, v1, 4, 1
v_mul_u32_u24 v244, 0xaaaa, v244
Mvav 152,16,216
W 8
Dc 1,8,81,0
v_add_u32 v244, 0x4444, v244
Mvav 156,24,216
Ta 16,4,32768
Ta 18,4,33792
Mvaa 156,28,136
v_lshl_or_b32 v240, v244, 16, v244
Mvaa 124,28,132
Ta 20,4,40960
v_mov_b32 v241, 0xff800000
Mvaa 124,24,60
Ta 22,4,41984
Dc 1,8,81,1024
Mvaa 92,24,56
v_add_u32 v242, 0x10000, v1
Mvaa 92,28,128
v_bfe_u32 v244, v1, 4, 4
Mvav 60,28,184
v_lshrrev_b32 v245, 8, v1
Mvaa 60,24,52
W 8
Dc 1,8,81,2048
v_lshlrev_b32 v245, 2, v245
Mvaa 32,0,140
Ta 24,5,32768
Ta 26,5,33792
Mvav 32,4,224
v_sub_u32 v244, v244, v245
Mvav 64,4,232
Ta 28,5,40960
v_cmp_le_i32_e64 s[72:73], 0, v244
Mvaa 64,0,144
Ta 30,5,41984
Dc 1,8,81,3072
Mvaa 96,0,148
s_add_u32 m0, s83, 4096
s_add_u32 s81, s82, 4096
v_cmp_le_i32_e64 s[74:75], 1, v244
Mvaa 96,4,248
v_cmp_le_i32_e64 s[76:77], 2, v244
Mvaa 128,4,252
v_cmp_le_i32_e64 s[78:79], 3, v244
Mvaa 128,0,152
W 8
Dc 1,8,81,0
Ta 0,6,32768
Mvaa 132,8,152
Ta 2,6,33792
v_add_u32 v243, 4096, v1
Mvaa 132,12,252
v_add_u32 v244, 8192, v1
Mvaa 100,12,248
Ta 4,6,40960
v_add_u32 v245, 12288, v1
Mvaa 100,8,148
Ta 6,6,41984
Dc 1,8,81,1024
Mvaa 68,8,144
s_mov_b32 s18, 0x3f803f80
Mvav 68,12,232
s_mov_b32 s19, 0x3f003f00
Mvav 36,12,224
Mvaa 36,8,140
W 8
Dc 1,8,81,2048
v_accvgpr_write_b32 a176, s18
Mvaa 40,16,140
Ta 8,7,32768
Ta 10,7,33792
Mvav 40,20,224
v_accvgpr_write_b32 a180, s19
v_mov_b32 v252, v241
Mvav 72,20,232
Ta 12,7,40960
Ta 14,7,41984
Mvaa 72,16,144
Dc 1,8,81,3072
v_accvgpr_write_b32 a177, s18
Mvaa 104,16,148
s_add_u32 m0, s83, 8192
s_add_u32 s81, s82, 8192
v_accvgpr_write_b32 a181, s19
Mvaa 104,20,248
v_mov_b32 v253, v241
Mvaa 136,20,252
Mvaa 136,16,152
W 8
Ta 16,8,32768
v_accvgpr_write_b32 a178, s18
Mvaa 140,24,152
Ta 18,8,33792
Dc 1,8,81,0
Mvaa 140,28,252
v_accvgpr_write_b32 a182, s19
Mvaa 108,28,248
Ta 20,8,40960
v_mov_b32 v254, v241
Mvaa 108,24,148
Ta 22,8,41984
v_accvgpr_write_b32 a179, s18
Mvaa 76,24,144
Dc 1,8,81,1024
v_accvgpr_write_b32 a183, s19
Mvav 76,28,232
Mvav 44,28,224
v_mov_b32 v255, v241
Mvaa 44,24,140
W 8
Ta 24,9,32768
v_accvgpr_write_b32 a160, 0
Mvaa 48,0,140
Ta 26,9,33792
Dc 1,8,81,2048
Mvav 48,4,224
v_accvgpr_write_b32 a161, 0
Mvav 80,4,232
Ta 28,9,40960
v_accvgpr_write_b32 a162, 0
Mvaa 80,0,144
Ta 30,9,41984
v_accvgpr_write_b32 a163, 0
Mvaa 112,0,148
Dc 1,8,81,3072
Mvaa 112,4,248
s_add_u32 m0, s83, 12288
s_add_u32 s81, s82, 12288
v_accvgpr_write_b32 a164, 0
Mvaa 144,4,252
v_accvgpr_write_b32 a165, 0
Mvaa 144,0,152
W 8
Ta 0,2,49152
v_accvgpr_write_b32 a166, 0
Mvaa 148,8,152
Ta 2,2,50176
Dc 1,8,81,0
Mvaa 148,12,252
v_accvgpr_write_b32 a167, 0
Mvaa 116,12,248
Ta 4,2,57344
v_accvgpr_write_b32 a168, 0
Mvaa 116,8,148
Ta 6,2,58368
v_accvgpr_write_b32 a169, 0
Mvaa 84,8,144
Dc 1,8,81,1024
Mvav 84,12,232
v_accvgpr_write_b32 a170, 0
Mvav 52,12,224
v_accvgpr_write_b32 a171, 0
Mvaa 52,8,140
W 8
Ta 8,3,49152
Ta 10,3,50176
Mvaa 56,16,140
Dc 1,8,81,2048
v_accvgpr_write_b32 a172, 0
Mvav 56,20,224
v_accvgpr_write_b32 a173, 0
Mvav 88,20,232
Ta 12,3,57344
v_accvgpr_write_b32 a174, 0
Mvaa 88,16,144
Ta 14,3,58368
Mvaa 120,16,148
Dc 1,8,81,3072
v_accvgpr_write_b32 a175, 0
Mvaa 120,20,248
s_mov_b32 s90, 0x5d800000
Mvaa 152,20,252
s_mov_b64 s[88:89], 0
Mvaa 152,16,152
W 8
Ta 16,4,49152
Ta 18,4,50176
Mvaa 156,24,152
s_mov_b32 s64, 0
Mvaa 156,28,252
s_and_b32 s18, s14, 1
Mvaa 124,28,248
s_lshl_b32 s62, s18, 14
Ta 20,4,57344
Ta 22,4,58368
Mvaa 124,24,148
Mvaa 92,24,144
s_lshl_b32 s20, s57, 15
Mvav 92,28,232
s_sub_u32 s20, 507904, s20
Mvav 60,28,224
Mvaa 60,24,140
W 8
s_add_u32 s20, s20, s62
Ta 24,5,49152
Mvav 32,0,192
s_lshl_b32 s65, s57, 1
Ta 26,5,50176
Mvav 32,4,204
s_sub_u32 s65, s65, 1
Mvav 64,4,212
Ta 28,5,57344
Ta 30,5,58368
Mvav 64,0,200
s_max_i32 s65, s65, 0
v_bfe_u32 v28, v1, 4, 4
v_lshlrev_b32 v28, 3, v28
Mvav 96,0,188
s_sub_u32 s66, s60, 1
s_lshl_b32 s80, s56, 9
Mvav 96,4,220
s_add_u32 s80, s80, 4096
s_cmp_lt_u32 s14, 2
s_cselect_b32 s20, s20, 0
s_cselect_b32 s66, s66, 15
s_cselect_b32 s21, 32768, 0
Mvav 128,4,176
buffer_load_dwordx2 v[28:29], v28, s[8:11], s80 offen sc1
Mvav 128,0,196
W 8
s_add_u32 s61, s91, s20
Ta 0,6,49152
Mvav 132,8,196
s_add_u32 s62, s62, s21
Ta 2,6,50176
Mvav 132,12,176
Mvav 100,12,220
s_sub_u32 s63, 16384, s21
Ta 4,6,57344
Ta 6,6,58368
Mvav 100,8,188
s_lshl_b32 s67, s60, 7
Mvav 68,8,200
s_add_u32 s67, s67, s56
Mvav 68,12,212
Mvav 36,12,204
s_lshl_b32 s67, s67, 12
Mvav 36,8,192
W 8
s_add_u32 s67, s67, 39845888
Ta 8,7,49152
Mvav 40,16,192
s_lshl_b32 s69, s60, 3
Ta 10,7,50176
Mvav 40,20,204
Mvav 72,20,212
s_add_u32 s69, s69, s59
Ta 12,7,57344
Ta 14,7,58368
Mvav 72,16,200
s_lshl_b32 s69, s69, 6
Mvav 104,16,188
Mvav 104,20,220
s_lshl_b32 s21, s58, 3
Mvav 136,20,176
s_add_u32 s69, s69, s21
Mvav 136,16,196
W 8
s_add_u32 s69, s69, 20480
Ta 16,8,49152
Mvav 140,24,196
Ta 18,8,50176
Mvav 140,28,176
s_mov_b32 s70, 0
Mvav 108,28,220
s_mov_b32 s37, 0
Ta 20,8,57344
Ta 22,8,58368
Mvav 108,24,188
s_sub_u32 s18, s57, 4
Mvav 76,24,200
Mvav 76,28,212
s_lshl_b32 s18, s18, 2
Mvav 44,28,204
s_add_u32 s18, s18, s58
Mvav 44,24,192
W 8
Ta 24,9,49152
Ta 26,9,50176
Mvav 48,0,192
s_mul_i32 s85, s59, 7
Mvav 48,4,204
s_add_u32 s85, s85, s18
Mvav 80,4,212
s_cmp_lt_u32 s18, 7
s_cselect_b32 s85, s85, 99
Ta 28,9,57344
Ta 30,9,58368
Mvav 80,0,200
Mvav 112,0,188
s_cmp_lt_u32 s85, 54
s_cselect_b32 s65, 2, s65
Mvav 112,4,220
s_mov_b32 s68, 0
Mvav 144,4,176
s_mov_b64 s[24:25], -1
Mvav 144,0,196
W 8
Mvav 148,8,196
s_mov_b32 s22, 0x3e0293ee
Mvav 148,12,176
s_mov_b32 s23, 0x3e0293ee
Mvav 116,12,220
Mvav 116,8,188
Mvav 84,8,200
Mvav 84,12,212
Mvav 52,12,204
Mvav 52,8,192
W 4
Mvav 56,16,192
Mvav 56,20,204
s_waitcnt vmcnt(0)
v_cmp_eq_u64 vcc, s[12:13], v[28:29]
s_cmp_eq_u64 vcc, -1
s_cbranch_scc0 .Lspin_62%=
.Lok_63%=:
Mvav 88,20,212
Mvav 88,16,200
Mvav 120,16,188
W 0
s_barrier
Ra 128,242,0
)"
R"(Mvav 120,20,220
Mvav 152,20,176
Ra 132,242,1024
Mvav 152,16,196
Mvav 156,24,196
Ra 136,242,2048
Mvav 156,28,176
Mvav 124,28,220
Ra 140,242,3072
Mvav 124,24,188
Mvav 92,24,200
Mvav 92,28,212
Mvav 60,28,204
Mvav 60,24,192
.Lbgo%=:
v_pk_mul_f32 v[32:33], v[32:33], s[22:23]
v_pk_mul_f32 v[34:35], v[34:35], s[22:23]
v_pk_mul_f32 v[36:37], v[36:37], s[22:23]
v_pk_mul_f32 v[38:39], v[38:39], s[22:23]
v_pk_mul_f32 v[64:65], v[64:65], s[22:23]
v_pk_mul_f32 v[66:67], v[66:67], s[22:23]
v_pk_mul_f32 v[68:69], v[68:69], s[22:23]
v_pk_mul_f32 v[70:71], v[70:71], s[22:23]
v_pk_mul_f32 v[96:97], v[96:97], s[22:23]
v_pk_mul_f32 v[98:99], v[98:99], s[22:23]
v_pk_mul_f32 v[100:101], v[100:101], s[22:23]
v_pk_mul_f32 v[102:103], v[102:103], s[22:23]
v_pk_mul_f32 v[128:129], v[128:129], s[22:23]
v_pk_mul_f32 v[130:131], v[130:131], s[22:23]
v_pk_mul_f32 v[132:133], v[132:133], s[22:23]
v_pk_mul_f32 v[134:135], v[134:135], s[22:23]
v_pk_mul_f32 v[40:41], v[40:41], s[22:23]
v_pk_mul_f32 v[42:43], v[42:43], s[22:23]
v_pk_mul_f32 v[44:45], v[44:45], s[22:23]
v_pk_mul_f32 v[46:47], v[46:47], s[22:23]
v_pk_mul_f32 v[72:73], v[72:73], s[22:23]
v_pk_mul_f32 v[74:75], v[74:75], s[22:23]
v_pk_mul_f32 v[76:77], v[76:77], s[22:23]
v_pk_mul_f32 v[78:79], v[78:79], s[22:23]
v_pk_mul_f32 v[104:105], v[104:105], s[22:23]
v_pk_mul_f32 v[106:107], v[106:107], s[22:23]
v_pk_mul_f32 v[108:109], v[108:109], s[22:23]
v_pk_mul_f32 v[110:111], v[110:111], s[22:23]
v_pk_mul_f32 v[136:137], v[136:137], s[22:23]
v_pk_mul_f32 v[138:139], v[138:139], s[22:23]
v_pk_mul_f32 v[140:141], v[140:141], s[22:23]
v_pk_mul_f32 v[142:143], v[142:143], s[22:23]
v_pk_mul_f32 v[48:49], v[48:49], s[22:23]
v_pk_mul_f32 v[50:51], v[50:51], s[22:23]
v_pk_mul_f32 v[52:53], v[52:53], s[22:23]
v_pk_mul_f32 v[54:55], v[54:55], s[22:23]
v_pk_mul_f32 v[80:81], v[80:81], s[22:23]
v_pk_mul_f32 v[82:83], v[82:83], s[22:23]
v_pk_mul_f32 v[84:85], v[84:85], s[22:23]
v_pk_mul_f32 v[86:87], v[86:87], s[22:23]
v_pk_mul_f32 v[112:113], v[112:113], s[22:23]
v_pk_mul_f32 v[114:115], v[114:115], s[22:23]
v_pk_mul_f32 v[116:117], v[116:117], s[22:23]
v_pk_mul_f32 v[118:119], v[118:119], s[22:23]
v_pk_mul_f32 v[144:145], v[144:145], s[22:23]
v_pk_mul_f32 v[146:147], v[146:147], s[22:23]
v_pk_mul_f32 v[148:149], v[148:149], s[22:23]
v_pk_mul_f32 v[150:151], v[150:151], s[22:23]
v_pk_mul_f32 v[56:57], v[56:57], s[22:23]
v_pk_mul_f32 v[58:59], v[58:59], s[22:23]
v_pk_mul_f32 v[60:61], v[60:61], s[22:23]
v_pk_mul_f32 v[62:63], v[62:63], s[22:23]
v_pk_mul_f32 v[88:89], v[88:89], s[22:23]
v_pk_mul_f32 v[90:91], v[90:91], s[22:23]
v_pk_mul_f32 v[92:93], v[92:93], s[22:23]
v_pk_mul_f32 v[94:95], v[94:95], s[22:23]
v_pk_mul_f32 v[120:121], v[120:121], s[22:23]
v_pk_mul_f32 v[122:123], v[122:123], s[22:23]
v_pk_mul_f32 v[124:125], v[124:125], s[22:23]
v_pk_mul_f32 v[126:127], v[126:127], s[22:23]
v_pk_mul_f32 v[152:153], v[152:153], s[22:23]
v_pk_mul_f32 v[154:155], v[154:155], s[22:23]
v_pk_mul_f32 v[156:157], v[156:157], s[22:23]
v_pk_mul_f32 v[158:159], v[158:159], s[22:23]
K 32,32,33
K 33,34,35
K 34,36,37
K 35,38,39
K 40,40,41
K 41,42,43
K 42,44,45
K 43,46,47
W 0
Zvav 196,128,32
s_add_u32 m0, s62, 0
Ra 144,242,4096
Ra 148,242,5120
Ra 152,242,6144
Ra 156,242,7168
Mvav 196,132,40
K 64,64,65
K 65,66,67
K 66,68,69
K 67,70,71
K 72,72,73
K 73,74,75
K 74,76,77
K 75,78,79
s_nop 1
Zvav 204,132,72
Dc 1,8,61,0
K 96,96,97
K 97,98,99
K 98,100,101
Mvav 204,128,64
K 99,102,103
K 104,104,105
K 105,106,107
K 106,108,109
K 107,110,111
Zvav 212,128,96
Dc 1,8,61,1024
K 128,128,129
K 129,130,131
K 130,132,133
Mvav 212,132,104
K 131,134,135
K 136,136,137
K 137,138,139
K 138,140,141
K 139,142,143
s_nop 1
Zvav 220,132,136
Dc 1,8,61,2048
K 48,48,49
K 49,50,51
K 50,52,53
Mvav 220,128,128
K 51,54,55
K 56,56,57
K 57,58,59
K 58,60,61
K 59,62,63
Mvav 196,136,48
Ra 128,242,8192
Ra 132,242,9216
K 80,80,81
K 81,82,83
Mvav 196,140,56
K 82,84,85
K 83,86,87
K 88,88,89
K 89,90,91
K 90,92,93
K 91,94,95
s_nop 1
Mvav 204,140,88
Dc 1,8,61,3072
s_add_u32 m0, s62, 4096
K 112,112,113
K 113,114,115
K 114,116,117
Mvav 204,136,80
K 115,118,119
K 120,120,121
K 121,122,123
K 122,124,125
K 123,126,127
Mvav 212,136,112
Dc 243,8,61,0
K 144,144,145
K 145,146,147
K 146,148,149
Mvav 212,140,120
K 147,150,151
K 152,152,153
K 153,154,155
K 154,156,157
K 155,158,159
s_nop 1
Mvav 220,140,152
s_add_u32 s80, s62, 131072
Dc 243,8,61,1024
v_add_u32 v12, s80, v1
Mvav 220,136,144
W 2
ds_write_b128 v12, v[32:35] offset:0
Ra 136,242,10240
Zvav 36,144,32
Ra 0,12,0
Ra 140,242,11264
Dc 243,8,61,2048
Mvav 36,148,40
v_max3_f32 v4, v196, v197, v198
v_max3_f32 v4, v4, v199, v252
v_max3_f32 v5, v204, v205, v206
v_max3_f32 v5, v5, v207, v253
Zvav 68,148,72
v_max3_f32 v6, v212, v213, v214
v_max3_f32 v6, v6, v215, v254
v_max3_f32 v7, v220, v221, v222
v_max3_f32 v7, v7, v223, v255
Mvav 68,144,64
v_mov_b32 v8, v4
v_mov_b32 v9, v5
v_mov_b32 v10, v6
v_mov_b32 v11, v7
Zvav 100,144,96
ds_write_b128 v12, v[40:43] offset:1024
v_permlane32_swap_b32 v4, v8
v_permlane32_swap_b32 v5, v9
v_permlane32_swap_b32 v6, v10
Mvav 100,148,104
v_permlane32_swap_b32 v7, v11
v_max_f32 v4, v4, v8
v_max_f32 v5, v5, v9
v_max_f32 v6, v6, v10
Zvav 132,148,136
v_max_f32 v7, v7, v11
v_mov_b32 v8, v4
v_mov_b32 v9, v5
v_mov_b32 v10, v6
Mvav 132,144,128
Ra 4,12,1024
v_mov_b32 v11, v7
v_permlane16_swap_b32 v4, v8
v_permlane16_swap_b32 v5, v9
Mvav 36,152,48
v_permlane16_swap_b32 v6, v10
v_permlane16_swap_b32 v7, v11
v_max_f32 v4, v4, v8
v_max_f32 v5, v5, v9
Mvav 36,156,56
v_max_f32 v6, v6, v10
v_max_f32 v7, v7, v11
v_sub_f32 v4, 0, v4
v_sub_f32 v5, 0, v5
Mvav 68,156,88
v_sub_f32 v6, 0, v6
v_sub_f32 v7, 0, v7
v_mov_b32 v224, v4
v_mov_b32 v225, v4
Mvav 68,152,80
v_mov_b32 v226, v4
v_mov_b32 v227, v4
v_mov_b32 v228, v5
v_mov_b32 v229, v5
Mvav 100,152,112
v_mov_b32 v230, v5
v_mov_b32 v231, v5
v_mov_b32 v232, v6
v_mov_b32 v233, v6
Mvav 100,156,120
v_mov_b32 v234, v6
v_mov_b32 v235, v6
v_mov_b32 v236, v7
v_mov_b32 v237, v7
Mvav 132,156,152
Dc 243,8,61,3072
s_add_u32 m0, s62, 8192
ds_write_b128 v12, v[48:51] offset:2048
v_mov_b32 v238, v7
v_mov_b32 v239, v7
Mvav 132,152,144
W 3
Ra 8,12,2048
ds_write_b128 v12, v[56:59] offset:3072
Ra 144,242,12288
Ra 12,12,3072
Cvav 160,128,32,224
s_and_b32 s80, s14, 1
Dc 244,8,61,0
Ra 148,242,13312
ds_write_b128 v12, v[64:67] offset:4096
Ra 16,12,4096
Mvav 160,132,40
s_lshl_b32 s80, s80, 14
ds_write_b128 v12, v[72:75] offset:5120
Ra 20,12,5120
ds_write_b128 v12, v[80:83] offset:6144
A 196,196,224
Cvav 164,132,72,228
s_add_u32 s80, s80, 98304
Dc 244,8,61,1024
Ra 24,12,6144
A 197,197,224
A 198,198,224
Mvav 164,128,64
Ra 152,242,14336
A 199,199,224
X 196
Cvav 168,128,96,232
Dc 244,8,61,2048
Ra 156,242,15360
X 197
Mvav 168,132,104
ds_write_b128 v12, v[88:91] offset:7168
Ra 28,12,7168
X 198
Cvav 172,132,136,236
Dc 244,8,61,3072
s_add_u32 m0, s62, 12288
K 192,196,197
X 199
Mvav 172,128,128
ds_write_b128 v12, v[96:99] offset:8192
A 204,204,228
v_add_u32 v8, s80, v1
K 193,198,199
Mvav 160,136,48
Dc 245,8,61,0
Ra 32,12,8192
A 205,205,228
A 206,206,228
Mvav 160,140,56
ds_write_b128 v8, v[4:7]
A 207,207,228
X 204
Mvav 164,140,88
Dc 245,8,61,1024
ds_write_b128 v12, v[104:107] offset:9216
X 205
Mvav 164,136,80
Ra 36,12,9216
X 206
K 200,204,205
Mvav 168,136,112
Dc 245,8,61,2048
Ra 128,242,24576
X 207
Mvav 168,140,120
ds_write_b128 v12, v[112:115] offset:10240
Ra 132,242,25600
A 212,212,232
K 201,206,207
Mvav 172,140,152
Dc 245,8,61,3072
s_add_u32 s61, s61, s63
Ra 40,12,10240
A 213,213,232
A 214,214,232
Mvav 172,136,144
W 11
ds_write_b128 v12, v[120:123] offset:11264
A 215,215,232
X 212
Cvav 176,144,32,224
X 213
X 214
Mvav 176,148,40
Ra 44,12,11264
K 208,212,213
X 215
Cvav 180,148,72,228
ds_write_b128 v12, v[128:131] offset:12288
A 220,220,236
K 209,214,215
A 221,221,236
Mvav 180,144,64
Ra 48,12,12288
Ra 136,242,26624
A 222,222,236
A 223,223,236
Cvav 184,144,96,232
ds_write_b128 v12, v[136:139] offset:13312
Ra 140,242,27648
X 220
Mvav 184,148,104
X 221
X 222
Cvav 188,148,136,236
Ra 52,12,13312
K 216,220,221
X 223
Mvav 188,144,128
ds_write_b128 v12, v[144:147] offset:14336
Ra 56,12,14336
A 36,36,224
K 217,222,223
Mvav 176,152,48
ds_write_b128 v12, v[152:155] offset:15360
A 37,37,224
A 38,38,224
A 39,39,224
Mvav 176,156,56
X 36
X 37
Mvav 180,156,88
Ra 60,12,15360
X 38
K 194,36,37
Mvav 180,152,80
Ra 144,242,28672
X 39
A 68,68,228
Mvav 184,152,112
Ra 148,242,29696
K 195,38,39
A 69,69,228
A 70,70,228
Mvav 184,156,120
W 3
s_barrier
A 71,71,228
X 68
)"
R"(Mvav 188,156,152
X 69
X 70
K 202,68,69
X 71
A 100,100,232
K 203,70,71
A 101,101,232
A 102,102,232
A 103,103,232
X 100
X 101
X 102
K 210,100,101
X 103
A 132,132,236
K 211,102,103
A 133,133,236
A 134,134,236
A 135,135,236
X 132
X 133
X 134
K 218,132,133
X 135
s_nop 0
K 219,134,135
Mvav 188,152,144
Ra 152,242,30720
Zvav 32,128,192
Ra 156,242,31744
Zvav 64,128,200
Zvav 96,128,208
s_xor_b32 s62, s62, 0x10000
Zvav 128,128,216
v_add_u32 v23, 61440, v21
Zvav 132,132,216
v_add_u32 v24, 61440, v22
Zvav 100,132,208
v_add_u32 v25, 122880, v21
Zvav 68,132,200
v_add_u32 v26, 122880, v22
Zvav 36,132,192
Zvav 40,136,192
X 172
Zvav 72,136,200
X 173
Zvav 104,136,208
X 174
K 220,172,173
Zvav 136,136,216
X 175
Zvav 140,140,216
X 168
K 221,174,175
Zvav 108,140,208
X 169
X 170
Zvav 76,140,200
s_waitcnt vmcnt(0) lgkmcnt(0)
s_barrier
v_xor_b32 v242, 0x10000, v242
K 212,168,169
Zvav 44,140,192
X 171
X 164
Zvav 48,144,192
Ra 128,242,0
K 213,170,171
X 165
Zvav 80,144,200
X 166
Zvav 112,144,208
Ra 132,242,1024
K 204,164,165
X 167
Zvav 144,144,216
X 160
Zvav 148,148,216
K 205,166,167
X 161
Zvav 116,148,208
Ra 136,242,2048
X 162
Zvav 84,148,200
K 196,160,161
X 163
Zvav 52,148,192
X 176
K 197,162,163
Zvav 56,152,192
Ra 140,242,3072
X 177
Zvav 88,152,200
X 178
K 198,176,177
Zvav 120,152,208
X 179
X 180
Zvav 152,152,216
K 199,178,179
Zvav 156,156,216
X 181
X 182
Zvav 124,156,208
K 206,180,181
X 183
Zvav 92,156,200
K 207,182,183
Zvav 60,156,192
s_cmp_eq_u64 s[24:25], 0
s_cbranch_scc1 .Ll1_32%=
W 0
Cvaa 160,128,0,224
Ra 144,242,4096
Ra 148,242,5120
Mvaa 160,132,4
s_add_u32 m0, s62, 0
Ra 152,242,6144
Cvaa 164,132,20,228
X 184
Mvaa 164,128,16
X 185
Cvaa 168,128,32,232
X 186
Mvaa 168,132,36
Ra 156,242,7168
K 214,184,185
Cvaa 172,132,52,236
Dc 1,8,61,0
Mvaa 172,128,48
X 187
Mvaa 160,136,8
X 188
Mvaa 160,140,12
Ra 128,242,16384
K 215,186,187
Mvaa 164,140,28
Dc 1,8,61,1024
Mvaa 164,136,24
X 189
Mvaa 168,136,40
X 190
Mvaa 168,140,44
Ra 132,242,17408
K 222,188,189
Mvaa 172,140,60
Dc 1,8,61,2048
Mvaa 172,136,56
X 191
v_smfmac_f32_16x16x64_bf16 a[160:163], a[176:179], v[192:199], v240
Ra 136,242,18432
K 223,190,191
v_smfmac_f32_16x16x64_bf16 a[164:167], a[176:179], v[200:207], v240
X 160
v_smfmac_f32_16x16x64_bf16 a[168:171], a[176:179], v[208:215], v240
Dc 1,8,61,3072
v_smfmac_f32_16x16x64_bf16 a[172:175], a[176:179], v[216:223], v240
W 3
X 161
Cvaa 176,144,0,224
s_add_u32 m0, s62, 4096
X 162
Mvaa 176,148,4
Ra 140,242,19456
K 192,160,161
Cvaa 180,148,20,228
X 163
Mvaa 180,144,16
Dc 243,8,61,0
Cvaa 184,144,32,232
X 164
Mvaa 184,148,36
K 193,162,163
Cvaa 188,148,52,236
X 165
Mvaa 188,144,48
Dc 243,8,61,1024
Ra 144,242,20480
Mvaa 176,152,8
X 166
Mvaa 176,156,12
Ra 148,242,21504
K 200,164,165
Mvaa 180,156,28
X 167
Mvaa 180,152,24
Dc 243,8,61,2048
Mvaa 184,152,40
X 168
Mvaa 184,156,44
K 201,166,167
Mvaa 188,156,60
X 169
Mvaa 188,152,56
W 2
Dc 243,8,61,3072
Ra 152,242,22528
Mvav 32,128,196
s_add_u32 m0, s62, 8192
X 170
Mvav 64,128,204
Ra 156,242,23552
K 208,168,169
Mvav 96,128,212
X 171
Mvav 128,128,220
Dc 244,8,61,0
Mvav 132,132,220
X 172
Mvav 100,132,212
Ra 128,242,8192
K 209,170,171
Mvav 68,132,204
X 173
Mvav 36,132,196
Dc 244,8,61,1024
Mvav 40,136,196
X 174
Mvav 72,136,204
Ra 132,242,9216
K 216,172,173
Mvav 104,136,212
X 175
Mvav 136,136,220
Dc 244,8,61,2048
Mvav 140,140,220
X 176
Mvav 108,140,212
Ra 136,242,10240
K 217,174,175
Mvav 76,140,204
X 177
Mvav 44,140,196
W 3
Dc 244,8,61,3072
Mvav 48,144,196
X 178
Mvav 80,144,204
s_add_u32 m0, s62, 12288
Ra 140,242,11264
Mvav 112,144,212
K 194,176,177
Mvav 144,144,220
X 179
Mvav 148,148,220
X 180
Mvav 116,148,212
Dc 245,8,61,0
Ra 144,242,12288
Mvav 84,148,204
K 195,178,179
Mvav 52,148,196
X 181
Mvav 56,152,196
X 182
Mvav 88,152,204
Dc 245,8,61,1024
Ra 148,242,13312
Mvav 120,152,212
s_cmp_eq_u32 s14, 2
s_cselect_b64 s[18:19], s[30:31], s[32:33]
K 202,180,181
Mvav 152,152,220
s_lshr_b32 s81, s15, 3
X 183
Mvav 156,156,220
s_mul_i32 s82, s81, 69120
X 184
Mvav 124,156,212
s_mul_i32 s83, s59, 448
Dc 245,8,61,2048
Ra 152,242,14336
Mvav 92,156,204
s_andn2_b32 s83, s83, 127
K 203,182,183
Mvav 60,156,196
W 3
X 185
Cvaa 160,128,0,224
s_add_u32 s82, s82, s83
X 186
Mvaa 160,132,4
Dc 245,8,61,3072
Ra 156,242,15360
Cvaa 164,132,20,228
s_add_u32 s61, s61, s63
v_min_u32 v246, 624, v1
v_lshrrev_b32 v247, 6, v246
Mvaa 164,128,16
K 210,184,185
v_mul_u32_u24 v247, 3456, v247
Cvaa 168,128,32,232
X 187
Mvaa 168,132,36
v_and_b32 v246, 48, v246
v_lshl_add_u32 v246, v246, 3, v247
Cvaa 172,132,52,236
s_xor_b32 s62, s62, 0x10000
X 188
Mvaa 172,128,48
v_add_u32 v246, s82, v246
K 211,186,187
Mvaa 160,136,8
global_load_dword v3, v246, s[18:19]
Ra 128,242,24576
Mvaa 160,140,12
s_add_u32 s64, s64, 1
X 189
Mvaa 164,140,28
Ra 132,242,25600
v_add_u32 v246, 34560, v246
Mvaa 164,136,24
X 190
Mvaa 168,136,40
v_min_u32 v246, 2211836, v246
K 218,188,189
Mvaa 168,140,44
global_load_dword v3, v246, s[18:19]
Mvaa 172,140,60
s_add_u32 s36, s29, 131072
X 191
Mvaa 172,136,56
W 2
X 160
Cvaa 176,144,0,224
Ra 136,242,26624
K 219,190,191
Mvaa 176,148,4
X 161
Cvaa 180,148,20,228
Ra 140,242,27648
Mvaa 180,144,16
X 162
Cvaa 184,144,32,232
buffer_store_dwordx4 a[124:127], v1, s[8:11], s36 offen offset:0 sc1
K 196,160,161
Mvaa 184,148,36
X 163
Cvaa 188,148,52,236
X 164
Mvaa 188,144,48
Ra 144,242,28672
K 197,162,163
Mvaa 176,152,8
X 165
Mvaa 176,156,12
Ra 148,242,29696
Mvaa 180,156,28
X 166
Mvaa 180,152,24
buffer_store_dwordx4 a[116:119], v1, s[8:11], s36 offen offset:1024 sc1
K 204,164,165
Mvaa 184,152,40
X 167
Mvaa 184,156,44
X 168
Mvaa 188,156,60
Mvaa 188,152,56
W 2
Ra 152,242,30720
K 205,166,167
Mvav 32,128,192
X 169
Mvav 64,128,200
buffer_store_dwordx4 a[108:111], v1, s[8:11], s36 offen offset:2048 sc1
Ra 156,242,31744
Mvav 96,128,208
X 170
Mvav 128,128,216
K 212,168,169
Mvav 132,132,216
X 171
Mvav 100,132,208
X 172
Mvav 68,132,200
buffer_store_dwordx4 a[204:207], v1, s[8:11], s36 offen offset:3072 sc1
Mvav 36,132,192
K 213,170,171
Mvav 40,136,192
X 173
Mvav 72,136,200
X 174
Mvav 104,136,208
Mvav 136,136,216
s_add_u32 s36, s29, 135168
K 220,172,173
Mvav 140,140,216
X 175
Mvav 108,140,208
X 176
Mvav 76,140,200
s_waitcnt vmcnt(6) lgkmcnt(0)
s_barrier
v_xor_b32 v242, 0x10000, v242
Mvav 44,140,192
Ra 128,242,0
K 221,174,175
Mvav 48,144,192
X 177
)"
R"(Mvav 80,144,200
buffer_store_dwordx4 a[212:215], v1, s[8:11], s36 offen offset:0 sc1
Mvav 112,144,208
X 178
Mvav 144,144,216
Ra 132,242,1024
K 198,176,177
Mvav 148,148,216
X 179
Mvav 116,148,208
X 180
Mvav 84,148,200
Ra 136,242,2048
Mvav 52,148,192
buffer_store_dwordx4 a[220:223], v1, s[8:11], s36 offen offset:1024 sc1
K 199,178,179
Mvav 56,152,192
X 181
Mvav 88,152,200
Ra 140,242,3072
Mvav 120,152,208
X 182
Mvav 152,152,216
K 206,180,181
Mvav 156,156,216
X 183
Mvav 124,156,208
Mvav 92,156,200
K 207,182,183
Mvav 60,156,192
W 0
Cvaa 160,128,0,224
Ra 144,242,4096
Ra 148,242,5120
Mvaa 160,132,4
s_add_u32 m0, s62, 0
Ra 152,242,6144
Cvaa 164,132,20,228
X 184
Mvaa 164,128,16
X 185
Cvaa 168,128,32,232
X 186
Mvaa 168,132,36
Ra 156,242,7168
K 214,184,185
Cvaa 172,132,52,236
Dc 1,8,61,0
Mvaa 172,128,48
X 187
Mvaa 160,136,8
X 188
Mvaa 160,140,12
Ra 128,242,16384
K 215,186,187
Mvaa 164,140,28
Dc 1,8,61,1024
Mvaa 164,136,24
X 189
Mvaa 168,136,40
X 190
Mvaa 168,140,44
Ra 132,242,17408
K 222,188,189
Mvaa 172,140,60
Dc 1,8,61,2048
Mvaa 172,136,56
X 191
v_smfmac_f32_16x16x64_bf16 a[160:163], a[176:179], v[192:199], v240
Ra 136,242,18432
K 223,190,191
v_smfmac_f32_16x16x64_bf16 a[164:167], a[176:179], v[200:207], v240
X 160
v_smfmac_f32_16x16x64_bf16 a[168:171], a[176:179], v[208:215], v240
Dc 1,8,61,3072
v_smfmac_f32_16x16x64_bf16 a[172:175], a[176:179], v[216:223], v240
W 3
X 161
Cvaa 176,144,0,224
s_add_u32 m0, s62, 4096
X 162
Mvaa 176,148,4
Ra 140,242,19456
K 192,160,161
Cvaa 180,148,20,228
X 163
Mvaa 180,144,16
Dc 243,8,61,0
Cvaa 184,144,32,232
X 164
Mvaa 184,148,36
K 193,162,163
Cvaa 188,148,52,236
X 165
Mvaa 188,144,48
Dc 243,8,61,1024
Ra 144,242,20480
Mvaa 176,152,8
X 166
Mvaa 176,156,12
Ra 148,242,21504
K 200,164,165
Mvaa 180,156,28
X 167
Mvaa 180,152,24
Dc 243,8,61,2048
Mvaa 184,152,40
X 168
Mvaa 184,156,44
K 201,166,167
Mvaa 188,156,60
X 169
Mvaa 188,152,56
W 2
Dc 243,8,61,3072
Ra 152,242,22528
Mvav 32,128,196
s_add_u32 m0, s62, 8192
X 170
Mvav 64,128,204
Ra 156,242,23552
K 208,168,169
Mvav 96,128,212
X 171
Mvav 128,128,220
Dc 244,8,61,0
Mvav 132,132,220
X 172
Mvav 100,132,212
Ra 128,242,8192
K 209,170,171
Mvav 68,132,204
X 173
Mvav 36,132,196
Dc 244,8,61,1024
Mvav 40,136,196
X 174
Mvav 72,136,204
Ra 132,242,9216
K 216,172,173
Mvav 104,136,212
X 175
Mvav 136,136,220
Dc 244,8,61,2048
Mvav 140,140,220
X 176
Mvav 108,140,212
Ra 136,242,10240
K 217,174,175
Mvav 76,140,204
X 177
Mvav 44,140,196
W 3
Dc 244,8,61,3072
Mvav 48,144,196
X 178
Mvav 80,144,204
s_add_u32 m0, s62, 12288
Ra 140,242,11264
Mvav 112,144,212
K 194,176,177
Mvav 144,144,220
X 179
Mvav 148,148,220
X 180
Mvav 116,148,212
Dc 245,8,61,0
Ra 144,242,12288
Mvav 84,148,204
K 195,178,179
Mvav 52,148,196
X 181
Mvav 56,152,196
X 182
Mvav 88,152,204
Dc 245,8,61,1024
Ra 148,242,13312
Mvav 120,152,212
K 202,180,181
Mvav 152,152,220
X 183
Mvav 156,156,220
X 184
Mvav 124,156,212
Dc 245,8,61,2048
Ra 152,242,14336
Mvav 92,156,204
K 203,182,183
v_bfe_u32 v28, v1, 4, 4
Mvav 60,156,196
W 3
X 185
Cvaa 160,128,0,224
Ra 156,242,15360
v_lshlrev_b32 v28, 3, v28
Mvaa 160,132,4
s_lshl_b32 s80, s56, 9
s_add_u32 s80, s80, 4224
Dc 245,8,61,3072
Cvaa 164,132,20,228
s_add_u32 s61, s61, s63
X 186
Mvaa 164,128,16
K 210,184,185
Cvaa 168,128,32,232
X 187
Mvaa 168,132,36
buffer_load_dwordx2 v[28:29], v28, s[8:11], s80 offen sc1
Cvaa 172,132,52,236
s_xor_b32 s62, s62, 0x10000
X 188
Mvaa 172,128,48
Ra 128,242,24576
K 211,186,187
Mvaa 160,136,8
X 189
Mvaa 160,140,12
s_add_u32 s64, s64, 1
X 190
Mvaa 164,140,28
Ra 132,242,25600
K 218,188,189
Mvaa 164,136,24
X 191
Mvaa 168,136,40
Mvaa 168,140,44
X 160
Mvaa 172,140,60
buffer_store_dwordx4 a[228:231], v1, s[8:11], s36 offen offset:2048 sc1
K 219,190,191
Mvaa 172,136,56
W 2
X 161
Cvaa 176,144,0,224
Ra 136,242,26624
Ra 140,242,27648
Mvaa 176,148,4
X 162
Cvaa 180,148,20,228
K 196,160,161
Mvaa 180,144,16
X 163
Cvaa 184,144,32,232
Mvaa 184,148,36
X 164
Cvaa 188,148,52,236
buffer_store_dwordx4 a[76:79], v1, s[8:11], s36 offen offset:3072 sc1
K 197,162,163
Mvaa 188,144,48
X 165
Mvaa 176,152,8
Ra 144,242,28672
Ra 148,242,29696
Mvaa 176,156,12
X 166
Mvaa 180,156,28
K 204,164,165
Mvaa 180,152,24
X 167
Mvaa 184,152,40
s_add_u32 s36, s29, 327680
Mvaa 184,156,44
X 168
Mvaa 188,156,60
Mvaa 188,152,56
W 2
Ra 152,242,30720
K 205,166,167
Mvav 32,128,192
X 169
Mvav 64,128,200
Ra 156,242,31744
Mvav 96,128,208
X 170
Mvav 128,128,216
buffer_store_dwordx4 a[84:87], v1, s[8:11], s36 offen offset:0 sc1
K 212,168,169
Mvav 132,132,216
X 171
Mvav 100,132,208
X 172
Mvav 68,132,200
Mvav 36,132,192
K 213,170,171
Mvav 40,136,192
X 173
Mvav 72,136,200
buffer_store_dwordx4 a[92:95], v1, s[8:11], s36 offen offset:1024 sc1
Mvav 104,136,208
X 174
Mvav 136,136,216
s_waitcnt vmcnt(4)
v_cmp_eq_u64 vcc, s[12:13], v[28:29]
s_cmp_eq_u64 vcc, -1
s_cbranch_scc0 .Lspin_65%=
.Lok_66%=:
K 220,172,173
Mvav 140,140,216
X 175
Mvav 108,140,208
X 176
Mvav 76,140,200
W 0
s_barrier
v_xor_b32 v242, 0x10000, v242
Mvav 44,140,192
buffer_store_dwordx4 a[188:191], v1, s[8:11], s36 offen offset:2048 sc1
K 221,174,175
Mvav 48,144,192
Ra 128,242,0
Mvav 80,144,200
X 177
Mvav 112,144,208
X 178
Mvav 144,144,216
Ra 132,242,1024
K 198,176,177
Mvav 148,148,216
X 179
Mvav 116,148,208
X 180
Mvav 84,148,200
Ra 136,242,2048
buffer_store_dwordx4 a[196:199], v1, s[8:11], s36 offen offset:3072 sc1
Mvav 52,148,192
K 199,178,179
Mvav 56,152,192
X 181
Mvav 88,152,200
Ra 140,242,3072
Mvav 120,152,208
X 182
Mvav 152,152,216
K 206,180,181
Mvav 156,156,216
X 183
Mvav 124,156,208
Mvav 92,156,200
K 207,182,183
Mvav 60,156,192
W 0
Cvaa 160,128,0,224
Ra 144,242,4096
Ra 148,242,5120
Mvaa 160,132,4
s_add_u32 m0, s62, 0
Ra 152,242,6144
Cvaa 164,132,20,228
X 184
Mvaa 164,128,16
X 185
Cvaa 168,128,32,232
X 186
Mvaa 168,132,36
Ra 156,242,7168
K 214,184,185
Cvaa 172,132,52,236
Dc 1,8,61,0
Mvaa 172,128,48
X 187
Mvaa 160,136,8
X 188
Mvaa 160,140,12
Ra 128,242,16384
K 215,186,187
Mvaa 164,140,28
Dc 1,8,61,1024
Mvaa 164,136,24
X 189
Mvaa 168,136,40
X 190
Mvaa 168,140,44
Ra 132,242,17408
K 222,188,189
Mvaa 172,140,60
Dc 1,8,61,2048
Mvaa 172,136,56
X 191
v_smfmac_f32_16x16x64_bf16 a[160:163], a[176:179], v[192:199], v240
Ra 136,242,18432
K 223,190,191
v_smfmac_f32_16x16x64_bf16 a[164:167], a[176:179], v[200:207], v240
X 160
v_smfmac_f32_16x16x64_bf16 a[168:171], a[176:179], v[208:215], v240
Dc 1,8,61,3072
v_smfmac_f32_16x16x64_bf16 a[172:175], a[176:179], v[216:223], v240
W 3
X 161
Cvaa 176,144,0,224
s_add_u32 m0, s62, 4096
X 162
Mvaa 176,148,4
Ra 140,242,19456
K 192,160,161
Cvaa 180,148,20,228
X 163
Mvaa 180,144,16
Dc 243,8,61,0
Cvaa 184,144,32,232
X 164
)"
R"(Mvaa 184,148,36
K 193,162,163
Cvaa 188,148,52,236
X 165
Mvaa 188,144,48
Dc 243,8,61,1024
Ra 144,242,20480
Mvaa 176,152,8
X 166
Mvaa 176,156,12
Ra 148,242,21504
K 200,164,165
Mvaa 180,156,28
X 167
Mvaa 180,152,24
Dc 243,8,61,2048
Mvaa 184,152,40
X 168
Mvaa 184,156,44
K 201,166,167
Mvaa 188,156,60
X 169
Mvaa 188,152,56
W 2
Dc 243,8,61,3072
Ra 152,242,22528
Mvav 32,128,196
s_add_u32 m0, s62, 8192
X 170
Mvav 64,128,204
Ra 156,242,23552
K 208,168,169
Mvav 96,128,212
X 171
Mvav 128,128,220
Dc 244,8,61,0
Mvav 132,132,220
X 172
Mvav 100,132,212
Ra 128,242,8192
K 209,170,171
Mvav 68,132,204
X 173
Mvav 36,132,196
Dc 244,8,61,1024
Mvav 40,136,196
X 174
Mvav 72,136,204
Ra 132,242,9216
K 216,172,173
Mvav 104,136,212
X 175
Mvav 136,136,220
Dc 244,8,61,2048
Mvav 140,140,220
X 176
Mvav 108,140,212
Ra 136,242,10240
K 217,174,175
Mvav 76,140,204
X 177
Mvav 44,140,196
W 3
Dc 244,8,61,3072
Mvav 48,144,196
X 178
Mvav 80,144,204
s_add_u32 m0, s62, 12288
Ra 140,242,11264
Mvav 112,144,212
K 194,176,177
Mvav 144,144,220
X 179
Mvav 148,148,220
X 180
Mvav 116,148,212
Dc 245,8,61,0
Ra 144,242,12288
Mvav 84,148,204
K 195,178,179
Mvav 52,148,196
X 181
Mvav 56,152,196
X 182
Mvav 88,152,204
Dc 245,8,61,1024
Ra 148,242,13312
Mvav 120,152,212
K 202,180,181
Mvav 152,152,220
X 183
Mvav 156,156,220
X 184
Mvav 124,156,212
Dc 245,8,61,2048
Ra 152,242,14336
Mvav 92,156,204
s_mov_b64 s[18:19], s[34:35]
K 203,182,183
Mvav 60,156,196
W 3
X 185
Cvaa 160,128,0,224
s_lshr_b32 s81, s15, 3
X 186
Mvaa 160,132,4
s_lshl_b32 s82, s59, 5
Dc 245,8,61,3072
Ra 156,242,15360
Cvaa 164,132,20,228
s_add_u32 s81, s81, s82
s_add_u32 s61, s61, s63
K 210,184,185
Mvaa 164,128,16
s_lshl_b32 s81, s81, 1
X 187
Cvaa 168,128,32,232
s_add_u32 s81, s81, s14
X 188
Mvaa 168,132,36
s_sub_u32 s81, s81, 2
s_mul_i32 s81, s81, 4480
Cvaa 172,132,52,236
s_xor_b32 s62, s62, 0x10000
K 211,186,187
v_min_u32 v246, 544, v1
Mvaa 172,128,48
X 189
Mvaa 160,136,8
Ra 128,242,24576
v_lshl_add_u32 v246, v246, 3, s81
Mvaa 160,140,12
s_add_u32 s64, s64, 1
X 190
Mvaa 164,140,28
Ra 132,242,25600
v_min_u32 v246, 2211836, v246
Mvaa 164,136,24
global_load_dword v3, v246, s[18:19]
K 218,188,189
Mvaa 168,136,40
X 191
Mvaa 168,140,44
X 160
Mvaa 172,140,60
s_add_u32 s36, s29, 331776
K 219,190,191
Mvaa 172,136,56
W 2
X 161
Cvaa 176,144,0,224
Ra 136,242,26624
Ra 140,242,27648
Mvaa 176,148,4
X 162
Cvaa 180,148,20,228
K 196,160,161
Mvaa 180,144,16
X 163
Cvaa 184,144,32,232
buffer_store_dwordx4 a[236:239], v1, s[8:11], s36 offen offset:0 sc1
Mvaa 184,148,36
X 164
Cvaa 188,148,52,236
K 197,162,163
Mvaa 188,144,48
X 165
Mvaa 176,152,8
Ra 144,242,28672
Ra 148,242,29696
Mvaa 176,156,12
X 166
Mvaa 180,156,28
buffer_store_dwordx4 a[244:247], v1, s[8:11], s36 offen offset:1024 sc1
K 204,164,165
Mvaa 180,152,24
X 167
Mvaa 184,152,40
Mvaa 184,156,44
X 168
Mvaa 188,156,60
Mvaa 188,152,56
W 2
Ra 152,242,30720
K 205,166,167
Mvav 32,128,192
X 169
Mvav 64,128,200
buffer_store_dwordx4 a[68:71], v1, s[8:11], s36 offen offset:2048 sc1
Ra 156,242,31744
Mvav 96,128,208
X 170
Mvav 128,128,216
K 212,168,169
Mvav 132,132,216
X 171
Mvav 100,132,208
X 172
Mvav 68,132,200
buffer_store_dwordx4 a[184:187], v1, s[8:11], s36 offen offset:3072 sc1
Mvav 36,132,192
K 213,170,171
Mvav 40,136,192
X 173
Mvav 72,136,200
X 174
Mvav 104,136,208
Mvav 136,136,216
s_add_u32 s36, s29, 196608
K 220,172,173
Mvav 140,140,216
X 175
Mvav 108,140,208
X 176
Mvav 76,140,200
s_waitcnt vmcnt(5) lgkmcnt(0)
s_barrier
v_xor_b32 v242, 0x10000, v242
Mvav 44,140,192
Ra 128,242,0
K 221,174,175
Mvav 48,144,192
X 177
Mvav 80,144,200
buffer_store_dwordx4 a[192:195], v1, s[8:11], s36 offen offset:0 sc1
Mvav 112,144,208
X 178
Mvav 144,144,216
Ra 132,242,1024
K 198,176,177
Mvav 148,148,216
X 179
Mvav 116,148,208
X 180
Mvav 84,148,200
Ra 136,242,2048
Mvav 52,148,192
buffer_store_dwordx4 a[200:203], v1, s[8:11], s36 offen offset:1024 sc1
K 199,178,179
Mvav 56,152,192
X 181
Mvav 88,152,200
Ra 140,242,3072
Mvav 120,152,208
X 182
Mvav 152,152,216
K 206,180,181
Mvav 156,156,216
X 183
Mvav 124,156,208
Mvav 92,156,200
K 207,182,183
Mvav 60,156,192
W 0
Cvaa 160,128,0,224
Ra 144,242,4096
Ra 148,242,5120
Mvaa 160,132,4
s_add_u32 m0, s62, 0
Ra 152,242,6144
Cvaa 164,132,20,228
X 184
Mvaa 164,128,16
X 185
Cvaa 168,128,32,232
X 186
Mvaa 168,132,36
Ra 156,242,7168
K 214,184,185
Cvaa 172,132,52,236
Dc 1,8,61,0
Mvaa 172,128,48
X 187
Mvaa 160,136,8
X 188
Mvaa 160,140,12
Ra 128,242,16384
K 215,186,187
Mvaa 164,140,28
Dc 1,8,61,1024
Mvaa 164,136,24
X 189
Mvaa 168,136,40
X 190
Mvaa 168,140,44
Ra 132,242,17408
K 222,188,189
Mvaa 172,140,60
Dc 1,8,61,2048
Mvaa 172,136,56
X 191
v_smfmac_f32_16x16x64_bf16 a[160:163], a[176:179], v[192:199], v240
Ra 136,242,18432
K 223,190,191
v_smfmac_f32_16x16x64_bf16 a[164:167], a[176:179], v[200:207], v240
X 160
v_smfmac_f32_16x16x64_bf16 a[168:171], a[176:179], v[208:215], v240
Dc 1,8,61,3072
v_smfmac_f32_16x16x64_bf16 a[172:175], a[176:179], v[216:223], v240
W 3
X 161
Cvaa 176,144,0,224
s_add_u32 m0, s62, 4096
X 162
Mvaa 176,148,4
Ra 140,242,19456
K 192,160,161
Cvaa 180,148,20,228
X 163
Mvaa 180,144,16
Dc 243,8,61,0
Cvaa 184,144,32,232
X 164
Mvaa 184,148,36
K 193,162,163
Cvaa 188,148,52,236
X 165
Mvaa 188,144,48
Dc 243,8,61,1024
Ra 144,242,20480
Mvaa 176,152,8
X 166
Mvaa 176,156,12
Ra 148,242,21504
K 200,164,165
Mvaa 180,156,28
X 167
Mvaa 180,152,24
Dc 243,8,61,2048
Mvaa 184,152,40
X 168
Mvaa 184,156,44
K 201,166,167
Mvaa 188,156,60
X 169
Mvaa 188,152,56
W 2
Dc 243,8,61,3072
Ra 152,242,22528
Mvav 32,128,196
s_add_u32 m0, s62, 8192
X 170
Mvav 64,128,204
Ra 156,242,23552
K 208,168,169
Mvav 96,128,212
X 171
Mvav 128,128,220
Dc 244,8,61,0
Mvav 132,132,220
X 172
Mvav 100,132,212
Ra 128,242,8192
K 209,170,171
Mvav 68,132,204
X 173
Mvav 36,132,196
Dc 244,8,61,1024
Mvav 40,136,196
X 174
Mvav 72,136,204
Ra 132,242,9216
K 216,172,173
Mvav 104,136,212
X 175
Mvav 136,136,220
Dc 244,8,61,2048
Mvav 140,140,220
X 176
Mvav 108,140,212
Ra 136,242,10240
K 217,174,175
Mvav 76,140,204
X 177
Mvav 44,140,196
W 3
Dc 244,8,61,3072
Mvav 48,144,196
X 178
Mvav 80,144,204
s_add_u32 m0, s62, 12288
Ra 140,242,11264
Mvav 112,144,212
K 194,176,177
Mvav 144,144,220
X 179
Mvav 148,148,220
X 180
Mvav 116,148,212
Dc 245,8,61,0
Ra 144,242,12288
Mvav 84,148,204
K 195,178,179
Mvav 52,148,196
X 181
Mvav 56,152,196
X 182
Mvav 88,152,204
Dc 245,8,61,1024
Ra 148,242,13312
)"
R"(Mvav 120,152,212
K 202,180,181
Mvav 152,152,220
X 183
Mvav 156,156,220
s_waitcnt vmcnt(16)
s_add_u32 s80, s71, 256
v_mov_b64 v[28:29], s[12:13]
s_mov_b64 exec, 1
buffer_store_dwordx2 v[28:29], off, s[8:11], s80 sc1
s_mov_b64 exec, -1
X 184
Mvav 124,156,212
Ra 152,242,14336
Mvav 92,156,204
K 203,182,183
Mvav 60,156,196
W 3
X 185
Cvaa 160,128,0,224
Dc 245,8,61,2048
Ra 156,242,15360
Mvaa 160,132,4
X 186
Cvaa 164,132,20,228
K 210,184,185
Mvaa 164,128,16
X 187
Cvaa 168,128,32,232
Dc 245,8,61,3072
Mvaa 168,132,36
s_add_u32 s61, s61, s63
X 188
Cvaa 172,132,52,236
s_xor_b32 s62, s62, 0x10000
K 211,186,187
Mvaa 172,128,48
X 189
Mvaa 160,136,8
Ra 128,242,24576
Ra 132,242,25600
Mvaa 160,140,12
s_add_u32 s64, s64, 1
X 190
Mvaa 164,140,28
K 218,188,189
Mvaa 164,136,24
X 191
Mvaa 168,136,40
Mvaa 168,140,44
X 160
Mvaa 172,140,60
buffer_store_dwordx4 a[208:211], v1, s[8:11], s36 offen offset:2048 sc1
K 219,190,191
Mvaa 172,136,56
W 2
X 161
Cvaa 176,144,0,224
Ra 136,242,26624
Ra 140,242,27648
Mvaa 176,148,4
X 162
Cvaa 180,148,20,228
K 196,160,161
Mvaa 180,144,16
X 163
Cvaa 184,144,32,232
Mvaa 184,148,36
X 164
Cvaa 188,148,52,236
buffer_store_dwordx4 a[216:219], v1, s[8:11], s36 offen offset:3072 sc1
K 197,162,163
Mvaa 188,144,48
X 165
Mvaa 176,152,8
Ra 144,242,28672
Ra 148,242,29696
Mvaa 176,156,12
X 166
Mvaa 180,156,28
K 204,164,165
Mvaa 180,152,24
X 167
Mvaa 184,152,40
s_add_u32 s36, s29, 200704
Mvaa 184,156,44
X 168
Mvaa 188,156,60
Mvaa 188,152,56
W 2
Ra 152,242,30720
K 205,166,167
Mvav 32,128,192
X 169
Mvav 64,128,200
Ra 156,242,31744
Mvav 96,128,208
X 170
Mvav 128,128,216
buffer_store_dwordx4 a[224:227], v1, s[8:11], s36 offen offset:0 sc1
K 212,168,169
Mvav 132,132,216
X 171
Mvav 100,132,208
X 172
Mvav 68,132,200
Mvav 36,132,192
K 213,170,171
Mvav 40,136,192
X 173
Mvav 72,136,200
buffer_store_dwordx4 a[232:235], v1, s[8:11], s36 offen offset:1024 sc1
Mvav 104,136,208
X 174
Mvav 136,136,216
K 220,172,173
Mvav 140,140,216
X 175
Mvav 108,140,208
X 176
Mvav 76,140,200
s_waitcnt vmcnt(4) lgkmcnt(0)
s_barrier
v_xor_b32 v242, 0x10000, v242
Mvav 44,140,192
buffer_store_dwordx4 a[240:243], v1, s[8:11], s36 offen offset:2048 sc1
K 221,174,175
Mvav 48,144,192
Ra 128,242,0
Mvav 80,144,200
X 177
Mvav 112,144,208
X 178
Mvav 144,144,216
Ra 132,242,1024
K 198,176,177
Mvav 148,148,216
X 179
Mvav 116,148,208
X 180
Mvav 84,148,200
Ra 136,242,2048
buffer_store_dwordx4 a[100:103], v1, s[8:11], s36 offen offset:3072 sc1
Mvav 52,148,192
K 199,178,179
Mvav 56,152,192
X 181
Mvav 88,152,200
Ra 140,242,3072
Mvav 120,152,208
X 182
Mvav 152,152,216
K 206,180,181
Mvav 156,156,216
X 183
Mvav 124,156,208
Mvav 92,156,200
K 207,182,183
Mvav 60,156,192
W 0
Cvaa 160,128,0,224
Ra 144,242,4096
Ra 148,242,5120
Mvaa 160,132,4
s_add_u32 m0, s62, 0
Ra 152,242,6144
Cvaa 164,132,20,228
X 184
Mvaa 164,128,16
X 185
Cvaa 168,128,32,232
X 186
Mvaa 168,132,36
Ra 156,242,7168
K 214,184,185
Cvaa 172,132,52,236
Dc 1,8,61,0
Mvaa 172,128,48
X 187
Mvaa 160,136,8
X 188
Mvaa 160,140,12
Ra 128,242,16384
K 215,186,187
Mvaa 164,140,28
Dc 1,8,61,1024
Mvaa 164,136,24
X 189
Mvaa 168,136,40
X 190
Mvaa 168,140,44
Ra 132,242,17408
K 222,188,189
Mvaa 172,140,60
Dc 1,8,61,2048
Mvaa 172,136,56
X 191
v_smfmac_f32_16x16x64_bf16 a[160:163], a[176:179], v[192:199], v240
Ra 136,242,18432
K 223,190,191
v_smfmac_f32_16x16x64_bf16 a[164:167], a[176:179], v[200:207], v240
X 160
v_smfmac_f32_16x16x64_bf16 a[168:171], a[176:179], v[208:215], v240
Dc 1,8,61,3072
v_smfmac_f32_16x16x64_bf16 a[172:175], a[176:179], v[216:223], v240
W 3
X 161
Cvaa 176,144,0,224
s_add_u32 m0, s62, 4096
X 162
Mvaa 176,148,4
Ra 140,242,19456
K 192,160,161
Cvaa 180,148,20,228
X 163
Mvaa 180,144,16
Dc 243,8,61,0
Cvaa 184,144,32,232
X 164
Mvaa 184,148,36
K 193,162,163
Cvaa 188,148,52,236
X 165
Mvaa 188,144,48
Dc 243,8,61,1024
Ra 144,242,20480
Mvaa 176,152,8
X 166
Mvaa 176,156,12
Ra 148,242,21504
K 200,164,165
Mvaa 180,156,28
X 167
Mvaa 180,152,24
Dc 243,8,61,2048
Mvaa 184,152,40
X 168
Mvaa 184,156,44
K 201,166,167
Mvaa 188,156,60
X 169
Mvaa 188,152,56
W 2
Dc 243,8,61,3072
Ra 152,242,22528
Mvav 32,128,196
s_add_u32 m0, s62, 8192
X 170
Mvav 64,128,204
Ra 156,242,23552
K 208,168,169
Mvav 96,128,212
X 171
Mvav 128,128,220
Dc 244,8,61,0
Mvav 132,132,220
X 172
Mvav 100,132,212
Ra 128,242,8192
K 209,170,171
Mvav 68,132,204
X 173
Mvav 36,132,196
Dc 244,8,61,1024
Mvav 40,136,196
X 174
Mvav 72,136,204
Ra 132,242,9216
K 216,172,173
Mvav 104,136,212
X 175
Mvav 136,136,220
Dc 244,8,61,2048
Mvav 140,140,220
X 176
Mvav 108,140,212
Ra 136,242,10240
K 217,174,175
Mvav 76,140,204
X 177
Mvav 44,140,196
W 3
Dc 244,8,61,3072
Mvav 48,144,196
X 178
Mvav 80,144,204
s_add_u32 m0, s62, 12288
Ra 140,242,11264
Mvav 112,144,212
K 194,176,177
Mvav 144,144,220
X 179
Mvav 148,148,220
X 180
Mvav 116,148,212
Dc 245,8,61,0
Ra 144,242,12288
Mvav 84,148,204
K 195,178,179
Mvav 52,148,196
X 181
Mvav 56,152,196
X 182
Mvav 88,152,204
Dc 245,8,61,1024
Ra 148,242,13312
Mvav 120,152,212
K 202,180,181
Mvav 152,152,220
X 183
Mvav 156,156,220
X 184
Mvav 124,156,212
Dc 245,8,61,2048
Ra 152,242,14336
Mvav 92,156,204
K 203,182,183
Mvav 60,156,196
W 3
X 185
Cvaa 160,128,0,224
X 186
Mvaa 160,132,4
Dc 245,8,61,3072
Ra 156,242,15360
Cvaa 164,132,20,228
s_add_u32 s61, s61, s63
K 210,184,185
Mvaa 164,128,16
X 187
Cvaa 168,128,32,232
X 188
Mvaa 168,132,36
Cvaa 172,132,52,236
s_xor_b32 s62, s62, 0x10000
K 211,186,187
Mvaa 172,128,48
X 189
Mvaa 160,136,8
Ra 128,242,24576
Ra 132,242,25600
Mvaa 160,140,12
s_add_u32 s64, s64, 1
X 190
Mvaa 164,140,28
K 218,188,189
Mvaa 164,136,24
X 191
Mvaa 168,136,40
Mvaa 168,140,44
X 160
Mvaa 172,140,60
s_add_u32 s36, s29, 262144
K 219,190,191
Mvaa 172,136,56
W 2
X 161
Cvaa 176,144,0,224
Ra 136,242,26624
Ra 140,242,27648
Mvaa 176,148,4
X 162
Cvaa 180,148,20,228
K 196,160,161
Mvaa 180,144,16
X 163
Cvaa 184,144,32,232
buffer_store_dwordx4 a[64:67], v1, s[8:11], s36 offen offset:0 sc1
Mvaa 184,148,36
X 164
Cvaa 188,148,52,236
K 197,162,163
Mvaa 188,144,48
X 165
Mvaa 176,152,8
Ra 144,242,28672
Ra 148,242,29696
Mvaa 176,156,12
X 166
Mvaa 180,156,28
buffer_store_dwordx4 a[72:75], v1, s[8:11], s36 offen offset:1024 sc1
K 204,164,165
Mvaa 180,152,24
X 167
Mvaa 184,152,40
Mvaa 184,156,44
X 168
Mvaa 188,156,60
Mvaa 188,152,56
W 2
Ra 152,242,30720
K 205,166,167
Mvav 32,128,192
X 169
Mvav 64,128,200
buffer_store_dwordx4 a[80:83], v1, s[8:11], s36 offen offset:2048 sc1
Ra 156,242,31744
Mvav 96,128,208
X 170
Mvav 128,128,216
)"
R"(K 212,168,169
Mvav 132,132,216
X 171
Mvav 100,132,208
X 172
Mvav 68,132,200
buffer_store_dwordx4 a[88:91], v1, s[8:11], s36 offen offset:3072 sc1
Mvav 36,132,192
K 213,170,171
Mvav 40,136,192
X 173
Mvav 72,136,200
X 174
Mvav 104,136,208
Mvav 136,136,216
s_add_u32 s36, s29, 266240
K 220,172,173
Mvav 140,140,216
X 175
Mvav 108,140,208
X 176
Mvav 76,140,200
s_waitcnt vmcnt(4) lgkmcnt(0)
s_barrier
v_xor_b32 v242, 0x10000, v242
Mvav 44,140,192
Ra 128,242,0
K 221,174,175
Mvav 48,144,192
X 177
Mvav 80,144,200
buffer_store_dwordx4 a[96:99], v1, s[8:11], s36 offen offset:0 sc1
Mvav 112,144,208
X 178
Mvav 144,144,216
Ra 132,242,1024
K 198,176,177
Mvav 148,148,216
X 179
Mvav 116,148,208
X 180
Mvav 84,148,200
Ra 136,242,2048
Mvav 52,148,192
buffer_store_dwordx4 a[104:107], v1, s[8:11], s36 offen offset:1024 sc1
K 199,178,179
Mvav 56,152,192
X 181
Mvav 88,152,200
Ra 140,242,3072
Mvav 120,152,208
X 182
Mvav 152,152,216
K 206,180,181
Mvav 156,156,216
X 183
Mvav 124,156,208
Mvav 92,156,200
K 207,182,183
Mvav 60,156,192
W 0
Cvaa 160,128,0,224
Ra 144,242,4096
Ra 148,242,5120
Mvaa 160,132,4
s_add_u32 m0, s62, 0
Ra 152,242,6144
Cvaa 164,132,20,228
X 184
Mvaa 164,128,16
X 185
Cvaa 168,128,32,232
X 186
Mvaa 168,132,36
Ra 156,242,7168
K 214,184,185
Cvaa 172,132,52,236
Dc 1,8,61,0
Mvaa 172,128,48
X 187
Mvaa 160,136,8
X 188
Mvaa 160,140,12
Ra 128,242,16384
K 215,186,187
Mvaa 164,140,28
Dc 1,8,61,1024
Mvaa 164,136,24
X 189
Mvaa 168,136,40
X 190
Mvaa 168,140,44
Ra 132,242,17408
K 222,188,189
Mvaa 172,140,60
Dc 1,8,61,2048
Mvaa 172,136,56
X 191
v_smfmac_f32_16x16x64_bf16 a[160:163], a[176:179], v[192:199], v240
Ra 136,242,18432
K 223,190,191
v_smfmac_f32_16x16x64_bf16 a[164:167], a[176:179], v[200:207], v240
X 160
v_smfmac_f32_16x16x64_bf16 a[168:171], a[176:179], v[208:215], v240
Dc 1,8,61,3072
v_smfmac_f32_16x16x64_bf16 a[172:175], a[176:179], v[216:223], v240
W 3
X 161
Cvaa 176,144,0,224
s_add_u32 m0, s62, 4096
X 162
Mvaa 176,148,4
Ra 140,242,19456
K 192,160,161
Cvaa 180,148,20,228
X 163
Mvaa 180,144,16
Dc 243,8,61,0
Cvaa 184,144,32,232
X 164
Mvaa 184,148,36
K 193,162,163
Cvaa 188,148,52,236
X 165
Mvaa 188,144,48
Dc 243,8,61,1024
Ra 144,242,20480
Mvaa 176,152,8
X 166
Mvaa 176,156,12
Ra 148,242,21504
K 200,164,165
Mvaa 180,156,28
X 167
Mvaa 180,152,24
Dc 243,8,61,2048
Mvaa 184,152,40
X 168
Mvaa 184,156,44
K 201,166,167
Mvaa 188,156,60
X 169
Mvaa 188,152,56
W 2
Dc 243,8,61,3072
Ra 152,242,22528
Mvav 32,128,196
s_add_u32 m0, s62, 8192
X 170
Mvav 64,128,204
Ra 156,242,23552
K 208,168,169
Mvav 96,128,212
X 171
Mvav 128,128,220
Dc 244,8,61,0
Mvav 132,132,220
X 172
Mvav 100,132,212
Ra 128,242,8192
K 209,170,171
Mvav 68,132,204
X 173
Mvav 36,132,196
Dc 244,8,61,1024
Mvav 40,136,196
X 174
Mvav 72,136,204
Ra 132,242,9216
K 216,172,173
Mvav 104,136,212
X 175
Mvav 136,136,220
Dc 244,8,61,2048
Mvav 140,140,220
X 176
Mvav 108,140,212
Ra 136,242,10240
K 217,174,175
Mvav 76,140,204
X 177
Mvav 44,140,196
W 3
Dc 244,8,61,3072
Mvav 48,144,196
X 178
Mvav 80,144,204
s_add_u32 m0, s62, 12288
Ra 140,242,11264
Mvav 112,144,212
K 194,176,177
Mvav 144,144,220
X 179
Mvav 148,148,220
X 180
Mvav 116,148,212
Dc 245,8,61,0
Ra 144,242,12288
Mvav 84,148,204
K 195,178,179
Mvav 52,148,196
X 181
Mvav 56,152,196
X 182
Mvav 88,152,204
Dc 245,8,61,1024
Ra 148,242,13312
Mvav 120,152,212
K 202,180,181
Mvav 152,152,220
X 183
Mvav 156,156,220
X 184
Mvav 124,156,212
Dc 245,8,61,2048
Ra 152,242,14336
Mvav 92,156,204
K 203,182,183
v_bfe_u32 v28, v1, 4, 4
Mvav 60,156,196
W 3
X 185
Cvaa 160,128,0,224
Ra 156,242,15360
v_lshlrev_b32 v28, 3, v28
Mvaa 160,132,4
s_lshl_b32 s80, s56, 9
s_add_u32 s80, s80, 4352
Dc 245,8,61,3072
Cvaa 164,132,20,228
s_add_u32 s61, s61, s63
X 186
Mvaa 164,128,16
K 210,184,185
Cvaa 168,128,32,232
X 187
Mvaa 168,132,36
buffer_load_dwordx2 v[28:29], v28, s[8:11], s80 offen sc1
Cvaa 172,132,52,236
s_xor_b32 s62, s62, 0x10000
X 188
Mvaa 172,128,48
Ra 128,242,24576
K 211,186,187
Mvaa 160,136,8
X 189
Mvaa 160,140,12
s_add_u32 s64, s64, 1
X 190
Mvaa 164,140,28
Ra 132,242,25600
K 218,188,189
Mvaa 164,136,24
X 191
Mvaa 168,136,40
Mvaa 168,140,44
X 160
Mvaa 172,140,60
buffer_store_dwordx4 a[112:115], v1, s[8:11], s36 offen offset:2048 sc1
K 219,190,191
Mvaa 172,136,56
W 2
X 161
Cvaa 176,144,0,224
Ra 136,242,26624
Ra 140,242,27648
Mvaa 176,148,4
X 162
Cvaa 180,148,20,228
K 196,160,161
Mvaa 180,144,16
X 163
Cvaa 184,144,32,232
Mvaa 184,148,36
X 164
Cvaa 188,148,52,236
K 197,162,163
Mvaa 188,144,48
X 165
Mvaa 176,152,8
Ra 144,242,28672
Ra 148,242,29696
Mvaa 176,156,12
X 166
Mvaa 180,156,28
K 204,164,165
Mvaa 180,152,24
X 167
Mvaa 184,152,40
Mvaa 184,156,44
X 168
Mvaa 188,156,60
Mvaa 188,152,56
W 2
Ra 152,242,30720
K 205,166,167
Mvav 32,128,192
X 169
Mvav 64,128,200
Ra 156,242,31744
Mvav 96,128,208
X 170
Mvav 128,128,216
K 212,168,169
Mvav 132,132,216
X 171
Mvav 100,132,208
X 172
Mvav 68,132,200
buffer_store_dwordx4 a[120:123], v1, s[8:11], s36 offen offset:3072 sc1
Mvav 36,132,192
K 213,170,171
Mvav 40,136,192
X 173
Mvav 72,136,200
X 174
Mvav 104,136,208
Mvav 136,136,216
s_waitcnt vmcnt(2)
v_cmp_eq_u64 vcc, s[12:13], v[28:29]
s_cmp_eq_u64 vcc, -1
s_cbranch_scc0 .Lspin_68%=
.Lok_69%=:
K 220,172,173
Mvav 140,140,216
X 175
Mvav 108,140,208
X 176
Mvav 76,140,200
W 0
s_barrier
v_xor_b32 v242, 0x10000, v242
Mvav 44,140,192
Ra 128,242,0
K 221,174,175
Mvav 48,144,192
X 177
Mvav 80,144,200
X 178
Mvav 112,144,208
Ra 132,242,1024
Mvav 144,144,216
K 198,176,177
Mvav 148,148,216
X 179
Mvav 116,148,208
X 180
Mvav 84,148,200
Ra 136,242,2048
Mvav 52,148,192
K 199,178,179
Mvav 56,152,192
X 181
Mvav 88,152,200
Ra 140,242,3072
Mvav 120,152,208
X 182
Mvav 152,152,216
K 206,180,181
Mvav 156,156,216
X 183
Mvav 124,156,208
Mvav 92,156,200
K 207,182,183
Mvav 60,156,192
W 0
Cvaa 160,128,0,224
Ra 144,242,4096
Ra 148,242,5120
Mvaa 160,132,4
s_add_u32 m0, s62, 0
Ra 152,242,6144
Cvaa 164,132,20,228
X 184
Mvaa 164,128,16
X 185
Cvaa 168,128,32,232
X 186
Mvaa 168,132,36
Ra 156,242,7168
K 214,184,185
Cvaa 172,132,52,236
Dc 1,8,61,0
Mvaa 172,128,48
X 187
Mvaa 160,136,8
X 188
Mvaa 160,140,12
Ra 128,242,16384
K 215,186,187
Mvaa 164,140,28
Dc 1,8,61,1024
Mvaa 164,136,24
X 189
Mvaa 168,136,40
X 190
Mvaa 168,140,44
Ra 132,242,17408
K 222,188,189
Mvaa 172,140,60
)"
R"(Dc 1,8,61,2048
Mvaa 172,136,56
X 191
v_smfmac_f32_16x16x64_bf16 a[160:163], a[176:179], v[192:199], v240
Ra 136,242,18432
K 223,190,191
v_smfmac_f32_16x16x64_bf16 a[164:167], a[176:179], v[200:207], v240
X 160
v_smfmac_f32_16x16x64_bf16 a[168:171], a[176:179], v[208:215], v240
Dc 1,8,61,3072
v_smfmac_f32_16x16x64_bf16 a[172:175], a[176:179], v[216:223], v240
W 3
X 161
Cvaa 176,144,0,224
s_add_u32 m0, s62, 4096
X 162
Mvaa 176,148,4
Ra 140,242,19456
K 192,160,161
Cvaa 180,148,20,228
X 163
Mvaa 180,144,16
Dc 243,8,61,0
Cvaa 184,144,32,232
X 164
Mvaa 184,148,36
K 193,162,163
Cvaa 188,148,52,236
X 165
Mvaa 188,144,48
Dc 243,8,61,1024
Ra 144,242,20480
Mvaa 176,152,8
X 166
Mvaa 176,156,12
Ra 148,242,21504
K 200,164,165
Mvaa 180,156,28
X 167
Mvaa 180,152,24
Dc 243,8,61,2048
Mvaa 184,152,40
X 168
Mvaa 184,156,44
K 201,166,167
Mvaa 188,156,60
X 169
Mvaa 188,152,56
W 2
Dc 243,8,61,3072
Ra 152,242,22528
Mvav 32,128,196
s_add_u32 m0, s62, 8192
X 170
Mvav 64,128,204
Ra 156,242,23552
K 208,168,169
Mvav 96,128,212
X 171
Mvav 128,128,220
Dc 244,8,61,0
Mvav 132,132,220
X 172
Mvav 100,132,212
Ra 128,242,8192
K 209,170,171
Mvav 68,132,204
X 173
Mvav 36,132,196
Dc 244,8,61,1024
Mvav 40,136,196
X 174
Mvav 72,136,204
Ra 132,242,9216
K 216,172,173
Mvav 104,136,212
X 175
Mvav 136,136,220
Dc 244,8,61,2048
Mvav 140,140,220
X 176
Mvav 108,140,212
Ra 136,242,10240
K 217,174,175
Mvav 76,140,204
X 177
Mvav 44,140,196
W 3
Dc 244,8,61,3072
Mvav 48,144,196
X 178
Mvav 80,144,204
s_add_u32 m0, s62, 12288
Ra 140,242,11264
Mvav 112,144,212
K 194,176,177
Mvav 144,144,220
X 179
Mvav 148,148,220
X 180
Mvav 116,148,212
Dc 245,8,61,0
Ra 144,242,12288
Mvav 84,148,204
K 195,178,179
Mvav 52,148,196
X 181
Mvav 56,152,196
X 182
Mvav 88,152,204
Dc 245,8,61,1024
Ra 148,242,13312
Mvav 120,152,212
K 202,180,181
Mvav 152,152,220
s_add_u32 s81, s57, 2
X 183
Mvav 156,156,220
s_waitcnt vmcnt(14)
s_add_u32 s80, s71, 384
v_mov_b64 v[28:29], s[12:13]
s_mov_b64 exec, 1
buffer_store_dwordx2 v[28:29], off, s[8:11], s80 sc1
s_mov_b64 exec, -1
X 184
Mvav 124,156,212
s_bitcmp0_b32 s81, 2
s_cselect_b32 s22, -1, 0
s_and_b32 s81, s81, 3
Ra 152,242,14336
Mvav 92,156,204
s_lshl_b32 s82, s56, 2
K 203,182,183
Mvav 60,156,196
W 3
X 185
Cvaa 160,128,0,224
s_add_u32 s81, s81, s82
Dc 245,8,61,2048
Ra 156,242,15360
Mvaa 160,132,4
s_lshl_b32 s81, s81, 1
X 186
Cvaa 164,132,20,228
s_add_u32 s81, s81, s14
s_sub_u32 s81, s81, 2
K 210,184,185
Mvaa 164,128,16
s_mul_i32 s81, s81, 20480
X 187
Cvaa 168,128,32,232
Dc 245,8,61,3072
v_min_u32 v2, 624, v1
Mvaa 168,132,36
s_add_u32 s61, s61, s63
X 188
Cvaa 172,132,52,236
s_xor_b32 s62, s62, 0x10000
v_lshl_add_u32 v2, v2, 3, s81
K 211,186,187
Mvaa 172,128,48
Ra 128,242,24576
v_and_b32 v2, s22, v2
Mvaa 160,136,8
s_and_b32 s22, s22, 5120
X 189
Mvaa 160,140,12
s_add_u32 s64, s64, 1
global_load_dword v3, v2, s[26:27]
Ra 132,242,25600
Mvaa 164,140,28
X 190
Mvaa 164,136,24
v_add_u32 v2, s22, v2
K 218,188,189
Mvaa 168,136,40
X 191
Mvaa 168,140,44
X 160
Mvaa 172,140,60
K 219,190,191
Mvaa 172,136,56
W 2
X 161
Cvaa 176,144,0,224
Ra 136,242,26624
Ra 140,242,27648
Mvaa 176,148,4
X 162
Cvaa 180,148,20,228
K 196,160,161
Mvaa 180,144,16
X 163
Cvaa 184,144,32,232
Mvaa 184,148,36
X 164
Cvaa 188,148,52,236
K 197,162,163
Mvaa 188,144,48
X 165
Mvaa 176,152,8
Ra 144,242,28672
Ra 148,242,29696
Mvaa 176,156,12
X 166
Mvaa 180,156,28
K 204,164,165
Mvaa 180,152,24
X 167
Mvaa 184,152,40
Mvaa 184,156,44
X 168
Mvaa 188,156,60
Mvaa 188,152,56
W 2
Ra 152,242,30720
K 205,166,167
Mvav 32,128,192
X 169
Mvav 64,128,200
Ra 156,242,31744
Mvav 96,128,208
X 170
Mvav 128,128,216
K 212,168,169
Mvav 132,132,216
X 171
Mvav 100,132,208
X 172
Mvav 68,132,200
Mvav 36,132,192
K 213,170,171
Mvav 40,136,192
X 173
Mvav 72,136,200
X 174
Mvav 104,136,208
Mvav 136,136,216
K 220,172,173
Mvav 140,140,216
X 175
Mvav 108,140,208
X 176
Mvav 76,140,200
s_waitcnt vmcnt(1) lgkmcnt(0)
s_barrier
v_xor_b32 v242, 0x10000, v242
Mvav 44,140,192
Ra 128,242,0
K 221,174,175
Mvav 48,144,192
X 177
Mvav 80,144,200
X 178
Mvav 112,144,208
Ra 132,242,1024
Mvav 144,144,216
K 198,176,177
Mvav 148,148,216
X 179
Mvav 116,148,208
X 180
Mvav 84,148,200
Ra 136,242,2048
Mvav 52,148,192
K 199,178,179
Mvav 56,152,192
X 181
Mvav 88,152,200
Ra 140,242,3072
Mvav 120,152,208
X 182
Mvav 152,152,216
K 206,180,181
Mvav 156,156,216
X 183
Mvav 124,156,208
Mvav 92,156,200
K 207,182,183
Mvav 60,156,192
W 0
Cvaa 160,128,0,224
Ra 144,242,4096
Ra 148,242,5120
Mvaa 160,132,4
s_add_u32 m0, s62, 0
Ra 152,242,6144
Cvaa 164,132,20,228
X 184
Mvaa 164,128,16
X 185
Cvaa 168,128,32,232
X 186
Mvaa 168,132,36
Ra 156,242,7168
K 214,184,185
Cvaa 172,132,52,236
Dc 1,8,61,0
Mvaa 172,128,48
X 187
Mvaa 160,136,8
X 188
Mvaa 160,140,12
Ra 128,242,16384
K 215,186,187
Mvaa 164,140,28
Dc 1,8,61,1024
Mvaa 164,136,24
X 189
Mvaa 168,136,40
X 190
Mvaa 168,140,44
Ra 132,242,17408
K 222,188,189
Mvaa 172,140,60
Dc 1,8,61,2048
Mvaa 172,136,56
X 191
v_smfmac_f32_16x16x64_bf16 a[160:163], a[176:179], v[192:199], v240
Ra 136,242,18432
K 223,190,191
v_smfmac_f32_16x16x64_bf16 a[164:167], a[176:179], v[200:207], v240
X 160
v_smfmac_f32_16x16x64_bf16 a[168:171], a[176:179], v[208:215], v240
Dc 1,8,61,3072
v_smfmac_f32_16x16x64_bf16 a[172:175], a[176:179], v[216:223], v240
W 3
X 161
Cvaa 176,144,0,224
s_add_u32 m0, s62, 4096
X 162
Mvaa 176,148,4
Ra 140,242,19456
K 192,160,161
Cvaa 180,148,20,228
X 163
Mvaa 180,144,16
Dc 243,8,61,0
Cvaa 184,144,32,232
X 164
Mvaa 184,148,36
K 193,162,163
Cvaa 188,148,52,236
X 165
Mvaa 188,144,48
Dc 243,8,61,1024
Ra 144,242,20480
Mvaa 176,152,8
X 166
Mvaa 176,156,12
Ra 148,242,21504
K 200,164,165
Mvaa 180,156,28
X 167
Mvaa 180,152,24
Dc 243,8,61,2048
Mvaa 184,152,40
X 168
Mvaa 184,156,44
K 201,166,167
Mvaa 188,156,60
X 169
Mvaa 188,152,56
W 2
Dc 243,8,61,3072
Ra 152,242,22528
Mvav 32,128,196
s_add_u32 m0, s62, 8192
X 170
Mvav 64,128,204
Ra 156,242,23552
K 208,168,169
Mvav 96,128,212
X 171
Mvav 128,128,220
Dc 244,8,61,0
Mvav 132,132,220
X 172
Mvav 100,132,212
Ra 128,242,8192
K 209,170,171
Mvav 68,132,204
X 173
Mvav 36,132,196
Dc 244,8,61,1024
Mvav 40,136,196
X 174
Mvav 72,136,204
Ra 132,242,9216
K 216,172,173
Mvav 104,136,212
X 175
Mvav 136,136,220
Dc 244,8,61,2048
Mvav 140,140,220
X 176
Mvav 108,140,212
Ra 136,242,10240
K 217,174,175
Mvav 76,140,204
)"
R"(X 177
Mvav 44,140,196
W 3
Dc 244,8,61,3072
Mvav 48,144,196
X 178
Mvav 80,144,204
s_add_u32 m0, s62, 12288
Ra 140,242,11264
Mvav 112,144,212
K 194,176,177
Mvav 144,144,220
X 179
Mvav 148,148,220
X 180
Mvav 116,148,212
Dc 245,8,61,0
Ra 144,242,12288
Mvav 84,148,204
K 195,178,179
Mvav 52,148,196
X 181
Mvav 56,152,196
X 182
Mvav 88,152,204
Dc 245,8,61,1024
Ra 148,242,13312
Mvav 120,152,212
K 202,180,181
Mvav 152,152,220
X 183
Mvav 156,156,220
X 184
Mvav 124,156,212
Dc 245,8,61,2048
Ra 152,242,14336
Mvav 92,156,204
K 203,182,183
Mvav 60,156,196
W 3
X 185
Cvaa 160,128,0,224
X 186
Mvaa 160,132,4
Dc 245,8,61,3072
Ra 156,242,15360
Cvaa 164,132,20,228
s_add_u32 s61, s61, s63
K 210,184,185
Mvaa 164,128,16
X 187
Cvaa 168,128,32,232
X 188
Mvaa 168,132,36
Cvaa 172,132,52,236
s_xor_b32 s62, s62, 0x10000
global_load_dword v3, v2, s[26:27]
K 211,186,187
Mvaa 172,128,48
X 189
Mvaa 160,136,8
Ra 128,242,24576
Ra 132,242,25600
Mvaa 160,140,12
s_add_u32 s64, s64, 1
X 190
Mvaa 164,140,28
v_add_u32 v2, s22, v2
K 218,188,189
Mvaa 164,136,24
X 191
Mvaa 168,136,40
Mvaa 168,140,44
X 160
Mvaa 172,140,60
K 219,190,191
Mvaa 172,136,56
W 2
X 161
Cvaa 176,144,0,224
Ra 136,242,26624
Ra 140,242,27648
Mvaa 176,148,4
X 162
Cvaa 180,148,20,228
K 196,160,161
Mvaa 180,144,16
X 163
Cvaa 184,144,32,232
Mvaa 184,148,36
X 164
Cvaa 188,148,52,236
K 197,162,163
Mvaa 188,144,48
X 165
Mvaa 176,152,8
Ra 144,242,28672
Ra 148,242,29696
Mvaa 176,156,12
X 166
Mvaa 180,156,28
K 204,164,165
Mvaa 180,152,24
X 167
Mvaa 184,152,40
Mvaa 184,156,44
X 168
Mvaa 188,156,60
Mvaa 188,152,56
W 2
Ra 152,242,30720
K 205,166,167
Mvav 32,128,192
X 169
Mvav 64,128,200
Ra 156,242,31744
Mvav 96,128,208
X 170
Mvav 128,128,216
K 212,168,169
Mvav 132,132,216
X 171
Mvav 100,132,208
X 172
Mvav 68,132,200
Mvav 36,132,192
K 213,170,171
Mvav 40,136,192
X 173
Mvav 72,136,200
X 174
Mvav 104,136,208
Mvav 136,136,216
K 220,172,173
Mvav 140,140,216
X 175
Mvav 108,140,208
X 176
Mvav 76,140,200
s_waitcnt vmcnt(1) lgkmcnt(0)
s_barrier
v_xor_b32 v242, 0x10000, v242
Mvav 44,140,192
Ra 128,242,0
K 221,174,175
Mvav 48,144,192
X 177
Mvav 80,144,200
X 178
Mvav 112,144,208
Ra 132,242,1024
Mvav 144,144,216
K 198,176,177
Mvav 148,148,216
X 179
Mvav 116,148,208
X 180
Mvav 84,148,200
Ra 136,242,2048
Mvav 52,148,192
K 199,178,179
Mvav 56,152,192
X 181
Mvav 88,152,200
Ra 140,242,3072
Mvav 120,152,208
X 182
Mvav 152,152,216
K 206,180,181
Mvav 156,156,216
X 183
Mvav 124,156,208
Mvav 92,156,200
K 207,182,183
Mvav 60,156,192
W 0
Cvaa 160,128,0,224
Ra 144,242,4096
Ra 148,242,5120
Mvaa 160,132,4
s_add_u32 m0, s62, 0
Ra 152,242,6144
Cvaa 164,132,20,228
X 184
Mvaa 164,128,16
X 185
Cvaa 168,128,32,232
X 186
Mvaa 168,132,36
Ra 156,242,7168
K 214,184,185
Cvaa 172,132,52,236
Dc 1,8,61,0
Mvaa 172,128,48
X 187
Mvaa 160,136,8
X 188
Mvaa 160,140,12
Ra 128,242,16384
K 215,186,187
Mvaa 164,140,28
Dc 1,8,61,1024
Mvaa 164,136,24
X 189
Mvaa 168,136,40
X 190
Mvaa 168,140,44
Ra 132,242,17408
K 222,188,189
Mvaa 172,140,60
Dc 1,8,61,2048
Mvaa 172,136,56
X 191
v_smfmac_f32_16x16x64_bf16 a[160:163], a[176:179], v[192:199], v240
Ra 136,242,18432
K 223,190,191
v_smfmac_f32_16x16x64_bf16 a[164:167], a[176:179], v[200:207], v240
X 160
v_smfmac_f32_16x16x64_bf16 a[168:171], a[176:179], v[208:215], v240
Dc 1,8,61,3072
v_smfmac_f32_16x16x64_bf16 a[172:175], a[176:179], v[216:223], v240
W 3
X 161
Cvaa 176,144,0,224
s_add_u32 m0, s62, 4096
X 162
Mvaa 176,148,4
Ra 140,242,19456
K 192,160,161
Cvaa 180,148,20,228
X 163
Mvaa 180,144,16
Dc 243,8,61,0
Cvaa 184,144,32,232
X 164
Mvaa 184,148,36
K 193,162,163
Cvaa 188,148,52,236
X 165
Mvaa 188,144,48
Dc 243,8,61,1024
Ra 144,242,20480
Mvaa 176,152,8
X 166
Mvaa 176,156,12
Ra 148,242,21504
K 200,164,165
Mvaa 180,156,28
X 167
Mvaa 180,152,24
Dc 243,8,61,2048
Mvaa 184,152,40
X 168
Mvaa 184,156,44
K 201,166,167
Mvaa 188,156,60
X 169
Mvaa 188,152,56
W 2
Dc 243,8,61,3072
Ra 152,242,22528
Mvav 32,128,196
s_add_u32 m0, s62, 8192
X 170
Mvav 64,128,204
Ra 156,242,23552
K 208,168,169
Mvav 96,128,212
X 171
Mvav 128,128,220
Dc 244,8,61,0
Mvav 132,132,220
X 172
Mvav 100,132,212
Ra 128,242,8192
K 209,170,171
Mvav 68,132,204
X 173
Mvav 36,132,196
Dc 244,8,61,1024
Mvav 40,136,196
X 174
Mvav 72,136,204
Ra 132,242,9216
K 216,172,173
Mvav 104,136,212
X 175
Mvav 136,136,220
Dc 244,8,61,2048
Mvav 140,140,220
X 176
Mvav 108,140,212
Ra 136,242,10240
K 217,174,175
Mvav 76,140,204
X 177
Mvav 44,140,196
W 3
Dc 244,8,61,3072
Mvav 48,144,196
X 178
Mvav 80,144,204
s_add_u32 m0, s62, 12288
Ra 140,242,11264
Mvav 112,144,212
K 194,176,177
Mvav 144,144,220
X 179
Mvav 148,148,220
X 180
Mvav 116,148,212
Dc 245,8,61,0
Ra 144,242,12288
Mvav 84,148,204
K 195,178,179
Mvav 52,148,196
X 181
Mvav 56,152,196
X 182
Mvav 88,152,204
Dc 245,8,61,1024
Ra 148,242,13312
Mvav 120,152,212
K 202,180,181
Mvav 152,152,220
X 183
Mvav 156,156,220
X 184
Mvav 124,156,212
Dc 245,8,61,2048
Ra 152,242,14336
Mvav 92,156,204
K 203,182,183
Mvav 60,156,196
W 3
X 185
Cvaa 160,128,0,224
X 186
Mvaa 160,132,4
Dc 245,8,61,3072
Ra 156,242,15360
Cvaa 164,132,20,228
s_add_u32 s61, s61, s63
K 210,184,185
Mvaa 164,128,16
X 187
Cvaa 168,128,32,232
X 188
Mvaa 168,132,36
Cvaa 172,132,52,236
s_xor_b32 s62, s62, 0x10000
global_load_dword v3, v2, s[26:27]
K 211,186,187
Mvaa 172,128,48
Ra 128,242,24576
v_add_u32 v2, s22, v2
Mvaa 160,136,8
s_lshr_b32 s80, s15, 3
s_lshl_b32 s80, s80, 1
s_add_u32 s80, s80, s14
Mvaa 160,140,12
s_sub_u32 s80, s80, 2
s_lshl_b32 s80, s80, 12
v_min_u32 v247, 496, v1
Mvaa 164,140,28
s_add_u32 s64, s64, 1
X 189
Mvaa 164,136,24
Ra 132,242,25600
v_lshl_add_u32 v247, v247, 3, s80
Mvaa 168,136,40
X 190
Mvaa 168,140,44
v_min_u32 v247, .Lend%=-.Lbeg%=-4, v247
K 218,188,189
Mvaa 172,140,60
global_load_dword v3, v247, s[6:7]
Mvaa 172,136,56
W 2
X 191
Cvaa 176,144,0,224
X 160
Mvaa 176,148,4
Ra 136,242,26624
K 219,190,191
Cvaa 180,148,20,228
X 161
Mvaa 180,144,16
Ra 140,242,27648
Cvaa 184,144,32,232
X 162
Mvaa 184,148,36
K 196,160,161
Cvaa 188,148,52,236
X 163
Mvaa 188,144,48
X 164
Mvaa 176,152,8
Ra 144,242,28672
K 197,162,163
)"
R"(Mvaa 176,156,12
X 165
Mvaa 180,156,28
Ra 148,242,29696
Mvaa 180,152,24
X 166
Mvaa 184,152,40
K 204,164,165
Mvaa 184,156,44
X 167
Mvaa 188,156,60
X 168
Mvaa 188,152,56
W 2
Ra 152,242,30720
K 205,166,167
Mvav 32,128,192
X 169
Mvav 64,128,200
Ra 156,242,31744
Mvav 96,128,208
X 170
Mvav 128,128,216
K 212,168,169
Mvav 132,132,216
X 171
Mvav 100,132,208
X 172
Mvav 68,132,200
Mvav 36,132,192
K 213,170,171
Mvav 40,136,192
X 173
Mvav 72,136,200
X 174
Mvav 104,136,208
Mvav 136,136,216
K 220,172,173
Mvav 140,140,216
X 175
Mvav 108,140,208
X 176
Mvav 76,140,200
s_waitcnt vmcnt(2) lgkmcnt(0)
s_barrier
v_xor_b32 v242, 0x10000, v242
Mvav 44,140,192
Ra 128,242,0
K 221,174,175
Mvav 48,144,192
X 177
Mvav 80,144,200
X 178
Mvav 112,144,208
Ra 132,242,1024
Mvav 144,144,216
K 198,176,177
Mvav 148,148,216
X 179
Mvav 116,148,208
X 180
Mvav 84,148,200
Ra 136,242,2048
Mvav 52,148,192
K 199,178,179
Mvav 56,152,192
X 181
Mvav 88,152,200
Ra 140,242,3072
Mvav 120,152,208
X 182
Mvav 152,152,216
K 206,180,181
Mvav 156,156,216
X 183
Mvav 124,156,208
Mvav 92,156,200
K 207,182,183
Mvav 60,156,192
W 0
Cvaa 160,128,0,224
Ra 144,242,4096
Ra 148,242,5120
Mvaa 160,132,4
s_add_u32 m0, s62, 0
Ra 152,242,6144
Cvaa 164,132,20,228
X 184
Mvaa 164,128,16
X 185
Cvaa 168,128,32,232
X 186
Mvaa 168,132,36
Ra 156,242,7168
K 214,184,185
Cvaa 172,132,52,236
Dc 1,8,61,0
Mvaa 172,128,48
X 187
Mvaa 160,136,8
X 188
Mvaa 160,140,12
Ra 128,242,16384
K 215,186,187
Mvaa 164,140,28
Dc 1,8,61,1024
Mvaa 164,136,24
X 189
Mvaa 168,136,40
X 190
Mvaa 168,140,44
Ra 132,242,17408
K 222,188,189
Mvaa 172,140,60
Dc 1,8,61,2048
Mvaa 172,136,56
X 191
v_smfmac_f32_16x16x64_bf16 a[160:163], a[176:179], v[192:199], v240
Ra 136,242,18432
K 223,190,191
v_smfmac_f32_16x16x64_bf16 a[164:167], a[176:179], v[200:207], v240
X 160
v_smfmac_f32_16x16x64_bf16 a[168:171], a[176:179], v[208:215], v240
Dc 1,8,61,3072
v_smfmac_f32_16x16x64_bf16 a[172:175], a[176:179], v[216:223], v240
W 3
X 161
Cvaa 176,144,0,224
s_add_u32 m0, s62, 4096
X 162
Mvaa 176,148,4
Ra 140,242,19456
K 192,160,161
Cvaa 180,148,20,228
X 163
Mvaa 180,144,16
Dc 243,8,61,0
Cvaa 184,144,32,232
X 164
Mvaa 184,148,36
K 193,162,163
Cvaa 188,148,52,236
X 165
Mvaa 188,144,48
Dc 243,8,61,1024
Ra 144,242,20480
Mvaa 176,152,8
X 166
Mvaa 176,156,12
Ra 148,242,21504
K 200,164,165
Mvaa 180,156,28
X 167
Mvaa 180,152,24
Dc 243,8,61,2048
Mvaa 184,152,40
X 168
Mvaa 184,156,44
K 201,166,167
Mvaa 188,156,60
X 169
Mvaa 188,152,56
W 2
Dc 243,8,61,3072
Ra 152,242,22528
Mvav 32,128,196
s_add_u32 m0, s62, 8192
X 170
Mvav 64,128,204
Ra 156,242,23552
K 208,168,169
Mvav 96,128,212
X 171
Mvav 128,128,220
Dc 244,8,61,0
Mvav 132,132,220
X 172
Mvav 100,132,212
Ra 128,242,8192
K 209,170,171
Mvav 68,132,204
X 173
Mvav 36,132,196
Dc 244,8,61,1024
Mvav 40,136,196
X 174
Mvav 72,136,204
Ra 132,242,9216
K 216,172,173
Mvav 104,136,212
X 175
Mvav 136,136,220
Dc 244,8,61,2048
Mvav 140,140,220
X 176
Mvav 108,140,212
Ra 136,242,10240
K 217,174,175
Mvav 76,140,204
X 177
Mvav 44,140,196
W 3
Dc 244,8,61,3072
Mvav 48,144,196
X 178
Mvav 80,144,204
s_add_u32 m0, s62, 12288
Ra 140,242,11264
Mvav 112,144,212
K 194,176,177
Mvav 144,144,220
X 179
Mvav 148,148,220
X 180
Mvav 116,148,212
Dc 245,8,61,0
Ra 144,242,12288
Mvav 84,148,204
K 195,178,179
Mvav 52,148,196
X 181
Mvav 56,152,196
X 182
Mvav 88,152,204
Dc 245,8,61,1024
Ra 148,242,13312
Mvav 120,152,212
K 202,180,181
Mvav 152,152,220
X 183
Mvav 156,156,220
X 184
Mvav 124,156,212
Dc 245,8,61,2048
Ra 152,242,14336
Mvav 92,156,204
K 203,182,183
v_bfe_u32 v28, v1, 4, 4
Mvav 60,156,196
W 3
X 185
Cvaa 160,128,0,224
Ra 156,242,15360
v_lshlrev_b32 v28, 3, v28
Mvaa 160,132,4
s_lshl_b32 s80, s56, 9
s_add_u32 s80, s80, 4480
Dc 245,8,61,3072
Cvaa 164,132,20,228
s_add_u32 s61, s61, s63
X 186
Mvaa 164,128,16
K 210,184,185
Cvaa 168,128,32,232
X 187
Mvaa 168,132,36
buffer_load_dwordx2 v[28:29], v28, s[8:11], s80 offen sc1
Cvaa 172,132,52,236
s_xor_b32 s62, s62, 0x10000
X 188
Mvaa 172,128,48
Ra 128,242,24576
K 211,186,187
Mvaa 160,136,8
X 189
Mvaa 160,140,12
s_add_u32 s64, s64, 1
global_load_dword v3, v2, s[26:27]
Ra 132,242,25600
Mvaa 164,140,28
X 190
Mvaa 164,136,24
v_add_u32 v2, s22, v2
K 218,188,189
Mvaa 168,136,40
X 191
Mvaa 168,140,44
X 160
Mvaa 172,140,60
K 219,190,191
Mvaa 172,136,56
W 2
X 161
Cvaa 176,144,0,224
Ra 136,242,26624
Ra 140,242,27648
Mvaa 176,148,4
X 162
Cvaa 180,148,20,228
K 196,160,161
Mvaa 180,144,16
X 163
Cvaa 184,144,32,232
Mvaa 184,148,36
X 164
Cvaa 188,148,52,236
K 197,162,163
Mvaa 188,144,48
X 165
Mvaa 176,152,8
Ra 144,242,28672
Ra 148,242,29696
Mvaa 176,156,12
X 166
Mvaa 180,156,28
K 204,164,165
Mvaa 180,152,24
X 167
Mvaa 184,152,40
Mvaa 184,156,44
X 168
Mvaa 188,156,60
Mvaa 188,152,56
W 2
Ra 152,242,30720
K 205,166,167
Mvav 32,128,192
X 169
Mvav 64,128,200
Ra 156,242,31744
Mvav 96,128,208
X 170
Mvav 128,128,216
K 212,168,169
Mvav 132,132,216
X 171
Mvav 100,132,208
X 172
Mvav 68,132,200
Mvav 36,132,192
K 213,170,171
Mvav 40,136,192
X 173
Mvav 72,136,200
X 174
Mvav 104,136,208
Mvav 136,136,216
s_waitcnt vmcnt(1)
v_cmp_eq_u64 vcc, s[12:13], v[28:29]
s_cmp_eq_u64 vcc, -1
s_cbranch_scc0 .Lspin_71%=
.Lok_72%=:
K 220,172,173
Mvav 140,140,216
X 175
Mvav 108,140,208
X 176
Mvav 76,140,200
W 0
s_barrier
v_xor_b32 v242, 0x10000, v242
Mvav 44,140,192
Ra 128,242,0
K 221,174,175
Mvav 48,144,192
X 177
Mvav 80,144,200
X 178
Mvav 112,144,208
Ra 132,242,1024
Mvav 144,144,216
K 198,176,177
Mvav 148,148,216
X 179
Mvav 116,148,208
X 180
Mvav 84,148,200
Ra 136,242,2048
Mvav 52,148,192
K 199,178,179
Mvav 56,152,192
X 181
Mvav 88,152,200
Ra 140,242,3072
Mvav 120,152,208
X 182
Mvav 152,152,216
K 206,180,181
Mvav 156,156,216
X 183
Mvav 124,156,208
Mvav 92,156,200
K 207,182,183
Mvav 60,156,192
W 0
Cvaa 160,128,0,224
Ra 144,242,4096
Ra 148,242,5120
Mvaa 160,132,4
s_add_u32 m0, s62, 0
Ra 152,242,6144
Cvaa 164,132,20,228
X 184
Mvaa 164,128,16
X 185
Cvaa 168,128,32,232
X 186
Mvaa 168,132,36
Ra 156,242,7168
K 214,184,185
)"
R"(Cvaa 172,132,52,236
Dc 1,8,61,0
Mvaa 172,128,48
X 187
Mvaa 160,136,8
X 188
Mvaa 160,140,12
Ra 128,242,16384
K 215,186,187
Mvaa 164,140,28
Dc 1,8,61,1024
Mvaa 164,136,24
X 189
Mvaa 168,136,40
X 190
Mvaa 168,140,44
Ra 132,242,17408
K 222,188,189
Mvaa 172,140,60
Dc 1,8,61,2048
Mvaa 172,136,56
X 191
v_smfmac_f32_16x16x64_bf16 a[160:163], a[176:179], v[192:199], v240
Ra 136,242,18432
K 223,190,191
v_smfmac_f32_16x16x64_bf16 a[164:167], a[176:179], v[200:207], v240
X 160
v_smfmac_f32_16x16x64_bf16 a[168:171], a[176:179], v[208:215], v240
Dc 1,8,61,3072
v_smfmac_f32_16x16x64_bf16 a[172:175], a[176:179], v[216:223], v240
W 3
X 161
Cvaa 176,144,0,224
s_add_u32 m0, s62, 4096
X 162
Mvaa 176,148,4
s_sub_u32 s81, s57, 1
Ra 140,242,19456
K 192,160,161
Cvaa 180,148,20,228
X 163
Mvaa 180,144,16
s_lshl_b32 s85, s81, 5
Dc 243,8,61,0
Cvaa 184,144,32,232
X 164
Mvaa 184,148,36
s_add_u32 s85, s85, s56
K 193,162,163
Cvaa 188,148,52,236
X 165
Mvaa 188,144,48
Dc 243,8,61,1024
Ra 144,242,20480
Mvaa 176,152,8
s_lshl_b32 s85, s85, 1
X 166
Mvaa 176,156,12
Ra 148,242,21504
K 200,164,165
Mvaa 180,156,28
s_add_u32 s85, s85, s14
X 167
Mvaa 180,152,24
Dc 243,8,61,2048
Mvaa 184,152,40
s_sub_u32 s85, s85, 2
X 168
Mvaa 184,156,44
K 201,166,167
Mvaa 188,156,60
X 169
Mvaa 188,152,56
W 2
s_cmp_lt_u32 s85, 160
s_cselect_b64 s[20:21], 0, -1
Dc 243,8,61,3072
Mvav 32,128,196
s_add_u32 m0, s62, 8192
Ra 152,242,22528
Ra 156,242,23552
Mvav 64,128,204
s_mul_i32 s18, s85, 205
X 170
Mvav 96,128,212
K 208,168,169
Mvav 128,128,220
s_lshr_b32 s18, s18, 12
X 171
Mvav 132,132,220
Dc 244,8,61,0
Ra 128,242,8192
Mvav 100,132,212
X 172
Mvav 68,132,204
s_and_b32 s18, s18, 7
K 209,170,171
Mvav 36,132,196
X 173
Mvav 40,136,196
Dc 244,8,61,1024
v_lshrrev_b32 v4, 1, v1
Mvav 72,136,204
X 174
Mvav 104,136,212
Ra 132,242,9216
K 216,172,173
Mvav 136,136,220
X 175
Mvav 140,140,220
Dc 244,8,61,2048
v_min_u32 v5, 344, v4
Mvav 108,140,212
s_lshl_b32 s81, s18, 3
X 176
Mvav 76,140,204
Ra 136,242,10240
K 217,174,175
Mvav 44,140,196
W 3
X 177
Mvav 48,144,196
Dc 244,8,61,3072
v_mov_b32 v6, s81
Mvav 80,144,204
s_add_u32 m0, s62, 12288
X 178
Mvav 112,144,212
Ra 140,242,11264
K 194,176,177
Mvav 144,144,220
X 179
Mvav 148,148,220
buffer_load_dwordx2 v[8:9], v4, s[8:11], 0 offen offset:2560 sc1
Ra 144,242,12288
Mvav 116,148,212
X 180
Mvav 84,148,204
K 195,178,179
Mvav 52,148,196
X 181
Mvav 56,152,196
buffer_load_dwordx2 v[10:11], v5, s[8:11], 0 offen offset:3072 sc1
Ra 148,242,13312
Mvav 88,152,204
X 182
Mvav 120,152,212
K 202,180,181
Mvav 152,152,220
X 183
Mvav 156,156,220
Dc 245,8,61,0
Ra 152,242,14336
Mvav 124,156,212
X 184
Mvav 92,156,204
K 203,182,183
Mvav 60,156,196
W 3
X 185
Cvaa 160,128,0,224
buffer_load_dwordx2 v[12:13], v6, s[8:11], 0 offen offset:3584 sc1
Ra 156,242,15360
Mvaa 160,132,4
X 186
Cvaa 164,132,20,228
K 210,184,185
Mvaa 164,128,16
X 187
Cvaa 168,128,32,232
Dc 245,8,61,1024
Mvaa 168,132,36
X 188
Cvaa 172,132,52,236
K 211,186,187
Mvaa 172,128,48
X 189
Mvaa 160,136,8
Dc 245,8,61,2048
Ra 128,242,24576
Mvaa 160,140,12
X 190
Mvaa 164,140,28
Ra 132,242,25600
K 218,188,189
Mvaa 164,136,24
X 191
Mvaa 168,136,40
Dc 245,8,61,3072
Mvaa 168,140,44
s_add_u32 s61, s61, s63
s_xor_b32 s62, s62, 0x10000
s_add_u32 s64, s64, 1
Mvaa 172,140,60
X 160
Mvaa 172,136,56
W 2
Ra 136,242,26624
K 219,190,191
Cvaa 176,144,0,224
X 161
Mvaa 176,148,4
Ra 140,242,27648
Cvaa 180,148,20,228
X 162
Mvaa 180,144,16
K 196,160,161
Cvaa 184,144,32,232
X 163
Mvaa 184,148,36
X 164
Cvaa 188,148,52,236
K 197,162,163
Mvaa 188,144,48
X 165
Mvaa 176,152,8
Ra 144,242,28672
Ra 148,242,29696
Mvaa 176,156,12
X 166
Mvaa 180,156,28
K 204,164,165
Mvaa 180,152,24
X 167
Mvaa 184,152,40
Mvaa 184,156,44
X 168
Mvaa 188,156,60
Mvaa 188,152,56
W 2
Ra 152,242,30720
K 205,166,167
Mvav 32,128,192
X 169
Mvav 64,128,200
Ra 156,242,31744
Mvav 96,128,208
X 170
Mvav 128,128,216
K 212,168,169
Mvav 132,132,216
X 171
Mvav 100,132,208
X 172
Mvav 68,132,200
Mvav 36,132,192
K 213,170,171
Mvav 40,136,192
X 173
Mvav 72,136,200
X 174
Mvav 104,136,208
Mvav 136,136,216
s_waitcnt vmcnt(3)
v_cmp_eq_u64 vcc, s[12:13], v[8:9]
v_cmp_eq_u64_e64 s[86:87], s[12:13], v[10:11]
s_and_b64 vcc, vcc, s[86:87]
s_or_b64 vcc, vcc, s[20:21]
s_cmp_eq_u64 vcc, -1
s_cbranch_scc0 .Lspin_74%=
.Lok_75%=:
K 220,172,173
Mvav 140,140,216
X 175
Mvav 108,140,208
X 176
Mvav 76,140,200
s_waitcnt vmcnt(0) lgkmcnt(0)
s_barrier
v_xor_b32 v242, 0x10000, v242
Mvav 44,140,192
Ra 128,242,0
K 221,174,175
Mvav 48,144,192
X 177
Mvav 80,144,200
X 178
Mvav 112,144,208
Ra 132,242,1024
Mvav 144,144,216
K 198,176,177
Mvav 148,148,216
X 179
Mvav 116,148,208
X 180
Mvav 84,148,200
Ra 136,242,2048
Mvav 52,148,192
K 199,178,179
Mvav 56,152,192
X 181
Mvav 88,152,200
Ra 140,242,3072
Mvav 120,152,208
X 182
Mvav 152,152,216
K 206,180,181
Mvav 156,156,216
X 183
Mvav 124,156,208
Mvav 92,156,200
K 207,182,183
Mvav 60,156,192
W 0
Cvaa 160,128,0,224
Ra 144,242,4096
Ra 148,242,5120
Mvaa 160,132,4
s_add_u32 m0, s62, 0
Ra 152,242,6144
Cvaa 164,132,20,228
X 184
Mvaa 164,128,16
X 185
Cvaa 168,128,32,232
X 186
Mvaa 168,132,36
Ra 156,242,7168
K 214,184,185
Cvaa 172,132,52,236
Dc 1,8,61,0
Mvaa 172,128,48
X 187
Mvaa 160,136,8
X 188
Mvaa 160,140,12
Ra 128,242,16384
K 215,186,187
Mvaa 164,140,28
Dc 1,8,61,1024
Mvaa 164,136,24
X 189
Mvaa 168,136,40
X 190
Mvaa 168,140,44
Ra 132,242,17408
K 222,188,189
Mvaa 172,140,60
Dc 1,8,61,2048
Mvaa 172,136,56
X 191
v_smfmac_f32_16x16x64_bf16 a[160:163], a[176:179], v[192:199], v240
Ra 136,242,18432
K 223,190,191
v_smfmac_f32_16x16x64_bf16 a[164:167], a[176:179], v[200:207], v240
X 160
v_smfmac_f32_16x16x64_bf16 a[168:171], a[176:179], v[208:215], v240
Dc 1,8,61,3072
v_smfmac_f32_16x16x64_bf16 a[172:175], a[176:179], v[216:223], v240
W 3
X 161
Cvaa 176,144,0,224
s_add_u32 m0, s62, 4096
X 162
Mvaa 176,148,4
Ra 140,242,19456
K 192,160,161
Cvaa 180,148,20,228
X 163
Mvaa 180,144,16
Dc 243,8,61,0
Cvaa 184,144,32,232
X 164
Mvaa 184,148,36
K 193,162,163
Cvaa 188,148,52,236
X 165
Mvaa 188,144,48
Dc 243,8,61,1024
Ra 144,242,20480
Mvaa 176,152,8
X 166
Mvaa 176,156,12
Ra 148,242,21504
K 200,164,165
Mvaa 180,156,28
X 167
Mvaa 180,152,24
Dc 243,8,61,2048
Mvaa 184,152,40
X 168
Mvaa 184,156,44
K 201,166,167
Mvaa 188,156,60
X 169
Mvaa 188,152,56
W 2
Dc 243,8,61,3072
Ra 152,242,22528
Mvav 32,128,196
s_add_u32 m0, s62, 8192
X 170
Mvav 64,128,204
Ra 156,242,23552
K 208,168,169
Mvav 96,128,212
X 171
Mvav 128,128,220
Dc 244,8,61,0
Mvav 132,132,220
)"
R"(X 172
Mvav 100,132,212
Ra 128,242,8192
K 209,170,171
Mvav 68,132,204
X 173
Mvav 36,132,196
Dc 244,8,61,1024
Mvav 40,136,196
X 174
Mvav 72,136,204
Ra 132,242,9216
K 216,172,173
Mvav 104,136,212
X 175
Mvav 136,136,220
Dc 244,8,61,2048
Mvav 140,140,220
X 176
Mvav 108,140,212
Ra 136,242,10240
K 217,174,175
Mvav 76,140,204
X 177
Mvav 44,140,196
W 3
Dc 244,8,61,3072
Mvav 48,144,196
X 178
Mvav 80,144,204
s_add_u32 m0, s62, 12288
Ra 140,242,11264
Mvav 112,144,212
K 194,176,177
Mvav 144,144,220
X 179
Mvav 148,148,220
X 180
Mvav 116,148,212
Dc 245,8,61,0
Ra 144,242,12288
Mvav 84,148,204
K 195,178,179
Mvav 52,148,196
X 181
Mvav 56,152,196
X 182
Mvav 88,152,204
Dc 245,8,61,1024
Ra 148,242,13312
Mvav 120,152,212
v_xor_b32 v6, s12, v12
K 202,180,181
Mvav 152,152,220
X 183
Mvav 156,156,220
Ra 152,242,14336
v_and_b32 v6, 2047, v6
Mvav 124,156,212
Dc 245,8,61,2048
v_lshlrev_b32 v6, 3, v6
Mvav 92,156,204
X 184
Mvav 60,156,196
W 3
K 203,182,183
Cvaa 160,128,0,224
s_mov_b32 s81, 40960
X 185
Mvaa 160,132,4
Dc 245,8,61,3072
Ra 156,242,15360
Cvaa 164,132,20,228
s_add_u32 s61, s61, s63
X 186
Mvaa 164,128,16
K 210,184,185
Cvaa 168,128,32,232
X 187
Mvaa 168,132,36
buffer_load_dword v7, v6, s[8:11], s81 offen sc1
v_lshrrev_b32 v5, 7, v1
Cvaa 172,132,52,236
s_xor_b32 s62, s62, 0x10000
X 188
Mvaa 172,128,48
K 211,186,187
v_mul_u32_u24 v4, 20480, v5
Mvaa 160,136,8
X 189
Mvaa 160,140,12
s_add_u32 s64, s64, 1
Ra 128,242,24576
Ra 132,242,25600
Mvaa 164,140,28
X 190
Mvaa 164,136,24
v_and_b32 v6, 0x70, v1
K 218,188,189
Mvaa 168,136,40
X 191
Mvaa 168,140,44
v_add_u32 v4, v4, v6
Mvaa 172,140,60
X 160
Mvaa 172,136,56
W 2
v_add_u32 v5, 2, v5
K 219,190,191
Cvaa 176,144,0,224
X 161
Mvaa 176,148,4
Ra 136,242,26624
v_lshrrev_b32 v5, 3, v5
Cvaa 180,148,20,228
Ra 140,242,27648
v_lshl_or_b32 v5, v5, 31, v4
Mvaa 180,144,16
X 162
Cvaa 184,144,32,232
K 196,160,161
Mvaa 184,148,36
s_lshl_b32 s19, s85, 7
X 163
Cvaa 188,148,52,236
X 164
Mvaa 188,144,48
s_add_u32 s19, s19, 2752512
Ra 144,242,28672
K 197,162,163
Mvaa 176,152,8
X 165
Mvaa 176,156,12
s_and_b32 s81, s20, 134217728
Ra 148,242,29696
Mvaa 180,156,28
X 166
Mvaa 180,152,24
K 204,164,165
Mvaa 184,152,40
s_or_b32 s19, s19, s81
X 167
Mvaa 184,156,44
X 168
Mvaa 188,156,60
buffer_load_dwordx4 a[64:67], v4, s[8:11], s19 offen sc1
Mvaa 188,152,56
W 2
Ra 152,242,30720
K 205,166,167
Mvav 32,128,192
s_add_u32 s19, s19, 163840
X 169
Mvav 64,128,200
Ra 156,242,31744
Mvav 96,128,208
X 170
Mvav 128,128,216
buffer_load_dwordx4 a[68:71], v4, s[8:11], s19 offen sc1
K 212,168,169
Mvav 132,132,216
X 171
Mvav 100,132,208
s_add_u32 s19, s19, 163840
X 172
Mvav 68,132,200
Mvav 36,132,192
buffer_load_dwordx4 a[72:75], v4, s[8:11], s19 offen sc1
K 213,170,171
Mvav 40,136,192
X 173
Mvav 72,136,200
X 174
Mvav 104,136,208
s_add_u32 s19, s19, 163840
Mvav 136,136,216
K 220,172,173
Mvav 140,140,216
X 175
Mvav 108,140,208
buffer_load_dwordx4 a[76:79], v4, s[8:11], s19 offen sc1
Mvav 76,140,200
s_waitcnt vmcnt(5) lgkmcnt(0)
s_barrier
v_xor_b32 v242, 0x10000, v242
Mvav 44,140,192
s_add_u32 s19, s19, 163840
X 176
Mvav 48,144,192
Ra 128,242,0
K 221,174,175
Mvav 80,144,200
X 177
Mvav 112,144,208
buffer_load_dwordx4 a[80:83], v4, s[8:11], s19 offen sc1
Ra 132,242,1024
Mvav 144,144,216
s_add_u32 s19, s19, 163840
X 178
Mvav 148,148,216
K 198,176,177
Mvav 116,148,208
X 179
Mvav 84,148,200
buffer_load_dwordx4 a[84:87], v4, s[8:11], s19 offen sc1
Ra 136,242,2048
Mvav 52,148,192
X 180
Mvav 56,152,192
s_add_u32 s19, s19, 163840
Ra 140,242,3072
K 199,178,179
Mvav 88,152,200
X 181
Mvav 120,152,208
X 182
Mvav 152,152,216
buffer_load_dwordx4 a[88:91], v5, s[8:11], s19 offen sc1
K 206,180,181
Mvav 156,156,216
X 183
Mvav 124,156,208
Mvav 92,156,200
K 207,182,183
Mvav 60,156,192
W 0
Cvaa 160,128,0,224
Ra 144,242,4096
Ra 148,242,5120
Mvaa 160,132,4
s_add_u32 m0, s62, 0
Ra 152,242,6144
Cvaa 164,132,20,228
X 184
Mvaa 164,128,16
X 185
Cvaa 168,128,32,232
X 186
Mvaa 168,132,36
Ra 156,242,7168
K 214,184,185
Cvaa 172,132,52,236
Dc 1,8,61,0
Mvaa 172,128,48
X 187
Mvaa 160,136,8
X 188
Mvaa 160,140,12
Ra 128,242,16384
K 215,186,187
Mvaa 164,140,28
Dc 1,8,61,1024
Mvaa 164,136,24
X 189
Mvaa 168,136,40
X 190
Mvaa 168,140,44
Ra 132,242,17408
K 222,188,189
Mvaa 172,140,60
Dc 1,8,61,2048
Mvaa 172,136,56
X 191
v_smfmac_f32_16x16x64_bf16 a[160:163], a[176:179], v[192:199], v240
Ra 136,242,18432
K 223,190,191
v_smfmac_f32_16x16x64_bf16 a[164:167], a[176:179], v[200:207], v240
X 160
v_smfmac_f32_16x16x64_bf16 a[168:171], a[176:179], v[208:215], v240
Dc 1,8,61,3072
v_smfmac_f32_16x16x64_bf16 a[172:175], a[176:179], v[216:223], v240
W 3
X 161
Cvaa 176,144,0,224
s_add_u32 m0, s62, 4096
X 162
Mvaa 176,148,4
Ra 140,242,19456
K 192,160,161
Cvaa 180,148,20,228
X 163
Mvaa 180,144,16
Dc 243,8,61,0
Cvaa 184,144,32,232
X 164
Mvaa 184,148,36
K 193,162,163
Cvaa 188,148,52,236
X 165
Mvaa 188,144,48
Dc 243,8,61,1024
Ra 144,242,20480
Mvaa 176,152,8
X 166
Mvaa 176,156,12
Ra 148,242,21504
K 200,164,165
Mvaa 180,156,28
X 167
Mvaa 180,152,24
Dc 243,8,61,2048
Mvaa 184,152,40
X 168
Mvaa 184,156,44
K 201,166,167
Mvaa 188,156,60
X 169
Mvaa 188,152,56
W 2
Dc 243,8,61,3072
Ra 152,242,22528
Mvav 32,128,196
s_add_u32 m0, s62, 8192
X 170
Mvav 64,128,204
Ra 156,242,23552
K 208,168,169
Mvav 96,128,212
X 171
Mvav 128,128,220
Dc 244,8,61,0
Mvav 132,132,220
X 172
Mvav 100,132,212
Ra 128,242,8192
K 209,170,171
Mvav 68,132,204
X 173
Mvav 36,132,196
Dc 244,8,61,1024
Mvav 40,136,196
X 174
Mvav 72,136,204
Ra 132,242,9216
K 216,172,173
Mvav 104,136,212
X 175
Mvav 136,136,220
Dc 244,8,61,2048
Mvav 140,140,220
s_bfe_u32 s81, s59, 0x10001
X 176
Mvav 108,140,212
s_lshl_b32 s82, s57, 1
Ra 136,242,10240
K 217,174,175
Mvav 76,140,204
s_add_u32 s81, s81, s82
X 177
Mvav 44,140,196
W 3
s_lshl_b32 s83, s81, 2
Dc 244,8,61,3072
Mvav 48,144,196
s_lshr_b32 s81, s59, 2
X 178
Mvav 80,144,204
s_add_u32 m0, s62, 12288
s_lshl_b32 s81, s81, 1
Ra 140,242,11264
Mvav 112,144,212
s_add_u32 s83, s83, s81
K 194,176,177
Mvav 144,144,220
s_and_b32 s81, s59, 1
X 179
Mvav 148,148,220
s_add_u32 s83, s83, s81
X 180
Mvav 116,148,212
s_and_b32 s81, s14, 1
Dc 245,8,61,0
Ra 144,242,12288
Mvav 84,148,204
s_mul_i32 s81, s81, 124
K 195,178,179
Mvav 52,148,196
s_xor_b32 s83, s83, s81
X 181
Mvav 56,152,196
s_mul_i32 s83, s83, 20480
X 182
Mvav 88,152,204
s_mul_i32 s81, s58, 320
Dc 245,8,61,1024
Ra 148,242,13312
Mvav 120,152,212
s_add_u32 s82, s83, s81
K 202,180,181
Mvav 152,152,220
X 183
Mvav 156,156,220
Ra 152,242,14336
v_bfe_u32 v246, v1, 4, 4
Mvav 124,156,212
Dc 245,8,61,2048
v_mul_u32_u24 v246, 1280, v246
Mvav 92,156,204
X 184
Mvav 60,156,196
)"
R"(W 3
v_lshrrev_b32 v247, 8, v1
K 203,182,183
Cvaa 160,128,0,224
v_mul_u32_u24 v247, 104, v247
v_add3_u32 v246, v246, v247, s82
Mvaa 160,132,4
X 185
Cvaa 164,132,20,228
Dc 245,8,61,3072
Ra 156,242,15360
Mvaa 164,128,16
s_add_u32 s61, s61, s63
X 186
Cvaa 168,128,32,232
K 210,184,185
Mvaa 168,132,36
X 187
Cvaa 172,132,52,236
s_xor_b32 s62, s62, 0x10000
X 188
Mvaa 172,128,48
global_load_dword v3, v246, s[16:17]
K 211,186,187
Mvaa 160,136,8
s_lshl_b32 s81, s81, 3
s_add_u32 s83, s83, s81
s_add_u32 s83, s83, 131072
Mvaa 160,140,12
s_add_u32 s64, s64, 1
v_lshrrev_b32 v246, 9, v1
v_mul_u32_u24 v246, 10240, v246
Mvaa 164,140,28
Ra 128,242,24576
v_and_b32 v247, 0x1f0, v1
Mvaa 164,136,24
X 189
Mvaa 168,136,40
Ra 132,242,25600
v_min_u32 v247, 304, v247
Mvaa 168,140,44
X 190
Mvaa 172,140,60
v_lshl_add_u32 v246, v247, 3, v246
K 218,188,189
Mvaa 172,136,56
W 2
buffer_load_dword v3, v246, s[8:11], s83 offen sc1
Ra 136,242,26624
Cvaa 176,144,0,224
s_lshl_b32 s92, s58, 2
X 191
Mvaa 176,148,4
X 160
Cvaa 180,148,20,228
s_add_u32 s91, s92, s51
Ra 140,242,27648
K 219,190,191
Mvaa 180,144,16
X 161
Cvaa 184,144,32,232
X 162
Mvaa 184,148,36
s_add_u32 s95, s92, 0
K 196,160,161
Cvaa 188,148,52,236
X 163
Mvaa 188,144,48
X 164
Mvaa 176,152,8
s_and_b32 s95, s95, 15
Ra 144,242,28672
K 197,162,163
Mvaa 176,156,12
X 165
Mvaa 180,156,28
Ra 148,242,29696
Mvaa 180,152,24
s_mul_i32 s95, s95, 40960
X 166
Mvaa 184,152,40
K 204,164,165
Mvaa 184,156,44
X 167
Mvaa 188,156,60
s_mul_i32 s71, s51, 0
X 168
Mvaa 188,152,56
W 2
Ra 152,242,30720
K 205,166,167
Mvav 32,128,192
X 169
Mvav 64,128,200
s_add_u32 s71, s71, 133120
Ra 156,242,31744
Mvav 96,128,208
X 170
Mvav 128,128,216
K 212,168,169
Mvav 132,132,216
s_add_u32 m0, s71, s48
X 171
Mvav 100,132,208
X 172
Mvav 68,132,200
Mvav 36,132,192
s_lshl2_add_u32 s94, s48, s95
K 213,170,171
Mvav 40,136,192
X 173
Mvav 72,136,200
X 174
Mvav 104,136,208
D 16,40,94,0
Mvav 136,136,216
K 220,172,173
Mvav 140,140,216
X 175
Mvav 108,140,208
X 176
Mvav 76,140,200
s_waitcnt vmcnt(3) lgkmcnt(0)
s_barrier
v_xor_b32 v242, 0x10000, v242
Mvav 44,140,192
D 17,40,94,1024
K 221,174,175
Mvav 48,144,192
Ra 128,242,0
Mvav 80,144,200
X 177
Mvav 112,144,208
X 178
Mvav 144,144,216
D 18,40,94,2048
Ra 132,242,1024
Mvav 148,148,216
K 198,176,177
Mvav 116,148,208
X 179
Mvav 84,148,200
X 180
Mvav 52,148,192
D 19,40,94,3072
Ra 136,242,2048
Mvav 56,152,192
s_add_u32 m0, m0, 4096
Ra 140,242,3072
K 199,178,179
Mvav 88,152,200
X 181
Mvav 120,152,208
X 182
Mvav 152,152,216
buffer_load_dwordx4 v20, s[40:43], s94 offen lds
K 206,180,181
Mvav 156,156,216
X 183
Mvav 124,156,208
Mvav 92,156,200
K 207,182,183
Mvav 60,156,192
W 0
Cvaa 160,128,0,224
Ra 144,242,4096
Ra 148,242,5120
Mvaa 160,132,4
s_add_u32 m0, s62, 0
Ra 152,242,6144
G 8,64
Cvaa 164,132,20,228
X 184
Mvaa 164,128,16
X 185
Cvaa 168,128,32,232
G 9,65
G 10,66
Mvaa 168,132,36
X 186
Cvaa 172,132,52,236
Ra 156,242,7168
K 214,184,185
Mvaa 172,128,48
Dc 1,8,61,0
G 11,67
Mvaa 160,136,8
X 187
Mvaa 160,140,12
X 188
Mvaa 164,140,28
Ra 128,242,16384
G 12,68
Mvaa 164,136,24
Dc 1,8,61,1024
G 13,69
Mvaa 168,136,40
K 215,186,187
G 14,70
Mvaa 168,140,44
X 189
Mvaa 172,140,60
X 190
Mvaa 172,136,56
Dc 1,8,61,2048
G 15,71
v_smfmac_f32_16x16x64_bf16 a[160:163], a[176:179], v[192:199], v240
Ra 132,242,17408
K 222,188,189
v_smfmac_f32_16x16x64_bf16 a[164:167], a[176:179], v[200:207], v240
X 191
s_nop 0
K 223,190,191
v_smfmac_f32_16x16x64_bf16 a[168:171], a[176:179], v[208:215], v240
A 8,8,12
A 9,9,13
v_smfmac_f32_16x16x64_bf16 a[172:175], a[176:179], v[216:223], v240
W 2
Dc 1,8,61,3072
Ra 136,242,18432
Cvaa 176,144,0,224
s_add_u32 m0, s62, 4096
X 160
Mvaa 176,148,4
A 10,10,14
A 11,11,15
Cvaa 180,148,20,228
X 161
Mvaa 180,144,16
X 162
Cvaa 184,144,32,232
Ra 140,242,19456
G 12,72
Mvaa 184,148,36
K 192,160,161
G 13,73
Cvaa 188,148,52,236
X 163
Mvaa 188,144,48
Dc 243,8,61,0
G 14,74
Mvaa 176,152,8
X 164
Mvaa 176,156,12
G 15,75
K 193,162,163
Mvaa 180,156,28
X 165
Mvaa 180,152,24
Dc 243,8,61,1024
A 8,8,12
Mvaa 184,152,40
X 166
Mvaa 184,156,44
Ra 144,242,20480
A 9,9,13
Mvaa 188,156,60
Ra 148,242,21504
K 200,164,165
Mvaa 188,152,56
W 2
Dc 243,8,61,2048
A 10,10,14
Mvav 32,128,196
X 167
Mvav 64,128,204
A 11,11,15
G 12,76
Mvav 96,128,212
X 168
Mvav 128,128,220
Dc 243,8,61,3072
K 201,166,167
Mvav 132,132,220
s_add_u32 m0, s62, 8192
X 169
Mvav 100,132,212
Ra 152,242,22528
G 13,77
Mvav 68,132,204
X 170
Mvav 36,132,196
Ra 156,242,23552
G 14,78
Mvav 40,136,196
K 208,168,169
G 15,79
Mvav 72,136,204
X 171
Mvav 104,136,212
Dc 244,8,61,0
A 8,8,12
Mvav 136,136,220
X 172
Mvav 140,140,220
Ra 128,242,8192
A 9,9,13
Mvav 108,140,212
K 209,170,171
A 10,10,14
Mvav 76,140,204
X 173
Mvav 44,140,196
W 1
Dc 244,8,61,1024
A 11,11,15
Mvav 48,144,196
X 174
Mvav 80,144,204
Ra 132,242,9216
G 12,80
Mvav 112,144,212
K 216,172,173
G 13,81
Mvav 144,144,220
Dc 244,8,61,2048
Ra 136,242,10240
Mvav 148,148,220
X 175
Mvav 116,148,212
X 176
Mvav 84,148,204
G 14,82
K 217,174,175
Mvav 52,148,196
Dc 244,8,61,3072
G 15,83
Mvav 56,152,196
s_add_u32 m0, s62, 12288
X 177
Mvav 88,152,204
Ra 140,242,11264
A 8,8,12
Mvav 120,152,212
X 178
Mvav 152,152,220
A 9,9,13
K 194,176,177
Mvav 156,156,220
X 179
Mvav 124,156,212
Dc 245,8,61,0
A 10,10,14
Mvav 92,156,204
X 180
Mvav 60,156,196
W 0
Ra 144,242,12288
A 11,11,15
Cvaa 160,128,0,224
s_andn2_b32 s81, s49, 262144
K 195,178,179
G 12,84
Mvaa 160,132,4
s_lshl_b32 s82, s58, 12
X 181
Cvaa 164,132,20,228
s_add_u32 s81, s81, s82
Dc 245,8,61,1024
G 13,85
Mvaa 164,128,16
s_cmp_lt_u32 s57, 6
s_cselect_b32 s81, s81, 79691776
X 182
Cvaa 168,128,32,232
Ra 148,242,13312
G 14,86
Mvaa 168,132,36
K 202,180,181
G 15,87
Cvaa 172,132,52,236
X 183
Mvaa 172,128,48
Dc 245,8,61,2048
Ra 152,242,14336
Mvaa 160,136,8
X 184
Mvaa 160,140,12
A 8,8,12
K 203,182,183
Mvaa 164,140,28
Ra 156,242,15360
A 9,9,13
Mvaa 164,136,24
X 185
Mvaa 168,136,40
Dc 245,8,61,3072
A 10,10,14
Mvaa 168,140,44
s_add_u32 s61, s61, s63
s_xor_b32 s62, s62, 0x10000
s_add_u32 s64, s64, 1
Mvaa 172,140,60
X 186
Mvaa 172,136,56
W 0
A 11,11,15
K 210,184,185
Cvaa 176,144,0,224
X 187
)"
R"(Mvaa 176,148,4
G 12,88
v_lshrrev_b32 v246, 9, v1
Cvaa 180,148,20,228
X 188
Mvaa 180,144,16
K 211,186,187
v_and_b32 v247, 0x1f0, v1
Cvaa 184,144,32,232
G 13,89
v_lshlrev_b32 v247, 3, v247
Mvaa 184,148,36
X 189
X 190
K 218,188,189
X 191
X 160
K 219,190,191
Cvaa 188,148,52,236
G 14,90
v_lshl_add_u32 v246, v246, 17, v247
Mvaa 188,144,48
buffer_load_dword v3, v246, s[8:11], s81 offen sc1
Ra 128,242,24576
Mvaa 176,152,8
s_add_u32 s81, s81, 262144
Ra 132,242,25600
G 15,91
Mvaa 176,156,12
A 8,8,12
A 9,9,13
Mvaa 180,156,28
Ra 136,242,26624
Ra 140,242,27648
Mvaa 180,152,24
buffer_load_dword v3, v246, s[8:11], s81 offen sc1
A 10,10,14
Mvaa 184,152,40
s_lshl_b32 s92, s58, 2
A 11,11,15
v_add_f32_dpp v8, v8, v8 row_ror:8 row_mask:0xf bank_mask:0xf
Mvaa 184,156,44
s_add_u32 s91, s92, s51
X 161
Mvaa 188,156,60
s_add_u32 s95, s92, 1
X 162
Mvaa 188,152,56
W 0
v_add_f32_dpp v9, v9, v9 row_ror:8 row_mask:0xf bank_mask:0xf
K 196,160,161
Mvav 32,128,192
s_and_b32 s95, s95, 15
v_add_f32_dpp v10, v10, v10 row_ror:8 row_mask:0xf bank_mask:0xf
v_add_f32_dpp v11, v11, v11 row_ror:8 row_mask:0xf bank_mask:0xf
Mvav 64,128,200
s_mul_i32 s95, s95, 40960
X 163
Mvav 96,128,208
s_mul_i32 s71, s51, 0
X 164
Mvav 128,128,216
s_add_u32 s71, s71, 143360
v_mov_b32 v12, v8
K 197,162,163
Mvav 132,132,216
s_add_u32 m0, s71, s48
X 165
Mvav 100,132,208
Ra 144,242,28672
v_mov_b32 v13, v9
Mvav 68,132,200
Ra 148,242,29696
v_mov_b32 v14, v10
Mvav 36,132,192
s_lshl2_add_u32 s94, s48, s95
X 166
Mvav 40,136,192
K 204,164,165
v_mov_b32 v15, v11
Mvav 72,136,200
X 167
Mvav 104,136,208
Ra 152,242,30720
Ra 156,242,31744
Mvav 136,136,216
v_permlane16_swap_b32 v8, v12
v_permlane16_swap_b32 v9, v13
Mvav 140,140,216
X 168
Mvav 108,140,208
K 205,166,167
v_permlane16_swap_b32 v10, v14
Mvav 76,140,200
s_waitcnt vmcnt(2) lgkmcnt(0)
s_barrier
v_xor_b32 v242, 0x10000, v242
Mvav 44,140,192
X 169
Mvav 48,144,192
X 170
Mvav 80,144,200
v_permlane16_swap_b32 v11, v15
K 212,168,169
Mvav 112,144,208
A 8,8,12
A 9,9,13
Mvav 144,144,216
X 171
Mvav 148,148,216
X 172
Mvav 116,148,208
A 10,10,14
K 213,170,171
Mvav 84,148,200
X 173
Mvav 52,148,192
A 11,11,15
v_mov_b32 v12, v8
Mvav 56,152,192
X 174
Mvav 88,152,200
D 16,40,94,0
K 220,172,173
Mvav 120,152,208
v_mov_b32 v13, v9
v_mov_b32 v14, v10
Mvav 152,152,216
X 175
Mvav 156,156,216
X 176
Mvav 124,156,208
D 17,40,94,1024
v_mov_b32 v15, v11
Mvav 92,156,200
Ra 128,242,0
K 221,174,175
Mvav 60,156,192
D 18,40,94,2048
Ra 132,242,1024
D 19,40,94,3072
s_add_u32 m0, m0, 4096
Ra 136,242,2048
Ra 140,242,3072
buffer_load_dwordx4 v20, s[40:43], s94 offen lds
v_permlane32_swap_b32 v8, v12
X 177
v_permlane32_swap_b32 v9, v13
X 178
v_permlane32_swap_b32 v10, v14
K 198,176,177
X 179
v_permlane32_swap_b32 v11, v15
X 180
A 8,8,12
K 199,178,179
A 9,9,13
X 181
A 10,10,14
X 182
A 11,11,15
K 206,180,181
X 183
s_nop 0
K 207,182,183
W 0
Cvaa 160,128,0,224
Ra 144,242,4096
Ra 148,242,5120
Mvaa 160,132,4
s_add_u32 m0, s62, 0
Ra 152,242,6144
v_and_b32 v7, 0xffff0000, v7
Cvaa 164,132,20,228
X 184
Mvaa 164,128,16
X 185
Cvaa 168,128,32,232
Ra 156,242,7168
v_ashrrev_i32 v6, 31, v7
Mvaa 168,132,36
X 186
Cvaa 172,132,52,236
K 214,184,185
v_not_b32 v6, v6
Mvaa 172,128,48
Dc 1,8,61,0
v_or_b32 v6, 0x80000000, v6
Mvaa 160,136,8
X 187
Mvaa 160,140,12
X 188
Mvaa 164,140,28
Ra 128,242,16384
v_xor_b32 v7, v7, v6
Mvaa 164,136,24
Dc 1,8,61,1024
K 215,186,187
Mvaa 168,136,40
X 189
Mvaa 168,140,44
Ra 132,242,17408
v_and_b32 v7, 0xffff0000, v7
Mvaa 172,140,60
X 190
Mvaa 172,136,56
Dc 1,8,61,2048
v_mul_f32 v7, 0xbfb8aa3b, v7
v_smfmac_f32_16x16x64_bf16 a[160:163], a[176:179], v[192:199], v240
Ra 136,242,18432
K 222,188,189
v_smfmac_f32_16x16x64_bf16 a[164:167], a[176:179], v[200:207], v240
X 191
s_nop 0
K 223,190,191
v_smfmac_f32_16x16x64_bf16 a[168:171], a[176:179], v[208:215], v240
X 7
v_smfmac_f32_16x16x64_bf16 a[172:175], a[176:179], v[216:223], v240
W 3
Dc 1,8,61,3072
v_add_f32 v7, 1.0, v7
Cvaa 176,144,0,224
s_add_u32 m0, s62, 4096
X 160
Mvaa 176,148,4
X 161
Cvaa 180,148,20,228
X 162
Mvaa 180,144,16
Ra 140,242,19456
K 192,160,161
Cvaa 184,144,32,232
v_rcp_f32 v7, v7
Mvaa 184,148,36
X 163
Cvaa 188,148,52,236
Dc 243,8,61,0
K 7,7,7
Mvaa 188,144,48
X 164
Mvaa 176,152,8
K 193,162,163
v_lshlrev_b32 v7, 16, v7
Mvaa 176,156,12
X 165
Mvaa 180,156,28
Dc 243,8,61,1024
Ra 144,242,20480
Mvaa 180,152,24
X 166
Mvaa 184,152,40
Ra 148,242,21504
K 12,8,9
Mvaa 184,156,44
K 200,164,165
K 13,10,11
Mvaa 188,156,60
Dc 243,8,61,2048
v_lshlrev_b32 v8, 16, v12
Mvaa 188,152,56
W 2
X 167
Mvav 32,128,196
X 168
Mvav 64,128,204
K 201,166,167
v_and_b32 v9, 0xffff0000, v12
Mvav 96,128,212
Dc 243,8,61,3072
Ra 152,242,22528
Mvav 128,128,220
s_add_u32 m0, s62, 8192
X 169
Mvav 132,132,220
X 170
Mvav 100,132,212
Ra 156,242,23552
v_lshlrev_b32 v10, 16, v13
Mvav 68,132,204
K 208,168,169
v_and_b32 v11, 0xffff0000, v13
Mvav 36,132,196
X 171
Mvav 40,136,196
Dc 244,8,61,0
Ra 128,242,8192
Mvav 72,136,204
X 172
Mvav 104,136,212
U 8,8,7
K 209,170,171
Mvav 136,136,220
X 173
Mvav 140,140,220
Dc 244,8,61,1024
U 9,9,7
Mvav 108,140,212
X 174
Mvav 76,140,204
Ra 132,242,9216
U 10,10,7
Mvav 44,140,196
W 2
K 216,172,173
U 11,11,7
Mvav 48,144,196
Dc 244,8,61,2048
Ra 136,242,10240
Mvav 80,144,204
X 175
Mvav 112,144,212
X 176
Mvav 144,144,220
K 12,8,9
K 217,174,175
Mvav 148,148,220
Dc 244,8,61,3072
K 14,10,11
Mvav 116,148,212
s_add_u32 m0, s62, 12288
X 177
Mvav 84,148,204
X 178
Mvav 52,148,196
Ra 140,242,11264
v_mov_b32 v13, s13
Mvav 56,152,196
K 194,176,177
v_mov_b32 v15, s13
Mvav 88,152,204
X 179
Mvav 120,152,212
X 180
Mvav 152,152,220
Dc 245,8,61,0
Ra 144,242,12288
Mvav 156,156,220
v_lshrrev_b32 v6, 7, v1
K 195,178,179
Mvav 124,156,212
X 181
Mvav 92,156,204
Ra 148,242,13312
v_lshl_or_b32 v6, v6, 28, v1
Mvav 60,156,196
W 2
s_lshl_b32 s19, s85, 7
Dc 245,8,61,1024
Cvaa 160,128,0,224
s_add_u32 s19, s19, 98304
X 182
Mvaa 160,132,4
s_and_b32 s81, s20, 134217728
Ra 152,242,14336
K 202,180,181
Cvaa 164,132,20,228
s_or_b32 s19, s19, s81
X 183
Mvaa 164,128,16
Dc 245,8,61,2048
Ra 156,242,15360
Cvaa 168,128,32,232
X 184
Mvaa 168,132,36
K 203,182,183
Cvaa 172,132,52,236
X 185
Mvaa 172,128,48
Dc 245,8,61,3072
Ra 128,242,24576
Mvaa 160,136,8
s_add_u32 s61, s61, s63
X 186
Mvaa 160,140,12
s_xor_b32 s62, s62, 0x10000
Ra 132,242,25600
K 210,184,185
Mvaa 164,140,28
s_add_u32 s64, s64, 1
buffer_store_dwordx4 v[12:15], v6, s[8:11], s19 offen sc1
Mvaa 164,136,24
X 187
Mvaa 168,136,40
X 188
Mvaa 168,140,44
K 211,186,187
Mvaa 172,140,60
s_lshl_b32 s92, s58, 2
X 189
Mvaa 172,136,56
W 2
X 190
Cvaa 176,144,0,224
Ra 136,242,26624
K 218,188,189
Mvaa 176,148,4
X 191
Cvaa 180,148,20,228
s_add_u32 s91, s92, s51
X 160
)"
R"(Mvaa 180,144,16
Ra 140,242,27648
K 219,190,191
Cvaa 184,144,32,232
X 161
Mvaa 184,148,36
s_add_u32 s95, s92, 2
X 162
Cvaa 188,148,52,236
K 196,160,161
Mvaa 188,144,48
X 163
Mvaa 176,152,8
s_and_b32 s95, s95, 15
X 164
Mvaa 176,156,12
Ra 144,242,28672
K 197,162,163
Mvaa 180,156,28
X 165
Mvaa 180,152,24
s_mul_i32 s95, s95, 40960
Ra 148,242,29696
Mvaa 184,152,40
X 166
Mvaa 184,156,44
K 204,164,165
Mvaa 188,156,60
s_mul_i32 s71, s51, 0
X 167
Mvaa 188,152,56
W 2
X 168
Mvav 32,128,192
Ra 152,242,30720
K 205,166,167
Mvav 64,128,200
s_add_u32 s71, s71, 153600
X 169
Mvav 96,128,208
Ra 156,242,31744
Mvav 128,128,216
X 170
Mvav 132,132,216
s_add_u32 m0, s71, s48
K 212,168,169
Mvav 100,132,208
X 171
Mvav 68,132,200
X 172
Mvav 36,132,192
s_lshl2_add_u32 s94, s48, s95
K 213,170,171
Mvav 40,136,192
X 173
Mvav 72,136,200
X 174
Mvav 104,136,208
D 16,40,94,0
Mvav 136,136,216
K 220,172,173
Mvav 140,140,216
X 175
Mvav 108,140,208
X 176
Mvav 76,140,200
s_waitcnt vmcnt(2) lgkmcnt(0)
s_barrier
v_xor_b32 v242, 0x10000, v242
Mvav 44,140,192
D 17,40,94,1024
K 221,174,175
Mvav 48,144,192
Ra 128,242,0
Mvav 80,144,200
X 177
Mvav 112,144,208
X 178
Mvav 144,144,216
D 18,40,94,2048
Ra 132,242,1024
Mvav 148,148,216
K 198,176,177
Mvav 116,148,208
X 179
Mvav 84,148,200
X 180
Mvav 52,148,192
D 19,40,94,3072
Ra 136,242,2048
Mvav 56,152,192
s_add_u32 m0, m0, 4096
Ra 140,242,3072
K 199,178,179
Mvav 88,152,200
X 181
Mvav 120,152,208
X 182
Mvav 152,152,216
buffer_load_dwordx4 v20, s[40:43], s94 offen lds
K 206,180,181
Mvav 156,156,216
X 183
Mvav 124,156,208
Mvav 92,156,200
K 207,182,183
Mvav 60,156,192
.Lf1_38%=:
W 0
Cvaa 160,128,0,224
s_lshl_b32 s92, s58, 2
s_add_u32 s91, s92, s51
s_add_u32 s95, s91, 3
Mvaa 160,132,4
s_and_b32 s95, s95, 15
s_mul_i32 s95, s95, 40960
s_mul_i32 s71, s51, 10240
Cvaa 164,132,20,228
s_add_u32 s71, s71, 0
s_add_u32 m0, s71, s48
s_lshl2_add_u32 s94, s48, s95
Mvaa 164,128,16
Ra 144,242,4096
Ra 148,242,5120
Cvaa 168,128,32,232
Ra 152,242,6144
D 16,40,94,0
Mvaa 168,132,36
s_add_u32 s95, s91, 5
s_and_b32 s95, s95, 15
s_mul_i32 s95, s95, 40960
Cvaa 172,132,52,236
s_mul_i32 s71, s51, 10240
s_add_u32 s71, s71, 20480
Ra 156,242,7168
Mvaa 172,128,48
X 184
Mvaa 160,136,8
X 185
Mvaa 160,140,12
X 186
Mvaa 164,140,28
D 17,40,94,1024
K 214,184,185
Mvaa 164,136,24
X 187
Mvaa 168,136,40
X 188
Mvaa 168,140,44
Ra 128,242,16384
K 215,186,187
Mvaa 172,140,60
X 189
Mvaa 172,136,56
X 190
v_smfmac_f32_16x16x64_bf16 a[160:163], a[176:179], v[192:199], v240
D 18,40,94,2048
K 222,188,189
v_smfmac_f32_16x16x64_bf16 a[164:167], a[176:179], v[200:207], v240
X 191
s_nop 0
K 223,190,191
v_smfmac_f32_16x16x64_bf16 a[168:171], a[176:179], v[208:215], v240
Ra 132,242,17408
Ra 136,242,18432
v_smfmac_f32_16x16x64_bf16 a[172:175], a[176:179], v[216:223], v240
W 3
D 19,40,94,3072
Cvaa 176,144,0,224
s_add_u32 m0, m0, 4096
X 160
Mvaa 176,148,4
X 161
Cvaa 180,148,20,228
X 162
Mvaa 180,144,16
buffer_load_dwordx4 v20, s[40:43], s94 offen lds
v_bfe_u32 v28, v1, 4, 2
Cvaa 184,144,32,232
v_lshlrev_b32 v28, 3, v28
K 192,160,161
Mvaa 184,148,36
X 163
Cvaa 188,148,52,236
X 164
Mvaa 188,144,48
Ra 140,242,19456
K 193,162,163
Mvaa 176,152,8
buffer_load_dwordx2 v[28:29], v28, s[8:11], s54 offen sc1
Ra 144,242,20480
Mvaa 176,156,12
s_add_u32 m0, s71, s48
s_lshl2_add_u32 s94, s48, s95
Ra 148,242,21504
Mvaa 180,156,28
X 165
Mvaa 180,152,24
X 166
Mvaa 184,152,40
D 16,40,94,0
K 200,164,165
Mvaa 184,156,44
X 167
Mvaa 188,156,60
X 172
Mvaa 188,152,56
s_cmp_eq_u32 s60, 16
s_cbranch_scc1 .Lmk_77%=
.Lmkr_78%=:
W 2
Mvav 32,128,196
D 17,40,94,1024
K 201,166,167
Mvav 64,128,204
X 173
Mvav 96,128,212
X 174
Mvav 128,128,220
D 18,40,94,2048
K 216,172,173
Mvav 132,132,220
s_mov_b32 s18, 40960
X 175
Mvav 100,132,212
Ra 152,242,22528
Ra 156,242,23552
Mvav 68,132,204
s_mov_b32 s19, 73728
X 168
Mvav 36,132,196
s_bitcmp1_b32 s14, 0
s_cselect_b32 s18, 49152, s18
s_cselect_b32 s19, 98304, s19
D 19,40,94,3072
K 217,174,175
Mvav 40,136,196
s_add_u32 m0, m0, 4096
X 169
Mvav 72,136,204
s_cmp_lt_u32 s14, 2
s_cselect_b32 s20, s18, s19
s_cselect_b32 s21, s19, s18
X 170
Mvav 104,136,212
buffer_load_dwordx4 v20, s[40:43], s94 offen lds
K 208,168,169
Mvav 136,136,220
s_waitcnt vmcnt(5)
v_cmp_eq_u64 vcc, s[12:13], v[28:29]
s_cmp_eq_u64 vcc, -1
s_cselect_b32 s46, 79691776, 0
s_add_u32 s81, s58, 0
s_and_b32 s81, s81, 3
Mvav 140,140,220
s_lshl_b32 s81, s81, 12
s_add_u32 s81, s81, s49
Ra 128,242,24576
Mvav 108,140,212
X 171
Mvav 76,140,204
X 176
Mvav 44,140,196
W 1
buffer_load_dwordx4 a[184:187], v1, s[44:47], s81 offen offset:0 sc1
K 209,170,171
Mvav 48,144,196
s_add_u32 s82, s81, 131072
X 177
Mvav 80,144,204
X 178
Mvav 112,144,212
K 194,176,177
v_add_u32 v2, s20, v1
Mvav 144,144,220
X 179
Mvav 148,148,220
X 180
Mvav 116,148,212
v_add_u32 v3, s21, v1
K 195,178,179
Mvav 84,148,204
s_lshl_b32 s18, s14, 9
buffer_load_dwordx4 a[200:203], v1, s[44:47], s82 offen offset:0 sc1
Ra 132,242,25600
Mvav 52,148,196
X 181
Mvav 56,152,196
v_lshrrev_b32 v232, 1, v1
v_add_u32 v232, s18, v232
Mvav 88,152,204
X 182
Mvav 120,152,212
K 202,180,181
v_bfe_u32 v12, v1, 6, 2
Mvav 152,152,220
buffer_load_dwordx4 a[188:191], v1, s[44:47], s81 offen offset:1024 sc1
v_lshrrev_b32 v13, 8, v1
Mvav 156,156,220
X 183
Mvav 124,156,212
X 184
Mvav 92,156,204
Ra 136,242,26624
Ra 140,242,27648
Mvav 60,156,196
K 203,182,183
v_bfe_u32 v14, v1, 4, 1
Maav 160,180,192
X 185
X 186
K 210,184,185
X 187
X 188
K 211,186,187
Maav 164,180,200
X 189
X 190
K 218,188,189
X 191
s_nop 0
K 219,190,191
Maav 168,180,208
buffer_load_dwordx4 a[204:207], v1, s[44:47], s82 offen offset:1024 sc1
v_lshlrev_b32 v14, 4, v14
Maav 172,180,216
W 0
v_mov_b32 v15, 0x3f80
v_lshlrev_b32 v15, v14, v15
Mvav 32,128,192
v_bfe_u32 v14, v1, 5, 1
v_xor_b32 v27, v12, v13
Mvav 64,128,200
buffer_load_dwordx4 a[192:195], v1, s[44:47], s81 offen offset:2048 sc1
v_lshl_or_b32 v27, v27, 1, v14
Mvav 96,128,208
Ra 144,242,28672
v_cmp_eq_u32_e64 s[86:87], 0, v27
Mvav 128,128,216
Ra 148,242,29696
v_mov_b32 v226, 0
Mvav 132,132,216
buffer_load_dwordx4 a[208:211], v1, s[44:47], s82 offen offset:2048 sc1
v_cndmask_b32_e64 v224, 0, v15, s[86:87]
Mvav 100,132,208
v_mov_b32 v228, 0
v_mov_b32 v230, v224
Mvav 68,132,200
v_cmp_eq_u32_e64 s[86:87], 1, v27
v_mov_b32 v227, 0
Mvav 36,132,192
buffer_load_dwordx4 a[196:199], v1, s[44:47], s81 offen offset:3072 sc1
Ra 152,242,30720
Mvav 40,136,192
v_cndmask_b32_e64 v225, 0, v15, s[86:87]
v_mov_b32 v229, 0
Mvav 72,136,200
Ra 156,242,31744
v_mov_b32 v231, v225
Mvav 104,136,208
buffer_load_dwordx4 a[212:215], v1, s[44:47], s82 offen offset:3072 sc1
G 4,160
Mvav 136,136,216
s_add_u32 s81, s58, 1
s_and_b32 s81, s81, 3
s_lshl_b32 s81, s81, 12
Mvav 140,140,216
s_add_u32 s81, s81, s49
G 8,161
G 5,164
Mvav 108,140,208
buffer_load_dwordx4 a[216:219], v1, s[44:47], s81 offen offset:0 sc1
G 9,165
Mvav 76,140,200
s_add_u32 s82, s81, 131072
G 6,168
G 10,169
Mvav 44,140,192
W 0
G 7,172
G 11,173
Mvav 48,144,192
buffer_load_dwordx4 a[232:235], v1, s[44:47], s82 offen offset:0 sc1
A 4,4,8
Mvav 80,144,200
A 5,5,9
A 6,6,10
Mvav 112,144,208
A 7,7,11
Mvav 144,144,216
ds_write_b64 v232, v[6:7] offset:57344
A 8,4,5
Mvav 148,148,216
A 9,6,7
A 8,8,9
Mvav 116,148,208
buffer_load_dwordx4 a[220:223], v1, s[44:47], s81 offen offset:1024 sc1
v_cmp_ngt_f32_e64 s[86:87], s90, v8
Mvav 84,148,200
s_lshl_b32 s19, s14, 2
)"
R"(v_mov_b32 v9, s19
Mvav 52,148,192
s_or_b64 s[88:89], s[88:89], s[86:87]
s_and_b64 s[88:89], s[88:89], s[24:25]
s_cselect_b32 s18, 1, 0
v_mov_b32 v8, s18
Mvav 56,152,192
ds_write_b32 v9, v8 offset:59392
v_xor_b32 v10, 1024, v232
Mvav 88,152,200
v_bfe_u32 v11, v1, 2, 4
K 96,96,97
Mvav 120,152,208
K 97,98,99
K 98,100,101
Mvav 152,152,216
buffer_load_dwordx4 a[236:239], v1, s[44:47], s82 offen offset:1024 sc1
K 99,102,103
Mvav 156,156,216
ds_write_b128 v2, v[96:99] offset:0
K 128,128,129
Mvav 124,156,208
K 129,130,131
K 130,132,133
Mvav 92,156,200
buffer_load_dwordx4 a[224:227], v1, s[44:47], s81 offen offset:2048 sc1
K 131,134,135
Mvav 60,156,192
ds_write_b128 v2, v[128:131] offset:4096
K 104,104,105
K 105,106,107
K 106,108,109
K 107,110,111
buffer_load_dwordx4 a[240:243], v1, s[44:47], s82 offen offset:2048 sc1
ds_write_b128 v2, v[104:107] offset:1024
K 136,136,137
K 137,138,139
K 138,140,141
K 139,142,143
ds_write_b128 v2, v[136:139] offset:5120
buffer_load_dwordx4 a[228:231], v1, s[44:47], s81 offen offset:3072 sc1
K 112,112,113
K 113,114,115
K 114,116,117
K 115,118,119
buffer_load_dwordx4 a[244:247], v1, s[44:47], s82 offen offset:3072 sc1
ds_write_b128 v2, v[112:115] offset:2048
K 144,144,145
K 145,146,147
K 146,148,149
K 147,150,151
ds_write_b128 v2, v[144:147] offset:6144
K 120,120,121
K 121,122,123
K 122,124,125
K 123,126,127
ds_write_b128 v2, v[120:123] offset:3072
K 152,152,153
K 153,154,155
K 154,156,157
K 155,158,159
ds_write_b128 v2, v[152:155] offset:7168
.Lend_41%=:
s_waitcnt vmcnt(8) lgkmcnt(0)
s_barrier
s_cmp_eq_u32 s46, 0
s_cbranch_scc1 .Llate_79%=
ds_read_b32 v11, v11 offset:59392
ds_read_b64 v[8:9], v10 offset:57344
Rv 176,3,0
Rv 180,3,1024
Rv 184,3,2048
Rv 188,3,3072
Rv 192,3,4096
Rv 196,3,5120
Rv 200,3,6144
Rv 204,3,7168
W 6
Mvvv 32,224,176
s_lshl_b32 s92, s58, 2
s_add_u32 s91, s92, s51
s_add_u32 s95, s91, 7
Mvvv 36,228,176
s_and_b32 s95, s95, 15
s_mul_i32 s95, s95, 40960
s_mul_i32 s71, s51, 20480
Mvvv 40,224,180
s_add_u32 s71, s71, 61440
s_add_u32 m0, s71, s48
s_lshl2_add_u32 s94, s48, s95
Mvvv 44,228,180
W 4
v_cmp_ne_u32 vcc, 0, v11
s_cmp_lg_u64 vcc, 0
s_cbranch_scc1 .Lredo%=
A 4,4,8
Mvvv 48,224,184
v_rcp_f32 v4, v4
Mvvv 52,228,184
Ta 128,25,10240
A 5,5,9
Mvvv 56,224,188
Ta 130,25,15360
D 16,40,94,0
Mvvv 60,228,188
W 4
s_add_u32 s95, s91, 9
s_and_b32 s95, s95, 15
Mvvv 64,224,192
s_mul_i32 s95, s95, 40960
s_mul_i32 s71, s51, 10240
s_add_u32 s71, s71, 112640
Mvvv 68,228,192
v_rcp_f32 v5, v5
Mvvv 72,224,196
s_lshl_b32 s93, s58, 12
s_add_u32 s93, s93, s50
Ta 132,25,20480
Mvvv 76,228,196
Ta 134,25,25600
W 4
Mvvv 80,224,200
U 32,32,4
U 33,33,4
Mvvv 84,228,200
Ta 136,26,10272
U 34,34,4
Mvvv 88,224,204
Ta 138,26,15392
U 35,35,4
Mvvv 92,228,204
Ta 140,26,20512
Ta 142,26,25632
W 4
Zvaa 96,128,184
U 36,36,4
U 37,37,4
Mvaa 96,132,188
D 17,40,94,1024
U 38,38,4
Zvaa 104,132,204
U 39,39,4
K 32,32,33
Mvaa 104,128,200
Ta 144,25,10304
Ta 146,25,15424
Ta 148,25,20544
Ta 150,25,25664
W 4
Zvaa 108,136,200
K 33,34,35
K 34,36,37
Mvaa 108,140,204
Ta 152,26,10336
K 35,38,39
Zvaa 100,140,188
buffer_store_dwordx4 v[32:35], v1, s[8:11], s93 offen offset:0 sc1
U 40,40,4
Mvaa 100,136,184
Ta 154,26,15456
Ta 156,26,20576
Ta 158,26,25696
W 4
Zvaa 112,144,184
U 41,41,4
U 42,42,4
Mvaa 112,148,188
U 43,43,4
U 44,44,4
Zvaa 120,148,204
D 18,40,94,2048
U 45,45,4
Mvaa 120,144,200
Ta 128,25,10368
Ta 130,25,15488
Ta 132,25,20608
Ta 134,25,25728
W 4
Zvaa 124,152,200
U 46,46,4
U 47,47,4
Mvaa 124,156,204
K 40,40,41
K 41,42,43
Zvaa 116,156,188
K 42,44,45
K 43,46,47
Mvaa 116,152,184
Ta 136,26,10400
Ta 138,26,15520
Ta 140,26,20640
Ta 142,26,25760
W 4
Zvaa 128,128,184
buffer_store_dwordx4 v[40:43], v1, s[8:11], s93 offen offset:1024 sc1
U 48,48,4
Mvaa 128,132,188
U 49,49,4
U 50,50,4
Zvaa 136,132,204
U 51,51,4
U 52,52,4
Mvaa 136,128,200
Ta 144,25,10432
Ta 146,25,15552
Ta 148,25,20672
Ta 150,25,25792
W 4
Zvaa 140,136,200
D 19,40,94,3072
U 53,53,4
Mvaa 140,140,204
s_add_u32 m0, m0, 4096
U 54,54,4
U 55,55,4
Zvaa 132,140,188
K 48,48,49
K 49,50,51
Mvaa 132,136,184
Ta 152,26,10464
Ta 154,26,15584
Ta 156,26,20704
Ta 158,26,25824
W 4
Zvaa 144,144,184
K 50,52,53
K 51,54,55
Mvaa 144,148,188
buffer_load_dwordx4 v20, s[40:43], s94 offen lds
Ta 128,25,10496
Zvaa 152,148,204
s_add_u32 m0, s71, s48
s_lshl2_add_u32 s94, s48, s95
Ta 130,25,15616
Mvaa 152,144,200
Ta 132,25,20736
Ta 134,25,25856
W 4
Zvaa 156,152,200
s_waitcnt vmcnt(7)
s_cmp_lg_u32 s70, 0
s_cbranch_scc1 .Lpd_81%=
.Lpdr_82%=:
Ta 136,26,10528
Ta 138,26,15648
Mvaa 156,156,204
buffer_store_dwordx4 v[48:51], v1, s[8:11], s93 offen offset:2048 sc1
U 56,56,4
Zvaa 148,156,188
U 57,57,4
U 58,58,4
Mvaa 148,152,184
Ta 140,26,20768
Ta 142,26,25888
W 4
Zvaa 160,128,184
U 59,59,4
U 60,60,4
Mvaa 160,132,188
U 61,61,4
U 62,62,4
Zvaa 168,132,204
U 63,63,4
K 56,56,57
Mvaa 168,128,200
Ta 144,25,30720
Ta 146,25,35840
Ta 148,21,0
Ta 150,21,5120
W 4
Zvaa 172,136,200
D 16,40,94,0
K 57,58,59
Mvaa 172,140,204
K 58,60,61
K 59,62,63
Zvaa 164,140,188
Ta 152,26,30752
Ta 154,26,35872
Mvaa 164,136,184
Ta 156,22,32
Ta 158,22,5152
W 4
Mvaa 96,144,192
buffer_store_dwordx4 v[56:59], v1, s[8:11], s93 offen offset:3072 sc1
U 64,64,5
Mvaa 96,148,196
U 65,65,5
U 66,66,5
Mvaa 104,148,212
U 67,67,5
U 68,68,5
Mvaa 104,144,208
Ta 128,25,30784
Ta 130,25,35904
Ta 132,21,64
Ta 134,21,5184
W 4
Mvaa 108,152,208
D 17,40,94,1024
U 69,69,5
Mvaa 108,156,212
U 70,70,5
U 71,71,5
Mvaa 100,156,196
K 64,64,65
K 65,66,67
Mvaa 100,152,192
Ta 136,26,30816
Ta 138,26,35936
Ta 140,22,96
Ta 142,22,5216
W 4
Mvaa 112,128,192
K 66,68,69
K 67,70,71
Mvaa 112,132,196
s_add_u32 s67, s93, 131072
buffer_store_dwordx4 v[64:67], v1, s[8:11], s67 offen offset:0 sc1
U 72,72,5
Mvaa 120,132,212
U 73,73,5
U 74,74,5
Mvaa 120,128,208
Ta 144,25,30848
Ta 146,25,35968
Ta 148,21,128
Ta 150,21,5248
W 4
Mvaa 124,136,208
U 75,75,5
U 76,76,5
Mvaa 124,140,212
D 18,40,94,2048
U 77,77,5
Mvaa 116,140,196
U 78,78,5
U 79,79,5
Mvaa 116,136,192
Ta 152,26,30880
Ta 154,26,36000
Ta 156,22,160
Ta 158,22,5280
W 4
Mvaa 128,144,192
K 72,72,73
K 73,74,75
Mvaa 128,148,196
K 74,76,77
K 75,78,79
Mvaa 136,148,212
buffer_store_dwordx4 v[72:75], v1, s[8:11], s67 offen offset:1024 sc1
U 80,80,5
Mvaa 136,144,208
Ta 128,25,30912
Ta 130,25,36032
Ta 132,21,192
Ta 134,21,5312
W 4
Mvaa 140,152,208
D 19,40,94,3072
s_add_u32 m0, m0, 4096
s_nop 0
buffer_load_dwordx4 v20, s[40:43], s94 offen lds
Mvaa 140,156,212
s_add_u32 s81, s58, 2
s_and_b32 s81, s81, 3
s_lshl_b32 s81, s81, 12
Mvaa 132,156,196
s_add_u32 s81, s81, s49
U 81,81,5
U 82,82,5
Mvaa 132,152,192
Ta 136,26,30944
Ta 138,26,36064
Ta 140,22,224
Ta 142,22,5344
W 4
Mvaa 144,128,192
U 83,83,5
U 84,84,5
Mvaa 144,132,196
U 85,85,5
U 86,86,5
Mvaa 152,132,212
U 87,87,5
K 80,80,81
Mvaa 152,128,208
Ta 144,25,30976
Ta 146,25,36096
Ta 148,21,256
Ta 150,21,5376
W 4
Mvaa 156,136,208
K 81,82,83
K 82,84,85
Mvaa 156,140,212
buffer_load_dwordx4 a[0:3], v1, s[8:11], s81 offen offset:0 sc1
K 83,86,87
Mvaa 148,140,196
)"
R"(s_add_u32 s82, s81, 131072
Ta 152,26,31008
Ta 154,26,36128
Mvaa 148,136,192
Ta 156,22,288
Ta 158,22,5408
W 4
Mvaa 160,144,192
Ta 128,21,10240
Ta 130,21,15360
Mvaa 160,148,196
buffer_store_dwordx4 v[80:83], v1, s[8:11], s67 offen offset:2048 sc1
U 88,88,5
Mvaa 168,148,212
U 89,89,5
U 90,90,5
Mvaa 168,144,208
Ta 132,21,20480
Ta 134,21,25600
W 4
Mvaa 172,152,208
U 91,91,5
U 92,92,5
Mvaa 172,156,212
U 93,93,5
U 94,94,5
Mvaa 164,156,196
U 95,95,5
K 88,88,89
Mvaa 164,152,192
Ta 136,22,10272
Ta 138,22,15392
Ta 140,22,20512
Ta 142,22,25632
W 4
Mvaa 96,128,216
K 89,90,91
K 90,92,93
Mvaa 96,132,220
buffer_load_dwordx4 a[16:19], v1, s[8:11], s82 offen offset:0 sc1
K 91,94,95
Mvaa 104,132,236
Ta 144,21,10304
Ta 146,21,15424
Mvaa 104,128,232
Ta 148,21,20544
Ta 150,21,25664
W 4
Mvaa 108,136,232
Ta 152,22,10336
Ta 154,22,15456
Mvaa 108,140,236
buffer_store_dwordx4 v[88:91], v1, s[8:11], s67 offen offset:3072 sc1
Ta 156,22,20576
Mvaa 100,140,220
Ta 158,22,25696
Ta 128,21,10368
Mvaa 100,136,216
W 5
Ta 130,21,15488
Ta 132,21,20608
Mvaa 112,144,216
Ta 134,21,25728
Ta 136,22,10400
Mvaa 112,148,220
buffer_load_dwordx4 a[4:7], v1, s[8:11], s81 offen offset:1024 sc1
Ta 138,22,15520
Mvaa 120,148,236
Ta 140,22,20640
Ta 142,22,25760
Mvaa 120,144,232
W 8
Ta 144,21,10432
Ta 146,21,15552
Mvaa 124,152,232
Mvaa 124,156,236
buffer_load_dwordx4 a[20:23], v1, s[8:11], s82 offen offset:1024 sc1
Ta 148,21,20672
Mvaa 116,156,220
Ta 150,21,25792
Mvaa 116,152,216
W 8
Ta 152,22,10464
Ta 154,22,15584
Mvaa 128,128,216
Mvaa 128,132,220
buffer_load_dwordx4 a[8:11], v1, s[8:11], s81 offen offset:2048 sc1
Ta 156,22,20704
Mvaa 136,132,236
Ta 158,22,25824
Mvaa 136,128,232
W 8
Ta 128,21,10496
Ta 130,21,15616
Mvaa 140,136,232
s_waitcnt vmcnt(7)
s_barrier
Mvaa 140,140,236
buffer_load_dwordx4 a[24:27], v1, s[8:11], s82 offen offset:2048 sc1
Ta 132,21,20736
Mvaa 132,140,220
Ta 134,21,25856
Mvaa 132,136,216
W 8
Ta 136,22,10528
Ta 138,22,15648
Mvaa 144,144,216
Mvaa 144,148,220
buffer_load_dwordx4 a[12:15], v1, s[8:11], s81 offen offset:3072 sc1
Ta 140,22,20768
Mvaa 152,148,236
Ta 142,22,25888
Mvaa 152,144,232
W 8
Ta 144,21,30720
Ta 146,21,35840
Mvaa 156,152,232
Mvaa 156,156,236
buffer_load_dwordx4 a[28:31], v1, s[8:11], s82 offen offset:3072 sc1
Ta 148,23,0
Mvaa 148,156,220
s_lshl_b32 s92, s58, 2
s_add_u32 s91, s92, s51
s_add_u32 s95, s91, 11
Mvaa 148,152,216
W 7
s_and_b32 s95, s95, 15
s_mul_i32 s95, s95, 40960
Mvaa 160,128,216
s_mul_i32 s71, s51, 10240
s_add_u32 s71, s71, 40960
s_add_u32 m0, s71, s48
Mvaa 160,132,220
s_lshl2_add_u32 s94, s48, s95
D 16,40,94,0
Ta 150,23,5120
Mvaa 168,132,236
s_add_u32 s95, s91, 13
s_and_b32 s95, s95, 15
s_mul_i32 s95, s95, 40960
Mvaa 168,128,232
W 4
s_mul_i32 s71, s51, 20480
s_add_u32 s71, s71, 71680
Mvaa 172,136,232
Ta 152,22,30752
Ta 154,22,35872
Mvaa 172,140,236
D 17,40,94,1024
Ta 156,24,32
Mvaa 164,140,220
Ta 158,24,5152
Ta 128,21,30784
Mvaa 164,136,216
W 5
Ta 130,21,35904
Ta 132,23,64
Mvaa 96,144,224
Ta 134,23,5184
Ta 136,22,30816
Mvaa 96,148,228
D 18,40,94,2048
Ta 138,22,35936
Mvaa 104,148,244
Ta 140,24,96
Ta 142,24,5216
Mvaa 104,144,240
W 8
Ta 144,21,30848
Ta 146,21,35968
Mvaa 108,152,240
Mvaa 108,156,244
D 19,40,94,3072
Ta 148,23,128
Mvaa 100,156,228
s_add_u32 m0, m0, 4096
Ta 150,23,5248
Mvaa 100,152,224
W 8
Ta 152,22,30880
Ta 154,22,36000
Mvaa 112,128,224
Mvaa 112,132,228
buffer_load_dwordx4 v20, s[40:43], s94 offen lds
Ta 156,24,160
Mvaa 120,132,244
s_add_u32 m0, s71, s48
s_lshl2_add_u32 s94, s48, s95
Ta 158,24,5280
Mvaa 120,128,240
W 8
Ta 128,21,30912
Ta 130,21,36032
Mvaa 124,136,240
Mvaa 124,140,244
D 16,40,94,0
Ta 132,23,192
Mvaa 116,140,228
s_add_u32 s95, s92, 15
s_and_b32 s95, s95, 15
s_mul_i32 s95, s95, 40960
Mvaa 116,136,224
W 7
s_mul_i32 s71, s51, 0
s_add_u32 s71, s71, 102400
Mvaa 128,144,224
Ta 134,23,5312
Ta 136,22,30944
Mvaa 128,148,228
D 17,40,94,1024
Ta 138,22,36064
Mvaa 136,148,244
Ta 140,24,224
Ta 142,24,5344
Mvaa 136,144,240
W 8
Ta 144,21,30976
Ta 146,21,36096
Mvaa 140,152,240
Mvaa 140,156,244
D 18,40,94,2048
Ta 148,23,256
Mvaa 132,156,228
Ta 150,23,5376
Mvaa 132,152,224
W 8
Ta 152,22,31008
Ta 154,22,36128
Mvaa 144,128,224
s_lshl_b32 s80, s58, 3
s_add_u32 s80, s80, s55
Mvaa 144,132,228
D 19,40,94,3072
Ta 156,24,288
Mvaa 152,132,244
s_add_u32 m0, m0, 4096
Ta 158,24,5408
Mvaa 152,128,240
W 8
Ta 128,23,20480
Ta 130,23,25600
Mvaa 156,136,240
s_waitcnt vmcnt(15)
v_mov_b64 v[28:29], s[12:13]
s_mov_b64 exec, 1
buffer_store_dwordx2 v[28:29], off, s[8:11], s80 sc1
s_mov_b64 exec, -1
Mvaa 156,140,244
Ta 132,23,51200
Ta 134,23,56320
Mvaa 148,140,228
Mvaa 148,136,224
W 8
Ta 136,24,20512
Ta 138,24,25632
Mvaa 160,144,224
buffer_load_dwordx4 v20, s[40:43], s94 offen lds
Mvaa 160,148,228
s_add_u32 m0, s71, s48
s_lshl2_add_u32 s94, s48, s95
Ta 140,24,51232
Mvaa 168,148,244
Ta 142,24,56352
Mvaa 168,144,240
W 8
Ta 144,23,20544
Ta 146,23,25664
Mvaa 172,152,240
D 16,40,94,0
Mvaa 172,156,244
Ta 148,23,51264
Ta 150,23,56384
Mvaa 164,156,228
Mvaa 164,152,224
s_waitcnt vmcnt(16) lgkmcnt(8)
Ta 152,24,20576
Ta 154,24,25696
Mvaa 96,128,0
D 17,40,94,1024
Mvaa 96,132,4
Ta 156,24,51296
Ta 158,24,56416
Mvaa 104,132,20
Mvaa 104,128,16
W 8
Ta 128,23,20608
Ta 130,23,25728
Mvaa 108,136,16
D 18,40,94,2048
Mvaa 108,140,20
Ta 132,23,51328
Ta 134,23,56448
Mvaa 100,140,4
Mvaa 100,136,0
W 8
Ta 136,24,20640
Ta 138,24,25760
Mvaa 112,144,0
D 19,40,94,3072
Mvaa 112,148,4
s_add_u32 m0, m0, 4096
Ta 140,24,51360
Ta 142,24,56480
Mvaa 120,148,20
Mvaa 120,144,16
W 8
Ta 144,23,20672
Ta 146,23,25792
Mvaa 124,152,16
buffer_load_dwordx4 v20, s[40:43], s94 offen lds
Mvaa 124,156,20
s_add_u32 s81, s58, 3
s_and_b32 s81, s81, 3
s_lshl_b32 s81, s81, 12
Mvaa 116,156,4
s_add_u32 s81, s81, s49
Ta 148,23,51392
Ta 150,23,56512
Mvaa 116,152,0
W 8
Ta 152,24,20704
Ta 154,24,25824
Mvaa 128,128,0
buffer_load_dwordx4 a[32:35], v1, s[8:11], s81 offen offset:0 sc1
Mvaa 128,132,4
s_add_u32 s82, s81, 131072
Ta 156,24,51424
Ta 158,24,56544
Mvaa 136,132,20
Mvaa 136,128,16
W 8
Ta 128,23,20736
Ta 130,23,25856
Mvaa 140,136,16
s_waitcnt vmcnt(12)
s_barrier
buffer_load_dwordx4 a[48:51], v1, s[8:11], s82 offen offset:0 sc1
Mvaa 140,140,20
Ta 132,23,51456
v_and_b32 v27, 1, v12
Mvaa 132,140,4
Ta 134,23,56576
Mvaa 132,136,0
W 8
Ta 136,24,20768
v_lshlrev_b32 v27, 1, v27
Mvaa 144,144,0
buffer_load_dwordx4 a[36:39], v1, s[8:11], s81 offen offset:1024 sc1
Ta 138,24,25888
Mvaa 144,148,4
Ta 140,24,51488
Ta 142,24,56608
Mvaa 152,148,20
v_xor_b32 v27, v27, v13
Mvaa 152,144,16
W 8
Ta 144,25,0
Ta 146,25,5120
Mvaa 156,152,16
buffer_load_dwordx4 a[52:55], v1, s[8:11], s82 offen offset:1024 sc1
Mvaa 156,156,20
Ta 148,21,40960
v_and_b32 v12, 2, v12
Mvaa 148,156,4
Ta 150,21,46080
Mvaa 148,152,0
W 8
Ta 152,26,32
v_or_b32 v12, v12, v14
Mvaa 160,128,0
buffer_load_dwordx4 a[40:43], v1, s[8:11], s81 offen offset:2048 sc1
Ta 154,26,5152
Mvaa 160,132,4
Ta 156,22,40992
Ta 158,22,46112
Mvaa 168,132,20
v_lshl_or_b32 v12, v27, 2, v12
Mvaa 168,128,16
W 8
Ta 128,25,64
Ta 130,25,5184
Mvaa 172,136,16
buffer_load_dwordx4 a[56:59], v1, s[8:11], s82 offen offset:2048 sc1
Mvaa 172,140,20
Ta 132,21,41024
v_cmp_eq_u32_e64 s[86:87], 0, v12
Mvaa 164,140,4
Ta 134,21,46144
Mvaa 164,136,0
W 8
Ta 136,26,96
v_cndmask_b32_e64 v4, 0, v15, s[86:87]
Mvaa 96,144,8
buffer_load_dwordx4 a[44:47], v1, s[8:11], s81 offen offset:3072 sc1
Ta 138,26,5216
Mvaa 96,148,12
Ta 140,22,41056
Ta 142,22,46176
Mvaa 104,148,28
v_cmp_eq_u32_e64 s[86:87], 1, v12
)"
R"(Mvaa 104,144,24
W 8
Ta 144,25,128
Ta 146,25,5248
Mvaa 108,152,24
buffer_load_dwordx4 a[60:63], v1, s[8:11], s82 offen offset:3072 sc1
Mvaa 108,156,28
Ta 148,21,41088
v_cndmask_b32_e64 v5, 0, v15, s[86:87]
Mvaa 100,156,12
Ta 150,21,46208
Mvaa 100,152,8
W 8
Ta 152,26,160
Ta 154,26,5280
Mvaa 112,128,8
v_cmp_eq_u32_e64 s[86:87], 2, v12
Mvaa 112,132,12
Ta 156,22,41120
Ta 158,22,46240
Mvaa 120,132,28
v_cndmask_b32_e64 v6, 0, v15, s[86:87]
Mvaa 120,128,24
W 8
Ta 128,25,192
Ta 130,25,5312
Mvaa 124,136,24
Mvaa 124,140,28
Ta 132,21,41152
v_cmp_eq_u32_e64 s[86:87], 3, v12
Mvaa 116,140,12
Ta 134,21,46272
Mvaa 116,136,8
W 8
Ta 136,26,224
Ta 138,26,5344
Mvaa 128,144,8
v_cndmask_b32_e64 v7, 0, v15, s[86:87]
Mvaa 128,148,12
Ta 140,22,41184
Ta 142,22,46304
Mvaa 136,148,28
v_cmp_eq_u32_e64 s[86:87], 4, v12
Mvaa 136,144,24
W 8
Ta 144,25,256
Ta 146,25,5376
Mvaa 140,152,24
s_waitcnt vmcnt(8)
s_barrier
Mvaa 140,156,28
Ta 148,21,41216
v_cndmask_b32_e64 v8, 0, v15, s[86:87]
Mvaa 132,156,12
Ta 150,21,46336
Mvaa 132,152,8
W 8
Ta 152,26,288
Ta 154,26,5408
Mvaa 144,128,8
v_cmp_eq_u32_e64 s[86:87], 5, v12
Mvaa 144,132,12
Ta 156,22,41248
Ta 158,22,46368
Mvaa 152,132,28
Mvaa 152,128,24
W 8
Ta 128,21,51200
v_cndmask_b32_e64 v9, 0, v15, s[86:87]
Mvaa 156,136,24
Ta 130,21,56320
Mvaa 156,140,28
Ta 132,23,10240
v_cmp_eq_u32_e64 s[86:87], 6, v12
Mvaa 148,140,12
Ta 134,23,15360
Mvaa 148,136,8
W 8
Ta 136,22,51232
Ta 138,22,56352
Mvaa 160,144,8
v_cndmask_b32_e64 v10, 0, v15, s[86:87]
v_bfe_u32 v28, v1, 4, 2
Mvaa 160,148,12
Ta 140,24,10272
v_lshlrev_b32 v28, 3, v28
Mvaa 168,148,28
Ta 142,24,15392
buffer_load_dwordx2 v[28:29], v28, s[8:11], s55 offen sc1
Mvaa 168,144,24
W 8
Ta 144,21,51264
v_cmp_eq_u32_e64 s[86:87], 7, v12
Mvaa 172,152,24
Ta 146,21,56384
Mvaa 172,156,28
Ta 148,23,10304
v_cndmask_b32_e64 v11, 0, v15, s[86:87]
Mvaa 164,156,12
Ta 150,23,15424
Mvaa 164,152,8
s_waitcnt vmcnt(5) lgkmcnt(8)
Ta 152,22,51296
Ta 154,22,56416
Mvaa 96,128,32
s_mov_b32 s20, s38
Mvaa 96,132,36
Ta 156,24,10336
Ta 158,24,15456
Mvaa 104,132,52
Mvaa 104,128,48
W 8
s_and_b32 s21, s39, 0xffff
Ta 128,21,51328
Mvaa 108,136,48
Ta 130,21,56448
Mvaa 108,140,52
Ta 132,23,10368
Ta 134,23,15488
Mvaa 100,140,36
s_mov_b32 s22, 2621440
Mvaa 100,136,32
W 8
Ta 136,22,51360
Ta 138,22,56480
Mvaa 112,144,32
s_mov_b32 s23, 0x20000
Mvaa 112,148,36
Ta 140,24,10400
Ta 142,24,15520
Mvaa 120,148,52
Mvaa 120,144,48
W 8
s_and_b32 s17, s17, 0xffff
Ta 144,21,51392
Mvaa 124,152,48
Ta 146,21,56512
Mvaa 124,156,52
Ta 148,23,10432
Ta 150,23,15552
Mvaa 116,156,36
s_mov_b32 s18, 2621440
Mvaa 116,152,32
W 8
Ta 152,22,51424
Ta 154,22,56544
Mvaa 128,128,32
s_mov_b32 s19, 0x20000
Mvaa 128,132,36
Ta 156,24,10464
Ta 158,24,15584
Mvaa 136,132,52
Mvaa 136,128,48
W 8
s_lshr_b32 s66, s15, 3
Ta 128,21,51456
Mvaa 140,136,48
Ta 130,21,56576
Mvaa 140,140,52
Ta 132,23,10496
Ta 134,23,15616
Mvaa 132,140,36
s_lshl_b32 s66, s66, 4
Mvaa 132,136,32
W 8
Ta 136,22,51488
Ta 138,22,56608
Mvaa 144,144,32
Mvaa 144,148,36
s_add_u32 s66, s66, s59
Ta 140,24,10528
Ta 142,24,15648
Mvaa 152,148,52
Mvaa 152,144,48
W 8
s_lshl_b32 s63, s66, 6
Ta 144,23,30720
Mvaa 156,152,48
Ta 146,23,35840
Mvaa 156,156,52
Ta 148,23,40960
Ta 150,23,46080
Mvaa 148,156,36
s_add_u32 s63, s63, 65536
Mvaa 148,152,32
W 8
Ta 152,24,30752
Ta 154,24,35872
Mvaa 160,128,32
Mvaa 160,132,36
s_mul_i32 s62, s66, 40960
Ta 156,24,40992
Ta 158,24,46112
Mvaa 168,132,52
s_waitcnt vmcnt(0)
v_cmp_eq_u64 vcc, s[12:13], v[28:29]
s_cmp_eq_u64 vcc, -1
s_cbranch_scc0 .Lspin_83%=
.Lok_84%=:
s_add_u32 s81, s58, 1
s_and_b32 s81, s81, 3
Mvaa 168,128,48
W 8
s_lshl_b32 s81, s81, 12
s_add_u32 s81, s81, s50
Mvaa 172,136,48
s_add_u32 s62, s62, 56623104
buffer_load_dwordx4 a[64:67], v1, s[8:11], s81 offen offset:0 sc1
Ta 128,23,30784
Mvaa 172,140,52
s_add_u32 s82, s81, 131072
Ta 130,23,35904
Ta 132,23,41024
Mvaa 164,140,36
s_lshl_b32 s66, s14, 10
Ta 134,23,46144
Mvaa 164,136,32
W 8
Ta 136,24,30816
Ta 138,24,35936
Mvaa 96,144,40
buffer_load_dwordx4 a[80:83], v1, s[8:11], s82 offen offset:0 sc1
Mvaa 96,148,44
s_add_u32 s78, s62, s66
Ta 140,24,41056
Ta 142,24,46176
Mvaa 104,148,60
Mvaa 104,144,56
W 8
Ta 144,23,30848
Ta 146,23,35968
Mvaa 108,152,56
s_add_u32 s79, s78, 327680
Mvaa 108,156,60
buffer_load_dwordx4 a[68:71], v1, s[8:11], s81 offen offset:1024 sc1
Ta 148,23,41088
Mvaa 100,156,44
s_and_b32 s66, s14, 1
Ta 150,23,46208
Mvaa 100,152,40
W 8
Ta 152,24,30880
Ta 154,24,36000
Mvaa 112,128,40
Mvaa 112,132,44
s_mul_i32 s66, s66, 327680
Ta 156,24,41120
Ta 158,24,46240
Mvaa 120,132,60
buffer_load_dwordx4 a[84:87], v1, s[8:11], s82 offen offset:1024 sc1
Mvaa 120,128,56
W 8
Ta 128,23,30912
Ta 130,23,36032
Mvaa 124,136,56
s_add_u32 s65, s62, s66
Mvaa 124,140,60
Ta 132,23,41152
Ta 134,23,46272
Mvaa 116,140,44
s_add_u32 s65, s65, 4096
Mvaa 116,136,40
W 8
buffer_load_dwordx4 a[72:75], v1, s[8:11], s81 offen offset:2048 sc1
Ta 136,24,30944
Mvaa 128,144,40
Ta 138,24,36064
Mvaa 128,148,44
s_mov_b32 s64, 0
Ta 140,24,41184
Ta 142,24,46304
Mvaa 136,148,60
Mvaa 136,144,56
W 8
Ta 144,23,30976
Ta 146,23,36096
Mvaa 140,152,56
buffer_load_dwordx4 a[88:91], v1, s[8:11], s82 offen offset:2048 sc1
v_bfe_u32 v27, v1, 4, 3
Mvaa 140,156,60
Ta 148,23,41216
Ta 150,23,46336
Mvaa 132,156,44
v_lshlrev_b32 v27, 3, v27
Mvaa 132,152,40
W 8
Ta 152,24,31008
Ta 154,24,36128
Mvaa 144,128,40
Mvaa 144,132,44
buffer_load_dwordx4 a[76:79], v1, s[8:11], s81 offen offset:3072 sc1
v_bfe_u32 v20, v1, 4, 4
Mvaa 152,132,60
Ta 156,24,41248
Ta 158,24,46368
Mvaa 152,128,56
W 8
Ta 128,25,10240
Ta 130,25,15360
Mvaa 156,136,56
v_mul_u32_u24 v20, 1280, v20
Mvaa 156,140,60
Ta 132,25,20480
Ta 134,25,25600
Mvaa 148,140,44
buffer_load_dwordx4 a[92:95], v1, s[8:11], s82 offen offset:3072 sc1
K 96,96,97
Mvaa 148,136,40
W 8
s_add_u32 s81, s58, 2
s_and_b32 s81, s81, 3
Mvaa 160,144,40
s_lshl_b32 s81, s81, 12
s_add_u32 s81, s81, s50
v_lshrrev_b32 v12, 8, v1
Mvaa 160,148,44
Ta 136,26,10272
Ta 138,26,15392
Mvaa 168,148,60
K 97,98,99
v_lshl_add_u32 v20, v12, 4, v20
Mvaa 168,144,56
W 6
Ta 140,26,20512
Ta 142,26,25632
Mvaa 172,152,56
s_bfe_u32 s66, s59, 0x10001
buffer_load_dwordx4 a[96:99], v1, s[8:11], s81 offen offset:0 sc1
K 98,100,101
Mvaa 172,156,60
s_add_u32 s82, s81, 131072
Ta 144,25,10304
Ta 146,25,15424
Mvaa 164,156,44
Ta 148,25,20544
Ta 150,25,25664
Mvaa 164,152,40
W 8
s_lshl_b32 s67, s57, 1
K 99,102,103
Zvav 176,128,32
s_add_u32 s83, s52, 0
Ta 152,26,10336
Ta 154,26,15456
Mvav 176,132,40
s_add_u32 s66, s66, s67
buffer_load_dwordx4 a[112:115], v1, s[8:11], s82 offen offset:0 sc1
Ta 156,26,20576
Zvav 184,132,72
Ta 158,26,25696
Mvav 184,128,64
W 8
Ta 128,25,10368
Ta 130,25,15488
Zvav 188,136,64
s_lshl_b32 s61, s66, 2
Mvav 188,140,72
buffer_store_dwordx4 v[96:99], v1, s[8:11], s83 offen sc1
K 104,104,105
Zvav 180,140,40
Ta 132,25,20608
Ta 134,25,25728
Mvav 180,136,32
W 8
s_lshr_b32 s66, s59, 2
K 105,106,107
Zvav 192,144,32
Ta 136,26,10400
Ta 138,26,15520
Mvav 192,148,40
buffer_load_dwordx4 a[100:103], v1, s[8:11], s81 offen offset:1024 sc1
K 106,108,109
Zvav 200,148,72
s_lshl_b32 s66, s66, 1
Ta 140,26,20640
Ta 142,26,25760
Mvav 200,144,64
W 8
Ta 144,25,10432
Ta 146,25,15552
Zvav 204,152,64
s_add_u32 s61, s61, s66
K 107,110,111
Mvav 204,156,72
buffer_load_dwordx4 a[116:119], v1, s[8:11], s82 offen offset:1024 sc1
Ta 148,25,20672
Zvav 196,156,40
s_add_u32 s83, s52, 40960
Ta 150,25,25792
Mvav 196,152,32
W 8
s_and_b32 s66, s59, 1
Ta 152,26,10464
Zvav 208,128,32
Ta 154,26,15584
Mvav 208,132,40
buffer_load_dwordx4 a[104:107], v1, s[8:11], s81 offen offset:2048 sc1
Ta 156,26,20704
Zvav 216,132,72
s_add_u32 s61, s61, s66
)"
R"(Ta 158,26,25824
Mvav 216,128,64
W 8
Ta 128,25,10496
Ta 130,25,15616
Zvav 220,136,64
s_xor_b32 s67, s61, 0
Mvav 220,140,72
buffer_store_dwordx4 v[104:107], v1, s[8:11], s83 offen sc1
K 112,112,113
Zvav 212,140,40
Ta 132,25,20736
K 113,114,115
Mvav 212,136,32
W 7
s_mul_i32 s72, s67, 20480
Ta 134,25,25856
Zvav 224,144,32
Ta 136,26,10528
K 114,116,117
Mvav 224,148,40
buffer_load_dwordx4 a[120:123], v1, s[8:11], s82 offen offset:2048 sc1
Ta 138,26,15648
Zvav 232,148,72
s_mul_i32 s66, s58, 320
Ta 140,26,20768
Ta 142,26,25888
Mvav 232,144,64
W 8
Ta 144,25,30720
K 115,118,119
Zvav 236,152,64
s_add_u32 s83, s52, 1024
Ta 146,25,35840
Mvav 236,156,72
s_add_u32 s72, s72, s66
buffer_load_dwordx4 a[108:111], v1, s[8:11], s81 offen offset:3072 sc1
Ta 148,21,0
Zvav 228,156,40
Ta 150,21,5120
Mvav 228,152,32
W 8
s_mul_i32 s75, s67, 20480
Ta 152,26,30752
Zvav 240,128,32
Ta 154,26,35872
Mvav 240,132,40
buffer_store_dwordx4 v[112:115], v1, s[8:11], s83 offen sc1
K 120,120,121
Zvav 248,132,72
s_mul_i32 s66, s58, 2560
Ta 156,22,32
Ta 158,22,5152
Mvav 248,128,64
W 8
Ta 128,25,30784
K 121,122,123
Zvav 252,136,64
Ta 130,25,35904
Mvav 252,140,72
s_add_u32 s75, s75, s66
buffer_load_dwordx4 a[124:127], v1, s[8:11], s82 offen offset:3072 sc1
K 122,124,125
Zvav 244,140,40
s_add_u32 s81, s58, 3
s_and_b32 s81, s81, 3
s_lshl_b32 s81, s81, 12
Mvav 244,136,32
W 6
s_add_u32 s81, s81, s50
Ta 132,21,64
Mvav 176,144,48
s_add_u32 s75, s75, 131072
Ta 134,21,5184
Ta 136,26,30816
Mvav 176,148,56
buffer_load_dwordx4 a[184:187], v1, s[8:11], s81 offen offset:0 sc1
Ta 138,26,35936
Mvav 184,148,88
s_add_u32 s82, s81, 131072
Ta 140,22,96
K 123,126,127
Mvav 184,144,80
W 7
s_add_u32 s83, s52, 41984
Ta 142,22,5216
Mvav 188,152,80
s_xor_b32 s67, s61, 124
Ta 144,25,30848
Ta 146,25,35968
Mvav 188,156,88
s_mul_i32 s73, s67, 20480
buffer_load_dwordx4 a[200:203], v1, s[8:11], s82 offen offset:0 sc1
Ta 148,21,128
Mvav 180,156,56
Ta 150,21,5248
Mvav 180,152,48
W 8
Ta 152,26,30880
Ta 154,26,36000
Mvav 192,128,48
s_mul_i32 s66, s58, 320
Mvav 192,132,56
buffer_store_dwordx4 v[120:123], v1, s[8:11], s83 offen sc1
K 128,128,129
Mvav 200,132,88
s_add_u32 s73, s73, s66
Ta 156,22,160
K 129,130,131
Mvav 200,128,80
W 7
Ta 158,22,5280
K 130,132,133
Mvav 204,136,80
Ta 128,25,30912
Ta 130,25,36032
Mvav 204,140,88
s_mul_i32 s76, s67, 20480
buffer_load_dwordx4 a[188:191], v1, s[8:11], s81 offen offset:1024 sc1
Ta 132,21,192
Mvav 196,140,56
Ta 134,21,5312
K 131,134,135
Mvav 196,136,48
W 8
Ta 136,26,30944
Ta 138,26,36064
Mvav 208,144,48
s_mul_i32 s66, s58, 2560
s_add_u32 s83, s52, 2048
Mvav 208,148,56
buffer_load_dwordx4 a[204:207], v1, s[8:11], s82 offen offset:1024 sc1
Ta 140,22,224
Mvav 216,148,88
s_add_u32 s76, s76, s66
Ta 142,22,5344
Mvav 216,144,80
W 8
Ta 144,25,30976
Ta 146,25,36096
Mvav 220,152,80
Mvav 220,156,88
s_add_u32 s76, s76, 131072
buffer_load_dwordx4 a[192:195], v1, s[8:11], s81 offen offset:2048 sc1
Ta 148,21,256
Mvav 212,156,56
Ta 150,21,5376
Mvav 212,152,48
W 8
Ta 152,26,31008
Ta 154,26,36128
Mvav 224,128,48
v_bfe_u32 v15, v1, 7, 1
Mvav 224,132,56
buffer_store_dwordx4 v[128:131], v1, s[8:11], s83 offen sc1
K 136,136,137
Mvav 232,132,88
K 137,138,139
K 138,140,141
Mvav 232,128,80
W 6
Ta 156,22,288
Ta 158,22,5408
Mvav 236,136,80
Ta 128,21,10240
v_mul_u32_u24 v14, 10240, v15
Mvav 236,140,88
buffer_load_dwordx4 a[208:211], v1, s[8:11], s82 offen offset:2048 sc1
Ta 130,21,15360
Mvav 228,140,56
K 139,142,143
v_lshrrev_b32 v15, 8, v1
Mvav 228,136,48
W 6
Ta 132,21,20480
Ta 134,21,25600
Mvav 240,144,48
s_add_u32 s83, s52, 43008
Ta 136,22,10272
Ta 138,22,15392
Mvav 240,148,56
buffer_load_dwordx4 a[196:199], v1, s[8:11], s81 offen offset:3072 sc1
v_lshl_add_u32 v14, v15, 7, v14
Mvav 248,148,88
Ta 140,22,20512
Ta 142,22,25632
Mvav 248,144,80
W 8
Ta 144,21,10304
v_and_b32 v15, 0x70, v1
Mvav 252,152,80
Ta 146,21,15424
Mvav 252,156,88
buffer_load_dwordx4 a[212:215], v1, s[8:11], s82 offen offset:3072 sc1
v_add_u32 v14, v14, v15
Mvav 244,156,56
Ta 148,21,20544
Ta 150,21,25664
Mvav 244,152,48
s_waitcnt vmcnt(25) lgkmcnt(8)
Ta 152,22,10336
Ta 154,22,15456
Mvaa 176,128,64
s_bitcmp1_b32 s14, 0
s_cselect_b32 s74, s73, s72
s_cselect_b32 s77, s76, s75
Mvaa 176,132,68
buffer_store_dwordx4 v[136:139], v1, s[8:11], s83 offen sc1
K 144,144,145
Mvaa 184,132,84
K 145,146,147
K 146,148,149
Mvaa 184,128,80
W 6
Ta 156,22,20576
Ta 158,22,25696
Mvaa 188,136,80
s_add_u32 s74, s74, 256
Ta 128,21,10368
K 147,150,151
Mvaa 188,140,84
s_add_u32 s83, s52, 3072
Ta 130,21,15488
Ta 132,21,20608
Mvaa 180,140,68
s_add_u32 s77, s77, 2048
Ta 134,21,25728
buffer_store_dwordx4 v[144:147], v1, s[8:11], s83 offen sc1
Mvaa 180,136,64
W 8
Ta 136,22,10400
Ta 138,22,15520
Mvaa 192,144,64
s_lshl_b32 s66, s14, 6
K 152,152,153
Mvaa 192,148,68
Ta 140,22,20640
Ta 142,22,25760
Mvaa 200,148,84
Mvaa 200,144,80
W 8
s_lshl_b32 s67, s14, 9
K 153,154,155
Mvaa 204,152,80
Ta 144,21,10432
Ta 146,21,15552
Mvaa 204,156,84
Ta 148,21,20672
K 154,156,157
Mvaa 196,156,68
s_add_u32 s72, s72, s66
Ta 150,21,25792
Mvaa 196,152,64
W 8
Ta 152,22,10464
Ta 154,22,15584
Mvaa 208,128,64
s_add_u32 s75, s75, s67
K 155,158,159
Mvaa 208,132,68
Ta 156,22,20704
Ta 158,22,25824
Mvaa 216,132,84
s_add_u32 s83, s52, 44032
Mvaa 216,128,80
W 8
s_add_u32 s73, s73, s66
Ta 128,21,10496
Mvaa 220,136,80
Ta 130,21,15616
buffer_store_dwordx4 v[152:155], v1, s[8:11], s83 offen sc1
Mvaa 220,140,84
Ta 132,21,20736
Ta 134,21,25856
Mvaa 212,140,68
s_add_u32 s76, s76, s67
K 160,160,161
Mvaa 212,136,64
W 8
Ta 136,22,10528
Ta 138,22,15648
Mvaa 224,144,64
buffer_load_dwordx4 v[36:39], v20, s[16:19], s72 offen
Mvaa 224,148,68
Ta 140,22,20768
K 161,162,163
Mvaa 232,148,84
Ta 142,22,25888
Mvaa 232,144,80
W 8
Ta 144,21,30720
K 162,164,165
Mvaa 236,152,80
Ta 146,21,35840
Mvaa 236,156,84
Ta 148,23,0
Ta 150,23,5120
Mvaa 228,156,68
K 163,166,167
Mvaa 228,152,64
W 8
buffer_load_dwordx4 v[44:47], v14, s[8:11], s75 offen sc1
Ta 152,22,30752
Mvaa 240,128,64
s_add_u32 s83, s52, 4096
Ta 154,22,35872
Mvaa 240,132,68
Ta 156,24,32
Ta 158,24,5152
Mvaa 248,132,84
Mvaa 248,128,80
W 8
buffer_store_dwordx4 v[160:163], v1, s[8:11], s83 offen sc1
Ta 128,21,30784
Mvaa 252,136,80
Ta 130,21,35904
K 168,168,169
Mvaa 252,140,84
Ta 132,23,64
Ta 134,23,5184
Mvaa 244,140,68
Mvaa 244,136,64
s_waitcnt vmcnt(27) lgkmcnt(8)
buffer_load_dwordx4 v[52:55], v20, s[16:19], s73 offen
K 169,170,171
Mvaa 176,144,72
Ta 136,22,30816
Ta 138,22,35936
Mvaa 176,148,76
Ta 140,24,96
K 170,172,173
Mvaa 184,148,92
Ta 142,24,5216
Mvaa 184,144,88
W 8
Ta 144,21,30848
Ta 146,21,35968
Mvaa 188,152,88
buffer_load_dwordx4 v[60:63], v14, s[8:11], s76 offen sc1
K 171,174,175
Mvaa 188,156,92
Ta 148,23,128
Ta 150,23,5248
Mvaa 180,156,76
s_add_u32 s83, s52, 45056
Mvaa 180,152,72
W 8
Ta 152,22,30880
Ta 154,22,36000
Mvaa 192,128,72
buffer_store_dwordx4 v[168:171], v1, s[8:11], s83 offen sc1
Mvaa 192,132,76
Ta 156,24,160
Ta 158,24,5280
Mvaa 200,132,92
Mvaa 200,128,88
W 8
Ta 128,21,30912
Ta 130,21,36032
Mvaa 204,136,88
buffer_load_dwordx4 v[68:71], v20, s[16:19], s74 offen
Mvaa 204,140,92
Ta 132,23,192
Ta 134,23,5312
Mvaa 196,140,76
Mvaa 196,136,72
W 8
Ta 136,22,30944
Ta 138,22,36064
Mvaa 208,144,72
Mvaa 208,148,76
Ta 140,24,224
Ta 142,24,5344
Mvaa 216,148,92
buffer_load_dwordx4 v[76:79], v14, s[8:11], s77 offen sc1
Mvaa 216,144,88
W 8
Ta 144,21,30976
Ta 146,21,36096
Mvaa 220,152,88
Mvaa 220,156,92
Ta 148,23,256
Ta 150,23,5376
Mvaa 212,156,76
Mvaa 212,152,72
W 8
Ta 152,22,31008
Ta 154,22,36128
Mvaa 224,128,72
buffer_load_dwordx2 v[12:13], v27, s[8:11], 0 offen offset:3584 sc1
Mvaa 224,132,76
Ta 156,24,288
Ta 158,24,5408
Mvaa 232,132,92
Mvaa 232,128,88
W 8
Ta 128,23,20480
Ta 130,23,25600
Mvaa 236,136,88
Mvaa 236,140,92
)"
R"(Ta 132,23,51200
Ta 134,23,56320
Mvaa 228,140,76
Mvaa 228,136,72
W 8
Ta 136,25,0
Ta 138,25,5120
Mvaa 240,144,72
Mvaa 240,148,76
Ta 140,21,40960
Ta 142,21,46080
Mvaa 248,148,92
Mvaa 248,144,88
W 8
Ta 144,21,51200
Ta 146,21,56320
Mvaa 252,152,88
Mvaa 252,156,92
Ta 148,23,10240
Ta 150,23,15360
Mvaa 244,156,76
Mvaa 244,152,72
s_waitcnt vmcnt(28) lgkmcnt(8)
Ta 152,23,30720
Ta 154,23,35840
Mvaa 176,128,96
Mvaa 176,132,100
Ta 156,23,40960
Ta 158,23,46080
Mvaa 184,132,116
Mvaa 184,128,112
s_waitcnt vmcnt(22) lgkmcnt(8)
Ta 128,24,20512
Ta 130,24,25632
Mvaa 184,136,120
Mvaa 184,140,124
Ta 132,24,51232
Ta 134,24,56352
Mvaa 176,140,108
Mvaa 176,136,104
s_waitcnt vmcnt(17) lgkmcnt(8)
Ta 136,26,32
Ta 138,26,5152
Mvaa 176,144,184
Mvaa 176,148,188
Ta 140,22,40992
Ta 142,22,46112
Mvaa 184,148,204
Mvaa 184,144,200
s_waitcnt vmcnt(12) lgkmcnt(8)
Ta 144,22,51232
Ta 146,22,56352
Mvaa 184,152,208
Mvaa 184,156,212
Ta 148,24,10272
Ta 150,24,15392
Mvaa 176,156,196
Mvaa 176,152,192
W 8
Ta 152,24,30752
Ta 154,24,35872
Mvaa 180,128,96
Mvaa 180,132,100
Ta 156,24,40992
Ta 158,24,46112
Mvaa 188,132,116
Mvaa 188,128,112
W 8
Ta 128,23,20544
Ta 130,23,25664
Mvaa 188,136,120
Mvaa 188,140,124
Ta 132,23,51264
Ta 134,23,56384
Mvaa 180,140,108
Mvaa 180,136,104
W 8
Ta 136,25,64
Ta 138,25,5184
Mvaa 180,144,184
Mvaa 180,148,188
Ta 140,21,41024
Ta 142,21,46144
Mvaa 188,148,204
Mvaa 188,144,200
W 8
Ta 144,21,51264
Ta 146,21,56384
Mvaa 188,152,208
Mvaa 188,156,212
Ta 148,23,10304
Ta 150,23,15424
Mvaa 180,156,196
Mvaa 180,152,192
W 8
Ta 152,23,30784
Ta 154,23,35904
Mvaa 192,128,96
Mvaa 192,132,100
Ta 156,23,41024
Ta 158,23,46144
Mvaa 200,132,116
K 176,176,177
Mvaa 200,128,112
W 8
Ta 128,24,20576
Ta 130,24,25696
Mvaa 200,136,120
K 177,178,179
Mvaa 200,140,124
Ta 132,24,51296
K 178,180,181
Mvaa 192,140,108
Ta 134,24,56416
K 179,182,183
Mvaa 192,136,104
W 8
Ta 136,26,96
Ta 138,26,5216
Mvaa 192,144,184
s_add_u32 s83, s52, 327680
Mvaa 192,148,188
buffer_store_dwordx4 v[176:179], v1, s[8:11], s83 offen sc1
Ta 140,22,41056
Mvaa 200,148,204
Ta 142,22,46176
Mvaa 200,144,200
s_waitcnt vmcnt(4)
v_lshlrev_b32 v2, 2, v1
v_mov_b64 v[28:29], s[12:13]
s_mov_b64 exec, 3
buffer_store_dwordx2 v[28:29], v2, s[8:11], s53 offen offset:0 sc1
s_mov_b64 exec, -1
K 184,184,185
W 8
Mvaa 200,152,208
K 185,186,187
K 186,188,189
Mvaa 200,156,212
s_waitcnt vmcnt(2)
v_xor_b32 v12, s12, v12
v_xor_b32 v13, s13, v13
v_lshrrev_b32 v3, 11, v12
v_or_b32 v13, v13, v3
v_cmp_eq_u32 vcc, 0, v13
s_cmp_eq_u64 vcc, -1
s_cbranch_scc0 .LL_88%=
.LL_89%=:
v_bfe_u32 v13, v12, 4, 7
s_xor_b32 s66, s61, 124
v_cmp_eq_u32 vcc, s61, v13
v_cmp_eq_u32_e64 s[86:87], s66, v13
s_or_b32 s68, vcc_lo, s86
s_and_b32 s68, s68, 0xff
s_cbranch_scc1 .Lup_86%=
.Lupr_87%=:
Ta 144,22,51296
K 187,190,191
Mvaa 192,156,196
s_add_u32 s83, s52, 368640
Ta 146,22,56416
Ta 148,24,10336
Mvaa 192,152,192
W 7
buffer_store_dwordx4 v[184:187], v1, s[8:11], s83 offen sc1
Ta 150,24,15456
Mvaa 196,128,96
Ta 152,24,30816
v_lshlrev_b32 v32, 16, v36
Mvaa 196,132,100
Ta 154,24,35936
v_and_b32 v33, 0xffff0000, v36
Mvaa 204,132,116
Ta 156,24,41056
v_lshlrev_b32 v34, 16, v37
Mvaa 204,128,112
W 7
Ta 158,24,46176
v_and_b32 v35, 0xffff0000, v37
Mvaa 204,136,120
Ta 128,23,20608
v_lshlrev_b32 v36, 16, v38
Mvaa 204,140,124
Ta 130,23,25728
v_and_b32 v37, 0xffff0000, v38
Mvaa 196,140,108
Ta 132,23,51328
v_lshlrev_b32 v38, 16, v39
Mvaa 196,136,104
W 7
Ta 134,23,56448
v_and_b32 v39, 0xffff0000, v39
Mvaa 196,144,184
Ta 136,25,128
v_lshlrev_b32 v40, 16, v44
Mvaa 196,148,188
Ta 138,25,5248
v_and_b32 v41, 0xffff0000, v44
Mvaa 204,148,204
Ta 140,21,41088
v_lshlrev_b32 v42, 16, v45
Mvaa 204,144,200
W 7
Ta 142,21,46208
v_and_b32 v43, 0xffff0000, v45
Mvaa 204,152,208
Ta 144,21,51328
v_lshlrev_b32 v44, 16, v46
Mvaa 204,156,212
Ta 146,21,56448
v_and_b32 v45, 0xffff0000, v46
Mvaa 196,156,196
Ta 148,23,10368
v_lshlrev_b32 v46, 16, v47
Mvaa 196,152,192
W 7
Ta 150,23,15488
v_and_b32 v47, 0xffff0000, v47
Mvaa 208,128,96
Ta 152,23,30848
v_lshlrev_b32 v48, 16, v52
Mvaa 208,132,100
Ta 154,23,35968
v_and_b32 v49, 0xffff0000, v52
Mvaa 216,132,116
Ta 156,23,41088
v_lshlrev_b32 v50, 16, v53
Mvaa 216,128,112
W 7
Ta 158,23,46208
v_and_b32 v51, 0xffff0000, v53
Mvaa 216,136,120
K 192,192,193
v_lshlrev_b32 v52, 16, v54
Mvaa 216,140,124
Ta 128,24,20640
Ta 130,24,25760
Mvaa 208,140,108
K 193,194,195
v_and_b32 v53, 0xffff0000, v54
Mvaa 208,136,104
W 6
v_lshlrev_b32 v54, 16, v55
K 194,196,197
Mvaa 208,144,184
Ta 132,24,51360
Ta 134,24,56480
Mvaa 208,148,188
v_and_b32 v55, 0xffff0000, v55
K 195,198,199
Mvaa 216,148,204
s_add_u32 s83, s52, 328704
Ta 136,26,160
v_lshlrev_b32 v56, 16, v60
Mvaa 216,144,200
W 5
Ta 138,26,5280
v_and_b32 v57, 0xffff0000, v60
Mvaa 216,152,208
buffer_store_dwordx4 v[192:195], v1, s[8:11], s83 offen sc1
v_lshlrev_b32 v58, 16, v61
Mvaa 216,156,212
K 200,200,201
K 201,202,203
Mvaa 208,156,196
K 202,204,205
K 203,206,207
Mvaa 208,152,192
Ta 140,22,41120
Ta 142,22,46240
W 4
Mvaa 212,128,96
s_add_u32 s83, s52, 369664
buffer_store_dwordx4 v[200:203], v1, s[8:11], s83 offen sc1
v_and_b32 v59, 0xffff0000, v61
Mvaa 212,132,100
Ta 144,22,51360
v_lshlrev_b32 v60, 16, v62
Mvaa 220,132,116
Ta 146,22,56480
v_and_b32 v61, 0xffff0000, v62
Mvaa 220,128,112
Ta 148,24,10400
Ta 150,24,15520
W 4
Mvaa 220,136,120
v_lshlrev_b32 v62, 16, v63
v_and_b32 v63, 0xffff0000, v63
Mvaa 220,140,124
Ta 152,24,30880
v_lshlrev_b32 v64, 16, v68
Mvaa 212,140,108
Ta 154,24,36000
v_and_b32 v65, 0xffff0000, v68
Mvaa 212,136,104
Ta 156,24,41120
Ta 158,24,46240
W 4
Mvaa 212,144,184
v_lshlrev_b32 v66, 16, v69
v_and_b32 v67, 0xffff0000, v69
Mvaa 212,148,188
Ta 128,23,20672
v_lshlrev_b32 v68, 16, v70
Mvaa 220,148,204
Ta 130,23,25792
v_and_b32 v69, 0xffff0000, v70
Mvaa 220,144,200
Ta 132,23,51392
Ta 134,23,56512
W 4
Mvaa 220,152,208
v_lshlrev_b32 v70, 16, v71
v_and_b32 v71, 0xffff0000, v71
Mvaa 220,156,212
Ta 136,25,192
v_lshlrev_b32 v72, 16, v76
Mvaa 212,156,196
Ta 138,25,5312
v_and_b32 v73, 0xffff0000, v76
Mvaa 212,152,192
Ta 140,21,41152
Ta 142,21,46272
W 4
Mvaa 224,128,96
v_lshlrev_b32 v74, 16, v77
v_and_b32 v75, 0xffff0000, v77
Mvaa 224,132,100
Ta 144,21,51392
v_lshlrev_b32 v76, 16, v78
Mvaa 232,132,116
Ta 146,21,56512
v_and_b32 v77, 0xffff0000, v78
Mvaa 232,128,112
Ta 148,23,10432
Ta 150,23,15552
W 4
Mvaa 232,136,120
v_lshlrev_b32 v78, 16, v79
v_and_b32 v79, 0xffff0000, v79
Mvaa 232,140,124
Ta 152,23,30912
Ta 154,23,36032
Mvaa 224,140,108
Ta 156,23,41152
Ta 158,23,46272
Mvaa 224,136,104
W 4
Ta 128,24,20704
K 208,208,209
Mvaa 224,144,184
Ta 130,24,25824
K 209,210,211
Mvaa 224,148,188
Ta 132,24,51424
K 210,212,213
Mvaa 232,148,204
Ta 134,24,56544
K 211,214,215
Mvaa 232,144,200
W 4
Ta 136,26,224
Ta 138,26,5344
Mvaa 232,152,208
s_add_u32 s83, s52, 329728
buffer_store_dwordx4 v[208:211], v1, s[8:11], s83 offen sc1
K 216,216,217
Mvaa 232,156,212
K 217,218,219
K 218,220,221
Mvaa 224,156,196
Ta 140,22,41184
K 219,222,223
Mvaa 224,152,192
s_add_u32 s83, s52, 370688
Ta 142,22,46304
W 4
Mvaa 228,128,96
buffer_store_dwordx4 v[216:219], v1, s[8:11], s83 offen sc1
Ta 144,22,51424
Mvaa 228,132,100
Ta 146,22,56544
Ta 148,24,10464
Mvaa 236,132,116
Ta 150,24,15584
Ta 152,24,30944
Mvaa 236,128,112
W 5
Ta 154,24,36064
Ta 156,24,41184
Mvaa 236,136,120
Ta 158,24,46304
Ta 128,23,20736
Mvaa 236,140,124
Ta 130,23,25856
Ta 132,23,51456
Mvaa 228,140,108
Ta 134,23,56576
Mvaa 228,136,104
W 8
)"
R"(Ta 136,25,256
Ta 138,25,5376
Mvaa 228,144,184
Mvaa 228,148,188
Ta 140,21,41216
Ta 142,21,46336
Mvaa 236,148,204
Mvaa 236,144,200
W 8
Ta 144,21,51456
Ta 146,21,56576
Mvaa 236,152,208
Mvaa 236,156,212
Ta 148,23,10496
Ta 150,23,15616
Mvaa 228,156,196
Mvaa 228,152,192
W 8
Ta 152,23,30976
Ta 154,23,36096
Mvaa 240,128,96
Mvaa 240,132,100
Ta 156,23,41216
Ta 158,23,46336
Mvaa 248,132,116
K 224,224,225
Mvaa 248,128,112
W 8
Ta 128,24,20768
Ta 130,24,25888
Mvaa 248,136,120
K 225,226,227
Mvaa 248,140,124
Ta 132,24,51488
K 226,228,229
Mvaa 240,140,108
Ta 134,24,56608
K 227,230,231
Mvaa 240,136,104
W 8
Ta 136,26,288
Ta 138,26,5408
Mvaa 240,144,184
s_add_u32 s83, s52, 330752
Mvaa 240,148,188
buffer_store_dwordx4 v[224:227], v1, s[8:11], s83 offen sc1
K 232,232,233
Mvaa 248,148,204
K 233,234,235
K 234,236,237
Mvaa 248,144,200
W 6
Ta 140,22,41248
K 235,238,239
Mvaa 248,152,208
s_add_u32 s83, s52, 371712
Ta 142,22,46368
Ta 144,22,51488
Mvaa 248,156,212
buffer_store_dwordx4 v[232:235], v1, s[8:11], s83 offen sc1
Ta 146,22,56608
Mvaa 240,156,196
Ta 148,24,10528
Ta 150,24,15648
Mvaa 240,152,192
W 8
Ta 152,24,31008
Ta 154,24,36128
Mvaa 244,128,96
Mvaa 244,132,100
Ta 156,24,41248
Ta 158,24,46368
Mvaa 252,132,116
Mvaa 252,128,112
W 8
Mvaa 252,136,120
Mvaa 252,140,124
Mvaa 244,140,108
Mvaa 244,136,104
W 4
Mvaa 244,144,184
Mvaa 244,148,188
Mvaa 252,148,204
Mvaa 252,144,200
W 0
Mvaa 252,152,208
Mvaa 252,156,212
Mvaa 244,156,196
Mvaa 244,152,192
K 240,240,241
K 241,242,243
s_nop 5
K 242,244,245
K 243,246,247
s_add_u32 s83, s52, 331776
buffer_store_dwordx4 v[240:243], v1, s[8:11], s83 offen sc1
K 248,248,249
K 249,250,251
K 250,252,253
K 251,254,255
s_add_u32 s83, s52, 372736
buffer_store_dwordx4 v[248:251], v1, s[8:11], s83 offen sc1
s_branch .Ltend_80%=
.Llate_79%=:
ds_read_b32 v11, v11 offset:59392
ds_read_b64 v[8:9], v10 offset:57344
Rv 176,3,0
Rv 180,3,1024
Rv 184,3,2048
Rv 188,3,3072
Rv 192,3,4096
Rv 196,3,5120
Rv 200,3,6144
Rv 204,3,7168
W 6
Mvvv 32,224,176
s_lshl_b32 s92, s58, 2
s_add_u32 s91, s92, s51
s_add_u32 s95, s91, 7
Mvvv 36,228,176
s_and_b32 s95, s95, 15
s_mul_i32 s95, s95, 40960
s_mul_i32 s71, s51, 20480
Mvvv 40,224,180
s_add_u32 s71, s71, 61440
s_add_u32 m0, s71, s48
s_lshl2_add_u32 s94, s48, s95
Mvvv 44,228,180
W 4
v_cmp_ne_u32 vcc, 0, v11
s_cmp_lg_u64 vcc, 0
s_cbranch_scc1 .Lredo%=
A 4,4,8
Mvvv 48,224,184
v_rcp_f32 v4, v4
Mvvv 52,228,184
Ta 128,25,10240
A 5,5,9
Mvvv 56,224,188
Ta 130,25,15360
D 16,40,94,0
Mvvv 60,228,188
W 4
s_add_u32 s95, s91, 9
s_and_b32 s95, s95, 15
Mvvv 64,224,192
s_mul_i32 s95, s95, 40960
s_mul_i32 s71, s51, 10240
s_add_u32 s71, s71, 112640
Mvvv 68,228,192
v_rcp_f32 v5, v5
Mvvv 72,224,196
s_lshl_b32 s93, s58, 12
s_add_u32 s93, s93, s50
Ta 132,25,20480
Mvvv 76,228,196
Ta 134,25,25600
W 4
Mvvv 80,224,200
U 32,32,4
U 33,33,4
Mvvv 84,228,200
Ta 136,26,10272
U 34,34,4
Mvvv 88,224,204
U 35,35,4
U 36,36,4
U 37,37,4
U 38,38,4
U 39,39,4
K 32,32,33
K 33,34,35
K 34,36,37
K 35,38,39
buffer_store_dwordx4 v[32:35], v1, s[8:11], s93 offen offset:0 sc1
U 40,40,4
U 41,41,4
U 42,42,4
U 43,43,4
U 44,44,4
U 45,45,4
U 46,46,4
U 47,47,4
K 40,40,41
K 41,42,43
K 42,44,45
K 43,46,47
buffer_store_dwordx4 v[40:43], v1, s[8:11], s93 offen offset:1024 sc1
U 64,64,5
U 65,65,5
U 66,66,5
U 67,67,5
U 68,68,5
U 69,69,5
U 70,70,5
U 71,71,5
K 64,64,65
K 65,66,67
K 66,68,69
K 67,70,71
s_add_u32 s67, s93, 131072
buffer_store_dwordx4 v[64:67], v1, s[8:11], s67 offen offset:0 sc1
U 72,72,5
U 73,73,5
U 74,74,5
U 75,75,5
U 76,76,5
U 77,77,5
U 78,78,5
U 79,79,5
K 72,72,73
K 73,74,75
K 74,76,77
K 75,78,79
Mvvv 92,228,204
Ta 138,26,15392
Ta 140,26,20512
Ta 142,26,25632
W 4
Zvav 176,128,32
Ta 144,25,10304
Ta 146,25,15424
Mvav 176,132,40
Ta 148,25,20544
Ta 150,25,25664
Zvav 184,132,72
D 17,40,94,1024
Ta 152,26,10336
Mvav 184,128,64
W 5
Ta 154,26,15456
Ta 156,26,20576
Zvav 188,136,64
Ta 158,26,25696
Ta 128,25,10368
Mvav 188,140,72
Ta 130,25,15488
Ta 132,25,20608
Zvav 180,140,40
D 18,40,94,2048
Ta 134,25,25728
Mvav 180,136,32
W 8
Ta 136,26,10400
Ta 138,26,15520
Zvav 192,144,32
Mvav 192,148,40
Ta 140,26,20640
Ta 142,26,25760
Zvav 200,148,72
D 19,40,94,3072
Mvav 200,144,64
W 8
s_add_u32 m0, m0, 4096
Ta 144,25,10432
Zvav 204,152,64
Ta 146,25,15552
Mvav 204,156,72
Ta 148,25,20672
Ta 150,25,25792
Zvav 196,156,40
buffer_load_dwordx4 v20, s[40:43], s94 offen lds
Mvav 196,152,32
W 8
s_add_u32 m0, s71, s48
s_lshl2_add_u32 s94, s48, s95
Zvav 208,128,32
Ta 152,26,10464
Ta 154,26,15584
Mvav 208,132,40
Ta 156,26,20704
Ta 158,26,25824
Zvav 216,132,72
Mvav 216,128,64
W 8
D 16,40,94,0
Ta 128,25,10496
Zvav 220,136,64
Ta 130,25,15616
Mvav 220,140,72
Ta 132,25,20736
Ta 134,25,25856
Zvav 212,140,40
Mvav 212,136,32
W 8
buffer_store_dwordx4 v[72:75], v1, s[8:11], s67 offen offset:1024 sc1
U 48,48,4
Zvav 224,144,32
Ta 136,26,10528
U 49,49,4
Mvav 224,148,40
Ta 138,26,15648
U 50,50,4
Zvav 232,148,72
U 51,51,4
U 52,52,4
Mvav 232,144,64
W 6
Ta 140,26,20768
Ta 142,26,25888
Zvav 236,152,64
s_waitcnt vmcnt(10)
s_cmp_lg_u32 s70, 0
s_cbranch_scc1 .Lpd_91%=
.Lpdr_92%=:
D 17,40,94,1024
U 53,53,4
Mvav 236,156,72
U 54,54,4
U 55,55,4
Zvav 228,156,40
Ta 144,25,30720
K 48,48,49
Mvav 228,152,32
W 5
Ta 146,25,35840
K 49,50,51
Zvav 240,128,32
K 50,52,53
K 51,54,55
Mvav 240,132,40
Ta 148,21,0
Ta 150,21,5120
Zvav 248,132,72
buffer_store_dwordx4 v[48:51], v1, s[8:11], s93 offen offset:2048 sc1
U 56,56,4
Mvav 248,128,64
W 4
Ta 152,26,30752
U 57,57,4
Zvav 252,136,64
Ta 154,26,35872
U 58,58,4
Mvav 252,140,72
U 59,59,4
U 60,60,4
Zvav 244,140,40
U 61,61,4
U 62,62,4
U 63,63,4
K 56,56,57
K 57,58,59
K 58,60,61
K 59,62,63
buffer_store_dwordx4 v[56:59], v1, s[8:11], s93 offen offset:3072 sc1
U 80,80,5
U 81,81,5
U 82,82,5
U 83,83,5
U 84,84,5
U 85,85,5
U 86,86,5
U 87,87,5
K 80,80,81
K 81,82,83
K 82,84,85
K 83,86,87
buffer_store_dwordx4 v[80:83], v1, s[8:11], s67 offen offset:2048 sc1
U 88,88,5
U 89,89,5
U 90,90,5
U 91,91,5
U 92,92,5
U 93,93,5
U 94,94,5
U 95,95,5
K 88,88,89
K 89,90,91
K 90,92,93
K 91,94,95
Mvav 244,136,32
Ta 156,22,32
Ta 158,22,5152
W 4
Mvav 176,144,48
Ta 128,25,30784
Ta 130,25,35904
Mvav 176,148,56
Ta 132,21,64
Ta 134,21,5184
Mvav 184,148,88
D 18,40,94,2048
Ta 136,26,30816
Mvav 184,144,80
W 5
Ta 138,26,35936
Ta 140,22,96
Mvav 188,152,80
Ta 142,22,5216
Ta 144,25,30848
Mvav 188,156,88
Ta 146,25,35968
Ta 148,21,128
Mvav 180,156,56
D 19,40,94,3072
Ta 150,21,5248
Mvav 180,152,48
W 8
s_add_u32 m0, m0, 4096
Ta 152,26,30880
)"
R"(Mvav 192,128,48
Ta 154,26,36000
Mvav 192,132,56
Ta 156,22,160
Ta 158,22,5280
Mvav 200,132,88
buffer_load_dwordx4 v20, s[40:43], s94 offen lds
v_bfe_u32 v28, v1, 4, 2
Mvav 200,128,80
W 8
Ta 128,25,30912
v_lshlrev_b32 v28, 3, v28
Mvav 204,136,80
Ta 130,25,36032
Mvav 204,140,88
Ta 132,21,192
Ta 134,21,5312
Mvav 196,140,56
buffer_load_dwordx2 v[28:29], v28, s[8:11], s54 offen sc1
Mvav 196,136,48
W 8
s_waitcnt vmcnt(0)
v_cmp_eq_u64 vcc, s[12:13], v[28:29]
s_cmp_eq_u64 vcc, -1
s_cbranch_scc0 .Lspin_93%=
.Lok_94%=:
s_add_u32 s81, s58, 0
Mvav 208,144,48
s_and_b32 s81, s81, 3
s_lshl_b32 s81, s81, 12
s_add_u32 s81, s81, s49
Mvav 208,148,56
Ta 136,26,30944
Ta 138,26,36064
Mvav 216,148,88
buffer_store_dwordx4 v[88:91], v1, s[8:11], s67 offen offset:3072 sc1
Ta 140,22,224
Mvav 216,144,80
W 7
Ta 142,22,5344
Ta 144,25,30976
Mvav 220,152,80
Ta 146,25,36096
Mvav 220,156,88
Ta 148,21,256
Ta 150,21,5376
Mvav 212,156,56
buffer_load_dwordx4 a[184:187], v1, s[8:11], s81 offen offset:0 sc1
Mvav 212,152,48
W 8
s_add_u32 s82, s81, 131072
Ta 152,26,31008
Mvav 224,128,48
Ta 154,26,36128
Mvav 224,132,56
Ta 156,22,288
Ta 158,22,5408
Mvav 232,132,88
buffer_load_dwordx4 a[200:203], v1, s[8:11], s82 offen offset:0 sc1
Mvav 232,128,80
W 8
Ta 128,25,10240
Ta 130,25,15360
Mvav 236,136,80
Mvav 236,140,88
Ta 132,25,20480
Ta 134,25,25600
Mvav 228,140,56
buffer_load_dwordx4 a[188:191], v1, s[8:11], s81 offen offset:1024 sc1
Mvav 228,136,48
W 8
Ta 136,26,10272
Ta 138,26,15392
Mvav 240,144,48
Mvav 240,148,56
Ta 140,26,20512
Ta 142,26,25632
Mvav 248,148,88
buffer_load_dwordx4 a[204:207], v1, s[8:11], s82 offen offset:1024 sc1
Mvav 248,144,80
W 8
Ta 144,25,10304
Ta 146,25,15424
Mvav 252,152,80
Mvav 252,156,88
Ta 148,25,20544
Ta 150,25,25664
Mvav 244,156,56
buffer_load_dwordx4 a[192:195], v1, s[8:11], s81 offen offset:2048 sc1
Mvav 244,152,48
s_waitcnt vmcnt(1) lgkmcnt(8)
Ta 152,26,10336
Ta 154,26,15456
Zvaa 96,128,184
Mvaa 96,132,188
Ta 156,26,20576
Ta 158,26,25696
Zvaa 104,132,204
buffer_load_dwordx4 a[208:211], v1, s[8:11], s82 offen offset:2048 sc1
Mvaa 104,128,200
W 8
Ta 128,25,10368
Ta 130,25,15488
Zvaa 108,136,200
Mvaa 108,140,204
Ta 132,25,20608
Ta 134,25,25728
Zvaa 100,140,188
buffer_load_dwordx4 a[196:199], v1, s[8:11], s81 offen offset:3072 sc1
Mvaa 100,136,184
W 8
Ta 136,26,10400
Ta 138,26,15520
Zvaa 112,144,184
Mvaa 112,148,188
Ta 140,26,20640
Ta 142,26,25760
Zvaa 120,148,204
buffer_load_dwordx4 a[212:215], v1, s[8:11], s82 offen offset:3072 sc1
Mvaa 120,144,200
W 8
s_lshl_b32 s92, s58, 2
s_add_u32 s91, s92, s51
Zvaa 124,152,200
s_add_u32 s95, s91, 11
s_and_b32 s95, s95, 15
s_mul_i32 s95, s95, 40960
Mvaa 124,156,204
s_mul_i32 s71, s51, 10240
s_add_u32 s71, s71, 40960
s_add_u32 m0, s71, s48
Zvaa 116,156,188
s_lshl2_add_u32 s94, s48, s95
Ta 144,25,10432
Ta 146,25,15552
Mvaa 116,152,184
W 6
Ta 148,25,20672
Ta 150,25,25792
Zvaa 128,128,184
s_lshl_b32 s80, s58, 3
Ta 152,26,10464
Ta 154,26,15584
Mvaa 128,132,188
s_add_u32 s80, s80, s55
v_mov_b64 v[28:29], s[12:13]
s_mov_b64 exec, 1
buffer_store_dwordx2 v[28:29], off, s[8:11], s80 sc1
s_mov_b64 exec, -1
Ta 156,26,20704
Zvaa 136,132,204
Ta 158,26,25824
Mvaa 136,128,200
W 8
Ta 128,25,10496
Ta 130,25,15616
Zvaa 140,136,200
s_barrier
Mvaa 140,140,204
D 16,40,94,0
Ta 132,25,20736
Zvaa 132,140,188
s_add_u32 s95, s91, 13
s_and_b32 s95, s95, 15
s_mul_i32 s95, s95, 40960
Mvaa 132,136,184
W 7
s_mul_i32 s71, s51, 20480
s_add_u32 s71, s71, 71680
Zvaa 144,144,184
Ta 134,25,25856
Ta 136,26,10528
Mvaa 144,148,188
Ta 138,26,15648
D 17,40,94,1024
Zvaa 152,148,204
Ta 140,26,20768
Ta 142,26,25888
Mvaa 152,144,200
W 8
Ta 144,25,30720
Ta 146,25,35840
Zvaa 156,152,200
Mvaa 156,156,204
D 18,40,94,2048
Ta 148,21,0
Zvaa 148,156,188
Ta 150,21,5120
Mvaa 148,152,184
W 8
Ta 152,26,30752
Ta 154,26,35872
Zvaa 160,128,184
Mvaa 160,132,188
D 19,40,94,3072
Ta 156,22,32
Zvaa 168,132,204
s_add_u32 m0, m0, 4096
Ta 158,22,5152
Mvaa 168,128,200
W 8
Ta 128,25,30784
Ta 130,25,35904
Zvaa 172,136,200
Mvaa 172,140,204
buffer_load_dwordx4 v20, s[40:43], s94 offen lds
Ta 132,21,64
Zvaa 164,140,188
s_add_u32 s81, s58, 1
s_and_b32 s81, s81, 3
s_lshl_b32 s81, s81, 12
Mvaa 164,136,184
s_waitcnt vmcnt(6) lgkmcnt(7)
s_add_u32 s81, s81, s49
Ta 134,21,5184
Mvaa 96,144,192
Ta 136,26,30816
Ta 138,26,35936
Mvaa 96,148,196
buffer_load_dwordx4 a[216:219], v1, s[8:11], s81 offen offset:0 sc1
Ta 140,22,96
Mvaa 104,148,212
s_add_u32 s82, s81, 131072
Ta 142,22,5216
Mvaa 104,144,208
W 8
Ta 144,25,30848
Ta 146,25,35968
Mvaa 108,152,208
Mvaa 108,156,212
buffer_load_dwordx4 a[232:235], v1, s[8:11], s82 offen offset:0 sc1
Ta 148,21,128
Mvaa 100,156,196
Ta 150,21,5248
Mvaa 100,152,192
W 8
Ta 152,26,30880
Ta 154,26,36000
Mvaa 112,128,192
Mvaa 112,132,196
buffer_load_dwordx4 a[220:223], v1, s[8:11], s81 offen offset:1024 sc1
Ta 156,22,160
Mvaa 120,132,212
Ta 158,22,5280
Mvaa 120,128,208
W 8
Ta 128,25,30912
Ta 130,25,36032
Mvaa 124,136,208
Mvaa 124,140,212
buffer_load_dwordx4 a[236:239], v1, s[8:11], s82 offen offset:1024 sc1
Ta 132,21,192
Mvaa 116,140,196
Ta 134,21,5312
Mvaa 116,136,192
W 8
Ta 136,26,30944
Ta 138,26,36064
Mvaa 128,144,192
Mvaa 128,148,196
buffer_load_dwordx4 a[224:227], v1, s[8:11], s81 offen offset:2048 sc1
Ta 140,22,224
Mvaa 136,148,212
Ta 142,22,5344
Mvaa 136,144,208
W 8
Ta 144,25,30976
Ta 146,25,36096
Mvaa 140,152,208
Mvaa 140,156,212
buffer_load_dwordx4 a[240:243], v1, s[8:11], s82 offen offset:2048 sc1
Ta 148,21,256
Mvaa 132,156,196
Ta 150,21,5376
Mvaa 132,152,192
W 8
Ta 152,26,31008
Ta 154,26,36128
Mvaa 144,128,192
Mvaa 144,132,196
buffer_load_dwordx4 a[228:231], v1, s[8:11], s81 offen offset:3072 sc1
Ta 156,22,288
Mvaa 152,132,212
Ta 158,22,5408
Mvaa 152,128,208
W 8
Ta 128,21,10240
Ta 130,21,15360
Mvaa 156,136,208
Mvaa 156,140,212
buffer_load_dwordx4 a[244:247], v1, s[8:11], s82 offen offset:3072 sc1
Ta 132,21,20480
Mvaa 148,140,196
s_add_u32 m0, s71, s48
s_lshl2_add_u32 s94, s48, s95
Ta 134,21,25600
Mvaa 148,136,192
W 8
Ta 136,22,10272
Ta 138,22,15392
Mvaa 160,144,192
Mvaa 160,148,196
D 16,40,94,0
Ta 140,22,20512
Mvaa 168,148,212
s_add_u32 s95, s92, 15
s_and_b32 s95, s95, 15
s_mul_i32 s95, s95, 40960
Mvaa 168,144,208
W 7
s_mul_i32 s71, s51, 0
s_add_u32 s71, s71, 102400
Mvaa 172,152,208
Ta 142,22,25632
Ta 144,21,10304
Mvaa 172,156,212
D 17,40,94,1024
Ta 146,21,15424
Mvaa 164,156,196
Ta 148,21,20544
Ta 150,21,25664
Mvaa 164,152,192
s_waitcnt vmcnt(6) lgkmcnt(8)
Ta 152,22,10336
Ta 154,22,15456
Mvaa 96,128,216
Mvaa 96,132,220
D 18,40,94,2048
Ta 156,22,20576
Mvaa 104,132,236
Ta 158,22,25696
Mvaa 104,128,232
W 8
Ta 128,21,10368
Ta 130,21,15488
Mvaa 108,136,232
Mvaa 108,140,236
D 19,40,94,3072
Ta 132,21,20608
Mvaa 100,140,220
s_add_u32 m0, m0, 4096
Ta 134,21,25728
Mvaa 100,136,216
W 8
Ta 136,22,10400
Ta 138,22,15520
Mvaa 112,144,216
Mvaa 112,148,220
buffer_load_dwordx4 v20, s[40:43], s94 offen lds
Ta 140,22,20640
Mvaa 120,148,236
s_add_u32 m0, s71, s48
s_lshl2_add_u32 s94, s48, s95
Ta 142,22,25760
Mvaa 120,144,232
W 8
Ta 144,21,10432
Ta 146,21,15552
Mvaa 124,152,232
Mvaa 124,156,236
D 16,40,94,0
Ta 148,21,20672
Mvaa 116,156,220
D 17,40,94,1024
D 18,40,94,2048
D 19,40,94,3072
s_add_u32 m0, m0, 4096
s_nop 0
buffer_load_dwordx4 v20, s[40:43], s94 offen lds
Mvaa 116,152,216
W 7
s_add_u32 s81, s58, 2
s_and_b32 s81, s81, 3
Mvaa 128,128,216
s_lshl_b32 s81, s81, 12
s_add_u32 s81, s81, s49
Ta 150,21,25792
Mvaa 128,132,220
Ta 152,22,10464
Ta 154,22,15584
Mvaa 136,132,236
Ta 156,22,20704
Ta 158,22,25824
Mvaa 136,128,232
W 8
Ta 128,21,10496
Ta 130,21,15616
Mvaa 140,136,232
s_barrier
buffer_load_dwordx4 a[0:3], v1, s[8:11], s81 offen offset:0 sc1
v_and_b32 v27, 1, v12
Mvaa 140,140,236
s_add_u32 s82, s81, 131072
Ta 132,21,20736
Ta 134,21,25856
Mvaa 132,140,220
Mvaa 132,136,216
W 8
Ta 136,22,10528
v_lshlrev_b32 v27, 1, v27
)"
R"(Mvaa 144,144,216
Ta 138,22,15648
buffer_load_dwordx4 a[16:19], v1, s[8:11], s82 offen offset:0 sc1
Mvaa 144,148,220
Ta 140,22,20768
Ta 142,22,25888
Mvaa 152,148,236
v_xor_b32 v27, v27, v13
Mvaa 152,144,232
W 8
Ta 144,21,30720
Ta 146,21,35840
Mvaa 156,152,232
buffer_load_dwordx4 a[4:7], v1, s[8:11], s81 offen offset:1024 sc1
Mvaa 156,156,236
Ta 148,23,0
v_and_b32 v12, 2, v12
Mvaa 148,156,220
Ta 150,23,5120
Mvaa 148,152,216
W 8
Ta 152,22,30752
Ta 154,22,35872
Mvaa 160,128,216
buffer_load_dwordx4 a[20:23], v1, s[8:11], s82 offen offset:1024 sc1
v_or_b32 v12, v12, v14
Mvaa 160,132,220
Ta 156,24,32
Ta 158,24,5152
Mvaa 168,132,236
Mvaa 168,128,232
W 8
Ta 128,21,30784
v_lshl_or_b32 v12, v27, 2, v12
Mvaa 172,136,232
Ta 130,21,35904
buffer_load_dwordx4 a[8:11], v1, s[8:11], s81 offen offset:2048 sc1
Mvaa 172,140,236
Ta 132,23,64
Ta 134,23,5184
Mvaa 164,140,220
v_cmp_eq_u32_e64 s[86:87], 0, v12
Mvaa 164,136,216
s_waitcnt vmcnt(15) lgkmcnt(8)
Ta 136,22,30816
Ta 138,22,35936
Mvaa 96,144,224
buffer_load_dwordx4 a[24:27], v1, s[8:11], s82 offen offset:2048 sc1
Mvaa 96,148,228
Ta 140,24,96
v_cndmask_b32_e64 v4, 0, v15, s[86:87]
Mvaa 104,148,244
Ta 142,24,5216
Mvaa 104,144,240
W 8
Ta 144,21,30848
Ta 146,21,35968
Mvaa 108,152,240
buffer_load_dwordx4 a[12:15], v1, s[8:11], s81 offen offset:3072 sc1
v_cmp_eq_u32_e64 s[86:87], 1, v12
Mvaa 108,156,244
Ta 148,23,128
Ta 150,23,5248
Mvaa 100,156,228
v_cndmask_b32_e64 v5, 0, v15, s[86:87]
Mvaa 100,152,224
W 8
Ta 152,22,30880
Ta 154,22,36000
Mvaa 112,128,224
buffer_load_dwordx4 a[28:31], v1, s[8:11], s82 offen offset:3072 sc1
Mvaa 112,132,228
s_add_u32 s81, s58, 3
s_and_b32 s81, s81, 3
s_lshl_b32 s81, s81, 12
Mvaa 120,132,244
s_add_u32 s81, s81, s49
Ta 156,24,160
v_cmp_eq_u32_e64 s[86:87], 2, v12
Mvaa 120,128,240
W 7
Ta 158,24,5280
Ta 128,21,30912
Mvaa 124,136,240
Ta 130,21,36032
v_cndmask_b32_e64 v6, 0, v15, s[86:87]
Mvaa 124,140,244
buffer_load_dwordx4 a[32:35], v1, s[8:11], s81 offen offset:0 sc1
Ta 132,23,192
Mvaa 116,140,228
s_add_u32 s82, s81, 131072
Ta 134,23,5312
Mvaa 116,136,224
W 8
Ta 136,22,30944
v_cmp_eq_u32_e64 s[86:87], 3, v12
Mvaa 128,144,224
Ta 138,22,36064
Mvaa 128,148,228
buffer_load_dwordx4 a[48:51], v1, s[8:11], s82 offen offset:0 sc1
Ta 140,24,224
Mvaa 136,148,244
Ta 142,24,5344
v_cndmask_b32_e64 v7, 0, v15, s[86:87]
Mvaa 136,144,240
W 8
Ta 144,21,30976
Ta 146,21,36096
Mvaa 140,152,240
s_waitcnt vmcnt(10)
s_barrier
Mvaa 140,156,244
buffer_load_dwordx4 a[36:39], v1, s[8:11], s81 offen offset:1024 sc1
v_cmp_eq_u32_e64 s[86:87], 4, v12
Mvaa 132,156,228
Ta 148,23,256
Ta 150,23,5376
Mvaa 132,152,224
W 8
Ta 152,22,31008
Ta 154,22,36128
Mvaa 144,128,224
v_cndmask_b32_e64 v8, 0, v15, s[86:87]
Mvaa 144,132,228
buffer_load_dwordx4 a[52:55], v1, s[8:11], s82 offen offset:1024 sc1
Ta 156,24,288
Mvaa 152,132,244
Ta 158,24,5408
Mvaa 152,128,240
W 8
Ta 128,23,20480
v_cmp_eq_u32_e64 s[86:87], 5, v12
Mvaa 156,136,240
Ta 130,23,25600
Mvaa 156,140,244
buffer_load_dwordx4 a[40:43], v1, s[8:11], s81 offen offset:2048 sc1
Ta 132,23,51200
Mvaa 148,140,228
Ta 134,23,56320
v_cndmask_b32_e64 v9, 0, v15, s[86:87]
Mvaa 148,136,224
W 8
Ta 136,24,20512
Ta 138,24,25632
Mvaa 160,144,224
v_cmp_eq_u32_e64 s[86:87], 6, v12
Mvaa 160,148,228
buffer_load_dwordx4 a[56:59], v1, s[8:11], s82 offen offset:2048 sc1
Ta 140,24,51232
Mvaa 168,148,244
Ta 142,24,56352
Mvaa 168,144,240
W 8
Ta 144,23,20544
v_cndmask_b32_e64 v10, 0, v15, s[86:87]
Mvaa 172,152,240
Ta 146,23,25664
Mvaa 172,156,244
buffer_load_dwordx4 a[44:47], v1, s[8:11], s81 offen offset:3072 sc1
Ta 148,23,51264
Mvaa 164,156,228
Ta 150,23,56384
v_cmp_eq_u32_e64 s[86:87], 7, v12
Mvaa 164,152,224
s_waitcnt vmcnt(11) lgkmcnt(8)
Ta 152,24,20576
Ta 154,24,25696
Mvaa 96,128,0
Mvaa 96,132,4
buffer_load_dwordx4 a[60:63], v1, s[8:11], s82 offen offset:3072 sc1
v_cndmask_b32_e64 v11, 0, v15, s[86:87]
Mvaa 104,132,20
Ta 156,24,51296
Ta 158,24,56416
Mvaa 104,128,16
W 8
Ta 128,23,20608
Ta 130,23,25728
Mvaa 108,136,16
s_mov_b32 s20, s38
Mvaa 108,140,20
Ta 132,23,51328
Ta 134,23,56448
Mvaa 100,140,4
Mvaa 100,136,0
W 8
s_and_b32 s21, s39, 0xffff
Ta 136,24,20640
Mvaa 112,144,0
Ta 138,24,25760
v_bfe_u32 v28, v1, 4, 2
Mvaa 112,148,4
Ta 140,24,51360
v_lshlrev_b32 v28, 3, v28
Mvaa 120,148,20
s_mov_b32 s22, 2621440
Ta 142,24,56480
buffer_load_dwordx2 v[28:29], v28, s[8:11], s55 offen sc1
Mvaa 120,144,16
W 8
Ta 144,23,20672
Ta 146,23,25792
Mvaa 124,152,16
Mvaa 124,156,20
s_mov_b32 s23, 0x20000
Ta 148,23,51392
Ta 150,23,56512
Mvaa 116,156,4
Mvaa 116,152,0
W 8
Ta 152,24,20704
Ta 154,24,25824
Mvaa 128,128,0
s_and_b32 s17, s17, 0xffff
Mvaa 128,132,4
Ta 156,24,51424
Ta 158,24,56544
Mvaa 136,132,20
s_mov_b32 s18, 2621440
Mvaa 136,128,16
W 8
Ta 128,23,20736
Ta 130,23,25856
Mvaa 140,136,16
Mvaa 140,140,20
s_mov_b32 s19, 0x20000
Ta 132,23,51456
Ta 134,23,56576
Mvaa 132,140,4
Mvaa 132,136,0
W 8
Ta 136,24,20768
Ta 138,24,25888
Mvaa 144,144,0
s_lshr_b32 s66, s15, 3
Mvaa 144,148,4
Ta 140,24,51488
Ta 142,24,56608
Mvaa 152,148,20
Mvaa 152,144,16
W 8
s_lshl_b32 s66, s66, 4
Ta 144,25,0
Mvaa 156,152,16
Ta 146,25,5120
Mvaa 156,156,20
Ta 148,21,40960
Ta 150,21,46080
Mvaa 148,156,4
s_add_u32 s66, s66, s59
Mvaa 148,152,0
W 8
Ta 152,26,32
Ta 154,26,5152
Mvaa 160,128,0
Mvaa 160,132,4
s_lshl_b32 s63, s66, 6
Ta 156,22,40992
Ta 158,22,46112
Mvaa 168,132,20
Mvaa 168,128,16
W 8
Ta 128,25,64
Ta 130,25,5184
Mvaa 172,136,16
s_add_u32 s63, s63, 65536
Mvaa 172,140,20
Ta 132,21,41024
Ta 134,21,46144
Mvaa 164,140,4
Mvaa 164,136,0
s_waitcnt vmcnt(9) lgkmcnt(8)
s_mul_i32 s62, s66, 40960
Ta 136,26,96
Mvaa 96,144,8
Ta 138,26,5216
Mvaa 96,148,12
Ta 140,22,41056
Ta 142,22,46176
Mvaa 104,148,28
s_add_u32 s62, s62, 56623104
Mvaa 104,144,24
W 8
Ta 144,25,128
Ta 146,25,5248
Mvaa 108,152,24
s_lshl_b32 s66, s14, 10
Mvaa 108,156,28
Ta 148,21,41088
Ta 150,21,46208
Mvaa 100,156,12
Mvaa 100,152,8
W 8
s_add_u32 s78, s62, s66
Ta 152,26,160
Mvaa 112,128,8
Ta 154,26,5280
Mvaa 112,132,12
Ta 156,22,41120
Ta 158,22,46240
Mvaa 120,132,28
s_add_u32 s79, s78, 327680
s_waitcnt vmcnt(0)
v_cmp_eq_u64 vcc, s[12:13], v[28:29]
s_cmp_eq_u64 vcc, -1
s_cbranch_scc0 .Lspin_96%=
.Lok_97%=:
s_add_u32 s81, s58, 1
Mvaa 120,128,24
W 8
s_and_b32 s81, s81, 3
s_lshl_b32 s81, s81, 12
Mvaa 124,136,24
s_add_u32 s81, s81, s50
buffer_load_dwordx4 a[64:67], v1, s[8:11], s81 offen offset:0 sc1
Ta 128,25,192
Mvaa 124,140,28
s_add_u32 s82, s81, 131072
s_and_b32 s66, s14, 1
Ta 130,25,5312
Mvaa 116,140,12
Ta 132,21,41152
Ta 134,21,46272
Mvaa 116,136,8
W 8
Ta 136,26,224
Ta 138,26,5344
Mvaa 128,144,8
s_mul_i32 s66, s66, 327680
buffer_load_dwordx4 a[80:83], v1, s[8:11], s82 offen offset:0 sc1
Mvaa 128,148,12
Ta 140,22,41184
Ta 142,22,46304
Mvaa 136,148,28
Mvaa 136,144,24
W 8
s_add_u32 s65, s62, s66
Ta 144,25,256
Mvaa 140,152,24
Ta 146,25,5376
Mvaa 140,156,28
buffer_load_dwordx4 a[68:71], v1, s[8:11], s81 offen offset:1024 sc1
Ta 148,21,41216
Mvaa 132,156,12
s_add_u32 s65, s65, 4096
Ta 150,21,46336
Mvaa 132,152,8
W 8
Ta 152,26,288
Ta 154,26,5408
Mvaa 144,128,8
Mvaa 144,132,12
s_mov_b32 s64, 0
Ta 156,22,41248
Ta 158,22,46368
Mvaa 152,132,28
buffer_load_dwordx4 a[84:87], v1, s[8:11], s82 offen offset:1024 sc1
Mvaa 152,128,24
W 8
Ta 128,21,51200
Ta 130,21,56320
Mvaa 156,136,24
v_bfe_u32 v27, v1, 4, 3
Mvaa 156,140,28
Ta 132,23,10240
Ta 134,23,15360
Mvaa 148,140,12
v_lshlrev_b32 v27, 3, v27
Mvaa 148,136,8
W 8
buffer_load_dwordx4 a[72:75], v1, s[8:11], s81 offen offset:2048 sc1
Ta 136,22,51232
Mvaa 160,144,8
Ta 138,22,56352
Mvaa 160,148,12
Ta 140,24,10272
v_bfe_u32 v20, v1, 4, 4
Mvaa 168,148,28
Ta 142,24,15392
Mvaa 168,144,24
W 8
Ta 144,21,51264
Ta 146,21,56384
Mvaa 172,152,24
buffer_load_dwordx4 a[88:91], v1, s[8:11], s82 offen offset:2048 sc1
v_mul_u32_u24 v20, 1280, v20
Mvaa 172,156,28
Ta 148,23,10304
Ta 150,23,15424
Mvaa 164,156,12
Mvaa 164,152,8
W 8
Ta 152,22,51296
v_lshrrev_b32 v12, 8, v1
Mvaa 96,128,32
Ta 154,22,56416
Mvaa 96,132,36
buffer_load_dwordx4 a[76:79], v1, s[8:11], s81 offen offset:3072 sc1
Ta 156,24,10336
Mvaa 104,132,52
)"
R"(Ta 158,24,15456
v_lshl_add_u32 v20, v12, 4, v20
Mvaa 104,128,48
W 8
Ta 128,21,51328
Ta 130,21,56448
Mvaa 108,136,48
Mvaa 108,140,52
s_bfe_u32 s66, s59, 0x10001
Ta 132,23,10368
Ta 134,23,15488
Mvaa 100,140,36
buffer_load_dwordx4 a[92:95], v1, s[8:11], s82 offen offset:3072 sc1
Mvaa 100,136,32
W 8
s_add_u32 s81, s58, 2
s_and_b32 s81, s81, 3
Mvaa 112,144,32
s_lshl_b32 s81, s81, 12
s_add_u32 s81, s81, s50
Ta 136,22,51360
Mvaa 112,148,36
s_lshl_b32 s67, s57, 1
Ta 138,22,56480
Ta 140,24,10400
Mvaa 120,148,52
Ta 142,24,15520
buffer_load_dwordx4 a[96:99], v1, s[8:11], s81 offen offset:0 sc1
Mvaa 120,144,48
W 8
s_add_u32 s82, s81, 131072
s_add_u32 s66, s66, s67
Mvaa 124,152,48
Ta 144,21,51392
Ta 146,21,56512
Mvaa 124,156,52
Ta 148,23,10432
Ta 150,23,15552
Mvaa 116,156,36
s_lshl_b32 s61, s66, 2
Mvaa 116,152,32
W 8
Ta 152,22,51424
Ta 154,22,56544
Mvaa 128,128,32
s_lshr_b32 s66, s59, 2
buffer_load_dwordx4 a[112:115], v1, s[8:11], s82 offen offset:0 sc1
Mvaa 128,132,36
Ta 156,24,10464
Ta 158,24,15584
Mvaa 136,132,52
Mvaa 136,128,48
W 8
s_lshl_b32 s66, s66, 1
Ta 128,21,51456
Mvaa 140,136,48
Ta 130,21,56576
Mvaa 140,140,52
buffer_load_dwordx4 a[100:103], v1, s[8:11], s81 offen offset:1024 sc1
Ta 132,23,10496
Mvaa 132,140,36
s_add_u32 s61, s61, s66
Ta 134,23,15616
Mvaa 132,136,32
W 8
Ta 136,22,51488
Ta 138,22,56608
Mvaa 144,144,32
Mvaa 144,148,36
s_and_b32 s66, s59, 1
Ta 140,24,10528
Ta 142,24,15648
Mvaa 152,148,52
buffer_load_dwordx4 a[116:119], v1, s[8:11], s82 offen offset:1024 sc1
Mvaa 152,144,48
W 8
Ta 144,23,30720
Ta 146,23,35840
Mvaa 156,152,48
s_add_u32 s61, s61, s66
Mvaa 156,156,52
Ta 148,23,40960
Ta 150,23,46080
Mvaa 148,156,36
Mvaa 148,152,32
W 8
s_xor_b32 s67, s61, 0
buffer_load_dwordx4 a[104:107], v1, s[8:11], s81 offen offset:2048 sc1
Mvaa 160,128,32
Ta 152,24,30752
Ta 154,24,35872
Mvaa 160,132,36
Ta 156,24,40992
Ta 158,24,46112
Mvaa 168,132,52
s_mul_i32 s72, s67, 20480
Mvaa 168,128,48
W 8
Ta 128,23,30784
Ta 130,23,35904
Mvaa 172,136,48
buffer_load_dwordx4 a[120:123], v1, s[8:11], s82 offen offset:2048 sc1
Mvaa 172,140,52
s_mul_i32 s66, s58, 320
Ta 132,23,41024
Ta 134,23,46144
Mvaa 164,140,36
Mvaa 164,136,32
W 8
Ta 136,24,30816
Ta 138,24,35936
Mvaa 96,144,40
s_add_u32 s72, s72, s66
Mvaa 96,148,44
buffer_load_dwordx4 a[108:111], v1, s[8:11], s81 offen offset:3072 sc1
Ta 140,24,41056
Mvaa 104,148,60
s_mul_i32 s75, s67, 20480
Ta 142,24,46176
Mvaa 104,144,56
W 8
Ta 144,23,30848
Ta 146,23,35968
Mvaa 108,152,56
Mvaa 108,156,60
s_mul_i32 s66, s58, 2560
Ta 148,23,41088
Ta 150,23,46208
Mvaa 100,156,44
buffer_load_dwordx4 a[124:127], v1, s[8:11], s82 offen offset:3072 sc1
Mvaa 100,152,40
W 8
s_add_u32 s81, s58, 3
s_and_b32 s81, s81, 3
Mvaa 112,128,40
s_lshl_b32 s81, s81, 12
s_add_u32 s81, s81, s50
Ta 152,24,30880
Mvaa 112,132,44
s_add_u32 s75, s75, s66
Ta 154,24,36000
Ta 156,24,41120
Mvaa 120,132,60
Ta 158,24,46240
buffer_load_dwordx4 a[184:187], v1, s[8:11], s81 offen offset:0 sc1
Mvaa 120,128,56
W 8
s_add_u32 s82, s81, 131072
s_add_u32 s75, s75, 131072
Mvaa 124,136,56
Ta 128,23,30912
Ta 130,23,36032
Mvaa 124,140,60
Ta 132,23,41152
Ta 134,23,46272
Mvaa 116,140,44
s_xor_b32 s67, s61, 124
Mvaa 116,136,40
W 8
Ta 136,24,30944
Ta 138,24,36064
Mvaa 128,144,40
buffer_load_dwordx4 a[200:203], v1, s[8:11], s82 offen offset:0 sc1
Mvaa 128,148,44
s_mul_i32 s73, s67, 20480
Ta 140,24,41184
Ta 142,24,46304
Mvaa 136,148,60
Mvaa 136,144,56
W 8
Ta 144,23,30976
Ta 146,23,36096
Mvaa 140,152,56
s_mul_i32 s66, s58, 320
Mvaa 140,156,60
buffer_load_dwordx4 a[188:191], v1, s[8:11], s81 offen offset:1024 sc1
Ta 148,23,41216
Mvaa 132,156,44
Ta 150,23,46336
Mvaa 132,152,40
W 8
s_add_u32 s73, s73, s66
Ta 152,24,31008
Mvaa 144,128,40
Ta 154,24,36128
Mvaa 144,132,44
Ta 156,24,41248
Ta 158,24,46368
Mvaa 152,132,60
s_mul_i32 s76, s67, 20480
buffer_load_dwordx4 a[204:207], v1, s[8:11], s82 offen offset:1024 sc1
Mvaa 152,128,56
W 8
Ta 128,21,10240
Ta 130,21,15360
Mvaa 156,136,56
s_mul_i32 s66, s58, 2560
Mvaa 156,140,60
Ta 132,21,20480
Ta 134,21,25600
Mvaa 148,140,44
K 96,96,97
Mvaa 148,136,40
W 8
s_add_u32 s76, s76, s66
buffer_load_dwordx4 a[192:195], v1, s[8:11], s81 offen offset:2048 sc1
Mvaa 160,144,40
Ta 136,22,10272
Ta 138,22,15392
Mvaa 160,148,44
Ta 140,22,20512
K 97,98,99
Mvaa 168,148,60
s_add_u32 s76, s76, 131072
Ta 142,22,25632
K 98,100,101
Mvaa 168,144,56
W 8
Ta 144,21,10304
Ta 146,21,15424
Mvaa 172,152,56
buffer_load_dwordx4 a[208:211], v1, s[8:11], s82 offen offset:2048 sc1
K 99,102,103
Mvaa 172,156,60
s_add_u32 s83, s52, 0
Ta 148,21,20544
v_bfe_u32 v15, v1, 7, 1
Mvaa 164,156,44
Ta 150,21,25664
Mvaa 164,152,40
s_waitcnt vmcnt(18) lgkmcnt(8)
Ta 152,22,10336
Ta 154,22,15456
Mvaa 176,128,64
buffer_store_dwordx4 v[96:99], v1, s[8:11], s83 offen sc1
v_mul_u32_u24 v14, 10240, v15
Mvaa 176,132,68
Ta 156,22,20576
K 104,104,105
Mvaa 184,132,84
Ta 158,22,25696
K 105,106,107
Mvaa 184,128,80
W 8
Ta 128,21,10368
v_lshrrev_b32 v15, 8, v1
Mvaa 188,136,80
buffer_load_dwordx4 a[196:199], v1, s[8:11], s81 offen offset:3072 sc1
Ta 130,21,15488
Mvaa 188,140,84
Ta 132,21,20608
K 106,108,109
Mvaa 180,140,68
Ta 134,21,25728
K 107,110,111
Mvaa 180,136,64
W 8
s_add_u32 s83, s52, 40960
v_lshl_add_u32 v14, v15, 7, v14
Mvaa 192,144,64
buffer_load_dwordx4 a[212:215], v1, s[8:11], s82 offen offset:3072 sc1
Ta 136,22,10400
Mvaa 192,148,68
Ta 138,22,15520
v_and_b32 v15, 0x70, v1
Mvaa 200,148,84
Ta 140,22,20640
Ta 142,22,25760
Mvaa 200,144,80
W 8
Ta 144,21,10432
Ta 146,21,15552
Mvaa 204,152,80
buffer_store_dwordx4 v[104:107], v1, s[8:11], s83 offen sc1
K 112,112,113
Mvaa 204,156,84
v_add_u32 v14, v14, v15
K 113,114,115
Mvaa 196,156,68
s_bitcmp1_b32 s14, 0
s_cselect_b32 s74, s73, s72
s_cselect_b32 s77, s76, s75
Ta 148,21,20672
Ta 150,21,25792
Mvaa 196,152,64
W 8
Ta 152,22,10464
K 114,116,117
Mvaa 208,128,64
Ta 154,22,15584
K 115,118,119
Mvaa 208,132,68
s_add_u32 s83, s52, 1024
s_add_u32 s74, s74, 256
Ta 156,22,20704
Mvaa 216,132,84
Ta 158,22,25824
Mvaa 216,128,80
W 8
buffer_store_dwordx4 v[112:115], v1, s[8:11], s83 offen sc1
Ta 128,21,10496
Mvaa 220,136,80
s_add_u32 s77, s77, 2048
Ta 130,21,15616
K 120,120,121
Mvaa 220,140,84
Ta 132,21,20736
Ta 134,21,25856
Mvaa 212,140,68
K 121,122,123
Mvaa 212,136,64
W 8
s_lshl_b32 s66, s14, 6
Ta 136,22,10528
Mvaa 224,144,64
Ta 138,22,15648
K 122,124,125
Mvaa 224,148,68
Ta 140,22,20768
Ta 142,22,25888
Mvaa 232,148,84
s_lshl_b32 s67, s14, 9
K 123,126,127
Mvaa 232,144,80
W 8
s_add_u32 s83, s52, 41984
Ta 144,21,30720
Mvaa 236,152,80
Ta 146,21,35840
Mvaa 236,156,84
s_add_u32 s72, s72, s66
buffer_store_dwordx4 v[120:123], v1, s[8:11], s83 offen sc1
Ta 148,23,0
Mvaa 228,156,68
Ta 150,23,5120
K 128,128,129
Mvaa 228,152,64
W 8
Ta 152,22,30752
Ta 154,22,35872
Mvaa 240,128,64
s_add_u32 s75, s75, s67
K 129,130,131
Mvaa 240,132,68
Ta 156,24,32
Ta 158,24,5152
Mvaa 248,132,84
K 130,132,133
Mvaa 248,128,80
W 8
s_add_u32 s73, s73, s66
Ta 128,21,30784
Mvaa 252,136,80
Ta 130,21,35904
K 131,134,135
Mvaa 252,140,84
s_add_u32 s83, s52, 2048
Ta 132,23,64
Ta 134,23,5184
Mvaa 244,140,68
s_add_u32 s76, s76, s67
Mvaa 244,136,64
s_waitcnt vmcnt(20) lgkmcnt(8)
buffer_store_dwordx4 v[128:131], v1, s[8:11], s83 offen sc1
Ta 136,22,30816
Mvaa 176,144,72
Ta 138,22,35936
K 136,136,137
Mvaa 176,148,76
Ta 140,24,96
Ta 142,24,5216
Mvaa 184,148,92
K 137,138,139
Mvaa 184,144,88
W 8
buffer_load_dwordx4 v[36:39], v20, s[16:19], s72 offen
Ta 144,21,30848
Mvaa 188,152,88
Ta 146,21,35968
K 138,140,141
Mvaa 188,156,92
Ta 148,23,128
Ta 150,23,5248
Mvaa 180,156,76
K 139,142,143
Mvaa 180,152,72
W 8
s_add_u32 s83, s52, 43008
buffer_load_dwordx4 v[44:47], v14, s[8:11], s75 offen sc1
Mvaa 192,128,72
Ta 152,22,30880
Ta 154,22,36000
Mvaa 192,132,76
Ta 156,24,160
Ta 158,24,5280
Mvaa 200,132,92
Mvaa 200,128,88
W 8
buffer_store_dwordx4 v[136:139], v1, s[8:11], s83 offen sc1
K 144,144,145
)"
R"(Mvaa 204,136,88
Ta 128,21,30912
Ta 130,21,36032
Mvaa 204,140,92
Ta 132,23,192
K 145,146,147
Mvaa 196,140,76
Ta 134,23,5312
K 146,148,149
Mvaa 196,136,72
W 8
buffer_load_dwordx4 v[52:55], v20, s[16:19], s73 offen
Ta 136,22,30944
Mvaa 208,144,72
Ta 138,22,36064
K 147,150,151
Mvaa 208,148,76
s_add_u32 s83, s52, 3072
Ta 140,24,224
Ta 142,24,5344
Mvaa 216,148,92
Mvaa 216,144,88
W 8
buffer_store_dwordx4 v[144:147], v1, s[8:11], s83 offen sc1
Ta 144,21,30976
Mvaa 220,152,88
Ta 146,21,36096
K 152,152,153
Mvaa 220,156,92
Ta 148,23,256
Ta 150,23,5376
Mvaa 212,156,76
K 153,154,155
Mvaa 212,152,72
W 8
buffer_load_dwordx4 v[60:63], v14, s[8:11], s76 offen sc1
Ta 152,22,31008
Mvaa 224,128,72
Ta 154,22,36128
K 154,156,157
Mvaa 224,132,76
Ta 156,24,288
Ta 158,24,5408
Mvaa 232,132,92
K 155,158,159
Mvaa 232,128,88
W 8
s_add_u32 s83, s52, 44032
buffer_load_dwordx4 v[68:71], v20, s[16:19], s74 offen
Mvaa 236,136,88
Ta 128,23,20480
Ta 130,23,25600
Mvaa 236,140,92
Ta 132,23,51200
Ta 134,23,56320
Mvaa 228,140,76
Mvaa 228,136,72
W 8
buffer_store_dwordx4 v[152:155], v1, s[8:11], s83 offen sc1
K 160,160,161
Mvaa 240,144,72
Ta 136,25,0
Ta 138,25,5120
Mvaa 240,148,76
Ta 140,21,40960
K 161,162,163
Mvaa 248,148,92
Ta 142,21,46080
K 162,164,165
Mvaa 248,144,88
W 8
buffer_load_dwordx4 v[76:79], v14, s[8:11], s77 offen sc1
Ta 144,21,51200
Mvaa 252,152,88
Ta 146,21,56320
K 163,166,167
Mvaa 252,156,92
s_add_u32 s83, s52, 4096
Ta 148,23,10240
Ta 150,23,15360
Mvaa 244,156,76
Mvaa 244,152,72
s_waitcnt vmcnt(26) lgkmcnt(8)
buffer_store_dwordx4 v[160:163], v1, s[8:11], s83 offen sc1
Ta 152,23,30720
Mvaa 176,128,96
Ta 154,23,35840
K 168,168,169
Mvaa 176,132,100
Ta 156,23,40960
Ta 158,23,46080
Mvaa 184,132,116
K 169,170,171
Mvaa 184,128,112
s_waitcnt vmcnt(23) lgkmcnt(8)
buffer_load_dwordx2 v[12:13], v27, s[8:11], 0 offen offset:3584 sc1
Ta 128,24,20512
Mvaa 184,136,120
Ta 130,24,25632
K 170,172,173
Mvaa 184,140,124
Ta 132,24,51232
Ta 134,24,56352
Mvaa 176,140,108
K 171,174,175
Mvaa 176,136,104
s_waitcnt vmcnt(20) lgkmcnt(8)
s_add_u32 s83, s52, 45056
Ta 136,26,32
Mvaa 176,144,184
Ta 138,26,5152
Mvaa 176,148,188
buffer_store_dwordx4 v[168:171], v1, s[8:11], s83 offen sc1
Ta 140,22,40992
Mvaa 184,148,204
Ta 142,22,46112
Mvaa 184,144,200
s_waitcnt vmcnt(16) lgkmcnt(8)
Ta 144,22,51232
Ta 146,22,56352
Mvaa 184,152,208
Mvaa 184,156,212
Ta 148,24,10272
Ta 150,24,15392
Mvaa 176,156,196
Mvaa 176,152,192
W 8
Ta 152,24,30752
Ta 154,24,35872
Mvaa 180,128,96
Mvaa 180,132,100
Ta 156,24,40992
Ta 158,24,46112
Mvaa 188,132,116
Mvaa 188,128,112
W 8
Ta 128,23,20544
Ta 130,23,25664
Mvaa 188,136,120
Mvaa 188,140,124
Ta 132,23,51264
Ta 134,23,56384
Mvaa 180,140,108
Mvaa 180,136,104
W 8
Ta 136,25,64
Ta 138,25,5184
Mvaa 180,144,184
Mvaa 180,148,188
Ta 140,21,41024
Ta 142,21,46144
Mvaa 188,148,204
Mvaa 188,144,200
W 8
Ta 144,21,51264
Ta 146,21,56384
Mvaa 188,152,208
Mvaa 188,156,212
Ta 148,23,10304
Ta 150,23,15424
Mvaa 180,156,196
Mvaa 180,152,192
W 8
Ta 152,23,30784
Ta 154,23,35904
Mvaa 192,128,96
Mvaa 192,132,100
Ta 156,23,41024
Ta 158,23,46144
Mvaa 200,132,116
K 176,176,177
Mvaa 200,128,112
W 8
Ta 128,24,20576
Ta 130,24,25696
Mvaa 200,136,120
K 177,178,179
Mvaa 200,140,124
Ta 132,24,51296
K 178,180,181
Mvaa 192,140,108
Ta 134,24,56416
K 179,182,183
Mvaa 192,136,104
W 8
Ta 136,26,96
Ta 138,26,5216
Mvaa 192,144,184
s_add_u32 s83, s52, 327680
Mvaa 192,148,188
buffer_store_dwordx4 v[176:179], v1, s[8:11], s83 offen sc1
K 184,184,185
Mvaa 200,148,204
K 185,186,187
K 186,188,189
Mvaa 200,144,200
W 6
Ta 140,22,41056
K 187,190,191
Mvaa 200,152,208
s_add_u32 s83, s52, 368640
Ta 142,22,46176
Ta 144,22,51296
Mvaa 200,156,212
buffer_store_dwordx4 v[184:187], v1, s[8:11], s83 offen sc1
Ta 146,22,56416
Mvaa 192,156,196
Ta 148,24,10336
Ta 150,24,15456
Mvaa 192,152,192
W 8
Ta 152,24,30816
Ta 154,24,35936
Mvaa 196,128,96
Mvaa 196,132,100
Ta 156,24,41056
Ta 158,24,46176
Mvaa 204,132,116
Mvaa 204,128,112
W 8
Ta 128,23,20608
Ta 130,23,25728
Mvaa 204,136,120
Mvaa 204,140,124
Ta 132,23,51328
Ta 134,23,56448
Mvaa 196,140,108
Mvaa 196,136,104
W 8
Ta 136,25,128
Ta 138,25,5248
Mvaa 196,144,184
Mvaa 196,148,188
Ta 140,21,41088
Ta 142,21,46208
Mvaa 204,148,204
Mvaa 204,144,200
W 8
Ta 144,21,51328
Ta 146,21,56448
Mvaa 204,152,208
Mvaa 204,156,212
Ta 148,23,10368
Ta 150,23,15488
Mvaa 196,156,196
Mvaa 196,152,192
W 8
Ta 152,23,30848
Ta 154,23,35968
Mvaa 208,128,96
Mvaa 208,132,100
Ta 156,23,41088
Ta 158,23,46208
Mvaa 216,132,116
K 192,192,193
Mvaa 216,128,112
W 8
Ta 128,24,20640
Ta 130,24,25760
Mvaa 216,136,120
K 193,194,195
Mvaa 216,140,124
Ta 132,24,51360
K 194,196,197
Mvaa 208,140,108
Ta 134,24,56480
K 195,198,199
Mvaa 208,136,104
W 8
Ta 136,26,160
Ta 138,26,5280
Mvaa 208,144,184
s_add_u32 s83, s52, 328704
Mvaa 208,148,188
buffer_store_dwordx4 v[192:195], v1, s[8:11], s83 offen sc1
K 200,200,201
Mvaa 216,148,204
K 201,202,203
K 202,204,205
Mvaa 216,144,200
W 6
Ta 140,22,41120
K 203,206,207
Mvaa 216,152,208
s_add_u32 s83, s52, 369664
Ta 142,22,46240
Ta 144,22,51360
Mvaa 216,156,212
buffer_store_dwordx4 v[200:203], v1, s[8:11], s83 offen sc1
Ta 146,22,56480
Mvaa 208,156,196
Ta 148,24,10400
Ta 150,24,15520
Mvaa 208,152,192
W 8
Ta 152,24,30880
Ta 154,24,36000
Mvaa 212,128,96
Mvaa 212,132,100
s_waitcnt vmcnt(4)
v_lshlrev_b32 v2, 2, v1
v_mov_b64 v[28:29], s[12:13]
s_mov_b64 exec, 3
buffer_store_dwordx2 v[28:29], v2, s[8:11], s53 offen offset:0 sc1
s_mov_b64 exec, -1
Ta 156,24,41120
Ta 158,24,46240
Mvaa 220,132,116
Mvaa 220,128,112
W 8
Ta 128,23,20672
Ta 130,23,25792
Mvaa 220,136,120
Mvaa 220,140,124
v_xor_b32 v12, s12, v12
v_xor_b32 v13, s13, v13
v_lshrrev_b32 v3, 11, v12
v_or_b32 v13, v13, v3
v_cmp_eq_u32 vcc, 0, v13
s_cmp_eq_u64 vcc, -1
s_cbranch_scc0 .LL_101%=
.LL_102%=:
v_bfe_u32 v13, v12, 4, 7
s_xor_b32 s66, s61, 124
v_cmp_eq_u32 vcc, s61, v13
v_cmp_eq_u32_e64 s[86:87], s66, v13
s_or_b32 s68, vcc_lo, s86
s_and_b32 s68, s68, 0xff
s_cbranch_scc1 .Lup_99%=
.Lupr_100%=:
Ta 132,23,51392
Ta 134,23,56512
Mvaa 212,140,108
Mvaa 212,136,104
W 8
Ta 136,25,192
v_lshlrev_b32 v32, 16, v36
Mvaa 212,144,184
Ta 138,25,5312
v_and_b32 v33, 0xffff0000, v36
Mvaa 212,148,188
Ta 140,21,41152
v_lshlrev_b32 v34, 16, v37
Mvaa 220,148,204
Ta 142,21,46272
v_and_b32 v35, 0xffff0000, v37
Mvaa 220,144,200
W 8
Ta 144,21,51392
v_lshlrev_b32 v36, 16, v38
Mvaa 220,152,208
Ta 146,21,56512
v_and_b32 v37, 0xffff0000, v38
Mvaa 220,156,212
Ta 148,23,10432
v_lshlrev_b32 v38, 16, v39
Mvaa 212,156,196
Ta 150,23,15552
v_and_b32 v39, 0xffff0000, v39
Mvaa 212,152,192
W 8
Ta 152,23,30912
v_lshlrev_b32 v40, 16, v44
Mvaa 224,128,96
Ta 154,23,36032
v_and_b32 v41, 0xffff0000, v44
Mvaa 224,132,100
Ta 156,23,41152
v_lshlrev_b32 v42, 16, v45
Mvaa 232,132,116
Ta 158,23,46272
v_and_b32 v43, 0xffff0000, v45
Mvaa 232,128,112
W 8
K 208,208,209
v_lshlrev_b32 v44, 16, v46
Mvaa 232,136,120
Ta 128,24,20704
Ta 130,24,25824
Mvaa 232,140,124
v_and_b32 v45, 0xffff0000, v46
K 209,210,211
Mvaa 224,140,108
v_lshlrev_b32 v46, 16, v47
K 210,212,213
Mvaa 224,136,104
W 6
Ta 132,24,51424
Ta 134,24,56544
Mvaa 224,144,184
v_and_b32 v47, 0xffff0000, v47
K 211,214,215
Mvaa 224,148,188
s_add_u32 s83, s52, 329728
Ta 136,26,224
v_lshlrev_b32 v48, 16, v52
Mvaa 232,148,204
Ta 138,26,5344
v_and_b32 v49, 0xffff0000, v52
Mvaa 232,144,200
)"
R"(W 6
buffer_store_dwordx4 v[208:211], v1, s[8:11], s83 offen sc1
v_lshlrev_b32 v50, 16, v53
Mvaa 232,152,208
K 216,216,217
K 217,218,219
Mvaa 232,156,212
K 218,220,221
K 219,222,223
Mvaa 224,156,196
s_add_u32 s83, s52, 370688
Ta 140,22,41184
Ta 142,22,46304
Mvaa 224,152,192
W 4
buffer_store_dwordx4 v[216:219], v1, s[8:11], s83 offen sc1
v_and_b32 v51, 0xffff0000, v53
Mvaa 228,128,96
Ta 144,22,51424
v_lshlrev_b32 v52, 16, v54
Mvaa 228,132,100
Ta 146,22,56544
v_and_b32 v53, 0xffff0000, v54
Mvaa 236,132,116
Ta 148,24,10464
v_lshlrev_b32 v54, 16, v55
Mvaa 236,128,112
Ta 150,24,15584
W 4
Mvaa 236,136,120
v_and_b32 v55, 0xffff0000, v55
v_lshlrev_b32 v56, 16, v60
Mvaa 236,140,124
Ta 152,24,30944
Ta 154,24,36064
Mvaa 228,140,108
v_and_b32 v57, 0xffff0000, v60
v_lshlrev_b32 v58, 16, v61
Mvaa 228,136,104
Ta 156,24,41184
Ta 158,24,46304
W 4
Mvaa 228,144,184
v_and_b32 v59, 0xffff0000, v61
v_lshlrev_b32 v60, 16, v62
Mvaa 228,148,188
Ta 128,23,20736
Ta 130,23,25856
Mvaa 236,148,204
v_and_b32 v61, 0xffff0000, v62
v_lshlrev_b32 v62, 16, v63
Mvaa 236,144,200
Ta 132,23,51456
Ta 134,23,56576
W 4
Mvaa 236,152,208
v_and_b32 v63, 0xffff0000, v63
v_lshlrev_b32 v64, 16, v68
Mvaa 236,156,212
Ta 136,25,256
Ta 138,25,5376
Mvaa 228,156,196
v_and_b32 v65, 0xffff0000, v68
v_lshlrev_b32 v66, 16, v69
Mvaa 228,152,192
Ta 140,21,41216
Ta 142,21,46336
W 4
Mvaa 240,128,96
v_and_b32 v67, 0xffff0000, v69
v_lshlrev_b32 v68, 16, v70
Mvaa 240,132,100
Ta 144,21,51456
Ta 146,21,56576
Mvaa 248,132,116
v_and_b32 v69, 0xffff0000, v70
v_lshlrev_b32 v70, 16, v71
Mvaa 248,128,112
Ta 148,23,10496
Ta 150,23,15616
W 4
Mvaa 248,136,120
v_and_b32 v71, 0xffff0000, v71
v_lshlrev_b32 v72, 16, v76
Mvaa 248,140,124
Ta 152,23,30976
Ta 154,23,36096
Mvaa 240,140,108
v_and_b32 v73, 0xffff0000, v76
v_lshlrev_b32 v74, 16, v77
Mvaa 240,136,104
Ta 156,23,41216
Ta 158,23,46336
W 4
Mvaa 240,144,184
v_and_b32 v75, 0xffff0000, v77
K 224,224,225
Mvaa 240,148,188
Ta 128,24,20768
v_lshlrev_b32 v76, 16, v78
Mvaa 248,148,204
Ta 130,24,25888
K 225,226,227
Mvaa 248,144,200
Ta 132,24,51488
Ta 134,24,56608
W 4
Mvaa 248,152,208
v_and_b32 v77, 0xffff0000, v78
K 226,228,229
Mvaa 248,156,212
v_lshlrev_b32 v78, 16, v79
v_and_b32 v79, 0xffff0000, v79
Mvaa 240,156,196
Ta 136,26,288
K 227,230,231
Mvaa 240,152,192
Ta 138,26,5408
Ta 140,22,41248
Ta 142,22,46368
W 4
Mvaa 244,128,96
s_add_u32 s83, s52, 330752
buffer_store_dwordx4 v[224:227], v1, s[8:11], s83 offen sc1
K 232,232,233
Mvaa 244,132,100
K 233,234,235
K 234,236,237
Mvaa 252,132,116
Ta 144,22,51488
K 235,238,239
Mvaa 252,128,112
Ta 146,22,56608
Ta 148,24,10528
Ta 150,24,15648
W 4
Mvaa 252,136,120
s_add_u32 s83, s52, 371712
buffer_store_dwordx4 v[232:235], v1, s[8:11], s83 offen sc1
Ta 152,24,31008
Mvaa 252,140,124
Ta 154,24,36128
Ta 156,24,41248
Mvaa 244,140,108
Ta 158,24,46368
Mvaa 244,136,104
W 4
Mvaa 244,144,184
Mvaa 244,148,188
Mvaa 252,148,204
Mvaa 252,144,200
W 0
Mvaa 252,152,208
Mvaa 252,156,212
Mvaa 244,156,196
Mvaa 244,152,192
K 240,240,241
K 241,242,243
s_nop 5
K 242,244,245
K 243,246,247
s_add_u32 s83, s52, 331776
buffer_store_dwordx4 v[240:243], v1, s[8:11], s83 offen sc1
K 248,248,249
K 249,250,251
K 250,252,253
K 251,254,255
s_add_u32 s83, s52, 372736
buffer_store_dwordx4 v[248:251], v1, s[8:11], s83 offen sc1
.Ltend_80%=:
v_bfe_u32 v26, v1, 7, 1
v_lshl_add_u32 v26, v26, 9, v27
buffer_load_dwordx2 v[24:25], v26, s[8:11], s63 offen sc1
s_cmp_lg_u32 s68, 0
s_cbranch_scc1 .Lup_104%=
.Lupr_105%=:
s_lshl_b32 s66, 0x101, s59
s_waitcnt vmcnt(1)
s_cmp_eq_u32 s64, 0
s_cbranch_scc0 .LL_110%=
v_lshlrev_b32 v2, 2, v1
v_mov_b64 v[28:29], s[12:13]
s_mov_b64 exec, 3
buffer_store_dwordx2 v[28:29], v2, s[8:11], s53 offen offset:512 sc1
s_mov_b64 exec, -1
.LL_110%=:
s_barrier
s_waitcnt vmcnt(1)
v_cmp_eq_u64 vcc, s[12:13], v[24:25]
s_or_b32 s67, vcc_lo, s66
s_cmp_eq_u32 s14, 0
s_cbranch_scc1 .LL_112%=
s_cmp_eq_u32 s14, 1
s_cbranch_scc1 .LL_113%=
s_mov_b32 s71, 0
s_mov_b32 s84, 0
.LL_114%=:
s_mov_b32 s70, s71
s_andn2_b32 s69, s67, s71
s_and_b32 s69, s69, 0xffff
s_cbranch_scc0 .LL_117%=
s_and_b32 s80, s69, 255
s_cbranch_scc0 .LL_118%=
s_bitcmp1_b32 s69, 0
s_cbranch_scc0 .LL_119%=
s_add_u32 s62, s78, 0
buffer_load_dwordx4 v[96:99], v1, s[8:11], s62 offen sc1
.LL_119%=:
s_bitcmp1_b32 s69, 1
s_cbranch_scc0 .LL_120%=
s_add_u32 s62, s78, 5120
buffer_load_dwordx4 v[100:103], v1, s[8:11], s62 offen sc1
.LL_120%=:
s_bitcmp1_b32 s69, 2
s_cbranch_scc0 .LL_121%=
s_add_u32 s62, s78, 10240
buffer_load_dwordx4 v[104:107], v1, s[8:11], s62 offen sc1
.LL_121%=:
s_bitcmp1_b32 s69, 3
s_cbranch_scc0 .LL_122%=
s_add_u32 s62, s78, 15360
buffer_load_dwordx4 v[108:111], v1, s[8:11], s62 offen sc1
.LL_122%=:
s_bitcmp1_b32 s69, 4
s_cbranch_scc0 .LL_123%=
s_add_u32 s62, s78, 20480
buffer_load_dwordx4 v[112:115], v1, s[8:11], s62 offen sc1
.LL_123%=:
s_bitcmp1_b32 s69, 5
s_cbranch_scc0 .LL_124%=
s_add_u32 s62, s78, 25600
buffer_load_dwordx4 v[116:119], v1, s[8:11], s62 offen sc1
.LL_124%=:
s_bitcmp1_b32 s69, 6
s_cbranch_scc0 .LL_125%=
s_add_u32 s62, s78, 30720
buffer_load_dwordx4 v[120:123], v1, s[8:11], s62 offen sc1
.LL_125%=:
s_bitcmp1_b32 s69, 7
s_cbranch_scc0 .LL_126%=
s_add_u32 s62, s78, 35840
buffer_load_dwordx4 v[124:127], v1, s[8:11], s62 offen sc1
.LL_126%=:
.LL_118%=:
s_and_b32 s80, s69, 65280
s_cbranch_scc0 .LL_127%=
s_bitcmp1_b32 s69, 8
s_cbranch_scc0 .LL_128%=
s_add_u32 s62, s79, 0
buffer_load_dwordx4 v[208:211], v1, s[8:11], s62 offen sc1
.LL_128%=:
s_bitcmp1_b32 s69, 9
s_cbranch_scc0 .LL_129%=
s_add_u32 s62, s79, 5120
buffer_load_dwordx4 v[212:215], v1, s[8:11], s62 offen sc1
.LL_129%=:
s_bitcmp1_b32 s69, 10
s_cbranch_scc0 .LL_130%=
s_add_u32 s62, s79, 10240
buffer_load_dwordx4 v[216:219], v1, s[8:11], s62 offen sc1
.LL_130%=:
s_bitcmp1_b32 s69, 11
s_cbranch_scc0 .LL_131%=
s_add_u32 s62, s79, 15360
buffer_load_dwordx4 v[220:223], v1, s[8:11], s62 offen sc1
.LL_131%=:
s_bitcmp1_b32 s69, 12
s_cbranch_scc0 .LL_132%=
s_add_u32 s62, s79, 20480
buffer_load_dwordx4 v[224:227], v1, s[8:11], s62 offen sc1
.LL_132%=:
s_bitcmp1_b32 s69, 13
s_cbranch_scc0 .LL_133%=
s_add_u32 s62, s79, 25600
buffer_load_dwordx4 v[228:231], v1, s[8:11], s62 offen sc1
.LL_133%=:
s_bitcmp1_b32 s69, 14
s_cbranch_scc0 .LL_134%=
s_add_u32 s62, s79, 30720
buffer_load_dwordx4 v[232:235], v1, s[8:11], s62 offen sc1
.LL_134%=:
s_bitcmp1_b32 s69, 15
s_cbranch_scc0 .LL_135%=
s_add_u32 s62, s79, 35840
buffer_load_dwordx4 v[236:239], v1, s[8:11], s62 offen sc1
.LL_135%=:
.LL_127%=:
s_or_b32 s71, s71, s69
s_andn2_b32 s80, 0xffff, s71
s_cbranch_scc0 .LL_116%=
.LL_117%=:
s_sleep 4
buffer_load_dwordx2 v[24:25], v26, s[8:11], s63 offen sc1
s_and_b32 s80, s70, 0x100ff
s_cmp_eq_u32 s80, 0xff
s_cbranch_scc0 .LL_115%=
Zvvv 80,4,96
Zvvv 84,8,96
Mvvv 80,4,100
Mvvv 84,8,100
Mvvv 80,4,104
Mvvv 84,8,104
Mvvv 80,4,108
Mvvv 84,8,108
Mvvv 80,4,112
Mvvv 84,8,112
Mvvv 80,4,116
Mvvv 84,8,116
Mvvv 80,4,120
Mvvv 84,8,120
Mvvv 80,4,124
Mvvv 84,8,124
s_nop 6
K 160,80,81
K 161,82,83
K 162,84,85
K 163,86,87
v_lshlrev_b32 v80, 16, v160
v_and_b32 v81, 0xffff0000, v160
v_lshlrev_b32 v82, 16, v161
v_and_b32 v83, 0xffff0000, v161
v_lshlrev_b32 v84, 16, v162
v_and_b32 v85, 0xffff0000, v162
v_lshlrev_b32 v86, 16, v163
v_and_b32 v87, 0xffff0000, v163
v_pk_add_f32 v[80:81], v[32:33], v[80:81]
v_pk_add_f32 v[82:83], v[34:35], v[82:83]
v_pk_add_f32 v[84:85], v[36:37], v[84:85]
v_pk_add_f32 v[86:87], v[38:39], v[86:87]
K 160,80,81
K 161,82,83
K 162,84,85
K 163,86,87
v_lshlrev_b32 v80, 16, v160
v_and_b32 v81, 0xffff0000, v160
v_lshlrev_b32 v82, 16, v161
v_and_b32 v83, 0xffff0000, v161
v_lshlrev_b32 v84, 16, v162
v_and_b32 v85, 0xffff0000, v162
v_lshlrev_b32 v86, 16, v163
v_and_b32 v87, 0xffff0000, v163
v_pk_add_f32 v[80:81], v[40:41], v[80:81]
v_pk_add_f32 v[82:83], v[42:43], v[82:83]
v_pk_add_f32 v[84:85], v[44:45], v[84:85]
v_pk_add_f32 v[86:87], v[46:47], v[86:87]
K 80,80,81
K 81,82,83
K 82,84,85
K 83,86,87
buffer_store_dwordx4 v[80:83], v20, s[20:23], s72 offen nt
s_bitset1_b32 s71, 16
.LL_115%=:
s_add_u32 s84, s84, 1
s_waitcnt vmcnt(0)
v_cmp_eq_u64 vcc, s[12:13], v[24:25]
s_or_b32 s67, vcc_lo, s66
s_cmp_lt_u32 s84, 4194304
s_cbranch_scc1 .LL_114%=
.LL_116%=:
s_bitcmp1_b32 s71, 16
s_cbranch_scc1 .LL_136%=
s_and_b32 s80, s70, 0xff
s_cmp_eq_u32 s80, 0xff
s_cbranch_scc1 .LL_137%=
s_waitcnt vmcnt(0)
.LL_137%=:
Zvvv 80,4,96
Zvvv 84,8,96
Mvvv 80,4,100
Mvvv 84,8,100
Mvvv 80,4,104
Mvvv 84,8,104
Mvvv 80,4,108
Mvvv 84,8,108
Mvvv 80,4,112
Mvvv 84,8,112
Mvvv 80,4,116
Mvvv 84,8,116
Mvvv 80,4,120
Mvvv 84,8,120
Mvvv 80,4,124
Mvvv 84,8,124
s_nop 6
K 160,80,81
K 161,82,83
K 162,84,85
K 163,86,87
v_lshlrev_b32 v80, 16, v160
v_and_b32 v81, 0xffff0000, v160
v_lshlrev_b32 v82, 16, v161
v_and_b32 v83, 0xffff0000, v161
v_lshlrev_b32 v84, 16, v162
v_and_b32 v85, 0xffff0000, v162
v_lshlrev_b32 v86, 16, v163
v_and_b32 v87, 0xffff0000, v163
v_pk_add_f32 v[80:81], v[32:33], v[80:81]
v_pk_add_f32 v[82:83], v[34:35], v[82:83]
v_pk_add_f32 v[84:85], v[36:37], v[84:85]
v_pk_add_f32 v[86:87], v[38:39], v[86:87]
K 160,80,81
)"
R"(K 161,82,83
K 162,84,85
K 163,86,87
v_lshlrev_b32 v80, 16, v160
v_and_b32 v81, 0xffff0000, v160
v_lshlrev_b32 v82, 16, v161
v_and_b32 v83, 0xffff0000, v161
v_lshlrev_b32 v84, 16, v162
v_and_b32 v85, 0xffff0000, v162
v_lshlrev_b32 v86, 16, v163
v_and_b32 v87, 0xffff0000, v163
v_pk_add_f32 v[80:81], v[40:41], v[80:81]
v_pk_add_f32 v[82:83], v[42:43], v[82:83]
v_pk_add_f32 v[84:85], v[44:45], v[84:85]
v_pk_add_f32 v[86:87], v[46:47], v[86:87]
K 80,80,81
K 81,82,83
K 82,84,85
K 83,86,87
buffer_store_dwordx4 v[80:83], v20, s[20:23], s72 offen nt
s_waitcnt vmcnt(1)
s_nop 0
Zvvv 80,4,208
Zvvv 84,8,208
Mvvv 80,4,212
Mvvv 84,8,212
Mvvv 80,4,216
Mvvv 84,8,216
Mvvv 80,4,220
Mvvv 84,8,220
Mvvv 80,4,224
Mvvv 84,8,224
Mvvv 80,4,228
Mvvv 84,8,228
Mvvv 80,4,232
Mvvv 84,8,232
Mvvv 80,4,236
Mvvv 84,8,236
s_nop 6
K 160,80,81
K 161,82,83
K 162,84,85
K 163,86,87
v_lshlrev_b32 v80, 16, v160
v_and_b32 v81, 0xffff0000, v160
v_lshlrev_b32 v82, 16, v161
v_and_b32 v83, 0xffff0000, v161
v_lshlrev_b32 v84, 16, v162
v_and_b32 v85, 0xffff0000, v162
v_lshlrev_b32 v86, 16, v163
v_and_b32 v87, 0xffff0000, v163
v_pk_add_f32 v[80:81], v[48:49], v[80:81]
v_pk_add_f32 v[82:83], v[50:51], v[82:83]
v_pk_add_f32 v[84:85], v[52:53], v[84:85]
v_pk_add_f32 v[86:87], v[54:55], v[86:87]
K 160,80,81
K 161,82,83
K 162,84,85
K 163,86,87
v_lshlrev_b32 v80, 16, v160
v_and_b32 v81, 0xffff0000, v160
v_lshlrev_b32 v82, 16, v161
v_and_b32 v83, 0xffff0000, v161
v_lshlrev_b32 v84, 16, v162
v_and_b32 v85, 0xffff0000, v162
v_lshlrev_b32 v86, 16, v163
v_and_b32 v87, 0xffff0000, v163
v_pk_add_f32 v[80:81], v[56:57], v[80:81]
v_pk_add_f32 v[82:83], v[58:59], v[82:83]
v_pk_add_f32 v[84:85], v[60:61], v[84:85]
v_pk_add_f32 v[86:87], v[62:63], v[86:87]
K 80,80,81
K 81,82,83
K 82,84,85
K 83,86,87
buffer_store_dwordx4 v[80:83], v20, s[20:23], s73 offen nt
s_branch .LL_111%=
.LL_136%=:
s_waitcnt vmcnt(0)
Zvvv 80,4,208
Zvvv 84,8,208
Mvvv 80,4,212
Mvvv 84,8,212
Mvvv 80,4,216
Mvvv 84,8,216
Mvvv 80,4,220
Mvvv 84,8,220
Mvvv 80,4,224
Mvvv 84,8,224
Mvvv 80,4,228
Mvvv 84,8,228
Mvvv 80,4,232
Mvvv 84,8,232
Mvvv 80,4,236
Mvvv 84,8,236
s_nop 6
K 160,80,81
K 161,82,83
K 162,84,85
K 163,86,87
v_lshlrev_b32 v80, 16, v160
v_and_b32 v81, 0xffff0000, v160
v_lshlrev_b32 v82, 16, v161
v_and_b32 v83, 0xffff0000, v161
v_lshlrev_b32 v84, 16, v162
v_and_b32 v85, 0xffff0000, v162
v_lshlrev_b32 v86, 16, v163
v_and_b32 v87, 0xffff0000, v163
v_pk_add_f32 v[80:81], v[48:49], v[80:81]
v_pk_add_f32 v[82:83], v[50:51], v[82:83]
v_pk_add_f32 v[84:85], v[52:53], v[84:85]
v_pk_add_f32 v[86:87], v[54:55], v[86:87]
K 160,80,81
K 161,82,83
K 162,84,85
K 163,86,87
v_lshlrev_b32 v80, 16, v160
v_and_b32 v81, 0xffff0000, v160
v_lshlrev_b32 v82, 16, v161
v_and_b32 v83, 0xffff0000, v161
v_lshlrev_b32 v84, 16, v162
v_and_b32 v85, 0xffff0000, v162
v_lshlrev_b32 v86, 16, v163
v_and_b32 v87, 0xffff0000, v163
v_pk_add_f32 v[80:81], v[56:57], v[80:81]
v_pk_add_f32 v[82:83], v[58:59], v[82:83]
v_pk_add_f32 v[84:85], v[60:61], v[84:85]
v_pk_add_f32 v[86:87], v[62:63], v[86:87]
K 80,80,81
K 81,82,83
K 82,84,85
K 83,86,87
buffer_store_dwordx4 v[80:83], v20, s[20:23], s73 offen nt
s_branch .LL_111%=
.LL_112%=:
s_mov_b32 s71, 0
s_mov_b32 s84, 0
.LL_138%=:
s_mov_b32 s70, s71
s_andn2_b32 s69, s67, s71
s_and_b32 s69, s69, 0xffff
s_cbranch_scc0 .LL_141%=
s_and_b32 s80, s69, 255
s_cbranch_scc0 .LL_142%=
s_bitcmp1_b32 s69, 0
s_cbranch_scc0 .LL_143%=
s_add_u32 s62, s78, 0
buffer_load_dwordx4 v[96:99], v1, s[8:11], s62 offen sc1
s_add_u32 s62, s65, 0
buffer_load_dwordx4 v[128:131], v1, s[8:11], s62 offen sc1
.LL_143%=:
s_bitcmp1_b32 s69, 1
s_cbranch_scc0 .LL_144%=
s_add_u32 s62, s78, 5120
buffer_load_dwordx4 v[100:103], v1, s[8:11], s62 offen sc1
s_add_u32 s62, s65, 5120
buffer_load_dwordx4 v[132:135], v1, s[8:11], s62 offen sc1
.LL_144%=:
s_bitcmp1_b32 s69, 2
s_cbranch_scc0 .LL_145%=
s_add_u32 s62, s78, 10240
buffer_load_dwordx4 v[104:107], v1, s[8:11], s62 offen sc1
s_add_u32 s62, s65, 10240
buffer_load_dwordx4 v[136:139], v1, s[8:11], s62 offen sc1
.LL_145%=:
s_bitcmp1_b32 s69, 3
s_cbranch_scc0 .LL_146%=
s_add_u32 s62, s78, 15360
buffer_load_dwordx4 v[108:111], v1, s[8:11], s62 offen sc1
s_add_u32 s62, s65, 15360
buffer_load_dwordx4 v[140:143], v1, s[8:11], s62 offen sc1
.LL_146%=:
s_bitcmp1_b32 s69, 4
s_cbranch_scc0 .LL_147%=
s_add_u32 s62, s78, 20480
buffer_load_dwordx4 v[112:115], v1, s[8:11], s62 offen sc1
s_add_u32 s62, s65, 20480
buffer_load_dwordx4 v[144:147], v1, s[8:11], s62 offen sc1
.LL_147%=:
s_bitcmp1_b32 s69, 5
s_cbranch_scc0 .LL_148%=
s_add_u32 s62, s78, 25600
buffer_load_dwordx4 v[116:119], v1, s[8:11], s62 offen sc1
s_add_u32 s62, s65, 25600
buffer_load_dwordx4 v[148:151], v1, s[8:11], s62 offen sc1
.LL_148%=:
s_bitcmp1_b32 s69, 6
s_cbranch_scc0 .LL_149%=
s_add_u32 s62, s78, 30720
buffer_load_dwordx4 v[120:123], v1, s[8:11], s62 offen sc1
s_add_u32 s62, s65, 30720
buffer_load_dwordx4 v[152:155], v1, s[8:11], s62 offen sc1
.LL_149%=:
s_bitcmp1_b32 s69, 7
s_cbranch_scc0 .LL_150%=
s_add_u32 s62, s78, 35840
buffer_load_dwordx4 v[124:127], v1, s[8:11], s62 offen sc1
s_add_u32 s62, s65, 35840
buffer_load_dwordx4 v[156:159], v1, s[8:11], s62 offen sc1
.LL_150%=:
.LL_142%=:
s_and_b32 s80, s69, 65280
s_cbranch_scc0 .LL_151%=
s_bitcmp1_b32 s69, 8
s_cbranch_scc0 .LL_152%=
s_add_u32 s62, s79, 0
buffer_load_dwordx4 v[208:211], v1, s[8:11], s62 offen sc1
.LL_152%=:
s_bitcmp1_b32 s69, 9
s_cbranch_scc0 .LL_153%=
s_add_u32 s62, s79, 5120
buffer_load_dwordx4 v[212:215], v1, s[8:11], s62 offen sc1
.LL_153%=:
s_bitcmp1_b32 s69, 10
s_cbranch_scc0 .LL_154%=
s_add_u32 s62, s79, 10240
buffer_load_dwordx4 v[216:219], v1, s[8:11], s62 offen sc1
.LL_154%=:
s_bitcmp1_b32 s69, 11
s_cbranch_scc0 .LL_155%=
s_add_u32 s62, s79, 15360
buffer_load_dwordx4 v[220:223], v1, s[8:11], s62 offen sc1
.LL_155%=:
s_bitcmp1_b32 s69, 12
s_cbranch_scc0 .LL_156%=
s_add_u32 s62, s79, 20480
buffer_load_dwordx4 v[224:227], v1, s[8:11], s62 offen sc1
.LL_156%=:
s_bitcmp1_b32 s69, 13
s_cbranch_scc0 .LL_157%=
s_add_u32 s62, s79, 25600
buffer_load_dwordx4 v[228:231], v1, s[8:11], s62 offen sc1
.LL_157%=:
s_bitcmp1_b32 s69, 14
s_cbranch_scc0 .LL_158%=
s_add_u32 s62, s79, 30720
buffer_load_dwordx4 v[232:235], v1, s[8:11], s62 offen sc1
.LL_158%=:
s_bitcmp1_b32 s69, 15
s_cbranch_scc0 .LL_159%=
s_add_u32 s62, s79, 35840
buffer_load_dwordx4 v[236:239], v1, s[8:11], s62 offen sc1
.LL_159%=:
.LL_151%=:
s_or_b32 s71, s71, s69
s_andn2_b32 s80, 0xffff, s71
s_cbranch_scc0 .LL_140%=
.LL_141%=:
s_sleep 4
buffer_load_dwordx2 v[24:25], v26, s[8:11], s63 offen sc1
s_and_b32 s80, s70, 0x100ff
s_cmp_eq_u32 s80, 0xff
s_cbranch_scc0 .LL_139%=
Zvvv 80,4,96
Zvvv 84,8,96
Zvvv 88,4,128
Zvvv 92,8,128
Mvvv 80,4,100
Mvvv 84,8,100
Mvvv 88,4,132
Mvvv 92,8,132
Mvvv 80,4,104
Mvvv 84,8,104
Mvvv 88,4,136
Mvvv 92,8,136
Mvvv 80,4,108
Mvvv 84,8,108
Mvvv 88,4,140
Mvvv 92,8,140
Mvvv 80,4,112
Mvvv 84,8,112
Mvvv 88,4,144
Mvvv 92,8,144
Mvvv 80,4,116
Mvvv 84,8,116
Mvvv 88,4,148
Mvvv 92,8,148
Mvvv 80,4,120
Mvvv 84,8,120
Mvvv 88,4,152
Mvvv 92,8,152
Mvvv 80,4,124
Mvvv 84,8,124
Mvvv 88,4,156
Mvvv 92,8,156
s_nop 4
K 160,80,81
K 161,82,83
K 162,84,85
K 163,86,87
v_lshlrev_b32 v80, 16, v160
v_and_b32 v81, 0xffff0000, v160
v_lshlrev_b32 v82, 16, v161
v_and_b32 v83, 0xffff0000, v161
v_lshlrev_b32 v84, 16, v162
v_and_b32 v85, 0xffff0000, v162
v_lshlrev_b32 v86, 16, v163
v_and_b32 v87, 0xffff0000, v163
v_pk_add_f32 v[80:81], v[32:33], v[80:81]
v_pk_add_f32 v[82:83], v[34:35], v[82:83]
v_pk_add_f32 v[84:85], v[36:37], v[84:85]
v_pk_add_f32 v[86:87], v[38:39], v[86:87]
K 160,80,81
K 161,82,83
K 162,84,85
K 163,86,87
v_lshlrev_b32 v80, 16, v160
v_and_b32 v81, 0xffff0000, v160
v_lshlrev_b32 v82, 16, v161
v_and_b32 v83, 0xffff0000, v161
v_lshlrev_b32 v84, 16, v162
v_and_b32 v85, 0xffff0000, v162
v_lshlrev_b32 v86, 16, v163
v_and_b32 v87, 0xffff0000, v163
v_pk_add_f32 v[80:81], v[40:41], v[80:81]
v_pk_add_f32 v[82:83], v[42:43], v[82:83]
v_pk_add_f32 v[84:85], v[44:45], v[84:85]
v_pk_add_f32 v[86:87], v[46:47], v[86:87]
K 80,80,81
K 81,82,83
K 82,84,85
K 83,86,87
buffer_store_dwordx4 v[80:83], v20, s[20:23], s72 offen nt
K 160,88,89
K 161,90,91
K 162,92,93
K 163,94,95
v_lshlrev_b32 v88, 16, v160
v_and_b32 v89, 0xffff0000, v160
v_lshlrev_b32 v90, 16, v161
v_and_b32 v91, 0xffff0000, v161
v_lshlrev_b32 v92, 16, v162
v_and_b32 v93, 0xffff0000, v162
v_lshlrev_b32 v94, 16, v163
v_and_b32 v95, 0xffff0000, v163
v_pk_add_f32 v[88:89], v[64:65], v[88:89]
v_pk_add_f32 v[90:91], v[66:67], v[90:91]
v_pk_add_f32 v[92:93], v[68:69], v[92:93]
v_pk_add_f32 v[94:95], v[70:71], v[94:95]
K 160,88,89
K 161,90,91
K 162,92,93
K 163,94,95
v_lshlrev_b32 v88, 16, v160
v_and_b32 v89, 0xffff0000, v160
v_lshlrev_b32 v90, 16, v161
v_and_b32 v91, 0xffff0000, v161
v_lshlrev_b32 v92, 16, v162
v_and_b32 v93, 0xffff0000, v162
v_lshlrev_b32 v94, 16, v163
v_and_b32 v95, 0xffff0000, v163
v_pk_add_f32 v[88:89], v[72:73], v[88:89]
v_pk_add_f32 v[90:91], v[74:75], v[90:91]
v_pk_add_f32 v[92:93], v[76:77], v[92:93]
v_pk_add_f32 v[94:95], v[78:79], v[94:95]
K 88,88,89
K 89,90,91
K 90,92,93
K 91,94,95
buffer_store_dwordx4 v[88:91], v20, s[20:23], s74 offen nt
s_bitset1_b32 s71, 16
.LL_139%=:
s_add_u32 s84, s84, 1
s_waitcnt vmcnt(0)
v_cmp_eq_u64 vcc, s[12:13], v[24:25]
s_or_b32 s67, vcc_lo, s66
s_cmp_lt_u32 s84, 4194304
s_cbranch_scc1 .LL_138%=
.LL_140%=:
s_bitcmp1_b32 s71, 16
s_cbranch_scc1 .LL_160%=
s_and_b32 s80, s70, 0xff
s_cmp_eq_u32 s80, 0xff
s_cbranch_scc1 .LL_161%=
s_waitcnt vmcnt(0)
.LL_161%=:
Zvvv 80,4,96
Zvvv 84,8,96
Zvvv 88,4,128
Zvvv 92,8,128
Mvvv 80,4,100
Mvvv 84,8,100
Mvvv 88,4,132
Mvvv 92,8,132
Mvvv 80,4,104
Mvvv 84,8,104
Mvvv 88,4,136
Mvvv 92,8,136
Mvvv 80,4,108
Mvvv 84,8,108
Mvvv 88,4,140
Mvvv 92,8,140
Mvvv 80,4,112
Mvvv 84,8,112
Mvvv 88,4,144
Mvvv 92,8,144
Mvvv 80,4,116
Mvvv 84,8,116
Mvvv 88,4,148
Mvvv 92,8,148
)"
R"(Mvvv 80,4,120
Mvvv 84,8,120
Mvvv 88,4,152
Mvvv 92,8,152
Mvvv 80,4,124
Mvvv 84,8,124
Mvvv 88,4,156
Mvvv 92,8,156
s_nop 4
K 160,80,81
K 161,82,83
K 162,84,85
K 163,86,87
v_lshlrev_b32 v80, 16, v160
v_and_b32 v81, 0xffff0000, v160
v_lshlrev_b32 v82, 16, v161
v_and_b32 v83, 0xffff0000, v161
v_lshlrev_b32 v84, 16, v162
v_and_b32 v85, 0xffff0000, v162
v_lshlrev_b32 v86, 16, v163
v_and_b32 v87, 0xffff0000, v163
v_pk_add_f32 v[80:81], v[32:33], v[80:81]
v_pk_add_f32 v[82:83], v[34:35], v[82:83]
v_pk_add_f32 v[84:85], v[36:37], v[84:85]
v_pk_add_f32 v[86:87], v[38:39], v[86:87]
K 160,80,81
K 161,82,83
K 162,84,85
K 163,86,87
v_lshlrev_b32 v80, 16, v160
v_and_b32 v81, 0xffff0000, v160
v_lshlrev_b32 v82, 16, v161
v_and_b32 v83, 0xffff0000, v161
v_lshlrev_b32 v84, 16, v162
v_and_b32 v85, 0xffff0000, v162
v_lshlrev_b32 v86, 16, v163
v_and_b32 v87, 0xffff0000, v163
v_pk_add_f32 v[80:81], v[40:41], v[80:81]
v_pk_add_f32 v[82:83], v[42:43], v[82:83]
v_pk_add_f32 v[84:85], v[44:45], v[84:85]
v_pk_add_f32 v[86:87], v[46:47], v[86:87]
K 80,80,81
K 81,82,83
K 82,84,85
K 83,86,87
buffer_store_dwordx4 v[80:83], v20, s[20:23], s72 offen nt
K 160,88,89
K 161,90,91
K 162,92,93
K 163,94,95
v_lshlrev_b32 v88, 16, v160
v_and_b32 v89, 0xffff0000, v160
v_lshlrev_b32 v90, 16, v161
v_and_b32 v91, 0xffff0000, v161
v_lshlrev_b32 v92, 16, v162
v_and_b32 v93, 0xffff0000, v162
v_lshlrev_b32 v94, 16, v163
v_and_b32 v95, 0xffff0000, v163
v_pk_add_f32 v[88:89], v[64:65], v[88:89]
v_pk_add_f32 v[90:91], v[66:67], v[90:91]
v_pk_add_f32 v[92:93], v[68:69], v[92:93]
v_pk_add_f32 v[94:95], v[70:71], v[94:95]
K 160,88,89
K 161,90,91
K 162,92,93
K 163,94,95
v_lshlrev_b32 v88, 16, v160
v_and_b32 v89, 0xffff0000, v160
v_lshlrev_b32 v90, 16, v161
v_and_b32 v91, 0xffff0000, v161
v_lshlrev_b32 v92, 16, v162
v_and_b32 v93, 0xffff0000, v162
v_lshlrev_b32 v94, 16, v163
v_and_b32 v95, 0xffff0000, v163
v_pk_add_f32 v[88:89], v[72:73], v[88:89]
v_pk_add_f32 v[90:91], v[74:75], v[90:91]
v_pk_add_f32 v[92:93], v[76:77], v[92:93]
v_pk_add_f32 v[94:95], v[78:79], v[94:95]
K 88,88,89
K 89,90,91
K 90,92,93
K 91,94,95
buffer_store_dwordx4 v[88:91], v20, s[20:23], s74 offen nt
s_waitcnt vmcnt(2)
Zvvv 80,4,208
Zvvv 84,8,208
Mvvv 80,4,212
Mvvv 84,8,212
Mvvv 80,4,216
Mvvv 84,8,216
Mvvv 80,4,220
Mvvv 84,8,220
Mvvv 80,4,224
Mvvv 84,8,224
Mvvv 80,4,228
Mvvv 84,8,228
Mvvv 80,4,232
Mvvv 84,8,232
Mvvv 80,4,236
Mvvv 84,8,236
s_nop 6
K 160,80,81
K 161,82,83
K 162,84,85
K 163,86,87
v_lshlrev_b32 v80, 16, v160
v_and_b32 v81, 0xffff0000, v160
v_lshlrev_b32 v82, 16, v161
v_and_b32 v83, 0xffff0000, v161
v_lshlrev_b32 v84, 16, v162
v_and_b32 v85, 0xffff0000, v162
v_lshlrev_b32 v86, 16, v163
v_and_b32 v87, 0xffff0000, v163
v_pk_add_f32 v[80:81], v[48:49], v[80:81]
v_pk_add_f32 v[82:83], v[50:51], v[82:83]
v_pk_add_f32 v[84:85], v[52:53], v[84:85]
v_pk_add_f32 v[86:87], v[54:55], v[86:87]
K 160,80,81
K 161,82,83
K 162,84,85
K 163,86,87
v_lshlrev_b32 v80, 16, v160
v_and_b32 v81, 0xffff0000, v160
v_lshlrev_b32 v82, 16, v161
v_and_b32 v83, 0xffff0000, v161
v_lshlrev_b32 v84, 16, v162
v_and_b32 v85, 0xffff0000, v162
v_lshlrev_b32 v86, 16, v163
v_and_b32 v87, 0xffff0000, v163
v_pk_add_f32 v[80:81], v[56:57], v[80:81]
v_pk_add_f32 v[82:83], v[58:59], v[82:83]
v_pk_add_f32 v[84:85], v[60:61], v[84:85]
v_pk_add_f32 v[86:87], v[62:63], v[86:87]
K 80,80,81
K 81,82,83
K 82,84,85
K 83,86,87
buffer_store_dwordx4 v[80:83], v20, s[20:23], s73 offen nt
s_branch .LL_111%=
.LL_160%=:
s_waitcnt vmcnt(0)
Zvvv 80,4,208
Zvvv 84,8,208
Mvvv 80,4,212
Mvvv 84,8,212
Mvvv 80,4,216
Mvvv 84,8,216
Mvvv 80,4,220
Mvvv 84,8,220
Mvvv 80,4,224
Mvvv 84,8,224
Mvvv 80,4,228
Mvvv 84,8,228
Mvvv 80,4,232
Mvvv 84,8,232
Mvvv 80,4,236
Mvvv 84,8,236
s_nop 6
K 160,80,81
K 161,82,83
K 162,84,85
K 163,86,87
v_lshlrev_b32 v80, 16, v160
v_and_b32 v81, 0xffff0000, v160
v_lshlrev_b32 v82, 16, v161
v_and_b32 v83, 0xffff0000, v161
v_lshlrev_b32 v84, 16, v162
v_and_b32 v85, 0xffff0000, v162
v_lshlrev_b32 v86, 16, v163
v_and_b32 v87, 0xffff0000, v163
v_pk_add_f32 v[80:81], v[48:49], v[80:81]
v_pk_add_f32 v[82:83], v[50:51], v[82:83]
v_pk_add_f32 v[84:85], v[52:53], v[84:85]
v_pk_add_f32 v[86:87], v[54:55], v[86:87]
K 160,80,81
K 161,82,83
K 162,84,85
K 163,86,87
v_lshlrev_b32 v80, 16, v160
v_and_b32 v81, 0xffff0000, v160
v_lshlrev_b32 v82, 16, v161
v_and_b32 v83, 0xffff0000, v161
v_lshlrev_b32 v84, 16, v162
v_and_b32 v85, 0xffff0000, v162
v_lshlrev_b32 v86, 16, v163
v_and_b32 v87, 0xffff0000, v163
v_pk_add_f32 v[80:81], v[56:57], v[80:81]
v_pk_add_f32 v[82:83], v[58:59], v[82:83]
v_pk_add_f32 v[84:85], v[60:61], v[84:85]
v_pk_add_f32 v[86:87], v[62:63], v[86:87]
K 80,80,81
K 81,82,83
K 82,84,85
K 83,86,87
buffer_store_dwordx4 v[80:83], v20, s[20:23], s73 offen nt
s_branch .LL_111%=
.LL_113%=:
s_mov_b32 s71, 0
s_mov_b32 s84, 0
.LL_162%=:
s_mov_b32 s70, s71
s_andn2_b32 s69, s67, s71
s_and_b32 s69, s69, 0xffff
s_cbranch_scc0 .LL_165%=
s_and_b32 s80, s69, 255
s_cbranch_scc0 .LL_166%=
s_bitcmp1_b32 s69, 0
s_cbranch_scc0 .LL_167%=
s_add_u32 s62, s78, 0
buffer_load_dwordx4 v[96:99], v1, s[8:11], s62 offen sc1
.LL_167%=:
s_bitcmp1_b32 s69, 1
s_cbranch_scc0 .LL_168%=
s_add_u32 s62, s78, 5120
buffer_load_dwordx4 v[100:103], v1, s[8:11], s62 offen sc1
.LL_168%=:
s_bitcmp1_b32 s69, 2
s_cbranch_scc0 .LL_169%=
s_add_u32 s62, s78, 10240
buffer_load_dwordx4 v[104:107], v1, s[8:11], s62 offen sc1
.LL_169%=:
s_bitcmp1_b32 s69, 3
s_cbranch_scc0 .LL_170%=
s_add_u32 s62, s78, 15360
buffer_load_dwordx4 v[108:111], v1, s[8:11], s62 offen sc1
.LL_170%=:
s_bitcmp1_b32 s69, 4
s_cbranch_scc0 .LL_171%=
s_add_u32 s62, s78, 20480
buffer_load_dwordx4 v[112:115], v1, s[8:11], s62 offen sc1
.LL_171%=:
s_bitcmp1_b32 s69, 5
s_cbranch_scc0 .LL_172%=
s_add_u32 s62, s78, 25600
buffer_load_dwordx4 v[116:119], v1, s[8:11], s62 offen sc1
.LL_172%=:
s_bitcmp1_b32 s69, 6
s_cbranch_scc0 .LL_173%=
s_add_u32 s62, s78, 30720
buffer_load_dwordx4 v[120:123], v1, s[8:11], s62 offen sc1
.LL_173%=:
s_bitcmp1_b32 s69, 7
s_cbranch_scc0 .LL_174%=
s_add_u32 s62, s78, 35840
buffer_load_dwordx4 v[124:127], v1, s[8:11], s62 offen sc1
.LL_174%=:
.LL_166%=:
s_and_b32 s80, s69, 65280
s_cbranch_scc0 .LL_175%=
s_bitcmp1_b32 s69, 8
s_cbranch_scc0 .LL_176%=
s_add_u32 s62, s79, 0
buffer_load_dwordx4 v[208:211], v1, s[8:11], s62 offen sc1
s_add_u32 s62, s65, 0
buffer_load_dwordx4 v[128:131], v1, s[8:11], s62 offen sc1
.LL_176%=:
s_bitcmp1_b32 s69, 9
s_cbranch_scc0 .LL_177%=
s_add_u32 s62, s79, 5120
buffer_load_dwordx4 v[212:215], v1, s[8:11], s62 offen sc1
s_add_u32 s62, s65, 5120
buffer_load_dwordx4 v[132:135], v1, s[8:11], s62 offen sc1
.LL_177%=:
s_bitcmp1_b32 s69, 10
s_cbranch_scc0 .LL_178%=
s_add_u32 s62, s79, 10240
buffer_load_dwordx4 v[216:219], v1, s[8:11], s62 offen sc1
s_add_u32 s62, s65, 10240
buffer_load_dwordx4 v[136:139], v1, s[8:11], s62 offen sc1
.LL_178%=:
s_bitcmp1_b32 s69, 11
s_cbranch_scc0 .LL_179%=
s_add_u32 s62, s79, 15360
buffer_load_dwordx4 v[220:223], v1, s[8:11], s62 offen sc1
s_add_u32 s62, s65, 15360
buffer_load_dwordx4 v[140:143], v1, s[8:11], s62 offen sc1
.LL_179%=:
s_bitcmp1_b32 s69, 12
s_cbranch_scc0 .LL_180%=
s_add_u32 s62, s79, 20480
buffer_load_dwordx4 v[224:227], v1, s[8:11], s62 offen sc1
s_add_u32 s62, s65, 20480
buffer_load_dwordx4 v[144:147], v1, s[8:11], s62 offen sc1
.LL_180%=:
s_bitcmp1_b32 s69, 13
s_cbranch_scc0 .LL_181%=
s_add_u32 s62, s79, 25600
buffer_load_dwordx4 v[228:231], v1, s[8:11], s62 offen sc1
s_add_u32 s62, s65, 25600
buffer_load_dwordx4 v[148:151], v1, s[8:11], s62 offen sc1
.LL_181%=:
s_bitcmp1_b32 s69, 14
s_cbranch_scc0 .LL_182%=
s_add_u32 s62, s79, 30720
buffer_load_dwordx4 v[232:235], v1, s[8:11], s62 offen sc1
s_add_u32 s62, s65, 30720
buffer_load_dwordx4 v[152:155], v1, s[8:11], s62 offen sc1
.LL_182%=:
s_bitcmp1_b32 s69, 15
s_cbranch_scc0 .LL_183%=
s_add_u32 s62, s79, 35840
buffer_load_dwordx4 v[236:239], v1, s[8:11], s62 offen sc1
s_add_u32 s62, s65, 35840
buffer_load_dwordx4 v[156:159], v1, s[8:11], s62 offen sc1
.LL_183%=:
.LL_175%=:
s_or_b32 s71, s71, s69
s_andn2_b32 s80, 0xffff, s71
s_cbranch_scc0 .LL_164%=
.LL_165%=:
s_sleep 4
buffer_load_dwordx2 v[24:25], v26, s[8:11], s63 offen sc1
s_and_b32 s80, s70, 0x100ff
s_cmp_eq_u32 s80, 0xff
s_cbranch_scc0 .LL_163%=
Zvvv 80,4,96
Zvvv 84,8,96
Mvvv 80,4,100
Mvvv 84,8,100
Mvvv 80,4,104
Mvvv 84,8,104
Mvvv 80,4,108
Mvvv 84,8,108
Mvvv 80,4,112
Mvvv 84,8,112
Mvvv 80,4,116
Mvvv 84,8,116
Mvvv 80,4,120
Mvvv 84,8,120
Mvvv 80,4,124
Mvvv 84,8,124
s_nop 6
K 160,80,81
K 161,82,83
K 162,84,85
K 163,86,87
v_lshlrev_b32 v80, 16, v160
v_and_b32 v81, 0xffff0000, v160
v_lshlrev_b32 v82, 16, v161
v_and_b32 v83, 0xffff0000, v161
v_lshlrev_b32 v84, 16, v162
v_and_b32 v85, 0xffff0000, v162
v_lshlrev_b32 v86, 16, v163
v_and_b32 v87, 0xffff0000, v163
v_pk_add_f32 v[80:81], v[32:33], v[80:81]
v_pk_add_f32 v[82:83], v[34:35], v[82:83]
v_pk_add_f32 v[84:85], v[36:37], v[84:85]
v_pk_add_f32 v[86:87], v[38:39], v[86:87]
K 160,80,81
K 161,82,83
K 162,84,85
K 163,86,87
v_lshlrev_b32 v80, 16, v160
v_and_b32 v81, 0xffff0000, v160
v_lshlrev_b32 v82, 16, v161
v_and_b32 v83, 0xffff0000, v161
v_lshlrev_b32 v84, 16, v162
v_and_b32 v85, 0xffff0000, v162
v_lshlrev_b32 v86, 16, v163
v_and_b32 v87, 0xffff0000, v163
v_pk_add_f32 v[80:81], v[40:41], v[80:81]
v_pk_add_f32 v[82:83], v[42:43], v[82:83]
v_pk_add_f32 v[84:85], v[44:45], v[84:85]
v_pk_add_f32 v[86:87], v[46:47], v[86:87]
K 80,80,81
K 81,82,83
K 82,84,85
K 83,86,87
buffer_store_dwordx4 v[80:83], v20, s[20:23], s72 offen nt
s_bitset1_b32 s71, 16
.LL_163%=:
s_add_u32 s84, s84, 1
s_waitcnt vmcnt(0)
v_cmp_eq_u64 vcc, s[12:13], v[24:25]
s_or_b32 s67, vcc_lo, s66
s_cmp_lt_u32 s84, 4194304
s_cbranch_scc1 .LL_162%=
.LL_164%=:
s_bitcmp1_b32 s71, 16
s_cbranch_scc1 .LL_184%=
s_and_b32 s80, s70, 0xff
s_cmp_eq_u32 s80, 0xff
s_cbranch_scc1 .LL_185%=
s_waitcnt vmcnt(0)
.LL_185%=:
Zvvv 80,4,96
Zvvv 84,8,96
Mvvv 80,4,100
Mvvv 84,8,100
Mvvv 80,4,104
Mvvv 84,8,104
Mvvv 80,4,108
Mvvv 84,8,108
Mvvv 80,4,112
Mvvv 84,8,112
Mvvv 80,4,116
Mvvv 84,8,116
Mvvv 80,4,120
Mvvv 84,8,120
Mvvv 80,4,124
)"
R"(Mvvv 84,8,124
s_nop 6
K 160,80,81
K 161,82,83
K 162,84,85
K 163,86,87
v_lshlrev_b32 v80, 16, v160
v_and_b32 v81, 0xffff0000, v160
v_lshlrev_b32 v82, 16, v161
v_and_b32 v83, 0xffff0000, v161
v_lshlrev_b32 v84, 16, v162
v_and_b32 v85, 0xffff0000, v162
v_lshlrev_b32 v86, 16, v163
v_and_b32 v87, 0xffff0000, v163
v_pk_add_f32 v[80:81], v[32:33], v[80:81]
v_pk_add_f32 v[82:83], v[34:35], v[82:83]
v_pk_add_f32 v[84:85], v[36:37], v[84:85]
v_pk_add_f32 v[86:87], v[38:39], v[86:87]
K 160,80,81
K 161,82,83
K 162,84,85
K 163,86,87
v_lshlrev_b32 v80, 16, v160
v_and_b32 v81, 0xffff0000, v160
v_lshlrev_b32 v82, 16, v161
v_and_b32 v83, 0xffff0000, v161
v_lshlrev_b32 v84, 16, v162
v_and_b32 v85, 0xffff0000, v162
v_lshlrev_b32 v86, 16, v163
v_and_b32 v87, 0xffff0000, v163
v_pk_add_f32 v[80:81], v[40:41], v[80:81]
v_pk_add_f32 v[82:83], v[42:43], v[82:83]
v_pk_add_f32 v[84:85], v[44:45], v[84:85]
v_pk_add_f32 v[86:87], v[46:47], v[86:87]
K 80,80,81
K 81,82,83
K 82,84,85
K 83,86,87
buffer_store_dwordx4 v[80:83], v20, s[20:23], s72 offen nt
s_waitcnt vmcnt(1)
s_nop 0
Zvvv 80,4,208
Zvvv 84,8,208
Zvvv 88,4,128
Zvvv 92,8,128
Mvvv 80,4,212
Mvvv 84,8,212
Mvvv 88,4,132
Mvvv 92,8,132
Mvvv 80,4,216
Mvvv 84,8,216
Mvvv 88,4,136
Mvvv 92,8,136
Mvvv 80,4,220
Mvvv 84,8,220
Mvvv 88,4,140
Mvvv 92,8,140
Mvvv 80,4,224
Mvvv 84,8,224
Mvvv 88,4,144
Mvvv 92,8,144
Mvvv 80,4,228
Mvvv 84,8,228
Mvvv 88,4,148
Mvvv 92,8,148
Mvvv 80,4,232
Mvvv 84,8,232
Mvvv 88,4,152
Mvvv 92,8,152
Mvvv 80,4,236
Mvvv 84,8,236
Mvvv 88,4,156
Mvvv 92,8,156
s_nop 4
K 160,80,81
K 161,82,83
K 162,84,85
K 163,86,87
v_lshlrev_b32 v80, 16, v160
v_and_b32 v81, 0xffff0000, v160
v_lshlrev_b32 v82, 16, v161
v_and_b32 v83, 0xffff0000, v161
v_lshlrev_b32 v84, 16, v162
v_and_b32 v85, 0xffff0000, v162
v_lshlrev_b32 v86, 16, v163
v_and_b32 v87, 0xffff0000, v163
v_pk_add_f32 v[80:81], v[48:49], v[80:81]
v_pk_add_f32 v[82:83], v[50:51], v[82:83]
v_pk_add_f32 v[84:85], v[52:53], v[84:85]
v_pk_add_f32 v[86:87], v[54:55], v[86:87]
K 160,80,81
K 161,82,83
K 162,84,85
K 163,86,87
v_lshlrev_b32 v80, 16, v160
v_and_b32 v81, 0xffff0000, v160
v_lshlrev_b32 v82, 16, v161
v_and_b32 v83, 0xffff0000, v161
v_lshlrev_b32 v84, 16, v162
v_and_b32 v85, 0xffff0000, v162
v_lshlrev_b32 v86, 16, v163
v_and_b32 v87, 0xffff0000, v163
v_pk_add_f32 v[80:81], v[56:57], v[80:81]
v_pk_add_f32 v[82:83], v[58:59], v[82:83]
v_pk_add_f32 v[84:85], v[60:61], v[84:85]
v_pk_add_f32 v[86:87], v[62:63], v[86:87]
K 80,80,81
K 81,82,83
K 82,84,85
K 83,86,87
buffer_store_dwordx4 v[80:83], v20, s[20:23], s73 offen nt
K 160,88,89
K 161,90,91
K 162,92,93
K 163,94,95
v_lshlrev_b32 v88, 16, v160
v_and_b32 v89, 0xffff0000, v160
v_lshlrev_b32 v90, 16, v161
v_and_b32 v91, 0xffff0000, v161
v_lshlrev_b32 v92, 16, v162
v_and_b32 v93, 0xffff0000, v162
v_lshlrev_b32 v94, 16, v163
v_and_b32 v95, 0xffff0000, v163
v_pk_add_f32 v[88:89], v[64:65], v[88:89]
v_pk_add_f32 v[90:91], v[66:67], v[90:91]
v_pk_add_f32 v[92:93], v[68:69], v[92:93]
v_pk_add_f32 v[94:95], v[70:71], v[94:95]
K 160,88,89
K 161,90,91
K 162,92,93
K 163,94,95
v_lshlrev_b32 v88, 16, v160
v_and_b32 v89, 0xffff0000, v160
v_lshlrev_b32 v90, 16, v161
v_and_b32 v91, 0xffff0000, v161
v_lshlrev_b32 v92, 16, v162
v_and_b32 v93, 0xffff0000, v162
v_lshlrev_b32 v94, 16, v163
v_and_b32 v95, 0xffff0000, v163
v_pk_add_f32 v[88:89], v[72:73], v[88:89]
v_pk_add_f32 v[90:91], v[74:75], v[90:91]
v_pk_add_f32 v[92:93], v[76:77], v[92:93]
v_pk_add_f32 v[94:95], v[78:79], v[94:95]
K 88,88,89
K 89,90,91
K 90,92,93
K 91,94,95
buffer_store_dwordx4 v[88:91], v20, s[20:23], s74 offen nt
s_branch .LL_111%=
.LL_184%=:
s_waitcnt vmcnt(0)
Zvvv 80,4,208
Zvvv 84,8,208
Zvvv 88,4,128
Zvvv 92,8,128
Mvvv 80,4,212
Mvvv 84,8,212
Mvvv 88,4,132
Mvvv 92,8,132
Mvvv 80,4,216
Mvvv 84,8,216
Mvvv 88,4,136
Mvvv 92,8,136
Mvvv 80,4,220
Mvvv 84,8,220
Mvvv 88,4,140
Mvvv 92,8,140
Mvvv 80,4,224
Mvvv 84,8,224
Mvvv 88,4,144
Mvvv 92,8,144
Mvvv 80,4,228
Mvvv 84,8,228
Mvvv 88,4,148
Mvvv 92,8,148
Mvvv 80,4,232
Mvvv 84,8,232
Mvvv 88,4,152
Mvvv 92,8,152
Mvvv 80,4,236
Mvvv 84,8,236
Mvvv 88,4,156
Mvvv 92,8,156
s_nop 4
K 160,80,81
K 161,82,83
K 162,84,85
K 163,86,87
v_lshlrev_b32 v80, 16, v160
v_and_b32 v81, 0xffff0000, v160
v_lshlrev_b32 v82, 16, v161
v_and_b32 v83, 0xffff0000, v161
v_lshlrev_b32 v84, 16, v162
v_and_b32 v85, 0xffff0000, v162
v_lshlrev_b32 v86, 16, v163
v_and_b32 v87, 0xffff0000, v163
v_pk_add_f32 v[80:81], v[48:49], v[80:81]
v_pk_add_f32 v[82:83], v[50:51], v[82:83]
v_pk_add_f32 v[84:85], v[52:53], v[84:85]
v_pk_add_f32 v[86:87], v[54:55], v[86:87]
K 160,80,81
K 161,82,83
K 162,84,85
K 163,86,87
v_lshlrev_b32 v80, 16, v160
v_and_b32 v81, 0xffff0000, v160
v_lshlrev_b32 v82, 16, v161
v_and_b32 v83, 0xffff0000, v161
v_lshlrev_b32 v84, 16, v162
v_and_b32 v85, 0xffff0000, v162
v_lshlrev_b32 v86, 16, v163
v_and_b32 v87, 0xffff0000, v163
v_pk_add_f32 v[80:81], v[56:57], v[80:81]
v_pk_add_f32 v[82:83], v[58:59], v[82:83]
v_pk_add_f32 v[84:85], v[60:61], v[84:85]
v_pk_add_f32 v[86:87], v[62:63], v[86:87]
K 80,80,81
K 81,82,83
K 82,84,85
K 83,86,87
buffer_store_dwordx4 v[80:83], v20, s[20:23], s73 offen nt
K 160,88,89
K 161,90,91
K 162,92,93
K 163,94,95
v_lshlrev_b32 v88, 16, v160
v_and_b32 v89, 0xffff0000, v160
v_lshlrev_b32 v90, 16, v161
v_and_b32 v91, 0xffff0000, v161
v_lshlrev_b32 v92, 16, v162
v_and_b32 v93, 0xffff0000, v162
v_lshlrev_b32 v94, 16, v163
v_and_b32 v95, 0xffff0000, v163
v_pk_add_f32 v[88:89], v[64:65], v[88:89]
v_pk_add_f32 v[90:91], v[66:67], v[90:91]
v_pk_add_f32 v[92:93], v[68:69], v[92:93]
v_pk_add_f32 v[94:95], v[70:71], v[94:95]
K 160,88,89
K 161,90,91
K 162,92,93
K 163,94,95
v_lshlrev_b32 v88, 16, v160
v_and_b32 v89, 0xffff0000, v160
v_lshlrev_b32 v90, 16, v161
v_and_b32 v91, 0xffff0000, v161
v_lshlrev_b32 v92, 16, v162
v_and_b32 v93, 0xffff0000, v162
v_lshlrev_b32 v94, 16, v163
v_and_b32 v95, 0xffff0000, v163
v_pk_add_f32 v[88:89], v[72:73], v[88:89]
v_pk_add_f32 v[90:91], v[74:75], v[90:91]
v_pk_add_f32 v[92:93], v[76:77], v[92:93]
v_pk_add_f32 v[94:95], v[78:79], v[94:95]
K 88,88,89
K 89,90,91
K 90,92,93
K 91,94,95
buffer_store_dwordx4 v[88:91], v20, s[20:23], s74 offen nt
.LL_111%=:
s_lshl_b32 s80, s15, 2
s_add_u32 s80, s80, s14
s_cmp_eq_u32 s80, 4
s_cbranch_scc0 .LL_186%=
v_mov_b64 v[28:29], s[12:13]
s_mov_b64 exec, 1
buffer_store_dwordx2 v[28:29], off, s[8:11], 0 sc1
s_mov_b64 exec, -1
.LL_186%=:
s_branch .Lend_187%=
.Lup_104%=:
s_waitcnt vmcnt(5)
s_mov_b32 s84, 0
.LL_106%=:
v_min3_u32 v176, v161, v163, v165
v_max3_u32 v177, v161, v163, v165
v_min3_u32 v176, v176, v167, v169
v_max3_u32 v177, v177, v167, v169
v_min3_u32 v176, v176, v171, v173
v_max3_u32 v177, v177, v171, v173
v_min_u32 v176, v176, v175
v_max_u32 v177, v177, v175
v_cmp_eq_u32 vcc, s13, v176
v_cmp_eq_u32_e64 s[86:87], s13, v177
s_and_b64 vcc, vcc, s[86:87]
s_cmp_eq_u64 vcc, -1
s_cbranch_scc1 .LL_107%=
s_add_u32 s84, s84, 1
s_cmp_lt_u32 s84, 1048576
s_cbranch_scc1 .LL_108%=
.LL_107%=:
v_readlane_b32 s70, v12, s69
v_bfe_u32 v2, v1, 4, 4
s_nop 2
s_lshr_b32 s80, s70, 4
s_and_b32 s70, s70, 15
v_cmp_eq_u32 vcc, s70, v2
s_cmp_eq_u32 s80, s61
s_cselect_b64 s[86:87], vcc, 0
s_cselect_b64 s[88:89], 0, vcc
s_bitcmp1_b32 s14, 0
s_cselect_b64 vcc, s[88:89], s[86:87]
v_lshlrev_b32 v176, 16, v160
v_and_b32 v177, 0xffff0000, v160
v_lshlrev_b32 v178, 16, v162
v_and_b32 v179, 0xffff0000, v162
v_lshlrev_b32 v180, 16, v164
v_and_b32 v181, 0xffff0000, v164
v_lshlrev_b32 v182, 16, v166
v_and_b32 v183, 0xffff0000, v166
A 176,176,40
A 177,177,41
A 178,178,42
A 179,179,43
A 180,180,44
A 181,181,45
A 182,182,46
A 183,183,47
K 184,176,177
K 185,178,179
K 186,180,181
K 187,182,183
v_lshlrev_b32 v176, 16, v184
v_and_b32 v177, 0xffff0000, v184
v_lshlrev_b32 v178, 16, v185
v_and_b32 v179, 0xffff0000, v185
v_lshlrev_b32 v180, 16, v186
v_and_b32 v181, 0xffff0000, v186
v_lshlrev_b32 v182, 16, v187
v_and_b32 v183, 0xffff0000, v187
v_cndmask_b32_e64 v40, v40, v176, s[86:87]
v_cndmask_b32_e64 v41, v41, v177, s[86:87]
v_cndmask_b32_e64 v42, v42, v178, s[86:87]
v_cndmask_b32_e64 v43, v43, v179, s[86:87]
v_cndmask_b32_e64 v44, v44, v180, s[86:87]
v_cndmask_b32_e64 v45, v45, v181, s[86:87]
v_cndmask_b32_e64 v46, v46, v182, s[86:87]
v_cndmask_b32_e64 v47, v47, v183, s[86:87]
v_lshlrev_b32 v176, 16, v160
v_and_b32 v177, 0xffff0000, v160
v_lshlrev_b32 v178, 16, v162
v_and_b32 v179, 0xffff0000, v162
v_lshlrev_b32 v180, 16, v164
v_and_b32 v181, 0xffff0000, v164
v_lshlrev_b32 v182, 16, v166
v_and_b32 v183, 0xffff0000, v166
A 176,176,56
A 177,177,57
A 178,178,58
A 179,179,59
A 180,180,60
A 181,181,61
A 182,182,62
A 183,183,63
K 184,176,177
K 185,178,179
K 186,180,181
K 187,182,183
v_lshlrev_b32 v176, 16, v184
v_and_b32 v177, 0xffff0000, v184
v_lshlrev_b32 v178, 16, v185
v_and_b32 v179, 0xffff0000, v185
v_lshlrev_b32 v180, 16, v186
v_and_b32 v181, 0xffff0000, v186
v_lshlrev_b32 v182, 16, v187
v_and_b32 v183, 0xffff0000, v187
v_cndmask_b32_e64 v56, v56, v176, s[88:89]
v_cndmask_b32_e64 v57, v57, v177, s[88:89]
v_cndmask_b32_e64 v58, v58, v178, s[88:89]
v_cndmask_b32_e64 v59, v59, v179, s[88:89]
v_cndmask_b32_e64 v60, v60, v180, s[88:89]
v_cndmask_b32_e64 v61, v61, v181, s[88:89]
v_cndmask_b32_e64 v62, v62, v182, s[88:89]
v_cndmask_b32_e64 v63, v63, v183, s[88:89]
v_lshlrev_b32 v176, 16, v168
v_and_b32 v177, 0xffff0000, v168
v_lshlrev_b32 v178, 16, v170
v_and_b32 v179, 0xffff0000, v170
v_lshlrev_b32 v180, 16, v172
v_and_b32 v181, 0xffff0000, v172
v_lshlrev_b32 v182, 16, v174
v_and_b32 v183, 0xffff0000, v174
A 176,176,72
A 177,177,73
A 178,178,74
A 179,179,75
A 180,180,76
A 181,181,77
A 182,182,78
A 183,183,79
K 184,176,177
K 185,178,179
K 186,180,181
K 187,182,183
v_lshlrev_b32 v176, 16, v184
v_and_b32 v177, 0xffff0000, v184
v_lshlrev_b32 v178, 16, v185
v_and_b32 v179, 0xffff0000, v185
v_lshlrev_b32 v180, 16, v186
v_and_b32 v181, 0xffff0000, v186
v_lshlrev_b32 v182, 16, v187
)"
R"(v_and_b32 v183, 0xffff0000, v187
v_cndmask_b32_e64 v72, v72, v176, vcc
v_cndmask_b32_e64 v73, v73, v177, vcc
v_cndmask_b32_e64 v74, v74, v178, vcc
v_cndmask_b32_e64 v75, v75, v179, vcc
v_cndmask_b32_e64 v76, v76, v180, vcc
v_cndmask_b32_e64 v77, v77, v181, vcc
v_cndmask_b32_e64 v78, v78, v182, vcc
v_cndmask_b32_e64 v79, v79, v183, vcc
s_bitset0_b32 s68, s69
s_cmp_eq_u32 s68, 0
s_cbranch_scc1 .Lupr_105%=
s_ff1_i32_b32 s69, s68
.LL_108%=:
s_cmp_eq_u32 s64, 0
s_cbranch_scc0 .LL_109%=
s_waitcnt vmcnt(0)
v_lshlrev_b32 v2, 2, v1
v_mov_b64 v[28:29], s[12:13]
s_mov_b64 exec, 3
buffer_store_dwordx2 v[28:29], v2, s[8:11], s53 offen offset:512 sc1
s_mov_b64 exec, -1
s_mov_b32 s64, 1
.LL_109%=:
s_mul_i32 s66, s69, 20
s_mul_i32 s67, s58, 5
s_add_u32 s66, s66, s67
s_lshl_b32 s66, s66, 7
s_add_u32 s66, s66, 98304
s_lshl_b32 s67, s14, 7
s_add_u32 s67, s67, s66
buffer_load_dwordx4 v[160:163], v3, s[8:11], s67 offen offset:0 sc1
buffer_load_dwordx4 v[164:167], v3, s[8:11], s67 offen offset:16 sc1
buffer_load_dwordx4 v[168:171], v3, s[8:11], s66 offen offset:512 sc1
buffer_load_dwordx4 v[172:175], v3, s[8:11], s66 offen offset:528 sc1
s_waitcnt vmcnt(0)
s_branch .LL_106%=
.Lspin_54%=:
s_mov_b32 s84, 0
.Llp_56%=:
s_sleep 2
v_mov_b32 v28, s74
buffer_load_dwordx2 v[28:29], v28, s[8:11], 0 offen offset:3584 sc1
s_waitcnt vmcnt(0)
s_add_u32 s84, s84, 1
v_xor_b32 v28, s12, v28
v_xor_b32 v29, s13, v29
v_lshrrev_b32 v248, 11, v28
v_or_b32 v29, v29, v248
v_cmp_eq_u32 vcc, 0, v29
s_or_b64 vcc, vcc, s[72:73]
s_cmp_eq_u64 vcc, -1
s_cbranch_scc1 .Lok_55%=
s_cmp_lt_u32 s84, 1048576
s_cbranch_scc1 .Llp_56%=
s_branch .Lok_55%=
.Lspin_59%=:
s_mov_b32 s84, 0
.Llp_61%=:
s_sleep 2
v_bfe_u32 v28, v1, 4, 2
v_lshlrev_b32 v28, 3, v28
s_lshl_b32 s80, s56, 9
s_add_u32 s80, s80, 4096
buffer_load_dwordx2 v[28:29], v28, s[8:11], s80 offen sc1
s_waitcnt vmcnt(0)
s_add_u32 s84, s84, 1
v_cmp_eq_u64 vcc, s[12:13], v[28:29]
s_cmp_eq_u64 vcc, -1
s_cbranch_scc1 .Lok_60%=
s_cmp_lt_u32 s84, 1048576
s_cbranch_scc1 .Llp_61%=
s_branch .Lok_60%=
.Lspin_62%=:
s_mov_b32 s84, 0
.Llp_64%=:
s_sleep 2
v_bfe_u32 v28, v1, 4, 4
v_lshlrev_b32 v28, 3, v28
s_lshl_b32 s80, s56, 9
s_add_u32 s80, s80, 4096
buffer_load_dwordx2 v[28:29], v28, s[8:11], s80 offen sc1
s_waitcnt vmcnt(0)
s_add_u32 s84, s84, 1
v_cmp_eq_u64 vcc, s[12:13], v[28:29]
s_cmp_eq_u64 vcc, -1
s_cbranch_scc1 .Lok_63%=
s_cmp_lt_u32 s84, 1048576
s_cbranch_scc1 .Llp_64%=
s_branch .Lok_63%=
.Lspin_65%=:
s_mov_b32 s84, 0
.Llp_67%=:
s_sleep 2
v_bfe_u32 v28, v1, 4, 4
v_lshlrev_b32 v28, 3, v28
s_lshl_b32 s80, s56, 9
s_add_u32 s80, s80, 4224
buffer_load_dwordx2 v[28:29], v28, s[8:11], s80 offen sc1
s_waitcnt vmcnt(0)
s_add_u32 s84, s84, 1
v_cmp_eq_u64 vcc, s[12:13], v[28:29]
s_cmp_eq_u64 vcc, -1
s_cbranch_scc1 .Lok_66%=
s_cmp_lt_u32 s84, 1048576
s_cbranch_scc1 .Llp_67%=
s_branch .Lok_66%=
.Lspin_68%=:
s_mov_b32 s84, 0
.Llp_70%=:
s_sleep 2
v_bfe_u32 v28, v1, 4, 4
v_lshlrev_b32 v28, 3, v28
s_lshl_b32 s80, s56, 9
s_add_u32 s80, s80, 4352
buffer_load_dwordx2 v[28:29], v28, s[8:11], s80 offen sc1
s_waitcnt vmcnt(0)
s_add_u32 s84, s84, 1
v_cmp_eq_u64 vcc, s[12:13], v[28:29]
s_cmp_eq_u64 vcc, -1
s_cbranch_scc1 .Lok_69%=
s_cmp_lt_u32 s84, 1048576
s_cbranch_scc1 .Llp_70%=
s_branch .Lok_69%=
.Lspin_71%=:
s_mov_b32 s84, 0
.Llp_73%=:
s_sleep 2
v_bfe_u32 v28, v1, 4, 4
v_lshlrev_b32 v28, 3, v28
s_lshl_b32 s80, s56, 9
s_add_u32 s80, s80, 4480
buffer_load_dwordx2 v[28:29], v28, s[8:11], s80 offen sc1
s_waitcnt vmcnt(0)
s_add_u32 s84, s84, 1
v_cmp_eq_u64 vcc, s[12:13], v[28:29]
s_cmp_eq_u64 vcc, -1
s_cbranch_scc1 .Lok_72%=
s_cmp_lt_u32 s84, 1048576
s_cbranch_scc1 .Llp_73%=
s_branch .Lok_72%=
.Lspin_74%=:
s_mov_b32 s84, 0
.Llp_76%=:
s_sleep 2
buffer_load_dwordx2 v[8:9], v4, s[8:11], 0 offen offset:2560 sc1
buffer_load_dwordx2 v[10:11], v5, s[8:11], 0 offen offset:3072 sc1
buffer_load_dwordx2 v[12:13], v6, s[8:11], 0 offen offset:3584 sc1
s_waitcnt vmcnt(0)
s_add_u32 s84, s84, 1
v_cmp_eq_u64 vcc, s[12:13], v[8:9]
v_cmp_eq_u64_e64 s[86:87], s[12:13], v[10:11]
s_and_b64 vcc, vcc, s[86:87]
s_or_b64 vcc, vcc, s[20:21]
s_cmp_eq_u64 vcc, -1
s_cbranch_scc1 .Lok_75%=
s_cmp_lt_u32 s84, 1048576
s_cbranch_scc1 .Llp_76%=
s_branch .Lok_75%=
.Lmk_77%=:
s_nop 7
v_cndmask_b32_e64 v168, v241, v168, s[72:73]
v_cndmask_b32_e64 v169, v241, v169, s[74:75]
v_cndmask_b32_e64 v170, v241, v170, s[76:77]
v_cndmask_b32_e64 v171, v241, v171, s[78:79]
v_cndmask_b32_e64 v188, v241, v188, s[72:73]
v_cndmask_b32_e64 v189, v241, v189, s[74:75]
v_cndmask_b32_e64 v190, v241, v190, s[76:77]
v_cndmask_b32_e64 v191, v241, v191, s[78:79]
v_mov_b32 v184, v241
v_mov_b32 v185, v241
v_mov_b32 v186, v241
v_mov_b32 v187, v241
s_branch .Lmkr_78%=
.Lpd_81%=:
v_mov_b64 v[28:29], s[12:13]
s_mov_b64 exec, 1
buffer_store_dwordx2 v[28:29], off, s[8:11], s69 sc1
s_mov_b64 exec, -1
s_branch .Lpdr_82%=
.Lspin_83%=:
s_mov_b32 s84, 0
.Llp_85%=:
s_sleep 2
v_bfe_u32 v28, v1, 4, 2
v_lshlrev_b32 v28, 3, v28
buffer_load_dwordx2 v[28:29], v28, s[8:11], s55 offen sc1
s_waitcnt vmcnt(0)
s_add_u32 s84, s84, 1
v_cmp_eq_u64 vcc, s[12:13], v[28:29]
s_cmp_eq_u64 vcc, -1
s_cbranch_scc1 .Lok_84%=
s_cmp_lt_u32 s84, 1048576
s_cbranch_scc1 .Llp_85%=
s_branch .Lok_84%=
.LL_88%=:
s_mov_b32 s84, 0
.LL_90%=:
s_sleep 2
buffer_load_dwordx2 v[12:13], v27, s[8:11], 0 offen offset:3584 sc1
s_waitcnt vmcnt(0)
s_add_u32 s84, s84, 1
v_xor_b32 v12, s12, v12
v_xor_b32 v13, s13, v13
v_lshrrev_b32 v3, 11, v12
v_or_b32 v13, v13, v3
v_cmp_eq_u32 vcc, 0, v13
s_cmp_eq_u64 vcc, -1
s_cbranch_scc1 .LL_89%=
s_cmp_lt_u32 s84, 1048576
s_cbranch_scc1 .LL_90%=
s_branch .LL_89%=
.Lup_86%=:
v_lshrrev_b32 v3, 8, v1
v_lshlrev_b32 v3, 5, v3
s_ff1_i32_b32 s69, s68
s_mul_i32 s66, s69, 20
s_mul_i32 s67, s58, 5
s_add_u32 s66, s66, s67
s_lshl_b32 s66, s66, 7
s_add_u32 s66, s66, 98304
s_lshl_b32 s67, s14, 7
s_add_u32 s67, s67, s66
buffer_load_dwordx4 v[160:163], v3, s[8:11], s67 offen offset:0 sc1
buffer_load_dwordx4 v[164:167], v3, s[8:11], s67 offen offset:16 sc1
buffer_load_dwordx4 v[168:171], v3, s[8:11], s66 offen offset:512 sc1
buffer_load_dwordx4 v[172:175], v3, s[8:11], s66 offen offset:528 sc1
s_branch .Lupr_87%=
.Lpd_91%=:
v_mov_b64 v[28:29], s[12:13]
s_mov_b64 exec, 1
buffer_store_dwordx2 v[28:29], off, s[8:11], s69 sc1
s_mov_b64 exec, -1
s_branch .Lpdr_92%=
.Lspin_93%=:
s_mov_b32 s84, 0
.Llp_95%=:
s_sleep 2
v_bfe_u32 v28, v1, 4, 2
v_lshlrev_b32 v28, 3, v28
buffer_load_dwordx2 v[28:29], v28, s[8:11], s54 offen sc1
s_waitcnt vmcnt(0)
s_add_u32 s84, s84, 1
v_cmp_eq_u64 vcc, s[12:13], v[28:29]
s_cmp_eq_u64 vcc, -1
s_cbranch_scc1 .Lok_94%=
s_cmp_lt_u32 s84, 1048576
s_cbranch_scc1 .Llp_95%=
s_branch .Lok_94%=
.Lspin_96%=:
s_mov_b32 s84, 0
.Llp_98%=:
s_sleep 2
v_bfe_u32 v28, v1, 4, 2
v_lshlrev_b32 v28, 3, v28
buffer_load_dwordx2 v[28:29], v28, s[8:11], s55 offen sc1
s_waitcnt vmcnt(0)
s_add_u32 s84, s84, 1
v_cmp_eq_u64 vcc, s[12:13], v[28:29]
s_cmp_eq_u64 vcc, -1
s_cbranch_scc1 .Lok_97%=
s_cmp_lt_u32 s84, 1048576
s_cbranch_scc1 .Llp_98%=
s_branch .Lok_97%=
.LL_101%=:
s_mov_b32 s84, 0
.LL_103%=:
s_sleep 2
buffer_load_dwordx2 v[12:13], v27, s[8:11], 0 offen offset:3584 sc1
s_waitcnt vmcnt(0)
s_add_u32 s84, s84, 1
v_xor_b32 v12, s12, v12
v_xor_b32 v13, s13, v13
v_lshrrev_b32 v3, 11, v12
v_or_b32 v13, v13, v3
v_cmp_eq_u32 vcc, 0, v13
s_cmp_eq_u64 vcc, -1
s_cbranch_scc1 .LL_102%=
s_cmp_lt_u32 s84, 1048576
s_cbranch_scc1 .LL_103%=
s_branch .LL_102%=
.Lup_99%=:
v_lshrrev_b32 v3, 8, v1
v_lshlrev_b32 v3, 5, v3
s_ff1_i32_b32 s69, s68
s_mul_i32 s66, s69, 20
s_mul_i32 s67, s58, 5
s_add_u32 s66, s66, s67
s_lshl_b32 s66, s66, 7
s_add_u32 s66, s66, 98304
s_lshl_b32 s67, s14, 7
s_add_u32 s67, s67, s66
buffer_load_dwordx4 v[160:163], v3, s[8:11], s67 offen offset:0 sc1
buffer_load_dwordx4 v[164:167], v3, s[8:11], s67 offen offset:16 sc1
buffer_load_dwordx4 v[168:171], v3, s[8:11], s66 offen offset:512 sc1
buffer_load_dwordx4 v[172:175], v3, s[8:11], s66 offen offset:528 sc1
s_branch .Lupr_100%=
.Lend_187%=:
.Lend%=:
.purgem W
.purgem D
.purgem Ta
.purgem Ra
.purgem Zvaa
.purgem Mvaa
.purgem Zaaa
.purgem Maaa
.purgem A
.purgem U
.purgem K
.purgem Rv
.purgem Mvav
.purgem G
.purgem Dc
.purgem Zvav
.purgem Cvav
.purgem X
.purgem Cvaa
.purgem Maav
.purgem Mvvv
.purgem Zvvv
)"
    : : [ka] "s"(__builtin_amdgcn_kernarg_segment_ptr()), [bid] "s"(blockIdx.x), [tid] "v"(threadIdx.x)
    : "v1", "v2", "v3", "v4", "v5", "v6", "v7", "v8", "v9", "v10", "v11", "v12", "v13", "v14", "v15", "v16", "v17", "v18", "v19", "v20", "v21", "v22", "v23", "v24", "v25", "v26", "v27", "v28", "v29", "v30", "v31", "v32", "v33", "v34", "v35", "v36", "v37", "v38", "v39", "v40", "v41", "v42", "v43", "v44", "v45", "v46", "v47", "v48", "v49", "v50", "v51", "v52", "v53", "v54", "v55", "v56", "v57", "v58", "v59", "v60", "v61", "v62", "v63", "v64", "v65", "v66", "v67", "v68", "v69", "v70", "v71", "v72", "v73", "v74", "v75", "v76", "v77", "v78", "v79", "v80", "v81", "v82", "v83", "v84", "v85", "v86", "v87", "v88", "v89", "v90", "v91", "v92", "v93", "v94", "v95", "v96", "v97", "v98", "v99", "v100", "v101", "v102", "v103", "v104", "v105", "v106", "v107", "v108", "v109", "v110", "v111", "v112", "v113", "v114", "v115", "v116", "v117", "v118", "v119", "v120", "v121", "v122", "v123", "v124", "v125", "v126", "v127", "v128", "v129", "v130", "v131", "v132", "v133", "v134", "v135", "v136", "v137", "v138", "v139", "v140", "v141", "v142", "v143", "v144", "v145", "v146", "v147", "v148", "v149", "v150", "v151", "v152", "v153", "v154", "v155", "v156", "v157", "v158", "v159", "v160", "v161", "v162", "v163", "v164", "v165", "v166", "v167", "v168", "v169", "v170", "v171", "v172", "v173", "v174", "v175", "v176", "v177", "v178", "v179", "v180", "v181", "v182", "v183", "v184", "v185", "v186", "v187", "v188", "v189", "v190", "v191", "v192", "v193", "v194", "v195", "v196", "v197", "v198", "v199", "v200", "v201", "v202", "v203", "v204", "v205", "v206", "v207", "v208", "v209", "v210", "v211", "v212", "v213", "v214", "v215", "v216", "v217", "v218", "v219", "v220", "v221", "v222", "v223", "v224", "v225", "v226", "v227", "v228", "v229", "v230", "v231", "v232", "v233", "v234", "v235", "v236", "v237", "v238", "v239", "v240", "v241", "v242", "v243", "v244", "v245", "v246", "v247", "v248", "v249", "v250", "v251", "v252", "v253", "v254", "v255", "a0", "a1", "a2", "a3", "a4", "a5", "a6", "a7", "a8", "a9", "a10", "a11", "a12", "a13", "a14", "a15", "a16", "a17", "a18", "a19", "a20", "a21", "a22", "a23", "a24", "a25", "a26", "a27", "a28", "a29", "a30", "a31", "a32", "a33", "a34", "a35", "a36", "a37", "a38", "a39", "a40", "a41", "a42", "a43", "a44", "a45", "a46", "a47", "a48", "a49", "a50", "a51", "a52", "a53", "a54", "a55", "a56", "a57", "a58", "a59", "a60", "a61", "a62", "a63", "a64", "a65", "a66", "a67", "a68", "a69", "a70", "a71", "a72", "a73", "a74", "a75", "a76", "a77", "a78", "a79", "a80", "a81", "a82", "a83", "a84", "a85", "a86", "a87", "a88", "a89", "a90", "a91", "a92", "a93", "a94", "a95", "a96", "a97", "a98", "a99", "a100", "a101", "a102", "a103", "a104", "a105", "a106", "a107", "a108", "a109", "a110", "a111", "a112", "a113", "a114", "a115", "a116", "a117", "a118", "a119", "a120", "a121", "a122", "a123", "a124", "a125", "a126", "a127", "a128", "a129", "a130", "a131", "a132", "a133", "a134", "a135", "a136", "a137", "a138", "a139", "a140", "a141", "a142", "a143", "a144", "a145", "a146", "a147", "a148", "a149", "a150", "a151", "a152", "a153", "a154", "a155", "a156", "a157", "a158", "a159", "a160", "a161", "a162", "a163", "a164", "a165", "a166", "a167", "a168", "a169", "a170", "a171", "a172", "a173", "a174", "a175", "a176", "a177", "a178", "a179", "a180", "a181", "a182", "a183", "a184", "a185", "a186", "a187", "a188", "a189", "a190", "a191", "a192", "a193", "a194", "a195", "a196", "a197", "a198", "a199", "a200", "a201", "a202", "a203", "a204", "a205", "a206", "a207", "a208", "a209", "a210", "a211", "a212", "a213", "a214", "a215", "a216", "a217", "a218", "a219", "a220", "a221", "a222", "a223", "a224", "a225", "a226", "a227", "a228", "a229", "a230", "a231", "a232", "a233", "a234", "a235", "a236", "a237", "a238", "a239", "a240", "a241", "a242", "a243", "a244", "a245", "a246", "a247", "a248", "a249", "a250", "a251", "a252", "a253", "a254", "a255", "s4", "s5", "s6", "s7", "s8", "s9", "s10", "s11", "s12", "s13", "s14", "s15", "s16", "s17", "s18", "s19", "s20", "s21", "s22", "s23", "s24", "s25", "s26", "s27", "s28", "s29", "s30", "s31", "s32", "s33", "s34", "s35", "s36", "s37", "s38", "s39", "s40", "s41", "s42", "s43", "s44", "s45", "s46", "s47", "s48", "s49", "s50", "s51", "s52", "s53", "s54", "s55", "s56", "s57", "s58", "s59", "s60", "s61", "s62", "s63", "s64", "s65", "s66", "s67", "s68", "s69", "s70", "s71", "s72", "s73", "s74", "s75", "s76", "s77", "s78", "s79", "s80", "s81", "s82", "s83", "s84", "s85", "s86", "s87", "s88", "s89", "s90", "s91", "s92", "s93", "s94", "s95", "s96", "s97", "s98", "s99", "vcc", "scc", "memory"
  );
}
extern "C" void kernel_launch(void* const* d_in, const int* in_sizes, int n_in, void* d_out, int out_size, void* d_ws, size_t ws_size, hipStream_t stream) {
  (void)hipFuncSetAttribute((const void*)mega_kernel, hipFuncAttributeMaxDynamicSharedMemorySize, LDS_BYTES);
  mega_kernel<<<256, 256, LDS_BYTES, stream>>>((const bf16*)d_in[0], (const bf16*)d_in[1], (const bf16*)d_in[2], (const bf16*)d_in[3], (const bf16*)d_in[4], (const bf16*)d_in[5],
      (const bf16*)d_in[7], (const bf16*)d_in[8], (const bf16*)d_in[9], (const bf16*)d_in[10], (char*)d_ws + 0, (bf16*)d_out);
}
```

```cpp
#include <hip/hip_runtime.h>
#include <hip/hip_bf16.h>
typedef __hip_bfloat16 bf16;
constexpr int LDS_BYTES = 163840;
__global__ __launch_bounds__(256, 1) void mega_kernel(const bf16* x, const bf16* ckv, const bf16* wq, const bf16* wuk, const bf16* wuv, const bf16* wo,
    const bf16* w_r, const bf16* wg, const bf16* wu, const bf16* wd, char* ws, bf16* out) {
  asm volatile(
R"(.macro W n
s_waitcnt lgkmcnt(\n)
.endm
.macro D v,r,s,o
buffer_load_dwordx4 v\v, s[\r:\r+3], s\s offen offset:\o lds
.endm
.macro Ta d,a,o
ds_read_b64_tr_b16 a[\d:\d+1], v\a offset:\o
.endm
.macro Ra d,a,o
ds_read_b128 a[\d:\d+3], v\a offset:\o
.endm
.macro Zvaa d,a,b
v_mfma_f32_16x16x32_bf16 v[\d:\d+3], a[\a:\a+3], a[\b:\b+3], 0
.endm
.macro Mvaa d,a,b
v_mfma_f32_16x16x32_bf16 v[\d:\d+3], a[\a:\a+3], a[\b:\b+3], v[\d:\d+3]
.endm
.macro Zaaa d,a,b
v_mfma_f32_16x16x32_bf16 a[\d:\d+3], a[\a:\a+3], a[\b:\b+3], 0
.endm
.macro Maaa d,a,b
v_mfma_f32_16x16x32_bf16 a[\d:\d+3], a[\a:\a+3], a[\b:\b+3], a[\d:\d+3]
.endm
.macro A d,a,b
v_add_f32 v\d, v\a, v\b
.endm
.macro U d,a,b
v_mul_f32 v\d, v\a, v\b
.endm
.macro K d,a,b
v_cvt_pk_bf16_f32 v\d, v\a, v\b
.endm
.macro Rv d,a,o
ds_read_b128 v[\d:\d+3], v\a offset:\o
.endm
.macro Mvav d,a,b
v_mfma_f32_16x16x32_bf16 v[\d:\d+3], a[\a:\a+3], v[\b:\b+3], v[\d:\d+3]
.endm
.macro G d,a
v_accvgpr_read_b32 v\d, a\a
.endm
.macro Dc v,r,s,o
buffer_load_dwordx4 v\v, s[\r:\r+3], s\s offen offset:\o sc1 lds
.endm
.macro Zvav d,a,b
v_mfma_f32_16x16x32_bf16 v[\d:\d+3], a[\a:\a+3], v[\b:\b+3], 0
.endm
.macro Cvav d,a,b,c
v_mfma_f32_16x16x32_bf16 v[\d:\d+3], a[\a:\a+3], v[\b:\b+3], v[\c:\c+3]
.endm
.macro X d
v_exp_f32 v\d, v\d
.endm
.macro Cvaa d,a,b,c
v_mfma_f32_16x16x32_bf16 v[\d:\d+3], a[\a:\a+3], a[\b:\b+3], v[\c:\c+3]
.endm
.macro Maav d,a,b
v_mfma_f32_16x16x32_bf16 a[\d:\d+3], a[\a:\a+3], v[\b:\b+3], a[\d:\d+3]
.endm
.macro Mvvv d,a,b
v_mfma_f32_16x16x32_bf16 v[\d:\d+3], v[\a:\a+3], v[\b:\b+3], v[\d:\d+3]
.endm
.macro Zvva d,a,b
v_mfma_f32_16x16x32_bf16 v[\d:\d+3], v[\a:\a+3], a[\b:\b+3], 0
.endm
.macro Mvva d,a,b
v_mfma_f32_16x16x32_bf16 v[\d:\d+3], v[\a:\a+3], a[\b:\b+3], v[\d:\d+3]
.endm
s_load_dwordx16 s[16:31], %[ka], 0
s_load_dwordx8 s[32:39], %[ka], 64
s_getpc_b64 s[6:7]
.Lbeg%=:
s_mov_b32 s15, %[bid]
v_lshrrev_b32 v2, 6, %[tid]
v_and_b32 v100, 63, %[tid]
v_readfirstlane_b32 s14, v2
v_lshlrev_b32 v1, 4, v100
v_lshrrev_b32 v101, 4, v100
v_and_b32 v102, 15, v100
s_and_b32 s59, s15, 7
s_bfe_u32 s57, s15, 0x30003
s_lshr_b32 s58, s15, 6
s_lshl_b32 s56, s59, 2
s_add_u32 s56, s56, s58
s_lshr_b32 s80, s15, 3
s_lshl_b32 s80, s80, 2
s_add_u32 s80, s80, s14
s_lshl_b32 s80, s80, 11
v_min_u32 v146, 240, v1
v_lshl_add_u32 v146, v146, 3, s80
v_min_u32 v146, .Lend%=-.Lbeg%=-4, v146
global_load_dword v142, v146, s[6:7]
v_and_b32 v88, 0x1f0, v1
v_lshrrev_b32 v105, 1, v101
v_mul_u32_u24 v105, 1280, v105
v_add_u32 v28, v88, v105
s_lshl_b32 s85, s15, 2
s_add_u32 s85, s85, s14
s_mul_i32 s84, s85, 2560
v_and_b32 v105, 1, v101
v_lshl_or_b32 v29, v105, 31, v28
v_lshlrev_b32 v104, 1, v101
v_xor_b32 v104, v104, v102
v_lshlrev_b32 v104, 4, v104
v_lshl_add_u32 v16, v101, 13, v104
v_xor_b32 v105, 128, v16
v_add_u32 v17, 31744, v16
v_add_u32 v18, 63488, v105
v_add_u32 v19, 95232, v105
v_add_u32 v20, 131072, v16
v_add_u32 v21, 162816, v16
v_add_u32 v22, 194560, v105
v_add_u32 v23, 226304, v105
s_lshl_b32 s80, s14, 3
s_add_u32 s80, s80, s57
s_lshl_b32 s80, s80, 15
v_and_b32 v104, 7, v100
v_xor_b32 v104, v104, v101
v_lshlrev_b32 v104, 4, v104
v_lshrrev_b32 v105, 3, v100
v_lshl_add_u32 v104, v105, 10, v104
v_add_u32 v2, s80, v104
v_xor_b32 v105, 64, v2
v_add_u32 v3, 7168, v105
v_add_u32 v4, 14336, v2
v_add_u32 v5, 21504, v105
v_xor_b32 v6, 1835008, v2
v_xor_b32 v7, 1835008, v3
v_xor_b32 v8, 1835008, v4
v_xor_b32 v9, 1835008, v5
s_lshl_b32 s63, s14, 13
s_add_u32 s61, s63, 65536
s_and_b32 s80, s14, 1
s_lshr_b32 s81, s14, 1
s_lshl_b32 s62, s80, 14
s_lshl_b32 s82, s81, 13
s_add_u32 s62, s62, s82
s_lshl_b32 s81, s81, 18
s_lshl_b32 s83, s56, 8
s_add_u32 s81, s81, s83
s_mov_b32 s64, 0
s_cmp_eq_u32 s80, 0
W 0
s_cselect_b32 s44, s22, s24
s_cselect_b32 s45, s23, s25
s_cmp_eq_u32 s14, 3
s_cbranch_scc0 .LL_1%=
s_lshr_b32 s88, s15, 3
s_cmp_lt_u32 s88, 14
s_cbranch_scc0 .LL_1%=
v_lshlrev_b32 v104, 16, v1
s_mov_b64 s[86:87], s[36:37]
s_mov_b32 s89, 38797312
s_cmp_eq_u32 s88, 1
s_cselect_b32 s90, 39845888, 0
s_add_u32 s86, s86, s90
s_addc_u32 s87, s87, 0
s_cmp_eq_u32 s88, 2
s_cselect_b64 s[86:87], s[26:27], s[86:87]
s_cselect_b32 s89, 5242876, s89
s_cmp_eq_u32 s88, 3
s_cselect_b64 s[86:87], s[30:31], s[86:87]
s_cselect_b32 s89, 2211836, s89
s_cmp_eq_u32 s88, 4
s_cselect_b64 s[86:87], s[32:33], s[86:87]
s_cselect_b32 s89, 2211836, s89
s_cmp_eq_u32 s88, 5
s_cselect_b64 s[86:87], s[34:35], s[86:87]
s_cselect_b32 s89, 2211836, s89
s_cmp_eq_u32 s88, 6
s_cselect_b64 s[86:87], s[38:39], s[86:87]
s_cselect_b32 s89, 2621436, s89
s_cmp_eq_u32 s88, 7
s_cselect_b64 s[86:87], s[20:21], s[86:87]
s_cselect_b32 s89, 5242876, s89
s_cmp_eq_u32 s88, 8
s_cselect_b64 s[86:87], s[18:19], s[86:87]
s_cselect_b32 s89, 2097148, s89
s_cmp_eq_u32 s88, 9
s_cselect_b64 s[86:87], s[22:23], s[86:87]
s_cselect_b32 s89, 4194300, s89
s_cmp_eq_u32 s88, 10
s_cselect_b64 s[86:87], s[24:25], s[86:87]
s_cselect_b32 s89, 4194300, s89
s_cmp_eq_u32 s88, 11
s_cselect_b64 s[86:87], s[16:17], s[86:87]
s_cselect_b32 s89, 2621436, s89
s_cmp_eq_u32 s88, 12
s_cselect_b64 s[86:87], s[28:29], s[86:87]
s_cselect_b32 s89, 1276, s89
s_cmp_eq_u32 s88, 13
s_cselect_b64 s[86:87], s[6:7], s[86:87]
s_cselect_b32 s89, 0, s89
v_min_u32 v104, s89, v104
global_load_dword v142, v104, s[86:87] sc1
.LL_1%=:
s_mov_b32 s40, s18
s_and_b32 s41, s19, 0xffff
s_mov_b32 s42, 0x200000
s_mov_b32 s43, 0x20000
s_add_u32 s44, s44, s81
s_addc_u32 s45, s45, 0
s_and_b32 s45, s45, 0xffff
s_mov_b32 s46, 0x400000
s_mov_b32 s47, 0x20000
s_sub_u32 s46, s46, s81
s_load_dwordx2 s[12:13], s[36:37], 0
s_add_u32 m0, s61, 0
s_nop 0
D 2,40,64,0
D 3,40,64,1024
D 4,40,64,2048
D 5,40,64,3072
s_add_u32 m0, s61, 4096
s_nop 0
D 6,40,64,0
D 7,40,64,1024
D 8,40,64,2048
D 9,40,64,3072
s_lshl_b32 s65, s64, 12
s_add_u32 m0, s62, 0
s_nop 0
D 16,44,65,0
D 17,44,65,1024
D 18,44,65,2048
D 19,44,65,3072
s_add_u32 m0, s62, 4096
s_nop 0
D 20,44,65,0
D 21,44,65,1024
D 22,44,65,2048
D 23,44,65,3072
s_mov_b32 s52, s16
s_and_b32 s53, s17, 0xffff
s_mov_b32 s54, 2621440
s_mov_b32 s55, 0x20000
buffer_load_dwordx4 v[32:35], v28, s[52:55], s84 offen
buffer_load_dwordx4 v[36:39], v28, s[52:55], s84 offen offset:512
buffer_load_dwordx4 v[40:43], v29, s[52:55], s84 offen offset:1024
s_mov_b32 s52, s28
s_and_b32 s53, s29, 0xffff
s_mov_b32 s54, 1280
s_mov_b32 s55, 0x20000
buffer_load_dwordx4 v[44:47], v88, s[52:55], 0 offen offset:0
buffer_load_dwordx4 v[48:51], v88, s[52:55], 0 offen offset:512
buffer_load_dwordx4 v[52:55], v88, s[52:55], 0 offen offset:1024
s_mov_b32 s69, 0
s_cmp_eq_u32 s14, 2
s_cbranch_scc1 .LL_2%=
s_cmp_eq_u32 s14, 3
s_cbranch_scc1 .LL_3%=
s_lshl_b32 s80, s58, 3
s_add_u32 s80, s80, s57
s_lshl_b32 s80, s80, 15
s_lshl_b32 s82, s14, 7
s_add_u32 s80, s80, s82
s_add_u32 s80, s80, 256
v_lshl_add_u32 v104, v88, 6, s80
v_and_b32 v105, 512, v1
v_mul_u32_u24 v105, 3584, v105
v_xor_b32 v104, v104, v105
v_mov_b32 v140, s18
v_mov_b32 v141, s19
s_mov_b32 s68, 256
s_branch .LL_4%=
.LL_2%=:
v_lshrrev_b32 v104, 6, v1
v_lshlrev_b32 v104, 13, v104
v_and_b32 v105, 16, v1
v_lshl_add_u32 v104, v105, 3, v104
s_lshl_b32 s80, s57, 17
s_add_u32 s80, s80, s83
s_add_u32 s80, s80, 1048576
v_add_u32 v104, s80, v104
v_and_b32 v105, 32, v1
v_cmp_eq_u32 vcc, 0, v105
v_mov_b32 v140, s24
v_mov_b32 v141, s25
v_mov_b32 v105, s22
v_mov_b32 v106, s23
v_cndmask_b32 v140, v140, v105, vcc
v_cndmask_b32 v141, v141, v106, vcc
s_mov_b32 s68, 1048576
s_branch .LL_4%=
.LL_3%=:
v_lshrrev_b32 v104, 5, v1
v_lshlrev_b32 v104, 13, v104
v_and_b32 v105, 16, v1
v_lshl_add_u32 v104, v105, 3, v104
s_mul_i32 s80, s57, 655360
s_add_u32 s80, s80, s83
v_add_u32 v104, s80, v104
v_mov_b32 v140, s20
v_mov_b32 v141, s21
s_mov_b32 s68, 196608
.LL_4%=:
v_mov_b32 v105, 0
v_lshl_add_u64 v[140:141], v[104:105], 0, v[140:141]
s_add_u32 s64, s64, 128
s_and_b32 s64, s64, 1023
s_add_u32 m0, s61, 32768
s_nop 0
D 2,40,64,0
D 3,40,64,1024
D 4,40,64,2048
D 5,40,64,3072
s_add_u32 m0, s61, 36864
s_nop 0
D 6,40,64,0
D 7,40,64,1024
D 8,40,64,2048
D 9,40,64,3072
s_lshl_b32 s65, s64, 12
v_lshrrev_b32 v104, 1, v102
v_xor_b32 v104, v104, v101
v_lshlrev_b32 v104, 4, v104
v_lshl_add_u32 v104, v102, 7, v104
s_lshr_b32 s81, s14, 1
s_lshl_b32 s81, s81, 14
s_add_u32 s81, s81, 65536
v_add_u32 v26, s81, v104
v_xor_b32 v27, 64, v26
v_and_b32 v105, 1, v101
v_lshrrev_b32 v104, 2, v102
v_lshl_or_b32 v105, v105, 2, v104
v_lshl_add_u32 v104, v101, 3, v104
v_lshlrev_b32 v104, 8, v104
v_lshl_add_u32 v104, v105, 5, v104
v_and_b32 v105, 3, v102
v_lshl_add_u32 v10, v105, 3, v104
v_xor_b32 v11, 32, v10
v_xor_b32 v12, 64, v10
v_xor_b32 v13, 96, v10
v_xor_b32 v14, 128, v10
v_xor_b32 v15, 160, v10
v_xor_b32 v24, 192, v10
v_xor_b32 v25, 224, v10
.LL_8%=:
s_lshr_b32 s81, s15, 3
s_cmp_eq_u32 s14, 2
s_cbranch_scc1 .LL_5%=
s_cmp_eq_u32 s14, 3
s_cbranch_scc1 .LL_6%=
s_cmp_eq_u32 s14, 0
s_cselect_b64 s[76:77], s[30:31], s[32:33]
s_mov_b32 s72, 512
s_mov_b32 s73, 24
s_mul_i32 s74, s81, 69120
s_mul_i32 s82, s59, 8640
s_andn2_b32 s82, s82, 127
s_add_u32 s74, s74, s82
s_mov_b32 s75, 2211836
s_branch .LL_7%=
.LL_5%=:
s_mov_b64 s[76:77], s[34:35]
s_mov_b32 s72, 512
s_mov_b32 s73, 24
s_mul_i32 s74, s81, 8960
s_mul_i32 s82, s59, 286720
s_add_u32 s74, s74, s82
s_mov_b32 s75, 2211836
s_branch .LL_7%=
.LL_6%=:
s_mov_b64 s[76:77], s[26:27]
s_mov_b32 s72, 512
s_mov_b32 s73, 4
s_mul_i32 s74, s81, 20480
s_mul_i32 s82, s59, 655360
s_add_u32 s74, s74, s82
s_mov_b32 s75, 5242876
s_branch .LL_7%=
.LL_7%=:
s_mov_b32 s8, s36
s_and_b32 s9, s37, 0xffff
s_mov_b32 s10, 79691776
s_mov_b32 s11, 0x20000
s_add_u32 s48, s20, s83
s_addc_u32 s49, s21, 0
s_and_b32 s49, s49, 0xffff
s_mov_b32 s50, 5242880
s_mov_b32 s51, 0x20000
s_sub_u32 s50, s50, s83
s_lshl_b32 s80, s57, 1
s_and_b32 s81, s14, 1
s_add_u32 s80, s80, s81
s_sub_u32 s81, 31, s80
s_cmp_lt_u32 s14, 2
s_cselect_b32 s60, s80, s81
s_mul_i32 s28, s60, 81920
s_add_u32 s28, s28, 131072
s_add_u32 s4, s28, 10240
s_mov_b32 s80, 0
s_lshl_b32 s81, s80, 9
s_add_u32 s28, s28, s81
s_add_u32 s5, s80, s14
s_cmp_ge_u32 s5, 20
s_cselect_b32 s81, 20, 0
s_sub_u32 s5, s5, s81
s_and_b32 s80, s14, 1
s_mul_i32 s29, s80, 17172480
s_mul_i32 s80, s56, 536576
s_add_u32 s29, s29, s80
s_lshl_b32 s80, s57, 13
s_add_u32 s29, s29, s80
s_add_u32 s29, s29, 4194304
W 0
s_add_u32 s12, s12, 0x7f4a7c15
s_addc_u32 s13, s13, 0x9e3779b9
s_bitcmp1_b32 s14, 0
s_cbranch_scc1 .Lv_9%=
s_waitcnt vmcnt(14)
s_barrier
Ta 0,10,0
Ta 2,10,1024
Ra 32,26,0
Ta 4,10,8192
Ta 6,10,9216
Ra 128,27,0
Ra 132,27,2048
Ra 36,26,2048
Ra 40,26,4096
Ra 136,27,4096
Ra 140,27,6144
Ra 44,26,6144
Ra 48,26,8192
Ra 144,27,8192
W 6
Zvaa 160,0,32
s_add_u32 m0, s62, 32768
Ra 148,27,10240
Ra 52,26,10240
Ra 56,26,12288
Ra 152,27,12288
Ra 156,27,14336
Ra 60,26,14336
Mvaa 160,4,128
Ta 8,11,0
Ta 10,11,1024
Ta 12,11,8192
Ta 14,11,9216
Zvaa 192,4,132
Ta 16,12,0
Ta 18,12,1024
Mvaa 192,0,36
W 14
)"
R"(Ta 20,12,8192
Ta 22,12,9216
Zvaa 224,0,40
D 16,44,65,0
Mvaa 224,4,136
Zaaa 160,4,140
Maaa 160,0,44
W 12
Zaaa 192,0,48
D 17,44,65,1024
Maaa 192,4,144
Zaaa 224,4,148
s_waitcnt vmcnt(13)
Maaa 224,0,52
W 8
v_lshlrev_b32 v56, 16, v32
v_and_b32 v57, 0xffff0000, v32
v_lshlrev_b32 v58, 16, v33
v_and_b32 v59, 0xffff0000, v33
Zaaa 64,0,56
D 18,44,65,2048
v_lshlrev_b32 v60, 16, v34
v_and_b32 v61, 0xffff0000, v34
v_lshlrev_b32 v62, 16, v35
v_and_b32 v63, 0xffff0000, v35
Maaa 64,4,152
v_lshlrev_b32 v64, 16, v36
v_and_b32 v65, 0xffff0000, v36
v_lshlrev_b32 v66, 16, v37
v_and_b32 v67, 0xffff0000, v37
v_lshlrev_b32 v68, 16, v38
Zaaa 96,4,156
v_and_b32 v69, 0xffff0000, v38
v_lshlrev_b32 v70, 16, v39
v_and_b32 v71, 0xffff0000, v39
v_lshlrev_b32 v72, 16, v40
Maaa 96,0,60
W 4
v_and_b32 v73, 0xffff0000, v40
v_lshlrev_b32 v74, 16, v41
v_and_b32 v75, 0xffff0000, v41
v_lshlrev_b32 v76, 16, v42
v_and_b32 v77, 0xffff0000, v42
Zaaa 100,8,60
D 19,44,65,3072
v_lshlrev_b32 v78, 16, v43
v_and_b32 v79, 0xffff0000, v43
A 80,56,57
A 81,58,59
Maaa 100,12,156
s_add_u32 m0, s62, 36864
A 80,80,60
A 81,81,61
A 80,80,62
A 81,81,63
A 80,80,64
Zaaa 68,12,152
Ta 24,13,0
Ta 26,13,1024
A 81,81,65
A 80,80,66
A 81,81,67
A 80,80,68
Maaa 68,8,56
Ta 28,13,8192
Ta 30,13,9216
A 81,81,69
A 80,80,70
A 81,81,71
A 80,80,72
Zaaa 228,8,52
D 20,44,65,0
A 81,81,73
A 80,80,74
A 81,81,75
A 80,80,76
A 81,81,77
Maaa 228,12,148
A 80,80,78
A 81,81,79
A 80,80,81
s_nop 1
v_add_f32_dpp v80, v80, v80 quad_perm:[1,0,3,2] row_mask:0xf bank_mask:0xf
Zaaa 196,12,144
Maaa 196,8,48
v_add_f32_dpp v80, v80, v80 quad_perm:[2,3,0,1] row_mask:0xf bank_mask:0xf
s_nop 1
v_add_f32_dpp v80, v80, v80 row_half_mirror row_mask:0xf bank_mask:0xf
s_nop 1
v_add_f32_dpp v80, v80, v80 row_mirror row_mask:0xf bank_mask:0xf
v_mov_b32 v81, v80
s_nop 1
v_permlane16_swap_b32 v80, v81
A 80,80,81
Zaaa 164,8,44
D 21,44,65,1024
v_mul_f32 v84, 0x3acccccd, v80
s_mov_b32 s86, 0xffff
s_mov_b32 s87, 0xffff
v_sub_f32 v82, v56, v84
U 80,82,82
v_sub_f32 v83, v57, v84
Maaa 164,12,140
v_fmac_f32 v80, v83, v83
v_sub_f32 v82, v58, v84
v_fmac_f32 v80, v82, v82
v_sub_f32 v83, v59, v84
v_fmac_f32 v80, v83, v83
v_sub_f32 v82, v60, v84
Zvaa 228,12,136
v_fmac_f32 v80, v82, v82
v_sub_f32 v83, v61, v84
v_fmac_f32 v80, v83, v83
v_sub_f32 v82, v62, v84
v_fmac_f32 v80, v82, v82
Mvaa 228,8,40
v_sub_f32 v83, v63, v84
v_fmac_f32 v80, v83, v83
v_sub_f32 v82, v64, v84
v_fmac_f32 v80, v82, v82
Zvaa 196,8,36
D 22,44,65,2048
v_sub_f32 v83, v65, v84
v_fmac_f32 v80, v83, v83
v_sub_f32 v82, v66, v84
v_fmac_f32 v80, v82, v82
Mvaa 196,12,132
v_sub_f32 v83, v67, v84
v_fmac_f32 v80, v83, v83
v_sub_f32 v82, v68, v84
v_fmac_f32 v80, v82, v82
v_sub_f32 v83, v69, v84
Zvaa 164,12,128
v_fmac_f32 v80, v83, v83
v_sub_f32 v82, v70, v84
v_fmac_f32 v80, v82, v82
v_sub_f32 v83, v71, v84
Mvaa 164,8,32
W 4
v_fmac_f32 v80, v83, v83
v_sub_f32 v82, v72, v84
U 81,82,82
v_sub_f32 v83, v73, v84
v_fmac_f32 v81, v83, v83
Zvaa 168,16,32
D 23,44,65,3072
v_sub_f32 v82, v74, v84
v_fmac_f32 v81, v82, v82
v_sub_f32 v83, v75, v84
v_fmac_f32 v81, v83, v83
Mvaa 168,20,128
v_sub_f32 v82, v76, v84
v_fmac_f32 v81, v82, v82
v_sub_f32 v83, v77, v84
v_fmac_f32 v81, v83, v83
v_sub_f32 v82, v78, v84
Zvaa 200,20,132
Ta 0,14,0
Ta 2,14,1024
v_fmac_f32 v81, v82, v82
v_sub_f32 v83, v79, v84
v_fmac_f32 v81, v83, v83
v_cndmask_b32_e64 v81, 0, v81, s[86:87]
Mvaa 200,16,36
Ta 4,14,8192
Ta 6,14,9216
A 80,80,81
s_nop 1
v_add_f32_dpp v80, v80, v80 quad_perm:[1,0,3,2] row_mask:0xf bank_mask:0xf
s_nop 1
v_add_f32_dpp v80, v80, v80 quad_perm:[2,3,0,1] row_mask:0xf bank_mask:0xf
s_nop 1
v_add_f32_dpp v80, v80, v80 row_half_mirror row_mask:0xf bank_mask:0xf
Zvaa 232,16,40
global_load_dword v142, v[140:141], off
v_lshl_add_u64 v[140:141], v[140:141], 0, s[68:69]
Mvaa 232,20,136
v_add_f32_dpp v80, v80, v80 row_mirror row_mask:0xf bank_mask:0xf
v_mov_b32 v81, v80
s_nop 1
v_permlane16_swap_b32 v80, v81
A 80,80,81
v_mul_f32 v80, 0x3acccccd, v80
K 80,80,80
Zaaa 168,20,140
v_lshlrev_b32 v80, 16, v80
v_add_f32 v80, 0x3727c5ac, v80
K 80,80,80
v_lshlrev_b32 v80, 16, v80
v_sqrt_f32 v81, v80
Maaa 168,16,44
v_rcp_f32 v82, v81
v_fma_f32 v83, -v81, v81, v80
v_mul_f32 v82, 0.5, v82
v_fma_f32 v81, v83, v82, v81
K 86,81,81
Zaaa 200,16,48
v_lshlrev_b32 v86, 16, v86
v_rcp_f32 v87, v86
K 85,84,84
v_lshlrev_b32 v85, 16, v85
Maaa 200,20,144
s_waitcnt vmcnt(17)
v_sub_f32 v56, v56, v85
v_sub_f32 v57, v57, v85
K 80,56,57
Zaaa 232,20,148
v_lshlrev_b32 v56, 16, v80
v_and_b32 v57, 0xffff0000, v80
U 80,56,87
U 81,57,87
K 32,80,81
Maaa 232,16,52
v_lshlrev_b32 v80, 16, v32
v_and_b32 v81, 0xffff0000, v32
v_lshlrev_b32 v82, 16, v44
v_and_b32 v83, 0xffff0000, v44
Zaaa 72,16,56
U 84,80,82
v_fmac_f32 v84, v81, v83
v_sub_f32 v58, v58, v85
v_sub_f32 v59, v59, v85
Maaa 72,20,152
K 80,58,59
v_lshlrev_b32 v58, 16, v80
v_and_b32 v59, 0xffff0000, v80
U 80,58,87
U 81,59,87
Zaaa 104,20,156
K 33,80,81
v_lshlrev_b32 v80, 16, v33
v_and_b32 v81, 0xffff0000, v33
v_lshlrev_b32 v82, 16, v45
Maaa 104,16,60
W 4
v_and_b32 v83, 0xffff0000, v45
v_fmac_f32 v84, v80, v82
v_fmac_f32 v84, v81, v83
v_sub_f32 v60, v60, v85
v_sub_f32 v61, v61, v85
Zaaa 108,24,60
K 80,60,61
v_lshlrev_b32 v60, 16, v80
v_and_b32 v61, 0xffff0000, v80
U 80,60,87
Maaa 108,28,156
U 81,61,87
K 34,80,81
v_lshlrev_b32 v80, 16, v34
v_and_b32 v81, 0xffff0000, v34
v_lshlrev_b32 v82, 16, v46
Zaaa 76,28,152
Ta 8,15,0
Ta 10,15,1024
v_and_b32 v83, 0xffff0000, v46
v_fmac_f32 v84, v80, v82
v_fmac_f32 v84, v81, v83
v_sub_f32 v62, v62, v85
Maaa 76,24,56
Ta 12,15,8192
v_sub_f32 v63, v63, v85
K 80,62,63
v_lshlrev_b32 v62, 16, v80
v_and_b32 v63, 0xffff0000, v80
U 80,62,87
Zaaa 236,24,52
Ta 14,15,9216
U 81,63,87
K 35,80,81
v_lshlrev_b32 v80, 16, v35
v_and_b32 v81, 0xffff0000, v35
Maaa 236,28,148
v_lshlrev_b32 v82, 16, v47
v_and_b32 v83, 0xffff0000, v47
v_fmac_f32 v84, v80, v82
v_fmac_f32 v84, v81, v83
Zaaa 204,28,144
v_sub_f32 v64, v64, v85
v_sub_f32 v65, v65, v85
K 80,64,65
v_lshlrev_b32 v64, 16, v80
v_and_b32 v65, 0xffff0000, v80
Maaa 204,24,48
U 80,64,87
U 81,65,87
K 36,80,81
v_lshlrev_b32 v80, 16, v36
Zaaa 172,24,44
v_and_b32 v81, 0xffff0000, v36
v_lshlrev_b32 v82, 16, v48
v_and_b32 v83, 0xffff0000, v48
v_fmac_f32 v84, v80, v82
v_fmac_f32 v84, v81, v83
Maaa 172,28,140
v_sub_f32 v66, v66, v85
v_sub_f32 v67, v67, v85
K 80,66,67
v_lshlrev_b32 v66, 16, v80
Zvaa 236,28,136
v_and_b32 v67, 0xffff0000, v80
U 80,66,87
U 81,67,87
K 37,80,81
v_lshlrev_b32 v80, 16, v37
Mvaa 236,24,40
v_and_b32 v81, 0xffff0000, v37
v_lshlrev_b32 v82, 16, v49
v_and_b32 v83, 0xffff0000, v49
v_fmac_f32 v84, v80, v82
Zvaa 204,24,36
v_fmac_f32 v84, v81, v83
v_sub_f32 v68, v68, v85
v_sub_f32 v69, v69, v85
K 80,68,69
v_lshlrev_b32 v68, 16, v80
Mvaa 204,28,132
v_and_b32 v69, 0xffff0000, v80
U 80,68,87
U 81,69,87
K 38,80,81
Zvaa 172,28,128
v_lshlrev_b32 v80, 16, v38
v_and_b32 v81, 0xffff0000, v38
v_lshlrev_b32 v82, 16, v50
v_and_b32 v83, 0xffff0000, v50
Mvaa 172,24,32
W 4
v_fmac_f32 v84, v80, v82
v_fmac_f32 v84, v81, v83
v_sub_f32 v70, v70, v85
v_sub_f32 v71, v71, v85
K 80,70,71
Zvaa 176,0,32
v_lshlrev_b32 v70, 16, v80
v_and_b32 v71, 0xffff0000, v80
U 80,70,87
U 81,71,87
Mvaa 176,4,128
K 39,80,81
v_lshlrev_b32 v80, 16, v39
v_and_b32 v81, 0xffff0000, v39
v_lshlrev_b32 v82, 16, v51
v_and_b32 v83, 0xffff0000, v51
Zvaa 208,4,132
Ta 16,24,0
Ta 18,24,1024
v_fmac_f32 v84, v80, v82
v_fmac_f32 v84, v81, v83
v_sub_f32 v72, v72, v85
v_sub_f32 v73, v73, v85
Mvaa 208,0,36
Ta 20,24,8192
K 80,72,73
v_lshlrev_b32 v72, 16, v80
v_and_b32 v73, 0xffff0000, v80
U 80,72,87
U 81,73,87
Zvaa 240,0,40
Ta 22,24,9216
K 40,80,81
v_lshlrev_b32 v80, 16, v40
v_and_b32 v81, 0xffff0000, v40
v_lshlrev_b32 v82, 16, v52
Mvaa 240,4,136
v_and_b32 v83, 0xffff0000, v52
v_fmac_f32 v84, v80, v82
v_fmac_f32 v84, v81, v83
v_sub_f32 v74, v74, v85
Zaaa 176,4,140
v_sub_f32 v75, v75, v85
K 80,74,75
v_lshlrev_b32 v74, 16, v80
v_and_b32 v75, 0xffff0000, v80
U 80,74,87
Maaa 176,0,44
U 81,75,87
K 41,80,81
v_lshlrev_b32 v80, 16, v41
v_and_b32 v81, 0xffff0000, v41
Zaaa 208,0,48
v_lshlrev_b32 v82, 16, v53
v_and_b32 v83, 0xffff0000, v53
v_fmac_f32 v84, v80, v82
v_fmac_f32 v84, v81, v83
v_sub_f32 v76, v76, v85
Maaa 208,4,144
v_sub_f32 v77, v77, v85
K 80,76,77
v_lshlrev_b32 v76, 16, v80
v_and_b32 v77, 0xffff0000, v80
Zaaa 240,4,148
U 80,76,87
U 81,77,87
K 42,80,81
v_lshlrev_b32 v80, 16, v42
v_and_b32 v81, 0xffff0000, v42
Maaa 240,0,52
v_lshlrev_b32 v82, 16, v54
v_and_b32 v83, 0xffff0000, v54
v_fmac_f32 v84, v80, v82
)"
R"(v_fmac_f32 v84, v81, v83
Zaaa 80,0,56
v_sub_f32 v78, v78, v85
v_sub_f32 v79, v79, v85
K 80,78,79
v_lshlrev_b32 v78, 16, v80
v_and_b32 v79, 0xffff0000, v80
Maaa 80,4,152
U 80,78,87
U 81,79,87
K 43,80,81
v_lshlrev_b32 v80, 16, v43
Zaaa 112,4,156
v_and_b32 v81, 0xffff0000, v43
v_lshlrev_b32 v82, 16, v55
v_and_b32 v83, 0xffff0000, v55
v_fmac_f32 v84, v80, v82
Maaa 112,0,60
W 4
v_fmac_f32 v84, v81, v83
v_lshrrev_b32 v82, 9, v1
s_lshl_b32 s80, s14, 1
v_add_u32 v82, s80, v82
v_mul_u32_u24 v83, 1552, v82
Zaaa 116,8,60
Ta 24,25,0
Ta 26,25,1024
v_add_u32 v83, v83, v88
v_add_u32 v83, 131072, v83
ds_write_b128 v83, v[32:35] offset:0
ds_write_b128 v83, v[36:39] offset:512
Maaa 116,12,156
ds_write_b128 v83, v[40:43] offset:1024
v_add_f32_dpp v84, v84, v84 quad_perm:[1,0,3,2] row_mask:0xf bank_mask:0xf
s_nop 1
v_add_f32_dpp v84, v84, v84 quad_perm:[2,3,0,1] row_mask:0xf bank_mask:0xf
s_nop 1
v_add_f32_dpp v84, v84, v84 row_half_mirror row_mask:0xf bank_mask:0xf
s_nop 1
v_add_f32_dpp v84, v84, v84 row_mirror row_mask:0xf bank_mask:0xf
Zaaa 84,12,152
Ta 28,25,8192
Ta 30,25,9216
v_mov_b32 v81, v84
Maaa 84,8,56
Zaaa 244,8,52
v_permlane16_swap_b32 v84, v81
A 84,84,81
K 80,84,84
v_lshlrev_b32 v80, 16, v80
s_lshl_b32 s80, s15, 3
v_add_u32 v82, s80, v82
v_sub_u32 v83, 2047, v82
Maaa 244,12,148
v_ashrrev_i32 v81, 31, v80
v_or_b32 v81, 0x80000000, v81
v_xor_b32 v80, v80, v81
v_and_b32 v80, 0xffff0000, v80
v_or_b32 v80, v80, v83
v_and_b32 v82, 7, v82
Zaaa 212,12,144
v_lshlrev_b32 v82, 2, v82
v_add_u32 v82, 143488, v82
ds_write_b32 v82, v80
Maaa 212,8,48
Zaaa 180,8,44
Maaa 180,12,140
Zvaa 244,12,136
s_waitcnt vmcnt(1) lgkmcnt(0)
s_barrier
s_add_u32 s64, s64, 128
Mvaa 244,8,40
v_bfe_u32 v80, v1, 4, 3
Zvaa 212,8,36
v_mul_u32_u24 v81, 1552, v80
Mvaa 212,12,132
s_and_b32 s64, s64, 1023
v_lshrrev_b32 v82, 7, v1
Zvaa 180,12,128
v_lshl_add_u32 v81, v82, 4, v81
Mvaa 180,8,32
s_lshl_b32 s80, s14, 7
Zvaa 184,16,32
s_add_u32 m0, s61, 0
s_add_u32 s80, s80, 131072
Mvaa 184,20,128
v_add_u32 v81, s80, v81
Zvaa 216,20,132
D 2,40,64,0
ds_read_b128 v[32:35], v81
Mvaa 216,16,36
Ta 0,10,32768
Ta 2,10,33792
Zvaa 248,16,40
Rv 36,81,512
Ta 4,10,40960
Mvaa 248,20,136
s_and_b32 s80, s14, 2
s_lshl_b32 s80, s80, 7
Ta 6,10,41984
Zaaa 184,20,140
D 3,40,64,1024
v_subrev_u32 v81, s80, v81
Maaa 184,16,44
Rv 40,81,1024
Zaaa 216,16,48
v_lshlrev_b32 v80, 2, v80
Maaa 216,20,144
v_add_u32 v80, 143488, v80
Zaaa 248,20,148
D 4,40,64,2048
ds_read_b32 v84, v80
Maaa 248,16,52
v_mov_b32 v85, s13
Zaaa 88,16,56
v_lshrrev_b32 v83, 1, v1
Maaa 88,20,152
s_mul_i32 s82, s15, 10240
Zaaa 120,20,156
s_add_u32 s82, s82, 131072
D 5,40,64,3072
Maaa 120,16,60
s_add_u32 m0, s61, 4096
s_lshl_b32 s80, s14, 10
Zaaa 124,24,60
s_add_u32 s83, s82, s80
Ra 60,26,47104
Maaa 124,28,156
Ra 156,27,47104
Zaaa 92,28,152
D 6,40,64,0
Ra 152,27,45056
Maaa 92,24,56
Ta 8,11,32768
Ta 10,11,33792
Zaaa 252,24,52
Ra 56,26,45056
Ta 12,11,40960
Maaa 252,28,148
Ta 14,11,41984
Ra 52,26,43008
Zaaa 220,28,144
W 9
buffer_store_dwordx4 v[32:35], v1, s[8:11], s83 offen sc1
s_add_u32 s83, s83, 4096
Maaa 220,24,48
Ra 148,27,43008
Ra 144,27,40960
Zaaa 188,24,44
Ra 48,26,40960
Ra 44,26,38912
Maaa 188,28,140
Ra 140,27,38912
Zvaa 252,28,136
D 7,40,64,1024
Ra 136,27,36864
Mvaa 252,24,40
Ra 40,26,36864
Zvaa 220,24,36
Ra 36,26,34816
Mvaa 220,28,132
Ra 132,27,34816
Zvaa 188,28,128
buffer_store_dwordx4 v[36:39], v1, s[8:11], s83 offen sc1
Ra 128,27,32768
Mvaa 188,24,32
W 13
s_and_b32 s80, s80, 1024
s_add_u32 s83, s82, s80
Maaa 96,0,60
s_add_u32 s83, s83, 8192
Ra 32,26,32768
Maaa 96,4,156
Maaa 64,4,152
D 8,40,64,2048
Ta 16,12,32768
Maaa 64,0,56
W 9
Ta 18,12,33792
Ta 20,12,40960
Maaa 224,0,52
Ta 22,12,41984
Maaa 224,4,148
Maaa 192,4,144
D 9,40,64,3072
Maaa 192,0,48
W 8
s_lshl_b32 s65, s64, 12
s_add_u32 m0, s62, 0
Maaa 160,0,44
Maaa 160,4,140
Mvaa 224,4,136
buffer_store_dwordx4 v[40:43], v1, s[8:11], s83 offen sc1
Mvaa 224,0,40
W 4
Mvaa 192,0,36
Mvaa 192,4,132
Mvaa 160,4,128
D 16,44,65,0
Mvaa 160,0,32
Mvaa 164,8,32
Mvaa 164,12,128
Mvaa 196,12,132
D 17,44,65,1024
Ta 24,13,32768
Mvaa 196,8,36
Ta 26,13,33792
Ta 28,13,40960
Mvaa 228,8,40
Ta 30,13,41984
Mvaa 228,12,136
Maaa 164,12,140
D 18,44,65,2048
Maaa 164,8,44
Maaa 196,8,48
Maaa 196,12,144
Maaa 228,12,148
D 19,44,65,3072
Maaa 228,8,52
s_add_u32 m0, s62, 4096
Maaa 68,8,56
Maaa 68,12,152
Maaa 100,12,156
D 20,44,65,0
Maaa 100,8,60
W 4
Maaa 104,16,60
Maaa 104,20,156
Maaa 72,20,152
D 21,44,65,1024
Ta 0,14,32768
Maaa 72,16,56
Ta 2,14,33792
Ta 4,14,40960
Maaa 232,16,52
Ta 6,14,41984
Maaa 232,20,148
Maaa 200,20,144
D 22,44,65,2048
Maaa 200,16,48
Maaa 168,16,44
Maaa 168,20,140
Mvaa 232,20,136
D 23,44,65,3072
Mvaa 232,16,40
Mvaa 200,16,36
Mvaa 200,20,132
Mvaa 168,20,128
global_load_dword v142, v[140:141], off
v_lshl_add_u64 v[140:141], v[140:141], 0, s[68:69]
Mvaa 168,16,32
W 4
Mvaa 172,24,32
Mvaa 172,28,128
Mvaa 204,28,132
Ta 8,15,32768
Ta 10,15,33792
Mvaa 204,24,36
Ta 12,15,40960
Ta 14,15,41984
Mvaa 236,24,40
Mvaa 236,28,136
Maaa 172,28,140
Maaa 172,24,44
Maaa 204,24,48
Maaa 204,28,144
Maaa 236,28,148
Maaa 236,24,52
Maaa 76,24,56
Maaa 76,28,152
Maaa 108,28,156
Maaa 108,24,60
W 4
Maaa 112,0,60
Maaa 112,4,156
Maaa 80,4,152
Ta 16,24,32768
Ta 18,24,33792
Maaa 80,0,56
Ta 20,24,40960
Ta 22,24,41984
Maaa 240,0,52
Maaa 240,4,148
Maaa 208,4,144
Maaa 208,0,48
Maaa 176,0,44
Maaa 176,4,140
Mvaa 240,4,136
Mvaa 240,0,40
Mvaa 208,0,36
Mvaa 208,4,132
Mvaa 176,4,128
Mvaa 176,0,32
W 4
Mvaa 180,8,32
Ta 24,25,32768
Ta 26,25,33792
Mvaa 180,12,128
Mvaa 212,12,132
Ta 28,25,40960
Ta 30,25,41984
Mvaa 212,8,36
Mvaa 244,8,40
Mvaa 244,12,136
Maaa 180,12,140
Maaa 180,8,44
Maaa 212,8,48
Maaa 212,12,144
Maaa 244,12,148
s_waitcnt vmcnt(1) lgkmcnt(0)
s_barrier
s_cmp_eq_u32 s14, 0
s_cbranch_scc0 .Lpubr_12%=
s_lshl_b32 s80, s15, 6
s_add_u32 s80, s80, 40960
s_mov_b64 exec, 0xff
buffer_store_dwordx2 v[84:85], v83, s[8:11], s80 offen sc1
s_mov_b64 exec, -1
.Lpubr_12%=:
Maaa 244,8,52
s_add_u32 s64, s64, 128
Maaa 84,8,56
Maaa 84,12,152
s_and_b32 s64, s64, 1023
Maaa 116,12,156
Maaa 116,8,60
Maaa 120,16,60
s_add_u32 m0, s61, 32768
Maaa 120,20,156
Maaa 88,20,152
D 2,40,64,0
Ta 0,10,0
Maaa 88,16,56
Ta 2,10,1024
Ta 4,10,8192
Maaa 248,16,52
Ta 6,10,9216
Maaa 248,20,148
Maaa 216,20,144
D 3,40,64,1024
Maaa 216,16,48
Maaa 184,16,44
Maaa 184,20,140
Mvaa 248,20,136
D 4,40,64,2048
Mvaa 248,16,40
Mvaa 216,16,36
Mvaa 216,20,132
Mvaa 184,20,128
D 5,40,64,3072
Mvaa 184,16,32
s_add_u32 m0, s61, 36864
Mvaa 188,24,32
Ra 32,26,0
Mvaa 188,28,128
Ra 128,27,0
Mvaa 220,28,132
D 6,40,64,0
Ra 132,27,2048
Mvaa 220,24,36
Ta 8,11,0
Ta 10,11,1024
Mvaa 252,24,40
Ra 36,26,2048
Ta 12,11,8192
Mvaa 252,28,136
Ta 14,11,9216
Ra 40,26,4096
Maaa 188,28,140
D 7,40,64,1024
Ra 136,27,4096
Maaa 188,24,44
Ra 140,27,6144
Ra 44,26,6144
Maaa 220,24,48
Ra 48,26,8192
Maaa 220,28,144
Ra 144,27,8192
Maaa 252,28,148
D 8,40,64,2048
Ra 148,27,10240
Maaa 252,24,52
Ra 52,26,10240
Maaa 92,24,56
Ra 56,26,12288
Maaa 92,28,152
Ra 152,27,12288
Maaa 124,28,156
D 9,40,64,3072
Ra 156,27,14336
Maaa 124,24,60
W 13
s_lshl_b32 s65, s64, 12
s_add_u32 m0, s62, 32768
Mvaa 160,0,32
Ra 60,26,14336
Mvaa 160,4,128
Mvaa 192,4,132
D 16,44,65,0
Ta 16,12,0
)"
R"(Mvaa 192,0,36
W 9
Ta 18,12,1024
Ta 20,12,8192
Mvaa 224,0,40
Ta 22,12,9216
Mvaa 224,4,136
Maaa 160,4,140
D 17,44,65,1024
Maaa 160,0,44
W 8
Maaa 192,0,48
Maaa 192,4,144
Maaa 224,4,148
D 18,44,65,2048
Maaa 224,0,52
W 4
Maaa 64,0,56
Maaa 64,4,152
Maaa 96,4,156
D 19,44,65,3072
Maaa 96,0,60
s_add_u32 m0, s62, 36864
Maaa 100,8,60
Maaa 100,12,156
Maaa 68,12,152
D 20,44,65,0
Ta 24,13,0
Maaa 68,8,56
Ta 26,13,1024
Ta 28,13,8192
Maaa 228,8,52
Ta 30,13,9216
Maaa 228,12,148
Maaa 196,12,144
D 21,44,65,1024
Maaa 196,8,48
Maaa 164,8,44
Maaa 164,12,140
Mvaa 228,12,136
D 22,44,65,2048
Mvaa 228,8,40
Mvaa 196,8,36
Mvaa 196,12,132
Mvaa 164,12,128
D 23,44,65,3072
Mvaa 164,8,32
W 4
Mvaa 168,16,32
Mvaa 168,20,128
Mvaa 200,20,132
global_load_dword v142, v[140:141], off
v_lshl_add_u64 v[140:141], v[140:141], 0, s[68:69]
Ta 0,14,0
Mvaa 200,16,36
Ta 2,14,1024
Ta 4,14,8192
Mvaa 232,16,40
Ta 6,14,9216
Mvaa 232,20,136
Maaa 168,20,140
Maaa 168,16,44
Maaa 200,16,48
Maaa 200,20,144
Maaa 232,20,148
Maaa 232,16,52
Maaa 72,16,56
Maaa 72,20,152
Maaa 104,20,156
Maaa 104,16,60
W 4
Maaa 108,24,60
Maaa 108,28,156
Maaa 76,28,152
Ta 8,15,0
Ta 10,15,1024
Maaa 76,24,56
Ta 12,15,8192
Ta 14,15,9216
Maaa 236,24,52
Maaa 236,28,148
Maaa 204,28,144
Maaa 204,24,48
Maaa 172,24,44
Maaa 172,28,140
Mvaa 236,28,136
Mvaa 236,24,40
Mvaa 204,24,36
Mvaa 204,28,132
Mvaa 172,28,128
Mvaa 172,24,32
W 4
Mvaa 176,0,32
Mvaa 176,4,128
Mvaa 208,4,132
Ta 16,24,0
Ta 18,24,1024
Mvaa 208,0,36
Ta 20,24,8192
Ta 22,24,9216
Mvaa 240,0,40
Mvaa 240,4,136
Maaa 176,4,140
Maaa 176,0,44
Maaa 208,0,48
Maaa 208,4,144
Maaa 240,4,148
Maaa 240,0,52
Maaa 80,0,56
Maaa 80,4,152
Maaa 112,4,156
Maaa 112,0,60
W 4
Maaa 116,8,60
Ta 24,25,0
Ta 26,25,1024
Maaa 116,12,156
Maaa 84,12,152
Ta 28,25,8192
Ta 30,25,9216
Maaa 84,8,56
Maaa 244,8,52
Maaa 244,12,148
Maaa 212,12,144
Maaa 212,8,48
Maaa 180,8,44
Maaa 180,12,140
Mvaa 244,12,136
s_waitcnt vmcnt(1) lgkmcnt(0)
s_barrier
s_add_u32 s64, s64, 128
Mvaa 244,8,40
Mvaa 212,8,36
Mvaa 212,12,132
s_and_b32 s64, s64, 1023
Mvaa 180,12,128
Mvaa 180,8,32
Mvaa 184,16,32
s_add_u32 m0, s61, 0
Mvaa 184,20,128
Mvaa 216,20,132
Ta 0,10,32768
Ta 2,10,33792
Mvaa 216,16,36
D 2,40,64,0
Ta 4,10,40960
Mvaa 248,16,40
Ta 6,10,41984
Mvaa 248,20,136
Maaa 184,20,140
Maaa 184,16,44
D 3,40,64,1024
Maaa 216,16,48
Maaa 216,20,144
Maaa 248,20,148
Maaa 248,16,52
D 4,40,64,2048
Maaa 88,16,56
Maaa 88,20,152
Maaa 120,20,156
Maaa 120,16,60
D 5,40,64,3072
Maaa 124,24,60
s_add_u32 m0, s61, 4096
Ra 60,26,47104
v_lshrrev_b32 v28, 1, v1
Maaa 124,28,156
s_lshl_b32 s80, s60, 9
Ra 156,27,47104
Maaa 92,28,152
s_add_u32 s80, s80, 40960
Ra 152,27,45056
Ta 8,11,32768
Maaa 92,24,56
D 6,40,64,0
Ta 10,11,33792
Maaa 252,24,52
Ra 56,26,45056
Ta 12,11,40960
Maaa 252,28,148
Ta 14,11,41984
Ra 52,26,43008
Maaa 220,28,144
Ra 148,27,43008
Ra 144,27,40960
Maaa 220,24,48
buffer_load_dwordx2 v[28:29], v28, s[8:11], s80 offen sc1
Ra 48,26,40960
Maaa 188,24,44
Ra 44,26,38912
Maaa 188,28,140
Ra 140,27,38912
Mvaa 252,28,136
Ra 136,27,36864
Mvaa 252,24,40
D 7,40,64,1024
Ra 40,26,36864
Mvaa 220,24,36
Ra 36,26,34816
Mvaa 220,28,132
Ra 132,27,34816
Mvaa 188,28,128
Ra 128,27,32768
Mvaa 188,24,32
W 13
D 8,40,64,2048
Ra 32,26,32768
Maaa 96,0,60
Maaa 96,4,156
Maaa 64,4,152
Ta 16,12,32768
Ta 18,12,33792
Maaa 64,0,56
W 10
D 9,40,64,3072
Ta 20,12,40960
Maaa 224,0,52
s_lshl_b32 s65, s64, 12
s_add_u32 m0, s62, 0
Ta 22,12,41984
Maaa 224,4,148
Maaa 192,4,144
Maaa 192,0,48
W 8
D 16,44,65,0
Maaa 160,0,44
Maaa 160,4,140
Mvaa 224,4,136
Mvaa 224,0,40
W 4
D 17,44,65,1024
Mvaa 192,0,36
Mvaa 192,4,132
Mvaa 160,4,128
Mvaa 160,0,32
D 18,44,65,2048
Mvaa 164,8,32
Mvaa 164,12,128
Mvaa 196,12,132
Ta 24,13,32768
Ta 26,13,33792
Mvaa 196,8,36
D 19,44,65,3072
Ta 28,13,40960
Mvaa 228,8,40
s_add_u32 m0, s62, 4096
Ta 30,13,41984
Mvaa 228,12,136
Maaa 164,12,140
Maaa 164,8,44
D 20,44,65,0
Maaa 196,8,48
Maaa 196,12,144
Maaa 228,12,148
Maaa 228,8,52
D 21,44,65,1024
Maaa 68,8,56
Maaa 68,12,152
Maaa 100,12,156
Maaa 100,8,60
W 4
D 22,44,65,2048
Maaa 104,16,60
Maaa 104,20,156
Maaa 72,20,152
Ta 0,14,32768
Ta 2,14,33792
Maaa 72,16,56
D 23,44,65,3072
Ta 4,14,40960
Maaa 232,16,52
Ta 6,14,41984
Maaa 232,20,148
Maaa 200,20,144
s_waitcnt vmcnt(11)
v_cmp_eq_u32 vcc, s13, v29
s_cmp_eq_u64 vcc, -1
s_cbranch_scc0 .Lspin_13%=
.Lok_14%=:
Maaa 200,16,48
Maaa 168,16,44
v_bfe_u32 v144, v1, 8, 1
Maaa 168,20,140
v_mul_u32_u24 v144, 10240, v144
Mvaa 232,20,136
v_and_b32 v145, 0xf0, v1
Mvaa 232,16,40
v_lshl_add_u32 v144, v145, 3, v144
Mvaa 200,16,36
s_mul_i32 s91, s58, 20480
Mvaa 200,20,132
s_add_u32 s91, s91, s28
Mvaa 168,20,128
buffer_load_dword v142, v144, s[8:11], s91 offen offset:0 sc1
Mvaa 168,16,32
W 4
Mvaa 172,24,32
Mvaa 172,28,128
Mvaa 204,28,132
Ta 8,15,32768
Ta 10,15,33792
Mvaa 204,24,36
Ta 12,15,40960
Ta 14,15,41984
Mvaa 236,24,40
Mvaa 236,28,136
Maaa 172,28,140
Maaa 172,24,44
Maaa 204,24,48
Maaa 204,28,144
Maaa 236,28,148
Maaa 236,24,52
Maaa 76,24,56
Maaa 76,28,152
Maaa 108,28,156
Maaa 108,24,60
W 4
Maaa 112,0,60
Maaa 112,4,156
Maaa 80,4,152
Ta 16,24,32768
Ta 18,24,33792
Maaa 80,0,56
Ta 20,24,40960
Ta 22,24,41984
Maaa 240,0,52
Maaa 240,4,148
Maaa 208,4,144
Maaa 208,0,48
Maaa 176,0,44
Maaa 176,4,140
Mvaa 240,4,136
Mvaa 240,0,40
Mvaa 208,0,36
Mvaa 208,4,132
Mvaa 176,4,128
Mvaa 176,0,32
W 4
Mvaa 180,8,32
Ta 24,25,32768
Ta 26,25,33792
Mvaa 180,12,128
Mvaa 212,12,132
Ta 28,25,40960
Ta 30,25,41984
Mvaa 212,8,36
Mvaa 244,8,40
Mvaa 244,12,136
Maaa 180,12,140
Maaa 180,8,44
Maaa 212,8,48
Maaa 212,12,144
Maaa 244,12,148
s_waitcnt vmcnt(1) lgkmcnt(0)
s_barrier
s_add_u32 s64, s64, 128
Maaa 244,8,52
Maaa 84,8,56
Maaa 84,12,152
s_and_b32 s64, s64, 1023
Maaa 116,12,156
Maaa 116,8,60
s_add_u32 m0, s61, 32768
Maaa 120,16,60
Maaa 120,20,156
Maaa 88,20,152
D 2,40,64,0
Ta 0,10,0
Maaa 88,16,56
Ta 2,10,1024
Ta 4,10,8192
Maaa 248,16,52
Ta 6,10,9216
Maaa 248,20,148
Maaa 216,20,144
D 3,40,64,1024
Maaa 216,16,48
Maaa 184,16,44
Maaa 184,20,140
Mvaa 248,20,136
D 4,40,64,2048
Mvaa 248,16,40
Mvaa 216,16,36
Mvaa 216,20,132
Mvaa 184,20,128
D 5,40,64,3072
Mvaa 184,16,32
s_add_u32 m0, s61, 36864
Mvaa 188,24,32
Ra 32,26,0
Mvaa 188,28,128
Ra 128,27,0
Mvaa 220,28,132
D 6,40,64,0
Ra 132,27,2048
Mvaa 220,24,36
Ta 8,11,0
Ta 10,11,1024
Mvaa 252,24,40
Ra 36,26,2048
)"
R"(Ta 12,11,8192
Mvaa 252,28,136
Ta 14,11,9216
Ra 40,26,4096
Maaa 188,28,140
D 7,40,64,1024
Ra 136,27,4096
Maaa 188,24,44
Ra 140,27,6144
Ra 44,26,6144
Maaa 220,24,48
Ra 48,26,8192
Maaa 220,28,144
Ra 144,27,8192
Maaa 252,28,148
D 8,40,64,2048
Ra 148,27,10240
Maaa 252,24,52
Ra 52,26,10240
Maaa 92,24,56
Ra 56,26,12288
Maaa 92,28,152
Ra 152,27,12288
Maaa 124,28,156
D 9,40,64,3072
Ra 156,27,14336
Maaa 124,24,60
W 13
s_lshl_b32 s65, s64, 12
s_add_u32 m0, s62, 32768
Mvaa 160,0,32
Ra 60,26,14336
Mvaa 160,4,128
Mvaa 192,4,132
D 16,44,65,0
Ta 16,12,0
Mvaa 192,0,36
W 9
Ta 18,12,1024
Ta 20,12,8192
Mvaa 224,0,40
Ta 22,12,9216
Mvaa 224,4,136
Maaa 160,4,140
D 17,44,65,1024
Maaa 160,0,44
W 8
Maaa 192,0,48
Maaa 192,4,144
Maaa 224,4,148
D 18,44,65,2048
Maaa 224,0,52
W 4
Maaa 64,0,56
Maaa 64,4,152
Maaa 96,4,156
D 19,44,65,3072
Maaa 96,0,60
s_add_u32 m0, s62, 36864
Maaa 100,8,60
Maaa 100,12,156
Maaa 68,12,152
D 20,44,65,0
Ta 24,13,0
Maaa 68,8,56
Ta 26,13,1024
Ta 28,13,8192
Maaa 228,8,52
Ta 30,13,9216
Maaa 228,12,148
Maaa 196,12,144
D 21,44,65,1024
Maaa 196,8,48
Maaa 164,8,44
Maaa 164,12,140
Mvaa 228,12,136
D 22,44,65,2048
Mvaa 228,8,40
Mvaa 196,8,36
Mvaa 196,12,132
Mvaa 164,12,128
D 23,44,65,3072
Mvaa 164,8,32
W 4
s_add_u32 s65, s5, 0
s_cmp_ge_u32 s65, 20
s_cselect_b32 s90, 20, 0
Mvaa 168,16,32
s_sub_u32 s65, s65, s90
s_lshl_b32 s65, s65, 18
s_add_u32 m0, s63, 131072
Mvaa 168,20,128
Mvaa 200,20,132
D 16,48,65,0
Ta 0,14,0
Mvaa 200,16,36
Ta 2,14,1024
Ta 4,14,8192
Mvaa 232,16,40
Ta 6,14,9216
Mvaa 232,20,136
Maaa 168,20,140
D 17,48,65,1024
Maaa 168,16,44
Maaa 200,16,48
Maaa 200,20,144
Maaa 232,20,148
D 18,48,65,2048
Maaa 232,16,52
Maaa 72,16,56
Maaa 72,20,152
Maaa 104,20,156
D 19,48,65,3072
Maaa 104,16,60
W 4
s_add_u32 m0, s63, 135168
Maaa 108,24,60
Maaa 108,28,156
Maaa 76,28,152
D 20,48,65,0
Ta 8,15,0
Maaa 76,24,56
Ta 10,15,1024
Ta 12,15,8192
Maaa 236,24,52
Ta 14,15,9216
Maaa 236,28,148
Maaa 204,28,144
buffer_load_dword v142, v144, s[8:11], s91 offen offset:2048 sc1
Maaa 204,24,48
s_add_u32 s91, s91, 4096
Maaa 172,24,44
Maaa 172,28,140
Mvaa 236,28,136
D 21,48,65,1024
Mvaa 236,24,40
Mvaa 204,24,36
Mvaa 204,28,132
Mvaa 172,28,128
D 22,48,65,2048
Mvaa 172,24,32
W 4
Mvaa 176,0,32
Mvaa 176,4,128
Mvaa 208,4,132
D 23,48,65,3072
v_lshrrev_b32 v138, 4, v1
Mvaa 208,0,36
s_sub_u32 s81, s73, 1
v_min_u32 v138, s81, v138
v_lshrrev_b32 v139, 2, v138
Mvaa 240,0,40
v_mul_u32_u24 v139, s72, v139
v_and_b32 v138, 3, v138
Mvaa 240,4,136
v_lshl_add_u32 v138, v138, 7, v139
v_add_u32 v138, s74, v138
Maaa 176,4,140
s_lshr_b32 s73, s73, 2
s_mul_i32 s73, s73, s72
v_min_u32 v139, s75, v138
Maaa 176,0,44
global_load_dword v142, v139, s[76:77]
v_add_u32 v138, s73, v138
Maaa 208,0,48
Ta 16,24,0
Ta 18,24,1024
Maaa 208,4,144
Ta 20,24,8192
Ta 22,24,9216
Maaa 240,4,148
Maaa 240,0,52
Maaa 80,0,56
Maaa 80,4,152
Maaa 112,4,156
Maaa 112,0,60
W 4
Maaa 116,8,60
Ta 24,25,0
Ta 26,25,1024
Maaa 116,12,156
Maaa 84,12,152
Ta 28,25,8192
Ta 30,25,9216
Maaa 84,8,56
Maaa 244,8,52
Maaa 244,12,148
Maaa 212,12,144
Maaa 212,8,48
Maaa 180,8,44
Maaa 180,12,140
Mvaa 244,12,136
s_waitcnt vmcnt(1) lgkmcnt(0)
s_barrier
s_add_u32 s64, s64, 128
Mvaa 244,8,40
Mvaa 212,8,36
Mvaa 212,12,132
s_and_b32 s64, s64, 1023
Mvaa 180,12,128
Mvaa 180,8,32
Mvaa 184,16,32
s_add_u32 m0, s61, 0
Mvaa 184,20,128
Mvaa 216,20,132
Ta 0,10,32768
Ta 2,10,33792
Mvaa 216,16,36
D 2,40,64,0
Ta 4,10,40960
Mvaa 248,16,40
Ta 6,10,41984
Mvaa 248,20,136
Maaa 184,20,140
Maaa 184,16,44
D 3,40,64,1024
Maaa 216,16,48
Maaa 216,20,144
Maaa 248,20,148
Maaa 248,16,52
D 4,40,64,2048
Maaa 88,16,56
Maaa 88,20,152
Maaa 120,20,156
Maaa 120,16,60
D 5,40,64,3072
Maaa 124,24,60
s_add_u32 m0, s61, 4096
Ra 60,26,47104
Maaa 124,28,156
Ra 156,27,47104
Maaa 92,28,152
Ra 152,27,45056
Ta 8,11,32768
Maaa 92,24,56
D 6,40,64,0
Ta 10,11,33792
Maaa 252,24,52
Ra 56,26,45056
Ta 12,11,40960
Maaa 252,28,148
Ta 14,11,41984
Ra 52,26,43008
Maaa 220,28,144
Ra 148,27,43008
Ra 144,27,40960
Maaa 220,24,48
D 7,40,64,1024
Ra 48,26,40960
Maaa 188,24,44
Ra 44,26,38912
Maaa 188,28,140
Ra 140,27,38912
Mvaa 252,28,136
Ra 136,27,36864
Mvaa 252,24,40
D 8,40,64,2048
Ra 40,26,36864
Mvaa 220,24,36
Ra 36,26,34816
Mvaa 220,28,132
Ra 132,27,34816
Mvaa 188,28,128
Ra 128,27,32768
Mvaa 188,24,32
W 13
D 9,40,64,3072
Ra 32,26,32768
Maaa 96,0,60
s_lshl_b32 s65, s64, 12
s_add_u32 m0, s62, 0
Maaa 96,4,156
Maaa 64,4,152
Ta 16,12,32768
Ta 18,12,33792
Maaa 64,0,56
W 10
D 16,44,65,0
Ta 20,12,40960
Maaa 224,0,52
Ta 22,12,41984
Maaa 224,4,148
Maaa 192,4,144
Maaa 192,0,48
W 8
D 17,44,65,1024
Maaa 160,0,44
Maaa 160,4,140
Mvaa 224,4,136
Mvaa 224,0,40
W 4
D 18,44,65,2048
Mvaa 192,0,36
Mvaa 192,4,132
Mvaa 160,4,128
Mvaa 160,0,32
D 19,44,65,3072
Mvaa 164,8,32
s_add_u32 m0, s62, 4096
Mvaa 164,12,128
Mvaa 196,12,132
Ta 24,13,32768
Ta 26,13,33792
Mvaa 196,8,36
D 20,44,65,0
Ta 28,13,40960
Mvaa 228,8,40
Ta 30,13,41984
Mvaa 228,12,136
Maaa 164,12,140
Maaa 164,8,44
D 21,44,65,1024
Maaa 196,8,48
Maaa 196,12,144
Maaa 228,12,148
Maaa 228,8,52
D 22,44,65,2048
Maaa 68,8,56
Maaa 68,12,152
Maaa 100,12,156
Maaa 100,8,60
W 4
D 23,44,65,3072
Maaa 104,16,60
v_min_u32 v139, s75, v138
Maaa 104,20,156
Maaa 72,20,152
Ta 0,14,32768
Ta 2,14,33792
Maaa 72,16,56
buffer_load_dword v142, v144, s[8:11], s91 offen offset:0 sc1
Ta 4,14,40960
Maaa 232,16,52
Ta 6,14,41984
Maaa 232,20,148
Maaa 200,20,144
Maaa 200,16,48
global_load_dword v142, v139, s[76:77]
Maaa 168,16,44
Maaa 168,20,140
Mvaa 232,20,136
v_add_u32 v138, s73, v138
Mvaa 232,16,40
global_load_dword v142, v146, s[6:7]
Mvaa 200,16,36
Mvaa 200,20,132
Mvaa 168,20,128
Mvaa 168,16,32
W 4
Mvaa 172,24,32
Mvaa 172,28,128
Mvaa 204,28,132
Ta 8,15,32768
Ta 10,15,33792
Mvaa 204,24,36
Ta 12,15,40960
Ta 14,15,41984
Mvaa 236,24,40
Mvaa 236,28,136
Maaa 172,28,140
Maaa 172,24,44
Maaa 204,24,48
Maaa 204,28,144
Maaa 236,28,148
Maaa 236,24,52
Maaa 76,24,56
Maaa 76,28,152
Maaa 108,28,156
Maaa 108,24,60
W 4
Maaa 112,0,60
Maaa 112,4,156
Maaa 80,4,152
Ta 16,24,32768
Ta 18,24,33792
Maaa 80,0,56
Ta 20,24,40960
Ta 22,24,41984
Maaa 240,0,52
Maaa 240,4,148
Maaa 208,4,144
Maaa 208,0,48
Maaa 176,0,44
Maaa 176,4,140
Mvaa 240,4,136
Mvaa 240,0,40
Mvaa 208,0,36
Mvaa 208,4,132
Mvaa 176,4,128
Mvaa 176,0,32
W 4
Mvaa 180,8,32
Ta 24,25,32768
Ta 26,25,33792
Mvaa 180,12,128
Mvaa 212,12,132
Ta 28,25,40960
Ta 30,25,41984
Mvaa 212,8,36
Mvaa 244,8,40
)"
R"(Mvaa 244,12,136
Maaa 180,12,140
Maaa 180,8,44
Maaa 212,8,48
Maaa 212,12,144
Maaa 244,12,148
s_waitcnt vmcnt(3) lgkmcnt(0)
s_barrier
s_add_u32 s64, s64, 128
Maaa 244,8,52
Maaa 84,8,56
Maaa 84,12,152
s_and_b32 s64, s64, 1023
Maaa 116,12,156
Maaa 116,8,60
Maaa 120,16,60
s_add_u32 m0, s61, 32768
Maaa 120,20,156
Maaa 88,20,152
Ta 0,10,0
Ta 2,10,1024
Maaa 88,16,56
D 2,40,64,0
Ta 4,10,8192
Maaa 248,16,52
Ta 6,10,9216
Maaa 248,20,148
Maaa 216,20,144
Maaa 216,16,48
D 3,40,64,1024
Maaa 184,16,44
Maaa 184,20,140
Mvaa 248,20,136
Mvaa 248,16,40
D 4,40,64,2048
Mvaa 216,16,36
Mvaa 216,20,132
Mvaa 184,20,128
Mvaa 184,16,32
D 5,40,64,3072
Mvaa 188,24,32
s_add_u32 m0, s61, 36864
Ra 32,26,0
Mvaa 188,28,128
Ra 128,27,0
Mvaa 220,28,132
Ra 132,27,2048
Ta 8,11,0
Mvaa 220,24,36
D 6,40,64,0
Ta 10,11,1024
Mvaa 252,24,40
Ra 36,26,2048
Ta 12,11,8192
Mvaa 252,28,136
Ta 14,11,9216
Ra 40,26,4096
Maaa 188,28,140
Ra 136,27,4096
Ra 140,27,6144
Maaa 188,24,44
D 7,40,64,1024
Ra 44,26,6144
Maaa 220,24,48
Ra 48,26,8192
Maaa 220,28,144
Ra 144,27,8192
Maaa 252,28,148
Ra 148,27,10240
Maaa 252,24,52
D 8,40,64,2048
Ra 52,26,10240
Maaa 92,24,56
Ra 56,26,12288
Maaa 92,28,152
Ra 152,27,12288
Maaa 124,28,156
Ra 156,27,14336
Maaa 124,24,60
W 13
D 9,40,64,3072
Ra 60,26,14336
Mvaa 160,0,32
s_lshl_b32 s65, s64, 12
s_add_u32 m0, s62, 32768
Mvaa 160,4,128
Mvaa 192,4,132
Ta 16,12,0
Ta 18,12,1024
Mvaa 192,0,36
W 10
D 16,44,65,0
Ta 20,12,8192
Mvaa 224,0,40
Ta 22,12,9216
Mvaa 224,4,136
Maaa 160,4,140
Maaa 160,0,44
W 8
D 17,44,65,1024
Maaa 192,0,48
Maaa 192,4,144
Maaa 224,4,148
Maaa 224,0,52
W 4
D 18,44,65,2048
Maaa 64,0,56
Maaa 64,4,152
Maaa 96,4,156
Maaa 96,0,60
D 19,44,65,3072
Maaa 100,8,60
s_add_u32 m0, s62, 36864
Maaa 100,12,156
Maaa 68,12,152
Ta 24,13,0
Ta 26,13,1024
Maaa 68,8,56
D 20,44,65,0
Ta 28,13,8192
Maaa 228,8,52
Ta 30,13,9216
Maaa 228,12,148
Maaa 196,12,144
Maaa 196,8,48
D 21,44,65,1024
Maaa 164,8,44
Maaa 164,12,140
Mvaa 228,12,136
Mvaa 228,8,40
D 22,44,65,2048
Mvaa 196,8,36
Mvaa 196,12,132
Mvaa 164,12,128
Mvaa 164,8,32
W 4
D 23,44,65,3072
Mvaa 168,16,32
v_min_u32 v139, s75, v138
Mvaa 168,20,128
Mvaa 200,20,132
Ta 0,14,0
Ta 2,14,1024
Mvaa 200,16,36
buffer_load_dword v142, v144, s[8:11], s91 offen offset:2048 sc1
Ta 4,14,8192
Mvaa 232,16,40
Ta 6,14,9216
Mvaa 232,20,136
s_add_u32 s91, s91, 4096
Maaa 168,20,140
Maaa 168,16,44
global_load_dword v142, v139, s[76:77]
Maaa 200,16,48
Maaa 200,20,144
Maaa 232,20,148
v_add_u32 v138, s73, v138
v_add_u32 v2, 131072, v10
Maaa 232,16,52
v_add_u32 v3, 131072, v11
v_add_u32 v4, 131072, v12
Maaa 72,16,56
v_add_u32 v5, 131072, v13
v_add_u32 v6, 131072, v14
Maaa 72,20,152
v_add_u32 v7, 131072, v15
v_add_u32 v8, 131072, v24
Maaa 104,20,156
v_add_u32 v9, 131072, v25
Maaa 104,16,60
W 4
Maaa 108,24,60
Maaa 108,28,156
Maaa 76,28,152
Ta 8,15,0
Ta 10,15,1024
Maaa 76,24,56
Ta 12,15,8192
Ta 14,15,9216
Maaa 236,24,52
Maaa 236,28,148
Maaa 204,28,144
Maaa 204,24,48
Maaa 172,24,44
Maaa 172,28,140
Mvaa 236,28,136
Mvaa 236,24,40
Mvaa 204,24,36
Mvaa 204,28,132
Mvaa 172,28,128
Mvaa 172,24,32
W 4
Mvaa 176,0,32
Mvaa 176,4,128
Mvaa 208,4,132
Ta 16,24,0
Ta 18,24,1024
Mvaa 208,0,36
Ta 20,24,8192
Ta 22,24,9216
Mvaa 240,0,40
Mvaa 240,4,136
Maaa 176,4,140
Maaa 176,0,44
Maaa 208,0,48
Maaa 208,4,144
Maaa 240,4,148
Maaa 240,0,52
Maaa 80,0,56
Maaa 80,4,152
Maaa 112,4,156
Maaa 112,0,60
W 4
Maaa 116,8,60
Ta 24,25,0
Ta 26,25,1024
Maaa 116,12,156
Maaa 84,12,152
Ta 28,25,8192
Ta 30,25,9216
Maaa 84,8,56
Maaa 244,8,52
Maaa 244,12,148
Maaa 212,12,144
Maaa 212,8,48
Maaa 180,8,44
Maaa 180,12,140
Mvaa 244,12,136
s_waitcnt vmcnt(2) lgkmcnt(0)
s_barrier
s_add_u32 s65, s5, 4
Mvaa 244,8,40
Mvaa 212,8,36
Mvaa 212,12,132
s_cmp_ge_u32 s65, 20
s_cselect_b32 s90, 20, 0
Mvaa 180,12,128
Mvaa 180,8,32
s_sub_u32 s65, s65, s90
Mvaa 184,16,32
Mvaa 184,20,128
s_lshl_b32 s65, s65, 18
Mvaa 216,20,132
Ta 0,10,32768
Ta 2,10,33792
Mvaa 216,16,36
Mvaa 248,16,40
s_add_u32 m0, s63, 0
Mvaa 248,20,136
Maaa 184,20,140
D 16,48,65,0
Maaa 184,16,44
Maaa 216,16,48
Maaa 216,20,144
Maaa 248,20,148
D 17,48,65,1024
Ta 4,11,32768
Maaa 248,16,52
Ta 6,11,33792
Maaa 88,16,56
Maaa 88,20,152
Maaa 120,20,156
D 18,48,65,2048
Maaa 120,16,60
Maaa 124,24,60
Ra 60,26,47104
Maaa 124,28,156
Maaa 92,28,152
D 19,48,65,3072
Ta 8,12,32768
Maaa 92,24,56
s_add_u32 m0, s63, 4096
Ta 10,12,33792
Ra 56,26,45056
Maaa 252,24,52
Ra 52,26,43008
Maaa 252,28,148
Maaa 220,28,144
D 20,48,65,0
Maaa 220,24,48
Ra 48,26,40960
Maaa 188,24,44
Ra 44,26,38912
Maaa 188,28,140
Mvaa 252,28,136
D 21,48,65,1024
Ta 12,13,32768
Mvaa 252,24,40
Ta 14,13,33792
Ra 40,26,36864
Mvaa 220,24,36
Ra 36,26,34816
Mvaa 220,28,132
Mvaa 188,28,128
D 22,48,65,2048
Mvaa 188,24,32
Ra 32,26,32768
W 6
Maaa 96,0,60
Maaa 64,0,56
Maaa 224,0,52
D 23,48,65,3072
Ta 16,14,32768
Maaa 192,0,48
W 1
s_add_u32 s65, s5, 8
s_cmp_ge_u32 s65, 20
s_cselect_b32 s90, 20, 0
Maaa 160,0,44
s_sub_u32 s65, s65, s90
s_lshl_b32 s65, s65, 18
Ta 18,14,33792
Mvaa 224,0,40
s_add_u32 m0, s63, 65536
Mvaa 192,0,36
D 16,48,65,0
Mvaa 160,0,32
Mvaa 164,4,32
Mvaa 196,4,36
Mvaa 228,4,40
D 17,48,65,1024
Ta 20,15,32768
Maaa 164,4,44
Ta 22,15,33792
Maaa 196,4,48
Maaa 228,4,52
Maaa 68,4,56
D 18,48,65,2048
Maaa 100,4,60
Maaa 104,8,60
Maaa 72,8,56
Maaa 232,8,52
D 19,48,65,3072
Ta 24,24,32768
Maaa 200,8,48
s_add_u32 m0, s63, 69632
Ta 26,24,33792
Maaa 168,8,44
Mvaa 232,8,40
Mvaa 200,8,36
D 20,48,65,0
Mvaa 168,8,32
Mvaa 172,12,32
Mvaa 204,12,36
Mvaa 236,12,40
D 21,48,65,1024
Ta 28,25,32768
Maaa 172,12,44
Ta 30,25,33792
Maaa 204,12,48
Ra 156,27,47104
Maaa 236,12,52
Maaa 76,12,56
D 22,48,65,2048
Maaa 108,12,60
W 7
Ra 152,27,45056
Maaa 112,16,60
Maaa 80,16,56
Maaa 240,16,52
D 23,48,65,3072
Ra 148,27,43008
Maaa 208,16,48
Ta 0,10,40960
Ta 2,10,41984
Maaa 176,16,44
Mvaa 240,16,40
Ra 144,27,40960
Mvaa 208,16,36
buffer_load_dword v142, v144, s[8:11], s91 offen offset:0 sc1
Mvaa 176,16,32
W 10
Mvaa 180,20,32
Ra 140,27,38912
Mvaa 212,20,36
Mvaa 244,20,40
Ta 4,11,40960
Ta 6,11,41984
Maaa 180,20,44
Ra 136,27,36864
Maaa 212,20,48
Maaa 244,20,52
v_bfe_u32 v29, v1, 7, 1
Maaa 84,20,56
Ra 132,27,34816
Maaa 116,20,60
W 13
v_mul_u32_u24 v28, 10240, v29
Maaa 120,24,60
Maaa 88,24,56
Ra 128,27,32768
v_lshrrev_b32 v29, 8, v1
Maaa 248,24,52
Ta 8,12,40960
Ta 10,12,41984
Maaa 216,24,48
v_lshl_add_u32 v28, v29, 7, v28
Maaa 184,24,44
v_and_b32 v29, 0x70, v1
Mvaa 248,24,40
)"
R"(Mvaa 216,24,36
v_add_u32 v28, v28, v29
Mvaa 184,24,32
W 14
Mvaa 188,28,32
s_and_b32 s80, s14, 2
Mvaa 220,28,36
s_mul_i32 s80, s80, 20480
Mvaa 252,28,40
Ta 12,13,40960
Ta 14,13,41984
Maaa 188,28,44
s_sub_u32 s81, 40960, s80
Maaa 220,28,48
Maaa 252,28,52
s_add_u32 s93, s28, 20480
Maaa 92,28,56
Maaa 124,28,60
W 10
v_add_u32 v26, s80, v28
Maaa 96,0,156
Maaa 64,0,152
Maaa 224,0,148
Ta 16,14,40960
v_add_u32 v27, s81, v28
Maaa 192,0,144
W 5
Ta 18,14,41984
Maaa 160,0,140
buffer_load_dwordx4 a[32:35], v26, s[8:11], s28 offen offset:0 sc1
Mvaa 224,0,136
Mvaa 192,0,132
Mvaa 160,0,128
Mvaa 164,4,128
buffer_load_dwordx4 a[36:39], v26, s[8:11], s93 offen offset:0 sc1
Mvaa 196,4,132
Mvaa 228,4,136
Ta 20,15,40960
Ta 22,15,41984
Maaa 164,4,140
Maaa 196,4,144
buffer_load_dwordx4 a[40:43], v27, s[8:11], s28 offen offset:0 sc1
Maaa 228,4,148
Maaa 68,4,152
Maaa 100,4,156
W 6
Maaa 104,8,156
buffer_load_dwordx4 a[44:47], v27, s[8:11], s93 offen offset:0 sc1
Ta 24,24,40960
Maaa 72,8,152
s_add_u32 s28, s28, 512
s_cmp_eq_u32 s28, s4
s_cselect_b32 s93, 10240, 0
Ta 26,24,41984
Maaa 232,8,148
s_sub_u32 s28, s28, s93
Ta 28,25,40960
Ta 30,25,41984
Maaa 200,8,144
s_add_u32 s93, s28, 20480
Maaa 168,8,140
Mvaa 232,8,136
Mvaa 200,8,132
s_add_u32 s65, s5, 12
s_cmp_ge_u32 s65, 20
s_cselect_b32 s90, 20, 0
buffer_load_dwordx4 a[48:51], v26, s[8:11], s28 offen offset:0 sc1
Mvaa 168,8,128
W 8
s_sub_u32 s65, s65, s90
s_lshl_b32 s65, s65, 18
Mvaa 172,12,128
s_add_u32 m0, s63, 32768
Mvaa 204,12,132
Mvaa 236,12,136
s_waitcnt vmcnt(6) lgkmcnt(0)
s_barrier
buffer_load_dwordx4 a[52:55], v26, s[8:11], s93 offen offset:0 sc1
Maaa 172,12,140
Maaa 204,12,144
Maaa 236,12,148
Maaa 76,12,152
buffer_load_dwordx4 a[56:59], v27, s[8:11], s28 offen offset:0 sc1
Maaa 108,12,156
Maaa 112,16,156
Maaa 80,16,152
Maaa 240,16,148
buffer_load_dwordx4 a[60:63], v27, s[8:11], s93 offen offset:0 sc1
Maaa 208,16,144
Maaa 176,16,140
Mvaa 240,16,136
Mvaa 208,16,132
D 16,48,65,0
Mvaa 176,16,128
Mvaa 180,20,128
Mvaa 212,20,132
Mvaa 244,20,136
D 17,48,65,1024
Ta 0,2,0
Maaa 180,20,140
Ta 2,2,1024
Maaa 212,20,144
Maaa 244,20,148
Maaa 84,20,152
D 18,48,65,2048
Ta 4,2,8192
Maaa 116,20,156
Ta 6,2,9216
Maaa 120,24,156
Maaa 88,24,152
Maaa 248,24,148
Ta 8,3,0
Ta 10,3,1024
Maaa 216,24,144
D 19,48,65,3072
Maaa 184,24,140
s_add_u32 m0, s63, 36864
Mvaa 248,24,136
Mvaa 216,24,132
Ta 12,3,8192
Ta 14,3,9216
Mvaa 184,24,128
D 20,48,65,0
Mvaa 188,28,128
Mvaa 220,28,132
Mvaa 252,28,136
Ta 16,4,0
Ta 18,4,1024
Maaa 188,28,140
Maaa 220,28,144
D 21,48,65,1024
Maaa 252,28,148
Maaa 92,28,152
Maaa 124,28,156
s_branch .Lq_10%=
.Lv_9%=:
s_waitcnt vmcnt(14)
s_barrier
Ta 0,10,16384
Ta 2,10,17408
Ra 32,26,0
Ta 4,10,24576
Ta 6,10,25600
Ra 128,27,0
Ra 132,27,2048
Ra 36,26,2048
Ra 40,26,4096
Ra 136,27,4096
Ra 140,27,6144
Ra 44,26,6144
Ra 48,26,8192
Ra 144,27,8192
W 6
Zvaa 160,32,0
s_add_u32 m0, s62, 32768
Ra 148,27,10240
Ra 52,26,10240
Ra 56,26,12288
Ra 152,27,12288
Ra 156,27,14336
Ra 60,26,14336
Mvaa 160,128,4
Ta 8,11,16384
Ta 10,11,17408
Ta 12,11,24576
Ta 14,11,25600
Zvaa 164,132,4
Ta 16,12,16384
Ta 18,12,17408
Mvaa 164,36,0
W 14
Ta 20,12,24576
Ta 22,12,25600
Zvaa 224,40,0
D 16,44,65,0
Mvaa 224,136,4
Zvaa 228,140,4
Mvaa 228,44,0
W 12
Zaaa 192,48,0
D 17,44,65,1024
Maaa 192,144,4
Zaaa 196,148,4
s_waitcnt vmcnt(13)
Maaa 196,52,0
W 8
v_lshlrev_b32 v56, 16, v32
v_and_b32 v57, 0xffff0000, v32
v_lshlrev_b32 v58, 16, v33
v_and_b32 v59, 0xffff0000, v33
Zaaa 64,56,0
D 18,44,65,2048
v_lshlrev_b32 v60, 16, v34
v_and_b32 v61, 0xffff0000, v34
v_lshlrev_b32 v62, 16, v35
v_and_b32 v63, 0xffff0000, v35
Maaa 64,152,4
v_lshlrev_b32 v64, 16, v36
v_and_b32 v65, 0xffff0000, v36
v_lshlrev_b32 v66, 16, v37
v_and_b32 v67, 0xffff0000, v37
v_lshlrev_b32 v68, 16, v38
Zaaa 68,156,4
v_and_b32 v69, 0xffff0000, v38
v_lshlrev_b32 v70, 16, v39
v_and_b32 v71, 0xffff0000, v39
v_lshlrev_b32 v72, 16, v40
Maaa 68,60,0
W 4
v_and_b32 v73, 0xffff0000, v40
v_lshlrev_b32 v74, 16, v41
v_and_b32 v75, 0xffff0000, v41
v_lshlrev_b32 v76, 16, v42
v_and_b32 v77, 0xffff0000, v42
Zaaa 76,60,8
D 19,44,65,3072
v_lshlrev_b32 v78, 16, v43
v_and_b32 v79, 0xffff0000, v43
A 80,56,57
A 81,58,59
Maaa 76,156,12
s_add_u32 m0, s62, 36864
A 80,80,60
A 81,81,61
A 80,80,62
A 81,81,63
A 80,80,64
Zaaa 72,152,12
Ta 24,13,16384
Ta 26,13,17408
A 81,81,65
A 80,80,66
A 81,81,67
A 80,80,68
Maaa 72,56,8
Ta 28,13,24576
Ta 30,13,25600
A 81,81,69
A 80,80,70
A 81,81,71
A 80,80,72
Zaaa 204,52,8
D 20,44,65,0
A 81,81,73
A 80,80,74
A 81,81,75
A 80,80,76
A 81,81,77
Maaa 204,148,12
A 80,80,78
A 81,81,79
A 80,80,81
s_nop 1
v_add_f32_dpp v80, v80, v80 quad_perm:[1,0,3,2] row_mask:0xf bank_mask:0xf
Zaaa 200,144,12
Maaa 200,48,8
v_add_f32_dpp v80, v80, v80 quad_perm:[2,3,0,1] row_mask:0xf bank_mask:0xf
s_nop 1
v_add_f32_dpp v80, v80, v80 row_half_mirror row_mask:0xf bank_mask:0xf
s_nop 1
v_add_f32_dpp v80, v80, v80 row_mirror row_mask:0xf bank_mask:0xf
v_mov_b32 v81, v80
s_nop 1
v_permlane16_swap_b32 v80, v81
A 80,80,81
Zvaa 236,44,8
D 21,44,65,1024
v_mul_f32 v84, 0x3acccccd, v80
s_mov_b32 s86, 0xffff
s_mov_b32 s87, 0xffff
v_sub_f32 v82, v56, v84
U 80,82,82
v_sub_f32 v83, v57, v84
Mvaa 236,140,12
v_fmac_f32 v80, v83, v83
v_sub_f32 v82, v58, v84
v_fmac_f32 v80, v82, v82
v_sub_f32 v83, v59, v84
v_fmac_f32 v80, v83, v83
v_sub_f32 v82, v60, v84
Zvaa 232,136,12
v_fmac_f32 v80, v82, v82
v_sub_f32 v83, v61, v84
v_fmac_f32 v80, v83, v83
v_sub_f32 v82, v62, v84
v_fmac_f32 v80, v82, v82
Mvaa 232,40,8
v_sub_f32 v83, v63, v84
v_fmac_f32 v80, v83, v83
v_sub_f32 v82, v64, v84
v_fmac_f32 v80, v82, v82
Zvaa 172,36,8
D 22,44,65,2048
v_sub_f32 v83, v65, v84
v_fmac_f32 v80, v83, v83
v_sub_f32 v82, v66, v84
v_fmac_f32 v80, v82, v82
Mvaa 172,132,12
v_sub_f32 v83, v67, v84
v_fmac_f32 v80, v83, v83
v_sub_f32 v82, v68, v84
v_fmac_f32 v80, v82, v82
v_sub_f32 v83, v69, v84
Zvaa 168,128,12
v_fmac_f32 v80, v83, v83
v_sub_f32 v82, v70, v84
v_fmac_f32 v80, v82, v82
v_sub_f32 v83, v71, v84
Mvaa 168,32,8
W 4
v_fmac_f32 v80, v83, v83
v_sub_f32 v82, v72, v84
U 81,82,82
v_sub_f32 v83, v73, v84
v_fmac_f32 v81, v83, v83
Zvaa 176,32,16
D 23,44,65,3072
v_sub_f32 v82, v74, v84
v_fmac_f32 v81, v82, v82
v_sub_f32 v83, v75, v84
v_fmac_f32 v81, v83, v83
Mvaa 176,128,20
v_sub_f32 v82, v76, v84
v_fmac_f32 v81, v82, v82
v_sub_f32 v83, v77, v84
v_fmac_f32 v81, v83, v83
v_sub_f32 v82, v78, v84
Zvaa 180,132,20
Ta 0,14,16384
Ta 2,14,17408
v_fmac_f32 v81, v82, v82
v_sub_f32 v83, v79, v84
v_fmac_f32 v81, v83, v83
v_cndmask_b32_e64 v81, 0, v81, s[86:87]
Mvaa 180,36,16
Ta 4,14,24576
Ta 6,14,25600
A 80,80,81
s_nop 1
v_add_f32_dpp v80, v80, v80 quad_perm:[1,0,3,2] row_mask:0xf bank_mask:0xf
s_nop 1
v_add_f32_dpp v80, v80, v80 quad_perm:[2,3,0,1] row_mask:0xf bank_mask:0xf
s_nop 1
v_add_f32_dpp v80, v80, v80 row_half_mirror row_mask:0xf bank_mask:0xf
Zvaa 240,40,16
global_load_dword v142, v[140:141], off
v_lshl_add_u64 v[140:141], v[140:141], 0, s[68:69]
Mvaa 240,136,20
v_add_f32_dpp v80, v80, v80 row_mirror row_mask:0xf bank_mask:0xf
v_mov_b32 v81, v80
s_nop 1
v_permlane16_swap_b32 v80, v81
A 80,80,81
v_mul_f32 v80, 0x3acccccd, v80
K 80,80,80
Zvaa 244,140,20
v_lshlrev_b32 v80, 16, v80
v_add_f32 v80, 0x3727c5ac, v80
K 80,80,80
v_lshlrev_b32 v80, 16, v80
v_sqrt_f32 v81, v80
Mvaa 244,44,16
v_rcp_f32 v82, v81
v_fma_f32 v83, -v81, v81, v80
v_mul_f32 v82, 0.5, v82
v_fma_f32 v81, v83, v82, v81
K 86,81,81
Zaaa 208,48,16
v_lshlrev_b32 v86, 16, v86
v_rcp_f32 v87, v86
K 85,84,84
v_lshlrev_b32 v85, 16, v85
Maaa 208,144,20
s_waitcnt vmcnt(17)
v_sub_f32 v56, v56, v85
v_sub_f32 v57, v57, v85
K 80,56,57
Zaaa 212,148,20
v_lshlrev_b32 v56, 16, v80
v_and_b32 v57, 0xffff0000, v80
U 80,56,87
U 81,57,87
K 32,80,81
Maaa 212,52,16
v_lshlrev_b32 v80, 16, v32
v_and_b32 v81, 0xffff0000, v32
v_lshlrev_b32 v82, 16, v44
v_and_b32 v83, 0xffff0000, v44
Zaaa 80,56,16
U 84,80,82
v_fmac_f32 v84, v81, v83
v_sub_f32 v58, v58, v85
v_sub_f32 v59, v59, v85
Maaa 80,152,20
K 80,58,59
v_lshlrev_b32 v58, 16, v80
v_and_b32 v59, 0xffff0000, v80
U 80,58,87
U 81,59,87
Zaaa 84,156,20
)"
R"(K 33,80,81
v_lshlrev_b32 v80, 16, v33
v_and_b32 v81, 0xffff0000, v33
v_lshlrev_b32 v82, 16, v45
Maaa 84,60,16
W 4
v_and_b32 v83, 0xffff0000, v45
v_fmac_f32 v84, v80, v82
v_fmac_f32 v84, v81, v83
v_sub_f32 v60, v60, v85
v_sub_f32 v61, v61, v85
Zaaa 92,60,24
K 80,60,61
v_lshlrev_b32 v60, 16, v80
v_and_b32 v61, 0xffff0000, v80
U 80,60,87
Maaa 92,156,28
U 81,61,87
K 34,80,81
v_lshlrev_b32 v80, 16, v34
v_and_b32 v81, 0xffff0000, v34
v_lshlrev_b32 v82, 16, v46
Zaaa 88,152,28
Ta 8,15,16384
Ta 10,15,17408
v_and_b32 v83, 0xffff0000, v46
v_fmac_f32 v84, v80, v82
v_fmac_f32 v84, v81, v83
v_sub_f32 v62, v62, v85
Maaa 88,56,24
Ta 12,15,24576
v_sub_f32 v63, v63, v85
K 80,62,63
v_lshlrev_b32 v62, 16, v80
v_and_b32 v63, 0xffff0000, v80
U 80,62,87
Zaaa 220,52,24
Ta 14,15,25600
U 81,63,87
K 35,80,81
v_lshlrev_b32 v80, 16, v35
v_and_b32 v81, 0xffff0000, v35
Maaa 220,148,28
v_lshlrev_b32 v82, 16, v47
v_and_b32 v83, 0xffff0000, v47
v_fmac_f32 v84, v80, v82
v_fmac_f32 v84, v81, v83
Zaaa 216,144,28
v_sub_f32 v64, v64, v85
v_sub_f32 v65, v65, v85
K 80,64,65
v_lshlrev_b32 v64, 16, v80
v_and_b32 v65, 0xffff0000, v80
Maaa 216,48,24
U 80,64,87
U 81,65,87
K 36,80,81
v_lshlrev_b32 v80, 16, v36
Zvaa 252,44,24
v_and_b32 v81, 0xffff0000, v36
v_lshlrev_b32 v82, 16, v48
v_and_b32 v83, 0xffff0000, v48
v_fmac_f32 v84, v80, v82
v_fmac_f32 v84, v81, v83
Mvaa 252,140,28
v_sub_f32 v66, v66, v85
v_sub_f32 v67, v67, v85
K 80,66,67
v_lshlrev_b32 v66, 16, v80
Zvaa 248,136,28
v_and_b32 v67, 0xffff0000, v80
U 80,66,87
U 81,67,87
K 37,80,81
v_lshlrev_b32 v80, 16, v37
Mvaa 248,40,24
v_and_b32 v81, 0xffff0000, v37
v_lshlrev_b32 v82, 16, v49
v_and_b32 v83, 0xffff0000, v49
v_fmac_f32 v84, v80, v82
Zvaa 188,36,24
v_fmac_f32 v84, v81, v83
v_sub_f32 v68, v68, v85
v_sub_f32 v69, v69, v85
K 80,68,69
v_lshlrev_b32 v68, 16, v80
Mvaa 188,132,28
v_and_b32 v69, 0xffff0000, v80
U 80,68,87
U 81,69,87
K 38,80,81
Zvaa 184,128,28
v_lshlrev_b32 v80, 16, v38
v_and_b32 v81, 0xffff0000, v38
v_lshlrev_b32 v82, 16, v50
v_and_b32 v83, 0xffff0000, v50
Mvaa 184,32,24
W 4
v_fmac_f32 v84, v80, v82
v_fmac_f32 v84, v81, v83
v_sub_f32 v70, v70, v85
v_sub_f32 v71, v71, v85
K 80,70,71
Zvaa 192,32,0
v_lshlrev_b32 v70, 16, v80
v_and_b32 v71, 0xffff0000, v80
U 80,70,87
U 81,71,87
Mvaa 192,128,4
K 39,80,81
v_lshlrev_b32 v80, 16, v39
v_and_b32 v81, 0xffff0000, v39
v_lshlrev_b32 v82, 16, v51
v_and_b32 v83, 0xffff0000, v51
Zvaa 196,132,4
Ta 16,24,16384
Ta 18,24,17408
v_fmac_f32 v84, v80, v82
v_fmac_f32 v84, v81, v83
v_sub_f32 v72, v72, v85
v_sub_f32 v73, v73, v85
Mvaa 196,36,0
Ta 20,24,24576
K 80,72,73
v_lshlrev_b32 v72, 16, v80
v_and_b32 v73, 0xffff0000, v80
U 80,72,87
U 81,73,87
Zaaa 160,40,0
Ta 22,24,25600
K 40,80,81
v_lshlrev_b32 v80, 16, v40
v_and_b32 v81, 0xffff0000, v40
v_lshlrev_b32 v82, 16, v52
Maaa 160,136,4
v_and_b32 v83, 0xffff0000, v52
v_fmac_f32 v84, v80, v82
v_fmac_f32 v84, v81, v83
v_sub_f32 v74, v74, v85
Zaaa 164,140,4
v_sub_f32 v75, v75, v85
K 80,74,75
v_lshlrev_b32 v74, 16, v80
v_and_b32 v75, 0xffff0000, v80
U 80,74,87
Maaa 164,44,0
U 81,75,87
K 41,80,81
v_lshlrev_b32 v80, 16, v41
v_and_b32 v81, 0xffff0000, v41
Zaaa 224,48,0
v_lshlrev_b32 v82, 16, v53
v_and_b32 v83, 0xffff0000, v53
v_fmac_f32 v84, v80, v82
v_fmac_f32 v84, v81, v83
v_sub_f32 v76, v76, v85
Maaa 224,144,4
v_sub_f32 v77, v77, v85
K 80,76,77
v_lshlrev_b32 v76, 16, v80
v_and_b32 v77, 0xffff0000, v80
Zaaa 228,148,4
U 80,76,87
U 81,77,87
K 42,80,81
v_lshlrev_b32 v80, 16, v42
v_and_b32 v81, 0xffff0000, v42
Maaa 228,52,0
v_lshlrev_b32 v82, 16, v54
v_and_b32 v83, 0xffff0000, v54
v_fmac_f32 v84, v80, v82
v_fmac_f32 v84, v81, v83
Zaaa 96,56,0
v_sub_f32 v78, v78, v85
v_sub_f32 v79, v79, v85
K 80,78,79
v_lshlrev_b32 v78, 16, v80
v_and_b32 v79, 0xffff0000, v80
Maaa 96,152,4
U 80,78,87
U 81,79,87
K 43,80,81
v_lshlrev_b32 v80, 16, v43
Zaaa 100,156,4
v_and_b32 v81, 0xffff0000, v43
v_lshlrev_b32 v82, 16, v55
v_and_b32 v83, 0xffff0000, v55
v_fmac_f32 v84, v80, v82
Maaa 100,60,0
W 4
v_fmac_f32 v84, v81, v83
v_lshrrev_b32 v82, 9, v1
s_lshl_b32 s80, s14, 1
v_add_u32 v82, s80, v82
v_mul_u32_u24 v83, 1552, v82
Zaaa 108,60,8
Ta 24,25,16384
Ta 26,25,17408
v_add_u32 v83, v83, v88
v_add_u32 v83, 131072, v83
ds_write_b128 v83, v[32:35] offset:0
ds_write_b128 v83, v[36:39] offset:512
Maaa 108,156,12
ds_write_b128 v83, v[40:43] offset:1024
v_add_f32_dpp v84, v84, v84 quad_perm:[1,0,3,2] row_mask:0xf bank_mask:0xf
s_nop 1
v_add_f32_dpp v84, v84, v84 quad_perm:[2,3,0,1] row_mask:0xf bank_mask:0xf
s_nop 1
v_add_f32_dpp v84, v84, v84 row_half_mirror row_mask:0xf bank_mask:0xf
s_nop 1
v_add_f32_dpp v84, v84, v84 row_mirror row_mask:0xf bank_mask:0xf
Zaaa 104,152,12
Ta 28,25,24576
Ta 30,25,25600
v_mov_b32 v81, v84
Maaa 104,56,8
Zaaa 236,52,8
v_permlane16_swap_b32 v84, v81
A 84,84,81
K 80,84,84
v_lshlrev_b32 v80, 16, v80
s_lshl_b32 s80, s15, 3
v_add_u32 v82, s80, v82
v_sub_u32 v83, 2047, v82
Maaa 236,148,12
v_ashrrev_i32 v81, 31, v80
v_or_b32 v81, 0x80000000, v81
v_xor_b32 v80, v80, v81
v_and_b32 v80, 0xffff0000, v80
v_or_b32 v80, v80, v83
v_and_b32 v82, 7, v82
Zaaa 232,144,12
v_lshlrev_b32 v82, 2, v82
v_add_u32 v82, 143488, v82
ds_write_b32 v82, v80
Maaa 232,48,8
Zaaa 172,44,8
Maaa 172,140,12
Zaaa 168,136,12
s_waitcnt vmcnt(1) lgkmcnt(0)
s_barrier
s_add_u32 s64, s64, 128
Maaa 168,40,8
v_bfe_u32 v80, v1, 4, 3
Zvaa 204,36,8
v_mul_u32_u24 v81, 1552, v80
Mvaa 204,132,12
s_and_b32 s64, s64, 1023
v_lshrrev_b32 v82, 7, v1
Zvaa 200,128,12
v_lshl_add_u32 v81, v82, 4, v81
Mvaa 200,32,8
s_lshl_b32 s80, s14, 7
Zvaa 208,32,16
s_add_u32 m0, s61, 0
s_add_u32 s80, s80, 131072
Mvaa 208,128,20
v_add_u32 v81, s80, v81
Zvaa 212,132,20
ds_read_b128 v[32:35], v81
Ta 0,10,49152
Mvaa 212,36,16
Ta 2,10,50176
D 2,40,64,0
Zaaa 176,40,16
Rv 36,81,512
Ta 4,10,57344
Maaa 176,136,20
s_and_b32 s80, s14, 2
s_lshl_b32 s80, s80, 7
Ta 6,10,58368
Zaaa 180,140,20
v_subrev_u32 v81, s80, v81
Maaa 180,44,16
D 3,40,64,1024
Rv 40,81,1024
Zaaa 240,48,16
v_lshlrev_b32 v80, 2, v80
Maaa 240,144,20
v_add_u32 v80, 143488, v80
Zaaa 244,148,20
ds_read_b32 v84, v80
Maaa 244,52,16
D 4,40,64,2048
v_mov_b32 v85, s13
Zaaa 112,56,16
v_lshrrev_b32 v83, 1, v1
Maaa 112,152,20
s_mul_i32 s82, s15, 10240
Zaaa 116,156,20
s_add_u32 s82, s82, 131072
Maaa 116,60,16
s_lshl_b32 s80, s14, 10
D 5,40,64,3072
Zaaa 124,60,24
s_add_u32 m0, s61, 4096
s_add_u32 s83, s82, s80
Ra 60,26,47104
Maaa 124,156,28
Ra 156,27,47104
Zaaa 120,152,28
Ra 152,27,45056
Ta 8,11,49152
Maaa 120,56,24
D 6,40,64,0
Ta 10,11,50176
Zaaa 252,52,24
Ra 56,26,45056
Ta 12,11,57344
Maaa 252,148,28
Ta 14,11,58368
Ra 52,26,43008
Zaaa 248,144,28
Ra 148,27,43008
Ra 144,27,40960
Maaa 248,48,24
W 11
buffer_store_dwordx4 v[32:35], v1, s[8:11], s83 offen sc1
s_add_u32 s83, s83, 4096
Zaaa 188,44,24
Ra 48,26,40960
Ra 44,26,38912
Maaa 188,140,28
Ra 140,27,38912
Zaaa 184,136,28
Ra 136,27,36864
Maaa 184,40,24
D 7,40,64,1024
Ra 40,26,36864
Zvaa 220,36,24
Ra 36,26,34816
Mvaa 220,132,28
Ra 132,27,34816
Zvaa 216,128,28
Ra 128,27,32768
Mvaa 216,32,24
W 13
buffer_store_dwordx4 v[36:39], v1, s[8:11], s83 offen sc1
Ra 32,26,32768
Maaa 68,60,0
s_and_b32 s80, s80, 1024
s_add_u32 s83, s82, s80
s_add_u32 s83, s83, 8192
Maaa 68,156,4
Maaa 64,152,4
Ta 16,12,49152
Ta 18,12,50176
Maaa 64,56,0
W 10
D 8,40,64,2048
Ta 20,12,57344
Maaa 196,52,0
Ta 22,12,58368
Maaa 196,148,4
Maaa 192,144,4
Maaa 192,48,0
W 8
buffer_store_dwordx4 v[40:43], v1, s[8:11], s83 offen sc1
Mvaa 228,44,0
Mvaa 228,140,4
Mvaa 224,136,4
Mvaa 224,40,0
W 4
D 9,40,64,3072
Mvaa 164,36,0
s_lshl_b32 s65, s64, 12
s_add_u32 m0, s62, 0
Mvaa 164,132,4
Mvaa 160,128,4
Mvaa 160,32,0
D 16,44,65,0
Mvaa 168,32,8
Mvaa 168,128,12
Mvaa 172,132,12
Ta 24,13,49152
Ta 26,13,50176
Mvaa 172,36,8
D 17,44,65,1024
Ta 28,13,57344
Mvaa 232,40,8
Ta 30,13,58368
Mvaa 232,136,12
Mvaa 236,140,12
Mvaa 236,44,8
D 18,44,65,2048
Maaa 200,48,8
Maaa 200,144,12
Maaa 204,148,12
Maaa 204,52,8
D 19,44,65,3072
Maaa 72,56,8
s_add_u32 m0, s62, 4096
Maaa 72,152,12
Maaa 76,156,12
Maaa 76,60,8
W 4
D 20,44,65,0
Maaa 84,60,16
Maaa 84,156,20
Maaa 80,152,20
)"
R"(Ta 0,14,49152
Ta 2,14,50176
Maaa 80,56,16
D 21,44,65,1024
Ta 4,14,57344
Maaa 212,52,16
Ta 6,14,58368
Maaa 212,148,20
Maaa 208,144,20
Maaa 208,48,16
D 22,44,65,2048
Mvaa 244,44,16
Mvaa 244,140,20
Mvaa 240,136,20
Mvaa 240,40,16
D 23,44,65,3072
Mvaa 180,36,16
Mvaa 180,132,20
Mvaa 176,128,20
Mvaa 176,32,16
W 4
Mvaa 184,32,24
Mvaa 184,128,28
Mvaa 188,132,28
Ta 8,15,49152
Ta 10,15,50176
Mvaa 188,36,24
Ta 12,15,57344
Ta 14,15,58368
Mvaa 248,40,24
Mvaa 248,136,28
Mvaa 252,140,28
Mvaa 252,44,24
Maaa 216,48,24
Maaa 216,144,28
Maaa 220,148,28
Maaa 220,52,24
Maaa 88,56,24
Maaa 88,152,28
Maaa 92,156,28
Maaa 92,60,24
W 4
Maaa 100,60,0
Maaa 100,156,4
Maaa 96,152,4
Ta 16,24,49152
Ta 18,24,50176
Maaa 96,56,0
Ta 20,24,57344
Ta 22,24,58368
Maaa 228,52,0
Maaa 228,148,4
Maaa 224,144,4
Maaa 224,48,0
Maaa 164,44,0
Maaa 164,140,4
Maaa 160,136,4
Maaa 160,40,0
Mvaa 196,36,0
Mvaa 196,132,4
Mvaa 192,128,4
Mvaa 192,32,0
W 4
Mvaa 200,32,8
Ta 24,25,49152
Ta 26,25,50176
Mvaa 200,128,12
Mvaa 204,132,12
Ta 28,25,57344
Ta 30,25,58368
Mvaa 204,36,8
Maaa 168,40,8
Maaa 168,136,12
Maaa 172,140,12
Maaa 172,44,8
Maaa 232,48,8
Maaa 232,144,12
Maaa 236,148,12
s_waitcnt vmcnt(0) lgkmcnt(0)
s_barrier
s_cmp_eq_u32 s14, 0
s_cbranch_scc0 .Lpubr_17%=
s_lshl_b32 s80, s15, 6
s_add_u32 s80, s80, 40960
s_mov_b64 exec, 0xff
buffer_store_dwordx2 v[84:85], v83, s[8:11], s80 offen sc1
s_mov_b64 exec, -1
.Lpubr_17%=:
Maaa 236,52,8
s_add_u32 s64, s64, 128
Maaa 104,56,8
Maaa 104,152,12
s_and_b32 s64, s64, 1023
Maaa 108,156,12
Maaa 108,60,8
Maaa 116,60,16
s_add_u32 m0, s61, 32768
Maaa 116,156,20
Maaa 112,152,20
D 2,40,64,0
Ta 0,10,16384
Maaa 112,56,16
Ta 2,10,17408
Ta 4,10,24576
Maaa 244,52,16
Ta 6,10,25600
Maaa 244,148,20
Maaa 240,144,20
D 3,40,64,1024
Maaa 240,48,16
Maaa 180,44,16
Maaa 180,140,20
Maaa 176,136,20
D 4,40,64,2048
Maaa 176,40,16
Mvaa 212,36,16
Mvaa 212,132,20
Mvaa 208,128,20
D 5,40,64,3072
Mvaa 208,32,16
s_add_u32 m0, s61, 36864
Mvaa 216,32,24
Ra 32,26,0
Mvaa 216,128,28
Ra 128,27,0
Mvaa 220,132,28
D 6,40,64,0
Ra 132,27,2048
Mvaa 220,36,24
Ta 8,11,16384
Ta 10,11,17408
Maaa 184,40,24
Ra 36,26,2048
Ta 12,11,24576
Maaa 184,136,28
Ta 14,11,25600
Ra 40,26,4096
Maaa 188,140,28
D 7,40,64,1024
Ra 136,27,4096
Maaa 188,44,24
Ra 140,27,6144
Ra 44,26,6144
Maaa 248,48,24
Ra 48,26,8192
Maaa 248,144,28
Ra 144,27,8192
Maaa 252,148,28
D 8,40,64,2048
Ra 148,27,10240
Maaa 252,52,24
Ra 52,26,10240
Maaa 120,56,24
Ra 56,26,12288
Maaa 120,152,28
Ra 152,27,12288
Maaa 124,156,28
D 9,40,64,3072
Ra 156,27,14336
Maaa 124,60,24
W 13
s_lshl_b32 s65, s64, 12
s_add_u32 m0, s62, 32768
Mvaa 160,32,0
Ra 60,26,14336
Mvaa 160,128,4
Mvaa 164,132,4
D 16,44,65,0
Ta 16,12,16384
Mvaa 164,36,0
W 9
Ta 18,12,17408
Ta 20,12,24576
Mvaa 224,40,0
Ta 22,12,25600
Mvaa 224,136,4
Mvaa 228,140,4
D 17,44,65,1024
Mvaa 228,44,0
W 8
Maaa 192,48,0
Maaa 192,144,4
Maaa 196,148,4
D 18,44,65,2048
Maaa 196,52,0
W 4
Maaa 64,56,0
Maaa 64,152,4
Maaa 68,156,4
D 19,44,65,3072
Maaa 68,60,0
s_add_u32 m0, s62, 36864
Maaa 76,60,8
Maaa 76,156,12
Maaa 72,152,12
D 20,44,65,0
Ta 24,13,16384
Maaa 72,56,8
Ta 26,13,17408
Ta 28,13,24576
Maaa 204,52,8
Ta 30,13,25600
Maaa 204,148,12
Maaa 200,144,12
D 21,44,65,1024
Maaa 200,48,8
Mvaa 236,44,8
Mvaa 236,140,12
Mvaa 232,136,12
D 22,44,65,2048
Mvaa 232,40,8
Mvaa 172,36,8
Mvaa 172,132,12
Mvaa 168,128,12
D 23,44,65,3072
Mvaa 168,32,8
W 4
Mvaa 176,32,16
Mvaa 176,128,20
Mvaa 180,132,20
global_load_dword v142, v[140:141], off
v_lshl_add_u64 v[140:141], v[140:141], 0, s[68:69]
Ta 0,14,16384
Mvaa 180,36,16
Ta 2,14,17408
Ta 4,14,24576
Mvaa 240,40,16
Ta 6,14,25600
Mvaa 240,136,20
Mvaa 244,140,20
Mvaa 244,44,16
Maaa 208,48,16
Maaa 208,144,20
Maaa 212,148,20
Maaa 212,52,16
Maaa 80,56,16
Maaa 80,152,20
Maaa 84,156,20
Maaa 84,60,16
W 4
Maaa 92,60,24
Maaa 92,156,28
Maaa 88,152,28
Ta 8,15,16384
Ta 10,15,17408
Maaa 88,56,24
Ta 12,15,24576
Ta 14,15,25600
Maaa 220,52,24
Maaa 220,148,28
Maaa 216,144,28
Maaa 216,48,24
Mvaa 252,44,24
Mvaa 252,140,28
Mvaa 248,136,28
Mvaa 248,40,24
Mvaa 188,36,24
Mvaa 188,132,28
Mvaa 184,128,28
Mvaa 184,32,24
W 4
Mvaa 192,32,0
Mvaa 192,128,4
Mvaa 196,132,4
Ta 16,24,16384
Ta 18,24,17408
Mvaa 196,36,0
Ta 20,24,24576
Ta 22,24,25600
Maaa 160,40,0
Maaa 160,136,4
Maaa 164,140,4
Maaa 164,44,0
Maaa 224,48,0
Maaa 224,144,4
Maaa 228,148,4
Maaa 228,52,0
Maaa 96,56,0
Maaa 96,152,4
Maaa 100,156,4
Maaa 100,60,0
W 4
Maaa 108,60,8
Ta 24,25,16384
Ta 26,25,17408
Maaa 108,156,12
Maaa 104,152,12
Ta 28,25,24576
Ta 30,25,25600
Maaa 104,56,8
Maaa 236,52,8
Maaa 236,148,12
Maaa 232,144,12
Maaa 232,48,8
Maaa 172,44,8
Maaa 172,140,12
Maaa 168,136,12
s_waitcnt vmcnt(1) lgkmcnt(0)
s_barrier
s_add_u32 s64, s64, 128
Maaa 168,40,8
Mvaa 204,36,8
Mvaa 204,132,12
s_and_b32 s64, s64, 1023
Mvaa 200,128,12
Mvaa 200,32,8
Mvaa 208,32,16
s_add_u32 m0, s61, 0
Mvaa 208,128,20
Mvaa 212,132,20
D 2,40,64,0
Ta 0,10,49152
Mvaa 212,36,16
Ta 2,10,50176
Ta 4,10,57344
Maaa 176,40,16
Ta 6,10,58368
Maaa 176,136,20
Maaa 180,140,20
D 3,40,64,1024
Maaa 180,44,16
Maaa 240,48,16
Maaa 240,144,20
Maaa 244,148,20
D 4,40,64,2048
Maaa 244,52,16
Maaa 112,56,16
Maaa 112,152,20
Maaa 116,156,20
D 5,40,64,3072
Maaa 116,60,16
s_add_u32 m0, s61, 4096
Maaa 124,60,24
Ra 60,26,47104
v_lshrrev_b32 v28, 1, v1
Maaa 124,156,28
s_lshl_b32 s80, s60, 9
Ra 156,27,47104
Maaa 120,152,28
s_add_u32 s80, s80, 40960
D 6,40,64,0
Ra 152,27,45056
Maaa 120,56,24
Ta 8,11,49152
Ta 10,11,50176
Maaa 252,52,24
Ra 56,26,45056
Ta 12,11,57344
Maaa 252,148,28
Ta 14,11,58368
Ra 52,26,43008
Maaa 248,144,28
D 7,40,64,1024
Ra 148,27,43008
Maaa 248,48,24
Ra 144,27,40960
Ra 48,26,40960
Maaa 188,44,24
Ra 44,26,38912
Maaa 188,140,28
Ra 140,27,38912
Maaa 184,136,28
buffer_load_dwordx2 v[28:29], v28, s[8:11], s80 offen sc1
Ra 136,27,36864
Maaa 184,40,24
Ra 40,26,36864
Mvaa 220,36,24
Ra 36,26,34816
Mvaa 220,132,28
Ra 132,27,34816
Mvaa 216,128,28
D 8,40,64,2048
Ra 128,27,32768
Mvaa 216,32,24
W 13
Ra 32,26,32768
Maaa 68,60,0
Maaa 68,156,4
Maaa 64,152,4
D 9,40,64,3072
Ta 16,12,49152
Maaa 64,56,0
W 9
s_lshl_b32 s65, s64, 12
s_add_u32 m0, s62, 0
Maaa 196,52,0
Ta 18,12,50176
Ta 20,12,57344
Maaa 196,148,4
Ta 22,12,58368
Maaa 192,144,4
D 16,44,65,0
Maaa 192,48,0
W 8
Mvaa 228,44,0
Mvaa 228,140,4
Mvaa 224,136,4
D 17,44,65,1024
Mvaa 224,40,0
W 4
)"
R"(Mvaa 164,36,0
Mvaa 164,132,4
Mvaa 160,128,4
D 18,44,65,2048
Mvaa 160,32,0
Mvaa 168,32,8
Mvaa 168,128,12
Mvaa 172,132,12
D 19,44,65,3072
Ta 24,13,49152
Mvaa 172,36,8
s_add_u32 m0, s62, 4096
Ta 26,13,50176
Ta 28,13,57344
Mvaa 232,40,8
Ta 30,13,58368
Mvaa 232,136,12
Mvaa 236,140,12
D 20,44,65,0
Mvaa 236,44,8
Maaa 200,48,8
Maaa 200,144,12
Maaa 204,148,12
D 21,44,65,1024
Maaa 204,52,8
Maaa 72,56,8
Maaa 72,152,12
Maaa 76,156,12
D 22,44,65,2048
Maaa 76,60,8
W 4
Maaa 84,60,16
Maaa 84,156,20
Maaa 80,152,20
D 23,44,65,3072
Ta 0,14,49152
Maaa 80,56,16
Ta 2,14,50176
Ta 4,14,57344
Maaa 212,52,16
Ta 6,14,58368
Maaa 212,148,20
Maaa 208,144,20
s_waitcnt vmcnt(10)
v_cmp_eq_u32 vcc, s13, v29
s_cmp_eq_u64 vcc, -1
s_cbranch_scc0 .Lspin_18%=
.Lok_19%=:
Maaa 208,48,16
global_load_dword v142, v[140:141], off
v_lshl_add_u64 v[140:141], v[140:141], 0, s[68:69]
Mvaa 244,44,16
v_bfe_u32 v144, v1, 8, 1
Mvaa 244,140,20
v_mul_u32_u24 v144, 10240, v144
Mvaa 240,136,20
v_and_b32 v145, 0xf0, v1
Mvaa 240,40,16
v_lshl_add_u32 v144, v145, 3, v144
Mvaa 180,36,16
s_mul_i32 s91, s58, 20480
Mvaa 180,132,20
s_add_u32 s91, s91, s28
Mvaa 176,128,20
buffer_load_dword v142, v144, s[8:11], s91 offen offset:0 sc1
Mvaa 176,32,16
W 4
Mvaa 184,32,24
Mvaa 184,128,28
Mvaa 188,132,28
Ta 8,15,49152
Ta 10,15,50176
Mvaa 188,36,24
Ta 12,15,57344
Ta 14,15,58368
Mvaa 248,40,24
Mvaa 248,136,28
Mvaa 252,140,28
Mvaa 252,44,24
Maaa 216,48,24
Maaa 216,144,28
Maaa 220,148,28
Maaa 220,52,24
Maaa 88,56,24
Maaa 88,152,28
Maaa 92,156,28
Maaa 92,60,24
W 4
Maaa 100,60,0
Maaa 100,156,4
Maaa 96,152,4
Ta 16,24,49152
Ta 18,24,50176
Maaa 96,56,0
Ta 20,24,57344
Ta 22,24,58368
Maaa 228,52,0
Maaa 228,148,4
Maaa 224,144,4
Maaa 224,48,0
Maaa 164,44,0
Maaa 164,140,4
Maaa 160,136,4
Maaa 160,40,0
Mvaa 196,36,0
Mvaa 196,132,4
Mvaa 192,128,4
Mvaa 192,32,0
W 4
Mvaa 200,32,8
Ta 24,25,49152
Ta 26,25,50176
Mvaa 200,128,12
Mvaa 204,132,12
Ta 28,25,57344
Ta 30,25,58368
Mvaa 204,36,8
Maaa 168,40,8
Maaa 168,136,12
Maaa 172,140,12
Maaa 172,44,8
Maaa 232,48,8
Maaa 232,144,12
Maaa 236,148,12
s_waitcnt vmcnt(2) lgkmcnt(0)
s_barrier
s_add_u32 s64, s64, 128
Maaa 236,52,8
Maaa 104,56,8
Maaa 104,152,12
s_and_b32 s64, s64, 1023
Maaa 108,156,12
Maaa 108,60,8
s_add_u32 m0, s61, 32768
Maaa 116,60,16
Maaa 116,156,20
Maaa 112,152,20
D 2,40,64,0
Ta 0,10,16384
Maaa 112,56,16
Ta 2,10,17408
Ta 4,10,24576
Maaa 244,52,16
Ta 6,10,25600
Maaa 244,148,20
Maaa 240,144,20
D 3,40,64,1024
Maaa 240,48,16
Maaa 180,44,16
Maaa 180,140,20
Maaa 176,136,20
D 4,40,64,2048
Maaa 176,40,16
Mvaa 212,36,16
Mvaa 212,132,20
Mvaa 208,128,20
D 5,40,64,3072
Mvaa 208,32,16
s_add_u32 m0, s61, 36864
Mvaa 216,32,24
Ra 32,26,0
Mvaa 216,128,28
Ra 128,27,0
Mvaa 220,132,28
D 6,40,64,0
Ra 132,27,2048
Mvaa 220,36,24
Ta 8,11,16384
Ta 10,11,17408
Maaa 184,40,24
Ra 36,26,2048
Ta 12,11,24576
Maaa 184,136,28
Ta 14,11,25600
Ra 40,26,4096
Maaa 188,140,28
D 7,40,64,1024
Ra 136,27,4096
Maaa 188,44,24
Ra 140,27,6144
Ra 44,26,6144
Maaa 248,48,24
Ra 48,26,8192
Maaa 248,144,28
Ra 144,27,8192
Maaa 252,148,28
D 8,40,64,2048
Ra 148,27,10240
Maaa 252,52,24
Ra 52,26,10240
Maaa 120,56,24
Ra 56,26,12288
Maaa 120,152,28
Ra 152,27,12288
Maaa 124,156,28
D 9,40,64,3072
Ra 156,27,14336
Maaa 124,60,24
W 13
s_lshl_b32 s65, s64, 12
s_add_u32 m0, s62, 32768
Mvaa 160,32,0
Ra 60,26,14336
Mvaa 160,128,4
Mvaa 164,132,4
D 16,44,65,0
Ta 16,12,16384
Mvaa 164,36,0
W 9
Ta 18,12,17408
Ta 20,12,24576
Mvaa 224,40,0
Ta 22,12,25600
Mvaa 224,136,4
Mvaa 228,140,4
D 17,44,65,1024
Mvaa 228,44,0
W 8
Maaa 192,48,0
Maaa 192,144,4
Maaa 196,148,4
D 18,44,65,2048
Maaa 196,52,0
W 4
Maaa 64,56,0
Maaa 64,152,4
Maaa 68,156,4
D 19,44,65,3072
Maaa 68,60,0
s_add_u32 m0, s62, 36864
Maaa 76,60,8
Maaa 76,156,12
Maaa 72,152,12
D 20,44,65,0
Ta 24,13,16384
Maaa 72,56,8
Ta 26,13,17408
Ta 28,13,24576
Maaa 204,52,8
Ta 30,13,25600
Maaa 204,148,12
Maaa 200,144,12
D 21,44,65,1024
Maaa 200,48,8
Mvaa 236,44,8
Mvaa 236,140,12
Mvaa 232,136,12
D 22,44,65,2048
Mvaa 232,40,8
Mvaa 172,36,8
Mvaa 172,132,12
Mvaa 168,128,12
D 23,44,65,3072
Mvaa 168,32,8
W 4
s_add_u32 s65, s5, 0
s_cmp_ge_u32 s65, 20
s_cselect_b32 s90, 20, 0
Mvaa 176,32,16
s_sub_u32 s65, s65, s90
s_lshl_b32 s65, s65, 18
s_add_u32 m0, s63, 131072
Mvaa 176,128,20
Mvaa 180,132,20
D 16,48,65,0
Ta 0,14,16384
Mvaa 180,36,16
Ta 2,14,17408
Ta 4,14,24576
Mvaa 240,40,16
Ta 6,14,25600
Mvaa 240,136,20
Mvaa 244,140,20
D 17,48,65,1024
Mvaa 244,44,16
Maaa 208,48,16
Maaa 208,144,20
Maaa 212,148,20
D 18,48,65,2048
Maaa 212,52,16
Maaa 80,56,16
Maaa 80,152,20
Maaa 84,156,20
D 19,48,65,3072
Maaa 84,60,16
W 4
s_add_u32 m0, s63, 135168
Maaa 92,60,24
Maaa 92,156,28
Maaa 88,152,28
D 20,48,65,0
Ta 8,15,16384
Maaa 88,56,24
Ta 10,15,17408
Ta 12,15,24576
Maaa 220,52,24
Ta 14,15,25600
Maaa 220,148,28
Maaa 216,144,28
buffer_load_dword v142, v144, s[8:11], s91 offen offset:2048 sc1
Maaa 216,48,24
s_add_u32 s91, s91, 4096
Mvaa 252,44,24
Mvaa 252,140,28
Mvaa 248,136,28
D 21,48,65,1024
Mvaa 248,40,24
Mvaa 188,36,24
Mvaa 188,132,28
Mvaa 184,128,28
D 22,48,65,2048
Mvaa 184,32,24
W 4
Mvaa 192,32,0
Mvaa 192,128,4
Mvaa 196,132,4
D 23,48,65,3072
v_lshrrev_b32 v138, 4, v1
Mvaa 196,36,0
s_sub_u32 s81, s73, 1
v_min_u32 v138, s81, v138
v_lshrrev_b32 v139, 2, v138
Maaa 160,40,0
v_mul_u32_u24 v139, s72, v139
v_and_b32 v138, 3, v138
Maaa 160,136,4
v_lshl_add_u32 v138, v138, 7, v139
v_add_u32 v138, s74, v138
Maaa 164,140,4
s_lshr_b32 s73, s73, 2
s_mul_i32 s73, s73, s72
v_min_u32 v139, s75, v138
Maaa 164,44,0
global_load_dword v142, v139, s[76:77]
v_add_u32 v138, s73, v138
Maaa 224,48,0
Ta 16,24,16384
Ta 18,24,17408
Maaa 224,144,4
Ta 20,24,24576
Ta 22,24,25600
Maaa 228,148,4
Maaa 228,52,0
Maaa 96,56,0
Maaa 96,152,4
Maaa 100,156,4
Maaa 100,60,0
W 4
Maaa 108,60,8
Ta 24,25,16384
Ta 26,25,17408
Maaa 108,156,12
Maaa 104,152,12
Ta 28,25,24576
Ta 30,25,25600
Maaa 104,56,8
Maaa 236,52,8
Maaa 236,148,12
Maaa 232,144,12
Maaa 232,48,8
Maaa 172,44,8
Maaa 172,140,12
Maaa 168,136,12
s_waitcnt vmcnt(1) lgkmcnt(0)
s_barrier
s_add_u32 s64, s64, 128
Maaa 168,40,8
Mvaa 204,36,8
Mvaa 204,132,12
s_and_b32 s64, s64, 1023
Mvaa 200,128,12
Mvaa 200,32,8
Mvaa 208,32,16
s_add_u32 m0, s61, 0
Mvaa 208,128,20
Mvaa 212,132,20
Ta 0,10,49152
Ta 2,10,50176
Mvaa 212,36,16
D 2,40,64,0
Ta 4,10,57344
Maaa 176,40,16
Ta 6,10,58368
Maaa 176,136,20
Maaa 180,140,20
Maaa 180,44,16
D 3,40,64,1024
Maaa 240,48,16
Maaa 240,144,20
Maaa 244,148,20
Maaa 244,52,16
D 4,40,64,2048
Maaa 112,56,16
Maaa 112,152,20
Maaa 116,156,20
Maaa 116,60,16
D 5,40,64,3072
Maaa 124,60,24
s_add_u32 m0, s61, 4096
Ra 60,26,47104
)"
R"(Maaa 124,156,28
Ra 156,27,47104
Maaa 120,152,28
Ra 152,27,45056
Ta 8,11,49152
Maaa 120,56,24
D 6,40,64,0
Ta 10,11,50176
Maaa 252,52,24
Ra 56,26,45056
Ta 12,11,57344
Maaa 252,148,28
Ta 14,11,58368
Ra 52,26,43008
Maaa 248,144,28
Ra 148,27,43008
Ra 144,27,40960
Maaa 248,48,24
D 7,40,64,1024
Ra 48,26,40960
Maaa 188,44,24
Ra 44,26,38912
Maaa 188,140,28
Ra 140,27,38912
Maaa 184,136,28
Ra 136,27,36864
Maaa 184,40,24
D 8,40,64,2048
Ra 40,26,36864
Mvaa 220,36,24
Ra 36,26,34816
Mvaa 220,132,28
Ra 132,27,34816
Mvaa 216,128,28
Ra 128,27,32768
Mvaa 216,32,24
W 13
D 9,40,64,3072
Ra 32,26,32768
Maaa 68,60,0
s_lshl_b32 s65, s64, 12
s_add_u32 m0, s62, 0
Maaa 68,156,4
Maaa 64,152,4
Ta 16,12,49152
Ta 18,12,50176
Maaa 64,56,0
W 10
D 16,44,65,0
Ta 20,12,57344
Maaa 196,52,0
Ta 22,12,58368
Maaa 196,148,4
Maaa 192,144,4
Maaa 192,48,0
W 8
D 17,44,65,1024
Mvaa 228,44,0
Mvaa 228,140,4
Mvaa 224,136,4
Mvaa 224,40,0
W 4
D 18,44,65,2048
Mvaa 164,36,0
Mvaa 164,132,4
Mvaa 160,128,4
Mvaa 160,32,0
D 19,44,65,3072
Mvaa 168,32,8
s_add_u32 m0, s62, 4096
Mvaa 168,128,12
Mvaa 172,132,12
Ta 24,13,49152
Ta 26,13,50176
Mvaa 172,36,8
D 20,44,65,0
Ta 28,13,57344
Mvaa 232,40,8
Ta 30,13,58368
Mvaa 232,136,12
Mvaa 236,140,12
Mvaa 236,44,8
D 21,44,65,1024
Maaa 200,48,8
Maaa 200,144,12
Maaa 204,148,12
Maaa 204,52,8
D 22,44,65,2048
Maaa 72,56,8
Maaa 72,152,12
Maaa 76,156,12
Maaa 76,60,8
W 4
D 23,44,65,3072
Maaa 84,60,16
v_min_u32 v139, s75, v138
Maaa 84,156,20
Maaa 80,152,20
Ta 0,14,49152
Ta 2,14,50176
Maaa 80,56,16
buffer_load_dword v142, v144, s[8:11], s91 offen offset:0 sc1
Ta 4,14,57344
Maaa 212,52,16
Ta 6,14,58368
Maaa 212,148,20
Maaa 208,144,20
Maaa 208,48,16
global_load_dword v142, v139, s[76:77]
Mvaa 244,44,16
Mvaa 244,140,20
Mvaa 240,136,20
v_add_u32 v138, s73, v138
Mvaa 240,40,16
global_load_dword v142, v146, s[6:7]
Mvaa 180,36,16
Mvaa 180,132,20
Mvaa 176,128,20
Mvaa 176,32,16
W 4
Mvaa 184,32,24
Mvaa 184,128,28
Mvaa 188,132,28
Ta 8,15,49152
Ta 10,15,50176
Mvaa 188,36,24
Ta 12,15,57344
Ta 14,15,58368
Mvaa 248,40,24
Mvaa 248,136,28
Mvaa 252,140,28
Mvaa 252,44,24
Maaa 216,48,24
Maaa 216,144,28
Maaa 220,148,28
Maaa 220,52,24
Maaa 88,56,24
Maaa 88,152,28
Maaa 92,156,28
Maaa 92,60,24
W 4
Maaa 100,60,0
Maaa 100,156,4
Maaa 96,152,4
Ta 16,24,49152
Ta 18,24,50176
Maaa 96,56,0
Ta 20,24,57344
Ta 22,24,58368
Maaa 228,52,0
Maaa 228,148,4
Maaa 224,144,4
Maaa 224,48,0
Maaa 164,44,0
Maaa 164,140,4
Maaa 160,136,4
Maaa 160,40,0
Mvaa 196,36,0
Mvaa 196,132,4
Mvaa 192,128,4
Mvaa 192,32,0
W 4
Mvaa 200,32,8
Ta 24,25,49152
Ta 26,25,50176
Mvaa 200,128,12
Mvaa 204,132,12
Ta 28,25,57344
Ta 30,25,58368
Mvaa 204,36,8
Maaa 168,40,8
Maaa 168,136,12
Maaa 172,140,12
Maaa 172,44,8
Maaa 232,48,8
Maaa 232,144,12
Maaa 236,148,12
s_waitcnt vmcnt(3) lgkmcnt(0)
s_barrier
s_cmp_eq_u32 s85, 107
s_cbranch_scc1 .Lrt_21%=
.Lrtr_22%=:
Maaa 236,52,8
s_add_u32 s64, s64, 128
Maaa 104,56,8
Maaa 104,152,12
s_and_b32 s64, s64, 1023
Maaa 108,156,12
Maaa 108,60,8
Maaa 116,60,16
s_add_u32 m0, s61, 32768
Maaa 116,156,20
Maaa 112,152,20
Ta 0,10,16384
Ta 2,10,17408
Maaa 112,56,16
D 2,40,64,0
Ta 4,10,24576
Maaa 244,52,16
Ta 6,10,25600
Maaa 244,148,20
Maaa 240,144,20
Maaa 240,48,16
D 3,40,64,1024
Maaa 180,44,16
Maaa 180,140,20
Maaa 176,136,20
Maaa 176,40,16
D 4,40,64,2048
Mvaa 212,36,16
Mvaa 212,132,20
Mvaa 208,128,20
Mvaa 208,32,16
D 5,40,64,3072
Mvaa 216,32,24
s_add_u32 m0, s61, 36864
Ra 32,26,0
Mvaa 216,128,28
Ra 128,27,0
Mvaa 220,132,28
Ra 132,27,2048
Ta 8,11,16384
Mvaa 220,36,24
D 6,40,64,0
Ta 10,11,17408
Maaa 184,40,24
Ra 36,26,2048
Ta 12,11,24576
Maaa 184,136,28
Ta 14,11,25600
Ra 40,26,4096
Maaa 188,140,28
Ra 136,27,4096
Ra 140,27,6144
Maaa 188,44,24
D 7,40,64,1024
Ra 44,26,6144
Maaa 248,48,24
Ra 48,26,8192
Maaa 248,144,28
Ra 144,27,8192
Maaa 252,148,28
Ra 148,27,10240
Maaa 252,52,24
D 8,40,64,2048
Ra 52,26,10240
Maaa 120,56,24
Ra 56,26,12288
Maaa 120,152,28
Ra 152,27,12288
Maaa 124,156,28
Ra 156,27,14336
Maaa 124,60,24
W 13
D 9,40,64,3072
Ra 60,26,14336
Mvaa 160,32,0
s_lshl_b32 s65, s64, 12
s_add_u32 m0, s62, 32768
Mvaa 160,128,4
Mvaa 164,132,4
Ta 16,12,16384
Ta 18,12,17408
Mvaa 164,36,0
W 10
D 16,44,65,0
Ta 20,12,24576
Mvaa 224,40,0
Ta 22,12,25600
Mvaa 224,136,4
Mvaa 228,140,4
Mvaa 228,44,0
W 8
D 17,44,65,1024
Maaa 192,48,0
Maaa 192,144,4
Maaa 196,148,4
Maaa 196,52,0
W 4
D 18,44,65,2048
Maaa 64,56,0
Maaa 64,152,4
Maaa 68,156,4
Maaa 68,60,0
D 19,44,65,3072
Maaa 76,60,8
s_add_u32 m0, s62, 36864
Maaa 76,156,12
Maaa 72,152,12
Ta 24,13,16384
Ta 26,13,17408
Maaa 72,56,8
D 20,44,65,0
Ta 28,13,24576
Maaa 204,52,8
Ta 30,13,25600
Maaa 204,148,12
Maaa 200,144,12
Maaa 200,48,8
D 21,44,65,1024
Mvaa 236,44,8
Mvaa 236,140,12
Mvaa 232,136,12
Mvaa 232,40,8
D 22,44,65,2048
Mvaa 172,36,8
Mvaa 172,132,12
Mvaa 168,128,12
Mvaa 168,32,8
W 4
D 23,44,65,3072
Mvaa 176,32,16
v_min_u32 v139, s75, v138
Mvaa 176,128,20
Mvaa 180,132,20
Ta 0,14,16384
Ta 2,14,17408
Mvaa 180,36,16
buffer_load_dword v142, v144, s[8:11], s91 offen offset:2048 sc1
Ta 4,14,24576
Mvaa 240,40,16
Ta 6,14,25600
Mvaa 240,136,20
s_add_u32 s91, s91, 4096
Mvaa 244,140,20
Mvaa 244,44,16
global_load_dword v142, v139, s[76:77]
Maaa 208,48,16
Maaa 208,144,20
Maaa 212,148,20
v_add_u32 v138, s73, v138
v_add_u32 v2, 131072, v10
Maaa 212,52,16
v_add_u32 v3, 131072, v11
v_add_u32 v4, 131072, v12
Maaa 80,56,16
v_add_u32 v5, 131072, v13
v_add_u32 v6, 131072, v14
Maaa 80,152,20
v_add_u32 v7, 131072, v15
v_add_u32 v8, 131072, v24
Maaa 84,156,20
v_add_u32 v9, 131072, v25
Maaa 84,60,16
W 4
Maaa 92,60,24
Maaa 92,156,28
Maaa 88,152,28
Ta 8,15,16384
Ta 10,15,17408
Maaa 88,56,24
Ta 12,15,24576
Ta 14,15,25600
Maaa 220,52,24
Maaa 220,148,28
Maaa 216,144,28
Maaa 216,48,24
Mvaa 252,44,24
Mvaa 252,140,28
Mvaa 248,136,28
Mvaa 248,40,24
Mvaa 188,36,24
Mvaa 188,132,28
Mvaa 184,128,28
Mvaa 184,32,24
W 4
Mvaa 192,32,0
Mvaa 192,128,4
Mvaa 196,132,4
Ta 16,24,16384
Ta 18,24,17408
Mvaa 196,36,0
Ta 20,24,24576
Ta 22,24,25600
Maaa 160,40,0
Maaa 160,136,4
Maaa 164,140,4
Maaa 164,44,0
Maaa 224,48,0
Maaa 224,144,4
Maaa 228,148,4
Maaa 228,52,0
Maaa 96,56,0
Maaa 96,152,4
Maaa 100,156,4
Maaa 100,60,0
W 4
Maaa 108,60,8
Ta 24,25,16384
Ta 26,25,17408
Maaa 108,156,12
Maaa 104,152,12
Ta 28,25,24576
Ta 30,25,25600
Maaa 104,56,8
Maaa 236,52,8
Maaa 236,148,12
Maaa 232,144,12
Maaa 232,48,8
Maaa 172,44,8
)"
R"(Maaa 172,140,12
Maaa 168,136,12
s_waitcnt vmcnt(2) lgkmcnt(0)
s_barrier
s_cmp_eq_u32 s85, 107
s_cbranch_scc1 .Lrt_23%=
.Lrtr_24%=:
Maaa 168,40,8
s_add_u32 s65, s5, 4
Mvaa 204,36,8
Mvaa 204,132,12
s_cmp_ge_u32 s65, 20
s_cselect_b32 s90, 20, 0
Mvaa 200,128,12
Mvaa 200,32,8
s_sub_u32 s65, s65, s90
Mvaa 208,32,16
Mvaa 208,128,20
s_lshl_b32 s65, s65, 18
Mvaa 212,132,20
Ta 0,10,49152
Ta 2,10,50176
Mvaa 212,36,16
Maaa 176,40,16
s_add_u32 m0, s63, 0
Maaa 176,136,20
Maaa 180,140,20
D 16,48,65,0
Maaa 180,44,16
Maaa 240,48,16
Maaa 240,144,20
Maaa 244,148,20
D 17,48,65,1024
Ta 4,11,49152
Maaa 244,52,16
Ta 6,11,50176
Maaa 112,56,16
Maaa 112,152,20
Maaa 116,156,20
D 18,48,65,2048
Maaa 116,60,16
Maaa 124,60,24
Ra 60,26,47104
Maaa 124,156,28
Maaa 120,152,28
D 19,48,65,3072
Ta 8,12,49152
Maaa 120,56,24
s_add_u32 m0, s63, 4096
Ta 10,12,50176
Ra 56,26,45056
Maaa 252,52,24
Ra 52,26,43008
Maaa 252,148,28
Maaa 248,144,28
D 20,48,65,0
Maaa 248,48,24
Ra 48,26,40960
Maaa 188,44,24
Ra 44,26,38912
Maaa 188,140,28
Maaa 184,136,28
D 21,48,65,1024
Ta 12,13,49152
Maaa 184,40,24
Ta 14,13,50176
Ra 40,26,36864
Mvaa 220,36,24
Ra 36,26,34816
Mvaa 220,132,28
Mvaa 216,128,28
D 22,48,65,2048
Mvaa 216,32,24
Ra 32,26,32768
W 6
Maaa 68,60,0
Maaa 64,56,0
Maaa 196,52,0
D 23,48,65,3072
Ta 16,14,49152
Maaa 192,48,0
W 1
s_add_u32 s65, s5, 8
s_cmp_ge_u32 s65, 20
s_cselect_b32 s90, 20, 0
Mvaa 228,44,0
s_sub_u32 s65, s65, s90
s_lshl_b32 s65, s65, 18
Ta 18,14,50176
Mvaa 224,40,0
s_add_u32 m0, s63, 65536
Mvaa 164,36,0
D 16,48,65,0
Mvaa 160,32,0
Mvaa 168,32,4
Mvaa 172,36,4
Mvaa 232,40,4
D 17,48,65,1024
Ta 20,15,49152
Mvaa 236,44,4
Ta 22,15,50176
Maaa 200,48,4
Maaa 204,52,4
Maaa 72,56,4
D 18,48,65,2048
Maaa 76,60,4
Maaa 84,60,8
Maaa 80,56,8
Maaa 212,52,8
D 19,48,65,3072
Ta 24,24,49152
Maaa 208,48,8
s_add_u32 m0, s63, 69632
Ta 26,24,50176
Mvaa 244,44,8
Mvaa 240,40,8
Mvaa 180,36,8
D 20,48,65,0
Mvaa 176,32,8
Mvaa 184,32,12
Mvaa 188,36,12
Mvaa 248,40,12
D 21,48,65,1024
Ta 28,25,49152
Mvaa 252,44,12
Ta 30,25,50176
Maaa 216,48,12
Ra 156,27,47104
Maaa 220,52,12
Maaa 88,56,12
D 22,48,65,2048
Maaa 92,60,12
W 7
Ra 152,27,45056
Maaa 100,60,16
Maaa 96,56,16
Maaa 228,52,16
D 23,48,65,3072
Ra 148,27,43008
Maaa 224,48,16
Ta 0,10,57344
Ta 2,10,58368
Maaa 164,44,16
Maaa 160,40,16
Ra 144,27,40960
Mvaa 196,36,16
buffer_load_dword v142, v144, s[8:11], s91 offen offset:0 sc1
Mvaa 192,32,16
W 10
Mvaa 200,32,20
Ra 140,27,38912
Mvaa 204,36,20
Maaa 168,40,20
Ta 4,11,57344
Ta 6,11,58368
Maaa 172,44,20
Ra 136,27,36864
Maaa 232,48,20
Maaa 236,52,20
v_bfe_u32 v29, v1, 7, 1
Maaa 104,56,20
Ra 132,27,34816
Maaa 108,60,20
W 13
v_mul_u32_u24 v28, 10240, v29
Maaa 116,60,24
Maaa 112,56,24
Ra 128,27,32768
v_lshrrev_b32 v29, 8, v1
Maaa 244,52,24
Ta 8,12,57344
Ta 10,12,58368
Maaa 240,48,24
v_lshl_add_u32 v28, v29, 7, v28
Maaa 180,44,24
v_and_b32 v29, 0x70, v1
Maaa 176,40,24
Mvaa 212,36,24
v_add_u32 v28, v28, v29
Mvaa 208,32,24
W 14
Mvaa 216,32,28
s_and_b32 s80, s14, 2
Mvaa 220,36,28
s_mul_i32 s80, s80, 20480
Maaa 184,40,28
Ta 12,13,57344
Ta 14,13,58368
Maaa 188,44,28
s_sub_u32 s81, 40960, s80
Maaa 248,48,28
Maaa 252,52,28
s_add_u32 s93, s28, 20480
Maaa 120,56,28
Maaa 124,60,28
W 10
v_add_u32 v26, s80, v28
Maaa 68,156,0
Maaa 64,152,0
Maaa 196,148,0
Ta 16,14,57344
v_add_u32 v27, s81, v28
Maaa 192,144,0
W 5
Ta 18,14,58368
Mvaa 228,140,0
buffer_load_dwordx4 a[32:35], v26, s[8:11], s28 offen offset:0 sc1
Mvaa 224,136,0
Mvaa 164,132,0
Mvaa 160,128,0
Mvaa 168,128,4
buffer_load_dwordx4 a[36:39], v26, s[8:11], s93 offen offset:0 sc1
Mvaa 172,132,4
Mvaa 232,136,4
Ta 20,15,57344
Ta 22,15,58368
Mvaa 236,140,4
Maaa 200,144,4
buffer_load_dwordx4 a[40:43], v27, s[8:11], s28 offen offset:0 sc1
Maaa 204,148,4
Maaa 72,152,4
Maaa 76,156,4
W 6
Maaa 84,156,8
buffer_load_dwordx4 a[44:47], v27, s[8:11], s93 offen offset:0 sc1
Ta 24,24,57344
Maaa 80,152,8
s_add_u32 s28, s28, 512
s_cmp_eq_u32 s28, s4
s_cselect_b32 s93, 10240, 0
Ta 26,24,58368
Maaa 212,148,8
s_sub_u32 s28, s28, s93
Ta 28,25,57344
Ta 30,25,58368
Maaa 208,144,8
s_add_u32 s93, s28, 20480
Mvaa 244,140,8
Mvaa 240,136,8
Mvaa 180,132,8
s_add_u32 s65, s5, 12
s_cmp_ge_u32 s65, 20
s_cselect_b32 s90, 20, 0
buffer_load_dwordx4 a[48:51], v26, s[8:11], s28 offen offset:0 sc1
Mvaa 176,128,8
W 8
s_sub_u32 s65, s65, s90
s_lshl_b32 s65, s65, 18
Mvaa 184,128,12
s_add_u32 m0, s63, 32768
Mvaa 188,132,12
Mvaa 248,136,12
s_waitcnt vmcnt(6) lgkmcnt(0)
s_barrier
buffer_load_dwordx4 a[52:55], v26, s[8:11], s93 offen offset:0 sc1
Mvaa 252,140,12
Maaa 216,144,12
Maaa 220,148,12
Maaa 88,152,12
buffer_load_dwordx4 a[56:59], v27, s[8:11], s28 offen offset:0 sc1
Maaa 92,156,12
Maaa 100,156,16
Maaa 96,152,16
Maaa 228,148,16
buffer_load_dwordx4 a[60:63], v27, s[8:11], s93 offen offset:0 sc1
Maaa 224,144,16
Maaa 164,140,16
Maaa 160,136,16
Mvaa 196,132,16
D 16,48,65,0
Mvaa 192,128,16
Mvaa 200,128,20
Mvaa 204,132,20
Maaa 168,136,20
D 17,48,65,1024
Ta 0,2,0
Maaa 172,140,20
Ta 2,2,1024
Maaa 232,144,20
Maaa 236,148,20
Maaa 104,152,20
D 18,48,65,2048
Ta 4,2,8192
Maaa 108,156,20
Ta 6,2,9216
Maaa 116,156,24
Maaa 112,152,24
Maaa 244,148,24
Ta 8,3,0
Ta 10,3,1024
Maaa 240,144,24
D 19,48,65,3072
Maaa 180,140,24
s_add_u32 m0, s63, 36864
Maaa 176,136,24
Mvaa 212,132,24
Ta 12,3,8192
Ta 14,3,9216
Mvaa 208,128,24
D 20,48,65,0
Mvaa 216,128,28
Mvaa 220,132,28
Maaa 184,136,28
Ta 16,4,0
Ta 18,4,1024
Maaa 188,140,28
Maaa 248,144,28
D 21,48,65,1024
Maaa 252,148,28
Maaa 120,152,28
Maaa 124,156,28
.Lq_10%=:
s_cmp_ge_u32 s14, 2
s_cbranch_scc1 .Lpb_29%=
s_waitcnt vmcnt(6) lgkmcnt(6)
Zvaa 32,0,32
s_add_u32 s28, s28, 512
Ta 20,4,8192
Ta 22,4,9216
Mvaa 32,4,48
s_cmp_eq_u32 s28, s4
s_cselect_b32 s93, 10240, 0
Ta 24,5,0
Ta 26,5,1024
Zvaa 64,4,52
s_sub_u32 s28, s28, s93
s_add_u32 s93, s28, 20480
buffer_load_dwordx4 a[128:131], v26, s[8:11], s28 offen offset:0 sc1
Mvaa 64,0,36
Ta 28,5,8192
Ta 30,5,9216
Zvaa 96,0,40
K 184,184,185
K 185,186,187
Mvaa 96,4,56
K 186,188,189
K 187,190,191
Zvaa 128,4,60
buffer_load_dwordx4 a[132:135], v26, s[8:11], s93 offen offset:0 sc1
K 192,192,193
Mvaa 128,0,44
W 8
Ta 0,6,0
K 193,194,195
Zvaa 132,8,44
Ta 2,6,1024
K 194,196,197
Mvaa 132,12,60
K 195,198,199
K 200,200,201
Zvaa 100,12,56
buffer_load_dwordx4 a[136:139], v27, s[8:11], s28 offen offset:0 sc1
K 201,202,203
Mvaa 100,8,40
Ta 4,6,8192
Ta 6,6,9216
Zvaa 68,8,36
K 202,204,205
K 203,206,207
Mvaa 68,12,52
K 208,208,209
K 209,210,211
Zvaa 36,12,48
buffer_load_dwordx4 a[140:143], v27, s[8:11], s93 offen offset:0 sc1
K 210,212,213
Mvaa 36,8,32
W 8
s_add_u32 s28, s28, 512
s_cmp_eq_u32 s28, s4
s_cselect_b32 s93, 10240, 0
Zvaa 40,16,32
s_sub_u32 s28, s28, s93
s_add_u32 s93, s28, 20480
K 211,214,215
Mvaa 40,20,48
Ta 8,7,0
Ta 10,7,1024
Zvaa 72,20,52
buffer_load_dwordx4 a[144:147], v26, s[8:11], s28 offen offset:0 sc1
K 216,216,217
Mvaa 72,16,36
K 217,218,219
K 218,220,221
K 219,222,223
Zvaa 104,16,40
Ta 12,7,8192
Ta 14,7,9216
Mvaa 104,20,56
K 224,224,225
K 225,226,227
Zvaa 136,20,60
K 226,228,229
K 227,230,231
Mvaa 136,16,44
W 8
buffer_load_dwordx4 a[148:151], v26, s[8:11], s93 offen offset:0 sc1
K 232,232,233
Zvaa 140,24,44
K 233,234,235
K 234,236,237
)"
R"(Mvaa 140,28,60
Ta 16,8,0
K 235,238,239
Zvaa 108,28,56
Ta 18,8,1024
K 240,240,241
Mvaa 108,24,40
buffer_load_dwordx4 a[152:155], v27, s[8:11], s28 offen offset:0 sc1
K 241,242,243
Zvaa 76,24,36
K 242,244,245
K 243,246,247
Mvaa 76,28,52
Ta 20,8,8192
Ta 22,8,9216
Zvaa 44,28,48
K 248,248,249
K 249,250,251
Mvaa 44,24,32
K 250,252,253
K 251,254,255
W 8
Zvaa 48,0,32
buffer_load_dwordx4 a[156:159], v27, s[8:11], s93 offen offset:0 sc1
Ta 24,9,0
Mvaa 48,4,48
s_add_u32 s28, s28, 512
s_cmp_eq_u32 s28, s4
s_cselect_b32 s93, 10240, 0
s_sub_u32 s28, s28, s93
Zvaa 80,4,52
s_add_u32 s93, s28, 20480
Ta 26,9,1024
K 160,160,161
Mvaa 80,0,36
Ta 28,9,8192
Ta 30,9,9216
Zvaa 112,0,40
buffer_load_dwordx4 v[188:191], v26, s[8:11], s28 offen offset:0 sc1
K 161,162,163
Mvaa 112,4,56
K 162,164,165
Zvaa 144,4,60
K 163,166,167
Mvaa 144,0,44
W 8
Ta 0,2,16384
Ta 2,2,17408
Zvaa 148,8,44
s_add_u32 s36, s29, 0
buffer_load_dwordx4 v[196:199], v26, s[8:11], s93 offen offset:0 sc1
Mvaa 148,12,60
Zvaa 116,12,56
Ta 4,2,24576
Ta 6,2,25600
Mvaa 116,8,40
Zvaa 84,8,36
buffer_load_dwordx4 v[204:207], v27, s[8:11], s28 offen offset:0 sc1
Mvaa 84,12,52
Zvaa 52,12,48
Mvaa 52,8,32
W 8
Ta 8,3,16384
Ta 10,3,17408
Zvaa 56,16,32
buffer_load_dwordx4 v[212:215], v27, s[8:11], s93 offen offset:0 sc1
Mvaa 56,20,48
s_add_u32 s28, s28, 512
s_cmp_eq_u32 s28, s4
s_cselect_b32 s93, 10240, 0
s_sub_u32 s28, s28, s93
Zvaa 88,20,52
s_add_u32 s93, s28, 20480
Ta 12,3,24576
Ta 14,3,25600
Mvaa 88,16,36
Zvaa 120,16,40
buffer_store_dwordx4 v[160:163], v1, s[8:11], s36 offen offset:0 sc1
K 168,168,169
Mvaa 120,20,56
K 169,170,171
K 170,172,173
Zvaa 152,20,60
Mvaa 152,16,44
W 8
Ta 16,4,16384
K 171,174,175
Zvaa 156,24,44
buffer_load_dwordx4 v[220:223], v26, s[8:11], s28 offen offset:0 sc1
Ta 18,4,17408
Mvaa 156,28,60
Zvaa 124,28,56
Ta 20,4,24576
Ta 22,4,25600
Mvaa 124,24,40
Zvaa 92,24,36
buffer_load_dwordx4 v[228:231], v26, s[8:11], s93 offen offset:0 sc1
Mvaa 92,28,52
Zvaa 60,28,48
Mvaa 60,24,32
s_waitcnt vmcnt(7) lgkmcnt(8)
Ta 24,5,16384
Ta 26,5,17408
Mvaa 32,0,128
buffer_load_dwordx4 v[236:239], v27, s[8:11], s28 offen offset:0 sc1
Mvaa 32,4,144
Mvaa 64,4,148
Ta 28,5,24576
Ta 30,5,25600
Mvaa 64,0,132
buffer_store_dwordx4 v[168:171], v1, s[8:11], s36 offen offset:1024 sc1
K 176,176,177
Mvaa 96,0,136
K 177,178,179
K 178,180,181
Mvaa 96,4,152
Mvaa 128,4,156
Mvaa 128,0,140
W 8
buffer_load_dwordx4 v[244:247], v27, s[8:11], s93 offen offset:0 sc1
K 179,182,183
Mvaa 132,8,140
s_add_u32 s28, s28, 512
s_cmp_eq_u32 s28, s4
s_cselect_b32 s93, 10240, 0
s_sub_u32 s28, s28, s93
Mvaa 132,12,156
s_add_u32 s93, s28, 20480
Ta 0,6,16384
Ta 2,6,17408
Mvaa 100,12,152
Ta 4,6,24576
Ta 6,6,25600
Mvaa 100,8,136
buffer_load_dwordx4 v[252:255], v26, s[8:11], s28 offen offset:0 sc1
Mvaa 68,8,132
Mvaa 68,12,148
Mvaa 36,12,144
Mvaa 36,8,128
W 8
D 22,48,65,2048
Ta 8,7,16384
Mvaa 40,16,128
Ta 10,7,17408
Mvaa 40,20,144
Mvaa 72,20,148
Ta 12,7,24576
Ta 14,7,25600
Mvaa 72,16,132
buffer_store_dwordx4 v[176:179], v1, s[8:11], s36 offen offset:2048 sc1
Mvaa 104,16,136
Mvaa 104,20,152
Mvaa 136,20,156
Mvaa 136,16,140
W 8
buffer_load_dwordx4 a[32:35], v26, s[8:11], s93 offen offset:0 sc1
Ta 16,8,16384
Mvaa 140,24,140
Ta 18,8,17408
Mvaa 140,28,156
Mvaa 108,28,152
Ta 20,8,24576
Ta 22,8,25600
Mvaa 108,24,136
buffer_load_dwordx4 a[36:39], v27, s[8:11], s28 offen offset:0 sc1
Mvaa 76,24,132
Mvaa 76,28,148
Mvaa 44,28,144
Mvaa 44,24,128
W 8
buffer_load_dwordx4 a[40:43], v27, s[8:11], s93 offen offset:0 sc1
Ta 24,9,16384
Mvaa 48,0,128
s_add_u32 s28, s28, 512
s_cmp_eq_u32 s28, s4
s_cselect_b32 s93, 10240, 0
s_sub_u32 s28, s28, s93
Mvaa 48,4,144
s_add_u32 s93, s28, 20480
Ta 26,9,17408
Mvaa 80,4,148
Ta 28,9,24576
Ta 30,9,25600
Mvaa 80,0,132
buffer_store_dwordx4 v[184:187], v1, s[8:11], s36 offen offset:3072 sc1
Mvaa 112,0,136
s_add_u32 s36, s29, 4096
s_lshl_b32 s80, s56, 9
Mvaa 112,4,152
Mvaa 144,4,156
s_lshl_b32 s81, s57, 4
Mvaa 144,0,140
W 8
s_add_u32 s80, s80, s81
buffer_load_dwordx4 a[44:47], v26, s[8:11], s28 offen offset:0 sc1
Mvaa 148,8,140
Ta 0,10,0
Ta 2,10,1024
Mvaa 148,12,156
s_and_b32 s81, s14, 1
Mvaa 116,12,152
s_lshl_b32 s81, s81, 3
Ta 4,10,8192
Ta 6,10,9216
Mvaa 116,8,136
buffer_load_dwordx4 a[48:51], v26, s[8:11], s93 offen offset:0 sc1
Mvaa 84,8,132
s_add_u32 s80, s80, s81
Mvaa 84,12,148
s_add_u32 s71, s80, 4096
Mvaa 52,12,144
Mvaa 52,8,128
W 8
buffer_load_dwordx4 a[52:55], v27, s[8:11], s28 offen offset:0 sc1
Ta 8,11,0
Mvaa 56,16,128
Ta 10,11,1024
Mvaa 56,20,144
Mvaa 88,20,148
buffer_store_dwordx4 v[192:195], v1, s[8:11], s36 offen offset:0 sc1
Ta 12,11,8192
Mvaa 88,16,132
Ta 14,11,9216
Mvaa 120,16,136
Mvaa 120,20,152
Mvaa 152,20,156
buffer_load_dwordx4 a[56:59], v27, s[8:11], s93 offen offset:0 sc1
Mvaa 152,16,140
W 8
s_add_u32 s28, s28, 512
s_cmp_eq_u32 s28, s4
s_cselect_b32 s93, 10240, 0
Mvaa 156,24,140
s_sub_u32 s28, s28, s93
s_add_u32 s93, s28, 20480
Ta 16,12,0
Mvaa 156,28,156
Ta 18,12,1024
Mvaa 124,28,152
buffer_load_dwordx4 a[60:63], v26, s[8:11], s28 offen offset:0 sc1
Ta 20,12,8192
Mvaa 124,24,136
Ta 22,12,9216
Mvaa 92,24,132
Mvaa 92,28,148
Mvaa 60,28,144
D 23,48,65,3072
Mvaa 60,24,128
s_waitcnt vmcnt(14) lgkmcnt(8)
s_add_u32 s65, s5, 16
s_cmp_ge_u32 s65, 20
s_cselect_b32 s90, 20, 0
Mvav 32,0,188
s_sub_u32 s65, s65, s90
s_lshl_b32 s65, s65, 18
s_add_u32 m0, s63, 98304
Mvav 32,4,220
Ta 24,13,0
Ta 26,13,1024
Mvav 64,4,228
buffer_store_dwordx4 v[200:203], v1, s[8:11], s36 offen offset:1024 sc1
Ta 28,13,8192
Mvav 64,0,196
Ta 30,13,9216
Mvav 96,0,204
Mvav 96,4,236
Mvav 128,4,244
D 16,48,65,0
Mvav 128,0,212
W 8
Ta 0,14,0
Ta 2,14,1024
Mvav 132,8,212
v_add_u32 v2, 65536, v10
Mvav 132,12,244
Mvav 100,12,236
buffer_load_dwordx4 v[184:187], v26, s[8:11], s93 offen offset:0 sc1
Ta 4,14,8192
Mvav 100,8,204
Ta 6,14,9216
v_add_u32 v3, 65536, v11
Mvav 68,8,196
Mvav 68,12,228
Mvav 36,12,220
buffer_load_dwordx4 v[192:195], v27, s[8:11], s28 offen offset:0 sc1
Mvav 36,8,188
W 8
Ta 8,15,0
Ta 10,15,1024
Mvav 40,16,188
v_add_u32 v4, 65536, v12
Mvav 40,20,220
Mvav 72,20,228
buffer_store_dwordx4 v[208:211], v1, s[8:11], s36 offen offset:2048 sc1
Ta 12,15,8192
Mvav 72,16,196
Ta 14,15,9216
v_add_u32 v5, 65536, v13
Mvav 104,16,204
Mvav 104,20,236
Mvav 136,20,244
D 17,48,65,1024
Mvav 136,16,212
W 8
Ta 16,24,0
Ta 18,24,1024
Mvav 140,24,212
v_add_u32 v6, 65536, v14
Mvav 140,28,244
Mvav 108,28,236
buffer_load_dwordx4 a[128:131], v27, s[8:11], s93 offen offset:0 sc1
Ta 20,24,8192
Mvav 108,24,204
s_add_u32 s28, s28, 512
s_cmp_eq_u32 s28, s4
s_cselect_b32 s93, 10240, 0
s_sub_u32 s28, s28, s93
Mvav 76,24,196
s_add_u32 s93, s28, 20480
Ta 22,24,9216
v_add_u32 v7, 65536, v15
Mvav 76,28,228
Mvav 44,28,220
buffer_load_dwordx4 a[132:135], v26, s[8:11], s28 offen offset:0 sc1
Mvav 44,24,188
W 8
Ta 24,25,0
Ta 26,25,1024
Mvav 48,0,188
v_add_u32 v8, 65536, v24
Mvav 48,4,220
Mvav 80,4,228
buffer_store_dwordx4 v[216:219], v1, s[8:11], s36 offen offset:3072 sc1
Ta 28,25,8192
Mvav 80,0,196
s_add_u32 s36, s29, 458752
Ta 30,25,9216
v_add_u32 v9, 65536, v25
Mvav 112,0,204
Mvav 112,4,236
Mvav 144,4,244
buffer_load_dwordx4 a[136:139], v26, s[8:11], s93 offen offset:0 sc1
Mvav 144,0,212
W 8
Ta 0,10,16384
Ta 2,10,17408
Mvav 148,8,212
Mvav 148,12,244
Mvav 116,12,236
buffer_load_dwordx4 a[140:143], v27, s[8:11], s28 offen offset:0 sc1
Ta 4,10,24576
Mvav 116,8,204
Ta 6,10,25600
Mvav 84,8,196
Mvav 84,12,228
Mvav 52,12,220
buffer_load_dwordx4 a[144:147], v27, s[8:11], s93 offen offset:0 sc1
Mvav 52,8,188
W 8
s_add_u32 s28, s28, 512
s_cmp_eq_u32 s28, s4
s_cselect_b32 s93, 10240, 0
Mvav 56,16,188
s_sub_u32 s28, s28, s93
s_add_u32 s93, s28, 20480
Ta 8,11,16384
Mvav 56,20,220
buffer_store_dwordx4 v[224:227], v1, s[8:11], s36 offen offset:0 sc1
Ta 10,11,17408
Mvav 88,20,228
Ta 12,11,24576
Ta 14,11,25600
Mvav 88,16,196
Mvav 120,16,204
Mvav 120,20,236
buffer_load_dwordx4 a[148:151], v26, s[8:11], s28 offen offset:0 sc1
Mvav 152,20,244
Mvav 152,16,212
W 8
Ta 16,12,16384
Ta 18,12,17408
Mvav 156,24,212
Mvav 156,28,244
buffer_load_dwordx4 a[152:155], v26, s[8:11], s93 offen offset:0 sc1
Mvav 124,28,236
Ta 20,12,24576
Ta 22,12,25600
Mvav 124,24,204
Mvav 92,24,196
Mvav 92,28,228
buffer_load_dwordx4 a[156:159], v27, s[8:11], s28 offen offset:0 sc1
Mvav 60,28,220
Mvav 60,24,188
s_waitcnt vmcnt(18) lgkmcnt(8)
Ta 24,13,16384
Ta 26,13,17408
Mvav 32,0,252
Mvaa 32,4,44
buffer_store_dwordx4 v[232:235], v1, s[8:11], s36 offen offset:1024 sc1
Mvaa 64,4,48
)"
R"(Ta 28,13,24576
Ta 30,13,25600
Mvaa 64,0,32
Mvaa 96,0,36
Mvaa 96,4,52
D 18,48,65,2048
Mvaa 128,4,56
Mvaa 128,0,40
W 8
Ta 0,14,16384
Ta 2,14,17408
Mvaa 132,8,40
Mvaa 132,12,56
buffer_load_dwordx4 v[200:203], v27, s[8:11], s93 offen offset:0 sc1
Mvaa 100,12,52
s_add_u32 s28, s28, 512
s_cmp_eq_u32 s28, s4
s_cselect_b32 s93, 10240, 0
s_sub_u32 s28, s28, s93
Mvaa 100,8,36
s_add_u32 s93, s28, 20480
Ta 4,14,24576
Ta 6,14,25600
Mvaa 68,8,32
Mvaa 68,12,48
buffer_load_dwordx4 v[208:211], v26, s[8:11], s28 offen offset:0 sc1
Mvaa 36,12,44
Mvav 36,8,252
W 8
Ta 8,15,16384
Ta 10,15,17408
Mvav 40,16,252
Mvaa 40,20,44
buffer_store_dwordx4 v[240:243], v1, s[8:11], s36 offen offset:2048 sc1
Mvaa 72,20,48
Ta 12,15,24576
Ta 14,15,25600
Mvaa 72,16,32
Mvaa 104,16,36
Mvaa 104,20,52
buffer_load_dwordx4 v[216:219], v26, s[8:11], s93 offen offset:0 sc1
Mvaa 136,20,56
Mvaa 136,16,40
W 8
Ta 16,24,16384
Ta 18,24,17408
Mvaa 140,24,40
Mvaa 140,28,56
buffer_load_dwordx4 v[224:227], v27, s[8:11], s28 offen offset:0 sc1
Mvaa 108,28,52
Ta 20,24,24576
Ta 22,24,25600
Mvaa 108,24,36
Mvaa 76,24,32
Mvaa 76,28,48
D 19,48,65,3072
Mvaa 44,28,44
s_add_u32 m0, s63, 102400
Mvav 44,24,252
W 8
Ta 24,25,16384
Ta 26,25,17408
Mvav 48,0,252
Mvaa 48,4,44
buffer_store_dwordx4 v[248:251], v1, s[8:11], s36 offen offset:3072 sc1
G 160,160
Mvaa 80,4,48
G 161,161
G 162,162
Mvaa 80,0,32
G 163,163
G 164,164
Mvaa 112,0,36
G 165,165
G 166,166
Mvaa 112,4,52
buffer_load_dwordx4 v[188:191], v27, s[8:11], s93 offen offset:0 sc1
Ta 28,25,24576
Mvaa 144,4,56
s_add_u32 s28, s28, 512
s_cmp_eq_u32 s28, s4
s_cselect_b32 s93, 10240, 0
s_sub_u32 s28, s28, s93
Mvaa 144,0,40
W 7
s_add_u32 s93, s28, 20480
Ta 30,25,25600
Mvaa 148,8,40
G 167,167
K 160,160,161
Mvaa 148,12,56
buffer_load_dwordx4 v[196:199], v26, s[8:11], s28 offen offset:0 sc1
K 161,162,163
Mvaa 116,12,52
K 162,164,165
K 163,166,167
Mvaa 116,8,36
s_add_u32 s36, s29, 462848
Ta 0,2,0
Ta 2,2,1024
Mvaa 84,8,32
Ta 4,2,8192
Ta 6,2,9216
Mvaa 84,12,48
buffer_load_dwordx4 v[204:207], v26, s[8:11], s93 offen offset:0 sc1
Mvaa 52,12,44
Mvav 52,8,252
W 8
Ta 8,3,0
Ta 10,3,1024
Mvav 56,16,252
buffer_store_dwordx4 v[160:163], v1, s[8:11], s36 offen offset:0 sc1
G 168,168
Mvaa 56,20,44
G 169,169
G 170,170
Mvaa 88,20,48
G 171,171
G 172,172
Mvaa 88,16,32
G 173,173
G 174,174
Mvaa 120,16,36
D 20,48,65,0
Ta 12,3,8192
Mvaa 120,20,52
Ta 14,3,9216
G 175,175
Mvaa 152,20,56
K 168,168,169
K 169,170,171
Mvaa 152,16,40
W 8
K 170,172,173
K 171,174,175
Mvaa 156,24,40
buffer_load_dwordx4 v[212:215], v27, s[8:11], s28 offen offset:0 sc1
Ta 16,4,0
Mvaa 156,28,56
Ta 18,4,1024
Mvaa 124,28,52
Ta 20,4,8192
Ta 22,4,9216
Mvaa 124,24,36
Mvaa 92,24,32
buffer_load_dwordx4 v[220:223], v27, s[8:11], s93 offen offset:0 sc1
Mvaa 92,28,48
s_add_u32 s28, s28, 512
s_cmp_eq_u32 s28, s4
s_cselect_b32 s93, 10240, 0
s_sub_u32 s28, s28, s93
Mvaa 60,28,44
s_add_u32 s93, s28, 20480
Mvav 60,24,252
s_waitcnt vmcnt(20) lgkmcnt(8)
Ta 24,5,0
Ta 26,5,1024
Mvaa 32,0,60
buffer_store_dwordx4 v[168:171], v1, s[8:11], s36 offen offset:1024 sc1
G 176,176
Mvaa 32,4,132
G 177,177
G 178,178
Mvaa 64,4,136
G 179,179
G 180,180
Mvav 64,0,184
G 181,181
G 182,182
Mvav 96,0,192
buffer_load_dwordx4 v[228:231], v26, s[8:11], s28 offen offset:0 sc1
Ta 28,5,8192
Mvaa 96,4,140
Ta 30,5,9216
G 183,183
Mvaa 128,4,144
K 176,176,177
K 177,178,179
Mvaa 128,0,128
W 8
K 178,180,181
K 179,182,183
Mvaa 132,8,128
buffer_load_dwordx4 v[236:239], v26, s[8:11], s93 offen offset:0 sc1
Ta 0,6,0
Mvaa 132,12,144
Ta 2,6,1024
Mvaa 100,12,140
Ta 4,6,8192
Ta 6,6,9216
Mvav 100,8,192
Mvav 68,8,184
D 21,48,65,1024
Mvaa 68,12,136
Mvaa 36,12,132
Mvaa 36,8,60
W 8
Ta 8,7,0
Ta 10,7,1024
Mvaa 40,16,60
buffer_store_dwordx4 v[176:179], v1, s[8:11], s36 offen offset:2048 sc1
G 160,184
Mvaa 40,20,132
G 161,185
G 162,186
Mvaa 72,20,136
G 163,187
G 164,188
Mvav 72,16,184
G 165,189
G 166,190
Mvav 104,16,192
buffer_load_dwordx4 v[232:235], v27, s[8:11], s28 offen offset:0 sc1
Ta 12,7,8192
Mvaa 104,20,140
Ta 14,7,9216
G 167,191
Mvaa 136,20,144
K 160,160,161
K 161,162,163
Mvaa 136,16,128
W 8
K 162,164,165
K 163,166,167
Mvaa 140,24,128
buffer_load_dwordx4 a[32:35], v27, s[8:11], s93 offen offset:0 sc1
Ta 16,8,0
Mvaa 140,28,144
s_add_u32 s28, s28, 512
s_cmp_eq_u32 s28, s4
s_cselect_b32 s93, 10240, 0
s_sub_u32 s28, s28, s93
Mvaa 108,28,140
s_add_u32 s93, s28, 20480
Ta 18,8,1024
Ta 20,8,8192
Mvav 108,24,192
Ta 22,8,9216
Mvav 76,24,184
buffer_load_dwordx4 a[36:39], v26, s[8:11], s28 offen offset:0 sc1
Mvaa 76,28,136
Mvaa 44,28,132
Mvaa 44,24,60
W 8
Ta 24,9,0
Ta 26,9,1024
Mvaa 48,0,60
buffer_store_dwordx4 v[160:163], v1, s[8:11], s36 offen offset:3072 sc1
G 168,192
Mvaa 48,4,132
G 169,193
G 170,194
Mvaa 80,4,136
G 171,195
G 172,196
Mvav 80,0,184
G 173,197
G 174,198
Mvav 112,0,192
D 22,48,65,2048
G 175,199
Mvaa 112,4,140
Ta 28,9,8192
Ta 30,9,9216
Mvaa 144,4,144
K 168,168,169
K 169,170,171
Mvaa 144,0,128
W 8
K 170,172,173
K 171,174,175
Mvaa 148,8,128
buffer_load_dwordx4 a[40:43], v26, s[8:11], s93 offen offset:0 sc1
Ta 0,2,16384
Mvaa 148,12,144
Ta 2,2,17408
v_accvgpr_write_b32 a192, v168
Mvaa 116,12,140
v_accvgpr_write_b32 a193, v169
v_accvgpr_write_b32 a194, v170
Mvav 116,8,192
Ta 4,2,24576
Ta 6,2,25600
Mvav 84,8,184
buffer_load_dwordx4 a[44:47], v27, s[8:11], s28 offen offset:0 sc1
v_accvgpr_write_b32 a195, v171
Mvaa 84,12,136
G 176,64
Mvaa 52,12,132
G 177,65
Mvaa 52,8,60
W 8
Ta 8,3,16384
G 178,66
Mvaa 56,16,60
D 23,48,65,3072
Ta 10,3,17408
Mvaa 56,20,132
G 179,67
G 180,68
Mvaa 88,20,136
Ta 12,3,24576
Ta 14,3,25600
Mvav 88,16,184
G 181,69
Mvav 120,16,192
buffer_load_dwordx4 a[48:51], v27, s[8:11], s93 offen offset:0 sc1
G 182,70
Mvaa 120,20,140
s_add_u32 s28, s28, 512
s_cmp_eq_u32 s28, s4
s_cselect_b32 s93, 10240, 0
s_sub_u32 s28, s28, s93
Mvaa 152,20,144
s_add_u32 s93, s28, 20480
G 183,71
v_lshrrev_b32 v20, 4, v1
Mvaa 152,16,128
W 8
K 176,176,177
v_add_u32 v20, 0, v20
Mvaa 156,24,128
buffer_load_dwordx4 a[52:55], v26, s[8:11], s28 offen offset:0 sc1
Ta 16,4,16384
Mvaa 156,28,144
Ta 18,4,17408
K 177,178,179
Mvaa 124,28,140
v_mul_u32_u24 v22, 205, v20
K 178,180,181
Mvav 124,24,192
Ta 20,4,24576
K 179,182,183
Mvav 92,24,184
buffer_load_dwordx4 a[56:59], v26, s[8:11], s93 offen offset:0 sc1
Ta 22,4,25600
Mvaa 92,28,136
v_lshrrev_b32 v22, 12, v22
v_accvgpr_write_b32 a196, v176
Mvaa 60,28,132
v_mul_u32_u24 v23, 20, v22
v_accvgpr_write_b32 a197, v177
Mvaa 60,24,60
s_waitcnt vmcnt(22) lgkmcnt(8)
v_accvgpr_write_b32 a198, v178
v_sub_u32 v20, v20, v23
Mvaa 32,0,148
buffer_load_dwordx4 a[184:187], v27, s[8:11], s28 offen offset:0 sc1
v_accvgpr_write_b32 a199, v179
Mvav 32,4,208
Ta 24,5,16384
Ta 26,5,17408
Mvav 64,4,216
v_lshrrev_b32 v23, 1, v22
G 168,200
Mvaa 64,0,152
v_and_b32 v23, 2, v23
G 169,201
Mvaa 96,0,156
buffer_load_dwordx4 a[188:191], v27, s[8:11], s93 offen offset:0 sc1
Ta 28,5,24576
Mvav 96,4,224
s_add_u32 s28, s28, 512
s_cmp_eq_u32 s28, s4
s_cselect_b32 s93, 10240, 0
s_sub_u32 s28, s28, s93
Mvav 128,4,188
s_add_u32 s93, s28, 20480
Ta 30,5,25600
G 170,202
Mvav 128,0,200
W 8
v_xor_b32 v20, v20, v23
G 171,203
Mvav 132,8,200
buffer_load_dwordx4 a[60:63], v26, s[8:11], s28 offen offset:0 sc1
v_mul_u32_u24 v22, 80, v22
Mvav 132,12,188
G 172,204
v_add_lshl_u32 v16, v20, v22, 4
Mvav 100,12,224
Ta 0,6,16384
G 173,205
Mvaa 100,8,156
Ta 2,6,17408
G 174,206
Mvaa 68,8,152
buffer_load_dwordx4 v[184:187], v26, s[8:11], s93 offen offset:0 sc1
v_lshrrev_b32 v20, 4, v1
Mvav 68,12,216
G 175,207
v_add_u32 v20, 64, v20
Mvav 36,12,208
Ta 4,6,24576
Ta 6,6,25600
Mvaa 36,8,148
W 8
K 168,168,169
K 169,170,171
Mvaa 40,16,148
)"
R"(v_mul_u32_u24 v22, 205, v20
K 170,172,173
Mvav 40,20,208
v_lshrrev_b32 v22, 12, v22
K 171,174,175
Mvav 72,20,216
Ta 8,7,16384
v_mul_u32_u24 v23, 20, v22
Mvaa 72,16,152
Ta 10,7,17408
v_accvgpr_write_b32 a200, v168
Mvaa 104,16,156
buffer_load_dwordx4 v[192:195], v27, s[8:11], s28 offen offset:0 sc1
v_accvgpr_write_b32 a201, v169
Mvav 104,20,224
v_sub_u32 v20, v20, v23
v_accvgpr_write_b32 a202, v170
Mvav 136,20,188
Ta 12,7,24576
Ta 14,7,25600
Mvav 136,16,200
W 8
v_lshrrev_b32 v23, 1, v22
v_accvgpr_write_b32 a203, v171
Mvav 140,24,200
v_and_b32 v23, 2, v23
G 176,72
Mvav 140,28,188
G 177,73
v_xor_b32 v20, v20, v23
Mvav 108,28,224
Ta 16,8,16384
G 178,74
Mvaa 108,24,156
Ta 18,8,17408
v_mul_u32_u24 v22, 80, v22
Mvaa 76,24,152
buffer_load_dwordx4 a[128:131], v27, s[8:11], s93 offen offset:0 sc1
G 179,75
Mvav 76,28,216
G 180,76
v_add_lshl_u32 v17, v20, v22, 4
Mvav 44,28,208
Ta 20,8,24576
Ta 22,8,25600
Mvaa 44,24,148
W 8
G 181,77
v_subrev_u32 v17, 1024, v17
Mvaa 48,0,148
s_add_u32 s28, s28, 512
G 182,78
v_lshrrev_b32 v20, 4, v1
Mvav 48,4,208
s_cmp_eq_u32 s28, s4
s_cselect_b32 s93, 10240, 0
G 183,79
K 176,176,177
Mvav 80,4,216
s_sub_u32 s28, s28, s93
Ta 24,9,16384
v_add_u32 v20, 128, v20
Mvaa 80,0,152
s_add_u32 s93, s28, 20480
Ta 26,9,17408
K 177,178,179
Mvaa 112,0,156
buffer_load_dwordx4 a[132:135], v26, s[8:11], s28 offen offset:0 sc1
v_mul_u32_u24 v22, 205, v20
Mvav 112,4,224
K 178,180,181
v_lshrrev_b32 v22, 12, v22
Mvav 144,4,188
Ta 28,9,24576
K 179,182,183
Mvav 144,0,200
W 7
Ta 30,9,25600
v_accvgpr_write_b32 a204, v176
Mvav 148,8,200
s_waitcnt vmcnt(14)
v_mov_b64 v[28:29], s[12:13]
s_mov_b64 exec, 1
buffer_store_dwordx2 v[28:29], off, s[8:11], s71 sc1
s_mov_b64 exec, -1
v_mul_u32_u24 v23, 20, v22
v_accvgpr_write_b32 a205, v177
Mvav 148,12,188
v_sub_u32 v20, v20, v23
v_accvgpr_write_b32 a206, v178
Mvav 116,12,224
v_accvgpr_write_b32 a207, v179
v_lshrrev_b32 v23, 1, v22
Mvaa 116,8,156
G 168,208
v_and_b32 v23, 2, v23
Mvaa 84,8,152
s_waitcnt vmcnt(11) lgkmcnt(0)
s_barrier
buffer_load_dwordx4 a[136:139], v26, s[8:11], s93 offen offset:0 sc1
Mvav 84,12,216
G 169,209
v_xor_b32 v20, v20, v23
Mvav 52,12,208
G 170,210
G 171,211
Mvaa 52,8,148
Ta 0,10,32768
Ta 2,10,33792
Mvaa 56,16,148
Ta 4,10,40960
Ta 6,10,41984
Mvav 56,20,208
v_mul_u32_u24 v22, 80, v22
G 172,212
Mvav 88,20,216
v_add_lshl_u32 v18, v20, v22, 4
G 173,213
Mvaa 88,16,152
Ta 8,11,32768
Ta 10,11,33792
Mvaa 120,16,156
G 174,214
v_subrev_u32 v18, 2048, v18
Mvav 120,20,224
buffer_load_dwordx4 a[140:143], v27, s[8:11], s28 offen offset:0 sc1
G 175,215
Mvav 152,20,188
Ta 12,11,40960
v_lshrrev_b32 v20, 4, v1
Mvav 152,16,200
Ta 14,11,41984
K 168,168,169
Mvav 156,24,200
v_add_u32 v20, 192, v20
K 169,170,171
Mvav 156,28,188
K 170,172,173
v_mul_u32_u24 v22, 205, v20
Mvav 124,28,224
K 171,174,175
v_lshrrev_b32 v22, 12, v22
Mvaa 124,24,156
Ta 16,12,32768
Ta 18,12,33792
Mvaa 92,24,152
buffer_load_dwordx4 a[144:147], v27, s[8:11], s93 offen offset:0 sc1
v_accvgpr_write_b32 a208, v168
Mvav 92,28,216
v_mul_u32_u24 v23, 20, v22
v_accvgpr_write_b32 a209, v169
Mvav 60,28,208
v_accvgpr_write_b32 a210, v170
v_sub_u32 v20, v20, v23
Mvaa 60,24,148
W 6
Ta 20,12,40960
Ta 22,12,41984
Mvav 32,0,196
s_add_u32 s28, s28, 512
v_accvgpr_write_b32 a211, v171
v_lshrrev_b32 v23, 1, v22
Mvav 32,4,228
s_cmp_eq_u32 s28, s4
s_cselect_b32 s93, 10240, 0
G 176,80
G 177,81
Mvav 64,4,236
s_sub_u32 s28, s28, s93
v_and_b32 v23, 2, v23
G 178,82
Mvav 64,0,204
s_add_u32 s93, s28, 20480
Ta 24,13,32768
v_xor_b32 v20, v20, v23
Mvav 96,0,212
Ta 26,13,33792
G 179,83
Mvav 96,4,232
buffer_load_dwordx4 v[200:203], v26, s[8:11], s28 offen offset:0 sc1
v_mul_u32_u24 v22, 80, v22
Mvaa 128,4,32
Ta 28,13,40960
G 180,84
Mvav 128,0,220
W 7
Ta 30,13,41984
G 181,85
Mvav 132,8,220
v_add_lshl_u32 v19, v20, v22, 4
G 182,86
Mvaa 132,12,32
v_subrev_u32 v19, 3072, v19
G 183,87
Mvav 100,12,232
v_lshrrev_b32 v20, 4, v1
K 176,176,177
Mvav 100,8,212
Ta 0,14,32768
Ta 2,14,33792
Mvav 68,8,204
K 177,178,179
v_add_u32 v20, 256, v20
Mvav 68,12,236
buffer_load_dwordx4 v[208:211], v26, s[8:11], s93 offen offset:0 sc1
K 178,180,181
Mvav 36,12,228
v_mul_u32_u24 v22, 205, v20
K 179,182,183
Mvav 36,8,196
W 6
Ta 4,14,40960
Ta 6,14,41984
Mvav 40,16,196
v_accvgpr_write_b32 a212, v176
v_lshrrev_b32 v22, 12, v22
Mvav 40,20,228
v_accvgpr_write_b32 a213, v177
v_mul_u32_u24 v23, 20, v22
Mvav 72,20,236
v_accvgpr_write_b32 a214, v178
v_sub_u32 v20, v20, v23
Mvav 72,16,204
Ta 8,15,32768
v_accvgpr_write_b32 a215, v179
Mvav 104,16,212
Ta 10,15,33792
buffer_load_dwordx4 v[216:219], v27, s[8:11], s28 offen offset:0 sc1
Mvav 104,20,232
G 168,216
v_lshrrev_b32 v23, 1, v22
Mvaa 136,20,32
G 169,217
v_and_b32 v23, 2, v23
Mvav 136,16,220
W 6
Ta 12,15,40960
Ta 14,15,41984
Mvav 140,24,220
G 170,218
v_xor_b32 v20, v20, v23
Mvaa 140,28,32
G 171,219
G 172,220
Mvav 108,28,232
v_mul_u32_u24 v22, 80, v22
G 173,221
Mvav 108,24,212
Ta 16,24,32768
Ta 18,24,33792
Mvav 76,24,204
v_add_lshl_u32 v20, v20, v22, 4
G 174,222
Mvav 76,28,236
buffer_load_dwordx4 v[224:227], v27, s[8:11], s93 offen offset:0 sc1
G 175,223
Mvav 44,28,228
Ta 20,24,40960
v_lshrrev_b32 v22, 6, v1
Mvav 44,24,196
W 7
Ta 22,24,41984
K 168,168,169
Mvav 48,0,196
s_add_u32 s28, s28, 512
v_mul_u32_u24 v22, 320, v22
K 169,170,171
Mvav 48,4,228
s_cmp_eq_u32 s28, s4
s_cselect_b32 s93, 10240, 0
v_bfe_u32 v23, v1, 4, 2
K 170,172,173
Mvav 80,4,236
s_sub_u32 s28, s28, s93
K 171,174,175
v_lshl_add_u32 v22, v23, 3, v22
Mvav 80,0,204
s_add_u32 s93, s28, 20480
Ta 24,25,32768
Ta 26,25,33792
Mvav 112,0,212
v_accvgpr_write_b32 a216, v168
v_bfe_u32 v23, v1, 8, 1
Mvav 112,4,232
buffer_load_dwordx4 v[188:191], v26, s[8:11], s28 offen offset:0 sc1
v_accvgpr_write_b32 a217, v169
Mvaa 144,4,32
v_lshlrev_b32 v23, 5, v23
v_accvgpr_write_b32 a218, v170
Mvav 144,0,220
W 6
Ta 28,25,40960
Ta 30,25,41984
Mvav 148,8,220
v_accvgpr_write_b32 a219, v171
v_add_u32 v21, v22, v23
Mvaa 148,12,32
G 176,88
v_sub_u32 v22, v22, v23
Mvav 116,12,232
s_mul_i32 s94, s59, 655360
G 177,89
G 178,90
Mvav 116,8,212
s_mul_i32 s95, s58, 320
Ta 0,10,49152
Ta 2,10,50176
Mvav 84,8,204
s_add_u32 s94, s94, s95
G 179,91
G 180,92
Mvav 84,12,236
Ta 4,10,57344
Ta 6,10,58368
Mvav 52,12,228
G 181,93
G 182,94
Mvav 52,8,196
W 8
s_add_u32 s40, s26, s94
s_addc_u32 s41, s27, 0
Mvav 56,16,196
s_and_b32 s41, s41, 0xffff
G 183,95
K 176,176,177
Mvav 56,20,228
s_mov_b32 s42, 5242880
Ta 8,11,49152
Ta 10,11,50176
Mvav 88,20,236
K 177,178,179
K 178,180,181
Mvav 88,16,204
s_mov_b32 s43, 0x20000
Ta 12,11,57344
K 179,182,183
Mvav 120,16,212
Ta 14,11,58368
v_accvgpr_write_b32 a220, v176
Mvav 120,20,232
s_sub_u32 s42, s42, s94
v_accvgpr_write_b32 a221, v177
Mvaa 152,20,32
v_accvgpr_write_b32 a222, v178
Mvav 152,16,220
W 8
s_mov_b64 s[44:45], s[8:9]
v_accvgpr_write_b32 a223, v179
Mvav 156,24,220
s_mov_b32 s47, 0x20000
Ta 16,12,49152
Ta 18,12,50176
Mvaa 156,28,32
G 168,224
Mvav 124,28,232
s_and_b32 s48, s14, 1
Ta 20,12,57344
G 169,225
Mvav 124,24,212
Ta 22,12,58368
G 170,226
Mvav 92,24,204
s_mul_i32 s48, s48, 5120
G 171,227
Mvav 92,28,236
s_lshr_b32 s51, s14, 1
G 172,228
Mvav 60,28,228
Mvav 60,24,196
s_waitcnt vmcnt(14) lgkmcnt(8)
s_and_b32 s94, s14, 1
G 173,229
Mvaa 32,0,36
Ta 24,13,49152
Ta 26,13,50176
Mvaa 32,4,52
s_lshl_b32 s95, s57, 1
buffer_load_dwordx4 v[196:199], v26, s[8:11], s93 offen offset:0 sc1
G 174,230
Mvaa 64,4,56
Ta 28,13,57344
G 175,231
Mvaa 64,0,40
s_add_u32 s94, s94, s95
Ta 30,13,58368
K 168,168,169
Mvaa 96,0,44
s_lshl_b32 s54, s94, 3
K 169,170,171
Mvaa 96,4,184
buffer_load_dwordx4 v[204:207], v27, s[8:11], s28 offen offset:0 sc1
K 170,172,173
Mvaa 128,4,188
s_add_u32 s54, s54, s59
K 171,174,175
Mvaa 128,0,48
W 8
Ta 0,14,49152
Ta 2,14,50176
Mvaa 132,8,48
)"
R"(s_lshl_b32 s54, s54, 6
v_accvgpr_write_b32 a224, v168
Mvaa 132,12,188
s_xor_b32 s55, s54, 15872
buffer_load_dwordx4 v[212:215], v27, s[8:11], s93 offen offset:0 sc1
v_accvgpr_write_b32 a225, v169
Mvaa 100,12,184
Ta 4,14,57344
v_accvgpr_write_b32 a226, v170
Mvaa 100,8,44
s_add_u32 s54, s54, 20480
Ta 6,14,58368
v_accvgpr_write_b32 a227, v171
Mvaa 68,8,40
s_and_b32 s81, s14, 1
G 176,96
Mvaa 68,12,56
s_add_u32 s55, s55, 20480
Mvaa 36,12,52
G 177,97
Mvaa 36,8,36
W 8
s_and_b32 s95, s14, 2
G 178,98
Mvaa 40,16,36
s_mul_i32 s91, s81, 17164288
Ta 8,15,49152
Ta 10,15,50176
Mvaa 40,20,52
s_lshl_b32 s94, s94, 2
G 179,99
G 180,100
Mvaa 72,20,56
s_mul_i32 s82, s56, 536576
s_add_u32 s94, s94, s95
Ta 12,15,57344
Mvaa 72,16,40
Ta 14,15,58368
G 181,101
Mvaa 104,16,44
s_lshl_b32 s95, s95, 4
s_add_u32 s91, s91, s82
G 182,102
Mvaa 104,20,184
G 183,103
Mvaa 136,20,188
s_add_u32 s55, s55, s95
s_add_u32 s91, s91, 4194304
K 176,176,177
Mvaa 136,16,48
W 8
s_lshl_b32 s49, s94, 17
Ta 16,24,49152
Mvaa 140,24,48
Ta 18,24,50176
K 177,178,179
Mvaa 140,28,188
s_lshl_b32 s95, s59, 14
s_lshr_b32 s82, s14, 1
K 178,180,181
Mvaa 108,28,184
Ta 20,24,57344
K 179,182,183
Mvaa 108,24,44
s_add_u32 s49, s49, s95
Ta 22,24,58368
v_accvgpr_write_b32 a228, v176
Mvaa 76,24,40
s_lshl_b32 s83, s82, 14
s_xor_b32 s50, s49, 16252928
Mvaa 76,28,56
v_accvgpr_write_b32 a229, v177
Mvaa 44,28,52
s_add_u32 s91, s91, s83
s_add_u32 s49, s49, 39845888
v_accvgpr_write_b32 a230, v178
Mvaa 44,24,36
W 8
Ta 24,25,49152
v_accvgpr_write_b32 a231, v179
Mvaa 48,0,36
s_add_u32 s50, s50, 39845888
Ta 26,25,50176
G 168,232
Mvaa 48,4,52
s_lshl_b32 s81, s81, 14
Mvaa 80,4,56
s_lshr_b32 s94, s15, 3
Ta 28,25,57344
G 169,233
Mvaa 80,0,40
s_lshl_b32 s82, s82, 16
Ta 30,25,58368
G 170,234
Mvaa 112,0,44
s_lshl_b32 s94, s94, 3
G 171,235
Mvaa 112,4,184
s_add_u32 s94, s94, s14
s_or_b32 s92, s81, s82
G 172,236
Mvaa 144,4,188
Mvaa 144,0,48
W 8
s_lshl_b32 s94, s94, 4
G 173,237
Mvaa 148,8,48
s_xor_b32 s92, s92, 0x10000
Ta 0,2,32768
Ta 2,2,33792
Mvaa 148,12,188
s_add_u32 s94, s94, s59
G 174,238
G 175,239
Mvaa 116,12,184
s_mul_i32 s52, s94, 5120
Ta 4,2,40960
Ta 6,2,41984
Mvaa 116,8,44
K 168,168,169
Mvaa 84,8,40
s_lshl_b32 s95, s59, 12
K 169,170,171
Mvaa 84,12,56
K 170,172,173
Mvaa 52,12,52
s_sub_u32 s52, s52, s95
K 171,174,175
Mvaa 52,8,36
W 8
s_add_u32 s52, s52, 56623104
Ta 8,3,32768
Mvaa 56,16,36
Ta 10,3,33792
v_accvgpr_write_b32 a232, v168
Mvaa 56,20,52
s_lshl_b32 s53, s94, 3
v_accvgpr_write_b32 a233, v169
Mvaa 88,20,56
Ta 12,3,40960
v_accvgpr_write_b32 a234, v170
Mvaa 88,16,40
s_add_u32 s53, s53, 65536
Ta 14,3,41984
v_accvgpr_write_b32 a235, v171
Mvaa 120,16,44
G 176,104
v_bfe_u32 v244, v1, 4, 1
Mvaa 120,20,184
Mvaa 152,20,188
G 177,105
v_mul_u32_u24 v244, 0xaaaa, v244
Mvaa 152,16,48
W 8
G 178,106
v_add_u32 v244, 0x4444, v244
Mvaa 156,24,48
Ta 16,4,32768
Ta 18,4,33792
Mvaa 156,28,188
G 179,107
v_lshl_or_b32 v240, v244, 16, v244
Mvaa 124,28,184
G 180,108
v_mov_b32 v241, 0xff800000
Mvaa 124,24,44
Ta 20,4,40960
Ta 22,4,41984
Mvaa 92,24,40
G 181,109
v_add_u32 v242, 0x10000, v1
Mvaa 92,28,56
G 182,110
v_bfe_u32 v244, v1, 4, 4
Mvaa 60,28,52
G 183,111
v_lshrrev_b32 v245, 8, v1
Mvaa 60,24,36
s_waitcnt vmcnt(8) lgkmcnt(8)
K 176,176,177
v_lshlrev_b32 v245, 2, v245
Mvaa 32,0,60
Ta 24,5,32768
Ta 26,5,33792
Mvaa 32,4,132
K 177,178,179
v_sub_u32 v244, v244, v245
Mvaa 64,4,136
K 178,180,181
v_cmp_le_i32_e64 s[72:73], 0, v244
Mvav 64,0,184
Ta 28,5,40960
K 179,182,183
Mvav 96,0,192
Ta 30,5,41984
v_accvgpr_write_b32 a236, v176
Mvaa 96,4,140
v_cmp_le_i32_e64 s[74:75], 1, v244
v_accvgpr_write_b32 a237, v177
Mvaa 128,4,144
v_cmp_le_i32_e64 s[76:77], 2, v244
v_accvgpr_write_b32 a238, v178
Mvaa 128,0,128
W 8
v_cmp_le_i32_e64 s[78:79], 3, v244
v_accvgpr_write_b32 a239, v179
Mvaa 132,8,128
Ta 0,6,32768
Ta 2,6,33792
Mvaa 132,12,144
v_add_u32 v243, 4096, v1
G 168,240
Mvaa 100,12,140
v_add_u32 v244, 8192, v1
G 169,241
Mvav 100,8,192
Ta 4,6,40960
v_add_u32 v245, 12288, v1
Mvav 68,8,184
s_mov_b32 s18, 0x3f803f80
Ta 6,6,41984
G 170,242
Mvaa 68,12,136
s_mov_b32 s19, 0x3f003f00
G 171,243
G 172,244
Mvaa 36,12,132
Mvaa 36,8,60
W 8
v_accvgpr_write_b32 a176, s18
G 173,245
Mvaa 40,16,60
Ta 8,7,32768
Ta 10,7,33792
Mvaa 40,20,132
v_accvgpr_write_b32 a180, s19
G 174,246
Mvaa 72,20,136
G 175,247
v_mov_b32 v252, v241
Mvav 72,16,184
Ta 12,7,40960
Ta 14,7,41984
Mvav 104,16,192
K 168,168,169
v_accvgpr_write_b32 a177, s18
Mvaa 104,20,140
K 169,170,171
v_accvgpr_write_b32 a181, s19
Mvaa 136,20,144
K 170,172,173
v_mov_b32 v253, v241
Mvaa 136,16,128
W 8
K 171,174,175
v_accvgpr_write_b32 a178, s18
Mvaa 140,24,128
Ta 16,8,32768
Ta 18,8,33792
Mvaa 140,28,144
v_accvgpr_write_b32 a240, v168
v_accvgpr_write_b32 a182, s19
Mvaa 108,28,140
v_accvgpr_write_b32 a241, v169
v_mov_b32 v254, v241
Mvav 108,24,192
Ta 20,8,40960
v_accvgpr_write_b32 a242, v170
Mvav 76,24,184
Ta 22,8,41984
v_accvgpr_write_b32 a179, s18
Mvaa 76,28,136
v_accvgpr_write_b32 a243, v171
v_accvgpr_write_b32 a183, s19
Mvaa 44,28,132
G 176,112
v_mov_b32 v255, v241
Mvaa 44,24,60
W 8
G 177,113
G 178,114
Mvaa 48,0,60
Ta 24,9,32768
v_accvgpr_write_b32 a160, 0
Mvaa 48,4,132
Ta 26,9,33792
G 179,115
Mvaa 80,4,136
v_accvgpr_write_b32 a161, 0
G 180,116
Mvav 80,0,184
Ta 28,9,40960
v_accvgpr_write_b32 a162, 0
Mvav 112,0,192
Ta 30,9,41984
G 181,117
Mvaa 112,4,140
v_accvgpr_write_b32 a163, 0
G 182,118
Mvaa 144,4,144
v_accvgpr_write_b32 a164, 0
G 183,119
Mvaa 144,0,128
W 8
v_accvgpr_write_b32 a165, 0
K 176,176,177
Mvaa 148,8,128
Ta 0,2,49152
v_accvgpr_write_b32 a166, 0
Mvaa 148,12,144
Ta 2,2,50176
K 177,178,179
Mvaa 116,12,140
v_accvgpr_write_b32 a167, 0
K 178,180,181
Mvav 116,8,192
v_accvgpr_write_b32 a168, 0
K 179,182,183
Mvav 84,8,184
Ta 4,2,57344
Ta 6,2,58368
Mvaa 84,12,136
v_accvgpr_write_b32 a169, 0
v_accvgpr_write_b32 a244, v176
Mvaa 52,12,132
v_accvgpr_write_b32 a245, v177
v_accvgpr_write_b32 a170, 0
Mvaa 52,8,60
W 8
v_accvgpr_write_b32 a246, v178
v_accvgpr_write_b32 a171, 0
Mvaa 56,16,60
Ta 8,3,49152
v_accvgpr_write_b32 a247, v179
Mvaa 56,20,132
Ta 10,3,50176
v_accvgpr_write_b32 a172, 0
Mvaa 88,20,136
G 168,248
v_accvgpr_write_b32 a173, 0
Mvav 88,16,184
G 169,249
v_accvgpr_write_b32 a174, 0
Mvav 120,16,192
Ta 12,3,57344
Ta 14,3,58368
Mvaa 120,20,140
G 170,250
v_accvgpr_write_b32 a175, 0
Mvaa 152,20,144
s_mov_b32 s90, 0x5d800000
G 171,251
G 172,252
Mvaa 152,16,128
W 8
s_mov_b64 s[88:89], 0
G 173,253
Mvaa 156,24,128
s_mov_b32 s64, 0
Ta 16,4,49152
Ta 18,4,50176
Mvaa 156,28,144
s_and_b32 s18, s14, 1
G 174,254
G 175,255
Mvaa 124,28,140
s_lshl_b32 s62, s18, 14
Ta 20,4,57344
Ta 22,4,58368
Mvav 124,24,192
K 168,168,169
Mvav 92,24,184
s_lshl_b32 s20, s57, 15
K 169,170,171
Mvaa 92,28,136
s_sub_u32 s20, 507904, s20
K 170,172,173
Mvaa 60,28,132
K 171,174,175
Mvaa 60,24,60
s_waitcnt vmcnt(0) lgkmcnt(8)
s_add_u32 s20, s20, s62
Ta 24,5,49152
Mvav 32,0,200
s_lshl_b32 s65, s57, 1
Ta 26,5,50176
v_accvgpr_write_b32 a248, v168
Mvav 32,4,188
s_sub_u32 s65, s65, 1
v_accvgpr_write_b32 a249, v169
Mvav 64,4,196
Ta 28,5,57344
v_accvgpr_write_b32 a250, v170
Mvav 64,0,208
s_max_i32 s65, s65, 0
Ta 30,5,58368
v_accvgpr_write_b32 a251, v171
Mvav 96,0,216
s_sub_u32 s66, s60, 1
G 176,120
)"
R"(Mvav 96,4,204
s_cmp_lt_u32 s14, 2
s_cselect_b32 s20, s20, 0
s_cselect_b32 s66, s66, 15
s_cselect_b32 s21, 32768, 0
Mvav 128,4,212
G 177,121
Mvav 128,0,224
W 8
s_add_u32 s61, s91, s20
G 178,122
Mvav 132,8,224
s_add_u32 s62, s62, s21
Ta 0,6,49152
Ta 2,6,50176
Mvav 132,12,212
G 179,123
G 180,124
Mvav 100,12,204
s_sub_u32 s63, 16384, s21
Ta 4,6,57344
Ta 6,6,58368
Mvav 100,8,216
s_lshl_b32 s67, s60, 7
G 181,125
Mvav 68,8,208
s_add_u32 s67, s67, s56
G 182,126
Mvav 68,12,196
G 183,127
Mvav 36,12,188
s_lshl_b32 s67, s67, 12
K 176,176,177
Mvav 36,8,200
W 8
s_add_u32 s67, s67, 39845888
Ta 8,7,49152
Mvav 40,16,200
s_lshl_b32 s69, s60, 3
Ta 10,7,50176
K 177,178,179
Mvav 40,20,188
K 178,180,181
Mvav 72,20,196
s_add_u32 s69, s69, s59
Ta 12,7,57344
K 179,182,183
Mvav 72,16,208
s_lshl_b32 s69, s69, 6
Ta 14,7,58368
v_accvgpr_write_b32 a252, v176
Mvav 104,16,216
Mvav 104,20,204
s_lshl_b32 s21, s58, 3
v_accvgpr_write_b32 a253, v177
Mvav 136,20,212
s_add_u32 s69, s69, s21
v_accvgpr_write_b32 a254, v178
Mvav 136,16,224
W 8
s_add_u32 s69, s69, 20480
v_accvgpr_write_b32 a255, v179
Mvav 140,24,224
Ta 16,8,49152
Ta 18,8,50176
Mvav 140,28,212
s_mov_b32 s70, 0
Mvav 108,28,204
s_mov_b32 s37, 0
Ta 20,8,57344
Ta 22,8,58368
Mvav 108,24,216
s_sub_u32 s18, s57, 4
Mvav 76,24,208
Mvav 76,28,196
s_lshl_b32 s18, s18, 2
Mvav 44,28,188
s_add_u32 s18, s18, s58
Mvav 44,24,200
W 8
Ta 24,9,49152
Ta 26,9,50176
Mvav 48,0,200
s_mul_i32 s85, s59, 7
Mvav 48,4,188
s_add_u32 s85, s85, s18
Mvav 80,4,196
s_cmp_lt_u32 s18, 7
s_cselect_b32 s85, s85, 99
Ta 28,9,57344
Ta 30,9,58368
Mvav 80,0,208
Mvav 112,0,216
s_cmp_lt_u32 s85, 54
s_cselect_b32 s65, 2, s65
Mvav 112,4,204
s_mov_b32 s68, 0
Mvav 144,4,212
s_mov_b64 s[24:25], -1
Mvav 144,0,224
W 8
Mvav 148,8,224
s_mov_b32 s22, 0x3e0293ee
Mvav 148,12,212
s_mov_b32 s23, 0x3e0293ee
Mvav 116,12,204
Mvav 116,8,216
Mvav 84,8,208
Mvav 84,12,196
Mvav 52,12,188
Mvav 52,8,200
W 4
Mvav 56,16,200
Mvav 56,20,188
Mvav 88,20,196
Mvav 88,16,208
Mvav 120,16,216
W 0
s_barrier
Ra 128,242,0
Mvav 120,20,204
Mvav 152,20,212
Ra 132,242,1024
Mvav 152,16,224
Mvav 156,24,224
Ra 136,242,2048
Mvav 156,28,212
Mvav 124,28,204
Ra 140,242,3072
Mvav 124,24,216
Mvav 92,24,208
Mvav 92,28,196
Mvav 60,28,188
Mvav 60,24,200
.Lago%=:
s_cmp_eq_u32 s57, 0
s_cbranch_scc0 .LL_46%=
s_add_u32 m0, s62, 0
s_nop 0
Dc 1,8,61,0
Dc 1,8,61,1024
Dc 1,8,61,2048
Dc 1,8,61,3072
s_add_u32 m0, s62, 4096
s_nop 0
Dc 243,8,61,0
Dc 243,8,61,1024
Dc 243,8,61,2048
Dc 243,8,61,3072
s_add_u32 m0, s62, 8192
s_nop 0
Dc 244,8,61,0
Dc 244,8,61,1024
Dc 244,8,61,2048
Dc 244,8,61,3072
s_add_u32 m0, s62, 12288
s_nop 0
Dc 245,8,61,0
Dc 245,8,61,1024
Dc 245,8,61,2048
Dc 245,8,61,3072
s_add_u32 s61, s61, s63
.LL_46%=:
s_cmp_eq_u32 s60, 0
s_cbranch_scc1 .Lpd_30%=
v_pk_mul_f32 v[32:33], v[32:33], s[22:23]
v_pk_mul_f32 v[34:35], v[34:35], s[22:23]
v_pk_mul_f32 v[36:37], v[36:37], s[22:23]
v_pk_mul_f32 v[38:39], v[38:39], s[22:23]
v_pk_mul_f32 v[64:65], v[64:65], s[22:23]
v_pk_mul_f32 v[66:67], v[66:67], s[22:23]
v_pk_mul_f32 v[68:69], v[68:69], s[22:23]
v_pk_mul_f32 v[70:71], v[70:71], s[22:23]
v_pk_mul_f32 v[96:97], v[96:97], s[22:23]
v_pk_mul_f32 v[98:99], v[98:99], s[22:23]
v_pk_mul_f32 v[100:101], v[100:101], s[22:23]
v_pk_mul_f32 v[102:103], v[102:103], s[22:23]
v_pk_mul_f32 v[128:129], v[128:129], s[22:23]
v_pk_mul_f32 v[130:131], v[130:131], s[22:23]
v_pk_mul_f32 v[132:133], v[132:133], s[22:23]
v_pk_mul_f32 v[134:135], v[134:135], s[22:23]
v_pk_mul_f32 v[40:41], v[40:41], s[22:23]
v_pk_mul_f32 v[42:43], v[42:43], s[22:23]
v_pk_mul_f32 v[44:45], v[44:45], s[22:23]
v_pk_mul_f32 v[46:47], v[46:47], s[22:23]
v_pk_mul_f32 v[72:73], v[72:73], s[22:23]
v_pk_mul_f32 v[74:75], v[74:75], s[22:23]
v_pk_mul_f32 v[76:77], v[76:77], s[22:23]
v_pk_mul_f32 v[78:79], v[78:79], s[22:23]
v_pk_mul_f32 v[104:105], v[104:105], s[22:23]
v_pk_mul_f32 v[106:107], v[106:107], s[22:23]
v_pk_mul_f32 v[108:109], v[108:109], s[22:23]
v_pk_mul_f32 v[110:111], v[110:111], s[22:23]
v_pk_mul_f32 v[136:137], v[136:137], s[22:23]
v_pk_mul_f32 v[138:139], v[138:139], s[22:23]
v_pk_mul_f32 v[140:141], v[140:141], s[22:23]
v_pk_mul_f32 v[142:143], v[142:143], s[22:23]
v_pk_mul_f32 v[48:49], v[48:49], s[22:23]
v_pk_mul_f32 v[50:51], v[50:51], s[22:23]
v_pk_mul_f32 v[52:53], v[52:53], s[22:23]
v_pk_mul_f32 v[54:55], v[54:55], s[22:23]
v_pk_mul_f32 v[80:81], v[80:81], s[22:23]
v_pk_mul_f32 v[82:83], v[82:83], s[22:23]
v_pk_mul_f32 v[84:85], v[84:85], s[22:23]
v_pk_mul_f32 v[86:87], v[86:87], s[22:23]
v_pk_mul_f32 v[112:113], v[112:113], s[22:23]
v_pk_mul_f32 v[114:115], v[114:115], s[22:23]
v_pk_mul_f32 v[116:117], v[116:117], s[22:23]
v_pk_mul_f32 v[118:119], v[118:119], s[22:23]
v_pk_mul_f32 v[144:145], v[144:145], s[22:23]
v_pk_mul_f32 v[146:147], v[146:147], s[22:23]
v_pk_mul_f32 v[148:149], v[148:149], s[22:23]
v_pk_mul_f32 v[150:151], v[150:151], s[22:23]
v_pk_mul_f32 v[56:57], v[56:57], s[22:23]
v_pk_mul_f32 v[58:59], v[58:59], s[22:23]
v_pk_mul_f32 v[60:61], v[60:61], s[22:23]
v_pk_mul_f32 v[62:63], v[62:63], s[22:23]
v_pk_mul_f32 v[88:89], v[88:89], s[22:23]
v_pk_mul_f32 v[90:91], v[90:91], s[22:23]
v_pk_mul_f32 v[92:93], v[92:93], s[22:23]
v_pk_mul_f32 v[94:95], v[94:95], s[22:23]
v_pk_mul_f32 v[120:121], v[120:121], s[22:23]
v_pk_mul_f32 v[122:123], v[122:123], s[22:23]
v_pk_mul_f32 v[124:125], v[124:125], s[22:23]
v_pk_mul_f32 v[126:127], v[126:127], s[22:23]
v_pk_mul_f32 v[152:153], v[152:153], s[22:23]
v_pk_mul_f32 v[154:155], v[154:155], s[22:23]
v_pk_mul_f32 v[156:157], v[156:157], s[22:23]
v_pk_mul_f32 v[158:159], v[158:159], s[22:23]
K 32,32,33
K 33,34,35
K 34,36,37
K 35,38,39
K 40,40,41
K 41,42,43
K 42,44,45
K 43,46,47
W 0
Zvav 196,128,32
s_add_u32 s36, s29, 65536
Ra 144,242,4096
Ra 148,242,5120
Ra 152,242,6144
Ra 156,242,7168
Mvav 196,132,40
K 64,64,65
K 65,66,67
K 66,68,69
K 67,70,71
K 72,72,73
K 73,74,75
K 74,76,77
K 75,78,79
s_nop 1
Zvav 204,132,72
buffer_store_dwordx4 a[192:195], v1, s[8:11], s36 offen offset:0 sc1
K 96,96,97
K 97,98,99
K 98,100,101
Mvav 204,128,64
K 99,102,103
K 104,104,105
K 105,106,107
K 106,108,109
K 107,110,111
Zvav 212,128,96
buffer_store_dwordx4 a[200:203], v1, s[8:11], s36 offen offset:1024 sc1
K 128,128,129
K 129,130,131
K 130,132,133
Mvav 212,132,104
K 131,134,135
K 136,136,137
K 137,138,139
K 138,140,141
K 139,142,143
s_nop 1
Zvav 220,132,136
K 48,48,49
K 49,50,51
K 50,52,53
K 51,54,55
Mvav 220,128,128
K 56,56,57
K 57,58,59
K 58,60,61
K 59,62,63
Mvav 196,136,48
buffer_store_dwordx4 a[208:211], v1, s[8:11], s36 offen offset:2048 sc1
Ra 128,242,8192
Ra 132,242,9216
K 80,80,81
Mvav 196,140,56
K 81,82,83
K 82,84,85
K 83,86,87
K 88,88,89
K 89,90,91
K 90,92,93
K 91,94,95
s_nop 1
Mvav 204,140,88
buffer_store_dwordx4 a[216:219], v1, s[8:11], s36 offen offset:3072 sc1
s_add_u32 s36, s29, 69632
K 112,112,113
K 113,114,115
K 114,116,117
Mvav 204,136,80
K 115,118,119
K 120,120,121
K 121,122,123
K 122,124,125
K 123,126,127
Mvav 212,136,112
K 144,144,145
K 145,146,147
K 146,148,149
K 147,150,151
Mvav 212,140,120
K 152,152,153
K 153,154,155
K 154,156,157
K 155,158,159
s_nop 1
Mvav 220,140,152
buffer_store_dwordx4 a[224:227], v1, s[8:11], s36 offen offset:0 sc1
v_accvgpr_write_b32 a0, v32
v_accvgpr_write_b32 a1, v33
v_accvgpr_write_b32 a2, v34
Mvav 220,136,144
W 2
Ra 136,242,10240
v_accvgpr_write_b32 a3, v35
v_accvgpr_write_b32 a4, v40
Zvav 36,144,32
Ra 140,242,11264
buffer_store_dwordx4 a[232:235], v1, s[8:11], s36 offen offset:1024 sc1
v_accvgpr_write_b32 a5, v41
Mvav 36,148,40
v_accvgpr_write_b32 a6, v42
v_accvgpr_write_b32 a7, v43
v_max3_f32 v4, v196, v197, v198
v_max3_f32 v4, v4, v199, v252
Zvav 68,148,72
v_max3_f32 v5, v204, v205, v206
v_max3_f32 v5, v5, v207, v253
v_max3_f32 v6, v212, v213, v214
v_max3_f32 v6, v6, v215, v254
Mvav 68,144,64
v_max3_f32 v7, v220, v221, v222
v_max3_f32 v7, v7, v223, v255
v_mov_b32 v8, v4
v_mov_b32 v9, v5
Zvav 100,144,96
v_mov_b32 v10, v6
v_mov_b32 v11, v7
v_permlane32_swap_b32 v4, v8
v_accvgpr_write_b32 a8, v48
Mvav 100,148,104
v_permlane32_swap_b32 v5, v9
v_permlane32_swap_b32 v6, v10
v_permlane32_swap_b32 v7, v11
v_max_f32 v4, v4, v8
Zvav 132,148,136
v_max_f32 v5, v5, v9
v_max_f32 v6, v6, v10
v_max_f32 v7, v7, v11
v_mov_b32 v8, v4
Mvav 132,144,128
v_mov_b32 v9, v5
v_mov_b32 v10, v6
v_mov_b32 v11, v7
v_accvgpr_write_b32 a9, v49
Mvav 36,152,48
v_permlane16_swap_b32 v4, v8
v_permlane16_swap_b32 v5, v9
v_permlane16_swap_b32 v6, v10
v_permlane16_swap_b32 v7, v11
Mvav 36,156,56
v_max_f32 v4, v4, v8
v_max_f32 v5, v5, v9
v_max_f32 v6, v6, v10
v_max_f32 v7, v7, v11
Mvav 68,156,88
v_sub_f32 v4, 0, v4
v_sub_f32 v5, 0, v5
v_sub_f32 v6, 0, v6
v_sub_f32 v7, 0, v7
Mvav 68,152,80
v_mov_b32 v224, v4
v_mov_b32 v225, v4
v_mov_b32 v226, v4
v_mov_b32 v227, v4
Mvav 100,152,112
v_mov_b32 v228, v5
v_mov_b32 v229, v5
v_mov_b32 v230, v5
v_mov_b32 v231, v5
Mvav 100,156,120
v_mov_b32 v232, v6
)"
R"(v_mov_b32 v233, v6
v_mov_b32 v234, v6
v_mov_b32 v235, v6
Mvav 132,156,152
v_mov_b32 v236, v7
v_mov_b32 v237, v7
v_mov_b32 v238, v7
v_mov_b32 v239, v7
Mvav 132,152,144
W 0
buffer_store_dwordx4 a[240:243], v1, s[8:11], s36 offen offset:2048 sc1
v_accvgpr_write_b32 a10, v50
v_accvgpr_write_b32 a11, v51
v_accvgpr_write_b32 a12, v56
Cvav 160,128,32,224
s_and_b32 s80, s14, 1
v_accvgpr_write_b32 a13, v57
v_accvgpr_write_b32 a14, v58
v_accvgpr_write_b32 a15, v59
v_accvgpr_write_b32 a16, v64
Mvav 160,132,40
buffer_store_dwordx4 a[248:251], v1, s[8:11], s36 offen offset:3072 sc1
s_add_u32 s36, s29, 393216
Ra 144,242,12288
Ra 148,242,13312
v_accvgpr_write_b32 a17, v65
Cvav 164,132,72,228
s_lshl_b32 s80, s80, 14
v_accvgpr_write_b32 a18, v66
v_accvgpr_write_b32 a19, v67
v_accvgpr_write_b32 a20, v72
v_accvgpr_write_b32 a21, v73
Mvav 164,128,64
s_add_u32 s80, s80, 98304
buffer_store_dwordx4 a[196:199], v1, s[8:11], s36 offen offset:0 sc1
v_accvgpr_write_b32 a22, v74
v_accvgpr_write_b32 a23, v75
v_accvgpr_write_b32 a24, v80
Cvav 168,128,96,232
v_accvgpr_write_b32 a25, v81
v_accvgpr_write_b32 a26, v82
A 196,196,224
v_accvgpr_write_b32 a27, v83
Mvav 168,132,104
buffer_store_dwordx4 a[204:207], v1, s[8:11], s36 offen offset:1024 sc1
A 197,197,224
A 198,198,224
v_accvgpr_write_b32 a28, v88
Cvav 172,132,136,236
Ra 152,242,14336
Ra 156,242,15360
A 199,199,224
v_accvgpr_write_b32 a29, v89
Mvav 172,128,128
buffer_store_dwordx4 a[212:215], v1, s[8:11], s36 offen offset:2048 sc1
X 196
v_accvgpr_write_b32 a30, v90
Mvav 160,136,48
X 197
v_accvgpr_write_b32 a31, v91
v_accvgpr_write_b32 a32, v96
Mvav 160,140,56
X 198
K 192,196,197
v_accvgpr_write_b32 a33, v97
Mvav 164,140,88
X 199
A 204,204,228
v_accvgpr_write_b32 a34, v98
Mvav 164,136,80
buffer_store_dwordx4 a[220:223], v1, s[8:11], s36 offen offset:3072 sc1
s_add_u32 s36, s29, 397312
K 193,198,199
v_accvgpr_write_b32 a35, v99
A 205,205,228
Mvav 168,136,112
A 206,206,228
v_accvgpr_write_b32 a36, v104
v_add_u32 v8, s80, v1
A 207,207,228
Mvav 168,140,120
X 204
v_accvgpr_write_b32 a37, v105
v_accvgpr_write_b32 a38, v106
Mvav 172,140,152
X 205
X 206
Mvav 172,136,144
W 0
buffer_store_dwordx4 a[228:231], v1, s[8:11], s36 offen offset:0 sc1
v_accvgpr_write_b32 a39, v107
v_accvgpr_write_b32 a40, v112
K 200,204,205
Cvav 176,144,32,224
Ra 128,242,24576
X 207
v_accvgpr_write_b32 a41, v113
Mvav 176,148,40
Ra 132,242,25600
A 212,212,232
v_accvgpr_write_b32 a42, v114
K 201,206,207
Cvav 180,148,72,228
A 213,213,232
v_accvgpr_write_b32 a43, v115
A 214,214,232
A 215,215,232
Mvav 180,144,64
buffer_store_dwordx4 a[236:239], v1, s[8:11], s36 offen offset:1024 sc1
v_accvgpr_write_b32 a44, v120
X 212
Cvav 184,144,96,232
v_accvgpr_write_b32 a45, v121
X 213
v_accvgpr_write_b32 a46, v122
Mvav 184,148,104
X 214
v_accvgpr_write_b32 a47, v123
K 208,212,213
Cvav 188,148,136,236
buffer_store_dwordx4 a[244:247], v1, s[8:11], s36 offen offset:2048 sc1
v_accvgpr_write_b32 a48, v128
X 215
Mvav 188,144,128
v_accvgpr_write_b32 a49, v129
A 220,220,236
v_accvgpr_write_b32 a50, v130
K 209,214,215
Mvav 176,152,48
A 221,221,236
v_accvgpr_write_b32 a51, v131
A 222,222,236
A 223,223,236
Mvav 176,156,56
Ra 136,242,26624
v_accvgpr_write_b32 a52, v136
X 220
Mvav 180,156,88
Ra 140,242,27648
v_accvgpr_write_b32 a53, v137
X 221
Mvav 180,152,80
buffer_store_dwordx4 a[252:255], v1, s[8:11], s36 offen offset:3072 sc1
v_accvgpr_write_b32 a54, v138
X 222
Mvav 184,152,112
v_accvgpr_write_b32 a55, v139
v_accvgpr_write_b32 a56, v144
v_accvgpr_write_b32 a57, v145
v_accvgpr_write_b32 a58, v146
v_accvgpr_write_b32 a59, v147
v_accvgpr_write_b32 a60, v152
v_accvgpr_write_b32 a61, v153
v_accvgpr_write_b32 a62, v154
v_accvgpr_write_b32 a63, v155
Mvav 184,156,120
s_barrier
K 216,220,221
X 223
A 36,36,224
Mvav 188,156,152
K 217,222,223
A 37,37,224
A 38,38,224
A 39,39,224
X 36
X 37
X 38
K 194,36,37
X 39
A 68,68,228
K 195,38,39
A 69,69,228
A 70,70,228
A 71,71,228
X 68
X 69
X 70
K 202,68,69
X 71
A 100,100,232
K 203,70,71
A 101,101,232
A 102,102,232
A 103,103,232
X 100
X 101
X 102
K 210,100,101
X 103
A 132,132,236
K 211,102,103
A 133,133,236
A 134,134,236
A 135,135,236
X 132
X 133
X 134
K 218,132,133
X 135
s_nop 0
K 219,134,135
Mvav 188,152,144
W 0
Ra 144,242,28672
Ra 148,242,29696
Ra 152,242,30720
Zvav 32,128,192
Ra 156,242,31744
Zvav 64,128,200
Zvav 96,128,208
s_and_b32 s80, s14, 1
s_xor_b32 s62, s62, 0x10000
Zvav 128,128,216
s_lshl_b32 s80, s80, 14
s_add_u32 s80, s80, 131072
v_add_u32 v23, 61440, v21
Zvav 132,132,216
v_add_u32 v12, s80, v1
Ra 64,12,8192
Ra 68,12,9216
v_add_u32 v24, 61440, v22
Zvav 100,132,208
Ra 72,12,10240
Ra 76,12,11264
Ra 80,12,12288
v_add_u32 v25, 122880, v21
Zvav 68,132,200
Ra 84,12,13312
Ra 88,12,14336
Ra 92,12,15360
v_add_u32 v26, 122880, v22
Zvav 36,132,192
Ra 96,12,0
Ra 100,12,1024
Ra 104,12,2048
Zvav 40,136,192
Ra 108,12,3072
Ra 112,12,4096
X 172
Zvav 72,136,200
Ra 116,12,5120
Ra 120,12,6144
X 173
Zvav 104,136,208
Ra 124,12,7168
X 174
K 220,172,173
Zvav 136,136,216
X 175
Zvav 140,140,216
X 168
K 221,174,175
Zvav 108,140,208
X 169
X 170
Zvav 76,140,200
s_waitcnt vmcnt(0) lgkmcnt(0)
s_barrier
v_xor_b32 v242, 0x10000, v242
ds_read_b128 v[248:251], v8
Zvav 44,140,192
s_add_u32 s80, s71, 128
v_mov_b64 v[28:29], s[12:13]
s_mov_b64 exec, 1
buffer_store_dwordx2 v[28:29], off, s[8:11], s80 sc1
s_mov_b64 exec, -1
Ra 128,242,0
K 212,168,169
X 171
Zvav 48,144,192
X 164
K 213,170,171
Zvav 80,144,200
X 165
X 166
Zvav 112,144,208
Ra 132,242,1024
K 204,164,165
X 167
Zvav 144,144,216
X 160
Zvav 148,148,216
K 205,166,167
X 161
Zvav 116,148,208
Ra 136,242,2048
X 162
Zvav 84,148,200
W 3
K 196,160,161
X 163
Zvav 52,148,192
X 176
K 197,162,163
Zvav 56,152,192
Ra 140,242,3072
X 177
Zvav 88,152,200
X 178
K 198,176,177
Zvav 120,152,208
X 179
X 180
Zvav 152,152,216
K 199,178,179
Zvav 156,156,216
X 181
X 182
Zvav 124,156,208
K 206,180,181
X 183
Zvav 92,156,200
K 207,182,183
Zvav 60,156,192
.Ll0t_44%=:
s_cmp_eq_u32 s64, s65
s_cbranch_scc1 .Ll1t_33%=
.Ll0_31%=:
W 0
Cvaa 160,128,0,224
Ra 144,242,4096
Ra 148,242,5120
Mvaa 160,132,4
s_xor_b32 s62, s62, 0x10000
Ra 152,242,6144
Cvaa 164,132,20,228
X 184
Mvaa 164,128,16
X 185
Cvaa 168,128,32,232
X 186
Mvaa 168,132,36
Ra 156,242,7168
K 214,184,185
Cvaa 172,132,52,236
X 187
Mvaa 172,128,48
X 188
Mvaa 160,136,8
Ra 128,242,16384
Mvaa 160,140,12
K 215,186,187
Mvaa 164,140,28
X 189
Mvaa 164,136,24
X 190
Mvaa 168,136,40
Ra 132,242,17408
Mvaa 168,140,44
K 222,188,189
Mvaa 172,140,60
X 191
Mvaa 172,136,56
K 223,190,191
v_smfmac_f32_16x16x64_bf16 a[160:163], a[176:179], v[192:199], v240
Ra 136,242,18432
v_smfmac_f32_16x16x64_bf16 a[164:167], a[176:179], v[200:207], v240
X 160
v_smfmac_f32_16x16x64_bf16 a[168:171], a[176:179], v[208:215], v240
X 161
v_smfmac_f32_16x16x64_bf16 a[172:175], a[176:179], v[216:223], v240
W 3
X 162
Cvaa 176,144,0,224
Ra 140,242,19456
Mvaa 176,148,4
K 192,160,161
Cvaa 180,148,20,228
X 163
Mvaa 180,144,16
X 164
Cvaa 184,144,32,232
Mvaa 184,148,36
K 193,162,163
Cvaa 188,148,52,236
X 165
Mvaa 188,144,48
X 166
Mvaa 176,152,8
Ra 144,242,20480
Ra 148,242,21504
Mvaa 176,156,12
K 200,164,165
Mvaa 180,156,28
X 167
Mvaa 180,152,24
X 168
Mvaa 184,152,40
Mvaa 184,156,44
K 201,166,167
Mvaa 188,156,60
X 169
Mvaa 188,152,56
W 2
Ra 152,242,22528
Mvav 32,128,196
X 170
Mvav 64,128,204
Ra 156,242,23552
)"
R"(K 208,168,169
Mvav 96,128,212
X 171
Mvav 128,128,220
Mvav 132,132,220
X 172
Mvav 100,132,212
s_add_u32 s64, s64, 1
Ra 128,242,8192
K 209,170,171
Mvav 68,132,204
X 173
Mvav 36,132,196
Mvav 40,136,196
X 174
Mvav 72,136,204
Ra 132,242,9216
K 216,172,173
Mvav 104,136,212
X 175
Mvav 136,136,220
Mvav 140,140,220
X 176
Mvav 108,140,212
Ra 136,242,10240
K 217,174,175
Mvav 76,140,204
X 177
Mvav 44,140,196
W 3
Mvav 48,144,196
X 178
Mvav 80,144,204
Ra 140,242,11264
Mvav 112,144,212
K 194,176,177
Mvav 144,144,220
X 179
Mvav 148,148,220
X 180
Mvav 116,148,212
Ra 144,242,12288
Mvav 84,148,204
K 195,178,179
Mvav 52,148,196
X 181
Mvav 56,152,196
X 182
Mvav 88,152,204
Ra 148,242,13312
Mvav 120,152,212
K 202,180,181
Mvav 152,152,220
X 183
Mvav 156,156,220
X 184
Mvav 124,156,212
Ra 152,242,14336
Mvav 92,156,204
K 203,182,183
Mvav 60,156,196
W 3
X 185
Cvaa 160,128,0,224
X 186
Mvaa 160,132,4
Ra 156,242,15360
Cvaa 164,132,20,228
K 210,184,185
Mvaa 164,128,16
X 187
Cvaa 168,128,32,232
X 188
Mvaa 168,132,36
Cvaa 172,132,52,236
K 211,186,187
Mvaa 172,128,48
X 189
Mvaa 160,136,8
Ra 128,242,24576
Ra 132,242,25600
Mvaa 160,140,12
X 190
Mvaa 164,140,28
K 218,188,189
Mvaa 164,136,24
X 191
Mvaa 168,136,40
Mvaa 168,140,44
Mvaa 172,140,60
X 160
Mvaa 172,136,56
W 2
Ra 136,242,26624
K 219,190,191
Cvaa 176,144,0,224
X 161
Mvaa 176,148,4
Ra 140,242,27648
Cvaa 180,148,20,228
X 162
Mvaa 180,144,16
K 196,160,161
Cvaa 184,144,32,232
X 163
Mvaa 184,148,36
X 164
Cvaa 188,148,52,236
K 197,162,163
Mvaa 188,144,48
X 165
Mvaa 176,152,8
Ra 144,242,28672
Ra 148,242,29696
Mvaa 176,156,12
X 166
Mvaa 180,156,28
K 204,164,165
Mvaa 180,152,24
X 167
Mvaa 184,152,40
Mvaa 184,156,44
X 168
Mvaa 188,156,60
Mvaa 188,152,56
W 2
Ra 152,242,30720
K 205,166,167
Mvav 32,128,192
X 169
Mvav 64,128,200
Ra 156,242,31744
Mvav 96,128,208
X 170
Mvav 128,128,216
K 212,168,169
Mvav 132,132,216
X 171
Mvav 100,132,208
X 172
Mvav 68,132,200
Mvav 36,132,192
K 213,170,171
Mvav 40,136,192
X 173
Mvav 72,136,200
X 174
Mvav 104,136,208
Mvav 136,136,216
K 220,172,173
Mvav 140,140,216
X 175
Mvav 108,140,208
X 176
Mvav 76,140,200
W 0
s_barrier
v_xor_b32 v242, 0x10000, v242
Mvav 44,140,192
Ra 128,242,0
K 221,174,175
Mvav 48,144,192
X 177
Mvav 80,144,200
X 178
Mvav 112,144,208
Ra 132,242,1024
Mvav 144,144,216
K 198,176,177
Mvav 148,148,216
X 179
Mvav 116,148,208
X 180
Mvav 84,148,200
Ra 136,242,2048
Mvav 52,148,192
K 199,178,179
Mvav 56,152,192
X 181
Mvav 88,152,200
Ra 140,242,3072
Mvav 120,152,208
X 182
Mvav 152,152,216
K 206,180,181
Mvav 156,156,216
X 183
Mvav 124,156,208
Mvav 92,156,200
K 207,182,183
Mvav 60,156,192
s_cmp_lg_u32 s64, s65
s_cbranch_scc1 .Ll0_31%=
s_cmp_lt_u32 s85, 54
s_cbranch_scc1 .Lmoe_42%=
.Ll1t_33%=:
s_cmp_eq_u32 s64, s66
s_cbranch_scc1 .Lsp1_34%=
.Ll1_32%=:
W 0
Cvaa 160,128,0,224
Ra 144,242,4096
Ra 148,242,5120
Mvaa 160,132,4
s_add_u32 m0, s62, 0
Ra 152,242,6144
Cvaa 164,132,20,228
X 184
Mvaa 164,128,16
X 185
Cvaa 168,128,32,232
X 186
Mvaa 168,132,36
Ra 156,242,7168
K 214,184,185
Cvaa 172,132,52,236
Dc 1,8,61,0
Mvaa 172,128,48
X 187
Mvaa 160,136,8
X 188
Mvaa 160,140,12
Ra 128,242,16384
K 215,186,187
Mvaa 164,140,28
Dc 1,8,61,1024
Mvaa 164,136,24
X 189
Mvaa 168,136,40
X 190
Mvaa 168,140,44
Ra 132,242,17408
K 222,188,189
Mvaa 172,140,60
Dc 1,8,61,2048
Mvaa 172,136,56
X 191
v_smfmac_f32_16x16x64_bf16 a[160:163], a[176:179], v[192:199], v240
Ra 136,242,18432
K 223,190,191
v_smfmac_f32_16x16x64_bf16 a[164:167], a[176:179], v[200:207], v240
X 160
v_smfmac_f32_16x16x64_bf16 a[168:171], a[176:179], v[208:215], v240
Dc 1,8,61,3072
v_smfmac_f32_16x16x64_bf16 a[172:175], a[176:179], v[216:223], v240
W 3
X 161
Cvaa 176,144,0,224
s_add_u32 m0, s62, 4096
X 162
Mvaa 176,148,4
Ra 140,242,19456
K 192,160,161
Cvaa 180,148,20,228
X 163
Mvaa 180,144,16
Dc 243,8,61,0
Cvaa 184,144,32,232
X 164
Mvaa 184,148,36
K 193,162,163
Cvaa 188,148,52,236
X 165
Mvaa 188,144,48
Dc 243,8,61,1024
Ra 144,242,20480
Mvaa 176,152,8
X 166
Mvaa 176,156,12
Ra 148,242,21504
K 200,164,165
Mvaa 180,156,28
X 167
Mvaa 180,152,24
Dc 243,8,61,2048
Mvaa 184,152,40
X 168
Mvaa 184,156,44
K 201,166,167
Mvaa 188,156,60
X 169
Mvaa 188,152,56
W 2
Dc 243,8,61,3072
Ra 152,242,22528
Mvav 32,128,196
s_add_u32 m0, s62, 8192
X 170
Mvav 64,128,204
Ra 156,242,23552
K 208,168,169
Mvav 96,128,212
X 171
Mvav 128,128,220
Dc 244,8,61,0
Mvav 132,132,220
X 172
Mvav 100,132,212
Ra 128,242,8192
K 209,170,171
Mvav 68,132,204
X 173
Mvav 36,132,196
Dc 244,8,61,1024
Mvav 40,136,196
X 174
Mvav 72,136,204
Ra 132,242,9216
K 216,172,173
Mvav 104,136,212
X 175
Mvav 136,136,220
Dc 244,8,61,2048
Mvav 140,140,220
X 176
Mvav 108,140,212
Ra 136,242,10240
K 217,174,175
Mvav 76,140,204
X 177
Mvav 44,140,196
W 3
Dc 244,8,61,3072
Mvav 48,144,196
X 178
Mvav 80,144,204
s_add_u32 m0, s62, 12288
Ra 140,242,11264
Mvav 112,144,212
K 194,176,177
Mvav 144,144,220
X 179
Mvav 148,148,220
X 180
Mvav 116,148,212
Dc 245,8,61,0
Ra 144,242,12288
Mvav 84,148,204
K 195,178,179
Mvav 52,148,196
X 181
Mvav 56,152,196
X 182
Mvav 88,152,204
Dc 245,8,61,1024
Ra 148,242,13312
Mvav 120,152,212
K 202,180,181
Mvav 152,152,220
X 183
Mvav 156,156,220
X 184
Mvav 124,156,212
Dc 245,8,61,2048
Ra 152,242,14336
Mvav 92,156,204
K 203,182,183
Mvav 60,156,196
W 3
X 185
Cvaa 160,128,0,224
X 186
Mvaa 160,132,4
Dc 245,8,61,3072
Ra 156,242,15360
Cvaa 164,132,20,228
s_add_u32 s61, s61, s63
K 210,184,185
Mvaa 164,128,16
X 187
Cvaa 168,128,32,232
X 188
Mvaa 168,132,36
Cvaa 172,132,52,236
s_xor_b32 s62, s62, 0x10000
K 211,186,187
Mvaa 172,128,48
X 189
Mvaa 160,136,8
Ra 128,242,24576
Ra 132,242,25600
Mvaa 160,140,12
s_add_u32 s64, s64, 1
X 190
Mvaa 164,140,28
K 218,188,189
Mvaa 164,136,24
X 191
Mvaa 168,136,40
Mvaa 168,140,44
X 160
Mvaa 172,140,60
K 219,190,191
Mvaa 172,136,56
W 2
X 161
Cvaa 176,144,0,224
Ra 136,242,26624
Ra 140,242,27648
Mvaa 176,148,4
)"
R"(X 162
Cvaa 180,148,20,228
K 196,160,161
Mvaa 180,144,16
X 163
Cvaa 184,144,32,232
Mvaa 184,148,36
X 164
Cvaa 188,148,52,236
K 197,162,163
Mvaa 188,144,48
X 165
Mvaa 176,152,8
Ra 144,242,28672
Ra 148,242,29696
Mvaa 176,156,12
X 166
Mvaa 180,156,28
K 204,164,165
Mvaa 180,152,24
X 167
Mvaa 184,152,40
Mvaa 184,156,44
X 168
Mvaa 188,156,60
Mvaa 188,152,56
W 2
Ra 152,242,30720
K 205,166,167
Mvav 32,128,192
X 169
Mvav 64,128,200
Ra 156,242,31744
Mvav 96,128,208
X 170
Mvav 128,128,216
K 212,168,169
Mvav 132,132,216
X 171
Mvav 100,132,208
X 172
Mvav 68,132,200
Mvav 36,132,192
K 213,170,171
Mvav 40,136,192
X 173
Mvav 72,136,200
X 174
Mvav 104,136,208
Mvav 136,136,216
K 220,172,173
Mvav 140,140,216
X 175
Mvav 108,140,208
X 176
Mvav 76,140,200
s_waitcnt vmcnt(0) lgkmcnt(0)
s_barrier
v_xor_b32 v242, 0x10000, v242
Mvav 44,140,192
Ra 128,242,0
K 221,174,175
Mvav 48,144,192
X 177
Mvav 80,144,200
X 178
Mvav 112,144,208
Ra 132,242,1024
Mvav 144,144,216
K 198,176,177
Mvav 148,148,216
X 179
Mvav 116,148,208
X 180
Mvav 84,148,200
Ra 136,242,2048
Mvav 52,148,192
K 199,178,179
Mvav 56,152,192
X 181
Mvav 88,152,200
Ra 140,242,3072
Mvav 120,152,208
X 182
Mvav 152,152,216
K 206,180,181
Mvav 156,156,216
X 183
Mvav 124,156,208
Mvav 92,156,200
K 207,182,183
Mvav 60,156,192
s_cmp_lg_u32 s64, s66
s_cbranch_scc1 .Ll1_32%=
s_cmp_ge_u32 s14, 2
s_cbranch_scc1 .Lf1r_45%=
.Lsp1_34%=:
W 0
Cvaa 160,128,0,224
Ra 144,242,4096
Ra 148,242,5120
Mvaa 160,132,4
s_add_u32 m0, s62, 0
Ra 152,242,6144
Cvaa 164,132,20,228
X 184
Mvaa 164,128,16
X 185
Cvaa 168,128,32,232
X 186
Mvaa 168,132,36
Ra 156,242,7168
K 214,184,185
Cvaa 172,132,52,236
Dc 1,8,61,0
Mvaa 172,128,48
X 187
Mvaa 160,136,8
X 188
Mvaa 160,140,12
Ra 128,242,16384
K 215,186,187
Mvaa 164,140,28
Dc 1,8,61,1024
Ra 132,242,17408
Mvaa 164,136,24
X 189
Mvaa 168,136,40
X 190
Mvaa 168,140,44
Dc 1,8,61,2048
K 222,188,189
Mvaa 172,140,60
X 191
Mvaa 172,136,56
Ra 136,242,18432
K 223,190,191
v_smfmac_f32_16x16x64_bf16 a[160:163], a[176:179], v[192:199], v240
Dc 1,8,61,3072
Ra 140,242,19456
v_smfmac_f32_16x16x64_bf16 a[164:167], a[176:179], v[200:207], v240
s_add_u32 m0, s62, 4096
v_cndmask_b32_e64 v160, v241, v160, s[72:73]
v_cndmask_b32_e64 v161, v241, v161, s[74:75]
v_smfmac_f32_16x16x64_bf16 a[168:171], a[176:179], v[208:215], v240
v_cndmask_b32_e64 v162, v241, v162, s[76:77]
v_cndmask_b32_e64 v163, v241, v163, s[78:79]
v_smfmac_f32_16x16x64_bf16 a[172:175], a[176:179], v[216:223], v240
W 4
X 160
Cvaa 180,144,16,228
X 161
Mvaa 180,148,20
Dc 243,8,61,0
Cvaa 184,148,36,232
X 162
Mvaa 184,144,32
K 192,160,161
Cvaa 188,144,48,236
X 163
Mvaa 188,148,52
Dc 243,8,61,1024
Ra 144,242,20480
Mvaa 180,152,24
X 164
Mvaa 180,156,28
Ra 148,242,21504
K 193,162,163
Mvaa 184,156,44
Dc 243,8,61,2048
Mvaa 184,152,40
X 165
Mvaa 188,152,56
X 166
Mvaa 188,156,60
W 2
Dc 243,8,61,3072
K 200,164,165
Mvav 32,128,196
s_add_u32 m0, s62, 8192
X 167
Mvav 64,128,204
Ra 152,242,22528
Ra 156,242,23552
Mvav 96,128,212
X 168
Mvav 128,128,220
Dc 244,8,61,0
K 201,166,167
Mvav 132,132,220
X 169
Mvav 100,132,212
X 170
Mvav 68,132,204
Ra 128,242,8192
K 208,168,169
Mvav 36,132,196
X 171
Mvav 40,136,196
Dc 244,8,61,1024
Ra 132,242,9216
Mvav 72,136,204
X 172
Mvav 104,136,212
K 209,170,171
Mvav 136,136,220
X 173
Mvav 140,140,220
Dc 244,8,61,2048
Ra 136,242,10240
Mvav 108,140,212
X 174
Mvav 76,140,204
K 216,172,173
Mvav 44,140,196
W 3
Dc 244,8,61,3072
Ra 140,242,11264
Mvav 48,144,196
s_add_u32 m0, s62, 12288
X 175
Mvav 80,144,204
v_mov_b32 v194, 0
K 217,174,175
Mvav 112,144,212
v_mov_b32 v195, 0
v_cndmask_b32_e64 v180, v241, v180, s[72:73]
Mvav 144,144,220
v_cndmask_b32_e64 v181, v241, v181, s[74:75]
v_cndmask_b32_e64 v182, v241, v182, s[76:77]
Mvav 148,148,220
Dc 245,8,61,0
v_cndmask_b32_e64 v183, v241, v183, s[78:79]
Mvav 116,148,212
X 180
Mvav 84,148,204
Ra 144,242,12288
Mvav 52,148,196
X 181
Mvav 56,152,196
Dc 245,8,61,1024
Ra 148,242,13312
Mvav 88,152,204
X 182
Mvav 120,152,212
K 202,180,181
Mvav 152,152,220
X 183
Mvav 156,156,220
Dc 245,8,61,2048
Ra 152,242,14336
Mvav 124,156,212
X 184
Mvav 92,156,204
K 203,182,183
Mvav 60,156,196
W 3
Dc 245,8,61,3072
Ra 156,242,15360
Cvaa 168,128,32,232
s_add_u32 s61, s61, s63
X 185
Mvaa 168,132,36
s_xor_b32 s62, s62, 0x10000
X 186
Cvaa 172,132,52,236
K 210,184,185
Mvaa 172,128,48
s_add_u32 s64, s64, 1
X 187
Mvaa 168,136,40
X 188
Mvaa 168,140,44
Ra 128,242,24576
K 211,186,187
Mvaa 172,140,60
Ra 132,242,25600
Mvaa 172,136,56
Ra 136,242,26624
Ra 140,242,27648
X 189
X 190
K 218,188,189
X 191
v_mov_b32 v196, 0
K 219,190,191
W 4
Cvaa 188,144,48,236
Ra 144,242,28672
Mvaa 188,148,52
v_mov_b32 v197, 0
v_mov_b32 v204, 0
Mvaa 188,152,56
Ra 148,242,29696
v_mov_b32 v205, 0
Mvaa 188,156,60
W 2
Ra 152,242,30720
v_cndmask_b32_e64 v168, v241, v168, s[72:73]
Mvav 32,128,192
v_cndmask_b32_e64 v169, v241, v169, s[74:75]
v_cndmask_b32_e64 v170, v241, v170, s[76:77]
Mvav 64,128,200
Ra 156,242,31744
v_cndmask_b32_e64 v171, v241, v171, s[78:79]
Mvav 96,128,208
X 168
Mvav 128,128,216
X 169
Mvav 132,132,216
X 170
Mvav 100,132,208
v_mov_b32 v224, v250
Mvav 68,132,200
K 212,168,169
v_mov_b32 v225, v250
Mvav 36,132,192
X 171
Mvav 40,136,192
v_mov_b32 v226, v250
Mvav 72,136,200
X 172
Mvav 104,136,208
v_mov_b32 v227, v250
K 213,170,171
Mvav 136,136,216
X 173
Mvav 140,140,216
v_mov_b32 v228, v251
v_mov_b32 v229, v251
Mvav 108,140,208
X 174
Mvav 76,140,200
K 220,172,173
v_mov_b32 v230, v251
Mvav 44,140,192
W 0
X 175
Mvav 48,144,192
v_mov_b32 v231, v251
v_mov_b32 v198, 0
Mvav 80,144,200
K 221,174,175
v_mov_b32 v232, v248
Mvav 112,144,208
v_mov_b32 v199, 0
v_mov_b32 v206, 0
Mvav 144,144,216
v_mov_b32 v233, v248
v_mov_b32 v207, 0
Mvav 148,148,216
v_mov_b32 v214, 0
v_mov_b32 v234, v248
Mvav 116,148,208
v_mov_b32 v215, 0
v_cndmask_b32_e64 v188, v241, v188, s[72:73]
Mvav 84,148,200
v_mov_b32 v235, v248
v_cndmask_b32_e64 v189, v241, v189, s[74:75]
Mvav 52,148,192
v_cndmask_b32_e64 v190, v241, v190, s[76:77]
v_mov_b32 v236, v249
Mvav 56,152,192
v_cndmask_b32_e64 v191, v241, v191, s[78:79]
v_mov_b32 v237, v249
Mvav 88,152,200
X 188
Mvav 120,152,208
X 189
Mvav 152,152,216
v_mov_b32 v238, v249
v_mov_b32 v239, v249
Mvav 156,156,216
X 190
Mvav 124,156,208
s_waitcnt vmcnt(0)
s_barrier
v_xor_b32 v242, 0x10000, v242
Mvav 92,156,200
Ra 128,242,40960
K 222,188,189
Mvav 60,156,192
Ra 132,242,41984
Ra 136,242,43008
Ra 140,242,44032
X 191
s_nop 0
K 223,190,191
s_cmp_eq_u32 s64, 15
s_cbranch_scc1 .Lfs_40%=
.Lsp2_35%=:
v_smfmac_f32_16x16x64_bf16 a[160:163], a[176:179], v[192:199], v240
s_add_u32 m0, s62, 0
Ra 144,242,45056
Ra 148,242,46080
Ra 152,242,47104
Ra 156,242,48128
v_smfmac_f32_16x16x64_bf16 a[164:167], a[176:179], v[200:207], v240
v_smfmac_f32_16x16x64_bf16 a[168:171], a[176:179], v[208:215], v240
v_smfmac_f32_16x16x64_bf16 a[172:175], a[176:179], v[216:223], v240
)"
R"(W 4
Dc 1,8,61,0
Cvaa 168,128,96,232
v_mov_b32 v192, 0
G 12,160
Mvaa 168,132,100
Dc 1,8,61,1024
G 4,161
A 12,12,4
v_cmp_nlt_f32_e64 s[86:87], v12, s90
v_mov_b32 v193, 0
Cvaa 172,132,116,236
v_rcp_f32 v12, v12
s_or_b64 s[88:89], s[88:89], s[86:87]
v_accvgpr_write_b32 a160, 0
v_mov_b32 v200, 0
v_accvgpr_write_b32 a161, 0
v_accvgpr_write_b32 a162, 0
Mvaa 172,128,112
Dc 1,8,61,2048
v_accvgpr_write_b32 a163, 0
G 13,164
v_mov_b32 v201, 0
G 5,165
A 13,13,5
Mvaa 168,136,104
Ra 128,242,16384
Ra 132,242,17408
v_cmp_nlt_f32_e64 s[86:87], v13, s90
v_rcp_f32 v13, v13
s_or_b64 s[88:89], s[88:89], s[86:87]
v_accvgpr_write_b32 a164, 0
Mvaa 168,140,108
Dc 1,8,61,3072
v_accvgpr_write_b32 a165, 0
v_accvgpr_write_b32 a166, 0
v_accvgpr_write_b32 a167, 0
U 32,32,12
U 33,33,12
Mvaa 172,140,124
U 34,34,12
U 35,35,12
U 36,36,12
U 37,37,12
U 38,38,12
U 39,39,12
Mvaa 172,136,120
s_add_u32 m0, s62, 4096
Ra 136,242,18432
Ra 140,242,19456
K 32,32,33
K 33,34,35
K 34,36,37
W 4
Cvaa 188,144,112,236
K 35,38,39
s_add_u32 s93, s67, 0
buffer_store_dwordx4 v[32:35], v1, s[8:11], s93 offen offset:0 sc1
U 40,40,12
U 41,41,12
U 42,42,12
U 43,43,12
Mvaa 188,148,116
Ra 144,242,20480
v_cndmask_b32_e64 v168, v241, v168, s[72:73]
v_cndmask_b32_e64 v169, v241, v169, s[74:75]
v_cndmask_b32_e64 v170, v241, v170, s[76:77]
v_cndmask_b32_e64 v171, v241, v171, s[78:79]
U 44,44,12
Mvaa 188,152,120
X 168
X 169
U 45,45,12
U 46,46,12
Mvaa 188,156,124
W 1
Ra 148,242,21504
Dc 243,8,61,0
U 47,47,12
K 40,40,41
K 41,42,43
K 42,44,45
Mvav 96,128,212
Ra 152,242,22528
Ra 156,242,23552
K 43,46,47
X 170
K 208,168,169
Mvav 128,128,220
buffer_store_dwordx4 v[40:43], v1, s[8:11], s93 offen offset:1024 sc1
U 48,48,12
U 49,49,12
U 50,50,12
U 51,51,12
U 52,52,12
Mvav 132,132,220
U 53,53,12
U 54,54,12
U 55,55,12
K 48,48,49
K 49,50,51
K 50,52,53
Mvav 100,132,212
Dc 243,8,61,1024
Ra 128,242,32768
Ra 132,242,33792
X 171
K 51,54,55
Mvav 104,136,212
X 172
K 209,170,171
X 173
Mvav 136,136,220
buffer_store_dwordx4 v[48:51], v1, s[8:11], s93 offen offset:2048 sc1
U 56,56,12
U 57,57,12
U 58,58,12
U 59,59,12
U 60,60,12
Mvav 140,140,220
U 61,61,12
U 62,62,12
U 63,63,12
K 56,56,57
K 57,58,59
K 58,60,61
Mvav 108,140,212
W 2
Dc 243,8,61,2048
Ra 136,242,34816
Ra 140,242,35840
K 59,62,63
X 174
Mvav 112,144,212
K 216,172,173
X 175
Mvav 144,144,220
buffer_store_dwordx4 v[56:59], v1, s[8:11], s93 offen offset:3072 sc1
Ra 144,242,36864
G 14,168
G 6,169
A 14,14,6
v_cmp_nlt_f32_e64 s[86:87], v14, s90
Mvav 148,148,220
v_rcp_f32 v14, v14
s_or_b64 s[88:89], s[88:89], s[86:87]
v_accvgpr_write_b32 a168, 0
v_accvgpr_write_b32 a169, 0
v_accvgpr_write_b32 a170, 0
v_accvgpr_write_b32 a171, 0
Mvav 116,148,212
Dc 243,8,61,3072
s_add_u32 m0, s62, 8192
G 15,172
G 7,173
A 15,15,7
v_cmp_nlt_f32_e64 s[86:87], v15, s90
v_mov_b32 v194, 0
Mvav 120,152,212
v_rcp_f32 v15, v15
s_or_b64 s[88:89], s[88:89], s[86:87]
v_accvgpr_write_b32 a172, 0
v_accvgpr_write_b32 a173, 0
v_accvgpr_write_b32 a174, 0
v_accvgpr_write_b32 a175, 0
Mvav 152,152,220
Dc 244,8,61,0
U 64,64,13
U 65,65,13
U 66,66,13
U 67,67,13
U 68,68,13
Mvav 156,156,220
U 69,69,13
U 70,70,13
U 71,71,13
K 64,64,65
K 65,66,67
K 66,68,69
Mvav 124,156,212
W 1
Dc 244,8,61,1024
Ra 148,242,37888
K 67,70,71
s_add_u32 s93, s67, 131072
K 217,174,175
v_mov_b32 v195, 0
Cvaa 160,128,64,224
Ra 152,242,38912
Ra 156,242,39936
v_mov_b32 v202, 0
v_mov_b32 v203, 0
v_mov_b32 v210, 0
v_mov_b32 v211, 0
Mvaa 160,132,68
buffer_store_dwordx4 v[64:67], v1, s[8:11], s93 offen offset:0 sc1
U 72,72,13
U 73,73,13
U 74,74,13
U 75,75,13
U 76,76,13
Cvaa 164,132,84,228
U 77,77,13
U 78,78,13
U 79,79,13
K 72,72,73
K 73,74,75
K 74,76,77
Mvaa 164,128,80
Dc 244,8,61,2048
K 75,78,79
v_cndmask_b32_e64 v188, v241, v188, s[72:73]
v_cndmask_b32_e64 v189, v241, v189, s[74:75]
v_cndmask_b32_e64 v190, v241, v190, s[76:77]
v_cndmask_b32_e64 v191, v241, v191, s[78:79]
Cvaa 168,128,96,232
X 188
X 189
Mvaa 168,132,100
buffer_store_dwordx4 v[72:75], v1, s[8:11], s93 offen offset:1024 sc1
U 80,80,13
U 81,81,13
U 82,82,13
U 83,83,13
U 84,84,13
Cvaa 172,132,116,236
U 85,85,13
U 86,86,13
U 87,87,13
K 80,80,81
K 81,82,83
K 82,84,85
Mvaa 172,128,112
Dc 244,8,61,3072
s_add_u32 m0, s62, 12288
Ra 128,242,49152
Ra 132,242,50176
K 83,86,87
X 190
Mvaa 160,136,72
K 218,188,189
X 191
Mvaa 160,140,76
buffer_store_dwordx4 v[80:83], v1, s[8:11], s93 offen offset:2048 sc1
U 88,88,13
U 89,89,13
U 90,90,13
U 91,91,13
U 92,92,13
Mvaa 164,140,92
U 93,93,13
U 94,94,13
U 95,95,13
K 88,88,89
K 89,90,91
K 90,92,93
Mvaa 164,136,88
K 91,94,95
buffer_store_dwordx4 v[88:91], v1, s[8:11], s93 offen offset:3072 sc1
U 96,96,14
U 97,97,14
U 98,98,14
U 99,99,14
Mvaa 168,136,104
U 100,100,14
U 101,101,14
U 102,102,14
U 103,103,14
K 96,96,97
K 97,98,99
Mvaa 168,140,108
Dc 245,8,61,0
K 98,100,101
K 99,102,103
s_add_u32 s93, s67, 262144
v_cndmask_b32_e64 v160, v241, v160, s[72:73]
K 219,190,191
v_cndmask_b32_e64 v161, v241, v161, s[74:75]
Mvaa 172,140,124
v_cndmask_b32_e64 v162, v241, v162, s[76:77]
v_cndmask_b32_e64 v163, v241, v163, s[78:79]
X 160
Mvaa 172,136,120
W 2
Dc 245,8,61,1024
Ra 136,242,51200
Ra 140,242,52224
X 161
Cvaa 180,144,80,228
Mvaa 180,148,84
buffer_store_dwordx4 v[96:99], v1, s[8:11], s93 offen offset:0 sc1
U 104,104,14
U 105,105,14
U 106,106,14
U 107,107,14
U 108,108,14
Cvaa 184,148,100,232
U 109,109,14
U 110,110,14
U 111,111,14
K 104,104,105
K 105,106,107
K 106,108,109
Mvaa 184,144,96
Dc 245,8,61,2048
K 107,110,111
X 162
K 196,160,161
Cvaa 188,144,112,236
Ra 144,242,53248
X 163
Mvaa 188,148,116
buffer_store_dwordx4 v[104:107], v1, s[8:11], s93 offen offset:1024 sc1
U 112,112,14
U 113,113,14
U 114,114,14
U 115,115,14
U 116,116,14
Mvaa 180,152,88
U 117,117,14
U 118,118,14
U 119,119,14
K 112,112,113
K 113,114,115
K 114,116,117
Mvaa 180,156,92
Dc 245,8,61,3072
s_add_u32 s61, s61, s63
s_xor_b32 s62, s62, 0x10000
s_add_u32 s64, s64, 1
K 115,118,119
X 164
Mvaa 184,156,108
Ra 148,242,54272
K 197,162,163
X 165
Mvaa 184,152,104
buffer_store_dwordx4 v[112:115], v1, s[8:11], s93 offen offset:2048 sc1
U 120,120,14
U 121,121,14
U 122,122,14
U 123,123,14
U 124,124,14
Mvaa 188,152,120
U 125,125,14
U 126,126,14
U 127,127,14
K 120,120,121
K 121,122,123
K 122,124,125
Mvaa 188,156,124
W 2
K 123,126,127
buffer_store_dwordx4 v[120:123], v1, s[8:11], s93 offen offset:3072 sc1
U 128,128,15
U 129,129,15
U 130,130,15
U 131,131,15
Zvav 32,128,192
U 132,132,15
U 133,133,15
U 134,134,15
U 135,135,15
K 128,128,129
K 129,130,131
Zvav 64,128,200
K 130,132,133
K 131,134,135
s_add_u32 s93, s67, 393216
buffer_store_dwordx4 v[128:131], v1, s[8:11], s93 offen offset:0 sc1
U 136,136,15
U 137,137,15
U 138,138,15
Zvav 96,128,208
U 139,139,15
U 140,140,15
U 141,141,15
U 142,142,15
U 143,143,15
K 136,136,137
Zvav 128,128,216
K 137,138,139
K 138,140,141
K 139,142,143
buffer_store_dwordx4 v[136:139], v1, s[8:11], s93 offen offset:1024 sc1
U 144,144,15
U 145,145,15
Zvav 132,132,216
U 146,146,15
U 147,147,15
U 148,148,15
U 149,149,15
U 150,150,15
U 151,151,15
Zvav 100,132,208
K 144,144,145
K 145,146,147
K 146,148,149
K 147,150,151
buffer_store_dwordx4 v[144:147], v1, s[8:11], s93 offen offset:2048 sc1
)"
R"(U 152,152,15
Zvav 68,132,200
U 153,153,15
U 154,154,15
U 155,155,15
U 156,156,15
U 157,157,15
U 158,158,15
Zvav 36,132,192
U 159,159,15
K 152,152,153
K 153,154,155
K 154,156,157
K 155,158,159
buffer_store_dwordx4 v[152:155], v1, s[8:11], s93 offen offset:3072 sc1
Zvav 40,136,192
Ra 152,242,55296
X 166
K 204,164,165
X 167
Zvav 72,136,200
Ra 156,242,56320
X 168
K 205,166,167
X 169
Zvav 104,136,208
X 170
K 212,168,169
X 171
Zvav 136,136,216
X 172
K 213,170,171
X 173
Zvav 140,140,216
Zvav 108,140,208
X 174
Zvav 76,140,200
s_waitcnt vmcnt(6) lgkmcnt(0)
s_barrier
v_xor_b32 v242, 0x10000, v242
Zvav 44,140,192
Ra 128,242,40960
K 220,172,173
X 175
Zvav 48,144,192
Zvav 80,144,200
v_mov_b32 v198, 0
Zvav 112,144,208
Ra 132,242,41984
K 221,174,175
Zvav 144,144,216
v_mov_b32 v199, 0
v_cndmask_b32_e64 v180, v241, v180, s[72:73]
Zvav 148,148,216
v_cndmask_b32_e64 v181, v241, v181, s[74:75]
Zvav 116,148,208
Ra 136,242,43008
v_cndmask_b32_e64 v182, v241, v182, s[76:77]
Zvav 84,148,200
v_cndmask_b32_e64 v183, v241, v183, s[78:79]
Zvav 52,148,192
X 180
Zvav 56,152,192
Ra 140,242,44032
X 181
Zvav 88,152,200
Zvav 120,152,208
X 182
Zvav 152,152,216
K 206,180,181
Zvav 156,156,216
X 183
Zvav 124,156,208
Zvav 92,156,200
s_waitcnt vmcnt(0)
s_and_b64 s[86:87], s[88:89], s[24:25]
s_cbranch_scc1 .LL_47%=
v_mov_b64 v[28:29], s[12:13]
s_mov_b64 exec, 1
buffer_store_dwordx2 v[28:29], off, s[8:11], s69 sc1
s_mov_b64 exec, -1
.LL_47%=:
K 207,182,183
Zvav 60,156,192
s_cmp_eq_u32 s64, 15
s_cbranch_scc1 .Lf2_39%=
.Ll2_36%=:
W 0
Cvaa 160,128,64,224
Ra 144,242,45056
Ra 148,242,46080
Mvaa 160,132,68
s_add_u32 m0, s62, 0
Ra 152,242,47104
Cvaa 164,132,84,228
X 184
Mvaa 164,128,80
X 185
Cvaa 168,128,96,232
X 186
Mvaa 168,132,100
Ra 156,242,48128
K 214,184,185
Cvaa 172,132,116,236
Dc 1,8,61,0
Mvaa 172,128,112
X 187
Mvaa 160,136,72
X 188
Mvaa 160,140,76
Ra 128,242,57344
K 215,186,187
Mvaa 164,140,92
Dc 1,8,61,1024
Mvaa 164,136,88
X 189
Mvaa 168,136,104
X 190
Mvaa 168,140,108
Ra 132,242,58368
K 222,188,189
Mvaa 172,140,124
Dc 1,8,61,2048
Mvaa 172,136,120
X 191
v_smfmac_f32_16x16x64_bf16 a[160:163], a[176:179], v[192:199], v240
Ra 136,242,59392
K 223,190,191
v_smfmac_f32_16x16x64_bf16 a[164:167], a[176:179], v[200:207], v240
X 160
v_smfmac_f32_16x16x64_bf16 a[168:171], a[176:179], v[208:215], v240
Dc 1,8,61,3072
v_smfmac_f32_16x16x64_bf16 a[172:175], a[176:179], v[216:223], v240
W 3
X 161
Cvaa 176,144,64,224
s_add_u32 m0, s62, 4096
X 162
Mvaa 176,148,68
Ra 140,242,60416
K 192,160,161
Cvaa 180,148,84,228
X 163
Mvaa 180,144,80
Dc 243,8,61,0
Cvaa 184,144,96,232
X 164
Mvaa 184,148,100
K 193,162,163
Cvaa 188,148,116,236
X 165
Mvaa 188,144,112
Dc 243,8,61,1024
Ra 144,242,61440
Mvaa 176,152,72
X 166
Mvaa 176,156,76
Ra 148,242,62464
K 200,164,165
Mvaa 180,156,92
X 167
Mvaa 180,152,88
Dc 243,8,61,2048
Mvaa 184,152,104
X 168
Mvaa 184,156,108
K 201,166,167
Mvaa 188,156,124
X 169
Mvaa 188,152,120
W 2
Dc 243,8,61,3072
Ra 152,242,63488
Mvav 32,128,196
s_add_u32 m0, s62, 8192
X 170
Mvav 64,128,204
Ra 156,242,64512
K 208,168,169
Mvav 96,128,212
X 171
Mvav 128,128,220
Dc 244,8,61,0
Mvav 132,132,220
X 172
Mvav 100,132,212
Ra 128,242,32768
K 209,170,171
Mvav 68,132,204
X 173
Mvav 36,132,196
Dc 244,8,61,1024
Mvav 40,136,196
X 174
Mvav 72,136,204
Ra 132,242,33792
K 216,172,173
Mvav 104,136,212
X 175
Mvav 136,136,220
Dc 244,8,61,2048
Mvav 140,140,220
X 176
Mvav 108,140,212
Ra 136,242,34816
K 217,174,175
Mvav 76,140,204
X 177
Mvav 44,140,196
W 3
Dc 244,8,61,3072
Mvav 48,144,196
X 178
Mvav 80,144,204
s_add_u32 m0, s62, 12288
Ra 140,242,35840
Mvav 112,144,212
K 194,176,177
Mvav 144,144,220
X 179
Mvav 148,148,220
X 180
Mvav 116,148,212
Dc 245,8,61,0
Ra 144,242,36864
Mvav 84,148,204
K 195,178,179
Mvav 52,148,196
X 181
Mvav 56,152,196
X 182
Mvav 88,152,204
Dc 245,8,61,1024
Ra 148,242,37888
Mvav 120,152,212
K 202,180,181
Mvav 152,152,220
X 183
Mvav 156,156,220
X 184
Mvav 124,156,212
Dc 245,8,61,2048
Ra 152,242,38912
Mvav 92,156,204
K 203,182,183
Mvav 60,156,196
W 3
X 185
Cvaa 160,128,64,224
X 186
Mvaa 160,132,68
Dc 245,8,61,3072
Ra 156,242,39936
Cvaa 164,132,84,228
s_add_u32 s61, s61, s63
K 210,184,185
Mvaa 164,128,80
X 187
Cvaa 168,128,96,232
X 188
Mvaa 168,132,100
Cvaa 172,132,116,236
s_xor_b32 s62, s62, 0x10000
K 211,186,187
Mvaa 172,128,112
X 189
Mvaa 160,136,72
Ra 128,242,49152
Ra 132,242,50176
Mvaa 160,140,76
s_add_u32 s64, s64, 1
X 190
Mvaa 164,140,92
K 218,188,189
Mvaa 164,136,88
X 191
Mvaa 168,136,104
Mvaa 168,140,108
X 160
Mvaa 172,140,124
K 219,190,191
Mvaa 172,136,120
W 2
X 161
Cvaa 176,144,64,224
Ra 136,242,51200
Ra 140,242,52224
Mvaa 176,148,68
X 162
Cvaa 180,148,84,228
K 196,160,161
Mvaa 180,144,80
X 163
Cvaa 184,144,96,232
Mvaa 184,148,100
X 164
Cvaa 188,148,116,236
K 197,162,163
Mvaa 188,144,112
X 165
Mvaa 176,152,72
Ra 144,242,53248
Ra 148,242,54272
Mvaa 176,156,76
X 166
Mvaa 180,156,92
K 204,164,165
Mvaa 180,152,88
X 167
Mvaa 184,152,104
Mvaa 184,156,108
X 168
Mvaa 188,156,124
Mvaa 188,152,120
W 2
Ra 152,242,55296
K 205,166,167
Mvav 32,128,192
X 169
Mvav 64,128,200
Ra 156,242,56320
Mvav 96,128,208
X 170
Mvav 128,128,216
K 212,168,169
Mvav 132,132,216
X 171
Mvav 100,132,208
X 172
Mvav 68,132,200
Mvav 36,132,192
K 213,170,171
Mvav 40,136,192
X 173
Mvav 72,136,200
X 174
Mvav 104,136,208
Mvav 136,136,216
K 220,172,173
Mvav 140,140,216
X 175
Mvav 108,140,208
X 176
Mvav 76,140,200
s_waitcnt vmcnt(0) lgkmcnt(0)
s_barrier
v_xor_b32 v242, 0x10000, v242
Mvav 44,140,192
Ra 128,242,40960
K 221,174,175
Mvav 48,144,192
X 177
Mvav 80,144,200
X 178
Mvav 112,144,208
Ra 132,242,41984
Mvav 144,144,216
K 198,176,177
Mvav 148,148,216
X 179
Mvav 116,148,208
X 180
Mvav 84,148,200
Ra 136,242,43008
Mvav 52,148,192
K 199,178,179
Mvav 56,152,192
X 181
Mvav 88,152,200
Ra 140,242,44032
Mvav 120,152,208
X 182
Mvav 152,152,216
K 206,180,181
Mvav 156,156,216
X 183
Mvav 124,156,208
Mvav 92,156,200
K 207,182,183
Mvav 60,156,192
s_cmp_lg_u32 s64, 15
s_cbranch_scc1 .Ll2_36%=
.Lf2_39%=:
W 0
Cvaa 160,128,64,224
v_bfe_u32 v28, v1, 4, 2
v_lshlrev_b32 v28, 3, v28
Mvaa 160,132,68
Ra 144,242,45056
Ra 148,242,46080
Cvaa 164,132,84,228
Ra 152,242,47104
buffer_load_dwordx2 v[28:29], v28, s[8:11], s54 offen sc1
Mvaa 164,128,80
s_lshl_b32 s92, s58, 2
s_add_u32 s91, s92, s51
s_add_u32 s95, s91, 3
Cvaa 168,128,96,232
s_and_b32 s95, s95, 15
s_mul_i32 s95, s95, 40960
)"
R"(s_mul_i32 s71, s51, 10240
Mvaa 168,132,100
s_add_u32 s71, s71, 0
s_add_u32 m0, s71, s48
s_lshl2_add_u32 s94, s48, s95
Cvaa 172,132,116,236
X 184
Mvaa 172,128,112
X 185
Mvaa 160,136,72
X 186
Mvaa 160,140,76
Ra 156,242,48128
K 214,184,185
Mvaa 164,140,92
X 187
Mvaa 164,136,88
D 16,40,94,0
Ra 128,242,57344
Mvaa 168,136,104
s_add_u32 s95, s91, 5
s_and_b32 s95, s95, 15
s_mul_i32 s95, s95, 40960
Mvaa 168,140,108
s_mul_i32 s71, s51, 10240
s_add_u32 s71, s71, 20480
Ra 132,242,58368
Mvaa 172,140,124
X 188
Mvaa 172,136,120
D 17,40,94,1024
K 215,186,187
v_smfmac_f32_16x16x64_bf16 a[160:163], a[176:179], v[192:199], v240
X 189
v_smfmac_f32_16x16x64_bf16 a[164:167], a[176:179], v[200:207], v240
X 190
K 222,188,189
X 191
s_nop 0
K 223,190,191
v_smfmac_f32_16x16x64_bf16 a[168:171], a[176:179], v[208:215], v240
D 18,40,94,2048
Ra 136,242,59392
v_smfmac_f32_16x16x64_bf16 a[172:175], a[176:179], v[216:223], v240
W 3
X 160
Cvaa 176,144,64,224
X 161
Mvaa 176,148,68
X 162
Cvaa 180,148,84,228
D 19,40,94,3072
K 192,160,161
Mvaa 180,144,80
s_add_u32 m0, m0, 4096
X 163
Cvaa 184,144,96,232
X 164
Mvaa 184,148,100
Ra 140,242,60416
K 193,162,163
Cvaa 188,148,116,236
buffer_load_dwordx4 v20, s[40:43], s94 offen lds
Mvaa 188,144,112
s_add_u32 m0, s71, s48
s_lshl2_add_u32 s94, s48, s95
Ra 144,242,61440
Mvaa 176,152,72
X 165
Mvaa 176,156,76
X 166
Mvaa 180,156,92
D 16,40,94,0
K 200,164,165
Mvaa 180,152,88
X 167
Mvaa 184,152,104
X 172
Mvaa 184,156,108
D 17,40,94,1024
K 201,166,167
Mvaa 188,156,124
X 173
Mvaa 188,152,120
W 1
Ra 148,242,62464
Ra 152,242,63488
Mvav 32,128,196
X 174
Mvav 64,128,204
D 18,40,94,2048
K 216,172,173
Mvav 96,128,212
X 175
Mvav 128,128,220
X 168
Mvav 132,132,220
s_mov_b32 s18, 40960
D 19,40,94,3072
K 217,174,175
Mvav 100,132,212
s_add_u32 m0, m0, 4096
X 169
Mvav 68,132,204
s_mov_b32 s19, 73728
X 170
Mvav 36,132,196
s_bitcmp1_b32 s14, 0
s_cselect_b32 s18, 49152, s18
s_cselect_b32 s19, 98304, s19
Ra 156,242,64512
K 208,168,169
Mvav 40,136,196
s_cmp_lt_u32 s14, 2
s_cselect_b32 s20, s18, s19
s_cselect_b32 s21, s19, s18
X 171
Mvav 72,136,204
X 176
Mvav 104,136,212
buffer_load_dwordx4 v20, s[40:43], s94 offen lds
K 209,170,171
Mvav 136,136,220
s_waitcnt vmcnt(10)
v_cmp_eq_u64 vcc, s[12:13], v[28:29]
s_cmp_eq_u64 vcc, -1
s_cselect_b32 s46, 79691776, 0
s_add_u32 s81, s58, 0
s_and_b32 s81, s81, 3
Mvav 140,140,220
s_lshl_b32 s81, s81, 12
s_add_u32 s81, s81, s49
Ra 128,242,49152
Mvav 108,140,212
X 177
Mvav 76,140,204
X 178
Mvav 44,140,196
W 1
buffer_load_dwordx4 a[184:187], v1, s[44:47], s81 offen offset:0 sc1
K 194,176,177
Mvav 48,144,196
s_add_u32 s82, s81, 131072
X 179
Mvav 80,144,204
v_add_u32 v2, s20, v1
v_add_u32 v3, s21, v1
Mvav 112,144,212
s_lshl_b32 s18, s14, 9
X 180
Mvav 144,144,220
buffer_load_dwordx4 a[200:203], v1, s[44:47], s82 offen offset:0 sc1
K 195,178,179
Mvav 148,148,220
X 181
Mvav 116,148,212
Ra 132,242,50176
v_lshrrev_b32 v232, 1, v1
Mvav 84,148,204
X 182
Mvav 52,148,196
v_add_u32 v232, s18, v232
K 202,180,181
Mvav 56,152,196
buffer_load_dwordx4 a[188:191], v1, s[44:47], s81 offen offset:1024 sc1
v_bfe_u32 v12, v1, 6, 2
Mvav 88,152,204
X 183
Mvav 120,152,212
Ra 136,242,51200
v_lshrrev_b32 v13, 8, v1
Mvav 152,152,220
X 184
Mvav 156,156,220
v_bfe_u32 v14, v1, 4, 1
K 203,182,183
Mvav 124,156,212
buffer_load_dwordx4 a[204:207], v1, s[44:47], s82 offen offset:1024 sc1
v_lshlrev_b32 v14, 4, v14
Mvav 92,156,204
Ra 140,242,52224
v_mov_b32 v15, 0x3f80
Mvav 60,156,196
X 185
Maav 160,180,192
X 186
K 210,184,185
X 187
X 188
K 211,186,187
Maav 164,180,200
X 189
X 190
K 218,188,189
X 191
s_nop 0
K 219,190,191
Maav 168,180,208
v_lshlrev_b32 v15, v14, v15
v_bfe_u32 v14, v1, 5, 1
Maav 172,180,216
W 0
buffer_load_dwordx4 a[192:195], v1, s[44:47], s81 offen offset:2048 sc1
v_xor_b32 v27, v12, v13
Mvav 32,128,192
Ra 144,242,53248
v_lshl_or_b32 v27, v27, 1, v14
Mvav 64,128,200
v_cmp_eq_u32_e64 s[86:87], 0, v27
v_mov_b32 v226, 0
Mvav 96,128,208
buffer_load_dwordx4 a[208:211], v1, s[44:47], s82 offen offset:2048 sc1
v_cndmask_b32_e64 v224, 0, v15, s[86:87]
Mvav 128,128,216
Ra 148,242,54272
v_mov_b32 v228, 0
Mvav 132,132,216
v_mov_b32 v230, v224
v_cmp_eq_u32_e64 s[86:87], 1, v27
Mvav 100,132,208
buffer_load_dwordx4 a[196:199], v1, s[44:47], s81 offen offset:3072 sc1
v_mov_b32 v227, 0
Mvav 68,132,200
Ra 152,242,55296
v_cndmask_b32_e64 v225, 0, v15, s[86:87]
Mvav 36,132,192
v_mov_b32 v229, 0
v_mov_b32 v231, v225
Mvav 40,136,192
buffer_load_dwordx4 a[212:215], v1, s[44:47], s82 offen offset:3072 sc1
Ra 156,242,56320
Mvav 72,136,200
s_add_u32 s81, s58, 1
s_and_b32 s81, s81, 3
s_lshl_b32 s81, s81, 12
Mvav 104,136,208
s_add_u32 s81, s81, s49
G 4,160
G 8,161
Mvav 136,136,216
buffer_load_dwordx4 a[216:219], v1, s[44:47], s81 offen offset:0 sc1
G 5,164
Mvav 140,140,216
s_add_u32 s82, s81, 131072
G 9,165
G 6,168
Mvav 108,140,208
G 10,169
G 7,172
Mvav 76,140,200
buffer_load_dwordx4 a[232:235], v1, s[44:47], s82 offen offset:0 sc1
G 11,173
Mvav 44,140,192
W 0
A 4,4,8
A 5,5,9
Mvav 48,144,192
A 6,6,10
A 7,7,11
Mvav 80,144,200
ds_write_b64 v232, v[6:7] offset:57344
A 8,4,5
Mvav 112,144,208
A 9,6,7
A 8,8,9
Mvav 144,144,216
buffer_load_dwordx4 a[220:223], v1, s[44:47], s81 offen offset:1024 sc1
v_cmp_ngt_f32_e64 s[86:87], s90, v8
Mvav 148,148,216
s_lshl_b32 s19, s14, 2
v_mov_b32 v9, s19
Mvav 116,148,208
s_or_b64 s[88:89], s[88:89], s[86:87]
s_and_b64 s[88:89], s[88:89], s[24:25]
s_cselect_b32 s18, 1, 0
v_mov_b32 v8, s18
Mvav 84,148,200
ds_write_b32 v9, v8 offset:59392
v_xor_b32 v10, 1024, v232
Mvav 52,148,192
v_bfe_u32 v11, v1, 2, 4
K 96,96,97
Mvav 56,152,192
K 97,98,99
K 98,100,101
Mvav 88,152,200
buffer_load_dwordx4 a[236:239], v1, s[44:47], s82 offen offset:1024 sc1
K 99,102,103
Mvav 120,152,208
ds_write_b128 v2, v[96:99] offset:0
K 128,128,129
Mvav 152,152,216
K 129,130,131
K 130,132,133
Mvav 156,156,216
buffer_load_dwordx4 a[224:227], v1, s[44:47], s81 offen offset:2048 sc1
K 131,134,135
Mvav 124,156,208
ds_write_b128 v2, v[128:131] offset:4096
K 104,104,105
Mvav 92,156,200
K 105,106,107
K 106,108,109
Mvav 60,156,192
buffer_load_dwordx4 a[240:243], v1, s[44:47], s82 offen offset:2048 sc1
K 107,110,111
ds_write_b128 v2, v[104:107] offset:1024
K 136,136,137
K 137,138,139
K 138,140,141
K 139,142,143
ds_write_b128 v2, v[136:139] offset:5120
buffer_load_dwordx4 a[228:231], v1, s[44:47], s81 offen offset:3072 sc1
K 112,112,113
K 113,114,115
K 114,116,117
K 115,118,119
buffer_load_dwordx4 a[244:247], v1, s[44:47], s82 offen offset:3072 sc1
ds_write_b128 v2, v[112:115] offset:2048
K 144,144,145
K 145,146,147
K 146,148,149
K 147,150,151
ds_write_b128 v2, v[144:147] offset:6144
K 120,120,121
K 121,122,123
K 122,124,125
K 123,126,127
ds_write_b128 v2, v[120:123] offset:3072
K 152,152,153
K 153,154,155
K 154,156,157
K 155,158,159
ds_write_b128 v2, v[152:155] offset:7168
s_branch .Lend_41%=
.Lfs_40%=:
v_smfmac_f32_16x16x64_bf16 a[160:163], a[176:179], v[192:199], v240
s_mov_b32 s46, 0
s_lshl_b32 s92, s58, 2
s_add_u32 s91, s92, s51
s_add_u32 s95, s91, 3
s_and_b32 s95, s95, 15
s_mul_i32 s95, s95, 40960
s_mul_i32 s71, s51, 10240
v_smfmac_f32_16x16x64_bf16 a[164:167], a[176:179], v[200:207], v240
s_add_u32 s71, s71, 0
s_add_u32 m0, s71, s48
s_lshl2_add_u32 s94, s48, s95
Ra 144,242,45056
Ra 148,242,46080
Ra 152,242,47104
Ra 156,242,48128
v_smfmac_f32_16x16x64_bf16 a[168:171], a[176:179], v[208:215], v240
D 16,40,94,0
s_add_u32 s95, s91, 5
s_and_b32 s95, s95, 15
s_mul_i32 s95, s95, 40960
s_mul_i32 s71, s51, 10240
s_add_u32 s71, s71, 20480
v_smfmac_f32_16x16x64_bf16 a[172:175], a[176:179], v[216:223], v240
W 4
Cvaa 168,128,96,232
v_mov_b32 v192, 0
G 12,160
Mvaa 168,132,100
D 17,40,94,1024
G 4,161
A 12,12,4
v_cmp_nlt_f32_e64 s[86:87], v12, s90
v_rcp_f32 v12, v12
s_or_b64 s[88:89], s[88:89], s[86:87]
Cvaa 172,132,116,236
v_accvgpr_write_b32 a160, 0
v_accvgpr_write_b32 a161, 0
v_accvgpr_write_b32 a162, 0
v_accvgpr_write_b32 a163, 0
G 13,164
G 5,165
Mvaa 172,128,112
v_mov_b32 v193, 0
A 13,13,5
v_cmp_nlt_f32_e64 s[86:87], v13, s90
v_rcp_f32 v13, v13
s_or_b64 s[88:89], s[88:89], s[86:87]
v_accvgpr_write_b32 a164, 0
Mvaa 168,136,104
v_accvgpr_write_b32 a165, 0
v_accvgpr_write_b32 a166, 0
v_accvgpr_write_b32 a167, 0
U 32,32,12
U 33,33,12
U 34,34,12
Mvaa 168,140,108
v_mov_b32 v200, 0
U 35,35,12
U 36,36,12
U 37,37,12
U 38,38,12
U 39,39,12
Mvaa 172,140,124
)"
R"(Ra 128,242,16384
Ra 132,242,17408
K 32,32,33
K 33,34,35
K 34,36,37
K 35,38,39
s_add_u32 s93, s67, 0
Mvaa 172,136,120
Ra 136,242,18432
Ra 140,242,19456
D 18,40,94,2048
v_mov_b32 v201, 0
W 4
Cvaa 188,144,112,236
Ra 144,242,20480
v_cndmask_b32_e64 v168, v241, v168, s[72:73]
v_cndmask_b32_e64 v169, v241, v169, s[74:75]
v_cndmask_b32_e64 v170, v241, v170, s[76:77]
Mvaa 188,148,116
buffer_store_dwordx4 v[32:35], v1, s[8:11], s93 offen offset:0 sc1
U 40,40,12
U 41,41,12
U 42,42,12
U 43,43,12
U 44,44,12
Mvaa 188,152,120
U 45,45,12
U 46,46,12
U 47,47,12
K 40,40,41
K 41,42,43
K 42,44,45
Mvaa 188,156,124
W 1
K 43,46,47
buffer_store_dwordx4 v[40:43], v1, s[8:11], s93 offen offset:1024 sc1
U 48,48,12
U 49,49,12
U 50,50,12
U 51,51,12
Mvav 96,128,212
U 52,52,12
U 53,53,12
U 54,54,12
U 55,55,12
K 48,48,49
K 49,50,51
Mvav 128,128,220
K 50,52,53
K 51,54,55
buffer_store_dwordx4 v[48:51], v1, s[8:11], s93 offen offset:2048 sc1
U 56,56,12
U 57,57,12
U 58,58,12
Mvav 132,132,220
Ra 148,242,21504
Ra 152,242,22528
Ra 156,242,23552
U 59,59,12
U 60,60,12
U 61,61,12
Mvav 100,132,212
D 19,40,94,3072
s_add_u32 m0, m0, 4096
U 62,62,12
U 63,63,12
K 56,56,57
K 57,58,59
K 58,60,61
Mvav 104,136,212
K 59,62,63
v_cndmask_b32_e64 v171, v241, v171, s[78:79]
X 168
X 169
Mvav 136,136,220
buffer_store_dwordx4 v[56:59], v1, s[8:11], s93 offen offset:3072 sc1
Ra 128,242,49152
G 14,168
G 6,169
A 14,14,6
v_cmp_nlt_f32_e64 s[86:87], v14, s90
Mvav 140,140,220
v_rcp_f32 v14, v14
s_or_b64 s[88:89], s[88:89], s[86:87]
v_accvgpr_write_b32 a168, 0
v_accvgpr_write_b32 a169, 0
v_accvgpr_write_b32 a170, 0
v_accvgpr_write_b32 a171, 0
Mvav 108,140,212
W 1
G 15,172
G 7,173
A 15,15,7
v_cmp_nlt_f32_e64 s[86:87], v15, s90
v_rcp_f32 v15, v15
Mvav 112,144,212
s_or_b64 s[88:89], s[88:89], s[86:87]
buffer_load_dwordx4 v20, s[40:43], s94 offen lds
v_accvgpr_write_b32 a172, 0
v_accvgpr_write_b32 a173, 0
v_accvgpr_write_b32 a174, 0
v_accvgpr_write_b32 a175, 0
U 64,64,13
Mvav 144,144,220
s_add_u32 m0, s71, s48
s_lshl2_add_u32 s94, s48, s95
U 65,65,13
U 66,66,13
U 67,67,13
U 68,68,13
U 69,69,13
Mvav 148,148,220
U 70,70,13
U 71,71,13
K 64,64,65
K 65,66,67
K 66,68,69
K 67,70,71
s_add_u32 s93, s67, 131072
Mvav 116,148,212
buffer_store_dwordx4 v[64:67], v1, s[8:11], s93 offen offset:0 sc1
U 72,72,13
U 73,73,13
U 74,74,13
U 75,75,13
U 76,76,13
Mvav 120,152,212
Ra 132,242,50176
Ra 136,242,51200
Ra 140,242,52224
U 77,77,13
U 78,78,13
U 79,79,13
Mvav 152,152,220
K 72,72,73
K 73,74,75
K 74,76,77
K 75,78,79
buffer_store_dwordx4 v[72:75], v1, s[8:11], s93 offen offset:1024 sc1
U 80,80,13
Mvav 156,156,220
X 170
K 208,168,169
X 171
X 172
K 209,170,171
X 173
X 174
K 216,172,173
X 175
v_mov_b32 v194, 0
K 217,174,175
v_mov_b32 v195, 0
v_mov_b32 v202, 0
v_mov_b32 v203, 0
v_mov_b32 v210, 0
v_mov_b32 v211, 0
Mvav 124,156,212
v_cndmask_b32_e64 v188, v241, v188, s[72:73]
v_cndmask_b32_e64 v189, v241, v189, s[74:75]
v_cndmask_b32_e64 v190, v241, v190, s[76:77]
v_cndmask_b32_e64 v191, v241, v191, s[78:79]
X 188
X 189
X 190
K 218,188,189
X 191
s_nop 0
K 219,190,191
Maav 168,180,208
D 16,40,94,0
U 81,81,13
U 82,82,13
U 83,83,13
U 84,84,13
U 85,85,13
Maav 172,180,216
W 0
U 86,86,13
U 87,87,13
K 80,80,81
K 81,82,83
K 82,84,85
K 83,86,87
Zvav 32,128,192
buffer_store_dwordx4 v[80:83], v1, s[8:11], s93 offen offset:2048 sc1
U 88,88,13
U 89,89,13
U 90,90,13
U 91,91,13
U 92,92,13
Zvav 64,128,200
U 93,93,13
U 94,94,13
U 95,95,13
K 88,88,89
K 89,90,91
K 90,92,93
K 91,94,95
buffer_store_dwordx4 v[88:91], v1, s[8:11], s93 offen offset:3072 sc1
U 96,96,14
U 97,97,14
U 98,98,14
U 99,99,14
U 100,100,14
U 101,101,14
U 102,102,14
U 103,103,14
K 96,96,97
K 97,98,99
K 98,100,101
K 99,102,103
s_add_u32 s93, s67, 262144
buffer_store_dwordx4 v[96:99], v1, s[8:11], s93 offen offset:0 sc1
s_nop 1
Zvav 96,128,208
U 104,104,14
U 105,105,14
U 106,106,14
U 107,107,14
U 108,108,14
U 109,109,14
U 110,110,14
U 111,111,14
K 104,104,105
K 105,106,107
K 106,108,109
K 107,110,111
buffer_store_dwordx4 v[104:107], v1, s[8:11], s93 offen offset:1024 sc1
U 112,112,14
U 113,113,14
U 114,114,14
U 115,115,14
U 116,116,14
U 117,117,14
U 118,118,14
U 119,119,14
K 112,112,113
K 113,114,115
K 114,116,117
K 115,118,119
buffer_store_dwordx4 v[112:115], v1, s[8:11], s93 offen offset:2048 sc1
U 120,120,14
U 121,121,14
U 122,122,14
U 123,123,14
U 124,124,14
U 125,125,14
U 126,126,14
U 127,127,14
K 120,120,121
K 121,122,123
K 122,124,125
K 123,126,127
buffer_store_dwordx4 v[120:123], v1, s[8:11], s93 offen offset:3072 sc1
U 128,128,15
U 129,129,15
U 130,130,15
U 131,131,15
U 132,132,15
U 133,133,15
U 134,134,15
U 135,135,15
K 128,128,129
K 129,130,131
K 130,132,133
K 131,134,135
s_add_u32 s93, s67, 393216
buffer_store_dwordx4 v[128:131], v1, s[8:11], s93 offen offset:0 sc1
s_nop 1
Zvav 128,128,216
Ra 144,242,53248
U 136,136,15
U 137,137,15
U 138,138,15
U 139,139,15
U 140,140,15
Zvav 132,132,216
D 17,40,94,1024
U 141,141,15
U 142,142,15
U 143,143,15
K 136,136,137
K 137,138,139
Zvav 100,132,208
Ra 148,242,54272
Ra 152,242,55296
Ra 156,242,56320
K 138,140,141
K 139,142,143
Zvav 68,132,200
D 18,40,94,2048
Zvav 36,132,192
Zvav 40,136,192
D 19,40,94,3072
s_add_u32 m0, m0, 4096
Zvav 72,136,200
Zvav 104,136,208
buffer_store_dwordx4 v[136:139], v1, s[8:11], s93 offen offset:1024 sc1
U 144,144,15
U 145,145,15
U 146,146,15
U 147,147,15
U 148,148,15
Zvav 136,136,216
U 149,149,15
U 150,150,15
U 151,151,15
K 144,144,145
K 145,146,147
K 146,148,149
Zvav 140,140,216
K 147,150,151
buffer_store_dwordx4 v[144:147], v1, s[8:11], s93 offen offset:2048 sc1
U 152,152,15
U 153,153,15
U 154,154,15
U 155,155,15
Zvav 108,140,208
U 156,156,15
U 157,157,15
U 158,158,15
U 159,159,15
K 152,152,153
K 153,154,155
Zvav 76,140,200
buffer_load_dwordx4 v20, s[40:43], s94 offen lds
K 154,156,157
K 155,158,159
Zvav 44,140,192
W 0
Zvav 48,144,192
buffer_store_dwordx4 v[152:155], v1, s[8:11], s93 offen offset:3072 sc1
s_mov_b32 s18, 40960
s_mov_b32 s19, 73728
s_bitcmp1_b32 s14, 0
s_cselect_b32 s18, 49152, s18
s_cselect_b32 s19, 98304, s19
s_cmp_lt_u32 s14, 2
s_cselect_b32 s20, s18, s19
s_cselect_b32 s21, s19, s18
v_add_u32 v2, s20, v1
v_add_u32 v3, s21, v1
Zvav 80,144,200
s_lshl_b32 s18, s14, 9
v_lshrrev_b32 v232, 1, v1
v_add_u32 v232, s18, v232
v_bfe_u32 v12, v1, 6, 2
v_lshrrev_b32 v13, 8, v1
v_bfe_u32 v14, v1, 4, 1
v_lshlrev_b32 v14, 4, v14
Zvav 112,144,208
v_mov_b32 v15, 0x3f80
v_lshlrev_b32 v15, v14, v15
v_bfe_u32 v14, v1, 5, 1
v_xor_b32 v27, v12, v13
v_lshl_or_b32 v27, v27, 1, v14
v_cmp_eq_u32_e64 s[86:87], 0, v27
Zvav 144,144,216
v_mov_b32 v226, 0
Zvav 148,148,216
v_cndmask_b32_e64 v224, 0, v15, s[86:87]
v_mov_b32 v228, 0
v_mov_b32 v230, v224
v_cmp_eq_u32_e64 s[86:87], 1, v27
v_mov_b32 v227, 0
s_nop 0
v_cndmask_b32_e64 v225, 0, v15, s[86:87]
Zvav 116,148,208
v_mov_b32 v229, 0
v_mov_b32 v231, v225
G 4,160
G 8,161
G 5,164
G 9,165
Zvav 84,148,200
G 6,168
G 10,169
G 7,172
G 11,173
A 4,4,8
A 5,5,9
Zvav 52,148,192
A 6,6,10
A 7,7,11
ds_write_b64 v232, v[6:7] offset:57344
A 8,4,5
A 9,6,7
A 8,8,9
Zvav 56,152,192
v_cmp_ngt_f32_e64 s[86:87], s90, v8
s_lshl_b32 s19, s14, 2
v_mov_b32 v9, s19
s_or_b64 s[88:89], s[88:89], s[86:87]
s_and_b64 s[88:89], s[88:89], s[24:25]
s_cselect_b32 s18, 1, 0
v_mov_b32 v8, s18
ds_write_b32 v9, v8 offset:59392
Zvav 88,152,200
v_xor_b32 v10, 1024, v232
)"
R"(v_bfe_u32 v11, v1, 2, 4
K 96,96,97
K 97,98,99
K 98,100,101
K 99,102,103
Zvav 120,152,208
ds_write_b128 v2, v[96:99] offset:0
K 128,128,129
K 129,130,131
K 130,132,133
K 131,134,135
ds_write_b128 v2, v[128:131] offset:4096
Zvav 152,152,216
K 104,104,105
K 105,106,107
K 106,108,109
K 107,110,111
ds_write_b128 v2, v[104:107] offset:1024
K 136,136,137
Zvav 156,156,216
K 137,138,139
K 138,140,141
K 139,142,143
ds_write_b128 v2, v[136:139] offset:5120
K 112,112,113
K 113,114,115
Zvav 124,156,208
K 114,116,117
K 115,118,119
ds_write_b128 v2, v[112:115] offset:2048
K 144,144,145
K 145,146,147
K 146,148,149
Zvav 92,156,200
K 147,150,151
ds_write_b128 v2, v[144:147] offset:6144
Zvav 60,156,192
K 120,120,121
K 121,122,123
K 122,124,125
K 123,126,127
ds_write_b128 v2, v[120:123] offset:3072
K 152,152,153
K 153,154,155
K 154,156,157
K 155,158,159
ds_write_b128 v2, v[152:155] offset:7168
s_waitcnt vmcnt(0)
s_and_b64 s[86:87], s[88:89], s[24:25]
s_cbranch_scc1 .LL_48%=
v_mov_b64 v[28:29], s[12:13]
s_mov_b64 exec, 1
buffer_store_dwordx2 v[28:29], off, s[8:11], s69 sc1
s_mov_b64 exec, -1
.LL_48%=:
s_mov_b32 s70, 0
s_branch .Lend_41%=
.Lpd_30%=:
v_pk_mul_f32 v[32:33], v[32:33], s[22:23]
v_pk_mul_f32 v[34:35], v[34:35], s[22:23]
v_pk_mul_f32 v[36:37], v[36:37], s[22:23]
v_pk_mul_f32 v[38:39], v[38:39], s[22:23]
v_pk_mul_f32 v[64:65], v[64:65], s[22:23]
v_pk_mul_f32 v[66:67], v[66:67], s[22:23]
v_pk_mul_f32 v[68:69], v[68:69], s[22:23]
v_pk_mul_f32 v[70:71], v[70:71], s[22:23]
v_pk_mul_f32 v[96:97], v[96:97], s[22:23]
v_pk_mul_f32 v[98:99], v[98:99], s[22:23]
v_pk_mul_f32 v[100:101], v[100:101], s[22:23]
v_pk_mul_f32 v[102:103], v[102:103], s[22:23]
v_pk_mul_f32 v[128:129], v[128:129], s[22:23]
v_pk_mul_f32 v[130:131], v[130:131], s[22:23]
v_pk_mul_f32 v[132:133], v[132:133], s[22:23]
v_pk_mul_f32 v[134:135], v[134:135], s[22:23]
v_pk_mul_f32 v[40:41], v[40:41], s[22:23]
v_pk_mul_f32 v[42:43], v[42:43], s[22:23]
v_pk_mul_f32 v[44:45], v[44:45], s[22:23]
v_pk_mul_f32 v[46:47], v[46:47], s[22:23]
v_pk_mul_f32 v[72:73], v[72:73], s[22:23]
v_pk_mul_f32 v[74:75], v[74:75], s[22:23]
v_pk_mul_f32 v[76:77], v[76:77], s[22:23]
v_pk_mul_f32 v[78:79], v[78:79], s[22:23]
v_pk_mul_f32 v[104:105], v[104:105], s[22:23]
v_pk_mul_f32 v[106:107], v[106:107], s[22:23]
v_pk_mul_f32 v[108:109], v[108:109], s[22:23]
v_pk_mul_f32 v[110:111], v[110:111], s[22:23]
v_pk_mul_f32 v[136:137], v[136:137], s[22:23]
v_pk_mul_f32 v[138:139], v[138:139], s[22:23]
v_pk_mul_f32 v[140:141], v[140:141], s[22:23]
v_pk_mul_f32 v[142:143], v[142:143], s[22:23]
v_pk_mul_f32 v[48:49], v[48:49], s[22:23]
v_pk_mul_f32 v[50:51], v[50:51], s[22:23]
v_pk_mul_f32 v[52:53], v[52:53], s[22:23]
v_pk_mul_f32 v[54:55], v[54:55], s[22:23]
v_pk_mul_f32 v[80:81], v[80:81], s[22:23]
v_pk_mul_f32 v[82:83], v[82:83], s[22:23]
v_pk_mul_f32 v[84:85], v[84:85], s[22:23]
v_pk_mul_f32 v[86:87], v[86:87], s[22:23]
v_pk_mul_f32 v[112:113], v[112:113], s[22:23]
v_pk_mul_f32 v[114:115], v[114:115], s[22:23]
v_pk_mul_f32 v[116:117], v[116:117], s[22:23]
v_pk_mul_f32 v[118:119], v[118:119], s[22:23]
v_pk_mul_f32 v[144:145], v[144:145], s[22:23]
v_pk_mul_f32 v[146:147], v[146:147], s[22:23]
v_pk_mul_f32 v[148:149], v[148:149], s[22:23]
v_pk_mul_f32 v[150:151], v[150:151], s[22:23]
v_pk_mul_f32 v[56:57], v[56:57], s[22:23]
v_pk_mul_f32 v[58:59], v[58:59], s[22:23]
v_pk_mul_f32 v[60:61], v[60:61], s[22:23]
v_pk_mul_f32 v[62:63], v[62:63], s[22:23]
v_pk_mul_f32 v[88:89], v[88:89], s[22:23]
v_pk_mul_f32 v[90:91], v[90:91], s[22:23]
v_pk_mul_f32 v[92:93], v[92:93], s[22:23]
v_pk_mul_f32 v[94:95], v[94:95], s[22:23]
v_pk_mul_f32 v[120:121], v[120:121], s[22:23]
v_pk_mul_f32 v[122:123], v[122:123], s[22:23]
v_pk_mul_f32 v[124:125], v[124:125], s[22:23]
v_pk_mul_f32 v[126:127], v[126:127], s[22:23]
v_pk_mul_f32 v[152:153], v[152:153], s[22:23]
v_pk_mul_f32 v[154:155], v[154:155], s[22:23]
v_pk_mul_f32 v[156:157], v[156:157], s[22:23]
v_pk_mul_f32 v[158:159], v[158:159], s[22:23]
K 32,32,33
K 33,34,35
K 34,36,37
K 35,38,39
K 40,40,41
K 41,42,43
K 42,44,45
K 43,46,47
W 0
Zvav 196,128,32
s_add_u32 s36, s29, 65536
Ra 144,242,4096
Ra 148,242,5120
Ra 152,242,6144
Ra 156,242,7168
Mvav 196,132,40
K 64,64,65
K 65,66,67
K 66,68,69
K 67,70,71
K 72,72,73
K 73,74,75
K 74,76,77
K 75,78,79
s_nop 1
Zvav 204,132,72
buffer_store_dwordx4 a[192:195], v1, s[8:11], s36 offen offset:0 sc1
K 96,96,97
K 97,98,99
K 98,100,101
Mvav 204,128,64
K 99,102,103
K 104,104,105
K 105,106,107
K 106,108,109
K 107,110,111
Zvav 212,128,96
buffer_store_dwordx4 a[200:203], v1, s[8:11], s36 offen offset:1024 sc1
K 128,128,129
K 129,130,131
K 130,132,133
Mvav 212,132,104
K 131,134,135
K 136,136,137
K 137,138,139
K 138,140,141
K 139,142,143
s_nop 1
Zvav 220,132,136
buffer_store_dwordx4 a[208:211], v1, s[8:11], s36 offen offset:2048 sc1
K 48,48,49
K 49,50,51
K 50,52,53
Mvav 220,128,128
K 51,54,55
K 56,56,57
K 57,58,59
K 58,60,61
K 59,62,63
Mvav 196,136,48
Ra 128,242,8192
Ra 132,242,9216
buffer_store_dwordx4 a[216:219], v1, s[8:11], s36 offen offset:3072 sc1
s_add_u32 s36, s29, 69632
K 80,80,81
Mvav 196,140,56
K 81,82,83
K 82,84,85
K 83,86,87
K 88,88,89
K 89,90,91
K 90,92,93
K 91,94,95
s_nop 1
Mvav 204,140,88
buffer_store_dwordx4 a[224:227], v1, s[8:11], s36 offen offset:0 sc1
K 112,112,113
K 113,114,115
K 114,116,117
Mvav 204,136,80
K 115,118,119
K 120,120,121
K 121,122,123
K 122,124,125
K 123,126,127
Mvav 212,136,112
K 144,144,145
K 145,146,147
K 146,148,149
K 147,150,151
Mvav 212,140,120
K 152,152,153
K 153,154,155
K 154,156,157
K 155,158,159
s_nop 1
Mvav 220,140,152
buffer_store_dwordx4 a[232:235], v1, s[8:11], s36 offen offset:1024 sc1
v_accvgpr_write_b32 a0, v32
v_accvgpr_write_b32 a1, v33
v_accvgpr_write_b32 a2, v34
Mvav 220,136,144
W 2
v_accvgpr_write_b32 a3, v35
v_accvgpr_write_b32 a4, v40
v_accvgpr_write_b32 a5, v41
v_accvgpr_write_b32 a6, v42
Zvav 68,144,64
buffer_store_dwordx4 a[240:243], v1, s[8:11], s36 offen offset:2048 sc1
v_accvgpr_write_b32 a7, v43
v_accvgpr_write_b32 a8, v48
v_accvgpr_write_b32 a9, v49
Mvav 68,148,72
Ra 136,242,10240
Ra 140,242,11264
v_accvgpr_write_b32 a10, v50
v_accvgpr_write_b32 a11, v51
Zvav 100,148,104
v_accvgpr_write_b32 a12, v56
v_accvgpr_write_b32 a13, v57
v_accvgpr_write_b32 a14, v58
v_accvgpr_write_b32 a15, v59
Mvav 100,144,96
buffer_store_dwordx4 a[248:251], v1, s[8:11], s36 offen offset:3072 sc1
s_add_u32 s36, s29, 393216
v_accvgpr_write_b32 a16, v64
v_cndmask_b32_e64 v196, v241, v196, s[72:73]
v_cndmask_b32_e64 v197, v241, v197, s[74:75]
Zvav 132,144,128
v_cndmask_b32_e64 v198, v241, v198, s[76:77]
v_cndmask_b32_e64 v199, v241, v199, s[78:79]
v_max3_f32 v4, v196, v197, v198
v_max3_f32 v4, v4, v199, v252
Mvav 132,148,136
v_max3_f32 v5, v204, v205, v206
v_max3_f32 v5, v5, v207, v253
v_max3_f32 v6, v212, v213, v214
v_max3_f32 v6, v6, v215, v254
Mvav 68,152,80
v_max3_f32 v7, v220, v221, v222
v_max3_f32 v7, v7, v223, v255
v_mov_b32 v8, v4
v_mov_b32 v9, v5
Mvav 68,156,88
v_mov_b32 v10, v6
v_mov_b32 v11, v7
v_permlane32_swap_b32 v4, v8
v_accvgpr_write_b32 a17, v65
Mvav 100,156,120
v_permlane32_swap_b32 v5, v9
v_permlane32_swap_b32 v6, v10
v_permlane32_swap_b32 v7, v11
v_max_f32 v4, v4, v8
Mvav 100,152,112
v_max_f32 v5, v5, v9
v_max_f32 v6, v6, v10
v_max_f32 v7, v7, v11
v_mov_b32 v8, v4
Mvav 132,152,144
v_mov_b32 v9, v5
v_mov_b32 v10, v6
v_mov_b32 v11, v7
v_permlane16_swap_b32 v4, v8
v_permlane16_swap_b32 v5, v9
v_permlane16_swap_b32 v6, v10
v_permlane16_swap_b32 v7, v11
v_max_f32 v4, v4, v8
v_max_f32 v5, v5, v9
v_max_f32 v6, v6, v10
v_max_f32 v7, v7, v11
v_sub_f32 v4, 0, v4
v_sub_f32 v5, 0, v5
v_sub_f32 v6, 0, v6
v_sub_f32 v7, 0, v7
v_mov_b32 v224, v4
v_mov_b32 v225, v4
v_mov_b32 v226, v4
v_mov_b32 v227, v4
v_mov_b32 v228, v5
v_mov_b32 v229, v5
v_mov_b32 v230, v5
v_mov_b32 v231, v5
v_mov_b32 v232, v6
v_mov_b32 v233, v6
v_mov_b32 v234, v6
v_mov_b32 v235, v6
v_mov_b32 v236, v7
v_mov_b32 v237, v7
v_mov_b32 v238, v7
v_mov_b32 v239, v7
Mvav 132,156,152
W 0
v_accvgpr_write_b32 a18, v66
v_accvgpr_write_b32 a19, v67
v_accvgpr_write_b32 a20, v72
v_accvgpr_write_b32 a21, v73
Cvav 168,128,96,232
s_and_b32 s80, s14, 1
buffer_store_dwordx4 a[196:199], v1, s[8:11], s36 offen offset:0 sc1
v_accvgpr_write_b32 a22, v74
v_accvgpr_write_b32 a23, v75
v_accvgpr_write_b32 a24, v80
Mvav 168,132,104
s_lshl_b32 s80, s80, 14
Ra 144,242,12288
v_accvgpr_write_b32 a25, v81
v_accvgpr_write_b32 a26, v82
v_accvgpr_write_b32 a27, v83
Cvav 172,132,136,236
Ra 148,242,13312
Ra 152,242,14336
Ra 156,242,15360
v_accvgpr_write_b32 a28, v88
Mvav 172,128,128
s_add_u32 s80, s80, 98304
v_accvgpr_write_b32 a29, v89
v_accvgpr_write_b32 a30, v90
v_accvgpr_write_b32 a31, v91
v_accvgpr_write_b32 a32, v96
Mvav 168,136,112
buffer_store_dwordx4 a[204:207], v1, s[8:11], s36 offen offset:1024 sc1
v_accvgpr_write_b32 a33, v97
v_accvgpr_write_b32 a34, v98
v_accvgpr_write_b32 a35, v99
Mvav 168,140,120
v_accvgpr_write_b32 a36, v104
v_accvgpr_write_b32 a37, v105
v_accvgpr_write_b32 a38, v106
v_accvgpr_write_b32 a39, v107
Mvav 172,140,152
Ra 128,242,24576
Ra 132,242,25600
buffer_store_dwordx4 a[212:215], v1, s[8:11], s36 offen offset:2048 sc1
v_accvgpr_write_b32 a40, v112
Mvav 172,136,144
Ra 136,242,26624
Ra 140,242,27648
v_accvgpr_write_b32 a41, v113
W 4
Cvav 188,144,128,236
v_accvgpr_write_b32 a42, v114
v_accvgpr_write_b32 a43, v115
v_accvgpr_write_b32 a44, v120
v_accvgpr_write_b32 a45, v121
v_accvgpr_write_b32 a46, v122
v_accvgpr_write_b32 a47, v123
v_accvgpr_write_b32 a48, v128
v_accvgpr_write_b32 a49, v129
v_accvgpr_write_b32 a50, v130
v_accvgpr_write_b32 a51, v131
v_accvgpr_write_b32 a52, v136
v_accvgpr_write_b32 a53, v137
v_accvgpr_write_b32 a54, v138
v_accvgpr_write_b32 a55, v139
v_accvgpr_write_b32 a56, v144
v_accvgpr_write_b32 a57, v145
v_accvgpr_write_b32 a58, v146
v_accvgpr_write_b32 a59, v147
v_accvgpr_write_b32 a60, v152
v_accvgpr_write_b32 a61, v153
v_accvgpr_write_b32 a62, v154
v_accvgpr_write_b32 a63, v155
Mvav 188,148,136
s_barrier
buffer_store_dwordx4 a[220:223], v1, s[8:11], s36 offen offset:3072 sc1
s_add_u32 s36, s29, 397312
buffer_store_dwordx4 a[228:231], v1, s[8:11], s36 offen offset:0 sc1
buffer_store_dwordx4 a[236:239], v1, s[8:11], s36 offen offset:1024 sc1
buffer_store_dwordx4 a[244:247], v1, s[8:11], s36 offen offset:2048 sc1
buffer_store_dwordx4 a[252:255], v1, s[8:11], s36 offen offset:3072 sc1
v_add_u32 v8, s80, v1
Mvav 188,152,144
A 196,196,224
A 197,197,224
A 198,198,224
A 199,199,224
X 196
X 197
)"
R"(X 198
K 192,196,197
X 199
A 204,204,228
K 193,198,199
A 205,205,228
A 206,206,228
A 207,207,228
X 204
X 205
X 206
K 200,204,205
X 207
A 212,212,232
K 201,206,207
A 213,213,232
A 214,214,232
A 215,215,232
X 212
X 213
X 214
K 208,212,213
X 215
A 220,220,236
K 209,214,215
A 221,221,236
A 222,222,236
A 223,223,236
X 220
X 221
X 222
K 216,220,221
X 223
v_mov_b32 v194, 0
K 217,222,223
v_mov_b32 v195, 0
v_cndmask_b32_e64 v68, v241, v68, s[72:73]
v_cndmask_b32_e64 v69, v241, v69, s[74:75]
v_cndmask_b32_e64 v70, v241, v70, s[76:77]
v_cndmask_b32_e64 v71, v241, v71, s[78:79]
A 68,68,228
A 69,69,228
A 70,70,228
A 71,71,228
X 68
X 69
X 70
K 202,68,69
X 71
A 100,100,232
K 203,70,71
A 101,101,232
A 102,102,232
A 103,103,232
X 100
X 101
X 102
K 210,100,101
X 103
A 132,132,236
K 211,102,103
A 133,133,236
A 134,134,236
A 135,135,236
X 132
X 133
X 134
K 218,132,133
X 135
s_nop 0
K 219,134,135
Mvav 188,156,152
W 0
Ra 144,242,28672
Ra 148,242,29696
Ra 152,242,30720
Zvav 32,128,192
Ra 156,242,31744
Zvav 64,128,200
Zvav 96,128,208
s_and_b32 s80, s14, 1
s_xor_b32 s62, s62, 0x10000
Zvav 128,128,216
s_lshl_b32 s80, s80, 14
s_add_u32 s80, s80, 131072
v_add_u32 v23, 61440, v21
Zvav 132,132,216
v_add_u32 v12, s80, v1
Ra 64,12,8192
Ra 68,12,9216
v_add_u32 v24, 61440, v22
Zvav 100,132,208
Ra 72,12,10240
Ra 76,12,11264
Ra 80,12,12288
v_add_u32 v25, 122880, v21
Zvav 68,132,200
Ra 84,12,13312
Ra 88,12,14336
Ra 92,12,15360
v_add_u32 v26, 122880, v22
Zvav 36,132,192
Ra 96,12,0
Ra 100,12,1024
Ra 104,12,2048
Zvav 40,136,192
Ra 108,12,3072
Ra 112,12,4096
Ra 116,12,5120
Zvav 72,136,200
Ra 120,12,6144
Ra 124,12,7168
Zvav 104,136,208
Zvav 136,136,216
Zvav 140,140,216
Zvav 108,140,208
Zvav 76,140,200
s_waitcnt vmcnt(0) lgkmcnt(0)
s_barrier
v_xor_b32 v242, 0x10000, v242
ds_read_b128 v[248:251], v8
Zvav 44,140,192
s_add_u32 s80, s71, 128
v_mov_b64 v[28:29], s[12:13]
s_mov_b64 exec, 1
buffer_store_dwordx2 v[28:29], off, s[8:11], s80 sc1
s_mov_b64 exec, -1
Ra 128,242,40960
Zvav 48,144,192
Zvav 80,144,200
X 172
X 173
Zvav 112,144,208
Ra 132,242,41984
X 174
K 220,172,173
Zvav 144,144,216
X 175
v_cndmask_b32_e64 v168, v241, v168, s[72:73]
K 221,174,175
Zvav 148,148,216
v_cndmask_b32_e64 v169, v241, v169, s[74:75]
v_cndmask_b32_e64 v170, v241, v170, s[76:77]
v_cndmask_b32_e64 v171, v241, v171, s[78:79]
Zvav 116,148,208
X 168
X 169
Zvav 84,148,200
W 2
Ra 136,242,43008
X 170
K 212,168,169
Zvav 52,148,192
v_mov_b32 v224, v250
v_mov_b32 v225, v250
v_mov_b32 v226, v250
v_mov_b32 v227, v250
Zvav 56,152,192
v_mov_b32 v228, v251
v_mov_b32 v229, v251
v_mov_b32 v230, v251
v_mov_b32 v231, v251
Zvav 88,152,200
v_mov_b32 v232, v248
v_mov_b32 v233, v248
v_mov_b32 v234, v248
v_mov_b32 v235, v248
Zvav 120,152,208
v_mov_b32 v236, v249
v_mov_b32 v237, v249
v_mov_b32 v238, v249
v_mov_b32 v239, v249
Zvav 152,152,216
X 171
v_mov_b32 v204, 0
K 213,170,171
Zvav 156,156,216
Ra 140,242,44032
v_mov_b32 v205, 0
v_mov_b32 v196, 0
v_mov_b32 v197, 0
Zvav 124,156,208
v_mov_b32 v198, 0
v_mov_b32 v199, 0
v_mov_b32 v206, 0
v_mov_b32 v207, 0
Zvav 92,156,200
v_mov_b32 v214, 0
v_mov_b32 v215, 0
v_cndmask_b32_e64 v188, v241, v188, s[72:73]
v_cndmask_b32_e64 v189, v241, v189, s[74:75]
Zvav 60,156,192
v_cndmask_b32_e64 v190, v241, v190, s[76:77]
v_cndmask_b32_e64 v191, v241, v191, s[78:79]
X 188
X 189
X 190
K 222,188,189
X 191
s_nop 0
K 223,190,191
s_nop 1
s_branch .Lsp2_35%=
.Lmoe_42%=:
W 0
Cvaa 160,128,0,224
s_cmp_eq_u32 s14, 0
s_cselect_b32 s20, s30, s32
s_cselect_b32 s21, s31, s33
Ra 144,242,4096
Ra 148,242,5120
Mvaa 160,132,4
s_xor_b32 s62, s62, 0x10000
Ra 152,242,6144
Cvaa 164,132,20,228
X 184
Mvaa 164,128,16
s_and_b32 s21, s21, 0xffff
X 185
Cvaa 168,128,32,232
X 186
Mvaa 168,132,36
Ra 156,242,7168
K 214,184,185
Cvaa 172,132,52,236
s_mov_b32 s22, 2211840
X 187
Mvaa 172,128,48
X 188
Mvaa 160,136,8
Ra 128,242,16384
Mvaa 160,140,12
s_mov_b32 s23, 0x20000
K 215,186,187
Mvaa 164,140,28
X 189
Mvaa 164,136,24
X 190
Mvaa 168,136,40
Ra 132,242,17408
v_lshrrev_b32 v4, 6, v1
Mvaa 168,140,44
K 222,188,189
Mvaa 172,140,60
X 191
Mvaa 172,136,56
v_mul_u32_u24 v4, 3456, v4
K 223,190,191
v_smfmac_f32_16x16x64_bf16 a[160:163], a[176:179], v[192:199], v240
Ra 136,242,18432
v_smfmac_f32_16x16x64_bf16 a[164:167], a[176:179], v[200:207], v240
X 160
v_smfmac_f32_16x16x64_bf16 a[168:171], a[176:179], v[208:215], v240
v_lshrrev_b32 v5, 4, v1
v_smfmac_f32_16x16x64_bf16 a[172:175], a[176:179], v[216:223], v240
W 3
X 161
Cvaa 176,144,0,224
X 162
Mvaa 176,148,4
Ra 140,242,19456
v_xor_b32 v5, v5, v1
Cvaa 180,148,20,228
K 192,160,161
Mvaa 180,144,16
X 163
Cvaa 184,144,32,232
v_and_b32 v5, 32, v5
Mvaa 184,148,36
X 164
Cvaa 188,148,52,236
K 193,162,163
v_and_b32 v6, 16, v1
Mvaa 188,144,48
X 165
Mvaa 176,152,8
X 166
Mvaa 176,156,12
Ra 144,242,20480
Ra 148,242,21504
Mvaa 180,156,28
v_add3_u32 v4, v4, v5, v6
K 200,164,165
Mvaa 180,152,24
X 167
Mvaa 184,152,40
X 168
Mvaa 184,156,44
v_add_u32 v5, 54272, v4
K 201,166,167
Mvaa 188,156,60
X 169
Mvaa 188,152,56
W 2
Ra 152,242,22528
v_add_u32 v6, 108544, v4
Mvav 32,128,196
X 170
Mvav 64,128,204
Ra 156,242,23552
v_add_u32 v7, 162816, v4
Mvav 96,128,212
K 208,168,169
Mvav 128,128,220
X 171
Mvav 132,132,220
s_lshl_b32 s94, s85, 6
X 172
Mvav 100,132,212
s_add_u32 s64, s64, 1
Ra 128,242,8192
K 209,170,171
Mvav 68,132,204
X 173
Mvav 36,132,196
s_mul_i32 s95, s14, 20480
Mvav 40,136,196
X 174
Mvav 72,136,204
Ra 132,242,9216
K 216,172,173
Mvav 104,136,212
s_add_u32 s95, s95, 98304
X 175
Mvav 136,136,220
Mvav 140,140,220
s_add_u32 m0, s95, 0
X 176
Mvav 108,140,212
Ra 136,242,10240
K 217,174,175
Mvav 76,140,204
X 177
Mvav 44,140,196
W 3
D 4,20,94,0
Mvav 48,144,196
X 178
Mvav 80,144,204
Ra 140,242,11264
Mvav 112,144,212
D 5,20,94,1024
K 194,176,177
Mvav 144,144,220
X 179
Mvav 148,148,220
X 180
Mvav 116,148,212
Ra 144,242,12288
D 6,20,94,2048
Mvav 84,148,204
K 195,178,179
Mvav 52,148,196
X 181
Mvav 56,152,196
D 7,20,94,3072
Ra 148,242,13312
Mvav 88,152,204
X 182
Mvav 120,152,212
s_add_u32 s94, s94, 221184
K 202,180,181
Mvav 152,152,220
X 183
Mvav 156,156,220
X 184
Mvav 124,156,212
s_add_u32 m0, s95, 4096
Ra 152,242,14336
Mvav 92,156,204
K 203,182,183
Mvav 60,156,196
W 3
X 185
Cvaa 160,128,0,224
D 4,20,94,0
Ra 156,242,15360
Mvaa 160,132,4
X 186
Cvaa 164,132,20,228
K 210,184,185
Mvaa 164,128,16
X 187
Cvaa 168,128,32,232
D 5,20,94,1024
Mvaa 168,132,36
X 188
Cvaa 172,132,52,236
D 6,20,94,2048
K 211,186,187
Mvaa 172,128,48
X 189
Mvaa 160,136,8
Ra 128,242,24576
Ra 132,242,25600
Mvaa 160,140,12
X 190
Mvaa 164,140,28
D 7,20,94,3072
K 218,188,189
)"
R"(Mvaa 164,136,24
s_add_u32 s94, s94, 221184
X 191
Mvaa 168,136,40
Mvaa 168,140,44
X 160
Mvaa 172,140,60
s_add_u32 m0, s95, 8192
K 219,190,191
Mvaa 172,136,56
W 2
X 161
Cvaa 176,144,0,224
Ra 136,242,26624
Ra 140,242,27648
Mvaa 176,148,4
X 162
Cvaa 180,148,20,228
D 4,20,94,0
K 196,160,161
Mvaa 180,144,16
X 163
Cvaa 184,144,32,232
D 5,20,94,1024
Mvaa 184,148,36
X 164
Cvaa 188,148,52,236
K 197,162,163
Mvaa 188,144,48
D 6,20,94,2048
Ra 144,242,28672
Mvaa 176,152,8
X 165
Mvaa 176,156,12
Ra 148,242,29696
D 7,20,94,3072
Mvaa 180,156,28
X 166
Mvaa 180,152,24
K 204,164,165
Mvaa 184,152,40
s_add_u32 s94, s94, 221184
X 167
Mvaa 184,156,44
X 168
Mvaa 188,156,60
Mvaa 188,152,56
W 2
s_add_u32 m0, s95, 12288
K 205,166,167
Mvav 32,128,192
X 169
Mvav 64,128,200
Ra 152,242,30720
Ra 156,242,31744
Mvav 96,128,208
X 170
Mvav 128,128,216
D 4,20,94,0
K 212,168,169
Mvav 132,132,216
X 171
Mvav 100,132,208
D 5,20,94,1024
Mvav 68,132,200
X 172
Mvav 36,132,192
K 213,170,171
Mvav 40,136,192
X 173
Mvav 72,136,200
D 6,20,94,2048
Mvav 104,136,208
X 174
Mvav 136,136,216
D 7,20,94,3072
K 220,172,173
Mvav 140,140,216
X 175
Mvav 108,140,208
s_add_u32 s94, s94, 221184
X 176
Mvav 76,140,200
W 0
s_barrier
v_xor_b32 v242, 0x10000, v242
Mvav 44,140,192
Ra 128,242,0
K 221,174,175
Mvav 48,144,192
X 177
Mvav 80,144,200
v_bfe_u32 v11, v1, 4, 3
Mvav 112,144,208
X 178
Mvav 144,144,216
Ra 132,242,1024
v_lshlrev_b32 v11, 3, v11
Mvav 148,148,216
K 198,176,177
Mvav 116,148,208
X 179
Mvav 84,148,200
X 180
Mvav 52,148,192
Ra 136,242,2048
buffer_load_dwordx2 v[8:9], v11, s[8:11], 0 offen offset:3840 sc1
Mvav 56,152,192
Ra 140,242,3072
K 199,178,179
Mvav 88,152,200
X 181
Mvav 120,152,208
X 182
Mvav 152,152,216
K 206,180,181
Mvav 156,156,216
X 183
Mvav 124,156,208
Mvav 92,156,200
K 207,182,183
Mvav 60,156,192
W 0
Cvaa 160,128,0,224
s_add_u32 m0, s95, 16384
Ra 144,242,4096
Ra 148,242,5120
Mvaa 160,132,4
s_xor_b32 s62, s62, 0x10000
Ra 152,242,6144
Cvaa 164,132,20,228
X 184
Mvaa 164,128,16
X 185
Cvaa 168,128,32,232
X 186
Mvaa 168,132,36
D 4,20,94,0
K 214,184,185
Cvaa 172,132,52,236
Ra 156,242,7168
Mvaa 172,128,48
X 187
Mvaa 160,136,8
X 188
Mvaa 160,140,12
D 5,20,94,1024
Ra 128,242,16384
Mvaa 164,140,28
K 215,186,187
Mvaa 164,136,24
X 189
Mvaa 168,136,40
X 190
Mvaa 168,140,44
D 6,20,94,2048
Ra 132,242,17408
Mvaa 172,140,60
K 222,188,189
Mvaa 172,136,56
X 191
v_smfmac_f32_16x16x64_bf16 a[160:163], a[176:179], v[192:199], v240
Ra 136,242,18432
K 223,190,191
v_smfmac_f32_16x16x64_bf16 a[164:167], a[176:179], v[200:207], v240
D 7,20,94,3072
v_smfmac_f32_16x16x64_bf16 a[168:171], a[176:179], v[208:215], v240
s_add_u32 s94, s94, 221184
v_mul_u32_u24 v10, 160, v11
v_smfmac_f32_16x16x64_bf16 a[172:175], a[176:179], v[216:223], v240
W 3
X 160
Cvaa 176,144,0,224
X 161
Mvaa 176,148,4
Ra 140,242,19456
v_lshrrev_b32 v12, 7, v1
Cvaa 180,148,20,228
X 162
Mvaa 180,144,16
K 192,160,161
v_lshl_add_u32 v10, v12, 4, v10
Cvaa 184,144,32,232
s_mul_i32 s82, s14, 5120
X 163
Mvaa 184,148,36
X 164
Cvaa 188,148,52,236
K 193,162,163
Mvaa 188,144,48
s_mul_i32 s81, s14, 640
X 165
Mvaa 176,152,8
X 166
Mvaa 176,156,12
s_add_u32 s81, s81, 118784
Ra 144,242,20480
Ra 148,242,21504
Mvaa 180,156,28
K 200,164,165
Mvaa 180,152,24
X 167
Mvaa 184,152,40
s_waitcnt vmcnt(4)
v_cmp_eq_u64 vcc, s[12:13], v[8:9]
s_cmp_eq_u64 vcc, -1
s_cbranch_scc0 .Lspin_49%=
.Lok_50%=:
X 168
Mvaa 184,156,44
K 201,166,167
Mvaa 188,156,60
s_add_u32 m0, s82, 53248
X 169
Mvaa 188,152,56
W 2
Ra 152,242,22528
Mvav 32,128,196
X 170
Mvav 64,128,204
Ra 156,242,23552
Dc 10,8,81,0
Mvav 96,128,212
K 208,168,169
Mvav 128,128,220
X 171
Mvav 132,132,220
s_add_u32 m0, s82, 54144
X 172
Mvav 100,132,212
s_add_u32 s64, s64, 1
Ra 128,242,8192
K 209,170,171
Mvav 68,132,204
X 173
Mvav 36,132,196
Dc 10,8,81,128
Mvav 40,136,196
X 174
Mvav 72,136,204
s_add_u32 m0, s82, 55040
Ra 132,242,9216
K 216,172,173
Mvav 104,136,212
X 175
Mvav 136,136,220
Dc 10,8,81,256
Mvav 140,140,220
X 176
Mvav 108,140,212
Ra 136,242,10240
K 217,174,175
Mvav 76,140,204
s_add_u32 m0, s82, 55936
X 177
Mvav 44,140,196
W 3
Mvav 48,144,196
X 178
Mvav 80,144,204
Ra 140,242,11264
Dc 10,8,81,384
Mvav 112,144,212
K 194,176,177
Mvav 144,144,220
s_add_u32 m0, s82, 56832
X 179
Mvav 148,148,220
X 180
Mvav 116,148,212
Ra 144,242,12288
Mvav 84,148,204
Dc 10,8,81,512
K 195,178,179
Mvav 52,148,196
X 181
Mvav 56,152,196
X 182
Mvav 88,152,204
Ra 148,242,13312
Mvav 120,152,212
K 202,180,181
Mvav 152,152,220
X 183
Mvav 156,156,220
X 184
Mvav 124,156,212
Ra 152,242,14336
Mvav 92,156,204
K 203,182,183
Mvav 60,156,196
W 3
X 185
Cvaa 160,128,0,224
X 186
Mvaa 160,132,4
Ra 156,242,15360
Cvaa 164,132,20,228
K 210,184,185
Mvaa 164,128,16
X 187
Cvaa 168,128,32,232
X 188
Mvaa 168,132,36
Cvaa 172,132,52,236
K 211,186,187
Mvaa 172,128,48
X 189
Mvaa 160,136,8
Ra 128,242,24576
Ra 132,242,25600
Mvaa 160,140,12
X 190
Mvaa 164,140,28
K 218,188,189
Mvaa 164,136,24
X 191
Mvaa 168,136,40
Mvaa 168,140,44
Mvaa 172,140,60
X 160
Mvaa 172,136,56
W 2
Ra 136,242,26624
K 219,190,191
Cvaa 176,144,0,224
X 161
Mvaa 176,148,4
Ra 140,242,27648
v_lshrrev_b32 v8, 8, v1
Cvaa 180,148,20,228
X 162
Mvaa 180,144,16
K 196,160,161
Cvaa 184,144,32,232
X 163
Mvaa 184,148,36
v_bfe_u32 v9, v1, 6, 2
Cvaa 188,148,52,236
X 164
Mvaa 188,144,48
K 197,162,163
v_lshl_add_u32 v9, v8, 3, v9
Mvaa 176,152,8
X 165
Mvaa 176,156,12
Ra 144,242,28672
Ra 148,242,29696
Mvaa 180,156,28
X 166
Mvaa 180,152,24
K 204,164,165
v_lshlrev_b32 v11, 6, v9
Mvaa 184,152,40
X 167
Mvaa 184,156,44
X 168
Mvaa 188,156,60
v_xor_b32 v9, s14, v8
Mvaa 188,152,56
W 2
Ra 152,242,30720
K 205,166,167
Mvav 32,128,192
X 169
Mvav 64,128,200
Ra 156,242,31744
v_and_b32 v9, 1, v9
Mvav 96,128,208
X 170
Mvav 128,128,216
K 212,168,169
Mvav 132,132,216
X 171
Mvav 100,132,208
v_lshl_add_u32 v11, v9, 5, v11
Mvav 68,132,200
X 172
Mvav 36,132,192
K 213,170,171
Mvav 40,136,192
X 173
Mvav 72,136,200
v_bfe_u32 v9, v1, 4, 2
Mvav 104,136,208
X 174
Mvav 136,136,216
K 220,172,173
v_lshl_add_u32 v12, v9, 3, v11
Mvav 140,140,216
X 175
Mvav 108,140,208
X 176
Mvav 76,140,200
s_waitcnt vmcnt(0) lgkmcnt(0)
s_barrier
v_xor_b32 v242, 0x10000, v242
Mvav 44,140,192
v_add_u32 v11, 98304, v12
K 221,174,175
)"
R"(Mvav 48,144,192
Ra 128,242,0
Mvav 80,144,200
X 177
Mvav 112,144,208
X 178
Mvav 144,144,216
Ra 132,242,1024
v_and_b32 v13, 0x70, v1
Mvav 148,148,216
K 198,176,177
Mvav 116,148,208
X 179
Mvav 84,148,200
Ra 136,242,2048
v_lshl_add_u32 v13, v8, 7, v13
Mvav 52,148,192
X 180
Mvav 56,152,192
Ra 140,242,3072
K 199,178,179
Mvav 88,152,200
X 181
Mvav 120,152,208
X 182
Mvav 152,152,216
K 206,180,181
Mvav 156,156,216
X 183
Mvav 124,156,208
Mvav 92,156,200
K 207,182,183
Mvav 60,156,192
W 0
Cvaa 160,128,0,224
s_cmp_eq_u32 s14, 0
s_cselect_b32 s95, 106496, 0
Ra 144,242,4096
Ra 148,242,5120
Mvaa 160,132,4
s_xor_b32 s62, s62, 0x10000
X 184
Cvaa 164,132,20,228
Ta 184,11,0
Ta 186,11,256
Mvaa 164,128,16
s_add_u32 s95, s95, 32768
Ta 188,11,20480
Ta 190,11,20736
Cvaa 168,128,32,232
Ra 192,13,53248
Ta 196,11,2048
Mvaa 168,132,36
s_add_u32 m0, s95, 0
X 185
Cvaa 172,132,52,236
Ta 198,11,2304
Ta 200,11,22528
Mvaa 172,128,48
W 3
X 186
Zaaa 232,184,192
Ta 202,11,22784
K 214,184,185
Zaaa 236,188,192
Ra 204,13,53760
Ra 152,242,6144
Mvaa 160,136,8
X 187
Mvaa 160,140,12
Ta 208,11,4096
D 4,20,94,0
Mvaa 164,140,28
X 188
Mvaa 164,136,24
Ta 210,11,4352
D 5,20,94,1024
Mvaa 168,136,40
Ra 156,242,7168
K 215,186,187
Mvaa 168,140,44
X 189
Mvaa 172,140,60
Ta 212,11,24576
D 6,20,94,2048
Mvaa 172,136,56
X 190
v_smfmac_f32_16x16x64_bf16 a[160:163], a[176:179], v[192:199], v240
W 5
Ta 214,11,24832
D 7,20,94,3072
Maaa 232,196,204
s_add_u32 s94, s94, 221184
Ra 216,13,54272
K 222,188,189
Maaa 236,200,204
s_add_u32 m0, s95, 4096
X 191
v_smfmac_f32_16x16x64_bf16 a[164:167], a[176:179], v[200:207], v240
X 160
K 223,190,191
v_smfmac_f32_16x16x64_bf16 a[168:171], a[176:179], v[208:215], v240
Ra 128,242,16384
Ta 220,11,6144
v_smfmac_f32_16x16x64_bf16 a[172:175], a[176:179], v[216:223], v240
W 5
Ta 222,11,6400
Ra 132,242,17408
Cvaa 176,144,0,224
X 161
Mvaa 176,148,4
D 4,20,94,0
Ta 224,11,26624
Cvaa 180,148,20,228
X 162
Mvaa 180,144,16
D 5,20,94,1024
K 192,160,161
Cvaa 184,144,32,232
Ta 226,11,26880
Ra 228,13,54784
Mvaa 184,148,36
W 7
X 163
Maaa 232,208,216
D 6,20,94,2048
Ra 136,242,18432
Maaa 236,212,216
X 164
Cvaa 188,148,52,236
D 7,20,94,3072
Ta 184,11,8192
Mvaa 188,144,48
s_add_u32 s94, s94, 221184
Ra 140,242,19456
K 193,162,163
Mvaa 176,152,8
s_add_u32 m0, s95, 8192
X 165
Mvaa 176,156,12
Ta 186,11,8448
Ta 188,11,28672
Mvaa 180,156,28
X 166
Mvaa 180,152,24
Ra 144,242,20480
K 200,164,165
Mvaa 184,152,40
X 167
Mvaa 184,156,44
Ta 190,11,28928
D 4,20,94,0
Mvaa 188,156,60
W 7
X 168
Maaa 232,220,228
s_add_u32 s64, s64, 1
Ra 192,13,55296
D 5,20,94,1024
Maaa 236,224,228
Ta 196,11,10240
K 201,166,167
Mvaa 188,152,56
W 6
X 169
Mvav 32,128,196
D 6,20,94,2048
Ra 148,242,21504
Mvav 64,128,204
X 170
Mvav 96,128,212
Ta 198,11,10496
D 7,20,94,3072
Mvav 128,128,220
s_add_u32 s94, s94, 221184
Ta 200,11,30720
K 208,168,169
Mvav 132,132,220
s_add_u32 m0, s95, 12288
X 171
Mvav 100,132,212
Ra 152,242,22528
Ta 202,11,30976
Mvav 68,132,204
X 172
Mvav 36,132,196
W 6
Ra 204,13,55808
K 209,170,171
Maaa 232,184,192
X 173
Maaa 236,188,192
D 4,20,94,0
Ra 156,242,23552
Mvav 40,136,196
X 174
Mvav 72,136,204
Ta 208,11,12288
D 5,20,94,1024
Mvav 104,136,212
Ta 210,11,12544
K 216,172,173
Mvav 136,136,220
X 175
Mvav 140,140,220
D 6,20,94,2048
Ra 128,242,8192
Mvav 108,140,212
Ta 212,11,32768
Ta 214,11,33024
Mvav 76,140,204
X 176
Mvav 44,140,196
W 5
D 7,20,94,3072
K 217,174,175
Mvav 48,144,196
s_add_u32 s94, s94, 221184
X 177
Maaa 232,196,204
s_add_u32 m0, s95, 16384
Ra 132,242,9216
Ra 216,13,56320
Maaa 236,200,204
X 178
Mvav 80,144,204
Ta 220,11,14336
K 194,176,177
Mvav 112,144,212
X 179
Mvav 144,144,220
D 4,20,94,0
Ta 222,11,14592
Mvav 148,148,220
Ra 136,242,10240
Ta 224,11,34816
Mvav 116,148,212
X 180
Mvav 84,148,204
D 5,20,94,1024
K 195,178,179
Mvav 52,148,196
X 181
Mvav 56,152,196
D 6,20,94,2048
Ta 226,11,35072
Mvav 88,152,204
W 5
Ra 140,242,11264
Ra 228,13,56832
Maaa 232,208,216
X 182
Maaa 236,212,216
D 7,20,94,3072
K 202,180,181
Mvav 120,152,212
s_add_u32 s94, s94, 221184
X 183
Mvav 152,152,220
Ta 184,11,16384
Ra 144,242,12288
Mvav 156,156,220
X 184
Mvav 124,156,212
Ta 186,11,16640
K 203,182,183
Mvav 92,156,204
X 185
Mvav 60,156,196
W 4
Ta 188,11,36864
Ra 148,242,13312
Cvaa 160,128,0,224
X 186
Mvaa 160,132,4
Ta 190,11,37120
K 210,184,185
Cvaa 164,132,20,228
W 6
Ra 192,13,57344
Ta 196,11,18432
Maaa 232,220,228
X 187
Maaa 236,224,228
X 188
Mvaa 164,128,16
Ra 152,242,14336
K 211,186,187
Cvaa 168,128,32,232
Ta 198,11,18688
Ta 200,11,38912
Mvaa 168,132,36
X 189
Cvaa 172,132,52,236
X 190
Mvaa 172,128,48
Ra 156,242,15360
K 218,188,189
Mvaa 160,136,8
Ta 202,11,39168
Ra 204,13,57856
Mvaa 160,140,12
X 191
Mvaa 164,140,28
Ra 128,242,24576
Mvaa 164,136,24
W 8
Maaa 232,184,192
Ra 132,242,25600
Maaa 236,188,192
X 160
Mvaa 168,136,40
K 219,190,191
Mvaa 168,140,44
X 161
Mvaa 172,140,60
X 162
Mvaa 172,136,56
W 4
Ra 136,242,26624
K 196,160,161
Cvaa 176,144,0,224
X 163
Mvaa 176,148,4
X 164
Cvaa 180,148,20,228
Ra 140,242,27648
K 197,162,163
Mvaa 180,144,16
X 165
Cvaa 184,144,32,232
W 4
Maaa 232,196,204
X 166
Maaa 236,200,204
Mvaa 184,148,36
K 204,164,165
Cvaa 188,148,52,236
X 167
Mvaa 188,144,48
Ra 144,242,28672
Mvaa 176,152,8
X 168
Mvaa 176,156,12
Ra 148,242,29696
Mvaa 180,156,28
K 205,166,167
Mvaa 180,152,24
X 169
Mvaa 184,152,40
Mvaa 184,156,44
X 170
Mvaa 188,156,60
K 212,168,169
Mvaa 188,152,56
W 2
X 171
Mvav 32,128,192
Ra 152,242,30720
Ra 156,242,31744
Mvav 64,128,200
X 172
Mvav 96,128,208
Mvav 128,128,216
K 213,170,171
Mvav 132,132,216
X 173
Mvav 100,132,208
Mvav 68,132,200
X 174
Mvav 36,132,192
Mvav 40,136,192
K 220,172,173
Mvav 72,136,200
X 175
Mvav 104,136,208
Mvav 136,136,216
X 176
Mvav 140,140,216
Mvav 108,140,208
K 221,174,175
Mvav 76,140,200
s_waitcnt vmcnt(0) lgkmcnt(0)
s_barrier
v_xor_b32 v242, 0x10000, v242
Mvav 44,140,192
X 177
Mvav 48,144,192
X 178
Mvav 80,144,200
Ra 128,242,0
K 198,176,177
Mvav 112,144,208
X 179
Mvav 144,144,216
Ra 132,242,1024
Mvav 148,148,216
X 180
Mvav 116,148,208
Ra 136,242,2048
)"
R"(Mvav 84,148,200
K 199,178,179
Mvav 52,148,192
X 181
Mvav 56,152,192
Ra 140,242,3072
Mvav 88,152,200
X 182
Mvav 120,152,208
Mvav 152,152,216
K 206,180,181
Mvav 156,156,216
X 183
Mvav 124,156,208
Mvav 92,156,200
K 207,182,183
Mvav 60,156,192
W 0
Cvaa 160,128,0,224
s_mov_b32 s20, s34
Ra 144,242,4096
Ra 148,242,5120
Mvaa 160,132,4
s_xor_b32 s62, s62, 0x10000
X 184
Cvaa 164,132,20,228
s_and_b32 s21, s35, 0xffff
Ta 184,11,40960
Ta 186,11,41216
Mvaa 164,128,16
Ta 188,12,32768
Ta 190,12,33024
Cvaa 168,128,32,232
s_mov_b32 s22, 2211840
Ra 192,13,58368
Ta 196,11,43008
Mvaa 168,132,36
s_mov_b32 s23, 0x20000
X 185
Cvaa 172,132,52,236
Ta 198,11,43264
Ta 200,12,34816
Mvaa 172,128,48
W 3
X 186
Maaa 232,184,192
Ta 202,12,35072
K 214,184,185
Maaa 236,188,192
Ra 204,13,58880
Ra 152,242,6144
Mvaa 160,136,8
X 187
Mvaa 160,140,12
Ta 208,11,45056
v_lshrrev_b32 v4, 8, v1
Mvaa 164,140,28
X 188
Mvaa 164,136,24
Ta 210,11,45312
v_lshlrev_b32 v5, 5, v4
Mvaa 168,136,40
v_xor_b32 v5, v5, v1
K 215,186,187
Mvaa 168,140,44
Ra 156,242,7168
Ta 212,12,36864
Mvaa 172,140,60
X 189
Mvaa 172,136,56
v_and_b32 v5, 0xf0, v5
v_mul_u32_u24 v4, 1280, v4
v_smfmac_f32_16x16x64_bf16 a[160:163], a[176:179], v[192:199], v240
W 5
X 190
Maaa 232,196,204
Ta 214,12,37120
v_add_u32 v4, v4, v5
Maaa 236,200,204
Ra 216,13,59392
K 222,188,189
v_smfmac_f32_16x16x64_bf16 a[164:167], a[176:179], v[200:207], v240
X 191
X 160
K 223,190,191
v_smfmac_f32_16x16x64_bf16 a[168:171], a[176:179], v[208:215], v240
Ra 128,242,16384
v_xor_b32 v5, 128, v4
v_smfmac_f32_16x16x64_bf16 a[172:175], a[176:179], v[216:223], v240
W 4
Ta 220,11,47104
v_add_u32 v6, 8192, v4
Cvaa 176,144,0,224
Ta 222,11,47360
v_add_u32 v7, 12288, v5
Mvaa 176,148,4
Ra 132,242,17408
v_add_u32 v5, 4096, v5
Cvaa 180,148,20,228
s_mul_i32 s94, s85, 40960
X 161
Mvaa 180,144,16
s_mul_i32 s81, s14, 20480
Ta 224,12,38912
Ta 226,12,39168
Cvaa 184,144,32,232
s_add_u32 s94, s94, s81
X 162
Mvaa 184,148,36
W 6
s_lshl_b32 s95, s14, 12
K 192,160,161
Maaa 232,208,216
s_add_u32 s95, s95, 98304
X 163
Maaa 236,212,216
s_add_u32 m0, s95, 0
Ra 228,13,59904
Ra 136,242,18432
Cvaa 188,148,52,236
X 164
Mvaa 188,144,48
Ta 184,11,49152
K 193,162,163
Mvaa 176,152,8
X 165
Mvaa 176,156,12
Ra 140,242,19456
Ta 186,11,49408
Mvaa 180,156,28
D 4,20,94,0
Ta 188,12,40960
Mvaa 180,152,24
X 166
Mvaa 184,152,40
D 5,20,94,1024
K 200,164,165
Mvaa 184,156,44
X 167
Mvaa 188,156,60
W 5
D 6,20,94,2048
Ra 144,242,20480
Maaa 232,220,228
s_add_u32 s64, s64, 1
Ta 190,12,41216
Ra 192,13,60416
Maaa 236,224,228
D 7,20,94,3072
Ta 196,11,51200
Mvaa 188,152,56
W 6
X 168
Mvav 32,128,196
s_add_u32 s94, s94, 256
s_add_u32 m0, s95, 8192
K 201,166,167
Mvav 64,128,204
X 169
Mvav 96,128,212
D 4,20,94,0
Ra 148,242,21504
Mvav 128,128,220
X 170
Mvav 132,132,220
D 5,20,94,1024
Ta 198,11,51456
Mvav 100,132,212
Ta 200,12,43008
K 208,168,169
Mvav 68,132,204
D 6,20,94,2048
Ra 152,242,22528
Mvav 36,132,196
W 5
X 171
Maaa 232,184,192
D 7,20,94,3072
Ta 202,12,43264
Maaa 236,188,192
s_add_u32 s94, s94, 256
X 172
Mvav 40,136,196
s_add_u32 m0, s95, 16384
Ra 204,13,60928
K 209,170,171
Mvav 72,136,204
X 173
Mvav 104,136,212
Ra 156,242,23552
D 4,20,94,0
Mvav 136,136,220
X 174
Mvav 140,140,220
D 5,20,94,1024
Ta 208,11,53248
Mvav 108,140,212
Ta 210,11,53504
K 216,172,173
Mvav 76,140,204
D 6,20,94,2048
Ra 128,242,8192
Mvav 44,140,196
W 3
X 175
Mvav 48,144,196
D 7,20,94,3072
Ta 212,12,45056
Maaa 232,196,204
s_add_u32 s94, s94, 256
X 176
Maaa 236,200,204
s_add_u32 m0, s95, 24576
Ta 214,12,45312
K 217,174,175
Mvav 80,144,204
D 4,20,94,0
Ra 132,242,9216
Mvav 112,144,212
X 177
Mvav 144,144,220
Ra 216,13,61440
D 5,20,94,1024
Mvav 148,148,220
X 178
Mvav 116,148,212
D 6,20,94,2048
Ta 220,11,55296
Mvav 84,148,204
Ta 222,11,55552
K 194,176,177
Mvav 52,148,196
D 7,20,94,3072
Ra 136,242,10240
Mvav 56,152,196
s_add_u32 s94, s94, 256
X 179
Mvav 88,152,204
W 3
X 180
Maaa 232,208,216
s_add_u32 m0, s95, 32768
Ta 224,12,47104
K 195,178,179
Maaa 236,212,216
D 4,20,94,0
Ta 226,12,47360
Mvav 120,152,212
Ra 140,242,11264
Ra 228,13,61952
Mvav 152,152,220
X 181
Mvav 156,156,220
D 5,20,94,1024
Ta 184,11,57344
Mvav 124,156,212
X 182
Mvav 92,156,204
D 6,20,94,2048
K 202,180,181
Mvav 60,156,196
W 2
X 183
Cvaa 160,128,0,224
D 7,20,94,3072
Ra 144,242,12288
Mvaa 160,132,4
s_add_u32 s94, s94, 256
X 184
Cvaa 164,132,20,228
W 2
Ta 186,11,57600
K 203,182,183
Maaa 232,220,228
X 185
Maaa 236,224,228
Ta 188,12,49152
Ra 148,242,13312
Mvaa 164,128,16
X 186
Cvaa 168,128,32,232
Ta 190,12,49408
K 210,184,185
Mvaa 168,132,36
Ra 192,13,62464
Ta 196,11,59392
Cvaa 172,132,52,236
X 187
Mvaa 172,128,48
X 188
Mvaa 160,136,8
Ra 152,242,14336
K 211,186,187
Mvaa 160,140,12
Ta 198,11,59648
Ta 200,12,51200
Mvaa 164,140,28
X 189
Mvaa 164,136,24
W 4
X 190
Maaa 232,184,192
Ra 156,242,15360
K 218,188,189
Maaa 236,188,192
Ta 202,12,51456
Ra 204,13,62976
Mvaa 168,136,40
X 191
Mvaa 168,140,44
Ra 128,242,24576
Ra 132,242,25600
Mvaa 172,140,60
X 160
Mvaa 172,136,56
W 4
Ra 136,242,26624
K 219,190,191
Cvaa 176,144,0,224
X 161
Mvaa 176,148,4
X 162
Cvaa 180,148,20,228
Ra 140,242,27648
K 196,160,161
Mvaa 180,144,16
X 163
Cvaa 184,144,32,232
W 4
X 164
Maaa 232,196,204
K 197,162,163
Maaa 236,200,204
X 165
Mvaa 184,148,36
X 166
Cvaa 188,148,52,236
K 204,164,165
Mvaa 188,144,48
X 167
Mvaa 176,152,8
Ra 144,242,28672
G 252,232
Mvaa 176,156,12
G 253,233
G 254,234
Mvaa 180,156,28
X 168
Mvaa 180,152,24
G 255,235
G 8,236
Mvaa 184,152,40
Ra 148,242,29696
G 9,237
Mvaa 184,156,44
G 10,238
G 14,239
Mvaa 188,156,60
K 252,252,8
K 253,253,9
Mvaa 188,152,56
W 2
K 205,166,167
K 254,254,10
Mvav 32,128,192
K 255,255,14
v_and_b32 v8, 0xffff0000, v252
Mvav 64,128,200
v_and_b32 v9, 0xffff0000, v253
v_and_b32 v10, 0xffff0000, v254
Mvav 96,128,208
X 169
Mvav 128,128,216
v_and_b32 v14, 0xffff0000, v255
v_lshlrev_b32 v252, 16, v252
Mvav 132,132,216
v_lshlrev_b32 v253, 16, v253
v_lshlrev_b32 v254, 16, v254
Mvav 100,132,208
v_lshlrev_b32 v255, 16, v255
v_mul_f32 v15, 0xbfb8aa3b, v252
Mvav 68,132,200
v_mul_f32 v2, 0xbfb8aa3b, v253
v_mul_f32 v3, 0xbfb8aa3b, v254
Mvav 36,132,192
X 170
Mvav 40,136,192
v_mul_f32 v27, 0xbfb8aa3b, v255
K 212,168,169
Mvav 72,136,200
X 15
Mvav 104,136,208
Ra 152,242,30720
Ra 156,242,31744
Mvav 136,136,216
X 2
X 3
X 27
v_add_f32 v15, 1.0, v15
v_add_f32 v2, 1.0, v2
)"
R"(v_add_f32 v3, 1.0, v3
v_add_f32 v27, 1.0, v27
v_rcp_f32 v15, v15
v_rcp_f32 v2, v2
v_rcp_f32 v3, v3
v_rcp_f32 v27, v27
U 252,252,15
U 253,253,2
U 254,254,3
U 255,255,27
K 252,252,252
K 253,253,253
K 254,254,254
K 255,255,255
v_lshlrev_b32 v252, 16, v252
v_lshlrev_b32 v253, 16, v253
v_lshlrev_b32 v254, 16, v254
v_lshlrev_b32 v255, 16, v255
U 252,252,8
U 253,253,9
U 254,254,10
U 255,255,14
K 246,252,253
K 247,254,255
s_lshl_b32 s81, s14, 3
v_add_u32 v2, s81, v1
s_mov_b32 s86, 0x00ff00ff
s_mov_b32 s87, 0x00ff00ff
v_cndmask_b32_e64 v252, 0, v246, s[86:87]
v_cndmask_b32_e64 v253, 0, v247, s[86:87]
v_cndmask_b32_e64 v246, v246, 0, s[86:87]
v_cndmask_b32_e64 v247, v247, 0, s[86:87]
ds_write_b64 v2, v[252:253] offset:63488
ds_write_b64 v2, v[246:247] offset:64512
Mvav 140,140,216
X 171
Mvav 108,140,208
X 172
Mvav 76,140,200
s_waitcnt vmcnt(0) lgkmcnt(0)
s_barrier
v_xor_b32 v242, 0x10000, v242
Mvav 44,140,192
Ra 128,242,0
K 213,170,171
Mvav 48,144,192
X 173
Mvav 80,144,200
X 174
Mvav 112,144,208
Ra 132,242,1024
K 220,172,173
Mvav 144,144,216
X 175
Mvav 148,148,216
X 176
Mvav 116,148,208
Ra 136,242,2048
K 221,174,175
Mvav 84,148,200
X 177
Mvav 52,148,192
X 178
Mvav 56,152,192
Ra 140,242,3072
K 198,176,177
Mvav 88,152,200
X 179
Mvav 120,152,208
X 180
Mvav 152,152,216
K 199,178,179
Mvav 156,156,216
X 181
Mvav 124,156,208
X 182
Mvav 92,156,200
K 206,180,181
Mvav 60,156,192
X 183
s_nop 0
K 207,182,183
W 0
Cvaa 160,128,0,224
Ra 144,242,4096
Ra 148,242,5120
Mvaa 160,132,4
s_xor_b32 s62, s62, 0x10000
X 184
Cvaa 164,132,20,228
Ra 216,1,63488
Ra 220,1,64512
Mvaa 164,128,16
v_lshrrev_b32 v8, 6, v1
v_and_b32 v9, 7, v8
Cvaa 168,128,32,232
s_lshl_b32 s81, s14, 2
X 185
Mvaa 168,132,36
v_xor_b32 v9, s81, v9
v_lshlrev_b32 v9, 5, v9
Cvaa 172,132,52,236
X 186
Mvaa 172,128,48
v_lshl_add_u32 v4, v8, 8, v9
v_bfe_u32 v9, v1, 4, 2
Mvaa 160,136,8
v_lshl_add_u32 v4, v9, 3, v4
v_add_u32 v4, 98304, v4
Mvaa 160,140,12
K 214,184,185
v_xor_b32 v5, 32, v4
Mvaa 164,140,28
v_xor_b32 v6, 64, v4
v_xor_b32 v7, 96, v4
Ta 184,4,0
Ta 186,4,4096
Mvaa 164,136,24
Ta 188,5,0
Ta 190,5,4096
Mvaa 168,136,40
Ra 152,242,6144
Ta 192,6,0
Mvaa 168,140,44
W 4
X 187
Zaaa 232,184,216
Ta 194,6,4096
v_and_b32 v11, 0x70, v1
Mvaa 172,140,60
v_mul_u32_u24 v11, 160, v11
v_lshrrev_b32 v10, 8, v1
Mvaa 172,136,56
X 188
v_smfmac_f32_16x16x64_bf16 a[160:163], a[176:179], v[192:199], v240
Ra 156,242,7168
W 4
Maaa 232,188,220
K 215,186,187
v_lshl_add_u32 v11, v10, 4, v11
v_smfmac_f32_16x16x64_bf16 a[164:167], a[176:179], v[200:207], v240
X 189
X 190
K 222,188,189
X 191
X 160
K 223,190,191
v_smfmac_f32_16x16x64_bf16 a[168:171], a[176:179], v[208:215], v240
Ta 196,7,0
v_bfe_u32 v10, v1, 7, 1
v_smfmac_f32_16x16x64_bf16 a[172:175], a[176:179], v[216:223], v240
W 2
Ta 198,7,4096
v_lshl_add_u32 v10, v10, 6, v11
Zaaa 236,192,216
W 2
s_mul_i32 s94, s85, 20480
s_lshl_b32 s82, s14, 8
Cvaa 176,144,0,224
s_add_u32 s94, s94, s82
Ta 200,4,8192
Ta 202,4,12288
Mvaa 176,148,4
s_add_u32 s94, s94, 2752512
s_lshl_b32 s71, s85, 4
Ra 128,242,16384
Cvaa 180,148,20,228
W 3
s_lshl_b32 s82, s14, 3
Ta 204,5,8192
Maaa 236,196,220
s_add_u32 s71, s71, s82
s_add_u32 s71, s71, 2560
Ta 206,5,12288
Mvaa 180,144,16
X 161
Cvaa 184,144,32,232
Ra 132,242,17408
Ta 208,6,8192
Mvaa 184,148,36
W 5
X 162
Zaaa 240,200,216
Ta 210,6,12288
K 192,160,161
Cvaa 188,148,52,236
buffer_store_dwordx4 a[232:235], v10, s[8:11], s94 offen offset:0 sc1
Ta 212,7,8192
Mvaa 188,144,48
X 163
Mvaa 176,152,8
W 4
X 164
Maaa 240,204,220
Ra 136,242,18432
Ta 214,7,12288
Mvaa 176,156,12
Ta 184,4,16384
K 193,162,163
Mvaa 180,156,28
X 165
Mvaa 180,152,24
W 4
Ta 186,4,20480
Ra 140,242,19456
Zaaa 244,208,216
X 166
Mvaa 184,152,40
buffer_store_dwordx4 a[236:239], v10, s[8:11], s94 offen offset:128 sc1
K 200,164,165
Mvaa 184,156,44
X 167
Mvaa 188,156,60
W 3
Ta 188,5,16384
Ta 190,5,20480
Maaa 244,212,220
s_add_u32 s64, s64, 1
X 168
Mvaa 188,152,56
W 2
Ra 144,242,20480
K 201,166,167
Mvav 32,128,196
Ta 192,6,16384
Ta 194,6,20480
Mvav 64,128,204
X 169
Zaaa 248,184,216
buffer_store_dwordx4 a[240:243], v10, s[8:11], s94 offen offset:512 sc1
Ta 196,7,16384
Mvav 96,128,212
X 170
Mvav 128,128,220
Ra 148,242,21504
K 208,168,169
Mvav 132,132,220
W 5
Ta 198,7,20480
Ta 200,4,24576
Maaa 248,188,220
X 171
Mvav 100,132,212
X 172
Mvav 68,132,204
Ta 202,4,28672
K 209,170,171
Mvav 36,132,196
W 5
Ra 152,242,22528
buffer_store_dwordx4 a[244:247], v10, s[8:11], s94 offen offset:640 sc1
Zaaa 252,192,216
X 173
Mvav 40,136,196
X 174
Mvav 72,136,204
Ta 204,5,24576
Ta 206,5,28672
Mvav 104,136,212
W 5
Ra 156,242,23552
K 216,172,173
Maaa 252,196,220
X 175
Mvav 136,136,220
Ta 208,6,24576
Ta 210,6,28672
Mvav 140,140,220
X 176
Mvav 108,140,212
W 6
buffer_store_dwordx4 a[248:251], v10, s[8:11], s94 offen offset:1024 sc1
K 217,174,175
Zaaa 232,200,216
X 177
Mvav 76,140,204
Ta 212,7,24576
Ra 128,242,8192
Mvav 44,140,196
W 4
Ta 214,7,28672
Ta 184,4,32768
Mvav 48,144,196
X 178
Maaa 232,204,220
Ra 132,242,9216
K 194,176,177
Mvav 80,144,204
X 179
Mvav 112,144,212
Ta 186,4,36864
buffer_store_dwordx4 a[252:255], v10, s[8:11], s94 offen offset:1152 sc1
Mvav 144,144,220
W 6
X 180
Zaaa 236,208,216
Ta 188,5,32768
K 195,178,179
Mvav 148,148,220
X 181
Mvav 116,148,212
Ta 190,5,36864
Ra 136,242,10240
Mvav 84,148,204
W 6
X 182
Maaa 236,212,220
Ta 192,6,32768
K 202,180,181
Mvav 52,148,196
Ta 194,6,36864
Ra 140,242,11264
Mvav 56,152,196
X 183
Mvav 88,152,204
W 6
buffer_store_dwordx4 a[232:235], v10, s[8:11], s94 offen offset:1536 sc1
Ta 196,7,32768
Zaaa 240,184,216
X 184
Mvav 120,152,212
Ta 198,7,36864
K 203,182,183
Mvav 152,152,220
Ra 144,242,12288
Ra 148,242,13312
Mvav 156,156,220
W 8
X 185
Maaa 240,188,220
X 186
Mvav 124,156,212
buffer_store_dwordx4 a[236:239], v10, s[8:11], s94 offen offset:1664 sc1
K 210,184,185
Mvav 92,156,204
X 187
Mvav 60,156,196
W 5
X 188
Zaaa 244,192,216
W 4
Ra 152,242,14336
K 211,186,187
Cvaa 160,128,0,224
X 189
Mvaa 160,132,4
X 190
Cvaa 164,132,20,228
W 3
Ra 156,242,15360
buffer_store_dwordx4 a[240:243], v10, s[8:11], s94 offen offset:2048 sc1
Maaa 244,196,220
K 218,188,189
Mvaa 164,128,16
X 191
Cvaa 168,128,32,232
Mvaa 168,132,36
Cvaa 172,132,52,236
Mvaa 172,128,48
Ra 128,242,24576
Mvaa 160,136,8
Ra 132,242,25600
buffer_store_dwordx4 a[244:247], v10, s[8:11], s94 offen offset:2176 sc1
Mvaa 160,140,12
Mvaa 164,140,28
Mvaa 164,136,24
Mvaa 168,136,40
Mvaa 168,140,44
Mvaa 172,140,60
Mvaa 172,136,56
W 2
Ra 136,242,26624
Cvaa 176,144,0,224
X 160
Mvaa 176,148,4
Ra 140,242,27648
K 219,190,191
Cvaa 180,148,20,228
X 161
Mvaa 180,144,16
X 162
Cvaa 184,144,32,232
K 196,160,161
Mvaa 184,148,36
X 163
Cvaa 188,148,52,236
X 164
Mvaa 188,144,48
Ra 144,242,28672
K 197,162,163
Mvaa 176,152,8
X 165
Mvaa 176,156,12
X 166
Mvaa 180,156,28
Ra 148,242,29696
K 204,164,165
Mvaa 180,152,24
X 167
Mvaa 184,152,40
)"
R"(X 168
Mvaa 184,156,44
K 205,166,167
Mvaa 188,156,60
X 169
Mvaa 188,152,56
W 2
X 170
Mvav 32,128,192
Ra 152,242,30720
K 212,168,169
Mvav 64,128,200
X 171
Mvav 96,128,208
Ra 156,242,31744
Mvav 128,128,216
X 172
Mvav 132,132,216
K 213,170,171
Mvav 100,132,208
X 173
Mvav 68,132,200
X 174
Mvav 36,132,192
Mvav 40,136,192
K 220,172,173
Mvav 72,136,200
X 175
Mvav 104,136,208
Mvav 136,136,216
X 176
Mvav 140,140,216
Mvav 108,140,208
K 221,174,175
Mvav 76,140,200
W 0
s_barrier
v_xor_b32 v242, 0x10000, v242
Mvav 44,140,192
X 177
Mvav 48,144,192
X 178
Mvav 80,144,200
Ra 128,242,0
K 198,176,177
Mvav 112,144,208
X 179
Mvav 144,144,216
Ra 132,242,1024
Mvav 148,148,216
X 180
Mvav 116,148,208
Ra 136,242,2048
Mvav 84,148,200
K 199,178,179
Mvav 52,148,192
X 181
Mvav 56,152,192
Ra 140,242,3072
Mvav 88,152,200
X 182
Mvav 120,152,208
Mvav 152,152,216
K 206,180,181
Mvav 156,156,216
X 183
Mvav 124,156,208
Mvav 92,156,200
s_waitcnt vmcnt(0)
v_mov_b64 v[28:29], s[12:13]
s_mov_b64 exec, 1
buffer_store_dwordx2 v[28:29], off, s[8:11], s71 sc1
s_mov_b64 exec, -1
K 207,182,183
Mvav 60,156,192
s_mov_b32 s85, 99
s_lshl_b32 s65, s57, 1
s_sub_u32 s65, s65, 1
s_branch .Ll0t_44%=
.Lf1r_45%=:
s_lshl_b32 s92, s58, 2
s_add_u32 s91, s92, s51
s_add_u32 s95, s92, 0
s_and_b32 s95, s95, 15
s_mul_i32 s95, s95, 40960
s_mul_i32 s71, s51, 0
s_add_u32 s71, s71, 133120
s_add_u32 m0, s71, s48
s_lshl2_add_u32 s94, s48, s95
D 16,40,94,0
s_add_u32 s95, s92, 1
s_and_b32 s95, s95, 15
s_mul_i32 s95, s95, 40960
s_mul_i32 s71, s51, 0
s_add_u32 s71, s71, 143360
D 17,40,94,1024
D 18,40,94,2048
D 19,40,94,3072
s_add_u32 m0, m0, 4096
s_nop 0
buffer_load_dwordx4 v20, s[40:43], s94 offen lds
s_add_u32 m0, s71, s48
s_lshl2_add_u32 s94, s48, s95
D 16,40,94,0
s_add_u32 s95, s92, 2
s_and_b32 s95, s95, 15
s_mul_i32 s95, s95, 40960
s_mul_i32 s71, s51, 0
s_add_u32 s71, s71, 153600
D 17,40,94,1024
D 18,40,94,2048
D 19,40,94,3072
s_add_u32 m0, m0, 4096
s_nop 0
buffer_load_dwordx4 v20, s[40:43], s94 offen lds
s_add_u32 m0, s71, s48
s_lshl2_add_u32 s94, s48, s95
D 16,40,94,0
D 17,40,94,1024
D 18,40,94,2048
D 19,40,94,3072
s_add_u32 m0, m0, 4096
s_nop 0
buffer_load_dwordx4 v20, s[40:43], s94 offen lds
s_branch .Lf1_38%=
.Lspin_13%=:
s_mov_b32 s84, 0
.Llp_15%=:
s_sleep 2
v_lshrrev_b32 v28, 1, v1
s_lshl_b32 s80, s60, 9
s_add_u32 s80, s80, 40960
buffer_load_dwordx2 v[28:29], v28, s[8:11], s80 offen sc1
s_waitcnt vmcnt(0)
s_add_u32 s84, s84, 1
v_cmp_eq_u32 vcc, s13, v29
s_cmp_eq_u64 vcc, -1
s_cbranch_scc1 .Lok_14%=
s_cmp_lt_u32 s84, 1048576
s_cbranch_scc1 .Llp_15%=
s_branch .Lok_14%=
.Lspin_18%=:
s_mov_b32 s84, 0
.Llp_20%=:
s_sleep 2
v_lshrrev_b32 v28, 1, v1
s_lshl_b32 s80, s60, 9
s_add_u32 s80, s80, 40960
buffer_load_dwordx2 v[28:29], v28, s[8:11], s80 offen sc1
s_waitcnt vmcnt(0)
s_add_u32 s84, s84, 1
v_cmp_eq_u32 vcc, s13, v29
s_cmp_eq_u64 vcc, -1
s_cbranch_scc1 .Lok_19%=
s_cmp_lt_u32 s84, 1048576
s_cbranch_scc1 .Llp_20%=
s_branch .Lok_19%=
.Lrt_21%=:
s_waitcnt vmcnt(0)
s_mov_b32 s86, 40960
buffer_load_dwordx4 v[32:35], v1, s[8:11], s86 offen offset:0 sc1
buffer_load_dwordx4 v[36:39], v1, s[8:11], s86 offen offset:1024 sc1
buffer_load_dwordx4 v[40:43], v1, s[8:11], s86 offen offset:2048 sc1
buffer_load_dwordx4 v[44:47], v1, s[8:11], s86 offen offset:3072 sc1
s_mov_b32 s86, 45056
buffer_load_dwordx4 v[48:51], v1, s[8:11], s86 offen offset:0 sc1
buffer_load_dwordx4 v[52:55], v1, s[8:11], s86 offen offset:1024 sc1
buffer_load_dwordx4 v[56:59], v1, s[8:11], s86 offen offset:2048 sc1
buffer_load_dwordx4 v[60:63], v1, s[8:11], s86 offen offset:3072 sc1
s_mov_b32 s86, 49152
buffer_load_dwordx4 v[64:67], v1, s[8:11], s86 offen offset:0 sc1
buffer_load_dwordx4 v[68:71], v1, s[8:11], s86 offen offset:1024 sc1
buffer_load_dwordx4 v[72:75], v1, s[8:11], s86 offen offset:2048 sc1
buffer_load_dwordx4 v[76:79], v1, s[8:11], s86 offen offset:3072 sc1
s_mov_b32 s86, 53248
buffer_load_dwordx4 v[80:83], v1, s[8:11], s86 offen offset:0 sc1
buffer_load_dwordx4 v[84:87], v1, s[8:11], s86 offen offset:1024 sc1
buffer_load_dwordx4 v[88:91], v1, s[8:11], s86 offen offset:2048 sc1
buffer_load_dwordx4 v[92:95], v1, s[8:11], s86 offen offset:3072 sc1
s_branch .Lrtr_22%=
.Lrt_23%=:
v_lshrrev_b32 v136, 1, v1
s_mov_b32 s84, 0
.LL_25%=:
s_waitcnt vmcnt(0)
v_min3_u32 v96, v33, v35, v95
v_max3_u32 v97, v33, v35, v95
v_min3_u32 v96, v96, v37, v39
v_max3_u32 v97, v97, v37, v39
v_min3_u32 v96, v96, v41, v43
v_max3_u32 v97, v97, v41, v43
v_min3_u32 v96, v96, v45, v47
v_max3_u32 v97, v97, v45, v47
v_min3_u32 v96, v96, v49, v51
v_max3_u32 v97, v97, v49, v51
v_min3_u32 v96, v96, v53, v55
v_max3_u32 v97, v97, v53, v55
v_min3_u32 v96, v96, v57, v59
v_max3_u32 v97, v97, v57, v59
v_min3_u32 v96, v96, v61, v63
v_max3_u32 v97, v97, v61, v63
v_min3_u32 v96, v96, v65, v67
v_max3_u32 v97, v97, v65, v67
v_min3_u32 v96, v96, v69, v71
v_max3_u32 v97, v97, v69, v71
v_min3_u32 v96, v96, v73, v75
v_max3_u32 v97, v97, v73, v75
v_min3_u32 v96, v96, v77, v79
v_max3_u32 v97, v97, v77, v79
v_min3_u32 v96, v96, v81, v83
v_max3_u32 v97, v97, v81, v83
v_min3_u32 v96, v96, v85, v87
v_max3_u32 v97, v97, v85, v87
v_min3_u32 v96, v96, v89, v91
v_max3_u32 v97, v97, v89, v91
v_min_u32 v96, v96, v93
v_max_u32 v97, v97, v93
v_cmp_eq_u32 vcc, s13, v96
v_cmp_eq_u32_e64 s[86:87], s13, v97
s_and_b64 vcc, vcc, s[86:87]
s_cmp_eq_u64 vcc, -1
s_cbranch_scc1 .LL_26%=
s_sleep 2
s_mov_b32 s86, 40960
buffer_load_dwordx4 v[32:35], v1, s[8:11], s86 offen offset:0 sc1
buffer_load_dwordx4 v[36:39], v1, s[8:11], s86 offen offset:1024 sc1
buffer_load_dwordx4 v[40:43], v1, s[8:11], s86 offen offset:2048 sc1
buffer_load_dwordx4 v[44:47], v1, s[8:11], s86 offen offset:3072 sc1
s_mov_b32 s86, 45056
buffer_load_dwordx4 v[48:51], v1, s[8:11], s86 offen offset:0 sc1
buffer_load_dwordx4 v[52:55], v1, s[8:11], s86 offen offset:1024 sc1
buffer_load_dwordx4 v[56:59], v1, s[8:11], s86 offen offset:2048 sc1
buffer_load_dwordx4 v[60:63], v1, s[8:11], s86 offen offset:3072 sc1
s_mov_b32 s86, 49152
buffer_load_dwordx4 v[64:67], v1, s[8:11], s86 offen offset:0 sc1
buffer_load_dwordx4 v[68:71], v1, s[8:11], s86 offen offset:1024 sc1
buffer_load_dwordx4 v[72:75], v1, s[8:11], s86 offen offset:2048 sc1
buffer_load_dwordx4 v[76:79], v1, s[8:11], s86 offen offset:3072 sc1
s_mov_b32 s86, 53248
buffer_load_dwordx4 v[80:83], v1, s[8:11], s86 offen offset:0 sc1
buffer_load_dwordx4 v[84:87], v1, s[8:11], s86 offen offset:1024 sc1
buffer_load_dwordx4 v[88:91], v1, s[8:11], s86 offen offset:2048 sc1
buffer_load_dwordx4 v[92:95], v1, s[8:11], s86 offen offset:3072 sc1
s_add_u32 s84, s84, 1
s_cmp_lt_u32 s84, 1048576
s_cbranch_scc1 .LL_25%=
s_waitcnt vmcnt(0)
.LL_26%=:
v_mov_b32 v96, v32
v_mov_b32 v97, 0
v_mov_b32 v98, 0
v_mov_b32 v100, 0
v_med3_u32 v98, v97, v98, v34
v_med3_u32 v97, v96, v97, v34
v_max_u32 v96, v96, v34
v_med3_u32 v98, v97, v98, v36
v_med3_u32 v97, v96, v97, v36
v_max_u32 v96, v96, v36
v_med3_u32 v98, v97, v98, v38
v_med3_u32 v97, v96, v97, v38
v_max_u32 v96, v96, v38
v_med3_u32 v98, v97, v98, v40
v_med3_u32 v97, v96, v97, v40
v_max_u32 v96, v96, v40
v_med3_u32 v98, v97, v98, v42
v_med3_u32 v97, v96, v97, v42
v_max_u32 v96, v96, v42
v_med3_u32 v98, v97, v98, v44
v_med3_u32 v97, v96, v97, v44
v_max_u32 v96, v96, v44
v_med3_u32 v98, v97, v98, v46
v_med3_u32 v97, v96, v97, v46
v_max_u32 v96, v96, v46
v_med3_u32 v98, v97, v98, v48
v_med3_u32 v97, v96, v97, v48
v_max_u32 v96, v96, v48
v_med3_u32 v98, v97, v98, v50
v_med3_u32 v97, v96, v97, v50
v_max_u32 v96, v96, v50
v_med3_u32 v98, v97, v98, v52
v_med3_u32 v97, v96, v97, v52
v_max_u32 v96, v96, v52
v_med3_u32 v98, v97, v98, v54
v_med3_u32 v97, v96, v97, v54
v_max_u32 v96, v96, v54
v_med3_u32 v98, v97, v98, v56
v_med3_u32 v97, v96, v97, v56
v_max_u32 v96, v96, v56
v_med3_u32 v98, v97, v98, v58
v_med3_u32 v97, v96, v97, v58
v_max_u32 v96, v96, v58
v_med3_u32 v98, v97, v98, v60
v_med3_u32 v97, v96, v97, v60
v_max_u32 v96, v96, v60
v_med3_u32 v98, v97, v98, v62
v_med3_u32 v97, v96, v97, v62
v_max_u32 v96, v96, v62
v_med3_u32 v98, v97, v98, v64
v_med3_u32 v97, v96, v97, v64
v_max_u32 v96, v96, v64
v_med3_u32 v98, v97, v98, v66
v_med3_u32 v97, v96, v97, v66
v_max_u32 v96, v96, v66
v_med3_u32 v98, v97, v98, v68
v_med3_u32 v97, v96, v97, v68
v_max_u32 v96, v96, v68
v_med3_u32 v98, v97, v98, v70
v_med3_u32 v97, v96, v97, v70
v_max_u32 v96, v96, v70
v_med3_u32 v98, v97, v98, v72
v_med3_u32 v97, v96, v97, v72
v_max_u32 v96, v96, v72
v_med3_u32 v98, v97, v98, v74
v_med3_u32 v97, v96, v97, v74
v_max_u32 v96, v96, v74
v_med3_u32 v98, v97, v98, v76
v_med3_u32 v97, v96, v97, v76
v_max_u32 v96, v96, v76
v_med3_u32 v98, v97, v98, v78
v_med3_u32 v97, v96, v97, v78
v_max_u32 v96, v96, v78
v_med3_u32 v98, v97, v98, v80
v_med3_u32 v97, v96, v97, v80
v_max_u32 v96, v96, v80
v_med3_u32 v98, v97, v98, v82
v_med3_u32 v97, v96, v97, v82
v_max_u32 v96, v96, v82
v_med3_u32 v98, v97, v98, v84
v_med3_u32 v97, v96, v97, v84
v_max_u32 v96, v96, v84
v_med3_u32 v98, v97, v98, v86
v_med3_u32 v97, v96, v97, v86
v_max_u32 v96, v96, v86
v_med3_u32 v98, v97, v98, v88
v_med3_u32 v97, v96, v97, v88
v_max_u32 v96, v96, v88
v_med3_u32 v98, v97, v98, v90
v_med3_u32 v97, v96, v97, v90
v_max_u32 v96, v96, v90
v_med3_u32 v98, v97, v98, v92
v_med3_u32 v97, v96, v97, v92
v_max_u32 v96, v96, v92
v_med3_u32 v98, v97, v98, v94
v_med3_u32 v97, v96, v97, v94
v_max_u32 v96, v96, v94
v_mov_b32 v99, v96
s_nop 1
v_max_u32_dpp v99, v99, v99 row_shr:1 row_mask:0xf bank_mask:0xf
s_nop 1
v_max_u32_dpp v99, v99, v99 row_shr:2 row_mask:0xf bank_mask:0xf
s_nop 1
v_max_u32_dpp v99, v99, v99 row_shr:4 row_mask:0xf bank_mask:0xf
s_nop 1
v_max_u32_dpp v99, v99, v99 row_shr:8 row_mask:0xf bank_mask:0xf
s_nop 1
v_max_u32_dpp v99, v99, v99 row_bcast:15 row_mask:0xa bank_mask:0xf
s_nop 1
v_max_u32_dpp v99, v99, v99 row_bcast:31 row_mask:0xc bank_mask:0xf
s_nop 0
v_readlane_b32 s86, v99, 63
s_nop 1
v_cmp_eq_u32 vcc, s86, v96
s_and_b32 s87, s86, 0xffff
s_sub_u32 s87, 2047, s87
v_cndmask_b32 v96, v96, v97, vcc
v_cndmask_b32_e64 v97, v97, 0, vcc
v_writelane_b32 v100, s87, 0
v_mov_b32 v99, v96
s_nop 1
v_max_u32_dpp v99, v99, v99 row_shr:1 row_mask:0xf bank_mask:0xf
s_nop 1
v_max_u32_dpp v99, v99, v99 row_shr:2 row_mask:0xf bank_mask:0xf
s_nop 1
v_max_u32_dpp v99, v99, v99 row_shr:4 row_mask:0xf bank_mask:0xf
s_nop 1
v_max_u32_dpp v99, v99, v99 row_shr:8 row_mask:0xf bank_mask:0xf
s_nop 1
v_max_u32_dpp v99, v99, v99 row_bcast:15 row_mask:0xa bank_mask:0xf
s_nop 1
v_max_u32_dpp v99, v99, v99 row_bcast:31 row_mask:0xc bank_mask:0xf
s_nop 0
v_readlane_b32 s86, v99, 63
s_nop 1
v_cmp_eq_u32 vcc, s86, v96
s_and_b32 s87, s86, 0xffff
s_sub_u32 s87, 2047, s87
v_cndmask_b32 v96, v96, v97, vcc
v_cndmask_b32_e64 v97, v97, 0, vcc
v_writelane_b32 v100, s87, 1
v_mov_b32 v99, v96
s_nop 1
v_max_u32_dpp v99, v99, v99 row_shr:1 row_mask:0xf bank_mask:0xf
s_nop 1
v_max_u32_dpp v99, v99, v99 row_shr:2 row_mask:0xf bank_mask:0xf
s_nop 1
v_max_u32_dpp v99, v99, v99 row_shr:4 row_mask:0xf bank_mask:0xf
s_nop 1
v_max_u32_dpp v99, v99, v99 row_shr:8 row_mask:0xf bank_mask:0xf
s_nop 1
v_max_u32_dpp v99, v99, v99 row_bcast:15 row_mask:0xa bank_mask:0xf
)"
R"(s_nop 1
v_max_u32_dpp v99, v99, v99 row_bcast:31 row_mask:0xc bank_mask:0xf
s_nop 0
v_readlane_b32 s86, v99, 63
s_nop 1
v_cmp_eq_u32 vcc, s86, v96
s_and_b32 s87, s86, 0xffff
s_sub_u32 s87, 2047, s87
v_cndmask_b32 v96, v96, v97, vcc
v_cndmask_b32_e64 v97, v97, 0, vcc
v_writelane_b32 v100, s87, 2
v_mov_b32 v99, v96
s_nop 1
v_max_u32_dpp v99, v99, v99 row_shr:1 row_mask:0xf bank_mask:0xf
s_nop 1
v_max_u32_dpp v99, v99, v99 row_shr:2 row_mask:0xf bank_mask:0xf
s_nop 1
v_max_u32_dpp v99, v99, v99 row_shr:4 row_mask:0xf bank_mask:0xf
s_nop 1
v_max_u32_dpp v99, v99, v99 row_shr:8 row_mask:0xf bank_mask:0xf
s_nop 1
v_max_u32_dpp v99, v99, v99 row_bcast:15 row_mask:0xa bank_mask:0xf
s_nop 1
v_max_u32_dpp v99, v99, v99 row_bcast:31 row_mask:0xc bank_mask:0xf
s_nop 0
v_readlane_b32 s86, v99, 63
s_nop 1
v_cmp_eq_u32 vcc, s86, v96
s_and_b32 s87, s86, 0xffff
s_sub_u32 s87, 2047, s87
v_cndmask_b32 v96, v96, v97, vcc
v_cndmask_b32_e64 v97, v97, 0, vcc
v_writelane_b32 v100, s87, 3
v_mov_b32 v99, v96
s_nop 1
v_max_u32_dpp v99, v99, v99 row_shr:1 row_mask:0xf bank_mask:0xf
s_nop 1
v_max_u32_dpp v99, v99, v99 row_shr:2 row_mask:0xf bank_mask:0xf
s_nop 1
v_max_u32_dpp v99, v99, v99 row_shr:4 row_mask:0xf bank_mask:0xf
s_nop 1
v_max_u32_dpp v99, v99, v99 row_shr:8 row_mask:0xf bank_mask:0xf
s_nop 1
v_max_u32_dpp v99, v99, v99 row_bcast:15 row_mask:0xa bank_mask:0xf
s_nop 1
v_max_u32_dpp v99, v99, v99 row_bcast:31 row_mask:0xc bank_mask:0xf
s_nop 0
v_readlane_b32 s86, v99, 63
s_nop 1
v_cmp_eq_u32 vcc, s86, v96
s_and_b32 s87, s86, 0xffff
s_sub_u32 s87, 2047, s87
v_cndmask_b32 v96, v96, v97, vcc
v_cndmask_b32_e64 v97, v97, 0, vcc
v_writelane_b32 v100, s87, 4
v_mov_b32 v99, v96
s_nop 1
v_max_u32_dpp v99, v99, v99 row_shr:1 row_mask:0xf bank_mask:0xf
s_nop 1
v_max_u32_dpp v99, v99, v99 row_shr:2 row_mask:0xf bank_mask:0xf
s_nop 1
v_max_u32_dpp v99, v99, v99 row_shr:4 row_mask:0xf bank_mask:0xf
s_nop 1
v_max_u32_dpp v99, v99, v99 row_shr:8 row_mask:0xf bank_mask:0xf
s_nop 1
v_max_u32_dpp v99, v99, v99 row_bcast:15 row_mask:0xa bank_mask:0xf
s_nop 1
v_max_u32_dpp v99, v99, v99 row_bcast:31 row_mask:0xc bank_mask:0xf
s_nop 0
v_readlane_b32 s86, v99, 63
s_nop 1
v_cmp_eq_u32 vcc, s86, v96
s_and_b32 s87, s86, 0xffff
s_sub_u32 s87, 2047, s87
v_cndmask_b32 v96, v96, v97, vcc
v_cndmask_b32_e64 v97, v97, 0, vcc
v_writelane_b32 v100, s87, 5
v_mov_b32 v99, v96
s_nop 1
v_max_u32_dpp v99, v99, v99 row_shr:1 row_mask:0xf bank_mask:0xf
s_nop 1
v_max_u32_dpp v99, v99, v99 row_shr:2 row_mask:0xf bank_mask:0xf
s_nop 1
v_max_u32_dpp v99, v99, v99 row_shr:4 row_mask:0xf bank_mask:0xf
s_nop 1
v_max_u32_dpp v99, v99, v99 row_shr:8 row_mask:0xf bank_mask:0xf
s_nop 1
v_max_u32_dpp v99, v99, v99 row_bcast:15 row_mask:0xa bank_mask:0xf
s_nop 1
v_max_u32_dpp v99, v99, v99 row_bcast:31 row_mask:0xc bank_mask:0xf
s_nop 0
v_readlane_b32 s86, v99, 63
s_nop 1
v_cmp_eq_u32 vcc, s86, v96
s_and_b32 s87, s86, 0xffff
s_sub_u32 s87, 2047, s87
v_cndmask_b32 v96, v96, v97, vcc
v_cndmask_b32_e64 v97, v97, 0, vcc
v_writelane_b32 v100, s87, 6
v_mov_b32 v99, v96
s_nop 1
v_max_u32_dpp v99, v99, v99 row_shr:1 row_mask:0xf bank_mask:0xf
s_nop 1
v_max_u32_dpp v99, v99, v99 row_shr:2 row_mask:0xf bank_mask:0xf
s_nop 1
v_max_u32_dpp v99, v99, v99 row_shr:4 row_mask:0xf bank_mask:0xf
s_nop 1
v_max_u32_dpp v99, v99, v99 row_shr:8 row_mask:0xf bank_mask:0xf
s_nop 1
v_max_u32_dpp v99, v99, v99 row_bcast:15 row_mask:0xa bank_mask:0xf
s_nop 1
v_max_u32_dpp v99, v99, v99 row_bcast:31 row_mask:0xc bank_mask:0xf
s_nop 0
v_readlane_b32 s86, v99, 63
s_nop 1
v_cmp_eq_u32 vcc, s86, v96
s_and_b32 s87, s86, 0xffff
s_sub_u32 s87, 2047, s87
v_cndmask_b32 v96, v96, v97, vcc
v_cndmask_b32_e64 v97, v97, 0, vcc
v_writelane_b32 v100, s87, 7
v_mov_b32 v99, v98
s_nop 1
v_max_u32_dpp v99, v99, v99 row_shr:1 row_mask:0xf bank_mask:0xf
s_nop 1
v_max_u32_dpp v99, v99, v99 row_shr:2 row_mask:0xf bank_mask:0xf
s_nop 1
v_max_u32_dpp v99, v99, v99 row_shr:4 row_mask:0xf bank_mask:0xf
s_nop 1
v_max_u32_dpp v99, v99, v99 row_shr:8 row_mask:0xf bank_mask:0xf
s_nop 1
v_max_u32_dpp v99, v99, v99 row_bcast:15 row_mask:0xa bank_mask:0xf
s_nop 1
v_max_u32_dpp v99, v99, v99 row_bcast:31 row_mask:0xc bank_mask:0xf
s_nop 0
v_readlane_b32 s87, v99, 63
s_nop 0
s_cmp_gt_u32 s87, s86
s_cbranch_scc0 .LL_27%=
s_mov_b32 m0, 0
.LL_28%=:
v_max3_u32 v96, v32, v34, v36
v_max3_u32 v96, v96, v38, v40
v_max3_u32 v96, v96, v42, v44
v_max3_u32 v96, v96, v46, v48
v_max3_u32 v96, v96, v50, v52
v_max3_u32 v96, v96, v54, v56
v_max3_u32 v96, v96, v58, v60
v_max3_u32 v96, v96, v62, v64
v_max3_u32 v96, v96, v66, v68
v_max3_u32 v96, v96, v70, v72
v_max3_u32 v96, v96, v74, v76
v_max3_u32 v96, v96, v78, v80
v_max3_u32 v96, v96, v82, v84
v_max3_u32 v96, v96, v86, v88
v_max3_u32 v96, v96, v90, v92
v_max_u32 v96, v96, v94
v_mov_b32 v99, v96
s_nop 1
v_max_u32_dpp v99, v99, v99 row_shr:1 row_mask:0xf bank_mask:0xf
s_nop 1
v_max_u32_dpp v99, v99, v99 row_shr:2 row_mask:0xf bank_mask:0xf
s_nop 1
v_max_u32_dpp v99, v99, v99 row_shr:4 row_mask:0xf bank_mask:0xf
s_nop 1
v_max_u32_dpp v99, v99, v99 row_shr:8 row_mask:0xf bank_mask:0xf
s_nop 1
v_max_u32_dpp v99, v99, v99 row_bcast:15 row_mask:0xa bank_mask:0xf
s_nop 1
v_max_u32_dpp v99, v99, v99 row_bcast:31 row_mask:0xc bank_mask:0xf
s_nop 0
v_readlane_b32 s86, v99, 63
s_nop 1
v_cmp_eq_u32 vcc, s86, v32
s_nop 1
v_cndmask_b32_e64 v32, v32, 0, vcc
v_cmp_eq_u32 vcc, s86, v34
s_nop 1
v_cndmask_b32_e64 v34, v34, 0, vcc
v_cmp_eq_u32 vcc, s86, v36
s_nop 1
v_cndmask_b32_e64 v36, v36, 0, vcc
v_cmp_eq_u32 vcc, s86, v38
s_nop 1
v_cndmask_b32_e64 v38, v38, 0, vcc
v_cmp_eq_u32 vcc, s86, v40
s_nop 1
v_cndmask_b32_e64 v40, v40, 0, vcc
v_cmp_eq_u32 vcc, s86, v42
s_nop 1
v_cndmask_b32_e64 v42, v42, 0, vcc
v_cmp_eq_u32 vcc, s86, v44
s_nop 1
v_cndmask_b32_e64 v44, v44, 0, vcc
v_cmp_eq_u32 vcc, s86, v46
s_nop 1
v_cndmask_b32_e64 v46, v46, 0, vcc
v_cmp_eq_u32 vcc, s86, v48
s_nop 1
v_cndmask_b32_e64 v48, v48, 0, vcc
v_cmp_eq_u32 vcc, s86, v50
s_nop 1
v_cndmask_b32_e64 v50, v50, 0, vcc
v_cmp_eq_u32 vcc, s86, v52
s_nop 1
v_cndmask_b32_e64 v52, v52, 0, vcc
v_cmp_eq_u32 vcc, s86, v54
s_nop 1
v_cndmask_b32_e64 v54, v54, 0, vcc
v_cmp_eq_u32 vcc, s86, v56
s_nop 1
v_cndmask_b32_e64 v56, v56, 0, vcc
v_cmp_eq_u32 vcc, s86, v58
s_nop 1
v_cndmask_b32_e64 v58, v58, 0, vcc
v_cmp_eq_u32 vcc, s86, v60
s_nop 1
v_cndmask_b32_e64 v60, v60, 0, vcc
v_cmp_eq_u32 vcc, s86, v62
s_nop 1
v_cndmask_b32_e64 v62, v62, 0, vcc
v_cmp_eq_u32 vcc, s86, v64
s_nop 1
v_cndmask_b32_e64 v64, v64, 0, vcc
v_cmp_eq_u32 vcc, s86, v66
s_nop 1
v_cndmask_b32_e64 v66, v66, 0, vcc
v_cmp_eq_u32 vcc, s86, v68
s_nop 1
v_cndmask_b32_e64 v68, v68, 0, vcc
v_cmp_eq_u32 vcc, s86, v70
s_nop 1
v_cndmask_b32_e64 v70, v70, 0, vcc
v_cmp_eq_u32 vcc, s86, v72
s_nop 1
v_cndmask_b32_e64 v72, v72, 0, vcc
v_cmp_eq_u32 vcc, s86, v74
s_nop 1
v_cndmask_b32_e64 v74, v74, 0, vcc
v_cmp_eq_u32 vcc, s86, v76
s_nop 1
v_cndmask_b32_e64 v76, v76, 0, vcc
v_cmp_eq_u32 vcc, s86, v78
s_nop 1
v_cndmask_b32_e64 v78, v78, 0, vcc
v_cmp_eq_u32 vcc, s86, v80
s_nop 1
v_cndmask_b32_e64 v80, v80, 0, vcc
v_cmp_eq_u32 vcc, s86, v82
s_nop 1
v_cndmask_b32_e64 v82, v82, 0, vcc
v_cmp_eq_u32 vcc, s86, v84
s_nop 1
v_cndmask_b32_e64 v84, v84, 0, vcc
v_cmp_eq_u32 vcc, s86, v86
s_nop 1
v_cndmask_b32_e64 v86, v86, 0, vcc
v_cmp_eq_u32 vcc, s86, v88
s_nop 1
v_cndmask_b32_e64 v88, v88, 0, vcc
v_cmp_eq_u32 vcc, s86, v90
s_nop 1
v_cndmask_b32_e64 v90, v90, 0, vcc
v_cmp_eq_u32 vcc, s86, v92
s_nop 1
v_cndmask_b32_e64 v92, v92, 0, vcc
v_cmp_eq_u32 vcc, s86, v94
s_nop 1
v_cndmask_b32_e64 v94, v94, 0, vcc
s_and_b32 s87, s86, 0xffff
s_sub_u32 s87, 2047, s87
s_nop 0
v_writelane_b32 v100, s87, m0
s_add_u32 m0, m0, 1
s_cmp_lt_u32 m0, 8
s_cbranch_scc1 .LL_28%=
.LL_27%=:
v_xor_b32 v102, s12, v100
v_mov_b32 v103, s13
s_mov_b64 exec, 0xff
buffer_store_dwordx2 v[102:103], v136, s[8:11], 0 offen offset:3584 sc1
s_mov_b64 exec, -1
s_branch .Lrtr_24%=
.Lspin_49%=:
s_mov_b32 s84, 0
.Llp_51%=:
s_sleep 2
buffer_load_dwordx2 v[8:9], v11, s[8:11], 0 offen offset:3840 sc1
s_waitcnt vmcnt(0)
s_add_u32 s84, s84, 1
v_cmp_eq_u64 vcc, s[12:13], v[8:9]
s_cmp_eq_u64 vcc, -1
s_cbranch_scc1 .Lok_50%=
s_cmp_lt_u32 s84, 1048576
s_cbranch_scc1 .Llp_51%=
s_branch .Lok_50%=
.Lredo%=:
s_waitcnt vmcnt(0) lgkmcnt(0)
s_barrier
s_and_b32 s81, s14, 1
s_mul_i32 s91, s81, 17164288
s_mul_i32 s82, s56, 536576
s_add_u32 s91, s91, s82
s_add_u32 s91, s91, 4194304
s_lshr_b32 s82, s14, 1
s_lshl_b32 s83, s82, 14
s_add_u32 s91, s91, s83
s_lshl_b32 s81, s81, 14
s_lshl_b32 s82, s82, 16
s_or_b32 s92, s81, s82
s_xor_b32 s92, s92, 0x10000
s_add_u32 m0, s92, 0
s_add_u32 s81, s91, 0
Dc 1,8,81,0
Dc 1,8,81,1024
Dc 1,8,81,2048
Dc 1,8,81,3072
s_add_u32 m0, s92, 4096
s_add_u32 s81, s91, 4096
Dc 1,8,81,0
Dc 1,8,81,1024
Dc 1,8,81,2048
Dc 1,8,81,3072
s_add_u32 m0, s92, 8192
s_add_u32 s81, s91, 8192
Dc 1,8,81,0
Dc 1,8,81,1024
Dc 1,8,81,2048
Dc 1,8,81,3072
s_add_u32 m0, s92, 12288
s_add_u32 s81, s91, 12288
Dc 1,8,81,0
Dc 1,8,81,1024
Dc 1,8,81,2048
Dc 1,8,81,3072
v_bfe_u32 v244, v1, 4, 1
v_mul_u32_u24 v244, 0xaaaa, v244
v_add_u32 v244, 0x4444, v244
v_lshl_or_b32 v240, v244, 16, v244
v_mov_b32 v241, 0xff800000
v_add_u32 v242, 0x10000, v1
v_bfe_u32 v244, v1, 4, 4
v_lshrrev_b32 v245, 8, v1
v_lshlrev_b32 v245, 2, v245
v_sub_u32 v244, v244, v245
v_cmp_le_i32_e64 s[72:73], 0, v244
v_cmp_le_i32_e64 s[74:75], 1, v244
v_cmp_le_i32_e64 s[76:77], 2, v244
v_cmp_le_i32_e64 s[78:79], 3, v244
v_add_u32 v243, 4096, v1
v_add_u32 v244, 8192, v1
v_add_u32 v245, 12288, v1
s_mov_b32 s18, 0x3f803f80
s_mov_b32 s19, 0x3f003f00
v_accvgpr_write_b32 a176, s18
v_accvgpr_write_b32 a180, s19
v_mov_b32 v252, v241
v_accvgpr_write_b32 a177, s18
v_accvgpr_write_b32 a181, s19
v_mov_b32 v253, v241
v_accvgpr_write_b32 a178, s18
v_accvgpr_write_b32 a182, s19
v_mov_b32 v254, v241
v_accvgpr_write_b32 a179, s18
v_accvgpr_write_b32 a183, s19
v_mov_b32 v255, v241
v_accvgpr_write_b32 a160, 0
v_accvgpr_write_b32 a161, 0
v_accvgpr_write_b32 a162, 0
v_accvgpr_write_b32 a163, 0
v_accvgpr_write_b32 a164, 0
v_accvgpr_write_b32 a165, 0
v_accvgpr_write_b32 a166, 0
v_accvgpr_write_b32 a167, 0
v_accvgpr_write_b32 a168, 0
v_accvgpr_write_b32 a169, 0
v_accvgpr_write_b32 a170, 0
v_accvgpr_write_b32 a171, 0
v_accvgpr_write_b32 a172, 0
v_accvgpr_write_b32 a173, 0
v_accvgpr_write_b32 a174, 0
v_accvgpr_write_b32 a175, 0
s_mov_b32 s90, 0x5d800000
s_mov_b64 s[88:89], 0
s_mov_b32 s64, 0
s_and_b32 s18, s14, 1
s_lshl_b32 s62, s18, 14
s_lshl_b32 s20, s57, 15
s_sub_u32 s20, 507904, s20
s_add_u32 s20, s20, s62
s_lshl_b32 s65, s57, 1
s_sub_u32 s65, s65, 1
s_max_i32 s65, s65, 0
s_sub_u32 s66, s60, 1
s_cmp_lt_u32 s14, 2
s_cselect_b32 s20, s20, 0
s_cselect_b32 s66, s66, 15
s_cselect_b32 s21, 32768, 0
s_add_u32 s61, s91, s20
s_add_u32 s62, s62, s21
)"
R"(s_sub_u32 s63, 16384, s21
s_lshl_b32 s67, s60, 7
s_add_u32 s67, s67, s56
s_lshl_b32 s67, s67, 12
s_add_u32 s67, s67, 39845888
s_lshl_b32 s69, s60, 3
s_add_u32 s69, s69, s59
s_lshl_b32 s69, s69, 6
s_lshl_b32 s21, s58, 3
s_add_u32 s69, s69, s21
s_add_u32 s69, s69, 20480
s_mov_b32 s70, 0
s_mov_b32 s37, 0
v_bfe_u32 v28, v1, 4, 4
v_lshrrev_b32 v29, 8, v1
v_lshlrev_b32 v29, 2, v29
v_sub_u32 v28, v28, v29
s_mov_b64 s[24:25], 0
s_mov_b32 s68, 0
s_mov_b32 s85, 99
s_lshl_b32 s80, s60, 6
s_and_b32 s81, s14, 2
s_lshl_b32 s81, s81, 4
s_xor_b32 s82, s81, 0
s_add_u32 s82, s82, s80
v_add_u32 v4, s82, v28
s_xor_b32 s82, s81, 16
s_add_u32 s82, s82, s80
v_add_u32 v5, s82, v28
s_xor_b32 s82, s81, 32
s_add_u32 s82, s82, s80
v_add_u32 v6, s82, v28
s_xor_b32 s82, s81, 48
s_add_u32 s82, s82, s80
v_add_u32 v7, s82, v28
s_mul_i32 s82, s56, 536576
s_add_u32 s82, s82, 4194304
s_lshl_b32 s84, s60, 1
s_add_u32 s84, s84, 2
.LL_52%=:
buffer_load_dwordx4 a[128:131], v1, s[8:11], s82 offen offset:0 sc1
buffer_load_dwordx4 a[132:135], v1, s[8:11], s82 offen offset:1024 sc1
buffer_load_dwordx4 a[136:139], v1, s[8:11], s82 offen offset:2048 sc1
buffer_load_dwordx4 a[140:143], v1, s[8:11], s82 offen offset:3072 sc1
buffer_load_dwordx4 a[144:147], v243, s[8:11], s82 offen offset:0 sc1
buffer_load_dwordx4 a[148:151], v243, s[8:11], s82 offen offset:1024 sc1
buffer_load_dwordx4 a[152:155], v243, s[8:11], s82 offen offset:2048 sc1
buffer_load_dwordx4 a[156:159], v243, s[8:11], s82 offen offset:3072 sc1
s_waitcnt vmcnt(0)
Zvaa 160,128,0
Zvaa 164,128,16
Zvaa 168,128,32
Zvaa 172,128,48
Mvaa 160,132,4
Mvaa 164,132,20
Mvaa 168,132,36
Mvaa 172,132,52
Mvaa 160,136,8
Mvaa 164,136,24
Mvaa 168,136,40
Mvaa 172,136,56
Mvaa 160,140,12
Mvaa 164,140,28
Mvaa 168,140,44
Mvaa 172,140,60
Zvaa 176,144,0
Zvaa 180,144,16
Zvaa 184,144,32
Zvaa 188,144,48
Mvaa 176,148,4
Mvaa 180,148,20
Mvaa 184,148,36
Mvaa 188,148,52
Mvaa 176,152,8
Mvaa 180,152,24
Mvaa 184,152,40
Mvaa 188,152,56
Mvaa 176,156,12
Mvaa 180,156,28
Mvaa 184,156,44
Mvaa 188,156,60
v_cmp_le_i32 vcc, 0, v4
s_nop 1
v_cndmask_b32 v160, v241, v160, vcc
v_max_f32 v252, v252, v160
v_cmp_le_i32 vcc, 1, v4
s_nop 1
v_cndmask_b32 v161, v241, v161, vcc
v_max_f32 v252, v252, v161
v_cmp_le_i32 vcc, 2, v4
s_nop 1
v_cndmask_b32 v162, v241, v162, vcc
v_max_f32 v252, v252, v162
v_cmp_le_i32 vcc, 3, v4
s_nop 1
v_cndmask_b32 v163, v241, v163, vcc
v_max_f32 v252, v252, v163
v_cmp_le_i32 vcc, 0, v5
s_nop 1
v_cndmask_b32 v164, v241, v164, vcc
v_max_f32 v253, v253, v164
v_cmp_le_i32 vcc, 1, v5
s_nop 1
v_cndmask_b32 v165, v241, v165, vcc
v_max_f32 v253, v253, v165
v_cmp_le_i32 vcc, 2, v5
s_nop 1
v_cndmask_b32 v166, v241, v166, vcc
v_max_f32 v253, v253, v166
v_cmp_le_i32 vcc, 3, v5
s_nop 1
v_cndmask_b32 v167, v241, v167, vcc
v_max_f32 v253, v253, v167
v_cmp_le_i32 vcc, 0, v6
s_nop 1
v_cndmask_b32 v168, v241, v168, vcc
v_max_f32 v254, v254, v168
v_cmp_le_i32 vcc, 1, v6
s_nop 1
v_cndmask_b32 v169, v241, v169, vcc
v_max_f32 v254, v254, v169
v_cmp_le_i32 vcc, 2, v6
s_nop 1
v_cndmask_b32 v170, v241, v170, vcc
v_max_f32 v254, v254, v170
v_cmp_le_i32 vcc, 3, v6
s_nop 1
v_cndmask_b32 v171, v241, v171, vcc
v_max_f32 v254, v254, v171
v_cmp_le_i32 vcc, 0, v7
s_nop 1
v_cndmask_b32 v172, v241, v172, vcc
v_max_f32 v255, v255, v172
v_cmp_le_i32 vcc, 1, v7
s_nop 1
v_cndmask_b32 v173, v241, v173, vcc
v_max_f32 v255, v255, v173
v_cmp_le_i32 vcc, 2, v7
s_nop 1
v_cndmask_b32 v174, v241, v174, vcc
v_max_f32 v255, v255, v174
v_cmp_le_i32 vcc, 3, v7
s_nop 1
v_cndmask_b32 v175, v241, v175, vcc
v_max_f32 v255, v255, v175
v_cmp_le_i32 vcc, 16, v4
s_nop 1
v_cndmask_b32 v176, v241, v176, vcc
v_max_f32 v252, v252, v176
v_cmp_le_i32 vcc, 17, v4
s_nop 1
v_cndmask_b32 v177, v241, v177, vcc
v_max_f32 v252, v252, v177
v_cmp_le_i32 vcc, 18, v4
s_nop 1
v_cndmask_b32 v178, v241, v178, vcc
v_max_f32 v252, v252, v178
v_cmp_le_i32 vcc, 19, v4
s_nop 1
v_cndmask_b32 v179, v241, v179, vcc
v_max_f32 v252, v252, v179
v_cmp_le_i32 vcc, 16, v5
s_nop 1
v_cndmask_b32 v180, v241, v180, vcc
v_max_f32 v253, v253, v180
v_cmp_le_i32 vcc, 17, v5
s_nop 1
v_cndmask_b32 v181, v241, v181, vcc
v_max_f32 v253, v253, v181
v_cmp_le_i32 vcc, 18, v5
s_nop 1
v_cndmask_b32 v182, v241, v182, vcc
v_max_f32 v253, v253, v182
v_cmp_le_i32 vcc, 19, v5
s_nop 1
v_cndmask_b32 v183, v241, v183, vcc
v_max_f32 v253, v253, v183
v_cmp_le_i32 vcc, 16, v6
s_nop 1
v_cndmask_b32 v184, v241, v184, vcc
v_max_f32 v254, v254, v184
v_cmp_le_i32 vcc, 17, v6
s_nop 1
v_cndmask_b32 v185, v241, v185, vcc
v_max_f32 v254, v254, v185
v_cmp_le_i32 vcc, 18, v6
s_nop 1
v_cndmask_b32 v186, v241, v186, vcc
v_max_f32 v254, v254, v186
v_cmp_le_i32 vcc, 19, v6
s_nop 1
v_cndmask_b32 v187, v241, v187, vcc
v_max_f32 v254, v254, v187
v_cmp_le_i32 vcc, 16, v7
s_nop 1
v_cndmask_b32 v188, v241, v188, vcc
v_max_f32 v255, v255, v188
v_cmp_le_i32 vcc, 17, v7
s_nop 1
v_cndmask_b32 v189, v241, v189, vcc
v_max_f32 v255, v255, v189
v_cmp_le_i32 vcc, 18, v7
s_nop 1
v_cndmask_b32 v190, v241, v190, vcc
v_max_f32 v255, v255, v190
v_cmp_le_i32 vcc, 19, v7
s_nop 1
v_cndmask_b32 v191, v241, v191, vcc
v_max_f32 v255, v255, v191
v_subrev_u32 v4, 32, v4
v_subrev_u32 v5, 32, v5
v_subrev_u32 v6, 32, v6
v_subrev_u32 v7, 32, v7
s_add_u32 s82, s82, 8192
s_sub_u32 s84, s84, 1
s_cmp_lg_u32 s84, 0
s_cbranch_scc1 .LL_52%=
G 28,0
v_lshlrev_b32 v32, 16, v28
v_and_b32 v33, 0xffff0000, v28
G 28,1
v_lshlrev_b32 v34, 16, v28
v_and_b32 v35, 0xffff0000, v28
G 28,2
v_lshlrev_b32 v36, 16, v28
v_and_b32 v37, 0xffff0000, v28
G 28,3
v_lshlrev_b32 v38, 16, v28
v_and_b32 v39, 0xffff0000, v28
G 28,4
v_lshlrev_b32 v40, 16, v28
v_and_b32 v41, 0xffff0000, v28
G 28,5
v_lshlrev_b32 v42, 16, v28
v_and_b32 v43, 0xffff0000, v28
G 28,6
v_lshlrev_b32 v44, 16, v28
v_and_b32 v45, 0xffff0000, v28
G 28,7
v_lshlrev_b32 v46, 16, v28
v_and_b32 v47, 0xffff0000, v28
G 28,8
v_lshlrev_b32 v48, 16, v28
v_and_b32 v49, 0xffff0000, v28
G 28,9
v_lshlrev_b32 v50, 16, v28
v_and_b32 v51, 0xffff0000, v28
G 28,10
v_lshlrev_b32 v52, 16, v28
v_and_b32 v53, 0xffff0000, v28
G 28,11
v_lshlrev_b32 v54, 16, v28
v_and_b32 v55, 0xffff0000, v28
G 28,12
v_lshlrev_b32 v56, 16, v28
v_and_b32 v57, 0xffff0000, v28
G 28,13
v_lshlrev_b32 v58, 16, v28
v_and_b32 v59, 0xffff0000, v28
G 28,14
v_lshlrev_b32 v60, 16, v28
v_and_b32 v61, 0xffff0000, v28
G 28,15
v_lshlrev_b32 v62, 16, v28
v_and_b32 v63, 0xffff0000, v28
G 28,16
v_lshlrev_b32 v64, 16, v28
v_and_b32 v65, 0xffff0000, v28
G 28,17
v_lshlrev_b32 v66, 16, v28
v_and_b32 v67, 0xffff0000, v28
G 28,18
v_lshlrev_b32 v68, 16, v28
v_and_b32 v69, 0xffff0000, v28
G 28,19
v_lshlrev_b32 v70, 16, v28
v_and_b32 v71, 0xffff0000, v28
G 28,20
v_lshlrev_b32 v72, 16, v28
v_and_b32 v73, 0xffff0000, v28
G 28,21
v_lshlrev_b32 v74, 16, v28
v_and_b32 v75, 0xffff0000, v28
G 28,22
v_lshlrev_b32 v76, 16, v28
v_and_b32 v77, 0xffff0000, v28
G 28,23
v_lshlrev_b32 v78, 16, v28
v_and_b32 v79, 0xffff0000, v28
G 28,24
v_lshlrev_b32 v80, 16, v28
v_and_b32 v81, 0xffff0000, v28
G 28,25
v_lshlrev_b32 v82, 16, v28
v_and_b32 v83, 0xffff0000, v28
G 28,26
v_lshlrev_b32 v84, 16, v28
v_and_b32 v85, 0xffff0000, v28
G 28,27
v_lshlrev_b32 v86, 16, v28
v_and_b32 v87, 0xffff0000, v28
G 28,28
v_lshlrev_b32 v88, 16, v28
v_and_b32 v89, 0xffff0000, v28
G 28,29
v_lshlrev_b32 v90, 16, v28
v_and_b32 v91, 0xffff0000, v28
G 28,30
v_lshlrev_b32 v92, 16, v28
v_and_b32 v93, 0xffff0000, v28
G 28,31
v_lshlrev_b32 v94, 16, v28
v_and_b32 v95, 0xffff0000, v28
G 28,32
v_lshlrev_b32 v96, 16, v28
v_and_b32 v97, 0xffff0000, v28
G 28,33
v_lshlrev_b32 v98, 16, v28
v_and_b32 v99, 0xffff0000, v28
G 28,34
v_lshlrev_b32 v100, 16, v28
v_and_b32 v101, 0xffff0000, v28
G 28,35
v_lshlrev_b32 v102, 16, v28
v_and_b32 v103, 0xffff0000, v28
G 28,36
v_lshlrev_b32 v104, 16, v28
v_and_b32 v105, 0xffff0000, v28
G 28,37
v_lshlrev_b32 v106, 16, v28
v_and_b32 v107, 0xffff0000, v28
G 28,38
v_lshlrev_b32 v108, 16, v28
v_and_b32 v109, 0xffff0000, v28
G 28,39
v_lshlrev_b32 v110, 16, v28
v_and_b32 v111, 0xffff0000, v28
G 28,40
v_lshlrev_b32 v112, 16, v28
v_and_b32 v113, 0xffff0000, v28
G 28,41
v_lshlrev_b32 v114, 16, v28
v_and_b32 v115, 0xffff0000, v28
G 28,42
v_lshlrev_b32 v116, 16, v28
v_and_b32 v117, 0xffff0000, v28
G 28,43
v_lshlrev_b32 v118, 16, v28
v_and_b32 v119, 0xffff0000, v28
G 28,44
v_lshlrev_b32 v120, 16, v28
v_and_b32 v121, 0xffff0000, v28
G 28,45
v_lshlrev_b32 v122, 16, v28
v_and_b32 v123, 0xffff0000, v28
G 28,46
v_lshlrev_b32 v124, 16, v28
v_and_b32 v125, 0xffff0000, v28
G 28,47
v_lshlrev_b32 v126, 16, v28
v_and_b32 v127, 0xffff0000, v28
G 28,48
v_lshlrev_b32 v128, 16, v28
v_and_b32 v129, 0xffff0000, v28
G 28,49
v_lshlrev_b32 v130, 16, v28
v_and_b32 v131, 0xffff0000, v28
G 28,50
v_lshlrev_b32 v132, 16, v28
v_and_b32 v133, 0xffff0000, v28
G 28,51
v_lshlrev_b32 v134, 16, v28
v_and_b32 v135, 0xffff0000, v28
G 28,52
v_lshlrev_b32 v136, 16, v28
v_and_b32 v137, 0xffff0000, v28
G 28,53
v_lshlrev_b32 v138, 16, v28
v_and_b32 v139, 0xffff0000, v28
G 28,54
v_lshlrev_b32 v140, 16, v28
v_and_b32 v141, 0xffff0000, v28
G 28,55
v_lshlrev_b32 v142, 16, v28
v_and_b32 v143, 0xffff0000, v28
G 28,56
v_lshlrev_b32 v144, 16, v28
v_and_b32 v145, 0xffff0000, v28
G 28,57
v_lshlrev_b32 v146, 16, v28
v_and_b32 v147, 0xffff0000, v28
G 28,58
v_lshlrev_b32 v148, 16, v28
v_and_b32 v149, 0xffff0000, v28
G 28,59
v_lshlrev_b32 v150, 16, v28
v_and_b32 v151, 0xffff0000, v28
G 28,60
v_lshlrev_b32 v152, 16, v28
v_and_b32 v153, 0xffff0000, v28
)"
R"(G 28,61
v_lshlrev_b32 v154, 16, v28
v_and_b32 v155, 0xffff0000, v28
G 28,62
v_lshlrev_b32 v156, 16, v28
v_and_b32 v157, 0xffff0000, v28
G 28,63
v_lshlrev_b32 v158, 16, v28
v_and_b32 v159, 0xffff0000, v28
s_mov_b32 s22, 1.0
s_mov_b32 s23, 1.0
s_mov_b32 s29, 134217728
s_mov_b32 s71, 3584
s_barrier
Ra 128,242,0
Ra 132,242,1024
Ra 136,242,2048
Ra 140,242,3072
s_cmp_ge_u32 s14, 2
s_cbranch_scc1 .Lbgo%=
s_branch .Lago%=
.Lpb_29%=:
s_waitcnt vmcnt(6) lgkmcnt(6)
Zvaa 32,0,32
s_add_u32 s28, s28, 512
Ta 20,4,8192
Ta 22,4,9216
Mvaa 32,4,48
s_cmp_eq_u32 s28, s4
s_cselect_b32 s93, 10240, 0
Ta 24,5,0
Ta 26,5,1024
Zvaa 64,4,52
s_sub_u32 s28, s28, s93
s_add_u32 s93, s28, 20480
buffer_load_dwordx4 a[128:131], v26, s[8:11], s28 offen offset:0 sc1
Mvaa 64,0,36
Ta 28,5,8192
Ta 30,5,9216
Zvaa 96,0,40
K 184,184,185
K 185,186,187
Mvaa 96,4,56
K 186,188,189
K 187,190,191
Zvaa 128,4,60
buffer_load_dwordx4 a[132:135], v26, s[8:11], s93 offen offset:0 sc1
K 192,192,193
Mvaa 128,0,44
W 8
Ta 0,6,0
K 193,194,195
Zvaa 132,8,44
Ta 2,6,1024
K 194,196,197
Mvaa 132,12,60
K 195,198,199
K 200,200,201
Zvaa 100,12,56
buffer_load_dwordx4 a[136:139], v27, s[8:11], s28 offen offset:0 sc1
K 201,202,203
Mvaa 100,8,40
Ta 4,6,8192
Ta 6,6,9216
Zvaa 68,8,36
K 202,204,205
K 203,206,207
Mvaa 68,12,52
K 208,208,209
K 209,210,211
Zvaa 36,12,48
buffer_load_dwordx4 a[140:143], v27, s[8:11], s93 offen offset:0 sc1
K 210,212,213
Mvaa 36,8,32
W 8
s_add_u32 s28, s28, 512
s_cmp_eq_u32 s28, s4
s_cselect_b32 s93, 10240, 0
Zvaa 40,16,32
s_sub_u32 s28, s28, s93
s_add_u32 s93, s28, 20480
K 211,214,215
Mvaa 40,20,48
Ta 8,7,0
Ta 10,7,1024
Zvaa 72,20,52
buffer_load_dwordx4 a[144:147], v26, s[8:11], s28 offen offset:0 sc1
K 216,216,217
Mvaa 72,16,36
K 217,218,219
K 218,220,221
K 219,222,223
Zvaa 104,16,40
Ta 12,7,8192
Ta 14,7,9216
Mvaa 104,20,56
K 224,224,225
K 225,226,227
Zvaa 136,20,60
K 226,228,229
K 227,230,231
Mvaa 136,16,44
W 8
buffer_load_dwordx4 a[148:151], v26, s[8:11], s93 offen offset:0 sc1
K 232,232,233
Zvaa 140,24,44
K 233,234,235
K 234,236,237
Mvaa 140,28,60
Ta 16,8,0
K 235,238,239
Zvaa 108,28,56
Ta 18,8,1024
K 240,240,241
Mvaa 108,24,40
buffer_load_dwordx4 a[152:155], v27, s[8:11], s28 offen offset:0 sc1
K 241,242,243
Zvaa 76,24,36
K 242,244,245
K 243,246,247
Mvaa 76,28,52
Ta 20,8,8192
Ta 22,8,9216
Zvaa 44,28,48
K 248,248,249
K 249,250,251
Mvaa 44,24,32
K 250,252,253
K 251,254,255
W 8
Zvaa 48,0,32
buffer_load_dwordx4 a[156:159], v27, s[8:11], s93 offen offset:0 sc1
Ta 24,9,0
Mvaa 48,4,48
s_add_u32 s28, s28, 512
s_cmp_eq_u32 s28, s4
s_cselect_b32 s93, 10240, 0
s_sub_u32 s28, s28, s93
Zvaa 80,4,52
s_add_u32 s93, s28, 20480
Ta 26,9,1024
K 160,160,161
Mvaa 80,0,36
K 161,162,163
K 162,164,165
Zvaa 112,0,40
buffer_load_dwordx4 v[188:191], v26, s[8:11], s28 offen offset:0 sc1
K 163,166,167
Mvaa 112,4,56
Ta 28,9,8192
Ta 30,9,9216
Zvaa 144,4,60
K 168,168,169
K 169,170,171
Mvaa 144,0,44
W 8
K 170,172,173
K 171,174,175
Zvaa 148,8,44
buffer_load_dwordx4 v[196:199], v26, s[8:11], s93 offen offset:0 sc1
G 164,184
Mvaa 148,12,60
G 165,185
G 166,186
Zvaa 116,12,56
Ta 0,2,16384
Ta 2,2,17408
Mvaa 116,8,40
G 167,187
G 172,188
Zvaa 84,8,36
buffer_load_dwordx4 v[204:207], v27, s[8:11], s28 offen offset:0 sc1
Ta 4,2,24576
Mvaa 84,12,52
Ta 6,2,25600
G 173,189
Zvaa 52,12,48
G 174,190
G 175,191
Mvaa 52,8,32
W 8
Ta 8,3,16384
K 164,164,165
Zvaa 56,16,32
buffer_load_dwordx4 v[212:215], v27, s[8:11], s93 offen offset:0 sc1
Ta 10,3,17408
Mvaa 56,20,48
s_add_u32 s28, s28, 512
s_cmp_eq_u32 s28, s4
s_cselect_b32 s93, 10240, 0
s_sub_u32 s28, s28, s93
Zvaa 88,20,52
s_add_u32 s93, s28, 20480
K 165,166,167
K 166,172,173
Mvaa 88,16,36
Ta 12,3,24576
K 167,174,175
Zvaa 120,16,40
buffer_load_dwordx4 v[220:223], v26, s[8:11], s28 offen offset:0 sc1
Ta 14,3,25600
Mvaa 120,20,56
v_accvgpr_write_b32 a184, v164
v_accvgpr_write_b32 a185, v165
Zvaa 152,20,60
v_accvgpr_write_b32 a186, v166
Mvaa 152,16,44
W 8
Ta 16,4,16384
v_accvgpr_write_b32 a187, v167
Zvaa 156,24,44
buffer_load_dwordx4 v[228:231], v26, s[8:11], s93 offen offset:0 sc1
Ta 18,4,17408
Mvaa 156,28,60
v_accvgpr_write_b32 a188, v240
Zvaa 124,28,56
Ta 20,4,24576
v_accvgpr_write_b32 a189, v241
Mvaa 124,24,40
Ta 22,4,25600
v_accvgpr_write_b32 a190, v242
Zvaa 92,24,36
buffer_load_dwordx4 v[236:239], v27, s[8:11], s28 offen offset:0 sc1
v_accvgpr_write_b32 a191, v243
Mvaa 92,28,52
G 164,192
Zvaa 60,28,48
Mvaa 60,24,32
s_waitcnt vmcnt(7) lgkmcnt(8)
Ta 24,5,16384
G 165,193
Mvaa 32,0,128
buffer_load_dwordx4 v[244:247], v27, s[8:11], s93 offen offset:0 sc1
Ta 26,5,17408
Mvaa 32,4,144
s_add_u32 s28, s28, 512
s_cmp_eq_u32 s28, s4
s_cselect_b32 s93, 10240, 0
s_sub_u32 s28, s28, s93
Mvaa 64,4,148
s_add_u32 s93, s28, 20480
G 166,194
G 167,195
Mvaa 64,0,132
Ta 28,5,24576
Ta 30,5,25600
Mvaa 96,0,136
D 22,48,65,2048
G 172,196
Mvaa 96,4,152
G 173,197
G 174,198
Mvaa 128,4,156
G 175,199
Mvaa 128,0,140
W 8
Ta 0,6,16384
Ta 2,6,17408
Mvaa 132,8,140
buffer_load_dwordx4 a[32:35], v26, s[8:11], s28 offen offset:0 sc1
K 164,164,165
Mvaa 132,12,156
K 165,166,167
Mvaa 100,12,152
Ta 4,6,24576
K 166,172,173
Mvaa 100,8,136
Ta 6,6,25600
K 167,174,175
Mvaa 68,8,132
buffer_load_dwordx4 a[36:39], v26, s[8:11], s93 offen offset:0 sc1
v_accvgpr_write_b32 a192, v164
Mvaa 68,12,148
Mvaa 36,12,144
v_accvgpr_write_b32 a193, v165
Mvaa 36,8,128
W 8
Ta 8,7,16384
v_accvgpr_write_b32 a194, v166
Mvaa 40,16,128
buffer_load_dwordx4 a[40:43], v27, s[8:11], s28 offen offset:0 sc1
Ta 10,7,17408
Mvaa 40,20,144
v_accvgpr_write_b32 a195, v167
v_accvgpr_write_b32 a196, v248
Mvaa 72,20,148
Ta 12,7,24576
Ta 14,7,25600
Mvaa 72,16,132
v_accvgpr_write_b32 a197, v249
Mvaa 104,16,136
buffer_load_dwordx4 a[44:47], v27, s[8:11], s93 offen offset:0 sc1
v_accvgpr_write_b32 a198, v250
Mvaa 104,20,152
s_add_u32 s28, s28, 512
s_cmp_eq_u32 s28, s4
s_cselect_b32 s93, 10240, 0
s_sub_u32 s28, s28, s93
Mvaa 136,20,156
s_add_u32 s93, s28, 20480
v_accvgpr_write_b32 a199, v251
G 164,200
Mvaa 136,16,140
W 8
Ta 16,8,16384
Ta 18,8,17408
Mvaa 140,24,140
buffer_load_dwordx4 a[48:51], v26, s[8:11], s28 offen offset:0 sc1
G 165,201
Mvaa 140,28,156
G 166,202
Mvaa 108,28,152
Ta 20,8,24576
G 167,203
Mvaa 108,24,136
Ta 22,8,25600
G 172,204
Mvaa 76,24,132
buffer_load_dwordx4 a[52:55], v26, s[8:11], s93 offen offset:0 sc1
Mvaa 76,28,148
G 173,205
Mvaa 44,28,144
G 174,206
Mvaa 44,24,128
W 8
Ta 24,9,16384
G 175,207
Mvaa 48,0,128
buffer_load_dwordx4 a[56:59], v27, s[8:11], s28 offen offset:0 sc1
Ta 26,9,17408
Mvaa 48,4,144
K 164,164,165
Mvaa 80,4,148
Ta 28,9,24576
K 165,166,167
Mvaa 80,0,132
Ta 30,9,25600
K 166,172,173
Mvaa 112,0,136
s_lshl_b32 s80, s56, 9
buffer_load_dwordx4 a[60:63], v27, s[8:11], s93 offen offset:0 sc1
K 167,174,175
Mvaa 112,4,152
v_accvgpr_write_b32 a200, v164
Mvaa 144,4,156
s_lshl_b32 s81, s57, 4
Mvaa 144,0,140
W 8
s_add_u32 s80, s80, s81
v_accvgpr_write_b32 a201, v165
Mvaa 148,8,140
D 23,48,65,3072
Ta 0,10,0
Mvaa 148,12,156
s_add_u32 s65, s5, 16
s_cmp_ge_u32 s65, 20
s_cselect_b32 s90, 20, 0
s_sub_u32 s65, s65, s90
Mvaa 116,12,152
s_lshl_b32 s65, s65, 18
s_add_u32 m0, s63, 98304
Ta 2,10,1024
Mvaa 116,8,136
s_and_b32 s81, s14, 1
v_accvgpr_write_b32 a202, v166
v_accvgpr_write_b32 a203, v167
Mvaa 84,8,132
s_lshl_b32 s81, s81, 3
D 16,48,65,0
Ta 4,10,8192
Mvaa 84,12,148
s_add_u32 s80, s80, s81
Ta 6,10,9216
v_accvgpr_write_b32 a204, v184
Mvaa 52,12,144
s_add_u32 s71, s80, 4096
v_accvgpr_write_b32 a205, v185
v_accvgpr_write_b32 a206, v186
Mvaa 52,8,128
W 8
Ta 8,11,0
v_accvgpr_write_b32 a207, v187
Mvaa 56,16,128
Ta 10,11,1024
G 164,208
Mvaa 56,20,144
G 165,209
Mvaa 88,20,148
Ta 12,11,8192
G 166,210
Mvaa 88,16,132
Ta 14,11,9216
G 167,211
Mvaa 120,16,136
Mvaa 120,20,152
G 172,212
Mvaa 152,20,156
G 173,213
Mvaa 152,16,140
W 8
)"
R"(Ta 16,12,0
G 174,214
Mvaa 156,24,140
s_add_u32 s28, s28, 512
s_cmp_eq_u32 s28, s4
s_cselect_b32 s93, 10240, 0
Ta 18,12,1024
Mvaa 156,28,156
s_sub_u32 s28, s28, s93
s_add_u32 s93, s28, 20480
buffer_load_dwordx4 v[184:187], v26, s[8:11], s28 offen offset:0 sc1
Mvaa 124,28,152
s_lshr_b32 s80, s15, 3
s_lshl_b32 s80, s80, 1
G 175,215
Mvaa 124,24,136
s_add_u32 s80, s80, s14
Ta 20,12,8192
K 164,164,165
Mvaa 92,24,132
s_sub_u32 s80, s80, 2
Ta 22,12,9216
K 165,166,167
Mvaa 92,28,148
s_lshl_b32 s80, s80, 12
D 17,48,65,1024
K 166,172,173
Mvaa 60,28,144
K 167,174,175
v_min_u32 v28, 496, v1
Mvaa 60,24,128
s_waitcnt vmcnt(13) lgkmcnt(8)
v_accvgpr_write_b32 a208, v164
v_lshl_add_u32 v28, v28, 3, s80
Mvav 32,0,188
Ta 24,13,0
Ta 26,13,1024
Mvav 32,4,220
buffer_load_dwordx4 a[128:131], v26, s[8:11], s93 offen offset:0 sc1
v_min_u32 v28, .Lend%=-.Lbeg%=-4, v28
Mvav 64,4,228
v_accvgpr_write_b32 a209, v165
v_accvgpr_write_b32 a210, v166
Mvav 64,0,196
Ta 28,13,8192
Ta 30,13,9216
Mvav 96,0,204
v_accvgpr_write_b32 a211, v167
v_accvgpr_write_b32 a212, v192
Mvav 96,4,236
buffer_load_dwordx4 a[132:135], v27, s[8:11], s28 offen offset:0 sc1
Mvav 128,4,244
v_accvgpr_write_b32 a213, v193
Mvav 128,0,212
W 8
Ta 0,14,0
v_accvgpr_write_b32 a214, v194
Mvav 132,8,212
Ta 2,14,1024
v_add_u32 v2, 65536, v10
Mvav 132,12,244
buffer_load_dwordx4 a[136:139], v27, s[8:11], s93 offen offset:0 sc1
v_accvgpr_write_b32 a215, v195
Mvav 100,12,236
s_add_u32 s28, s28, 512
s_cmp_eq_u32 s28, s4
s_cselect_b32 s93, 10240, 0
s_sub_u32 s28, s28, s93
Mvav 100,8,204
s_add_u32 s93, s28, 20480
Ta 4,14,8192
G 164,216
Mvav 68,8,196
Ta 6,14,9216
v_add_u32 v3, 65536, v11
Mvav 68,12,228
buffer_load_dwordx4 a[140:143], v26, s[8:11], s28 offen offset:0 sc1
G 165,217
Mvav 36,12,220
G 166,218
G 167,219
Mvav 36,8,188
W 8
Ta 8,15,0
G 172,220
Mvav 40,16,188
Ta 10,15,1024
v_add_u32 v4, 65536, v12
Mvav 40,20,220
buffer_load_dwordx4 a[144:147], v26, s[8:11], s93 offen offset:0 sc1
G 173,221
Mvav 72,20,228
G 174,222
G 175,223
Mvav 72,16,196
Ta 12,15,8192
Ta 14,15,9216
Mvav 104,16,204
v_add_u32 v5, 65536, v13
K 164,164,165
Mvav 104,20,236
buffer_load_dwordx4 a[148:151], v27, s[8:11], s28 offen offset:0 sc1
K 165,166,167
Mvav 136,20,244
K 166,172,173
Mvav 136,16,212
W 8
Ta 16,24,0
K 167,174,175
Mvav 140,24,212
Ta 18,24,1024
v_add_u32 v6, 65536, v14
Mvav 140,28,244
buffer_load_dwordx4 a[152:155], v27, s[8:11], s93 offen offset:0 sc1
v_accvgpr_write_b32 a216, v164
Mvav 108,28,236
s_add_u32 s28, s28, 512
s_cmp_eq_u32 s28, s4
s_cselect_b32 s93, 10240, 0
s_sub_u32 s28, s28, s93
Mvav 108,24,204
s_add_u32 s93, s28, 20480
Ta 20,24,8192
v_accvgpr_write_b32 a217, v165
Mvav 76,24,196
Ta 22,24,9216
v_add_u32 v7, 65536, v15
Mvav 76,28,228
buffer_load_dwordx4 a[156:159], v26, s[8:11], s28 offen offset:0 sc1
v_accvgpr_write_b32 a218, v166
Mvav 44,28,220
v_accvgpr_write_b32 a219, v167
v_accvgpr_write_b32 a220, v200
Mvav 44,24,188
W 8
Ta 24,25,0
v_accvgpr_write_b32 a221, v201
Mvav 48,0,188
Ta 26,25,1024
v_add_u32 v8, 65536, v24
Mvav 48,4,220
global_load_dword v29, v28, s[6:7]
v_accvgpr_write_b32 a222, v202
Mvav 80,4,228
s_sub_u32 s74, s85, 66
s_and_b32 s75, s74, 0xffffffe3
s_cselect_b64 s[72:73], -1, 0
s_and_b32 s74, s74, 28
Mvav 80,0,196
s_lshl_b32 s74, s74, 1
v_mov_b32 v28, s74
v_accvgpr_write_b32 a223, v203
Mvav 112,0,204
Ta 28,25,8192
Ta 30,25,9216
Mvav 112,4,236
buffer_load_dwordx4 v[192:195], v26, s[8:11], s93 offen offset:0 sc1
v_add_u32 v9, 65536, v25
Mvav 144,4,244
G 164,224
G 165,225
Mvav 144,0,212
W 8
G 166,226
G 167,227
Mvav 148,8,212
Ta 0,10,16384
Ta 2,10,17408
Mvav 148,12,244
D 18,48,65,2048
G 172,228
Mvav 116,12,236
G 173,229
G 174,230
Mvav 116,8,204
Ta 4,10,24576
Ta 6,10,25600
Mvav 84,8,196
G 175,231
K 164,164,165
Mvav 84,12,228
buffer_load_dwordx2 v[28:29], v28, s[8:11], 0 offen offset:3584 sc1
Mvav 52,12,220
K 165,166,167
Mvav 52,8,188
W 8
Ta 8,11,16384
K 166,172,173
Mvav 56,16,188
Ta 10,11,17408
K 167,174,175
Mvav 56,20,220
buffer_load_dwordx4 v[200:203], v27, s[8:11], s28 offen offset:0 sc1
v_accvgpr_write_b32 a224, v164
Mvav 88,20,228
Ta 12,11,24576
Ta 14,11,25600
Mvav 88,16,196
v_accvgpr_write_b32 a225, v165
Mvav 120,16,204
v_accvgpr_write_b32 a226, v166
Mvav 120,20,236
D 19,48,65,3072
v_accvgpr_write_b32 a227, v167
Mvav 152,20,244
s_add_u32 m0, s63, 102400
v_accvgpr_write_b32 a228, v208
Mvav 152,16,212
W 8
Ta 16,12,16384
Ta 18,12,17408
Mvav 156,24,212
v_accvgpr_write_b32 a229, v209
Mvav 156,28,244
v_accvgpr_write_b32 a230, v210
Mvav 124,28,236
Ta 20,12,24576
v_accvgpr_write_b32 a231, v211
Mvav 124,24,204
Ta 22,12,25600
G 164,232
Mvav 92,24,196
G 165,233
Mvav 92,28,228
G 166,234
Mvav 60,28,220
G 167,235
Mvav 60,24,188
s_waitcnt vmcnt(18) lgkmcnt(8)
G 172,236
G 173,237
Mvaa 32,0,32
Ta 24,13,16384
Ta 26,13,17408
Mvaa 32,4,48
buffer_load_dwordx4 v[188:191], v27, s[8:11], s93 offen offset:0 sc1
G 174,238
Mvaa 64,4,52
s_add_u32 s28, s28, 512
s_cmp_eq_u32 s28, s4
s_cselect_b32 s93, 10240, 0
s_sub_u32 s28, s28, s93
Mvaa 64,0,36
s_add_u32 s93, s28, 20480
G 175,239
K 164,164,165
Mvaa 96,0,40
Ta 28,13,24576
Ta 30,13,25600
Mvaa 96,4,56
buffer_load_dwordx4 v[196:199], v26, s[8:11], s28 offen offset:0 sc1
K 165,166,167
Mvaa 128,4,60
K 166,172,173
K 167,174,175
Mvaa 128,0,44
W 8
v_accvgpr_write_b32 a232, v164
v_accvgpr_write_b32 a233, v165
Mvaa 132,8,44
v_accvgpr_write_b32 a234, v166
v_accvgpr_write_b32 a235, v167
Mvaa 132,12,60
buffer_load_dwordx4 v[204:207], v26, s[8:11], s93 offen offset:0 sc1
Ta 0,14,16384
Mvaa 100,12,56
Ta 2,14,17408
G 164,160
Mvaa 100,8,40
G 165,161
G 166,162
Mvaa 68,8,36
Ta 4,14,24576
G 167,163
Mvaa 68,12,52
buffer_load_dwordx4 v[212:215], v27, s[8:11], s28 offen offset:0 sc1
Ta 6,14,25600
Mvaa 36,12,48
G 172,164
G 173,165
Mvaa 36,8,32
W 8
G 174,166
G 175,167
Mvaa 40,16,32
K 164,164,165
K 165,166,167
Mvaa 40,20,48
buffer_load_dwordx4 v[220:223], v27, s[8:11], s93 offen offset:0 sc1
Ta 8,15,16384
Mvaa 72,20,52
s_add_u32 s28, s28, 512
s_cmp_eq_u32 s28, s4
s_cselect_b32 s93, 10240, 0
s_sub_u32 s28, s28, s93
Mvaa 72,16,36
s_add_u32 s93, s28, 20480
Ta 10,15,17408
K 166,172,173
Mvaa 104,16,40
s_cmp_lg_u32 s72, 0
s_cbranch_scc1 .LL_53%=
s_waitcnt vmcnt(7)
.LL_53%=:
v_xor_b32 v28, s12, v28
v_xor_b32 v29, s13, v29
v_lshrrev_b32 v248, 11, v28
v_or_b32 v29, v29, v248
v_cmp_eq_u32 vcc, 0, v29
s_or_b64 vcc, vcc, s[72:73]
s_cmp_eq_u64 vcc, -1
s_cbranch_scc0 .Lspin_54%=
.Lok_55%=:
K 167,174,175
v_accvgpr_write_b32 a236, v164
Mvaa 104,20,56
buffer_load_dwordx4 v[228:231], v26, s[8:11], s28 offen offset:0 sc1
v_accvgpr_write_b32 a237, v165
Mvaa 136,20,60
Ta 12,15,24576
Ta 14,15,25600
Mvaa 136,16,44
W 8
v_accvgpr_write_b32 a238, v166
v_accvgpr_write_b32 a239, v167
Mvaa 140,24,44
G 164,240
G 165,241
Mvaa 140,28,60
buffer_load_dwordx4 v[236:239], v26, s[8:11], s93 offen offset:0 sc1
G 166,242
Mvaa 108,28,56
v_bfe_u32 v248, v28, 3, 8
G 167,243
Mvaa 108,24,40
Ta 16,24,16384
Ta 18,24,17408
Mvaa 76,24,36
v_mul_u32_u24 v248, 10240, v248
G 172,244
Mvaa 76,28,52
D 20,48,65,0
G 173,245
Mvaa 44,28,48
v_and_b32 v28, 7, v28
G 174,246
Mvaa 44,24,32
W 6
v_lshl_add_u32 v248, v28, 4, v248
G 175,247
Mvaa 48,0,32
Ta 20,24,24576
Ta 22,24,25600
Mvaa 48,4,48
buffer_load_dwordx4 v[208:211], v27, s[8:11], s28 offen offset:0 sc1
K 164,164,165
Mvaa 80,4,52
v_lshl_add_u32 v248, v1, 3, v248
K 165,166,167
Mvaa 80,0,36
v_lshrrev_b32 v29, 8, v1
K 166,172,173
Mvaa 112,0,40
K 167,174,175
v_lshlrev_b32 v29, 28, v29
Mvaa 112,4,56
v_accvgpr_write_b32 a240, v164
v_or_b32 v249, v248, v29
Mvaa 144,4,60
s_and_b32 s75, s72, 134217728
Ta 24,25,16384
v_accvgpr_write_b32 a241, v165
Mvaa 144,0,44
W 5
Ta 26,25,17408
v_accvgpr_write_b32 a242, v166
Mvaa 148,8,44
s_add_u32 s76, s75, 131072
v_accvgpr_write_b32 a243, v167
G 164,168
Mvaa 148,12,60
buffer_load_dwordx4 v[240:243], v248, s[8:11], s76 offen sc1
G 165,169
Mvaa 116,12,56
s_add_u32 s76, s76, 8192
Ta 28,25,24576
Ta 30,25,25600
Mvaa 116,8,40
G 166,170
G 167,171
Mvaa 84,8,36
G 172,172
G 173,173
Mvaa 84,12,52
buffer_load_dwordx4 v[244:247], v249, s[8:11], s76 offen sc1
v_or_b32 v249, v1, v29
Mvaa 52,12,48
s_mul_i32 s76, s74, 160
G 174,174
G 175,175
)"
R"(Mvaa 52,8,32
W 4
Ta 0,2,0
Ta 2,2,1024
Mvaa 56,16,32
s_add_u32 s76, s76, s75
K 164,164,165
K 165,166,167
Mvaa 56,20,48
s_add_u32 s76, s76, 118784
K 166,172,173
K 167,174,175
Mvaa 88,20,52
Ta 4,2,8192
Ta 6,2,9216
Mvaa 88,16,36
D 21,48,65,1024
v_accvgpr_write_b32 a244, v164
Mvaa 120,16,40
v_accvgpr_write_b32 a245, v165
v_accvgpr_write_b32 a246, v166
Mvaa 120,20,56
v_accvgpr_write_b32 a247, v167
G 164,64
Mvaa 152,20,60
Ta 8,3,0
G 165,65
Mvaa 152,16,44
W 5
Ta 10,3,1024
G 166,66
Mvaa 156,24,44
G 167,67
G 172,68
Mvaa 156,28,60
Ta 12,3,8192
G 173,69
Mvaa 124,28,56
Ta 14,3,9216
G 174,70
Mvaa 124,24,40
G 175,71
K 164,164,165
Mvaa 92,24,36
K 165,166,167
K 166,172,173
Mvaa 92,28,52
Ta 16,4,0
K 167,174,175
Mvaa 60,28,48
Ta 18,4,1024
v_accvgpr_write_b32 a64, v164
Mvaa 60,24,32
s_waitcnt vmcnt(19) lgkmcnt(6)
v_accvgpr_write_b32 a65, v165
v_accvgpr_write_b32 a66, v166
Mvav 32,0,184
Ta 20,4,8192
v_accvgpr_write_b32 a67, v167
Mvaa 32,4,140
Ta 22,4,9216
G 164,176
Mvaa 64,4,144
G 165,177
G 166,178
Mvaa 64,0,128
G 167,179
G 172,180
Mvaa 96,0,132
Ta 24,5,0
G 173,181
Mvaa 96,4,148
Ta 26,5,1024
buffer_load_dwordx4 a[32:35], v27, s[8:11], s93 offen offset:0 sc1
Mvaa 128,4,152
s_add_u32 s28, s28, 512
s_cmp_eq_u32 s28, s4
s_cselect_b32 s93, 10240, 0
s_sub_u32 s28, s28, s93
Mvaa 128,0,136
W 6
s_add_u32 s93, s28, 20480
G 174,182
Mvaa 132,8,136
G 175,183
K 164,164,165
Mvaa 132,12,152
buffer_load_dwordx4 a[36:39], v26, s[8:11], s28 offen offset:0 sc1
K 165,166,167
Mvaa 100,12,148
Ta 28,5,8192
Ta 30,5,9216
Mvaa 100,8,132
K 166,172,173
K 167,174,175
Mvaa 68,8,128
v_accvgpr_write_b32 a68, v164
v_accvgpr_write_b32 a69, v165
Mvaa 68,12,144
buffer_load_dwordx4 a[40:43], v26, s[8:11], s93 offen offset:0 sc1
v_accvgpr_write_b32 a70, v166
Mvaa 36,12,140
Ta 0,6,0
v_accvgpr_write_b32 a71, v167
Mvav 36,8,184
W 5
Ta 2,6,1024
G 164,72
Mvav 40,16,184
G 165,73
G 166,74
Mvaa 40,20,140
buffer_load_dwordx4 a[44:47], v27, s[8:11], s28 offen offset:0 sc1
Ta 4,6,8192
Mvaa 72,20,144
Ta 6,6,9216
G 167,75
Mvaa 72,16,128
G 172,76
G 173,77
Mvaa 104,16,132
Ta 8,7,0
G 174,78
Mvaa 104,20,148
buffer_load_dwordx4 a[48:51], v27, s[8:11], s93 offen offset:0 sc1
Ta 10,7,1024
Mvaa 136,20,152
s_add_u32 s28, s28, 512
s_cmp_eq_u32 s28, s4
s_cselect_b32 s93, 10240, 0
s_sub_u32 s28, s28, s93
Mvaa 136,16,136
W 6
s_add_u32 s93, s28, 20480
G 175,79
Mvaa 140,24,136
Ta 12,7,8192
K 164,164,165
Mvaa 140,28,152
buffer_load_dwordx4 a[52:55], v26, s[8:11], s28 offen offset:0 sc1
Ta 14,7,9216
Mvaa 108,28,148
K 165,166,167
K 166,172,173
Mvaa 108,24,132
K 167,174,175
v_accvgpr_write_b32 a72, v164
Mvaa 76,24,128
Ta 16,8,0
Ta 18,8,1024
Mvaa 76,28,144
D 22,48,65,2048
v_accvgpr_write_b32 a73, v165
Mvaa 44,28,140
v_accvgpr_write_b32 a74, v166
v_accvgpr_write_b32 a75, v167
Mvav 44,24,184
W 6
Ta 20,8,8192
Ta 22,8,9216
Mvav 48,0,184
v_accvgpr_write_b32 a76, v216
v_accvgpr_write_b32 a77, v217
Mvaa 48,4,140
buffer_load_dwordx4 a[56:59], v26, s[8:11], s93 offen offset:0 sc1
v_accvgpr_write_b32 a78, v218
Mvaa 80,4,144
Ta 24,9,0
v_accvgpr_write_b32 a79, v219
Mvaa 80,0,128
Ta 26,9,1024
G 164,80
Mvaa 112,0,132
Ta 28,9,8192
G 165,81
Mvaa 112,4,148
buffer_load_dwordx4 a[60:63], v27, s[8:11], s28 offen offset:0 sc1
Ta 30,9,9216
Mvaa 144,4,152
G 166,82
G 167,83
Mvaa 144,0,136
W 8
G 172,84
G 173,85
Mvaa 148,8,136
Ta 0,2,16384
Ta 2,2,17408
Mvaa 148,12,152
D 23,48,65,3072
G 174,86
Mvaa 116,12,148
Ta 4,2,24576
G 175,87
Mvaa 116,8,132
Ta 6,2,25600
K 164,164,165
Mvaa 84,8,128
K 165,166,167
Mvaa 84,12,144
buffer_load_dwordx4 v[216:219], v27, s[8:11], s93 offen offset:0 sc1
K 166,172,173
Mvaa 52,12,140
K 167,174,175
Mvav 52,8,184
W 8
Ta 8,3,16384
Ta 10,3,17408
Mvav 56,16,184
v_accvgpr_write_b32 a80, v164
Mvaa 56,20,140
v_accvgpr_write_b32 a81, v165
Mvaa 88,20,144
Ta 12,3,24576
v_accvgpr_write_b32 a82, v166
Mvaa 88,16,128
Ta 14,3,25600
v_accvgpr_write_b32 a83, v167
Mvaa 120,16,132
v_accvgpr_write_b32 a84, v224
Mvaa 120,20,148
v_lshrrev_b32 v20, 4, v1
Mvaa 152,20,152
v_accvgpr_write_b32 a85, v225
Mvaa 152,16,136
W 8
v_accvgpr_write_b32 a86, v226
v_add_u32 v20, 0, v20
Mvaa 156,24,136
Ta 16,4,16384
Ta 18,4,17408
Mvaa 156,28,152
v_accvgpr_write_b32 a87, v227
v_mul_u32_u24 v22, 205, v20
Mvaa 124,28,148
Ta 20,4,24576
G 164,88
Mvaa 124,24,132
Ta 22,4,25600
v_lshrrev_b32 v22, 12, v22
Mvaa 92,24,128
G 165,89
G 166,90
Mvaa 92,28,144
v_mul_u32_u24 v23, 20, v22
G 167,91
Mvaa 60,28,140
v_sub_u32 v20, v20, v23
Mvav 60,24,184
s_waitcnt vmcnt(18) lgkmcnt(8)
Ta 24,5,16384
G 172,92
Mvaa 32,0,156
s_add_u32 s28, s28, 512
s_cmp_eq_u32 s28, s4
s_cselect_b32 s93, 10240, 0
Ta 26,5,17408
Mvav 32,4,196
s_sub_u32 s28, s28, s93
s_add_u32 s93, s28, 20480
buffer_load_dwordx4 v[184:187], v26, s[8:11], s28 offen offset:0 sc1
Mvav 64,4,204
v_lshrrev_b32 v23, 1, v22
G 173,93
Mvav 64,0,192
G 174,94
v_and_b32 v23, 2, v23
Mvav 96,0,200
Ta 28,5,24576
Ta 30,5,25600
Mvav 96,4,212
buffer_load_dwordx4 a[128:131], v26, s[8:11], s93 offen offset:0 sc1
G 175,95
Mvav 128,4,220
v_xor_b32 v20, v20, v23
K 164,164,165
Mvav 128,0,188
W 8
v_mul_u32_u24 v22, 80, v22
K 165,166,167
Mvav 132,8,188
K 166,172,173
v_add_lshl_u32 v16, v20, v22, 4
Mvav 132,12,220
buffer_load_dwordx4 a[132:135], v27, s[8:11], s28 offen offset:0 sc1
Ta 0,6,16384
Mvav 100,12,212
Ta 2,6,17408
K 167,174,175
Mvav 100,8,200
v_lshrrev_b32 v20, 4, v1
v_accvgpr_write_b32 a88, v164
Mvav 68,8,192
v_add_u32 v20, 64, v20
v_accvgpr_write_b32 a89, v165
Mvav 68,12,204
buffer_load_dwordx4 a[136:139], v27, s[8:11], s93 offen offset:0 sc1
Ta 4,6,24576
Mvav 36,12,196
s_add_u32 s28, s28, 512
s_cmp_eq_u32 s28, s4
s_cselect_b32 s93, 10240, 0
s_sub_u32 s28, s28, s93
Mvaa 36,8,156
W 7
s_add_u32 s93, s28, 20480
Ta 6,6,25600
Mvaa 40,16,156
v_accvgpr_write_b32 a90, v166
v_mul_u32_u24 v22, 205, v20
Mvav 40,20,196
buffer_load_dwordx4 a[140:143], v26, s[8:11], s28 offen offset:0 sc1
v_accvgpr_write_b32 a91, v167
Mvav 72,20,204
v_lshrrev_b32 v22, 12, v22
v_accvgpr_write_b32 a92, v232
Mvav 72,16,192
Ta 8,7,16384
v_mul_u32_u24 v23, 20, v22
Mvav 104,16,200
Ta 10,7,17408
v_accvgpr_write_b32 a93, v233
Mvav 104,20,212
s_cmp_lg_u32 s72, 0
s_cbranch_scc1 .LL_57%=
s_waitcnt vmcnt(17)
.LL_57%=:
buffer_store_dwordx4 v[240:243], v1, s[8:11], s76 offen sc1
v_accvgpr_write_b32 a94, v234
v_sub_u32 v20, v20, v23
Mvav 136,20,220
Ta 12,7,24576
v_accvgpr_write_b32 a95, v235
Mvav 136,16,188
W 7
Ta 14,7,25600
v_lshrrev_b32 v23, 1, v22
Mvav 140,24,188
G 164,96
G 165,97
Mvav 140,28,220
buffer_store_dwordx4 v[244:247], v249, s[8:11], s76 offen offset:1024 sc1
v_and_b32 v23, 2, v23
Mvav 108,28,212
s_add_u32 s76, s74, s75
s_add_u32 s76, s76, 3840
G 166,98
Mvav 108,24,200
G 167,99
v_xor_b32 v20, v20, v23
Mvav 76,24,192
G 172,100
G 173,101
Mvav 76,28,204
buffer_load_dwordx4 a[144:147], v26, s[8:11], s93 offen offset:0 sc1
Ta 16,8,16384
Mvav 44,28,196
Ta 18,8,17408
v_mul_u32_u24 v22, 80, v22
Mvaa 44,24,156
W 6
G 174,102
G 175,103
Mvaa 48,0,156
v_add_lshl_u32 v17, v20, v22, 4
K 164,164,165
Mvav 48,4,196
buffer_load_dwordx4 a[148:151], v27, s[8:11], s28 offen offset:0 sc1
Ta 20,8,24576
Mvav 80,4,204
Ta 22,8,25600
K 165,166,167
Mvav 80,0,192
K 166,172,173
v_subrev_u32 v17, 1024, v17
Mvav 112,0,200
K 167,174,175
v_accvgpr_write_b32 a96, v164
Mvav 112,4,212
buffer_load_dwordx4 a[152:155], v27, s[8:11], s93 offen offset:0 sc1
v_lshrrev_b32 v20, 4, v1
Mvav 144,4,220
s_add_u32 s28, s28, 512
v_accvgpr_write_b32 a97, v165
v_accvgpr_write_b32 a98, v166
Mvav 144,0,188
W 4
v_add_u32 v20, 128, v20
v_accvgpr_write_b32 a99, v167
Mvav 148,8,188
s_cmp_eq_u32 s28, s4
s_cselect_b32 s93, 10240, 0
Ta 24,9,16384
Ta 26,9,17408
Mvav 148,12,220
s_sub_u32 s28, s28, s93
Ta 28,9,24576
Ta 30,9,25600
Mvav 116,12,212
s_add_u32 s93, s28, 20480
)"
R"(buffer_load_dwordx4 v[224:227], v26, s[8:11], s28 offen offset:0 sc1
G 164,248
Mvav 116,8,200
v_mul_u32_u24 v22, 205, v20
G 165,249
Mvav 84,8,192
s_waitcnt vmcnt(12) lgkmcnt(0)
s_barrier
G 166,250
Mvav 84,12,204
v_lshrrev_b32 v22, 12, v22
G 167,251
Mvav 52,12,196
G 172,252
G 173,253
Mvaa 52,8,156
v_mul_u32_u24 v23, 20, v22
G 174,254
Mvaa 56,16,156
G 175,255
v_sub_u32 v20, v20, v23
Mvav 56,20,196
K 164,164,165
K 165,166,167
Mvav 88,20,204
v_lshrrev_b32 v23, 1, v22
K 166,172,173
Mvav 88,16,192
buffer_load_dwordx4 v[232:235], v26, s[8:11], s93 offen offset:0 sc1
K 167,174,175
Mvav 120,16,200
v_accvgpr_write_b32 a100, v164
v_accvgpr_write_b32 a101, v165
v_accvgpr_write_b32 a102, v166
v_accvgpr_write_b32 a103, v167
Mvav 120,20,212
v_and_b32 v23, 2, v23
v_xor_b32 v20, v20, v23
Mvav 152,20,220
Ta 0,10,32768
Ta 2,10,33792
Mvav 152,16,188
Ta 4,10,40960
Ta 6,10,41984
Mvav 156,24,188
G 164,104
v_mul_u32_u24 v22, 80, v22
Mvav 156,28,220
G 165,105
v_add_lshl_u32 v18, v20, v22, 4
Mvav 124,28,212
Ta 8,11,32768
G 166,106
Mvav 124,24,200
Ta 10,11,33792
G 167,107
Mvav 92,24,192
v_subrev_u32 v18, 2048, v18
G 172,108
Mvav 92,28,204
G 173,109
v_lshrrev_b32 v20, 4, v1
Mvav 60,28,196
Ta 12,11,40960
Ta 14,11,41984
Mvaa 60,24,156
W 4
G 174,110
v_add_u32 v20, 192, v20
Mvav 32,0,228
G 175,111
K 164,164,165
Mvaa 32,4,36
buffer_load_dwordx4 a[248:251], v27, s[8:11], s28 offen offset:0 sc1
v_mul_u32_u24 v22, 205, v20
Mvaa 64,4,40
K 165,166,167
v_lshrrev_b32 v22, 12, v22
Mvav 64,0,236
Ta 16,12,32768
K 166,172,173
Mvav 96,0,208
Ta 18,12,33792
K 167,174,175
Mvaa 96,4,44
buffer_load_dwordx4 a[252:255], v27, s[8:11], s93 offen offset:0 sc1
v_mul_u32_u24 v23, 20, v22
Mvaa 128,4,48
s_add_u32 s28, s28, 512
v_accvgpr_write_b32 a104, v164
v_accvgpr_write_b32 a105, v165
Mvaa 128,0,32
W 2
Ta 20,12,40960
v_sub_u32 v20, v20, v23
Mvaa 132,8,32
s_cmp_eq_u32 s28, s4
s_cselect_b32 s93, 10240, 0
Ta 22,12,41984
v_accvgpr_write_b32 a106, v166
Mvaa 132,12,48
s_sub_u32 s28, s28, s93
v_lshrrev_b32 v23, 1, v22
v_accvgpr_write_b32 a107, v167
Mvaa 100,12,44
s_add_u32 s93, s28, 20480
K 176,176,177
v_and_b32 v23, 2, v23
Mvav 100,8,208
K 177,178,179
K 178,180,181
Mvav 68,8,236
Ta 24,13,32768
v_xor_b32 v20, v20, v23
Mvaa 68,12,40
K 179,182,183
v_accvgpr_write_b32 a108, v176
v_accvgpr_write_b32 a109, v177
v_accvgpr_write_b32 a110, v178
v_accvgpr_write_b32 a111, v179
Mvaa 36,12,36
Ta 26,13,33792
buffer_load_dwordx4 v[192:195], v26, s[8:11], s28 offen offset:0 sc1
Mvav 36,8,228
W 2
Ta 28,13,40960
v_mul_u32_u24 v22, 80, v22
Mvav 40,16,228
Ta 30,13,41984
v_add_lshl_u32 v19, v20, v22, 4
Mvaa 40,20,36
v_subrev_u32 v19, 3072, v19
G 164,112
Mvaa 72,20,40
s_cmp_lg_u32 s72, 0
s_cbranch_scc1 .LL_58%=
s_waitcnt vmcnt(8)
.LL_58%=:
v_mov_b64 v[28:29], s[12:13]
s_mov_b64 exec, 1
buffer_store_dwordx2 v[28:29], off, s[8:11], s76 sc1
s_mov_b64 exec, -1
v_lshrrev_b32 v20, 4, v1
G 165,113
Mvav 72,16,236
Ta 0,14,32768
Ta 2,14,33792
Mvav 104,16,208
G 166,114
v_add_u32 v20, 256, v20
Mvaa 104,20,44
G 167,115
v_mul_u32_u24 v22, 205, v20
Mvaa 136,20,48
buffer_load_dwordx4 v[200:203], v26, s[8:11], s93 offen offset:0 sc1
Ta 4,14,40960
Mvaa 136,16,32
W 3
Ta 6,14,41984
G 172,116
Mvaa 140,24,32
v_lshrrev_b32 v22, 12, v22
G 173,117
Mvaa 140,28,48
G 174,118
v_mul_u32_u24 v23, 20, v22
Mvaa 108,28,44
G 175,119
v_sub_u32 v20, v20, v23
Mvav 108,24,208
Ta 8,15,32768
Ta 10,15,33792
Mvav 76,24,236
buffer_load_dwordx4 v[188:191], v27, s[8:11], s28 offen offset:0 sc1
K 164,164,165
Mvaa 76,28,40
v_lshrrev_b32 v23, 1, v22
K 165,166,167
Mvaa 44,28,36
K 166,172,173
v_and_b32 v23, 2, v23
Mvav 44,24,228
W 2
Ta 12,15,40960
Ta 14,15,41984
Mvav 48,0,228
K 167,174,175
v_xor_b32 v20, v20, v23
Mvaa 48,4,36
v_accvgpr_write_b32 a112, v164
v_accvgpr_write_b32 a113, v165
Mvaa 80,4,40
v_mul_u32_u24 v22, 80, v22
v_accvgpr_write_b32 a114, v166
Mvav 80,0,236
Ta 16,24,32768
Ta 18,24,33792
Mvav 112,0,208
v_add_lshl_u32 v20, v20, v22, 4
v_accvgpr_write_b32 a115, v167
Mvaa 112,4,44
buffer_load_dwordx4 v[196:199], v27, s[8:11], s93 offen offset:0 sc1
v_lshrrev_b32 v22, 6, v1
Mvaa 144,4,48
v_accvgpr_write_b32 a116, v168
v_accvgpr_write_b32 a117, v169
v_accvgpr_write_b32 a118, v170
v_accvgpr_write_b32 a119, v171
Mvaa 144,0,32
W 2
Ta 20,24,40960
Ta 22,24,41984
Mvaa 148,8,32
s_add_u32 s28, s28, 512
v_mul_u32_u24 v22, 320, v22
v_bfe_u32 v23, v1, 4, 2
Mvaa 148,12,48
s_cmp_eq_u32 s28, s4
s_cselect_b32 s93, 10240, 0
v_lshl_add_u32 v22, v23, 3, v22
G 164,120
Mvaa 116,12,44
s_sub_u32 s28, s28, s93
Ta 24,25,32768
Ta 26,25,33792
Mvav 116,8,208
s_add_u32 s93, s28, 20480
G 165,121
v_bfe_u32 v23, v1, 8, 1
Mvav 84,8,236
buffer_load_dwordx4 v[204:207], v26, s[8:11], s28 offen offset:0 sc1
G 166,122
Mvaa 84,12,40
v_lshlrev_b32 v23, 5, v23
G 167,123
Mvaa 52,12,36
Ta 28,25,40960
Ta 30,25,41984
Mvav 52,8,228
W 4
v_add_u32 v21, v22, v23
G 172,124
Mvav 56,16,228
G 173,125
v_sub_u32 v22, v22, v23
Mvaa 56,20,36
s_mul_i32 s94, s59, 655360
Ta 0,10,49152
G 174,126
Mvaa 88,20,40
s_mul_i32 s95, s58, 320
Ta 2,10,50176
G 175,127
Mvav 88,16,236
s_add_u32 s94, s94, s95
buffer_load_dwordx4 v[212:215], v26, s[8:11], s93 offen offset:0 sc1
K 164,164,165
Mvav 120,16,208
Ta 4,10,57344
K 165,166,167
Mvaa 120,20,44
Ta 6,10,58368
K 166,172,173
Mvaa 152,20,48
s_add_u32 s40, s26, s94
s_addc_u32 s41, s27, 0
K 167,174,175
Mvaa 152,16,32
W 4
v_accvgpr_write_b32 a120, v164
v_accvgpr_write_b32 a121, v165
Mvaa 156,24,32
s_and_b32 s41, s41, 0xffff
Ta 8,11,49152
Ta 10,11,50176
Mvaa 156,28,48
s_mov_b32 s42, 5242880
buffer_load_dwordx4 v[220:223], v27, s[8:11], s28 offen offset:0 sc1
v_accvgpr_write_b32 a122, v166
Mvaa 124,28,44
s_mov_b32 s43, 0x20000
Ta 12,11,57344
v_accvgpr_write_b32 a123, v167
Mvav 124,24,208
s_sub_u32 s42, s42, s94
Ta 14,11,58368
v_accvgpr_write_b32 a124, v160
Mvav 92,24,236
s_mov_b64 s[44:45], s[8:9]
v_accvgpr_write_b32 a125, v161
v_accvgpr_write_b32 a126, v162
Mvaa 92,28,40
s_mov_b32 s47, 0x20000
Ta 16,12,49152
v_accvgpr_write_b32 a127, v163
Mvaa 60,28,36
s_and_b32 s48, s14, 1
Ta 18,12,50176
buffer_load_dwordx4 v[176:179], v27, s[8:11], s93 offen offset:0 sc1
Mvav 60,24,228
s_waitcnt vmcnt(19) lgkmcnt(6)
v_bfe_u32 v28, v1, 4, 2
v_lshlrev_b32 v28, 3, v28
Mvaa 32,0,52
s_lshl_b32 s80, s56, 9
Ta 20,12,57344
Ta 22,12,58368
Mvav 32,4,184
s_mul_i32 s48, s48, 5120
s_add_u32 s80, s80, 4096
s_lshr_b32 s51, s14, 1
Mvaa 64,4,128
s_and_b32 s94, s14, 1
buffer_load_dwordx2 v[28:29], v28, s[8:11], s80 offen sc1
Ta 24,13,49152
Mvaa 64,0,56
s_lshl_b32 s95, s57, 1
Ta 26,13,50176
Ta 28,13,57344
Mvaa 96,0,60
s_add_u32 s94, s94, s95
s_lshl_b32 s54, s94, 3
Ta 30,13,58368
Mvaa 96,4,132
Mvaa 128,4,136
s_add_u32 s54, s54, s59
Mvav 128,0,216
W 8
Ta 0,14,49152
Ta 2,14,50176
Mvav 132,8,216
s_lshl_b32 s54, s54, 6
Mvaa 132,12,136
s_xor_b32 s55, s54, 15872
Mvaa 100,12,132
Ta 4,14,57344
Ta 6,14,58368
Mvaa 100,8,60
s_add_u32 s54, s54, 20480
Mvaa 68,8,56
s_and_b32 s81, s14, 1
Mvaa 68,12,128
s_add_u32 s55, s55, 20480
Mvav 36,12,184
Mvaa 36,8,52
W 8
s_and_b32 s95, s14, 2
s_mul_i32 s91, s81, 17164288
Mvaa 40,16,52
s_lshl_b32 s94, s94, 2
Ta 8,15,49152
Ta 10,15,50176
Mvav 40,20,184
s_mul_i32 s82, s56, 536576
Mvaa 72,20,128
s_add_u32 s94, s94, s95
Ta 12,15,57344
Ta 14,15,58368
Mvaa 72,16,56
Mvaa 104,16,60
s_lshl_b32 s95, s95, 4
s_add_u32 s91, s91, s82
Mvaa 104,20,132
Mvaa 136,20,136
s_add_u32 s55, s55, s95
s_add_u32 s91, s91, 4194304
Mvav 136,16,216
W 8
s_lshl_b32 s49, s94, 17
Ta 16,24,49152
Mvav 140,24,216
Ta 18,24,50176
Mvaa 140,28,136
s_lshl_b32 s95, s59, 14
s_lshr_b32 s82, s14, 1
Mvaa 108,28,132
Ta 20,24,57344
Ta 22,24,58368
Mvaa 108,24,60
s_add_u32 s49, s49, s95
s_lshl_b32 s83, s82, 14
Mvaa 76,24,56
s_xor_b32 s50, s49, 16252928
Mvaa 76,28,128
Mvav 44,28,184
s_add_u32 s91, s91, s83
s_add_u32 s49, s49, 39845888
Mvaa 44,24,52
W 8
Ta 24,25,49152
Ta 26,25,50176
Mvaa 48,0,52
s_add_u32 s50, s50, 39845888
s_lshl_b32 s81, s81, 14
Mvav 48,4,184
Mvaa 80,4,128
s_lshr_b32 s94, s15, 3
Ta 28,25,57344
Ta 30,25,58368
Mvaa 80,0,56
)"
R"(s_lshl_b32 s82, s82, 16
s_lshl_b32 s94, s94, 3
Mvaa 112,0,60
Mvaa 112,4,132
s_add_u32 s94, s94, s14
s_or_b32 s92, s81, s82
Mvaa 144,4,136
Mvav 144,0,216
W 8
s_lshl_b32 s94, s94, 4
Ta 0,2,32768
Mvav 148,8,216
s_xor_b32 s92, s92, 0x10000
s_add_u32 s94, s94, s59
Ta 2,2,33792
Mvaa 148,12,136
Mvaa 116,12,132
s_mul_i32 s52, s94, 5120
Ta 4,2,40960
Ta 6,2,41984
Mvaa 116,8,60
Mvaa 84,8,56
s_lshl_b32 s95, s59, 12
Mvaa 84,12,128
Mvav 52,12,184
s_sub_u32 s52, s52, s95
Mvaa 52,8,52
W 8
s_add_u32 s52, s52, 56623104
Ta 8,3,32768
Mvaa 56,16,52
Ta 10,3,33792
Mvav 56,20,184
s_lshl_b32 s53, s94, 3
Mvaa 88,20,128
Ta 12,3,40960
Ta 14,3,41984
Mvaa 88,16,56
s_add_u32 s53, s53, 65536
Mvaa 120,16,60
s_waitcnt vmcnt(0)
v_cmp_eq_u64 vcc, s[12:13], v[28:29]
s_cmp_eq_u64 vcc, -1
s_cbranch_scc0 .Lspin_59%=
.Lok_60%=:
s_sub_u32 s82, s91, 16384
Mvaa 120,20,132
s_xor_b32 s83, s92, 0x10000
s_add_u32 m0, s83, 0
s_add_u32 s81, s82, 0
Mvaa 152,20,136
v_bfe_u32 v244, v1, 4, 1
v_mul_u32_u24 v244, 0xaaaa, v244
Mvav 152,16,216
W 8
Dc 1,8,81,0
v_add_u32 v244, 0x4444, v244
Mvav 156,24,216
Ta 16,4,32768
Ta 18,4,33792
Mvaa 156,28,136
v_lshl_or_b32 v240, v244, 16, v244
Mvaa 124,28,132
Ta 20,4,40960
v_mov_b32 v241, 0xff800000
Mvaa 124,24,60
Ta 22,4,41984
Dc 1,8,81,1024
Mvaa 92,24,56
v_add_u32 v242, 0x10000, v1
Mvaa 92,28,128
v_bfe_u32 v244, v1, 4, 4
Mvav 60,28,184
v_lshrrev_b32 v245, 8, v1
Mvaa 60,24,52
W 8
Dc 1,8,81,2048
v_lshlrev_b32 v245, 2, v245
Mvaa 32,0,140
Ta 24,5,32768
Ta 26,5,33792
Mvav 32,4,224
v_sub_u32 v244, v244, v245
Mvav 64,4,232
Ta 28,5,40960
v_cmp_le_i32_e64 s[72:73], 0, v244
Mvaa 64,0,144
Ta 30,5,41984
Dc 1,8,81,3072
Mvaa 96,0,148
s_add_u32 m0, s83, 4096
s_add_u32 s81, s82, 4096
v_cmp_le_i32_e64 s[74:75], 1, v244
Mvaa 96,4,248
v_cmp_le_i32_e64 s[76:77], 2, v244
Mvaa 128,4,252
v_cmp_le_i32_e64 s[78:79], 3, v244
Mvaa 128,0,152
W 8
Dc 1,8,81,0
Ta 0,6,32768
Mvaa 132,8,152
Ta 2,6,33792
v_add_u32 v243, 4096, v1
Mvaa 132,12,252
v_add_u32 v244, 8192, v1
Mvaa 100,12,248
Ta 4,6,40960
v_add_u32 v245, 12288, v1
Mvaa 100,8,148
Ta 6,6,41984
Dc 1,8,81,1024
Mvaa 68,8,144
s_mov_b32 s18, 0x3f803f80
Mvav 68,12,232
s_mov_b32 s19, 0x3f003f00
Mvav 36,12,224
Mvaa 36,8,140
W 8
Dc 1,8,81,2048
v_accvgpr_write_b32 a176, s18
Mvaa 40,16,140
Ta 8,7,32768
Ta 10,7,33792
Mvav 40,20,224
v_accvgpr_write_b32 a180, s19
v_mov_b32 v252, v241
Mvav 72,20,232
Ta 12,7,40960
Ta 14,7,41984
Mvaa 72,16,144
Dc 1,8,81,3072
v_accvgpr_write_b32 a177, s18
Mvaa 104,16,148
s_add_u32 m0, s83, 8192
s_add_u32 s81, s82, 8192
v_accvgpr_write_b32 a181, s19
Mvaa 104,20,248
v_mov_b32 v253, v241
Mvaa 136,20,252
Mvaa 136,16,152
W 8
Ta 16,8,32768
v_accvgpr_write_b32 a178, s18
Mvaa 140,24,152
Ta 18,8,33792
Dc 1,8,81,0
Mvaa 140,28,252
v_accvgpr_write_b32 a182, s19
Mvaa 108,28,248
Ta 20,8,40960
v_mov_b32 v254, v241
Mvaa 108,24,148
Ta 22,8,41984
v_accvgpr_write_b32 a179, s18
Mvaa 76,24,144
Dc 1,8,81,1024
v_accvgpr_write_b32 a183, s19
Mvav 76,28,232
Mvav 44,28,224
v_mov_b32 v255, v241
Mvaa 44,24,140
W 8
Ta 24,9,32768
v_accvgpr_write_b32 a160, 0
Mvaa 48,0,140
Ta 26,9,33792
Dc 1,8,81,2048
Mvav 48,4,224
v_accvgpr_write_b32 a161, 0
Mvav 80,4,232
Ta 28,9,40960
v_accvgpr_write_b32 a162, 0
Mvaa 80,0,144
Ta 30,9,41984
v_accvgpr_write_b32 a163, 0
Mvaa 112,0,148
Dc 1,8,81,3072
Mvaa 112,4,248
s_add_u32 m0, s83, 12288
s_add_u32 s81, s82, 12288
v_accvgpr_write_b32 a164, 0
Mvaa 144,4,252
v_accvgpr_write_b32 a165, 0
Mvaa 144,0,152
W 8
Ta 0,2,49152
v_accvgpr_write_b32 a166, 0
Mvaa 148,8,152
Ta 2,2,50176
Dc 1,8,81,0
Mvaa 148,12,252
v_accvgpr_write_b32 a167, 0
Mvaa 116,12,248
Ta 4,2,57344
v_accvgpr_write_b32 a168, 0
Mvaa 116,8,148
Ta 6,2,58368
v_accvgpr_write_b32 a169, 0
Mvaa 84,8,144
Dc 1,8,81,1024
Mvav 84,12,232
v_accvgpr_write_b32 a170, 0
Mvav 52,12,224
v_accvgpr_write_b32 a171, 0
Mvaa 52,8,140
W 8
Ta 8,3,49152
Ta 10,3,50176
Mvaa 56,16,140
Dc 1,8,81,2048
v_accvgpr_write_b32 a172, 0
Mvav 56,20,224
v_accvgpr_write_b32 a173, 0
Mvav 88,20,232
Ta 12,3,57344
v_accvgpr_write_b32 a174, 0
Mvaa 88,16,144
Ta 14,3,58368
Mvaa 120,16,148
Dc 1,8,81,3072
v_accvgpr_write_b32 a175, 0
Mvaa 120,20,248
s_mov_b32 s90, 0x5d800000
Mvaa 152,20,252
s_mov_b64 s[88:89], 0
Mvaa 152,16,152
W 8
Ta 16,4,49152
Ta 18,4,50176
Mvaa 156,24,152
s_mov_b32 s64, 0
Mvaa 156,28,252
s_and_b32 s18, s14, 1
Mvaa 124,28,248
s_lshl_b32 s62, s18, 14
Ta 20,4,57344
Ta 22,4,58368
Mvaa 124,24,148
Mvaa 92,24,144
s_lshl_b32 s20, s57, 15
Mvav 92,28,232
s_sub_u32 s20, 507904, s20
Mvav 60,28,224
Mvaa 60,24,140
W 8
s_add_u32 s20, s20, s62
Ta 24,5,49152
Mvav 32,0,192
s_lshl_b32 s65, s57, 1
Ta 26,5,50176
Mvav 32,4,204
s_sub_u32 s65, s65, 1
Mvav 64,4,212
Ta 28,5,57344
Ta 30,5,58368
Mvav 64,0,200
s_max_i32 s65, s65, 0
v_bfe_u32 v28, v1, 4, 4
v_lshlrev_b32 v28, 3, v28
Mvav 96,0,188
s_sub_u32 s66, s60, 1
s_lshl_b32 s80, s56, 9
Mvav 96,4,220
s_add_u32 s80, s80, 4096
s_cmp_lt_u32 s14, 2
s_cselect_b32 s20, s20, 0
s_cselect_b32 s66, s66, 15
s_cselect_b32 s21, 32768, 0
Mvav 128,4,176
buffer_load_dwordx2 v[28:29], v28, s[8:11], s80 offen sc1
Mvav 128,0,196
W 8
s_add_u32 s61, s91, s20
Ta 0,6,49152
Mvav 132,8,196
s_add_u32 s62, s62, s21
Ta 2,6,50176
Mvav 132,12,176
Mvav 100,12,220
s_sub_u32 s63, 16384, s21
Ta 4,6,57344
Ta 6,6,58368
Mvav 100,8,188
s_lshl_b32 s67, s60, 7
Mvav 68,8,200
s_add_u32 s67, s67, s56
Mvav 68,12,212
Mvav 36,12,204
s_lshl_b32 s67, s67, 12
Mvav 36,8,192
W 8
s_add_u32 s67, s67, 39845888
Ta 8,7,49152
Mvav 40,16,192
s_lshl_b32 s69, s60, 3
Ta 10,7,50176
Mvav 40,20,204
Mvav 72,20,212
s_add_u32 s69, s69, s59
Ta 12,7,57344
Ta 14,7,58368
Mvav 72,16,200
s_lshl_b32 s69, s69, 6
Mvav 104,16,188
Mvav 104,20,220
s_lshl_b32 s21, s58, 3
Mvav 136,20,176
s_add_u32 s69, s69, s21
Mvav 136,16,196
W 8
s_add_u32 s69, s69, 20480
Ta 16,8,49152
Mvav 140,24,196
Ta 18,8,50176
Mvav 140,28,176
s_mov_b32 s70, 0
Mvav 108,28,220
s_mov_b32 s37, 0
Ta 20,8,57344
Ta 22,8,58368
Mvav 108,24,188
s_sub_u32 s18, s57, 4
Mvav 76,24,200
Mvav 76,28,212
s_lshl_b32 s18, s18, 2
Mvav 44,28,204
s_add_u32 s18, s18, s58
Mvav 44,24,192
W 8
Ta 24,9,49152
Ta 26,9,50176
Mvav 48,0,192
s_mul_i32 s85, s59, 7
Mvav 48,4,204
s_add_u32 s85, s85, s18
Mvav 80,4,212
s_cmp_lt_u32 s18, 7
s_cselect_b32 s85, s85, 99
Ta 28,9,57344
Ta 30,9,58368
Mvav 80,0,200
Mvav 112,0,188
s_cmp_lt_u32 s85, 54
s_cselect_b32 s65, 2, s65
Mvav 112,4,220
s_mov_b32 s68, 0
Mvav 144,4,176
s_mov_b64 s[24:25], -1
Mvav 144,0,196
W 8
Mvav 148,8,196
s_mov_b32 s22, 0x3e0293ee
Mvav 148,12,176
s_mov_b32 s23, 0x3e0293ee
Mvav 116,12,220
Mvav 116,8,188
Mvav 84,8,200
Mvav 84,12,212
Mvav 52,12,204
Mvav 52,8,192
W 4
Mvav 56,16,192
Mvav 56,20,204
s_waitcnt vmcnt(0)
v_cmp_eq_u64 vcc, s[12:13], v[28:29]
s_cmp_eq_u64 vcc, -1
s_cbranch_scc0 .Lspin_62%=
.Lok_63%=:
Mvav 88,20,212
Mvav 88,16,200
Mvav 120,16,188
W 0
s_barrier
Ra 128,242,0
Mvav 120,20,220
Mvav 152,20,176
Ra 132,242,1024
Mvav 152,16,196
Mvav 156,24,196
Ra 136,242,2048
Mvav 156,28,176
Mvav 124,28,220
Ra 140,242,3072
Mvav 124,24,188
Mvav 92,24,200
Mvav 92,28,212
Mvav 60,28,204
Mvav 60,24,192
.Lbgo%=:
v_pk_mul_f32 v[32:33], v[32:33], s[22:23]
v_pk_mul_f32 v[34:35], v[34:35], s[22:23]
v_pk_mul_f32 v[36:37], v[36:37], s[22:23]
v_pk_mul_f32 v[38:39], v[38:39], s[22:23]
v_pk_mul_f32 v[64:65], v[64:65], s[22:23]
v_pk_mul_f32 v[66:67], v[66:67], s[22:23]
v_pk_mul_f32 v[68:69], v[68:69], s[22:23]
v_pk_mul_f32 v[70:71], v[70:71], s[22:23]
v_pk_mul_f32 v[96:97], v[96:97], s[22:23]
v_pk_mul_f32 v[98:99], v[98:99], s[22:23]
)"
R"(v_pk_mul_f32 v[100:101], v[100:101], s[22:23]
v_pk_mul_f32 v[102:103], v[102:103], s[22:23]
v_pk_mul_f32 v[128:129], v[128:129], s[22:23]
v_pk_mul_f32 v[130:131], v[130:131], s[22:23]
v_pk_mul_f32 v[132:133], v[132:133], s[22:23]
v_pk_mul_f32 v[134:135], v[134:135], s[22:23]
v_pk_mul_f32 v[40:41], v[40:41], s[22:23]
v_pk_mul_f32 v[42:43], v[42:43], s[22:23]
v_pk_mul_f32 v[44:45], v[44:45], s[22:23]
v_pk_mul_f32 v[46:47], v[46:47], s[22:23]
v_pk_mul_f32 v[72:73], v[72:73], s[22:23]
v_pk_mul_f32 v[74:75], v[74:75], s[22:23]
v_pk_mul_f32 v[76:77], v[76:77], s[22:23]
v_pk_mul_f32 v[78:79], v[78:79], s[22:23]
v_pk_mul_f32 v[104:105], v[104:105], s[22:23]
v_pk_mul_f32 v[106:107], v[106:107], s[22:23]
v_pk_mul_f32 v[108:109], v[108:109], s[22:23]
v_pk_mul_f32 v[110:111], v[110:111], s[22:23]
v_pk_mul_f32 v[136:137], v[136:137], s[22:23]
v_pk_mul_f32 v[138:139], v[138:139], s[22:23]
v_pk_mul_f32 v[140:141], v[140:141], s[22:23]
v_pk_mul_f32 v[142:143], v[142:143], s[22:23]
v_pk_mul_f32 v[48:49], v[48:49], s[22:23]
v_pk_mul_f32 v[50:51], v[50:51], s[22:23]
v_pk_mul_f32 v[52:53], v[52:53], s[22:23]
v_pk_mul_f32 v[54:55], v[54:55], s[22:23]
v_pk_mul_f32 v[80:81], v[80:81], s[22:23]
v_pk_mul_f32 v[82:83], v[82:83], s[22:23]
v_pk_mul_f32 v[84:85], v[84:85], s[22:23]
v_pk_mul_f32 v[86:87], v[86:87], s[22:23]
v_pk_mul_f32 v[112:113], v[112:113], s[22:23]
v_pk_mul_f32 v[114:115], v[114:115], s[22:23]
v_pk_mul_f32 v[116:117], v[116:117], s[22:23]
v_pk_mul_f32 v[118:119], v[118:119], s[22:23]
v_pk_mul_f32 v[144:145], v[144:145], s[22:23]
v_pk_mul_f32 v[146:147], v[146:147], s[22:23]
v_pk_mul_f32 v[148:149], v[148:149], s[22:23]
v_pk_mul_f32 v[150:151], v[150:151], s[22:23]
v_pk_mul_f32 v[56:57], v[56:57], s[22:23]
v_pk_mul_f32 v[58:59], v[58:59], s[22:23]
v_pk_mul_f32 v[60:61], v[60:61], s[22:23]
v_pk_mul_f32 v[62:63], v[62:63], s[22:23]
v_pk_mul_f32 v[88:89], v[88:89], s[22:23]
v_pk_mul_f32 v[90:91], v[90:91], s[22:23]
v_pk_mul_f32 v[92:93], v[92:93], s[22:23]
v_pk_mul_f32 v[94:95], v[94:95], s[22:23]
v_pk_mul_f32 v[120:121], v[120:121], s[22:23]
v_pk_mul_f32 v[122:123], v[122:123], s[22:23]
v_pk_mul_f32 v[124:125], v[124:125], s[22:23]
v_pk_mul_f32 v[126:127], v[126:127], s[22:23]
v_pk_mul_f32 v[152:153], v[152:153], s[22:23]
v_pk_mul_f32 v[154:155], v[154:155], s[22:23]
v_pk_mul_f32 v[156:157], v[156:157], s[22:23]
v_pk_mul_f32 v[158:159], v[158:159], s[22:23]
K 32,32,33
K 33,34,35
K 34,36,37
K 35,38,39
K 40,40,41
K 41,42,43
K 42,44,45
K 43,46,47
W 0
Zvav 196,128,32
s_add_u32 m0, s62, 0
Ra 144,242,4096
Ra 148,242,5120
Ra 152,242,6144
Ra 156,242,7168
Mvav 196,132,40
K 64,64,65
K 65,66,67
K 66,68,69
K 67,70,71
K 72,72,73
K 73,74,75
K 74,76,77
K 75,78,79
s_nop 1
Zvav 204,132,72
Dc 1,8,61,0
K 96,96,97
K 97,98,99
K 98,100,101
Mvav 204,128,64
K 99,102,103
K 104,104,105
K 105,106,107
K 106,108,109
K 107,110,111
Zvav 212,128,96
Dc 1,8,61,1024
K 128,128,129
K 129,130,131
K 130,132,133
Mvav 212,132,104
K 131,134,135
K 136,136,137
K 137,138,139
K 138,140,141
K 139,142,143
s_nop 1
Zvav 220,132,136
Dc 1,8,61,2048
K 48,48,49
K 49,50,51
K 50,52,53
Mvav 220,128,128
K 51,54,55
K 56,56,57
K 57,58,59
K 58,60,61
K 59,62,63
Mvav 196,136,48
Ra 128,242,8192
Ra 132,242,9216
K 80,80,81
K 81,82,83
Mvav 196,140,56
K 82,84,85
K 83,86,87
K 88,88,89
K 89,90,91
K 90,92,93
K 91,94,95
s_nop 1
Mvav 204,140,88
Dc 1,8,61,3072
s_add_u32 m0, s62, 4096
K 112,112,113
K 113,114,115
K 114,116,117
Mvav 204,136,80
K 115,118,119
K 120,120,121
K 121,122,123
K 122,124,125
K 123,126,127
Mvav 212,136,112
Dc 243,8,61,0
K 144,144,145
K 145,146,147
K 146,148,149
Mvav 212,140,120
K 147,150,151
K 152,152,153
K 153,154,155
K 154,156,157
K 155,158,159
s_nop 1
Mvav 220,140,152
s_add_u32 s80, s62, 131072
Dc 243,8,61,1024
v_add_u32 v12, s80, v1
Mvav 220,136,144
W 2
ds_write_b128 v12, v[32:35] offset:0
Ra 136,242,10240
Zvav 36,144,32
Ra 0,12,0
Ra 140,242,11264
Dc 243,8,61,2048
Mvav 36,148,40
v_max3_f32 v4, v196, v197, v198
v_max3_f32 v4, v4, v199, v252
v_max3_f32 v5, v204, v205, v206
v_max3_f32 v5, v5, v207, v253
Zvav 68,148,72
v_max3_f32 v6, v212, v213, v214
v_max3_f32 v6, v6, v215, v254
v_max3_f32 v7, v220, v221, v222
v_max3_f32 v7, v7, v223, v255
Mvav 68,144,64
v_mov_b32 v8, v4
v_mov_b32 v9, v5
v_mov_b32 v10, v6
v_mov_b32 v11, v7
Zvav 100,144,96
ds_write_b128 v12, v[40:43] offset:1024
v_permlane32_swap_b32 v4, v8
v_permlane32_swap_b32 v5, v9
v_permlane32_swap_b32 v6, v10
Mvav 100,148,104
v_permlane32_swap_b32 v7, v11
v_max_f32 v4, v4, v8
v_max_f32 v5, v5, v9
v_max_f32 v6, v6, v10
Zvav 132,148,136
v_max_f32 v7, v7, v11
v_mov_b32 v8, v4
v_mov_b32 v9, v5
v_mov_b32 v10, v6
Mvav 132,144,128
Ra 4,12,1024
v_mov_b32 v11, v7
v_permlane16_swap_b32 v4, v8
v_permlane16_swap_b32 v5, v9
Mvav 36,152,48
v_permlane16_swap_b32 v6, v10
v_permlane16_swap_b32 v7, v11
v_max_f32 v4, v4, v8
v_max_f32 v5, v5, v9
Mvav 36,156,56
v_max_f32 v6, v6, v10
v_max_f32 v7, v7, v11
v_sub_f32 v4, 0, v4
v_sub_f32 v5, 0, v5
Mvav 68,156,88
v_sub_f32 v6, 0, v6
v_sub_f32 v7, 0, v7
v_mov_b32 v224, v4
v_mov_b32 v225, v4
Mvav 68,152,80
v_mov_b32 v226, v4
v_mov_b32 v227, v4
v_mov_b32 v228, v5
v_mov_b32 v229, v5
Mvav 100,152,112
v_mov_b32 v230, v5
v_mov_b32 v231, v5
v_mov_b32 v232, v6
v_mov_b32 v233, v6
Mvav 100,156,120
v_mov_b32 v234, v6
v_mov_b32 v235, v6
v_mov_b32 v236, v7
v_mov_b32 v237, v7
Mvav 132,156,152
Dc 243,8,61,3072
s_add_u32 m0, s62, 8192
ds_write_b128 v12, v[48:51] offset:2048
v_mov_b32 v238, v7
v_mov_b32 v239, v7
Mvav 132,152,144
W 3
Ra 8,12,2048
ds_write_b128 v12, v[56:59] offset:3072
Ra 144,242,12288
Ra 12,12,3072
Cvav 160,128,32,224
s_and_b32 s80, s14, 1
Dc 244,8,61,0
Ra 148,242,13312
ds_write_b128 v12, v[64:67] offset:4096
Ra 16,12,4096
Mvav 160,132,40
s_lshl_b32 s80, s80, 14
ds_write_b128 v12, v[72:75] offset:5120
Ra 20,12,5120
ds_write_b128 v12, v[80:83] offset:6144
A 196,196,224
Cvav 164,132,72,228
s_add_u32 s80, s80, 98304
Dc 244,8,61,1024
Ra 24,12,6144
A 197,197,224
A 198,198,224
Mvav 164,128,64
Ra 152,242,14336
A 199,199,224
X 196
Cvav 168,128,96,232
Dc 244,8,61,2048
Ra 156,242,15360
X 197
Mvav 168,132,104
ds_write_b128 v12, v[88:91] offset:7168
Ra 28,12,7168
X 198
Cvav 172,132,136,236
Dc 244,8,61,3072
s_add_u32 m0, s62, 12288
K 192,196,197
X 199
Mvav 172,128,128
ds_write_b128 v12, v[96:99] offset:8192
A 204,204,228
v_add_u32 v8, s80, v1
K 193,198,199
Mvav 160,136,48
Dc 245,8,61,0
Ra 32,12,8192
A 205,205,228
A 206,206,228
Mvav 160,140,56
ds_write_b128 v8, v[4:7]
A 207,207,228
X 204
Mvav 164,140,88
Dc 245,8,61,1024
ds_write_b128 v12, v[104:107] offset:9216
X 205
Mvav 164,136,80
Ra 36,12,9216
X 206
K 200,204,205
Mvav 168,136,112
Dc 245,8,61,2048
Ra 128,242,24576
X 207
Mvav 168,140,120
ds_write_b128 v12, v[112:115] offset:10240
Ra 132,242,25600
A 212,212,232
K 201,206,207
Mvav 172,140,152
Dc 245,8,61,3072
s_add_u32 s61, s61, s63
Ra 40,12,10240
A 213,213,232
A 214,214,232
Mvav 172,136,144
W 11
ds_write_b128 v12, v[120:123] offset:11264
A 215,215,232
X 212
Cvav 176,144,32,224
X 213
X 214
Mvav 176,148,40
Ra 44,12,11264
K 208,212,213
X 215
Cvav 180,148,72,228
ds_write_b128 v12, v[128:131] offset:12288
A 220,220,236
K 209,214,215
A 221,221,236
Mvav 180,144,64
Ra 48,12,12288
Ra 136,242,26624
A 222,222,236
A 223,223,236
Cvav 184,144,96,232
ds_write_b128 v12, v[136:139] offset:13312
Ra 140,242,27648
X 220
Mvav 184,148,104
X 221
X 222
Cvav 188,148,136,236
Ra 52,12,13312
K 216,220,221
X 223
Mvav 188,144,128
ds_write_b128 v12, v[144:147] offset:14336
Ra 56,12,14336
A 36,36,224
K 217,222,223
Mvav 176,152,48
ds_write_b128 v12, v[152:155] offset:15360
A 37,37,224
A 38,38,224
A 39,39,224
Mvav 176,156,56
X 36
X 37
Mvav 180,156,88
Ra 60,12,15360
X 38
K 194,36,37
Mvav 180,152,80
Ra 144,242,28672
X 39
A 68,68,228
Mvav 184,152,112
Ra 148,242,29696
K 195,38,39
A 69,69,228
A 70,70,228
Mvav 184,156,120
W 3
s_barrier
A 71,71,228
X 68
Mvav 188,156,152
X 69
X 70
K 202,68,69
X 71
A 100,100,232
K 203,70,71
A 101,101,232
A 102,102,232
A 103,103,232
X 100
X 101
X 102
K 210,100,101
X 103
A 132,132,236
K 211,102,103
A 133,133,236
A 134,134,236
A 135,135,236
X 132
X 133
X 134
K 218,132,133
X 135
)"
R"(s_nop 0
K 219,134,135
Mvav 188,152,144
Ra 152,242,30720
Zvav 32,128,192
Ra 156,242,31744
Zvav 64,128,200
Zvav 96,128,208
s_xor_b32 s62, s62, 0x10000
Zvav 128,128,216
v_add_u32 v23, 61440, v21
Zvav 132,132,216
v_add_u32 v24, 61440, v22
Zvav 100,132,208
v_add_u32 v25, 122880, v21
Zvav 68,132,200
v_add_u32 v26, 122880, v22
Zvav 36,132,192
Zvav 40,136,192
X 172
Zvav 72,136,200
X 173
Zvav 104,136,208
X 174
K 220,172,173
Zvav 136,136,216
X 175
Zvav 140,140,216
X 168
K 221,174,175
Zvav 108,140,208
X 169
X 170
Zvav 76,140,200
s_waitcnt vmcnt(0) lgkmcnt(0)
s_barrier
v_xor_b32 v242, 0x10000, v242
K 212,168,169
Zvav 44,140,192
X 171
X 164
Zvav 48,144,192
Ra 128,242,0
K 213,170,171
X 165
Zvav 80,144,200
X 166
Zvav 112,144,208
Ra 132,242,1024
K 204,164,165
X 167
Zvav 144,144,216
X 160
Zvav 148,148,216
K 205,166,167
X 161
Zvav 116,148,208
Ra 136,242,2048
X 162
Zvav 84,148,200
K 196,160,161
X 163
Zvav 52,148,192
X 176
K 197,162,163
Zvav 56,152,192
Ra 140,242,3072
X 177
Zvav 88,152,200
X 178
K 198,176,177
Zvav 120,152,208
X 179
X 180
Zvav 152,152,216
K 199,178,179
Zvav 156,156,216
X 181
X 182
Zvav 124,156,208
K 206,180,181
X 183
Zvav 92,156,200
K 207,182,183
Zvav 60,156,192
s_cmp_eq_u64 s[24:25], 0
s_cbranch_scc1 .Ll1_32%=
W 0
Cvaa 160,128,0,224
Ra 144,242,4096
Ra 148,242,5120
Mvaa 160,132,4
s_add_u32 m0, s62, 0
Ra 152,242,6144
Cvaa 164,132,20,228
X 184
Mvaa 164,128,16
X 185
Cvaa 168,128,32,232
X 186
Mvaa 168,132,36
Ra 156,242,7168
K 214,184,185
Cvaa 172,132,52,236
Dc 1,8,61,0
Mvaa 172,128,48
X 187
Mvaa 160,136,8
X 188
Mvaa 160,140,12
Ra 128,242,16384
K 215,186,187
Mvaa 164,140,28
Dc 1,8,61,1024
Mvaa 164,136,24
X 189
Mvaa 168,136,40
X 190
Mvaa 168,140,44
Ra 132,242,17408
K 222,188,189
Mvaa 172,140,60
Dc 1,8,61,2048
Mvaa 172,136,56
X 191
v_smfmac_f32_16x16x64_bf16 a[160:163], a[176:179], v[192:199], v240
Ra 136,242,18432
K 223,190,191
v_smfmac_f32_16x16x64_bf16 a[164:167], a[176:179], v[200:207], v240
X 160
v_smfmac_f32_16x16x64_bf16 a[168:171], a[176:179], v[208:215], v240
Dc 1,8,61,3072
v_smfmac_f32_16x16x64_bf16 a[172:175], a[176:179], v[216:223], v240
W 3
X 161
Cvaa 176,144,0,224
s_add_u32 m0, s62, 4096
X 162
Mvaa 176,148,4
Ra 140,242,19456
K 192,160,161
Cvaa 180,148,20,228
X 163
Mvaa 180,144,16
Dc 243,8,61,0
Cvaa 184,144,32,232
X 164
Mvaa 184,148,36
K 193,162,163
Cvaa 188,148,52,236
X 165
Mvaa 188,144,48
Dc 243,8,61,1024
Ra 144,242,20480
Mvaa 176,152,8
X 166
Mvaa 176,156,12
Ra 148,242,21504
K 200,164,165
Mvaa 180,156,28
X 167
Mvaa 180,152,24
Dc 243,8,61,2048
Mvaa 184,152,40
X 168
Mvaa 184,156,44
K 201,166,167
Mvaa 188,156,60
X 169
Mvaa 188,152,56
W 2
Dc 243,8,61,3072
Ra 152,242,22528
Mvav 32,128,196
s_add_u32 m0, s62, 8192
X 170
Mvav 64,128,204
Ra 156,242,23552
K 208,168,169
Mvav 96,128,212
X 171
Mvav 128,128,220
Dc 244,8,61,0
Mvav 132,132,220
X 172
Mvav 100,132,212
Ra 128,242,8192
K 209,170,171
Mvav 68,132,204
X 173
Mvav 36,132,196
Dc 244,8,61,1024
Mvav 40,136,196
X 174
Mvav 72,136,204
Ra 132,242,9216
K 216,172,173
Mvav 104,136,212
X 175
Mvav 136,136,220
Dc 244,8,61,2048
Mvav 140,140,220
X 176
Mvav 108,140,212
Ra 136,242,10240
K 217,174,175
Mvav 76,140,204
X 177
Mvav 44,140,196
W 3
Dc 244,8,61,3072
Mvav 48,144,196
X 178
Mvav 80,144,204
s_add_u32 m0, s62, 12288
Ra 140,242,11264
Mvav 112,144,212
K 194,176,177
Mvav 144,144,220
X 179
Mvav 148,148,220
X 180
Mvav 116,148,212
Dc 245,8,61,0
Ra 144,242,12288
Mvav 84,148,204
K 195,178,179
Mvav 52,148,196
X 181
Mvav 56,152,196
X 182
Mvav 88,152,204
Dc 245,8,61,1024
Ra 148,242,13312
Mvav 120,152,212
s_cmp_eq_u32 s14, 2
s_cselect_b64 s[18:19], s[30:31], s[32:33]
K 202,180,181
Mvav 152,152,220
s_lshr_b32 s81, s15, 3
X 183
Mvav 156,156,220
s_mul_i32 s82, s81, 69120
X 184
Mvav 124,156,212
s_mul_i32 s83, s59, 448
Dc 245,8,61,2048
Ra 152,242,14336
Mvav 92,156,204
s_andn2_b32 s83, s83, 127
K 203,182,183
Mvav 60,156,196
W 3
X 185
Cvaa 160,128,0,224
s_add_u32 s82, s82, s83
X 186
Mvaa 160,132,4
Dc 245,8,61,3072
Ra 156,242,15360
Cvaa 164,132,20,228
s_add_u32 s61, s61, s63
v_min_u32 v246, 624, v1
v_lshrrev_b32 v247, 6, v246
Mvaa 164,128,16
K 210,184,185
v_mul_u32_u24 v247, 3456, v247
Cvaa 168,128,32,232
X 187
Mvaa 168,132,36
v_and_b32 v246, 48, v246
v_lshl_add_u32 v246, v246, 3, v247
Cvaa 172,132,52,236
s_xor_b32 s62, s62, 0x10000
X 188
Mvaa 172,128,48
v_add_u32 v246, s82, v246
K 211,186,187
Mvaa 160,136,8
global_load_dword v3, v246, s[18:19]
Ra 128,242,24576
Mvaa 160,140,12
s_add_u32 s64, s64, 1
X 189
Mvaa 164,140,28
Ra 132,242,25600
v_add_u32 v246, 34560, v246
Mvaa 164,136,24
X 190
Mvaa 168,136,40
v_min_u32 v246, 2211836, v246
K 218,188,189
Mvaa 168,140,44
global_load_dword v3, v246, s[18:19]
Mvaa 172,140,60
s_add_u32 s36, s29, 131072
X 191
Mvaa 172,136,56
W 2
X 160
Cvaa 176,144,0,224
Ra 136,242,26624
K 219,190,191
Mvaa 176,148,4
X 161
Cvaa 180,148,20,228
Ra 140,242,27648
Mvaa 180,144,16
X 162
Cvaa 184,144,32,232
buffer_store_dwordx4 a[124:127], v1, s[8:11], s36 offen offset:0 sc1
K 196,160,161
Mvaa 184,148,36
X 163
Cvaa 188,148,52,236
X 164
Mvaa 188,144,48
Ra 144,242,28672
K 197,162,163
Mvaa 176,152,8
X 165
Mvaa 176,156,12
Ra 148,242,29696
Mvaa 180,156,28
X 166
Mvaa 180,152,24
buffer_store_dwordx4 a[116:119], v1, s[8:11], s36 offen offset:1024 sc1
K 204,164,165
Mvaa 184,152,40
X 167
Mvaa 184,156,44
X 168
Mvaa 188,156,60
Mvaa 188,152,56
W 2
Ra 152,242,30720
K 205,166,167
Mvav 32,128,192
X 169
Mvav 64,128,200
buffer_store_dwordx4 a[108:111], v1, s[8:11], s36 offen offset:2048 sc1
Ra 156,242,31744
Mvav 96,128,208
X 170
Mvav 128,128,216
K 212,168,169
Mvav 132,132,216
X 171
Mvav 100,132,208
X 172
Mvav 68,132,200
buffer_store_dwordx4 a[204:207], v1, s[8:11], s36 offen offset:3072 sc1
Mvav 36,132,192
K 213,170,171
Mvav 40,136,192
X 173
Mvav 72,136,200
X 174
Mvav 104,136,208
Mvav 136,136,216
s_add_u32 s36, s29, 135168
K 220,172,173
Mvav 140,140,216
X 175
Mvav 108,140,208
X 176
Mvav 76,140,200
s_waitcnt vmcnt(6) lgkmcnt(0)
s_barrier
v_xor_b32 v242, 0x10000, v242
Mvav 44,140,192
Ra 128,242,0
K 221,174,175
Mvav 48,144,192
X 177
Mvav 80,144,200
buffer_store_dwordx4 a[212:215], v1, s[8:11], s36 offen offset:0 sc1
Mvav 112,144,208
X 178
Mvav 144,144,216
Ra 132,242,1024
K 198,176,177
Mvav 148,148,216
X 179
Mvav 116,148,208
X 180
Mvav 84,148,200
Ra 136,242,2048
Mvav 52,148,192
buffer_store_dwordx4 a[220:223], v1, s[8:11], s36 offen offset:1024 sc1
K 199,178,179
Mvav 56,152,192
X 181
Mvav 88,152,200
Ra 140,242,3072
Mvav 120,152,208
X 182
Mvav 152,152,216
K 206,180,181
Mvav 156,156,216
)"
R"(X 183
Mvav 124,156,208
Mvav 92,156,200
K 207,182,183
Mvav 60,156,192
W 0
Cvaa 160,128,0,224
Ra 144,242,4096
Ra 148,242,5120
Mvaa 160,132,4
s_add_u32 m0, s62, 0
Ra 152,242,6144
Cvaa 164,132,20,228
X 184
Mvaa 164,128,16
X 185
Cvaa 168,128,32,232
X 186
Mvaa 168,132,36
Ra 156,242,7168
K 214,184,185
Cvaa 172,132,52,236
Dc 1,8,61,0
Mvaa 172,128,48
X 187
Mvaa 160,136,8
X 188
Mvaa 160,140,12
Ra 128,242,16384
K 215,186,187
Mvaa 164,140,28
Dc 1,8,61,1024
Mvaa 164,136,24
X 189
Mvaa 168,136,40
X 190
Mvaa 168,140,44
Ra 132,242,17408
K 222,188,189
Mvaa 172,140,60
Dc 1,8,61,2048
Mvaa 172,136,56
X 191
v_smfmac_f32_16x16x64_bf16 a[160:163], a[176:179], v[192:199], v240
Ra 136,242,18432
K 223,190,191
v_smfmac_f32_16x16x64_bf16 a[164:167], a[176:179], v[200:207], v240
X 160
v_smfmac_f32_16x16x64_bf16 a[168:171], a[176:179], v[208:215], v240
Dc 1,8,61,3072
v_smfmac_f32_16x16x64_bf16 a[172:175], a[176:179], v[216:223], v240
W 3
X 161
Cvaa 176,144,0,224
s_add_u32 m0, s62, 4096
X 162
Mvaa 176,148,4
Ra 140,242,19456
K 192,160,161
Cvaa 180,148,20,228
X 163
Mvaa 180,144,16
Dc 243,8,61,0
Cvaa 184,144,32,232
X 164
Mvaa 184,148,36
K 193,162,163
Cvaa 188,148,52,236
X 165
Mvaa 188,144,48
Dc 243,8,61,1024
Ra 144,242,20480
Mvaa 176,152,8
X 166
Mvaa 176,156,12
Ra 148,242,21504
K 200,164,165
Mvaa 180,156,28
X 167
Mvaa 180,152,24
Dc 243,8,61,2048
Mvaa 184,152,40
X 168
Mvaa 184,156,44
K 201,166,167
Mvaa 188,156,60
X 169
Mvaa 188,152,56
W 2
Dc 243,8,61,3072
Ra 152,242,22528
Mvav 32,128,196
s_add_u32 m0, s62, 8192
X 170
Mvav 64,128,204
Ra 156,242,23552
K 208,168,169
Mvav 96,128,212
X 171
Mvav 128,128,220
Dc 244,8,61,0
Mvav 132,132,220
X 172
Mvav 100,132,212
Ra 128,242,8192
K 209,170,171
Mvav 68,132,204
X 173
Mvav 36,132,196
Dc 244,8,61,1024
Mvav 40,136,196
X 174
Mvav 72,136,204
Ra 132,242,9216
K 216,172,173
Mvav 104,136,212
X 175
Mvav 136,136,220
Dc 244,8,61,2048
Mvav 140,140,220
X 176
Mvav 108,140,212
Ra 136,242,10240
K 217,174,175
Mvav 76,140,204
X 177
Mvav 44,140,196
W 3
Dc 244,8,61,3072
Mvav 48,144,196
X 178
Mvav 80,144,204
s_add_u32 m0, s62, 12288
Ra 140,242,11264
Mvav 112,144,212
K 194,176,177
Mvav 144,144,220
X 179
Mvav 148,148,220
X 180
Mvav 116,148,212
Dc 245,8,61,0
Ra 144,242,12288
Mvav 84,148,204
K 195,178,179
Mvav 52,148,196
X 181
Mvav 56,152,196
X 182
Mvav 88,152,204
Dc 245,8,61,1024
Ra 148,242,13312
Mvav 120,152,212
K 202,180,181
Mvav 152,152,220
X 183
Mvav 156,156,220
X 184
Mvav 124,156,212
Dc 245,8,61,2048
Ra 152,242,14336
Mvav 92,156,204
K 203,182,183
v_bfe_u32 v28, v1, 4, 4
Mvav 60,156,196
W 3
X 185
Cvaa 160,128,0,224
Ra 156,242,15360
v_lshlrev_b32 v28, 3, v28
Mvaa 160,132,4
s_lshl_b32 s80, s56, 9
s_add_u32 s80, s80, 4224
Dc 245,8,61,3072
Cvaa 164,132,20,228
s_add_u32 s61, s61, s63
X 186
Mvaa 164,128,16
K 210,184,185
Cvaa 168,128,32,232
X 187
Mvaa 168,132,36
buffer_load_dwordx2 v[28:29], v28, s[8:11], s80 offen sc1
Cvaa 172,132,52,236
s_xor_b32 s62, s62, 0x10000
X 188
Mvaa 172,128,48
Ra 128,242,24576
K 211,186,187
Mvaa 160,136,8
X 189
Mvaa 160,140,12
s_add_u32 s64, s64, 1
X 190
Mvaa 164,140,28
Ra 132,242,25600
K 218,188,189
Mvaa 164,136,24
X 191
Mvaa 168,136,40
Mvaa 168,140,44
X 160
Mvaa 172,140,60
buffer_store_dwordx4 a[228:231], v1, s[8:11], s36 offen offset:2048 sc1
K 219,190,191
Mvaa 172,136,56
W 2
X 161
Cvaa 176,144,0,224
Ra 136,242,26624
Ra 140,242,27648
Mvaa 176,148,4
X 162
Cvaa 180,148,20,228
K 196,160,161
Mvaa 180,144,16
X 163
Cvaa 184,144,32,232
Mvaa 184,148,36
X 164
Cvaa 188,148,52,236
buffer_store_dwordx4 a[76:79], v1, s[8:11], s36 offen offset:3072 sc1
K 197,162,163
Mvaa 188,144,48
X 165
Mvaa 176,152,8
Ra 144,242,28672
Ra 148,242,29696
Mvaa 176,156,12
X 166
Mvaa 180,156,28
K 204,164,165
Mvaa 180,152,24
X 167
Mvaa 184,152,40
s_add_u32 s36, s29, 327680
Mvaa 184,156,44
X 168
Mvaa 188,156,60
Mvaa 188,152,56
W 2
Ra 152,242,30720
K 205,166,167
Mvav 32,128,192
X 169
Mvav 64,128,200
Ra 156,242,31744
Mvav 96,128,208
X 170
Mvav 128,128,216
buffer_store_dwordx4 a[84:87], v1, s[8:11], s36 offen offset:0 sc1
K 212,168,169
Mvav 132,132,216
X 171
Mvav 100,132,208
X 172
Mvav 68,132,200
Mvav 36,132,192
K 213,170,171
Mvav 40,136,192
X 173
Mvav 72,136,200
buffer_store_dwordx4 a[92:95], v1, s[8:11], s36 offen offset:1024 sc1
Mvav 104,136,208
X 174
Mvav 136,136,216
s_waitcnt vmcnt(4)
v_cmp_eq_u64 vcc, s[12:13], v[28:29]
s_cmp_eq_u64 vcc, -1
s_cbranch_scc0 .Lspin_65%=
.Lok_66%=:
K 220,172,173
Mvav 140,140,216
X 175
Mvav 108,140,208
X 176
Mvav 76,140,200
W 0
s_barrier
v_xor_b32 v242, 0x10000, v242
Mvav 44,140,192
buffer_store_dwordx4 a[188:191], v1, s[8:11], s36 offen offset:2048 sc1
K 221,174,175
Mvav 48,144,192
Ra 128,242,0
Mvav 80,144,200
X 177
Mvav 112,144,208
X 178
Mvav 144,144,216
Ra 132,242,1024
K 198,176,177
Mvav 148,148,216
X 179
Mvav 116,148,208
X 180
Mvav 84,148,200
Ra 136,242,2048
buffer_store_dwordx4 a[196:199], v1, s[8:11], s36 offen offset:3072 sc1
Mvav 52,148,192
K 199,178,179
Mvav 56,152,192
X 181
Mvav 88,152,200
Ra 140,242,3072
Mvav 120,152,208
X 182
Mvav 152,152,216
K 206,180,181
Mvav 156,156,216
X 183
Mvav 124,156,208
Mvav 92,156,200
K 207,182,183
Mvav 60,156,192
W 0
Cvaa 160,128,0,224
Ra 144,242,4096
Ra 148,242,5120
Mvaa 160,132,4
s_add_u32 m0, s62, 0
Ra 152,242,6144
Cvaa 164,132,20,228
X 184
Mvaa 164,128,16
X 185
Cvaa 168,128,32,232
X 186
Mvaa 168,132,36
Ra 156,242,7168
K 214,184,185
Cvaa 172,132,52,236
Dc 1,8,61,0
Mvaa 172,128,48
X 187
Mvaa 160,136,8
X 188
Mvaa 160,140,12
Ra 128,242,16384
K 215,186,187
Mvaa 164,140,28
Dc 1,8,61,1024
Mvaa 164,136,24
X 189
Mvaa 168,136,40
X 190
Mvaa 168,140,44
Ra 132,242,17408
K 222,188,189
Mvaa 172,140,60
Dc 1,8,61,2048
Mvaa 172,136,56
X 191
v_smfmac_f32_16x16x64_bf16 a[160:163], a[176:179], v[192:199], v240
Ra 136,242,18432
K 223,190,191
v_smfmac_f32_16x16x64_bf16 a[164:167], a[176:179], v[200:207], v240
X 160
v_smfmac_f32_16x16x64_bf16 a[168:171], a[176:179], v[208:215], v240
Dc 1,8,61,3072
v_smfmac_f32_16x16x64_bf16 a[172:175], a[176:179], v[216:223], v240
W 3
X 161
Cvaa 176,144,0,224
s_add_u32 m0, s62, 4096
X 162
Mvaa 176,148,4
Ra 140,242,19456
K 192,160,161
Cvaa 180,148,20,228
X 163
Mvaa 180,144,16
Dc 243,8,61,0
Cvaa 184,144,32,232
X 164
Mvaa 184,148,36
K 193,162,163
Cvaa 188,148,52,236
X 165
Mvaa 188,144,48
Dc 243,8,61,1024
Ra 144,242,20480
Mvaa 176,152,8
X 166
Mvaa 176,156,12
Ra 148,242,21504
K 200,164,165
Mvaa 180,156,28
X 167
Mvaa 180,152,24
Dc 243,8,61,2048
Mvaa 184,152,40
X 168
Mvaa 184,156,44
K 201,166,167
Mvaa 188,156,60
X 169
Mvaa 188,152,56
W 2
Dc 243,8,61,3072
)"
R"(Ra 152,242,22528
Mvav 32,128,196
s_add_u32 m0, s62, 8192
X 170
Mvav 64,128,204
Ra 156,242,23552
K 208,168,169
Mvav 96,128,212
X 171
Mvav 128,128,220
Dc 244,8,61,0
Mvav 132,132,220
X 172
Mvav 100,132,212
Ra 128,242,8192
K 209,170,171
Mvav 68,132,204
X 173
Mvav 36,132,196
Dc 244,8,61,1024
Mvav 40,136,196
X 174
Mvav 72,136,204
Ra 132,242,9216
K 216,172,173
Mvav 104,136,212
X 175
Mvav 136,136,220
Dc 244,8,61,2048
Mvav 140,140,220
X 176
Mvav 108,140,212
Ra 136,242,10240
K 217,174,175
Mvav 76,140,204
X 177
Mvav 44,140,196
W 3
Dc 244,8,61,3072
Mvav 48,144,196
X 178
Mvav 80,144,204
s_add_u32 m0, s62, 12288
Ra 140,242,11264
Mvav 112,144,212
K 194,176,177
Mvav 144,144,220
X 179
Mvav 148,148,220
X 180
Mvav 116,148,212
Dc 245,8,61,0
Ra 144,242,12288
Mvav 84,148,204
K 195,178,179
Mvav 52,148,196
X 181
Mvav 56,152,196
X 182
Mvav 88,152,204
Dc 245,8,61,1024
Ra 148,242,13312
Mvav 120,152,212
K 202,180,181
Mvav 152,152,220
X 183
Mvav 156,156,220
X 184
Mvav 124,156,212
Dc 245,8,61,2048
Ra 152,242,14336
Mvav 92,156,204
s_mov_b64 s[18:19], s[34:35]
K 203,182,183
Mvav 60,156,196
W 3
X 185
Cvaa 160,128,0,224
s_lshr_b32 s81, s15, 3
X 186
Mvaa 160,132,4
s_lshl_b32 s82, s59, 5
Dc 245,8,61,3072
Ra 156,242,15360
Cvaa 164,132,20,228
s_add_u32 s81, s81, s82
s_add_u32 s61, s61, s63
K 210,184,185
Mvaa 164,128,16
s_lshl_b32 s81, s81, 1
X 187
Cvaa 168,128,32,232
s_add_u32 s81, s81, s14
X 188
Mvaa 168,132,36
s_sub_u32 s81, s81, 2
s_mul_i32 s81, s81, 4480
Cvaa 172,132,52,236
s_xor_b32 s62, s62, 0x10000
K 211,186,187
v_min_u32 v246, 544, v1
Mvaa 172,128,48
X 189
Mvaa 160,136,8
Ra 128,242,24576
v_lshl_add_u32 v246, v246, 3, s81
Mvaa 160,140,12
s_add_u32 s64, s64, 1
X 190
Mvaa 164,140,28
Ra 132,242,25600
v_min_u32 v246, 2211836, v246
Mvaa 164,136,24
global_load_dword v3, v246, s[18:19]
K 218,188,189
Mvaa 168,136,40
X 191
Mvaa 168,140,44
X 160
Mvaa 172,140,60
s_add_u32 s36, s29, 331776
K 219,190,191
Mvaa 172,136,56
W 2
X 161
Cvaa 176,144,0,224
Ra 136,242,26624
Ra 140,242,27648
Mvaa 176,148,4
X 162
Cvaa 180,148,20,228
K 196,160,161
Mvaa 180,144,16
X 163
Cvaa 184,144,32,232
buffer_store_dwordx4 a[236:239], v1, s[8:11], s36 offen offset:0 sc1
Mvaa 184,148,36
X 164
Cvaa 188,148,52,236
K 197,162,163
Mvaa 188,144,48
X 165
Mvaa 176,152,8
Ra 144,242,28672
Ra 148,242,29696
Mvaa 176,156,12
X 166
Mvaa 180,156,28
buffer_store_dwordx4 a[244:247], v1, s[8:11], s36 offen offset:1024 sc1
K 204,164,165
Mvaa 180,152,24
X 167
Mvaa 184,152,40
Mvaa 184,156,44
X 168
Mvaa 188,156,60
Mvaa 188,152,56
W 2
Ra 152,242,30720
K 205,166,167
Mvav 32,128,192
X 169
Mvav 64,128,200
buffer_store_dwordx4 a[68:71], v1, s[8:11], s36 offen offset:2048 sc1
Ra 156,242,31744
Mvav 96,128,208
X 170
Mvav 128,128,216
K 212,168,169
Mvav 132,132,216
X 171
Mvav 100,132,208
X 172
Mvav 68,132,200
buffer_store_dwordx4 a[184:187], v1, s[8:11], s36 offen offset:3072 sc1
Mvav 36,132,192
K 213,170,171
Mvav 40,136,192
X 173
Mvav 72,136,200
X 174
Mvav 104,136,208
Mvav 136,136,216
s_add_u32 s36, s29, 196608
K 220,172,173
Mvav 140,140,216
X 175
Mvav 108,140,208
X 176
Mvav 76,140,200
s_waitcnt vmcnt(5) lgkmcnt(0)
s_barrier
v_xor_b32 v242, 0x10000, v242
Mvav 44,140,192
Ra 128,242,0
K 221,174,175
Mvav 48,144,192
X 177
Mvav 80,144,200
buffer_store_dwordx4 a[192:195], v1, s[8:11], s36 offen offset:0 sc1
Mvav 112,144,208
X 178
Mvav 144,144,216
Ra 132,242,1024
K 198,176,177
Mvav 148,148,216
X 179
Mvav 116,148,208
X 180
Mvav 84,148,200
Ra 136,242,2048
Mvav 52,148,192
buffer_store_dwordx4 a[200:203], v1, s[8:11], s36 offen offset:1024 sc1
K 199,178,179
Mvav 56,152,192
X 181
Mvav 88,152,200
Ra 140,242,3072
Mvav 120,152,208
X 182
Mvav 152,152,216
K 206,180,181
Mvav 156,156,216
X 183
Mvav 124,156,208
Mvav 92,156,200
K 207,182,183
Mvav 60,156,192
W 0
Cvaa 160,128,0,224
Ra 144,242,4096
Ra 148,242,5120
Mvaa 160,132,4
s_add_u32 m0, s62, 0
Ra 152,242,6144
Cvaa 164,132,20,228
X 184
Mvaa 164,128,16
X 185
Cvaa 168,128,32,232
X 186
Mvaa 168,132,36
Ra 156,242,7168
K 214,184,185
Cvaa 172,132,52,236
Dc 1,8,61,0
Mvaa 172,128,48
X 187
Mvaa 160,136,8
X 188
Mvaa 160,140,12
Ra 128,242,16384
K 215,186,187
Mvaa 164,140,28
Dc 1,8,61,1024
Mvaa 164,136,24
X 189
Mvaa 168,136,40
X 190
Mvaa 168,140,44
Ra 132,242,17408
K 222,188,189
Mvaa 172,140,60
Dc 1,8,61,2048
Mvaa 172,136,56
X 191
v_smfmac_f32_16x16x64_bf16 a[160:163], a[176:179], v[192:199], v240
Ra 136,242,18432
K 223,190,191
v_smfmac_f32_16x16x64_bf16 a[164:167], a[176:179], v[200:207], v240
X 160
v_smfmac_f32_16x16x64_bf16 a[168:171], a[176:179], v[208:215], v240
Dc 1,8,61,3072
v_smfmac_f32_16x16x64_bf16 a[172:175], a[176:179], v[216:223], v240
W 3
X 161
Cvaa 176,144,0,224
s_add_u32 m0, s62, 4096
X 162
Mvaa 176,148,4
Ra 140,242,19456
K 192,160,161
Cvaa 180,148,20,228
X 163
Mvaa 180,144,16
Dc 243,8,61,0
Cvaa 184,144,32,232
X 164
Mvaa 184,148,36
K 193,162,163
Cvaa 188,148,52,236
X 165
Mvaa 188,144,48
Dc 243,8,61,1024
Ra 144,242,20480
Mvaa 176,152,8
X 166
Mvaa 176,156,12
Ra 148,242,21504
K 200,164,165
Mvaa 180,156,28
X 167
Mvaa 180,152,24
Dc 243,8,61,2048
Mvaa 184,152,40
X 168
Mvaa 184,156,44
K 201,166,167
Mvaa 188,156,60
X 169
Mvaa 188,152,56
W 2
Dc 243,8,61,3072
Ra 152,242,22528
Mvav 32,128,196
s_add_u32 m0, s62, 8192
X 170
Mvav 64,128,204
Ra 156,242,23552
K 208,168,169
Mvav 96,128,212
X 171
Mvav 128,128,220
Dc 244,8,61,0
Mvav 132,132,220
X 172
Mvav 100,132,212
Ra 128,242,8192
K 209,170,171
Mvav 68,132,204
X 173
Mvav 36,132,196
Dc 244,8,61,1024
Mvav 40,136,196
X 174
Mvav 72,136,204
Ra 132,242,9216
K 216,172,173
Mvav 104,136,212
X 175
Mvav 136,136,220
Dc 244,8,61,2048
Mvav 140,140,220
X 176
Mvav 108,140,212
Ra 136,242,10240
K 217,174,175
Mvav 76,140,204
X 177
Mvav 44,140,196
W 3
Dc 244,8,61,3072
Mvav 48,144,196
X 178
Mvav 80,144,204
s_add_u32 m0, s62, 12288
Ra 140,242,11264
Mvav 112,144,212
K 194,176,177
Mvav 144,144,220
X 179
Mvav 148,148,220
X 180
Mvav 116,148,212
Dc 245,8,61,0
Ra 144,242,12288
Mvav 84,148,204
K 195,178,179
Mvav 52,148,196
X 181
Mvav 56,152,196
X 182
Mvav 88,152,204
Dc 245,8,61,1024
Ra 148,242,13312
Mvav 120,152,212
K 202,180,181
Mvav 152,152,220
X 183
Mvav 156,156,220
s_waitcnt vmcnt(16)
s_add_u32 s80, s71, 256
v_mov_b64 v[28:29], s[12:13]
s_mov_b64 exec, 1
buffer_store_dwordx2 v[28:29], off, s[8:11], s80 sc1
s_mov_b64 exec, -1
X 184
Mvav 124,156,212
Ra 152,242,14336
Mvav 92,156,204
K 203,182,183
Mvav 60,156,196
W 3
X 185
Cvaa 160,128,0,224
Dc 245,8,61,2048
Ra 156,242,15360
Mvaa 160,132,4
X 186
Cvaa 164,132,20,228
)"
R"(K 210,184,185
Mvaa 164,128,16
X 187
Cvaa 168,128,32,232
Dc 245,8,61,3072
Mvaa 168,132,36
s_add_u32 s61, s61, s63
X 188
Cvaa 172,132,52,236
s_xor_b32 s62, s62, 0x10000
K 211,186,187
Mvaa 172,128,48
X 189
Mvaa 160,136,8
Ra 128,242,24576
Ra 132,242,25600
Mvaa 160,140,12
s_add_u32 s64, s64, 1
X 190
Mvaa 164,140,28
K 218,188,189
Mvaa 164,136,24
X 191
Mvaa 168,136,40
Mvaa 168,140,44
X 160
Mvaa 172,140,60
buffer_store_dwordx4 a[208:211], v1, s[8:11], s36 offen offset:2048 sc1
K 219,190,191
Mvaa 172,136,56
W 2
X 161
Cvaa 176,144,0,224
Ra 136,242,26624
Ra 140,242,27648
Mvaa 176,148,4
X 162
Cvaa 180,148,20,228
K 196,160,161
Mvaa 180,144,16
X 163
Cvaa 184,144,32,232
Mvaa 184,148,36
X 164
Cvaa 188,148,52,236
buffer_store_dwordx4 a[216:219], v1, s[8:11], s36 offen offset:3072 sc1
K 197,162,163
Mvaa 188,144,48
X 165
Mvaa 176,152,8
Ra 144,242,28672
Ra 148,242,29696
Mvaa 176,156,12
X 166
Mvaa 180,156,28
K 204,164,165
Mvaa 180,152,24
X 167
Mvaa 184,152,40
s_add_u32 s36, s29, 200704
Mvaa 184,156,44
X 168
Mvaa 188,156,60
Mvaa 188,152,56
W 2
Ra 152,242,30720
K 205,166,167
Mvav 32,128,192
X 169
Mvav 64,128,200
Ra 156,242,31744
Mvav 96,128,208
X 170
Mvav 128,128,216
buffer_store_dwordx4 a[224:227], v1, s[8:11], s36 offen offset:0 sc1
K 212,168,169
Mvav 132,132,216
X 171
Mvav 100,132,208
X 172
Mvav 68,132,200
Mvav 36,132,192
K 213,170,171
Mvav 40,136,192
X 173
Mvav 72,136,200
buffer_store_dwordx4 a[232:235], v1, s[8:11], s36 offen offset:1024 sc1
Mvav 104,136,208
X 174
Mvav 136,136,216
K 220,172,173
Mvav 140,140,216
X 175
Mvav 108,140,208
X 176
Mvav 76,140,200
s_waitcnt vmcnt(4) lgkmcnt(0)
s_barrier
v_xor_b32 v242, 0x10000, v242
Mvav 44,140,192
buffer_store_dwordx4 a[240:243], v1, s[8:11], s36 offen offset:2048 sc1
K 221,174,175
Mvav 48,144,192
Ra 128,242,0
Mvav 80,144,200
X 177
Mvav 112,144,208
X 178
Mvav 144,144,216
Ra 132,242,1024
K 198,176,177
Mvav 148,148,216
X 179
Mvav 116,148,208
X 180
Mvav 84,148,200
Ra 136,242,2048
buffer_store_dwordx4 a[100:103], v1, s[8:11], s36 offen offset:3072 sc1
Mvav 52,148,192
K 199,178,179
Mvav 56,152,192
X 181
Mvav 88,152,200
Ra 140,242,3072
Mvav 120,152,208
X 182
Mvav 152,152,216
K 206,180,181
Mvav 156,156,216
X 183
Mvav 124,156,208
Mvav 92,156,200
K 207,182,183
Mvav 60,156,192
W 0
Cvaa 160,128,0,224
Ra 144,242,4096
Ra 148,242,5120
Mvaa 160,132,4
s_add_u32 m0, s62, 0
Ra 152,242,6144
Cvaa 164,132,20,228
X 184
Mvaa 164,128,16
X 185
Cvaa 168,128,32,232
X 186
Mvaa 168,132,36
Ra 156,242,7168
K 214,184,185
Cvaa 172,132,52,236
Dc 1,8,61,0
Mvaa 172,128,48
X 187
Mvaa 160,136,8
X 188
Mvaa 160,140,12
Ra 128,242,16384
K 215,186,187
Mvaa 164,140,28
Dc 1,8,61,1024
Mvaa 164,136,24
X 189
Mvaa 168,136,40
X 190
Mvaa 168,140,44
Ra 132,242,17408
K 222,188,189
Mvaa 172,140,60
Dc 1,8,61,2048
Mvaa 172,136,56
X 191
v_smfmac_f32_16x16x64_bf16 a[160:163], a[176:179], v[192:199], v240
Ra 136,242,18432
K 223,190,191
v_smfmac_f32_16x16x64_bf16 a[164:167], a[176:179], v[200:207], v240
X 160
v_smfmac_f32_16x16x64_bf16 a[168:171], a[176:179], v[208:215], v240
Dc 1,8,61,3072
v_smfmac_f32_16x16x64_bf16 a[172:175], a[176:179], v[216:223], v240
W 3
X 161
Cvaa 176,144,0,224
s_add_u32 m0, s62, 4096
X 162
Mvaa 176,148,4
Ra 140,242,19456
K 192,160,161
Cvaa 180,148,20,228
X 163
Mvaa 180,144,16
Dc 243,8,61,0
Cvaa 184,144,32,232
X 164
Mvaa 184,148,36
K 193,162,163
Cvaa 188,148,52,236
X 165
Mvaa 188,144,48
Dc 243,8,61,1024
Ra 144,242,20480
Mvaa 176,152,8
X 166
Mvaa 176,156,12
Ra 148,242,21504
K 200,164,165
Mvaa 180,156,28
X 167
Mvaa 180,152,24
Dc 243,8,61,2048
Mvaa 184,152,40
X 168
Mvaa 184,156,44
K 201,166,167
Mvaa 188,156,60
X 169
Mvaa 188,152,56
W 2
Dc 243,8,61,3072
Ra 152,242,22528
Mvav 32,128,196
s_add_u32 m0, s62, 8192
X 170
Mvav 64,128,204
Ra 156,242,23552
K 208,168,169
Mvav 96,128,212
X 171
Mvav 128,128,220
Dc 244,8,61,0
Mvav 132,132,220
X 172
Mvav 100,132,212
Ra 128,242,8192
K 209,170,171
Mvav 68,132,204
X 173
Mvav 36,132,196
Dc 244,8,61,1024
Mvav 40,136,196
X 174
Mvav 72,136,204
Ra 132,242,9216
K 216,172,173
Mvav 104,136,212
X 175
Mvav 136,136,220
Dc 244,8,61,2048
Mvav 140,140,220
X 176
Mvav 108,140,212
Ra 136,242,10240
K 217,174,175
Mvav 76,140,204
X 177
Mvav 44,140,196
W 3
Dc 244,8,61,3072
Mvav 48,144,196
X 178
Mvav 80,144,204
s_add_u32 m0, s62, 12288
Ra 140,242,11264
Mvav 112,144,212
K 194,176,177
Mvav 144,144,220
X 179
Mvav 148,148,220
X 180
Mvav 116,148,212
Dc 245,8,61,0
Ra 144,242,12288
Mvav 84,148,204
K 195,178,179
Mvav 52,148,196
X 181
Mvav 56,152,196
X 182
Mvav 88,152,204
Dc 245,8,61,1024
Ra 148,242,13312
Mvav 120,152,212
K 202,180,181
Mvav 152,152,220
X 183
Mvav 156,156,220
X 184
Mvav 124,156,212
Dc 245,8,61,2048
Ra 152,242,14336
Mvav 92,156,204
K 203,182,183
Mvav 60,156,196
W 3
X 185
Cvaa 160,128,0,224
X 186
Mvaa 160,132,4
Dc 245,8,61,3072
Ra 156,242,15360
Cvaa 164,132,20,228
s_add_u32 s61, s61, s63
K 210,184,185
Mvaa 164,128,16
X 187
Cvaa 168,128,32,232
X 188
Mvaa 168,132,36
Cvaa 172,132,52,236
s_xor_b32 s62, s62, 0x10000
K 211,186,187
Mvaa 172,128,48
X 189
Mvaa 160,136,8
Ra 128,242,24576
Ra 132,242,25600
Mvaa 160,140,12
s_add_u32 s64, s64, 1
X 190
Mvaa 164,140,28
K 218,188,189
Mvaa 164,136,24
X 191
Mvaa 168,136,40
Mvaa 168,140,44
X 160
Mvaa 172,140,60
s_add_u32 s36, s29, 262144
K 219,190,191
Mvaa 172,136,56
W 2
X 161
Cvaa 176,144,0,224
Ra 136,242,26624
Ra 140,242,27648
Mvaa 176,148,4
X 162
Cvaa 180,148,20,228
K 196,160,161
Mvaa 180,144,16
X 163
Cvaa 184,144,32,232
buffer_store_dwordx4 a[64:67], v1, s[8:11], s36 offen offset:0 sc1
Mvaa 184,148,36
X 164
Cvaa 188,148,52,236
K 197,162,163
Mvaa 188,144,48
X 165
Mvaa 176,152,8
Ra 144,242,28672
Ra 148,242,29696
Mvaa 176,156,12
X 166
Mvaa 180,156,28
buffer_store_dwordx4 a[72:75], v1, s[8:11], s36 offen offset:1024 sc1
K 204,164,165
Mvaa 180,152,24
X 167
Mvaa 184,152,40
Mvaa 184,156,44
X 168
Mvaa 188,156,60
Mvaa 188,152,56
W 2
Ra 152,242,30720
K 205,166,167
Mvav 32,128,192
X 169
Mvav 64,128,200
buffer_store_dwordx4 a[80:83], v1, s[8:11], s36 offen offset:2048 sc1
Ra 156,242,31744
Mvav 96,128,208
X 170
Mvav 128,128,216
K 212,168,169
Mvav 132,132,216
X 171
Mvav 100,132,208
X 172
Mvav 68,132,200
buffer_store_dwordx4 a[88:91], v1, s[8:11], s36 offen offset:3072 sc1
Mvav 36,132,192
K 213,170,171
Mvav 40,136,192
X 173
Mvav 72,136,200
X 174
Mvav 104,136,208
Mvav 136,136,216
s_add_u32 s36, s29, 266240
K 220,172,173
Mvav 140,140,216
X 175
Mvav 108,140,208
X 176
Mvav 76,140,200
s_waitcnt vmcnt(4) lgkmcnt(0)
s_barrier
v_xor_b32 v242, 0x10000, v242
)"
R"(Mvav 44,140,192
Ra 128,242,0
K 221,174,175
Mvav 48,144,192
X 177
Mvav 80,144,200
buffer_store_dwordx4 a[96:99], v1, s[8:11], s36 offen offset:0 sc1
Mvav 112,144,208
X 178
Mvav 144,144,216
Ra 132,242,1024
K 198,176,177
Mvav 148,148,216
X 179
Mvav 116,148,208
X 180
Mvav 84,148,200
Ra 136,242,2048
Mvav 52,148,192
buffer_store_dwordx4 a[104:107], v1, s[8:11], s36 offen offset:1024 sc1
K 199,178,179
Mvav 56,152,192
X 181
Mvav 88,152,200
Ra 140,242,3072
Mvav 120,152,208
X 182
Mvav 152,152,216
K 206,180,181
Mvav 156,156,216
X 183
Mvav 124,156,208
Mvav 92,156,200
K 207,182,183
Mvav 60,156,192
W 0
Cvaa 160,128,0,224
Ra 144,242,4096
Ra 148,242,5120
Mvaa 160,132,4
s_add_u32 m0, s62, 0
Ra 152,242,6144
Cvaa 164,132,20,228
X 184
Mvaa 164,128,16
X 185
Cvaa 168,128,32,232
X 186
Mvaa 168,132,36
Ra 156,242,7168
K 214,184,185
Cvaa 172,132,52,236
Dc 1,8,61,0
Mvaa 172,128,48
X 187
Mvaa 160,136,8
X 188
Mvaa 160,140,12
Ra 128,242,16384
K 215,186,187
Mvaa 164,140,28
Dc 1,8,61,1024
Mvaa 164,136,24
X 189
Mvaa 168,136,40
X 190
Mvaa 168,140,44
Ra 132,242,17408
K 222,188,189
Mvaa 172,140,60
Dc 1,8,61,2048
Mvaa 172,136,56
X 191
v_smfmac_f32_16x16x64_bf16 a[160:163], a[176:179], v[192:199], v240
Ra 136,242,18432
K 223,190,191
v_smfmac_f32_16x16x64_bf16 a[164:167], a[176:179], v[200:207], v240
X 160
v_smfmac_f32_16x16x64_bf16 a[168:171], a[176:179], v[208:215], v240
Dc 1,8,61,3072
v_smfmac_f32_16x16x64_bf16 a[172:175], a[176:179], v[216:223], v240
W 3
X 161
Cvaa 176,144,0,224
s_add_u32 m0, s62, 4096
X 162
Mvaa 176,148,4
Ra 140,242,19456
K 192,160,161
Cvaa 180,148,20,228
X 163
Mvaa 180,144,16
Dc 243,8,61,0
Cvaa 184,144,32,232
X 164
Mvaa 184,148,36
K 193,162,163
Cvaa 188,148,52,236
X 165
Mvaa 188,144,48
Dc 243,8,61,1024
Ra 144,242,20480
Mvaa 176,152,8
X 166
Mvaa 176,156,12
Ra 148,242,21504
K 200,164,165
Mvaa 180,156,28
X 167
Mvaa 180,152,24
Dc 243,8,61,2048
Mvaa 184,152,40
X 168
Mvaa 184,156,44
K 201,166,167
Mvaa 188,156,60
X 169
Mvaa 188,152,56
W 2
Dc 243,8,61,3072
Ra 152,242,22528
Mvav 32,128,196
s_add_u32 m0, s62, 8192
X 170
Mvav 64,128,204
Ra 156,242,23552
K 208,168,169
Mvav 96,128,212
X 171
Mvav 128,128,220
Dc 244,8,61,0
Mvav 132,132,220
X 172
Mvav 100,132,212
Ra 128,242,8192
K 209,170,171
Mvav 68,132,204
X 173
Mvav 36,132,196
Dc 244,8,61,1024
Mvav 40,136,196
X 174
Mvav 72,136,204
Ra 132,242,9216
K 216,172,173
Mvav 104,136,212
X 175
Mvav 136,136,220
Dc 244,8,61,2048
Mvav 140,140,220
X 176
Mvav 108,140,212
Ra 136,242,10240
K 217,174,175
Mvav 76,140,204
X 177
Mvav 44,140,196
W 3
Dc 244,8,61,3072
Mvav 48,144,196
X 178
Mvav 80,144,204
s_add_u32 m0, s62, 12288
Ra 140,242,11264
Mvav 112,144,212
K 194,176,177
Mvav 144,144,220
X 179
Mvav 148,148,220
X 180
Mvav 116,148,212
Dc 245,8,61,0
Ra 144,242,12288
Mvav 84,148,204
K 195,178,179
Mvav 52,148,196
X 181
Mvav 56,152,196
X 182
Mvav 88,152,204
Dc 245,8,61,1024
Ra 148,242,13312
Mvav 120,152,212
K 202,180,181
Mvav 152,152,220
X 183
Mvav 156,156,220
X 184
Mvav 124,156,212
Dc 245,8,61,2048
Ra 152,242,14336
Mvav 92,156,204
K 203,182,183
v_bfe_u32 v28, v1, 4, 4
Mvav 60,156,196
W 3
X 185
Cvaa 160,128,0,224
Ra 156,242,15360
v_lshlrev_b32 v28, 3, v28
Mvaa 160,132,4
s_lshl_b32 s80, s56, 9
s_add_u32 s80, s80, 4352
Dc 245,8,61,3072
Cvaa 164,132,20,228
s_add_u32 s61, s61, s63
X 186
Mvaa 164,128,16
K 210,184,185
Cvaa 168,128,32,232
X 187
Mvaa 168,132,36
buffer_load_dwordx2 v[28:29], v28, s[8:11], s80 offen sc1
Cvaa 172,132,52,236
s_xor_b32 s62, s62, 0x10000
X 188
Mvaa 172,128,48
Ra 128,242,24576
K 211,186,187
Mvaa 160,136,8
X 189
Mvaa 160,140,12
s_add_u32 s64, s64, 1
X 190
Mvaa 164,140,28
Ra 132,242,25600
K 218,188,189
Mvaa 164,136,24
X 191
Mvaa 168,136,40
Mvaa 168,140,44
X 160
Mvaa 172,140,60
buffer_store_dwordx4 a[112:115], v1, s[8:11], s36 offen offset:2048 sc1
K 219,190,191
Mvaa 172,136,56
W 2
X 161
Cvaa 176,144,0,224
Ra 136,242,26624
Ra 140,242,27648
Mvaa 176,148,4
X 162
Cvaa 180,148,20,228
K 196,160,161
Mvaa 180,144,16
X 163
Cvaa 184,144,32,232
Mvaa 184,148,36
X 164
Cvaa 188,148,52,236
K 197,162,163
Mvaa 188,144,48
X 165
Mvaa 176,152,8
Ra 144,242,28672
Ra 148,242,29696
Mvaa 176,156,12
X 166
Mvaa 180,156,28
K 204,164,165
Mvaa 180,152,24
X 167
Mvaa 184,152,40
Mvaa 184,156,44
X 168
Mvaa 188,156,60
Mvaa 188,152,56
W 2
Ra 152,242,30720
K 205,166,167
Mvav 32,128,192
X 169
Mvav 64,128,200
Ra 156,242,31744
Mvav 96,128,208
X 170
Mvav 128,128,216
K 212,168,169
Mvav 132,132,216
X 171
Mvav 100,132,208
X 172
Mvav 68,132,200
buffer_store_dwordx4 a[120:123], v1, s[8:11], s36 offen offset:3072 sc1
Mvav 36,132,192
K 213,170,171
Mvav 40,136,192
X 173
Mvav 72,136,200
X 174
Mvav 104,136,208
Mvav 136,136,216
s_waitcnt vmcnt(2)
v_cmp_eq_u64 vcc, s[12:13], v[28:29]
s_cmp_eq_u64 vcc, -1
s_cbranch_scc0 .Lspin_68%=
.Lok_69%=:
K 220,172,173
Mvav 140,140,216
X 175
Mvav 108,140,208
X 176
Mvav 76,140,200
W 0
s_barrier
v_xor_b32 v242, 0x10000, v242
Mvav 44,140,192
Ra 128,242,0
K 221,174,175
Mvav 48,144,192
X 177
Mvav 80,144,200
X 178
Mvav 112,144,208
Ra 132,242,1024
Mvav 144,144,216
K 198,176,177
Mvav 148,148,216
X 179
Mvav 116,148,208
X 180
Mvav 84,148,200
Ra 136,242,2048
Mvav 52,148,192
K 199,178,179
Mvav 56,152,192
X 181
Mvav 88,152,200
Ra 140,242,3072
Mvav 120,152,208
X 182
Mvav 152,152,216
K 206,180,181
Mvav 156,156,216
X 183
Mvav 124,156,208
Mvav 92,156,200
K 207,182,183
Mvav 60,156,192
W 0
Cvaa 160,128,0,224
Ra 144,242,4096
Ra 148,242,5120
Mvaa 160,132,4
s_add_u32 m0, s62, 0
Ra 152,242,6144
Cvaa 164,132,20,228
X 184
Mvaa 164,128,16
X 185
Cvaa 168,128,32,232
X 186
Mvaa 168,132,36
Ra 156,242,7168
K 214,184,185
Cvaa 172,132,52,236
Dc 1,8,61,0
Mvaa 172,128,48
X 187
Mvaa 160,136,8
X 188
Mvaa 160,140,12
Ra 128,242,16384
K 215,186,187
Mvaa 164,140,28
Dc 1,8,61,1024
Mvaa 164,136,24
X 189
Mvaa 168,136,40
X 190
Mvaa 168,140,44
Ra 132,242,17408
K 222,188,189
Mvaa 172,140,60
Dc 1,8,61,2048
Mvaa 172,136,56
X 191
v_smfmac_f32_16x16x64_bf16 a[160:163], a[176:179], v[192:199], v240
Ra 136,242,18432
K 223,190,191
v_smfmac_f32_16x16x64_bf16 a[164:167], a[176:179], v[200:207], v240
X 160
v_smfmac_f32_16x16x64_bf16 a[168:171], a[176:179], v[208:215], v240
Dc 1,8,61,3072
v_smfmac_f32_16x16x64_bf16 a[172:175], a[176:179], v[216:223], v240
W 3
X 161
Cvaa 176,144,0,224
s_add_u32 m0, s62, 4096
X 162
Mvaa 176,148,4
Ra 140,242,19456
K 192,160,161
Cvaa 180,148,20,228
X 163
Mvaa 180,144,16
Dc 243,8,61,0
Cvaa 184,144,32,232
X 164
)"
R"(Mvaa 184,148,36
K 193,162,163
Cvaa 188,148,52,236
X 165
Mvaa 188,144,48
Dc 243,8,61,1024
Ra 144,242,20480
Mvaa 176,152,8
X 166
Mvaa 176,156,12
Ra 148,242,21504
K 200,164,165
Mvaa 180,156,28
X 167
Mvaa 180,152,24
Dc 243,8,61,2048
Mvaa 184,152,40
X 168
Mvaa 184,156,44
K 201,166,167
Mvaa 188,156,60
X 169
Mvaa 188,152,56
W 2
Dc 243,8,61,3072
Ra 152,242,22528
Mvav 32,128,196
s_add_u32 m0, s62, 8192
X 170
Mvav 64,128,204
Ra 156,242,23552
K 208,168,169
Mvav 96,128,212
X 171
Mvav 128,128,220
Dc 244,8,61,0
Mvav 132,132,220
X 172
Mvav 100,132,212
Ra 128,242,8192
K 209,170,171
Mvav 68,132,204
X 173
Mvav 36,132,196
Dc 244,8,61,1024
Mvav 40,136,196
X 174
Mvav 72,136,204
Ra 132,242,9216
K 216,172,173
Mvav 104,136,212
X 175
Mvav 136,136,220
Dc 244,8,61,2048
Mvav 140,140,220
X 176
Mvav 108,140,212
Ra 136,242,10240
K 217,174,175
Mvav 76,140,204
X 177
Mvav 44,140,196
W 3
Dc 244,8,61,3072
Mvav 48,144,196
X 178
Mvav 80,144,204
s_add_u32 m0, s62, 12288
Ra 140,242,11264
Mvav 112,144,212
K 194,176,177
Mvav 144,144,220
X 179
Mvav 148,148,220
X 180
Mvav 116,148,212
Dc 245,8,61,0
Ra 144,242,12288
Mvav 84,148,204
K 195,178,179
Mvav 52,148,196
X 181
Mvav 56,152,196
X 182
Mvav 88,152,204
Dc 245,8,61,1024
Ra 148,242,13312
Mvav 120,152,212
K 202,180,181
Mvav 152,152,220
s_add_u32 s81, s57, 2
X 183
Mvav 156,156,220
s_waitcnt vmcnt(14)
s_add_u32 s80, s71, 384
v_mov_b64 v[28:29], s[12:13]
s_mov_b64 exec, 1
buffer_store_dwordx2 v[28:29], off, s[8:11], s80 sc1
s_mov_b64 exec, -1
X 184
Mvav 124,156,212
s_bitcmp0_b32 s81, 2
s_cselect_b32 s22, -1, 0
s_and_b32 s81, s81, 3
Ra 152,242,14336
Mvav 92,156,204
s_lshl_b32 s82, s56, 2
K 203,182,183
Mvav 60,156,196
W 3
X 185
Cvaa 160,128,0,224
s_add_u32 s81, s81, s82
Dc 245,8,61,2048
Ra 156,242,15360
Mvaa 160,132,4
s_lshl_b32 s81, s81, 1
X 186
Cvaa 164,132,20,228
s_add_u32 s81, s81, s14
s_sub_u32 s81, s81, 2
K 210,184,185
Mvaa 164,128,16
s_mul_i32 s81, s81, 20480
X 187
Cvaa 168,128,32,232
Dc 245,8,61,3072
v_min_u32 v2, 624, v1
Mvaa 168,132,36
s_add_u32 s61, s61, s63
X 188
Cvaa 172,132,52,236
s_xor_b32 s62, s62, 0x10000
v_lshl_add_u32 v2, v2, 3, s81
K 211,186,187
Mvaa 172,128,48
Ra 128,242,24576
v_and_b32 v2, s22, v2
Mvaa 160,136,8
s_and_b32 s22, s22, 5120
X 189
Mvaa 160,140,12
s_add_u32 s64, s64, 1
global_load_dword v3, v2, s[26:27]
Ra 132,242,25600
Mvaa 164,140,28
X 190
Mvaa 164,136,24
v_add_u32 v2, s22, v2
K 218,188,189
Mvaa 168,136,40
X 191
Mvaa 168,140,44
X 160
Mvaa 172,140,60
K 219,190,191
Mvaa 172,136,56
W 2
X 161
Cvaa 176,144,0,224
Ra 136,242,26624
Ra 140,242,27648
Mvaa 176,148,4
X 162
Cvaa 180,148,20,228
K 196,160,161
Mvaa 180,144,16
X 163
Cvaa 184,144,32,232
Mvaa 184,148,36
X 164
Cvaa 188,148,52,236
K 197,162,163
Mvaa 188,144,48
X 165
Mvaa 176,152,8
Ra 144,242,28672
Ra 148,242,29696
Mvaa 176,156,12
X 166
Mvaa 180,156,28
K 204,164,165
Mvaa 180,152,24
X 167
Mvaa 184,152,40
Mvaa 184,156,44
X 168
Mvaa 188,156,60
Mvaa 188,152,56
W 2
Ra 152,242,30720
K 205,166,167
Mvav 32,128,192
X 169
Mvav 64,128,200
Ra 156,242,31744
Mvav 96,128,208
X 170
Mvav 128,128,216
K 212,168,169
Mvav 132,132,216
X 171
Mvav 100,132,208
X 172
Mvav 68,132,200
Mvav 36,132,192
K 213,170,171
Mvav 40,136,192
X 173
Mvav 72,136,200
X 174
Mvav 104,136,208
Mvav 136,136,216
K 220,172,173
Mvav 140,140,216
X 175
Mvav 108,140,208
X 176
Mvav 76,140,200
s_waitcnt vmcnt(1) lgkmcnt(0)
s_barrier
v_xor_b32 v242, 0x10000, v242
Mvav 44,140,192
Ra 128,242,0
K 221,174,175
Mvav 48,144,192
X 177
Mvav 80,144,200
X 178
Mvav 112,144,208
Ra 132,242,1024
Mvav 144,144,216
K 198,176,177
Mvav 148,148,216
X 179
Mvav 116,148,208
X 180
Mvav 84,148,200
Ra 136,242,2048
Mvav 52,148,192
K 199,178,179
Mvav 56,152,192
X 181
Mvav 88,152,200
Ra 140,242,3072
Mvav 120,152,208
X 182
Mvav 152,152,216
K 206,180,181
Mvav 156,156,216
X 183
Mvav 124,156,208
Mvav 92,156,200
K 207,182,183
Mvav 60,156,192
W 0
Cvaa 160,128,0,224
Ra 144,242,4096
Ra 148,242,5120
Mvaa 160,132,4
s_add_u32 m0, s62, 0
Ra 152,242,6144
Cvaa 164,132,20,228
X 184
Mvaa 164,128,16
X 185
Cvaa 168,128,32,232
X 186
Mvaa 168,132,36
Ra 156,242,7168
K 214,184,185
Cvaa 172,132,52,236
Dc 1,8,61,0
Mvaa 172,128,48
X 187
Mvaa 160,136,8
X 188
Mvaa 160,140,12
Ra 128,242,16384
K 215,186,187
Mvaa 164,140,28
Dc 1,8,61,1024
Mvaa 164,136,24
X 189
Mvaa 168,136,40
X 190
Mvaa 168,140,44
Ra 132,242,17408
K 222,188,189
Mvaa 172,140,60
Dc 1,8,61,2048
Mvaa 172,136,56
X 191
v_smfmac_f32_16x16x64_bf16 a[160:163], a[176:179], v[192:199], v240
Ra 136,242,18432
K 223,190,191
v_smfmac_f32_16x16x64_bf16 a[164:167], a[176:179], v[200:207], v240
X 160
v_smfmac_f32_16x16x64_bf16 a[168:171], a[176:179], v[208:215], v240
Dc 1,8,61,3072
v_smfmac_f32_16x16x64_bf16 a[172:175], a[176:179], v[216:223], v240
W 3
X 161
Cvaa 176,144,0,224
s_add_u32 m0, s62, 4096
X 162
Mvaa 176,148,4
Ra 140,242,19456
K 192,160,161
Cvaa 180,148,20,228
X 163
Mvaa 180,144,16
Dc 243,8,61,0
Cvaa 184,144,32,232
X 164
Mvaa 184,148,36
K 193,162,163
Cvaa 188,148,52,236
X 165
Mvaa 188,144,48
Dc 243,8,61,1024
Ra 144,242,20480
Mvaa 176,152,8
X 166
Mvaa 176,156,12
Ra 148,242,21504
K 200,164,165
Mvaa 180,156,28
X 167
Mvaa 180,152,24
Dc 243,8,61,2048
Mvaa 184,152,40
X 168
Mvaa 184,156,44
K 201,166,167
Mvaa 188,156,60
X 169
Mvaa 188,152,56
W 2
Dc 243,8,61,3072
Ra 152,242,22528
Mvav 32,128,196
s_add_u32 m0, s62, 8192
X 170
Mvav 64,128,204
Ra 156,242,23552
K 208,168,169
Mvav 96,128,212
X 171
Mvav 128,128,220
Dc 244,8,61,0
Mvav 132,132,220
X 172
Mvav 100,132,212
Ra 128,242,8192
K 209,170,171
Mvav 68,132,204
X 173
Mvav 36,132,196
Dc 244,8,61,1024
Mvav 40,136,196
X 174
Mvav 72,136,204
Ra 132,242,9216
K 216,172,173
Mvav 104,136,212
X 175
Mvav 136,136,220
Dc 244,8,61,2048
Mvav 140,140,220
X 176
Mvav 108,140,212
Ra 136,242,10240
K 217,174,175
Mvav 76,140,204
X 177
Mvav 44,140,196
W 3
Dc 244,8,61,3072
Mvav 48,144,196
X 178
Mvav 80,144,204
s_add_u32 m0, s62, 12288
Ra 140,242,11264
Mvav 112,144,212
K 194,176,177
Mvav 144,144,220
X 179
Mvav 148,148,220
X 180
Mvav 116,148,212
Dc 245,8,61,0
Ra 144,242,12288
Mvav 84,148,204
K 195,178,179
Mvav 52,148,196
X 181
Mvav 56,152,196
X 182
Mvav 88,152,204
)"
R"(Dc 245,8,61,1024
Ra 148,242,13312
Mvav 120,152,212
K 202,180,181
Mvav 152,152,220
X 183
Mvav 156,156,220
X 184
Mvav 124,156,212
Dc 245,8,61,2048
Ra 152,242,14336
Mvav 92,156,204
K 203,182,183
Mvav 60,156,196
W 3
X 185
Cvaa 160,128,0,224
X 186
Mvaa 160,132,4
Dc 245,8,61,3072
Ra 156,242,15360
Cvaa 164,132,20,228
s_add_u32 s61, s61, s63
K 210,184,185
Mvaa 164,128,16
X 187
Cvaa 168,128,32,232
X 188
Mvaa 168,132,36
Cvaa 172,132,52,236
s_xor_b32 s62, s62, 0x10000
global_load_dword v3, v2, s[26:27]
K 211,186,187
Mvaa 172,128,48
X 189
Mvaa 160,136,8
Ra 128,242,24576
Ra 132,242,25600
Mvaa 160,140,12
s_add_u32 s64, s64, 1
X 190
Mvaa 164,140,28
v_add_u32 v2, s22, v2
K 218,188,189
Mvaa 164,136,24
X 191
Mvaa 168,136,40
Mvaa 168,140,44
X 160
Mvaa 172,140,60
K 219,190,191
Mvaa 172,136,56
W 2
X 161
Cvaa 176,144,0,224
Ra 136,242,26624
Ra 140,242,27648
Mvaa 176,148,4
X 162
Cvaa 180,148,20,228
K 196,160,161
Mvaa 180,144,16
X 163
Cvaa 184,144,32,232
Mvaa 184,148,36
X 164
Cvaa 188,148,52,236
K 197,162,163
Mvaa 188,144,48
X 165
Mvaa 176,152,8
Ra 144,242,28672
Ra 148,242,29696
Mvaa 176,156,12
X 166
Mvaa 180,156,28
K 204,164,165
Mvaa 180,152,24
X 167
Mvaa 184,152,40
Mvaa 184,156,44
X 168
Mvaa 188,156,60
Mvaa 188,152,56
W 2
Ra 152,242,30720
K 205,166,167
Mvav 32,128,192
X 169
Mvav 64,128,200
Ra 156,242,31744
Mvav 96,128,208
X 170
Mvav 128,128,216
K 212,168,169
Mvav 132,132,216
X 171
Mvav 100,132,208
X 172
Mvav 68,132,200
Mvav 36,132,192
K 213,170,171
Mvav 40,136,192
X 173
Mvav 72,136,200
X 174
Mvav 104,136,208
Mvav 136,136,216
K 220,172,173
Mvav 140,140,216
X 175
Mvav 108,140,208
X 176
Mvav 76,140,200
s_waitcnt vmcnt(1) lgkmcnt(0)
s_barrier
v_xor_b32 v242, 0x10000, v242
Mvav 44,140,192
Ra 128,242,0
K 221,174,175
Mvav 48,144,192
X 177
Mvav 80,144,200
X 178
Mvav 112,144,208
Ra 132,242,1024
Mvav 144,144,216
K 198,176,177
Mvav 148,148,216
X 179
Mvav 116,148,208
X 180
Mvav 84,148,200
Ra 136,242,2048
Mvav 52,148,192
K 199,178,179
Mvav 56,152,192
X 181
Mvav 88,152,200
Ra 140,242,3072
Mvav 120,152,208
X 182
Mvav 152,152,216
K 206,180,181
Mvav 156,156,216
X 183
Mvav 124,156,208
Mvav 92,156,200
K 207,182,183
Mvav 60,156,192
W 0
Cvaa 160,128,0,224
Ra 144,242,4096
Ra 148,242,5120
Mvaa 160,132,4
s_add_u32 m0, s62, 0
Ra 152,242,6144
Cvaa 164,132,20,228
X 184
Mvaa 164,128,16
X 185
Cvaa 168,128,32,232
X 186
Mvaa 168,132,36
Ra 156,242,7168
K 214,184,185
Cvaa 172,132,52,236
Dc 1,8,61,0
Mvaa 172,128,48
X 187
Mvaa 160,136,8
X 188
Mvaa 160,140,12
Ra 128,242,16384
K 215,186,187
Mvaa 164,140,28
Dc 1,8,61,1024
Mvaa 164,136,24
X 189
Mvaa 168,136,40
X 190
Mvaa 168,140,44
Ra 132,242,17408
K 222,188,189
Mvaa 172,140,60
Dc 1,8,61,2048
Mvaa 172,136,56
X 191
v_smfmac_f32_16x16x64_bf16 a[160:163], a[176:179], v[192:199], v240
Ra 136,242,18432
K 223,190,191
v_smfmac_f32_16x16x64_bf16 a[164:167], a[176:179], v[200:207], v240
X 160
v_smfmac_f32_16x16x64_bf16 a[168:171], a[176:179], v[208:215], v240
Dc 1,8,61,3072
v_smfmac_f32_16x16x64_bf16 a[172:175], a[176:179], v[216:223], v240
W 3
X 161
Cvaa 176,144,0,224
s_add_u32 m0, s62, 4096
X 162
Mvaa 176,148,4
Ra 140,242,19456
K 192,160,161
Cvaa 180,148,20,228
X 163
Mvaa 180,144,16
Dc 243,8,61,0
Cvaa 184,144,32,232
X 164
Mvaa 184,148,36
K 193,162,163
Cvaa 188,148,52,236
X 165
Mvaa 188,144,48
Dc 243,8,61,1024
Ra 144,242,20480
Mvaa 176,152,8
X 166
Mvaa 176,156,12
Ra 148,242,21504
K 200,164,165
Mvaa 180,156,28
X 167
Mvaa 180,152,24
Dc 243,8,61,2048
Mvaa 184,152,40
X 168
Mvaa 184,156,44
K 201,166,167
Mvaa 188,156,60
X 169
Mvaa 188,152,56
W 2
Dc 243,8,61,3072
Ra 152,242,22528
Mvav 32,128,196
s_add_u32 m0, s62, 8192
X 170
Mvav 64,128,204
Ra 156,242,23552
K 208,168,169
Mvav 96,128,212
X 171
Mvav 128,128,220
Dc 244,8,61,0
Mvav 132,132,220
X 172
Mvav 100,132,212
Ra 128,242,8192
K 209,170,171
Mvav 68,132,204
X 173
Mvav 36,132,196
Dc 244,8,61,1024
Mvav 40,136,196
X 174
Mvav 72,136,204
Ra 132,242,9216
K 216,172,173
Mvav 104,136,212
X 175
Mvav 136,136,220
Dc 244,8,61,2048
Mvav 140,140,220
X 176
Mvav 108,140,212
Ra 136,242,10240
K 217,174,175
Mvav 76,140,204
X 177
Mvav 44,140,196
W 3
Dc 244,8,61,3072
Mvav 48,144,196
X 178
Mvav 80,144,204
s_add_u32 m0, s62, 12288
Ra 140,242,11264
Mvav 112,144,212
K 194,176,177
Mvav 144,144,220
X 179
Mvav 148,148,220
X 180
Mvav 116,148,212
Dc 245,8,61,0
Ra 144,242,12288
Mvav 84,148,204
K 195,178,179
Mvav 52,148,196
X 181
Mvav 56,152,196
X 182
Mvav 88,152,204
Dc 245,8,61,1024
Ra 148,242,13312
Mvav 120,152,212
K 202,180,181
Mvav 152,152,220
X 183
Mvav 156,156,220
X 184
Mvav 124,156,212
Dc 245,8,61,2048
Ra 152,242,14336
Mvav 92,156,204
K 203,182,183
Mvav 60,156,196
W 3
X 185
Cvaa 160,128,0,224
X 186
Mvaa 160,132,4
Dc 245,8,61,3072
Ra 156,242,15360
Cvaa 164,132,20,228
s_add_u32 s61, s61, s63
K 210,184,185
Mvaa 164,128,16
X 187
Cvaa 168,128,32,232
X 188
Mvaa 168,132,36
Cvaa 172,132,52,236
s_xor_b32 s62, s62, 0x10000
global_load_dword v3, v2, s[26:27]
K 211,186,187
Mvaa 172,128,48
Ra 128,242,24576
v_add_u32 v2, s22, v2
Mvaa 160,136,8
s_lshr_b32 s80, s15, 3
s_lshl_b32 s80, s80, 1
s_add_u32 s80, s80, s14
Mvaa 160,140,12
s_sub_u32 s80, s80, 2
s_lshl_b32 s80, s80, 12
v_min_u32 v27, 496, v1
Mvaa 164,140,28
s_add_u32 s64, s64, 1
X 189
Mvaa 164,136,24
Ra 132,242,25600
v_lshl_add_u32 v27, v27, 3, s80
Mvaa 168,136,40
X 190
Mvaa 168,140,44
v_min_u32 v27, .Lend%=-.Lbeg%=-4, v27
K 218,188,189
Mvaa 172,140,60
global_load_dword v3, v27, s[6:7]
Mvaa 172,136,56
W 2
X 191
Cvaa 176,144,0,224
X 160
Mvaa 176,148,4
Ra 136,242,26624
K 219,190,191
Cvaa 180,148,20,228
X 161
Mvaa 180,144,16
Ra 140,242,27648
Cvaa 184,144,32,232
X 162
Mvaa 184,148,36
K 196,160,161
Cvaa 188,148,52,236
X 163
Mvaa 188,144,48
X 164
Mvaa 176,152,8
Ra 144,242,28672
K 197,162,163
Mvaa 176,156,12
X 165
Mvaa 180,156,28
Ra 148,242,29696
Mvaa 180,152,24
X 166
Mvaa 184,152,40
K 204,164,165
Mvaa 184,156,44
X 167
Mvaa 188,156,60
X 168
Mvaa 188,152,56
W 2
Ra 152,242,30720
K 205,166,167
Mvav 32,128,192
X 169
Mvav 64,128,200
Ra 156,242,31744
Mvav 96,128,208
X 170
Mvav 128,128,216
K 212,168,169
Mvav 132,132,216
)"
R"(X 171
Mvav 100,132,208
X 172
Mvav 68,132,200
Mvav 36,132,192
K 213,170,171
Mvav 40,136,192
X 173
Mvav 72,136,200
X 174
Mvav 104,136,208
Mvav 136,136,216
K 220,172,173
Mvav 140,140,216
X 175
Mvav 108,140,208
X 176
Mvav 76,140,200
s_waitcnt vmcnt(2) lgkmcnt(0)
s_barrier
v_xor_b32 v242, 0x10000, v242
Mvav 44,140,192
Ra 128,242,0
K 221,174,175
Mvav 48,144,192
X 177
Mvav 80,144,200
X 178
Mvav 112,144,208
Ra 132,242,1024
Mvav 144,144,216
K 198,176,177
Mvav 148,148,216
X 179
Mvav 116,148,208
X 180
Mvav 84,148,200
Ra 136,242,2048
Mvav 52,148,192
K 199,178,179
Mvav 56,152,192
X 181
Mvav 88,152,200
Ra 140,242,3072
Mvav 120,152,208
X 182
Mvav 152,152,216
K 206,180,181
Mvav 156,156,216
X 183
Mvav 124,156,208
Mvav 92,156,200
K 207,182,183
Mvav 60,156,192
W 0
Cvaa 160,128,0,224
Ra 144,242,4096
Ra 148,242,5120
Mvaa 160,132,4
s_add_u32 m0, s62, 0
Ra 152,242,6144
Cvaa 164,132,20,228
X 184
Mvaa 164,128,16
X 185
Cvaa 168,128,32,232
X 186
Mvaa 168,132,36
Ra 156,242,7168
K 214,184,185
Cvaa 172,132,52,236
Dc 1,8,61,0
Mvaa 172,128,48
X 187
Mvaa 160,136,8
X 188
Mvaa 160,140,12
Ra 128,242,16384
K 215,186,187
Mvaa 164,140,28
Dc 1,8,61,1024
Mvaa 164,136,24
X 189
Mvaa 168,136,40
X 190
Mvaa 168,140,44
Ra 132,242,17408
K 222,188,189
Mvaa 172,140,60
Dc 1,8,61,2048
Mvaa 172,136,56
X 191
v_smfmac_f32_16x16x64_bf16 a[160:163], a[176:179], v[192:199], v240
Ra 136,242,18432
K 223,190,191
v_smfmac_f32_16x16x64_bf16 a[164:167], a[176:179], v[200:207], v240
X 160
v_smfmac_f32_16x16x64_bf16 a[168:171], a[176:179], v[208:215], v240
Dc 1,8,61,3072
v_smfmac_f32_16x16x64_bf16 a[172:175], a[176:179], v[216:223], v240
W 3
X 161
Cvaa 176,144,0,224
s_add_u32 m0, s62, 4096
X 162
Mvaa 176,148,4
Ra 140,242,19456
K 192,160,161
Cvaa 180,148,20,228
X 163
Mvaa 180,144,16
Dc 243,8,61,0
Cvaa 184,144,32,232
X 164
Mvaa 184,148,36
K 193,162,163
Cvaa 188,148,52,236
X 165
Mvaa 188,144,48
Dc 243,8,61,1024
Ra 144,242,20480
Mvaa 176,152,8
X 166
Mvaa 176,156,12
Ra 148,242,21504
K 200,164,165
Mvaa 180,156,28
X 167
Mvaa 180,152,24
Dc 243,8,61,2048
Mvaa 184,152,40
X 168
Mvaa 184,156,44
K 201,166,167
Mvaa 188,156,60
X 169
Mvaa 188,152,56
W 2
Dc 243,8,61,3072
Ra 152,242,22528
Mvav 32,128,196
s_add_u32 m0, s62, 8192
X 170
Mvav 64,128,204
Ra 156,242,23552
K 208,168,169
Mvav 96,128,212
X 171
Mvav 128,128,220
Dc 244,8,61,0
Mvav 132,132,220
X 172
Mvav 100,132,212
Ra 128,242,8192
K 209,170,171
Mvav 68,132,204
X 173
Mvav 36,132,196
Dc 244,8,61,1024
Mvav 40,136,196
X 174
Mvav 72,136,204
Ra 132,242,9216
K 216,172,173
Mvav 104,136,212
X 175
Mvav 136,136,220
Dc 244,8,61,2048
Mvav 140,140,220
X 176
Mvav 108,140,212
Ra 136,242,10240
K 217,174,175
Mvav 76,140,204
X 177
Mvav 44,140,196
W 3
Dc 244,8,61,3072
Mvav 48,144,196
X 178
Mvav 80,144,204
s_add_u32 m0, s62, 12288
Ra 140,242,11264
Mvav 112,144,212
K 194,176,177
Mvav 144,144,220
X 179
Mvav 148,148,220
X 180
Mvav 116,148,212
Dc 245,8,61,0
Ra 144,242,12288
Mvav 84,148,204
K 195,178,179
Mvav 52,148,196
X 181
Mvav 56,152,196
X 182
Mvav 88,152,204
Dc 245,8,61,1024
Ra 148,242,13312
Mvav 120,152,212
K 202,180,181
Mvav 152,152,220
X 183
Mvav 156,156,220
X 184
Mvav 124,156,212
Dc 245,8,61,2048
Ra 152,242,14336
Mvav 92,156,204
K 203,182,183
v_bfe_u32 v28, v1, 4, 4
Mvav 60,156,196
W 3
X 185
Cvaa 160,128,0,224
Ra 156,242,15360
v_lshlrev_b32 v28, 3, v28
Mvaa 160,132,4
s_lshl_b32 s80, s56, 9
s_add_u32 s80, s80, 4480
Dc 245,8,61,3072
Cvaa 164,132,20,228
s_add_u32 s61, s61, s63
X 186
Mvaa 164,128,16
K 210,184,185
Cvaa 168,128,32,232
X 187
Mvaa 168,132,36
buffer_load_dwordx2 v[28:29], v28, s[8:11], s80 offen sc1
Cvaa 172,132,52,236
s_xor_b32 s62, s62, 0x10000
X 188
Mvaa 172,128,48
Ra 128,242,24576
K 211,186,187
Mvaa 160,136,8
X 189
Mvaa 160,140,12
s_add_u32 s64, s64, 1
global_load_dword v3, v2, s[26:27]
Ra 132,242,25600
Mvaa 164,140,28
X 190
Mvaa 164,136,24
v_add_u32 v2, s22, v2
K 218,188,189
Mvaa 168,136,40
X 191
Mvaa 168,140,44
X 160
Mvaa 172,140,60
K 219,190,191
Mvaa 172,136,56
W 2
X 161
Cvaa 176,144,0,224
Ra 136,242,26624
Ra 140,242,27648
Mvaa 176,148,4
X 162
Cvaa 180,148,20,228
K 196,160,161
Mvaa 180,144,16
X 163
Cvaa 184,144,32,232
Mvaa 184,148,36
X 164
Cvaa 188,148,52,236
K 197,162,163
Mvaa 188,144,48
X 165
Mvaa 176,152,8
Ra 144,242,28672
Ra 148,242,29696
Mvaa 176,156,12
X 166
Mvaa 180,156,28
K 204,164,165
Mvaa 180,152,24
X 167
Mvaa 184,152,40
Mvaa 184,156,44
X 168
Mvaa 188,156,60
Mvaa 188,152,56
W 2
Ra 152,242,30720
K 205,166,167
Mvav 32,128,192
X 169
Mvav 64,128,200
Ra 156,242,31744
Mvav 96,128,208
X 170
Mvav 128,128,216
K 212,168,169
Mvav 132,132,216
X 171
Mvav 100,132,208
X 172
Mvav 68,132,200
Mvav 36,132,192
K 213,170,171
Mvav 40,136,192
X 173
Mvav 72,136,200
X 174
Mvav 104,136,208
Mvav 136,136,216
s_waitcnt vmcnt(1)
v_cmp_eq_u64 vcc, s[12:13], v[28:29]
s_cmp_eq_u64 vcc, -1
s_cbranch_scc0 .Lspin_71%=
.Lok_72%=:
K 220,172,173
Mvav 140,140,216
X 175
Mvav 108,140,208
X 176
Mvav 76,140,200
W 0
s_barrier
v_xor_b32 v242, 0x10000, v242
Mvav 44,140,192
Ra 128,242,0
K 221,174,175
Mvav 48,144,192
X 177
Mvav 80,144,200
X 178
Mvav 112,144,208
Ra 132,242,1024
Mvav 144,144,216
K 198,176,177
Mvav 148,148,216
X 179
Mvav 116,148,208
X 180
Mvav 84,148,200
Ra 136,242,2048
Mvav 52,148,192
K 199,178,179
Mvav 56,152,192
X 181
Mvav 88,152,200
Ra 140,242,3072
Mvav 120,152,208
X 182
Mvav 152,152,216
K 206,180,181
Mvav 156,156,216
X 183
Mvav 124,156,208
Mvav 92,156,200
K 207,182,183
Mvav 60,156,192
W 0
Cvaa 160,128,0,224
Ra 144,242,4096
Ra 148,242,5120
Mvaa 160,132,4
s_add_u32 m0, s62, 0
Ra 152,242,6144
Cvaa 164,132,20,228
X 184
Mvaa 164,128,16
X 185
Cvaa 168,128,32,232
X 186
Mvaa 168,132,36
Ra 156,242,7168
K 214,184,185
Cvaa 172,132,52,236
Dc 1,8,61,0
Mvaa 172,128,48
X 187
Mvaa 160,136,8
X 188
Mvaa 160,140,12
Ra 128,242,16384
K 215,186,187
Mvaa 164,140,28
Dc 1,8,61,1024
Mvaa 164,136,24
X 189
Mvaa 168,136,40
X 190
Mvaa 168,140,44
Ra 132,242,17408
K 222,188,189
Mvaa 172,140,60
Dc 1,8,61,2048
Mvaa 172,136,56
X 191
v_smfmac_f32_16x16x64_bf16 a[160:163], a[176:179], v[192:199], v240
Ra 136,242,18432
K 223,190,191
)"
R"(v_smfmac_f32_16x16x64_bf16 a[164:167], a[176:179], v[200:207], v240
X 160
v_smfmac_f32_16x16x64_bf16 a[168:171], a[176:179], v[208:215], v240
Dc 1,8,61,3072
v_smfmac_f32_16x16x64_bf16 a[172:175], a[176:179], v[216:223], v240
W 3
X 161
Cvaa 176,144,0,224
s_add_u32 m0, s62, 4096
X 162
Mvaa 176,148,4
s_sub_u32 s81, s57, 1
Ra 140,242,19456
K 192,160,161
Cvaa 180,148,20,228
X 163
Mvaa 180,144,16
s_lshl_b32 s85, s81, 5
Dc 243,8,61,0
Cvaa 184,144,32,232
X 164
Mvaa 184,148,36
s_add_u32 s85, s85, s56
K 193,162,163
Cvaa 188,148,52,236
X 165
Mvaa 188,144,48
Dc 243,8,61,1024
Ra 144,242,20480
Mvaa 176,152,8
s_lshl_b32 s85, s85, 1
X 166
Mvaa 176,156,12
Ra 148,242,21504
K 200,164,165
Mvaa 180,156,28
s_add_u32 s85, s85, s14
X 167
Mvaa 180,152,24
Dc 243,8,61,2048
Mvaa 184,152,40
s_sub_u32 s85, s85, 2
X 168
Mvaa 184,156,44
K 201,166,167
Mvaa 188,156,60
X 169
Mvaa 188,152,56
W 2
s_cmp_lt_u32 s85, 160
s_cselect_b64 s[20:21], 0, -1
Dc 243,8,61,3072
Mvav 32,128,196
s_add_u32 m0, s62, 8192
Ra 152,242,22528
Ra 156,242,23552
Mvav 64,128,204
s_mul_i32 s18, s85, 205
X 170
Mvav 96,128,212
K 208,168,169
Mvav 128,128,220
s_lshr_b32 s18, s18, 12
X 171
Mvav 132,132,220
Dc 244,8,61,0
Ra 128,242,8192
Mvav 100,132,212
X 172
Mvav 68,132,204
s_and_b32 s18, s18, 7
K 209,170,171
Mvav 36,132,196
X 173
Mvav 40,136,196
Dc 244,8,61,1024
v_lshrrev_b32 v4, 1, v1
Mvav 72,136,204
X 174
Mvav 104,136,212
Ra 132,242,9216
K 216,172,173
Mvav 136,136,220
X 175
Mvav 140,140,220
Dc 244,8,61,2048
v_min_u32 v5, 344, v4
Mvav 108,140,212
s_lshl_b32 s81, s18, 3
X 176
Mvav 76,140,204
Ra 136,242,10240
K 217,174,175
Mvav 44,140,196
W 3
X 177
Mvav 48,144,196
Dc 244,8,61,3072
v_mov_b32 v6, s81
Mvav 80,144,204
s_add_u32 m0, s62, 12288
X 178
Mvav 112,144,212
Ra 140,242,11264
K 194,176,177
Mvav 144,144,220
X 179
Mvav 148,148,220
buffer_load_dwordx2 v[8:9], v4, s[8:11], 0 offen offset:2560 sc1
Ra 144,242,12288
Mvav 116,148,212
X 180
Mvav 84,148,204
K 195,178,179
Mvav 52,148,196
X 181
Mvav 56,152,196
buffer_load_dwordx2 v[10:11], v5, s[8:11], 0 offen offset:3072 sc1
Ra 148,242,13312
Mvav 88,152,204
X 182
Mvav 120,152,212
K 202,180,181
Mvav 152,152,220
X 183
Mvav 156,156,220
Dc 245,8,61,0
Ra 152,242,14336
Mvav 124,156,212
X 184
Mvav 92,156,204
K 203,182,183
Mvav 60,156,196
W 3
X 185
Cvaa 160,128,0,224
buffer_load_dwordx2 v[12:13], v6, s[8:11], 0 offen offset:3584 sc1
Ra 156,242,15360
Mvaa 160,132,4
X 186
Cvaa 164,132,20,228
K 210,184,185
Mvaa 164,128,16
X 187
Cvaa 168,128,32,232
Dc 245,8,61,1024
Mvaa 168,132,36
X 188
Cvaa 172,132,52,236
K 211,186,187
Mvaa 172,128,48
X 189
Mvaa 160,136,8
Dc 245,8,61,2048
Ra 128,242,24576
Mvaa 160,140,12
X 190
Mvaa 164,140,28
Ra 132,242,25600
K 218,188,189
Mvaa 164,136,24
X 191
Mvaa 168,136,40
Dc 245,8,61,3072
Mvaa 168,140,44
s_add_u32 s61, s61, s63
s_xor_b32 s62, s62, 0x10000
s_add_u32 s64, s64, 1
Mvaa 172,140,60
X 160
Mvaa 172,136,56
W 2
Ra 136,242,26624
K 219,190,191
Cvaa 176,144,0,224
X 161
Mvaa 176,148,4
Ra 140,242,27648
Cvaa 180,148,20,228
X 162
Mvaa 180,144,16
K 196,160,161
Cvaa 184,144,32,232
X 163
Mvaa 184,148,36
X 164
Cvaa 188,148,52,236
K 197,162,163
Mvaa 188,144,48
X 165
Mvaa 176,152,8
Ra 144,242,28672
Ra 148,242,29696
Mvaa 176,156,12
X 166
Mvaa 180,156,28
K 204,164,165
Mvaa 180,152,24
X 167
Mvaa 184,152,40
Mvaa 184,156,44
X 168
Mvaa 188,156,60
Mvaa 188,152,56
W 2
Ra 152,242,30720
K 205,166,167
Mvav 32,128,192
X 169
Mvav 64,128,200
Ra 156,242,31744
Mvav 96,128,208
X 170
Mvav 128,128,216
K 212,168,169
Mvav 132,132,216
X 171
Mvav 100,132,208
X 172
Mvav 68,132,200
Mvav 36,132,192
K 213,170,171
Mvav 40,136,192
X 173
Mvav 72,136,200
X 174
Mvav 104,136,208
Mvav 136,136,216
s_waitcnt vmcnt(3)
v_cmp_eq_u64 vcc, s[12:13], v[8:9]
v_cmp_eq_u64_e64 s[86:87], s[12:13], v[10:11]
s_and_b64 vcc, vcc, s[86:87]
s_or_b64 vcc, vcc, s[20:21]
s_cmp_eq_u64 vcc, -1
s_cbranch_scc0 .Lspin_74%=
.Lok_75%=:
K 220,172,173
Mvav 140,140,216
X 175
Mvav 108,140,208
X 176
Mvav 76,140,200
s_waitcnt vmcnt(0) lgkmcnt(0)
s_barrier
v_xor_b32 v242, 0x10000, v242
Mvav 44,140,192
Ra 128,242,0
K 221,174,175
Mvav 48,144,192
X 177
Mvav 80,144,200
X 178
Mvav 112,144,208
Ra 132,242,1024
Mvav 144,144,216
K 198,176,177
Mvav 148,148,216
X 179
Mvav 116,148,208
X 180
Mvav 84,148,200
Ra 136,242,2048
Mvav 52,148,192
K 199,178,179
Mvav 56,152,192
X 181
Mvav 88,152,200
Ra 140,242,3072
Mvav 120,152,208
X 182
Mvav 152,152,216
K 206,180,181
Mvav 156,156,216
X 183
Mvav 124,156,208
Mvav 92,156,200
K 207,182,183
Mvav 60,156,192
W 0
Cvaa 160,128,0,224
Ra 144,242,4096
Ra 148,242,5120
Mvaa 160,132,4
s_add_u32 m0, s62, 0
Ra 152,242,6144
Cvaa 164,132,20,228
X 184
Mvaa 164,128,16
X 185
Cvaa 168,128,32,232
X 186
Mvaa 168,132,36
Ra 156,242,7168
K 214,184,185
Cvaa 172,132,52,236
Dc 1,8,61,0
Mvaa 172,128,48
X 187
Mvaa 160,136,8
X 188
Mvaa 160,140,12
Ra 128,242,16384
K 215,186,187
Mvaa 164,140,28
Dc 1,8,61,1024
Mvaa 164,136,24
X 189
Mvaa 168,136,40
X 190
Mvaa 168,140,44
Ra 132,242,17408
K 222,188,189
Mvaa 172,140,60
Dc 1,8,61,2048
Mvaa 172,136,56
X 191
v_smfmac_f32_16x16x64_bf16 a[160:163], a[176:179], v[192:199], v240
Ra 136,242,18432
K 223,190,191
v_smfmac_f32_16x16x64_bf16 a[164:167], a[176:179], v[200:207], v240
X 160
v_smfmac_f32_16x16x64_bf16 a[168:171], a[176:179], v[208:215], v240
Dc 1,8,61,3072
v_smfmac_f32_16x16x64_bf16 a[172:175], a[176:179], v[216:223], v240
W 3
X 161
Cvaa 176,144,0,224
s_add_u32 m0, s62, 4096
X 162
Mvaa 176,148,4
Ra 140,242,19456
K 192,160,161
Cvaa 180,148,20,228
X 163
Mvaa 180,144,16
Dc 243,8,61,0
Cvaa 184,144,32,232
X 164
Mvaa 184,148,36
K 193,162,163
Cvaa 188,148,52,236
X 165
Mvaa 188,144,48
Dc 243,8,61,1024
Ra 144,242,20480
Mvaa 176,152,8
X 166
Mvaa 176,156,12
Ra 148,242,21504
K 200,164,165
Mvaa 180,156,28
X 167
Mvaa 180,152,24
Dc 243,8,61,2048
Mvaa 184,152,40
X 168
Mvaa 184,156,44
K 201,166,167
Mvaa 188,156,60
X 169
Mvaa 188,152,56
W 2
Dc 243,8,61,3072
Ra 152,242,22528
Mvav 32,128,196
s_add_u32 m0, s62, 8192
X 170
Mvav 64,128,204
Ra 156,242,23552
K 208,168,169
Mvav 96,128,212
X 171
Mvav 128,128,220
Dc 244,8,61,0
Mvav 132,132,220
X 172
Mvav 100,132,212
Ra 128,242,8192
K 209,170,171
Mvav 68,132,204
X 173
Mvav 36,132,196
Dc 244,8,61,1024
Mvav 40,136,196
X 174
Mvav 72,136,204
Ra 132,242,9216
K 216,172,173
Mvav 104,136,212
X 175
Mvav 136,136,220
Dc 244,8,61,2048
Mvav 140,140,220
X 176
Mvav 108,140,212
Ra 136,242,10240
K 217,174,175
Mvav 76,140,204
X 177
Mvav 44,140,196
)"
R"(W 3
Dc 244,8,61,3072
Mvav 48,144,196
X 178
Mvav 80,144,204
s_add_u32 m0, s62, 12288
Ra 140,242,11264
Mvav 112,144,212
K 194,176,177
Mvav 144,144,220
X 179
Mvav 148,148,220
X 180
Mvav 116,148,212
Dc 245,8,61,0
Ra 144,242,12288
Mvav 84,148,204
K 195,178,179
Mvav 52,148,196
X 181
Mvav 56,152,196
X 182
Mvav 88,152,204
Dc 245,8,61,1024
Ra 148,242,13312
Mvav 120,152,212
v_xor_b32 v6, s12, v12
K 202,180,181
Mvav 152,152,220
X 183
Mvav 156,156,220
Ra 152,242,14336
v_and_b32 v6, 2047, v6
Mvav 124,156,212
Dc 245,8,61,2048
v_lshlrev_b32 v6, 3, v6
Mvav 92,156,204
X 184
Mvav 60,156,196
W 3
K 203,182,183
Cvaa 160,128,0,224
s_mov_b32 s81, 40960
X 185
Mvaa 160,132,4
Dc 245,8,61,3072
Ra 156,242,15360
Cvaa 164,132,20,228
s_add_u32 s61, s61, s63
X 186
Mvaa 164,128,16
K 210,184,185
Cvaa 168,128,32,232
X 187
Mvaa 168,132,36
buffer_load_dword v7, v6, s[8:11], s81 offen sc1
v_lshrrev_b32 v5, 7, v1
Cvaa 172,132,52,236
s_xor_b32 s62, s62, 0x10000
X 188
Mvaa 172,128,48
K 211,186,187
v_mul_u32_u24 v4, 20480, v5
Mvaa 160,136,8
X 189
Mvaa 160,140,12
s_add_u32 s64, s64, 1
Ra 128,242,24576
Ra 132,242,25600
Mvaa 164,140,28
X 190
Mvaa 164,136,24
v_and_b32 v6, 0x70, v1
K 218,188,189
Mvaa 168,136,40
X 191
Mvaa 168,140,44
v_add_u32 v4, v4, v6
Mvaa 172,140,60
X 160
Mvaa 172,136,56
W 2
v_add_u32 v5, 2, v5
K 219,190,191
Cvaa 176,144,0,224
X 161
Mvaa 176,148,4
Ra 136,242,26624
v_lshrrev_b32 v5, 3, v5
Cvaa 180,148,20,228
Ra 140,242,27648
v_lshl_or_b32 v5, v5, 31, v4
Mvaa 180,144,16
X 162
Cvaa 184,144,32,232
K 196,160,161
Mvaa 184,148,36
s_lshl_b32 s19, s85, 7
X 163
Cvaa 188,148,52,236
X 164
Mvaa 188,144,48
s_add_u32 s19, s19, 2752512
Ra 144,242,28672
K 197,162,163
Mvaa 176,152,8
X 165
Mvaa 176,156,12
s_and_b32 s81, s20, 134217728
Ra 148,242,29696
Mvaa 180,156,28
X 166
Mvaa 180,152,24
K 204,164,165
Mvaa 184,152,40
s_or_b32 s19, s19, s81
X 167
Mvaa 184,156,44
X 168
Mvaa 188,156,60
buffer_load_dwordx4 a[64:67], v4, s[8:11], s19 offen sc1
Mvaa 188,152,56
W 2
Ra 152,242,30720
K 205,166,167
Mvav 32,128,192
s_add_u32 s19, s19, 163840
X 169
Mvav 64,128,200
Ra 156,242,31744
Mvav 96,128,208
X 170
Mvav 128,128,216
buffer_load_dwordx4 a[68:71], v4, s[8:11], s19 offen sc1
K 212,168,169
Mvav 132,132,216
X 171
Mvav 100,132,208
s_add_u32 s19, s19, 163840
X 172
Mvav 68,132,200
Mvav 36,132,192
buffer_load_dwordx4 a[72:75], v4, s[8:11], s19 offen sc1
K 213,170,171
Mvav 40,136,192
X 173
Mvav 72,136,200
X 174
Mvav 104,136,208
s_add_u32 s19, s19, 163840
Mvav 136,136,216
K 220,172,173
Mvav 140,140,216
X 175
Mvav 108,140,208
buffer_load_dwordx4 a[76:79], v4, s[8:11], s19 offen sc1
Mvav 76,140,200
s_waitcnt vmcnt(5) lgkmcnt(0)
s_barrier
v_xor_b32 v242, 0x10000, v242
Mvav 44,140,192
s_add_u32 s19, s19, 163840
X 176
Mvav 48,144,192
Ra 128,242,0
K 221,174,175
Mvav 80,144,200
X 177
Mvav 112,144,208
buffer_load_dwordx4 a[80:83], v4, s[8:11], s19 offen sc1
Ra 132,242,1024
Mvav 144,144,216
s_add_u32 s19, s19, 163840
X 178
Mvav 148,148,216
K 198,176,177
Mvav 116,148,208
X 179
Mvav 84,148,200
buffer_load_dwordx4 a[84:87], v4, s[8:11], s19 offen sc1
Ra 136,242,2048
Mvav 52,148,192
X 180
Mvav 56,152,192
s_add_u32 s19, s19, 163840
Ra 140,242,3072
K 199,178,179
Mvav 88,152,200
X 181
Mvav 120,152,208
X 182
Mvav 152,152,216
buffer_load_dwordx4 a[88:91], v5, s[8:11], s19 offen sc1
K 206,180,181
Mvav 156,156,216
X 183
Mvav 124,156,208
Mvav 92,156,200
K 207,182,183
Mvav 60,156,192
W 0
Cvaa 160,128,0,224
Ra 144,242,4096
Ra 148,242,5120
Mvaa 160,132,4
s_add_u32 m0, s62, 0
Ra 152,242,6144
Cvaa 164,132,20,228
X 184
Mvaa 164,128,16
X 185
Cvaa 168,128,32,232
X 186
Mvaa 168,132,36
Ra 156,242,7168
K 214,184,185
Cvaa 172,132,52,236
Dc 1,8,61,0
Mvaa 172,128,48
X 187
Mvaa 160,136,8
X 188
Mvaa 160,140,12
Ra 128,242,16384
K 215,186,187
Mvaa 164,140,28
Dc 1,8,61,1024
Mvaa 164,136,24
X 189
Mvaa 168,136,40
X 190
Mvaa 168,140,44
Ra 132,242,17408
K 222,188,189
Mvaa 172,140,60
Dc 1,8,61,2048
Mvaa 172,136,56
X 191
v_smfmac_f32_16x16x64_bf16 a[160:163], a[176:179], v[192:199], v240
Ra 136,242,18432
K 223,190,191
v_smfmac_f32_16x16x64_bf16 a[164:167], a[176:179], v[200:207], v240
X 160
v_smfmac_f32_16x16x64_bf16 a[168:171], a[176:179], v[208:215], v240
Dc 1,8,61,3072
v_smfmac_f32_16x16x64_bf16 a[172:175], a[176:179], v[216:223], v240
W 3
X 161
Cvaa 176,144,0,224
s_add_u32 m0, s62, 4096
X 162
Mvaa 176,148,4
Ra 140,242,19456
K 192,160,161
Cvaa 180,148,20,228
X 163
Mvaa 180,144,16
Dc 243,8,61,0
Cvaa 184,144,32,232
X 164
Mvaa 184,148,36
K 193,162,163
Cvaa 188,148,52,236
X 165
Mvaa 188,144,48
Dc 243,8,61,1024
Ra 144,242,20480
Mvaa 176,152,8
X 166
Mvaa 176,156,12
Ra 148,242,21504
K 200,164,165
Mvaa 180,156,28
X 167
Mvaa 180,152,24
Dc 243,8,61,2048
Mvaa 184,152,40
X 168
Mvaa 184,156,44
K 201,166,167
Mvaa 188,156,60
X 169
Mvaa 188,152,56
W 2
Dc 243,8,61,3072
Ra 152,242,22528
Mvav 32,128,196
s_add_u32 m0, s62, 8192
X 170
Mvav 64,128,204
Ra 156,242,23552
K 208,168,169
Mvav 96,128,212
X 171
Mvav 128,128,220
Dc 244,8,61,0
Mvav 132,132,220
X 172
Mvav 100,132,212
Ra 128,242,8192
K 209,170,171
Mvav 68,132,204
X 173
Mvav 36,132,196
Dc 244,8,61,1024
Mvav 40,136,196
X 174
Mvav 72,136,204
Ra 132,242,9216
K 216,172,173
Mvav 104,136,212
X 175
Mvav 136,136,220
Dc 244,8,61,2048
Mvav 140,140,220
s_bfe_u32 s81, s59, 0x10001
X 176
Mvav 108,140,212
s_lshl_b32 s82, s57, 1
Ra 136,242,10240
K 217,174,175
Mvav 76,140,204
s_add_u32 s81, s81, s82
X 177
Mvav 44,140,196
W 3
s_lshl_b32 s83, s81, 2
Dc 244,8,61,3072
Mvav 48,144,196
s_lshr_b32 s81, s59, 2
X 178
Mvav 80,144,204
s_add_u32 m0, s62, 12288
s_lshl_b32 s81, s81, 1
Ra 140,242,11264
Mvav 112,144,212
s_add_u32 s83, s83, s81
K 194,176,177
Mvav 144,144,220
s_and_b32 s81, s59, 1
X 179
Mvav 148,148,220
s_add_u32 s83, s83, s81
X 180
Mvav 116,148,212
s_and_b32 s81, s14, 1
Dc 245,8,61,0
Ra 144,242,12288
Mvav 84,148,204
s_mul_i32 s81, s81, 124
K 195,178,179
Mvav 52,148,196
s_xor_b32 s83, s83, s81
X 181
Mvav 56,152,196
s_mul_i32 s83, s83, 20480
X 182
Mvav 88,152,204
s_mul_i32 s81, s58, 320
Dc 245,8,61,1024
Ra 148,242,13312
Mvav 120,152,212
s_add_u32 s82, s83, s81
K 202,180,181
Mvav 152,152,220
X 183
Mvav 156,156,220
Ra 152,242,14336
v_bfe_u32 v246, v1, 4, 4
Mvav 124,156,212
Dc 245,8,61,2048
v_mul_u32_u24 v246, 1280, v246
Mvav 92,156,204
X 184
Mvav 60,156,196
W 3
v_lshrrev_b32 v247, 8, v1
K 203,182,183
Cvaa 160,128,0,224
v_mul_u32_u24 v247, 104, v247
v_add3_u32 v246, v246, v247, s82
Mvaa 160,132,4
X 185
Cvaa 164,132,20,228
Dc 245,8,61,3072
Ra 156,242,15360
Mvaa 164,128,16
s_add_u32 s61, s61, s63
X 186
Cvaa 168,128,32,232
K 210,184,185
Mvaa 168,132,36
X 187
Cvaa 172,132,52,236
s_xor_b32 s62, s62, 0x10000
X 188
Mvaa 172,128,48
global_load_dword v3, v246, s[16:17]
K 211,186,187
Mvaa 160,136,8
)"
R"(s_lshl_b32 s81, s81, 3
s_add_u32 s83, s83, s81
s_add_u32 s83, s83, 131072
Mvaa 160,140,12
s_add_u32 s64, s64, 1
v_lshrrev_b32 v246, 9, v1
v_mul_u32_u24 v246, 10240, v246
Mvaa 164,140,28
Ra 128,242,24576
v_and_b32 v247, 0x1f0, v1
Mvaa 164,136,24
X 189
Mvaa 168,136,40
Ra 132,242,25600
v_min_u32 v247, 304, v247
Mvaa 168,140,44
X 190
Mvaa 172,140,60
v_lshl_add_u32 v246, v247, 3, v246
K 218,188,189
Mvaa 172,136,56
W 2
buffer_load_dword v3, v246, s[8:11], s83 offen sc1
Ra 136,242,26624
Cvaa 176,144,0,224
s_lshl_b32 s92, s58, 2
X 191
Mvaa 176,148,4
X 160
Cvaa 180,148,20,228
s_add_u32 s91, s92, s51
Ra 140,242,27648
K 219,190,191
Mvaa 180,144,16
X 161
Cvaa 184,144,32,232
X 162
Mvaa 184,148,36
s_add_u32 s95, s92, 0
K 196,160,161
Cvaa 188,148,52,236
X 163
Mvaa 188,144,48
X 164
Mvaa 176,152,8
s_and_b32 s95, s95, 15
Ra 144,242,28672
K 197,162,163
Mvaa 176,156,12
X 165
Mvaa 180,156,28
Ra 148,242,29696
Mvaa 180,152,24
s_mul_i32 s95, s95, 40960
X 166
Mvaa 184,152,40
K 204,164,165
Mvaa 184,156,44
X 167
Mvaa 188,156,60
s_mul_i32 s71, s51, 0
X 168
Mvaa 188,152,56
W 2
Ra 152,242,30720
K 205,166,167
Mvav 32,128,192
X 169
Mvav 64,128,200
s_add_u32 s71, s71, 133120
Ra 156,242,31744
Mvav 96,128,208
X 170
Mvav 128,128,216
K 212,168,169
Mvav 132,132,216
s_add_u32 m0, s71, s48
X 171
Mvav 100,132,208
X 172
Mvav 68,132,200
Mvav 36,132,192
s_lshl2_add_u32 s94, s48, s95
K 213,170,171
Mvav 40,136,192
X 173
Mvav 72,136,200
X 174
Mvav 104,136,208
D 16,40,94,0
Mvav 136,136,216
K 220,172,173
Mvav 140,140,216
X 175
Mvav 108,140,208
X 176
Mvav 76,140,200
s_waitcnt vmcnt(3) lgkmcnt(0)
s_barrier
v_xor_b32 v242, 0x10000, v242
Mvav 44,140,192
D 17,40,94,1024
K 221,174,175
Mvav 48,144,192
Ra 128,242,0
Mvav 80,144,200
X 177
Mvav 112,144,208
X 178
Mvav 144,144,216
D 18,40,94,2048
Ra 132,242,1024
Mvav 148,148,216
K 198,176,177
Mvav 116,148,208
X 179
Mvav 84,148,200
X 180
Mvav 52,148,192
D 19,40,94,3072
Ra 136,242,2048
Mvav 56,152,192
s_add_u32 m0, m0, 4096
Ra 140,242,3072
K 199,178,179
Mvav 88,152,200
X 181
Mvav 120,152,208
X 182
Mvav 152,152,216
buffer_load_dwordx4 v20, s[40:43], s94 offen lds
K 206,180,181
Mvav 156,156,216
X 183
Mvav 124,156,208
Mvav 92,156,200
K 207,182,183
Mvav 60,156,192
W 0
Cvaa 160,128,0,224
Ra 144,242,4096
Ra 148,242,5120
Mvaa 160,132,4
s_add_u32 m0, s62, 0
Ra 152,242,6144
G 8,64
Cvaa 164,132,20,228
X 184
Mvaa 164,128,16
X 185
Cvaa 168,128,32,232
G 9,65
G 10,66
Mvaa 168,132,36
X 186
Cvaa 172,132,52,236
Ra 156,242,7168
K 214,184,185
Mvaa 172,128,48
Dc 1,8,61,0
G 11,67
Mvaa 160,136,8
X 187
Mvaa 160,140,12
X 188
Mvaa 164,140,28
Ra 128,242,16384
G 12,68
Mvaa 164,136,24
Dc 1,8,61,1024
G 13,69
Mvaa 168,136,40
K 215,186,187
G 14,70
Mvaa 168,140,44
X 189
Mvaa 172,140,60
X 190
Mvaa 172,136,56
Dc 1,8,61,2048
G 15,71
v_smfmac_f32_16x16x64_bf16 a[160:163], a[176:179], v[192:199], v240
Ra 132,242,17408
K 222,188,189
v_smfmac_f32_16x16x64_bf16 a[164:167], a[176:179], v[200:207], v240
X 191
s_nop 0
K 223,190,191
v_smfmac_f32_16x16x64_bf16 a[168:171], a[176:179], v[208:215], v240
A 8,8,12
A 9,9,13
v_smfmac_f32_16x16x64_bf16 a[172:175], a[176:179], v[216:223], v240
W 2
Dc 1,8,61,3072
Ra 136,242,18432
Cvaa 176,144,0,224
s_add_u32 m0, s62, 4096
X 160
Mvaa 176,148,4
A 10,10,14
A 11,11,15
Cvaa 180,148,20,228
X 161
Mvaa 180,144,16
X 162
Cvaa 184,144,32,232
Ra 140,242,19456
G 12,72
Mvaa 184,148,36
K 192,160,161
G 13,73
Cvaa 188,148,52,236
X 163
Mvaa 188,144,48
Dc 243,8,61,0
G 14,74
Mvaa 176,152,8
X 164
Mvaa 176,156,12
G 15,75
K 193,162,163
Mvaa 180,156,28
X 165
Mvaa 180,152,24
Dc 243,8,61,1024
A 8,8,12
Mvaa 184,152,40
X 166
Mvaa 184,156,44
Ra 144,242,20480
A 9,9,13
Mvaa 188,156,60
Ra 148,242,21504
K 200,164,165
Mvaa 188,152,56
W 2
Dc 243,8,61,2048
A 10,10,14
Mvav 32,128,196
X 167
Mvav 64,128,204
A 11,11,15
G 12,76
Mvav 96,128,212
X 168
Mvav 128,128,220
Dc 243,8,61,3072
K 201,166,167
Mvav 132,132,220
s_add_u32 m0, s62, 8192
X 169
Mvav 100,132,212
Ra 152,242,22528
G 13,77
Mvav 68,132,204
X 170
Mvav 36,132,196
Ra 156,242,23552
G 14,78
Mvav 40,136,196
K 208,168,169
G 15,79
Mvav 72,136,204
X 171
Mvav 104,136,212
Dc 244,8,61,0
A 8,8,12
Mvav 136,136,220
X 172
Mvav 140,140,220
Ra 128,242,8192
A 9,9,13
Mvav 108,140,212
K 209,170,171
A 10,10,14
Mvav 76,140,204
X 173
Mvav 44,140,196
W 1
Dc 244,8,61,1024
A 11,11,15
Mvav 48,144,196
X 174
Mvav 80,144,204
Ra 132,242,9216
G 12,80
Mvav 112,144,212
K 216,172,173
G 13,81
Mvav 144,144,220
Dc 244,8,61,2048
Ra 136,242,10240
Mvav 148,148,220
X 175
Mvav 116,148,212
X 176
Mvav 84,148,204
G 14,82
K 217,174,175
Mvav 52,148,196
Dc 244,8,61,3072
G 15,83
Mvav 56,152,196
s_add_u32 m0, s62, 12288
X 177
Mvav 88,152,204
Ra 140,242,11264
A 8,8,12
Mvav 120,152,212
X 178
Mvav 152,152,220
A 9,9,13
K 194,176,177
Mvav 156,156,220
X 179
Mvav 124,156,212
Dc 245,8,61,0
A 10,10,14
Mvav 92,156,204
X 180
Mvav 60,156,196
W 0
Ra 144,242,12288
A 11,11,15
Cvaa 160,128,0,224
K 195,178,179
G 12,84
Mvaa 160,132,4
X 181
Cvaa 164,132,20,228
Dc 245,8,61,1024
G 13,85
Mvaa 164,128,16
X 182
Cvaa 168,128,32,232
Ra 148,242,13312
G 14,86
Mvaa 168,132,36
K 202,180,181
G 15,87
Cvaa 172,132,52,236
X 183
Mvaa 172,128,48
Dc 245,8,61,2048
Ra 152,242,14336
Mvaa 160,136,8
X 184
Mvaa 160,140,12
A 8,8,12
K 203,182,183
Mvaa 164,140,28
Ra 156,242,15360
A 9,9,13
Mvaa 164,136,24
X 185
Mvaa 168,136,40
Dc 245,8,61,3072
A 10,10,14
Mvaa 168,140,44
s_add_u32 s61, s61, s63
s_xor_b32 s62, s62, 0x10000
s_add_u32 s64, s64, 1
Mvaa 172,140,60
s_lshl_b32 s92, s58, 2
X 186
Mvaa 172,136,56
W 0
A 11,11,15
K 210,184,185
Cvaa 176,144,0,224
X 187
Mvaa 176,148,4
G 12,88
G 13,89
Cvaa 180,148,20,228
s_add_u32 s91, s92, s51
X 188
Mvaa 180,144,16
K 211,186,187
G 14,90
Cvaa 184,144,32,232
X 189
Mvaa 184,148,36
X 190
K 218,188,189
X 191
X 160
K 219,190,191
Cvaa 188,148,52,236
s_add_u32 s95, s92, 1
Ra 128,242,24576
G 15,91
Mvaa 188,144,48
Ra 132,242,25600
A 8,8,12
Mvaa 176,152,8
s_and_b32 s95, s95, 15
A 9,9,13
A 10,10,14
Mvaa 176,156,12
)"
R"(Ra 136,242,26624
A 11,11,15
Mvaa 180,156,28
Ra 140,242,27648
v_add_f32_dpp v8, v8, v8 row_ror:8 row_mask:0xf bank_mask:0xf
Mvaa 180,152,24
s_mul_i32 s95, s95, 40960
X 161
Mvaa 184,152,40
X 162
Mvaa 184,156,44
v_add_f32_dpp v9, v9, v9 row_ror:8 row_mask:0xf bank_mask:0xf
K 196,160,161
Mvaa 188,156,60
s_mul_i32 s71, s51, 0
v_add_f32_dpp v10, v10, v10 row_ror:8 row_mask:0xf bank_mask:0xf
v_add_f32_dpp v11, v11, v11 row_ror:8 row_mask:0xf bank_mask:0xf
Mvaa 188,152,56
W 0
X 163
Mvav 32,128,192
X 164
Mvav 64,128,200
s_add_u32 s71, s71, 143360
v_mov_b32 v12, v8
K 197,162,163
Mvav 96,128,208
X 165
Mvav 128,128,216
Ra 144,242,28672
v_mov_b32 v13, v9
Mvav 132,132,216
s_add_u32 m0, s71, s48
Ra 148,242,29696
v_mov_b32 v14, v10
Mvav 100,132,208
X 166
Mvav 68,132,200
K 204,164,165
v_mov_b32 v15, v11
Mvav 36,132,192
s_lshl2_add_u32 s94, s48, s95
X 167
Mvav 40,136,192
v_permlane16_swap_b32 v8, v12
v_permlane16_swap_b32 v9, v13
Mvav 72,136,200
X 168
Mvav 104,136,208
Ra 152,242,30720
Ra 156,242,31744
Mvav 136,136,216
K 205,166,167
v_permlane16_swap_b32 v10, v14
Mvav 140,140,216
X 169
Mvav 108,140,208
X 170
Mvav 76,140,200
s_waitcnt vmcnt(0) lgkmcnt(0)
s_barrier
v_xor_b32 v242, 0x10000, v242
Mvav 44,140,192
v_permlane16_swap_b32 v11, v15
K 212,168,169
Mvav 48,144,192
A 8,8,12
A 9,9,13
Mvav 80,144,200
X 171
Mvav 112,144,208
X 172
Mvav 144,144,216
A 10,10,14
K 213,170,171
Mvav 148,148,216
X 173
Mvav 116,148,208
A 11,11,15
v_mov_b32 v12, v8
Mvav 84,148,200
X 174
Mvav 52,148,192
D 16,40,94,0
K 220,172,173
Mvav 56,152,192
v_mov_b32 v13, v9
v_mov_b32 v14, v10
Mvav 88,152,200
X 175
Mvav 120,152,208
X 176
Mvav 152,152,216
D 17,40,94,1024
v_mov_b32 v15, v11
Mvav 156,156,216
Ra 128,242,0
K 221,174,175
Mvav 124,156,208
v_permlane32_swap_b32 v8, v12
v_permlane32_swap_b32 v9, v13
Mvav 92,156,200
X 177
Mvav 60,156,192
D 18,40,94,2048
Ra 132,242,1024
D 19,40,94,3072
s_add_u32 m0, m0, 4096
Ra 136,242,2048
Ra 140,242,3072
buffer_load_dwordx4 v20, s[40:43], s94 offen lds
X 178
v_permlane32_swap_b32 v10, v14
K 198,176,177
X 179
v_permlane32_swap_b32 v11, v15
X 180
A 8,8,12
K 199,178,179
A 9,9,13
X 181
A 10,10,14
X 182
A 11,11,15
K 206,180,181
X 183
s_nop 0
K 207,182,183
W 0
Cvaa 160,128,0,224
Ra 144,242,4096
Ra 148,242,5120
Mvaa 160,132,4
s_add_u32 m0, s62, 0
Ra 152,242,6144
v_and_b32 v7, 0xffff0000, v7
Cvaa 164,132,20,228
X 184
Mvaa 164,128,16
X 185
Cvaa 168,128,32,232
Ra 156,242,7168
v_ashrrev_i32 v6, 31, v7
Mvaa 168,132,36
X 186
Cvaa 172,132,52,236
K 214,184,185
v_not_b32 v6, v6
Mvaa 172,128,48
Dc 1,8,61,0
v_or_b32 v6, 0x80000000, v6
Mvaa 160,136,8
X 187
Mvaa 160,140,12
X 188
Mvaa 164,140,28
Ra 128,242,16384
v_xor_b32 v7, v7, v6
Mvaa 164,136,24
Dc 1,8,61,1024
K 215,186,187
Mvaa 168,136,40
X 189
Mvaa 168,140,44
Ra 132,242,17408
v_and_b32 v7, 0xffff0000, v7
Mvaa 172,140,60
X 190
Mvaa 172,136,56
Dc 1,8,61,2048
v_mul_f32 v7, 0xbfb8aa3b, v7
v_smfmac_f32_16x16x64_bf16 a[160:163], a[176:179], v[192:199], v240
Ra 136,242,18432
K 222,188,189
v_smfmac_f32_16x16x64_bf16 a[164:167], a[176:179], v[200:207], v240
X 191
s_nop 0
K 223,190,191
v_smfmac_f32_16x16x64_bf16 a[168:171], a[176:179], v[208:215], v240
X 7
v_smfmac_f32_16x16x64_bf16 a[172:175], a[176:179], v[216:223], v240
W 3
Dc 1,8,61,3072
v_add_f32 v7, 1.0, v7
Cvaa 176,144,0,224
s_add_u32 m0, s62, 4096
X 160
Mvaa 176,148,4
X 161
Cvaa 180,148,20,228
X 162
Mvaa 180,144,16
Ra 140,242,19456
K 192,160,161
Cvaa 184,144,32,232
v_rcp_f32 v7, v7
Mvaa 184,148,36
X 163
Cvaa 188,148,52,236
Dc 243,8,61,0
K 7,7,7
Mvaa 188,144,48
X 164
Mvaa 176,152,8
K 193,162,163
v_lshlrev_b32 v7, 16, v7
Mvaa 176,156,12
X 165
Mvaa 180,156,28
Dc 243,8,61,1024
Ra 144,242,20480
Mvaa 180,152,24
X 166
Mvaa 184,152,40
Ra 148,242,21504
K 12,8,9
Mvaa 184,156,44
K 200,164,165
K 13,10,11
Mvaa 188,156,60
Dc 243,8,61,2048
v_lshlrev_b32 v8, 16, v12
Mvaa 188,152,56
W 2
X 167
Mvav 32,128,196
X 168
Mvav 64,128,204
K 201,166,167
v_and_b32 v9, 0xffff0000, v12
Mvav 96,128,212
Dc 243,8,61,3072
Ra 152,242,22528
Mvav 128,128,220
s_add_u32 m0, s62, 8192
X 169
Mvav 132,132,220
X 170
Mvav 100,132,212
Ra 156,242,23552
v_lshlrev_b32 v10, 16, v13
Mvav 68,132,204
K 208,168,169
v_and_b32 v11, 0xffff0000, v13
Mvav 36,132,196
X 171
Mvav 40,136,196
Dc 244,8,61,0
Ra 128,242,8192
Mvav 72,136,204
X 172
Mvav 104,136,212
U 8,8,7
K 209,170,171
Mvav 136,136,220
X 173
Mvav 140,140,220
Dc 244,8,61,1024
U 9,9,7
Mvav 108,140,212
X 174
Mvav 76,140,204
Ra 132,242,9216
U 10,10,7
Mvav 44,140,196
W 2
K 216,172,173
U 11,11,7
Mvav 48,144,196
Dc 244,8,61,2048
Ra 136,242,10240
Mvav 80,144,204
X 175
Mvav 112,144,212
X 176
Mvav 144,144,220
K 12,8,9
K 217,174,175
Mvav 148,148,220
Dc 244,8,61,3072
K 14,10,11
Mvav 116,148,212
s_add_u32 m0, s62, 12288
X 177
Mvav 84,148,204
X 178
Mvav 52,148,196
Ra 140,242,11264
v_mov_b32 v13, s13
Mvav 56,152,196
K 194,176,177
v_mov_b32 v15, s13
Mvav 88,152,204
X 179
Mvav 120,152,212
X 180
Mvav 152,152,220
Dc 245,8,61,0
Ra 144,242,12288
Mvav 156,156,220
v_lshrrev_b32 v6, 7, v1
K 195,178,179
Mvav 124,156,212
X 181
Mvav 92,156,204
Ra 148,242,13312
v_lshl_or_b32 v6, v6, 28, v1
Mvav 60,156,196
W 2
s_lshl_b32 s19, s85, 7
Dc 245,8,61,1024
Cvaa 160,128,0,224
s_add_u32 s19, s19, 98304
X 182
Mvaa 160,132,4
s_and_b32 s81, s20, 134217728
Ra 152,242,14336
K 202,180,181
Cvaa 164,132,20,228
s_or_b32 s19, s19, s81
X 183
Mvaa 164,128,16
Dc 245,8,61,2048
Ra 156,242,15360
Cvaa 168,128,32,232
X 184
Mvaa 168,132,36
K 203,182,183
Cvaa 172,132,52,236
X 185
Mvaa 172,128,48
Dc 245,8,61,3072
Ra 128,242,24576
Mvaa 160,136,8
s_add_u32 s61, s61, s63
X 186
Mvaa 160,140,12
s_xor_b32 s62, s62, 0x10000
Ra 132,242,25600
K 210,184,185
Mvaa 164,140,28
s_add_u32 s64, s64, 1
buffer_store_dwordx4 v[12:15], v6, s[8:11], s19 offen sc1
Mvaa 164,136,24
X 187
Mvaa 168,136,40
X 188
Mvaa 168,140,44
K 211,186,187
Mvaa 172,140,60
s_lshl_b32 s92, s58, 2
X 189
Mvaa 172,136,56
W 2
X 190
Cvaa 176,144,0,224
Ra 136,242,26624
K 218,188,189
Mvaa 176,148,4
X 191
Cvaa 180,148,20,228
s_add_u32 s91, s92, s51
X 160
Mvaa 180,144,16
Ra 140,242,27648
K 219,190,191
Cvaa 184,144,32,232
X 161
Mvaa 184,148,36
s_add_u32 s95, s92, 2
X 162
Cvaa 188,148,52,236
K 196,160,161
Mvaa 188,144,48
X 163
Mvaa 176,152,8
s_and_b32 s95, s95, 15
X 164
Mvaa 176,156,12
Ra 144,242,28672
K 197,162,163
Mvaa 180,156,28
X 165
Mvaa 180,152,24
s_mul_i32 s95, s95, 40960
Ra 148,242,29696
Mvaa 184,152,40
X 166
Mvaa 184,156,44
K 204,164,165
Mvaa 188,156,60
s_mul_i32 s71, s51, 0
X 167
Mvaa 188,152,56
W 2
X 168
Mvav 32,128,192
Ra 152,242,30720
K 205,166,167
Mvav 64,128,200
)"
R"(s_add_u32 s71, s71, 153600
X 169
Mvav 96,128,208
Ra 156,242,31744
Mvav 128,128,216
X 170
Mvav 132,132,216
s_add_u32 m0, s71, s48
K 212,168,169
Mvav 100,132,208
X 171
Mvav 68,132,200
X 172
Mvav 36,132,192
s_lshl2_add_u32 s94, s48, s95
K 213,170,171
Mvav 40,136,192
X 173
Mvav 72,136,200
X 174
Mvav 104,136,208
D 16,40,94,0
Mvav 136,136,216
K 220,172,173
Mvav 140,140,216
X 175
Mvav 108,140,208
X 176
Mvav 76,140,200
s_waitcnt vmcnt(2) lgkmcnt(0)
s_barrier
v_xor_b32 v242, 0x10000, v242
Mvav 44,140,192
D 17,40,94,1024
K 221,174,175
Mvav 48,144,192
Ra 128,242,0
Mvav 80,144,200
X 177
Mvav 112,144,208
X 178
Mvav 144,144,216
D 18,40,94,2048
Ra 132,242,1024
Mvav 148,148,216
K 198,176,177
Mvav 116,148,208
X 179
Mvav 84,148,200
X 180
Mvav 52,148,192
D 19,40,94,3072
Ra 136,242,2048
Mvav 56,152,192
s_add_u32 m0, m0, 4096
Ra 140,242,3072
K 199,178,179
Mvav 88,152,200
X 181
Mvav 120,152,208
X 182
Mvav 152,152,216
buffer_load_dwordx4 v20, s[40:43], s94 offen lds
K 206,180,181
Mvav 156,156,216
X 183
Mvav 124,156,208
Mvav 92,156,200
K 207,182,183
Mvav 60,156,192
.Lf1_38%=:
W 0
Cvaa 160,128,0,224
v_bfe_u32 v28, v1, 4, 2
v_lshlrev_b32 v28, 3, v28
Mvaa 160,132,4
Ra 144,242,4096
Ra 148,242,5120
Cvaa 164,132,20,228
Ra 152,242,6144
buffer_load_dwordx2 v[28:29], v28, s[8:11], s54 offen sc1
Mvaa 164,128,16
s_lshl_b32 s92, s58, 2
s_add_u32 s91, s92, s51
s_add_u32 s95, s91, 3
Cvaa 168,128,32,232
s_and_b32 s95, s95, 15
s_mul_i32 s95, s95, 40960
s_mul_i32 s71, s51, 10240
Mvaa 168,132,36
s_add_u32 s71, s71, 0
s_add_u32 m0, s71, s48
s_lshl2_add_u32 s94, s48, s95
Cvaa 172,132,52,236
X 184
Mvaa 172,128,48
X 185
Mvaa 160,136,8
X 186
Mvaa 160,140,12
Ra 156,242,7168
K 214,184,185
Mvaa 164,140,28
X 187
Mvaa 164,136,24
D 16,40,94,0
Ra 128,242,16384
Mvaa 168,136,40
s_add_u32 s95, s91, 5
s_and_b32 s95, s95, 15
s_mul_i32 s95, s95, 40960
Mvaa 168,140,44
s_mul_i32 s71, s51, 10240
s_add_u32 s71, s71, 20480
Ra 132,242,17408
Mvaa 172,140,60
X 188
Mvaa 172,136,56
D 17,40,94,1024
K 215,186,187
v_smfmac_f32_16x16x64_bf16 a[160:163], a[176:179], v[192:199], v240
X 189
v_smfmac_f32_16x16x64_bf16 a[164:167], a[176:179], v[200:207], v240
X 190
K 222,188,189
X 191
s_nop 0
K 223,190,191
v_smfmac_f32_16x16x64_bf16 a[168:171], a[176:179], v[208:215], v240
D 18,40,94,2048
Ra 136,242,18432
v_smfmac_f32_16x16x64_bf16 a[172:175], a[176:179], v[216:223], v240
W 3
X 160
Cvaa 176,144,0,224
X 161
Mvaa 176,148,4
X 162
Cvaa 180,148,20,228
D 19,40,94,3072
K 192,160,161
Mvaa 180,144,16
s_add_u32 m0, m0, 4096
X 163
Cvaa 184,144,32,232
X 164
Mvaa 184,148,36
Ra 140,242,19456
K 193,162,163
Cvaa 188,148,52,236
buffer_load_dwordx4 v20, s[40:43], s94 offen lds
Mvaa 188,144,48
s_add_u32 m0, s71, s48
s_lshl2_add_u32 s94, s48, s95
Ra 144,242,20480
Mvaa 176,152,8
X 165
Mvaa 176,156,12
X 166
Mvaa 180,156,28
D 16,40,94,0
K 200,164,165
Mvaa 180,152,24
X 167
Mvaa 184,152,40
X 172
Mvaa 184,156,44
D 17,40,94,1024
K 201,166,167
Mvaa 188,156,60
X 173
Mvaa 188,152,56
s_cmp_eq_u32 s60, 16
s_cbranch_scc1 .Lmk_77%=
.Lmkr_78%=:
W 1
Mvav 32,128,196
Ra 148,242,21504
D 18,40,94,2048
Mvav 64,128,204
X 174
Mvav 96,128,212
Ra 152,242,22528
K 216,172,173
Mvav 128,128,220
X 175
Mvav 132,132,220
s_mov_b32 s18, 40960
X 168
Mvav 100,132,212
D 19,40,94,3072
K 217,174,175
Mvav 68,132,204
s_add_u32 m0, m0, 4096
X 169
Mvav 36,132,196
s_mov_b32 s19, 73728
X 170
Mvav 40,136,196
s_bitcmp1_b32 s14, 0
s_cselect_b32 s18, 49152, s18
s_cselect_b32 s19, 98304, s19
Ra 156,242,23552
K 208,168,169
Mvav 72,136,204
s_cmp_lt_u32 s14, 2
s_cselect_b32 s20, s18, s19
s_cselect_b32 s21, s19, s18
X 171
Mvav 104,136,212
X 176
Mvav 136,136,220
buffer_load_dwordx4 v20, s[40:43], s94 offen lds
K 209,170,171
Mvav 140,140,220
s_waitcnt vmcnt(10)
v_cmp_eq_u64 vcc, s[12:13], v[28:29]
s_cmp_eq_u64 vcc, -1
s_cselect_b32 s46, 79691776, 0
s_add_u32 s81, s58, 0
s_and_b32 s81, s81, 3
Mvav 108,140,212
s_lshl_b32 s81, s81, 12
s_add_u32 s81, s81, s49
Ra 128,242,24576
Mvav 76,140,204
X 177
Mvav 44,140,196
W 1
X 178
Mvav 48,144,196
buffer_load_dwordx4 a[184:187], v1, s[44:47], s81 offen offset:0 sc1
K 194,176,177
Mvav 80,144,204
s_add_u32 s82, s81, 131072
X 179
Mvav 112,144,212
v_add_u32 v2, s20, v1
v_add_u32 v3, s21, v1
Mvav 144,144,220
s_lshl_b32 s18, s14, 9
X 180
Mvav 148,148,220
buffer_load_dwordx4 a[200:203], v1, s[44:47], s82 offen offset:0 sc1
K 195,178,179
Mvav 116,148,212
X 181
Mvav 84,148,204
Ra 132,242,25600
v_lshrrev_b32 v232, 1, v1
Mvav 52,148,196
X 182
Mvav 56,152,196
v_add_u32 v232, s18, v232
K 202,180,181
Mvav 88,152,204
buffer_load_dwordx4 a[188:191], v1, s[44:47], s81 offen offset:1024 sc1
v_bfe_u32 v12, v1, 6, 2
Mvav 120,152,212
X 183
Mvav 152,152,220
Ra 136,242,26624
v_lshrrev_b32 v13, 8, v1
Mvav 156,156,220
X 184
Mvav 124,156,212
v_bfe_u32 v14, v1, 4, 1
K 203,182,183
Mvav 92,156,204
Ra 140,242,27648
v_lshlrev_b32 v14, 4, v14
Mvav 60,156,196
X 185
Maav 160,180,192
X 186
K 210,184,185
X 187
X 188
K 211,186,187
Maav 164,180,200
X 189
X 190
K 218,188,189
X 191
s_nop 0
K 219,190,191
Maav 168,180,208
buffer_load_dwordx4 a[204:207], v1, s[44:47], s82 offen offset:1024 sc1
v_mov_b32 v15, 0x3f80
Maav 172,180,216
W 0
v_lshlrev_b32 v15, v14, v15
v_bfe_u32 v14, v1, 5, 1
Mvav 32,128,192
v_xor_b32 v27, v12, v13
v_lshl_or_b32 v27, v27, 1, v14
Mvav 64,128,200
buffer_load_dwordx4 a[192:195], v1, s[44:47], s81 offen offset:2048 sc1
Ra 144,242,28672
Mvav 96,128,208
v_cmp_eq_u32_e64 s[86:87], 0, v27
v_mov_b32 v226, 0
Mvav 128,128,216
Ra 148,242,29696
v_cndmask_b32_e64 v224, 0, v15, s[86:87]
Mvav 132,132,216
buffer_load_dwordx4 a[208:211], v1, s[44:47], s82 offen offset:2048 sc1
v_mov_b32 v228, 0
Mvav 100,132,208
v_mov_b32 v230, v224
v_cmp_eq_u32_e64 s[86:87], 1, v27
Mvav 68,132,200
Ra 152,242,30720
v_mov_b32 v227, 0
Mvav 36,132,192
buffer_load_dwordx4 a[196:199], v1, s[44:47], s81 offen offset:3072 sc1
v_cndmask_b32_e64 v225, 0, v15, s[86:87]
Mvav 40,136,192
v_mov_b32 v229, 0
v_mov_b32 v231, v225
Mvav 72,136,200
Ra 156,242,31744
G 4,160
Mvav 104,136,208
buffer_load_dwordx4 a[212:215], v1, s[44:47], s82 offen offset:3072 sc1
G 8,161
Mvav 136,136,216
s_add_u32 s81, s58, 1
s_and_b32 s81, s81, 3
s_lshl_b32 s81, s81, 12
Mvav 140,140,216
s_add_u32 s81, s81, s49
G 5,164
G 9,165
Mvav 108,140,208
buffer_load_dwordx4 a[216:219], v1, s[44:47], s81 offen offset:0 sc1
G 6,168
Mvav 76,140,200
s_add_u32 s82, s81, 131072
G 10,169
G 7,172
Mvav 44,140,192
W 0
G 11,173
A 4,4,8
Mvav 48,144,192
buffer_load_dwordx4 a[232:235], v1, s[44:47], s82 offen offset:0 sc1
A 5,5,9
Mvav 80,144,200
A 6,6,10
A 7,7,11
Mvav 112,144,208
ds_write_b64 v232, v[6:7] offset:57344
A 8,4,5
Mvav 144,144,216
A 9,6,7
A 8,8,9
Mvav 148,148,216
buffer_load_dwordx4 a[220:223], v1, s[44:47], s81 offen offset:1024 sc1
v_cmp_ngt_f32_e64 s[86:87], s90, v8
Mvav 116,148,208
s_lshl_b32 s19, s14, 2
v_mov_b32 v9, s19
Mvav 84,148,200
s_or_b64 s[88:89], s[88:89], s[86:87]
s_and_b64 s[88:89], s[88:89], s[24:25]
s_cselect_b32 s18, 1, 0
v_mov_b32 v8, s18
Mvav 52,148,192
ds_write_b32 v9, v8 offset:59392
v_xor_b32 v10, 1024, v232
Mvav 56,152,192
v_bfe_u32 v11, v1, 2, 4
K 96,96,97
Mvav 88,152,200
K 97,98,99
K 98,100,101
Mvav 120,152,208
buffer_load_dwordx4 a[236:239], v1, s[44:47], s82 offen offset:1024 sc1
K 99,102,103
Mvav 152,152,216
ds_write_b128 v2, v[96:99] offset:0
K 128,128,129
Mvav 156,156,216
K 129,130,131
K 130,132,133
Mvav 124,156,208
buffer_load_dwordx4 a[224:227], v1, s[44:47], s81 offen offset:2048 sc1
K 131,134,135
Mvav 92,156,200
ds_write_b128 v2, v[128:131] offset:4096
K 104,104,105
Mvav 60,156,192
K 105,106,107
K 106,108,109
K 107,110,111
buffer_load_dwordx4 a[240:243], v1, s[44:47], s82 offen offset:2048 sc1
ds_write_b128 v2, v[104:107] offset:1024
K 136,136,137
K 137,138,139
)"
R"(K 138,140,141
K 139,142,143
ds_write_b128 v2, v[136:139] offset:5120
buffer_load_dwordx4 a[228:231], v1, s[44:47], s81 offen offset:3072 sc1
K 112,112,113
K 113,114,115
K 114,116,117
K 115,118,119
buffer_load_dwordx4 a[244:247], v1, s[44:47], s82 offen offset:3072 sc1
ds_write_b128 v2, v[112:115] offset:2048
K 144,144,145
K 145,146,147
K 146,148,149
K 147,150,151
ds_write_b128 v2, v[144:147] offset:6144
K 120,120,121
K 121,122,123
K 122,124,125
K 123,126,127
ds_write_b128 v2, v[120:123] offset:3072
K 152,152,153
K 153,154,155
K 154,156,157
K 155,158,159
ds_write_b128 v2, v[152:155] offset:7168
.Lend_41%=:
s_waitcnt vmcnt(8) lgkmcnt(0)
s_barrier
s_cmp_eq_u32 s46, 0
s_cbranch_scc1 .Llate_79%=
ds_read_b32 v11, v11 offset:59392
ds_read_b64 v[8:9], v10 offset:57344
Rv 176,3,0
Rv 180,3,1024
Rv 184,3,2048
Rv 188,3,3072
Rv 192,3,4096
Rv 196,3,5120
Rv 200,3,6144
Rv 204,3,7168
W 6
Mvvv 32,224,176
s_lshl_b32 s92, s58, 2
s_add_u32 s91, s92, s51
s_add_u32 s95, s91, 7
Mvvv 36,228,176
s_and_b32 s95, s95, 15
s_mul_i32 s95, s95, 40960
s_mul_i32 s71, s51, 20480
Mvvv 40,224,180
s_add_u32 s71, s71, 61440
s_add_u32 m0, s71, s48
s_lshl2_add_u32 s94, s48, s95
Mvvv 44,228,180
W 4
v_cmp_ne_u32 vcc, 0, v11
s_cmp_lg_u64 vcc, 0
s_cbranch_scc1 .Lredo%=
A 4,4,8
Mvvv 48,224,184
v_rcp_f32 v4, v4
Mvvv 52,228,184
Ta 128,25,10240
A 5,5,9
Mvvv 56,224,188
Ta 130,25,15360
D 16,40,94,0
Mvvv 60,228,188
W 4
s_add_u32 s95, s91, 9
s_and_b32 s95, s95, 15
Mvvv 64,224,192
s_mul_i32 s95, s95, 40960
s_mul_i32 s71, s51, 10240
s_add_u32 s71, s71, 112640
Mvvv 68,228,192
v_rcp_f32 v5, v5
Mvvv 72,224,196
s_lshl_b32 s93, s58, 12
s_add_u32 s93, s93, s50
Ta 132,25,20480
Mvvv 76,228,196
Ta 134,25,25600
W 4
Mvvv 80,224,200
U 32,32,4
U 33,33,4
Mvvv 84,228,200
Ta 136,26,10272
U 34,34,4
Mvvv 88,224,204
Ta 138,26,15392
U 35,35,4
Mvvv 92,228,204
Ta 140,26,20512
Ta 142,26,25632
W 4
Zvaa 96,128,184
U 36,36,4
U 37,37,4
Mvaa 96,132,188
D 17,40,94,1024
U 38,38,4
Zvaa 104,132,204
U 39,39,4
K 32,32,33
Mvaa 104,128,200
Ta 144,25,10304
Ta 146,25,15424
Ta 148,25,20544
Ta 150,25,25664
W 4
Zvaa 108,136,200
K 33,34,35
K 34,36,37
Mvaa 108,140,204
Ta 152,26,10336
K 35,38,39
Zvaa 100,140,188
buffer_store_dwordx4 v[32:35], v1, s[8:11], s93 offen offset:0 sc1
U 40,40,4
Mvaa 100,136,184
Ta 154,26,15456
Ta 156,26,20576
Ta 158,26,25696
W 4
Zvaa 112,144,184
U 41,41,4
U 42,42,4
Mvaa 112,148,188
U 43,43,4
U 44,44,4
Zvaa 120,148,204
D 18,40,94,2048
U 45,45,4
Mvaa 120,144,200
Ta 128,25,10368
Ta 130,25,15488
Ta 132,25,20608
Ta 134,25,25728
W 4
Zvaa 124,152,200
U 46,46,4
U 47,47,4
Mvaa 124,156,204
K 40,40,41
K 41,42,43
Zvaa 116,156,188
K 42,44,45
K 43,46,47
Mvaa 116,152,184
Ta 136,26,10400
Ta 138,26,15520
Ta 140,26,20640
Ta 142,26,25760
W 4
Zvaa 128,128,184
buffer_store_dwordx4 v[40:43], v1, s[8:11], s93 offen offset:1024 sc1
U 48,48,4
Mvaa 128,132,188
U 49,49,4
U 50,50,4
Zvaa 136,132,204
U 51,51,4
U 52,52,4
Mvaa 136,128,200
Ta 144,25,10432
Ta 146,25,15552
Ta 148,25,20672
Ta 150,25,25792
W 4
Zvaa 140,136,200
D 19,40,94,3072
U 53,53,4
Mvaa 140,140,204
s_add_u32 m0, m0, 4096
U 54,54,4
U 55,55,4
Zvaa 132,140,188
K 48,48,49
K 49,50,51
Mvaa 132,136,184
Ta 152,26,10464
Ta 154,26,15584
Ta 156,26,20704
Ta 158,26,25824
W 4
Zvaa 144,144,184
K 50,52,53
K 51,54,55
Mvaa 144,148,188
buffer_load_dwordx4 v20, s[40:43], s94 offen lds
Ta 128,25,10496
Zvaa 152,148,204
s_add_u32 m0, s71, s48
s_lshl2_add_u32 s94, s48, s95
Ta 130,25,15616
Mvaa 152,144,200
Ta 132,25,20736
Ta 134,25,25856
W 4
Zvaa 156,152,200
Ta 136,26,10528
Ta 138,26,15648
Mvaa 156,156,204
buffer_store_dwordx4 v[48:51], v1, s[8:11], s93 offen offset:2048 sc1
U 56,56,4
Zvaa 148,156,188
U 57,57,4
U 58,58,4
Mvaa 148,152,184
Ta 140,26,20768
Ta 142,26,25888
W 4
Zvaa 160,128,184
U 59,59,4
U 60,60,4
Mvaa 160,132,188
U 61,61,4
U 62,62,4
Zvaa 168,132,204
U 63,63,4
K 56,56,57
Mvaa 168,128,200
Ta 144,25,30720
Ta 146,25,35840
Ta 148,21,0
Ta 150,21,5120
W 4
Zvaa 172,136,200
D 16,40,94,0
K 57,58,59
Mvaa 172,140,204
K 58,60,61
K 59,62,63
Zvaa 164,140,188
Ta 152,26,30752
Ta 154,26,35872
Mvaa 164,136,184
Ta 156,22,32
Ta 158,22,5152
W 4
Mvaa 96,144,192
buffer_store_dwordx4 v[56:59], v1, s[8:11], s93 offen offset:3072 sc1
U 64,64,5
Mvaa 96,148,196
U 65,65,5
U 66,66,5
Mvaa 104,148,212
U 67,67,5
U 68,68,5
Mvaa 104,144,208
Ta 128,25,30784
Ta 130,25,35904
Ta 132,21,64
Ta 134,21,5184
W 4
Mvaa 108,152,208
D 17,40,94,1024
U 69,69,5
Mvaa 108,156,212
U 70,70,5
U 71,71,5
Mvaa 100,156,196
K 64,64,65
K 65,66,67
Mvaa 100,152,192
Ta 136,26,30816
Ta 138,26,35936
Ta 140,22,96
Ta 142,22,5216
W 4
Mvaa 112,128,192
K 66,68,69
K 67,70,71
Mvaa 112,132,196
s_add_u32 s67, s93, 131072
buffer_store_dwordx4 v[64:67], v1, s[8:11], s67 offen offset:0 sc1
U 72,72,5
Mvaa 120,132,212
U 73,73,5
U 74,74,5
Mvaa 120,128,208
Ta 144,25,30848
Ta 146,25,35968
Ta 148,21,128
Ta 150,21,5248
W 4
Mvaa 124,136,208
U 75,75,5
U 76,76,5
Mvaa 124,140,212
D 18,40,94,2048
U 77,77,5
Mvaa 116,140,196
U 78,78,5
U 79,79,5
Mvaa 116,136,192
Ta 152,26,30880
Ta 154,26,36000
Ta 156,22,160
Ta 158,22,5280
W 4
Mvaa 128,144,192
K 72,72,73
K 73,74,75
Mvaa 128,148,196
K 74,76,77
K 75,78,79
Mvaa 136,148,212
buffer_store_dwordx4 v[72:75], v1, s[8:11], s67 offen offset:1024 sc1
U 80,80,5
Mvaa 136,144,208
Ta 128,25,30912
Ta 130,25,36032
Ta 132,21,192
Ta 134,21,5312
W 4
Mvaa 140,152,208
D 19,40,94,3072
s_add_u32 m0, m0, 4096
s_nop 0
buffer_load_dwordx4 v20, s[40:43], s94 offen lds
Mvaa 140,156,212
s_add_u32 s81, s58, 2
s_and_b32 s81, s81, 3
s_lshl_b32 s81, s81, 12
Mvaa 132,156,196
s_add_u32 s81, s81, s49
U 81,81,5
U 82,82,5
Mvaa 132,152,192
Ta 136,26,30944
Ta 138,26,36064
Ta 140,22,224
Ta 142,22,5344
W 4
Mvaa 144,128,192
U 83,83,5
U 84,84,5
Mvaa 144,132,196
U 85,85,5
U 86,86,5
Mvaa 152,132,212
U 87,87,5
K 80,80,81
Mvaa 152,128,208
Ta 144,25,30976
Ta 146,25,36096
Ta 148,21,256
Ta 150,21,5376
W 4
Mvaa 156,136,208
K 81,82,83
K 82,84,85
Mvaa 156,140,212
buffer_load_dwordx4 a[0:3], v1, s[8:11], s81 offen offset:0 sc1
K 83,86,87
Mvaa 148,140,196
s_add_u32 s82, s81, 131072
Ta 152,26,31008
Ta 154,26,36128
Mvaa 148,136,192
Ta 156,22,288
Ta 158,22,5408
W 4
Mvaa 160,144,192
Ta 128,21,10240
Ta 130,21,15360
Mvaa 160,148,196
buffer_store_dwordx4 v[80:83], v1, s[8:11], s67 offen offset:2048 sc1
U 88,88,5
Mvaa 168,148,212
U 89,89,5
U 90,90,5
Mvaa 168,144,208
Ta 132,21,20480
Ta 134,21,25600
W 4
Mvaa 172,152,208
U 91,91,5
U 92,92,5
Mvaa 172,156,212
U 93,93,5
U 94,94,5
Mvaa 164,156,196
U 95,95,5
K 88,88,89
Mvaa 164,152,192
Ta 136,22,10272
Ta 138,22,15392
Ta 140,22,20512
Ta 142,22,25632
s_waitcnt vmcnt(22) lgkmcnt(4)
Mvaa 96,128,216
K 89,90,91
K 90,92,93
Mvaa 96,132,220
buffer_load_dwordx4 a[16:19], v1, s[8:11], s82 offen offset:0 sc1
K 91,94,95
)"
R"(Mvaa 104,132,236
Ta 144,21,10304
Ta 146,21,15424
Mvaa 104,128,232
Ta 148,21,20544
Ta 150,21,25664
W 4
Mvaa 108,136,232
Ta 152,22,10336
Ta 154,22,15456
Mvaa 108,140,236
buffer_store_dwordx4 v[88:91], v1, s[8:11], s67 offen offset:3072 sc1
Ta 156,22,20576
Mvaa 100,140,220
Ta 158,22,25696
Ta 128,21,10368
Mvaa 100,136,216
W 5
Ta 130,21,15488
Ta 132,21,20608
Mvaa 112,144,216
Ta 134,21,25728
Ta 136,22,10400
Mvaa 112,148,220
buffer_load_dwordx4 a[4:7], v1, s[8:11], s81 offen offset:1024 sc1
Ta 138,22,15520
Mvaa 120,148,236
Ta 140,22,20640
Ta 142,22,25760
Mvaa 120,144,232
W 8
Ta 144,21,10432
Ta 146,21,15552
Mvaa 124,152,232
Mvaa 124,156,236
buffer_load_dwordx4 a[20:23], v1, s[8:11], s82 offen offset:1024 sc1
Ta 148,21,20672
Mvaa 116,156,220
Ta 150,21,25792
Mvaa 116,152,216
W 8
Ta 152,22,10464
Ta 154,22,15584
Mvaa 128,128,216
Mvaa 128,132,220
buffer_load_dwordx4 a[8:11], v1, s[8:11], s81 offen offset:2048 sc1
Ta 156,22,20704
Mvaa 136,132,236
Ta 158,22,25824
Mvaa 136,128,232
W 8
Ta 128,21,10496
Ta 130,21,15616
Mvaa 140,136,232
s_waitcnt vmcnt(7)
s_barrier
Mvaa 140,140,236
buffer_load_dwordx4 a[24:27], v1, s[8:11], s82 offen offset:2048 sc1
Ta 132,21,20736
Mvaa 132,140,220
Ta 134,21,25856
Mvaa 132,136,216
W 8
Ta 136,22,10528
Ta 138,22,15648
Mvaa 144,144,216
Mvaa 144,148,220
buffer_load_dwordx4 a[12:15], v1, s[8:11], s81 offen offset:3072 sc1
Ta 140,22,20768
Mvaa 152,148,236
Ta 142,22,25888
Mvaa 152,144,232
W 8
Ta 144,21,30720
Ta 146,21,35840
Mvaa 156,152,232
Mvaa 156,156,236
buffer_load_dwordx4 a[28:31], v1, s[8:11], s82 offen offset:3072 sc1
Ta 148,23,0
Mvaa 148,156,220
s_lshl_b32 s92, s58, 2
s_add_u32 s91, s92, s51
s_add_u32 s95, s91, 11
Mvaa 148,152,216
W 7
s_and_b32 s95, s95, 15
s_mul_i32 s95, s95, 40960
Mvaa 160,128,216
s_mul_i32 s71, s51, 10240
s_add_u32 s71, s71, 40960
s_add_u32 m0, s71, s48
Mvaa 160,132,220
s_lshl2_add_u32 s94, s48, s95
D 16,40,94,0
Ta 150,23,5120
Mvaa 168,132,236
s_add_u32 s95, s91, 13
s_and_b32 s95, s95, 15
s_mul_i32 s95, s95, 40960
Mvaa 168,128,232
W 4
s_mul_i32 s71, s51, 20480
s_add_u32 s71, s71, 71680
Mvaa 172,136,232
Ta 152,22,30752
Ta 154,22,35872
Mvaa 172,140,236
D 17,40,94,1024
Ta 156,24,32
Mvaa 164,140,220
Ta 158,24,5152
Ta 128,21,30784
Mvaa 164,136,216
W 5
Ta 130,21,35904
Ta 132,23,64
Mvaa 96,144,224
Ta 134,23,5184
Ta 136,22,30816
Mvaa 96,148,228
D 18,40,94,2048
Ta 138,22,35936
Mvaa 104,148,244
Ta 140,24,96
Ta 142,24,5216
Mvaa 104,144,240
W 8
Ta 144,21,30848
Ta 146,21,35968
Mvaa 108,152,240
Mvaa 108,156,244
D 19,40,94,3072
Ta 148,23,128
Mvaa 100,156,228
s_add_u32 m0, m0, 4096
Ta 150,23,5248
Mvaa 100,152,224
W 8
Ta 152,22,30880
Ta 154,22,36000
Mvaa 112,128,224
Mvaa 112,132,228
buffer_load_dwordx4 v20, s[40:43], s94 offen lds
Ta 156,24,160
Mvaa 120,132,244
s_add_u32 m0, s71, s48
s_lshl2_add_u32 s94, s48, s95
Ta 158,24,5280
Mvaa 120,128,240
W 8
Ta 128,21,30912
Ta 130,21,36032
Mvaa 124,136,240
Mvaa 124,140,244
D 16,40,94,0
Ta 132,23,192
Mvaa 116,140,228
s_add_u32 s95, s92, 15
s_and_b32 s95, s95, 15
s_mul_i32 s95, s95, 40960
Mvaa 116,136,224
W 7
s_mul_i32 s71, s51, 0
s_add_u32 s71, s71, 102400
Mvaa 128,144,224
Ta 134,23,5312
Ta 136,22,30944
Mvaa 128,148,228
D 17,40,94,1024
Ta 138,22,36064
Mvaa 136,148,244
Ta 140,24,224
Ta 142,24,5344
Mvaa 136,144,240
W 8
Ta 144,21,30976
Ta 146,21,36096
Mvaa 140,152,240
Mvaa 140,156,244
D 18,40,94,2048
Ta 148,23,256
Mvaa 132,156,228
Ta 150,23,5376
Mvaa 132,152,224
W 8
Ta 152,22,31008
Ta 154,22,36128
Mvaa 144,128,224
s_lshl_b32 s80, s58, 3
s_add_u32 s80, s80, s55
Mvaa 144,132,228
D 19,40,94,3072
Ta 156,24,288
Mvaa 152,132,244
s_add_u32 m0, m0, 4096
Ta 158,24,5408
Mvaa 152,128,240
W 8
Ta 128,23,20480
Ta 130,23,25600
Mvaa 156,136,240
s_waitcnt vmcnt(15)
v_mov_b64 v[28:29], s[12:13]
s_mov_b64 exec, 1
buffer_store_dwordx2 v[28:29], off, s[8:11], s80 sc1
s_mov_b64 exec, -1
Mvaa 156,140,244
Ta 132,23,51200
Ta 134,23,56320
Mvaa 148,140,228
Mvaa 148,136,224
W 8
Ta 136,24,20512
Ta 138,24,25632
Mvaa 160,144,224
buffer_load_dwordx4 v20, s[40:43], s94 offen lds
Mvaa 160,148,228
s_add_u32 m0, s71, s48
s_lshl2_add_u32 s94, s48, s95
Ta 140,24,51232
Mvaa 168,148,244
Ta 142,24,56352
Mvaa 168,144,240
W 8
Ta 144,23,20544
Ta 146,23,25664
Mvaa 172,152,240
D 16,40,94,0
Mvaa 172,156,244
Ta 148,23,51264
Ta 150,23,56384
Mvaa 164,156,228
Mvaa 164,152,224
s_waitcnt vmcnt(16) lgkmcnt(8)
Ta 152,24,20576
Ta 154,24,25696
Mvaa 96,128,0
D 17,40,94,1024
Mvaa 96,132,4
Ta 156,24,51296
Ta 158,24,56416
Mvaa 104,132,20
Mvaa 104,128,16
W 8
Ta 128,23,20608
Ta 130,23,25728
Mvaa 108,136,16
D 18,40,94,2048
Mvaa 108,140,20
Ta 132,23,51328
Ta 134,23,56448
Mvaa 100,140,4
Mvaa 100,136,0
W 8
Ta 136,24,20640
Ta 138,24,25760
Mvaa 112,144,0
D 19,40,94,3072
Mvaa 112,148,4
s_add_u32 m0, m0, 4096
Ta 140,24,51360
Ta 142,24,56480
Mvaa 120,148,20
Mvaa 120,144,16
W 8
Ta 144,23,20672
Ta 146,23,25792
Mvaa 124,152,16
buffer_load_dwordx4 v20, s[40:43], s94 offen lds
Mvaa 124,156,20
s_add_u32 s81, s58, 3
s_and_b32 s81, s81, 3
s_lshl_b32 s81, s81, 12
Mvaa 116,156,4
s_add_u32 s81, s81, s49
Ta 148,23,51392
Ta 150,23,56512
Mvaa 116,152,0
W 8
Ta 152,24,20704
Ta 154,24,25824
Mvaa 128,128,0
buffer_load_dwordx4 a[32:35], v1, s[8:11], s81 offen offset:0 sc1
Mvaa 128,132,4
s_add_u32 s82, s81, 131072
Ta 156,24,51424
Ta 158,24,56544
Mvaa 136,132,20
Mvaa 136,128,16
W 8
Ta 128,23,20736
Ta 130,23,25856
Mvaa 140,136,16
s_waitcnt vmcnt(12)
s_barrier
buffer_load_dwordx4 a[48:51], v1, s[8:11], s82 offen offset:0 sc1
Mvaa 140,140,20
Ta 132,23,51456
v_and_b32 v27, 1, v12
Mvaa 132,140,4
Ta 134,23,56576
Mvaa 132,136,0
W 8
Ta 136,24,20768
v_lshlrev_b32 v27, 1, v27
Mvaa 144,144,0
buffer_load_dwordx4 a[36:39], v1, s[8:11], s81 offen offset:1024 sc1
Ta 138,24,25888
Mvaa 144,148,4
Ta 140,24,51488
Ta 142,24,56608
Mvaa 152,148,20
v_xor_b32 v27, v27, v13
Mvaa 152,144,16
W 8
Ta 144,25,0
Ta 146,25,5120
Mvaa 156,152,16
buffer_load_dwordx4 a[52:55], v1, s[8:11], s82 offen offset:1024 sc1
v_and_b32 v12, 2, v12
Mvaa 156,156,20
Ta 148,21,40960
Ta 150,21,46080
Mvaa 148,156,4
Mvaa 148,152,0
W 8
Ta 152,26,32
v_or_b32 v12, v12, v14
Mvaa 160,128,0
buffer_load_dwordx4 a[40:43], v1, s[8:11], s81 offen offset:2048 sc1
Ta 154,26,5152
Mvaa 160,132,4
Ta 156,22,40992
v_lshl_or_b32 v12, v27, 2, v12
Mvaa 168,132,20
Ta 158,22,46112
Mvaa 168,128,16
W 8
Ta 128,25,64
Ta 130,25,5184
Mvaa 172,136,16
buffer_load_dwordx4 a[56:59], v1, s[8:11], s82 offen offset:2048 sc1
v_cmp_eq_u32_e64 s[86:87], 0, v12
Mvaa 172,140,20
Ta 132,21,41024
Ta 134,21,46144
Mvaa 164,140,4
Mvaa 164,136,0
W 8
Ta 136,26,96
v_cndmask_b32_e64 v4, 0, v15, s[86:87]
Mvaa 96,144,8
buffer_load_dwordx4 a[44:47], v1, s[8:11], s81 offen offset:3072 sc1
Ta 138,26,5216
Mvaa 96,148,12
Ta 140,22,41056
v_cmp_eq_u32_e64 s[86:87], 1, v12
Mvaa 104,148,28
Ta 142,22,46176
Mvaa 104,144,24
W 8
Ta 144,25,128
Ta 146,25,5248
Mvaa 108,152,24
buffer_load_dwordx4 a[60:63], v1, s[8:11], s82 offen offset:3072 sc1
v_cndmask_b32_e64 v5, 0, v15, s[86:87]
Mvaa 108,156,28
Ta 148,21,41088
Ta 150,21,46208
Mvaa 100,156,12
v_cmp_eq_u32_e64 s[86:87], 2, v12
Mvaa 100,152,8
W 8
Ta 152,26,160
Ta 154,26,5280
Mvaa 112,128,8
Mvaa 112,132,12
Ta 156,22,41120
v_cndmask_b32_e64 v6, 0, v15, s[86:87]
Mvaa 120,132,28
Ta 158,22,46240
Mvaa 120,128,24
W 8
Ta 128,25,192
Ta 130,25,5312
Mvaa 124,136,24
v_cmp_eq_u32_e64 s[86:87], 3, v12
Mvaa 124,140,28
Ta 132,21,41152
Ta 134,21,46272
Mvaa 116,140,12
v_cndmask_b32_e64 v7, 0, v15, s[86:87]
Mvaa 116,136,8
W 8
Ta 136,26,224
Ta 138,26,5344
Mvaa 128,144,8
Mvaa 128,148,12
Ta 140,22,41184
v_cmp_eq_u32_e64 s[86:87], 4, v12
)"
R"(Mvaa 136,148,28
Ta 142,22,46304
Mvaa 136,144,24
W 8
Ta 144,25,256
v_cndmask_b32_e64 v8, 0, v15, s[86:87]
Mvaa 140,152,24
s_waitcnt vmcnt(8)
s_barrier
Ta 146,25,5376
Mvaa 140,156,28
Ta 148,21,41216
Ta 150,21,46336
Mvaa 132,156,12
v_cmp_eq_u32_e64 s[86:87], 5, v12
Mvaa 132,152,8
W 8
Ta 152,26,288
Ta 154,26,5408
Mvaa 144,128,8
Mvaa 144,132,12
Ta 156,22,41248
v_cndmask_b32_e64 v9, 0, v15, s[86:87]
Mvaa 152,132,28
Ta 158,22,46368
Mvaa 152,128,24
W 8
Ta 128,21,51200
v_cmp_eq_u32_e64 s[86:87], 6, v12
Mvaa 156,136,24
Ta 130,21,56320
Mvaa 156,140,28
Ta 132,23,10240
Ta 134,23,15360
Mvaa 148,140,12
v_cndmask_b32_e64 v10, 0, v15, s[86:87]
Mvaa 148,136,8
W 8
Ta 136,22,51232
Ta 138,22,56352
Mvaa 160,144,8
v_cmp_eq_u32_e64 s[86:87], 7, v12
v_bfe_u32 v28, v1, 4, 2
Mvaa 160,148,12
Ta 140,24,10272
v_lshlrev_b32 v28, 3, v28
Mvaa 168,148,28
Ta 142,24,15392
buffer_load_dwordx2 v[28:29], v28, s[8:11], s55 offen sc1
Mvaa 168,144,24
W 8
Ta 144,21,51264
v_cndmask_b32_e64 v11, 0, v15, s[86:87]
Mvaa 172,152,24
Ta 146,21,56384
Mvaa 172,156,28
Ta 148,23,10304
Ta 150,23,15424
Mvaa 164,156,12
s_mov_b32 s20, s38
Mvaa 164,152,8
s_waitcnt vmcnt(5) lgkmcnt(8)
Ta 152,22,51296
Ta 154,22,56416
Mvaa 96,128,32
s_and_b32 s21, s39, 0xffff
Mvaa 96,132,36
Ta 156,24,10336
Ta 158,24,15456
Mvaa 104,132,52
Mvaa 104,128,48
W 8
s_mov_b32 s22, 2621440
Ta 128,21,51328
Mvaa 108,136,48
Ta 130,21,56448
Mvaa 108,140,52
s_mov_b32 s23, 0x20000
Ta 132,23,10368
Ta 134,23,15488
Mvaa 100,140,36
Mvaa 100,136,32
W 8
Ta 136,22,51360
Ta 138,22,56480
Mvaa 112,144,32
s_and_b32 s17, s17, 0xffff
Mvaa 112,148,36
Ta 140,24,10400
Ta 142,24,15520
Mvaa 120,148,52
Mvaa 120,144,48
W 8
s_mov_b32 s18, 2621440
Ta 144,21,51392
Mvaa 124,152,48
Ta 146,21,56512
Mvaa 124,156,52
s_mov_b32 s19, 0x20000
Ta 148,23,10432
Ta 150,23,15552
Mvaa 116,156,36
Mvaa 116,152,32
W 8
Ta 152,22,51424
Ta 154,22,56544
Mvaa 128,128,32
s_lshr_b32 s66, s15, 3
Mvaa 128,132,36
Ta 156,24,10464
Ta 158,24,15584
Mvaa 136,132,52
s_lshl_b32 s66, s66, 4
Mvaa 136,128,48
W 8
Ta 128,21,51456
Ta 130,21,56576
Mvaa 140,136,48
Mvaa 140,140,52
s_add_u32 s66, s66, s59
Ta 132,23,10496
Ta 134,23,15616
Mvaa 132,140,36
Mvaa 132,136,32
W 8
Ta 136,22,51488
Ta 138,22,56608
Mvaa 144,144,32
s_lshl_b32 s63, s66, 6
Mvaa 144,148,36
Ta 140,24,10528
Ta 142,24,15648
Mvaa 152,148,52
s_add_u32 s63, s63, 65536
Mvaa 152,144,48
W 8
Ta 144,23,30720
Ta 146,23,35840
Mvaa 156,152,48
Mvaa 156,156,52
s_mul_i32 s62, s66, 40960
Ta 148,23,40960
Ta 150,23,46080
Mvaa 148,156,36
Mvaa 148,152,32
W 8
s_add_u32 s62, s62, 56623104
Ta 152,24,30752
Mvaa 160,128,32
Ta 154,24,35872
Mvaa 160,132,36
Ta 156,24,40992
Ta 158,24,46112
Mvaa 168,132,52
s_lshl_b32 s66, s14, 13
s_waitcnt vmcnt(0)
v_cmp_eq_u64 vcc, s[12:13], v[28:29]
s_cmp_eq_u64 vcc, -1
s_cbranch_scc0 .Lspin_81%=
.Lok_82%=:
s_add_u32 s81, s58, 1
Mvaa 168,128,48
W 8
s_and_b32 s81, s81, 3
s_lshl_b32 s81, s81, 12
Mvaa 172,136,48
s_add_u32 s81, s81, s50
buffer_load_dwordx4 a[64:67], v1, s[8:11], s81 offen offset:0 sc1
Ta 128,23,30784
Mvaa 172,140,52
s_add_u32 s82, s81, 131072
s_add_u32 s78, s62, s66
Ta 130,23,35904
Mvaa 164,140,36
Ta 132,23,41024
Ta 134,23,46144
Mvaa 164,136,32
W 8
s_add_u32 s79, s78, 327680
Ta 136,24,30816
Mvaa 96,144,40
Ta 138,24,35936
buffer_load_dwordx4 a[80:83], v1, s[8:11], s82 offen offset:0 sc1
Mvaa 96,148,44
Ta 140,24,41056
Ta 142,24,46176
Mvaa 104,148,60
s_and_b32 s66, s14, 1
Mvaa 104,144,56
W 8
Ta 144,23,30848
Ta 146,23,35968
Mvaa 108,152,56
s_mul_i32 s66, s66, 327680
Mvaa 108,156,60
buffer_load_dwordx4 a[68:71], v1, s[8:11], s81 offen offset:1024 sc1
Ta 148,23,41088
Mvaa 100,156,44
Ta 150,23,46208
Mvaa 100,152,40
W 8
s_add_u32 s65, s62, s66
Ta 152,24,30880
Mvaa 112,128,40
Ta 154,24,36000
Mvaa 112,132,44
Ta 156,24,41120
Ta 158,24,46240
Mvaa 120,132,60
s_add_u32 s65, s65, 32768
buffer_load_dwordx4 a[84:87], v1, s[8:11], s82 offen offset:1024 sc1
Mvaa 120,128,56
W 8
Ta 128,23,30912
Ta 130,23,36032
Mvaa 124,136,56
s_mov_b32 s64, 0
Mvaa 124,140,60
Ta 132,23,41152
Ta 134,23,46272
Mvaa 116,140,44
Mvaa 116,136,40
W 8
buffer_load_dwordx4 a[72:75], v1, s[8:11], s81 offen offset:2048 sc1
v_add_u32 v19, 4096, v1
Mvaa 128,144,40
Ta 136,24,30944
Ta 138,24,36064
Mvaa 128,148,44
Ta 140,24,41184
v_bfe_u32 v27, v1, 4, 3
Mvaa 136,148,60
Ta 142,24,46304
Mvaa 136,144,56
W 8
Ta 144,23,30976
Ta 146,23,36096
Mvaa 140,152,56
buffer_load_dwordx4 a[88:91], v1, s[8:11], s82 offen offset:2048 sc1
v_lshlrev_b32 v27, 3, v27
Mvaa 140,156,60
Ta 148,23,41216
Ta 150,23,46336
Mvaa 132,156,44
v_bfe_u32 v18, v1, 7, 1
Mvaa 132,152,40
W 8
Ta 152,24,31008
Ta 154,24,36128
Mvaa 144,128,40
Mvaa 144,132,44
buffer_load_dwordx4 a[76:79], v1, s[8:11], s81 offen offset:3072 sc1
v_lshl_add_u32 v18, v18, 9, v27
Mvaa 152,132,60
Ta 156,24,41248
Ta 158,24,46368
Mvaa 152,128,56
W 8
Ta 128,25,10240
Ta 130,25,15360
Mvaa 156,136,56
s_mov_b32 s71, 0
Mvaa 156,140,60
Ta 132,25,20480
Ta 134,25,25600
Mvaa 148,140,44
buffer_load_dwordx4 a[92:95], v1, s[8:11], s82 offen offset:3072 sc1
v_bfe_u32 v20, v1, 4, 4
Mvaa 148,136,40
W 8
s_add_u32 s81, s58, 2
s_and_b32 s81, s81, 3
Mvaa 160,144,40
s_lshl_b32 s81, s81, 12
s_add_u32 s81, s81, s50
K 96,96,97
Mvaa 160,148,44
Ta 136,26,10272
Ta 138,26,15392
Mvaa 168,148,60
K 97,98,99
v_mul_u32_u24 v20, 1280, v20
Mvaa 168,144,56
W 6
Ta 140,26,20512
Ta 142,26,25632
Mvaa 172,152,56
buffer_load_dwordx4 a[96:99], v1, s[8:11], s81 offen offset:0 sc1
K 98,100,101
Mvaa 172,156,60
s_add_u32 s82, s81, 131072
Ta 144,25,10304
v_lshrrev_b32 v12, 8, v1
Mvaa 164,156,44
Ta 146,25,15424
Ta 148,25,20544
Mvaa 164,152,40
W 7
Ta 150,25,25664
K 99,102,103
Zvav 176,128,32
s_add_u32 s83, s52, 0
Ta 152,26,10336
v_lshl_add_u32 v20, v12, 4, v20
Mvav 176,132,40
s_bfe_u32 s66, s59, 0x10001
Ta 154,26,15456
buffer_load_dwordx4 a[112:115], v1, s[8:11], s82 offen offset:0 sc1
Zvav 184,132,72
Ta 156,26,20576
Ta 158,26,25696
Mvav 184,128,64
W 8
s_lshl_b32 s67, s57, 1
Ta 128,25,10368
Zvav 188,136,64
Ta 130,25,15488
Mvav 188,140,72
buffer_store_dwordx4 v[96:99], v1, s[8:11], s83 offen sc1
K 104,104,105
Zvav 180,140,40
s_add_u32 s66, s66, s67
Ta 132,25,20608
Ta 134,25,25728
Mvav 180,136,32
W 8
Ta 136,26,10400
K 105,106,107
Zvav 192,144,32
s_lshl_b32 s61, s66, 2
Ta 138,26,15520
Mvav 192,148,40
buffer_load_dwordx4 a[100:103], v1, s[8:11], s81 offen offset:1024 sc1
K 106,108,109
Zvav 200,148,72
Ta 140,26,20640
Ta 142,26,25760
Mvav 200,144,64
W 8
s_lshr_b32 s66, s59, 2
Ta 144,25,10432
Zvav 204,152,64
Ta 146,25,15552
K 107,110,111
Mvav 204,156,72
buffer_load_dwordx4 a[116:119], v1, s[8:11], s82 offen offset:1024 sc1
Ta 148,25,20672
Zvav 196,156,40
s_lshl_b32 s66, s66, 1
s_add_u32 s83, s52, 40960
Ta 150,25,25792
Mvav 196,152,32
W 8
Ta 152,26,10464
Ta 154,26,15584
Zvav 208,128,32
s_add_u32 s61, s61, s66
Mvav 208,132,40
buffer_load_dwordx4 a[104:107], v1, s[8:11], s81 offen offset:2048 sc1
Ta 156,26,20704
Zvav 216,132,72
Ta 158,26,25824
Mvav 216,128,64
W 8
s_and_b32 s66, s59, 1
Ta 128,25,10496
Zvav 220,136,64
Ta 130,25,15616
Mvav 220,140,72
s_add_u32 s61, s61, s66
buffer_store_dwordx4 v[104:107], v1, s[8:11], s83 offen sc1
K 112,112,113
Zvav 212,140,40
Ta 132,25,20736
K 113,114,115
Mvav 212,136,32
W 7
Ta 134,25,25856
K 114,116,117
Zvav 224,144,32
s_xor_b32 s67, s61, 0
Ta 136,26,10528
Ta 138,26,15648
Mvav 224,148,40
buffer_load_dwordx4 a[120:123], v1, s[8:11], s82 offen offset:2048 sc1
Ta 140,26,20768
Zvav 232,148,72
Ta 142,26,25888
K 115,118,119
Mvav 232,144,64
W 8
s_mul_i32 s72, s67, 20480
Ta 144,25,30720
Zvav 236,152,64
s_add_u32 s83, s52, 8192
Ta 146,25,35840
Mvav 236,156,72
s_mul_i32 s66, s58, 320
buffer_load_dwordx4 a[108:111], v1, s[8:11], s81 offen offset:3072 sc1
Ta 148,21,0
)"
R"(Zvav 228,156,40
Ta 150,21,5120
Mvav 228,152,32
W 8
Ta 152,26,30752
Ta 154,26,35872
Zvav 240,128,32
s_add_u32 s72, s72, s66
Mvav 240,132,40
buffer_store_dwordx4 v[112:115], v1, s[8:11], s83 offen sc1
K 120,120,121
Zvav 248,132,72
s_mul_i32 s75, s67, 20480
Ta 156,22,32
Ta 158,22,5152
Mvav 248,128,64
W 8
Ta 128,25,30784
K 121,122,123
Zvav 252,136,64
Ta 130,25,35904
Mvav 252,140,72
s_mul_i32 s66, s58, 2560
buffer_load_dwordx4 a[124:127], v1, s[8:11], s82 offen offset:3072 sc1
K 122,124,125
Zvav 244,140,40
s_add_u32 s81, s58, 3
s_and_b32 s81, s81, 3
s_lshl_b32 s81, s81, 12
Mvav 244,136,32
W 6
s_add_u32 s81, s81, s50
Ta 132,21,64
Mvav 176,144,48
s_add_u32 s75, s75, s66
Ta 134,21,5184
Ta 136,26,30816
Mvav 176,148,56
buffer_load_dwordx4 a[184:187], v1, s[8:11], s81 offen offset:0 sc1
Ta 138,26,35936
Mvav 184,148,88
s_add_u32 s82, s81, 131072
Ta 140,22,96
K 123,126,127
Mvav 184,144,80
W 7
s_add_u32 s83, s52, 49152
Ta 142,22,5216
Mvav 188,152,80
s_add_u32 s75, s75, 131072
Ta 144,25,30848
Ta 146,25,35968
Mvav 188,156,88
s_xor_b32 s67, s61, 124
buffer_load_dwordx4 a[200:203], v1, s[8:11], s82 offen offset:0 sc1
Ta 148,21,128
Mvav 180,156,56
Ta 150,21,5248
Mvav 180,152,48
W 8
s_mul_i32 s73, s67, 20480
Ta 152,26,30880
Mvav 192,128,48
Ta 154,26,36000
Mvav 192,132,56
buffer_store_dwordx4 v[120:123], v1, s[8:11], s83 offen sc1
K 128,128,129
Mvav 200,132,88
s_mul_i32 s66, s58, 320
Ta 156,22,160
K 129,130,131
Mvav 200,128,80
W 7
Ta 158,22,5280
K 130,132,133
Mvav 204,136,80
Ta 128,25,30912
Ta 130,25,36032
Mvav 204,140,88
s_add_u32 s73, s73, s66
buffer_load_dwordx4 a[188:191], v1, s[8:11], s81 offen offset:1024 sc1
Ta 132,21,192
Mvav 196,140,56
Ta 134,21,5312
K 131,134,135
Mvav 196,136,48
W 8
s_mul_i32 s76, s67, 20480
Ta 136,26,30944
Mvav 208,144,48
s_add_u32 s83, s52, 16384
Ta 138,26,36064
Mvav 208,148,56
buffer_load_dwordx4 a[204:207], v1, s[8:11], s82 offen offset:1024 sc1
Ta 140,22,224
Mvav 216,148,88
s_mul_i32 s66, s58, 2560
Ta 142,22,5344
Mvav 216,144,80
W 8
Ta 144,25,30976
Ta 146,25,36096
Mvav 220,152,80
s_add_u32 s76, s76, s66
Mvav 220,156,88
buffer_load_dwordx4 a[192:195], v1, s[8:11], s81 offen offset:2048 sc1
Ta 148,21,256
Mvav 212,156,56
Ta 150,21,5376
Mvav 212,152,48
W 8
s_add_u32 s76, s76, 131072
Ta 152,26,31008
Mvav 224,128,48
Ta 154,26,36128
Mvav 224,132,56
buffer_store_dwordx4 v[128:131], v1, s[8:11], s83 offen sc1
K 136,136,137
Mvav 232,132,88
K 137,138,139
K 138,140,141
Mvav 232,128,80
W 6
Ta 156,22,288
Ta 158,22,5408
Mvav 236,136,80
Ta 128,21,10240
v_bfe_u32 v15, v1, 7, 1
Mvav 236,140,88
buffer_load_dwordx4 a[208:211], v1, s[8:11], s82 offen offset:2048 sc1
Ta 130,21,15360
Mvav 228,140,56
K 139,142,143
v_mul_u32_u24 v14, 10240, v15
Mvav 228,136,48
W 6
Ta 132,21,20480
Ta 134,21,25600
Mvav 240,144,48
s_add_u32 s83, s52, 57344
Ta 136,22,10272
v_lshrrev_b32 v15, 8, v1
Mvav 240,148,56
buffer_load_dwordx4 a[196:199], v1, s[8:11], s81 offen offset:3072 sc1
Ta 138,22,15392
Mvav 248,148,88
Ta 140,22,20512
v_lshl_add_u32 v14, v15, 7, v14
Mvav 248,144,80
W 7
Ta 142,22,25632
Ta 144,21,10304
Mvav 252,152,80
Ta 146,21,15424
v_and_b32 v15, 0x70, v1
Mvav 252,156,88
buffer_load_dwordx4 a[212:215], v1, s[8:11], s82 offen offset:3072 sc1
Ta 148,21,20544
Mvav 244,156,56
Ta 150,21,25664
Mvav 244,152,48
s_waitcnt vmcnt(25) lgkmcnt(8)
Ta 152,22,10336
v_add_u32 v14, v14, v15
Mvaa 176,128,64
Ta 154,22,15456
Mvaa 176,132,68
s_bitcmp1_b32 s14, 0
s_cselect_b32 s74, s73, s72
s_cselect_b32 s77, s76, s75
buffer_store_dwordx4 v[136:139], v1, s[8:11], s83 offen sc1
K 144,144,145
Mvaa 184,132,84
K 145,146,147
K 146,148,149
Mvaa 184,128,80
W 6
Ta 156,22,20576
Ta 158,22,25696
Mvaa 188,136,80
s_add_u32 s74, s74, 256
Ta 128,21,10368
K 147,150,151
Mvaa 188,140,84
s_add_u32 s83, s52, 24576
Ta 130,21,15488
Ta 132,21,20608
Mvaa 180,140,68
s_add_u32 s77, s77, 2048
Ta 134,21,25728
buffer_store_dwordx4 v[144:147], v1, s[8:11], s83 offen sc1
Mvaa 180,136,64
W 8
Ta 136,22,10400
Ta 138,22,15520
Mvaa 192,144,64
K 152,152,153
Mvaa 192,148,68
s_lshl_b32 s66, s14, 6
Ta 140,22,20640
Ta 142,22,25760
Mvaa 200,148,84
Mvaa 200,144,80
W 8
Ta 144,21,10432
K 153,154,155
Mvaa 204,152,80
s_lshl_b32 s67, s14, 9
Ta 146,21,15552
Mvaa 204,156,84
Ta 148,21,20672
K 154,156,157
Mvaa 196,156,68
s_add_u32 s72, s72, s66
Ta 150,21,25792
Mvaa 196,152,64
W 8
Ta 152,22,10464
Ta 154,22,15584
Mvaa 208,128,64
K 155,158,159
Mvaa 208,132,68
s_add_u32 s75, s75, s67
Ta 156,22,20704
Ta 158,22,25824
Mvaa 216,132,84
s_add_u32 s83, s52, 65536
Mvaa 216,128,80
W 8
s_add_u32 s73, s73, s66
Ta 128,21,10496
Mvaa 220,136,80
Ta 130,21,15616
buffer_store_dwordx4 v[152:155], v1, s[8:11], s83 offen sc1
Mvaa 220,140,84
Ta 132,21,20736
Ta 134,21,25856
Mvaa 212,140,68
s_add_u32 s76, s76, s67
K 160,160,161
Mvaa 212,136,64
W 8
Ta 136,22,10528
Ta 138,22,15648
Mvaa 224,144,64
buffer_load_dwordx4 v[36:39], v20, s[16:19], s72 offen
Mvaa 224,148,68
Ta 140,22,20768
K 161,162,163
Mvaa 232,148,84
Ta 142,22,25888
Mvaa 232,144,80
W 8
Ta 144,21,30720
K 162,164,165
Mvaa 236,152,80
Ta 146,21,35840
Mvaa 236,156,84
Ta 148,23,0
Ta 150,23,5120
Mvaa 228,156,68
K 163,166,167
Mvaa 228,152,64
W 8
buffer_load_dwordx4 v[44:47], v14, s[8:11], s75 offen sc1
Ta 152,22,30752
Mvaa 240,128,64
s_add_u32 s83, s52, 32768
Ta 154,22,35872
Mvaa 240,132,68
Ta 156,24,32
Ta 158,24,5152
Mvaa 248,132,84
Mvaa 248,128,80
W 8
buffer_store_dwordx4 v[160:163], v1, s[8:11], s83 offen sc1
Ta 128,21,30784
Mvaa 252,136,80
Ta 130,21,35904
K 168,168,169
Mvaa 252,140,84
Ta 132,23,64
Ta 134,23,5184
Mvaa 244,140,68
Mvaa 244,136,64
s_waitcnt vmcnt(27) lgkmcnt(8)
buffer_load_dwordx4 v[52:55], v20, s[16:19], s73 offen
K 169,170,171
Mvaa 176,144,72
Ta 136,22,30816
Ta 138,22,35936
Mvaa 176,148,76
Ta 140,24,96
K 170,172,173
Mvaa 184,148,92
Ta 142,24,5216
Mvaa 184,144,88
W 8
Ta 144,21,30848
Ta 146,21,35968
Mvaa 188,152,88
buffer_load_dwordx4 v[60:63], v14, s[8:11], s76 offen sc1
K 171,174,175
Mvaa 188,156,92
Ta 148,23,128
Ta 150,23,5248
Mvaa 180,156,76
s_add_u32 s83, s52, 73728
Mvaa 180,152,72
W 8
Ta 152,22,30880
Ta 154,22,36000
Mvaa 192,128,72
buffer_store_dwordx4 v[168:171], v1, s[8:11], s83 offen sc1
Mvaa 192,132,76
Ta 156,24,160
Ta 158,24,5280
Mvaa 200,132,92
Mvaa 200,128,88
W 8
Ta 128,21,30912
Ta 130,21,36032
Mvaa 204,136,88
buffer_load_dwordx4 v[68:71], v20, s[16:19], s74 offen
Mvaa 204,140,92
Ta 132,23,192
Ta 134,23,5312
Mvaa 196,140,76
Mvaa 196,136,72
W 8
Ta 136,22,30944
Ta 138,22,36064
Mvaa 208,144,72
Mvaa 208,148,76
Ta 140,24,224
Ta 142,24,5344
Mvaa 216,148,92
buffer_load_dwordx4 v[76:79], v14, s[8:11], s77 offen sc1
Mvaa 216,144,88
W 8
Ta 144,21,30976
Ta 146,21,36096
Mvaa 220,152,88
Mvaa 220,156,92
Ta 148,23,256
Ta 150,23,5376
Mvaa 212,156,76
Mvaa 212,152,72
W 8
Ta 152,22,31008
Ta 154,22,36128
Mvaa 224,128,72
buffer_load_dwordx2 v[12:13], v27, s[8:11], 0 offen offset:3584 sc1
Mvaa 224,132,76
Ta 156,24,288
Ta 158,24,5408
Mvaa 232,132,92
Mvaa 232,128,88
W 8
Ta 128,23,20480
Ta 130,23,25600
Mvaa 236,136,88
Mvaa 236,140,92
Ta 132,23,51200
Ta 134,23,56320
Mvaa 228,140,76
Mvaa 228,136,72
W 8
Ta 136,25,0
Ta 138,25,5120
Mvaa 240,144,72
Mvaa 240,148,76
Ta 140,21,40960
Ta 142,21,46080
Mvaa 248,148,92
Mvaa 248,144,88
W 8
Ta 144,21,51200
Ta 146,21,56320
Mvaa 252,152,88
Mvaa 252,156,92
Ta 148,23,10240
Ta 150,23,15360
Mvaa 244,156,76
Mvaa 244,152,72
s_waitcnt vmcnt(28) lgkmcnt(8)
Ta 152,23,30720
Ta 154,23,35840
Mvaa 176,128,96
Mvaa 176,132,100
Ta 156,23,40960
Ta 158,23,46080
Mvaa 184,132,116
Mvaa 184,128,112
s_waitcnt vmcnt(22) lgkmcnt(8)
Ta 128,24,20512
Ta 130,24,25632
Mvaa 184,136,120
Mvaa 184,140,124
Ta 132,24,51232
)"
R"(Ta 134,24,56352
Mvaa 176,140,108
Mvaa 176,136,104
s_waitcnt vmcnt(17) lgkmcnt(8)
Ta 136,26,32
Ta 138,26,5152
Mvaa 176,144,184
Mvaa 176,148,188
Ta 140,22,40992
Ta 142,22,46112
Mvaa 184,148,204
Mvaa 184,144,200
s_waitcnt vmcnt(12) lgkmcnt(8)
Ta 144,22,51232
Ta 146,22,56352
Mvaa 184,152,208
Mvaa 184,156,212
Ta 148,24,10272
Ta 150,24,15392
Mvaa 176,156,196
Mvaa 176,152,192
W 8
Ta 152,24,30752
Ta 154,24,35872
Mvaa 180,128,96
Mvaa 180,132,100
Ta 156,24,40992
Ta 158,24,46112
Mvaa 188,132,116
Mvaa 188,128,112
W 8
Ta 128,23,20544
Ta 130,23,25664
Mvaa 188,136,120
Mvaa 188,140,124
Ta 132,23,51264
Ta 134,23,56384
Mvaa 180,140,108
Mvaa 180,136,104
W 8
Ta 136,25,64
Ta 138,25,5184
Mvaa 180,144,184
Mvaa 180,148,188
Ta 140,21,41024
Ta 142,21,46144
Mvaa 188,148,204
Mvaa 188,144,200
W 8
Ta 144,21,51264
Ta 146,21,56384
Mvaa 188,152,208
Mvaa 188,156,212
Ta 148,23,10304
Ta 150,23,15424
Mvaa 180,156,196
Mvaa 180,152,192
W 8
Ta 152,23,30784
Ta 154,23,35904
Mvaa 192,128,96
Mvaa 192,132,100
Ta 156,23,41024
Ta 158,23,46144
Mvaa 200,132,116
K 176,176,177
Mvaa 200,128,112
W 8
Ta 128,24,20576
Ta 130,24,25696
Mvaa 200,136,120
K 177,178,179
Mvaa 200,140,124
Ta 132,24,51296
K 178,180,181
Mvaa 192,140,108
Ta 134,24,56416
K 179,182,183
Mvaa 192,136,104
W 8
Ta 136,26,96
Ta 138,26,5216
Mvaa 192,144,184
s_add_u32 s83, s52, 327680
Mvaa 192,148,188
buffer_store_dwordx4 v[176:179], v1, s[8:11], s83 offen sc1
Ta 140,22,41056
Mvaa 200,148,204
Ta 142,22,46176
Mvaa 200,144,200
s_waitcnt vmcnt(4)
v_lshlrev_b32 v2, 2, v1
v_mov_b64 v[28:29], s[12:13]
s_mov_b64 exec, 3
buffer_store_dwordx2 v[28:29], v2, s[8:11], s53 offen offset:0 sc1
s_mov_b64 exec, -1
K 184,184,185
W 8
Mvaa 200,152,208
K 185,186,187
K 186,188,189
Mvaa 200,156,212
s_waitcnt vmcnt(2)
v_xor_b32 v12, s12, v12
v_xor_b32 v13, s13, v13
v_lshrrev_b32 v3, 11, v12
v_or_b32 v13, v13, v3
v_cmp_eq_u32 vcc, 0, v13
s_cmp_eq_u64 vcc, -1
s_cbranch_scc0 .LL_86%=
.LL_87%=:
v_bfe_u32 v13, v12, 4, 7
s_xor_b32 s66, s61, 124
v_cmp_eq_u32 vcc, s61, v13
v_cmp_eq_u32_e64 s[86:87], s66, v13
s_or_b32 s68, vcc_lo, s86
s_and_b32 s68, s68, 0xff
s_cbranch_scc1 .Lup_84%=
.Lupr_85%=:
Ta 144,22,51296
K 187,190,191
Mvaa 192,156,196
s_add_u32 s83, s52, 368640
buffer_store_dwordx4 v[184:187], v1, s[8:11], s83 offen sc1
Ta 146,22,56416
Mvaa 192,152,192
W 6
Ta 148,24,10336
Ta 150,24,15456
Mvaa 196,128,96
Ta 152,24,30816
v_lshlrev_b32 v32, 16, v36
Mvaa 196,132,100
Ta 154,24,35936
v_and_b32 v33, 0xffff0000, v36
Mvaa 204,132,116
Ta 156,24,41056
v_lshlrev_b32 v34, 16, v37
Mvaa 204,128,112
W 7
Ta 158,24,46176
v_and_b32 v35, 0xffff0000, v37
Mvaa 204,136,120
Ta 128,23,20608
v_lshlrev_b32 v36, 16, v38
Mvaa 204,140,124
Ta 130,23,25728
v_and_b32 v37, 0xffff0000, v38
Mvaa 196,140,108
Ta 132,23,51328
v_lshlrev_b32 v38, 16, v39
Mvaa 196,136,104
W 7
Ta 134,23,56448
v_and_b32 v39, 0xffff0000, v39
Mvaa 196,144,184
Ta 136,25,128
v_lshlrev_b32 v40, 16, v44
Mvaa 196,148,188
Ta 138,25,5248
v_and_b32 v41, 0xffff0000, v44
Mvaa 204,148,204
Ta 140,21,41088
v_lshlrev_b32 v42, 16, v45
Mvaa 204,144,200
W 7
Ta 142,21,46208
v_and_b32 v43, 0xffff0000, v45
Mvaa 204,152,208
Ta 144,21,51328
v_lshlrev_b32 v44, 16, v46
Mvaa 204,156,212
Ta 146,21,56448
v_and_b32 v45, 0xffff0000, v46
Mvaa 196,156,196
Ta 148,23,10368
v_lshlrev_b32 v46, 16, v47
Mvaa 196,152,192
W 7
Ta 150,23,15488
v_and_b32 v47, 0xffff0000, v47
Mvaa 208,128,96
Ta 152,23,30848
v_lshlrev_b32 v48, 16, v52
Mvaa 208,132,100
Ta 154,23,35968
v_and_b32 v49, 0xffff0000, v52
Mvaa 216,132,116
Ta 156,23,41088
v_lshlrev_b32 v50, 16, v53
Mvaa 216,128,112
W 7
Ta 158,23,46208
v_and_b32 v51, 0xffff0000, v53
Mvaa 216,136,120
K 192,192,193
v_lshlrev_b32 v52, 16, v54
Mvaa 216,140,124
Ta 128,24,20640
Ta 130,24,25760
Mvaa 208,140,108
K 193,194,195
v_and_b32 v53, 0xffff0000, v54
Mvaa 208,136,104
W 6
v_lshlrev_b32 v54, 16, v55
K 194,196,197
Mvaa 208,144,184
Ta 132,24,51360
Ta 134,24,56480
Mvaa 208,148,188
v_and_b32 v55, 0xffff0000, v55
K 195,198,199
Mvaa 216,148,204
s_add_u32 s83, s52, 335872
Ta 136,26,160
v_lshlrev_b32 v56, 16, v60
Mvaa 216,144,200
W 5
Ta 138,26,5280
v_and_b32 v57, 0xffff0000, v60
Mvaa 216,152,208
buffer_store_dwordx4 v[192:195], v1, s[8:11], s83 offen sc1
v_lshlrev_b32 v58, 16, v61
Mvaa 216,156,212
K 200,200,201
K 201,202,203
Mvaa 208,156,196
K 202,204,205
K 203,206,207
s_add_u32 s83, s52, 376832
buffer_store_dwordx4 v[200:203], v1, s[8:11], s83 offen sc1
Mvaa 208,152,192
Ta 140,22,41120
Ta 142,22,46240
W 4
Mvaa 212,128,96
v_and_b32 v59, 0xffff0000, v61
v_lshlrev_b32 v60, 16, v62
Mvaa 212,132,100
Ta 144,22,51360
Ta 146,22,56480
Mvaa 220,132,116
v_and_b32 v61, 0xffff0000, v62
v_lshlrev_b32 v62, 16, v63
Mvaa 220,128,112
Ta 148,24,10400
Ta 150,24,15520
W 4
Mvaa 220,136,120
v_and_b32 v63, 0xffff0000, v63
v_lshlrev_b32 v64, 16, v68
Mvaa 220,140,124
Ta 152,24,30880
Ta 154,24,36000
Mvaa 212,140,108
v_and_b32 v65, 0xffff0000, v68
v_lshlrev_b32 v66, 16, v69
Mvaa 212,136,104
Ta 156,24,41120
Ta 158,24,46240
W 4
Mvaa 212,144,184
v_and_b32 v67, 0xffff0000, v69
v_lshlrev_b32 v68, 16, v70
Mvaa 212,148,188
Ta 128,23,20672
Ta 130,23,25792
Mvaa 220,148,204
v_and_b32 v69, 0xffff0000, v70
v_lshlrev_b32 v70, 16, v71
Mvaa 220,144,200
Ta 132,23,51392
Ta 134,23,56512
W 4
Mvaa 220,152,208
v_and_b32 v71, 0xffff0000, v71
v_lshlrev_b32 v72, 16, v76
Mvaa 220,156,212
Ta 136,25,192
Ta 138,25,5312
Mvaa 212,156,196
v_and_b32 v73, 0xffff0000, v76
v_lshlrev_b32 v74, 16, v77
Mvaa 212,152,192
Ta 140,21,41152
Ta 142,21,46272
W 4
Mvaa 224,128,96
v_and_b32 v75, 0xffff0000, v77
v_lshlrev_b32 v76, 16, v78
Mvaa 224,132,100
Ta 144,21,51392
Ta 146,21,56512
Mvaa 232,132,116
v_and_b32 v77, 0xffff0000, v78
v_lshlrev_b32 v78, 16, v79
Mvaa 232,128,112
Ta 148,23,10432
Ta 150,23,15552
W 4
Mvaa 232,136,120
Ta 152,23,30912
v_and_b32 v79, 0xffff0000, v79
Mvaa 232,140,124
Ta 154,23,36032
Ta 156,23,41152
Mvaa 224,140,108
Ta 158,23,46272
K 208,208,209
Mvaa 224,136,104
W 4
Ta 128,24,20704
Ta 130,24,25824
Mvaa 224,144,184
K 209,210,211
K 210,212,213
Mvaa 224,148,188
Ta 132,24,51424
Ta 134,24,56544
Mvaa 232,148,204
Ta 136,26,224
K 211,214,215
Mvaa 232,144,200
W 5
s_add_u32 s83, s52, 344064
Ta 138,26,5344
Mvaa 232,152,208
buffer_store_dwordx4 v[208:211], v1, s[8:11], s83 offen sc1
K 216,216,217
Mvaa 232,156,212
K 217,218,219
K 218,220,221
Mvaa 224,156,196
K 219,222,223
s_add_u32 s83, s52, 385024
buffer_store_dwordx4 v[216:219], v1, s[8:11], s83 offen sc1
Mvaa 224,152,192
Ta 140,22,41184
Ta 142,22,46304
W 4
Mvaa 228,128,96
Ta 144,22,51424
Ta 146,22,56544
Mvaa 228,132,100
Ta 148,24,10464
Ta 150,24,15584
Mvaa 236,132,116
Ta 152,24,30944
Ta 154,24,36064
Mvaa 236,128,112
W 6
Ta 156,24,41184
Ta 158,24,46304
Mvaa 236,136,120
Ta 128,23,20736
Ta 130,23,25856
Mvaa 236,140,124
Ta 132,23,51456
Ta 134,23,56576
Mvaa 228,140,108
Mvaa 228,136,104
W 8
Ta 136,25,256
Ta 138,25,5376
Mvaa 228,144,184
Mvaa 228,148,188
Ta 140,21,41216
Ta 142,21,46336
Mvaa 236,148,204
Mvaa 236,144,200
W 8
Ta 144,21,51456
Ta 146,21,56576
Mvaa 236,152,208
Mvaa 236,156,212
Ta 148,23,10496
Ta 150,23,15616
Mvaa 228,156,196
Mvaa 228,152,192
W 8
Ta 152,23,30976
Ta 154,23,36096
Mvaa 240,128,96
Mvaa 240,132,100
Ta 156,23,41216
Ta 158,23,46336
Mvaa 248,132,116
K 224,224,225
Mvaa 248,128,112
W 8
Ta 128,24,20768
Ta 130,24,25888
Mvaa 248,136,120
K 225,226,227
Mvaa 248,140,124
Ta 132,24,51488
K 226,228,229
Mvaa 240,140,108
Ta 134,24,56608
)"
R"(K 227,230,231
Mvaa 240,136,104
W 8
Ta 136,26,288
Ta 138,26,5408
Mvaa 240,144,184
s_add_u32 s83, s52, 352256
Mvaa 240,148,188
buffer_store_dwordx4 v[224:227], v1, s[8:11], s83 offen sc1
K 232,232,233
Mvaa 248,148,204
K 233,234,235
K 234,236,237
Mvaa 248,144,200
W 6
Ta 140,22,41248
K 235,238,239
Mvaa 248,152,208
s_add_u32 s83, s52, 393216
Ta 142,22,46368
Ta 144,22,51488
Mvaa 248,156,212
buffer_store_dwordx4 v[232:235], v1, s[8:11], s83 offen sc1
Ta 146,22,56608
Mvaa 240,156,196
Ta 148,24,10528
Ta 150,24,15648
Mvaa 240,152,192
W 8
Ta 152,24,31008
Ta 154,24,36128
Mvaa 244,128,96
Mvaa 244,132,100
Ta 156,24,41248
Ta 158,24,46368
Mvaa 252,132,116
Mvaa 252,128,112
W 8
Mvaa 252,136,120
Mvaa 252,140,124
Mvaa 244,140,108
Mvaa 244,136,104
W 4
Mvaa 244,144,184
Mvaa 244,148,188
Mvaa 252,148,204
Mvaa 252,144,200
W 0
Mvaa 252,152,208
Mvaa 252,156,212
Mvaa 244,156,196
Mvaa 244,152,192
K 240,240,241
K 241,242,243
s_nop 5
K 242,244,245
K 243,246,247
s_add_u32 s83, s52, 360448
buffer_store_dwordx4 v[240:243], v1, s[8:11], s83 offen sc1
K 248,248,249
K 249,250,251
K 250,252,253
K 251,254,255
s_add_u32 s83, s52, 401408
buffer_store_dwordx4 v[248:251], v1, s[8:11], s83 offen sc1
s_branch .Ltend_80%=
.Llate_79%=:
ds_read_b32 v11, v11 offset:59392
ds_read_b64 v[8:9], v10 offset:57344
Rv 176,3,0
Rv 180,3,1024
Rv 184,3,2048
Rv 188,3,3072
Rv 192,3,4096
Rv 196,3,5120
Rv 200,3,6144
Rv 204,3,7168
W 6
Mvvv 32,224,176
s_lshl_b32 s92, s58, 2
s_add_u32 s91, s92, s51
s_add_u32 s95, s91, 7
Mvvv 36,228,176
s_and_b32 s95, s95, 15
s_mul_i32 s95, s95, 40960
s_mul_i32 s71, s51, 20480
Mvvv 40,224,180
s_add_u32 s71, s71, 61440
s_add_u32 m0, s71, s48
s_lshl2_add_u32 s94, s48, s95
Mvvv 44,228,180
W 4
v_cmp_ne_u32 vcc, 0, v11
s_cmp_lg_u64 vcc, 0
s_cbranch_scc1 .Lredo%=
A 4,4,8
Mvvv 48,224,184
v_rcp_f32 v4, v4
Mvvv 52,228,184
Ta 128,25,10240
A 5,5,9
Mvvv 56,224,188
Ta 130,25,15360
D 16,40,94,0
Mvvv 60,228,188
W 4
s_add_u32 s95, s91, 9
s_and_b32 s95, s95, 15
Mvvv 64,224,192
s_mul_i32 s95, s95, 40960
s_mul_i32 s71, s51, 10240
s_add_u32 s71, s71, 112640
Mvvv 68,228,192
v_rcp_f32 v5, v5
Mvvv 72,224,196
s_lshl_b32 s93, s58, 12
s_add_u32 s93, s93, s50
Ta 132,25,20480
Mvvv 76,228,196
Ta 134,25,25600
W 4
Mvvv 80,224,200
U 32,32,4
U 33,33,4
Mvvv 84,228,200
Ta 136,26,10272
U 34,34,4
Mvvv 88,224,204
U 35,35,4
U 36,36,4
U 37,37,4
U 38,38,4
U 39,39,4
K 32,32,33
K 33,34,35
K 34,36,37
K 35,38,39
buffer_store_dwordx4 v[32:35], v1, s[8:11], s93 offen offset:0 sc1
U 40,40,4
U 41,41,4
U 42,42,4
U 43,43,4
U 44,44,4
U 45,45,4
U 46,46,4
U 47,47,4
K 40,40,41
K 41,42,43
K 42,44,45
K 43,46,47
buffer_store_dwordx4 v[40:43], v1, s[8:11], s93 offen offset:1024 sc1
U 64,64,5
U 65,65,5
U 66,66,5
U 67,67,5
U 68,68,5
U 69,69,5
U 70,70,5
U 71,71,5
K 64,64,65
K 65,66,67
K 66,68,69
K 67,70,71
s_add_u32 s67, s93, 131072
buffer_store_dwordx4 v[64:67], v1, s[8:11], s67 offen offset:0 sc1
U 72,72,5
U 73,73,5
U 74,74,5
U 75,75,5
U 76,76,5
U 77,77,5
U 78,78,5
U 79,79,5
K 72,72,73
K 73,74,75
K 74,76,77
K 75,78,79
Mvvv 92,228,204
Ta 138,26,15392
Ta 140,26,20512
Ta 142,26,25632
W 4
Zvav 176,128,32
Ta 144,25,10304
Ta 146,25,15424
Mvav 176,132,40
Ta 148,25,20544
Ta 150,25,25664
Zvav 184,132,72
D 17,40,94,1024
Ta 152,26,10336
Mvav 184,128,64
W 5
Ta 154,26,15456
Ta 156,26,20576
Zvav 188,136,64
Ta 158,26,25696
Ta 128,25,10368
Mvav 188,140,72
Ta 130,25,15488
Ta 132,25,20608
Zvav 180,140,40
D 18,40,94,2048
Ta 134,25,25728
Mvav 180,136,32
W 8
Ta 136,26,10400
Ta 138,26,15520
Zvav 192,144,32
Mvav 192,148,40
Ta 140,26,20640
Ta 142,26,25760
Zvav 200,148,72
D 19,40,94,3072
Mvav 200,144,64
W 8
s_add_u32 m0, m0, 4096
Ta 144,25,10432
Zvav 204,152,64
Ta 146,25,15552
Mvav 204,156,72
Ta 148,25,20672
Ta 150,25,25792
Zvav 196,156,40
buffer_load_dwordx4 v20, s[40:43], s94 offen lds
Mvav 196,152,32
W 8
s_add_u32 m0, s71, s48
s_lshl2_add_u32 s94, s48, s95
Zvav 208,128,32
Ta 152,26,10464
Ta 154,26,15584
Mvav 208,132,40
Ta 156,26,20704
Ta 158,26,25824
Zvav 216,132,72
D 16,40,94,0
v_bfe_u32 v28, v1, 4, 2
Mvav 216,128,64
W 8
Ta 128,25,10496
v_lshlrev_b32 v28, 3, v28
Zvav 220,136,64
Ta 130,25,15616
Mvav 220,140,72
Ta 132,25,20736
Ta 134,25,25856
Zvav 212,140,40
buffer_load_dwordx2 v[28:29], v28, s[8:11], s54 offen sc1
Mvav 212,136,32
W 8
Ta 136,26,10528
Ta 138,26,15648
Zvav 224,144,32
Mvav 224,148,40
Ta 140,26,20768
Ta 142,26,25888
Zvav 232,148,72
D 17,40,94,1024
Mvav 232,144,64
W 8
Ta 144,25,30720
Ta 146,25,35840
Zvav 236,152,64
Mvav 236,156,72
Ta 148,21,0
Ta 150,21,5120
Zvav 228,156,40
D 18,40,94,2048
Mvav 228,152,32
W 8
Ta 152,26,30752
Ta 154,26,35872
Zvav 240,128,32
Mvav 240,132,40
Ta 156,22,32
Ta 158,22,5152
Zvav 248,132,72
D 19,40,94,3072
Mvav 248,128,64
W 8
s_add_u32 m0, m0, 4096
Ta 128,25,30784
Zvav 252,136,64
Ta 130,25,35904
Mvav 252,140,72
Ta 132,21,64
Ta 134,21,5184
Zvav 244,140,40
buffer_store_dwordx4 v[72:75], v1, s[8:11], s67 offen offset:1024 sc1
U 48,48,4
U 49,49,4
U 50,50,4
U 51,51,4
U 52,52,4
U 53,53,4
U 54,54,4
U 55,55,4
K 48,48,49
K 49,50,51
K 50,52,53
K 51,54,55
buffer_store_dwordx4 v[48:51], v1, s[8:11], s93 offen offset:2048 sc1
U 56,56,4
U 57,57,4
U 58,58,4
U 59,59,4
U 60,60,4
U 61,61,4
U 62,62,4
U 63,63,4
K 56,56,57
K 57,58,59
K 58,60,61
K 59,62,63
buffer_store_dwordx4 v[56:59], v1, s[8:11], s93 offen offset:3072 sc1
U 80,80,5
U 81,81,5
U 82,82,5
U 83,83,5
U 84,84,5
U 85,85,5
U 86,86,5
U 87,87,5
K 80,80,81
K 81,82,83
K 82,84,85
K 83,86,87
buffer_store_dwordx4 v[80:83], v1, s[8:11], s67 offen offset:2048 sc1
U 88,88,5
U 89,89,5
U 90,90,5
U 91,91,5
U 92,92,5
U 93,93,5
U 94,94,5
U 95,95,5
K 88,88,89
K 89,90,91
K 90,92,93
K 91,94,95
Mvav 244,136,32
W 8
Ta 136,26,30816
Ta 138,26,35936
Mvav 176,144,48
Mvav 176,148,56
Ta 140,22,96
Ta 142,22,5216
Mvav 184,148,88
buffer_load_dwordx4 v20, s[40:43], s94 offen lds
Mvav 184,144,80
s_waitcnt vmcnt(8)
v_cmp_eq_u64 vcc, s[12:13], v[28:29]
s_cmp_eq_u64 vcc, -1
s_cbranch_scc0 .Lspin_89%=
.Lok_90%=:
s_add_u32 s81, s58, 0
W 8
Mvav 188,152,80
s_and_b32 s81, s81, 3
s_lshl_b32 s81, s81, 12
s_add_u32 s81, s81, s49
Mvav 188,156,88
Ta 144,25,30848
Ta 146,25,35968
Mvav 180,156,56
buffer_load_dwordx4 a[184:187], v1, s[8:11], s81 offen offset:0 sc1
Ta 148,21,128
Mvav 180,152,48
W 7
s_add_u32 s82, s81, 131072
Ta 150,21,5248
Mvav 192,128,48
Ta 152,26,30880
Ta 154,26,36000
Mvav 192,132,56
Ta 156,22,160
Ta 158,22,5280
Mvav 200,132,88
buffer_load_dwordx4 a[200:203], v1, s[8:11], s82 offen offset:0 sc1
Mvav 200,128,80
W 8
Ta 128,25,30912
Ta 130,25,36032
Mvav 204,136,80
Mvav 204,140,88
Ta 132,21,192
Ta 134,21,5312
Mvav 196,140,56
buffer_load_dwordx4 a[188:191], v1, s[8:11], s81 offen offset:1024 sc1
Mvav 196,136,48
W 8
Ta 136,26,30944
Ta 138,26,36064
Mvav 208,144,48
Mvav 208,148,56
Ta 140,22,224
Ta 142,22,5344
Mvav 216,148,88
)"
R"(buffer_load_dwordx4 a[204:207], v1, s[8:11], s82 offen offset:1024 sc1
Mvav 216,144,80
W 8
Ta 144,25,30976
Ta 146,25,36096
Mvav 220,152,80
Mvav 220,156,88
Ta 148,21,256
Ta 150,21,5376
Mvav 212,156,56
buffer_load_dwordx4 a[192:195], v1, s[8:11], s81 offen offset:2048 sc1
Mvav 212,152,48
W 8
Ta 152,26,31008
Ta 154,26,36128
Mvav 224,128,48
Mvav 224,132,56
Ta 156,22,288
Ta 158,22,5408
Mvav 232,132,88
buffer_store_dwordx4 v[88:91], v1, s[8:11], s67 offen offset:3072 sc1
Mvav 232,128,80
W 8
Ta 128,25,10240
Ta 130,25,15360
Mvav 236,136,80
Mvav 236,140,88
Ta 132,25,20480
Ta 134,25,25600
Mvav 228,140,56
buffer_load_dwordx4 a[208:211], v1, s[8:11], s82 offen offset:2048 sc1
Mvav 228,136,48
W 8
Ta 136,26,10272
Ta 138,26,15392
Mvav 240,144,48
Mvav 240,148,56
Ta 140,26,20512
Ta 142,26,25632
Mvav 248,148,88
buffer_load_dwordx4 a[196:199], v1, s[8:11], s81 offen offset:3072 sc1
Mvav 248,144,80
W 8
Ta 144,25,10304
Ta 146,25,15424
Mvav 252,152,80
Mvav 252,156,88
Ta 148,25,20544
Ta 150,25,25664
Mvav 244,156,56
buffer_load_dwordx4 a[212:215], v1, s[8:11], s82 offen offset:3072 sc1
Mvav 244,152,48
s_waitcnt vmcnt(5) lgkmcnt(8)
Ta 152,26,10336
Ta 154,26,15456
Zvaa 96,128,184
Mvaa 96,132,188
Ta 156,26,20576
Ta 158,26,25696
Zvaa 104,132,204
Mvaa 104,128,200
W 8
Ta 128,25,10368
Ta 130,25,15488
Zvaa 108,136,200
Mvaa 108,140,204
Ta 132,25,20608
Ta 134,25,25728
Zvaa 100,140,188
s_lshl_b32 s92, s58, 2
s_add_u32 s91, s92, s51
s_add_u32 s95, s91, 11
Mvaa 100,136,184
W 8
s_and_b32 s95, s95, 15
s_mul_i32 s95, s95, 40960
Zvaa 112,144,184
s_mul_i32 s71, s51, 10240
s_add_u32 s71, s71, 40960
s_add_u32 m0, s71, s48
Mvaa 112,148,188
s_lshl2_add_u32 s94, s48, s95
Ta 136,26,10400
Ta 138,26,15520
Zvaa 120,148,204
Ta 140,26,20640
Ta 142,26,25760
Mvaa 120,144,200
W 8
Ta 144,25,10432
Ta 146,25,15552
Zvaa 124,152,200
Mvaa 124,156,204
Ta 148,25,20672
Ta 150,25,25792
Zvaa 116,156,188
Mvaa 116,152,184
W 8
Ta 152,26,10464
Ta 154,26,15584
Zvaa 128,128,184
s_lshl_b32 s80, s58, 3
s_add_u32 s80, s80, s55
s_waitcnt vmcnt(3)
v_mov_b64 v[28:29], s[12:13]
s_mov_b64 exec, 1
buffer_store_dwordx2 v[28:29], off, s[8:11], s80 sc1
s_mov_b64 exec, -1
Mvaa 128,132,188
Ta 156,26,20704
Ta 158,26,25824
Zvaa 136,132,204
Mvaa 136,128,200
W 8
Ta 128,25,10496
Ta 130,25,15616
Zvaa 140,136,200
s_barrier
D 16,40,94,0
Mvaa 140,140,204
s_add_u32 s95, s91, 13
s_and_b32 s95, s95, 15
s_mul_i32 s95, s95, 40960
Zvaa 132,140,188
s_mul_i32 s71, s51, 20480
s_add_u32 s71, s71, 71680
Ta 132,25,20736
Mvaa 132,136,184
W 7
Ta 134,25,25856
Ta 136,26,10528
Zvaa 144,144,184
Ta 138,26,15648
D 17,40,94,1024
Mvaa 144,148,188
Ta 140,26,20768
Ta 142,26,25888
Zvaa 152,148,204
Mvaa 152,144,200
W 8
Ta 144,25,30720
Ta 146,25,35840
Zvaa 156,152,200
D 18,40,94,2048
Mvaa 156,156,204
Ta 148,21,0
Ta 150,21,5120
Zvaa 148,156,188
Mvaa 148,152,184
W 8
Ta 152,26,30752
Ta 154,26,35872
Zvaa 160,128,184
D 19,40,94,3072
Mvaa 160,132,188
s_add_u32 m0, m0, 4096
Ta 156,22,32
Ta 158,22,5152
Zvaa 168,132,204
Mvaa 168,128,200
W 8
Ta 128,25,30784
Ta 130,25,35904
Zvaa 172,136,200
buffer_load_dwordx4 v20, s[40:43], s94 offen lds
Mvaa 172,140,204
s_add_u32 s81, s58, 1
s_and_b32 s81, s81, 3
s_lshl_b32 s81, s81, 12
Zvaa 164,140,188
s_add_u32 s81, s81, s49
Ta 132,21,64
Ta 134,21,5184
Mvaa 164,136,184
s_waitcnt vmcnt(6) lgkmcnt(8)
Ta 136,26,30816
Ta 138,26,35936
Mvaa 96,144,192
buffer_load_dwordx4 a[216:219], v1, s[8:11], s81 offen offset:0 sc1
Mvaa 96,148,196
s_add_u32 s82, s81, 131072
Ta 140,22,96
Ta 142,22,5216
Mvaa 104,148,212
Mvaa 104,144,208
W 8
Ta 144,25,30848
Ta 146,25,35968
Mvaa 108,152,208
buffer_load_dwordx4 a[232:235], v1, s[8:11], s82 offen offset:0 sc1
Mvaa 108,156,212
Ta 148,21,128
Ta 150,21,5248
Mvaa 100,156,196
Mvaa 100,152,192
W 8
Ta 152,26,30880
Ta 154,26,36000
Mvaa 112,128,192
buffer_load_dwordx4 a[220:223], v1, s[8:11], s81 offen offset:1024 sc1
Mvaa 112,132,196
Ta 156,22,160
Ta 158,22,5280
Mvaa 120,132,212
Mvaa 120,128,208
W 8
Ta 128,25,30912
Ta 130,25,36032
Mvaa 124,136,208
buffer_load_dwordx4 a[236:239], v1, s[8:11], s82 offen offset:1024 sc1
Mvaa 124,140,212
Ta 132,21,192
Ta 134,21,5312
Mvaa 116,140,196
Mvaa 116,136,192
W 8
Ta 136,26,30944
Ta 138,26,36064
Mvaa 128,144,192
buffer_load_dwordx4 a[224:227], v1, s[8:11], s81 offen offset:2048 sc1
Mvaa 128,148,196
Ta 140,22,224
Ta 142,22,5344
Mvaa 136,148,212
Mvaa 136,144,208
W 8
Ta 144,25,30976
Ta 146,25,36096
Mvaa 140,152,208
buffer_load_dwordx4 a[240:243], v1, s[8:11], s82 offen offset:2048 sc1
Mvaa 140,156,212
Ta 148,21,256
Ta 150,21,5376
Mvaa 132,156,196
Mvaa 132,152,192
W 8
Ta 152,26,31008
Ta 154,26,36128
Mvaa 144,128,192
buffer_load_dwordx4 a[228:231], v1, s[8:11], s81 offen offset:3072 sc1
Mvaa 144,132,196
Ta 156,22,288
Ta 158,22,5408
Mvaa 152,132,212
Mvaa 152,128,208
W 8
Ta 128,21,10240
Ta 130,21,15360
Mvaa 156,136,208
buffer_load_dwordx4 a[244:247], v1, s[8:11], s82 offen offset:3072 sc1
Mvaa 156,140,212
s_add_u32 m0, s71, s48
s_lshl2_add_u32 s94, s48, s95
Ta 132,21,20480
Mvaa 148,140,196
Ta 134,21,25600
Mvaa 148,136,192
W 8
Ta 136,22,10272
Ta 138,22,15392
Mvaa 160,144,192
D 16,40,94,0
Mvaa 160,148,196
s_add_u32 s95, s92, 15
s_and_b32 s95, s95, 15
s_mul_i32 s95, s95, 40960
Mvaa 168,148,212
s_mul_i32 s71, s51, 0
s_add_u32 s71, s71, 102400
Ta 140,22,20512
Mvaa 168,144,208
W 7
Ta 142,22,25632
Ta 144,21,10304
Mvaa 172,152,208
D 17,40,94,1024
Ta 146,21,15424
Mvaa 172,156,212
Ta 148,21,20544
Ta 150,21,25664
Mvaa 164,156,196
Mvaa 164,152,192
s_waitcnt vmcnt(6) lgkmcnt(8)
Ta 152,22,10336
Ta 154,22,15456
Mvaa 96,128,216
D 18,40,94,2048
Mvaa 96,132,220
Ta 156,22,20576
Ta 158,22,25696
Mvaa 104,132,236
Mvaa 104,128,232
W 8
Ta 128,21,10368
Ta 130,21,15488
Mvaa 108,136,232
D 19,40,94,3072
Mvaa 108,140,236
s_add_u32 m0, m0, 4096
Ta 132,21,20608
Ta 134,21,25728
Mvaa 100,140,220
Mvaa 100,136,216
W 8
Ta 136,22,10400
Ta 138,22,15520
Mvaa 112,144,216
buffer_load_dwordx4 v20, s[40:43], s94 offen lds
Mvaa 112,148,220
s_add_u32 m0, s71, s48
s_lshl2_add_u32 s94, s48, s95
Ta 140,22,20640
Mvaa 120,148,236
Ta 142,22,25760
Mvaa 120,144,232
W 8
Ta 144,21,10432
Ta 146,21,15552
Mvaa 124,152,232
D 16,40,94,0
Mvaa 124,156,236
Ta 148,21,20672
Ta 150,21,25792
Mvaa 116,156,220
D 17,40,94,1024
D 18,40,94,2048
D 19,40,94,3072
s_add_u32 m0, m0, 4096
s_nop 0
buffer_load_dwordx4 v20, s[40:43], s94 offen lds
Mvaa 116,152,216
W 8
s_add_u32 s81, s58, 2
s_and_b32 s81, s81, 3
Mvaa 128,128,216
s_lshl_b32 s81, s81, 12
s_add_u32 s81, s81, s49
Ta 152,22,10464
Mvaa 128,132,220
Ta 154,22,15584
Ta 156,22,20704
Mvaa 136,132,236
Ta 158,22,25824
Mvaa 136,128,232
W 8
Ta 128,21,10496
Ta 130,21,15616
Mvaa 140,136,232
s_barrier
buffer_load_dwordx4 a[0:3], v1, s[8:11], s81 offen offset:0 sc1
v_and_b32 v27, 1, v12
Mvaa 140,140,236
s_add_u32 s82, s81, 131072
Ta 132,21,20736
Ta 134,21,25856
Mvaa 132,140,220
Mvaa 132,136,216
W 8
Ta 136,22,10528
v_lshlrev_b32 v27, 1, v27
Mvaa 144,144,216
Ta 138,22,15648
buffer_load_dwordx4 a[16:19], v1, s[8:11], s82 offen offset:0 sc1
Mvaa 144,148,220
Ta 140,22,20768
Ta 142,22,25888
Mvaa 152,148,236
v_xor_b32 v27, v27, v13
Mvaa 152,144,232
W 8
Ta 144,21,30720
Ta 146,21,35840
Mvaa 156,152,232
buffer_load_dwordx4 a[4:7], v1, s[8:11], s81 offen offset:1024 sc1
Mvaa 156,156,236
Ta 148,23,0
v_and_b32 v12, 2, v12
Mvaa 148,156,220
Ta 150,23,5120
Mvaa 148,152,216
W 8
Ta 152,22,30752
Ta 154,22,35872
Mvaa 160,128,216
buffer_load_dwordx4 a[20:23], v1, s[8:11], s82 offen offset:1024 sc1
v_or_b32 v12, v12, v14
Mvaa 160,132,220
Ta 156,24,32
Ta 158,24,5152
Mvaa 168,132,236
v_lshl_or_b32 v12, v27, 2, v12
Mvaa 168,128,232
W 8
Ta 128,21,30784
Ta 130,21,35904
Mvaa 172,136,232
buffer_load_dwordx4 a[8:11], v1, s[8:11], s81 offen offset:2048 sc1
Mvaa 172,140,236
Ta 132,23,64
v_cmp_eq_u32_e64 s[86:87], 0, v12
)"
R"(Mvaa 164,140,220
Ta 134,23,5184
Mvaa 164,136,216
s_waitcnt vmcnt(15) lgkmcnt(8)
Ta 136,22,30816
Ta 138,22,35936
Mvaa 96,144,224
buffer_load_dwordx4 a[24:27], v1, s[8:11], s82 offen offset:2048 sc1
v_cndmask_b32_e64 v4, 0, v15, s[86:87]
Mvaa 96,148,228
Ta 140,24,96
Ta 142,24,5216
Mvaa 104,148,244
Mvaa 104,144,240
W 8
Ta 144,21,30848
v_cmp_eq_u32_e64 s[86:87], 1, v12
Mvaa 108,152,240
Ta 146,21,35968
buffer_load_dwordx4 a[12:15], v1, s[8:11], s81 offen offset:3072 sc1
Mvaa 108,156,244
Ta 148,23,128
v_cndmask_b32_e64 v5, 0, v15, s[86:87]
Mvaa 100,156,228
Ta 150,23,5248
Mvaa 100,152,224
W 8
Ta 152,22,30880
Ta 154,22,36000
Mvaa 112,128,224
buffer_load_dwordx4 a[28:31], v1, s[8:11], s82 offen offset:3072 sc1
v_cmp_eq_u32_e64 s[86:87], 2, v12
Mvaa 112,132,228
s_add_u32 s81, s58, 3
s_and_b32 s81, s81, 3
s_lshl_b32 s81, s81, 12
Mvaa 120,132,244
s_add_u32 s81, s81, s49
Ta 156,24,160
Ta 158,24,5280
Mvaa 120,128,240
W 8
Ta 128,21,30912
v_cndmask_b32_e64 v6, 0, v15, s[86:87]
Mvaa 124,136,240
Ta 130,21,36032
buffer_load_dwordx4 a[32:35], v1, s[8:11], s81 offen offset:0 sc1
Mvaa 124,140,244
s_add_u32 s82, s81, 131072
Ta 132,23,192
Ta 134,23,5312
Mvaa 116,140,228
v_cmp_eq_u32_e64 s[86:87], 3, v12
Mvaa 116,136,224
W 8
Ta 136,22,30944
Ta 138,22,36064
Mvaa 128,144,224
buffer_load_dwordx4 a[48:51], v1, s[8:11], s82 offen offset:0 sc1
v_cndmask_b32_e64 v7, 0, v15, s[86:87]
Mvaa 128,148,228
Ta 140,24,224
Ta 142,24,5344
Mvaa 136,148,244
Mvaa 136,144,240
W 8
Ta 144,21,30976
v_cmp_eq_u32_e64 s[86:87], 4, v12
Mvaa 140,152,240
s_waitcnt vmcnt(10)
s_barrier
Ta 146,21,36096
Mvaa 140,156,244
buffer_load_dwordx4 a[36:39], v1, s[8:11], s81 offen offset:1024 sc1
Ta 148,23,256
Mvaa 132,156,228
Ta 150,23,5376
v_cndmask_b32_e64 v8, 0, v15, s[86:87]
Mvaa 132,152,224
W 8
Ta 152,22,31008
Ta 154,22,36128
Mvaa 144,128,224
Mvaa 144,132,228
buffer_load_dwordx4 a[52:55], v1, s[8:11], s82 offen offset:1024 sc1
v_cmp_eq_u32_e64 s[86:87], 5, v12
Mvaa 152,132,244
Ta 156,24,288
Ta 158,24,5408
Mvaa 152,128,240
W 8
Ta 128,23,20480
Ta 130,23,25600
Mvaa 156,136,240
v_cndmask_b32_e64 v9, 0, v15, s[86:87]
Mvaa 156,140,244
buffer_load_dwordx4 a[40:43], v1, s[8:11], s81 offen offset:2048 sc1
Ta 132,23,51200
Mvaa 148,140,228
Ta 134,23,56320
v_cmp_eq_u32_e64 s[86:87], 6, v12
Mvaa 148,136,224
W 8
Ta 136,24,20512
Ta 138,24,25632
Mvaa 160,144,224
Mvaa 160,148,228
buffer_load_dwordx4 a[56:59], v1, s[8:11], s82 offen offset:2048 sc1
v_cndmask_b32_e64 v10, 0, v15, s[86:87]
Mvaa 168,148,244
Ta 140,24,51232
Ta 142,24,56352
Mvaa 168,144,240
W 8
Ta 144,23,20544
Ta 146,23,25664
Mvaa 172,152,240
v_cmp_eq_u32_e64 s[86:87], 7, v12
Mvaa 172,156,244
buffer_load_dwordx4 a[44:47], v1, s[8:11], s81 offen offset:3072 sc1
Ta 148,23,51264
Mvaa 164,156,228
Ta 150,23,56384
Mvaa 164,152,224
s_waitcnt vmcnt(11) lgkmcnt(8)
Ta 152,24,20576
v_cndmask_b32_e64 v11, 0, v15, s[86:87]
Mvaa 96,128,0
Ta 154,24,25696
Mvaa 96,132,4
s_mov_b32 s20, s38
buffer_load_dwordx4 a[60:63], v1, s[8:11], s82 offen offset:3072 sc1
Ta 156,24,51296
Mvaa 104,132,20
Ta 158,24,56416
Mvaa 104,128,16
W 8
Ta 128,23,20608
Ta 130,23,25728
Mvaa 108,136,16
s_and_b32 s21, s39, 0xffff
Mvaa 108,140,20
Ta 132,23,51328
Ta 134,23,56448
Mvaa 100,140,4
Mvaa 100,136,0
W 8
s_mov_b32 s22, 2621440
Ta 136,24,20640
Mvaa 112,144,0
Ta 138,24,25760
v_bfe_u32 v28, v1, 4, 2
Mvaa 112,148,4
Ta 140,24,51360
v_lshlrev_b32 v28, 3, v28
Mvaa 120,148,20
s_mov_b32 s23, 0x20000
Ta 142,24,56480
buffer_load_dwordx2 v[28:29], v28, s[8:11], s55 offen sc1
Mvaa 120,144,16
W 8
Ta 144,23,20672
Ta 146,23,25792
Mvaa 124,152,16
s_and_b32 s17, s17, 0xffff
Mvaa 124,156,20
Ta 148,23,51392
Ta 150,23,56512
Mvaa 116,156,4
Mvaa 116,152,0
W 8
s_mov_b32 s18, 2621440
Ta 152,24,20704
Mvaa 128,128,0
Ta 154,24,25824
Mvaa 128,132,4
Ta 156,24,51424
Ta 158,24,56544
Mvaa 136,132,20
s_mov_b32 s19, 0x20000
Mvaa 136,128,16
W 8
Ta 128,23,20736
Ta 130,23,25856
Mvaa 140,136,16
Mvaa 140,140,20
s_lshr_b32 s66, s15, 3
Ta 132,23,51456
Ta 134,23,56576
Mvaa 132,140,4
Mvaa 132,136,0
W 8
s_lshl_b32 s66, s66, 4
Ta 136,24,20768
Mvaa 144,144,0
Ta 138,24,25888
Mvaa 144,148,4
Ta 140,24,51488
Ta 142,24,56608
Mvaa 152,148,20
s_add_u32 s66, s66, s59
Mvaa 152,144,16
W 8
Ta 144,25,0
Ta 146,25,5120
Mvaa 156,152,16
Mvaa 156,156,20
s_lshl_b32 s63, s66, 6
Ta 148,21,40960
Ta 150,21,46080
Mvaa 148,156,4
Mvaa 148,152,0
W 8
Ta 152,26,32
Ta 154,26,5152
Mvaa 160,128,0
s_add_u32 s63, s63, 65536
Mvaa 160,132,4
Ta 156,22,40992
Ta 158,22,46112
Mvaa 168,132,20
Mvaa 168,128,16
W 8
s_mul_i32 s62, s66, 40960
Ta 128,25,64
Mvaa 172,136,16
Ta 130,25,5184
Mvaa 172,140,20
s_add_u32 s62, s62, 56623104
Ta 132,21,41024
Ta 134,21,46144
Mvaa 164,140,4
Mvaa 164,136,0
s_waitcnt vmcnt(9) lgkmcnt(8)
Ta 136,26,96
Ta 138,26,5216
Mvaa 96,144,8
s_lshl_b32 s66, s14, 13
Mvaa 96,148,12
Ta 140,22,41056
Ta 142,22,46176
Mvaa 104,148,28
Mvaa 104,144,24
W 8
s_add_u32 s78, s62, s66
Ta 144,25,128
Mvaa 108,152,24
Ta 146,25,5248
Mvaa 108,156,28
Ta 148,21,41088
Ta 150,21,46208
Mvaa 100,156,12
s_add_u32 s79, s78, 327680
Mvaa 100,152,8
W 8
Ta 152,26,160
Ta 154,26,5280
Mvaa 112,128,8
s_and_b32 s66, s14, 1
Mvaa 112,132,12
Ta 156,22,41120
Ta 158,22,46240
Mvaa 120,132,28
s_waitcnt vmcnt(0)
v_cmp_eq_u64 vcc, s[12:13], v[28:29]
s_cmp_eq_u64 vcc, -1
s_cbranch_scc0 .Lspin_92%=
.Lok_93%=:
s_add_u32 s81, s58, 1
s_and_b32 s81, s81, 3
Mvaa 120,128,24
W 8
s_lshl_b32 s81, s81, 12
s_add_u32 s81, s81, s50
Mvaa 124,136,24
s_mul_i32 s66, s66, 327680
buffer_load_dwordx4 a[64:67], v1, s[8:11], s81 offen offset:0 sc1
Ta 128,25,192
Mvaa 124,140,28
s_add_u32 s82, s81, 131072
Ta 130,25,5312
Ta 132,21,41152
Mvaa 116,140,12
s_add_u32 s65, s62, s66
Ta 134,21,46272
Mvaa 116,136,8
W 8
Ta 136,26,224
Ta 138,26,5344
Mvaa 128,144,8
buffer_load_dwordx4 a[80:83], v1, s[8:11], s82 offen offset:0 sc1
Mvaa 128,148,12
s_add_u32 s65, s65, 32768
Ta 140,22,41184
Ta 142,22,46304
Mvaa 136,148,28
Mvaa 136,144,24
W 8
s_mov_b32 s64, 0
Ta 144,25,256
Mvaa 140,152,24
Ta 146,25,5376
Mvaa 140,156,28
buffer_load_dwordx4 a[68:71], v1, s[8:11], s81 offen offset:1024 sc1
Ta 148,21,41216
Mvaa 132,156,12
Ta 150,21,46336
v_add_u32 v19, 4096, v1
Mvaa 132,152,8
W 8
Ta 152,26,288
Ta 154,26,5408
Mvaa 144,128,8
Mvaa 144,132,12
Ta 156,22,41248
v_bfe_u32 v27, v1, 4, 3
Mvaa 152,132,28
Ta 158,22,46368
buffer_load_dwordx4 a[84:87], v1, s[8:11], s82 offen offset:1024 sc1
Mvaa 152,128,24
W 8
Ta 128,21,51200
Ta 130,21,56320
Mvaa 156,136,24
v_lshlrev_b32 v27, 3, v27
Mvaa 156,140,28
Ta 132,23,10240
Ta 134,23,15360
Mvaa 148,140,12
v_bfe_u32 v18, v1, 7, 1
Mvaa 148,136,8
W 8
buffer_load_dwordx4 a[72:75], v1, s[8:11], s81 offen offset:2048 sc1
Ta 136,22,51232
Mvaa 160,144,8
Ta 138,22,56352
Mvaa 160,148,12
Ta 140,24,10272
v_lshl_add_u32 v18, v18, 9, v27
Mvaa 168,148,28
Ta 142,24,15392
Mvaa 168,144,24
W 8
Ta 144,21,51264
Ta 146,21,56384
Mvaa 172,152,24
s_mov_b32 s71, 0
buffer_load_dwordx4 a[88:91], v1, s[8:11], s82 offen offset:2048 sc1
Mvaa 172,156,28
Ta 148,23,10304
Ta 150,23,15424
Mvaa 164,156,12
Mvaa 164,152,8
W 8
Ta 152,22,51296
v_bfe_u32 v20, v1, 4, 4
Mvaa 96,128,32
Ta 154,22,56416
Mvaa 96,132,36
buffer_load_dwordx4 a[76:79], v1, s[8:11], s81 offen offset:3072 sc1
Ta 156,24,10336
Mvaa 104,132,52
Ta 158,24,15456
v_mul_u32_u24 v20, 1280, v20
Mvaa 104,128,48
W 8
Ta 128,21,51328
Ta 130,21,56448
Mvaa 108,136,48
v_lshrrev_b32 v12, 8, v1
Mvaa 108,140,52
Ta 132,23,10368
Ta 134,23,15488
Mvaa 100,140,36
buffer_load_dwordx4 a[92:95], v1, s[8:11], s82 offen offset:3072 sc1
Mvaa 100,136,32
W 8
s_add_u32 s81, s58, 2
s_and_b32 s81, s81, 3
Mvaa 112,144,32
s_lshl_b32 s81, s81, 12
s_add_u32 s81, s81, s50
v_lshl_add_u32 v20, v12, 4, v20
Mvaa 112,148,36
Ta 136,22,51360
Ta 138,22,56480
Mvaa 120,148,52
s_bfe_u32 s66, s59, 0x10001
Ta 140,24,10400
Ta 142,24,15520
Mvaa 120,144,48
W 8
buffer_load_dwordx4 a[96:99], v1, s[8:11], s81 offen offset:0 sc1
Ta 144,21,51392
Mvaa 124,152,48
s_add_u32 s82, s81, 131072
Ta 146,21,56512
Mvaa 124,156,52
s_lshl_b32 s67, s57, 1
Ta 148,23,10432
)"
R"(Ta 150,23,15552
Mvaa 116,156,36
Mvaa 116,152,32
W 8
s_add_u32 s66, s66, s67
Ta 152,22,51424
Mvaa 128,128,32
Ta 154,22,56544
buffer_load_dwordx4 a[112:115], v1, s[8:11], s82 offen offset:0 sc1
Mvaa 128,132,36
Ta 156,24,10464
Ta 158,24,15584
Mvaa 136,132,52
s_lshl_b32 s61, s66, 2
Mvaa 136,128,48
W 8
Ta 128,21,51456
Ta 130,21,56576
Mvaa 140,136,48
Mvaa 140,140,52
s_lshr_b32 s66, s59, 2
buffer_load_dwordx4 a[100:103], v1, s[8:11], s81 offen offset:1024 sc1
Ta 132,23,10496
Mvaa 132,140,36
Ta 134,23,15616
Mvaa 132,136,32
W 8
Ta 136,22,51488
Ta 138,22,56608
Mvaa 144,144,32
s_lshl_b32 s66, s66, 1
Mvaa 144,148,36
Ta 140,24,10528
Ta 142,24,15648
Mvaa 152,148,52
s_add_u32 s61, s61, s66
buffer_load_dwordx4 a[116:119], v1, s[8:11], s82 offen offset:1024 sc1
Mvaa 152,144,48
W 8
Ta 144,23,30720
Ta 146,23,35840
Mvaa 156,152,48
Mvaa 156,156,52
s_and_b32 s66, s59, 1
Ta 148,23,40960
Ta 150,23,46080
Mvaa 148,156,36
Mvaa 148,152,32
W 8
buffer_load_dwordx4 a[104:107], v1, s[8:11], s81 offen offset:2048 sc1
Ta 152,24,30752
Mvaa 160,128,32
s_add_u32 s61, s61, s66
Ta 154,24,35872
Mvaa 160,132,36
Ta 156,24,40992
Ta 158,24,46112
Mvaa 168,132,52
Mvaa 168,128,48
W 8
s_xor_b32 s67, s61, 0
Ta 128,23,30784
Mvaa 172,136,48
Ta 130,23,35904
buffer_load_dwordx4 a[120:123], v1, s[8:11], s82 offen offset:2048 sc1
Mvaa 172,140,52
Ta 132,23,41024
Ta 134,23,46144
Mvaa 164,140,36
s_mul_i32 s72, s67, 20480
Mvaa 164,136,32
W 8
Ta 136,24,30816
Ta 138,24,35936
Mvaa 96,144,40
s_mul_i32 s66, s58, 320
Mvaa 96,148,44
buffer_load_dwordx4 a[108:111], v1, s[8:11], s81 offen offset:3072 sc1
Ta 140,24,41056
Mvaa 104,148,60
Ta 142,24,46176
Mvaa 104,144,56
W 8
s_add_u32 s72, s72, s66
Ta 144,23,30848
Mvaa 108,152,56
Ta 146,23,35968
Mvaa 108,156,60
Ta 148,23,41088
Ta 150,23,46208
Mvaa 100,156,44
s_mul_i32 s75, s67, 20480
buffer_load_dwordx4 a[124:127], v1, s[8:11], s82 offen offset:3072 sc1
Mvaa 100,152,40
W 8
s_add_u32 s81, s58, 3
s_and_b32 s81, s81, 3
Mvaa 112,128,40
s_lshl_b32 s81, s81, 12
s_add_u32 s81, s81, s50
Ta 152,24,30880
Mvaa 112,132,44
s_mul_i32 s66, s58, 2560
Ta 154,24,36000
Ta 156,24,41120
Mvaa 120,132,60
Ta 158,24,46240
buffer_load_dwordx4 a[184:187], v1, s[8:11], s81 offen offset:0 sc1
Mvaa 120,128,56
W 8
s_add_u32 s82, s81, 131072
s_add_u32 s75, s75, s66
Mvaa 124,136,56
Ta 128,23,30912
Ta 130,23,36032
Mvaa 124,140,60
Ta 132,23,41152
Ta 134,23,46272
Mvaa 116,140,44
s_add_u32 s75, s75, 131072
Mvaa 116,136,40
W 8
Ta 136,24,30944
Ta 138,24,36064
Mvaa 128,144,40
buffer_load_dwordx4 a[200:203], v1, s[8:11], s82 offen offset:0 sc1
Mvaa 128,148,44
s_xor_b32 s67, s61, 124
Ta 140,24,41184
Ta 142,24,46304
Mvaa 136,148,60
Mvaa 136,144,56
W 8
Ta 144,23,30976
Ta 146,23,36096
Mvaa 140,152,56
s_mul_i32 s73, s67, 20480
Mvaa 140,156,60
buffer_load_dwordx4 a[188:191], v1, s[8:11], s81 offen offset:1024 sc1
Ta 148,23,41216
Mvaa 132,156,44
s_mul_i32 s66, s58, 320
Ta 150,23,46336
Mvaa 132,152,40
W 8
Ta 152,24,31008
Ta 154,24,36128
Mvaa 144,128,40
Mvaa 144,132,44
s_add_u32 s73, s73, s66
Ta 156,24,41248
Ta 158,24,46368
Mvaa 152,132,60
buffer_load_dwordx4 a[204:207], v1, s[8:11], s82 offen offset:1024 sc1
Mvaa 152,128,56
W 8
Ta 128,21,10240
Ta 130,21,15360
Mvaa 156,136,56
s_mul_i32 s76, s67, 20480
Mvaa 156,140,60
Ta 132,21,20480
Ta 134,21,25600
Mvaa 148,140,44
K 96,96,97
Mvaa 148,136,40
W 8
s_mul_i32 s66, s58, 2560
buffer_load_dwordx4 a[192:195], v1, s[8:11], s81 offen offset:2048 sc1
Mvaa 160,144,40
Ta 136,22,10272
Ta 138,22,15392
Mvaa 160,148,44
s_add_u32 s76, s76, s66
Ta 140,22,20512
K 97,98,99
Mvaa 168,148,60
Ta 142,22,25632
K 98,100,101
Mvaa 168,144,56
W 8
Ta 144,21,10304
Ta 146,21,15424
Mvaa 172,152,56
s_add_u32 s76, s76, 131072
buffer_load_dwordx4 a[208:211], v1, s[8:11], s82 offen offset:2048 sc1
K 99,102,103
Mvaa 172,156,60
s_add_u32 s83, s52, 0
Ta 148,21,20544
Ta 150,21,25664
Mvaa 164,156,44
Mvaa 164,152,40
s_waitcnt vmcnt(18) lgkmcnt(8)
Ta 152,22,10336
v_bfe_u32 v15, v1, 7, 1
Mvaa 176,128,64
buffer_store_dwordx4 v[96:99], v1, s[8:11], s83 offen sc1
Ta 154,22,15456
Mvaa 176,132,68
Ta 156,22,20576
K 104,104,105
Mvaa 184,132,84
Ta 158,22,25696
v_mul_u32_u24 v14, 10240, v15
Mvaa 184,128,80
W 8
Ta 128,21,10368
K 105,106,107
Mvaa 188,136,80
buffer_load_dwordx4 a[196:199], v1, s[8:11], s81 offen offset:3072 sc1
Ta 130,21,15488
Mvaa 188,140,84
K 106,108,109
v_lshrrev_b32 v15, 8, v1
Mvaa 180,140,68
Ta 132,21,20608
Ta 134,21,25728
Mvaa 180,136,64
W 8
K 107,110,111
v_lshl_add_u32 v14, v15, 7, v14
Mvaa 192,144,64
s_add_u32 s83, s52, 40960
buffer_load_dwordx4 a[212:215], v1, s[8:11], s82 offen offset:3072 sc1
Ta 136,22,10400
Mvaa 192,148,68
Ta 138,22,15520
Ta 140,22,20640
Mvaa 200,148,84
Ta 142,22,25760
v_and_b32 v15, 0x70, v1
Mvaa 200,144,80
W 8
Ta 144,21,10432
Ta 146,21,15552
Mvaa 204,152,80
buffer_store_dwordx4 v[104:107], v1, s[8:11], s83 offen sc1
K 112,112,113
Mvaa 204,156,84
K 113,114,115
v_add_u32 v14, v14, v15
Mvaa 196,156,68
Ta 148,21,20672
Ta 150,21,25792
Mvaa 196,152,64
W 8
Ta 152,22,10464
K 114,116,117
Mvaa 208,128,64
s_bitcmp1_b32 s14, 0
s_cselect_b32 s74, s73, s72
s_cselect_b32 s77, s76, s75
Ta 154,22,15584
K 115,118,119
Mvaa 208,132,68
s_add_u32 s83, s52, 8192
Ta 156,22,20704
Ta 158,22,25824
Mvaa 216,132,84
s_add_u32 s74, s74, 256
Mvaa 216,128,80
W 8
buffer_store_dwordx4 v[112:115], v1, s[8:11], s83 offen sc1
Ta 128,21,10496
Mvaa 220,136,80
Ta 130,21,15616
K 120,120,121
Mvaa 220,140,84
s_add_u32 s77, s77, 2048
Ta 132,21,20736
Ta 134,21,25856
Mvaa 212,140,68
K 121,122,123
Mvaa 212,136,64
W 8
Ta 136,22,10528
Ta 138,22,15648
Mvaa 224,144,64
s_lshl_b32 s66, s14, 6
K 122,124,125
Mvaa 224,148,68
Ta 140,22,20768
Ta 142,22,25888
Mvaa 232,148,84
K 123,126,127
Mvaa 232,144,80
W 8
s_lshl_b32 s67, s14, 9
s_add_u32 s83, s52, 49152
Mvaa 236,152,80
Ta 144,21,30720
Ta 146,21,35840
Mvaa 236,156,84
s_add_u32 s72, s72, s66
buffer_store_dwordx4 v[120:123], v1, s[8:11], s83 offen sc1
Ta 148,23,0
Mvaa 228,156,68
Ta 150,23,5120
K 128,128,129
Mvaa 228,152,64
W 8
Ta 152,22,30752
Ta 154,22,35872
Mvaa 240,128,64
s_add_u32 s75, s75, s67
K 129,130,131
Mvaa 240,132,68
Ta 156,24,32
Ta 158,24,5152
Mvaa 248,132,84
K 130,132,133
Mvaa 248,128,80
W 8
s_add_u32 s73, s73, s66
Ta 128,21,30784
Mvaa 252,136,80
Ta 130,21,35904
K 131,134,135
Mvaa 252,140,84
s_add_u32 s83, s52, 16384
Ta 132,23,64
Ta 134,23,5184
Mvaa 244,140,68
s_add_u32 s76, s76, s67
Mvaa 244,136,64
s_waitcnt vmcnt(20) lgkmcnt(8)
buffer_store_dwordx4 v[128:131], v1, s[8:11], s83 offen sc1
Ta 136,22,30816
Mvaa 176,144,72
Ta 138,22,35936
K 136,136,137
Mvaa 176,148,76
Ta 140,24,96
Ta 142,24,5216
Mvaa 184,148,92
K 137,138,139
Mvaa 184,144,88
W 8
buffer_load_dwordx4 v[36:39], v20, s[16:19], s72 offen
Ta 144,21,30848
Mvaa 188,152,88
Ta 146,21,35968
K 138,140,141
Mvaa 188,156,92
Ta 148,23,128
Ta 150,23,5248
Mvaa 180,156,76
K 139,142,143
Mvaa 180,152,72
W 8
s_add_u32 s83, s52, 57344
buffer_load_dwordx4 v[44:47], v14, s[8:11], s75 offen sc1
Mvaa 192,128,72
Ta 152,22,30880
Ta 154,22,36000
Mvaa 192,132,76
Ta 156,24,160
Ta 158,24,5280
Mvaa 200,132,92
Mvaa 200,128,88
W 8
buffer_store_dwordx4 v[136:139], v1, s[8:11], s83 offen sc1
K 144,144,145
Mvaa 204,136,88
Ta 128,21,30912
Ta 130,21,36032
Mvaa 204,140,92
Ta 132,23,192
K 145,146,147
Mvaa 196,140,76
Ta 134,23,5312
K 146,148,149
Mvaa 196,136,72
W 8
buffer_load_dwordx4 v[52:55], v20, s[16:19], s73 offen
Ta 136,22,30944
Mvaa 208,144,72
Ta 138,22,36064
K 147,150,151
Mvaa 208,148,76
s_add_u32 s83, s52, 24576
Ta 140,24,224
Ta 142,24,5344
Mvaa 216,148,92
Mvaa 216,144,88
W 8
buffer_store_dwordx4 v[144:147], v1, s[8:11], s83 offen sc1
Ta 144,21,30976
Mvaa 220,152,88
Ta 146,21,36096
K 152,152,153
Mvaa 220,156,92
Ta 148,23,256
Ta 150,23,5376
Mvaa 212,156,76
K 153,154,155
Mvaa 212,152,72
W 8
buffer_load_dwordx4 v[60:63], v14, s[8:11], s76 offen sc1
)"
R"(Ta 152,22,31008
Mvaa 224,128,72
Ta 154,22,36128
K 154,156,157
Mvaa 224,132,76
Ta 156,24,288
Ta 158,24,5408
Mvaa 232,132,92
K 155,158,159
Mvaa 232,128,88
W 8
s_add_u32 s83, s52, 65536
buffer_load_dwordx4 v[68:71], v20, s[16:19], s74 offen
Mvaa 236,136,88
Ta 128,23,20480
Ta 130,23,25600
Mvaa 236,140,92
Ta 132,23,51200
Ta 134,23,56320
Mvaa 228,140,76
Mvaa 228,136,72
W 8
buffer_store_dwordx4 v[152:155], v1, s[8:11], s83 offen sc1
K 160,160,161
Mvaa 240,144,72
Ta 136,25,0
Ta 138,25,5120
Mvaa 240,148,76
Ta 140,21,40960
K 161,162,163
Mvaa 248,148,92
Ta 142,21,46080
K 162,164,165
Mvaa 248,144,88
W 8
buffer_load_dwordx4 v[76:79], v14, s[8:11], s77 offen sc1
Ta 144,21,51200
Mvaa 252,152,88
Ta 146,21,56320
K 163,166,167
Mvaa 252,156,92
s_add_u32 s83, s52, 32768
Ta 148,23,10240
Ta 150,23,15360
Mvaa 244,156,76
Mvaa 244,152,72
s_waitcnt vmcnt(26) lgkmcnt(8)
buffer_store_dwordx4 v[160:163], v1, s[8:11], s83 offen sc1
Ta 152,23,30720
Mvaa 176,128,96
Ta 154,23,35840
K 168,168,169
Mvaa 176,132,100
Ta 156,23,40960
Ta 158,23,46080
Mvaa 184,132,116
K 169,170,171
Mvaa 184,128,112
s_waitcnt vmcnt(23) lgkmcnt(8)
buffer_load_dwordx2 v[12:13], v27, s[8:11], 0 offen offset:3584 sc1
Ta 128,24,20512
Mvaa 184,136,120
Ta 130,24,25632
K 170,172,173
Mvaa 184,140,124
Ta 132,24,51232
Ta 134,24,56352
Mvaa 176,140,108
K 171,174,175
Mvaa 176,136,104
s_waitcnt vmcnt(20) lgkmcnt(8)
s_add_u32 s83, s52, 73728
Ta 136,26,32
Mvaa 176,144,184
Ta 138,26,5152
Mvaa 176,148,188
buffer_store_dwordx4 v[168:171], v1, s[8:11], s83 offen sc1
Ta 140,22,40992
Mvaa 184,148,204
Ta 142,22,46112
Mvaa 184,144,200
s_waitcnt vmcnt(16) lgkmcnt(8)
Ta 144,22,51232
Ta 146,22,56352
Mvaa 184,152,208
Mvaa 184,156,212
Ta 148,24,10272
Ta 150,24,15392
Mvaa 176,156,196
Mvaa 176,152,192
W 8
Ta 152,24,30752
Ta 154,24,35872
Mvaa 180,128,96
Mvaa 180,132,100
Ta 156,24,40992
Ta 158,24,46112
Mvaa 188,132,116
Mvaa 188,128,112
W 8
Ta 128,23,20544
Ta 130,23,25664
Mvaa 188,136,120
Mvaa 188,140,124
Ta 132,23,51264
Ta 134,23,56384
Mvaa 180,140,108
Mvaa 180,136,104
W 8
Ta 136,25,64
Ta 138,25,5184
Mvaa 180,144,184
Mvaa 180,148,188
Ta 140,21,41024
Ta 142,21,46144
Mvaa 188,148,204
Mvaa 188,144,200
W 8
Ta 144,21,51264
Ta 146,21,56384
Mvaa 188,152,208
Mvaa 188,156,212
Ta 148,23,10304
Ta 150,23,15424
Mvaa 180,156,196
Mvaa 180,152,192
W 8
Ta 152,23,30784
Ta 154,23,35904
Mvaa 192,128,96
Mvaa 192,132,100
Ta 156,23,41024
Ta 158,23,46144
Mvaa 200,132,116
K 176,176,177
Mvaa 200,128,112
W 8
Ta 128,24,20576
Ta 130,24,25696
Mvaa 200,136,120
K 177,178,179
Mvaa 200,140,124
Ta 132,24,51296
K 178,180,181
Mvaa 192,140,108
Ta 134,24,56416
K 179,182,183
Mvaa 192,136,104
W 8
Ta 136,26,96
Ta 138,26,5216
Mvaa 192,144,184
s_add_u32 s83, s52, 327680
Mvaa 192,148,188
buffer_store_dwordx4 v[176:179], v1, s[8:11], s83 offen sc1
K 184,184,185
Mvaa 200,148,204
K 185,186,187
K 186,188,189
Mvaa 200,144,200
W 6
Ta 140,22,41056
K 187,190,191
Mvaa 200,152,208
s_add_u32 s83, s52, 368640
Ta 142,22,46176
Ta 144,22,51296
Mvaa 200,156,212
buffer_store_dwordx4 v[184:187], v1, s[8:11], s83 offen sc1
Ta 146,22,56416
Mvaa 192,156,196
Ta 148,24,10336
Ta 150,24,15456
Mvaa 192,152,192
W 8
Ta 152,24,30816
Ta 154,24,35936
Mvaa 196,128,96
Mvaa 196,132,100
Ta 156,24,41056
Ta 158,24,46176
Mvaa 204,132,116
Mvaa 204,128,112
W 8
Ta 128,23,20608
Ta 130,23,25728
Mvaa 204,136,120
Mvaa 204,140,124
Ta 132,23,51328
Ta 134,23,56448
Mvaa 196,140,108
Mvaa 196,136,104
W 8
Ta 136,25,128
Ta 138,25,5248
Mvaa 196,144,184
Mvaa 196,148,188
Ta 140,21,41088
Ta 142,21,46208
Mvaa 204,148,204
Mvaa 204,144,200
W 8
Ta 144,21,51328
Ta 146,21,56448
Mvaa 204,152,208
Mvaa 204,156,212
Ta 148,23,10368
Ta 150,23,15488
Mvaa 196,156,196
Mvaa 196,152,192
W 8
Ta 152,23,30848
Ta 154,23,35968
Mvaa 208,128,96
Mvaa 208,132,100
Ta 156,23,41088
Ta 158,23,46208
Mvaa 216,132,116
K 192,192,193
Mvaa 216,128,112
W 8
Ta 128,24,20640
Ta 130,24,25760
Mvaa 216,136,120
K 193,194,195
Mvaa 216,140,124
Ta 132,24,51360
K 194,196,197
Mvaa 208,140,108
Ta 134,24,56480
K 195,198,199
Mvaa 208,136,104
W 8
Ta 136,26,160
Ta 138,26,5280
Mvaa 208,144,184
s_add_u32 s83, s52, 335872
Mvaa 208,148,188
buffer_store_dwordx4 v[192:195], v1, s[8:11], s83 offen sc1
K 200,200,201
Mvaa 216,148,204
K 201,202,203
K 202,204,205
Mvaa 216,144,200
W 6
Ta 140,22,41120
K 203,206,207
Mvaa 216,152,208
s_add_u32 s83, s52, 376832
Ta 142,22,46240
Ta 144,22,51360
Mvaa 216,156,212
buffer_store_dwordx4 v[200:203], v1, s[8:11], s83 offen sc1
Ta 146,22,56480
Mvaa 208,156,196
Ta 148,24,10400
Ta 150,24,15520
Mvaa 208,152,192
W 8
Ta 152,24,30880
Ta 154,24,36000
Mvaa 212,128,96
Mvaa 212,132,100
s_waitcnt vmcnt(4)
v_lshlrev_b32 v2, 2, v1
v_mov_b64 v[28:29], s[12:13]
s_mov_b64 exec, 3
buffer_store_dwordx2 v[28:29], v2, s[8:11], s53 offen offset:0 sc1
s_mov_b64 exec, -1
Ta 156,24,41120
Ta 158,24,46240
Mvaa 220,132,116
Mvaa 220,128,112
W 8
Ta 128,23,20672
Ta 130,23,25792
Mvaa 220,136,120
Mvaa 220,140,124
v_xor_b32 v12, s12, v12
v_xor_b32 v13, s13, v13
v_lshrrev_b32 v3, 11, v12
v_or_b32 v13, v13, v3
v_cmp_eq_u32 vcc, 0, v13
s_cmp_eq_u64 vcc, -1
s_cbranch_scc0 .LL_97%=
.LL_98%=:
v_bfe_u32 v13, v12, 4, 7
s_xor_b32 s66, s61, 124
v_cmp_eq_u32 vcc, s61, v13
v_cmp_eq_u32_e64 s[86:87], s66, v13
s_or_b32 s68, vcc_lo, s86
s_and_b32 s68, s68, 0xff
s_cbranch_scc1 .Lup_95%=
.Lupr_96%=:
Ta 132,23,51392
Ta 134,23,56512
Mvaa 212,140,108
Mvaa 212,136,104
W 8
Ta 136,25,192
v_lshlrev_b32 v32, 16, v36
Mvaa 212,144,184
Ta 138,25,5312
v_and_b32 v33, 0xffff0000, v36
Mvaa 212,148,188
Ta 140,21,41152
v_lshlrev_b32 v34, 16, v37
Mvaa 220,148,204
Ta 142,21,46272
v_and_b32 v35, 0xffff0000, v37
Mvaa 220,144,200
W 8
Ta 144,21,51392
v_lshlrev_b32 v36, 16, v38
Mvaa 220,152,208
Ta 146,21,56512
v_and_b32 v37, 0xffff0000, v38
Mvaa 220,156,212
Ta 148,23,10432
v_lshlrev_b32 v38, 16, v39
Mvaa 212,156,196
Ta 150,23,15552
v_and_b32 v39, 0xffff0000, v39
Mvaa 212,152,192
W 8
Ta 152,23,30912
v_lshlrev_b32 v40, 16, v44
Mvaa 224,128,96
Ta 154,23,36032
v_and_b32 v41, 0xffff0000, v44
Mvaa 224,132,100
Ta 156,23,41152
v_lshlrev_b32 v42, 16, v45
Mvaa 232,132,116
Ta 158,23,46272
v_and_b32 v43, 0xffff0000, v45
Mvaa 232,128,112
W 8
K 208,208,209
v_lshlrev_b32 v44, 16, v46
Mvaa 232,136,120
Ta 128,24,20704
Ta 130,24,25824
Mvaa 232,140,124
v_and_b32 v45, 0xffff0000, v46
K 209,210,211
Mvaa 224,140,108
v_lshlrev_b32 v46, 16, v47
K 210,212,213
Mvaa 224,136,104
W 6
Ta 132,24,51424
Ta 134,24,56544
Mvaa 224,144,184
v_and_b32 v47, 0xffff0000, v47
K 211,214,215
Mvaa 224,148,188
s_add_u32 s83, s52, 344064
Ta 136,26,224
v_lshlrev_b32 v48, 16, v52
Mvaa 232,148,204
Ta 138,26,5344
v_and_b32 v49, 0xffff0000, v52
Mvaa 232,144,200
W 6
buffer_store_dwordx4 v[208:211], v1, s[8:11], s83 offen sc1
v_lshlrev_b32 v50, 16, v53
Mvaa 232,152,208
K 216,216,217
K 217,218,219
Mvaa 232,156,212
K 218,220,221
K 219,222,223
Mvaa 224,156,196
s_add_u32 s83, s52, 385024
buffer_store_dwordx4 v[216:219], v1, s[8:11], s83 offen sc1
Ta 140,22,41184
Mvaa 224,152,192
Ta 142,22,46304
W 4
Mvaa 228,128,96
v_and_b32 v51, 0xffff0000, v53
v_lshlrev_b32 v52, 16, v54
Mvaa 228,132,100
Ta 144,22,51424
Ta 146,22,56544
Mvaa 236,132,116
v_and_b32 v53, 0xffff0000, v54
v_lshlrev_b32 v54, 16, v55
Mvaa 236,128,112
Ta 148,24,10464
Ta 150,24,15584
W 4
Mvaa 236,136,120
v_and_b32 v55, 0xffff0000, v55
v_lshlrev_b32 v56, 16, v60
Mvaa 236,140,124
Ta 152,24,30944
Ta 154,24,36064
Mvaa 228,140,108
)"
R"(v_and_b32 v57, 0xffff0000, v60
v_lshlrev_b32 v58, 16, v61
Mvaa 228,136,104
Ta 156,24,41184
Ta 158,24,46304
W 4
Mvaa 228,144,184
v_and_b32 v59, 0xffff0000, v61
v_lshlrev_b32 v60, 16, v62
Mvaa 228,148,188
Ta 128,23,20736
Ta 130,23,25856
Mvaa 236,148,204
v_and_b32 v61, 0xffff0000, v62
v_lshlrev_b32 v62, 16, v63
Mvaa 236,144,200
Ta 132,23,51456
Ta 134,23,56576
W 4
Mvaa 236,152,208
v_and_b32 v63, 0xffff0000, v63
v_lshlrev_b32 v64, 16, v68
Mvaa 236,156,212
Ta 136,25,256
Ta 138,25,5376
Mvaa 228,156,196
v_and_b32 v65, 0xffff0000, v68
v_lshlrev_b32 v66, 16, v69
Mvaa 228,152,192
Ta 140,21,41216
Ta 142,21,46336
W 4
Mvaa 240,128,96
v_and_b32 v67, 0xffff0000, v69
v_lshlrev_b32 v68, 16, v70
Mvaa 240,132,100
Ta 144,21,51456
Ta 146,21,56576
Mvaa 248,132,116
v_and_b32 v69, 0xffff0000, v70
v_lshlrev_b32 v70, 16, v71
Mvaa 248,128,112
Ta 148,23,10496
Ta 150,23,15616
W 4
Mvaa 248,136,120
v_and_b32 v71, 0xffff0000, v71
v_lshlrev_b32 v72, 16, v76
Mvaa 248,140,124
Ta 152,23,30976
Ta 154,23,36096
Mvaa 240,140,108
v_and_b32 v73, 0xffff0000, v76
v_lshlrev_b32 v74, 16, v77
Mvaa 240,136,104
Ta 156,23,41216
Ta 158,23,46336
W 4
Mvaa 240,144,184
v_and_b32 v75, 0xffff0000, v77
K 224,224,225
Mvaa 240,148,188
Ta 128,24,20768
v_lshlrev_b32 v76, 16, v78
Mvaa 248,148,204
Ta 130,24,25888
K 225,226,227
Mvaa 248,144,200
Ta 132,24,51488
Ta 134,24,56608
W 4
Mvaa 248,152,208
v_and_b32 v77, 0xffff0000, v78
K 226,228,229
Mvaa 248,156,212
v_lshlrev_b32 v78, 16, v79
v_and_b32 v79, 0xffff0000, v79
Mvaa 240,156,196
K 227,230,231
s_add_u32 s83, s52, 352256
buffer_store_dwordx4 v[224:227], v1, s[8:11], s83 offen sc1
K 232,232,233
K 233,234,235
K 234,236,237
K 235,238,239
s_add_u32 s83, s52, 393216
buffer_store_dwordx4 v[232:235], v1, s[8:11], s83 offen sc1
Mvaa 240,152,192
Ta 136,26,288
Ta 138,26,5408
Ta 140,22,41248
Ta 142,22,46368
W 4
Mvaa 244,128,96
Ta 144,22,51488
Ta 146,22,56608
Mvaa 244,132,100
Ta 148,24,10528
Ta 150,24,15648
Mvaa 252,132,116
Ta 152,24,31008
Ta 154,24,36128
Mvaa 252,128,112
W 6
Ta 156,24,41248
Ta 158,24,46368
Mvaa 252,136,120
Mvaa 252,140,124
Mvaa 244,140,108
Mvaa 244,136,104
W 4
Mvaa 244,144,184
Mvaa 244,148,188
Mvaa 252,148,204
Mvaa 252,144,200
W 0
Mvaa 252,152,208
Mvaa 252,156,212
Mvaa 244,156,196
Mvaa 244,152,192
K 240,240,241
K 241,242,243
s_nop 5
K 242,244,245
K 243,246,247
s_add_u32 s83, s52, 360448
buffer_store_dwordx4 v[240:243], v1, s[8:11], s83 offen sc1
K 248,248,249
K 249,250,251
K 250,252,253
K 251,254,255
s_add_u32 s83, s52, 401408
buffer_store_dwordx4 v[248:251], v1, s[8:11], s83 offen sc1
.Ltend_80%=:
buffer_load_dwordx2 v[16:17], v18, s[8:11], s63 offen sc1
s_cmp_lg_u32 s68, 0
s_cbranch_scc1 .Lup_100%=
.Lupr_101%=:
s_lshl_b32 s66, 0x101, s59
s_waitcnt vmcnt(1)
s_cmp_eq_u32 s64, 0
s_cbranch_scc0 .LL_106%=
v_lshlrev_b32 v2, 2, v1
v_mov_b64 v[28:29], s[12:13]
s_mov_b64 exec, 3
buffer_store_dwordx2 v[28:29], v2, s[8:11], s53 offen offset:512 sc1
s_mov_b64 exec, -1
.LL_106%=:
s_barrier
s_waitcnt vmcnt(1)
v_cmp_eq_u64 vcc, s[12:13], v[16:17]
s_or_b32 s67, vcc_lo, s66
s_cmp_eq_u32 s14, 0
s_cbranch_scc1 .LL_108%=
s_cmp_eq_u32 s14, 1
s_cbranch_scc1 .LL_109%=
s_mov_b32 s84, 0
.LL_110%=:
s_mov_b32 s70, s71
s_andn2_b32 s69, s67, s71
s_and_b32 s69, s69, 0xffff
s_cbranch_scc0 .LL_113%=
s_and_b32 s80, s69, 255
s_cbranch_scc0 .LL_114%=
s_bitcmp1_b32 s69, 0
s_cbranch_scc0 .LL_115%=
buffer_load_dwordx4 a[0:3], v1, s[8:11], s78 offen offset:0 sc1
.LL_115%=:
s_bitcmp1_b32 s69, 1
s_cbranch_scc0 .LL_116%=
buffer_load_dwordx4 a[4:7], v1, s[8:11], s78 offen offset:1024 sc1
.LL_116%=:
s_bitcmp1_b32 s69, 2
s_cbranch_scc0 .LL_117%=
buffer_load_dwordx4 a[8:11], v1, s[8:11], s78 offen offset:2048 sc1
.LL_117%=:
s_bitcmp1_b32 s69, 3
s_cbranch_scc0 .LL_118%=
buffer_load_dwordx4 a[12:15], v1, s[8:11], s78 offen offset:3072 sc1
.LL_118%=:
s_bitcmp1_b32 s69, 4
s_cbranch_scc0 .LL_119%=
buffer_load_dwordx4 a[16:19], v19, s[8:11], s78 offen offset:0 sc1
.LL_119%=:
s_bitcmp1_b32 s69, 5
s_cbranch_scc0 .LL_120%=
buffer_load_dwordx4 a[20:23], v19, s[8:11], s78 offen offset:1024 sc1
.LL_120%=:
s_bitcmp1_b32 s69, 6
s_cbranch_scc0 .LL_121%=
buffer_load_dwordx4 a[24:27], v19, s[8:11], s78 offen offset:2048 sc1
.LL_121%=:
s_bitcmp1_b32 s69, 7
s_cbranch_scc0 .LL_122%=
buffer_load_dwordx4 a[28:31], v19, s[8:11], s78 offen offset:3072 sc1
.LL_122%=:
.LL_114%=:
s_and_b32 s80, s69, 65280
s_cbranch_scc0 .LL_123%=
s_bitcmp1_b32 s69, 8
s_cbranch_scc0 .LL_124%=
buffer_load_dwordx4 a[32:35], v1, s[8:11], s79 offen offset:0 sc1
.LL_124%=:
s_bitcmp1_b32 s69, 9
s_cbranch_scc0 .LL_125%=
buffer_load_dwordx4 a[36:39], v1, s[8:11], s79 offen offset:1024 sc1
.LL_125%=:
s_bitcmp1_b32 s69, 10
s_cbranch_scc0 .LL_126%=
buffer_load_dwordx4 a[40:43], v1, s[8:11], s79 offen offset:2048 sc1
.LL_126%=:
s_bitcmp1_b32 s69, 11
s_cbranch_scc0 .LL_127%=
buffer_load_dwordx4 a[44:47], v1, s[8:11], s79 offen offset:3072 sc1
.LL_127%=:
s_bitcmp1_b32 s69, 12
s_cbranch_scc0 .LL_128%=
buffer_load_dwordx4 a[48:51], v19, s[8:11], s79 offen offset:0 sc1
.LL_128%=:
s_bitcmp1_b32 s69, 13
s_cbranch_scc0 .LL_129%=
buffer_load_dwordx4 a[52:55], v19, s[8:11], s79 offen offset:1024 sc1
.LL_129%=:
s_bitcmp1_b32 s69, 14
s_cbranch_scc0 .LL_130%=
buffer_load_dwordx4 a[56:59], v19, s[8:11], s79 offen offset:2048 sc1
.LL_130%=:
s_bitcmp1_b32 s69, 15
s_cbranch_scc0 .LL_131%=
buffer_load_dwordx4 a[60:63], v19, s[8:11], s79 offen offset:3072 sc1
.LL_131%=:
.LL_123%=:
s_or_b32 s71, s71, s69
.LL_113%=:
s_andn2_b32 s80, 0xffff, s71
s_cbranch_scc0 .LL_112%=
s_sleep 4
buffer_load_dwordx2 v[16:17], v18, s[8:11], s63 offen sc1
s_and_b32 s80, s70, 0x100ff
s_cmp_eq_u32 s80, 0xff
s_cbranch_scc0 .LL_111%=
Zvva 80,4,0
Zvva 84,8,0
Mvva 80,4,4
Mvva 84,8,4
Mvva 80,4,8
Mvva 84,8,8
Mvva 80,4,12
Mvva 84,8,12
Mvva 80,4,16
Mvva 84,8,16
Mvva 80,4,20
Mvva 84,8,20
Mvva 80,4,24
Mvva 84,8,24
Mvva 80,4,28
Mvva 84,8,28
s_nop 6
K 160,80,81
K 161,82,83
K 162,84,85
K 163,86,87
v_lshlrev_b32 v80, 16, v160
v_and_b32 v81, 0xffff0000, v160
v_lshlrev_b32 v82, 16, v161
v_and_b32 v83, 0xffff0000, v161
v_lshlrev_b32 v84, 16, v162
v_and_b32 v85, 0xffff0000, v162
v_lshlrev_b32 v86, 16, v163
v_and_b32 v87, 0xffff0000, v163
v_pk_add_f32 v[80:81], v[32:33], v[80:81]
v_pk_add_f32 v[82:83], v[34:35], v[82:83]
v_pk_add_f32 v[84:85], v[36:37], v[84:85]
v_pk_add_f32 v[86:87], v[38:39], v[86:87]
K 160,80,81
K 161,82,83
K 162,84,85
K 163,86,87
v_lshlrev_b32 v80, 16, v160
v_and_b32 v81, 0xffff0000, v160
v_lshlrev_b32 v82, 16, v161
v_and_b32 v83, 0xffff0000, v161
v_lshlrev_b32 v84, 16, v162
v_and_b32 v85, 0xffff0000, v162
v_lshlrev_b32 v86, 16, v163
v_and_b32 v87, 0xffff0000, v163
v_pk_add_f32 v[80:81], v[40:41], v[80:81]
v_pk_add_f32 v[82:83], v[42:43], v[82:83]
v_pk_add_f32 v[84:85], v[44:45], v[84:85]
v_pk_add_f32 v[86:87], v[46:47], v[86:87]
K 80,80,81
K 81,82,83
K 82,84,85
K 83,86,87
buffer_store_dwordx4 v[80:83], v20, s[20:23], s72 offen nt
s_bitset1_b32 s71, 16
.LL_111%=:
s_add_u32 s84, s84, 1
s_waitcnt vmcnt(0)
v_cmp_eq_u64 vcc, s[12:13], v[16:17]
s_or_b32 s67, vcc_lo, s66
s_cmp_lt_u32 s84, 4194304
s_cbranch_scc1 .LL_110%=
.LL_112%=:
s_bitcmp1_b32 s71, 16
s_cbranch_scc1 .LL_132%=
s_and_b32 s80, s70, 0xff
s_cmp_eq_u32 s80, 0xff
s_cbranch_scc1 .LL_133%=
s_waitcnt vmcnt(0)
.LL_133%=:
Zvva 80,4,0
Zvva 84,8,0
Mvva 80,4,4
Mvva 84,8,4
Mvva 80,4,8
Mvva 84,8,8
Mvva 80,4,12
Mvva 84,8,12
Mvva 80,4,16
Mvva 84,8,16
Mvva 80,4,20
Mvva 84,8,20
Mvva 80,4,24
Mvva 84,8,24
Mvva 80,4,28
Mvva 84,8,28
s_nop 6
K 160,80,81
K 161,82,83
K 162,84,85
K 163,86,87
v_lshlrev_b32 v80, 16, v160
v_and_b32 v81, 0xffff0000, v160
v_lshlrev_b32 v82, 16, v161
v_and_b32 v83, 0xffff0000, v161
v_lshlrev_b32 v84, 16, v162
v_and_b32 v85, 0xffff0000, v162
v_lshlrev_b32 v86, 16, v163
v_and_b32 v87, 0xffff0000, v163
v_pk_add_f32 v[80:81], v[32:33], v[80:81]
v_pk_add_f32 v[82:83], v[34:35], v[82:83]
v_pk_add_f32 v[84:85], v[36:37], v[84:85]
v_pk_add_f32 v[86:87], v[38:39], v[86:87]
K 160,80,81
K 161,82,83
K 162,84,85
K 163,86,87
v_lshlrev_b32 v80, 16, v160
v_and_b32 v81, 0xffff0000, v160
v_lshlrev_b32 v82, 16, v161
v_and_b32 v83, 0xffff0000, v161
v_lshlrev_b32 v84, 16, v162
v_and_b32 v85, 0xffff0000, v162
v_lshlrev_b32 v86, 16, v163
v_and_b32 v87, 0xffff0000, v163
v_pk_add_f32 v[80:81], v[40:41], v[80:81]
v_pk_add_f32 v[82:83], v[42:43], v[82:83]
v_pk_add_f32 v[84:85], v[44:45], v[84:85]
v_pk_add_f32 v[86:87], v[46:47], v[86:87]
K 80,80,81
K 81,82,83
K 82,84,85
K 83,86,87
buffer_store_dwordx4 v[80:83], v20, s[20:23], s72 offen nt
s_waitcnt vmcnt(1)
s_nop 0
Zvva 80,4,32
Zvva 84,8,32
Mvva 80,4,36
Mvva 84,8,36
Mvva 80,4,40
Mvva 84,8,40
Mvva 80,4,44
Mvva 84,8,44
Mvva 80,4,48
Mvva 84,8,48
Mvva 80,4,52
Mvva 84,8,52
Mvva 80,4,56
Mvva 84,8,56
Mvva 80,4,60
Mvva 84,8,60
s_nop 6
K 160,80,81
K 161,82,83
K 162,84,85
K 163,86,87
v_lshlrev_b32 v80, 16, v160
v_and_b32 v81, 0xffff0000, v160
v_lshlrev_b32 v82, 16, v161
v_and_b32 v83, 0xffff0000, v161
v_lshlrev_b32 v84, 16, v162
v_and_b32 v85, 0xffff0000, v162
v_lshlrev_b32 v86, 16, v163
v_and_b32 v87, 0xffff0000, v163
v_pk_add_f32 v[80:81], v[48:49], v[80:81]
v_pk_add_f32 v[82:83], v[50:51], v[82:83]
v_pk_add_f32 v[84:85], v[52:53], v[84:85]
v_pk_add_f32 v[86:87], v[54:55], v[86:87]
)"
R"(K 160,80,81
K 161,82,83
K 162,84,85
K 163,86,87
v_lshlrev_b32 v80, 16, v160
v_and_b32 v81, 0xffff0000, v160
v_lshlrev_b32 v82, 16, v161
v_and_b32 v83, 0xffff0000, v161
v_lshlrev_b32 v84, 16, v162
v_and_b32 v85, 0xffff0000, v162
v_lshlrev_b32 v86, 16, v163
v_and_b32 v87, 0xffff0000, v163
v_pk_add_f32 v[80:81], v[56:57], v[80:81]
v_pk_add_f32 v[82:83], v[58:59], v[82:83]
v_pk_add_f32 v[84:85], v[60:61], v[84:85]
v_pk_add_f32 v[86:87], v[62:63], v[86:87]
K 80,80,81
K 81,82,83
K 82,84,85
K 83,86,87
buffer_store_dwordx4 v[80:83], v20, s[20:23], s73 offen nt
s_branch .LL_107%=
.LL_132%=:
s_waitcnt vmcnt(0)
Zvva 80,4,32
Zvva 84,8,32
Mvva 80,4,36
Mvva 84,8,36
Mvva 80,4,40
Mvva 84,8,40
Mvva 80,4,44
Mvva 84,8,44
Mvva 80,4,48
Mvva 84,8,48
Mvva 80,4,52
Mvva 84,8,52
Mvva 80,4,56
Mvva 84,8,56
Mvva 80,4,60
Mvva 84,8,60
s_nop 6
K 160,80,81
K 161,82,83
K 162,84,85
K 163,86,87
v_lshlrev_b32 v80, 16, v160
v_and_b32 v81, 0xffff0000, v160
v_lshlrev_b32 v82, 16, v161
v_and_b32 v83, 0xffff0000, v161
v_lshlrev_b32 v84, 16, v162
v_and_b32 v85, 0xffff0000, v162
v_lshlrev_b32 v86, 16, v163
v_and_b32 v87, 0xffff0000, v163
v_pk_add_f32 v[80:81], v[48:49], v[80:81]
v_pk_add_f32 v[82:83], v[50:51], v[82:83]
v_pk_add_f32 v[84:85], v[52:53], v[84:85]
v_pk_add_f32 v[86:87], v[54:55], v[86:87]
K 160,80,81
K 161,82,83
K 162,84,85
K 163,86,87
v_lshlrev_b32 v80, 16, v160
v_and_b32 v81, 0xffff0000, v160
v_lshlrev_b32 v82, 16, v161
v_and_b32 v83, 0xffff0000, v161
v_lshlrev_b32 v84, 16, v162
v_and_b32 v85, 0xffff0000, v162
v_lshlrev_b32 v86, 16, v163
v_and_b32 v87, 0xffff0000, v163
v_pk_add_f32 v[80:81], v[56:57], v[80:81]
v_pk_add_f32 v[82:83], v[58:59], v[82:83]
v_pk_add_f32 v[84:85], v[60:61], v[84:85]
v_pk_add_f32 v[86:87], v[62:63], v[86:87]
K 80,80,81
K 81,82,83
K 82,84,85
K 83,86,87
buffer_store_dwordx4 v[80:83], v20, s[20:23], s73 offen nt
s_branch .LL_107%=
.LL_108%=:
s_mov_b32 s84, 0
.LL_134%=:
s_mov_b32 s70, s71
s_andn2_b32 s69, s67, s71
s_and_b32 s69, s69, 0xffff
s_cbranch_scc0 .LL_137%=
s_and_b32 s80, s69, 255
s_cbranch_scc0 .LL_138%=
s_bitcmp1_b32 s69, 0
s_cbranch_scc0 .LL_139%=
buffer_load_dwordx4 a[0:3], v1, s[8:11], s78 offen offset:0 sc1
buffer_load_dwordx4 a[216:219], v1, s[8:11], s65 offen offset:0 sc1
.LL_139%=:
s_bitcmp1_b32 s69, 1
s_cbranch_scc0 .LL_140%=
buffer_load_dwordx4 a[4:7], v1, s[8:11], s78 offen offset:1024 sc1
buffer_load_dwordx4 a[220:223], v1, s[8:11], s65 offen offset:1024 sc1
.LL_140%=:
s_bitcmp1_b32 s69, 2
s_cbranch_scc0 .LL_141%=
buffer_load_dwordx4 a[8:11], v1, s[8:11], s78 offen offset:2048 sc1
buffer_load_dwordx4 a[224:227], v1, s[8:11], s65 offen offset:2048 sc1
.LL_141%=:
s_bitcmp1_b32 s69, 3
s_cbranch_scc0 .LL_142%=
buffer_load_dwordx4 a[12:15], v1, s[8:11], s78 offen offset:3072 sc1
buffer_load_dwordx4 a[228:231], v1, s[8:11], s65 offen offset:3072 sc1
.LL_142%=:
s_bitcmp1_b32 s69, 4
s_cbranch_scc0 .LL_143%=
buffer_load_dwordx4 a[16:19], v19, s[8:11], s78 offen offset:0 sc1
buffer_load_dwordx4 a[232:235], v19, s[8:11], s65 offen offset:0 sc1
.LL_143%=:
s_bitcmp1_b32 s69, 5
s_cbranch_scc0 .LL_144%=
buffer_load_dwordx4 a[20:23], v19, s[8:11], s78 offen offset:1024 sc1
buffer_load_dwordx4 a[236:239], v19, s[8:11], s65 offen offset:1024 sc1
.LL_144%=:
s_bitcmp1_b32 s69, 6
s_cbranch_scc0 .LL_145%=
buffer_load_dwordx4 a[24:27], v19, s[8:11], s78 offen offset:2048 sc1
buffer_load_dwordx4 a[240:243], v19, s[8:11], s65 offen offset:2048 sc1
.LL_145%=:
s_bitcmp1_b32 s69, 7
s_cbranch_scc0 .LL_146%=
buffer_load_dwordx4 a[28:31], v19, s[8:11], s78 offen offset:3072 sc1
buffer_load_dwordx4 a[244:247], v19, s[8:11], s65 offen offset:3072 sc1
.LL_146%=:
.LL_138%=:
s_and_b32 s80, s69, 65280
s_cbranch_scc0 .LL_147%=
s_bitcmp1_b32 s69, 8
s_cbranch_scc0 .LL_148%=
buffer_load_dwordx4 a[32:35], v1, s[8:11], s79 offen offset:0 sc1
.LL_148%=:
s_bitcmp1_b32 s69, 9
s_cbranch_scc0 .LL_149%=
buffer_load_dwordx4 a[36:39], v1, s[8:11], s79 offen offset:1024 sc1
.LL_149%=:
s_bitcmp1_b32 s69, 10
s_cbranch_scc0 .LL_150%=
buffer_load_dwordx4 a[40:43], v1, s[8:11], s79 offen offset:2048 sc1
.LL_150%=:
s_bitcmp1_b32 s69, 11
s_cbranch_scc0 .LL_151%=
buffer_load_dwordx4 a[44:47], v1, s[8:11], s79 offen offset:3072 sc1
.LL_151%=:
s_bitcmp1_b32 s69, 12
s_cbranch_scc0 .LL_152%=
buffer_load_dwordx4 a[48:51], v19, s[8:11], s79 offen offset:0 sc1
.LL_152%=:
s_bitcmp1_b32 s69, 13
s_cbranch_scc0 .LL_153%=
buffer_load_dwordx4 a[52:55], v19, s[8:11], s79 offen offset:1024 sc1
.LL_153%=:
s_bitcmp1_b32 s69, 14
s_cbranch_scc0 .LL_154%=
buffer_load_dwordx4 a[56:59], v19, s[8:11], s79 offen offset:2048 sc1
.LL_154%=:
s_bitcmp1_b32 s69, 15
s_cbranch_scc0 .LL_155%=
buffer_load_dwordx4 a[60:63], v19, s[8:11], s79 offen offset:3072 sc1
.LL_155%=:
.LL_147%=:
s_or_b32 s71, s71, s69
.LL_137%=:
s_andn2_b32 s80, 0xffff, s71
s_cbranch_scc0 .LL_136%=
s_sleep 4
buffer_load_dwordx2 v[16:17], v18, s[8:11], s63 offen sc1
s_and_b32 s80, s70, 0x100ff
s_cmp_eq_u32 s80, 0xff
s_cbranch_scc0 .LL_135%=
Zvva 80,4,0
Zvva 84,8,0
Zvva 88,4,216
Zvva 92,8,216
Mvva 80,4,4
Mvva 84,8,4
Mvva 88,4,220
Mvva 92,8,220
Mvva 80,4,8
Mvva 84,8,8
Mvva 88,4,224
Mvva 92,8,224
Mvva 80,4,12
Mvva 84,8,12
Mvva 88,4,228
Mvva 92,8,228
Mvva 80,4,16
Mvva 84,8,16
Mvva 88,4,232
Mvva 92,8,232
Mvva 80,4,20
Mvva 84,8,20
Mvva 88,4,236
Mvva 92,8,236
Mvva 80,4,24
Mvva 84,8,24
Mvva 88,4,240
Mvva 92,8,240
Mvva 80,4,28
Mvva 84,8,28
Mvva 88,4,244
Mvva 92,8,244
s_nop 4
K 160,80,81
K 161,82,83
K 162,84,85
K 163,86,87
v_lshlrev_b32 v80, 16, v160
v_and_b32 v81, 0xffff0000, v160
v_lshlrev_b32 v82, 16, v161
v_and_b32 v83, 0xffff0000, v161
v_lshlrev_b32 v84, 16, v162
v_and_b32 v85, 0xffff0000, v162
v_lshlrev_b32 v86, 16, v163
v_and_b32 v87, 0xffff0000, v163
v_pk_add_f32 v[80:81], v[32:33], v[80:81]
v_pk_add_f32 v[82:83], v[34:35], v[82:83]
v_pk_add_f32 v[84:85], v[36:37], v[84:85]
v_pk_add_f32 v[86:87], v[38:39], v[86:87]
K 160,80,81
K 161,82,83
K 162,84,85
K 163,86,87
v_lshlrev_b32 v80, 16, v160
v_and_b32 v81, 0xffff0000, v160
v_lshlrev_b32 v82, 16, v161
v_and_b32 v83, 0xffff0000, v161
v_lshlrev_b32 v84, 16, v162
v_and_b32 v85, 0xffff0000, v162
v_lshlrev_b32 v86, 16, v163
v_and_b32 v87, 0xffff0000, v163
v_pk_add_f32 v[80:81], v[40:41], v[80:81]
v_pk_add_f32 v[82:83], v[42:43], v[82:83]
v_pk_add_f32 v[84:85], v[44:45], v[84:85]
v_pk_add_f32 v[86:87], v[46:47], v[86:87]
K 80,80,81
K 81,82,83
K 82,84,85
K 83,86,87
buffer_store_dwordx4 v[80:83], v20, s[20:23], s72 offen nt
K 160,88,89
K 161,90,91
K 162,92,93
K 163,94,95
v_lshlrev_b32 v88, 16, v160
v_and_b32 v89, 0xffff0000, v160
v_lshlrev_b32 v90, 16, v161
v_and_b32 v91, 0xffff0000, v161
v_lshlrev_b32 v92, 16, v162
v_and_b32 v93, 0xffff0000, v162
v_lshlrev_b32 v94, 16, v163
v_and_b32 v95, 0xffff0000, v163
v_pk_add_f32 v[88:89], v[64:65], v[88:89]
v_pk_add_f32 v[90:91], v[66:67], v[90:91]
v_pk_add_f32 v[92:93], v[68:69], v[92:93]
v_pk_add_f32 v[94:95], v[70:71], v[94:95]
K 160,88,89
K 161,90,91
K 162,92,93
K 163,94,95
v_lshlrev_b32 v88, 16, v160
v_and_b32 v89, 0xffff0000, v160
v_lshlrev_b32 v90, 16, v161
v_and_b32 v91, 0xffff0000, v161
v_lshlrev_b32 v92, 16, v162
v_and_b32 v93, 0xffff0000, v162
v_lshlrev_b32 v94, 16, v163
v_and_b32 v95, 0xffff0000, v163
v_pk_add_f32 v[88:89], v[72:73], v[88:89]
v_pk_add_f32 v[90:91], v[74:75], v[90:91]
v_pk_add_f32 v[92:93], v[76:77], v[92:93]
v_pk_add_f32 v[94:95], v[78:79], v[94:95]
K 88,88,89
K 89,90,91
K 90,92,93
K 91,94,95
buffer_store_dwordx4 v[88:91], v20, s[20:23], s74 offen nt
s_bitset1_b32 s71, 16
.LL_135%=:
s_add_u32 s84, s84, 1
s_waitcnt vmcnt(0)
v_cmp_eq_u64 vcc, s[12:13], v[16:17]
s_or_b32 s67, vcc_lo, s66
s_cmp_lt_u32 s84, 4194304
s_cbranch_scc1 .LL_134%=
.LL_136%=:
s_bitcmp1_b32 s71, 16
s_cbranch_scc1 .LL_156%=
s_and_b32 s80, s70, 0xff
s_cmp_eq_u32 s80, 0xff
s_cbranch_scc1 .LL_157%=
s_waitcnt vmcnt(0)
.LL_157%=:
Zvva 80,4,0
Zvva 84,8,0
Zvva 88,4,216
Zvva 92,8,216
Mvva 80,4,4
Mvva 84,8,4
Mvva 88,4,220
Mvva 92,8,220
Mvva 80,4,8
Mvva 84,8,8
Mvva 88,4,224
Mvva 92,8,224
Mvva 80,4,12
Mvva 84,8,12
Mvva 88,4,228
Mvva 92,8,228
Mvva 80,4,16
Mvva 84,8,16
Mvva 88,4,232
Mvva 92,8,232
Mvva 80,4,20
Mvva 84,8,20
Mvva 88,4,236
Mvva 92,8,236
Mvva 80,4,24
Mvva 84,8,24
Mvva 88,4,240
Mvva 92,8,240
Mvva 80,4,28
Mvva 84,8,28
Mvva 88,4,244
Mvva 92,8,244
s_nop 4
K 160,80,81
K 161,82,83
K 162,84,85
K 163,86,87
v_lshlrev_b32 v80, 16, v160
v_and_b32 v81, 0xffff0000, v160
v_lshlrev_b32 v82, 16, v161
v_and_b32 v83, 0xffff0000, v161
v_lshlrev_b32 v84, 16, v162
v_and_b32 v85, 0xffff0000, v162
v_lshlrev_b32 v86, 16, v163
v_and_b32 v87, 0xffff0000, v163
v_pk_add_f32 v[80:81], v[32:33], v[80:81]
v_pk_add_f32 v[82:83], v[34:35], v[82:83]
v_pk_add_f32 v[84:85], v[36:37], v[84:85]
v_pk_add_f32 v[86:87], v[38:39], v[86:87]
K 160,80,81
K 161,82,83
K 162,84,85
K 163,86,87
v_lshlrev_b32 v80, 16, v160
v_and_b32 v81, 0xffff0000, v160
v_lshlrev_b32 v82, 16, v161
v_and_b32 v83, 0xffff0000, v161
v_lshlrev_b32 v84, 16, v162
v_and_b32 v85, 0xffff0000, v162
v_lshlrev_b32 v86, 16, v163
v_and_b32 v87, 0xffff0000, v163
v_pk_add_f32 v[80:81], v[40:41], v[80:81]
v_pk_add_f32 v[82:83], v[42:43], v[82:83]
v_pk_add_f32 v[84:85], v[44:45], v[84:85]
v_pk_add_f32 v[86:87], v[46:47], v[86:87]
K 80,80,81
K 81,82,83
K 82,84,85
K 83,86,87
buffer_store_dwordx4 v[80:83], v20, s[20:23], s72 offen nt
K 160,88,89
K 161,90,91
K 162,92,93
K 163,94,95
v_lshlrev_b32 v88, 16, v160
v_and_b32 v89, 0xffff0000, v160
v_lshlrev_b32 v90, 16, v161
v_and_b32 v91, 0xffff0000, v161
v_lshlrev_b32 v92, 16, v162
v_and_b32 v93, 0xffff0000, v162
v_lshlrev_b32 v94, 16, v163
v_and_b32 v95, 0xffff0000, v163
v_pk_add_f32 v[88:89], v[64:65], v[88:89]
v_pk_add_f32 v[90:91], v[66:67], v[90:91]
v_pk_add_f32 v[92:93], v[68:69], v[92:93]
v_pk_add_f32 v[94:95], v[70:71], v[94:95]
K 160,88,89
K 161,90,91
K 162,92,93
K 163,94,95
v_lshlrev_b32 v88, 16, v160
v_and_b32 v89, 0xffff0000, v160
v_lshlrev_b32 v90, 16, v161
v_and_b32 v91, 0xffff0000, v161
v_lshlrev_b32 v92, 16, v162
v_and_b32 v93, 0xffff0000, v162
v_lshlrev_b32 v94, 16, v163
v_and_b32 v95, 0xffff0000, v163
v_pk_add_f32 v[88:89], v[72:73], v[88:89]
v_pk_add_f32 v[90:91], v[74:75], v[90:91]
v_pk_add_f32 v[92:93], v[76:77], v[92:93]
v_pk_add_f32 v[94:95], v[78:79], v[94:95]
K 88,88,89
K 89,90,91
)"
R"(K 90,92,93
K 91,94,95
buffer_store_dwordx4 v[88:91], v20, s[20:23], s74 offen nt
s_waitcnt vmcnt(2)
Zvva 80,4,32
Zvva 84,8,32
Mvva 80,4,36
Mvva 84,8,36
Mvva 80,4,40
Mvva 84,8,40
Mvva 80,4,44
Mvva 84,8,44
Mvva 80,4,48
Mvva 84,8,48
Mvva 80,4,52
Mvva 84,8,52
Mvva 80,4,56
Mvva 84,8,56
Mvva 80,4,60
Mvva 84,8,60
s_nop 6
K 160,80,81
K 161,82,83
K 162,84,85
K 163,86,87
v_lshlrev_b32 v80, 16, v160
v_and_b32 v81, 0xffff0000, v160
v_lshlrev_b32 v82, 16, v161
v_and_b32 v83, 0xffff0000, v161
v_lshlrev_b32 v84, 16, v162
v_and_b32 v85, 0xffff0000, v162
v_lshlrev_b32 v86, 16, v163
v_and_b32 v87, 0xffff0000, v163
v_pk_add_f32 v[80:81], v[48:49], v[80:81]
v_pk_add_f32 v[82:83], v[50:51], v[82:83]
v_pk_add_f32 v[84:85], v[52:53], v[84:85]
v_pk_add_f32 v[86:87], v[54:55], v[86:87]
K 160,80,81
K 161,82,83
K 162,84,85
K 163,86,87
v_lshlrev_b32 v80, 16, v160
v_and_b32 v81, 0xffff0000, v160
v_lshlrev_b32 v82, 16, v161
v_and_b32 v83, 0xffff0000, v161
v_lshlrev_b32 v84, 16, v162
v_and_b32 v85, 0xffff0000, v162
v_lshlrev_b32 v86, 16, v163
v_and_b32 v87, 0xffff0000, v163
v_pk_add_f32 v[80:81], v[56:57], v[80:81]
v_pk_add_f32 v[82:83], v[58:59], v[82:83]
v_pk_add_f32 v[84:85], v[60:61], v[84:85]
v_pk_add_f32 v[86:87], v[62:63], v[86:87]
K 80,80,81
K 81,82,83
K 82,84,85
K 83,86,87
buffer_store_dwordx4 v[80:83], v20, s[20:23], s73 offen nt
s_branch .LL_107%=
.LL_156%=:
s_waitcnt vmcnt(0)
Zvva 80,4,32
Zvva 84,8,32
Mvva 80,4,36
Mvva 84,8,36
Mvva 80,4,40
Mvva 84,8,40
Mvva 80,4,44
Mvva 84,8,44
Mvva 80,4,48
Mvva 84,8,48
Mvva 80,4,52
Mvva 84,8,52
Mvva 80,4,56
Mvva 84,8,56
Mvva 80,4,60
Mvva 84,8,60
s_nop 6
K 160,80,81
K 161,82,83
K 162,84,85
K 163,86,87
v_lshlrev_b32 v80, 16, v160
v_and_b32 v81, 0xffff0000, v160
v_lshlrev_b32 v82, 16, v161
v_and_b32 v83, 0xffff0000, v161
v_lshlrev_b32 v84, 16, v162
v_and_b32 v85, 0xffff0000, v162
v_lshlrev_b32 v86, 16, v163
v_and_b32 v87, 0xffff0000, v163
v_pk_add_f32 v[80:81], v[48:49], v[80:81]
v_pk_add_f32 v[82:83], v[50:51], v[82:83]
v_pk_add_f32 v[84:85], v[52:53], v[84:85]
v_pk_add_f32 v[86:87], v[54:55], v[86:87]
K 160,80,81
K 161,82,83
K 162,84,85
K 163,86,87
v_lshlrev_b32 v80, 16, v160
v_and_b32 v81, 0xffff0000, v160
v_lshlrev_b32 v82, 16, v161
v_and_b32 v83, 0xffff0000, v161
v_lshlrev_b32 v84, 16, v162
v_and_b32 v85, 0xffff0000, v162
v_lshlrev_b32 v86, 16, v163
v_and_b32 v87, 0xffff0000, v163
v_pk_add_f32 v[80:81], v[56:57], v[80:81]
v_pk_add_f32 v[82:83], v[58:59], v[82:83]
v_pk_add_f32 v[84:85], v[60:61], v[84:85]
v_pk_add_f32 v[86:87], v[62:63], v[86:87]
K 80,80,81
K 81,82,83
K 82,84,85
K 83,86,87
buffer_store_dwordx4 v[80:83], v20, s[20:23], s73 offen nt
s_branch .LL_107%=
.LL_109%=:
s_mov_b32 s84, 0
.LL_158%=:
s_mov_b32 s70, s71
s_andn2_b32 s69, s67, s71
s_and_b32 s69, s69, 0xffff
s_cbranch_scc0 .LL_161%=
s_and_b32 s80, s69, 255
s_cbranch_scc0 .LL_162%=
s_bitcmp1_b32 s69, 0
s_cbranch_scc0 .LL_163%=
buffer_load_dwordx4 a[0:3], v1, s[8:11], s78 offen offset:0 sc1
.LL_163%=:
s_bitcmp1_b32 s69, 1
s_cbranch_scc0 .LL_164%=
buffer_load_dwordx4 a[4:7], v1, s[8:11], s78 offen offset:1024 sc1
.LL_164%=:
s_bitcmp1_b32 s69, 2
s_cbranch_scc0 .LL_165%=
buffer_load_dwordx4 a[8:11], v1, s[8:11], s78 offen offset:2048 sc1
.LL_165%=:
s_bitcmp1_b32 s69, 3
s_cbranch_scc0 .LL_166%=
buffer_load_dwordx4 a[12:15], v1, s[8:11], s78 offen offset:3072 sc1
.LL_166%=:
s_bitcmp1_b32 s69, 4
s_cbranch_scc0 .LL_167%=
buffer_load_dwordx4 a[16:19], v19, s[8:11], s78 offen offset:0 sc1
.LL_167%=:
s_bitcmp1_b32 s69, 5
s_cbranch_scc0 .LL_168%=
buffer_load_dwordx4 a[20:23], v19, s[8:11], s78 offen offset:1024 sc1
.LL_168%=:
s_bitcmp1_b32 s69, 6
s_cbranch_scc0 .LL_169%=
buffer_load_dwordx4 a[24:27], v19, s[8:11], s78 offen offset:2048 sc1
.LL_169%=:
s_bitcmp1_b32 s69, 7
s_cbranch_scc0 .LL_170%=
buffer_load_dwordx4 a[28:31], v19, s[8:11], s78 offen offset:3072 sc1
.LL_170%=:
.LL_162%=:
s_and_b32 s80, s69, 65280
s_cbranch_scc0 .LL_171%=
s_bitcmp1_b32 s69, 8
s_cbranch_scc0 .LL_172%=
buffer_load_dwordx4 a[32:35], v1, s[8:11], s79 offen offset:0 sc1
buffer_load_dwordx4 a[216:219], v1, s[8:11], s65 offen offset:0 sc1
.LL_172%=:
s_bitcmp1_b32 s69, 9
s_cbranch_scc0 .LL_173%=
buffer_load_dwordx4 a[36:39], v1, s[8:11], s79 offen offset:1024 sc1
buffer_load_dwordx4 a[220:223], v1, s[8:11], s65 offen offset:1024 sc1
.LL_173%=:
s_bitcmp1_b32 s69, 10
s_cbranch_scc0 .LL_174%=
buffer_load_dwordx4 a[40:43], v1, s[8:11], s79 offen offset:2048 sc1
buffer_load_dwordx4 a[224:227], v1, s[8:11], s65 offen offset:2048 sc1
.LL_174%=:
s_bitcmp1_b32 s69, 11
s_cbranch_scc0 .LL_175%=
buffer_load_dwordx4 a[44:47], v1, s[8:11], s79 offen offset:3072 sc1
buffer_load_dwordx4 a[228:231], v1, s[8:11], s65 offen offset:3072 sc1
.LL_175%=:
s_bitcmp1_b32 s69, 12
s_cbranch_scc0 .LL_176%=
buffer_load_dwordx4 a[48:51], v19, s[8:11], s79 offen offset:0 sc1
buffer_load_dwordx4 a[232:235], v19, s[8:11], s65 offen offset:0 sc1
.LL_176%=:
s_bitcmp1_b32 s69, 13
s_cbranch_scc0 .LL_177%=
buffer_load_dwordx4 a[52:55], v19, s[8:11], s79 offen offset:1024 sc1
buffer_load_dwordx4 a[236:239], v19, s[8:11], s65 offen offset:1024 sc1
.LL_177%=:
s_bitcmp1_b32 s69, 14
s_cbranch_scc0 .LL_178%=
buffer_load_dwordx4 a[56:59], v19, s[8:11], s79 offen offset:2048 sc1
buffer_load_dwordx4 a[240:243], v19, s[8:11], s65 offen offset:2048 sc1
.LL_178%=:
s_bitcmp1_b32 s69, 15
s_cbranch_scc0 .LL_179%=
buffer_load_dwordx4 a[60:63], v19, s[8:11], s79 offen offset:3072 sc1
buffer_load_dwordx4 a[244:247], v19, s[8:11], s65 offen offset:3072 sc1
.LL_179%=:
.LL_171%=:
s_or_b32 s71, s71, s69
.LL_161%=:
s_andn2_b32 s80, 0xffff, s71
s_cbranch_scc0 .LL_160%=
s_sleep 4
buffer_load_dwordx2 v[16:17], v18, s[8:11], s63 offen sc1
s_and_b32 s80, s70, 0x100ff
s_cmp_eq_u32 s80, 0xff
s_cbranch_scc0 .LL_159%=
Zvva 80,4,0
Zvva 84,8,0
Mvva 80,4,4
Mvva 84,8,4
Mvva 80,4,8
Mvva 84,8,8
Mvva 80,4,12
Mvva 84,8,12
Mvva 80,4,16
Mvva 84,8,16
Mvva 80,4,20
Mvva 84,8,20
Mvva 80,4,24
Mvva 84,8,24
Mvva 80,4,28
Mvva 84,8,28
s_nop 6
K 160,80,81
K 161,82,83
K 162,84,85
K 163,86,87
v_lshlrev_b32 v80, 16, v160
v_and_b32 v81, 0xffff0000, v160
v_lshlrev_b32 v82, 16, v161
v_and_b32 v83, 0xffff0000, v161
v_lshlrev_b32 v84, 16, v162
v_and_b32 v85, 0xffff0000, v162
v_lshlrev_b32 v86, 16, v163
v_and_b32 v87, 0xffff0000, v163
v_pk_add_f32 v[80:81], v[32:33], v[80:81]
v_pk_add_f32 v[82:83], v[34:35], v[82:83]
v_pk_add_f32 v[84:85], v[36:37], v[84:85]
v_pk_add_f32 v[86:87], v[38:39], v[86:87]
K 160,80,81
K 161,82,83
K 162,84,85
K 163,86,87
v_lshlrev_b32 v80, 16, v160
v_and_b32 v81, 0xffff0000, v160
v_lshlrev_b32 v82, 16, v161
v_and_b32 v83, 0xffff0000, v161
v_lshlrev_b32 v84, 16, v162
v_and_b32 v85, 0xffff0000, v162
v_lshlrev_b32 v86, 16, v163
v_and_b32 v87, 0xffff0000, v163
v_pk_add_f32 v[80:81], v[40:41], v[80:81]
v_pk_add_f32 v[82:83], v[42:43], v[82:83]
v_pk_add_f32 v[84:85], v[44:45], v[84:85]
v_pk_add_f32 v[86:87], v[46:47], v[86:87]
K 80,80,81
K 81,82,83
K 82,84,85
K 83,86,87
buffer_store_dwordx4 v[80:83], v20, s[20:23], s72 offen nt
s_bitset1_b32 s71, 16
.LL_159%=:
s_add_u32 s84, s84, 1
s_waitcnt vmcnt(0)
v_cmp_eq_u64 vcc, s[12:13], v[16:17]
s_or_b32 s67, vcc_lo, s66
s_cmp_lt_u32 s84, 4194304
s_cbranch_scc1 .LL_158%=
.LL_160%=:
s_bitcmp1_b32 s71, 16
s_cbranch_scc1 .LL_180%=
s_and_b32 s80, s70, 0xff
s_cmp_eq_u32 s80, 0xff
s_cbranch_scc1 .LL_181%=
s_waitcnt vmcnt(0)
.LL_181%=:
Zvva 80,4,0
Zvva 84,8,0
Mvva 80,4,4
Mvva 84,8,4
Mvva 80,4,8
Mvva 84,8,8
Mvva 80,4,12
Mvva 84,8,12
Mvva 80,4,16
Mvva 84,8,16
Mvva 80,4,20
Mvva 84,8,20
Mvva 80,4,24
Mvva 84,8,24
Mvva 80,4,28
Mvva 84,8,28
s_nop 6
K 160,80,81
K 161,82,83
K 162,84,85
K 163,86,87
v_lshlrev_b32 v80, 16, v160
v_and_b32 v81, 0xffff0000, v160
v_lshlrev_b32 v82, 16, v161
v_and_b32 v83, 0xffff0000, v161
v_lshlrev_b32 v84, 16, v162
v_and_b32 v85, 0xffff0000, v162
v_lshlrev_b32 v86, 16, v163
v_and_b32 v87, 0xffff0000, v163
v_pk_add_f32 v[80:81], v[32:33], v[80:81]
v_pk_add_f32 v[82:83], v[34:35], v[82:83]
v_pk_add_f32 v[84:85], v[36:37], v[84:85]
v_pk_add_f32 v[86:87], v[38:39], v[86:87]
K 160,80,81
K 161,82,83
K 162,84,85
K 163,86,87
v_lshlrev_b32 v80, 16, v160
v_and_b32 v81, 0xffff0000, v160
v_lshlrev_b32 v82, 16, v161
v_and_b32 v83, 0xffff0000, v161
v_lshlrev_b32 v84, 16, v162
v_and_b32 v85, 0xffff0000, v162
v_lshlrev_b32 v86, 16, v163
v_and_b32 v87, 0xffff0000, v163
v_pk_add_f32 v[80:81], v[40:41], v[80:81]
v_pk_add_f32 v[82:83], v[42:43], v[82:83]
v_pk_add_f32 v[84:85], v[44:45], v[84:85]
v_pk_add_f32 v[86:87], v[46:47], v[86:87]
K 80,80,81
K 81,82,83
K 82,84,85
K 83,86,87
buffer_store_dwordx4 v[80:83], v20, s[20:23], s72 offen nt
s_waitcnt vmcnt(1)
s_nop 0
Zvva 80,4,32
Zvva 84,8,32
Zvva 88,4,216
Zvva 92,8,216
Mvva 80,4,36
Mvva 84,8,36
Mvva 88,4,220
Mvva 92,8,220
Mvva 80,4,40
Mvva 84,8,40
Mvva 88,4,224
Mvva 92,8,224
Mvva 80,4,44
Mvva 84,8,44
Mvva 88,4,228
Mvva 92,8,228
Mvva 80,4,48
Mvva 84,8,48
Mvva 88,4,232
Mvva 92,8,232
Mvva 80,4,52
Mvva 84,8,52
Mvva 88,4,236
Mvva 92,8,236
Mvva 80,4,56
Mvva 84,8,56
Mvva 88,4,240
Mvva 92,8,240
Mvva 80,4,60
Mvva 84,8,60
Mvva 88,4,244
Mvva 92,8,244
s_nop 4
K 160,80,81
K 161,82,83
K 162,84,85
K 163,86,87
v_lshlrev_b32 v80, 16, v160
v_and_b32 v81, 0xffff0000, v160
v_lshlrev_b32 v82, 16, v161
v_and_b32 v83, 0xffff0000, v161
v_lshlrev_b32 v84, 16, v162
v_and_b32 v85, 0xffff0000, v162
v_lshlrev_b32 v86, 16, v163
v_and_b32 v87, 0xffff0000, v163
v_pk_add_f32 v[80:81], v[48:49], v[80:81]
v_pk_add_f32 v[82:83], v[50:51], v[82:83]
v_pk_add_f32 v[84:85], v[52:53], v[84:85]
v_pk_add_f32 v[86:87], v[54:55], v[86:87]
K 160,80,81
K 161,82,83
K 162,84,85
K 163,86,87
v_lshlrev_b32 v80, 16, v160
v_and_b32 v81, 0xffff0000, v160
v_lshlrev_b32 v82, 16, v161
v_and_b32 v83, 0xffff0000, v161
v_lshlrev_b32 v84, 16, v162
v_and_b32 v85, 0xffff0000, v162
v_lshlrev_b32 v86, 16, v163
v_and_b32 v87, 0xffff0000, v163
v_pk_add_f32 v[80:81], v[56:57], v[80:81]
v_pk_add_f32 v[82:83], v[58:59], v[82:83]
v_pk_add_f32 v[84:85], v[60:61], v[84:85]
)"
R"(v_pk_add_f32 v[86:87], v[62:63], v[86:87]
K 80,80,81
K 81,82,83
K 82,84,85
K 83,86,87
buffer_store_dwordx4 v[80:83], v20, s[20:23], s73 offen nt
K 160,88,89
K 161,90,91
K 162,92,93
K 163,94,95
v_lshlrev_b32 v88, 16, v160
v_and_b32 v89, 0xffff0000, v160
v_lshlrev_b32 v90, 16, v161
v_and_b32 v91, 0xffff0000, v161
v_lshlrev_b32 v92, 16, v162
v_and_b32 v93, 0xffff0000, v162
v_lshlrev_b32 v94, 16, v163
v_and_b32 v95, 0xffff0000, v163
v_pk_add_f32 v[88:89], v[64:65], v[88:89]
v_pk_add_f32 v[90:91], v[66:67], v[90:91]
v_pk_add_f32 v[92:93], v[68:69], v[92:93]
v_pk_add_f32 v[94:95], v[70:71], v[94:95]
K 160,88,89
K 161,90,91
K 162,92,93
K 163,94,95
v_lshlrev_b32 v88, 16, v160
v_and_b32 v89, 0xffff0000, v160
v_lshlrev_b32 v90, 16, v161
v_and_b32 v91, 0xffff0000, v161
v_lshlrev_b32 v92, 16, v162
v_and_b32 v93, 0xffff0000, v162
v_lshlrev_b32 v94, 16, v163
v_and_b32 v95, 0xffff0000, v163
v_pk_add_f32 v[88:89], v[72:73], v[88:89]
v_pk_add_f32 v[90:91], v[74:75], v[90:91]
v_pk_add_f32 v[92:93], v[76:77], v[92:93]
v_pk_add_f32 v[94:95], v[78:79], v[94:95]
K 88,88,89
K 89,90,91
K 90,92,93
K 91,94,95
buffer_store_dwordx4 v[88:91], v20, s[20:23], s74 offen nt
s_branch .LL_107%=
.LL_180%=:
s_waitcnt vmcnt(0)
Zvva 80,4,32
Zvva 84,8,32
Zvva 88,4,216
Zvva 92,8,216
Mvva 80,4,36
Mvva 84,8,36
Mvva 88,4,220
Mvva 92,8,220
Mvva 80,4,40
Mvva 84,8,40
Mvva 88,4,224
Mvva 92,8,224
Mvva 80,4,44
Mvva 84,8,44
Mvva 88,4,228
Mvva 92,8,228
Mvva 80,4,48
Mvva 84,8,48
Mvva 88,4,232
Mvva 92,8,232
Mvva 80,4,52
Mvva 84,8,52
Mvva 88,4,236
Mvva 92,8,236
Mvva 80,4,56
Mvva 84,8,56
Mvva 88,4,240
Mvva 92,8,240
Mvva 80,4,60
Mvva 84,8,60
Mvva 88,4,244
Mvva 92,8,244
s_nop 4
K 160,80,81
K 161,82,83
K 162,84,85
K 163,86,87
v_lshlrev_b32 v80, 16, v160
v_and_b32 v81, 0xffff0000, v160
v_lshlrev_b32 v82, 16, v161
v_and_b32 v83, 0xffff0000, v161
v_lshlrev_b32 v84, 16, v162
v_and_b32 v85, 0xffff0000, v162
v_lshlrev_b32 v86, 16, v163
v_and_b32 v87, 0xffff0000, v163
v_pk_add_f32 v[80:81], v[48:49], v[80:81]
v_pk_add_f32 v[82:83], v[50:51], v[82:83]
v_pk_add_f32 v[84:85], v[52:53], v[84:85]
v_pk_add_f32 v[86:87], v[54:55], v[86:87]
K 160,80,81
K 161,82,83
K 162,84,85
K 163,86,87
v_lshlrev_b32 v80, 16, v160
v_and_b32 v81, 0xffff0000, v160
v_lshlrev_b32 v82, 16, v161
v_and_b32 v83, 0xffff0000, v161
v_lshlrev_b32 v84, 16, v162
v_and_b32 v85, 0xffff0000, v162
v_lshlrev_b32 v86, 16, v163
v_and_b32 v87, 0xffff0000, v163
v_pk_add_f32 v[80:81], v[56:57], v[80:81]
v_pk_add_f32 v[82:83], v[58:59], v[82:83]
v_pk_add_f32 v[84:85], v[60:61], v[84:85]
v_pk_add_f32 v[86:87], v[62:63], v[86:87]
K 80,80,81
K 81,82,83
K 82,84,85
K 83,86,87
buffer_store_dwordx4 v[80:83], v20, s[20:23], s73 offen nt
K 160,88,89
K 161,90,91
K 162,92,93
K 163,94,95
v_lshlrev_b32 v88, 16, v160
v_and_b32 v89, 0xffff0000, v160
v_lshlrev_b32 v90, 16, v161
v_and_b32 v91, 0xffff0000, v161
v_lshlrev_b32 v92, 16, v162
v_and_b32 v93, 0xffff0000, v162
v_lshlrev_b32 v94, 16, v163
v_and_b32 v95, 0xffff0000, v163
v_pk_add_f32 v[88:89], v[64:65], v[88:89]
v_pk_add_f32 v[90:91], v[66:67], v[90:91]
v_pk_add_f32 v[92:93], v[68:69], v[92:93]
v_pk_add_f32 v[94:95], v[70:71], v[94:95]
K 160,88,89
K 161,90,91
K 162,92,93
K 163,94,95
v_lshlrev_b32 v88, 16, v160
v_and_b32 v89, 0xffff0000, v160
v_lshlrev_b32 v90, 16, v161
v_and_b32 v91, 0xffff0000, v161
v_lshlrev_b32 v92, 16, v162
v_and_b32 v93, 0xffff0000, v162
v_lshlrev_b32 v94, 16, v163
v_and_b32 v95, 0xffff0000, v163
v_pk_add_f32 v[88:89], v[72:73], v[88:89]
v_pk_add_f32 v[90:91], v[74:75], v[90:91]
v_pk_add_f32 v[92:93], v[76:77], v[92:93]
v_pk_add_f32 v[94:95], v[78:79], v[94:95]
K 88,88,89
K 89,90,91
K 90,92,93
K 91,94,95
buffer_store_dwordx4 v[88:91], v20, s[20:23], s74 offen nt
.LL_107%=:
s_lshl_b32 s80, s15, 2
s_add_u32 s80, s80, s14
s_cmp_eq_u32 s80, 4
s_cbranch_scc0 .LL_182%=
v_mov_b64 v[28:29], s[12:13]
s_mov_b64 exec, 1
buffer_store_dwordx2 v[28:29], off, s[8:11], 0 sc1
s_mov_b64 exec, -1
.LL_182%=:
s_branch .Lend_183%=
.Lup_100%=:
s_waitcnt vmcnt(3)
s_mov_b32 s84, 0
.LL_102%=:
v_min3_u32 v176, v161, v163, v165
v_max3_u32 v177, v161, v163, v165
v_min3_u32 v176, v176, v167, v169
v_max3_u32 v177, v177, v167, v169
v_min3_u32 v176, v176, v171, v173
v_max3_u32 v177, v177, v171, v173
v_min_u32 v176, v176, v175
v_max_u32 v177, v177, v175
v_cmp_eq_u32 vcc, s13, v176
v_cmp_eq_u32_e64 s[86:87], s13, v177
s_and_b64 vcc, vcc, s[86:87]
s_cmp_eq_u64 vcc, -1
s_cbranch_scc1 .LL_103%=
s_add_u32 s84, s84, 1
s_cmp_lt_u32 s84, 1048576
s_cbranch_scc1 .LL_104%=
.LL_103%=:
v_readlane_b32 s70, v12, s69
v_bfe_u32 v2, v1, 4, 4
s_nop 2
s_lshr_b32 s80, s70, 4
s_and_b32 s70, s70, 15
v_cmp_eq_u32 vcc, s70, v2
s_cmp_eq_u32 s80, s61
s_cselect_b64 s[86:87], vcc, 0
s_cselect_b64 s[88:89], 0, vcc
s_bitcmp1_b32 s14, 0
s_cselect_b64 vcc, s[88:89], s[86:87]
v_lshlrev_b32 v176, 16, v160
v_and_b32 v177, 0xffff0000, v160
v_lshlrev_b32 v178, 16, v162
v_and_b32 v179, 0xffff0000, v162
v_lshlrev_b32 v180, 16, v164
v_and_b32 v181, 0xffff0000, v164
v_lshlrev_b32 v182, 16, v166
v_and_b32 v183, 0xffff0000, v166
A 176,176,40
A 177,177,41
A 178,178,42
A 179,179,43
A 180,180,44
A 181,181,45
A 182,182,46
A 183,183,47
K 184,176,177
K 185,178,179
K 186,180,181
K 187,182,183
v_lshlrev_b32 v176, 16, v184
v_and_b32 v177, 0xffff0000, v184
v_lshlrev_b32 v178, 16, v185
v_and_b32 v179, 0xffff0000, v185
v_lshlrev_b32 v180, 16, v186
v_and_b32 v181, 0xffff0000, v186
v_lshlrev_b32 v182, 16, v187
v_and_b32 v183, 0xffff0000, v187
v_cndmask_b32_e64 v40, v40, v176, s[86:87]
v_cndmask_b32_e64 v41, v41, v177, s[86:87]
v_cndmask_b32_e64 v42, v42, v178, s[86:87]
v_cndmask_b32_e64 v43, v43, v179, s[86:87]
v_cndmask_b32_e64 v44, v44, v180, s[86:87]
v_cndmask_b32_e64 v45, v45, v181, s[86:87]
v_cndmask_b32_e64 v46, v46, v182, s[86:87]
v_cndmask_b32_e64 v47, v47, v183, s[86:87]
v_lshlrev_b32 v176, 16, v160
v_and_b32 v177, 0xffff0000, v160
v_lshlrev_b32 v178, 16, v162
v_and_b32 v179, 0xffff0000, v162
v_lshlrev_b32 v180, 16, v164
v_and_b32 v181, 0xffff0000, v164
v_lshlrev_b32 v182, 16, v166
v_and_b32 v183, 0xffff0000, v166
A 176,176,56
A 177,177,57
A 178,178,58
A 179,179,59
A 180,180,60
A 181,181,61
A 182,182,62
A 183,183,63
K 184,176,177
K 185,178,179
K 186,180,181
K 187,182,183
v_lshlrev_b32 v176, 16, v184
v_and_b32 v177, 0xffff0000, v184
v_lshlrev_b32 v178, 16, v185
v_and_b32 v179, 0xffff0000, v185
v_lshlrev_b32 v180, 16, v186
v_and_b32 v181, 0xffff0000, v186
v_lshlrev_b32 v182, 16, v187
v_and_b32 v183, 0xffff0000, v187
v_cndmask_b32_e64 v56, v56, v176, s[88:89]
v_cndmask_b32_e64 v57, v57, v177, s[88:89]
v_cndmask_b32_e64 v58, v58, v178, s[88:89]
v_cndmask_b32_e64 v59, v59, v179, s[88:89]
v_cndmask_b32_e64 v60, v60, v180, s[88:89]
v_cndmask_b32_e64 v61, v61, v181, s[88:89]
v_cndmask_b32_e64 v62, v62, v182, s[88:89]
v_cndmask_b32_e64 v63, v63, v183, s[88:89]
v_lshlrev_b32 v176, 16, v168
v_and_b32 v177, 0xffff0000, v168
v_lshlrev_b32 v178, 16, v170
v_and_b32 v179, 0xffff0000, v170
v_lshlrev_b32 v180, 16, v172
v_and_b32 v181, 0xffff0000, v172
v_lshlrev_b32 v182, 16, v174
v_and_b32 v183, 0xffff0000, v174
A 176,176,72
A 177,177,73
A 178,178,74
A 179,179,75
A 180,180,76
A 181,181,77
A 182,182,78
A 183,183,79
K 184,176,177
K 185,178,179
K 186,180,181
K 187,182,183
v_lshlrev_b32 v176, 16, v184
v_and_b32 v177, 0xffff0000, v184
v_lshlrev_b32 v178, 16, v185
v_and_b32 v179, 0xffff0000, v185
v_lshlrev_b32 v180, 16, v186
v_and_b32 v181, 0xffff0000, v186
v_lshlrev_b32 v182, 16, v187
v_and_b32 v183, 0xffff0000, v187
v_cndmask_b32_e64 v72, v72, v176, vcc
v_cndmask_b32_e64 v73, v73, v177, vcc
v_cndmask_b32_e64 v74, v74, v178, vcc
v_cndmask_b32_e64 v75, v75, v179, vcc
v_cndmask_b32_e64 v76, v76, v180, vcc
v_cndmask_b32_e64 v77, v77, v181, vcc
v_cndmask_b32_e64 v78, v78, v182, vcc
v_cndmask_b32_e64 v79, v79, v183, vcc
s_bitset0_b32 s68, s69
s_cmp_eq_u32 s68, 0
s_cbranch_scc1 .Lupr_101%=
s_ff1_i32_b32 s69, s68
.LL_104%=:
s_cmp_eq_u32 s64, 0
s_cbranch_scc0 .LL_105%=
s_waitcnt vmcnt(0)
v_lshlrev_b32 v2, 2, v1
v_mov_b64 v[28:29], s[12:13]
s_mov_b64 exec, 3
buffer_store_dwordx2 v[28:29], v2, s[8:11], s53 offen offset:512 sc1
s_mov_b64 exec, -1
s_mov_b32 s64, 1
.LL_105%=:
s_mul_i32 s66, s69, 20
s_mul_i32 s67, s58, 5
s_add_u32 s66, s66, s67
s_lshl_b32 s66, s66, 7
s_add_u32 s66, s66, 98304
s_lshl_b32 s67, s14, 7
s_add_u32 s67, s67, s66
buffer_load_dwordx4 v[160:163], v3, s[8:11], s67 offen offset:0 sc1
buffer_load_dwordx4 v[164:167], v3, s[8:11], s67 offen offset:16 sc1
buffer_load_dwordx4 v[168:171], v3, s[8:11], s66 offen offset:512 sc1
buffer_load_dwordx4 v[172:175], v3, s[8:11], s66 offen offset:528 sc1
s_waitcnt vmcnt(0)
s_branch .LL_102%=
.Lspin_54%=:
s_mov_b32 s84, 0
.Llp_56%=:
s_sleep 2
v_mov_b32 v28, s74
buffer_load_dwordx2 v[28:29], v28, s[8:11], 0 offen offset:3584 sc1
s_waitcnt vmcnt(0)
s_add_u32 s84, s84, 1
v_xor_b32 v28, s12, v28
v_xor_b32 v29, s13, v29
v_lshrrev_b32 v248, 11, v28
v_or_b32 v29, v29, v248
v_cmp_eq_u32 vcc, 0, v29
s_or_b64 vcc, vcc, s[72:73]
s_cmp_eq_u64 vcc, -1
s_cbranch_scc1 .Lok_55%=
s_cmp_lt_u32 s84, 1048576
s_cbranch_scc1 .Llp_56%=
s_branch .Lok_55%=
.Lspin_59%=:
s_mov_b32 s84, 0
.Llp_61%=:
s_sleep 2
v_bfe_u32 v28, v1, 4, 2
v_lshlrev_b32 v28, 3, v28
s_lshl_b32 s80, s56, 9
s_add_u32 s80, s80, 4096
buffer_load_dwordx2 v[28:29], v28, s[8:11], s80 offen sc1
s_waitcnt vmcnt(0)
s_add_u32 s84, s84, 1
v_cmp_eq_u64 vcc, s[12:13], v[28:29]
s_cmp_eq_u64 vcc, -1
s_cbranch_scc1 .Lok_60%=
s_cmp_lt_u32 s84, 1048576
s_cbranch_scc1 .Llp_61%=
s_branch .Lok_60%=
.Lspin_62%=:
s_mov_b32 s84, 0
.Llp_64%=:
s_sleep 2
v_bfe_u32 v28, v1, 4, 4
v_lshlrev_b32 v28, 3, v28
s_lshl_b32 s80, s56, 9
s_add_u32 s80, s80, 4096
buffer_load_dwordx2 v[28:29], v28, s[8:11], s80 offen sc1
s_waitcnt vmcnt(0)
s_add_u32 s84, s84, 1
v_cmp_eq_u64 vcc, s[12:13], v[28:29]
s_cmp_eq_u64 vcc, -1
s_cbranch_scc1 .Lok_63%=
s_cmp_lt_u32 s84, 1048576
s_cbranch_scc1 .Llp_64%=
s_branch .Lok_63%=
.Lspin_65%=:
s_mov_b32 s84, 0
.Llp_67%=:
s_sleep 2
v_bfe_u32 v28, v1, 4, 4
v_lshlrev_b32 v28, 3, v28
s_lshl_b32 s80, s56, 9
s_add_u32 s80, s80, 4224
buffer_load_dwordx2 v[28:29], v28, s[8:11], s80 offen sc1
s_waitcnt vmcnt(0)
s_add_u32 s84, s84, 1
v_cmp_eq_u64 vcc, s[12:13], v[28:29]
s_cmp_eq_u64 vcc, -1
s_cbranch_scc1 .Lok_66%=
s_cmp_lt_u32 s84, 1048576
)"
R"(s_cbranch_scc1 .Llp_67%=
s_branch .Lok_66%=
.Lspin_68%=:
s_mov_b32 s84, 0
.Llp_70%=:
s_sleep 2
v_bfe_u32 v28, v1, 4, 4
v_lshlrev_b32 v28, 3, v28
s_lshl_b32 s80, s56, 9
s_add_u32 s80, s80, 4352
buffer_load_dwordx2 v[28:29], v28, s[8:11], s80 offen sc1
s_waitcnt vmcnt(0)
s_add_u32 s84, s84, 1
v_cmp_eq_u64 vcc, s[12:13], v[28:29]
s_cmp_eq_u64 vcc, -1
s_cbranch_scc1 .Lok_69%=
s_cmp_lt_u32 s84, 1048576
s_cbranch_scc1 .Llp_70%=
s_branch .Lok_69%=
.Lspin_71%=:
s_mov_b32 s84, 0
.Llp_73%=:
s_sleep 2
v_bfe_u32 v28, v1, 4, 4
v_lshlrev_b32 v28, 3, v28
s_lshl_b32 s80, s56, 9
s_add_u32 s80, s80, 4480
buffer_load_dwordx2 v[28:29], v28, s[8:11], s80 offen sc1
s_waitcnt vmcnt(0)
s_add_u32 s84, s84, 1
v_cmp_eq_u64 vcc, s[12:13], v[28:29]
s_cmp_eq_u64 vcc, -1
s_cbranch_scc1 .Lok_72%=
s_cmp_lt_u32 s84, 1048576
s_cbranch_scc1 .Llp_73%=
s_branch .Lok_72%=
.Lspin_74%=:
s_mov_b32 s84, 0
.Llp_76%=:
s_sleep 2
buffer_load_dwordx2 v[8:9], v4, s[8:11], 0 offen offset:2560 sc1
buffer_load_dwordx2 v[10:11], v5, s[8:11], 0 offen offset:3072 sc1
buffer_load_dwordx2 v[12:13], v6, s[8:11], 0 offen offset:3584 sc1
s_waitcnt vmcnt(0)
s_add_u32 s84, s84, 1
v_cmp_eq_u64 vcc, s[12:13], v[8:9]
v_cmp_eq_u64_e64 s[86:87], s[12:13], v[10:11]
s_and_b64 vcc, vcc, s[86:87]
s_or_b64 vcc, vcc, s[20:21]
s_cmp_eq_u64 vcc, -1
s_cbranch_scc1 .Lok_75%=
s_cmp_lt_u32 s84, 1048576
s_cbranch_scc1 .Llp_76%=
s_branch .Lok_75%=
.Lmk_77%=:
s_nop 7
v_cndmask_b32_e64 v168, v241, v168, s[72:73]
v_cndmask_b32_e64 v169, v241, v169, s[74:75]
v_cndmask_b32_e64 v170, v241, v170, s[76:77]
v_cndmask_b32_e64 v171, v241, v171, s[78:79]
v_cndmask_b32_e64 v188, v241, v188, s[72:73]
v_cndmask_b32_e64 v189, v241, v189, s[74:75]
v_cndmask_b32_e64 v190, v241, v190, s[76:77]
v_cndmask_b32_e64 v191, v241, v191, s[78:79]
v_mov_b32 v184, v241
v_mov_b32 v185, v241
v_mov_b32 v186, v241
v_mov_b32 v187, v241
s_branch .Lmkr_78%=
.Lspin_81%=:
s_mov_b32 s84, 0
.Llp_83%=:
s_sleep 2
v_bfe_u32 v28, v1, 4, 2
v_lshlrev_b32 v28, 3, v28
buffer_load_dwordx2 v[28:29], v28, s[8:11], s55 offen sc1
s_waitcnt vmcnt(0)
s_add_u32 s84, s84, 1
v_cmp_eq_u64 vcc, s[12:13], v[28:29]
s_cmp_eq_u64 vcc, -1
s_cbranch_scc1 .Lok_82%=
s_cmp_lt_u32 s84, 1048576
s_cbranch_scc1 .Llp_83%=
s_branch .Lok_82%=
.LL_86%=:
s_mov_b32 s84, 0
.LL_88%=:
s_sleep 2
buffer_load_dwordx2 v[12:13], v27, s[8:11], 0 offen offset:3584 sc1
s_waitcnt vmcnt(0)
s_add_u32 s84, s84, 1
v_xor_b32 v12, s12, v12
v_xor_b32 v13, s13, v13
v_lshrrev_b32 v3, 11, v12
v_or_b32 v13, v13, v3
v_cmp_eq_u32 vcc, 0, v13
s_cmp_eq_u64 vcc, -1
s_cbranch_scc1 .LL_87%=
s_cmp_lt_u32 s84, 1048576
s_cbranch_scc1 .LL_88%=
s_branch .LL_87%=
.Lup_84%=:
v_lshrrev_b32 v3, 8, v1
v_lshlrev_b32 v3, 5, v3
s_ff1_i32_b32 s69, s68
s_mul_i32 s66, s69, 20
s_mul_i32 s67, s58, 5
s_add_u32 s66, s66, s67
s_lshl_b32 s66, s66, 7
s_add_u32 s66, s66, 98304
s_lshl_b32 s67, s14, 7
s_add_u32 s67, s67, s66
buffer_load_dwordx4 v[160:163], v3, s[8:11], s67 offen offset:0 sc1
buffer_load_dwordx4 v[164:167], v3, s[8:11], s67 offen offset:16 sc1
buffer_load_dwordx4 v[168:171], v3, s[8:11], s66 offen offset:512 sc1
buffer_load_dwordx4 v[172:175], v3, s[8:11], s66 offen offset:528 sc1
s_branch .Lupr_85%=
.Lspin_89%=:
s_mov_b32 s84, 0
.Llp_91%=:
s_sleep 2
v_bfe_u32 v28, v1, 4, 2
v_lshlrev_b32 v28, 3, v28
buffer_load_dwordx2 v[28:29], v28, s[8:11], s54 offen sc1
s_waitcnt vmcnt(0)
s_add_u32 s84, s84, 1
v_cmp_eq_u64 vcc, s[12:13], v[28:29]
s_cmp_eq_u64 vcc, -1
s_cbranch_scc1 .Lok_90%=
s_cmp_lt_u32 s84, 1048576
s_cbranch_scc1 .Llp_91%=
s_branch .Lok_90%=
.Lspin_92%=:
s_mov_b32 s84, 0
.Llp_94%=:
s_sleep 2
v_bfe_u32 v28, v1, 4, 2
v_lshlrev_b32 v28, 3, v28
buffer_load_dwordx2 v[28:29], v28, s[8:11], s55 offen sc1
s_waitcnt vmcnt(0)
s_add_u32 s84, s84, 1
v_cmp_eq_u64 vcc, s[12:13], v[28:29]
s_cmp_eq_u64 vcc, -1
s_cbranch_scc1 .Lok_93%=
s_cmp_lt_u32 s84, 1048576
s_cbranch_scc1 .Llp_94%=
s_branch .Lok_93%=
.LL_97%=:
s_mov_b32 s84, 0
.LL_99%=:
s_sleep 2
buffer_load_dwordx2 v[12:13], v27, s[8:11], 0 offen offset:3584 sc1
s_waitcnt vmcnt(0)
s_add_u32 s84, s84, 1
v_xor_b32 v12, s12, v12
v_xor_b32 v13, s13, v13
v_lshrrev_b32 v3, 11, v12
v_or_b32 v13, v13, v3
v_cmp_eq_u32 vcc, 0, v13
s_cmp_eq_u64 vcc, -1
s_cbranch_scc1 .LL_98%=
s_cmp_lt_u32 s84, 1048576
s_cbranch_scc1 .LL_99%=
s_branch .LL_98%=
.Lup_95%=:
v_lshrrev_b32 v3, 8, v1
v_lshlrev_b32 v3, 5, v3
s_ff1_i32_b32 s69, s68
s_mul_i32 s66, s69, 20
s_mul_i32 s67, s58, 5
s_add_u32 s66, s66, s67
s_lshl_b32 s66, s66, 7
s_add_u32 s66, s66, 98304
s_lshl_b32 s67, s14, 7
s_add_u32 s67, s67, s66
buffer_load_dwordx4 v[160:163], v3, s[8:11], s67 offen offset:0 sc1
buffer_load_dwordx4 v[164:167], v3, s[8:11], s67 offen offset:16 sc1
buffer_load_dwordx4 v[168:171], v3, s[8:11], s66 offen offset:512 sc1
buffer_load_dwordx4 v[172:175], v3, s[8:11], s66 offen offset:528 sc1
s_branch .Lupr_96%=
.Lend_183%=:
.Lend%=:
.purgem W
.purgem D
.purgem Ta
.purgem Ra
.purgem Zvaa
.purgem Mvaa
.purgem Zaaa
.purgem Maaa
.purgem A
.purgem U
.purgem K
.purgem Rv
.purgem Mvav
.purgem G
.purgem Dc
.purgem Zvav
.purgem Cvav
.purgem X
.purgem Cvaa
.purgem Maav
.purgem Mvvv
.purgem Zvva
.purgem Mvva
)"
    : : [ka] "s"(__builtin_amdgcn_kernarg_segment_ptr()), [bid] "s"(blockIdx.x), [tid] "v"(threadIdx.x)
    : "v1", "v2", "v3", "v4", "v5", "v6", "v7", "v8", "v9", "v10", "v11", "v12", "v13", "v14", "v15", "v16", "v17", "v18", "v19", "v20", "v21", "v22", "v23", "v24", "v25", "v26", "v27", "v28", "v29", "v30", "v31", "v32", "v33", "v34", "v35", "v36", "v37", "v38", "v39", "v40", "v41", "v42", "v43", "v44", "v45", "v46", "v47", "v48", "v49", "v50", "v51", "v52", "v53", "v54", "v55", "v56", "v57", "v58", "v59", "v60", "v61", "v62", "v63", "v64", "v65", "v66", "v67", "v68", "v69", "v70", "v71", "v72", "v73", "v74", "v75", "v76", "v77", "v78", "v79", "v80", "v81", "v82", "v83", "v84", "v85", "v86", "v87", "v88", "v89", "v90", "v91", "v92", "v93", "v94", "v95", "v96", "v97", "v98", "v99", "v100", "v101", "v102", "v103", "v104", "v105", "v106", "v107", "v108", "v109", "v110", "v111", "v112", "v113", "v114", "v115", "v116", "v117", "v118", "v119", "v120", "v121", "v122", "v123", "v124", "v125", "v126", "v127", "v128", "v129", "v130", "v131", "v132", "v133", "v134", "v135", "v136", "v137", "v138", "v139", "v140", "v141", "v142", "v143", "v144", "v145", "v146", "v147", "v148", "v149", "v150", "v151", "v152", "v153", "v154", "v155", "v156", "v157", "v158", "v159", "v160", "v161", "v162", "v163", "v164", "v165", "v166", "v167", "v168", "v169", "v170", "v171", "v172", "v173", "v174", "v175", "v176", "v177", "v178", "v179", "v180", "v181", "v182", "v183", "v184", "v185", "v186", "v187", "v188", "v189", "v190", "v191", "v192", "v193", "v194", "v195", "v196", "v197", "v198", "v199", "v200", "v201", "v202", "v203", "v204", "v205", "v206", "v207", "v208", "v209", "v210", "v211", "v212", "v213", "v214", "v215", "v216", "v217", "v218", "v219", "v220", "v221", "v222", "v223", "v224", "v225", "v226", "v227", "v228", "v229", "v230", "v231", "v232", "v233", "v234", "v235", "v236", "v237", "v238", "v239", "v240", "v241", "v242", "v243", "v244", "v245", "v246", "v247", "v248", "v249", "v250", "v251", "v252", "v253", "v254", "v255", "a0", "a1", "a2", "a3", "a4", "a5", "a6", "a7", "a8", "a9", "a10", "a11", "a12", "a13", "a14", "a15", "a16", "a17", "a18", "a19", "a20", "a21", "a22", "a23", "a24", "a25", "a26", "a27", "a28", "a29", "a30", "a31", "a32", "a33", "a34", "a35", "a36", "a37", "a38", "a39", "a40", "a41", "a42", "a43", "a44", "a45", "a46", "a47", "a48", "a49", "a50", "a51", "a52", "a53", "a54", "a55", "a56", "a57", "a58", "a59", "a60", "a61", "a62", "a63", "a64", "a65", "a66", "a67", "a68", "a69", "a70", "a71", "a72", "a73", "a74", "a75", "a76", "a77", "a78", "a79", "a80", "a81", "a82", "a83", "a84", "a85", "a86", "a87", "a88", "a89", "a90", "a91", "a92", "a93", "a94", "a95", "a96", "a97", "a98", "a99", "a100", "a101", "a102", "a103", "a104", "a105", "a106", "a107", "a108", "a109", "a110", "a111", "a112", "a113", "a114", "a115", "a116", "a117", "a118", "a119", "a120", "a121", "a122", "a123", "a124", "a125", "a126", "a127", "a128", "a129", "a130", "a131", "a132", "a133", "a134", "a135", "a136", "a137", "a138", "a139", "a140", "a141", "a142", "a143", "a144", "a145", "a146", "a147", "a148", "a149", "a150", "a151", "a152", "a153", "a154", "a155", "a156", "a157", "a158", "a159", "a160", "a161", "a162", "a163", "a164", "a165", "a166", "a167", "a168", "a169", "a170", "a171", "a172", "a173", "a174", "a175", "a176", "a177", "a178", "a179", "a180", "a181", "a182", "a183", "a184", "a185", "a186", "a187", "a188", "a189", "a190", "a191", "a192", "a193", "a194", "a195", "a196", "a197", "a198", "a199", "a200", "a201", "a202", "a203", "a204", "a205", "a206", "a207", "a208", "a209", "a210", "a211", "a212", "a213", "a214", "a215", "a216", "a217", "a218", "a219", "a220", "a221", "a222", "a223", "a224", "a225", "a226", "a227", "a228", "a229", "a230", "a231", "a232", "a233", "a234", "a235", "a236", "a237", "a238", "a239", "a240", "a241", "a242", "a243", "a244", "a245", "a246", "a247", "a248", "a249", "a250", "a251", "a252", "a253", "a254", "a255", "s4", "s5", "s6", "s7", "s8", "s9", "s10", "s11", "s12", "s13", "s14", "s15", "s16", "s17", "s18", "s19", "s20", "s21", "s22", "s23", "s24", "s25", "s26", "s27", "s28", "s29", "s30", "s31", "s32", "s33", "s34", "s35", "s36", "s37", "s38", "s39", "s40", "s41", "s42", "s43", "s44", "s45", "s46", "s47", "s48", "s49", "s50", "s51", "s52", "s53", "s54", "s55", "s56", "s57", "s58", "s59", "s60", "s61", "s62", "s63", "s64", "s65", "s66", "s67", "s68", "s69", "s70", "s71", "s72", "s73", "s74", "s75", "s76", "s77", "s78", "s79", "s80", "s81", "s82", "s83", "s84", "s85", "s86", "s87", "s88", "s89", "s90", "s91", "s92", "s93", "s94", "s95", "s96", "s97", "s98", "s99", "vcc", "scc", "memory"
  );
}
extern "C" void kernel_launch(void* const* d_in, const int* in_sizes, int n_in, void* d_out, int out_size, void* d_ws, size_t ws_size, hipStream_t stream) {
  (void)hipFuncSetAttribute((const void*)mega_kernel, hipFuncAttributeMaxDynamicSharedMemorySize, LDS_BYTES);
  mega_kernel<<<256, 256, LDS_BYTES, stream>>>((const bf16*)d_in[0], (const bf16*)d_in[1], (const bf16*)d_in[2], (const bf16*)d_in[3], (const bf16*)d_in[4], (const bf16*)d_in[5],
      (const bf16*)d_in[7], (const bf16*)d_in[8], (const bf16*)d_in[9], (const bf16*)d_in[10], (char*)d_ws + 0, (bf16*)d_out);
}
```

```cpp
#include <hip/hip_runtime.h>
#include <hip/hip_bf16.h>
typedef __hip_bfloat16 bf16;
constexpr int LDS_BYTES = 163840;
__global__ __launch_bounds__(256, 1) void mega_kernel(const bf16* x, const bf16* ckv, const bf16* wq, const bf16* wuk, const bf16* wuv, const bf16* wo,
    const bf16* w_r, const bf16* wg, const bf16* wu, const bf16* wd, char* ws, bf16* out) {
  asm volatile(
R"(.macro W n
s_waitcnt lgkmcnt(\n)
.endm
.macro D v,r,s,o
buffer_load_dwordx4 v\v, s[\r:\r+3], s\s offen offset:\o lds
.endm
.macro Ta d,a,o
ds_read_b64_tr_b16 a[\d:\d+1], v\a offset:\o
.endm
.macro Ra d,a,o
ds_read_b128 a[\d:\d+3], v\a offset:\o
.endm
.macro Zvaa d,a,b
v_mfma_f32_16x16x32_bf16 v[\d:\d+3], a[\a:\a+3], a[\b:\b+3], 0
.endm
.macro Mvaa d,a,b
v_mfma_f32_16x16x32_bf16 v[\d:\d+3], a[\a:\a+3], a[\b:\b+3], v[\d:\d+3]
.endm
.macro Zaaa d,a,b
v_mfma_f32_16x16x32_bf16 a[\d:\d+3], a[\a:\a+3], a[\b:\b+3], 0
.endm
.macro Maaa d,a,b
v_mfma_f32_16x16x32_bf16 a[\d:\d+3], a[\a:\a+3], a[\b:\b+3], a[\d:\d+3]
.endm
.macro A d,a,b
v_add_f32 v\d, v\a, v\b
.endm
.macro U d,a,b
v_mul_f32 v\d, v\a, v\b
.endm
.macro K d,a,b
v_cvt_pk_bf16_f32 v\d, v\a, v\b
.endm
.macro Rv d,a,o
ds_read_b128 v[\d:\d+3], v\a offset:\o
.endm
.macro Mvav d,a,b
v_mfma_f32_16x16x32_bf16 v[\d:\d+3], a[\a:\a+3], v[\b:\b+3], v[\d:\d+3]
.endm
.macro G d,a
v_accvgpr_read_b32 v\d, a\a
.endm
.macro Dc v,r,s,o
buffer_load_dwordx4 v\v, s[\r:\r+3], s\s offen offset:\o sc1 lds
.endm
.macro Zvav d,a,b
v_mfma_f32_16x16x32_bf16 v[\d:\d+3], a[\a:\a+3], v[\b:\b+3], 0
.endm
.macro Cvav d,a,b,c
v_mfma_f32_16x16x32_bf16 v[\d:\d+3], a[\a:\a+3], v[\b:\b+3], v[\c:\c+3]
.endm
.macro X d
v_exp_f32 v\d, v\d
.endm
.macro Cvaa d,a,b,c
v_mfma_f32_16x16x32_bf16 v[\d:\d+3], a[\a:\a+3], a[\b:\b+3], v[\c:\c+3]
.endm
.macro Maav d,a,b
v_mfma_f32_16x16x32_bf16 a[\d:\d+3], a[\a:\a+3], v[\b:\b+3], a[\d:\d+3]
.endm
.macro Mvvv d,a,b
v_mfma_f32_16x16x32_bf16 v[\d:\d+3], v[\a:\a+3], v[\b:\b+3], v[\d:\d+3]
.endm
.macro Zvva d,a,b
v_mfma_f32_16x16x32_bf16 v[\d:\d+3], v[\a:\a+3], a[\b:\b+3], 0
.endm
.macro Mvva d,a,b
v_mfma_f32_16x16x32_bf16 v[\d:\d+3], v[\a:\a+3], a[\b:\b+3], v[\d:\d+3]
.endm
s_load_dwordx16 s[16:31], %[ka], 0
s_load_dwordx8 s[32:39], %[ka], 64
s_getpc_b64 s[6:7]
.Lbeg%=:
s_mov_b32 s15, %[bid]
v_lshrrev_b32 v2, 6, %[tid]
v_and_b32 v100, 63, %[tid]
v_readfirstlane_b32 s14, v2
v_lshlrev_b32 v1, 4, v100
v_lshrrev_b32 v101, 4, v100
v_and_b32 v102, 15, v100
s_and_b32 s59, s15, 7
s_bfe_u32 s57, s15, 0x30003
s_lshr_b32 s58, s15, 6
s_lshl_b32 s56, s59, 2
s_add_u32 s56, s56, s58
s_lshr_b32 s80, s15, 3
s_lshl_b32 s80, s80, 2
s_add_u32 s80, s80, s14
s_lshl_b32 s80, s80, 11
v_min_u32 v146, 240, v1
v_lshl_add_u32 v146, v146, 3, s80
v_min_u32 v146, .Lend%=-.Lbeg%=-4, v146
global_load_dword v142, v146, s[6:7]
v_and_b32 v88, 0x1f0, v1
v_lshrrev_b32 v105, 1, v101
v_mul_u32_u24 v105, 1280, v105
v_add_u32 v28, v88, v105
s_lshl_b32 s85, s15, 2
s_add_u32 s85, s85, s14
s_mul_i32 s84, s85, 2560
v_and_b32 v105, 1, v101
v_lshl_or_b32 v29, v105, 31, v28
v_lshlrev_b32 v104, 1, v101
v_xor_b32 v104, v104, v102
v_lshlrev_b32 v104, 4, v104
v_lshl_add_u32 v16, v101, 13, v104
v_xor_b32 v105, 128, v16
v_add_u32 v17, 31744, v16
v_add_u32 v18, 63488, v105
v_add_u32 v19, 95232, v105
v_add_u32 v20, 131072, v16
v_add_u32 v21, 162816, v16
v_add_u32 v22, 194560, v105
v_add_u32 v23, 226304, v105
s_lshl_b32 s80, s14, 3
s_add_u32 s80, s80, s57
s_lshl_b32 s80, s80, 15
v_and_b32 v104, 7, v100
v_xor_b32 v104, v104, v101
v_lshlrev_b32 v104, 4, v104
v_lshrrev_b32 v105, 3, v100
v_lshl_add_u32 v104, v105, 10, v104
v_add_u32 v2, s80, v104
v_xor_b32 v105, 64, v2
v_add_u32 v3, 7168, v105
v_add_u32 v4, 14336, v2
v_add_u32 v5, 21504, v105
v_xor_b32 v6, 1835008, v2
v_xor_b32 v7, 1835008, v3
v_xor_b32 v8, 1835008, v4
v_xor_b32 v9, 1835008, v5
s_lshl_b32 s63, s14, 13
s_add_u32 s61, s63, 65536
s_and_b32 s80, s14, 1
s_lshr_b32 s81, s14, 1
s_lshl_b32 s62, s80, 14
s_lshl_b32 s82, s81, 13
s_add_u32 s62, s62, s82
s_lshl_b32 s81, s81, 18
s_lshl_b32 s83, s56, 8
s_add_u32 s81, s81, s83
s_mov_b32 s64, 0
s_cmp_eq_u32 s80, 0
W 0
s_cselect_b32 s44, s22, s24
s_cselect_b32 s45, s23, s25
s_cmp_eq_u32 s14, 3
s_cbranch_scc0 .LL_1%=
s_lshr_b32 s88, s15, 3
s_cmp_lt_u32 s88, 14
s_cbranch_scc0 .LL_1%=
v_lshlrev_b32 v104, 16, v1
s_mov_b64 s[86:87], s[36:37]
s_mov_b32 s89, 38797312
s_cmp_eq_u32 s88, 1
s_cselect_b32 s90, 39845888, 0
s_add_u32 s86, s86, s90
s_addc_u32 s87, s87, 0
s_cmp_eq_u32 s88, 2
s_cselect_b64 s[86:87], s[26:27], s[86:87]
s_cselect_b32 s89, 5242876, s89
s_cmp_eq_u32 s88, 3
s_cselect_b64 s[86:87], s[30:31], s[86:87]
s_cselect_b32 s89, 2211836, s89
s_cmp_eq_u32 s88, 4
s_cselect_b64 s[86:87], s[32:33], s[86:87]
s_cselect_b32 s89, 2211836, s89
s_cmp_eq_u32 s88, 5
s_cselect_b64 s[86:87], s[34:35], s[86:87]
s_cselect_b32 s89, 2211836, s89
s_cmp_eq_u32 s88, 6
s_cselect_b64 s[86:87], s[38:39], s[86:87]
s_cselect_b32 s89, 2621436, s89
s_cmp_eq_u32 s88, 7
s_cselect_b64 s[86:87], s[20:21], s[86:87]
s_cselect_b32 s89, 5242876, s89
s_cmp_eq_u32 s88, 8
s_cselect_b64 s[86:87], s[18:19], s[86:87]
s_cselect_b32 s89, 2097148, s89
s_cmp_eq_u32 s88, 9
s_cselect_b64 s[86:87], s[22:23], s[86:87]
s_cselect_b32 s89, 4194300, s89
s_cmp_eq_u32 s88, 10
s_cselect_b64 s[86:87], s[24:25], s[86:87]
s_cselect_b32 s89, 4194300, s89
s_cmp_eq_u32 s88, 11
s_cselect_b64 s[86:87], s[16:17], s[86:87]
s_cselect_b32 s89, 2621436, s89
s_cmp_eq_u32 s88, 12
s_cselect_b64 s[86:87], s[28:29], s[86:87]
s_cselect_b32 s89, 1276, s89
s_cmp_eq_u32 s88, 13
s_cselect_b64 s[86:87], s[6:7], s[86:87]
s_cselect_b32 s89, 0, s89
v_min_u32 v104, s89, v104
global_load_dword v142, v104, s[86:87] sc1
.LL_1%=:
s_mov_b32 s40, s18
s_and_b32 s41, s19, 0xffff
s_mov_b32 s42, 0x200000
s_mov_b32 s43, 0x20000
s_add_u32 s44, s44, s81
s_addc_u32 s45, s45, 0
s_and_b32 s45, s45, 0xffff
s_mov_b32 s46, 0x400000
s_mov_b32 s47, 0x20000
s_sub_u32 s46, s46, s81
s_load_dwordx2 s[12:13], s[36:37], 0
s_add_u32 m0, s61, 0
s_nop 0
D 2,40,64,0
D 3,40,64,1024
D 4,40,64,2048
D 5,40,64,3072
s_add_u32 m0, s61, 4096
s_nop 0
D 6,40,64,0
D 7,40,64,1024
D 8,40,64,2048
D 9,40,64,3072
s_lshl_b32 s65, s64, 12
s_add_u32 m0, s62, 0
s_nop 0
D 16,44,65,0
D 17,44,65,1024
D 18,44,65,2048
D 19,44,65,3072
s_add_u32 m0, s62, 4096
s_nop 0
D 20,44,65,0
D 21,44,65,1024
D 22,44,65,2048
D 23,44,65,3072
s_mov_b32 s52, s16
s_and_b32 s53, s17, 0xffff
s_mov_b32 s54, 2621440
s_mov_b32 s55, 0x20000
buffer_load_dwordx4 v[32:35], v28, s[52:55], s84 offen
buffer_load_dwordx4 v[36:39], v28, s[52:55], s84 offen offset:512
buffer_load_dwordx4 v[40:43], v29, s[52:55], s84 offen offset:1024
s_mov_b32 s52, s28
s_and_b32 s53, s29, 0xffff
s_mov_b32 s54, 1280
s_mov_b32 s55, 0x20000
buffer_load_dwordx4 v[44:47], v88, s[52:55], 0 offen offset:0
buffer_load_dwordx4 v[48:51], v88, s[52:55], 0 offen offset:512
buffer_load_dwordx4 v[52:55], v88, s[52:55], 0 offen offset:1024
s_mov_b32 s69, 0
s_cmp_eq_u32 s14, 2
s_cbranch_scc1 .LL_2%=
s_cmp_eq_u32 s14, 3
s_cbranch_scc1 .LL_3%=
s_lshl_b32 s80, s58, 3
s_add_u32 s80, s80, s57
s_lshl_b32 s80, s80, 15
s_lshl_b32 s82, s14, 7
s_add_u32 s80, s80, s82
s_add_u32 s80, s80, 256
v_lshl_add_u32 v104, v88, 6, s80
v_and_b32 v105, 512, v1
v_mul_u32_u24 v105, 3584, v105
v_xor_b32 v104, v104, v105
v_mov_b32 v140, s18
v_mov_b32 v141, s19
s_mov_b32 s68, 256
s_branch .LL_4%=
.LL_2%=:
v_lshrrev_b32 v104, 6, v1
v_lshlrev_b32 v104, 13, v104
v_and_b32 v105, 16, v1
v_lshl_add_u32 v104, v105, 3, v104
s_lshl_b32 s80, s57, 17
s_add_u32 s80, s80, s83
s_add_u32 s80, s80, 1048576
v_add_u32 v104, s80, v104
v_and_b32 v105, 32, v1
v_cmp_eq_u32 vcc, 0, v105
v_mov_b32 v140, s24
v_mov_b32 v141, s25
v_mov_b32 v105, s22
v_mov_b32 v106, s23
v_cndmask_b32 v140, v140, v105, vcc
v_cndmask_b32 v141, v141, v106, vcc
s_mov_b32 s68, 1048576
s_branch .LL_4%=
.LL_3%=:
v_lshrrev_b32 v104, 5, v1
v_lshlrev_b32 v104, 13, v104
v_and_b32 v105, 16, v1
v_lshl_add_u32 v104, v105, 3, v104
s_mul_i32 s80, s57, 655360
s_add_u32 s80, s80, s83
v_add_u32 v104, s80, v104
v_mov_b32 v140, s20
v_mov_b32 v141, s21
s_mov_b32 s68, 196608
.LL_4%=:
v_mov_b32 v105, 0
v_lshl_add_u64 v[140:141], v[104:105], 0, v[140:141]
s_add_u32 s64, s64, 128
s_and_b32 s64, s64, 1023
s_add_u32 m0, s61, 32768
s_nop 0
D 2,40,64,0
D 3,40,64,1024
D 4,40,64,2048
D 5,40,64,3072
s_add_u32 m0, s61, 36864
s_nop 0
D 6,40,64,0
D 7,40,64,1024
D 8,40,64,2048
D 9,40,64,3072
s_lshl_b32 s65, s64, 12
v_lshrrev_b32 v104, 1, v102
v_xor_b32 v104, v104, v101
v_lshlrev_b32 v104, 4, v104
v_lshl_add_u32 v104, v102, 7, v104
s_lshr_b32 s81, s14, 1
s_lshl_b32 s81, s81, 14
s_add_u32 s81, s81, 65536
v_add_u32 v26, s81, v104
v_xor_b32 v27, 64, v26
v_and_b32 v105, 1, v101
v_lshrrev_b32 v104, 2, v102
v_lshl_or_b32 v105, v105, 2, v104
v_lshl_add_u32 v104, v101, 3, v104
v_lshlrev_b32 v104, 8, v104
v_lshl_add_u32 v104, v105, 5, v104
v_and_b32 v105, 3, v102
v_lshl_add_u32 v10, v105, 3, v104
v_xor_b32 v11, 32, v10
v_xor_b32 v12, 64, v10
v_xor_b32 v13, 96, v10
v_xor_b32 v14, 128, v10
v_xor_b32 v15, 160, v10
v_xor_b32 v24, 192, v10
v_xor_b32 v25, 224, v10
.LL_8%=:
s_lshr_b32 s81, s15, 3
s_cmp_eq_u32 s14, 2
s_cbranch_scc1 .LL_5%=
s_cmp_eq_u32 s14, 3
s_cbranch_scc1 .LL_6%=
s_cmp_eq_u32 s14, 0
s_cselect_b64 s[76:77], s[30:31], s[32:33]
s_mov_b32 s72, 512
s_mov_b32 s73, 24
s_mul_i32 s74, s81, 69120
s_mul_i32 s82, s59, 8640
s_andn2_b32 s82, s82, 127
s_add_u32 s74, s74, s82
s_mov_b32 s75, 2211836
s_branch .LL_7%=
.LL_5%=:
s_mov_b64 s[76:77], s[34:35]
s_mov_b32 s72, 512
s_mov_b32 s73, 24
s_mul_i32 s74, s81, 8960
s_mul_i32 s82, s59, 286720
s_add_u32 s74, s74, s82
s_mov_b32 s75, 2211836
s_branch .LL_7%=
.LL_6%=:
s_mov_b64 s[76:77], s[26:27]
s_mov_b32 s72, 512
s_mov_b32 s73, 4
s_mul_i32 s74, s81, 20480
s_mul_i32 s82, s59, 655360
s_add_u32 s74, s74, s82
s_mov_b32 s75, 5242876
s_branch .LL_7%=
.LL_7%=:
s_mov_b32 s8, s36
s_and_b32 s9, s37, 0xffff
s_mov_b32 s10, 79691776
s_mov_b32 s11, 0x20000
s_add_u32 s48, s20, s83
s_addc_u32 s49, s21, 0
s_and_b32 s49, s49, 0xffff
s_mov_b32 s50, 5242880
s_mov_b32 s51, 0x20000
s_sub_u32 s50, s50, s83
s_lshl_b32 s80, s57, 1
s_and_b32 s81, s14, 1
s_add_u32 s80, s80, s81
s_sub_u32 s81, 31, s80
s_cmp_lt_u32 s14, 2
s_cselect_b32 s60, s80, s81
s_mul_i32 s28, s60, 81920
s_add_u32 s28, s28, 131072
s_add_u32 s4, s28, 10240
s_mov_b32 s80, 0
s_lshl_b32 s81, s80, 9
s_add_u32 s28, s28, s81
s_add_u32 s5, s80, s14
s_cmp_ge_u32 s5, 20
s_cselect_b32 s81, 20, 0
s_sub_u32 s5, s5, s81
s_and_b32 s80, s14, 1
s_mul_i32 s29, s80, 17172480
s_mul_i32 s80, s56, 536576
s_add_u32 s29, s29, s80
s_lshl_b32 s80, s57, 13
s_add_u32 s29, s29, s80
s_add_u32 s29, s29, 4194304
W 0
s_add_u32 s12, s12, 0x7f4a7c15
s_addc_u32 s13, s13, 0x9e3779b9
s_mov_b64 exec, 0
s_cmp_eq_u32 s14, 0
s_cbranch_scc1 .Lh_0_0%=
s_cmp_eq_u32 s14, 1
s_cbranch_scc1 .Lh_0_1%=
s_cmp_eq_u32 s14, 2
s_cbranch_scc1 .Lh_0_2%=
s_branch .Lh_0_3%=
.Lhr_9%=:
s_mov_b64 exec, -1
s_bitcmp1_b32 s14, 0
s_cbranch_scc1 .Lv_10%=
s_branch .LL_12%=
.p2align 12
.LL_12%=:
s_waitcnt vmcnt(14)
s_barrier
Ta 0,10,0
Ta 2,10,1024
Ra 32,26,0
Ta 4,10,8192
.Lh_0_0%=:
s_cbranch_execz .Lh_0_0_1%=
Ta 6,10,9216
Ra 128,27,0
Ra 132,27,2048
Ra 36,26,2048
Ra 40,26,4096
Ra 136,27,4096
Ra 140,27,6144
Ra 44,26,6144
Ra 48,26,8192
Ra 144,27,8192
W 6
Zvaa 160,0,32
s_add_u32 m0, s62, 32768
Ra 148,27,10240
)"
R"(Ra 52,26,10240
Ra 56,26,12288
Ra 152,27,12288
Ra 156,27,14336
Ra 60,26,14336
Mvaa 160,4,128
Ta 8,11,0
Ta 10,11,1024
Ta 12,11,8192
Ta 14,11,9216
Zvaa 192,4,132
Ta 16,12,0
Ta 18,12,1024
Mvaa 192,0,36
W 14
Ta 20,12,8192
Ta 22,12,9216
Zvaa 224,0,40
D 16,44,65,0
Mvaa 224,4,136
Zaaa 160,4,140
Maaa 160,0,44
W 12
Zaaa 192,0,48
D 17,44,65,1024
Maaa 192,4,144
Zaaa 224,4,148
s_waitcnt vmcnt(13)
Maaa 224,0,52
W 8
v_lshlrev_b32 v56, 16, v32
v_and_b32 v57, 0xffff0000, v32
v_lshlrev_b32 v58, 16, v33
v_and_b32 v59, 0xffff0000, v33
Zaaa 64,0,56
D 18,44,65,2048
v_lshlrev_b32 v60, 16, v34
v_and_b32 v61, 0xffff0000, v34
v_lshlrev_b32 v62, 16, v35
v_and_b32 v63, 0xffff0000, v35
Maaa 64,4,152
v_lshlrev_b32 v64, 16, v36
v_and_b32 v65, 0xffff0000, v36
v_lshlrev_b32 v66, 16, v37
v_and_b32 v67, 0xffff0000, v37
v_lshlrev_b32 v68, 16, v38
Zaaa 96,4,156
v_and_b32 v69, 0xffff0000, v38
v_lshlrev_b32 v70, 16, v39
v_and_b32 v71, 0xffff0000, v39
v_lshlrev_b32 v72, 16, v40
Maaa 96,0,60
W 4
v_and_b32 v73, 0xffff0000, v40
v_lshlrev_b32 v74, 16, v41
v_and_b32 v75, 0xffff0000, v41
v_lshlrev_b32 v76, 16, v42
v_and_b32 v77, 0xffff0000, v42
Zaaa 100,8,60
D 19,44,65,3072
v_lshlrev_b32 v78, 16, v43
v_and_b32 v79, 0xffff0000, v43
A 80,56,57
A 81,58,59
Maaa 100,12,156
s_add_u32 m0, s62, 36864
A 80,80,60
A 81,81,61
A 80,80,62
A 81,81,63
A 80,80,64
Zaaa 68,12,152
Ta 24,13,0
Ta 26,13,1024
A 81,81,65
A 80,80,66
A 81,81,67
A 80,80,68
Maaa 68,8,56
Ta 28,13,8192
Ta 30,13,9216
A 81,81,69
A 80,80,70
A 81,81,71
A 80,80,72
Zaaa 228,8,52
D 20,44,65,0
A 81,81,73
A 80,80,74
A 81,81,75
A 80,80,76
A 81,81,77
Maaa 228,12,148
A 80,80,78
A 81,81,79
A 80,80,81
s_nop 1
v_add_f32_dpp v80, v80, v80 quad_perm:[1,0,3,2] row_mask:0xf bank_mask:0xf
Zaaa 196,12,144
Maaa 196,8,48
v_add_f32_dpp v80, v80, v80 quad_perm:[2,3,0,1] row_mask:0xf bank_mask:0xf
s_nop 1
v_add_f32_dpp v80, v80, v80 row_half_mirror row_mask:0xf bank_mask:0xf
s_nop 1
v_add_f32_dpp v80, v80, v80 row_mirror row_mask:0xf bank_mask:0xf
v_mov_b32 v81, v80
s_nop 1
v_permlane16_swap_b32 v80, v81
A 80,80,81
Zaaa 164,8,44
D 21,44,65,1024
v_mul_f32 v84, 0x3acccccd, v80
s_mov_b32 s86, 0xffff
s_mov_b32 s87, 0xffff
v_sub_f32 v82, v56, v84
U 80,82,82
v_sub_f32 v83, v57, v84
Maaa 164,12,140
v_fmac_f32 v80, v83, v83
v_sub_f32 v82, v58, v84
v_fmac_f32 v80, v82, v82
v_sub_f32 v83, v59, v84
v_fmac_f32 v80, v83, v83
v_sub_f32 v82, v60, v84
Zvaa 228,12,136
v_fmac_f32 v80, v82, v82
v_sub_f32 v83, v61, v84
v_fmac_f32 v80, v83, v83
v_sub_f32 v82, v62, v84
v_fmac_f32 v80, v82, v82
Mvaa 228,8,40
v_sub_f32 v83, v63, v84
v_fmac_f32 v80, v83, v83
v_sub_f32 v82, v64, v84
v_fmac_f32 v80, v82, v82
Zvaa 196,8,36
D 22,44,65,2048
v_sub_f32 v83, v65, v84
v_fmac_f32 v80, v83, v83
v_sub_f32 v82, v66, v84
v_fmac_f32 v80, v82, v82
Mvaa 196,12,132
v_sub_f32 v83, v67, v84
v_fmac_f32 v80, v83, v83
v_sub_f32 v82, v68, v84
v_fmac_f32 v80, v82, v82
v_sub_f32 v83, v69, v84
Zvaa 164,12,128
v_fmac_f32 v80, v83, v83
v_sub_f32 v82, v70, v84
v_fmac_f32 v80, v82, v82
v_sub_f32 v83, v71, v84
Mvaa 164,8,32
W 4
v_fmac_f32 v80, v83, v83
v_sub_f32 v82, v72, v84
U 81,82,82
v_sub_f32 v83, v73, v84
v_fmac_f32 v81, v83, v83
Zvaa 168,16,32
D 23,44,65,3072
v_sub_f32 v82, v74, v84
v_fmac_f32 v81, v82, v82
v_sub_f32 v83, v75, v84
v_fmac_f32 v81, v83, v83
Mvaa 168,20,128
v_sub_f32 v82, v76, v84
v_fmac_f32 v81, v82, v82
v_sub_f32 v83, v77, v84
v_fmac_f32 v81, v83, v83
v_sub_f32 v82, v78, v84
Zvaa 200,20,132
Ta 0,14,0
Ta 2,14,1024
v_fmac_f32 v81, v82, v82
v_sub_f32 v83, v79, v84
v_fmac_f32 v81, v83, v83
v_cndmask_b32_e64 v81, 0, v81, s[86:87]
Mvaa 200,16,36
Ta 4,14,8192
Ta 6,14,9216
A 80,80,81
s_nop 1
v_add_f32_dpp v80, v80, v80 quad_perm:[1,0,3,2] row_mask:0xf bank_mask:0xf
s_nop 1
v_add_f32_dpp v80, v80, v80 quad_perm:[2,3,0,1] row_mask:0xf bank_mask:0xf
s_nop 1
v_add_f32_dpp v80, v80, v80 row_half_mirror row_mask:0xf bank_mask:0xf
Zvaa 232,16,40
global_load_dword v142, v[140:141], off
v_lshl_add_u64 v[140:141], v[140:141], 0, s[68:69]
Mvaa 232,20,136
v_add_f32_dpp v80, v80, v80 row_mirror row_mask:0xf bank_mask:0xf
v_mov_b32 v81, v80
s_nop 1
v_permlane16_swap_b32 v80, v81
A 80,80,81
v_mul_f32 v80, 0x3acccccd, v80
K 80,80,80
Zaaa 168,20,140
v_lshlrev_b32 v80, 16, v80
v_add_f32 v80, 0x3727c5ac, v80
K 80,80,80
v_lshlrev_b32 v80, 16, v80
v_sqrt_f32 v81, v80
Maaa 168,16,44
v_rcp_f32 v82, v81
v_fma_f32 v83, -v81, v81, v80
v_mul_f32 v82, 0.5, v82
v_fma_f32 v81, v83, v82, v81
K 86,81,81
Zaaa 200,16,48
v_lshlrev_b32 v86, 16, v86
v_rcp_f32 v87, v86
K 85,84,84
v_lshlrev_b32 v85, 16, v85
Maaa 200,20,144
s_waitcnt vmcnt(17)
v_sub_f32 v56, v56, v85
v_sub_f32 v57, v57, v85
K 80,56,57
Zaaa 232,20,148
v_lshlrev_b32 v56, 16, v80
v_and_b32 v57, 0xffff0000, v80
U 80,56,87
U 81,57,87
K 32,80,81
Maaa 232,16,52
v_lshlrev_b32 v80, 16, v32
v_and_b32 v81, 0xffff0000, v32
v_lshlrev_b32 v82, 16, v44
v_and_b32 v83, 0xffff0000, v44
Zaaa 72,16,56
U 84,80,82
v_fmac_f32 v84, v81, v83
v_sub_f32 v58, v58, v85
v_sub_f32 v59, v59, v85
Maaa 72,20,152
K 80,58,59
v_lshlrev_b32 v58, 16, v80
v_and_b32 v59, 0xffff0000, v80
U 80,58,87
U 81,59,87
Zaaa 104,20,156
K 33,80,81
v_lshlrev_b32 v80, 16, v33
v_and_b32 v81, 0xffff0000, v33
v_lshlrev_b32 v82, 16, v45
Maaa 104,16,60
W 4
v_and_b32 v83, 0xffff0000, v45
v_fmac_f32 v84, v80, v82
v_fmac_f32 v84, v81, v83
v_sub_f32 v60, v60, v85
v_sub_f32 v61, v61, v85
Zaaa 108,24,60
K 80,60,61
v_lshlrev_b32 v60, 16, v80
v_and_b32 v61, 0xffff0000, v80
U 80,60,87
Maaa 108,28,156
U 81,61,87
K 34,80,81
v_lshlrev_b32 v80, 16, v34
v_and_b32 v81, 0xffff0000, v34
v_lshlrev_b32 v82, 16, v46
Zaaa 76,28,152
Ta 8,15,0
Ta 10,15,1024
v_and_b32 v83, 0xffff0000, v46
v_fmac_f32 v84, v80, v82
v_fmac_f32 v84, v81, v83
v_sub_f32 v62, v62, v85
Maaa 76,24,56
Ta 12,15,8192
v_sub_f32 v63, v63, v85
K 80,62,63
v_lshlrev_b32 v62, 16, v80
v_and_b32 v63, 0xffff0000, v80
U 80,62,87
Zaaa 236,24,52
Ta 14,15,9216
U 81,63,87
K 35,80,81
v_lshlrev_b32 v80, 16, v35
v_and_b32 v81, 0xffff0000, v35
Maaa 236,28,148
v_lshlrev_b32 v82, 16, v47
v_and_b32 v83, 0xffff0000, v47
v_fmac_f32 v84, v80, v82
v_fmac_f32 v84, v81, v83
Zaaa 204,28,144
v_sub_f32 v64, v64, v85
v_sub_f32 v65, v65, v85
K 80,64,65
v_lshlrev_b32 v64, 16, v80
v_and_b32 v65, 0xffff0000, v80
Maaa 204,24,48
U 80,64,87
U 81,65,87
K 36,80,81
v_lshlrev_b32 v80, 16, v36
Zaaa 172,24,44
v_and_b32 v81, 0xffff0000, v36
v_lshlrev_b32 v82, 16, v48
v_and_b32 v83, 0xffff0000, v48
v_fmac_f32 v84, v80, v82
v_fmac_f32 v84, v81, v83
Maaa 172,28,140
v_sub_f32 v66, v66, v85
v_sub_f32 v67, v67, v85
K 80,66,67
v_lshlrev_b32 v66, 16, v80
Zvaa 236,28,136
v_and_b32 v67, 0xffff0000, v80
U 80,66,87
U 81,67,87
K 37,80,81
v_lshlrev_b32 v80, 16, v37
Mvaa 236,24,40
v_and_b32 v81, 0xffff0000, v37
v_lshlrev_b32 v82, 16, v49
v_and_b32 v83, 0xffff0000, v49
v_fmac_f32 v84, v80, v82
Zvaa 204,24,36
v_fmac_f32 v84, v81, v83
v_sub_f32 v68, v68, v85
v_sub_f32 v69, v69, v85
K 80,68,69
v_lshlrev_b32 v68, 16, v80
Mvaa 204,28,132
v_and_b32 v69, 0xffff0000, v80
U 80,68,87
U 81,69,87
K 38,80,81
Zvaa 172,28,128
v_lshlrev_b32 v80, 16, v38
v_and_b32 v81, 0xffff0000, v38
v_lshlrev_b32 v82, 16, v50
v_and_b32 v83, 0xffff0000, v50
Mvaa 172,24,32
W 4
v_fmac_f32 v84, v80, v82
v_fmac_f32 v84, v81, v83
v_sub_f32 v70, v70, v85
v_sub_f32 v71, v71, v85
K 80,70,71
Zvaa 176,0,32
v_lshlrev_b32 v70, 16, v80
v_and_b32 v71, 0xffff0000, v80
U 80,70,87
U 81,71,87
Mvaa 176,4,128
K 39,80,81
v_lshlrev_b32 v80, 16, v39
v_and_b32 v81, 0xffff0000, v39
v_lshlrev_b32 v82, 16, v51
v_and_b32 v83, 0xffff0000, v51
Zvaa 208,4,132
Ta 16,24,0
Ta 18,24,1024
v_fmac_f32 v84, v80, v82
v_fmac_f32 v84, v81, v83
v_sub_f32 v72, v72, v85
v_sub_f32 v73, v73, v85
Mvaa 208,0,36
Ta 20,24,8192
K 80,72,73
v_lshlrev_b32 v72, 16, v80
v_and_b32 v73, 0xffff0000, v80
U 80,72,87
U 81,73,87
Zvaa 240,0,40
Ta 22,24,9216
K 40,80,81
v_lshlrev_b32 v80, 16, v40
v_and_b32 v81, 0xffff0000, v40
v_lshlrev_b32 v82, 16, v52
Mvaa 240,4,136
v_and_b32 v83, 0xffff0000, v52
v_fmac_f32 v84, v80, v82
v_fmac_f32 v84, v81, v83
v_sub_f32 v74, v74, v85
Zaaa 176,4,140
v_sub_f32 v75, v75, v85
K 80,74,75
v_lshlrev_b32 v74, 16, v80
v_and_b32 v75, 0xffff0000, v80
U 80,74,87
Maaa 176,0,44
U 81,75,87
K 41,80,81
v_lshlrev_b32 v80, 16, v41
v_and_b32 v81, 0xffff0000, v41
Zaaa 208,0,48
v_lshlrev_b32 v82, 16, v53
v_and_b32 v83, 0xffff0000, v53
v_fmac_f32 v84, v80, v82
v_fmac_f32 v84, v81, v83
v_sub_f32 v76, v76, v85
)"
R"(Maaa 208,4,144
v_sub_f32 v77, v77, v85
K 80,76,77
v_lshlrev_b32 v76, 16, v80
v_and_b32 v77, 0xffff0000, v80
Zaaa 240,4,148
U 80,76,87
U 81,77,87
K 42,80,81
v_lshlrev_b32 v80, 16, v42
v_and_b32 v81, 0xffff0000, v42
Maaa 240,0,52
v_lshlrev_b32 v82, 16, v54
v_and_b32 v83, 0xffff0000, v54
v_fmac_f32 v84, v80, v82
v_fmac_f32 v84, v81, v83
Zaaa 80,0,56
v_sub_f32 v78, v78, v85
v_sub_f32 v79, v79, v85
K 80,78,79
v_lshlrev_b32 v78, 16, v80
v_and_b32 v79, 0xffff0000, v80
Maaa 80,4,152
U 80,78,87
U 81,79,87
K 43,80,81
v_lshlrev_b32 v80, 16, v43
Zaaa 112,4,156
v_and_b32 v81, 0xffff0000, v43
v_lshlrev_b32 v82, 16, v55
v_and_b32 v83, 0xffff0000, v55
v_fmac_f32 v84, v80, v82
Maaa 112,0,60
W 4
v_fmac_f32 v84, v81, v83
v_lshrrev_b32 v82, 9, v1
s_lshl_b32 s80, s14, 1
v_add_u32 v82, s80, v82
v_mul_u32_u24 v83, 1552, v82
Zaaa 116,8,60
Ta 24,25,0
Ta 26,25,1024
v_add_u32 v83, v83, v88
v_add_u32 v83, 131072, v83
ds_write_b128 v83, v[32:35] offset:0
ds_write_b128 v83, v[36:39] offset:512
Maaa 116,12,156
ds_write_b128 v83, v[40:43] offset:1024
v_add_f32_dpp v84, v84, v84 quad_perm:[1,0,3,2] row_mask:0xf bank_mask:0xf
s_nop 1
v_add_f32_dpp v84, v84, v84 quad_perm:[2,3,0,1] row_mask:0xf bank_mask:0xf
s_nop 1
v_add_f32_dpp v84, v84, v84 row_half_mirror row_mask:0xf bank_mask:0xf
s_nop 1
v_add_f32_dpp v84, v84, v84 row_mirror row_mask:0xf bank_mask:0xf
Zaaa 84,12,152
Ta 28,25,8192
Ta 30,25,9216
v_mov_b32 v81, v84
Maaa 84,8,56
Zaaa 244,8,52
v_permlane16_swap_b32 v84, v81
A 84,84,81
K 80,84,84
v_lshlrev_b32 v80, 16, v80
s_lshl_b32 s80, s15, 3
v_add_u32 v82, s80, v82
v_sub_u32 v83, 2047, v82
Maaa 244,12,148
v_ashrrev_i32 v81, 31, v80
v_or_b32 v81, 0x80000000, v81
v_xor_b32 v80, v80, v81
v_and_b32 v80, 0xffff0000, v80
v_or_b32 v80, v80, v83
v_and_b32 v82, 7, v82
Zaaa 212,12,144
v_lshlrev_b32 v82, 2, v82
v_add_u32 v82, 143488, v82
ds_write_b32 v82, v80
Maaa 212,8,48
Zaaa 180,8,44
Maaa 180,12,140
Zvaa 244,12,136
s_waitcnt vmcnt(1) lgkmcnt(0)
s_barrier
s_add_u32 s64, s64, 128
Mvaa 244,8,40
v_bfe_u32 v80, v1, 4, 3
Zvaa 212,8,36
v_mul_u32_u24 v81, 1552, v80
Mvaa 212,12,132
s_and_b32 s64, s64, 1023
v_lshrrev_b32 v82, 7, v1
Zvaa 180,12,128
v_lshl_add_u32 v81, v82, 4, v81
Mvaa 180,8,32
s_lshl_b32 s80, s14, 7
Zvaa 184,16,32
s_add_u32 m0, s61, 0
s_add_u32 s80, s80, 131072
Mvaa 184,20,128
v_add_u32 v81, s80, v81
Zvaa 216,20,132
D 2,40,64,0
ds_read_b128 v[32:35], v81
Mvaa 216,16,36
Ta 0,10,32768
Ta 2,10,33792
Zvaa 248,16,40
Rv 36,81,512
Ta 4,10,40960
Mvaa 248,20,136
s_and_b32 s80, s14, 2
s_lshl_b32 s80, s80, 7
Ta 6,10,41984
Zaaa 184,20,140
D 3,40,64,1024
v_subrev_u32 v81, s80, v81
Maaa 184,16,44
Rv 40,81,1024
Zaaa 216,16,48
v_lshlrev_b32 v80, 2, v80
Maaa 216,20,144
v_add_u32 v80, 143488, v80
Zaaa 248,20,148
D 4,40,64,2048
ds_read_b32 v84, v80
Maaa 248,16,52
v_mov_b32 v85, s13
Zaaa 88,16,56
v_lshrrev_b32 v83, 1, v1
Maaa 88,20,152
s_mul_i32 s82, s15, 10240
Zaaa 120,20,156
s_add_u32 s82, s82, 131072
D 5,40,64,3072
Maaa 120,16,60
s_add_u32 m0, s61, 4096
s_lshl_b32 s80, s14, 10
Zaaa 124,24,60
s_add_u32 s83, s82, s80
Ra 60,26,47104
Maaa 124,28,156
Ra 156,27,47104
Zaaa 92,28,152
D 6,40,64,0
Ra 152,27,45056
Maaa 92,24,56
Ta 8,11,32768
Ta 10,11,33792
Zaaa 252,24,52
Ra 56,26,45056
Ta 12,11,40960
Maaa 252,28,148
Ta 14,11,41984
Ra 52,26,43008
Zaaa 220,28,144
W 9
buffer_store_dwordx4 v[32:35], v1, s[8:11], s83 offen sc1
s_add_u32 s83, s83, 4096
Maaa 220,24,48
Ra 148,27,43008
Ra 144,27,40960
Zaaa 188,24,44
Ra 48,26,40960
Ra 44,26,38912
Maaa 188,28,140
Ra 140,27,38912
Zvaa 252,28,136
D 7,40,64,1024
Ra 136,27,36864
Mvaa 252,24,40
Ra 40,26,36864
Zvaa 220,24,36
Ra 36,26,34816
Mvaa 220,28,132
Ra 132,27,34816
Zvaa 188,28,128
buffer_store_dwordx4 v[36:39], v1, s[8:11], s83 offen sc1
Ra 128,27,32768
Mvaa 188,24,32
W 13
s_and_b32 s80, s80, 1024
s_add_u32 s83, s82, s80
Maaa 96,0,60
s_add_u32 s83, s83, 8192
Ra 32,26,32768
Maaa 96,4,156
Maaa 64,4,152
D 8,40,64,2048
Ta 16,12,32768
Maaa 64,0,56
W 9
Ta 18,12,33792
Ta 20,12,40960
Maaa 224,0,52
Ta 22,12,41984
Maaa 224,4,148
Maaa 192,4,144
D 9,40,64,3072
Maaa 192,0,48
W 8
s_lshl_b32 s65, s64, 12
s_add_u32 m0, s62, 0
Maaa 160,0,44
Maaa 160,4,140
Mvaa 224,4,136
buffer_store_dwordx4 v[40:43], v1, s[8:11], s83 offen sc1
Mvaa 224,0,40
W 4
Mvaa 192,0,36
Mvaa 192,4,132
Mvaa 160,4,128
D 16,44,65,0
Mvaa 160,0,32
Mvaa 164,8,32
Mvaa 164,12,128
Mvaa 196,12,132
D 17,44,65,1024
Ta 24,13,32768
Mvaa 196,8,36
Ta 26,13,33792
Ta 28,13,40960
Mvaa 228,8,40
Ta 30,13,41984
Mvaa 228,12,136
.Lh_0_1%=:
s_cbranch_execz .Lh_0_1_1%=
Maaa 164,12,140
D 18,44,65,2048
Maaa 164,8,44
Maaa 196,8,48
Maaa 196,12,144
Maaa 228,12,148
D 19,44,65,3072
Maaa 228,8,52
s_add_u32 m0, s62, 4096
Maaa 68,8,56
Maaa 68,12,152
Maaa 100,12,156
D 20,44,65,0
Maaa 100,8,60
W 4
Maaa 104,16,60
Maaa 104,20,156
Maaa 72,20,152
D 21,44,65,1024
Ta 0,14,32768
Maaa 72,16,56
Ta 2,14,33792
Ta 4,14,40960
Maaa 232,16,52
Ta 6,14,41984
Maaa 232,20,148
Maaa 200,20,144
D 22,44,65,2048
Maaa 200,16,48
Maaa 168,16,44
Maaa 168,20,140
Mvaa 232,20,136
D 23,44,65,3072
Mvaa 232,16,40
Mvaa 200,16,36
Mvaa 200,20,132
Mvaa 168,20,128
global_load_dword v142, v[140:141], off
v_lshl_add_u64 v[140:141], v[140:141], 0, s[68:69]
Mvaa 168,16,32
W 4
Mvaa 172,24,32
Mvaa 172,28,128
Mvaa 204,28,132
Ta 8,15,32768
Ta 10,15,33792
Mvaa 204,24,36
Ta 12,15,40960
Ta 14,15,41984
Mvaa 236,24,40
Mvaa 236,28,136
Maaa 172,28,140
Maaa 172,24,44
Maaa 204,24,48
Maaa 204,28,144
Maaa 236,28,148
Maaa 236,24,52
Maaa 76,24,56
Maaa 76,28,152
Maaa 108,28,156
Maaa 108,24,60
W 4
Maaa 112,0,60
Maaa 112,4,156
Maaa 80,4,152
Ta 16,24,32768
Ta 18,24,33792
Maaa 80,0,56
Ta 20,24,40960
Ta 22,24,41984
Maaa 240,0,52
Maaa 240,4,148
Maaa 208,4,144
Maaa 208,0,48
Maaa 176,0,44
Maaa 176,4,140
Mvaa 240,4,136
Mvaa 240,0,40
Mvaa 208,0,36
Mvaa 208,4,132
Mvaa 176,4,128
Mvaa 176,0,32
W 4
Mvaa 180,8,32
Ta 24,25,32768
Ta 26,25,33792
Mvaa 180,12,128
Mvaa 212,12,132
Ta 28,25,40960
Ta 30,25,41984
Mvaa 212,8,36
Mvaa 244,8,40
Mvaa 244,12,136
Maaa 180,12,140
Maaa 180,8,44
Maaa 212,8,48
Maaa 212,12,144
Maaa 244,12,148
s_waitcnt vmcnt(1) lgkmcnt(0)
s_barrier
s_cmp_eq_u32 s14, 0
s_cbranch_scc0 .Lpubr_14%=
s_lshl_b32 s80, s15, 6
s_add_u32 s80, s80, 40960
s_mov_b64 exec, 0xff
buffer_store_dwordx2 v[84:85], v83, s[8:11], s80 offen sc1
s_mov_b64 exec, -1
.Lpubr_14%=:
Maaa 244,8,52
s_add_u32 s64, s64, 128
Maaa 84,8,56
Maaa 84,12,152
s_and_b32 s64, s64, 1023
Maaa 116,12,156
Maaa 116,8,60
Maaa 120,16,60
s_add_u32 m0, s61, 32768
Maaa 120,20,156
Maaa 88,20,152
D 2,40,64,0
Ta 0,10,0
Maaa 88,16,56
Ta 2,10,1024
Ta 4,10,8192
Maaa 248,16,52
Ta 6,10,9216
Maaa 248,20,148
Maaa 216,20,144
D 3,40,64,1024
Maaa 216,16,48
Maaa 184,16,44
Maaa 184,20,140
Mvaa 248,20,136
D 4,40,64,2048
Mvaa 248,16,40
Mvaa 216,16,36
Mvaa 216,20,132
Mvaa 184,20,128
D 5,40,64,3072
Mvaa 184,16,32
s_add_u32 m0, s61, 36864
Mvaa 188,24,32
Ra 32,26,0
Mvaa 188,28,128
Ra 128,27,0
Mvaa 220,28,132
D 6,40,64,0
Ra 132,27,2048
Mvaa 220,24,36
Ta 8,11,0
Ta 10,11,1024
Mvaa 252,24,40
Ra 36,26,2048
Ta 12,11,8192
Mvaa 252,28,136
Ta 14,11,9216
Ra 40,26,4096
Maaa 188,28,140
D 7,40,64,1024
Ra 136,27,4096
Maaa 188,24,44
Ra 140,27,6144
Ra 44,26,6144
Maaa 220,24,48
Ra 48,26,8192
Maaa 220,28,144
Ra 144,27,8192
Maaa 252,28,148
D 8,40,64,2048
Ra 148,27,10240
Maaa 252,24,52
Ra 52,26,10240
)"
R"(Maaa 92,24,56
Ra 56,26,12288
Maaa 92,28,152
Ra 152,27,12288
Maaa 124,28,156
D 9,40,64,3072
Ra 156,27,14336
Maaa 124,24,60
W 13
s_lshl_b32 s65, s64, 12
s_add_u32 m0, s62, 32768
Mvaa 160,0,32
Ra 60,26,14336
Mvaa 160,4,128
Mvaa 192,4,132
D 16,44,65,0
Ta 16,12,0
Mvaa 192,0,36
W 9
Ta 18,12,1024
Ta 20,12,8192
Mvaa 224,0,40
Ta 22,12,9216
Mvaa 224,4,136
Maaa 160,4,140
D 17,44,65,1024
Maaa 160,0,44
W 8
Maaa 192,0,48
Maaa 192,4,144
Maaa 224,4,148
D 18,44,65,2048
Maaa 224,0,52
W 4
Maaa 64,0,56
Maaa 64,4,152
Maaa 96,4,156
D 19,44,65,3072
Maaa 96,0,60
s_add_u32 m0, s62, 36864
Maaa 100,8,60
Maaa 100,12,156
Maaa 68,12,152
D 20,44,65,0
Ta 24,13,0
Maaa 68,8,56
Ta 26,13,1024
Ta 28,13,8192
Maaa 228,8,52
Ta 30,13,9216
Maaa 228,12,148
Maaa 196,12,144
D 21,44,65,1024
Maaa 196,8,48
Maaa 164,8,44
Maaa 164,12,140
Mvaa 228,12,136
D 22,44,65,2048
Mvaa 228,8,40
Mvaa 196,8,36
Mvaa 196,12,132
Mvaa 164,12,128
D 23,44,65,3072
Mvaa 164,8,32
W 4
Mvaa 168,16,32
Mvaa 168,20,128
Mvaa 200,20,132
global_load_dword v142, v[140:141], off
v_lshl_add_u64 v[140:141], v[140:141], 0, s[68:69]
Ta 0,14,0
Mvaa 200,16,36
Ta 2,14,1024
Ta 4,14,8192
Mvaa 232,16,40
Ta 6,14,9216
Mvaa 232,20,136
Maaa 168,20,140
Maaa 168,16,44
Maaa 200,16,48
Maaa 200,20,144
Maaa 232,20,148
Maaa 232,16,52
Maaa 72,16,56
Maaa 72,20,152
Maaa 104,20,156
Maaa 104,16,60
W 4
Maaa 108,24,60
Maaa 108,28,156
Maaa 76,28,152
Ta 8,15,0
Ta 10,15,1024
Maaa 76,24,56
Ta 12,15,8192
Ta 14,15,9216
Maaa 236,24,52
Maaa 236,28,148
Maaa 204,28,144
Maaa 204,24,48
Maaa 172,24,44
Maaa 172,28,140
Mvaa 236,28,136
Mvaa 236,24,40
Mvaa 204,24,36
Mvaa 204,28,132
Mvaa 172,28,128
Mvaa 172,24,32
W 4
Mvaa 176,0,32
Mvaa 176,4,128
Mvaa 208,4,132
Ta 16,24,0
Ta 18,24,1024
Mvaa 208,0,36
Ta 20,24,8192
Ta 22,24,9216
Mvaa 240,0,40
Mvaa 240,4,136
Maaa 176,4,140
Maaa 176,0,44
Maaa 208,0,48
Maaa 208,4,144
Maaa 240,4,148
Maaa 240,0,52
Maaa 80,0,56
Maaa 80,4,152
Maaa 112,4,156
Maaa 112,0,60
W 4
Maaa 116,8,60
Ta 24,25,0
Ta 26,25,1024
Maaa 116,12,156
Maaa 84,12,152
Ta 28,25,8192
Ta 30,25,9216
Maaa 84,8,56
Maaa 244,8,52
Maaa 244,12,148
Maaa 212,12,144
Maaa 212,8,48
Maaa 180,8,44
Maaa 180,12,140
Mvaa 244,12,136
s_waitcnt vmcnt(1) lgkmcnt(0)
s_barrier
s_add_u32 s64, s64, 128
Mvaa 244,8,40
Mvaa 212,8,36
Mvaa 212,12,132
s_and_b32 s64, s64, 1023
Mvaa 180,12,128
Mvaa 180,8,32
Mvaa 184,16,32
s_add_u32 m0, s61, 0
Mvaa 184,20,128
Mvaa 216,20,132
Ta 0,10,32768
Ta 2,10,33792
Mvaa 216,16,36
D 2,40,64,0
Ta 4,10,40960
Mvaa 248,16,40
Ta 6,10,41984
Mvaa 248,20,136
Maaa 184,20,140
Maaa 184,16,44
D 3,40,64,1024
Maaa 216,16,48
Maaa 216,20,144
Maaa 248,20,148
Maaa 248,16,52
D 4,40,64,2048
Maaa 88,16,56
Maaa 88,20,152
Maaa 120,20,156
Maaa 120,16,60
D 5,40,64,3072
Maaa 124,24,60
s_add_u32 m0, s61, 4096
Ra 60,26,47104
v_lshrrev_b32 v28, 1, v1
Maaa 124,28,156
s_lshl_b32 s80, s60, 9
Ra 156,27,47104
Maaa 92,28,152
s_add_u32 s80, s80, 40960
Ra 152,27,45056
Ta 8,11,32768
Maaa 92,24,56
D 6,40,64,0
Ta 10,11,33792
Maaa 252,24,52
Ra 56,26,45056
Ta 12,11,40960
Maaa 252,28,148
Ta 14,11,41984
Ra 52,26,43008
Maaa 220,28,144
Ra 148,27,43008
Ra 144,27,40960
Maaa 220,24,48
buffer_load_dwordx2 v[28:29], v28, s[8:11], s80 offen sc1
Ra 48,26,40960
Maaa 188,24,44
Ra 44,26,38912
Maaa 188,28,140
Ra 140,27,38912
Mvaa 252,28,136
Ra 136,27,36864
Mvaa 252,24,40
D 7,40,64,1024
Ra 40,26,36864
Mvaa 220,24,36
Ra 36,26,34816
Mvaa 220,28,132
Ra 132,27,34816
Mvaa 188,28,128
Ra 128,27,32768
Mvaa 188,24,32
W 13
D 8,40,64,2048
Ra 32,26,32768
Maaa 96,0,60
Maaa 96,4,156
Maaa 64,4,152
Ta 16,12,32768
Ta 18,12,33792
Maaa 64,0,56
W 10
D 9,40,64,3072
Ta 20,12,40960
Maaa 224,0,52
s_lshl_b32 s65, s64, 12
s_add_u32 m0, s62, 0
Ta 22,12,41984
Maaa 224,4,148
Maaa 192,4,144
Maaa 192,0,48
W 8
D 16,44,65,0
Maaa 160,0,44
Maaa 160,4,140
Mvaa 224,4,136
Mvaa 224,0,40
W 4
D 17,44,65,1024
Mvaa 192,0,36
Mvaa 192,4,132
Mvaa 160,4,128
Mvaa 160,0,32
D 18,44,65,2048
Mvaa 164,8,32
Mvaa 164,12,128
Mvaa 196,12,132
Ta 24,13,32768
Ta 26,13,33792
Mvaa 196,8,36
D 19,44,65,3072
Ta 28,13,40960
Mvaa 228,8,40
s_add_u32 m0, s62, 4096
Ta 30,13,41984
Mvaa 228,12,136
Maaa 164,12,140
Maaa 164,8,44
D 20,44,65,0
Maaa 196,8,48
Maaa 196,12,144
Maaa 228,12,148
Maaa 228,8,52
D 21,44,65,1024
Maaa 68,8,56
Maaa 68,12,152
Maaa 100,12,156
Maaa 100,8,60
W 4
D 22,44,65,2048
Maaa 104,16,60
Maaa 104,20,156
Maaa 72,20,152
Ta 0,14,32768
Ta 2,14,33792
Maaa 72,16,56
D 23,44,65,3072
Ta 4,14,40960
Maaa 232,16,52
Ta 6,14,41984
Maaa 232,20,148
Maaa 200,20,144
s_waitcnt vmcnt(11)
v_cmp_eq_u32 vcc, s13, v29
s_cmp_eq_u64 vcc, -1
s_cbranch_scc0 .Lspin_15%=
.Lok_16%=:
Maaa 200,16,48
Maaa 168,16,44
v_bfe_u32 v144, v1, 8, 1
Maaa 168,20,140
v_mul_u32_u24 v144, 10240, v144
Mvaa 232,20,136
v_and_b32 v145, 0xf0, v1
Mvaa 232,16,40
v_lshl_add_u32 v144, v145, 3, v144
Mvaa 200,16,36
s_mul_i32 s91, s58, 20480
Mvaa 200,20,132
s_add_u32 s91, s91, s28
Mvaa 168,20,128
buffer_load_dword v142, v144, s[8:11], s91 offen offset:0 sc1
Mvaa 168,16,32
W 4
Mvaa 172,24,32
Mvaa 172,28,128
Mvaa 204,28,132
Ta 8,15,32768
Ta 10,15,33792
Mvaa 204,24,36
Ta 12,15,40960
Ta 14,15,41984
Mvaa 236,24,40
Mvaa 236,28,136
Maaa 172,28,140
Maaa 172,24,44
Maaa 204,24,48
Maaa 204,28,144
Maaa 236,28,148
Maaa 236,24,52
Maaa 76,24,56
Maaa 76,28,152
Maaa 108,28,156
Maaa 108,24,60
W 4
Maaa 112,0,60
Maaa 112,4,156
Maaa 80,4,152
Ta 16,24,32768
Ta 18,24,33792
Maaa 80,0,56
Ta 20,24,40960
Ta 22,24,41984
Maaa 240,0,52
Maaa 240,4,148
Maaa 208,4,144
Maaa 208,0,48
Maaa 176,0,44
Maaa 176,4,140
Mvaa 240,4,136
Mvaa 240,0,40
Mvaa 208,0,36
Mvaa 208,4,132
Mvaa 176,4,128
Mvaa 176,0,32
W 4
Mvaa 180,8,32
Ta 24,25,32768
Ta 26,25,33792
Mvaa 180,12,128
Mvaa 212,12,132
.Lh_0_2%=:
s_cbranch_execz .Lh_0_2_1%=
Ta 28,25,40960
Ta 30,25,41984
Mvaa 212,8,36
Mvaa 244,8,40
Mvaa 244,12,136
Maaa 180,12,140
Maaa 180,8,44
Maaa 212,8,48
Maaa 212,12,144
Maaa 244,12,148
s_waitcnt vmcnt(1) lgkmcnt(0)
s_barrier
s_add_u32 s64, s64, 128
Maaa 244,8,52
Maaa 84,8,56
Maaa 84,12,152
s_and_b32 s64, s64, 1023
Maaa 116,12,156
Maaa 116,8,60
Maaa 120,16,60
s_add_u32 m0, s61, 32768
Maaa 120,20,156
Maaa 88,20,152
Ta 0,10,0
Ta 2,10,1024
Maaa 88,16,56
D 2,40,64,0
Ta 4,10,8192
Maaa 248,16,52
Ta 6,10,9216
Maaa 248,20,148
Maaa 216,20,144
Maaa 216,16,48
D 3,40,64,1024
Maaa 184,16,44
Maaa 184,20,140
Mvaa 248,20,136
Mvaa 248,16,40
)"
R"(D 4,40,64,2048
Mvaa 216,16,36
Mvaa 216,20,132
Mvaa 184,20,128
Mvaa 184,16,32
D 5,40,64,3072
Mvaa 188,24,32
s_add_u32 m0, s61, 36864
Ra 32,26,0
Mvaa 188,28,128
Ra 128,27,0
Mvaa 220,28,132
Ra 132,27,2048
Ta 8,11,0
Mvaa 220,24,36
D 6,40,64,0
Ta 10,11,1024
Mvaa 252,24,40
Ra 36,26,2048
Ta 12,11,8192
Mvaa 252,28,136
Ta 14,11,9216
Ra 40,26,4096
Maaa 188,28,140
Ra 136,27,4096
Ra 140,27,6144
Maaa 188,24,44
D 7,40,64,1024
Ra 44,26,6144
Maaa 220,24,48
Ra 48,26,8192
Maaa 220,28,144
Ra 144,27,8192
Maaa 252,28,148
Ra 148,27,10240
Maaa 252,24,52
D 8,40,64,2048
Ra 52,26,10240
Maaa 92,24,56
Ra 56,26,12288
Maaa 92,28,152
Ra 152,27,12288
Maaa 124,28,156
Ra 156,27,14336
Maaa 124,24,60
W 13
D 9,40,64,3072
Ra 60,26,14336
Mvaa 160,0,32
s_lshl_b32 s65, s64, 12
s_add_u32 m0, s62, 32768
Mvaa 160,4,128
Mvaa 192,4,132
Ta 16,12,0
Ta 18,12,1024
Mvaa 192,0,36
W 10
D 16,44,65,0
Ta 20,12,8192
Mvaa 224,0,40
Ta 22,12,9216
Mvaa 224,4,136
Maaa 160,4,140
Maaa 160,0,44
W 8
D 17,44,65,1024
Maaa 192,0,48
Maaa 192,4,144
Maaa 224,4,148
Maaa 224,0,52
W 4
D 18,44,65,2048
Maaa 64,0,56
Maaa 64,4,152
Maaa 96,4,156
Maaa 96,0,60
D 19,44,65,3072
Maaa 100,8,60
s_add_u32 m0, s62, 36864
Maaa 100,12,156
Maaa 68,12,152
Ta 24,13,0
Ta 26,13,1024
Maaa 68,8,56
D 20,44,65,0
Ta 28,13,8192
Maaa 228,8,52
Ta 30,13,9216
Maaa 228,12,148
Maaa 196,12,144
Maaa 196,8,48
D 21,44,65,1024
Maaa 164,8,44
Maaa 164,12,140
Mvaa 228,12,136
Mvaa 228,8,40
D 22,44,65,2048
Mvaa 196,8,36
Mvaa 196,12,132
Mvaa 164,12,128
Mvaa 164,8,32
W 4
D 23,44,65,3072
Mvaa 168,16,32
v_lshrrev_b32 v138, 4, v1
Mvaa 168,20,128
Mvaa 200,20,132
s_sub_u32 s81, s73, 1
Ta 0,14,0
Ta 2,14,1024
Mvaa 200,16,36
Ta 4,14,8192
v_min_u32 v138, s81, v138
Mvaa 232,16,40
Ta 6,14,9216
v_lshrrev_b32 v139, 2, v138
Mvaa 232,20,136
v_mul_u32_u24 v139, s72, v139
Maaa 168,20,140
v_and_b32 v138, 3, v138
Maaa 168,16,44
v_lshl_add_u32 v138, v138, 7, v139
Maaa 200,16,48
v_add_u32 v138, s74, v138
Maaa 200,20,144
s_lshr_b32 s73, s73, 2
Maaa 232,20,148
s_mul_i32 s73, s73, s72
buffer_load_dword v142, v144, s[8:11], s91 offen offset:2048 sc1
Maaa 232,16,52
v_min_u32 v139, s75, v138
Maaa 72,16,56
Maaa 72,20,152
s_add_u32 s91, s91, 4096
Maaa 104,20,156
global_load_dword v142, v139, s[76:77]
v_add_u32 v138, s73, v138
Maaa 104,16,60
W 4
s_add_u32 s65, s5, 0
s_cmp_ge_u32 s65, 20
s_cselect_b32 s90, 20, 0
Maaa 108,24,60
s_sub_u32 s65, s65, s90
s_lshl_b32 s65, s65, 18
s_add_u32 m0, s63, 131072
Maaa 108,28,156
Maaa 76,28,152
Ta 8,15,0
Ta 10,15,1024
Maaa 76,24,56
D 16,48,65,0
Ta 12,15,8192
Maaa 236,24,52
Ta 14,15,9216
Maaa 236,28,148
Maaa 204,28,144
Maaa 204,24,48
D 17,48,65,1024
Maaa 172,24,44
Maaa 172,28,140
Mvaa 236,28,136
Mvaa 236,24,40
Mvaa 204,24,36
D 18,48,65,2048
Mvaa 204,28,132
Mvaa 172,28,128
Mvaa 172,24,32
W 4
Mvaa 176,0,32
Mvaa 176,4,128
D 19,48,65,3072
Mvaa 208,4,132
s_add_u32 m0, s63, 135168
Ta 16,24,0
Ta 18,24,1024
Mvaa 208,0,36
Ta 20,24,8192
Ta 22,24,9216
Mvaa 240,0,40
Mvaa 240,4,136
Maaa 176,4,140
D 20,48,65,0
Maaa 176,0,44
Maaa 208,0,48
Maaa 208,4,144
Maaa 240,4,148
Maaa 240,0,52
D 21,48,65,1024
Maaa 80,0,56
Maaa 80,4,152
Maaa 112,4,156
Maaa 112,0,60
W 4
Maaa 116,8,60
D 22,48,65,2048
Ta 24,25,0
Maaa 116,12,156
Ta 26,25,1024
Maaa 84,12,152
Ta 28,25,8192
Ta 30,25,9216
Maaa 84,8,56
Maaa 244,8,52
D 23,48,65,3072
Maaa 244,12,148
Maaa 212,12,144
Maaa 212,8,48
Maaa 180,8,44
Maaa 180,12,140
Mvaa 244,12,136
s_waitcnt vmcnt(10) lgkmcnt(0)
s_barrier
s_add_u32 s64, s64, 128
Mvaa 244,8,40
Mvaa 212,8,36
Mvaa 212,12,132
s_and_b32 s64, s64, 1023
Mvaa 180,12,128
Mvaa 180,8,32
Mvaa 184,16,32
s_add_u32 m0, s61, 0
Mvaa 184,20,128
Mvaa 216,20,132
Ta 0,10,32768
Ta 2,10,33792
Mvaa 216,16,36
D 2,40,64,0
Ta 4,10,40960
Mvaa 248,16,40
Ta 6,10,41984
Mvaa 248,20,136
Maaa 184,20,140
Maaa 184,16,44
D 3,40,64,1024
Maaa 216,16,48
Maaa 216,20,144
Maaa 248,20,148
Maaa 248,16,52
D 4,40,64,2048
Maaa 88,16,56
Maaa 88,20,152
Maaa 120,20,156
Maaa 120,16,60
D 5,40,64,3072
Maaa 124,24,60
s_add_u32 m0, s61, 4096
Ra 60,26,47104
Maaa 124,28,156
Ra 156,27,47104
Maaa 92,28,152
Ra 152,27,45056
Ta 8,11,32768
Maaa 92,24,56
D 6,40,64,0
Ta 10,11,33792
Maaa 252,24,52
Ra 56,26,45056
Ta 12,11,40960
Maaa 252,28,148
Ta 14,11,41984
Ra 52,26,43008
Maaa 220,28,144
Ra 148,27,43008
Ra 144,27,40960
Maaa 220,24,48
D 7,40,64,1024
Ra 48,26,40960
Maaa 188,24,44
Ra 44,26,38912
Maaa 188,28,140
Ra 140,27,38912
Mvaa 252,28,136
Ra 136,27,36864
Mvaa 252,24,40
D 8,40,64,2048
Ra 40,26,36864
Mvaa 220,24,36
Ra 36,26,34816
Mvaa 220,28,132
Ra 132,27,34816
Mvaa 188,28,128
Ra 128,27,32768
Mvaa 188,24,32
W 13
D 9,40,64,3072
Ra 32,26,32768
Maaa 96,0,60
s_lshl_b32 s65, s64, 12
s_add_u32 m0, s62, 0
Maaa 96,4,156
Maaa 64,4,152
Ta 16,12,32768
Ta 18,12,33792
Maaa 64,0,56
W 10
D 16,44,65,0
Ta 20,12,40960
Maaa 224,0,52
Ta 22,12,41984
Maaa 224,4,148
Maaa 192,4,144
Maaa 192,0,48
W 8
D 17,44,65,1024
Maaa 160,0,44
Maaa 160,4,140
Mvaa 224,4,136
Mvaa 224,0,40
W 4
D 18,44,65,2048
Mvaa 192,0,36
Mvaa 192,4,132
Mvaa 160,4,128
Mvaa 160,0,32
D 19,44,65,3072
Mvaa 164,8,32
s_add_u32 m0, s62, 4096
Mvaa 164,12,128
Mvaa 196,12,132
Ta 24,13,32768
Ta 26,13,33792
Mvaa 196,8,36
D 20,44,65,0
Ta 28,13,40960
Mvaa 228,8,40
Ta 30,13,41984
Mvaa 228,12,136
Maaa 164,12,140
Maaa 164,8,44
D 21,44,65,1024
Maaa 196,8,48
Maaa 196,12,144
Maaa 228,12,148
Maaa 228,8,52
D 22,44,65,2048
Maaa 68,8,56
Maaa 68,12,152
Maaa 100,12,156
Maaa 100,8,60
W 4
D 23,44,65,3072
Maaa 104,16,60
v_min_u32 v139, s75, v138
Maaa 104,20,156
Maaa 72,20,152
Ta 0,14,32768
Ta 2,14,33792
Maaa 72,16,56
buffer_load_dword v142, v144, s[8:11], s91 offen offset:0 sc1
Ta 4,14,40960
Maaa 232,16,52
Ta 6,14,41984
Maaa 232,20,148
Maaa 200,20,144
Maaa 200,16,48
global_load_dword v142, v139, s[76:77]
Maaa 168,16,44
Maaa 168,20,140
Mvaa 232,20,136
v_add_u32 v138, s73, v138
Mvaa 232,16,40
global_load_dword v142, v146, s[6:7]
Mvaa 200,16,36
Mvaa 200,20,132
Mvaa 168,20,128
Mvaa 168,16,32
W 4
Mvaa 172,24,32
Mvaa 172,28,128
Mvaa 204,28,132
Ta 8,15,32768
Ta 10,15,33792
Mvaa 204,24,36
Ta 12,15,40960
Ta 14,15,41984
Mvaa 236,24,40
Mvaa 236,28,136
Maaa 172,28,140
Maaa 172,24,44
Maaa 204,24,48
Maaa 204,28,144
Maaa 236,28,148
Maaa 236,24,52
Maaa 76,24,56
Maaa 76,28,152
Maaa 108,28,156
Maaa 108,24,60
W 4
Maaa 112,0,60
Maaa 112,4,156
Maaa 80,4,152
Ta 16,24,32768
Ta 18,24,33792
Maaa 80,0,56
Ta 20,24,40960
Ta 22,24,41984
Maaa 240,0,52
Maaa 240,4,148
Maaa 208,4,144
)"
R"(Maaa 208,0,48
Maaa 176,0,44
Maaa 176,4,140
Mvaa 240,4,136
Mvaa 240,0,40
Mvaa 208,0,36
Mvaa 208,4,132
Mvaa 176,4,128
Mvaa 176,0,32
W 4
Mvaa 180,8,32
Ta 24,25,32768
Ta 26,25,33792
Mvaa 180,12,128
Mvaa 212,12,132
Ta 28,25,40960
Ta 30,25,41984
Mvaa 212,8,36
Mvaa 244,8,40
Mvaa 244,12,136
Maaa 180,12,140
Maaa 180,8,44
Maaa 212,8,48
Maaa 212,12,144
Maaa 244,12,148
s_waitcnt vmcnt(3) lgkmcnt(0)
s_barrier
s_add_u32 s64, s64, 128
Maaa 244,8,52
Maaa 84,8,56
Maaa 84,12,152
s_and_b32 s64, s64, 1023
Maaa 116,12,156
Maaa 116,8,60
Maaa 120,16,60
s_add_u32 m0, s61, 32768
Maaa 120,20,156
Maaa 88,20,152
Ta 0,10,0
Ta 2,10,1024
Maaa 88,16,56
D 2,40,64,0
Ta 4,10,8192
Maaa 248,16,52
Ta 6,10,9216
Maaa 248,20,148
Maaa 216,20,144
Maaa 216,16,48
D 3,40,64,1024
Maaa 184,16,44
Maaa 184,20,140
Mvaa 248,20,136
Mvaa 248,16,40
D 4,40,64,2048
Mvaa 216,16,36
Mvaa 216,20,132
Mvaa 184,20,128
Mvaa 184,16,32
D 5,40,64,3072
Mvaa 188,24,32
s_add_u32 m0, s61, 36864
Ra 32,26,0
Mvaa 188,28,128
Ra 128,27,0
Mvaa 220,28,132
Ra 132,27,2048
Ta 8,11,0
Mvaa 220,24,36
D 6,40,64,0
Ta 10,11,1024
Mvaa 252,24,40
Ra 36,26,2048
Ta 12,11,8192
Mvaa 252,28,136
Ta 14,11,9216
Ra 40,26,4096
Maaa 188,28,140
Ra 136,27,4096
Ra 140,27,6144
Maaa 188,24,44
D 7,40,64,1024
Ra 44,26,6144
Maaa 220,24,48
Ra 48,26,8192
Maaa 220,28,144
Ra 144,27,8192
Maaa 252,28,148
Ra 148,27,10240
Maaa 252,24,52
D 8,40,64,2048
Ra 52,26,10240
Maaa 92,24,56
Ra 56,26,12288
Maaa 92,28,152
Ra 152,27,12288
Maaa 124,28,156
.Lh_0_3%=:
s_cbranch_execz .Lh_0_3_1%=
Ra 156,27,14336
Maaa 124,24,60
W 13
D 9,40,64,3072
Ra 60,26,14336
Mvaa 160,0,32
s_lshl_b32 s65, s64, 12
s_add_u32 m0, s62, 32768
Mvaa 160,4,128
Mvaa 192,4,132
Ta 16,12,0
Ta 18,12,1024
Mvaa 192,0,36
W 10
D 16,44,65,0
Ta 20,12,8192
Mvaa 224,0,40
Ta 22,12,9216
Mvaa 224,4,136
Maaa 160,4,140
Maaa 160,0,44
W 8
D 17,44,65,1024
Maaa 192,0,48
Maaa 192,4,144
Maaa 224,4,148
Maaa 224,0,52
W 4
D 18,44,65,2048
Maaa 64,0,56
Maaa 64,4,152
Maaa 96,4,156
Maaa 96,0,60
D 19,44,65,3072
Maaa 100,8,60
s_add_u32 m0, s62, 36864
Maaa 100,12,156
Maaa 68,12,152
Ta 24,13,0
Ta 26,13,1024
Maaa 68,8,56
D 20,44,65,0
Ta 28,13,8192
Maaa 228,8,52
Ta 30,13,9216
Maaa 228,12,148
Maaa 196,12,144
Maaa 196,8,48
D 21,44,65,1024
Maaa 164,8,44
Maaa 164,12,140
Mvaa 228,12,136
Mvaa 228,8,40
D 22,44,65,2048
Mvaa 196,8,36
Mvaa 196,12,132
Mvaa 164,12,128
Mvaa 164,8,32
W 4
D 23,44,65,3072
Mvaa 168,16,32
v_min_u32 v139, s75, v138
Mvaa 168,20,128
Mvaa 200,20,132
Ta 0,14,0
Ta 2,14,1024
Mvaa 200,16,36
buffer_load_dword v142, v144, s[8:11], s91 offen offset:2048 sc1
Ta 4,14,8192
Mvaa 232,16,40
Ta 6,14,9216
Mvaa 232,20,136
s_add_u32 s91, s91, 4096
Maaa 168,20,140
Maaa 168,16,44
global_load_dword v142, v139, s[76:77]
Maaa 200,16,48
Maaa 200,20,144
Maaa 232,20,148
v_add_u32 v138, s73, v138
v_add_u32 v2, 131072, v10
Maaa 232,16,52
v_add_u32 v3, 131072, v11
v_add_u32 v4, 131072, v12
Maaa 72,16,56
v_add_u32 v5, 131072, v13
v_add_u32 v6, 131072, v14
Maaa 72,20,152
v_add_u32 v7, 131072, v15
v_add_u32 v8, 131072, v24
Maaa 104,20,156
v_add_u32 v9, 131072, v25
Maaa 104,16,60
W 4
Maaa 108,24,60
Maaa 108,28,156
Maaa 76,28,152
Ta 8,15,0
Ta 10,15,1024
Maaa 76,24,56
Ta 12,15,8192
Ta 14,15,9216
Maaa 236,24,52
Maaa 236,28,148
Maaa 204,28,144
Maaa 204,24,48
Maaa 172,24,44
Maaa 172,28,140
Mvaa 236,28,136
Mvaa 236,24,40
Mvaa 204,24,36
Mvaa 204,28,132
Mvaa 172,28,128
Mvaa 172,24,32
W 4
Mvaa 176,0,32
Mvaa 176,4,128
Mvaa 208,4,132
Ta 16,24,0
Ta 18,24,1024
Mvaa 208,0,36
Ta 20,24,8192
Ta 22,24,9216
Mvaa 240,0,40
Mvaa 240,4,136
Maaa 176,4,140
Maaa 176,0,44
Maaa 208,0,48
Maaa 208,4,144
Maaa 240,4,148
Maaa 240,0,52
Maaa 80,0,56
Maaa 80,4,152
Maaa 112,4,156
Maaa 112,0,60
W 4
Maaa 116,8,60
Ta 24,25,0
Ta 26,25,1024
Maaa 116,12,156
Maaa 84,12,152
Ta 28,25,8192
Ta 30,25,9216
Maaa 84,8,56
Maaa 244,8,52
Maaa 244,12,148
Maaa 212,12,144
Maaa 212,8,48
Maaa 180,8,44
Maaa 180,12,140
Mvaa 244,12,136
s_waitcnt vmcnt(2) lgkmcnt(0)
s_barrier
s_add_u32 s65, s5, 4
Mvaa 244,8,40
Mvaa 212,8,36
Mvaa 212,12,132
s_cmp_ge_u32 s65, 20
s_cselect_b32 s90, 20, 0
Mvaa 180,12,128
Mvaa 180,8,32
s_sub_u32 s65, s65, s90
Mvaa 184,16,32
Mvaa 184,20,128
s_lshl_b32 s65, s65, 18
Mvaa 216,20,132
Ta 0,10,32768
Ta 2,10,33792
Mvaa 216,16,36
Mvaa 248,16,40
s_add_u32 m0, s63, 0
Mvaa 248,20,136
Maaa 184,20,140
D 16,48,65,0
Maaa 184,16,44
Maaa 216,16,48
Maaa 216,20,144
Maaa 248,20,148
D 17,48,65,1024
Ta 4,11,32768
Maaa 248,16,52
Ta 6,11,33792
Maaa 88,16,56
Maaa 88,20,152
Maaa 120,20,156
D 18,48,65,2048
Maaa 120,16,60
Maaa 124,24,60
Ra 60,26,47104
Maaa 124,28,156
Maaa 92,28,152
D 19,48,65,3072
Ta 8,12,32768
Maaa 92,24,56
s_add_u32 m0, s63, 4096
Ta 10,12,33792
Ra 56,26,45056
Maaa 252,24,52
Ra 52,26,43008
Maaa 252,28,148
Maaa 220,28,144
D 20,48,65,0
Maaa 220,24,48
Ra 48,26,40960
Maaa 188,24,44
Ra 44,26,38912
Maaa 188,28,140
Mvaa 252,28,136
D 21,48,65,1024
Ta 12,13,32768
Mvaa 252,24,40
Ta 14,13,33792
Ra 40,26,36864
Mvaa 220,24,36
Ra 36,26,34816
Mvaa 220,28,132
Mvaa 188,28,128
D 22,48,65,2048
Mvaa 188,24,32
Ra 32,26,32768
W 6
Maaa 96,0,60
Maaa 64,0,56
Maaa 224,0,52
D 23,48,65,3072
Ta 16,14,32768
Maaa 192,0,48
W 1
s_add_u32 s65, s5, 8
s_cmp_ge_u32 s65, 20
s_cselect_b32 s90, 20, 0
Maaa 160,0,44
s_sub_u32 s65, s65, s90
s_lshl_b32 s65, s65, 18
Ta 18,14,33792
Mvaa 224,0,40
s_add_u32 m0, s63, 65536
Mvaa 192,0,36
D 16,48,65,0
Mvaa 160,0,32
Mvaa 164,4,32
Mvaa 196,4,36
Mvaa 228,4,40
D 17,48,65,1024
Ta 20,15,32768
Maaa 164,4,44
Ta 22,15,33792
Maaa 196,4,48
Maaa 228,4,52
Maaa 68,4,56
D 18,48,65,2048
Maaa 100,4,60
Maaa 104,8,60
Maaa 72,8,56
Maaa 232,8,52
D 19,48,65,3072
Ta 24,24,32768
Maaa 200,8,48
s_add_u32 m0, s63, 69632
Ta 26,24,33792
Maaa 168,8,44
Mvaa 232,8,40
Mvaa 200,8,36
D 20,48,65,0
Mvaa 168,8,32
Mvaa 172,12,32
Mvaa 204,12,36
Mvaa 236,12,40
D 21,48,65,1024
Ta 28,25,32768
Maaa 172,12,44
Ta 30,25,33792
Maaa 204,12,48
Ra 156,27,47104
Maaa 236,12,52
Maaa 76,12,56
D 22,48,65,2048
Maaa 108,12,60
W 7
Ra 152,27,45056
Maaa 112,16,60
Maaa 80,16,56
Maaa 240,16,52
D 23,48,65,3072
Ra 148,27,43008
Maaa 208,16,48
Ta 0,10,40960
Ta 2,10,41984
Maaa 176,16,44
Mvaa 240,16,40
Ra 144,27,40960
Mvaa 208,16,36
buffer_load_dword v142, v144, s[8:11], s91 offen offset:0 sc1
Mvaa 176,16,32
W 10
Mvaa 180,20,32
Ra 140,27,38912
Mvaa 212,20,36
Mvaa 244,20,40
Ta 4,11,40960
Ta 6,11,41984
Maaa 180,20,44
)"
R"(Ra 136,27,36864
Maaa 212,20,48
Maaa 244,20,52
v_bfe_u32 v29, v1, 7, 1
Maaa 84,20,56
Ra 132,27,34816
Maaa 116,20,60
W 13
v_mul_u32_u24 v28, 10240, v29
Maaa 120,24,60
Maaa 88,24,56
Ra 128,27,32768
v_lshrrev_b32 v29, 8, v1
Maaa 248,24,52
Ta 8,12,40960
Ta 10,12,41984
Maaa 216,24,48
v_lshl_add_u32 v28, v29, 7, v28
Maaa 184,24,44
v_and_b32 v29, 0x70, v1
Mvaa 248,24,40
Mvaa 216,24,36
v_add_u32 v28, v28, v29
Mvaa 184,24,32
W 14
Mvaa 188,28,32
s_and_b32 s80, s14, 2
Mvaa 220,28,36
s_mul_i32 s80, s80, 20480
Mvaa 252,28,40
Ta 12,13,40960
Ta 14,13,41984
Maaa 188,28,44
s_sub_u32 s81, 40960, s80
Maaa 220,28,48
Maaa 252,28,52
s_add_u32 s93, s28, 20480
Maaa 92,28,56
Maaa 124,28,60
W 10
v_add_u32 v26, s80, v28
Maaa 96,0,156
Maaa 64,0,152
Maaa 224,0,148
Ta 16,14,40960
v_add_u32 v27, s81, v28
Maaa 192,0,144
W 5
Ta 18,14,41984
Maaa 160,0,140
buffer_load_dwordx4 a[32:35], v26, s[8:11], s28 offen offset:0 sc1
Mvaa 224,0,136
Mvaa 192,0,132
Mvaa 160,0,128
Mvaa 164,4,128
buffer_load_dwordx4 a[36:39], v26, s[8:11], s93 offen offset:0 sc1
Mvaa 196,4,132
Mvaa 228,4,136
Ta 20,15,40960
Ta 22,15,41984
Maaa 164,4,140
Maaa 196,4,144
buffer_load_dwordx4 a[40:43], v27, s[8:11], s28 offen offset:0 sc1
Maaa 228,4,148
Maaa 68,4,152
Maaa 100,4,156
W 6
Maaa 104,8,156
buffer_load_dwordx4 a[44:47], v27, s[8:11], s93 offen offset:0 sc1
Ta 24,24,40960
Maaa 72,8,152
s_add_u32 s28, s28, 512
s_cmp_eq_u32 s28, s4
s_cselect_b32 s93, 10240, 0
Ta 26,24,41984
Maaa 232,8,148
s_sub_u32 s28, s28, s93
Ta 28,25,40960
Ta 30,25,41984
Maaa 200,8,144
s_add_u32 s93, s28, 20480
Maaa 168,8,140
Mvaa 232,8,136
Mvaa 200,8,132
s_add_u32 s65, s5, 12
s_cmp_ge_u32 s65, 20
s_cselect_b32 s90, 20, 0
buffer_load_dwordx4 a[48:51], v26, s[8:11], s28 offen offset:0 sc1
Mvaa 168,8,128
W 8
s_sub_u32 s65, s65, s90
s_lshl_b32 s65, s65, 18
Mvaa 172,12,128
s_add_u32 m0, s63, 32768
Mvaa 204,12,132
Mvaa 236,12,136
s_waitcnt vmcnt(6) lgkmcnt(0)
s_barrier
buffer_load_dwordx4 a[52:55], v26, s[8:11], s93 offen offset:0 sc1
Maaa 172,12,140
Maaa 204,12,144
Maaa 236,12,148
Maaa 76,12,152
buffer_load_dwordx4 a[56:59], v27, s[8:11], s28 offen offset:0 sc1
Maaa 108,12,156
Maaa 112,16,156
Maaa 80,16,152
Maaa 240,16,148
buffer_load_dwordx4 a[60:63], v27, s[8:11], s93 offen offset:0 sc1
Maaa 208,16,144
Maaa 176,16,140
Mvaa 240,16,136
Mvaa 208,16,132
D 16,48,65,0
Mvaa 176,16,128
Mvaa 180,20,128
Mvaa 212,20,132
Mvaa 244,20,136
D 17,48,65,1024
Ta 0,2,0
Maaa 180,20,140
Ta 2,2,1024
Maaa 212,20,144
Maaa 244,20,148
Maaa 84,20,152
D 18,48,65,2048
Ta 4,2,8192
Maaa 116,20,156
Ta 6,2,9216
Maaa 120,24,156
Maaa 88,24,152
Maaa 248,24,148
Ta 8,3,0
Ta 10,3,1024
Maaa 216,24,144
D 19,48,65,3072
Maaa 184,24,140
s_add_u32 m0, s63, 36864
Mvaa 248,24,136
Mvaa 216,24,132
Ta 12,3,8192
Ta 14,3,9216
Mvaa 184,24,128
D 20,48,65,0
Mvaa 188,28,128
Mvaa 220,28,132
Mvaa 252,28,136
Ta 16,4,0
Ta 18,4,1024
Maaa 188,28,140
Maaa 220,28,144
D 21,48,65,1024
Maaa 252,28,148
Maaa 92,28,152
Maaa 124,28,156
s_branch .Lq_11%=
.p2align 12
.Lv_10%=:
s_waitcnt vmcnt(14)
s_barrier
Ta 0,10,16384
Ta 2,10,17408
Ra 32,26,0
Ta 4,10,24576
.Lh_0_0_1%=:
s_cbranch_execz .Lhr_9%=
Ta 6,10,25600
Ra 128,27,0
Ra 132,27,2048
Ra 36,26,2048
Ra 40,26,4096
Ra 136,27,4096
Ra 140,27,6144
Ra 44,26,6144
Ra 48,26,8192
Ra 144,27,8192
W 6
Zvaa 160,32,0
s_add_u32 m0, s62, 32768
Ra 148,27,10240
Ra 52,26,10240
Ra 56,26,12288
Ra 152,27,12288
Ra 156,27,14336
Ra 60,26,14336
Mvaa 160,128,4
Ta 8,11,16384
Ta 10,11,17408
Ta 12,11,24576
Ta 14,11,25600
Zvaa 164,132,4
Ta 16,12,16384
Ta 18,12,17408
Mvaa 164,36,0
W 14
Ta 20,12,24576
Ta 22,12,25600
Zvaa 224,40,0
D 16,44,65,0
Mvaa 224,136,4
Zvaa 228,140,4
Mvaa 228,44,0
W 12
Zaaa 192,48,0
D 17,44,65,1024
Maaa 192,144,4
Zaaa 196,148,4
s_waitcnt vmcnt(13)
Maaa 196,52,0
W 8
v_lshlrev_b32 v56, 16, v32
v_and_b32 v57, 0xffff0000, v32
v_lshlrev_b32 v58, 16, v33
v_and_b32 v59, 0xffff0000, v33
Zaaa 64,56,0
D 18,44,65,2048
v_lshlrev_b32 v60, 16, v34
v_and_b32 v61, 0xffff0000, v34
v_lshlrev_b32 v62, 16, v35
v_and_b32 v63, 0xffff0000, v35
Maaa 64,152,4
v_lshlrev_b32 v64, 16, v36
v_and_b32 v65, 0xffff0000, v36
v_lshlrev_b32 v66, 16, v37
v_and_b32 v67, 0xffff0000, v37
v_lshlrev_b32 v68, 16, v38
Zaaa 68,156,4
v_and_b32 v69, 0xffff0000, v38
v_lshlrev_b32 v70, 16, v39
v_and_b32 v71, 0xffff0000, v39
v_lshlrev_b32 v72, 16, v40
Maaa 68,60,0
W 4
v_and_b32 v73, 0xffff0000, v40
v_lshlrev_b32 v74, 16, v41
v_and_b32 v75, 0xffff0000, v41
v_lshlrev_b32 v76, 16, v42
v_and_b32 v77, 0xffff0000, v42
Zaaa 76,60,8
D 19,44,65,3072
v_lshlrev_b32 v78, 16, v43
v_and_b32 v79, 0xffff0000, v43
A 80,56,57
A 81,58,59
Maaa 76,156,12
s_add_u32 m0, s62, 36864
A 80,80,60
A 81,81,61
A 80,80,62
A 81,81,63
A 80,80,64
Zaaa 72,152,12
Ta 24,13,16384
Ta 26,13,17408
A 81,81,65
A 80,80,66
A 81,81,67
A 80,80,68
Maaa 72,56,8
Ta 28,13,24576
Ta 30,13,25600
A 81,81,69
A 80,80,70
A 81,81,71
A 80,80,72
Zaaa 204,52,8
D 20,44,65,0
A 81,81,73
A 80,80,74
A 81,81,75
A 80,80,76
A 81,81,77
Maaa 204,148,12
A 80,80,78
A 81,81,79
A 80,80,81
s_nop 1
v_add_f32_dpp v80, v80, v80 quad_perm:[1,0,3,2] row_mask:0xf bank_mask:0xf
Zaaa 200,144,12
Maaa 200,48,8
v_add_f32_dpp v80, v80, v80 quad_perm:[2,3,0,1] row_mask:0xf bank_mask:0xf
s_nop 1
v_add_f32_dpp v80, v80, v80 row_half_mirror row_mask:0xf bank_mask:0xf
s_nop 1
v_add_f32_dpp v80, v80, v80 row_mirror row_mask:0xf bank_mask:0xf
v_mov_b32 v81, v80
s_nop 1
v_permlane16_swap_b32 v80, v81
A 80,80,81
Zvaa 236,44,8
D 21,44,65,1024
v_mul_f32 v84, 0x3acccccd, v80
s_mov_b32 s86, 0xffff
s_mov_b32 s87, 0xffff
v_sub_f32 v82, v56, v84
U 80,82,82
v_sub_f32 v83, v57, v84
Mvaa 236,140,12
v_fmac_f32 v80, v83, v83
v_sub_f32 v82, v58, v84
v_fmac_f32 v80, v82, v82
v_sub_f32 v83, v59, v84
v_fmac_f32 v80, v83, v83
v_sub_f32 v82, v60, v84
Zvaa 232,136,12
v_fmac_f32 v80, v82, v82
v_sub_f32 v83, v61, v84
v_fmac_f32 v80, v83, v83
v_sub_f32 v82, v62, v84
v_fmac_f32 v80, v82, v82
Mvaa 232,40,8
v_sub_f32 v83, v63, v84
v_fmac_f32 v80, v83, v83
v_sub_f32 v82, v64, v84
v_fmac_f32 v80, v82, v82
Zvaa 172,36,8
D 22,44,65,2048
v_sub_f32 v83, v65, v84
v_fmac_f32 v80, v83, v83
v_sub_f32 v82, v66, v84
v_fmac_f32 v80, v82, v82
Mvaa 172,132,12
v_sub_f32 v83, v67, v84
v_fmac_f32 v80, v83, v83
v_sub_f32 v82, v68, v84
v_fmac_f32 v80, v82, v82
v_sub_f32 v83, v69, v84
Zvaa 168,128,12
v_fmac_f32 v80, v83, v83
v_sub_f32 v82, v70, v84
v_fmac_f32 v80, v82, v82
v_sub_f32 v83, v71, v84
Mvaa 168,32,8
W 4
v_fmac_f32 v80, v83, v83
v_sub_f32 v82, v72, v84
U 81,82,82
v_sub_f32 v83, v73, v84
v_fmac_f32 v81, v83, v83
Zvaa 176,32,16
D 23,44,65,3072
v_sub_f32 v82, v74, v84
v_fmac_f32 v81, v82, v82
v_sub_f32 v83, v75, v84
v_fmac_f32 v81, v83, v83
Mvaa 176,128,20
v_sub_f32 v82, v76, v84
v_fmac_f32 v81, v82, v82
v_sub_f32 v83, v77, v84
v_fmac_f32 v81, v83, v83
v_sub_f32 v82, v78, v84
Zvaa 180,132,20
Ta 0,14,16384
Ta 2,14,17408
v_fmac_f32 v81, v82, v82
v_sub_f32 v83, v79, v84
v_fmac_f32 v81, v83, v83
v_cndmask_b32_e64 v81, 0, v81, s[86:87]
Mvaa 180,36,16
Ta 4,14,24576
Ta 6,14,25600
A 80,80,81
s_nop 1
v_add_f32_dpp v80, v80, v80 quad_perm:[1,0,3,2] row_mask:0xf bank_mask:0xf
s_nop 1
v_add_f32_dpp v80, v80, v80 quad_perm:[2,3,0,1] row_mask:0xf bank_mask:0xf
s_nop 1
v_add_f32_dpp v80, v80, v80 row_half_mirror row_mask:0xf bank_mask:0xf
Zvaa 240,40,16
global_load_dword v142, v[140:141], off
v_lshl_add_u64 v[140:141], v[140:141], 0, s[68:69]
Mvaa 240,136,20
v_add_f32_dpp v80, v80, v80 row_mirror row_mask:0xf bank_mask:0xf
v_mov_b32 v81, v80
s_nop 1
v_permlane16_swap_b32 v80, v81
A 80,80,81
v_mul_f32 v80, 0x3acccccd, v80
K 80,80,80
Zvaa 244,140,20
v_lshlrev_b32 v80, 16, v80
v_add_f32 v80, 0x3727c5ac, v80
K 80,80,80
v_lshlrev_b32 v80, 16, v80
v_sqrt_f32 v81, v80
Mvaa 244,44,16
v_rcp_f32 v82, v81
v_fma_f32 v83, -v81, v81, v80
v_mul_f32 v82, 0.5, v82
v_fma_f32 v81, v83, v82, v81
K 86,81,81
Zaaa 208,48,16
v_lshlrev_b32 v86, 16, v86
v_rcp_f32 v87, v86
K 85,84,84
v_lshlrev_b32 v85, 16, v85
Maaa 208,144,20
s_waitcnt vmcnt(17)
v_sub_f32 v56, v56, v85
v_sub_f32 v57, v57, v85
)"
R"(K 80,56,57
Zaaa 212,148,20
v_lshlrev_b32 v56, 16, v80
v_and_b32 v57, 0xffff0000, v80
U 80,56,87
U 81,57,87
K 32,80,81
Maaa 212,52,16
v_lshlrev_b32 v80, 16, v32
v_and_b32 v81, 0xffff0000, v32
v_lshlrev_b32 v82, 16, v44
v_and_b32 v83, 0xffff0000, v44
Zaaa 80,56,16
U 84,80,82
v_fmac_f32 v84, v81, v83
v_sub_f32 v58, v58, v85
v_sub_f32 v59, v59, v85
Maaa 80,152,20
K 80,58,59
v_lshlrev_b32 v58, 16, v80
v_and_b32 v59, 0xffff0000, v80
U 80,58,87
U 81,59,87
Zaaa 84,156,20
K 33,80,81
v_lshlrev_b32 v80, 16, v33
v_and_b32 v81, 0xffff0000, v33
v_lshlrev_b32 v82, 16, v45
Maaa 84,60,16
W 4
v_and_b32 v83, 0xffff0000, v45
v_fmac_f32 v84, v80, v82
v_fmac_f32 v84, v81, v83
v_sub_f32 v60, v60, v85
v_sub_f32 v61, v61, v85
Zaaa 92,60,24
K 80,60,61
v_lshlrev_b32 v60, 16, v80
v_and_b32 v61, 0xffff0000, v80
U 80,60,87
Maaa 92,156,28
U 81,61,87
K 34,80,81
v_lshlrev_b32 v80, 16, v34
v_and_b32 v81, 0xffff0000, v34
v_lshlrev_b32 v82, 16, v46
Zaaa 88,152,28
Ta 8,15,16384
Ta 10,15,17408
v_and_b32 v83, 0xffff0000, v46
v_fmac_f32 v84, v80, v82
v_fmac_f32 v84, v81, v83
v_sub_f32 v62, v62, v85
Maaa 88,56,24
Ta 12,15,24576
v_sub_f32 v63, v63, v85
K 80,62,63
v_lshlrev_b32 v62, 16, v80
v_and_b32 v63, 0xffff0000, v80
U 80,62,87
Zaaa 220,52,24
Ta 14,15,25600
U 81,63,87
K 35,80,81
v_lshlrev_b32 v80, 16, v35
v_and_b32 v81, 0xffff0000, v35
Maaa 220,148,28
v_lshlrev_b32 v82, 16, v47
v_and_b32 v83, 0xffff0000, v47
v_fmac_f32 v84, v80, v82
v_fmac_f32 v84, v81, v83
Zaaa 216,144,28
v_sub_f32 v64, v64, v85
v_sub_f32 v65, v65, v85
K 80,64,65
v_lshlrev_b32 v64, 16, v80
v_and_b32 v65, 0xffff0000, v80
Maaa 216,48,24
U 80,64,87
U 81,65,87
K 36,80,81
v_lshlrev_b32 v80, 16, v36
Zvaa 252,44,24
v_and_b32 v81, 0xffff0000, v36
v_lshlrev_b32 v82, 16, v48
v_and_b32 v83, 0xffff0000, v48
v_fmac_f32 v84, v80, v82
v_fmac_f32 v84, v81, v83
Mvaa 252,140,28
v_sub_f32 v66, v66, v85
v_sub_f32 v67, v67, v85
K 80,66,67
v_lshlrev_b32 v66, 16, v80
Zvaa 248,136,28
v_and_b32 v67, 0xffff0000, v80
U 80,66,87
U 81,67,87
K 37,80,81
v_lshlrev_b32 v80, 16, v37
Mvaa 248,40,24
v_and_b32 v81, 0xffff0000, v37
v_lshlrev_b32 v82, 16, v49
v_and_b32 v83, 0xffff0000, v49
v_fmac_f32 v84, v80, v82
Zvaa 188,36,24
v_fmac_f32 v84, v81, v83
v_sub_f32 v68, v68, v85
v_sub_f32 v69, v69, v85
K 80,68,69
v_lshlrev_b32 v68, 16, v80
Mvaa 188,132,28
v_and_b32 v69, 0xffff0000, v80
U 80,68,87
U 81,69,87
K 38,80,81
Zvaa 184,128,28
v_lshlrev_b32 v80, 16, v38
v_and_b32 v81, 0xffff0000, v38
v_lshlrev_b32 v82, 16, v50
v_and_b32 v83, 0xffff0000, v50
Mvaa 184,32,24
W 4
v_fmac_f32 v84, v80, v82
v_fmac_f32 v84, v81, v83
v_sub_f32 v70, v70, v85
v_sub_f32 v71, v71, v85
K 80,70,71
Zvaa 192,32,0
v_lshlrev_b32 v70, 16, v80
v_and_b32 v71, 0xffff0000, v80
U 80,70,87
U 81,71,87
Mvaa 192,128,4
K 39,80,81
v_lshlrev_b32 v80, 16, v39
v_and_b32 v81, 0xffff0000, v39
v_lshlrev_b32 v82, 16, v51
v_and_b32 v83, 0xffff0000, v51
Zvaa 196,132,4
Ta 16,24,16384
Ta 18,24,17408
v_fmac_f32 v84, v80, v82
v_fmac_f32 v84, v81, v83
v_sub_f32 v72, v72, v85
v_sub_f32 v73, v73, v85
Mvaa 196,36,0
Ta 20,24,24576
K 80,72,73
v_lshlrev_b32 v72, 16, v80
v_and_b32 v73, 0xffff0000, v80
U 80,72,87
U 81,73,87
Zaaa 160,40,0
Ta 22,24,25600
K 40,80,81
v_lshlrev_b32 v80, 16, v40
v_and_b32 v81, 0xffff0000, v40
v_lshlrev_b32 v82, 16, v52
Maaa 160,136,4
v_and_b32 v83, 0xffff0000, v52
v_fmac_f32 v84, v80, v82
v_fmac_f32 v84, v81, v83
v_sub_f32 v74, v74, v85
Zaaa 164,140,4
v_sub_f32 v75, v75, v85
K 80,74,75
v_lshlrev_b32 v74, 16, v80
v_and_b32 v75, 0xffff0000, v80
U 80,74,87
Maaa 164,44,0
U 81,75,87
K 41,80,81
v_lshlrev_b32 v80, 16, v41
v_and_b32 v81, 0xffff0000, v41
Zaaa 224,48,0
v_lshlrev_b32 v82, 16, v53
v_and_b32 v83, 0xffff0000, v53
v_fmac_f32 v84, v80, v82
v_fmac_f32 v84, v81, v83
v_sub_f32 v76, v76, v85
Maaa 224,144,4
v_sub_f32 v77, v77, v85
K 80,76,77
v_lshlrev_b32 v76, 16, v80
v_and_b32 v77, 0xffff0000, v80
Zaaa 228,148,4
U 80,76,87
U 81,77,87
K 42,80,81
v_lshlrev_b32 v80, 16, v42
v_and_b32 v81, 0xffff0000, v42
Maaa 228,52,0
v_lshlrev_b32 v82, 16, v54
v_and_b32 v83, 0xffff0000, v54
v_fmac_f32 v84, v80, v82
v_fmac_f32 v84, v81, v83
Zaaa 96,56,0
v_sub_f32 v78, v78, v85
v_sub_f32 v79, v79, v85
K 80,78,79
v_lshlrev_b32 v78, 16, v80
v_and_b32 v79, 0xffff0000, v80
Maaa 96,152,4
U 80,78,87
U 81,79,87
K 43,80,81
v_lshlrev_b32 v80, 16, v43
Zaaa 100,156,4
v_and_b32 v81, 0xffff0000, v43
v_lshlrev_b32 v82, 16, v55
v_and_b32 v83, 0xffff0000, v55
v_fmac_f32 v84, v80, v82
Maaa 100,60,0
W 4
v_fmac_f32 v84, v81, v83
v_lshrrev_b32 v82, 9, v1
s_lshl_b32 s80, s14, 1
v_add_u32 v82, s80, v82
v_mul_u32_u24 v83, 1552, v82
Zaaa 108,60,8
Ta 24,25,16384
Ta 26,25,17408
v_add_u32 v83, v83, v88
v_add_u32 v83, 131072, v83
ds_write_b128 v83, v[32:35] offset:0
ds_write_b128 v83, v[36:39] offset:512
Maaa 108,156,12
ds_write_b128 v83, v[40:43] offset:1024
v_add_f32_dpp v84, v84, v84 quad_perm:[1,0,3,2] row_mask:0xf bank_mask:0xf
s_nop 1
v_add_f32_dpp v84, v84, v84 quad_perm:[2,3,0,1] row_mask:0xf bank_mask:0xf
s_nop 1
v_add_f32_dpp v84, v84, v84 row_half_mirror row_mask:0xf bank_mask:0xf
s_nop 1
v_add_f32_dpp v84, v84, v84 row_mirror row_mask:0xf bank_mask:0xf
Zaaa 104,152,12
Ta 28,25,24576
Ta 30,25,25600
v_mov_b32 v81, v84
Maaa 104,56,8
Zaaa 236,52,8
v_permlane16_swap_b32 v84, v81
A 84,84,81
K 80,84,84
v_lshlrev_b32 v80, 16, v80
s_lshl_b32 s80, s15, 3
v_add_u32 v82, s80, v82
v_sub_u32 v83, 2047, v82
Maaa 236,148,12
v_ashrrev_i32 v81, 31, v80
v_or_b32 v81, 0x80000000, v81
v_xor_b32 v80, v80, v81
v_and_b32 v80, 0xffff0000, v80
v_or_b32 v80, v80, v83
v_and_b32 v82, 7, v82
Zaaa 232,144,12
v_lshlrev_b32 v82, 2, v82
v_add_u32 v82, 143488, v82
ds_write_b32 v82, v80
Maaa 232,48,8
Zaaa 172,44,8
Maaa 172,140,12
Zaaa 168,136,12
s_waitcnt vmcnt(1) lgkmcnt(0)
s_barrier
s_add_u32 s64, s64, 128
Maaa 168,40,8
v_bfe_u32 v80, v1, 4, 3
Zvaa 204,36,8
v_mul_u32_u24 v81, 1552, v80
Mvaa 204,132,12
s_and_b32 s64, s64, 1023
v_lshrrev_b32 v82, 7, v1
Zvaa 200,128,12
v_lshl_add_u32 v81, v82, 4, v81
Mvaa 200,32,8
s_lshl_b32 s80, s14, 7
Zvaa 208,32,16
s_add_u32 m0, s61, 0
s_add_u32 s80, s80, 131072
Mvaa 208,128,20
v_add_u32 v81, s80, v81
Zvaa 212,132,20
ds_read_b128 v[32:35], v81
Ta 0,10,49152
Mvaa 212,36,16
Ta 2,10,50176
D 2,40,64,0
Zaaa 176,40,16
Rv 36,81,512
Ta 4,10,57344
Maaa 176,136,20
s_and_b32 s80, s14, 2
s_lshl_b32 s80, s80, 7
Ta 6,10,58368
Zaaa 180,140,20
v_subrev_u32 v81, s80, v81
Maaa 180,44,16
D 3,40,64,1024
Rv 40,81,1024
Zaaa 240,48,16
v_lshlrev_b32 v80, 2, v80
Maaa 240,144,20
v_add_u32 v80, 143488, v80
Zaaa 244,148,20
ds_read_b32 v84, v80
Maaa 244,52,16
D 4,40,64,2048
v_mov_b32 v85, s13
Zaaa 112,56,16
v_lshrrev_b32 v83, 1, v1
Maaa 112,152,20
s_mul_i32 s82, s15, 10240
Zaaa 116,156,20
s_add_u32 s82, s82, 131072
Maaa 116,60,16
s_lshl_b32 s80, s14, 10
D 5,40,64,3072
Zaaa 124,60,24
s_add_u32 m0, s61, 4096
s_add_u32 s83, s82, s80
Ra 60,26,47104
Maaa 124,156,28
Ra 156,27,47104
Zaaa 120,152,28
Ra 152,27,45056
Ta 8,11,49152
Maaa 120,56,24
D 6,40,64,0
Ta 10,11,50176
Zaaa 252,52,24
Ra 56,26,45056
Ta 12,11,57344
Maaa 252,148,28
Ta 14,11,58368
Ra 52,26,43008
Zaaa 248,144,28
Ra 148,27,43008
Ra 144,27,40960
Maaa 248,48,24
W 11
buffer_store_dwordx4 v[32:35], v1, s[8:11], s83 offen sc1
s_add_u32 s83, s83, 4096
Zaaa 188,44,24
Ra 48,26,40960
Ra 44,26,38912
Maaa 188,140,28
Ra 140,27,38912
Zaaa 184,136,28
Ra 136,27,36864
Maaa 184,40,24
D 7,40,64,1024
Ra 40,26,36864
Zvaa 220,36,24
Ra 36,26,34816
Mvaa 220,132,28
Ra 132,27,34816
Zvaa 216,128,28
Ra 128,27,32768
Mvaa 216,32,24
W 13
buffer_store_dwordx4 v[36:39], v1, s[8:11], s83 offen sc1
Ra 32,26,32768
Maaa 68,60,0
s_and_b32 s80, s80, 1024
s_add_u32 s83, s82, s80
s_add_u32 s83, s83, 8192
Maaa 68,156,4
Maaa 64,152,4
Ta 16,12,49152
Ta 18,12,50176
Maaa 64,56,0
W 10
D 8,40,64,2048
Ta 20,12,57344
Maaa 196,52,0
Ta 22,12,58368
Maaa 196,148,4
Maaa 192,144,4
Maaa 192,48,0
W 8
buffer_store_dwordx4 v[40:43], v1, s[8:11], s83 offen sc1
Mvaa 228,44,0
Mvaa 228,140,4
Mvaa 224,136,4
Mvaa 224,40,0
W 4
D 9,40,64,3072
Mvaa 164,36,0
s_lshl_b32 s65, s64, 12
s_add_u32 m0, s62, 0
Mvaa 164,132,4
Mvaa 160,128,4
Mvaa 160,32,0
D 16,44,65,0
Mvaa 168,32,8
Mvaa 168,128,12
Mvaa 172,132,12
Ta 24,13,49152
Ta 26,13,50176
)"
R"(Mvaa 172,36,8
D 17,44,65,1024
Ta 28,13,57344
Mvaa 232,40,8
Ta 30,13,58368
Mvaa 232,136,12
.Lh_0_1_1%=:
s_cbranch_execz .Lhr_9%=
Mvaa 236,140,12
Mvaa 236,44,8
D 18,44,65,2048
Maaa 200,48,8
Maaa 200,144,12
Maaa 204,148,12
Maaa 204,52,8
D 19,44,65,3072
Maaa 72,56,8
s_add_u32 m0, s62, 4096
Maaa 72,152,12
Maaa 76,156,12
Maaa 76,60,8
W 4
D 20,44,65,0
Maaa 84,60,16
Maaa 84,156,20
Maaa 80,152,20
Ta 0,14,49152
Ta 2,14,50176
Maaa 80,56,16
D 21,44,65,1024
Ta 4,14,57344
Maaa 212,52,16
Ta 6,14,58368
Maaa 212,148,20
Maaa 208,144,20
Maaa 208,48,16
D 22,44,65,2048
Mvaa 244,44,16
Mvaa 244,140,20
Mvaa 240,136,20
Mvaa 240,40,16
D 23,44,65,3072
Mvaa 180,36,16
Mvaa 180,132,20
Mvaa 176,128,20
Mvaa 176,32,16
W 4
Mvaa 184,32,24
Mvaa 184,128,28
Mvaa 188,132,28
Ta 8,15,49152
Ta 10,15,50176
Mvaa 188,36,24
Ta 12,15,57344
Ta 14,15,58368
Mvaa 248,40,24
Mvaa 248,136,28
Mvaa 252,140,28
Mvaa 252,44,24
Maaa 216,48,24
Maaa 216,144,28
Maaa 220,148,28
Maaa 220,52,24
Maaa 88,56,24
Maaa 88,152,28
Maaa 92,156,28
Maaa 92,60,24
W 4
Maaa 100,60,0
Maaa 100,156,4
Maaa 96,152,4
Ta 16,24,49152
Ta 18,24,50176
Maaa 96,56,0
Ta 20,24,57344
Ta 22,24,58368
Maaa 228,52,0
Maaa 228,148,4
Maaa 224,144,4
Maaa 224,48,0
Maaa 164,44,0
Maaa 164,140,4
Maaa 160,136,4
Maaa 160,40,0
Mvaa 196,36,0
Mvaa 196,132,4
Mvaa 192,128,4
Mvaa 192,32,0
W 4
Mvaa 200,32,8
Ta 24,25,49152
Ta 26,25,50176
Mvaa 200,128,12
Mvaa 204,132,12
Ta 28,25,57344
Ta 30,25,58368
Mvaa 204,36,8
Maaa 168,40,8
Maaa 168,136,12
Maaa 172,140,12
Maaa 172,44,8
Maaa 232,48,8
Maaa 232,144,12
Maaa 236,148,12
s_waitcnt vmcnt(0) lgkmcnt(0)
s_barrier
s_cmp_eq_u32 s14, 0
s_cbranch_scc0 .Lpubr_19%=
s_lshl_b32 s80, s15, 6
s_add_u32 s80, s80, 40960
s_mov_b64 exec, 0xff
buffer_store_dwordx2 v[84:85], v83, s[8:11], s80 offen sc1
s_mov_b64 exec, -1
.Lpubr_19%=:
Maaa 236,52,8
s_add_u32 s64, s64, 128
Maaa 104,56,8
Maaa 104,152,12
s_and_b32 s64, s64, 1023
Maaa 108,156,12
Maaa 108,60,8
Maaa 116,60,16
s_add_u32 m0, s61, 32768
Maaa 116,156,20
Maaa 112,152,20
D 2,40,64,0
Ta 0,10,16384
Maaa 112,56,16
Ta 2,10,17408
Ta 4,10,24576
Maaa 244,52,16
Ta 6,10,25600
Maaa 244,148,20
Maaa 240,144,20
D 3,40,64,1024
Maaa 240,48,16
Maaa 180,44,16
Maaa 180,140,20
Maaa 176,136,20
D 4,40,64,2048
Maaa 176,40,16
Mvaa 212,36,16
Mvaa 212,132,20
Mvaa 208,128,20
D 5,40,64,3072
Mvaa 208,32,16
s_add_u32 m0, s61, 36864
Mvaa 216,32,24
Ra 32,26,0
Mvaa 216,128,28
Ra 128,27,0
Mvaa 220,132,28
D 6,40,64,0
Ra 132,27,2048
Mvaa 220,36,24
Ta 8,11,16384
Ta 10,11,17408
Maaa 184,40,24
Ra 36,26,2048
Ta 12,11,24576
Maaa 184,136,28
Ta 14,11,25600
Ra 40,26,4096
Maaa 188,140,28
D 7,40,64,1024
Ra 136,27,4096
Maaa 188,44,24
Ra 140,27,6144
Ra 44,26,6144
Maaa 248,48,24
Ra 48,26,8192
Maaa 248,144,28
Ra 144,27,8192
Maaa 252,148,28
D 8,40,64,2048
Ra 148,27,10240
Maaa 252,52,24
Ra 52,26,10240
Maaa 120,56,24
Ra 56,26,12288
Maaa 120,152,28
Ra 152,27,12288
Maaa 124,156,28
D 9,40,64,3072
Ra 156,27,14336
Maaa 124,60,24
W 13
s_lshl_b32 s65, s64, 12
s_add_u32 m0, s62, 32768
Mvaa 160,32,0
Ra 60,26,14336
Mvaa 160,128,4
Mvaa 164,132,4
D 16,44,65,0
Ta 16,12,16384
Mvaa 164,36,0
W 9
Ta 18,12,17408
Ta 20,12,24576
Mvaa 224,40,0
Ta 22,12,25600
Mvaa 224,136,4
Mvaa 228,140,4
D 17,44,65,1024
Mvaa 228,44,0
W 8
Maaa 192,48,0
Maaa 192,144,4
Maaa 196,148,4
D 18,44,65,2048
Maaa 196,52,0
W 4
Maaa 64,56,0
Maaa 64,152,4
Maaa 68,156,4
D 19,44,65,3072
Maaa 68,60,0
s_add_u32 m0, s62, 36864
Maaa 76,60,8
Maaa 76,156,12
Maaa 72,152,12
D 20,44,65,0
Ta 24,13,16384
Maaa 72,56,8
Ta 26,13,17408
Ta 28,13,24576
Maaa 204,52,8
Ta 30,13,25600
Maaa 204,148,12
Maaa 200,144,12
D 21,44,65,1024
Maaa 200,48,8
Mvaa 236,44,8
Mvaa 236,140,12
Mvaa 232,136,12
D 22,44,65,2048
Mvaa 232,40,8
Mvaa 172,36,8
Mvaa 172,132,12
Mvaa 168,128,12
D 23,44,65,3072
Mvaa 168,32,8
W 4
Mvaa 176,32,16
Mvaa 176,128,20
Mvaa 180,132,20
global_load_dword v142, v[140:141], off
v_lshl_add_u64 v[140:141], v[140:141], 0, s[68:69]
Ta 0,14,16384
Mvaa 180,36,16
Ta 2,14,17408
Ta 4,14,24576
Mvaa 240,40,16
Ta 6,14,25600
Mvaa 240,136,20
Mvaa 244,140,20
Mvaa 244,44,16
Maaa 208,48,16
Maaa 208,144,20
Maaa 212,148,20
Maaa 212,52,16
Maaa 80,56,16
Maaa 80,152,20
Maaa 84,156,20
Maaa 84,60,16
W 4
Maaa 92,60,24
Maaa 92,156,28
Maaa 88,152,28
Ta 8,15,16384
Ta 10,15,17408
Maaa 88,56,24
Ta 12,15,24576
Ta 14,15,25600
Maaa 220,52,24
Maaa 220,148,28
Maaa 216,144,28
Maaa 216,48,24
Mvaa 252,44,24
Mvaa 252,140,28
Mvaa 248,136,28
Mvaa 248,40,24
Mvaa 188,36,24
Mvaa 188,132,28
Mvaa 184,128,28
Mvaa 184,32,24
W 4
Mvaa 192,32,0
Mvaa 192,128,4
Mvaa 196,132,4
Ta 16,24,16384
Ta 18,24,17408
Mvaa 196,36,0
Ta 20,24,24576
Ta 22,24,25600
Maaa 160,40,0
Maaa 160,136,4
Maaa 164,140,4
Maaa 164,44,0
Maaa 224,48,0
Maaa 224,144,4
Maaa 228,148,4
Maaa 228,52,0
Maaa 96,56,0
Maaa 96,152,4
Maaa 100,156,4
Maaa 100,60,0
W 4
Maaa 108,60,8
Ta 24,25,16384
Ta 26,25,17408
Maaa 108,156,12
Maaa 104,152,12
Ta 28,25,24576
Ta 30,25,25600
Maaa 104,56,8
Maaa 236,52,8
Maaa 236,148,12
Maaa 232,144,12
Maaa 232,48,8
Maaa 172,44,8
Maaa 172,140,12
Maaa 168,136,12
s_waitcnt vmcnt(1) lgkmcnt(0)
s_barrier
s_add_u32 s64, s64, 128
Maaa 168,40,8
Mvaa 204,36,8
Mvaa 204,132,12
s_and_b32 s64, s64, 1023
Mvaa 200,128,12
Mvaa 200,32,8
Mvaa 208,32,16
s_add_u32 m0, s61, 0
Mvaa 208,128,20
Mvaa 212,132,20
D 2,40,64,0
Ta 0,10,49152
Mvaa 212,36,16
Ta 2,10,50176
Ta 4,10,57344
Maaa 176,40,16
Ta 6,10,58368
Maaa 176,136,20
Maaa 180,140,20
D 3,40,64,1024
Maaa 180,44,16
Maaa 240,48,16
Maaa 240,144,20
Maaa 244,148,20
D 4,40,64,2048
Maaa 244,52,16
Maaa 112,56,16
Maaa 112,152,20
Maaa 116,156,20
D 5,40,64,3072
Maaa 116,60,16
s_add_u32 m0, s61, 4096
Maaa 124,60,24
Ra 60,26,47104
v_lshrrev_b32 v28, 1, v1
Maaa 124,156,28
s_lshl_b32 s80, s60, 9
Ra 156,27,47104
Maaa 120,152,28
s_add_u32 s80, s80, 40960
D 6,40,64,0
Ra 152,27,45056
Maaa 120,56,24
Ta 8,11,49152
Ta 10,11,50176
Maaa 252,52,24
Ra 56,26,45056
Ta 12,11,57344
Maaa 252,148,28
Ta 14,11,58368
Ra 52,26,43008
Maaa 248,144,28
D 7,40,64,1024
Ra 148,27,43008
Maaa 248,48,24
Ra 144,27,40960
Ra 48,26,40960
Maaa 188,44,24
Ra 44,26,38912
Maaa 188,140,28
Ra 140,27,38912
Maaa 184,136,28
buffer_load_dwordx2 v[28:29], v28, s[8:11], s80 offen sc1
Ra 136,27,36864
Maaa 184,40,24
Ra 40,26,36864
Mvaa 220,36,24
Ra 36,26,34816
Mvaa 220,132,28
Ra 132,27,34816
Mvaa 216,128,28
D 8,40,64,2048
Ra 128,27,32768
Mvaa 216,32,24
)"
R"(W 13
Ra 32,26,32768
Maaa 68,60,0
Maaa 68,156,4
Maaa 64,152,4
D 9,40,64,3072
Ta 16,12,49152
Maaa 64,56,0
W 9
s_lshl_b32 s65, s64, 12
s_add_u32 m0, s62, 0
Maaa 196,52,0
Ta 18,12,50176
Ta 20,12,57344
Maaa 196,148,4
Ta 22,12,58368
Maaa 192,144,4
D 16,44,65,0
Maaa 192,48,0
W 8
Mvaa 228,44,0
Mvaa 228,140,4
Mvaa 224,136,4
D 17,44,65,1024
Mvaa 224,40,0
W 4
Mvaa 164,36,0
Mvaa 164,132,4
Mvaa 160,128,4
D 18,44,65,2048
Mvaa 160,32,0
Mvaa 168,32,8
Mvaa 168,128,12
Mvaa 172,132,12
D 19,44,65,3072
Ta 24,13,49152
Mvaa 172,36,8
s_add_u32 m0, s62, 4096
Ta 26,13,50176
Ta 28,13,57344
Mvaa 232,40,8
Ta 30,13,58368
Mvaa 232,136,12
Mvaa 236,140,12
D 20,44,65,0
Mvaa 236,44,8
Maaa 200,48,8
Maaa 200,144,12
Maaa 204,148,12
D 21,44,65,1024
Maaa 204,52,8
Maaa 72,56,8
Maaa 72,152,12
Maaa 76,156,12
D 22,44,65,2048
Maaa 76,60,8
W 4
Maaa 84,60,16
Maaa 84,156,20
Maaa 80,152,20
D 23,44,65,3072
Ta 0,14,49152
Maaa 80,56,16
Ta 2,14,50176
Ta 4,14,57344
Maaa 212,52,16
Ta 6,14,58368
Maaa 212,148,20
Maaa 208,144,20
s_waitcnt vmcnt(10)
v_cmp_eq_u32 vcc, s13, v29
s_cmp_eq_u64 vcc, -1
s_cbranch_scc0 .Lspin_20%=
.Lok_21%=:
Maaa 208,48,16
global_load_dword v142, v[140:141], off
v_lshl_add_u64 v[140:141], v[140:141], 0, s[68:69]
Mvaa 244,44,16
v_bfe_u32 v144, v1, 8, 1
Mvaa 244,140,20
v_mul_u32_u24 v144, 10240, v144
Mvaa 240,136,20
v_and_b32 v145, 0xf0, v1
Mvaa 240,40,16
v_lshl_add_u32 v144, v145, 3, v144
Mvaa 180,36,16
s_mul_i32 s91, s58, 20480
Mvaa 180,132,20
s_add_u32 s91, s91, s28
Mvaa 176,128,20
buffer_load_dword v142, v144, s[8:11], s91 offen offset:0 sc1
Mvaa 176,32,16
W 4
Mvaa 184,32,24
Mvaa 184,128,28
Mvaa 188,132,28
Ta 8,15,49152
Ta 10,15,50176
Mvaa 188,36,24
Ta 12,15,57344
Ta 14,15,58368
Mvaa 248,40,24
Mvaa 248,136,28
Mvaa 252,140,28
Mvaa 252,44,24
Maaa 216,48,24
Maaa 216,144,28
Maaa 220,148,28
Maaa 220,52,24
Maaa 88,56,24
Maaa 88,152,28
Maaa 92,156,28
Maaa 92,60,24
W 4
Maaa 100,60,0
Maaa 100,156,4
Maaa 96,152,4
Ta 16,24,49152
Ta 18,24,50176
Maaa 96,56,0
Ta 20,24,57344
Ta 22,24,58368
Maaa 228,52,0
Maaa 228,148,4
Maaa 224,144,4
Maaa 224,48,0
Maaa 164,44,0
Maaa 164,140,4
Maaa 160,136,4
Maaa 160,40,0
Mvaa 196,36,0
Mvaa 196,132,4
Mvaa 192,128,4
Mvaa 192,32,0
W 4
Mvaa 200,32,8
Ta 24,25,49152
Ta 26,25,50176
Mvaa 200,128,12
Mvaa 204,132,12
.Lh_0_2_1%=:
s_cbranch_execz .Lhr_9%=
Ta 28,25,57344
Ta 30,25,58368
Mvaa 204,36,8
Maaa 168,40,8
Maaa 168,136,12
Maaa 172,140,12
Maaa 172,44,8
Maaa 232,48,8
Maaa 232,144,12
Maaa 236,148,12
s_waitcnt vmcnt(2) lgkmcnt(0)
s_barrier
s_add_u32 s64, s64, 128
Maaa 236,52,8
Maaa 104,56,8
Maaa 104,152,12
s_and_b32 s64, s64, 1023
Maaa 108,156,12
Maaa 108,60,8
Maaa 116,60,16
s_add_u32 m0, s61, 32768
Maaa 116,156,20
Maaa 112,152,20
Ta 0,10,16384
Ta 2,10,17408
Maaa 112,56,16
D 2,40,64,0
Ta 4,10,24576
Maaa 244,52,16
Ta 6,10,25600
Maaa 244,148,20
Maaa 240,144,20
Maaa 240,48,16
D 3,40,64,1024
Maaa 180,44,16
Maaa 180,140,20
Maaa 176,136,20
Maaa 176,40,16
D 4,40,64,2048
Mvaa 212,36,16
Mvaa 212,132,20
Mvaa 208,128,20
Mvaa 208,32,16
D 5,40,64,3072
Mvaa 216,32,24
s_add_u32 m0, s61, 36864
Ra 32,26,0
Mvaa 216,128,28
Ra 128,27,0
Mvaa 220,132,28
Ra 132,27,2048
Ta 8,11,16384
Mvaa 220,36,24
D 6,40,64,0
Ta 10,11,17408
Maaa 184,40,24
Ra 36,26,2048
Ta 12,11,24576
Maaa 184,136,28
Ta 14,11,25600
Ra 40,26,4096
Maaa 188,140,28
Ra 136,27,4096
Ra 140,27,6144
Maaa 188,44,24
D 7,40,64,1024
Ra 44,26,6144
Maaa 248,48,24
Ra 48,26,8192
Maaa 248,144,28
Ra 144,27,8192
Maaa 252,148,28
Ra 148,27,10240
Maaa 252,52,24
D 8,40,64,2048
Ra 52,26,10240
Maaa 120,56,24
Ra 56,26,12288
Maaa 120,152,28
Ra 152,27,12288
Maaa 124,156,28
Ra 156,27,14336
Maaa 124,60,24
W 13
D 9,40,64,3072
Ra 60,26,14336
Mvaa 160,32,0
s_lshl_b32 s65, s64, 12
s_add_u32 m0, s62, 32768
Mvaa 160,128,4
Mvaa 164,132,4
Ta 16,12,16384
Ta 18,12,17408
Mvaa 164,36,0
W 10
D 16,44,65,0
Ta 20,12,24576
Mvaa 224,40,0
Ta 22,12,25600
Mvaa 224,136,4
Mvaa 228,140,4
Mvaa 228,44,0
W 8
D 17,44,65,1024
Maaa 192,48,0
Maaa 192,144,4
Maaa 196,148,4
Maaa 196,52,0
W 4
D 18,44,65,2048
Maaa 64,56,0
Maaa 64,152,4
Maaa 68,156,4
Maaa 68,60,0
D 19,44,65,3072
Maaa 76,60,8
s_add_u32 m0, s62, 36864
Maaa 76,156,12
Maaa 72,152,12
Ta 24,13,16384
Ta 26,13,17408
Maaa 72,56,8
D 20,44,65,0
Ta 28,13,24576
Maaa 204,52,8
Ta 30,13,25600
Maaa 204,148,12
Maaa 200,144,12
Maaa 200,48,8
D 21,44,65,1024
Mvaa 236,44,8
Mvaa 236,140,12
Mvaa 232,136,12
Mvaa 232,40,8
D 22,44,65,2048
Mvaa 172,36,8
Mvaa 172,132,12
Mvaa 168,128,12
Mvaa 168,32,8
W 4
D 23,44,65,3072
Mvaa 176,32,16
v_lshrrev_b32 v138, 4, v1
Mvaa 176,128,20
Mvaa 180,132,20
s_sub_u32 s81, s73, 1
Ta 0,14,16384
Ta 2,14,17408
Mvaa 180,36,16
Ta 4,14,24576
v_min_u32 v138, s81, v138
Mvaa 240,40,16
Ta 6,14,25600
v_lshrrev_b32 v139, 2, v138
Mvaa 240,136,20
v_mul_u32_u24 v139, s72, v139
Mvaa 244,140,20
v_and_b32 v138, 3, v138
Mvaa 244,44,16
v_lshl_add_u32 v138, v138, 7, v139
Maaa 208,48,16
v_add_u32 v138, s74, v138
Maaa 208,144,20
s_lshr_b32 s73, s73, 2
Maaa 212,148,20
s_mul_i32 s73, s73, s72
buffer_load_dword v142, v144, s[8:11], s91 offen offset:2048 sc1
Maaa 212,52,16
v_min_u32 v139, s75, v138
Maaa 80,56,16
Maaa 80,152,20
s_add_u32 s91, s91, 4096
Maaa 84,156,20
global_load_dword v142, v139, s[76:77]
v_add_u32 v138, s73, v138
Maaa 84,60,16
W 4
s_add_u32 s65, s5, 0
s_cmp_ge_u32 s65, 20
s_cselect_b32 s90, 20, 0
Maaa 92,60,24
s_sub_u32 s65, s65, s90
s_lshl_b32 s65, s65, 18
s_add_u32 m0, s63, 131072
Maaa 92,156,28
Maaa 88,152,28
Ta 8,15,16384
Ta 10,15,17408
Maaa 88,56,24
D 16,48,65,0
Ta 12,15,24576
Maaa 220,52,24
Ta 14,15,25600
Maaa 220,148,28
Maaa 216,144,28
Maaa 216,48,24
D 17,48,65,1024
Mvaa 252,44,24
Mvaa 252,140,28
Mvaa 248,136,28
Mvaa 248,40,24
Mvaa 188,36,24
D 18,48,65,2048
Mvaa 188,132,28
Mvaa 184,128,28
Mvaa 184,32,24
W 4
Mvaa 192,32,0
Mvaa 192,128,4
D 19,48,65,3072
Mvaa 196,132,4
s_add_u32 m0, s63, 135168
Ta 16,24,16384
Ta 18,24,17408
Mvaa 196,36,0
Ta 20,24,24576
Ta 22,24,25600
Maaa 160,40,0
Maaa 160,136,4
Maaa 164,140,4
D 20,48,65,0
Maaa 164,44,0
Maaa 224,48,0
Maaa 224,144,4
Maaa 228,148,4
Maaa 228,52,0
D 21,48,65,1024
Maaa 96,56,0
Maaa 96,152,4
Maaa 100,156,4
Maaa 100,60,0
W 4
Maaa 108,60,8
D 22,48,65,2048
Ta 24,25,16384
Maaa 108,156,12
Ta 26,25,17408
Maaa 104,152,12
Ta 28,25,24576
Ta 30,25,25600
Maaa 104,56,8
Maaa 236,52,8
D 23,48,65,3072
Maaa 236,148,12
Maaa 232,144,12
Maaa 232,48,8
Maaa 172,44,8
Maaa 172,140,12
Maaa 168,136,12
s_waitcnt vmcnt(10) lgkmcnt(0)
s_barrier
s_add_u32 s64, s64, 128
Maaa 168,40,8
Mvaa 204,36,8
Mvaa 204,132,12
s_and_b32 s64, s64, 1023
Mvaa 200,128,12
Mvaa 200,32,8
)"
R"(Mvaa 208,32,16
s_add_u32 m0, s61, 0
Mvaa 208,128,20
Mvaa 212,132,20
Ta 0,10,49152
Ta 2,10,50176
Mvaa 212,36,16
D 2,40,64,0
Ta 4,10,57344
Maaa 176,40,16
Ta 6,10,58368
Maaa 176,136,20
Maaa 180,140,20
Maaa 180,44,16
D 3,40,64,1024
Maaa 240,48,16
Maaa 240,144,20
Maaa 244,148,20
Maaa 244,52,16
D 4,40,64,2048
Maaa 112,56,16
Maaa 112,152,20
Maaa 116,156,20
Maaa 116,60,16
D 5,40,64,3072
Maaa 124,60,24
s_add_u32 m0, s61, 4096
Ra 60,26,47104
Maaa 124,156,28
Ra 156,27,47104
Maaa 120,152,28
Ra 152,27,45056
Ta 8,11,49152
Maaa 120,56,24
D 6,40,64,0
Ta 10,11,50176
Maaa 252,52,24
Ra 56,26,45056
Ta 12,11,57344
Maaa 252,148,28
Ta 14,11,58368
Ra 52,26,43008
Maaa 248,144,28
Ra 148,27,43008
Ra 144,27,40960
Maaa 248,48,24
D 7,40,64,1024
Ra 48,26,40960
Maaa 188,44,24
Ra 44,26,38912
Maaa 188,140,28
Ra 140,27,38912
Maaa 184,136,28
Ra 136,27,36864
Maaa 184,40,24
D 8,40,64,2048
Ra 40,26,36864
Mvaa 220,36,24
Ra 36,26,34816
Mvaa 220,132,28
Ra 132,27,34816
Mvaa 216,128,28
Ra 128,27,32768
Mvaa 216,32,24
W 13
D 9,40,64,3072
Ra 32,26,32768
Maaa 68,60,0
s_lshl_b32 s65, s64, 12
s_add_u32 m0, s62, 0
Maaa 68,156,4
Maaa 64,152,4
Ta 16,12,49152
Ta 18,12,50176
Maaa 64,56,0
W 10
D 16,44,65,0
Ta 20,12,57344
Maaa 196,52,0
Ta 22,12,58368
Maaa 196,148,4
Maaa 192,144,4
Maaa 192,48,0
W 8
D 17,44,65,1024
Mvaa 228,44,0
Mvaa 228,140,4
Mvaa 224,136,4
Mvaa 224,40,0
W 4
D 18,44,65,2048
Mvaa 164,36,0
Mvaa 164,132,4
Mvaa 160,128,4
Mvaa 160,32,0
D 19,44,65,3072
Mvaa 168,32,8
s_add_u32 m0, s62, 4096
Mvaa 168,128,12
Mvaa 172,132,12
Ta 24,13,49152
Ta 26,13,50176
Mvaa 172,36,8
D 20,44,65,0
Ta 28,13,57344
Mvaa 232,40,8
Ta 30,13,58368
Mvaa 232,136,12
Mvaa 236,140,12
Mvaa 236,44,8
D 21,44,65,1024
Maaa 200,48,8
Maaa 200,144,12
Maaa 204,148,12
Maaa 204,52,8
D 22,44,65,2048
Maaa 72,56,8
Maaa 72,152,12
Maaa 76,156,12
Maaa 76,60,8
W 4
D 23,44,65,3072
Maaa 84,60,16
v_min_u32 v139, s75, v138
Maaa 84,156,20
Maaa 80,152,20
Ta 0,14,49152
Ta 2,14,50176
Maaa 80,56,16
buffer_load_dword v142, v144, s[8:11], s91 offen offset:0 sc1
Ta 4,14,57344
Maaa 212,52,16
Ta 6,14,58368
Maaa 212,148,20
Maaa 208,144,20
Maaa 208,48,16
global_load_dword v142, v139, s[76:77]
Mvaa 244,44,16
Mvaa 244,140,20
Mvaa 240,136,20
v_add_u32 v138, s73, v138
Mvaa 240,40,16
global_load_dword v142, v146, s[6:7]
Mvaa 180,36,16
Mvaa 180,132,20
Mvaa 176,128,20
Mvaa 176,32,16
W 4
Mvaa 184,32,24
Mvaa 184,128,28
Mvaa 188,132,28
Ta 8,15,49152
Ta 10,15,50176
Mvaa 188,36,24
Ta 12,15,57344
Ta 14,15,58368
Mvaa 248,40,24
Mvaa 248,136,28
Mvaa 252,140,28
Mvaa 252,44,24
Maaa 216,48,24
Maaa 216,144,28
Maaa 220,148,28
Maaa 220,52,24
Maaa 88,56,24
Maaa 88,152,28
Maaa 92,156,28
Maaa 92,60,24
W 4
Maaa 100,60,0
Maaa 100,156,4
Maaa 96,152,4
Ta 16,24,49152
Ta 18,24,50176
Maaa 96,56,0
Ta 20,24,57344
Ta 22,24,58368
Maaa 228,52,0
Maaa 228,148,4
Maaa 224,144,4
Maaa 224,48,0
Maaa 164,44,0
Maaa 164,140,4
Maaa 160,136,4
Maaa 160,40,0
Mvaa 196,36,0
Mvaa 196,132,4
Mvaa 192,128,4
Mvaa 192,32,0
W 4
Mvaa 200,32,8
Ta 24,25,49152
Ta 26,25,50176
Mvaa 200,128,12
Mvaa 204,132,12
Ta 28,25,57344
Ta 30,25,58368
Mvaa 204,36,8
Maaa 168,40,8
Maaa 168,136,12
Maaa 172,140,12
Maaa 172,44,8
Maaa 232,48,8
Maaa 232,144,12
Maaa 236,148,12
s_waitcnt vmcnt(3) lgkmcnt(0)
s_barrier
s_cmp_eq_u32 s85, 107
s_cbranch_scc1 .Lrt_23%=
.Lrtr_24%=:
Maaa 236,52,8
s_add_u32 s64, s64, 128
Maaa 104,56,8
Maaa 104,152,12
s_and_b32 s64, s64, 1023
Maaa 108,156,12
Maaa 108,60,8
Maaa 116,60,16
s_add_u32 m0, s61, 32768
Maaa 116,156,20
Maaa 112,152,20
Ta 0,10,16384
Ta 2,10,17408
Maaa 112,56,16
D 2,40,64,0
Ta 4,10,24576
Maaa 244,52,16
Ta 6,10,25600
Maaa 244,148,20
Maaa 240,144,20
Maaa 240,48,16
D 3,40,64,1024
Maaa 180,44,16
Maaa 180,140,20
Maaa 176,136,20
Maaa 176,40,16
D 4,40,64,2048
Mvaa 212,36,16
Mvaa 212,132,20
Mvaa 208,128,20
Mvaa 208,32,16
D 5,40,64,3072
Mvaa 216,32,24
s_add_u32 m0, s61, 36864
Ra 32,26,0
Mvaa 216,128,28
Ra 128,27,0
Mvaa 220,132,28
Ra 132,27,2048
Ta 8,11,16384
Mvaa 220,36,24
D 6,40,64,0
Ta 10,11,17408
Maaa 184,40,24
Ra 36,26,2048
Ta 12,11,24576
Maaa 184,136,28
Ta 14,11,25600
Ra 40,26,4096
Maaa 188,140,28
Ra 136,27,4096
Ra 140,27,6144
Maaa 188,44,24
D 7,40,64,1024
Ra 44,26,6144
Maaa 248,48,24
Ra 48,26,8192
Maaa 248,144,28
Ra 144,27,8192
Maaa 252,148,28
Ra 148,27,10240
Maaa 252,52,24
D 8,40,64,2048
Ra 52,26,10240
Maaa 120,56,24
Ra 56,26,12288
Maaa 120,152,28
.Lh_0_3_1%=:
s_cbranch_execz .Lhr_9%=
Ra 152,27,12288
Maaa 124,156,28
Ra 156,27,14336
Maaa 124,60,24
W 13
D 9,40,64,3072
Ra 60,26,14336
Mvaa 160,32,0
s_lshl_b32 s65, s64, 12
s_add_u32 m0, s62, 32768
Mvaa 160,128,4
Mvaa 164,132,4
Ta 16,12,16384
Ta 18,12,17408
Mvaa 164,36,0
W 10
D 16,44,65,0
Ta 20,12,24576
Mvaa 224,40,0
Ta 22,12,25600
Mvaa 224,136,4
Mvaa 228,140,4
Mvaa 228,44,0
W 8
D 17,44,65,1024
Maaa 192,48,0
Maaa 192,144,4
Maaa 196,148,4
Maaa 196,52,0
W 4
D 18,44,65,2048
Maaa 64,56,0
Maaa 64,152,4
Maaa 68,156,4
Maaa 68,60,0
D 19,44,65,3072
Maaa 76,60,8
s_add_u32 m0, s62, 36864
Maaa 76,156,12
Maaa 72,152,12
Ta 24,13,16384
Ta 26,13,17408
Maaa 72,56,8
D 20,44,65,0
Ta 28,13,24576
Maaa 204,52,8
Ta 30,13,25600
Maaa 204,148,12
Maaa 200,144,12
Maaa 200,48,8
D 21,44,65,1024
Mvaa 236,44,8
Mvaa 236,140,12
Mvaa 232,136,12
Mvaa 232,40,8
D 22,44,65,2048
Mvaa 172,36,8
Mvaa 172,132,12
Mvaa 168,128,12
Mvaa 168,32,8
W 4
D 23,44,65,3072
Mvaa 176,32,16
v_min_u32 v139, s75, v138
Mvaa 176,128,20
Mvaa 180,132,20
Ta 0,14,16384
Ta 2,14,17408
Mvaa 180,36,16
buffer_load_dword v142, v144, s[8:11], s91 offen offset:2048 sc1
Ta 4,14,24576
Mvaa 240,40,16
Ta 6,14,25600
Mvaa 240,136,20
s_add_u32 s91, s91, 4096
Mvaa 244,140,20
Mvaa 244,44,16
global_load_dword v142, v139, s[76:77]
Maaa 208,48,16
Maaa 208,144,20
Maaa 212,148,20
v_add_u32 v138, s73, v138
v_add_u32 v2, 131072, v10
Maaa 212,52,16
v_add_u32 v3, 131072, v11
v_add_u32 v4, 131072, v12
Maaa 80,56,16
v_add_u32 v5, 131072, v13
v_add_u32 v6, 131072, v14
Maaa 80,152,20
v_add_u32 v7, 131072, v15
v_add_u32 v8, 131072, v24
Maaa 84,156,20
v_add_u32 v9, 131072, v25
Maaa 84,60,16
W 4
Maaa 92,60,24
Maaa 92,156,28
Maaa 88,152,28
Ta 8,15,16384
Ta 10,15,17408
Maaa 88,56,24
Ta 12,15,24576
Ta 14,15,25600
Maaa 220,52,24
Maaa 220,148,28
Maaa 216,144,28
Maaa 216,48,24
Mvaa 252,44,24
Mvaa 252,140,28
Mvaa 248,136,28
Mvaa 248,40,24
Mvaa 188,36,24
Mvaa 188,132,28
Mvaa 184,128,28
Mvaa 184,32,24
W 4
Mvaa 192,32,0
Mvaa 192,128,4
Mvaa 196,132,4
Ta 16,24,16384
)"
R"(Ta 18,24,17408
Mvaa 196,36,0
Ta 20,24,24576
Ta 22,24,25600
Maaa 160,40,0
Maaa 160,136,4
Maaa 164,140,4
Maaa 164,44,0
Maaa 224,48,0
Maaa 224,144,4
Maaa 228,148,4
Maaa 228,52,0
Maaa 96,56,0
Maaa 96,152,4
Maaa 100,156,4
Maaa 100,60,0
W 4
Maaa 108,60,8
Ta 24,25,16384
Ta 26,25,17408
Maaa 108,156,12
Maaa 104,152,12
Ta 28,25,24576
Ta 30,25,25600
Maaa 104,56,8
Maaa 236,52,8
Maaa 236,148,12
Maaa 232,144,12
Maaa 232,48,8
Maaa 172,44,8
Maaa 172,140,12
Maaa 168,136,12
s_waitcnt vmcnt(2) lgkmcnt(0)
s_barrier
s_cmp_eq_u32 s85, 107
s_cbranch_scc1 .Lrt_25%=
.Lrtr_26%=:
Maaa 168,40,8
s_add_u32 s65, s5, 4
Mvaa 204,36,8
Mvaa 204,132,12
s_cmp_ge_u32 s65, 20
s_cselect_b32 s90, 20, 0
Mvaa 200,128,12
Mvaa 200,32,8
s_sub_u32 s65, s65, s90
Mvaa 208,32,16
Mvaa 208,128,20
s_lshl_b32 s65, s65, 18
Mvaa 212,132,20
Ta 0,10,49152
Ta 2,10,50176
Mvaa 212,36,16
Maaa 176,40,16
s_add_u32 m0, s63, 0
Maaa 176,136,20
Maaa 180,140,20
D 16,48,65,0
Maaa 180,44,16
Maaa 240,48,16
Maaa 240,144,20
Maaa 244,148,20
D 17,48,65,1024
Ta 4,11,49152
Maaa 244,52,16
Ta 6,11,50176
Maaa 112,56,16
Maaa 112,152,20
Maaa 116,156,20
D 18,48,65,2048
Maaa 116,60,16
Maaa 124,60,24
Ra 60,26,47104
Maaa 124,156,28
Maaa 120,152,28
D 19,48,65,3072
Ta 8,12,49152
Maaa 120,56,24
s_add_u32 m0, s63, 4096
Ta 10,12,50176
Ra 56,26,45056
Maaa 252,52,24
Ra 52,26,43008
Maaa 252,148,28
Maaa 248,144,28
D 20,48,65,0
Maaa 248,48,24
Ra 48,26,40960
Maaa 188,44,24
Ra 44,26,38912
Maaa 188,140,28
Maaa 184,136,28
D 21,48,65,1024
Ta 12,13,49152
Maaa 184,40,24
Ta 14,13,50176
Ra 40,26,36864
Mvaa 220,36,24
Ra 36,26,34816
Mvaa 220,132,28
Mvaa 216,128,28
D 22,48,65,2048
Mvaa 216,32,24
Ra 32,26,32768
W 6
Maaa 68,60,0
Maaa 64,56,0
Maaa 196,52,0
D 23,48,65,3072
Ta 16,14,49152
Maaa 192,48,0
W 1
s_add_u32 s65, s5, 8
s_cmp_ge_u32 s65, 20
s_cselect_b32 s90, 20, 0
Mvaa 228,44,0
s_sub_u32 s65, s65, s90
s_lshl_b32 s65, s65, 18
Ta 18,14,50176
Mvaa 224,40,0
s_add_u32 m0, s63, 65536
Mvaa 164,36,0
D 16,48,65,0
Mvaa 160,32,0
Mvaa 168,32,4
Mvaa 172,36,4
Mvaa 232,40,4
D 17,48,65,1024
Ta 20,15,49152
Mvaa 236,44,4
Ta 22,15,50176
Maaa 200,48,4
Maaa 204,52,4
Maaa 72,56,4
D 18,48,65,2048
Maaa 76,60,4
Maaa 84,60,8
Maaa 80,56,8
Maaa 212,52,8
D 19,48,65,3072
Ta 24,24,49152
Maaa 208,48,8
s_add_u32 m0, s63, 69632
Ta 26,24,50176
Mvaa 244,44,8
Mvaa 240,40,8
Mvaa 180,36,8
D 20,48,65,0
Mvaa 176,32,8
Mvaa 184,32,12
Mvaa 188,36,12
Mvaa 248,40,12
D 21,48,65,1024
Ta 28,25,49152
Mvaa 252,44,12
Ta 30,25,50176
Maaa 216,48,12
Ra 156,27,47104
Maaa 220,52,12
Maaa 88,56,12
D 22,48,65,2048
Maaa 92,60,12
W 7
Ra 152,27,45056
Maaa 100,60,16
Maaa 96,56,16
Maaa 228,52,16
D 23,48,65,3072
Ra 148,27,43008
Maaa 224,48,16
Ta 0,10,57344
Ta 2,10,58368
Maaa 164,44,16
Maaa 160,40,16
Ra 144,27,40960
Mvaa 196,36,16
buffer_load_dword v142, v144, s[8:11], s91 offen offset:0 sc1
Mvaa 192,32,16
W 10
Mvaa 200,32,20
Ra 140,27,38912
Mvaa 204,36,20
Maaa 168,40,20
Ta 4,11,57344
Ta 6,11,58368
Maaa 172,44,20
Ra 136,27,36864
Maaa 232,48,20
Maaa 236,52,20
v_bfe_u32 v29, v1, 7, 1
Maaa 104,56,20
Ra 132,27,34816
Maaa 108,60,20
W 13
v_mul_u32_u24 v28, 10240, v29
Maaa 116,60,24
Maaa 112,56,24
Ra 128,27,32768
v_lshrrev_b32 v29, 8, v1
Maaa 244,52,24
Ta 8,12,57344
Ta 10,12,58368
Maaa 240,48,24
v_lshl_add_u32 v28, v29, 7, v28
Maaa 180,44,24
v_and_b32 v29, 0x70, v1
Maaa 176,40,24
Mvaa 212,36,24
v_add_u32 v28, v28, v29
Mvaa 208,32,24
W 14
Mvaa 216,32,28
s_and_b32 s80, s14, 2
Mvaa 220,36,28
s_mul_i32 s80, s80, 20480
Maaa 184,40,28
Ta 12,13,57344
Ta 14,13,58368
Maaa 188,44,28
s_sub_u32 s81, 40960, s80
Maaa 248,48,28
Maaa 252,52,28
s_add_u32 s93, s28, 20480
Maaa 120,56,28
Maaa 124,60,28
W 10
v_add_u32 v26, s80, v28
Maaa 68,156,0
Maaa 64,152,0
Maaa 196,148,0
Ta 16,14,57344
v_add_u32 v27, s81, v28
Maaa 192,144,0
W 5
Ta 18,14,58368
Mvaa 228,140,0
buffer_load_dwordx4 a[32:35], v26, s[8:11], s28 offen offset:0 sc1
Mvaa 224,136,0
Mvaa 164,132,0
Mvaa 160,128,0
Mvaa 168,128,4
buffer_load_dwordx4 a[36:39], v26, s[8:11], s93 offen offset:0 sc1
Mvaa 172,132,4
Mvaa 232,136,4
Ta 20,15,57344
Ta 22,15,58368
Mvaa 236,140,4
Maaa 200,144,4
buffer_load_dwordx4 a[40:43], v27, s[8:11], s28 offen offset:0 sc1
Maaa 204,148,4
Maaa 72,152,4
Maaa 76,156,4
W 6
Maaa 84,156,8
buffer_load_dwordx4 a[44:47], v27, s[8:11], s93 offen offset:0 sc1
Ta 24,24,57344
Maaa 80,152,8
s_add_u32 s28, s28, 512
s_cmp_eq_u32 s28, s4
s_cselect_b32 s93, 10240, 0
Ta 26,24,58368
Maaa 212,148,8
s_sub_u32 s28, s28, s93
Ta 28,25,57344
Ta 30,25,58368
Maaa 208,144,8
s_add_u32 s93, s28, 20480
Mvaa 244,140,8
Mvaa 240,136,8
Mvaa 180,132,8
s_add_u32 s65, s5, 12
s_cmp_ge_u32 s65, 20
s_cselect_b32 s90, 20, 0
buffer_load_dwordx4 a[48:51], v26, s[8:11], s28 offen offset:0 sc1
Mvaa 176,128,8
W 8
s_sub_u32 s65, s65, s90
s_lshl_b32 s65, s65, 18
Mvaa 184,128,12
s_add_u32 m0, s63, 32768
Mvaa 188,132,12
Mvaa 248,136,12
s_waitcnt vmcnt(6) lgkmcnt(0)
s_barrier
buffer_load_dwordx4 a[52:55], v26, s[8:11], s93 offen offset:0 sc1
Mvaa 252,140,12
Maaa 216,144,12
Maaa 220,148,12
Maaa 88,152,12
buffer_load_dwordx4 a[56:59], v27, s[8:11], s28 offen offset:0 sc1
Maaa 92,156,12
Maaa 100,156,16
Maaa 96,152,16
Maaa 228,148,16
buffer_load_dwordx4 a[60:63], v27, s[8:11], s93 offen offset:0 sc1
Maaa 224,144,16
Maaa 164,140,16
Maaa 160,136,16
Mvaa 196,132,16
D 16,48,65,0
Mvaa 192,128,16
Mvaa 200,128,20
Mvaa 204,132,20
Maaa 168,136,20
D 17,48,65,1024
Ta 0,2,0
Maaa 172,140,20
Ta 2,2,1024
Maaa 232,144,20
Maaa 236,148,20
Maaa 104,152,20
D 18,48,65,2048
Ta 4,2,8192
Maaa 108,156,20
Ta 6,2,9216
Maaa 116,156,24
Maaa 112,152,24
Maaa 244,148,24
Ta 8,3,0
Ta 10,3,1024
Maaa 240,144,24
D 19,48,65,3072
Maaa 180,140,24
s_add_u32 m0, s63, 36864
Maaa 176,136,24
Mvaa 212,132,24
Ta 12,3,8192
Ta 14,3,9216
Mvaa 208,128,24
D 20,48,65,0
Mvaa 216,128,28
Mvaa 220,132,28
Maaa 184,136,28
Ta 16,4,0
Ta 18,4,1024
Maaa 188,140,28
Maaa 248,144,28
D 21,48,65,1024
Maaa 252,148,28
Maaa 120,152,28
Maaa 124,156,28
.Lq_11%=:
s_cmp_ge_u32 s14, 2
s_cbranch_scc1 .Lpb_31%=
s_waitcnt vmcnt(6) lgkmcnt(6)
Zvaa 32,0,32
s_add_u32 s28, s28, 512
Ta 20,4,8192
Ta 22,4,9216
Mvaa 32,4,48
s_cmp_eq_u32 s28, s4
s_cselect_b32 s93, 10240, 0
Ta 24,5,0
Ta 26,5,1024
Zvaa 64,4,52
s_sub_u32 s28, s28, s93
s_add_u32 s93, s28, 20480
buffer_load_dwordx4 a[128:131], v26, s[8:11], s28 offen offset:0 sc1
Mvaa 64,0,36
Ta 28,5,8192
Ta 30,5,9216
Zvaa 96,0,40
K 184,184,185
K 185,186,187
Mvaa 96,4,56
K 186,188,189
K 187,190,191
Zvaa 128,4,60
K 192,192,193
Mvaa 128,0,44
W 8
buffer_load_dwordx4 a[132:135], v26, s[8:11], s93 offen offset:0 sc1
K 193,194,195
Zvaa 132,8,44
Ta 0,6,0
Ta 2,6,1024
Mvaa 132,12,60
K 194,196,197
K 195,198,199
Zvaa 100,12,56
K 200,200,201
K 201,202,203
Mvaa 100,8,40
Ta 4,6,8192
Ta 6,6,9216
Zvaa 68,8,36
buffer_load_dwordx4 a[136:139], v27, s[8:11], s28 offen offset:0 sc1
K 202,204,205
Mvaa 68,12,52
K 203,206,207
K 208,208,209
Zvaa 36,12,48
K 209,210,211
K 210,212,213
Mvaa 36,8,32
W 8
Ta 8,7,0
K 211,214,215
Zvaa 40,16,32
Ta 10,7,1024
)"
R"(K 216,216,217
Mvaa 40,20,48
buffer_load_dwordx4 a[140:143], v27, s[8:11], s93 offen offset:0 sc1
K 217,218,219
Zvaa 72,20,52
s_add_u32 s28, s28, 512
s_cmp_eq_u32 s28, s4
s_cselect_b32 s93, 10240, 0
s_sub_u32 s28, s28, s93
Mvaa 72,16,36
s_add_u32 s93, s28, 20480
K 218,220,221
K 219,222,223
Zvaa 104,16,40
Ta 12,7,8192
Ta 14,7,9216
Mvaa 104,20,56
K 224,224,225
K 225,226,227
Zvaa 136,20,60
K 226,228,229
K 227,230,231
Mvaa 136,16,44
W 8
buffer_load_dwordx4 a[144:147], v26, s[8:11], s28 offen offset:0 sc1
K 232,232,233
Zvaa 140,24,44
K 233,234,235
K 234,236,237
Mvaa 140,28,60
Ta 16,8,0
K 235,238,239
Zvaa 108,28,56
Ta 18,8,1024
K 240,240,241
Mvaa 108,24,40
K 241,242,243
K 242,244,245
Zvaa 76,24,36
buffer_load_dwordx4 a[148:151], v26, s[8:11], s93 offen offset:0 sc1
K 243,246,247
Mvaa 76,28,52
Ta 20,8,8192
Ta 22,8,9216
Zvaa 44,28,48
K 248,248,249
K 249,250,251
Mvaa 44,24,32
K 250,252,253
K 251,254,255
W 8
Zvaa 48,0,32
Ta 24,9,0
Ta 26,9,1024
Mvaa 48,4,48
buffer_load_dwordx4 a[152:155], v27, s[8:11], s28 offen offset:0 sc1
K 160,160,161
Zvaa 80,4,52
Ta 28,9,8192
Ta 30,9,9216
Mvaa 80,0,36
K 161,162,163
Zvaa 112,0,40
K 162,164,165
Mvaa 112,4,56
Zvaa 144,4,60
buffer_load_dwordx4 a[156:159], v27, s[8:11], s93 offen offset:0 sc1
K 163,166,167
Mvaa 144,0,44
W 8
s_add_u32 s28, s28, 512
s_cmp_eq_u32 s28, s4
s_cselect_b32 s93, 10240, 0
Zvaa 148,8,44
s_sub_u32 s28, s28, s93
s_add_u32 s93, s28, 20480
Ta 0,2,16384
Mvaa 148,12,60
s_add_u32 s36, s29, 0
Ta 2,2,17408
Zvaa 116,12,56
Ta 4,2,24576
Ta 6,2,25600
Mvaa 116,8,40
buffer_load_dwordx4 v[188:191], v26, s[8:11], s28 offen offset:0 sc1
Zvaa 84,8,36
Mvaa 84,12,52
Zvaa 52,12,48
Mvaa 52,8,32
W 8
Ta 8,3,16384
Ta 10,3,17408
Zvaa 56,16,32
buffer_load_dwordx4 v[196:199], v26, s[8:11], s93 offen offset:0 sc1
Mvaa 56,20,48
Zvaa 88,20,52
Ta 12,3,24576
Ta 14,3,25600
Mvaa 88,16,36
Zvaa 120,16,40
buffer_store_dwordx4 v[160:163], v1, s[8:11], s36 offen offset:0 sc1
K 168,168,169
Mvaa 120,20,56
K 169,170,171
K 170,172,173
Zvaa 152,20,60
Mvaa 152,16,44
W 8
Ta 16,4,16384
K 171,174,175
Zvaa 156,24,44
Ta 18,4,17408
Mvaa 156,28,60
buffer_load_dwordx4 v[204:207], v27, s[8:11], s28 offen offset:0 sc1
Zvaa 124,28,56
Ta 20,4,24576
Ta 22,4,25600
Mvaa 124,24,40
Zvaa 92,24,36
Mvaa 92,28,52
Zvaa 60,28,48
buffer_load_dwordx4 v[212:215], v27, s[8:11], s93 offen offset:0 sc1
Mvaa 60,24,32
s_waitcnt vmcnt(5) lgkmcnt(8)
s_add_u32 s28, s28, 512
s_cmp_eq_u32 s28, s4
s_cselect_b32 s93, 10240, 0
Mvaa 32,0,128
s_sub_u32 s28, s28, s93
s_add_u32 s93, s28, 20480
Ta 24,5,16384
Mvaa 32,4,144
Ta 26,5,17408
Mvaa 64,4,148
Ta 28,5,24576
Ta 30,5,25600
Mvaa 64,0,132
buffer_store_dwordx4 v[168:171], v1, s[8:11], s36 offen offset:1024 sc1
K 176,176,177
Mvaa 96,0,136
K 177,178,179
K 178,180,181
Mvaa 96,4,152
Mvaa 128,4,156
Mvaa 128,0,140
W 8
Ta 0,6,16384
K 179,182,183
Mvaa 132,8,140
buffer_load_dwordx4 v[220:223], v26, s[8:11], s28 offen offset:0 sc1
Ta 2,6,17408
Mvaa 132,12,156
Mvaa 100,12,152
Ta 4,6,24576
Ta 6,6,25600
Mvaa 100,8,136
Mvaa 68,8,132
Mvaa 68,12,148
buffer_load_dwordx4 v[228:231], v26, s[8:11], s93 offen offset:0 sc1
Mvaa 36,12,144
Mvaa 36,8,128
W 8
Ta 8,7,16384
Ta 10,7,17408
Mvaa 40,16,128
Mvaa 40,20,144
Mvaa 72,20,148
buffer_load_dwordx4 v[236:239], v27, s[8:11], s28 offen offset:0 sc1
Ta 12,7,24576
Mvaa 72,16,132
buffer_store_dwordx4 v[176:179], v1, s[8:11], s36 offen offset:2048 sc1
Ta 14,7,25600
Mvaa 104,16,136
Mvaa 104,20,152
Mvaa 136,20,156
Mvaa 136,16,140
W 8
Ta 16,8,16384
Ta 18,8,17408
Mvaa 140,24,140
buffer_load_dwordx4 v[244:247], v27, s[8:11], s93 offen offset:0 sc1
Mvaa 140,28,156
s_add_u32 s28, s28, 512
s_cmp_eq_u32 s28, s4
s_cselect_b32 s93, 10240, 0
s_sub_u32 s28, s28, s93
Mvaa 108,28,152
s_add_u32 s93, s28, 20480
Ta 20,8,24576
Ta 22,8,25600
Mvaa 108,24,136
Mvaa 76,24,132
Mvaa 76,28,148
buffer_load_dwordx4 v[252:255], v26, s[8:11], s28 offen offset:0 sc1
Mvaa 44,28,144
Mvaa 44,24,128
W 8
Ta 24,9,16384
Ta 26,9,17408
Mvaa 48,0,128
Mvaa 48,4,144
Mvaa 80,4,148
D 22,48,65,2048
Ta 28,9,24576
Mvaa 80,0,132
buffer_store_dwordx4 v[184:187], v1, s[8:11], s36 offen offset:3072 sc1
Ta 30,9,25600
Mvaa 112,0,136
s_add_u32 s36, s29, 4096
s_lshl_b32 s80, s56, 9
Mvaa 112,4,152
Mvaa 144,4,156
s_lshl_b32 s81, s57, 4
Mvaa 144,0,140
W 8
s_add_u32 s80, s80, s81
Ta 0,10,0
Mvaa 148,8,140
buffer_load_dwordx4 a[32:35], v26, s[8:11], s93 offen offset:0 sc1
Ta 2,10,1024
Mvaa 148,12,156
s_and_b32 s81, s14, 1
Mvaa 116,12,152
s_lshl_b32 s81, s81, 3
Ta 4,10,8192
Ta 6,10,9216
Mvaa 116,8,136
Mvaa 84,8,132
s_add_u32 s80, s80, s81
Mvaa 84,12,148
s_add_u32 s71, s80, 4096
buffer_load_dwordx4 a[36:39], v27, s[8:11], s28 offen offset:0 sc1
Mvaa 52,12,144
Mvaa 52,8,128
W 8
Ta 8,11,0
Ta 10,11,1024
Mvaa 56,16,128
Mvaa 56,20,144
Mvaa 88,20,148
buffer_store_dwordx4 v[192:195], v1, s[8:11], s36 offen offset:0 sc1
Ta 12,11,8192
Mvaa 88,16,132
Ta 14,11,9216
Mvaa 120,16,136
Mvaa 120,20,152
Mvaa 152,20,156
Mvaa 152,16,140
W 8
buffer_load_dwordx4 a[40:43], v27, s[8:11], s93 offen offset:0 sc1
Ta 16,12,0
Mvaa 156,24,140
s_add_u32 s28, s28, 512
s_cmp_eq_u32 s28, s4
s_cselect_b32 s93, 10240, 0
s_sub_u32 s28, s28, s93
Mvaa 156,28,156
s_add_u32 s93, s28, 20480
Ta 18,12,1024
Mvaa 124,28,152
Ta 20,12,8192
Ta 22,12,9216
Mvaa 124,24,136
Mvaa 92,24,132
buffer_load_dwordx4 a[44:47], v26, s[8:11], s28 offen offset:0 sc1
Mvaa 92,28,148
Mvaa 60,28,144
Mvaa 60,24,128
s_waitcnt vmcnt(8) lgkmcnt(8)
Ta 24,13,0
Ta 26,13,1024
Mvav 32,0,188
Mvav 32,4,220
D 23,48,65,3072
Mvav 64,4,228
s_add_u32 s65, s5, 16
s_cmp_ge_u32 s65, 20
s_cselect_b32 s90, 20, 0
buffer_store_dwordx4 v[200:203], v1, s[8:11], s36 offen offset:1024 sc1
Mvav 64,0,196
s_sub_u32 s65, s65, s90
s_lshl_b32 s65, s65, 18
s_add_u32 m0, s63, 98304
Mvav 96,0,204
Ta 28,13,8192
Ta 30,13,9216
Mvav 96,4,236
Mvav 128,4,244
Mvav 128,0,212
W 8
buffer_load_dwordx4 a[48:51], v26, s[8:11], s93 offen offset:0 sc1
Ta 0,14,0
Mvav 132,8,212
Ta 2,14,1024
v_add_u32 v2, 65536, v10
Mvav 132,12,244
Mvav 100,12,236
Ta 4,14,8192
Ta 6,14,9216
Mvav 100,8,204
v_add_u32 v3, 65536, v11
Mvav 68,8,196
buffer_load_dwordx4 a[52:55], v27, s[8:11], s28 offen offset:0 sc1
Mvav 68,12,228
Mvav 36,12,220
Mvav 36,8,188
W 8
Ta 8,15,0
Ta 10,15,1024
Mvav 40,16,188
v_add_u32 v4, 65536, v12
Mvav 40,20,220
buffer_load_dwordx4 a[56:59], v27, s[8:11], s93 offen offset:0 sc1
Mvav 72,20,228
s_add_u32 s28, s28, 512
s_cmp_eq_u32 s28, s4
s_cselect_b32 s93, 10240, 0
buffer_store_dwordx4 v[208:211], v1, s[8:11], s36 offen offset:2048 sc1
Mvav 72,16,196
s_sub_u32 s28, s28, s93
s_add_u32 s93, s28, 20480
Ta 12,15,8192
Mvav 104,16,204
Ta 14,15,9216
v_add_u32 v5, 65536, v13
Mvav 104,20,236
Mvav 136,20,244
Mvav 136,16,212
W 8
D 16,48,65,0
Ta 16,24,0
Mvav 140,24,212
Ta 18,24,1024
v_add_u32 v6, 65536, v14
Mvav 140,28,244
Mvav 108,28,236
Ta 20,24,8192
Ta 22,24,9216
Mvav 108,24,204
v_add_u32 v7, 65536, v15
Mvav 76,24,196
buffer_load_dwordx4 a[60:63], v26, s[8:11], s28 offen offset:0 sc1
Mvav 76,28,228
Mvav 44,28,220
Mvav 44,24,188
W 8
Ta 24,25,0
Ta 26,25,1024
Mvav 48,0,188
v_add_u32 v8, 65536, v24
Mvav 48,4,220
buffer_load_dwordx4 v[184:187], v26, s[8:11], s93 offen offset:0 sc1
Mvav 80,4,228
buffer_store_dwordx4 v[216:219], v1, s[8:11], s36 offen offset:3072 sc1
Ta 28,25,8192
Mvav 80,0,196
s_add_u32 s36, s29, 458752
Ta 30,25,9216
v_add_u32 v9, 65536, v25
Mvav 112,0,204
Mvav 112,4,236
Mvav 144,4,244
Mvav 144,0,212
W 8
buffer_load_dwordx4 v[192:195], v27, s[8:11], s28 offen offset:0 sc1
Ta 0,10,16384
Mvav 148,8,212
Ta 2,10,17408
Mvav 148,12,244
Mvav 116,12,236
Ta 4,10,24576
Ta 6,10,25600
Mvav 116,8,204
Mvav 84,8,196
D 17,48,65,1024
Mvav 84,12,228
Mvav 52,12,220
Mvav 52,8,188
W 8
Ta 8,11,16384
Ta 10,11,17408
Mvav 56,16,188
Mvav 56,20,220
buffer_store_dwordx4 v[224:227], v1, s[8:11], s36 offen offset:0 sc1
Mvav 88,20,228
Ta 12,11,24576
Ta 14,11,25600
Mvav 88,16,196
Mvav 120,16,204
Mvav 120,20,236
Mvav 152,20,244
buffer_load_dwordx4 a[128:131], v27, s[8:11], s93 offen offset:0 sc1
Mvav 152,16,212
W 8
s_add_u32 s28, s28, 512
s_cmp_eq_u32 s28, s4
s_cselect_b32 s93, 10240, 0
Mvav 156,24,212
s_sub_u32 s28, s28, s93
s_add_u32 s93, s28, 20480
)"
R"(Ta 16,12,16384
Mvav 156,28,244
Ta 18,12,17408
Mvav 124,28,236
Ta 20,12,24576
Ta 22,12,25600
Mvav 124,24,204
buffer_load_dwordx4 a[132:135], v26, s[8:11], s28 offen offset:0 sc1
Mvav 92,24,196
Mvav 92,28,228
Mvav 60,28,220
Mvav 60,24,188
s_waitcnt vmcnt(10) lgkmcnt(8)
Ta 24,13,16384
Ta 26,13,17408
Mvav 32,0,252
buffer_load_dwordx4 a[136:139], v26, s[8:11], s93 offen offset:0 sc1
Mvaa 32,4,44
buffer_store_dwordx4 v[232:235], v1, s[8:11], s36 offen offset:1024 sc1
Mvaa 64,4,48
Ta 28,13,24576
Ta 30,13,25600
Mvaa 64,0,32
Mvaa 96,0,36
Mvaa 96,4,52
Mvaa 128,4,56
buffer_load_dwordx4 a[140:143], v27, s[8:11], s28 offen offset:0 sc1
Mvaa 128,0,40
W 8
Ta 0,14,16384
Ta 2,14,17408
Mvaa 132,8,40
Mvaa 132,12,56
Mvaa 100,12,52
Ta 4,14,24576
Ta 6,14,25600
Mvaa 100,8,36
D 18,48,65,2048
Mvaa 68,8,32
Mvaa 68,12,48
Mvaa 36,12,44
Mvav 36,8,252
W 8
Ta 8,15,16384
Ta 10,15,17408
Mvav 40,16,252
buffer_load_dwordx4 a[144:147], v27, s[8:11], s93 offen offset:0 sc1
Mvaa 40,20,44
s_add_u32 s28, s28, 512
s_cmp_eq_u32 s28, s4
s_cselect_b32 s93, 10240, 0
buffer_store_dwordx4 v[240:243], v1, s[8:11], s36 offen offset:2048 sc1
Mvaa 72,20,48
s_sub_u32 s28, s28, s93
s_add_u32 s93, s28, 20480
Ta 12,15,24576
Mvaa 72,16,32
Ta 14,15,25600
Mvaa 104,16,36
Mvaa 104,20,52
Mvaa 136,20,56
buffer_load_dwordx4 a[148:151], v26, s[8:11], s28 offen offset:0 sc1
Mvaa 136,16,40
W 8
Ta 16,24,16384
Ta 18,24,17408
Mvaa 140,24,40
Mvaa 140,28,56
Mvaa 108,28,52
Ta 20,24,24576
Ta 22,24,25600
Mvaa 108,24,36
buffer_load_dwordx4 a[152:155], v26, s[8:11], s93 offen offset:0 sc1
Mvaa 76,24,32
Mvaa 76,28,48
Mvaa 44,28,44
Mvav 44,24,252
W 8
Ta 24,25,16384
Ta 26,25,17408
Mvav 48,0,252
buffer_load_dwordx4 a[156:159], v27, s[8:11], s28 offen offset:0 sc1
Mvaa 48,4,44
buffer_store_dwordx4 v[248:251], v1, s[8:11], s36 offen offset:3072 sc1
G 160,160
Mvaa 80,4,48
G 161,161
G 162,162
Mvaa 80,0,32
G 163,163
G 164,164
Mvaa 112,0,36
G 165,165
G 166,166
Mvaa 112,4,52
Ta 28,25,24576
Ta 30,25,25600
Mvaa 144,4,56
D 19,48,65,3072
G 167,167
Mvaa 144,0,40
W 8
s_add_u32 m0, s63, 102400
K 160,160,161
Mvaa 148,8,40
K 161,162,163
K 162,164,165
Mvaa 148,12,56
Ta 0,2,0
K 163,166,167
Mvaa 116,12,52
s_add_u32 s36, s29, 462848
Ta 2,2,1024
Ta 4,2,8192
Mvaa 116,8,36
buffer_load_dwordx4 v[200:203], v27, s[8:11], s93 offen offset:0 sc1
Ta 6,2,9216
Mvaa 84,8,32
s_add_u32 s28, s28, 512
s_cmp_eq_u32 s28, s4
s_cselect_b32 s93, 10240, 0
s_sub_u32 s28, s28, s93
Mvaa 84,12,48
s_add_u32 s93, s28, 20480
Mvaa 52,12,44
Mvav 52,8,252
W 8
Ta 8,3,0
Ta 10,3,1024
Mvav 56,16,252
buffer_store_dwordx4 v[160:163], v1, s[8:11], s36 offen offset:0 sc1
G 168,168
Mvaa 56,20,44
G 169,169
G 170,170
Mvaa 88,20,48
G 171,171
G 172,172
Mvaa 88,16,32
G 173,173
G 174,174
Mvaa 120,16,36
Ta 12,3,8192
Ta 14,3,9216
Mvaa 120,20,52
buffer_load_dwordx4 v[208:211], v26, s[8:11], s28 offen offset:0 sc1
G 175,175
Mvaa 152,20,56
K 168,168,169
K 169,170,171
Mvaa 152,16,40
W 8
K 170,172,173
K 171,174,175
Mvaa 156,24,40
Ta 16,4,0
Ta 18,4,1024
Mvaa 156,28,56
Mvaa 124,28,52
buffer_load_dwordx4 v[216:219], v26, s[8:11], s93 offen offset:0 sc1
Ta 20,4,8192
Mvaa 124,24,36
Ta 22,4,9216
Mvaa 92,24,32
Mvaa 92,28,48
Mvaa 60,28,44
Mvav 60,24,252
s_waitcnt vmcnt(10) lgkmcnt(8)
D 20,48,65,0
Ta 24,5,0
Mvaa 32,0,60
buffer_store_dwordx4 v[168:171], v1, s[8:11], s36 offen offset:1024 sc1
G 176,176
Mvaa 32,4,132
G 177,177
G 178,178
Mvaa 64,4,136
Ta 26,5,1024
G 179,179
Mvav 64,0,184
G 180,180
G 181,181
Mvav 96,0,192
Ta 28,5,8192
G 182,182
Mvaa 96,4,140
buffer_load_dwordx4 v[224:227], v27, s[8:11], s28 offen offset:0 sc1
Ta 30,5,9216
Mvaa 128,4,144
G 183,183
K 176,176,177
Mvaa 128,0,128
W 8
K 177,178,179
K 178,180,181
Mvaa 132,8,128
Ta 0,6,0
K 179,182,183
Mvaa 132,12,144
Ta 2,6,1024
Mvaa 100,12,140
buffer_load_dwordx4 v[188:191], v27, s[8:11], s93 offen offset:0 sc1
Ta 4,6,8192
Mvav 100,8,192
s_add_u32 s28, s28, 512
s_cmp_eq_u32 s28, s4
s_cselect_b32 s93, 10240, 0
s_sub_u32 s28, s28, s93
Mvav 68,8,184
s_add_u32 s93, s28, 20480
Ta 6,6,9216
Mvaa 68,12,136
Mvaa 36,12,132
Mvaa 36,8,60
W 8
buffer_load_dwordx4 v[196:199], v26, s[8:11], s28 offen offset:0 sc1
Ta 8,7,0
Mvaa 40,16,60
buffer_store_dwordx4 v[176:179], v1, s[8:11], s36 offen offset:2048 sc1
G 160,184
Mvaa 40,20,132
G 161,185
G 162,186
Mvaa 72,20,136
Ta 10,7,1024
G 163,187
Mvav 72,16,184
G 164,188
G 165,189
Mvav 104,16,192
Ta 12,7,8192
G 166,190
Mvaa 104,20,140
buffer_load_dwordx4 v[204:207], v26, s[8:11], s93 offen offset:0 sc1
Ta 14,7,9216
Mvaa 136,20,144
G 167,191
K 160,160,161
Mvaa 136,16,128
W 8
K 161,162,163
K 162,164,165
Mvaa 140,24,128
Ta 16,8,0
K 163,166,167
Mvaa 140,28,144
Ta 18,8,1024
Mvaa 108,28,140
D 21,48,65,1024
Ta 20,8,8192
Mvav 108,24,192
Ta 22,8,9216
Mvav 76,24,184
Mvaa 76,28,136
Mvaa 44,28,132
Mvaa 44,24,60
W 8
buffer_load_dwordx4 v[212:215], v27, s[8:11], s28 offen offset:0 sc1
Ta 24,9,0
Mvaa 48,0,60
buffer_store_dwordx4 v[160:163], v1, s[8:11], s36 offen offset:3072 sc1
G 168,192
Mvaa 48,4,132
G 169,193
G 170,194
Mvaa 80,4,136
G 171,195
G 172,196
Mvav 80,0,184
Ta 26,9,1024
G 173,197
Mvav 112,0,192
G 174,198
G 175,199
Mvaa 112,4,140
buffer_load_dwordx4 v[220:223], v27, s[8:11], s93 offen offset:0 sc1
Ta 28,9,8192
Mvaa 144,4,144
s_add_u32 s28, s28, 512
s_cmp_eq_u32 s28, s4
s_cselect_b32 s93, 10240, 0
s_sub_u32 s28, s28, s93
Mvaa 144,0,128
W 7
s_add_u32 s93, s28, 20480
Ta 30,9,9216
Mvaa 148,8,128
K 168,168,169
K 169,170,171
Mvaa 148,12,144
K 170,172,173
K 171,174,175
Mvaa 116,12,140
buffer_load_dwordx4 v[228:231], v26, s[8:11], s28 offen offset:0 sc1
Ta 0,2,16384
Mvav 116,8,192
Ta 2,2,17408
v_accvgpr_write_b32 a192, v168
Mvav 84,8,184
v_accvgpr_write_b32 a193, v169
v_accvgpr_write_b32 a194, v170
Mvaa 84,12,136
Ta 4,2,24576
Ta 6,2,25600
Mvaa 52,12,132
v_accvgpr_write_b32 a195, v171
G 176,64
Mvaa 52,8,60
W 8
D 22,48,65,2048
G 177,65
Mvaa 56,16,60
D 23,48,65,3072
G 178,66
Mvaa 56,20,132
Ta 8,3,16384
Ta 10,3,17408
Mvaa 88,20,136
G 179,67
G 180,68
Mvav 88,16,184
Ta 12,3,24576
Ta 14,3,25600
Mvav 120,16,192
G 181,69
G 182,70
Mvaa 120,20,140
buffer_load_dwordx4 v[236:239], v26, s[8:11], s93 offen offset:0 sc1
G 183,71
Mvaa 152,20,144
v_lshrrev_b32 v20, 4, v1
K 176,176,177
Mvaa 152,16,128
W 8
Ta 16,4,16384
v_add_u32 v20, 0, v20
Mvaa 156,24,128
Ta 18,4,17408
K 177,178,179
Mvaa 156,28,144
v_mul_u32_u24 v22, 205, v20
K 178,180,181
Mvaa 124,28,140
buffer_load_dwordx4 v[232:235], v27, s[8:11], s28 offen offset:0 sc1
K 179,182,183
Mvav 124,24,192
Ta 20,4,24576
Ta 22,4,25600
Mvav 92,24,184
v_lshrrev_b32 v22, 12, v22
v_accvgpr_write_b32 a196, v176
Mvaa 92,28,136
v_mul_u32_u24 v23, 20, v22
v_accvgpr_write_b32 a197, v177
Mvaa 60,28,132
v_accvgpr_write_b32 a198, v178
v_sub_u32 v20, v20, v23
Mvaa 60,24,60
s_waitcnt vmcnt(12) lgkmcnt(8)
buffer_load_dwordx4 a[32:35], v27, s[8:11], s93 offen offset:0 sc1
v_accvgpr_write_b32 a199, v179
Mvaa 32,0,148
s_add_u32 s28, s28, 512
s_cmp_eq_u32 s28, s4
s_cselect_b32 s93, 10240, 0
s_sub_u32 s28, s28, s93
Mvav 32,4,208
s_add_u32 s93, s28, 20480
Ta 24,5,16384
Ta 26,5,17408
Mvav 64,4,216
v_lshrrev_b32 v23, 1, v22
G 168,200
Mvaa 64,0,152
v_and_b32 v23, 2, v23
G 169,201
Mvaa 96,0,156
buffer_load_dwordx4 a[36:39], v26, s[8:11], s28 offen offset:0 sc1
Ta 28,5,24576
Mvav 96,4,224
Ta 30,5,25600
G 170,202
Mvav 128,4,188
v_xor_b32 v20, v20, v23
G 171,203
Mvav 128,0,200
W 8
v_mul_u32_u24 v22, 80, v22
G 172,204
Mvav 132,8,200
v_add_lshl_u32 v16, v20, v22, 4
G 173,205
Mvav 132,12,188
buffer_load_dwordx4 a[40:43], v26, s[8:11], s93 offen offset:0 sc1
Ta 0,6,16384
Mvav 100,12,224
Ta 2,6,17408
G 174,206
Mvaa 100,8,156
v_lshrrev_b32 v20, 4, v1
)"
R"(G 175,207
Mvaa 68,8,152
Ta 4,6,24576
v_add_u32 v20, 64, v20
Mvav 68,12,216
Ta 6,6,25600
K 168,168,169
Mvav 36,12,208
buffer_load_dwordx4 a[44:47], v27, s[8:11], s28 offen offset:0 sc1
K 169,170,171
Mvaa 36,8,148
W 8
v_mul_u32_u24 v22, 205, v20
K 170,172,173
Mvaa 40,16,148
v_lshrrev_b32 v22, 12, v22
K 171,174,175
Mvav 40,20,208
Ta 8,7,16384
v_mul_u32_u24 v23, 20, v22
Mvav 72,20,216
Ta 10,7,17408
v_accvgpr_write_b32 a200, v168
Mvaa 72,16,152
buffer_load_dwordx4 a[48:51], v27, s[8:11], s93 offen offset:0 sc1
v_accvgpr_write_b32 a201, v169
Mvaa 104,16,156
s_add_u32 s28, s28, 512
s_cmp_eq_u32 s28, s4
s_cselect_b32 s93, 10240, 0
s_sub_u32 s28, s28, s93
Mvav 104,20,224
s_add_u32 s93, s28, 20480
v_sub_u32 v20, v20, v23
v_accvgpr_write_b32 a202, v170
Mvav 136,20,188
Ta 12,7,24576
Ta 14,7,25600
Mvav 136,16,200
W 8
v_lshrrev_b32 v23, 1, v22
v_accvgpr_write_b32 a203, v171
Mvav 140,24,200
buffer_load_dwordx4 a[52:55], v26, s[8:11], s28 offen offset:0 sc1
v_and_b32 v23, 2, v23
Mvav 140,28,188
G 176,72
G 177,73
Mvav 108,28,224
v_xor_b32 v20, v20, v23
G 178,74
Mvaa 108,24,156
Ta 16,8,16384
Ta 18,8,17408
Mvaa 76,24,152
v_mul_u32_u24 v22, 80, v22
G 179,75
Mvav 76,28,216
buffer_load_dwordx4 a[56:59], v26, s[8:11], s93 offen offset:0 sc1
G 180,76
Mvav 44,28,208
Ta 20,8,24576
v_add_lshl_u32 v17, v20, v22, 4
Mvaa 44,24,148
W 7
Ta 22,8,25600
G 181,77
Mvaa 48,0,148
v_subrev_u32 v17, 1024, v17
G 182,78
Mvav 48,4,208
v_lshrrev_b32 v20, 4, v1
G 183,79
Mvav 80,4,216
buffer_load_dwordx4 a[184:187], v27, s[8:11], s28 offen offset:0 sc1
K 176,176,177
Mvaa 80,0,152
Ta 24,9,16384
v_add_u32 v20, 128, v20
Mvaa 112,0,156
Ta 26,9,17408
K 177,178,179
Mvav 112,4,224
v_mul_u32_u24 v22, 205, v20
K 178,180,181
Mvav 144,4,188
v_lshrrev_b32 v22, 12, v22
K 179,182,183
Mvav 144,0,200
W 6
buffer_load_dwordx4 a[188:191], v27, s[8:11], s93 offen offset:0 sc1
Ta 28,9,24576
Mvav 148,8,200
s_add_u32 s28, s28, 512
s_cmp_eq_u32 s28, s4
s_cselect_b32 s93, 10240, 0
s_sub_u32 s28, s28, s93
Mvav 148,12,188
s_add_u32 s93, s28, 20480
Ta 30,9,25600
v_accvgpr_write_b32 a204, v176
Mvav 116,12,224
v_mul_u32_u24 v23, 20, v22
v_accvgpr_write_b32 a205, v177
Mvaa 116,8,156
s_waitcnt vmcnt(15)
v_mov_b64 v[28:29], s[12:13]
s_mov_b64 exec, 1
buffer_store_dwordx2 v[28:29], off, s[8:11], s71 sc1
s_mov_b64 exec, -1
v_sub_u32 v20, v20, v23
v_accvgpr_write_b32 a206, v178
Mvaa 84,8,152
s_waitcnt vmcnt(12) lgkmcnt(0)
s_barrier
v_accvgpr_write_b32 a207, v179
Mvav 84,12,216
v_lshrrev_b32 v23, 1, v22
G 168,208
Mvav 52,12,208
v_and_b32 v23, 2, v23
G 169,209
Mvaa 52,8,148
v_xor_b32 v20, v20, v23
G 170,210
Mvaa 56,16,148
buffer_load_dwordx4 a[60:63], v26, s[8:11], s28 offen offset:0 sc1
G 171,211
Mvav 56,20,208
Ta 0,10,32768
Ta 2,10,33792
Mvav 88,20,216
Ta 4,10,40960
Ta 6,10,41984
Mvaa 88,16,152
v_mul_u32_u24 v22, 80, v22
G 172,212
Mvaa 120,16,156
v_add_lshl_u32 v18, v20, v22, 4
G 173,213
Mvav 120,20,224
buffer_load_dwordx4 v[184:187], v26, s[8:11], s93 offen offset:0 sc1
Ta 8,11,32768
Mvav 152,20,188
Ta 10,11,33792
G 174,214
Mvav 152,16,200
v_subrev_u32 v18, 2048, v18
G 175,215
Mvav 156,24,200
Ta 12,11,40960
v_lshrrev_b32 v20, 4, v1
Mvav 156,28,188
Ta 14,11,41984
K 168,168,169
Mvav 124,28,224
buffer_load_dwordx4 v[192:195], v27, s[8:11], s28 offen offset:0 sc1
v_add_u32 v20, 192, v20
Mvaa 124,24,156
K 169,170,171
K 170,172,173
Mvaa 92,24,152
v_mul_u32_u24 v22, 205, v20
K 171,174,175
Mvav 92,28,216
Ta 16,12,32768
v_lshrrev_b32 v22, 12, v22
Mvav 60,28,208
Ta 18,12,33792
v_accvgpr_write_b32 a208, v168
Mvaa 60,24,148
s_waitcnt vmcnt(12) lgkmcnt(6)
buffer_load_dwordx4 a[128:131], v27, s[8:11], s93 offen offset:0 sc1
v_mul_u32_u24 v23, 20, v22
Mvav 32,0,196
s_add_u32 s28, s28, 512
v_accvgpr_write_b32 a209, v169
v_accvgpr_write_b32 a210, v170
Mvav 32,4,228
s_cmp_eq_u32 s28, s4
s_cselect_b32 s93, 10240, 0
Ta 20,12,40960
v_sub_u32 v20, v20, v23
Mvav 64,4,236
s_sub_u32 s28, s28, s93
Ta 22,12,41984
v_accvgpr_write_b32 a211, v171
Mvav 64,0,204
s_add_u32 s93, s28, 20480
v_lshrrev_b32 v23, 1, v22
G 176,80
Mvav 96,0,212
G 177,81
v_and_b32 v23, 2, v23
Mvav 96,4,232
G 178,82
v_xor_b32 v20, v20, v23
Mvaa 128,4,32
Ta 24,13,32768
Ta 26,13,33792
Mvav 128,0,220
W 6
buffer_load_dwordx4 a[132:135], v26, s[8:11], s28 offen offset:0 sc1
G 179,83
Mvav 132,8,220
v_mul_u32_u24 v22, 80, v22
G 180,84
Mvaa 132,12,32
Ta 28,13,40960
Ta 30,13,41984
Mvav 100,12,232
G 181,85
v_add_lshl_u32 v19, v20, v22, 4
Mvav 100,8,212
G 182,86
v_subrev_u32 v19, 3072, v19
Mvav 68,8,204
G 183,87
v_lshrrev_b32 v20, 4, v1
Mvav 68,12,236
Ta 0,14,32768
K 176,176,177
Mvav 36,12,228
Ta 2,14,33792
K 177,178,179
Mvav 36,8,196
W 6
buffer_load_dwordx4 a[136:139], v26, s[8:11], s93 offen offset:0 sc1
v_add_u32 v20, 256, v20
Mvav 40,16,196
K 178,180,181
v_mul_u32_u24 v22, 205, v20
Mvav 40,20,228
Ta 4,14,40960
K 179,182,183
Mvav 72,20,236
Ta 6,14,41984
v_accvgpr_write_b32 a212, v176
Mvav 72,16,204
v_lshrrev_b32 v22, 12, v22
v_accvgpr_write_b32 a213, v177
Mvav 104,16,212
v_mul_u32_u24 v23, 20, v22
v_accvgpr_write_b32 a214, v178
Mvav 104,20,232
v_sub_u32 v20, v20, v23
v_accvgpr_write_b32 a215, v179
Mvaa 136,20,32
Ta 8,15,32768
Ta 10,15,33792
Mvav 136,16,220
W 6
buffer_load_dwordx4 a[140:143], v27, s[8:11], s28 offen offset:0 sc1
G 168,216
Mvav 140,24,220
v_lshrrev_b32 v23, 1, v22
G 169,217
Mvaa 140,28,32
Ta 12,15,40960
v_and_b32 v23, 2, v23
Mvav 108,28,232
Ta 14,15,41984
G 170,218
Mvav 108,24,212
v_xor_b32 v20, v20, v23
G 171,219
Mvav 76,24,204
G 172,220
v_mul_u32_u24 v22, 80, v22
Mvav 76,28,236
Ta 16,24,32768
G 173,221
Mvav 44,28,228
Ta 18,24,33792
v_add_lshl_u32 v20, v20, v22, 4
Mvav 44,24,196
W 6
buffer_load_dwordx4 a[144:147], v27, s[8:11], s93 offen offset:0 sc1
G 174,222
Mvav 48,0,196
s_add_u32 s28, s28, 512
G 175,223
v_lshrrev_b32 v22, 6, v1
Mvav 48,4,228
s_cmp_eq_u32 s28, s4
s_cselect_b32 s93, 10240, 0
Ta 20,24,40960
Ta 22,24,41984
Mvav 80,4,236
s_sub_u32 s28, s28, s93
K 168,168,169
v_mul_u32_u24 v22, 320, v22
Mvav 80,0,204
s_add_u32 s93, s28, 20480
K 169,170,171
v_bfe_u32 v23, v1, 4, 2
Mvav 112,0,212
K 170,172,173
K 171,174,175
Mvav 112,4,232
Ta 24,25,32768
v_lshl_add_u32 v22, v23, 3, v22
Mvaa 144,4,32
Ta 26,25,33792
v_accvgpr_write_b32 a216, v168
Mvav 144,0,220
W 6
buffer_load_dwordx4 v[200:203], v26, s[8:11], s28 offen offset:0 sc1
v_bfe_u32 v23, v1, 8, 1
Mvav 148,8,220
v_accvgpr_write_b32 a217, v169
v_lshlrev_b32 v23, 5, v23
Mvaa 148,12,32
Ta 28,25,40960
v_accvgpr_write_b32 a218, v170
Mvav 116,12,232
Ta 30,25,41984
v_accvgpr_write_b32 a219, v171
Mvav 116,8,212
v_add_u32 v21, v22, v23
G 176,88
Mvav 84,8,204
v_sub_u32 v22, v22, v23
G 177,89
Mvav 84,12,236
s_mul_i32 s94, s59, 655360
Ta 0,10,49152
G 178,90
Mvav 52,12,228
s_mul_i32 s95, s58, 320
Ta 2,10,50176
G 179,91
Mvav 52,8,196
W 6
buffer_load_dwordx4 v[208:211], v26, s[8:11], s93 offen offset:0 sc1
G 180,92
Mvav 56,16,196
s_add_u32 s94, s94, s95
Ta 4,10,57344
Ta 6,10,58368
Mvav 56,20,228
G 181,93
G 182,94
Mvav 88,20,236
s_add_u32 s40, s26, s94
s_addc_u32 s41, s27, 0
G 183,95
Mvav 88,16,204
s_and_b32 s41, s41, 0xffff
Ta 8,11,49152
K 176,176,177
Mvav 120,16,212
s_mov_b32 s42, 5242880
Ta 10,11,50176
K 177,178,179
Mvav 120,20,232
s_mov_b32 s43, 0x20000
buffer_load_dwordx4 v[216:219], v27, s[8:11], s28 offen offset:0 sc1
K 178,180,181
Mvaa 152,20,32
s_sub_u32 s42, s42, s94
Ta 12,11,57344
K 179,182,183
Mvav 152,16,220
W 7
Ta 14,11,58368
v_accvgpr_write_b32 a220, v176
Mvav 156,24,220
s_mov_b64 s[44:45], s[8:9]
v_accvgpr_write_b32 a221, v177
v_accvgpr_write_b32 a222, v178
Mvaa 156,28,32
s_mov_b32 s47, 0x20000
Ta 16,12,49152
v_accvgpr_write_b32 a223, v179
Mvav 124,28,232
s_and_b32 s48, s14, 1
Ta 18,12,50176
buffer_load_dwordx4 v[224:227], v27, s[8:11], s93 offen offset:0 sc1
Mvav 124,24,212
G 168,224
G 169,225
Mvav 92,24,204
s_mul_i32 s48, s48, 5120
Ta 20,12,57344
Ta 22,12,58368
Mvav 92,28,236
s_lshr_b32 s51, s14, 1
G 170,226
G 171,227
Mvav 60,28,228
G 172,228
Mvav 60,24,196
s_waitcnt vmcnt(13) lgkmcnt(8)
s_and_b32 s94, s14, 1
G 173,229
Mvaa 32,0,36
s_add_u32 s28, s28, 512
Ta 24,13,49152
Ta 26,13,50176
)"
R"(Mvaa 32,4,52
s_cmp_eq_u32 s28, s4
s_cselect_b32 s93, 10240, 0
s_sub_u32 s28, s28, s93
G 174,230
Mvaa 64,4,56
s_add_u32 s93, s28, 20480
s_lshl_b32 s95, s57, 1
buffer_load_dwordx4 v[188:191], v26, s[8:11], s28 offen offset:0 sc1
Mvaa 64,0,40
s_add_u32 s94, s94, s95
Ta 28,13,57344
G 175,231
Mvaa 96,0,44
s_lshl_b32 s54, s94, 3
Ta 30,13,58368
K 168,168,169
Mvaa 96,4,184
K 169,170,171
K 170,172,173
Mvaa 128,4,188
s_add_u32 s54, s54, s59
K 171,174,175
Mvaa 128,0,48
W 8
Ta 0,14,49152
Ta 2,14,50176
Mvaa 132,8,48
s_lshl_b32 s54, s54, 6
buffer_load_dwordx4 v[196:199], v26, s[8:11], s93 offen offset:0 sc1
v_accvgpr_write_b32 a224, v168
Mvaa 132,12,188
s_xor_b32 s55, s54, 15872
v_accvgpr_write_b32 a225, v169
Mvaa 100,12,184
Ta 4,14,57344
v_accvgpr_write_b32 a226, v170
Mvaa 100,8,44
s_add_u32 s54, s54, 20480
Ta 6,14,58368
v_accvgpr_write_b32 a227, v171
Mvaa 68,8,40
s_and_b32 s81, s14, 1
G 176,96
Mvaa 68,12,56
s_add_u32 s55, s55, 20480
Mvaa 36,12,52
G 177,97
Mvaa 36,8,36
W 8
s_and_b32 s95, s14, 2
G 178,98
Mvaa 40,16,36
s_mul_i32 s91, s81, 17164288
Ta 8,15,49152
Ta 10,15,50176
Mvaa 40,20,52
s_lshl_b32 s94, s94, 2
buffer_load_dwordx4 v[204:207], v27, s[8:11], s28 offen offset:0 sc1
G 179,99
Mvaa 72,20,56
s_mul_i32 s82, s56, 536576
s_add_u32 s94, s94, s95
G 180,100
Mvaa 72,16,40
Ta 12,15,57344
Ta 14,15,58368
Mvaa 104,16,44
s_lshl_b32 s95, s95, 4
G 181,101
G 182,102
Mvaa 104,20,184
s_add_u32 s91, s91, s82
G 183,103
Mvaa 136,20,188
s_add_u32 s55, s55, s95
s_add_u32 s91, s91, 4194304
K 176,176,177
Mvaa 136,16,48
W 8
s_lshl_b32 s49, s94, 17
Ta 16,24,49152
Mvaa 140,24,48
Ta 18,24,50176
K 177,178,179
Mvaa 140,28,188
s_lshl_b32 s95, s59, 14
buffer_load_dwordx4 v[212:215], v27, s[8:11], s93 offen offset:0 sc1
K 178,180,181
Mvaa 108,28,184
s_lshr_b32 s82, s14, 1
Ta 20,24,57344
K 179,182,183
Mvaa 108,24,44
s_add_u32 s49, s49, s95
Ta 22,24,58368
v_accvgpr_write_b32 a228, v176
Mvaa 76,24,40
s_lshl_b32 s83, s82, 14
s_xor_b32 s50, s49, 16252928
Mvaa 76,28,56
v_accvgpr_write_b32 a229, v177
Mvaa 44,28,52
s_add_u32 s91, s91, s83
s_add_u32 s49, s49, 39845888
v_accvgpr_write_b32 a230, v178
Mvaa 44,24,36
W 8
Ta 24,25,49152
v_accvgpr_write_b32 a231, v179
Mvaa 48,0,36
s_add_u32 s50, s50, 39845888
Ta 26,25,50176
G 168,232
Mvaa 48,4,52
s_lshl_b32 s81, s81, 14
Mvaa 80,4,56
s_lshr_b32 s94, s15, 3
Ta 28,25,57344
G 169,233
Mvaa 80,0,40
s_lshl_b32 s82, s82, 16
Ta 30,25,58368
G 170,234
Mvaa 112,0,44
s_lshl_b32 s94, s94, 3
G 171,235
Mvaa 112,4,184
s_add_u32 s94, s94, s14
s_or_b32 s92, s81, s82
G 172,236
Mvaa 144,4,188
Mvaa 144,0,48
W 8
s_lshl_b32 s94, s94, 4
G 173,237
Mvaa 148,8,48
s_xor_b32 s92, s92, 0x10000
Ta 0,2,32768
Ta 2,2,33792
Mvaa 148,12,188
s_add_u32 s94, s94, s59
G 174,238
G 175,239
Mvaa 116,12,184
s_mul_i32 s52, s94, 5120
Ta 4,2,40960
Ta 6,2,41984
Mvaa 116,8,44
K 168,168,169
Mvaa 84,8,40
s_lshl_b32 s95, s59, 12
K 169,170,171
Mvaa 84,12,56
K 170,172,173
Mvaa 52,12,52
s_sub_u32 s52, s52, s95
K 171,174,175
Mvaa 52,8,36
W 8
s_add_u32 s52, s52, 56623104
Ta 8,3,32768
Mvaa 56,16,36
Ta 10,3,33792
v_accvgpr_write_b32 a232, v168
Mvaa 56,20,52
s_lshl_b32 s53, s94, 3
v_accvgpr_write_b32 a233, v169
Mvaa 88,20,56
Ta 12,3,40960
v_accvgpr_write_b32 a234, v170
Mvaa 88,16,40
s_add_u32 s53, s53, 65536
Ta 14,3,41984
v_accvgpr_write_b32 a235, v171
Mvaa 120,16,44
G 176,104
v_bfe_u32 v244, v1, 4, 1
Mvaa 120,20,184
Mvaa 152,20,188
G 177,105
v_mul_u32_u24 v244, 0xaaaa, v244
Mvaa 152,16,48
W 8
G 178,106
v_add_u32 v244, 0x4444, v244
Mvaa 156,24,48
Ta 16,4,32768
Ta 18,4,33792
Mvaa 156,28,188
G 179,107
v_lshl_or_b32 v240, v244, 16, v244
Mvaa 124,28,184
G 180,108
v_mov_b32 v241, 0xff800000
Mvaa 124,24,44
Ta 20,4,40960
Ta 22,4,41984
Mvaa 92,24,40
G 181,109
v_add_u32 v242, 0x10000, v1
Mvaa 92,28,56
G 182,110
v_bfe_u32 v244, v1, 4, 4
Mvaa 60,28,52
G 183,111
v_lshrrev_b32 v245, 8, v1
Mvaa 60,24,36
s_waitcnt vmcnt(8) lgkmcnt(8)
K 176,176,177
v_lshlrev_b32 v245, 2, v245
Mvaa 32,0,60
Ta 24,5,32768
Ta 26,5,33792
Mvaa 32,4,132
K 177,178,179
v_sub_u32 v244, v244, v245
Mvaa 64,4,136
K 178,180,181
v_cmp_le_i32_e64 s[72:73], 0, v244
Mvav 64,0,184
Ta 28,5,40960
K 179,182,183
Mvav 96,0,192
Ta 30,5,41984
v_accvgpr_write_b32 a236, v176
Mvaa 96,4,140
v_cmp_le_i32_e64 s[74:75], 1, v244
v_accvgpr_write_b32 a237, v177
Mvaa 128,4,144
v_cmp_le_i32_e64 s[76:77], 2, v244
v_accvgpr_write_b32 a238, v178
Mvaa 128,0,128
W 8
v_cmp_le_i32_e64 s[78:79], 3, v244
v_accvgpr_write_b32 a239, v179
Mvaa 132,8,128
Ta 0,6,32768
Ta 2,6,33792
Mvaa 132,12,144
v_add_u32 v243, 4096, v1
G 168,240
Mvaa 100,12,140
v_add_u32 v244, 8192, v1
G 169,241
Mvav 100,8,192
Ta 4,6,40960
v_add_u32 v245, 12288, v1
Mvav 68,8,184
s_mov_b32 s18, 0x3f803f80
Ta 6,6,41984
G 170,242
Mvaa 68,12,136
s_mov_b32 s19, 0x3f003f00
G 171,243
G 172,244
Mvaa 36,12,132
Mvaa 36,8,60
W 8
v_accvgpr_write_b32 a176, s18
G 173,245
Mvaa 40,16,60
Ta 8,7,32768
Ta 10,7,33792
Mvaa 40,20,132
v_accvgpr_write_b32 a180, s19
G 174,246
Mvaa 72,20,136
G 175,247
v_mov_b32 v252, v241
Mvav 72,16,184
Ta 12,7,40960
Ta 14,7,41984
Mvav 104,16,192
K 168,168,169
v_accvgpr_write_b32 a177, s18
Mvaa 104,20,140
K 169,170,171
v_accvgpr_write_b32 a181, s19
Mvaa 136,20,144
K 170,172,173
v_mov_b32 v253, v241
Mvaa 136,16,128
W 8
K 171,174,175
v_accvgpr_write_b32 a178, s18
Mvaa 140,24,128
Ta 16,8,32768
Ta 18,8,33792
Mvaa 140,28,144
v_accvgpr_write_b32 a240, v168
v_accvgpr_write_b32 a182, s19
Mvaa 108,28,140
v_accvgpr_write_b32 a241, v169
v_mov_b32 v254, v241
Mvav 108,24,192
Ta 20,8,40960
v_accvgpr_write_b32 a242, v170
Mvav 76,24,184
Ta 22,8,41984
v_accvgpr_write_b32 a179, s18
Mvaa 76,28,136
v_accvgpr_write_b32 a243, v171
v_accvgpr_write_b32 a183, s19
Mvaa 44,28,132
G 176,112
v_mov_b32 v255, v241
Mvaa 44,24,60
W 8
G 177,113
G 178,114
Mvaa 48,0,60
Ta 24,9,32768
v_accvgpr_write_b32 a160, 0
Mvaa 48,4,132
Ta 26,9,33792
G 179,115
Mvaa 80,4,136
v_accvgpr_write_b32 a161, 0
G 180,116
Mvav 80,0,184
Ta 28,9,40960
v_accvgpr_write_b32 a162, 0
Mvav 112,0,192
Ta 30,9,41984
G 181,117
Mvaa 112,4,140
v_accvgpr_write_b32 a163, 0
G 182,118
Mvaa 144,4,144
v_accvgpr_write_b32 a164, 0
G 183,119
Mvaa 144,0,128
W 8
v_accvgpr_write_b32 a165, 0
K 176,176,177
Mvaa 148,8,128
Ta 0,2,49152
v_accvgpr_write_b32 a166, 0
Mvaa 148,12,144
Ta 2,2,50176
K 177,178,179
Mvaa 116,12,140
v_accvgpr_write_b32 a167, 0
K 178,180,181
Mvav 116,8,192
v_accvgpr_write_b32 a168, 0
K 179,182,183
Mvav 84,8,184
Ta 4,2,57344
Ta 6,2,58368
Mvaa 84,12,136
v_accvgpr_write_b32 a169, 0
v_accvgpr_write_b32 a244, v176
Mvaa 52,12,132
v_accvgpr_write_b32 a245, v177
v_accvgpr_write_b32 a170, 0
Mvaa 52,8,60
W 8
v_accvgpr_write_b32 a246, v178
v_accvgpr_write_b32 a171, 0
Mvaa 56,16,60
Ta 8,3,49152
v_accvgpr_write_b32 a247, v179
Mvaa 56,20,132
Ta 10,3,50176
v_accvgpr_write_b32 a172, 0
Mvaa 88,20,136
G 168,248
v_accvgpr_write_b32 a173, 0
Mvav 88,16,184
G 169,249
v_accvgpr_write_b32 a174, 0
Mvav 120,16,192
Ta 12,3,57344
Ta 14,3,58368
Mvaa 120,20,140
G 170,250
v_accvgpr_write_b32 a175, 0
Mvaa 152,20,144
s_mov_b32 s90, 0x5d800000
G 171,251
G 172,252
Mvaa 152,16,128
W 8
s_mov_b64 s[88:89], 0
G 173,253
Mvaa 156,24,128
s_mov_b32 s64, 0
Ta 16,4,49152
Ta 18,4,50176
Mvaa 156,28,144
s_and_b32 s18, s14, 1
G 174,254
G 175,255
Mvaa 124,28,140
s_lshl_b32 s62, s18, 14
Ta 20,4,57344
Ta 22,4,58368
Mvav 124,24,192
)"
R"(K 168,168,169
Mvav 92,24,184
s_lshl_b32 s20, s57, 15
K 169,170,171
Mvaa 92,28,136
s_sub_u32 s20, 507904, s20
K 170,172,173
Mvaa 60,28,132
K 171,174,175
Mvaa 60,24,60
s_waitcnt vmcnt(0) lgkmcnt(8)
s_add_u32 s20, s20, s62
Ta 24,5,49152
Mvav 32,0,200
s_lshl_b32 s65, s57, 1
Ta 26,5,50176
v_accvgpr_write_b32 a248, v168
Mvav 32,4,188
s_sub_u32 s65, s65, 1
v_accvgpr_write_b32 a249, v169
Mvav 64,4,196
Ta 28,5,57344
v_accvgpr_write_b32 a250, v170
Mvav 64,0,208
s_max_i32 s65, s65, 0
Ta 30,5,58368
v_accvgpr_write_b32 a251, v171
Mvav 96,0,216
s_sub_u32 s66, s60, 1
G 176,120
Mvav 96,4,204
s_cmp_lt_u32 s14, 2
s_cselect_b32 s20, s20, 0
s_cselect_b32 s66, s66, 15
s_cselect_b32 s21, 32768, 0
Mvav 128,4,212
G 177,121
Mvav 128,0,224
W 8
s_add_u32 s61, s91, s20
G 178,122
Mvav 132,8,224
s_add_u32 s62, s62, s21
Ta 0,6,49152
Ta 2,6,50176
Mvav 132,12,212
G 179,123
G 180,124
Mvav 100,12,204
s_sub_u32 s63, 16384, s21
Ta 4,6,57344
Ta 6,6,58368
Mvav 100,8,216
s_lshl_b32 s67, s60, 7
G 181,125
Mvav 68,8,208
s_add_u32 s67, s67, s56
G 182,126
Mvav 68,12,196
G 183,127
Mvav 36,12,188
s_lshl_b32 s67, s67, 12
K 176,176,177
Mvav 36,8,200
W 8
s_add_u32 s67, s67, 39845888
Ta 8,7,49152
Mvav 40,16,200
s_lshl_b32 s69, s60, 3
Ta 10,7,50176
K 177,178,179
Mvav 40,20,188
K 178,180,181
Mvav 72,20,196
s_add_u32 s69, s69, s59
Ta 12,7,57344
K 179,182,183
Mvav 72,16,208
s_lshl_b32 s69, s69, 6
Ta 14,7,58368
v_accvgpr_write_b32 a252, v176
Mvav 104,16,216
Mvav 104,20,204
s_lshl_b32 s21, s58, 3
v_accvgpr_write_b32 a253, v177
Mvav 136,20,212
s_add_u32 s69, s69, s21
v_accvgpr_write_b32 a254, v178
Mvav 136,16,224
W 8
s_add_u32 s69, s69, 20480
v_accvgpr_write_b32 a255, v179
Mvav 140,24,224
Ta 16,8,49152
Ta 18,8,50176
Mvav 140,28,212
s_mov_b32 s70, 0
Mvav 108,28,204
s_mov_b32 s37, 0
Ta 20,8,57344
Ta 22,8,58368
Mvav 108,24,216
s_sub_u32 s18, s57, 4
Mvav 76,24,208
Mvav 76,28,196
s_lshl_b32 s18, s18, 2
Mvav 44,28,188
s_add_u32 s18, s18, s58
Mvav 44,24,200
W 8
Ta 24,9,49152
Ta 26,9,50176
Mvav 48,0,200
s_mul_i32 s85, s59, 7
Mvav 48,4,188
s_add_u32 s85, s85, s18
Mvav 80,4,196
s_cmp_lt_u32 s18, 7
s_cselect_b32 s85, s85, 99
Ta 28,9,57344
Ta 30,9,58368
Mvav 80,0,208
Mvav 112,0,216
s_cmp_lt_u32 s85, 54
s_cselect_b32 s65, 2, s65
Mvav 112,4,204
s_mov_b32 s68, 0
Mvav 144,4,212
s_mov_b64 s[24:25], -1
Mvav 144,0,224
W 8
Mvav 148,8,224
s_mov_b32 s22, 0x3e0293ee
Mvav 148,12,212
s_mov_b32 s23, 0x3e0293ee
Mvav 116,12,204
Mvav 116,8,216
Mvav 84,8,208
Mvav 84,12,196
Mvav 52,12,188
Mvav 52,8,200
W 4
Mvav 56,16,200
Mvav 56,20,188
Mvav 88,20,196
Mvav 88,16,208
Mvav 120,16,216
W 0
s_barrier
Ra 128,242,0
Mvav 120,20,204
Mvav 152,20,212
Ra 132,242,1024
Mvav 152,16,224
Mvav 156,24,224
Ra 136,242,2048
Mvav 156,28,212
Mvav 124,28,204
Ra 140,242,3072
Mvav 124,24,216
Mvav 92,24,208
Mvav 92,28,196
Mvav 60,28,188
Mvav 60,24,200
.Lago%=:
s_cmp_eq_u32 s57, 0
s_cbranch_scc0 .LL_48%=
s_add_u32 m0, s62, 0
s_nop 0
Dc 1,8,61,0
Dc 1,8,61,1024
Dc 1,8,61,2048
Dc 1,8,61,3072
s_add_u32 m0, s62, 4096
s_nop 0
Dc 243,8,61,0
Dc 243,8,61,1024
Dc 243,8,61,2048
Dc 243,8,61,3072
s_add_u32 m0, s62, 8192
s_nop 0
Dc 244,8,61,0
Dc 244,8,61,1024
Dc 244,8,61,2048
Dc 244,8,61,3072
s_add_u32 m0, s62, 12288
s_nop 0
Dc 245,8,61,0
Dc 245,8,61,1024
Dc 245,8,61,2048
Dc 245,8,61,3072
s_add_u32 s61, s61, s63
.LL_48%=:
s_cmp_eq_u32 s60, 0
s_cbranch_scc1 .Lpd_32%=
v_pk_mul_f32 v[32:33], v[32:33], s[22:23]
v_pk_mul_f32 v[34:35], v[34:35], s[22:23]
v_pk_mul_f32 v[36:37], v[36:37], s[22:23]
v_pk_mul_f32 v[38:39], v[38:39], s[22:23]
v_pk_mul_f32 v[64:65], v[64:65], s[22:23]
v_pk_mul_f32 v[66:67], v[66:67], s[22:23]
v_pk_mul_f32 v[68:69], v[68:69], s[22:23]
v_pk_mul_f32 v[70:71], v[70:71], s[22:23]
v_pk_mul_f32 v[96:97], v[96:97], s[22:23]
v_pk_mul_f32 v[98:99], v[98:99], s[22:23]
v_pk_mul_f32 v[100:101], v[100:101], s[22:23]
v_pk_mul_f32 v[102:103], v[102:103], s[22:23]
v_pk_mul_f32 v[128:129], v[128:129], s[22:23]
v_pk_mul_f32 v[130:131], v[130:131], s[22:23]
v_pk_mul_f32 v[132:133], v[132:133], s[22:23]
v_pk_mul_f32 v[134:135], v[134:135], s[22:23]
v_pk_mul_f32 v[40:41], v[40:41], s[22:23]
v_pk_mul_f32 v[42:43], v[42:43], s[22:23]
v_pk_mul_f32 v[44:45], v[44:45], s[22:23]
v_pk_mul_f32 v[46:47], v[46:47], s[22:23]
v_pk_mul_f32 v[72:73], v[72:73], s[22:23]
v_pk_mul_f32 v[74:75], v[74:75], s[22:23]
v_pk_mul_f32 v[76:77], v[76:77], s[22:23]
v_pk_mul_f32 v[78:79], v[78:79], s[22:23]
v_pk_mul_f32 v[104:105], v[104:105], s[22:23]
v_pk_mul_f32 v[106:107], v[106:107], s[22:23]
v_pk_mul_f32 v[108:109], v[108:109], s[22:23]
v_pk_mul_f32 v[110:111], v[110:111], s[22:23]
v_pk_mul_f32 v[136:137], v[136:137], s[22:23]
v_pk_mul_f32 v[138:139], v[138:139], s[22:23]
v_pk_mul_f32 v[140:141], v[140:141], s[22:23]
v_pk_mul_f32 v[142:143], v[142:143], s[22:23]
v_pk_mul_f32 v[48:49], v[48:49], s[22:23]
v_pk_mul_f32 v[50:51], v[50:51], s[22:23]
v_pk_mul_f32 v[52:53], v[52:53], s[22:23]
v_pk_mul_f32 v[54:55], v[54:55], s[22:23]
v_pk_mul_f32 v[80:81], v[80:81], s[22:23]
v_pk_mul_f32 v[82:83], v[82:83], s[22:23]
v_pk_mul_f32 v[84:85], v[84:85], s[22:23]
v_pk_mul_f32 v[86:87], v[86:87], s[22:23]
v_pk_mul_f32 v[112:113], v[112:113], s[22:23]
v_pk_mul_f32 v[114:115], v[114:115], s[22:23]
v_pk_mul_f32 v[116:117], v[116:117], s[22:23]
v_pk_mul_f32 v[118:119], v[118:119], s[22:23]
v_pk_mul_f32 v[144:145], v[144:145], s[22:23]
v_pk_mul_f32 v[146:147], v[146:147], s[22:23]
v_pk_mul_f32 v[148:149], v[148:149], s[22:23]
v_pk_mul_f32 v[150:151], v[150:151], s[22:23]
v_pk_mul_f32 v[56:57], v[56:57], s[22:23]
v_pk_mul_f32 v[58:59], v[58:59], s[22:23]
v_pk_mul_f32 v[60:61], v[60:61], s[22:23]
v_pk_mul_f32 v[62:63], v[62:63], s[22:23]
v_pk_mul_f32 v[88:89], v[88:89], s[22:23]
v_pk_mul_f32 v[90:91], v[90:91], s[22:23]
v_pk_mul_f32 v[92:93], v[92:93], s[22:23]
v_pk_mul_f32 v[94:95], v[94:95], s[22:23]
v_pk_mul_f32 v[120:121], v[120:121], s[22:23]
v_pk_mul_f32 v[122:123], v[122:123], s[22:23]
v_pk_mul_f32 v[124:125], v[124:125], s[22:23]
v_pk_mul_f32 v[126:127], v[126:127], s[22:23]
v_pk_mul_f32 v[152:153], v[152:153], s[22:23]
v_pk_mul_f32 v[154:155], v[154:155], s[22:23]
v_pk_mul_f32 v[156:157], v[156:157], s[22:23]
v_pk_mul_f32 v[158:159], v[158:159], s[22:23]
K 32,32,33
K 33,34,35
K 34,36,37
K 35,38,39
K 40,40,41
K 41,42,43
K 42,44,45
K 43,46,47
W 0
Zvav 196,128,32
s_add_u32 s36, s29, 65536
Ra 144,242,4096
Ra 148,242,5120
Ra 152,242,6144
Ra 156,242,7168
Mvav 196,132,40
K 64,64,65
K 65,66,67
K 66,68,69
K 67,70,71
K 72,72,73
K 73,74,75
K 74,76,77
K 75,78,79
s_nop 1
Zvav 204,132,72
buffer_store_dwordx4 a[192:195], v1, s[8:11], s36 offen offset:0 sc1
K 96,96,97
K 97,98,99
K 98,100,101
Mvav 204,128,64
K 99,102,103
K 104,104,105
K 105,106,107
K 106,108,109
K 107,110,111
Zvav 212,128,96
buffer_store_dwordx4 a[200:203], v1, s[8:11], s36 offen offset:1024 sc1
K 128,128,129
K 129,130,131
K 130,132,133
Mvav 212,132,104
K 131,134,135
K 136,136,137
K 137,138,139
K 138,140,141
K 139,142,143
s_nop 1
Zvav 220,132,136
K 48,48,49
K 49,50,51
K 50,52,53
K 51,54,55
Mvav 220,128,128
K 56,56,57
K 57,58,59
K 58,60,61
K 59,62,63
Mvav 196,136,48
buffer_store_dwordx4 a[208:211], v1, s[8:11], s36 offen offset:2048 sc1
Ra 128,242,8192
Ra 132,242,9216
K 80,80,81
Mvav 196,140,56
K 81,82,83
K 82,84,85
K 83,86,87
K 88,88,89
K 89,90,91
K 90,92,93
K 91,94,95
s_nop 1
Mvav 204,140,88
buffer_store_dwordx4 a[216:219], v1, s[8:11], s36 offen offset:3072 sc1
s_add_u32 s36, s29, 69632
K 112,112,113
K 113,114,115
K 114,116,117
Mvav 204,136,80
K 115,118,119
K 120,120,121
K 121,122,123
K 122,124,125
K 123,126,127
Mvav 212,136,112
K 144,144,145
K 145,146,147
K 146,148,149
K 147,150,151
Mvav 212,140,120
K 152,152,153
K 153,154,155
K 154,156,157
K 155,158,159
s_nop 1
Mvav 220,140,152
buffer_store_dwordx4 a[224:227], v1, s[8:11], s36 offen offset:0 sc1
v_accvgpr_write_b32 a0, v32
v_accvgpr_write_b32 a1, v33
v_accvgpr_write_b32 a2, v34
Mvav 220,136,144
W 2
Ra 136,242,10240
v_accvgpr_write_b32 a3, v35
v_accvgpr_write_b32 a4, v40
Zvav 36,144,32
Ra 140,242,11264
buffer_store_dwordx4 a[232:235], v1, s[8:11], s36 offen offset:1024 sc1
v_accvgpr_write_b32 a5, v41
Mvav 36,148,40
v_accvgpr_write_b32 a6, v42
v_accvgpr_write_b32 a7, v43
v_max3_f32 v4, v196, v197, v198
v_max3_f32 v4, v4, v199, v252
Zvav 68,148,72
v_max3_f32 v5, v204, v205, v206
v_max3_f32 v5, v5, v207, v253
v_max3_f32 v6, v212, v213, v214
v_max3_f32 v6, v6, v215, v254
Mvav 68,144,64
v_max3_f32 v7, v220, v221, v222
v_max3_f32 v7, v7, v223, v255
v_mov_b32 v8, v4
v_mov_b32 v9, v5
Zvav 100,144,96
v_mov_b32 v10, v6
v_mov_b32 v11, v7
v_permlane32_swap_b32 v4, v8
v_accvgpr_write_b32 a8, v48
Mvav 100,148,104
v_permlane32_swap_b32 v5, v9
v_permlane32_swap_b32 v6, v10
v_permlane32_swap_b32 v7, v11
v_max_f32 v4, v4, v8
Zvav 132,148,136
v_max_f32 v5, v5, v9
v_max_f32 v6, v6, v10
v_max_f32 v7, v7, v11
v_mov_b32 v8, v4
Mvav 132,144,128
v_mov_b32 v9, v5
)"
R"(v_mov_b32 v10, v6
v_mov_b32 v11, v7
v_accvgpr_write_b32 a9, v49
Mvav 36,152,48
v_permlane16_swap_b32 v4, v8
v_permlane16_swap_b32 v5, v9
v_permlane16_swap_b32 v6, v10
v_permlane16_swap_b32 v7, v11
Mvav 36,156,56
v_max_f32 v4, v4, v8
v_max_f32 v5, v5, v9
v_max_f32 v6, v6, v10
v_max_f32 v7, v7, v11
Mvav 68,156,88
v_sub_f32 v4, 0, v4
v_sub_f32 v5, 0, v5
v_sub_f32 v6, 0, v6
v_sub_f32 v7, 0, v7
Mvav 68,152,80
v_mov_b32 v224, v4
v_mov_b32 v225, v4
v_mov_b32 v226, v4
v_mov_b32 v227, v4
Mvav 100,152,112
v_mov_b32 v228, v5
v_mov_b32 v229, v5
v_mov_b32 v230, v5
v_mov_b32 v231, v5
Mvav 100,156,120
v_mov_b32 v232, v6
v_mov_b32 v233, v6
v_mov_b32 v234, v6
v_mov_b32 v235, v6
Mvav 132,156,152
v_mov_b32 v236, v7
v_mov_b32 v237, v7
v_mov_b32 v238, v7
v_mov_b32 v239, v7
Mvav 132,152,144
W 0
buffer_store_dwordx4 a[240:243], v1, s[8:11], s36 offen offset:2048 sc1
v_accvgpr_write_b32 a10, v50
v_accvgpr_write_b32 a11, v51
v_accvgpr_write_b32 a12, v56
Cvav 160,128,32,224
s_and_b32 s80, s14, 1
v_accvgpr_write_b32 a13, v57
v_accvgpr_write_b32 a14, v58
v_accvgpr_write_b32 a15, v59
v_accvgpr_write_b32 a16, v64
Mvav 160,132,40
buffer_store_dwordx4 a[248:251], v1, s[8:11], s36 offen offset:3072 sc1
s_add_u32 s36, s29, 393216
Ra 144,242,12288
Ra 148,242,13312
v_accvgpr_write_b32 a17, v65
Cvav 164,132,72,228
s_lshl_b32 s80, s80, 14
v_accvgpr_write_b32 a18, v66
v_accvgpr_write_b32 a19, v67
v_accvgpr_write_b32 a20, v72
v_accvgpr_write_b32 a21, v73
Mvav 164,128,64
s_add_u32 s80, s80, 98304
buffer_store_dwordx4 a[196:199], v1, s[8:11], s36 offen offset:0 sc1
v_accvgpr_write_b32 a22, v74
v_accvgpr_write_b32 a23, v75
v_accvgpr_write_b32 a24, v80
Cvav 168,128,96,232
v_accvgpr_write_b32 a25, v81
v_accvgpr_write_b32 a26, v82
A 196,196,224
v_accvgpr_write_b32 a27, v83
Mvav 168,132,104
buffer_store_dwordx4 a[204:207], v1, s[8:11], s36 offen offset:1024 sc1
A 197,197,224
A 198,198,224
v_accvgpr_write_b32 a28, v88
Cvav 172,132,136,236
Ra 152,242,14336
Ra 156,242,15360
A 199,199,224
v_accvgpr_write_b32 a29, v89
Mvav 172,128,128
buffer_store_dwordx4 a[212:215], v1, s[8:11], s36 offen offset:2048 sc1
X 196
v_accvgpr_write_b32 a30, v90
Mvav 160,136,48
X 197
v_accvgpr_write_b32 a31, v91
v_accvgpr_write_b32 a32, v96
Mvav 160,140,56
X 198
K 192,196,197
v_accvgpr_write_b32 a33, v97
Mvav 164,140,88
X 199
A 204,204,228
v_accvgpr_write_b32 a34, v98
Mvav 164,136,80
buffer_store_dwordx4 a[220:223], v1, s[8:11], s36 offen offset:3072 sc1
s_add_u32 s36, s29, 397312
K 193,198,199
v_accvgpr_write_b32 a35, v99
A 205,205,228
Mvav 168,136,112
A 206,206,228
v_accvgpr_write_b32 a36, v104
v_add_u32 v8, s80, v1
A 207,207,228
Mvav 168,140,120
X 204
v_accvgpr_write_b32 a37, v105
v_accvgpr_write_b32 a38, v106
Mvav 172,140,152
X 205
X 206
Mvav 172,136,144
W 0
buffer_store_dwordx4 a[228:231], v1, s[8:11], s36 offen offset:0 sc1
v_accvgpr_write_b32 a39, v107
v_accvgpr_write_b32 a40, v112
K 200,204,205
Cvav 176,144,32,224
Ra 128,242,24576
X 207
v_accvgpr_write_b32 a41, v113
Mvav 176,148,40
Ra 132,242,25600
A 212,212,232
v_accvgpr_write_b32 a42, v114
K 201,206,207
Cvav 180,148,72,228
A 213,213,232
v_accvgpr_write_b32 a43, v115
A 214,214,232
A 215,215,232
Mvav 180,144,64
buffer_store_dwordx4 a[236:239], v1, s[8:11], s36 offen offset:1024 sc1
v_accvgpr_write_b32 a44, v120
X 212
Cvav 184,144,96,232
v_accvgpr_write_b32 a45, v121
X 213
v_accvgpr_write_b32 a46, v122
Mvav 184,148,104
X 214
v_accvgpr_write_b32 a47, v123
K 208,212,213
Cvav 188,148,136,236
buffer_store_dwordx4 a[244:247], v1, s[8:11], s36 offen offset:2048 sc1
v_accvgpr_write_b32 a48, v128
X 215
Mvav 188,144,128
v_accvgpr_write_b32 a49, v129
A 220,220,236
v_accvgpr_write_b32 a50, v130
K 209,214,215
Mvav 176,152,48
A 221,221,236
v_accvgpr_write_b32 a51, v131
A 222,222,236
A 223,223,236
Mvav 176,156,56
Ra 136,242,26624
v_accvgpr_write_b32 a52, v136
X 220
Mvav 180,156,88
Ra 140,242,27648
v_accvgpr_write_b32 a53, v137
X 221
Mvav 180,152,80
buffer_store_dwordx4 a[252:255], v1, s[8:11], s36 offen offset:3072 sc1
v_accvgpr_write_b32 a54, v138
X 222
Mvav 184,152,112
v_accvgpr_write_b32 a55, v139
v_accvgpr_write_b32 a56, v144
v_accvgpr_write_b32 a57, v145
v_accvgpr_write_b32 a58, v146
v_accvgpr_write_b32 a59, v147
v_accvgpr_write_b32 a60, v152
v_accvgpr_write_b32 a61, v153
v_accvgpr_write_b32 a62, v154
v_accvgpr_write_b32 a63, v155
Mvav 184,156,120
s_barrier
K 216,220,221
X 223
A 36,36,224
Mvav 188,156,152
K 217,222,223
A 37,37,224
A 38,38,224
A 39,39,224
X 36
X 37
X 38
K 194,36,37
X 39
A 68,68,228
K 195,38,39
A 69,69,228
A 70,70,228
A 71,71,228
X 68
X 69
X 70
K 202,68,69
X 71
A 100,100,232
K 203,70,71
A 101,101,232
A 102,102,232
A 103,103,232
X 100
X 101
X 102
K 210,100,101
X 103
A 132,132,236
K 211,102,103
A 133,133,236
A 134,134,236
A 135,135,236
X 132
X 133
X 134
K 218,132,133
X 135
s_nop 0
K 219,134,135
Mvav 188,152,144
W 0
Ra 144,242,28672
Ra 148,242,29696
Ra 152,242,30720
Zvav 32,128,192
Ra 156,242,31744
Zvav 64,128,200
Zvav 96,128,208
s_and_b32 s80, s14, 1
s_xor_b32 s62, s62, 0x10000
Zvav 128,128,216
s_lshl_b32 s80, s80, 14
s_add_u32 s80, s80, 131072
v_add_u32 v23, 61440, v21
Zvav 132,132,216
v_add_u32 v12, s80, v1
Ra 64,12,8192
Ra 68,12,9216
v_add_u32 v24, 61440, v22
Zvav 100,132,208
Ra 72,12,10240
Ra 76,12,11264
Ra 80,12,12288
v_add_u32 v25, 122880, v21
Zvav 68,132,200
Ra 84,12,13312
Ra 88,12,14336
Ra 92,12,15360
v_add_u32 v26, 122880, v22
Zvav 36,132,192
Ra 96,12,0
Ra 100,12,1024
Ra 104,12,2048
Zvav 40,136,192
Ra 108,12,3072
Ra 112,12,4096
X 172
Zvav 72,136,200
Ra 116,12,5120
Ra 120,12,6144
X 173
Zvav 104,136,208
Ra 124,12,7168
X 174
K 220,172,173
Zvav 136,136,216
X 175
Zvav 140,140,216
X 168
K 221,174,175
Zvav 108,140,208
X 169
X 170
Zvav 76,140,200
s_waitcnt vmcnt(0) lgkmcnt(0)
s_barrier
v_xor_b32 v242, 0x10000, v242
ds_read_b128 v[248:251], v8
Zvav 44,140,192
s_add_u32 s80, s71, 128
v_mov_b64 v[28:29], s[12:13]
s_mov_b64 exec, 1
buffer_store_dwordx2 v[28:29], off, s[8:11], s80 sc1
s_mov_b64 exec, -1
Ra 128,242,0
K 212,168,169
X 171
Zvav 48,144,192
X 164
K 213,170,171
Zvav 80,144,200
X 165
X 166
Zvav 112,144,208
Ra 132,242,1024
K 204,164,165
X 167
Zvav 144,144,216
X 160
Zvav 148,148,216
K 205,166,167
X 161
Zvav 116,148,208
Ra 136,242,2048
X 162
Zvav 84,148,200
W 3
K 196,160,161
X 163
Zvav 52,148,192
X 176
K 197,162,163
Zvav 56,152,192
Ra 140,242,3072
X 177
Zvav 88,152,200
X 178
K 198,176,177
Zvav 120,152,208
X 179
X 180
Zvav 152,152,216
K 199,178,179
Zvav 156,156,216
X 181
X 182
Zvav 124,156,208
K 206,180,181
X 183
Zvav 92,156,200
K 207,182,183
Zvav 60,156,192
.Ll0t_46%=:
s_cmp_eq_u32 s64, s65
s_cbranch_scc1 .Ll1t_35%=
.Ll0_33%=:
W 0
Cvaa 160,128,0,224
Ra 144,242,4096
Ra 148,242,5120
Mvaa 160,132,4
s_xor_b32 s62, s62, 0x10000
Ra 152,242,6144
Cvaa 164,132,20,228
X 184
Mvaa 164,128,16
X 185
Cvaa 168,128,32,232
X 186
Mvaa 168,132,36
Ra 156,242,7168
K 214,184,185
Cvaa 172,132,52,236
X 187
Mvaa 172,128,48
X 188
Mvaa 160,136,8
Ra 128,242,16384
Mvaa 160,140,12
K 215,186,187
Mvaa 164,140,28
X 189
Mvaa 164,136,24
X 190
Mvaa 168,136,40
Ra 132,242,17408
Mvaa 168,140,44
K 222,188,189
Mvaa 172,140,60
X 191
Mvaa 172,136,56
K 223,190,191
v_smfmac_f32_16x16x64_bf16 a[160:163], a[176:179], v[192:199], v240
Ra 136,242,18432
v_smfmac_f32_16x16x64_bf16 a[164:167], a[176:179], v[200:207], v240
X 160
v_smfmac_f32_16x16x64_bf16 a[168:171], a[176:179], v[208:215], v240
X 161
v_smfmac_f32_16x16x64_bf16 a[172:175], a[176:179], v[216:223], v240
W 3
X 162
Cvaa 176,144,0,224
Ra 140,242,19456
Mvaa 176,148,4
K 192,160,161
Cvaa 180,148,20,228
X 163
)"
R"(Mvaa 180,144,16
X 164
Cvaa 184,144,32,232
Mvaa 184,148,36
K 193,162,163
Cvaa 188,148,52,236
X 165
Mvaa 188,144,48
X 166
Mvaa 176,152,8
Ra 144,242,20480
Ra 148,242,21504
Mvaa 176,156,12
K 200,164,165
Mvaa 180,156,28
X 167
Mvaa 180,152,24
X 168
Mvaa 184,152,40
Mvaa 184,156,44
K 201,166,167
Mvaa 188,156,60
X 169
Mvaa 188,152,56
W 2
Ra 152,242,22528
Mvav 32,128,196
X 170
Mvav 64,128,204
Ra 156,242,23552
K 208,168,169
Mvav 96,128,212
X 171
Mvav 128,128,220
Mvav 132,132,220
X 172
Mvav 100,132,212
s_add_u32 s64, s64, 1
Ra 128,242,8192
K 209,170,171
Mvav 68,132,204
X 173
Mvav 36,132,196
Mvav 40,136,196
X 174
Mvav 72,136,204
Ra 132,242,9216
K 216,172,173
Mvav 104,136,212
X 175
Mvav 136,136,220
Mvav 140,140,220
X 176
Mvav 108,140,212
Ra 136,242,10240
K 217,174,175
Mvav 76,140,204
X 177
Mvav 44,140,196
W 3
Mvav 48,144,196
X 178
Mvav 80,144,204
Ra 140,242,11264
Mvav 112,144,212
K 194,176,177
Mvav 144,144,220
X 179
Mvav 148,148,220
X 180
Mvav 116,148,212
Ra 144,242,12288
Mvav 84,148,204
K 195,178,179
Mvav 52,148,196
X 181
Mvav 56,152,196
X 182
Mvav 88,152,204
Ra 148,242,13312
Mvav 120,152,212
K 202,180,181
Mvav 152,152,220
X 183
Mvav 156,156,220
X 184
Mvav 124,156,212
Ra 152,242,14336
Mvav 92,156,204
K 203,182,183
Mvav 60,156,196
W 3
X 185
Cvaa 160,128,0,224
X 186
Mvaa 160,132,4
Ra 156,242,15360
Cvaa 164,132,20,228
K 210,184,185
Mvaa 164,128,16
X 187
Cvaa 168,128,32,232
X 188
Mvaa 168,132,36
Cvaa 172,132,52,236
K 211,186,187
Mvaa 172,128,48
X 189
Mvaa 160,136,8
Ra 128,242,24576
Ra 132,242,25600
Mvaa 160,140,12
X 190
Mvaa 164,140,28
K 218,188,189
Mvaa 164,136,24
X 191
Mvaa 168,136,40
Mvaa 168,140,44
Mvaa 172,140,60
X 160
Mvaa 172,136,56
W 2
Ra 136,242,26624
K 219,190,191
Cvaa 176,144,0,224
X 161
Mvaa 176,148,4
Ra 140,242,27648
Cvaa 180,148,20,228
X 162
Mvaa 180,144,16
K 196,160,161
Cvaa 184,144,32,232
X 163
Mvaa 184,148,36
X 164
Cvaa 188,148,52,236
K 197,162,163
Mvaa 188,144,48
X 165
Mvaa 176,152,8
Ra 144,242,28672
Ra 148,242,29696
Mvaa 176,156,12
X 166
Mvaa 180,156,28
K 204,164,165
Mvaa 180,152,24
X 167
Mvaa 184,152,40
Mvaa 184,156,44
X 168
Mvaa 188,156,60
Mvaa 188,152,56
W 2
Ra 152,242,30720
K 205,166,167
Mvav 32,128,192
X 169
Mvav 64,128,200
Ra 156,242,31744
Mvav 96,128,208
X 170
Mvav 128,128,216
K 212,168,169
Mvav 132,132,216
X 171
Mvav 100,132,208
X 172
Mvav 68,132,200
Mvav 36,132,192
K 213,170,171
Mvav 40,136,192
X 173
Mvav 72,136,200
X 174
Mvav 104,136,208
Mvav 136,136,216
K 220,172,173
Mvav 140,140,216
X 175
Mvav 108,140,208
X 176
Mvav 76,140,200
W 0
s_barrier
v_xor_b32 v242, 0x10000, v242
Mvav 44,140,192
Ra 128,242,0
K 221,174,175
Mvav 48,144,192
X 177
Mvav 80,144,200
X 178
Mvav 112,144,208
Ra 132,242,1024
Mvav 144,144,216
K 198,176,177
Mvav 148,148,216
X 179
Mvav 116,148,208
X 180
Mvav 84,148,200
Ra 136,242,2048
Mvav 52,148,192
K 199,178,179
Mvav 56,152,192
X 181
Mvav 88,152,200
Ra 140,242,3072
Mvav 120,152,208
X 182
Mvav 152,152,216
K 206,180,181
Mvav 156,156,216
X 183
Mvav 124,156,208
Mvav 92,156,200
K 207,182,183
Mvav 60,156,192
s_cmp_lg_u32 s64, s65
s_cbranch_scc1 .Ll0_33%=
s_cmp_lt_u32 s85, 54
s_cbranch_scc1 .Lmoe_44%=
.Ll1t_35%=:
s_cmp_eq_u32 s64, s66
s_cbranch_scc1 .Lsp1_36%=
.Ll1_34%=:
W 0
Cvaa 160,128,0,224
Ra 144,242,4096
Ra 148,242,5120
Mvaa 160,132,4
s_add_u32 m0, s62, 0
Ra 152,242,6144
Cvaa 164,132,20,228
X 184
Mvaa 164,128,16
X 185
Cvaa 168,128,32,232
X 186
Mvaa 168,132,36
Ra 156,242,7168
K 214,184,185
Cvaa 172,132,52,236
Dc 1,8,61,0
Mvaa 172,128,48
X 187
Mvaa 160,136,8
X 188
Mvaa 160,140,12
Ra 128,242,16384
K 215,186,187
Mvaa 164,140,28
Dc 1,8,61,1024
Mvaa 164,136,24
X 189
Mvaa 168,136,40
X 190
Mvaa 168,140,44
Ra 132,242,17408
K 222,188,189
Mvaa 172,140,60
Dc 1,8,61,2048
Mvaa 172,136,56
X 191
v_smfmac_f32_16x16x64_bf16 a[160:163], a[176:179], v[192:199], v240
Ra 136,242,18432
K 223,190,191
v_smfmac_f32_16x16x64_bf16 a[164:167], a[176:179], v[200:207], v240
X 160
v_smfmac_f32_16x16x64_bf16 a[168:171], a[176:179], v[208:215], v240
Dc 1,8,61,3072
v_smfmac_f32_16x16x64_bf16 a[172:175], a[176:179], v[216:223], v240
W 3
X 161
Cvaa 176,144,0,224
s_add_u32 m0, s62, 4096
X 162
Mvaa 176,148,4
Ra 140,242,19456
K 192,160,161
Cvaa 180,148,20,228
X 163
Mvaa 180,144,16
Dc 243,8,61,0
Cvaa 184,144,32,232
X 164
Mvaa 184,148,36
K 193,162,163
Cvaa 188,148,52,236
X 165
Mvaa 188,144,48
Dc 243,8,61,1024
Ra 144,242,20480
Mvaa 176,152,8
X 166
Mvaa 176,156,12
Ra 148,242,21504
K 200,164,165
Mvaa 180,156,28
X 167
Mvaa 180,152,24
Dc 243,8,61,2048
Mvaa 184,152,40
X 168
Mvaa 184,156,44
K 201,166,167
Mvaa 188,156,60
X 169
Mvaa 188,152,56
W 2
Dc 243,8,61,3072
Ra 152,242,22528
Mvav 32,128,196
s_add_u32 m0, s62, 8192
X 170
Mvav 64,128,204
Ra 156,242,23552
K 208,168,169
Mvav 96,128,212
X 171
Mvav 128,128,220
Dc 244,8,61,0
Mvav 132,132,220
X 172
Mvav 100,132,212
Ra 128,242,8192
K 209,170,171
Mvav 68,132,204
X 173
Mvav 36,132,196
Dc 244,8,61,1024
Mvav 40,136,196
X 174
Mvav 72,136,204
Ra 132,242,9216
K 216,172,173
Mvav 104,136,212
X 175
Mvav 136,136,220
Dc 244,8,61,2048
Mvav 140,140,220
X 176
Mvav 108,140,212
Ra 136,242,10240
K 217,174,175
Mvav 76,140,204
X 177
Mvav 44,140,196
W 3
Dc 244,8,61,3072
Mvav 48,144,196
X 178
Mvav 80,144,204
s_add_u32 m0, s62, 12288
Ra 140,242,11264
Mvav 112,144,212
K 194,176,177
Mvav 144,144,220
X 179
Mvav 148,148,220
X 180
Mvav 116,148,212
Dc 245,8,61,0
Ra 144,242,12288
Mvav 84,148,204
K 195,178,179
Mvav 52,148,196
X 181
Mvav 56,152,196
X 182
Mvav 88,152,204
Dc 245,8,61,1024
Ra 148,242,13312
Mvav 120,152,212
K 202,180,181
Mvav 152,152,220
X 183
Mvav 156,156,220
X 184
Mvav 124,156,212
Dc 245,8,61,2048
Ra 152,242,14336
Mvav 92,156,204
K 203,182,183
Mvav 60,156,196
W 3
X 185
Cvaa 160,128,0,224
X 186
Mvaa 160,132,4
Dc 245,8,61,3072
Ra 156,242,15360
Cvaa 164,132,20,228
s_add_u32 s61, s61, s63
K 210,184,185
Mvaa 164,128,16
X 187
)"
R"(Cvaa 168,128,32,232
X 188
Mvaa 168,132,36
Cvaa 172,132,52,236
s_xor_b32 s62, s62, 0x10000
K 211,186,187
Mvaa 172,128,48
X 189
Mvaa 160,136,8
Ra 128,242,24576
Ra 132,242,25600
Mvaa 160,140,12
s_add_u32 s64, s64, 1
X 190
Mvaa 164,140,28
K 218,188,189
Mvaa 164,136,24
X 191
Mvaa 168,136,40
Mvaa 168,140,44
X 160
Mvaa 172,140,60
K 219,190,191
Mvaa 172,136,56
W 2
X 161
Cvaa 176,144,0,224
Ra 136,242,26624
Ra 140,242,27648
Mvaa 176,148,4
X 162
Cvaa 180,148,20,228
K 196,160,161
Mvaa 180,144,16
X 163
Cvaa 184,144,32,232
Mvaa 184,148,36
X 164
Cvaa 188,148,52,236
K 197,162,163
Mvaa 188,144,48
X 165
Mvaa 176,152,8
Ra 144,242,28672
Ra 148,242,29696
Mvaa 176,156,12
X 166
Mvaa 180,156,28
K 204,164,165
Mvaa 180,152,24
X 167
Mvaa 184,152,40
Mvaa 184,156,44
X 168
Mvaa 188,156,60
Mvaa 188,152,56
W 2
Ra 152,242,30720
K 205,166,167
Mvav 32,128,192
X 169
Mvav 64,128,200
Ra 156,242,31744
Mvav 96,128,208
X 170
Mvav 128,128,216
K 212,168,169
Mvav 132,132,216
X 171
Mvav 100,132,208
X 172
Mvav 68,132,200
Mvav 36,132,192
K 213,170,171
Mvav 40,136,192
X 173
Mvav 72,136,200
X 174
Mvav 104,136,208
Mvav 136,136,216
K 220,172,173
Mvav 140,140,216
X 175
Mvav 108,140,208
X 176
Mvav 76,140,200
s_waitcnt vmcnt(0) lgkmcnt(0)
s_barrier
v_xor_b32 v242, 0x10000, v242
Mvav 44,140,192
Ra 128,242,0
K 221,174,175
Mvav 48,144,192
X 177
Mvav 80,144,200
X 178
Mvav 112,144,208
Ra 132,242,1024
Mvav 144,144,216
K 198,176,177
Mvav 148,148,216
X 179
Mvav 116,148,208
X 180
Mvav 84,148,200
Ra 136,242,2048
Mvav 52,148,192
K 199,178,179
Mvav 56,152,192
X 181
Mvav 88,152,200
Ra 140,242,3072
Mvav 120,152,208
X 182
Mvav 152,152,216
K 206,180,181
Mvav 156,156,216
X 183
Mvav 124,156,208
Mvav 92,156,200
K 207,182,183
Mvav 60,156,192
s_cmp_lg_u32 s64, s66
s_cbranch_scc1 .Ll1_34%=
s_cmp_ge_u32 s14, 2
s_cbranch_scc1 .Lf1r_47%=
.Lsp1_36%=:
W 0
Cvaa 160,128,0,224
Ra 144,242,4096
Ra 148,242,5120
Mvaa 160,132,4
s_add_u32 m0, s62, 0
Ra 152,242,6144
Cvaa 164,132,20,228
X 184
Mvaa 164,128,16
X 185
Cvaa 168,128,32,232
X 186
Mvaa 168,132,36
Ra 156,242,7168
K 214,184,185
Cvaa 172,132,52,236
Dc 1,8,61,0
Mvaa 172,128,48
X 187
Mvaa 160,136,8
X 188
Mvaa 160,140,12
Ra 128,242,16384
K 215,186,187
Mvaa 164,140,28
Dc 1,8,61,1024
Ra 132,242,17408
Mvaa 164,136,24
X 189
Mvaa 168,136,40
X 190
Mvaa 168,140,44
Dc 1,8,61,2048
K 222,188,189
Mvaa 172,140,60
X 191
Mvaa 172,136,56
Ra 136,242,18432
K 223,190,191
v_smfmac_f32_16x16x64_bf16 a[160:163], a[176:179], v[192:199], v240
Dc 1,8,61,3072
Ra 140,242,19456
v_smfmac_f32_16x16x64_bf16 a[164:167], a[176:179], v[200:207], v240
s_add_u32 m0, s62, 4096
v_cndmask_b32_e64 v160, v241, v160, s[72:73]
v_cndmask_b32_e64 v161, v241, v161, s[74:75]
v_smfmac_f32_16x16x64_bf16 a[168:171], a[176:179], v[208:215], v240
v_cndmask_b32_e64 v162, v241, v162, s[76:77]
v_cndmask_b32_e64 v163, v241, v163, s[78:79]
v_smfmac_f32_16x16x64_bf16 a[172:175], a[176:179], v[216:223], v240
W 4
X 160
Cvaa 180,144,16,228
X 161
Mvaa 180,148,20
Dc 243,8,61,0
Cvaa 184,148,36,232
X 162
Mvaa 184,144,32
K 192,160,161
Cvaa 188,144,48,236
X 163
Mvaa 188,148,52
Dc 243,8,61,1024
Ra 144,242,20480
Mvaa 180,152,24
X 164
Mvaa 180,156,28
Ra 148,242,21504
K 193,162,163
Mvaa 184,156,44
Dc 243,8,61,2048
Mvaa 184,152,40
X 165
Mvaa 188,152,56
X 166
Mvaa 188,156,60
W 2
Dc 243,8,61,3072
K 200,164,165
Mvav 32,128,196
s_add_u32 m0, s62, 8192
X 167
Mvav 64,128,204
Ra 152,242,22528
Ra 156,242,23552
Mvav 96,128,212
X 168
Mvav 128,128,220
Dc 244,8,61,0
K 201,166,167
Mvav 132,132,220
X 169
Mvav 100,132,212
X 170
Mvav 68,132,204
Ra 128,242,8192
K 208,168,169
Mvav 36,132,196
X 171
Mvav 40,136,196
Dc 244,8,61,1024
Ra 132,242,9216
Mvav 72,136,204
X 172
Mvav 104,136,212
K 209,170,171
Mvav 136,136,220
X 173
Mvav 140,140,220
Dc 244,8,61,2048
Ra 136,242,10240
Mvav 108,140,212
X 174
Mvav 76,140,204
K 216,172,173
Mvav 44,140,196
W 3
Dc 244,8,61,3072
Ra 140,242,11264
Mvav 48,144,196
s_add_u32 m0, s62, 12288
X 175
Mvav 80,144,204
v_mov_b32 v194, 0
K 217,174,175
Mvav 112,144,212
v_mov_b32 v195, 0
v_cndmask_b32_e64 v180, v241, v180, s[72:73]
Mvav 144,144,220
v_cndmask_b32_e64 v181, v241, v181, s[74:75]
v_cndmask_b32_e64 v182, v241, v182, s[76:77]
Mvav 148,148,220
Dc 245,8,61,0
v_cndmask_b32_e64 v183, v241, v183, s[78:79]
Mvav 116,148,212
X 180
Mvav 84,148,204
Ra 144,242,12288
Mvav 52,148,196
X 181
Mvav 56,152,196
Dc 245,8,61,1024
Ra 148,242,13312
Mvav 88,152,204
X 182
Mvav 120,152,212
K 202,180,181
Mvav 152,152,220
X 183
Mvav 156,156,220
Dc 245,8,61,2048
Ra 152,242,14336
Mvav 124,156,212
X 184
Mvav 92,156,204
K 203,182,183
Mvav 60,156,196
W 3
Dc 245,8,61,3072
Ra 156,242,15360
Cvaa 168,128,32,232
s_add_u32 s61, s61, s63
X 185
Mvaa 168,132,36
s_xor_b32 s62, s62, 0x10000
X 186
Cvaa 172,132,52,236
K 210,184,185
Mvaa 172,128,48
s_add_u32 s64, s64, 1
X 187
Mvaa 168,136,40
X 188
Mvaa 168,140,44
Ra 128,242,24576
K 211,186,187
Mvaa 172,140,60
Ra 132,242,25600
Mvaa 172,136,56
Ra 136,242,26624
Ra 140,242,27648
X 189
X 190
K 218,188,189
X 191
v_mov_b32 v196, 0
K 219,190,191
W 4
Cvaa 188,144,48,236
Ra 144,242,28672
Mvaa 188,148,52
v_mov_b32 v197, 0
v_mov_b32 v204, 0
Mvaa 188,152,56
Ra 148,242,29696
v_mov_b32 v205, 0
Mvaa 188,156,60
W 2
Ra 152,242,30720
v_cndmask_b32_e64 v168, v241, v168, s[72:73]
Mvav 32,128,192
v_cndmask_b32_e64 v169, v241, v169, s[74:75]
v_cndmask_b32_e64 v170, v241, v170, s[76:77]
Mvav 64,128,200
Ra 156,242,31744
v_cndmask_b32_e64 v171, v241, v171, s[78:79]
Mvav 96,128,208
X 168
Mvav 128,128,216
X 169
Mvav 132,132,216
X 170
Mvav 100,132,208
v_mov_b32 v224, v250
Mvav 68,132,200
K 212,168,169
v_mov_b32 v225, v250
Mvav 36,132,192
X 171
Mvav 40,136,192
v_mov_b32 v226, v250
Mvav 72,136,200
X 172
Mvav 104,136,208
v_mov_b32 v227, v250
K 213,170,171
Mvav 136,136,216
X 173
Mvav 140,140,216
v_mov_b32 v228, v251
v_mov_b32 v229, v251
Mvav 108,140,208
X 174
Mvav 76,140,200
K 220,172,173
v_mov_b32 v230, v251
Mvav 44,140,192
W 0
X 175
Mvav 48,144,192
v_mov_b32 v231, v251
v_mov_b32 v198, 0
Mvav 80,144,200
K 221,174,175
v_mov_b32 v232, v248
Mvav 112,144,208
v_mov_b32 v199, 0
v_mov_b32 v206, 0
Mvav 144,144,216
v_mov_b32 v233, v248
v_mov_b32 v207, 0
Mvav 148,148,216
v_mov_b32 v214, 0
v_mov_b32 v234, v248
Mvav 116,148,208
v_mov_b32 v215, 0
v_cndmask_b32_e64 v188, v241, v188, s[72:73]
Mvav 84,148,200
v_mov_b32 v235, v248
v_cndmask_b32_e64 v189, v241, v189, s[74:75]
Mvav 52,148,192
v_cndmask_b32_e64 v190, v241, v190, s[76:77]
v_mov_b32 v236, v249
Mvav 56,152,192
v_cndmask_b32_e64 v191, v241, v191, s[78:79]
v_mov_b32 v237, v249
Mvav 88,152,200
X 188
Mvav 120,152,208
X 189
Mvav 152,152,216
)"
R"(v_mov_b32 v238, v249
v_mov_b32 v239, v249
Mvav 156,156,216
X 190
Mvav 124,156,208
s_waitcnt vmcnt(0)
s_barrier
v_xor_b32 v242, 0x10000, v242
Mvav 92,156,200
Ra 128,242,40960
K 222,188,189
Mvav 60,156,192
Ra 132,242,41984
Ra 136,242,43008
Ra 140,242,44032
X 191
s_nop 0
K 223,190,191
s_cmp_eq_u32 s64, 15
s_cbranch_scc1 .Lfs_42%=
.Lsp2_37%=:
v_smfmac_f32_16x16x64_bf16 a[160:163], a[176:179], v[192:199], v240
s_add_u32 m0, s62, 0
Ra 144,242,45056
Ra 148,242,46080
Ra 152,242,47104
Ra 156,242,48128
v_smfmac_f32_16x16x64_bf16 a[164:167], a[176:179], v[200:207], v240
v_smfmac_f32_16x16x64_bf16 a[168:171], a[176:179], v[208:215], v240
v_smfmac_f32_16x16x64_bf16 a[172:175], a[176:179], v[216:223], v240
W 4
Dc 1,8,61,0
Cvaa 168,128,96,232
v_mov_b32 v192, 0
G 12,160
Mvaa 168,132,100
Dc 1,8,61,1024
G 4,161
A 12,12,4
v_cmp_nlt_f32_e64 s[86:87], v12, s90
v_mov_b32 v193, 0
Cvaa 172,132,116,236
v_rcp_f32 v12, v12
s_or_b64 s[88:89], s[88:89], s[86:87]
v_accvgpr_write_b32 a160, 0
v_mov_b32 v200, 0
v_accvgpr_write_b32 a161, 0
v_accvgpr_write_b32 a162, 0
Mvaa 172,128,112
Dc 1,8,61,2048
v_accvgpr_write_b32 a163, 0
G 13,164
v_mov_b32 v201, 0
G 5,165
A 13,13,5
Mvaa 168,136,104
Ra 128,242,16384
Ra 132,242,17408
v_cmp_nlt_f32_e64 s[86:87], v13, s90
v_rcp_f32 v13, v13
s_or_b64 s[88:89], s[88:89], s[86:87]
v_accvgpr_write_b32 a164, 0
Mvaa 168,140,108
Dc 1,8,61,3072
v_accvgpr_write_b32 a165, 0
v_accvgpr_write_b32 a166, 0
v_accvgpr_write_b32 a167, 0
U 32,32,12
U 33,33,12
Mvaa 172,140,124
U 34,34,12
U 35,35,12
U 36,36,12
U 37,37,12
U 38,38,12
U 39,39,12
Mvaa 172,136,120
s_add_u32 m0, s62, 4096
Ra 136,242,18432
Ra 140,242,19456
K 32,32,33
K 33,34,35
K 34,36,37
W 4
Cvaa 188,144,112,236
K 35,38,39
s_add_u32 s93, s67, 0
buffer_store_dwordx4 v[32:35], v1, s[8:11], s93 offen offset:0 sc1
U 40,40,12
U 41,41,12
U 42,42,12
U 43,43,12
Mvaa 188,148,116
Ra 144,242,20480
v_cndmask_b32_e64 v168, v241, v168, s[72:73]
v_cndmask_b32_e64 v169, v241, v169, s[74:75]
v_cndmask_b32_e64 v170, v241, v170, s[76:77]
v_cndmask_b32_e64 v171, v241, v171, s[78:79]
U 44,44,12
Mvaa 188,152,120
X 168
X 169
U 45,45,12
U 46,46,12
Mvaa 188,156,124
W 1
Ra 148,242,21504
Dc 243,8,61,0
U 47,47,12
K 40,40,41
K 41,42,43
K 42,44,45
Mvav 96,128,212
Ra 152,242,22528
Ra 156,242,23552
K 43,46,47
X 170
K 208,168,169
Mvav 128,128,220
buffer_store_dwordx4 v[40:43], v1, s[8:11], s93 offen offset:1024 sc1
U 48,48,12
U 49,49,12
U 50,50,12
U 51,51,12
U 52,52,12
Mvav 132,132,220
U 53,53,12
U 54,54,12
U 55,55,12
K 48,48,49
K 49,50,51
K 50,52,53
Mvav 100,132,212
Dc 243,8,61,1024
Ra 128,242,32768
Ra 132,242,33792
X 171
K 51,54,55
Mvav 104,136,212
X 172
K 209,170,171
X 173
Mvav 136,136,220
buffer_store_dwordx4 v[48:51], v1, s[8:11], s93 offen offset:2048 sc1
U 56,56,12
U 57,57,12
U 58,58,12
U 59,59,12
U 60,60,12
Mvav 140,140,220
U 61,61,12
U 62,62,12
U 63,63,12
K 56,56,57
K 57,58,59
K 58,60,61
Mvav 108,140,212
W 2
Dc 243,8,61,2048
Ra 136,242,34816
Ra 140,242,35840
K 59,62,63
X 174
Mvav 112,144,212
K 216,172,173
X 175
Mvav 144,144,220
buffer_store_dwordx4 v[56:59], v1, s[8:11], s93 offen offset:3072 sc1
Ra 144,242,36864
G 14,168
G 6,169
A 14,14,6
v_cmp_nlt_f32_e64 s[86:87], v14, s90
Mvav 148,148,220
v_rcp_f32 v14, v14
s_or_b64 s[88:89], s[88:89], s[86:87]
v_accvgpr_write_b32 a168, 0
v_accvgpr_write_b32 a169, 0
v_accvgpr_write_b32 a170, 0
v_accvgpr_write_b32 a171, 0
Mvav 116,148,212
Dc 243,8,61,3072
s_add_u32 m0, s62, 8192
G 15,172
G 7,173
A 15,15,7
v_cmp_nlt_f32_e64 s[86:87], v15, s90
v_mov_b32 v194, 0
Mvav 120,152,212
v_rcp_f32 v15, v15
s_or_b64 s[88:89], s[88:89], s[86:87]
v_accvgpr_write_b32 a172, 0
v_accvgpr_write_b32 a173, 0
v_accvgpr_write_b32 a174, 0
v_accvgpr_write_b32 a175, 0
Mvav 152,152,220
Dc 244,8,61,0
U 64,64,13
U 65,65,13
U 66,66,13
U 67,67,13
U 68,68,13
Mvav 156,156,220
U 69,69,13
U 70,70,13
U 71,71,13
K 64,64,65
K 65,66,67
K 66,68,69
Mvav 124,156,212
W 1
Dc 244,8,61,1024
Ra 148,242,37888
K 67,70,71
s_add_u32 s93, s67, 131072
K 217,174,175
v_mov_b32 v195, 0
Cvaa 160,128,64,224
Ra 152,242,38912
Ra 156,242,39936
v_mov_b32 v202, 0
v_mov_b32 v203, 0
v_mov_b32 v210, 0
v_mov_b32 v211, 0
Mvaa 160,132,68
buffer_store_dwordx4 v[64:67], v1, s[8:11], s93 offen offset:0 sc1
U 72,72,13
U 73,73,13
U 74,74,13
U 75,75,13
U 76,76,13
Cvaa 164,132,84,228
U 77,77,13
U 78,78,13
U 79,79,13
K 72,72,73
K 73,74,75
K 74,76,77
Mvaa 164,128,80
Dc 244,8,61,2048
K 75,78,79
v_cndmask_b32_e64 v188, v241, v188, s[72:73]
v_cndmask_b32_e64 v189, v241, v189, s[74:75]
v_cndmask_b32_e64 v190, v241, v190, s[76:77]
v_cndmask_b32_e64 v191, v241, v191, s[78:79]
Cvaa 168,128,96,232
X 188
X 189
Mvaa 168,132,100
buffer_store_dwordx4 v[72:75], v1, s[8:11], s93 offen offset:1024 sc1
U 80,80,13
U 81,81,13
U 82,82,13
U 83,83,13
U 84,84,13
Cvaa 172,132,116,236
U 85,85,13
U 86,86,13
U 87,87,13
K 80,80,81
K 81,82,83
K 82,84,85
Mvaa 172,128,112
Dc 244,8,61,3072
s_add_u32 m0, s62, 12288
Ra 128,242,49152
Ra 132,242,50176
K 83,86,87
X 190
Mvaa 160,136,72
K 218,188,189
X 191
Mvaa 160,140,76
buffer_store_dwordx4 v[80:83], v1, s[8:11], s93 offen offset:2048 sc1
U 88,88,13
U 89,89,13
U 90,90,13
U 91,91,13
U 92,92,13
Mvaa 164,140,92
U 93,93,13
U 94,94,13
U 95,95,13
K 88,88,89
K 89,90,91
K 90,92,93
Mvaa 164,136,88
K 91,94,95
buffer_store_dwordx4 v[88:91], v1, s[8:11], s93 offen offset:3072 sc1
U 96,96,14
U 97,97,14
U 98,98,14
U 99,99,14
Mvaa 168,136,104
U 100,100,14
U 101,101,14
U 102,102,14
U 103,103,14
K 96,96,97
K 97,98,99
Mvaa 168,140,108
Dc 245,8,61,0
K 98,100,101
K 99,102,103
s_add_u32 s93, s67, 262144
v_cndmask_b32_e64 v160, v241, v160, s[72:73]
K 219,190,191
v_cndmask_b32_e64 v161, v241, v161, s[74:75]
Mvaa 172,140,124
v_cndmask_b32_e64 v162, v241, v162, s[76:77]
v_cndmask_b32_e64 v163, v241, v163, s[78:79]
X 160
Mvaa 172,136,120
W 2
Dc 245,8,61,1024
Ra 136,242,51200
Ra 140,242,52224
X 161
Cvaa 180,144,80,228
Mvaa 180,148,84
buffer_store_dwordx4 v[96:99], v1, s[8:11], s93 offen offset:0 sc1
U 104,104,14
U 105,105,14
U 106,106,14
U 107,107,14
U 108,108,14
Cvaa 184,148,100,232
U 109,109,14
U 110,110,14
U 111,111,14
K 104,104,105
K 105,106,107
K 106,108,109
Mvaa 184,144,96
Dc 245,8,61,2048
K 107,110,111
X 162
K 196,160,161
Cvaa 188,144,112,236
Ra 144,242,53248
X 163
Mvaa 188,148,116
buffer_store_dwordx4 v[104:107], v1, s[8:11], s93 offen offset:1024 sc1
U 112,112,14
U 113,113,14
U 114,114,14
U 115,115,14
U 116,116,14
Mvaa 180,152,88
U 117,117,14
U 118,118,14
U 119,119,14
K 112,112,113
K 113,114,115
K 114,116,117
Mvaa 180,156,92
Dc 245,8,61,3072
s_add_u32 s61, s61, s63
s_xor_b32 s62, s62, 0x10000
s_add_u32 s64, s64, 1
K 115,118,119
X 164
Mvaa 184,156,108
Ra 148,242,54272
K 197,162,163
X 165
Mvaa 184,152,104
buffer_store_dwordx4 v[112:115], v1, s[8:11], s93 offen offset:2048 sc1
U 120,120,14
U 121,121,14
U 122,122,14
U 123,123,14
U 124,124,14
Mvaa 188,152,120
U 125,125,14
U 126,126,14
U 127,127,14
K 120,120,121
K 121,122,123
K 122,124,125
Mvaa 188,156,124
W 2
K 123,126,127
buffer_store_dwordx4 v[120:123], v1, s[8:11], s93 offen offset:3072 sc1
U 128,128,15
U 129,129,15
U 130,130,15
U 131,131,15
Zvav 32,128,192
U 132,132,15
U 133,133,15
U 134,134,15
U 135,135,15
K 128,128,129
K 129,130,131
Zvav 64,128,200
K 130,132,133
K 131,134,135
s_add_u32 s93, s67, 393216
buffer_store_dwordx4 v[128:131], v1, s[8:11], s93 offen offset:0 sc1
)"
R"(U 136,136,15
U 137,137,15
U 138,138,15
Zvav 96,128,208
U 139,139,15
U 140,140,15
U 141,141,15
U 142,142,15
U 143,143,15
K 136,136,137
Zvav 128,128,216
K 137,138,139
K 138,140,141
K 139,142,143
buffer_store_dwordx4 v[136:139], v1, s[8:11], s93 offen offset:1024 sc1
U 144,144,15
U 145,145,15
Zvav 132,132,216
U 146,146,15
U 147,147,15
U 148,148,15
U 149,149,15
U 150,150,15
U 151,151,15
Zvav 100,132,208
K 144,144,145
K 145,146,147
K 146,148,149
K 147,150,151
buffer_store_dwordx4 v[144:147], v1, s[8:11], s93 offen offset:2048 sc1
U 152,152,15
Zvav 68,132,200
U 153,153,15
U 154,154,15
U 155,155,15
U 156,156,15
U 157,157,15
U 158,158,15
Zvav 36,132,192
U 159,159,15
K 152,152,153
K 153,154,155
K 154,156,157
K 155,158,159
buffer_store_dwordx4 v[152:155], v1, s[8:11], s93 offen offset:3072 sc1
Zvav 40,136,192
Ra 152,242,55296
X 166
K 204,164,165
X 167
Zvav 72,136,200
Ra 156,242,56320
X 168
K 205,166,167
X 169
Zvav 104,136,208
X 170
K 212,168,169
X 171
Zvav 136,136,216
X 172
K 213,170,171
X 173
Zvav 140,140,216
Zvav 108,140,208
X 174
Zvav 76,140,200
s_waitcnt vmcnt(6) lgkmcnt(0)
s_barrier
v_xor_b32 v242, 0x10000, v242
Zvav 44,140,192
Ra 128,242,40960
K 220,172,173
X 175
Zvav 48,144,192
Zvav 80,144,200
v_mov_b32 v198, 0
Zvav 112,144,208
Ra 132,242,41984
K 221,174,175
Zvav 144,144,216
v_mov_b32 v199, 0
v_cndmask_b32_e64 v180, v241, v180, s[72:73]
Zvav 148,148,216
v_cndmask_b32_e64 v181, v241, v181, s[74:75]
Zvav 116,148,208
Ra 136,242,43008
v_cndmask_b32_e64 v182, v241, v182, s[76:77]
Zvav 84,148,200
v_cndmask_b32_e64 v183, v241, v183, s[78:79]
Zvav 52,148,192
X 180
Zvav 56,152,192
Ra 140,242,44032
X 181
Zvav 88,152,200
Zvav 120,152,208
X 182
Zvav 152,152,216
K 206,180,181
Zvav 156,156,216
X 183
Zvav 124,156,208
Zvav 92,156,200
s_waitcnt vmcnt(0)
s_and_b64 s[86:87], s[88:89], s[24:25]
s_cbranch_scc1 .LL_49%=
v_mov_b64 v[28:29], s[12:13]
s_mov_b64 exec, 1
buffer_store_dwordx2 v[28:29], off, s[8:11], s69 sc1
s_mov_b64 exec, -1
.LL_49%=:
K 207,182,183
Zvav 60,156,192
s_cmp_eq_u32 s64, 15
s_cbranch_scc1 .Lf2_41%=
.Ll2_38%=:
W 0
Cvaa 160,128,64,224
Ra 144,242,45056
Ra 148,242,46080
Mvaa 160,132,68
s_add_u32 m0, s62, 0
Ra 152,242,47104
Cvaa 164,132,84,228
X 184
Mvaa 164,128,80
X 185
Cvaa 168,128,96,232
X 186
Mvaa 168,132,100
Ra 156,242,48128
K 214,184,185
Cvaa 172,132,116,236
Dc 1,8,61,0
Mvaa 172,128,112
X 187
Mvaa 160,136,72
X 188
Mvaa 160,140,76
Ra 128,242,57344
K 215,186,187
Mvaa 164,140,92
Dc 1,8,61,1024
Mvaa 164,136,88
X 189
Mvaa 168,136,104
X 190
Mvaa 168,140,108
Ra 132,242,58368
K 222,188,189
Mvaa 172,140,124
Dc 1,8,61,2048
Mvaa 172,136,120
X 191
v_smfmac_f32_16x16x64_bf16 a[160:163], a[176:179], v[192:199], v240
Ra 136,242,59392
K 223,190,191
v_smfmac_f32_16x16x64_bf16 a[164:167], a[176:179], v[200:207], v240
X 160
v_smfmac_f32_16x16x64_bf16 a[168:171], a[176:179], v[208:215], v240
Dc 1,8,61,3072
v_smfmac_f32_16x16x64_bf16 a[172:175], a[176:179], v[216:223], v240
W 3
X 161
Cvaa 176,144,64,224
s_add_u32 m0, s62, 4096
X 162
Mvaa 176,148,68
Ra 140,242,60416
K 192,160,161
Cvaa 180,148,84,228
X 163
Mvaa 180,144,80
Dc 243,8,61,0
Cvaa 184,144,96,232
X 164
Mvaa 184,148,100
K 193,162,163
Cvaa 188,148,116,236
X 165
Mvaa 188,144,112
Dc 243,8,61,1024
Ra 144,242,61440
Mvaa 176,152,72
X 166
Mvaa 176,156,76
Ra 148,242,62464
K 200,164,165
Mvaa 180,156,92
X 167
Mvaa 180,152,88
Dc 243,8,61,2048
Mvaa 184,152,104
X 168
Mvaa 184,156,108
K 201,166,167
Mvaa 188,156,124
X 169
Mvaa 188,152,120
W 2
Dc 243,8,61,3072
Ra 152,242,63488
Mvav 32,128,196
s_add_u32 m0, s62, 8192
X 170
Mvav 64,128,204
Ra 156,242,64512
K 208,168,169
Mvav 96,128,212
X 171
Mvav 128,128,220
Dc 244,8,61,0
Mvav 132,132,220
X 172
Mvav 100,132,212
Ra 128,242,32768
K 209,170,171
Mvav 68,132,204
X 173
Mvav 36,132,196
Dc 244,8,61,1024
Mvav 40,136,196
X 174
Mvav 72,136,204
Ra 132,242,33792
K 216,172,173
Mvav 104,136,212
X 175
Mvav 136,136,220
Dc 244,8,61,2048
Mvav 140,140,220
X 176
Mvav 108,140,212
Ra 136,242,34816
K 217,174,175
Mvav 76,140,204
X 177
Mvav 44,140,196
W 3
Dc 244,8,61,3072
Mvav 48,144,196
X 178
Mvav 80,144,204
s_add_u32 m0, s62, 12288
Ra 140,242,35840
Mvav 112,144,212
K 194,176,177
Mvav 144,144,220
X 179
Mvav 148,148,220
X 180
Mvav 116,148,212
Dc 245,8,61,0
Ra 144,242,36864
Mvav 84,148,204
K 195,178,179
Mvav 52,148,196
X 181
Mvav 56,152,196
X 182
Mvav 88,152,204
Dc 245,8,61,1024
Ra 148,242,37888
Mvav 120,152,212
K 202,180,181
Mvav 152,152,220
X 183
Mvav 156,156,220
X 184
Mvav 124,156,212
Dc 245,8,61,2048
Ra 152,242,38912
Mvav 92,156,204
K 203,182,183
Mvav 60,156,196
W 3
X 185
Cvaa 160,128,64,224
X 186
Mvaa 160,132,68
Dc 245,8,61,3072
Ra 156,242,39936
Cvaa 164,132,84,228
s_add_u32 s61, s61, s63
K 210,184,185
Mvaa 164,128,80
X 187
Cvaa 168,128,96,232
X 188
Mvaa 168,132,100
Cvaa 172,132,116,236
s_xor_b32 s62, s62, 0x10000
K 211,186,187
Mvaa 172,128,112
X 189
Mvaa 160,136,72
Ra 128,242,49152
Ra 132,242,50176
Mvaa 160,140,76
s_add_u32 s64, s64, 1
X 190
Mvaa 164,140,92
K 218,188,189
Mvaa 164,136,88
X 191
Mvaa 168,136,104
Mvaa 168,140,108
X 160
Mvaa 172,140,124
K 219,190,191
Mvaa 172,136,120
W 2
X 161
Cvaa 176,144,64,224
Ra 136,242,51200
Ra 140,242,52224
Mvaa 176,148,68
X 162
Cvaa 180,148,84,228
K 196,160,161
Mvaa 180,144,80
X 163
Cvaa 184,144,96,232
Mvaa 184,148,100
X 164
Cvaa 188,148,116,236
K 197,162,163
Mvaa 188,144,112
X 165
Mvaa 176,152,72
Ra 144,242,53248
Ra 148,242,54272
Mvaa 176,156,76
X 166
Mvaa 180,156,92
K 204,164,165
Mvaa 180,152,88
X 167
Mvaa 184,152,104
Mvaa 184,156,108
X 168
Mvaa 188,156,124
Mvaa 188,152,120
W 2
Ra 152,242,55296
K 205,166,167
Mvav 32,128,192
X 169
Mvav 64,128,200
Ra 156,242,56320
Mvav 96,128,208
X 170
Mvav 128,128,216
K 212,168,169
Mvav 132,132,216
X 171
Mvav 100,132,208
X 172
Mvav 68,132,200
Mvav 36,132,192
K 213,170,171
Mvav 40,136,192
X 173
Mvav 72,136,200
X 174
Mvav 104,136,208
Mvav 136,136,216
K 220,172,173
Mvav 140,140,216
X 175
Mvav 108,140,208
X 176
Mvav 76,140,200
s_waitcnt vmcnt(0) lgkmcnt(0)
s_barrier
v_xor_b32 v242, 0x10000, v242
Mvav 44,140,192
Ra 128,242,40960
K 221,174,175
Mvav 48,144,192
X 177
Mvav 80,144,200
X 178
Mvav 112,144,208
Ra 132,242,41984
Mvav 144,144,216
K 198,176,177
Mvav 148,148,216
X 179
Mvav 116,148,208
X 180
Mvav 84,148,200
Ra 136,242,43008
Mvav 52,148,192
K 199,178,179
Mvav 56,152,192
X 181
Mvav 88,152,200
Ra 140,242,44032
)"
R"(Mvav 120,152,208
X 182
Mvav 152,152,216
K 206,180,181
Mvav 156,156,216
X 183
Mvav 124,156,208
Mvav 92,156,200
K 207,182,183
Mvav 60,156,192
s_cmp_lg_u32 s64, 15
s_cbranch_scc1 .Ll2_38%=
.Lf2_41%=:
W 0
Cvaa 160,128,64,224
v_bfe_u32 v28, v1, 4, 2
v_lshlrev_b32 v28, 3, v28
Mvaa 160,132,68
Ra 144,242,45056
Ra 148,242,46080
Cvaa 164,132,84,228
Ra 152,242,47104
buffer_load_dwordx2 v[28:29], v28, s[8:11], s54 offen sc1
Mvaa 164,128,80
s_lshl_b32 s92, s58, 2
s_add_u32 s91, s92, s51
s_add_u32 s95, s91, 3
Cvaa 168,128,96,232
s_and_b32 s95, s95, 15
s_mul_i32 s95, s95, 40960
s_mul_i32 s71, s51, 10240
Mvaa 168,132,100
s_add_u32 s71, s71, 0
s_add_u32 m0, s71, s48
s_lshl2_add_u32 s94, s48, s95
Cvaa 172,132,116,236
X 184
Mvaa 172,128,112
X 185
Mvaa 160,136,72
X 186
Mvaa 160,140,76
Ra 156,242,48128
K 214,184,185
Mvaa 164,140,92
X 187
Mvaa 164,136,88
D 16,40,94,0
Ra 128,242,57344
Mvaa 168,136,104
s_add_u32 s95, s91, 5
s_and_b32 s95, s95, 15
s_mul_i32 s95, s95, 40960
Mvaa 168,140,108
s_mul_i32 s71, s51, 10240
s_add_u32 s71, s71, 20480
Ra 132,242,58368
Mvaa 172,140,124
X 188
Mvaa 172,136,120
D 17,40,94,1024
K 215,186,187
v_smfmac_f32_16x16x64_bf16 a[160:163], a[176:179], v[192:199], v240
X 189
v_smfmac_f32_16x16x64_bf16 a[164:167], a[176:179], v[200:207], v240
X 190
K 222,188,189
X 191
s_nop 0
K 223,190,191
v_smfmac_f32_16x16x64_bf16 a[168:171], a[176:179], v[208:215], v240
D 18,40,94,2048
Ra 136,242,59392
v_smfmac_f32_16x16x64_bf16 a[172:175], a[176:179], v[216:223], v240
W 3
X 160
Cvaa 176,144,64,224
X 161
Mvaa 176,148,68
X 162
Cvaa 180,148,84,228
D 19,40,94,3072
K 192,160,161
Mvaa 180,144,80
s_add_u32 m0, m0, 4096
X 163
Cvaa 184,144,96,232
X 164
Mvaa 184,148,100
Ra 140,242,60416
K 193,162,163
Cvaa 188,148,116,236
buffer_load_dwordx4 v20, s[40:43], s94 offen lds
Mvaa 188,144,112
s_add_u32 m0, s71, s48
s_lshl2_add_u32 s94, s48, s95
Ra 144,242,61440
Mvaa 176,152,72
X 165
Mvaa 176,156,76
X 166
Mvaa 180,156,92
D 16,40,94,0
K 200,164,165
Mvaa 180,152,88
X 167
Mvaa 184,152,104
X 172
Mvaa 184,156,108
D 17,40,94,1024
K 201,166,167
Mvaa 188,156,124
X 173
Mvaa 188,152,120
W 1
Ra 148,242,62464
Ra 152,242,63488
Mvav 32,128,196
X 174
Mvav 64,128,204
D 18,40,94,2048
K 216,172,173
Mvav 96,128,212
X 175
Mvav 128,128,220
X 168
Mvav 132,132,220
s_mov_b32 s18, 40960
D 19,40,94,3072
K 217,174,175
Mvav 100,132,212
s_add_u32 m0, m0, 4096
X 169
Mvav 68,132,204
s_mov_b32 s19, 73728
X 170
Mvav 36,132,196
s_bitcmp1_b32 s14, 0
s_cselect_b32 s18, 49152, s18
s_cselect_b32 s19, 98304, s19
Ra 156,242,64512
K 208,168,169
Mvav 40,136,196
s_cmp_lt_u32 s14, 2
s_cselect_b32 s20, s18, s19
s_cselect_b32 s21, s19, s18
X 171
Mvav 72,136,204
X 184
Mvav 104,136,212
buffer_load_dwordx4 v20, s[40:43], s94 offen lds
K 209,170,171
Mvav 136,136,220
s_waitcnt vmcnt(10)
v_cmp_eq_u64 vcc, s[12:13], v[28:29]
s_cmp_eq_u64 vcc, -1
s_cselect_b32 s46, 79691776, 0
s_add_u32 s81, s58, 0
s_and_b32 s81, s81, 3
Mvav 140,140,220
s_lshl_b32 s81, s81, 12
s_add_u32 s81, s81, s49
Ra 128,242,49152
Mvav 108,140,212
X 185
Mvav 76,140,204
X 186
Mvav 44,140,196
W 1
buffer_load_dwordx4 a[184:187], v1, s[44:47], s81 offen offset:0 sc1
K 210,184,185
Mvav 48,144,196
s_add_u32 s82, s81, 131072
X 187
Mvav 80,144,204
v_add_u32 v2, s20, v1
v_add_u32 v3, s21, v1
Mvav 112,144,212
s_lshl_b32 s18, s14, 9
X 188
Mvav 144,144,220
Ra 132,242,50176
K 211,186,187
Mvav 148,148,220
buffer_load_dwordx4 a[200:203], v1, s[44:47], s82 offen offset:0 sc1
Ra 136,242,51200
Mvav 116,148,212
X 189
Mvav 84,148,204
v_lshrrev_b32 v232, 1, v1
v_add_u32 v232, s18, v232
Mvav 52,148,196
X 190
Mvav 56,152,196
K 218,188,189
v_bfe_u32 v12, v1, 6, 2
Mvav 88,152,204
buffer_load_dwordx4 a[188:191], v1, s[44:47], s81 offen offset:1024 sc1
Ra 140,242,52224
Mvav 120,152,212
X 191
Mvav 152,152,220
v_lshrrev_b32 v13, 8, v1
v_bfe_u32 v14, v1, 4, 1
Mvav 156,156,220
X 176
Mvav 124,156,212
K 219,190,191
v_lshlrev_b32 v14, 4, v14
Mvav 92,156,204
X 177
Mvav 60,156,196
buffer_load_dwordx4 a[204:207], v1, s[44:47], s82 offen offset:1024 sc1
v_mov_b32 v15, 0x3f80
Maav 168,180,208
X 178
K 194,176,177
X 179
X 180
K 195,178,179
Maav 172,180,216
X 181
X 182
K 202,180,181
X 183
s_nop 0
K 203,182,183
Maav 160,180,192
Ra 144,242,53248
v_lshlrev_b32 v15, v14, v15
Maav 164,180,200
W 1
buffer_load_dwordx4 a[192:195], v1, s[44:47], s81 offen offset:2048 sc1
v_bfe_u32 v14, v1, 5, 1
Mvav 96,128,208
Ra 148,242,54272
v_xor_b32 v27, v12, v13
v_lshl_or_b32 v27, v27, 1, v14
Mvav 128,128,216
v_cmp_eq_u32_e64 s[86:87], 0, v27
v_mov_b32 v226, 0
s_nop 0
v_cndmask_b32_e64 v224, 0, v15, s[86:87]
Mvav 132,132,216
Ra 152,242,55296
Ra 156,242,56320
buffer_load_dwordx4 a[208:211], v1, s[44:47], s82 offen offset:2048 sc1
Mvav 100,132,208
v_mov_b32 v228, 0
v_mov_b32 v230, v224
v_cmp_eq_u32_e64 s[86:87], 1, v27
Mvav 104,136,208
v_mov_b32 v227, 0
Mvav 136,136,216
buffer_load_dwordx4 a[196:199], v1, s[44:47], s81 offen offset:3072 sc1
v_cndmask_b32_e64 v225, 0, v15, s[86:87]
v_mov_b32 v229, 0
Mvav 140,140,216
v_mov_b32 v231, v225
G 4,160
G 8,161
Mvav 108,140,208
W 0
G 5,164
G 9,165
G 6,168
Mvav 112,144,208
buffer_load_dwordx4 a[212:215], v1, s[44:47], s82 offen offset:3072 sc1
s_add_u32 s81, s58, 1
s_and_b32 s81, s81, 3
s_lshl_b32 s81, s81, 12
Mvav 144,144,216
s_add_u32 s81, s81, s49
G 10,169
G 7,172
G 11,173
Mvav 148,148,216
A 4,4,8
A 5,5,9
A 6,6,10
Mvav 116,148,208
buffer_load_dwordx4 a[216:219], v1, s[44:47], s81 offen offset:0 sc1
s_add_u32 s82, s81, 131072
A 7,7,11
ds_write_b64 v232, v[6:7] offset:57344
Mvav 120,152,208
A 8,4,5
A 9,6,7
A 8,8,9
Mvav 152,152,216
v_cmp_ngt_f32_e64 s[86:87], s90, v8
s_lshl_b32 s19, s14, 2
v_mov_b32 v9, s19
s_or_b64 s[88:89], s[88:89], s[86:87]
Mvav 156,156,216
s_and_b64 s[88:89], s[88:89], s[24:25]
s_cselect_b32 s18, 1, 0
buffer_load_dwordx4 a[232:235], v1, s[44:47], s82 offen offset:0 sc1
v_mov_b32 v8, s18
ds_write_b32 v9, v8 offset:59392
Mvav 124,156,208
v_xor_b32 v10, 1024, v232
v_bfe_u32 v11, v1, 2, 4
K 96,96,97
Mvav 32,128,192
K 97,98,99
K 98,100,101
K 99,102,103
Mvav 64,128,200
ds_write_b128 v2, v[96:99] offset:0
K 128,128,129
K 129,130,131
Mvav 68,132,200
K 130,132,133
K 131,134,135
ds_write_b128 v2, v[128:131] offset:4096
Mvav 36,132,192
buffer_load_dwordx4 a[220:223], v1, s[44:47], s81 offen offset:1024 sc1
K 104,104,105
K 105,106,107
Mvav 40,136,192
K 106,108,109
K 107,110,111
ds_write_b128 v2, v[104:107] offset:1024
Mvav 72,136,200
K 136,136,137
K 137,138,139
K 138,140,141
Mvav 76,140,200
buffer_load_dwordx4 a[236:239], v1, s[44:47], s82 offen offset:1024 sc1
K 139,142,143
ds_write_b128 v2, v[136:139] offset:5120
Mvav 44,140,192
K 112,112,113
K 113,114,115
K 114,116,117
Mvav 48,144,192
K 115,118,119
ds_write_b128 v2, v[112:115] offset:2048
K 144,144,145
Mvav 80,144,200
K 145,146,147
K 146,148,149
K 147,150,151
Mvav 84,148,200
ds_write_b128 v2, v[144:147] offset:6144
buffer_load_dwordx4 a[224:227], v1, s[44:47], s81 offen offset:2048 sc1
K 120,120,121
Mvav 52,148,192
K 121,122,123
K 122,124,125
K 123,126,127
Mvav 56,152,192
ds_write_b128 v2, v[120:123] offset:3072
K 152,152,153
K 153,154,155
Mvav 88,152,200
buffer_load_dwordx4 a[240:243], v1, s[44:47], s82 offen offset:2048 sc1
K 154,156,157
K 155,158,159
Mvav 92,156,200
ds_write_b128 v2, v[152:155] offset:7168
Mvav 60,156,192
buffer_load_dwordx4 a[228:231], v1, s[44:47], s81 offen offset:3072 sc1
buffer_load_dwordx4 a[244:247], v1, s[44:47], s82 offen offset:3072 sc1
s_branch .Lend_43%=
.p2align 12
.Lfs_42%=:
v_smfmac_f32_16x16x64_bf16 a[160:163], a[176:179], v[192:199], v240
s_mov_b32 s46, 0
s_lshl_b32 s92, s58, 2
s_add_u32 s91, s92, s51
s_add_u32 s95, s91, 3
s_and_b32 s95, s95, 15
s_mul_i32 s95, s95, 40960
s_mul_i32 s71, s51, 10240
v_smfmac_f32_16x16x64_bf16 a[164:167], a[176:179], v[200:207], v240
s_add_u32 s71, s71, 0
s_add_u32 m0, s71, s48
s_lshl2_add_u32 s94, s48, s95
Ra 144,242,45056
Ra 148,242,46080
Ra 152,242,47104
Ra 156,242,48128
v_smfmac_f32_16x16x64_bf16 a[168:171], a[176:179], v[208:215], v240
D 16,40,94,0
s_add_u32 s95, s91, 5
s_and_b32 s95, s95, 15
s_mul_i32 s95, s95, 40960
s_mul_i32 s71, s51, 10240
s_add_u32 s71, s71, 20480
v_smfmac_f32_16x16x64_bf16 a[172:175], a[176:179], v[216:223], v240
W 4
Cvaa 168,128,96,232
v_mov_b32 v192, 0
G 12,160
Mvaa 168,132,100
D 17,40,94,1024
G 4,161
A 12,12,4
)"
R"(v_cmp_nlt_f32_e64 s[86:87], v12, s90
v_rcp_f32 v12, v12
s_or_b64 s[88:89], s[88:89], s[86:87]
Cvaa 172,132,116,236
v_accvgpr_write_b32 a160, 0
v_accvgpr_write_b32 a161, 0
v_accvgpr_write_b32 a162, 0
v_accvgpr_write_b32 a163, 0
G 13,164
G 5,165
Mvaa 172,128,112
v_mov_b32 v193, 0
A 13,13,5
v_cmp_nlt_f32_e64 s[86:87], v13, s90
v_rcp_f32 v13, v13
s_or_b64 s[88:89], s[88:89], s[86:87]
v_accvgpr_write_b32 a164, 0
Mvaa 168,136,104
v_accvgpr_write_b32 a165, 0
v_accvgpr_write_b32 a166, 0
v_accvgpr_write_b32 a167, 0
U 32,32,12
U 33,33,12
U 34,34,12
Mvaa 168,140,108
v_mov_b32 v200, 0
U 35,35,12
U 36,36,12
U 37,37,12
U 38,38,12
U 39,39,12
Mvaa 172,140,124
Ra 128,242,16384
Ra 132,242,17408
K 32,32,33
K 33,34,35
K 34,36,37
K 35,38,39
s_add_u32 s93, s67, 0
Mvaa 172,136,120
Ra 136,242,18432
Ra 140,242,19456
D 18,40,94,2048
v_mov_b32 v201, 0
W 4
Cvaa 188,144,112,236
Ra 144,242,20480
v_cndmask_b32_e64 v168, v241, v168, s[72:73]
v_cndmask_b32_e64 v169, v241, v169, s[74:75]
v_cndmask_b32_e64 v170, v241, v170, s[76:77]
Mvaa 188,148,116
buffer_store_dwordx4 v[32:35], v1, s[8:11], s93 offen offset:0 sc1
U 40,40,12
U 41,41,12
U 42,42,12
U 43,43,12
U 44,44,12
Mvaa 188,152,120
U 45,45,12
U 46,46,12
U 47,47,12
K 40,40,41
K 41,42,43
K 42,44,45
Mvaa 188,156,124
W 1
K 43,46,47
buffer_store_dwordx4 v[40:43], v1, s[8:11], s93 offen offset:1024 sc1
U 48,48,12
U 49,49,12
U 50,50,12
U 51,51,12
Mvav 96,128,212
U 52,52,12
U 53,53,12
U 54,54,12
U 55,55,12
K 48,48,49
K 49,50,51
Mvav 128,128,220
K 50,52,53
K 51,54,55
buffer_store_dwordx4 v[48:51], v1, s[8:11], s93 offen offset:2048 sc1
U 56,56,12
U 57,57,12
U 58,58,12
Mvav 132,132,220
Ra 148,242,21504
Ra 152,242,22528
Ra 156,242,23552
U 59,59,12
U 60,60,12
U 61,61,12
Mvav 100,132,212
D 19,40,94,3072
s_add_u32 m0, m0, 4096
U 62,62,12
U 63,63,12
K 56,56,57
K 57,58,59
K 58,60,61
Mvav 104,136,212
K 59,62,63
v_cndmask_b32_e64 v171, v241, v171, s[78:79]
X 168
X 169
Mvav 136,136,220
buffer_store_dwordx4 v[56:59], v1, s[8:11], s93 offen offset:3072 sc1
Ra 128,242,49152
G 14,168
G 6,169
A 14,14,6
v_cmp_nlt_f32_e64 s[86:87], v14, s90
Mvav 140,140,220
v_rcp_f32 v14, v14
s_or_b64 s[88:89], s[88:89], s[86:87]
v_accvgpr_write_b32 a168, 0
v_accvgpr_write_b32 a169, 0
v_accvgpr_write_b32 a170, 0
v_accvgpr_write_b32 a171, 0
Mvav 108,140,212
W 1
G 15,172
G 7,173
A 15,15,7
v_cmp_nlt_f32_e64 s[86:87], v15, s90
v_rcp_f32 v15, v15
Mvav 112,144,212
s_or_b64 s[88:89], s[88:89], s[86:87]
buffer_load_dwordx4 v20, s[40:43], s94 offen lds
v_accvgpr_write_b32 a172, 0
v_accvgpr_write_b32 a173, 0
v_accvgpr_write_b32 a174, 0
v_accvgpr_write_b32 a175, 0
U 64,64,13
Mvav 144,144,220
s_add_u32 m0, s71, s48
s_lshl2_add_u32 s94, s48, s95
U 65,65,13
U 66,66,13
U 67,67,13
U 68,68,13
U 69,69,13
Mvav 148,148,220
U 70,70,13
U 71,71,13
K 64,64,65
K 65,66,67
K 66,68,69
K 67,70,71
s_add_u32 s93, s67, 131072
Mvav 116,148,212
buffer_store_dwordx4 v[64:67], v1, s[8:11], s93 offen offset:0 sc1
U 72,72,13
U 73,73,13
U 74,74,13
U 75,75,13
U 76,76,13
Mvav 120,152,212
Ra 132,242,50176
Ra 136,242,51200
Ra 140,242,52224
U 77,77,13
U 78,78,13
U 79,79,13
Mvav 152,152,220
K 72,72,73
K 73,74,75
K 74,76,77
K 75,78,79
buffer_store_dwordx4 v[72:75], v1, s[8:11], s93 offen offset:1024 sc1
U 80,80,13
Mvav 156,156,220
X 170
K 208,168,169
X 171
X 172
K 209,170,171
X 173
X 174
K 216,172,173
X 175
v_mov_b32 v194, 0
K 217,174,175
v_mov_b32 v195, 0
v_mov_b32 v202, 0
v_mov_b32 v203, 0
v_mov_b32 v210, 0
v_mov_b32 v211, 0
Mvav 124,156,212
v_cndmask_b32_e64 v188, v241, v188, s[72:73]
v_cndmask_b32_e64 v189, v241, v189, s[74:75]
v_cndmask_b32_e64 v190, v241, v190, s[76:77]
v_cndmask_b32_e64 v191, v241, v191, s[78:79]
X 188
X 189
X 190
K 218,188,189
X 191
s_nop 0
K 219,190,191
Maav 168,180,208
D 16,40,94,0
U 81,81,13
U 82,82,13
U 83,83,13
U 84,84,13
U 85,85,13
Maav 172,180,216
W 0
U 86,86,13
U 87,87,13
K 80,80,81
K 81,82,83
K 82,84,85
K 83,86,87
Zvav 32,128,192
buffer_store_dwordx4 v[80:83], v1, s[8:11], s93 offen offset:2048 sc1
U 88,88,13
U 89,89,13
U 90,90,13
U 91,91,13
U 92,92,13
Zvav 64,128,200
U 93,93,13
U 94,94,13
U 95,95,13
K 88,88,89
K 89,90,91
K 90,92,93
K 91,94,95
buffer_store_dwordx4 v[88:91], v1, s[8:11], s93 offen offset:3072 sc1
U 96,96,14
U 97,97,14
U 98,98,14
U 99,99,14
U 100,100,14
U 101,101,14
U 102,102,14
U 103,103,14
K 96,96,97
K 97,98,99
K 98,100,101
K 99,102,103
s_add_u32 s93, s67, 262144
buffer_store_dwordx4 v[96:99], v1, s[8:11], s93 offen offset:0 sc1
s_nop 1
Zvav 96,128,208
U 104,104,14
U 105,105,14
U 106,106,14
U 107,107,14
U 108,108,14
U 109,109,14
U 110,110,14
U 111,111,14
K 104,104,105
K 105,106,107
K 106,108,109
K 107,110,111
buffer_store_dwordx4 v[104:107], v1, s[8:11], s93 offen offset:1024 sc1
U 112,112,14
U 113,113,14
U 114,114,14
U 115,115,14
U 116,116,14
U 117,117,14
U 118,118,14
U 119,119,14
K 112,112,113
K 113,114,115
K 114,116,117
K 115,118,119
buffer_store_dwordx4 v[112:115], v1, s[8:11], s93 offen offset:2048 sc1
U 120,120,14
U 121,121,14
U 122,122,14
U 123,123,14
U 124,124,14
U 125,125,14
U 126,126,14
U 127,127,14
K 120,120,121
K 121,122,123
K 122,124,125
K 123,126,127
buffer_store_dwordx4 v[120:123], v1, s[8:11], s93 offen offset:3072 sc1
U 128,128,15
U 129,129,15
U 130,130,15
U 131,131,15
U 132,132,15
U 133,133,15
U 134,134,15
U 135,135,15
K 128,128,129
K 129,130,131
K 130,132,133
K 131,134,135
s_add_u32 s93, s67, 393216
buffer_store_dwordx4 v[128:131], v1, s[8:11], s93 offen offset:0 sc1
s_nop 1
Zvav 128,128,216
Ra 144,242,53248
U 136,136,15
U 137,137,15
U 138,138,15
U 139,139,15
U 140,140,15
Zvav 132,132,216
D 17,40,94,1024
U 141,141,15
U 142,142,15
U 143,143,15
K 136,136,137
K 137,138,139
Zvav 100,132,208
Ra 148,242,54272
Ra 152,242,55296
Ra 156,242,56320
K 138,140,141
K 139,142,143
Zvav 68,132,200
D 18,40,94,2048
Zvav 36,132,192
Zvav 40,136,192
D 19,40,94,3072
s_add_u32 m0, m0, 4096
Zvav 72,136,200
Zvav 104,136,208
buffer_store_dwordx4 v[136:139], v1, s[8:11], s93 offen offset:1024 sc1
U 144,144,15
U 145,145,15
U 146,146,15
U 147,147,15
U 148,148,15
Zvav 136,136,216
U 149,149,15
U 150,150,15
U 151,151,15
K 144,144,145
K 145,146,147
K 146,148,149
Zvav 140,140,216
K 147,150,151
buffer_store_dwordx4 v[144:147], v1, s[8:11], s93 offen offset:2048 sc1
U 152,152,15
U 153,153,15
U 154,154,15
U 155,155,15
Zvav 108,140,208
U 156,156,15
U 157,157,15
U 158,158,15
U 159,159,15
K 152,152,153
K 153,154,155
Zvav 76,140,200
buffer_load_dwordx4 v20, s[40:43], s94 offen lds
K 154,156,157
K 155,158,159
Zvav 44,140,192
W 0
Zvav 48,144,192
buffer_store_dwordx4 v[152:155], v1, s[8:11], s93 offen offset:3072 sc1
s_mov_b32 s18, 40960
s_mov_b32 s19, 73728
s_bitcmp1_b32 s14, 0
s_cselect_b32 s18, 49152, s18
s_cselect_b32 s19, 98304, s19
s_cmp_lt_u32 s14, 2
s_cselect_b32 s20, s18, s19
s_cselect_b32 s21, s19, s18
v_add_u32 v2, s20, v1
v_add_u32 v3, s21, v1
Zvav 80,144,200
s_lshl_b32 s18, s14, 9
v_lshrrev_b32 v232, 1, v1
v_add_u32 v232, s18, v232
v_bfe_u32 v12, v1, 6, 2
v_lshrrev_b32 v13, 8, v1
v_bfe_u32 v14, v1, 4, 1
v_lshlrev_b32 v14, 4, v14
Zvav 112,144,208
v_mov_b32 v15, 0x3f80
v_lshlrev_b32 v15, v14, v15
v_bfe_u32 v14, v1, 5, 1
v_xor_b32 v27, v12, v13
v_lshl_or_b32 v27, v27, 1, v14
v_cmp_eq_u32_e64 s[86:87], 0, v27
Zvav 144,144,216
v_mov_b32 v226, 0
Zvav 148,148,216
v_cndmask_b32_e64 v224, 0, v15, s[86:87]
v_mov_b32 v228, 0
v_mov_b32 v230, v224
v_cmp_eq_u32_e64 s[86:87], 1, v27
v_mov_b32 v227, 0
s_nop 0
v_cndmask_b32_e64 v225, 0, v15, s[86:87]
)"
R"(Zvav 116,148,208
v_mov_b32 v229, 0
v_mov_b32 v231, v225
G 4,160
G 8,161
G 5,164
G 9,165
Zvav 84,148,200
G 6,168
G 10,169
G 7,172
G 11,173
A 4,4,8
A 5,5,9
Zvav 52,148,192
A 6,6,10
A 7,7,11
ds_write_b64 v232, v[6:7] offset:57344
A 8,4,5
A 9,6,7
A 8,8,9
Zvav 56,152,192
v_cmp_ngt_f32_e64 s[86:87], s90, v8
s_lshl_b32 s19, s14, 2
v_mov_b32 v9, s19
s_or_b64 s[88:89], s[88:89], s[86:87]
s_and_b64 s[88:89], s[88:89], s[24:25]
s_cselect_b32 s18, 1, 0
v_mov_b32 v8, s18
ds_write_b32 v9, v8 offset:59392
Zvav 88,152,200
v_xor_b32 v10, 1024, v232
v_bfe_u32 v11, v1, 2, 4
K 96,96,97
K 97,98,99
K 98,100,101
K 99,102,103
Zvav 120,152,208
ds_write_b128 v2, v[96:99] offset:0
K 128,128,129
K 129,130,131
K 130,132,133
K 131,134,135
ds_write_b128 v2, v[128:131] offset:4096
Zvav 152,152,216
K 104,104,105
K 105,106,107
K 106,108,109
K 107,110,111
ds_write_b128 v2, v[104:107] offset:1024
K 136,136,137
Zvav 156,156,216
K 137,138,139
K 138,140,141
K 139,142,143
ds_write_b128 v2, v[136:139] offset:5120
K 112,112,113
K 113,114,115
Zvav 124,156,208
K 114,116,117
K 115,118,119
ds_write_b128 v2, v[112:115] offset:2048
K 144,144,145
K 145,146,147
K 146,148,149
Zvav 92,156,200
K 147,150,151
ds_write_b128 v2, v[144:147] offset:6144
Zvav 60,156,192
K 120,120,121
K 121,122,123
K 122,124,125
K 123,126,127
ds_write_b128 v2, v[120:123] offset:3072
K 152,152,153
K 153,154,155
K 154,156,157
K 155,158,159
ds_write_b128 v2, v[152:155] offset:7168
s_waitcnt vmcnt(0)
s_and_b64 s[86:87], s[88:89], s[24:25]
s_cbranch_scc1 .LL_50%=
v_mov_b64 v[28:29], s[12:13]
s_mov_b64 exec, 1
buffer_store_dwordx2 v[28:29], off, s[8:11], s69 sc1
s_mov_b64 exec, -1
.LL_50%=:
s_mov_b32 s70, 0
s_branch .Lend_43%=
.p2align 12
.Lpd_32%=:
v_pk_mul_f32 v[32:33], v[32:33], s[22:23]
v_pk_mul_f32 v[34:35], v[34:35], s[22:23]
v_pk_mul_f32 v[36:37], v[36:37], s[22:23]
v_pk_mul_f32 v[38:39], v[38:39], s[22:23]
v_pk_mul_f32 v[64:65], v[64:65], s[22:23]
v_pk_mul_f32 v[66:67], v[66:67], s[22:23]
v_pk_mul_f32 v[68:69], v[68:69], s[22:23]
v_pk_mul_f32 v[70:71], v[70:71], s[22:23]
v_pk_mul_f32 v[96:97], v[96:97], s[22:23]
v_pk_mul_f32 v[98:99], v[98:99], s[22:23]
v_pk_mul_f32 v[100:101], v[100:101], s[22:23]
v_pk_mul_f32 v[102:103], v[102:103], s[22:23]
v_pk_mul_f32 v[128:129], v[128:129], s[22:23]
v_pk_mul_f32 v[130:131], v[130:131], s[22:23]
v_pk_mul_f32 v[132:133], v[132:133], s[22:23]
v_pk_mul_f32 v[134:135], v[134:135], s[22:23]
v_pk_mul_f32 v[40:41], v[40:41], s[22:23]
v_pk_mul_f32 v[42:43], v[42:43], s[22:23]
v_pk_mul_f32 v[44:45], v[44:45], s[22:23]
v_pk_mul_f32 v[46:47], v[46:47], s[22:23]
v_pk_mul_f32 v[72:73], v[72:73], s[22:23]
v_pk_mul_f32 v[74:75], v[74:75], s[22:23]
v_pk_mul_f32 v[76:77], v[76:77], s[22:23]
v_pk_mul_f32 v[78:79], v[78:79], s[22:23]
v_pk_mul_f32 v[104:105], v[104:105], s[22:23]
v_pk_mul_f32 v[106:107], v[106:107], s[22:23]
v_pk_mul_f32 v[108:109], v[108:109], s[22:23]
v_pk_mul_f32 v[110:111], v[110:111], s[22:23]
v_pk_mul_f32 v[136:137], v[136:137], s[22:23]
v_pk_mul_f32 v[138:139], v[138:139], s[22:23]
v_pk_mul_f32 v[140:141], v[140:141], s[22:23]
v_pk_mul_f32 v[142:143], v[142:143], s[22:23]
v_pk_mul_f32 v[48:49], v[48:49], s[22:23]
v_pk_mul_f32 v[50:51], v[50:51], s[22:23]
v_pk_mul_f32 v[52:53], v[52:53], s[22:23]
v_pk_mul_f32 v[54:55], v[54:55], s[22:23]
v_pk_mul_f32 v[80:81], v[80:81], s[22:23]
v_pk_mul_f32 v[82:83], v[82:83], s[22:23]
v_pk_mul_f32 v[84:85], v[84:85], s[22:23]
v_pk_mul_f32 v[86:87], v[86:87], s[22:23]
v_pk_mul_f32 v[112:113], v[112:113], s[22:23]
v_pk_mul_f32 v[114:115], v[114:115], s[22:23]
v_pk_mul_f32 v[116:117], v[116:117], s[22:23]
v_pk_mul_f32 v[118:119], v[118:119], s[22:23]
v_pk_mul_f32 v[144:145], v[144:145], s[22:23]
v_pk_mul_f32 v[146:147], v[146:147], s[22:23]
v_pk_mul_f32 v[148:149], v[148:149], s[22:23]
v_pk_mul_f32 v[150:151], v[150:151], s[22:23]
v_pk_mul_f32 v[56:57], v[56:57], s[22:23]
v_pk_mul_f32 v[58:59], v[58:59], s[22:23]
v_pk_mul_f32 v[60:61], v[60:61], s[22:23]
v_pk_mul_f32 v[62:63], v[62:63], s[22:23]
v_pk_mul_f32 v[88:89], v[88:89], s[22:23]
v_pk_mul_f32 v[90:91], v[90:91], s[22:23]
v_pk_mul_f32 v[92:93], v[92:93], s[22:23]
v_pk_mul_f32 v[94:95], v[94:95], s[22:23]
v_pk_mul_f32 v[120:121], v[120:121], s[22:23]
v_pk_mul_f32 v[122:123], v[122:123], s[22:23]
v_pk_mul_f32 v[124:125], v[124:125], s[22:23]
v_pk_mul_f32 v[126:127], v[126:127], s[22:23]
v_pk_mul_f32 v[152:153], v[152:153], s[22:23]
v_pk_mul_f32 v[154:155], v[154:155], s[22:23]
v_pk_mul_f32 v[156:157], v[156:157], s[22:23]
v_pk_mul_f32 v[158:159], v[158:159], s[22:23]
K 32,32,33
K 33,34,35
K 34,36,37
K 35,38,39
K 40,40,41
K 41,42,43
K 42,44,45
K 43,46,47
W 0
Zvav 196,128,32
s_add_u32 s36, s29, 65536
Ra 144,242,4096
Ra 148,242,5120
Ra 152,242,6144
Ra 156,242,7168
Mvav 196,132,40
K 64,64,65
K 65,66,67
K 66,68,69
K 67,70,71
K 72,72,73
K 73,74,75
K 74,76,77
K 75,78,79
s_nop 1
Zvav 204,132,72
buffer_store_dwordx4 a[192:195], v1, s[8:11], s36 offen offset:0 sc1
K 96,96,97
K 97,98,99
K 98,100,101
Mvav 204,128,64
K 99,102,103
K 104,104,105
K 105,106,107
K 106,108,109
K 107,110,111
Zvav 212,128,96
buffer_store_dwordx4 a[200:203], v1, s[8:11], s36 offen offset:1024 sc1
K 128,128,129
K 129,130,131
K 130,132,133
Mvav 212,132,104
K 131,134,135
K 136,136,137
K 137,138,139
K 138,140,141
K 139,142,143
s_nop 1
Zvav 220,132,136
buffer_store_dwordx4 a[208:211], v1, s[8:11], s36 offen offset:2048 sc1
K 48,48,49
K 49,50,51
K 50,52,53
Mvav 220,128,128
K 51,54,55
K 56,56,57
K 57,58,59
K 58,60,61
K 59,62,63
Mvav 196,136,48
Ra 128,242,8192
Ra 132,242,9216
buffer_store_dwordx4 a[216:219], v1, s[8:11], s36 offen offset:3072 sc1
s_add_u32 s36, s29, 69632
K 80,80,81
Mvav 196,140,56
K 81,82,83
K 82,84,85
K 83,86,87
K 88,88,89
K 89,90,91
K 90,92,93
K 91,94,95
s_nop 1
Mvav 204,140,88
buffer_store_dwordx4 a[224:227], v1, s[8:11], s36 offen offset:0 sc1
K 112,112,113
K 113,114,115
K 114,116,117
Mvav 204,136,80
K 115,118,119
K 120,120,121
K 121,122,123
K 122,124,125
K 123,126,127
Mvav 212,136,112
K 144,144,145
K 145,146,147
K 146,148,149
K 147,150,151
Mvav 212,140,120
K 152,152,153
K 153,154,155
K 154,156,157
K 155,158,159
s_nop 1
Mvav 220,140,152
buffer_store_dwordx4 a[232:235], v1, s[8:11], s36 offen offset:1024 sc1
v_accvgpr_write_b32 a0, v32
v_accvgpr_write_b32 a1, v33
v_accvgpr_write_b32 a2, v34
Mvav 220,136,144
W 2
v_accvgpr_write_b32 a3, v35
v_accvgpr_write_b32 a4, v40
v_accvgpr_write_b32 a5, v41
v_accvgpr_write_b32 a6, v42
Zvav 68,144,64
buffer_store_dwordx4 a[240:243], v1, s[8:11], s36 offen offset:2048 sc1
v_accvgpr_write_b32 a7, v43
v_accvgpr_write_b32 a8, v48
v_accvgpr_write_b32 a9, v49
Mvav 68,148,72
Ra 136,242,10240
Ra 140,242,11264
v_accvgpr_write_b32 a10, v50
v_accvgpr_write_b32 a11, v51
Zvav 100,148,104
v_accvgpr_write_b32 a12, v56
v_accvgpr_write_b32 a13, v57
v_accvgpr_write_b32 a14, v58
v_accvgpr_write_b32 a15, v59
Mvav 100,144,96
buffer_store_dwordx4 a[248:251], v1, s[8:11], s36 offen offset:3072 sc1
s_add_u32 s36, s29, 393216
v_accvgpr_write_b32 a16, v64
v_cndmask_b32_e64 v196, v241, v196, s[72:73]
v_cndmask_b32_e64 v197, v241, v197, s[74:75]
Zvav 132,144,128
v_cndmask_b32_e64 v198, v241, v198, s[76:77]
v_cndmask_b32_e64 v199, v241, v199, s[78:79]
v_max3_f32 v4, v196, v197, v198
v_max3_f32 v4, v4, v199, v252
Mvav 132,148,136
v_max3_f32 v5, v204, v205, v206
v_max3_f32 v5, v5, v207, v253
v_max3_f32 v6, v212, v213, v214
v_max3_f32 v6, v6, v215, v254
Mvav 68,152,80
v_max3_f32 v7, v220, v221, v222
v_max3_f32 v7, v7, v223, v255
v_mov_b32 v8, v4
v_mov_b32 v9, v5
Mvav 68,156,88
v_mov_b32 v10, v6
v_mov_b32 v11, v7
v_permlane32_swap_b32 v4, v8
v_accvgpr_write_b32 a17, v65
Mvav 100,156,120
v_permlane32_swap_b32 v5, v9
v_permlane32_swap_b32 v6, v10
v_permlane32_swap_b32 v7, v11
v_max_f32 v4, v4, v8
Mvav 100,152,112
v_max_f32 v5, v5, v9
v_max_f32 v6, v6, v10
v_max_f32 v7, v7, v11
v_mov_b32 v8, v4
Mvav 132,152,144
v_mov_b32 v9, v5
v_mov_b32 v10, v6
v_mov_b32 v11, v7
v_permlane16_swap_b32 v4, v8
v_permlane16_swap_b32 v5, v9
v_permlane16_swap_b32 v6, v10
v_permlane16_swap_b32 v7, v11
v_max_f32 v4, v4, v8
v_max_f32 v5, v5, v9
v_max_f32 v6, v6, v10
v_max_f32 v7, v7, v11
v_sub_f32 v4, 0, v4
v_sub_f32 v5, 0, v5
v_sub_f32 v6, 0, v6
v_sub_f32 v7, 0, v7
v_mov_b32 v224, v4
v_mov_b32 v225, v4
v_mov_b32 v226, v4
v_mov_b32 v227, v4
v_mov_b32 v228, v5
v_mov_b32 v229, v5
v_mov_b32 v230, v5
v_mov_b32 v231, v5
v_mov_b32 v232, v6
v_mov_b32 v233, v6
v_mov_b32 v234, v6
v_mov_b32 v235, v6
v_mov_b32 v236, v7
v_mov_b32 v237, v7
v_mov_b32 v238, v7
v_mov_b32 v239, v7
Mvav 132,156,152
W 0
v_accvgpr_write_b32 a18, v66
v_accvgpr_write_b32 a19, v67
v_accvgpr_write_b32 a20, v72
v_accvgpr_write_b32 a21, v73
Cvav 168,128,96,232
s_and_b32 s80, s14, 1
buffer_store_dwordx4 a[196:199], v1, s[8:11], s36 offen offset:0 sc1
v_accvgpr_write_b32 a22, v74
v_accvgpr_write_b32 a23, v75
v_accvgpr_write_b32 a24, v80
Mvav 168,132,104
s_lshl_b32 s80, s80, 14
Ra 144,242,12288
v_accvgpr_write_b32 a25, v81
v_accvgpr_write_b32 a26, v82
v_accvgpr_write_b32 a27, v83
Cvav 172,132,136,236
Ra 148,242,13312
Ra 152,242,14336
Ra 156,242,15360
v_accvgpr_write_b32 a28, v88
Mvav 172,128,128
s_add_u32 s80, s80, 98304
v_accvgpr_write_b32 a29, v89
v_accvgpr_write_b32 a30, v90
v_accvgpr_write_b32 a31, v91
v_accvgpr_write_b32 a32, v96
Mvav 168,136,112
buffer_store_dwordx4 a[204:207], v1, s[8:11], s36 offen offset:1024 sc1
v_accvgpr_write_b32 a33, v97
v_accvgpr_write_b32 a34, v98
v_accvgpr_write_b32 a35, v99
Mvav 168,140,120
v_accvgpr_write_b32 a36, v104
v_accvgpr_write_b32 a37, v105
v_accvgpr_write_b32 a38, v106
v_accvgpr_write_b32 a39, v107
Mvav 172,140,152
Ra 128,242,24576
Ra 132,242,25600
buffer_store_dwordx4 a[212:215], v1, s[8:11], s36 offen offset:2048 sc1
v_accvgpr_write_b32 a40, v112
Mvav 172,136,144
Ra 136,242,26624
Ra 140,242,27648
v_accvgpr_write_b32 a41, v113
W 4
Cvav 188,144,128,236
v_accvgpr_write_b32 a42, v114
v_accvgpr_write_b32 a43, v115
v_accvgpr_write_b32 a44, v120
v_accvgpr_write_b32 a45, v121
v_accvgpr_write_b32 a46, v122
)"
R"(v_accvgpr_write_b32 a47, v123
v_accvgpr_write_b32 a48, v128
v_accvgpr_write_b32 a49, v129
v_accvgpr_write_b32 a50, v130
v_accvgpr_write_b32 a51, v131
v_accvgpr_write_b32 a52, v136
v_accvgpr_write_b32 a53, v137
v_accvgpr_write_b32 a54, v138
v_accvgpr_write_b32 a55, v139
v_accvgpr_write_b32 a56, v144
v_accvgpr_write_b32 a57, v145
v_accvgpr_write_b32 a58, v146
v_accvgpr_write_b32 a59, v147
v_accvgpr_write_b32 a60, v152
v_accvgpr_write_b32 a61, v153
v_accvgpr_write_b32 a62, v154
v_accvgpr_write_b32 a63, v155
Mvav 188,148,136
s_barrier
buffer_store_dwordx4 a[220:223], v1, s[8:11], s36 offen offset:3072 sc1
s_add_u32 s36, s29, 397312
buffer_store_dwordx4 a[228:231], v1, s[8:11], s36 offen offset:0 sc1
buffer_store_dwordx4 a[236:239], v1, s[8:11], s36 offen offset:1024 sc1
buffer_store_dwordx4 a[244:247], v1, s[8:11], s36 offen offset:2048 sc1
buffer_store_dwordx4 a[252:255], v1, s[8:11], s36 offen offset:3072 sc1
v_add_u32 v8, s80, v1
Mvav 188,152,144
A 196,196,224
A 197,197,224
A 198,198,224
A 199,199,224
X 196
X 197
X 198
K 192,196,197
X 199
A 204,204,228
K 193,198,199
A 205,205,228
A 206,206,228
A 207,207,228
X 204
X 205
X 206
K 200,204,205
X 207
A 212,212,232
K 201,206,207
A 213,213,232
A 214,214,232
A 215,215,232
X 212
X 213
X 214
K 208,212,213
X 215
A 220,220,236
K 209,214,215
A 221,221,236
A 222,222,236
A 223,223,236
X 220
X 221
X 222
K 216,220,221
X 223
v_mov_b32 v194, 0
K 217,222,223
v_mov_b32 v195, 0
v_cndmask_b32_e64 v68, v241, v68, s[72:73]
v_cndmask_b32_e64 v69, v241, v69, s[74:75]
v_cndmask_b32_e64 v70, v241, v70, s[76:77]
v_cndmask_b32_e64 v71, v241, v71, s[78:79]
A 68,68,228
A 69,69,228
A 70,70,228
A 71,71,228
X 68
X 69
X 70
K 202,68,69
X 71
A 100,100,232
K 203,70,71
A 101,101,232
A 102,102,232
A 103,103,232
X 100
X 101
X 102
K 210,100,101
X 103
A 132,132,236
K 211,102,103
A 133,133,236
A 134,134,236
A 135,135,236
X 132
X 133
X 134
K 218,132,133
X 135
s_nop 0
K 219,134,135
Mvav 188,156,152
W 0
Ra 144,242,28672
Ra 148,242,29696
Ra 152,242,30720
Zvav 32,128,192
Ra 156,242,31744
Zvav 64,128,200
Zvav 96,128,208
s_and_b32 s80, s14, 1
s_xor_b32 s62, s62, 0x10000
Zvav 128,128,216
s_lshl_b32 s80, s80, 14
s_add_u32 s80, s80, 131072
v_add_u32 v23, 61440, v21
Zvav 132,132,216
v_add_u32 v12, s80, v1
Ra 64,12,8192
Ra 68,12,9216
v_add_u32 v24, 61440, v22
Zvav 100,132,208
Ra 72,12,10240
Ra 76,12,11264
Ra 80,12,12288
v_add_u32 v25, 122880, v21
Zvav 68,132,200
Ra 84,12,13312
Ra 88,12,14336
Ra 92,12,15360
v_add_u32 v26, 122880, v22
Zvav 36,132,192
Ra 96,12,0
Ra 100,12,1024
Ra 104,12,2048
Zvav 40,136,192
Ra 108,12,3072
Ra 112,12,4096
Ra 116,12,5120
Zvav 72,136,200
Ra 120,12,6144
Ra 124,12,7168
Zvav 104,136,208
Zvav 136,136,216
Zvav 140,140,216
Zvav 108,140,208
Zvav 76,140,200
s_waitcnt vmcnt(0) lgkmcnt(0)
s_barrier
v_xor_b32 v242, 0x10000, v242
ds_read_b128 v[248:251], v8
Zvav 44,140,192
s_add_u32 s80, s71, 128
v_mov_b64 v[28:29], s[12:13]
s_mov_b64 exec, 1
buffer_store_dwordx2 v[28:29], off, s[8:11], s80 sc1
s_mov_b64 exec, -1
Ra 128,242,40960
Zvav 48,144,192
Zvav 80,144,200
X 172
X 173
Zvav 112,144,208
Ra 132,242,41984
X 174
K 220,172,173
Zvav 144,144,216
X 175
v_cndmask_b32_e64 v168, v241, v168, s[72:73]
K 221,174,175
Zvav 148,148,216
v_cndmask_b32_e64 v169, v241, v169, s[74:75]
v_cndmask_b32_e64 v170, v241, v170, s[76:77]
v_cndmask_b32_e64 v171, v241, v171, s[78:79]
Zvav 116,148,208
X 168
X 169
Zvav 84,148,200
W 2
Ra 136,242,43008
X 170
K 212,168,169
Zvav 52,148,192
v_mov_b32 v224, v250
v_mov_b32 v225, v250
v_mov_b32 v226, v250
v_mov_b32 v227, v250
Zvav 56,152,192
v_mov_b32 v228, v251
v_mov_b32 v229, v251
v_mov_b32 v230, v251
v_mov_b32 v231, v251
Zvav 88,152,200
v_mov_b32 v232, v248
v_mov_b32 v233, v248
v_mov_b32 v234, v248
v_mov_b32 v235, v248
Zvav 120,152,208
v_mov_b32 v236, v249
v_mov_b32 v237, v249
v_mov_b32 v238, v249
v_mov_b32 v239, v249
Zvav 152,152,216
X 171
v_mov_b32 v204, 0
K 213,170,171
Zvav 156,156,216
Ra 140,242,44032
v_mov_b32 v205, 0
v_mov_b32 v196, 0
v_mov_b32 v197, 0
Zvav 124,156,208
v_mov_b32 v198, 0
v_mov_b32 v199, 0
v_mov_b32 v206, 0
v_mov_b32 v207, 0
Zvav 92,156,200
v_mov_b32 v214, 0
v_mov_b32 v215, 0
v_cndmask_b32_e64 v188, v241, v188, s[72:73]
v_cndmask_b32_e64 v189, v241, v189, s[74:75]
Zvav 60,156,192
v_cndmask_b32_e64 v190, v241, v190, s[76:77]
v_cndmask_b32_e64 v191, v241, v191, s[78:79]
X 188
X 189
X 190
K 222,188,189
X 191
s_nop 0
K 223,190,191
s_nop 1
s_branch .Lsp2_37%=
.p2align 12
.Lmoe_44%=:
W 0
Cvaa 160,128,0,224
s_cmp_eq_u32 s14, 0
s_cselect_b32 s20, s30, s32
s_cselect_b32 s21, s31, s33
Ra 144,242,4096
Ra 148,242,5120
Mvaa 160,132,4
s_xor_b32 s62, s62, 0x10000
Ra 152,242,6144
Cvaa 164,132,20,228
X 184
Mvaa 164,128,16
s_and_b32 s21, s21, 0xffff
X 185
Cvaa 168,128,32,232
X 186
Mvaa 168,132,36
Ra 156,242,7168
K 214,184,185
Cvaa 172,132,52,236
s_mov_b32 s22, 2211840
X 187
Mvaa 172,128,48
X 188
Mvaa 160,136,8
Ra 128,242,16384
Mvaa 160,140,12
s_mov_b32 s23, 0x20000
K 215,186,187
Mvaa 164,140,28
X 189
Mvaa 164,136,24
X 190
Mvaa 168,136,40
Ra 132,242,17408
v_lshrrev_b32 v4, 6, v1
Mvaa 168,140,44
K 222,188,189
Mvaa 172,140,60
X 191
Mvaa 172,136,56
v_mul_u32_u24 v4, 3456, v4
K 223,190,191
v_smfmac_f32_16x16x64_bf16 a[160:163], a[176:179], v[192:199], v240
Ra 136,242,18432
v_smfmac_f32_16x16x64_bf16 a[164:167], a[176:179], v[200:207], v240
X 160
v_smfmac_f32_16x16x64_bf16 a[168:171], a[176:179], v[208:215], v240
v_lshrrev_b32 v5, 4, v1
v_smfmac_f32_16x16x64_bf16 a[172:175], a[176:179], v[216:223], v240
W 3
X 161
Cvaa 176,144,0,224
X 162
Mvaa 176,148,4
Ra 140,242,19456
v_xor_b32 v5, v5, v1
Cvaa 180,148,20,228
K 192,160,161
Mvaa 180,144,16
X 163
Cvaa 184,144,32,232
v_and_b32 v5, 32, v5
Mvaa 184,148,36
X 164
Cvaa 188,148,52,236
K 193,162,163
v_and_b32 v6, 16, v1
Mvaa 188,144,48
X 165
Mvaa 176,152,8
X 166
Mvaa 176,156,12
Ra 144,242,20480
Ra 148,242,21504
Mvaa 180,156,28
v_add3_u32 v4, v4, v5, v6
K 200,164,165
Mvaa 180,152,24
X 167
Mvaa 184,152,40
X 168
Mvaa 184,156,44
v_add_u32 v5, 54272, v4
K 201,166,167
Mvaa 188,156,60
X 169
Mvaa 188,152,56
W 2
Ra 152,242,22528
v_add_u32 v6, 108544, v4
Mvav 32,128,196
X 170
Mvav 64,128,204
Ra 156,242,23552
v_add_u32 v7, 162816, v4
Mvav 96,128,212
K 208,168,169
Mvav 128,128,220
X 171
Mvav 132,132,220
s_lshl_b32 s94, s85, 6
X 172
Mvav 100,132,212
s_add_u32 s64, s64, 1
Ra 128,242,8192
K 209,170,171
Mvav 68,132,204
X 173
Mvav 36,132,196
s_mul_i32 s95, s14, 20480
Mvav 40,136,196
X 174
Mvav 72,136,204
Ra 132,242,9216
K 216,172,173
Mvav 104,136,212
s_add_u32 s95, s95, 98304
X 175
Mvav 136,136,220
Mvav 140,140,220
s_add_u32 m0, s95, 0
X 176
Mvav 108,140,212
Ra 136,242,10240
K 217,174,175
Mvav 76,140,204
X 177
Mvav 44,140,196
W 3
D 4,20,94,0
Mvav 48,144,196
X 178
Mvav 80,144,204
Ra 140,242,11264
Mvav 112,144,212
D 5,20,94,1024
K 194,176,177
Mvav 144,144,220
X 179
Mvav 148,148,220
X 180
Mvav 116,148,212
Ra 144,242,12288
D 6,20,94,2048
Mvav 84,148,204
K 195,178,179
Mvav 52,148,196
X 181
Mvav 56,152,196
D 7,20,94,3072
Ra 148,242,13312
Mvav 88,152,204
X 182
Mvav 120,152,212
s_add_u32 s94, s94, 221184
K 202,180,181
Mvav 152,152,220
X 183
Mvav 156,156,220
X 184
)"
R"(Mvav 124,156,212
s_add_u32 m0, s95, 4096
Ra 152,242,14336
Mvav 92,156,204
K 203,182,183
Mvav 60,156,196
W 3
X 185
Cvaa 160,128,0,224
D 4,20,94,0
Ra 156,242,15360
Mvaa 160,132,4
X 186
Cvaa 164,132,20,228
K 210,184,185
Mvaa 164,128,16
X 187
Cvaa 168,128,32,232
D 5,20,94,1024
Mvaa 168,132,36
X 188
Cvaa 172,132,52,236
D 6,20,94,2048
K 211,186,187
Mvaa 172,128,48
X 189
Mvaa 160,136,8
Ra 128,242,24576
Ra 132,242,25600
Mvaa 160,140,12
X 190
Mvaa 164,140,28
D 7,20,94,3072
K 218,188,189
Mvaa 164,136,24
s_add_u32 s94, s94, 221184
X 191
Mvaa 168,136,40
Mvaa 168,140,44
X 160
Mvaa 172,140,60
s_add_u32 m0, s95, 8192
K 219,190,191
Mvaa 172,136,56
W 2
X 161
Cvaa 176,144,0,224
Ra 136,242,26624
Ra 140,242,27648
Mvaa 176,148,4
X 162
Cvaa 180,148,20,228
D 4,20,94,0
K 196,160,161
Mvaa 180,144,16
X 163
Cvaa 184,144,32,232
D 5,20,94,1024
Mvaa 184,148,36
X 164
Cvaa 188,148,52,236
K 197,162,163
Mvaa 188,144,48
D 6,20,94,2048
Ra 144,242,28672
Mvaa 176,152,8
X 165
Mvaa 176,156,12
Ra 148,242,29696
D 7,20,94,3072
Mvaa 180,156,28
X 166
Mvaa 180,152,24
K 204,164,165
Mvaa 184,152,40
s_add_u32 s94, s94, 221184
X 167
Mvaa 184,156,44
X 168
Mvaa 188,156,60
Mvaa 188,152,56
W 2
s_add_u32 m0, s95, 12288
K 205,166,167
Mvav 32,128,192
X 169
Mvav 64,128,200
Ra 152,242,30720
Ra 156,242,31744
Mvav 96,128,208
X 170
Mvav 128,128,216
D 4,20,94,0
K 212,168,169
Mvav 132,132,216
X 171
Mvav 100,132,208
D 5,20,94,1024
Mvav 68,132,200
X 172
Mvav 36,132,192
K 213,170,171
Mvav 40,136,192
X 173
Mvav 72,136,200
D 6,20,94,2048
Mvav 104,136,208
X 174
Mvav 136,136,216
D 7,20,94,3072
K 220,172,173
Mvav 140,140,216
X 175
Mvav 108,140,208
s_add_u32 s94, s94, 221184
X 176
Mvav 76,140,200
W 0
s_barrier
v_xor_b32 v242, 0x10000, v242
Mvav 44,140,192
Ra 128,242,0
K 221,174,175
Mvav 48,144,192
X 177
Mvav 80,144,200
v_bfe_u32 v11, v1, 4, 3
Mvav 112,144,208
X 178
Mvav 144,144,216
Ra 132,242,1024
v_lshlrev_b32 v11, 3, v11
Mvav 148,148,216
K 198,176,177
Mvav 116,148,208
X 179
Mvav 84,148,200
X 180
Mvav 52,148,192
Ra 136,242,2048
buffer_load_dwordx2 v[8:9], v11, s[8:11], 0 offen offset:3840 sc1
Mvav 56,152,192
Ra 140,242,3072
K 199,178,179
Mvav 88,152,200
X 181
Mvav 120,152,208
X 182
Mvav 152,152,216
K 206,180,181
Mvav 156,156,216
X 183
Mvav 124,156,208
Mvav 92,156,200
K 207,182,183
Mvav 60,156,192
W 0
Cvaa 160,128,0,224
s_add_u32 m0, s95, 16384
Ra 144,242,4096
Ra 148,242,5120
Mvaa 160,132,4
s_xor_b32 s62, s62, 0x10000
Ra 152,242,6144
Cvaa 164,132,20,228
X 184
Mvaa 164,128,16
X 185
Cvaa 168,128,32,232
X 186
Mvaa 168,132,36
D 4,20,94,0
K 214,184,185
Cvaa 172,132,52,236
Ra 156,242,7168
Mvaa 172,128,48
X 187
Mvaa 160,136,8
X 188
Mvaa 160,140,12
D 5,20,94,1024
Ra 128,242,16384
Mvaa 164,140,28
K 215,186,187
Mvaa 164,136,24
X 189
Mvaa 168,136,40
X 190
Mvaa 168,140,44
D 6,20,94,2048
Ra 132,242,17408
Mvaa 172,140,60
K 222,188,189
Mvaa 172,136,56
X 191
v_smfmac_f32_16x16x64_bf16 a[160:163], a[176:179], v[192:199], v240
Ra 136,242,18432
K 223,190,191
v_smfmac_f32_16x16x64_bf16 a[164:167], a[176:179], v[200:207], v240
D 7,20,94,3072
v_smfmac_f32_16x16x64_bf16 a[168:171], a[176:179], v[208:215], v240
s_add_u32 s94, s94, 221184
v_mul_u32_u24 v10, 160, v11
v_smfmac_f32_16x16x64_bf16 a[172:175], a[176:179], v[216:223], v240
W 3
X 160
Cvaa 176,144,0,224
X 161
Mvaa 176,148,4
Ra 140,242,19456
v_lshrrev_b32 v12, 7, v1
Cvaa 180,148,20,228
X 162
Mvaa 180,144,16
K 192,160,161
v_lshl_add_u32 v10, v12, 4, v10
Cvaa 184,144,32,232
s_mul_i32 s82, s14, 5120
X 163
Mvaa 184,148,36
X 164
Cvaa 188,148,52,236
K 193,162,163
Mvaa 188,144,48
s_mul_i32 s81, s14, 640
X 165
Mvaa 176,152,8
X 166
Mvaa 176,156,12
s_add_u32 s81, s81, 118784
Ra 144,242,20480
Ra 148,242,21504
Mvaa 180,156,28
K 200,164,165
Mvaa 180,152,24
X 167
Mvaa 184,152,40
s_waitcnt vmcnt(4)
v_cmp_eq_u64 vcc, s[12:13], v[8:9]
s_cmp_eq_u64 vcc, -1
s_cbranch_scc0 .Lspin_51%=
.Lok_52%=:
X 168
Mvaa 184,156,44
K 201,166,167
Mvaa 188,156,60
s_add_u32 m0, s82, 53248
X 169
Mvaa 188,152,56
W 2
Ra 152,242,22528
Mvav 32,128,196
X 170
Mvav 64,128,204
Ra 156,242,23552
Dc 10,8,81,0
Mvav 96,128,212
K 208,168,169
Mvav 128,128,220
X 171
Mvav 132,132,220
s_add_u32 m0, s82, 54144
X 172
Mvav 100,132,212
s_add_u32 s64, s64, 1
Ra 128,242,8192
K 209,170,171
Mvav 68,132,204
X 173
Mvav 36,132,196
Dc 10,8,81,128
Mvav 40,136,196
X 174
Mvav 72,136,204
s_add_u32 m0, s82, 55040
Ra 132,242,9216
K 216,172,173
Mvav 104,136,212
X 175
Mvav 136,136,220
Dc 10,8,81,256
Mvav 140,140,220
X 176
Mvav 108,140,212
Ra 136,242,10240
K 217,174,175
Mvav 76,140,204
s_add_u32 m0, s82, 55936
X 177
Mvav 44,140,196
W 3
Mvav 48,144,196
X 178
Mvav 80,144,204
Ra 140,242,11264
Dc 10,8,81,384
Mvav 112,144,212
K 194,176,177
Mvav 144,144,220
s_add_u32 m0, s82, 56832
X 179
Mvav 148,148,220
X 180
Mvav 116,148,212
Ra 144,242,12288
Mvav 84,148,204
Dc 10,8,81,512
K 195,178,179
Mvav 52,148,196
X 181
Mvav 56,152,196
X 182
Mvav 88,152,204
Ra 148,242,13312
Mvav 120,152,212
K 202,180,181
Mvav 152,152,220
X 183
Mvav 156,156,220
X 184
Mvav 124,156,212
Ra 152,242,14336
Mvav 92,156,204
K 203,182,183
Mvav 60,156,196
W 3
X 185
Cvaa 160,128,0,224
X 186
Mvaa 160,132,4
Ra 156,242,15360
Cvaa 164,132,20,228
K 210,184,185
Mvaa 164,128,16
X 187
Cvaa 168,128,32,232
X 188
Mvaa 168,132,36
Cvaa 172,132,52,236
K 211,186,187
Mvaa 172,128,48
X 189
Mvaa 160,136,8
Ra 128,242,24576
Ra 132,242,25600
Mvaa 160,140,12
X 190
Mvaa 164,140,28
K 218,188,189
Mvaa 164,136,24
X 191
Mvaa 168,136,40
Mvaa 168,140,44
Mvaa 172,140,60
X 160
Mvaa 172,136,56
W 2
Ra 136,242,26624
K 219,190,191
Cvaa 176,144,0,224
X 161
Mvaa 176,148,4
Ra 140,242,27648
v_lshrrev_b32 v8, 8, v1
Cvaa 180,148,20,228
X 162
Mvaa 180,144,16
K 196,160,161
Cvaa 184,144,32,232
X 163
Mvaa 184,148,36
v_bfe_u32 v9, v1, 6, 2
Cvaa 188,148,52,236
X 164
Mvaa 188,144,48
K 197,162,163
v_lshl_add_u32 v9, v8, 3, v9
Mvaa 176,152,8
X 165
Mvaa 176,156,12
Ra 144,242,28672
Ra 148,242,29696
Mvaa 180,156,28
X 166
Mvaa 180,152,24
K 204,164,165
v_lshlrev_b32 v11, 6, v9
Mvaa 184,152,40
X 167
Mvaa 184,156,44
X 168
Mvaa 188,156,60
v_xor_b32 v9, s14, v8
Mvaa 188,152,56
W 2
Ra 152,242,30720
K 205,166,167
Mvav 32,128,192
X 169
Mvav 64,128,200
)"
R"(Ra 156,242,31744
v_and_b32 v9, 1, v9
Mvav 96,128,208
X 170
Mvav 128,128,216
K 212,168,169
Mvav 132,132,216
X 171
Mvav 100,132,208
v_lshl_add_u32 v11, v9, 5, v11
Mvav 68,132,200
X 172
Mvav 36,132,192
K 213,170,171
Mvav 40,136,192
X 173
Mvav 72,136,200
v_bfe_u32 v9, v1, 4, 2
Mvav 104,136,208
X 174
Mvav 136,136,216
K 220,172,173
v_lshl_add_u32 v12, v9, 3, v11
Mvav 140,140,216
X 175
Mvav 108,140,208
X 176
Mvav 76,140,200
s_waitcnt vmcnt(0) lgkmcnt(0)
s_barrier
v_xor_b32 v242, 0x10000, v242
Mvav 44,140,192
v_add_u32 v11, 98304, v12
K 221,174,175
Mvav 48,144,192
Ra 128,242,0
Mvav 80,144,200
X 177
Mvav 112,144,208
X 178
Mvav 144,144,216
Ra 132,242,1024
v_and_b32 v13, 0x70, v1
Mvav 148,148,216
K 198,176,177
Mvav 116,148,208
X 179
Mvav 84,148,200
Ra 136,242,2048
v_lshl_add_u32 v13, v8, 7, v13
Mvav 52,148,192
X 180
Mvav 56,152,192
Ra 140,242,3072
K 199,178,179
Mvav 88,152,200
X 181
Mvav 120,152,208
X 182
Mvav 152,152,216
K 206,180,181
Mvav 156,156,216
X 183
Mvav 124,156,208
Mvav 92,156,200
K 207,182,183
Mvav 60,156,192
W 0
Cvaa 160,128,0,224
s_cmp_eq_u32 s14, 0
s_cselect_b32 s95, 106496, 0
Ra 144,242,4096
Ra 148,242,5120
Mvaa 160,132,4
s_xor_b32 s62, s62, 0x10000
X 184
Cvaa 164,132,20,228
Ta 184,11,0
Ta 186,11,256
Mvaa 164,128,16
s_add_u32 s95, s95, 32768
Ta 188,11,20480
Ta 190,11,20736
Cvaa 168,128,32,232
Ra 192,13,53248
Ta 196,11,2048
Mvaa 168,132,36
s_add_u32 m0, s95, 0
X 185
Cvaa 172,132,52,236
Ta 198,11,2304
Ta 200,11,22528
Mvaa 172,128,48
W 3
X 186
Zaaa 232,184,192
Ta 202,11,22784
K 214,184,185
Zaaa 236,188,192
Ra 204,13,53760
Ra 152,242,6144
Mvaa 160,136,8
X 187
Mvaa 160,140,12
Ta 208,11,4096
D 4,20,94,0
Mvaa 164,140,28
X 188
Mvaa 164,136,24
Ta 210,11,4352
D 5,20,94,1024
Mvaa 168,136,40
Ra 156,242,7168
K 215,186,187
Mvaa 168,140,44
X 189
Mvaa 172,140,60
Ta 212,11,24576
D 6,20,94,2048
Mvaa 172,136,56
X 190
v_smfmac_f32_16x16x64_bf16 a[160:163], a[176:179], v[192:199], v240
W 5
Ta 214,11,24832
D 7,20,94,3072
Maaa 232,196,204
s_add_u32 s94, s94, 221184
Ra 216,13,54272
K 222,188,189
Maaa 236,200,204
s_add_u32 m0, s95, 4096
X 191
v_smfmac_f32_16x16x64_bf16 a[164:167], a[176:179], v[200:207], v240
X 160
K 223,190,191
v_smfmac_f32_16x16x64_bf16 a[168:171], a[176:179], v[208:215], v240
Ra 128,242,16384
Ta 220,11,6144
v_smfmac_f32_16x16x64_bf16 a[172:175], a[176:179], v[216:223], v240
W 5
Ta 222,11,6400
Ra 132,242,17408
Cvaa 176,144,0,224
X 161
Mvaa 176,148,4
D 4,20,94,0
Ta 224,11,26624
Cvaa 180,148,20,228
X 162
Mvaa 180,144,16
D 5,20,94,1024
K 192,160,161
Cvaa 184,144,32,232
Ta 226,11,26880
Ra 228,13,54784
Mvaa 184,148,36
W 7
X 163
Maaa 232,208,216
D 6,20,94,2048
Ra 136,242,18432
Maaa 236,212,216
X 164
Cvaa 188,148,52,236
D 7,20,94,3072
Ta 184,11,8192
Mvaa 188,144,48
s_add_u32 s94, s94, 221184
Ra 140,242,19456
K 193,162,163
Mvaa 176,152,8
s_add_u32 m0, s95, 8192
X 165
Mvaa 176,156,12
Ta 186,11,8448
Ta 188,11,28672
Mvaa 180,156,28
X 166
Mvaa 180,152,24
Ra 144,242,20480
K 200,164,165
Mvaa 184,152,40
X 167
Mvaa 184,156,44
Ta 190,11,28928
D 4,20,94,0
Mvaa 188,156,60
W 7
X 168
Maaa 232,220,228
s_add_u32 s64, s64, 1
Ra 192,13,55296
D 5,20,94,1024
Maaa 236,224,228
Ta 196,11,10240
K 201,166,167
Mvaa 188,152,56
W 6
X 169
Mvav 32,128,196
D 6,20,94,2048
Ra 148,242,21504
Mvav 64,128,204
X 170
Mvav 96,128,212
Ta 198,11,10496
D 7,20,94,3072
Mvav 128,128,220
s_add_u32 s94, s94, 221184
Ta 200,11,30720
K 208,168,169
Mvav 132,132,220
s_add_u32 m0, s95, 12288
X 171
Mvav 100,132,212
Ra 152,242,22528
Ta 202,11,30976
Mvav 68,132,204
X 172
Mvav 36,132,196
W 6
Ra 204,13,55808
K 209,170,171
Maaa 232,184,192
X 173
Maaa 236,188,192
D 4,20,94,0
Ra 156,242,23552
Mvav 40,136,196
X 174
Mvav 72,136,204
Ta 208,11,12288
D 5,20,94,1024
Mvav 104,136,212
Ta 210,11,12544
K 216,172,173
Mvav 136,136,220
X 175
Mvav 140,140,220
D 6,20,94,2048
Ra 128,242,8192
Mvav 108,140,212
Ta 212,11,32768
Ta 214,11,33024
Mvav 76,140,204
X 176
Mvav 44,140,196
W 5
D 7,20,94,3072
K 217,174,175
Mvav 48,144,196
s_add_u32 s94, s94, 221184
X 177
Maaa 232,196,204
s_add_u32 m0, s95, 16384
Ra 132,242,9216
Ra 216,13,56320
Maaa 236,200,204
X 178
Mvav 80,144,204
Ta 220,11,14336
K 194,176,177
Mvav 112,144,212
X 179
Mvav 144,144,220
D 4,20,94,0
Ta 222,11,14592
Mvav 148,148,220
Ra 136,242,10240
Ta 224,11,34816
Mvav 116,148,212
X 180
Mvav 84,148,204
D 5,20,94,1024
K 195,178,179
Mvav 52,148,196
X 181
Mvav 56,152,196
D 6,20,94,2048
Ta 226,11,35072
Mvav 88,152,204
W 5
Ra 140,242,11264
Ra 228,13,56832
Maaa 232,208,216
X 182
Maaa 236,212,216
D 7,20,94,3072
K 202,180,181
Mvav 120,152,212
s_add_u32 s94, s94, 221184
X 183
Mvav 152,152,220
Ta 184,11,16384
Ra 144,242,12288
Mvav 156,156,220
X 184
Mvav 124,156,212
Ta 186,11,16640
K 203,182,183
Mvav 92,156,204
X 185
Mvav 60,156,196
W 4
Ta 188,11,36864
Ra 148,242,13312
Cvaa 160,128,0,224
X 186
Mvaa 160,132,4
Ta 190,11,37120
K 210,184,185
Cvaa 164,132,20,228
W 6
Ra 192,13,57344
Ta 196,11,18432
Maaa 232,220,228
X 187
Maaa 236,224,228
X 188
Mvaa 164,128,16
Ra 152,242,14336
K 211,186,187
Cvaa 168,128,32,232
Ta 198,11,18688
Ta 200,11,38912
Mvaa 168,132,36
X 189
Cvaa 172,132,52,236
X 190
Mvaa 172,128,48
Ra 156,242,15360
K 218,188,189
Mvaa 160,136,8
Ta 202,11,39168
Ra 204,13,57856
Mvaa 160,140,12
X 191
Mvaa 164,140,28
Ra 128,242,24576
Mvaa 164,136,24
W 8
Maaa 232,184,192
Ra 132,242,25600
Maaa 236,188,192
X 160
Mvaa 168,136,40
K 219,190,191
Mvaa 168,140,44
X 161
Mvaa 172,140,60
X 162
Mvaa 172,136,56
W 4
Ra 136,242,26624
K 196,160,161
Cvaa 176,144,0,224
X 163
Mvaa 176,148,4
X 164
Cvaa 180,148,20,228
Ra 140,242,27648
K 197,162,163
Mvaa 180,144,16
X 165
Cvaa 184,144,32,232
W 4
Maaa 232,196,204
X 166
Maaa 236,200,204
Mvaa 184,148,36
K 204,164,165
Cvaa 188,148,52,236
X 167
Mvaa 188,144,48
Ra 144,242,28672
Mvaa 176,152,8
X 168
Mvaa 176,156,12
Ra 148,242,29696
Mvaa 180,156,28
K 205,166,167
Mvaa 180,152,24
X 169
Mvaa 184,152,40
Mvaa 184,156,44
X 170
Mvaa 188,156,60
K 212,168,169
Mvaa 188,152,56
W 2
X 171
Mvav 32,128,192
Ra 152,242,30720
Ra 156,242,31744
Mvav 64,128,200
X 172
Mvav 96,128,208
Mvav 128,128,216
K 213,170,171
Mvav 132,132,216
)"
R"(X 173
Mvav 100,132,208
Mvav 68,132,200
X 174
Mvav 36,132,192
Mvav 40,136,192
K 220,172,173
Mvav 72,136,200
X 175
Mvav 104,136,208
Mvav 136,136,216
X 176
Mvav 140,140,216
Mvav 108,140,208
K 221,174,175
Mvav 76,140,200
s_waitcnt vmcnt(0) lgkmcnt(0)
s_barrier
v_xor_b32 v242, 0x10000, v242
Mvav 44,140,192
X 177
Mvav 48,144,192
X 178
Mvav 80,144,200
Ra 128,242,0
K 198,176,177
Mvav 112,144,208
X 179
Mvav 144,144,216
Ra 132,242,1024
Mvav 148,148,216
X 180
Mvav 116,148,208
Ra 136,242,2048
Mvav 84,148,200
K 199,178,179
Mvav 52,148,192
X 181
Mvav 56,152,192
Ra 140,242,3072
Mvav 88,152,200
X 182
Mvav 120,152,208
Mvav 152,152,216
K 206,180,181
Mvav 156,156,216
X 183
Mvav 124,156,208
Mvav 92,156,200
K 207,182,183
Mvav 60,156,192
W 0
Cvaa 160,128,0,224
s_mov_b32 s20, s34
Ra 144,242,4096
Ra 148,242,5120
Mvaa 160,132,4
s_xor_b32 s62, s62, 0x10000
X 184
Cvaa 164,132,20,228
s_and_b32 s21, s35, 0xffff
Ta 184,11,40960
Ta 186,11,41216
Mvaa 164,128,16
Ta 188,12,32768
Ta 190,12,33024
Cvaa 168,128,32,232
s_mov_b32 s22, 2211840
Ra 192,13,58368
Ta 196,11,43008
Mvaa 168,132,36
s_mov_b32 s23, 0x20000
X 185
Cvaa 172,132,52,236
Ta 198,11,43264
Ta 200,12,34816
Mvaa 172,128,48
W 3
X 186
Maaa 232,184,192
Ta 202,12,35072
K 214,184,185
Maaa 236,188,192
Ra 204,13,58880
Ra 152,242,6144
Mvaa 160,136,8
X 187
Mvaa 160,140,12
Ta 208,11,45056
v_lshrrev_b32 v4, 8, v1
Mvaa 164,140,28
X 188
Mvaa 164,136,24
Ta 210,11,45312
v_lshlrev_b32 v5, 5, v4
Mvaa 168,136,40
v_xor_b32 v5, v5, v1
K 215,186,187
Mvaa 168,140,44
Ra 156,242,7168
Ta 212,12,36864
Mvaa 172,140,60
X 189
Mvaa 172,136,56
v_and_b32 v5, 0xf0, v5
v_mul_u32_u24 v4, 1280, v4
v_smfmac_f32_16x16x64_bf16 a[160:163], a[176:179], v[192:199], v240
W 5
X 190
Maaa 232,196,204
Ta 214,12,37120
v_add_u32 v4, v4, v5
Maaa 236,200,204
Ra 216,13,59392
K 222,188,189
v_smfmac_f32_16x16x64_bf16 a[164:167], a[176:179], v[200:207], v240
X 191
X 160
K 223,190,191
v_smfmac_f32_16x16x64_bf16 a[168:171], a[176:179], v[208:215], v240
Ra 128,242,16384
v_xor_b32 v5, 128, v4
v_smfmac_f32_16x16x64_bf16 a[172:175], a[176:179], v[216:223], v240
W 4
Ta 220,11,47104
v_add_u32 v6, 8192, v4
Cvaa 176,144,0,224
Ta 222,11,47360
v_add_u32 v7, 12288, v5
Mvaa 176,148,4
Ra 132,242,17408
v_add_u32 v5, 4096, v5
Cvaa 180,148,20,228
s_mul_i32 s94, s85, 40960
X 161
Mvaa 180,144,16
s_mul_i32 s81, s14, 20480
Ta 224,12,38912
Ta 226,12,39168
Cvaa 184,144,32,232
s_add_u32 s94, s94, s81
X 162
Mvaa 184,148,36
W 6
s_lshl_b32 s95, s14, 12
K 192,160,161
Maaa 232,208,216
s_add_u32 s95, s95, 98304
X 163
Maaa 236,212,216
s_add_u32 m0, s95, 0
Ra 228,13,59904
Ra 136,242,18432
Cvaa 188,148,52,236
X 164
Mvaa 188,144,48
Ta 184,11,49152
K 193,162,163
Mvaa 176,152,8
X 165
Mvaa 176,156,12
Ra 140,242,19456
Ta 186,11,49408
Mvaa 180,156,28
D 4,20,94,0
Ta 188,12,40960
Mvaa 180,152,24
X 166
Mvaa 184,152,40
D 5,20,94,1024
K 200,164,165
Mvaa 184,156,44
X 167
Mvaa 188,156,60
W 5
D 6,20,94,2048
Ra 144,242,20480
Maaa 232,220,228
s_add_u32 s64, s64, 1
Ta 190,12,41216
Ra 192,13,60416
Maaa 236,224,228
D 7,20,94,3072
Ta 196,11,51200
Mvaa 188,152,56
W 6
X 168
Mvav 32,128,196
s_add_u32 s94, s94, 256
s_add_u32 m0, s95, 8192
K 201,166,167
Mvav 64,128,204
X 169
Mvav 96,128,212
D 4,20,94,0
Ra 148,242,21504
Mvav 128,128,220
X 170
Mvav 132,132,220
D 5,20,94,1024
Ta 198,11,51456
Mvav 100,132,212
Ta 200,12,43008
K 208,168,169
Mvav 68,132,204
D 6,20,94,2048
Ra 152,242,22528
Mvav 36,132,196
W 5
X 171
Maaa 232,184,192
D 7,20,94,3072
Ta 202,12,43264
Maaa 236,188,192
s_add_u32 s94, s94, 256
X 172
Mvav 40,136,196
s_add_u32 m0, s95, 16384
Ra 204,13,60928
K 209,170,171
Mvav 72,136,204
X 173
Mvav 104,136,212
Ra 156,242,23552
D 4,20,94,0
Mvav 136,136,220
X 174
Mvav 140,140,220
D 5,20,94,1024
Ta 208,11,53248
Mvav 108,140,212
Ta 210,11,53504
K 216,172,173
Mvav 76,140,204
D 6,20,94,2048
Ra 128,242,8192
Mvav 44,140,196
W 3
X 175
Mvav 48,144,196
D 7,20,94,3072
Ta 212,12,45056
Maaa 232,196,204
s_add_u32 s94, s94, 256
X 176
Maaa 236,200,204
s_add_u32 m0, s95, 24576
Ta 214,12,45312
K 217,174,175
Mvav 80,144,204
D 4,20,94,0
Ra 132,242,9216
Mvav 112,144,212
X 177
Mvav 144,144,220
Ra 216,13,61440
D 5,20,94,1024
Mvav 148,148,220
X 178
Mvav 116,148,212
D 6,20,94,2048
Ta 220,11,55296
Mvav 84,148,204
Ta 222,11,55552
K 194,176,177
Mvav 52,148,196
D 7,20,94,3072
Ra 136,242,10240
Mvav 56,152,196
s_add_u32 s94, s94, 256
X 179
Mvav 88,152,204
W 3
X 180
Maaa 232,208,216
s_add_u32 m0, s95, 32768
Ta 224,12,47104
K 195,178,179
Maaa 236,212,216
D 4,20,94,0
Ta 226,12,47360
Mvav 120,152,212
Ra 140,242,11264
Ra 228,13,61952
Mvav 152,152,220
X 181
Mvav 156,156,220
D 5,20,94,1024
Ta 184,11,57344
Mvav 124,156,212
X 182
Mvav 92,156,204
D 6,20,94,2048
K 202,180,181
Mvav 60,156,196
W 2
X 183
Cvaa 160,128,0,224
D 7,20,94,3072
Ra 144,242,12288
Mvaa 160,132,4
s_add_u32 s94, s94, 256
X 184
Cvaa 164,132,20,228
W 2
Ta 186,11,57600
K 203,182,183
Maaa 232,220,228
X 185
Maaa 236,224,228
Ta 188,12,49152
Ra 148,242,13312
Mvaa 164,128,16
X 186
Cvaa 168,128,32,232
Ta 190,12,49408
K 210,184,185
Mvaa 168,132,36
Ra 192,13,62464
Ta 196,11,59392
Cvaa 172,132,52,236
X 187
Mvaa 172,128,48
X 188
Mvaa 160,136,8
Ra 152,242,14336
K 211,186,187
Mvaa 160,140,12
Ta 198,11,59648
Ta 200,12,51200
Mvaa 164,140,28
X 189
Mvaa 164,136,24
W 4
X 190
Maaa 232,184,192
Ra 156,242,15360
K 218,188,189
Maaa 236,188,192
Ta 202,12,51456
Ra 204,13,62976
Mvaa 168,136,40
X 191
Mvaa 168,140,44
Ra 128,242,24576
Ra 132,242,25600
Mvaa 172,140,60
X 160
Mvaa 172,136,56
W 4
Ra 136,242,26624
K 219,190,191
Cvaa 176,144,0,224
X 161
Mvaa 176,148,4
X 162
Cvaa 180,148,20,228
Ra 140,242,27648
K 196,160,161
Mvaa 180,144,16
X 163
Cvaa 184,144,32,232
W 4
X 164
Maaa 232,196,204
K 197,162,163
Maaa 236,200,204
X 165
Mvaa 184,148,36
X 166
Cvaa 188,148,52,236
K 204,164,165
Mvaa 188,144,48
X 167
Mvaa 176,152,8
Ra 144,242,28672
G 252,232
Mvaa 176,156,12
G 253,233
G 254,234
Mvaa 180,156,28
X 168
Mvaa 180,152,24
G 255,235
G 8,236
Mvaa 184,152,40
Ra 148,242,29696
G 9,237
Mvaa 184,156,44
G 10,238
G 14,239
Mvaa 188,156,60
K 252,252,8
K 253,253,9
Mvaa 188,152,56
W 2
K 205,166,167
K 254,254,10
Mvav 32,128,192
K 255,255,14
)"
R"(v_and_b32 v8, 0xffff0000, v252
Mvav 64,128,200
v_and_b32 v9, 0xffff0000, v253
v_and_b32 v10, 0xffff0000, v254
Mvav 96,128,208
X 169
Mvav 128,128,216
v_and_b32 v14, 0xffff0000, v255
v_lshlrev_b32 v252, 16, v252
Mvav 132,132,216
v_lshlrev_b32 v253, 16, v253
v_lshlrev_b32 v254, 16, v254
Mvav 100,132,208
v_lshlrev_b32 v255, 16, v255
v_mul_f32 v15, 0xbfb8aa3b, v252
Mvav 68,132,200
v_mul_f32 v2, 0xbfb8aa3b, v253
v_mul_f32 v3, 0xbfb8aa3b, v254
Mvav 36,132,192
X 170
Mvav 40,136,192
v_mul_f32 v27, 0xbfb8aa3b, v255
K 212,168,169
Mvav 72,136,200
X 15
Mvav 104,136,208
Ra 152,242,30720
Ra 156,242,31744
Mvav 136,136,216
X 2
X 3
X 27
v_add_f32 v15, 1.0, v15
v_add_f32 v2, 1.0, v2
v_add_f32 v3, 1.0, v3
v_add_f32 v27, 1.0, v27
v_rcp_f32 v15, v15
v_rcp_f32 v2, v2
v_rcp_f32 v3, v3
v_rcp_f32 v27, v27
U 252,252,15
U 253,253,2
U 254,254,3
U 255,255,27
K 252,252,252
K 253,253,253
K 254,254,254
K 255,255,255
v_lshlrev_b32 v252, 16, v252
v_lshlrev_b32 v253, 16, v253
v_lshlrev_b32 v254, 16, v254
v_lshlrev_b32 v255, 16, v255
U 252,252,8
U 253,253,9
U 254,254,10
U 255,255,14
K 246,252,253
K 247,254,255
s_lshl_b32 s81, s14, 3
v_add_u32 v2, s81, v1
s_mov_b32 s86, 0x00ff00ff
s_mov_b32 s87, 0x00ff00ff
v_cndmask_b32_e64 v252, 0, v246, s[86:87]
v_cndmask_b32_e64 v253, 0, v247, s[86:87]
v_cndmask_b32_e64 v246, v246, 0, s[86:87]
v_cndmask_b32_e64 v247, v247, 0, s[86:87]
ds_write_b64 v2, v[252:253] offset:63488
ds_write_b64 v2, v[246:247] offset:64512
Mvav 140,140,216
X 171
Mvav 108,140,208
X 172
Mvav 76,140,200
s_waitcnt vmcnt(0) lgkmcnt(0)
s_barrier
v_xor_b32 v242, 0x10000, v242
Mvav 44,140,192
Ra 128,242,0
K 213,170,171
Mvav 48,144,192
X 173
Mvav 80,144,200
X 174
Mvav 112,144,208
Ra 132,242,1024
K 220,172,173
Mvav 144,144,216
X 175
Mvav 148,148,216
X 176
Mvav 116,148,208
Ra 136,242,2048
K 221,174,175
Mvav 84,148,200
X 177
Mvav 52,148,192
X 178
Mvav 56,152,192
Ra 140,242,3072
K 198,176,177
Mvav 88,152,200
X 179
Mvav 120,152,208
X 180
Mvav 152,152,216
K 199,178,179
Mvav 156,156,216
X 181
Mvav 124,156,208
X 182
Mvav 92,156,200
K 206,180,181
Mvav 60,156,192
X 183
s_nop 0
K 207,182,183
W 0
Cvaa 160,128,0,224
Ra 144,242,4096
Ra 148,242,5120
Mvaa 160,132,4
s_xor_b32 s62, s62, 0x10000
X 184
Cvaa 164,132,20,228
Ra 216,1,63488
Ra 220,1,64512
Mvaa 164,128,16
v_lshrrev_b32 v8, 6, v1
v_and_b32 v9, 7, v8
Cvaa 168,128,32,232
s_lshl_b32 s81, s14, 2
X 185
Mvaa 168,132,36
v_xor_b32 v9, s81, v9
v_lshlrev_b32 v9, 5, v9
Cvaa 172,132,52,236
X 186
Mvaa 172,128,48
v_lshl_add_u32 v4, v8, 8, v9
v_bfe_u32 v9, v1, 4, 2
Mvaa 160,136,8
v_lshl_add_u32 v4, v9, 3, v4
v_add_u32 v4, 98304, v4
Mvaa 160,140,12
K 214,184,185
v_xor_b32 v5, 32, v4
Mvaa 164,140,28
v_xor_b32 v6, 64, v4
v_xor_b32 v7, 96, v4
Ta 184,4,0
Ta 186,4,4096
Mvaa 164,136,24
Ta 188,5,0
Ta 190,5,4096
Mvaa 168,136,40
Ra 152,242,6144
Ta 192,6,0
Mvaa 168,140,44
W 4
X 187
Zaaa 232,184,216
Ta 194,6,4096
v_and_b32 v11, 0x70, v1
Mvaa 172,140,60
v_mul_u32_u24 v11, 160, v11
v_lshrrev_b32 v10, 8, v1
Mvaa 172,136,56
X 188
v_smfmac_f32_16x16x64_bf16 a[160:163], a[176:179], v[192:199], v240
Ra 156,242,7168
W 4
Maaa 232,188,220
K 215,186,187
v_lshl_add_u32 v11, v10, 4, v11
v_smfmac_f32_16x16x64_bf16 a[164:167], a[176:179], v[200:207], v240
X 189
X 190
K 222,188,189
X 191
X 160
K 223,190,191
v_smfmac_f32_16x16x64_bf16 a[168:171], a[176:179], v[208:215], v240
Ta 196,7,0
v_bfe_u32 v10, v1, 7, 1
v_smfmac_f32_16x16x64_bf16 a[172:175], a[176:179], v[216:223], v240
W 2
Ta 198,7,4096
v_lshl_add_u32 v10, v10, 6, v11
Zaaa 236,192,216
W 2
s_mul_i32 s94, s85, 20480
s_lshl_b32 s82, s14, 8
Cvaa 176,144,0,224
s_add_u32 s94, s94, s82
Ta 200,4,8192
Ta 202,4,12288
Mvaa 176,148,4
s_add_u32 s94, s94, 2752512
s_lshl_b32 s71, s85, 4
Ra 128,242,16384
Cvaa 180,148,20,228
W 3
s_lshl_b32 s82, s14, 3
Ta 204,5,8192
Maaa 236,196,220
s_add_u32 s71, s71, s82
s_add_u32 s71, s71, 2560
Ta 206,5,12288
Mvaa 180,144,16
X 161
Cvaa 184,144,32,232
Ra 132,242,17408
Ta 208,6,8192
Mvaa 184,148,36
W 5
X 162
Zaaa 240,200,216
Ta 210,6,12288
K 192,160,161
Cvaa 188,148,52,236
buffer_store_dwordx4 a[232:235], v10, s[8:11], s94 offen offset:0 sc1
Ta 212,7,8192
Mvaa 188,144,48
X 163
Mvaa 176,152,8
W 4
X 164
Maaa 240,204,220
Ra 136,242,18432
Ta 214,7,12288
Mvaa 176,156,12
Ta 184,4,16384
K 193,162,163
Mvaa 180,156,28
X 165
Mvaa 180,152,24
W 4
Ta 186,4,20480
Ra 140,242,19456
Zaaa 244,208,216
X 166
Mvaa 184,152,40
buffer_store_dwordx4 a[236:239], v10, s[8:11], s94 offen offset:128 sc1
K 200,164,165
Mvaa 184,156,44
X 167
Mvaa 188,156,60
W 3
Ta 188,5,16384
Ta 190,5,20480
Maaa 244,212,220
s_add_u32 s64, s64, 1
X 168
Mvaa 188,152,56
W 2
Ra 144,242,20480
K 201,166,167
Mvav 32,128,196
Ta 192,6,16384
Ta 194,6,20480
Mvav 64,128,204
X 169
Zaaa 248,184,216
buffer_store_dwordx4 a[240:243], v10, s[8:11], s94 offen offset:512 sc1
Ta 196,7,16384
Mvav 96,128,212
X 170
Mvav 128,128,220
Ra 148,242,21504
K 208,168,169
Mvav 132,132,220
W 5
Ta 198,7,20480
Ta 200,4,24576
Maaa 248,188,220
X 171
Mvav 100,132,212
X 172
Mvav 68,132,204
Ta 202,4,28672
K 209,170,171
Mvav 36,132,196
W 5
Ra 152,242,22528
buffer_store_dwordx4 a[244:247], v10, s[8:11], s94 offen offset:640 sc1
Zaaa 252,192,216
X 173
Mvav 40,136,196
X 174
Mvav 72,136,204
Ta 204,5,24576
Ta 206,5,28672
Mvav 104,136,212
W 5
Ra 156,242,23552
K 216,172,173
Maaa 252,196,220
X 175
Mvav 136,136,220
Ta 208,6,24576
Ta 210,6,28672
Mvav 140,140,220
X 176
Mvav 108,140,212
W 6
buffer_store_dwordx4 a[248:251], v10, s[8:11], s94 offen offset:1024 sc1
K 217,174,175
Zaaa 232,200,216
X 177
Mvav 76,140,204
Ta 212,7,24576
Ra 128,242,8192
Mvav 44,140,196
W 4
Ta 214,7,28672
Ta 184,4,32768
Mvav 48,144,196
X 178
Maaa 232,204,220
Ra 132,242,9216
K 194,176,177
Mvav 80,144,204
X 179
Mvav 112,144,212
Ta 186,4,36864
buffer_store_dwordx4 a[252:255], v10, s[8:11], s94 offen offset:1152 sc1
Mvav 144,144,220
W 6
X 180
Zaaa 236,208,216
Ta 188,5,32768
K 195,178,179
Mvav 148,148,220
X 181
Mvav 116,148,212
Ta 190,5,36864
Ra 136,242,10240
Mvav 84,148,204
W 6
X 182
Maaa 236,212,220
Ta 192,6,32768
K 202,180,181
Mvav 52,148,196
Ta 194,6,36864
Ra 140,242,11264
Mvav 56,152,196
X 183
Mvav 88,152,204
W 6
buffer_store_dwordx4 a[232:235], v10, s[8:11], s94 offen offset:1536 sc1
Ta 196,7,32768
Zaaa 240,184,216
X 184
Mvav 120,152,212
Ta 198,7,36864
K 203,182,183
Mvav 152,152,220
Ra 144,242,12288
Ra 148,242,13312
Mvav 156,156,220
W 8
X 185
Maaa 240,188,220
X 186
Mvav 124,156,212
buffer_store_dwordx4 a[236:239], v10, s[8:11], s94 offen offset:1664 sc1
K 210,184,185
Mvav 92,156,204
X 187
Mvav 60,156,196
W 5
X 188
Zaaa 244,192,216
W 4
Ra 152,242,14336
K 211,186,187
Cvaa 160,128,0,224
X 189
Mvaa 160,132,4
X 190
Cvaa 164,132,20,228
W 3
Ra 156,242,15360
buffer_store_dwordx4 a[240:243], v10, s[8:11], s94 offen offset:2048 sc1
Maaa 244,196,220
K 218,188,189
Mvaa 164,128,16
X 191
Cvaa 168,128,32,232
Mvaa 168,132,36
Cvaa 172,132,52,236
Mvaa 172,128,48
Ra 128,242,24576
Mvaa 160,136,8
Ra 132,242,25600
buffer_store_dwordx4 a[244:247], v10, s[8:11], s94 offen offset:2176 sc1
Mvaa 160,140,12
Mvaa 164,140,28
Mvaa 164,136,24
)"
R"(Mvaa 168,136,40
Mvaa 168,140,44
Mvaa 172,140,60
Mvaa 172,136,56
W 2
Ra 136,242,26624
Cvaa 176,144,0,224
X 160
Mvaa 176,148,4
Ra 140,242,27648
K 219,190,191
Cvaa 180,148,20,228
X 161
Mvaa 180,144,16
X 162
Cvaa 184,144,32,232
K 196,160,161
Mvaa 184,148,36
X 163
Cvaa 188,148,52,236
X 164
Mvaa 188,144,48
Ra 144,242,28672
K 197,162,163
Mvaa 176,152,8
X 165
Mvaa 176,156,12
X 166
Mvaa 180,156,28
Ra 148,242,29696
K 204,164,165
Mvaa 180,152,24
X 167
Mvaa 184,152,40
X 168
Mvaa 184,156,44
K 205,166,167
Mvaa 188,156,60
X 169
Mvaa 188,152,56
W 2
X 170
Mvav 32,128,192
Ra 152,242,30720
K 212,168,169
Mvav 64,128,200
X 171
Mvav 96,128,208
Ra 156,242,31744
Mvav 128,128,216
X 172
Mvav 132,132,216
K 213,170,171
Mvav 100,132,208
X 173
Mvav 68,132,200
X 174
Mvav 36,132,192
Mvav 40,136,192
K 220,172,173
Mvav 72,136,200
X 175
Mvav 104,136,208
Mvav 136,136,216
X 176
Mvav 140,140,216
Mvav 108,140,208
K 221,174,175
Mvav 76,140,200
W 0
s_barrier
v_xor_b32 v242, 0x10000, v242
Mvav 44,140,192
X 177
Mvav 48,144,192
X 178
Mvav 80,144,200
Ra 128,242,0
K 198,176,177
Mvav 112,144,208
X 179
Mvav 144,144,216
Ra 132,242,1024
Mvav 148,148,216
X 180
Mvav 116,148,208
Ra 136,242,2048
Mvav 84,148,200
K 199,178,179
Mvav 52,148,192
X 181
Mvav 56,152,192
Ra 140,242,3072
Mvav 88,152,200
X 182
Mvav 120,152,208
Mvav 152,152,216
K 206,180,181
Mvav 156,156,216
X 183
Mvav 124,156,208
Mvav 92,156,200
s_waitcnt vmcnt(0)
v_mov_b64 v[28:29], s[12:13]
s_mov_b64 exec, 1
buffer_store_dwordx2 v[28:29], off, s[8:11], s71 sc1
s_mov_b64 exec, -1
K 207,182,183
Mvav 60,156,192
s_mov_b32 s85, 99
s_lshl_b32 s65, s57, 1
s_sub_u32 s65, s65, 1
s_branch .Ll0t_46%=
.Lf1r_47%=:
s_lshl_b32 s92, s58, 2
s_add_u32 s91, s92, s51
s_add_u32 s95, s92, 0
s_and_b32 s95, s95, 15
s_mul_i32 s95, s95, 40960
s_mul_i32 s71, s51, 0
s_add_u32 s71, s71, 133120
s_add_u32 m0, s71, s48
s_lshl2_add_u32 s94, s48, s95
D 16,40,94,0
s_add_u32 s95, s92, 1
s_and_b32 s95, s95, 15
s_mul_i32 s95, s95, 40960
s_mul_i32 s71, s51, 0
s_add_u32 s71, s71, 143360
D 17,40,94,1024
D 18,40,94,2048
D 19,40,94,3072
s_add_u32 m0, m0, 4096
s_nop 0
buffer_load_dwordx4 v20, s[40:43], s94 offen lds
s_add_u32 m0, s71, s48
s_lshl2_add_u32 s94, s48, s95
D 16,40,94,0
s_add_u32 s95, s92, 2
s_and_b32 s95, s95, 15
s_mul_i32 s95, s95, 40960
s_mul_i32 s71, s51, 0
s_add_u32 s71, s71, 153600
D 17,40,94,1024
D 18,40,94,2048
D 19,40,94,3072
s_add_u32 m0, m0, 4096
s_nop 0
buffer_load_dwordx4 v20, s[40:43], s94 offen lds
s_add_u32 m0, s71, s48
s_lshl2_add_u32 s94, s48, s95
D 16,40,94,0
D 17,40,94,1024
D 18,40,94,2048
D 19,40,94,3072
s_add_u32 m0, m0, 4096
s_nop 0
buffer_load_dwordx4 v20, s[40:43], s94 offen lds
s_branch .Lf1_40%=
.Lspin_15%=:
s_mov_b32 s84, 0
.Llp_17%=:
s_sleep 2
v_lshrrev_b32 v28, 1, v1
s_lshl_b32 s80, s60, 9
s_add_u32 s80, s80, 40960
buffer_load_dwordx2 v[28:29], v28, s[8:11], s80 offen sc1
s_waitcnt vmcnt(0)
s_add_u32 s84, s84, 1
v_cmp_eq_u32 vcc, s13, v29
s_cmp_eq_u64 vcc, -1
s_cbranch_scc1 .Lok_16%=
s_cmp_lt_u32 s84, 1048576
s_cbranch_scc1 .Llp_17%=
s_branch .Lok_16%=
.Lspin_20%=:
s_mov_b32 s84, 0
.Llp_22%=:
s_sleep 2
v_lshrrev_b32 v28, 1, v1
s_lshl_b32 s80, s60, 9
s_add_u32 s80, s80, 40960
buffer_load_dwordx2 v[28:29], v28, s[8:11], s80 offen sc1
s_waitcnt vmcnt(0)
s_add_u32 s84, s84, 1
v_cmp_eq_u32 vcc, s13, v29
s_cmp_eq_u64 vcc, -1
s_cbranch_scc1 .Lok_21%=
s_cmp_lt_u32 s84, 1048576
s_cbranch_scc1 .Llp_22%=
s_branch .Lok_21%=
.Lrt_23%=:
s_waitcnt vmcnt(0)
s_mov_b32 s86, 40960
buffer_load_dwordx4 v[32:35], v1, s[8:11], s86 offen offset:0 sc1
buffer_load_dwordx4 v[36:39], v1, s[8:11], s86 offen offset:1024 sc1
buffer_load_dwordx4 v[40:43], v1, s[8:11], s86 offen offset:2048 sc1
buffer_load_dwordx4 v[44:47], v1, s[8:11], s86 offen offset:3072 sc1
s_mov_b32 s86, 45056
buffer_load_dwordx4 v[48:51], v1, s[8:11], s86 offen offset:0 sc1
buffer_load_dwordx4 v[52:55], v1, s[8:11], s86 offen offset:1024 sc1
buffer_load_dwordx4 v[56:59], v1, s[8:11], s86 offen offset:2048 sc1
buffer_load_dwordx4 v[60:63], v1, s[8:11], s86 offen offset:3072 sc1
s_mov_b32 s86, 49152
buffer_load_dwordx4 v[64:67], v1, s[8:11], s86 offen offset:0 sc1
buffer_load_dwordx4 v[68:71], v1, s[8:11], s86 offen offset:1024 sc1
buffer_load_dwordx4 v[72:75], v1, s[8:11], s86 offen offset:2048 sc1
buffer_load_dwordx4 v[76:79], v1, s[8:11], s86 offen offset:3072 sc1
s_mov_b32 s86, 53248
buffer_load_dwordx4 v[80:83], v1, s[8:11], s86 offen offset:0 sc1
buffer_load_dwordx4 v[84:87], v1, s[8:11], s86 offen offset:1024 sc1
buffer_load_dwordx4 v[88:91], v1, s[8:11], s86 offen offset:2048 sc1
buffer_load_dwordx4 v[92:95], v1, s[8:11], s86 offen offset:3072 sc1
s_branch .Lrtr_24%=
.Lrt_25%=:
v_lshrrev_b32 v136, 1, v1
s_mov_b32 s84, 0
.LL_27%=:
s_waitcnt vmcnt(0)
v_min3_u32 v96, v33, v35, v95
v_max3_u32 v97, v33, v35, v95
v_min3_u32 v96, v96, v37, v39
v_max3_u32 v97, v97, v37, v39
v_min3_u32 v96, v96, v41, v43
v_max3_u32 v97, v97, v41, v43
v_min3_u32 v96, v96, v45, v47
v_max3_u32 v97, v97, v45, v47
v_min3_u32 v96, v96, v49, v51
v_max3_u32 v97, v97, v49, v51
v_min3_u32 v96, v96, v53, v55
v_max3_u32 v97, v97, v53, v55
v_min3_u32 v96, v96, v57, v59
v_max3_u32 v97, v97, v57, v59
v_min3_u32 v96, v96, v61, v63
v_max3_u32 v97, v97, v61, v63
v_min3_u32 v96, v96, v65, v67
v_max3_u32 v97, v97, v65, v67
v_min3_u32 v96, v96, v69, v71
v_max3_u32 v97, v97, v69, v71
v_min3_u32 v96, v96, v73, v75
v_max3_u32 v97, v97, v73, v75
v_min3_u32 v96, v96, v77, v79
v_max3_u32 v97, v97, v77, v79
v_min3_u32 v96, v96, v81, v83
v_max3_u32 v97, v97, v81, v83
v_min3_u32 v96, v96, v85, v87
v_max3_u32 v97, v97, v85, v87
v_min3_u32 v96, v96, v89, v91
v_max3_u32 v97, v97, v89, v91
v_min_u32 v96, v96, v93
v_max_u32 v97, v97, v93
v_cmp_eq_u32 vcc, s13, v96
v_cmp_eq_u32_e64 s[86:87], s13, v97
s_and_b64 vcc, vcc, s[86:87]
s_cmp_eq_u64 vcc, -1
s_cbranch_scc1 .LL_28%=
s_sleep 2
s_mov_b32 s86, 40960
buffer_load_dwordx4 v[32:35], v1, s[8:11], s86 offen offset:0 sc1
buffer_load_dwordx4 v[36:39], v1, s[8:11], s86 offen offset:1024 sc1
buffer_load_dwordx4 v[40:43], v1, s[8:11], s86 offen offset:2048 sc1
buffer_load_dwordx4 v[44:47], v1, s[8:11], s86 offen offset:3072 sc1
s_mov_b32 s86, 45056
buffer_load_dwordx4 v[48:51], v1, s[8:11], s86 offen offset:0 sc1
buffer_load_dwordx4 v[52:55], v1, s[8:11], s86 offen offset:1024 sc1
buffer_load_dwordx4 v[56:59], v1, s[8:11], s86 offen offset:2048 sc1
buffer_load_dwordx4 v[60:63], v1, s[8:11], s86 offen offset:3072 sc1
s_mov_b32 s86, 49152
buffer_load_dwordx4 v[64:67], v1, s[8:11], s86 offen offset:0 sc1
buffer_load_dwordx4 v[68:71], v1, s[8:11], s86 offen offset:1024 sc1
buffer_load_dwordx4 v[72:75], v1, s[8:11], s86 offen offset:2048 sc1
buffer_load_dwordx4 v[76:79], v1, s[8:11], s86 offen offset:3072 sc1
s_mov_b32 s86, 53248
buffer_load_dwordx4 v[80:83], v1, s[8:11], s86 offen offset:0 sc1
buffer_load_dwordx4 v[84:87], v1, s[8:11], s86 offen offset:1024 sc1
buffer_load_dwordx4 v[88:91], v1, s[8:11], s86 offen offset:2048 sc1
buffer_load_dwordx4 v[92:95], v1, s[8:11], s86 offen offset:3072 sc1
s_add_u32 s84, s84, 1
s_cmp_lt_u32 s84, 1048576
s_cbranch_scc1 .LL_27%=
s_waitcnt vmcnt(0)
.LL_28%=:
v_mov_b32 v96, v32
v_mov_b32 v97, 0
v_mov_b32 v98, 0
v_mov_b32 v100, 0
v_med3_u32 v98, v97, v98, v34
v_med3_u32 v97, v96, v97, v34
v_max_u32 v96, v96, v34
v_med3_u32 v98, v97, v98, v36
v_med3_u32 v97, v96, v97, v36
v_max_u32 v96, v96, v36
v_med3_u32 v98, v97, v98, v38
v_med3_u32 v97, v96, v97, v38
v_max_u32 v96, v96, v38
v_med3_u32 v98, v97, v98, v40
v_med3_u32 v97, v96, v97, v40
v_max_u32 v96, v96, v40
v_med3_u32 v98, v97, v98, v42
v_med3_u32 v97, v96, v97, v42
v_max_u32 v96, v96, v42
v_med3_u32 v98, v97, v98, v44
v_med3_u32 v97, v96, v97, v44
v_max_u32 v96, v96, v44
v_med3_u32 v98, v97, v98, v46
v_med3_u32 v97, v96, v97, v46
v_max_u32 v96, v96, v46
v_med3_u32 v98, v97, v98, v48
v_med3_u32 v97, v96, v97, v48
v_max_u32 v96, v96, v48
v_med3_u32 v98, v97, v98, v50
v_med3_u32 v97, v96, v97, v50
v_max_u32 v96, v96, v50
v_med3_u32 v98, v97, v98, v52
v_med3_u32 v97, v96, v97, v52
v_max_u32 v96, v96, v52
v_med3_u32 v98, v97, v98, v54
v_med3_u32 v97, v96, v97, v54
v_max_u32 v96, v96, v54
v_med3_u32 v98, v97, v98, v56
v_med3_u32 v97, v96, v97, v56
v_max_u32 v96, v96, v56
v_med3_u32 v98, v97, v98, v58
v_med3_u32 v97, v96, v97, v58
v_max_u32 v96, v96, v58
v_med3_u32 v98, v97, v98, v60
v_med3_u32 v97, v96, v97, v60
v_max_u32 v96, v96, v60
v_med3_u32 v98, v97, v98, v62
v_med3_u32 v97, v96, v97, v62
v_max_u32 v96, v96, v62
v_med3_u32 v98, v97, v98, v64
v_med3_u32 v97, v96, v97, v64
v_max_u32 v96, v96, v64
v_med3_u32 v98, v97, v98, v66
v_med3_u32 v97, v96, v97, v66
v_max_u32 v96, v96, v66
v_med3_u32 v98, v97, v98, v68
v_med3_u32 v97, v96, v97, v68
v_max_u32 v96, v96, v68
v_med3_u32 v98, v97, v98, v70
v_med3_u32 v97, v96, v97, v70
v_max_u32 v96, v96, v70
v_med3_u32 v98, v97, v98, v72
v_med3_u32 v97, v96, v97, v72
v_max_u32 v96, v96, v72
v_med3_u32 v98, v97, v98, v74
v_med3_u32 v97, v96, v97, v74
v_max_u32 v96, v96, v74
v_med3_u32 v98, v97, v98, v76
v_med3_u32 v97, v96, v97, v76
v_max_u32 v96, v96, v76
v_med3_u32 v98, v97, v98, v78
v_med3_u32 v97, v96, v97, v78
v_max_u32 v96, v96, v78
v_med3_u32 v98, v97, v98, v80
v_med3_u32 v97, v96, v97, v80
v_max_u32 v96, v96, v80
v_med3_u32 v98, v97, v98, v82
v_med3_u32 v97, v96, v97, v82
v_max_u32 v96, v96, v82
v_med3_u32 v98, v97, v98, v84
v_med3_u32 v97, v96, v97, v84
v_max_u32 v96, v96, v84
v_med3_u32 v98, v97, v98, v86
v_med3_u32 v97, v96, v97, v86
v_max_u32 v96, v96, v86
v_med3_u32 v98, v97, v98, v88
v_med3_u32 v97, v96, v97, v88
v_max_u32 v96, v96, v88
v_med3_u32 v98, v97, v98, v90
v_med3_u32 v97, v96, v97, v90
v_max_u32 v96, v96, v90
v_med3_u32 v98, v97, v98, v92
v_med3_u32 v97, v96, v97, v92
v_max_u32 v96, v96, v92
v_med3_u32 v98, v97, v98, v94
v_med3_u32 v97, v96, v97, v94
v_max_u32 v96, v96, v94
v_mov_b32 v99, v96
s_nop 1
v_max_u32_dpp v99, v99, v99 row_shr:1 row_mask:0xf bank_mask:0xf
s_nop 1
v_max_u32_dpp v99, v99, v99 row_shr:2 row_mask:0xf bank_mask:0xf
s_nop 1
v_max_u32_dpp v99, v99, v99 row_shr:4 row_mask:0xf bank_mask:0xf
s_nop 1
v_max_u32_dpp v99, v99, v99 row_shr:8 row_mask:0xf bank_mask:0xf
s_nop 1
v_max_u32_dpp v99, v99, v99 row_bcast:15 row_mask:0xa bank_mask:0xf
s_nop 1
v_max_u32_dpp v99, v99, v99 row_bcast:31 row_mask:0xc bank_mask:0xf
s_nop 0
v_readlane_b32 s86, v99, 63
s_nop 1
v_cmp_eq_u32 vcc, s86, v96
s_and_b32 s87, s86, 0xffff
s_sub_u32 s87, 2047, s87
v_cndmask_b32 v96, v96, v97, vcc
v_cndmask_b32_e64 v97, v97, 0, vcc
)"
R"(v_writelane_b32 v100, s87, 0
v_mov_b32 v99, v96
s_nop 1
v_max_u32_dpp v99, v99, v99 row_shr:1 row_mask:0xf bank_mask:0xf
s_nop 1
v_max_u32_dpp v99, v99, v99 row_shr:2 row_mask:0xf bank_mask:0xf
s_nop 1
v_max_u32_dpp v99, v99, v99 row_shr:4 row_mask:0xf bank_mask:0xf
s_nop 1
v_max_u32_dpp v99, v99, v99 row_shr:8 row_mask:0xf bank_mask:0xf
s_nop 1
v_max_u32_dpp v99, v99, v99 row_bcast:15 row_mask:0xa bank_mask:0xf
s_nop 1
v_max_u32_dpp v99, v99, v99 row_bcast:31 row_mask:0xc bank_mask:0xf
s_nop 0
v_readlane_b32 s86, v99, 63
s_nop 1
v_cmp_eq_u32 vcc, s86, v96
s_and_b32 s87, s86, 0xffff
s_sub_u32 s87, 2047, s87
v_cndmask_b32 v96, v96, v97, vcc
v_cndmask_b32_e64 v97, v97, 0, vcc
v_writelane_b32 v100, s87, 1
v_mov_b32 v99, v96
s_nop 1
v_max_u32_dpp v99, v99, v99 row_shr:1 row_mask:0xf bank_mask:0xf
s_nop 1
v_max_u32_dpp v99, v99, v99 row_shr:2 row_mask:0xf bank_mask:0xf
s_nop 1
v_max_u32_dpp v99, v99, v99 row_shr:4 row_mask:0xf bank_mask:0xf
s_nop 1
v_max_u32_dpp v99, v99, v99 row_shr:8 row_mask:0xf bank_mask:0xf
s_nop 1
v_max_u32_dpp v99, v99, v99 row_bcast:15 row_mask:0xa bank_mask:0xf
s_nop 1
v_max_u32_dpp v99, v99, v99 row_bcast:31 row_mask:0xc bank_mask:0xf
s_nop 0
v_readlane_b32 s86, v99, 63
s_nop 1
v_cmp_eq_u32 vcc, s86, v96
s_and_b32 s87, s86, 0xffff
s_sub_u32 s87, 2047, s87
v_cndmask_b32 v96, v96, v97, vcc
v_cndmask_b32_e64 v97, v97, 0, vcc
v_writelane_b32 v100, s87, 2
v_mov_b32 v99, v96
s_nop 1
v_max_u32_dpp v99, v99, v99 row_shr:1 row_mask:0xf bank_mask:0xf
s_nop 1
v_max_u32_dpp v99, v99, v99 row_shr:2 row_mask:0xf bank_mask:0xf
s_nop 1
v_max_u32_dpp v99, v99, v99 row_shr:4 row_mask:0xf bank_mask:0xf
s_nop 1
v_max_u32_dpp v99, v99, v99 row_shr:8 row_mask:0xf bank_mask:0xf
s_nop 1
v_max_u32_dpp v99, v99, v99 row_bcast:15 row_mask:0xa bank_mask:0xf
s_nop 1
v_max_u32_dpp v99, v99, v99 row_bcast:31 row_mask:0xc bank_mask:0xf
s_nop 0
v_readlane_b32 s86, v99, 63
s_nop 1
v_cmp_eq_u32 vcc, s86, v96
s_and_b32 s87, s86, 0xffff
s_sub_u32 s87, 2047, s87
v_cndmask_b32 v96, v96, v97, vcc
v_cndmask_b32_e64 v97, v97, 0, vcc
v_writelane_b32 v100, s87, 3
v_mov_b32 v99, v96
s_nop 1
v_max_u32_dpp v99, v99, v99 row_shr:1 row_mask:0xf bank_mask:0xf
s_nop 1
v_max_u32_dpp v99, v99, v99 row_shr:2 row_mask:0xf bank_mask:0xf
s_nop 1
v_max_u32_dpp v99, v99, v99 row_shr:4 row_mask:0xf bank_mask:0xf
s_nop 1
v_max_u32_dpp v99, v99, v99 row_shr:8 row_mask:0xf bank_mask:0xf
s_nop 1
v_max_u32_dpp v99, v99, v99 row_bcast:15 row_mask:0xa bank_mask:0xf
s_nop 1
v_max_u32_dpp v99, v99, v99 row_bcast:31 row_mask:0xc bank_mask:0xf
s_nop 0
v_readlane_b32 s86, v99, 63
s_nop 1
v_cmp_eq_u32 vcc, s86, v96
s_and_b32 s87, s86, 0xffff
s_sub_u32 s87, 2047, s87
v_cndmask_b32 v96, v96, v97, vcc
v_cndmask_b32_e64 v97, v97, 0, vcc
v_writelane_b32 v100, s87, 4
v_mov_b32 v99, v96
s_nop 1
v_max_u32_dpp v99, v99, v99 row_shr:1 row_mask:0xf bank_mask:0xf
s_nop 1
v_max_u32_dpp v99, v99, v99 row_shr:2 row_mask:0xf bank_mask:0xf
s_nop 1
v_max_u32_dpp v99, v99, v99 row_shr:4 row_mask:0xf bank_mask:0xf
s_nop 1
v_max_u32_dpp v99, v99, v99 row_shr:8 row_mask:0xf bank_mask:0xf
s_nop 1
v_max_u32_dpp v99, v99, v99 row_bcast:15 row_mask:0xa bank_mask:0xf
s_nop 1
v_max_u32_dpp v99, v99, v99 row_bcast:31 row_mask:0xc bank_mask:0xf
s_nop 0
v_readlane_b32 s86, v99, 63
s_nop 1
v_cmp_eq_u32 vcc, s86, v96
s_and_b32 s87, s86, 0xffff
s_sub_u32 s87, 2047, s87
v_cndmask_b32 v96, v96, v97, vcc
v_cndmask_b32_e64 v97, v97, 0, vcc
v_writelane_b32 v100, s87, 5
v_mov_b32 v99, v96
s_nop 1
v_max_u32_dpp v99, v99, v99 row_shr:1 row_mask:0xf bank_mask:0xf
s_nop 1
v_max_u32_dpp v99, v99, v99 row_shr:2 row_mask:0xf bank_mask:0xf
s_nop 1
v_max_u32_dpp v99, v99, v99 row_shr:4 row_mask:0xf bank_mask:0xf
s_nop 1
v_max_u32_dpp v99, v99, v99 row_shr:8 row_mask:0xf bank_mask:0xf
s_nop 1
v_max_u32_dpp v99, v99, v99 row_bcast:15 row_mask:0xa bank_mask:0xf
s_nop 1
v_max_u32_dpp v99, v99, v99 row_bcast:31 row_mask:0xc bank_mask:0xf
s_nop 0
v_readlane_b32 s86, v99, 63
s_nop 1
v_cmp_eq_u32 vcc, s86, v96
s_and_b32 s87, s86, 0xffff
s_sub_u32 s87, 2047, s87
v_cndmask_b32 v96, v96, v97, vcc
v_cndmask_b32_e64 v97, v97, 0, vcc
v_writelane_b32 v100, s87, 6
v_mov_b32 v99, v96
s_nop 1
v_max_u32_dpp v99, v99, v99 row_shr:1 row_mask:0xf bank_mask:0xf
s_nop 1
v_max_u32_dpp v99, v99, v99 row_shr:2 row_mask:0xf bank_mask:0xf
s_nop 1
v_max_u32_dpp v99, v99, v99 row_shr:4 row_mask:0xf bank_mask:0xf
s_nop 1
v_max_u32_dpp v99, v99, v99 row_shr:8 row_mask:0xf bank_mask:0xf
s_nop 1
v_max_u32_dpp v99, v99, v99 row_bcast:15 row_mask:0xa bank_mask:0xf
s_nop 1
v_max_u32_dpp v99, v99, v99 row_bcast:31 row_mask:0xc bank_mask:0xf
s_nop 0
v_readlane_b32 s86, v99, 63
s_nop 1
v_cmp_eq_u32 vcc, s86, v96
s_and_b32 s87, s86, 0xffff
s_sub_u32 s87, 2047, s87
v_cndmask_b32 v96, v96, v97, vcc
v_cndmask_b32_e64 v97, v97, 0, vcc
v_writelane_b32 v100, s87, 7
v_mov_b32 v99, v98
s_nop 1
v_max_u32_dpp v99, v99, v99 row_shr:1 row_mask:0xf bank_mask:0xf
s_nop 1
v_max_u32_dpp v99, v99, v99 row_shr:2 row_mask:0xf bank_mask:0xf
s_nop 1
v_max_u32_dpp v99, v99, v99 row_shr:4 row_mask:0xf bank_mask:0xf
s_nop 1
v_max_u32_dpp v99, v99, v99 row_shr:8 row_mask:0xf bank_mask:0xf
s_nop 1
v_max_u32_dpp v99, v99, v99 row_bcast:15 row_mask:0xa bank_mask:0xf
s_nop 1
v_max_u32_dpp v99, v99, v99 row_bcast:31 row_mask:0xc bank_mask:0xf
s_nop 0
v_readlane_b32 s87, v99, 63
s_nop 0
s_cmp_gt_u32 s87, s86
s_cbranch_scc0 .LL_29%=
s_mov_b32 m0, 0
.LL_30%=:
v_max3_u32 v96, v32, v34, v36
v_max3_u32 v96, v96, v38, v40
v_max3_u32 v96, v96, v42, v44
v_max3_u32 v96, v96, v46, v48
v_max3_u32 v96, v96, v50, v52
v_max3_u32 v96, v96, v54, v56
v_max3_u32 v96, v96, v58, v60
v_max3_u32 v96, v96, v62, v64
v_max3_u32 v96, v96, v66, v68
v_max3_u32 v96, v96, v70, v72
v_max3_u32 v96, v96, v74, v76
v_max3_u32 v96, v96, v78, v80
v_max3_u32 v96, v96, v82, v84
v_max3_u32 v96, v96, v86, v88
v_max3_u32 v96, v96, v90, v92
v_max_u32 v96, v96, v94
v_mov_b32 v99, v96
s_nop 1
v_max_u32_dpp v99, v99, v99 row_shr:1 row_mask:0xf bank_mask:0xf
s_nop 1
v_max_u32_dpp v99, v99, v99 row_shr:2 row_mask:0xf bank_mask:0xf
s_nop 1
v_max_u32_dpp v99, v99, v99 row_shr:4 row_mask:0xf bank_mask:0xf
s_nop 1
v_max_u32_dpp v99, v99, v99 row_shr:8 row_mask:0xf bank_mask:0xf
s_nop 1
v_max_u32_dpp v99, v99, v99 row_bcast:15 row_mask:0xa bank_mask:0xf
s_nop 1
v_max_u32_dpp v99, v99, v99 row_bcast:31 row_mask:0xc bank_mask:0xf
s_nop 0
v_readlane_b32 s86, v99, 63
s_nop 1
v_cmp_eq_u32 vcc, s86, v32
s_nop 1
v_cndmask_b32_e64 v32, v32, 0, vcc
v_cmp_eq_u32 vcc, s86, v34
s_nop 1
v_cndmask_b32_e64 v34, v34, 0, vcc
v_cmp_eq_u32 vcc, s86, v36
s_nop 1
v_cndmask_b32_e64 v36, v36, 0, vcc
v_cmp_eq_u32 vcc, s86, v38
s_nop 1
v_cndmask_b32_e64 v38, v38, 0, vcc
v_cmp_eq_u32 vcc, s86, v40
s_nop 1
v_cndmask_b32_e64 v40, v40, 0, vcc
v_cmp_eq_u32 vcc, s86, v42
s_nop 1
v_cndmask_b32_e64 v42, v42, 0, vcc
v_cmp_eq_u32 vcc, s86, v44
s_nop 1
v_cndmask_b32_e64 v44, v44, 0, vcc
v_cmp_eq_u32 vcc, s86, v46
s_nop 1
v_cndmask_b32_e64 v46, v46, 0, vcc
v_cmp_eq_u32 vcc, s86, v48
s_nop 1
v_cndmask_b32_e64 v48, v48, 0, vcc
v_cmp_eq_u32 vcc, s86, v50
s_nop 1
v_cndmask_b32_e64 v50, v50, 0, vcc
v_cmp_eq_u32 vcc, s86, v52
s_nop 1
v_cndmask_b32_e64 v52, v52, 0, vcc
v_cmp_eq_u32 vcc, s86, v54
s_nop 1
v_cndmask_b32_e64 v54, v54, 0, vcc
v_cmp_eq_u32 vcc, s86, v56
s_nop 1
v_cndmask_b32_e64 v56, v56, 0, vcc
v_cmp_eq_u32 vcc, s86, v58
s_nop 1
v_cndmask_b32_e64 v58, v58, 0, vcc
v_cmp_eq_u32 vcc, s86, v60
s_nop 1
v_cndmask_b32_e64 v60, v60, 0, vcc
v_cmp_eq_u32 vcc, s86, v62
s_nop 1
v_cndmask_b32_e64 v62, v62, 0, vcc
v_cmp_eq_u32 vcc, s86, v64
s_nop 1
v_cndmask_b32_e64 v64, v64, 0, vcc
v_cmp_eq_u32 vcc, s86, v66
s_nop 1
v_cndmask_b32_e64 v66, v66, 0, vcc
v_cmp_eq_u32 vcc, s86, v68
s_nop 1
v_cndmask_b32_e64 v68, v68, 0, vcc
v_cmp_eq_u32 vcc, s86, v70
s_nop 1
v_cndmask_b32_e64 v70, v70, 0, vcc
v_cmp_eq_u32 vcc, s86, v72
s_nop 1
v_cndmask_b32_e64 v72, v72, 0, vcc
v_cmp_eq_u32 vcc, s86, v74
s_nop 1
v_cndmask_b32_e64 v74, v74, 0, vcc
v_cmp_eq_u32 vcc, s86, v76
s_nop 1
v_cndmask_b32_e64 v76, v76, 0, vcc
v_cmp_eq_u32 vcc, s86, v78
s_nop 1
v_cndmask_b32_e64 v78, v78, 0, vcc
v_cmp_eq_u32 vcc, s86, v80
s_nop 1
v_cndmask_b32_e64 v80, v80, 0, vcc
v_cmp_eq_u32 vcc, s86, v82
s_nop 1
v_cndmask_b32_e64 v82, v82, 0, vcc
v_cmp_eq_u32 vcc, s86, v84
s_nop 1
v_cndmask_b32_e64 v84, v84, 0, vcc
v_cmp_eq_u32 vcc, s86, v86
s_nop 1
v_cndmask_b32_e64 v86, v86, 0, vcc
v_cmp_eq_u32 vcc, s86, v88
s_nop 1
v_cndmask_b32_e64 v88, v88, 0, vcc
v_cmp_eq_u32 vcc, s86, v90
s_nop 1
v_cndmask_b32_e64 v90, v90, 0, vcc
v_cmp_eq_u32 vcc, s86, v92
s_nop 1
v_cndmask_b32_e64 v92, v92, 0, vcc
v_cmp_eq_u32 vcc, s86, v94
s_nop 1
v_cndmask_b32_e64 v94, v94, 0, vcc
s_and_b32 s87, s86, 0xffff
s_sub_u32 s87, 2047, s87
s_nop 0
v_writelane_b32 v100, s87, m0
s_add_u32 m0, m0, 1
s_cmp_lt_u32 m0, 8
s_cbranch_scc1 .LL_30%=
.LL_29%=:
v_xor_b32 v102, s12, v100
v_mov_b32 v103, s13
s_mov_b64 exec, 0xff
buffer_store_dwordx2 v[102:103], v136, s[8:11], 0 offen offset:3584 sc1
s_mov_b64 exec, -1
s_branch .Lrtr_26%=
.Lspin_51%=:
s_mov_b32 s84, 0
.Llp_53%=:
s_sleep 2
buffer_load_dwordx2 v[8:9], v11, s[8:11], 0 offen offset:3840 sc1
s_waitcnt vmcnt(0)
s_add_u32 s84, s84, 1
v_cmp_eq_u64 vcc, s[12:13], v[8:9]
s_cmp_eq_u64 vcc, -1
s_cbranch_scc1 .Lok_52%=
s_cmp_lt_u32 s84, 1048576
s_cbranch_scc1 .Llp_53%=
s_branch .Lok_52%=
.Lredo%=:
s_waitcnt vmcnt(0) lgkmcnt(0)
s_barrier
s_and_b32 s81, s14, 1
s_mul_i32 s91, s81, 17164288
s_mul_i32 s82, s56, 536576
s_add_u32 s91, s91, s82
s_add_u32 s91, s91, 4194304
s_lshr_b32 s82, s14, 1
s_lshl_b32 s83, s82, 14
s_add_u32 s91, s91, s83
s_lshl_b32 s81, s81, 14
s_lshl_b32 s82, s82, 16
s_or_b32 s92, s81, s82
s_xor_b32 s92, s92, 0x10000
s_add_u32 m0, s92, 0
s_add_u32 s81, s91, 0
Dc 1,8,81,0
Dc 1,8,81,1024
Dc 1,8,81,2048
Dc 1,8,81,3072
s_add_u32 m0, s92, 4096
s_add_u32 s81, s91, 4096
Dc 1,8,81,0
Dc 1,8,81,1024
Dc 1,8,81,2048
Dc 1,8,81,3072
s_add_u32 m0, s92, 8192
s_add_u32 s81, s91, 8192
Dc 1,8,81,0
Dc 1,8,81,1024
Dc 1,8,81,2048
Dc 1,8,81,3072
s_add_u32 m0, s92, 12288
s_add_u32 s81, s91, 12288
Dc 1,8,81,0
Dc 1,8,81,1024
Dc 1,8,81,2048
Dc 1,8,81,3072
v_bfe_u32 v244, v1, 4, 1
v_mul_u32_u24 v244, 0xaaaa, v244
v_add_u32 v244, 0x4444, v244
v_lshl_or_b32 v240, v244, 16, v244
v_mov_b32 v241, 0xff800000
v_add_u32 v242, 0x10000, v1
v_bfe_u32 v244, v1, 4, 4
v_lshrrev_b32 v245, 8, v1
v_lshlrev_b32 v245, 2, v245
v_sub_u32 v244, v244, v245
v_cmp_le_i32_e64 s[72:73], 0, v244
v_cmp_le_i32_e64 s[74:75], 1, v244
v_cmp_le_i32_e64 s[76:77], 2, v244
v_cmp_le_i32_e64 s[78:79], 3, v244
v_add_u32 v243, 4096, v1
v_add_u32 v244, 8192, v1
v_add_u32 v245, 12288, v1
s_mov_b32 s18, 0x3f803f80
s_mov_b32 s19, 0x3f003f00
v_accvgpr_write_b32 a176, s18
v_accvgpr_write_b32 a180, s19
v_mov_b32 v252, v241
v_accvgpr_write_b32 a177, s18
v_accvgpr_write_b32 a181, s19
v_mov_b32 v253, v241
v_accvgpr_write_b32 a178, s18
v_accvgpr_write_b32 a182, s19
v_mov_b32 v254, v241
v_accvgpr_write_b32 a179, s18
v_accvgpr_write_b32 a183, s19
v_mov_b32 v255, v241
)"
R"(v_accvgpr_write_b32 a160, 0
v_accvgpr_write_b32 a161, 0
v_accvgpr_write_b32 a162, 0
v_accvgpr_write_b32 a163, 0
v_accvgpr_write_b32 a164, 0
v_accvgpr_write_b32 a165, 0
v_accvgpr_write_b32 a166, 0
v_accvgpr_write_b32 a167, 0
v_accvgpr_write_b32 a168, 0
v_accvgpr_write_b32 a169, 0
v_accvgpr_write_b32 a170, 0
v_accvgpr_write_b32 a171, 0
v_accvgpr_write_b32 a172, 0
v_accvgpr_write_b32 a173, 0
v_accvgpr_write_b32 a174, 0
v_accvgpr_write_b32 a175, 0
s_mov_b32 s90, 0x5d800000
s_mov_b64 s[88:89], 0
s_mov_b32 s64, 0
s_and_b32 s18, s14, 1
s_lshl_b32 s62, s18, 14
s_lshl_b32 s20, s57, 15
s_sub_u32 s20, 507904, s20
s_add_u32 s20, s20, s62
s_lshl_b32 s65, s57, 1
s_sub_u32 s65, s65, 1
s_max_i32 s65, s65, 0
s_sub_u32 s66, s60, 1
s_cmp_lt_u32 s14, 2
s_cselect_b32 s20, s20, 0
s_cselect_b32 s66, s66, 15
s_cselect_b32 s21, 32768, 0
s_add_u32 s61, s91, s20
s_add_u32 s62, s62, s21
s_sub_u32 s63, 16384, s21
s_lshl_b32 s67, s60, 7
s_add_u32 s67, s67, s56
s_lshl_b32 s67, s67, 12
s_add_u32 s67, s67, 39845888
s_lshl_b32 s69, s60, 3
s_add_u32 s69, s69, s59
s_lshl_b32 s69, s69, 6
s_lshl_b32 s21, s58, 3
s_add_u32 s69, s69, s21
s_add_u32 s69, s69, 20480
s_mov_b32 s70, 0
s_mov_b32 s37, 0
v_bfe_u32 v28, v1, 4, 4
v_lshrrev_b32 v29, 8, v1
v_lshlrev_b32 v29, 2, v29
v_sub_u32 v28, v28, v29
s_mov_b64 s[24:25], 0
s_mov_b32 s68, 0
s_mov_b32 s85, 99
s_lshl_b32 s80, s60, 6
s_and_b32 s81, s14, 2
s_lshl_b32 s81, s81, 4
s_xor_b32 s82, s81, 0
s_add_u32 s82, s82, s80
v_add_u32 v4, s82, v28
s_xor_b32 s82, s81, 16
s_add_u32 s82, s82, s80
v_add_u32 v5, s82, v28
s_xor_b32 s82, s81, 32
s_add_u32 s82, s82, s80
v_add_u32 v6, s82, v28
s_xor_b32 s82, s81, 48
s_add_u32 s82, s82, s80
v_add_u32 v7, s82, v28
s_mul_i32 s82, s56, 536576
s_add_u32 s82, s82, 4194304
s_lshl_b32 s84, s60, 1
s_add_u32 s84, s84, 2
.LL_54%=:
buffer_load_dwordx4 a[128:131], v1, s[8:11], s82 offen offset:0 sc1
buffer_load_dwordx4 a[132:135], v1, s[8:11], s82 offen offset:1024 sc1
buffer_load_dwordx4 a[136:139], v1, s[8:11], s82 offen offset:2048 sc1
buffer_load_dwordx4 a[140:143], v1, s[8:11], s82 offen offset:3072 sc1
buffer_load_dwordx4 a[144:147], v243, s[8:11], s82 offen offset:0 sc1
buffer_load_dwordx4 a[148:151], v243, s[8:11], s82 offen offset:1024 sc1
buffer_load_dwordx4 a[152:155], v243, s[8:11], s82 offen offset:2048 sc1
buffer_load_dwordx4 a[156:159], v243, s[8:11], s82 offen offset:3072 sc1
s_waitcnt vmcnt(0)
Zvaa 160,128,0
Zvaa 164,128,16
Zvaa 168,128,32
Zvaa 172,128,48
Mvaa 160,132,4
Mvaa 164,132,20
Mvaa 168,132,36
Mvaa 172,132,52
Mvaa 160,136,8
Mvaa 164,136,24
Mvaa 168,136,40
Mvaa 172,136,56
Mvaa 160,140,12
Mvaa 164,140,28
Mvaa 168,140,44
Mvaa 172,140,60
Zvaa 176,144,0
Zvaa 180,144,16
Zvaa 184,144,32
Zvaa 188,144,48
Mvaa 176,148,4
Mvaa 180,148,20
Mvaa 184,148,36
Mvaa 188,148,52
Mvaa 176,152,8
Mvaa 180,152,24
Mvaa 184,152,40
Mvaa 188,152,56
Mvaa 176,156,12
Mvaa 180,156,28
Mvaa 184,156,44
Mvaa 188,156,60
v_cmp_le_i32 vcc, 0, v4
s_nop 1
v_cndmask_b32 v160, v241, v160, vcc
v_max_f32 v252, v252, v160
v_cmp_le_i32 vcc, 1, v4
s_nop 1
v_cndmask_b32 v161, v241, v161, vcc
v_max_f32 v252, v252, v161
v_cmp_le_i32 vcc, 2, v4
s_nop 1
v_cndmask_b32 v162, v241, v162, vcc
v_max_f32 v252, v252, v162
v_cmp_le_i32 vcc, 3, v4
s_nop 1
v_cndmask_b32 v163, v241, v163, vcc
v_max_f32 v252, v252, v163
v_cmp_le_i32 vcc, 0, v5
s_nop 1
v_cndmask_b32 v164, v241, v164, vcc
v_max_f32 v253, v253, v164
v_cmp_le_i32 vcc, 1, v5
s_nop 1
v_cndmask_b32 v165, v241, v165, vcc
v_max_f32 v253, v253, v165
v_cmp_le_i32 vcc, 2, v5
s_nop 1
v_cndmask_b32 v166, v241, v166, vcc
v_max_f32 v253, v253, v166
v_cmp_le_i32 vcc, 3, v5
s_nop 1
v_cndmask_b32 v167, v241, v167, vcc
v_max_f32 v253, v253, v167
v_cmp_le_i32 vcc, 0, v6
s_nop 1
v_cndmask_b32 v168, v241, v168, vcc
v_max_f32 v254, v254, v168
v_cmp_le_i32 vcc, 1, v6
s_nop 1
v_cndmask_b32 v169, v241, v169, vcc
v_max_f32 v254, v254, v169
v_cmp_le_i32 vcc, 2, v6
s_nop 1
v_cndmask_b32 v170, v241, v170, vcc
v_max_f32 v254, v254, v170
v_cmp_le_i32 vcc, 3, v6
s_nop 1
v_cndmask_b32 v171, v241, v171, vcc
v_max_f32 v254, v254, v171
v_cmp_le_i32 vcc, 0, v7
s_nop 1
v_cndmask_b32 v172, v241, v172, vcc
v_max_f32 v255, v255, v172
v_cmp_le_i32 vcc, 1, v7
s_nop 1
v_cndmask_b32 v173, v241, v173, vcc
v_max_f32 v255, v255, v173
v_cmp_le_i32 vcc, 2, v7
s_nop 1
v_cndmask_b32 v174, v241, v174, vcc
v_max_f32 v255, v255, v174
v_cmp_le_i32 vcc, 3, v7
s_nop 1
v_cndmask_b32 v175, v241, v175, vcc
v_max_f32 v255, v255, v175
v_cmp_le_i32 vcc, 16, v4
s_nop 1
v_cndmask_b32 v176, v241, v176, vcc
v_max_f32 v252, v252, v176
v_cmp_le_i32 vcc, 17, v4
s_nop 1
v_cndmask_b32 v177, v241, v177, vcc
v_max_f32 v252, v252, v177
v_cmp_le_i32 vcc, 18, v4
s_nop 1
v_cndmask_b32 v178, v241, v178, vcc
v_max_f32 v252, v252, v178
v_cmp_le_i32 vcc, 19, v4
s_nop 1
v_cndmask_b32 v179, v241, v179, vcc
v_max_f32 v252, v252, v179
v_cmp_le_i32 vcc, 16, v5
s_nop 1
v_cndmask_b32 v180, v241, v180, vcc
v_max_f32 v253, v253, v180
v_cmp_le_i32 vcc, 17, v5
s_nop 1
v_cndmask_b32 v181, v241, v181, vcc
v_max_f32 v253, v253, v181
v_cmp_le_i32 vcc, 18, v5
s_nop 1
v_cndmask_b32 v182, v241, v182, vcc
v_max_f32 v253, v253, v182
v_cmp_le_i32 vcc, 19, v5
s_nop 1
v_cndmask_b32 v183, v241, v183, vcc
v_max_f32 v253, v253, v183
v_cmp_le_i32 vcc, 16, v6
s_nop 1
v_cndmask_b32 v184, v241, v184, vcc
v_max_f32 v254, v254, v184
v_cmp_le_i32 vcc, 17, v6
s_nop 1
v_cndmask_b32 v185, v241, v185, vcc
v_max_f32 v254, v254, v185
v_cmp_le_i32 vcc, 18, v6
s_nop 1
v_cndmask_b32 v186, v241, v186, vcc
v_max_f32 v254, v254, v186
v_cmp_le_i32 vcc, 19, v6
s_nop 1
v_cndmask_b32 v187, v241, v187, vcc
v_max_f32 v254, v254, v187
v_cmp_le_i32 vcc, 16, v7
s_nop 1
v_cndmask_b32 v188, v241, v188, vcc
v_max_f32 v255, v255, v188
v_cmp_le_i32 vcc, 17, v7
s_nop 1
v_cndmask_b32 v189, v241, v189, vcc
v_max_f32 v255, v255, v189
v_cmp_le_i32 vcc, 18, v7
s_nop 1
v_cndmask_b32 v190, v241, v190, vcc
v_max_f32 v255, v255, v190
v_cmp_le_i32 vcc, 19, v7
s_nop 1
v_cndmask_b32 v191, v241, v191, vcc
v_max_f32 v255, v255, v191
v_subrev_u32 v4, 32, v4
v_subrev_u32 v5, 32, v5
v_subrev_u32 v6, 32, v6
v_subrev_u32 v7, 32, v7
s_add_u32 s82, s82, 8192
s_sub_u32 s84, s84, 1
s_cmp_lg_u32 s84, 0
s_cbranch_scc1 .LL_54%=
G 28,0
v_lshlrev_b32 v32, 16, v28
v_and_b32 v33, 0xffff0000, v28
G 28,1
v_lshlrev_b32 v34, 16, v28
v_and_b32 v35, 0xffff0000, v28
G 28,2
v_lshlrev_b32 v36, 16, v28
v_and_b32 v37, 0xffff0000, v28
G 28,3
v_lshlrev_b32 v38, 16, v28
v_and_b32 v39, 0xffff0000, v28
G 28,4
v_lshlrev_b32 v40, 16, v28
v_and_b32 v41, 0xffff0000, v28
G 28,5
v_lshlrev_b32 v42, 16, v28
v_and_b32 v43, 0xffff0000, v28
G 28,6
v_lshlrev_b32 v44, 16, v28
v_and_b32 v45, 0xffff0000, v28
G 28,7
v_lshlrev_b32 v46, 16, v28
v_and_b32 v47, 0xffff0000, v28
G 28,8
v_lshlrev_b32 v48, 16, v28
v_and_b32 v49, 0xffff0000, v28
G 28,9
v_lshlrev_b32 v50, 16, v28
v_and_b32 v51, 0xffff0000, v28
G 28,10
v_lshlrev_b32 v52, 16, v28
v_and_b32 v53, 0xffff0000, v28
G 28,11
v_lshlrev_b32 v54, 16, v28
v_and_b32 v55, 0xffff0000, v28
G 28,12
v_lshlrev_b32 v56, 16, v28
v_and_b32 v57, 0xffff0000, v28
G 28,13
v_lshlrev_b32 v58, 16, v28
v_and_b32 v59, 0xffff0000, v28
G 28,14
v_lshlrev_b32 v60, 16, v28
v_and_b32 v61, 0xffff0000, v28
G 28,15
v_lshlrev_b32 v62, 16, v28
v_and_b32 v63, 0xffff0000, v28
G 28,16
v_lshlrev_b32 v64, 16, v28
v_and_b32 v65, 0xffff0000, v28
G 28,17
v_lshlrev_b32 v66, 16, v28
v_and_b32 v67, 0xffff0000, v28
G 28,18
v_lshlrev_b32 v68, 16, v28
v_and_b32 v69, 0xffff0000, v28
G 28,19
v_lshlrev_b32 v70, 16, v28
v_and_b32 v71, 0xffff0000, v28
G 28,20
v_lshlrev_b32 v72, 16, v28
v_and_b32 v73, 0xffff0000, v28
G 28,21
v_lshlrev_b32 v74, 16, v28
v_and_b32 v75, 0xffff0000, v28
G 28,22
v_lshlrev_b32 v76, 16, v28
v_and_b32 v77, 0xffff0000, v28
G 28,23
v_lshlrev_b32 v78, 16, v28
v_and_b32 v79, 0xffff0000, v28
G 28,24
v_lshlrev_b32 v80, 16, v28
v_and_b32 v81, 0xffff0000, v28
G 28,25
v_lshlrev_b32 v82, 16, v28
v_and_b32 v83, 0xffff0000, v28
G 28,26
v_lshlrev_b32 v84, 16, v28
v_and_b32 v85, 0xffff0000, v28
G 28,27
v_lshlrev_b32 v86, 16, v28
v_and_b32 v87, 0xffff0000, v28
G 28,28
v_lshlrev_b32 v88, 16, v28
v_and_b32 v89, 0xffff0000, v28
G 28,29
v_lshlrev_b32 v90, 16, v28
v_and_b32 v91, 0xffff0000, v28
G 28,30
v_lshlrev_b32 v92, 16, v28
v_and_b32 v93, 0xffff0000, v28
G 28,31
v_lshlrev_b32 v94, 16, v28
v_and_b32 v95, 0xffff0000, v28
G 28,32
v_lshlrev_b32 v96, 16, v28
v_and_b32 v97, 0xffff0000, v28
G 28,33
v_lshlrev_b32 v98, 16, v28
v_and_b32 v99, 0xffff0000, v28
G 28,34
v_lshlrev_b32 v100, 16, v28
v_and_b32 v101, 0xffff0000, v28
G 28,35
v_lshlrev_b32 v102, 16, v28
v_and_b32 v103, 0xffff0000, v28
G 28,36
v_lshlrev_b32 v104, 16, v28
v_and_b32 v105, 0xffff0000, v28
G 28,37
v_lshlrev_b32 v106, 16, v28
v_and_b32 v107, 0xffff0000, v28
G 28,38
v_lshlrev_b32 v108, 16, v28
v_and_b32 v109, 0xffff0000, v28
G 28,39
v_lshlrev_b32 v110, 16, v28
v_and_b32 v111, 0xffff0000, v28
G 28,40
v_lshlrev_b32 v112, 16, v28
v_and_b32 v113, 0xffff0000, v28
G 28,41
v_lshlrev_b32 v114, 16, v28
v_and_b32 v115, 0xffff0000, v28
G 28,42
v_lshlrev_b32 v116, 16, v28
v_and_b32 v117, 0xffff0000, v28
G 28,43
v_lshlrev_b32 v118, 16, v28
v_and_b32 v119, 0xffff0000, v28
G 28,44
v_lshlrev_b32 v120, 16, v28
v_and_b32 v121, 0xffff0000, v28
G 28,45
v_lshlrev_b32 v122, 16, v28
v_and_b32 v123, 0xffff0000, v28
G 28,46
v_lshlrev_b32 v124, 16, v28
v_and_b32 v125, 0xffff0000, v28
G 28,47
v_lshlrev_b32 v126, 16, v28
v_and_b32 v127, 0xffff0000, v28
G 28,48
v_lshlrev_b32 v128, 16, v28
v_and_b32 v129, 0xffff0000, v28
G 28,49
v_lshlrev_b32 v130, 16, v28
)"
R"(v_and_b32 v131, 0xffff0000, v28
G 28,50
v_lshlrev_b32 v132, 16, v28
v_and_b32 v133, 0xffff0000, v28
G 28,51
v_lshlrev_b32 v134, 16, v28
v_and_b32 v135, 0xffff0000, v28
G 28,52
v_lshlrev_b32 v136, 16, v28
v_and_b32 v137, 0xffff0000, v28
G 28,53
v_lshlrev_b32 v138, 16, v28
v_and_b32 v139, 0xffff0000, v28
G 28,54
v_lshlrev_b32 v140, 16, v28
v_and_b32 v141, 0xffff0000, v28
G 28,55
v_lshlrev_b32 v142, 16, v28
v_and_b32 v143, 0xffff0000, v28
G 28,56
v_lshlrev_b32 v144, 16, v28
v_and_b32 v145, 0xffff0000, v28
G 28,57
v_lshlrev_b32 v146, 16, v28
v_and_b32 v147, 0xffff0000, v28
G 28,58
v_lshlrev_b32 v148, 16, v28
v_and_b32 v149, 0xffff0000, v28
G 28,59
v_lshlrev_b32 v150, 16, v28
v_and_b32 v151, 0xffff0000, v28
G 28,60
v_lshlrev_b32 v152, 16, v28
v_and_b32 v153, 0xffff0000, v28
G 28,61
v_lshlrev_b32 v154, 16, v28
v_and_b32 v155, 0xffff0000, v28
G 28,62
v_lshlrev_b32 v156, 16, v28
v_and_b32 v157, 0xffff0000, v28
G 28,63
v_lshlrev_b32 v158, 16, v28
v_and_b32 v159, 0xffff0000, v28
s_mov_b32 s22, 1.0
s_mov_b32 s23, 1.0
s_mov_b32 s29, 134217728
s_mov_b32 s71, 3584
s_barrier
Ra 128,242,0
Ra 132,242,1024
Ra 136,242,2048
Ra 140,242,3072
s_cmp_ge_u32 s14, 2
s_cbranch_scc1 .Lbgo%=
s_branch .Lago%=
.p2align 12
.Lpb_31%=:
s_waitcnt vmcnt(6) lgkmcnt(6)
Zvaa 32,0,32
s_add_u32 s28, s28, 512
Ta 20,4,8192
Ta 22,4,9216
Mvaa 32,4,48
s_cmp_eq_u32 s28, s4
s_cselect_b32 s93, 10240, 0
Ta 24,5,0
Ta 26,5,1024
Zvaa 64,4,52
s_sub_u32 s28, s28, s93
s_add_u32 s93, s28, 20480
buffer_load_dwordx4 a[128:131], v26, s[8:11], s28 offen offset:0 sc1
Mvaa 64,0,36
Ta 28,5,8192
Ta 30,5,9216
Zvaa 96,0,40
K 184,184,185
K 185,186,187
Mvaa 96,4,56
K 186,188,189
K 187,190,191
Zvaa 128,4,60
K 192,192,193
Mvaa 128,0,44
W 8
buffer_load_dwordx4 a[132:135], v26, s[8:11], s93 offen offset:0 sc1
K 193,194,195
Zvaa 132,8,44
Ta 0,6,0
Ta 2,6,1024
Mvaa 132,12,60
K 194,196,197
K 195,198,199
Zvaa 100,12,56
K 200,200,201
K 201,202,203
Mvaa 100,8,40
Ta 4,6,8192
Ta 6,6,9216
Zvaa 68,8,36
buffer_load_dwordx4 a[136:139], v27, s[8:11], s28 offen offset:0 sc1
K 202,204,205
Mvaa 68,12,52
K 203,206,207
K 208,208,209
Zvaa 36,12,48
K 209,210,211
K 210,212,213
Mvaa 36,8,32
W 8
Ta 8,7,0
K 211,214,215
Zvaa 40,16,32
Ta 10,7,1024
K 216,216,217
Mvaa 40,20,48
buffer_load_dwordx4 a[140:143], v27, s[8:11], s93 offen offset:0 sc1
K 217,218,219
Zvaa 72,20,52
s_add_u32 s28, s28, 512
s_cmp_eq_u32 s28, s4
s_cselect_b32 s93, 10240, 0
s_sub_u32 s28, s28, s93
Mvaa 72,16,36
s_add_u32 s93, s28, 20480
K 218,220,221
K 219,222,223
Zvaa 104,16,40
Ta 12,7,8192
Ta 14,7,9216
Mvaa 104,20,56
K 224,224,225
K 225,226,227
Zvaa 136,20,60
K 226,228,229
K 227,230,231
Mvaa 136,16,44
W 8
buffer_load_dwordx4 a[144:147], v26, s[8:11], s28 offen offset:0 sc1
K 232,232,233
Zvaa 140,24,44
K 233,234,235
K 234,236,237
Mvaa 140,28,60
Ta 16,8,0
K 235,238,239
Zvaa 108,28,56
Ta 18,8,1024
K 240,240,241
Mvaa 108,24,40
K 241,242,243
K 242,244,245
Zvaa 76,24,36
buffer_load_dwordx4 a[148:151], v26, s[8:11], s93 offen offset:0 sc1
K 243,246,247
Mvaa 76,28,52
Ta 20,8,8192
Ta 22,8,9216
Zvaa 44,28,48
K 248,248,249
K 249,250,251
Mvaa 44,24,32
K 250,252,253
K 251,254,255
W 8
Zvaa 48,0,32
Ta 24,9,0
Ta 26,9,1024
Mvaa 48,4,48
buffer_load_dwordx4 a[152:155], v27, s[8:11], s28 offen offset:0 sc1
K 160,160,161
Zvaa 80,4,52
K 161,162,163
K 162,164,165
Mvaa 80,0,36
Ta 28,9,8192
K 163,166,167
Zvaa 112,0,40
Ta 30,9,9216
K 168,168,169
Mvaa 112,4,56
K 169,170,171
K 170,172,173
Zvaa 144,4,60
buffer_load_dwordx4 a[156:159], v27, s[8:11], s93 offen offset:0 sc1
K 171,174,175
Mvaa 144,0,44
W 8
s_add_u32 s28, s28, 512
s_cmp_eq_u32 s28, s4
s_cselect_b32 s93, 10240, 0
Zvaa 148,8,44
s_sub_u32 s28, s28, s93
s_add_u32 s93, s28, 20480
G 164,184
Mvaa 148,12,60
G 165,185
G 166,186
Zvaa 116,12,56
Ta 0,2,16384
Ta 2,2,17408
Mvaa 116,8,40
buffer_load_dwordx4 v[188:191], v26, s[8:11], s28 offen offset:0 sc1
G 167,187
Zvaa 84,8,36
Ta 4,2,24576
G 172,188
Mvaa 84,12,52
Ta 6,2,25600
G 173,189
Zvaa 52,12,48
G 174,190
G 175,191
Mvaa 52,8,32
W 8
Ta 8,3,16384
K 164,164,165
Zvaa 56,16,32
buffer_load_dwordx4 v[196:199], v26, s[8:11], s93 offen offset:0 sc1
Ta 10,3,17408
Mvaa 56,20,48
K 165,166,167
K 166,172,173
Zvaa 88,20,52
Ta 12,3,24576
K 167,174,175
Mvaa 88,16,36
Ta 14,3,25600
v_accvgpr_write_b32 a184, v164
Zvaa 120,16,40
Mvaa 120,20,56
buffer_load_dwordx4 v[204:207], v27, s[8:11], s28 offen offset:0 sc1
v_accvgpr_write_b32 a185, v165
Zvaa 152,20,60
v_accvgpr_write_b32 a186, v166
Mvaa 152,16,44
W 8
Ta 16,4,16384
v_accvgpr_write_b32 a187, v167
Zvaa 156,24,44
Ta 18,4,17408
v_accvgpr_write_b32 a188, v240
Mvaa 156,28,60
Zvaa 124,28,56
buffer_load_dwordx4 v[212:215], v27, s[8:11], s93 offen offset:0 sc1
v_accvgpr_write_b32 a189, v241
Mvaa 124,24,40
s_add_u32 s28, s28, 512
s_cmp_eq_u32 s28, s4
s_cselect_b32 s93, 10240, 0
s_sub_u32 s28, s28, s93
Zvaa 92,24,36
s_add_u32 s93, s28, 20480
Ta 20,4,24576
Ta 22,4,25600
Mvaa 92,28,52
v_accvgpr_write_b32 a190, v242
v_accvgpr_write_b32 a191, v243
Zvaa 60,28,48
G 164,192
Mvaa 60,24,32
s_waitcnt vmcnt(4) lgkmcnt(8)
buffer_load_dwordx4 v[220:223], v26, s[8:11], s28 offen offset:0 sc1
G 165,193
Mvaa 32,0,128
Ta 24,5,16384
Ta 26,5,17408
Mvaa 32,4,144
G 166,194
G 167,195
Mvaa 64,4,148
Ta 28,5,24576
Ta 30,5,25600
Mvaa 64,0,132
G 172,196
Mvaa 96,0,136
buffer_load_dwordx4 v[228:231], v26, s[8:11], s93 offen offset:0 sc1
G 173,197
Mvaa 96,4,152
G 174,198
Mvaa 128,4,156
G 175,199
Mvaa 128,0,140
W 8
Ta 0,6,16384
Ta 2,6,17408
Mvaa 132,8,140
K 164,164,165
Mvaa 132,12,156
buffer_load_dwordx4 v[236:239], v27, s[8:11], s28 offen offset:0 sc1
K 165,166,167
Mvaa 100,12,152
Ta 4,6,24576
K 166,172,173
Mvaa 100,8,136
Ta 6,6,25600
K 167,174,175
Mvaa 68,8,132
v_accvgpr_write_b32 a192, v164
Mvaa 68,12,148
Mvaa 36,12,144
buffer_load_dwordx4 v[244:247], v27, s[8:11], s93 offen offset:0 sc1
v_accvgpr_write_b32 a193, v165
Mvaa 36,8,128
W 8
s_add_u32 s28, s28, 512
s_cmp_eq_u32 s28, s4
s_cselect_b32 s93, 10240, 0
Mvaa 40,16,128
s_sub_u32 s28, s28, s93
s_add_u32 s93, s28, 20480
v_accvgpr_write_b32 a194, v166
Mvaa 40,20,144
Ta 8,7,16384
Ta 10,7,17408
Mvaa 72,20,148
v_accvgpr_write_b32 a195, v167
v_accvgpr_write_b32 a196, v248
Mvaa 72,16,132
D 22,48,65,2048
Ta 12,7,24576
Mvaa 104,16,136
Ta 14,7,25600
v_accvgpr_write_b32 a197, v249
Mvaa 104,20,152
v_accvgpr_write_b32 a198, v250
v_accvgpr_write_b32 a199, v251
Mvaa 136,20,156
G 164,200
Mvaa 136,16,140
W 8
Ta 16,8,16384
Ta 18,8,17408
Mvaa 140,24,140
buffer_load_dwordx4 a[32:35], v26, s[8:11], s28 offen offset:0 sc1
G 165,201
Mvaa 140,28,156
G 166,202
Mvaa 108,28,152
Ta 20,8,24576
G 167,203
Mvaa 108,24,136
Ta 22,8,25600
G 172,204
Mvaa 76,24,132
Mvaa 76,28,148
buffer_load_dwordx4 a[36:39], v26, s[8:11], s93 offen offset:0 sc1
G 173,205
Mvaa 44,28,144
G 174,206
Mvaa 44,24,128
W 8
Ta 24,9,16384
G 175,207
Mvaa 48,0,128
Ta 26,9,17408
K 164,164,165
Mvaa 48,4,144
Mvaa 80,4,148
buffer_load_dwordx4 a[40:43], v27, s[8:11], s28 offen offset:0 sc1
K 165,166,167
Mvaa 80,0,132
Ta 28,9,24576
Ta 30,9,25600
Mvaa 112,0,136
s_lshl_b32 s80, s56, 9
K 166,172,173
K 167,174,175
Mvaa 112,4,152
v_accvgpr_write_b32 a200, v164
Mvaa 144,4,156
s_lshl_b32 s81, s57, 4
Mvaa 144,0,140
W 8
buffer_load_dwordx4 a[44:47], v27, s[8:11], s93 offen offset:0 sc1
v_accvgpr_write_b32 a201, v165
Mvaa 148,8,140
s_add_u32 s28, s28, 512
s_cmp_eq_u32 s28, s4
s_cselect_b32 s93, 10240, 0
s_sub_u32 s28, s28, s93
Mvaa 148,12,156
s_add_u32 s93, s28, 20480
s_add_u32 s80, s80, s81
Ta 0,10,0
Mvaa 116,12,152
s_and_b32 s81, s14, 1
Ta 2,10,1024
v_accvgpr_write_b32 a202, v166
Mvaa 116,8,136
s_lshl_b32 s81, s81, 3
Ta 4,10,8192
v_accvgpr_write_b32 a203, v167
Mvaa 84,8,132
s_add_u32 s80, s80, s81
buffer_load_dwordx4 a[48:51], v26, s[8:11], s28 offen offset:0 sc1
Ta 6,10,9216
Mvaa 84,12,148
s_add_u32 s71, s80, 4096
v_accvgpr_write_b32 a204, v184
v_accvgpr_write_b32 a205, v185
)"
R"(Mvaa 52,12,144
v_accvgpr_write_b32 a206, v186
v_accvgpr_write_b32 a207, v187
Mvaa 52,8,128
W 8
Ta 8,11,0
Ta 10,11,1024
Mvaa 56,16,128
G 164,208
Mvaa 56,20,144
D 23,48,65,3072
G 165,209
Mvaa 88,20,148
s_add_u32 s65, s5, 16
s_cmp_ge_u32 s65, 20
s_cselect_b32 s90, 20, 0
s_sub_u32 s65, s65, s90
Mvaa 88,16,132
s_lshl_b32 s65, s65, 18
s_add_u32 m0, s63, 98304
G 166,210
Mvaa 120,16,136
Ta 12,11,8192
Ta 14,11,9216
Mvaa 120,20,152
G 167,211
G 172,212
Mvaa 152,20,156
buffer_load_dwordx4 a[52:55], v26, s[8:11], s93 offen offset:0 sc1
G 173,213
Mvaa 152,16,140
W 8
Ta 16,12,0
G 174,214
Mvaa 156,24,140
s_lshr_b32 s80, s15, 3
Ta 18,12,1024
Mvaa 156,28,156
G 175,215
Mvaa 124,28,152
s_lshl_b32 s80, s80, 1
Ta 20,12,8192
K 164,164,165
Mvaa 124,24,136
s_add_u32 s80, s80, s14
buffer_load_dwordx4 a[56:59], v27, s[8:11], s28 offen offset:0 sc1
Ta 22,12,9216
Mvaa 92,24,132
s_sub_u32 s80, s80, 2
K 165,166,167
K 166,172,173
Mvaa 92,28,148
s_lshl_b32 s80, s80, 12
Mvaa 60,28,144
K 167,174,175
v_min_u32 v28, 496, v1
Mvaa 60,24,128
s_waitcnt vmcnt(9) lgkmcnt(8)
v_accvgpr_write_b32 a208, v164
v_lshl_add_u32 v28, v28, 3, s80
Mvav 32,0,188
buffer_load_dwordx4 a[60:63], v27, s[8:11], s93 offen offset:0 sc1
Ta 24,13,0
Mvav 32,4,220
s_add_u32 s28, s28, 512
s_cmp_eq_u32 s28, s4
s_cselect_b32 s93, 10240, 0
s_sub_u32 s28, s28, s93
Mvav 64,4,228
s_add_u32 s93, s28, 20480
Ta 26,13,1024
v_min_u32 v28, .Lend%=-.Lbeg%=-4, v28
Mvav 64,0,196
v_accvgpr_write_b32 a209, v165
v_accvgpr_write_b32 a210, v166
Mvav 96,0,204
Ta 28,13,8192
Ta 30,13,9216
Mvav 96,4,236
D 16,48,65,0
v_accvgpr_write_b32 a211, v167
Mvav 128,4,244
v_accvgpr_write_b32 a212, v192
v_accvgpr_write_b32 a213, v193
Mvav 128,0,212
W 8
Ta 0,14,0
v_accvgpr_write_b32 a214, v194
Mvav 132,8,212
Ta 2,14,1024
v_add_u32 v2, 65536, v10
Mvav 132,12,244
v_accvgpr_write_b32 a215, v195
G 164,216
Mvav 100,12,236
buffer_load_dwordx4 v[184:187], v26, s[8:11], s28 offen offset:0 sc1
Ta 4,14,8192
Mvav 100,8,204
Ta 6,14,9216
v_add_u32 v3, 65536, v11
Mvav 68,8,196
G 165,217
G 166,218
Mvav 68,12,228
G 167,219
Mvav 36,12,220
G 172,220
Mvav 36,8,188
W 8
D 17,48,65,1024
Ta 8,15,0
Mvav 40,16,188
Ta 10,15,1024
v_add_u32 v4, 65536, v12
Mvav 40,20,220
G 173,221
G 174,222
Mvav 72,20,228
Ta 12,15,8192
G 175,223
Mvav 72,16,196
Ta 14,15,9216
v_add_u32 v5, 65536, v13
Mvav 104,16,204
buffer_load_dwordx4 a[128:131], v26, s[8:11], s93 offen offset:0 sc1
K 164,164,165
Mvav 104,20,236
K 165,166,167
Mvav 136,20,244
K 166,172,173
Mvav 136,16,212
W 8
Ta 16,24,0
K 167,174,175
Mvav 140,24,212
Ta 18,24,1024
v_add_u32 v6, 65536, v14
Mvav 140,28,244
buffer_load_dwordx4 a[132:135], v27, s[8:11], s28 offen offset:0 sc1
v_accvgpr_write_b32 a216, v164
Mvav 108,28,236
Ta 20,24,8192
v_accvgpr_write_b32 a217, v165
Mvav 108,24,204
Ta 22,24,9216
v_add_u32 v7, 65536, v15
Mvav 76,24,196
v_accvgpr_write_b32 a218, v166
v_accvgpr_write_b32 a219, v167
Mvav 76,28,228
v_accvgpr_write_b32 a220, v200
Mvav 44,28,220
buffer_load_dwordx4 a[136:139], v27, s[8:11], s93 offen offset:0 sc1
Mvav 44,24,188
W 8
s_add_u32 s28, s28, 512
s_cmp_eq_u32 s28, s4
s_cselect_b32 s93, 10240, 0
Mvav 48,0,188
s_sub_u32 s28, s28, s93
s_add_u32 s93, s28, 20480
v_accvgpr_write_b32 a221, v201
Mvav 48,4,220
Ta 24,25,0
Ta 26,25,1024
Mvav 80,4,228
v_add_u32 v8, 65536, v24
v_accvgpr_write_b32 a222, v202
Mvav 80,0,196
buffer_load_dwordx4 a[140:143], v26, s[8:11], s28 offen offset:0 sc1
v_accvgpr_write_b32 a223, v203
Mvav 112,0,204
Ta 28,25,8192
Ta 30,25,9216
Mvav 112,4,236
v_add_u32 v9, 65536, v25
G 164,224
Mvav 144,4,244
G 165,225
G 166,226
Mvav 144,0,212
W 8
Ta 0,10,16384
G 167,227
Mvav 148,8,212
global_load_dword v29, v28, s[6:7]
Ta 2,10,17408
Mvav 148,12,244
s_sub_u32 s74, s85, 66
s_and_b32 s75, s74, 0xffffffe3
s_cselect_b64 s[72:73], -1, 0
s_and_b32 s74, s74, 28
Mvav 116,12,236
s_lshl_b32 s74, s74, 1
v_mov_b32 v28, s74
G 172,228
Mvav 116,8,204
G 173,229
G 174,230
Mvav 84,8,196
Ta 4,10,24576
Ta 6,10,25600
Mvav 84,12,228
buffer_load_dwordx4 a[144:147], v26, s[8:11], s93 offen offset:0 sc1
G 175,231
Mvav 52,12,220
K 164,164,165
K 165,166,167
Mvav 52,8,188
W 8
Ta 8,11,16384
K 166,172,173
Mvav 56,16,188
Ta 10,11,17408
K 167,174,175
Mvav 56,20,220
v_accvgpr_write_b32 a224, v164
Mvav 88,20,228
buffer_load_dwordx4 a[148:151], v27, s[8:11], s28 offen offset:0 sc1
Ta 12,11,24576
Mvav 88,16,196
Ta 14,11,25600
v_accvgpr_write_b32 a225, v165
Mvav 120,16,204
v_accvgpr_write_b32 a226, v166
Mvav 120,20,236
v_accvgpr_write_b32 a227, v167
Mvav 152,20,244
v_accvgpr_write_b32 a228, v208
Mvav 152,16,212
W 8
D 18,48,65,2048
Ta 16,12,16384
Mvav 156,24,212
Ta 18,12,17408
v_accvgpr_write_b32 a229, v209
Mvav 156,28,244
v_accvgpr_write_b32 a230, v210
Mvav 124,28,236
Ta 20,12,24576
v_accvgpr_write_b32 a231, v211
Mvav 124,24,204
Ta 22,12,25600
G 164,232
Mvav 92,24,196
buffer_load_dwordx4 a[152:155], v27, s[8:11], s93 offen offset:0 sc1
G 165,233
Mvav 92,28,228
s_add_u32 s28, s28, 512
s_cmp_eq_u32 s28, s4
s_cselect_b32 s93, 10240, 0
s_sub_u32 s28, s28, s93
Mvav 60,28,220
s_add_u32 s93, s28, 20480
G 166,234
G 167,235
Mvav 60,24,188
s_waitcnt vmcnt(12) lgkmcnt(8)
G 172,236
G 173,237
Mvaa 32,0,32
Ta 24,13,16384
Ta 26,13,17408
Mvaa 32,4,48
buffer_load_dwordx2 v[28:29], v28, s[8:11], 0 offen offset:3584 sc1
G 174,238
Mvaa 64,4,52
G 175,239
K 164,164,165
Mvaa 64,0,36
Ta 28,13,24576
Ta 30,13,25600
Mvaa 96,0,40
K 165,166,167
K 166,172,173
Mvaa 96,4,56
K 167,174,175
v_accvgpr_write_b32 a232, v164
Mvaa 128,4,60
buffer_load_dwordx4 a[156:159], v26, s[8:11], s28 offen offset:0 sc1
v_accvgpr_write_b32 a233, v165
Mvaa 128,0,44
W 8
v_accvgpr_write_b32 a234, v166
v_accvgpr_write_b32 a235, v167
Mvaa 132,8,44
Ta 0,14,16384
Ta 2,14,17408
Mvaa 132,12,60
G 164,160
G 165,161
Mvaa 100,12,56
G 166,162
G 167,163
Mvaa 100,8,40
buffer_load_dwordx4 v[192:195], v26, s[8:11], s93 offen offset:0 sc1
Ta 4,14,24576
Mvaa 68,8,36
Ta 6,14,25600
G 172,164
Mvaa 68,12,52
G 173,165
G 174,166
Mvaa 36,12,48
G 175,167
K 164,164,165
Mvaa 36,8,32
W 8
Ta 8,15,16384
K 165,166,167
Mvaa 40,16,32
buffer_load_dwordx4 v[200:203], v27, s[8:11], s28 offen offset:0 sc1
Ta 10,15,17408
Mvaa 40,20,48
K 166,172,173
K 167,174,175
Mvaa 72,20,52
s_cmp_lg_u32 s72, 0
s_cbranch_scc1 .LL_55%=
s_waitcnt vmcnt(3)
.LL_55%=:
v_xor_b32 v28, s12, v28
v_xor_b32 v29, s13, v29
v_lshrrev_b32 v248, 11, v28
v_or_b32 v29, v29, v248
v_cmp_eq_u32 vcc, 0, v29
s_or_b64 vcc, vcc, s[72:73]
s_cmp_eq_u64 vcc, -1
s_cbranch_scc0 .Lspin_56%=
.Lok_57%=:
v_accvgpr_write_b32 a236, v164
v_accvgpr_write_b32 a237, v165
Mvaa 72,16,36
Ta 12,15,24576
Ta 14,15,25600
Mvaa 104,16,40
v_accvgpr_write_b32 a238, v166
v_accvgpr_write_b32 a239, v167
Mvaa 104,20,56
D 19,48,65,3072
G 164,240
Mvaa 136,20,60
s_add_u32 m0, s63, 102400
G 165,241
G 166,242
Mvaa 136,16,44
W 8
v_bfe_u32 v248, v28, 3, 8
G 167,243
Mvaa 140,24,44
Ta 16,24,16384
Ta 18,24,17408
Mvaa 140,28,60
v_mul_u32_u24 v248, 10240, v248
G 172,244
Mvaa 108,28,56
buffer_load_dwordx4 v[188:191], v27, s[8:11], s93 offen offset:0 sc1
G 173,245
Mvaa 108,24,40
s_add_u32 s28, s28, 512
s_cmp_eq_u32 s28, s4
s_cselect_b32 s93, 10240, 0
s_sub_u32 s28, s28, s93
Mvaa 76,24,36
s_add_u32 s93, s28, 20480
v_and_b32 v28, 7, v28
G 174,246
Mvaa 76,28,52
v_lshl_add_u32 v248, v28, 4, v248
G 175,247
Mvaa 44,28,48
Ta 20,24,24576
Ta 22,24,25600
Mvaa 44,24,32
W 8
buffer_load_dwordx4 v[196:199], v26, s[8:11], s28 offen offset:0 sc1
K 164,164,165
Mvaa 48,0,32
v_lshl_add_u32 v248, v1, 3, v248
K 165,166,167
Mvaa 48,4,48
v_lshrrev_b32 v29, 8, v1
K 166,172,173
Mvaa 80,4,52
K 167,174,175
v_lshlrev_b32 v29, 28, v29
Mvaa 80,0,36
v_accvgpr_write_b32 a240, v164
v_or_b32 v249, v248, v29
Mvaa 112,0,40
s_and_b32 s75, s72, 134217728
buffer_load_dwordx4 v[204:207], v26, s[8:11], s93 offen offset:0 sc1
v_accvgpr_write_b32 a241, v165
Mvaa 112,4,56
s_add_u32 s76, s75, 131072
Ta 24,25,16384
Ta 26,25,17408
Mvaa 144,4,60
v_accvgpr_write_b32 a242, v166
v_accvgpr_write_b32 a243, v167
)"
R"(Mvaa 144,0,44
W 6
G 164,168
G 165,169
Mvaa 148,8,44
Ta 28,25,24576
Ta 30,25,25600
Mvaa 148,12,60
D 20,48,65,0
G 166,170
Mvaa 116,12,56
G 167,171
G 172,172
Mvaa 116,8,40
G 173,173
G 174,174
Mvaa 84,8,36
Ta 0,2,0
G 175,175
Mvaa 84,12,52
Ta 2,2,1024
K 164,164,165
Mvaa 52,12,48
buffer_load_dwordx4 v[212:215], v27, s[8:11], s28 offen offset:0 sc1
K 165,166,167
Mvaa 52,8,32
W 6
K 166,172,173
K 167,174,175
Mvaa 56,16,32
Ta 4,2,8192
Ta 6,2,9216
Mvaa 56,20,48
v_accvgpr_write_b32 a244, v164
v_accvgpr_write_b32 a245, v165
Mvaa 88,20,52
v_accvgpr_write_b32 a246, v166
v_accvgpr_write_b32 a247, v167
Mvaa 88,16,36
buffer_load_dwordx4 v[220:223], v27, s[8:11], s93 offen offset:0 sc1
G 164,64
Mvaa 120,16,40
s_add_u32 s28, s28, 512
s_cmp_eq_u32 s28, s4
s_cselect_b32 s93, 10240, 0
s_sub_u32 s28, s28, s93
Mvaa 120,20,56
s_add_u32 s93, s28, 20480
Ta 8,3,0
G 165,65
Mvaa 152,20,60
Ta 10,3,1024
G 166,66
Mvaa 152,16,44
W 6
G 167,67
G 172,68
Mvaa 156,24,44
buffer_load_dwordx4 v[228:231], v26, s[8:11], s28 offen offset:0 sc1
G 173,69
Mvaa 156,28,60
Ta 12,3,8192
Ta 14,3,9216
Mvaa 124,28,56
G 174,70
G 175,71
Mvaa 124,24,40
K 164,164,165
K 165,166,167
Mvaa 92,24,36
K 166,172,173
K 167,174,175
Mvaa 92,28,52
buffer_load_dwordx4 v[240:243], v248, s[8:11], s76 offen sc1
Ta 16,4,0
Mvaa 60,28,48
s_add_u32 s76, s76, 8192
Ta 18,4,1024
v_accvgpr_write_b32 a64, v164
Mvaa 60,24,32
s_waitcnt vmcnt(13) lgkmcnt(6)
v_accvgpr_write_b32 a65, v165
v_accvgpr_write_b32 a66, v166
Mvav 32,0,184
Ta 20,4,8192
v_accvgpr_write_b32 a67, v167
Mvaa 32,4,140
Ta 22,4,9216
G 164,176
Mvaa 64,4,144
buffer_load_dwordx4 v[244:247], v249, s[8:11], s76 offen sc1
v_or_b32 v249, v1, v29
Mvaa 64,0,128
s_mul_i32 s76, s74, 160
s_add_u32 s76, s76, s75
s_add_u32 s76, s76, 118784
Mvaa 96,0,132
G 165,177
G 166,178
Mvaa 96,4,148
G 167,179
G 172,180
Mvaa 128,4,152
Ta 24,5,0
G 173,181
Mvaa 128,0,136
W 5
buffer_load_dwordx4 v[236:239], v26, s[8:11], s93 offen offset:0 sc1
Ta 26,5,1024
Mvaa 132,8,136
G 174,182
G 175,183
Mvaa 132,12,152
K 164,164,165
K 165,166,167
Mvaa 100,12,148
Ta 28,5,8192
Ta 30,5,9216
Mvaa 100,8,132
K 166,172,173
K 167,174,175
Mvaa 68,8,128
D 21,48,65,1024
v_accvgpr_write_b32 a68, v164
Mvaa 68,12,144
v_accvgpr_write_b32 a69, v165
v_accvgpr_write_b32 a70, v166
Mvaa 36,12,140
Ta 0,6,0
v_accvgpr_write_b32 a71, v167
Mvav 36,8,184
W 5
Ta 2,6,1024
G 164,72
Mvav 40,16,184
G 165,73
G 166,74
Mvaa 40,20,140
buffer_load_dwordx4 v[208:211], v27, s[8:11], s28 offen offset:0 sc1
Ta 4,6,8192
Mvaa 72,20,144
Ta 6,6,9216
G 167,75
Mvaa 72,16,128
G 172,76
G 173,77
Mvaa 104,16,132
Ta 8,7,0
G 174,78
Mvaa 104,20,148
Ta 10,7,1024
G 175,79
Mvaa 136,20,152
buffer_load_dwordx4 a[32:35], v27, s[8:11], s93 offen offset:0 sc1
K 164,164,165
Mvaa 136,16,136
W 6
s_add_u32 s28, s28, 512
s_cmp_eq_u32 s28, s4
s_cselect_b32 s93, 10240, 0
Mvaa 140,24,136
s_sub_u32 s28, s28, s93
s_add_u32 s93, s28, 20480
Ta 12,7,8192
Mvaa 140,28,152
Ta 14,7,9216
K 165,166,167
Mvaa 108,28,148
K 166,172,173
K 167,174,175
Mvaa 108,24,132
buffer_load_dwordx4 a[36:39], v26, s[8:11], s28 offen offset:0 sc1
v_accvgpr_write_b32 a72, v164
Mvaa 76,24,128
Ta 16,8,0
Ta 18,8,1024
Mvaa 76,28,144
v_accvgpr_write_b32 a73, v165
v_accvgpr_write_b32 a74, v166
Mvaa 44,28,140
Ta 20,8,8192
v_accvgpr_write_b32 a75, v167
Mvav 44,24,184
W 7
Ta 22,8,9216
v_accvgpr_write_b32 a76, v216
Mvav 48,0,184
D 22,48,65,2048
v_accvgpr_write_b32 a77, v217
Mvaa 48,4,140
v_accvgpr_write_b32 a78, v218
v_accvgpr_write_b32 a79, v219
Mvaa 80,4,144
Ta 24,9,0
Ta 26,9,1024
Mvaa 80,0,128
G 164,80
G 165,81
Mvaa 112,0,132
Ta 28,9,8192
Ta 30,9,9216
Mvaa 112,4,148
buffer_load_dwordx4 a[40:43], v26, s[8:11], s93 offen offset:0 sc1
G 166,82
Mvaa 144,4,152
G 167,83
G 172,84
Mvaa 144,0,136
W 8
Ta 0,2,16384
G 173,85
Mvaa 148,8,136
Ta 2,2,17408
G 174,86
Mvaa 148,12,152
G 175,87
Mvaa 116,12,148
buffer_load_dwordx4 a[44:47], v27, s[8:11], s28 offen offset:0 sc1
Ta 4,2,24576
Mvaa 116,8,132
Ta 6,2,25600
K 164,164,165
Mvaa 84,8,128
K 165,166,167
Mvaa 84,12,144
K 166,172,173
Mvaa 52,12,140
K 167,174,175
Mvav 52,8,184
W 8
buffer_load_dwordx4 a[48:51], v27, s[8:11], s93 offen offset:0 sc1
Ta 8,3,16384
Mvav 56,16,184
s_add_u32 s28, s28, 512
s_cmp_eq_u32 s28, s4
s_cselect_b32 s93, 10240, 0
D 23,48,65,3072
Mvaa 56,20,140
s_sub_u32 s28, s28, s93
s_add_u32 s93, s28, 20480
Ta 10,3,17408
Mvaa 88,20,144
v_accvgpr_write_b32 a80, v164
v_accvgpr_write_b32 a81, v165
Mvaa 88,16,128
Ta 12,3,24576
v_accvgpr_write_b32 a82, v166
Mvaa 120,16,132
Ta 14,3,25600
v_accvgpr_write_b32 a83, v167
Mvaa 120,20,148
buffer_load_dwordx4 a[52:55], v26, s[8:11], s28 offen offset:0 sc1
v_accvgpr_write_b32 a84, v224
Mvaa 152,20,152
v_lshrrev_b32 v20, 4, v1
v_accvgpr_write_b32 a85, v225
Mvaa 152,16,136
W 8
v_accvgpr_write_b32 a86, v226
v_add_u32 v20, 0, v20
Mvaa 156,24,136
Ta 16,4,16384
Ta 18,4,17408
Mvaa 156,28,152
v_accvgpr_write_b32 a87, v227
v_mul_u32_u24 v22, 205, v20
Mvaa 124,28,148
buffer_load_dwordx4 a[56:59], v26, s[8:11], s93 offen offset:0 sc1
G 164,88
Mvaa 124,24,132
Ta 20,4,24576
Ta 22,4,25600
Mvaa 92,24,128
v_lshrrev_b32 v22, 12, v22
G 165,89
Mvaa 92,28,144
G 166,90
v_mul_u32_u24 v23, 20, v22
Mvaa 60,28,140
G 167,91
v_sub_u32 v20, v20, v23
Mvav 60,24,184
s_waitcnt vmcnt(15) lgkmcnt(8)
buffer_load_dwordx4 a[60:63], v27, s[8:11], s28 offen offset:0 sc1
G 172,92
Mvaa 32,0,156
Ta 24,5,16384
Ta 26,5,17408
Mvav 32,4,196
v_lshrrev_b32 v23, 1, v22
G 173,93
Mvav 64,4,204
G 174,94
v_and_b32 v23, 2, v23
Mvav 64,0,192
Ta 28,5,24576
Ta 30,5,25600
Mvav 96,0,200
buffer_load_dwordx4 v[216:219], v27, s[8:11], s93 offen offset:0 sc1
G 175,95
Mvav 96,4,212
s_add_u32 s28, s28, 512
s_cmp_eq_u32 s28, s4
s_cselect_b32 s93, 10240, 0
s_sub_u32 s28, s28, s93
Mvav 128,4,220
s_add_u32 s93, s28, 20480
v_xor_b32 v20, v20, v23
K 164,164,165
Mvav 128,0,188
W 8
v_mul_u32_u24 v22, 80, v22
K 165,166,167
Mvav 132,8,188
K 166,172,173
v_add_lshl_u32 v16, v20, v22, 4
Mvav 132,12,220
buffer_load_dwordx4 v[184:187], v26, s[8:11], s28 offen offset:0 sc1
Ta 0,6,16384
Mvav 100,12,212
Ta 2,6,17408
K 167,174,175
Mvav 100,8,200
v_lshrrev_b32 v20, 4, v1
v_accvgpr_write_b32 a88, v164
Mvav 68,8,192
v_add_u32 v20, 64, v20
v_accvgpr_write_b32 a89, v165
Mvav 68,12,204
Ta 4,6,24576
Ta 6,6,25600
Mvav 36,12,196
s_cmp_lg_u32 s72, 0
s_cbranch_scc1 .LL_59%=
s_waitcnt vmcnt(15)
.LL_59%=:
buffer_store_dwordx4 v[240:243], v1, s[8:11], s76 offen sc1
v_accvgpr_write_b32 a90, v166
v_mul_u32_u24 v22, 205, v20
Mvaa 36,8,156
W 8
v_accvgpr_write_b32 a91, v167
v_lshrrev_b32 v22, 12, v22
Mvaa 40,16,156
v_accvgpr_write_b32 a92, v232
v_mul_u32_u24 v23, 20, v22
Mvav 40,20,196
Ta 8,7,16384
Ta 10,7,17408
Mvav 72,20,204
v_accvgpr_write_b32 a93, v233
v_accvgpr_write_b32 a94, v234
Mvav 72,16,192
buffer_store_dwordx4 v[244:247], v249, s[8:11], s76 offen offset:1024 sc1
v_sub_u32 v20, v20, v23
Mvav 104,16,200
s_add_u32 s76, s74, s75
s_add_u32 s76, s76, 3840
v_accvgpr_write_b32 a95, v235
Mvav 104,20,212
Ta 12,7,24576
Ta 14,7,25600
Mvav 136,20,220
v_lshrrev_b32 v23, 1, v22
G 164,96
Mvav 136,16,188
W 8
G 165,97
v_and_b32 v23, 2, v23
Mvav 140,24,188
buffer_load_dwordx4 a[128:131], v26, s[8:11], s93 offen offset:0 sc1
G 166,98
Mvav 140,28,220
G 167,99
v_xor_b32 v20, v20, v23
Mvav 108,28,212
G 172,100
G 173,101
Mvav 108,24,200
Ta 16,8,16384
Ta 18,8,17408
Mvav 76,24,192
v_mul_u32_u24 v22, 80, v22
G 174,102
Mvav 76,28,204
buffer_load_dwordx4 a[132:135], v27, s[8:11], s28 offen offset:0 sc1
G 175,103
Mvav 44,28,196
v_add_lshl_u32 v17, v20, v22, 4
K 164,164,165
Mvaa 44,24,156
W 6
Ta 20,8,24576
Ta 22,8,25600
Mvaa 48,0,156
K 165,166,167
K 166,172,173
Mvav 48,4,196
v_subrev_u32 v17, 1024, v17
K 167,174,175
)"
R"(Mvav 80,4,204
buffer_load_dwordx4 a[136:139], v27, s[8:11], s93 offen offset:0 sc1
v_accvgpr_write_b32 a96, v164
Mvav 80,0,192
s_add_u32 s28, s28, 512
v_lshrrev_b32 v20, 4, v1
v_accvgpr_write_b32 a97, v165
Mvav 112,0,200
s_cmp_eq_u32 s28, s4
s_cselect_b32 s93, 10240, 0
v_accvgpr_write_b32 a98, v166
v_add_u32 v20, 128, v20
Mvav 112,4,212
s_sub_u32 s28, s28, s93
Ta 24,9,16384
v_accvgpr_write_b32 a99, v167
Mvav 144,4,220
s_add_u32 s93, s28, 20480
Ta 26,9,17408
G 164,248
Mvav 144,0,188
W 6
buffer_load_dwordx4 a[140:143], v26, s[8:11], s28 offen offset:0 sc1
v_mul_u32_u24 v22, 205, v20
Mvav 148,8,188
G 165,249
G 166,250
Mvav 148,12,220
Ta 28,9,24576
Ta 30,9,25600
Mvav 116,12,212
v_lshrrev_b32 v22, 12, v22
G 167,251
Mvav 116,8,200
G 172,252
G 173,253
Mvav 84,8,192
s_waitcnt vmcnt(11) lgkmcnt(0)
s_barrier
v_mul_u32_u24 v23, 20, v22
Mvav 84,12,204
G 174,254
G 175,255
Mvav 52,12,196
v_sub_u32 v20, v20, v23
K 164,164,165
Mvaa 52,8,156
K 165,166,167
v_lshrrev_b32 v23, 1, v22
Mvaa 56,16,156
buffer_load_dwordx4 a[144:147], v26, s[8:11], s93 offen offset:0 sc1
K 166,172,173
Mvav 56,20,196
K 167,174,175
v_and_b32 v23, 2, v23
Mvav 88,20,204
v_accvgpr_write_b32 a100, v164
v_accvgpr_write_b32 a101, v165
Mvav 88,16,192
v_xor_b32 v20, v20, v23
v_accvgpr_write_b32 a102, v166
Mvav 120,16,200
Ta 0,10,32768
v_accvgpr_write_b32 a103, v167
Mvav 120,20,212
Ta 2,10,33792
Ta 4,10,40960
Mvav 152,20,220
Ta 6,10,41984
G 164,104
Mvav 152,16,188
v_mul_u32_u24 v22, 80, v22
G 165,105
Mvav 156,24,188
v_add_lshl_u32 v18, v20, v22, 4
G 166,106
Mvav 156,28,220
Ta 8,11,32768
Ta 10,11,33792
Mvav 124,28,212
G 167,107
v_subrev_u32 v18, 2048, v18
Mvav 124,24,200
buffer_load_dwordx4 a[148:151], v27, s[8:11], s28 offen offset:0 sc1
G 172,108
Mvav 92,24,192
G 173,109
v_lshrrev_b32 v20, 4, v1
Mvav 92,28,204
Ta 12,11,40960
Ta 14,11,41984
Mvav 60,28,196
G 174,110
v_add_u32 v20, 192, v20
Mvaa 60,24,156
W 4
G 175,111
K 164,164,165
Mvav 32,0,228
v_mul_u32_u24 v22, 205, v20
K 165,166,167
Mvaa 32,4,36
v_lshrrev_b32 v22, 12, v22
K 166,172,173
Mvaa 64,4,40
Ta 16,12,32768
Ta 18,12,33792
Mvav 64,0,236
buffer_load_dwordx4 a[152:155], v27, s[8:11], s93 offen offset:0 sc1
K 167,174,175
Mvav 96,0,208
s_add_u32 s28, s28, 512
v_mul_u32_u24 v23, 20, v22
v_accvgpr_write_b32 a104, v164
Mvaa 96,4,44
s_cmp_eq_u32 s28, s4
s_cselect_b32 s93, 10240, 0
v_accvgpr_write_b32 a105, v165
v_sub_u32 v20, v20, v23
Mvaa 128,4,48
s_sub_u32 s28, s28, s93
Ta 20,12,40960
Ta 22,12,41984
Mvaa 128,0,32
W 4
v_accvgpr_write_b32 a106, v166
v_lshrrev_b32 v23, 1, v22
Mvaa 132,8,32
s_add_u32 s93, s28, 20480
v_accvgpr_write_b32 a107, v167
K 176,176,177
Mvaa 132,12,48
v_and_b32 v23, 2, v23
K 177,178,179
Mvaa 100,12,44
K 178,180,181
v_xor_b32 v20, v20, v23
Mvav 100,8,208
Ta 24,13,32768
Ta 26,13,33792
Mvav 68,8,236
buffer_load_dwordx4 v[224:227], v26, s[8:11], s28 offen offset:0 sc1
K 179,182,183
Mvaa 68,12,40
v_accvgpr_write_b32 a108, v176
v_accvgpr_write_b32 a109, v177
v_accvgpr_write_b32 a110, v178
v_accvgpr_write_b32 a111, v179
Mvaa 36,12,36
Ta 28,13,40960
v_mul_u32_u24 v22, 80, v22
Mvav 36,8,228
W 3
Ta 30,13,41984
v_add_lshl_u32 v19, v20, v22, 4
Mvav 40,16,228
v_subrev_u32 v19, 3072, v19
G 164,112
Mvaa 40,20,36
s_cmp_lg_u32 s72, 0
s_cbranch_scc1 .LL_60%=
s_waitcnt vmcnt(8)
.LL_60%=:
v_mov_b64 v[28:29], s[12:13]
s_mov_b64 exec, 1
buffer_store_dwordx2 v[28:29], off, s[8:11], s76 sc1
s_mov_b64 exec, -1
v_lshrrev_b32 v20, 4, v1
G 165,113
Mvaa 72,20,40
Ta 0,14,32768
Ta 2,14,33792
Mvav 72,16,236
G 166,114
v_add_u32 v20, 256, v20
Mvav 104,16,208
G 167,115
v_mul_u32_u24 v22, 205, v20
Mvaa 104,20,44
Ta 4,14,40960
Ta 6,14,41984
Mvaa 136,20,48
buffer_load_dwordx4 v[232:235], v26, s[8:11], s93 offen offset:0 sc1
G 172,116
Mvaa 136,16,32
W 4
v_lshrrev_b32 v22, 12, v22
G 173,117
Mvaa 140,24,32
G 174,118
v_mul_u32_u24 v23, 20, v22
Mvaa 140,28,48
G 175,119
v_sub_u32 v20, v20, v23
Mvaa 108,28,44
Ta 8,15,32768
Ta 10,15,33792
Mvav 108,24,208
buffer_load_dwordx4 a[248:251], v27, s[8:11], s28 offen offset:0 sc1
K 164,164,165
Mvav 76,24,236
v_lshrrev_b32 v23, 1, v22
K 165,166,167
Mvaa 76,28,40
K 166,172,173
v_and_b32 v23, 2, v23
Mvaa 44,28,36
Ta 12,15,40960
Ta 14,15,41984
Mvav 44,24,228
W 4
K 167,174,175
v_xor_b32 v20, v20, v23
Mvav 48,0,228
v_accvgpr_write_b32 a112, v164
v_accvgpr_write_b32 a113, v165
Mvaa 48,4,36
v_mul_u32_u24 v22, 80, v22
v_accvgpr_write_b32 a114, v166
Mvaa 80,4,40
Ta 16,24,32768
Ta 18,24,33792
Mvav 80,0,236
v_add_lshl_u32 v20, v20, v22, 4
v_accvgpr_write_b32 a115, v167
Mvav 112,0,208
buffer_load_dwordx4 a[252:255], v27, s[8:11], s93 offen offset:0 sc1
v_lshrrev_b32 v22, 6, v1
Mvaa 112,4,44
s_add_u32 s28, s28, 512
Ta 20,24,40960
v_accvgpr_write_b32 a116, v168
Mvaa 144,4,48
v_accvgpr_write_b32 a117, v169
v_accvgpr_write_b32 a118, v170
v_accvgpr_write_b32 a119, v171
Mvaa 144,0,32
W 3
Ta 22,24,41984
v_mul_u32_u24 v22, 320, v22
Mvaa 148,8,32
s_cmp_eq_u32 s28, s4
s_cselect_b32 s93, 10240, 0
v_bfe_u32 v23, v1, 4, 2
v_lshl_add_u32 v22, v23, 3, v22
Mvaa 148,12,48
s_sub_u32 s28, s28, s93
Ta 24,25,32768
G 164,120
Mvaa 116,12,44
s_add_u32 s93, s28, 20480
Ta 26,25,33792
G 165,121
Mvav 116,8,208
buffer_load_dwordx4 v[192:195], v26, s[8:11], s28 offen offset:0 sc1
v_bfe_u32 v23, v1, 8, 1
Mvav 84,8,236
G 166,122
v_lshlrev_b32 v23, 5, v23
Mvaa 84,12,40
Ta 28,25,40960
G 167,123
Mvaa 52,12,36
Ta 30,25,41984
v_add_u32 v21, v22, v23
Mvav 52,8,228
W 4
G 172,124
G 173,125
Mvav 56,16,228
v_sub_u32 v22, v22, v23
G 174,126
Mvaa 56,20,36
s_mul_i32 s94, s59, 655360
Ta 0,10,49152
Ta 2,10,50176
Mvaa 88,20,40
s_mul_i32 s95, s58, 320
buffer_load_dwordx4 v[200:203], v26, s[8:11], s93 offen offset:0 sc1
G 175,127
Mvav 88,16,236
s_add_u32 s94, s94, s95
K 164,164,165
K 165,166,167
Mvav 120,16,208
Ta 4,10,57344
Ta 6,10,58368
Mvaa 120,20,44
s_add_u32 s40, s26, s94
s_addc_u32 s41, s27, 0
K 166,172,173
Mvaa 152,20,48
s_and_b32 s41, s41, 0xffff
K 167,174,175
v_accvgpr_write_b32 a120, v164
Mvaa 152,16,32
W 4
Ta 8,11,49152
v_accvgpr_write_b32 a121, v165
Mvaa 156,24,32
s_mov_b32 s42, 5242880
Ta 10,11,50176
buffer_load_dwordx4 v[188:191], v27, s[8:11], s28 offen offset:0 sc1
Mvaa 156,28,48
s_mov_b32 s43, 0x20000
v_accvgpr_write_b32 a122, v166
v_accvgpr_write_b32 a123, v167
Mvaa 124,28,44
s_sub_u32 s42, s42, s94
Ta 12,11,57344
Ta 14,11,58368
Mvav 124,24,208
s_mov_b64 s[44:45], s[8:9]
v_accvgpr_write_b32 a124, v160
v_accvgpr_write_b32 a125, v161
Mvav 92,24,236
s_mov_b32 s47, 0x20000
v_accvgpr_write_b32 a126, v162
v_accvgpr_write_b32 a127, v163
Mvaa 92,28,40
s_and_b32 s48, s14, 1
Ta 16,12,49152
Ta 18,12,50176
Mvaa 60,28,36
s_mul_i32 s48, s48, 5120
buffer_load_dwordx4 v[196:199], v27, s[8:11], s93 offen offset:0 sc1
v_bfe_u32 v28, v1, 4, 2
Mvav 60,24,228
s_waitcnt vmcnt(13) lgkmcnt(6)
Ta 20,12,57344
v_lshlrev_b32 v28, 3, v28
Mvaa 32,0,52
s_lshl_b32 s80, s56, 9
s_add_u32 s80, s80, 4096
Ta 22,12,58368
Mvav 32,4,184
s_lshr_b32 s51, s14, 1
s_and_b32 s94, s14, 1
Ta 24,13,49152
Mvaa 64,4,128
s_add_u32 s28, s28, 512
s_cmp_eq_u32 s28, s4
s_cselect_b32 s93, 10240, 0
Ta 26,13,50176
Mvaa 64,0,56
s_sub_u32 s28, s28, s93
s_add_u32 s93, s28, 20480
buffer_load_dwordx2 v[28:29], v28, s[8:11], s80 offen sc1
Mvaa 96,0,60
s_lshl_b32 s95, s57, 1
Ta 28,13,57344
Ta 30,13,58368
Mvaa 96,4,132
s_add_u32 s94, s94, s95
s_lshl_b32 s54, s94, 3
Mvaa 128,4,136
s_add_u32 s54, s54, s59
Mvav 128,0,216
W 8
Ta 0,14,49152
Ta 2,14,50176
Mvav 132,8,216
s_lshl_b32 s54, s54, 6
buffer_load_dwordx4 v[204:207], v26, s[8:11], s28 offen offset:0 sc1
Mvaa 132,12,136
s_xor_b32 s55, s54, 15872
Mvaa 100,12,132
Ta 4,14,57344
Ta 6,14,58368
Mvaa 100,8,60
s_add_u32 s54, s54, 20480
Mvaa 68,8,56
s_and_b32 s81, s14, 1
Mvaa 68,12,128
s_add_u32 s55, s55, 20480
buffer_load_dwordx4 v[212:215], v26, s[8:11], s93 offen offset:0 sc1
Mvav 36,12,184
Mvaa 36,8,52
W 8
s_and_b32 s95, s14, 2
s_mul_i32 s91, s81, 17164288
Mvaa 40,16,52
s_lshl_b32 s94, s94, 2
Ta 8,15,49152
Ta 10,15,50176
Mvav 40,20,184
s_mul_i32 s82, s56, 536576
Mvaa 72,20,128
s_add_u32 s94, s94, s95
buffer_load_dwordx4 v[220:223], v27, s[8:11], s28 offen offset:0 sc1
Ta 12,15,57344
Mvaa 72,16,56
Ta 14,15,58368
Mvaa 104,16,60
s_lshl_b32 s95, s95, 4
s_add_u32 s91, s91, s82
Mvaa 104,20,132
Mvaa 136,20,136
s_add_u32 s55, s55, s95
s_add_u32 s91, s91, 4194304
)"
R"(Mvav 136,16,216
W 8
s_lshl_b32 s49, s94, 17
Ta 16,24,49152
Mvav 140,24,216
Ta 18,24,50176
buffer_load_dwordx4 v[176:179], v27, s[8:11], s93 offen offset:0 sc1
Mvaa 140,28,136
s_lshl_b32 s95, s59, 14
s_lshr_b32 s82, s14, 1
Mvaa 108,28,132
Ta 20,24,57344
Ta 22,24,58368
Mvaa 108,24,60
s_add_u32 s49, s49, s95
s_lshl_b32 s83, s82, 14
Mvaa 76,24,56
s_xor_b32 s50, s49, 16252928
Mvaa 76,28,128
Mvav 44,28,184
s_add_u32 s91, s91, s83
s_add_u32 s49, s49, 39845888
Mvaa 44,24,52
W 8
Ta 24,25,49152
Ta 26,25,50176
Mvaa 48,0,52
s_add_u32 s50, s50, 39845888
s_lshl_b32 s81, s81, 14
Mvav 48,4,184
Mvaa 80,4,128
s_lshr_b32 s94, s15, 3
Ta 28,25,57344
Ta 30,25,58368
Mvaa 80,0,56
s_lshl_b32 s82, s82, 16
s_lshl_b32 s94, s94, 3
Mvaa 112,0,60
Mvaa 112,4,132
s_add_u32 s94, s94, s14
s_or_b32 s92, s81, s82
Mvaa 144,4,136
Mvav 144,0,216
W 8
s_lshl_b32 s94, s94, 4
Ta 0,2,32768
Mvav 148,8,216
s_xor_b32 s92, s92, 0x10000
s_add_u32 s94, s94, s59
Ta 2,2,33792
Mvaa 148,12,136
Mvaa 116,12,132
s_mul_i32 s52, s94, 5120
Ta 4,2,40960
Ta 6,2,41984
Mvaa 116,8,60
Mvaa 84,8,56
s_lshl_b32 s95, s59, 12
Mvaa 84,12,128
Mvav 52,12,184
s_sub_u32 s52, s52, s95
Mvaa 52,8,52
W 8
s_add_u32 s52, s52, 56623104
Ta 8,3,32768
Mvaa 56,16,52
Ta 10,3,33792
Mvav 56,20,184
s_lshl_b32 s53, s94, 3
Mvaa 88,20,128
Ta 12,3,40960
Ta 14,3,41984
Mvaa 88,16,56
s_add_u32 s53, s53, 65536
Mvaa 120,16,60
s_waitcnt vmcnt(4)
v_cmp_eq_u64 vcc, s[12:13], v[28:29]
s_cmp_eq_u64 vcc, -1
s_cbranch_scc0 .Lspin_61%=
.Lok_62%=:
s_sub_u32 s82, s91, 16384
Mvaa 120,20,132
s_xor_b32 s83, s92, 0x10000
s_add_u32 m0, s83, 0
s_add_u32 s81, s82, 0
Mvaa 152,20,136
v_bfe_u32 v244, v1, 4, 1
v_mul_u32_u24 v244, 0xaaaa, v244
Mvav 152,16,216
W 8
Dc 1,8,81,0
v_add_u32 v244, 0x4444, v244
Mvav 156,24,216
Ta 16,4,32768
Ta 18,4,33792
Mvaa 156,28,136
v_lshl_or_b32 v240, v244, 16, v244
Mvaa 124,28,132
Ta 20,4,40960
v_mov_b32 v241, 0xff800000
Mvaa 124,24,60
Ta 22,4,41984
v_add_u32 v242, 0x10000, v1
Mvaa 92,24,56
Dc 1,8,81,1024
Mvaa 92,28,128
v_bfe_u32 v244, v1, 4, 4
Mvav 60,28,184
v_lshrrev_b32 v245, 8, v1
Mvaa 60,24,52
W 8
Ta 24,5,32768
v_lshlrev_b32 v245, 2, v245
Mvaa 32,0,140
Ta 26,5,33792
Mvav 32,4,224
Dc 1,8,81,2048
v_sub_u32 v244, v244, v245
Mvav 64,4,232
Ta 28,5,40960
v_cmp_le_i32_e64 s[72:73], 0, v244
Mvaa 64,0,144
Ta 30,5,41984
Mvaa 96,0,148
v_cmp_le_i32_e64 s[74:75], 1, v244
Mvaa 96,4,248
v_cmp_le_i32_e64 s[76:77], 2, v244
Mvaa 128,4,252
Dc 1,8,81,3072
v_cmp_le_i32_e64 s[78:79], 3, v244
Mvaa 128,0,152
W 8
s_add_u32 m0, s83, 4096
s_add_u32 s81, s82, 4096
Mvaa 132,8,152
Ta 0,6,32768
Ta 2,6,33792
Mvaa 132,12,252
v_add_u32 v243, 4096, v1
v_add_u32 v244, 8192, v1
Mvaa 100,12,248
Ta 4,6,40960
v_add_u32 v245, 12288, v1
Mvaa 100,8,148
Dc 1,8,81,0
Ta 6,6,41984
Mvaa 68,8,144
s_mov_b32 s18, 0x3f803f80
Mvav 68,12,232
s_mov_b32 s19, 0x3f003f00
Mvav 36,12,224
Mvaa 36,8,140
W 8
Ta 8,7,32768
v_accvgpr_write_b32 a176, s18
Mvaa 40,16,140
Dc 1,8,81,1024
Ta 10,7,33792
Mvav 40,20,224
v_accvgpr_write_b32 a180, s19
v_mov_b32 v252, v241
Mvav 72,20,232
Ta 12,7,40960
Ta 14,7,41984
Mvaa 72,16,144
v_accvgpr_write_b32 a177, s18
Mvaa 104,16,148
v_accvgpr_write_b32 a181, s19
Mvaa 104,20,248
Dc 1,8,81,2048
v_mov_b32 v253, v241
Mvaa 136,20,252
Mvaa 136,16,152
W 8
Ta 16,8,32768
v_accvgpr_write_b32 a178, s18
Mvaa 140,24,152
Ta 18,8,33792
v_accvgpr_write_b32 a182, s19
Mvaa 140,28,252
Mvaa 108,28,248
Dc 1,8,81,3072
v_mov_b32 v254, v241
Mvaa 108,24,148
s_add_u32 m0, s83, 8192
s_add_u32 s81, s82, 8192
Ta 20,8,40960
Mvaa 76,24,144
Ta 22,8,41984
v_accvgpr_write_b32 a179, s18
Mvav 76,28,232
v_accvgpr_write_b32 a183, s19
Mvav 44,28,224
v_mov_b32 v255, v241
Mvaa 44,24,140
W 8
Dc 1,8,81,0
v_accvgpr_write_b32 a160, 0
Mvaa 48,0,140
Ta 24,9,32768
Ta 26,9,33792
Mvav 48,4,224
v_accvgpr_write_b32 a161, 0
Mvav 80,4,232
Ta 28,9,40960
v_accvgpr_write_b32 a162, 0
Mvaa 80,0,144
Ta 30,9,41984
v_accvgpr_write_b32 a163, 0
Mvaa 112,0,148
Dc 1,8,81,1024
Mvaa 112,4,248
v_accvgpr_write_b32 a164, 0
Mvaa 144,4,252
v_accvgpr_write_b32 a165, 0
Mvaa 144,0,152
W 8
Ta 0,2,49152
v_accvgpr_write_b32 a166, 0
Mvaa 148,8,152
Ta 2,2,50176
Mvaa 148,12,252
Dc 1,8,81,2048
v_accvgpr_write_b32 a167, 0
Mvaa 116,12,248
Ta 4,2,57344
v_accvgpr_write_b32 a168, 0
Mvaa 116,8,148
Ta 6,2,58368
v_accvgpr_write_b32 a169, 0
Mvaa 84,8,144
Mvav 84,12,232
v_accvgpr_write_b32 a170, 0
Mvav 52,12,224
Dc 1,8,81,3072
v_accvgpr_write_b32 a171, 0
Mvaa 52,8,140
W 8
s_add_u32 m0, s83, 12288
s_add_u32 s81, s82, 12288
Mvaa 56,16,140
Ta 8,3,49152
Ta 10,3,50176
Mvav 56,20,224
v_accvgpr_write_b32 a172, 0
v_accvgpr_write_b32 a173, 0
Mvav 88,20,232
Ta 12,3,57344
v_accvgpr_write_b32 a174, 0
Mvaa 88,16,144
Dc 1,8,81,0
Ta 14,3,58368
Mvaa 120,16,148
v_accvgpr_write_b32 a175, 0
Mvaa 120,20,248
s_mov_b32 s90, 0x5d800000
Mvaa 152,20,252
s_mov_b64 s[88:89], 0
Mvaa 152,16,152
W 8
Ta 16,4,49152
Ta 18,4,50176
Mvaa 156,24,152
s_mov_b32 s64, 0
Dc 1,8,81,1024
Mvaa 156,28,252
s_and_b32 s18, s14, 1
Mvaa 124,28,248
s_lshl_b32 s62, s18, 14
Ta 20,4,57344
Ta 22,4,58368
Mvaa 124,24,148
Mvaa 92,24,144
s_lshl_b32 s20, s57, 15
Mvav 92,28,232
s_sub_u32 s20, 507904, s20
Dc 1,8,81,2048
Mvav 60,28,224
Mvaa 60,24,140
s_waitcnt vmcnt(15) lgkmcnt(8)
s_add_u32 s20, s20, s62
Ta 24,5,49152
Mvav 32,0,192
s_lshl_b32 s65, s57, 1
Ta 26,5,50176
Mvav 32,4,204
s_sub_u32 s65, s65, 1
Mvav 64,4,212
Dc 1,8,81,3072
Ta 28,5,57344
Mvav 64,0,200
s_max_i32 s65, s65, 0
Ta 30,5,58368
v_bfe_u32 v28, v1, 4, 4
Mvav 96,0,188
s_sub_u32 s66, s60, 1
v_lshlrev_b32 v28, 3, v28
Mvav 96,4,220
s_lshl_b32 s80, s56, 9
s_add_u32 s80, s80, 4096
s_cmp_lt_u32 s14, 2
s_cselect_b32 s20, s20, 0
s_cselect_b32 s66, s66, 15
s_cselect_b32 s21, 32768, 0
Mvav 128,4,176
Mvav 128,0,196
W 8
s_add_u32 s61, s91, s20
buffer_load_dwordx2 v[28:29], v28, s[8:11], s80 offen sc1
Mvav 132,8,196
s_add_u32 s62, s62, s21
Ta 0,6,49152
Ta 2,6,50176
Mvav 132,12,176
Mvav 100,12,220
s_sub_u32 s63, 16384, s21
Ta 4,6,57344
Ta 6,6,58368
Mvav 100,8,188
s_lshl_b32 s67, s60, 7
Mvav 68,8,200
s_add_u32 s67, s67, s56
Mvav 68,12,212
Mvav 36,12,204
s_lshl_b32 s67, s67, 12
Mvav 36,8,192
W 8
s_add_u32 s67, s67, 39845888
Ta 8,7,49152
Mvav 40,16,192
s_lshl_b32 s69, s60, 3
Ta 10,7,50176
Mvav 40,20,204
Mvav 72,20,212
s_add_u32 s69, s69, s59
Ta 12,7,57344
Ta 14,7,58368
Mvav 72,16,200
s_lshl_b32 s69, s69, 6
Mvav 104,16,188
Mvav 104,20,220
s_lshl_b32 s21, s58, 3
Mvav 136,20,176
s_add_u32 s69, s69, s21
Mvav 136,16,196
W 8
s_add_u32 s69, s69, 20480
Ta 16,8,49152
Mvav 140,24,196
Ta 18,8,50176
Mvav 140,28,176
s_mov_b32 s70, 0
Mvav 108,28,220
s_mov_b32 s37, 0
Ta 20,8,57344
Ta 22,8,58368
Mvav 108,24,188
s_sub_u32 s18, s57, 4
Mvav 76,24,200
Mvav 76,28,212
s_lshl_b32 s18, s18, 2
Mvav 44,28,204
s_add_u32 s18, s18, s58
Mvav 44,24,192
W 8
Ta 24,9,49152
Ta 26,9,50176
Mvav 48,0,192
s_mul_i32 s85, s59, 7
Mvav 48,4,204
s_add_u32 s85, s85, s18
Mvav 80,4,212
s_cmp_lt_u32 s18, 7
s_cselect_b32 s85, s85, 99
Ta 28,9,57344
Ta 30,9,58368
Mvav 80,0,200
Mvav 112,0,188
s_cmp_lt_u32 s85, 54
s_cselect_b32 s65, 2, s65
Mvav 112,4,220
s_mov_b32 s68, 0
Mvav 144,4,176
s_mov_b64 s[24:25], -1
Mvav 144,0,196
W 8
Mvav 148,8,196
s_mov_b32 s22, 0x3e0293ee
Mvav 148,12,176
s_mov_b32 s23, 0x3e0293ee
Mvav 116,12,220
Mvav 116,8,188
Mvav 84,8,200
Mvav 84,12,212
Mvav 52,12,204
Mvav 52,8,192
W 4
Mvav 56,16,192
Mvav 56,20,204
s_waitcnt vmcnt(0)
)"
R"(v_cmp_eq_u64 vcc, s[12:13], v[28:29]
s_cmp_eq_u64 vcc, -1
s_cbranch_scc0 .Lspin_64%=
.Lok_65%=:
Mvav 88,20,212
Mvav 88,16,200
Mvav 120,16,188
W 0
s_barrier
Ra 128,242,0
Mvav 120,20,220
Mvav 152,20,176
Ra 132,242,1024
Mvav 152,16,196
Mvav 156,24,196
Ra 136,242,2048
Mvav 156,28,176
Mvav 124,28,220
Ra 140,242,3072
Mvav 124,24,188
Mvav 92,24,200
Mvav 92,28,212
Mvav 60,28,204
Mvav 60,24,192
.Lbgo%=:
v_pk_mul_f32 v[32:33], v[32:33], s[22:23]
v_pk_mul_f32 v[34:35], v[34:35], s[22:23]
v_pk_mul_f32 v[36:37], v[36:37], s[22:23]
v_pk_mul_f32 v[38:39], v[38:39], s[22:23]
v_pk_mul_f32 v[64:65], v[64:65], s[22:23]
v_pk_mul_f32 v[66:67], v[66:67], s[22:23]
v_pk_mul_f32 v[68:69], v[68:69], s[22:23]
v_pk_mul_f32 v[70:71], v[70:71], s[22:23]
v_pk_mul_f32 v[96:97], v[96:97], s[22:23]
v_pk_mul_f32 v[98:99], v[98:99], s[22:23]
v_pk_mul_f32 v[100:101], v[100:101], s[22:23]
v_pk_mul_f32 v[102:103], v[102:103], s[22:23]
v_pk_mul_f32 v[128:129], v[128:129], s[22:23]
v_pk_mul_f32 v[130:131], v[130:131], s[22:23]
v_pk_mul_f32 v[132:133], v[132:133], s[22:23]
v_pk_mul_f32 v[134:135], v[134:135], s[22:23]
v_pk_mul_f32 v[40:41], v[40:41], s[22:23]
v_pk_mul_f32 v[42:43], v[42:43], s[22:23]
v_pk_mul_f32 v[44:45], v[44:45], s[22:23]
v_pk_mul_f32 v[46:47], v[46:47], s[22:23]
v_pk_mul_f32 v[72:73], v[72:73], s[22:23]
v_pk_mul_f32 v[74:75], v[74:75], s[22:23]
v_pk_mul_f32 v[76:77], v[76:77], s[22:23]
v_pk_mul_f32 v[78:79], v[78:79], s[22:23]
v_pk_mul_f32 v[104:105], v[104:105], s[22:23]
v_pk_mul_f32 v[106:107], v[106:107], s[22:23]
v_pk_mul_f32 v[108:109], v[108:109], s[22:23]
v_pk_mul_f32 v[110:111], v[110:111], s[22:23]
v_pk_mul_f32 v[136:137], v[136:137], s[22:23]
v_pk_mul_f32 v[138:139], v[138:139], s[22:23]
v_pk_mul_f32 v[140:141], v[140:141], s[22:23]
v_pk_mul_f32 v[142:143], v[142:143], s[22:23]
v_pk_mul_f32 v[48:49], v[48:49], s[22:23]
v_pk_mul_f32 v[50:51], v[50:51], s[22:23]
v_pk_mul_f32 v[52:53], v[52:53], s[22:23]
v_pk_mul_f32 v[54:55], v[54:55], s[22:23]
v_pk_mul_f32 v[80:81], v[80:81], s[22:23]
v_pk_mul_f32 v[82:83], v[82:83], s[22:23]
v_pk_mul_f32 v[84:85], v[84:85], s[22:23]
v_pk_mul_f32 v[86:87], v[86:87], s[22:23]
v_pk_mul_f32 v[112:113], v[112:113], s[22:23]
v_pk_mul_f32 v[114:115], v[114:115], s[22:23]
v_pk_mul_f32 v[116:117], v[116:117], s[22:23]
v_pk_mul_f32 v[118:119], v[118:119], s[22:23]
v_pk_mul_f32 v[144:145], v[144:145], s[22:23]
v_pk_mul_f32 v[146:147], v[146:147], s[22:23]
v_pk_mul_f32 v[148:149], v[148:149], s[22:23]
v_pk_mul_f32 v[150:151], v[150:151], s[22:23]
v_pk_mul_f32 v[56:57], v[56:57], s[22:23]
v_pk_mul_f32 v[58:59], v[58:59], s[22:23]
v_pk_mul_f32 v[60:61], v[60:61], s[22:23]
v_pk_mul_f32 v[62:63], v[62:63], s[22:23]
v_pk_mul_f32 v[88:89], v[88:89], s[22:23]
v_pk_mul_f32 v[90:91], v[90:91], s[22:23]
v_pk_mul_f32 v[92:93], v[92:93], s[22:23]
v_pk_mul_f32 v[94:95], v[94:95], s[22:23]
v_pk_mul_f32 v[120:121], v[120:121], s[22:23]
v_pk_mul_f32 v[122:123], v[122:123], s[22:23]
v_pk_mul_f32 v[124:125], v[124:125], s[22:23]
v_pk_mul_f32 v[126:127], v[126:127], s[22:23]
v_pk_mul_f32 v[152:153], v[152:153], s[22:23]
v_pk_mul_f32 v[154:155], v[154:155], s[22:23]
v_pk_mul_f32 v[156:157], v[156:157], s[22:23]
v_pk_mul_f32 v[158:159], v[158:159], s[22:23]
K 32,32,33
K 33,34,35
K 34,36,37
K 35,38,39
K 40,40,41
K 41,42,43
K 42,44,45
K 43,46,47
W 0
Zvav 196,128,32
s_add_u32 m0, s62, 0
Ra 144,242,4096
Ra 148,242,5120
Ra 152,242,6144
Ra 156,242,7168
Mvav 196,132,40
K 64,64,65
K 65,66,67
K 66,68,69
K 67,70,71
K 72,72,73
K 73,74,75
K 74,76,77
K 75,78,79
s_nop 1
Zvav 204,132,72
Dc 1,8,61,0
K 96,96,97
K 97,98,99
K 98,100,101
Mvav 204,128,64
K 99,102,103
K 104,104,105
K 105,106,107
K 106,108,109
K 107,110,111
Zvav 212,128,96
Dc 1,8,61,1024
K 128,128,129
K 129,130,131
K 130,132,133
Mvav 212,132,104
K 131,134,135
K 136,136,137
K 137,138,139
K 138,140,141
K 139,142,143
s_nop 1
Zvav 220,132,136
Dc 1,8,61,2048
K 48,48,49
K 49,50,51
K 50,52,53
Mvav 220,128,128
K 51,54,55
K 56,56,57
K 57,58,59
K 58,60,61
K 59,62,63
Mvav 196,136,48
Ra 128,242,8192
Ra 132,242,9216
K 80,80,81
K 81,82,83
Mvav 196,140,56
K 82,84,85
K 83,86,87
K 88,88,89
K 89,90,91
K 90,92,93
K 91,94,95
s_nop 1
Mvav 204,140,88
Dc 1,8,61,3072
s_add_u32 m0, s62, 4096
K 112,112,113
K 113,114,115
K 114,116,117
Mvav 204,136,80
K 115,118,119
K 120,120,121
K 121,122,123
K 122,124,125
K 123,126,127
Mvav 212,136,112
Dc 243,8,61,0
K 144,144,145
K 145,146,147
K 146,148,149
Mvav 212,140,120
K 147,150,151
K 152,152,153
K 153,154,155
K 154,156,157
K 155,158,159
s_nop 1
Mvav 220,140,152
s_add_u32 s80, s62, 131072
Dc 243,8,61,1024
v_add_u32 v12, s80, v1
Mvav 220,136,144
W 2
ds_write_b128 v12, v[32:35] offset:0
Ra 136,242,10240
Zvav 36,144,32
Ra 0,12,0
Ra 140,242,11264
Dc 243,8,61,2048
Mvav 36,148,40
v_max3_f32 v4, v196, v197, v198
v_max3_f32 v4, v4, v199, v252
v_max3_f32 v5, v204, v205, v206
v_max3_f32 v5, v5, v207, v253
Zvav 68,148,72
v_max3_f32 v6, v212, v213, v214
v_max3_f32 v6, v6, v215, v254
v_max3_f32 v7, v220, v221, v222
v_max3_f32 v7, v7, v223, v255
Mvav 68,144,64
v_mov_b32 v8, v4
v_mov_b32 v9, v5
v_mov_b32 v10, v6
v_mov_b32 v11, v7
Zvav 100,144,96
ds_write_b128 v12, v[40:43] offset:1024
v_permlane32_swap_b32 v4, v8
v_permlane32_swap_b32 v5, v9
v_permlane32_swap_b32 v6, v10
Mvav 100,148,104
v_permlane32_swap_b32 v7, v11
v_max_f32 v4, v4, v8
v_max_f32 v5, v5, v9
v_max_f32 v6, v6, v10
Zvav 132,148,136
v_max_f32 v7, v7, v11
v_mov_b32 v8, v4
v_mov_b32 v9, v5
v_mov_b32 v10, v6
Mvav 132,144,128
Ra 4,12,1024
v_mov_b32 v11, v7
v_permlane16_swap_b32 v4, v8
v_permlane16_swap_b32 v5, v9
Mvav 36,152,48
v_permlane16_swap_b32 v6, v10
v_permlane16_swap_b32 v7, v11
v_max_f32 v4, v4, v8
v_max_f32 v5, v5, v9
Mvav 36,156,56
v_max_f32 v6, v6, v10
v_max_f32 v7, v7, v11
v_sub_f32 v4, 0, v4
v_sub_f32 v5, 0, v5
Mvav 68,156,88
v_sub_f32 v6, 0, v6
v_sub_f32 v7, 0, v7
v_mov_b32 v224, v4
v_mov_b32 v225, v4
Mvav 68,152,80
v_mov_b32 v226, v4
v_mov_b32 v227, v4
v_mov_b32 v228, v5
v_mov_b32 v229, v5
Mvav 100,152,112
v_mov_b32 v230, v5
v_mov_b32 v231, v5
v_mov_b32 v232, v6
v_mov_b32 v233, v6
Mvav 100,156,120
v_mov_b32 v234, v6
v_mov_b32 v235, v6
v_mov_b32 v236, v7
v_mov_b32 v237, v7
Mvav 132,156,152
Dc 243,8,61,3072
s_add_u32 m0, s62, 8192
ds_write_b128 v12, v[48:51] offset:2048
v_mov_b32 v238, v7
v_mov_b32 v239, v7
Mvav 132,152,144
W 3
Ra 8,12,2048
ds_write_b128 v12, v[56:59] offset:3072
Ra 144,242,12288
Ra 12,12,3072
Cvav 160,128,32,224
s_and_b32 s80, s14, 1
Dc 244,8,61,0
Ra 148,242,13312
ds_write_b128 v12, v[64:67] offset:4096
Ra 16,12,4096
Mvav 160,132,40
s_lshl_b32 s80, s80, 14
ds_write_b128 v12, v[72:75] offset:5120
Ra 20,12,5120
ds_write_b128 v12, v[80:83] offset:6144
A 196,196,224
Cvav 164,132,72,228
s_add_u32 s80, s80, 98304
Dc 244,8,61,1024
Ra 24,12,6144
A 197,197,224
A 198,198,224
Mvav 164,128,64
Ra 152,242,14336
A 199,199,224
X 196
Cvav 168,128,96,232
Dc 244,8,61,2048
Ra 156,242,15360
X 197
Mvav 168,132,104
ds_write_b128 v12, v[88:91] offset:7168
Ra 28,12,7168
X 198
Cvav 172,132,136,236
Dc 244,8,61,3072
s_add_u32 m0, s62, 12288
K 192,196,197
X 199
Mvav 172,128,128
ds_write_b128 v12, v[96:99] offset:8192
A 204,204,228
v_add_u32 v8, s80, v1
K 193,198,199
Mvav 160,136,48
Dc 245,8,61,0
Ra 32,12,8192
A 205,205,228
A 206,206,228
Mvav 160,140,56
ds_write_b128 v8, v[4:7]
A 207,207,228
X 204
Mvav 164,140,88
Dc 245,8,61,1024
ds_write_b128 v12, v[104:107] offset:9216
X 205
Mvav 164,136,80
Ra 36,12,9216
X 206
K 200,204,205
Mvav 168,136,112
Dc 245,8,61,2048
Ra 128,242,24576
X 207
Mvav 168,140,120
ds_write_b128 v12, v[112:115] offset:10240
Ra 132,242,25600
A 212,212,232
K 201,206,207
Mvav 172,140,152
Dc 245,8,61,3072
s_add_u32 s61, s61, s63
Ra 40,12,10240
A 213,213,232
A 214,214,232
Mvav 172,136,144
W 11
ds_write_b128 v12, v[120:123] offset:11264
A 215,215,232
X 212
Cvav 176,144,32,224
X 213
X 214
Mvav 176,148,40
Ra 44,12,11264
K 208,212,213
X 215
Cvav 180,148,72,228
ds_write_b128 v12, v[128:131] offset:12288
A 220,220,236
K 209,214,215
A 221,221,236
Mvav 180,144,64
Ra 48,12,12288
Ra 136,242,26624
A 222,222,236
A 223,223,236
Cvav 184,144,96,232
ds_write_b128 v12, v[136:139] offset:13312
Ra 140,242,27648
X 220
Mvav 184,148,104
X 221
X 222
Cvav 188,148,136,236
Ra 52,12,13312
K 216,220,221
X 223
Mvav 188,144,128
ds_write_b128 v12, v[144:147] offset:14336
Ra 56,12,14336
A 36,36,224
K 217,222,223
Mvav 176,152,48
ds_write_b128 v12, v[152:155] offset:15360
A 37,37,224
A 38,38,224
A 39,39,224
Mvav 176,156,56
X 36
X 37
Mvav 180,156,88
Ra 60,12,15360
X 38
K 194,36,37
Mvav 180,152,80
Ra 144,242,28672
X 39
A 68,68,228
)"
R"(Mvav 184,152,112
Ra 148,242,29696
K 195,38,39
A 69,69,228
A 70,70,228
Mvav 184,156,120
W 3
s_barrier
A 71,71,228
X 68
Mvav 188,156,152
X 69
X 70
K 202,68,69
X 71
A 100,100,232
K 203,70,71
A 101,101,232
A 102,102,232
A 103,103,232
X 100
X 101
X 102
K 210,100,101
X 103
A 132,132,236
K 211,102,103
A 133,133,236
A 134,134,236
A 135,135,236
X 132
X 133
X 134
K 218,132,133
X 135
s_nop 0
K 219,134,135
Mvav 188,152,144
Ra 152,242,30720
Zvav 32,128,192
Ra 156,242,31744
Zvav 64,128,200
Zvav 96,128,208
s_xor_b32 s62, s62, 0x10000
Zvav 128,128,216
v_add_u32 v23, 61440, v21
Zvav 132,132,216
v_add_u32 v24, 61440, v22
Zvav 100,132,208
v_add_u32 v25, 122880, v21
Zvav 68,132,200
v_add_u32 v26, 122880, v22
Zvav 36,132,192
Zvav 40,136,192
X 172
Zvav 72,136,200
X 173
Zvav 104,136,208
X 174
K 220,172,173
Zvav 136,136,216
X 175
Zvav 140,140,216
X 168
K 221,174,175
Zvav 108,140,208
X 169
X 170
Zvav 76,140,200
s_waitcnt vmcnt(0) lgkmcnt(0)
s_barrier
v_xor_b32 v242, 0x10000, v242
K 212,168,169
Zvav 44,140,192
X 171
X 164
Zvav 48,144,192
Ra 128,242,0
K 213,170,171
X 165
Zvav 80,144,200
X 166
Zvav 112,144,208
Ra 132,242,1024
K 204,164,165
X 167
Zvav 144,144,216
X 160
Zvav 148,148,216
K 205,166,167
X 161
Zvav 116,148,208
Ra 136,242,2048
X 162
Zvav 84,148,200
K 196,160,161
X 163
Zvav 52,148,192
X 176
K 197,162,163
Zvav 56,152,192
Ra 140,242,3072
X 177
Zvav 88,152,200
X 178
K 198,176,177
Zvav 120,152,208
X 179
X 180
Zvav 152,152,216
K 199,178,179
Zvav 156,156,216
X 181
X 182
Zvav 124,156,208
K 206,180,181
X 183
Zvav 92,156,200
K 207,182,183
Zvav 60,156,192
s_cmp_eq_u64 s[24:25], 0
s_cbranch_scc1 .Ll1_34%=
W 0
Cvaa 160,128,0,224
Ra 144,242,4096
Ra 148,242,5120
Mvaa 160,132,4
s_add_u32 m0, s62, 0
Ra 152,242,6144
Cvaa 164,132,20,228
X 184
Mvaa 164,128,16
X 185
Cvaa 168,128,32,232
X 186
Mvaa 168,132,36
Ra 156,242,7168
K 214,184,185
Cvaa 172,132,52,236
Dc 1,8,61,0
Mvaa 172,128,48
X 187
Mvaa 160,136,8
X 188
Mvaa 160,140,12
Ra 128,242,16384
K 215,186,187
Mvaa 164,140,28
Dc 1,8,61,1024
Mvaa 164,136,24
X 189
Mvaa 168,136,40
X 190
Mvaa 168,140,44
Ra 132,242,17408
K 222,188,189
Mvaa 172,140,60
Dc 1,8,61,2048
Mvaa 172,136,56
X 191
v_smfmac_f32_16x16x64_bf16 a[160:163], a[176:179], v[192:199], v240
Ra 136,242,18432
K 223,190,191
v_smfmac_f32_16x16x64_bf16 a[164:167], a[176:179], v[200:207], v240
X 160
v_smfmac_f32_16x16x64_bf16 a[168:171], a[176:179], v[208:215], v240
Dc 1,8,61,3072
v_smfmac_f32_16x16x64_bf16 a[172:175], a[176:179], v[216:223], v240
W 3
X 161
Cvaa 176,144,0,224
s_add_u32 m0, s62, 4096
X 162
Mvaa 176,148,4
Ra 140,242,19456
K 192,160,161
Cvaa 180,148,20,228
X 163
Mvaa 180,144,16
Dc 243,8,61,0
Cvaa 184,144,32,232
X 164
Mvaa 184,148,36
K 193,162,163
Cvaa 188,148,52,236
X 165
Mvaa 188,144,48
Dc 243,8,61,1024
Ra 144,242,20480
Mvaa 176,152,8
X 166
Mvaa 176,156,12
Ra 148,242,21504
K 200,164,165
Mvaa 180,156,28
X 167
Mvaa 180,152,24
Dc 243,8,61,2048
Mvaa 184,152,40
X 168
Mvaa 184,156,44
K 201,166,167
Mvaa 188,156,60
X 169
Mvaa 188,152,56
W 2
Dc 243,8,61,3072
Ra 152,242,22528
Mvav 32,128,196
s_add_u32 m0, s62, 8192
X 170
Mvav 64,128,204
Ra 156,242,23552
K 208,168,169
Mvav 96,128,212
X 171
Mvav 128,128,220
Dc 244,8,61,0
Mvav 132,132,220
X 172
Mvav 100,132,212
Ra 128,242,8192
K 209,170,171
Mvav 68,132,204
X 173
Mvav 36,132,196
Dc 244,8,61,1024
Mvav 40,136,196
X 174
Mvav 72,136,204
Ra 132,242,9216
K 216,172,173
Mvav 104,136,212
X 175
Mvav 136,136,220
Dc 244,8,61,2048
Mvav 140,140,220
X 176
Mvav 108,140,212
Ra 136,242,10240
K 217,174,175
Mvav 76,140,204
X 177
Mvav 44,140,196
W 3
Dc 244,8,61,3072
Mvav 48,144,196
X 178
Mvav 80,144,204
s_add_u32 m0, s62, 12288
Ra 140,242,11264
Mvav 112,144,212
K 194,176,177
Mvav 144,144,220
X 179
Mvav 148,148,220
X 180
Mvav 116,148,212
Dc 245,8,61,0
Ra 144,242,12288
Mvav 84,148,204
K 195,178,179
Mvav 52,148,196
X 181
Mvav 56,152,196
X 182
Mvav 88,152,204
Dc 245,8,61,1024
Ra 148,242,13312
Mvav 120,152,212
s_cmp_eq_u32 s14, 2
s_cselect_b64 s[18:19], s[30:31], s[32:33]
K 202,180,181
Mvav 152,152,220
s_lshr_b32 s81, s15, 3
X 183
Mvav 156,156,220
s_mul_i32 s82, s81, 69120
X 184
Mvav 124,156,212
s_mul_i32 s83, s59, 448
Dc 245,8,61,2048
Ra 152,242,14336
Mvav 92,156,204
s_andn2_b32 s83, s83, 127
K 203,182,183
Mvav 60,156,196
W 3
X 185
Cvaa 160,128,0,224
s_add_u32 s82, s82, s83
X 186
Mvaa 160,132,4
Dc 245,8,61,3072
Ra 156,242,15360
Cvaa 164,132,20,228
s_add_u32 s61, s61, s63
v_min_u32 v246, 624, v1
v_lshrrev_b32 v247, 6, v246
Mvaa 164,128,16
K 210,184,185
v_mul_u32_u24 v247, 3456, v247
Cvaa 168,128,32,232
X 187
Mvaa 168,132,36
v_and_b32 v246, 48, v246
v_lshl_add_u32 v246, v246, 3, v247
Cvaa 172,132,52,236
s_xor_b32 s62, s62, 0x10000
X 188
Mvaa 172,128,48
v_add_u32 v246, s82, v246
K 211,186,187
Mvaa 160,136,8
global_load_dword v3, v246, s[18:19]
Ra 128,242,24576
Mvaa 160,140,12
s_add_u32 s64, s64, 1
X 189
Mvaa 164,140,28
Ra 132,242,25600
v_add_u32 v246, 34560, v246
Mvaa 164,136,24
X 190
Mvaa 168,136,40
v_min_u32 v246, 2211836, v246
K 218,188,189
Mvaa 168,140,44
global_load_dword v3, v246, s[18:19]
Mvaa 172,140,60
s_add_u32 s36, s29, 131072
X 191
Mvaa 172,136,56
W 2
X 160
Cvaa 176,144,0,224
Ra 136,242,26624
K 219,190,191
Mvaa 176,148,4
X 161
Cvaa 180,148,20,228
Ra 140,242,27648
Mvaa 180,144,16
X 162
Cvaa 184,144,32,232
buffer_store_dwordx4 a[124:127], v1, s[8:11], s36 offen offset:0 sc1
K 196,160,161
Mvaa 184,148,36
X 163
Cvaa 188,148,52,236
X 164
Mvaa 188,144,48
Ra 144,242,28672
K 197,162,163
Mvaa 176,152,8
X 165
Mvaa 176,156,12
Ra 148,242,29696
Mvaa 180,156,28
X 166
Mvaa 180,152,24
buffer_store_dwordx4 a[116:119], v1, s[8:11], s36 offen offset:1024 sc1
K 204,164,165
Mvaa 184,152,40
X 167
Mvaa 184,156,44
X 168
Mvaa 188,156,60
Mvaa 188,152,56
W 2
Ra 152,242,30720
K 205,166,167
Mvav 32,128,192
X 169
Mvav 64,128,200
buffer_store_dwordx4 a[108:111], v1, s[8:11], s36 offen offset:2048 sc1
Ra 156,242,31744
Mvav 96,128,208
X 170
Mvav 128,128,216
K 212,168,169
Mvav 132,132,216
X 171
Mvav 100,132,208
X 172
Mvav 68,132,200
buffer_store_dwordx4 a[204:207], v1, s[8:11], s36 offen offset:3072 sc1
Mvav 36,132,192
K 213,170,171
Mvav 40,136,192
X 173
Mvav 72,136,200
X 174
Mvav 104,136,208
Mvav 136,136,216
s_add_u32 s36, s29, 135168
K 220,172,173
Mvav 140,140,216
X 175
Mvav 108,140,208
)"
R"(X 176
Mvav 76,140,200
s_waitcnt vmcnt(6) lgkmcnt(0)
s_barrier
v_xor_b32 v242, 0x10000, v242
Mvav 44,140,192
Ra 128,242,0
K 221,174,175
Mvav 48,144,192
X 177
Mvav 80,144,200
buffer_store_dwordx4 a[212:215], v1, s[8:11], s36 offen offset:0 sc1
Mvav 112,144,208
X 178
Mvav 144,144,216
Ra 132,242,1024
K 198,176,177
Mvav 148,148,216
X 179
Mvav 116,148,208
X 180
Mvav 84,148,200
Ra 136,242,2048
Mvav 52,148,192
buffer_store_dwordx4 a[220:223], v1, s[8:11], s36 offen offset:1024 sc1
K 199,178,179
Mvav 56,152,192
X 181
Mvav 88,152,200
Ra 140,242,3072
Mvav 120,152,208
X 182
Mvav 152,152,216
K 206,180,181
Mvav 156,156,216
X 183
Mvav 124,156,208
Mvav 92,156,200
K 207,182,183
Mvav 60,156,192
W 0
Cvaa 160,128,0,224
Ra 144,242,4096
Ra 148,242,5120
Mvaa 160,132,4
s_add_u32 m0, s62, 0
Ra 152,242,6144
Cvaa 164,132,20,228
X 184
Mvaa 164,128,16
X 185
Cvaa 168,128,32,232
X 186
Mvaa 168,132,36
Ra 156,242,7168
K 214,184,185
Cvaa 172,132,52,236
Dc 1,8,61,0
Mvaa 172,128,48
X 187
Mvaa 160,136,8
X 188
Mvaa 160,140,12
Ra 128,242,16384
K 215,186,187
Mvaa 164,140,28
Dc 1,8,61,1024
Mvaa 164,136,24
X 189
Mvaa 168,136,40
X 190
Mvaa 168,140,44
Ra 132,242,17408
K 222,188,189
Mvaa 172,140,60
Dc 1,8,61,2048
Mvaa 172,136,56
X 191
v_smfmac_f32_16x16x64_bf16 a[160:163], a[176:179], v[192:199], v240
Ra 136,242,18432
K 223,190,191
v_smfmac_f32_16x16x64_bf16 a[164:167], a[176:179], v[200:207], v240
X 160
v_smfmac_f32_16x16x64_bf16 a[168:171], a[176:179], v[208:215], v240
Dc 1,8,61,3072
v_smfmac_f32_16x16x64_bf16 a[172:175], a[176:179], v[216:223], v240
W 3
X 161
Cvaa 176,144,0,224
s_add_u32 m0, s62, 4096
X 162
Mvaa 176,148,4
Ra 140,242,19456
K 192,160,161
Cvaa 180,148,20,228
X 163
Mvaa 180,144,16
Dc 243,8,61,0
Cvaa 184,144,32,232
X 164
Mvaa 184,148,36
K 193,162,163
Cvaa 188,148,52,236
X 165
Mvaa 188,144,48
Dc 243,8,61,1024
Ra 144,242,20480
Mvaa 176,152,8
X 166
Mvaa 176,156,12
Ra 148,242,21504
K 200,164,165
Mvaa 180,156,28
X 167
Mvaa 180,152,24
Dc 243,8,61,2048
Mvaa 184,152,40
X 168
Mvaa 184,156,44
K 201,166,167
Mvaa 188,156,60
X 169
Mvaa 188,152,56
W 2
Dc 243,8,61,3072
Ra 152,242,22528
Mvav 32,128,196
s_add_u32 m0, s62, 8192
X 170
Mvav 64,128,204
Ra 156,242,23552
K 208,168,169
Mvav 96,128,212
X 171
Mvav 128,128,220
Dc 244,8,61,0
Mvav 132,132,220
X 172
Mvav 100,132,212
Ra 128,242,8192
K 209,170,171
Mvav 68,132,204
X 173
Mvav 36,132,196
Dc 244,8,61,1024
Mvav 40,136,196
X 174
Mvav 72,136,204
Ra 132,242,9216
K 216,172,173
Mvav 104,136,212
X 175
Mvav 136,136,220
Dc 244,8,61,2048
Mvav 140,140,220
X 176
Mvav 108,140,212
Ra 136,242,10240
K 217,174,175
Mvav 76,140,204
X 177
Mvav 44,140,196
W 3
Dc 244,8,61,3072
Mvav 48,144,196
X 178
Mvav 80,144,204
s_add_u32 m0, s62, 12288
Ra 140,242,11264
Mvav 112,144,212
K 194,176,177
Mvav 144,144,220
X 179
Mvav 148,148,220
X 180
Mvav 116,148,212
Dc 245,8,61,0
Ra 144,242,12288
Mvav 84,148,204
K 195,178,179
Mvav 52,148,196
X 181
Mvav 56,152,196
X 182
Mvav 88,152,204
Dc 245,8,61,1024
Ra 148,242,13312
Mvav 120,152,212
K 202,180,181
Mvav 152,152,220
X 183
Mvav 156,156,220
X 184
Mvav 124,156,212
Dc 245,8,61,2048
Ra 152,242,14336
Mvav 92,156,204
K 203,182,183
v_bfe_u32 v28, v1, 4, 4
Mvav 60,156,196
W 3
X 185
Cvaa 160,128,0,224
Ra 156,242,15360
v_lshlrev_b32 v28, 3, v28
Mvaa 160,132,4
s_lshl_b32 s80, s56, 9
s_add_u32 s80, s80, 4224
Dc 245,8,61,3072
Cvaa 164,132,20,228
s_add_u32 s61, s61, s63
X 186
Mvaa 164,128,16
K 210,184,185
Cvaa 168,128,32,232
X 187
Mvaa 168,132,36
buffer_load_dwordx2 v[28:29], v28, s[8:11], s80 offen sc1
Cvaa 172,132,52,236
s_xor_b32 s62, s62, 0x10000
X 188
Mvaa 172,128,48
Ra 128,242,24576
K 211,186,187
Mvaa 160,136,8
X 189
Mvaa 160,140,12
s_add_u32 s64, s64, 1
X 190
Mvaa 164,140,28
Ra 132,242,25600
K 218,188,189
Mvaa 164,136,24
X 191
Mvaa 168,136,40
Mvaa 168,140,44
X 160
Mvaa 172,140,60
buffer_store_dwordx4 a[228:231], v1, s[8:11], s36 offen offset:2048 sc1
K 219,190,191
Mvaa 172,136,56
W 2
X 161
Cvaa 176,144,0,224
Ra 136,242,26624
Ra 140,242,27648
Mvaa 176,148,4
X 162
Cvaa 180,148,20,228
K 196,160,161
Mvaa 180,144,16
X 163
Cvaa 184,144,32,232
Mvaa 184,148,36
X 164
Cvaa 188,148,52,236
buffer_store_dwordx4 a[76:79], v1, s[8:11], s36 offen offset:3072 sc1
K 197,162,163
Mvaa 188,144,48
X 165
Mvaa 176,152,8
Ra 144,242,28672
Ra 148,242,29696
Mvaa 176,156,12
X 166
Mvaa 180,156,28
K 204,164,165
Mvaa 180,152,24
X 167
Mvaa 184,152,40
s_add_u32 s36, s29, 327680
Mvaa 184,156,44
X 168
Mvaa 188,156,60
Mvaa 188,152,56
W 2
Ra 152,242,30720
K 205,166,167
Mvav 32,128,192
X 169
Mvav 64,128,200
Ra 156,242,31744
Mvav 96,128,208
X 170
Mvav 128,128,216
buffer_store_dwordx4 a[84:87], v1, s[8:11], s36 offen offset:0 sc1
K 212,168,169
Mvav 132,132,216
X 171
Mvav 100,132,208
X 172
Mvav 68,132,200
Mvav 36,132,192
K 213,170,171
Mvav 40,136,192
X 173
Mvav 72,136,200
buffer_store_dwordx4 a[92:95], v1, s[8:11], s36 offen offset:1024 sc1
Mvav 104,136,208
X 174
Mvav 136,136,216
s_waitcnt vmcnt(4)
v_cmp_eq_u64 vcc, s[12:13], v[28:29]
s_cmp_eq_u64 vcc, -1
s_cbranch_scc0 .Lspin_67%=
.Lok_68%=:
K 220,172,173
Mvav 140,140,216
X 175
Mvav 108,140,208
X 176
Mvav 76,140,200
W 0
s_barrier
v_xor_b32 v242, 0x10000, v242
Mvav 44,140,192
buffer_store_dwordx4 a[188:191], v1, s[8:11], s36 offen offset:2048 sc1
K 221,174,175
Mvav 48,144,192
Ra 128,242,0
Mvav 80,144,200
X 177
Mvav 112,144,208
X 178
Mvav 144,144,216
Ra 132,242,1024
K 198,176,177
Mvav 148,148,216
X 179
Mvav 116,148,208
X 180
Mvav 84,148,200
Ra 136,242,2048
buffer_store_dwordx4 a[196:199], v1, s[8:11], s36 offen offset:3072 sc1
Mvav 52,148,192
K 199,178,179
Mvav 56,152,192
X 181
Mvav 88,152,200
Ra 140,242,3072
Mvav 120,152,208
X 182
Mvav 152,152,216
K 206,180,181
Mvav 156,156,216
X 183
Mvav 124,156,208
Mvav 92,156,200
K 207,182,183
Mvav 60,156,192
W 0
Cvaa 160,128,0,224
Ra 144,242,4096
Ra 148,242,5120
Mvaa 160,132,4
s_add_u32 m0, s62, 0
Ra 152,242,6144
Cvaa 164,132,20,228
X 184
Mvaa 164,128,16
X 185
Cvaa 168,128,32,232
X 186
Mvaa 168,132,36
Ra 156,242,7168
K 214,184,185
Cvaa 172,132,52,236
Dc 1,8,61,0
Mvaa 172,128,48
X 187
Mvaa 160,136,8
X 188
Mvaa 160,140,12
Ra 128,242,16384
K 215,186,187
Mvaa 164,140,28
Dc 1,8,61,1024
Mvaa 164,136,24
X 189
Mvaa 168,136,40
X 190
Mvaa 168,140,44
Ra 132,242,17408
K 222,188,189
Mvaa 172,140,60
Dc 1,8,61,2048
Mvaa 172,136,56
X 191
v_smfmac_f32_16x16x64_bf16 a[160:163], a[176:179], v[192:199], v240
Ra 136,242,18432
K 223,190,191
v_smfmac_f32_16x16x64_bf16 a[164:167], a[176:179], v[200:207], v240
X 160
v_smfmac_f32_16x16x64_bf16 a[168:171], a[176:179], v[208:215], v240
Dc 1,8,61,3072
v_smfmac_f32_16x16x64_bf16 a[172:175], a[176:179], v[216:223], v240
W 3
X 161
Cvaa 176,144,0,224
s_add_u32 m0, s62, 4096
)"
R"(X 162
Mvaa 176,148,4
Ra 140,242,19456
K 192,160,161
Cvaa 180,148,20,228
X 163
Mvaa 180,144,16
Dc 243,8,61,0
Cvaa 184,144,32,232
X 164
Mvaa 184,148,36
K 193,162,163
Cvaa 188,148,52,236
X 165
Mvaa 188,144,48
Dc 243,8,61,1024
Ra 144,242,20480
Mvaa 176,152,8
X 166
Mvaa 176,156,12
Ra 148,242,21504
K 200,164,165
Mvaa 180,156,28
X 167
Mvaa 180,152,24
Dc 243,8,61,2048
Mvaa 184,152,40
X 168
Mvaa 184,156,44
K 201,166,167
Mvaa 188,156,60
X 169
Mvaa 188,152,56
W 2
Dc 243,8,61,3072
Ra 152,242,22528
Mvav 32,128,196
s_add_u32 m0, s62, 8192
X 170
Mvav 64,128,204
Ra 156,242,23552
K 208,168,169
Mvav 96,128,212
X 171
Mvav 128,128,220
Dc 244,8,61,0
Mvav 132,132,220
X 172
Mvav 100,132,212
Ra 128,242,8192
K 209,170,171
Mvav 68,132,204
X 173
Mvav 36,132,196
Dc 244,8,61,1024
Mvav 40,136,196
X 174
Mvav 72,136,204
Ra 132,242,9216
K 216,172,173
Mvav 104,136,212
X 175
Mvav 136,136,220
Dc 244,8,61,2048
Mvav 140,140,220
X 176
Mvav 108,140,212
Ra 136,242,10240
K 217,174,175
Mvav 76,140,204
X 177
Mvav 44,140,196
W 3
Dc 244,8,61,3072
Mvav 48,144,196
X 178
Mvav 80,144,204
s_add_u32 m0, s62, 12288
Ra 140,242,11264
Mvav 112,144,212
K 194,176,177
Mvav 144,144,220
X 179
Mvav 148,148,220
X 180
Mvav 116,148,212
Dc 245,8,61,0
Ra 144,242,12288
Mvav 84,148,204
K 195,178,179
Mvav 52,148,196
X 181
Mvav 56,152,196
X 182
Mvav 88,152,204
Dc 245,8,61,1024
Ra 148,242,13312
Mvav 120,152,212
K 202,180,181
Mvav 152,152,220
X 183
Mvav 156,156,220
X 184
Mvav 124,156,212
Dc 245,8,61,2048
Ra 152,242,14336
Mvav 92,156,204
s_mov_b64 s[18:19], s[34:35]
K 203,182,183
Mvav 60,156,196
W 3
X 185
Cvaa 160,128,0,224
s_lshr_b32 s81, s15, 3
X 186
Mvaa 160,132,4
s_lshl_b32 s82, s59, 5
Dc 245,8,61,3072
Ra 156,242,15360
Cvaa 164,132,20,228
s_add_u32 s81, s81, s82
s_add_u32 s61, s61, s63
K 210,184,185
Mvaa 164,128,16
s_lshl_b32 s81, s81, 1
X 187
Cvaa 168,128,32,232
s_add_u32 s81, s81, s14
X 188
Mvaa 168,132,36
s_sub_u32 s81, s81, 2
s_mul_i32 s81, s81, 4480
Cvaa 172,132,52,236
s_xor_b32 s62, s62, 0x10000
K 211,186,187
v_min_u32 v246, 544, v1
Mvaa 172,128,48
X 189
Mvaa 160,136,8
Ra 128,242,24576
v_lshl_add_u32 v246, v246, 3, s81
Mvaa 160,140,12
s_add_u32 s64, s64, 1
X 190
Mvaa 164,140,28
Ra 132,242,25600
v_min_u32 v246, 2211836, v246
Mvaa 164,136,24
global_load_dword v3, v246, s[18:19]
K 218,188,189
Mvaa 168,136,40
X 191
Mvaa 168,140,44
X 160
Mvaa 172,140,60
s_add_u32 s36, s29, 331776
K 219,190,191
Mvaa 172,136,56
W 2
X 161
Cvaa 176,144,0,224
Ra 136,242,26624
Ra 140,242,27648
Mvaa 176,148,4
X 162
Cvaa 180,148,20,228
K 196,160,161
Mvaa 180,144,16
X 163
Cvaa 184,144,32,232
buffer_store_dwordx4 a[236:239], v1, s[8:11], s36 offen offset:0 sc1
Mvaa 184,148,36
X 164
Cvaa 188,148,52,236
K 197,162,163
Mvaa 188,144,48
X 165
Mvaa 176,152,8
Ra 144,242,28672
Ra 148,242,29696
Mvaa 176,156,12
X 166
Mvaa 180,156,28
buffer_store_dwordx4 a[244:247], v1, s[8:11], s36 offen offset:1024 sc1
K 204,164,165
Mvaa 180,152,24
X 167
Mvaa 184,152,40
Mvaa 184,156,44
X 168
Mvaa 188,156,60
Mvaa 188,152,56
W 2
Ra 152,242,30720
K 205,166,167
Mvav 32,128,192
X 169
Mvav 64,128,200
buffer_store_dwordx4 a[68:71], v1, s[8:11], s36 offen offset:2048 sc1
Ra 156,242,31744
Mvav 96,128,208
X 170
Mvav 128,128,216
K 212,168,169
Mvav 132,132,216
X 171
Mvav 100,132,208
X 172
Mvav 68,132,200
buffer_store_dwordx4 a[184:187], v1, s[8:11], s36 offen offset:3072 sc1
Mvav 36,132,192
K 213,170,171
Mvav 40,136,192
X 173
Mvav 72,136,200
X 174
Mvav 104,136,208
Mvav 136,136,216
s_add_u32 s36, s29, 196608
K 220,172,173
Mvav 140,140,216
X 175
Mvav 108,140,208
X 176
Mvav 76,140,200
s_waitcnt vmcnt(5) lgkmcnt(0)
s_barrier
v_xor_b32 v242, 0x10000, v242
Mvav 44,140,192
Ra 128,242,0
K 221,174,175
Mvav 48,144,192
X 177
Mvav 80,144,200
buffer_store_dwordx4 a[192:195], v1, s[8:11], s36 offen offset:0 sc1
Mvav 112,144,208
X 178
Mvav 144,144,216
Ra 132,242,1024
K 198,176,177
Mvav 148,148,216
X 179
Mvav 116,148,208
X 180
Mvav 84,148,200
Ra 136,242,2048
Mvav 52,148,192
buffer_store_dwordx4 a[200:203], v1, s[8:11], s36 offen offset:1024 sc1
K 199,178,179
Mvav 56,152,192
X 181
Mvav 88,152,200
Ra 140,242,3072
Mvav 120,152,208
X 182
Mvav 152,152,216
K 206,180,181
Mvav 156,156,216
X 183
Mvav 124,156,208
Mvav 92,156,200
K 207,182,183
Mvav 60,156,192
W 0
Cvaa 160,128,0,224
Ra 144,242,4096
Ra 148,242,5120
Mvaa 160,132,4
s_add_u32 m0, s62, 0
Ra 152,242,6144
Cvaa 164,132,20,228
X 184
Mvaa 164,128,16
X 185
Cvaa 168,128,32,232
X 186
Mvaa 168,132,36
Ra 156,242,7168
K 214,184,185
Cvaa 172,132,52,236
Dc 1,8,61,0
Mvaa 172,128,48
X 187
Mvaa 160,136,8
X 188
Mvaa 160,140,12
Ra 128,242,16384
K 215,186,187
Mvaa 164,140,28
Dc 1,8,61,1024
Mvaa 164,136,24
X 189
Mvaa 168,136,40
X 190
Mvaa 168,140,44
Ra 132,242,17408
K 222,188,189
Mvaa 172,140,60
Dc 1,8,61,2048
Mvaa 172,136,56
X 191
v_smfmac_f32_16x16x64_bf16 a[160:163], a[176:179], v[192:199], v240
Ra 136,242,18432
K 223,190,191
v_smfmac_f32_16x16x64_bf16 a[164:167], a[176:179], v[200:207], v240
X 160
v_smfmac_f32_16x16x64_bf16 a[168:171], a[176:179], v[208:215], v240
Dc 1,8,61,3072
v_smfmac_f32_16x16x64_bf16 a[172:175], a[176:179], v[216:223], v240
W 3
X 161
Cvaa 176,144,0,224
s_add_u32 m0, s62, 4096
X 162
Mvaa 176,148,4
Ra 140,242,19456
K 192,160,161
Cvaa 180,148,20,228
X 163
Mvaa 180,144,16
Dc 243,8,61,0
Cvaa 184,144,32,232
X 164
Mvaa 184,148,36
K 193,162,163
Cvaa 188,148,52,236
X 165
Mvaa 188,144,48
Dc 243,8,61,1024
Ra 144,242,20480
Mvaa 176,152,8
X 166
Mvaa 176,156,12
Ra 148,242,21504
K 200,164,165
Mvaa 180,156,28
X 167
Mvaa 180,152,24
Dc 243,8,61,2048
Mvaa 184,152,40
X 168
Mvaa 184,156,44
K 201,166,167
Mvaa 188,156,60
X 169
Mvaa 188,152,56
W 2
Dc 243,8,61,3072
Ra 152,242,22528
Mvav 32,128,196
s_add_u32 m0, s62, 8192
X 170
Mvav 64,128,204
Ra 156,242,23552
K 208,168,169
Mvav 96,128,212
X 171
Mvav 128,128,220
Dc 244,8,61,0
Mvav 132,132,220
X 172
Mvav 100,132,212
Ra 128,242,8192
K 209,170,171
Mvav 68,132,204
X 173
Mvav 36,132,196
Dc 244,8,61,1024
Mvav 40,136,196
X 174
Mvav 72,136,204
Ra 132,242,9216
K 216,172,173
Mvav 104,136,212
X 175
Mvav 136,136,220
Dc 244,8,61,2048
Mvav 140,140,220
X 176
Mvav 108,140,212
Ra 136,242,10240
K 217,174,175
Mvav 76,140,204
X 177
Mvav 44,140,196
W 3
Dc 244,8,61,3072
Mvav 48,144,196
X 178
Mvav 80,144,204
s_add_u32 m0, s62, 12288
Ra 140,242,11264
Mvav 112,144,212
K 194,176,177
Mvav 144,144,220
X 179
Mvav 148,148,220
X 180
Mvav 116,148,212
Dc 245,8,61,0
)"
R"(Ra 144,242,12288
Mvav 84,148,204
K 195,178,179
Mvav 52,148,196
X 181
Mvav 56,152,196
X 182
Mvav 88,152,204
Dc 245,8,61,1024
Ra 148,242,13312
Mvav 120,152,212
K 202,180,181
Mvav 152,152,220
X 183
Mvav 156,156,220
s_waitcnt vmcnt(16)
s_add_u32 s80, s71, 256
v_mov_b64 v[28:29], s[12:13]
s_mov_b64 exec, 1
buffer_store_dwordx2 v[28:29], off, s[8:11], s80 sc1
s_mov_b64 exec, -1
X 184
Mvav 124,156,212
Ra 152,242,14336
Mvav 92,156,204
K 203,182,183
Mvav 60,156,196
W 3
X 185
Cvaa 160,128,0,224
Dc 245,8,61,2048
Ra 156,242,15360
Mvaa 160,132,4
X 186
Cvaa 164,132,20,228
K 210,184,185
Mvaa 164,128,16
X 187
Cvaa 168,128,32,232
Dc 245,8,61,3072
Mvaa 168,132,36
s_add_u32 s61, s61, s63
X 188
Cvaa 172,132,52,236
s_xor_b32 s62, s62, 0x10000
K 211,186,187
Mvaa 172,128,48
X 189
Mvaa 160,136,8
Ra 128,242,24576
Ra 132,242,25600
Mvaa 160,140,12
s_add_u32 s64, s64, 1
X 190
Mvaa 164,140,28
K 218,188,189
Mvaa 164,136,24
X 191
Mvaa 168,136,40
Mvaa 168,140,44
X 160
Mvaa 172,140,60
buffer_store_dwordx4 a[208:211], v1, s[8:11], s36 offen offset:2048 sc1
K 219,190,191
Mvaa 172,136,56
W 2
X 161
Cvaa 176,144,0,224
Ra 136,242,26624
Ra 140,242,27648
Mvaa 176,148,4
X 162
Cvaa 180,148,20,228
K 196,160,161
Mvaa 180,144,16
X 163
Cvaa 184,144,32,232
Mvaa 184,148,36
X 164
Cvaa 188,148,52,236
buffer_store_dwordx4 a[216:219], v1, s[8:11], s36 offen offset:3072 sc1
K 197,162,163
Mvaa 188,144,48
X 165
Mvaa 176,152,8
Ra 144,242,28672
Ra 148,242,29696
Mvaa 176,156,12
X 166
Mvaa 180,156,28
K 204,164,165
Mvaa 180,152,24
X 167
Mvaa 184,152,40
s_add_u32 s36, s29, 200704
Mvaa 184,156,44
X 168
Mvaa 188,156,60
Mvaa 188,152,56
W 2
Ra 152,242,30720
K 205,166,167
Mvav 32,128,192
X 169
Mvav 64,128,200
Ra 156,242,31744
Mvav 96,128,208
X 170
Mvav 128,128,216
buffer_store_dwordx4 a[224:227], v1, s[8:11], s36 offen offset:0 sc1
K 212,168,169
Mvav 132,132,216
X 171
Mvav 100,132,208
X 172
Mvav 68,132,200
Mvav 36,132,192
K 213,170,171
Mvav 40,136,192
X 173
Mvav 72,136,200
buffer_store_dwordx4 a[232:235], v1, s[8:11], s36 offen offset:1024 sc1
Mvav 104,136,208
X 174
Mvav 136,136,216
K 220,172,173
Mvav 140,140,216
X 175
Mvav 108,140,208
X 176
Mvav 76,140,200
s_waitcnt vmcnt(4) lgkmcnt(0)
s_barrier
v_xor_b32 v242, 0x10000, v242
Mvav 44,140,192
buffer_store_dwordx4 a[240:243], v1, s[8:11], s36 offen offset:2048 sc1
K 221,174,175
Mvav 48,144,192
Ra 128,242,0
Mvav 80,144,200
X 177
Mvav 112,144,208
X 178
Mvav 144,144,216
Ra 132,242,1024
K 198,176,177
Mvav 148,148,216
X 179
Mvav 116,148,208
X 180
Mvav 84,148,200
Ra 136,242,2048
buffer_store_dwordx4 a[100:103], v1, s[8:11], s36 offen offset:3072 sc1
Mvav 52,148,192
K 199,178,179
Mvav 56,152,192
X 181
Mvav 88,152,200
Ra 140,242,3072
Mvav 120,152,208
X 182
Mvav 152,152,216
K 206,180,181
Mvav 156,156,216
X 183
Mvav 124,156,208
Mvav 92,156,200
K 207,182,183
Mvav 60,156,192
W 0
Cvaa 160,128,0,224
Ra 144,242,4096
Ra 148,242,5120
Mvaa 160,132,4
s_add_u32 m0, s62, 0
Ra 152,242,6144
Cvaa 164,132,20,228
X 184
Mvaa 164,128,16
X 185
Cvaa 168,128,32,232
X 186
Mvaa 168,132,36
Ra 156,242,7168
K 214,184,185
Cvaa 172,132,52,236
Dc 1,8,61,0
Mvaa 172,128,48
X 187
Mvaa 160,136,8
X 188
Mvaa 160,140,12
Ra 128,242,16384
K 215,186,187
Mvaa 164,140,28
Dc 1,8,61,1024
Mvaa 164,136,24
X 189
Mvaa 168,136,40
X 190
Mvaa 168,140,44
Ra 132,242,17408
K 222,188,189
Mvaa 172,140,60
Dc 1,8,61,2048
Mvaa 172,136,56
X 191
v_smfmac_f32_16x16x64_bf16 a[160:163], a[176:179], v[192:199], v240
Ra 136,242,18432
K 223,190,191
v_smfmac_f32_16x16x64_bf16 a[164:167], a[176:179], v[200:207], v240
X 160
v_smfmac_f32_16x16x64_bf16 a[168:171], a[176:179], v[208:215], v240
Dc 1,8,61,3072
v_smfmac_f32_16x16x64_bf16 a[172:175], a[176:179], v[216:223], v240
W 3
X 161
Cvaa 176,144,0,224
s_add_u32 m0, s62, 4096
X 162
Mvaa 176,148,4
Ra 140,242,19456
K 192,160,161
Cvaa 180,148,20,228
X 163
Mvaa 180,144,16
Dc 243,8,61,0
Cvaa 184,144,32,232
X 164
Mvaa 184,148,36
K 193,162,163
Cvaa 188,148,52,236
X 165
Mvaa 188,144,48
Dc 243,8,61,1024
Ra 144,242,20480
Mvaa 176,152,8
X 166
Mvaa 176,156,12
Ra 148,242,21504
K 200,164,165
Mvaa 180,156,28
X 167
Mvaa 180,152,24
Dc 243,8,61,2048
Mvaa 184,152,40
X 168
Mvaa 184,156,44
K 201,166,167
Mvaa 188,156,60
X 169
Mvaa 188,152,56
W 2
Dc 243,8,61,3072
Ra 152,242,22528
Mvav 32,128,196
s_add_u32 m0, s62, 8192
X 170
Mvav 64,128,204
Ra 156,242,23552
K 208,168,169
Mvav 96,128,212
X 171
Mvav 128,128,220
Dc 244,8,61,0
Mvav 132,132,220
X 172
Mvav 100,132,212
Ra 128,242,8192
K 209,170,171
Mvav 68,132,204
X 173
Mvav 36,132,196
Dc 244,8,61,1024
Mvav 40,136,196
X 174
Mvav 72,136,204
Ra 132,242,9216
K 216,172,173
Mvav 104,136,212
X 175
Mvav 136,136,220
Dc 244,8,61,2048
Mvav 140,140,220
X 176
Mvav 108,140,212
Ra 136,242,10240
K 217,174,175
Mvav 76,140,204
X 177
Mvav 44,140,196
W 3
Dc 244,8,61,3072
Mvav 48,144,196
X 178
Mvav 80,144,204
s_add_u32 m0, s62, 12288
Ra 140,242,11264
Mvav 112,144,212
K 194,176,177
Mvav 144,144,220
X 179
Mvav 148,148,220
X 180
Mvav 116,148,212
Dc 245,8,61,0
Ra 144,242,12288
Mvav 84,148,204
K 195,178,179
Mvav 52,148,196
X 181
Mvav 56,152,196
X 182
Mvav 88,152,204
Dc 245,8,61,1024
Ra 148,242,13312
Mvav 120,152,212
K 202,180,181
Mvav 152,152,220
X 183
Mvav 156,156,220
X 184
Mvav 124,156,212
Dc 245,8,61,2048
Ra 152,242,14336
Mvav 92,156,204
K 203,182,183
Mvav 60,156,196
W 3
X 185
Cvaa 160,128,0,224
X 186
Mvaa 160,132,4
Dc 245,8,61,3072
Ra 156,242,15360
Cvaa 164,132,20,228
s_add_u32 s61, s61, s63
K 210,184,185
Mvaa 164,128,16
X 187
Cvaa 168,128,32,232
X 188
Mvaa 168,132,36
Cvaa 172,132,52,236
s_xor_b32 s62, s62, 0x10000
K 211,186,187
Mvaa 172,128,48
X 189
Mvaa 160,136,8
Ra 128,242,24576
Ra 132,242,25600
Mvaa 160,140,12
s_add_u32 s64, s64, 1
X 190
Mvaa 164,140,28
K 218,188,189
Mvaa 164,136,24
X 191
Mvaa 168,136,40
Mvaa 168,140,44
X 160
Mvaa 172,140,60
s_add_u32 s36, s29, 262144
K 219,190,191
Mvaa 172,136,56
W 2
X 161
Cvaa 176,144,0,224
Ra 136,242,26624
Ra 140,242,27648
Mvaa 176,148,4
X 162
Cvaa 180,148,20,228
K 196,160,161
Mvaa 180,144,16
X 163
Cvaa 184,144,32,232
buffer_store_dwordx4 a[64:67], v1, s[8:11], s36 offen offset:0 sc1
Mvaa 184,148,36
X 164
Cvaa 188,148,52,236
K 197,162,163
Mvaa 188,144,48
X 165
Mvaa 176,152,8
Ra 144,242,28672
Ra 148,242,29696
Mvaa 176,156,12
X 166
Mvaa 180,156,28
buffer_store_dwordx4 a[72:75], v1, s[8:11], s36 offen offset:1024 sc1
K 204,164,165
Mvaa 180,152,24
X 167
Mvaa 184,152,40
Mvaa 184,156,44
X 168
Mvaa 188,156,60
Mvaa 188,152,56
W 2
)"
R"(Ra 152,242,30720
K 205,166,167
Mvav 32,128,192
X 169
Mvav 64,128,200
buffer_store_dwordx4 a[80:83], v1, s[8:11], s36 offen offset:2048 sc1
Ra 156,242,31744
Mvav 96,128,208
X 170
Mvav 128,128,216
K 212,168,169
Mvav 132,132,216
X 171
Mvav 100,132,208
X 172
Mvav 68,132,200
buffer_store_dwordx4 a[88:91], v1, s[8:11], s36 offen offset:3072 sc1
Mvav 36,132,192
K 213,170,171
Mvav 40,136,192
X 173
Mvav 72,136,200
X 174
Mvav 104,136,208
Mvav 136,136,216
s_add_u32 s36, s29, 266240
K 220,172,173
Mvav 140,140,216
X 175
Mvav 108,140,208
X 176
Mvav 76,140,200
s_waitcnt vmcnt(4) lgkmcnt(0)
s_barrier
v_xor_b32 v242, 0x10000, v242
Mvav 44,140,192
Ra 128,242,0
K 221,174,175
Mvav 48,144,192
X 177
Mvav 80,144,200
buffer_store_dwordx4 a[96:99], v1, s[8:11], s36 offen offset:0 sc1
Mvav 112,144,208
X 178
Mvav 144,144,216
Ra 132,242,1024
K 198,176,177
Mvav 148,148,216
X 179
Mvav 116,148,208
X 180
Mvav 84,148,200
Ra 136,242,2048
Mvav 52,148,192
buffer_store_dwordx4 a[104:107], v1, s[8:11], s36 offen offset:1024 sc1
K 199,178,179
Mvav 56,152,192
X 181
Mvav 88,152,200
Ra 140,242,3072
Mvav 120,152,208
X 182
Mvav 152,152,216
K 206,180,181
Mvav 156,156,216
X 183
Mvav 124,156,208
Mvav 92,156,200
K 207,182,183
Mvav 60,156,192
W 0
Cvaa 160,128,0,224
Ra 144,242,4096
Ra 148,242,5120
Mvaa 160,132,4
s_add_u32 m0, s62, 0
Ra 152,242,6144
Cvaa 164,132,20,228
X 184
Mvaa 164,128,16
X 185
Cvaa 168,128,32,232
X 186
Mvaa 168,132,36
Ra 156,242,7168
K 214,184,185
Cvaa 172,132,52,236
Dc 1,8,61,0
Mvaa 172,128,48
X 187
Mvaa 160,136,8
X 188
Mvaa 160,140,12
Ra 128,242,16384
K 215,186,187
Mvaa 164,140,28
Dc 1,8,61,1024
Mvaa 164,136,24
X 189
Mvaa 168,136,40
X 190
Mvaa 168,140,44
Ra 132,242,17408
K 222,188,189
Mvaa 172,140,60
Dc 1,8,61,2048
Mvaa 172,136,56
X 191
v_smfmac_f32_16x16x64_bf16 a[160:163], a[176:179], v[192:199], v240
Ra 136,242,18432
K 223,190,191
v_smfmac_f32_16x16x64_bf16 a[164:167], a[176:179], v[200:207], v240
X 160
v_smfmac_f32_16x16x64_bf16 a[168:171], a[176:179], v[208:215], v240
Dc 1,8,61,3072
v_smfmac_f32_16x16x64_bf16 a[172:175], a[176:179], v[216:223], v240
W 3
X 161
Cvaa 176,144,0,224
s_add_u32 m0, s62, 4096
X 162
Mvaa 176,148,4
Ra 140,242,19456
K 192,160,161
Cvaa 180,148,20,228
X 163
Mvaa 180,144,16
Dc 243,8,61,0
Cvaa 184,144,32,232
X 164
Mvaa 184,148,36
K 193,162,163
Cvaa 188,148,52,236
X 165
Mvaa 188,144,48
Dc 243,8,61,1024
Ra 144,242,20480
Mvaa 176,152,8
X 166
Mvaa 176,156,12
Ra 148,242,21504
K 200,164,165
Mvaa 180,156,28
X 167
Mvaa 180,152,24
Dc 243,8,61,2048
Mvaa 184,152,40
X 168
Mvaa 184,156,44
K 201,166,167
Mvaa 188,156,60
X 169
Mvaa 188,152,56
W 2
Dc 243,8,61,3072
Ra 152,242,22528
Mvav 32,128,196
s_add_u32 m0, s62, 8192
X 170
Mvav 64,128,204
Ra 156,242,23552
K 208,168,169
Mvav 96,128,212
X 171
Mvav 128,128,220
Dc 244,8,61,0
Mvav 132,132,220
X 172
Mvav 100,132,212
Ra 128,242,8192
K 209,170,171
Mvav 68,132,204
X 173
Mvav 36,132,196
Dc 244,8,61,1024
Mvav 40,136,196
X 174
Mvav 72,136,204
Ra 132,242,9216
K 216,172,173
Mvav 104,136,212
X 175
Mvav 136,136,220
Dc 244,8,61,2048
Mvav 140,140,220
X 176
Mvav 108,140,212
Ra 136,242,10240
K 217,174,175
Mvav 76,140,204
X 177
Mvav 44,140,196
W 3
Dc 244,8,61,3072
Mvav 48,144,196
X 178
Mvav 80,144,204
s_add_u32 m0, s62, 12288
Ra 140,242,11264
Mvav 112,144,212
K 194,176,177
Mvav 144,144,220
X 179
Mvav 148,148,220
X 180
Mvav 116,148,212
Dc 245,8,61,0
Ra 144,242,12288
Mvav 84,148,204
K 195,178,179
Mvav 52,148,196
X 181
Mvav 56,152,196
X 182
Mvav 88,152,204
Dc 245,8,61,1024
Ra 148,242,13312
Mvav 120,152,212
K 202,180,181
Mvav 152,152,220
X 183
Mvav 156,156,220
X 184
Mvav 124,156,212
Dc 245,8,61,2048
Ra 152,242,14336
Mvav 92,156,204
K 203,182,183
v_bfe_u32 v28, v1, 4, 4
Mvav 60,156,196
W 3
X 185
Cvaa 160,128,0,224
Ra 156,242,15360
v_lshlrev_b32 v28, 3, v28
Mvaa 160,132,4
s_lshl_b32 s80, s56, 9
s_add_u32 s80, s80, 4352
Dc 245,8,61,3072
Cvaa 164,132,20,228
s_add_u32 s61, s61, s63
X 186
Mvaa 164,128,16
K 210,184,185
Cvaa 168,128,32,232
X 187
Mvaa 168,132,36
buffer_load_dwordx2 v[28:29], v28, s[8:11], s80 offen sc1
Cvaa 172,132,52,236
s_xor_b32 s62, s62, 0x10000
X 188
Mvaa 172,128,48
Ra 128,242,24576
K 211,186,187
Mvaa 160,136,8
X 189
Mvaa 160,140,12
s_add_u32 s64, s64, 1
X 190
Mvaa 164,140,28
Ra 132,242,25600
K 218,188,189
Mvaa 164,136,24
X 191
Mvaa 168,136,40
Mvaa 168,140,44
X 160
Mvaa 172,140,60
buffer_store_dwordx4 a[112:115], v1, s[8:11], s36 offen offset:2048 sc1
K 219,190,191
Mvaa 172,136,56
W 2
X 161
Cvaa 176,144,0,224
Ra 136,242,26624
Ra 140,242,27648
Mvaa 176,148,4
X 162
Cvaa 180,148,20,228
K 196,160,161
Mvaa 180,144,16
X 163
Cvaa 184,144,32,232
Mvaa 184,148,36
X 164
Cvaa 188,148,52,236
K 197,162,163
Mvaa 188,144,48
X 165
Mvaa 176,152,8
Ra 144,242,28672
Ra 148,242,29696
Mvaa 176,156,12
X 166
Mvaa 180,156,28
K 204,164,165
Mvaa 180,152,24
X 167
Mvaa 184,152,40
Mvaa 184,156,44
X 168
Mvaa 188,156,60
Mvaa 188,152,56
W 2
Ra 152,242,30720
K 205,166,167
Mvav 32,128,192
X 169
Mvav 64,128,200
Ra 156,242,31744
Mvav 96,128,208
X 170
Mvav 128,128,216
K 212,168,169
Mvav 132,132,216
X 171
Mvav 100,132,208
X 172
Mvav 68,132,200
buffer_store_dwordx4 a[120:123], v1, s[8:11], s36 offen offset:3072 sc1
Mvav 36,132,192
K 213,170,171
Mvav 40,136,192
X 173
Mvav 72,136,200
X 174
Mvav 104,136,208
Mvav 136,136,216
s_waitcnt vmcnt(2)
v_cmp_eq_u64 vcc, s[12:13], v[28:29]
s_cmp_eq_u64 vcc, -1
s_cbranch_scc0 .Lspin_70%=
.Lok_71%=:
K 220,172,173
Mvav 140,140,216
X 175
Mvav 108,140,208
X 176
Mvav 76,140,200
W 0
s_barrier
v_xor_b32 v242, 0x10000, v242
Mvav 44,140,192
Ra 128,242,0
K 221,174,175
Mvav 48,144,192
X 177
Mvav 80,144,200
X 178
Mvav 112,144,208
Ra 132,242,1024
Mvav 144,144,216
K 198,176,177
Mvav 148,148,216
X 179
Mvav 116,148,208
X 180
Mvav 84,148,200
Ra 136,242,2048
Mvav 52,148,192
K 199,178,179
Mvav 56,152,192
X 181
Mvav 88,152,200
Ra 140,242,3072
Mvav 120,152,208
X 182
Mvav 152,152,216
K 206,180,181
Mvav 156,156,216
X 183
Mvav 124,156,208
Mvav 92,156,200
K 207,182,183
Mvav 60,156,192
W 0
Cvaa 160,128,0,224
Ra 144,242,4096
Ra 148,242,5120
Mvaa 160,132,4
s_add_u32 m0, s62, 0
Ra 152,242,6144
Cvaa 164,132,20,228
X 184
Mvaa 164,128,16
X 185
Cvaa 168,128,32,232
X 186
Mvaa 168,132,36
Ra 156,242,7168
K 214,184,185
Cvaa 172,132,52,236
Dc 1,8,61,0
Mvaa 172,128,48
X 187
Mvaa 160,136,8
X 188
Mvaa 160,140,12
Ra 128,242,16384
K 215,186,187
)"
R"(Mvaa 164,140,28
Dc 1,8,61,1024
Mvaa 164,136,24
X 189
Mvaa 168,136,40
X 190
Mvaa 168,140,44
Ra 132,242,17408
K 222,188,189
Mvaa 172,140,60
Dc 1,8,61,2048
Mvaa 172,136,56
X 191
v_smfmac_f32_16x16x64_bf16 a[160:163], a[176:179], v[192:199], v240
Ra 136,242,18432
K 223,190,191
v_smfmac_f32_16x16x64_bf16 a[164:167], a[176:179], v[200:207], v240
X 160
v_smfmac_f32_16x16x64_bf16 a[168:171], a[176:179], v[208:215], v240
Dc 1,8,61,3072
v_smfmac_f32_16x16x64_bf16 a[172:175], a[176:179], v[216:223], v240
W 3
X 161
Cvaa 176,144,0,224
s_add_u32 m0, s62, 4096
X 162
Mvaa 176,148,4
Ra 140,242,19456
K 192,160,161
Cvaa 180,148,20,228
X 163
Mvaa 180,144,16
Dc 243,8,61,0
Cvaa 184,144,32,232
X 164
Mvaa 184,148,36
K 193,162,163
Cvaa 188,148,52,236
X 165
Mvaa 188,144,48
Dc 243,8,61,1024
Ra 144,242,20480
Mvaa 176,152,8
X 166
Mvaa 176,156,12
Ra 148,242,21504
K 200,164,165
Mvaa 180,156,28
X 167
Mvaa 180,152,24
Dc 243,8,61,2048
Mvaa 184,152,40
X 168
Mvaa 184,156,44
K 201,166,167
Mvaa 188,156,60
X 169
Mvaa 188,152,56
W 2
Dc 243,8,61,3072
Ra 152,242,22528
Mvav 32,128,196
s_add_u32 m0, s62, 8192
X 170
Mvav 64,128,204
Ra 156,242,23552
K 208,168,169
Mvav 96,128,212
X 171
Mvav 128,128,220
Dc 244,8,61,0
Mvav 132,132,220
X 172
Mvav 100,132,212
Ra 128,242,8192
K 209,170,171
Mvav 68,132,204
X 173
Mvav 36,132,196
Dc 244,8,61,1024
Mvav 40,136,196
X 174
Mvav 72,136,204
Ra 132,242,9216
K 216,172,173
Mvav 104,136,212
X 175
Mvav 136,136,220
Dc 244,8,61,2048
Mvav 140,140,220
X 176
Mvav 108,140,212
Ra 136,242,10240
K 217,174,175
Mvav 76,140,204
X 177
Mvav 44,140,196
W 3
Dc 244,8,61,3072
Mvav 48,144,196
X 178
Mvav 80,144,204
s_add_u32 m0, s62, 12288
Ra 140,242,11264
Mvav 112,144,212
K 194,176,177
Mvav 144,144,220
X 179
Mvav 148,148,220
X 180
Mvav 116,148,212
Dc 245,8,61,0
Ra 144,242,12288
Mvav 84,148,204
K 195,178,179
Mvav 52,148,196
X 181
Mvav 56,152,196
X 182
Mvav 88,152,204
Dc 245,8,61,1024
Ra 148,242,13312
Mvav 120,152,212
K 202,180,181
Mvav 152,152,220
s_add_u32 s81, s57, 2
X 183
Mvav 156,156,220
s_waitcnt vmcnt(14)
s_add_u32 s80, s71, 384
v_mov_b64 v[28:29], s[12:13]
s_mov_b64 exec, 1
buffer_store_dwordx2 v[28:29], off, s[8:11], s80 sc1
s_mov_b64 exec, -1
X 184
Mvav 124,156,212
s_bitcmp0_b32 s81, 2
s_cselect_b32 s22, -1, 0
s_and_b32 s81, s81, 3
Ra 152,242,14336
Mvav 92,156,204
s_lshl_b32 s82, s56, 2
K 203,182,183
Mvav 60,156,196
W 3
X 185
Cvaa 160,128,0,224
s_add_u32 s81, s81, s82
Dc 245,8,61,2048
Ra 156,242,15360
Mvaa 160,132,4
s_lshl_b32 s81, s81, 1
X 186
Cvaa 164,132,20,228
s_add_u32 s81, s81, s14
s_sub_u32 s81, s81, 2
K 210,184,185
Mvaa 164,128,16
s_mul_i32 s81, s81, 20480
X 187
Cvaa 168,128,32,232
Dc 245,8,61,3072
v_min_u32 v2, 624, v1
Mvaa 168,132,36
s_add_u32 s61, s61, s63
X 188
Cvaa 172,132,52,236
s_xor_b32 s62, s62, 0x10000
v_lshl_add_u32 v2, v2, 3, s81
K 211,186,187
Mvaa 172,128,48
Ra 128,242,24576
v_and_b32 v2, s22, v2
Mvaa 160,136,8
s_and_b32 s22, s22, 5120
X 189
Mvaa 160,140,12
s_add_u32 s64, s64, 1
global_load_dword v3, v2, s[26:27]
Ra 132,242,25600
Mvaa 164,140,28
X 190
Mvaa 164,136,24
v_add_u32 v2, s22, v2
K 218,188,189
Mvaa 168,136,40
X 191
Mvaa 168,140,44
X 160
Mvaa 172,140,60
K 219,190,191
Mvaa 172,136,56
W 2
X 161
Cvaa 176,144,0,224
Ra 136,242,26624
Ra 140,242,27648
Mvaa 176,148,4
X 162
Cvaa 180,148,20,228
K 196,160,161
Mvaa 180,144,16
X 163
Cvaa 184,144,32,232
Mvaa 184,148,36
X 164
Cvaa 188,148,52,236
K 197,162,163
Mvaa 188,144,48
X 165
Mvaa 176,152,8
Ra 144,242,28672
Ra 148,242,29696
Mvaa 176,156,12
X 166
Mvaa 180,156,28
K 204,164,165
Mvaa 180,152,24
X 167
Mvaa 184,152,40
Mvaa 184,156,44
X 168
Mvaa 188,156,60
Mvaa 188,152,56
W 2
Ra 152,242,30720
K 205,166,167
Mvav 32,128,192
X 169
Mvav 64,128,200
Ra 156,242,31744
Mvav 96,128,208
X 170
Mvav 128,128,216
K 212,168,169
Mvav 132,132,216
X 171
Mvav 100,132,208
X 172
Mvav 68,132,200
Mvav 36,132,192
K 213,170,171
Mvav 40,136,192
X 173
Mvav 72,136,200
X 174
Mvav 104,136,208
Mvav 136,136,216
K 220,172,173
Mvav 140,140,216
X 175
Mvav 108,140,208
X 176
Mvav 76,140,200
s_waitcnt vmcnt(1) lgkmcnt(0)
s_barrier
v_xor_b32 v242, 0x10000, v242
Mvav 44,140,192
Ra 128,242,0
K 221,174,175
Mvav 48,144,192
X 177
Mvav 80,144,200
X 178
Mvav 112,144,208
Ra 132,242,1024
Mvav 144,144,216
K 198,176,177
Mvav 148,148,216
X 179
Mvav 116,148,208
X 180
Mvav 84,148,200
Ra 136,242,2048
Mvav 52,148,192
K 199,178,179
Mvav 56,152,192
X 181
Mvav 88,152,200
Ra 140,242,3072
Mvav 120,152,208
X 182
Mvav 152,152,216
K 206,180,181
Mvav 156,156,216
X 183
Mvav 124,156,208
Mvav 92,156,200
K 207,182,183
Mvav 60,156,192
W 0
Cvaa 160,128,0,224
Ra 144,242,4096
Ra 148,242,5120
Mvaa 160,132,4
s_add_u32 m0, s62, 0
Ra 152,242,6144
Cvaa 164,132,20,228
X 184
Mvaa 164,128,16
X 185
Cvaa 168,128,32,232
X 186
Mvaa 168,132,36
Ra 156,242,7168
K 214,184,185
Cvaa 172,132,52,236
Dc 1,8,61,0
Mvaa 172,128,48
X 187
Mvaa 160,136,8
X 188
Mvaa 160,140,12
Ra 128,242,16384
K 215,186,187
Mvaa 164,140,28
Dc 1,8,61,1024
Mvaa 164,136,24
X 189
Mvaa 168,136,40
X 190
Mvaa 168,140,44
Ra 132,242,17408
K 222,188,189
Mvaa 172,140,60
Dc 1,8,61,2048
Mvaa 172,136,56
X 191
v_smfmac_f32_16x16x64_bf16 a[160:163], a[176:179], v[192:199], v240
Ra 136,242,18432
K 223,190,191
v_smfmac_f32_16x16x64_bf16 a[164:167], a[176:179], v[200:207], v240
X 160
v_smfmac_f32_16x16x64_bf16 a[168:171], a[176:179], v[208:215], v240
Dc 1,8,61,3072
v_smfmac_f32_16x16x64_bf16 a[172:175], a[176:179], v[216:223], v240
W 3
X 161
Cvaa 176,144,0,224
s_add_u32 m0, s62, 4096
X 162
Mvaa 176,148,4
Ra 140,242,19456
K 192,160,161
Cvaa 180,148,20,228
X 163
Mvaa 180,144,16
Dc 243,8,61,0
Cvaa 184,144,32,232
X 164
Mvaa 184,148,36
K 193,162,163
Cvaa 188,148,52,236
X 165
Mvaa 188,144,48
Dc 243,8,61,1024
Ra 144,242,20480
Mvaa 176,152,8
X 166
Mvaa 176,156,12
Ra 148,242,21504
K 200,164,165
Mvaa 180,156,28
X 167
Mvaa 180,152,24
Dc 243,8,61,2048
Mvaa 184,152,40
X 168
Mvaa 184,156,44
K 201,166,167
Mvaa 188,156,60
X 169
Mvaa 188,152,56
W 2
Dc 243,8,61,3072
Ra 152,242,22528
Mvav 32,128,196
s_add_u32 m0, s62, 8192
X 170
Mvav 64,128,204
Ra 156,242,23552
K 208,168,169
Mvav 96,128,212
X 171
Mvav 128,128,220
Dc 244,8,61,0
Mvav 132,132,220
X 172
Mvav 100,132,212
Ra 128,242,8192
K 209,170,171
Mvav 68,132,204
X 173
Mvav 36,132,196
Dc 244,8,61,1024
Mvav 40,136,196
X 174
Mvav 72,136,204
Ra 132,242,9216
K 216,172,173
)"
R"(Mvav 104,136,212
X 175
Mvav 136,136,220
Dc 244,8,61,2048
Mvav 140,140,220
X 176
Mvav 108,140,212
Ra 136,242,10240
K 217,174,175
Mvav 76,140,204
X 177
Mvav 44,140,196
W 3
Dc 244,8,61,3072
Mvav 48,144,196
X 178
Mvav 80,144,204
s_add_u32 m0, s62, 12288
Ra 140,242,11264
Mvav 112,144,212
K 194,176,177
Mvav 144,144,220
X 179
Mvav 148,148,220
X 180
Mvav 116,148,212
Dc 245,8,61,0
Ra 144,242,12288
Mvav 84,148,204
K 195,178,179
Mvav 52,148,196
X 181
Mvav 56,152,196
X 182
Mvav 88,152,204
Dc 245,8,61,1024
Ra 148,242,13312
Mvav 120,152,212
K 202,180,181
Mvav 152,152,220
X 183
Mvav 156,156,220
X 184
Mvav 124,156,212
Dc 245,8,61,2048
Ra 152,242,14336
Mvav 92,156,204
K 203,182,183
Mvav 60,156,196
W 3
X 185
Cvaa 160,128,0,224
X 186
Mvaa 160,132,4
Dc 245,8,61,3072
Ra 156,242,15360
Cvaa 164,132,20,228
s_add_u32 s61, s61, s63
K 210,184,185
Mvaa 164,128,16
X 187
Cvaa 168,128,32,232
X 188
Mvaa 168,132,36
Cvaa 172,132,52,236
s_xor_b32 s62, s62, 0x10000
global_load_dword v3, v2, s[26:27]
K 211,186,187
Mvaa 172,128,48
X 189
Mvaa 160,136,8
Ra 128,242,24576
Ra 132,242,25600
Mvaa 160,140,12
s_add_u32 s64, s64, 1
X 190
Mvaa 164,140,28
v_add_u32 v2, s22, v2
K 218,188,189
Mvaa 164,136,24
X 191
Mvaa 168,136,40
Mvaa 168,140,44
X 160
Mvaa 172,140,60
K 219,190,191
Mvaa 172,136,56
W 2
X 161
Cvaa 176,144,0,224
Ra 136,242,26624
Ra 140,242,27648
Mvaa 176,148,4
X 162
Cvaa 180,148,20,228
K 196,160,161
Mvaa 180,144,16
X 163
Cvaa 184,144,32,232
Mvaa 184,148,36
X 164
Cvaa 188,148,52,236
K 197,162,163
Mvaa 188,144,48
X 165
Mvaa 176,152,8
Ra 144,242,28672
Ra 148,242,29696
Mvaa 176,156,12
X 166
Mvaa 180,156,28
K 204,164,165
Mvaa 180,152,24
X 167
Mvaa 184,152,40
Mvaa 184,156,44
X 168
Mvaa 188,156,60
Mvaa 188,152,56
W 2
Ra 152,242,30720
K 205,166,167
Mvav 32,128,192
X 169
Mvav 64,128,200
Ra 156,242,31744
Mvav 96,128,208
X 170
Mvav 128,128,216
K 212,168,169
Mvav 132,132,216
X 171
Mvav 100,132,208
X 172
Mvav 68,132,200
Mvav 36,132,192
K 213,170,171
Mvav 40,136,192
X 173
Mvav 72,136,200
X 174
Mvav 104,136,208
Mvav 136,136,216
K 220,172,173
Mvav 140,140,216
X 175
Mvav 108,140,208
X 176
Mvav 76,140,200
s_waitcnt vmcnt(1) lgkmcnt(0)
s_barrier
v_xor_b32 v242, 0x10000, v242
Mvav 44,140,192
Ra 128,242,0
K 221,174,175
Mvav 48,144,192
X 177
Mvav 80,144,200
X 178
Mvav 112,144,208
Ra 132,242,1024
Mvav 144,144,216
K 198,176,177
Mvav 148,148,216
X 179
Mvav 116,148,208
X 180
Mvav 84,148,200
Ra 136,242,2048
Mvav 52,148,192
K 199,178,179
Mvav 56,152,192
X 181
Mvav 88,152,200
Ra 140,242,3072
Mvav 120,152,208
X 182
Mvav 152,152,216
K 206,180,181
Mvav 156,156,216
X 183
Mvav 124,156,208
Mvav 92,156,200
K 207,182,183
Mvav 60,156,192
W 0
Cvaa 160,128,0,224
Ra 144,242,4096
Ra 148,242,5120
Mvaa 160,132,4
s_add_u32 m0, s62, 0
Ra 152,242,6144
Cvaa 164,132,20,228
X 184
Mvaa 164,128,16
X 185
Cvaa 168,128,32,232
X 186
Mvaa 168,132,36
Ra 156,242,7168
K 214,184,185
Cvaa 172,132,52,236
Dc 1,8,61,0
Mvaa 172,128,48
X 187
Mvaa 160,136,8
X 188
Mvaa 160,140,12
Ra 128,242,16384
K 215,186,187
Mvaa 164,140,28
Dc 1,8,61,1024
Mvaa 164,136,24
X 189
Mvaa 168,136,40
X 190
Mvaa 168,140,44
Ra 132,242,17408
K 222,188,189
Mvaa 172,140,60
Dc 1,8,61,2048
Mvaa 172,136,56
X 191
v_smfmac_f32_16x16x64_bf16 a[160:163], a[176:179], v[192:199], v240
Ra 136,242,18432
K 223,190,191
v_smfmac_f32_16x16x64_bf16 a[164:167], a[176:179], v[200:207], v240
X 160
v_smfmac_f32_16x16x64_bf16 a[168:171], a[176:179], v[208:215], v240
Dc 1,8,61,3072
v_smfmac_f32_16x16x64_bf16 a[172:175], a[176:179], v[216:223], v240
W 3
X 161
Cvaa 176,144,0,224
s_add_u32 m0, s62, 4096
X 162
Mvaa 176,148,4
Ra 140,242,19456
K 192,160,161
Cvaa 180,148,20,228
X 163
Mvaa 180,144,16
Dc 243,8,61,0
Cvaa 184,144,32,232
X 164
Mvaa 184,148,36
K 193,162,163
Cvaa 188,148,52,236
X 165
Mvaa 188,144,48
Dc 243,8,61,1024
Ra 144,242,20480
Mvaa 176,152,8
X 166
Mvaa 176,156,12
Ra 148,242,21504
K 200,164,165
Mvaa 180,156,28
X 167
Mvaa 180,152,24
Dc 243,8,61,2048
Mvaa 184,152,40
X 168
Mvaa 184,156,44
K 201,166,167
Mvaa 188,156,60
X 169
Mvaa 188,152,56
W 2
Dc 243,8,61,3072
Ra 152,242,22528
Mvav 32,128,196
s_add_u32 m0, s62, 8192
X 170
Mvav 64,128,204
Ra 156,242,23552
K 208,168,169
Mvav 96,128,212
X 171
Mvav 128,128,220
Dc 244,8,61,0
Mvav 132,132,220
X 172
Mvav 100,132,212
Ra 128,242,8192
K 209,170,171
Mvav 68,132,204
X 173
Mvav 36,132,196
Dc 244,8,61,1024
Mvav 40,136,196
X 174
Mvav 72,136,204
Ra 132,242,9216
K 216,172,173
Mvav 104,136,212
X 175
Mvav 136,136,220
Dc 244,8,61,2048
Mvav 140,140,220
X 176
Mvav 108,140,212
Ra 136,242,10240
K 217,174,175
Mvav 76,140,204
X 177
Mvav 44,140,196
W 3
Dc 244,8,61,3072
Mvav 48,144,196
X 178
Mvav 80,144,204
s_add_u32 m0, s62, 12288
Ra 140,242,11264
Mvav 112,144,212
K 194,176,177
Mvav 144,144,220
X 179
Mvav 148,148,220
X 180
Mvav 116,148,212
Dc 245,8,61,0
Ra 144,242,12288
Mvav 84,148,204
K 195,178,179
Mvav 52,148,196
X 181
Mvav 56,152,196
X 182
Mvav 88,152,204
Dc 245,8,61,1024
Ra 148,242,13312
Mvav 120,152,212
K 202,180,181
Mvav 152,152,220
X 183
Mvav 156,156,220
X 184
Mvav 124,156,212
Dc 245,8,61,2048
Ra 152,242,14336
Mvav 92,156,204
K 203,182,183
Mvav 60,156,196
W 3
X 185
Cvaa 160,128,0,224
X 186
Mvaa 160,132,4
Dc 245,8,61,3072
Ra 156,242,15360
Cvaa 164,132,20,228
s_add_u32 s61, s61, s63
K 210,184,185
Mvaa 164,128,16
X 187
Cvaa 168,128,32,232
X 188
Mvaa 168,132,36
Cvaa 172,132,52,236
s_xor_b32 s62, s62, 0x10000
global_load_dword v3, v2, s[26:27]
K 211,186,187
Mvaa 172,128,48
Ra 128,242,24576
v_add_u32 v2, s22, v2
Mvaa 160,136,8
s_lshr_b32 s80, s15, 3
s_lshl_b32 s80, s80, 1
s_add_u32 s80, s80, s14
Mvaa 160,140,12
s_sub_u32 s80, s80, 2
s_lshl_b32 s80, s80, 12
v_min_u32 v27, 496, v1
Mvaa 164,140,28
s_add_u32 s64, s64, 1
X 189
Mvaa 164,136,24
Ra 132,242,25600
v_lshl_add_u32 v27, v27, 3, s80
Mvaa 168,136,40
X 190
Mvaa 168,140,44
v_min_u32 v27, .Lend%=-.Lbeg%=-4, v27
K 218,188,189
Mvaa 172,140,60
global_load_dword v3, v27, s[6:7]
Mvaa 172,136,56
W 2
X 191
Cvaa 176,144,0,224
X 160
Mvaa 176,148,4
Ra 136,242,26624
K 219,190,191
Cvaa 180,148,20,228
X 161
Mvaa 180,144,16
Ra 140,242,27648
Cvaa 184,144,32,232
)"
R"(X 162
Mvaa 184,148,36
K 196,160,161
Cvaa 188,148,52,236
X 163
Mvaa 188,144,48
X 164
Mvaa 176,152,8
Ra 144,242,28672
K 197,162,163
Mvaa 176,156,12
X 165
Mvaa 180,156,28
Ra 148,242,29696
Mvaa 180,152,24
X 166
Mvaa 184,152,40
K 204,164,165
Mvaa 184,156,44
X 167
Mvaa 188,156,60
X 168
Mvaa 188,152,56
W 2
Ra 152,242,30720
K 205,166,167
Mvav 32,128,192
X 169
Mvav 64,128,200
Ra 156,242,31744
Mvav 96,128,208
X 170
Mvav 128,128,216
K 212,168,169
Mvav 132,132,216
X 171
Mvav 100,132,208
X 172
Mvav 68,132,200
Mvav 36,132,192
K 213,170,171
Mvav 40,136,192
X 173
Mvav 72,136,200
X 174
Mvav 104,136,208
Mvav 136,136,216
K 220,172,173
Mvav 140,140,216
X 175
Mvav 108,140,208
X 176
Mvav 76,140,200
s_waitcnt vmcnt(2) lgkmcnt(0)
s_barrier
v_xor_b32 v242, 0x10000, v242
Mvav 44,140,192
Ra 128,242,0
K 221,174,175
Mvav 48,144,192
X 177
Mvav 80,144,200
X 178
Mvav 112,144,208
Ra 132,242,1024
Mvav 144,144,216
K 198,176,177
Mvav 148,148,216
X 179
Mvav 116,148,208
X 180
Mvav 84,148,200
Ra 136,242,2048
Mvav 52,148,192
K 199,178,179
Mvav 56,152,192
X 181
Mvav 88,152,200
Ra 140,242,3072
Mvav 120,152,208
X 182
Mvav 152,152,216
K 206,180,181
Mvav 156,156,216
X 183
Mvav 124,156,208
Mvav 92,156,200
K 207,182,183
Mvav 60,156,192
W 0
Cvaa 160,128,0,224
Ra 144,242,4096
Ra 148,242,5120
Mvaa 160,132,4
s_add_u32 m0, s62, 0
Ra 152,242,6144
Cvaa 164,132,20,228
X 184
Mvaa 164,128,16
X 185
Cvaa 168,128,32,232
X 186
Mvaa 168,132,36
Ra 156,242,7168
K 214,184,185
Cvaa 172,132,52,236
Dc 1,8,61,0
Mvaa 172,128,48
X 187
Mvaa 160,136,8
X 188
Mvaa 160,140,12
Ra 128,242,16384
K 215,186,187
Mvaa 164,140,28
Dc 1,8,61,1024
Mvaa 164,136,24
X 189
Mvaa 168,136,40
X 190
Mvaa 168,140,44
Ra 132,242,17408
K 222,188,189
Mvaa 172,140,60
Dc 1,8,61,2048
Mvaa 172,136,56
X 191
v_smfmac_f32_16x16x64_bf16 a[160:163], a[176:179], v[192:199], v240
Ra 136,242,18432
K 223,190,191
v_smfmac_f32_16x16x64_bf16 a[164:167], a[176:179], v[200:207], v240
X 160
v_smfmac_f32_16x16x64_bf16 a[168:171], a[176:179], v[208:215], v240
Dc 1,8,61,3072
v_smfmac_f32_16x16x64_bf16 a[172:175], a[176:179], v[216:223], v240
W 3
X 161
Cvaa 176,144,0,224
s_add_u32 m0, s62, 4096
X 162
Mvaa 176,148,4
s_sub_u32 s81, s57, 1
Ra 140,242,19456
K 192,160,161
Cvaa 180,148,20,228
X 163
Mvaa 180,144,16
s_lshl_b32 s85, s81, 5
Dc 243,8,61,0
Cvaa 184,144,32,232
X 164
Mvaa 184,148,36
s_add_u32 s85, s85, s56
K 193,162,163
Cvaa 188,148,52,236
X 165
Mvaa 188,144,48
Dc 243,8,61,1024
Ra 144,242,20480
Mvaa 176,152,8
s_lshl_b32 s85, s85, 1
X 166
Mvaa 176,156,12
Ra 148,242,21504
K 200,164,165
Mvaa 180,156,28
s_add_u32 s85, s85, s14
X 167
Mvaa 180,152,24
Dc 243,8,61,2048
Mvaa 184,152,40
s_sub_u32 s85, s85, 2
X 168
Mvaa 184,156,44
K 201,166,167
Mvaa 188,156,60
X 169
Mvaa 188,152,56
W 2
s_cmp_lt_u32 s85, 160
s_cselect_b64 s[20:21], 0, -1
Dc 243,8,61,3072
Mvav 32,128,196
s_add_u32 m0, s62, 8192
Ra 152,242,22528
Ra 156,242,23552
Mvav 64,128,204
s_mul_i32 s18, s85, 205
X 170
Mvav 96,128,212
K 208,168,169
Mvav 128,128,220
s_lshr_b32 s18, s18, 12
X 171
Mvav 132,132,220
Dc 244,8,61,0
Ra 128,242,8192
Mvav 100,132,212
X 172
Mvav 68,132,204
s_and_b32 s18, s18, 7
K 209,170,171
Mvav 36,132,196
X 173
Mvav 40,136,196
Dc 244,8,61,1024
v_lshrrev_b32 v4, 1, v1
Mvav 72,136,204
X 174
Mvav 104,136,212
Ra 132,242,9216
K 216,172,173
Mvav 136,136,220
X 175
Mvav 140,140,220
Dc 244,8,61,2048
v_min_u32 v5, 344, v4
Mvav 108,140,212
s_lshl_b32 s81, s18, 3
X 176
Mvav 76,140,204
Ra 136,242,10240
K 217,174,175
Mvav 44,140,196
W 3
X 177
Mvav 48,144,196
Dc 244,8,61,3072
v_mov_b32 v6, s81
Mvav 80,144,204
s_add_u32 m0, s62, 12288
X 178
Mvav 112,144,212
Ra 140,242,11264
K 194,176,177
Mvav 144,144,220
X 179
Mvav 148,148,220
buffer_load_dwordx2 v[8:9], v4, s[8:11], 0 offen offset:2560 sc1
Ra 144,242,12288
Mvav 116,148,212
X 180
Mvav 84,148,204
K 195,178,179
Mvav 52,148,196
X 181
Mvav 56,152,196
buffer_load_dwordx2 v[10:11], v5, s[8:11], 0 offen offset:3072 sc1
Ra 148,242,13312
Mvav 88,152,204
X 182
Mvav 120,152,212
K 202,180,181
Mvav 152,152,220
X 183
Mvav 156,156,220
Dc 245,8,61,0
Ra 152,242,14336
Mvav 124,156,212
X 184
Mvav 92,156,204
K 203,182,183
v_bfe_u32 v28, v1, 4, 4
Mvav 60,156,196
W 3
X 185
Cvaa 160,128,0,224
buffer_load_dwordx2 v[12:13], v6, s[8:11], 0 offen offset:3584 sc1
v_lshlrev_b32 v28, 3, v28
Mvaa 160,132,4
s_lshl_b32 s80, s56, 9
X 186
Cvaa 164,132,20,228
s_add_u32 s80, s80, 4480
Ra 156,242,15360
K 210,184,185
Mvaa 164,128,16
X 187
Cvaa 168,128,32,232
Dc 245,8,61,1024
Mvaa 168,132,36
X 188
Cvaa 172,132,52,236
K 211,186,187
Mvaa 172,128,48
X 189
Mvaa 160,136,8
Dc 245,8,61,2048
Ra 128,242,24576
Mvaa 160,140,12
X 190
Mvaa 164,140,28
Ra 132,242,25600
K 218,188,189
Mvaa 164,136,24
X 191
Mvaa 168,136,40
buffer_load_dwordx2 v[28:29], v28, s[8:11], s80 offen sc1
Mvaa 168,140,44
Dc 245,8,61,3072
s_add_u32 s61, s61, s63
s_xor_b32 s62, s62, 0x10000
s_add_u32 s64, s64, 1
Mvaa 172,140,60
X 160
Mvaa 172,136,56
W 2
Ra 136,242,26624
K 219,190,191
Cvaa 176,144,0,224
X 161
Mvaa 176,148,4
global_load_dword v3, v2, s[26:27]
v_add_u32 v2, s22, v2
Cvaa 180,148,20,228
Ra 140,242,27648
Mvaa 180,144,16
X 162
Cvaa 184,144,32,232
K 196,160,161
Mvaa 184,148,36
X 163
Cvaa 188,148,52,236
X 164
Mvaa 188,144,48
Ra 144,242,28672
K 197,162,163
Mvaa 176,152,8
X 165
Mvaa 176,156,12
Ra 148,242,29696
Mvaa 180,156,28
X 166
Mvaa 180,152,24
K 204,164,165
Mvaa 184,152,40
X 167
Mvaa 184,156,44
X 168
Mvaa 188,156,60
Mvaa 188,152,56
W 2
Ra 152,242,30720
K 205,166,167
Mvav 32,128,192
X 169
Mvav 64,128,200
Ra 156,242,31744
Mvav 96,128,208
X 170
Mvav 128,128,216
K 212,168,169
Mvav 132,132,216
X 171
Mvav 100,132,208
X 172
Mvav 68,132,200
Mvav 36,132,192
K 213,170,171
Mvav 40,136,192
X 173
Mvav 72,136,200
X 174
Mvav 104,136,208
Mvav 136,136,216
s_waitcnt vmcnt(2)
v_cmp_eq_u64 vcc, s[12:13], v[28:29]
s_cmp_eq_u64 vcc, -1
s_cbranch_scc0 .Lspin_73%=
.Lok_74%=:
v_cmp_eq_u64 vcc, s[12:13], v[8:9]
v_cmp_eq_u64_e64 s[86:87], s[12:13], v[10:11]
s_and_b64 vcc, vcc, s[86:87]
s_or_b64 vcc, vcc, s[20:21]
s_cmp_eq_u64 vcc, -1
s_cbranch_scc0 .Lspin_76%=
.Lok_77%=:
K 220,172,173
Mvav 140,140,216
X 175
Mvav 108,140,208
X 176
Mvav 76,140,200
s_waitcnt vmcnt(1) lgkmcnt(0)
s_barrier
v_xor_b32 v242, 0x10000, v242
Mvav 44,140,192
Ra 128,242,0
K 221,174,175
Mvav 48,144,192
X 177
Mvav 80,144,200
X 178
Mvav 112,144,208
Ra 132,242,1024
Mvav 144,144,216
K 198,176,177
Mvav 148,148,216
X 179
Mvav 116,148,208
)"
R"(X 180
Mvav 84,148,200
Ra 136,242,2048
Mvav 52,148,192
K 199,178,179
Mvav 56,152,192
X 181
Mvav 88,152,200
Ra 140,242,3072
Mvav 120,152,208
X 182
Mvav 152,152,216
K 206,180,181
Mvav 156,156,216
X 183
Mvav 124,156,208
Mvav 92,156,200
K 207,182,183
Mvav 60,156,192
W 0
Cvaa 160,128,0,224
Ra 144,242,4096
Ra 148,242,5120
Mvaa 160,132,4
s_add_u32 m0, s62, 0
Ra 152,242,6144
Cvaa 164,132,20,228
X 184
Mvaa 164,128,16
X 185
Cvaa 168,128,32,232
X 186
Mvaa 168,132,36
Ra 156,242,7168
K 214,184,185
Cvaa 172,132,52,236
Dc 1,8,61,0
Mvaa 172,128,48
X 187
Mvaa 160,136,8
X 188
Mvaa 160,140,12
Ra 128,242,16384
K 215,186,187
Mvaa 164,140,28
Dc 1,8,61,1024
Mvaa 164,136,24
X 189
Mvaa 168,136,40
X 190
Mvaa 168,140,44
Ra 132,242,17408
K 222,188,189
Mvaa 172,140,60
Dc 1,8,61,2048
Mvaa 172,136,56
X 191
v_smfmac_f32_16x16x64_bf16 a[160:163], a[176:179], v[192:199], v240
Ra 136,242,18432
K 223,190,191
v_smfmac_f32_16x16x64_bf16 a[164:167], a[176:179], v[200:207], v240
X 160
v_smfmac_f32_16x16x64_bf16 a[168:171], a[176:179], v[208:215], v240
Dc 1,8,61,3072
v_smfmac_f32_16x16x64_bf16 a[172:175], a[176:179], v[216:223], v240
W 3
X 161
Cvaa 176,144,0,224
s_add_u32 m0, s62, 4096
X 162
Mvaa 176,148,4
Ra 140,242,19456
K 192,160,161
Cvaa 180,148,20,228
X 163
Mvaa 180,144,16
Dc 243,8,61,0
Cvaa 184,144,32,232
X 164
Mvaa 184,148,36
K 193,162,163
Cvaa 188,148,52,236
X 165
Mvaa 188,144,48
Dc 243,8,61,1024
Ra 144,242,20480
Mvaa 176,152,8
X 166
Mvaa 176,156,12
Ra 148,242,21504
K 200,164,165
Mvaa 180,156,28
X 167
Mvaa 180,152,24
Dc 243,8,61,2048
Mvaa 184,152,40
X 168
Mvaa 184,156,44
K 201,166,167
Mvaa 188,156,60
X 169
Mvaa 188,152,56
W 2
Dc 243,8,61,3072
Ra 152,242,22528
Mvav 32,128,196
s_add_u32 m0, s62, 8192
X 170
Mvav 64,128,204
Ra 156,242,23552
K 208,168,169
Mvav 96,128,212
X 171
Mvav 128,128,220
Dc 244,8,61,0
Mvav 132,132,220
X 172
Mvav 100,132,212
Ra 128,242,8192
K 209,170,171
Mvav 68,132,204
X 173
Mvav 36,132,196
Dc 244,8,61,1024
Mvav 40,136,196
X 174
Mvav 72,136,204
Ra 132,242,9216
K 216,172,173
Mvav 104,136,212
X 175
Mvav 136,136,220
Dc 244,8,61,2048
Mvav 140,140,220
X 176
Mvav 108,140,212
Ra 136,242,10240
K 217,174,175
Mvav 76,140,204
X 177
Mvav 44,140,196
W 3
Dc 244,8,61,3072
Mvav 48,144,196
X 178
Mvav 80,144,204
s_add_u32 m0, s62, 12288
Ra 140,242,11264
Mvav 112,144,212
K 194,176,177
Mvav 144,144,220
X 179
Mvav 148,148,220
X 180
Mvav 116,148,212
Dc 245,8,61,0
Ra 144,242,12288
Mvav 84,148,204
K 195,178,179
Mvav 52,148,196
X 181
Mvav 56,152,196
X 182
Mvav 88,152,204
Dc 245,8,61,1024
Ra 148,242,13312
Mvav 120,152,212
v_xor_b32 v6, s12, v12
K 202,180,181
Mvav 152,152,220
X 183
Mvav 156,156,220
Ra 152,242,14336
v_and_b32 v6, 2047, v6
Mvav 124,156,212
Dc 245,8,61,2048
v_lshlrev_b32 v6, 3, v6
Mvav 92,156,204
X 184
Mvav 60,156,196
W 3
K 203,182,183
Cvaa 160,128,0,224
s_mov_b32 s81, 40960
X 185
Mvaa 160,132,4
Dc 245,8,61,3072
Ra 156,242,15360
Cvaa 164,132,20,228
s_add_u32 s61, s61, s63
X 186
Mvaa 164,128,16
K 210,184,185
Cvaa 168,128,32,232
X 187
Mvaa 168,132,36
buffer_load_dword v7, v6, s[8:11], s81 offen sc1
v_lshrrev_b32 v5, 7, v1
Cvaa 172,132,52,236
s_xor_b32 s62, s62, 0x10000
X 188
Mvaa 172,128,48
K 211,186,187
v_mul_u32_u24 v4, 20480, v5
Mvaa 160,136,8
X 189
Mvaa 160,140,12
s_add_u32 s64, s64, 1
Ra 128,242,24576
Ra 132,242,25600
Mvaa 164,140,28
X 190
Mvaa 164,136,24
v_and_b32 v6, 0x70, v1
K 218,188,189
Mvaa 168,136,40
X 191
Mvaa 168,140,44
v_add_u32 v4, v4, v6
Mvaa 172,140,60
X 160
Mvaa 172,136,56
W 2
v_add_u32 v5, 2, v5
K 219,190,191
Cvaa 176,144,0,224
X 161
Mvaa 176,148,4
Ra 136,242,26624
v_lshrrev_b32 v5, 3, v5
Cvaa 180,148,20,228
Ra 140,242,27648
v_lshl_or_b32 v5, v5, 31, v4
Mvaa 180,144,16
X 162
Cvaa 184,144,32,232
K 196,160,161
Mvaa 184,148,36
s_lshl_b32 s19, s85, 7
X 163
Cvaa 188,148,52,236
X 164
Mvaa 188,144,48
s_add_u32 s19, s19, 2752512
Ra 144,242,28672
K 197,162,163
Mvaa 176,152,8
X 165
Mvaa 176,156,12
s_and_b32 s81, s20, 134217728
Ra 148,242,29696
Mvaa 180,156,28
X 166
Mvaa 180,152,24
K 204,164,165
Mvaa 184,152,40
s_or_b32 s19, s19, s81
X 167
Mvaa 184,156,44
X 168
Mvaa 188,156,60
buffer_load_dwordx4 a[64:67], v4, s[8:11], s19 offen sc1
Mvaa 188,152,56
W 2
Ra 152,242,30720
K 205,166,167
Mvav 32,128,192
s_add_u32 s19, s19, 163840
X 169
Mvav 64,128,200
Ra 156,242,31744
Mvav 96,128,208
X 170
Mvav 128,128,216
buffer_load_dwordx4 a[68:71], v4, s[8:11], s19 offen sc1
K 212,168,169
Mvav 132,132,216
X 171
Mvav 100,132,208
s_add_u32 s19, s19, 163840
X 172
Mvav 68,132,200
Mvav 36,132,192
buffer_load_dwordx4 a[72:75], v4, s[8:11], s19 offen sc1
K 213,170,171
Mvav 40,136,192
X 173
Mvav 72,136,200
X 174
Mvav 104,136,208
s_add_u32 s19, s19, 163840
Mvav 136,136,216
K 220,172,173
Mvav 140,140,216
X 175
Mvav 108,140,208
buffer_load_dwordx4 a[76:79], v4, s[8:11], s19 offen sc1
Mvav 76,140,200
s_waitcnt vmcnt(5) lgkmcnt(0)
s_barrier
v_xor_b32 v242, 0x10000, v242
Mvav 44,140,192
s_add_u32 s19, s19, 163840
X 176
Mvav 48,144,192
Ra 128,242,0
K 221,174,175
Mvav 80,144,200
X 177
Mvav 112,144,208
buffer_load_dwordx4 a[80:83], v4, s[8:11], s19 offen sc1
Ra 132,242,1024
Mvav 144,144,216
s_add_u32 s19, s19, 163840
X 178
Mvav 148,148,216
K 198,176,177
Mvav 116,148,208
X 179
Mvav 84,148,200
buffer_load_dwordx4 a[84:87], v4, s[8:11], s19 offen sc1
Ra 136,242,2048
Mvav 52,148,192
X 180
Mvav 56,152,192
s_add_u32 s19, s19, 163840
Ra 140,242,3072
K 199,178,179
Mvav 88,152,200
X 181
Mvav 120,152,208
X 182
Mvav 152,152,216
buffer_load_dwordx4 a[88:91], v5, s[8:11], s19 offen sc1
K 206,180,181
Mvav 156,156,216
X 183
Mvav 124,156,208
Mvav 92,156,200
K 207,182,183
Mvav 60,156,192
W 0
Cvaa 160,128,0,224
Ra 144,242,4096
Ra 148,242,5120
Mvaa 160,132,4
s_add_u32 m0, s62, 0
Ra 152,242,6144
Cvaa 164,132,20,228
X 184
Mvaa 164,128,16
X 185
Cvaa 168,128,32,232
X 186
Mvaa 168,132,36
Ra 156,242,7168
K 214,184,185
Cvaa 172,132,52,236
Dc 1,8,61,0
Mvaa 172,128,48
X 187
Mvaa 160,136,8
X 188
Mvaa 160,140,12
Ra 128,242,16384
K 215,186,187
Mvaa 164,140,28
Dc 1,8,61,1024
Mvaa 164,136,24
X 189
Mvaa 168,136,40
X 190
Mvaa 168,140,44
Ra 132,242,17408
K 222,188,189
Mvaa 172,140,60
Dc 1,8,61,2048
Mvaa 172,136,56
X 191
v_smfmac_f32_16x16x64_bf16 a[160:163], a[176:179], v[192:199], v240
Ra 136,242,18432
K 223,190,191
v_smfmac_f32_16x16x64_bf16 a[164:167], a[176:179], v[200:207], v240
X 160
v_smfmac_f32_16x16x64_bf16 a[168:171], a[176:179], v[208:215], v240
Dc 1,8,61,3072
v_smfmac_f32_16x16x64_bf16 a[172:175], a[176:179], v[216:223], v240
W 3
X 161
Cvaa 176,144,0,224
s_add_u32 m0, s62, 4096
X 162
Mvaa 176,148,4
Ra 140,242,19456
K 192,160,161
Cvaa 180,148,20,228
X 163
Mvaa 180,144,16
Dc 243,8,61,0
Cvaa 184,144,32,232
)"
R"(X 164
Mvaa 184,148,36
K 193,162,163
Cvaa 188,148,52,236
X 165
Mvaa 188,144,48
Dc 243,8,61,1024
Ra 144,242,20480
Mvaa 176,152,8
X 166
Mvaa 176,156,12
Ra 148,242,21504
K 200,164,165
Mvaa 180,156,28
X 167
Mvaa 180,152,24
Dc 243,8,61,2048
Mvaa 184,152,40
X 168
Mvaa 184,156,44
K 201,166,167
Mvaa 188,156,60
X 169
Mvaa 188,152,56
W 2
Dc 243,8,61,3072
Ra 152,242,22528
Mvav 32,128,196
s_add_u32 m0, s62, 8192
X 170
Mvav 64,128,204
Ra 156,242,23552
K 208,168,169
Mvav 96,128,212
X 171
Mvav 128,128,220
Dc 244,8,61,0
Mvav 132,132,220
X 172
Mvav 100,132,212
Ra 128,242,8192
K 209,170,171
Mvav 68,132,204
X 173
Mvav 36,132,196
Dc 244,8,61,1024
Mvav 40,136,196
X 174
Mvav 72,136,204
Ra 132,242,9216
K 216,172,173
Mvav 104,136,212
X 175
Mvav 136,136,220
Dc 244,8,61,2048
Mvav 140,140,220
s_bfe_u32 s81, s59, 0x10001
X 176
Mvav 108,140,212
s_lshl_b32 s82, s57, 1
Ra 136,242,10240
K 217,174,175
Mvav 76,140,204
s_add_u32 s81, s81, s82
X 177
Mvav 44,140,196
W 3
s_lshl_b32 s83, s81, 2
Dc 244,8,61,3072
Mvav 48,144,196
s_lshr_b32 s81, s59, 2
X 178
Mvav 80,144,204
s_add_u32 m0, s62, 12288
s_lshl_b32 s81, s81, 1
Ra 140,242,11264
Mvav 112,144,212
s_add_u32 s83, s83, s81
K 194,176,177
Mvav 144,144,220
s_and_b32 s81, s59, 1
X 179
Mvav 148,148,220
s_add_u32 s83, s83, s81
X 180
Mvav 116,148,212
s_and_b32 s81, s14, 1
Dc 245,8,61,0
Ra 144,242,12288
Mvav 84,148,204
s_mul_i32 s81, s81, 124
K 195,178,179
Mvav 52,148,196
s_xor_b32 s83, s83, s81
X 181
Mvav 56,152,196
s_mul_i32 s83, s83, 20480
X 182
Mvav 88,152,204
s_mul_i32 s81, s58, 320
Dc 245,8,61,1024
Ra 148,242,13312
Mvav 120,152,212
s_add_u32 s82, s83, s81
K 202,180,181
Mvav 152,152,220
X 183
Mvav 156,156,220
Ra 152,242,14336
v_bfe_u32 v246, v1, 4, 4
Mvav 124,156,212
Dc 245,8,61,2048
v_mul_u32_u24 v246, 1280, v246
Mvav 92,156,204
X 184
Mvav 60,156,196
W 3
v_lshrrev_b32 v247, 8, v1
K 203,182,183
Cvaa 160,128,0,224
v_mul_u32_u24 v247, 104, v247
v_add3_u32 v246, v246, v247, s82
Mvaa 160,132,4
X 185
Cvaa 164,132,20,228
Dc 245,8,61,3072
Ra 156,242,15360
Mvaa 164,128,16
s_add_u32 s61, s61, s63
X 186
Cvaa 168,128,32,232
K 210,184,185
Mvaa 168,132,36
X 187
Cvaa 172,132,52,236
s_xor_b32 s62, s62, 0x10000
X 188
Mvaa 172,128,48
global_load_dword v3, v246, s[16:17]
K 211,186,187
Mvaa 160,136,8
s_lshl_b32 s81, s81, 3
s_add_u32 s83, s83, s81
s_add_u32 s83, s83, 131072
Mvaa 160,140,12
s_add_u32 s64, s64, 1
v_lshrrev_b32 v246, 9, v1
v_mul_u32_u24 v246, 10240, v246
Mvaa 164,140,28
Ra 128,242,24576
v_and_b32 v247, 0x1f0, v1
Mvaa 164,136,24
X 189
Mvaa 168,136,40
Ra 132,242,25600
v_min_u32 v247, 304, v247
Mvaa 168,140,44
X 190
Mvaa 172,140,60
v_lshl_add_u32 v246, v247, 3, v246
K 218,188,189
Mvaa 172,136,56
W 2
buffer_load_dword v3, v246, s[8:11], s83 offen sc1
Ra 136,242,26624
Cvaa 176,144,0,224
X 191
Mvaa 176,148,4
X 160
Cvaa 180,148,20,228
Ra 140,242,27648
K 219,190,191
Mvaa 180,144,16
X 161
Cvaa 184,144,32,232
X 162
Mvaa 184,148,36
K 196,160,161
Cvaa 188,148,52,236
X 163
Mvaa 188,144,48
X 164
Mvaa 176,152,8
Ra 144,242,28672
K 197,162,163
Mvaa 176,156,12
X 165
Mvaa 180,156,28
Ra 148,242,29696
Mvaa 180,152,24
X 166
Mvaa 184,152,40
K 204,164,165
Mvaa 184,156,44
X 167
Mvaa 188,156,60
X 168
Mvaa 188,152,56
W 2
Ra 152,242,30720
K 205,166,167
Mvav 32,128,192
X 169
Mvav 64,128,200
Ra 156,242,31744
Mvav 96,128,208
X 170
Mvav 128,128,216
K 212,168,169
Mvav 132,132,216
X 171
Mvav 100,132,208
X 172
Mvav 68,132,200
Mvav 36,132,192
K 213,170,171
Mvav 40,136,192
X 173
Mvav 72,136,200
X 174
Mvav 104,136,208
Mvav 136,136,216
K 220,172,173
Mvav 140,140,216
X 175
Mvav 108,140,208
X 176
Mvav 76,140,200
s_waitcnt vmcnt(2) lgkmcnt(0)
s_barrier
v_xor_b32 v242, 0x10000, v242
Mvav 44,140,192
Ra 128,242,0
K 221,174,175
Mvav 48,144,192
X 177
Mvav 80,144,200
X 178
Mvav 112,144,208
Ra 132,242,1024
Mvav 144,144,216
K 198,176,177
Mvav 148,148,216
X 179
Mvav 116,148,208
X 180
Mvav 84,148,200
Ra 136,242,2048
Mvav 52,148,192
K 199,178,179
Mvav 56,152,192
X 181
Mvav 88,152,200
Ra 140,242,3072
Mvav 120,152,208
X 182
Mvav 152,152,216
K 206,180,181
Mvav 156,156,216
X 183
Mvav 124,156,208
Mvav 92,156,200
K 207,182,183
Mvav 60,156,192
W 0
Cvaa 160,128,0,224
Ra 144,242,4096
Ra 148,242,5120
Mvaa 160,132,4
s_add_u32 m0, s62, 0
Ra 152,242,6144
G 8,64
Cvaa 164,132,20,228
X 184
Mvaa 164,128,16
X 185
Cvaa 168,128,32,232
X 186
Mvaa 168,132,36
G 9,65
K 214,184,185
Cvaa 172,132,52,236
Ra 156,242,7168
Dc 1,8,61,0
Mvaa 172,128,48
X 187
Mvaa 160,136,8
X 188
Mvaa 160,140,12
Ra 128,242,16384
G 10,66
Mvaa 164,140,28
Dc 1,8,61,1024
K 215,186,187
Mvaa 164,136,24
X 189
Mvaa 168,136,40
Ra 132,242,17408
G 11,67
Mvaa 168,140,44
X 190
Mvaa 172,140,60
Dc 1,8,61,2048
K 222,188,189
Mvaa 172,136,56
X 191
v_smfmac_f32_16x16x64_bf16 a[160:163], a[176:179], v[192:199], v240
G 12,68
K 223,190,191
v_smfmac_f32_16x16x64_bf16 a[164:167], a[176:179], v[200:207], v240
Ra 136,242,18432
v_smfmac_f32_16x16x64_bf16 a[168:171], a[176:179], v[208:215], v240
Dc 1,8,61,3072
G 13,69
v_smfmac_f32_16x16x64_bf16 a[172:175], a[176:179], v[216:223], v240
W 3
X 160
Cvaa 176,144,0,224
s_add_u32 m0, s62, 4096
X 161
Mvaa 176,148,4
X 162
Cvaa 180,148,20,228
Ra 140,242,19456
K 192,160,161
Mvaa 180,144,16
Dc 243,8,61,0
G 14,70
Cvaa 184,144,32,232
X 163
Mvaa 184,148,36
X 164
Cvaa 188,148,52,236
G 15,71
K 193,162,163
Mvaa 188,144,48
Dc 243,8,61,1024
Ra 144,242,20480
Mvaa 176,152,8
X 165
Mvaa 176,156,12
X 166
Mvaa 180,156,28
Ra 148,242,21504
A 8,8,12
Mvaa 180,152,24
Dc 243,8,61,2048
K 200,164,165
Mvaa 184,152,40
X 167
Mvaa 184,156,44
A 9,9,13
Mvaa 188,156,60
X 168
Mvaa 188,152,56
W 2
Dc 243,8,61,3072
K 201,166,167
Mvav 32,128,196
s_add_u32 m0, s62, 8192
X 169
Mvav 64,128,204
Ra 152,242,22528
A 10,10,14
Mvav 96,128,212
X 170
Mvav 128,128,220
Ra 156,242,23552
K 208,168,169
Mvav 132,132,220
Dc 244,8,61,0
A 11,11,15
Mvav 100,132,212
X 171
Mvav 68,132,204
X 172
Mvav 36,132,196
Ra 128,242,8192
K 209,170,171
Mvav 40,136,196
Dc 244,8,61,1024
G 12,72
Mvav 72,136,204
X 173
Mvav 104,136,212
X 174
Mvav 136,136,220
Ra 132,242,9216
G 13,73
Mvav 140,140,220
Dc 244,8,61,2048
K 216,172,173
Mvav 108,140,212
X 175
Mvav 76,140,204
X 176
Mvav 44,140,196
W 2
Ra 136,242,10240
G 14,74
)"
R"(Mvav 48,144,196
Dc 244,8,61,3072
K 217,174,175
Mvav 80,144,204
s_add_u32 m0, s62, 12288
X 177
Mvav 112,144,212
Ra 140,242,11264
G 15,75
Mvav 144,144,220
X 178
Mvav 148,148,220
K 194,176,177
A 8,8,12
Mvav 116,148,212
X 179
Mvav 84,148,204
X 180
Mvav 52,148,196
Dc 245,8,61,0
Ra 144,242,12288
Mvav 56,152,196
K 195,178,179
A 9,9,13
Mvav 88,152,204
X 181
Mvav 120,152,212
X 182
Mvav 152,152,220
Dc 245,8,61,1024
Ra 148,242,13312
Mvav 156,156,220
K 202,180,181
A 10,10,14
Mvav 124,156,212
X 183
Mvav 92,156,204
X 184
Mvav 60,156,196
W 2
Dc 245,8,61,2048
Ra 152,242,14336
Cvaa 160,128,0,224
A 11,11,15
K 203,182,183
Mvaa 160,132,4
X 185
Cvaa 164,132,20,228
X 186
Mvaa 164,128,16
Dc 245,8,61,3072
Ra 156,242,15360
Cvaa 168,128,32,232
s_add_u32 s61, s61, s63
G 12,76
K 210,184,185
Mvaa 168,132,36
X 187
Cvaa 172,132,52,236
s_xor_b32 s62, s62, 0x10000
G 13,77
Mvaa 172,128,48
X 188
Mvaa 160,136,8
K 211,186,187
G 14,78
Mvaa 160,140,12
s_add_u32 s64, s64, 1
X 189
Mvaa 164,140,28
Ra 128,242,24576
Ra 132,242,25600
Mvaa 164,136,24
X 190
Mvaa 168,136,40
K 218,188,189
G 15,79
Mvaa 168,140,44
X 191
Mvaa 172,140,60
s_lshl_b32 s92, s58, 2
X 160
Mvaa 172,136,56
W 2
K 219,190,191
A 8,8,12
Cvaa 176,144,0,224
X 161
Mvaa 176,148,4
Ra 136,242,26624
Ra 140,242,27648
Cvaa 180,148,20,228
s_add_u32 s91, s92, s51
X 162
Mvaa 180,144,16
A 9,9,13
K 196,160,161
Cvaa 184,144,32,232
X 163
Mvaa 184,148,36
s_add_u32 s95, s92, 0
X 164
Cvaa 188,148,52,236
A 10,10,14
K 197,162,163
Mvaa 188,144,48
X 165
Mvaa 176,152,8
s_and_b32 s95, s95, 15
Ra 144,242,28672
A 11,11,15
Mvaa 176,156,12
Ra 148,242,29696
Mvaa 180,156,28
X 166
Mvaa 180,152,24
s_mul_i32 s95, s95, 40960
K 204,164,165
Mvaa 184,152,40
X 167
Mvaa 184,156,44
G 12,80
Mvaa 188,156,60
s_mul_i32 s71, s51, 0
X 168
Mvaa 188,152,56
W 2
K 205,166,167
G 13,81
Mvav 32,128,192
X 169
Mvav 64,128,200
s_add_u32 s71, s71, 133120
Ra 152,242,30720
Ra 156,242,31744
Mvav 96,128,208
X 170
Mvav 128,128,216
G 14,82
K 212,168,169
Mvav 132,132,216
s_add_u32 m0, s71, s48
X 171
Mvav 100,132,208
X 172
Mvav 68,132,200
G 15,83
Mvav 36,132,192
s_lshl2_add_u32 s94, s48, s95
K 213,170,171
Mvav 40,136,192
X 173
Mvav 72,136,200
X 174
Mvav 104,136,208
D 16,40,94,0
A 8,8,12
Mvav 136,136,216
K 220,172,173
Mvav 140,140,216
X 175
Mvav 108,140,208
A 9,9,13
Mvav 76,140,200
s_waitcnt vmcnt(1) lgkmcnt(0)
s_barrier
v_xor_b32 v242, 0x10000, v242
Mvav 44,140,192
D 17,40,94,1024
Ra 128,242,0
Mvav 48,144,192
X 176
Mvav 80,144,200
K 221,174,175
A 10,10,14
Mvav 112,144,208
X 177
Mvav 144,144,216
D 18,40,94,2048
Ra 132,242,1024
Mvav 148,148,216
X 178
Mvav 116,148,208
K 198,176,177
A 11,11,15
Mvav 84,148,200
X 179
Mvav 52,148,192
D 19,40,94,3072
Ra 136,242,2048
Mvav 56,152,192
s_add_u32 m0, m0, 4096
X 180
Mvav 88,152,200
G 12,84
K 199,178,179
Mvav 120,152,208
X 181
Mvav 152,152,216
Ra 140,242,3072
buffer_load_dwordx4 v20, s[40:43], s94 offen lds
Mvav 156,156,216
X 182
Mvav 124,156,208
K 206,180,181
Mvav 92,156,200
X 183
Mvav 60,156,192
K 207,182,183
W 0
Cvaa 160,128,0,224
Ra 144,242,4096
Ra 148,242,5120
Mvaa 160,132,4
s_add_u32 m0, s62, 0
Ra 152,242,6144
G 13,85
Cvaa 164,132,20,228
X 184
Mvaa 164,128,16
X 185
Cvaa 168,128,32,232
X 186
Mvaa 168,132,36
G 14,86
K 214,184,185
Cvaa 172,132,52,236
Ra 156,242,7168
Dc 1,8,61,0
Mvaa 172,128,48
X 187
Mvaa 160,136,8
X 188
Mvaa 160,140,12
Ra 128,242,16384
G 15,87
Mvaa 164,140,28
Dc 1,8,61,1024
K 215,186,187
Mvaa 164,136,24
X 189
Mvaa 168,136,40
Ra 132,242,17408
A 8,8,12
Mvaa 168,140,44
X 190
Mvaa 172,140,60
Dc 1,8,61,2048
K 222,188,189
Mvaa 172,136,56
X 191
v_smfmac_f32_16x16x64_bf16 a[160:163], a[176:179], v[192:199], v240
A 9,9,13
K 223,190,191
v_smfmac_f32_16x16x64_bf16 a[164:167], a[176:179], v[200:207], v240
Ra 136,242,18432
A 10,10,14
v_smfmac_f32_16x16x64_bf16 a[168:171], a[176:179], v[208:215], v240
Dc 1,8,61,3072
v_smfmac_f32_16x16x64_bf16 a[172:175], a[176:179], v[216:223], v240
W 3
X 160
Cvaa 176,144,0,224
s_add_u32 m0, s62, 4096
X 161
Mvaa 176,148,4
X 162
Cvaa 180,148,20,228
Ra 140,242,19456
A 11,11,15
Mvaa 180,144,16
Dc 243,8,61,0
K 192,160,161
Cvaa 184,144,32,232
X 163
Mvaa 184,148,36
X 164
Cvaa 188,148,52,236
G 12,88
K 193,162,163
Mvaa 188,144,48
Dc 243,8,61,1024
G 13,89
Mvaa 176,152,8
X 165
Mvaa 176,156,12
X 166
Mvaa 180,156,28
Ra 144,242,20480
Ra 148,242,21504
Mvaa 180,152,24
Dc 243,8,61,2048
K 200,164,165
Mvaa 184,152,40
X 167
Mvaa 184,156,44
G 14,90
Mvaa 188,156,60
X 168
Mvaa 188,152,56
W 2
Dc 243,8,61,3072
K 201,166,167
Mvav 32,128,196
s_add_u32 m0, s62, 8192
Ra 152,242,22528
G 15,91
Mvav 64,128,204
X 169
Mvav 96,128,212
X 170
Mvav 128,128,220
Ra 156,242,23552
A 8,8,12
Mvav 132,132,220
Dc 244,8,61,0
K 208,168,169
Mvav 100,132,212
X 171
Mvav 68,132,204
X 172
Mvav 36,132,196
Ra 128,242,8192
A 9,9,13
Mvav 40,136,196
Dc 244,8,61,1024
K 209,170,171
Mvav 72,136,204
X 173
Mvav 104,136,212
Ra 132,242,9216
A 10,10,14
Mvav 136,136,220
X 174
Mvav 140,140,220
Dc 244,8,61,2048
K 216,172,173
Mvav 108,140,212
X 175
Mvav 76,140,204
Ra 136,242,10240
A 11,11,15
Mvav 44,140,196
W 3
X 176
Mvav 48,144,196
Dc 244,8,61,3072
K 217,174,175
Mvav 80,144,204
s_add_u32 m0, s62, 12288
Ra 140,242,11264
v_add_f32_dpp v8, v8, v8 row_ror:8 row_mask:0xf bank_mask:0xf
Mvav 112,144,212
X 177
Mvav 144,144,220
X 178
Mvav 148,148,220
K 194,176,177
v_add_f32_dpp v9, v9, v9 row_ror:8 row_mask:0xf bank_mask:0xf
Mvav 116,148,212
X 179
Mvav 84,148,204
X 180
Mvav 52,148,196
Dc 245,8,61,0
Ra 144,242,12288
Mvav 56,152,196
v_add_f32_dpp v10, v10, v10 row_ror:8 row_mask:0xf bank_mask:0xf
K 195,178,179
Mvav 88,152,204
X 181
Mvav 120,152,212
X 182
Mvav 152,152,220
Dc 245,8,61,1024
v_add_f32_dpp v11, v11, v11 row_ror:8 row_mask:0xf bank_mask:0xf
Mvav 156,156,220
Ra 148,242,13312
K 202,180,181
Mvav 124,156,212
X 183
Mvav 92,156,204
Ra 152,242,14336
v_mov_b32 v12, v8
Mvav 60,156,196
W 3
Dc 245,8,61,2048
v_mov_b32 v13, v9
Cvaa 160,128,0,224
X 184
Mvaa 160,132,4
Ra 156,242,15360
K 203,182,183
Cvaa 164,132,20,228
X 185
Mvaa 164,128,16
Dc 245,8,61,3072
v_mov_b32 v14, v10
Cvaa 168,128,32,232
s_add_u32 s61, s61, s63
)"
R"(X 186
Mvaa 168,132,36
K 210,184,185
v_mov_b32 v15, v11
Cvaa 172,132,52,236
s_xor_b32 s62, s62, 0x10000
X 187
Mvaa 172,128,48
X 188
Mvaa 160,136,8
Ra 128,242,24576
K 211,186,187
Mvaa 160,140,12
s_add_u32 s64, s64, 1
X 189
Mvaa 164,140,28
X 190
Mvaa 164,136,24
Ra 132,242,25600
v_permlane16_swap_b32 v8, v12
Mvaa 168,136,40
K 218,188,189
v_permlane16_swap_b32 v9, v13
Mvaa 168,140,44
X 191
Mvaa 172,140,60
s_lshl_b32 s92, s58, 2
X 160
Mvaa 172,136,56
W 2
K 219,190,191
v_permlane16_swap_b32 v10, v14
Cvaa 176,144,0,224
X 161
Mvaa 176,148,4
Ra 136,242,26624
Ra 140,242,27648
Cvaa 180,148,20,228
s_add_u32 s91, s92, s51
X 162
Mvaa 180,144,16
K 196,160,161
v_permlane16_swap_b32 v11, v15
Cvaa 184,144,32,232
X 163
Mvaa 184,148,36
s_add_u32 s95, s92, 1
X 164
Cvaa 188,148,52,236
A 8,8,12
K 197,162,163
Mvaa 188,144,48
X 165
Mvaa 176,152,8
s_and_b32 s95, s95, 15
Ra 144,242,28672
Ra 148,242,29696
Mvaa 176,156,12
X 166
Mvaa 180,156,28
A 9,9,13
K 204,164,165
Mvaa 180,152,24
s_mul_i32 s95, s95, 40960
X 167
Mvaa 184,152,40
Mvaa 184,156,44
A 10,10,14
Mvaa 188,156,60
s_mul_i32 s71, s51, 0
X 168
Mvaa 188,152,56
W 2
Ra 152,242,30720
K 205,166,167
Mvav 32,128,192
X 169
Mvav 64,128,200
s_add_u32 s71, s71, 143360
Ra 156,242,31744
A 11,11,15
Mvav 96,128,208
X 170
Mvav 128,128,216
K 212,168,169
v_mov_b32 v12, v8
Mvav 132,132,216
s_add_u32 m0, s71, s48
X 171
Mvav 100,132,208
X 172
Mvav 68,132,200
Mvav 36,132,192
s_lshl2_add_u32 s94, s48, s95
v_mov_b32 v13, v9
K 213,170,171
Mvav 40,136,192
X 173
Mvav 72,136,200
X 174
Mvav 104,136,208
D 16,40,94,0
v_mov_b32 v14, v10
Mvav 136,136,216
K 220,172,173
Mvav 140,140,216
X 175
Mvav 108,140,208
v_mov_b32 v15, v11
Mvav 76,140,200
s_waitcnt vmcnt(1) lgkmcnt(0)
s_barrier
v_xor_b32 v242, 0x10000, v242
Mvav 44,140,192
D 17,40,94,1024
Ra 128,242,0
Mvav 48,144,192
X 176
Mvav 80,144,200
K 221,174,175
v_permlane32_swap_b32 v8, v12
Mvav 112,144,208
X 177
Mvav 144,144,216
D 18,40,94,2048
Ra 132,242,1024
Mvav 148,148,216
X 178
Mvav 116,148,208
K 198,176,177
v_permlane32_swap_b32 v9, v13
Mvav 84,148,200
X 179
Mvav 52,148,192
D 19,40,94,3072
Ra 136,242,2048
Mvav 56,152,192
s_add_u32 m0, m0, 4096
X 180
Mvav 88,152,200
K 199,178,179
v_permlane32_swap_b32 v10, v14
Mvav 120,152,208
X 181
Mvav 152,152,216
Ra 140,242,3072
buffer_load_dwordx4 v20, s[40:43], s94 offen lds
Mvav 156,156,216
X 182
Mvav 124,156,208
K 206,180,181
Mvav 92,156,200
X 183
Mvav 60,156,192
K 207,182,183
W 0
Cvaa 160,128,0,224
Ra 144,242,4096
Ra 148,242,5120
Mvaa 160,132,4
s_add_u32 m0, s62, 0
Ra 152,242,6144
v_permlane32_swap_b32 v11, v15
Cvaa 164,132,20,228
X 184
Mvaa 164,128,16
X 185
Cvaa 168,128,32,232
X 186
Mvaa 168,132,36
A 8,8,12
K 214,184,185
Cvaa 172,132,52,236
Ra 156,242,7168
Dc 1,8,61,0
Mvaa 172,128,48
X 187
Mvaa 160,136,8
Ra 128,242,16384
A 9,9,13
Mvaa 160,140,12
X 188
Mvaa 164,140,28
Dc 1,8,61,1024
K 215,186,187
Mvaa 164,136,24
X 189
Mvaa 168,136,40
Ra 132,242,17408
A 10,10,14
Mvaa 168,140,44
X 190
Mvaa 172,140,60
Dc 1,8,61,2048
K 222,188,189
Mvaa 172,136,56
A 11,11,15
v_smfmac_f32_16x16x64_bf16 a[160:163], a[176:179], v[192:199], v240
X 191
v_smfmac_f32_16x16x64_bf16 a[164:167], a[176:179], v[200:207], v240
K 223,190,191
v_and_b32 v7, 0xffff0000, v7
v_smfmac_f32_16x16x64_bf16 a[168:171], a[176:179], v[208:215], v240
Dc 1,8,61,3072
Ra 136,242,18432
v_smfmac_f32_16x16x64_bf16 a[172:175], a[176:179], v[216:223], v240
W 3
X 160
Cvaa 176,144,0,224
s_add_u32 m0, s62, 4096
X 161
Mvaa 176,148,4
X 162
Cvaa 180,148,20,228
Ra 140,242,19456
v_ashrrev_i32 v6, 31, v7
Mvaa 180,144,16
Dc 243,8,61,0
K 192,160,161
Cvaa 184,144,32,232
X 163
Mvaa 184,148,36
v_not_b32 v6, v6
Cvaa 188,148,52,236
X 164
Mvaa 188,144,48
Dc 243,8,61,1024
K 193,162,163
Mvaa 176,152,8
X 165
Mvaa 176,156,12
Ra 144,242,20480
v_or_b32 v6, 0x80000000, v6
Mvaa 180,156,28
X 166
Mvaa 180,152,24
Ra 148,242,21504
v_xor_b32 v7, v7, v6
Mvaa 184,152,40
Dc 243,8,61,2048
K 200,164,165
Mvaa 184,156,44
X 167
Mvaa 188,156,60
X 168
Mvaa 188,152,56
W 2
v_and_b32 v7, 0xffff0000, v7
K 201,166,167
Mvav 32,128,196
Dc 243,8,61,3072
v_mul_f32 v7, 0xbfb8aa3b, v7
Mvav 64,128,204
s_add_u32 m0, s62, 8192
X 169
Mvav 96,128,212
Ra 152,242,22528
Ra 156,242,23552
Mvav 128,128,220
X 170
Mvav 132,132,220
Dc 244,8,61,0
K 208,168,169
Mvav 100,132,212
X 171
Mvav 68,132,204
X 7
Mvav 36,132,196
X 172
Mvav 40,136,196
Ra 128,242,8192
K 209,170,171
Mvav 72,136,204
X 173
Mvav 104,136,212
Dc 244,8,61,1024
Ra 132,242,9216
Mvav 136,136,220
X 174
Mvav 140,140,220
v_add_f32 v7, 1.0, v7
K 216,172,173
Mvav 108,140,212
X 175
Mvav 76,140,204
Dc 244,8,61,2048
Ra 136,242,10240
Mvav 44,140,196
W 3
v_rcp_f32 v7, v7
Mvav 48,144,196
X 176
Mvav 80,144,204
Ra 140,242,11264
K 217,174,175
Mvav 112,144,212
Dc 244,8,61,3072
K 7,7,7
Mvav 144,144,220
s_add_u32 m0, s62, 12288
X 177
Mvav 148,148,220
X 178
Mvav 116,148,212
Ra 144,242,12288
K 194,176,177
Mvav 84,148,204
X 179
Mvav 52,148,196
X 180
Mvav 56,152,196
Dc 245,8,61,0
v_lshlrev_b32 v7, 16, v7
Mvav 88,152,204
K 195,178,179
K 12,8,9
Mvav 120,152,212
X 181
Mvav 152,152,220
X 182
Mvav 156,156,220
Dc 245,8,61,1024
Ra 148,242,13312
Mvav 124,156,212
K 202,180,181
K 13,10,11
Mvav 92,156,204
X 183
Mvav 60,156,196
W 2
X 184
Cvaa 160,128,0,224
Dc 245,8,61,2048
Ra 152,242,14336
Mvaa 160,132,4
K 203,182,183
v_lshlrev_b32 v8, 16, v12
Cvaa 164,132,20,228
X 185
Mvaa 164,128,16
X 186
Cvaa 168,128,32,232
Dc 245,8,61,3072
Ra 156,242,15360
Mvaa 168,132,36
s_add_u32 s61, s61, s63
v_and_b32 v9, 0xffff0000, v12
K 210,184,185
Cvaa 172,132,52,236
s_xor_b32 s62, s62, 0x10000
X 187
Mvaa 172,128,48
X 188
Mvaa 160,136,8
v_lshlrev_b32 v10, 16, v13
K 211,186,187
Mvaa 160,140,12
s_add_u32 s64, s64, 1
X 189
Mvaa 164,140,28
Ra 128,242,24576
v_and_b32 v11, 0xffff0000, v13
Mvaa 164,136,24
X 190
Mvaa 168,136,40
Ra 132,242,25600
K 218,188,189
Mvaa 168,140,44
X 191
Mvaa 172,140,60
s_lshl_b32 s92, s58, 2
U 8,8,7
U 9,9,7
Mvaa 172,136,56
W 2
X 160
Cvaa 176,144,0,224
Ra 136,242,26624
K 219,190,191
Mvaa 176,148,4
X 161
Cvaa 180,148,20,228
s_add_u32 s91, s92, s51
Ra 140,242,27648
U 10,10,7
Mvaa 180,144,16
X 162
Cvaa 184,144,32,232
K 196,160,161
Mvaa 184,148,36
s_add_u32 s95, s92, 2
X 163
Cvaa 188,148,52,236
U 11,11,7
Mvaa 188,144,48
X 164
Mvaa 176,152,8
s_and_b32 s95, s95, 15
Ra 144,242,28672
K 197,162,163
)"
R"(Mvaa 176,156,12
X 165
Mvaa 180,156,28
Ra 148,242,29696
K 12,8,9
Mvaa 180,152,24
s_mul_i32 s95, s95, 40960
X 166
Mvaa 184,152,40
K 204,164,165
K 14,10,11
Mvaa 184,156,44
X 167
Mvaa 188,156,60
s_mul_i32 s71, s51, 0
X 168
Mvaa 188,152,56
W 2
v_mov_b32 v13, s13
K 205,166,167
Mvav 32,128,192
X 169
Mvav 64,128,200
s_add_u32 s71, s71, 153600
Ra 152,242,30720
Ra 156,242,31744
Mvav 96,128,208
X 170
Mvav 128,128,216
v_mov_b32 v15, s13
K 212,168,169
Mvav 132,132,216
s_add_u32 m0, s71, s48
X 171
Mvav 100,132,208
v_lshrrev_b32 v6, 7, v1
Mvav 68,132,200
X 172
Mvav 36,132,192
s_lshl2_add_u32 s94, s48, s95
K 213,170,171
Mvav 40,136,192
X 173
Mvav 72,136,200
v_lshl_or_b32 v6, v6, 28, v1
Mvav 104,136,208
X 174
Mvav 136,136,216
s_lshl_b32 s19, s85, 7
D 16,40,94,0
K 220,172,173
Mvav 140,140,216
X 175
Mvav 108,140,208
X 176
Mvav 76,140,200
s_waitcnt vmcnt(1) lgkmcnt(0)
s_barrier
v_xor_b32 v242, 0x10000, v242
Mvav 44,140,192
s_add_u32 s19, s19, 98304
D 17,40,94,1024
K 221,174,175
Mvav 48,144,192
Ra 128,242,0
Mvav 80,144,200
s_and_b32 s21, s20, 134217728
X 177
Mvav 112,144,208
X 178
Mvav 144,144,216
D 18,40,94,2048
Ra 132,242,1024
Mvav 148,148,216
s_or_b32 s19, s19, s21
K 198,176,177
Mvav 116,148,208
X 179
Mvav 84,148,200
X 180
Mvav 52,148,192
D 19,40,94,3072
Ra 136,242,2048
Mvav 56,152,192
s_add_u32 m0, m0, 4096
Ra 140,242,3072
K 199,178,179
Mvav 88,152,200
X 181
Mvav 120,152,208
X 182
Mvav 152,152,216
buffer_store_dwordx4 v[12:15], v6, s[8:11], s19 offen sc1
K 206,180,181
Mvav 156,156,216
X 183
Mvav 124,156,208
Mvav 92,156,200
K 207,182,183
Mvav 60,156,192
buffer_load_dwordx4 v20, s[40:43], s94 offen lds
.Lf1_40%=:
W 0
Cvaa 160,128,0,224
v_bfe_u32 v28, v1, 4, 2
v_lshlrev_b32 v28, 3, v28
Mvaa 160,132,4
Ra 144,242,4096
Ra 148,242,5120
Cvaa 164,132,20,228
Ra 152,242,6144
buffer_load_dwordx2 v[28:29], v28, s[8:11], s54 offen sc1
Mvaa 164,128,16
s_lshl_b32 s92, s58, 2
s_add_u32 s91, s92, s51
s_add_u32 s95, s91, 3
Cvaa 168,128,32,232
s_and_b32 s95, s95, 15
s_mul_i32 s95, s95, 40960
s_mul_i32 s71, s51, 10240
Mvaa 168,132,36
s_add_u32 s71, s71, 0
s_add_u32 m0, s71, s48
s_lshl2_add_u32 s94, s48, s95
Cvaa 172,132,52,236
X 184
Mvaa 172,128,48
X 185
Mvaa 160,136,8
X 186
Mvaa 160,140,12
Ra 156,242,7168
K 214,184,185
Mvaa 164,140,28
X 187
Mvaa 164,136,24
D 16,40,94,0
Ra 128,242,16384
Mvaa 168,136,40
s_add_u32 s95, s91, 5
s_and_b32 s95, s95, 15
s_mul_i32 s95, s95, 40960
Mvaa 168,140,44
s_mul_i32 s71, s51, 10240
s_add_u32 s71, s71, 20480
Ra 132,242,17408
Mvaa 172,140,60
X 188
Mvaa 172,136,56
D 17,40,94,1024
K 215,186,187
v_smfmac_f32_16x16x64_bf16 a[160:163], a[176:179], v[192:199], v240
X 189
v_smfmac_f32_16x16x64_bf16 a[164:167], a[176:179], v[200:207], v240
X 190
K 222,188,189
X 191
s_nop 0
K 223,190,191
v_smfmac_f32_16x16x64_bf16 a[168:171], a[176:179], v[208:215], v240
D 18,40,94,2048
Ra 136,242,18432
v_smfmac_f32_16x16x64_bf16 a[172:175], a[176:179], v[216:223], v240
W 3
X 160
Cvaa 176,144,0,224
X 161
Mvaa 176,148,4
X 162
Cvaa 180,148,20,228
D 19,40,94,3072
K 192,160,161
Mvaa 180,144,16
s_add_u32 m0, m0, 4096
X 163
Cvaa 184,144,32,232
X 164
Mvaa 184,148,36
Ra 140,242,19456
K 193,162,163
Cvaa 188,148,52,236
buffer_load_dwordx4 v20, s[40:43], s94 offen lds
Mvaa 188,144,48
s_add_u32 m0, s71, s48
s_lshl2_add_u32 s94, s48, s95
Ra 144,242,20480
Mvaa 176,152,8
X 165
Mvaa 176,156,12
X 166
Mvaa 180,156,28
D 16,40,94,0
K 200,164,165
Mvaa 180,152,24
X 167
Mvaa 184,152,40
X 172
Mvaa 184,156,44
D 17,40,94,1024
K 201,166,167
Mvaa 188,156,60
X 173
Mvaa 188,152,56
s_cmp_eq_u32 s60, 16
s_cbranch_scc1 .Lmk_79%=
.Lmkr_80%=:
W 1
Mvav 32,128,196
Ra 148,242,21504
D 18,40,94,2048
Mvav 64,128,204
X 174
Mvav 96,128,212
Ra 152,242,22528
K 216,172,173
Mvav 128,128,220
X 175
Mvav 132,132,220
s_mov_b32 s18, 40960
X 168
Mvav 100,132,212
D 19,40,94,3072
K 217,174,175
Mvav 68,132,204
s_add_u32 m0, m0, 4096
X 169
Mvav 36,132,196
s_mov_b32 s19, 73728
X 170
Mvav 40,136,196
s_bitcmp1_b32 s14, 0
s_cselect_b32 s18, 49152, s18
s_cselect_b32 s19, 98304, s19
Ra 156,242,23552
K 208,168,169
Mvav 72,136,204
s_cmp_lt_u32 s14, 2
s_cselect_b32 s20, s18, s19
s_cselect_b32 s21, s19, s18
X 171
Mvav 104,136,212
X 184
Mvav 136,136,220
buffer_load_dwordx4 v20, s[40:43], s94 offen lds
K 209,170,171
Mvav 140,140,220
s_waitcnt vmcnt(10)
v_cmp_eq_u64 vcc, s[12:13], v[28:29]
s_cmp_eq_u64 vcc, -1
s_cselect_b32 s46, 79691776, 0
s_add_u32 s81, s58, 0
s_and_b32 s81, s81, 3
Mvav 108,140,212
s_lshl_b32 s81, s81, 12
s_add_u32 s81, s81, s49
Ra 128,242,24576
Mvav 76,140,204
X 185
Mvav 44,140,196
W 1
X 186
Mvav 48,144,196
buffer_load_dwordx4 a[184:187], v1, s[44:47], s81 offen offset:0 sc1
K 210,184,185
Mvav 80,144,204
s_add_u32 s82, s81, 131072
X 187
Mvav 112,144,212
v_add_u32 v2, s20, v1
v_add_u32 v3, s21, v1
Mvav 144,144,220
s_lshl_b32 s18, s14, 9
X 188
Mvav 148,148,220
Ra 132,242,25600
K 211,186,187
Mvav 116,148,212
buffer_load_dwordx4 a[200:203], v1, s[44:47], s82 offen offset:0 sc1
Ra 136,242,26624
Mvav 84,148,204
X 189
Mvav 52,148,196
v_lshrrev_b32 v232, 1, v1
v_add_u32 v232, s18, v232
Mvav 56,152,196
X 190
Mvav 88,152,204
K 218,188,189
v_bfe_u32 v12, v1, 6, 2
Mvav 120,152,212
buffer_load_dwordx4 a[188:191], v1, s[44:47], s81 offen offset:1024 sc1
Ra 140,242,27648
Mvav 152,152,220
X 191
Mvav 156,156,220
v_lshrrev_b32 v13, 8, v1
v_bfe_u32 v14, v1, 4, 1
Mvav 124,156,212
X 176
Mvav 92,156,204
K 219,190,191
v_lshlrev_b32 v14, 4, v14
Mvav 60,156,196
X 177
Maav 168,180,208
X 178
K 194,176,177
X 179
X 180
K 195,178,179
Maav 172,180,216
X 181
X 182
K 202,180,181
X 183
s_nop 0
K 203,182,183
Maav 160,180,192
buffer_load_dwordx4 a[204:207], v1, s[44:47], s82 offen offset:1024 sc1
v_mov_b32 v15, 0x3f80
Maav 164,180,200
W 0
Ra 144,242,28672
v_lshlrev_b32 v15, v14, v15
Mvav 96,128,208
Ra 148,242,29696
v_bfe_u32 v14, v1, 5, 1
v_xor_b32 v27, v12, v13
Mvav 128,128,216
buffer_load_dwordx4 a[192:195], v1, s[44:47], s81 offen offset:2048 sc1
v_lshl_or_b32 v27, v27, 1, v14
v_cmp_eq_u32_e64 s[86:87], 0, v27
Mvav 132,132,216
Ra 152,242,30720
Ra 156,242,31744
v_mov_b32 v226, 0
Mvav 100,132,208
v_cndmask_b32_e64 v224, 0, v15, s[86:87]
v_mov_b32 v228, 0
v_mov_b32 v230, v224
Mvav 104,136,208
buffer_load_dwordx4 a[208:211], v1, s[44:47], s82 offen offset:2048 sc1
v_cmp_eq_u32_e64 s[86:87], 1, v27
v_mov_b32 v227, 0
Mvav 136,136,216
v_cndmask_b32_e64 v225, 0, v15, s[86:87]
v_mov_b32 v229, 0
v_mov_b32 v231, v225
Mvav 140,140,216
G 4,160
G 8,161
G 5,164
Mvav 108,140,208
W 0
buffer_load_dwordx4 a[196:199], v1, s[44:47], s81 offen offset:3072 sc1
G 9,165
G 6,168
Mvav 112,144,208
G 10,169
G 7,172
G 11,173
Mvav 144,144,216
A 4,4,8
A 5,5,9
A 6,6,10
Mvav 148,148,216
buffer_load_dwordx4 a[212:215], v1, s[44:47], s82 offen offset:3072 sc1
s_add_u32 s81, s58, 1
s_and_b32 s81, s81, 3
s_lshl_b32 s81, s81, 12
Mvav 116,148,208
s_add_u32 s81, s81, s49
A 7,7,11
ds_write_b64 v232, v[6:7] offset:57344
A 8,4,5
Mvav 120,152,208
A 9,6,7
A 8,8,9
v_cmp_ngt_f32_e64 s[86:87], s90, v8
s_lshl_b32 s19, s14, 2
Mvav 152,152,216
buffer_load_dwordx4 a[216:219], v1, s[44:47], s81 offen offset:0 sc1
s_add_u32 s82, s81, 131072
v_mov_b32 v9, s19
s_or_b64 s[88:89], s[88:89], s[86:87]
Mvav 156,156,216
s_and_b64 s[88:89], s[88:89], s[24:25]
s_cselect_b32 s18, 1, 0
v_mov_b32 v8, s18
ds_write_b32 v9, v8 offset:59392
v_xor_b32 v10, 1024, v232
Mvav 124,156,208
v_bfe_u32 v11, v1, 2, 4
K 96,96,97
K 97,98,99
Mvav 32,128,192
buffer_load_dwordx4 a[232:235], v1, s[44:47], s82 offen offset:0 sc1
K 98,100,101
K 99,102,103
Mvav 64,128,200
)"
R"(ds_write_b128 v2, v[96:99] offset:0
K 128,128,129
K 129,130,131
Mvav 68,132,200
K 130,132,133
K 131,134,135
ds_write_b128 v2, v[128:131] offset:4096
Mvav 36,132,192
buffer_load_dwordx4 a[220:223], v1, s[44:47], s81 offen offset:1024 sc1
K 104,104,105
K 105,106,107
Mvav 40,136,192
K 106,108,109
K 107,110,111
ds_write_b128 v2, v[104:107] offset:1024
Mvav 72,136,200
K 136,136,137
K 137,138,139
K 138,140,141
Mvav 76,140,200
buffer_load_dwordx4 a[236:239], v1, s[44:47], s82 offen offset:1024 sc1
K 139,142,143
ds_write_b128 v2, v[136:139] offset:5120
Mvav 44,140,192
K 112,112,113
K 113,114,115
K 114,116,117
Mvav 48,144,192
K 115,118,119
ds_write_b128 v2, v[112:115] offset:2048
K 144,144,145
Mvav 80,144,200
K 145,146,147
K 146,148,149
K 147,150,151
Mvav 84,148,200
ds_write_b128 v2, v[144:147] offset:6144
buffer_load_dwordx4 a[224:227], v1, s[44:47], s81 offen offset:2048 sc1
K 120,120,121
Mvav 52,148,192
K 121,122,123
K 122,124,125
K 123,126,127
Mvav 56,152,192
ds_write_b128 v2, v[120:123] offset:3072
K 152,152,153
K 153,154,155
Mvav 88,152,200
buffer_load_dwordx4 a[240:243], v1, s[44:47], s82 offen offset:2048 sc1
K 154,156,157
K 155,158,159
Mvav 92,156,200
ds_write_b128 v2, v[152:155] offset:7168
Mvav 60,156,192
buffer_load_dwordx4 a[228:231], v1, s[44:47], s81 offen offset:3072 sc1
buffer_load_dwordx4 a[244:247], v1, s[44:47], s82 offen offset:3072 sc1
s_branch .LL_81%=
.p2align 12
.LL_81%=:
.Lend_43%=:
s_waitcnt vmcnt(8) lgkmcnt(0)
s_barrier
s_cmp_eq_u32 s46, 0
s_cbranch_scc1 .Llate_82%=
ds_read_b32 v11, v11 offset:59392
ds_read_b64 v[8:9], v10 offset:57344
Rv 176,3,0
Rv 180,3,1024
Rv 184,3,2048
Rv 188,3,3072
Rv 192,3,4096
Rv 196,3,5120
Rv 200,3,6144
Rv 204,3,7168
W 6
Mvvv 32,224,176
s_lshl_b32 s92, s58, 2
s_add_u32 s91, s92, s51
s_add_u32 s95, s91, 7
Mvvv 36,228,176
s_and_b32 s95, s95, 15
s_mul_i32 s95, s95, 40960
s_mul_i32 s71, s51, 20480
Mvvv 40,224,180
s_add_u32 s71, s71, 61440
s_add_u32 m0, s71, s48
s_lshl2_add_u32 s94, s48, s95
Mvvv 44,228,180
W 4
v_cmp_ne_u32 vcc, 0, v11
s_cmp_lg_u64 vcc, 0
s_cbranch_scc1 .Lredo%=
A 4,4,8
Mvvv 48,224,184
v_rcp_f32 v4, v4
Mvvv 52,228,184
Ta 128,25,10240
A 5,5,9
Mvvv 56,224,188
Ta 130,25,15360
D 16,40,94,0
Mvvv 60,228,188
W 4
s_add_u32 s95, s91, 9
s_and_b32 s95, s95, 15
Mvvv 64,224,192
s_mul_i32 s95, s95, 40960
s_mul_i32 s71, s51, 10240
s_add_u32 s71, s71, 112640
Mvvv 68,228,192
v_rcp_f32 v5, v5
Mvvv 72,224,196
s_lshl_b32 s93, s58, 12
s_add_u32 s93, s93, s50
Ta 132,25,20480
Mvvv 76,228,196
Ta 134,25,25600
W 4
Mvvv 80,224,200
U 32,32,4
U 33,33,4
Mvvv 84,228,200
Ta 136,26,10272
U 34,34,4
Mvvv 88,224,204
Ta 138,26,15392
U 35,35,4
Mvvv 92,228,204
Ta 140,26,20512
Ta 142,26,25632
W 4
Zvaa 96,128,184
U 36,36,4
U 37,37,4
Mvaa 96,132,188
D 17,40,94,1024
U 38,38,4
Zvaa 104,132,204
U 39,39,4
K 32,32,33
Mvaa 104,128,200
Ta 144,25,10304
Ta 146,25,15424
Ta 148,25,20544
Ta 150,25,25664
W 4
Zvaa 108,136,200
K 33,34,35
K 34,36,37
Mvaa 108,140,204
Ta 152,26,10336
K 35,38,39
Zvaa 100,140,188
buffer_store_dwordx4 v[32:35], v1, s[8:11], s93 offen offset:0 sc1
U 40,40,4
Mvaa 100,136,184
Ta 154,26,15456
Ta 156,26,20576
Ta 158,26,25696
W 4
Zvaa 112,144,184
U 41,41,4
U 42,42,4
Mvaa 112,148,188
U 43,43,4
U 44,44,4
Zvaa 120,148,204
D 18,40,94,2048
U 45,45,4
Mvaa 120,144,200
Ta 128,25,10368
Ta 130,25,15488
Ta 132,25,20608
Ta 134,25,25728
W 4
Zvaa 124,152,200
U 46,46,4
U 47,47,4
Mvaa 124,156,204
K 40,40,41
K 41,42,43
Zvaa 116,156,188
K 42,44,45
K 43,46,47
Mvaa 116,152,184
Ta 136,26,10400
Ta 138,26,15520
Ta 140,26,20640
Ta 142,26,25760
W 4
Zvaa 128,128,184
buffer_store_dwordx4 v[40:43], v1, s[8:11], s93 offen offset:1024 sc1
U 48,48,4
Mvaa 128,132,188
U 49,49,4
U 50,50,4
Zvaa 136,132,204
U 51,51,4
U 52,52,4
Mvaa 136,128,200
Ta 144,25,10432
Ta 146,25,15552
Ta 148,25,20672
Ta 150,25,25792
W 4
Zvaa 140,136,200
D 19,40,94,3072
U 53,53,4
Mvaa 140,140,204
s_add_u32 m0, m0, 4096
U 54,54,4
U 55,55,4
Zvaa 132,140,188
K 48,48,49
K 49,50,51
Mvaa 132,136,184
Ta 152,26,10464
Ta 154,26,15584
Ta 156,26,20704
Ta 158,26,25824
W 4
Zvaa 144,144,184
K 50,52,53
K 51,54,55
Mvaa 144,148,188
buffer_load_dwordx4 v20, s[40:43], s94 offen lds
Ta 128,25,10496
Zvaa 152,148,204
s_add_u32 m0, s71, s48
s_lshl2_add_u32 s94, s48, s95
Ta 130,25,15616
Mvaa 152,144,200
Ta 132,25,20736
Ta 134,25,25856
W 4
Zvaa 156,152,200
Ta 136,26,10528
Ta 138,26,15648
Mvaa 156,156,204
buffer_store_dwordx4 v[48:51], v1, s[8:11], s93 offen offset:2048 sc1
U 56,56,4
Zvaa 148,156,188
U 57,57,4
U 58,58,4
Mvaa 148,152,184
Ta 140,26,20768
Ta 142,26,25888
W 4
Zvaa 160,128,184
U 59,59,4
U 60,60,4
Mvaa 160,132,188
U 61,61,4
U 62,62,4
Zvaa 168,132,204
U 63,63,4
K 56,56,57
Mvaa 168,128,200
Ta 144,25,30720
Ta 146,25,35840
Ta 148,21,0
Ta 150,21,5120
W 4
Zvaa 172,136,200
D 16,40,94,0
K 57,58,59
Mvaa 172,140,204
K 58,60,61
K 59,62,63
Zvaa 164,140,188
Ta 152,26,30752
Ta 154,26,35872
Mvaa 164,136,184
Ta 156,22,32
Ta 158,22,5152
W 4
Mvaa 96,144,192
buffer_store_dwordx4 v[56:59], v1, s[8:11], s93 offen offset:3072 sc1
U 64,64,5
Mvaa 96,148,196
U 65,65,5
U 66,66,5
Mvaa 104,148,212
U 67,67,5
U 68,68,5
Mvaa 104,144,208
Ta 128,25,30784
Ta 130,25,35904
Ta 132,21,64
Ta 134,21,5184
W 4
Mvaa 108,152,208
D 17,40,94,1024
U 69,69,5
Mvaa 108,156,212
U 70,70,5
U 71,71,5
Mvaa 100,156,196
K 64,64,65
K 65,66,67
Mvaa 100,152,192
Ta 136,26,30816
Ta 138,26,35936
Ta 140,22,96
Ta 142,22,5216
W 4
Mvaa 112,128,192
K 66,68,69
K 67,70,71
Mvaa 112,132,196
s_add_u32 s67, s93, 131072
buffer_store_dwordx4 v[64:67], v1, s[8:11], s67 offen offset:0 sc1
U 72,72,5
Mvaa 120,132,212
U 73,73,5
U 74,74,5
Mvaa 120,128,208
Ta 144,25,30848
Ta 146,25,35968
Ta 148,21,128
Ta 150,21,5248
W 4
Mvaa 124,136,208
U 75,75,5
U 76,76,5
Mvaa 124,140,212
D 18,40,94,2048
U 77,77,5
Mvaa 116,140,196
U 78,78,5
U 79,79,5
Mvaa 116,136,192
Ta 152,26,30880
Ta 154,26,36000
Ta 156,22,160
Ta 158,22,5280
W 4
Mvaa 128,144,192
K 72,72,73
K 73,74,75
Mvaa 128,148,196
K 74,76,77
K 75,78,79
Mvaa 136,148,212
buffer_store_dwordx4 v[72:75], v1, s[8:11], s67 offen offset:1024 sc1
U 80,80,5
Mvaa 136,144,208
Ta 128,25,30912
Ta 130,25,36032
Ta 132,21,192
Ta 134,21,5312
W 4
Mvaa 140,152,208
D 19,40,94,3072
s_add_u32 m0, m0, 4096
s_nop 0
buffer_load_dwordx4 v20, s[40:43], s94 offen lds
Mvaa 140,156,212
s_add_u32 s81, s58, 2
s_and_b32 s81, s81, 3
s_lshl_b32 s81, s81, 12
Mvaa 132,156,196
s_add_u32 s81, s81, s49
U 81,81,5
U 82,82,5
Mvaa 132,152,192
Ta 136,26,30944
Ta 138,26,36064
Ta 140,22,224
Ta 142,22,5344
W 4
Mvaa 144,128,192
U 83,83,5
U 84,84,5
Mvaa 144,132,196
U 85,85,5
U 86,86,5
Mvaa 152,132,212
U 87,87,5
K 80,80,81
Mvaa 152,128,208
Ta 144,25,30976
Ta 146,25,36096
Ta 148,21,256
Ta 150,21,5376
W 4
Mvaa 156,136,208
K 81,82,83
K 82,84,85
Mvaa 156,140,212
buffer_load_dwordx4 a[0:3], v1, s[8:11], s81 offen offset:0 sc1
K 83,86,87
Mvaa 148,140,196
s_add_u32 s82, s81, 131072
Ta 152,26,31008
Ta 154,26,36128
Mvaa 148,136,192
Ta 156,22,288
Ta 158,22,5408
W 4
)"
R"(Mvaa 160,144,192
Ta 128,21,10240
Ta 130,21,15360
Mvaa 160,148,196
buffer_store_dwordx4 v[80:83], v1, s[8:11], s67 offen offset:2048 sc1
U 88,88,5
Mvaa 168,148,212
U 89,89,5
U 90,90,5
Mvaa 168,144,208
Ta 132,21,20480
Ta 134,21,25600
W 4
Mvaa 172,152,208
U 91,91,5
U 92,92,5
Mvaa 172,156,212
U 93,93,5
U 94,94,5
Mvaa 164,156,196
U 95,95,5
K 88,88,89
Mvaa 164,152,192
Ta 136,22,10272
Ta 138,22,15392
Ta 140,22,20512
Ta 142,22,25632
s_waitcnt vmcnt(22) lgkmcnt(4)
Mvaa 96,128,216
K 89,90,91
K 90,92,93
Mvaa 96,132,220
buffer_load_dwordx4 a[16:19], v1, s[8:11], s82 offen offset:0 sc1
K 91,94,95
Mvaa 104,132,236
Ta 144,21,10304
Ta 146,21,15424
Mvaa 104,128,232
Ta 148,21,20544
Ta 150,21,25664
W 4
Mvaa 108,136,232
Ta 152,22,10336
Ta 154,22,15456
Mvaa 108,140,236
buffer_store_dwordx4 v[88:91], v1, s[8:11], s67 offen offset:3072 sc1
Ta 156,22,20576
Mvaa 100,140,220
Ta 158,22,25696
Ta 128,21,10368
Mvaa 100,136,216
W 5
Ta 130,21,15488
Ta 132,21,20608
Mvaa 112,144,216
Ta 134,21,25728
Ta 136,22,10400
Mvaa 112,148,220
buffer_load_dwordx4 a[4:7], v1, s[8:11], s81 offen offset:1024 sc1
Ta 138,22,15520
Mvaa 120,148,236
Ta 140,22,20640
Ta 142,22,25760
Mvaa 120,144,232
W 8
Ta 144,21,10432
Ta 146,21,15552
Mvaa 124,152,232
Mvaa 124,156,236
buffer_load_dwordx4 a[20:23], v1, s[8:11], s82 offen offset:1024 sc1
Ta 148,21,20672
Mvaa 116,156,220
Ta 150,21,25792
Mvaa 116,152,216
W 8
Ta 152,22,10464
Ta 154,22,15584
Mvaa 128,128,216
Mvaa 128,132,220
buffer_load_dwordx4 a[8:11], v1, s[8:11], s81 offen offset:2048 sc1
Ta 156,22,20704
Mvaa 136,132,236
Ta 158,22,25824
Mvaa 136,128,232
W 8
Ta 128,21,10496
Ta 130,21,15616
Mvaa 140,136,232
s_waitcnt vmcnt(7)
s_barrier
Mvaa 140,140,236
buffer_load_dwordx4 a[24:27], v1, s[8:11], s82 offen offset:2048 sc1
Ta 132,21,20736
Mvaa 132,140,220
Ta 134,21,25856
Mvaa 132,136,216
W 8
Ta 136,22,10528
Ta 138,22,15648
Mvaa 144,144,216
Mvaa 144,148,220
buffer_load_dwordx4 a[12:15], v1, s[8:11], s81 offen offset:3072 sc1
Ta 140,22,20768
Mvaa 152,148,236
Ta 142,22,25888
Mvaa 152,144,232
W 8
Ta 144,21,30720
Ta 146,21,35840
Mvaa 156,152,232
Mvaa 156,156,236
buffer_load_dwordx4 a[28:31], v1, s[8:11], s82 offen offset:3072 sc1
Ta 148,23,0
Mvaa 148,156,220
s_lshl_b32 s92, s58, 2
s_add_u32 s91, s92, s51
s_add_u32 s95, s91, 11
Mvaa 148,152,216
W 7
s_and_b32 s95, s95, 15
s_mul_i32 s95, s95, 40960
Mvaa 160,128,216
s_mul_i32 s71, s51, 10240
s_add_u32 s71, s71, 40960
s_add_u32 m0, s71, s48
Mvaa 160,132,220
s_lshl2_add_u32 s94, s48, s95
D 16,40,94,0
Ta 150,23,5120
Mvaa 168,132,236
s_add_u32 s95, s91, 13
s_and_b32 s95, s95, 15
s_mul_i32 s95, s95, 40960
Mvaa 168,128,232
W 4
s_mul_i32 s71, s51, 20480
s_add_u32 s71, s71, 71680
Mvaa 172,136,232
Ta 152,22,30752
Ta 154,22,35872
Mvaa 172,140,236
D 17,40,94,1024
Ta 156,24,32
Mvaa 164,140,220
Ta 158,24,5152
Ta 128,21,30784
Mvaa 164,136,216
W 5
Ta 130,21,35904
Ta 132,23,64
Mvaa 96,144,224
Ta 134,23,5184
Ta 136,22,30816
Mvaa 96,148,228
D 18,40,94,2048
Ta 138,22,35936
Mvaa 104,148,244
Ta 140,24,96
Ta 142,24,5216
Mvaa 104,144,240
W 8
Ta 144,21,30848
Ta 146,21,35968
Mvaa 108,152,240
Mvaa 108,156,244
D 19,40,94,3072
Ta 148,23,128
Mvaa 100,156,228
s_add_u32 m0, m0, 4096
Ta 150,23,5248
Mvaa 100,152,224
W 8
Ta 152,22,30880
Ta 154,22,36000
Mvaa 112,128,224
Mvaa 112,132,228
buffer_load_dwordx4 v20, s[40:43], s94 offen lds
Ta 156,24,160
Mvaa 120,132,244
s_add_u32 m0, s71, s48
s_lshl2_add_u32 s94, s48, s95
Ta 158,24,5280
Mvaa 120,128,240
W 8
Ta 128,21,30912
Ta 130,21,36032
Mvaa 124,136,240
Mvaa 124,140,244
D 16,40,94,0
Ta 132,23,192
Mvaa 116,140,228
s_add_u32 s95, s92, 15
s_and_b32 s95, s95, 15
s_mul_i32 s95, s95, 40960
Mvaa 116,136,224
W 7
s_mul_i32 s71, s51, 0
s_add_u32 s71, s71, 102400
Mvaa 128,144,224
Ta 134,23,5312
Ta 136,22,30944
Mvaa 128,148,228
D 17,40,94,1024
Ta 138,22,36064
Mvaa 136,148,244
Ta 140,24,224
Ta 142,24,5344
Mvaa 136,144,240
W 8
Ta 144,21,30976
Ta 146,21,36096
Mvaa 140,152,240
Mvaa 140,156,244
D 18,40,94,2048
Ta 148,23,256
Mvaa 132,156,228
Ta 150,23,5376
Mvaa 132,152,224
W 8
Ta 152,22,31008
Ta 154,22,36128
Mvaa 144,128,224
s_lshl_b32 s80, s58, 3
s_add_u32 s80, s80, s55
Mvaa 144,132,228
D 19,40,94,3072
Ta 156,24,288
Mvaa 152,132,244
s_add_u32 m0, m0, 4096
Ta 158,24,5408
Mvaa 152,128,240
W 8
Ta 128,23,20480
Ta 130,23,25600
Mvaa 156,136,240
s_waitcnt vmcnt(15)
v_mov_b64 v[28:29], s[12:13]
s_mov_b64 exec, 1
buffer_store_dwordx2 v[28:29], off, s[8:11], s80 sc1
s_mov_b64 exec, -1
Mvaa 156,140,244
Ta 132,23,51200
Ta 134,23,56320
Mvaa 148,140,228
Mvaa 148,136,224
W 8
Ta 136,24,20512
Ta 138,24,25632
Mvaa 160,144,224
buffer_load_dwordx4 v20, s[40:43], s94 offen lds
Mvaa 160,148,228
s_add_u32 m0, s71, s48
s_lshl2_add_u32 s94, s48, s95
Ta 140,24,51232
Mvaa 168,148,244
Ta 142,24,56352
Mvaa 168,144,240
W 8
Ta 144,23,20544
Ta 146,23,25664
Mvaa 172,152,240
D 16,40,94,0
Mvaa 172,156,244
Ta 148,23,51264
Ta 150,23,56384
Mvaa 164,156,228
Mvaa 164,152,224
s_waitcnt vmcnt(16) lgkmcnt(8)
Ta 152,24,20576
Ta 154,24,25696
Mvaa 96,128,0
D 17,40,94,1024
Mvaa 96,132,4
Ta 156,24,51296
Ta 158,24,56416
Mvaa 104,132,20
Mvaa 104,128,16
W 8
Ta 128,23,20608
Ta 130,23,25728
Mvaa 108,136,16
D 18,40,94,2048
Mvaa 108,140,20
Ta 132,23,51328
Ta 134,23,56448
Mvaa 100,140,4
Mvaa 100,136,0
W 8
Ta 136,24,20640
Ta 138,24,25760
Mvaa 112,144,0
D 19,40,94,3072
Mvaa 112,148,4
s_add_u32 m0, m0, 4096
Ta 140,24,51360
Ta 142,24,56480
Mvaa 120,148,20
Mvaa 120,144,16
W 8
Ta 144,23,20672
Ta 146,23,25792
Mvaa 124,152,16
buffer_load_dwordx4 v20, s[40:43], s94 offen lds
Mvaa 124,156,20
s_add_u32 s81, s58, 3
s_and_b32 s81, s81, 3
s_lshl_b32 s81, s81, 12
Mvaa 116,156,4
s_add_u32 s81, s81, s49
Ta 148,23,51392
Ta 150,23,56512
Mvaa 116,152,0
W 8
Ta 152,24,20704
Ta 154,24,25824
Mvaa 128,128,0
buffer_load_dwordx4 a[32:35], v1, s[8:11], s81 offen offset:0 sc1
Mvaa 128,132,4
s_add_u32 s82, s81, 131072
Ta 156,24,51424
Ta 158,24,56544
Mvaa 136,132,20
Mvaa 136,128,16
W 8
Ta 128,23,20736
Ta 130,23,25856
Mvaa 140,136,16
s_waitcnt vmcnt(12)
s_barrier
buffer_load_dwordx4 a[48:51], v1, s[8:11], s82 offen offset:0 sc1
Mvaa 140,140,20
Ta 132,23,51456
v_and_b32 v27, 1, v12
Mvaa 132,140,4
Ta 134,23,56576
Mvaa 132,136,0
W 8
Ta 136,24,20768
v_lshlrev_b32 v27, 1, v27
Mvaa 144,144,0
buffer_load_dwordx4 a[36:39], v1, s[8:11], s81 offen offset:1024 sc1
Ta 138,24,25888
Mvaa 144,148,4
Ta 140,24,51488
Ta 142,24,56608
Mvaa 152,148,20
v_xor_b32 v27, v27, v13
Mvaa 152,144,16
W 8
Ta 144,25,0
Ta 146,25,5120
Mvaa 156,152,16
buffer_load_dwordx4 a[52:55], v1, s[8:11], s82 offen offset:1024 sc1
v_and_b32 v12, 2, v12
Mvaa 156,156,20
Ta 148,21,40960
Ta 150,21,46080
Mvaa 148,156,4
Mvaa 148,152,0
W 8
Ta 152,26,32
v_or_b32 v12, v12, v14
Mvaa 160,128,0
buffer_load_dwordx4 a[40:43], v1, s[8:11], s81 offen offset:2048 sc1
Ta 154,26,5152
Mvaa 160,132,4
Ta 156,22,40992
v_lshl_or_b32 v12, v27, 2, v12
Mvaa 168,132,20
Ta 158,22,46112
Mvaa 168,128,16
W 8
Ta 128,25,64
Ta 130,25,5184
Mvaa 172,136,16
buffer_load_dwordx4 a[56:59], v1, s[8:11], s82 offen offset:2048 sc1
v_cmp_eq_u32_e64 s[86:87], 0, v12
Mvaa 172,140,20
Ta 132,21,41024
Ta 134,21,46144
Mvaa 164,140,4
Mvaa 164,136,0
W 8
Ta 136,26,96
v_cndmask_b32_e64 v4, 0, v15, s[86:87]
Mvaa 96,144,8
buffer_load_dwordx4 a[44:47], v1, s[8:11], s81 offen offset:3072 sc1
Ta 138,26,5216
Mvaa 96,148,12
Ta 140,22,41056
v_cmp_eq_u32_e64 s[86:87], 1, v12
Mvaa 104,148,28
Ta 142,22,46176
Mvaa 104,144,24
W 8
Ta 144,25,128
Ta 146,25,5248
Mvaa 108,152,24
buffer_load_dwordx4 a[60:63], v1, s[8:11], s82 offen offset:3072 sc1
v_cndmask_b32_e64 v5, 0, v15, s[86:87]
)"
R"(Mvaa 108,156,28
Ta 148,21,41088
Ta 150,21,46208
Mvaa 100,156,12
v_cmp_eq_u32_e64 s[86:87], 2, v12
Mvaa 100,152,8
W 8
Ta 152,26,160
Ta 154,26,5280
Mvaa 112,128,8
Mvaa 112,132,12
Ta 156,22,41120
v_cndmask_b32_e64 v6, 0, v15, s[86:87]
Mvaa 120,132,28
Ta 158,22,46240
Mvaa 120,128,24
W 8
Ta 128,25,192
Ta 130,25,5312
Mvaa 124,136,24
v_cmp_eq_u32_e64 s[86:87], 3, v12
Mvaa 124,140,28
Ta 132,21,41152
Ta 134,21,46272
Mvaa 116,140,12
v_cndmask_b32_e64 v7, 0, v15, s[86:87]
Mvaa 116,136,8
W 8
Ta 136,26,224
Ta 138,26,5344
Mvaa 128,144,8
Mvaa 128,148,12
Ta 140,22,41184
v_cmp_eq_u32_e64 s[86:87], 4, v12
Mvaa 136,148,28
Ta 142,22,46304
Mvaa 136,144,24
W 8
Ta 144,25,256
v_cndmask_b32_e64 v8, 0, v15, s[86:87]
Mvaa 140,152,24
s_waitcnt vmcnt(8)
s_barrier
Ta 146,25,5376
Mvaa 140,156,28
Ta 148,21,41216
Ta 150,21,46336
Mvaa 132,156,12
v_cmp_eq_u32_e64 s[86:87], 5, v12
Mvaa 132,152,8
W 8
Ta 152,26,288
Ta 154,26,5408
Mvaa 144,128,8
Mvaa 144,132,12
Ta 156,22,41248
v_cndmask_b32_e64 v9, 0, v15, s[86:87]
Mvaa 152,132,28
Ta 158,22,46368
Mvaa 152,128,24
W 8
Ta 128,21,51200
v_cmp_eq_u32_e64 s[86:87], 6, v12
Mvaa 156,136,24
Ta 130,21,56320
Mvaa 156,140,28
Ta 132,23,10240
Ta 134,23,15360
Mvaa 148,140,12
v_cndmask_b32_e64 v10, 0, v15, s[86:87]
Mvaa 148,136,8
W 8
Ta 136,22,51232
Ta 138,22,56352
Mvaa 160,144,8
v_cmp_eq_u32_e64 s[86:87], 7, v12
v_bfe_u32 v28, v1, 4, 2
Mvaa 160,148,12
Ta 140,24,10272
v_lshlrev_b32 v28, 3, v28
Mvaa 168,148,28
Ta 142,24,15392
buffer_load_dwordx2 v[28:29], v28, s[8:11], s55 offen sc1
Mvaa 168,144,24
W 8
Ta 144,21,51264
v_cndmask_b32_e64 v11, 0, v15, s[86:87]
Mvaa 172,152,24
Ta 146,21,56384
Mvaa 172,156,28
Ta 148,23,10304
Ta 150,23,15424
Mvaa 164,156,12
s_mov_b32 s20, s38
Mvaa 164,152,8
s_waitcnt vmcnt(5) lgkmcnt(8)
Ta 152,22,51296
Ta 154,22,56416
Mvaa 96,128,32
s_and_b32 s21, s39, 0xffff
Mvaa 96,132,36
Ta 156,24,10336
Ta 158,24,15456
Mvaa 104,132,52
Mvaa 104,128,48
W 8
s_mov_b32 s22, 2621440
Ta 128,21,51328
Mvaa 108,136,48
Ta 130,21,56448
Mvaa 108,140,52
s_mov_b32 s23, 0x20000
Ta 132,23,10368
Ta 134,23,15488
Mvaa 100,140,36
Mvaa 100,136,32
W 8
Ta 136,22,51360
Ta 138,22,56480
Mvaa 112,144,32
s_and_b32 s17, s17, 0xffff
Mvaa 112,148,36
Ta 140,24,10400
Ta 142,24,15520
Mvaa 120,148,52
Mvaa 120,144,48
W 8
s_mov_b32 s18, 2621440
Ta 144,21,51392
Mvaa 124,152,48
Ta 146,21,56512
Mvaa 124,156,52
s_mov_b32 s19, 0x20000
Ta 148,23,10432
Ta 150,23,15552
Mvaa 116,156,36
Mvaa 116,152,32
W 8
Ta 152,22,51424
Ta 154,22,56544
Mvaa 128,128,32
s_lshr_b32 s66, s15, 3
Mvaa 128,132,36
Ta 156,24,10464
Ta 158,24,15584
Mvaa 136,132,52
s_lshl_b32 s66, s66, 4
Mvaa 136,128,48
W 8
Ta 128,21,51456
Ta 130,21,56576
Mvaa 140,136,48
Mvaa 140,140,52
s_add_u32 s66, s66, s59
Ta 132,23,10496
Ta 134,23,15616
Mvaa 132,140,36
Mvaa 132,136,32
W 8
Ta 136,22,51488
Ta 138,22,56608
Mvaa 144,144,32
s_lshl_b32 s63, s66, 6
Mvaa 144,148,36
Ta 140,24,10528
Ta 142,24,15648
Mvaa 152,148,52
s_add_u32 s63, s63, 65536
Mvaa 152,144,48
W 8
Ta 144,23,30720
Ta 146,23,35840
Mvaa 156,152,48
Mvaa 156,156,52
s_mul_i32 s62, s66, 40960
Ta 148,23,40960
Ta 150,23,46080
Mvaa 148,156,36
Mvaa 148,152,32
W 8
s_add_u32 s62, s62, 56623104
Ta 152,24,30752
Mvaa 160,128,32
Ta 154,24,35872
Mvaa 160,132,36
Ta 156,24,40992
Ta 158,24,46112
Mvaa 168,132,52
s_lshl_b32 s66, s14, 13
s_waitcnt vmcnt(0)
v_cmp_eq_u64 vcc, s[12:13], v[28:29]
s_cmp_eq_u64 vcc, -1
s_cbranch_scc0 .Lspin_84%=
.Lok_85%=:
s_add_u32 s81, s58, 1
Mvaa 168,128,48
W 8
s_and_b32 s81, s81, 3
s_lshl_b32 s81, s81, 12
Mvaa 172,136,48
s_add_u32 s81, s81, s50
buffer_load_dwordx4 a[64:67], v1, s[8:11], s81 offen offset:0 sc1
Ta 128,23,30784
Mvaa 172,140,52
s_add_u32 s82, s81, 131072
s_add_u32 s78, s62, s66
Ta 130,23,35904
Mvaa 164,140,36
Ta 132,23,41024
Ta 134,23,46144
Mvaa 164,136,32
W 8
s_add_u32 s79, s78, 327680
Ta 136,24,30816
Mvaa 96,144,40
Ta 138,24,35936
buffer_load_dwordx4 a[80:83], v1, s[8:11], s82 offen offset:0 sc1
Mvaa 96,148,44
Ta 140,24,41056
Ta 142,24,46176
Mvaa 104,148,60
s_and_b32 s66, s14, 1
Mvaa 104,144,56
W 8
Ta 144,23,30848
Ta 146,23,35968
Mvaa 108,152,56
s_mul_i32 s66, s66, 327680
Mvaa 108,156,60
buffer_load_dwordx4 a[68:71], v1, s[8:11], s81 offen offset:1024 sc1
Ta 148,23,41088
Mvaa 100,156,44
Ta 150,23,46208
Mvaa 100,152,40
W 8
s_add_u32 s65, s62, s66
Ta 152,24,30880
Mvaa 112,128,40
Ta 154,24,36000
Mvaa 112,132,44
Ta 156,24,41120
Ta 158,24,46240
Mvaa 120,132,60
s_add_u32 s65, s65, 32768
buffer_load_dwordx4 a[84:87], v1, s[8:11], s82 offen offset:1024 sc1
Mvaa 120,128,56
W 8
Ta 128,23,30912
Ta 130,23,36032
Mvaa 124,136,56
s_mov_b32 s64, 0
Mvaa 124,140,60
Ta 132,23,41152
Ta 134,23,46272
Mvaa 116,140,44
Mvaa 116,136,40
W 8
buffer_load_dwordx4 a[72:75], v1, s[8:11], s81 offen offset:2048 sc1
v_add_u32 v19, 4096, v1
Mvaa 128,144,40
Ta 136,24,30944
Ta 138,24,36064
Mvaa 128,148,44
Ta 140,24,41184
v_bfe_u32 v27, v1, 4, 3
Mvaa 136,148,60
Ta 142,24,46304
Mvaa 136,144,56
W 8
Ta 144,23,30976
Ta 146,23,36096
Mvaa 140,152,56
buffer_load_dwordx4 a[88:91], v1, s[8:11], s82 offen offset:2048 sc1
v_lshlrev_b32 v27, 3, v27
Mvaa 140,156,60
Ta 148,23,41216
Ta 150,23,46336
Mvaa 132,156,44
v_bfe_u32 v18, v1, 7, 1
Mvaa 132,152,40
W 8
Ta 152,24,31008
Ta 154,24,36128
Mvaa 144,128,40
Mvaa 144,132,44
buffer_load_dwordx4 a[76:79], v1, s[8:11], s81 offen offset:3072 sc1
v_lshl_add_u32 v18, v18, 9, v27
Mvaa 152,132,60
Ta 156,24,41248
Ta 158,24,46368
Mvaa 152,128,56
W 8
Ta 128,25,10240
Ta 130,25,15360
Mvaa 156,136,56
s_mov_b32 s71, 0
Mvaa 156,140,60
Ta 132,25,20480
Ta 134,25,25600
Mvaa 148,140,44
buffer_load_dwordx4 a[92:95], v1, s[8:11], s82 offen offset:3072 sc1
v_bfe_u32 v20, v1, 4, 4
Mvaa 148,136,40
W 8
s_add_u32 s81, s58, 2
s_and_b32 s81, s81, 3
Mvaa 160,144,40
s_lshl_b32 s81, s81, 12
s_add_u32 s81, s81, s50
K 96,96,97
Mvaa 160,148,44
Ta 136,26,10272
Ta 138,26,15392
Mvaa 168,148,60
K 97,98,99
v_mul_u32_u24 v20, 1280, v20
Mvaa 168,144,56
W 6
Ta 140,26,20512
Ta 142,26,25632
Mvaa 172,152,56
buffer_load_dwordx4 a[96:99], v1, s[8:11], s81 offen offset:0 sc1
K 98,100,101
Mvaa 172,156,60
s_add_u32 s82, s81, 131072
Ta 144,25,10304
v_lshrrev_b32 v12, 8, v1
Mvaa 164,156,44
Ta 146,25,15424
Ta 148,25,20544
Mvaa 164,152,40
W 7
Ta 150,25,25664
K 99,102,103
Zvav 176,128,32
s_add_u32 s83, s52, 0
Ta 152,26,10336
v_lshl_add_u32 v20, v12, 4, v20
Mvav 176,132,40
s_bfe_u32 s66, s59, 0x10001
Ta 154,26,15456
buffer_load_dwordx4 a[112:115], v1, s[8:11], s82 offen offset:0 sc1
Zvav 184,132,72
Ta 156,26,20576
Ta 158,26,25696
Mvav 184,128,64
W 8
s_lshl_b32 s67, s57, 1
Ta 128,25,10368
Zvav 188,136,64
Ta 130,25,15488
Mvav 188,140,72
buffer_store_dwordx4 v[96:99], v1, s[8:11], s83 offen sc1
K 104,104,105
Zvav 180,140,40
s_add_u32 s66, s66, s67
Ta 132,25,20608
Ta 134,25,25728
Mvav 180,136,32
W 8
Ta 136,26,10400
K 105,106,107
Zvav 192,144,32
s_lshl_b32 s61, s66, 2
Ta 138,26,15520
Mvav 192,148,40
buffer_load_dwordx4 a[100:103], v1, s[8:11], s81 offen offset:1024 sc1
K 106,108,109
Zvav 200,148,72
Ta 140,26,20640
Ta 142,26,25760
Mvav 200,144,64
W 8
s_lshr_b32 s66, s59, 2
Ta 144,25,10432
Zvav 204,152,64
Ta 146,25,15552
K 107,110,111
Mvav 204,156,72
buffer_load_dwordx4 a[116:119], v1, s[8:11], s82 offen offset:1024 sc1
Ta 148,25,20672
Zvav 196,156,40
s_lshl_b32 s66, s66, 1
s_add_u32 s83, s52, 40960
Ta 150,25,25792
Mvav 196,152,32
W 8
Ta 152,26,10464
Ta 154,26,15584
Zvav 208,128,32
s_add_u32 s61, s61, s66
Mvav 208,132,40
buffer_load_dwordx4 a[104:107], v1, s[8:11], s81 offen offset:2048 sc1
Ta 156,26,20704
Zvav 216,132,72
Ta 158,26,25824
Mvav 216,128,64
W 8
s_and_b32 s66, s59, 1
Ta 128,25,10496
)"
R"(Zvav 220,136,64
Ta 130,25,15616
Mvav 220,140,72
s_add_u32 s61, s61, s66
buffer_store_dwordx4 v[104:107], v1, s[8:11], s83 offen sc1
K 112,112,113
Zvav 212,140,40
Ta 132,25,20736
K 113,114,115
Mvav 212,136,32
W 7
Ta 134,25,25856
K 114,116,117
Zvav 224,144,32
s_xor_b32 s67, s61, 0
Ta 136,26,10528
Ta 138,26,15648
Mvav 224,148,40
buffer_load_dwordx4 a[120:123], v1, s[8:11], s82 offen offset:2048 sc1
Ta 140,26,20768
Zvav 232,148,72
Ta 142,26,25888
K 115,118,119
Mvav 232,144,64
W 8
s_mul_i32 s72, s67, 20480
Ta 144,25,30720
Zvav 236,152,64
s_add_u32 s83, s52, 8192
Ta 146,25,35840
Mvav 236,156,72
s_mul_i32 s66, s58, 320
buffer_load_dwordx4 a[108:111], v1, s[8:11], s81 offen offset:3072 sc1
Ta 148,21,0
Zvav 228,156,40
Ta 150,21,5120
Mvav 228,152,32
W 8
Ta 152,26,30752
Ta 154,26,35872
Zvav 240,128,32
s_add_u32 s72, s72, s66
Mvav 240,132,40
buffer_store_dwordx4 v[112:115], v1, s[8:11], s83 offen sc1
K 120,120,121
Zvav 248,132,72
s_mul_i32 s75, s67, 20480
Ta 156,22,32
Ta 158,22,5152
Mvav 248,128,64
W 8
Ta 128,25,30784
K 121,122,123
Zvav 252,136,64
Ta 130,25,35904
Mvav 252,140,72
s_mul_i32 s66, s58, 2560
buffer_load_dwordx4 a[124:127], v1, s[8:11], s82 offen offset:3072 sc1
K 122,124,125
Zvav 244,140,40
s_add_u32 s81, s58, 3
s_and_b32 s81, s81, 3
s_lshl_b32 s81, s81, 12
Mvav 244,136,32
W 6
s_add_u32 s81, s81, s50
Ta 132,21,64
Mvav 176,144,48
s_add_u32 s75, s75, s66
Ta 134,21,5184
Ta 136,26,30816
Mvav 176,148,56
buffer_load_dwordx4 a[184:187], v1, s[8:11], s81 offen offset:0 sc1
Ta 138,26,35936
Mvav 184,148,88
s_add_u32 s82, s81, 131072
Ta 140,22,96
K 123,126,127
Mvav 184,144,80
W 7
s_add_u32 s83, s52, 49152
Ta 142,22,5216
Mvav 188,152,80
s_add_u32 s75, s75, 131072
Ta 144,25,30848
Ta 146,25,35968
Mvav 188,156,88
s_xor_b32 s67, s61, 124
buffer_load_dwordx4 a[200:203], v1, s[8:11], s82 offen offset:0 sc1
Ta 148,21,128
Mvav 180,156,56
Ta 150,21,5248
Mvav 180,152,48
W 8
s_mul_i32 s73, s67, 20480
Ta 152,26,30880
Mvav 192,128,48
Ta 154,26,36000
Mvav 192,132,56
buffer_store_dwordx4 v[120:123], v1, s[8:11], s83 offen sc1
K 128,128,129
Mvav 200,132,88
s_mul_i32 s66, s58, 320
Ta 156,22,160
K 129,130,131
Mvav 200,128,80
W 7
Ta 158,22,5280
K 130,132,133
Mvav 204,136,80
Ta 128,25,30912
Ta 130,25,36032
Mvav 204,140,88
s_add_u32 s73, s73, s66
buffer_load_dwordx4 a[188:191], v1, s[8:11], s81 offen offset:1024 sc1
Ta 132,21,192
Mvav 196,140,56
Ta 134,21,5312
K 131,134,135
Mvav 196,136,48
W 8
s_mul_i32 s76, s67, 20480
Ta 136,26,30944
Mvav 208,144,48
s_add_u32 s83, s52, 16384
Ta 138,26,36064
Mvav 208,148,56
buffer_load_dwordx4 a[204:207], v1, s[8:11], s82 offen offset:1024 sc1
Ta 140,22,224
Mvav 216,148,88
s_mul_i32 s66, s58, 2560
Ta 142,22,5344
Mvav 216,144,80
W 8
Ta 144,25,30976
Ta 146,25,36096
Mvav 220,152,80
s_add_u32 s76, s76, s66
Mvav 220,156,88
buffer_load_dwordx4 a[192:195], v1, s[8:11], s81 offen offset:2048 sc1
Ta 148,21,256
Mvav 212,156,56
Ta 150,21,5376
Mvav 212,152,48
W 8
s_add_u32 s76, s76, 131072
Ta 152,26,31008
Mvav 224,128,48
Ta 154,26,36128
Mvav 224,132,56
buffer_store_dwordx4 v[128:131], v1, s[8:11], s83 offen sc1
K 136,136,137
Mvav 232,132,88
K 137,138,139
K 138,140,141
Mvav 232,128,80
W 6
Ta 156,22,288
Ta 158,22,5408
Mvav 236,136,80
Ta 128,21,10240
v_bfe_u32 v15, v1, 7, 1
Mvav 236,140,88
buffer_load_dwordx4 a[208:211], v1, s[8:11], s82 offen offset:2048 sc1
Ta 130,21,15360
Mvav 228,140,56
K 139,142,143
v_mul_u32_u24 v14, 10240, v15
Mvav 228,136,48
W 6
Ta 132,21,20480
Ta 134,21,25600
Mvav 240,144,48
s_add_u32 s83, s52, 57344
Ta 136,22,10272
v_lshrrev_b32 v15, 8, v1
Mvav 240,148,56
buffer_load_dwordx4 a[196:199], v1, s[8:11], s81 offen offset:3072 sc1
Ta 138,22,15392
Mvav 248,148,88
Ta 140,22,20512
v_lshl_add_u32 v14, v15, 7, v14
Mvav 248,144,80
W 7
Ta 142,22,25632
Ta 144,21,10304
Mvav 252,152,80
Ta 146,21,15424
v_and_b32 v15, 0x70, v1
Mvav 252,156,88
buffer_load_dwordx4 a[212:215], v1, s[8:11], s82 offen offset:3072 sc1
Ta 148,21,20544
Mvav 244,156,56
Ta 150,21,25664
Mvav 244,152,48
s_waitcnt vmcnt(25) lgkmcnt(8)
Ta 152,22,10336
v_add_u32 v14, v14, v15
Mvaa 176,128,64
Ta 154,22,15456
Mvaa 176,132,68
s_bitcmp1_b32 s14, 0
s_cselect_b32 s74, s73, s72
s_cselect_b32 s77, s76, s75
buffer_store_dwordx4 v[136:139], v1, s[8:11], s83 offen sc1
K 144,144,145
Mvaa 184,132,84
K 145,146,147
K 146,148,149
Mvaa 184,128,80
W 6
Ta 156,22,20576
Ta 158,22,25696
Mvaa 188,136,80
s_add_u32 s74, s74, 256
Ta 128,21,10368
K 147,150,151
Mvaa 188,140,84
s_add_u32 s83, s52, 24576
Ta 130,21,15488
Ta 132,21,20608
Mvaa 180,140,68
s_add_u32 s77, s77, 2048
Ta 134,21,25728
buffer_store_dwordx4 v[144:147], v1, s[8:11], s83 offen sc1
Mvaa 180,136,64
W 8
Ta 136,22,10400
Ta 138,22,15520
Mvaa 192,144,64
K 152,152,153
Mvaa 192,148,68
s_lshl_b32 s66, s14, 6
Ta 140,22,20640
Ta 142,22,25760
Mvaa 200,148,84
Mvaa 200,144,80
W 8
Ta 144,21,10432
K 153,154,155
Mvaa 204,152,80
s_lshl_b32 s67, s14, 9
Ta 146,21,15552
Mvaa 204,156,84
Ta 148,21,20672
K 154,156,157
Mvaa 196,156,68
s_add_u32 s72, s72, s66
Ta 150,21,25792
Mvaa 196,152,64
W 8
Ta 152,22,10464
Ta 154,22,15584
Mvaa 208,128,64
K 155,158,159
Mvaa 208,132,68
s_add_u32 s75, s75, s67
Ta 156,22,20704
Ta 158,22,25824
Mvaa 216,132,84
s_add_u32 s83, s52, 65536
Mvaa 216,128,80
W 8
s_add_u32 s73, s73, s66
Ta 128,21,10496
Mvaa 220,136,80
Ta 130,21,15616
buffer_store_dwordx4 v[152:155], v1, s[8:11], s83 offen sc1
Mvaa 220,140,84
Ta 132,21,20736
Ta 134,21,25856
Mvaa 212,140,68
s_add_u32 s76, s76, s67
K 160,160,161
Mvaa 212,136,64
W 8
Ta 136,22,10528
Ta 138,22,15648
Mvaa 224,144,64
buffer_load_dwordx4 v[36:39], v20, s[16:19], s72 offen
Mvaa 224,148,68
Ta 140,22,20768
K 161,162,163
Mvaa 232,148,84
Ta 142,22,25888
Mvaa 232,144,80
W 8
Ta 144,21,30720
K 162,164,165
Mvaa 236,152,80
Ta 146,21,35840
Mvaa 236,156,84
Ta 148,23,0
Ta 150,23,5120
Mvaa 228,156,68
K 163,166,167
Mvaa 228,152,64
W 8
buffer_load_dwordx4 v[44:47], v14, s[8:11], s75 offen sc1
Ta 152,22,30752
Mvaa 240,128,64
s_add_u32 s83, s52, 32768
Ta 154,22,35872
Mvaa 240,132,68
Ta 156,24,32
Ta 158,24,5152
Mvaa 248,132,84
Mvaa 248,128,80
W 8
buffer_store_dwordx4 v[160:163], v1, s[8:11], s83 offen sc1
Ta 128,21,30784
Mvaa 252,136,80
Ta 130,21,35904
K 168,168,169
Mvaa 252,140,84
Ta 132,23,64
Ta 134,23,5184
Mvaa 244,140,68
Mvaa 244,136,64
s_waitcnt vmcnt(27) lgkmcnt(8)
buffer_load_dwordx4 v[52:55], v20, s[16:19], s73 offen
K 169,170,171
Mvaa 176,144,72
Ta 136,22,30816
Ta 138,22,35936
Mvaa 176,148,76
Ta 140,24,96
K 170,172,173
Mvaa 184,148,92
Ta 142,24,5216
Mvaa 184,144,88
W 8
Ta 144,21,30848
Ta 146,21,35968
Mvaa 188,152,88
buffer_load_dwordx4 v[60:63], v14, s[8:11], s76 offen sc1
K 171,174,175
Mvaa 188,156,92
Ta 148,23,128
Ta 150,23,5248
Mvaa 180,156,76
s_add_u32 s83, s52, 73728
Mvaa 180,152,72
W 8
Ta 152,22,30880
Ta 154,22,36000
Mvaa 192,128,72
buffer_store_dwordx4 v[168:171], v1, s[8:11], s83 offen sc1
Mvaa 192,132,76
Ta 156,24,160
Ta 158,24,5280
Mvaa 200,132,92
Mvaa 200,128,88
W 8
Ta 128,21,30912
Ta 130,21,36032
Mvaa 204,136,88
buffer_load_dwordx4 v[68:71], v20, s[16:19], s74 offen
Mvaa 204,140,92
Ta 132,23,192
Ta 134,23,5312
Mvaa 196,140,76
Mvaa 196,136,72
W 8
Ta 136,22,30944
Ta 138,22,36064
Mvaa 208,144,72
Mvaa 208,148,76
Ta 140,24,224
Ta 142,24,5344
Mvaa 216,148,92
buffer_load_dwordx4 v[76:79], v14, s[8:11], s77 offen sc1
Mvaa 216,144,88
W 8
Ta 144,21,30976
Ta 146,21,36096
Mvaa 220,152,88
Mvaa 220,156,92
Ta 148,23,256
Ta 150,23,5376
Mvaa 212,156,76
Mvaa 212,152,72
W 8
Ta 152,22,31008
Ta 154,22,36128
Mvaa 224,128,72
buffer_load_dwordx2 v[12:13], v27, s[8:11], 0 offen offset:3584 sc1
Mvaa 224,132,76
Ta 156,24,288
Ta 158,24,5408
Mvaa 232,132,92
Mvaa 232,128,88
W 8
Ta 128,23,20480
Ta 130,23,25600
Mvaa 236,136,88
Mvaa 236,140,92
Ta 132,23,51200
Ta 134,23,56320
Mvaa 228,140,76
)"
R"(Mvaa 228,136,72
W 8
Ta 136,25,0
Ta 138,25,5120
Mvaa 240,144,72
Mvaa 240,148,76
Ta 140,21,40960
Ta 142,21,46080
Mvaa 248,148,92
Mvaa 248,144,88
W 8
Ta 144,21,51200
Ta 146,21,56320
Mvaa 252,152,88
Mvaa 252,156,92
Ta 148,23,10240
Ta 150,23,15360
Mvaa 244,156,76
Mvaa 244,152,72
s_waitcnt vmcnt(28) lgkmcnt(8)
Ta 152,23,30720
Ta 154,23,35840
Mvaa 176,128,96
Mvaa 176,132,100
Ta 156,23,40960
Ta 158,23,46080
Mvaa 184,132,116
Mvaa 184,128,112
s_waitcnt vmcnt(22) lgkmcnt(8)
Ta 128,24,20512
Ta 130,24,25632
Mvaa 184,136,120
Mvaa 184,140,124
Ta 132,24,51232
Ta 134,24,56352
Mvaa 176,140,108
Mvaa 176,136,104
s_waitcnt vmcnt(17) lgkmcnt(8)
Ta 136,26,32
Ta 138,26,5152
Mvaa 176,144,184
Mvaa 176,148,188
Ta 140,22,40992
Ta 142,22,46112
Mvaa 184,148,204
Mvaa 184,144,200
s_waitcnt vmcnt(12) lgkmcnt(8)
Ta 144,22,51232
Ta 146,22,56352
Mvaa 184,152,208
Mvaa 184,156,212
Ta 148,24,10272
Ta 150,24,15392
Mvaa 176,156,196
Mvaa 176,152,192
W 8
Ta 152,24,30752
Ta 154,24,35872
Mvaa 180,128,96
Mvaa 180,132,100
Ta 156,24,40992
Ta 158,24,46112
Mvaa 188,132,116
Mvaa 188,128,112
W 8
Ta 128,23,20544
Ta 130,23,25664
Mvaa 188,136,120
Mvaa 188,140,124
Ta 132,23,51264
Ta 134,23,56384
Mvaa 180,140,108
Mvaa 180,136,104
W 8
Ta 136,25,64
Ta 138,25,5184
Mvaa 180,144,184
Mvaa 180,148,188
Ta 140,21,41024
Ta 142,21,46144
Mvaa 188,148,204
Mvaa 188,144,200
W 8
Ta 144,21,51264
Ta 146,21,56384
Mvaa 188,152,208
Mvaa 188,156,212
Ta 148,23,10304
Ta 150,23,15424
Mvaa 180,156,196
Mvaa 180,152,192
W 8
Ta 152,23,30784
Ta 154,23,35904
Mvaa 192,128,96
Mvaa 192,132,100
Ta 156,23,41024
Ta 158,23,46144
Mvaa 200,132,116
K 176,176,177
Mvaa 200,128,112
W 8
Ta 128,24,20576
Ta 130,24,25696
Mvaa 200,136,120
K 177,178,179
Mvaa 200,140,124
Ta 132,24,51296
K 178,180,181
Mvaa 192,140,108
Ta 134,24,56416
K 179,182,183
Mvaa 192,136,104
W 8
Ta 136,26,96
Ta 138,26,5216
Mvaa 192,144,184
s_add_u32 s83, s52, 327680
Mvaa 192,148,188
buffer_store_dwordx4 v[176:179], v1, s[8:11], s83 offen sc1
Ta 140,22,41056
Mvaa 200,148,204
Ta 142,22,46176
Mvaa 200,144,200
s_waitcnt vmcnt(4)
v_lshlrev_b32 v2, 2, v1
v_mov_b64 v[28:29], s[12:13]
s_mov_b64 exec, 3
buffer_store_dwordx2 v[28:29], v2, s[8:11], s53 offen offset:0 sc1
s_mov_b64 exec, -1
K 184,184,185
W 8
Mvaa 200,152,208
K 185,186,187
K 186,188,189
Mvaa 200,156,212
s_waitcnt vmcnt(2)
v_xor_b32 v12, s12, v12
v_xor_b32 v13, s13, v13
v_lshrrev_b32 v3, 11, v12
v_or_b32 v13, v13, v3
v_cmp_eq_u32 vcc, 0, v13
s_cmp_eq_u64 vcc, -1
s_cbranch_scc0 .LL_89%=
.LL_90%=:
v_bfe_u32 v13, v12, 4, 7
s_xor_b32 s66, s61, 124
v_cmp_eq_u32 vcc, s61, v13
v_cmp_eq_u32_e64 s[86:87], s66, v13
s_or_b32 s68, vcc_lo, s86
s_and_b32 s68, s68, 0xff
s_cbranch_scc1 .Lup_87%=
.Lupr_88%=:
Ta 144,22,51296
K 187,190,191
Mvaa 192,156,196
s_add_u32 s83, s52, 368640
buffer_store_dwordx4 v[184:187], v1, s[8:11], s83 offen sc1
Ta 146,22,56416
Mvaa 192,152,192
W 6
Ta 148,24,10336
Ta 150,24,15456
Mvaa 196,128,96
Ta 152,24,30816
v_lshlrev_b32 v32, 16, v36
Mvaa 196,132,100
Ta 154,24,35936
v_and_b32 v33, 0xffff0000, v36
Mvaa 204,132,116
Ta 156,24,41056
v_lshlrev_b32 v34, 16, v37
Mvaa 204,128,112
W 7
Ta 158,24,46176
v_and_b32 v35, 0xffff0000, v37
Mvaa 204,136,120
Ta 128,23,20608
v_lshlrev_b32 v36, 16, v38
Mvaa 204,140,124
Ta 130,23,25728
v_and_b32 v37, 0xffff0000, v38
Mvaa 196,140,108
Ta 132,23,51328
v_lshlrev_b32 v38, 16, v39
Mvaa 196,136,104
W 7
Ta 134,23,56448
v_and_b32 v39, 0xffff0000, v39
Mvaa 196,144,184
Ta 136,25,128
v_lshlrev_b32 v40, 16, v44
Mvaa 196,148,188
Ta 138,25,5248
v_and_b32 v41, 0xffff0000, v44
Mvaa 204,148,204
Ta 140,21,41088
v_lshlrev_b32 v42, 16, v45
Mvaa 204,144,200
W 7
Ta 142,21,46208
v_and_b32 v43, 0xffff0000, v45
Mvaa 204,152,208
Ta 144,21,51328
v_lshlrev_b32 v44, 16, v46
Mvaa 204,156,212
Ta 146,21,56448
v_and_b32 v45, 0xffff0000, v46
Mvaa 196,156,196
Ta 148,23,10368
v_lshlrev_b32 v46, 16, v47
Mvaa 196,152,192
W 7
Ta 150,23,15488
v_and_b32 v47, 0xffff0000, v47
Mvaa 208,128,96
Ta 152,23,30848
v_lshlrev_b32 v48, 16, v52
Mvaa 208,132,100
Ta 154,23,35968
v_and_b32 v49, 0xffff0000, v52
Mvaa 216,132,116
Ta 156,23,41088
v_lshlrev_b32 v50, 16, v53
Mvaa 216,128,112
W 7
Ta 158,23,46208
v_and_b32 v51, 0xffff0000, v53
Mvaa 216,136,120
K 192,192,193
v_lshlrev_b32 v52, 16, v54
Mvaa 216,140,124
Ta 128,24,20640
Ta 130,24,25760
Mvaa 208,140,108
K 193,194,195
v_and_b32 v53, 0xffff0000, v54
Mvaa 208,136,104
W 6
v_lshlrev_b32 v54, 16, v55
K 194,196,197
Mvaa 208,144,184
Ta 132,24,51360
Ta 134,24,56480
Mvaa 208,148,188
v_and_b32 v55, 0xffff0000, v55
K 195,198,199
Mvaa 216,148,204
s_add_u32 s83, s52, 335872
Ta 136,26,160
v_lshlrev_b32 v56, 16, v60
Mvaa 216,144,200
W 5
Ta 138,26,5280
v_and_b32 v57, 0xffff0000, v60
Mvaa 216,152,208
buffer_store_dwordx4 v[192:195], v1, s[8:11], s83 offen sc1
v_lshlrev_b32 v58, 16, v61
Mvaa 216,156,212
K 200,200,201
K 201,202,203
Mvaa 208,156,196
K 202,204,205
K 203,206,207
s_add_u32 s83, s52, 376832
buffer_store_dwordx4 v[200:203], v1, s[8:11], s83 offen sc1
Mvaa 208,152,192
Ta 140,22,41120
Ta 142,22,46240
W 4
Mvaa 212,128,96
v_and_b32 v59, 0xffff0000, v61
v_lshlrev_b32 v60, 16, v62
Mvaa 212,132,100
Ta 144,22,51360
Ta 146,22,56480
Mvaa 220,132,116
v_and_b32 v61, 0xffff0000, v62
v_lshlrev_b32 v62, 16, v63
Mvaa 220,128,112
Ta 148,24,10400
Ta 150,24,15520
W 4
Mvaa 220,136,120
v_and_b32 v63, 0xffff0000, v63
v_lshlrev_b32 v64, 16, v68
Mvaa 220,140,124
Ta 152,24,30880
Ta 154,24,36000
Mvaa 212,140,108
v_and_b32 v65, 0xffff0000, v68
v_lshlrev_b32 v66, 16, v69
Mvaa 212,136,104
Ta 156,24,41120
Ta 158,24,46240
W 4
Mvaa 212,144,184
v_and_b32 v67, 0xffff0000, v69
v_lshlrev_b32 v68, 16, v70
Mvaa 212,148,188
Ta 128,23,20672
Ta 130,23,25792
Mvaa 220,148,204
v_and_b32 v69, 0xffff0000, v70
v_lshlrev_b32 v70, 16, v71
Mvaa 220,144,200
Ta 132,23,51392
Ta 134,23,56512
W 4
Mvaa 220,152,208
v_and_b32 v71, 0xffff0000, v71
v_lshlrev_b32 v72, 16, v76
Mvaa 220,156,212
Ta 136,25,192
Ta 138,25,5312
Mvaa 212,156,196
v_and_b32 v73, 0xffff0000, v76
v_lshlrev_b32 v74, 16, v77
Mvaa 212,152,192
Ta 140,21,41152
Ta 142,21,46272
W 4
Mvaa 224,128,96
v_and_b32 v75, 0xffff0000, v77
v_lshlrev_b32 v76, 16, v78
Mvaa 224,132,100
Ta 144,21,51392
Ta 146,21,56512
Mvaa 232,132,116
v_and_b32 v77, 0xffff0000, v78
v_lshlrev_b32 v78, 16, v79
Mvaa 232,128,112
Ta 148,23,10432
Ta 150,23,15552
W 4
Mvaa 232,136,120
Ta 152,23,30912
v_and_b32 v79, 0xffff0000, v79
Mvaa 232,140,124
Ta 154,23,36032
Ta 156,23,41152
Mvaa 224,140,108
Ta 158,23,46272
K 208,208,209
Mvaa 224,136,104
W 4
Ta 128,24,20704
Ta 130,24,25824
Mvaa 224,144,184
K 209,210,211
K 210,212,213
Mvaa 224,148,188
Ta 132,24,51424
Ta 134,24,56544
Mvaa 232,148,204
Ta 136,26,224
K 211,214,215
Mvaa 232,144,200
W 5
s_add_u32 s83, s52, 344064
Ta 138,26,5344
Mvaa 232,152,208
buffer_store_dwordx4 v[208:211], v1, s[8:11], s83 offen sc1
K 216,216,217
Mvaa 232,156,212
K 217,218,219
K 218,220,221
Mvaa 224,156,196
K 219,222,223
s_add_u32 s83, s52, 385024
buffer_store_dwordx4 v[216:219], v1, s[8:11], s83 offen sc1
Mvaa 224,152,192
Ta 140,22,41184
Ta 142,22,46304
W 4
Mvaa 228,128,96
Ta 144,22,51424
Ta 146,22,56544
Mvaa 228,132,100
Ta 148,24,10464
Ta 150,24,15584
Mvaa 236,132,116
Ta 152,24,30944
Ta 154,24,36064
Mvaa 236,128,112
W 6
Ta 156,24,41184
Ta 158,24,46304
Mvaa 236,136,120
Ta 128,23,20736
Ta 130,23,25856
Mvaa 236,140,124
Ta 132,23,51456
Ta 134,23,56576
Mvaa 228,140,108
Mvaa 228,136,104
W 8
Ta 136,25,256
Ta 138,25,5376
Mvaa 228,144,184
)"
R"(Mvaa 228,148,188
Ta 140,21,41216
Ta 142,21,46336
Mvaa 236,148,204
Mvaa 236,144,200
W 8
Ta 144,21,51456
Ta 146,21,56576
Mvaa 236,152,208
Mvaa 236,156,212
Ta 148,23,10496
Ta 150,23,15616
Mvaa 228,156,196
Mvaa 228,152,192
W 8
Ta 152,23,30976
Ta 154,23,36096
Mvaa 240,128,96
Mvaa 240,132,100
Ta 156,23,41216
Ta 158,23,46336
Mvaa 248,132,116
K 224,224,225
Mvaa 248,128,112
W 8
Ta 128,24,20768
Ta 130,24,25888
Mvaa 248,136,120
K 225,226,227
Mvaa 248,140,124
Ta 132,24,51488
K 226,228,229
Mvaa 240,140,108
Ta 134,24,56608
K 227,230,231
Mvaa 240,136,104
W 8
Ta 136,26,288
Ta 138,26,5408
Mvaa 240,144,184
s_add_u32 s83, s52, 352256
Mvaa 240,148,188
buffer_store_dwordx4 v[224:227], v1, s[8:11], s83 offen sc1
K 232,232,233
Mvaa 248,148,204
K 233,234,235
K 234,236,237
Mvaa 248,144,200
W 6
Ta 140,22,41248
K 235,238,239
Mvaa 248,152,208
s_add_u32 s83, s52, 393216
Ta 142,22,46368
Ta 144,22,51488
Mvaa 248,156,212
buffer_store_dwordx4 v[232:235], v1, s[8:11], s83 offen sc1
Ta 146,22,56608
Mvaa 240,156,196
Ta 148,24,10528
Ta 150,24,15648
Mvaa 240,152,192
W 8
Ta 152,24,31008
Ta 154,24,36128
Mvaa 244,128,96
Mvaa 244,132,100
Ta 156,24,41248
Ta 158,24,46368
Mvaa 252,132,116
Mvaa 252,128,112
W 8
Mvaa 252,136,120
Mvaa 252,140,124
Mvaa 244,140,108
Mvaa 244,136,104
W 4
Mvaa 244,144,184
Mvaa 244,148,188
Mvaa 252,148,204
Mvaa 252,144,200
W 0
Mvaa 252,152,208
Mvaa 252,156,212
Mvaa 244,156,196
Mvaa 244,152,192
K 240,240,241
K 241,242,243
s_nop 5
K 242,244,245
K 243,246,247
s_add_u32 s83, s52, 360448
buffer_store_dwordx4 v[240:243], v1, s[8:11], s83 offen sc1
K 248,248,249
K 249,250,251
K 250,252,253
K 251,254,255
s_add_u32 s83, s52, 401408
buffer_store_dwordx4 v[248:251], v1, s[8:11], s83 offen sc1
s_branch .Ltend_83%=
.p2align 12
.Llate_82%=:
ds_read_b32 v11, v11 offset:59392
ds_read_b64 v[8:9], v10 offset:57344
Rv 176,3,0
Rv 180,3,1024
Rv 184,3,2048
Rv 188,3,3072
Rv 192,3,4096
Rv 196,3,5120
Rv 200,3,6144
Rv 204,3,7168
W 6
Mvvv 32,224,176
s_lshl_b32 s92, s58, 2
s_add_u32 s91, s92, s51
s_add_u32 s95, s91, 7
Mvvv 36,228,176
s_and_b32 s95, s95, 15
s_mul_i32 s95, s95, 40960
s_mul_i32 s71, s51, 20480
Mvvv 40,224,180
s_add_u32 s71, s71, 61440
s_add_u32 m0, s71, s48
s_lshl2_add_u32 s94, s48, s95
Mvvv 44,228,180
W 4
v_cmp_ne_u32 vcc, 0, v11
s_cmp_lg_u64 vcc, 0
s_cbranch_scc1 .Lredo%=
A 4,4,8
Mvvv 48,224,184
v_rcp_f32 v4, v4
Mvvv 52,228,184
Ta 128,25,10240
A 5,5,9
Mvvv 56,224,188
Ta 130,25,15360
D 16,40,94,0
Mvvv 60,228,188
W 4
s_add_u32 s95, s91, 9
s_and_b32 s95, s95, 15
Mvvv 64,224,192
s_mul_i32 s95, s95, 40960
s_mul_i32 s71, s51, 10240
s_add_u32 s71, s71, 112640
Mvvv 68,228,192
v_rcp_f32 v5, v5
Mvvv 72,224,196
s_lshl_b32 s93, s58, 12
s_add_u32 s93, s93, s50
Ta 132,25,20480
Mvvv 76,228,196
Ta 134,25,25600
W 4
Mvvv 80,224,200
U 32,32,4
U 33,33,4
Mvvv 84,228,200
Ta 136,26,10272
U 34,34,4
Mvvv 88,224,204
U 35,35,4
U 36,36,4
U 37,37,4
U 38,38,4
U 39,39,4
K 32,32,33
K 33,34,35
K 34,36,37
K 35,38,39
buffer_store_dwordx4 v[32:35], v1, s[8:11], s93 offen offset:0 sc1
U 40,40,4
U 41,41,4
U 42,42,4
U 43,43,4
U 44,44,4
U 45,45,4
U 46,46,4
U 47,47,4
K 40,40,41
K 41,42,43
K 42,44,45
K 43,46,47
buffer_store_dwordx4 v[40:43], v1, s[8:11], s93 offen offset:1024 sc1
U 64,64,5
U 65,65,5
U 66,66,5
U 67,67,5
U 68,68,5
U 69,69,5
U 70,70,5
U 71,71,5
K 64,64,65
K 65,66,67
K 66,68,69
K 67,70,71
s_add_u32 s67, s93, 131072
buffer_store_dwordx4 v[64:67], v1, s[8:11], s67 offen offset:0 sc1
U 72,72,5
U 73,73,5
U 74,74,5
U 75,75,5
U 76,76,5
U 77,77,5
U 78,78,5
U 79,79,5
K 72,72,73
K 73,74,75
K 74,76,77
K 75,78,79
Mvvv 92,228,204
Ta 138,26,15392
Ta 140,26,20512
Ta 142,26,25632
W 4
Zvav 176,128,32
Ta 144,25,10304
Ta 146,25,15424
Mvav 176,132,40
Ta 148,25,20544
Ta 150,25,25664
Zvav 184,132,72
D 17,40,94,1024
Ta 152,26,10336
Mvav 184,128,64
W 5
Ta 154,26,15456
Ta 156,26,20576
Zvav 188,136,64
Ta 158,26,25696
Ta 128,25,10368
Mvav 188,140,72
Ta 130,25,15488
Ta 132,25,20608
Zvav 180,140,40
D 18,40,94,2048
Ta 134,25,25728
Mvav 180,136,32
W 8
Ta 136,26,10400
Ta 138,26,15520
Zvav 192,144,32
Mvav 192,148,40
Ta 140,26,20640
Ta 142,26,25760
Zvav 200,148,72
D 19,40,94,3072
Mvav 200,144,64
W 8
s_add_u32 m0, m0, 4096
Ta 144,25,10432
Zvav 204,152,64
Ta 146,25,15552
Mvav 204,156,72
Ta 148,25,20672
Ta 150,25,25792
Zvav 196,156,40
buffer_load_dwordx4 v20, s[40:43], s94 offen lds
Mvav 196,152,32
W 8
s_add_u32 m0, s71, s48
s_lshl2_add_u32 s94, s48, s95
Zvav 208,128,32
Ta 152,26,10464
Ta 154,26,15584
Mvav 208,132,40
Ta 156,26,20704
Ta 158,26,25824
Zvav 216,132,72
D 16,40,94,0
v_bfe_u32 v28, v1, 4, 2
Mvav 216,128,64
W 8
Ta 128,25,10496
v_lshlrev_b32 v28, 3, v28
Zvav 220,136,64
Ta 130,25,15616
Mvav 220,140,72
Ta 132,25,20736
Ta 134,25,25856
Zvav 212,140,40
buffer_load_dwordx2 v[28:29], v28, s[8:11], s54 offen sc1
Mvav 212,136,32
W 8
Ta 136,26,10528
Ta 138,26,15648
Zvav 224,144,32
Mvav 224,148,40
Ta 140,26,20768
Ta 142,26,25888
Zvav 232,148,72
D 17,40,94,1024
Mvav 232,144,64
W 8
Ta 144,25,30720
Ta 146,25,35840
Zvav 236,152,64
Mvav 236,156,72
Ta 148,21,0
Ta 150,21,5120
Zvav 228,156,40
D 18,40,94,2048
Mvav 228,152,32
W 8
Ta 152,26,30752
Ta 154,26,35872
Zvav 240,128,32
Mvav 240,132,40
Ta 156,22,32
Ta 158,22,5152
Zvav 248,132,72
D 19,40,94,3072
Mvav 248,128,64
W 8
s_add_u32 m0, m0, 4096
Ta 128,25,30784
Zvav 252,136,64
Ta 130,25,35904
Mvav 252,140,72
Ta 132,21,64
Ta 134,21,5184
Zvav 244,140,40
buffer_store_dwordx4 v[72:75], v1, s[8:11], s67 offen offset:1024 sc1
U 48,48,4
U 49,49,4
U 50,50,4
U 51,51,4
U 52,52,4
U 53,53,4
U 54,54,4
U 55,55,4
K 48,48,49
K 49,50,51
K 50,52,53
K 51,54,55
buffer_store_dwordx4 v[48:51], v1, s[8:11], s93 offen offset:2048 sc1
U 56,56,4
U 57,57,4
U 58,58,4
U 59,59,4
U 60,60,4
U 61,61,4
U 62,62,4
U 63,63,4
K 56,56,57
K 57,58,59
K 58,60,61
K 59,62,63
buffer_store_dwordx4 v[56:59], v1, s[8:11], s93 offen offset:3072 sc1
U 80,80,5
U 81,81,5
U 82,82,5
U 83,83,5
U 84,84,5
U 85,85,5
U 86,86,5
U 87,87,5
K 80,80,81
K 81,82,83
K 82,84,85
K 83,86,87
buffer_store_dwordx4 v[80:83], v1, s[8:11], s67 offen offset:2048 sc1
U 88,88,5
U 89,89,5
U 90,90,5
U 91,91,5
U 92,92,5
U 93,93,5
U 94,94,5
U 95,95,5
K 88,88,89
K 89,90,91
K 90,92,93
K 91,94,95
Mvav 244,136,32
W 8
Ta 136,26,30816
Ta 138,26,35936
Mvav 176,144,48
Mvav 176,148,56
Ta 140,22,96
Ta 142,22,5216
Mvav 184,148,88
buffer_load_dwordx4 v20, s[40:43], s94 offen lds
Mvav 184,144,80
s_waitcnt vmcnt(8)
v_cmp_eq_u64 vcc, s[12:13], v[28:29]
s_cmp_eq_u64 vcc, -1
s_cbranch_scc0 .Lspin_92%=
.Lok_93%=:
s_add_u32 s81, s58, 0
W 8
Mvav 188,152,80
s_and_b32 s81, s81, 3
s_lshl_b32 s81, s81, 12
s_add_u32 s81, s81, s49
Mvav 188,156,88
Ta 144,25,30848
)"
R"(Ta 146,25,35968
Mvav 180,156,56
buffer_load_dwordx4 a[184:187], v1, s[8:11], s81 offen offset:0 sc1
Ta 148,21,128
Mvav 180,152,48
W 7
s_add_u32 s82, s81, 131072
Ta 150,21,5248
Mvav 192,128,48
Ta 152,26,30880
Ta 154,26,36000
Mvav 192,132,56
Ta 156,22,160
Ta 158,22,5280
Mvav 200,132,88
buffer_load_dwordx4 a[200:203], v1, s[8:11], s82 offen offset:0 sc1
Mvav 200,128,80
W 8
Ta 128,25,30912
Ta 130,25,36032
Mvav 204,136,80
Mvav 204,140,88
Ta 132,21,192
Ta 134,21,5312
Mvav 196,140,56
buffer_load_dwordx4 a[188:191], v1, s[8:11], s81 offen offset:1024 sc1
Mvav 196,136,48
W 8
Ta 136,26,30944
Ta 138,26,36064
Mvav 208,144,48
Mvav 208,148,56
Ta 140,22,224
Ta 142,22,5344
Mvav 216,148,88
buffer_load_dwordx4 a[204:207], v1, s[8:11], s82 offen offset:1024 sc1
Mvav 216,144,80
W 8
Ta 144,25,30976
Ta 146,25,36096
Mvav 220,152,80
Mvav 220,156,88
Ta 148,21,256
Ta 150,21,5376
Mvav 212,156,56
buffer_load_dwordx4 a[192:195], v1, s[8:11], s81 offen offset:2048 sc1
Mvav 212,152,48
W 8
Ta 152,26,31008
Ta 154,26,36128
Mvav 224,128,48
Mvav 224,132,56
Ta 156,22,288
Ta 158,22,5408
Mvav 232,132,88
buffer_store_dwordx4 v[88:91], v1, s[8:11], s67 offen offset:3072 sc1
Mvav 232,128,80
W 8
Ta 128,25,10240
Ta 130,25,15360
Mvav 236,136,80
Mvav 236,140,88
Ta 132,25,20480
Ta 134,25,25600
Mvav 228,140,56
buffer_load_dwordx4 a[208:211], v1, s[8:11], s82 offen offset:2048 sc1
Mvav 228,136,48
W 8
Ta 136,26,10272
Ta 138,26,15392
Mvav 240,144,48
Mvav 240,148,56
Ta 140,26,20512
Ta 142,26,25632
Mvav 248,148,88
buffer_load_dwordx4 a[196:199], v1, s[8:11], s81 offen offset:3072 sc1
Mvav 248,144,80
W 8
Ta 144,25,10304
Ta 146,25,15424
Mvav 252,152,80
Mvav 252,156,88
Ta 148,25,20544
Ta 150,25,25664
Mvav 244,156,56
buffer_load_dwordx4 a[212:215], v1, s[8:11], s82 offen offset:3072 sc1
Mvav 244,152,48
s_waitcnt vmcnt(5) lgkmcnt(8)
Ta 152,26,10336
Ta 154,26,15456
Zvaa 96,128,184
Mvaa 96,132,188
Ta 156,26,20576
Ta 158,26,25696
Zvaa 104,132,204
Mvaa 104,128,200
W 8
Ta 128,25,10368
Ta 130,25,15488
Zvaa 108,136,200
Mvaa 108,140,204
Ta 132,25,20608
Ta 134,25,25728
Zvaa 100,140,188
s_lshl_b32 s92, s58, 2
s_add_u32 s91, s92, s51
s_add_u32 s95, s91, 11
Mvaa 100,136,184
W 8
s_and_b32 s95, s95, 15
s_mul_i32 s95, s95, 40960
Zvaa 112,144,184
s_mul_i32 s71, s51, 10240
s_add_u32 s71, s71, 40960
s_add_u32 m0, s71, s48
Mvaa 112,148,188
s_lshl2_add_u32 s94, s48, s95
Ta 136,26,10400
Ta 138,26,15520
Zvaa 120,148,204
Ta 140,26,20640
Ta 142,26,25760
Mvaa 120,144,200
W 8
Ta 144,25,10432
Ta 146,25,15552
Zvaa 124,152,200
Mvaa 124,156,204
Ta 148,25,20672
Ta 150,25,25792
Zvaa 116,156,188
Mvaa 116,152,184
W 8
Ta 152,26,10464
Ta 154,26,15584
Zvaa 128,128,184
s_lshl_b32 s80, s58, 3
s_add_u32 s80, s80, s55
s_waitcnt vmcnt(3)
v_mov_b64 v[28:29], s[12:13]
s_mov_b64 exec, 1
buffer_store_dwordx2 v[28:29], off, s[8:11], s80 sc1
s_mov_b64 exec, -1
Mvaa 128,132,188
Ta 156,26,20704
Ta 158,26,25824
Zvaa 136,132,204
Mvaa 136,128,200
W 8
Ta 128,25,10496
Ta 130,25,15616
Zvaa 140,136,200
s_barrier
D 16,40,94,0
Mvaa 140,140,204
s_add_u32 s95, s91, 13
s_and_b32 s95, s95, 15
s_mul_i32 s95, s95, 40960
Zvaa 132,140,188
s_mul_i32 s71, s51, 20480
s_add_u32 s71, s71, 71680
Ta 132,25,20736
Mvaa 132,136,184
W 7
Ta 134,25,25856
Ta 136,26,10528
Zvaa 144,144,184
Ta 138,26,15648
D 17,40,94,1024
Mvaa 144,148,188
Ta 140,26,20768
Ta 142,26,25888
Zvaa 152,148,204
Mvaa 152,144,200
W 8
Ta 144,25,30720
Ta 146,25,35840
Zvaa 156,152,200
D 18,40,94,2048
Mvaa 156,156,204
Ta 148,21,0
Ta 150,21,5120
Zvaa 148,156,188
Mvaa 148,152,184
W 8
Ta 152,26,30752
Ta 154,26,35872
Zvaa 160,128,184
D 19,40,94,3072
Mvaa 160,132,188
s_add_u32 m0, m0, 4096
Ta 156,22,32
Ta 158,22,5152
Zvaa 168,132,204
Mvaa 168,128,200
W 8
Ta 128,25,30784
Ta 130,25,35904
Zvaa 172,136,200
buffer_load_dwordx4 v20, s[40:43], s94 offen lds
Mvaa 172,140,204
s_add_u32 s81, s58, 1
s_and_b32 s81, s81, 3
s_lshl_b32 s81, s81, 12
Zvaa 164,140,188
s_add_u32 s81, s81, s49
Ta 132,21,64
Ta 134,21,5184
Mvaa 164,136,184
s_waitcnt vmcnt(6) lgkmcnt(8)
Ta 136,26,30816
Ta 138,26,35936
Mvaa 96,144,192
buffer_load_dwordx4 a[216:219], v1, s[8:11], s81 offen offset:0 sc1
Mvaa 96,148,196
s_add_u32 s82, s81, 131072
Ta 140,22,96
Ta 142,22,5216
Mvaa 104,148,212
Mvaa 104,144,208
W 8
Ta 144,25,30848
Ta 146,25,35968
Mvaa 108,152,208
buffer_load_dwordx4 a[232:235], v1, s[8:11], s82 offen offset:0 sc1
Mvaa 108,156,212
Ta 148,21,128
Ta 150,21,5248
Mvaa 100,156,196
Mvaa 100,152,192
W 8
Ta 152,26,30880
Ta 154,26,36000
Mvaa 112,128,192
buffer_load_dwordx4 a[220:223], v1, s[8:11], s81 offen offset:1024 sc1
Mvaa 112,132,196
Ta 156,22,160
Ta 158,22,5280
Mvaa 120,132,212
Mvaa 120,128,208
W 8
Ta 128,25,30912
Ta 130,25,36032
Mvaa 124,136,208
buffer_load_dwordx4 a[236:239], v1, s[8:11], s82 offen offset:1024 sc1
Mvaa 124,140,212
Ta 132,21,192
Ta 134,21,5312
Mvaa 116,140,196
Mvaa 116,136,192
W 8
Ta 136,26,30944
Ta 138,26,36064
Mvaa 128,144,192
buffer_load_dwordx4 a[224:227], v1, s[8:11], s81 offen offset:2048 sc1
Mvaa 128,148,196
Ta 140,22,224
Ta 142,22,5344
Mvaa 136,148,212
Mvaa 136,144,208
W 8
Ta 144,25,30976
Ta 146,25,36096
Mvaa 140,152,208
buffer_load_dwordx4 a[240:243], v1, s[8:11], s82 offen offset:2048 sc1
Mvaa 140,156,212
Ta 148,21,256
Ta 150,21,5376
Mvaa 132,156,196
Mvaa 132,152,192
W 8
Ta 152,26,31008
Ta 154,26,36128
Mvaa 144,128,192
buffer_load_dwordx4 a[228:231], v1, s[8:11], s81 offen offset:3072 sc1
Mvaa 144,132,196
Ta 156,22,288
Ta 158,22,5408
Mvaa 152,132,212
Mvaa 152,128,208
W 8
Ta 128,21,10240
Ta 130,21,15360
Mvaa 156,136,208
buffer_load_dwordx4 a[244:247], v1, s[8:11], s82 offen offset:3072 sc1
Mvaa 156,140,212
s_add_u32 m0, s71, s48
s_lshl2_add_u32 s94, s48, s95
Ta 132,21,20480
Mvaa 148,140,196
Ta 134,21,25600
Mvaa 148,136,192
W 8
Ta 136,22,10272
Ta 138,22,15392
Mvaa 160,144,192
D 16,40,94,0
Mvaa 160,148,196
s_add_u32 s95, s92, 15
s_and_b32 s95, s95, 15
s_mul_i32 s95, s95, 40960
Mvaa 168,148,212
s_mul_i32 s71, s51, 0
s_add_u32 s71, s71, 102400
Ta 140,22,20512
Mvaa 168,144,208
W 7
Ta 142,22,25632
Ta 144,21,10304
Mvaa 172,152,208
D 17,40,94,1024
Ta 146,21,15424
Mvaa 172,156,212
Ta 148,21,20544
Ta 150,21,25664
Mvaa 164,156,196
Mvaa 164,152,192
s_waitcnt vmcnt(6) lgkmcnt(8)
Ta 152,22,10336
Ta 154,22,15456
Mvaa 96,128,216
D 18,40,94,2048
Mvaa 96,132,220
Ta 156,22,20576
Ta 158,22,25696
Mvaa 104,132,236
Mvaa 104,128,232
W 8
Ta 128,21,10368
Ta 130,21,15488
Mvaa 108,136,232
D 19,40,94,3072
Mvaa 108,140,236
s_add_u32 m0, m0, 4096
Ta 132,21,20608
Ta 134,21,25728
Mvaa 100,140,220
Mvaa 100,136,216
W 8
Ta 136,22,10400
Ta 138,22,15520
Mvaa 112,144,216
buffer_load_dwordx4 v20, s[40:43], s94 offen lds
Mvaa 112,148,220
s_add_u32 m0, s71, s48
s_lshl2_add_u32 s94, s48, s95
Ta 140,22,20640
Mvaa 120,148,236
Ta 142,22,25760
Mvaa 120,144,232
W 8
Ta 144,21,10432
Ta 146,21,15552
Mvaa 124,152,232
D 16,40,94,0
Mvaa 124,156,236
Ta 148,21,20672
Ta 150,21,25792
Mvaa 116,156,220
D 17,40,94,1024
D 18,40,94,2048
D 19,40,94,3072
s_add_u32 m0, m0, 4096
s_nop 0
buffer_load_dwordx4 v20, s[40:43], s94 offen lds
Mvaa 116,152,216
W 8
s_add_u32 s81, s58, 2
s_and_b32 s81, s81, 3
Mvaa 128,128,216
s_lshl_b32 s81, s81, 12
s_add_u32 s81, s81, s49
Ta 152,22,10464
Mvaa 128,132,220
Ta 154,22,15584
Ta 156,22,20704
Mvaa 136,132,236
Ta 158,22,25824
Mvaa 136,128,232
W 8
Ta 128,21,10496
Ta 130,21,15616
Mvaa 140,136,232
s_barrier
buffer_load_dwordx4 a[0:3], v1, s[8:11], s81 offen offset:0 sc1
v_and_b32 v27, 1, v12
Mvaa 140,140,236
s_add_u32 s82, s81, 131072
Ta 132,21,20736
Ta 134,21,25856
Mvaa 132,140,220
Mvaa 132,136,216
W 8
Ta 136,22,10528
v_lshlrev_b32 v27, 1, v27
Mvaa 144,144,216
Ta 138,22,15648
buffer_load_dwordx4 a[16:19], v1, s[8:11], s82 offen offset:0 sc1
Mvaa 144,148,220
Ta 140,22,20768
)"
R"(Ta 142,22,25888
Mvaa 152,148,236
v_xor_b32 v27, v27, v13
Mvaa 152,144,232
W 8
Ta 144,21,30720
Ta 146,21,35840
Mvaa 156,152,232
buffer_load_dwordx4 a[4:7], v1, s[8:11], s81 offen offset:1024 sc1
Mvaa 156,156,236
Ta 148,23,0
v_and_b32 v12, 2, v12
Mvaa 148,156,220
Ta 150,23,5120
Mvaa 148,152,216
W 8
Ta 152,22,30752
Ta 154,22,35872
Mvaa 160,128,216
buffer_load_dwordx4 a[20:23], v1, s[8:11], s82 offen offset:1024 sc1
v_or_b32 v12, v12, v14
Mvaa 160,132,220
Ta 156,24,32
Ta 158,24,5152
Mvaa 168,132,236
v_lshl_or_b32 v12, v27, 2, v12
Mvaa 168,128,232
W 8
Ta 128,21,30784
Ta 130,21,35904
Mvaa 172,136,232
buffer_load_dwordx4 a[8:11], v1, s[8:11], s81 offen offset:2048 sc1
Mvaa 172,140,236
Ta 132,23,64
v_cmp_eq_u32_e64 s[86:87], 0, v12
Mvaa 164,140,220
Ta 134,23,5184
Mvaa 164,136,216
s_waitcnt vmcnt(15) lgkmcnt(8)
Ta 136,22,30816
Ta 138,22,35936
Mvaa 96,144,224
buffer_load_dwordx4 a[24:27], v1, s[8:11], s82 offen offset:2048 sc1
v_cndmask_b32_e64 v4, 0, v15, s[86:87]
Mvaa 96,148,228
Ta 140,24,96
Ta 142,24,5216
Mvaa 104,148,244
Mvaa 104,144,240
W 8
Ta 144,21,30848
v_cmp_eq_u32_e64 s[86:87], 1, v12
Mvaa 108,152,240
Ta 146,21,35968
buffer_load_dwordx4 a[12:15], v1, s[8:11], s81 offen offset:3072 sc1
Mvaa 108,156,244
Ta 148,23,128
v_cndmask_b32_e64 v5, 0, v15, s[86:87]
Mvaa 100,156,228
Ta 150,23,5248
Mvaa 100,152,224
W 8
Ta 152,22,30880
Ta 154,22,36000
Mvaa 112,128,224
buffer_load_dwordx4 a[28:31], v1, s[8:11], s82 offen offset:3072 sc1
v_cmp_eq_u32_e64 s[86:87], 2, v12
Mvaa 112,132,228
s_add_u32 s81, s58, 3
s_and_b32 s81, s81, 3
s_lshl_b32 s81, s81, 12
Mvaa 120,132,244
s_add_u32 s81, s81, s49
Ta 156,24,160
Ta 158,24,5280
Mvaa 120,128,240
W 8
Ta 128,21,30912
v_cndmask_b32_e64 v6, 0, v15, s[86:87]
Mvaa 124,136,240
Ta 130,21,36032
buffer_load_dwordx4 a[32:35], v1, s[8:11], s81 offen offset:0 sc1
Mvaa 124,140,244
s_add_u32 s82, s81, 131072
Ta 132,23,192
Ta 134,23,5312
Mvaa 116,140,228
v_cmp_eq_u32_e64 s[86:87], 3, v12
Mvaa 116,136,224
W 8
Ta 136,22,30944
Ta 138,22,36064
Mvaa 128,144,224
buffer_load_dwordx4 a[48:51], v1, s[8:11], s82 offen offset:0 sc1
v_cndmask_b32_e64 v7, 0, v15, s[86:87]
Mvaa 128,148,228
Ta 140,24,224
Ta 142,24,5344
Mvaa 136,148,244
Mvaa 136,144,240
W 8
Ta 144,21,30976
v_cmp_eq_u32_e64 s[86:87], 4, v12
Mvaa 140,152,240
s_waitcnt vmcnt(10)
s_barrier
Ta 146,21,36096
Mvaa 140,156,244
buffer_load_dwordx4 a[36:39], v1, s[8:11], s81 offen offset:1024 sc1
Ta 148,23,256
Mvaa 132,156,228
Ta 150,23,5376
v_cndmask_b32_e64 v8, 0, v15, s[86:87]
Mvaa 132,152,224
W 8
Ta 152,22,31008
Ta 154,22,36128
Mvaa 144,128,224
Mvaa 144,132,228
buffer_load_dwordx4 a[52:55], v1, s[8:11], s82 offen offset:1024 sc1
v_cmp_eq_u32_e64 s[86:87], 5, v12
Mvaa 152,132,244
Ta 156,24,288
Ta 158,24,5408
Mvaa 152,128,240
W 8
Ta 128,23,20480
Ta 130,23,25600
Mvaa 156,136,240
v_cndmask_b32_e64 v9, 0, v15, s[86:87]
Mvaa 156,140,244
buffer_load_dwordx4 a[40:43], v1, s[8:11], s81 offen offset:2048 sc1
Ta 132,23,51200
Mvaa 148,140,228
Ta 134,23,56320
v_cmp_eq_u32_e64 s[86:87], 6, v12
Mvaa 148,136,224
W 8
Ta 136,24,20512
Ta 138,24,25632
Mvaa 160,144,224
Mvaa 160,148,228
buffer_load_dwordx4 a[56:59], v1, s[8:11], s82 offen offset:2048 sc1
v_cndmask_b32_e64 v10, 0, v15, s[86:87]
Mvaa 168,148,244
Ta 140,24,51232
Ta 142,24,56352
Mvaa 168,144,240
W 8
Ta 144,23,20544
Ta 146,23,25664
Mvaa 172,152,240
v_cmp_eq_u32_e64 s[86:87], 7, v12
Mvaa 172,156,244
buffer_load_dwordx4 a[44:47], v1, s[8:11], s81 offen offset:3072 sc1
Ta 148,23,51264
Mvaa 164,156,228
Ta 150,23,56384
Mvaa 164,152,224
s_waitcnt vmcnt(11) lgkmcnt(8)
Ta 152,24,20576
v_cndmask_b32_e64 v11, 0, v15, s[86:87]
Mvaa 96,128,0
Ta 154,24,25696
Mvaa 96,132,4
s_mov_b32 s20, s38
buffer_load_dwordx4 a[60:63], v1, s[8:11], s82 offen offset:3072 sc1
Ta 156,24,51296
Mvaa 104,132,20
Ta 158,24,56416
Mvaa 104,128,16
W 8
Ta 128,23,20608
Ta 130,23,25728
Mvaa 108,136,16
s_and_b32 s21, s39, 0xffff
Mvaa 108,140,20
Ta 132,23,51328
Ta 134,23,56448
Mvaa 100,140,4
Mvaa 100,136,0
W 8
s_mov_b32 s22, 2621440
Ta 136,24,20640
Mvaa 112,144,0
Ta 138,24,25760
v_bfe_u32 v28, v1, 4, 2
Mvaa 112,148,4
Ta 140,24,51360
v_lshlrev_b32 v28, 3, v28
Mvaa 120,148,20
s_mov_b32 s23, 0x20000
Ta 142,24,56480
buffer_load_dwordx2 v[28:29], v28, s[8:11], s55 offen sc1
Mvaa 120,144,16
W 8
Ta 144,23,20672
Ta 146,23,25792
Mvaa 124,152,16
s_and_b32 s17, s17, 0xffff
Mvaa 124,156,20
Ta 148,23,51392
Ta 150,23,56512
Mvaa 116,156,4
Mvaa 116,152,0
W 8
s_mov_b32 s18, 2621440
Ta 152,24,20704
Mvaa 128,128,0
Ta 154,24,25824
Mvaa 128,132,4
Ta 156,24,51424
Ta 158,24,56544
Mvaa 136,132,20
s_mov_b32 s19, 0x20000
Mvaa 136,128,16
W 8
Ta 128,23,20736
Ta 130,23,25856
Mvaa 140,136,16
Mvaa 140,140,20
s_lshr_b32 s66, s15, 3
Ta 132,23,51456
Ta 134,23,56576
Mvaa 132,140,4
Mvaa 132,136,0
W 8
s_lshl_b32 s66, s66, 4
Ta 136,24,20768
Mvaa 144,144,0
Ta 138,24,25888
Mvaa 144,148,4
Ta 140,24,51488
Ta 142,24,56608
Mvaa 152,148,20
s_add_u32 s66, s66, s59
Mvaa 152,144,16
W 8
Ta 144,25,0
Ta 146,25,5120
Mvaa 156,152,16
Mvaa 156,156,20
s_lshl_b32 s63, s66, 6
Ta 148,21,40960
Ta 150,21,46080
Mvaa 148,156,4
Mvaa 148,152,0
W 8
Ta 152,26,32
Ta 154,26,5152
Mvaa 160,128,0
s_add_u32 s63, s63, 65536
Mvaa 160,132,4
Ta 156,22,40992
Ta 158,22,46112
Mvaa 168,132,20
Mvaa 168,128,16
W 8
s_mul_i32 s62, s66, 40960
Ta 128,25,64
Mvaa 172,136,16
Ta 130,25,5184
Mvaa 172,140,20
s_add_u32 s62, s62, 56623104
Ta 132,21,41024
Ta 134,21,46144
Mvaa 164,140,4
Mvaa 164,136,0
s_waitcnt vmcnt(9) lgkmcnt(8)
Ta 136,26,96
Ta 138,26,5216
Mvaa 96,144,8
s_lshl_b32 s66, s14, 13
Mvaa 96,148,12
Ta 140,22,41056
Ta 142,22,46176
Mvaa 104,148,28
Mvaa 104,144,24
W 8
s_add_u32 s78, s62, s66
Ta 144,25,128
Mvaa 108,152,24
Ta 146,25,5248
Mvaa 108,156,28
Ta 148,21,41088
Ta 150,21,46208
Mvaa 100,156,12
s_add_u32 s79, s78, 327680
Mvaa 100,152,8
W 8
Ta 152,26,160
Ta 154,26,5280
Mvaa 112,128,8
s_and_b32 s66, s14, 1
Mvaa 112,132,12
Ta 156,22,41120
Ta 158,22,46240
Mvaa 120,132,28
s_waitcnt vmcnt(0)
v_cmp_eq_u64 vcc, s[12:13], v[28:29]
s_cmp_eq_u64 vcc, -1
s_cbranch_scc0 .Lspin_95%=
.Lok_96%=:
s_add_u32 s81, s58, 1
s_and_b32 s81, s81, 3
Mvaa 120,128,24
W 8
s_lshl_b32 s81, s81, 12
s_add_u32 s81, s81, s50
Mvaa 124,136,24
s_mul_i32 s66, s66, 327680
buffer_load_dwordx4 a[64:67], v1, s[8:11], s81 offen offset:0 sc1
Ta 128,25,192
Mvaa 124,140,28
s_add_u32 s82, s81, 131072
Ta 130,25,5312
Ta 132,21,41152
Mvaa 116,140,12
s_add_u32 s65, s62, s66
Ta 134,21,46272
Mvaa 116,136,8
W 8
Ta 136,26,224
Ta 138,26,5344
Mvaa 128,144,8
buffer_load_dwordx4 a[80:83], v1, s[8:11], s82 offen offset:0 sc1
Mvaa 128,148,12
s_add_u32 s65, s65, 32768
Ta 140,22,41184
Ta 142,22,46304
Mvaa 136,148,28
Mvaa 136,144,24
W 8
s_mov_b32 s64, 0
Ta 144,25,256
Mvaa 140,152,24
Ta 146,25,5376
Mvaa 140,156,28
buffer_load_dwordx4 a[68:71], v1, s[8:11], s81 offen offset:1024 sc1
Ta 148,21,41216
Mvaa 132,156,12
Ta 150,21,46336
v_add_u32 v19, 4096, v1
Mvaa 132,152,8
W 8
Ta 152,26,288
Ta 154,26,5408
Mvaa 144,128,8
Mvaa 144,132,12
Ta 156,22,41248
v_bfe_u32 v27, v1, 4, 3
Mvaa 152,132,28
Ta 158,22,46368
buffer_load_dwordx4 a[84:87], v1, s[8:11], s82 offen offset:1024 sc1
Mvaa 152,128,24
W 8
Ta 128,21,51200
Ta 130,21,56320
Mvaa 156,136,24
v_lshlrev_b32 v27, 3, v27
Mvaa 156,140,28
Ta 132,23,10240
Ta 134,23,15360
Mvaa 148,140,12
v_bfe_u32 v18, v1, 7, 1
Mvaa 148,136,8
W 8
buffer_load_dwordx4 a[72:75], v1, s[8:11], s81 offen offset:2048 sc1
Ta 136,22,51232
Mvaa 160,144,8
Ta 138,22,56352
Mvaa 160,148,12
Ta 140,24,10272
v_lshl_add_u32 v18, v18, 9, v27
Mvaa 168,148,28
Ta 142,24,15392
Mvaa 168,144,24
W 8
Ta 144,21,51264
Ta 146,21,56384
Mvaa 172,152,24
s_mov_b32 s71, 0
buffer_load_dwordx4 a[88:91], v1, s[8:11], s82 offen offset:2048 sc1
Mvaa 172,156,28
Ta 148,23,10304
Ta 150,23,15424
Mvaa 164,156,12
Mvaa 164,152,8
W 8
Ta 152,22,51296
v_bfe_u32 v20, v1, 4, 4
Mvaa 96,128,32
Ta 154,22,56416
Mvaa 96,132,36
buffer_load_dwordx4 a[76:79], v1, s[8:11], s81 offen offset:3072 sc1
Ta 156,24,10336
Mvaa 104,132,52
Ta 158,24,15456
v_mul_u32_u24 v20, 1280, v20
Mvaa 104,128,48
)"
R"(W 8
Ta 128,21,51328
Ta 130,21,56448
Mvaa 108,136,48
v_lshrrev_b32 v12, 8, v1
Mvaa 108,140,52
Ta 132,23,10368
Ta 134,23,15488
Mvaa 100,140,36
buffer_load_dwordx4 a[92:95], v1, s[8:11], s82 offen offset:3072 sc1
Mvaa 100,136,32
W 8
s_add_u32 s81, s58, 2
s_and_b32 s81, s81, 3
Mvaa 112,144,32
s_lshl_b32 s81, s81, 12
s_add_u32 s81, s81, s50
v_lshl_add_u32 v20, v12, 4, v20
Mvaa 112,148,36
Ta 136,22,51360
Ta 138,22,56480
Mvaa 120,148,52
s_bfe_u32 s66, s59, 0x10001
Ta 140,24,10400
Ta 142,24,15520
Mvaa 120,144,48
W 8
buffer_load_dwordx4 a[96:99], v1, s[8:11], s81 offen offset:0 sc1
Ta 144,21,51392
Mvaa 124,152,48
s_add_u32 s82, s81, 131072
Ta 146,21,56512
Mvaa 124,156,52
s_lshl_b32 s67, s57, 1
Ta 148,23,10432
Ta 150,23,15552
Mvaa 116,156,36
Mvaa 116,152,32
W 8
s_add_u32 s66, s66, s67
Ta 152,22,51424
Mvaa 128,128,32
Ta 154,22,56544
buffer_load_dwordx4 a[112:115], v1, s[8:11], s82 offen offset:0 sc1
Mvaa 128,132,36
Ta 156,24,10464
Ta 158,24,15584
Mvaa 136,132,52
s_lshl_b32 s61, s66, 2
Mvaa 136,128,48
W 8
Ta 128,21,51456
Ta 130,21,56576
Mvaa 140,136,48
Mvaa 140,140,52
s_lshr_b32 s66, s59, 2
buffer_load_dwordx4 a[100:103], v1, s[8:11], s81 offen offset:1024 sc1
Ta 132,23,10496
Mvaa 132,140,36
Ta 134,23,15616
Mvaa 132,136,32
W 8
Ta 136,22,51488
Ta 138,22,56608
Mvaa 144,144,32
s_lshl_b32 s66, s66, 1
Mvaa 144,148,36
Ta 140,24,10528
Ta 142,24,15648
Mvaa 152,148,52
s_add_u32 s61, s61, s66
buffer_load_dwordx4 a[116:119], v1, s[8:11], s82 offen offset:1024 sc1
Mvaa 152,144,48
W 8
Ta 144,23,30720
Ta 146,23,35840
Mvaa 156,152,48
Mvaa 156,156,52
s_and_b32 s66, s59, 1
Ta 148,23,40960
Ta 150,23,46080
Mvaa 148,156,36
Mvaa 148,152,32
W 8
buffer_load_dwordx4 a[104:107], v1, s[8:11], s81 offen offset:2048 sc1
Ta 152,24,30752
Mvaa 160,128,32
s_add_u32 s61, s61, s66
Ta 154,24,35872
Mvaa 160,132,36
Ta 156,24,40992
Ta 158,24,46112
Mvaa 168,132,52
Mvaa 168,128,48
W 8
s_xor_b32 s67, s61, 0
Ta 128,23,30784
Mvaa 172,136,48
Ta 130,23,35904
buffer_load_dwordx4 a[120:123], v1, s[8:11], s82 offen offset:2048 sc1
Mvaa 172,140,52
Ta 132,23,41024
Ta 134,23,46144
Mvaa 164,140,36
s_mul_i32 s72, s67, 20480
Mvaa 164,136,32
W 8
Ta 136,24,30816
Ta 138,24,35936
Mvaa 96,144,40
s_mul_i32 s66, s58, 320
Mvaa 96,148,44
buffer_load_dwordx4 a[108:111], v1, s[8:11], s81 offen offset:3072 sc1
Ta 140,24,41056
Mvaa 104,148,60
Ta 142,24,46176
Mvaa 104,144,56
W 8
s_add_u32 s72, s72, s66
Ta 144,23,30848
Mvaa 108,152,56
Ta 146,23,35968
Mvaa 108,156,60
Ta 148,23,41088
Ta 150,23,46208
Mvaa 100,156,44
s_mul_i32 s75, s67, 20480
buffer_load_dwordx4 a[124:127], v1, s[8:11], s82 offen offset:3072 sc1
Mvaa 100,152,40
W 8
s_add_u32 s81, s58, 3
s_and_b32 s81, s81, 3
Mvaa 112,128,40
s_lshl_b32 s81, s81, 12
s_add_u32 s81, s81, s50
Ta 152,24,30880
Mvaa 112,132,44
s_mul_i32 s66, s58, 2560
Ta 154,24,36000
Ta 156,24,41120
Mvaa 120,132,60
Ta 158,24,46240
buffer_load_dwordx4 a[184:187], v1, s[8:11], s81 offen offset:0 sc1
Mvaa 120,128,56
W 8
s_add_u32 s82, s81, 131072
s_add_u32 s75, s75, s66
Mvaa 124,136,56
Ta 128,23,30912
Ta 130,23,36032
Mvaa 124,140,60
Ta 132,23,41152
Ta 134,23,46272
Mvaa 116,140,44
s_add_u32 s75, s75, 131072
Mvaa 116,136,40
W 8
Ta 136,24,30944
Ta 138,24,36064
Mvaa 128,144,40
buffer_load_dwordx4 a[200:203], v1, s[8:11], s82 offen offset:0 sc1
Mvaa 128,148,44
s_xor_b32 s67, s61, 124
Ta 140,24,41184
Ta 142,24,46304
Mvaa 136,148,60
Mvaa 136,144,56
W 8
Ta 144,23,30976
Ta 146,23,36096
Mvaa 140,152,56
s_mul_i32 s73, s67, 20480
Mvaa 140,156,60
buffer_load_dwordx4 a[188:191], v1, s[8:11], s81 offen offset:1024 sc1
Ta 148,23,41216
Mvaa 132,156,44
s_mul_i32 s66, s58, 320
Ta 150,23,46336
Mvaa 132,152,40
W 8
Ta 152,24,31008
Ta 154,24,36128
Mvaa 144,128,40
Mvaa 144,132,44
s_add_u32 s73, s73, s66
Ta 156,24,41248
Ta 158,24,46368
Mvaa 152,132,60
buffer_load_dwordx4 a[204:207], v1, s[8:11], s82 offen offset:1024 sc1
Mvaa 152,128,56
W 8
Ta 128,21,10240
Ta 130,21,15360
Mvaa 156,136,56
s_mul_i32 s76, s67, 20480
Mvaa 156,140,60
Ta 132,21,20480
Ta 134,21,25600
Mvaa 148,140,44
K 96,96,97
Mvaa 148,136,40
W 8
s_mul_i32 s66, s58, 2560
buffer_load_dwordx4 a[192:195], v1, s[8:11], s81 offen offset:2048 sc1
Mvaa 160,144,40
Ta 136,22,10272
Ta 138,22,15392
Mvaa 160,148,44
s_add_u32 s76, s76, s66
Ta 140,22,20512
K 97,98,99
Mvaa 168,148,60
Ta 142,22,25632
K 98,100,101
Mvaa 168,144,56
W 8
Ta 144,21,10304
Ta 146,21,15424
Mvaa 172,152,56
s_add_u32 s76, s76, 131072
buffer_load_dwordx4 a[208:211], v1, s[8:11], s82 offen offset:2048 sc1
K 99,102,103
Mvaa 172,156,60
s_add_u32 s83, s52, 0
Ta 148,21,20544
Ta 150,21,25664
Mvaa 164,156,44
Mvaa 164,152,40
s_waitcnt vmcnt(18) lgkmcnt(8)
Ta 152,22,10336
v_bfe_u32 v15, v1, 7, 1
Mvaa 176,128,64
buffer_store_dwordx4 v[96:99], v1, s[8:11], s83 offen sc1
Ta 154,22,15456
Mvaa 176,132,68
Ta 156,22,20576
K 104,104,105
Mvaa 184,132,84
Ta 158,22,25696
v_mul_u32_u24 v14, 10240, v15
Mvaa 184,128,80
W 8
Ta 128,21,10368
K 105,106,107
Mvaa 188,136,80
buffer_load_dwordx4 a[196:199], v1, s[8:11], s81 offen offset:3072 sc1
Ta 130,21,15488
Mvaa 188,140,84
K 106,108,109
v_lshrrev_b32 v15, 8, v1
Mvaa 180,140,68
Ta 132,21,20608
Ta 134,21,25728
Mvaa 180,136,64
W 8
K 107,110,111
v_lshl_add_u32 v14, v15, 7, v14
Mvaa 192,144,64
s_add_u32 s83, s52, 40960
buffer_load_dwordx4 a[212:215], v1, s[8:11], s82 offen offset:3072 sc1
Ta 136,22,10400
Mvaa 192,148,68
Ta 138,22,15520
Ta 140,22,20640
Mvaa 200,148,84
Ta 142,22,25760
v_and_b32 v15, 0x70, v1
Mvaa 200,144,80
W 8
Ta 144,21,10432
Ta 146,21,15552
Mvaa 204,152,80
buffer_store_dwordx4 v[104:107], v1, s[8:11], s83 offen sc1
K 112,112,113
Mvaa 204,156,84
K 113,114,115
v_add_u32 v14, v14, v15
Mvaa 196,156,68
Ta 148,21,20672
Ta 150,21,25792
Mvaa 196,152,64
W 8
Ta 152,22,10464
K 114,116,117
Mvaa 208,128,64
s_bitcmp1_b32 s14, 0
s_cselect_b32 s74, s73, s72
s_cselect_b32 s77, s76, s75
Ta 154,22,15584
K 115,118,119
Mvaa 208,132,68
s_add_u32 s83, s52, 8192
Ta 156,22,20704
Ta 158,22,25824
Mvaa 216,132,84
s_add_u32 s74, s74, 256
Mvaa 216,128,80
W 8
buffer_store_dwordx4 v[112:115], v1, s[8:11], s83 offen sc1
Ta 128,21,10496
Mvaa 220,136,80
Ta 130,21,15616
K 120,120,121
Mvaa 220,140,84
s_add_u32 s77, s77, 2048
Ta 132,21,20736
Ta 134,21,25856
Mvaa 212,140,68
K 121,122,123
Mvaa 212,136,64
W 8
Ta 136,22,10528
Ta 138,22,15648
Mvaa 224,144,64
s_lshl_b32 s66, s14, 6
K 122,124,125
Mvaa 224,148,68
Ta 140,22,20768
Ta 142,22,25888
Mvaa 232,148,84
K 123,126,127
Mvaa 232,144,80
W 8
s_lshl_b32 s67, s14, 9
s_add_u32 s83, s52, 49152
Mvaa 236,152,80
Ta 144,21,30720
Ta 146,21,35840
Mvaa 236,156,84
s_add_u32 s72, s72, s66
buffer_store_dwordx4 v[120:123], v1, s[8:11], s83 offen sc1
Ta 148,23,0
Mvaa 228,156,68
Ta 150,23,5120
K 128,128,129
Mvaa 228,152,64
W 8
Ta 152,22,30752
Ta 154,22,35872
Mvaa 240,128,64
s_add_u32 s75, s75, s67
K 129,130,131
Mvaa 240,132,68
Ta 156,24,32
Ta 158,24,5152
Mvaa 248,132,84
K 130,132,133
Mvaa 248,128,80
W 8
s_add_u32 s73, s73, s66
Ta 128,21,30784
Mvaa 252,136,80
Ta 130,21,35904
K 131,134,135
Mvaa 252,140,84
s_add_u32 s83, s52, 16384
Ta 132,23,64
Ta 134,23,5184
Mvaa 244,140,68
s_add_u32 s76, s76, s67
Mvaa 244,136,64
s_waitcnt vmcnt(20) lgkmcnt(8)
buffer_store_dwordx4 v[128:131], v1, s[8:11], s83 offen sc1
Ta 136,22,30816
Mvaa 176,144,72
Ta 138,22,35936
K 136,136,137
Mvaa 176,148,76
Ta 140,24,96
Ta 142,24,5216
Mvaa 184,148,92
K 137,138,139
Mvaa 184,144,88
W 8
buffer_load_dwordx4 v[36:39], v20, s[16:19], s72 offen
Ta 144,21,30848
Mvaa 188,152,88
Ta 146,21,35968
K 138,140,141
Mvaa 188,156,92
Ta 148,23,128
Ta 150,23,5248
Mvaa 180,156,76
K 139,142,143
Mvaa 180,152,72
W 8
s_add_u32 s83, s52, 57344
buffer_load_dwordx4 v[44:47], v14, s[8:11], s75 offen sc1
Mvaa 192,128,72
Ta 152,22,30880
Ta 154,22,36000
Mvaa 192,132,76
Ta 156,24,160
Ta 158,24,5280
Mvaa 200,132,92
Mvaa 200,128,88
W 8
buffer_store_dwordx4 v[136:139], v1, s[8:11], s83 offen sc1
K 144,144,145
Mvaa 204,136,88
)"
R"(Ta 128,21,30912
Ta 130,21,36032
Mvaa 204,140,92
Ta 132,23,192
K 145,146,147
Mvaa 196,140,76
Ta 134,23,5312
K 146,148,149
Mvaa 196,136,72
W 8
buffer_load_dwordx4 v[52:55], v20, s[16:19], s73 offen
Ta 136,22,30944
Mvaa 208,144,72
Ta 138,22,36064
K 147,150,151
Mvaa 208,148,76
s_add_u32 s83, s52, 24576
Ta 140,24,224
Ta 142,24,5344
Mvaa 216,148,92
Mvaa 216,144,88
W 8
buffer_store_dwordx4 v[144:147], v1, s[8:11], s83 offen sc1
Ta 144,21,30976
Mvaa 220,152,88
Ta 146,21,36096
K 152,152,153
Mvaa 220,156,92
Ta 148,23,256
Ta 150,23,5376
Mvaa 212,156,76
K 153,154,155
Mvaa 212,152,72
W 8
buffer_load_dwordx4 v[60:63], v14, s[8:11], s76 offen sc1
Ta 152,22,31008
Mvaa 224,128,72
Ta 154,22,36128
K 154,156,157
Mvaa 224,132,76
Ta 156,24,288
Ta 158,24,5408
Mvaa 232,132,92
K 155,158,159
Mvaa 232,128,88
W 8
s_add_u32 s83, s52, 65536
buffer_load_dwordx4 v[68:71], v20, s[16:19], s74 offen
Mvaa 236,136,88
Ta 128,23,20480
Ta 130,23,25600
Mvaa 236,140,92
Ta 132,23,51200
Ta 134,23,56320
Mvaa 228,140,76
Mvaa 228,136,72
W 8
buffer_store_dwordx4 v[152:155], v1, s[8:11], s83 offen sc1
K 160,160,161
Mvaa 240,144,72
Ta 136,25,0
Ta 138,25,5120
Mvaa 240,148,76
Ta 140,21,40960
K 161,162,163
Mvaa 248,148,92
Ta 142,21,46080
K 162,164,165
Mvaa 248,144,88
W 8
buffer_load_dwordx4 v[76:79], v14, s[8:11], s77 offen sc1
Ta 144,21,51200
Mvaa 252,152,88
Ta 146,21,56320
K 163,166,167
Mvaa 252,156,92
s_add_u32 s83, s52, 32768
Ta 148,23,10240
Ta 150,23,15360
Mvaa 244,156,76
Mvaa 244,152,72
s_waitcnt vmcnt(26) lgkmcnt(8)
buffer_store_dwordx4 v[160:163], v1, s[8:11], s83 offen sc1
Ta 152,23,30720
Mvaa 176,128,96
Ta 154,23,35840
K 168,168,169
Mvaa 176,132,100
Ta 156,23,40960
Ta 158,23,46080
Mvaa 184,132,116
K 169,170,171
Mvaa 184,128,112
s_waitcnt vmcnt(23) lgkmcnt(8)
buffer_load_dwordx2 v[12:13], v27, s[8:11], 0 offen offset:3584 sc1
Ta 128,24,20512
Mvaa 184,136,120
Ta 130,24,25632
K 170,172,173
Mvaa 184,140,124
Ta 132,24,51232
Ta 134,24,56352
Mvaa 176,140,108
K 171,174,175
Mvaa 176,136,104
s_waitcnt vmcnt(20) lgkmcnt(8)
s_add_u32 s83, s52, 73728
Ta 136,26,32
Mvaa 176,144,184
Ta 138,26,5152
Mvaa 176,148,188
buffer_store_dwordx4 v[168:171], v1, s[8:11], s83 offen sc1
Ta 140,22,40992
Mvaa 184,148,204
Ta 142,22,46112
Mvaa 184,144,200
s_waitcnt vmcnt(16) lgkmcnt(8)
Ta 144,22,51232
Ta 146,22,56352
Mvaa 184,152,208
Mvaa 184,156,212
Ta 148,24,10272
Ta 150,24,15392
Mvaa 176,156,196
Mvaa 176,152,192
W 8
Ta 152,24,30752
Ta 154,24,35872
Mvaa 180,128,96
Mvaa 180,132,100
Ta 156,24,40992
Ta 158,24,46112
Mvaa 188,132,116
Mvaa 188,128,112
W 8
Ta 128,23,20544
Ta 130,23,25664
Mvaa 188,136,120
Mvaa 188,140,124
Ta 132,23,51264
Ta 134,23,56384
Mvaa 180,140,108
Mvaa 180,136,104
W 8
Ta 136,25,64
Ta 138,25,5184
Mvaa 180,144,184
Mvaa 180,148,188
Ta 140,21,41024
Ta 142,21,46144
Mvaa 188,148,204
Mvaa 188,144,200
W 8
Ta 144,21,51264
Ta 146,21,56384
Mvaa 188,152,208
Mvaa 188,156,212
Ta 148,23,10304
Ta 150,23,15424
Mvaa 180,156,196
Mvaa 180,152,192
W 8
Ta 152,23,30784
Ta 154,23,35904
Mvaa 192,128,96
Mvaa 192,132,100
Ta 156,23,41024
Ta 158,23,46144
Mvaa 200,132,116
K 176,176,177
Mvaa 200,128,112
W 8
Ta 128,24,20576
Ta 130,24,25696
Mvaa 200,136,120
K 177,178,179
Mvaa 200,140,124
Ta 132,24,51296
K 178,180,181
Mvaa 192,140,108
Ta 134,24,56416
K 179,182,183
Mvaa 192,136,104
W 8
Ta 136,26,96
Ta 138,26,5216
Mvaa 192,144,184
s_add_u32 s83, s52, 327680
Mvaa 192,148,188
buffer_store_dwordx4 v[176:179], v1, s[8:11], s83 offen sc1
K 184,184,185
Mvaa 200,148,204
K 185,186,187
K 186,188,189
Mvaa 200,144,200
W 6
Ta 140,22,41056
K 187,190,191
Mvaa 200,152,208
s_add_u32 s83, s52, 368640
Ta 142,22,46176
Ta 144,22,51296
Mvaa 200,156,212
buffer_store_dwordx4 v[184:187], v1, s[8:11], s83 offen sc1
Ta 146,22,56416
Mvaa 192,156,196
Ta 148,24,10336
Ta 150,24,15456
Mvaa 192,152,192
W 8
Ta 152,24,30816
Ta 154,24,35936
Mvaa 196,128,96
Mvaa 196,132,100
Ta 156,24,41056
Ta 158,24,46176
Mvaa 204,132,116
Mvaa 204,128,112
W 8
Ta 128,23,20608
Ta 130,23,25728
Mvaa 204,136,120
Mvaa 204,140,124
Ta 132,23,51328
Ta 134,23,56448
Mvaa 196,140,108
Mvaa 196,136,104
W 8
Ta 136,25,128
Ta 138,25,5248
Mvaa 196,144,184
Mvaa 196,148,188
Ta 140,21,41088
Ta 142,21,46208
Mvaa 204,148,204
Mvaa 204,144,200
W 8
Ta 144,21,51328
Ta 146,21,56448
Mvaa 204,152,208
Mvaa 204,156,212
Ta 148,23,10368
Ta 150,23,15488
Mvaa 196,156,196
Mvaa 196,152,192
W 8
Ta 152,23,30848
Ta 154,23,35968
Mvaa 208,128,96
Mvaa 208,132,100
Ta 156,23,41088
Ta 158,23,46208
Mvaa 216,132,116
K 192,192,193
Mvaa 216,128,112
W 8
Ta 128,24,20640
Ta 130,24,25760
Mvaa 216,136,120
K 193,194,195
Mvaa 216,140,124
Ta 132,24,51360
K 194,196,197
Mvaa 208,140,108
Ta 134,24,56480
K 195,198,199
Mvaa 208,136,104
W 8
Ta 136,26,160
Ta 138,26,5280
Mvaa 208,144,184
s_add_u32 s83, s52, 335872
Mvaa 208,148,188
buffer_store_dwordx4 v[192:195], v1, s[8:11], s83 offen sc1
K 200,200,201
Mvaa 216,148,204
K 201,202,203
K 202,204,205
Mvaa 216,144,200
W 6
Ta 140,22,41120
K 203,206,207
Mvaa 216,152,208
s_add_u32 s83, s52, 376832
Ta 142,22,46240
Ta 144,22,51360
Mvaa 216,156,212
buffer_store_dwordx4 v[200:203], v1, s[8:11], s83 offen sc1
Ta 146,22,56480
Mvaa 208,156,196
Ta 148,24,10400
Ta 150,24,15520
Mvaa 208,152,192
W 8
Ta 152,24,30880
Ta 154,24,36000
Mvaa 212,128,96
Mvaa 212,132,100
s_waitcnt vmcnt(4)
v_lshlrev_b32 v2, 2, v1
v_mov_b64 v[28:29], s[12:13]
s_mov_b64 exec, 3
buffer_store_dwordx2 v[28:29], v2, s[8:11], s53 offen offset:0 sc1
s_mov_b64 exec, -1
Ta 156,24,41120
Ta 158,24,46240
Mvaa 220,132,116
Mvaa 220,128,112
W 8
Ta 128,23,20672
Ta 130,23,25792
Mvaa 220,136,120
Mvaa 220,140,124
v_xor_b32 v12, s12, v12
v_xor_b32 v13, s13, v13
v_lshrrev_b32 v3, 11, v12
v_or_b32 v13, v13, v3
v_cmp_eq_u32 vcc, 0, v13
s_cmp_eq_u64 vcc, -1
s_cbranch_scc0 .LL_100%=
.LL_101%=:
v_bfe_u32 v13, v12, 4, 7
s_xor_b32 s66, s61, 124
v_cmp_eq_u32 vcc, s61, v13
v_cmp_eq_u32_e64 s[86:87], s66, v13
s_or_b32 s68, vcc_lo, s86
s_and_b32 s68, s68, 0xff
s_cbranch_scc1 .Lup_98%=
.Lupr_99%=:
Ta 132,23,51392
Ta 134,23,56512
Mvaa 212,140,108
Mvaa 212,136,104
W 8
Ta 136,25,192
v_lshlrev_b32 v32, 16, v36
Mvaa 212,144,184
Ta 138,25,5312
v_and_b32 v33, 0xffff0000, v36
Mvaa 212,148,188
Ta 140,21,41152
v_lshlrev_b32 v34, 16, v37
Mvaa 220,148,204
Ta 142,21,46272
v_and_b32 v35, 0xffff0000, v37
Mvaa 220,144,200
W 8
Ta 144,21,51392
v_lshlrev_b32 v36, 16, v38
Mvaa 220,152,208
Ta 146,21,56512
v_and_b32 v37, 0xffff0000, v38
Mvaa 220,156,212
Ta 148,23,10432
v_lshlrev_b32 v38, 16, v39
Mvaa 212,156,196
Ta 150,23,15552
v_and_b32 v39, 0xffff0000, v39
Mvaa 212,152,192
W 8
Ta 152,23,30912
v_lshlrev_b32 v40, 16, v44
Mvaa 224,128,96
Ta 154,23,36032
v_and_b32 v41, 0xffff0000, v44
Mvaa 224,132,100
Ta 156,23,41152
v_lshlrev_b32 v42, 16, v45
Mvaa 232,132,116
Ta 158,23,46272
v_and_b32 v43, 0xffff0000, v45
Mvaa 232,128,112
W 8
K 208,208,209
v_lshlrev_b32 v44, 16, v46
Mvaa 232,136,120
Ta 128,24,20704
Ta 130,24,25824
Mvaa 232,140,124
v_and_b32 v45, 0xffff0000, v46
K 209,210,211
Mvaa 224,140,108
v_lshlrev_b32 v46, 16, v47
K 210,212,213
Mvaa 224,136,104
W 6
Ta 132,24,51424
Ta 134,24,56544
Mvaa 224,144,184
v_and_b32 v47, 0xffff0000, v47
K 211,214,215
Mvaa 224,148,188
s_add_u32 s83, s52, 344064
Ta 136,26,224
v_lshlrev_b32 v48, 16, v52
Mvaa 232,148,204
Ta 138,26,5344
v_and_b32 v49, 0xffff0000, v52
Mvaa 232,144,200
W 6
)"
R"(buffer_store_dwordx4 v[208:211], v1, s[8:11], s83 offen sc1
v_lshlrev_b32 v50, 16, v53
Mvaa 232,152,208
K 216,216,217
K 217,218,219
Mvaa 232,156,212
K 218,220,221
K 219,222,223
Mvaa 224,156,196
s_add_u32 s83, s52, 385024
buffer_store_dwordx4 v[216:219], v1, s[8:11], s83 offen sc1
Ta 140,22,41184
Mvaa 224,152,192
Ta 142,22,46304
W 4
Mvaa 228,128,96
v_and_b32 v51, 0xffff0000, v53
v_lshlrev_b32 v52, 16, v54
Mvaa 228,132,100
Ta 144,22,51424
Ta 146,22,56544
Mvaa 236,132,116
v_and_b32 v53, 0xffff0000, v54
v_lshlrev_b32 v54, 16, v55
Mvaa 236,128,112
Ta 148,24,10464
Ta 150,24,15584
W 4
Mvaa 236,136,120
v_and_b32 v55, 0xffff0000, v55
v_lshlrev_b32 v56, 16, v60
Mvaa 236,140,124
Ta 152,24,30944
Ta 154,24,36064
Mvaa 228,140,108
v_and_b32 v57, 0xffff0000, v60
v_lshlrev_b32 v58, 16, v61
Mvaa 228,136,104
Ta 156,24,41184
Ta 158,24,46304
W 4
Mvaa 228,144,184
v_and_b32 v59, 0xffff0000, v61
v_lshlrev_b32 v60, 16, v62
Mvaa 228,148,188
Ta 128,23,20736
Ta 130,23,25856
Mvaa 236,148,204
v_and_b32 v61, 0xffff0000, v62
v_lshlrev_b32 v62, 16, v63
Mvaa 236,144,200
Ta 132,23,51456
Ta 134,23,56576
W 4
Mvaa 236,152,208
v_and_b32 v63, 0xffff0000, v63
v_lshlrev_b32 v64, 16, v68
Mvaa 236,156,212
Ta 136,25,256
Ta 138,25,5376
Mvaa 228,156,196
v_and_b32 v65, 0xffff0000, v68
v_lshlrev_b32 v66, 16, v69
Mvaa 228,152,192
Ta 140,21,41216
Ta 142,21,46336
W 4
Mvaa 240,128,96
v_and_b32 v67, 0xffff0000, v69
v_lshlrev_b32 v68, 16, v70
Mvaa 240,132,100
Ta 144,21,51456
Ta 146,21,56576
Mvaa 248,132,116
v_and_b32 v69, 0xffff0000, v70
v_lshlrev_b32 v70, 16, v71
Mvaa 248,128,112
Ta 148,23,10496
Ta 150,23,15616
W 4
Mvaa 248,136,120
v_and_b32 v71, 0xffff0000, v71
v_lshlrev_b32 v72, 16, v76
Mvaa 248,140,124
Ta 152,23,30976
Ta 154,23,36096
Mvaa 240,140,108
v_and_b32 v73, 0xffff0000, v76
v_lshlrev_b32 v74, 16, v77
Mvaa 240,136,104
Ta 156,23,41216
Ta 158,23,46336
W 4
Mvaa 240,144,184
v_and_b32 v75, 0xffff0000, v77
K 224,224,225
Mvaa 240,148,188
Ta 128,24,20768
v_lshlrev_b32 v76, 16, v78
Mvaa 248,148,204
Ta 130,24,25888
K 225,226,227
Mvaa 248,144,200
Ta 132,24,51488
Ta 134,24,56608
W 4
Mvaa 248,152,208
v_and_b32 v77, 0xffff0000, v78
K 226,228,229
Mvaa 248,156,212
v_lshlrev_b32 v78, 16, v79
v_and_b32 v79, 0xffff0000, v79
Mvaa 240,156,196
K 227,230,231
s_add_u32 s83, s52, 352256
buffer_store_dwordx4 v[224:227], v1, s[8:11], s83 offen sc1
K 232,232,233
K 233,234,235
K 234,236,237
K 235,238,239
s_add_u32 s83, s52, 393216
buffer_store_dwordx4 v[232:235], v1, s[8:11], s83 offen sc1
Mvaa 240,152,192
Ta 136,26,288
Ta 138,26,5408
Ta 140,22,41248
Ta 142,22,46368
W 4
Mvaa 244,128,96
Ta 144,22,51488
Ta 146,22,56608
Mvaa 244,132,100
Ta 148,24,10528
Ta 150,24,15648
Mvaa 252,132,116
Ta 152,24,31008
Ta 154,24,36128
Mvaa 252,128,112
W 6
Ta 156,24,41248
Ta 158,24,46368
Mvaa 252,136,120
Mvaa 252,140,124
Mvaa 244,140,108
Mvaa 244,136,104
W 4
Mvaa 244,144,184
Mvaa 244,148,188
Mvaa 252,148,204
Mvaa 252,144,200
W 0
Mvaa 252,152,208
Mvaa 252,156,212
Mvaa 244,156,196
Mvaa 244,152,192
K 240,240,241
K 241,242,243
s_nop 5
K 242,244,245
K 243,246,247
s_add_u32 s83, s52, 360448
buffer_store_dwordx4 v[240:243], v1, s[8:11], s83 offen sc1
K 248,248,249
K 249,250,251
K 250,252,253
K 251,254,255
s_add_u32 s83, s52, 401408
buffer_store_dwordx4 v[248:251], v1, s[8:11], s83 offen sc1
.Ltend_83%=:
buffer_load_dwordx2 v[16:17], v18, s[8:11], s63 offen sc1
s_cmp_lg_u32 s68, 0
s_cbranch_scc1 .Lup_103%=
.Lupr_104%=:
s_lshl_b32 s66, 0x101, s59
s_waitcnt vmcnt(1)
s_cmp_eq_u32 s64, 0
s_cbranch_scc0 .LL_109%=
v_lshlrev_b32 v2, 2, v1
v_mov_b64 v[28:29], s[12:13]
s_mov_b64 exec, 3
buffer_store_dwordx2 v[28:29], v2, s[8:11], s53 offen offset:512 sc1
s_mov_b64 exec, -1
.LL_109%=:
s_barrier
s_waitcnt vmcnt(1)
v_cmp_eq_u64 vcc, s[12:13], v[16:17]
s_or_b32 s67, vcc_lo, s66
s_cmp_eq_u32 s14, 0
s_cbranch_scc1 .LL_111%=
s_cmp_eq_u32 s14, 1
s_cbranch_scc1 .LL_112%=
s_mov_b32 s84, 0
.LL_113%=:
s_mov_b32 s70, s71
s_andn2_b32 s69, s67, s71
s_and_b32 s69, s69, 0xffff
s_cbranch_scc0 .LL_116%=
s_and_b32 s80, s69, 255
s_cbranch_scc0 .LL_117%=
s_bitcmp1_b32 s69, 0
s_cbranch_scc0 .LL_118%=
buffer_load_dwordx4 a[0:3], v1, s[8:11], s78 offen offset:0 sc1
.LL_118%=:
s_bitcmp1_b32 s69, 1
s_cbranch_scc0 .LL_119%=
buffer_load_dwordx4 a[4:7], v1, s[8:11], s78 offen offset:1024 sc1
.LL_119%=:
s_bitcmp1_b32 s69, 2
s_cbranch_scc0 .LL_120%=
buffer_load_dwordx4 a[8:11], v1, s[8:11], s78 offen offset:2048 sc1
.LL_120%=:
s_bitcmp1_b32 s69, 3
s_cbranch_scc0 .LL_121%=
buffer_load_dwordx4 a[12:15], v1, s[8:11], s78 offen offset:3072 sc1
.LL_121%=:
s_bitcmp1_b32 s69, 4
s_cbranch_scc0 .LL_122%=
buffer_load_dwordx4 a[16:19], v19, s[8:11], s78 offen offset:0 sc1
.LL_122%=:
s_bitcmp1_b32 s69, 5
s_cbranch_scc0 .LL_123%=
buffer_load_dwordx4 a[20:23], v19, s[8:11], s78 offen offset:1024 sc1
.LL_123%=:
s_bitcmp1_b32 s69, 6
s_cbranch_scc0 .LL_124%=
buffer_load_dwordx4 a[24:27], v19, s[8:11], s78 offen offset:2048 sc1
.LL_124%=:
s_bitcmp1_b32 s69, 7
s_cbranch_scc0 .LL_125%=
buffer_load_dwordx4 a[28:31], v19, s[8:11], s78 offen offset:3072 sc1
.LL_125%=:
.LL_117%=:
s_and_b32 s80, s69, 65280
s_cbranch_scc0 .LL_126%=
s_bitcmp1_b32 s69, 8
s_cbranch_scc0 .LL_127%=
buffer_load_dwordx4 a[32:35], v1, s[8:11], s79 offen offset:0 sc1
.LL_127%=:
s_bitcmp1_b32 s69, 9
s_cbranch_scc0 .LL_128%=
buffer_load_dwordx4 a[36:39], v1, s[8:11], s79 offen offset:1024 sc1
.LL_128%=:
s_bitcmp1_b32 s69, 10
s_cbranch_scc0 .LL_129%=
buffer_load_dwordx4 a[40:43], v1, s[8:11], s79 offen offset:2048 sc1
.LL_129%=:
s_bitcmp1_b32 s69, 11
s_cbranch_scc0 .LL_130%=
buffer_load_dwordx4 a[44:47], v1, s[8:11], s79 offen offset:3072 sc1
.LL_130%=:
s_bitcmp1_b32 s69, 12
s_cbranch_scc0 .LL_131%=
buffer_load_dwordx4 a[48:51], v19, s[8:11], s79 offen offset:0 sc1
.LL_131%=:
s_bitcmp1_b32 s69, 13
s_cbranch_scc0 .LL_132%=
buffer_load_dwordx4 a[52:55], v19, s[8:11], s79 offen offset:1024 sc1
.LL_132%=:
s_bitcmp1_b32 s69, 14
s_cbranch_scc0 .LL_133%=
buffer_load_dwordx4 a[56:59], v19, s[8:11], s79 offen offset:2048 sc1
.LL_133%=:
s_bitcmp1_b32 s69, 15
s_cbranch_scc0 .LL_134%=
buffer_load_dwordx4 a[60:63], v19, s[8:11], s79 offen offset:3072 sc1
.LL_134%=:
.LL_126%=:
s_or_b32 s71, s71, s69
.LL_116%=:
s_andn2_b32 s80, 0xffff, s71
s_cbranch_scc0 .LL_115%=
s_sleep 4
buffer_load_dwordx2 v[16:17], v18, s[8:11], s63 offen sc1
s_and_b32 s80, s70, 0x100ff
s_cmp_eq_u32 s80, 0xff
s_cbranch_scc0 .LL_114%=
Zvva 80,4,0
Zvva 84,8,0
Mvva 80,4,4
Mvva 84,8,4
Mvva 80,4,8
Mvva 84,8,8
Mvva 80,4,12
Mvva 84,8,12
Mvva 80,4,16
Mvva 84,8,16
Mvva 80,4,20
Mvva 84,8,20
Mvva 80,4,24
Mvva 84,8,24
Mvva 80,4,28
Mvva 84,8,28
s_nop 6
K 160,80,81
K 161,82,83
K 162,84,85
K 163,86,87
v_lshlrev_b32 v80, 16, v160
v_and_b32 v81, 0xffff0000, v160
v_lshlrev_b32 v82, 16, v161
v_and_b32 v83, 0xffff0000, v161
v_lshlrev_b32 v84, 16, v162
v_and_b32 v85, 0xffff0000, v162
v_lshlrev_b32 v86, 16, v163
v_and_b32 v87, 0xffff0000, v163
v_pk_add_f32 v[80:81], v[32:33], v[80:81]
v_pk_add_f32 v[82:83], v[34:35], v[82:83]
v_pk_add_f32 v[84:85], v[36:37], v[84:85]
v_pk_add_f32 v[86:87], v[38:39], v[86:87]
K 160,80,81
K 161,82,83
K 162,84,85
K 163,86,87
v_lshlrev_b32 v80, 16, v160
v_and_b32 v81, 0xffff0000, v160
v_lshlrev_b32 v82, 16, v161
v_and_b32 v83, 0xffff0000, v161
v_lshlrev_b32 v84, 16, v162
v_and_b32 v85, 0xffff0000, v162
v_lshlrev_b32 v86, 16, v163
v_and_b32 v87, 0xffff0000, v163
v_pk_add_f32 v[80:81], v[40:41], v[80:81]
v_pk_add_f32 v[82:83], v[42:43], v[82:83]
v_pk_add_f32 v[84:85], v[44:45], v[84:85]
v_pk_add_f32 v[86:87], v[46:47], v[86:87]
K 80,80,81
K 81,82,83
K 82,84,85
K 83,86,87
buffer_store_dwordx4 v[80:83], v20, s[20:23], s72 offen nt
s_bitset1_b32 s71, 16
.LL_114%=:
s_add_u32 s84, s84, 1
s_waitcnt vmcnt(0)
v_cmp_eq_u64 vcc, s[12:13], v[16:17]
s_or_b32 s67, vcc_lo, s66
s_cmp_lt_u32 s84, 4194304
s_cbranch_scc1 .LL_113%=
.LL_115%=:
s_bitcmp1_b32 s71, 16
s_cbranch_scc1 .LL_135%=
s_and_b32 s80, s70, 0xff
s_cmp_eq_u32 s80, 0xff
s_cbranch_scc1 .LL_136%=
s_waitcnt vmcnt(0)
.LL_136%=:
Zvva 80,4,0
Zvva 84,8,0
Mvva 80,4,4
Mvva 84,8,4
Mvva 80,4,8
Mvva 84,8,8
Mvva 80,4,12
Mvva 84,8,12
Mvva 80,4,16
Mvva 84,8,16
Mvva 80,4,20
Mvva 84,8,20
Mvva 80,4,24
Mvva 84,8,24
Mvva 80,4,28
Mvva 84,8,28
s_nop 6
K 160,80,81
K 161,82,83
K 162,84,85
K 163,86,87
v_lshlrev_b32 v80, 16, v160
v_and_b32 v81, 0xffff0000, v160
v_lshlrev_b32 v82, 16, v161
v_and_b32 v83, 0xffff0000, v161
v_lshlrev_b32 v84, 16, v162
v_and_b32 v85, 0xffff0000, v162
v_lshlrev_b32 v86, 16, v163
v_and_b32 v87, 0xffff0000, v163
v_pk_add_f32 v[80:81], v[32:33], v[80:81]
v_pk_add_f32 v[82:83], v[34:35], v[82:83]
v_pk_add_f32 v[84:85], v[36:37], v[84:85]
v_pk_add_f32 v[86:87], v[38:39], v[86:87]
K 160,80,81
K 161,82,83
K 162,84,85
K 163,86,87
v_lshlrev_b32 v80, 16, v160
v_and_b32 v81, 0xffff0000, v160
v_lshlrev_b32 v82, 16, v161
v_and_b32 v83, 0xffff0000, v161
v_lshlrev_b32 v84, 16, v162
v_and_b32 v85, 0xffff0000, v162
v_lshlrev_b32 v86, 16, v163
v_and_b32 v87, 0xffff0000, v163
v_pk_add_f32 v[80:81], v[40:41], v[80:81]
v_pk_add_f32 v[82:83], v[42:43], v[82:83]
v_pk_add_f32 v[84:85], v[44:45], v[84:85]
v_pk_add_f32 v[86:87], v[46:47], v[86:87]
K 80,80,81
K 81,82,83
K 82,84,85
K 83,86,87
buffer_store_dwordx4 v[80:83], v20, s[20:23], s72 offen nt
)"
R"(s_waitcnt vmcnt(1)
s_nop 0
Zvva 80,4,32
Zvva 84,8,32
Mvva 80,4,36
Mvva 84,8,36
Mvva 80,4,40
Mvva 84,8,40
Mvva 80,4,44
Mvva 84,8,44
Mvva 80,4,48
Mvva 84,8,48
Mvva 80,4,52
Mvva 84,8,52
Mvva 80,4,56
Mvva 84,8,56
Mvva 80,4,60
Mvva 84,8,60
s_nop 6
K 160,80,81
K 161,82,83
K 162,84,85
K 163,86,87
v_lshlrev_b32 v80, 16, v160
v_and_b32 v81, 0xffff0000, v160
v_lshlrev_b32 v82, 16, v161
v_and_b32 v83, 0xffff0000, v161
v_lshlrev_b32 v84, 16, v162
v_and_b32 v85, 0xffff0000, v162
v_lshlrev_b32 v86, 16, v163
v_and_b32 v87, 0xffff0000, v163
v_pk_add_f32 v[80:81], v[48:49], v[80:81]
v_pk_add_f32 v[82:83], v[50:51], v[82:83]
v_pk_add_f32 v[84:85], v[52:53], v[84:85]
v_pk_add_f32 v[86:87], v[54:55], v[86:87]
K 160,80,81
K 161,82,83
K 162,84,85
K 163,86,87
v_lshlrev_b32 v80, 16, v160
v_and_b32 v81, 0xffff0000, v160
v_lshlrev_b32 v82, 16, v161
v_and_b32 v83, 0xffff0000, v161
v_lshlrev_b32 v84, 16, v162
v_and_b32 v85, 0xffff0000, v162
v_lshlrev_b32 v86, 16, v163
v_and_b32 v87, 0xffff0000, v163
v_pk_add_f32 v[80:81], v[56:57], v[80:81]
v_pk_add_f32 v[82:83], v[58:59], v[82:83]
v_pk_add_f32 v[84:85], v[60:61], v[84:85]
v_pk_add_f32 v[86:87], v[62:63], v[86:87]
K 80,80,81
K 81,82,83
K 82,84,85
K 83,86,87
buffer_store_dwordx4 v[80:83], v20, s[20:23], s73 offen nt
s_branch .LL_110%=
.LL_135%=:
s_waitcnt vmcnt(0)
Zvva 80,4,32
Zvva 84,8,32
Mvva 80,4,36
Mvva 84,8,36
Mvva 80,4,40
Mvva 84,8,40
Mvva 80,4,44
Mvva 84,8,44
Mvva 80,4,48
Mvva 84,8,48
Mvva 80,4,52
Mvva 84,8,52
Mvva 80,4,56
Mvva 84,8,56
Mvva 80,4,60
Mvva 84,8,60
s_nop 6
K 160,80,81
K 161,82,83
K 162,84,85
K 163,86,87
v_lshlrev_b32 v80, 16, v160
v_and_b32 v81, 0xffff0000, v160
v_lshlrev_b32 v82, 16, v161
v_and_b32 v83, 0xffff0000, v161
v_lshlrev_b32 v84, 16, v162
v_and_b32 v85, 0xffff0000, v162
v_lshlrev_b32 v86, 16, v163
v_and_b32 v87, 0xffff0000, v163
v_pk_add_f32 v[80:81], v[48:49], v[80:81]
v_pk_add_f32 v[82:83], v[50:51], v[82:83]
v_pk_add_f32 v[84:85], v[52:53], v[84:85]
v_pk_add_f32 v[86:87], v[54:55], v[86:87]
K 160,80,81
K 161,82,83
K 162,84,85
K 163,86,87
v_lshlrev_b32 v80, 16, v160
v_and_b32 v81, 0xffff0000, v160
v_lshlrev_b32 v82, 16, v161
v_and_b32 v83, 0xffff0000, v161
v_lshlrev_b32 v84, 16, v162
v_and_b32 v85, 0xffff0000, v162
v_lshlrev_b32 v86, 16, v163
v_and_b32 v87, 0xffff0000, v163
v_pk_add_f32 v[80:81], v[56:57], v[80:81]
v_pk_add_f32 v[82:83], v[58:59], v[82:83]
v_pk_add_f32 v[84:85], v[60:61], v[84:85]
v_pk_add_f32 v[86:87], v[62:63], v[86:87]
K 80,80,81
K 81,82,83
K 82,84,85
K 83,86,87
buffer_store_dwordx4 v[80:83], v20, s[20:23], s73 offen nt
s_branch .LL_110%=
.LL_111%=:
s_mov_b32 s84, 0
.LL_137%=:
s_mov_b32 s70, s71
s_andn2_b32 s69, s67, s71
s_and_b32 s69, s69, 0xffff
s_cbranch_scc0 .LL_140%=
s_and_b32 s80, s69, 255
s_cbranch_scc0 .LL_141%=
s_bitcmp1_b32 s69, 0
s_cbranch_scc0 .LL_142%=
buffer_load_dwordx4 a[0:3], v1, s[8:11], s78 offen offset:0 sc1
buffer_load_dwordx4 a[216:219], v1, s[8:11], s65 offen offset:0 sc1
.LL_142%=:
s_bitcmp1_b32 s69, 1
s_cbranch_scc0 .LL_143%=
buffer_load_dwordx4 a[4:7], v1, s[8:11], s78 offen offset:1024 sc1
buffer_load_dwordx4 a[220:223], v1, s[8:11], s65 offen offset:1024 sc1
.LL_143%=:
s_bitcmp1_b32 s69, 2
s_cbranch_scc0 .LL_144%=
buffer_load_dwordx4 a[8:11], v1, s[8:11], s78 offen offset:2048 sc1
buffer_load_dwordx4 a[224:227], v1, s[8:11], s65 offen offset:2048 sc1
.LL_144%=:
s_bitcmp1_b32 s69, 3
s_cbranch_scc0 .LL_145%=
buffer_load_dwordx4 a[12:15], v1, s[8:11], s78 offen offset:3072 sc1
buffer_load_dwordx4 a[228:231], v1, s[8:11], s65 offen offset:3072 sc1
.LL_145%=:
s_bitcmp1_b32 s69, 4
s_cbranch_scc0 .LL_146%=
buffer_load_dwordx4 a[16:19], v19, s[8:11], s78 offen offset:0 sc1
buffer_load_dwordx4 a[232:235], v19, s[8:11], s65 offen offset:0 sc1
.LL_146%=:
s_bitcmp1_b32 s69, 5
s_cbranch_scc0 .LL_147%=
buffer_load_dwordx4 a[20:23], v19, s[8:11], s78 offen offset:1024 sc1
buffer_load_dwordx4 a[236:239], v19, s[8:11], s65 offen offset:1024 sc1
.LL_147%=:
s_bitcmp1_b32 s69, 6
s_cbranch_scc0 .LL_148%=
buffer_load_dwordx4 a[24:27], v19, s[8:11], s78 offen offset:2048 sc1
buffer_load_dwordx4 a[240:243], v19, s[8:11], s65 offen offset:2048 sc1
.LL_148%=:
s_bitcmp1_b32 s69, 7
s_cbranch_scc0 .LL_149%=
buffer_load_dwordx4 a[28:31], v19, s[8:11], s78 offen offset:3072 sc1
buffer_load_dwordx4 a[244:247], v19, s[8:11], s65 offen offset:3072 sc1
.LL_149%=:
.LL_141%=:
s_and_b32 s80, s69, 65280
s_cbranch_scc0 .LL_150%=
s_bitcmp1_b32 s69, 8
s_cbranch_scc0 .LL_151%=
buffer_load_dwordx4 a[32:35], v1, s[8:11], s79 offen offset:0 sc1
.LL_151%=:
s_bitcmp1_b32 s69, 9
s_cbranch_scc0 .LL_152%=
buffer_load_dwordx4 a[36:39], v1, s[8:11], s79 offen offset:1024 sc1
.LL_152%=:
s_bitcmp1_b32 s69, 10
s_cbranch_scc0 .LL_153%=
buffer_load_dwordx4 a[40:43], v1, s[8:11], s79 offen offset:2048 sc1
.LL_153%=:
s_bitcmp1_b32 s69, 11
s_cbranch_scc0 .LL_154%=
buffer_load_dwordx4 a[44:47], v1, s[8:11], s79 offen offset:3072 sc1
.LL_154%=:
s_bitcmp1_b32 s69, 12
s_cbranch_scc0 .LL_155%=
buffer_load_dwordx4 a[48:51], v19, s[8:11], s79 offen offset:0 sc1
.LL_155%=:
s_bitcmp1_b32 s69, 13
s_cbranch_scc0 .LL_156%=
buffer_load_dwordx4 a[52:55], v19, s[8:11], s79 offen offset:1024 sc1
.LL_156%=:
s_bitcmp1_b32 s69, 14
s_cbranch_scc0 .LL_157%=
buffer_load_dwordx4 a[56:59], v19, s[8:11], s79 offen offset:2048 sc1
.LL_157%=:
s_bitcmp1_b32 s69, 15
s_cbranch_scc0 .LL_158%=
buffer_load_dwordx4 a[60:63], v19, s[8:11], s79 offen offset:3072 sc1
.LL_158%=:
.LL_150%=:
s_or_b32 s71, s71, s69
.LL_140%=:
s_andn2_b32 s80, 0xffff, s71
s_cbranch_scc0 .LL_139%=
s_sleep 4
buffer_load_dwordx2 v[16:17], v18, s[8:11], s63 offen sc1
s_and_b32 s80, s70, 0x100ff
s_cmp_eq_u32 s80, 0xff
s_cbranch_scc0 .LL_138%=
Zvva 80,4,0
Zvva 84,8,0
Zvva 88,4,216
Zvva 92,8,216
Mvva 80,4,4
Mvva 84,8,4
Mvva 88,4,220
Mvva 92,8,220
Mvva 80,4,8
Mvva 84,8,8
Mvva 88,4,224
Mvva 92,8,224
Mvva 80,4,12
Mvva 84,8,12
Mvva 88,4,228
Mvva 92,8,228
Mvva 80,4,16
Mvva 84,8,16
Mvva 88,4,232
Mvva 92,8,232
Mvva 80,4,20
Mvva 84,8,20
Mvva 88,4,236
Mvva 92,8,236
Mvva 80,4,24
Mvva 84,8,24
Mvva 88,4,240
Mvva 92,8,240
Mvva 80,4,28
Mvva 84,8,28
Mvva 88,4,244
Mvva 92,8,244
s_nop 4
K 160,80,81
K 161,82,83
K 162,84,85
K 163,86,87
v_lshlrev_b32 v80, 16, v160
v_and_b32 v81, 0xffff0000, v160
v_lshlrev_b32 v82, 16, v161
v_and_b32 v83, 0xffff0000, v161
v_lshlrev_b32 v84, 16, v162
v_and_b32 v85, 0xffff0000, v162
v_lshlrev_b32 v86, 16, v163
v_and_b32 v87, 0xffff0000, v163
v_pk_add_f32 v[80:81], v[32:33], v[80:81]
v_pk_add_f32 v[82:83], v[34:35], v[82:83]
v_pk_add_f32 v[84:85], v[36:37], v[84:85]
v_pk_add_f32 v[86:87], v[38:39], v[86:87]
K 160,80,81
K 161,82,83
K 162,84,85
K 163,86,87
v_lshlrev_b32 v80, 16, v160
v_and_b32 v81, 0xffff0000, v160
v_lshlrev_b32 v82, 16, v161
v_and_b32 v83, 0xffff0000, v161
v_lshlrev_b32 v84, 16, v162
v_and_b32 v85, 0xffff0000, v162
v_lshlrev_b32 v86, 16, v163
v_and_b32 v87, 0xffff0000, v163
v_pk_add_f32 v[80:81], v[40:41], v[80:81]
v_pk_add_f32 v[82:83], v[42:43], v[82:83]
v_pk_add_f32 v[84:85], v[44:45], v[84:85]
v_pk_add_f32 v[86:87], v[46:47], v[86:87]
K 80,80,81
K 81,82,83
K 82,84,85
K 83,86,87
buffer_store_dwordx4 v[80:83], v20, s[20:23], s72 offen nt
K 160,88,89
K 161,90,91
K 162,92,93
K 163,94,95
v_lshlrev_b32 v88, 16, v160
v_and_b32 v89, 0xffff0000, v160
v_lshlrev_b32 v90, 16, v161
v_and_b32 v91, 0xffff0000, v161
v_lshlrev_b32 v92, 16, v162
v_and_b32 v93, 0xffff0000, v162
v_lshlrev_b32 v94, 16, v163
v_and_b32 v95, 0xffff0000, v163
v_pk_add_f32 v[88:89], v[64:65], v[88:89]
v_pk_add_f32 v[90:91], v[66:67], v[90:91]
v_pk_add_f32 v[92:93], v[68:69], v[92:93]
v_pk_add_f32 v[94:95], v[70:71], v[94:95]
K 160,88,89
K 161,90,91
K 162,92,93
K 163,94,95
v_lshlrev_b32 v88, 16, v160
v_and_b32 v89, 0xffff0000, v160
v_lshlrev_b32 v90, 16, v161
v_and_b32 v91, 0xffff0000, v161
v_lshlrev_b32 v92, 16, v162
v_and_b32 v93, 0xffff0000, v162
v_lshlrev_b32 v94, 16, v163
v_and_b32 v95, 0xffff0000, v163
v_pk_add_f32 v[88:89], v[72:73], v[88:89]
v_pk_add_f32 v[90:91], v[74:75], v[90:91]
v_pk_add_f32 v[92:93], v[76:77], v[92:93]
v_pk_add_f32 v[94:95], v[78:79], v[94:95]
K 88,88,89
K 89,90,91
K 90,92,93
K 91,94,95
buffer_store_dwordx4 v[88:91], v20, s[20:23], s74 offen nt
s_bitset1_b32 s71, 16
.LL_138%=:
s_add_u32 s84, s84, 1
s_waitcnt vmcnt(0)
v_cmp_eq_u64 vcc, s[12:13], v[16:17]
s_or_b32 s67, vcc_lo, s66
s_cmp_lt_u32 s84, 4194304
s_cbranch_scc1 .LL_137%=
.LL_139%=:
s_bitcmp1_b32 s71, 16
s_cbranch_scc1 .LL_159%=
s_and_b32 s80, s70, 0xff
s_cmp_eq_u32 s80, 0xff
s_cbranch_scc1 .LL_160%=
s_waitcnt vmcnt(0)
.LL_160%=:
Zvva 80,4,0
Zvva 84,8,0
Zvva 88,4,216
Zvva 92,8,216
Mvva 80,4,4
Mvva 84,8,4
Mvva 88,4,220
Mvva 92,8,220
Mvva 80,4,8
Mvva 84,8,8
Mvva 88,4,224
Mvva 92,8,224
Mvva 80,4,12
Mvva 84,8,12
Mvva 88,4,228
Mvva 92,8,228
Mvva 80,4,16
Mvva 84,8,16
Mvva 88,4,232
Mvva 92,8,232
Mvva 80,4,20
Mvva 84,8,20
Mvva 88,4,236
Mvva 92,8,236
Mvva 80,4,24
Mvva 84,8,24
Mvva 88,4,240
Mvva 92,8,240
Mvva 80,4,28
Mvva 84,8,28
Mvva 88,4,244
Mvva 92,8,244
s_nop 4
K 160,80,81
K 161,82,83
K 162,84,85
K 163,86,87
v_lshlrev_b32 v80, 16, v160
v_and_b32 v81, 0xffff0000, v160
v_lshlrev_b32 v82, 16, v161
v_and_b32 v83, 0xffff0000, v161
v_lshlrev_b32 v84, 16, v162
v_and_b32 v85, 0xffff0000, v162
v_lshlrev_b32 v86, 16, v163
v_and_b32 v87, 0xffff0000, v163
v_pk_add_f32 v[80:81], v[32:33], v[80:81]
v_pk_add_f32 v[82:83], v[34:35], v[82:83]
v_pk_add_f32 v[84:85], v[36:37], v[84:85]
v_pk_add_f32 v[86:87], v[38:39], v[86:87]
K 160,80,81
K 161,82,83
K 162,84,85
K 163,86,87
v_lshlrev_b32 v80, 16, v160
v_and_b32 v81, 0xffff0000, v160
v_lshlrev_b32 v82, 16, v161
v_and_b32 v83, 0xffff0000, v161
v_lshlrev_b32 v84, 16, v162
v_and_b32 v85, 0xffff0000, v162
v_lshlrev_b32 v86, 16, v163
v_and_b32 v87, 0xffff0000, v163
v_pk_add_f32 v[80:81], v[40:41], v[80:81]
v_pk_add_f32 v[82:83], v[42:43], v[82:83]
v_pk_add_f32 v[84:85], v[44:45], v[84:85]
v_pk_add_f32 v[86:87], v[46:47], v[86:87]
K 80,80,81
K 81,82,83
K 82,84,85
K 83,86,87
)"
R"(buffer_store_dwordx4 v[80:83], v20, s[20:23], s72 offen nt
K 160,88,89
K 161,90,91
K 162,92,93
K 163,94,95
v_lshlrev_b32 v88, 16, v160
v_and_b32 v89, 0xffff0000, v160
v_lshlrev_b32 v90, 16, v161
v_and_b32 v91, 0xffff0000, v161
v_lshlrev_b32 v92, 16, v162
v_and_b32 v93, 0xffff0000, v162
v_lshlrev_b32 v94, 16, v163
v_and_b32 v95, 0xffff0000, v163
v_pk_add_f32 v[88:89], v[64:65], v[88:89]
v_pk_add_f32 v[90:91], v[66:67], v[90:91]
v_pk_add_f32 v[92:93], v[68:69], v[92:93]
v_pk_add_f32 v[94:95], v[70:71], v[94:95]
K 160,88,89
K 161,90,91
K 162,92,93
K 163,94,95
v_lshlrev_b32 v88, 16, v160
v_and_b32 v89, 0xffff0000, v160
v_lshlrev_b32 v90, 16, v161
v_and_b32 v91, 0xffff0000, v161
v_lshlrev_b32 v92, 16, v162
v_and_b32 v93, 0xffff0000, v162
v_lshlrev_b32 v94, 16, v163
v_and_b32 v95, 0xffff0000, v163
v_pk_add_f32 v[88:89], v[72:73], v[88:89]
v_pk_add_f32 v[90:91], v[74:75], v[90:91]
v_pk_add_f32 v[92:93], v[76:77], v[92:93]
v_pk_add_f32 v[94:95], v[78:79], v[94:95]
K 88,88,89
K 89,90,91
K 90,92,93
K 91,94,95
buffer_store_dwordx4 v[88:91], v20, s[20:23], s74 offen nt
s_waitcnt vmcnt(2)
Zvva 80,4,32
Zvva 84,8,32
Mvva 80,4,36
Mvva 84,8,36
Mvva 80,4,40
Mvva 84,8,40
Mvva 80,4,44
Mvva 84,8,44
Mvva 80,4,48
Mvva 84,8,48
Mvva 80,4,52
Mvva 84,8,52
Mvva 80,4,56
Mvva 84,8,56
Mvva 80,4,60
Mvva 84,8,60
s_nop 6
K 160,80,81
K 161,82,83
K 162,84,85
K 163,86,87
v_lshlrev_b32 v80, 16, v160
v_and_b32 v81, 0xffff0000, v160
v_lshlrev_b32 v82, 16, v161
v_and_b32 v83, 0xffff0000, v161
v_lshlrev_b32 v84, 16, v162
v_and_b32 v85, 0xffff0000, v162
v_lshlrev_b32 v86, 16, v163
v_and_b32 v87, 0xffff0000, v163
v_pk_add_f32 v[80:81], v[48:49], v[80:81]
v_pk_add_f32 v[82:83], v[50:51], v[82:83]
v_pk_add_f32 v[84:85], v[52:53], v[84:85]
v_pk_add_f32 v[86:87], v[54:55], v[86:87]
K 160,80,81
K 161,82,83
K 162,84,85
K 163,86,87
v_lshlrev_b32 v80, 16, v160
v_and_b32 v81, 0xffff0000, v160
v_lshlrev_b32 v82, 16, v161
v_and_b32 v83, 0xffff0000, v161
v_lshlrev_b32 v84, 16, v162
v_and_b32 v85, 0xffff0000, v162
v_lshlrev_b32 v86, 16, v163
v_and_b32 v87, 0xffff0000, v163
v_pk_add_f32 v[80:81], v[56:57], v[80:81]
v_pk_add_f32 v[82:83], v[58:59], v[82:83]
v_pk_add_f32 v[84:85], v[60:61], v[84:85]
v_pk_add_f32 v[86:87], v[62:63], v[86:87]
K 80,80,81
K 81,82,83
K 82,84,85
K 83,86,87
buffer_store_dwordx4 v[80:83], v20, s[20:23], s73 offen nt
s_branch .LL_110%=
.LL_159%=:
s_waitcnt vmcnt(0)
Zvva 80,4,32
Zvva 84,8,32
Mvva 80,4,36
Mvva 84,8,36
Mvva 80,4,40
Mvva 84,8,40
Mvva 80,4,44
Mvva 84,8,44
Mvva 80,4,48
Mvva 84,8,48
Mvva 80,4,52
Mvva 84,8,52
Mvva 80,4,56
Mvva 84,8,56
Mvva 80,4,60
Mvva 84,8,60
s_nop 6
K 160,80,81
K 161,82,83
K 162,84,85
K 163,86,87
v_lshlrev_b32 v80, 16, v160
v_and_b32 v81, 0xffff0000, v160
v_lshlrev_b32 v82, 16, v161
v_and_b32 v83, 0xffff0000, v161
v_lshlrev_b32 v84, 16, v162
v_and_b32 v85, 0xffff0000, v162
v_lshlrev_b32 v86, 16, v163
v_and_b32 v87, 0xffff0000, v163
v_pk_add_f32 v[80:81], v[48:49], v[80:81]
v_pk_add_f32 v[82:83], v[50:51], v[82:83]
v_pk_add_f32 v[84:85], v[52:53], v[84:85]
v_pk_add_f32 v[86:87], v[54:55], v[86:87]
K 160,80,81
K 161,82,83
K 162,84,85
K 163,86,87
v_lshlrev_b32 v80, 16, v160
v_and_b32 v81, 0xffff0000, v160
v_lshlrev_b32 v82, 16, v161
v_and_b32 v83, 0xffff0000, v161
v_lshlrev_b32 v84, 16, v162
v_and_b32 v85, 0xffff0000, v162
v_lshlrev_b32 v86, 16, v163
v_and_b32 v87, 0xffff0000, v163
v_pk_add_f32 v[80:81], v[56:57], v[80:81]
v_pk_add_f32 v[82:83], v[58:59], v[82:83]
v_pk_add_f32 v[84:85], v[60:61], v[84:85]
v_pk_add_f32 v[86:87], v[62:63], v[86:87]
K 80,80,81
K 81,82,83
K 82,84,85
K 83,86,87
buffer_store_dwordx4 v[80:83], v20, s[20:23], s73 offen nt
s_branch .LL_110%=
.LL_112%=:
s_mov_b32 s84, 0
.LL_161%=:
s_mov_b32 s70, s71
s_andn2_b32 s69, s67, s71
s_and_b32 s69, s69, 0xffff
s_cbranch_scc0 .LL_164%=
s_and_b32 s80, s69, 255
s_cbranch_scc0 .LL_165%=
s_bitcmp1_b32 s69, 0
s_cbranch_scc0 .LL_166%=
buffer_load_dwordx4 a[0:3], v1, s[8:11], s78 offen offset:0 sc1
.LL_166%=:
s_bitcmp1_b32 s69, 1
s_cbranch_scc0 .LL_167%=
buffer_load_dwordx4 a[4:7], v1, s[8:11], s78 offen offset:1024 sc1
.LL_167%=:
s_bitcmp1_b32 s69, 2
s_cbranch_scc0 .LL_168%=
buffer_load_dwordx4 a[8:11], v1, s[8:11], s78 offen offset:2048 sc1
.LL_168%=:
s_bitcmp1_b32 s69, 3
s_cbranch_scc0 .LL_169%=
buffer_load_dwordx4 a[12:15], v1, s[8:11], s78 offen offset:3072 sc1
.LL_169%=:
s_bitcmp1_b32 s69, 4
s_cbranch_scc0 .LL_170%=
buffer_load_dwordx4 a[16:19], v19, s[8:11], s78 offen offset:0 sc1
.LL_170%=:
s_bitcmp1_b32 s69, 5
s_cbranch_scc0 .LL_171%=
buffer_load_dwordx4 a[20:23], v19, s[8:11], s78 offen offset:1024 sc1
.LL_171%=:
s_bitcmp1_b32 s69, 6
s_cbranch_scc0 .LL_172%=
buffer_load_dwordx4 a[24:27], v19, s[8:11], s78 offen offset:2048 sc1
.LL_172%=:
s_bitcmp1_b32 s69, 7
s_cbranch_scc0 .LL_173%=
buffer_load_dwordx4 a[28:31], v19, s[8:11], s78 offen offset:3072 sc1
.LL_173%=:
.LL_165%=:
s_and_b32 s80, s69, 65280
s_cbranch_scc0 .LL_174%=
s_bitcmp1_b32 s69, 8
s_cbranch_scc0 .LL_175%=
buffer_load_dwordx4 a[32:35], v1, s[8:11], s79 offen offset:0 sc1
buffer_load_dwordx4 a[216:219], v1, s[8:11], s65 offen offset:0 sc1
.LL_175%=:
s_bitcmp1_b32 s69, 9
s_cbranch_scc0 .LL_176%=
buffer_load_dwordx4 a[36:39], v1, s[8:11], s79 offen offset:1024 sc1
buffer_load_dwordx4 a[220:223], v1, s[8:11], s65 offen offset:1024 sc1
.LL_176%=:
s_bitcmp1_b32 s69, 10
s_cbranch_scc0 .LL_177%=
buffer_load_dwordx4 a[40:43], v1, s[8:11], s79 offen offset:2048 sc1
buffer_load_dwordx4 a[224:227], v1, s[8:11], s65 offen offset:2048 sc1
.LL_177%=:
s_bitcmp1_b32 s69, 11
s_cbranch_scc0 .LL_178%=
buffer_load_dwordx4 a[44:47], v1, s[8:11], s79 offen offset:3072 sc1
buffer_load_dwordx4 a[228:231], v1, s[8:11], s65 offen offset:3072 sc1
.LL_178%=:
s_bitcmp1_b32 s69, 12
s_cbranch_scc0 .LL_179%=
buffer_load_dwordx4 a[48:51], v19, s[8:11], s79 offen offset:0 sc1
buffer_load_dwordx4 a[232:235], v19, s[8:11], s65 offen offset:0 sc1
.LL_179%=:
s_bitcmp1_b32 s69, 13
s_cbranch_scc0 .LL_180%=
buffer_load_dwordx4 a[52:55], v19, s[8:11], s79 offen offset:1024 sc1
buffer_load_dwordx4 a[236:239], v19, s[8:11], s65 offen offset:1024 sc1
.LL_180%=:
s_bitcmp1_b32 s69, 14
s_cbranch_scc0 .LL_181%=
buffer_load_dwordx4 a[56:59], v19, s[8:11], s79 offen offset:2048 sc1
buffer_load_dwordx4 a[240:243], v19, s[8:11], s65 offen offset:2048 sc1
.LL_181%=:
s_bitcmp1_b32 s69, 15
s_cbranch_scc0 .LL_182%=
buffer_load_dwordx4 a[60:63], v19, s[8:11], s79 offen offset:3072 sc1
buffer_load_dwordx4 a[244:247], v19, s[8:11], s65 offen offset:3072 sc1
.LL_182%=:
.LL_174%=:
s_or_b32 s71, s71, s69
.LL_164%=:
s_andn2_b32 s80, 0xffff, s71
s_cbranch_scc0 .LL_163%=
s_sleep 4
buffer_load_dwordx2 v[16:17], v18, s[8:11], s63 offen sc1
s_and_b32 s80, s70, 0x100ff
s_cmp_eq_u32 s80, 0xff
s_cbranch_scc0 .LL_162%=
Zvva 80,4,0
Zvva 84,8,0
Mvva 80,4,4
Mvva 84,8,4
Mvva 80,4,8
Mvva 84,8,8
Mvva 80,4,12
Mvva 84,8,12
Mvva 80,4,16
Mvva 84,8,16
Mvva 80,4,20
Mvva 84,8,20
Mvva 80,4,24
Mvva 84,8,24
Mvva 80,4,28
Mvva 84,8,28
s_nop 6
K 160,80,81
K 161,82,83
K 162,84,85
K 163,86,87
v_lshlrev_b32 v80, 16, v160
v_and_b32 v81, 0xffff0000, v160
v_lshlrev_b32 v82, 16, v161
v_and_b32 v83, 0xffff0000, v161
v_lshlrev_b32 v84, 16, v162
v_and_b32 v85, 0xffff0000, v162
v_lshlrev_b32 v86, 16, v163
v_and_b32 v87, 0xffff0000, v163
v_pk_add_f32 v[80:81], v[32:33], v[80:81]
v_pk_add_f32 v[82:83], v[34:35], v[82:83]
v_pk_add_f32 v[84:85], v[36:37], v[84:85]
v_pk_add_f32 v[86:87], v[38:39], v[86:87]
K 160,80,81
K 161,82,83
K 162,84,85
K 163,86,87
v_lshlrev_b32 v80, 16, v160
v_and_b32 v81, 0xffff0000, v160
v_lshlrev_b32 v82, 16, v161
v_and_b32 v83, 0xffff0000, v161
v_lshlrev_b32 v84, 16, v162
v_and_b32 v85, 0xffff0000, v162
v_lshlrev_b32 v86, 16, v163
v_and_b32 v87, 0xffff0000, v163
v_pk_add_f32 v[80:81], v[40:41], v[80:81]
v_pk_add_f32 v[82:83], v[42:43], v[82:83]
v_pk_add_f32 v[84:85], v[44:45], v[84:85]
v_pk_add_f32 v[86:87], v[46:47], v[86:87]
K 80,80,81
K 81,82,83
K 82,84,85
K 83,86,87
buffer_store_dwordx4 v[80:83], v20, s[20:23], s72 offen nt
s_bitset1_b32 s71, 16
.LL_162%=:
s_add_u32 s84, s84, 1
s_waitcnt vmcnt(0)
v_cmp_eq_u64 vcc, s[12:13], v[16:17]
s_or_b32 s67, vcc_lo, s66
s_cmp_lt_u32 s84, 4194304
s_cbranch_scc1 .LL_161%=
.LL_163%=:
s_bitcmp1_b32 s71, 16
s_cbranch_scc1 .LL_183%=
s_and_b32 s80, s70, 0xff
s_cmp_eq_u32 s80, 0xff
s_cbranch_scc1 .LL_184%=
s_waitcnt vmcnt(0)
.LL_184%=:
Zvva 80,4,0
Zvva 84,8,0
Mvva 80,4,4
Mvva 84,8,4
Mvva 80,4,8
Mvva 84,8,8
Mvva 80,4,12
Mvva 84,8,12
Mvva 80,4,16
Mvva 84,8,16
Mvva 80,4,20
Mvva 84,8,20
Mvva 80,4,24
Mvva 84,8,24
Mvva 80,4,28
Mvva 84,8,28
s_nop 6
K 160,80,81
K 161,82,83
K 162,84,85
K 163,86,87
v_lshlrev_b32 v80, 16, v160
v_and_b32 v81, 0xffff0000, v160
v_lshlrev_b32 v82, 16, v161
v_and_b32 v83, 0xffff0000, v161
v_lshlrev_b32 v84, 16, v162
v_and_b32 v85, 0xffff0000, v162
v_lshlrev_b32 v86, 16, v163
v_and_b32 v87, 0xffff0000, v163
v_pk_add_f32 v[80:81], v[32:33], v[80:81]
v_pk_add_f32 v[82:83], v[34:35], v[82:83]
v_pk_add_f32 v[84:85], v[36:37], v[84:85]
v_pk_add_f32 v[86:87], v[38:39], v[86:87]
K 160,80,81
K 161,82,83
K 162,84,85
K 163,86,87
v_lshlrev_b32 v80, 16, v160
v_and_b32 v81, 0xffff0000, v160
v_lshlrev_b32 v82, 16, v161
v_and_b32 v83, 0xffff0000, v161
v_lshlrev_b32 v84, 16, v162
v_and_b32 v85, 0xffff0000, v162
v_lshlrev_b32 v86, 16, v163
v_and_b32 v87, 0xffff0000, v163
v_pk_add_f32 v[80:81], v[40:41], v[80:81]
v_pk_add_f32 v[82:83], v[42:43], v[82:83]
v_pk_add_f32 v[84:85], v[44:45], v[84:85]
v_pk_add_f32 v[86:87], v[46:47], v[86:87]
K 80,80,81
K 81,82,83
K 82,84,85
K 83,86,87
buffer_store_dwordx4 v[80:83], v20, s[20:23], s72 offen nt
s_waitcnt vmcnt(1)
s_nop 0
Zvva 80,4,32
Zvva 84,8,32
Zvva 88,4,216
Zvva 92,8,216
Mvva 80,4,36
Mvva 84,8,36
Mvva 88,4,220
Mvva 92,8,220
Mvva 80,4,40
Mvva 84,8,40
Mvva 88,4,224
Mvva 92,8,224
Mvva 80,4,44
Mvva 84,8,44
Mvva 88,4,228
Mvva 92,8,228
Mvva 80,4,48
Mvva 84,8,48
Mvva 88,4,232
Mvva 92,8,232
Mvva 80,4,52
Mvva 84,8,52
Mvva 88,4,236
Mvva 92,8,236
Mvva 80,4,56
Mvva 84,8,56
Mvva 88,4,240
Mvva 92,8,240
Mvva 80,4,60
)"
R"(Mvva 84,8,60
Mvva 88,4,244
Mvva 92,8,244
s_nop 4
K 160,80,81
K 161,82,83
K 162,84,85
K 163,86,87
v_lshlrev_b32 v80, 16, v160
v_and_b32 v81, 0xffff0000, v160
v_lshlrev_b32 v82, 16, v161
v_and_b32 v83, 0xffff0000, v161
v_lshlrev_b32 v84, 16, v162
v_and_b32 v85, 0xffff0000, v162
v_lshlrev_b32 v86, 16, v163
v_and_b32 v87, 0xffff0000, v163
v_pk_add_f32 v[80:81], v[48:49], v[80:81]
v_pk_add_f32 v[82:83], v[50:51], v[82:83]
v_pk_add_f32 v[84:85], v[52:53], v[84:85]
v_pk_add_f32 v[86:87], v[54:55], v[86:87]
K 160,80,81
K 161,82,83
K 162,84,85
K 163,86,87
v_lshlrev_b32 v80, 16, v160
v_and_b32 v81, 0xffff0000, v160
v_lshlrev_b32 v82, 16, v161
v_and_b32 v83, 0xffff0000, v161
v_lshlrev_b32 v84, 16, v162
v_and_b32 v85, 0xffff0000, v162
v_lshlrev_b32 v86, 16, v163
v_and_b32 v87, 0xffff0000, v163
v_pk_add_f32 v[80:81], v[56:57], v[80:81]
v_pk_add_f32 v[82:83], v[58:59], v[82:83]
v_pk_add_f32 v[84:85], v[60:61], v[84:85]
v_pk_add_f32 v[86:87], v[62:63], v[86:87]
K 80,80,81
K 81,82,83
K 82,84,85
K 83,86,87
buffer_store_dwordx4 v[80:83], v20, s[20:23], s73 offen nt
K 160,88,89
K 161,90,91
K 162,92,93
K 163,94,95
v_lshlrev_b32 v88, 16, v160
v_and_b32 v89, 0xffff0000, v160
v_lshlrev_b32 v90, 16, v161
v_and_b32 v91, 0xffff0000, v161
v_lshlrev_b32 v92, 16, v162
v_and_b32 v93, 0xffff0000, v162
v_lshlrev_b32 v94, 16, v163
v_and_b32 v95, 0xffff0000, v163
v_pk_add_f32 v[88:89], v[64:65], v[88:89]
v_pk_add_f32 v[90:91], v[66:67], v[90:91]
v_pk_add_f32 v[92:93], v[68:69], v[92:93]
v_pk_add_f32 v[94:95], v[70:71], v[94:95]
K 160,88,89
K 161,90,91
K 162,92,93
K 163,94,95
v_lshlrev_b32 v88, 16, v160
v_and_b32 v89, 0xffff0000, v160
v_lshlrev_b32 v90, 16, v161
v_and_b32 v91, 0xffff0000, v161
v_lshlrev_b32 v92, 16, v162
v_and_b32 v93, 0xffff0000, v162
v_lshlrev_b32 v94, 16, v163
v_and_b32 v95, 0xffff0000, v163
v_pk_add_f32 v[88:89], v[72:73], v[88:89]
v_pk_add_f32 v[90:91], v[74:75], v[90:91]
v_pk_add_f32 v[92:93], v[76:77], v[92:93]
v_pk_add_f32 v[94:95], v[78:79], v[94:95]
K 88,88,89
K 89,90,91
K 90,92,93
K 91,94,95
buffer_store_dwordx4 v[88:91], v20, s[20:23], s74 offen nt
s_branch .LL_110%=
.LL_183%=:
s_waitcnt vmcnt(0)
Zvva 80,4,32
Zvva 84,8,32
Zvva 88,4,216
Zvva 92,8,216
Mvva 80,4,36
Mvva 84,8,36
Mvva 88,4,220
Mvva 92,8,220
Mvva 80,4,40
Mvva 84,8,40
Mvva 88,4,224
Mvva 92,8,224
Mvva 80,4,44
Mvva 84,8,44
Mvva 88,4,228
Mvva 92,8,228
Mvva 80,4,48
Mvva 84,8,48
Mvva 88,4,232
Mvva 92,8,232
Mvva 80,4,52
Mvva 84,8,52
Mvva 88,4,236
Mvva 92,8,236
Mvva 80,4,56
Mvva 84,8,56
Mvva 88,4,240
Mvva 92,8,240
Mvva 80,4,60
Mvva 84,8,60
Mvva 88,4,244
Mvva 92,8,244
s_nop 4
K 160,80,81
K 161,82,83
K 162,84,85
K 163,86,87
v_lshlrev_b32 v80, 16, v160
v_and_b32 v81, 0xffff0000, v160
v_lshlrev_b32 v82, 16, v161
v_and_b32 v83, 0xffff0000, v161
v_lshlrev_b32 v84, 16, v162
v_and_b32 v85, 0xffff0000, v162
v_lshlrev_b32 v86, 16, v163
v_and_b32 v87, 0xffff0000, v163
v_pk_add_f32 v[80:81], v[48:49], v[80:81]
v_pk_add_f32 v[82:83], v[50:51], v[82:83]
v_pk_add_f32 v[84:85], v[52:53], v[84:85]
v_pk_add_f32 v[86:87], v[54:55], v[86:87]
K 160,80,81
K 161,82,83
K 162,84,85
K 163,86,87
v_lshlrev_b32 v80, 16, v160
v_and_b32 v81, 0xffff0000, v160
v_lshlrev_b32 v82, 16, v161
v_and_b32 v83, 0xffff0000, v161
v_lshlrev_b32 v84, 16, v162
v_and_b32 v85, 0xffff0000, v162
v_lshlrev_b32 v86, 16, v163
v_and_b32 v87, 0xffff0000, v163
v_pk_add_f32 v[80:81], v[56:57], v[80:81]
v_pk_add_f32 v[82:83], v[58:59], v[82:83]
v_pk_add_f32 v[84:85], v[60:61], v[84:85]
v_pk_add_f32 v[86:87], v[62:63], v[86:87]
K 80,80,81
K 81,82,83
K 82,84,85
K 83,86,87
buffer_store_dwordx4 v[80:83], v20, s[20:23], s73 offen nt
K 160,88,89
K 161,90,91
K 162,92,93
K 163,94,95
v_lshlrev_b32 v88, 16, v160
v_and_b32 v89, 0xffff0000, v160
v_lshlrev_b32 v90, 16, v161
v_and_b32 v91, 0xffff0000, v161
v_lshlrev_b32 v92, 16, v162
v_and_b32 v93, 0xffff0000, v162
v_lshlrev_b32 v94, 16, v163
v_and_b32 v95, 0xffff0000, v163
v_pk_add_f32 v[88:89], v[64:65], v[88:89]
v_pk_add_f32 v[90:91], v[66:67], v[90:91]
v_pk_add_f32 v[92:93], v[68:69], v[92:93]
v_pk_add_f32 v[94:95], v[70:71], v[94:95]
K 160,88,89
K 161,90,91
K 162,92,93
K 163,94,95
v_lshlrev_b32 v88, 16, v160
v_and_b32 v89, 0xffff0000, v160
v_lshlrev_b32 v90, 16, v161
v_and_b32 v91, 0xffff0000, v161
v_lshlrev_b32 v92, 16, v162
v_and_b32 v93, 0xffff0000, v162
v_lshlrev_b32 v94, 16, v163
v_and_b32 v95, 0xffff0000, v163
v_pk_add_f32 v[88:89], v[72:73], v[88:89]
v_pk_add_f32 v[90:91], v[74:75], v[90:91]
v_pk_add_f32 v[92:93], v[76:77], v[92:93]
v_pk_add_f32 v[94:95], v[78:79], v[94:95]
K 88,88,89
K 89,90,91
K 90,92,93
K 91,94,95
buffer_store_dwordx4 v[88:91], v20, s[20:23], s74 offen nt
.LL_110%=:
s_lshl_b32 s80, s15, 2
s_add_u32 s80, s80, s14
s_cmp_eq_u32 s80, 4
s_cbranch_scc0 .LL_185%=
v_mov_b64 v[28:29], s[12:13]
s_mov_b64 exec, 1
buffer_store_dwordx2 v[28:29], off, s[8:11], 0 sc1
s_mov_b64 exec, -1
.LL_185%=:
s_branch .Lend_186%=
.Lup_103%=:
s_waitcnt vmcnt(3)
s_mov_b32 s84, 0
.LL_105%=:
v_min3_u32 v176, v161, v163, v165
v_max3_u32 v177, v161, v163, v165
v_min3_u32 v176, v176, v167, v169
v_max3_u32 v177, v177, v167, v169
v_min3_u32 v176, v176, v171, v173
v_max3_u32 v177, v177, v171, v173
v_min_u32 v176, v176, v175
v_max_u32 v177, v177, v175
v_cmp_eq_u32 vcc, s13, v176
v_cmp_eq_u32_e64 s[86:87], s13, v177
s_and_b64 vcc, vcc, s[86:87]
s_cmp_eq_u64 vcc, -1
s_cbranch_scc1 .LL_106%=
s_add_u32 s84, s84, 1
s_cmp_lt_u32 s84, 1048576
s_cbranch_scc1 .LL_107%=
.LL_106%=:
v_readlane_b32 s70, v12, s69
v_bfe_u32 v2, v1, 4, 4
s_nop 2
s_lshr_b32 s80, s70, 4
s_and_b32 s70, s70, 15
v_cmp_eq_u32 vcc, s70, v2
s_cmp_eq_u32 s80, s61
s_cselect_b64 s[86:87], vcc, 0
s_cselect_b64 s[88:89], 0, vcc
s_bitcmp1_b32 s14, 0
s_cselect_b64 vcc, s[88:89], s[86:87]
v_lshlrev_b32 v176, 16, v160
v_and_b32 v177, 0xffff0000, v160
v_lshlrev_b32 v178, 16, v162
v_and_b32 v179, 0xffff0000, v162
v_lshlrev_b32 v180, 16, v164
v_and_b32 v181, 0xffff0000, v164
v_lshlrev_b32 v182, 16, v166
v_and_b32 v183, 0xffff0000, v166
A 176,176,40
A 177,177,41
A 178,178,42
A 179,179,43
A 180,180,44
A 181,181,45
A 182,182,46
A 183,183,47
K 184,176,177
K 185,178,179
K 186,180,181
K 187,182,183
v_lshlrev_b32 v176, 16, v184
v_and_b32 v177, 0xffff0000, v184
v_lshlrev_b32 v178, 16, v185
v_and_b32 v179, 0xffff0000, v185
v_lshlrev_b32 v180, 16, v186
v_and_b32 v181, 0xffff0000, v186
v_lshlrev_b32 v182, 16, v187
v_and_b32 v183, 0xffff0000, v187
v_cndmask_b32_e64 v40, v40, v176, s[86:87]
v_cndmask_b32_e64 v41, v41, v177, s[86:87]
v_cndmask_b32_e64 v42, v42, v178, s[86:87]
v_cndmask_b32_e64 v43, v43, v179, s[86:87]
v_cndmask_b32_e64 v44, v44, v180, s[86:87]
v_cndmask_b32_e64 v45, v45, v181, s[86:87]
v_cndmask_b32_e64 v46, v46, v182, s[86:87]
v_cndmask_b32_e64 v47, v47, v183, s[86:87]
v_lshlrev_b32 v176, 16, v160
v_and_b32 v177, 0xffff0000, v160
v_lshlrev_b32 v178, 16, v162
v_and_b32 v179, 0xffff0000, v162
v_lshlrev_b32 v180, 16, v164
v_and_b32 v181, 0xffff0000, v164
v_lshlrev_b32 v182, 16, v166
v_and_b32 v183, 0xffff0000, v166
A 176,176,56
A 177,177,57
A 178,178,58
A 179,179,59
A 180,180,60
A 181,181,61
A 182,182,62
A 183,183,63
K 184,176,177
K 185,178,179
K 186,180,181
K 187,182,183
v_lshlrev_b32 v176, 16, v184
v_and_b32 v177, 0xffff0000, v184
v_lshlrev_b32 v178, 16, v185
v_and_b32 v179, 0xffff0000, v185
v_lshlrev_b32 v180, 16, v186
v_and_b32 v181, 0xffff0000, v186
v_lshlrev_b32 v182, 16, v187
v_and_b32 v183, 0xffff0000, v187
v_cndmask_b32_e64 v56, v56, v176, s[88:89]
v_cndmask_b32_e64 v57, v57, v177, s[88:89]
v_cndmask_b32_e64 v58, v58, v178, s[88:89]
v_cndmask_b32_e64 v59, v59, v179, s[88:89]
v_cndmask_b32_e64 v60, v60, v180, s[88:89]
v_cndmask_b32_e64 v61, v61, v181, s[88:89]
v_cndmask_b32_e64 v62, v62, v182, s[88:89]
v_cndmask_b32_e64 v63, v63, v183, s[88:89]
v_lshlrev_b32 v176, 16, v168
v_and_b32 v177, 0xffff0000, v168
v_lshlrev_b32 v178, 16, v170
v_and_b32 v179, 0xffff0000, v170
v_lshlrev_b32 v180, 16, v172
v_and_b32 v181, 0xffff0000, v172
v_lshlrev_b32 v182, 16, v174
v_and_b32 v183, 0xffff0000, v174
A 176,176,72
A 177,177,73
A 178,178,74
A 179,179,75
A 180,180,76
A 181,181,77
A 182,182,78
A 183,183,79
K 184,176,177
K 185,178,179
K 186,180,181
K 187,182,183
v_lshlrev_b32 v176, 16, v184
v_and_b32 v177, 0xffff0000, v184
v_lshlrev_b32 v178, 16, v185
v_and_b32 v179, 0xffff0000, v185
v_lshlrev_b32 v180, 16, v186
v_and_b32 v181, 0xffff0000, v186
v_lshlrev_b32 v182, 16, v187
v_and_b32 v183, 0xffff0000, v187
v_cndmask_b32_e64 v72, v72, v176, vcc
v_cndmask_b32_e64 v73, v73, v177, vcc
v_cndmask_b32_e64 v74, v74, v178, vcc
v_cndmask_b32_e64 v75, v75, v179, vcc
v_cndmask_b32_e64 v76, v76, v180, vcc
v_cndmask_b32_e64 v77, v77, v181, vcc
v_cndmask_b32_e64 v78, v78, v182, vcc
v_cndmask_b32_e64 v79, v79, v183, vcc
s_bitset0_b32 s68, s69
s_cmp_eq_u32 s68, 0
s_cbranch_scc1 .Lupr_104%=
s_ff1_i32_b32 s69, s68
.LL_107%=:
s_cmp_eq_u32 s64, 0
s_cbranch_scc0 .LL_108%=
s_waitcnt vmcnt(0)
v_lshlrev_b32 v2, 2, v1
v_mov_b64 v[28:29], s[12:13]
s_mov_b64 exec, 3
buffer_store_dwordx2 v[28:29], v2, s[8:11], s53 offen offset:512 sc1
s_mov_b64 exec, -1
s_mov_b32 s64, 1
.LL_108%=:
s_mul_i32 s66, s69, 20
s_mul_i32 s67, s58, 5
s_add_u32 s66, s66, s67
s_lshl_b32 s66, s66, 7
s_add_u32 s66, s66, 98304
s_lshl_b32 s67, s14, 7
s_add_u32 s67, s67, s66
buffer_load_dwordx4 v[160:163], v3, s[8:11], s67 offen offset:0 sc1
buffer_load_dwordx4 v[164:167], v3, s[8:11], s67 offen offset:16 sc1
buffer_load_dwordx4 v[168:171], v3, s[8:11], s66 offen offset:512 sc1
buffer_load_dwordx4 v[172:175], v3, s[8:11], s66 offen offset:528 sc1
s_waitcnt vmcnt(0)
s_branch .LL_105%=
.Lspin_56%=:
s_mov_b32 s84, 0
.Llp_58%=:
s_sleep 2
v_mov_b32 v28, s74
buffer_load_dwordx2 v[28:29], v28, s[8:11], 0 offen offset:3584 sc1
s_waitcnt vmcnt(0)
s_add_u32 s84, s84, 1
v_xor_b32 v28, s12, v28
v_xor_b32 v29, s13, v29
v_lshrrev_b32 v248, 11, v28
v_or_b32 v29, v29, v248
v_cmp_eq_u32 vcc, 0, v29
s_or_b64 vcc, vcc, s[72:73]
s_cmp_eq_u64 vcc, -1
s_cbranch_scc1 .Lok_57%=
s_cmp_lt_u32 s84, 1048576
s_cbranch_scc1 .Llp_58%=
s_branch .Lok_57%=
.Lspin_61%=:
s_mov_b32 s84, 0
.Llp_63%=:
s_sleep 2
v_bfe_u32 v28, v1, 4, 2
v_lshlrev_b32 v28, 3, v28
s_lshl_b32 s80, s56, 9
s_add_u32 s80, s80, 4096
buffer_load_dwordx2 v[28:29], v28, s[8:11], s80 offen sc1
s_waitcnt vmcnt(0)
s_add_u32 s84, s84, 1
v_cmp_eq_u64 vcc, s[12:13], v[28:29]
s_cmp_eq_u64 vcc, -1
s_cbranch_scc1 .Lok_62%=
)"
R"(s_cmp_lt_u32 s84, 1048576
s_cbranch_scc1 .Llp_63%=
s_branch .Lok_62%=
.Lspin_64%=:
s_mov_b32 s84, 0
.Llp_66%=:
s_sleep 2
v_bfe_u32 v28, v1, 4, 4
v_lshlrev_b32 v28, 3, v28
s_lshl_b32 s80, s56, 9
s_add_u32 s80, s80, 4096
buffer_load_dwordx2 v[28:29], v28, s[8:11], s80 offen sc1
s_waitcnt vmcnt(0)
s_add_u32 s84, s84, 1
v_cmp_eq_u64 vcc, s[12:13], v[28:29]
s_cmp_eq_u64 vcc, -1
s_cbranch_scc1 .Lok_65%=
s_cmp_lt_u32 s84, 1048576
s_cbranch_scc1 .Llp_66%=
s_branch .Lok_65%=
.Lspin_67%=:
s_mov_b32 s84, 0
.Llp_69%=:
s_sleep 2
v_bfe_u32 v28, v1, 4, 4
v_lshlrev_b32 v28, 3, v28
s_lshl_b32 s80, s56, 9
s_add_u32 s80, s80, 4224
buffer_load_dwordx2 v[28:29], v28, s[8:11], s80 offen sc1
s_waitcnt vmcnt(0)
s_add_u32 s84, s84, 1
v_cmp_eq_u64 vcc, s[12:13], v[28:29]
s_cmp_eq_u64 vcc, -1
s_cbranch_scc1 .Lok_68%=
s_cmp_lt_u32 s84, 1048576
s_cbranch_scc1 .Llp_69%=
s_branch .Lok_68%=
.Lspin_70%=:
s_mov_b32 s84, 0
.Llp_72%=:
s_sleep 2
v_bfe_u32 v28, v1, 4, 4
v_lshlrev_b32 v28, 3, v28
s_lshl_b32 s80, s56, 9
s_add_u32 s80, s80, 4352
buffer_load_dwordx2 v[28:29], v28, s[8:11], s80 offen sc1
s_waitcnt vmcnt(0)
s_add_u32 s84, s84, 1
v_cmp_eq_u64 vcc, s[12:13], v[28:29]
s_cmp_eq_u64 vcc, -1
s_cbranch_scc1 .Lok_71%=
s_cmp_lt_u32 s84, 1048576
s_cbranch_scc1 .Llp_72%=
s_branch .Lok_71%=
.Lspin_73%=:
s_mov_b32 s84, 0
.Llp_75%=:
s_sleep 2
v_bfe_u32 v28, v1, 4, 4
v_lshlrev_b32 v28, 3, v28
s_lshl_b32 s80, s56, 9
s_add_u32 s80, s80, 4480
buffer_load_dwordx2 v[28:29], v28, s[8:11], s80 offen sc1
s_waitcnt vmcnt(0)
s_add_u32 s84, s84, 1
v_cmp_eq_u64 vcc, s[12:13], v[28:29]
s_cmp_eq_u64 vcc, -1
s_cbranch_scc1 .Lok_74%=
s_cmp_lt_u32 s84, 1048576
s_cbranch_scc1 .Llp_75%=
s_branch .Lok_74%=
.Lspin_76%=:
s_mov_b32 s84, 0
.Llp_78%=:
s_sleep 2
buffer_load_dwordx2 v[8:9], v4, s[8:11], 0 offen offset:2560 sc1
buffer_load_dwordx2 v[10:11], v5, s[8:11], 0 offen offset:3072 sc1
buffer_load_dwordx2 v[12:13], v6, s[8:11], 0 offen offset:3584 sc1
s_waitcnt vmcnt(0)
s_add_u32 s84, s84, 1
v_cmp_eq_u64 vcc, s[12:13], v[8:9]
v_cmp_eq_u64_e64 s[86:87], s[12:13], v[10:11]
s_and_b64 vcc, vcc, s[86:87]
s_or_b64 vcc, vcc, s[20:21]
s_cmp_eq_u64 vcc, -1
s_cbranch_scc1 .Lok_77%=
s_cmp_lt_u32 s84, 1048576
s_cbranch_scc1 .Llp_78%=
s_branch .Lok_77%=
.Lmk_79%=:
s_nop 7
v_cndmask_b32_e64 v168, v241, v168, s[72:73]
v_cndmask_b32_e64 v169, v241, v169, s[74:75]
v_cndmask_b32_e64 v170, v241, v170, s[76:77]
v_cndmask_b32_e64 v171, v241, v171, s[78:79]
v_cndmask_b32_e64 v188, v241, v188, s[72:73]
v_cndmask_b32_e64 v189, v241, v189, s[74:75]
v_cndmask_b32_e64 v190, v241, v190, s[76:77]
v_cndmask_b32_e64 v191, v241, v191, s[78:79]
v_mov_b32 v184, v241
v_mov_b32 v185, v241
v_mov_b32 v186, v241
v_mov_b32 v187, v241
s_branch .Lmkr_80%=
.Lspin_84%=:
s_mov_b32 s84, 0
.Llp_86%=:
s_sleep 2
v_bfe_u32 v28, v1, 4, 2
v_lshlrev_b32 v28, 3, v28
buffer_load_dwordx2 v[28:29], v28, s[8:11], s55 offen sc1
s_waitcnt vmcnt(0)
s_add_u32 s84, s84, 1
v_cmp_eq_u64 vcc, s[12:13], v[28:29]
s_cmp_eq_u64 vcc, -1
s_cbranch_scc1 .Lok_85%=
s_cmp_lt_u32 s84, 1048576
s_cbranch_scc1 .Llp_86%=
s_branch .Lok_85%=
.LL_89%=:
s_mov_b32 s84, 0
.LL_91%=:
s_sleep 2
buffer_load_dwordx2 v[12:13], v27, s[8:11], 0 offen offset:3584 sc1
s_waitcnt vmcnt(0)
s_add_u32 s84, s84, 1
v_xor_b32 v12, s12, v12
v_xor_b32 v13, s13, v13
v_lshrrev_b32 v3, 11, v12
v_or_b32 v13, v13, v3
v_cmp_eq_u32 vcc, 0, v13
s_cmp_eq_u64 vcc, -1
s_cbranch_scc1 .LL_90%=
s_cmp_lt_u32 s84, 1048576
s_cbranch_scc1 .LL_91%=
s_branch .LL_90%=
.Lup_87%=:
v_lshrrev_b32 v3, 8, v1
v_lshlrev_b32 v3, 5, v3
s_ff1_i32_b32 s69, s68
s_mul_i32 s66, s69, 20
s_mul_i32 s67, s58, 5
s_add_u32 s66, s66, s67
s_lshl_b32 s66, s66, 7
s_add_u32 s66, s66, 98304
s_lshl_b32 s67, s14, 7
s_add_u32 s67, s67, s66
buffer_load_dwordx4 v[160:163], v3, s[8:11], s67 offen offset:0 sc1
buffer_load_dwordx4 v[164:167], v3, s[8:11], s67 offen offset:16 sc1
buffer_load_dwordx4 v[168:171], v3, s[8:11], s66 offen offset:512 sc1
buffer_load_dwordx4 v[172:175], v3, s[8:11], s66 offen offset:528 sc1
s_branch .Lupr_88%=
.Lspin_92%=:
s_mov_b32 s84, 0
.Llp_94%=:
s_sleep 2
v_bfe_u32 v28, v1, 4, 2
v_lshlrev_b32 v28, 3, v28
buffer_load_dwordx2 v[28:29], v28, s[8:11], s54 offen sc1
s_waitcnt vmcnt(0)
s_add_u32 s84, s84, 1
v_cmp_eq_u64 vcc, s[12:13], v[28:29]
s_cmp_eq_u64 vcc, -1
s_cbranch_scc1 .Lok_93%=
s_cmp_lt_u32 s84, 1048576
s_cbranch_scc1 .Llp_94%=
s_branch .Lok_93%=
.Lspin_95%=:
s_mov_b32 s84, 0
.Llp_97%=:
s_sleep 2
v_bfe_u32 v28, v1, 4, 2
v_lshlrev_b32 v28, 3, v28
buffer_load_dwordx2 v[28:29], v28, s[8:11], s55 offen sc1
s_waitcnt vmcnt(0)
s_add_u32 s84, s84, 1
v_cmp_eq_u64 vcc, s[12:13], v[28:29]
s_cmp_eq_u64 vcc, -1
s_cbranch_scc1 .Lok_96%=
s_cmp_lt_u32 s84, 1048576
s_cbranch_scc1 .Llp_97%=
s_branch .Lok_96%=
.LL_100%=:
s_mov_b32 s84, 0
.LL_102%=:
s_sleep 2
buffer_load_dwordx2 v[12:13], v27, s[8:11], 0 offen offset:3584 sc1
s_waitcnt vmcnt(0)
s_add_u32 s84, s84, 1
v_xor_b32 v12, s12, v12
v_xor_b32 v13, s13, v13
v_lshrrev_b32 v3, 11, v12
v_or_b32 v13, v13, v3
v_cmp_eq_u32 vcc, 0, v13
s_cmp_eq_u64 vcc, -1
s_cbranch_scc1 .LL_101%=
s_cmp_lt_u32 s84, 1048576
s_cbranch_scc1 .LL_102%=
s_branch .LL_101%=
.Lup_98%=:
v_lshrrev_b32 v3, 8, v1
v_lshlrev_b32 v3, 5, v3
s_ff1_i32_b32 s69, s68
s_mul_i32 s66, s69, 20
s_mul_i32 s67, s58, 5
s_add_u32 s66, s66, s67
s_lshl_b32 s66, s66, 7
s_add_u32 s66, s66, 98304
s_lshl_b32 s67, s14, 7
s_add_u32 s67, s67, s66
buffer_load_dwordx4 v[160:163], v3, s[8:11], s67 offen offset:0 sc1
buffer_load_dwordx4 v[164:167], v3, s[8:11], s67 offen offset:16 sc1
buffer_load_dwordx4 v[168:171], v3, s[8:11], s66 offen offset:512 sc1
buffer_load_dwordx4 v[172:175], v3, s[8:11], s66 offen offset:528 sc1
s_branch .Lupr_99%=
.Lend_186%=:
.Lend%=:
.purgem W
.purgem D
.purgem Ta
.purgem Ra
.purgem Zvaa
.purgem Mvaa
.purgem Zaaa
.purgem Maaa
.purgem A
.purgem U
.purgem K
.purgem Rv
.purgem Mvav
.purgem G
.purgem Dc
.purgem Zvav
.purgem Cvav
.purgem X
.purgem Cvaa
.purgem Maav
.purgem Mvvv
.purgem Zvva
.purgem Mvva
)"
    : : [ka] "s"(__builtin_amdgcn_kernarg_segment_ptr()), [bid] "s"(blockIdx.x), [tid] "v"(threadIdx.x)
    : "v1", "v2", "v3", "v4", "v5", "v6", "v7", "v8", "v9", "v10", "v11", "v12", "v13", "v14", "v15", "v16", "v17", "v18", "v19", "v20", "v21", "v22", "v23", "v24", "v25", "v26", "v27", "v28", "v29", "v30", "v31", "v32", "v33", "v34", "v35", "v36", "v37", "v38", "v39", "v40", "v41", "v42", "v43", "v44", "v45", "v46", "v47", "v48", "v49", "v50", "v51", "v52", "v53", "v54", "v55", "v56", "v57", "v58", "v59", "v60", "v61", "v62", "v63", "v64", "v65", "v66", "v67", "v68", "v69", "v70", "v71", "v72", "v73", "v74", "v75", "v76", "v77", "v78", "v79", "v80", "v81", "v82", "v83", "v84", "v85", "v86", "v87", "v88", "v89", "v90", "v91", "v92", "v93", "v94", "v95", "v96", "v97", "v98", "v99", "v100", "v101", "v102", "v103", "v104", "v105", "v106", "v107", "v108", "v109", "v110", "v111", "v112", "v113", "v114", "v115", "v116", "v117", "v118", "v119", "v120", "v121", "v122", "v123", "v124", "v125", "v126", "v127", "v128", "v129", "v130", "v131", "v132", "v133", "v134", "v135", "v136", "v137", "v138", "v139", "v140", "v141", "v142", "v143", "v144", "v145", "v146", "v147", "v148", "v149", "v150", "v151", "v152", "v153", "v154", "v155", "v156", "v157", "v158", "v159", "v160", "v161", "v162", "v163", "v164", "v165", "v166", "v167", "v168", "v169", "v170", "v171", "v172", "v173", "v174", "v175", "v176", "v177", "v178", "v179", "v180", "v181", "v182", "v183", "v184", "v185", "v186", "v187", "v188", "v189", "v190", "v191", "v192", "v193", "v194", "v195", "v196", "v197", "v198", "v199", "v200", "v201", "v202", "v203", "v204", "v205", "v206", "v207", "v208", "v209", "v210", "v211", "v212", "v213", "v214", "v215", "v216", "v217", "v218", "v219", "v220", "v221", "v222", "v223", "v224", "v225", "v226", "v227", "v228", "v229", "v230", "v231", "v232", "v233", "v234", "v235", "v236", "v237", "v238", "v239", "v240", "v241", "v242", "v243", "v244", "v245", "v246", "v247", "v248", "v249", "v250", "v251", "v252", "v253", "v254", "v255", "a0", "a1", "a2", "a3", "a4", "a5", "a6", "a7", "a8", "a9", "a10", "a11", "a12", "a13", "a14", "a15", "a16", "a17", "a18", "a19", "a20", "a21", "a22", "a23", "a24", "a25", "a26", "a27", "a28", "a29", "a30", "a31", "a32", "a33", "a34", "a35", "a36", "a37", "a38", "a39", "a40", "a41", "a42", "a43", "a44", "a45", "a46", "a47", "a48", "a49", "a50", "a51", "a52", "a53", "a54", "a55", "a56", "a57", "a58", "a59", "a60", "a61", "a62", "a63", "a64", "a65", "a66", "a67", "a68", "a69", "a70", "a71", "a72", "a73", "a74", "a75", "a76", "a77", "a78", "a79", "a80", "a81", "a82", "a83", "a84", "a85", "a86", "a87", "a88", "a89", "a90", "a91", "a92", "a93", "a94", "a95", "a96", "a97", "a98", "a99", "a100", "a101", "a102", "a103", "a104", "a105", "a106", "a107", "a108", "a109", "a110", "a111", "a112", "a113", "a114", "a115", "a116", "a117", "a118", "a119", "a120", "a121", "a122", "a123", "a124", "a125", "a126", "a127", "a128", "a129", "a130", "a131", "a132", "a133", "a134", "a135", "a136", "a137", "a138", "a139", "a140", "a141", "a142", "a143", "a144", "a145", "a146", "a147", "a148", "a149", "a150", "a151", "a152", "a153", "a154", "a155", "a156", "a157", "a158", "a159", "a160", "a161", "a162", "a163", "a164", "a165", "a166", "a167", "a168", "a169", "a170", "a171", "a172", "a173", "a174", "a175", "a176", "a177", "a178", "a179", "a180", "a181", "a182", "a183", "a184", "a185", "a186", "a187", "a188", "a189", "a190", "a191", "a192", "a193", "a194", "a195", "a196", "a197", "a198", "a199", "a200", "a201", "a202", "a203", "a204", "a205", "a206", "a207", "a208", "a209", "a210", "a211", "a212", "a213", "a214", "a215", "a216", "a217", "a218", "a219", "a220", "a221", "a222", "a223", "a224", "a225", "a226", "a227", "a228", "a229", "a230", "a231", "a232", "a233", "a234", "a235", "a236", "a237", "a238", "a239", "a240", "a241", "a242", "a243", "a244", "a245", "a246", "a247", "a248", "a249", "a250", "a251", "a252", "a253", "a254", "a255", "s4", "s5", "s6", "s7", "s8", "s9", "s10", "s11", "s12", "s13", "s14", "s15", "s16", "s17", "s18", "s19", "s20", "s21", "s22", "s23", "s24", "s25", "s26", "s27", "s28", "s29", "s30", "s31", "s32", "s33", "s34", "s35", "s36", "s37", "s38", "s39", "s40", "s41", "s42", "s43", "s44", "s45", "s46", "s47", "s48", "s49", "s50", "s51", "s52", "s53", "s54", "s55", "s56", "s57", "s58", "s59", "s60", "s61", "s62", "s63", "s64", "s65", "s66", "s67", "s68", "s69", "s70", "s71", "s72", "s73", "s74", "s75", "s76", "s77", "s78", "s79", "s80", "s81", "s82", "s83", "s84", "s85", "s86", "s87", "s88", "s89", "s90", "s91", "s92", "s93", "s94", "s95", "s96", "s97", "s98", "s99", "vcc", "scc", "memory"
  );
}
extern "C" void kernel_launch(void* const* d_in, const int* in_sizes, int n_in, void* d_out, int out_size, void* d_ws, size_t ws_size, hipStream_t stream) {
  (void)hipFuncSetAttribute((const void*)mega_kernel, hipFuncAttributeMaxDynamicSharedMemorySize, LDS_BYTES);
  mega_kernel<<<256, 256, LDS_BYTES, stream>>>((const bf16*)d_in[0], (const bf16*)d_in[1], (const bf16*)d_in[2], (const bf16*)d_in[3], (const bf16*)d_in[4], (const bf16*)d_in[5],
      (const bf16*)d_in[7], (const bf16*)d_in[8], (const bf16*)d_in[9], (const bf16*)d_in[10], (char*)d_ws + 0, (bf16*)d_out);
}
```

```cpp
#include <hip/hip_runtime.h>
#include <hip/hip_bf16.h>
typedef __hip_bfloat16 bf16;
constexpr int LDS_BYTES = 163840;
__global__ __launch_bounds__(256, 1) void mega_kernel(const bf16* x, const bf16* ckv, const bf16* wq, const bf16* wuk, const bf16* wuv, const bf16* wo,
    const bf16* w_r, const bf16* wg, const bf16* wu, const bf16* wd, char* ws, bf16* out) {
  asm volatile(
R"(.macro W n
s_waitcnt lgkmcnt(\n)
.endm
.macro D v,r,s,o
buffer_load_dwordx4 v\v, s[\r:\r+3], s\s offen offset:\o lds
.endm
.macro Ta d,a,o
ds_read_b64_tr_b16 a[\d:\d+1], v\a offset:\o
.endm
.macro Ra d,a,o
ds_read_b128 a[\d:\d+3], v\a offset:\o
.endm
.macro Zvaa d,a,b
v_mfma_f32_16x16x32_bf16 v[\d:\d+3], a[\a:\a+3], a[\b:\b+3], 0
.endm
.macro Mvaa d,a,b
v_mfma_f32_16x16x32_bf16 v[\d:\d+3], a[\a:\a+3], a[\b:\b+3], v[\d:\d+3]
.endm
.macro Zaaa d,a,b
v_mfma_f32_16x16x32_bf16 a[\d:\d+3], a[\a:\a+3], a[\b:\b+3], 0
.endm
.macro Maaa d,a,b
v_mfma_f32_16x16x32_bf16 a[\d:\d+3], a[\a:\a+3], a[\b:\b+3], a[\d:\d+3]
.endm
.macro A d,a,b
v_add_f32 v\d, v\a, v\b
.endm
.macro U d,a,b
v_mul_f32 v\d, v\a, v\b
.endm
.macro K d,a,b
v_cvt_pk_bf16_f32 v\d, v\a, v\b
.endm
.macro Rv d,a,o
ds_read_b128 v[\d:\d+3], v\a offset:\o
.endm
.macro Mvav d,a,b
v_mfma_f32_16x16x32_bf16 v[\d:\d+3], a[\a:\a+3], v[\b:\b+3], v[\d:\d+3]
.endm
.macro G d,a
v_accvgpr_read_b32 v\d, a\a
.endm
.macro Zvav d,a,b
v_mfma_f32_16x16x32_bf16 v[\d:\d+3], a[\a:\a+3], v[\b:\b+3], 0
.endm
.macro Cvav d,a,b,c
v_mfma_f32_16x16x32_bf16 v[\d:\d+3], a[\a:\a+3], v[\b:\b+3], v[\c:\c+3]
.endm
.macro X d
v_exp_f32 v\d, v\d
.endm
.macro Cvaa d,a,b,c
v_mfma_f32_16x16x32_bf16 v[\d:\d+3], a[\a:\a+3], a[\b:\b+3], v[\c:\c+3]
.endm
.macro Dc v,r,s,o
buffer_load_dwordx4 v\v, s[\r:\r+3], s\s offen offset:\o sc1 lds
.endm
.macro Maav d,a,b
v_mfma_f32_16x16x32_bf16 a[\d:\d+3], a[\a:\a+3], v[\b:\b+3], a[\d:\d+3]
.endm
.macro Mvvv d,a,b
v_mfma_f32_16x16x32_bf16 v[\d:\d+3], v[\a:\a+3], v[\b:\b+3], v[\d:\d+3]
.endm
.macro Zvva d,a,b
v_mfma_f32_16x16x32_bf16 v[\d:\d+3], v[\a:\a+3], a[\b:\b+3], 0
.endm
.macro Mvva d,a,b
v_mfma_f32_16x16x32_bf16 v[\d:\d+3], v[\a:\a+3], a[\b:\b+3], v[\d:\d+3]
.endm
s_load_dwordx16 s[16:31], %[ka], 0
s_load_dwordx8 s[32:39], %[ka], 64
s_getpc_b64 s[6:7]
.Lbeg%=:
s_mov_b32 s15, %[bid]
v_lshrrev_b32 v2, 6, %[tid]
v_and_b32 v100, 63, %[tid]
v_readfirstlane_b32 s14, v2
v_lshlrev_b32 v1, 4, v100
v_lshrrev_b32 v101, 4, v100
v_and_b32 v102, 15, v100
s_and_b32 s59, s15, 7
s_bfe_u32 s57, s15, 0x30003
s_lshr_b32 s58, s15, 6
s_lshl_b32 s56, s59, 2
s_add_u32 s56, s56, s58
s_lshr_b32 s80, s15, 3
s_lshl_b32 s80, s80, 2
s_add_u32 s80, s80, s14
s_lshl_b32 s80, s80, 11
v_min_u32 v146, 240, v1
v_lshl_add_u32 v146, v146, 3, s80
v_min_u32 v146, .Lend%=-.Lbeg%=-4, v146
global_load_dword v142, v146, s[6:7]
v_and_b32 v88, 0x1f0, v1
v_lshrrev_b32 v105, 1, v101
v_mul_u32_u24 v105, 1280, v105
v_add_u32 v28, v88, v105
s_lshl_b32 s85, s15, 2
s_add_u32 s85, s85, s14
s_mul_i32 s84, s85, 2560
v_and_b32 v105, 1, v101
v_lshl_or_b32 v29, v105, 31, v28
v_lshlrev_b32 v104, 1, v101
v_xor_b32 v104, v104, v102
v_lshlrev_b32 v104, 4, v104
v_lshl_add_u32 v16, v101, 13, v104
v_xor_b32 v105, 128, v16
v_add_u32 v17, 31744, v16
v_add_u32 v18, 63488, v105
v_add_u32 v19, 95232, v105
v_add_u32 v20, 131072, v16
v_add_u32 v21, 162816, v16
v_add_u32 v22, 194560, v105
v_add_u32 v23, 226304, v105
s_lshl_b32 s80, s14, 3
s_add_u32 s80, s80, s57
s_lshl_b32 s80, s80, 15
v_and_b32 v104, 7, v100
v_xor_b32 v104, v104, v101
v_lshlrev_b32 v104, 4, v104
v_lshrrev_b32 v105, 3, v100
v_lshl_add_u32 v104, v105, 10, v104
v_add_u32 v2, s80, v104
v_xor_b32 v105, 64, v2
v_add_u32 v3, 7168, v105
v_add_u32 v4, 14336, v2
v_add_u32 v5, 21504, v105
v_xor_b32 v6, 1835008, v2
v_xor_b32 v7, 1835008, v3
v_xor_b32 v8, 1835008, v4
v_xor_b32 v9, 1835008, v5
s_lshl_b32 s63, s14, 13
s_add_u32 s61, s63, 65536
s_and_b32 s80, s14, 1
s_lshr_b32 s81, s14, 1
s_lshl_b32 s62, s80, 14
s_lshl_b32 s82, s81, 13
s_add_u32 s62, s62, s82
s_lshl_b32 s81, s81, 18
s_lshl_b32 s83, s56, 8
s_add_u32 s81, s81, s83
s_mov_b32 s64, 0
s_cmp_eq_u32 s80, 0
W 0
s_cselect_b32 s44, s22, s24
s_cselect_b32 s45, s23, s25
s_cmp_eq_u32 s14, 3
s_cbranch_scc0 .LL_1%=
s_lshr_b32 s88, s15, 3
s_cmp_lt_u32 s88, 14
s_cbranch_scc0 .LL_1%=
v_lshlrev_b32 v104, 16, v1
s_mov_b64 s[86:87], s[36:37]
s_mov_b32 s89, 38797312
s_cmp_eq_u32 s88, 1
s_cselect_b32 s90, 39845888, 0
s_add_u32 s86, s86, s90
s_addc_u32 s87, s87, 0
s_cmp_eq_u32 s88, 2
s_cselect_b64 s[86:87], s[26:27], s[86:87]
s_cselect_b32 s89, 5242876, s89
s_cmp_eq_u32 s88, 3
s_cselect_b64 s[86:87], s[30:31], s[86:87]
s_cselect_b32 s89, 2211836, s89
s_cmp_eq_u32 s88, 4
s_cselect_b64 s[86:87], s[32:33], s[86:87]
s_cselect_b32 s89, 2211836, s89
s_cmp_eq_u32 s88, 5
s_cselect_b64 s[86:87], s[34:35], s[86:87]
s_cselect_b32 s89, 2211836, s89
s_cmp_eq_u32 s88, 6
s_cselect_b64 s[86:87], s[38:39], s[86:87]
s_cselect_b32 s89, 2621436, s89
s_cmp_eq_u32 s88, 7
s_cselect_b64 s[86:87], s[20:21], s[86:87]
s_cselect_b32 s89, 5242876, s89
s_cmp_eq_u32 s88, 8
s_cselect_b64 s[86:87], s[18:19], s[86:87]
s_cselect_b32 s89, 2097148, s89
s_cmp_eq_u32 s88, 9
s_cselect_b64 s[86:87], s[22:23], s[86:87]
s_cselect_b32 s89, 4194300, s89
s_cmp_eq_u32 s88, 10
s_cselect_b64 s[86:87], s[24:25], s[86:87]
s_cselect_b32 s89, 4194300, s89
s_cmp_eq_u32 s88, 11
s_cselect_b64 s[86:87], s[16:17], s[86:87]
s_cselect_b32 s89, 2621436, s89
s_cmp_eq_u32 s88, 12
s_cselect_b64 s[86:87], s[28:29], s[86:87]
s_cselect_b32 s89, 1276, s89
s_cmp_eq_u32 s88, 13
s_cselect_b64 s[86:87], s[6:7], s[86:87]
s_cselect_b32 s89, 0, s89
v_min_u32 v104, s89, v104
global_load_dword v142, v104, s[86:87] sc1
.LL_1%=:
s_mov_b32 s40, s18
s_and_b32 s41, s19, 0xffff
s_mov_b32 s42, 0x200000
s_mov_b32 s43, 0x20000
s_add_u32 s44, s44, s81
s_addc_u32 s45, s45, 0
s_and_b32 s45, s45, 0xffff
s_mov_b32 s46, 0x400000
s_mov_b32 s47, 0x20000
s_sub_u32 s46, s46, s81
s_load_dwordx2 s[12:13], s[36:37], 0
s_lshl_b32 s65, s64, 12
s_add_u32 m0, s62, 0
s_nop 0
D 16,44,65,0
D 17,44,65,1024
D 18,44,65,2048
D 19,44,65,3072
s_add_u32 m0, s62, 4096
s_nop 0
D 20,44,65,0
D 21,44,65,1024
D 22,44,65,2048
D 23,44,65,3072
s_add_u32 m0, s61, 0
s_nop 0
D 2,40,64,0
D 3,40,64,1024
D 4,40,64,2048
D 5,40,64,3072
s_add_u32 m0, s61, 4096
s_nop 0
D 6,40,64,0
D 7,40,64,1024
D 8,40,64,2048
D 9,40,64,3072
s_mov_b32 s52, s16
s_and_b32 s53, s17, 0xffff
s_mov_b32 s54, 2621440
s_mov_b32 s55, 0x20000
buffer_load_dwordx4 v[32:35], v28, s[52:55], s84 offen
buffer_load_dwordx4 v[36:39], v28, s[52:55], s84 offen offset:512
buffer_load_dwordx4 v[40:43], v29, s[52:55], s84 offen offset:1024
s_mov_b32 s52, s28
s_and_b32 s53, s29, 0xffff
s_mov_b32 s54, 1280
s_mov_b32 s55, 0x20000
buffer_load_dwordx4 v[44:47], v88, s[52:55], 0 offen offset:0
buffer_load_dwordx4 v[48:51], v88, s[52:55], 0 offen offset:512
buffer_load_dwordx4 v[52:55], v88, s[52:55], 0 offen offset:1024
s_mov_b32 s69, 0
s_cmp_eq_u32 s14, 2
s_cbranch_scc1 .LL_2%=
s_cmp_eq_u32 s14, 3
s_cbranch_scc1 .LL_3%=
s_lshl_b32 s80, s58, 3
s_add_u32 s80, s80, s57
s_lshl_b32 s80, s80, 15
s_lshl_b32 s82, s14, 7
s_add_u32 s80, s80, s82
s_add_u32 s80, s80, 256
v_lshl_add_u32 v104, v88, 6, s80
v_and_b32 v105, 512, v1
v_mul_u32_u24 v105, 3584, v105
v_xor_b32 v104, v104, v105
v_mov_b32 v140, s18
v_mov_b32 v141, s19
s_mov_b32 s68, 256
s_branch .LL_4%=
.LL_2%=:
v_lshrrev_b32 v104, 6, v1
v_lshlrev_b32 v104, 13, v104
v_and_b32 v105, 16, v1
v_lshl_add_u32 v104, v105, 3, v104
s_lshl_b32 s80, s57, 17
s_add_u32 s80, s80, s83
s_add_u32 s80, s80, 1048576
v_add_u32 v104, s80, v104
v_and_b32 v105, 32, v1
v_cmp_eq_u32 vcc, 0, v105
v_mov_b32 v140, s24
v_mov_b32 v141, s25
v_mov_b32 v105, s22
v_mov_b32 v106, s23
v_cndmask_b32 v140, v140, v105, vcc
v_cndmask_b32 v141, v141, v106, vcc
s_mov_b32 s68, 1048576
s_branch .LL_4%=
.LL_3%=:
v_lshrrev_b32 v104, 5, v1
v_lshlrev_b32 v104, 13, v104
v_and_b32 v105, 16, v1
v_lshl_add_u32 v104, v105, 3, v104
s_mul_i32 s80, s57, 655360
s_add_u32 s80, s80, s83
v_add_u32 v104, s80, v104
v_mov_b32 v140, s20
v_mov_b32 v141, s21
s_mov_b32 s68, 196608
v_mov_b32 v104, 0
s_mov_b32 s68, 0
.LL_4%=:
v_mov_b32 v105, 0
v_lshl_add_u64 v[140:141], v[104:105], 0, v[140:141]
s_add_u32 s64, s64, 128
s_and_b32 s64, s64, 1023
s_lshl_b32 s65, s64, 12
s_add_u32 m0, s62, 32768
s_nop 0
D 16,44,65,0
D 17,44,65,1024
D 18,44,65,2048
D 19,44,65,3072
s_add_u32 m0, s62, 36864
s_nop 0
D 20,44,65,0
D 21,44,65,1024
D 22,44,65,2048
D 23,44,65,3072
v_lshrrev_b32 v104, 1, v102
v_xor_b32 v104, v104, v101
v_lshlrev_b32 v104, 4, v104
v_lshl_add_u32 v104, v102, 7, v104
s_lshr_b32 s81, s14, 1
s_lshl_b32 s81, s81, 14
s_add_u32 s81, s81, 65536
v_add_u32 v26, s81, v104
v_xor_b32 v27, 64, v26
v_and_b32 v105, 1, v101
v_lshrrev_b32 v104, 2, v102
v_lshl_or_b32 v105, v105, 2, v104
v_lshl_add_u32 v104, v101, 3, v104
v_lshlrev_b32 v104, 8, v104
v_lshl_add_u32 v104, v105, 5, v104
v_and_b32 v105, 3, v102
v_lshl_add_u32 v10, v105, 3, v104
v_xor_b32 v11, 32, v10
v_xor_b32 v12, 64, v10
v_xor_b32 v13, 96, v10
v_xor_b32 v14, 128, v10
v_xor_b32 v15, 160, v10
v_xor_b32 v24, 192, v10
v_xor_b32 v25, 224, v10
.LL_8%=:
s_lshr_b32 s81, s15, 3
s_cmp_eq_u32 s14, 2
s_cbranch_scc1 .LL_5%=
s_cmp_eq_u32 s14, 3
s_cbranch_scc1 .LL_6%=
s_cmp_eq_u32 s14, 0
s_cselect_b64 s[76:77], s[30:31], s[32:33]
s_mov_b32 s72, 512
s_mov_b32 s73, 24
s_mul_i32 s74, s81, 69120
s_mul_i32 s82, s59, 8640
s_andn2_b32 s82, s82, 127
s_add_u32 s74, s74, s82
s_mov_b32 s75, 2211836
s_branch .LL_7%=
.LL_5%=:
s_mov_b64 s[76:77], s[34:35]
s_mov_b32 s72, 512
s_mov_b32 s73, 24
s_mul_i32 s74, s81, 8960
s_mul_i32 s82, s59, 286720
s_add_u32 s74, s74, s82
s_mov_b32 s75, 2211836
s_branch .LL_7%=
.LL_6%=:
s_mov_b64 s[76:77], s[26:27]
s_mov_b32 s72, 512
s_mov_b32 s73, 4
s_mul_i32 s74, s81, 20480
s_mul_i32 s82, s59, 655360
s_add_u32 s74, s74, s82
s_mov_b32 s75, 5242876
s_branch .LL_7%=
.LL_7%=:
s_mov_b32 s8, s36
s_and_b32 s9, s37, 0xffff
s_mov_b32 s10, 79691776
s_mov_b32 s11, 0x20000
s_add_u32 s48, s20, s83
s_addc_u32 s49, s21, 0
s_and_b32 s49, s49, 0xffff
s_mov_b32 s50, 5242880
s_mov_b32 s51, 0x20000
s_sub_u32 s50, s50, s83
s_lshl_b32 s80, s57, 1
s_and_b32 s81, s14, 1
s_add_u32 s80, s80, s81
s_sub_u32 s81, 31, s80
s_cmp_lt_u32 s14, 2
s_cselect_b32 s60, s80, s81
s_mul_i32 s28, s60, 81920
s_add_u32 s28, s28, 131072
s_add_u32 s4, s28, 10240
s_mov_b32 s80, 0
s_lshl_b32 s81, s80, 9
s_add_u32 s28, s28, s81
s_add_u32 s5, s80, s14
s_cmp_ge_u32 s5, 20
s_cselect_b32 s81, 20, 0
s_sub_u32 s5, s5, s81
s_and_b32 s80, s14, 1
s_mul_i32 s29, s80, 17172480
s_mul_i32 s80, s56, 536576
s_add_u32 s29, s29, s80
s_lshl_b32 s80, s57, 13
s_add_u32 s29, s29, s80
s_add_u32 s29, s29, 4194304
W 0
s_add_u32 s12, s12, 0x7f4a7c15
s_addc_u32 s13, s13, 0x9e3779b9
s_mov_b64 exec, 0
s_cmp_eq_u32 s14, 0
s_cbranch_scc1 .Lh_0_0%=
s_cmp_eq_u32 s14, 1
s_cbranch_scc1 .Lh_0_1%=
s_cmp_eq_u32 s14, 2
s_cbranch_scc1 .Lh_0_2%=
s_branch .Lh_0_3%=
.Lhr_9%=:
s_mov_b64 exec, -1
s_bitcmp1_b32 s14, 0
s_cbranch_scc1 .Lv_10%=
s_branch .LL_12%=
.p2align 12
.LL_12%=:
s_waitcnt vmcnt(14)
s_barrier
Ta 0,10,0
Ta 2,10,1024
Ra 32,26,0
Ta 4,10,8192
.Lh_0_0%=:
s_cbranch_execz .Lh_0_0_1%=
Ta 6,10,9216
Ra 128,27,0
Ra 132,27,2048
Ra 36,26,2048
Ra 40,26,4096
Ra 136,27,4096
Ra 140,27,6144
Ra 44,26,6144
Ra 48,26,8192
Ra 144,27,8192
W 6
Zvaa 160,0,32
)"
R"(s_add_u32 m0, s61, 32768
Ra 148,27,10240
Ra 52,26,10240
Ra 56,26,12288
Ra 152,27,12288
Ra 156,27,14336
Ra 60,26,14336
Mvaa 160,4,128
Ta 8,11,0
Ta 10,11,1024
Ta 12,11,8192
Ta 14,11,9216
Zvaa 192,4,132
Ta 16,12,0
Ta 18,12,1024
Mvaa 192,0,36
W 14
Ta 20,12,8192
Ta 22,12,9216
Zvaa 224,0,40
D 2,40,64,0
Mvaa 224,4,136
Zaaa 160,4,140
Maaa 160,0,44
W 12
Zaaa 192,0,48
D 3,40,64,1024
Maaa 192,4,144
Zaaa 224,4,148
s_waitcnt vmcnt(13)
Maaa 224,0,52
W 8
v_lshlrev_b32 v56, 16, v32
v_and_b32 v57, 0xffff0000, v32
v_lshlrev_b32 v58, 16, v33
v_and_b32 v59, 0xffff0000, v33
Zaaa 64,0,56
D 4,40,64,2048
v_lshlrev_b32 v60, 16, v34
v_and_b32 v61, 0xffff0000, v34
v_lshlrev_b32 v62, 16, v35
v_and_b32 v63, 0xffff0000, v35
Maaa 64,4,152
v_lshlrev_b32 v64, 16, v36
v_and_b32 v65, 0xffff0000, v36
v_lshlrev_b32 v66, 16, v37
v_and_b32 v67, 0xffff0000, v37
v_lshlrev_b32 v68, 16, v38
Zaaa 96,4,156
v_and_b32 v69, 0xffff0000, v38
v_lshlrev_b32 v70, 16, v39
v_and_b32 v71, 0xffff0000, v39
v_lshlrev_b32 v72, 16, v40
Maaa 96,0,60
W 4
v_and_b32 v73, 0xffff0000, v40
v_lshlrev_b32 v74, 16, v41
v_and_b32 v75, 0xffff0000, v41
v_lshlrev_b32 v76, 16, v42
v_and_b32 v77, 0xffff0000, v42
Zaaa 100,8,60
D 5,40,64,3072
v_lshlrev_b32 v78, 16, v43
v_and_b32 v79, 0xffff0000, v43
A 80,56,57
A 81,58,59
Maaa 100,12,156
s_add_u32 m0, s61, 36864
A 80,80,60
A 81,81,61
A 80,80,62
A 81,81,63
A 80,80,64
Zaaa 68,12,152
Ta 24,13,0
Ta 26,13,1024
A 81,81,65
A 80,80,66
A 81,81,67
A 80,80,68
Maaa 68,8,56
Ta 28,13,8192
Ta 30,13,9216
A 81,81,69
A 80,80,70
A 81,81,71
A 80,80,72
Zaaa 228,8,52
D 6,40,64,0
A 81,81,73
A 80,80,74
A 81,81,75
A 80,80,76
A 81,81,77
Maaa 228,12,148
A 80,80,78
A 81,81,79
A 80,80,81
s_nop 1
v_add_f32_dpp v80, v80, v80 quad_perm:[1,0,3,2] row_mask:0xf bank_mask:0xf
Zaaa 196,12,144
Maaa 196,8,48
v_add_f32_dpp v80, v80, v80 quad_perm:[2,3,0,1] row_mask:0xf bank_mask:0xf
s_nop 1
v_add_f32_dpp v80, v80, v80 row_half_mirror row_mask:0xf bank_mask:0xf
s_nop 1
v_add_f32_dpp v80, v80, v80 row_mirror row_mask:0xf bank_mask:0xf
v_mov_b32 v81, v80
s_nop 1
v_permlane16_swap_b32 v80, v81
A 80,80,81
Zaaa 164,8,44
D 7,40,64,1024
v_mul_f32 v84, 0x3acccccd, v80
s_mov_b32 s86, 0xffff
s_mov_b32 s87, 0xffff
v_sub_f32 v82, v56, v84
U 80,82,82
v_sub_f32 v83, v57, v84
Maaa 164,12,140
v_fmac_f32 v80, v83, v83
v_sub_f32 v82, v58, v84
v_fmac_f32 v80, v82, v82
v_sub_f32 v83, v59, v84
v_fmac_f32 v80, v83, v83
v_sub_f32 v82, v60, v84
Zvaa 228,12,136
v_fmac_f32 v80, v82, v82
v_sub_f32 v83, v61, v84
v_fmac_f32 v80, v83, v83
v_sub_f32 v82, v62, v84
v_fmac_f32 v80, v82, v82
Mvaa 228,8,40
v_sub_f32 v83, v63, v84
v_fmac_f32 v80, v83, v83
v_sub_f32 v82, v64, v84
v_fmac_f32 v80, v82, v82
Zvaa 196,8,36
D 8,40,64,2048
v_sub_f32 v83, v65, v84
v_fmac_f32 v80, v83, v83
v_sub_f32 v82, v66, v84
v_fmac_f32 v80, v82, v82
Mvaa 196,12,132
v_sub_f32 v83, v67, v84
v_fmac_f32 v80, v83, v83
v_sub_f32 v82, v68, v84
v_fmac_f32 v80, v82, v82
v_sub_f32 v83, v69, v84
Zvaa 164,12,128
v_fmac_f32 v80, v83, v83
v_sub_f32 v82, v70, v84
v_fmac_f32 v80, v82, v82
v_sub_f32 v83, v71, v84
Mvaa 164,8,32
W 4
v_fmac_f32 v80, v83, v83
v_sub_f32 v82, v72, v84
U 81,82,82
v_sub_f32 v83, v73, v84
v_fmac_f32 v81, v83, v83
Zvaa 168,16,32
D 9,40,64,3072
v_sub_f32 v82, v74, v84
v_fmac_f32 v81, v82, v82
v_sub_f32 v83, v75, v84
v_fmac_f32 v81, v83, v83
Mvaa 168,20,128
v_sub_f32 v82, v76, v84
v_fmac_f32 v81, v82, v82
v_sub_f32 v83, v77, v84
v_fmac_f32 v81, v83, v83
v_sub_f32 v82, v78, v84
Zvaa 200,20,132
Ta 0,14,0
Ta 2,14,1024
v_fmac_f32 v81, v82, v82
v_sub_f32 v83, v79, v84
v_fmac_f32 v81, v83, v83
v_cndmask_b32_e64 v81, 0, v81, s[86:87]
Mvaa 200,16,36
Ta 4,14,8192
Ta 6,14,9216
A 80,80,81
s_nop 1
v_add_f32_dpp v80, v80, v80 quad_perm:[1,0,3,2] row_mask:0xf bank_mask:0xf
s_nop 1
v_add_f32_dpp v80, v80, v80 quad_perm:[2,3,0,1] row_mask:0xf bank_mask:0xf
s_nop 1
v_add_f32_dpp v80, v80, v80 row_half_mirror row_mask:0xf bank_mask:0xf
Zvaa 232,16,40
global_load_dword v142, v[140:141], off
v_lshl_add_u64 v[140:141], v[140:141], 0, s[68:69]
Mvaa 232,20,136
v_add_f32_dpp v80, v80, v80 row_mirror row_mask:0xf bank_mask:0xf
v_mov_b32 v81, v80
s_nop 1
v_permlane16_swap_b32 v80, v81
A 80,80,81
v_mul_f32 v80, 0x3acccccd, v80
K 80,80,80
Zaaa 168,20,140
v_lshlrev_b32 v80, 16, v80
v_add_f32 v80, 0x3727c5ac, v80
K 80,80,80
v_lshlrev_b32 v80, 16, v80
v_sqrt_f32 v81, v80
Maaa 168,16,44
v_rcp_f32 v82, v81
v_fma_f32 v83, -v81, v81, v80
v_mul_f32 v82, 0.5, v82
v_fma_f32 v81, v83, v82, v81
K 86,81,81
Zaaa 200,16,48
v_lshlrev_b32 v86, 16, v86
v_rcp_f32 v87, v86
K 85,84,84
v_lshlrev_b32 v85, 16, v85
Maaa 200,20,144
s_waitcnt vmcnt(17)
v_sub_f32 v56, v56, v85
v_sub_f32 v57, v57, v85
K 80,56,57
Zaaa 232,20,148
v_lshlrev_b32 v56, 16, v80
v_and_b32 v57, 0xffff0000, v80
U 80,56,87
U 81,57,87
K 32,80,81
Maaa 232,16,52
v_lshlrev_b32 v80, 16, v32
v_and_b32 v81, 0xffff0000, v32
v_lshlrev_b32 v82, 16, v44
v_and_b32 v83, 0xffff0000, v44
Zaaa 72,16,56
U 84,80,82
v_fmac_f32 v84, v81, v83
v_sub_f32 v58, v58, v85
v_sub_f32 v59, v59, v85
Maaa 72,20,152
K 80,58,59
v_lshlrev_b32 v58, 16, v80
v_and_b32 v59, 0xffff0000, v80
U 80,58,87
U 81,59,87
Zaaa 104,20,156
K 33,80,81
v_lshlrev_b32 v80, 16, v33
v_and_b32 v81, 0xffff0000, v33
v_lshlrev_b32 v82, 16, v45
Maaa 104,16,60
W 4
v_and_b32 v83, 0xffff0000, v45
v_fmac_f32 v84, v80, v82
v_fmac_f32 v84, v81, v83
v_sub_f32 v60, v60, v85
v_sub_f32 v61, v61, v85
Zaaa 108,24,60
K 80,60,61
v_lshlrev_b32 v60, 16, v80
v_and_b32 v61, 0xffff0000, v80
U 80,60,87
Maaa 108,28,156
U 81,61,87
K 34,80,81
v_lshlrev_b32 v80, 16, v34
v_and_b32 v81, 0xffff0000, v34
v_lshlrev_b32 v82, 16, v46
Zaaa 76,28,152
Ta 8,15,0
Ta 10,15,1024
v_and_b32 v83, 0xffff0000, v46
v_fmac_f32 v84, v80, v82
v_fmac_f32 v84, v81, v83
v_sub_f32 v62, v62, v85
Maaa 76,24,56
Ta 12,15,8192
v_sub_f32 v63, v63, v85
K 80,62,63
v_lshlrev_b32 v62, 16, v80
v_and_b32 v63, 0xffff0000, v80
U 80,62,87
Zaaa 236,24,52
Ta 14,15,9216
U 81,63,87
K 35,80,81
v_lshlrev_b32 v80, 16, v35
v_and_b32 v81, 0xffff0000, v35
Maaa 236,28,148
v_lshlrev_b32 v82, 16, v47
v_and_b32 v83, 0xffff0000, v47
v_fmac_f32 v84, v80, v82
v_fmac_f32 v84, v81, v83
Zaaa 204,28,144
v_sub_f32 v64, v64, v85
v_sub_f32 v65, v65, v85
K 80,64,65
v_lshlrev_b32 v64, 16, v80
v_and_b32 v65, 0xffff0000, v80
Maaa 204,24,48
U 80,64,87
U 81,65,87
K 36,80,81
v_lshlrev_b32 v80, 16, v36
Zaaa 172,24,44
v_and_b32 v81, 0xffff0000, v36
v_lshlrev_b32 v82, 16, v48
v_and_b32 v83, 0xffff0000, v48
v_fmac_f32 v84, v80, v82
v_fmac_f32 v84, v81, v83
Maaa 172,28,140
v_sub_f32 v66, v66, v85
v_sub_f32 v67, v67, v85
K 80,66,67
v_lshlrev_b32 v66, 16, v80
Zvaa 236,28,136
v_and_b32 v67, 0xffff0000, v80
U 80,66,87
U 81,67,87
K 37,80,81
v_lshlrev_b32 v80, 16, v37
Mvaa 236,24,40
v_and_b32 v81, 0xffff0000, v37
v_lshlrev_b32 v82, 16, v49
v_and_b32 v83, 0xffff0000, v49
v_fmac_f32 v84, v80, v82
Zvaa 204,24,36
v_fmac_f32 v84, v81, v83
v_sub_f32 v68, v68, v85
v_sub_f32 v69, v69, v85
K 80,68,69
v_lshlrev_b32 v68, 16, v80
Mvaa 204,28,132
v_and_b32 v69, 0xffff0000, v80
U 80,68,87
U 81,69,87
K 38,80,81
Zvaa 172,28,128
v_lshlrev_b32 v80, 16, v38
v_and_b32 v81, 0xffff0000, v38
v_lshlrev_b32 v82, 16, v50
v_and_b32 v83, 0xffff0000, v50
Mvaa 172,24,32
W 4
v_fmac_f32 v84, v80, v82
v_fmac_f32 v84, v81, v83
v_sub_f32 v70, v70, v85
v_sub_f32 v71, v71, v85
K 80,70,71
Zvaa 176,0,32
v_lshlrev_b32 v70, 16, v80
v_and_b32 v71, 0xffff0000, v80
U 80,70,87
U 81,71,87
Mvaa 176,4,128
K 39,80,81
v_lshlrev_b32 v80, 16, v39
v_and_b32 v81, 0xffff0000, v39
v_lshlrev_b32 v82, 16, v51
v_and_b32 v83, 0xffff0000, v51
Zvaa 208,4,132
Ta 16,24,0
Ta 18,24,1024
v_fmac_f32 v84, v80, v82
v_fmac_f32 v84, v81, v83
v_sub_f32 v72, v72, v85
v_sub_f32 v73, v73, v85
Mvaa 208,0,36
Ta 20,24,8192
K 80,72,73
v_lshlrev_b32 v72, 16, v80
v_and_b32 v73, 0xffff0000, v80
U 80,72,87
U 81,73,87
Zvaa 240,0,40
Ta 22,24,9216
K 40,80,81
v_lshlrev_b32 v80, 16, v40
v_and_b32 v81, 0xffff0000, v40
v_lshlrev_b32 v82, 16, v52
Mvaa 240,4,136
v_and_b32 v83, 0xffff0000, v52
v_fmac_f32 v84, v80, v82
v_fmac_f32 v84, v81, v83
v_sub_f32 v74, v74, v85
Zaaa 176,4,140
v_sub_f32 v75, v75, v85
K 80,74,75
v_lshlrev_b32 v74, 16, v80
v_and_b32 v75, 0xffff0000, v80
U 80,74,87
Maaa 176,0,44
U 81,75,87
K 41,80,81
v_lshlrev_b32 v80, 16, v41
v_and_b32 v81, 0xffff0000, v41
Zaaa 208,0,48
v_lshlrev_b32 v82, 16, v53
v_and_b32 v83, 0xffff0000, v53
v_fmac_f32 v84, v80, v82
)"
R"(v_fmac_f32 v84, v81, v83
v_sub_f32 v76, v76, v85
Maaa 208,4,144
v_sub_f32 v77, v77, v85
K 80,76,77
v_lshlrev_b32 v76, 16, v80
v_and_b32 v77, 0xffff0000, v80
Zaaa 240,4,148
U 80,76,87
U 81,77,87
K 42,80,81
v_lshlrev_b32 v80, 16, v42
v_and_b32 v81, 0xffff0000, v42
Maaa 240,0,52
v_lshlrev_b32 v82, 16, v54
v_and_b32 v83, 0xffff0000, v54
v_fmac_f32 v84, v80, v82
v_fmac_f32 v84, v81, v83
Zaaa 80,0,56
v_sub_f32 v78, v78, v85
v_sub_f32 v79, v79, v85
K 80,78,79
v_lshlrev_b32 v78, 16, v80
v_and_b32 v79, 0xffff0000, v80
Maaa 80,4,152
U 80,78,87
U 81,79,87
K 43,80,81
v_lshlrev_b32 v80, 16, v43
Zaaa 112,4,156
v_and_b32 v81, 0xffff0000, v43
v_lshlrev_b32 v82, 16, v55
v_and_b32 v83, 0xffff0000, v55
v_fmac_f32 v84, v80, v82
Maaa 112,0,60
W 4
v_fmac_f32 v84, v81, v83
v_lshrrev_b32 v82, 9, v1
s_lshl_b32 s80, s14, 1
v_add_u32 v82, s80, v82
v_mul_u32_u24 v83, 1552, v82
Zaaa 116,8,60
Ta 24,25,0
Ta 26,25,1024
v_add_u32 v83, v83, v88
v_add_u32 v83, 131072, v83
ds_write_b128 v83, v[32:35] offset:0
ds_write_b128 v83, v[36:39] offset:512
Maaa 116,12,156
ds_write_b128 v83, v[40:43] offset:1024
v_add_f32_dpp v84, v84, v84 quad_perm:[1,0,3,2] row_mask:0xf bank_mask:0xf
s_nop 1
v_add_f32_dpp v84, v84, v84 quad_perm:[2,3,0,1] row_mask:0xf bank_mask:0xf
s_nop 1
v_add_f32_dpp v84, v84, v84 row_half_mirror row_mask:0xf bank_mask:0xf
s_nop 1
v_add_f32_dpp v84, v84, v84 row_mirror row_mask:0xf bank_mask:0xf
Zaaa 84,12,152
Ta 28,25,8192
Ta 30,25,9216
v_mov_b32 v81, v84
Maaa 84,8,56
Zaaa 244,8,52
v_permlane16_swap_b32 v84, v81
A 84,84,81
K 80,84,84
v_lshlrev_b32 v80, 16, v80
s_lshl_b32 s80, s15, 3
v_add_u32 v82, s80, v82
v_sub_u32 v83, 2047, v82
Maaa 244,12,148
v_ashrrev_i32 v81, 31, v80
v_or_b32 v81, 0x80000000, v81
v_xor_b32 v80, v80, v81
v_and_b32 v80, 0xffff0000, v80
v_or_b32 v80, v80, v83
v_and_b32 v82, 7, v82
Zaaa 212,12,144
v_lshlrev_b32 v82, 2, v82
v_add_u32 v82, 143488, v82
ds_write_b32 v82, v80
Maaa 212,8,48
Zaaa 180,8,44
Maaa 180,12,140
Zvaa 244,12,136
s_waitcnt vmcnt(1) lgkmcnt(0)
s_barrier
s_add_u32 s64, s64, 128
Mvaa 244,8,40
v_bfe_u32 v80, v1, 4, 3
Zvaa 212,8,36
v_mul_u32_u24 v81, 1552, v80
Mvaa 212,12,132
s_and_b32 s64, s64, 1023
v_lshrrev_b32 v82, 7, v1
Zvaa 180,12,128
v_lshl_add_u32 v81, v82, 4, v81
Mvaa 180,8,32
s_lshl_b32 s80, s14, 7
Zvaa 184,16,32
s_lshl_b32 s65, s64, 12
s_add_u32 s80, s80, 131072
Mvaa 184,20,128
v_add_u32 v81, s80, v81
Zvaa 216,20,132
ds_read_b128 v[32:35], v81
Ta 0,10,32768
Mvaa 216,16,36
s_add_u32 m0, s62, 0
Ta 2,10,33792
Rv 36,81,512
Zvaa 248,16,40
s_and_b32 s80, s14, 2
Ta 4,10,40960
Ta 6,10,41984
Mvaa 248,20,136
s_lshl_b32 s80, s80, 7
Zaaa 184,20,140
D 16,44,65,0
v_subrev_u32 v81, s80, v81
Maaa 184,16,44
Rv 40,81,1024
Zaaa 216,16,48
v_lshlrev_b32 v80, 2, v80
Maaa 216,20,144
v_add_u32 v80, 143488, v80
Zaaa 248,20,148
D 17,44,65,1024
ds_read_b32 v84, v80
Maaa 248,16,52
v_mov_b32 v85, s13
Zaaa 88,16,56
v_lshrrev_b32 v83, 1, v1
Maaa 88,20,152
s_mul_i32 s82, s15, 10240
Zaaa 120,20,156
s_add_u32 s82, s82, 131072
D 18,44,65,2048
Maaa 120,16,60
s_lshl_b32 s80, s14, 10
Zaaa 124,24,60
s_add_u32 s83, s82, s80
Ra 60,26,47104
Maaa 124,28,156
Ra 156,27,47104
Zaaa 92,28,152
D 19,44,65,3072
Ra 152,27,45056
Maaa 92,24,56
s_add_u32 m0, s62, 4096
Ta 8,11,32768
Ta 10,11,33792
Zaaa 252,24,52
Ra 56,26,45056
Ta 12,11,40960
Maaa 252,28,148
Ta 14,11,41984
Ra 52,26,43008
Zaaa 220,28,144
W 9
buffer_store_dwordx4 v[32:35], v1, s[8:11], s83 offen sc1
s_add_u32 s83, s83, 4096
Maaa 220,24,48
Ra 148,27,43008
Ra 144,27,40960
Zaaa 188,24,44
Ra 48,26,40960
Ra 44,26,38912
Maaa 188,28,140
Ra 140,27,38912
Zvaa 252,28,136
D 20,44,65,0
Ra 136,27,36864
Mvaa 252,24,40
Ra 40,26,36864
Zvaa 220,24,36
Ra 36,26,34816
Mvaa 220,28,132
Ra 132,27,34816
Zvaa 188,28,128
buffer_store_dwordx4 v[36:39], v1, s[8:11], s83 offen sc1
Ra 128,27,32768
Mvaa 188,24,32
W 13
s_and_b32 s80, s80, 1024
s_add_u32 s83, s82, s80
Maaa 96,0,60
s_add_u32 s83, s83, 8192
Ra 32,26,32768
Maaa 96,4,156
Maaa 64,4,152
D 21,44,65,1024
Ta 16,12,32768
Maaa 64,0,56
W 9
Ta 18,12,33792
Ta 20,12,40960
Maaa 224,0,52
Ta 22,12,41984
Maaa 224,4,148
Maaa 192,4,144
buffer_store_dwordx4 v[40:43], v1, s[8:11], s83 offen sc1
Maaa 192,0,48
W 8
Maaa 160,0,44
Maaa 160,4,140
Mvaa 224,4,136
D 22,44,65,2048
Mvaa 224,0,40
W 4
Mvaa 192,0,36
Mvaa 192,4,132
Mvaa 160,4,128
D 23,44,65,3072
Mvaa 160,0,32
s_add_u32 m0, s61, 0
Mvaa 164,8,32
Mvaa 164,12,128
Mvaa 196,12,132
D 2,40,64,0
Ta 24,13,32768
Mvaa 196,8,36
Ta 26,13,33792
Ta 28,13,40960
Mvaa 228,8,40
Ta 30,13,41984
Mvaa 228,12,136
.Lh_0_1%=:
s_cbranch_execz .Lh_0_1_1%=
Maaa 164,12,140
D 3,40,64,1024
Maaa 164,8,44
Maaa 196,8,48
Maaa 196,12,144
Maaa 228,12,148
D 4,40,64,2048
Maaa 228,8,52
Maaa 68,8,56
Maaa 68,12,152
Maaa 100,12,156
D 5,40,64,3072
Maaa 100,8,60
W 4
s_add_u32 m0, s61, 4096
Maaa 104,16,60
Maaa 104,20,156
Maaa 72,20,152
D 6,40,64,0
Ta 0,14,32768
Maaa 72,16,56
Ta 2,14,33792
Ta 4,14,40960
Maaa 232,16,52
Ta 6,14,41984
Maaa 232,20,148
Maaa 200,20,144
D 7,40,64,1024
Maaa 200,16,48
Maaa 168,16,44
Maaa 168,20,140
Mvaa 232,20,136
D 8,40,64,2048
Mvaa 232,16,40
Mvaa 200,16,36
Mvaa 200,20,132
Mvaa 168,20,128
D 9,40,64,3072
Mvaa 168,16,32
W 4
Mvaa 172,24,32
Mvaa 172,28,128
Mvaa 204,28,132
Ta 8,15,32768
Ta 10,15,33792
Mvaa 204,24,36
Ta 12,15,40960
Ta 14,15,41984
Mvaa 236,24,40
Mvaa 236,28,136
Maaa 172,28,140
Maaa 172,24,44
Maaa 204,24,48
Maaa 204,28,144
Maaa 236,28,148
Maaa 236,24,52
Maaa 76,24,56
Maaa 76,28,152
Maaa 108,28,156
Maaa 108,24,60
W 4
Maaa 112,0,60
Maaa 112,4,156
Maaa 80,4,152
Ta 16,24,32768
Ta 18,24,33792
Maaa 80,0,56
Ta 20,24,40960
Ta 22,24,41984
Maaa 240,0,52
Maaa 240,4,148
Maaa 208,4,144
Maaa 208,0,48
Maaa 176,0,44
Maaa 176,4,140
Mvaa 240,4,136
Mvaa 240,0,40
Mvaa 208,0,36
Mvaa 208,4,132
Mvaa 176,4,128
Mvaa 176,0,32
W 4
Mvaa 180,8,32
Ta 24,25,32768
Ta 26,25,33792
Mvaa 180,12,128
Mvaa 212,12,132
Ta 28,25,40960
Ta 30,25,41984
Mvaa 212,8,36
Mvaa 244,8,40
Mvaa 244,12,136
Maaa 180,12,140
Maaa 180,8,44
Maaa 212,8,48
Maaa 212,12,144
Maaa 244,12,148
s_waitcnt vmcnt(0) lgkmcnt(0)
s_barrier
s_cmp_eq_u32 s14, 0
s_cbranch_scc0 .Lpubr_14%=
s_lshl_b32 s80, s15, 6
s_add_u32 s80, s80, 40960
s_mov_b64 exec, 0xff
buffer_store_dwordx2 v[84:85], v83, s[8:11], s80 offen sc1
s_mov_b64 exec, -1
.Lpubr_14%=:
Maaa 244,8,52
s_add_u32 s64, s64, 128
Maaa 84,8,56
Maaa 84,12,152
s_and_b32 s64, s64, 1023
Maaa 116,12,156
Maaa 116,8,60
Maaa 120,16,60
s_lshl_b32 s65, s64, 12
Maaa 120,20,156
Maaa 88,20,152
s_add_u32 m0, s62, 32768
Ta 0,10,0
Ta 2,10,1024
Maaa 88,16,56
Ta 4,10,8192
Ta 6,10,9216
Maaa 248,16,52
Maaa 248,20,148
D 16,44,65,0
Maaa 216,20,144
Maaa 216,16,48
Maaa 184,16,44
Maaa 184,20,140
D 17,44,65,1024
Mvaa 248,20,136
Mvaa 248,16,40
Mvaa 216,16,36
Mvaa 216,20,132
D 18,44,65,2048
Mvaa 184,20,128
Mvaa 184,16,32
Mvaa 188,24,32
Ra 32,26,0
Mvaa 188,28,128
D 19,44,65,3072
Ra 128,27,0
Mvaa 220,28,132
s_add_u32 m0, s62, 36864
Ra 132,27,2048
Ta 8,11,0
Mvaa 220,24,36
Ta 10,11,1024
Ra 36,26,2048
Mvaa 252,24,40
Ta 12,11,8192
Ta 14,11,9216
Mvaa 252,28,136
D 20,44,65,0
Ra 40,26,4096
Maaa 188,28,140
Ra 136,27,4096
Ra 140,27,6144
Maaa 188,24,44
Ra 44,26,6144
Maaa 220,24,48
Ra 48,26,8192
Maaa 220,28,144
D 21,44,65,1024
Ra 144,27,8192
Maaa 252,28,148
Ra 148,27,10240
Maaa 252,24,52
Ra 52,26,10240
)"
R"(Maaa 92,24,56
Ra 56,26,12288
Maaa 92,28,152
D 22,44,65,2048
Ra 152,27,12288
Maaa 124,28,156
Ra 156,27,14336
Maaa 124,24,60
W 13
Ra 60,26,14336
Mvaa 160,0,32
Mvaa 160,4,128
D 23,44,65,3072
Mvaa 192,4,132
s_add_u32 m0, s61, 32768
Ta 16,12,0
Ta 18,12,1024
Mvaa 192,0,36
W 10
Ta 20,12,8192
Ta 22,12,9216
Mvaa 224,0,40
Mvaa 224,4,136
D 2,40,64,0
Maaa 160,4,140
Maaa 160,0,44
W 8
Maaa 192,0,48
Maaa 192,4,144
D 3,40,64,1024
Maaa 224,4,148
Maaa 224,0,52
W 4
Maaa 64,0,56
Maaa 64,4,152
D 4,40,64,2048
Maaa 96,4,156
Maaa 96,0,60
Maaa 100,8,60
Maaa 100,12,156
D 5,40,64,3072
Maaa 68,12,152
s_add_u32 m0, s61, 36864
Ta 24,13,0
Ta 26,13,1024
Maaa 68,8,56
Ta 28,13,8192
Ta 30,13,9216
Maaa 228,8,52
Maaa 228,12,148
D 6,40,64,0
Maaa 196,12,144
Maaa 196,8,48
Maaa 164,8,44
Maaa 164,12,140
D 7,40,64,1024
Mvaa 228,12,136
Mvaa 228,8,40
Mvaa 196,8,36
Mvaa 196,12,132
D 8,40,64,2048
Mvaa 164,12,128
Mvaa 164,8,32
W 4
Mvaa 168,16,32
Mvaa 168,20,128
D 9,40,64,3072
Mvaa 200,20,132
Ta 0,14,0
Ta 2,14,1024
Mvaa 200,16,36
Ta 4,14,8192
Ta 6,14,9216
Mvaa 232,16,40
Mvaa 232,20,136
global_load_dword v142, v[140:141], off
v_lshl_add_u64 v[140:141], v[140:141], 0, s[68:69]
Maaa 168,20,140
Maaa 168,16,44
Maaa 200,16,48
Maaa 200,20,144
Maaa 232,20,148
Maaa 232,16,52
Maaa 72,16,56
Maaa 72,20,152
Maaa 104,20,156
Maaa 104,16,60
W 4
Maaa 108,24,60
Maaa 108,28,156
Maaa 76,28,152
Ta 8,15,0
Ta 10,15,1024
Maaa 76,24,56
Ta 12,15,8192
Ta 14,15,9216
Maaa 236,24,52
Maaa 236,28,148
Maaa 204,28,144
Maaa 204,24,48
Maaa 172,24,44
Maaa 172,28,140
Mvaa 236,28,136
Mvaa 236,24,40
Mvaa 204,24,36
Mvaa 204,28,132
Mvaa 172,28,128
Mvaa 172,24,32
W 4
Mvaa 176,0,32
Mvaa 176,4,128
Mvaa 208,4,132
Ta 16,24,0
Ta 18,24,1024
Mvaa 208,0,36
Ta 20,24,8192
Ta 22,24,9216
Mvaa 240,0,40
Mvaa 240,4,136
Maaa 176,4,140
Maaa 176,0,44
Maaa 208,0,48
Maaa 208,4,144
Maaa 240,4,148
Maaa 240,0,52
Maaa 80,0,56
Maaa 80,4,152
Maaa 112,4,156
Maaa 112,0,60
W 4
Maaa 116,8,60
Ta 24,25,0
Ta 26,25,1024
Maaa 116,12,156
Maaa 84,12,152
Ta 28,25,8192
Ta 30,25,9216
Maaa 84,8,56
Maaa 244,8,52
Maaa 244,12,148
Maaa 212,12,144
Maaa 212,8,48
Maaa 180,8,44
Maaa 180,12,140
Mvaa 244,12,136
s_waitcnt vmcnt(1) lgkmcnt(0)
s_barrier
s_add_u32 s64, s64, 128
Mvaa 244,8,40
Mvaa 212,8,36
Mvaa 212,12,132
s_and_b32 s64, s64, 1023
Mvaa 180,12,128
Mvaa 180,8,32
Mvaa 184,16,32
s_lshl_b32 s65, s64, 12
Mvaa 184,20,128
Mvaa 216,20,132
Ta 0,10,32768
Ta 2,10,33792
Mvaa 216,16,36
s_add_u32 m0, s62, 0
Ta 4,10,40960
Ta 6,10,41984
Mvaa 248,16,40
Mvaa 248,20,136
Maaa 184,20,140
D 16,44,65,0
Maaa 184,16,44
Maaa 216,16,48
Maaa 216,20,144
Maaa 248,20,148
D 17,44,65,1024
Maaa 248,16,52
Maaa 88,16,56
Maaa 88,20,152
Maaa 120,20,156
D 18,44,65,2048
Maaa 120,16,60
Maaa 124,24,60
Ra 60,26,47104
Maaa 124,28,156
Ra 156,27,47104
Maaa 92,28,152
D 19,44,65,3072
Ra 152,27,45056
Maaa 92,24,56
s_add_u32 m0, s62, 4096
Ta 8,11,32768
Ta 10,11,33792
Maaa 252,24,52
Ra 56,26,45056
Ta 12,11,40960
Maaa 252,28,148
Ta 14,11,41984
Ra 52,26,43008
Maaa 220,28,144
D 20,44,65,0
Ra 148,27,43008
Maaa 220,24,48
Ra 144,27,40960
Ra 48,26,40960
Maaa 188,24,44
Ra 44,26,38912
Maaa 188,28,140
Ra 140,27,38912
Mvaa 252,28,136
D 21,44,65,1024
Ra 136,27,36864
Mvaa 252,24,40
Ra 40,26,36864
Mvaa 220,24,36
Ra 36,26,34816
Mvaa 220,28,132
Ra 132,27,34816
Mvaa 188,28,128
D 22,44,65,2048
Ra 128,27,32768
Mvaa 188,24,32
W 13
Ra 32,26,32768
Maaa 96,0,60
Maaa 96,4,156
Maaa 64,4,152
D 23,44,65,3072
Ta 16,12,32768
Maaa 64,0,56
W 9
s_add_u32 m0, s61, 0
Ta 18,12,33792
Maaa 224,0,52
Ta 20,12,40960
Ta 22,12,41984
Maaa 224,4,148
Maaa 192,4,144
D 2,40,64,0
Maaa 192,0,48
W 8
Maaa 160,0,44
Maaa 160,4,140
Mvaa 224,4,136
D 3,40,64,1024
Mvaa 224,0,40
W 4
Mvaa 192,0,36
Mvaa 192,4,132
Mvaa 160,4,128
D 4,40,64,2048
Mvaa 160,0,32
Mvaa 164,8,32
Mvaa 164,12,128
Mvaa 196,12,132
D 5,40,64,3072
Ta 24,13,32768
Mvaa 196,8,36
s_add_u32 m0, s61, 4096
Ta 26,13,33792
Ta 28,13,40960
Mvaa 228,8,40
Ta 30,13,41984
Mvaa 228,12,136
Maaa 164,12,140
D 6,40,64,0
Maaa 164,8,44
Maaa 196,8,48
Maaa 196,12,144
Maaa 228,12,148
D 7,40,64,1024
Maaa 228,8,52
Maaa 68,8,56
Maaa 68,12,152
Maaa 100,12,156
D 8,40,64,2048
Maaa 100,8,60
W 4
Maaa 104,16,60
Maaa 104,20,156
Maaa 72,20,152
D 9,40,64,3072
v_bfe_u32 v147, v1, 5, 3
Maaa 72,16,56
v_lshlrev_b32 v147, 13, v147
v_and_b32 v148, 16, v1
Maaa 232,16,52
Ta 0,14,32768
v_lshl_add_u32 v147, v148, 3, v147
Maaa 232,20,148
s_lshl_b32 s92, s57, 2
s_add_u32 s92, s92, s14
s_lshl_b32 s92, s92, 16
Maaa 200,20,144
buffer_load_dword v142, v147, s[48:51], s92 offen
Ta 2,14,33792
Maaa 200,16,48
Ta 4,14,40960
Ta 6,14,41984
Maaa 168,16,44
Maaa 168,20,140
Mvaa 232,20,136
Mvaa 232,16,40
Mvaa 200,16,36
Mvaa 200,20,132
Mvaa 168,20,128
Mvaa 168,16,32
W 4
Mvaa 172,24,32
Mvaa 172,28,128
Mvaa 204,28,132
Ta 8,15,32768
Ta 10,15,33792
Mvaa 204,24,36
Ta 12,15,40960
Ta 14,15,41984
Mvaa 236,24,40
Mvaa 236,28,136
Maaa 172,28,140
Maaa 172,24,44
Maaa 204,24,48
Maaa 204,28,144
Maaa 236,28,148
Maaa 236,24,52
Maaa 76,24,56
Maaa 76,28,152
Maaa 108,28,156
Maaa 108,24,60
W 4
Maaa 112,0,60
Maaa 112,4,156
Maaa 80,4,152
Ta 16,24,32768
Ta 18,24,33792
Maaa 80,0,56
Ta 20,24,40960
Ta 22,24,41984
Maaa 240,0,52
Maaa 240,4,148
Maaa 208,4,144
Maaa 208,0,48
Maaa 176,0,44
Maaa 176,4,140
Mvaa 240,4,136
Mvaa 240,0,40
Mvaa 208,0,36
Mvaa 208,4,132
Mvaa 176,4,128
Mvaa 176,0,32
W 4
Mvaa 180,8,32
Ta 24,25,32768
Ta 26,25,33792
Mvaa 180,12,128
Mvaa 212,12,132
Ta 28,25,40960
Ta 30,25,41984
Mvaa 212,8,36
Mvaa 244,8,40
Mvaa 244,12,136
Maaa 180,12,140
Maaa 180,8,44
Maaa 212,8,48
.Lh_0_2%=:
s_cbranch_execz .Lh_0_2_1%=
Maaa 212,12,144
Maaa 244,12,148
s_waitcnt vmcnt(1) lgkmcnt(0)
s_barrier
s_add_u32 s64, s64, 128
Maaa 244,8,52
Maaa 84,8,56
Maaa 84,12,152
s_and_b32 s64, s64, 1023
Maaa 116,12,156
Maaa 116,8,60
Maaa 120,16,60
s_lshl_b32 s65, s64, 12
Maaa 120,20,156
Maaa 88,20,152
s_add_u32 m0, s62, 32768
Ta 0,10,0
Ta 2,10,1024
Maaa 88,16,56
Ta 4,10,8192
Ta 6,10,9216
Maaa 248,16,52
Maaa 248,20,148
D 16,44,65,0
Maaa 216,20,144
Maaa 216,16,48
Maaa 184,16,44
Maaa 184,20,140
D 17,44,65,1024
Mvaa 248,20,136
Mvaa 248,16,40
Mvaa 216,16,36
Mvaa 216,20,132
D 18,44,65,2048
Mvaa 184,20,128
Mvaa 184,16,32
Mvaa 188,24,32
Ra 32,26,0
)"
R"(Mvaa 188,28,128
D 19,44,65,3072
Ra 128,27,0
Mvaa 220,28,132
s_add_u32 m0, s62, 36864
Ra 132,27,2048
Ta 8,11,0
Mvaa 220,24,36
Ta 10,11,1024
Ra 36,26,2048
Mvaa 252,24,40
Ta 12,11,8192
Ta 14,11,9216
Mvaa 252,28,136
D 20,44,65,0
Ra 40,26,4096
Maaa 188,28,140
Ra 136,27,4096
Ra 140,27,6144
Maaa 188,24,44
Ra 44,26,6144
Maaa 220,24,48
Ra 48,26,8192
Maaa 220,28,144
D 21,44,65,1024
Ra 144,27,8192
Maaa 252,28,148
Ra 148,27,10240
Maaa 252,24,52
Ra 52,26,10240
Maaa 92,24,56
Ra 56,26,12288
Maaa 92,28,152
D 22,44,65,2048
Ra 152,27,12288
Maaa 124,28,156
Ra 156,27,14336
Maaa 124,24,60
W 13
Ra 60,26,14336
Mvaa 160,0,32
Mvaa 160,4,128
D 23,44,65,3072
Mvaa 192,4,132
s_add_u32 m0, s61, 32768
Ta 16,12,0
Ta 18,12,1024
Mvaa 192,0,36
W 10
Ta 20,12,8192
Ta 22,12,9216
Mvaa 224,0,40
Mvaa 224,4,136
D 2,40,64,0
Maaa 160,4,140
Maaa 160,0,44
W 8
Maaa 192,0,48
Maaa 192,4,144
D 3,40,64,1024
Maaa 224,4,148
Maaa 224,0,52
W 4
Maaa 64,0,56
Maaa 64,4,152
D 4,40,64,2048
Maaa 96,4,156
Maaa 96,0,60
Maaa 100,8,60
Maaa 100,12,156
D 5,40,64,3072
Maaa 68,12,152
s_add_u32 m0, s61, 36864
Ta 24,13,0
Ta 26,13,1024
Maaa 68,8,56
Ta 28,13,8192
Ta 30,13,9216
Maaa 228,8,52
Maaa 228,12,148
D 6,40,64,0
Maaa 196,12,144
Maaa 196,8,48
Maaa 164,8,44
Maaa 164,12,140
D 7,40,64,1024
Mvaa 228,12,136
Mvaa 228,8,40
Mvaa 196,8,36
Mvaa 196,12,132
D 8,40,64,2048
Mvaa 164,12,128
Mvaa 164,8,32
W 4
Mvaa 168,16,32
Mvaa 168,20,128
D 9,40,64,3072
Mvaa 200,20,132
Ta 0,14,0
Ta 2,14,1024
Mvaa 200,16,36
Ta 4,14,8192
Ta 6,14,9216
Mvaa 232,16,40
Mvaa 232,20,136
global_load_dword v142, v[140:141], off
v_lshl_add_u64 v[140:141], v[140:141], 0, s[68:69]
Maaa 168,20,140
Maaa 168,16,44
Maaa 200,16,48
Maaa 200,20,144
Maaa 232,20,148
Maaa 232,16,52
Maaa 72,16,56
Maaa 72,20,152
Maaa 104,20,156
Maaa 104,16,60
W 4
Maaa 108,24,60
Maaa 108,28,156
Maaa 76,28,152
Ta 8,15,0
Ta 10,15,1024
Maaa 76,24,56
Ta 12,15,8192
Ta 14,15,9216
Maaa 236,24,52
Maaa 236,28,148
Maaa 204,28,144
Maaa 204,24,48
Maaa 172,24,44
Maaa 172,28,140
Mvaa 236,28,136
Mvaa 236,24,40
Mvaa 204,24,36
Mvaa 204,28,132
Mvaa 172,28,128
Mvaa 172,24,32
W 4
Mvaa 176,0,32
Mvaa 176,4,128
Mvaa 208,4,132
Ta 16,24,0
Ta 18,24,1024
Mvaa 208,0,36
Ta 20,24,8192
Ta 22,24,9216
Mvaa 240,0,40
Mvaa 240,4,136
Maaa 176,4,140
Maaa 176,0,44
Maaa 208,0,48
Maaa 208,4,144
Maaa 240,4,148
Maaa 240,0,52
Maaa 80,0,56
Maaa 80,4,152
Maaa 112,4,156
Maaa 112,0,60
W 4
Maaa 116,8,60
Ta 24,25,0
Ta 26,25,1024
Maaa 116,12,156
Maaa 84,12,152
Ta 28,25,8192
Ta 30,25,9216
Maaa 84,8,56
Maaa 244,8,52
Maaa 244,12,148
Maaa 212,12,144
Maaa 212,8,48
Maaa 180,8,44
Maaa 180,12,140
Mvaa 244,12,136
s_waitcnt vmcnt(1) lgkmcnt(0)
s_barrier
s_add_u32 s64, s64, 128
Mvaa 244,8,40
Mvaa 212,8,36
Mvaa 212,12,132
s_and_b32 s64, s64, 1023
Mvaa 180,12,128
Mvaa 180,8,32
Mvaa 184,16,32
s_lshl_b32 s65, s64, 12
Mvaa 184,20,128
Mvaa 216,20,132
Ta 0,10,32768
Ta 2,10,33792
Mvaa 216,16,36
s_add_u32 m0, s62, 0
Ta 4,10,40960
Ta 6,10,41984
Mvaa 248,16,40
Mvaa 248,20,136
Maaa 184,20,140
D 16,44,65,0
Maaa 184,16,44
Maaa 216,16,48
Maaa 216,20,144
Maaa 248,20,148
D 17,44,65,1024
v_lshrrev_b32 v28, 1, v1
Maaa 248,16,52
s_lshl_b32 s80, s60, 9
Maaa 88,16,56
s_add_u32 s80, s80, 40960
Maaa 88,20,152
Maaa 120,20,156
D 18,44,65,2048
Maaa 120,16,60
Maaa 124,24,60
Ra 60,26,47104
Maaa 124,28,156
Ra 156,27,47104
Maaa 92,28,152
buffer_load_dwordx2 v[28:29], v28, s[8:11], s80 offen sc1
Ra 152,27,45056
Maaa 92,24,56
Ta 8,11,32768
Ta 10,11,33792
Maaa 252,24,52
Ra 56,26,45056
Ta 12,11,40960
Maaa 252,28,148
Ta 14,11,41984
Ra 52,26,43008
Maaa 220,28,144
D 19,44,65,3072
Ra 148,27,43008
Maaa 220,24,48
s_add_u32 m0, s62, 4096
Ra 144,27,40960
Ra 48,26,40960
Maaa 188,24,44
Ra 44,26,38912
Maaa 188,28,140
Ra 140,27,38912
Mvaa 252,28,136
D 20,44,65,0
Ra 136,27,36864
Mvaa 252,24,40
Ra 40,26,36864
Mvaa 220,24,36
Ra 36,26,34816
Mvaa 220,28,132
Ra 132,27,34816
Mvaa 188,28,128
D 21,44,65,1024
Ra 128,27,32768
Mvaa 188,24,32
W 13
Ra 32,26,32768
Maaa 96,0,60
Maaa 96,4,156
Maaa 64,4,152
D 22,44,65,2048
Ta 16,12,32768
Maaa 64,0,56
W 9
Ta 18,12,33792
Ta 20,12,40960
Maaa 224,0,52
Ta 22,12,41984
Maaa 224,4,148
Maaa 192,4,144
D 23,44,65,3072
Maaa 192,0,48
W 8
s_add_u32 m0, s61, 0
Maaa 160,0,44
Maaa 160,4,140
Mvaa 224,4,136
D 2,40,64,0
Mvaa 224,0,40
W 4
Mvaa 192,0,36
Mvaa 192,4,132
Mvaa 160,4,128
D 3,40,64,1024
Mvaa 160,0,32
Mvaa 164,8,32
Mvaa 164,12,128
Mvaa 196,12,132
D 4,40,64,2048
Ta 24,13,32768
Mvaa 196,8,36
Ta 26,13,33792
Ta 28,13,40960
Mvaa 228,8,40
Ta 30,13,41984
Mvaa 228,12,136
Maaa 164,12,140
D 5,40,64,3072
Maaa 164,8,44
s_add_u32 m0, s61, 4096
Maaa 196,8,48
Maaa 196,12,144
Maaa 228,12,148
D 6,40,64,0
Maaa 228,8,52
Maaa 68,8,56
Maaa 68,12,152
Maaa 100,12,156
D 7,40,64,1024
Maaa 100,8,60
W 4
Maaa 104,16,60
s_waitcnt vmcnt(11)
v_cmp_eq_u32 vcc, s13, v29
s_cmp_eq_u64 vcc, -1
s_cbranch_scc0 .Lspin_15%=
.Lok_16%=:
Maaa 104,20,156
Maaa 72,20,152
D 8,40,64,2048
Ta 0,14,32768
Maaa 72,16,56
Ta 2,14,33792
v_bfe_u32 v144, v1, 8, 1
Maaa 232,16,52
Ta 4,14,40960
v_mul_u32_u24 v144, 10240, v144
Maaa 232,20,148
Ta 6,14,41984
v_and_b32 v145, 0xf0, v1
Maaa 200,20,144
D 9,40,64,3072
v_lshl_add_u32 v144, v145, 3, v144
Maaa 200,16,48
s_mul_i32 s91, s58, 20480
s_add_u32 s91, s91, s28
Maaa 168,16,44
Maaa 168,20,140
Mvaa 232,20,136
global_load_dword v142, v146, s[6:7]
Mvaa 232,16,40
s_add_u32 s92, s92, 2097152
Mvaa 200,16,36
Mvaa 200,20,132
Mvaa 168,20,128
buffer_load_dword v142, v147, s[48:51], s92 offen
Mvaa 168,16,32
W 4
s_add_u32 s92, s92, 2097152
Mvaa 172,24,32
Mvaa 172,28,128
Mvaa 204,28,132
buffer_load_dword v142, v147, s[48:51], s92 offen
Ta 8,15,32768
Mvaa 204,24,36
Ta 10,15,33792
Ta 12,15,40960
Mvaa 236,24,40
Ta 14,15,41984
Mvaa 236,28,136
Maaa 172,28,140
buffer_load_dword v142, v144, s[8:11], s91 offen offset:0 sc1
Maaa 172,24,44
Maaa 204,24,48
Maaa 204,28,144
Maaa 236,28,148
Maaa 236,24,52
Maaa 76,24,56
Maaa 76,28,152
Maaa 108,28,156
Maaa 108,24,60
W 4
Maaa 112,0,60
Maaa 112,4,156
Maaa 80,4,152
Ta 16,24,32768
Ta 18,24,33792
Maaa 80,0,56
Ta 20,24,40960
Ta 22,24,41984
Maaa 240,0,52
Maaa 240,4,148
Maaa 208,4,144
Maaa 208,0,48
Maaa 176,0,44
Maaa 176,4,140
Mvaa 240,4,136
Mvaa 240,0,40
Mvaa 208,0,36
Mvaa 208,4,132
Mvaa 176,4,128
Mvaa 176,0,32
W 4
Mvaa 180,8,32
Ta 24,25,32768
Ta 26,25,33792
Mvaa 180,12,128
Mvaa 212,12,132
Ta 28,25,40960
Ta 30,25,41984
Mvaa 212,8,36
Mvaa 244,8,40
Mvaa 244,12,136
)"
R"(Maaa 180,12,140
Maaa 180,8,44
Maaa 212,8,48
Maaa 212,12,144
Maaa 244,12,148
s_waitcnt vmcnt(4) lgkmcnt(0)
s_barrier
s_add_u32 s64, s64, 128
Maaa 244,8,52
Maaa 84,8,56
Maaa 84,12,152
s_and_b32 s64, s64, 1023
Maaa 116,12,156
Maaa 116,8,60
Maaa 120,16,60
s_lshl_b32 s65, s64, 12
Maaa 120,20,156
Maaa 88,20,152
Ta 0,10,0
Ta 2,10,1024
Maaa 88,16,56
s_add_u32 m0, s62, 32768
Ta 4,10,8192
Ta 6,10,9216
Maaa 248,16,52
Maaa 248,20,148
Maaa 216,20,144
D 16,44,65,0
Maaa 216,16,48
Maaa 184,16,44
Maaa 184,20,140
Mvaa 248,20,136
D 17,44,65,1024
Mvaa 248,16,40
Mvaa 216,16,36
Mvaa 216,20,132
Mvaa 184,20,128
D 18,44,65,2048
Mvaa 184,16,32
Mvaa 188,24,32
Ra 32,26,0
Mvaa 188,28,128
Ra 128,27,0
Mvaa 220,28,132
D 19,44,65,3072
Ra 132,27,2048
Mvaa 220,24,36
s_add_u32 m0, s62, 36864
Ta 8,11,0
Ta 10,11,1024
Mvaa 252,24,40
Ra 36,26,2048
Ta 12,11,8192
Mvaa 252,28,136
Ta 14,11,9216
Ra 40,26,4096
Maaa 188,28,140
D 20,44,65,0
Ra 136,27,4096
Maaa 188,24,44
Ra 140,27,6144
Ra 44,26,6144
Maaa 220,24,48
Ra 48,26,8192
Maaa 220,28,144
Ra 144,27,8192
Maaa 252,28,148
D 21,44,65,1024
Ra 148,27,10240
Maaa 252,24,52
Ra 52,26,10240
Maaa 92,24,56
Ra 56,26,12288
Maaa 92,28,152
Ra 152,27,12288
Maaa 124,28,156
D 22,44,65,2048
Ra 156,27,14336
Maaa 124,24,60
W 13
Ra 60,26,14336
Mvaa 160,0,32
Mvaa 160,4,128
Mvaa 192,4,132
D 23,44,65,3072
Ta 16,12,0
Mvaa 192,0,36
W 9
s_add_u32 m0, s61, 32768
Ta 18,12,1024
Mvaa 224,0,40
Ta 20,12,8192
Ta 22,12,9216
Mvaa 224,4,136
.Lh_0_3%=:
s_cbranch_execz .Lh_0_3_1%=
Maaa 160,4,140
D 2,40,64,0
Maaa 160,0,44
W 8
Maaa 192,0,48
Maaa 192,4,144
Maaa 224,4,148
D 3,40,64,1024
Maaa 224,0,52
W 4
Maaa 64,0,56
Maaa 64,4,152
Maaa 96,4,156
D 4,40,64,2048
Maaa 96,0,60
Maaa 100,8,60
Maaa 100,12,156
Maaa 68,12,152
D 5,40,64,3072
Ta 24,13,0
Maaa 68,8,56
s_add_u32 m0, s61, 36864
Ta 26,13,1024
Ta 28,13,8192
Maaa 228,8,52
Ta 30,13,9216
Maaa 228,12,148
Maaa 196,12,144
D 6,40,64,0
Maaa 196,8,48
Maaa 164,8,44
Maaa 164,12,140
Mvaa 228,12,136
D 7,40,64,1024
Mvaa 228,8,40
Mvaa 196,8,36
Mvaa 196,12,132
Mvaa 164,12,128
D 8,40,64,2048
Mvaa 164,8,32
W 4
Mvaa 168,16,32
Mvaa 168,20,128
Mvaa 200,20,132
D 9,40,64,3072
v_add_u32 v2, 131072, v10
Mvaa 200,16,36
Ta 0,14,0
v_add_u32 v3, 131072, v11
Mvaa 232,16,40
Ta 2,14,1024
v_add_u32 v4, 131072, v12
Mvaa 232,20,136
Ta 4,14,8192
Ta 6,14,9216
Maaa 168,20,140
buffer_load_dword v142, v144, s[8:11], s91 offen offset:2048 sc1
v_add_u32 v5, 131072, v13
Maaa 168,16,44
s_add_u32 s91, s91, 4096
v_add_u32 v6, 131072, v14
Maaa 200,16,48
v_add_u32 v7, 131072, v15
Maaa 200,20,144
Maaa 232,20,148
v_add_u32 v8, 131072, v24
Maaa 232,16,52
Maaa 72,16,56
v_add_u32 v9, 131072, v25
Maaa 72,20,152
s_add_u32 s65, s5, 0
s_cmp_ge_u32 s65, 20
s_cselect_b32 s90, 20, 0
s_sub_u32 s65, s65, s90
Maaa 104,20,156
s_lshl_b32 s65, s65, 18
s_add_u32 m0, s63, 131072
Maaa 104,16,60
W 4
Maaa 108,24,60
Maaa 108,28,156
Maaa 76,28,152
Ta 8,15,0
Ta 10,15,1024
Maaa 76,24,56
D 16,48,65,0
Ta 12,15,8192
Maaa 236,24,52
Ta 14,15,9216
Maaa 236,28,148
Maaa 204,28,144
Maaa 204,24,48
D 17,48,65,1024
Maaa 172,24,44
Maaa 172,28,140
Mvaa 236,28,136
Mvaa 236,24,40
Mvaa 204,24,36
D 18,48,65,2048
Mvaa 204,28,132
Mvaa 172,28,128
Mvaa 172,24,32
W 4
Mvaa 176,0,32
Mvaa 176,4,128
D 19,48,65,3072
Mvaa 208,4,132
s_add_u32 m0, s63, 135168
Ta 16,24,0
Ta 18,24,1024
Mvaa 208,0,36
Ta 20,24,8192
Ta 22,24,9216
Mvaa 240,0,40
Mvaa 240,4,136
Maaa 176,4,140
D 20,48,65,0
Maaa 176,0,44
Maaa 208,0,48
Maaa 208,4,144
Maaa 240,4,148
Maaa 240,0,52
D 21,48,65,1024
Maaa 80,0,56
Maaa 80,4,152
Maaa 112,4,156
Maaa 112,0,60
W 4
Maaa 116,8,60
D 22,48,65,2048
Ta 24,25,0
Maaa 116,12,156
Ta 26,25,1024
Maaa 84,12,152
Ta 28,25,8192
Ta 30,25,9216
Maaa 84,8,56
Maaa 244,8,52
D 23,48,65,3072
Maaa 244,12,148
Maaa 212,12,144
Maaa 212,8,48
Maaa 180,8,44
Maaa 180,12,140
Mvaa 244,12,136
s_waitcnt vmcnt(9) lgkmcnt(0)
s_barrier
s_add_u32 s65, s5, 4
Mvaa 244,8,40
Mvaa 212,8,36
Mvaa 212,12,132
s_cmp_ge_u32 s65, 20
s_cselect_b32 s90, 20, 0
Mvaa 180,12,128
Mvaa 180,8,32
s_sub_u32 s65, s65, s90
Mvaa 184,16,32
Mvaa 184,20,128
s_lshl_b32 s65, s65, 18
Mvaa 216,20,132
Ta 0,10,32768
Ta 2,10,33792
Mvaa 216,16,36
Mvaa 248,16,40
s_add_u32 m0, s63, 0
Mvaa 248,20,136
Maaa 184,20,140
D 16,48,65,0
Maaa 184,16,44
Maaa 216,16,48
Maaa 216,20,144
Maaa 248,20,148
D 17,48,65,1024
Ta 4,11,32768
Maaa 248,16,52
Ta 6,11,33792
Maaa 88,16,56
Maaa 88,20,152
Maaa 120,20,156
D 18,48,65,2048
Maaa 120,16,60
Maaa 124,24,60
Ra 60,26,47104
Maaa 124,28,156
Maaa 92,28,152
D 19,48,65,3072
Ta 8,12,32768
Maaa 92,24,56
s_add_u32 m0, s63, 4096
Ta 10,12,33792
Ra 56,26,45056
Maaa 252,24,52
Ra 52,26,43008
Maaa 252,28,148
Maaa 220,28,144
D 20,48,65,0
Maaa 220,24,48
Ra 48,26,40960
Maaa 188,24,44
Ra 44,26,38912
Maaa 188,28,140
Mvaa 252,28,136
D 21,48,65,1024
Ta 12,13,32768
Mvaa 252,24,40
Ta 14,13,33792
Ra 40,26,36864
Mvaa 220,24,36
Ra 36,26,34816
Mvaa 220,28,132
Mvaa 188,28,128
D 22,48,65,2048
Mvaa 188,24,32
Ra 32,26,32768
W 6
Maaa 96,0,60
Maaa 64,0,56
Maaa 224,0,52
D 23,48,65,3072
Ta 16,14,32768
Maaa 192,0,48
W 1
s_add_u32 s65, s5, 8
s_cmp_ge_u32 s65, 20
s_cselect_b32 s90, 20, 0
Maaa 160,0,44
s_sub_u32 s65, s65, s90
s_lshl_b32 s65, s65, 18
Ta 18,14,33792
Mvaa 224,0,40
s_add_u32 m0, s63, 65536
Mvaa 192,0,36
D 16,48,65,0
Mvaa 160,0,32
Mvaa 164,4,32
Mvaa 196,4,36
Mvaa 228,4,40
D 17,48,65,1024
Ta 20,15,32768
Maaa 164,4,44
Ta 22,15,33792
Maaa 196,4,48
Maaa 228,4,52
Maaa 68,4,56
D 18,48,65,2048
Maaa 100,4,60
Maaa 104,8,60
Maaa 72,8,56
Maaa 232,8,52
D 19,48,65,3072
Ta 24,24,32768
Maaa 200,8,48
s_add_u32 m0, s63, 69632
Ta 26,24,33792
Maaa 168,8,44
Mvaa 232,8,40
Mvaa 200,8,36
D 20,48,65,0
Mvaa 168,8,32
Mvaa 172,12,32
Mvaa 204,12,36
Mvaa 236,12,40
D 21,48,65,1024
Ta 28,25,32768
Maaa 172,12,44
Ta 30,25,33792
Maaa 204,12,48
Ra 156,27,47104
Maaa 236,12,52
Maaa 76,12,56
D 22,48,65,2048
Maaa 108,12,60
W 7
Ra 152,27,45056
Maaa 112,16,60
Maaa 80,16,56
Maaa 240,16,52
D 23,48,65,3072
Ra 148,27,43008
Maaa 208,16,48
Ta 0,10,40960
Ta 2,10,41984
Maaa 176,16,44
Mvaa 240,16,40
Ra 144,27,40960
Mvaa 208,16,36
buffer_load_dword v142, v144, s[8:11], s91 offen offset:0 sc1
Mvaa 176,16,32
W 10
Mvaa 180,20,32
Ra 140,27,38912
Mvaa 212,20,36
Mvaa 244,20,40
Ta 4,11,40960
Ta 6,11,41984
Maaa 180,20,44
Ra 136,27,36864
Maaa 212,20,48
Maaa 244,20,52
v_bfe_u32 v29, v1, 7, 1
Maaa 84,20,56
Ra 132,27,34816
Maaa 116,20,60
W 13
)"
R"(v_mul_u32_u24 v28, 10240, v29
Maaa 120,24,60
Maaa 88,24,56
Ra 128,27,32768
v_lshrrev_b32 v29, 8, v1
Maaa 248,24,52
Ta 8,12,40960
Ta 10,12,41984
Maaa 216,24,48
v_lshl_add_u32 v28, v29, 7, v28
Maaa 184,24,44
v_and_b32 v29, 0x70, v1
Mvaa 248,24,40
Mvaa 216,24,36
v_add_u32 v28, v28, v29
Mvaa 184,24,32
W 14
Mvaa 188,28,32
s_and_b32 s80, s14, 2
Mvaa 220,28,36
s_mul_i32 s80, s80, 20480
Mvaa 252,28,40
Ta 12,13,40960
Ta 14,13,41984
Maaa 188,28,44
s_sub_u32 s81, 40960, s80
Maaa 220,28,48
Maaa 252,28,52
s_add_u32 s93, s28, 20480
Maaa 92,28,56
Maaa 124,28,60
W 10
v_add_u32 v26, s80, v28
Maaa 96,0,156
Maaa 64,0,152
Maaa 224,0,148
Ta 16,14,40960
v_add_u32 v27, s81, v28
Maaa 192,0,144
W 5
Ta 18,14,41984
Maaa 160,0,140
buffer_load_dwordx4 a[32:35], v26, s[8:11], s28 offen offset:0 sc1
Mvaa 224,0,136
Mvaa 192,0,132
Mvaa 160,0,128
Mvaa 164,4,128
buffer_load_dwordx4 a[36:39], v26, s[8:11], s93 offen offset:0 sc1
Mvaa 196,4,132
Mvaa 228,4,136
Ta 20,15,40960
Ta 22,15,41984
Maaa 164,4,140
Maaa 196,4,144
buffer_load_dwordx4 a[40:43], v27, s[8:11], s28 offen offset:0 sc1
Maaa 228,4,148
Maaa 68,4,152
Maaa 100,4,156
W 6
Maaa 104,8,156
buffer_load_dwordx4 a[44:47], v27, s[8:11], s93 offen offset:0 sc1
Ta 24,24,40960
Maaa 72,8,152
s_add_u32 s28, s28, 512
s_cmp_eq_u32 s28, s4
s_cselect_b32 s93, 10240, 0
Ta 26,24,41984
Maaa 232,8,148
s_sub_u32 s28, s28, s93
Ta 28,25,40960
Ta 30,25,41984
Maaa 200,8,144
s_add_u32 s93, s28, 20480
Maaa 168,8,140
Mvaa 232,8,136
Mvaa 200,8,132
s_add_u32 s65, s5, 12
s_cmp_ge_u32 s65, 20
s_cselect_b32 s90, 20, 0
buffer_load_dwordx4 a[48:51], v26, s[8:11], s28 offen offset:0 sc1
Mvaa 168,8,128
W 8
s_sub_u32 s65, s65, s90
s_lshl_b32 s65, s65, 18
Mvaa 172,12,128
s_add_u32 m0, s63, 32768
Mvaa 204,12,132
Mvaa 236,12,136
s_waitcnt vmcnt(6) lgkmcnt(0)
s_barrier
buffer_load_dwordx4 a[52:55], v26, s[8:11], s93 offen offset:0 sc1
Maaa 172,12,140
Maaa 204,12,144
Maaa 236,12,148
Maaa 76,12,152
buffer_load_dwordx4 a[56:59], v27, s[8:11], s28 offen offset:0 sc1
Maaa 108,12,156
Maaa 112,16,156
Maaa 80,16,152
Maaa 240,16,148
buffer_load_dwordx4 a[60:63], v27, s[8:11], s93 offen offset:0 sc1
Maaa 208,16,144
Maaa 176,16,140
Mvaa 240,16,136
Mvaa 208,16,132
D 16,48,65,0
Mvaa 176,16,128
Mvaa 180,20,128
Mvaa 212,20,132
Mvaa 244,20,136
D 17,48,65,1024
Ta 0,2,0
Maaa 180,20,140
Ta 2,2,1024
Maaa 212,20,144
Maaa 244,20,148
Maaa 84,20,152
D 18,48,65,2048
Ta 4,2,8192
Maaa 116,20,156
Ta 6,2,9216
Maaa 120,24,156
Maaa 88,24,152
Maaa 248,24,148
Ta 8,3,0
Ta 10,3,1024
Maaa 216,24,144
D 19,48,65,3072
Maaa 184,24,140
s_add_u32 m0, s63, 36864
Mvaa 248,24,136
Mvaa 216,24,132
Ta 12,3,8192
Ta 14,3,9216
Mvaa 184,24,128
D 20,48,65,0
Mvaa 188,28,128
Mvaa 220,28,132
Mvaa 252,28,136
Ta 16,4,0
Ta 18,4,1024
Maaa 188,28,140
Maaa 220,28,144
D 21,48,65,1024
Maaa 252,28,148
Maaa 92,28,152
Maaa 124,28,156
s_branch .Lq_11%=
.p2align 12
.Lv_10%=:
s_waitcnt vmcnt(14)
s_barrier
Ta 0,10,16384
Ta 2,10,17408
Ra 32,26,0
Ta 4,10,24576
.Lh_0_0_1%=:
s_cbranch_execz .Lhr_9%=
Ta 6,10,25600
Ra 128,27,0
Ra 132,27,2048
Ra 36,26,2048
Ra 40,26,4096
Ra 136,27,4096
Ra 140,27,6144
Ra 44,26,6144
Ra 48,26,8192
Ra 144,27,8192
W 6
Zvaa 160,32,0
s_add_u32 m0, s61, 32768
Ra 148,27,10240
Ra 52,26,10240
Ra 56,26,12288
Ra 152,27,12288
Ra 156,27,14336
Ra 60,26,14336
Mvaa 160,128,4
Ta 8,11,16384
Ta 10,11,17408
Ta 12,11,24576
Ta 14,11,25600
Zvaa 164,132,4
Ta 16,12,16384
Ta 18,12,17408
Mvaa 164,36,0
W 14
Ta 20,12,24576
Ta 22,12,25600
Zvaa 224,40,0
D 2,40,64,0
Mvaa 224,136,4
Zvaa 228,140,4
Mvaa 228,44,0
W 12
Zaaa 192,48,0
D 3,40,64,1024
Maaa 192,144,4
Zaaa 196,148,4
s_waitcnt vmcnt(13)
Maaa 196,52,0
W 8
v_lshlrev_b32 v56, 16, v32
v_and_b32 v57, 0xffff0000, v32
v_lshlrev_b32 v58, 16, v33
v_and_b32 v59, 0xffff0000, v33
Zaaa 64,56,0
D 4,40,64,2048
v_lshlrev_b32 v60, 16, v34
v_and_b32 v61, 0xffff0000, v34
v_lshlrev_b32 v62, 16, v35
v_and_b32 v63, 0xffff0000, v35
Maaa 64,152,4
v_lshlrev_b32 v64, 16, v36
v_and_b32 v65, 0xffff0000, v36
v_lshlrev_b32 v66, 16, v37
v_and_b32 v67, 0xffff0000, v37
v_lshlrev_b32 v68, 16, v38
Zaaa 68,156,4
v_and_b32 v69, 0xffff0000, v38
v_lshlrev_b32 v70, 16, v39
v_and_b32 v71, 0xffff0000, v39
v_lshlrev_b32 v72, 16, v40
Maaa 68,60,0
W 4
v_and_b32 v73, 0xffff0000, v40
v_lshlrev_b32 v74, 16, v41
v_and_b32 v75, 0xffff0000, v41
v_lshlrev_b32 v76, 16, v42
v_and_b32 v77, 0xffff0000, v42
Zaaa 76,60,8
D 5,40,64,3072
v_lshlrev_b32 v78, 16, v43
v_and_b32 v79, 0xffff0000, v43
A 80,56,57
A 81,58,59
Maaa 76,156,12
s_add_u32 m0, s61, 36864
A 80,80,60
A 81,81,61
A 80,80,62
A 81,81,63
A 80,80,64
Zaaa 72,152,12
Ta 24,13,16384
Ta 26,13,17408
A 81,81,65
A 80,80,66
A 81,81,67
A 80,80,68
Maaa 72,56,8
Ta 28,13,24576
Ta 30,13,25600
A 81,81,69
A 80,80,70
A 81,81,71
A 80,80,72
Zaaa 204,52,8
D 6,40,64,0
A 81,81,73
A 80,80,74
A 81,81,75
A 80,80,76
A 81,81,77
Maaa 204,148,12
A 80,80,78
A 81,81,79
A 80,80,81
s_nop 1
v_add_f32_dpp v80, v80, v80 quad_perm:[1,0,3,2] row_mask:0xf bank_mask:0xf
Zaaa 200,144,12
Maaa 200,48,8
v_add_f32_dpp v80, v80, v80 quad_perm:[2,3,0,1] row_mask:0xf bank_mask:0xf
s_nop 1
v_add_f32_dpp v80, v80, v80 row_half_mirror row_mask:0xf bank_mask:0xf
s_nop 1
v_add_f32_dpp v80, v80, v80 row_mirror row_mask:0xf bank_mask:0xf
v_mov_b32 v81, v80
s_nop 1
v_permlane16_swap_b32 v80, v81
A 80,80,81
Zvaa 236,44,8
D 7,40,64,1024
v_mul_f32 v84, 0x3acccccd, v80
s_mov_b32 s86, 0xffff
s_mov_b32 s87, 0xffff
v_sub_f32 v82, v56, v84
U 80,82,82
v_sub_f32 v83, v57, v84
Mvaa 236,140,12
v_fmac_f32 v80, v83, v83
v_sub_f32 v82, v58, v84
v_fmac_f32 v80, v82, v82
v_sub_f32 v83, v59, v84
v_fmac_f32 v80, v83, v83
v_sub_f32 v82, v60, v84
Zvaa 232,136,12
v_fmac_f32 v80, v82, v82
v_sub_f32 v83, v61, v84
v_fmac_f32 v80, v83, v83
v_sub_f32 v82, v62, v84
v_fmac_f32 v80, v82, v82
Mvaa 232,40,8
v_sub_f32 v83, v63, v84
v_fmac_f32 v80, v83, v83
v_sub_f32 v82, v64, v84
v_fmac_f32 v80, v82, v82
Zvaa 172,36,8
D 8,40,64,2048
v_sub_f32 v83, v65, v84
v_fmac_f32 v80, v83, v83
v_sub_f32 v82, v66, v84
v_fmac_f32 v80, v82, v82
Mvaa 172,132,12
v_sub_f32 v83, v67, v84
v_fmac_f32 v80, v83, v83
v_sub_f32 v82, v68, v84
v_fmac_f32 v80, v82, v82
v_sub_f32 v83, v69, v84
Zvaa 168,128,12
v_fmac_f32 v80, v83, v83
v_sub_f32 v82, v70, v84
v_fmac_f32 v80, v82, v82
v_sub_f32 v83, v71, v84
Mvaa 168,32,8
W 4
v_fmac_f32 v80, v83, v83
v_sub_f32 v82, v72, v84
U 81,82,82
v_sub_f32 v83, v73, v84
v_fmac_f32 v81, v83, v83
Zvaa 176,32,16
D 9,40,64,3072
v_sub_f32 v82, v74, v84
v_fmac_f32 v81, v82, v82
v_sub_f32 v83, v75, v84
v_fmac_f32 v81, v83, v83
Mvaa 176,128,20
v_sub_f32 v82, v76, v84
v_fmac_f32 v81, v82, v82
v_sub_f32 v83, v77, v84
v_fmac_f32 v81, v83, v83
v_sub_f32 v82, v78, v84
Zvaa 180,132,20
Ta 0,14,16384
Ta 2,14,17408
v_fmac_f32 v81, v82, v82
v_sub_f32 v83, v79, v84
v_fmac_f32 v81, v83, v83
v_cndmask_b32_e64 v81, 0, v81, s[86:87]
Mvaa 180,36,16
Ta 4,14,24576
Ta 6,14,25600
A 80,80,81
s_nop 1
v_add_f32_dpp v80, v80, v80 quad_perm:[1,0,3,2] row_mask:0xf bank_mask:0xf
s_nop 1
v_add_f32_dpp v80, v80, v80 quad_perm:[2,3,0,1] row_mask:0xf bank_mask:0xf
s_nop 1
v_add_f32_dpp v80, v80, v80 row_half_mirror row_mask:0xf bank_mask:0xf
Zvaa 240,40,16
global_load_dword v142, v[140:141], off
v_lshl_add_u64 v[140:141], v[140:141], 0, s[68:69]
Mvaa 240,136,20
v_add_f32_dpp v80, v80, v80 row_mirror row_mask:0xf bank_mask:0xf
v_mov_b32 v81, v80
s_nop 1
v_permlane16_swap_b32 v80, v81
A 80,80,81
v_mul_f32 v80, 0x3acccccd, v80
K 80,80,80
Zvaa 244,140,20
v_lshlrev_b32 v80, 16, v80
v_add_f32 v80, 0x3727c5ac, v80
K 80,80,80
v_lshlrev_b32 v80, 16, v80
v_sqrt_f32 v81, v80
Mvaa 244,44,16
v_rcp_f32 v82, v81
v_fma_f32 v83, -v81, v81, v80
v_mul_f32 v82, 0.5, v82
v_fma_f32 v81, v83, v82, v81
K 86,81,81
Zaaa 208,48,16
v_lshlrev_b32 v86, 16, v86
v_rcp_f32 v87, v86
K 85,84,84
v_lshlrev_b32 v85, 16, v85
Maaa 208,144,20
s_waitcnt vmcnt(17)
v_sub_f32 v56, v56, v85
v_sub_f32 v57, v57, v85
K 80,56,57
Zaaa 212,148,20
v_lshlrev_b32 v56, 16, v80
v_and_b32 v57, 0xffff0000, v80
U 80,56,87
U 81,57,87
K 32,80,81
Maaa 212,52,16
)"
R"(v_lshlrev_b32 v80, 16, v32
v_and_b32 v81, 0xffff0000, v32
v_lshlrev_b32 v82, 16, v44
v_and_b32 v83, 0xffff0000, v44
Zaaa 80,56,16
U 84,80,82
v_fmac_f32 v84, v81, v83
v_sub_f32 v58, v58, v85
v_sub_f32 v59, v59, v85
Maaa 80,152,20
K 80,58,59
v_lshlrev_b32 v58, 16, v80
v_and_b32 v59, 0xffff0000, v80
U 80,58,87
U 81,59,87
Zaaa 84,156,20
K 33,80,81
v_lshlrev_b32 v80, 16, v33
v_and_b32 v81, 0xffff0000, v33
v_lshlrev_b32 v82, 16, v45
Maaa 84,60,16
W 4
v_and_b32 v83, 0xffff0000, v45
v_fmac_f32 v84, v80, v82
v_fmac_f32 v84, v81, v83
v_sub_f32 v60, v60, v85
v_sub_f32 v61, v61, v85
Zaaa 92,60,24
K 80,60,61
v_lshlrev_b32 v60, 16, v80
v_and_b32 v61, 0xffff0000, v80
U 80,60,87
Maaa 92,156,28
U 81,61,87
K 34,80,81
v_lshlrev_b32 v80, 16, v34
v_and_b32 v81, 0xffff0000, v34
v_lshlrev_b32 v82, 16, v46
Zaaa 88,152,28
Ta 8,15,16384
Ta 10,15,17408
v_and_b32 v83, 0xffff0000, v46
v_fmac_f32 v84, v80, v82
v_fmac_f32 v84, v81, v83
v_sub_f32 v62, v62, v85
Maaa 88,56,24
Ta 12,15,24576
v_sub_f32 v63, v63, v85
K 80,62,63
v_lshlrev_b32 v62, 16, v80
v_and_b32 v63, 0xffff0000, v80
U 80,62,87
Zaaa 220,52,24
Ta 14,15,25600
U 81,63,87
K 35,80,81
v_lshlrev_b32 v80, 16, v35
v_and_b32 v81, 0xffff0000, v35
Maaa 220,148,28
v_lshlrev_b32 v82, 16, v47
v_and_b32 v83, 0xffff0000, v47
v_fmac_f32 v84, v80, v82
v_fmac_f32 v84, v81, v83
Zaaa 216,144,28
v_sub_f32 v64, v64, v85
v_sub_f32 v65, v65, v85
K 80,64,65
v_lshlrev_b32 v64, 16, v80
v_and_b32 v65, 0xffff0000, v80
Maaa 216,48,24
U 80,64,87
U 81,65,87
K 36,80,81
v_lshlrev_b32 v80, 16, v36
Zvaa 252,44,24
v_and_b32 v81, 0xffff0000, v36
v_lshlrev_b32 v82, 16, v48
v_and_b32 v83, 0xffff0000, v48
v_fmac_f32 v84, v80, v82
v_fmac_f32 v84, v81, v83
Mvaa 252,140,28
v_sub_f32 v66, v66, v85
v_sub_f32 v67, v67, v85
K 80,66,67
v_lshlrev_b32 v66, 16, v80
Zvaa 248,136,28
v_and_b32 v67, 0xffff0000, v80
U 80,66,87
U 81,67,87
K 37,80,81
v_lshlrev_b32 v80, 16, v37
Mvaa 248,40,24
v_and_b32 v81, 0xffff0000, v37
v_lshlrev_b32 v82, 16, v49
v_and_b32 v83, 0xffff0000, v49
v_fmac_f32 v84, v80, v82
Zvaa 188,36,24
v_fmac_f32 v84, v81, v83
v_sub_f32 v68, v68, v85
v_sub_f32 v69, v69, v85
K 80,68,69
v_lshlrev_b32 v68, 16, v80
Mvaa 188,132,28
v_and_b32 v69, 0xffff0000, v80
U 80,68,87
U 81,69,87
K 38,80,81
Zvaa 184,128,28
v_lshlrev_b32 v80, 16, v38
v_and_b32 v81, 0xffff0000, v38
v_lshlrev_b32 v82, 16, v50
v_and_b32 v83, 0xffff0000, v50
Mvaa 184,32,24
W 4
v_fmac_f32 v84, v80, v82
v_fmac_f32 v84, v81, v83
v_sub_f32 v70, v70, v85
v_sub_f32 v71, v71, v85
K 80,70,71
Zvaa 192,32,0
v_lshlrev_b32 v70, 16, v80
v_and_b32 v71, 0xffff0000, v80
U 80,70,87
U 81,71,87
Mvaa 192,128,4
K 39,80,81
v_lshlrev_b32 v80, 16, v39
v_and_b32 v81, 0xffff0000, v39
v_lshlrev_b32 v82, 16, v51
v_and_b32 v83, 0xffff0000, v51
Zvaa 196,132,4
Ta 16,24,16384
Ta 18,24,17408
v_fmac_f32 v84, v80, v82
v_fmac_f32 v84, v81, v83
v_sub_f32 v72, v72, v85
v_sub_f32 v73, v73, v85
Mvaa 196,36,0
Ta 20,24,24576
K 80,72,73
v_lshlrev_b32 v72, 16, v80
v_and_b32 v73, 0xffff0000, v80
U 80,72,87
U 81,73,87
Zaaa 160,40,0
Ta 22,24,25600
K 40,80,81
v_lshlrev_b32 v80, 16, v40
v_and_b32 v81, 0xffff0000, v40
v_lshlrev_b32 v82, 16, v52
Maaa 160,136,4
v_and_b32 v83, 0xffff0000, v52
v_fmac_f32 v84, v80, v82
v_fmac_f32 v84, v81, v83
v_sub_f32 v74, v74, v85
Zaaa 164,140,4
v_sub_f32 v75, v75, v85
K 80,74,75
v_lshlrev_b32 v74, 16, v80
v_and_b32 v75, 0xffff0000, v80
U 80,74,87
Maaa 164,44,0
U 81,75,87
K 41,80,81
v_lshlrev_b32 v80, 16, v41
v_and_b32 v81, 0xffff0000, v41
Zaaa 224,48,0
v_lshlrev_b32 v82, 16, v53
v_and_b32 v83, 0xffff0000, v53
v_fmac_f32 v84, v80, v82
v_fmac_f32 v84, v81, v83
v_sub_f32 v76, v76, v85
Maaa 224,144,4
v_sub_f32 v77, v77, v85
K 80,76,77
v_lshlrev_b32 v76, 16, v80
v_and_b32 v77, 0xffff0000, v80
Zaaa 228,148,4
U 80,76,87
U 81,77,87
K 42,80,81
v_lshlrev_b32 v80, 16, v42
v_and_b32 v81, 0xffff0000, v42
Maaa 228,52,0
v_lshlrev_b32 v82, 16, v54
v_and_b32 v83, 0xffff0000, v54
v_fmac_f32 v84, v80, v82
v_fmac_f32 v84, v81, v83
Zaaa 96,56,0
v_sub_f32 v78, v78, v85
v_sub_f32 v79, v79, v85
K 80,78,79
v_lshlrev_b32 v78, 16, v80
v_and_b32 v79, 0xffff0000, v80
Maaa 96,152,4
U 80,78,87
U 81,79,87
K 43,80,81
v_lshlrev_b32 v80, 16, v43
Zaaa 100,156,4
v_and_b32 v81, 0xffff0000, v43
v_lshlrev_b32 v82, 16, v55
v_and_b32 v83, 0xffff0000, v55
v_fmac_f32 v84, v80, v82
Maaa 100,60,0
W 4
v_fmac_f32 v84, v81, v83
v_lshrrev_b32 v82, 9, v1
s_lshl_b32 s80, s14, 1
v_add_u32 v82, s80, v82
v_mul_u32_u24 v83, 1552, v82
Zaaa 108,60,8
Ta 24,25,16384
Ta 26,25,17408
v_add_u32 v83, v83, v88
v_add_u32 v83, 131072, v83
ds_write_b128 v83, v[32:35] offset:0
ds_write_b128 v83, v[36:39] offset:512
Maaa 108,156,12
ds_write_b128 v83, v[40:43] offset:1024
v_add_f32_dpp v84, v84, v84 quad_perm:[1,0,3,2] row_mask:0xf bank_mask:0xf
s_nop 1
v_add_f32_dpp v84, v84, v84 quad_perm:[2,3,0,1] row_mask:0xf bank_mask:0xf
s_nop 1
v_add_f32_dpp v84, v84, v84 row_half_mirror row_mask:0xf bank_mask:0xf
s_nop 1
v_add_f32_dpp v84, v84, v84 row_mirror row_mask:0xf bank_mask:0xf
Zaaa 104,152,12
Ta 28,25,24576
Ta 30,25,25600
v_mov_b32 v81, v84
Maaa 104,56,8
Zaaa 236,52,8
v_permlane16_swap_b32 v84, v81
A 84,84,81
K 80,84,84
v_lshlrev_b32 v80, 16, v80
s_lshl_b32 s80, s15, 3
v_add_u32 v82, s80, v82
v_sub_u32 v83, 2047, v82
Maaa 236,148,12
v_ashrrev_i32 v81, 31, v80
v_or_b32 v81, 0x80000000, v81
v_xor_b32 v80, v80, v81
v_and_b32 v80, 0xffff0000, v80
v_or_b32 v80, v80, v83
v_and_b32 v82, 7, v82
Zaaa 232,144,12
v_lshlrev_b32 v82, 2, v82
v_add_u32 v82, 143488, v82
ds_write_b32 v82, v80
Maaa 232,48,8
Zaaa 172,44,8
Maaa 172,140,12
Zaaa 168,136,12
s_waitcnt vmcnt(1) lgkmcnt(0)
s_barrier
s_add_u32 s64, s64, 128
Maaa 168,40,8
v_bfe_u32 v80, v1, 4, 3
Zvaa 204,36,8
v_mul_u32_u24 v81, 1552, v80
Mvaa 204,132,12
s_and_b32 s64, s64, 1023
v_lshrrev_b32 v82, 7, v1
Zvaa 200,128,12
v_lshl_add_u32 v81, v82, 4, v81
Mvaa 200,32,8
s_lshl_b32 s80, s14, 7
Zvaa 208,32,16
s_lshl_b32 s65, s64, 12
s_add_u32 s80, s80, 131072
Mvaa 208,128,20
v_add_u32 v81, s80, v81
Zvaa 212,132,20
s_add_u32 m0, s62, 0
ds_read_b128 v[32:35], v81
Ta 0,10,49152
Mvaa 212,36,16
Ta 2,10,50176
Rv 36,81,512
Zaaa 176,40,16
s_and_b32 s80, s14, 2
Ta 4,10,57344
Ta 6,10,58368
Maaa 176,136,20
s_lshl_b32 s80, s80, 7
D 16,44,65,0
Zaaa 180,140,20
v_subrev_u32 v81, s80, v81
Maaa 180,44,16
Rv 40,81,1024
Zaaa 240,48,16
v_lshlrev_b32 v80, 2, v80
Maaa 240,144,20
D 17,44,65,1024
v_add_u32 v80, 143488, v80
Zaaa 244,148,20
ds_read_b32 v84, v80
Maaa 244,52,16
v_mov_b32 v85, s13
Zaaa 112,56,16
v_lshrrev_b32 v83, 1, v1
Maaa 112,152,20
s_mul_i32 s82, s15, 10240
D 18,44,65,2048
Zaaa 116,156,20
s_add_u32 s82, s82, 131072
Maaa 116,60,16
s_lshl_b32 s80, s14, 10
Zaaa 124,60,24
s_add_u32 s83, s82, s80
Ra 60,26,47104
Maaa 124,156,28
D 19,44,65,3072
Ra 156,27,47104
Zaaa 120,152,28
s_add_u32 m0, s62, 4096
Ra 152,27,45056
Ta 8,11,49152
Maaa 120,56,24
Ta 10,11,50176
Ra 56,26,45056
Zaaa 252,52,24
Ta 12,11,57344
Ta 14,11,58368
Maaa 252,148,28
W 8
buffer_store_dwordx4 v[32:35], v1, s[8:11], s83 offen sc1
s_add_u32 s83, s83, 4096
Zaaa 248,144,28
Ra 52,26,43008
Ra 148,27,43008
Maaa 248,48,24
Ra 144,27,40960
Ra 48,26,40960
Zaaa 188,44,24
Ra 44,26,38912
Maaa 188,140,28
D 20,44,65,0
Ra 140,27,38912
Zaaa 184,136,28
Ra 136,27,36864
Maaa 184,40,24
Ra 40,26,36864
Zvaa 220,36,24
Ra 36,26,34816
Mvaa 220,132,28
buffer_store_dwordx4 v[36:39], v1, s[8:11], s83 offen sc1
Ra 132,27,34816
Zvaa 216,128,28
s_and_b32 s80, s80, 1024
s_add_u32 s83, s82, s80
s_add_u32 s83, s83, 8192
Mvaa 216,32,24
W 12
Ra 128,27,32768
Ra 32,26,32768
Maaa 68,60,0
Maaa 68,156,4
D 21,44,65,1024
Maaa 64,152,4
Ta 16,12,49152
Ta 18,12,50176
Maaa 64,56,0
W 10
Ta 20,12,57344
Ta 22,12,58368
Maaa 196,52,0
Maaa 196,148,4
D 22,44,65,2048
Maaa 192,144,4
Maaa 192,48,0
W 8
Mvaa 228,44,0
Mvaa 228,140,4
buffer_store_dwordx4 v[40:43], v1, s[8:11], s83 offen sc1
Mvaa 224,136,4
Mvaa 224,40,0
W 4
Mvaa 164,36,0
Mvaa 164,132,4
D 23,44,65,3072
Mvaa 160,128,4
s_add_u32 m0, s61, 0
Mvaa 160,32,0
Mvaa 168,32,8
Mvaa 168,128,12
D 2,40,64,0
Mvaa 172,132,12
Ta 24,13,49152
Ta 26,13,50176
Mvaa 172,36,8
Ta 28,13,57344
Ta 30,13,58368
Mvaa 232,40,8
Mvaa 232,136,12
.Lh_0_1_1%=:
s_cbranch_execz .Lhr_9%=
D 3,40,64,1024
)"
R"(Mvaa 236,140,12
Mvaa 236,44,8
Maaa 200,48,8
Maaa 200,144,12
D 4,40,64,2048
Maaa 204,148,12
Maaa 204,52,8
Maaa 72,56,8
Maaa 72,152,12
D 5,40,64,3072
Maaa 76,156,12
s_add_u32 m0, s61, 4096
Maaa 76,60,8
W 4
Maaa 84,60,16
Maaa 84,156,20
D 6,40,64,0
Maaa 80,152,20
Ta 0,14,49152
Ta 2,14,50176
Maaa 80,56,16
Ta 4,14,57344
Ta 6,14,58368
Maaa 212,52,16
Maaa 212,148,20
D 7,40,64,1024
Maaa 208,144,20
Maaa 208,48,16
Mvaa 244,44,16
Mvaa 244,140,20
D 8,40,64,2048
Mvaa 240,136,20
Mvaa 240,40,16
Mvaa 180,36,16
Mvaa 180,132,20
D 9,40,64,3072
Mvaa 176,128,20
Mvaa 176,32,16
W 4
Mvaa 184,32,24
Mvaa 184,128,28
global_load_dword v142, v[140:141], off
v_lshl_add_u64 v[140:141], v[140:141], 0, s[68:69]
Mvaa 188,132,28
Ta 8,15,49152
Ta 10,15,50176
Mvaa 188,36,24
Ta 12,15,57344
Ta 14,15,58368
Mvaa 248,40,24
Mvaa 248,136,28
Mvaa 252,140,28
Mvaa 252,44,24
Maaa 216,48,24
Maaa 216,144,28
Maaa 220,148,28
Maaa 220,52,24
Maaa 88,56,24
Maaa 88,152,28
Maaa 92,156,28
Maaa 92,60,24
W 4
Maaa 100,60,0
Maaa 100,156,4
Maaa 96,152,4
Ta 16,24,49152
Ta 18,24,50176
Maaa 96,56,0
Ta 20,24,57344
Ta 22,24,58368
Maaa 228,52,0
Maaa 228,148,4
Maaa 224,144,4
Maaa 224,48,0
Maaa 164,44,0
Maaa 164,140,4
Maaa 160,136,4
Maaa 160,40,0
Mvaa 196,36,0
Mvaa 196,132,4
Mvaa 192,128,4
Mvaa 192,32,0
W 4
Mvaa 200,32,8
Ta 24,25,49152
Ta 26,25,50176
Mvaa 200,128,12
Mvaa 204,132,12
Ta 28,25,57344
Ta 30,25,58368
Mvaa 204,36,8
Maaa 168,40,8
Maaa 168,136,12
Maaa 172,140,12
Maaa 172,44,8
Maaa 232,48,8
Maaa 232,144,12
Maaa 236,148,12
s_waitcnt vmcnt(1) lgkmcnt(0)
s_barrier
s_cmp_eq_u32 s14, 0
s_cbranch_scc0 .Lpubr_19%=
s_lshl_b32 s80, s15, 6
s_add_u32 s80, s80, 40960
s_mov_b64 exec, 0xff
buffer_store_dwordx2 v[84:85], v83, s[8:11], s80 offen sc1
s_mov_b64 exec, -1
.Lpubr_19%=:
Maaa 236,52,8
s_add_u32 s64, s64, 128
Maaa 104,56,8
Maaa 104,152,12
s_and_b32 s64, s64, 1023
Maaa 108,156,12
Maaa 108,60,8
Maaa 116,60,16
s_lshl_b32 s65, s64, 12
Maaa 116,156,20
Maaa 112,152,20
Ta 0,10,16384
Ta 2,10,17408
Maaa 112,56,16
s_add_u32 m0, s62, 32768
Ta 4,10,24576
Ta 6,10,25600
Maaa 244,52,16
Maaa 244,148,20
Maaa 240,144,20
D 16,44,65,0
Maaa 240,48,16
Maaa 180,44,16
Maaa 180,140,20
Maaa 176,136,20
D 17,44,65,1024
Maaa 176,40,16
Mvaa 212,36,16
Mvaa 212,132,20
Mvaa 208,128,20
D 18,44,65,2048
Mvaa 208,32,16
Mvaa 216,32,24
Ra 32,26,0
Mvaa 216,128,28
Ra 128,27,0
Mvaa 220,132,28
D 19,44,65,3072
Ra 132,27,2048
Mvaa 220,36,24
s_add_u32 m0, s62, 36864
Ta 8,11,16384
Ta 10,11,17408
Maaa 184,40,24
Ra 36,26,2048
Ta 12,11,24576
Maaa 184,136,28
Ta 14,11,25600
Ra 40,26,4096
Maaa 188,140,28
D 20,44,65,0
Ra 136,27,4096
Maaa 188,44,24
Ra 140,27,6144
Ra 44,26,6144
Maaa 248,48,24
Ra 48,26,8192
Maaa 248,144,28
Ra 144,27,8192
Maaa 252,148,28
D 21,44,65,1024
Ra 148,27,10240
Maaa 252,52,24
Ra 52,26,10240
Maaa 120,56,24
Ra 56,26,12288
Maaa 120,152,28
Ra 152,27,12288
Maaa 124,156,28
D 22,44,65,2048
Ra 156,27,14336
Maaa 124,60,24
W 13
Ra 60,26,14336
Mvaa 160,32,0
Mvaa 160,128,4
Mvaa 164,132,4
D 23,44,65,3072
Ta 16,12,16384
Mvaa 164,36,0
W 9
s_add_u32 m0, s61, 32768
Ta 18,12,17408
Mvaa 224,40,0
Ta 20,12,24576
Ta 22,12,25600
Mvaa 224,136,4
Mvaa 228,140,4
D 2,40,64,0
Mvaa 228,44,0
W 8
Maaa 192,48,0
Maaa 192,144,4
Maaa 196,148,4
D 3,40,64,1024
Maaa 196,52,0
W 4
Maaa 64,56,0
Maaa 64,152,4
Maaa 68,156,4
D 4,40,64,2048
Maaa 68,60,0
Maaa 76,60,8
Maaa 76,156,12
Maaa 72,152,12
D 5,40,64,3072
Ta 24,13,16384
Maaa 72,56,8
s_add_u32 m0, s61, 36864
Ta 26,13,17408
Ta 28,13,24576
Maaa 204,52,8
Ta 30,13,25600
Maaa 204,148,12
Maaa 200,144,12
D 6,40,64,0
Maaa 200,48,8
Mvaa 236,44,8
Mvaa 236,140,12
Mvaa 232,136,12
D 7,40,64,1024
Mvaa 232,40,8
Mvaa 172,36,8
Mvaa 172,132,12
Mvaa 168,128,12
D 8,40,64,2048
Mvaa 168,32,8
W 4
Mvaa 176,32,16
Mvaa 176,128,20
Mvaa 180,132,20
D 9,40,64,3072
Ta 0,14,16384
Mvaa 180,36,16
Ta 2,14,17408
Ta 4,14,24576
Mvaa 240,40,16
Ta 6,14,25600
Mvaa 240,136,20
Mvaa 244,140,20
Mvaa 244,44,16
Maaa 208,48,16
Maaa 208,144,20
Maaa 212,148,20
Maaa 212,52,16
Maaa 80,56,16
Maaa 80,152,20
Maaa 84,156,20
Maaa 84,60,16
W 4
Maaa 92,60,24
Maaa 92,156,28
Maaa 88,152,28
Ta 8,15,16384
Ta 10,15,17408
Maaa 88,56,24
Ta 12,15,24576
Ta 14,15,25600
Maaa 220,52,24
Maaa 220,148,28
Maaa 216,144,28
Maaa 216,48,24
Mvaa 252,44,24
Mvaa 252,140,28
Mvaa 248,136,28
Mvaa 248,40,24
Mvaa 188,36,24
Mvaa 188,132,28
Mvaa 184,128,28
Mvaa 184,32,24
W 4
Mvaa 192,32,0
Mvaa 192,128,4
Mvaa 196,132,4
Ta 16,24,16384
Ta 18,24,17408
Mvaa 196,36,0
Ta 20,24,24576
Ta 22,24,25600
Maaa 160,40,0
Maaa 160,136,4
Maaa 164,140,4
Maaa 164,44,0
Maaa 224,48,0
Maaa 224,144,4
Maaa 228,148,4
Maaa 228,52,0
Maaa 96,56,0
Maaa 96,152,4
Maaa 100,156,4
Maaa 100,60,0
W 4
Maaa 108,60,8
Ta 24,25,16384
Ta 26,25,17408
Maaa 108,156,12
Maaa 104,152,12
Ta 28,25,24576
Ta 30,25,25600
Maaa 104,56,8
Maaa 236,52,8
Maaa 236,148,12
Maaa 232,144,12
Maaa 232,48,8
Maaa 172,44,8
Maaa 172,140,12
Maaa 168,136,12
s_waitcnt vmcnt(0) lgkmcnt(0)
s_barrier
s_add_u32 s64, s64, 128
Maaa 168,40,8
Mvaa 204,36,8
Mvaa 204,132,12
s_and_b32 s64, s64, 1023
Mvaa 200,128,12
Mvaa 200,32,8
Mvaa 208,32,16
s_lshl_b32 s65, s64, 12
Mvaa 208,128,20
Mvaa 212,132,20
s_add_u32 m0, s62, 0
Ta 0,10,49152
Ta 2,10,50176
Mvaa 212,36,16
Ta 4,10,57344
Ta 6,10,58368
Maaa 176,40,16
Maaa 176,136,20
D 16,44,65,0
Maaa 180,140,20
Maaa 180,44,16
Maaa 240,48,16
Maaa 240,144,20
D 17,44,65,1024
Maaa 244,148,20
Maaa 244,52,16
Maaa 112,56,16
Maaa 112,152,20
D 18,44,65,2048
Maaa 116,156,20
Maaa 116,60,16
Maaa 124,60,24
Ra 60,26,47104
Maaa 124,156,28
D 19,44,65,3072
Ra 156,27,47104
Maaa 120,152,28
s_add_u32 m0, s62, 4096
Ra 152,27,45056
Ta 8,11,49152
Maaa 120,56,24
Ta 10,11,50176
Ra 56,26,45056
Maaa 252,52,24
Ta 12,11,57344
Ta 14,11,58368
Maaa 252,148,28
D 20,44,65,0
Ra 52,26,43008
Maaa 248,144,28
Ra 148,27,43008
Ra 144,27,40960
Maaa 248,48,24
Ra 48,26,40960
Maaa 188,44,24
Ra 44,26,38912
Maaa 188,140,28
D 21,44,65,1024
Ra 140,27,38912
Maaa 184,136,28
Ra 136,27,36864
Maaa 184,40,24
Ra 40,26,36864
Mvaa 220,36,24
Ra 36,26,34816
Mvaa 220,132,28
D 22,44,65,2048
Ra 132,27,34816
Mvaa 216,128,28
Ra 128,27,32768
Mvaa 216,32,24
W 13
Ra 32,26,32768
Maaa 68,60,0
Maaa 68,156,4
D 23,44,65,3072
Maaa 64,152,4
s_add_u32 m0, s61, 0
Ta 16,12,49152
Ta 18,12,50176
Maaa 64,56,0
W 10
)"
R"(Ta 20,12,57344
Ta 22,12,58368
Maaa 196,52,0
Maaa 196,148,4
D 2,40,64,0
Maaa 192,144,4
Maaa 192,48,0
W 8
Mvaa 228,44,0
Mvaa 228,140,4
D 3,40,64,1024
Mvaa 224,136,4
Mvaa 224,40,0
W 4
Mvaa 164,36,0
Mvaa 164,132,4
D 4,40,64,2048
Mvaa 160,128,4
Mvaa 160,32,0
Mvaa 168,32,8
Mvaa 168,128,12
D 5,40,64,3072
Mvaa 172,132,12
s_add_u32 m0, s61, 4096
Ta 24,13,49152
Ta 26,13,50176
Mvaa 172,36,8
Ta 28,13,57344
Ta 30,13,58368
Mvaa 232,40,8
Mvaa 232,136,12
D 6,40,64,0
Mvaa 236,140,12
Mvaa 236,44,8
Maaa 200,48,8
Maaa 200,144,12
D 7,40,64,1024
Maaa 204,148,12
Maaa 204,52,8
Maaa 72,56,8
Maaa 72,152,12
D 8,40,64,2048
Maaa 76,156,12
Maaa 76,60,8
W 4
Maaa 84,60,16
Maaa 84,156,20
D 9,40,64,3072
Maaa 80,152,20
Ta 0,14,49152
Ta 2,14,50176
Maaa 80,56,16
Ta 4,14,57344
Ta 6,14,58368
Maaa 212,52,16
Maaa 212,148,20
global_load_dword v142, v[140:141], off
v_lshl_add_u64 v[140:141], v[140:141], 0, s[68:69]
v_bfe_u32 v147, v1, 5, 3
Maaa 208,144,20
v_lshlrev_b32 v147, 13, v147
v_and_b32 v148, 16, v1
Maaa 208,48,16
v_lshl_add_u32 v147, v148, 3, v147
Mvaa 244,44,16
s_lshl_b32 s92, s57, 2
s_add_u32 s92, s92, s14
s_lshl_b32 s92, s92, 16
Mvaa 244,140,20
buffer_load_dword v142, v147, s[48:51], s92 offen
Mvaa 240,136,20
Mvaa 240,40,16
Mvaa 180,36,16
Mvaa 180,132,20
Mvaa 176,128,20
Mvaa 176,32,16
W 4
Mvaa 184,32,24
Mvaa 184,128,28
Mvaa 188,132,28
Ta 8,15,49152
Ta 10,15,50176
Mvaa 188,36,24
Ta 12,15,57344
Ta 14,15,58368
Mvaa 248,40,24
Mvaa 248,136,28
Mvaa 252,140,28
Mvaa 252,44,24
Maaa 216,48,24
Maaa 216,144,28
Maaa 220,148,28
Maaa 220,52,24
Maaa 88,56,24
Maaa 88,152,28
Maaa 92,156,28
Maaa 92,60,24
W 4
Maaa 100,60,0
Maaa 100,156,4
Maaa 96,152,4
Ta 16,24,49152
Ta 18,24,50176
Maaa 96,56,0
Ta 20,24,57344
Ta 22,24,58368
Maaa 228,52,0
Maaa 228,148,4
Maaa 224,144,4
Maaa 224,48,0
Maaa 164,44,0
Maaa 164,140,4
Maaa 160,136,4
Maaa 160,40,0
Mvaa 196,36,0
Mvaa 196,132,4
Mvaa 192,128,4
Mvaa 192,32,0
W 4
Mvaa 200,32,8
Ta 24,25,49152
Ta 26,25,50176
Mvaa 200,128,12
Mvaa 204,132,12
Ta 28,25,57344
Ta 30,25,58368
Mvaa 204,36,8
Maaa 168,40,8
Maaa 168,136,12
Maaa 172,140,12
.Lh_0_2_1%=:
s_cbranch_execz .Lhr_9%=
Maaa 172,44,8
Maaa 232,48,8
Maaa 232,144,12
Maaa 236,148,12
s_waitcnt vmcnt(2) lgkmcnt(0)
s_barrier
s_add_u32 s64, s64, 128
Maaa 236,52,8
Maaa 104,56,8
Maaa 104,152,12
s_and_b32 s64, s64, 1023
Maaa 108,156,12
Maaa 108,60,8
Maaa 116,60,16
s_lshl_b32 s65, s64, 12
Maaa 116,156,20
Maaa 112,152,20
Ta 0,10,16384
Ta 2,10,17408
Maaa 112,56,16
s_add_u32 m0, s62, 32768
Ta 4,10,24576
Ta 6,10,25600
Maaa 244,52,16
Maaa 244,148,20
Maaa 240,144,20
D 16,44,65,0
Maaa 240,48,16
Maaa 180,44,16
Maaa 180,140,20
Maaa 176,136,20
D 17,44,65,1024
Maaa 176,40,16
Mvaa 212,36,16
Mvaa 212,132,20
Mvaa 208,128,20
D 18,44,65,2048
Mvaa 208,32,16
Mvaa 216,32,24
Ra 32,26,0
Mvaa 216,128,28
Ra 128,27,0
Mvaa 220,132,28
D 19,44,65,3072
Ra 132,27,2048
Mvaa 220,36,24
s_add_u32 m0, s62, 36864
Ta 8,11,16384
Ta 10,11,17408
Maaa 184,40,24
Ra 36,26,2048
Ta 12,11,24576
Maaa 184,136,28
Ta 14,11,25600
Ra 40,26,4096
Maaa 188,140,28
D 20,44,65,0
Ra 136,27,4096
Maaa 188,44,24
Ra 140,27,6144
Ra 44,26,6144
Maaa 248,48,24
Ra 48,26,8192
Maaa 248,144,28
Ra 144,27,8192
Maaa 252,148,28
D 21,44,65,1024
Ra 148,27,10240
Maaa 252,52,24
Ra 52,26,10240
Maaa 120,56,24
Ra 56,26,12288
Maaa 120,152,28
Ra 152,27,12288
Maaa 124,156,28
D 22,44,65,2048
Ra 156,27,14336
Maaa 124,60,24
W 13
Ra 60,26,14336
Mvaa 160,32,0
Mvaa 160,128,4
Mvaa 164,132,4
D 23,44,65,3072
Ta 16,12,16384
Mvaa 164,36,0
W 9
s_add_u32 m0, s61, 32768
Ta 18,12,17408
Mvaa 224,40,0
Ta 20,12,24576
Ta 22,12,25600
Mvaa 224,136,4
Mvaa 228,140,4
D 2,40,64,0
Mvaa 228,44,0
W 8
Maaa 192,48,0
Maaa 192,144,4
Maaa 196,148,4
D 3,40,64,1024
Maaa 196,52,0
W 4
Maaa 64,56,0
Maaa 64,152,4
Maaa 68,156,4
D 4,40,64,2048
Maaa 68,60,0
Maaa 76,60,8
Maaa 76,156,12
Maaa 72,152,12
D 5,40,64,3072
Ta 24,13,16384
Maaa 72,56,8
s_add_u32 m0, s61, 36864
Ta 26,13,17408
Ta 28,13,24576
Maaa 204,52,8
Ta 30,13,25600
Maaa 204,148,12
Maaa 200,144,12
D 6,40,64,0
Maaa 200,48,8
Mvaa 236,44,8
Mvaa 236,140,12
Mvaa 232,136,12
D 7,40,64,1024
Mvaa 232,40,8
Mvaa 172,36,8
Mvaa 172,132,12
Mvaa 168,128,12
D 8,40,64,2048
Mvaa 168,32,8
W 4
Mvaa 176,32,16
Mvaa 176,128,20
Mvaa 180,132,20
D 9,40,64,3072
Ta 0,14,16384
Mvaa 180,36,16
Ta 2,14,17408
Ta 4,14,24576
Mvaa 240,40,16
Ta 6,14,25600
Mvaa 240,136,20
Mvaa 244,140,20
Mvaa 244,44,16
Maaa 208,48,16
Maaa 208,144,20
Maaa 212,148,20
Maaa 212,52,16
Maaa 80,56,16
Maaa 80,152,20
Maaa 84,156,20
Maaa 84,60,16
W 4
Maaa 92,60,24
Maaa 92,156,28
Maaa 88,152,28
Ta 8,15,16384
Ta 10,15,17408
Maaa 88,56,24
Ta 12,15,24576
Ta 14,15,25600
Maaa 220,52,24
Maaa 220,148,28
Maaa 216,144,28
Maaa 216,48,24
Mvaa 252,44,24
Mvaa 252,140,28
Mvaa 248,136,28
Mvaa 248,40,24
Mvaa 188,36,24
Mvaa 188,132,28
Mvaa 184,128,28
Mvaa 184,32,24
W 4
Mvaa 192,32,0
Mvaa 192,128,4
Mvaa 196,132,4
Ta 16,24,16384
Ta 18,24,17408
Mvaa 196,36,0
Ta 20,24,24576
Ta 22,24,25600
Maaa 160,40,0
Maaa 160,136,4
Maaa 164,140,4
Maaa 164,44,0
Maaa 224,48,0
Maaa 224,144,4
Maaa 228,148,4
Maaa 228,52,0
Maaa 96,56,0
Maaa 96,152,4
Maaa 100,156,4
Maaa 100,60,0
W 4
Maaa 108,60,8
Ta 24,25,16384
Ta 26,25,17408
Maaa 108,156,12
Maaa 104,152,12
Ta 28,25,24576
Ta 30,25,25600
Maaa 104,56,8
Maaa 236,52,8
Maaa 236,148,12
Maaa 232,144,12
Maaa 232,48,8
Maaa 172,44,8
Maaa 172,140,12
Maaa 168,136,12
s_waitcnt vmcnt(0) lgkmcnt(0)
s_barrier
s_add_u32 s64, s64, 128
Maaa 168,40,8
Mvaa 204,36,8
Mvaa 204,132,12
s_and_b32 s64, s64, 1023
Mvaa 200,128,12
Mvaa 200,32,8
Mvaa 208,32,16
s_lshl_b32 s65, s64, 12
Mvaa 208,128,20
Mvaa 212,132,20
Ta 0,10,49152
Ta 2,10,50176
Mvaa 212,36,16
s_add_u32 m0, s62, 0
Ta 4,10,57344
Ta 6,10,58368
Maaa 176,40,16
Maaa 176,136,20
Maaa 180,140,20
D 16,44,65,0
Maaa 180,44,16
Maaa 240,48,16
Maaa 240,144,20
Maaa 244,148,20
D 17,44,65,1024
v_lshrrev_b32 v28, 1, v1
Maaa 244,52,16
s_lshl_b32 s80, s60, 9
Maaa 112,56,16
s_add_u32 s80, s80, 40960
Maaa 112,152,20
Maaa 116,156,20
D 18,44,65,2048
Maaa 116,60,16
Maaa 124,60,24
Ra 60,26,47104
Maaa 124,156,28
Ra 156,27,47104
Maaa 120,152,28
buffer_load_dwordx2 v[28:29], v28, s[8:11], s80 offen sc1
Ra 152,27,45056
Maaa 120,56,24
Ta 8,11,49152
Ta 10,11,50176
Maaa 252,52,24
Ra 56,26,45056
Ta 12,11,57344
Maaa 252,148,28
Ta 14,11,58368
Ra 52,26,43008
Maaa 248,144,28
D 19,44,65,3072
)"
R"(Ra 148,27,43008
Maaa 248,48,24
s_add_u32 m0, s62, 4096
Ra 144,27,40960
Ra 48,26,40960
Maaa 188,44,24
Ra 44,26,38912
Maaa 188,140,28
Ra 140,27,38912
Maaa 184,136,28
D 20,44,65,0
Ra 136,27,36864
Maaa 184,40,24
Ra 40,26,36864
Mvaa 220,36,24
Ra 36,26,34816
Mvaa 220,132,28
Ra 132,27,34816
Mvaa 216,128,28
D 21,44,65,1024
Ra 128,27,32768
Mvaa 216,32,24
W 13
Ra 32,26,32768
Maaa 68,60,0
Maaa 68,156,4
Maaa 64,152,4
D 22,44,65,2048
Ta 16,12,49152
Maaa 64,56,0
W 9
Ta 18,12,50176
Ta 20,12,57344
Maaa 196,52,0
Ta 22,12,58368
Maaa 196,148,4
Maaa 192,144,4
D 23,44,65,3072
Maaa 192,48,0
W 8
s_add_u32 m0, s61, 0
Mvaa 228,44,0
Mvaa 228,140,4
Mvaa 224,136,4
D 2,40,64,0
Mvaa 224,40,0
W 4
Mvaa 164,36,0
Mvaa 164,132,4
Mvaa 160,128,4
D 3,40,64,1024
Mvaa 160,32,0
Mvaa 168,32,8
Mvaa 168,128,12
Mvaa 172,132,12
D 4,40,64,2048
Ta 24,13,49152
Mvaa 172,36,8
Ta 26,13,50176
Ta 28,13,57344
Mvaa 232,40,8
Ta 30,13,58368
Mvaa 232,136,12
Mvaa 236,140,12
D 5,40,64,3072
Mvaa 236,44,8
s_add_u32 m0, s61, 4096
Maaa 200,48,8
Maaa 200,144,12
Maaa 204,148,12
D 6,40,64,0
Maaa 204,52,8
Maaa 72,56,8
Maaa 72,152,12
Maaa 76,156,12
D 7,40,64,1024
Maaa 76,60,8
W 4
Maaa 84,60,16
s_waitcnt vmcnt(11)
v_cmp_eq_u32 vcc, s13, v29
s_cmp_eq_u64 vcc, -1
s_cbranch_scc0 .Lspin_20%=
.Lok_21%=:
Maaa 84,156,20
Maaa 80,152,20
D 8,40,64,2048
Ta 0,14,49152
Maaa 80,56,16
Ta 2,14,50176
v_bfe_u32 v144, v1, 8, 1
Maaa 212,52,16
Ta 4,14,57344
v_mul_u32_u24 v144, 10240, v144
Maaa 212,148,20
Ta 6,14,58368
v_and_b32 v145, 0xf0, v1
Maaa 208,144,20
D 9,40,64,3072
v_lshl_add_u32 v144, v145, 3, v144
Maaa 208,48,16
s_mul_i32 s91, s58, 20480
s_add_u32 s91, s91, s28
Mvaa 244,44,16
Mvaa 244,140,20
Mvaa 240,136,20
global_load_dword v142, v146, s[6:7]
Mvaa 240,40,16
s_add_u32 s92, s92, 2097152
Mvaa 180,36,16
Mvaa 180,132,20
Mvaa 176,128,20
buffer_load_dword v142, v147, s[48:51], s92 offen
Mvaa 176,32,16
W 4
s_add_u32 s92, s92, 2097152
Mvaa 184,32,24
Mvaa 184,128,28
Mvaa 188,132,28
buffer_load_dword v142, v147, s[48:51], s92 offen
Ta 8,15,49152
Mvaa 188,36,24
Ta 10,15,50176
Ta 12,15,57344
Mvaa 248,40,24
Ta 14,15,58368
Mvaa 248,136,28
Mvaa 252,140,28
buffer_load_dword v142, v144, s[8:11], s91 offen offset:0 sc1
Mvaa 252,44,24
Maaa 216,48,24
Maaa 216,144,28
Maaa 220,148,28
Maaa 220,52,24
Maaa 88,56,24
Maaa 88,152,28
Maaa 92,156,28
Maaa 92,60,24
W 4
Maaa 100,60,0
Maaa 100,156,4
Maaa 96,152,4
Ta 16,24,49152
Ta 18,24,50176
Maaa 96,56,0
Ta 20,24,57344
Ta 22,24,58368
Maaa 228,52,0
Maaa 228,148,4
Maaa 224,144,4
Maaa 224,48,0
Maaa 164,44,0
Maaa 164,140,4
Maaa 160,136,4
Maaa 160,40,0
Mvaa 196,36,0
Mvaa 196,132,4
Mvaa 192,128,4
Mvaa 192,32,0
W 4
Mvaa 200,32,8
Ta 24,25,49152
Ta 26,25,50176
Mvaa 200,128,12
Mvaa 204,132,12
Ta 28,25,57344
Ta 30,25,58368
Mvaa 204,36,8
Maaa 168,40,8
Maaa 168,136,12
Maaa 172,140,12
Maaa 172,44,8
Maaa 232,48,8
Maaa 232,144,12
Maaa 236,148,12
s_waitcnt vmcnt(4) lgkmcnt(0)
s_barrier
s_cmp_eq_u32 s85, 107
s_cbranch_scc1 .Lrt_23%=
.Lrtr_24%=:
Maaa 236,52,8
s_add_u32 s64, s64, 128
Maaa 104,56,8
Maaa 104,152,12
s_and_b32 s64, s64, 1023
Maaa 108,156,12
Maaa 108,60,8
Maaa 116,60,16
s_lshl_b32 s65, s64, 12
Maaa 116,156,20
Maaa 112,152,20
Ta 0,10,16384
Ta 2,10,17408
Maaa 112,56,16
s_add_u32 m0, s62, 32768
Ta 4,10,24576
Ta 6,10,25600
Maaa 244,52,16
Maaa 244,148,20
Maaa 240,144,20
D 16,44,65,0
Maaa 240,48,16
Maaa 180,44,16
Maaa 180,140,20
Maaa 176,136,20
D 17,44,65,1024
Maaa 176,40,16
Mvaa 212,36,16
Mvaa 212,132,20
Mvaa 208,128,20
D 18,44,65,2048
Mvaa 208,32,16
Mvaa 216,32,24
Ra 32,26,0
Mvaa 216,128,28
Ra 128,27,0
Mvaa 220,132,28
D 19,44,65,3072
Ra 132,27,2048
Mvaa 220,36,24
s_add_u32 m0, s62, 36864
Ta 8,11,16384
Ta 10,11,17408
Maaa 184,40,24
Ra 36,26,2048
Ta 12,11,24576
Maaa 184,136,28
Ta 14,11,25600
Ra 40,26,4096
Maaa 188,140,28
D 20,44,65,0
Ra 136,27,4096
Maaa 188,44,24
Ra 140,27,6144
Ra 44,26,6144
Maaa 248,48,24
Ra 48,26,8192
Maaa 248,144,28
Ra 144,27,8192
Maaa 252,148,28
D 21,44,65,1024
Ra 148,27,10240
Maaa 252,52,24
Ra 52,26,10240
Maaa 120,56,24
Ra 56,26,12288
Maaa 120,152,28
Ra 152,27,12288
Maaa 124,156,28
D 22,44,65,2048
Ra 156,27,14336
Maaa 124,60,24
W 13
Ra 60,26,14336
Mvaa 160,32,0
Mvaa 160,128,4
Mvaa 164,132,4
D 23,44,65,3072
Ta 16,12,16384
Mvaa 164,36,0
W 9
s_add_u32 m0, s61, 32768
Ta 18,12,17408
Mvaa 224,40,0
.Lh_0_3_1%=:
s_cbranch_execz .Lhr_9%=
Ta 20,12,24576
Ta 22,12,25600
Mvaa 224,136,4
Mvaa 228,140,4
D 2,40,64,0
Mvaa 228,44,0
W 8
Maaa 192,48,0
Maaa 192,144,4
Maaa 196,148,4
D 3,40,64,1024
Maaa 196,52,0
W 4
Maaa 64,56,0
Maaa 64,152,4
Maaa 68,156,4
D 4,40,64,2048
Maaa 68,60,0
Maaa 76,60,8
Maaa 76,156,12
Maaa 72,152,12
D 5,40,64,3072
Ta 24,13,16384
Maaa 72,56,8
s_add_u32 m0, s61, 36864
Ta 26,13,17408
Ta 28,13,24576
Maaa 204,52,8
Ta 30,13,25600
Maaa 204,148,12
Maaa 200,144,12
D 6,40,64,0
Maaa 200,48,8
Mvaa 236,44,8
Mvaa 236,140,12
Mvaa 232,136,12
D 7,40,64,1024
Mvaa 232,40,8
Mvaa 172,36,8
Mvaa 172,132,12
Mvaa 168,128,12
D 8,40,64,2048
Mvaa 168,32,8
W 4
Mvaa 176,32,16
Mvaa 176,128,20
Mvaa 180,132,20
D 9,40,64,3072
v_add_u32 v2, 131072, v10
Mvaa 180,36,16
Ta 0,14,16384
v_add_u32 v3, 131072, v11
Mvaa 240,40,16
Ta 2,14,17408
v_add_u32 v4, 131072, v12
Mvaa 240,136,20
Ta 4,14,24576
Ta 6,14,25600
Mvaa 244,140,20
buffer_load_dword v142, v144, s[8:11], s91 offen offset:2048 sc1
v_add_u32 v5, 131072, v13
Mvaa 244,44,16
s_add_u32 s91, s91, 4096
v_add_u32 v6, 131072, v14
Maaa 208,48,16
v_add_u32 v7, 131072, v15
Maaa 208,144,20
Maaa 212,148,20
v_add_u32 v8, 131072, v24
Maaa 212,52,16
Maaa 80,56,16
v_add_u32 v9, 131072, v25
Maaa 80,152,20
s_add_u32 s65, s5, 0
s_cmp_ge_u32 s65, 20
s_cselect_b32 s90, 20, 0
s_sub_u32 s65, s65, s90
Maaa 84,156,20
s_lshl_b32 s65, s65, 18
s_add_u32 m0, s63, 131072
Maaa 84,60,16
W 4
Maaa 92,60,24
Maaa 92,156,28
Maaa 88,152,28
Ta 8,15,16384
Ta 10,15,17408
Maaa 88,56,24
D 16,48,65,0
Ta 12,15,24576
Maaa 220,52,24
Ta 14,15,25600
Maaa 220,148,28
Maaa 216,144,28
Maaa 216,48,24
D 17,48,65,1024
Mvaa 252,44,24
Mvaa 252,140,28
Mvaa 248,136,28
Mvaa 248,40,24
Mvaa 188,36,24
D 18,48,65,2048
Mvaa 188,132,28
Mvaa 184,128,28
Mvaa 184,32,24
W 4
Mvaa 192,32,0
Mvaa 192,128,4
D 19,48,65,3072
Mvaa 196,132,4
s_add_u32 m0, s63, 135168
Ta 16,24,16384
Ta 18,24,17408
Mvaa 196,36,0
Ta 20,24,24576
Ta 22,24,25600
Maaa 160,40,0
Maaa 160,136,4
Maaa 164,140,4
D 20,48,65,0
Maaa 164,44,0
Maaa 224,48,0
Maaa 224,144,4
Maaa 228,148,4
Maaa 228,52,0
D 21,48,65,1024
Maaa 96,56,0
Maaa 96,152,4
Maaa 100,156,4
Maaa 100,60,0
W 4
Maaa 108,60,8
D 22,48,65,2048
Ta 24,25,16384
)"
R"(Maaa 108,156,12
Ta 26,25,17408
Maaa 104,152,12
Ta 28,25,24576
Ta 30,25,25600
Maaa 104,56,8
Maaa 236,52,8
D 23,48,65,3072
Maaa 236,148,12
Maaa 232,144,12
Maaa 232,48,8
Maaa 172,44,8
Maaa 172,140,12
Maaa 168,136,12
s_waitcnt vmcnt(9) lgkmcnt(0)
s_barrier
s_cmp_eq_u32 s85, 107
s_cbranch_scc1 .Lrt_25%=
.Lrtr_26%=:
Maaa 168,40,8
s_add_u32 s65, s5, 4
Mvaa 204,36,8
Mvaa 204,132,12
s_cmp_ge_u32 s65, 20
s_cselect_b32 s90, 20, 0
Mvaa 200,128,12
Mvaa 200,32,8
s_sub_u32 s65, s65, s90
Mvaa 208,32,16
Mvaa 208,128,20
s_lshl_b32 s65, s65, 18
Mvaa 212,132,20
Ta 0,10,49152
Ta 2,10,50176
Mvaa 212,36,16
Maaa 176,40,16
s_add_u32 m0, s63, 0
Maaa 176,136,20
Maaa 180,140,20
D 16,48,65,0
Maaa 180,44,16
Maaa 240,48,16
Maaa 240,144,20
Maaa 244,148,20
D 17,48,65,1024
Ta 4,11,49152
Maaa 244,52,16
Ta 6,11,50176
Maaa 112,56,16
Maaa 112,152,20
Maaa 116,156,20
D 18,48,65,2048
Maaa 116,60,16
Maaa 124,60,24
Ra 60,26,47104
Maaa 124,156,28
Maaa 120,152,28
D 19,48,65,3072
Ta 8,12,49152
Maaa 120,56,24
s_add_u32 m0, s63, 4096
Ta 10,12,50176
Ra 56,26,45056
Maaa 252,52,24
Ra 52,26,43008
Maaa 252,148,28
Maaa 248,144,28
D 20,48,65,0
Maaa 248,48,24
Ra 48,26,40960
Maaa 188,44,24
Ra 44,26,38912
Maaa 188,140,28
Maaa 184,136,28
D 21,48,65,1024
Ta 12,13,49152
Maaa 184,40,24
Ta 14,13,50176
Ra 40,26,36864
Mvaa 220,36,24
Ra 36,26,34816
Mvaa 220,132,28
Mvaa 216,128,28
D 22,48,65,2048
Mvaa 216,32,24
Ra 32,26,32768
W 6
Maaa 68,60,0
Maaa 64,56,0
Maaa 196,52,0
D 23,48,65,3072
Ta 16,14,49152
Maaa 192,48,0
W 1
s_add_u32 s65, s5, 8
s_cmp_ge_u32 s65, 20
s_cselect_b32 s90, 20, 0
Mvaa 228,44,0
s_sub_u32 s65, s65, s90
s_lshl_b32 s65, s65, 18
Ta 18,14,50176
Mvaa 224,40,0
s_add_u32 m0, s63, 65536
Mvaa 164,36,0
D 16,48,65,0
Mvaa 160,32,0
Mvaa 168,32,4
Mvaa 172,36,4
Mvaa 232,40,4
D 17,48,65,1024
Ta 20,15,49152
Mvaa 236,44,4
Ta 22,15,50176
Maaa 200,48,4
Maaa 204,52,4
Maaa 72,56,4
D 18,48,65,2048
Maaa 76,60,4
Maaa 84,60,8
Maaa 80,56,8
Maaa 212,52,8
D 19,48,65,3072
Ta 24,24,49152
Maaa 208,48,8
s_add_u32 m0, s63, 69632
Ta 26,24,50176
Mvaa 244,44,8
Mvaa 240,40,8
Mvaa 180,36,8
D 20,48,65,0
Mvaa 176,32,8
Mvaa 184,32,12
Mvaa 188,36,12
Mvaa 248,40,12
D 21,48,65,1024
Ta 28,25,49152
Mvaa 252,44,12
Ta 30,25,50176
Maaa 216,48,12
Ra 156,27,47104
Maaa 220,52,12
Maaa 88,56,12
D 22,48,65,2048
Maaa 92,60,12
W 7
Ra 152,27,45056
Maaa 100,60,16
Maaa 96,56,16
Maaa 228,52,16
D 23,48,65,3072
Ra 148,27,43008
Maaa 224,48,16
Ta 0,10,57344
Ta 2,10,58368
Maaa 164,44,16
Maaa 160,40,16
Ra 144,27,40960
Mvaa 196,36,16
buffer_load_dword v142, v144, s[8:11], s91 offen offset:0 sc1
Mvaa 192,32,16
W 10
Mvaa 200,32,20
Ra 140,27,38912
Mvaa 204,36,20
Maaa 168,40,20
Ta 4,11,57344
Ta 6,11,58368
Maaa 172,44,20
Ra 136,27,36864
Maaa 232,48,20
Maaa 236,52,20
v_bfe_u32 v29, v1, 7, 1
Maaa 104,56,20
Ra 132,27,34816
Maaa 108,60,20
W 13
v_mul_u32_u24 v28, 10240, v29
Maaa 116,60,24
Maaa 112,56,24
Ra 128,27,32768
v_lshrrev_b32 v29, 8, v1
Maaa 244,52,24
Ta 8,12,57344
Ta 10,12,58368
Maaa 240,48,24
v_lshl_add_u32 v28, v29, 7, v28
Maaa 180,44,24
v_and_b32 v29, 0x70, v1
Maaa 176,40,24
Mvaa 212,36,24
v_add_u32 v28, v28, v29
Mvaa 208,32,24
W 14
Mvaa 216,32,28
s_and_b32 s80, s14, 2
Mvaa 220,36,28
s_mul_i32 s80, s80, 20480
Maaa 184,40,28
Ta 12,13,57344
Ta 14,13,58368
Maaa 188,44,28
s_sub_u32 s81, 40960, s80
Maaa 248,48,28
Maaa 252,52,28
s_add_u32 s93, s28, 20480
Maaa 120,56,28
Maaa 124,60,28
W 10
v_add_u32 v26, s80, v28
Maaa 68,156,0
Maaa 64,152,0
Maaa 196,148,0
Ta 16,14,57344
v_add_u32 v27, s81, v28
Maaa 192,144,0
W 5
Ta 18,14,58368
Mvaa 228,140,0
buffer_load_dwordx4 a[32:35], v26, s[8:11], s28 offen offset:0 sc1
Mvaa 224,136,0
Mvaa 164,132,0
Mvaa 160,128,0
Mvaa 168,128,4
buffer_load_dwordx4 a[36:39], v26, s[8:11], s93 offen offset:0 sc1
Mvaa 172,132,4
Mvaa 232,136,4
Ta 20,15,57344
Ta 22,15,58368
Mvaa 236,140,4
Maaa 200,144,4
buffer_load_dwordx4 a[40:43], v27, s[8:11], s28 offen offset:0 sc1
Maaa 204,148,4
Maaa 72,152,4
Maaa 76,156,4
W 6
Maaa 84,156,8
buffer_load_dwordx4 a[44:47], v27, s[8:11], s93 offen offset:0 sc1
Ta 24,24,57344
Maaa 80,152,8
s_add_u32 s28, s28, 512
s_cmp_eq_u32 s28, s4
s_cselect_b32 s93, 10240, 0
Ta 26,24,58368
Maaa 212,148,8
s_sub_u32 s28, s28, s93
Ta 28,25,57344
Ta 30,25,58368
Maaa 208,144,8
s_add_u32 s93, s28, 20480
Mvaa 244,140,8
Mvaa 240,136,8
Mvaa 180,132,8
s_add_u32 s65, s5, 12
s_cmp_ge_u32 s65, 20
s_cselect_b32 s90, 20, 0
buffer_load_dwordx4 a[48:51], v26, s[8:11], s28 offen offset:0 sc1
Mvaa 176,128,8
W 8
s_sub_u32 s65, s65, s90
s_lshl_b32 s65, s65, 18
Mvaa 184,128,12
s_add_u32 m0, s63, 32768
Mvaa 188,132,12
Mvaa 248,136,12
s_waitcnt vmcnt(6) lgkmcnt(0)
s_barrier
buffer_load_dwordx4 a[52:55], v26, s[8:11], s93 offen offset:0 sc1
Mvaa 252,140,12
Maaa 216,144,12
Maaa 220,148,12
Maaa 88,152,12
buffer_load_dwordx4 a[56:59], v27, s[8:11], s28 offen offset:0 sc1
Maaa 92,156,12
Maaa 100,156,16
Maaa 96,152,16
Maaa 228,148,16
buffer_load_dwordx4 a[60:63], v27, s[8:11], s93 offen offset:0 sc1
Maaa 224,144,16
Maaa 164,140,16
Maaa 160,136,16
Mvaa 196,132,16
D 16,48,65,0
Mvaa 192,128,16
Mvaa 200,128,20
Mvaa 204,132,20
Maaa 168,136,20
D 17,48,65,1024
Ta 0,2,0
Maaa 172,140,20
Ta 2,2,1024
Maaa 232,144,20
Maaa 236,148,20
Maaa 104,152,20
D 18,48,65,2048
Ta 4,2,8192
Maaa 108,156,20
Ta 6,2,9216
Maaa 116,156,24
Maaa 112,152,24
Maaa 244,148,24
Ta 8,3,0
Ta 10,3,1024
Maaa 240,144,24
D 19,48,65,3072
Maaa 180,140,24
s_add_u32 m0, s63, 36864
Maaa 176,136,24
Mvaa 212,132,24
Ta 12,3,8192
Ta 14,3,9216
Mvaa 208,128,24
D 20,48,65,0
Mvaa 216,128,28
Mvaa 220,132,28
Maaa 184,136,28
Ta 16,4,0
Ta 18,4,1024
Maaa 188,140,28
Maaa 248,144,28
D 21,48,65,1024
Maaa 252,148,28
Maaa 120,152,28
Maaa 124,156,28
.Lq_11%=:
s_cmp_ge_u32 s14, 2
s_cbranch_scc1 .Lpb_31%=
s_waitcnt vmcnt(6) lgkmcnt(6)
Zvaa 32,0,32
s_add_u32 s28, s28, 512
Ta 20,4,8192
Ta 22,4,9216
Mvaa 32,4,48
s_cmp_eq_u32 s28, s4
s_cselect_b32 s93, 10240, 0
Ta 24,5,0
Ta 26,5,1024
Zvaa 64,4,52
s_sub_u32 s28, s28, s93
s_add_u32 s93, s28, 20480
buffer_load_dwordx4 a[128:131], v26, s[8:11], s28 offen offset:0 sc1
Mvaa 64,0,36
Ta 28,5,8192
Ta 30,5,9216
Zvaa 96,0,40
K 184,184,185
K 185,186,187
Mvaa 96,4,56
K 186,188,189
K 187,190,191
Zvaa 128,4,60
K 192,192,193
Mvaa 128,0,44
W 8
buffer_load_dwordx4 a[132:135], v26, s[8:11], s93 offen offset:0 sc1
K 193,194,195
Zvaa 132,8,44
Ta 0,6,0
Ta 2,6,1024
Mvaa 132,12,60
K 194,196,197
K 195,198,199
Zvaa 100,12,56
K 200,200,201
K 201,202,203
Mvaa 100,8,40
Ta 4,6,8192
Ta 6,6,9216
Zvaa 68,8,36
buffer_load_dwordx4 a[136:139], v27, s[8:11], s28 offen offset:0 sc1
K 202,204,205
Mvaa 68,12,52
K 203,206,207
K 208,208,209
Zvaa 36,12,48
K 209,210,211
K 210,212,213
Mvaa 36,8,32
W 8
Ta 8,7,0
K 211,214,215
Zvaa 40,16,32
Ta 10,7,1024
K 216,216,217
Mvaa 40,20,48
buffer_load_dwordx4 a[140:143], v27, s[8:11], s93 offen offset:0 sc1
K 217,218,219
Zvaa 72,20,52
s_add_u32 s28, s28, 512
s_cmp_eq_u32 s28, s4
s_cselect_b32 s93, 10240, 0
s_sub_u32 s28, s28, s93
Mvaa 72,16,36
s_add_u32 s93, s28, 20480
K 218,220,221
K 219,222,223
Zvaa 104,16,40
Ta 12,7,8192
Ta 14,7,9216
Mvaa 104,20,56
K 224,224,225
)"
R"(K 225,226,227
Zvaa 136,20,60
K 226,228,229
K 227,230,231
Mvaa 136,16,44
W 8
buffer_load_dwordx4 a[144:147], v26, s[8:11], s28 offen offset:0 sc1
K 232,232,233
Zvaa 140,24,44
K 233,234,235
K 234,236,237
Mvaa 140,28,60
Ta 16,8,0
K 235,238,239
Zvaa 108,28,56
Ta 18,8,1024
K 240,240,241
Mvaa 108,24,40
K 241,242,243
K 242,244,245
Zvaa 76,24,36
buffer_load_dwordx4 a[148:151], v26, s[8:11], s93 offen offset:0 sc1
K 243,246,247
Mvaa 76,28,52
Ta 20,8,8192
Ta 22,8,9216
Zvaa 44,28,48
K 248,248,249
K 249,250,251
Mvaa 44,24,32
K 250,252,253
K 251,254,255
W 8
Zvaa 48,0,32
Ta 24,9,0
Ta 26,9,1024
Mvaa 48,4,48
buffer_load_dwordx4 a[152:155], v27, s[8:11], s28 offen offset:0 sc1
K 160,160,161
Zvaa 80,4,52
Ta 28,9,8192
Ta 30,9,9216
Mvaa 80,0,36
K 161,162,163
Zvaa 112,0,40
K 162,164,165
Mvaa 112,4,56
Zvaa 144,4,60
buffer_load_dwordx4 a[156:159], v27, s[8:11], s93 offen offset:0 sc1
K 163,166,167
Mvaa 144,0,44
W 8
s_add_u32 s28, s28, 512
s_cmp_eq_u32 s28, s4
s_cselect_b32 s93, 10240, 0
Zvaa 148,8,44
s_sub_u32 s28, s28, s93
s_add_u32 s93, s28, 20480
Ta 0,2,16384
Mvaa 148,12,60
s_add_u32 s36, s29, 0
Ta 2,2,17408
Zvaa 116,12,56
Ta 4,2,24576
Ta 6,2,25600
Mvaa 116,8,40
buffer_load_dwordx4 v[188:191], v26, s[8:11], s28 offen offset:0 sc1
Zvaa 84,8,36
Mvaa 84,12,52
Zvaa 52,12,48
Mvaa 52,8,32
W 8
Ta 8,3,16384
Ta 10,3,17408
Zvaa 56,16,32
buffer_load_dwordx4 v[196:199], v26, s[8:11], s93 offen offset:0 sc1
Mvaa 56,20,48
Zvaa 88,20,52
Ta 12,3,24576
Ta 14,3,25600
Mvaa 88,16,36
Zvaa 120,16,40
buffer_store_dwordx4 v[160:163], v1, s[8:11], s36 offen offset:0 sc1
K 168,168,169
Mvaa 120,20,56
K 169,170,171
K 170,172,173
Zvaa 152,20,60
Mvaa 152,16,44
W 8
Ta 16,4,16384
K 171,174,175
Zvaa 156,24,44
Ta 18,4,17408
Mvaa 156,28,60
buffer_load_dwordx4 v[204:207], v27, s[8:11], s28 offen offset:0 sc1
Zvaa 124,28,56
Ta 20,4,24576
Ta 22,4,25600
Mvaa 124,24,40
Zvaa 92,24,36
Mvaa 92,28,52
Zvaa 60,28,48
buffer_load_dwordx4 v[212:215], v27, s[8:11], s93 offen offset:0 sc1
Mvaa 60,24,32
s_waitcnt vmcnt(5) lgkmcnt(8)
s_add_u32 s28, s28, 512
s_cmp_eq_u32 s28, s4
s_cselect_b32 s93, 10240, 0
Mvaa 32,0,128
s_sub_u32 s28, s28, s93
s_add_u32 s93, s28, 20480
Ta 24,5,16384
Mvaa 32,4,144
Ta 26,5,17408
Mvaa 64,4,148
Ta 28,5,24576
Ta 30,5,25600
Mvaa 64,0,132
buffer_store_dwordx4 v[168:171], v1, s[8:11], s36 offen offset:1024 sc1
K 176,176,177
Mvaa 96,0,136
K 177,178,179
K 178,180,181
Mvaa 96,4,152
Mvaa 128,4,156
Mvaa 128,0,140
W 8
Ta 0,6,16384
K 179,182,183
Mvaa 132,8,140
buffer_load_dwordx4 v[220:223], v26, s[8:11], s28 offen offset:0 sc1
Ta 2,6,17408
Mvaa 132,12,156
Mvaa 100,12,152
Ta 4,6,24576
Ta 6,6,25600
Mvaa 100,8,136
Mvaa 68,8,132
Mvaa 68,12,148
buffer_load_dwordx4 v[228:231], v26, s[8:11], s93 offen offset:0 sc1
Mvaa 36,12,144
Mvaa 36,8,128
W 8
Ta 8,7,16384
Ta 10,7,17408
Mvaa 40,16,128
Mvaa 40,20,144
Mvaa 72,20,148
buffer_load_dwordx4 v[236:239], v27, s[8:11], s28 offen offset:0 sc1
Ta 12,7,24576
Mvaa 72,16,132
buffer_store_dwordx4 v[176:179], v1, s[8:11], s36 offen offset:2048 sc1
Ta 14,7,25600
Mvaa 104,16,136
Mvaa 104,20,152
Mvaa 136,20,156
Mvaa 136,16,140
W 8
Ta 16,8,16384
Ta 18,8,17408
Mvaa 140,24,140
buffer_load_dwordx4 v[244:247], v27, s[8:11], s93 offen offset:0 sc1
Mvaa 140,28,156
s_add_u32 s28, s28, 512
s_cmp_eq_u32 s28, s4
s_cselect_b32 s93, 10240, 0
s_sub_u32 s28, s28, s93
Mvaa 108,28,152
s_add_u32 s93, s28, 20480
Ta 20,8,24576
Ta 22,8,25600
Mvaa 108,24,136
Mvaa 76,24,132
Mvaa 76,28,148
buffer_load_dwordx4 v[252:255], v26, s[8:11], s28 offen offset:0 sc1
Mvaa 44,28,144
Mvaa 44,24,128
W 8
Ta 24,9,16384
Ta 26,9,17408
Mvaa 48,0,128
Mvaa 48,4,144
Mvaa 80,4,148
D 22,48,65,2048
Ta 28,9,24576
Mvaa 80,0,132
buffer_store_dwordx4 v[184:187], v1, s[8:11], s36 offen offset:3072 sc1
Ta 30,9,25600
Mvaa 112,0,136
s_add_u32 s36, s29, 4096
s_lshl_b32 s80, s56, 9
Mvaa 112,4,152
Mvaa 144,4,156
s_lshl_b32 s81, s57, 4
Mvaa 144,0,140
W 8
s_add_u32 s80, s80, s81
Ta 0,10,0
Mvaa 148,8,140
buffer_load_dwordx4 a[32:35], v26, s[8:11], s93 offen offset:0 sc1
Ta 2,10,1024
Mvaa 148,12,156
s_and_b32 s81, s14, 1
Mvaa 116,12,152
s_lshl_b32 s81, s81, 3
Ta 4,10,8192
Ta 6,10,9216
Mvaa 116,8,136
Mvaa 84,8,132
s_add_u32 s80, s80, s81
Mvaa 84,12,148
s_add_u32 s71, s80, 4096
buffer_load_dwordx4 a[36:39], v27, s[8:11], s28 offen offset:0 sc1
Mvaa 52,12,144
Mvaa 52,8,128
W 8
Ta 8,11,0
Ta 10,11,1024
Mvaa 56,16,128
Mvaa 56,20,144
Mvaa 88,20,148
buffer_store_dwordx4 v[192:195], v1, s[8:11], s36 offen offset:0 sc1
Ta 12,11,8192
Mvaa 88,16,132
Ta 14,11,9216
Mvaa 120,16,136
Mvaa 120,20,152
Mvaa 152,20,156
Mvaa 152,16,140
W 8
buffer_load_dwordx4 a[40:43], v27, s[8:11], s93 offen offset:0 sc1
Ta 16,12,0
Mvaa 156,24,140
s_add_u32 s28, s28, 512
s_cmp_eq_u32 s28, s4
s_cselect_b32 s93, 10240, 0
s_sub_u32 s28, s28, s93
Mvaa 156,28,156
s_add_u32 s93, s28, 20480
Ta 18,12,1024
Mvaa 124,28,152
Ta 20,12,8192
Ta 22,12,9216
Mvaa 124,24,136
Mvaa 92,24,132
buffer_load_dwordx4 a[44:47], v26, s[8:11], s28 offen offset:0 sc1
Mvaa 92,28,148
Mvaa 60,28,144
Mvaa 60,24,128
s_waitcnt vmcnt(8) lgkmcnt(8)
Ta 24,13,0
Ta 26,13,1024
Mvav 32,0,188
Mvav 32,4,220
D 23,48,65,3072
Mvav 64,4,228
s_add_u32 s65, s5, 16
s_cmp_ge_u32 s65, 20
s_cselect_b32 s90, 20, 0
buffer_store_dwordx4 v[200:203], v1, s[8:11], s36 offen offset:1024 sc1
Mvav 64,0,196
s_sub_u32 s65, s65, s90
s_lshl_b32 s65, s65, 18
s_add_u32 m0, s63, 98304
Mvav 96,0,204
Ta 28,13,8192
Ta 30,13,9216
Mvav 96,4,236
Mvav 128,4,244
Mvav 128,0,212
W 8
buffer_load_dwordx4 a[48:51], v26, s[8:11], s93 offen offset:0 sc1
Ta 0,14,0
Mvav 132,8,212
Ta 2,14,1024
v_add_u32 v2, 65536, v10
Mvav 132,12,244
Mvav 100,12,236
Ta 4,14,8192
Ta 6,14,9216
Mvav 100,8,204
v_add_u32 v3, 65536, v11
Mvav 68,8,196
buffer_load_dwordx4 a[52:55], v27, s[8:11], s28 offen offset:0 sc1
Mvav 68,12,228
Mvav 36,12,220
Mvav 36,8,188
W 8
Ta 8,15,0
Ta 10,15,1024
Mvav 40,16,188
v_add_u32 v4, 65536, v12
Mvav 40,20,220
buffer_load_dwordx4 a[56:59], v27, s[8:11], s93 offen offset:0 sc1
Mvav 72,20,228
s_add_u32 s28, s28, 512
s_cmp_eq_u32 s28, s4
s_cselect_b32 s93, 10240, 0
buffer_store_dwordx4 v[208:211], v1, s[8:11], s36 offen offset:2048 sc1
Mvav 72,16,196
s_sub_u32 s28, s28, s93
s_add_u32 s93, s28, 20480
Ta 12,15,8192
Mvav 104,16,204
Ta 14,15,9216
v_add_u32 v5, 65536, v13
Mvav 104,20,236
Mvav 136,20,244
Mvav 136,16,212
W 8
D 16,48,65,0
Ta 16,24,0
Mvav 140,24,212
Ta 18,24,1024
v_add_u32 v6, 65536, v14
Mvav 140,28,244
Mvav 108,28,236
Ta 20,24,8192
Ta 22,24,9216
Mvav 108,24,204
v_add_u32 v7, 65536, v15
Mvav 76,24,196
buffer_load_dwordx4 a[60:63], v26, s[8:11], s28 offen offset:0 sc1
Mvav 76,28,228
Mvav 44,28,220
Mvav 44,24,188
W 8
Ta 24,25,0
Ta 26,25,1024
Mvav 48,0,188
v_add_u32 v8, 65536, v24
Mvav 48,4,220
buffer_load_dwordx4 v[184:187], v26, s[8:11], s93 offen offset:0 sc1
Mvav 80,4,228
buffer_store_dwordx4 v[216:219], v1, s[8:11], s36 offen offset:3072 sc1
Ta 28,25,8192
Mvav 80,0,196
s_add_u32 s36, s29, 458752
Ta 30,25,9216
v_add_u32 v9, 65536, v25
Mvav 112,0,204
Mvav 112,4,236
Mvav 144,4,244
Mvav 144,0,212
W 8
buffer_load_dwordx4 v[192:195], v27, s[8:11], s28 offen offset:0 sc1
Ta 0,10,16384
Mvav 148,8,212
Ta 2,10,17408
Mvav 148,12,244
Mvav 116,12,236
Ta 4,10,24576
Ta 6,10,25600
Mvav 116,8,204
Mvav 84,8,196
D 17,48,65,1024
Mvav 84,12,228
Mvav 52,12,220
Mvav 52,8,188
W 8
Ta 8,11,16384
Ta 10,11,17408
Mvav 56,16,188
Mvav 56,20,220
buffer_store_dwordx4 v[224:227], v1, s[8:11], s36 offen offset:0 sc1
Mvav 88,20,228
Ta 12,11,24576
Ta 14,11,25600
Mvav 88,16,196
Mvav 120,16,204
Mvav 120,20,236
Mvav 152,20,244
buffer_load_dwordx4 a[128:131], v27, s[8:11], s93 offen offset:0 sc1
Mvav 152,16,212
W 8
s_add_u32 s28, s28, 512
s_cmp_eq_u32 s28, s4
s_cselect_b32 s93, 10240, 0
Mvav 156,24,212
s_sub_u32 s28, s28, s93
s_add_u32 s93, s28, 20480
Ta 16,12,16384
Mvav 156,28,244
Ta 18,12,17408
Mvav 124,28,236
Ta 20,12,24576
Ta 22,12,25600
Mvav 124,24,204
buffer_load_dwordx4 a[132:135], v26, s[8:11], s28 offen offset:0 sc1
Mvav 92,24,196
Mvav 92,28,228
Mvav 60,28,220
Mvav 60,24,188
s_waitcnt vmcnt(10) lgkmcnt(8)
Ta 24,13,16384
Ta 26,13,17408
Mvav 32,0,252
buffer_load_dwordx4 a[136:139], v26, s[8:11], s93 offen offset:0 sc1
Mvaa 32,4,44
)"
R"(buffer_store_dwordx4 v[232:235], v1, s[8:11], s36 offen offset:1024 sc1
Mvaa 64,4,48
Ta 28,13,24576
Ta 30,13,25600
Mvaa 64,0,32
Mvaa 96,0,36
Mvaa 96,4,52
Mvaa 128,4,56
buffer_load_dwordx4 a[140:143], v27, s[8:11], s28 offen offset:0 sc1
Mvaa 128,0,40
W 8
Ta 0,14,16384
Ta 2,14,17408
Mvaa 132,8,40
Mvaa 132,12,56
Mvaa 100,12,52
Ta 4,14,24576
Ta 6,14,25600
Mvaa 100,8,36
D 18,48,65,2048
Mvaa 68,8,32
Mvaa 68,12,48
Mvaa 36,12,44
Mvav 36,8,252
W 8
Ta 8,15,16384
Ta 10,15,17408
Mvav 40,16,252
buffer_load_dwordx4 a[144:147], v27, s[8:11], s93 offen offset:0 sc1
Mvaa 40,20,44
s_add_u32 s28, s28, 512
s_cmp_eq_u32 s28, s4
s_cselect_b32 s93, 10240, 0
buffer_store_dwordx4 v[240:243], v1, s[8:11], s36 offen offset:2048 sc1
Mvaa 72,20,48
s_sub_u32 s28, s28, s93
s_add_u32 s93, s28, 20480
Ta 12,15,24576
Mvaa 72,16,32
Ta 14,15,25600
Mvaa 104,16,36
Mvaa 104,20,52
Mvaa 136,20,56
buffer_load_dwordx4 a[148:151], v26, s[8:11], s28 offen offset:0 sc1
Mvaa 136,16,40
W 8
Ta 16,24,16384
Ta 18,24,17408
Mvaa 140,24,40
Mvaa 140,28,56
Mvaa 108,28,52
Ta 20,24,24576
Ta 22,24,25600
Mvaa 108,24,36
buffer_load_dwordx4 a[152:155], v26, s[8:11], s93 offen offset:0 sc1
Mvaa 76,24,32
Mvaa 76,28,48
Mvaa 44,28,44
Mvav 44,24,252
W 8
Ta 24,25,16384
Ta 26,25,17408
Mvav 48,0,252
buffer_load_dwordx4 a[156:159], v27, s[8:11], s28 offen offset:0 sc1
Mvaa 48,4,44
buffer_store_dwordx4 v[248:251], v1, s[8:11], s36 offen offset:3072 sc1
G 160,160
Mvaa 80,4,48
G 161,161
G 162,162
Mvaa 80,0,32
G 163,163
G 164,164
Mvaa 112,0,36
G 165,165
G 166,166
Mvaa 112,4,52
Ta 28,25,24576
Ta 30,25,25600
Mvaa 144,4,56
D 19,48,65,3072
G 167,167
Mvaa 144,0,40
W 8
s_add_u32 m0, s63, 102400
K 160,160,161
Mvaa 148,8,40
K 161,162,163
K 162,164,165
Mvaa 148,12,56
Ta 0,2,0
K 163,166,167
Mvaa 116,12,52
s_add_u32 s36, s29, 462848
Ta 2,2,1024
Ta 4,2,8192
Mvaa 116,8,36
buffer_load_dwordx4 v[200:203], v27, s[8:11], s93 offen offset:0 sc1
Ta 6,2,9216
Mvaa 84,8,32
s_add_u32 s28, s28, 512
s_cmp_eq_u32 s28, s4
s_cselect_b32 s93, 10240, 0
s_sub_u32 s28, s28, s93
Mvaa 84,12,48
s_add_u32 s93, s28, 20480
Mvaa 52,12,44
Mvav 52,8,252
W 8
Ta 8,3,0
Ta 10,3,1024
Mvav 56,16,252
buffer_store_dwordx4 v[160:163], v1, s[8:11], s36 offen offset:0 sc1
G 168,168
Mvaa 56,20,44
G 169,169
G 170,170
Mvaa 88,20,48
G 171,171
G 172,172
Mvaa 88,16,32
G 173,173
G 174,174
Mvaa 120,16,36
Ta 12,3,8192
Ta 14,3,9216
Mvaa 120,20,52
buffer_load_dwordx4 v[208:211], v26, s[8:11], s28 offen offset:0 sc1
G 175,175
Mvaa 152,20,56
K 168,168,169
K 169,170,171
Mvaa 152,16,40
W 8
K 170,172,173
K 171,174,175
Mvaa 156,24,40
Ta 16,4,0
Ta 18,4,1024
Mvaa 156,28,56
Mvaa 124,28,52
buffer_load_dwordx4 v[216:219], v26, s[8:11], s93 offen offset:0 sc1
Ta 20,4,8192
Mvaa 124,24,36
Ta 22,4,9216
Mvaa 92,24,32
Mvaa 92,28,48
Mvaa 60,28,44
Mvav 60,24,252
s_waitcnt vmcnt(10) lgkmcnt(8)
D 20,48,65,0
Ta 24,5,0
Mvaa 32,0,60
buffer_store_dwordx4 v[168:171], v1, s[8:11], s36 offen offset:1024 sc1
G 176,176
Mvaa 32,4,132
G 177,177
G 178,178
Mvaa 64,4,136
Ta 26,5,1024
G 179,179
Mvav 64,0,184
G 180,180
G 181,181
Mvav 96,0,192
Ta 28,5,8192
G 182,182
Mvaa 96,4,140
buffer_load_dwordx4 v[224:227], v27, s[8:11], s28 offen offset:0 sc1
Ta 30,5,9216
Mvaa 128,4,144
G 183,183
K 176,176,177
Mvaa 128,0,128
W 8
K 177,178,179
K 178,180,181
Mvaa 132,8,128
Ta 0,6,0
K 179,182,183
Mvaa 132,12,144
Ta 2,6,1024
Mvaa 100,12,140
buffer_load_dwordx4 v[188:191], v27, s[8:11], s93 offen offset:0 sc1
Ta 4,6,8192
Mvav 100,8,192
s_add_u32 s28, s28, 512
s_cmp_eq_u32 s28, s4
s_cselect_b32 s93, 10240, 0
s_sub_u32 s28, s28, s93
Mvav 68,8,184
s_add_u32 s93, s28, 20480
Ta 6,6,9216
Mvaa 68,12,136
Mvaa 36,12,132
Mvaa 36,8,60
W 8
buffer_load_dwordx4 v[196:199], v26, s[8:11], s28 offen offset:0 sc1
Ta 8,7,0
Mvaa 40,16,60
buffer_store_dwordx4 v[176:179], v1, s[8:11], s36 offen offset:2048 sc1
G 160,184
Mvaa 40,20,132
G 161,185
G 162,186
Mvaa 72,20,136
Ta 10,7,1024
G 163,187
Mvav 72,16,184
G 164,188
G 165,189
Mvav 104,16,192
Ta 12,7,8192
G 166,190
Mvaa 104,20,140
buffer_load_dwordx4 v[204:207], v26, s[8:11], s93 offen offset:0 sc1
Ta 14,7,9216
Mvaa 136,20,144
G 167,191
K 160,160,161
Mvaa 136,16,128
W 8
K 161,162,163
K 162,164,165
Mvaa 140,24,128
Ta 16,8,0
K 163,166,167
Mvaa 140,28,144
Ta 18,8,1024
Mvaa 108,28,140
D 21,48,65,1024
Ta 20,8,8192
Mvav 108,24,192
Ta 22,8,9216
Mvav 76,24,184
Mvaa 76,28,136
Mvaa 44,28,132
Mvaa 44,24,60
W 8
buffer_load_dwordx4 v[212:215], v27, s[8:11], s28 offen offset:0 sc1
Ta 24,9,0
Mvaa 48,0,60
buffer_store_dwordx4 v[160:163], v1, s[8:11], s36 offen offset:3072 sc1
G 168,192
Mvaa 48,4,132
G 169,193
G 170,194
Mvaa 80,4,136
G 171,195
G 172,196
Mvav 80,0,184
Ta 26,9,1024
G 173,197
Mvav 112,0,192
G 174,198
G 175,199
Mvaa 112,4,140
buffer_load_dwordx4 v[220:223], v27, s[8:11], s93 offen offset:0 sc1
Ta 28,9,8192
Mvaa 144,4,144
s_add_u32 s28, s28, 512
s_cmp_eq_u32 s28, s4
s_cselect_b32 s93, 10240, 0
s_sub_u32 s28, s28, s93
Mvaa 144,0,128
W 7
s_add_u32 s93, s28, 20480
Ta 30,9,9216
Mvaa 148,8,128
K 168,168,169
K 169,170,171
Mvaa 148,12,144
K 170,172,173
K 171,174,175
Mvaa 116,12,140
buffer_load_dwordx4 v[228:231], v26, s[8:11], s28 offen offset:0 sc1
Ta 0,2,16384
Mvav 116,8,192
Ta 2,2,17408
v_accvgpr_write_b32 a192, v168
Mvav 84,8,184
v_accvgpr_write_b32 a193, v169
v_accvgpr_write_b32 a194, v170
Mvaa 84,12,136
Ta 4,2,24576
Ta 6,2,25600
Mvaa 52,12,132
v_accvgpr_write_b32 a195, v171
G 176,64
Mvaa 52,8,60
W 8
D 22,48,65,2048
G 177,65
Mvaa 56,16,60
D 23,48,65,3072
G 178,66
Mvaa 56,20,132
Ta 8,3,16384
Ta 10,3,17408
Mvaa 88,20,136
G 179,67
G 180,68
Mvav 88,16,184
Ta 12,3,24576
Ta 14,3,25600
Mvav 120,16,192
G 181,69
G 182,70
Mvaa 120,20,140
buffer_load_dwordx4 v[236:239], v26, s[8:11], s93 offen offset:0 sc1
G 183,71
Mvaa 152,20,144
v_lshrrev_b32 v20, 4, v1
K 176,176,177
Mvaa 152,16,128
W 8
Ta 16,4,16384
v_add_u32 v20, 0, v20
Mvaa 156,24,128
Ta 18,4,17408
K 177,178,179
Mvaa 156,28,144
v_mul_u32_u24 v22, 205, v20
K 178,180,181
Mvaa 124,28,140
buffer_load_dwordx4 v[232:235], v27, s[8:11], s28 offen offset:0 sc1
K 179,182,183
Mvav 124,24,192
Ta 20,4,24576
Ta 22,4,25600
Mvav 92,24,184
v_lshrrev_b32 v22, 12, v22
v_accvgpr_write_b32 a196, v176
Mvaa 92,28,136
v_mul_u32_u24 v23, 20, v22
v_accvgpr_write_b32 a197, v177
Mvaa 60,28,132
v_accvgpr_write_b32 a198, v178
v_sub_u32 v20, v20, v23
Mvaa 60,24,60
s_waitcnt vmcnt(12) lgkmcnt(8)
buffer_load_dwordx4 a[32:35], v27, s[8:11], s93 offen offset:0 sc1
v_accvgpr_write_b32 a199, v179
Mvaa 32,0,148
s_add_u32 s28, s28, 512
s_cmp_eq_u32 s28, s4
s_cselect_b32 s93, 10240, 0
s_sub_u32 s28, s28, s93
Mvav 32,4,208
s_add_u32 s93, s28, 20480
Ta 24,5,16384
Ta 26,5,17408
Mvav 64,4,216
v_lshrrev_b32 v23, 1, v22
G 168,200
Mvaa 64,0,152
v_and_b32 v23, 2, v23
G 169,201
Mvaa 96,0,156
buffer_load_dwordx4 a[36:39], v26, s[8:11], s28 offen offset:0 sc1
Ta 28,5,24576
Mvav 96,4,224
Ta 30,5,25600
G 170,202
Mvav 128,4,188
v_xor_b32 v20, v20, v23
G 171,203
Mvav 128,0,200
W 8
v_mul_u32_u24 v22, 80, v22
G 172,204
Mvav 132,8,200
v_add_lshl_u32 v16, v20, v22, 4
G 173,205
Mvav 132,12,188
buffer_load_dwordx4 a[40:43], v26, s[8:11], s93 offen offset:0 sc1
Ta 0,6,16384
Mvav 100,12,224
Ta 2,6,17408
G 174,206
Mvaa 100,8,156
v_lshrrev_b32 v20, 4, v1
G 175,207
Mvaa 68,8,152
Ta 4,6,24576
v_add_u32 v20, 64, v20
Mvav 68,12,216
Ta 6,6,25600
K 168,168,169
Mvav 36,12,208
buffer_load_dwordx4 a[44:47], v27, s[8:11], s28 offen offset:0 sc1
K 169,170,171
Mvaa 36,8,148
W 8
v_mul_u32_u24 v22, 205, v20
K 170,172,173
Mvaa 40,16,148
v_lshrrev_b32 v22, 12, v22
K 171,174,175
Mvav 40,20,208
)"
R"(Ta 8,7,16384
v_mul_u32_u24 v23, 20, v22
Mvav 72,20,216
Ta 10,7,17408
v_accvgpr_write_b32 a200, v168
Mvaa 72,16,152
buffer_load_dwordx4 a[48:51], v27, s[8:11], s93 offen offset:0 sc1
v_accvgpr_write_b32 a201, v169
Mvaa 104,16,156
s_add_u32 s28, s28, 512
s_cmp_eq_u32 s28, s4
s_cselect_b32 s93, 10240, 0
s_sub_u32 s28, s28, s93
Mvav 104,20,224
s_add_u32 s93, s28, 20480
v_sub_u32 v20, v20, v23
v_accvgpr_write_b32 a202, v170
Mvav 136,20,188
Ta 12,7,24576
Ta 14,7,25600
Mvav 136,16,200
W 8
v_lshrrev_b32 v23, 1, v22
v_accvgpr_write_b32 a203, v171
Mvav 140,24,200
buffer_load_dwordx4 a[52:55], v26, s[8:11], s28 offen offset:0 sc1
v_and_b32 v23, 2, v23
Mvav 140,28,188
G 176,72
G 177,73
Mvav 108,28,224
v_xor_b32 v20, v20, v23
G 178,74
Mvaa 108,24,156
Ta 16,8,16384
Ta 18,8,17408
Mvaa 76,24,152
v_mul_u32_u24 v22, 80, v22
G 179,75
Mvav 76,28,216
buffer_load_dwordx4 a[56:59], v26, s[8:11], s93 offen offset:0 sc1
G 180,76
Mvav 44,28,208
Ta 20,8,24576
v_add_lshl_u32 v17, v20, v22, 4
Mvaa 44,24,148
W 7
Ta 22,8,25600
G 181,77
Mvaa 48,0,148
v_subrev_u32 v17, 1024, v17
G 182,78
Mvav 48,4,208
v_lshrrev_b32 v20, 4, v1
G 183,79
Mvav 80,4,216
buffer_load_dwordx4 a[184:187], v27, s[8:11], s28 offen offset:0 sc1
K 176,176,177
Mvaa 80,0,152
Ta 24,9,16384
v_add_u32 v20, 128, v20
Mvaa 112,0,156
Ta 26,9,17408
K 177,178,179
Mvav 112,4,224
v_mul_u32_u24 v22, 205, v20
K 178,180,181
Mvav 144,4,188
v_lshrrev_b32 v22, 12, v22
K 179,182,183
Mvav 144,0,200
W 6
buffer_load_dwordx4 a[188:191], v27, s[8:11], s93 offen offset:0 sc1
Ta 28,9,24576
Mvav 148,8,200
s_add_u32 s28, s28, 512
s_cmp_eq_u32 s28, s4
s_cselect_b32 s93, 10240, 0
s_sub_u32 s28, s28, s93
Mvav 148,12,188
s_add_u32 s93, s28, 20480
Ta 30,9,25600
v_accvgpr_write_b32 a204, v176
Mvav 116,12,224
v_mul_u32_u24 v23, 20, v22
v_accvgpr_write_b32 a205, v177
Mvaa 116,8,156
s_waitcnt vmcnt(15)
v_mov_b64 v[28:29], s[12:13]
s_mov_b64 exec, 1
buffer_store_dwordx2 v[28:29], off, s[8:11], s71 sc1
s_mov_b64 exec, -1
v_sub_u32 v20, v20, v23
v_accvgpr_write_b32 a206, v178
Mvaa 84,8,152
s_waitcnt vmcnt(12) lgkmcnt(0)
s_barrier
v_accvgpr_write_b32 a207, v179
Mvav 84,12,216
v_lshrrev_b32 v23, 1, v22
G 168,208
Mvav 52,12,208
v_and_b32 v23, 2, v23
G 169,209
Mvaa 52,8,148
v_xor_b32 v20, v20, v23
G 170,210
Mvaa 56,16,148
buffer_load_dwordx4 a[60:63], v26, s[8:11], s28 offen offset:0 sc1
G 171,211
Mvav 56,20,208
Ta 0,10,32768
Ta 2,10,33792
Mvav 88,20,216
Ta 4,10,40960
Ta 6,10,41984
Mvaa 88,16,152
v_mul_u32_u24 v22, 80, v22
G 172,212
Mvaa 120,16,156
v_add_lshl_u32 v18, v20, v22, 4
G 173,213
Mvav 120,20,224
buffer_load_dwordx4 v[184:187], v26, s[8:11], s93 offen offset:0 sc1
Ta 8,11,32768
Mvav 152,20,188
Ta 10,11,33792
G 174,214
Mvav 152,16,200
v_subrev_u32 v18, 2048, v18
G 175,215
Mvav 156,24,200
Ta 12,11,40960
v_lshrrev_b32 v20, 4, v1
Mvav 156,28,188
Ta 14,11,41984
K 168,168,169
Mvav 124,28,224
buffer_load_dwordx4 v[192:195], v27, s[8:11], s28 offen offset:0 sc1
v_add_u32 v20, 192, v20
Mvaa 124,24,156
K 169,170,171
K 170,172,173
Mvaa 92,24,152
v_mul_u32_u24 v22, 205, v20
K 171,174,175
Mvav 92,28,216
Ta 16,12,32768
v_lshrrev_b32 v22, 12, v22
Mvav 60,28,208
Ta 18,12,33792
v_accvgpr_write_b32 a208, v168
Mvaa 60,24,148
s_waitcnt vmcnt(12) lgkmcnt(6)
buffer_load_dwordx4 a[128:131], v27, s[8:11], s93 offen offset:0 sc1
v_mul_u32_u24 v23, 20, v22
Mvav 32,0,196
s_add_u32 s28, s28, 512
v_accvgpr_write_b32 a209, v169
v_accvgpr_write_b32 a210, v170
Mvav 32,4,228
s_cmp_eq_u32 s28, s4
s_cselect_b32 s93, 10240, 0
Ta 20,12,40960
v_sub_u32 v20, v20, v23
Mvav 64,4,236
s_sub_u32 s28, s28, s93
Ta 22,12,41984
v_accvgpr_write_b32 a211, v171
Mvav 64,0,204
s_add_u32 s93, s28, 20480
v_lshrrev_b32 v23, 1, v22
G 176,80
Mvav 96,0,212
G 177,81
v_and_b32 v23, 2, v23
Mvav 96,4,232
G 178,82
v_xor_b32 v20, v20, v23
Mvaa 128,4,32
Ta 24,13,32768
Ta 26,13,33792
Mvav 128,0,220
W 6
buffer_load_dwordx4 a[132:135], v26, s[8:11], s28 offen offset:0 sc1
G 179,83
Mvav 132,8,220
v_mul_u32_u24 v22, 80, v22
G 180,84
Mvaa 132,12,32
Ta 28,13,40960
Ta 30,13,41984
Mvav 100,12,232
G 181,85
v_add_lshl_u32 v19, v20, v22, 4
Mvav 100,8,212
G 182,86
v_subrev_u32 v19, 3072, v19
Mvav 68,8,204
G 183,87
v_lshrrev_b32 v20, 4, v1
Mvav 68,12,236
Ta 0,14,32768
K 176,176,177
Mvav 36,12,228
Ta 2,14,33792
K 177,178,179
Mvav 36,8,196
W 6
buffer_load_dwordx4 a[136:139], v26, s[8:11], s93 offen offset:0 sc1
v_add_u32 v20, 256, v20
Mvav 40,16,196
K 178,180,181
v_mul_u32_u24 v22, 205, v20
Mvav 40,20,228
Ta 4,14,40960
K 179,182,183
Mvav 72,20,236
Ta 6,14,41984
v_accvgpr_write_b32 a212, v176
Mvav 72,16,204
v_lshrrev_b32 v22, 12, v22
v_accvgpr_write_b32 a213, v177
Mvav 104,16,212
v_mul_u32_u24 v23, 20, v22
v_accvgpr_write_b32 a214, v178
Mvav 104,20,232
v_sub_u32 v20, v20, v23
v_accvgpr_write_b32 a215, v179
Mvaa 136,20,32
Ta 8,15,32768
Ta 10,15,33792
Mvav 136,16,220
W 6
buffer_load_dwordx4 a[140:143], v27, s[8:11], s28 offen offset:0 sc1
G 168,216
Mvav 140,24,220
v_lshrrev_b32 v23, 1, v22
G 169,217
Mvaa 140,28,32
Ta 12,15,40960
v_and_b32 v23, 2, v23
Mvav 108,28,232
Ta 14,15,41984
G 170,218
Mvav 108,24,212
v_xor_b32 v20, v20, v23
G 171,219
Mvav 76,24,204
G 172,220
v_mul_u32_u24 v22, 80, v22
Mvav 76,28,236
Ta 16,24,32768
G 173,221
Mvav 44,28,228
Ta 18,24,33792
v_add_lshl_u32 v20, v20, v22, 4
Mvav 44,24,196
W 6
buffer_load_dwordx4 a[144:147], v27, s[8:11], s93 offen offset:0 sc1
G 174,222
Mvav 48,0,196
s_add_u32 s28, s28, 512
G 175,223
v_lshrrev_b32 v22, 6, v1
Mvav 48,4,228
s_cmp_eq_u32 s28, s4
s_cselect_b32 s93, 10240, 0
Ta 20,24,40960
Ta 22,24,41984
Mvav 80,4,236
s_sub_u32 s28, s28, s93
K 168,168,169
v_mul_u32_u24 v22, 320, v22
Mvav 80,0,204
s_add_u32 s93, s28, 20480
K 169,170,171
v_bfe_u32 v23, v1, 4, 2
Mvav 112,0,212
K 170,172,173
K 171,174,175
Mvav 112,4,232
Ta 24,25,32768
v_lshl_add_u32 v22, v23, 3, v22
Mvaa 144,4,32
Ta 26,25,33792
v_accvgpr_write_b32 a216, v168
Mvav 144,0,220
W 6
buffer_load_dwordx4 v[200:203], v26, s[8:11], s28 offen offset:0 sc1
v_bfe_u32 v23, v1, 8, 1
Mvav 148,8,220
v_accvgpr_write_b32 a217, v169
v_lshlrev_b32 v23, 5, v23
Mvaa 148,12,32
Ta 28,25,40960
v_accvgpr_write_b32 a218, v170
Mvav 116,12,232
Ta 30,25,41984
v_accvgpr_write_b32 a219, v171
Mvav 116,8,212
v_add_u32 v21, v22, v23
G 176,88
Mvav 84,8,204
v_sub_u32 v22, v22, v23
G 177,89
Mvav 84,12,236
s_mul_i32 s94, s59, 655360
Ta 0,10,49152
G 178,90
Mvav 52,12,228
s_mul_i32 s95, s58, 320
Ta 2,10,50176
G 179,91
Mvav 52,8,196
W 6
buffer_load_dwordx4 v[208:211], v26, s[8:11], s93 offen offset:0 sc1
G 180,92
Mvav 56,16,196
s_add_u32 s94, s94, s95
Ta 4,10,57344
Ta 6,10,58368
Mvav 56,20,228
G 181,93
G 182,94
Mvav 88,20,236
s_add_u32 s40, s26, s94
s_addc_u32 s41, s27, 0
G 183,95
Mvav 88,16,204
s_and_b32 s41, s41, 0xffff
Ta 8,11,49152
K 176,176,177
Mvav 120,16,212
s_mov_b32 s42, 5242880
Ta 10,11,50176
K 177,178,179
Mvav 120,20,232
s_mov_b32 s43, 0x20000
buffer_load_dwordx4 v[216:219], v27, s[8:11], s28 offen offset:0 sc1
K 178,180,181
Mvaa 152,20,32
s_sub_u32 s42, s42, s94
Ta 12,11,57344
K 179,182,183
Mvav 152,16,220
W 7
Ta 14,11,58368
v_accvgpr_write_b32 a220, v176
Mvav 156,24,220
s_mov_b64 s[44:45], s[8:9]
v_accvgpr_write_b32 a221, v177
v_accvgpr_write_b32 a222, v178
Mvaa 156,28,32
s_mov_b32 s47, 0x20000
Ta 16,12,49152
v_accvgpr_write_b32 a223, v179
Mvav 124,28,232
s_and_b32 s48, s14, 1
Ta 18,12,50176
buffer_load_dwordx4 v[224:227], v27, s[8:11], s93 offen offset:0 sc1
Mvav 124,24,212
G 168,224
G 169,225
Mvav 92,24,204
s_mul_i32 s48, s48, 5120
Ta 20,12,57344
Ta 22,12,58368
Mvav 92,28,236
s_lshr_b32 s51, s14, 1
G 170,226
G 171,227
Mvav 60,28,228
G 172,228
Mvav 60,24,196
s_waitcnt vmcnt(13) lgkmcnt(8)
s_and_b32 s94, s14, 1
G 173,229
Mvaa 32,0,36
s_add_u32 s28, s28, 512
Ta 24,13,49152
Ta 26,13,50176
Mvaa 32,4,52
s_cmp_eq_u32 s28, s4
s_cselect_b32 s93, 10240, 0
s_sub_u32 s28, s28, s93
G 174,230
Mvaa 64,4,56
s_add_u32 s93, s28, 20480
s_lshl_b32 s95, s57, 1
buffer_load_dwordx4 v[188:191], v26, s[8:11], s28 offen offset:0 sc1
Mvaa 64,0,40
s_add_u32 s94, s94, s95
Ta 28,13,57344
G 175,231
Mvaa 96,0,44
s_lshl_b32 s54, s94, 3
Ta 30,13,58368
K 168,168,169
Mvaa 96,4,184
)"
R"(K 169,170,171
K 170,172,173
Mvaa 128,4,188
s_add_u32 s54, s54, s59
K 171,174,175
Mvaa 128,0,48
W 8
Ta 0,14,49152
Ta 2,14,50176
Mvaa 132,8,48
s_lshl_b32 s54, s54, 6
buffer_load_dwordx4 v[196:199], v26, s[8:11], s93 offen offset:0 sc1
v_accvgpr_write_b32 a224, v168
Mvaa 132,12,188
s_xor_b32 s55, s54, 15872
v_accvgpr_write_b32 a225, v169
Mvaa 100,12,184
Ta 4,14,57344
v_accvgpr_write_b32 a226, v170
Mvaa 100,8,44
s_add_u32 s54, s54, 20480
Ta 6,14,58368
v_accvgpr_write_b32 a227, v171
Mvaa 68,8,40
s_and_b32 s81, s14, 1
G 176,96
Mvaa 68,12,56
s_add_u32 s55, s55, 20480
Mvaa 36,12,52
G 177,97
Mvaa 36,8,36
W 8
s_and_b32 s95, s14, 2
G 178,98
Mvaa 40,16,36
s_mul_i32 s91, s81, 17164288
Ta 8,15,49152
Ta 10,15,50176
Mvaa 40,20,52
s_lshl_b32 s94, s94, 2
buffer_load_dwordx4 v[204:207], v27, s[8:11], s28 offen offset:0 sc1
G 179,99
Mvaa 72,20,56
s_mul_i32 s82, s56, 536576
s_add_u32 s94, s94, s95
G 180,100
Mvaa 72,16,40
Ta 12,15,57344
Ta 14,15,58368
Mvaa 104,16,44
s_lshl_b32 s95, s95, 4
G 181,101
G 182,102
Mvaa 104,20,184
s_add_u32 s91, s91, s82
G 183,103
Mvaa 136,20,188
s_add_u32 s55, s55, s95
s_add_u32 s91, s91, 4194304
K 176,176,177
Mvaa 136,16,48
W 8
s_lshl_b32 s49, s94, 17
Ta 16,24,49152
Mvaa 140,24,48
Ta 18,24,50176
K 177,178,179
Mvaa 140,28,188
s_lshl_b32 s95, s59, 14
buffer_load_dwordx4 v[212:215], v27, s[8:11], s93 offen offset:0 sc1
K 178,180,181
Mvaa 108,28,184
s_lshr_b32 s82, s14, 1
Ta 20,24,57344
K 179,182,183
Mvaa 108,24,44
s_add_u32 s49, s49, s95
Ta 22,24,58368
v_accvgpr_write_b32 a228, v176
Mvaa 76,24,40
s_lshl_b32 s83, s82, 14
s_xor_b32 s50, s49, 16252928
Mvaa 76,28,56
v_accvgpr_write_b32 a229, v177
Mvaa 44,28,52
s_add_u32 s91, s91, s83
s_add_u32 s49, s49, 39845888
v_accvgpr_write_b32 a230, v178
Mvaa 44,24,36
W 8
Ta 24,25,49152
v_accvgpr_write_b32 a231, v179
Mvaa 48,0,36
s_add_u32 s50, s50, 39845888
Ta 26,25,50176
G 168,232
Mvaa 48,4,52
s_lshl_b32 s81, s81, 14
Mvaa 80,4,56
s_lshr_b32 s94, s15, 3
Ta 28,25,57344
G 169,233
Mvaa 80,0,40
s_lshl_b32 s82, s82, 16
Ta 30,25,58368
G 170,234
Mvaa 112,0,44
s_lshl_b32 s94, s94, 3
G 171,235
Mvaa 112,4,184
s_add_u32 s94, s94, s14
s_or_b32 s92, s81, s82
G 172,236
Mvaa 144,4,188
Mvaa 144,0,48
W 8
s_lshl_b32 s94, s94, 4
G 173,237
Mvaa 148,8,48
s_xor_b32 s92, s92, 0x10000
Ta 0,2,32768
Ta 2,2,33792
Mvaa 148,12,188
s_add_u32 s94, s94, s59
G 174,238
G 175,239
Mvaa 116,12,184
s_mul_i32 s52, s94, 5120
Ta 4,2,40960
Ta 6,2,41984
Mvaa 116,8,44
K 168,168,169
Mvaa 84,8,40
s_lshl_b32 s95, s59, 12
K 169,170,171
Mvaa 84,12,56
K 170,172,173
Mvaa 52,12,52
s_sub_u32 s52, s52, s95
K 171,174,175
Mvaa 52,8,36
W 8
s_add_u32 s52, s52, 56623104
Ta 8,3,32768
Mvaa 56,16,36
Ta 10,3,33792
v_accvgpr_write_b32 a232, v168
Mvaa 56,20,52
s_lshl_b32 s53, s94, 3
v_accvgpr_write_b32 a233, v169
Mvaa 88,20,56
Ta 12,3,40960
v_accvgpr_write_b32 a234, v170
Mvaa 88,16,40
s_add_u32 s53, s53, 65536
Ta 14,3,41984
v_accvgpr_write_b32 a235, v171
Mvaa 120,16,44
G 176,104
v_bfe_u32 v244, v1, 4, 1
Mvaa 120,20,184
Mvaa 152,20,188
G 177,105
v_mul_u32_u24 v244, 0xaaaa, v244
Mvaa 152,16,48
W 8
G 178,106
v_add_u32 v244, 0x4444, v244
Mvaa 156,24,48
Ta 16,4,32768
Ta 18,4,33792
Mvaa 156,28,188
G 179,107
v_lshl_or_b32 v240, v244, 16, v244
Mvaa 124,28,184
G 180,108
v_mov_b32 v241, 0xff800000
Mvaa 124,24,44
Ta 20,4,40960
Ta 22,4,41984
Mvaa 92,24,40
G 181,109
v_add_u32 v242, 0x10000, v1
Mvaa 92,28,56
G 182,110
v_bfe_u32 v244, v1, 4, 4
Mvaa 60,28,52
G 183,111
v_lshrrev_b32 v245, 8, v1
Mvaa 60,24,36
s_waitcnt vmcnt(8) lgkmcnt(8)
K 176,176,177
v_lshlrev_b32 v245, 2, v245
Mvaa 32,0,60
Ta 24,5,32768
Ta 26,5,33792
Mvaa 32,4,132
K 177,178,179
v_sub_u32 v244, v244, v245
Mvaa 64,4,136
K 178,180,181
v_cmp_le_i32_e64 s[72:73], 0, v244
Mvav 64,0,184
Ta 28,5,40960
K 179,182,183
Mvav 96,0,192
Ta 30,5,41984
v_accvgpr_write_b32 a236, v176
Mvaa 96,4,140
v_cmp_le_i32_e64 s[74:75], 1, v244
v_accvgpr_write_b32 a237, v177
Mvaa 128,4,144
v_cmp_le_i32_e64 s[76:77], 2, v244
v_accvgpr_write_b32 a238, v178
Mvaa 128,0,128
W 8
v_cmp_le_i32_e64 s[78:79], 3, v244
v_accvgpr_write_b32 a239, v179
Mvaa 132,8,128
Ta 0,6,32768
Ta 2,6,33792
Mvaa 132,12,144
v_add_u32 v243, 4096, v1
G 168,240
Mvaa 100,12,140
v_add_u32 v244, 8192, v1
G 169,241
Mvav 100,8,192
Ta 4,6,40960
v_add_u32 v245, 12288, v1
Mvav 68,8,184
s_mov_b32 s18, 0x3f803f80
Ta 6,6,41984
G 170,242
Mvaa 68,12,136
s_mov_b32 s19, 0x3f003f00
G 171,243
G 172,244
Mvaa 36,12,132
Mvaa 36,8,60
W 8
v_accvgpr_write_b32 a176, s18
G 173,245
Mvaa 40,16,60
Ta 8,7,32768
Ta 10,7,33792
Mvaa 40,20,132
v_accvgpr_write_b32 a180, s19
G 174,246
Mvaa 72,20,136
G 175,247
v_mov_b32 v252, v241
Mvav 72,16,184
Ta 12,7,40960
Ta 14,7,41984
Mvav 104,16,192
K 168,168,169
v_accvgpr_write_b32 a177, s18
Mvaa 104,20,140
K 169,170,171
v_accvgpr_write_b32 a181, s19
Mvaa 136,20,144
K 170,172,173
v_mov_b32 v253, v241
Mvaa 136,16,128
W 8
K 171,174,175
v_accvgpr_write_b32 a178, s18
Mvaa 140,24,128
Ta 16,8,32768
Ta 18,8,33792
Mvaa 140,28,144
v_accvgpr_write_b32 a240, v168
v_accvgpr_write_b32 a182, s19
Mvaa 108,28,140
v_accvgpr_write_b32 a241, v169
v_mov_b32 v254, v241
Mvav 108,24,192
Ta 20,8,40960
v_accvgpr_write_b32 a242, v170
Mvav 76,24,184
Ta 22,8,41984
v_accvgpr_write_b32 a179, s18
Mvaa 76,28,136
v_accvgpr_write_b32 a243, v171
v_accvgpr_write_b32 a183, s19
Mvaa 44,28,132
G 176,112
v_mov_b32 v255, v241
Mvaa 44,24,60
W 8
G 177,113
G 178,114
Mvaa 48,0,60
Ta 24,9,32768
v_accvgpr_write_b32 a160, 0
Mvaa 48,4,132
Ta 26,9,33792
G 179,115
Mvaa 80,4,136
v_accvgpr_write_b32 a161, 0
G 180,116
Mvav 80,0,184
Ta 28,9,40960
v_accvgpr_write_b32 a162, 0
Mvav 112,0,192
Ta 30,9,41984
G 181,117
Mvaa 112,4,140
v_accvgpr_write_b32 a163, 0
G 182,118
Mvaa 144,4,144
v_accvgpr_write_b32 a164, 0
G 183,119
Mvaa 144,0,128
W 8
v_accvgpr_write_b32 a165, 0
K 176,176,177
Mvaa 148,8,128
Ta 0,2,49152
v_accvgpr_write_b32 a166, 0
Mvaa 148,12,144
Ta 2,2,50176
K 177,178,179
Mvaa 116,12,140
v_accvgpr_write_b32 a167, 0
K 178,180,181
Mvav 116,8,192
v_accvgpr_write_b32 a168, 0
K 179,182,183
Mvav 84,8,184
Ta 4,2,57344
Ta 6,2,58368
Mvaa 84,12,136
v_accvgpr_write_b32 a169, 0
v_accvgpr_write_b32 a244, v176
Mvaa 52,12,132
v_accvgpr_write_b32 a245, v177
v_accvgpr_write_b32 a170, 0
Mvaa 52,8,60
W 8
v_accvgpr_write_b32 a246, v178
v_accvgpr_write_b32 a171, 0
Mvaa 56,16,60
Ta 8,3,49152
v_accvgpr_write_b32 a247, v179
Mvaa 56,20,132
Ta 10,3,50176
v_accvgpr_write_b32 a172, 0
Mvaa 88,20,136
G 168,248
v_accvgpr_write_b32 a173, 0
Mvav 88,16,184
G 169,249
v_accvgpr_write_b32 a174, 0
Mvav 120,16,192
Ta 12,3,57344
Ta 14,3,58368
Mvaa 120,20,140
G 170,250
v_accvgpr_write_b32 a175, 0
Mvaa 152,20,144
s_mov_b32 s90, 0x5d800000
G 171,251
G 172,252
Mvaa 152,16,128
W 8
s_mov_b64 s[88:89], 0
G 173,253
Mvaa 156,24,128
s_mov_b32 s64, 0
Ta 16,4,49152
Ta 18,4,50176
Mvaa 156,28,144
s_and_b32 s18, s14, 1
G 174,254
G 175,255
Mvaa 124,28,140
s_lshl_b32 s62, s18, 14
Ta 20,4,57344
Ta 22,4,58368
Mvav 124,24,192
K 168,168,169
Mvav 92,24,184
s_lshl_b32 s20, s57, 15
K 169,170,171
Mvaa 92,28,136
s_sub_u32 s20, 507904, s20
K 170,172,173
Mvaa 60,28,132
K 171,174,175
Mvaa 60,24,60
s_waitcnt vmcnt(0) lgkmcnt(8)
s_add_u32 s20, s20, s62
Ta 24,5,49152
Mvav 32,0,200
s_lshl_b32 s65, s57, 1
Ta 26,5,50176
v_accvgpr_write_b32 a248, v168
Mvav 32,4,188
)"
R"(s_sub_u32 s65, s65, 1
v_accvgpr_write_b32 a249, v169
Mvav 64,4,196
Ta 28,5,57344
v_accvgpr_write_b32 a250, v170
Mvav 64,0,208
s_max_i32 s65, s65, 0
Ta 30,5,58368
v_accvgpr_write_b32 a251, v171
Mvav 96,0,216
s_sub_u32 s66, s60, 1
G 176,120
Mvav 96,4,204
s_cmp_lt_u32 s14, 2
s_cselect_b32 s20, s20, 0
s_cselect_b32 s66, s66, 15
s_cselect_b32 s21, 32768, 0
Mvav 128,4,212
G 177,121
Mvav 128,0,224
W 8
s_add_u32 s61, s91, s20
G 178,122
Mvav 132,8,224
s_add_u32 s62, s62, s21
Ta 0,6,49152
Ta 2,6,50176
Mvav 132,12,212
G 179,123
G 180,124
Mvav 100,12,204
s_sub_u32 s63, 16384, s21
Ta 4,6,57344
Ta 6,6,58368
Mvav 100,8,216
s_lshl_b32 s67, s60, 7
G 181,125
Mvav 68,8,208
s_add_u32 s67, s67, s56
G 182,126
Mvav 68,12,196
G 183,127
Mvav 36,12,188
s_lshl_b32 s67, s67, 12
K 176,176,177
Mvav 36,8,200
W 8
s_add_u32 s67, s67, 39845888
Ta 8,7,49152
Mvav 40,16,200
s_lshl_b32 s69, s60, 3
Ta 10,7,50176
K 177,178,179
Mvav 40,20,188
K 178,180,181
Mvav 72,20,196
s_add_u32 s69, s69, s59
Ta 12,7,57344
K 179,182,183
Mvav 72,16,208
s_lshl_b32 s69, s69, 6
Ta 14,7,58368
v_accvgpr_write_b32 a252, v176
Mvav 104,16,216
Mvav 104,20,204
s_lshl_b32 s21, s58, 3
v_accvgpr_write_b32 a253, v177
Mvav 136,20,212
s_add_u32 s69, s69, s21
v_accvgpr_write_b32 a254, v178
Mvav 136,16,224
W 8
s_add_u32 s69, s69, 20480
v_accvgpr_write_b32 a255, v179
Mvav 140,24,224
Ta 16,8,49152
Ta 18,8,50176
Mvav 140,28,212
s_mov_b32 s70, 0
Mvav 108,28,204
s_mov_b32 s37, 0
Ta 20,8,57344
Ta 22,8,58368
Mvav 108,24,216
s_sub_u32 s18, s57, 4
Mvav 76,24,208
Mvav 76,28,196
s_lshl_b32 s18, s18, 2
Mvav 44,28,188
s_add_u32 s18, s18, s58
Mvav 44,24,200
W 8
Ta 24,9,49152
Ta 26,9,50176
Mvav 48,0,200
s_mul_i32 s85, s59, 7
Mvav 48,4,188
s_add_u32 s85, s85, s18
Mvav 80,4,196
s_cmp_lt_u32 s18, 7
s_cselect_b32 s85, s85, 99
Ta 28,9,57344
Ta 30,9,58368
Mvav 80,0,208
Mvav 112,0,216
s_cmp_lt_u32 s85, 54
s_cselect_b32 s65, 2, s65
Mvav 112,4,204
s_mov_b32 s68, 0
Mvav 144,4,212
s_mov_b64 s[24:25], -1
Mvav 144,0,224
W 8
Mvav 148,8,224
s_mov_b32 s22, 0x3e0293ee
Mvav 148,12,212
s_mov_b32 s23, 0x3e0293ee
Mvav 116,12,204
Mvav 116,8,216
Mvav 84,8,208
Mvav 84,12,196
Mvav 52,12,188
Mvav 52,8,200
W 4
Mvav 56,16,200
Mvav 56,20,188
Mvav 88,20,196
Mvav 88,16,208
Mvav 120,16,216
W 0
s_barrier
Ra 128,242,0
Mvav 120,20,204
Mvav 152,20,212
Ra 132,242,1024
Mvav 152,16,224
Mvav 156,24,224
Ra 136,242,2048
Mvav 156,28,212
Mvav 124,28,204
Ra 140,242,3072
Mvav 124,24,216
Mvav 92,24,208
Mvav 92,28,196
Mvav 60,28,188
Mvav 60,24,200
.Lago%=:
s_cmp_eq_u32 s60, 1
s_cbranch_scc1 .Lpe_48%=
s_cmp_eq_u32 s60, 0
s_cbranch_scc1 .Lpd_32%=
v_pk_mul_f32 v[32:33], v[32:33], s[22:23]
v_pk_mul_f32 v[34:35], v[34:35], s[22:23]
v_pk_mul_f32 v[36:37], v[36:37], s[22:23]
v_pk_mul_f32 v[38:39], v[38:39], s[22:23]
v_pk_mul_f32 v[64:65], v[64:65], s[22:23]
v_pk_mul_f32 v[66:67], v[66:67], s[22:23]
v_pk_mul_f32 v[68:69], v[68:69], s[22:23]
v_pk_mul_f32 v[70:71], v[70:71], s[22:23]
v_pk_mul_f32 v[96:97], v[96:97], s[22:23]
v_pk_mul_f32 v[98:99], v[98:99], s[22:23]
v_pk_mul_f32 v[100:101], v[100:101], s[22:23]
v_pk_mul_f32 v[102:103], v[102:103], s[22:23]
v_pk_mul_f32 v[128:129], v[128:129], s[22:23]
v_pk_mul_f32 v[130:131], v[130:131], s[22:23]
v_pk_mul_f32 v[132:133], v[132:133], s[22:23]
v_pk_mul_f32 v[134:135], v[134:135], s[22:23]
v_pk_mul_f32 v[40:41], v[40:41], s[22:23]
v_pk_mul_f32 v[42:43], v[42:43], s[22:23]
v_pk_mul_f32 v[44:45], v[44:45], s[22:23]
v_pk_mul_f32 v[46:47], v[46:47], s[22:23]
v_pk_mul_f32 v[72:73], v[72:73], s[22:23]
v_pk_mul_f32 v[74:75], v[74:75], s[22:23]
v_pk_mul_f32 v[76:77], v[76:77], s[22:23]
v_pk_mul_f32 v[78:79], v[78:79], s[22:23]
v_pk_mul_f32 v[104:105], v[104:105], s[22:23]
v_pk_mul_f32 v[106:107], v[106:107], s[22:23]
v_pk_mul_f32 v[108:109], v[108:109], s[22:23]
v_pk_mul_f32 v[110:111], v[110:111], s[22:23]
v_pk_mul_f32 v[136:137], v[136:137], s[22:23]
v_pk_mul_f32 v[138:139], v[138:139], s[22:23]
v_pk_mul_f32 v[140:141], v[140:141], s[22:23]
v_pk_mul_f32 v[142:143], v[142:143], s[22:23]
v_pk_mul_f32 v[48:49], v[48:49], s[22:23]
v_pk_mul_f32 v[50:51], v[50:51], s[22:23]
v_pk_mul_f32 v[52:53], v[52:53], s[22:23]
v_pk_mul_f32 v[54:55], v[54:55], s[22:23]
v_pk_mul_f32 v[80:81], v[80:81], s[22:23]
v_pk_mul_f32 v[82:83], v[82:83], s[22:23]
v_pk_mul_f32 v[84:85], v[84:85], s[22:23]
v_pk_mul_f32 v[86:87], v[86:87], s[22:23]
v_pk_mul_f32 v[112:113], v[112:113], s[22:23]
v_pk_mul_f32 v[114:115], v[114:115], s[22:23]
v_pk_mul_f32 v[116:117], v[116:117], s[22:23]
v_pk_mul_f32 v[118:119], v[118:119], s[22:23]
v_pk_mul_f32 v[144:145], v[144:145], s[22:23]
v_pk_mul_f32 v[146:147], v[146:147], s[22:23]
v_pk_mul_f32 v[148:149], v[148:149], s[22:23]
v_pk_mul_f32 v[150:151], v[150:151], s[22:23]
v_pk_mul_f32 v[56:57], v[56:57], s[22:23]
v_pk_mul_f32 v[58:59], v[58:59], s[22:23]
v_pk_mul_f32 v[60:61], v[60:61], s[22:23]
v_pk_mul_f32 v[62:63], v[62:63], s[22:23]
v_pk_mul_f32 v[88:89], v[88:89], s[22:23]
v_pk_mul_f32 v[90:91], v[90:91], s[22:23]
v_pk_mul_f32 v[92:93], v[92:93], s[22:23]
v_pk_mul_f32 v[94:95], v[94:95], s[22:23]
v_pk_mul_f32 v[120:121], v[120:121], s[22:23]
v_pk_mul_f32 v[122:123], v[122:123], s[22:23]
v_pk_mul_f32 v[124:125], v[124:125], s[22:23]
v_pk_mul_f32 v[126:127], v[126:127], s[22:23]
v_pk_mul_f32 v[152:153], v[152:153], s[22:23]
v_pk_mul_f32 v[154:155], v[154:155], s[22:23]
v_pk_mul_f32 v[156:157], v[156:157], s[22:23]
v_pk_mul_f32 v[158:159], v[158:159], s[22:23]
K 32,32,33
K 33,34,35
K 34,36,37
K 35,38,39
K 40,40,41
K 41,42,43
K 42,44,45
K 43,46,47
W 0
Zvav 196,128,32
s_add_u32 s36, s29, 65536
Ra 144,242,4096
Ra 148,242,5120
Ra 152,242,6144
Ra 156,242,7168
Mvav 196,132,40
K 64,64,65
K 65,66,67
K 66,68,69
K 67,70,71
K 72,72,73
K 73,74,75
K 74,76,77
K 75,78,79
s_nop 1
Zvav 204,132,72
buffer_store_dwordx4 a[192:195], v1, s[8:11], s36 offen offset:0 sc1
K 96,96,97
K 97,98,99
K 98,100,101
Mvav 204,128,64
K 99,102,103
K 104,104,105
K 105,106,107
K 106,108,109
K 107,110,111
Zvav 212,128,96
buffer_store_dwordx4 a[200:203], v1, s[8:11], s36 offen offset:1024 sc1
K 128,128,129
K 129,130,131
K 130,132,133
Mvav 212,132,104
K 131,134,135
K 136,136,137
K 137,138,139
K 138,140,141
K 139,142,143
s_nop 1
Zvav 220,132,136
K 48,48,49
K 49,50,51
K 50,52,53
K 51,54,55
Mvav 220,128,128
K 56,56,57
K 57,58,59
K 58,60,61
K 59,62,63
Mvav 196,136,48
buffer_store_dwordx4 a[208:211], v1, s[8:11], s36 offen offset:2048 sc1
Ra 128,242,8192
Ra 132,242,9216
K 80,80,81
Mvav 196,140,56
K 81,82,83
K 82,84,85
K 83,86,87
K 88,88,89
K 89,90,91
K 90,92,93
K 91,94,95
s_nop 1
Mvav 204,140,88
buffer_store_dwordx4 a[216:219], v1, s[8:11], s36 offen offset:3072 sc1
s_add_u32 s36, s29, 69632
K 112,112,113
K 113,114,115
K 114,116,117
Mvav 204,136,80
K 115,118,119
K 120,120,121
K 121,122,123
K 122,124,125
K 123,126,127
Mvav 212,136,112
K 144,144,145
K 145,146,147
K 146,148,149
K 147,150,151
Mvav 212,140,120
K 152,152,153
K 153,154,155
K 154,156,157
K 155,158,159
s_nop 1
Mvav 220,140,152
buffer_store_dwordx4 a[224:227], v1, s[8:11], s36 offen offset:0 sc1
v_accvgpr_write_b32 a0, v32
v_accvgpr_write_b32 a1, v33
v_accvgpr_write_b32 a2, v34
Mvav 220,136,144
W 2
Ra 136,242,10240
v_accvgpr_write_b32 a3, v35
v_accvgpr_write_b32 a4, v40
Zvav 36,144,32
Ra 140,242,11264
buffer_store_dwordx4 a[232:235], v1, s[8:11], s36 offen offset:1024 sc1
v_accvgpr_write_b32 a5, v41
Mvav 36,148,40
v_accvgpr_write_b32 a6, v42
v_accvgpr_write_b32 a7, v43
v_max3_f32 v4, v196, v197, v198
v_max3_f32 v4, v4, v199, v252
Zvav 68,148,72
v_max3_f32 v5, v204, v205, v206
v_max3_f32 v5, v5, v207, v253
v_max3_f32 v6, v212, v213, v214
v_max3_f32 v6, v6, v215, v254
Mvav 68,144,64
v_max3_f32 v7, v220, v221, v222
v_max3_f32 v7, v7, v223, v255
v_mov_b32 v8, v4
v_mov_b32 v9, v5
Zvav 100,144,96
v_mov_b32 v10, v6
v_mov_b32 v11, v7
v_permlane32_swap_b32 v4, v8
v_accvgpr_write_b32 a8, v48
Mvav 100,148,104
v_permlane32_swap_b32 v5, v9
v_permlane32_swap_b32 v6, v10
v_permlane32_swap_b32 v7, v11
v_max_f32 v4, v4, v8
Zvav 132,148,136
v_max_f32 v5, v5, v9
v_max_f32 v6, v6, v10
v_max_f32 v7, v7, v11
v_mov_b32 v8, v4
Mvav 132,144,128
v_mov_b32 v9, v5
v_mov_b32 v10, v6
v_mov_b32 v11, v7
v_accvgpr_write_b32 a9, v49
Mvav 36,152,48
v_permlane16_swap_b32 v4, v8
v_permlane16_swap_b32 v5, v9
v_permlane16_swap_b32 v6, v10
v_permlane16_swap_b32 v7, v11
Mvav 36,156,56
v_max_f32 v4, v4, v8
v_max_f32 v5, v5, v9
v_max_f32 v6, v6, v10
v_max_f32 v7, v7, v11
Mvav 68,156,88
v_sub_f32 v4, 0, v4
v_sub_f32 v5, 0, v5
v_sub_f32 v6, 0, v6
v_sub_f32 v7, 0, v7
Mvav 68,152,80
v_mov_b32 v224, v4
v_mov_b32 v225, v4
v_mov_b32 v226, v4
v_mov_b32 v227, v4
Mvav 100,152,112
v_mov_b32 v228, v5
v_mov_b32 v229, v5
v_mov_b32 v230, v5
v_mov_b32 v231, v5
Mvav 100,156,120
v_mov_b32 v232, v6
v_mov_b32 v233, v6
v_mov_b32 v234, v6
v_mov_b32 v235, v6
Mvav 132,156,152
v_mov_b32 v236, v7
v_mov_b32 v237, v7
v_mov_b32 v238, v7
v_mov_b32 v239, v7
Mvav 132,152,144
W 0
buffer_store_dwordx4 a[240:243], v1, s[8:11], s36 offen offset:2048 sc1
v_accvgpr_write_b32 a10, v50
v_accvgpr_write_b32 a11, v51
v_accvgpr_write_b32 a12, v56
)"
R"(Cvav 160,128,32,224
s_and_b32 s80, s14, 1
v_accvgpr_write_b32 a13, v57
v_accvgpr_write_b32 a14, v58
v_accvgpr_write_b32 a15, v59
v_accvgpr_write_b32 a16, v64
Mvav 160,132,40
buffer_store_dwordx4 a[248:251], v1, s[8:11], s36 offen offset:3072 sc1
s_add_u32 s36, s29, 393216
Ra 144,242,12288
Ra 148,242,13312
v_accvgpr_write_b32 a17, v65
Cvav 164,132,72,228
s_lshl_b32 s80, s80, 14
v_accvgpr_write_b32 a18, v66
v_accvgpr_write_b32 a19, v67
v_accvgpr_write_b32 a20, v72
v_accvgpr_write_b32 a21, v73
Mvav 164,128,64
s_add_u32 s80, s80, 98304
buffer_store_dwordx4 a[196:199], v1, s[8:11], s36 offen offset:0 sc1
v_accvgpr_write_b32 a22, v74
v_accvgpr_write_b32 a23, v75
v_accvgpr_write_b32 a24, v80
Cvav 168,128,96,232
v_accvgpr_write_b32 a25, v81
v_accvgpr_write_b32 a26, v82
A 196,196,224
v_accvgpr_write_b32 a27, v83
Mvav 168,132,104
buffer_store_dwordx4 a[204:207], v1, s[8:11], s36 offen offset:1024 sc1
A 197,197,224
A 198,198,224
v_accvgpr_write_b32 a28, v88
Cvav 172,132,136,236
Ra 152,242,14336
Ra 156,242,15360
A 199,199,224
v_accvgpr_write_b32 a29, v89
Mvav 172,128,128
buffer_store_dwordx4 a[212:215], v1, s[8:11], s36 offen offset:2048 sc1
X 196
v_accvgpr_write_b32 a30, v90
Mvav 160,136,48
X 197
v_accvgpr_write_b32 a31, v91
v_accvgpr_write_b32 a32, v96
Mvav 160,140,56
X 198
K 192,196,197
v_accvgpr_write_b32 a33, v97
Mvav 164,140,88
X 199
A 204,204,228
v_accvgpr_write_b32 a34, v98
Mvav 164,136,80
buffer_store_dwordx4 a[220:223], v1, s[8:11], s36 offen offset:3072 sc1
s_add_u32 s36, s29, 397312
K 193,198,199
v_accvgpr_write_b32 a35, v99
A 205,205,228
Mvav 168,136,112
A 206,206,228
v_accvgpr_write_b32 a36, v104
v_add_u32 v8, s80, v1
A 207,207,228
Mvav 168,140,120
X 204
v_accvgpr_write_b32 a37, v105
v_accvgpr_write_b32 a38, v106
Mvav 172,140,152
X 205
X 206
Mvav 172,136,144
W 0
buffer_store_dwordx4 a[228:231], v1, s[8:11], s36 offen offset:0 sc1
v_accvgpr_write_b32 a39, v107
v_accvgpr_write_b32 a40, v112
K 200,204,205
Cvav 176,144,32,224
Ra 128,242,24576
X 207
v_accvgpr_write_b32 a41, v113
Mvav 176,148,40
Ra 132,242,25600
A 212,212,232
v_accvgpr_write_b32 a42, v114
K 201,206,207
Cvav 180,148,72,228
A 213,213,232
v_accvgpr_write_b32 a43, v115
A 214,214,232
A 215,215,232
Mvav 180,144,64
buffer_store_dwordx4 a[236:239], v1, s[8:11], s36 offen offset:1024 sc1
v_accvgpr_write_b32 a44, v120
X 212
Cvav 184,144,96,232
v_accvgpr_write_b32 a45, v121
X 213
v_accvgpr_write_b32 a46, v122
Mvav 184,148,104
X 214
v_accvgpr_write_b32 a47, v123
K 208,212,213
Cvav 188,148,136,236
buffer_store_dwordx4 a[244:247], v1, s[8:11], s36 offen offset:2048 sc1
v_accvgpr_write_b32 a48, v128
X 215
Mvav 188,144,128
v_accvgpr_write_b32 a49, v129
A 220,220,236
v_accvgpr_write_b32 a50, v130
K 209,214,215
Mvav 176,152,48
A 221,221,236
v_accvgpr_write_b32 a51, v131
A 222,222,236
A 223,223,236
Mvav 176,156,56
Ra 136,242,26624
v_accvgpr_write_b32 a52, v136
X 220
Mvav 180,156,88
Ra 140,242,27648
v_accvgpr_write_b32 a53, v137
X 221
Mvav 180,152,80
buffer_store_dwordx4 a[252:255], v1, s[8:11], s36 offen offset:3072 sc1
v_accvgpr_write_b32 a54, v138
X 222
Mvav 184,152,112
v_accvgpr_write_b32 a55, v139
v_accvgpr_write_b32 a56, v144
v_accvgpr_write_b32 a57, v145
v_accvgpr_write_b32 a58, v146
v_accvgpr_write_b32 a59, v147
v_accvgpr_write_b32 a60, v152
v_accvgpr_write_b32 a61, v153
v_accvgpr_write_b32 a62, v154
v_accvgpr_write_b32 a63, v155
Mvav 184,156,120
s_barrier
K 216,220,221
X 223
A 36,36,224
Mvav 188,156,152
K 217,222,223
A 37,37,224
A 38,38,224
A 39,39,224
X 36
X 37
X 38
K 194,36,37
X 39
A 68,68,228
K 195,38,39
A 69,69,228
A 70,70,228
A 71,71,228
X 68
X 69
X 70
K 202,68,69
X 71
A 100,100,232
K 203,70,71
A 101,101,232
A 102,102,232
A 103,103,232
X 100
X 101
X 102
K 210,100,101
X 103
A 132,132,236
K 211,102,103
A 133,133,236
A 134,134,236
A 135,135,236
X 132
X 133
X 134
K 218,132,133
X 135
s_nop 0
K 219,134,135
Mvav 188,152,144
W 0
Ra 144,242,28672
Ra 148,242,29696
Ra 152,242,30720
Zvav 32,128,192
Ra 156,242,31744
Zvav 64,128,200
Zvav 96,128,208
s_and_b32 s80, s14, 1
s_xor_b32 s62, s62, 0x10000
Zvav 128,128,216
s_lshl_b32 s80, s80, 14
s_add_u32 s80, s80, 131072
v_add_u32 v23, 61440, v21
Zvav 132,132,216
v_add_u32 v12, s80, v1
Ra 64,12,8192
Ra 68,12,9216
v_add_u32 v24, 61440, v22
Zvav 100,132,208
Ra 72,12,10240
Ra 76,12,11264
Ra 80,12,12288
v_add_u32 v25, 122880, v21
Zvav 68,132,200
Ra 84,12,13312
Ra 88,12,14336
Ra 92,12,15360
v_add_u32 v26, 122880, v22
Zvav 36,132,192
Ra 96,12,0
Ra 100,12,1024
Ra 104,12,2048
Zvav 40,136,192
Ra 108,12,3072
Ra 112,12,4096
X 172
Zvav 72,136,200
Ra 116,12,5120
Ra 120,12,6144
X 173
Zvav 104,136,208
Ra 124,12,7168
X 174
K 220,172,173
Zvav 136,136,216
X 175
Zvav 140,140,216
X 168
K 221,174,175
Zvav 108,140,208
X 169
X 170
Zvav 76,140,200
s_waitcnt vmcnt(0) lgkmcnt(0)
s_barrier
v_xor_b32 v242, 0x10000, v242
ds_read_b128 v[248:251], v8
Zvav 44,140,192
s_add_u32 s80, s71, 128
v_mov_b64 v[28:29], s[12:13]
s_mov_b64 exec, 1
buffer_store_dwordx2 v[28:29], off, s[8:11], s80 sc1
s_mov_b64 exec, -1
Ra 128,242,0
K 212,168,169
X 171
Zvav 48,144,192
X 164
K 213,170,171
Zvav 80,144,200
X 165
X 166
Zvav 112,144,208
Ra 132,242,1024
K 204,164,165
X 167
Zvav 144,144,216
X 160
Zvav 148,148,216
K 205,166,167
X 161
Zvav 116,148,208
Ra 136,242,2048
X 162
Zvav 84,148,200
W 3
K 196,160,161
X 163
Zvav 52,148,192
X 176
K 197,162,163
Zvav 56,152,192
Ra 140,242,3072
X 177
Zvav 88,152,200
X 178
K 198,176,177
Zvav 120,152,208
X 179
X 180
Zvav 152,152,216
K 199,178,179
Zvav 156,156,216
X 181
X 182
Zvav 124,156,208
K 206,180,181
X 183
Zvav 92,156,200
K 207,182,183
Zvav 60,156,192
.Ll0t_46%=:
s_cmp_eq_u32 s64, s65
s_cbranch_scc1 .Ll1t_35%=
.Ll0_33%=:
W 0
Cvaa 160,128,0,224
Ra 144,242,4096
Ra 148,242,5120
Mvaa 160,132,4
s_xor_b32 s62, s62, 0x10000
Ra 152,242,6144
Cvaa 164,132,20,228
X 184
Mvaa 164,128,16
X 185
Cvaa 168,128,32,232
X 186
Mvaa 168,132,36
Ra 156,242,7168
K 214,184,185
Cvaa 172,132,52,236
X 187
Mvaa 172,128,48
X 188
Mvaa 160,136,8
Ra 128,242,16384
Mvaa 160,140,12
K 215,186,187
Mvaa 164,140,28
X 189
Mvaa 164,136,24
X 190
Mvaa 168,136,40
Ra 132,242,17408
Mvaa 168,140,44
K 222,188,189
Mvaa 172,140,60
X 191
Mvaa 172,136,56
K 223,190,191
v_smfmac_f32_16x16x64_bf16 a[160:163], a[176:179], v[192:199], v240
Ra 136,242,18432
v_smfmac_f32_16x16x64_bf16 a[164:167], a[176:179], v[200:207], v240
X 160
v_smfmac_f32_16x16x64_bf16 a[168:171], a[176:179], v[208:215], v240
X 161
v_smfmac_f32_16x16x64_bf16 a[172:175], a[176:179], v[216:223], v240
W 3
X 162
Cvaa 176,144,0,224
Ra 140,242,19456
Mvaa 176,148,4
K 192,160,161
Cvaa 180,148,20,228
X 163
Mvaa 180,144,16
X 164
Cvaa 184,144,32,232
Mvaa 184,148,36
K 193,162,163
Cvaa 188,148,52,236
X 165
Mvaa 188,144,48
X 166
Mvaa 176,152,8
Ra 144,242,20480
Ra 148,242,21504
Mvaa 176,156,12
K 200,164,165
Mvaa 180,156,28
X 167
Mvaa 180,152,24
X 168
Mvaa 184,152,40
Mvaa 184,156,44
K 201,166,167
Mvaa 188,156,60
X 169
Mvaa 188,152,56
W 2
Ra 152,242,22528
Mvav 32,128,196
X 170
Mvav 64,128,204
Ra 156,242,23552
K 208,168,169
Mvav 96,128,212
X 171
Mvav 128,128,220
Mvav 132,132,220
X 172
Mvav 100,132,212
s_add_u32 s64, s64, 1
Ra 128,242,8192
K 209,170,171
Mvav 68,132,204
X 173
Mvav 36,132,196
Mvav 40,136,196
)"
R"(X 174
Mvav 72,136,204
Ra 132,242,9216
K 216,172,173
Mvav 104,136,212
X 175
Mvav 136,136,220
Mvav 140,140,220
X 176
Mvav 108,140,212
Ra 136,242,10240
K 217,174,175
Mvav 76,140,204
X 177
Mvav 44,140,196
W 3
Mvav 48,144,196
X 178
Mvav 80,144,204
Ra 140,242,11264
Mvav 112,144,212
K 194,176,177
Mvav 144,144,220
X 179
Mvav 148,148,220
X 180
Mvav 116,148,212
Ra 144,242,12288
Mvav 84,148,204
K 195,178,179
Mvav 52,148,196
X 181
Mvav 56,152,196
X 182
Mvav 88,152,204
Ra 148,242,13312
Mvav 120,152,212
K 202,180,181
Mvav 152,152,220
X 183
Mvav 156,156,220
X 184
Mvav 124,156,212
Ra 152,242,14336
Mvav 92,156,204
K 203,182,183
Mvav 60,156,196
W 3
X 185
Cvaa 160,128,0,224
X 186
Mvaa 160,132,4
Ra 156,242,15360
Cvaa 164,132,20,228
K 210,184,185
Mvaa 164,128,16
X 187
Cvaa 168,128,32,232
X 188
Mvaa 168,132,36
Cvaa 172,132,52,236
K 211,186,187
Mvaa 172,128,48
X 189
Mvaa 160,136,8
Ra 128,242,24576
Ra 132,242,25600
Mvaa 160,140,12
X 190
Mvaa 164,140,28
K 218,188,189
Mvaa 164,136,24
X 191
Mvaa 168,136,40
Mvaa 168,140,44
Mvaa 172,140,60
X 160
Mvaa 172,136,56
W 2
Ra 136,242,26624
K 219,190,191
Cvaa 176,144,0,224
X 161
Mvaa 176,148,4
Ra 140,242,27648
Cvaa 180,148,20,228
X 162
Mvaa 180,144,16
K 196,160,161
Cvaa 184,144,32,232
X 163
Mvaa 184,148,36
X 164
Cvaa 188,148,52,236
K 197,162,163
Mvaa 188,144,48
X 165
Mvaa 176,152,8
Ra 144,242,28672
Ra 148,242,29696
Mvaa 176,156,12
X 166
Mvaa 180,156,28
K 204,164,165
Mvaa 180,152,24
X 167
Mvaa 184,152,40
Mvaa 184,156,44
X 168
Mvaa 188,156,60
Mvaa 188,152,56
W 2
Ra 152,242,30720
K 205,166,167
Mvav 32,128,192
X 169
Mvav 64,128,200
Ra 156,242,31744
Mvav 96,128,208
X 170
Mvav 128,128,216
K 212,168,169
Mvav 132,132,216
X 171
Mvav 100,132,208
X 172
Mvav 68,132,200
Mvav 36,132,192
K 213,170,171
Mvav 40,136,192
X 173
Mvav 72,136,200
X 174
Mvav 104,136,208
Mvav 136,136,216
K 220,172,173
Mvav 140,140,216
X 175
Mvav 108,140,208
X 176
Mvav 76,140,200
W 0
s_barrier
v_xor_b32 v242, 0x10000, v242
Mvav 44,140,192
Ra 128,242,0
K 221,174,175
Mvav 48,144,192
X 177
Mvav 80,144,200
X 178
Mvav 112,144,208
Ra 132,242,1024
Mvav 144,144,216
K 198,176,177
Mvav 148,148,216
X 179
Mvav 116,148,208
X 180
Mvav 84,148,200
Ra 136,242,2048
Mvav 52,148,192
K 199,178,179
Mvav 56,152,192
X 181
Mvav 88,152,200
Ra 140,242,3072
Mvav 120,152,208
X 182
Mvav 152,152,216
K 206,180,181
Mvav 156,156,216
X 183
Mvav 124,156,208
Mvav 92,156,200
K 207,182,183
Mvav 60,156,192
s_cmp_lg_u32 s64, s65
s_cbranch_scc1 .Ll0_33%=
s_cmp_lt_u32 s85, 54
s_cbranch_scc1 .Lmoe_44%=
.Ll1t_35%=:
s_cmp_eq_u32 s64, s66
s_cbranch_scc1 .Lsp1_36%=
.Ll1_34%=:
W 0
Cvaa 160,128,0,224
Ra 144,242,4096
Ra 148,242,5120
Mvaa 160,132,4
s_add_u32 m0, s62, 0
Ra 152,242,6144
Cvaa 164,132,20,228
X 184
Mvaa 164,128,16
X 185
Cvaa 168,128,32,232
X 186
Mvaa 168,132,36
Ra 156,242,7168
K 214,184,185
Cvaa 172,132,52,236
Dc 1,8,61,0
Mvaa 172,128,48
X 187
Mvaa 160,136,8
X 188
Mvaa 160,140,12
Ra 128,242,16384
K 215,186,187
Mvaa 164,140,28
Dc 1,8,61,1024
Mvaa 164,136,24
X 189
Mvaa 168,136,40
X 190
Mvaa 168,140,44
Ra 132,242,17408
K 222,188,189
Mvaa 172,140,60
Dc 1,8,61,2048
Mvaa 172,136,56
X 191
v_smfmac_f32_16x16x64_bf16 a[160:163], a[176:179], v[192:199], v240
Ra 136,242,18432
K 223,190,191
v_smfmac_f32_16x16x64_bf16 a[164:167], a[176:179], v[200:207], v240
X 160
v_smfmac_f32_16x16x64_bf16 a[168:171], a[176:179], v[208:215], v240
Dc 1,8,61,3072
v_smfmac_f32_16x16x64_bf16 a[172:175], a[176:179], v[216:223], v240
W 3
X 161
Cvaa 176,144,0,224
s_add_u32 m0, s62, 4096
X 162
Mvaa 176,148,4
Ra 140,242,19456
K 192,160,161
Cvaa 180,148,20,228
X 163
Mvaa 180,144,16
Dc 243,8,61,0
Cvaa 184,144,32,232
X 164
Mvaa 184,148,36
K 193,162,163
Cvaa 188,148,52,236
X 165
Mvaa 188,144,48
Dc 243,8,61,1024
Ra 144,242,20480
Mvaa 176,152,8
X 166
Mvaa 176,156,12
Ra 148,242,21504
K 200,164,165
Mvaa 180,156,28
X 167
Mvaa 180,152,24
Dc 243,8,61,2048
Mvaa 184,152,40
X 168
Mvaa 184,156,44
K 201,166,167
Mvaa 188,156,60
X 169
Mvaa 188,152,56
W 2
Dc 243,8,61,3072
Ra 152,242,22528
Mvav 32,128,196
s_add_u32 m0, s62, 8192
X 170
Mvav 64,128,204
Ra 156,242,23552
K 208,168,169
Mvav 96,128,212
X 171
Mvav 128,128,220
Dc 244,8,61,0
Mvav 132,132,220
X 172
Mvav 100,132,212
Ra 128,242,8192
K 209,170,171
Mvav 68,132,204
X 173
Mvav 36,132,196
Dc 244,8,61,1024
Mvav 40,136,196
X 174
Mvav 72,136,204
Ra 132,242,9216
K 216,172,173
Mvav 104,136,212
X 175
Mvav 136,136,220
Dc 244,8,61,2048
Mvav 140,140,220
X 176
Mvav 108,140,212
Ra 136,242,10240
K 217,174,175
Mvav 76,140,204
X 177
Mvav 44,140,196
W 3
Dc 244,8,61,3072
Mvav 48,144,196
X 178
Mvav 80,144,204
s_add_u32 m0, s62, 12288
Ra 140,242,11264
Mvav 112,144,212
K 194,176,177
Mvav 144,144,220
X 179
Mvav 148,148,220
X 180
Mvav 116,148,212
Dc 245,8,61,0
Ra 144,242,12288
Mvav 84,148,204
K 195,178,179
Mvav 52,148,196
X 181
Mvav 56,152,196
X 182
Mvav 88,152,204
Dc 245,8,61,1024
Ra 148,242,13312
Mvav 120,152,212
K 202,180,181
Mvav 152,152,220
X 183
Mvav 156,156,220
X 184
Mvav 124,156,212
Dc 245,8,61,2048
Ra 152,242,14336
Mvav 92,156,204
K 203,182,183
Mvav 60,156,196
W 3
X 185
Cvaa 160,128,0,224
X 186
Mvaa 160,132,4
Dc 245,8,61,3072
Ra 156,242,15360
Cvaa 164,132,20,228
s_add_u32 s61, s61, s63
K 210,184,185
Mvaa 164,128,16
X 187
Cvaa 168,128,32,232
X 188
Mvaa 168,132,36
Cvaa 172,132,52,236
s_xor_b32 s62, s62, 0x10000
K 211,186,187
Mvaa 172,128,48
X 189
Mvaa 160,136,8
Ra 128,242,24576
Ra 132,242,25600
Mvaa 160,140,12
s_add_u32 s64, s64, 1
X 190
Mvaa 164,140,28
K 218,188,189
Mvaa 164,136,24
X 191
Mvaa 168,136,40
Mvaa 168,140,44
X 160
Mvaa 172,140,60
K 219,190,191
Mvaa 172,136,56
W 2
X 161
Cvaa 176,144,0,224
Ra 136,242,26624
Ra 140,242,27648
Mvaa 176,148,4
X 162
Cvaa 180,148,20,228
K 196,160,161
Mvaa 180,144,16
X 163
Cvaa 184,144,32,232
Mvaa 184,148,36
X 164
Cvaa 188,148,52,236
K 197,162,163
Mvaa 188,144,48
X 165
Mvaa 176,152,8
Ra 144,242,28672
)"
R"(Ra 148,242,29696
Mvaa 176,156,12
X 166
Mvaa 180,156,28
K 204,164,165
Mvaa 180,152,24
X 167
Mvaa 184,152,40
Mvaa 184,156,44
X 168
Mvaa 188,156,60
Mvaa 188,152,56
W 2
Ra 152,242,30720
K 205,166,167
Mvav 32,128,192
X 169
Mvav 64,128,200
Ra 156,242,31744
Mvav 96,128,208
X 170
Mvav 128,128,216
K 212,168,169
Mvav 132,132,216
X 171
Mvav 100,132,208
X 172
Mvav 68,132,200
Mvav 36,132,192
K 213,170,171
Mvav 40,136,192
X 173
Mvav 72,136,200
X 174
Mvav 104,136,208
Mvav 136,136,216
K 220,172,173
Mvav 140,140,216
X 175
Mvav 108,140,208
X 176
Mvav 76,140,200
s_waitcnt vmcnt(0) lgkmcnt(0)
s_barrier
v_xor_b32 v242, 0x10000, v242
Mvav 44,140,192
Ra 128,242,0
K 221,174,175
Mvav 48,144,192
X 177
Mvav 80,144,200
X 178
Mvav 112,144,208
Ra 132,242,1024
Mvav 144,144,216
K 198,176,177
Mvav 148,148,216
X 179
Mvav 116,148,208
X 180
Mvav 84,148,200
Ra 136,242,2048
Mvav 52,148,192
K 199,178,179
Mvav 56,152,192
X 181
Mvav 88,152,200
Ra 140,242,3072
Mvav 120,152,208
X 182
Mvav 152,152,216
K 206,180,181
Mvav 156,156,216
X 183
Mvav 124,156,208
Mvav 92,156,200
K 207,182,183
Mvav 60,156,192
s_cmp_lg_u32 s64, s66
s_cbranch_scc1 .Ll1_34%=
s_cmp_ge_u32 s14, 2
s_cbranch_scc1 .Lf1r_47%=
.Lsp1_36%=:
W 0
Cvaa 160,128,0,224
Ra 144,242,4096
Ra 148,242,5120
Mvaa 160,132,4
s_add_u32 m0, s62, 0
Ra 152,242,6144
Cvaa 164,132,20,228
X 184
Mvaa 164,128,16
X 185
Cvaa 168,128,32,232
X 186
Mvaa 168,132,36
Ra 156,242,7168
K 214,184,185
Cvaa 172,132,52,236
Dc 1,8,61,0
Mvaa 172,128,48
X 187
Mvaa 160,136,8
X 188
Mvaa 160,140,12
Ra 128,242,16384
K 215,186,187
Mvaa 164,140,28
Dc 1,8,61,1024
Ra 132,242,17408
Mvaa 164,136,24
X 189
Mvaa 168,136,40
X 190
Mvaa 168,140,44
Dc 1,8,61,2048
K 222,188,189
Mvaa 172,140,60
X 191
Mvaa 172,136,56
Ra 136,242,18432
K 223,190,191
v_smfmac_f32_16x16x64_bf16 a[160:163], a[176:179], v[192:199], v240
Dc 1,8,61,3072
Ra 140,242,19456
v_smfmac_f32_16x16x64_bf16 a[164:167], a[176:179], v[200:207], v240
s_add_u32 m0, s62, 4096
v_cndmask_b32_e64 v160, v241, v160, s[72:73]
v_cndmask_b32_e64 v161, v241, v161, s[74:75]
v_smfmac_f32_16x16x64_bf16 a[168:171], a[176:179], v[208:215], v240
v_cndmask_b32_e64 v162, v241, v162, s[76:77]
v_cndmask_b32_e64 v163, v241, v163, s[78:79]
v_smfmac_f32_16x16x64_bf16 a[172:175], a[176:179], v[216:223], v240
W 4
X 160
Cvaa 180,144,16,228
X 161
Mvaa 180,148,20
Dc 243,8,61,0
Cvaa 184,148,36,232
X 162
Mvaa 184,144,32
K 192,160,161
Cvaa 188,144,48,236
X 163
Mvaa 188,148,52
Dc 243,8,61,1024
Ra 144,242,20480
Mvaa 180,152,24
X 164
Mvaa 180,156,28
Ra 148,242,21504
K 193,162,163
Mvaa 184,156,44
Dc 243,8,61,2048
Mvaa 184,152,40
X 165
Mvaa 188,152,56
X 166
Mvaa 188,156,60
W 2
Dc 243,8,61,3072
K 200,164,165
Mvav 32,128,196
s_add_u32 m0, s62, 8192
X 167
Mvav 64,128,204
Ra 152,242,22528
Ra 156,242,23552
Mvav 96,128,212
X 168
Mvav 128,128,220
Dc 244,8,61,0
K 201,166,167
Mvav 132,132,220
X 169
Mvav 100,132,212
X 170
Mvav 68,132,204
Ra 128,242,8192
K 208,168,169
Mvav 36,132,196
X 171
Mvav 40,136,196
Dc 244,8,61,1024
Ra 132,242,9216
Mvav 72,136,204
X 172
Mvav 104,136,212
K 209,170,171
Mvav 136,136,220
X 173
Mvav 140,140,220
Dc 244,8,61,2048
Ra 136,242,10240
Mvav 108,140,212
X 174
Mvav 76,140,204
K 216,172,173
Mvav 44,140,196
W 3
Dc 244,8,61,3072
Ra 140,242,11264
Mvav 48,144,196
s_add_u32 m0, s62, 12288
X 175
Mvav 80,144,204
v_mov_b32 v194, 0
K 217,174,175
Mvav 112,144,212
v_mov_b32 v195, 0
v_cndmask_b32_e64 v180, v241, v180, s[72:73]
Mvav 144,144,220
v_cndmask_b32_e64 v181, v241, v181, s[74:75]
v_cndmask_b32_e64 v182, v241, v182, s[76:77]
Mvav 148,148,220
Dc 245,8,61,0
v_cndmask_b32_e64 v183, v241, v183, s[78:79]
Mvav 116,148,212
X 180
Mvav 84,148,204
Ra 144,242,12288
Mvav 52,148,196
X 181
Mvav 56,152,196
Dc 245,8,61,1024
Ra 148,242,13312
Mvav 88,152,204
X 182
Mvav 120,152,212
K 202,180,181
Mvav 152,152,220
X 183
Mvav 156,156,220
Dc 245,8,61,2048
Ra 152,242,14336
Mvav 124,156,212
X 184
Mvav 92,156,204
K 203,182,183
Mvav 60,156,196
W 3
Dc 245,8,61,3072
Ra 156,242,15360
Cvaa 168,128,32,232
s_add_u32 s61, s61, s63
X 185
Mvaa 168,132,36
s_xor_b32 s62, s62, 0x10000
X 186
Cvaa 172,132,52,236
K 210,184,185
Mvaa 172,128,48
s_add_u32 s64, s64, 1
X 187
Mvaa 168,136,40
X 188
Mvaa 168,140,44
Ra 128,242,24576
K 211,186,187
Mvaa 172,140,60
Ra 132,242,25600
Mvaa 172,136,56
Ra 136,242,26624
Ra 140,242,27648
X 189
X 190
K 218,188,189
X 191
v_mov_b32 v196, 0
K 219,190,191
W 4
Cvaa 188,144,48,236
Ra 144,242,28672
Mvaa 188,148,52
v_mov_b32 v197, 0
v_mov_b32 v204, 0
Mvaa 188,152,56
Ra 148,242,29696
v_mov_b32 v205, 0
Mvaa 188,156,60
W 2
Ra 152,242,30720
v_cndmask_b32_e64 v168, v241, v168, s[72:73]
Mvav 32,128,192
v_cndmask_b32_e64 v169, v241, v169, s[74:75]
v_cndmask_b32_e64 v170, v241, v170, s[76:77]
Mvav 64,128,200
Ra 156,242,31744
v_cndmask_b32_e64 v171, v241, v171, s[78:79]
Mvav 96,128,208
X 168
Mvav 128,128,216
X 169
Mvav 132,132,216
X 170
Mvav 100,132,208
v_mov_b32 v224, v250
Mvav 68,132,200
K 212,168,169
v_mov_b32 v225, v250
Mvav 36,132,192
X 171
Mvav 40,136,192
v_mov_b32 v226, v250
Mvav 72,136,200
X 172
Mvav 104,136,208
v_mov_b32 v227, v250
K 213,170,171
Mvav 136,136,216
X 173
Mvav 140,140,216
v_mov_b32 v228, v251
v_mov_b32 v229, v251
Mvav 108,140,208
X 174
Mvav 76,140,200
K 220,172,173
v_mov_b32 v230, v251
Mvav 44,140,192
W 0
X 175
Mvav 48,144,192
v_mov_b32 v231, v251
v_mov_b32 v198, 0
Mvav 80,144,200
K 221,174,175
v_mov_b32 v232, v248
Mvav 112,144,208
v_mov_b32 v199, 0
v_mov_b32 v206, 0
Mvav 144,144,216
v_mov_b32 v233, v248
v_mov_b32 v207, 0
Mvav 148,148,216
v_mov_b32 v214, 0
v_mov_b32 v234, v248
Mvav 116,148,208
v_mov_b32 v215, 0
v_cndmask_b32_e64 v188, v241, v188, s[72:73]
Mvav 84,148,200
v_mov_b32 v235, v248
v_cndmask_b32_e64 v189, v241, v189, s[74:75]
Mvav 52,148,192
v_cndmask_b32_e64 v190, v241, v190, s[76:77]
v_mov_b32 v236, v249
Mvav 56,152,192
v_cndmask_b32_e64 v191, v241, v191, s[78:79]
v_mov_b32 v237, v249
Mvav 88,152,200
X 188
Mvav 120,152,208
X 189
Mvav 152,152,216
v_mov_b32 v238, v249
v_mov_b32 v239, v249
Mvav 156,156,216
X 190
Mvav 124,156,208
s_waitcnt vmcnt(0)
s_barrier
v_xor_b32 v242, 0x10000, v242
Mvav 92,156,200
Ra 128,242,40960
K 222,188,189
Mvav 60,156,192
Ra 132,242,41984
Ra 136,242,43008
Ra 140,242,44032
X 191
s_nop 0
K 223,190,191
s_cmp_eq_u32 s64, 15
s_cbranch_scc1 .Lfs_42%=
.Lsp2_37%=:
v_smfmac_f32_16x16x64_bf16 a[160:163], a[176:179], v[192:199], v240
s_add_u32 m0, s62, 0
Ra 144,242,45056
Ra 148,242,46080
Ra 152,242,47104
Ra 156,242,48128
v_smfmac_f32_16x16x64_bf16 a[164:167], a[176:179], v[200:207], v240
v_smfmac_f32_16x16x64_bf16 a[168:171], a[176:179], v[208:215], v240
v_smfmac_f32_16x16x64_bf16 a[172:175], a[176:179], v[216:223], v240
W 4
Dc 1,8,61,0
Cvaa 168,128,96,232
v_mov_b32 v192, 0
G 12,160
Mvaa 168,132,100
Dc 1,8,61,1024
G 4,161
A 12,12,4
v_cmp_nlt_f32_e64 s[86:87], v12, s90
v_mov_b32 v193, 0
Cvaa 172,132,116,236
v_rcp_f32 v12, v12
s_or_b64 s[88:89], s[88:89], s[86:87]
)"
R"(v_accvgpr_write_b32 a160, 0
v_mov_b32 v200, 0
v_accvgpr_write_b32 a161, 0
v_accvgpr_write_b32 a162, 0
Mvaa 172,128,112
Dc 1,8,61,2048
v_accvgpr_write_b32 a163, 0
G 13,164
v_mov_b32 v201, 0
G 5,165
A 13,13,5
Mvaa 168,136,104
Ra 128,242,16384
Ra 132,242,17408
v_cmp_nlt_f32_e64 s[86:87], v13, s90
v_rcp_f32 v13, v13
s_or_b64 s[88:89], s[88:89], s[86:87]
v_accvgpr_write_b32 a164, 0
Mvaa 168,140,108
Dc 1,8,61,3072
v_accvgpr_write_b32 a165, 0
v_accvgpr_write_b32 a166, 0
v_accvgpr_write_b32 a167, 0
U 32,32,12
U 33,33,12
Mvaa 172,140,124
U 34,34,12
U 35,35,12
U 36,36,12
U 37,37,12
U 38,38,12
U 39,39,12
Mvaa 172,136,120
s_add_u32 m0, s62, 4096
Ra 136,242,18432
Ra 140,242,19456
K 32,32,33
K 33,34,35
K 34,36,37
W 4
Cvaa 188,144,112,236
K 35,38,39
s_add_u32 s93, s67, 0
buffer_store_dwordx4 v[32:35], v1, s[8:11], s93 offen offset:0 sc1
U 40,40,12
U 41,41,12
U 42,42,12
U 43,43,12
Mvaa 188,148,116
Ra 144,242,20480
v_cndmask_b32_e64 v168, v241, v168, s[72:73]
v_cndmask_b32_e64 v169, v241, v169, s[74:75]
v_cndmask_b32_e64 v170, v241, v170, s[76:77]
v_cndmask_b32_e64 v171, v241, v171, s[78:79]
U 44,44,12
Mvaa 188,152,120
X 168
X 169
U 45,45,12
U 46,46,12
Mvaa 188,156,124
W 1
Ra 148,242,21504
Dc 243,8,61,0
U 47,47,12
K 40,40,41
K 41,42,43
K 42,44,45
Mvav 96,128,212
Ra 152,242,22528
Ra 156,242,23552
K 43,46,47
X 170
K 208,168,169
Mvav 128,128,220
buffer_store_dwordx4 v[40:43], v1, s[8:11], s93 offen offset:1024 sc1
U 48,48,12
U 49,49,12
U 50,50,12
U 51,51,12
U 52,52,12
Mvav 132,132,220
U 53,53,12
U 54,54,12
U 55,55,12
K 48,48,49
K 49,50,51
K 50,52,53
Mvav 100,132,212
Dc 243,8,61,1024
Ra 128,242,32768
Ra 132,242,33792
X 171
K 51,54,55
Mvav 104,136,212
X 172
K 209,170,171
X 173
Mvav 136,136,220
buffer_store_dwordx4 v[48:51], v1, s[8:11], s93 offen offset:2048 sc1
U 56,56,12
U 57,57,12
U 58,58,12
U 59,59,12
U 60,60,12
Mvav 140,140,220
U 61,61,12
U 62,62,12
U 63,63,12
K 56,56,57
K 57,58,59
K 58,60,61
Mvav 108,140,212
W 2
Dc 243,8,61,2048
Ra 136,242,34816
Ra 140,242,35840
K 59,62,63
X 174
Mvav 112,144,212
K 216,172,173
X 175
Mvav 144,144,220
buffer_store_dwordx4 v[56:59], v1, s[8:11], s93 offen offset:3072 sc1
Ra 144,242,36864
G 14,168
G 6,169
A 14,14,6
v_cmp_nlt_f32_e64 s[86:87], v14, s90
Mvav 148,148,220
v_rcp_f32 v14, v14
s_or_b64 s[88:89], s[88:89], s[86:87]
v_accvgpr_write_b32 a168, 0
v_accvgpr_write_b32 a169, 0
v_accvgpr_write_b32 a170, 0
v_accvgpr_write_b32 a171, 0
Mvav 116,148,212
Dc 243,8,61,3072
s_add_u32 m0, s62, 8192
G 15,172
G 7,173
A 15,15,7
v_cmp_nlt_f32_e64 s[86:87], v15, s90
v_mov_b32 v194, 0
Mvav 120,152,212
v_rcp_f32 v15, v15
s_or_b64 s[88:89], s[88:89], s[86:87]
v_accvgpr_write_b32 a172, 0
v_accvgpr_write_b32 a173, 0
v_accvgpr_write_b32 a174, 0
v_accvgpr_write_b32 a175, 0
Mvav 152,152,220
Dc 244,8,61,0
U 64,64,13
U 65,65,13
U 66,66,13
U 67,67,13
U 68,68,13
Mvav 156,156,220
U 69,69,13
U 70,70,13
U 71,71,13
K 64,64,65
K 65,66,67
K 66,68,69
Mvav 124,156,212
W 1
Dc 244,8,61,1024
Ra 148,242,37888
K 67,70,71
s_add_u32 s93, s67, 131072
K 217,174,175
v_mov_b32 v195, 0
Cvaa 160,128,64,224
Ra 152,242,38912
Ra 156,242,39936
v_mov_b32 v202, 0
v_mov_b32 v203, 0
v_mov_b32 v210, 0
v_mov_b32 v211, 0
Mvaa 160,132,68
buffer_store_dwordx4 v[64:67], v1, s[8:11], s93 offen offset:0 sc1
U 72,72,13
U 73,73,13
U 74,74,13
U 75,75,13
U 76,76,13
Cvaa 164,132,84,228
U 77,77,13
U 78,78,13
U 79,79,13
K 72,72,73
K 73,74,75
K 74,76,77
Mvaa 164,128,80
Dc 244,8,61,2048
K 75,78,79
v_cndmask_b32_e64 v188, v241, v188, s[72:73]
v_cndmask_b32_e64 v189, v241, v189, s[74:75]
v_cndmask_b32_e64 v190, v241, v190, s[76:77]
v_cndmask_b32_e64 v191, v241, v191, s[78:79]
Cvaa 168,128,96,232
X 188
X 189
Mvaa 168,132,100
buffer_store_dwordx4 v[72:75], v1, s[8:11], s93 offen offset:1024 sc1
U 80,80,13
U 81,81,13
U 82,82,13
U 83,83,13
U 84,84,13
Cvaa 172,132,116,236
U 85,85,13
U 86,86,13
U 87,87,13
K 80,80,81
K 81,82,83
K 82,84,85
Mvaa 172,128,112
Dc 244,8,61,3072
s_add_u32 m0, s62, 12288
Ra 128,242,49152
Ra 132,242,50176
K 83,86,87
X 190
Mvaa 160,136,72
K 218,188,189
X 191
Mvaa 160,140,76
buffer_store_dwordx4 v[80:83], v1, s[8:11], s93 offen offset:2048 sc1
U 88,88,13
U 89,89,13
U 90,90,13
U 91,91,13
U 92,92,13
Mvaa 164,140,92
U 93,93,13
U 94,94,13
U 95,95,13
K 88,88,89
K 89,90,91
K 90,92,93
Mvaa 164,136,88
K 91,94,95
buffer_store_dwordx4 v[88:91], v1, s[8:11], s93 offen offset:3072 sc1
U 96,96,14
U 97,97,14
U 98,98,14
U 99,99,14
Mvaa 168,136,104
U 100,100,14
U 101,101,14
U 102,102,14
U 103,103,14
K 96,96,97
K 97,98,99
Mvaa 168,140,108
Dc 245,8,61,0
K 98,100,101
K 99,102,103
s_add_u32 s93, s67, 262144
v_cndmask_b32_e64 v160, v241, v160, s[72:73]
K 219,190,191
v_cndmask_b32_e64 v161, v241, v161, s[74:75]
Mvaa 172,140,124
v_cndmask_b32_e64 v162, v241, v162, s[76:77]
v_cndmask_b32_e64 v163, v241, v163, s[78:79]
X 160
Mvaa 172,136,120
W 2
Dc 245,8,61,1024
Ra 136,242,51200
Ra 140,242,52224
X 161
Cvaa 180,144,80,228
Mvaa 180,148,84
buffer_store_dwordx4 v[96:99], v1, s[8:11], s93 offen offset:0 sc1
U 104,104,14
U 105,105,14
U 106,106,14
U 107,107,14
U 108,108,14
Cvaa 184,148,100,232
U 109,109,14
U 110,110,14
U 111,111,14
K 104,104,105
K 105,106,107
K 106,108,109
Mvaa 184,144,96
Dc 245,8,61,2048
K 107,110,111
X 162
K 196,160,161
Cvaa 188,144,112,236
Ra 144,242,53248
X 163
Mvaa 188,148,116
buffer_store_dwordx4 v[104:107], v1, s[8:11], s93 offen offset:1024 sc1
U 112,112,14
U 113,113,14
U 114,114,14
U 115,115,14
U 116,116,14
Mvaa 180,152,88
U 117,117,14
U 118,118,14
U 119,119,14
K 112,112,113
K 113,114,115
K 114,116,117
Mvaa 180,156,92
Dc 245,8,61,3072
s_add_u32 s61, s61, s63
s_xor_b32 s62, s62, 0x10000
s_add_u32 s64, s64, 1
K 115,118,119
X 164
Mvaa 184,156,108
Ra 148,242,54272
K 197,162,163
X 165
Mvaa 184,152,104
buffer_store_dwordx4 v[112:115], v1, s[8:11], s93 offen offset:2048 sc1
U 120,120,14
U 121,121,14
U 122,122,14
U 123,123,14
U 124,124,14
Mvaa 188,152,120
U 125,125,14
U 126,126,14
U 127,127,14
K 120,120,121
K 121,122,123
K 122,124,125
Mvaa 188,156,124
W 2
K 123,126,127
buffer_store_dwordx4 v[120:123], v1, s[8:11], s93 offen offset:3072 sc1
U 128,128,15
U 129,129,15
U 130,130,15
U 131,131,15
Zvav 32,128,192
U 132,132,15
U 133,133,15
U 134,134,15
U 135,135,15
K 128,128,129
K 129,130,131
Zvav 64,128,200
K 130,132,133
K 131,134,135
s_add_u32 s93, s67, 393216
buffer_store_dwordx4 v[128:131], v1, s[8:11], s93 offen offset:0 sc1
U 136,136,15
U 137,137,15
U 138,138,15
Zvav 96,128,208
U 139,139,15
U 140,140,15
U 141,141,15
U 142,142,15
U 143,143,15
K 136,136,137
Zvav 128,128,216
K 137,138,139
K 138,140,141
K 139,142,143
buffer_store_dwordx4 v[136:139], v1, s[8:11], s93 offen offset:1024 sc1
U 144,144,15
U 145,145,15
Zvav 132,132,216
U 146,146,15
U 147,147,15
U 148,148,15
U 149,149,15
U 150,150,15
U 151,151,15
Zvav 100,132,208
K 144,144,145
K 145,146,147
K 146,148,149
K 147,150,151
buffer_store_dwordx4 v[144:147], v1, s[8:11], s93 offen offset:2048 sc1
U 152,152,15
Zvav 68,132,200
U 153,153,15
U 154,154,15
U 155,155,15
U 156,156,15
U 157,157,15
U 158,158,15
Zvav 36,132,192
U 159,159,15
K 152,152,153
K 153,154,155
K 154,156,157
K 155,158,159
)"
R"(buffer_store_dwordx4 v[152:155], v1, s[8:11], s93 offen offset:3072 sc1
Zvav 40,136,192
Ra 152,242,55296
X 166
K 204,164,165
X 167
Zvav 72,136,200
Ra 156,242,56320
X 168
K 205,166,167
X 169
Zvav 104,136,208
X 170
K 212,168,169
X 171
Zvav 136,136,216
X 172
K 213,170,171
X 173
Zvav 140,140,216
Zvav 108,140,208
X 174
Zvav 76,140,200
s_waitcnt vmcnt(6) lgkmcnt(0)
s_barrier
v_xor_b32 v242, 0x10000, v242
Zvav 44,140,192
Ra 128,242,40960
K 220,172,173
X 175
Zvav 48,144,192
Zvav 80,144,200
v_mov_b32 v198, 0
Zvav 112,144,208
Ra 132,242,41984
K 221,174,175
Zvav 144,144,216
v_mov_b32 v199, 0
v_cndmask_b32_e64 v180, v241, v180, s[72:73]
Zvav 148,148,216
v_cndmask_b32_e64 v181, v241, v181, s[74:75]
Zvav 116,148,208
Ra 136,242,43008
v_cndmask_b32_e64 v182, v241, v182, s[76:77]
Zvav 84,148,200
v_cndmask_b32_e64 v183, v241, v183, s[78:79]
Zvav 52,148,192
X 180
Zvav 56,152,192
Ra 140,242,44032
X 181
Zvav 88,152,200
Zvav 120,152,208
X 182
Zvav 152,152,216
K 206,180,181
Zvav 156,156,216
X 183
Zvav 124,156,208
Zvav 92,156,200
s_waitcnt vmcnt(0)
s_and_b64 s[86:87], s[88:89], s[24:25]
s_cbranch_scc1 .LL_49%=
v_mov_b64 v[28:29], s[12:13]
s_mov_b64 exec, 1
buffer_store_dwordx2 v[28:29], off, s[8:11], s69 sc1
s_mov_b64 exec, -1
.LL_49%=:
K 207,182,183
Zvav 60,156,192
s_cmp_eq_u32 s64, 15
s_cbranch_scc1 .Lf2_41%=
.Ll2_38%=:
W 0
Cvaa 160,128,64,224
Ra 144,242,45056
Ra 148,242,46080
Mvaa 160,132,68
s_add_u32 m0, s62, 0
Ra 152,242,47104
Cvaa 164,132,84,228
X 184
Mvaa 164,128,80
X 185
Cvaa 168,128,96,232
X 186
Mvaa 168,132,100
Ra 156,242,48128
K 214,184,185
Cvaa 172,132,116,236
Dc 1,8,61,0
Mvaa 172,128,112
X 187
Mvaa 160,136,72
X 188
Mvaa 160,140,76
Ra 128,242,57344
K 215,186,187
Mvaa 164,140,92
Dc 1,8,61,1024
Mvaa 164,136,88
X 189
Mvaa 168,136,104
X 190
Mvaa 168,140,108
Ra 132,242,58368
K 222,188,189
Mvaa 172,140,124
Dc 1,8,61,2048
Mvaa 172,136,120
X 191
v_smfmac_f32_16x16x64_bf16 a[160:163], a[176:179], v[192:199], v240
Ra 136,242,59392
K 223,190,191
v_smfmac_f32_16x16x64_bf16 a[164:167], a[176:179], v[200:207], v240
X 160
v_smfmac_f32_16x16x64_bf16 a[168:171], a[176:179], v[208:215], v240
Dc 1,8,61,3072
v_smfmac_f32_16x16x64_bf16 a[172:175], a[176:179], v[216:223], v240
W 3
X 161
Cvaa 176,144,64,224
s_add_u32 m0, s62, 4096
X 162
Mvaa 176,148,68
Ra 140,242,60416
K 192,160,161
Cvaa 180,148,84,228
X 163
Mvaa 180,144,80
Dc 243,8,61,0
Cvaa 184,144,96,232
X 164
Mvaa 184,148,100
K 193,162,163
Cvaa 188,148,116,236
X 165
Mvaa 188,144,112
Dc 243,8,61,1024
Ra 144,242,61440
Mvaa 176,152,72
X 166
Mvaa 176,156,76
Ra 148,242,62464
K 200,164,165
Mvaa 180,156,92
X 167
Mvaa 180,152,88
Dc 243,8,61,2048
Mvaa 184,152,104
X 168
Mvaa 184,156,108
K 201,166,167
Mvaa 188,156,124
X 169
Mvaa 188,152,120
W 2
Dc 243,8,61,3072
Ra 152,242,63488
Mvav 32,128,196
s_add_u32 m0, s62, 8192
X 170
Mvav 64,128,204
Ra 156,242,64512
K 208,168,169
Mvav 96,128,212
X 171
Mvav 128,128,220
Dc 244,8,61,0
Mvav 132,132,220
X 172
Mvav 100,132,212
Ra 128,242,32768
K 209,170,171
Mvav 68,132,204
X 173
Mvav 36,132,196
Dc 244,8,61,1024
Mvav 40,136,196
X 174
Mvav 72,136,204
Ra 132,242,33792
K 216,172,173
Mvav 104,136,212
X 175
Mvav 136,136,220
Dc 244,8,61,2048
Mvav 140,140,220
X 176
Mvav 108,140,212
Ra 136,242,34816
K 217,174,175
Mvav 76,140,204
X 177
Mvav 44,140,196
W 3
Dc 244,8,61,3072
Mvav 48,144,196
X 178
Mvav 80,144,204
s_add_u32 m0, s62, 12288
Ra 140,242,35840
Mvav 112,144,212
K 194,176,177
Mvav 144,144,220
X 179
Mvav 148,148,220
X 180
Mvav 116,148,212
Dc 245,8,61,0
Ra 144,242,36864
Mvav 84,148,204
K 195,178,179
Mvav 52,148,196
X 181
Mvav 56,152,196
X 182
Mvav 88,152,204
Dc 245,8,61,1024
Ra 148,242,37888
Mvav 120,152,212
K 202,180,181
Mvav 152,152,220
X 183
Mvav 156,156,220
X 184
Mvav 124,156,212
Dc 245,8,61,2048
Ra 152,242,38912
Mvav 92,156,204
K 203,182,183
Mvav 60,156,196
W 3
X 185
Cvaa 160,128,64,224
X 186
Mvaa 160,132,68
Dc 245,8,61,3072
Ra 156,242,39936
Cvaa 164,132,84,228
s_add_u32 s61, s61, s63
K 210,184,185
Mvaa 164,128,80
X 187
Cvaa 168,128,96,232
X 188
Mvaa 168,132,100
Cvaa 172,132,116,236
s_xor_b32 s62, s62, 0x10000
K 211,186,187
Mvaa 172,128,112
X 189
Mvaa 160,136,72
Ra 128,242,49152
Ra 132,242,50176
Mvaa 160,140,76
s_add_u32 s64, s64, 1
X 190
Mvaa 164,140,92
K 218,188,189
Mvaa 164,136,88
X 191
Mvaa 168,136,104
Mvaa 168,140,108
X 160
Mvaa 172,140,124
K 219,190,191
Mvaa 172,136,120
W 2
X 161
Cvaa 176,144,64,224
Ra 136,242,51200
Ra 140,242,52224
Mvaa 176,148,68
X 162
Cvaa 180,148,84,228
K 196,160,161
Mvaa 180,144,80
X 163
Cvaa 184,144,96,232
Mvaa 184,148,100
X 164
Cvaa 188,148,116,236
K 197,162,163
Mvaa 188,144,112
X 165
Mvaa 176,152,72
Ra 144,242,53248
Ra 148,242,54272
Mvaa 176,156,76
X 166
Mvaa 180,156,92
K 204,164,165
Mvaa 180,152,88
X 167
Mvaa 184,152,104
Mvaa 184,156,108
X 168
Mvaa 188,156,124
Mvaa 188,152,120
W 2
Ra 152,242,55296
K 205,166,167
Mvav 32,128,192
X 169
Mvav 64,128,200
Ra 156,242,56320
Mvav 96,128,208
X 170
Mvav 128,128,216
K 212,168,169
Mvav 132,132,216
X 171
Mvav 100,132,208
X 172
Mvav 68,132,200
Mvav 36,132,192
K 213,170,171
Mvav 40,136,192
X 173
Mvav 72,136,200
X 174
Mvav 104,136,208
Mvav 136,136,216
K 220,172,173
Mvav 140,140,216
X 175
Mvav 108,140,208
X 176
Mvav 76,140,200
s_waitcnt vmcnt(0) lgkmcnt(0)
s_barrier
v_xor_b32 v242, 0x10000, v242
Mvav 44,140,192
Ra 128,242,40960
K 221,174,175
Mvav 48,144,192
X 177
Mvav 80,144,200
X 178
Mvav 112,144,208
Ra 132,242,41984
Mvav 144,144,216
K 198,176,177
Mvav 148,148,216
X 179
Mvav 116,148,208
X 180
Mvav 84,148,200
Ra 136,242,43008
Mvav 52,148,192
K 199,178,179
Mvav 56,152,192
X 181
Mvav 88,152,200
Ra 140,242,44032
Mvav 120,152,208
X 182
Mvav 152,152,216
K 206,180,181
Mvav 156,156,216
X 183
Mvav 124,156,208
Mvav 92,156,200
K 207,182,183
Mvav 60,156,192
s_cmp_lg_u32 s64, 15
s_cbranch_scc1 .Ll2_38%=
.Lf2_41%=:
W 0
Cvaa 160,128,64,224
v_bfe_u32 v28, v1, 4, 2
v_lshlrev_b32 v28, 3, v28
Mvaa 160,132,68
Ra 144,242,45056
Ra 148,242,46080
Cvaa 164,132,84,228
Ra 152,242,47104
buffer_load_dwordx2 v[28:29], v28, s[8:11], s54 offen sc1
Mvaa 164,128,80
s_lshl_b32 s92, s58, 2
s_add_u32 s91, s92, s51
s_add_u32 s95, s91, 3
Cvaa 168,128,96,232
s_and_b32 s95, s95, 15
s_mul_i32 s95, s95, 40960
s_mul_i32 s71, s51, 10240
Mvaa 168,132,100
s_add_u32 s71, s71, 0
s_add_u32 m0, s71, s48
s_lshl2_add_u32 s94, s48, s95
Cvaa 172,132,116,236
X 184
Mvaa 172,128,112
X 185
Mvaa 160,136,72
X 186
Mvaa 160,140,76
Ra 156,242,48128
K 214,184,185
)"
R"(Mvaa 164,140,92
X 187
Mvaa 164,136,88
D 16,40,94,0
Ra 128,242,57344
Mvaa 168,136,104
s_add_u32 s95, s91, 5
s_and_b32 s95, s95, 15
s_mul_i32 s95, s95, 40960
Mvaa 168,140,108
s_mul_i32 s71, s51, 10240
s_add_u32 s71, s71, 20480
Ra 132,242,58368
Mvaa 172,140,124
X 188
Mvaa 172,136,120
D 17,40,94,1024
K 215,186,187
v_smfmac_f32_16x16x64_bf16 a[160:163], a[176:179], v[192:199], v240
X 189
v_smfmac_f32_16x16x64_bf16 a[164:167], a[176:179], v[200:207], v240
X 190
K 222,188,189
X 191
s_nop 0
K 223,190,191
v_smfmac_f32_16x16x64_bf16 a[168:171], a[176:179], v[208:215], v240
D 18,40,94,2048
Ra 136,242,59392
v_smfmac_f32_16x16x64_bf16 a[172:175], a[176:179], v[216:223], v240
W 3
X 160
Cvaa 176,144,64,224
X 161
Mvaa 176,148,68
X 162
Cvaa 180,148,84,228
D 19,40,94,3072
K 192,160,161
Mvaa 180,144,80
s_add_u32 m0, m0, 4096
X 163
Cvaa 184,144,96,232
X 164
Mvaa 184,148,100
Ra 140,242,60416
K 193,162,163
Cvaa 188,148,116,236
buffer_load_dwordx4 v20, s[40:43], s94 offen lds
Mvaa 188,144,112
s_add_u32 m0, s71, s48
s_lshl2_add_u32 s94, s48, s95
Ra 144,242,61440
Mvaa 176,152,72
X 165
Mvaa 176,156,76
X 166
Mvaa 180,156,92
D 16,40,94,0
K 200,164,165
Mvaa 180,152,88
X 167
Mvaa 184,152,104
X 172
Mvaa 184,156,108
D 17,40,94,1024
K 201,166,167
Mvaa 188,156,124
X 173
Mvaa 188,152,120
W 1
Ra 148,242,62464
Ra 152,242,63488
Mvav 32,128,196
X 174
Mvav 64,128,204
D 18,40,94,2048
K 216,172,173
Mvav 96,128,212
X 175
Mvav 128,128,220
X 168
Mvav 132,132,220
s_mov_b32 s18, 40960
D 19,40,94,3072
K 217,174,175
Mvav 100,132,212
s_add_u32 m0, m0, 4096
X 169
Mvav 68,132,204
s_mov_b32 s19, 73728
X 170
Mvav 36,132,196
s_bitcmp1_b32 s14, 0
s_cselect_b32 s18, 49152, s18
s_cselect_b32 s19, 98304, s19
Ra 156,242,64512
K 208,168,169
Mvav 40,136,196
s_cmp_lt_u32 s14, 2
s_cselect_b32 s20, s18, s19
s_cselect_b32 s21, s19, s18
X 171
Mvav 72,136,204
X 184
Mvav 104,136,212
buffer_load_dwordx4 v20, s[40:43], s94 offen lds
K 209,170,171
Mvav 136,136,220
s_waitcnt vmcnt(10)
v_cmp_eq_u64 vcc, s[12:13], v[28:29]
s_cmp_eq_u64 vcc, -1
s_cselect_b32 s46, 79691776, 0
s_add_u32 s81, s58, 0
s_and_b32 s81, s81, 3
Mvav 140,140,220
s_lshl_b32 s81, s81, 12
s_add_u32 s81, s81, s49
Ra 128,242,49152
Mvav 108,140,212
X 185
Mvav 76,140,204
X 186
Mvav 44,140,196
W 1
buffer_load_dwordx4 a[184:187], v1, s[44:47], s81 offen offset:0 sc1
K 210,184,185
Mvav 48,144,196
s_add_u32 s82, s81, 131072
X 187
Mvav 80,144,204
v_add_u32 v2, s20, v1
v_add_u32 v3, s21, v1
Mvav 112,144,212
s_lshl_b32 s18, s14, 9
X 188
Mvav 144,144,220
Ra 132,242,50176
K 211,186,187
Mvav 148,148,220
buffer_load_dwordx4 a[200:203], v1, s[44:47], s82 offen offset:0 sc1
Ra 136,242,51200
Mvav 116,148,212
X 189
Mvav 84,148,204
v_lshrrev_b32 v232, 1, v1
v_add_u32 v232, s18, v232
Mvav 52,148,196
X 190
Mvav 56,152,196
K 218,188,189
v_bfe_u32 v12, v1, 6, 2
Mvav 88,152,204
buffer_load_dwordx4 a[188:191], v1, s[44:47], s81 offen offset:1024 sc1
Ra 140,242,52224
Mvav 120,152,212
X 191
Mvav 152,152,220
v_lshrrev_b32 v13, 8, v1
v_bfe_u32 v14, v1, 4, 1
Mvav 156,156,220
X 176
Mvav 124,156,212
K 219,190,191
v_lshlrev_b32 v14, 4, v14
Mvav 92,156,204
X 177
Mvav 60,156,196
buffer_load_dwordx4 a[204:207], v1, s[44:47], s82 offen offset:1024 sc1
v_mov_b32 v15, 0x3f80
Maav 168,180,208
X 178
K 194,176,177
X 179
X 180
K 195,178,179
Maav 172,180,216
X 181
X 182
K 202,180,181
X 183
s_nop 0
K 203,182,183
Maav 160,180,192
Ra 144,242,53248
v_lshlrev_b32 v15, v14, v15
Maav 164,180,200
W 1
buffer_load_dwordx4 a[192:195], v1, s[44:47], s81 offen offset:2048 sc1
v_bfe_u32 v14, v1, 5, 1
Mvav 96,128,208
Ra 148,242,54272
v_xor_b32 v27, v12, v13
v_lshl_or_b32 v27, v27, 1, v14
Mvav 128,128,216
v_cmp_eq_u32_e64 s[86:87], 0, v27
v_mov_b32 v226, 0
s_nop 0
v_cndmask_b32_e64 v224, 0, v15, s[86:87]
Mvav 132,132,216
Ra 152,242,55296
Ra 156,242,56320
buffer_load_dwordx4 a[208:211], v1, s[44:47], s82 offen offset:2048 sc1
Mvav 100,132,208
v_mov_b32 v228, 0
v_mov_b32 v230, v224
v_cmp_eq_u32_e64 s[86:87], 1, v27
Mvav 104,136,208
v_mov_b32 v227, 0
Mvav 136,136,216
buffer_load_dwordx4 a[196:199], v1, s[44:47], s81 offen offset:3072 sc1
v_cndmask_b32_e64 v225, 0, v15, s[86:87]
v_mov_b32 v229, 0
Mvav 140,140,216
v_mov_b32 v231, v225
G 4,160
G 8,161
Mvav 108,140,208
W 0
G 5,164
G 9,165
G 6,168
Mvav 112,144,208
buffer_load_dwordx4 a[212:215], v1, s[44:47], s82 offen offset:3072 sc1
s_add_u32 s81, s58, 1
s_and_b32 s81, s81, 3
s_lshl_b32 s81, s81, 12
Mvav 144,144,216
s_add_u32 s81, s81, s49
G 10,169
G 7,172
G 11,173
Mvav 148,148,216
A 4,4,8
A 5,5,9
A 6,6,10
Mvav 116,148,208
buffer_load_dwordx4 a[216:219], v1, s[44:47], s81 offen offset:0 sc1
s_add_u32 s82, s81, 131072
A 7,7,11
ds_write_b64 v232, v[6:7] offset:57344
Mvav 120,152,208
A 8,4,5
A 9,6,7
A 8,8,9
Mvav 152,152,216
v_cmp_ngt_f32_e64 s[86:87], s90, v8
s_lshl_b32 s19, s14, 2
v_mov_b32 v9, s19
s_or_b64 s[88:89], s[88:89], s[86:87]
Mvav 156,156,216
s_and_b64 s[88:89], s[88:89], s[24:25]
s_cselect_b32 s18, 1, 0
buffer_load_dwordx4 a[232:235], v1, s[44:47], s82 offen offset:0 sc1
v_mov_b32 v8, s18
ds_write_b32 v9, v8 offset:59392
Mvav 124,156,208
v_xor_b32 v10, 1024, v232
v_bfe_u32 v11, v1, 2, 4
K 96,96,97
Mvav 32,128,192
K 97,98,99
K 98,100,101
K 99,102,103
Mvav 64,128,200
ds_write_b128 v2, v[96:99] offset:0
K 128,128,129
K 129,130,131
Mvav 68,132,200
K 130,132,133
K 131,134,135
ds_write_b128 v2, v[128:131] offset:4096
Mvav 36,132,192
buffer_load_dwordx4 a[220:223], v1, s[44:47], s81 offen offset:1024 sc1
K 104,104,105
K 105,106,107
Mvav 40,136,192
K 106,108,109
K 107,110,111
ds_write_b128 v2, v[104:107] offset:1024
Mvav 72,136,200
K 136,136,137
K 137,138,139
K 138,140,141
Mvav 76,140,200
buffer_load_dwordx4 a[236:239], v1, s[44:47], s82 offen offset:1024 sc1
K 139,142,143
ds_write_b128 v2, v[136:139] offset:5120
Mvav 44,140,192
K 112,112,113
K 113,114,115
K 114,116,117
Mvav 48,144,192
K 115,118,119
ds_write_b128 v2, v[112:115] offset:2048
K 144,144,145
Mvav 80,144,200
K 145,146,147
K 146,148,149
K 147,150,151
Mvav 84,148,200
ds_write_b128 v2, v[144:147] offset:6144
buffer_load_dwordx4 a[224:227], v1, s[44:47], s81 offen offset:2048 sc1
K 120,120,121
Mvav 52,148,192
K 121,122,123
K 122,124,125
K 123,126,127
Mvav 56,152,192
ds_write_b128 v2, v[120:123] offset:3072
K 152,152,153
K 153,154,155
Mvav 88,152,200
buffer_load_dwordx4 a[240:243], v1, s[44:47], s82 offen offset:2048 sc1
K 154,156,157
K 155,158,159
Mvav 92,156,200
ds_write_b128 v2, v[152:155] offset:7168
Mvav 60,156,192
buffer_load_dwordx4 a[228:231], v1, s[44:47], s81 offen offset:3072 sc1
buffer_load_dwordx4 a[244:247], v1, s[44:47], s82 offen offset:3072 sc1
s_branch .Lend_43%=
.p2align 12
.Lfs_42%=:
v_smfmac_f32_16x16x64_bf16 a[160:163], a[176:179], v[192:199], v240
s_mov_b32 s46, 0
s_lshl_b32 s92, s58, 2
s_add_u32 s91, s92, s51
s_add_u32 s95, s91, 3
s_and_b32 s95, s95, 15
s_mul_i32 s95, s95, 40960
s_mul_i32 s71, s51, 10240
v_smfmac_f32_16x16x64_bf16 a[164:167], a[176:179], v[200:207], v240
s_add_u32 s71, s71, 0
s_add_u32 m0, s71, s48
s_lshl2_add_u32 s94, s48, s95
Ra 144,242,45056
Ra 148,242,46080
Ra 152,242,47104
Ra 156,242,48128
v_smfmac_f32_16x16x64_bf16 a[168:171], a[176:179], v[208:215], v240
D 16,40,94,0
s_add_u32 s95, s91, 5
s_and_b32 s95, s95, 15
s_mul_i32 s95, s95, 40960
s_mul_i32 s71, s51, 10240
s_add_u32 s71, s71, 20480
v_smfmac_f32_16x16x64_bf16 a[172:175], a[176:179], v[216:223], v240
W 4
Cvaa 168,128,96,232
v_mov_b32 v192, 0
G 12,160
Mvaa 168,132,100
D 17,40,94,1024
G 4,161
A 12,12,4
v_cmp_nlt_f32_e64 s[86:87], v12, s90
v_rcp_f32 v12, v12
s_or_b64 s[88:89], s[88:89], s[86:87]
Cvaa 172,132,116,236
v_accvgpr_write_b32 a160, 0
v_accvgpr_write_b32 a161, 0
v_accvgpr_write_b32 a162, 0
v_accvgpr_write_b32 a163, 0
G 13,164
G 5,165
Mvaa 172,128,112
v_mov_b32 v193, 0
A 13,13,5
v_cmp_nlt_f32_e64 s[86:87], v13, s90
v_rcp_f32 v13, v13
s_or_b64 s[88:89], s[88:89], s[86:87]
v_accvgpr_write_b32 a164, 0
Mvaa 168,136,104
v_accvgpr_write_b32 a165, 0
v_accvgpr_write_b32 a166, 0
v_accvgpr_write_b32 a167, 0
U 32,32,12
U 33,33,12
U 34,34,12
Mvaa 168,140,108
v_mov_b32 v200, 0
U 35,35,12
U 36,36,12
U 37,37,12
U 38,38,12
U 39,39,12
Mvaa 172,140,124
Ra 128,242,16384
Ra 132,242,17408
K 32,32,33
K 33,34,35
K 34,36,37
K 35,38,39
s_add_u32 s93, s67, 0
Mvaa 172,136,120
Ra 136,242,18432
Ra 140,242,19456
D 18,40,94,2048
v_mov_b32 v201, 0
)"
R"(W 4
Cvaa 188,144,112,236
Ra 144,242,20480
v_cndmask_b32_e64 v168, v241, v168, s[72:73]
v_cndmask_b32_e64 v169, v241, v169, s[74:75]
v_cndmask_b32_e64 v170, v241, v170, s[76:77]
Mvaa 188,148,116
buffer_store_dwordx4 v[32:35], v1, s[8:11], s93 offen offset:0 sc1
U 40,40,12
U 41,41,12
U 42,42,12
U 43,43,12
U 44,44,12
Mvaa 188,152,120
U 45,45,12
U 46,46,12
U 47,47,12
K 40,40,41
K 41,42,43
K 42,44,45
Mvaa 188,156,124
W 1
K 43,46,47
buffer_store_dwordx4 v[40:43], v1, s[8:11], s93 offen offset:1024 sc1
U 48,48,12
U 49,49,12
U 50,50,12
U 51,51,12
Mvav 96,128,212
U 52,52,12
U 53,53,12
U 54,54,12
U 55,55,12
K 48,48,49
K 49,50,51
Mvav 128,128,220
K 50,52,53
K 51,54,55
buffer_store_dwordx4 v[48:51], v1, s[8:11], s93 offen offset:2048 sc1
U 56,56,12
U 57,57,12
U 58,58,12
Mvav 132,132,220
Ra 148,242,21504
Ra 152,242,22528
Ra 156,242,23552
U 59,59,12
U 60,60,12
U 61,61,12
Mvav 100,132,212
D 19,40,94,3072
s_add_u32 m0, m0, 4096
U 62,62,12
U 63,63,12
K 56,56,57
K 57,58,59
K 58,60,61
Mvav 104,136,212
K 59,62,63
v_cndmask_b32_e64 v171, v241, v171, s[78:79]
X 168
X 169
Mvav 136,136,220
buffer_store_dwordx4 v[56:59], v1, s[8:11], s93 offen offset:3072 sc1
Ra 128,242,49152
G 14,168
G 6,169
A 14,14,6
v_cmp_nlt_f32_e64 s[86:87], v14, s90
Mvav 140,140,220
v_rcp_f32 v14, v14
s_or_b64 s[88:89], s[88:89], s[86:87]
v_accvgpr_write_b32 a168, 0
v_accvgpr_write_b32 a169, 0
v_accvgpr_write_b32 a170, 0
v_accvgpr_write_b32 a171, 0
Mvav 108,140,212
W 1
G 15,172
G 7,173
A 15,15,7
v_cmp_nlt_f32_e64 s[86:87], v15, s90
v_rcp_f32 v15, v15
Mvav 112,144,212
s_or_b64 s[88:89], s[88:89], s[86:87]
buffer_load_dwordx4 v20, s[40:43], s94 offen lds
v_accvgpr_write_b32 a172, 0
v_accvgpr_write_b32 a173, 0
v_accvgpr_write_b32 a174, 0
v_accvgpr_write_b32 a175, 0
U 64,64,13
Mvav 144,144,220
s_add_u32 m0, s71, s48
s_lshl2_add_u32 s94, s48, s95
U 65,65,13
U 66,66,13
U 67,67,13
U 68,68,13
U 69,69,13
Mvav 148,148,220
U 70,70,13
U 71,71,13
K 64,64,65
K 65,66,67
K 66,68,69
K 67,70,71
s_add_u32 s93, s67, 131072
Mvav 116,148,212
buffer_store_dwordx4 v[64:67], v1, s[8:11], s93 offen offset:0 sc1
U 72,72,13
U 73,73,13
U 74,74,13
U 75,75,13
U 76,76,13
Mvav 120,152,212
Ra 132,242,50176
Ra 136,242,51200
Ra 140,242,52224
U 77,77,13
U 78,78,13
U 79,79,13
Mvav 152,152,220
K 72,72,73
K 73,74,75
K 74,76,77
K 75,78,79
buffer_store_dwordx4 v[72:75], v1, s[8:11], s93 offen offset:1024 sc1
U 80,80,13
Mvav 156,156,220
X 170
K 208,168,169
X 171
X 172
K 209,170,171
X 173
X 174
K 216,172,173
X 175
v_mov_b32 v194, 0
K 217,174,175
v_mov_b32 v195, 0
v_mov_b32 v202, 0
v_mov_b32 v203, 0
v_mov_b32 v210, 0
v_mov_b32 v211, 0
Mvav 124,156,212
v_cndmask_b32_e64 v188, v241, v188, s[72:73]
v_cndmask_b32_e64 v189, v241, v189, s[74:75]
v_cndmask_b32_e64 v190, v241, v190, s[76:77]
v_cndmask_b32_e64 v191, v241, v191, s[78:79]
X 188
X 189
X 190
K 218,188,189
X 191
s_nop 0
K 219,190,191
Maav 168,180,208
D 16,40,94,0
U 81,81,13
U 82,82,13
U 83,83,13
U 84,84,13
U 85,85,13
Maav 172,180,216
W 0
U 86,86,13
U 87,87,13
K 80,80,81
K 81,82,83
K 82,84,85
K 83,86,87
Zvav 32,128,192
buffer_store_dwordx4 v[80:83], v1, s[8:11], s93 offen offset:2048 sc1
U 88,88,13
U 89,89,13
U 90,90,13
U 91,91,13
U 92,92,13
Zvav 64,128,200
U 93,93,13
U 94,94,13
U 95,95,13
K 88,88,89
K 89,90,91
K 90,92,93
K 91,94,95
buffer_store_dwordx4 v[88:91], v1, s[8:11], s93 offen offset:3072 sc1
U 96,96,14
U 97,97,14
U 98,98,14
U 99,99,14
U 100,100,14
U 101,101,14
U 102,102,14
U 103,103,14
K 96,96,97
K 97,98,99
K 98,100,101
K 99,102,103
s_add_u32 s93, s67, 262144
buffer_store_dwordx4 v[96:99], v1, s[8:11], s93 offen offset:0 sc1
s_nop 1
Zvav 96,128,208
U 104,104,14
U 105,105,14
U 106,106,14
U 107,107,14
U 108,108,14
U 109,109,14
U 110,110,14
U 111,111,14
K 104,104,105
K 105,106,107
K 106,108,109
K 107,110,111
buffer_store_dwordx4 v[104:107], v1, s[8:11], s93 offen offset:1024 sc1
U 112,112,14
U 113,113,14
U 114,114,14
U 115,115,14
U 116,116,14
U 117,117,14
U 118,118,14
U 119,119,14
K 112,112,113
K 113,114,115
K 114,116,117
K 115,118,119
buffer_store_dwordx4 v[112:115], v1, s[8:11], s93 offen offset:2048 sc1
U 120,120,14
U 121,121,14
U 122,122,14
U 123,123,14
U 124,124,14
U 125,125,14
U 126,126,14
U 127,127,14
K 120,120,121
K 121,122,123
K 122,124,125
K 123,126,127
buffer_store_dwordx4 v[120:123], v1, s[8:11], s93 offen offset:3072 sc1
U 128,128,15
U 129,129,15
U 130,130,15
U 131,131,15
U 132,132,15
U 133,133,15
U 134,134,15
U 135,135,15
K 128,128,129
K 129,130,131
K 130,132,133
K 131,134,135
s_add_u32 s93, s67, 393216
buffer_store_dwordx4 v[128:131], v1, s[8:11], s93 offen offset:0 sc1
s_nop 1
Zvav 128,128,216
Ra 144,242,53248
U 136,136,15
U 137,137,15
U 138,138,15
U 139,139,15
U 140,140,15
Zvav 132,132,216
D 17,40,94,1024
U 141,141,15
U 142,142,15
U 143,143,15
K 136,136,137
K 137,138,139
Zvav 100,132,208
Ra 148,242,54272
Ra 152,242,55296
Ra 156,242,56320
K 138,140,141
K 139,142,143
Zvav 68,132,200
D 18,40,94,2048
Zvav 36,132,192
Zvav 40,136,192
D 19,40,94,3072
s_add_u32 m0, m0, 4096
Zvav 72,136,200
Zvav 104,136,208
buffer_store_dwordx4 v[136:139], v1, s[8:11], s93 offen offset:1024 sc1
U 144,144,15
U 145,145,15
U 146,146,15
U 147,147,15
U 148,148,15
Zvav 136,136,216
U 149,149,15
U 150,150,15
U 151,151,15
K 144,144,145
K 145,146,147
K 146,148,149
Zvav 140,140,216
K 147,150,151
buffer_store_dwordx4 v[144:147], v1, s[8:11], s93 offen offset:2048 sc1
U 152,152,15
U 153,153,15
U 154,154,15
U 155,155,15
Zvav 108,140,208
U 156,156,15
U 157,157,15
U 158,158,15
U 159,159,15
K 152,152,153
K 153,154,155
Zvav 76,140,200
buffer_load_dwordx4 v20, s[40:43], s94 offen lds
K 154,156,157
K 155,158,159
Zvav 44,140,192
W 0
Zvav 48,144,192
buffer_store_dwordx4 v[152:155], v1, s[8:11], s93 offen offset:3072 sc1
s_mov_b32 s18, 40960
s_mov_b32 s19, 73728
s_bitcmp1_b32 s14, 0
s_cselect_b32 s18, 49152, s18
s_cselect_b32 s19, 98304, s19
s_cmp_lt_u32 s14, 2
s_cselect_b32 s20, s18, s19
s_cselect_b32 s21, s19, s18
v_add_u32 v2, s20, v1
v_add_u32 v3, s21, v1
Zvav 80,144,200
s_lshl_b32 s18, s14, 9
v_lshrrev_b32 v232, 1, v1
v_add_u32 v232, s18, v232
v_bfe_u32 v12, v1, 6, 2
v_lshrrev_b32 v13, 8, v1
v_bfe_u32 v14, v1, 4, 1
v_lshlrev_b32 v14, 4, v14
Zvav 112,144,208
v_mov_b32 v15, 0x3f80
v_lshlrev_b32 v15, v14, v15
v_bfe_u32 v14, v1, 5, 1
v_xor_b32 v27, v12, v13
v_lshl_or_b32 v27, v27, 1, v14
v_cmp_eq_u32_e64 s[86:87], 0, v27
Zvav 144,144,216
v_mov_b32 v226, 0
Zvav 148,148,216
v_cndmask_b32_e64 v224, 0, v15, s[86:87]
v_mov_b32 v228, 0
v_mov_b32 v230, v224
v_cmp_eq_u32_e64 s[86:87], 1, v27
v_mov_b32 v227, 0
s_nop 0
v_cndmask_b32_e64 v225, 0, v15, s[86:87]
Zvav 116,148,208
v_mov_b32 v229, 0
v_mov_b32 v231, v225
G 4,160
G 8,161
G 5,164
G 9,165
Zvav 84,148,200
G 6,168
G 10,169
G 7,172
G 11,173
A 4,4,8
A 5,5,9
Zvav 52,148,192
A 6,6,10
A 7,7,11
ds_write_b64 v232, v[6:7] offset:57344
A 8,4,5
A 9,6,7
A 8,8,9
Zvav 56,152,192
v_cmp_ngt_f32_e64 s[86:87], s90, v8
s_lshl_b32 s19, s14, 2
v_mov_b32 v9, s19
s_or_b64 s[88:89], s[88:89], s[86:87]
s_and_b64 s[88:89], s[88:89], s[24:25]
s_cselect_b32 s18, 1, 0
v_mov_b32 v8, s18
ds_write_b32 v9, v8 offset:59392
Zvav 88,152,200
v_xor_b32 v10, 1024, v232
v_bfe_u32 v11, v1, 2, 4
K 96,96,97
K 97,98,99
K 98,100,101
K 99,102,103
Zvav 120,152,208
ds_write_b128 v2, v[96:99] offset:0
K 128,128,129
K 129,130,131
K 130,132,133
K 131,134,135
ds_write_b128 v2, v[128:131] offset:4096
)"
R"(Zvav 152,152,216
K 104,104,105
K 105,106,107
K 106,108,109
K 107,110,111
ds_write_b128 v2, v[104:107] offset:1024
K 136,136,137
Zvav 156,156,216
K 137,138,139
K 138,140,141
K 139,142,143
ds_write_b128 v2, v[136:139] offset:5120
K 112,112,113
K 113,114,115
Zvav 124,156,208
K 114,116,117
K 115,118,119
ds_write_b128 v2, v[112:115] offset:2048
K 144,144,145
K 145,146,147
K 146,148,149
Zvav 92,156,200
K 147,150,151
ds_write_b128 v2, v[144:147] offset:6144
Zvav 60,156,192
K 120,120,121
K 121,122,123
K 122,124,125
K 123,126,127
ds_write_b128 v2, v[120:123] offset:3072
K 152,152,153
K 153,154,155
K 154,156,157
K 155,158,159
ds_write_b128 v2, v[152:155] offset:7168
s_waitcnt vmcnt(0)
s_and_b64 s[86:87], s[88:89], s[24:25]
s_cbranch_scc1 .LL_50%=
v_mov_b64 v[28:29], s[12:13]
s_mov_b64 exec, 1
buffer_store_dwordx2 v[28:29], off, s[8:11], s69 sc1
s_mov_b64 exec, -1
.LL_50%=:
s_mov_b32 s70, 0
s_branch .Lend_43%=
.p2align 12
.Lpd_32%=:
v_pk_mul_f32 v[32:33], v[32:33], s[22:23]
v_pk_mul_f32 v[34:35], v[34:35], s[22:23]
v_pk_mul_f32 v[36:37], v[36:37], s[22:23]
v_pk_mul_f32 v[38:39], v[38:39], s[22:23]
v_pk_mul_f32 v[64:65], v[64:65], s[22:23]
v_pk_mul_f32 v[66:67], v[66:67], s[22:23]
v_pk_mul_f32 v[68:69], v[68:69], s[22:23]
v_pk_mul_f32 v[70:71], v[70:71], s[22:23]
v_pk_mul_f32 v[96:97], v[96:97], s[22:23]
v_pk_mul_f32 v[98:99], v[98:99], s[22:23]
v_pk_mul_f32 v[100:101], v[100:101], s[22:23]
v_pk_mul_f32 v[102:103], v[102:103], s[22:23]
v_pk_mul_f32 v[128:129], v[128:129], s[22:23]
v_pk_mul_f32 v[130:131], v[130:131], s[22:23]
v_pk_mul_f32 v[132:133], v[132:133], s[22:23]
v_pk_mul_f32 v[134:135], v[134:135], s[22:23]
v_pk_mul_f32 v[40:41], v[40:41], s[22:23]
v_pk_mul_f32 v[42:43], v[42:43], s[22:23]
v_pk_mul_f32 v[44:45], v[44:45], s[22:23]
v_pk_mul_f32 v[46:47], v[46:47], s[22:23]
v_pk_mul_f32 v[72:73], v[72:73], s[22:23]
v_pk_mul_f32 v[74:75], v[74:75], s[22:23]
v_pk_mul_f32 v[76:77], v[76:77], s[22:23]
v_pk_mul_f32 v[78:79], v[78:79], s[22:23]
v_pk_mul_f32 v[104:105], v[104:105], s[22:23]
v_pk_mul_f32 v[106:107], v[106:107], s[22:23]
v_pk_mul_f32 v[108:109], v[108:109], s[22:23]
v_pk_mul_f32 v[110:111], v[110:111], s[22:23]
v_pk_mul_f32 v[136:137], v[136:137], s[22:23]
v_pk_mul_f32 v[138:139], v[138:139], s[22:23]
v_pk_mul_f32 v[140:141], v[140:141], s[22:23]
v_pk_mul_f32 v[142:143], v[142:143], s[22:23]
v_pk_mul_f32 v[48:49], v[48:49], s[22:23]
v_pk_mul_f32 v[50:51], v[50:51], s[22:23]
v_pk_mul_f32 v[52:53], v[52:53], s[22:23]
v_pk_mul_f32 v[54:55], v[54:55], s[22:23]
v_pk_mul_f32 v[80:81], v[80:81], s[22:23]
v_pk_mul_f32 v[82:83], v[82:83], s[22:23]
v_pk_mul_f32 v[84:85], v[84:85], s[22:23]
v_pk_mul_f32 v[86:87], v[86:87], s[22:23]
v_pk_mul_f32 v[112:113], v[112:113], s[22:23]
v_pk_mul_f32 v[114:115], v[114:115], s[22:23]
v_pk_mul_f32 v[116:117], v[116:117], s[22:23]
v_pk_mul_f32 v[118:119], v[118:119], s[22:23]
v_pk_mul_f32 v[144:145], v[144:145], s[22:23]
v_pk_mul_f32 v[146:147], v[146:147], s[22:23]
v_pk_mul_f32 v[148:149], v[148:149], s[22:23]
v_pk_mul_f32 v[150:151], v[150:151], s[22:23]
v_pk_mul_f32 v[56:57], v[56:57], s[22:23]
v_pk_mul_f32 v[58:59], v[58:59], s[22:23]
v_pk_mul_f32 v[60:61], v[60:61], s[22:23]
v_pk_mul_f32 v[62:63], v[62:63], s[22:23]
v_pk_mul_f32 v[88:89], v[88:89], s[22:23]
v_pk_mul_f32 v[90:91], v[90:91], s[22:23]
v_pk_mul_f32 v[92:93], v[92:93], s[22:23]
v_pk_mul_f32 v[94:95], v[94:95], s[22:23]
v_pk_mul_f32 v[120:121], v[120:121], s[22:23]
v_pk_mul_f32 v[122:123], v[122:123], s[22:23]
v_pk_mul_f32 v[124:125], v[124:125], s[22:23]
v_pk_mul_f32 v[126:127], v[126:127], s[22:23]
v_pk_mul_f32 v[152:153], v[152:153], s[22:23]
v_pk_mul_f32 v[154:155], v[154:155], s[22:23]
v_pk_mul_f32 v[156:157], v[156:157], s[22:23]
v_pk_mul_f32 v[158:159], v[158:159], s[22:23]
K 32,32,33
K 33,34,35
K 34,36,37
K 35,38,39
K 40,40,41
K 41,42,43
K 42,44,45
K 43,46,47
W 0
Zvav 196,128,32
s_add_u32 m0, s62, 0
Ra 144,242,4096
Ra 148,242,5120
Ra 152,242,6144
Ra 156,242,7168
Mvav 196,132,40
K 64,64,65
K 65,66,67
K 66,68,69
K 67,70,71
K 72,72,73
K 73,74,75
K 74,76,77
K 75,78,79
s_nop 1
Zvav 204,132,72
Dc 1,8,61,0
K 96,96,97
K 97,98,99
K 98,100,101
Mvav 204,128,64
K 99,102,103
K 104,104,105
K 105,106,107
K 106,108,109
K 107,110,111
Zvav 212,128,96
Dc 1,8,61,1024
K 128,128,129
K 129,130,131
K 130,132,133
Mvav 212,132,104
K 131,134,135
K 136,136,137
K 137,138,139
K 138,140,141
K 139,142,143
s_nop 1
Zvav 220,132,136
Dc 1,8,61,2048
K 48,48,49
K 49,50,51
K 50,52,53
Mvav 220,128,128
K 51,54,55
K 56,56,57
K 57,58,59
K 58,60,61
K 59,62,63
Mvav 196,136,48
Dc 1,8,61,3072
s_add_u32 m0, s62, 4096
Ra 128,242,8192
Ra 132,242,9216
K 80,80,81
Mvav 196,140,56
K 81,82,83
K 82,84,85
K 83,86,87
K 88,88,89
K 89,90,91
K 90,92,93
K 91,94,95
s_nop 1
Mvav 204,140,88
Dc 243,8,61,0
K 112,112,113
K 113,114,115
K 114,116,117
Mvav 204,136,80
K 115,118,119
K 120,120,121
K 121,122,123
K 122,124,125
K 123,126,127
Mvav 212,136,112
Dc 243,8,61,1024
K 144,144,145
K 145,146,147
K 146,148,149
Mvav 212,140,120
K 147,150,151
K 152,152,153
K 153,154,155
K 154,156,157
K 155,158,159
s_nop 1
Mvav 220,140,152
Dc 243,8,61,2048
v_accvgpr_write_b32 a0, v32
v_accvgpr_write_b32 a1, v33
v_accvgpr_write_b32 a2, v34
Mvav 220,136,144
W 2
Dc 243,8,61,3072
s_add_u32 m0, s62, 8192
v_accvgpr_write_b32 a3, v35
v_accvgpr_write_b32 a4, v40
Zvav 68,144,64
Dc 244,8,61,0
v_accvgpr_write_b32 a5, v41
v_accvgpr_write_b32 a6, v42
v_accvgpr_write_b32 a7, v43
Mvav 68,148,72
Dc 244,8,61,1024
v_accvgpr_write_b32 a8, v48
v_accvgpr_write_b32 a9, v49
v_accvgpr_write_b32 a10, v50
Zvav 100,148,104
Dc 244,8,61,2048
Ra 136,242,10240
Ra 140,242,11264
v_accvgpr_write_b32 a11, v51
Mvav 100,144,96
Dc 244,8,61,3072
s_add_u32 m0, s62, 12288
v_accvgpr_write_b32 a12, v56
v_accvgpr_write_b32 a13, v57
v_accvgpr_write_b32 a14, v58
Zvav 132,144,128
Dc 245,8,61,0
v_accvgpr_write_b32 a15, v59
v_accvgpr_write_b32 a16, v64
v_cndmask_b32_e64 v196, v241, v196, s[72:73]
Mvav 132,148,136
Dc 245,8,61,1024
v_cndmask_b32_e64 v197, v241, v197, s[74:75]
v_cndmask_b32_e64 v198, v241, v198, s[76:77]
v_cndmask_b32_e64 v199, v241, v199, s[78:79]
Mvav 68,152,80
Dc 245,8,61,2048
v_max3_f32 v4, v196, v197, v198
v_max3_f32 v4, v4, v199, v252
v_max3_f32 v5, v204, v205, v206
Mvav 68,156,88
v_max3_f32 v5, v5, v207, v253
v_max3_f32 v6, v212, v213, v214
v_max3_f32 v6, v6, v215, v254
v_max3_f32 v7, v220, v221, v222
Mvav 100,156,120
v_max3_f32 v7, v7, v223, v255
v_mov_b32 v8, v4
v_mov_b32 v9, v5
v_mov_b32 v10, v6
Mvav 100,152,112
v_mov_b32 v11, v7
v_permlane32_swap_b32 v4, v8
v_permlane32_swap_b32 v5, v9
v_accvgpr_write_b32 a17, v65
Mvav 132,152,144
v_permlane32_swap_b32 v6, v10
v_permlane32_swap_b32 v7, v11
v_max_f32 v4, v4, v8
v_max_f32 v5, v5, v9
v_max_f32 v6, v6, v10
v_max_f32 v7, v7, v11
v_mov_b32 v8, v4
v_mov_b32 v9, v5
v_mov_b32 v10, v6
v_mov_b32 v11, v7
v_permlane16_swap_b32 v4, v8
v_permlane16_swap_b32 v5, v9
v_permlane16_swap_b32 v6, v10
v_permlane16_swap_b32 v7, v11
v_max_f32 v4, v4, v8
v_max_f32 v5, v5, v9
v_max_f32 v6, v6, v10
v_max_f32 v7, v7, v11
v_sub_f32 v4, 0, v4
v_sub_f32 v5, 0, v5
v_sub_f32 v6, 0, v6
v_sub_f32 v7, 0, v7
v_mov_b32 v224, v4
v_mov_b32 v225, v4
v_mov_b32 v226, v4
v_mov_b32 v227, v4
v_mov_b32 v228, v5
v_mov_b32 v229, v5
v_mov_b32 v230, v5
v_mov_b32 v231, v5
v_mov_b32 v232, v6
v_mov_b32 v233, v6
v_mov_b32 v234, v6
v_mov_b32 v235, v6
v_mov_b32 v236, v7
v_mov_b32 v237, v7
v_mov_b32 v238, v7
v_mov_b32 v239, v7
Mvav 132,156,152
W 0
Dc 245,8,61,3072
v_accvgpr_write_b32 a18, v66
v_accvgpr_write_b32 a19, v67
v_accvgpr_write_b32 a20, v72
Cvav 168,128,96,232
s_add_u32 s61, s61, s63
s_add_u32 s36, s29, 65536
v_accvgpr_write_b32 a21, v73
v_accvgpr_write_b32 a22, v74
v_accvgpr_write_b32 a23, v75
Mvav 168,132,104
s_and_b32 s80, s14, 1
buffer_store_dwordx4 a[192:195], v1, s[8:11], s36 offen offset:0 sc1
v_accvgpr_write_b32 a24, v80
v_accvgpr_write_b32 a25, v81
v_accvgpr_write_b32 a26, v82
Cvav 172,132,136,236
s_lshl_b32 s80, s80, 14
Ra 144,242,12288
Ra 148,242,13312
Ra 152,242,14336
Ra 156,242,15360
Mvav 172,128,128
s_add_u32 s80, s80, 98304
v_accvgpr_write_b32 a27, v83
v_accvgpr_write_b32 a28, v88
v_accvgpr_write_b32 a29, v89
v_accvgpr_write_b32 a30, v90
Mvav 168,136,112
buffer_store_dwordx4 a[200:203], v1, s[8:11], s36 offen offset:1024 sc1
v_accvgpr_write_b32 a31, v91
v_accvgpr_write_b32 a32, v96
v_accvgpr_write_b32 a33, v97
Mvav 168,140,120
v_accvgpr_write_b32 a34, v98
v_accvgpr_write_b32 a35, v99
v_accvgpr_write_b32 a36, v104
v_accvgpr_write_b32 a37, v105
Mvav 172,140,152
Ra 128,242,24576
Ra 132,242,25600
buffer_store_dwordx4 a[208:211], v1, s[8:11], s36 offen offset:2048 sc1
v_accvgpr_write_b32 a38, v106
Mvav 172,136,144
Ra 136,242,26624
Ra 140,242,27648
v_accvgpr_write_b32 a39, v107
W 4
Cvav 188,144,128,236
v_accvgpr_write_b32 a40, v112
v_accvgpr_write_b32 a41, v113
v_accvgpr_write_b32 a42, v114
v_accvgpr_write_b32 a43, v115
v_accvgpr_write_b32 a44, v120
v_accvgpr_write_b32 a45, v121
v_accvgpr_write_b32 a46, v122
v_accvgpr_write_b32 a47, v123
v_accvgpr_write_b32 a48, v128
v_accvgpr_write_b32 a49, v129
v_accvgpr_write_b32 a50, v130
v_accvgpr_write_b32 a51, v131
v_accvgpr_write_b32 a52, v136
v_accvgpr_write_b32 a53, v137
v_accvgpr_write_b32 a54, v138
v_accvgpr_write_b32 a55, v139
v_accvgpr_write_b32 a56, v144
v_accvgpr_write_b32 a57, v145
v_accvgpr_write_b32 a58, v146
v_accvgpr_write_b32 a59, v147
v_accvgpr_write_b32 a60, v152
v_accvgpr_write_b32 a61, v153
v_accvgpr_write_b32 a62, v154
v_accvgpr_write_b32 a63, v155
Mvav 188,148,136
s_barrier
buffer_store_dwordx4 a[216:219], v1, s[8:11], s36 offen offset:3072 sc1
s_add_u32 s36, s29, 69632
v_add_u32 v8, s80, v1
A 196,196,224
Mvav 188,152,144
A 197,197,224
A 198,198,224
A 199,199,224
X 196
X 197
X 198
K 192,196,197
X 199
A 204,204,228
)"
R"(K 193,198,199
A 205,205,228
A 206,206,228
A 207,207,228
X 204
X 205
X 206
K 200,204,205
X 207
A 212,212,232
K 201,206,207
A 213,213,232
A 214,214,232
A 215,215,232
X 212
X 213
X 214
K 208,212,213
X 215
A 220,220,236
K 209,214,215
A 221,221,236
A 222,222,236
A 223,223,236
X 220
X 221
X 222
K 216,220,221
X 223
v_mov_b32 v194, 0
K 217,222,223
v_mov_b32 v195, 0
v_cndmask_b32_e64 v68, v241, v68, s[72:73]
v_cndmask_b32_e64 v69, v241, v69, s[74:75]
v_cndmask_b32_e64 v70, v241, v70, s[76:77]
v_cndmask_b32_e64 v71, v241, v71, s[78:79]
A 68,68,228
A 69,69,228
A 70,70,228
A 71,71,228
X 68
X 69
X 70
K 202,68,69
X 71
A 100,100,232
K 203,70,71
A 101,101,232
A 102,102,232
A 103,103,232
X 100
X 101
X 102
K 210,100,101
X 103
A 132,132,236
K 211,102,103
A 133,133,236
A 134,134,236
A 135,135,236
X 132
X 133
X 134
K 218,132,133
X 135
s_nop 0
K 219,134,135
Mvav 188,156,152
W 0
buffer_store_dwordx4 a[224:227], v1, s[8:11], s36 offen offset:0 sc1
Ra 144,242,28672
Ra 148,242,29696
Ra 152,242,30720
Zvav 32,128,192
buffer_store_dwordx4 a[232:235], v1, s[8:11], s36 offen offset:1024 sc1
Ra 156,242,31744
Zvav 64,128,200
buffer_store_dwordx4 a[240:243], v1, s[8:11], s36 offen offset:2048 sc1
Zvav 96,128,208
s_and_b32 s80, s14, 1
s_xor_b32 s62, s62, 0x10000
buffer_store_dwordx4 a[248:251], v1, s[8:11], s36 offen offset:3072 sc1
s_add_u32 s36, s29, 393216
Zvav 128,128,216
s_lshl_b32 s80, s80, 14
s_add_u32 s80, s80, 131072
buffer_store_dwordx4 a[196:199], v1, s[8:11], s36 offen offset:0 sc1
v_add_u32 v23, 61440, v21
Zvav 132,132,216
buffer_store_dwordx4 a[204:207], v1, s[8:11], s36 offen offset:1024 sc1
v_add_u32 v12, s80, v1
Ra 64,12,8192
v_add_u32 v24, 61440, v22
Zvav 100,132,208
buffer_store_dwordx4 a[212:215], v1, s[8:11], s36 offen offset:2048 sc1
Ra 68,12,9216
Ra 72,12,10240
v_add_u32 v25, 122880, v21
Zvav 68,132,200
buffer_store_dwordx4 a[220:223], v1, s[8:11], s36 offen offset:3072 sc1
s_add_u32 s36, s29, 397312
Ra 76,12,11264
Ra 80,12,12288
Ra 84,12,13312
Zvav 36,132,192
buffer_store_dwordx4 a[228:231], v1, s[8:11], s36 offen offset:0 sc1
Ra 88,12,14336
Ra 92,12,15360
v_add_u32 v26, 122880, v22
Zvav 40,136,192
buffer_store_dwordx4 a[236:239], v1, s[8:11], s36 offen offset:1024 sc1
Ra 96,12,0
Ra 100,12,1024
Ra 104,12,2048
Zvav 72,136,200
Ra 108,12,3072
Ra 112,12,4096
buffer_store_dwordx4 a[244:247], v1, s[8:11], s36 offen offset:2048 sc1
Ra 116,12,5120
Zvav 104,136,208
Ra 120,12,6144
Ra 124,12,7168
buffer_store_dwordx4 a[252:255], v1, s[8:11], s36 offen offset:3072 sc1
Zvav 136,136,216
Zvav 140,140,216
Zvav 108,140,208
Zvav 76,140,200
s_waitcnt vmcnt(16) lgkmcnt(0)
s_barrier
v_xor_b32 v242, 0x10000, v242
ds_read_b128 v[248:251], v8
Zvav 44,140,192
Ra 128,242,40960
Zvav 48,144,192
Zvav 80,144,200
X 172
X 173
Zvav 112,144,208
Ra 132,242,41984
X 174
K 220,172,173
Zvav 144,144,216
X 175
v_cndmask_b32_e64 v168, v241, v168, s[72:73]
K 221,174,175
Zvav 148,148,216
v_cndmask_b32_e64 v169, v241, v169, s[74:75]
v_cndmask_b32_e64 v170, v241, v170, s[76:77]
v_cndmask_b32_e64 v171, v241, v171, s[78:79]
Zvav 116,148,208
X 168
X 169
Zvav 84,148,200
W 2
Ra 136,242,43008
X 170
K 212,168,169
Zvav 52,148,192
v_mov_b32 v224, v250
v_mov_b32 v225, v250
v_mov_b32 v226, v250
v_mov_b32 v227, v250
Zvav 56,152,192
v_mov_b32 v228, v251
v_mov_b32 v229, v251
v_mov_b32 v230, v251
v_mov_b32 v231, v251
Zvav 88,152,200
v_mov_b32 v232, v248
v_mov_b32 v233, v248
v_mov_b32 v234, v248
v_mov_b32 v235, v248
Zvav 120,152,208
v_mov_b32 v236, v249
v_mov_b32 v237, v249
v_mov_b32 v238, v249
v_mov_b32 v239, v249
Zvav 152,152,216
X 171
v_mov_b32 v204, 0
K 213,170,171
Zvav 156,156,216
Ra 140,242,44032
v_mov_b32 v205, 0
v_mov_b32 v196, 0
v_mov_b32 v197, 0
Zvav 124,156,208
v_mov_b32 v198, 0
v_mov_b32 v199, 0
v_mov_b32 v206, 0
v_mov_b32 v207, 0
Zvav 92,156,200
v_mov_b32 v214, 0
v_mov_b32 v215, 0
v_cndmask_b32_e64 v188, v241, v188, s[72:73]
v_cndmask_b32_e64 v189, v241, v189, s[74:75]
Zvav 60,156,192
v_cndmask_b32_e64 v190, v241, v190, s[76:77]
v_cndmask_b32_e64 v191, v241, v191, s[78:79]
X 188
X 189
X 190
K 222,188,189
X 191
s_nop 0
K 223,190,191
s_waitcnt vmcnt(0)
s_add_u32 s80, s71, 128
v_mov_b64 v[28:29], s[12:13]
s_mov_b64 exec, 1
buffer_store_dwordx2 v[28:29], off, s[8:11], s80 sc1
s_mov_b64 exec, -1
s_nop 1
s_branch .Lsp2_37%=
.p2align 12
.Lpe_48%=:
v_pk_mul_f32 v[32:33], v[32:33], s[22:23]
v_pk_mul_f32 v[34:35], v[34:35], s[22:23]
v_pk_mul_f32 v[36:37], v[36:37], s[22:23]
v_pk_mul_f32 v[38:39], v[38:39], s[22:23]
v_pk_mul_f32 v[64:65], v[64:65], s[22:23]
v_pk_mul_f32 v[66:67], v[66:67], s[22:23]
v_pk_mul_f32 v[68:69], v[68:69], s[22:23]
v_pk_mul_f32 v[70:71], v[70:71], s[22:23]
v_pk_mul_f32 v[96:97], v[96:97], s[22:23]
v_pk_mul_f32 v[98:99], v[98:99], s[22:23]
v_pk_mul_f32 v[100:101], v[100:101], s[22:23]
v_pk_mul_f32 v[102:103], v[102:103], s[22:23]
v_pk_mul_f32 v[128:129], v[128:129], s[22:23]
v_pk_mul_f32 v[130:131], v[130:131], s[22:23]
v_pk_mul_f32 v[132:133], v[132:133], s[22:23]
v_pk_mul_f32 v[134:135], v[134:135], s[22:23]
v_pk_mul_f32 v[40:41], v[40:41], s[22:23]
v_pk_mul_f32 v[42:43], v[42:43], s[22:23]
v_pk_mul_f32 v[44:45], v[44:45], s[22:23]
v_pk_mul_f32 v[46:47], v[46:47], s[22:23]
v_pk_mul_f32 v[72:73], v[72:73], s[22:23]
v_pk_mul_f32 v[74:75], v[74:75], s[22:23]
v_pk_mul_f32 v[76:77], v[76:77], s[22:23]
v_pk_mul_f32 v[78:79], v[78:79], s[22:23]
v_pk_mul_f32 v[104:105], v[104:105], s[22:23]
v_pk_mul_f32 v[106:107], v[106:107], s[22:23]
v_pk_mul_f32 v[108:109], v[108:109], s[22:23]
v_pk_mul_f32 v[110:111], v[110:111], s[22:23]
v_pk_mul_f32 v[136:137], v[136:137], s[22:23]
v_pk_mul_f32 v[138:139], v[138:139], s[22:23]
v_pk_mul_f32 v[140:141], v[140:141], s[22:23]
v_pk_mul_f32 v[142:143], v[142:143], s[22:23]
v_pk_mul_f32 v[48:49], v[48:49], s[22:23]
v_pk_mul_f32 v[50:51], v[50:51], s[22:23]
v_pk_mul_f32 v[52:53], v[52:53], s[22:23]
v_pk_mul_f32 v[54:55], v[54:55], s[22:23]
v_pk_mul_f32 v[80:81], v[80:81], s[22:23]
v_pk_mul_f32 v[82:83], v[82:83], s[22:23]
v_pk_mul_f32 v[84:85], v[84:85], s[22:23]
v_pk_mul_f32 v[86:87], v[86:87], s[22:23]
v_pk_mul_f32 v[112:113], v[112:113], s[22:23]
v_pk_mul_f32 v[114:115], v[114:115], s[22:23]
v_pk_mul_f32 v[116:117], v[116:117], s[22:23]
v_pk_mul_f32 v[118:119], v[118:119], s[22:23]
v_pk_mul_f32 v[144:145], v[144:145], s[22:23]
v_pk_mul_f32 v[146:147], v[146:147], s[22:23]
v_pk_mul_f32 v[148:149], v[148:149], s[22:23]
v_pk_mul_f32 v[150:151], v[150:151], s[22:23]
v_pk_mul_f32 v[56:57], v[56:57], s[22:23]
v_pk_mul_f32 v[58:59], v[58:59], s[22:23]
v_pk_mul_f32 v[60:61], v[60:61], s[22:23]
v_pk_mul_f32 v[62:63], v[62:63], s[22:23]
v_pk_mul_f32 v[88:89], v[88:89], s[22:23]
v_pk_mul_f32 v[90:91], v[90:91], s[22:23]
v_pk_mul_f32 v[92:93], v[92:93], s[22:23]
v_pk_mul_f32 v[94:95], v[94:95], s[22:23]
v_pk_mul_f32 v[120:121], v[120:121], s[22:23]
v_pk_mul_f32 v[122:123], v[122:123], s[22:23]
v_pk_mul_f32 v[124:125], v[124:125], s[22:23]
v_pk_mul_f32 v[126:127], v[126:127], s[22:23]
v_pk_mul_f32 v[152:153], v[152:153], s[22:23]
v_pk_mul_f32 v[154:155], v[154:155], s[22:23]
v_pk_mul_f32 v[156:157], v[156:157], s[22:23]
v_pk_mul_f32 v[158:159], v[158:159], s[22:23]
K 32,32,33
K 33,34,35
K 34,36,37
K 35,38,39
K 40,40,41
K 41,42,43
K 42,44,45
K 43,46,47
W 0
Zvav 196,128,32
s_add_u32 m0, s62, 0
Ra 144,242,4096
Ra 148,242,5120
Ra 152,242,6144
Ra 156,242,7168
Mvav 196,132,40
K 64,64,65
K 65,66,67
K 66,68,69
K 67,70,71
K 72,72,73
K 73,74,75
K 74,76,77
K 75,78,79
s_nop 1
Zvav 204,132,72
Dc 1,8,61,0
K 96,96,97
K 97,98,99
K 98,100,101
Mvav 204,128,64
K 99,102,103
K 104,104,105
K 105,106,107
K 106,108,109
K 107,110,111
Zvav 212,128,96
Dc 1,8,61,1024
K 128,128,129
K 129,130,131
K 130,132,133
Mvav 212,132,104
K 131,134,135
K 136,136,137
K 137,138,139
K 138,140,141
K 139,142,143
s_nop 1
Zvav 220,132,136
Dc 1,8,61,2048
K 48,48,49
K 49,50,51
K 50,52,53
Mvav 220,128,128
K 51,54,55
K 56,56,57
K 57,58,59
K 58,60,61
K 59,62,63
Mvav 196,136,48
Dc 1,8,61,3072
s_add_u32 m0, s62, 4096
Ra 128,242,8192
Ra 132,242,9216
K 80,80,81
Mvav 196,140,56
K 81,82,83
K 82,84,85
K 83,86,87
K 88,88,89
K 89,90,91
K 90,92,93
K 91,94,95
s_nop 1
Mvav 204,140,88
Dc 243,8,61,0
K 112,112,113
K 113,114,115
K 114,116,117
Mvav 204,136,80
K 115,118,119
K 120,120,121
K 121,122,123
K 122,124,125
K 123,126,127
Mvav 212,136,112
Dc 243,8,61,1024
K 144,144,145
K 145,146,147
K 146,148,149
Mvav 212,140,120
K 147,150,151
K 152,152,153
K 153,154,155
K 154,156,157
K 155,158,159
s_nop 1
Mvav 220,140,152
Dc 243,8,61,2048
v_accvgpr_write_b32 a0, v32
v_accvgpr_write_b32 a1, v33
v_accvgpr_write_b32 a2, v34
Mvav 220,136,144
W 2
Dc 243,8,61,3072
s_add_u32 m0, s62, 8192
v_accvgpr_write_b32 a3, v35
v_accvgpr_write_b32 a4, v40
Zvav 36,144,32
Ra 136,242,10240
Ra 140,242,11264
Dc 244,8,61,0
v_accvgpr_write_b32 a5, v41
Mvav 36,148,40
v_accvgpr_write_b32 a6, v42
v_accvgpr_write_b32 a7, v43
v_max3_f32 v4, v196, v197, v198
v_max3_f32 v4, v4, v199, v252
Zvav 68,148,72
v_max3_f32 v5, v204, v205, v206
)"
R"(v_max3_f32 v5, v5, v207, v253
v_max3_f32 v6, v212, v213, v214
v_max3_f32 v6, v6, v215, v254
Mvav 68,144,64
v_max3_f32 v7, v220, v221, v222
v_max3_f32 v7, v7, v223, v255
v_mov_b32 v8, v4
v_mov_b32 v9, v5
Zvav 100,144,96
v_mov_b32 v10, v6
v_mov_b32 v11, v7
v_permlane32_swap_b32 v4, v8
v_accvgpr_write_b32 a8, v48
Mvav 100,148,104
v_permlane32_swap_b32 v5, v9
v_permlane32_swap_b32 v6, v10
v_permlane32_swap_b32 v7, v11
v_max_f32 v4, v4, v8
Zvav 132,148,136
v_max_f32 v5, v5, v9
v_max_f32 v6, v6, v10
v_max_f32 v7, v7, v11
v_mov_b32 v8, v4
Mvav 132,144,128
Dc 244,8,61,1024
v_mov_b32 v9, v5
v_mov_b32 v10, v6
v_mov_b32 v11, v7
Mvav 36,152,48
v_permlane16_swap_b32 v4, v8
v_permlane16_swap_b32 v5, v9
v_permlane16_swap_b32 v6, v10
v_accvgpr_write_b32 a9, v49
Mvav 36,156,56
v_permlane16_swap_b32 v7, v11
v_max_f32 v4, v4, v8
v_max_f32 v5, v5, v9
v_max_f32 v6, v6, v10
Mvav 68,156,88
v_max_f32 v7, v7, v11
v_sub_f32 v4, 0, v4
v_sub_f32 v5, 0, v5
v_sub_f32 v6, 0, v6
Mvav 68,152,80
v_sub_f32 v7, 0, v7
v_mov_b32 v224, v4
v_mov_b32 v225, v4
v_mov_b32 v226, v4
Mvav 100,152,112
v_mov_b32 v227, v4
v_mov_b32 v228, v5
v_mov_b32 v229, v5
v_mov_b32 v230, v5
Mvav 100,156,120
v_mov_b32 v231, v5
v_mov_b32 v232, v6
v_mov_b32 v233, v6
v_mov_b32 v234, v6
Mvav 132,156,152
v_mov_b32 v235, v6
v_mov_b32 v236, v7
v_mov_b32 v237, v7
v_mov_b32 v238, v7
v_mov_b32 v239, v7
Mvav 132,152,144
W 0
Dc 244,8,61,2048
v_accvgpr_write_b32 a10, v50
v_accvgpr_write_b32 a11, v51
v_accvgpr_write_b32 a12, v56
Cvav 160,128,32,224
Dc 244,8,61,3072
s_add_u32 m0, s62, 12288
v_accvgpr_write_b32 a13, v57
v_accvgpr_write_b32 a14, v58
v_accvgpr_write_b32 a15, v59
Mvav 160,132,40
s_and_b32 s80, s14, 1
Ra 144,242,12288
Ra 148,242,13312
v_accvgpr_write_b32 a16, v64
v_accvgpr_write_b32 a17, v65
Cvav 164,132,72,228
s_lshl_b32 s80, s80, 14
Dc 245,8,61,0
v_accvgpr_write_b32 a18, v66
v_accvgpr_write_b32 a19, v67
v_accvgpr_write_b32 a20, v72
Mvav 164,128,64
s_add_u32 s80, s80, 98304
Dc 245,8,61,1024
v_accvgpr_write_b32 a21, v73
v_accvgpr_write_b32 a22, v74
v_accvgpr_write_b32 a23, v75
Cvav 168,128,96,232
Dc 245,8,61,2048
v_accvgpr_write_b32 a24, v80
v_accvgpr_write_b32 a25, v81
v_accvgpr_write_b32 a26, v82
Mvav 168,132,104
Dc 245,8,61,3072
s_add_u32 s61, s61, s63
s_add_u32 s36, s29, 65536
A 196,196,224
v_accvgpr_write_b32 a27, v83
Cvav 172,132,136,236
Ra 152,242,14336
A 197,197,224
A 198,198,224
v_accvgpr_write_b32 a28, v88
Mvav 172,128,128
buffer_store_dwordx4 a[192:195], v1, s[8:11], s36 offen offset:0 sc1
Ra 156,242,15360
A 199,199,224
v_accvgpr_write_b32 a29, v89
Mvav 160,136,48
X 196
v_accvgpr_write_b32 a30, v90
v_accvgpr_write_b32 a31, v91
Mvav 160,140,56
X 197
X 198
Mvav 164,140,88
buffer_store_dwordx4 a[200:203], v1, s[8:11], s36 offen offset:1024 sc1
v_accvgpr_write_b32 a32, v96
K 192,196,197
v_accvgpr_write_b32 a33, v97
Mvav 164,136,80
X 199
A 204,204,228
v_accvgpr_write_b32 a34, v98
Mvav 168,136,112
K 193,198,199
v_accvgpr_write_b32 a35, v99
A 205,205,228
A 206,206,228
Mvav 168,140,120
buffer_store_dwordx4 a[208:211], v1, s[8:11], s36 offen offset:2048 sc1
v_accvgpr_write_b32 a36, v104
v_add_u32 v8, s80, v1
A 207,207,228
Mvav 172,140,152
X 204
v_accvgpr_write_b32 a37, v105
v_accvgpr_write_b32 a38, v106
Mvav 172,136,144
W 0
X 205
X 206
Cvav 176,144,32,224
Ra 128,242,24576
v_accvgpr_write_b32 a39, v107
v_accvgpr_write_b32 a40, v112
K 200,204,205
Mvav 176,148,40
buffer_store_dwordx4 a[216:219], v1, s[8:11], s36 offen offset:3072 sc1
s_add_u32 s36, s29, 69632
X 207
v_accvgpr_write_b32 a41, v113
Cvav 180,148,72,228
Ra 132,242,25600
A 212,212,232
v_accvgpr_write_b32 a42, v114
K 201,206,207
Mvav 180,144,64
A 213,213,232
v_accvgpr_write_b32 a43, v115
A 214,214,232
A 215,215,232
Cvav 184,144,96,232
v_accvgpr_write_b32 a44, v120
X 212
v_accvgpr_write_b32 a45, v121
Mvav 184,148,104
buffer_store_dwordx4 a[224:227], v1, s[8:11], s36 offen offset:0 sc1
X 213
v_accvgpr_write_b32 a46, v122
Cvav 188,148,136,236
X 214
v_accvgpr_write_b32 a47, v123
K 208,212,213
Mvav 188,144,128
v_accvgpr_write_b32 a48, v128
X 215
v_accvgpr_write_b32 a49, v129
Mvav 176,152,48
buffer_store_dwordx4 a[232:235], v1, s[8:11], s36 offen offset:1024 sc1
A 220,220,236
v_accvgpr_write_b32 a50, v130
K 209,214,215
Mvav 176,156,56
A 221,221,236
v_accvgpr_write_b32 a51, v131
A 222,222,236
A 223,223,236
Mvav 180,156,88
Ra 136,242,26624
Ra 140,242,27648
v_accvgpr_write_b32 a52, v136
v_accvgpr_write_b32 a53, v137
Mvav 180,152,80
X 220
X 221
Mvav 184,152,112
v_accvgpr_write_b32 a54, v138
v_accvgpr_write_b32 a55, v139
v_accvgpr_write_b32 a56, v144
v_accvgpr_write_b32 a57, v145
v_accvgpr_write_b32 a58, v146
v_accvgpr_write_b32 a59, v147
v_accvgpr_write_b32 a60, v152
v_accvgpr_write_b32 a61, v153
v_accvgpr_write_b32 a62, v154
v_accvgpr_write_b32 a63, v155
Mvav 184,156,120
s_barrier
buffer_store_dwordx4 a[240:243], v1, s[8:11], s36 offen offset:2048 sc1
X 222
K 216,220,221
Mvav 188,156,152
X 223
A 36,36,224
K 217,222,223
A 37,37,224
A 38,38,224
A 39,39,224
X 36
X 37
X 38
K 194,36,37
X 39
A 68,68,228
K 195,38,39
A 69,69,228
A 70,70,228
A 71,71,228
X 68
X 69
X 70
K 202,68,69
X 71
A 100,100,232
K 203,70,71
A 101,101,232
A 102,102,232
A 103,103,232
X 100
X 101
X 102
K 210,100,101
X 103
A 132,132,236
K 211,102,103
A 133,133,236
A 134,134,236
A 135,135,236
X 132
X 133
X 134
K 218,132,133
X 135
s_nop 0
K 219,134,135
Mvav 188,152,144
W 0
buffer_store_dwordx4 a[248:251], v1, s[8:11], s36 offen offset:3072 sc1
s_add_u32 s36, s29, 393216
Ra 144,242,28672
Ra 148,242,29696
Zvav 32,128,192
buffer_store_dwordx4 a[196:199], v1, s[8:11], s36 offen offset:0 sc1
Ra 152,242,30720
Ra 156,242,31744
Zvav 64,128,200
buffer_store_dwordx4 a[204:207], v1, s[8:11], s36 offen offset:1024 sc1
Zvav 96,128,208
s_and_b32 s80, s14, 1
s_xor_b32 s62, s62, 0x10000
buffer_store_dwordx4 a[212:215], v1, s[8:11], s36 offen offset:2048 sc1
Zvav 128,128,216
s_lshl_b32 s80, s80, 14
s_add_u32 s80, s80, 131072
buffer_store_dwordx4 a[220:223], v1, s[8:11], s36 offen offset:3072 sc1
s_add_u32 s36, s29, 397312
v_add_u32 v23, 61440, v21
Zvav 132,132,216
buffer_store_dwordx4 a[228:231], v1, s[8:11], s36 offen offset:0 sc1
v_add_u32 v12, s80, v1
Ra 64,12,8192
v_add_u32 v24, 61440, v22
Zvav 100,132,208
Ra 68,12,9216
Ra 72,12,10240
buffer_store_dwordx4 a[236:239], v1, s[8:11], s36 offen offset:1024 sc1
v_add_u32 v25, 122880, v21
Zvav 68,132,200
Ra 76,12,11264
Ra 80,12,12288
Ra 84,12,13312
v_add_u32 v26, 122880, v22
Zvav 36,132,192
Ra 88,12,14336
Ra 92,12,15360
Ra 96,12,0
Ra 100,12,1024
Zvav 40,136,192
Ra 104,12,2048
buffer_store_dwordx4 a[244:247], v1, s[8:11], s36 offen offset:2048 sc1
Ra 108,12,3072
Ra 112,12,4096
Zvav 72,136,200
Ra 116,12,5120
Ra 120,12,6144
X 172
Zvav 104,136,208
Ra 124,12,7168
buffer_store_dwordx4 a[252:255], v1, s[8:11], s36 offen offset:3072 sc1
X 173
Zvav 136,136,216
X 174
K 220,172,173
Zvav 140,140,216
X 175
X 168
Zvav 108,140,208
K 221,174,175
X 169
Zvav 76,140,200
s_waitcnt vmcnt(16) lgkmcnt(0)
s_barrier
v_xor_b32 v242, 0x10000, v242
ds_read_b128 v[248:251], v8
Zvav 44,140,192
Ra 128,242,0
X 170
K 212,168,169
Zvav 48,144,192
X 171
X 164
Zvav 80,144,200
K 213,170,171
X 165
Zvav 112,144,208
Ra 132,242,1024
X 166
K 204,164,165
Zvav 144,144,216
X 167
X 160
Zvav 148,148,216
K 205,166,167
X 161
Zvav 116,148,208
Ra 136,242,2048
X 162
Zvav 84,148,200
W 3
K 196,160,161
X 163
Zvav 52,148,192
X 176
K 197,162,163
Zvav 56,152,192
Ra 140,242,3072
X 177
Zvav 88,152,200
X 178
K 198,176,177
Zvav 120,152,208
X 179
X 180
Zvav 152,152,216
K 199,178,179
Zvav 156,156,216
X 181
X 182
Zvav 124,156,208
K 206,180,181
X 183
Zvav 92,156,200
K 207,182,183
Zvav 60,156,192
s_waitcnt vmcnt(0)
s_add_u32 s80, s71, 128
v_mov_b64 v[28:29], s[12:13]
s_mov_b64 exec, 1
buffer_store_dwordx2 v[28:29], off, s[8:11], s80 sc1
s_mov_b64 exec, -1
s_branch .Lsp1_36%=
.p2align 12
.Lmoe_44%=:
W 0
Cvaa 160,128,0,224
s_cmp_eq_u32 s14, 0
s_cselect_b32 s20, s30, s32
s_cselect_b32 s21, s31, s33
Ra 144,242,4096
Ra 148,242,5120
Mvaa 160,132,4
s_xor_b32 s62, s62, 0x10000
)"
R"(Ra 152,242,6144
Cvaa 164,132,20,228
X 184
Mvaa 164,128,16
s_and_b32 s21, s21, 0xffff
X 185
Cvaa 168,128,32,232
X 186
Mvaa 168,132,36
Ra 156,242,7168
K 214,184,185
Cvaa 172,132,52,236
s_mov_b32 s22, 2211840
X 187
Mvaa 172,128,48
X 188
Mvaa 160,136,8
Ra 128,242,16384
Mvaa 160,140,12
s_mov_b32 s23, 0x20000
K 215,186,187
Mvaa 164,140,28
X 189
Mvaa 164,136,24
X 190
Mvaa 168,136,40
Ra 132,242,17408
v_lshrrev_b32 v4, 6, v1
Mvaa 168,140,44
K 222,188,189
Mvaa 172,140,60
X 191
Mvaa 172,136,56
v_mul_u32_u24 v4, 3456, v4
K 223,190,191
v_smfmac_f32_16x16x64_bf16 a[160:163], a[176:179], v[192:199], v240
Ra 136,242,18432
v_smfmac_f32_16x16x64_bf16 a[164:167], a[176:179], v[200:207], v240
X 160
v_smfmac_f32_16x16x64_bf16 a[168:171], a[176:179], v[208:215], v240
v_lshrrev_b32 v5, 4, v1
v_smfmac_f32_16x16x64_bf16 a[172:175], a[176:179], v[216:223], v240
W 3
X 161
Cvaa 176,144,0,224
X 162
Mvaa 176,148,4
Ra 140,242,19456
v_xor_b32 v5, v5, v1
Cvaa 180,148,20,228
K 192,160,161
Mvaa 180,144,16
X 163
Cvaa 184,144,32,232
v_and_b32 v5, 32, v5
Mvaa 184,148,36
X 164
Cvaa 188,148,52,236
K 193,162,163
v_and_b32 v6, 16, v1
Mvaa 188,144,48
X 165
Mvaa 176,152,8
X 166
Mvaa 176,156,12
Ra 144,242,20480
Ra 148,242,21504
Mvaa 180,156,28
v_add3_u32 v4, v4, v5, v6
K 200,164,165
Mvaa 180,152,24
X 167
Mvaa 184,152,40
X 168
Mvaa 184,156,44
v_add_u32 v5, 54272, v4
K 201,166,167
Mvaa 188,156,60
X 169
Mvaa 188,152,56
W 2
Ra 152,242,22528
v_add_u32 v6, 108544, v4
Mvav 32,128,196
X 170
Mvav 64,128,204
Ra 156,242,23552
v_add_u32 v7, 162816, v4
Mvav 96,128,212
K 208,168,169
Mvav 128,128,220
X 171
Mvav 132,132,220
s_lshl_b32 s94, s85, 6
X 172
Mvav 100,132,212
s_add_u32 s64, s64, 1
Ra 128,242,8192
K 209,170,171
Mvav 68,132,204
X 173
Mvav 36,132,196
s_mul_i32 s95, s14, 20480
Mvav 40,136,196
X 174
Mvav 72,136,204
Ra 132,242,9216
K 216,172,173
Mvav 104,136,212
s_add_u32 s95, s95, 98304
X 175
Mvav 136,136,220
Mvav 140,140,220
s_add_u32 m0, s95, 0
X 176
Mvav 108,140,212
Ra 136,242,10240
K 217,174,175
Mvav 76,140,204
X 177
Mvav 44,140,196
W 3
D 4,20,94,0
Mvav 48,144,196
X 178
Mvav 80,144,204
Ra 140,242,11264
Mvav 112,144,212
D 5,20,94,1024
K 194,176,177
Mvav 144,144,220
X 179
Mvav 148,148,220
X 180
Mvav 116,148,212
Ra 144,242,12288
D 6,20,94,2048
Mvav 84,148,204
K 195,178,179
Mvav 52,148,196
X 181
Mvav 56,152,196
D 7,20,94,3072
Ra 148,242,13312
Mvav 88,152,204
X 182
Mvav 120,152,212
s_add_u32 s94, s94, 221184
K 202,180,181
Mvav 152,152,220
X 183
Mvav 156,156,220
X 184
Mvav 124,156,212
s_add_u32 m0, s95, 4096
Ra 152,242,14336
Mvav 92,156,204
K 203,182,183
Mvav 60,156,196
W 3
X 185
Cvaa 160,128,0,224
D 4,20,94,0
Ra 156,242,15360
Mvaa 160,132,4
X 186
Cvaa 164,132,20,228
K 210,184,185
Mvaa 164,128,16
X 187
Cvaa 168,128,32,232
D 5,20,94,1024
Mvaa 168,132,36
X 188
Cvaa 172,132,52,236
D 6,20,94,2048
K 211,186,187
Mvaa 172,128,48
X 189
Mvaa 160,136,8
Ra 128,242,24576
Ra 132,242,25600
Mvaa 160,140,12
X 190
Mvaa 164,140,28
D 7,20,94,3072
K 218,188,189
Mvaa 164,136,24
s_add_u32 s94, s94, 221184
X 191
Mvaa 168,136,40
Mvaa 168,140,44
X 160
Mvaa 172,140,60
s_add_u32 m0, s95, 8192
K 219,190,191
Mvaa 172,136,56
W 2
X 161
Cvaa 176,144,0,224
Ra 136,242,26624
Ra 140,242,27648
Mvaa 176,148,4
X 162
Cvaa 180,148,20,228
D 4,20,94,0
K 196,160,161
Mvaa 180,144,16
X 163
Cvaa 184,144,32,232
D 5,20,94,1024
Mvaa 184,148,36
X 164
Cvaa 188,148,52,236
K 197,162,163
Mvaa 188,144,48
D 6,20,94,2048
Ra 144,242,28672
Mvaa 176,152,8
X 165
Mvaa 176,156,12
Ra 148,242,29696
D 7,20,94,3072
Mvaa 180,156,28
X 166
Mvaa 180,152,24
K 204,164,165
Mvaa 184,152,40
s_add_u32 s94, s94, 221184
X 167
Mvaa 184,156,44
X 168
Mvaa 188,156,60
Mvaa 188,152,56
W 2
s_add_u32 m0, s95, 12288
K 205,166,167
Mvav 32,128,192
X 169
Mvav 64,128,200
Ra 152,242,30720
Ra 156,242,31744
Mvav 96,128,208
X 170
Mvav 128,128,216
D 4,20,94,0
K 212,168,169
Mvav 132,132,216
X 171
Mvav 100,132,208
D 5,20,94,1024
Mvav 68,132,200
X 172
Mvav 36,132,192
K 213,170,171
Mvav 40,136,192
X 173
Mvav 72,136,200
D 6,20,94,2048
Mvav 104,136,208
X 174
Mvav 136,136,216
D 7,20,94,3072
K 220,172,173
Mvav 140,140,216
X 175
Mvav 108,140,208
s_add_u32 s94, s94, 221184
X 176
Mvav 76,140,200
W 0
s_barrier
v_xor_b32 v242, 0x10000, v242
Mvav 44,140,192
Ra 128,242,0
K 221,174,175
Mvav 48,144,192
X 177
Mvav 80,144,200
v_bfe_u32 v11, v1, 4, 3
Mvav 112,144,208
X 178
Mvav 144,144,216
Ra 132,242,1024
v_lshlrev_b32 v11, 3, v11
Mvav 148,148,216
K 198,176,177
Mvav 116,148,208
X 179
Mvav 84,148,200
X 180
Mvav 52,148,192
Ra 136,242,2048
buffer_load_dwordx2 v[8:9], v11, s[8:11], 0 offen offset:3840 sc1
Mvav 56,152,192
Ra 140,242,3072
K 199,178,179
Mvav 88,152,200
X 181
Mvav 120,152,208
X 182
Mvav 152,152,216
K 206,180,181
Mvav 156,156,216
X 183
Mvav 124,156,208
Mvav 92,156,200
K 207,182,183
Mvav 60,156,192
W 0
Cvaa 160,128,0,224
s_add_u32 m0, s95, 16384
Ra 144,242,4096
Ra 148,242,5120
Mvaa 160,132,4
s_xor_b32 s62, s62, 0x10000
Ra 152,242,6144
Cvaa 164,132,20,228
X 184
Mvaa 164,128,16
X 185
Cvaa 168,128,32,232
X 186
Mvaa 168,132,36
D 4,20,94,0
K 214,184,185
Cvaa 172,132,52,236
Ra 156,242,7168
Mvaa 172,128,48
X 187
Mvaa 160,136,8
X 188
Mvaa 160,140,12
D 5,20,94,1024
Ra 128,242,16384
Mvaa 164,140,28
K 215,186,187
Mvaa 164,136,24
X 189
Mvaa 168,136,40
X 190
Mvaa 168,140,44
D 6,20,94,2048
Ra 132,242,17408
Mvaa 172,140,60
K 222,188,189
Mvaa 172,136,56
X 191
v_smfmac_f32_16x16x64_bf16 a[160:163], a[176:179], v[192:199], v240
Ra 136,242,18432
K 223,190,191
v_smfmac_f32_16x16x64_bf16 a[164:167], a[176:179], v[200:207], v240
D 7,20,94,3072
v_smfmac_f32_16x16x64_bf16 a[168:171], a[176:179], v[208:215], v240
s_add_u32 s94, s94, 221184
v_mul_u32_u24 v10, 160, v11
v_smfmac_f32_16x16x64_bf16 a[172:175], a[176:179], v[216:223], v240
W 3
X 160
Cvaa 176,144,0,224
X 161
Mvaa 176,148,4
Ra 140,242,19456
v_lshrrev_b32 v12, 7, v1
Cvaa 180,148,20,228
X 162
Mvaa 180,144,16
K 192,160,161
v_lshl_add_u32 v10, v12, 4, v10
Cvaa 184,144,32,232
s_mul_i32 s82, s14, 5120
X 163
Mvaa 184,148,36
X 164
Cvaa 188,148,52,236
K 193,162,163
Mvaa 188,144,48
s_mul_i32 s81, s14, 640
X 165
Mvaa 176,152,8
X 166
Mvaa 176,156,12
s_add_u32 s81, s81, 118784
Ra 144,242,20480
Ra 148,242,21504
Mvaa 180,156,28
K 200,164,165
Mvaa 180,152,24
X 167
Mvaa 184,152,40
s_waitcnt vmcnt(4)
v_cmp_eq_u64 vcc, s[12:13], v[8:9]
s_cmp_eq_u64 vcc, -1
s_cbranch_scc0 .Lspin_51%=
.Lok_52%=:
X 168
Mvaa 184,156,44
K 201,166,167
Mvaa 188,156,60
s_add_u32 m0, s82, 53248
)"
R"(X 169
Mvaa 188,152,56
W 2
Ra 152,242,22528
Mvav 32,128,196
X 170
Mvav 64,128,204
Ra 156,242,23552
Dc 10,8,81,0
Mvav 96,128,212
K 208,168,169
Mvav 128,128,220
X 171
Mvav 132,132,220
s_add_u32 m0, s82, 54144
X 172
Mvav 100,132,212
s_add_u32 s64, s64, 1
Ra 128,242,8192
K 209,170,171
Mvav 68,132,204
X 173
Mvav 36,132,196
Dc 10,8,81,128
Mvav 40,136,196
X 174
Mvav 72,136,204
s_add_u32 m0, s82, 55040
Ra 132,242,9216
K 216,172,173
Mvav 104,136,212
X 175
Mvav 136,136,220
Dc 10,8,81,256
Mvav 140,140,220
X 176
Mvav 108,140,212
Ra 136,242,10240
K 217,174,175
Mvav 76,140,204
s_add_u32 m0, s82, 55936
X 177
Mvav 44,140,196
W 3
Mvav 48,144,196
X 178
Mvav 80,144,204
Ra 140,242,11264
Dc 10,8,81,384
Mvav 112,144,212
K 194,176,177
Mvav 144,144,220
s_add_u32 m0, s82, 56832
X 179
Mvav 148,148,220
X 180
Mvav 116,148,212
Ra 144,242,12288
Mvav 84,148,204
Dc 10,8,81,512
K 195,178,179
Mvav 52,148,196
X 181
Mvav 56,152,196
X 182
Mvav 88,152,204
Ra 148,242,13312
Mvav 120,152,212
K 202,180,181
Mvav 152,152,220
X 183
Mvav 156,156,220
X 184
Mvav 124,156,212
Ra 152,242,14336
Mvav 92,156,204
K 203,182,183
Mvav 60,156,196
W 3
X 185
Cvaa 160,128,0,224
X 186
Mvaa 160,132,4
Ra 156,242,15360
Cvaa 164,132,20,228
K 210,184,185
Mvaa 164,128,16
X 187
Cvaa 168,128,32,232
X 188
Mvaa 168,132,36
Cvaa 172,132,52,236
K 211,186,187
Mvaa 172,128,48
X 189
Mvaa 160,136,8
Ra 128,242,24576
Ra 132,242,25600
Mvaa 160,140,12
X 190
Mvaa 164,140,28
K 218,188,189
Mvaa 164,136,24
X 191
Mvaa 168,136,40
Mvaa 168,140,44
Mvaa 172,140,60
X 160
Mvaa 172,136,56
W 2
Ra 136,242,26624
K 219,190,191
Cvaa 176,144,0,224
X 161
Mvaa 176,148,4
Ra 140,242,27648
v_lshrrev_b32 v8, 8, v1
Cvaa 180,148,20,228
X 162
Mvaa 180,144,16
K 196,160,161
Cvaa 184,144,32,232
X 163
Mvaa 184,148,36
v_bfe_u32 v9, v1, 6, 2
Cvaa 188,148,52,236
X 164
Mvaa 188,144,48
K 197,162,163
v_lshl_add_u32 v9, v8, 3, v9
Mvaa 176,152,8
X 165
Mvaa 176,156,12
Ra 144,242,28672
Ra 148,242,29696
Mvaa 180,156,28
X 166
Mvaa 180,152,24
K 204,164,165
v_lshlrev_b32 v11, 6, v9
Mvaa 184,152,40
X 167
Mvaa 184,156,44
X 168
Mvaa 188,156,60
v_xor_b32 v9, s14, v8
Mvaa 188,152,56
W 2
Ra 152,242,30720
K 205,166,167
Mvav 32,128,192
X 169
Mvav 64,128,200
Ra 156,242,31744
v_and_b32 v9, 1, v9
Mvav 96,128,208
X 170
Mvav 128,128,216
K 212,168,169
Mvav 132,132,216
X 171
Mvav 100,132,208
v_lshl_add_u32 v11, v9, 5, v11
Mvav 68,132,200
X 172
Mvav 36,132,192
K 213,170,171
Mvav 40,136,192
X 173
Mvav 72,136,200
v_bfe_u32 v9, v1, 4, 2
Mvav 104,136,208
X 174
Mvav 136,136,216
K 220,172,173
v_lshl_add_u32 v12, v9, 3, v11
Mvav 140,140,216
X 175
Mvav 108,140,208
X 176
Mvav 76,140,200
s_waitcnt vmcnt(0) lgkmcnt(0)
s_barrier
v_xor_b32 v242, 0x10000, v242
Mvav 44,140,192
v_add_u32 v11, 98304, v12
K 221,174,175
Mvav 48,144,192
Ra 128,242,0
Mvav 80,144,200
X 177
Mvav 112,144,208
X 178
Mvav 144,144,216
Ra 132,242,1024
v_and_b32 v13, 0x70, v1
Mvav 148,148,216
K 198,176,177
Mvav 116,148,208
X 179
Mvav 84,148,200
Ra 136,242,2048
v_lshl_add_u32 v13, v8, 7, v13
Mvav 52,148,192
X 180
Mvav 56,152,192
Ra 140,242,3072
K 199,178,179
Mvav 88,152,200
X 181
Mvav 120,152,208
X 182
Mvav 152,152,216
K 206,180,181
Mvav 156,156,216
X 183
Mvav 124,156,208
Mvav 92,156,200
K 207,182,183
Mvav 60,156,192
W 0
Cvaa 160,128,0,224
s_cmp_eq_u32 s14, 0
s_cselect_b32 s95, 106496, 0
Ra 144,242,4096
Ra 148,242,5120
Mvaa 160,132,4
s_xor_b32 s62, s62, 0x10000
X 184
Cvaa 164,132,20,228
Ta 184,11,0
Ta 186,11,256
Mvaa 164,128,16
s_add_u32 s95, s95, 32768
Ta 188,11,20480
Ta 190,11,20736
Cvaa 168,128,32,232
Ra 192,13,53248
Ta 196,11,2048
Mvaa 168,132,36
s_add_u32 m0, s95, 0
X 185
Cvaa 172,132,52,236
Ta 198,11,2304
Ta 200,11,22528
Mvaa 172,128,48
W 3
X 186
Zaaa 232,184,192
Ta 202,11,22784
K 214,184,185
Zaaa 236,188,192
Ra 204,13,53760
Ra 152,242,6144
Mvaa 160,136,8
X 187
Mvaa 160,140,12
Ta 208,11,4096
D 4,20,94,0
Mvaa 164,140,28
X 188
Mvaa 164,136,24
Ta 210,11,4352
D 5,20,94,1024
Mvaa 168,136,40
Ra 156,242,7168
K 215,186,187
Mvaa 168,140,44
X 189
Mvaa 172,140,60
Ta 212,11,24576
D 6,20,94,2048
Mvaa 172,136,56
X 190
v_smfmac_f32_16x16x64_bf16 a[160:163], a[176:179], v[192:199], v240
W 5
Ta 214,11,24832
D 7,20,94,3072
Maaa 232,196,204
s_add_u32 s94, s94, 221184
Ra 216,13,54272
K 222,188,189
Maaa 236,200,204
s_add_u32 m0, s95, 4096
X 191
v_smfmac_f32_16x16x64_bf16 a[164:167], a[176:179], v[200:207], v240
X 160
K 223,190,191
v_smfmac_f32_16x16x64_bf16 a[168:171], a[176:179], v[208:215], v240
Ra 128,242,16384
Ta 220,11,6144
v_smfmac_f32_16x16x64_bf16 a[172:175], a[176:179], v[216:223], v240
W 5
Ta 222,11,6400
Ra 132,242,17408
Cvaa 176,144,0,224
X 161
Mvaa 176,148,4
D 4,20,94,0
Ta 224,11,26624
Cvaa 180,148,20,228
X 162
Mvaa 180,144,16
D 5,20,94,1024
K 192,160,161
Cvaa 184,144,32,232
Ta 226,11,26880
Ra 228,13,54784
Mvaa 184,148,36
W 7
X 163
Maaa 232,208,216
D 6,20,94,2048
Ra 136,242,18432
Maaa 236,212,216
X 164
Cvaa 188,148,52,236
D 7,20,94,3072
Ta 184,11,8192
Mvaa 188,144,48
s_add_u32 s94, s94, 221184
Ra 140,242,19456
K 193,162,163
Mvaa 176,152,8
s_add_u32 m0, s95, 8192
X 165
Mvaa 176,156,12
Ta 186,11,8448
Ta 188,11,28672
Mvaa 180,156,28
X 166
Mvaa 180,152,24
Ra 144,242,20480
K 200,164,165
Mvaa 184,152,40
X 167
Mvaa 184,156,44
Ta 190,11,28928
D 4,20,94,0
Mvaa 188,156,60
W 7
X 168
Maaa 232,220,228
s_add_u32 s64, s64, 1
Ra 192,13,55296
D 5,20,94,1024
Maaa 236,224,228
Ta 196,11,10240
K 201,166,167
Mvaa 188,152,56
W 6
X 169
Mvav 32,128,196
D 6,20,94,2048
Ra 148,242,21504
Mvav 64,128,204
X 170
Mvav 96,128,212
Ta 198,11,10496
D 7,20,94,3072
Mvav 128,128,220
s_add_u32 s94, s94, 221184
Ta 200,11,30720
K 208,168,169
Mvav 132,132,220
s_add_u32 m0, s95, 12288
X 171
Mvav 100,132,212
Ra 152,242,22528
Ta 202,11,30976
Mvav 68,132,204
X 172
Mvav 36,132,196
W 6
Ra 204,13,55808
K 209,170,171
Maaa 232,184,192
X 173
Maaa 236,188,192
D 4,20,94,0
Ra 156,242,23552
Mvav 40,136,196
X 174
Mvav 72,136,204
Ta 208,11,12288
D 5,20,94,1024
Mvav 104,136,212
Ta 210,11,12544
K 216,172,173
Mvav 136,136,220
X 175
Mvav 140,140,220
D 6,20,94,2048
Ra 128,242,8192
Mvav 108,140,212
Ta 212,11,32768
Ta 214,11,33024
Mvav 76,140,204
X 176
Mvav 44,140,196
)"
R"(W 5
D 7,20,94,3072
K 217,174,175
Mvav 48,144,196
s_add_u32 s94, s94, 221184
X 177
Maaa 232,196,204
s_add_u32 m0, s95, 16384
Ra 132,242,9216
Ra 216,13,56320
Maaa 236,200,204
X 178
Mvav 80,144,204
Ta 220,11,14336
K 194,176,177
Mvav 112,144,212
X 179
Mvav 144,144,220
D 4,20,94,0
Ta 222,11,14592
Mvav 148,148,220
Ra 136,242,10240
Ta 224,11,34816
Mvav 116,148,212
X 180
Mvav 84,148,204
D 5,20,94,1024
K 195,178,179
Mvav 52,148,196
X 181
Mvav 56,152,196
D 6,20,94,2048
Ta 226,11,35072
Mvav 88,152,204
W 5
Ra 140,242,11264
Ra 228,13,56832
Maaa 232,208,216
X 182
Maaa 236,212,216
D 7,20,94,3072
K 202,180,181
Mvav 120,152,212
s_add_u32 s94, s94, 221184
X 183
Mvav 152,152,220
Ta 184,11,16384
Ra 144,242,12288
Mvav 156,156,220
X 184
Mvav 124,156,212
Ta 186,11,16640
K 203,182,183
Mvav 92,156,204
X 185
Mvav 60,156,196
W 4
Ta 188,11,36864
Ra 148,242,13312
Cvaa 160,128,0,224
X 186
Mvaa 160,132,4
Ta 190,11,37120
K 210,184,185
Cvaa 164,132,20,228
W 6
Ra 192,13,57344
Ta 196,11,18432
Maaa 232,220,228
X 187
Maaa 236,224,228
X 188
Mvaa 164,128,16
Ra 152,242,14336
K 211,186,187
Cvaa 168,128,32,232
Ta 198,11,18688
Ta 200,11,38912
Mvaa 168,132,36
X 189
Cvaa 172,132,52,236
X 190
Mvaa 172,128,48
Ra 156,242,15360
K 218,188,189
Mvaa 160,136,8
Ta 202,11,39168
Ra 204,13,57856
Mvaa 160,140,12
X 191
Mvaa 164,140,28
Ra 128,242,24576
Mvaa 164,136,24
W 8
Maaa 232,184,192
Ra 132,242,25600
Maaa 236,188,192
X 160
Mvaa 168,136,40
K 219,190,191
Mvaa 168,140,44
X 161
Mvaa 172,140,60
X 162
Mvaa 172,136,56
W 4
Ra 136,242,26624
K 196,160,161
Cvaa 176,144,0,224
X 163
Mvaa 176,148,4
X 164
Cvaa 180,148,20,228
Ra 140,242,27648
K 197,162,163
Mvaa 180,144,16
X 165
Cvaa 184,144,32,232
W 4
Maaa 232,196,204
X 166
Maaa 236,200,204
Mvaa 184,148,36
K 204,164,165
Cvaa 188,148,52,236
X 167
Mvaa 188,144,48
Ra 144,242,28672
Mvaa 176,152,8
X 168
Mvaa 176,156,12
Ra 148,242,29696
Mvaa 180,156,28
K 205,166,167
Mvaa 180,152,24
X 169
Mvaa 184,152,40
Mvaa 184,156,44
X 170
Mvaa 188,156,60
K 212,168,169
Mvaa 188,152,56
W 2
X 171
Mvav 32,128,192
Ra 152,242,30720
Ra 156,242,31744
Mvav 64,128,200
X 172
Mvav 96,128,208
Mvav 128,128,216
K 213,170,171
Mvav 132,132,216
X 173
Mvav 100,132,208
Mvav 68,132,200
X 174
Mvav 36,132,192
Mvav 40,136,192
K 220,172,173
Mvav 72,136,200
X 175
Mvav 104,136,208
Mvav 136,136,216
X 176
Mvav 140,140,216
Mvav 108,140,208
K 221,174,175
Mvav 76,140,200
s_waitcnt vmcnt(0) lgkmcnt(0)
s_barrier
v_xor_b32 v242, 0x10000, v242
Mvav 44,140,192
X 177
Mvav 48,144,192
X 178
Mvav 80,144,200
Ra 128,242,0
K 198,176,177
Mvav 112,144,208
X 179
Mvav 144,144,216
Ra 132,242,1024
Mvav 148,148,216
X 180
Mvav 116,148,208
Ra 136,242,2048
Mvav 84,148,200
K 199,178,179
Mvav 52,148,192
X 181
Mvav 56,152,192
Ra 140,242,3072
Mvav 88,152,200
X 182
Mvav 120,152,208
Mvav 152,152,216
K 206,180,181
Mvav 156,156,216
X 183
Mvav 124,156,208
Mvav 92,156,200
K 207,182,183
Mvav 60,156,192
W 0
Cvaa 160,128,0,224
s_mov_b32 s20, s34
Ra 144,242,4096
Ra 148,242,5120
Mvaa 160,132,4
s_xor_b32 s62, s62, 0x10000
X 184
Cvaa 164,132,20,228
s_and_b32 s21, s35, 0xffff
Ta 184,11,40960
Ta 186,11,41216
Mvaa 164,128,16
Ta 188,12,32768
Ta 190,12,33024
Cvaa 168,128,32,232
s_mov_b32 s22, 2211840
Ra 192,13,58368
Ta 196,11,43008
Mvaa 168,132,36
s_mov_b32 s23, 0x20000
X 185
Cvaa 172,132,52,236
Ta 198,11,43264
Ta 200,12,34816
Mvaa 172,128,48
W 3
X 186
Maaa 232,184,192
Ta 202,12,35072
K 214,184,185
Maaa 236,188,192
Ra 204,13,58880
Ra 152,242,6144
Mvaa 160,136,8
X 187
Mvaa 160,140,12
Ta 208,11,45056
v_lshrrev_b32 v4, 8, v1
Mvaa 164,140,28
X 188
Mvaa 164,136,24
Ta 210,11,45312
v_lshlrev_b32 v5, 5, v4
Mvaa 168,136,40
v_xor_b32 v5, v5, v1
K 215,186,187
Mvaa 168,140,44
Ra 156,242,7168
Ta 212,12,36864
Mvaa 172,140,60
X 189
Mvaa 172,136,56
v_and_b32 v5, 0xf0, v5
v_mul_u32_u24 v4, 1280, v4
v_smfmac_f32_16x16x64_bf16 a[160:163], a[176:179], v[192:199], v240
W 5
X 190
Maaa 232,196,204
Ta 214,12,37120
v_add_u32 v4, v4, v5
Maaa 236,200,204
Ra 216,13,59392
K 222,188,189
v_smfmac_f32_16x16x64_bf16 a[164:167], a[176:179], v[200:207], v240
X 191
X 160
K 223,190,191
v_smfmac_f32_16x16x64_bf16 a[168:171], a[176:179], v[208:215], v240
Ra 128,242,16384
v_xor_b32 v5, 128, v4
v_smfmac_f32_16x16x64_bf16 a[172:175], a[176:179], v[216:223], v240
W 4
Ta 220,11,47104
v_add_u32 v6, 8192, v4
Cvaa 176,144,0,224
Ta 222,11,47360
v_add_u32 v7, 12288, v5
Mvaa 176,148,4
Ra 132,242,17408
v_add_u32 v5, 4096, v5
Cvaa 180,148,20,228
s_mul_i32 s94, s85, 40960
X 161
Mvaa 180,144,16
s_mul_i32 s81, s14, 20480
Ta 224,12,38912
Ta 226,12,39168
Cvaa 184,144,32,232
s_add_u32 s94, s94, s81
X 162
Mvaa 184,148,36
W 6
s_lshl_b32 s95, s14, 12
K 192,160,161
Maaa 232,208,216
s_add_u32 s95, s95, 98304
X 163
Maaa 236,212,216
s_add_u32 m0, s95, 0
Ra 228,13,59904
Ra 136,242,18432
Cvaa 188,148,52,236
X 164
Mvaa 188,144,48
Ta 184,11,49152
K 193,162,163
Mvaa 176,152,8
X 165
Mvaa 176,156,12
Ra 140,242,19456
Ta 186,11,49408
Mvaa 180,156,28
D 4,20,94,0
Ta 188,12,40960
Mvaa 180,152,24
X 166
Mvaa 184,152,40
D 5,20,94,1024
K 200,164,165
Mvaa 184,156,44
X 167
Mvaa 188,156,60
W 5
D 6,20,94,2048
Ra 144,242,20480
Maaa 232,220,228
s_add_u32 s64, s64, 1
Ta 190,12,41216
Ra 192,13,60416
Maaa 236,224,228
D 7,20,94,3072
Ta 196,11,51200
Mvaa 188,152,56
W 6
X 168
Mvav 32,128,196
s_add_u32 s94, s94, 256
s_add_u32 m0, s95, 8192
K 201,166,167
Mvav 64,128,204
X 169
Mvav 96,128,212
D 4,20,94,0
Ra 148,242,21504
Mvav 128,128,220
X 170
Mvav 132,132,220
D 5,20,94,1024
Ta 198,11,51456
Mvav 100,132,212
Ta 200,12,43008
K 208,168,169
Mvav 68,132,204
D 6,20,94,2048
Ra 152,242,22528
Mvav 36,132,196
W 5
X 171
Maaa 232,184,192
D 7,20,94,3072
Ta 202,12,43264
Maaa 236,188,192
s_add_u32 s94, s94, 256
X 172
Mvav 40,136,196
s_add_u32 m0, s95, 16384
Ra 204,13,60928
K 209,170,171
Mvav 72,136,204
X 173
Mvav 104,136,212
Ra 156,242,23552
D 4,20,94,0
Mvav 136,136,220
X 174
Mvav 140,140,220
D 5,20,94,1024
Ta 208,11,53248
Mvav 108,140,212
Ta 210,11,53504
K 216,172,173
Mvav 76,140,204
D 6,20,94,2048
Ra 128,242,8192
Mvav 44,140,196
W 3
X 175
Mvav 48,144,196
D 7,20,94,3072
Ta 212,12,45056
Maaa 232,196,204
s_add_u32 s94, s94, 256
X 176
Maaa 236,200,204
s_add_u32 m0, s95, 24576
)"
R"(Ta 214,12,45312
K 217,174,175
Mvav 80,144,204
D 4,20,94,0
Ra 132,242,9216
Mvav 112,144,212
X 177
Mvav 144,144,220
Ra 216,13,61440
D 5,20,94,1024
Mvav 148,148,220
X 178
Mvav 116,148,212
D 6,20,94,2048
Ta 220,11,55296
Mvav 84,148,204
Ta 222,11,55552
K 194,176,177
Mvav 52,148,196
D 7,20,94,3072
Ra 136,242,10240
Mvav 56,152,196
s_add_u32 s94, s94, 256
X 179
Mvav 88,152,204
W 3
X 180
Maaa 232,208,216
s_add_u32 m0, s95, 32768
Ta 224,12,47104
K 195,178,179
Maaa 236,212,216
D 4,20,94,0
Ta 226,12,47360
Mvav 120,152,212
Ra 140,242,11264
Ra 228,13,61952
Mvav 152,152,220
X 181
Mvav 156,156,220
D 5,20,94,1024
Ta 184,11,57344
Mvav 124,156,212
X 182
Mvav 92,156,204
D 6,20,94,2048
K 202,180,181
Mvav 60,156,196
W 2
X 183
Cvaa 160,128,0,224
D 7,20,94,3072
Ra 144,242,12288
Mvaa 160,132,4
s_add_u32 s94, s94, 256
X 184
Cvaa 164,132,20,228
W 2
Ta 186,11,57600
K 203,182,183
Maaa 232,220,228
X 185
Maaa 236,224,228
Ta 188,12,49152
Ra 148,242,13312
Mvaa 164,128,16
X 186
Cvaa 168,128,32,232
Ta 190,12,49408
K 210,184,185
Mvaa 168,132,36
Ra 192,13,62464
Ta 196,11,59392
Cvaa 172,132,52,236
X 187
Mvaa 172,128,48
X 188
Mvaa 160,136,8
Ra 152,242,14336
K 211,186,187
Mvaa 160,140,12
Ta 198,11,59648
Ta 200,12,51200
Mvaa 164,140,28
X 189
Mvaa 164,136,24
W 4
X 190
Maaa 232,184,192
Ra 156,242,15360
K 218,188,189
Maaa 236,188,192
Ta 202,12,51456
Ra 204,13,62976
Mvaa 168,136,40
X 191
Mvaa 168,140,44
Ra 128,242,24576
Ra 132,242,25600
Mvaa 172,140,60
X 160
Mvaa 172,136,56
W 4
Ra 136,242,26624
K 219,190,191
Cvaa 176,144,0,224
X 161
Mvaa 176,148,4
X 162
Cvaa 180,148,20,228
Ra 140,242,27648
K 196,160,161
Mvaa 180,144,16
X 163
Cvaa 184,144,32,232
W 4
X 164
Maaa 232,196,204
K 197,162,163
Maaa 236,200,204
X 165
Mvaa 184,148,36
X 166
Cvaa 188,148,52,236
K 204,164,165
Mvaa 188,144,48
X 167
Mvaa 176,152,8
Ra 144,242,28672
G 252,232
Mvaa 176,156,12
G 253,233
G 254,234
Mvaa 180,156,28
X 168
Mvaa 180,152,24
G 255,235
G 8,236
Mvaa 184,152,40
Ra 148,242,29696
G 9,237
Mvaa 184,156,44
G 10,238
G 14,239
Mvaa 188,156,60
K 252,252,8
K 253,253,9
Mvaa 188,152,56
W 2
K 205,166,167
K 254,254,10
Mvav 32,128,192
K 255,255,14
v_and_b32 v8, 0xffff0000, v252
Mvav 64,128,200
v_and_b32 v9, 0xffff0000, v253
v_and_b32 v10, 0xffff0000, v254
Mvav 96,128,208
X 169
Mvav 128,128,216
v_and_b32 v14, 0xffff0000, v255
v_lshlrev_b32 v252, 16, v252
Mvav 132,132,216
v_lshlrev_b32 v253, 16, v253
v_lshlrev_b32 v254, 16, v254
Mvav 100,132,208
v_lshlrev_b32 v255, 16, v255
v_mul_f32 v15, 0xbfb8aa3b, v252
Mvav 68,132,200
v_mul_f32 v2, 0xbfb8aa3b, v253
v_mul_f32 v3, 0xbfb8aa3b, v254
Mvav 36,132,192
X 170
Mvav 40,136,192
v_mul_f32 v27, 0xbfb8aa3b, v255
K 212,168,169
Mvav 72,136,200
X 15
Mvav 104,136,208
Ra 152,242,30720
Ra 156,242,31744
Mvav 136,136,216
X 2
X 3
X 27
v_add_f32 v15, 1.0, v15
v_add_f32 v2, 1.0, v2
v_add_f32 v3, 1.0, v3
v_add_f32 v27, 1.0, v27
v_rcp_f32 v15, v15
v_rcp_f32 v2, v2
v_rcp_f32 v3, v3
v_rcp_f32 v27, v27
U 252,252,15
U 253,253,2
U 254,254,3
U 255,255,27
K 252,252,252
K 253,253,253
K 254,254,254
K 255,255,255
v_lshlrev_b32 v252, 16, v252
v_lshlrev_b32 v253, 16, v253
v_lshlrev_b32 v254, 16, v254
v_lshlrev_b32 v255, 16, v255
U 252,252,8
U 253,253,9
U 254,254,10
U 255,255,14
K 246,252,253
K 247,254,255
s_lshl_b32 s81, s14, 3
v_add_u32 v2, s81, v1
s_mov_b32 s86, 0x00ff00ff
s_mov_b32 s87, 0x00ff00ff
v_cndmask_b32_e64 v252, 0, v246, s[86:87]
v_cndmask_b32_e64 v253, 0, v247, s[86:87]
v_cndmask_b32_e64 v246, v246, 0, s[86:87]
v_cndmask_b32_e64 v247, v247, 0, s[86:87]
ds_write_b64 v2, v[252:253] offset:63488
ds_write_b64 v2, v[246:247] offset:64512
Mvav 140,140,216
X 171
Mvav 108,140,208
X 172
Mvav 76,140,200
s_waitcnt vmcnt(0) lgkmcnt(0)
s_barrier
v_xor_b32 v242, 0x10000, v242
Mvav 44,140,192
Ra 128,242,0
K 213,170,171
Mvav 48,144,192
X 173
Mvav 80,144,200
X 174
Mvav 112,144,208
Ra 132,242,1024
K 220,172,173
Mvav 144,144,216
X 175
Mvav 148,148,216
X 176
Mvav 116,148,208
Ra 136,242,2048
K 221,174,175
Mvav 84,148,200
X 177
Mvav 52,148,192
X 178
Mvav 56,152,192
Ra 140,242,3072
K 198,176,177
Mvav 88,152,200
X 179
Mvav 120,152,208
X 180
Mvav 152,152,216
K 199,178,179
Mvav 156,156,216
X 181
Mvav 124,156,208
X 182
Mvav 92,156,200
K 206,180,181
Mvav 60,156,192
X 183
s_nop 0
K 207,182,183
W 0
Cvaa 160,128,0,224
Ra 144,242,4096
Ra 148,242,5120
Mvaa 160,132,4
s_xor_b32 s62, s62, 0x10000
X 184
Cvaa 164,132,20,228
Ra 216,1,63488
Ra 220,1,64512
Mvaa 164,128,16
v_lshrrev_b32 v8, 6, v1
v_and_b32 v9, 7, v8
Cvaa 168,128,32,232
s_lshl_b32 s81, s14, 2
X 185
Mvaa 168,132,36
v_xor_b32 v9, s81, v9
v_lshlrev_b32 v9, 5, v9
Cvaa 172,132,52,236
X 186
Mvaa 172,128,48
v_lshl_add_u32 v4, v8, 8, v9
v_bfe_u32 v9, v1, 4, 2
Mvaa 160,136,8
v_lshl_add_u32 v4, v9, 3, v4
v_add_u32 v4, 98304, v4
Mvaa 160,140,12
K 214,184,185
v_xor_b32 v5, 32, v4
Mvaa 164,140,28
v_xor_b32 v6, 64, v4
v_xor_b32 v7, 96, v4
Ta 184,4,0
Ta 186,4,4096
Mvaa 164,136,24
Ta 188,5,0
Ta 190,5,4096
Mvaa 168,136,40
Ra 152,242,6144
Ta 192,6,0
Mvaa 168,140,44
W 4
X 187
Zaaa 232,184,216
Ta 194,6,4096
v_and_b32 v11, 0x70, v1
Mvaa 172,140,60
v_mul_u32_u24 v11, 160, v11
v_lshrrev_b32 v10, 8, v1
Mvaa 172,136,56
X 188
v_smfmac_f32_16x16x64_bf16 a[160:163], a[176:179], v[192:199], v240
Ra 156,242,7168
W 4
Maaa 232,188,220
K 215,186,187
v_lshl_add_u32 v11, v10, 4, v11
v_smfmac_f32_16x16x64_bf16 a[164:167], a[176:179], v[200:207], v240
X 189
X 190
K 222,188,189
X 191
X 160
K 223,190,191
v_smfmac_f32_16x16x64_bf16 a[168:171], a[176:179], v[208:215], v240
Ta 196,7,0
v_bfe_u32 v10, v1, 7, 1
v_smfmac_f32_16x16x64_bf16 a[172:175], a[176:179], v[216:223], v240
W 2
Ta 198,7,4096
v_lshl_add_u32 v10, v10, 6, v11
Zaaa 236,192,216
W 2
s_mul_i32 s94, s85, 20480
s_lshl_b32 s82, s14, 8
Cvaa 176,144,0,224
s_add_u32 s94, s94, s82
Ta 200,4,8192
Ta 202,4,12288
Mvaa 176,148,4
s_add_u32 s94, s94, 2752512
s_lshl_b32 s71, s85, 4
Ra 128,242,16384
Cvaa 180,148,20,228
W 3
s_lshl_b32 s82, s14, 3
Ta 204,5,8192
Maaa 236,196,220
s_add_u32 s71, s71, s82
s_add_u32 s71, s71, 2560
Ta 206,5,12288
Mvaa 180,144,16
X 161
Cvaa 184,144,32,232
Ra 132,242,17408
Ta 208,6,8192
Mvaa 184,148,36
W 5
X 162
Zaaa 240,200,216
Ta 210,6,12288
K 192,160,161
Cvaa 188,148,52,236
buffer_store_dwordx4 a[232:235], v10, s[8:11], s94 offen offset:0 sc1
Ta 212,7,8192
Mvaa 188,144,48
X 163
Mvaa 176,152,8
W 4
X 164
Maaa 240,204,220
Ra 136,242,18432
Ta 214,7,12288
Mvaa 176,156,12
Ta 184,4,16384
K 193,162,163
Mvaa 180,156,28
X 165
Mvaa 180,152,24
W 4
Ta 186,4,20480
Ra 140,242,19456
Zaaa 244,208,216
X 166
Mvaa 184,152,40
buffer_store_dwordx4 a[236:239], v10, s[8:11], s94 offen offset:128 sc1
K 200,164,165
Mvaa 184,156,44
X 167
Mvaa 188,156,60
)"
R"(W 3
Ta 188,5,16384
Ta 190,5,20480
Maaa 244,212,220
s_add_u32 s64, s64, 1
X 168
Mvaa 188,152,56
W 2
Ra 144,242,20480
K 201,166,167
Mvav 32,128,196
Ta 192,6,16384
Ta 194,6,20480
Mvav 64,128,204
X 169
Zaaa 248,184,216
buffer_store_dwordx4 a[240:243], v10, s[8:11], s94 offen offset:512 sc1
Ta 196,7,16384
Mvav 96,128,212
X 170
Mvav 128,128,220
Ra 148,242,21504
K 208,168,169
Mvav 132,132,220
W 5
Ta 198,7,20480
Ta 200,4,24576
Maaa 248,188,220
X 171
Mvav 100,132,212
X 172
Mvav 68,132,204
Ta 202,4,28672
K 209,170,171
Mvav 36,132,196
W 5
Ra 152,242,22528
buffer_store_dwordx4 a[244:247], v10, s[8:11], s94 offen offset:640 sc1
Zaaa 252,192,216
X 173
Mvav 40,136,196
X 174
Mvav 72,136,204
Ta 204,5,24576
Ta 206,5,28672
Mvav 104,136,212
W 5
Ra 156,242,23552
K 216,172,173
Maaa 252,196,220
X 175
Mvav 136,136,220
Ta 208,6,24576
Ta 210,6,28672
Mvav 140,140,220
X 176
Mvav 108,140,212
W 6
buffer_store_dwordx4 a[248:251], v10, s[8:11], s94 offen offset:1024 sc1
K 217,174,175
Zaaa 232,200,216
X 177
Mvav 76,140,204
Ta 212,7,24576
Ra 128,242,8192
Mvav 44,140,196
W 4
Ta 214,7,28672
Ta 184,4,32768
Mvav 48,144,196
X 178
Maaa 232,204,220
Ra 132,242,9216
K 194,176,177
Mvav 80,144,204
X 179
Mvav 112,144,212
Ta 186,4,36864
buffer_store_dwordx4 a[252:255], v10, s[8:11], s94 offen offset:1152 sc1
Mvav 144,144,220
W 6
X 180
Zaaa 236,208,216
Ta 188,5,32768
K 195,178,179
Mvav 148,148,220
X 181
Mvav 116,148,212
Ta 190,5,36864
Ra 136,242,10240
Mvav 84,148,204
W 6
X 182
Maaa 236,212,220
Ta 192,6,32768
K 202,180,181
Mvav 52,148,196
Ta 194,6,36864
Ra 140,242,11264
Mvav 56,152,196
X 183
Mvav 88,152,204
W 6
buffer_store_dwordx4 a[232:235], v10, s[8:11], s94 offen offset:1536 sc1
Ta 196,7,32768
Zaaa 240,184,216
X 184
Mvav 120,152,212
Ta 198,7,36864
K 203,182,183
Mvav 152,152,220
Ra 144,242,12288
Ra 148,242,13312
Mvav 156,156,220
W 8
X 185
Maaa 240,188,220
X 186
Mvav 124,156,212
buffer_store_dwordx4 a[236:239], v10, s[8:11], s94 offen offset:1664 sc1
K 210,184,185
Mvav 92,156,204
X 187
Mvav 60,156,196
W 5
X 188
Zaaa 244,192,216
W 4
Ra 152,242,14336
K 211,186,187
Cvaa 160,128,0,224
X 189
Mvaa 160,132,4
X 190
Cvaa 164,132,20,228
W 3
Ra 156,242,15360
buffer_store_dwordx4 a[240:243], v10, s[8:11], s94 offen offset:2048 sc1
Maaa 244,196,220
K 218,188,189
Mvaa 164,128,16
X 191
Cvaa 168,128,32,232
Mvaa 168,132,36
Cvaa 172,132,52,236
Mvaa 172,128,48
Ra 128,242,24576
Mvaa 160,136,8
Ra 132,242,25600
buffer_store_dwordx4 a[244:247], v10, s[8:11], s94 offen offset:2176 sc1
Mvaa 160,140,12
Mvaa 164,140,28
Mvaa 164,136,24
Mvaa 168,136,40
Mvaa 168,140,44
Mvaa 172,140,60
Mvaa 172,136,56
W 2
Ra 136,242,26624
Cvaa 176,144,0,224
X 160
Mvaa 176,148,4
Ra 140,242,27648
K 219,190,191
Cvaa 180,148,20,228
X 161
Mvaa 180,144,16
X 162
Cvaa 184,144,32,232
K 196,160,161
Mvaa 184,148,36
X 163
Cvaa 188,148,52,236
X 164
Mvaa 188,144,48
Ra 144,242,28672
K 197,162,163
Mvaa 176,152,8
X 165
Mvaa 176,156,12
X 166
Mvaa 180,156,28
Ra 148,242,29696
K 204,164,165
Mvaa 180,152,24
X 167
Mvaa 184,152,40
X 168
Mvaa 184,156,44
K 205,166,167
Mvaa 188,156,60
X 169
Mvaa 188,152,56
W 2
X 170
Mvav 32,128,192
Ra 152,242,30720
K 212,168,169
Mvav 64,128,200
X 171
Mvav 96,128,208
Ra 156,242,31744
Mvav 128,128,216
X 172
Mvav 132,132,216
K 213,170,171
Mvav 100,132,208
X 173
Mvav 68,132,200
X 174
Mvav 36,132,192
Mvav 40,136,192
K 220,172,173
Mvav 72,136,200
X 175
Mvav 104,136,208
Mvav 136,136,216
X 176
Mvav 140,140,216
Mvav 108,140,208
K 221,174,175
Mvav 76,140,200
W 0
s_barrier
v_xor_b32 v242, 0x10000, v242
Mvav 44,140,192
X 177
Mvav 48,144,192
X 178
Mvav 80,144,200
Ra 128,242,0
K 198,176,177
Mvav 112,144,208
X 179
Mvav 144,144,216
Ra 132,242,1024
Mvav 148,148,216
X 180
Mvav 116,148,208
Ra 136,242,2048
Mvav 84,148,200
K 199,178,179
Mvav 52,148,192
X 181
Mvav 56,152,192
Ra 140,242,3072
Mvav 88,152,200
X 182
Mvav 120,152,208
Mvav 152,152,216
K 206,180,181
Mvav 156,156,216
X 183
Mvav 124,156,208
Mvav 92,156,200
s_waitcnt vmcnt(0)
v_mov_b64 v[28:29], s[12:13]
s_mov_b64 exec, 1
buffer_store_dwordx2 v[28:29], off, s[8:11], s71 sc1
s_mov_b64 exec, -1
K 207,182,183
Mvav 60,156,192
s_mov_b32 s85, 99
s_lshl_b32 s65, s57, 1
s_sub_u32 s65, s65, 1
s_branch .Ll0t_46%=
.Lf1r_47%=:
s_lshl_b32 s92, s58, 2
s_add_u32 s91, s92, s51
s_add_u32 s95, s92, 0
s_and_b32 s95, s95, 15
s_mul_i32 s95, s95, 40960
s_mul_i32 s71, s51, 0
s_add_u32 s71, s71, 133120
s_add_u32 m0, s71, s48
s_lshl2_add_u32 s94, s48, s95
D 16,40,94,0
s_add_u32 s95, s92, 1
s_and_b32 s95, s95, 15
s_mul_i32 s95, s95, 40960
s_mul_i32 s71, s51, 0
s_add_u32 s71, s71, 143360
D 17,40,94,1024
D 18,40,94,2048
D 19,40,94,3072
s_add_u32 m0, m0, 4096
s_nop 0
buffer_load_dwordx4 v20, s[40:43], s94 offen lds
s_add_u32 m0, s71, s48
s_lshl2_add_u32 s94, s48, s95
D 16,40,94,0
s_add_u32 s95, s92, 2
s_and_b32 s95, s95, 15
s_mul_i32 s95, s95, 40960
s_mul_i32 s71, s51, 0
s_add_u32 s71, s71, 153600
D 17,40,94,1024
D 18,40,94,2048
D 19,40,94,3072
s_add_u32 m0, m0, 4096
s_nop 0
buffer_load_dwordx4 v20, s[40:43], s94 offen lds
s_add_u32 m0, s71, s48
s_lshl2_add_u32 s94, s48, s95
D 16,40,94,0
D 17,40,94,1024
D 18,40,94,2048
D 19,40,94,3072
s_add_u32 m0, m0, 4096
s_nop 0
buffer_load_dwordx4 v20, s[40:43], s94 offen lds
s_branch .Lf1_40%=
.Lspin_15%=:
s_mov_b32 s84, 0
.Llp_17%=:
s_sleep 2
v_lshrrev_b32 v28, 1, v1
s_lshl_b32 s80, s60, 9
s_add_u32 s80, s80, 40960
buffer_load_dwordx2 v[28:29], v28, s[8:11], s80 offen sc1
s_waitcnt vmcnt(0)
s_add_u32 s84, s84, 1
v_cmp_eq_u32 vcc, s13, v29
s_cmp_eq_u64 vcc, -1
s_cbranch_scc1 .Lok_16%=
s_cmp_lt_u32 s84, 1048576
s_cbranch_scc1 .Llp_17%=
s_branch .Lok_16%=
.Lspin_20%=:
s_mov_b32 s84, 0
.Llp_22%=:
s_sleep 2
v_lshrrev_b32 v28, 1, v1
s_lshl_b32 s80, s60, 9
s_add_u32 s80, s80, 40960
buffer_load_dwordx2 v[28:29], v28, s[8:11], s80 offen sc1
s_waitcnt vmcnt(0)
s_add_u32 s84, s84, 1
v_cmp_eq_u32 vcc, s13, v29
s_cmp_eq_u64 vcc, -1
s_cbranch_scc1 .Lok_21%=
s_cmp_lt_u32 s84, 1048576
s_cbranch_scc1 .Llp_22%=
s_branch .Lok_21%=
.Lrt_23%=:
s_waitcnt vmcnt(0)
s_mov_b32 s86, 40960
buffer_load_dwordx4 v[32:35], v1, s[8:11], s86 offen offset:0 sc1
buffer_load_dwordx4 v[36:39], v1, s[8:11], s86 offen offset:1024 sc1
buffer_load_dwordx4 v[40:43], v1, s[8:11], s86 offen offset:2048 sc1
buffer_load_dwordx4 v[44:47], v1, s[8:11], s86 offen offset:3072 sc1
s_mov_b32 s86, 45056
buffer_load_dwordx4 v[48:51], v1, s[8:11], s86 offen offset:0 sc1
buffer_load_dwordx4 v[52:55], v1, s[8:11], s86 offen offset:1024 sc1
buffer_load_dwordx4 v[56:59], v1, s[8:11], s86 offen offset:2048 sc1
buffer_load_dwordx4 v[60:63], v1, s[8:11], s86 offen offset:3072 sc1
s_mov_b32 s86, 49152
buffer_load_dwordx4 v[64:67], v1, s[8:11], s86 offen offset:0 sc1
buffer_load_dwordx4 v[68:71], v1, s[8:11], s86 offen offset:1024 sc1
buffer_load_dwordx4 v[72:75], v1, s[8:11], s86 offen offset:2048 sc1
buffer_load_dwordx4 v[76:79], v1, s[8:11], s86 offen offset:3072 sc1
s_mov_b32 s86, 53248
buffer_load_dwordx4 v[80:83], v1, s[8:11], s86 offen offset:0 sc1
buffer_load_dwordx4 v[84:87], v1, s[8:11], s86 offen offset:1024 sc1
buffer_load_dwordx4 v[88:91], v1, s[8:11], s86 offen offset:2048 sc1
buffer_load_dwordx4 v[92:95], v1, s[8:11], s86 offen offset:3072 sc1
s_branch .Lrtr_24%=
.Lrt_25%=:
v_lshrrev_b32 v136, 1, v1
s_mov_b32 s84, 0
.LL_27%=:
s_waitcnt vmcnt(0)
v_min3_u32 v96, v33, v35, v95
v_max3_u32 v97, v33, v35, v95
v_min3_u32 v96, v96, v37, v39
v_max3_u32 v97, v97, v37, v39
v_min3_u32 v96, v96, v41, v43
v_max3_u32 v97, v97, v41, v43
v_min3_u32 v96, v96, v45, v47
v_max3_u32 v97, v97, v45, v47
v_min3_u32 v96, v96, v49, v51
v_max3_u32 v97, v97, v49, v51
v_min3_u32 v96, v96, v53, v55
v_max3_u32 v97, v97, v53, v55
v_min3_u32 v96, v96, v57, v59
v_max3_u32 v97, v97, v57, v59
v_min3_u32 v96, v96, v61, v63
v_max3_u32 v97, v97, v61, v63
v_min3_u32 v96, v96, v65, v67
v_max3_u32 v97, v97, v65, v67
v_min3_u32 v96, v96, v69, v71
v_max3_u32 v97, v97, v69, v71
v_min3_u32 v96, v96, v73, v75
v_max3_u32 v97, v97, v73, v75
v_min3_u32 v96, v96, v77, v79
v_max3_u32 v97, v97, v77, v79
v_min3_u32 v96, v96, v81, v83
v_max3_u32 v97, v97, v81, v83
v_min3_u32 v96, v96, v85, v87
v_max3_u32 v97, v97, v85, v87
)"
R"(v_min3_u32 v96, v96, v89, v91
v_max3_u32 v97, v97, v89, v91
v_min_u32 v96, v96, v93
v_max_u32 v97, v97, v93
v_cmp_eq_u32 vcc, s13, v96
v_cmp_eq_u32_e64 s[86:87], s13, v97
s_and_b64 vcc, vcc, s[86:87]
s_cmp_eq_u64 vcc, -1
s_cbranch_scc1 .LL_28%=
s_sleep 2
s_mov_b32 s86, 40960
buffer_load_dwordx4 v[32:35], v1, s[8:11], s86 offen offset:0 sc1
buffer_load_dwordx4 v[36:39], v1, s[8:11], s86 offen offset:1024 sc1
buffer_load_dwordx4 v[40:43], v1, s[8:11], s86 offen offset:2048 sc1
buffer_load_dwordx4 v[44:47], v1, s[8:11], s86 offen offset:3072 sc1
s_mov_b32 s86, 45056
buffer_load_dwordx4 v[48:51], v1, s[8:11], s86 offen offset:0 sc1
buffer_load_dwordx4 v[52:55], v1, s[8:11], s86 offen offset:1024 sc1
buffer_load_dwordx4 v[56:59], v1, s[8:11], s86 offen offset:2048 sc1
buffer_load_dwordx4 v[60:63], v1, s[8:11], s86 offen offset:3072 sc1
s_mov_b32 s86, 49152
buffer_load_dwordx4 v[64:67], v1, s[8:11], s86 offen offset:0 sc1
buffer_load_dwordx4 v[68:71], v1, s[8:11], s86 offen offset:1024 sc1
buffer_load_dwordx4 v[72:75], v1, s[8:11], s86 offen offset:2048 sc1
buffer_load_dwordx4 v[76:79], v1, s[8:11], s86 offen offset:3072 sc1
s_mov_b32 s86, 53248
buffer_load_dwordx4 v[80:83], v1, s[8:11], s86 offen offset:0 sc1
buffer_load_dwordx4 v[84:87], v1, s[8:11], s86 offen offset:1024 sc1
buffer_load_dwordx4 v[88:91], v1, s[8:11], s86 offen offset:2048 sc1
buffer_load_dwordx4 v[92:95], v1, s[8:11], s86 offen offset:3072 sc1
s_add_u32 s84, s84, 1
s_cmp_lt_u32 s84, 1048576
s_cbranch_scc1 .LL_27%=
s_waitcnt vmcnt(0)
.LL_28%=:
v_mov_b32 v96, v32
v_mov_b32 v97, 0
v_mov_b32 v98, 0
v_mov_b32 v100, 0
v_med3_u32 v98, v97, v98, v34
v_med3_u32 v97, v96, v97, v34
v_max_u32 v96, v96, v34
v_med3_u32 v98, v97, v98, v36
v_med3_u32 v97, v96, v97, v36
v_max_u32 v96, v96, v36
v_med3_u32 v98, v97, v98, v38
v_med3_u32 v97, v96, v97, v38
v_max_u32 v96, v96, v38
v_med3_u32 v98, v97, v98, v40
v_med3_u32 v97, v96, v97, v40
v_max_u32 v96, v96, v40
v_med3_u32 v98, v97, v98, v42
v_med3_u32 v97, v96, v97, v42
v_max_u32 v96, v96, v42
v_med3_u32 v98, v97, v98, v44
v_med3_u32 v97, v96, v97, v44
v_max_u32 v96, v96, v44
v_med3_u32 v98, v97, v98, v46
v_med3_u32 v97, v96, v97, v46
v_max_u32 v96, v96, v46
v_med3_u32 v98, v97, v98, v48
v_med3_u32 v97, v96, v97, v48
v_max_u32 v96, v96, v48
v_med3_u32 v98, v97, v98, v50
v_med3_u32 v97, v96, v97, v50
v_max_u32 v96, v96, v50
v_med3_u32 v98, v97, v98, v52
v_med3_u32 v97, v96, v97, v52
v_max_u32 v96, v96, v52
v_med3_u32 v98, v97, v98, v54
v_med3_u32 v97, v96, v97, v54
v_max_u32 v96, v96, v54
v_med3_u32 v98, v97, v98, v56
v_med3_u32 v97, v96, v97, v56
v_max_u32 v96, v96, v56
v_med3_u32 v98, v97, v98, v58
v_med3_u32 v97, v96, v97, v58
v_max_u32 v96, v96, v58
v_med3_u32 v98, v97, v98, v60
v_med3_u32 v97, v96, v97, v60
v_max_u32 v96, v96, v60
v_med3_u32 v98, v97, v98, v62
v_med3_u32 v97, v96, v97, v62
v_max_u32 v96, v96, v62
v_med3_u32 v98, v97, v98, v64
v_med3_u32 v97, v96, v97, v64
v_max_u32 v96, v96, v64
v_med3_u32 v98, v97, v98, v66
v_med3_u32 v97, v96, v97, v66
v_max_u32 v96, v96, v66
v_med3_u32 v98, v97, v98, v68
v_med3_u32 v97, v96, v97, v68
v_max_u32 v96, v96, v68
v_med3_u32 v98, v97, v98, v70
v_med3_u32 v97, v96, v97, v70
v_max_u32 v96, v96, v70
v_med3_u32 v98, v97, v98, v72
v_med3_u32 v97, v96, v97, v72
v_max_u32 v96, v96, v72
v_med3_u32 v98, v97, v98, v74
v_med3_u32 v97, v96, v97, v74
v_max_u32 v96, v96, v74
v_med3_u32 v98, v97, v98, v76
v_med3_u32 v97, v96, v97, v76
v_max_u32 v96, v96, v76
v_med3_u32 v98, v97, v98, v78
v_med3_u32 v97, v96, v97, v78
v_max_u32 v96, v96, v78
v_med3_u32 v98, v97, v98, v80
v_med3_u32 v97, v96, v97, v80
v_max_u32 v96, v96, v80
v_med3_u32 v98, v97, v98, v82
v_med3_u32 v97, v96, v97, v82
v_max_u32 v96, v96, v82
v_med3_u32 v98, v97, v98, v84
v_med3_u32 v97, v96, v97, v84
v_max_u32 v96, v96, v84
v_med3_u32 v98, v97, v98, v86
v_med3_u32 v97, v96, v97, v86
v_max_u32 v96, v96, v86
v_med3_u32 v98, v97, v98, v88
v_med3_u32 v97, v96, v97, v88
v_max_u32 v96, v96, v88
v_med3_u32 v98, v97, v98, v90
v_med3_u32 v97, v96, v97, v90
v_max_u32 v96, v96, v90
v_med3_u32 v98, v97, v98, v92
v_med3_u32 v97, v96, v97, v92
v_max_u32 v96, v96, v92
v_med3_u32 v98, v97, v98, v94
v_med3_u32 v97, v96, v97, v94
v_max_u32 v96, v96, v94
v_mov_b32 v99, v96
s_nop 1
v_max_u32_dpp v99, v99, v99 row_shr:1 row_mask:0xf bank_mask:0xf
s_nop 1
v_max_u32_dpp v99, v99, v99 row_shr:2 row_mask:0xf bank_mask:0xf
s_nop 1
v_max_u32_dpp v99, v99, v99 row_shr:4 row_mask:0xf bank_mask:0xf
s_nop 1
v_max_u32_dpp v99, v99, v99 row_shr:8 row_mask:0xf bank_mask:0xf
s_nop 1
v_max_u32_dpp v99, v99, v99 row_bcast:15 row_mask:0xa bank_mask:0xf
s_nop 1
v_max_u32_dpp v99, v99, v99 row_bcast:31 row_mask:0xc bank_mask:0xf
s_nop 0
v_readlane_b32 s86, v99, 63
s_nop 1
v_cmp_eq_u32 vcc, s86, v96
s_and_b32 s87, s86, 0xffff
s_sub_u32 s87, 2047, s87
v_cndmask_b32 v96, v96, v97, vcc
v_cndmask_b32_e64 v97, v97, 0, vcc
v_writelane_b32 v100, s87, 0
v_mov_b32 v99, v96
s_nop 1
v_max_u32_dpp v99, v99, v99 row_shr:1 row_mask:0xf bank_mask:0xf
s_nop 1
v_max_u32_dpp v99, v99, v99 row_shr:2 row_mask:0xf bank_mask:0xf
s_nop 1
v_max_u32_dpp v99, v99, v99 row_shr:4 row_mask:0xf bank_mask:0xf
s_nop 1
v_max_u32_dpp v99, v99, v99 row_shr:8 row_mask:0xf bank_mask:0xf
s_nop 1
v_max_u32_dpp v99, v99, v99 row_bcast:15 row_mask:0xa bank_mask:0xf
s_nop 1
v_max_u32_dpp v99, v99, v99 row_bcast:31 row_mask:0xc bank_mask:0xf
s_nop 0
v_readlane_b32 s86, v99, 63
s_nop 1
v_cmp_eq_u32 vcc, s86, v96
s_and_b32 s87, s86, 0xffff
s_sub_u32 s87, 2047, s87
v_cndmask_b32 v96, v96, v97, vcc
v_cndmask_b32_e64 v97, v97, 0, vcc
v_writelane_b32 v100, s87, 1
v_mov_b32 v99, v96
s_nop 1
v_max_u32_dpp v99, v99, v99 row_shr:1 row_mask:0xf bank_mask:0xf
s_nop 1
v_max_u32_dpp v99, v99, v99 row_shr:2 row_mask:0xf bank_mask:0xf
s_nop 1
v_max_u32_dpp v99, v99, v99 row_shr:4 row_mask:0xf bank_mask:0xf
s_nop 1
v_max_u32_dpp v99, v99, v99 row_shr:8 row_mask:0xf bank_mask:0xf
s_nop 1
v_max_u32_dpp v99, v99, v99 row_bcast:15 row_mask:0xa bank_mask:0xf
s_nop 1
v_max_u32_dpp v99, v99, v99 row_bcast:31 row_mask:0xc bank_mask:0xf
s_nop 0
v_readlane_b32 s86, v99, 63
s_nop 1
v_cmp_eq_u32 vcc, s86, v96
s_and_b32 s87, s86, 0xffff
s_sub_u32 s87, 2047, s87
v_cndmask_b32 v96, v96, v97, vcc
v_cndmask_b32_e64 v97, v97, 0, vcc
v_writelane_b32 v100, s87, 2
v_mov_b32 v99, v96
s_nop 1
v_max_u32_dpp v99, v99, v99 row_shr:1 row_mask:0xf bank_mask:0xf
s_nop 1
v_max_u32_dpp v99, v99, v99 row_shr:2 row_mask:0xf bank_mask:0xf
s_nop 1
v_max_u32_dpp v99, v99, v99 row_shr:4 row_mask:0xf bank_mask:0xf
s_nop 1
v_max_u32_dpp v99, v99, v99 row_shr:8 row_mask:0xf bank_mask:0xf
s_nop 1
v_max_u32_dpp v99, v99, v99 row_bcast:15 row_mask:0xa bank_mask:0xf
s_nop 1
v_max_u32_dpp v99, v99, v99 row_bcast:31 row_mask:0xc bank_mask:0xf
s_nop 0
v_readlane_b32 s86, v99, 63
s_nop 1
v_cmp_eq_u32 vcc, s86, v96
s_and_b32 s87, s86, 0xffff
s_sub_u32 s87, 2047, s87
v_cndmask_b32 v96, v96, v97, vcc
v_cndmask_b32_e64 v97, v97, 0, vcc
v_writelane_b32 v100, s87, 3
v_mov_b32 v99, v96
s_nop 1
v_max_u32_dpp v99, v99, v99 row_shr:1 row_mask:0xf bank_mask:0xf
s_nop 1
v_max_u32_dpp v99, v99, v99 row_shr:2 row_mask:0xf bank_mask:0xf
s_nop 1
v_max_u32_dpp v99, v99, v99 row_shr:4 row_mask:0xf bank_mask:0xf
s_nop 1
v_max_u32_dpp v99, v99, v99 row_shr:8 row_mask:0xf bank_mask:0xf
s_nop 1
v_max_u32_dpp v99, v99, v99 row_bcast:15 row_mask:0xa bank_mask:0xf
s_nop 1
v_max_u32_dpp v99, v99, v99 row_bcast:31 row_mask:0xc bank_mask:0xf
s_nop 0
v_readlane_b32 s86, v99, 63
s_nop 1
v_cmp_eq_u32 vcc, s86, v96
s_and_b32 s87, s86, 0xffff
s_sub_u32 s87, 2047, s87
v_cndmask_b32 v96, v96, v97, vcc
v_cndmask_b32_e64 v97, v97, 0, vcc
v_writelane_b32 v100, s87, 4
v_mov_b32 v99, v96
s_nop 1
v_max_u32_dpp v99, v99, v99 row_shr:1 row_mask:0xf bank_mask:0xf
s_nop 1
v_max_u32_dpp v99, v99, v99 row_shr:2 row_mask:0xf bank_mask:0xf
s_nop 1
v_max_u32_dpp v99, v99, v99 row_shr:4 row_mask:0xf bank_mask:0xf
s_nop 1
v_max_u32_dpp v99, v99, v99 row_shr:8 row_mask:0xf bank_mask:0xf
s_nop 1
v_max_u32_dpp v99, v99, v99 row_bcast:15 row_mask:0xa bank_mask:0xf
s_nop 1
v_max_u32_dpp v99, v99, v99 row_bcast:31 row_mask:0xc bank_mask:0xf
s_nop 0
v_readlane_b32 s86, v99, 63
s_nop 1
v_cmp_eq_u32 vcc, s86, v96
s_and_b32 s87, s86, 0xffff
s_sub_u32 s87, 2047, s87
v_cndmask_b32 v96, v96, v97, vcc
v_cndmask_b32_e64 v97, v97, 0, vcc
v_writelane_b32 v100, s87, 5
v_mov_b32 v99, v96
s_nop 1
v_max_u32_dpp v99, v99, v99 row_shr:1 row_mask:0xf bank_mask:0xf
s_nop 1
v_max_u32_dpp v99, v99, v99 row_shr:2 row_mask:0xf bank_mask:0xf
s_nop 1
v_max_u32_dpp v99, v99, v99 row_shr:4 row_mask:0xf bank_mask:0xf
s_nop 1
v_max_u32_dpp v99, v99, v99 row_shr:8 row_mask:0xf bank_mask:0xf
s_nop 1
v_max_u32_dpp v99, v99, v99 row_bcast:15 row_mask:0xa bank_mask:0xf
s_nop 1
v_max_u32_dpp v99, v99, v99 row_bcast:31 row_mask:0xc bank_mask:0xf
s_nop 0
v_readlane_b32 s86, v99, 63
s_nop 1
v_cmp_eq_u32 vcc, s86, v96
s_and_b32 s87, s86, 0xffff
s_sub_u32 s87, 2047, s87
v_cndmask_b32 v96, v96, v97, vcc
v_cndmask_b32_e64 v97, v97, 0, vcc
v_writelane_b32 v100, s87, 6
v_mov_b32 v99, v96
s_nop 1
v_max_u32_dpp v99, v99, v99 row_shr:1 row_mask:0xf bank_mask:0xf
s_nop 1
v_max_u32_dpp v99, v99, v99 row_shr:2 row_mask:0xf bank_mask:0xf
s_nop 1
v_max_u32_dpp v99, v99, v99 row_shr:4 row_mask:0xf bank_mask:0xf
s_nop 1
v_max_u32_dpp v99, v99, v99 row_shr:8 row_mask:0xf bank_mask:0xf
s_nop 1
v_max_u32_dpp v99, v99, v99 row_bcast:15 row_mask:0xa bank_mask:0xf
s_nop 1
v_max_u32_dpp v99, v99, v99 row_bcast:31 row_mask:0xc bank_mask:0xf
s_nop 0
v_readlane_b32 s86, v99, 63
s_nop 1
v_cmp_eq_u32 vcc, s86, v96
s_and_b32 s87, s86, 0xffff
s_sub_u32 s87, 2047, s87
v_cndmask_b32 v96, v96, v97, vcc
v_cndmask_b32_e64 v97, v97, 0, vcc
v_writelane_b32 v100, s87, 7
v_mov_b32 v99, v98
s_nop 1
v_max_u32_dpp v99, v99, v99 row_shr:1 row_mask:0xf bank_mask:0xf
s_nop 1
v_max_u32_dpp v99, v99, v99 row_shr:2 row_mask:0xf bank_mask:0xf
s_nop 1
v_max_u32_dpp v99, v99, v99 row_shr:4 row_mask:0xf bank_mask:0xf
s_nop 1
v_max_u32_dpp v99, v99, v99 row_shr:8 row_mask:0xf bank_mask:0xf
s_nop 1
v_max_u32_dpp v99, v99, v99 row_bcast:15 row_mask:0xa bank_mask:0xf
s_nop 1
v_max_u32_dpp v99, v99, v99 row_bcast:31 row_mask:0xc bank_mask:0xf
s_nop 0
v_readlane_b32 s87, v99, 63
s_nop 0
s_cmp_gt_u32 s87, s86
s_cbranch_scc0 .LL_29%=
s_mov_b32 m0, 0
.LL_30%=:
v_max3_u32 v96, v32, v34, v36
v_max3_u32 v96, v96, v38, v40
v_max3_u32 v96, v96, v42, v44
v_max3_u32 v96, v96, v46, v48
v_max3_u32 v96, v96, v50, v52
v_max3_u32 v96, v96, v54, v56
v_max3_u32 v96, v96, v58, v60
v_max3_u32 v96, v96, v62, v64
v_max3_u32 v96, v96, v66, v68
v_max3_u32 v96, v96, v70, v72
v_max3_u32 v96, v96, v74, v76
v_max3_u32 v96, v96, v78, v80
v_max3_u32 v96, v96, v82, v84
v_max3_u32 v96, v96, v86, v88
v_max3_u32 v96, v96, v90, v92
v_max_u32 v96, v96, v94
v_mov_b32 v99, v96
s_nop 1
v_max_u32_dpp v99, v99, v99 row_shr:1 row_mask:0xf bank_mask:0xf
s_nop 1
v_max_u32_dpp v99, v99, v99 row_shr:2 row_mask:0xf bank_mask:0xf
s_nop 1
v_max_u32_dpp v99, v99, v99 row_shr:4 row_mask:0xf bank_mask:0xf
s_nop 1
v_max_u32_dpp v99, v99, v99 row_shr:8 row_mask:0xf bank_mask:0xf
s_nop 1
v_max_u32_dpp v99, v99, v99 row_bcast:15 row_mask:0xa bank_mask:0xf
s_nop 1
v_max_u32_dpp v99, v99, v99 row_bcast:31 row_mask:0xc bank_mask:0xf
s_nop 0
v_readlane_b32 s86, v99, 63
s_nop 1
v_cmp_eq_u32 vcc, s86, v32
s_nop 1
v_cndmask_b32_e64 v32, v32, 0, vcc
v_cmp_eq_u32 vcc, s86, v34
s_nop 1
v_cndmask_b32_e64 v34, v34, 0, vcc
v_cmp_eq_u32 vcc, s86, v36
s_nop 1
v_cndmask_b32_e64 v36, v36, 0, vcc
v_cmp_eq_u32 vcc, s86, v38
s_nop 1
v_cndmask_b32_e64 v38, v38, 0, vcc
v_cmp_eq_u32 vcc, s86, v40
s_nop 1
v_cndmask_b32_e64 v40, v40, 0, vcc
v_cmp_eq_u32 vcc, s86, v42
s_nop 1
v_cndmask_b32_e64 v42, v42, 0, vcc
v_cmp_eq_u32 vcc, s86, v44
s_nop 1
v_cndmask_b32_e64 v44, v44, 0, vcc
v_cmp_eq_u32 vcc, s86, v46
s_nop 1
v_cndmask_b32_e64 v46, v46, 0, vcc
v_cmp_eq_u32 vcc, s86, v48
s_nop 1
v_cndmask_b32_e64 v48, v48, 0, vcc
v_cmp_eq_u32 vcc, s86, v50
s_nop 1
v_cndmask_b32_e64 v50, v50, 0, vcc
v_cmp_eq_u32 vcc, s86, v52
s_nop 1
v_cndmask_b32_e64 v52, v52, 0, vcc
v_cmp_eq_u32 vcc, s86, v54
s_nop 1
v_cndmask_b32_e64 v54, v54, 0, vcc
v_cmp_eq_u32 vcc, s86, v56
s_nop 1
v_cndmask_b32_e64 v56, v56, 0, vcc
v_cmp_eq_u32 vcc, s86, v58
)"
R"(s_nop 1
v_cndmask_b32_e64 v58, v58, 0, vcc
v_cmp_eq_u32 vcc, s86, v60
s_nop 1
v_cndmask_b32_e64 v60, v60, 0, vcc
v_cmp_eq_u32 vcc, s86, v62
s_nop 1
v_cndmask_b32_e64 v62, v62, 0, vcc
v_cmp_eq_u32 vcc, s86, v64
s_nop 1
v_cndmask_b32_e64 v64, v64, 0, vcc
v_cmp_eq_u32 vcc, s86, v66
s_nop 1
v_cndmask_b32_e64 v66, v66, 0, vcc
v_cmp_eq_u32 vcc, s86, v68
s_nop 1
v_cndmask_b32_e64 v68, v68, 0, vcc
v_cmp_eq_u32 vcc, s86, v70
s_nop 1
v_cndmask_b32_e64 v70, v70, 0, vcc
v_cmp_eq_u32 vcc, s86, v72
s_nop 1
v_cndmask_b32_e64 v72, v72, 0, vcc
v_cmp_eq_u32 vcc, s86, v74
s_nop 1
v_cndmask_b32_e64 v74, v74, 0, vcc
v_cmp_eq_u32 vcc, s86, v76
s_nop 1
v_cndmask_b32_e64 v76, v76, 0, vcc
v_cmp_eq_u32 vcc, s86, v78
s_nop 1
v_cndmask_b32_e64 v78, v78, 0, vcc
v_cmp_eq_u32 vcc, s86, v80
s_nop 1
v_cndmask_b32_e64 v80, v80, 0, vcc
v_cmp_eq_u32 vcc, s86, v82
s_nop 1
v_cndmask_b32_e64 v82, v82, 0, vcc
v_cmp_eq_u32 vcc, s86, v84
s_nop 1
v_cndmask_b32_e64 v84, v84, 0, vcc
v_cmp_eq_u32 vcc, s86, v86
s_nop 1
v_cndmask_b32_e64 v86, v86, 0, vcc
v_cmp_eq_u32 vcc, s86, v88
s_nop 1
v_cndmask_b32_e64 v88, v88, 0, vcc
v_cmp_eq_u32 vcc, s86, v90
s_nop 1
v_cndmask_b32_e64 v90, v90, 0, vcc
v_cmp_eq_u32 vcc, s86, v92
s_nop 1
v_cndmask_b32_e64 v92, v92, 0, vcc
v_cmp_eq_u32 vcc, s86, v94
s_nop 1
v_cndmask_b32_e64 v94, v94, 0, vcc
s_and_b32 s87, s86, 0xffff
s_sub_u32 s87, 2047, s87
s_nop 0
v_writelane_b32 v100, s87, m0
s_add_u32 m0, m0, 1
s_cmp_lt_u32 m0, 8
s_cbranch_scc1 .LL_30%=
.LL_29%=:
v_xor_b32 v102, s12, v100
v_mov_b32 v103, s13
s_mov_b64 exec, 0xff
buffer_store_dwordx2 v[102:103], v136, s[8:11], 0 offen offset:3584 sc1
s_mov_b64 exec, -1
s_branch .Lrtr_26%=
.Lspin_51%=:
s_mov_b32 s84, 0
.Llp_53%=:
s_sleep 2
buffer_load_dwordx2 v[8:9], v11, s[8:11], 0 offen offset:3840 sc1
s_waitcnt vmcnt(0)
s_add_u32 s84, s84, 1
v_cmp_eq_u64 vcc, s[12:13], v[8:9]
s_cmp_eq_u64 vcc, -1
s_cbranch_scc1 .Lok_52%=
s_cmp_lt_u32 s84, 1048576
s_cbranch_scc1 .Llp_53%=
s_branch .Lok_52%=
.Lredo%=:
s_waitcnt vmcnt(0) lgkmcnt(0)
s_barrier
s_and_b32 s81, s14, 1
s_mul_i32 s91, s81, 17164288
s_mul_i32 s82, s56, 536576
s_add_u32 s91, s91, s82
s_add_u32 s91, s91, 4194304
s_lshr_b32 s82, s14, 1
s_lshl_b32 s83, s82, 14
s_add_u32 s91, s91, s83
s_lshl_b32 s81, s81, 14
s_lshl_b32 s82, s82, 16
s_or_b32 s92, s81, s82
s_xor_b32 s92, s92, 0x10000
s_add_u32 m0, s92, 0
s_add_u32 s81, s91, 0
Dc 1,8,81,0
Dc 1,8,81,1024
Dc 1,8,81,2048
Dc 1,8,81,3072
s_add_u32 m0, s92, 4096
s_add_u32 s81, s91, 4096
Dc 1,8,81,0
Dc 1,8,81,1024
Dc 1,8,81,2048
Dc 1,8,81,3072
s_add_u32 m0, s92, 8192
s_add_u32 s81, s91, 8192
Dc 1,8,81,0
Dc 1,8,81,1024
Dc 1,8,81,2048
Dc 1,8,81,3072
s_add_u32 m0, s92, 12288
s_add_u32 s81, s91, 12288
Dc 1,8,81,0
Dc 1,8,81,1024
Dc 1,8,81,2048
Dc 1,8,81,3072
v_bfe_u32 v244, v1, 4, 1
v_mul_u32_u24 v244, 0xaaaa, v244
v_add_u32 v244, 0x4444, v244
v_lshl_or_b32 v240, v244, 16, v244
v_mov_b32 v241, 0xff800000
v_add_u32 v242, 0x10000, v1
v_bfe_u32 v244, v1, 4, 4
v_lshrrev_b32 v245, 8, v1
v_lshlrev_b32 v245, 2, v245
v_sub_u32 v244, v244, v245
v_cmp_le_i32_e64 s[72:73], 0, v244
v_cmp_le_i32_e64 s[74:75], 1, v244
v_cmp_le_i32_e64 s[76:77], 2, v244
v_cmp_le_i32_e64 s[78:79], 3, v244
v_add_u32 v243, 4096, v1
v_add_u32 v244, 8192, v1
v_add_u32 v245, 12288, v1
s_mov_b32 s18, 0x3f803f80
s_mov_b32 s19, 0x3f003f00
v_accvgpr_write_b32 a176, s18
v_accvgpr_write_b32 a180, s19
v_mov_b32 v252, v241
v_accvgpr_write_b32 a177, s18
v_accvgpr_write_b32 a181, s19
v_mov_b32 v253, v241
v_accvgpr_write_b32 a178, s18
v_accvgpr_write_b32 a182, s19
v_mov_b32 v254, v241
v_accvgpr_write_b32 a179, s18
v_accvgpr_write_b32 a183, s19
v_mov_b32 v255, v241
v_accvgpr_write_b32 a160, 0
v_accvgpr_write_b32 a161, 0
v_accvgpr_write_b32 a162, 0
v_accvgpr_write_b32 a163, 0
v_accvgpr_write_b32 a164, 0
v_accvgpr_write_b32 a165, 0
v_accvgpr_write_b32 a166, 0
v_accvgpr_write_b32 a167, 0
v_accvgpr_write_b32 a168, 0
v_accvgpr_write_b32 a169, 0
v_accvgpr_write_b32 a170, 0
v_accvgpr_write_b32 a171, 0
v_accvgpr_write_b32 a172, 0
v_accvgpr_write_b32 a173, 0
v_accvgpr_write_b32 a174, 0
v_accvgpr_write_b32 a175, 0
s_mov_b32 s90, 0x5d800000
s_mov_b64 s[88:89], 0
s_mov_b32 s64, 0
s_and_b32 s18, s14, 1
s_lshl_b32 s62, s18, 14
s_lshl_b32 s20, s57, 15
s_sub_u32 s20, 507904, s20
s_add_u32 s20, s20, s62
s_lshl_b32 s65, s57, 1
s_sub_u32 s65, s65, 1
s_max_i32 s65, s65, 0
s_sub_u32 s66, s60, 1
s_cmp_lt_u32 s14, 2
s_cselect_b32 s20, s20, 0
s_cselect_b32 s66, s66, 15
s_cselect_b32 s21, 32768, 0
s_add_u32 s61, s91, s20
s_add_u32 s62, s62, s21
s_sub_u32 s63, 16384, s21
s_lshl_b32 s67, s60, 7
s_add_u32 s67, s67, s56
s_lshl_b32 s67, s67, 12
s_add_u32 s67, s67, 39845888
s_lshl_b32 s69, s60, 3
s_add_u32 s69, s69, s59
s_lshl_b32 s69, s69, 6
s_lshl_b32 s21, s58, 3
s_add_u32 s69, s69, s21
s_add_u32 s69, s69, 20480
s_mov_b32 s70, 0
s_mov_b32 s37, 0
v_bfe_u32 v28, v1, 4, 4
v_lshrrev_b32 v29, 8, v1
v_lshlrev_b32 v29, 2, v29
v_sub_u32 v28, v28, v29
s_mov_b64 s[24:25], 0
s_mov_b32 s68, 0
s_mov_b32 s85, 99
s_lshl_b32 s80, s60, 6
s_and_b32 s81, s14, 2
s_lshl_b32 s81, s81, 4
s_xor_b32 s82, s81, 0
s_add_u32 s82, s82, s80
v_add_u32 v4, s82, v28
s_xor_b32 s82, s81, 16
s_add_u32 s82, s82, s80
v_add_u32 v5, s82, v28
s_xor_b32 s82, s81, 32
s_add_u32 s82, s82, s80
v_add_u32 v6, s82, v28
s_xor_b32 s82, s81, 48
s_add_u32 s82, s82, s80
v_add_u32 v7, s82, v28
s_mul_i32 s82, s56, 536576
s_add_u32 s82, s82, 4194304
s_lshl_b32 s84, s60, 1
s_add_u32 s84, s84, 2
.LL_54%=:
buffer_load_dwordx4 a[128:131], v1, s[8:11], s82 offen offset:0 sc1
buffer_load_dwordx4 a[132:135], v1, s[8:11], s82 offen offset:1024 sc1
buffer_load_dwordx4 a[136:139], v1, s[8:11], s82 offen offset:2048 sc1
buffer_load_dwordx4 a[140:143], v1, s[8:11], s82 offen offset:3072 sc1
buffer_load_dwordx4 a[144:147], v243, s[8:11], s82 offen offset:0 sc1
buffer_load_dwordx4 a[148:151], v243, s[8:11], s82 offen offset:1024 sc1
buffer_load_dwordx4 a[152:155], v243, s[8:11], s82 offen offset:2048 sc1
buffer_load_dwordx4 a[156:159], v243, s[8:11], s82 offen offset:3072 sc1
s_waitcnt vmcnt(0)
Zvaa 160,128,0
Zvaa 164,128,16
Zvaa 168,128,32
Zvaa 172,128,48
Mvaa 160,132,4
Mvaa 164,132,20
Mvaa 168,132,36
Mvaa 172,132,52
Mvaa 160,136,8
Mvaa 164,136,24
Mvaa 168,136,40
Mvaa 172,136,56
Mvaa 160,140,12
Mvaa 164,140,28
Mvaa 168,140,44
Mvaa 172,140,60
Zvaa 176,144,0
Zvaa 180,144,16
Zvaa 184,144,32
Zvaa 188,144,48
Mvaa 176,148,4
Mvaa 180,148,20
Mvaa 184,148,36
Mvaa 188,148,52
Mvaa 176,152,8
Mvaa 180,152,24
Mvaa 184,152,40
Mvaa 188,152,56
Mvaa 176,156,12
Mvaa 180,156,28
Mvaa 184,156,44
Mvaa 188,156,60
v_cmp_le_i32 vcc, 0, v4
s_nop 1
v_cndmask_b32 v160, v241, v160, vcc
v_max_f32 v252, v252, v160
v_cmp_le_i32 vcc, 1, v4
s_nop 1
v_cndmask_b32 v161, v241, v161, vcc
v_max_f32 v252, v252, v161
v_cmp_le_i32 vcc, 2, v4
s_nop 1
v_cndmask_b32 v162, v241, v162, vcc
v_max_f32 v252, v252, v162
v_cmp_le_i32 vcc, 3, v4
s_nop 1
v_cndmask_b32 v163, v241, v163, vcc
v_max_f32 v252, v252, v163
v_cmp_le_i32 vcc, 0, v5
s_nop 1
v_cndmask_b32 v164, v241, v164, vcc
v_max_f32 v253, v253, v164
v_cmp_le_i32 vcc, 1, v5
s_nop 1
v_cndmask_b32 v165, v241, v165, vcc
v_max_f32 v253, v253, v165
v_cmp_le_i32 vcc, 2, v5
s_nop 1
v_cndmask_b32 v166, v241, v166, vcc
v_max_f32 v253, v253, v166
v_cmp_le_i32 vcc, 3, v5
s_nop 1
v_cndmask_b32 v167, v241, v167, vcc
v_max_f32 v253, v253, v167
v_cmp_le_i32 vcc, 0, v6
s_nop 1
v_cndmask_b32 v168, v241, v168, vcc
v_max_f32 v254, v254, v168
v_cmp_le_i32 vcc, 1, v6
s_nop 1
v_cndmask_b32 v169, v241, v169, vcc
v_max_f32 v254, v254, v169
v_cmp_le_i32 vcc, 2, v6
s_nop 1
v_cndmask_b32 v170, v241, v170, vcc
v_max_f32 v254, v254, v170
v_cmp_le_i32 vcc, 3, v6
s_nop 1
v_cndmask_b32 v171, v241, v171, vcc
v_max_f32 v254, v254, v171
v_cmp_le_i32 vcc, 0, v7
s_nop 1
v_cndmask_b32 v172, v241, v172, vcc
v_max_f32 v255, v255, v172
v_cmp_le_i32 vcc, 1, v7
s_nop 1
v_cndmask_b32 v173, v241, v173, vcc
v_max_f32 v255, v255, v173
v_cmp_le_i32 vcc, 2, v7
s_nop 1
v_cndmask_b32 v174, v241, v174, vcc
v_max_f32 v255, v255, v174
v_cmp_le_i32 vcc, 3, v7
s_nop 1
v_cndmask_b32 v175, v241, v175, vcc
v_max_f32 v255, v255, v175
v_cmp_le_i32 vcc, 16, v4
s_nop 1
v_cndmask_b32 v176, v241, v176, vcc
v_max_f32 v252, v252, v176
v_cmp_le_i32 vcc, 17, v4
s_nop 1
v_cndmask_b32 v177, v241, v177, vcc
v_max_f32 v252, v252, v177
v_cmp_le_i32 vcc, 18, v4
s_nop 1
v_cndmask_b32 v178, v241, v178, vcc
v_max_f32 v252, v252, v178
v_cmp_le_i32 vcc, 19, v4
s_nop 1
v_cndmask_b32 v179, v241, v179, vcc
v_max_f32 v252, v252, v179
v_cmp_le_i32 vcc, 16, v5
s_nop 1
v_cndmask_b32 v180, v241, v180, vcc
v_max_f32 v253, v253, v180
v_cmp_le_i32 vcc, 17, v5
s_nop 1
v_cndmask_b32 v181, v241, v181, vcc
v_max_f32 v253, v253, v181
v_cmp_le_i32 vcc, 18, v5
s_nop 1
v_cndmask_b32 v182, v241, v182, vcc
v_max_f32 v253, v253, v182
v_cmp_le_i32 vcc, 19, v5
s_nop 1
v_cndmask_b32 v183, v241, v183, vcc
v_max_f32 v253, v253, v183
v_cmp_le_i32 vcc, 16, v6
s_nop 1
v_cndmask_b32 v184, v241, v184, vcc
v_max_f32 v254, v254, v184
v_cmp_le_i32 vcc, 17, v6
s_nop 1
v_cndmask_b32 v185, v241, v185, vcc
v_max_f32 v254, v254, v185
v_cmp_le_i32 vcc, 18, v6
s_nop 1
v_cndmask_b32 v186, v241, v186, vcc
v_max_f32 v254, v254, v186
v_cmp_le_i32 vcc, 19, v6
s_nop 1
v_cndmask_b32 v187, v241, v187, vcc
v_max_f32 v254, v254, v187
v_cmp_le_i32 vcc, 16, v7
s_nop 1
v_cndmask_b32 v188, v241, v188, vcc
v_max_f32 v255, v255, v188
v_cmp_le_i32 vcc, 17, v7
s_nop 1
v_cndmask_b32 v189, v241, v189, vcc
v_max_f32 v255, v255, v189
v_cmp_le_i32 vcc, 18, v7
s_nop 1
v_cndmask_b32 v190, v241, v190, vcc
v_max_f32 v255, v255, v190
v_cmp_le_i32 vcc, 19, v7
s_nop 1
v_cndmask_b32 v191, v241, v191, vcc
v_max_f32 v255, v255, v191
v_subrev_u32 v4, 32, v4
v_subrev_u32 v5, 32, v5
v_subrev_u32 v6, 32, v6
v_subrev_u32 v7, 32, v7
)"
R"(s_add_u32 s82, s82, 8192
s_sub_u32 s84, s84, 1
s_cmp_lg_u32 s84, 0
s_cbranch_scc1 .LL_54%=
G 28,0
v_lshlrev_b32 v32, 16, v28
v_and_b32 v33, 0xffff0000, v28
G 28,1
v_lshlrev_b32 v34, 16, v28
v_and_b32 v35, 0xffff0000, v28
G 28,2
v_lshlrev_b32 v36, 16, v28
v_and_b32 v37, 0xffff0000, v28
G 28,3
v_lshlrev_b32 v38, 16, v28
v_and_b32 v39, 0xffff0000, v28
G 28,4
v_lshlrev_b32 v40, 16, v28
v_and_b32 v41, 0xffff0000, v28
G 28,5
v_lshlrev_b32 v42, 16, v28
v_and_b32 v43, 0xffff0000, v28
G 28,6
v_lshlrev_b32 v44, 16, v28
v_and_b32 v45, 0xffff0000, v28
G 28,7
v_lshlrev_b32 v46, 16, v28
v_and_b32 v47, 0xffff0000, v28
G 28,8
v_lshlrev_b32 v48, 16, v28
v_and_b32 v49, 0xffff0000, v28
G 28,9
v_lshlrev_b32 v50, 16, v28
v_and_b32 v51, 0xffff0000, v28
G 28,10
v_lshlrev_b32 v52, 16, v28
v_and_b32 v53, 0xffff0000, v28
G 28,11
v_lshlrev_b32 v54, 16, v28
v_and_b32 v55, 0xffff0000, v28
G 28,12
v_lshlrev_b32 v56, 16, v28
v_and_b32 v57, 0xffff0000, v28
G 28,13
v_lshlrev_b32 v58, 16, v28
v_and_b32 v59, 0xffff0000, v28
G 28,14
v_lshlrev_b32 v60, 16, v28
v_and_b32 v61, 0xffff0000, v28
G 28,15
v_lshlrev_b32 v62, 16, v28
v_and_b32 v63, 0xffff0000, v28
G 28,16
v_lshlrev_b32 v64, 16, v28
v_and_b32 v65, 0xffff0000, v28
G 28,17
v_lshlrev_b32 v66, 16, v28
v_and_b32 v67, 0xffff0000, v28
G 28,18
v_lshlrev_b32 v68, 16, v28
v_and_b32 v69, 0xffff0000, v28
G 28,19
v_lshlrev_b32 v70, 16, v28
v_and_b32 v71, 0xffff0000, v28
G 28,20
v_lshlrev_b32 v72, 16, v28
v_and_b32 v73, 0xffff0000, v28
G 28,21
v_lshlrev_b32 v74, 16, v28
v_and_b32 v75, 0xffff0000, v28
G 28,22
v_lshlrev_b32 v76, 16, v28
v_and_b32 v77, 0xffff0000, v28
G 28,23
v_lshlrev_b32 v78, 16, v28
v_and_b32 v79, 0xffff0000, v28
G 28,24
v_lshlrev_b32 v80, 16, v28
v_and_b32 v81, 0xffff0000, v28
G 28,25
v_lshlrev_b32 v82, 16, v28
v_and_b32 v83, 0xffff0000, v28
G 28,26
v_lshlrev_b32 v84, 16, v28
v_and_b32 v85, 0xffff0000, v28
G 28,27
v_lshlrev_b32 v86, 16, v28
v_and_b32 v87, 0xffff0000, v28
G 28,28
v_lshlrev_b32 v88, 16, v28
v_and_b32 v89, 0xffff0000, v28
G 28,29
v_lshlrev_b32 v90, 16, v28
v_and_b32 v91, 0xffff0000, v28
G 28,30
v_lshlrev_b32 v92, 16, v28
v_and_b32 v93, 0xffff0000, v28
G 28,31
v_lshlrev_b32 v94, 16, v28
v_and_b32 v95, 0xffff0000, v28
G 28,32
v_lshlrev_b32 v96, 16, v28
v_and_b32 v97, 0xffff0000, v28
G 28,33
v_lshlrev_b32 v98, 16, v28
v_and_b32 v99, 0xffff0000, v28
G 28,34
v_lshlrev_b32 v100, 16, v28
v_and_b32 v101, 0xffff0000, v28
G 28,35
v_lshlrev_b32 v102, 16, v28
v_and_b32 v103, 0xffff0000, v28
G 28,36
v_lshlrev_b32 v104, 16, v28
v_and_b32 v105, 0xffff0000, v28
G 28,37
v_lshlrev_b32 v106, 16, v28
v_and_b32 v107, 0xffff0000, v28
G 28,38
v_lshlrev_b32 v108, 16, v28
v_and_b32 v109, 0xffff0000, v28
G 28,39
v_lshlrev_b32 v110, 16, v28
v_and_b32 v111, 0xffff0000, v28
G 28,40
v_lshlrev_b32 v112, 16, v28
v_and_b32 v113, 0xffff0000, v28
G 28,41
v_lshlrev_b32 v114, 16, v28
v_and_b32 v115, 0xffff0000, v28
G 28,42
v_lshlrev_b32 v116, 16, v28
v_and_b32 v117, 0xffff0000, v28
G 28,43
v_lshlrev_b32 v118, 16, v28
v_and_b32 v119, 0xffff0000, v28
G 28,44
v_lshlrev_b32 v120, 16, v28
v_and_b32 v121, 0xffff0000, v28
G 28,45
v_lshlrev_b32 v122, 16, v28
v_and_b32 v123, 0xffff0000, v28
G 28,46
v_lshlrev_b32 v124, 16, v28
v_and_b32 v125, 0xffff0000, v28
G 28,47
v_lshlrev_b32 v126, 16, v28
v_and_b32 v127, 0xffff0000, v28
G 28,48
v_lshlrev_b32 v128, 16, v28
v_and_b32 v129, 0xffff0000, v28
G 28,49
v_lshlrev_b32 v130, 16, v28
v_and_b32 v131, 0xffff0000, v28
G 28,50
v_lshlrev_b32 v132, 16, v28
v_and_b32 v133, 0xffff0000, v28
G 28,51
v_lshlrev_b32 v134, 16, v28
v_and_b32 v135, 0xffff0000, v28
G 28,52
v_lshlrev_b32 v136, 16, v28
v_and_b32 v137, 0xffff0000, v28
G 28,53
v_lshlrev_b32 v138, 16, v28
v_and_b32 v139, 0xffff0000, v28
G 28,54
v_lshlrev_b32 v140, 16, v28
v_and_b32 v141, 0xffff0000, v28
G 28,55
v_lshlrev_b32 v142, 16, v28
v_and_b32 v143, 0xffff0000, v28
G 28,56
v_lshlrev_b32 v144, 16, v28
v_and_b32 v145, 0xffff0000, v28
G 28,57
v_lshlrev_b32 v146, 16, v28
v_and_b32 v147, 0xffff0000, v28
G 28,58
v_lshlrev_b32 v148, 16, v28
v_and_b32 v149, 0xffff0000, v28
G 28,59
v_lshlrev_b32 v150, 16, v28
v_and_b32 v151, 0xffff0000, v28
G 28,60
v_lshlrev_b32 v152, 16, v28
v_and_b32 v153, 0xffff0000, v28
G 28,61
v_lshlrev_b32 v154, 16, v28
v_and_b32 v155, 0xffff0000, v28
G 28,62
v_lshlrev_b32 v156, 16, v28
v_and_b32 v157, 0xffff0000, v28
G 28,63
v_lshlrev_b32 v158, 16, v28
v_and_b32 v159, 0xffff0000, v28
s_mov_b32 s22, 1.0
s_mov_b32 s23, 1.0
s_mov_b32 s29, 134217728
s_mov_b32 s71, 3584
s_barrier
Ra 128,242,0
Ra 132,242,1024
Ra 136,242,2048
Ra 140,242,3072
s_cmp_ge_u32 s14, 2
s_cbranch_scc1 .Lbgo%=
s_branch .Lago%=
.p2align 12
.Lpb_31%=:
s_waitcnt vmcnt(6) lgkmcnt(6)
Zvaa 32,0,32
s_add_u32 s28, s28, 512
Ta 20,4,8192
Ta 22,4,9216
Mvaa 32,4,48
s_cmp_eq_u32 s28, s4
s_cselect_b32 s93, 10240, 0
Ta 24,5,0
Ta 26,5,1024
Zvaa 64,4,52
s_sub_u32 s28, s28, s93
s_add_u32 s93, s28, 20480
buffer_load_dwordx4 a[128:131], v26, s[8:11], s28 offen offset:0 sc1
Mvaa 64,0,36
Ta 28,5,8192
Ta 30,5,9216
Zvaa 96,0,40
K 184,184,185
K 185,186,187
Mvaa 96,4,56
K 186,188,189
K 187,190,191
Zvaa 128,4,60
K 192,192,193
Mvaa 128,0,44
W 8
buffer_load_dwordx4 a[132:135], v26, s[8:11], s93 offen offset:0 sc1
K 193,194,195
Zvaa 132,8,44
Ta 0,6,0
Ta 2,6,1024
Mvaa 132,12,60
K 194,196,197
K 195,198,199
Zvaa 100,12,56
K 200,200,201
K 201,202,203
Mvaa 100,8,40
Ta 4,6,8192
Ta 6,6,9216
Zvaa 68,8,36
buffer_load_dwordx4 a[136:139], v27, s[8:11], s28 offen offset:0 sc1
K 202,204,205
Mvaa 68,12,52
K 203,206,207
K 208,208,209
Zvaa 36,12,48
K 209,210,211
K 210,212,213
Mvaa 36,8,32
W 8
Ta 8,7,0
K 211,214,215
Zvaa 40,16,32
Ta 10,7,1024
K 216,216,217
Mvaa 40,20,48
buffer_load_dwordx4 a[140:143], v27, s[8:11], s93 offen offset:0 sc1
K 217,218,219
Zvaa 72,20,52
s_add_u32 s28, s28, 512
s_cmp_eq_u32 s28, s4
s_cselect_b32 s93, 10240, 0
s_sub_u32 s28, s28, s93
Mvaa 72,16,36
s_add_u32 s93, s28, 20480
K 218,220,221
K 219,222,223
Zvaa 104,16,40
Ta 12,7,8192
Ta 14,7,9216
Mvaa 104,20,56
K 224,224,225
K 225,226,227
Zvaa 136,20,60
K 226,228,229
K 227,230,231
Mvaa 136,16,44
W 8
buffer_load_dwordx4 a[144:147], v26, s[8:11], s28 offen offset:0 sc1
K 232,232,233
Zvaa 140,24,44
K 233,234,235
K 234,236,237
Mvaa 140,28,60
Ta 16,8,0
K 235,238,239
Zvaa 108,28,56
Ta 18,8,1024
K 240,240,241
Mvaa 108,24,40
K 241,242,243
K 242,244,245
Zvaa 76,24,36
buffer_load_dwordx4 a[148:151], v26, s[8:11], s93 offen offset:0 sc1
K 243,246,247
Mvaa 76,28,52
Ta 20,8,8192
Ta 22,8,9216
Zvaa 44,28,48
K 248,248,249
K 249,250,251
Mvaa 44,24,32
K 250,252,253
K 251,254,255
W 8
Zvaa 48,0,32
Ta 24,9,0
Ta 26,9,1024
Mvaa 48,4,48
buffer_load_dwordx4 a[152:155], v27, s[8:11], s28 offen offset:0 sc1
K 160,160,161
Zvaa 80,4,52
K 161,162,163
K 162,164,165
Mvaa 80,0,36
Ta 28,9,8192
K 163,166,167
Zvaa 112,0,40
Ta 30,9,9216
K 168,168,169
Mvaa 112,4,56
K 169,170,171
K 170,172,173
Zvaa 144,4,60
buffer_load_dwordx4 a[156:159], v27, s[8:11], s93 offen offset:0 sc1
K 171,174,175
Mvaa 144,0,44
W 8
s_add_u32 s28, s28, 512
s_cmp_eq_u32 s28, s4
s_cselect_b32 s93, 10240, 0
Zvaa 148,8,44
s_sub_u32 s28, s28, s93
s_add_u32 s93, s28, 20480
G 164,184
Mvaa 148,12,60
G 165,185
G 166,186
Zvaa 116,12,56
Ta 0,2,16384
Ta 2,2,17408
Mvaa 116,8,40
buffer_load_dwordx4 v[188:191], v26, s[8:11], s28 offen offset:0 sc1
G 167,187
Zvaa 84,8,36
Ta 4,2,24576
G 172,188
Mvaa 84,12,52
Ta 6,2,25600
G 173,189
Zvaa 52,12,48
G 174,190
G 175,191
Mvaa 52,8,32
W 8
Ta 8,3,16384
K 164,164,165
Zvaa 56,16,32
buffer_load_dwordx4 v[196:199], v26, s[8:11], s93 offen offset:0 sc1
Ta 10,3,17408
Mvaa 56,20,48
K 165,166,167
K 166,172,173
Zvaa 88,20,52
Ta 12,3,24576
K 167,174,175
Mvaa 88,16,36
Ta 14,3,25600
v_accvgpr_write_b32 a184, v164
Zvaa 120,16,40
Mvaa 120,20,56
buffer_load_dwordx4 v[204:207], v27, s[8:11], s28 offen offset:0 sc1
v_accvgpr_write_b32 a185, v165
Zvaa 152,20,60
v_accvgpr_write_b32 a186, v166
Mvaa 152,16,44
W 8
Ta 16,4,16384
v_accvgpr_write_b32 a187, v167
Zvaa 156,24,44
Ta 18,4,17408
v_accvgpr_write_b32 a188, v240
Mvaa 156,28,60
Zvaa 124,28,56
buffer_load_dwordx4 v[212:215], v27, s[8:11], s93 offen offset:0 sc1
v_accvgpr_write_b32 a189, v241
Mvaa 124,24,40
s_add_u32 s28, s28, 512
)"
R"(s_cmp_eq_u32 s28, s4
s_cselect_b32 s93, 10240, 0
s_sub_u32 s28, s28, s93
Zvaa 92,24,36
s_add_u32 s93, s28, 20480
Ta 20,4,24576
Ta 22,4,25600
Mvaa 92,28,52
v_accvgpr_write_b32 a190, v242
v_accvgpr_write_b32 a191, v243
Zvaa 60,28,48
G 164,192
Mvaa 60,24,32
s_waitcnt vmcnt(4) lgkmcnt(8)
buffer_load_dwordx4 v[220:223], v26, s[8:11], s28 offen offset:0 sc1
G 165,193
Mvaa 32,0,128
Ta 24,5,16384
Ta 26,5,17408
Mvaa 32,4,144
G 166,194
G 167,195
Mvaa 64,4,148
Ta 28,5,24576
Ta 30,5,25600
Mvaa 64,0,132
G 172,196
Mvaa 96,0,136
buffer_load_dwordx4 v[228:231], v26, s[8:11], s93 offen offset:0 sc1
G 173,197
Mvaa 96,4,152
G 174,198
Mvaa 128,4,156
G 175,199
Mvaa 128,0,140
W 8
Ta 0,6,16384
Ta 2,6,17408
Mvaa 132,8,140
K 164,164,165
Mvaa 132,12,156
buffer_load_dwordx4 v[236:239], v27, s[8:11], s28 offen offset:0 sc1
K 165,166,167
Mvaa 100,12,152
Ta 4,6,24576
K 166,172,173
Mvaa 100,8,136
Ta 6,6,25600
K 167,174,175
Mvaa 68,8,132
v_accvgpr_write_b32 a192, v164
Mvaa 68,12,148
Mvaa 36,12,144
buffer_load_dwordx4 v[244:247], v27, s[8:11], s93 offen offset:0 sc1
v_accvgpr_write_b32 a193, v165
Mvaa 36,8,128
W 8
s_add_u32 s28, s28, 512
s_cmp_eq_u32 s28, s4
s_cselect_b32 s93, 10240, 0
Mvaa 40,16,128
s_sub_u32 s28, s28, s93
s_add_u32 s93, s28, 20480
v_accvgpr_write_b32 a194, v166
Mvaa 40,20,144
Ta 8,7,16384
Ta 10,7,17408
Mvaa 72,20,148
v_accvgpr_write_b32 a195, v167
v_accvgpr_write_b32 a196, v248
Mvaa 72,16,132
D 22,48,65,2048
Ta 12,7,24576
Mvaa 104,16,136
Ta 14,7,25600
v_accvgpr_write_b32 a197, v249
Mvaa 104,20,152
v_accvgpr_write_b32 a198, v250
v_accvgpr_write_b32 a199, v251
Mvaa 136,20,156
G 164,200
Mvaa 136,16,140
W 8
Ta 16,8,16384
Ta 18,8,17408
Mvaa 140,24,140
buffer_load_dwordx4 a[32:35], v26, s[8:11], s28 offen offset:0 sc1
G 165,201
Mvaa 140,28,156
G 166,202
Mvaa 108,28,152
Ta 20,8,24576
G 167,203
Mvaa 108,24,136
Ta 22,8,25600
G 172,204
Mvaa 76,24,132
Mvaa 76,28,148
buffer_load_dwordx4 a[36:39], v26, s[8:11], s93 offen offset:0 sc1
G 173,205
Mvaa 44,28,144
G 174,206
Mvaa 44,24,128
W 8
Ta 24,9,16384
G 175,207
Mvaa 48,0,128
Ta 26,9,17408
K 164,164,165
Mvaa 48,4,144
Mvaa 80,4,148
buffer_load_dwordx4 a[40:43], v27, s[8:11], s28 offen offset:0 sc1
K 165,166,167
Mvaa 80,0,132
Ta 28,9,24576
Ta 30,9,25600
Mvaa 112,0,136
s_lshl_b32 s80, s56, 9
K 166,172,173
K 167,174,175
Mvaa 112,4,152
v_accvgpr_write_b32 a200, v164
Mvaa 144,4,156
s_lshl_b32 s81, s57, 4
Mvaa 144,0,140
W 8
buffer_load_dwordx4 a[44:47], v27, s[8:11], s93 offen offset:0 sc1
v_accvgpr_write_b32 a201, v165
Mvaa 148,8,140
s_add_u32 s28, s28, 512
s_cmp_eq_u32 s28, s4
s_cselect_b32 s93, 10240, 0
s_sub_u32 s28, s28, s93
Mvaa 148,12,156
s_add_u32 s93, s28, 20480
s_add_u32 s80, s80, s81
Ta 0,10,0
Mvaa 116,12,152
s_and_b32 s81, s14, 1
Ta 2,10,1024
v_accvgpr_write_b32 a202, v166
Mvaa 116,8,136
s_lshl_b32 s81, s81, 3
Ta 4,10,8192
v_accvgpr_write_b32 a203, v167
Mvaa 84,8,132
s_add_u32 s80, s80, s81
buffer_load_dwordx4 a[48:51], v26, s[8:11], s28 offen offset:0 sc1
Ta 6,10,9216
Mvaa 84,12,148
s_add_u32 s71, s80, 4096
v_accvgpr_write_b32 a204, v184
v_accvgpr_write_b32 a205, v185
Mvaa 52,12,144
v_accvgpr_write_b32 a206, v186
v_accvgpr_write_b32 a207, v187
Mvaa 52,8,128
W 8
Ta 8,11,0
Ta 10,11,1024
Mvaa 56,16,128
G 164,208
Mvaa 56,20,144
D 23,48,65,3072
G 165,209
Mvaa 88,20,148
s_add_u32 s65, s5, 16
s_cmp_ge_u32 s65, 20
s_cselect_b32 s90, 20, 0
s_sub_u32 s65, s65, s90
Mvaa 88,16,132
s_lshl_b32 s65, s65, 18
s_add_u32 m0, s63, 98304
G 166,210
Mvaa 120,16,136
Ta 12,11,8192
Ta 14,11,9216
Mvaa 120,20,152
G 167,211
G 172,212
Mvaa 152,20,156
buffer_load_dwordx4 a[52:55], v26, s[8:11], s93 offen offset:0 sc1
G 173,213
Mvaa 152,16,140
W 8
Ta 16,12,0
G 174,214
Mvaa 156,24,140
s_lshr_b32 s80, s15, 3
Ta 18,12,1024
Mvaa 156,28,156
G 175,215
Mvaa 124,28,152
s_lshl_b32 s80, s80, 1
Ta 20,12,8192
K 164,164,165
Mvaa 124,24,136
s_add_u32 s80, s80, s14
buffer_load_dwordx4 a[56:59], v27, s[8:11], s28 offen offset:0 sc1
Ta 22,12,9216
Mvaa 92,24,132
s_sub_u32 s80, s80, 2
K 165,166,167
K 166,172,173
Mvaa 92,28,148
s_lshl_b32 s80, s80, 12
Mvaa 60,28,144
K 167,174,175
v_min_u32 v28, 496, v1
Mvaa 60,24,128
s_waitcnt vmcnt(9) lgkmcnt(8)
v_accvgpr_write_b32 a208, v164
v_lshl_add_u32 v28, v28, 3, s80
Mvav 32,0,188
buffer_load_dwordx4 a[60:63], v27, s[8:11], s93 offen offset:0 sc1
Ta 24,13,0
Mvav 32,4,220
s_add_u32 s28, s28, 512
s_cmp_eq_u32 s28, s4
s_cselect_b32 s93, 10240, 0
s_sub_u32 s28, s28, s93
Mvav 64,4,228
s_add_u32 s93, s28, 20480
Ta 26,13,1024
v_min_u32 v28, .Lend%=-.Lbeg%=-4, v28
Mvav 64,0,196
v_accvgpr_write_b32 a209, v165
v_accvgpr_write_b32 a210, v166
Mvav 96,0,204
Ta 28,13,8192
Ta 30,13,9216
Mvav 96,4,236
D 16,48,65,0
v_accvgpr_write_b32 a211, v167
Mvav 128,4,244
v_accvgpr_write_b32 a212, v192
v_accvgpr_write_b32 a213, v193
Mvav 128,0,212
W 8
Ta 0,14,0
v_accvgpr_write_b32 a214, v194
Mvav 132,8,212
Ta 2,14,1024
v_add_u32 v2, 65536, v10
Mvav 132,12,244
v_accvgpr_write_b32 a215, v195
G 164,216
Mvav 100,12,236
buffer_load_dwordx4 v[184:187], v26, s[8:11], s28 offen offset:0 sc1
Ta 4,14,8192
Mvav 100,8,204
Ta 6,14,9216
v_add_u32 v3, 65536, v11
Mvav 68,8,196
G 165,217
G 166,218
Mvav 68,12,228
G 167,219
Mvav 36,12,220
G 172,220
Mvav 36,8,188
W 8
D 17,48,65,1024
Ta 8,15,0
Mvav 40,16,188
Ta 10,15,1024
v_add_u32 v4, 65536, v12
Mvav 40,20,220
G 173,221
G 174,222
Mvav 72,20,228
Ta 12,15,8192
G 175,223
Mvav 72,16,196
Ta 14,15,9216
v_add_u32 v5, 65536, v13
Mvav 104,16,204
buffer_load_dwordx4 a[128:131], v26, s[8:11], s93 offen offset:0 sc1
K 164,164,165
Mvav 104,20,236
K 165,166,167
Mvav 136,20,244
K 166,172,173
Mvav 136,16,212
W 8
Ta 16,24,0
K 167,174,175
Mvav 140,24,212
Ta 18,24,1024
v_add_u32 v6, 65536, v14
Mvav 140,28,244
buffer_load_dwordx4 a[132:135], v27, s[8:11], s28 offen offset:0 sc1
v_accvgpr_write_b32 a216, v164
Mvav 108,28,236
Ta 20,24,8192
v_accvgpr_write_b32 a217, v165
Mvav 108,24,204
Ta 22,24,9216
v_add_u32 v7, 65536, v15
Mvav 76,24,196
v_accvgpr_write_b32 a218, v166
v_accvgpr_write_b32 a219, v167
Mvav 76,28,228
v_accvgpr_write_b32 a220, v200
Mvav 44,28,220
buffer_load_dwordx4 a[136:139], v27, s[8:11], s93 offen offset:0 sc1
Mvav 44,24,188
W 8
s_add_u32 s28, s28, 512
s_cmp_eq_u32 s28, s4
s_cselect_b32 s93, 10240, 0
Mvav 48,0,188
s_sub_u32 s28, s28, s93
s_add_u32 s93, s28, 20480
v_accvgpr_write_b32 a221, v201
Mvav 48,4,220
Ta 24,25,0
Ta 26,25,1024
Mvav 80,4,228
v_add_u32 v8, 65536, v24
v_accvgpr_write_b32 a222, v202
Mvav 80,0,196
buffer_load_dwordx4 a[140:143], v26, s[8:11], s28 offen offset:0 sc1
v_accvgpr_write_b32 a223, v203
Mvav 112,0,204
Ta 28,25,8192
Ta 30,25,9216
Mvav 112,4,236
v_add_u32 v9, 65536, v25
G 164,224
Mvav 144,4,244
G 165,225
G 166,226
Mvav 144,0,212
W 8
Ta 0,10,16384
G 167,227
Mvav 148,8,212
global_load_dword v29, v28, s[6:7]
Ta 2,10,17408
Mvav 148,12,244
s_sub_u32 s74, s85, 66
s_and_b32 s75, s74, 0xffffffe3
s_cselect_b64 s[72:73], -1, 0
s_and_b32 s74, s74, 28
Mvav 116,12,236
s_lshl_b32 s74, s74, 1
v_mov_b32 v28, s74
G 172,228
Mvav 116,8,204
G 173,229
G 174,230
Mvav 84,8,196
Ta 4,10,24576
Ta 6,10,25600
Mvav 84,12,228
buffer_load_dwordx4 a[144:147], v26, s[8:11], s93 offen offset:0 sc1
G 175,231
Mvav 52,12,220
K 164,164,165
K 165,166,167
Mvav 52,8,188
W 8
Ta 8,11,16384
K 166,172,173
Mvav 56,16,188
Ta 10,11,17408
K 167,174,175
Mvav 56,20,220
v_accvgpr_write_b32 a224, v164
Mvav 88,20,228
buffer_load_dwordx4 a[148:151], v27, s[8:11], s28 offen offset:0 sc1
Ta 12,11,24576
Mvav 88,16,196
Ta 14,11,25600
v_accvgpr_write_b32 a225, v165
Mvav 120,16,204
v_accvgpr_write_b32 a226, v166
Mvav 120,20,236
v_accvgpr_write_b32 a227, v167
Mvav 152,20,244
v_accvgpr_write_b32 a228, v208
Mvav 152,16,212
W 8
D 18,48,65,2048
Ta 16,12,16384
Mvav 156,24,212
Ta 18,12,17408
v_accvgpr_write_b32 a229, v209
Mvav 156,28,244
v_accvgpr_write_b32 a230, v210
Mvav 124,28,236
Ta 20,12,24576
v_accvgpr_write_b32 a231, v211
Mvav 124,24,204
Ta 22,12,25600
G 164,232
Mvav 92,24,196
buffer_load_dwordx4 a[152:155], v27, s[8:11], s93 offen offset:0 sc1
G 165,233
)"
R"(Mvav 92,28,228
s_add_u32 s28, s28, 512
s_cmp_eq_u32 s28, s4
s_cselect_b32 s93, 10240, 0
s_sub_u32 s28, s28, s93
Mvav 60,28,220
s_add_u32 s93, s28, 20480
G 166,234
G 167,235
Mvav 60,24,188
s_waitcnt vmcnt(12) lgkmcnt(8)
G 172,236
G 173,237
Mvaa 32,0,32
Ta 24,13,16384
Ta 26,13,17408
Mvaa 32,4,48
buffer_load_dwordx2 v[28:29], v28, s[8:11], 0 offen offset:3584 sc1
G 174,238
Mvaa 64,4,52
G 175,239
K 164,164,165
Mvaa 64,0,36
Ta 28,13,24576
Ta 30,13,25600
Mvaa 96,0,40
K 165,166,167
K 166,172,173
Mvaa 96,4,56
K 167,174,175
v_accvgpr_write_b32 a232, v164
Mvaa 128,4,60
buffer_load_dwordx4 a[156:159], v26, s[8:11], s28 offen offset:0 sc1
v_accvgpr_write_b32 a233, v165
Mvaa 128,0,44
W 8
v_accvgpr_write_b32 a234, v166
v_accvgpr_write_b32 a235, v167
Mvaa 132,8,44
Ta 0,14,16384
Ta 2,14,17408
Mvaa 132,12,60
G 164,160
G 165,161
Mvaa 100,12,56
G 166,162
G 167,163
Mvaa 100,8,40
buffer_load_dwordx4 v[192:195], v26, s[8:11], s93 offen offset:0 sc1
Ta 4,14,24576
Mvaa 68,8,36
Ta 6,14,25600
G 172,164
Mvaa 68,12,52
G 173,165
G 174,166
Mvaa 36,12,48
G 175,167
K 164,164,165
Mvaa 36,8,32
W 8
Ta 8,15,16384
K 165,166,167
Mvaa 40,16,32
buffer_load_dwordx4 v[200:203], v27, s[8:11], s28 offen offset:0 sc1
Ta 10,15,17408
Mvaa 40,20,48
K 166,172,173
K 167,174,175
Mvaa 72,20,52
s_cmp_lg_u32 s72, 0
s_cbranch_scc1 .LL_55%=
s_waitcnt vmcnt(3)
.LL_55%=:
v_xor_b32 v28, s12, v28
v_xor_b32 v29, s13, v29
v_lshrrev_b32 v248, 11, v28
v_or_b32 v29, v29, v248
v_cmp_eq_u32 vcc, 0, v29
s_or_b64 vcc, vcc, s[72:73]
s_cmp_eq_u64 vcc, -1
s_cbranch_scc0 .Lspin_56%=
.Lok_57%=:
v_accvgpr_write_b32 a236, v164
v_accvgpr_write_b32 a237, v165
Mvaa 72,16,36
Ta 12,15,24576
Ta 14,15,25600
Mvaa 104,16,40
v_accvgpr_write_b32 a238, v166
v_accvgpr_write_b32 a239, v167
Mvaa 104,20,56
D 19,48,65,3072
G 164,240
Mvaa 136,20,60
s_add_u32 m0, s63, 102400
G 165,241
G 166,242
Mvaa 136,16,44
W 8
v_bfe_u32 v248, v28, 3, 8
G 167,243
Mvaa 140,24,44
Ta 16,24,16384
Ta 18,24,17408
Mvaa 140,28,60
v_mul_u32_u24 v248, 10240, v248
G 172,244
Mvaa 108,28,56
buffer_load_dwordx4 v[188:191], v27, s[8:11], s93 offen offset:0 sc1
G 173,245
Mvaa 108,24,40
s_add_u32 s28, s28, 512
s_cmp_eq_u32 s28, s4
s_cselect_b32 s93, 10240, 0
s_sub_u32 s28, s28, s93
Mvaa 76,24,36
s_add_u32 s93, s28, 20480
v_and_b32 v28, 7, v28
G 174,246
Mvaa 76,28,52
v_lshl_add_u32 v248, v28, 4, v248
G 175,247
Mvaa 44,28,48
Ta 20,24,24576
Ta 22,24,25600
Mvaa 44,24,32
W 8
buffer_load_dwordx4 v[196:199], v26, s[8:11], s28 offen offset:0 sc1
K 164,164,165
Mvaa 48,0,32
v_lshl_add_u32 v248, v1, 3, v248
K 165,166,167
Mvaa 48,4,48
v_lshrrev_b32 v29, 8, v1
K 166,172,173
Mvaa 80,4,52
K 167,174,175
v_lshlrev_b32 v29, 28, v29
Mvaa 80,0,36
v_accvgpr_write_b32 a240, v164
v_or_b32 v249, v248, v29
Mvaa 112,0,40
s_and_b32 s75, s72, 134217728
buffer_load_dwordx4 v[204:207], v26, s[8:11], s93 offen offset:0 sc1
v_accvgpr_write_b32 a241, v165
Mvaa 112,4,56
s_add_u32 s76, s75, 131072
Ta 24,25,16384
Ta 26,25,17408
Mvaa 144,4,60
v_accvgpr_write_b32 a242, v166
v_accvgpr_write_b32 a243, v167
Mvaa 144,0,44
W 6
G 164,168
G 165,169
Mvaa 148,8,44
Ta 28,25,24576
Ta 30,25,25600
Mvaa 148,12,60
D 20,48,65,0
G 166,170
Mvaa 116,12,56
G 167,171
G 172,172
Mvaa 116,8,40
G 173,173
G 174,174
Mvaa 84,8,36
Ta 0,2,0
G 175,175
Mvaa 84,12,52
Ta 2,2,1024
K 164,164,165
Mvaa 52,12,48
buffer_load_dwordx4 v[212:215], v27, s[8:11], s28 offen offset:0 sc1
K 165,166,167
Mvaa 52,8,32
W 6
K 166,172,173
K 167,174,175
Mvaa 56,16,32
Ta 4,2,8192
Ta 6,2,9216
Mvaa 56,20,48
v_accvgpr_write_b32 a244, v164
v_accvgpr_write_b32 a245, v165
Mvaa 88,20,52
v_accvgpr_write_b32 a246, v166
v_accvgpr_write_b32 a247, v167
Mvaa 88,16,36
buffer_load_dwordx4 v[220:223], v27, s[8:11], s93 offen offset:0 sc1
G 164,64
Mvaa 120,16,40
s_add_u32 s28, s28, 512
s_cmp_eq_u32 s28, s4
s_cselect_b32 s93, 10240, 0
s_sub_u32 s28, s28, s93
Mvaa 120,20,56
s_add_u32 s93, s28, 20480
Ta 8,3,0
G 165,65
Mvaa 152,20,60
Ta 10,3,1024
G 166,66
Mvaa 152,16,44
W 6
G 167,67
G 172,68
Mvaa 156,24,44
buffer_load_dwordx4 v[228:231], v26, s[8:11], s28 offen offset:0 sc1
G 173,69
Mvaa 156,28,60
Ta 12,3,8192
Ta 14,3,9216
Mvaa 124,28,56
G 174,70
G 175,71
Mvaa 124,24,40
K 164,164,165
K 165,166,167
Mvaa 92,24,36
K 166,172,173
K 167,174,175
Mvaa 92,28,52
buffer_load_dwordx4 v[240:243], v248, s[8:11], s76 offen sc1
Ta 16,4,0
Mvaa 60,28,48
s_add_u32 s76, s76, 8192
Ta 18,4,1024
v_accvgpr_write_b32 a64, v164
Mvaa 60,24,32
s_waitcnt vmcnt(13) lgkmcnt(6)
v_accvgpr_write_b32 a65, v165
v_accvgpr_write_b32 a66, v166
Mvav 32,0,184
Ta 20,4,8192
v_accvgpr_write_b32 a67, v167
Mvaa 32,4,140
Ta 22,4,9216
G 164,176
Mvaa 64,4,144
buffer_load_dwordx4 v[244:247], v249, s[8:11], s76 offen sc1
v_or_b32 v249, v1, v29
Mvaa 64,0,128
s_mul_i32 s76, s74, 160
s_add_u32 s76, s76, s75
s_add_u32 s76, s76, 118784
Mvaa 96,0,132
G 165,177
G 166,178
Mvaa 96,4,148
G 167,179
G 172,180
Mvaa 128,4,152
Ta 24,5,0
G 173,181
Mvaa 128,0,136
W 5
buffer_load_dwordx4 v[236:239], v26, s[8:11], s93 offen offset:0 sc1
Ta 26,5,1024
Mvaa 132,8,136
G 174,182
G 175,183
Mvaa 132,12,152
K 164,164,165
K 165,166,167
Mvaa 100,12,148
Ta 28,5,8192
Ta 30,5,9216
Mvaa 100,8,132
K 166,172,173
K 167,174,175
Mvaa 68,8,128
D 21,48,65,1024
v_accvgpr_write_b32 a68, v164
Mvaa 68,12,144
v_accvgpr_write_b32 a69, v165
v_accvgpr_write_b32 a70, v166
Mvaa 36,12,140
Ta 0,6,0
v_accvgpr_write_b32 a71, v167
Mvav 36,8,184
W 5
Ta 2,6,1024
G 164,72
Mvav 40,16,184
G 165,73
G 166,74
Mvaa 40,20,140
buffer_load_dwordx4 v[208:211], v27, s[8:11], s28 offen offset:0 sc1
Ta 4,6,8192
Mvaa 72,20,144
Ta 6,6,9216
G 167,75
Mvaa 72,16,128
G 172,76
G 173,77
Mvaa 104,16,132
Ta 8,7,0
G 174,78
Mvaa 104,20,148
Ta 10,7,1024
G 175,79
Mvaa 136,20,152
buffer_load_dwordx4 a[32:35], v27, s[8:11], s93 offen offset:0 sc1
K 164,164,165
Mvaa 136,16,136
W 6
s_add_u32 s28, s28, 512
s_cmp_eq_u32 s28, s4
s_cselect_b32 s93, 10240, 0
Mvaa 140,24,136
s_sub_u32 s28, s28, s93
s_add_u32 s93, s28, 20480
Ta 12,7,8192
Mvaa 140,28,152
Ta 14,7,9216
K 165,166,167
Mvaa 108,28,148
K 166,172,173
K 167,174,175
Mvaa 108,24,132
buffer_load_dwordx4 a[36:39], v26, s[8:11], s28 offen offset:0 sc1
v_accvgpr_write_b32 a72, v164
Mvaa 76,24,128
Ta 16,8,0
Ta 18,8,1024
Mvaa 76,28,144
v_accvgpr_write_b32 a73, v165
v_accvgpr_write_b32 a74, v166
Mvaa 44,28,140
Ta 20,8,8192
v_accvgpr_write_b32 a75, v167
Mvav 44,24,184
W 7
Ta 22,8,9216
v_accvgpr_write_b32 a76, v216
Mvav 48,0,184
D 22,48,65,2048
v_accvgpr_write_b32 a77, v217
Mvaa 48,4,140
v_accvgpr_write_b32 a78, v218
v_accvgpr_write_b32 a79, v219
Mvaa 80,4,144
Ta 24,9,0
Ta 26,9,1024
Mvaa 80,0,128
G 164,80
G 165,81
Mvaa 112,0,132
Ta 28,9,8192
Ta 30,9,9216
Mvaa 112,4,148
buffer_load_dwordx4 a[40:43], v26, s[8:11], s93 offen offset:0 sc1
G 166,82
Mvaa 144,4,152
G 167,83
G 172,84
Mvaa 144,0,136
W 8
Ta 0,2,16384
G 173,85
Mvaa 148,8,136
s_cmp_eq_u32 s14, 2
s_cselect_b64 s[88:89], s[30:31], s[32:33]
Ta 2,2,17408
G 174,86
Mvaa 148,12,152
s_lshr_b32 s77, s15, 3
s_mul_i32 s78, s77, 69120
s_mul_i32 s79, s59, 448
Mvaa 116,12,148
s_andn2_b32 s79, s79, 127
s_add_u32 s78, s78, s79
buffer_load_dwordx4 a[44:47], v27, s[8:11], s28 offen offset:0 sc1
Mvaa 116,8,132
v_min_u32 v250, 624, v1
v_lshrrev_b32 v251, 6, v250
Mvaa 84,8,128
v_mul_u32_u24 v251, 3456, v251
v_and_b32 v250, 48, v250
Mvaa 84,12,144
v_lshl_add_u32 v250, v250, 3, v251
v_add_u32 v250, s78, v250
Mvaa 52,12,140
Ta 4,2,24576
G 175,87
Mvav 52,8,184
W 7
buffer_load_dwordx4 a[48:51], v27, s[8:11], s93 offen offset:0 sc1
Ta 6,2,25600
Mvav 56,16,184
s_add_u32 s28, s28, 512
s_cmp_eq_u32 s28, s4
s_cselect_b32 s93, 10240, 0
D 23,48,65,3072
Mvaa 56,20,140
s_sub_u32 s28, s28, s93
)"
R"(s_add_u32 s93, s28, 20480
K 164,164,165
Mvaa 88,20,144
K 165,166,167
K 166,172,173
Mvaa 88,16,128
Ta 8,3,16384
K 167,174,175
Mvaa 120,16,132
Ta 10,3,17408
v_accvgpr_write_b32 a80, v164
Mvaa 120,20,148
buffer_load_dwordx4 a[52:55], v26, s[8:11], s28 offen offset:0 sc1
v_accvgpr_write_b32 a81, v165
Mvaa 152,20,152
Ta 12,3,24576
v_accvgpr_write_b32 a82, v166
Mvaa 152,16,136
W 7
Ta 14,3,25600
v_accvgpr_write_b32 a83, v167
Mvaa 156,24,136
v_accvgpr_write_b32 a84, v224
v_lshrrev_b32 v20, 4, v1
Mvaa 156,28,152
v_accvgpr_write_b32 a85, v225
v_accvgpr_write_b32 a86, v226
Mvaa 124,28,148
buffer_load_dwordx4 a[56:59], v26, s[8:11], s93 offen offset:0 sc1
v_add_u32 v20, 0, v20
Mvaa 124,24,132
Ta 16,4,16384
Ta 18,4,17408
Mvaa 92,24,128
v_accvgpr_write_b32 a87, v227
v_mul_u32_u24 v22, 205, v20
Mvaa 92,28,144
Ta 20,4,24576
G 164,88
Mvaa 60,28,140
Ta 22,4,25600
v_lshrrev_b32 v22, 12, v22
Mvav 60,24,184
s_waitcnt vmcnt(15) lgkmcnt(8)
buffer_load_dwordx4 a[60:63], v27, s[8:11], s28 offen offset:0 sc1
G 165,89
Mvaa 32,0,156
G 166,90
v_mul_u32_u24 v23, 20, v22
Mvav 32,4,196
G 167,91
v_sub_u32 v20, v20, v23
Mvav 64,4,204
Ta 24,5,16384
G 172,92
Mvav 64,0,192
Ta 26,5,17408
v_lshrrev_b32 v23, 1, v22
Mvav 96,0,200
global_load_dword v251, v250, s[88:89]
v_add_u32 v250, 34560, v250
Mvav 96,4,212
v_min_u32 v250, 2211836, v250
G 173,93
Mvav 128,4,220
G 174,94
v_and_b32 v23, 2, v23
Mvav 128,0,188
W 6
Ta 28,5,24576
Ta 30,5,25600
Mvav 132,8,188
G 175,95
v_xor_b32 v20, v20, v23
Mvav 132,12,220
buffer_load_dwordx4 v[216:219], v27, s[8:11], s93 offen offset:0 sc1
K 164,164,165
Mvav 100,12,212
s_add_u32 s28, s28, 512
s_cmp_eq_u32 s28, s4
s_cselect_b32 s93, 10240, 0
s_sub_u32 s28, s28, s93
Mvav 100,8,200
s_add_u32 s93, s28, 20480
v_mul_u32_u24 v22, 80, v22
K 165,166,167
Mvav 68,8,192
K 166,172,173
v_add_lshl_u32 v16, v20, v22, 4
Mvav 68,12,204
Ta 0,6,16384
Ta 2,6,17408
Mvav 36,12,196
buffer_load_dwordx4 v[184:187], v26, s[8:11], s28 offen offset:0 sc1
K 167,174,175
Mvaa 36,8,156
W 6
v_lshrrev_b32 v20, 4, v1
v_accvgpr_write_b32 a88, v164
Mvaa 40,16,156
v_add_u32 v20, 64, v20
v_accvgpr_write_b32 a89, v165
Mvav 40,20,196
Ta 4,6,24576
Ta 6,6,25600
Mvav 72,20,204
v_accvgpr_write_b32 a90, v166
v_mul_u32_u24 v22, 205, v20
Mvav 72,16,192
s_cmp_lg_u32 s72, 0
s_cbranch_scc1 .LL_59%=
s_waitcnt vmcnt(16)
.LL_59%=:
buffer_store_dwordx4 v[240:243], v1, s[8:11], s76 offen sc1
v_accvgpr_write_b32 a91, v167
v_lshrrev_b32 v22, 12, v22
Mvav 104,16,200
v_accvgpr_write_b32 a92, v232
v_mul_u32_u24 v23, 20, v22
Mvav 104,20,212
Ta 8,7,16384
Ta 10,7,17408
Mvav 136,20,220
v_accvgpr_write_b32 a93, v233
v_accvgpr_write_b32 a94, v234
Mvav 136,16,188
W 6
v_sub_u32 v20, v20, v23
v_accvgpr_write_b32 a95, v235
Mvav 140,24,188
buffer_store_dwordx4 v[244:247], v249, s[8:11], s76 offen offset:1024 sc1
Ta 12,7,24576
Mvav 140,28,220
s_add_u32 s76, s74, s75
s_add_u32 s76, s76, 3840
Ta 14,7,25600
Mvav 108,28,212
v_lshrrev_b32 v23, 1, v22
G 164,96
Mvav 108,24,200
G 165,97
v_and_b32 v23, 2, v23
Mvav 76,24,192
G 166,98
G 167,99
Mvav 76,28,204
buffer_load_dwordx4 a[128:131], v26, s[8:11], s93 offen offset:0 sc1
v_xor_b32 v20, v20, v23
Mvav 44,28,196
G 172,100
G 173,101
Mvaa 44,24,156
W 4
Ta 16,8,16384
Ta 18,8,17408
Mvaa 48,0,156
v_mul_u32_u24 v22, 80, v22
G 174,102
Mvav 48,4,196
G 175,103
v_add_lshl_u32 v17, v20, v22, 4
Mvav 80,4,204
buffer_load_dwordx4 a[132:135], v27, s[8:11], s28 offen offset:0 sc1
K 164,164,165
Mvav 80,0,192
Ta 20,8,24576
Ta 22,8,25600
Mvav 112,0,200
K 165,166,167
K 166,172,173
Mvav 112,4,212
v_subrev_u32 v17, 1024, v17
K 167,174,175
Mvav 144,4,220
v_accvgpr_write_b32 a96, v164
v_lshrrev_b32 v20, 4, v1
Mvav 144,0,188
W 4
buffer_load_dwordx4 a[136:139], v27, s[8:11], s93 offen offset:0 sc1
v_accvgpr_write_b32 a97, v165
Mvav 148,8,188
s_add_u32 s28, s28, 512
v_accvgpr_write_b32 a98, v166
v_add_u32 v20, 128, v20
Mvav 148,12,220
Ta 24,9,16384
Ta 26,9,17408
Ta 28,9,24576
Ta 30,9,25600
Mvav 116,12,212
s_cmp_eq_u32 s28, s4
s_cselect_b32 s93, 10240, 0
v_accvgpr_write_b32 a99, v167
G 164,248
Mvav 116,8,200
s_sub_u32 s28, s28, s93
s_add_u32 s93, s28, 20480
v_mul_u32_u24 v22, 205, v20
Mvav 84,8,192
s_waitcnt vmcnt(11) lgkmcnt(0)
s_barrier
global_load_dword v251, v250, s[88:89]
Mvav 84,12,204
s_mov_b64 s[88:89], s[34:35]
s_lshr_b32 s77, s15, 3
s_lshl_b32 s78, s59, 5
Mvav 52,12,196
s_add_u32 s77, s77, s78
s_lshl_b32 s77, s77, 1
s_add_u32 s77, s77, s14
Mvaa 52,8,156
s_sub_u32 s77, s77, 2
s_mul_i32 s77, s77, 4480
v_min_u32 v250, 544, v1
Mvaa 56,16,156
v_lshl_add_u32 v250, v250, 3, s77
v_min_u32 v250, 2211836, v250
Mvav 56,20,196
buffer_load_dwordx4 a[140:143], v26, s[8:11], s28 offen offset:0 sc1
G 165,249
Mvav 88,20,204
G 166,250
v_lshrrev_b32 v22, 12, v22
Mvav 88,16,192
G 167,251
G 172,252
Mvav 120,16,200
G 173,253
G 174,254
G 175,255
K 164,164,165
K 165,166,167
K 166,172,173
K 167,174,175
v_accvgpr_write_b32 a100, v164
v_accvgpr_write_b32 a101, v165
v_accvgpr_write_b32 a102, v166
v_accvgpr_write_b32 a103, v167
Mvav 120,20,212
v_mul_u32_u24 v23, 20, v22
v_sub_u32 v20, v20, v23
Mvav 152,20,220
buffer_load_dwordx4 a[144:147], v26, s[8:11], s93 offen offset:0 sc1
v_lshrrev_b32 v23, 1, v22
Mvav 152,16,188
v_and_b32 v23, 2, v23
v_xor_b32 v20, v20, v23
Mvav 156,24,188
Ta 0,10,32768
Ta 2,10,33792
Mvav 156,28,220
Ta 4,10,40960
Ta 6,10,41984
Mvav 124,28,212
G 164,104
v_mul_u32_u24 v22, 80, v22
Mvav 124,24,200
G 165,105
v_add_lshl_u32 v18, v20, v22, 4
Mvav 92,24,192
Ta 8,11,32768
G 166,106
Mvav 92,28,204
Ta 10,11,33792
G 167,107
Mvav 60,28,196
buffer_load_dwordx4 a[148:151], v27, s[8:11], s28 offen offset:0 sc1
v_subrev_u32 v18, 2048, v18
Mvaa 60,24,156
W 2
G 172,108
G 173,109
Mvav 32,0,228
Ta 12,11,40960
v_lshrrev_b32 v20, 4, v1
Mvaa 32,4,36
Ta 14,11,41984
G 174,110
Mvaa 64,4,40
v_add_u32 v20, 192, v20
G 175,111
Mvav 64,0,236
K 164,164,165
v_mul_u32_u24 v22, 205, v20
Mvav 96,0,208
K 165,166,167
v_lshrrev_b32 v22, 12, v22
Mvaa 96,4,44
Ta 16,12,32768
K 166,172,173
Mvaa 128,4,48
Ta 18,12,33792
K 167,174,175
Mvaa 128,0,32
W 2
buffer_load_dwordx4 a[152:155], v27, s[8:11], s93 offen offset:0 sc1
v_mul_u32_u24 v23, 20, v22
Mvaa 132,8,32
s_add_u32 s28, s28, 512
v_accvgpr_write_b32 a104, v164
v_accvgpr_write_b32 a105, v165
Mvaa 132,12,48
s_cmp_eq_u32 s28, s4
s_cselect_b32 s93, 10240, 0
Ta 20,12,40960
v_sub_u32 v20, v20, v23
Mvaa 100,12,44
s_sub_u32 s28, s28, s93
Ta 22,12,41984
v_accvgpr_write_b32 a106, v166
Mvav 100,8,208
s_add_u32 s93, s28, 20480
v_lshrrev_b32 v23, 1, v22
v_accvgpr_write_b32 a107, v167
Mvav 68,8,236
K 176,176,177
v_and_b32 v23, 2, v23
Mvaa 68,12,40
K 177,178,179
K 178,180,181
K 179,182,183
v_accvgpr_write_b32 a108, v176
v_accvgpr_write_b32 a109, v177
v_accvgpr_write_b32 a110, v178
v_accvgpr_write_b32 a111, v179
Mvaa 36,12,36
Ta 24,13,32768
v_xor_b32 v20, v20, v23
Mvav 36,8,228
W 1
Ta 26,13,33792
buffer_load_dwordx4 v[224:227], v26, s[8:11], s28 offen offset:0 sc1
Mvav 40,16,228
Ta 28,13,40960
v_mul_u32_u24 v22, 80, v22
Mvaa 40,20,36
Ta 30,13,41984
v_add_lshl_u32 v19, v20, v22, 4
Mvaa 72,20,40
v_subrev_u32 v19, 3072, v19
G 164,112
Mvav 72,16,236
v_lshrrev_b32 v20, 4, v1
G 165,113
Mvav 104,16,208
s_cmp_lg_u32 s72, 0
s_cbranch_scc1 .LL_60%=
s_waitcnt vmcnt(9)
.LL_60%=:
v_mov_b64 v[28:29], s[12:13]
s_mov_b64 exec, 1
buffer_store_dwordx2 v[28:29], off, s[8:11], s76 sc1
s_mov_b64 exec, -1
Ta 0,14,32768
Ta 2,14,33792
Mvaa 104,20,44
G 166,114
v_add_u32 v20, 256, v20
Mvaa 136,20,48
G 167,115
v_mul_u32_u24 v22, 205, v20
Mvaa 136,16,32
W 2
Ta 4,14,40960
Ta 6,14,41984
Mvaa 140,24,32
G 172,116
v_lshrrev_b32 v22, 12, v22
Mvaa 140,28,48
global_load_dword v251, v250, s[88:89]
G 173,117
Mvaa 108,28,44
G 174,118
v_mul_u32_u24 v23, 20, v22
Mvav 108,24,208
G 175,119
v_sub_u32 v20, v20, v23
Mvav 76,24,236
Ta 8,15,32768
Ta 10,15,33792
Mvaa 76,28,40
K 164,164,165
v_lshrrev_b32 v23, 1, v22
Mvaa 44,28,36
buffer_load_dwordx4 v[232:235], v26, s[8:11], s93 offen offset:0 sc1
K 165,166,167
Mvav 44,24,228
W 2
K 166,172,173
v_and_b32 v23, 2, v23
Mvav 48,0,228
Ta 12,15,40960
Ta 14,15,41984
Mvaa 48,4,36
K 167,174,175
v_xor_b32 v20, v20, v23
Mvaa 80,4,40
v_accvgpr_write_b32 a112, v164
v_accvgpr_write_b32 a113, v165
Mvav 80,0,236
)"
R"(buffer_load_dwordx4 a[248:251], v27, s[8:11], s28 offen offset:0 sc1
v_mul_u32_u24 v22, 80, v22
Mvav 112,0,208
Ta 16,24,32768
v_accvgpr_write_b32 a114, v166
Mvaa 112,4,44
Ta 18,24,33792
v_add_lshl_u32 v20, v20, v22, 4
Mvaa 144,4,48
v_accvgpr_write_b32 a115, v167
v_accvgpr_write_b32 a116, v168
v_accvgpr_write_b32 a117, v169
v_accvgpr_write_b32 a118, v170
v_accvgpr_write_b32 a119, v171
Mvaa 144,0,32
W 2
Ta 20,24,40960
v_lshrrev_b32 v22, 6, v1
Mvaa 148,8,32
buffer_load_dwordx4 a[252:255], v27, s[8:11], s93 offen offset:0 sc1
Ta 22,24,41984
Mvaa 148,12,48
s_add_u32 s28, s28, 512
v_mul_u32_u24 v22, 320, v22
v_bfe_u32 v23, v1, 4, 2
Mvaa 116,12,44
s_cmp_eq_u32 s28, s4
s_cselect_b32 s93, 10240, 0
v_lshl_add_u32 v22, v23, 3, v22
G 164,120
Mvav 116,8,208
s_sub_u32 s28, s28, s93
Ta 24,25,32768
Ta 26,25,33792
Mvav 84,8,236
s_add_u32 s93, s28, 20480
G 165,121
v_bfe_u32 v23, v1, 8, 1
Mvaa 84,12,40
buffer_load_dwordx4 v[192:195], v26, s[8:11], s28 offen offset:0 sc1
G 166,122
Mvaa 52,12,36
v_lshlrev_b32 v23, 5, v23
G 167,123
Mvav 52,8,228
W 2
Ta 28,25,40960
Ta 30,25,41984
Mvav 56,16,228
v_add_u32 v21, v22, v23
G 172,124
Mvaa 56,20,36
G 173,125
v_sub_u32 v22, v22, v23
Mvaa 88,20,40
s_mul_i32 s94, s59, 655360
Ta 0,10,49152
G 174,126
Mvav 88,16,236
s_mul_i32 s95, s58, 320
Ta 2,10,50176
G 175,127
Mvav 120,16,208
s_add_u32 s94, s94, s95
buffer_load_dwordx4 v[200:203], v26, s[8:11], s93 offen offset:0 sc1
K 164,164,165
Mvaa 120,20,44
Ta 4,10,57344
K 165,166,167
Mvaa 152,20,48
Ta 6,10,58368
K 166,172,173
Mvaa 152,16,32
W 4
s_add_u32 s40, s26, s94
s_addc_u32 s41, s27, 0
Mvaa 156,24,32
s_and_b32 s41, s41, 0xffff
K 167,174,175
v_accvgpr_write_b32 a120, v164
Mvaa 156,28,48
s_mov_b32 s42, 5242880
Ta 8,11,49152
v_accvgpr_write_b32 a121, v165
Mvaa 124,28,44
s_mov_b32 s43, 0x20000
Ta 10,11,50176
buffer_load_dwordx4 v[188:191], v27, s[8:11], s28 offen offset:0 sc1
Mvav 124,24,208
s_sub_u32 s42, s42, s94
v_accvgpr_write_b32 a122, v166
v_accvgpr_write_b32 a123, v167
Mvav 92,24,236
s_mov_b64 s[44:45], s[8:9]
Ta 12,11,57344
Ta 14,11,58368
Mvaa 92,28,40
s_mov_b32 s47, 0x20000
v_accvgpr_write_b32 a124, v160
v_accvgpr_write_b32 a125, v161
Mvaa 60,28,36
s_and_b32 s48, s14, 1
v_accvgpr_write_b32 a126, v162
v_accvgpr_write_b32 a127, v163
Mvav 60,24,228
s_waitcnt vmcnt(14) lgkmcnt(4)
Ta 16,12,49152
Ta 18,12,50176
Mvaa 32,0,52
s_mul_i32 s48, s48, 5120
buffer_load_dwordx4 v[196:199], v27, s[8:11], s93 offen offset:0 sc1
v_bfe_u32 v28, v1, 4, 2
Mvav 32,4,184
s_lshr_b32 s51, s14, 1
Ta 20,12,57344
v_lshlrev_b32 v28, 3, v28
Mvaa 64,4,128
s_lshl_b32 s80, s56, 9
s_add_u32 s80, s80, 4096
Ta 22,12,58368
Mvaa 64,0,56
s_and_b32 s94, s14, 1
Ta 24,13,49152
Ta 26,13,50176
Mvaa 96,0,60
s_add_u32 s28, s28, 512
s_cmp_eq_u32 s28, s4
s_cselect_b32 s93, 10240, 0
s_sub_u32 s28, s28, s93
Mvaa 96,4,132
s_add_u32 s93, s28, 20480
s_lshl_b32 s95, s57, 1
buffer_load_dwordx2 v[28:29], v28, s[8:11], s80 offen sc1
Mvaa 128,4,136
s_add_u32 s94, s94, s95
Ta 28,13,57344
Ta 30,13,58368
Mvav 128,0,216
W 8
s_lshl_b32 s54, s94, 3
s_add_u32 s54, s54, s59
Mvav 132,8,216
s_lshl_b32 s54, s54, 6
Ta 0,14,49152
Ta 2,14,50176
Mvaa 132,12,136
s_xor_b32 s55, s54, 15872
Mvaa 100,12,132
buffer_load_dwordx4 v[204:207], v26, s[8:11], s28 offen offset:0 sc1
Ta 4,14,57344
Mvaa 100,8,60
s_add_u32 s54, s54, 20480
Ta 6,14,58368
Mvaa 68,8,56
s_and_b32 s81, s14, 1
Mvaa 68,12,128
s_add_u32 s55, s55, 20480
Mvav 36,12,184
Mvaa 36,8,52
W 8
s_and_b32 s95, s14, 2
buffer_load_dwordx4 v[212:215], v26, s[8:11], s93 offen offset:0 sc1
Mvaa 40,16,52
s_mul_i32 s91, s81, 17164288
Ta 8,15,49152
Ta 10,15,50176
Mvav 40,20,184
s_lshl_b32 s94, s94, 2
s_mul_i32 s82, s56, 536576
Mvaa 72,20,128
s_add_u32 s94, s94, s95
Ta 12,15,57344
Ta 14,15,58368
Mvaa 72,16,56
Mvaa 104,16,60
s_lshl_b32 s95, s95, 4
s_add_u32 s91, s91, s82
buffer_load_dwordx4 v[220:223], v27, s[8:11], s28 offen offset:0 sc1
Mvaa 104,20,132
Mvaa 136,20,136
s_add_u32 s55, s55, s95
s_add_u32 s91, s91, 4194304
Mvav 136,16,216
W 8
s_lshl_b32 s49, s94, 17
Ta 16,24,49152
Mvav 140,24,216
Ta 18,24,50176
Mvaa 140,28,136
s_lshl_b32 s95, s59, 14
s_lshr_b32 s82, s14, 1
buffer_load_dwordx4 v[176:179], v27, s[8:11], s93 offen offset:0 sc1
Mvaa 108,28,132
Ta 20,24,57344
Ta 22,24,58368
Mvaa 108,24,60
s_add_u32 s49, s49, s95
s_lshl_b32 s83, s82, 14
Mvaa 76,24,56
s_xor_b32 s50, s49, 16252928
Mvaa 76,28,128
Mvav 44,28,184
s_add_u32 s91, s91, s83
s_add_u32 s49, s49, 39845888
Mvaa 44,24,52
W 8
Ta 24,25,49152
Ta 26,25,50176
Mvaa 48,0,52
s_add_u32 s50, s50, 39845888
s_lshl_b32 s81, s81, 14
Mvav 48,4,184
Mvaa 80,4,128
s_lshr_b32 s94, s15, 3
Ta 28,25,57344
Ta 30,25,58368
Mvaa 80,0,56
s_lshl_b32 s82, s82, 16
s_lshl_b32 s94, s94, 3
Mvaa 112,0,60
Mvaa 112,4,132
s_add_u32 s94, s94, s14
s_or_b32 s92, s81, s82
Mvaa 144,4,136
Mvav 144,0,216
W 8
s_lshl_b32 s94, s94, 4
Ta 0,2,32768
Mvav 148,8,216
s_xor_b32 s92, s92, 0x10000
s_add_u32 s94, s94, s59
Ta 2,2,33792
Mvaa 148,12,136
Mvaa 116,12,132
s_mul_i32 s52, s94, 5120
Ta 4,2,40960
Ta 6,2,41984
Mvaa 116,8,60
Mvaa 84,8,56
s_lshl_b32 s95, s59, 12
Mvaa 84,12,128
Mvav 52,12,184
s_sub_u32 s52, s52, s95
Mvaa 52,8,52
W 8
s_add_u32 s52, s52, 56623104
Ta 8,3,32768
Mvaa 56,16,52
Ta 10,3,33792
Mvav 56,20,184
s_lshl_b32 s53, s94, 3
Mvaa 88,20,128
Ta 12,3,40960
Ta 14,3,41984
Mvaa 88,16,56
s_add_u32 s53, s53, 65536
Mvaa 120,16,60
s_waitcnt vmcnt(4)
v_cmp_eq_u64 vcc, s[12:13], v[28:29]
s_cmp_eq_u64 vcc, -1
s_cbranch_scc0 .Lspin_61%=
.Lok_62%=:
s_sub_u32 s82, s91, 16384
Mvaa 120,20,132
s_xor_b32 s83, s92, 0x10000
s_add_u32 m0, s83, 0
s_add_u32 s81, s82, 0
Mvaa 152,20,136
v_bfe_u32 v244, v1, 4, 1
v_mul_u32_u24 v244, 0xaaaa, v244
Mvav 152,16,216
W 8
Dc 1,8,81,0
v_add_u32 v244, 0x4444, v244
Mvav 156,24,216
Ta 16,4,32768
Ta 18,4,33792
Mvaa 156,28,136
v_lshl_or_b32 v240, v244, 16, v244
Mvaa 124,28,132
Ta 20,4,40960
v_mov_b32 v241, 0xff800000
Mvaa 124,24,60
Ta 22,4,41984
v_add_u32 v242, 0x10000, v1
Mvaa 92,24,56
Dc 1,8,81,1024
Mvaa 92,28,128
v_bfe_u32 v244, v1, 4, 4
Mvav 60,28,184
v_lshrrev_b32 v245, 8, v1
Mvaa 60,24,52
W 8
Ta 24,5,32768
v_lshlrev_b32 v245, 2, v245
Mvaa 32,0,140
Ta 26,5,33792
Mvav 32,4,224
Dc 1,8,81,2048
v_sub_u32 v244, v244, v245
Mvav 64,4,232
Ta 28,5,40960
v_cmp_le_i32_e64 s[72:73], 0, v244
Mvaa 64,0,144
Ta 30,5,41984
Mvaa 96,0,148
v_cmp_le_i32_e64 s[74:75], 1, v244
Mvaa 96,4,248
v_cmp_le_i32_e64 s[76:77], 2, v244
Mvaa 128,4,252
Dc 1,8,81,3072
v_cmp_le_i32_e64 s[78:79], 3, v244
Mvaa 128,0,152
W 8
s_add_u32 m0, s83, 4096
s_add_u32 s81, s82, 4096
Mvaa 132,8,152
Ta 0,6,32768
Ta 2,6,33792
Mvaa 132,12,252
v_add_u32 v243, 4096, v1
v_add_u32 v244, 8192, v1
Mvaa 100,12,248
Ta 4,6,40960
v_add_u32 v245, 12288, v1
Mvaa 100,8,148
Dc 1,8,81,0
Ta 6,6,41984
Mvaa 68,8,144
s_mov_b32 s18, 0x3f803f80
Mvav 68,12,232
s_mov_b32 s19, 0x3f003f00
Mvav 36,12,224
Mvaa 36,8,140
W 8
Ta 8,7,32768
v_accvgpr_write_b32 a176, s18
Mvaa 40,16,140
Dc 1,8,81,1024
Ta 10,7,33792
Mvav 40,20,224
v_accvgpr_write_b32 a180, s19
v_mov_b32 v252, v241
Mvav 72,20,232
Ta 12,7,40960
Ta 14,7,41984
Mvaa 72,16,144
v_accvgpr_write_b32 a177, s18
Mvaa 104,16,148
v_accvgpr_write_b32 a181, s19
Mvaa 104,20,248
Dc 1,8,81,2048
v_mov_b32 v253, v241
Mvaa 136,20,252
Mvaa 136,16,152
W 8
Ta 16,8,32768
v_accvgpr_write_b32 a178, s18
Mvaa 140,24,152
Ta 18,8,33792
v_accvgpr_write_b32 a182, s19
Mvaa 140,28,252
Mvaa 108,28,248
Dc 1,8,81,3072
v_mov_b32 v254, v241
Mvaa 108,24,148
s_add_u32 m0, s83, 8192
s_add_u32 s81, s82, 8192
Ta 20,8,40960
Mvaa 76,24,144
Ta 22,8,41984
v_accvgpr_write_b32 a179, s18
Mvav 76,28,232
v_accvgpr_write_b32 a183, s19
Mvav 44,28,224
v_mov_b32 v255, v241
Mvaa 44,24,140
W 8
Dc 1,8,81,0
v_accvgpr_write_b32 a160, 0
Mvaa 48,0,140
Ta 24,9,32768
Ta 26,9,33792
Mvav 48,4,224
v_accvgpr_write_b32 a161, 0
Mvav 80,4,232
Ta 28,9,40960
v_accvgpr_write_b32 a162, 0
Mvaa 80,0,144
Ta 30,9,41984
v_accvgpr_write_b32 a163, 0
Mvaa 112,0,148
Dc 1,8,81,1024
Mvaa 112,4,248
v_accvgpr_write_b32 a164, 0
Mvaa 144,4,252
v_accvgpr_write_b32 a165, 0
Mvaa 144,0,152
W 8
Ta 0,2,49152
)"
R"(v_accvgpr_write_b32 a166, 0
Mvaa 148,8,152
Ta 2,2,50176
Mvaa 148,12,252
Dc 1,8,81,2048
v_accvgpr_write_b32 a167, 0
Mvaa 116,12,248
Ta 4,2,57344
v_accvgpr_write_b32 a168, 0
Mvaa 116,8,148
Ta 6,2,58368
v_accvgpr_write_b32 a169, 0
Mvaa 84,8,144
Mvav 84,12,232
v_accvgpr_write_b32 a170, 0
Mvav 52,12,224
Dc 1,8,81,3072
v_accvgpr_write_b32 a171, 0
Mvaa 52,8,140
W 8
s_add_u32 m0, s83, 12288
s_add_u32 s81, s82, 12288
Mvaa 56,16,140
Ta 8,3,49152
Ta 10,3,50176
Mvav 56,20,224
v_accvgpr_write_b32 a172, 0
v_accvgpr_write_b32 a173, 0
Mvav 88,20,232
Ta 12,3,57344
v_accvgpr_write_b32 a174, 0
Mvaa 88,16,144
Dc 1,8,81,0
Ta 14,3,58368
Mvaa 120,16,148
v_accvgpr_write_b32 a175, 0
Mvaa 120,20,248
s_mov_b32 s90, 0x5d800000
Mvaa 152,20,252
s_mov_b64 s[88:89], 0
Mvaa 152,16,152
W 8
Ta 16,4,49152
Ta 18,4,50176
Mvaa 156,24,152
s_mov_b32 s64, 0
Dc 1,8,81,1024
Mvaa 156,28,252
s_and_b32 s18, s14, 1
Mvaa 124,28,248
s_lshl_b32 s62, s18, 14
Ta 20,4,57344
Ta 22,4,58368
Mvaa 124,24,148
Mvaa 92,24,144
s_lshl_b32 s20, s57, 15
Mvav 92,28,232
s_sub_u32 s20, 507904, s20
Dc 1,8,81,2048
Mvav 60,28,224
Mvaa 60,24,140
s_waitcnt vmcnt(15) lgkmcnt(8)
s_add_u32 s20, s20, s62
Ta 24,5,49152
Mvav 32,0,192
s_lshl_b32 s65, s57, 1
Ta 26,5,50176
Mvav 32,4,204
s_sub_u32 s65, s65, 1
Mvav 64,4,212
Dc 1,8,81,3072
Ta 28,5,57344
Mvav 64,0,200
s_max_i32 s65, s65, 0
Ta 30,5,58368
v_bfe_u32 v28, v1, 4, 4
Mvav 96,0,188
s_sub_u32 s66, s60, 1
v_lshlrev_b32 v28, 3, v28
Mvav 96,4,220
s_lshl_b32 s80, s56, 9
s_add_u32 s80, s80, 4096
s_cmp_lt_u32 s14, 2
s_cselect_b32 s20, s20, 0
s_cselect_b32 s66, s66, 15
s_cselect_b32 s21, 32768, 0
Mvav 128,4,176
Mvav 128,0,196
W 8
s_add_u32 s61, s91, s20
buffer_load_dwordx2 v[28:29], v28, s[8:11], s80 offen sc1
Mvav 132,8,196
s_add_u32 s62, s62, s21
Ta 0,6,49152
Ta 2,6,50176
Mvav 132,12,176
Mvav 100,12,220
s_sub_u32 s63, 16384, s21
Ta 4,6,57344
Ta 6,6,58368
Mvav 100,8,188
s_lshl_b32 s67, s60, 7
Mvav 68,8,200
s_add_u32 s67, s67, s56
Mvav 68,12,212
Mvav 36,12,204
s_lshl_b32 s67, s67, 12
Mvav 36,8,192
W 8
s_add_u32 s67, s67, 39845888
Ta 8,7,49152
Mvav 40,16,192
s_lshl_b32 s69, s60, 3
Ta 10,7,50176
Mvav 40,20,204
Mvav 72,20,212
s_add_u32 s69, s69, s59
Ta 12,7,57344
Ta 14,7,58368
Mvav 72,16,200
s_lshl_b32 s69, s69, 6
Mvav 104,16,188
Mvav 104,20,220
s_lshl_b32 s21, s58, 3
Mvav 136,20,176
s_add_u32 s69, s69, s21
Mvav 136,16,196
W 8
s_add_u32 s69, s69, 20480
Ta 16,8,49152
Mvav 140,24,196
Ta 18,8,50176
Mvav 140,28,176
s_mov_b32 s70, 0
Mvav 108,28,220
s_mov_b32 s37, 0
Ta 20,8,57344
Ta 22,8,58368
Mvav 108,24,188
s_sub_u32 s18, s57, 4
Mvav 76,24,200
Mvav 76,28,212
s_lshl_b32 s18, s18, 2
Mvav 44,28,204
s_add_u32 s18, s18, s58
Mvav 44,24,192
W 8
Ta 24,9,49152
Ta 26,9,50176
Mvav 48,0,192
s_mul_i32 s85, s59, 7
Mvav 48,4,204
s_add_u32 s85, s85, s18
Mvav 80,4,212
s_cmp_lt_u32 s18, 7
s_cselect_b32 s85, s85, 99
Ta 28,9,57344
Ta 30,9,58368
Mvav 80,0,200
Mvav 112,0,188
s_cmp_lt_u32 s85, 54
s_cselect_b32 s65, 2, s65
Mvav 112,4,220
s_mov_b32 s68, 0
Mvav 144,4,176
s_mov_b64 s[24:25], -1
Mvav 144,0,196
W 8
Mvav 148,8,196
s_mov_b32 s22, 0x3e0293ee
Mvav 148,12,176
s_mov_b32 s23, 0x3e0293ee
Mvav 116,12,220
Mvav 116,8,188
Mvav 84,8,200
Mvav 84,12,212
Mvav 52,12,204
Mvav 52,8,192
W 4
Mvav 56,16,192
Mvav 56,20,204
s_waitcnt vmcnt(0)
v_cmp_eq_u64 vcc, s[12:13], v[28:29]
s_cmp_eq_u64 vcc, -1
s_cbranch_scc0 .Lspin_64%=
.Lok_65%=:
Mvav 88,20,212
Mvav 88,16,200
Mvav 120,16,188
W 0
s_barrier
Ra 128,242,0
Mvav 120,20,220
Mvav 152,20,176
Ra 132,242,1024
Mvav 152,16,196
Mvav 156,24,196
Ra 136,242,2048
Mvav 156,28,176
Mvav 124,28,220
Ra 140,242,3072
Mvav 124,24,188
Mvav 92,24,200
Mvav 92,28,212
Mvav 60,28,204
Mvav 60,24,192
.Lbgo%=:
v_pk_mul_f32 v[32:33], v[32:33], s[22:23]
v_pk_mul_f32 v[34:35], v[34:35], s[22:23]
v_pk_mul_f32 v[36:37], v[36:37], s[22:23]
v_pk_mul_f32 v[38:39], v[38:39], s[22:23]
v_pk_mul_f32 v[64:65], v[64:65], s[22:23]
v_pk_mul_f32 v[66:67], v[66:67], s[22:23]
v_pk_mul_f32 v[68:69], v[68:69], s[22:23]
v_pk_mul_f32 v[70:71], v[70:71], s[22:23]
v_pk_mul_f32 v[96:97], v[96:97], s[22:23]
v_pk_mul_f32 v[98:99], v[98:99], s[22:23]
v_pk_mul_f32 v[100:101], v[100:101], s[22:23]
v_pk_mul_f32 v[102:103], v[102:103], s[22:23]
v_pk_mul_f32 v[128:129], v[128:129], s[22:23]
v_pk_mul_f32 v[130:131], v[130:131], s[22:23]
v_pk_mul_f32 v[132:133], v[132:133], s[22:23]
v_pk_mul_f32 v[134:135], v[134:135], s[22:23]
v_pk_mul_f32 v[40:41], v[40:41], s[22:23]
v_pk_mul_f32 v[42:43], v[42:43], s[22:23]
v_pk_mul_f32 v[44:45], v[44:45], s[22:23]
v_pk_mul_f32 v[46:47], v[46:47], s[22:23]
v_pk_mul_f32 v[72:73], v[72:73], s[22:23]
v_pk_mul_f32 v[74:75], v[74:75], s[22:23]
v_pk_mul_f32 v[76:77], v[76:77], s[22:23]
v_pk_mul_f32 v[78:79], v[78:79], s[22:23]
v_pk_mul_f32 v[104:105], v[104:105], s[22:23]
v_pk_mul_f32 v[106:107], v[106:107], s[22:23]
v_pk_mul_f32 v[108:109], v[108:109], s[22:23]
v_pk_mul_f32 v[110:111], v[110:111], s[22:23]
v_pk_mul_f32 v[136:137], v[136:137], s[22:23]
v_pk_mul_f32 v[138:139], v[138:139], s[22:23]
v_pk_mul_f32 v[140:141], v[140:141], s[22:23]
v_pk_mul_f32 v[142:143], v[142:143], s[22:23]
v_pk_mul_f32 v[48:49], v[48:49], s[22:23]
v_pk_mul_f32 v[50:51], v[50:51], s[22:23]
v_pk_mul_f32 v[52:53], v[52:53], s[22:23]
v_pk_mul_f32 v[54:55], v[54:55], s[22:23]
v_pk_mul_f32 v[80:81], v[80:81], s[22:23]
v_pk_mul_f32 v[82:83], v[82:83], s[22:23]
v_pk_mul_f32 v[84:85], v[84:85], s[22:23]
v_pk_mul_f32 v[86:87], v[86:87], s[22:23]
v_pk_mul_f32 v[112:113], v[112:113], s[22:23]
v_pk_mul_f32 v[114:115], v[114:115], s[22:23]
v_pk_mul_f32 v[116:117], v[116:117], s[22:23]
v_pk_mul_f32 v[118:119], v[118:119], s[22:23]
v_pk_mul_f32 v[144:145], v[144:145], s[22:23]
v_pk_mul_f32 v[146:147], v[146:147], s[22:23]
v_pk_mul_f32 v[148:149], v[148:149], s[22:23]
v_pk_mul_f32 v[150:151], v[150:151], s[22:23]
v_pk_mul_f32 v[56:57], v[56:57], s[22:23]
v_pk_mul_f32 v[58:59], v[58:59], s[22:23]
v_pk_mul_f32 v[60:61], v[60:61], s[22:23]
v_pk_mul_f32 v[62:63], v[62:63], s[22:23]
v_pk_mul_f32 v[88:89], v[88:89], s[22:23]
v_pk_mul_f32 v[90:91], v[90:91], s[22:23]
v_pk_mul_f32 v[92:93], v[92:93], s[22:23]
v_pk_mul_f32 v[94:95], v[94:95], s[22:23]
v_pk_mul_f32 v[120:121], v[120:121], s[22:23]
v_pk_mul_f32 v[122:123], v[122:123], s[22:23]
v_pk_mul_f32 v[124:125], v[124:125], s[22:23]
v_pk_mul_f32 v[126:127], v[126:127], s[22:23]
v_pk_mul_f32 v[152:153], v[152:153], s[22:23]
v_pk_mul_f32 v[154:155], v[154:155], s[22:23]
v_pk_mul_f32 v[156:157], v[156:157], s[22:23]
v_pk_mul_f32 v[158:159], v[158:159], s[22:23]
K 32,32,33
K 33,34,35
K 34,36,37
K 35,38,39
K 40,40,41
K 41,42,43
K 42,44,45
K 43,46,47
W 0
Zvav 196,128,32
s_add_u32 m0, s62, 0
Ra 144,242,4096
Ra 148,242,5120
Ra 152,242,6144
Ra 156,242,7168
Mvav 196,132,40
K 64,64,65
K 65,66,67
K 66,68,69
K 67,70,71
K 72,72,73
K 73,74,75
K 74,76,77
K 75,78,79
s_nop 1
Zvav 204,132,72
Dc 1,8,61,0
K 96,96,97
K 97,98,99
K 98,100,101
Mvav 204,128,64
K 99,102,103
K 104,104,105
K 105,106,107
K 106,108,109
K 107,110,111
Zvav 212,128,96
Dc 1,8,61,1024
K 128,128,129
K 129,130,131
K 130,132,133
Mvav 212,132,104
K 131,134,135
K 136,136,137
K 137,138,139
K 138,140,141
K 139,142,143
s_nop 1
Zvav 220,132,136
Dc 1,8,61,2048
K 48,48,49
K 49,50,51
K 50,52,53
Mvav 220,128,128
K 51,54,55
K 56,56,57
K 57,58,59
K 58,60,61
K 59,62,63
Mvav 196,136,48
Ra 128,242,8192
Ra 132,242,9216
K 80,80,81
K 81,82,83
Mvav 196,140,56
K 82,84,85
K 83,86,87
K 88,88,89
K 89,90,91
K 90,92,93
K 91,94,95
s_nop 1
Mvav 204,140,88
Dc 1,8,61,3072
s_add_u32 m0, s62, 4096
K 112,112,113
K 113,114,115
K 114,116,117
Mvav 204,136,80
K 115,118,119
K 120,120,121
K 121,122,123
K 122,124,125
K 123,126,127
Mvav 212,136,112
Dc 243,8,61,0
K 144,144,145
K 145,146,147
K 146,148,149
Mvav 212,140,120
K 147,150,151
K 152,152,153
K 153,154,155
K 154,156,157
K 155,158,159
s_nop 1
Mvav 220,140,152
s_add_u32 s80, s62, 131072
Dc 243,8,61,1024
v_add_u32 v12, s80, v1
Mvav 220,136,144
W 2
ds_write_b128 v12, v[32:35] offset:0
Ra 136,242,10240
Zvav 36,144,32
Ra 0,12,0
Ra 140,242,11264
Dc 243,8,61,2048
Mvav 36,148,40
v_max3_f32 v4, v196, v197, v198
v_max3_f32 v4, v4, v199, v252
v_max3_f32 v5, v204, v205, v206
v_max3_f32 v5, v5, v207, v253
Zvav 68,148,72
v_max3_f32 v6, v212, v213, v214
v_max3_f32 v6, v6, v215, v254
v_max3_f32 v7, v220, v221, v222
v_max3_f32 v7, v7, v223, v255
Mvav 68,144,64
v_mov_b32 v8, v4
v_mov_b32 v9, v5
v_mov_b32 v10, v6
v_mov_b32 v11, v7
Zvav 100,144,96
ds_write_b128 v12, v[40:43] offset:1024
v_permlane32_swap_b32 v4, v8
v_permlane32_swap_b32 v5, v9
v_permlane32_swap_b32 v6, v10
Mvav 100,148,104
)"
R"(v_permlane32_swap_b32 v7, v11
v_max_f32 v4, v4, v8
v_max_f32 v5, v5, v9
v_max_f32 v6, v6, v10
Zvav 132,148,136
v_max_f32 v7, v7, v11
v_mov_b32 v8, v4
v_mov_b32 v9, v5
v_mov_b32 v10, v6
Mvav 132,144,128
Ra 4,12,1024
v_mov_b32 v11, v7
v_permlane16_swap_b32 v4, v8
v_permlane16_swap_b32 v5, v9
Mvav 36,152,48
v_permlane16_swap_b32 v6, v10
v_permlane16_swap_b32 v7, v11
v_max_f32 v4, v4, v8
v_max_f32 v5, v5, v9
Mvav 36,156,56
v_max_f32 v6, v6, v10
v_max_f32 v7, v7, v11
v_sub_f32 v4, 0, v4
v_sub_f32 v5, 0, v5
Mvav 68,156,88
v_sub_f32 v6, 0, v6
v_sub_f32 v7, 0, v7
v_mov_b32 v224, v4
v_mov_b32 v225, v4
Mvav 68,152,80
v_mov_b32 v226, v4
v_mov_b32 v227, v4
v_mov_b32 v228, v5
v_mov_b32 v229, v5
Mvav 100,152,112
v_mov_b32 v230, v5
v_mov_b32 v231, v5
v_mov_b32 v232, v6
v_mov_b32 v233, v6
Mvav 100,156,120
v_mov_b32 v234, v6
v_mov_b32 v235, v6
v_mov_b32 v236, v7
v_mov_b32 v237, v7
Mvav 132,156,152
Dc 243,8,61,3072
s_add_u32 m0, s62, 8192
ds_write_b128 v12, v[48:51] offset:2048
v_mov_b32 v238, v7
v_mov_b32 v239, v7
Mvav 132,152,144
W 3
Ra 8,12,2048
ds_write_b128 v12, v[56:59] offset:3072
Ra 144,242,12288
Ra 12,12,3072
Cvav 160,128,32,224
s_and_b32 s80, s14, 1
Dc 244,8,61,0
Ra 148,242,13312
ds_write_b128 v12, v[64:67] offset:4096
Ra 16,12,4096
Mvav 160,132,40
s_lshl_b32 s80, s80, 14
ds_write_b128 v12, v[72:75] offset:5120
Ra 20,12,5120
ds_write_b128 v12, v[80:83] offset:6144
A 196,196,224
Cvav 164,132,72,228
s_add_u32 s80, s80, 98304
Dc 244,8,61,1024
Ra 24,12,6144
A 197,197,224
A 198,198,224
Mvav 164,128,64
Ra 152,242,14336
A 199,199,224
X 196
Cvav 168,128,96,232
Dc 244,8,61,2048
Ra 156,242,15360
X 197
Mvav 168,132,104
ds_write_b128 v12, v[88:91] offset:7168
Ra 28,12,7168
X 198
Cvav 172,132,136,236
Dc 244,8,61,3072
s_add_u32 m0, s62, 12288
K 192,196,197
X 199
Mvav 172,128,128
ds_write_b128 v12, v[96:99] offset:8192
A 204,204,228
v_add_u32 v8, s80, v1
K 193,198,199
Mvav 160,136,48
Dc 245,8,61,0
Ra 32,12,8192
A 205,205,228
A 206,206,228
Mvav 160,140,56
ds_write_b128 v8, v[4:7]
A 207,207,228
X 204
Mvav 164,140,88
Dc 245,8,61,1024
ds_write_b128 v12, v[104:107] offset:9216
X 205
Mvav 164,136,80
Ra 36,12,9216
X 206
K 200,204,205
Mvav 168,136,112
Dc 245,8,61,2048
Ra 128,242,24576
X 207
Mvav 168,140,120
ds_write_b128 v12, v[112:115] offset:10240
Ra 132,242,25600
A 212,212,232
K 201,206,207
Mvav 172,140,152
Dc 245,8,61,3072
s_add_u32 s61, s61, s63
Ra 40,12,10240
A 213,213,232
A 214,214,232
Mvav 172,136,144
W 11
ds_write_b128 v12, v[120:123] offset:11264
A 215,215,232
X 212
Cvav 176,144,32,224
X 213
X 214
Mvav 176,148,40
Ra 44,12,11264
K 208,212,213
X 215
Cvav 180,148,72,228
ds_write_b128 v12, v[128:131] offset:12288
A 220,220,236
K 209,214,215
A 221,221,236
Mvav 180,144,64
Ra 48,12,12288
Ra 136,242,26624
A 222,222,236
A 223,223,236
Cvav 184,144,96,232
ds_write_b128 v12, v[136:139] offset:13312
Ra 140,242,27648
X 220
Mvav 184,148,104
X 221
X 222
Cvav 188,148,136,236
Ra 52,12,13312
K 216,220,221
X 223
Mvav 188,144,128
ds_write_b128 v12, v[144:147] offset:14336
Ra 56,12,14336
A 36,36,224
K 217,222,223
Mvav 176,152,48
ds_write_b128 v12, v[152:155] offset:15360
A 37,37,224
A 38,38,224
A 39,39,224
Mvav 176,156,56
X 36
X 37
Mvav 180,156,88
Ra 60,12,15360
X 38
K 194,36,37
Mvav 180,152,80
Ra 144,242,28672
X 39
A 68,68,228
Mvav 184,152,112
Ra 148,242,29696
K 195,38,39
A 69,69,228
A 70,70,228
Mvav 184,156,120
W 3
s_barrier
A 71,71,228
X 68
Mvav 188,156,152
X 69
X 70
K 202,68,69
X 71
A 100,100,232
K 203,70,71
A 101,101,232
A 102,102,232
A 103,103,232
X 100
X 101
X 102
K 210,100,101
X 103
A 132,132,236
K 211,102,103
A 133,133,236
A 134,134,236
A 135,135,236
X 132
X 133
X 134
K 218,132,133
X 135
s_nop 0
K 219,134,135
Mvav 188,152,144
Ra 152,242,30720
Zvav 32,128,192
Ra 156,242,31744
Zvav 64,128,200
Zvav 96,128,208
s_xor_b32 s62, s62, 0x10000
Zvav 128,128,216
v_add_u32 v23, 61440, v21
Zvav 132,132,216
v_add_u32 v24, 61440, v22
Zvav 100,132,208
v_add_u32 v25, 122880, v21
Zvav 68,132,200
v_add_u32 v26, 122880, v22
Zvav 36,132,192
Zvav 40,136,192
X 172
Zvav 72,136,200
X 173
Zvav 104,136,208
X 174
K 220,172,173
Zvav 136,136,216
X 175
Zvav 140,140,216
X 168
K 221,174,175
Zvav 108,140,208
X 169
X 170
Zvav 76,140,200
s_waitcnt vmcnt(0) lgkmcnt(0)
s_barrier
v_xor_b32 v242, 0x10000, v242
K 212,168,169
Zvav 44,140,192
X 171
X 164
Zvav 48,144,192
Ra 128,242,0
K 213,170,171
X 165
Zvav 80,144,200
X 166
Zvav 112,144,208
Ra 132,242,1024
K 204,164,165
X 167
Zvav 144,144,216
X 160
Zvav 148,148,216
K 205,166,167
X 161
Zvav 116,148,208
Ra 136,242,2048
X 162
Zvav 84,148,200
K 196,160,161
X 163
Zvav 52,148,192
X 176
K 197,162,163
Zvav 56,152,192
Ra 140,242,3072
X 177
Zvav 88,152,200
X 178
K 198,176,177
Zvav 120,152,208
X 179
X 180
Zvav 152,152,216
K 199,178,179
Zvav 156,156,216
X 181
X 182
Zvav 124,156,208
K 206,180,181
X 183
Zvav 92,156,200
K 207,182,183
Zvav 60,156,192
s_cmp_eq_u64 s[24:25], 0
s_cbranch_scc1 .Ll1_34%=
W 0
Cvaa 160,128,0,224
Ra 144,242,4096
Ra 148,242,5120
Mvaa 160,132,4
s_add_u32 m0, s62, 0
Ra 152,242,6144
Cvaa 164,132,20,228
X 184
Mvaa 164,128,16
X 185
Cvaa 168,128,32,232
X 186
Mvaa 168,132,36
Ra 156,242,7168
K 214,184,185
Cvaa 172,132,52,236
Dc 1,8,61,0
Mvaa 172,128,48
X 187
Mvaa 160,136,8
X 188
Mvaa 160,140,12
Ra 128,242,16384
K 215,186,187
Mvaa 164,140,28
Dc 1,8,61,1024
Mvaa 164,136,24
X 189
Mvaa 168,136,40
X 190
Mvaa 168,140,44
Ra 132,242,17408
K 222,188,189
Mvaa 172,140,60
Dc 1,8,61,2048
Mvaa 172,136,56
X 191
v_smfmac_f32_16x16x64_bf16 a[160:163], a[176:179], v[192:199], v240
Ra 136,242,18432
K 223,190,191
v_smfmac_f32_16x16x64_bf16 a[164:167], a[176:179], v[200:207], v240
X 160
v_smfmac_f32_16x16x64_bf16 a[168:171], a[176:179], v[208:215], v240
Dc 1,8,61,3072
v_smfmac_f32_16x16x64_bf16 a[172:175], a[176:179], v[216:223], v240
W 3
X 161
Cvaa 176,144,0,224
s_add_u32 m0, s62, 4096
X 162
Mvaa 176,148,4
Ra 140,242,19456
K 192,160,161
Cvaa 180,148,20,228
X 163
Mvaa 180,144,16
Dc 243,8,61,0
Cvaa 184,144,32,232
X 164
Mvaa 184,148,36
K 193,162,163
Cvaa 188,148,52,236
X 165
Mvaa 188,144,48
Dc 243,8,61,1024
Ra 144,242,20480
Mvaa 176,152,8
X 166
Mvaa 176,156,12
Ra 148,242,21504
K 200,164,165
Mvaa 180,156,28
X 167
Mvaa 180,152,24
Dc 243,8,61,2048
Mvaa 184,152,40
X 168
Mvaa 184,156,44
K 201,166,167
Mvaa 188,156,60
X 169
Mvaa 188,152,56
W 2
Dc 243,8,61,3072
Ra 152,242,22528
Mvav 32,128,196
s_add_u32 m0, s62, 8192
X 170
Mvav 64,128,204
Ra 156,242,23552
K 208,168,169
Mvav 96,128,212
X 171
Mvav 128,128,220
Dc 244,8,61,0
)"
R"(Mvav 132,132,220
X 172
Mvav 100,132,212
Ra 128,242,8192
K 209,170,171
Mvav 68,132,204
X 173
Mvav 36,132,196
Dc 244,8,61,1024
Mvav 40,136,196
X 174
Mvav 72,136,204
Ra 132,242,9216
K 216,172,173
Mvav 104,136,212
X 175
Mvav 136,136,220
Dc 244,8,61,2048
Mvav 140,140,220
X 176
Mvav 108,140,212
Ra 136,242,10240
K 217,174,175
Mvav 76,140,204
X 177
Mvav 44,140,196
W 3
Dc 244,8,61,3072
Mvav 48,144,196
X 178
Mvav 80,144,204
s_add_u32 m0, s62, 12288
Ra 140,242,11264
Mvav 112,144,212
K 194,176,177
Mvav 144,144,220
X 179
Mvav 148,148,220
X 180
Mvav 116,148,212
Dc 245,8,61,0
Ra 144,242,12288
Mvav 84,148,204
K 195,178,179
Mvav 52,148,196
X 181
Mvav 56,152,196
X 182
Mvav 88,152,204
Dc 245,8,61,1024
Ra 148,242,13312
Mvav 120,152,212
K 202,180,181
Mvav 152,152,220
X 183
Mvav 156,156,220
X 184
Mvav 124,156,212
Dc 245,8,61,2048
Ra 152,242,14336
Mvav 92,156,204
K 203,182,183
Mvav 60,156,196
W 3
X 185
Cvaa 160,128,0,224
X 186
Mvaa 160,132,4
Dc 245,8,61,3072
Ra 156,242,15360
Cvaa 164,132,20,228
s_add_u32 s61, s61, s63
K 210,184,185
Mvaa 164,128,16
X 187
Cvaa 168,128,32,232
X 188
Mvaa 168,132,36
Cvaa 172,132,52,236
s_xor_b32 s62, s62, 0x10000
K 211,186,187
Mvaa 172,128,48
X 189
Mvaa 160,136,8
Ra 128,242,24576
Ra 132,242,25600
Mvaa 160,140,12
s_add_u32 s64, s64, 1
X 190
Mvaa 164,140,28
K 218,188,189
Mvaa 164,136,24
X 191
Mvaa 168,136,40
Mvaa 168,140,44
X 160
Mvaa 172,140,60
s_add_u32 s36, s29, 131072
K 219,190,191
Mvaa 172,136,56
W 2
X 161
Cvaa 176,144,0,224
Ra 136,242,26624
Ra 140,242,27648
Mvaa 176,148,4
X 162
Cvaa 180,148,20,228
K 196,160,161
Mvaa 180,144,16
X 163
Cvaa 184,144,32,232
buffer_store_dwordx4 a[124:127], v1, s[8:11], s36 offen offset:0 sc1
Mvaa 184,148,36
X 164
Cvaa 188,148,52,236
K 197,162,163
Mvaa 188,144,48
X 165
Mvaa 176,152,8
Ra 144,242,28672
Ra 148,242,29696
Mvaa 176,156,12
X 166
Mvaa 180,156,28
buffer_store_dwordx4 a[116:119], v1, s[8:11], s36 offen offset:1024 sc1
K 204,164,165
Mvaa 180,152,24
X 167
Mvaa 184,152,40
Mvaa 184,156,44
X 168
Mvaa 188,156,60
Mvaa 188,152,56
W 2
Ra 152,242,30720
K 205,166,167
Mvav 32,128,192
X 169
Mvav 64,128,200
buffer_store_dwordx4 a[108:111], v1, s[8:11], s36 offen offset:2048 sc1
Ra 156,242,31744
Mvav 96,128,208
X 170
Mvav 128,128,216
K 212,168,169
Mvav 132,132,216
X 171
Mvav 100,132,208
X 172
Mvav 68,132,200
buffer_store_dwordx4 a[204:207], v1, s[8:11], s36 offen offset:3072 sc1
Mvav 36,132,192
K 213,170,171
Mvav 40,136,192
X 173
Mvav 72,136,200
X 174
Mvav 104,136,208
Mvav 136,136,216
s_add_u32 s36, s29, 135168
K 220,172,173
Mvav 140,140,216
X 175
Mvav 108,140,208
X 176
Mvav 76,140,200
s_waitcnt vmcnt(4) lgkmcnt(0)
s_barrier
v_xor_b32 v242, 0x10000, v242
Mvav 44,140,192
Ra 128,242,0
K 221,174,175
Mvav 48,144,192
X 177
Mvav 80,144,200
buffer_store_dwordx4 a[212:215], v1, s[8:11], s36 offen offset:0 sc1
Mvav 112,144,208
X 178
Mvav 144,144,216
Ra 132,242,1024
K 198,176,177
Mvav 148,148,216
X 179
Mvav 116,148,208
X 180
Mvav 84,148,200
Ra 136,242,2048
Mvav 52,148,192
buffer_store_dwordx4 a[220:223], v1, s[8:11], s36 offen offset:1024 sc1
K 199,178,179
Mvav 56,152,192
X 181
Mvav 88,152,200
Ra 140,242,3072
Mvav 120,152,208
X 182
Mvav 152,152,216
K 206,180,181
Mvav 156,156,216
X 183
Mvav 124,156,208
Mvav 92,156,200
K 207,182,183
Mvav 60,156,192
W 0
Cvaa 160,128,0,224
Ra 144,242,4096
Ra 148,242,5120
Mvaa 160,132,4
s_add_u32 m0, s62, 0
Ra 152,242,6144
Cvaa 164,132,20,228
X 184
Mvaa 164,128,16
X 185
Cvaa 168,128,32,232
X 186
Mvaa 168,132,36
Ra 156,242,7168
K 214,184,185
Cvaa 172,132,52,236
Dc 1,8,61,0
Mvaa 172,128,48
X 187
Mvaa 160,136,8
X 188
Mvaa 160,140,12
Ra 128,242,16384
K 215,186,187
Mvaa 164,140,28
Dc 1,8,61,1024
Mvaa 164,136,24
X 189
Mvaa 168,136,40
X 190
Mvaa 168,140,44
Ra 132,242,17408
K 222,188,189
Mvaa 172,140,60
Dc 1,8,61,2048
Mvaa 172,136,56
X 191
v_smfmac_f32_16x16x64_bf16 a[160:163], a[176:179], v[192:199], v240
Ra 136,242,18432
K 223,190,191
v_smfmac_f32_16x16x64_bf16 a[164:167], a[176:179], v[200:207], v240
X 160
v_smfmac_f32_16x16x64_bf16 a[168:171], a[176:179], v[208:215], v240
Dc 1,8,61,3072
v_smfmac_f32_16x16x64_bf16 a[172:175], a[176:179], v[216:223], v240
W 3
X 161
Cvaa 176,144,0,224
s_add_u32 m0, s62, 4096
X 162
Mvaa 176,148,4
Ra 140,242,19456
K 192,160,161
Cvaa 180,148,20,228
X 163
Mvaa 180,144,16
Dc 243,8,61,0
Cvaa 184,144,32,232
X 164
Mvaa 184,148,36
K 193,162,163
Cvaa 188,148,52,236
X 165
Mvaa 188,144,48
Dc 243,8,61,1024
Ra 144,242,20480
Mvaa 176,152,8
X 166
Mvaa 176,156,12
Ra 148,242,21504
K 200,164,165
Mvaa 180,156,28
X 167
Mvaa 180,152,24
Dc 243,8,61,2048
Mvaa 184,152,40
X 168
Mvaa 184,156,44
K 201,166,167
Mvaa 188,156,60
X 169
Mvaa 188,152,56
W 2
Dc 243,8,61,3072
Ra 152,242,22528
Mvav 32,128,196
s_add_u32 m0, s62, 8192
X 170
Mvav 64,128,204
Ra 156,242,23552
K 208,168,169
Mvav 96,128,212
X 171
Mvav 128,128,220
Dc 244,8,61,0
Mvav 132,132,220
X 172
Mvav 100,132,212
Ra 128,242,8192
K 209,170,171
Mvav 68,132,204
X 173
Mvav 36,132,196
Dc 244,8,61,1024
Mvav 40,136,196
X 174
Mvav 72,136,204
Ra 132,242,9216
K 216,172,173
Mvav 104,136,212
X 175
Mvav 136,136,220
Dc 244,8,61,2048
Mvav 140,140,220
X 176
Mvav 108,140,212
Ra 136,242,10240
K 217,174,175
Mvav 76,140,204
X 177
Mvav 44,140,196
W 3
Dc 244,8,61,3072
Mvav 48,144,196
X 178
Mvav 80,144,204
s_add_u32 m0, s62, 12288
Ra 140,242,11264
Mvav 112,144,212
K 194,176,177
Mvav 144,144,220
X 179
Mvav 148,148,220
X 180
Mvav 116,148,212
Dc 245,8,61,0
Ra 144,242,12288
Mvav 84,148,204
K 195,178,179
Mvav 52,148,196
X 181
Mvav 56,152,196
X 182
Mvav 88,152,204
Dc 245,8,61,1024
Ra 148,242,13312
Mvav 120,152,212
K 202,180,181
Mvav 152,152,220
X 183
Mvav 156,156,220
X 184
Mvav 124,156,212
Dc 245,8,61,2048
Ra 152,242,14336
Mvav 92,156,204
K 203,182,183
v_bfe_u32 v28, v1, 4, 4
Mvav 60,156,196
W 3
X 185
Cvaa 160,128,0,224
Ra 156,242,15360
v_lshlrev_b32 v28, 3, v28
Mvaa 160,132,4
s_lshl_b32 s80, s56, 9
s_add_u32 s80, s80, 4224
Dc 245,8,61,3072
Cvaa 164,132,20,228
s_add_u32 s61, s61, s63
X 186
Mvaa 164,128,16
K 210,184,185
Cvaa 168,128,32,232
X 187
Mvaa 168,132,36
buffer_load_dwordx2 v[28:29], v28, s[8:11], s80 offen sc1
Cvaa 172,132,52,236
s_xor_b32 s62, s62, 0x10000
X 188
Mvaa 172,128,48
Ra 128,242,24576
K 211,186,187
Mvaa 160,136,8
X 189
Mvaa 160,140,12
s_add_u32 s64, s64, 1
X 190
Mvaa 164,140,28
Ra 132,242,25600
K 218,188,189
Mvaa 164,136,24
X 191
Mvaa 168,136,40
)"
R"(Mvaa 168,140,44
X 160
Mvaa 172,140,60
buffer_store_dwordx4 a[228:231], v1, s[8:11], s36 offen offset:2048 sc1
K 219,190,191
Mvaa 172,136,56
W 2
X 161
Cvaa 176,144,0,224
Ra 136,242,26624
Ra 140,242,27648
Mvaa 176,148,4
X 162
Cvaa 180,148,20,228
K 196,160,161
Mvaa 180,144,16
X 163
Cvaa 184,144,32,232
Mvaa 184,148,36
X 164
Cvaa 188,148,52,236
buffer_store_dwordx4 a[76:79], v1, s[8:11], s36 offen offset:3072 sc1
K 197,162,163
Mvaa 188,144,48
X 165
Mvaa 176,152,8
Ra 144,242,28672
Ra 148,242,29696
Mvaa 176,156,12
X 166
Mvaa 180,156,28
K 204,164,165
Mvaa 180,152,24
X 167
Mvaa 184,152,40
s_add_u32 s36, s29, 327680
Mvaa 184,156,44
X 168
Mvaa 188,156,60
Mvaa 188,152,56
W 2
Ra 152,242,30720
K 205,166,167
Mvav 32,128,192
X 169
Mvav 64,128,200
Ra 156,242,31744
Mvav 96,128,208
X 170
Mvav 128,128,216
buffer_store_dwordx4 a[84:87], v1, s[8:11], s36 offen offset:0 sc1
K 212,168,169
Mvav 132,132,216
X 171
Mvav 100,132,208
X 172
Mvav 68,132,200
Mvav 36,132,192
K 213,170,171
Mvav 40,136,192
X 173
Mvav 72,136,200
buffer_store_dwordx4 a[92:95], v1, s[8:11], s36 offen offset:1024 sc1
Mvav 104,136,208
X 174
Mvav 136,136,216
s_waitcnt vmcnt(4)
v_cmp_eq_u64 vcc, s[12:13], v[28:29]
s_cmp_eq_u64 vcc, -1
s_cbranch_scc0 .Lspin_67%=
.Lok_68%=:
K 220,172,173
Mvav 140,140,216
X 175
Mvav 108,140,208
X 176
Mvav 76,140,200
W 0
s_barrier
v_xor_b32 v242, 0x10000, v242
Mvav 44,140,192
buffer_store_dwordx4 a[188:191], v1, s[8:11], s36 offen offset:2048 sc1
K 221,174,175
Mvav 48,144,192
Ra 128,242,0
Mvav 80,144,200
X 177
Mvav 112,144,208
X 178
Mvav 144,144,216
Ra 132,242,1024
K 198,176,177
Mvav 148,148,216
X 179
Mvav 116,148,208
X 180
Mvav 84,148,200
Ra 136,242,2048
buffer_store_dwordx4 a[196:199], v1, s[8:11], s36 offen offset:3072 sc1
Mvav 52,148,192
K 199,178,179
Mvav 56,152,192
X 181
Mvav 88,152,200
Ra 140,242,3072
Mvav 120,152,208
X 182
Mvav 152,152,216
K 206,180,181
Mvav 156,156,216
X 183
Mvav 124,156,208
Mvav 92,156,200
K 207,182,183
Mvav 60,156,192
W 0
Cvaa 160,128,0,224
Ra 144,242,4096
Ra 148,242,5120
Mvaa 160,132,4
s_add_u32 m0, s62, 0
Ra 152,242,6144
Cvaa 164,132,20,228
X 184
Mvaa 164,128,16
X 185
Cvaa 168,128,32,232
X 186
Mvaa 168,132,36
Ra 156,242,7168
K 214,184,185
Cvaa 172,132,52,236
Dc 1,8,61,0
Mvaa 172,128,48
X 187
Mvaa 160,136,8
X 188
Mvaa 160,140,12
Ra 128,242,16384
K 215,186,187
Mvaa 164,140,28
Dc 1,8,61,1024
Mvaa 164,136,24
X 189
Mvaa 168,136,40
X 190
Mvaa 168,140,44
Ra 132,242,17408
K 222,188,189
Mvaa 172,140,60
Dc 1,8,61,2048
Mvaa 172,136,56
X 191
v_smfmac_f32_16x16x64_bf16 a[160:163], a[176:179], v[192:199], v240
Ra 136,242,18432
K 223,190,191
v_smfmac_f32_16x16x64_bf16 a[164:167], a[176:179], v[200:207], v240
X 160
v_smfmac_f32_16x16x64_bf16 a[168:171], a[176:179], v[208:215], v240
Dc 1,8,61,3072
v_smfmac_f32_16x16x64_bf16 a[172:175], a[176:179], v[216:223], v240
W 3
X 161
Cvaa 176,144,0,224
s_add_u32 m0, s62, 4096
X 162
Mvaa 176,148,4
Ra 140,242,19456
K 192,160,161
Cvaa 180,148,20,228
X 163
Mvaa 180,144,16
Dc 243,8,61,0
Cvaa 184,144,32,232
X 164
Mvaa 184,148,36
K 193,162,163
Cvaa 188,148,52,236
X 165
Mvaa 188,144,48
Dc 243,8,61,1024
Ra 144,242,20480
Mvaa 176,152,8
X 166
Mvaa 176,156,12
Ra 148,242,21504
K 200,164,165
Mvaa 180,156,28
X 167
Mvaa 180,152,24
Dc 243,8,61,2048
Mvaa 184,152,40
X 168
Mvaa 184,156,44
K 201,166,167
Mvaa 188,156,60
X 169
Mvaa 188,152,56
W 2
Dc 243,8,61,3072
Ra 152,242,22528
Mvav 32,128,196
s_add_u32 m0, s62, 8192
X 170
Mvav 64,128,204
Ra 156,242,23552
K 208,168,169
Mvav 96,128,212
X 171
Mvav 128,128,220
Dc 244,8,61,0
Mvav 132,132,220
X 172
Mvav 100,132,212
Ra 128,242,8192
K 209,170,171
Mvav 68,132,204
X 173
Mvav 36,132,196
Dc 244,8,61,1024
Mvav 40,136,196
X 174
Mvav 72,136,204
Ra 132,242,9216
K 216,172,173
Mvav 104,136,212
X 175
Mvav 136,136,220
Dc 244,8,61,2048
Mvav 140,140,220
X 176
Mvav 108,140,212
Ra 136,242,10240
K 217,174,175
Mvav 76,140,204
X 177
Mvav 44,140,196
W 3
Dc 244,8,61,3072
Mvav 48,144,196
X 178
Mvav 80,144,204
s_add_u32 m0, s62, 12288
Ra 140,242,11264
Mvav 112,144,212
K 194,176,177
Mvav 144,144,220
X 179
Mvav 148,148,220
X 180
Mvav 116,148,212
Dc 245,8,61,0
Ra 144,242,12288
Mvav 84,148,204
K 195,178,179
Mvav 52,148,196
X 181
Mvav 56,152,196
X 182
Mvav 88,152,204
Dc 245,8,61,1024
Ra 148,242,13312
Mvav 120,152,212
K 202,180,181
Mvav 152,152,220
X 183
Mvav 156,156,220
X 184
Mvav 124,156,212
Dc 245,8,61,2048
Ra 152,242,14336
Mvav 92,156,204
K 203,182,183
Mvav 60,156,196
W 3
X 185
Cvaa 160,128,0,224
X 186
Mvaa 160,132,4
Dc 245,8,61,3072
Ra 156,242,15360
Cvaa 164,132,20,228
s_add_u32 s61, s61, s63
K 210,184,185
Mvaa 164,128,16
X 187
Cvaa 168,128,32,232
X 188
Mvaa 168,132,36
Cvaa 172,132,52,236
s_xor_b32 s62, s62, 0x10000
K 211,186,187
Mvaa 172,128,48
X 189
Mvaa 160,136,8
Ra 128,242,24576
Ra 132,242,25600
Mvaa 160,140,12
s_add_u32 s64, s64, 1
X 190
Mvaa 164,140,28
K 218,188,189
Mvaa 164,136,24
X 191
Mvaa 168,136,40
Mvaa 168,140,44
X 160
Mvaa 172,140,60
s_add_u32 s36, s29, 331776
K 219,190,191
Mvaa 172,136,56
W 2
X 161
Cvaa 176,144,0,224
Ra 136,242,26624
Ra 140,242,27648
Mvaa 176,148,4
X 162
Cvaa 180,148,20,228
K 196,160,161
Mvaa 180,144,16
X 163
Cvaa 184,144,32,232
buffer_store_dwordx4 a[236:239], v1, s[8:11], s36 offen offset:0 sc1
Mvaa 184,148,36
X 164
Cvaa 188,148,52,236
K 197,162,163
Mvaa 188,144,48
X 165
Mvaa 176,152,8
Ra 144,242,28672
Ra 148,242,29696
Mvaa 176,156,12
X 166
Mvaa 180,156,28
buffer_store_dwordx4 a[244:247], v1, s[8:11], s36 offen offset:1024 sc1
K 204,164,165
Mvaa 180,152,24
X 167
Mvaa 184,152,40
Mvaa 184,156,44
X 168
Mvaa 188,156,60
Mvaa 188,152,56
W 2
Ra 152,242,30720
K 205,166,167
Mvav 32,128,192
X 169
Mvav 64,128,200
buffer_store_dwordx4 a[68:71], v1, s[8:11], s36 offen offset:2048 sc1
Ra 156,242,31744
Mvav 96,128,208
X 170
Mvav 128,128,216
K 212,168,169
Mvav 132,132,216
X 171
Mvav 100,132,208
X 172
Mvav 68,132,200
buffer_store_dwordx4 a[184:187], v1, s[8:11], s36 offen offset:3072 sc1
Mvav 36,132,192
K 213,170,171
Mvav 40,136,192
X 173
Mvav 72,136,200
X 174
Mvav 104,136,208
Mvav 136,136,216
s_add_u32 s36, s29, 196608
K 220,172,173
Mvav 140,140,216
X 175
Mvav 108,140,208
X 176
Mvav 76,140,200
s_waitcnt vmcnt(4) lgkmcnt(0)
s_barrier
v_xor_b32 v242, 0x10000, v242
Mvav 44,140,192
Ra 128,242,0
K 221,174,175
Mvav 48,144,192
X 177
Mvav 80,144,200
buffer_store_dwordx4 a[192:195], v1, s[8:11], s36 offen offset:0 sc1
Mvav 112,144,208
X 178
Mvav 144,144,216
Ra 132,242,1024
K 198,176,177
Mvav 148,148,216
X 179
Mvav 116,148,208
X 180
Mvav 84,148,200
Ra 136,242,2048
Mvav 52,148,192
)"
R"(buffer_store_dwordx4 a[200:203], v1, s[8:11], s36 offen offset:1024 sc1
K 199,178,179
Mvav 56,152,192
X 181
Mvav 88,152,200
Ra 140,242,3072
Mvav 120,152,208
X 182
Mvav 152,152,216
K 206,180,181
Mvav 156,156,216
X 183
Mvav 124,156,208
Mvav 92,156,200
K 207,182,183
Mvav 60,156,192
W 0
Cvaa 160,128,0,224
Ra 144,242,4096
Ra 148,242,5120
Mvaa 160,132,4
s_add_u32 m0, s62, 0
Ra 152,242,6144
Cvaa 164,132,20,228
X 184
Mvaa 164,128,16
X 185
Cvaa 168,128,32,232
X 186
Mvaa 168,132,36
Ra 156,242,7168
K 214,184,185
Cvaa 172,132,52,236
Dc 1,8,61,0
Mvaa 172,128,48
X 187
Mvaa 160,136,8
X 188
Mvaa 160,140,12
Ra 128,242,16384
K 215,186,187
Mvaa 164,140,28
Dc 1,8,61,1024
Mvaa 164,136,24
X 189
Mvaa 168,136,40
X 190
Mvaa 168,140,44
Ra 132,242,17408
K 222,188,189
Mvaa 172,140,60
Dc 1,8,61,2048
Mvaa 172,136,56
X 191
v_smfmac_f32_16x16x64_bf16 a[160:163], a[176:179], v[192:199], v240
Ra 136,242,18432
K 223,190,191
v_smfmac_f32_16x16x64_bf16 a[164:167], a[176:179], v[200:207], v240
X 160
v_smfmac_f32_16x16x64_bf16 a[168:171], a[176:179], v[208:215], v240
Dc 1,8,61,3072
v_smfmac_f32_16x16x64_bf16 a[172:175], a[176:179], v[216:223], v240
W 3
X 161
Cvaa 176,144,0,224
s_add_u32 m0, s62, 4096
X 162
Mvaa 176,148,4
Ra 140,242,19456
K 192,160,161
Cvaa 180,148,20,228
X 163
Mvaa 180,144,16
Dc 243,8,61,0
Cvaa 184,144,32,232
X 164
Mvaa 184,148,36
K 193,162,163
Cvaa 188,148,52,236
X 165
Mvaa 188,144,48
Dc 243,8,61,1024
Ra 144,242,20480
Mvaa 176,152,8
X 166
Mvaa 176,156,12
Ra 148,242,21504
K 200,164,165
Mvaa 180,156,28
X 167
Mvaa 180,152,24
Dc 243,8,61,2048
Mvaa 184,152,40
X 168
Mvaa 184,156,44
K 201,166,167
Mvaa 188,156,60
X 169
Mvaa 188,152,56
W 2
Dc 243,8,61,3072
Ra 152,242,22528
Mvav 32,128,196
s_add_u32 m0, s62, 8192
X 170
Mvav 64,128,204
Ra 156,242,23552
K 208,168,169
Mvav 96,128,212
X 171
Mvav 128,128,220
Dc 244,8,61,0
Mvav 132,132,220
X 172
Mvav 100,132,212
Ra 128,242,8192
K 209,170,171
Mvav 68,132,204
X 173
Mvav 36,132,196
Dc 244,8,61,1024
Mvav 40,136,196
X 174
Mvav 72,136,204
Ra 132,242,9216
K 216,172,173
Mvav 104,136,212
X 175
Mvav 136,136,220
Dc 244,8,61,2048
Mvav 140,140,220
X 176
Mvav 108,140,212
Ra 136,242,10240
K 217,174,175
Mvav 76,140,204
X 177
Mvav 44,140,196
W 3
Dc 244,8,61,3072
Mvav 48,144,196
X 178
Mvav 80,144,204
s_add_u32 m0, s62, 12288
Ra 140,242,11264
Mvav 112,144,212
K 194,176,177
Mvav 144,144,220
X 179
Mvav 148,148,220
X 180
Mvav 116,148,212
Dc 245,8,61,0
Ra 144,242,12288
Mvav 84,148,204
K 195,178,179
Mvav 52,148,196
X 181
Mvav 56,152,196
X 182
Mvav 88,152,204
Dc 245,8,61,1024
Ra 148,242,13312
Mvav 120,152,212
K 202,180,181
Mvav 152,152,220
X 183
Mvav 156,156,220
s_waitcnt vmcnt(16)
s_add_u32 s80, s71, 256
v_mov_b64 v[28:29], s[12:13]
s_mov_b64 exec, 1
buffer_store_dwordx2 v[28:29], off, s[8:11], s80 sc1
s_mov_b64 exec, -1
X 184
Mvav 124,156,212
Ra 152,242,14336
Mvav 92,156,204
K 203,182,183
Mvav 60,156,196
W 3
X 185
Cvaa 160,128,0,224
Dc 245,8,61,2048
Ra 156,242,15360
Mvaa 160,132,4
X 186
Cvaa 164,132,20,228
K 210,184,185
Mvaa 164,128,16
X 187
Cvaa 168,128,32,232
Dc 245,8,61,3072
Mvaa 168,132,36
s_add_u32 s61, s61, s63
X 188
Cvaa 172,132,52,236
s_xor_b32 s62, s62, 0x10000
K 211,186,187
Mvaa 172,128,48
X 189
Mvaa 160,136,8
Ra 128,242,24576
Ra 132,242,25600
Mvaa 160,140,12
s_add_u32 s64, s64, 1
X 190
Mvaa 164,140,28
K 218,188,189
Mvaa 164,136,24
X 191
Mvaa 168,136,40
Mvaa 168,140,44
X 160
Mvaa 172,140,60
buffer_store_dwordx4 a[208:211], v1, s[8:11], s36 offen offset:2048 sc1
K 219,190,191
Mvaa 172,136,56
W 2
X 161
Cvaa 176,144,0,224
Ra 136,242,26624
Ra 140,242,27648
Mvaa 176,148,4
X 162
Cvaa 180,148,20,228
K 196,160,161
Mvaa 180,144,16
X 163
Cvaa 184,144,32,232
Mvaa 184,148,36
X 164
Cvaa 188,148,52,236
buffer_store_dwordx4 a[216:219], v1, s[8:11], s36 offen offset:3072 sc1
K 197,162,163
Mvaa 188,144,48
X 165
Mvaa 176,152,8
Ra 144,242,28672
Ra 148,242,29696
Mvaa 176,156,12
X 166
Mvaa 180,156,28
K 204,164,165
Mvaa 180,152,24
X 167
Mvaa 184,152,40
s_add_u32 s36, s29, 200704
Mvaa 184,156,44
X 168
Mvaa 188,156,60
Mvaa 188,152,56
W 2
Ra 152,242,30720
K 205,166,167
Mvav 32,128,192
X 169
Mvav 64,128,200
Ra 156,242,31744
Mvav 96,128,208
X 170
Mvav 128,128,216
buffer_store_dwordx4 a[224:227], v1, s[8:11], s36 offen offset:0 sc1
K 212,168,169
Mvav 132,132,216
X 171
Mvav 100,132,208
X 172
Mvav 68,132,200
Mvav 36,132,192
K 213,170,171
Mvav 40,136,192
X 173
Mvav 72,136,200
buffer_store_dwordx4 a[232:235], v1, s[8:11], s36 offen offset:1024 sc1
Mvav 104,136,208
X 174
Mvav 136,136,216
K 220,172,173
Mvav 140,140,216
X 175
Mvav 108,140,208
X 176
Mvav 76,140,200
s_waitcnt vmcnt(4) lgkmcnt(0)
s_barrier
v_xor_b32 v242, 0x10000, v242
Mvav 44,140,192
buffer_store_dwordx4 a[240:243], v1, s[8:11], s36 offen offset:2048 sc1
K 221,174,175
Mvav 48,144,192
Ra 128,242,0
Mvav 80,144,200
X 177
Mvav 112,144,208
X 178
Mvav 144,144,216
Ra 132,242,1024
K 198,176,177
Mvav 148,148,216
X 179
Mvav 116,148,208
X 180
Mvav 84,148,200
Ra 136,242,2048
buffer_store_dwordx4 a[100:103], v1, s[8:11], s36 offen offset:3072 sc1
Mvav 52,148,192
K 199,178,179
Mvav 56,152,192
X 181
Mvav 88,152,200
Ra 140,242,3072
Mvav 120,152,208
X 182
Mvav 152,152,216
K 206,180,181
Mvav 156,156,216
X 183
Mvav 124,156,208
Mvav 92,156,200
K 207,182,183
Mvav 60,156,192
W 0
Cvaa 160,128,0,224
Ra 144,242,4096
Ra 148,242,5120
Mvaa 160,132,4
s_add_u32 m0, s62, 0
Ra 152,242,6144
Cvaa 164,132,20,228
X 184
Mvaa 164,128,16
X 185
Cvaa 168,128,32,232
X 186
Mvaa 168,132,36
Ra 156,242,7168
K 214,184,185
Cvaa 172,132,52,236
Dc 1,8,61,0
Mvaa 172,128,48
X 187
Mvaa 160,136,8
X 188
Mvaa 160,140,12
Ra 128,242,16384
K 215,186,187
Mvaa 164,140,28
Dc 1,8,61,1024
Mvaa 164,136,24
X 189
Mvaa 168,136,40
X 190
Mvaa 168,140,44
Ra 132,242,17408
K 222,188,189
Mvaa 172,140,60
Dc 1,8,61,2048
Mvaa 172,136,56
X 191
v_smfmac_f32_16x16x64_bf16 a[160:163], a[176:179], v[192:199], v240
Ra 136,242,18432
K 223,190,191
v_smfmac_f32_16x16x64_bf16 a[164:167], a[176:179], v[200:207], v240
X 160
v_smfmac_f32_16x16x64_bf16 a[168:171], a[176:179], v[208:215], v240
Dc 1,8,61,3072
v_smfmac_f32_16x16x64_bf16 a[172:175], a[176:179], v[216:223], v240
W 3
X 161
Cvaa 176,144,0,224
s_add_u32 m0, s62, 4096
X 162
Mvaa 176,148,4
Ra 140,242,19456
K 192,160,161
Cvaa 180,148,20,228
X 163
Mvaa 180,144,16
Dc 243,8,61,0
Cvaa 184,144,32,232
X 164
Mvaa 184,148,36
K 193,162,163
Cvaa 188,148,52,236
X 165
Mvaa 188,144,48
Dc 243,8,61,1024
Ra 144,242,20480
Mvaa 176,152,8
X 166
Mvaa 176,156,12
Ra 148,242,21504
K 200,164,165
Mvaa 180,156,28
X 167
Mvaa 180,152,24
Dc 243,8,61,2048
Mvaa 184,152,40
X 168
)"
R"(Mvaa 184,156,44
K 201,166,167
Mvaa 188,156,60
X 169
Mvaa 188,152,56
W 2
Dc 243,8,61,3072
Ra 152,242,22528
Mvav 32,128,196
s_add_u32 m0, s62, 8192
X 170
Mvav 64,128,204
Ra 156,242,23552
K 208,168,169
Mvav 96,128,212
X 171
Mvav 128,128,220
Dc 244,8,61,0
Mvav 132,132,220
X 172
Mvav 100,132,212
Ra 128,242,8192
K 209,170,171
Mvav 68,132,204
X 173
Mvav 36,132,196
Dc 244,8,61,1024
Mvav 40,136,196
X 174
Mvav 72,136,204
Ra 132,242,9216
K 216,172,173
Mvav 104,136,212
X 175
Mvav 136,136,220
Dc 244,8,61,2048
Mvav 140,140,220
X 176
Mvav 108,140,212
Ra 136,242,10240
K 217,174,175
Mvav 76,140,204
X 177
Mvav 44,140,196
W 3
Dc 244,8,61,3072
Mvav 48,144,196
X 178
Mvav 80,144,204
s_add_u32 m0, s62, 12288
Ra 140,242,11264
Mvav 112,144,212
K 194,176,177
Mvav 144,144,220
X 179
Mvav 148,148,220
X 180
Mvav 116,148,212
Dc 245,8,61,0
Ra 144,242,12288
Mvav 84,148,204
K 195,178,179
Mvav 52,148,196
X 181
Mvav 56,152,196
X 182
Mvav 88,152,204
Dc 245,8,61,1024
Ra 148,242,13312
Mvav 120,152,212
K 202,180,181
Mvav 152,152,220
X 183
Mvav 156,156,220
X 184
Mvav 124,156,212
Dc 245,8,61,2048
Ra 152,242,14336
Mvav 92,156,204
K 203,182,183
Mvav 60,156,196
W 3
X 185
Cvaa 160,128,0,224
X 186
Mvaa 160,132,4
Dc 245,8,61,3072
Ra 156,242,15360
Cvaa 164,132,20,228
s_add_u32 s61, s61, s63
K 210,184,185
Mvaa 164,128,16
X 187
Cvaa 168,128,32,232
X 188
Mvaa 168,132,36
Cvaa 172,132,52,236
s_xor_b32 s62, s62, 0x10000
K 211,186,187
Mvaa 172,128,48
X 189
Mvaa 160,136,8
Ra 128,242,24576
Ra 132,242,25600
Mvaa 160,140,12
s_add_u32 s64, s64, 1
X 190
Mvaa 164,140,28
K 218,188,189
Mvaa 164,136,24
X 191
Mvaa 168,136,40
Mvaa 168,140,44
X 160
Mvaa 172,140,60
s_add_u32 s36, s29, 262144
K 219,190,191
Mvaa 172,136,56
W 2
X 161
Cvaa 176,144,0,224
Ra 136,242,26624
Ra 140,242,27648
Mvaa 176,148,4
X 162
Cvaa 180,148,20,228
K 196,160,161
Mvaa 180,144,16
X 163
Cvaa 184,144,32,232
buffer_store_dwordx4 a[64:67], v1, s[8:11], s36 offen offset:0 sc1
Mvaa 184,148,36
X 164
Cvaa 188,148,52,236
K 197,162,163
Mvaa 188,144,48
X 165
Mvaa 176,152,8
Ra 144,242,28672
Ra 148,242,29696
Mvaa 176,156,12
X 166
Mvaa 180,156,28
buffer_store_dwordx4 a[72:75], v1, s[8:11], s36 offen offset:1024 sc1
K 204,164,165
Mvaa 180,152,24
X 167
Mvaa 184,152,40
Mvaa 184,156,44
X 168
Mvaa 188,156,60
Mvaa 188,152,56
W 2
Ra 152,242,30720
K 205,166,167
Mvav 32,128,192
X 169
Mvav 64,128,200
buffer_store_dwordx4 a[80:83], v1, s[8:11], s36 offen offset:2048 sc1
Ra 156,242,31744
Mvav 96,128,208
X 170
Mvav 128,128,216
K 212,168,169
Mvav 132,132,216
X 171
Mvav 100,132,208
X 172
Mvav 68,132,200
buffer_store_dwordx4 a[88:91], v1, s[8:11], s36 offen offset:3072 sc1
Mvav 36,132,192
K 213,170,171
Mvav 40,136,192
X 173
Mvav 72,136,200
X 174
Mvav 104,136,208
Mvav 136,136,216
s_add_u32 s36, s29, 266240
K 220,172,173
Mvav 140,140,216
X 175
Mvav 108,140,208
X 176
Mvav 76,140,200
s_waitcnt vmcnt(4) lgkmcnt(0)
s_barrier
v_xor_b32 v242, 0x10000, v242
Mvav 44,140,192
Ra 128,242,0
K 221,174,175
Mvav 48,144,192
X 177
Mvav 80,144,200
buffer_store_dwordx4 a[96:99], v1, s[8:11], s36 offen offset:0 sc1
Mvav 112,144,208
X 178
Mvav 144,144,216
Ra 132,242,1024
K 198,176,177
Mvav 148,148,216
X 179
Mvav 116,148,208
X 180
Mvav 84,148,200
Ra 136,242,2048
Mvav 52,148,192
buffer_store_dwordx4 a[104:107], v1, s[8:11], s36 offen offset:1024 sc1
K 199,178,179
Mvav 56,152,192
X 181
Mvav 88,152,200
Ra 140,242,3072
Mvav 120,152,208
X 182
Mvav 152,152,216
K 206,180,181
Mvav 156,156,216
X 183
Mvav 124,156,208
Mvav 92,156,200
K 207,182,183
Mvav 60,156,192
W 0
Cvaa 160,128,0,224
Ra 144,242,4096
Ra 148,242,5120
Mvaa 160,132,4
s_add_u32 m0, s62, 0
Ra 152,242,6144
Cvaa 164,132,20,228
X 184
Mvaa 164,128,16
X 185
Cvaa 168,128,32,232
X 186
Mvaa 168,132,36
Ra 156,242,7168
K 214,184,185
Cvaa 172,132,52,236
Dc 1,8,61,0
Mvaa 172,128,48
X 187
Mvaa 160,136,8
X 188
Mvaa 160,140,12
Ra 128,242,16384
K 215,186,187
Mvaa 164,140,28
Dc 1,8,61,1024
Mvaa 164,136,24
X 189
Mvaa 168,136,40
X 190
Mvaa 168,140,44
Ra 132,242,17408
K 222,188,189
Mvaa 172,140,60
Dc 1,8,61,2048
Mvaa 172,136,56
X 191
v_smfmac_f32_16x16x64_bf16 a[160:163], a[176:179], v[192:199], v240
Ra 136,242,18432
K 223,190,191
v_smfmac_f32_16x16x64_bf16 a[164:167], a[176:179], v[200:207], v240
X 160
v_smfmac_f32_16x16x64_bf16 a[168:171], a[176:179], v[208:215], v240
Dc 1,8,61,3072
v_smfmac_f32_16x16x64_bf16 a[172:175], a[176:179], v[216:223], v240
W 3
X 161
Cvaa 176,144,0,224
s_add_u32 m0, s62, 4096
X 162
Mvaa 176,148,4
Ra 140,242,19456
K 192,160,161
Cvaa 180,148,20,228
X 163
Mvaa 180,144,16
Dc 243,8,61,0
Cvaa 184,144,32,232
X 164
Mvaa 184,148,36
K 193,162,163
Cvaa 188,148,52,236
X 165
Mvaa 188,144,48
Dc 243,8,61,1024
Ra 144,242,20480
Mvaa 176,152,8
X 166
Mvaa 176,156,12
Ra 148,242,21504
K 200,164,165
Mvaa 180,156,28
X 167
Mvaa 180,152,24
Dc 243,8,61,2048
Mvaa 184,152,40
X 168
Mvaa 184,156,44
K 201,166,167
Mvaa 188,156,60
X 169
Mvaa 188,152,56
W 2
Dc 243,8,61,3072
Ra 152,242,22528
Mvav 32,128,196
s_add_u32 m0, s62, 8192
X 170
Mvav 64,128,204
Ra 156,242,23552
K 208,168,169
Mvav 96,128,212
X 171
Mvav 128,128,220
Dc 244,8,61,0
Mvav 132,132,220
X 172
Mvav 100,132,212
Ra 128,242,8192
K 209,170,171
Mvav 68,132,204
X 173
Mvav 36,132,196
Dc 244,8,61,1024
Mvav 40,136,196
X 174
Mvav 72,136,204
Ra 132,242,9216
K 216,172,173
Mvav 104,136,212
X 175
Mvav 136,136,220
Dc 244,8,61,2048
Mvav 140,140,220
X 176
Mvav 108,140,212
Ra 136,242,10240
K 217,174,175
Mvav 76,140,204
X 177
Mvav 44,140,196
W 3
Dc 244,8,61,3072
Mvav 48,144,196
X 178
Mvav 80,144,204
s_add_u32 m0, s62, 12288
Ra 140,242,11264
Mvav 112,144,212
K 194,176,177
Mvav 144,144,220
X 179
Mvav 148,148,220
X 180
Mvav 116,148,212
Dc 245,8,61,0
Ra 144,242,12288
Mvav 84,148,204
K 195,178,179
Mvav 52,148,196
X 181
Mvav 56,152,196
X 182
Mvav 88,152,204
Dc 245,8,61,1024
Ra 148,242,13312
Mvav 120,152,212
K 202,180,181
Mvav 152,152,220
X 183
Mvav 156,156,220
X 184
Mvav 124,156,212
Dc 245,8,61,2048
Ra 152,242,14336
Mvav 92,156,204
K 203,182,183
v_bfe_u32 v28, v1, 4, 4
Mvav 60,156,196
W 3
X 185
Cvaa 160,128,0,224
Ra 156,242,15360
v_lshlrev_b32 v28, 3, v28
Mvaa 160,132,4
s_lshl_b32 s80, s56, 9
s_add_u32 s80, s80, 4352
Dc 245,8,61,3072
Cvaa 164,132,20,228
s_add_u32 s61, s61, s63
X 186
Mvaa 164,128,16
K 210,184,185
Cvaa 168,128,32,232
X 187
Mvaa 168,132,36
)"
R"(buffer_load_dwordx2 v[28:29], v28, s[8:11], s80 offen sc1
Cvaa 172,132,52,236
s_xor_b32 s62, s62, 0x10000
X 188
Mvaa 172,128,48
Ra 128,242,24576
K 211,186,187
Mvaa 160,136,8
X 189
Mvaa 160,140,12
s_add_u32 s64, s64, 1
X 190
Mvaa 164,140,28
Ra 132,242,25600
K 218,188,189
Mvaa 164,136,24
X 191
Mvaa 168,136,40
Mvaa 168,140,44
X 160
Mvaa 172,140,60
buffer_store_dwordx4 a[112:115], v1, s[8:11], s36 offen offset:2048 sc1
K 219,190,191
Mvaa 172,136,56
W 2
X 161
Cvaa 176,144,0,224
Ra 136,242,26624
Ra 140,242,27648
Mvaa 176,148,4
X 162
Cvaa 180,148,20,228
K 196,160,161
Mvaa 180,144,16
X 163
Cvaa 184,144,32,232
Mvaa 184,148,36
X 164
Cvaa 188,148,52,236
K 197,162,163
Mvaa 188,144,48
X 165
Mvaa 176,152,8
Ra 144,242,28672
Ra 148,242,29696
Mvaa 176,156,12
X 166
Mvaa 180,156,28
K 204,164,165
Mvaa 180,152,24
X 167
Mvaa 184,152,40
Mvaa 184,156,44
X 168
Mvaa 188,156,60
Mvaa 188,152,56
W 2
Ra 152,242,30720
K 205,166,167
Mvav 32,128,192
X 169
Mvav 64,128,200
Ra 156,242,31744
Mvav 96,128,208
X 170
Mvav 128,128,216
K 212,168,169
Mvav 132,132,216
X 171
Mvav 100,132,208
X 172
Mvav 68,132,200
buffer_store_dwordx4 a[120:123], v1, s[8:11], s36 offen offset:3072 sc1
Mvav 36,132,192
K 213,170,171
Mvav 40,136,192
X 173
Mvav 72,136,200
X 174
Mvav 104,136,208
Mvav 136,136,216
s_waitcnt vmcnt(2)
v_cmp_eq_u64 vcc, s[12:13], v[28:29]
s_cmp_eq_u64 vcc, -1
s_cbranch_scc0 .Lspin_70%=
.Lok_71%=:
K 220,172,173
Mvav 140,140,216
X 175
Mvav 108,140,208
X 176
Mvav 76,140,200
W 0
s_barrier
v_xor_b32 v242, 0x10000, v242
Mvav 44,140,192
Ra 128,242,0
K 221,174,175
Mvav 48,144,192
X 177
Mvav 80,144,200
X 178
Mvav 112,144,208
Ra 132,242,1024
Mvav 144,144,216
K 198,176,177
Mvav 148,148,216
X 179
Mvav 116,148,208
X 180
Mvav 84,148,200
Ra 136,242,2048
Mvav 52,148,192
K 199,178,179
Mvav 56,152,192
X 181
Mvav 88,152,200
Ra 140,242,3072
Mvav 120,152,208
X 182
Mvav 152,152,216
K 206,180,181
Mvav 156,156,216
X 183
Mvav 124,156,208
Mvav 92,156,200
K 207,182,183
Mvav 60,156,192
W 0
Cvaa 160,128,0,224
Ra 144,242,4096
Ra 148,242,5120
Mvaa 160,132,4
s_add_u32 m0, s62, 0
Ra 152,242,6144
Cvaa 164,132,20,228
X 184
Mvaa 164,128,16
X 185
Cvaa 168,128,32,232
X 186
Mvaa 168,132,36
Ra 156,242,7168
K 214,184,185
Cvaa 172,132,52,236
Dc 1,8,61,0
Mvaa 172,128,48
X 187
Mvaa 160,136,8
X 188
Mvaa 160,140,12
Ra 128,242,16384
K 215,186,187
Mvaa 164,140,28
Dc 1,8,61,1024
Mvaa 164,136,24
X 189
Mvaa 168,136,40
X 190
Mvaa 168,140,44
Ra 132,242,17408
K 222,188,189
Mvaa 172,140,60
Dc 1,8,61,2048
Mvaa 172,136,56
X 191
v_smfmac_f32_16x16x64_bf16 a[160:163], a[176:179], v[192:199], v240
Ra 136,242,18432
K 223,190,191
v_smfmac_f32_16x16x64_bf16 a[164:167], a[176:179], v[200:207], v240
X 160
v_smfmac_f32_16x16x64_bf16 a[168:171], a[176:179], v[208:215], v240
Dc 1,8,61,3072
v_smfmac_f32_16x16x64_bf16 a[172:175], a[176:179], v[216:223], v240
W 3
X 161
Cvaa 176,144,0,224
s_add_u32 m0, s62, 4096
X 162
Mvaa 176,148,4
Ra 140,242,19456
K 192,160,161
Cvaa 180,148,20,228
X 163
Mvaa 180,144,16
Dc 243,8,61,0
Cvaa 184,144,32,232
X 164
Mvaa 184,148,36
K 193,162,163
Cvaa 188,148,52,236
X 165
Mvaa 188,144,48
Dc 243,8,61,1024
Ra 144,242,20480
Mvaa 176,152,8
X 166
Mvaa 176,156,12
Ra 148,242,21504
K 200,164,165
Mvaa 180,156,28
X 167
Mvaa 180,152,24
Dc 243,8,61,2048
Mvaa 184,152,40
X 168
Mvaa 184,156,44
K 201,166,167
Mvaa 188,156,60
X 169
Mvaa 188,152,56
W 2
Dc 243,8,61,3072
Ra 152,242,22528
Mvav 32,128,196
s_add_u32 m0, s62, 8192
X 170
Mvav 64,128,204
Ra 156,242,23552
K 208,168,169
Mvav 96,128,212
X 171
Mvav 128,128,220
Dc 244,8,61,0
Mvav 132,132,220
X 172
Mvav 100,132,212
Ra 128,242,8192
K 209,170,171
Mvav 68,132,204
X 173
Mvav 36,132,196
Dc 244,8,61,1024
Mvav 40,136,196
X 174
Mvav 72,136,204
Ra 132,242,9216
K 216,172,173
Mvav 104,136,212
X 175
Mvav 136,136,220
Dc 244,8,61,2048
Mvav 140,140,220
X 176
Mvav 108,140,212
Ra 136,242,10240
K 217,174,175
Mvav 76,140,204
X 177
Mvav 44,140,196
W 3
Dc 244,8,61,3072
Mvav 48,144,196
X 178
Mvav 80,144,204
s_add_u32 m0, s62, 12288
Ra 140,242,11264
Mvav 112,144,212
K 194,176,177
Mvav 144,144,220
X 179
Mvav 148,148,220
X 180
Mvav 116,148,212
Dc 245,8,61,0
Ra 144,242,12288
Mvav 84,148,204
K 195,178,179
Mvav 52,148,196
X 181
Mvav 56,152,196
X 182
Mvav 88,152,204
Dc 245,8,61,1024
Ra 148,242,13312
Mvav 120,152,212
K 202,180,181
Mvav 152,152,220
s_add_u32 s81, s57, 2
X 183
Mvav 156,156,220
s_waitcnt vmcnt(14)
s_add_u32 s80, s71, 384
v_mov_b64 v[28:29], s[12:13]
s_mov_b64 exec, 1
buffer_store_dwordx2 v[28:29], off, s[8:11], s80 sc1
s_mov_b64 exec, -1
X 184
Mvav 124,156,212
s_bitcmp0_b32 s81, 2
s_cselect_b32 s22, -1, 0
s_and_b32 s81, s81, 3
Ra 152,242,14336
Mvav 92,156,204
s_lshl_b32 s82, s56, 2
K 203,182,183
Mvav 60,156,196
W 3
X 185
Cvaa 160,128,0,224
s_add_u32 s81, s81, s82
Dc 245,8,61,2048
Ra 156,242,15360
Mvaa 160,132,4
s_lshl_b32 s81, s81, 1
X 186
Cvaa 164,132,20,228
s_add_u32 s81, s81, s14
s_sub_u32 s81, s81, 2
K 210,184,185
Mvaa 164,128,16
s_mul_i32 s81, s81, 20480
X 187
Cvaa 168,128,32,232
Dc 245,8,61,3072
v_min_u32 v2, 624, v1
Mvaa 168,132,36
s_add_u32 s61, s61, s63
X 188
Cvaa 172,132,52,236
s_xor_b32 s62, s62, 0x10000
v_lshl_add_u32 v2, v2, 3, s81
K 211,186,187
Mvaa 172,128,48
Ra 128,242,24576
v_and_b32 v2, s22, v2
Mvaa 160,136,8
s_and_b32 s22, s22, 5120
X 189
Mvaa 160,140,12
s_add_u32 s64, s64, 1
global_load_dword v3, v2, s[26:27]
Ra 132,242,25600
Mvaa 164,140,28
X 190
Mvaa 164,136,24
v_add_u32 v2, s22, v2
K 218,188,189
Mvaa 168,136,40
X 191
Mvaa 168,140,44
X 160
Mvaa 172,140,60
K 219,190,191
Mvaa 172,136,56
W 2
X 161
Cvaa 176,144,0,224
Ra 136,242,26624
Ra 140,242,27648
Mvaa 176,148,4
X 162
Cvaa 180,148,20,228
K 196,160,161
Mvaa 180,144,16
X 163
Cvaa 184,144,32,232
Mvaa 184,148,36
X 164
Cvaa 188,148,52,236
K 197,162,163
Mvaa 188,144,48
X 165
Mvaa 176,152,8
Ra 144,242,28672
Ra 148,242,29696
Mvaa 176,156,12
X 166
Mvaa 180,156,28
K 204,164,165
Mvaa 180,152,24
X 167
Mvaa 184,152,40
Mvaa 184,156,44
X 168
Mvaa 188,156,60
Mvaa 188,152,56
W 2
Ra 152,242,30720
K 205,166,167
Mvav 32,128,192
X 169
Mvav 64,128,200
Ra 156,242,31744
Mvav 96,128,208
X 170
Mvav 128,128,216
K 212,168,169
Mvav 132,132,216
X 171
Mvav 100,132,208
X 172
Mvav 68,132,200
Mvav 36,132,192
K 213,170,171
Mvav 40,136,192
X 173
Mvav 72,136,200
X 174
Mvav 104,136,208
)"
R"(Mvav 136,136,216
K 220,172,173
Mvav 140,140,216
X 175
Mvav 108,140,208
X 176
Mvav 76,140,200
s_waitcnt vmcnt(1) lgkmcnt(0)
s_barrier
v_xor_b32 v242, 0x10000, v242
Mvav 44,140,192
Ra 128,242,0
K 221,174,175
Mvav 48,144,192
X 177
Mvav 80,144,200
X 178
Mvav 112,144,208
Ra 132,242,1024
Mvav 144,144,216
K 198,176,177
Mvav 148,148,216
X 179
Mvav 116,148,208
X 180
Mvav 84,148,200
Ra 136,242,2048
Mvav 52,148,192
K 199,178,179
Mvav 56,152,192
X 181
Mvav 88,152,200
Ra 140,242,3072
Mvav 120,152,208
X 182
Mvav 152,152,216
K 206,180,181
Mvav 156,156,216
X 183
Mvav 124,156,208
Mvav 92,156,200
K 207,182,183
Mvav 60,156,192
W 0
Cvaa 160,128,0,224
Ra 144,242,4096
Ra 148,242,5120
Mvaa 160,132,4
s_add_u32 m0, s62, 0
Ra 152,242,6144
Cvaa 164,132,20,228
X 184
Mvaa 164,128,16
X 185
Cvaa 168,128,32,232
X 186
Mvaa 168,132,36
Ra 156,242,7168
K 214,184,185
Cvaa 172,132,52,236
Dc 1,8,61,0
Mvaa 172,128,48
X 187
Mvaa 160,136,8
X 188
Mvaa 160,140,12
Ra 128,242,16384
K 215,186,187
Mvaa 164,140,28
Dc 1,8,61,1024
Mvaa 164,136,24
X 189
Mvaa 168,136,40
X 190
Mvaa 168,140,44
Ra 132,242,17408
K 222,188,189
Mvaa 172,140,60
Dc 1,8,61,2048
Mvaa 172,136,56
X 191
v_smfmac_f32_16x16x64_bf16 a[160:163], a[176:179], v[192:199], v240
Ra 136,242,18432
K 223,190,191
v_smfmac_f32_16x16x64_bf16 a[164:167], a[176:179], v[200:207], v240
X 160
v_smfmac_f32_16x16x64_bf16 a[168:171], a[176:179], v[208:215], v240
Dc 1,8,61,3072
v_smfmac_f32_16x16x64_bf16 a[172:175], a[176:179], v[216:223], v240
W 3
X 161
Cvaa 176,144,0,224
s_add_u32 m0, s62, 4096
X 162
Mvaa 176,148,4
Ra 140,242,19456
K 192,160,161
Cvaa 180,148,20,228
X 163
Mvaa 180,144,16
Dc 243,8,61,0
Cvaa 184,144,32,232
X 164
Mvaa 184,148,36
K 193,162,163
Cvaa 188,148,52,236
X 165
Mvaa 188,144,48
Dc 243,8,61,1024
Ra 144,242,20480
Mvaa 176,152,8
X 166
Mvaa 176,156,12
Ra 148,242,21504
K 200,164,165
Mvaa 180,156,28
X 167
Mvaa 180,152,24
Dc 243,8,61,2048
Mvaa 184,152,40
X 168
Mvaa 184,156,44
K 201,166,167
Mvaa 188,156,60
X 169
Mvaa 188,152,56
W 2
Dc 243,8,61,3072
Ra 152,242,22528
Mvav 32,128,196
s_add_u32 m0, s62, 8192
X 170
Mvav 64,128,204
Ra 156,242,23552
K 208,168,169
Mvav 96,128,212
X 171
Mvav 128,128,220
Dc 244,8,61,0
Mvav 132,132,220
X 172
Mvav 100,132,212
Ra 128,242,8192
K 209,170,171
Mvav 68,132,204
X 173
Mvav 36,132,196
Dc 244,8,61,1024
Mvav 40,136,196
X 174
Mvav 72,136,204
Ra 132,242,9216
K 216,172,173
Mvav 104,136,212
X 175
Mvav 136,136,220
Dc 244,8,61,2048
Mvav 140,140,220
X 176
Mvav 108,140,212
Ra 136,242,10240
K 217,174,175
Mvav 76,140,204
X 177
Mvav 44,140,196
W 3
Dc 244,8,61,3072
Mvav 48,144,196
X 178
Mvav 80,144,204
s_add_u32 m0, s62, 12288
Ra 140,242,11264
Mvav 112,144,212
K 194,176,177
Mvav 144,144,220
X 179
Mvav 148,148,220
X 180
Mvav 116,148,212
Dc 245,8,61,0
Ra 144,242,12288
Mvav 84,148,204
K 195,178,179
Mvav 52,148,196
X 181
Mvav 56,152,196
X 182
Mvav 88,152,204
Dc 245,8,61,1024
Ra 148,242,13312
Mvav 120,152,212
K 202,180,181
Mvav 152,152,220
X 183
Mvav 156,156,220
X 184
Mvav 124,156,212
Dc 245,8,61,2048
Ra 152,242,14336
Mvav 92,156,204
K 203,182,183
Mvav 60,156,196
W 3
X 185
Cvaa 160,128,0,224
X 186
Mvaa 160,132,4
Dc 245,8,61,3072
Ra 156,242,15360
Cvaa 164,132,20,228
s_add_u32 s61, s61, s63
K 210,184,185
Mvaa 164,128,16
X 187
Cvaa 168,128,32,232
X 188
Mvaa 168,132,36
Cvaa 172,132,52,236
s_xor_b32 s62, s62, 0x10000
global_load_dword v3, v2, s[26:27]
K 211,186,187
Mvaa 172,128,48
X 189
Mvaa 160,136,8
Ra 128,242,24576
Ra 132,242,25600
Mvaa 160,140,12
s_add_u32 s64, s64, 1
X 190
Mvaa 164,140,28
v_add_u32 v2, s22, v2
K 218,188,189
Mvaa 164,136,24
X 191
Mvaa 168,136,40
Mvaa 168,140,44
X 160
Mvaa 172,140,60
K 219,190,191
Mvaa 172,136,56
W 2
X 161
Cvaa 176,144,0,224
Ra 136,242,26624
Ra 140,242,27648
Mvaa 176,148,4
X 162
Cvaa 180,148,20,228
K 196,160,161
Mvaa 180,144,16
X 163
Cvaa 184,144,32,232
Mvaa 184,148,36
X 164
Cvaa 188,148,52,236
K 197,162,163
Mvaa 188,144,48
X 165
Mvaa 176,152,8
Ra 144,242,28672
Ra 148,242,29696
Mvaa 176,156,12
X 166
Mvaa 180,156,28
K 204,164,165
Mvaa 180,152,24
X 167
Mvaa 184,152,40
Mvaa 184,156,44
X 168
Mvaa 188,156,60
Mvaa 188,152,56
W 2
Ra 152,242,30720
K 205,166,167
Mvav 32,128,192
X 169
Mvav 64,128,200
Ra 156,242,31744
Mvav 96,128,208
X 170
Mvav 128,128,216
K 212,168,169
Mvav 132,132,216
X 171
Mvav 100,132,208
X 172
Mvav 68,132,200
Mvav 36,132,192
K 213,170,171
Mvav 40,136,192
X 173
Mvav 72,136,200
X 174
Mvav 104,136,208
Mvav 136,136,216
K 220,172,173
Mvav 140,140,216
X 175
Mvav 108,140,208
X 176
Mvav 76,140,200
s_waitcnt vmcnt(1) lgkmcnt(0)
s_barrier
v_xor_b32 v242, 0x10000, v242
Mvav 44,140,192
Ra 128,242,0
K 221,174,175
Mvav 48,144,192
X 177
Mvav 80,144,200
X 178
Mvav 112,144,208
Ra 132,242,1024
Mvav 144,144,216
K 198,176,177
Mvav 148,148,216
X 179
Mvav 116,148,208
X 180
Mvav 84,148,200
Ra 136,242,2048
Mvav 52,148,192
K 199,178,179
Mvav 56,152,192
X 181
Mvav 88,152,200
Ra 140,242,3072
Mvav 120,152,208
X 182
Mvav 152,152,216
K 206,180,181
Mvav 156,156,216
X 183
Mvav 124,156,208
Mvav 92,156,200
K 207,182,183
Mvav 60,156,192
W 0
Cvaa 160,128,0,224
Ra 144,242,4096
Ra 148,242,5120
Mvaa 160,132,4
s_add_u32 m0, s62, 0
Ra 152,242,6144
Cvaa 164,132,20,228
X 184
Mvaa 164,128,16
X 185
Cvaa 168,128,32,232
X 186
Mvaa 168,132,36
Ra 156,242,7168
K 214,184,185
Cvaa 172,132,52,236
Dc 1,8,61,0
Mvaa 172,128,48
X 187
Mvaa 160,136,8
X 188
Mvaa 160,140,12
Ra 128,242,16384
K 215,186,187
Mvaa 164,140,28
Dc 1,8,61,1024
Mvaa 164,136,24
X 189
Mvaa 168,136,40
X 190
Mvaa 168,140,44
Ra 132,242,17408
K 222,188,189
Mvaa 172,140,60
Dc 1,8,61,2048
Mvaa 172,136,56
X 191
v_smfmac_f32_16x16x64_bf16 a[160:163], a[176:179], v[192:199], v240
Ra 136,242,18432
K 223,190,191
v_smfmac_f32_16x16x64_bf16 a[164:167], a[176:179], v[200:207], v240
X 160
v_smfmac_f32_16x16x64_bf16 a[168:171], a[176:179], v[208:215], v240
Dc 1,8,61,3072
v_smfmac_f32_16x16x64_bf16 a[172:175], a[176:179], v[216:223], v240
W 3
X 161
Cvaa 176,144,0,224
s_add_u32 m0, s62, 4096
X 162
Mvaa 176,148,4
Ra 140,242,19456
K 192,160,161
Cvaa 180,148,20,228
X 163
Mvaa 180,144,16
Dc 243,8,61,0
Cvaa 184,144,32,232
X 164
Mvaa 184,148,36
K 193,162,163
)"
R"(Cvaa 188,148,52,236
X 165
Mvaa 188,144,48
Dc 243,8,61,1024
Ra 144,242,20480
Mvaa 176,152,8
X 166
Mvaa 176,156,12
Ra 148,242,21504
K 200,164,165
Mvaa 180,156,28
X 167
Mvaa 180,152,24
Dc 243,8,61,2048
Mvaa 184,152,40
X 168
Mvaa 184,156,44
K 201,166,167
Mvaa 188,156,60
X 169
Mvaa 188,152,56
W 2
Dc 243,8,61,3072
Ra 152,242,22528
Mvav 32,128,196
s_add_u32 m0, s62, 8192
X 170
Mvav 64,128,204
Ra 156,242,23552
K 208,168,169
Mvav 96,128,212
X 171
Mvav 128,128,220
Dc 244,8,61,0
Mvav 132,132,220
X 172
Mvav 100,132,212
Ra 128,242,8192
K 209,170,171
Mvav 68,132,204
X 173
Mvav 36,132,196
Dc 244,8,61,1024
Mvav 40,136,196
X 174
Mvav 72,136,204
Ra 132,242,9216
K 216,172,173
Mvav 104,136,212
X 175
Mvav 136,136,220
Dc 244,8,61,2048
Mvav 140,140,220
X 176
Mvav 108,140,212
Ra 136,242,10240
K 217,174,175
Mvav 76,140,204
X 177
Mvav 44,140,196
W 3
Dc 244,8,61,3072
Mvav 48,144,196
X 178
Mvav 80,144,204
s_add_u32 m0, s62, 12288
Ra 140,242,11264
Mvav 112,144,212
K 194,176,177
Mvav 144,144,220
X 179
Mvav 148,148,220
X 180
Mvav 116,148,212
Dc 245,8,61,0
Ra 144,242,12288
Mvav 84,148,204
K 195,178,179
Mvav 52,148,196
X 181
Mvav 56,152,196
X 182
Mvav 88,152,204
Dc 245,8,61,1024
Ra 148,242,13312
Mvav 120,152,212
K 202,180,181
Mvav 152,152,220
X 183
Mvav 156,156,220
X 184
Mvav 124,156,212
Dc 245,8,61,2048
Ra 152,242,14336
Mvav 92,156,204
K 203,182,183
Mvav 60,156,196
W 3
X 185
Cvaa 160,128,0,224
X 186
Mvaa 160,132,4
Dc 245,8,61,3072
Ra 156,242,15360
Cvaa 164,132,20,228
s_add_u32 s61, s61, s63
K 210,184,185
Mvaa 164,128,16
X 187
Cvaa 168,128,32,232
X 188
Mvaa 168,132,36
Cvaa 172,132,52,236
s_xor_b32 s62, s62, 0x10000
global_load_dword v3, v2, s[26:27]
K 211,186,187
Mvaa 172,128,48
Ra 128,242,24576
v_add_u32 v2, s22, v2
Mvaa 160,136,8
s_lshr_b32 s80, s15, 3
s_lshl_b32 s80, s80, 1
s_add_u32 s80, s80, s14
Mvaa 160,140,12
s_sub_u32 s80, s80, 2
s_lshl_b32 s80, s80, 12
v_min_u32 v27, 496, v1
Mvaa 164,140,28
s_add_u32 s64, s64, 1
X 189
Mvaa 164,136,24
Ra 132,242,25600
v_lshl_add_u32 v27, v27, 3, s80
Mvaa 168,136,40
X 190
Mvaa 168,140,44
v_min_u32 v27, .Lend%=-.Lbeg%=-4, v27
K 218,188,189
Mvaa 172,140,60
global_load_dword v3, v27, s[6:7]
Mvaa 172,136,56
W 2
X 191
Cvaa 176,144,0,224
X 160
Mvaa 176,148,4
Ra 136,242,26624
K 219,190,191
Cvaa 180,148,20,228
X 161
Mvaa 180,144,16
Ra 140,242,27648
Cvaa 184,144,32,232
X 162
Mvaa 184,148,36
K 196,160,161
Cvaa 188,148,52,236
X 163
Mvaa 188,144,48
X 164
Mvaa 176,152,8
Ra 144,242,28672
K 197,162,163
Mvaa 176,156,12
X 165
Mvaa 180,156,28
Ra 148,242,29696
Mvaa 180,152,24
X 166
Mvaa 184,152,40
K 204,164,165
Mvaa 184,156,44
X 167
Mvaa 188,156,60
X 168
Mvaa 188,152,56
W 2
Ra 152,242,30720
K 205,166,167
Mvav 32,128,192
X 169
Mvav 64,128,200
Ra 156,242,31744
Mvav 96,128,208
X 170
Mvav 128,128,216
K 212,168,169
Mvav 132,132,216
X 171
Mvav 100,132,208
X 172
Mvav 68,132,200
Mvav 36,132,192
K 213,170,171
Mvav 40,136,192
X 173
Mvav 72,136,200
X 174
Mvav 104,136,208
Mvav 136,136,216
K 220,172,173
Mvav 140,140,216
X 175
Mvav 108,140,208
X 176
Mvav 76,140,200
s_waitcnt vmcnt(2) lgkmcnt(0)
s_barrier
v_xor_b32 v242, 0x10000, v242
Mvav 44,140,192
Ra 128,242,0
K 221,174,175
Mvav 48,144,192
X 177
Mvav 80,144,200
X 178
Mvav 112,144,208
Ra 132,242,1024
Mvav 144,144,216
K 198,176,177
Mvav 148,148,216
X 179
Mvav 116,148,208
X 180
Mvav 84,148,200
Ra 136,242,2048
Mvav 52,148,192
K 199,178,179
Mvav 56,152,192
X 181
Mvav 88,152,200
Ra 140,242,3072
Mvav 120,152,208
X 182
Mvav 152,152,216
K 206,180,181
Mvav 156,156,216
X 183
Mvav 124,156,208
Mvav 92,156,200
K 207,182,183
Mvav 60,156,192
W 0
Cvaa 160,128,0,224
Ra 144,242,4096
Ra 148,242,5120
Mvaa 160,132,4
s_add_u32 m0, s62, 0
Ra 152,242,6144
Cvaa 164,132,20,228
X 184
Mvaa 164,128,16
X 185
Cvaa 168,128,32,232
X 186
Mvaa 168,132,36
Ra 156,242,7168
K 214,184,185
Cvaa 172,132,52,236
Dc 1,8,61,0
Mvaa 172,128,48
X 187
Mvaa 160,136,8
X 188
Mvaa 160,140,12
Ra 128,242,16384
K 215,186,187
Mvaa 164,140,28
Dc 1,8,61,1024
Mvaa 164,136,24
X 189
Mvaa 168,136,40
X 190
Mvaa 168,140,44
Ra 132,242,17408
K 222,188,189
Mvaa 172,140,60
Dc 1,8,61,2048
Mvaa 172,136,56
X 191
v_smfmac_f32_16x16x64_bf16 a[160:163], a[176:179], v[192:199], v240
Ra 136,242,18432
K 223,190,191
v_smfmac_f32_16x16x64_bf16 a[164:167], a[176:179], v[200:207], v240
X 160
v_smfmac_f32_16x16x64_bf16 a[168:171], a[176:179], v[208:215], v240
Dc 1,8,61,3072
v_smfmac_f32_16x16x64_bf16 a[172:175], a[176:179], v[216:223], v240
W 3
X 161
Cvaa 176,144,0,224
s_add_u32 m0, s62, 4096
X 162
Mvaa 176,148,4
s_sub_u32 s81, s57, 1
Ra 140,242,19456
K 192,160,161
Cvaa 180,148,20,228
X 163
Mvaa 180,144,16
s_lshl_b32 s85, s81, 5
Dc 243,8,61,0
Cvaa 184,144,32,232
X 164
Mvaa 184,148,36
s_add_u32 s85, s85, s56
K 193,162,163
Cvaa 188,148,52,236
X 165
Mvaa 188,144,48
Dc 243,8,61,1024
Ra 144,242,20480
Mvaa 176,152,8
s_lshl_b32 s85, s85, 1
X 166
Mvaa 176,156,12
Ra 148,242,21504
K 200,164,165
Mvaa 180,156,28
s_add_u32 s85, s85, s14
X 167
Mvaa 180,152,24
Dc 243,8,61,2048
Mvaa 184,152,40
s_sub_u32 s85, s85, 2
X 168
Mvaa 184,156,44
K 201,166,167
Mvaa 188,156,60
X 169
Mvaa 188,152,56
W 2
s_cmp_lt_u32 s85, 160
s_cselect_b64 s[20:21], 0, -1
Dc 243,8,61,3072
Mvav 32,128,196
s_add_u32 m0, s62, 8192
Ra 152,242,22528
Ra 156,242,23552
Mvav 64,128,204
s_mul_i32 s18, s85, 205
X 170
Mvav 96,128,212
K 208,168,169
Mvav 128,128,220
s_lshr_b32 s18, s18, 12
X 171
Mvav 132,132,220
Dc 244,8,61,0
Ra 128,242,8192
Mvav 100,132,212
X 172
Mvav 68,132,204
s_and_b32 s18, s18, 7
K 209,170,171
Mvav 36,132,196
X 173
Mvav 40,136,196
Dc 244,8,61,1024
v_lshrrev_b32 v4, 1, v1
Mvav 72,136,204
X 174
Mvav 104,136,212
Ra 132,242,9216
K 216,172,173
Mvav 136,136,220
X 175
Mvav 140,140,220
Dc 244,8,61,2048
v_min_u32 v5, 344, v4
Mvav 108,140,212
s_lshl_b32 s81, s18, 3
X 176
Mvav 76,140,204
Ra 136,242,10240
K 217,174,175
Mvav 44,140,196
W 3
X 177
Mvav 48,144,196
Dc 244,8,61,3072
v_mov_b32 v6, s81
Mvav 80,144,204
s_add_u32 m0, s62, 12288
X 178
Mvav 112,144,212
Ra 140,242,11264
K 194,176,177
Mvav 144,144,220
X 179
Mvav 148,148,220
buffer_load_dwordx2 v[8:9], v4, s[8:11], 0 offen offset:2560 sc1
Ra 144,242,12288
Mvav 116,148,212
X 180
Mvav 84,148,204
K 195,178,179
Mvav 52,148,196
X 181
Mvav 56,152,196
)"
R"(buffer_load_dwordx2 v[10:11], v5, s[8:11], 0 offen offset:3072 sc1
Ra 148,242,13312
Mvav 88,152,204
X 182
Mvav 120,152,212
K 202,180,181
Mvav 152,152,220
X 183
Mvav 156,156,220
Dc 245,8,61,0
Ra 152,242,14336
Mvav 124,156,212
X 184
Mvav 92,156,204
K 203,182,183
v_bfe_u32 v28, v1, 4, 4
Mvav 60,156,196
W 3
X 185
Cvaa 160,128,0,224
buffer_load_dwordx2 v[12:13], v6, s[8:11], 0 offen offset:3584 sc1
v_lshlrev_b32 v28, 3, v28
Mvaa 160,132,4
s_lshl_b32 s80, s56, 9
X 186
Cvaa 164,132,20,228
s_add_u32 s80, s80, 4480
Ra 156,242,15360
K 210,184,185
Mvaa 164,128,16
X 187
Cvaa 168,128,32,232
Dc 245,8,61,1024
Mvaa 168,132,36
X 188
Cvaa 172,132,52,236
K 211,186,187
Mvaa 172,128,48
X 189
Mvaa 160,136,8
Dc 245,8,61,2048
Ra 128,242,24576
Mvaa 160,140,12
X 190
Mvaa 164,140,28
Ra 132,242,25600
K 218,188,189
Mvaa 164,136,24
X 191
Mvaa 168,136,40
buffer_load_dwordx2 v[28:29], v28, s[8:11], s80 offen sc1
Mvaa 168,140,44
Dc 245,8,61,3072
s_add_u32 s61, s61, s63
s_xor_b32 s62, s62, 0x10000
s_add_u32 s64, s64, 1
Mvaa 172,140,60
X 160
Mvaa 172,136,56
W 2
Ra 136,242,26624
K 219,190,191
Cvaa 176,144,0,224
X 161
Mvaa 176,148,4
global_load_dword v3, v2, s[26:27]
v_add_u32 v2, s22, v2
Cvaa 180,148,20,228
Ra 140,242,27648
Mvaa 180,144,16
X 162
Cvaa 184,144,32,232
K 196,160,161
Mvaa 184,148,36
X 163
Cvaa 188,148,52,236
X 164
Mvaa 188,144,48
Ra 144,242,28672
K 197,162,163
Mvaa 176,152,8
X 165
Mvaa 176,156,12
Ra 148,242,29696
Mvaa 180,156,28
X 166
Mvaa 180,152,24
K 204,164,165
Mvaa 184,152,40
X 167
Mvaa 184,156,44
X 168
Mvaa 188,156,60
Mvaa 188,152,56
W 2
Ra 152,242,30720
K 205,166,167
Mvav 32,128,192
X 169
Mvav 64,128,200
Ra 156,242,31744
Mvav 96,128,208
X 170
Mvav 128,128,216
K 212,168,169
Mvav 132,132,216
X 171
Mvav 100,132,208
X 172
Mvav 68,132,200
Mvav 36,132,192
K 213,170,171
Mvav 40,136,192
X 173
Mvav 72,136,200
X 174
Mvav 104,136,208
Mvav 136,136,216
s_waitcnt vmcnt(2)
v_cmp_eq_u64 vcc, s[12:13], v[28:29]
s_cmp_eq_u64 vcc, -1
s_cbranch_scc0 .Lspin_73%=
.Lok_74%=:
v_cmp_eq_u64 vcc, s[12:13], v[8:9]
v_cmp_eq_u64_e64 s[86:87], s[12:13], v[10:11]
s_and_b64 vcc, vcc, s[86:87]
s_or_b64 vcc, vcc, s[20:21]
s_cmp_eq_u64 vcc, -1
s_cbranch_scc0 .Lspin_76%=
.Lok_77%=:
K 220,172,173
Mvav 140,140,216
X 175
Mvav 108,140,208
X 176
Mvav 76,140,200
s_waitcnt vmcnt(1) lgkmcnt(0)
s_barrier
v_xor_b32 v242, 0x10000, v242
Mvav 44,140,192
Ra 128,242,0
K 221,174,175
Mvav 48,144,192
X 177
Mvav 80,144,200
X 178
Mvav 112,144,208
Ra 132,242,1024
Mvav 144,144,216
K 198,176,177
Mvav 148,148,216
X 179
Mvav 116,148,208
X 180
Mvav 84,148,200
Ra 136,242,2048
Mvav 52,148,192
K 199,178,179
Mvav 56,152,192
X 181
Mvav 88,152,200
Ra 140,242,3072
Mvav 120,152,208
X 182
Mvav 152,152,216
K 206,180,181
Mvav 156,156,216
X 183
Mvav 124,156,208
Mvav 92,156,200
K 207,182,183
Mvav 60,156,192
W 0
Cvaa 160,128,0,224
Ra 144,242,4096
Ra 148,242,5120
Mvaa 160,132,4
s_add_u32 m0, s62, 0
Ra 152,242,6144
Cvaa 164,132,20,228
X 184
Mvaa 164,128,16
X 185
Cvaa 168,128,32,232
X 186
Mvaa 168,132,36
Ra 156,242,7168
K 214,184,185
Cvaa 172,132,52,236
Dc 1,8,61,0
Mvaa 172,128,48
X 187
Mvaa 160,136,8
X 188
Mvaa 160,140,12
Ra 128,242,16384
K 215,186,187
Mvaa 164,140,28
Dc 1,8,61,1024
Mvaa 164,136,24
X 189
Mvaa 168,136,40
X 190
Mvaa 168,140,44
Ra 132,242,17408
K 222,188,189
Mvaa 172,140,60
Dc 1,8,61,2048
Mvaa 172,136,56
X 191
v_smfmac_f32_16x16x64_bf16 a[160:163], a[176:179], v[192:199], v240
Ra 136,242,18432
K 223,190,191
v_smfmac_f32_16x16x64_bf16 a[164:167], a[176:179], v[200:207], v240
X 160
v_smfmac_f32_16x16x64_bf16 a[168:171], a[176:179], v[208:215], v240
Dc 1,8,61,3072
v_smfmac_f32_16x16x64_bf16 a[172:175], a[176:179], v[216:223], v240
W 3
X 161
Cvaa 176,144,0,224
s_add_u32 m0, s62, 4096
X 162
Mvaa 176,148,4
Ra 140,242,19456
K 192,160,161
Cvaa 180,148,20,228
X 163
Mvaa 180,144,16
Dc 243,8,61,0
Cvaa 184,144,32,232
X 164
Mvaa 184,148,36
K 193,162,163
Cvaa 188,148,52,236
X 165
Mvaa 188,144,48
Dc 243,8,61,1024
Ra 144,242,20480
Mvaa 176,152,8
X 166
Mvaa 176,156,12
Ra 148,242,21504
K 200,164,165
Mvaa 180,156,28
X 167
Mvaa 180,152,24
Dc 243,8,61,2048
Mvaa 184,152,40
X 168
Mvaa 184,156,44
K 201,166,167
Mvaa 188,156,60
X 169
Mvaa 188,152,56
W 2
Dc 243,8,61,3072
Ra 152,242,22528
Mvav 32,128,196
s_add_u32 m0, s62, 8192
X 170
Mvav 64,128,204
Ra 156,242,23552
K 208,168,169
Mvav 96,128,212
X 171
Mvav 128,128,220
Dc 244,8,61,0
Mvav 132,132,220
X 172
Mvav 100,132,212
Ra 128,242,8192
K 209,170,171
Mvav 68,132,204
X 173
Mvav 36,132,196
Dc 244,8,61,1024
Mvav 40,136,196
X 174
Mvav 72,136,204
Ra 132,242,9216
K 216,172,173
Mvav 104,136,212
X 175
Mvav 136,136,220
Dc 244,8,61,2048
Mvav 140,140,220
X 176
Mvav 108,140,212
Ra 136,242,10240
K 217,174,175
Mvav 76,140,204
X 177
Mvav 44,140,196
W 3
Dc 244,8,61,3072
Mvav 48,144,196
X 178
Mvav 80,144,204
s_add_u32 m0, s62, 12288
Ra 140,242,11264
Mvav 112,144,212
K 194,176,177
Mvav 144,144,220
X 179
Mvav 148,148,220
X 180
Mvav 116,148,212
Dc 245,8,61,0
Ra 144,242,12288
Mvav 84,148,204
K 195,178,179
Mvav 52,148,196
X 181
Mvav 56,152,196
X 182
Mvav 88,152,204
Dc 245,8,61,1024
Ra 148,242,13312
Mvav 120,152,212
v_xor_b32 v6, s12, v12
K 202,180,181
Mvav 152,152,220
X 183
Mvav 156,156,220
Ra 152,242,14336
v_and_b32 v6, 2047, v6
Mvav 124,156,212
Dc 245,8,61,2048
v_lshlrev_b32 v6, 3, v6
Mvav 92,156,204
X 184
Mvav 60,156,196
W 3
K 203,182,183
Cvaa 160,128,0,224
s_mov_b32 s81, 40960
X 185
Mvaa 160,132,4
Dc 245,8,61,3072
Ra 156,242,15360
Cvaa 164,132,20,228
s_add_u32 s61, s61, s63
X 186
Mvaa 164,128,16
K 210,184,185
Cvaa 168,128,32,232
X 187
Mvaa 168,132,36
buffer_load_dword v7, v6, s[8:11], s81 offen sc1
v_lshrrev_b32 v5, 7, v1
Cvaa 172,132,52,236
s_xor_b32 s62, s62, 0x10000
X 188
Mvaa 172,128,48
K 211,186,187
v_mul_u32_u24 v4, 20480, v5
Mvaa 160,136,8
X 189
Mvaa 160,140,12
s_add_u32 s64, s64, 1
Ra 128,242,24576
Ra 132,242,25600
Mvaa 164,140,28
X 190
Mvaa 164,136,24
v_and_b32 v6, 0x70, v1
K 218,188,189
Mvaa 168,136,40
X 191
Mvaa 168,140,44
v_add_u32 v4, v4, v6
Mvaa 172,140,60
X 160
Mvaa 172,136,56
W 2
v_add_u32 v5, 2, v5
K 219,190,191
Cvaa 176,144,0,224
X 161
Mvaa 176,148,4
Ra 136,242,26624
v_lshrrev_b32 v5, 3, v5
Cvaa 180,148,20,228
Ra 140,242,27648
v_lshl_or_b32 v5, v5, 31, v4
Mvaa 180,144,16
X 162
Cvaa 184,144,32,232
K 196,160,161
Mvaa 184,148,36
s_lshl_b32 s19, s85, 7
X 163
Cvaa 188,148,52,236
X 164
Mvaa 188,144,48
s_add_u32 s19, s19, 2752512
Ra 144,242,28672
K 197,162,163
Mvaa 176,152,8
)"
R"(X 165
Mvaa 176,156,12
s_and_b32 s81, s20, 134217728
Ra 148,242,29696
Mvaa 180,156,28
X 166
Mvaa 180,152,24
K 204,164,165
Mvaa 184,152,40
s_or_b32 s19, s19, s81
X 167
Mvaa 184,156,44
X 168
Mvaa 188,156,60
buffer_load_dwordx4 a[64:67], v4, s[8:11], s19 offen sc1
Mvaa 188,152,56
W 2
Ra 152,242,30720
K 205,166,167
Mvav 32,128,192
s_add_u32 s19, s19, 163840
X 169
Mvav 64,128,200
Ra 156,242,31744
Mvav 96,128,208
X 170
Mvav 128,128,216
buffer_load_dwordx4 a[68:71], v4, s[8:11], s19 offen sc1
K 212,168,169
Mvav 132,132,216
X 171
Mvav 100,132,208
s_add_u32 s19, s19, 163840
X 172
Mvav 68,132,200
Mvav 36,132,192
buffer_load_dwordx4 a[72:75], v4, s[8:11], s19 offen sc1
K 213,170,171
Mvav 40,136,192
X 173
Mvav 72,136,200
X 174
Mvav 104,136,208
s_add_u32 s19, s19, 163840
Mvav 136,136,216
K 220,172,173
Mvav 140,140,216
X 175
Mvav 108,140,208
buffer_load_dwordx4 a[76:79], v4, s[8:11], s19 offen sc1
Mvav 76,140,200
s_waitcnt vmcnt(5) lgkmcnt(0)
s_barrier
v_xor_b32 v242, 0x10000, v242
Mvav 44,140,192
s_add_u32 s19, s19, 163840
X 176
Mvav 48,144,192
Ra 128,242,0
K 221,174,175
Mvav 80,144,200
X 177
Mvav 112,144,208
buffer_load_dwordx4 a[80:83], v4, s[8:11], s19 offen sc1
Ra 132,242,1024
Mvav 144,144,216
s_add_u32 s19, s19, 163840
X 178
Mvav 148,148,216
K 198,176,177
Mvav 116,148,208
X 179
Mvav 84,148,200
buffer_load_dwordx4 a[84:87], v4, s[8:11], s19 offen sc1
Ra 136,242,2048
Mvav 52,148,192
X 180
Mvav 56,152,192
s_add_u32 s19, s19, 163840
Ra 140,242,3072
K 199,178,179
Mvav 88,152,200
X 181
Mvav 120,152,208
X 182
Mvav 152,152,216
buffer_load_dwordx4 a[88:91], v5, s[8:11], s19 offen sc1
K 206,180,181
Mvav 156,156,216
X 183
Mvav 124,156,208
Mvav 92,156,200
K 207,182,183
Mvav 60,156,192
W 0
Cvaa 160,128,0,224
Ra 144,242,4096
Ra 148,242,5120
Mvaa 160,132,4
s_add_u32 m0, s62, 0
Ra 152,242,6144
Cvaa 164,132,20,228
X 184
Mvaa 164,128,16
X 185
Cvaa 168,128,32,232
X 186
Mvaa 168,132,36
Ra 156,242,7168
K 214,184,185
Cvaa 172,132,52,236
Dc 1,8,61,0
Mvaa 172,128,48
X 187
Mvaa 160,136,8
X 188
Mvaa 160,140,12
Ra 128,242,16384
K 215,186,187
Mvaa 164,140,28
Dc 1,8,61,1024
Mvaa 164,136,24
X 189
Mvaa 168,136,40
X 190
Mvaa 168,140,44
Ra 132,242,17408
K 222,188,189
Mvaa 172,140,60
Dc 1,8,61,2048
Mvaa 172,136,56
X 191
v_smfmac_f32_16x16x64_bf16 a[160:163], a[176:179], v[192:199], v240
Ra 136,242,18432
K 223,190,191
v_smfmac_f32_16x16x64_bf16 a[164:167], a[176:179], v[200:207], v240
X 160
v_smfmac_f32_16x16x64_bf16 a[168:171], a[176:179], v[208:215], v240
Dc 1,8,61,3072
v_smfmac_f32_16x16x64_bf16 a[172:175], a[176:179], v[216:223], v240
W 3
X 161
Cvaa 176,144,0,224
s_add_u32 m0, s62, 4096
X 162
Mvaa 176,148,4
Ra 140,242,19456
K 192,160,161
Cvaa 180,148,20,228
X 163
Mvaa 180,144,16
Dc 243,8,61,0
Cvaa 184,144,32,232
X 164
Mvaa 184,148,36
K 193,162,163
Cvaa 188,148,52,236
X 165
Mvaa 188,144,48
Dc 243,8,61,1024
Ra 144,242,20480
Mvaa 176,152,8
X 166
Mvaa 176,156,12
Ra 148,242,21504
K 200,164,165
Mvaa 180,156,28
X 167
Mvaa 180,152,24
Dc 243,8,61,2048
Mvaa 184,152,40
X 168
Mvaa 184,156,44
K 201,166,167
Mvaa 188,156,60
X 169
Mvaa 188,152,56
W 2
Dc 243,8,61,3072
Ra 152,242,22528
Mvav 32,128,196
s_add_u32 m0, s62, 8192
X 170
Mvav 64,128,204
Ra 156,242,23552
K 208,168,169
Mvav 96,128,212
X 171
Mvav 128,128,220
Dc 244,8,61,0
Mvav 132,132,220
X 172
Mvav 100,132,212
Ra 128,242,8192
K 209,170,171
Mvav 68,132,204
X 173
Mvav 36,132,196
Dc 244,8,61,1024
Mvav 40,136,196
X 174
Mvav 72,136,204
Ra 132,242,9216
K 216,172,173
Mvav 104,136,212
X 175
Mvav 136,136,220
Dc 244,8,61,2048
Mvav 140,140,220
s_bfe_u32 s81, s59, 0x10001
X 176
Mvav 108,140,212
s_lshl_b32 s82, s57, 1
Ra 136,242,10240
K 217,174,175
Mvav 76,140,204
s_add_u32 s81, s81, s82
X 177
Mvav 44,140,196
W 3
s_lshl_b32 s83, s81, 2
Dc 244,8,61,3072
Mvav 48,144,196
s_lshr_b32 s81, s59, 2
X 178
Mvav 80,144,204
s_add_u32 m0, s62, 12288
s_lshl_b32 s81, s81, 1
Ra 140,242,11264
Mvav 112,144,212
s_add_u32 s83, s83, s81
K 194,176,177
Mvav 144,144,220
s_and_b32 s81, s59, 1
X 179
Mvav 148,148,220
s_add_u32 s83, s83, s81
X 180
Mvav 116,148,212
s_and_b32 s81, s14, 1
Dc 245,8,61,0
Ra 144,242,12288
Mvav 84,148,204
s_mul_i32 s81, s81, 124
K 195,178,179
Mvav 52,148,196
s_xor_b32 s83, s83, s81
X 181
Mvav 56,152,196
s_mul_i32 s83, s83, 20480
X 182
Mvav 88,152,204
s_mul_i32 s81, s58, 320
Dc 245,8,61,1024
Ra 148,242,13312
Mvav 120,152,212
s_add_u32 s82, s83, s81
K 202,180,181
Mvav 152,152,220
X 183
Mvav 156,156,220
Ra 152,242,14336
v_bfe_u32 v246, v1, 4, 4
Mvav 124,156,212
Dc 245,8,61,2048
v_mul_u32_u24 v246, 1280, v246
Mvav 92,156,204
X 184
Mvav 60,156,196
W 3
v_lshrrev_b32 v247, 8, v1
K 203,182,183
Cvaa 160,128,0,224
v_mul_u32_u24 v247, 104, v247
v_add3_u32 v246, v246, v247, s82
Mvaa 160,132,4
X 185
Cvaa 164,132,20,228
Dc 245,8,61,3072
Ra 156,242,15360
Mvaa 164,128,16
s_add_u32 s61, s61, s63
X 186
Cvaa 168,128,32,232
K 210,184,185
Mvaa 168,132,36
X 187
Cvaa 172,132,52,236
s_xor_b32 s62, s62, 0x10000
X 188
Mvaa 172,128,48
global_load_dword v3, v246, s[16:17]
K 211,186,187
Mvaa 160,136,8
s_lshl_b32 s81, s81, 3
s_add_u32 s83, s83, s81
s_add_u32 s83, s83, 131072
Mvaa 160,140,12
s_add_u32 s64, s64, 1
v_lshrrev_b32 v246, 9, v1
v_mul_u32_u24 v246, 10240, v246
Mvaa 164,140,28
Ra 128,242,24576
v_and_b32 v247, 0x1f0, v1
Mvaa 164,136,24
X 189
Mvaa 168,136,40
Ra 132,242,25600
v_min_u32 v247, 304, v247
Mvaa 168,140,44
X 190
Mvaa 172,140,60
v_lshl_add_u32 v246, v247, 3, v246
K 218,188,189
Mvaa 172,136,56
W 2
buffer_load_dword v3, v246, s[8:11], s83 offen sc1
Ra 136,242,26624
Cvaa 176,144,0,224
X 191
Mvaa 176,148,4
X 160
Cvaa 180,148,20,228
Ra 140,242,27648
K 219,190,191
Mvaa 180,144,16
X 161
Cvaa 184,144,32,232
X 162
Mvaa 184,148,36
K 196,160,161
Cvaa 188,148,52,236
X 163
Mvaa 188,144,48
X 164
Mvaa 176,152,8
Ra 144,242,28672
K 197,162,163
Mvaa 176,156,12
X 165
Mvaa 180,156,28
Ra 148,242,29696
Mvaa 180,152,24
X 166
Mvaa 184,152,40
K 204,164,165
Mvaa 184,156,44
X 167
Mvaa 188,156,60
X 168
Mvaa 188,152,56
W 2
Ra 152,242,30720
K 205,166,167
Mvav 32,128,192
X 169
Mvav 64,128,200
Ra 156,242,31744
Mvav 96,128,208
X 170
Mvav 128,128,216
K 212,168,169
Mvav 132,132,216
X 171
Mvav 100,132,208
X 172
Mvav 68,132,200
Mvav 36,132,192
K 213,170,171
Mvav 40,136,192
X 173
Mvav 72,136,200
X 174
Mvav 104,136,208
Mvav 136,136,216
K 220,172,173
Mvav 140,140,216
X 175
Mvav 108,140,208
X 176
Mvav 76,140,200
s_waitcnt vmcnt(2) lgkmcnt(0)
s_barrier
v_xor_b32 v242, 0x10000, v242
Mvav 44,140,192
Ra 128,242,0
K 221,174,175
Mvav 48,144,192
X 177
Mvav 80,144,200
X 178
Mvav 112,144,208
Ra 132,242,1024
Mvav 144,144,216
K 198,176,177
Mvav 148,148,216
X 179
Mvav 116,148,208
X 180
Mvav 84,148,200
)"
R"(Ra 136,242,2048
Mvav 52,148,192
K 199,178,179
Mvav 56,152,192
X 181
Mvav 88,152,200
Ra 140,242,3072
Mvav 120,152,208
X 182
Mvav 152,152,216
K 206,180,181
Mvav 156,156,216
X 183
Mvav 124,156,208
Mvav 92,156,200
K 207,182,183
Mvav 60,156,192
W 0
Cvaa 160,128,0,224
Ra 144,242,4096
Ra 148,242,5120
Mvaa 160,132,4
s_add_u32 m0, s62, 0
Ra 152,242,6144
G 8,64
Cvaa 164,132,20,228
X 184
Mvaa 164,128,16
X 185
Cvaa 168,128,32,232
X 186
Mvaa 168,132,36
G 9,65
K 214,184,185
Cvaa 172,132,52,236
Ra 156,242,7168
Dc 1,8,61,0
Mvaa 172,128,48
X 187
Mvaa 160,136,8
X 188
Mvaa 160,140,12
Ra 128,242,16384
G 10,66
Mvaa 164,140,28
Dc 1,8,61,1024
K 215,186,187
Mvaa 164,136,24
X 189
Mvaa 168,136,40
Ra 132,242,17408
G 11,67
Mvaa 168,140,44
X 190
Mvaa 172,140,60
Dc 1,8,61,2048
K 222,188,189
Mvaa 172,136,56
X 191
v_smfmac_f32_16x16x64_bf16 a[160:163], a[176:179], v[192:199], v240
G 12,68
K 223,190,191
v_smfmac_f32_16x16x64_bf16 a[164:167], a[176:179], v[200:207], v240
Ra 136,242,18432
v_smfmac_f32_16x16x64_bf16 a[168:171], a[176:179], v[208:215], v240
Dc 1,8,61,3072
G 13,69
v_smfmac_f32_16x16x64_bf16 a[172:175], a[176:179], v[216:223], v240
W 3
X 160
Cvaa 176,144,0,224
s_add_u32 m0, s62, 4096
X 161
Mvaa 176,148,4
X 162
Cvaa 180,148,20,228
Ra 140,242,19456
K 192,160,161
Mvaa 180,144,16
Dc 243,8,61,0
G 14,70
Cvaa 184,144,32,232
X 163
Mvaa 184,148,36
X 164
Cvaa 188,148,52,236
G 15,71
K 193,162,163
Mvaa 188,144,48
Dc 243,8,61,1024
Ra 144,242,20480
Mvaa 176,152,8
X 165
Mvaa 176,156,12
X 166
Mvaa 180,156,28
Ra 148,242,21504
A 8,8,12
Mvaa 180,152,24
Dc 243,8,61,2048
K 200,164,165
Mvaa 184,152,40
X 167
Mvaa 184,156,44
A 9,9,13
Mvaa 188,156,60
X 168
Mvaa 188,152,56
W 2
Dc 243,8,61,3072
K 201,166,167
Mvav 32,128,196
s_add_u32 m0, s62, 8192
X 169
Mvav 64,128,204
Ra 152,242,22528
A 10,10,14
Mvav 96,128,212
X 170
Mvav 128,128,220
Ra 156,242,23552
K 208,168,169
Mvav 132,132,220
Dc 244,8,61,0
A 11,11,15
Mvav 100,132,212
X 171
Mvav 68,132,204
X 172
Mvav 36,132,196
Ra 128,242,8192
K 209,170,171
Mvav 40,136,196
Dc 244,8,61,1024
G 12,72
Mvav 72,136,204
X 173
Mvav 104,136,212
X 174
Mvav 136,136,220
Ra 132,242,9216
G 13,73
Mvav 140,140,220
Dc 244,8,61,2048
K 216,172,173
Mvav 108,140,212
X 175
Mvav 76,140,204
X 176
Mvav 44,140,196
W 2
Ra 136,242,10240
G 14,74
Mvav 48,144,196
Dc 244,8,61,3072
K 217,174,175
Mvav 80,144,204
s_add_u32 m0, s62, 12288
X 177
Mvav 112,144,212
Ra 140,242,11264
G 15,75
Mvav 144,144,220
X 178
Mvav 148,148,220
K 194,176,177
A 8,8,12
Mvav 116,148,212
X 179
Mvav 84,148,204
X 180
Mvav 52,148,196
Dc 245,8,61,0
Ra 144,242,12288
Mvav 56,152,196
K 195,178,179
A 9,9,13
Mvav 88,152,204
X 181
Mvav 120,152,212
X 182
Mvav 152,152,220
Dc 245,8,61,1024
Ra 148,242,13312
Mvav 156,156,220
K 202,180,181
A 10,10,14
Mvav 124,156,212
X 183
Mvav 92,156,204
X 184
Mvav 60,156,196
W 2
Dc 245,8,61,2048
Ra 152,242,14336
Cvaa 160,128,0,224
A 11,11,15
K 203,182,183
Mvaa 160,132,4
X 185
Cvaa 164,132,20,228
X 186
Mvaa 164,128,16
Dc 245,8,61,3072
Ra 156,242,15360
Cvaa 168,128,32,232
s_add_u32 s61, s61, s63
G 12,76
K 210,184,185
Mvaa 168,132,36
X 187
Cvaa 172,132,52,236
s_xor_b32 s62, s62, 0x10000
G 13,77
Mvaa 172,128,48
X 188
Mvaa 160,136,8
K 211,186,187
G 14,78
Mvaa 160,140,12
s_add_u32 s64, s64, 1
X 189
Mvaa 164,140,28
Ra 128,242,24576
Ra 132,242,25600
Mvaa 164,136,24
X 190
Mvaa 168,136,40
K 218,188,189
G 15,79
Mvaa 168,140,44
X 191
Mvaa 172,140,60
s_lshl_b32 s92, s58, 2
X 160
Mvaa 172,136,56
W 2
K 219,190,191
A 8,8,12
Cvaa 176,144,0,224
X 161
Mvaa 176,148,4
Ra 136,242,26624
Ra 140,242,27648
Cvaa 180,148,20,228
s_add_u32 s91, s92, s51
X 162
Mvaa 180,144,16
A 9,9,13
K 196,160,161
Cvaa 184,144,32,232
X 163
Mvaa 184,148,36
s_add_u32 s95, s92, 0
X 164
Cvaa 188,148,52,236
A 10,10,14
K 197,162,163
Mvaa 188,144,48
X 165
Mvaa 176,152,8
s_and_b32 s95, s95, 15
Ra 144,242,28672
A 11,11,15
Mvaa 176,156,12
Ra 148,242,29696
Mvaa 180,156,28
X 166
Mvaa 180,152,24
s_mul_i32 s95, s95, 40960
K 204,164,165
Mvaa 184,152,40
X 167
Mvaa 184,156,44
G 12,80
Mvaa 188,156,60
s_mul_i32 s71, s51, 0
X 168
Mvaa 188,152,56
W 2
K 205,166,167
G 13,81
Mvav 32,128,192
X 169
Mvav 64,128,200
s_add_u32 s71, s71, 133120
Ra 152,242,30720
Ra 156,242,31744
Mvav 96,128,208
X 170
Mvav 128,128,216
G 14,82
K 212,168,169
Mvav 132,132,216
s_add_u32 m0, s71, s48
X 171
Mvav 100,132,208
X 172
Mvav 68,132,200
G 15,83
Mvav 36,132,192
s_lshl2_add_u32 s94, s48, s95
K 213,170,171
Mvav 40,136,192
X 173
Mvav 72,136,200
X 174
Mvav 104,136,208
D 16,40,94,0
A 8,8,12
Mvav 136,136,216
K 220,172,173
Mvav 140,140,216
X 175
Mvav 108,140,208
A 9,9,13
Mvav 76,140,200
s_waitcnt vmcnt(1) lgkmcnt(0)
s_barrier
v_xor_b32 v242, 0x10000, v242
Mvav 44,140,192
D 17,40,94,1024
Ra 128,242,0
Mvav 48,144,192
X 176
Mvav 80,144,200
K 221,174,175
A 10,10,14
Mvav 112,144,208
X 177
Mvav 144,144,216
D 18,40,94,2048
Ra 132,242,1024
Mvav 148,148,216
X 178
Mvav 116,148,208
K 198,176,177
A 11,11,15
Mvav 84,148,200
X 179
Mvav 52,148,192
D 19,40,94,3072
Ra 136,242,2048
Mvav 56,152,192
s_add_u32 m0, m0, 4096
X 180
Mvav 88,152,200
G 12,84
K 199,178,179
Mvav 120,152,208
X 181
Mvav 152,152,216
Ra 140,242,3072
buffer_load_dwordx4 v20, s[40:43], s94 offen lds
Mvav 156,156,216
X 182
Mvav 124,156,208
K 206,180,181
Mvav 92,156,200
X 183
Mvav 60,156,192
K 207,182,183
W 0
Cvaa 160,128,0,224
Ra 144,242,4096
Ra 148,242,5120
Mvaa 160,132,4
s_add_u32 m0, s62, 0
Ra 152,242,6144
G 13,85
Cvaa 164,132,20,228
X 184
Mvaa 164,128,16
X 185
Cvaa 168,128,32,232
X 186
Mvaa 168,132,36
G 14,86
K 214,184,185
Cvaa 172,132,52,236
Ra 156,242,7168
Dc 1,8,61,0
Mvaa 172,128,48
X 187
Mvaa 160,136,8
X 188
Mvaa 160,140,12
Ra 128,242,16384
G 15,87
Mvaa 164,140,28
Dc 1,8,61,1024
K 215,186,187
Mvaa 164,136,24
X 189
Mvaa 168,136,40
Ra 132,242,17408
A 8,8,12
Mvaa 168,140,44
X 190
Mvaa 172,140,60
)"
R"(Dc 1,8,61,2048
K 222,188,189
Mvaa 172,136,56
X 191
v_smfmac_f32_16x16x64_bf16 a[160:163], a[176:179], v[192:199], v240
A 9,9,13
K 223,190,191
v_smfmac_f32_16x16x64_bf16 a[164:167], a[176:179], v[200:207], v240
Ra 136,242,18432
A 10,10,14
v_smfmac_f32_16x16x64_bf16 a[168:171], a[176:179], v[208:215], v240
Dc 1,8,61,3072
v_smfmac_f32_16x16x64_bf16 a[172:175], a[176:179], v[216:223], v240
W 3
X 160
Cvaa 176,144,0,224
s_add_u32 m0, s62, 4096
X 161
Mvaa 176,148,4
X 162
Cvaa 180,148,20,228
Ra 140,242,19456
A 11,11,15
Mvaa 180,144,16
Dc 243,8,61,0
K 192,160,161
Cvaa 184,144,32,232
X 163
Mvaa 184,148,36
X 164
Cvaa 188,148,52,236
G 12,88
K 193,162,163
Mvaa 188,144,48
Dc 243,8,61,1024
G 13,89
Mvaa 176,152,8
X 165
Mvaa 176,156,12
X 166
Mvaa 180,156,28
Ra 144,242,20480
Ra 148,242,21504
Mvaa 180,152,24
Dc 243,8,61,2048
K 200,164,165
Mvaa 184,152,40
X 167
Mvaa 184,156,44
G 14,90
Mvaa 188,156,60
X 168
Mvaa 188,152,56
W 2
Dc 243,8,61,3072
K 201,166,167
Mvav 32,128,196
s_add_u32 m0, s62, 8192
Ra 152,242,22528
G 15,91
Mvav 64,128,204
X 169
Mvav 96,128,212
X 170
Mvav 128,128,220
Ra 156,242,23552
A 8,8,12
Mvav 132,132,220
Dc 244,8,61,0
K 208,168,169
Mvav 100,132,212
X 171
Mvav 68,132,204
X 172
Mvav 36,132,196
Ra 128,242,8192
A 9,9,13
Mvav 40,136,196
Dc 244,8,61,1024
K 209,170,171
Mvav 72,136,204
X 173
Mvav 104,136,212
Ra 132,242,9216
A 10,10,14
Mvav 136,136,220
X 174
Mvav 140,140,220
Dc 244,8,61,2048
K 216,172,173
Mvav 108,140,212
X 175
Mvav 76,140,204
Ra 136,242,10240
A 11,11,15
Mvav 44,140,196
W 3
X 176
Mvav 48,144,196
Dc 244,8,61,3072
K 217,174,175
Mvav 80,144,204
s_add_u32 m0, s62, 12288
Ra 140,242,11264
v_add_f32_dpp v8, v8, v8 row_ror:8 row_mask:0xf bank_mask:0xf
Mvav 112,144,212
X 177
Mvav 144,144,220
X 178
Mvav 148,148,220
K 194,176,177
v_add_f32_dpp v9, v9, v9 row_ror:8 row_mask:0xf bank_mask:0xf
Mvav 116,148,212
X 179
Mvav 84,148,204
X 180
Mvav 52,148,196
Dc 245,8,61,0
Ra 144,242,12288
Mvav 56,152,196
v_add_f32_dpp v10, v10, v10 row_ror:8 row_mask:0xf bank_mask:0xf
K 195,178,179
Mvav 88,152,204
X 181
Mvav 120,152,212
X 182
Mvav 152,152,220
Dc 245,8,61,1024
v_add_f32_dpp v11, v11, v11 row_ror:8 row_mask:0xf bank_mask:0xf
Mvav 156,156,220
Ra 148,242,13312
K 202,180,181
Mvav 124,156,212
X 183
Mvav 92,156,204
Ra 152,242,14336
v_mov_b32 v12, v8
Mvav 60,156,196
W 3
Dc 245,8,61,2048
v_mov_b32 v13, v9
Cvaa 160,128,0,224
X 184
Mvaa 160,132,4
Ra 156,242,15360
K 203,182,183
Cvaa 164,132,20,228
X 185
Mvaa 164,128,16
Dc 245,8,61,3072
v_mov_b32 v14, v10
Cvaa 168,128,32,232
s_add_u32 s61, s61, s63
X 186
Mvaa 168,132,36
K 210,184,185
v_mov_b32 v15, v11
Cvaa 172,132,52,236
s_xor_b32 s62, s62, 0x10000
X 187
Mvaa 172,128,48
X 188
Mvaa 160,136,8
Ra 128,242,24576
K 211,186,187
Mvaa 160,140,12
s_add_u32 s64, s64, 1
X 189
Mvaa 164,140,28
X 190
Mvaa 164,136,24
Ra 132,242,25600
v_permlane16_swap_b32 v8, v12
Mvaa 168,136,40
K 218,188,189
v_permlane16_swap_b32 v9, v13
Mvaa 168,140,44
X 191
Mvaa 172,140,60
s_lshl_b32 s92, s58, 2
X 160
Mvaa 172,136,56
W 2
K 219,190,191
v_permlane16_swap_b32 v10, v14
Cvaa 176,144,0,224
X 161
Mvaa 176,148,4
Ra 136,242,26624
Ra 140,242,27648
Cvaa 180,148,20,228
s_add_u32 s91, s92, s51
X 162
Mvaa 180,144,16
K 196,160,161
v_permlane16_swap_b32 v11, v15
Cvaa 184,144,32,232
X 163
Mvaa 184,148,36
s_add_u32 s95, s92, 1
X 164
Cvaa 188,148,52,236
A 8,8,12
K 197,162,163
Mvaa 188,144,48
X 165
Mvaa 176,152,8
s_and_b32 s95, s95, 15
Ra 144,242,28672
Ra 148,242,29696
Mvaa 176,156,12
X 166
Mvaa 180,156,28
A 9,9,13
K 204,164,165
Mvaa 180,152,24
s_mul_i32 s95, s95, 40960
X 167
Mvaa 184,152,40
Mvaa 184,156,44
A 10,10,14
Mvaa 188,156,60
s_mul_i32 s71, s51, 0
X 168
Mvaa 188,152,56
W 2
Ra 152,242,30720
K 205,166,167
Mvav 32,128,192
X 169
Mvav 64,128,200
s_add_u32 s71, s71, 143360
Ra 156,242,31744
A 11,11,15
Mvav 96,128,208
X 170
Mvav 128,128,216
K 212,168,169
v_mov_b32 v12, v8
Mvav 132,132,216
s_add_u32 m0, s71, s48
X 171
Mvav 100,132,208
X 172
Mvav 68,132,200
Mvav 36,132,192
s_lshl2_add_u32 s94, s48, s95
v_mov_b32 v13, v9
K 213,170,171
Mvav 40,136,192
X 173
Mvav 72,136,200
X 174
Mvav 104,136,208
D 16,40,94,0
v_mov_b32 v14, v10
Mvav 136,136,216
K 220,172,173
Mvav 140,140,216
X 175
Mvav 108,140,208
v_mov_b32 v15, v11
Mvav 76,140,200
s_waitcnt vmcnt(1) lgkmcnt(0)
s_barrier
v_xor_b32 v242, 0x10000, v242
Mvav 44,140,192
D 17,40,94,1024
Ra 128,242,0
Mvav 48,144,192
X 176
Mvav 80,144,200
K 221,174,175
v_permlane32_swap_b32 v8, v12
Mvav 112,144,208
X 177
Mvav 144,144,216
D 18,40,94,2048
Ra 132,242,1024
Mvav 148,148,216
X 178
Mvav 116,148,208
K 198,176,177
v_permlane32_swap_b32 v9, v13
Mvav 84,148,200
X 179
Mvav 52,148,192
D 19,40,94,3072
Ra 136,242,2048
Mvav 56,152,192
s_add_u32 m0, m0, 4096
X 180
Mvav 88,152,200
K 199,178,179
v_permlane32_swap_b32 v10, v14
Mvav 120,152,208
X 181
Mvav 152,152,216
Ra 140,242,3072
buffer_load_dwordx4 v20, s[40:43], s94 offen lds
Mvav 156,156,216
X 182
Mvav 124,156,208
K 206,180,181
Mvav 92,156,200
X 183
Mvav 60,156,192
K 207,182,183
W 0
Cvaa 160,128,0,224
Ra 144,242,4096
Ra 148,242,5120
Mvaa 160,132,4
s_add_u32 m0, s62, 0
Ra 152,242,6144
v_permlane32_swap_b32 v11, v15
Cvaa 164,132,20,228
X 184
Mvaa 164,128,16
X 185
Cvaa 168,128,32,232
X 186
Mvaa 168,132,36
A 8,8,12
K 214,184,185
Cvaa 172,132,52,236
Ra 156,242,7168
Dc 1,8,61,0
Mvaa 172,128,48
X 187
Mvaa 160,136,8
Ra 128,242,16384
A 9,9,13
Mvaa 160,140,12
X 188
Mvaa 164,140,28
Dc 1,8,61,1024
K 215,186,187
Mvaa 164,136,24
X 189
Mvaa 168,136,40
Ra 132,242,17408
A 10,10,14
Mvaa 168,140,44
X 190
Mvaa 172,140,60
Dc 1,8,61,2048
K 222,188,189
Mvaa 172,136,56
A 11,11,15
v_smfmac_f32_16x16x64_bf16 a[160:163], a[176:179], v[192:199], v240
X 191
v_smfmac_f32_16x16x64_bf16 a[164:167], a[176:179], v[200:207], v240
K 223,190,191
v_and_b32 v7, 0xffff0000, v7
v_smfmac_f32_16x16x64_bf16 a[168:171], a[176:179], v[208:215], v240
Dc 1,8,61,3072
Ra 136,242,18432
v_smfmac_f32_16x16x64_bf16 a[172:175], a[176:179], v[216:223], v240
W 3
X 160
Cvaa 176,144,0,224
s_add_u32 m0, s62, 4096
X 161
Mvaa 176,148,4
X 162
Cvaa 180,148,20,228
Ra 140,242,19456
v_ashrrev_i32 v6, 31, v7
Mvaa 180,144,16
Dc 243,8,61,0
K 192,160,161
Cvaa 184,144,32,232
X 163
Mvaa 184,148,36
v_not_b32 v6, v6
Cvaa 188,148,52,236
X 164
Mvaa 188,144,48
Dc 243,8,61,1024
K 193,162,163
Mvaa 176,152,8
X 165
Mvaa 176,156,12
Ra 144,242,20480
v_or_b32 v6, 0x80000000, v6
Mvaa 180,156,28
X 166
Mvaa 180,152,24
Ra 148,242,21504
v_xor_b32 v7, v7, v6
Mvaa 184,152,40
Dc 243,8,61,2048
K 200,164,165
Mvaa 184,156,44
X 167
Mvaa 188,156,60
X 168
Mvaa 188,152,56
W 2
)"
R"(v_and_b32 v7, 0xffff0000, v7
K 201,166,167
Mvav 32,128,196
Dc 243,8,61,3072
v_mul_f32 v7, 0xbfb8aa3b, v7
Mvav 64,128,204
s_add_u32 m0, s62, 8192
X 169
Mvav 96,128,212
Ra 152,242,22528
Ra 156,242,23552
Mvav 128,128,220
X 170
Mvav 132,132,220
Dc 244,8,61,0
K 208,168,169
Mvav 100,132,212
X 171
Mvav 68,132,204
X 7
Mvav 36,132,196
X 172
Mvav 40,136,196
Ra 128,242,8192
K 209,170,171
Mvav 72,136,204
X 173
Mvav 104,136,212
Dc 244,8,61,1024
Ra 132,242,9216
Mvav 136,136,220
X 174
Mvav 140,140,220
v_add_f32 v7, 1.0, v7
K 216,172,173
Mvav 108,140,212
X 175
Mvav 76,140,204
Dc 244,8,61,2048
Ra 136,242,10240
Mvav 44,140,196
W 3
v_rcp_f32 v7, v7
Mvav 48,144,196
X 176
Mvav 80,144,204
Ra 140,242,11264
K 217,174,175
Mvav 112,144,212
Dc 244,8,61,3072
K 7,7,7
Mvav 144,144,220
s_add_u32 m0, s62, 12288
X 177
Mvav 148,148,220
X 178
Mvav 116,148,212
Ra 144,242,12288
K 194,176,177
Mvav 84,148,204
X 179
Mvav 52,148,196
X 180
Mvav 56,152,196
Dc 245,8,61,0
v_lshlrev_b32 v7, 16, v7
Mvav 88,152,204
K 195,178,179
K 12,8,9
Mvav 120,152,212
X 181
Mvav 152,152,220
X 182
Mvav 156,156,220
Dc 245,8,61,1024
Ra 148,242,13312
Mvav 124,156,212
K 202,180,181
K 13,10,11
Mvav 92,156,204
X 183
Mvav 60,156,196
W 2
X 184
Cvaa 160,128,0,224
Dc 245,8,61,2048
Ra 152,242,14336
Mvaa 160,132,4
K 203,182,183
v_lshlrev_b32 v8, 16, v12
Cvaa 164,132,20,228
X 185
Mvaa 164,128,16
X 186
Cvaa 168,128,32,232
Dc 245,8,61,3072
Ra 156,242,15360
Mvaa 168,132,36
s_add_u32 s61, s61, s63
v_and_b32 v9, 0xffff0000, v12
K 210,184,185
Cvaa 172,132,52,236
s_xor_b32 s62, s62, 0x10000
X 187
Mvaa 172,128,48
X 188
Mvaa 160,136,8
v_lshlrev_b32 v10, 16, v13
K 211,186,187
Mvaa 160,140,12
s_add_u32 s64, s64, 1
X 189
Mvaa 164,140,28
Ra 128,242,24576
v_and_b32 v11, 0xffff0000, v13
Mvaa 164,136,24
X 190
Mvaa 168,136,40
Ra 132,242,25600
K 218,188,189
Mvaa 168,140,44
X 191
Mvaa 172,140,60
s_lshl_b32 s92, s58, 2
U 8,8,7
U 9,9,7
Mvaa 172,136,56
W 2
X 160
Cvaa 176,144,0,224
Ra 136,242,26624
K 219,190,191
Mvaa 176,148,4
X 161
Cvaa 180,148,20,228
s_add_u32 s91, s92, s51
Ra 140,242,27648
U 10,10,7
Mvaa 180,144,16
X 162
Cvaa 184,144,32,232
K 196,160,161
Mvaa 184,148,36
s_add_u32 s95, s92, 2
X 163
Cvaa 188,148,52,236
U 11,11,7
Mvaa 188,144,48
X 164
Mvaa 176,152,8
s_and_b32 s95, s95, 15
Ra 144,242,28672
K 197,162,163
Mvaa 176,156,12
X 165
Mvaa 180,156,28
Ra 148,242,29696
K 12,8,9
Mvaa 180,152,24
s_mul_i32 s95, s95, 40960
X 166
Mvaa 184,152,40
K 204,164,165
K 14,10,11
Mvaa 184,156,44
X 167
Mvaa 188,156,60
s_mul_i32 s71, s51, 0
X 168
Mvaa 188,152,56
W 2
v_mov_b32 v13, s13
K 205,166,167
Mvav 32,128,192
X 169
Mvav 64,128,200
s_add_u32 s71, s71, 153600
Ra 152,242,30720
Ra 156,242,31744
Mvav 96,128,208
X 170
Mvav 128,128,216
v_mov_b32 v15, s13
K 212,168,169
Mvav 132,132,216
s_add_u32 m0, s71, s48
X 171
Mvav 100,132,208
v_lshrrev_b32 v6, 7, v1
Mvav 68,132,200
X 172
Mvav 36,132,192
s_lshl2_add_u32 s94, s48, s95
K 213,170,171
Mvav 40,136,192
X 173
Mvav 72,136,200
v_lshl_or_b32 v6, v6, 28, v1
Mvav 104,136,208
X 174
Mvav 136,136,216
s_lshl_b32 s19, s85, 7
D 16,40,94,0
K 220,172,173
Mvav 140,140,216
X 175
Mvav 108,140,208
X 176
Mvav 76,140,200
s_waitcnt vmcnt(1) lgkmcnt(0)
s_barrier
v_xor_b32 v242, 0x10000, v242
Mvav 44,140,192
s_add_u32 s19, s19, 98304
D 17,40,94,1024
K 221,174,175
Mvav 48,144,192
Ra 128,242,0
Mvav 80,144,200
s_and_b32 s21, s20, 134217728
X 177
Mvav 112,144,208
X 178
Mvav 144,144,216
D 18,40,94,2048
Ra 132,242,1024
Mvav 148,148,216
s_or_b32 s19, s19, s21
K 198,176,177
Mvav 116,148,208
X 179
Mvav 84,148,200
X 180
Mvav 52,148,192
D 19,40,94,3072
Ra 136,242,2048
Mvav 56,152,192
s_add_u32 m0, m0, 4096
Ra 140,242,3072
K 199,178,179
Mvav 88,152,200
X 181
Mvav 120,152,208
X 182
Mvav 152,152,216
buffer_store_dwordx4 v[12:15], v6, s[8:11], s19 offen sc1
K 206,180,181
Mvav 156,156,216
X 183
Mvav 124,156,208
Mvav 92,156,200
K 207,182,183
Mvav 60,156,192
buffer_load_dwordx4 v20, s[40:43], s94 offen lds
.Lf1_40%=:
W 0
Cvaa 160,128,0,224
v_bfe_u32 v28, v1, 4, 2
v_lshlrev_b32 v28, 3, v28
Mvaa 160,132,4
Ra 144,242,4096
Ra 148,242,5120
Cvaa 164,132,20,228
Ra 152,242,6144
buffer_load_dwordx2 v[28:29], v28, s[8:11], s54 offen sc1
Mvaa 164,128,16
s_lshl_b32 s92, s58, 2
s_add_u32 s91, s92, s51
s_add_u32 s95, s91, 3
Cvaa 168,128,32,232
s_and_b32 s95, s95, 15
s_mul_i32 s95, s95, 40960
s_mul_i32 s71, s51, 10240
Mvaa 168,132,36
s_add_u32 s71, s71, 0
s_add_u32 m0, s71, s48
s_lshl2_add_u32 s94, s48, s95
Cvaa 172,132,52,236
X 184
Mvaa 172,128,48
X 185
Mvaa 160,136,8
X 186
Mvaa 160,140,12
Ra 156,242,7168
K 214,184,185
Mvaa 164,140,28
X 187
Mvaa 164,136,24
D 16,40,94,0
Ra 128,242,16384
Mvaa 168,136,40
s_add_u32 s95, s91, 5
s_and_b32 s95, s95, 15
s_mul_i32 s95, s95, 40960
Mvaa 168,140,44
s_mul_i32 s71, s51, 10240
s_add_u32 s71, s71, 20480
Ra 132,242,17408
Mvaa 172,140,60
X 188
Mvaa 172,136,56
D 17,40,94,1024
K 215,186,187
v_smfmac_f32_16x16x64_bf16 a[160:163], a[176:179], v[192:199], v240
X 189
v_smfmac_f32_16x16x64_bf16 a[164:167], a[176:179], v[200:207], v240
X 190
K 222,188,189
X 191
s_nop 0
K 223,190,191
v_smfmac_f32_16x16x64_bf16 a[168:171], a[176:179], v[208:215], v240
D 18,40,94,2048
Ra 136,242,18432
v_smfmac_f32_16x16x64_bf16 a[172:175], a[176:179], v[216:223], v240
W 3
X 160
Cvaa 176,144,0,224
X 161
Mvaa 176,148,4
X 162
Cvaa 180,148,20,228
D 19,40,94,3072
K 192,160,161
Mvaa 180,144,16
s_add_u32 m0, m0, 4096
X 163
Cvaa 184,144,32,232
X 164
Mvaa 184,148,36
Ra 140,242,19456
K 193,162,163
Cvaa 188,148,52,236
buffer_load_dwordx4 v20, s[40:43], s94 offen lds
Mvaa 188,144,48
s_add_u32 m0, s71, s48
s_lshl2_add_u32 s94, s48, s95
Ra 144,242,20480
Mvaa 176,152,8
X 165
Mvaa 176,156,12
X 166
Mvaa 180,156,28
D 16,40,94,0
K 200,164,165
Mvaa 180,152,24
X 167
Mvaa 184,152,40
X 172
Mvaa 184,156,44
D 17,40,94,1024
K 201,166,167
Mvaa 188,156,60
X 173
Mvaa 188,152,56
s_cmp_eq_u32 s60, 16
s_cbranch_scc1 .Lmk_79%=
.Lmkr_80%=:
W 1
Mvav 32,128,196
Ra 148,242,21504
D 18,40,94,2048
Mvav 64,128,204
X 174
Mvav 96,128,212
Ra 152,242,22528
K 216,172,173
Mvav 128,128,220
X 175
Mvav 132,132,220
s_mov_b32 s18, 40960
X 168
Mvav 100,132,212
D 19,40,94,3072
K 217,174,175
Mvav 68,132,204
s_add_u32 m0, m0, 4096
X 169
Mvav 36,132,196
s_mov_b32 s19, 73728
X 170
Mvav 40,136,196
s_bitcmp1_b32 s14, 0
s_cselect_b32 s18, 49152, s18
s_cselect_b32 s19, 98304, s19
Ra 156,242,23552
K 208,168,169
Mvav 72,136,204
s_cmp_lt_u32 s14, 2
s_cselect_b32 s20, s18, s19
s_cselect_b32 s21, s19, s18
X 171
Mvav 104,136,212
X 184
Mvav 136,136,220
buffer_load_dwordx4 v20, s[40:43], s94 offen lds
K 209,170,171
Mvav 140,140,220
s_waitcnt vmcnt(10)
)"
R"(v_cmp_eq_u64 vcc, s[12:13], v[28:29]
s_cmp_eq_u64 vcc, -1
s_cselect_b32 s46, 79691776, 0
s_add_u32 s81, s58, 0
s_and_b32 s81, s81, 3
Mvav 108,140,212
s_lshl_b32 s81, s81, 12
s_add_u32 s81, s81, s49
Ra 128,242,24576
Mvav 76,140,204
X 185
Mvav 44,140,196
W 1
X 186
Mvav 48,144,196
buffer_load_dwordx4 a[184:187], v1, s[44:47], s81 offen offset:0 sc1
K 210,184,185
Mvav 80,144,204
s_add_u32 s82, s81, 131072
X 187
Mvav 112,144,212
v_add_u32 v2, s20, v1
v_add_u32 v3, s21, v1
Mvav 144,144,220
s_lshl_b32 s18, s14, 9
X 188
Mvav 148,148,220
Ra 132,242,25600
K 211,186,187
Mvav 116,148,212
buffer_load_dwordx4 a[200:203], v1, s[44:47], s82 offen offset:0 sc1
Ra 136,242,26624
Mvav 84,148,204
X 189
Mvav 52,148,196
v_lshrrev_b32 v232, 1, v1
v_add_u32 v232, s18, v232
Mvav 56,152,196
X 190
Mvav 88,152,204
K 218,188,189
v_bfe_u32 v12, v1, 6, 2
Mvav 120,152,212
buffer_load_dwordx4 a[188:191], v1, s[44:47], s81 offen offset:1024 sc1
Ra 140,242,27648
Mvav 152,152,220
X 191
Mvav 156,156,220
v_lshrrev_b32 v13, 8, v1
v_bfe_u32 v14, v1, 4, 1
Mvav 124,156,212
X 176
Mvav 92,156,204
K 219,190,191
v_lshlrev_b32 v14, 4, v14
Mvav 60,156,196
X 177
Maav 168,180,208
X 178
K 194,176,177
X 179
X 180
K 195,178,179
Maav 172,180,216
X 181
X 182
K 202,180,181
X 183
s_nop 0
K 203,182,183
Maav 160,180,192
buffer_load_dwordx4 a[204:207], v1, s[44:47], s82 offen offset:1024 sc1
v_mov_b32 v15, 0x3f80
Maav 164,180,200
W 0
Ra 144,242,28672
v_lshlrev_b32 v15, v14, v15
Mvav 96,128,208
Ra 148,242,29696
v_bfe_u32 v14, v1, 5, 1
v_xor_b32 v27, v12, v13
Mvav 128,128,216
buffer_load_dwordx4 a[192:195], v1, s[44:47], s81 offen offset:2048 sc1
v_lshl_or_b32 v27, v27, 1, v14
v_cmp_eq_u32_e64 s[86:87], 0, v27
Mvav 132,132,216
Ra 152,242,30720
Ra 156,242,31744
v_mov_b32 v226, 0
Mvav 100,132,208
v_cndmask_b32_e64 v224, 0, v15, s[86:87]
v_mov_b32 v228, 0
v_mov_b32 v230, v224
Mvav 104,136,208
buffer_load_dwordx4 a[208:211], v1, s[44:47], s82 offen offset:2048 sc1
v_cmp_eq_u32_e64 s[86:87], 1, v27
v_mov_b32 v227, 0
Mvav 136,136,216
v_cndmask_b32_e64 v225, 0, v15, s[86:87]
v_mov_b32 v229, 0
v_mov_b32 v231, v225
Mvav 140,140,216
G 4,160
G 8,161
G 5,164
Mvav 108,140,208
W 0
buffer_load_dwordx4 a[196:199], v1, s[44:47], s81 offen offset:3072 sc1
G 9,165
G 6,168
Mvav 112,144,208
G 10,169
G 7,172
G 11,173
Mvav 144,144,216
A 4,4,8
A 5,5,9
A 6,6,10
Mvav 148,148,216
buffer_load_dwordx4 a[212:215], v1, s[44:47], s82 offen offset:3072 sc1
s_add_u32 s81, s58, 1
s_and_b32 s81, s81, 3
s_lshl_b32 s81, s81, 12
Mvav 116,148,208
s_add_u32 s81, s81, s49
A 7,7,11
ds_write_b64 v232, v[6:7] offset:57344
A 8,4,5
Mvav 120,152,208
A 9,6,7
A 8,8,9
v_cmp_ngt_f32_e64 s[86:87], s90, v8
s_lshl_b32 s19, s14, 2
Mvav 152,152,216
buffer_load_dwordx4 a[216:219], v1, s[44:47], s81 offen offset:0 sc1
s_add_u32 s82, s81, 131072
v_mov_b32 v9, s19
s_or_b64 s[88:89], s[88:89], s[86:87]
Mvav 156,156,216
s_and_b64 s[88:89], s[88:89], s[24:25]
s_cselect_b32 s18, 1, 0
v_mov_b32 v8, s18
ds_write_b32 v9, v8 offset:59392
v_xor_b32 v10, 1024, v232
Mvav 124,156,208
v_bfe_u32 v11, v1, 2, 4
K 96,96,97
K 97,98,99
Mvav 32,128,192
buffer_load_dwordx4 a[232:235], v1, s[44:47], s82 offen offset:0 sc1
K 98,100,101
K 99,102,103
Mvav 64,128,200
ds_write_b128 v2, v[96:99] offset:0
K 128,128,129
K 129,130,131
Mvav 68,132,200
K 130,132,133
K 131,134,135
ds_write_b128 v2, v[128:131] offset:4096
Mvav 36,132,192
buffer_load_dwordx4 a[220:223], v1, s[44:47], s81 offen offset:1024 sc1
K 104,104,105
K 105,106,107
Mvav 40,136,192
K 106,108,109
K 107,110,111
ds_write_b128 v2, v[104:107] offset:1024
Mvav 72,136,200
K 136,136,137
K 137,138,139
K 138,140,141
Mvav 76,140,200
buffer_load_dwordx4 a[236:239], v1, s[44:47], s82 offen offset:1024 sc1
K 139,142,143
ds_write_b128 v2, v[136:139] offset:5120
Mvav 44,140,192
K 112,112,113
K 113,114,115
K 114,116,117
Mvav 48,144,192
K 115,118,119
ds_write_b128 v2, v[112:115] offset:2048
K 144,144,145
Mvav 80,144,200
K 145,146,147
K 146,148,149
K 147,150,151
Mvav 84,148,200
ds_write_b128 v2, v[144:147] offset:6144
buffer_load_dwordx4 a[224:227], v1, s[44:47], s81 offen offset:2048 sc1
K 120,120,121
Mvav 52,148,192
K 121,122,123
K 122,124,125
K 123,126,127
Mvav 56,152,192
ds_write_b128 v2, v[120:123] offset:3072
K 152,152,153
K 153,154,155
Mvav 88,152,200
buffer_load_dwordx4 a[240:243], v1, s[44:47], s82 offen offset:2048 sc1
K 154,156,157
K 155,158,159
Mvav 92,156,200
ds_write_b128 v2, v[152:155] offset:7168
Mvav 60,156,192
buffer_load_dwordx4 a[228:231], v1, s[44:47], s81 offen offset:3072 sc1
buffer_load_dwordx4 a[244:247], v1, s[44:47], s82 offen offset:3072 sc1
s_branch .LL_81%=
.p2align 12
.LL_81%=:
.Lend_43%=:
s_waitcnt vmcnt(8) lgkmcnt(0)
s_barrier
s_cmp_eq_u32 s46, 0
s_cbranch_scc1 .Llate_82%=
ds_read_b32 v11, v11 offset:59392
ds_read_b64 v[8:9], v10 offset:57344
Rv 176,3,0
Rv 180,3,1024
Rv 184,3,2048
Rv 188,3,3072
Rv 192,3,4096
Rv 196,3,5120
Rv 200,3,6144
Rv 204,3,7168
W 6
Mvvv 32,224,176
s_lshl_b32 s92, s58, 2
s_add_u32 s91, s92, s51
s_add_u32 s95, s91, 7
Mvvv 36,228,176
s_and_b32 s95, s95, 15
s_mul_i32 s95, s95, 40960
s_mul_i32 s71, s51, 20480
Mvvv 40,224,180
s_add_u32 s71, s71, 61440
s_add_u32 m0, s71, s48
s_lshl2_add_u32 s94, s48, s95
Mvvv 44,228,180
W 4
v_cmp_ne_u32 vcc, 0, v11
s_cmp_lg_u64 vcc, 0
s_cbranch_scc1 .Lredo%=
A 4,4,8
Mvvv 48,224,184
v_rcp_f32 v4, v4
Mvvv 52,228,184
Ta 128,25,10240
A 5,5,9
Mvvv 56,224,188
Ta 130,25,15360
D 16,40,94,0
Mvvv 60,228,188
W 4
s_add_u32 s95, s91, 9
s_and_b32 s95, s95, 15
Mvvv 64,224,192
s_mul_i32 s95, s95, 40960
s_mul_i32 s71, s51, 10240
s_add_u32 s71, s71, 112640
Mvvv 68,228,192
v_rcp_f32 v5, v5
Mvvv 72,224,196
s_lshl_b32 s93, s58, 12
s_add_u32 s93, s93, s50
Ta 132,25,20480
Mvvv 76,228,196
Ta 134,25,25600
W 4
Mvvv 80,224,200
U 32,32,4
U 33,33,4
Mvvv 84,228,200
Ta 136,26,10272
U 34,34,4
Mvvv 88,224,204
Ta 138,26,15392
U 35,35,4
Mvvv 92,228,204
Ta 140,26,20512
Ta 142,26,25632
W 4
Zvaa 96,128,184
U 36,36,4
U 37,37,4
Mvaa 96,132,188
D 17,40,94,1024
U 38,38,4
Zvaa 104,132,204
U 39,39,4
K 32,32,33
Mvaa 104,128,200
Ta 144,25,10304
Ta 146,25,15424
Ta 148,25,20544
Ta 150,25,25664
W 4
Zvaa 108,136,200
K 33,34,35
K 34,36,37
Mvaa 108,140,204
Ta 152,26,10336
K 35,38,39
Zvaa 100,140,188
buffer_store_dwordx4 v[32:35], v1, s[8:11], s93 offen offset:0 sc1
U 40,40,4
Mvaa 100,136,184
Ta 154,26,15456
Ta 156,26,20576
Ta 158,26,25696
W 4
Zvaa 112,144,184
U 41,41,4
U 42,42,4
Mvaa 112,148,188
U 43,43,4
U 44,44,4
Zvaa 120,148,204
D 18,40,94,2048
U 45,45,4
Mvaa 120,144,200
Ta 128,25,10368
Ta 130,25,15488
Ta 132,25,20608
Ta 134,25,25728
W 4
Zvaa 124,152,200
U 46,46,4
U 47,47,4
Mvaa 124,156,204
K 40,40,41
K 41,42,43
Zvaa 116,156,188
K 42,44,45
K 43,46,47
Mvaa 116,152,184
Ta 136,26,10400
Ta 138,26,15520
Ta 140,26,20640
Ta 142,26,25760
W 4
Zvaa 128,128,184
buffer_store_dwordx4 v[40:43], v1, s[8:11], s93 offen offset:1024 sc1
U 48,48,4
Mvaa 128,132,188
U 49,49,4
U 50,50,4
Zvaa 136,132,204
U 51,51,4
U 52,52,4
Mvaa 136,128,200
Ta 144,25,10432
Ta 146,25,15552
Ta 148,25,20672
Ta 150,25,25792
W 4
Zvaa 140,136,200
D 19,40,94,3072
U 53,53,4
Mvaa 140,140,204
s_add_u32 m0, m0, 4096
U 54,54,4
U 55,55,4
Zvaa 132,140,188
K 48,48,49
K 49,50,51
Mvaa 132,136,184
Ta 152,26,10464
Ta 154,26,15584
Ta 156,26,20704
Ta 158,26,25824
W 4
Zvaa 144,144,184
K 50,52,53
K 51,54,55
Mvaa 144,148,188
buffer_load_dwordx4 v20, s[40:43], s94 offen lds
Ta 128,25,10496
Zvaa 152,148,204
s_add_u32 m0, s71, s48
s_lshl2_add_u32 s94, s48, s95
Ta 130,25,15616
Mvaa 152,144,200
Ta 132,25,20736
Ta 134,25,25856
W 4
Zvaa 156,152,200
Ta 136,26,10528
Ta 138,26,15648
Mvaa 156,156,204
buffer_store_dwordx4 v[48:51], v1, s[8:11], s93 offen offset:2048 sc1
U 56,56,4
Zvaa 148,156,188
U 57,57,4
U 58,58,4
Mvaa 148,152,184
Ta 140,26,20768
Ta 142,26,25888
)"
R"(W 4
Zvaa 160,128,184
U 59,59,4
U 60,60,4
Mvaa 160,132,188
U 61,61,4
U 62,62,4
Zvaa 168,132,204
U 63,63,4
K 56,56,57
Mvaa 168,128,200
Ta 144,25,30720
Ta 146,25,35840
Ta 148,21,0
Ta 150,21,5120
W 4
Zvaa 172,136,200
D 16,40,94,0
K 57,58,59
Mvaa 172,140,204
K 58,60,61
K 59,62,63
Zvaa 164,140,188
Ta 152,26,30752
Ta 154,26,35872
Mvaa 164,136,184
Ta 156,22,32
Ta 158,22,5152
W 4
Mvaa 96,144,192
buffer_store_dwordx4 v[56:59], v1, s[8:11], s93 offen offset:3072 sc1
U 64,64,5
Mvaa 96,148,196
U 65,65,5
U 66,66,5
Mvaa 104,148,212
U 67,67,5
U 68,68,5
Mvaa 104,144,208
Ta 128,25,30784
Ta 130,25,35904
Ta 132,21,64
Ta 134,21,5184
W 4
Mvaa 108,152,208
D 17,40,94,1024
U 69,69,5
Mvaa 108,156,212
U 70,70,5
U 71,71,5
Mvaa 100,156,196
K 64,64,65
K 65,66,67
Mvaa 100,152,192
Ta 136,26,30816
Ta 138,26,35936
Ta 140,22,96
Ta 142,22,5216
W 4
Mvaa 112,128,192
K 66,68,69
K 67,70,71
Mvaa 112,132,196
s_add_u32 s67, s93, 131072
buffer_store_dwordx4 v[64:67], v1, s[8:11], s67 offen offset:0 sc1
U 72,72,5
Mvaa 120,132,212
U 73,73,5
U 74,74,5
Mvaa 120,128,208
Ta 144,25,30848
Ta 146,25,35968
Ta 148,21,128
Ta 150,21,5248
W 4
Mvaa 124,136,208
U 75,75,5
U 76,76,5
Mvaa 124,140,212
D 18,40,94,2048
U 77,77,5
Mvaa 116,140,196
U 78,78,5
U 79,79,5
Mvaa 116,136,192
Ta 152,26,30880
Ta 154,26,36000
Ta 156,22,160
Ta 158,22,5280
W 4
Mvaa 128,144,192
K 72,72,73
K 73,74,75
Mvaa 128,148,196
K 74,76,77
K 75,78,79
Mvaa 136,148,212
buffer_store_dwordx4 v[72:75], v1, s[8:11], s67 offen offset:1024 sc1
U 80,80,5
Mvaa 136,144,208
Ta 128,25,30912
Ta 130,25,36032
Ta 132,21,192
Ta 134,21,5312
W 4
Mvaa 140,152,208
D 19,40,94,3072
s_add_u32 m0, m0, 4096
s_nop 0
buffer_load_dwordx4 v20, s[40:43], s94 offen lds
Mvaa 140,156,212
s_add_u32 s81, s58, 2
s_and_b32 s81, s81, 3
s_lshl_b32 s81, s81, 12
Mvaa 132,156,196
s_add_u32 s81, s81, s49
U 81,81,5
U 82,82,5
Mvaa 132,152,192
Ta 136,26,30944
Ta 138,26,36064
Ta 140,22,224
Ta 142,22,5344
W 4
Mvaa 144,128,192
U 83,83,5
U 84,84,5
Mvaa 144,132,196
U 85,85,5
U 86,86,5
Mvaa 152,132,212
U 87,87,5
K 80,80,81
Mvaa 152,128,208
Ta 144,25,30976
Ta 146,25,36096
Ta 148,21,256
Ta 150,21,5376
W 4
Mvaa 156,136,208
K 81,82,83
K 82,84,85
Mvaa 156,140,212
buffer_load_dwordx4 a[0:3], v1, s[8:11], s81 offen offset:0 sc1
K 83,86,87
Mvaa 148,140,196
s_add_u32 s82, s81, 131072
Ta 152,26,31008
Ta 154,26,36128
Mvaa 148,136,192
Ta 156,22,288
Ta 158,22,5408
W 4
Mvaa 160,144,192
Ta 128,21,10240
Ta 130,21,15360
Mvaa 160,148,196
buffer_store_dwordx4 v[80:83], v1, s[8:11], s67 offen offset:2048 sc1
U 88,88,5
Mvaa 168,148,212
U 89,89,5
U 90,90,5
Mvaa 168,144,208
Ta 132,21,20480
Ta 134,21,25600
W 4
Mvaa 172,152,208
U 91,91,5
U 92,92,5
Mvaa 172,156,212
U 93,93,5
U 94,94,5
Mvaa 164,156,196
U 95,95,5
K 88,88,89
Mvaa 164,152,192
Ta 136,22,10272
Ta 138,22,15392
Ta 140,22,20512
Ta 142,22,25632
s_waitcnt vmcnt(22) lgkmcnt(4)
Mvaa 96,128,216
K 89,90,91
K 90,92,93
Mvaa 96,132,220
buffer_load_dwordx4 a[16:19], v1, s[8:11], s82 offen offset:0 sc1
K 91,94,95
Mvaa 104,132,236
Ta 144,21,10304
Ta 146,21,15424
Mvaa 104,128,232
Ta 148,21,20544
Ta 150,21,25664
W 4
Mvaa 108,136,232
Ta 152,22,10336
Ta 154,22,15456
Mvaa 108,140,236
buffer_store_dwordx4 v[88:91], v1, s[8:11], s67 offen offset:3072 sc1
Ta 156,22,20576
Mvaa 100,140,220
Ta 158,22,25696
Ta 128,21,10368
Mvaa 100,136,216
W 5
Ta 130,21,15488
Ta 132,21,20608
Mvaa 112,144,216
Ta 134,21,25728
Ta 136,22,10400
Mvaa 112,148,220
buffer_load_dwordx4 a[4:7], v1, s[8:11], s81 offen offset:1024 sc1
Ta 138,22,15520
Mvaa 120,148,236
Ta 140,22,20640
Ta 142,22,25760
Mvaa 120,144,232
W 8
Ta 144,21,10432
Ta 146,21,15552
Mvaa 124,152,232
Mvaa 124,156,236
buffer_load_dwordx4 a[20:23], v1, s[8:11], s82 offen offset:1024 sc1
Ta 148,21,20672
Mvaa 116,156,220
Ta 150,21,25792
Mvaa 116,152,216
W 8
Ta 152,22,10464
Ta 154,22,15584
Mvaa 128,128,216
Mvaa 128,132,220
buffer_load_dwordx4 a[8:11], v1, s[8:11], s81 offen offset:2048 sc1
Ta 156,22,20704
Mvaa 136,132,236
Ta 158,22,25824
Mvaa 136,128,232
W 8
Ta 128,21,10496
Ta 130,21,15616
Mvaa 140,136,232
s_waitcnt vmcnt(7)
s_barrier
Mvaa 140,140,236
buffer_load_dwordx4 a[24:27], v1, s[8:11], s82 offen offset:2048 sc1
Ta 132,21,20736
Mvaa 132,140,220
Ta 134,21,25856
Mvaa 132,136,216
W 8
Ta 136,22,10528
Ta 138,22,15648
Mvaa 144,144,216
Mvaa 144,148,220
buffer_load_dwordx4 a[12:15], v1, s[8:11], s81 offen offset:3072 sc1
Ta 140,22,20768
Mvaa 152,148,236
Ta 142,22,25888
Mvaa 152,144,232
W 8
Ta 144,21,30720
Ta 146,21,35840
Mvaa 156,152,232
Mvaa 156,156,236
buffer_load_dwordx4 a[28:31], v1, s[8:11], s82 offen offset:3072 sc1
Ta 148,23,0
Mvaa 148,156,220
s_lshl_b32 s92, s58, 2
s_add_u32 s91, s92, s51
s_add_u32 s95, s91, 11
Mvaa 148,152,216
W 7
s_and_b32 s95, s95, 15
s_mul_i32 s95, s95, 40960
Mvaa 160,128,216
s_mul_i32 s71, s51, 10240
s_add_u32 s71, s71, 40960
s_add_u32 m0, s71, s48
Mvaa 160,132,220
s_lshl2_add_u32 s94, s48, s95
D 16,40,94,0
Ta 150,23,5120
Mvaa 168,132,236
s_add_u32 s95, s91, 13
s_and_b32 s95, s95, 15
s_mul_i32 s95, s95, 40960
Mvaa 168,128,232
W 4
s_mul_i32 s71, s51, 20480
s_add_u32 s71, s71, 71680
Mvaa 172,136,232
Ta 152,22,30752
Ta 154,22,35872
Mvaa 172,140,236
D 17,40,94,1024
Ta 156,24,32
Mvaa 164,140,220
Ta 158,24,5152
Ta 128,21,30784
Mvaa 164,136,216
W 5
Ta 130,21,35904
Ta 132,23,64
Mvaa 96,144,224
Ta 134,23,5184
Ta 136,22,30816
Mvaa 96,148,228
D 18,40,94,2048
Ta 138,22,35936
Mvaa 104,148,244
Ta 140,24,96
Ta 142,24,5216
Mvaa 104,144,240
W 8
Ta 144,21,30848
Ta 146,21,35968
Mvaa 108,152,240
Mvaa 108,156,244
D 19,40,94,3072
Ta 148,23,128
Mvaa 100,156,228
s_add_u32 m0, m0, 4096
Ta 150,23,5248
Mvaa 100,152,224
W 8
Ta 152,22,30880
Ta 154,22,36000
Mvaa 112,128,224
Mvaa 112,132,228
buffer_load_dwordx4 v20, s[40:43], s94 offen lds
Ta 156,24,160
Mvaa 120,132,244
s_add_u32 m0, s71, s48
s_lshl2_add_u32 s94, s48, s95
Ta 158,24,5280
Mvaa 120,128,240
W 8
Ta 128,21,30912
Ta 130,21,36032
Mvaa 124,136,240
Mvaa 124,140,244
D 16,40,94,0
Ta 132,23,192
Mvaa 116,140,228
s_add_u32 s95, s92, 15
s_and_b32 s95, s95, 15
s_mul_i32 s95, s95, 40960
Mvaa 116,136,224
W 7
s_mul_i32 s71, s51, 0
s_add_u32 s71, s71, 102400
Mvaa 128,144,224
Ta 134,23,5312
Ta 136,22,30944
Mvaa 128,148,228
D 17,40,94,1024
Ta 138,22,36064
Mvaa 136,148,244
Ta 140,24,224
Ta 142,24,5344
Mvaa 136,144,240
W 8
Ta 144,21,30976
Ta 146,21,36096
Mvaa 140,152,240
Mvaa 140,156,244
D 18,40,94,2048
Ta 148,23,256
Mvaa 132,156,228
Ta 150,23,5376
Mvaa 132,152,224
W 8
Ta 152,22,31008
Ta 154,22,36128
Mvaa 144,128,224
s_lshl_b32 s80, s58, 3
s_add_u32 s80, s80, s55
Mvaa 144,132,228
D 19,40,94,3072
Ta 156,24,288
Mvaa 152,132,244
s_add_u32 m0, m0, 4096
Ta 158,24,5408
Mvaa 152,128,240
W 8
Ta 128,23,20480
Ta 130,23,25600
Mvaa 156,136,240
s_waitcnt vmcnt(15)
v_mov_b64 v[28:29], s[12:13]
s_mov_b64 exec, 1
buffer_store_dwordx2 v[28:29], off, s[8:11], s80 sc1
s_mov_b64 exec, -1
Mvaa 156,140,244
Ta 132,23,51200
Ta 134,23,56320
Mvaa 148,140,228
Mvaa 148,136,224
W 8
Ta 136,24,20512
)"
R"(Ta 138,24,25632
Mvaa 160,144,224
buffer_load_dwordx4 v20, s[40:43], s94 offen lds
Mvaa 160,148,228
s_add_u32 m0, s71, s48
s_lshl2_add_u32 s94, s48, s95
Ta 140,24,51232
Mvaa 168,148,244
Ta 142,24,56352
Mvaa 168,144,240
W 8
Ta 144,23,20544
Ta 146,23,25664
Mvaa 172,152,240
D 16,40,94,0
Mvaa 172,156,244
Ta 148,23,51264
Ta 150,23,56384
Mvaa 164,156,228
Mvaa 164,152,224
s_waitcnt vmcnt(16) lgkmcnt(8)
Ta 152,24,20576
Ta 154,24,25696
Mvaa 96,128,0
D 17,40,94,1024
Mvaa 96,132,4
Ta 156,24,51296
Ta 158,24,56416
Mvaa 104,132,20
Mvaa 104,128,16
W 8
Ta 128,23,20608
Ta 130,23,25728
Mvaa 108,136,16
D 18,40,94,2048
Mvaa 108,140,20
Ta 132,23,51328
Ta 134,23,56448
Mvaa 100,140,4
Mvaa 100,136,0
W 8
Ta 136,24,20640
Ta 138,24,25760
Mvaa 112,144,0
D 19,40,94,3072
Mvaa 112,148,4
s_add_u32 m0, m0, 4096
Ta 140,24,51360
Ta 142,24,56480
Mvaa 120,148,20
Mvaa 120,144,16
W 8
Ta 144,23,20672
Ta 146,23,25792
Mvaa 124,152,16
buffer_load_dwordx4 v20, s[40:43], s94 offen lds
Mvaa 124,156,20
s_add_u32 s81, s58, 3
s_and_b32 s81, s81, 3
s_lshl_b32 s81, s81, 12
Mvaa 116,156,4
s_add_u32 s81, s81, s49
Ta 148,23,51392
Ta 150,23,56512
Mvaa 116,152,0
W 8
Ta 152,24,20704
Ta 154,24,25824
Mvaa 128,128,0
buffer_load_dwordx4 a[32:35], v1, s[8:11], s81 offen offset:0 sc1
Mvaa 128,132,4
s_add_u32 s82, s81, 131072
Ta 156,24,51424
Ta 158,24,56544
Mvaa 136,132,20
Mvaa 136,128,16
W 8
Ta 128,23,20736
Ta 130,23,25856
Mvaa 140,136,16
s_waitcnt vmcnt(12)
s_barrier
buffer_load_dwordx4 a[48:51], v1, s[8:11], s82 offen offset:0 sc1
Mvaa 140,140,20
Ta 132,23,51456
v_and_b32 v27, 1, v12
Mvaa 132,140,4
Ta 134,23,56576
Mvaa 132,136,0
W 8
Ta 136,24,20768
v_lshlrev_b32 v27, 1, v27
Mvaa 144,144,0
buffer_load_dwordx4 a[36:39], v1, s[8:11], s81 offen offset:1024 sc1
Ta 138,24,25888
Mvaa 144,148,4
Ta 140,24,51488
Ta 142,24,56608
Mvaa 152,148,20
v_xor_b32 v27, v27, v13
Mvaa 152,144,16
W 8
Ta 144,25,0
Ta 146,25,5120
Mvaa 156,152,16
buffer_load_dwordx4 a[52:55], v1, s[8:11], s82 offen offset:1024 sc1
v_and_b32 v12, 2, v12
Mvaa 156,156,20
Ta 148,21,40960
Ta 150,21,46080
Mvaa 148,156,4
Mvaa 148,152,0
W 8
Ta 152,26,32
v_or_b32 v12, v12, v14
Mvaa 160,128,0
buffer_load_dwordx4 a[40:43], v1, s[8:11], s81 offen offset:2048 sc1
Ta 154,26,5152
Mvaa 160,132,4
Ta 156,22,40992
v_lshl_or_b32 v12, v27, 2, v12
Mvaa 168,132,20
Ta 158,22,46112
Mvaa 168,128,16
W 8
Ta 128,25,64
Ta 130,25,5184
Mvaa 172,136,16
buffer_load_dwordx4 a[56:59], v1, s[8:11], s82 offen offset:2048 sc1
v_cmp_eq_u32_e64 s[86:87], 0, v12
Mvaa 172,140,20
Ta 132,21,41024
Ta 134,21,46144
Mvaa 164,140,4
Mvaa 164,136,0
W 8
Ta 136,26,96
v_cndmask_b32_e64 v4, 0, v15, s[86:87]
Mvaa 96,144,8
buffer_load_dwordx4 a[44:47], v1, s[8:11], s81 offen offset:3072 sc1
Ta 138,26,5216
Mvaa 96,148,12
Ta 140,22,41056
v_cmp_eq_u32_e64 s[86:87], 1, v12
Mvaa 104,148,28
Ta 142,22,46176
Mvaa 104,144,24
W 8
Ta 144,25,128
Ta 146,25,5248
Mvaa 108,152,24
buffer_load_dwordx4 a[60:63], v1, s[8:11], s82 offen offset:3072 sc1
v_cndmask_b32_e64 v5, 0, v15, s[86:87]
Mvaa 108,156,28
Ta 148,21,41088
Ta 150,21,46208
Mvaa 100,156,12
v_cmp_eq_u32_e64 s[86:87], 2, v12
Mvaa 100,152,8
W 8
Ta 152,26,160
Ta 154,26,5280
Mvaa 112,128,8
Mvaa 112,132,12
Ta 156,22,41120
v_cndmask_b32_e64 v6, 0, v15, s[86:87]
Mvaa 120,132,28
Ta 158,22,46240
Mvaa 120,128,24
W 8
Ta 128,25,192
Ta 130,25,5312
Mvaa 124,136,24
v_cmp_eq_u32_e64 s[86:87], 3, v12
Mvaa 124,140,28
Ta 132,21,41152
Ta 134,21,46272
Mvaa 116,140,12
v_cndmask_b32_e64 v7, 0, v15, s[86:87]
Mvaa 116,136,8
W 8
Ta 136,26,224
Ta 138,26,5344
Mvaa 128,144,8
Mvaa 128,148,12
Ta 140,22,41184
v_cmp_eq_u32_e64 s[86:87], 4, v12
Mvaa 136,148,28
Ta 142,22,46304
Mvaa 136,144,24
W 8
Ta 144,25,256
v_cndmask_b32_e64 v8, 0, v15, s[86:87]
Mvaa 140,152,24
s_waitcnt vmcnt(8)
s_barrier
Ta 146,25,5376
Mvaa 140,156,28
Ta 148,21,41216
Ta 150,21,46336
Mvaa 132,156,12
v_cmp_eq_u32_e64 s[86:87], 5, v12
Mvaa 132,152,8
W 8
Ta 152,26,288
Ta 154,26,5408
Mvaa 144,128,8
Mvaa 144,132,12
Ta 156,22,41248
v_cndmask_b32_e64 v9, 0, v15, s[86:87]
Mvaa 152,132,28
Ta 158,22,46368
Mvaa 152,128,24
W 8
Ta 128,21,51200
v_cmp_eq_u32_e64 s[86:87], 6, v12
Mvaa 156,136,24
Ta 130,21,56320
Mvaa 156,140,28
Ta 132,23,10240
Ta 134,23,15360
Mvaa 148,140,12
v_cndmask_b32_e64 v10, 0, v15, s[86:87]
Mvaa 148,136,8
W 8
Ta 136,22,51232
Ta 138,22,56352
Mvaa 160,144,8
v_cmp_eq_u32_e64 s[86:87], 7, v12
v_bfe_u32 v28, v1, 4, 2
Mvaa 160,148,12
Ta 140,24,10272
v_lshlrev_b32 v28, 3, v28
Mvaa 168,148,28
Ta 142,24,15392
buffer_load_dwordx2 v[28:29], v28, s[8:11], s55 offen sc1
Mvaa 168,144,24
W 8
Ta 144,21,51264
v_cndmask_b32_e64 v11, 0, v15, s[86:87]
Mvaa 172,152,24
Ta 146,21,56384
Mvaa 172,156,28
Ta 148,23,10304
Ta 150,23,15424
Mvaa 164,156,12
s_mov_b32 s20, s38
Mvaa 164,152,8
s_waitcnt vmcnt(5) lgkmcnt(8)
Ta 152,22,51296
Ta 154,22,56416
Mvaa 96,128,32
s_and_b32 s21, s39, 0xffff
Mvaa 96,132,36
Ta 156,24,10336
Ta 158,24,15456
Mvaa 104,132,52
Mvaa 104,128,48
W 8
s_mov_b32 s22, 2621440
Ta 128,21,51328
Mvaa 108,136,48
Ta 130,21,56448
Mvaa 108,140,52
s_mov_b32 s23, 0x20000
Ta 132,23,10368
Ta 134,23,15488
Mvaa 100,140,36
Mvaa 100,136,32
W 8
Ta 136,22,51360
Ta 138,22,56480
Mvaa 112,144,32
s_and_b32 s17, s17, 0xffff
Mvaa 112,148,36
Ta 140,24,10400
Ta 142,24,15520
Mvaa 120,148,52
Mvaa 120,144,48
W 8
s_mov_b32 s18, 2621440
Ta 144,21,51392
Mvaa 124,152,48
Ta 146,21,56512
Mvaa 124,156,52
s_mov_b32 s19, 0x20000
Ta 148,23,10432
Ta 150,23,15552
Mvaa 116,156,36
Mvaa 116,152,32
W 8
Ta 152,22,51424
Ta 154,22,56544
Mvaa 128,128,32
s_lshr_b32 s66, s15, 3
Mvaa 128,132,36
Ta 156,24,10464
Ta 158,24,15584
Mvaa 136,132,52
s_lshl_b32 s66, s66, 4
Mvaa 136,128,48
W 8
Ta 128,21,51456
Ta 130,21,56576
Mvaa 140,136,48
Mvaa 140,140,52
s_add_u32 s66, s66, s59
Ta 132,23,10496
Ta 134,23,15616
Mvaa 132,140,36
Mvaa 132,136,32
W 8
Ta 136,22,51488
Ta 138,22,56608
Mvaa 144,144,32
s_lshl_b32 s63, s66, 6
Mvaa 144,148,36
Ta 140,24,10528
Ta 142,24,15648
Mvaa 152,148,52
s_add_u32 s63, s63, 65536
Mvaa 152,144,48
W 8
Ta 144,23,30720
Ta 146,23,35840
Mvaa 156,152,48
Mvaa 156,156,52
s_mul_i32 s62, s66, 40960
Ta 148,23,40960
Ta 150,23,46080
Mvaa 148,156,36
Mvaa 148,152,32
W 8
s_add_u32 s62, s62, 56623104
Ta 152,24,30752
Mvaa 160,128,32
Ta 154,24,35872
Mvaa 160,132,36
Ta 156,24,40992
Ta 158,24,46112
Mvaa 168,132,52
s_lshl_b32 s66, s14, 13
s_waitcnt vmcnt(0)
v_cmp_eq_u64 vcc, s[12:13], v[28:29]
s_cmp_eq_u64 vcc, -1
s_cbranch_scc0 .Lspin_84%=
.Lok_85%=:
s_add_u32 s81, s58, 1
Mvaa 168,128,48
W 8
s_and_b32 s81, s81, 3
s_lshl_b32 s81, s81, 12
Mvaa 172,136,48
s_add_u32 s81, s81, s50
buffer_load_dwordx4 a[64:67], v1, s[8:11], s81 offen offset:0 sc1
Ta 128,23,30784
Mvaa 172,140,52
s_add_u32 s82, s81, 131072
s_add_u32 s78, s62, s66
Ta 130,23,35904
Mvaa 164,140,36
Ta 132,23,41024
Ta 134,23,46144
Mvaa 164,136,32
W 8
s_add_u32 s79, s78, 327680
Ta 136,24,30816
Mvaa 96,144,40
Ta 138,24,35936
buffer_load_dwordx4 a[80:83], v1, s[8:11], s82 offen offset:0 sc1
Mvaa 96,148,44
Ta 140,24,41056
Ta 142,24,46176
Mvaa 104,148,60
s_and_b32 s66, s14, 1
Mvaa 104,144,56
W 8
Ta 144,23,30848
Ta 146,23,35968
Mvaa 108,152,56
s_mul_i32 s66, s66, 327680
Mvaa 108,156,60
buffer_load_dwordx4 a[68:71], v1, s[8:11], s81 offen offset:1024 sc1
Ta 148,23,41088
Mvaa 100,156,44
Ta 150,23,46208
Mvaa 100,152,40
W 8
s_add_u32 s65, s62, s66
Ta 152,24,30880
Mvaa 112,128,40
Ta 154,24,36000
Mvaa 112,132,44
Ta 156,24,41120
Ta 158,24,46240
Mvaa 120,132,60
s_add_u32 s65, s65, 32768
buffer_load_dwordx4 a[84:87], v1, s[8:11], s82 offen offset:1024 sc1
Mvaa 120,128,56
W 8
)"
R"(Ta 128,23,30912
Ta 130,23,36032
Mvaa 124,136,56
s_mov_b32 s64, 0
Mvaa 124,140,60
Ta 132,23,41152
Ta 134,23,46272
Mvaa 116,140,44
Mvaa 116,136,40
W 8
buffer_load_dwordx4 a[72:75], v1, s[8:11], s81 offen offset:2048 sc1
v_add_u32 v19, 4096, v1
Mvaa 128,144,40
Ta 136,24,30944
Ta 138,24,36064
Mvaa 128,148,44
Ta 140,24,41184
v_bfe_u32 v27, v1, 4, 3
Mvaa 136,148,60
Ta 142,24,46304
Mvaa 136,144,56
W 8
Ta 144,23,30976
Ta 146,23,36096
Mvaa 140,152,56
buffer_load_dwordx4 a[88:91], v1, s[8:11], s82 offen offset:2048 sc1
v_lshlrev_b32 v27, 3, v27
Mvaa 140,156,60
Ta 148,23,41216
Ta 150,23,46336
Mvaa 132,156,44
v_bfe_u32 v18, v1, 7, 1
Mvaa 132,152,40
W 8
Ta 152,24,31008
Ta 154,24,36128
Mvaa 144,128,40
Mvaa 144,132,44
buffer_load_dwordx4 a[76:79], v1, s[8:11], s81 offen offset:3072 sc1
v_lshl_add_u32 v18, v18, 9, v27
Mvaa 152,132,60
Ta 156,24,41248
Ta 158,24,46368
Mvaa 152,128,56
W 8
Ta 128,25,10240
Ta 130,25,15360
Mvaa 156,136,56
s_mov_b32 s71, 0
Mvaa 156,140,60
Ta 132,25,20480
Ta 134,25,25600
Mvaa 148,140,44
buffer_load_dwordx4 a[92:95], v1, s[8:11], s82 offen offset:3072 sc1
v_bfe_u32 v20, v1, 4, 4
Mvaa 148,136,40
W 8
s_add_u32 s81, s58, 2
s_and_b32 s81, s81, 3
Mvaa 160,144,40
s_lshl_b32 s81, s81, 12
s_add_u32 s81, s81, s50
K 96,96,97
Mvaa 160,148,44
Ta 136,26,10272
Ta 138,26,15392
Mvaa 168,148,60
K 97,98,99
v_mul_u32_u24 v20, 1280, v20
Mvaa 168,144,56
W 6
Ta 140,26,20512
Ta 142,26,25632
Mvaa 172,152,56
buffer_load_dwordx4 a[96:99], v1, s[8:11], s81 offen offset:0 sc1
K 98,100,101
Mvaa 172,156,60
s_add_u32 s82, s81, 131072
Ta 144,25,10304
v_lshrrev_b32 v12, 8, v1
Mvaa 164,156,44
Ta 146,25,15424
Ta 148,25,20544
Mvaa 164,152,40
W 7
Ta 150,25,25664
K 99,102,103
Zvav 176,128,32
s_add_u32 s83, s52, 0
Ta 152,26,10336
v_lshl_add_u32 v20, v12, 4, v20
Mvav 176,132,40
s_bfe_u32 s66, s59, 0x10001
Ta 154,26,15456
buffer_load_dwordx4 a[112:115], v1, s[8:11], s82 offen offset:0 sc1
Zvav 184,132,72
Ta 156,26,20576
Ta 158,26,25696
Mvav 184,128,64
W 8
s_lshl_b32 s67, s57, 1
Ta 128,25,10368
Zvav 188,136,64
Ta 130,25,15488
Mvav 188,140,72
buffer_store_dwordx4 v[96:99], v1, s[8:11], s83 offen sc1
K 104,104,105
Zvav 180,140,40
s_add_u32 s66, s66, s67
Ta 132,25,20608
Ta 134,25,25728
Mvav 180,136,32
W 8
Ta 136,26,10400
K 105,106,107
Zvav 192,144,32
s_lshl_b32 s61, s66, 2
Ta 138,26,15520
Mvav 192,148,40
buffer_load_dwordx4 a[100:103], v1, s[8:11], s81 offen offset:1024 sc1
K 106,108,109
Zvav 200,148,72
Ta 140,26,20640
Ta 142,26,25760
Mvav 200,144,64
W 8
s_lshr_b32 s66, s59, 2
Ta 144,25,10432
Zvav 204,152,64
Ta 146,25,15552
K 107,110,111
Mvav 204,156,72
buffer_load_dwordx4 a[116:119], v1, s[8:11], s82 offen offset:1024 sc1
Ta 148,25,20672
Zvav 196,156,40
s_lshl_b32 s66, s66, 1
s_add_u32 s83, s52, 40960
Ta 150,25,25792
Mvav 196,152,32
W 8
Ta 152,26,10464
Ta 154,26,15584
Zvav 208,128,32
s_add_u32 s61, s61, s66
Mvav 208,132,40
buffer_load_dwordx4 a[104:107], v1, s[8:11], s81 offen offset:2048 sc1
Ta 156,26,20704
Zvav 216,132,72
Ta 158,26,25824
Mvav 216,128,64
W 8
s_and_b32 s66, s59, 1
Ta 128,25,10496
Zvav 220,136,64
Ta 130,25,15616
Mvav 220,140,72
s_add_u32 s61, s61, s66
buffer_store_dwordx4 v[104:107], v1, s[8:11], s83 offen sc1
K 112,112,113
Zvav 212,140,40
Ta 132,25,20736
K 113,114,115
Mvav 212,136,32
W 7
Ta 134,25,25856
K 114,116,117
Zvav 224,144,32
s_xor_b32 s67, s61, 0
Ta 136,26,10528
Ta 138,26,15648
Mvav 224,148,40
buffer_load_dwordx4 a[120:123], v1, s[8:11], s82 offen offset:2048 sc1
Ta 140,26,20768
Zvav 232,148,72
Ta 142,26,25888
K 115,118,119
Mvav 232,144,64
W 8
s_mul_i32 s72, s67, 20480
Ta 144,25,30720
Zvav 236,152,64
s_add_u32 s83, s52, 8192
Ta 146,25,35840
Mvav 236,156,72
s_mul_i32 s66, s58, 320
buffer_load_dwordx4 a[108:111], v1, s[8:11], s81 offen offset:3072 sc1
Ta 148,21,0
Zvav 228,156,40
Ta 150,21,5120
Mvav 228,152,32
W 8
Ta 152,26,30752
Ta 154,26,35872
Zvav 240,128,32
s_add_u32 s72, s72, s66
Mvav 240,132,40
buffer_store_dwordx4 v[112:115], v1, s[8:11], s83 offen sc1
K 120,120,121
Zvav 248,132,72
s_mul_i32 s75, s67, 20480
Ta 156,22,32
Ta 158,22,5152
Mvav 248,128,64
W 8
Ta 128,25,30784
K 121,122,123
Zvav 252,136,64
Ta 130,25,35904
Mvav 252,140,72
s_mul_i32 s66, s58, 2560
buffer_load_dwordx4 a[124:127], v1, s[8:11], s82 offen offset:3072 sc1
K 122,124,125
Zvav 244,140,40
s_add_u32 s81, s58, 3
s_and_b32 s81, s81, 3
s_lshl_b32 s81, s81, 12
Mvav 244,136,32
W 6
s_add_u32 s81, s81, s50
Ta 132,21,64
Mvav 176,144,48
s_add_u32 s75, s75, s66
Ta 134,21,5184
Ta 136,26,30816
Mvav 176,148,56
buffer_load_dwordx4 a[184:187], v1, s[8:11], s81 offen offset:0 sc1
Ta 138,26,35936
Mvav 184,148,88
s_add_u32 s82, s81, 131072
Ta 140,22,96
K 123,126,127
Mvav 184,144,80
W 7
s_add_u32 s83, s52, 49152
Ta 142,22,5216
Mvav 188,152,80
s_add_u32 s75, s75, 131072
Ta 144,25,30848
Ta 146,25,35968
Mvav 188,156,88
s_xor_b32 s67, s61, 124
buffer_load_dwordx4 a[200:203], v1, s[8:11], s82 offen offset:0 sc1
Ta 148,21,128
Mvav 180,156,56
Ta 150,21,5248
Mvav 180,152,48
W 8
s_mul_i32 s73, s67, 20480
Ta 152,26,30880
Mvav 192,128,48
Ta 154,26,36000
Mvav 192,132,56
buffer_store_dwordx4 v[120:123], v1, s[8:11], s83 offen sc1
K 128,128,129
Mvav 200,132,88
s_mul_i32 s66, s58, 320
Ta 156,22,160
K 129,130,131
Mvav 200,128,80
W 7
Ta 158,22,5280
K 130,132,133
Mvav 204,136,80
Ta 128,25,30912
Ta 130,25,36032
Mvav 204,140,88
s_add_u32 s73, s73, s66
buffer_load_dwordx4 a[188:191], v1, s[8:11], s81 offen offset:1024 sc1
Ta 132,21,192
Mvav 196,140,56
Ta 134,21,5312
K 131,134,135
Mvav 196,136,48
W 8
s_mul_i32 s76, s67, 20480
Ta 136,26,30944
Mvav 208,144,48
s_add_u32 s83, s52, 16384
Ta 138,26,36064
Mvav 208,148,56
buffer_load_dwordx4 a[204:207], v1, s[8:11], s82 offen offset:1024 sc1
Ta 140,22,224
Mvav 216,148,88
s_mul_i32 s66, s58, 2560
Ta 142,22,5344
Mvav 216,144,80
W 8
Ta 144,25,30976
Ta 146,25,36096
Mvav 220,152,80
s_add_u32 s76, s76, s66
Mvav 220,156,88
buffer_load_dwordx4 a[192:195], v1, s[8:11], s81 offen offset:2048 sc1
Ta 148,21,256
Mvav 212,156,56
Ta 150,21,5376
Mvav 212,152,48
W 8
s_add_u32 s76, s76, 131072
Ta 152,26,31008
Mvav 224,128,48
Ta 154,26,36128
Mvav 224,132,56
buffer_store_dwordx4 v[128:131], v1, s[8:11], s83 offen sc1
K 136,136,137
Mvav 232,132,88
K 137,138,139
K 138,140,141
Mvav 232,128,80
W 6
Ta 156,22,288
Ta 158,22,5408
Mvav 236,136,80
Ta 128,21,10240
v_bfe_u32 v15, v1, 7, 1
Mvav 236,140,88
buffer_load_dwordx4 a[208:211], v1, s[8:11], s82 offen offset:2048 sc1
Ta 130,21,15360
Mvav 228,140,56
K 139,142,143
v_mul_u32_u24 v14, 10240, v15
Mvav 228,136,48
W 6
Ta 132,21,20480
Ta 134,21,25600
Mvav 240,144,48
s_add_u32 s83, s52, 57344
Ta 136,22,10272
v_lshrrev_b32 v15, 8, v1
Mvav 240,148,56
buffer_load_dwordx4 a[196:199], v1, s[8:11], s81 offen offset:3072 sc1
Ta 138,22,15392
Mvav 248,148,88
Ta 140,22,20512
v_lshl_add_u32 v14, v15, 7, v14
Mvav 248,144,80
W 7
Ta 142,22,25632
Ta 144,21,10304
Mvav 252,152,80
Ta 146,21,15424
v_and_b32 v15, 0x70, v1
Mvav 252,156,88
buffer_load_dwordx4 a[212:215], v1, s[8:11], s82 offen offset:3072 sc1
Ta 148,21,20544
Mvav 244,156,56
Ta 150,21,25664
Mvav 244,152,48
s_waitcnt vmcnt(25) lgkmcnt(8)
Ta 152,22,10336
v_add_u32 v14, v14, v15
Mvaa 176,128,64
Ta 154,22,15456
Mvaa 176,132,68
s_bitcmp1_b32 s14, 0
s_cselect_b32 s74, s73, s72
s_cselect_b32 s77, s76, s75
buffer_store_dwordx4 v[136:139], v1, s[8:11], s83 offen sc1
K 144,144,145
Mvaa 184,132,84
K 145,146,147
K 146,148,149
Mvaa 184,128,80
W 6
Ta 156,22,20576
Ta 158,22,25696
Mvaa 188,136,80
s_add_u32 s74, s74, 256
Ta 128,21,10368
K 147,150,151
Mvaa 188,140,84
s_add_u32 s83, s52, 24576
Ta 130,21,15488
Ta 132,21,20608
Mvaa 180,140,68
s_add_u32 s77, s77, 2048
Ta 134,21,25728
buffer_store_dwordx4 v[144:147], v1, s[8:11], s83 offen sc1
Mvaa 180,136,64
W 8
Ta 136,22,10400
Ta 138,22,15520
Mvaa 192,144,64
K 152,152,153
Mvaa 192,148,68
s_lshl_b32 s66, s14, 6
Ta 140,22,20640
Ta 142,22,25760
Mvaa 200,148,84
Mvaa 200,144,80
W 8
Ta 144,21,10432
K 153,154,155
Mvaa 204,152,80
s_lshl_b32 s67, s14, 9
Ta 146,21,15552
Mvaa 204,156,84
Ta 148,21,20672
K 154,156,157
Mvaa 196,156,68
)"
R"(s_add_u32 s72, s72, s66
Ta 150,21,25792
Mvaa 196,152,64
W 8
Ta 152,22,10464
Ta 154,22,15584
Mvaa 208,128,64
K 155,158,159
Mvaa 208,132,68
s_add_u32 s75, s75, s67
Ta 156,22,20704
Ta 158,22,25824
Mvaa 216,132,84
s_add_u32 s83, s52, 65536
Mvaa 216,128,80
W 8
s_add_u32 s73, s73, s66
Ta 128,21,10496
Mvaa 220,136,80
Ta 130,21,15616
buffer_store_dwordx4 v[152:155], v1, s[8:11], s83 offen sc1
Mvaa 220,140,84
Ta 132,21,20736
Ta 134,21,25856
Mvaa 212,140,68
s_add_u32 s76, s76, s67
K 160,160,161
Mvaa 212,136,64
W 8
Ta 136,22,10528
Ta 138,22,15648
Mvaa 224,144,64
buffer_load_dwordx4 v[36:39], v20, s[16:19], s72 offen
Mvaa 224,148,68
Ta 140,22,20768
K 161,162,163
Mvaa 232,148,84
Ta 142,22,25888
Mvaa 232,144,80
W 8
Ta 144,21,30720
K 162,164,165
Mvaa 236,152,80
Ta 146,21,35840
Mvaa 236,156,84
Ta 148,23,0
Ta 150,23,5120
Mvaa 228,156,68
K 163,166,167
Mvaa 228,152,64
W 8
buffer_load_dwordx4 v[44:47], v14, s[8:11], s75 offen sc1
Ta 152,22,30752
Mvaa 240,128,64
s_add_u32 s83, s52, 32768
Ta 154,22,35872
Mvaa 240,132,68
Ta 156,24,32
Ta 158,24,5152
Mvaa 248,132,84
Mvaa 248,128,80
W 8
buffer_store_dwordx4 v[160:163], v1, s[8:11], s83 offen sc1
Ta 128,21,30784
Mvaa 252,136,80
Ta 130,21,35904
K 168,168,169
Mvaa 252,140,84
Ta 132,23,64
Ta 134,23,5184
Mvaa 244,140,68
Mvaa 244,136,64
s_waitcnt vmcnt(27) lgkmcnt(8)
buffer_load_dwordx4 v[52:55], v20, s[16:19], s73 offen
K 169,170,171
Mvaa 176,144,72
Ta 136,22,30816
Ta 138,22,35936
Mvaa 176,148,76
Ta 140,24,96
K 170,172,173
Mvaa 184,148,92
Ta 142,24,5216
Mvaa 184,144,88
W 8
Ta 144,21,30848
Ta 146,21,35968
Mvaa 188,152,88
buffer_load_dwordx4 v[60:63], v14, s[8:11], s76 offen sc1
K 171,174,175
Mvaa 188,156,92
Ta 148,23,128
Ta 150,23,5248
Mvaa 180,156,76
s_add_u32 s83, s52, 73728
Mvaa 180,152,72
W 8
Ta 152,22,30880
Ta 154,22,36000
Mvaa 192,128,72
buffer_store_dwordx4 v[168:171], v1, s[8:11], s83 offen sc1
Mvaa 192,132,76
Ta 156,24,160
Ta 158,24,5280
Mvaa 200,132,92
Mvaa 200,128,88
W 8
Ta 128,21,30912
Ta 130,21,36032
Mvaa 204,136,88
buffer_load_dwordx4 v[68:71], v20, s[16:19], s74 offen
Mvaa 204,140,92
Ta 132,23,192
Ta 134,23,5312
Mvaa 196,140,76
Mvaa 196,136,72
W 8
Ta 136,22,30944
Ta 138,22,36064
Mvaa 208,144,72
Mvaa 208,148,76
Ta 140,24,224
Ta 142,24,5344
Mvaa 216,148,92
buffer_load_dwordx4 v[76:79], v14, s[8:11], s77 offen sc1
Mvaa 216,144,88
W 8
Ta 144,21,30976
Ta 146,21,36096
Mvaa 220,152,88
Mvaa 220,156,92
Ta 148,23,256
Ta 150,23,5376
Mvaa 212,156,76
Mvaa 212,152,72
W 8
Ta 152,22,31008
Ta 154,22,36128
Mvaa 224,128,72
buffer_load_dwordx2 v[12:13], v27, s[8:11], 0 offen offset:3584 sc1
Mvaa 224,132,76
Ta 156,24,288
Ta 158,24,5408
Mvaa 232,132,92
Mvaa 232,128,88
W 8
Ta 128,23,20480
Ta 130,23,25600
Mvaa 236,136,88
Mvaa 236,140,92
Ta 132,23,51200
Ta 134,23,56320
Mvaa 228,140,76
Mvaa 228,136,72
W 8
Ta 136,25,0
Ta 138,25,5120
Mvaa 240,144,72
Mvaa 240,148,76
Ta 140,21,40960
Ta 142,21,46080
Mvaa 248,148,92
Mvaa 248,144,88
W 8
Ta 144,21,51200
Ta 146,21,56320
Mvaa 252,152,88
Mvaa 252,156,92
Ta 148,23,10240
Ta 150,23,15360
Mvaa 244,156,76
Mvaa 244,152,72
s_waitcnt vmcnt(28) lgkmcnt(8)
Ta 152,23,30720
Ta 154,23,35840
Mvaa 176,128,96
Mvaa 176,132,100
Ta 156,23,40960
Ta 158,23,46080
Mvaa 184,132,116
Mvaa 184,128,112
s_waitcnt vmcnt(22) lgkmcnt(8)
Ta 128,24,20512
Ta 130,24,25632
Mvaa 184,136,120
Mvaa 184,140,124
Ta 132,24,51232
Ta 134,24,56352
Mvaa 176,140,108
Mvaa 176,136,104
s_waitcnt vmcnt(17) lgkmcnt(8)
Ta 136,26,32
Ta 138,26,5152
Mvaa 176,144,184
Mvaa 176,148,188
Ta 140,22,40992
Ta 142,22,46112
Mvaa 184,148,204
Mvaa 184,144,200
s_waitcnt vmcnt(12) lgkmcnt(8)
Ta 144,22,51232
Ta 146,22,56352
Mvaa 184,152,208
Mvaa 184,156,212
Ta 148,24,10272
Ta 150,24,15392
Mvaa 176,156,196
Mvaa 176,152,192
W 8
Ta 152,24,30752
Ta 154,24,35872
Mvaa 180,128,96
Mvaa 180,132,100
Ta 156,24,40992
Ta 158,24,46112
Mvaa 188,132,116
Mvaa 188,128,112
W 8
Ta 128,23,20544
Ta 130,23,25664
Mvaa 188,136,120
Mvaa 188,140,124
Ta 132,23,51264
Ta 134,23,56384
Mvaa 180,140,108
Mvaa 180,136,104
W 8
Ta 136,25,64
Ta 138,25,5184
Mvaa 180,144,184
Mvaa 180,148,188
Ta 140,21,41024
Ta 142,21,46144
Mvaa 188,148,204
Mvaa 188,144,200
W 8
Ta 144,21,51264
Ta 146,21,56384
Mvaa 188,152,208
Mvaa 188,156,212
Ta 148,23,10304
Ta 150,23,15424
Mvaa 180,156,196
Mvaa 180,152,192
W 8
Ta 152,23,30784
Ta 154,23,35904
Mvaa 192,128,96
Mvaa 192,132,100
Ta 156,23,41024
Ta 158,23,46144
Mvaa 200,132,116
K 176,176,177
Mvaa 200,128,112
W 8
Ta 128,24,20576
Ta 130,24,25696
Mvaa 200,136,120
K 177,178,179
Mvaa 200,140,124
Ta 132,24,51296
K 178,180,181
Mvaa 192,140,108
Ta 134,24,56416
K 179,182,183
Mvaa 192,136,104
W 8
Ta 136,26,96
Ta 138,26,5216
Mvaa 192,144,184
s_add_u32 s83, s52, 327680
Mvaa 192,148,188
buffer_store_dwordx4 v[176:179], v1, s[8:11], s83 offen sc1
Ta 140,22,41056
Mvaa 200,148,204
Ta 142,22,46176
Mvaa 200,144,200
s_waitcnt vmcnt(4)
v_lshlrev_b32 v2, 2, v1
v_mov_b64 v[28:29], s[12:13]
s_mov_b64 exec, 3
buffer_store_dwordx2 v[28:29], v2, s[8:11], s53 offen offset:0 sc1
s_mov_b64 exec, -1
K 184,184,185
W 8
Mvaa 200,152,208
K 185,186,187
K 186,188,189
Mvaa 200,156,212
s_waitcnt vmcnt(2)
v_xor_b32 v12, s12, v12
v_xor_b32 v13, s13, v13
v_lshrrev_b32 v3, 11, v12
v_or_b32 v13, v13, v3
v_cmp_eq_u32 vcc, 0, v13
s_cmp_eq_u64 vcc, -1
s_cbranch_scc0 .LL_89%=
.LL_90%=:
v_bfe_u32 v13, v12, 4, 7
s_xor_b32 s66, s61, 124
v_cmp_eq_u32 vcc, s61, v13
v_cmp_eq_u32_e64 s[86:87], s66, v13
s_or_b32 s68, vcc_lo, s86
s_and_b32 s68, s68, 0xff
s_cbranch_scc1 .Lup_87%=
.Lupr_88%=:
Ta 144,22,51296
K 187,190,191
Mvaa 192,156,196
s_add_u32 s83, s52, 368640
buffer_store_dwordx4 v[184:187], v1, s[8:11], s83 offen sc1
Ta 146,22,56416
Mvaa 192,152,192
W 6
Ta 148,24,10336
Ta 150,24,15456
Mvaa 196,128,96
Ta 152,24,30816
v_lshlrev_b32 v32, 16, v36
Mvaa 196,132,100
Ta 154,24,35936
v_and_b32 v33, 0xffff0000, v36
Mvaa 204,132,116
Ta 156,24,41056
v_lshlrev_b32 v34, 16, v37
Mvaa 204,128,112
W 7
Ta 158,24,46176
v_and_b32 v35, 0xffff0000, v37
Mvaa 204,136,120
Ta 128,23,20608
v_lshlrev_b32 v36, 16, v38
Mvaa 204,140,124
Ta 130,23,25728
v_and_b32 v37, 0xffff0000, v38
Mvaa 196,140,108
Ta 132,23,51328
v_lshlrev_b32 v38, 16, v39
Mvaa 196,136,104
W 7
Ta 134,23,56448
v_and_b32 v39, 0xffff0000, v39
Mvaa 196,144,184
Ta 136,25,128
v_lshlrev_b32 v40, 16, v44
Mvaa 196,148,188
Ta 138,25,5248
v_and_b32 v41, 0xffff0000, v44
Mvaa 204,148,204
Ta 140,21,41088
v_lshlrev_b32 v42, 16, v45
Mvaa 204,144,200
W 7
Ta 142,21,46208
v_and_b32 v43, 0xffff0000, v45
Mvaa 204,152,208
Ta 144,21,51328
v_lshlrev_b32 v44, 16, v46
Mvaa 204,156,212
Ta 146,21,56448
v_and_b32 v45, 0xffff0000, v46
Mvaa 196,156,196
Ta 148,23,10368
v_lshlrev_b32 v46, 16, v47
Mvaa 196,152,192
W 7
Ta 150,23,15488
v_and_b32 v47, 0xffff0000, v47
Mvaa 208,128,96
Ta 152,23,30848
v_lshlrev_b32 v48, 16, v52
Mvaa 208,132,100
Ta 154,23,35968
v_and_b32 v49, 0xffff0000, v52
Mvaa 216,132,116
Ta 156,23,41088
v_lshlrev_b32 v50, 16, v53
Mvaa 216,128,112
W 7
Ta 158,23,46208
v_and_b32 v51, 0xffff0000, v53
Mvaa 216,136,120
K 192,192,193
v_lshlrev_b32 v52, 16, v54
Mvaa 216,140,124
Ta 128,24,20640
Ta 130,24,25760
Mvaa 208,140,108
K 193,194,195
v_and_b32 v53, 0xffff0000, v54
Mvaa 208,136,104
W 6
v_lshlrev_b32 v54, 16, v55
K 194,196,197
Mvaa 208,144,184
Ta 132,24,51360
Ta 134,24,56480
Mvaa 208,148,188
v_and_b32 v55, 0xffff0000, v55
K 195,198,199
)"
R"(Mvaa 216,148,204
s_add_u32 s83, s52, 335872
Ta 136,26,160
v_lshlrev_b32 v56, 16, v60
Mvaa 216,144,200
W 5
Ta 138,26,5280
v_and_b32 v57, 0xffff0000, v60
Mvaa 216,152,208
buffer_store_dwordx4 v[192:195], v1, s[8:11], s83 offen sc1
v_lshlrev_b32 v58, 16, v61
Mvaa 216,156,212
K 200,200,201
K 201,202,203
Mvaa 208,156,196
K 202,204,205
K 203,206,207
s_add_u32 s83, s52, 376832
buffer_store_dwordx4 v[200:203], v1, s[8:11], s83 offen sc1
Mvaa 208,152,192
Ta 140,22,41120
Ta 142,22,46240
W 4
Mvaa 212,128,96
v_and_b32 v59, 0xffff0000, v61
v_lshlrev_b32 v60, 16, v62
Mvaa 212,132,100
Ta 144,22,51360
Ta 146,22,56480
Mvaa 220,132,116
v_and_b32 v61, 0xffff0000, v62
v_lshlrev_b32 v62, 16, v63
Mvaa 220,128,112
Ta 148,24,10400
Ta 150,24,15520
W 4
Mvaa 220,136,120
v_and_b32 v63, 0xffff0000, v63
v_lshlrev_b32 v64, 16, v68
Mvaa 220,140,124
Ta 152,24,30880
Ta 154,24,36000
Mvaa 212,140,108
v_and_b32 v65, 0xffff0000, v68
v_lshlrev_b32 v66, 16, v69
Mvaa 212,136,104
Ta 156,24,41120
Ta 158,24,46240
W 4
Mvaa 212,144,184
v_and_b32 v67, 0xffff0000, v69
v_lshlrev_b32 v68, 16, v70
Mvaa 212,148,188
Ta 128,23,20672
Ta 130,23,25792
Mvaa 220,148,204
v_and_b32 v69, 0xffff0000, v70
v_lshlrev_b32 v70, 16, v71
Mvaa 220,144,200
Ta 132,23,51392
Ta 134,23,56512
W 4
Mvaa 220,152,208
v_and_b32 v71, 0xffff0000, v71
v_lshlrev_b32 v72, 16, v76
Mvaa 220,156,212
Ta 136,25,192
Ta 138,25,5312
Mvaa 212,156,196
v_and_b32 v73, 0xffff0000, v76
v_lshlrev_b32 v74, 16, v77
Mvaa 212,152,192
Ta 140,21,41152
Ta 142,21,46272
W 4
Mvaa 224,128,96
v_and_b32 v75, 0xffff0000, v77
v_lshlrev_b32 v76, 16, v78
Mvaa 224,132,100
Ta 144,21,51392
Ta 146,21,56512
Mvaa 232,132,116
v_and_b32 v77, 0xffff0000, v78
v_lshlrev_b32 v78, 16, v79
Mvaa 232,128,112
Ta 148,23,10432
Ta 150,23,15552
W 4
Mvaa 232,136,120
Ta 152,23,30912
v_and_b32 v79, 0xffff0000, v79
Mvaa 232,140,124
Ta 154,23,36032
Ta 156,23,41152
Mvaa 224,140,108
Ta 158,23,46272
K 208,208,209
Mvaa 224,136,104
W 4
Ta 128,24,20704
Ta 130,24,25824
Mvaa 224,144,184
K 209,210,211
K 210,212,213
Mvaa 224,148,188
Ta 132,24,51424
Ta 134,24,56544
Mvaa 232,148,204
Ta 136,26,224
K 211,214,215
Mvaa 232,144,200
W 5
s_add_u32 s83, s52, 344064
Ta 138,26,5344
Mvaa 232,152,208
buffer_store_dwordx4 v[208:211], v1, s[8:11], s83 offen sc1
K 216,216,217
Mvaa 232,156,212
K 217,218,219
K 218,220,221
Mvaa 224,156,196
K 219,222,223
s_add_u32 s83, s52, 385024
buffer_store_dwordx4 v[216:219], v1, s[8:11], s83 offen sc1
Mvaa 224,152,192
Ta 140,22,41184
Ta 142,22,46304
W 4
Mvaa 228,128,96
Ta 144,22,51424
Ta 146,22,56544
Mvaa 228,132,100
Ta 148,24,10464
Ta 150,24,15584
Mvaa 236,132,116
Ta 152,24,30944
Ta 154,24,36064
Mvaa 236,128,112
W 6
Ta 156,24,41184
Ta 158,24,46304
Mvaa 236,136,120
Ta 128,23,20736
Ta 130,23,25856
Mvaa 236,140,124
Ta 132,23,51456
Ta 134,23,56576
Mvaa 228,140,108
Mvaa 228,136,104
W 8
Ta 136,25,256
Ta 138,25,5376
Mvaa 228,144,184
Mvaa 228,148,188
Ta 140,21,41216
Ta 142,21,46336
Mvaa 236,148,204
Mvaa 236,144,200
W 8
Ta 144,21,51456
Ta 146,21,56576
Mvaa 236,152,208
Mvaa 236,156,212
Ta 148,23,10496
Ta 150,23,15616
Mvaa 228,156,196
Mvaa 228,152,192
W 8
Ta 152,23,30976
Ta 154,23,36096
Mvaa 240,128,96
Mvaa 240,132,100
Ta 156,23,41216
Ta 158,23,46336
Mvaa 248,132,116
K 224,224,225
Mvaa 248,128,112
W 8
Ta 128,24,20768
Ta 130,24,25888
Mvaa 248,136,120
K 225,226,227
Mvaa 248,140,124
Ta 132,24,51488
K 226,228,229
Mvaa 240,140,108
Ta 134,24,56608
K 227,230,231
Mvaa 240,136,104
W 8
Ta 136,26,288
Ta 138,26,5408
Mvaa 240,144,184
s_add_u32 s83, s52, 352256
Mvaa 240,148,188
buffer_store_dwordx4 v[224:227], v1, s[8:11], s83 offen sc1
K 232,232,233
Mvaa 248,148,204
K 233,234,235
K 234,236,237
Mvaa 248,144,200
W 6
Ta 140,22,41248
K 235,238,239
Mvaa 248,152,208
s_add_u32 s83, s52, 393216
Ta 142,22,46368
Ta 144,22,51488
Mvaa 248,156,212
buffer_store_dwordx4 v[232:235], v1, s[8:11], s83 offen sc1
Ta 146,22,56608
Mvaa 240,156,196
Ta 148,24,10528
Ta 150,24,15648
Mvaa 240,152,192
W 8
Ta 152,24,31008
Ta 154,24,36128
Mvaa 244,128,96
Mvaa 244,132,100
Ta 156,24,41248
Ta 158,24,46368
Mvaa 252,132,116
Mvaa 252,128,112
W 8
Mvaa 252,136,120
Mvaa 252,140,124
Mvaa 244,140,108
Mvaa 244,136,104
W 4
Mvaa 244,144,184
Mvaa 244,148,188
Mvaa 252,148,204
Mvaa 252,144,200
W 0
Mvaa 252,152,208
Mvaa 252,156,212
Mvaa 244,156,196
Mvaa 244,152,192
K 240,240,241
K 241,242,243
s_nop 5
K 242,244,245
K 243,246,247
s_add_u32 s83, s52, 360448
buffer_store_dwordx4 v[240:243], v1, s[8:11], s83 offen sc1
K 248,248,249
K 249,250,251
K 250,252,253
K 251,254,255
s_add_u32 s83, s52, 401408
buffer_store_dwordx4 v[248:251], v1, s[8:11], s83 offen sc1
s_branch .Ltend_83%=
.p2align 12
.Llate_82%=:
ds_read_b32 v11, v11 offset:59392
ds_read_b64 v[8:9], v10 offset:57344
Rv 176,3,0
Rv 180,3,1024
Rv 184,3,2048
Rv 188,3,3072
Rv 192,3,4096
Rv 196,3,5120
Rv 200,3,6144
Rv 204,3,7168
W 6
Mvvv 32,224,176
s_lshl_b32 s92, s58, 2
s_add_u32 s91, s92, s51
s_add_u32 s95, s91, 7
Mvvv 36,228,176
s_and_b32 s95, s95, 15
s_mul_i32 s95, s95, 40960
s_mul_i32 s71, s51, 20480
Mvvv 40,224,180
s_add_u32 s71, s71, 61440
s_add_u32 m0, s71, s48
s_lshl2_add_u32 s94, s48, s95
Mvvv 44,228,180
W 4
v_cmp_ne_u32 vcc, 0, v11
s_cmp_lg_u64 vcc, 0
s_cbranch_scc1 .Lredo%=
A 4,4,8
Mvvv 48,224,184
v_rcp_f32 v4, v4
Mvvv 52,228,184
Ta 128,25,10240
A 5,5,9
Mvvv 56,224,188
Ta 130,25,15360
D 16,40,94,0
Mvvv 60,228,188
W 4
s_add_u32 s95, s91, 9
s_and_b32 s95, s95, 15
Mvvv 64,224,192
s_mul_i32 s95, s95, 40960
s_mul_i32 s71, s51, 10240
s_add_u32 s71, s71, 112640
Mvvv 68,228,192
v_rcp_f32 v5, v5
Mvvv 72,224,196
s_lshl_b32 s93, s58, 12
s_add_u32 s93, s93, s50
Ta 132,25,20480
Mvvv 76,228,196
Ta 134,25,25600
W 4
Mvvv 80,224,200
U 32,32,4
U 33,33,4
Mvvv 84,228,200
Ta 136,26,10272
U 34,34,4
Mvvv 88,224,204
U 35,35,4
U 36,36,4
U 37,37,4
U 38,38,4
U 39,39,4
K 32,32,33
K 33,34,35
K 34,36,37
K 35,38,39
buffer_store_dwordx4 v[32:35], v1, s[8:11], s93 offen offset:0 sc1
U 40,40,4
U 41,41,4
U 42,42,4
U 43,43,4
U 44,44,4
U 45,45,4
U 46,46,4
U 47,47,4
K 40,40,41
K 41,42,43
K 42,44,45
K 43,46,47
buffer_store_dwordx4 v[40:43], v1, s[8:11], s93 offen offset:1024 sc1
U 64,64,5
U 65,65,5
U 66,66,5
U 67,67,5
U 68,68,5
U 69,69,5
U 70,70,5
U 71,71,5
K 64,64,65
K 65,66,67
K 66,68,69
K 67,70,71
s_add_u32 s67, s93, 131072
buffer_store_dwordx4 v[64:67], v1, s[8:11], s67 offen offset:0 sc1
U 72,72,5
U 73,73,5
U 74,74,5
U 75,75,5
U 76,76,5
U 77,77,5
U 78,78,5
U 79,79,5
K 72,72,73
K 73,74,75
K 74,76,77
K 75,78,79
Mvvv 92,228,204
Ta 138,26,15392
Ta 140,26,20512
Ta 142,26,25632
W 4
Zvav 176,128,32
Ta 144,25,10304
Ta 146,25,15424
Mvav 176,132,40
Ta 148,25,20544
Ta 150,25,25664
Zvav 184,132,72
D 17,40,94,1024
Ta 152,26,10336
Mvav 184,128,64
W 5
Ta 154,26,15456
Ta 156,26,20576
Zvav 188,136,64
Ta 158,26,25696
Ta 128,25,10368
Mvav 188,140,72
Ta 130,25,15488
Ta 132,25,20608
Zvav 180,140,40
D 18,40,94,2048
Ta 134,25,25728
Mvav 180,136,32
W 8
Ta 136,26,10400
Ta 138,26,15520
Zvav 192,144,32
Mvav 192,148,40
Ta 140,26,20640
Ta 142,26,25760
)"
R"(Zvav 200,148,72
D 19,40,94,3072
Mvav 200,144,64
W 8
s_add_u32 m0, m0, 4096
Ta 144,25,10432
Zvav 204,152,64
Ta 146,25,15552
Mvav 204,156,72
Ta 148,25,20672
Ta 150,25,25792
Zvav 196,156,40
buffer_load_dwordx4 v20, s[40:43], s94 offen lds
Mvav 196,152,32
W 8
s_add_u32 m0, s71, s48
s_lshl2_add_u32 s94, s48, s95
Zvav 208,128,32
Ta 152,26,10464
Ta 154,26,15584
Mvav 208,132,40
Ta 156,26,20704
Ta 158,26,25824
Zvav 216,132,72
D 16,40,94,0
v_bfe_u32 v28, v1, 4, 2
Mvav 216,128,64
W 8
Ta 128,25,10496
v_lshlrev_b32 v28, 3, v28
Zvav 220,136,64
Ta 130,25,15616
Mvav 220,140,72
Ta 132,25,20736
Ta 134,25,25856
Zvav 212,140,40
buffer_load_dwordx2 v[28:29], v28, s[8:11], s54 offen sc1
Mvav 212,136,32
W 8
Ta 136,26,10528
Ta 138,26,15648
Zvav 224,144,32
Mvav 224,148,40
Ta 140,26,20768
Ta 142,26,25888
Zvav 232,148,72
D 17,40,94,1024
Mvav 232,144,64
W 8
Ta 144,25,30720
Ta 146,25,35840
Zvav 236,152,64
Mvav 236,156,72
Ta 148,21,0
Ta 150,21,5120
Zvav 228,156,40
D 18,40,94,2048
Mvav 228,152,32
W 8
Ta 152,26,30752
Ta 154,26,35872
Zvav 240,128,32
Mvav 240,132,40
Ta 156,22,32
Ta 158,22,5152
Zvav 248,132,72
D 19,40,94,3072
Mvav 248,128,64
W 8
s_add_u32 m0, m0, 4096
Ta 128,25,30784
Zvav 252,136,64
Ta 130,25,35904
Mvav 252,140,72
Ta 132,21,64
Ta 134,21,5184
Zvav 244,140,40
buffer_store_dwordx4 v[72:75], v1, s[8:11], s67 offen offset:1024 sc1
U 48,48,4
U 49,49,4
U 50,50,4
U 51,51,4
U 52,52,4
U 53,53,4
U 54,54,4
U 55,55,4
K 48,48,49
K 49,50,51
K 50,52,53
K 51,54,55
buffer_store_dwordx4 v[48:51], v1, s[8:11], s93 offen offset:2048 sc1
U 56,56,4
U 57,57,4
U 58,58,4
U 59,59,4
U 60,60,4
U 61,61,4
U 62,62,4
U 63,63,4
K 56,56,57
K 57,58,59
K 58,60,61
K 59,62,63
buffer_store_dwordx4 v[56:59], v1, s[8:11], s93 offen offset:3072 sc1
U 80,80,5
U 81,81,5
U 82,82,5
U 83,83,5
U 84,84,5
U 85,85,5
U 86,86,5
U 87,87,5
K 80,80,81
K 81,82,83
K 82,84,85
K 83,86,87
buffer_store_dwordx4 v[80:83], v1, s[8:11], s67 offen offset:2048 sc1
U 88,88,5
U 89,89,5
U 90,90,5
U 91,91,5
U 92,92,5
U 93,93,5
U 94,94,5
U 95,95,5
K 88,88,89
K 89,90,91
K 90,92,93
K 91,94,95
Mvav 244,136,32
W 8
Ta 136,26,30816
Ta 138,26,35936
Mvav 176,144,48
Mvav 176,148,56
Ta 140,22,96
Ta 142,22,5216
Mvav 184,148,88
buffer_load_dwordx4 v20, s[40:43], s94 offen lds
Mvav 184,144,80
s_waitcnt vmcnt(8)
v_cmp_eq_u64 vcc, s[12:13], v[28:29]
s_cmp_eq_u64 vcc, -1
s_cbranch_scc0 .Lspin_92%=
.Lok_93%=:
s_add_u32 s81, s58, 0
W 8
Mvav 188,152,80
s_and_b32 s81, s81, 3
s_lshl_b32 s81, s81, 12
s_add_u32 s81, s81, s49
Mvav 188,156,88
Ta 144,25,30848
Ta 146,25,35968
Mvav 180,156,56
buffer_load_dwordx4 a[184:187], v1, s[8:11], s81 offen offset:0 sc1
Ta 148,21,128
Mvav 180,152,48
W 7
s_add_u32 s82, s81, 131072
Ta 150,21,5248
Mvav 192,128,48
Ta 152,26,30880
Ta 154,26,36000
Mvav 192,132,56
Ta 156,22,160
Ta 158,22,5280
Mvav 200,132,88
buffer_load_dwordx4 a[200:203], v1, s[8:11], s82 offen offset:0 sc1
Mvav 200,128,80
W 8
Ta 128,25,30912
Ta 130,25,36032
Mvav 204,136,80
Mvav 204,140,88
Ta 132,21,192
Ta 134,21,5312
Mvav 196,140,56
buffer_load_dwordx4 a[188:191], v1, s[8:11], s81 offen offset:1024 sc1
Mvav 196,136,48
W 8
Ta 136,26,30944
Ta 138,26,36064
Mvav 208,144,48
Mvav 208,148,56
Ta 140,22,224
Ta 142,22,5344
Mvav 216,148,88
buffer_load_dwordx4 a[204:207], v1, s[8:11], s82 offen offset:1024 sc1
Mvav 216,144,80
W 8
Ta 144,25,30976
Ta 146,25,36096
Mvav 220,152,80
Mvav 220,156,88
Ta 148,21,256
Ta 150,21,5376
Mvav 212,156,56
buffer_load_dwordx4 a[192:195], v1, s[8:11], s81 offen offset:2048 sc1
Mvav 212,152,48
W 8
Ta 152,26,31008
Ta 154,26,36128
Mvav 224,128,48
Mvav 224,132,56
Ta 156,22,288
Ta 158,22,5408
Mvav 232,132,88
buffer_store_dwordx4 v[88:91], v1, s[8:11], s67 offen offset:3072 sc1
Mvav 232,128,80
W 8
Ta 128,25,10240
Ta 130,25,15360
Mvav 236,136,80
Mvav 236,140,88
Ta 132,25,20480
Ta 134,25,25600
Mvav 228,140,56
buffer_load_dwordx4 a[208:211], v1, s[8:11], s82 offen offset:2048 sc1
Mvav 228,136,48
W 8
Ta 136,26,10272
Ta 138,26,15392
Mvav 240,144,48
Mvav 240,148,56
Ta 140,26,20512
Ta 142,26,25632
Mvav 248,148,88
buffer_load_dwordx4 a[196:199], v1, s[8:11], s81 offen offset:3072 sc1
Mvav 248,144,80
W 8
Ta 144,25,10304
Ta 146,25,15424
Mvav 252,152,80
Mvav 252,156,88
Ta 148,25,20544
Ta 150,25,25664
Mvav 244,156,56
buffer_load_dwordx4 a[212:215], v1, s[8:11], s82 offen offset:3072 sc1
Mvav 244,152,48
s_waitcnt vmcnt(5) lgkmcnt(8)
Ta 152,26,10336
Ta 154,26,15456
Zvaa 96,128,184
Mvaa 96,132,188
Ta 156,26,20576
Ta 158,26,25696
Zvaa 104,132,204
Mvaa 104,128,200
W 8
Ta 128,25,10368
Ta 130,25,15488
Zvaa 108,136,200
Mvaa 108,140,204
Ta 132,25,20608
Ta 134,25,25728
Zvaa 100,140,188
s_lshl_b32 s92, s58, 2
s_add_u32 s91, s92, s51
s_add_u32 s95, s91, 11
Mvaa 100,136,184
W 8
s_and_b32 s95, s95, 15
s_mul_i32 s95, s95, 40960
Zvaa 112,144,184
s_mul_i32 s71, s51, 10240
s_add_u32 s71, s71, 40960
s_add_u32 m0, s71, s48
Mvaa 112,148,188
s_lshl2_add_u32 s94, s48, s95
Ta 136,26,10400
Ta 138,26,15520
Zvaa 120,148,204
Ta 140,26,20640
Ta 142,26,25760
Mvaa 120,144,200
W 8
Ta 144,25,10432
Ta 146,25,15552
Zvaa 124,152,200
Mvaa 124,156,204
Ta 148,25,20672
Ta 150,25,25792
Zvaa 116,156,188
Mvaa 116,152,184
W 8
Ta 152,26,10464
Ta 154,26,15584
Zvaa 128,128,184
s_lshl_b32 s80, s58, 3
s_add_u32 s80, s80, s55
s_waitcnt vmcnt(3)
v_mov_b64 v[28:29], s[12:13]
s_mov_b64 exec, 1
buffer_store_dwordx2 v[28:29], off, s[8:11], s80 sc1
s_mov_b64 exec, -1
Mvaa 128,132,188
Ta 156,26,20704
Ta 158,26,25824
Zvaa 136,132,204
Mvaa 136,128,200
W 8
Ta 128,25,10496
Ta 130,25,15616
Zvaa 140,136,200
s_barrier
D 16,40,94,0
Mvaa 140,140,204
s_add_u32 s95, s91, 13
s_and_b32 s95, s95, 15
s_mul_i32 s95, s95, 40960
Zvaa 132,140,188
s_mul_i32 s71, s51, 20480
s_add_u32 s71, s71, 71680
Ta 132,25,20736
Mvaa 132,136,184
W 7
Ta 134,25,25856
Ta 136,26,10528
Zvaa 144,144,184
Ta 138,26,15648
D 17,40,94,1024
Mvaa 144,148,188
Ta 140,26,20768
Ta 142,26,25888
Zvaa 152,148,204
Mvaa 152,144,200
W 8
Ta 144,25,30720
Ta 146,25,35840
Zvaa 156,152,200
D 18,40,94,2048
Mvaa 156,156,204
Ta 148,21,0
Ta 150,21,5120
Zvaa 148,156,188
Mvaa 148,152,184
W 8
Ta 152,26,30752
Ta 154,26,35872
Zvaa 160,128,184
D 19,40,94,3072
Mvaa 160,132,188
s_add_u32 m0, m0, 4096
Ta 156,22,32
Ta 158,22,5152
Zvaa 168,132,204
Mvaa 168,128,200
W 8
Ta 128,25,30784
Ta 130,25,35904
Zvaa 172,136,200
buffer_load_dwordx4 v20, s[40:43], s94 offen lds
Mvaa 172,140,204
s_add_u32 s81, s58, 1
s_and_b32 s81, s81, 3
s_lshl_b32 s81, s81, 12
Zvaa 164,140,188
s_add_u32 s81, s81, s49
Ta 132,21,64
Ta 134,21,5184
Mvaa 164,136,184
s_waitcnt vmcnt(6) lgkmcnt(8)
Ta 136,26,30816
Ta 138,26,35936
Mvaa 96,144,192
buffer_load_dwordx4 a[216:219], v1, s[8:11], s81 offen offset:0 sc1
Mvaa 96,148,196
s_add_u32 s82, s81, 131072
Ta 140,22,96
Ta 142,22,5216
Mvaa 104,148,212
Mvaa 104,144,208
W 8
Ta 144,25,30848
Ta 146,25,35968
Mvaa 108,152,208
buffer_load_dwordx4 a[232:235], v1, s[8:11], s82 offen offset:0 sc1
Mvaa 108,156,212
Ta 148,21,128
Ta 150,21,5248
Mvaa 100,156,196
Mvaa 100,152,192
W 8
Ta 152,26,30880
Ta 154,26,36000
Mvaa 112,128,192
buffer_load_dwordx4 a[220:223], v1, s[8:11], s81 offen offset:1024 sc1
Mvaa 112,132,196
Ta 156,22,160
Ta 158,22,5280
Mvaa 120,132,212
Mvaa 120,128,208
W 8
Ta 128,25,30912
Ta 130,25,36032
Mvaa 124,136,208
buffer_load_dwordx4 a[236:239], v1, s[8:11], s82 offen offset:1024 sc1
Mvaa 124,140,212
Ta 132,21,192
)"
R"(Ta 134,21,5312
Mvaa 116,140,196
Mvaa 116,136,192
W 8
Ta 136,26,30944
Ta 138,26,36064
Mvaa 128,144,192
buffer_load_dwordx4 a[224:227], v1, s[8:11], s81 offen offset:2048 sc1
Mvaa 128,148,196
Ta 140,22,224
Ta 142,22,5344
Mvaa 136,148,212
Mvaa 136,144,208
W 8
Ta 144,25,30976
Ta 146,25,36096
Mvaa 140,152,208
buffer_load_dwordx4 a[240:243], v1, s[8:11], s82 offen offset:2048 sc1
Mvaa 140,156,212
Ta 148,21,256
Ta 150,21,5376
Mvaa 132,156,196
Mvaa 132,152,192
W 8
Ta 152,26,31008
Ta 154,26,36128
Mvaa 144,128,192
buffer_load_dwordx4 a[228:231], v1, s[8:11], s81 offen offset:3072 sc1
Mvaa 144,132,196
Ta 156,22,288
Ta 158,22,5408
Mvaa 152,132,212
Mvaa 152,128,208
W 8
Ta 128,21,10240
Ta 130,21,15360
Mvaa 156,136,208
buffer_load_dwordx4 a[244:247], v1, s[8:11], s82 offen offset:3072 sc1
Mvaa 156,140,212
s_add_u32 m0, s71, s48
s_lshl2_add_u32 s94, s48, s95
Ta 132,21,20480
Mvaa 148,140,196
Ta 134,21,25600
Mvaa 148,136,192
W 8
Ta 136,22,10272
Ta 138,22,15392
Mvaa 160,144,192
D 16,40,94,0
Mvaa 160,148,196
s_add_u32 s95, s92, 15
s_and_b32 s95, s95, 15
s_mul_i32 s95, s95, 40960
Mvaa 168,148,212
s_mul_i32 s71, s51, 0
s_add_u32 s71, s71, 102400
Ta 140,22,20512
Mvaa 168,144,208
W 7
Ta 142,22,25632
Ta 144,21,10304
Mvaa 172,152,208
D 17,40,94,1024
Ta 146,21,15424
Mvaa 172,156,212
Ta 148,21,20544
Ta 150,21,25664
Mvaa 164,156,196
Mvaa 164,152,192
s_waitcnt vmcnt(6) lgkmcnt(8)
Ta 152,22,10336
Ta 154,22,15456
Mvaa 96,128,216
D 18,40,94,2048
Mvaa 96,132,220
Ta 156,22,20576
Ta 158,22,25696
Mvaa 104,132,236
Mvaa 104,128,232
W 8
Ta 128,21,10368
Ta 130,21,15488
Mvaa 108,136,232
D 19,40,94,3072
Mvaa 108,140,236
s_add_u32 m0, m0, 4096
Ta 132,21,20608
Ta 134,21,25728
Mvaa 100,140,220
Mvaa 100,136,216
W 8
Ta 136,22,10400
Ta 138,22,15520
Mvaa 112,144,216
buffer_load_dwordx4 v20, s[40:43], s94 offen lds
Mvaa 112,148,220
s_add_u32 m0, s71, s48
s_lshl2_add_u32 s94, s48, s95
Ta 140,22,20640
Mvaa 120,148,236
Ta 142,22,25760
Mvaa 120,144,232
W 8
Ta 144,21,10432
Ta 146,21,15552
Mvaa 124,152,232
D 16,40,94,0
Mvaa 124,156,236
Ta 148,21,20672
Ta 150,21,25792
Mvaa 116,156,220
D 17,40,94,1024
D 18,40,94,2048
D 19,40,94,3072
s_add_u32 m0, m0, 4096
s_nop 0
buffer_load_dwordx4 v20, s[40:43], s94 offen lds
Mvaa 116,152,216
W 8
s_add_u32 s81, s58, 2
s_and_b32 s81, s81, 3
Mvaa 128,128,216
s_lshl_b32 s81, s81, 12
s_add_u32 s81, s81, s49
Ta 152,22,10464
Mvaa 128,132,220
Ta 154,22,15584
Ta 156,22,20704
Mvaa 136,132,236
Ta 158,22,25824
Mvaa 136,128,232
W 8
Ta 128,21,10496
Ta 130,21,15616
Mvaa 140,136,232
s_barrier
buffer_load_dwordx4 a[0:3], v1, s[8:11], s81 offen offset:0 sc1
v_and_b32 v27, 1, v12
Mvaa 140,140,236
s_add_u32 s82, s81, 131072
Ta 132,21,20736
Ta 134,21,25856
Mvaa 132,140,220
Mvaa 132,136,216
W 8
Ta 136,22,10528
v_lshlrev_b32 v27, 1, v27
Mvaa 144,144,216
Ta 138,22,15648
buffer_load_dwordx4 a[16:19], v1, s[8:11], s82 offen offset:0 sc1
Mvaa 144,148,220
Ta 140,22,20768
Ta 142,22,25888
Mvaa 152,148,236
v_xor_b32 v27, v27, v13
Mvaa 152,144,232
W 8
Ta 144,21,30720
Ta 146,21,35840
Mvaa 156,152,232
buffer_load_dwordx4 a[4:7], v1, s[8:11], s81 offen offset:1024 sc1
Mvaa 156,156,236
Ta 148,23,0
v_and_b32 v12, 2, v12
Mvaa 148,156,220
Ta 150,23,5120
Mvaa 148,152,216
W 8
Ta 152,22,30752
Ta 154,22,35872
Mvaa 160,128,216
buffer_load_dwordx4 a[20:23], v1, s[8:11], s82 offen offset:1024 sc1
v_or_b32 v12, v12, v14
Mvaa 160,132,220
Ta 156,24,32
Ta 158,24,5152
Mvaa 168,132,236
v_lshl_or_b32 v12, v27, 2, v12
Mvaa 168,128,232
W 8
Ta 128,21,30784
Ta 130,21,35904
Mvaa 172,136,232
buffer_load_dwordx4 a[8:11], v1, s[8:11], s81 offen offset:2048 sc1
Mvaa 172,140,236
Ta 132,23,64
v_cmp_eq_u32_e64 s[86:87], 0, v12
Mvaa 164,140,220
Ta 134,23,5184
Mvaa 164,136,216
s_waitcnt vmcnt(15) lgkmcnt(8)
Ta 136,22,30816
Ta 138,22,35936
Mvaa 96,144,224
buffer_load_dwordx4 a[24:27], v1, s[8:11], s82 offen offset:2048 sc1
v_cndmask_b32_e64 v4, 0, v15, s[86:87]
Mvaa 96,148,228
Ta 140,24,96
Ta 142,24,5216
Mvaa 104,148,244
Mvaa 104,144,240
W 8
Ta 144,21,30848
v_cmp_eq_u32_e64 s[86:87], 1, v12
Mvaa 108,152,240
Ta 146,21,35968
buffer_load_dwordx4 a[12:15], v1, s[8:11], s81 offen offset:3072 sc1
Mvaa 108,156,244
Ta 148,23,128
v_cndmask_b32_e64 v5, 0, v15, s[86:87]
Mvaa 100,156,228
Ta 150,23,5248
Mvaa 100,152,224
W 8
Ta 152,22,30880
Ta 154,22,36000
Mvaa 112,128,224
buffer_load_dwordx4 a[28:31], v1, s[8:11], s82 offen offset:3072 sc1
v_cmp_eq_u32_e64 s[86:87], 2, v12
Mvaa 112,132,228
s_add_u32 s81, s58, 3
s_and_b32 s81, s81, 3
s_lshl_b32 s81, s81, 12
Mvaa 120,132,244
s_add_u32 s81, s81, s49
Ta 156,24,160
Ta 158,24,5280
Mvaa 120,128,240
W 8
Ta 128,21,30912
v_cndmask_b32_e64 v6, 0, v15, s[86:87]
Mvaa 124,136,240
Ta 130,21,36032
buffer_load_dwordx4 a[32:35], v1, s[8:11], s81 offen offset:0 sc1
Mvaa 124,140,244
s_add_u32 s82, s81, 131072
Ta 132,23,192
Ta 134,23,5312
Mvaa 116,140,228
v_cmp_eq_u32_e64 s[86:87], 3, v12
Mvaa 116,136,224
W 8
Ta 136,22,30944
Ta 138,22,36064
Mvaa 128,144,224
buffer_load_dwordx4 a[48:51], v1, s[8:11], s82 offen offset:0 sc1
v_cndmask_b32_e64 v7, 0, v15, s[86:87]
Mvaa 128,148,228
Ta 140,24,224
Ta 142,24,5344
Mvaa 136,148,244
Mvaa 136,144,240
W 8
Ta 144,21,30976
v_cmp_eq_u32_e64 s[86:87], 4, v12
Mvaa 140,152,240
s_waitcnt vmcnt(10)
s_barrier
Ta 146,21,36096
Mvaa 140,156,244
buffer_load_dwordx4 a[36:39], v1, s[8:11], s81 offen offset:1024 sc1
Ta 148,23,256
Mvaa 132,156,228
Ta 150,23,5376
v_cndmask_b32_e64 v8, 0, v15, s[86:87]
Mvaa 132,152,224
W 8
Ta 152,22,31008
Ta 154,22,36128
Mvaa 144,128,224
Mvaa 144,132,228
buffer_load_dwordx4 a[52:55], v1, s[8:11], s82 offen offset:1024 sc1
v_cmp_eq_u32_e64 s[86:87], 5, v12
Mvaa 152,132,244
Ta 156,24,288
Ta 158,24,5408
Mvaa 152,128,240
W 8
Ta 128,23,20480
Ta 130,23,25600
Mvaa 156,136,240
v_cndmask_b32_e64 v9, 0, v15, s[86:87]
Mvaa 156,140,244
buffer_load_dwordx4 a[40:43], v1, s[8:11], s81 offen offset:2048 sc1
Ta 132,23,51200
Mvaa 148,140,228
Ta 134,23,56320
v_cmp_eq_u32_e64 s[86:87], 6, v12
Mvaa 148,136,224
W 8
Ta 136,24,20512
Ta 138,24,25632
Mvaa 160,144,224
Mvaa 160,148,228
buffer_load_dwordx4 a[56:59], v1, s[8:11], s82 offen offset:2048 sc1
v_cndmask_b32_e64 v10, 0, v15, s[86:87]
Mvaa 168,148,244
Ta 140,24,51232
Ta 142,24,56352
Mvaa 168,144,240
W 8
Ta 144,23,20544
Ta 146,23,25664
Mvaa 172,152,240
v_cmp_eq_u32_e64 s[86:87], 7, v12
Mvaa 172,156,244
buffer_load_dwordx4 a[44:47], v1, s[8:11], s81 offen offset:3072 sc1
Ta 148,23,51264
Mvaa 164,156,228
Ta 150,23,56384
Mvaa 164,152,224
s_waitcnt vmcnt(11) lgkmcnt(8)
Ta 152,24,20576
v_cndmask_b32_e64 v11, 0, v15, s[86:87]
Mvaa 96,128,0
Ta 154,24,25696
Mvaa 96,132,4
s_mov_b32 s20, s38
buffer_load_dwordx4 a[60:63], v1, s[8:11], s82 offen offset:3072 sc1
Ta 156,24,51296
Mvaa 104,132,20
Ta 158,24,56416
Mvaa 104,128,16
W 8
Ta 128,23,20608
Ta 130,23,25728
Mvaa 108,136,16
s_and_b32 s21, s39, 0xffff
Mvaa 108,140,20
Ta 132,23,51328
Ta 134,23,56448
Mvaa 100,140,4
Mvaa 100,136,0
W 8
s_mov_b32 s22, 2621440
Ta 136,24,20640
Mvaa 112,144,0
Ta 138,24,25760
v_bfe_u32 v28, v1, 4, 2
Mvaa 112,148,4
Ta 140,24,51360
v_lshlrev_b32 v28, 3, v28
Mvaa 120,148,20
s_mov_b32 s23, 0x20000
Ta 142,24,56480
buffer_load_dwordx2 v[28:29], v28, s[8:11], s55 offen sc1
Mvaa 120,144,16
W 8
Ta 144,23,20672
Ta 146,23,25792
Mvaa 124,152,16
s_and_b32 s17, s17, 0xffff
Mvaa 124,156,20
Ta 148,23,51392
Ta 150,23,56512
Mvaa 116,156,4
Mvaa 116,152,0
W 8
s_mov_b32 s18, 2621440
Ta 152,24,20704
Mvaa 128,128,0
Ta 154,24,25824
Mvaa 128,132,4
Ta 156,24,51424
Ta 158,24,56544
Mvaa 136,132,20
s_mov_b32 s19, 0x20000
Mvaa 136,128,16
W 8
Ta 128,23,20736
Ta 130,23,25856
Mvaa 140,136,16
Mvaa 140,140,20
s_lshr_b32 s66, s15, 3
Ta 132,23,51456
Ta 134,23,56576
Mvaa 132,140,4
Mvaa 132,136,0
W 8
s_lshl_b32 s66, s66, 4
Ta 136,24,20768
Mvaa 144,144,0
Ta 138,24,25888
Mvaa 144,148,4
Ta 140,24,51488
Ta 142,24,56608
Mvaa 152,148,20
s_add_u32 s66, s66, s59
Mvaa 152,144,16
W 8
Ta 144,25,0
Ta 146,25,5120
Mvaa 156,152,16
Mvaa 156,156,20
s_lshl_b32 s63, s66, 6
Ta 148,21,40960
Ta 150,21,46080
Mvaa 148,156,4
Mvaa 148,152,0
)"
R"(W 8
Ta 152,26,32
Ta 154,26,5152
Mvaa 160,128,0
s_add_u32 s63, s63, 65536
Mvaa 160,132,4
Ta 156,22,40992
Ta 158,22,46112
Mvaa 168,132,20
Mvaa 168,128,16
W 8
s_mul_i32 s62, s66, 40960
Ta 128,25,64
Mvaa 172,136,16
Ta 130,25,5184
Mvaa 172,140,20
s_add_u32 s62, s62, 56623104
Ta 132,21,41024
Ta 134,21,46144
Mvaa 164,140,4
Mvaa 164,136,0
s_waitcnt vmcnt(9) lgkmcnt(8)
Ta 136,26,96
Ta 138,26,5216
Mvaa 96,144,8
s_lshl_b32 s66, s14, 13
Mvaa 96,148,12
Ta 140,22,41056
Ta 142,22,46176
Mvaa 104,148,28
Mvaa 104,144,24
W 8
s_add_u32 s78, s62, s66
Ta 144,25,128
Mvaa 108,152,24
Ta 146,25,5248
Mvaa 108,156,28
Ta 148,21,41088
Ta 150,21,46208
Mvaa 100,156,12
s_add_u32 s79, s78, 327680
Mvaa 100,152,8
W 8
Ta 152,26,160
Ta 154,26,5280
Mvaa 112,128,8
s_and_b32 s66, s14, 1
Mvaa 112,132,12
Ta 156,22,41120
Ta 158,22,46240
Mvaa 120,132,28
s_waitcnt vmcnt(0)
v_cmp_eq_u64 vcc, s[12:13], v[28:29]
s_cmp_eq_u64 vcc, -1
s_cbranch_scc0 .Lspin_95%=
.Lok_96%=:
s_add_u32 s81, s58, 1
s_and_b32 s81, s81, 3
Mvaa 120,128,24
W 8
s_lshl_b32 s81, s81, 12
s_add_u32 s81, s81, s50
Mvaa 124,136,24
s_mul_i32 s66, s66, 327680
buffer_load_dwordx4 a[64:67], v1, s[8:11], s81 offen offset:0 sc1
Ta 128,25,192
Mvaa 124,140,28
s_add_u32 s82, s81, 131072
Ta 130,25,5312
Ta 132,21,41152
Mvaa 116,140,12
s_add_u32 s65, s62, s66
Ta 134,21,46272
Mvaa 116,136,8
W 8
Ta 136,26,224
Ta 138,26,5344
Mvaa 128,144,8
buffer_load_dwordx4 a[80:83], v1, s[8:11], s82 offen offset:0 sc1
Mvaa 128,148,12
s_add_u32 s65, s65, 32768
Ta 140,22,41184
Ta 142,22,46304
Mvaa 136,148,28
Mvaa 136,144,24
W 8
s_mov_b32 s64, 0
Ta 144,25,256
Mvaa 140,152,24
Ta 146,25,5376
Mvaa 140,156,28
buffer_load_dwordx4 a[68:71], v1, s[8:11], s81 offen offset:1024 sc1
Ta 148,21,41216
Mvaa 132,156,12
Ta 150,21,46336
v_add_u32 v19, 4096, v1
Mvaa 132,152,8
W 8
Ta 152,26,288
Ta 154,26,5408
Mvaa 144,128,8
Mvaa 144,132,12
Ta 156,22,41248
v_bfe_u32 v27, v1, 4, 3
Mvaa 152,132,28
Ta 158,22,46368
buffer_load_dwordx4 a[84:87], v1, s[8:11], s82 offen offset:1024 sc1
Mvaa 152,128,24
W 8
Ta 128,21,51200
Ta 130,21,56320
Mvaa 156,136,24
v_lshlrev_b32 v27, 3, v27
Mvaa 156,140,28
Ta 132,23,10240
Ta 134,23,15360
Mvaa 148,140,12
v_bfe_u32 v18, v1, 7, 1
Mvaa 148,136,8
W 8
buffer_load_dwordx4 a[72:75], v1, s[8:11], s81 offen offset:2048 sc1
Ta 136,22,51232
Mvaa 160,144,8
Ta 138,22,56352
Mvaa 160,148,12
Ta 140,24,10272
v_lshl_add_u32 v18, v18, 9, v27
Mvaa 168,148,28
Ta 142,24,15392
Mvaa 168,144,24
W 8
Ta 144,21,51264
Ta 146,21,56384
Mvaa 172,152,24
s_mov_b32 s71, 0
buffer_load_dwordx4 a[88:91], v1, s[8:11], s82 offen offset:2048 sc1
Mvaa 172,156,28
Ta 148,23,10304
Ta 150,23,15424
Mvaa 164,156,12
Mvaa 164,152,8
W 8
Ta 152,22,51296
v_bfe_u32 v20, v1, 4, 4
Mvaa 96,128,32
Ta 154,22,56416
Mvaa 96,132,36
buffer_load_dwordx4 a[76:79], v1, s[8:11], s81 offen offset:3072 sc1
Ta 156,24,10336
Mvaa 104,132,52
Ta 158,24,15456
v_mul_u32_u24 v20, 1280, v20
Mvaa 104,128,48
W 8
Ta 128,21,51328
Ta 130,21,56448
Mvaa 108,136,48
v_lshrrev_b32 v12, 8, v1
Mvaa 108,140,52
Ta 132,23,10368
Ta 134,23,15488
Mvaa 100,140,36
buffer_load_dwordx4 a[92:95], v1, s[8:11], s82 offen offset:3072 sc1
Mvaa 100,136,32
W 8
s_add_u32 s81, s58, 2
s_and_b32 s81, s81, 3
Mvaa 112,144,32
s_lshl_b32 s81, s81, 12
s_add_u32 s81, s81, s50
v_lshl_add_u32 v20, v12, 4, v20
Mvaa 112,148,36
Ta 136,22,51360
Ta 138,22,56480
Mvaa 120,148,52
s_bfe_u32 s66, s59, 0x10001
Ta 140,24,10400
Ta 142,24,15520
Mvaa 120,144,48
W 8
buffer_load_dwordx4 a[96:99], v1, s[8:11], s81 offen offset:0 sc1
Ta 144,21,51392
Mvaa 124,152,48
s_add_u32 s82, s81, 131072
Ta 146,21,56512
Mvaa 124,156,52
s_lshl_b32 s67, s57, 1
Ta 148,23,10432
Ta 150,23,15552
Mvaa 116,156,36
Mvaa 116,152,32
W 8
s_add_u32 s66, s66, s67
Ta 152,22,51424
Mvaa 128,128,32
Ta 154,22,56544
buffer_load_dwordx4 a[112:115], v1, s[8:11], s82 offen offset:0 sc1
Mvaa 128,132,36
Ta 156,24,10464
Ta 158,24,15584
Mvaa 136,132,52
s_lshl_b32 s61, s66, 2
Mvaa 136,128,48
W 8
Ta 128,21,51456
Ta 130,21,56576
Mvaa 140,136,48
Mvaa 140,140,52
s_lshr_b32 s66, s59, 2
buffer_load_dwordx4 a[100:103], v1, s[8:11], s81 offen offset:1024 sc1
Ta 132,23,10496
Mvaa 132,140,36
Ta 134,23,15616
Mvaa 132,136,32
W 8
Ta 136,22,51488
Ta 138,22,56608
Mvaa 144,144,32
s_lshl_b32 s66, s66, 1
Mvaa 144,148,36
Ta 140,24,10528
Ta 142,24,15648
Mvaa 152,148,52
s_add_u32 s61, s61, s66
buffer_load_dwordx4 a[116:119], v1, s[8:11], s82 offen offset:1024 sc1
Mvaa 152,144,48
W 8
Ta 144,23,30720
Ta 146,23,35840
Mvaa 156,152,48
Mvaa 156,156,52
s_and_b32 s66, s59, 1
Ta 148,23,40960
Ta 150,23,46080
Mvaa 148,156,36
Mvaa 148,152,32
W 8
buffer_load_dwordx4 a[104:107], v1, s[8:11], s81 offen offset:2048 sc1
Ta 152,24,30752
Mvaa 160,128,32
s_add_u32 s61, s61, s66
Ta 154,24,35872
Mvaa 160,132,36
Ta 156,24,40992
Ta 158,24,46112
Mvaa 168,132,52
Mvaa 168,128,48
W 8
s_xor_b32 s67, s61, 0
Ta 128,23,30784
Mvaa 172,136,48
Ta 130,23,35904
buffer_load_dwordx4 a[120:123], v1, s[8:11], s82 offen offset:2048 sc1
Mvaa 172,140,52
Ta 132,23,41024
Ta 134,23,46144
Mvaa 164,140,36
s_mul_i32 s72, s67, 20480
Mvaa 164,136,32
W 8
Ta 136,24,30816
Ta 138,24,35936
Mvaa 96,144,40
s_mul_i32 s66, s58, 320
Mvaa 96,148,44
buffer_load_dwordx4 a[108:111], v1, s[8:11], s81 offen offset:3072 sc1
Ta 140,24,41056
Mvaa 104,148,60
Ta 142,24,46176
Mvaa 104,144,56
W 8
s_add_u32 s72, s72, s66
Ta 144,23,30848
Mvaa 108,152,56
Ta 146,23,35968
Mvaa 108,156,60
Ta 148,23,41088
Ta 150,23,46208
Mvaa 100,156,44
s_mul_i32 s75, s67, 20480
buffer_load_dwordx4 a[124:127], v1, s[8:11], s82 offen offset:3072 sc1
Mvaa 100,152,40
W 8
s_add_u32 s81, s58, 3
s_and_b32 s81, s81, 3
Mvaa 112,128,40
s_lshl_b32 s81, s81, 12
s_add_u32 s81, s81, s50
Ta 152,24,30880
Mvaa 112,132,44
s_mul_i32 s66, s58, 2560
Ta 154,24,36000
Ta 156,24,41120
Mvaa 120,132,60
Ta 158,24,46240
buffer_load_dwordx4 a[184:187], v1, s[8:11], s81 offen offset:0 sc1
Mvaa 120,128,56
W 8
s_add_u32 s82, s81, 131072
s_add_u32 s75, s75, s66
Mvaa 124,136,56
Ta 128,23,30912
Ta 130,23,36032
Mvaa 124,140,60
Ta 132,23,41152
Ta 134,23,46272
Mvaa 116,140,44
s_add_u32 s75, s75, 131072
Mvaa 116,136,40
W 8
Ta 136,24,30944
Ta 138,24,36064
Mvaa 128,144,40
buffer_load_dwordx4 a[200:203], v1, s[8:11], s82 offen offset:0 sc1
Mvaa 128,148,44
s_xor_b32 s67, s61, 124
Ta 140,24,41184
Ta 142,24,46304
Mvaa 136,148,60
Mvaa 136,144,56
W 8
Ta 144,23,30976
Ta 146,23,36096
Mvaa 140,152,56
s_mul_i32 s73, s67, 20480
Mvaa 140,156,60
buffer_load_dwordx4 a[188:191], v1, s[8:11], s81 offen offset:1024 sc1
Ta 148,23,41216
Mvaa 132,156,44
s_mul_i32 s66, s58, 320
Ta 150,23,46336
Mvaa 132,152,40
W 8
Ta 152,24,31008
Ta 154,24,36128
Mvaa 144,128,40
Mvaa 144,132,44
s_add_u32 s73, s73, s66
Ta 156,24,41248
Ta 158,24,46368
Mvaa 152,132,60
buffer_load_dwordx4 a[204:207], v1, s[8:11], s82 offen offset:1024 sc1
Mvaa 152,128,56
W 8
Ta 128,21,10240
Ta 130,21,15360
Mvaa 156,136,56
s_mul_i32 s76, s67, 20480
Mvaa 156,140,60
Ta 132,21,20480
Ta 134,21,25600
Mvaa 148,140,44
K 96,96,97
Mvaa 148,136,40
W 8
s_mul_i32 s66, s58, 2560
buffer_load_dwordx4 a[192:195], v1, s[8:11], s81 offen offset:2048 sc1
Mvaa 160,144,40
Ta 136,22,10272
Ta 138,22,15392
Mvaa 160,148,44
s_add_u32 s76, s76, s66
Ta 140,22,20512
K 97,98,99
Mvaa 168,148,60
Ta 142,22,25632
K 98,100,101
Mvaa 168,144,56
W 8
Ta 144,21,10304
Ta 146,21,15424
Mvaa 172,152,56
s_add_u32 s76, s76, 131072
buffer_load_dwordx4 a[208:211], v1, s[8:11], s82 offen offset:2048 sc1
K 99,102,103
Mvaa 172,156,60
s_add_u32 s83, s52, 0
Ta 148,21,20544
Ta 150,21,25664
Mvaa 164,156,44
Mvaa 164,152,40
s_waitcnt vmcnt(18) lgkmcnt(8)
Ta 152,22,10336
v_bfe_u32 v15, v1, 7, 1
Mvaa 176,128,64
buffer_store_dwordx4 v[96:99], v1, s[8:11], s83 offen sc1
Ta 154,22,15456
Mvaa 176,132,68
Ta 156,22,20576
K 104,104,105
Mvaa 184,132,84
Ta 158,22,25696
v_mul_u32_u24 v14, 10240, v15
Mvaa 184,128,80
W 8
Ta 128,21,10368
K 105,106,107
Mvaa 188,136,80
buffer_load_dwordx4 a[196:199], v1, s[8:11], s81 offen offset:3072 sc1
Ta 130,21,15488
)"
R"(Mvaa 188,140,84
K 106,108,109
v_lshrrev_b32 v15, 8, v1
Mvaa 180,140,68
Ta 132,21,20608
Ta 134,21,25728
Mvaa 180,136,64
W 8
K 107,110,111
v_lshl_add_u32 v14, v15, 7, v14
Mvaa 192,144,64
s_add_u32 s83, s52, 40960
buffer_load_dwordx4 a[212:215], v1, s[8:11], s82 offen offset:3072 sc1
Ta 136,22,10400
Mvaa 192,148,68
Ta 138,22,15520
Ta 140,22,20640
Mvaa 200,148,84
Ta 142,22,25760
v_and_b32 v15, 0x70, v1
Mvaa 200,144,80
W 8
Ta 144,21,10432
Ta 146,21,15552
Mvaa 204,152,80
buffer_store_dwordx4 v[104:107], v1, s[8:11], s83 offen sc1
K 112,112,113
Mvaa 204,156,84
K 113,114,115
v_add_u32 v14, v14, v15
Mvaa 196,156,68
Ta 148,21,20672
Ta 150,21,25792
Mvaa 196,152,64
W 8
Ta 152,22,10464
K 114,116,117
Mvaa 208,128,64
s_bitcmp1_b32 s14, 0
s_cselect_b32 s74, s73, s72
s_cselect_b32 s77, s76, s75
Ta 154,22,15584
K 115,118,119
Mvaa 208,132,68
s_add_u32 s83, s52, 8192
Ta 156,22,20704
Ta 158,22,25824
Mvaa 216,132,84
s_add_u32 s74, s74, 256
Mvaa 216,128,80
W 8
buffer_store_dwordx4 v[112:115], v1, s[8:11], s83 offen sc1
Ta 128,21,10496
Mvaa 220,136,80
Ta 130,21,15616
K 120,120,121
Mvaa 220,140,84
s_add_u32 s77, s77, 2048
Ta 132,21,20736
Ta 134,21,25856
Mvaa 212,140,68
K 121,122,123
Mvaa 212,136,64
W 8
Ta 136,22,10528
Ta 138,22,15648
Mvaa 224,144,64
s_lshl_b32 s66, s14, 6
K 122,124,125
Mvaa 224,148,68
Ta 140,22,20768
Ta 142,22,25888
Mvaa 232,148,84
K 123,126,127
Mvaa 232,144,80
W 8
s_lshl_b32 s67, s14, 9
s_add_u32 s83, s52, 49152
Mvaa 236,152,80
Ta 144,21,30720
Ta 146,21,35840
Mvaa 236,156,84
s_add_u32 s72, s72, s66
buffer_store_dwordx4 v[120:123], v1, s[8:11], s83 offen sc1
Ta 148,23,0
Mvaa 228,156,68
Ta 150,23,5120
K 128,128,129
Mvaa 228,152,64
W 8
Ta 152,22,30752
Ta 154,22,35872
Mvaa 240,128,64
s_add_u32 s75, s75, s67
K 129,130,131
Mvaa 240,132,68
Ta 156,24,32
Ta 158,24,5152
Mvaa 248,132,84
K 130,132,133
Mvaa 248,128,80
W 8
s_add_u32 s73, s73, s66
Ta 128,21,30784
Mvaa 252,136,80
Ta 130,21,35904
K 131,134,135
Mvaa 252,140,84
s_add_u32 s83, s52, 16384
Ta 132,23,64
Ta 134,23,5184
Mvaa 244,140,68
s_add_u32 s76, s76, s67
Mvaa 244,136,64
s_waitcnt vmcnt(20) lgkmcnt(8)
buffer_store_dwordx4 v[128:131], v1, s[8:11], s83 offen sc1
Ta 136,22,30816
Mvaa 176,144,72
Ta 138,22,35936
K 136,136,137
Mvaa 176,148,76
Ta 140,24,96
Ta 142,24,5216
Mvaa 184,148,92
K 137,138,139
Mvaa 184,144,88
W 8
buffer_load_dwordx4 v[36:39], v20, s[16:19], s72 offen
Ta 144,21,30848
Mvaa 188,152,88
Ta 146,21,35968
K 138,140,141
Mvaa 188,156,92
Ta 148,23,128
Ta 150,23,5248
Mvaa 180,156,76
K 139,142,143
Mvaa 180,152,72
W 8
s_add_u32 s83, s52, 57344
buffer_load_dwordx4 v[44:47], v14, s[8:11], s75 offen sc1
Mvaa 192,128,72
Ta 152,22,30880
Ta 154,22,36000
Mvaa 192,132,76
Ta 156,24,160
Ta 158,24,5280
Mvaa 200,132,92
Mvaa 200,128,88
W 8
buffer_store_dwordx4 v[136:139], v1, s[8:11], s83 offen sc1
K 144,144,145
Mvaa 204,136,88
Ta 128,21,30912
Ta 130,21,36032
Mvaa 204,140,92
Ta 132,23,192
K 145,146,147
Mvaa 196,140,76
Ta 134,23,5312
K 146,148,149
Mvaa 196,136,72
W 8
buffer_load_dwordx4 v[52:55], v20, s[16:19], s73 offen
Ta 136,22,30944
Mvaa 208,144,72
Ta 138,22,36064
K 147,150,151
Mvaa 208,148,76
s_add_u32 s83, s52, 24576
Ta 140,24,224
Ta 142,24,5344
Mvaa 216,148,92
Mvaa 216,144,88
W 8
buffer_store_dwordx4 v[144:147], v1, s[8:11], s83 offen sc1
Ta 144,21,30976
Mvaa 220,152,88
Ta 146,21,36096
K 152,152,153
Mvaa 220,156,92
Ta 148,23,256
Ta 150,23,5376
Mvaa 212,156,76
K 153,154,155
Mvaa 212,152,72
W 8
buffer_load_dwordx4 v[60:63], v14, s[8:11], s76 offen sc1
Ta 152,22,31008
Mvaa 224,128,72
Ta 154,22,36128
K 154,156,157
Mvaa 224,132,76
Ta 156,24,288
Ta 158,24,5408
Mvaa 232,132,92
K 155,158,159
Mvaa 232,128,88
W 8
s_add_u32 s83, s52, 65536
buffer_load_dwordx4 v[68:71], v20, s[16:19], s74 offen
Mvaa 236,136,88
Ta 128,23,20480
Ta 130,23,25600
Mvaa 236,140,92
Ta 132,23,51200
Ta 134,23,56320
Mvaa 228,140,76
Mvaa 228,136,72
W 8
buffer_store_dwordx4 v[152:155], v1, s[8:11], s83 offen sc1
K 160,160,161
Mvaa 240,144,72
Ta 136,25,0
Ta 138,25,5120
Mvaa 240,148,76
Ta 140,21,40960
K 161,162,163
Mvaa 248,148,92
Ta 142,21,46080
K 162,164,165
Mvaa 248,144,88
W 8
buffer_load_dwordx4 v[76:79], v14, s[8:11], s77 offen sc1
Ta 144,21,51200
Mvaa 252,152,88
Ta 146,21,56320
K 163,166,167
Mvaa 252,156,92
s_add_u32 s83, s52, 32768
Ta 148,23,10240
Ta 150,23,15360
Mvaa 244,156,76
Mvaa 244,152,72
s_waitcnt vmcnt(26) lgkmcnt(8)
buffer_store_dwordx4 v[160:163], v1, s[8:11], s83 offen sc1
Ta 152,23,30720
Mvaa 176,128,96
Ta 154,23,35840
K 168,168,169
Mvaa 176,132,100
Ta 156,23,40960
Ta 158,23,46080
Mvaa 184,132,116
K 169,170,171
Mvaa 184,128,112
s_waitcnt vmcnt(23) lgkmcnt(8)
buffer_load_dwordx2 v[12:13], v27, s[8:11], 0 offen offset:3584 sc1
Ta 128,24,20512
Mvaa 184,136,120
Ta 130,24,25632
K 170,172,173
Mvaa 184,140,124
Ta 132,24,51232
Ta 134,24,56352
Mvaa 176,140,108
K 171,174,175
Mvaa 176,136,104
s_waitcnt vmcnt(20) lgkmcnt(8)
s_add_u32 s83, s52, 73728
Ta 136,26,32
Mvaa 176,144,184
Ta 138,26,5152
Mvaa 176,148,188
buffer_store_dwordx4 v[168:171], v1, s[8:11], s83 offen sc1
Ta 140,22,40992
Mvaa 184,148,204
Ta 142,22,46112
Mvaa 184,144,200
s_waitcnt vmcnt(16) lgkmcnt(8)
Ta 144,22,51232
Ta 146,22,56352
Mvaa 184,152,208
Mvaa 184,156,212
Ta 148,24,10272
Ta 150,24,15392
Mvaa 176,156,196
Mvaa 176,152,192
W 8
Ta 152,24,30752
Ta 154,24,35872
Mvaa 180,128,96
Mvaa 180,132,100
Ta 156,24,40992
Ta 158,24,46112
Mvaa 188,132,116
Mvaa 188,128,112
W 8
Ta 128,23,20544
Ta 130,23,25664
Mvaa 188,136,120
Mvaa 188,140,124
Ta 132,23,51264
Ta 134,23,56384
Mvaa 180,140,108
Mvaa 180,136,104
W 8
Ta 136,25,64
Ta 138,25,5184
Mvaa 180,144,184
Mvaa 180,148,188
Ta 140,21,41024
Ta 142,21,46144
Mvaa 188,148,204
Mvaa 188,144,200
W 8
Ta 144,21,51264
Ta 146,21,56384
Mvaa 188,152,208
Mvaa 188,156,212
Ta 148,23,10304
Ta 150,23,15424
Mvaa 180,156,196
Mvaa 180,152,192
W 8
Ta 152,23,30784
Ta 154,23,35904
Mvaa 192,128,96
Mvaa 192,132,100
Ta 156,23,41024
Ta 158,23,46144
Mvaa 200,132,116
K 176,176,177
Mvaa 200,128,112
W 8
Ta 128,24,20576
Ta 130,24,25696
Mvaa 200,136,120
K 177,178,179
Mvaa 200,140,124
Ta 132,24,51296
K 178,180,181
Mvaa 192,140,108
Ta 134,24,56416
K 179,182,183
Mvaa 192,136,104
W 8
Ta 136,26,96
Ta 138,26,5216
Mvaa 192,144,184
s_add_u32 s83, s52, 327680
Mvaa 192,148,188
buffer_store_dwordx4 v[176:179], v1, s[8:11], s83 offen sc1
K 184,184,185
Mvaa 200,148,204
K 185,186,187
K 186,188,189
Mvaa 200,144,200
W 6
Ta 140,22,41056
K 187,190,191
Mvaa 200,152,208
s_add_u32 s83, s52, 368640
Ta 142,22,46176
Ta 144,22,51296
Mvaa 200,156,212
buffer_store_dwordx4 v[184:187], v1, s[8:11], s83 offen sc1
Ta 146,22,56416
Mvaa 192,156,196
Ta 148,24,10336
Ta 150,24,15456
Mvaa 192,152,192
W 8
Ta 152,24,30816
Ta 154,24,35936
Mvaa 196,128,96
Mvaa 196,132,100
Ta 156,24,41056
Ta 158,24,46176
Mvaa 204,132,116
Mvaa 204,128,112
W 8
Ta 128,23,20608
Ta 130,23,25728
Mvaa 204,136,120
Mvaa 204,140,124
Ta 132,23,51328
Ta 134,23,56448
Mvaa 196,140,108
Mvaa 196,136,104
W 8
Ta 136,25,128
Ta 138,25,5248
Mvaa 196,144,184
Mvaa 196,148,188
Ta 140,21,41088
Ta 142,21,46208
Mvaa 204,148,204
Mvaa 204,144,200
W 8
Ta 144,21,51328
Ta 146,21,56448
Mvaa 204,152,208
Mvaa 204,156,212
Ta 148,23,10368
Ta 150,23,15488
Mvaa 196,156,196
Mvaa 196,152,192
W 8
Ta 152,23,30848
)"
R"(Ta 154,23,35968
Mvaa 208,128,96
Mvaa 208,132,100
Ta 156,23,41088
Ta 158,23,46208
Mvaa 216,132,116
K 192,192,193
Mvaa 216,128,112
W 8
Ta 128,24,20640
Ta 130,24,25760
Mvaa 216,136,120
K 193,194,195
Mvaa 216,140,124
Ta 132,24,51360
K 194,196,197
Mvaa 208,140,108
Ta 134,24,56480
K 195,198,199
Mvaa 208,136,104
W 8
Ta 136,26,160
Ta 138,26,5280
Mvaa 208,144,184
s_add_u32 s83, s52, 335872
Mvaa 208,148,188
buffer_store_dwordx4 v[192:195], v1, s[8:11], s83 offen sc1
K 200,200,201
Mvaa 216,148,204
K 201,202,203
K 202,204,205
Mvaa 216,144,200
W 6
Ta 140,22,41120
K 203,206,207
Mvaa 216,152,208
s_add_u32 s83, s52, 376832
Ta 142,22,46240
Ta 144,22,51360
Mvaa 216,156,212
buffer_store_dwordx4 v[200:203], v1, s[8:11], s83 offen sc1
Ta 146,22,56480
Mvaa 208,156,196
Ta 148,24,10400
Ta 150,24,15520
Mvaa 208,152,192
W 8
Ta 152,24,30880
Ta 154,24,36000
Mvaa 212,128,96
Mvaa 212,132,100
s_waitcnt vmcnt(4)
v_lshlrev_b32 v2, 2, v1
v_mov_b64 v[28:29], s[12:13]
s_mov_b64 exec, 3
buffer_store_dwordx2 v[28:29], v2, s[8:11], s53 offen offset:0 sc1
s_mov_b64 exec, -1
Ta 156,24,41120
Ta 158,24,46240
Mvaa 220,132,116
Mvaa 220,128,112
W 8
Ta 128,23,20672
Ta 130,23,25792
Mvaa 220,136,120
Mvaa 220,140,124
v_xor_b32 v12, s12, v12
v_xor_b32 v13, s13, v13
v_lshrrev_b32 v3, 11, v12
v_or_b32 v13, v13, v3
v_cmp_eq_u32 vcc, 0, v13
s_cmp_eq_u64 vcc, -1
s_cbranch_scc0 .LL_100%=
.LL_101%=:
v_bfe_u32 v13, v12, 4, 7
s_xor_b32 s66, s61, 124
v_cmp_eq_u32 vcc, s61, v13
v_cmp_eq_u32_e64 s[86:87], s66, v13
s_or_b32 s68, vcc_lo, s86
s_and_b32 s68, s68, 0xff
s_cbranch_scc1 .Lup_98%=
.Lupr_99%=:
Ta 132,23,51392
Ta 134,23,56512
Mvaa 212,140,108
Mvaa 212,136,104
W 8
Ta 136,25,192
v_lshlrev_b32 v32, 16, v36
Mvaa 212,144,184
Ta 138,25,5312
v_and_b32 v33, 0xffff0000, v36
Mvaa 212,148,188
Ta 140,21,41152
v_lshlrev_b32 v34, 16, v37
Mvaa 220,148,204
Ta 142,21,46272
v_and_b32 v35, 0xffff0000, v37
Mvaa 220,144,200
W 8
Ta 144,21,51392
v_lshlrev_b32 v36, 16, v38
Mvaa 220,152,208
Ta 146,21,56512
v_and_b32 v37, 0xffff0000, v38
Mvaa 220,156,212
Ta 148,23,10432
v_lshlrev_b32 v38, 16, v39
Mvaa 212,156,196
Ta 150,23,15552
v_and_b32 v39, 0xffff0000, v39
Mvaa 212,152,192
W 8
Ta 152,23,30912
v_lshlrev_b32 v40, 16, v44
Mvaa 224,128,96
Ta 154,23,36032
v_and_b32 v41, 0xffff0000, v44
Mvaa 224,132,100
Ta 156,23,41152
v_lshlrev_b32 v42, 16, v45
Mvaa 232,132,116
Ta 158,23,46272
v_and_b32 v43, 0xffff0000, v45
Mvaa 232,128,112
W 8
K 208,208,209
v_lshlrev_b32 v44, 16, v46
Mvaa 232,136,120
Ta 128,24,20704
Ta 130,24,25824
Mvaa 232,140,124
v_and_b32 v45, 0xffff0000, v46
K 209,210,211
Mvaa 224,140,108
v_lshlrev_b32 v46, 16, v47
K 210,212,213
Mvaa 224,136,104
W 6
Ta 132,24,51424
Ta 134,24,56544
Mvaa 224,144,184
v_and_b32 v47, 0xffff0000, v47
K 211,214,215
Mvaa 224,148,188
s_add_u32 s83, s52, 344064
Ta 136,26,224
v_lshlrev_b32 v48, 16, v52
Mvaa 232,148,204
Ta 138,26,5344
v_and_b32 v49, 0xffff0000, v52
Mvaa 232,144,200
W 6
buffer_store_dwordx4 v[208:211], v1, s[8:11], s83 offen sc1
v_lshlrev_b32 v50, 16, v53
Mvaa 232,152,208
K 216,216,217
K 217,218,219
Mvaa 232,156,212
K 218,220,221
K 219,222,223
Mvaa 224,156,196
s_add_u32 s83, s52, 385024
buffer_store_dwordx4 v[216:219], v1, s[8:11], s83 offen sc1
Ta 140,22,41184
Mvaa 224,152,192
Ta 142,22,46304
W 4
Mvaa 228,128,96
v_and_b32 v51, 0xffff0000, v53
v_lshlrev_b32 v52, 16, v54
Mvaa 228,132,100
Ta 144,22,51424
Ta 146,22,56544
Mvaa 236,132,116
v_and_b32 v53, 0xffff0000, v54
v_lshlrev_b32 v54, 16, v55
Mvaa 236,128,112
Ta 148,24,10464
Ta 150,24,15584
W 4
Mvaa 236,136,120
v_and_b32 v55, 0xffff0000, v55
v_lshlrev_b32 v56, 16, v60
Mvaa 236,140,124
Ta 152,24,30944
Ta 154,24,36064
Mvaa 228,140,108
v_and_b32 v57, 0xffff0000, v60
v_lshlrev_b32 v58, 16, v61
Mvaa 228,136,104
Ta 156,24,41184
Ta 158,24,46304
W 4
Mvaa 228,144,184
v_and_b32 v59, 0xffff0000, v61
v_lshlrev_b32 v60, 16, v62
Mvaa 228,148,188
Ta 128,23,20736
Ta 130,23,25856
Mvaa 236,148,204
v_and_b32 v61, 0xffff0000, v62
v_lshlrev_b32 v62, 16, v63
Mvaa 236,144,200
Ta 132,23,51456
Ta 134,23,56576
W 4
Mvaa 236,152,208
v_and_b32 v63, 0xffff0000, v63
v_lshlrev_b32 v64, 16, v68
Mvaa 236,156,212
Ta 136,25,256
Ta 138,25,5376
Mvaa 228,156,196
v_and_b32 v65, 0xffff0000, v68
v_lshlrev_b32 v66, 16, v69
Mvaa 228,152,192
Ta 140,21,41216
Ta 142,21,46336
W 4
Mvaa 240,128,96
v_and_b32 v67, 0xffff0000, v69
v_lshlrev_b32 v68, 16, v70
Mvaa 240,132,100
Ta 144,21,51456
Ta 146,21,56576
Mvaa 248,132,116
v_and_b32 v69, 0xffff0000, v70
v_lshlrev_b32 v70, 16, v71
Mvaa 248,128,112
Ta 148,23,10496
Ta 150,23,15616
W 4
Mvaa 248,136,120
v_and_b32 v71, 0xffff0000, v71
v_lshlrev_b32 v72, 16, v76
Mvaa 248,140,124
Ta 152,23,30976
Ta 154,23,36096
Mvaa 240,140,108
v_and_b32 v73, 0xffff0000, v76
v_lshlrev_b32 v74, 16, v77
Mvaa 240,136,104
Ta 156,23,41216
Ta 158,23,46336
W 4
Mvaa 240,144,184
v_and_b32 v75, 0xffff0000, v77
K 224,224,225
Mvaa 240,148,188
Ta 128,24,20768
v_lshlrev_b32 v76, 16, v78
Mvaa 248,148,204
Ta 130,24,25888
K 225,226,227
Mvaa 248,144,200
Ta 132,24,51488
Ta 134,24,56608
W 4
Mvaa 248,152,208
v_and_b32 v77, 0xffff0000, v78
K 226,228,229
Mvaa 248,156,212
v_lshlrev_b32 v78, 16, v79
v_and_b32 v79, 0xffff0000, v79
Mvaa 240,156,196
K 227,230,231
s_add_u32 s83, s52, 352256
buffer_store_dwordx4 v[224:227], v1, s[8:11], s83 offen sc1
K 232,232,233
K 233,234,235
K 234,236,237
K 235,238,239
s_add_u32 s83, s52, 393216
buffer_store_dwordx4 v[232:235], v1, s[8:11], s83 offen sc1
Mvaa 240,152,192
Ta 136,26,288
Ta 138,26,5408
Ta 140,22,41248
Ta 142,22,46368
W 4
Mvaa 244,128,96
Ta 144,22,51488
Ta 146,22,56608
Mvaa 244,132,100
Ta 148,24,10528
Ta 150,24,15648
Mvaa 252,132,116
Ta 152,24,31008
Ta 154,24,36128
Mvaa 252,128,112
W 6
Ta 156,24,41248
Ta 158,24,46368
Mvaa 252,136,120
Mvaa 252,140,124
Mvaa 244,140,108
Mvaa 244,136,104
W 4
Mvaa 244,144,184
Mvaa 244,148,188
Mvaa 252,148,204
Mvaa 252,144,200
W 0
Mvaa 252,152,208
Mvaa 252,156,212
Mvaa 244,156,196
Mvaa 244,152,192
K 240,240,241
K 241,242,243
s_nop 5
K 242,244,245
K 243,246,247
s_add_u32 s83, s52, 360448
buffer_store_dwordx4 v[240:243], v1, s[8:11], s83 offen sc1
K 248,248,249
K 249,250,251
K 250,252,253
K 251,254,255
s_add_u32 s83, s52, 401408
buffer_store_dwordx4 v[248:251], v1, s[8:11], s83 offen sc1
.Ltend_83%=:
buffer_load_dwordx2 v[16:17], v18, s[8:11], s63 offen sc1
s_cmp_lg_u32 s68, 0
s_cbranch_scc1 .Lup_103%=
.Lupr_104%=:
s_lshl_b32 s66, 0x101, s59
s_waitcnt vmcnt(1)
s_cmp_eq_u32 s64, 0
s_cbranch_scc0 .LL_109%=
v_lshlrev_b32 v2, 2, v1
v_mov_b64 v[28:29], s[12:13]
s_mov_b64 exec, 3
buffer_store_dwordx2 v[28:29], v2, s[8:11], s53 offen offset:512 sc1
s_mov_b64 exec, -1
.LL_109%=:
s_barrier
s_waitcnt vmcnt(1)
v_cmp_eq_u64 vcc, s[12:13], v[16:17]
s_or_b32 s67, vcc_lo, s66
s_cmp_eq_u32 s14, 0
s_cbranch_scc1 .LL_111%=
s_cmp_eq_u32 s14, 1
s_cbranch_scc1 .LL_112%=
s_mov_b32 s84, 0
.LL_113%=:
s_mov_b32 s70, s71
s_andn2_b32 s69, s67, s71
s_and_b32 s69, s69, 0xffff
s_cbranch_scc0 .LL_116%=
s_and_b32 s80, s69, 255
s_cbranch_scc0 .LL_117%=
s_bitcmp1_b32 s69, 0
s_cbranch_scc0 .LL_118%=
buffer_load_dwordx4 a[0:3], v1, s[8:11], s78 offen offset:0 sc1
.LL_118%=:
s_bitcmp1_b32 s69, 1
s_cbranch_scc0 .LL_119%=
buffer_load_dwordx4 a[4:7], v1, s[8:11], s78 offen offset:1024 sc1
.LL_119%=:
s_bitcmp1_b32 s69, 2
s_cbranch_scc0 .LL_120%=
buffer_load_dwordx4 a[8:11], v1, s[8:11], s78 offen offset:2048 sc1
.LL_120%=:
s_bitcmp1_b32 s69, 3
s_cbranch_scc0 .LL_121%=
buffer_load_dwordx4 a[12:15], v1, s[8:11], s78 offen offset:3072 sc1
.LL_121%=:
s_bitcmp1_b32 s69, 4
s_cbranch_scc0 .LL_122%=
buffer_load_dwordx4 a[16:19], v19, s[8:11], s78 offen offset:0 sc1
.LL_122%=:
s_bitcmp1_b32 s69, 5
s_cbranch_scc0 .LL_123%=
buffer_load_dwordx4 a[20:23], v19, s[8:11], s78 offen offset:1024 sc1
.LL_123%=:
s_bitcmp1_b32 s69, 6
s_cbranch_scc0 .LL_124%=
buffer_load_dwordx4 a[24:27], v19, s[8:11], s78 offen offset:2048 sc1
.LL_124%=:
s_bitcmp1_b32 s69, 7
s_cbranch_scc0 .LL_125%=
buffer_load_dwordx4 a[28:31], v19, s[8:11], s78 offen offset:3072 sc1
.LL_125%=:
.LL_117%=:
s_and_b32 s80, s69, 65280
s_cbranch_scc0 .LL_126%=
s_bitcmp1_b32 s69, 8
s_cbranch_scc0 .LL_127%=
buffer_load_dwordx4 a[32:35], v1, s[8:11], s79 offen offset:0 sc1
.LL_127%=:
s_bitcmp1_b32 s69, 9
s_cbranch_scc0 .LL_128%=
buffer_load_dwordx4 a[36:39], v1, s[8:11], s79 offen offset:1024 sc1
.LL_128%=:
s_bitcmp1_b32 s69, 10
s_cbranch_scc0 .LL_129%=
buffer_load_dwordx4 a[40:43], v1, s[8:11], s79 offen offset:2048 sc1
.LL_129%=:
s_bitcmp1_b32 s69, 11
)"
R"(s_cbranch_scc0 .LL_130%=
buffer_load_dwordx4 a[44:47], v1, s[8:11], s79 offen offset:3072 sc1
.LL_130%=:
s_bitcmp1_b32 s69, 12
s_cbranch_scc0 .LL_131%=
buffer_load_dwordx4 a[48:51], v19, s[8:11], s79 offen offset:0 sc1
.LL_131%=:
s_bitcmp1_b32 s69, 13
s_cbranch_scc0 .LL_132%=
buffer_load_dwordx4 a[52:55], v19, s[8:11], s79 offen offset:1024 sc1
.LL_132%=:
s_bitcmp1_b32 s69, 14
s_cbranch_scc0 .LL_133%=
buffer_load_dwordx4 a[56:59], v19, s[8:11], s79 offen offset:2048 sc1
.LL_133%=:
s_bitcmp1_b32 s69, 15
s_cbranch_scc0 .LL_134%=
buffer_load_dwordx4 a[60:63], v19, s[8:11], s79 offen offset:3072 sc1
.LL_134%=:
.LL_126%=:
s_or_b32 s71, s71, s69
.LL_116%=:
s_andn2_b32 s80, 0xffff, s71
s_cbranch_scc0 .LL_115%=
s_sleep 4
buffer_load_dwordx2 v[16:17], v18, s[8:11], s63 offen sc1
s_and_b32 s80, s70, 0x100ff
s_cmp_eq_u32 s80, 0xff
s_cbranch_scc0 .LL_114%=
Zvva 80,4,0
Zvva 84,8,0
Mvva 80,4,4
Mvva 84,8,4
Mvva 80,4,8
Mvva 84,8,8
Mvva 80,4,12
Mvva 84,8,12
Mvva 80,4,16
Mvva 84,8,16
Mvva 80,4,20
Mvva 84,8,20
Mvva 80,4,24
Mvva 84,8,24
Mvva 80,4,28
Mvva 84,8,28
s_nop 6
K 160,80,81
K 161,82,83
K 162,84,85
K 163,86,87
v_lshlrev_b32 v80, 16, v160
v_and_b32 v81, 0xffff0000, v160
v_lshlrev_b32 v82, 16, v161
v_and_b32 v83, 0xffff0000, v161
v_lshlrev_b32 v84, 16, v162
v_and_b32 v85, 0xffff0000, v162
v_lshlrev_b32 v86, 16, v163
v_and_b32 v87, 0xffff0000, v163
v_pk_add_f32 v[80:81], v[32:33], v[80:81]
v_pk_add_f32 v[82:83], v[34:35], v[82:83]
v_pk_add_f32 v[84:85], v[36:37], v[84:85]
v_pk_add_f32 v[86:87], v[38:39], v[86:87]
K 160,80,81
K 161,82,83
K 162,84,85
K 163,86,87
v_lshlrev_b32 v80, 16, v160
v_and_b32 v81, 0xffff0000, v160
v_lshlrev_b32 v82, 16, v161
v_and_b32 v83, 0xffff0000, v161
v_lshlrev_b32 v84, 16, v162
v_and_b32 v85, 0xffff0000, v162
v_lshlrev_b32 v86, 16, v163
v_and_b32 v87, 0xffff0000, v163
v_pk_add_f32 v[80:81], v[40:41], v[80:81]
v_pk_add_f32 v[82:83], v[42:43], v[82:83]
v_pk_add_f32 v[84:85], v[44:45], v[84:85]
v_pk_add_f32 v[86:87], v[46:47], v[86:87]
K 80,80,81
K 81,82,83
K 82,84,85
K 83,86,87
buffer_store_dwordx4 v[80:83], v20, s[20:23], s72 offen nt
s_bitset1_b32 s71, 16
.LL_114%=:
s_add_u32 s84, s84, 1
s_waitcnt vmcnt(0)
v_cmp_eq_u64 vcc, s[12:13], v[16:17]
s_or_b32 s67, vcc_lo, s66
s_cmp_lt_u32 s84, 4194304
s_cbranch_scc1 .LL_113%=
.LL_115%=:
s_bitcmp1_b32 s71, 16
s_cbranch_scc1 .LL_135%=
s_and_b32 s80, s70, 0xff
s_cmp_eq_u32 s80, 0xff
s_cbranch_scc1 .LL_136%=
s_waitcnt vmcnt(0)
.LL_136%=:
Zvva 80,4,0
Zvva 84,8,0
Mvva 80,4,4
Mvva 84,8,4
Mvva 80,4,8
Mvva 84,8,8
Mvva 80,4,12
Mvva 84,8,12
Mvva 80,4,16
Mvva 84,8,16
Mvva 80,4,20
Mvva 84,8,20
Mvva 80,4,24
Mvva 84,8,24
Mvva 80,4,28
Mvva 84,8,28
s_nop 6
K 160,80,81
K 161,82,83
K 162,84,85
K 163,86,87
v_lshlrev_b32 v80, 16, v160
v_and_b32 v81, 0xffff0000, v160
v_lshlrev_b32 v82, 16, v161
v_and_b32 v83, 0xffff0000, v161
v_lshlrev_b32 v84, 16, v162
v_and_b32 v85, 0xffff0000, v162
v_lshlrev_b32 v86, 16, v163
v_and_b32 v87, 0xffff0000, v163
v_pk_add_f32 v[80:81], v[32:33], v[80:81]
v_pk_add_f32 v[82:83], v[34:35], v[82:83]
v_pk_add_f32 v[84:85], v[36:37], v[84:85]
v_pk_add_f32 v[86:87], v[38:39], v[86:87]
K 160,80,81
K 161,82,83
K 162,84,85
K 163,86,87
v_lshlrev_b32 v80, 16, v160
v_and_b32 v81, 0xffff0000, v160
v_lshlrev_b32 v82, 16, v161
v_and_b32 v83, 0xffff0000, v161
v_lshlrev_b32 v84, 16, v162
v_and_b32 v85, 0xffff0000, v162
v_lshlrev_b32 v86, 16, v163
v_and_b32 v87, 0xffff0000, v163
v_pk_add_f32 v[80:81], v[40:41], v[80:81]
v_pk_add_f32 v[82:83], v[42:43], v[82:83]
v_pk_add_f32 v[84:85], v[44:45], v[84:85]
v_pk_add_f32 v[86:87], v[46:47], v[86:87]
K 80,80,81
K 81,82,83
K 82,84,85
K 83,86,87
buffer_store_dwordx4 v[80:83], v20, s[20:23], s72 offen nt
s_waitcnt vmcnt(1)
s_nop 0
Zvva 80,4,32
Zvva 84,8,32
Mvva 80,4,36
Mvva 84,8,36
Mvva 80,4,40
Mvva 84,8,40
Mvva 80,4,44
Mvva 84,8,44
Mvva 80,4,48
Mvva 84,8,48
Mvva 80,4,52
Mvva 84,8,52
Mvva 80,4,56
Mvva 84,8,56
Mvva 80,4,60
Mvva 84,8,60
s_nop 6
K 160,80,81
K 161,82,83
K 162,84,85
K 163,86,87
v_lshlrev_b32 v80, 16, v160
v_and_b32 v81, 0xffff0000, v160
v_lshlrev_b32 v82, 16, v161
v_and_b32 v83, 0xffff0000, v161
v_lshlrev_b32 v84, 16, v162
v_and_b32 v85, 0xffff0000, v162
v_lshlrev_b32 v86, 16, v163
v_and_b32 v87, 0xffff0000, v163
v_pk_add_f32 v[80:81], v[48:49], v[80:81]
v_pk_add_f32 v[82:83], v[50:51], v[82:83]
v_pk_add_f32 v[84:85], v[52:53], v[84:85]
v_pk_add_f32 v[86:87], v[54:55], v[86:87]
K 160,80,81
K 161,82,83
K 162,84,85
K 163,86,87
v_lshlrev_b32 v80, 16, v160
v_and_b32 v81, 0xffff0000, v160
v_lshlrev_b32 v82, 16, v161
v_and_b32 v83, 0xffff0000, v161
v_lshlrev_b32 v84, 16, v162
v_and_b32 v85, 0xffff0000, v162
v_lshlrev_b32 v86, 16, v163
v_and_b32 v87, 0xffff0000, v163
v_pk_add_f32 v[80:81], v[56:57], v[80:81]
v_pk_add_f32 v[82:83], v[58:59], v[82:83]
v_pk_add_f32 v[84:85], v[60:61], v[84:85]
v_pk_add_f32 v[86:87], v[62:63], v[86:87]
K 80,80,81
K 81,82,83
K 82,84,85
K 83,86,87
buffer_store_dwordx4 v[80:83], v20, s[20:23], s73 offen nt
s_branch .LL_110%=
.LL_135%=:
s_waitcnt vmcnt(0)
Zvva 80,4,32
Zvva 84,8,32
Mvva 80,4,36
Mvva 84,8,36
Mvva 80,4,40
Mvva 84,8,40
Mvva 80,4,44
Mvva 84,8,44
Mvva 80,4,48
Mvva 84,8,48
Mvva 80,4,52
Mvva 84,8,52
Mvva 80,4,56
Mvva 84,8,56
Mvva 80,4,60
Mvva 84,8,60
s_nop 6
K 160,80,81
K 161,82,83
K 162,84,85
K 163,86,87
v_lshlrev_b32 v80, 16, v160
v_and_b32 v81, 0xffff0000, v160
v_lshlrev_b32 v82, 16, v161
v_and_b32 v83, 0xffff0000, v161
v_lshlrev_b32 v84, 16, v162
v_and_b32 v85, 0xffff0000, v162
v_lshlrev_b32 v86, 16, v163
v_and_b32 v87, 0xffff0000, v163
v_pk_add_f32 v[80:81], v[48:49], v[80:81]
v_pk_add_f32 v[82:83], v[50:51], v[82:83]
v_pk_add_f32 v[84:85], v[52:53], v[84:85]
v_pk_add_f32 v[86:87], v[54:55], v[86:87]
K 160,80,81
K 161,82,83
K 162,84,85
K 163,86,87
v_lshlrev_b32 v80, 16, v160
v_and_b32 v81, 0xffff0000, v160
v_lshlrev_b32 v82, 16, v161
v_and_b32 v83, 0xffff0000, v161
v_lshlrev_b32 v84, 16, v162
v_and_b32 v85, 0xffff0000, v162
v_lshlrev_b32 v86, 16, v163
v_and_b32 v87, 0xffff0000, v163
v_pk_add_f32 v[80:81], v[56:57], v[80:81]
v_pk_add_f32 v[82:83], v[58:59], v[82:83]
v_pk_add_f32 v[84:85], v[60:61], v[84:85]
v_pk_add_f32 v[86:87], v[62:63], v[86:87]
K 80,80,81
K 81,82,83
K 82,84,85
K 83,86,87
buffer_store_dwordx4 v[80:83], v20, s[20:23], s73 offen nt
s_branch .LL_110%=
.LL_111%=:
s_mov_b32 s84, 0
.LL_137%=:
s_mov_b32 s70, s71
s_andn2_b32 s69, s67, s71
s_and_b32 s69, s69, 0xffff
s_cbranch_scc0 .LL_140%=
s_and_b32 s80, s69, 255
s_cbranch_scc0 .LL_141%=
s_bitcmp1_b32 s69, 0
s_cbranch_scc0 .LL_142%=
buffer_load_dwordx4 a[0:3], v1, s[8:11], s78 offen offset:0 sc1
buffer_load_dwordx4 a[216:219], v1, s[8:11], s65 offen offset:0 sc1
.LL_142%=:
s_bitcmp1_b32 s69, 1
s_cbranch_scc0 .LL_143%=
buffer_load_dwordx4 a[4:7], v1, s[8:11], s78 offen offset:1024 sc1
buffer_load_dwordx4 a[220:223], v1, s[8:11], s65 offen offset:1024 sc1
.LL_143%=:
s_bitcmp1_b32 s69, 2
s_cbranch_scc0 .LL_144%=
buffer_load_dwordx4 a[8:11], v1, s[8:11], s78 offen offset:2048 sc1
buffer_load_dwordx4 a[224:227], v1, s[8:11], s65 offen offset:2048 sc1
.LL_144%=:
s_bitcmp1_b32 s69, 3
s_cbranch_scc0 .LL_145%=
buffer_load_dwordx4 a[12:15], v1, s[8:11], s78 offen offset:3072 sc1
buffer_load_dwordx4 a[228:231], v1, s[8:11], s65 offen offset:3072 sc1
.LL_145%=:
s_bitcmp1_b32 s69, 4
s_cbranch_scc0 .LL_146%=
buffer_load_dwordx4 a[16:19], v19, s[8:11], s78 offen offset:0 sc1
buffer_load_dwordx4 a[232:235], v19, s[8:11], s65 offen offset:0 sc1
.LL_146%=:
s_bitcmp1_b32 s69, 5
s_cbranch_scc0 .LL_147%=
buffer_load_dwordx4 a[20:23], v19, s[8:11], s78 offen offset:1024 sc1
buffer_load_dwordx4 a[236:239], v19, s[8:11], s65 offen offset:1024 sc1
.LL_147%=:
s_bitcmp1_b32 s69, 6
s_cbranch_scc0 .LL_148%=
buffer_load_dwordx4 a[24:27], v19, s[8:11], s78 offen offset:2048 sc1
buffer_load_dwordx4 a[240:243], v19, s[8:11], s65 offen offset:2048 sc1
.LL_148%=:
s_bitcmp1_b32 s69, 7
s_cbranch_scc0 .LL_149%=
buffer_load_dwordx4 a[28:31], v19, s[8:11], s78 offen offset:3072 sc1
buffer_load_dwordx4 a[244:247], v19, s[8:11], s65 offen offset:3072 sc1
.LL_149%=:
.LL_141%=:
s_and_b32 s80, s69, 65280
s_cbranch_scc0 .LL_150%=
s_bitcmp1_b32 s69, 8
s_cbranch_scc0 .LL_151%=
buffer_load_dwordx4 a[32:35], v1, s[8:11], s79 offen offset:0 sc1
.LL_151%=:
s_bitcmp1_b32 s69, 9
s_cbranch_scc0 .LL_152%=
buffer_load_dwordx4 a[36:39], v1, s[8:11], s79 offen offset:1024 sc1
.LL_152%=:
s_bitcmp1_b32 s69, 10
s_cbranch_scc0 .LL_153%=
buffer_load_dwordx4 a[40:43], v1, s[8:11], s79 offen offset:2048 sc1
.LL_153%=:
s_bitcmp1_b32 s69, 11
s_cbranch_scc0 .LL_154%=
buffer_load_dwordx4 a[44:47], v1, s[8:11], s79 offen offset:3072 sc1
.LL_154%=:
s_bitcmp1_b32 s69, 12
s_cbranch_scc0 .LL_155%=
buffer_load_dwordx4 a[48:51], v19, s[8:11], s79 offen offset:0 sc1
.LL_155%=:
s_bitcmp1_b32 s69, 13
s_cbranch_scc0 .LL_156%=
buffer_load_dwordx4 a[52:55], v19, s[8:11], s79 offen offset:1024 sc1
.LL_156%=:
s_bitcmp1_b32 s69, 14
s_cbranch_scc0 .LL_157%=
buffer_load_dwordx4 a[56:59], v19, s[8:11], s79 offen offset:2048 sc1
.LL_157%=:
s_bitcmp1_b32 s69, 15
s_cbranch_scc0 .LL_158%=
buffer_load_dwordx4 a[60:63], v19, s[8:11], s79 offen offset:3072 sc1
.LL_158%=:
.LL_150%=:
s_or_b32 s71, s71, s69
.LL_140%=:
s_andn2_b32 s80, 0xffff, s71
s_cbranch_scc0 .LL_139%=
s_sleep 4
buffer_load_dwordx2 v[16:17], v18, s[8:11], s63 offen sc1
s_and_b32 s80, s70, 0x100ff
s_cmp_eq_u32 s80, 0xff
s_cbranch_scc0 .LL_138%=
Zvva 80,4,0
Zvva 84,8,0
Zvva 88,4,216
Zvva 92,8,216
Mvva 80,4,4
Mvva 84,8,4
Mvva 88,4,220
Mvva 92,8,220
Mvva 80,4,8
Mvva 84,8,8
Mvva 88,4,224
Mvva 92,8,224
Mvva 80,4,12
Mvva 84,8,12
Mvva 88,4,228
Mvva 92,8,228
Mvva 80,4,16
Mvva 84,8,16
Mvva 88,4,232
Mvva 92,8,232
Mvva 80,4,20
Mvva 84,8,20
Mvva 88,4,236
Mvva 92,8,236
Mvva 80,4,24
Mvva 84,8,24
Mvva 88,4,240
Mvva 92,8,240
Mvva 80,4,28
Mvva 84,8,28
Mvva 88,4,244
Mvva 92,8,244
s_nop 4
K 160,80,81
K 161,82,83
K 162,84,85
K 163,86,87
v_lshlrev_b32 v80, 16, v160
v_and_b32 v81, 0xffff0000, v160
)"
R"(v_lshlrev_b32 v82, 16, v161
v_and_b32 v83, 0xffff0000, v161
v_lshlrev_b32 v84, 16, v162
v_and_b32 v85, 0xffff0000, v162
v_lshlrev_b32 v86, 16, v163
v_and_b32 v87, 0xffff0000, v163
v_pk_add_f32 v[80:81], v[32:33], v[80:81]
v_pk_add_f32 v[82:83], v[34:35], v[82:83]
v_pk_add_f32 v[84:85], v[36:37], v[84:85]
v_pk_add_f32 v[86:87], v[38:39], v[86:87]
K 160,80,81
K 161,82,83
K 162,84,85
K 163,86,87
v_lshlrev_b32 v80, 16, v160
v_and_b32 v81, 0xffff0000, v160
v_lshlrev_b32 v82, 16, v161
v_and_b32 v83, 0xffff0000, v161
v_lshlrev_b32 v84, 16, v162
v_and_b32 v85, 0xffff0000, v162
v_lshlrev_b32 v86, 16, v163
v_and_b32 v87, 0xffff0000, v163
v_pk_add_f32 v[80:81], v[40:41], v[80:81]
v_pk_add_f32 v[82:83], v[42:43], v[82:83]
v_pk_add_f32 v[84:85], v[44:45], v[84:85]
v_pk_add_f32 v[86:87], v[46:47], v[86:87]
K 80,80,81
K 81,82,83
K 82,84,85
K 83,86,87
buffer_store_dwordx4 v[80:83], v20, s[20:23], s72 offen nt
K 160,88,89
K 161,90,91
K 162,92,93
K 163,94,95
v_lshlrev_b32 v88, 16, v160
v_and_b32 v89, 0xffff0000, v160
v_lshlrev_b32 v90, 16, v161
v_and_b32 v91, 0xffff0000, v161
v_lshlrev_b32 v92, 16, v162
v_and_b32 v93, 0xffff0000, v162
v_lshlrev_b32 v94, 16, v163
v_and_b32 v95, 0xffff0000, v163
v_pk_add_f32 v[88:89], v[64:65], v[88:89]
v_pk_add_f32 v[90:91], v[66:67], v[90:91]
v_pk_add_f32 v[92:93], v[68:69], v[92:93]
v_pk_add_f32 v[94:95], v[70:71], v[94:95]
K 160,88,89
K 161,90,91
K 162,92,93
K 163,94,95
v_lshlrev_b32 v88, 16, v160
v_and_b32 v89, 0xffff0000, v160
v_lshlrev_b32 v90, 16, v161
v_and_b32 v91, 0xffff0000, v161
v_lshlrev_b32 v92, 16, v162
v_and_b32 v93, 0xffff0000, v162
v_lshlrev_b32 v94, 16, v163
v_and_b32 v95, 0xffff0000, v163
v_pk_add_f32 v[88:89], v[72:73], v[88:89]
v_pk_add_f32 v[90:91], v[74:75], v[90:91]
v_pk_add_f32 v[92:93], v[76:77], v[92:93]
v_pk_add_f32 v[94:95], v[78:79], v[94:95]
K 88,88,89
K 89,90,91
K 90,92,93
K 91,94,95
buffer_store_dwordx4 v[88:91], v20, s[20:23], s74 offen nt
s_bitset1_b32 s71, 16
.LL_138%=:
s_add_u32 s84, s84, 1
s_waitcnt vmcnt(0)
v_cmp_eq_u64 vcc, s[12:13], v[16:17]
s_or_b32 s67, vcc_lo, s66
s_cmp_lt_u32 s84, 4194304
s_cbranch_scc1 .LL_137%=
.LL_139%=:
s_bitcmp1_b32 s71, 16
s_cbranch_scc1 .LL_159%=
s_and_b32 s80, s70, 0xff
s_cmp_eq_u32 s80, 0xff
s_cbranch_scc1 .LL_160%=
s_waitcnt vmcnt(0)
.LL_160%=:
Zvva 80,4,0
Zvva 84,8,0
Zvva 88,4,216
Zvva 92,8,216
Mvva 80,4,4
Mvva 84,8,4
Mvva 88,4,220
Mvva 92,8,220
Mvva 80,4,8
Mvva 84,8,8
Mvva 88,4,224
Mvva 92,8,224
Mvva 80,4,12
Mvva 84,8,12
Mvva 88,4,228
Mvva 92,8,228
Mvva 80,4,16
Mvva 84,8,16
Mvva 88,4,232
Mvva 92,8,232
Mvva 80,4,20
Mvva 84,8,20
Mvva 88,4,236
Mvva 92,8,236
Mvva 80,4,24
Mvva 84,8,24
Mvva 88,4,240
Mvva 92,8,240
Mvva 80,4,28
Mvva 84,8,28
Mvva 88,4,244
Mvva 92,8,244
s_nop 4
K 160,80,81
K 161,82,83
K 162,84,85
K 163,86,87
v_lshlrev_b32 v80, 16, v160
v_and_b32 v81, 0xffff0000, v160
v_lshlrev_b32 v82, 16, v161
v_and_b32 v83, 0xffff0000, v161
v_lshlrev_b32 v84, 16, v162
v_and_b32 v85, 0xffff0000, v162
v_lshlrev_b32 v86, 16, v163
v_and_b32 v87, 0xffff0000, v163
v_pk_add_f32 v[80:81], v[32:33], v[80:81]
v_pk_add_f32 v[82:83], v[34:35], v[82:83]
v_pk_add_f32 v[84:85], v[36:37], v[84:85]
v_pk_add_f32 v[86:87], v[38:39], v[86:87]
K 160,80,81
K 161,82,83
K 162,84,85
K 163,86,87
v_lshlrev_b32 v80, 16, v160
v_and_b32 v81, 0xffff0000, v160
v_lshlrev_b32 v82, 16, v161
v_and_b32 v83, 0xffff0000, v161
v_lshlrev_b32 v84, 16, v162
v_and_b32 v85, 0xffff0000, v162
v_lshlrev_b32 v86, 16, v163
v_and_b32 v87, 0xffff0000, v163
v_pk_add_f32 v[80:81], v[40:41], v[80:81]
v_pk_add_f32 v[82:83], v[42:43], v[82:83]
v_pk_add_f32 v[84:85], v[44:45], v[84:85]
v_pk_add_f32 v[86:87], v[46:47], v[86:87]
K 80,80,81
K 81,82,83
K 82,84,85
K 83,86,87
buffer_store_dwordx4 v[80:83], v20, s[20:23], s72 offen nt
K 160,88,89
K 161,90,91
K 162,92,93
K 163,94,95
v_lshlrev_b32 v88, 16, v160
v_and_b32 v89, 0xffff0000, v160
v_lshlrev_b32 v90, 16, v161
v_and_b32 v91, 0xffff0000, v161
v_lshlrev_b32 v92, 16, v162
v_and_b32 v93, 0xffff0000, v162
v_lshlrev_b32 v94, 16, v163
v_and_b32 v95, 0xffff0000, v163
v_pk_add_f32 v[88:89], v[64:65], v[88:89]
v_pk_add_f32 v[90:91], v[66:67], v[90:91]
v_pk_add_f32 v[92:93], v[68:69], v[92:93]
v_pk_add_f32 v[94:95], v[70:71], v[94:95]
K 160,88,89
K 161,90,91
K 162,92,93
K 163,94,95
v_lshlrev_b32 v88, 16, v160
v_and_b32 v89, 0xffff0000, v160
v_lshlrev_b32 v90, 16, v161
v_and_b32 v91, 0xffff0000, v161
v_lshlrev_b32 v92, 16, v162
v_and_b32 v93, 0xffff0000, v162
v_lshlrev_b32 v94, 16, v163
v_and_b32 v95, 0xffff0000, v163
v_pk_add_f32 v[88:89], v[72:73], v[88:89]
v_pk_add_f32 v[90:91], v[74:75], v[90:91]
v_pk_add_f32 v[92:93], v[76:77], v[92:93]
v_pk_add_f32 v[94:95], v[78:79], v[94:95]
K 88,88,89
K 89,90,91
K 90,92,93
K 91,94,95
buffer_store_dwordx4 v[88:91], v20, s[20:23], s74 offen nt
s_waitcnt vmcnt(2)
Zvva 80,4,32
Zvva 84,8,32
Mvva 80,4,36
Mvva 84,8,36
Mvva 80,4,40
Mvva 84,8,40
Mvva 80,4,44
Mvva 84,8,44
Mvva 80,4,48
Mvva 84,8,48
Mvva 80,4,52
Mvva 84,8,52
Mvva 80,4,56
Mvva 84,8,56
Mvva 80,4,60
Mvva 84,8,60
s_nop 6
K 160,80,81
K 161,82,83
K 162,84,85
K 163,86,87
v_lshlrev_b32 v80, 16, v160
v_and_b32 v81, 0xffff0000, v160
v_lshlrev_b32 v82, 16, v161
v_and_b32 v83, 0xffff0000, v161
v_lshlrev_b32 v84, 16, v162
v_and_b32 v85, 0xffff0000, v162
v_lshlrev_b32 v86, 16, v163
v_and_b32 v87, 0xffff0000, v163
v_pk_add_f32 v[80:81], v[48:49], v[80:81]
v_pk_add_f32 v[82:83], v[50:51], v[82:83]
v_pk_add_f32 v[84:85], v[52:53], v[84:85]
v_pk_add_f32 v[86:87], v[54:55], v[86:87]
K 160,80,81
K 161,82,83
K 162,84,85
K 163,86,87
v_lshlrev_b32 v80, 16, v160
v_and_b32 v81, 0xffff0000, v160
v_lshlrev_b32 v82, 16, v161
v_and_b32 v83, 0xffff0000, v161
v_lshlrev_b32 v84, 16, v162
v_and_b32 v85, 0xffff0000, v162
v_lshlrev_b32 v86, 16, v163
v_and_b32 v87, 0xffff0000, v163
v_pk_add_f32 v[80:81], v[56:57], v[80:81]
v_pk_add_f32 v[82:83], v[58:59], v[82:83]
v_pk_add_f32 v[84:85], v[60:61], v[84:85]
v_pk_add_f32 v[86:87], v[62:63], v[86:87]
K 80,80,81
K 81,82,83
K 82,84,85
K 83,86,87
buffer_store_dwordx4 v[80:83], v20, s[20:23], s73 offen nt
s_branch .LL_110%=
.LL_159%=:
s_waitcnt vmcnt(0)
Zvva 80,4,32
Zvva 84,8,32
Mvva 80,4,36
Mvva 84,8,36
Mvva 80,4,40
Mvva 84,8,40
Mvva 80,4,44
Mvva 84,8,44
Mvva 80,4,48
Mvva 84,8,48
Mvva 80,4,52
Mvva 84,8,52
Mvva 80,4,56
Mvva 84,8,56
Mvva 80,4,60
Mvva 84,8,60
s_nop 6
K 160,80,81
K 161,82,83
K 162,84,85
K 163,86,87
v_lshlrev_b32 v80, 16, v160
v_and_b32 v81, 0xffff0000, v160
v_lshlrev_b32 v82, 16, v161
v_and_b32 v83, 0xffff0000, v161
v_lshlrev_b32 v84, 16, v162
v_and_b32 v85, 0xffff0000, v162
v_lshlrev_b32 v86, 16, v163
v_and_b32 v87, 0xffff0000, v163
v_pk_add_f32 v[80:81], v[48:49], v[80:81]
v_pk_add_f32 v[82:83], v[50:51], v[82:83]
v_pk_add_f32 v[84:85], v[52:53], v[84:85]
v_pk_add_f32 v[86:87], v[54:55], v[86:87]
K 160,80,81
K 161,82,83
K 162,84,85
K 163,86,87
v_lshlrev_b32 v80, 16, v160
v_and_b32 v81, 0xffff0000, v160
v_lshlrev_b32 v82, 16, v161
v_and_b32 v83, 0xffff0000, v161
v_lshlrev_b32 v84, 16, v162
v_and_b32 v85, 0xffff0000, v162
v_lshlrev_b32 v86, 16, v163
v_and_b32 v87, 0xffff0000, v163
v_pk_add_f32 v[80:81], v[56:57], v[80:81]
v_pk_add_f32 v[82:83], v[58:59], v[82:83]
v_pk_add_f32 v[84:85], v[60:61], v[84:85]
v_pk_add_f32 v[86:87], v[62:63], v[86:87]
K 80,80,81
K 81,82,83
K 82,84,85
K 83,86,87
buffer_store_dwordx4 v[80:83], v20, s[20:23], s73 offen nt
s_branch .LL_110%=
.LL_112%=:
s_mov_b32 s84, 0
.LL_161%=:
s_mov_b32 s70, s71
s_andn2_b32 s69, s67, s71
s_and_b32 s69, s69, 0xffff
s_cbranch_scc0 .LL_164%=
s_and_b32 s80, s69, 255
s_cbranch_scc0 .LL_165%=
s_bitcmp1_b32 s69, 0
s_cbranch_scc0 .LL_166%=
buffer_load_dwordx4 a[0:3], v1, s[8:11], s78 offen offset:0 sc1
.LL_166%=:
s_bitcmp1_b32 s69, 1
s_cbranch_scc0 .LL_167%=
buffer_load_dwordx4 a[4:7], v1, s[8:11], s78 offen offset:1024 sc1
.LL_167%=:
s_bitcmp1_b32 s69, 2
s_cbranch_scc0 .LL_168%=
buffer_load_dwordx4 a[8:11], v1, s[8:11], s78 offen offset:2048 sc1
.LL_168%=:
s_bitcmp1_b32 s69, 3
s_cbranch_scc0 .LL_169%=
buffer_load_dwordx4 a[12:15], v1, s[8:11], s78 offen offset:3072 sc1
.LL_169%=:
s_bitcmp1_b32 s69, 4
s_cbranch_scc0 .LL_170%=
buffer_load_dwordx4 a[16:19], v19, s[8:11], s78 offen offset:0 sc1
.LL_170%=:
s_bitcmp1_b32 s69, 5
s_cbranch_scc0 .LL_171%=
buffer_load_dwordx4 a[20:23], v19, s[8:11], s78 offen offset:1024 sc1
.LL_171%=:
s_bitcmp1_b32 s69, 6
s_cbranch_scc0 .LL_172%=
buffer_load_dwordx4 a[24:27], v19, s[8:11], s78 offen offset:2048 sc1
.LL_172%=:
s_bitcmp1_b32 s69, 7
s_cbranch_scc0 .LL_173%=
buffer_load_dwordx4 a[28:31], v19, s[8:11], s78 offen offset:3072 sc1
.LL_173%=:
.LL_165%=:
s_and_b32 s80, s69, 65280
s_cbranch_scc0 .LL_174%=
s_bitcmp1_b32 s69, 8
s_cbranch_scc0 .LL_175%=
buffer_load_dwordx4 a[32:35], v1, s[8:11], s79 offen offset:0 sc1
buffer_load_dwordx4 a[216:219], v1, s[8:11], s65 offen offset:0 sc1
.LL_175%=:
s_bitcmp1_b32 s69, 9
s_cbranch_scc0 .LL_176%=
buffer_load_dwordx4 a[36:39], v1, s[8:11], s79 offen offset:1024 sc1
buffer_load_dwordx4 a[220:223], v1, s[8:11], s65 offen offset:1024 sc1
.LL_176%=:
s_bitcmp1_b32 s69, 10
s_cbranch_scc0 .LL_177%=
buffer_load_dwordx4 a[40:43], v1, s[8:11], s79 offen offset:2048 sc1
buffer_load_dwordx4 a[224:227], v1, s[8:11], s65 offen offset:2048 sc1
.LL_177%=:
s_bitcmp1_b32 s69, 11
s_cbranch_scc0 .LL_178%=
buffer_load_dwordx4 a[44:47], v1, s[8:11], s79 offen offset:3072 sc1
buffer_load_dwordx4 a[228:231], v1, s[8:11], s65 offen offset:3072 sc1
.LL_178%=:
s_bitcmp1_b32 s69, 12
s_cbranch_scc0 .LL_179%=
buffer_load_dwordx4 a[48:51], v19, s[8:11], s79 offen offset:0 sc1
buffer_load_dwordx4 a[232:235], v19, s[8:11], s65 offen offset:0 sc1
.LL_179%=:
s_bitcmp1_b32 s69, 13
s_cbranch_scc0 .LL_180%=
buffer_load_dwordx4 a[52:55], v19, s[8:11], s79 offen offset:1024 sc1
buffer_load_dwordx4 a[236:239], v19, s[8:11], s65 offen offset:1024 sc1
.LL_180%=:
s_bitcmp1_b32 s69, 14
s_cbranch_scc0 .LL_181%=
buffer_load_dwordx4 a[56:59], v19, s[8:11], s79 offen offset:2048 sc1
buffer_load_dwordx4 a[240:243], v19, s[8:11], s65 offen offset:2048 sc1
.LL_181%=:
s_bitcmp1_b32 s69, 15
s_cbranch_scc0 .LL_182%=
buffer_load_dwordx4 a[60:63], v19, s[8:11], s79 offen offset:3072 sc1
buffer_load_dwordx4 a[244:247], v19, s[8:11], s65 offen offset:3072 sc1
.LL_182%=:
.LL_174%=:
s_or_b32 s71, s71, s69
.LL_164%=:
s_andn2_b32 s80, 0xffff, s71
s_cbranch_scc0 .LL_163%=
s_sleep 4
buffer_load_dwordx2 v[16:17], v18, s[8:11], s63 offen sc1
s_and_b32 s80, s70, 0x100ff
s_cmp_eq_u32 s80, 0xff
s_cbranch_scc0 .LL_162%=
Zvva 80,4,0
Zvva 84,8,0
)"
R"(Mvva 80,4,4
Mvva 84,8,4
Mvva 80,4,8
Mvva 84,8,8
Mvva 80,4,12
Mvva 84,8,12
Mvva 80,4,16
Mvva 84,8,16
Mvva 80,4,20
Mvva 84,8,20
Mvva 80,4,24
Mvva 84,8,24
Mvva 80,4,28
Mvva 84,8,28
s_nop 6
K 160,80,81
K 161,82,83
K 162,84,85
K 163,86,87
v_lshlrev_b32 v80, 16, v160
v_and_b32 v81, 0xffff0000, v160
v_lshlrev_b32 v82, 16, v161
v_and_b32 v83, 0xffff0000, v161
v_lshlrev_b32 v84, 16, v162
v_and_b32 v85, 0xffff0000, v162
v_lshlrev_b32 v86, 16, v163
v_and_b32 v87, 0xffff0000, v163
v_pk_add_f32 v[80:81], v[32:33], v[80:81]
v_pk_add_f32 v[82:83], v[34:35], v[82:83]
v_pk_add_f32 v[84:85], v[36:37], v[84:85]
v_pk_add_f32 v[86:87], v[38:39], v[86:87]
K 160,80,81
K 161,82,83
K 162,84,85
K 163,86,87
v_lshlrev_b32 v80, 16, v160
v_and_b32 v81, 0xffff0000, v160
v_lshlrev_b32 v82, 16, v161
v_and_b32 v83, 0xffff0000, v161
v_lshlrev_b32 v84, 16, v162
v_and_b32 v85, 0xffff0000, v162
v_lshlrev_b32 v86, 16, v163
v_and_b32 v87, 0xffff0000, v163
v_pk_add_f32 v[80:81], v[40:41], v[80:81]
v_pk_add_f32 v[82:83], v[42:43], v[82:83]
v_pk_add_f32 v[84:85], v[44:45], v[84:85]
v_pk_add_f32 v[86:87], v[46:47], v[86:87]
K 80,80,81
K 81,82,83
K 82,84,85
K 83,86,87
buffer_store_dwordx4 v[80:83], v20, s[20:23], s72 offen nt
s_bitset1_b32 s71, 16
.LL_162%=:
s_add_u32 s84, s84, 1
s_waitcnt vmcnt(0)
v_cmp_eq_u64 vcc, s[12:13], v[16:17]
s_or_b32 s67, vcc_lo, s66
s_cmp_lt_u32 s84, 4194304
s_cbranch_scc1 .LL_161%=
.LL_163%=:
s_bitcmp1_b32 s71, 16
s_cbranch_scc1 .LL_183%=
s_and_b32 s80, s70, 0xff
s_cmp_eq_u32 s80, 0xff
s_cbranch_scc1 .LL_184%=
s_waitcnt vmcnt(0)
.LL_184%=:
Zvva 80,4,0
Zvva 84,8,0
Mvva 80,4,4
Mvva 84,8,4
Mvva 80,4,8
Mvva 84,8,8
Mvva 80,4,12
Mvva 84,8,12
Mvva 80,4,16
Mvva 84,8,16
Mvva 80,4,20
Mvva 84,8,20
Mvva 80,4,24
Mvva 84,8,24
Mvva 80,4,28
Mvva 84,8,28
s_nop 6
K 160,80,81
K 161,82,83
K 162,84,85
K 163,86,87
v_lshlrev_b32 v80, 16, v160
v_and_b32 v81, 0xffff0000, v160
v_lshlrev_b32 v82, 16, v161
v_and_b32 v83, 0xffff0000, v161
v_lshlrev_b32 v84, 16, v162
v_and_b32 v85, 0xffff0000, v162
v_lshlrev_b32 v86, 16, v163
v_and_b32 v87, 0xffff0000, v163
v_pk_add_f32 v[80:81], v[32:33], v[80:81]
v_pk_add_f32 v[82:83], v[34:35], v[82:83]
v_pk_add_f32 v[84:85], v[36:37], v[84:85]
v_pk_add_f32 v[86:87], v[38:39], v[86:87]
K 160,80,81
K 161,82,83
K 162,84,85
K 163,86,87
v_lshlrev_b32 v80, 16, v160
v_and_b32 v81, 0xffff0000, v160
v_lshlrev_b32 v82, 16, v161
v_and_b32 v83, 0xffff0000, v161
v_lshlrev_b32 v84, 16, v162
v_and_b32 v85, 0xffff0000, v162
v_lshlrev_b32 v86, 16, v163
v_and_b32 v87, 0xffff0000, v163
v_pk_add_f32 v[80:81], v[40:41], v[80:81]
v_pk_add_f32 v[82:83], v[42:43], v[82:83]
v_pk_add_f32 v[84:85], v[44:45], v[84:85]
v_pk_add_f32 v[86:87], v[46:47], v[86:87]
K 80,80,81
K 81,82,83
K 82,84,85
K 83,86,87
buffer_store_dwordx4 v[80:83], v20, s[20:23], s72 offen nt
s_waitcnt vmcnt(1)
s_nop 0
Zvva 80,4,32
Zvva 84,8,32
Zvva 88,4,216
Zvva 92,8,216
Mvva 80,4,36
Mvva 84,8,36
Mvva 88,4,220
Mvva 92,8,220
Mvva 80,4,40
Mvva 84,8,40
Mvva 88,4,224
Mvva 92,8,224
Mvva 80,4,44
Mvva 84,8,44
Mvva 88,4,228
Mvva 92,8,228
Mvva 80,4,48
Mvva 84,8,48
Mvva 88,4,232
Mvva 92,8,232
Mvva 80,4,52
Mvva 84,8,52
Mvva 88,4,236
Mvva 92,8,236
Mvva 80,4,56
Mvva 84,8,56
Mvva 88,4,240
Mvva 92,8,240
Mvva 80,4,60
Mvva 84,8,60
Mvva 88,4,244
Mvva 92,8,244
s_nop 4
K 160,80,81
K 161,82,83
K 162,84,85
K 163,86,87
v_lshlrev_b32 v80, 16, v160
v_and_b32 v81, 0xffff0000, v160
v_lshlrev_b32 v82, 16, v161
v_and_b32 v83, 0xffff0000, v161
v_lshlrev_b32 v84, 16, v162
v_and_b32 v85, 0xffff0000, v162
v_lshlrev_b32 v86, 16, v163
v_and_b32 v87, 0xffff0000, v163
v_pk_add_f32 v[80:81], v[48:49], v[80:81]
v_pk_add_f32 v[82:83], v[50:51], v[82:83]
v_pk_add_f32 v[84:85], v[52:53], v[84:85]
v_pk_add_f32 v[86:87], v[54:55], v[86:87]
K 160,80,81
K 161,82,83
K 162,84,85
K 163,86,87
v_lshlrev_b32 v80, 16, v160
v_and_b32 v81, 0xffff0000, v160
v_lshlrev_b32 v82, 16, v161
v_and_b32 v83, 0xffff0000, v161
v_lshlrev_b32 v84, 16, v162
v_and_b32 v85, 0xffff0000, v162
v_lshlrev_b32 v86, 16, v163
v_and_b32 v87, 0xffff0000, v163
v_pk_add_f32 v[80:81], v[56:57], v[80:81]
v_pk_add_f32 v[82:83], v[58:59], v[82:83]
v_pk_add_f32 v[84:85], v[60:61], v[84:85]
v_pk_add_f32 v[86:87], v[62:63], v[86:87]
K 80,80,81
K 81,82,83
K 82,84,85
K 83,86,87
buffer_store_dwordx4 v[80:83], v20, s[20:23], s73 offen nt
K 160,88,89
K 161,90,91
K 162,92,93
K 163,94,95
v_lshlrev_b32 v88, 16, v160
v_and_b32 v89, 0xffff0000, v160
v_lshlrev_b32 v90, 16, v161
v_and_b32 v91, 0xffff0000, v161
v_lshlrev_b32 v92, 16, v162
v_and_b32 v93, 0xffff0000, v162
v_lshlrev_b32 v94, 16, v163
v_and_b32 v95, 0xffff0000, v163
v_pk_add_f32 v[88:89], v[64:65], v[88:89]
v_pk_add_f32 v[90:91], v[66:67], v[90:91]
v_pk_add_f32 v[92:93], v[68:69], v[92:93]
v_pk_add_f32 v[94:95], v[70:71], v[94:95]
K 160,88,89
K 161,90,91
K 162,92,93
K 163,94,95
v_lshlrev_b32 v88, 16, v160
v_and_b32 v89, 0xffff0000, v160
v_lshlrev_b32 v90, 16, v161
v_and_b32 v91, 0xffff0000, v161
v_lshlrev_b32 v92, 16, v162
v_and_b32 v93, 0xffff0000, v162
v_lshlrev_b32 v94, 16, v163
v_and_b32 v95, 0xffff0000, v163
v_pk_add_f32 v[88:89], v[72:73], v[88:89]
v_pk_add_f32 v[90:91], v[74:75], v[90:91]
v_pk_add_f32 v[92:93], v[76:77], v[92:93]
v_pk_add_f32 v[94:95], v[78:79], v[94:95]
K 88,88,89
K 89,90,91
K 90,92,93
K 91,94,95
buffer_store_dwordx4 v[88:91], v20, s[20:23], s74 offen nt
s_branch .LL_110%=
.LL_183%=:
s_waitcnt vmcnt(0)
Zvva 80,4,32
Zvva 84,8,32
Zvva 88,4,216
Zvva 92,8,216
Mvva 80,4,36
Mvva 84,8,36
Mvva 88,4,220
Mvva 92,8,220
Mvva 80,4,40
Mvva 84,8,40
Mvva 88,4,224
Mvva 92,8,224
Mvva 80,4,44
Mvva 84,8,44
Mvva 88,4,228
Mvva 92,8,228
Mvva 80,4,48
Mvva 84,8,48
Mvva 88,4,232
Mvva 92,8,232
Mvva 80,4,52
Mvva 84,8,52
Mvva 88,4,236
Mvva 92,8,236
Mvva 80,4,56
Mvva 84,8,56
Mvva 88,4,240
Mvva 92,8,240
Mvva 80,4,60
Mvva 84,8,60
Mvva 88,4,244
Mvva 92,8,244
s_nop 4
K 160,80,81
K 161,82,83
K 162,84,85
K 163,86,87
v_lshlrev_b32 v80, 16, v160
v_and_b32 v81, 0xffff0000, v160
v_lshlrev_b32 v82, 16, v161
v_and_b32 v83, 0xffff0000, v161
v_lshlrev_b32 v84, 16, v162
v_and_b32 v85, 0xffff0000, v162
v_lshlrev_b32 v86, 16, v163
v_and_b32 v87, 0xffff0000, v163
v_pk_add_f32 v[80:81], v[48:49], v[80:81]
v_pk_add_f32 v[82:83], v[50:51], v[82:83]
v_pk_add_f32 v[84:85], v[52:53], v[84:85]
v_pk_add_f32 v[86:87], v[54:55], v[86:87]
K 160,80,81
K 161,82,83
K 162,84,85
K 163,86,87
v_lshlrev_b32 v80, 16, v160
v_and_b32 v81, 0xffff0000, v160
v_lshlrev_b32 v82, 16, v161
v_and_b32 v83, 0xffff0000, v161
v_lshlrev_b32 v84, 16, v162
v_and_b32 v85, 0xffff0000, v162
v_lshlrev_b32 v86, 16, v163
v_and_b32 v87, 0xffff0000, v163
v_pk_add_f32 v[80:81], v[56:57], v[80:81]
v_pk_add_f32 v[82:83], v[58:59], v[82:83]
v_pk_add_f32 v[84:85], v[60:61], v[84:85]
v_pk_add_f32 v[86:87], v[62:63], v[86:87]
K 80,80,81
K 81,82,83
K 82,84,85
K 83,86,87
buffer_store_dwordx4 v[80:83], v20, s[20:23], s73 offen nt
K 160,88,89
K 161,90,91
K 162,92,93
K 163,94,95
v_lshlrev_b32 v88, 16, v160
v_and_b32 v89, 0xffff0000, v160
v_lshlrev_b32 v90, 16, v161
v_and_b32 v91, 0xffff0000, v161
v_lshlrev_b32 v92, 16, v162
v_and_b32 v93, 0xffff0000, v162
v_lshlrev_b32 v94, 16, v163
v_and_b32 v95, 0xffff0000, v163
v_pk_add_f32 v[88:89], v[64:65], v[88:89]
v_pk_add_f32 v[90:91], v[66:67], v[90:91]
v_pk_add_f32 v[92:93], v[68:69], v[92:93]
v_pk_add_f32 v[94:95], v[70:71], v[94:95]
K 160,88,89
K 161,90,91
K 162,92,93
K 163,94,95
v_lshlrev_b32 v88, 16, v160
v_and_b32 v89, 0xffff0000, v160
v_lshlrev_b32 v90, 16, v161
v_and_b32 v91, 0xffff0000, v161
v_lshlrev_b32 v92, 16, v162
v_and_b32 v93, 0xffff0000, v162
v_lshlrev_b32 v94, 16, v163
v_and_b32 v95, 0xffff0000, v163
v_pk_add_f32 v[88:89], v[72:73], v[88:89]
v_pk_add_f32 v[90:91], v[74:75], v[90:91]
v_pk_add_f32 v[92:93], v[76:77], v[92:93]
v_pk_add_f32 v[94:95], v[78:79], v[94:95]
K 88,88,89
K 89,90,91
K 90,92,93
K 91,94,95
buffer_store_dwordx4 v[88:91], v20, s[20:23], s74 offen nt
.LL_110%=:
s_lshl_b32 s80, s15, 2
s_add_u32 s80, s80, s14
s_cmp_eq_u32 s80, 4
s_cbranch_scc0 .LL_185%=
v_mov_b64 v[28:29], s[12:13]
s_mov_b64 exec, 1
buffer_store_dwordx2 v[28:29], off, s[8:11], 0 sc1
s_mov_b64 exec, -1
.LL_185%=:
s_branch .Lend_186%=
.Lup_103%=:
s_waitcnt vmcnt(3)
s_mov_b32 s84, 0
.LL_105%=:
v_min3_u32 v176, v161, v163, v165
v_max3_u32 v177, v161, v163, v165
v_min3_u32 v176, v176, v167, v169
v_max3_u32 v177, v177, v167, v169
v_min3_u32 v176, v176, v171, v173
v_max3_u32 v177, v177, v171, v173
v_min_u32 v176, v176, v175
v_max_u32 v177, v177, v175
v_cmp_eq_u32 vcc, s13, v176
v_cmp_eq_u32_e64 s[86:87], s13, v177
s_and_b64 vcc, vcc, s[86:87]
s_cmp_eq_u64 vcc, -1
s_cbranch_scc1 .LL_106%=
s_add_u32 s84, s84, 1
s_cmp_lt_u32 s84, 1048576
s_cbranch_scc1 .LL_107%=
.LL_106%=:
v_readlane_b32 s70, v12, s69
v_bfe_u32 v2, v1, 4, 4
s_nop 2
s_lshr_b32 s80, s70, 4
s_and_b32 s70, s70, 15
v_cmp_eq_u32 vcc, s70, v2
s_cmp_eq_u32 s80, s61
s_cselect_b64 s[86:87], vcc, 0
s_cselect_b64 s[88:89], 0, vcc
s_bitcmp1_b32 s14, 0
s_cselect_b64 vcc, s[88:89], s[86:87]
v_lshlrev_b32 v176, 16, v160
v_and_b32 v177, 0xffff0000, v160
v_lshlrev_b32 v178, 16, v162
v_and_b32 v179, 0xffff0000, v162
v_lshlrev_b32 v180, 16, v164
v_and_b32 v181, 0xffff0000, v164
v_lshlrev_b32 v182, 16, v166
v_and_b32 v183, 0xffff0000, v166
A 176,176,40
A 177,177,41
A 178,178,42
A 179,179,43
A 180,180,44
A 181,181,45
A 182,182,46
A 183,183,47
)"
R"(K 184,176,177
K 185,178,179
K 186,180,181
K 187,182,183
v_lshlrev_b32 v176, 16, v184
v_and_b32 v177, 0xffff0000, v184
v_lshlrev_b32 v178, 16, v185
v_and_b32 v179, 0xffff0000, v185
v_lshlrev_b32 v180, 16, v186
v_and_b32 v181, 0xffff0000, v186
v_lshlrev_b32 v182, 16, v187
v_and_b32 v183, 0xffff0000, v187
v_cndmask_b32_e64 v40, v40, v176, s[86:87]
v_cndmask_b32_e64 v41, v41, v177, s[86:87]
v_cndmask_b32_e64 v42, v42, v178, s[86:87]
v_cndmask_b32_e64 v43, v43, v179, s[86:87]
v_cndmask_b32_e64 v44, v44, v180, s[86:87]
v_cndmask_b32_e64 v45, v45, v181, s[86:87]
v_cndmask_b32_e64 v46, v46, v182, s[86:87]
v_cndmask_b32_e64 v47, v47, v183, s[86:87]
v_lshlrev_b32 v176, 16, v160
v_and_b32 v177, 0xffff0000, v160
v_lshlrev_b32 v178, 16, v162
v_and_b32 v179, 0xffff0000, v162
v_lshlrev_b32 v180, 16, v164
v_and_b32 v181, 0xffff0000, v164
v_lshlrev_b32 v182, 16, v166
v_and_b32 v183, 0xffff0000, v166
A 176,176,56
A 177,177,57
A 178,178,58
A 179,179,59
A 180,180,60
A 181,181,61
A 182,182,62
A 183,183,63
K 184,176,177
K 185,178,179
K 186,180,181
K 187,182,183
v_lshlrev_b32 v176, 16, v184
v_and_b32 v177, 0xffff0000, v184
v_lshlrev_b32 v178, 16, v185
v_and_b32 v179, 0xffff0000, v185
v_lshlrev_b32 v180, 16, v186
v_and_b32 v181, 0xffff0000, v186
v_lshlrev_b32 v182, 16, v187
v_and_b32 v183, 0xffff0000, v187
v_cndmask_b32_e64 v56, v56, v176, s[88:89]
v_cndmask_b32_e64 v57, v57, v177, s[88:89]
v_cndmask_b32_e64 v58, v58, v178, s[88:89]
v_cndmask_b32_e64 v59, v59, v179, s[88:89]
v_cndmask_b32_e64 v60, v60, v180, s[88:89]
v_cndmask_b32_e64 v61, v61, v181, s[88:89]
v_cndmask_b32_e64 v62, v62, v182, s[88:89]
v_cndmask_b32_e64 v63, v63, v183, s[88:89]
v_lshlrev_b32 v176, 16, v168
v_and_b32 v177, 0xffff0000, v168
v_lshlrev_b32 v178, 16, v170
v_and_b32 v179, 0xffff0000, v170
v_lshlrev_b32 v180, 16, v172
v_and_b32 v181, 0xffff0000, v172
v_lshlrev_b32 v182, 16, v174
v_and_b32 v183, 0xffff0000, v174
A 176,176,72
A 177,177,73
A 178,178,74
A 179,179,75
A 180,180,76
A 181,181,77
A 182,182,78
A 183,183,79
K 184,176,177
K 185,178,179
K 186,180,181
K 187,182,183
v_lshlrev_b32 v176, 16, v184
v_and_b32 v177, 0xffff0000, v184
v_lshlrev_b32 v178, 16, v185
v_and_b32 v179, 0xffff0000, v185
v_lshlrev_b32 v180, 16, v186
v_and_b32 v181, 0xffff0000, v186
v_lshlrev_b32 v182, 16, v187
v_and_b32 v183, 0xffff0000, v187
v_cndmask_b32_e64 v72, v72, v176, vcc
v_cndmask_b32_e64 v73, v73, v177, vcc
v_cndmask_b32_e64 v74, v74, v178, vcc
v_cndmask_b32_e64 v75, v75, v179, vcc
v_cndmask_b32_e64 v76, v76, v180, vcc
v_cndmask_b32_e64 v77, v77, v181, vcc
v_cndmask_b32_e64 v78, v78, v182, vcc
v_cndmask_b32_e64 v79, v79, v183, vcc
s_bitset0_b32 s68, s69
s_cmp_eq_u32 s68, 0
s_cbranch_scc1 .Lupr_104%=
s_ff1_i32_b32 s69, s68
.LL_107%=:
s_cmp_eq_u32 s64, 0
s_cbranch_scc0 .LL_108%=
s_waitcnt vmcnt(0)
v_lshlrev_b32 v2, 2, v1
v_mov_b64 v[28:29], s[12:13]
s_mov_b64 exec, 3
buffer_store_dwordx2 v[28:29], v2, s[8:11], s53 offen offset:512 sc1
s_mov_b64 exec, -1
s_mov_b32 s64, 1
.LL_108%=:
s_mul_i32 s66, s69, 20
s_mul_i32 s67, s58, 5
s_add_u32 s66, s66, s67
s_lshl_b32 s66, s66, 7
s_add_u32 s66, s66, 98304
s_lshl_b32 s67, s14, 7
s_add_u32 s67, s67, s66
buffer_load_dwordx4 v[160:163], v3, s[8:11], s67 offen offset:0 sc1
buffer_load_dwordx4 v[164:167], v3, s[8:11], s67 offen offset:16 sc1
buffer_load_dwordx4 v[168:171], v3, s[8:11], s66 offen offset:512 sc1
buffer_load_dwordx4 v[172:175], v3, s[8:11], s66 offen offset:528 sc1
s_waitcnt vmcnt(0)
s_branch .LL_105%=
.Lspin_56%=:
s_mov_b32 s84, 0
.Llp_58%=:
s_sleep 2
v_mov_b32 v28, s74
buffer_load_dwordx2 v[28:29], v28, s[8:11], 0 offen offset:3584 sc1
s_waitcnt vmcnt(0)
s_add_u32 s84, s84, 1
v_xor_b32 v28, s12, v28
v_xor_b32 v29, s13, v29
v_lshrrev_b32 v248, 11, v28
v_or_b32 v29, v29, v248
v_cmp_eq_u32 vcc, 0, v29
s_or_b64 vcc, vcc, s[72:73]
s_cmp_eq_u64 vcc, -1
s_cbranch_scc1 .Lok_57%=
s_cmp_lt_u32 s84, 1048576
s_cbranch_scc1 .Llp_58%=
s_branch .Lok_57%=
.Lspin_61%=:
s_mov_b32 s84, 0
.Llp_63%=:
s_sleep 2
v_bfe_u32 v28, v1, 4, 2
v_lshlrev_b32 v28, 3, v28
s_lshl_b32 s80, s56, 9
s_add_u32 s80, s80, 4096
buffer_load_dwordx2 v[28:29], v28, s[8:11], s80 offen sc1
s_waitcnt vmcnt(0)
s_add_u32 s84, s84, 1
v_cmp_eq_u64 vcc, s[12:13], v[28:29]
s_cmp_eq_u64 vcc, -1
s_cbranch_scc1 .Lok_62%=
s_cmp_lt_u32 s84, 1048576
s_cbranch_scc1 .Llp_63%=
s_branch .Lok_62%=
.Lspin_64%=:
s_mov_b32 s84, 0
.Llp_66%=:
s_sleep 2
v_bfe_u32 v28, v1, 4, 4
v_lshlrev_b32 v28, 3, v28
s_lshl_b32 s80, s56, 9
s_add_u32 s80, s80, 4096
buffer_load_dwordx2 v[28:29], v28, s[8:11], s80 offen sc1
s_waitcnt vmcnt(0)
s_add_u32 s84, s84, 1
v_cmp_eq_u64 vcc, s[12:13], v[28:29]
s_cmp_eq_u64 vcc, -1
s_cbranch_scc1 .Lok_65%=
s_cmp_lt_u32 s84, 1048576
s_cbranch_scc1 .Llp_66%=
s_branch .Lok_65%=
.Lspin_67%=:
s_mov_b32 s84, 0
.Llp_69%=:
s_sleep 2
v_bfe_u32 v28, v1, 4, 4
v_lshlrev_b32 v28, 3, v28
s_lshl_b32 s80, s56, 9
s_add_u32 s80, s80, 4224
buffer_load_dwordx2 v[28:29], v28, s[8:11], s80 offen sc1
s_waitcnt vmcnt(0)
s_add_u32 s84, s84, 1
v_cmp_eq_u64 vcc, s[12:13], v[28:29]
s_cmp_eq_u64 vcc, -1
s_cbranch_scc1 .Lok_68%=
s_cmp_lt_u32 s84, 1048576
s_cbranch_scc1 .Llp_69%=
s_branch .Lok_68%=
.Lspin_70%=:
s_mov_b32 s84, 0
.Llp_72%=:
s_sleep 2
v_bfe_u32 v28, v1, 4, 4
v_lshlrev_b32 v28, 3, v28
s_lshl_b32 s80, s56, 9
s_add_u32 s80, s80, 4352
buffer_load_dwordx2 v[28:29], v28, s[8:11], s80 offen sc1
s_waitcnt vmcnt(0)
s_add_u32 s84, s84, 1
v_cmp_eq_u64 vcc, s[12:13], v[28:29]
s_cmp_eq_u64 vcc, -1
s_cbranch_scc1 .Lok_71%=
s_cmp_lt_u32 s84, 1048576
s_cbranch_scc1 .Llp_72%=
s_branch .Lok_71%=
.Lspin_73%=:
s_mov_b32 s84, 0
.Llp_75%=:
s_sleep 2
v_bfe_u32 v28, v1, 4, 4
v_lshlrev_b32 v28, 3, v28
s_lshl_b32 s80, s56, 9
s_add_u32 s80, s80, 4480
buffer_load_dwordx2 v[28:29], v28, s[8:11], s80 offen sc1
s_waitcnt vmcnt(0)
s_add_u32 s84, s84, 1
v_cmp_eq_u64 vcc, s[12:13], v[28:29]
s_cmp_eq_u64 vcc, -1
s_cbranch_scc1 .Lok_74%=
s_cmp_lt_u32 s84, 1048576
s_cbranch_scc1 .Llp_75%=
s_branch .Lok_74%=
.Lspin_76%=:
s_mov_b32 s84, 0
.Llp_78%=:
s_sleep 2
buffer_load_dwordx2 v[8:9], v4, s[8:11], 0 offen offset:2560 sc1
buffer_load_dwordx2 v[10:11], v5, s[8:11], 0 offen offset:3072 sc1
buffer_load_dwordx2 v[12:13], v6, s[8:11], 0 offen offset:3584 sc1
s_waitcnt vmcnt(0)
s_add_u32 s84, s84, 1
v_cmp_eq_u64 vcc, s[12:13], v[8:9]
v_cmp_eq_u64_e64 s[86:87], s[12:13], v[10:11]
s_and_b64 vcc, vcc, s[86:87]
s_or_b64 vcc, vcc, s[20:21]
s_cmp_eq_u64 vcc, -1
s_cbranch_scc1 .Lok_77%=
s_cmp_lt_u32 s84, 1048576
s_cbranch_scc1 .Llp_78%=
s_branch .Lok_77%=
.Lmk_79%=:
s_nop 7
v_cndmask_b32_e64 v168, v241, v168, s[72:73]
v_cndmask_b32_e64 v169, v241, v169, s[74:75]
v_cndmask_b32_e64 v170, v241, v170, s[76:77]
v_cndmask_b32_e64 v171, v241, v171, s[78:79]
v_cndmask_b32_e64 v188, v241, v188, s[72:73]
v_cndmask_b32_e64 v189, v241, v189, s[74:75]
v_cndmask_b32_e64 v190, v241, v190, s[76:77]
v_cndmask_b32_e64 v191, v241, v191, s[78:79]
v_mov_b32 v184, v241
v_mov_b32 v185, v241
v_mov_b32 v186, v241
v_mov_b32 v187, v241
s_branch .Lmkr_80%=
.Lspin_84%=:
s_mov_b32 s84, 0
.Llp_86%=:
s_sleep 2
v_bfe_u32 v28, v1, 4, 2
v_lshlrev_b32 v28, 3, v28
buffer_load_dwordx2 v[28:29], v28, s[8:11], s55 offen sc1
s_waitcnt vmcnt(0)
s_add_u32 s84, s84, 1
v_cmp_eq_u64 vcc, s[12:13], v[28:29]
s_cmp_eq_u64 vcc, -1
s_cbranch_scc1 .Lok_85%=
s_cmp_lt_u32 s84, 1048576
s_cbranch_scc1 .Llp_86%=
s_branch .Lok_85%=
.LL_89%=:
s_mov_b32 s84, 0
.LL_91%=:
s_sleep 2
buffer_load_dwordx2 v[12:13], v27, s[8:11], 0 offen offset:3584 sc1
s_waitcnt vmcnt(0)
s_add_u32 s84, s84, 1
v_xor_b32 v12, s12, v12
v_xor_b32 v13, s13, v13
v_lshrrev_b32 v3, 11, v12
v_or_b32 v13, v13, v3
v_cmp_eq_u32 vcc, 0, v13
s_cmp_eq_u64 vcc, -1
s_cbranch_scc1 .LL_90%=
s_cmp_lt_u32 s84, 1048576
s_cbranch_scc1 .LL_91%=
s_branch .LL_90%=
.Lup_87%=:
v_lshrrev_b32 v3, 8, v1
v_lshlrev_b32 v3, 5, v3
s_ff1_i32_b32 s69, s68
s_mul_i32 s66, s69, 20
s_mul_i32 s67, s58, 5
s_add_u32 s66, s66, s67
s_lshl_b32 s66, s66, 7
s_add_u32 s66, s66, 98304
s_lshl_b32 s67, s14, 7
s_add_u32 s67, s67, s66
buffer_load_dwordx4 v[160:163], v3, s[8:11], s67 offen offset:0 sc1
buffer_load_dwordx4 v[164:167], v3, s[8:11], s67 offen offset:16 sc1
buffer_load_dwordx4 v[168:171], v3, s[8:11], s66 offen offset:512 sc1
buffer_load_dwordx4 v[172:175], v3, s[8:11], s66 offen offset:528 sc1
s_branch .Lupr_88%=
.Lspin_92%=:
s_mov_b32 s84, 0
.Llp_94%=:
s_sleep 2
v_bfe_u32 v28, v1, 4, 2
v_lshlrev_b32 v28, 3, v28
buffer_load_dwordx2 v[28:29], v28, s[8:11], s54 offen sc1
s_waitcnt vmcnt(0)
s_add_u32 s84, s84, 1
v_cmp_eq_u64 vcc, s[12:13], v[28:29]
s_cmp_eq_u64 vcc, -1
s_cbranch_scc1 .Lok_93%=
s_cmp_lt_u32 s84, 1048576
s_cbranch_scc1 .Llp_94%=
s_branch .Lok_93%=
.Lspin_95%=:
s_mov_b32 s84, 0
.Llp_97%=:
s_sleep 2
v_bfe_u32 v28, v1, 4, 2
v_lshlrev_b32 v28, 3, v28
buffer_load_dwordx2 v[28:29], v28, s[8:11], s55 offen sc1
s_waitcnt vmcnt(0)
s_add_u32 s84, s84, 1
v_cmp_eq_u64 vcc, s[12:13], v[28:29]
s_cmp_eq_u64 vcc, -1
s_cbranch_scc1 .Lok_96%=
s_cmp_lt_u32 s84, 1048576
s_cbranch_scc1 .Llp_97%=
s_branch .Lok_96%=
.LL_100%=:
s_mov_b32 s84, 0
.LL_102%=:
s_sleep 2
buffer_load_dwordx2 v[12:13], v27, s[8:11], 0 offen offset:3584 sc1
s_waitcnt vmcnt(0)
s_add_u32 s84, s84, 1
v_xor_b32 v12, s12, v12
v_xor_b32 v13, s13, v13
v_lshrrev_b32 v3, 11, v12
v_or_b32 v13, v13, v3
v_cmp_eq_u32 vcc, 0, v13
s_cmp_eq_u64 vcc, -1
s_cbranch_scc1 .LL_101%=
s_cmp_lt_u32 s84, 1048576
s_cbranch_scc1 .LL_102%=
s_branch .LL_101%=
.Lup_98%=:
v_lshrrev_b32 v3, 8, v1
v_lshlrev_b32 v3, 5, v3
s_ff1_i32_b32 s69, s68
s_mul_i32 s66, s69, 20
s_mul_i32 s67, s58, 5
s_add_u32 s66, s66, s67
s_lshl_b32 s66, s66, 7
s_add_u32 s66, s66, 98304
s_lshl_b32 s67, s14, 7
s_add_u32 s67, s67, s66
buffer_load_dwordx4 v[160:163], v3, s[8:11], s67 offen offset:0 sc1
buffer_load_dwordx4 v[164:167], v3, s[8:11], s67 offen offset:16 sc1
buffer_load_dwordx4 v[168:171], v3, s[8:11], s66 offen offset:512 sc1
buffer_load_dwordx4 v[172:175], v3, s[8:11], s66 offen offset:528 sc1
s_branch .Lupr_99%=
.Lend_186%=:
.Lend%=:
.purgem W
.purgem D
.purgem Ta
.purgem Ra
.purgem Zvaa
.purgem Mvaa
.purgem Zaaa
.purgem Maaa
.purgem A
.purgem U
.purgem K
.purgem Rv
.purgem Mvav
.purgem G
.purgem Zvav
.purgem Cvav
.purgem X
.purgem Cvaa
.purgem Dc
.purgem Maav
.purgem Mvvv
.purgem Zvva
.purgem Mvva
)"
    : : [ka] "s"(__builtin_amdgcn_kernarg_segment_ptr()), [bid] "s"(blockIdx.x), [tid] "v"(threadIdx.x)
    : "v1", "v2", "v3", "v4", "v5", "v6", "v7", "v8", "v9", "v10", "v11", "v12", "v13", "v14", "v15", "v16", "v17", "v18", "v19", "v20", "v21", "v22", "v23", "v24", "v25", "v26", "v27", "v28", "v29", "v30", "v31", "v32", "v33", "v34", "v35", "v36", "v37", "v38", "v39", "v40", "v41", "v42", "v43", "v44", "v45", "v46", "v47", "v48", "v49", "v50", "v51", "v52", "v53", "v54", "v55", "v56", "v57", "v58", "v59", "v60", "v61", "v62", "v63", "v64", "v65", "v66", "v67", "v68", "v69", "v70", "v71", "v72", "v73", "v74", "v75", "v76", "v77", "v78", "v79", "v80", "v81", "v82", "v83", "v84", "v85", "v86", "v87", "v88", "v89", "v90", "v91", "v92", "v93", "v94", "v95", "v96", "v97", "v98", "v99", "v100", "v101", "v102", "v103", "v104", "v105", "v106", "v107", "v108", "v109", "v110", "v111", "v112", "v113", "v114", "v115", "v116", "v117", "v118", "v119", "v120", "v121", "v122", "v123", "v124", "v125", "v126", "v127", "v128", "v129", "v130", "v131", "v132", "v133", "v134", "v135", "v136", "v137", "v138", "v139", "v140", "v141", "v142", "v143", "v144", "v145", "v146", "v147", "v148", "v149", "v150", "v151", "v152", "v153", "v154", "v155", "v156", "v157", "v158", "v159", "v160", "v161", "v162", "v163", "v164", "v165", "v166", "v167", "v168", "v169", "v170", "v171", "v172", "v173", "v174", "v175", "v176", "v177", "v178", "v179", "v180", "v181", "v182", "v183", "v184", "v185", "v186", "v187", "v188", "v189", "v190", "v191", "v192", "v193", "v194", "v195", "v196", "v197", "v198", "v199", "v200", "v201", "v202", "v203", "v204", "v205", "v206", "v207", "v208", "v209", "v210", "v211", "v212", "v213", "v214", "v215", "v216", "v217", "v218", "v219", "v220", "v221", "v222", "v223", "v224", "v225", "v226", "v227", "v228", "v229", "v230", "v231", "v232", "v233", "v234", "v235", "v236", "v237", "v238", "v239", "v240", "v241", "v242", "v243", "v244", "v245", "v246", "v247", "v248", "v249", "v250", "v251", "v252", "v253", "v254", "v255", "a0", "a1", "a2", "a3", "a4", "a5", "a6", "a7", "a8", "a9", "a10", "a11", "a12", "a13", "a14", "a15", "a16", "a17", "a18", "a19", "a20", "a21", "a22", "a23", "a24", "a25", "a26", "a27", "a28", "a29", "a30", "a31", "a32", "a33", "a34", "a35", "a36", "a37", "a38", "a39", "a40", "a41", "a42", "a43", "a44", "a45", "a46", "a47", "a48", "a49", "a50", "a51", "a52", "a53", "a54", "a55", "a56", "a57", "a58", "a59", "a60", "a61", "a62", "a63", "a64", "a65", "a66", "a67", "a68", "a69", "a70", "a71", "a72", "a73", "a74", "a75", "a76", "a77", "a78", "a79", "a80", "a81", "a82", "a83", "a84", "a85", "a86", "a87", "a88", "a89", "a90", "a91", "a92", "a93", "a94", "a95", "a96", "a97", "a98", "a99", "a100", "a101", "a102", "a103", "a104", "a105", "a106", "a107", "a108", "a109", "a110", "a111", "a112", "a113", "a114", "a115", "a116", "a117", "a118", "a119", "a120", "a121", "a122", "a123", "a124", "a125", "a126", "a127", "a128", "a129", "a130", "a131", "a132", "a133", "a134", "a135", "a136", "a137", "a138", "a139", "a140", "a141", "a142", "a143", "a144", "a145", "a146", "a147", "a148", "a149", "a150", "a151", "a152", "a153", "a154", "a155", "a156", "a157", "a158", "a159", "a160", "a161", "a162", "a163", "a164", "a165", "a166", "a167", "a168", "a169", "a170", "a171", "a172", "a173", "a174", "a175", "a176", "a177", "a178", "a179", "a180", "a181", "a182", "a183", "a184", "a185", "a186", "a187", "a188", "a189", "a190", "a191", "a192", "a193", "a194", "a195", "a196", "a197", "a198", "a199", "a200", "a201", "a202", "a203", "a204", "a205", "a206", "a207", "a208", "a209", "a210", "a211", "a212", "a213", "a214", "a215", "a216", "a217", "a218", "a219", "a220", "a221", "a222", "a223", "a224", "a225", "a226", "a227", "a228", "a229", "a230", "a231", "a232", "a233", "a234", "a235", "a236", "a237", "a238", "a239", "a240", "a241", "a242", "a243", "a244", "a245", "a246", "a247", "a248", "a249", "a250", "a251", "a252", "a253", "a254", "a255", "s4", "s5", "s6", "s7", "s8", "s9", "s10", "s11", "s12", "s13", "s14", "s15", "s16", "s17", "s18", "s19", "s20", "s21", "s22", "s23", "s24", "s25", "s26", "s27", "s28", "s29", "s30", "s31", "s32", "s33", "s34", "s35", "s36", "s37", "s38", "s39", "s40", "s41", "s42", "s43", "s44", "s45", "s46", "s47", "s48", "s49", "s50", "s51", "s52", "s53", "s54", "s55", "s56", "s57", "s58", "s59", "s60", "s61", "s62", "s63", "s64", "s65", "s66", "s67", "s68", "s69", "s70", "s71", "s72", "s73", "s74", "s75", "s76", "s77", "s78", "s79", "s80", "s81", "s82", "s83", "s84", "s85", "s86", "s87", "s88", "s89", "s90", "s91", "s92", "s93", "s94", "s95", "s96", "s97", "s98", "s99", "vcc", "scc", "memory"
  );
}
extern "C" void kernel_launch(void* const* d_in, const int* in_sizes, int n_in, void* d_out, int out_size, void* d_ws, size_t ws_size, hipStream_t stream) {
  (void)hipFuncSetAttribute((const void*)mega_kernel, hipFuncAttributeMaxDynamicSharedMemorySize, LDS_BYTES);
  mega_kernel<<<256, 256, LDS_BYTES, stream>>>((const bf16*)d_in[0], (const bf16*)d_in[1], (const bf16*)d_in[2], (const bf16*)d_in[3], (const bf16*)d_in[4], (const bf16*)d_in[5],
      (const bf16*)d_in[7], (const bf16*)d_in[8], (const bf16*)d_in[9], (const bf16*)d_in[10], (char*)d_ws + 0, (bf16*)d_out);
}
```

```cpp
#include <hip/hip_runtime.h>
#include <hip/hip_bf16.h>
typedef __hip_bfloat16 bf16;
constexpr int LDS_BYTES = 163840;
__global__ __launch_bounds__(256, 1) void mega_kernel(const bf16* x, const bf16* ckv, const bf16* wq, const bf16* wuk, const bf16* wuv, const bf16* wo,
    const bf16* w_r, const bf16* wg, const bf16* wu, const bf16* wd, char* ws, bf16* out) {
  asm volatile(
R"(.macro W n
s_waitcnt lgkmcnt(\n)
.endm
.macro D v,r,s,o
buffer_load_dwordx4 v\v, s[\r:\r+3], s\s offen offset:\o lds
.endm
.macro Ta d,a,o
ds_read_b64_tr_b16 a[\d:\d+1], v\a offset:\o
.endm
.macro Ra d,a,o
ds_read_b128 a[\d:\d+3], v\a offset:\o
.endm
.macro Zvaa d,a,b
v_mfma_f32_16x16x32_bf16 v[\d:\d+3], a[\a:\a+3], a[\b:\b+3], 0
.endm
.macro Mvaa d,a,b
v_mfma_f32_16x16x32_bf16 v[\d:\d+3], a[\a:\a+3], a[\b:\b+3], v[\d:\d+3]
.endm
.macro Zaaa d,a,b
v_mfma_f32_16x16x32_bf16 a[\d:\d+3], a[\a:\a+3], a[\b:\b+3], 0
.endm
.macro Maaa d,a,b
v_mfma_f32_16x16x32_bf16 a[\d:\d+3], a[\a:\a+3], a[\b:\b+3], a[\d:\d+3]
.endm
.macro A d,a,b
v_add_f32 v\d, v\a, v\b
.endm
.macro U d,a,b
v_mul_f32 v\d, v\a, v\b
.endm
.macro K d,a,b
v_cvt_pk_bf16_f32 v\d, v\a, v\b
.endm
.macro Rv d,a,o
ds_read_b128 v[\d:\d+3], v\a offset:\o
.endm
.macro Mvav d,a,b
v_mfma_f32_16x16x32_bf16 v[\d:\d+3], a[\a:\a+3], v[\b:\b+3], v[\d:\d+3]
.endm
.macro G d,a
v_accvgpr_read_b32 v\d, a\a
.endm
.macro Zvav d,a,b
v_mfma_f32_16x16x32_bf16 v[\d:\d+3], a[\a:\a+3], v[\b:\b+3], 0
.endm
.macro Cvav d,a,b,c
v_mfma_f32_16x16x32_bf16 v[\d:\d+3], a[\a:\a+3], v[\b:\b+3], v[\c:\c+3]
.endm
.macro X d
v_exp_f32 v\d, v\d
.endm
.macro Cvaa d,a,b,c
v_mfma_f32_16x16x32_bf16 v[\d:\d+3], a[\a:\a+3], a[\b:\b+3], v[\c:\c+3]
.endm
.macro Dc v,r,s,o
buffer_load_dwordx4 v\v, s[\r:\r+3], s\s offen offset:\o sc1 lds
.endm
.macro Maav d,a,b
v_mfma_f32_16x16x32_bf16 a[\d:\d+3], a[\a:\a+3], v[\b:\b+3], a[\d:\d+3]
.endm
.macro Mvvv d,a,b
v_mfma_f32_16x16x32_bf16 v[\d:\d+3], v[\a:\a+3], v[\b:\b+3], v[\d:\d+3]
.endm
.macro Zvva d,a,b
v_mfma_f32_16x16x32_bf16 v[\d:\d+3], v[\a:\a+3], a[\b:\b+3], 0
.endm
.macro Mvva d,a,b
v_mfma_f32_16x16x32_bf16 v[\d:\d+3], v[\a:\a+3], a[\b:\b+3], v[\d:\d+3]
.endm
s_load_dwordx16 s[16:31], %[ka], 0
s_load_dwordx8 s[32:39], %[ka], 64
s_getpc_b64 s[6:7]
.Lbeg%=:
s_mov_b32 s15, %[bid]
v_lshrrev_b32 v2, 6, %[tid]
v_and_b32 v100, 63, %[tid]
v_readfirstlane_b32 s14, v2
v_lshlrev_b32 v1, 4, v100
v_lshrrev_b32 v101, 4, v100
v_and_b32 v102, 15, v100
s_and_b32 s59, s15, 7
s_bfe_u32 s57, s15, 0x30003
s_lshr_b32 s58, s15, 6
s_lshl_b32 s56, s59, 2
s_add_u32 s56, s56, s58
s_lshr_b32 s80, s15, 3
s_lshl_b32 s80, s80, 2
s_add_u32 s80, s80, s14
s_lshl_b32 s80, s80, 11
v_min_u32 v146, 240, v1
v_lshl_add_u32 v146, v146, 3, s80
v_min_u32 v146, .Lend%=-.Lbeg%=-4, v146
global_load_dword v142, v146, s[6:7]
v_and_b32 v88, 0x1f0, v1
v_lshrrev_b32 v105, 1, v101
v_mul_u32_u24 v105, 1280, v105
v_add_u32 v28, v88, v105
s_lshl_b32 s85, s15, 2
s_add_u32 s85, s85, s14
s_mul_i32 s84, s85, 2560
v_and_b32 v105, 1, v101
v_lshl_or_b32 v29, v105, 31, v28
v_lshlrev_b32 v104, 1, v101
v_xor_b32 v104, v104, v102
v_lshlrev_b32 v104, 4, v104
v_lshl_add_u32 v16, v101, 13, v104
v_xor_b32 v105, 128, v16
v_add_u32 v17, 31744, v16
v_add_u32 v18, 63488, v105
v_add_u32 v19, 95232, v105
v_add_u32 v20, 131072, v16
v_add_u32 v21, 162816, v16
v_add_u32 v22, 194560, v105
v_add_u32 v23, 226304, v105
s_lshl_b32 s80, s14, 3
s_add_u32 s80, s80, s57
s_lshl_b32 s80, s80, 15
v_and_b32 v104, 7, v100
v_xor_b32 v104, v104, v101
v_lshlrev_b32 v104, 4, v104
v_lshrrev_b32 v105, 3, v100
v_lshl_add_u32 v104, v105, 10, v104
v_add_u32 v2, s80, v104
v_xor_b32 v105, 64, v2
v_add_u32 v3, 7168, v105
v_add_u32 v4, 14336, v2
v_add_u32 v5, 21504, v105
v_xor_b32 v6, 1835008, v2
v_xor_b32 v7, 1835008, v3
v_xor_b32 v8, 1835008, v4
v_xor_b32 v9, 1835008, v5
s_lshl_b32 s63, s14, 13
s_add_u32 s61, s63, 65536
s_and_b32 s80, s14, 1
s_lshr_b32 s81, s14, 1
s_lshl_b32 s62, s80, 14
s_lshl_b32 s82, s81, 13
s_add_u32 s62, s62, s82
s_lshl_b32 s81, s81, 18
s_lshl_b32 s83, s56, 8
s_add_u32 s81, s81, s83
s_mov_b32 s64, 0
s_cmp_eq_u32 s80, 0
W 0
s_cselect_b32 s44, s22, s24
s_cselect_b32 s45, s23, s25
s_cmp_eq_u32 s14, 3
s_cbranch_scc0 .LL_1%=
s_lshr_b32 s88, s15, 3
s_cmp_lt_u32 s88, 14
s_cbranch_scc0 .LL_1%=
v_lshlrev_b32 v104, 16, v1
s_mov_b64 s[86:87], s[36:37]
s_mov_b32 s89, 38797312
s_cmp_eq_u32 s88, 1
s_cselect_b32 s90, 39845888, 0
s_add_u32 s86, s86, s90
s_addc_u32 s87, s87, 0
s_cmp_eq_u32 s88, 2
s_cselect_b64 s[86:87], s[26:27], s[86:87]
s_cselect_b32 s89, 5242876, s89
s_cmp_eq_u32 s88, 3
s_cselect_b64 s[86:87], s[30:31], s[86:87]
s_cselect_b32 s89, 2211836, s89
s_cmp_eq_u32 s88, 4
s_cselect_b64 s[86:87], s[32:33], s[86:87]
s_cselect_b32 s89, 2211836, s89
s_cmp_eq_u32 s88, 5
s_cselect_b64 s[86:87], s[34:35], s[86:87]
s_cselect_b32 s89, 2211836, s89
s_cmp_eq_u32 s88, 6
s_cselect_b64 s[86:87], s[38:39], s[86:87]
s_cselect_b32 s89, 2621436, s89
s_cmp_eq_u32 s88, 7
s_cselect_b64 s[86:87], s[20:21], s[86:87]
s_cselect_b32 s89, 5242876, s89
s_cmp_eq_u32 s88, 8
s_cselect_b64 s[86:87], s[18:19], s[86:87]
s_cselect_b32 s89, 2097148, s89
s_cmp_eq_u32 s88, 9
s_cselect_b64 s[86:87], s[22:23], s[86:87]
s_cselect_b32 s89, 4194300, s89
s_cmp_eq_u32 s88, 10
s_cselect_b64 s[86:87], s[24:25], s[86:87]
s_cselect_b32 s89, 4194300, s89
s_cmp_eq_u32 s88, 11
s_cselect_b64 s[86:87], s[16:17], s[86:87]
s_cselect_b32 s89, 2621436, s89
s_cmp_eq_u32 s88, 12
s_cselect_b64 s[86:87], s[28:29], s[86:87]
s_cselect_b32 s89, 1276, s89
s_cmp_eq_u32 s88, 13
s_cselect_b64 s[86:87], s[6:7], s[86:87]
s_cselect_b32 s89, 0, s89
v_min_u32 v104, s89, v104
global_load_dword v142, v104, s[86:87] sc1
.LL_1%=:
s_mov_b32 s40, s18
s_and_b32 s41, s19, 0xffff
s_mov_b32 s42, 0x200000
s_mov_b32 s43, 0x20000
s_add_u32 s44, s44, s81
s_addc_u32 s45, s45, 0
s_and_b32 s45, s45, 0xffff
s_mov_b32 s46, 0x400000
s_mov_b32 s47, 0x20000
s_sub_u32 s46, s46, s81
s_load_dwordx2 s[12:13], s[36:37], 0
s_lshl_b32 s65, s64, 12
s_add_u32 m0, s62, 0
s_nop 0
D 16,44,65,0
D 17,44,65,1024
D 18,44,65,2048
D 19,44,65,3072
s_add_u32 m0, s62, 4096
s_nop 0
D 20,44,65,0
D 21,44,65,1024
D 22,44,65,2048
D 23,44,65,3072
s_add_u32 m0, s61, 0
s_nop 0
D 2,40,64,0
D 3,40,64,1024
D 4,40,64,2048
D 5,40,64,3072
s_add_u32 m0, s61, 4096
s_nop 0
D 6,40,64,0
D 7,40,64,1024
D 8,40,64,2048
D 9,40,64,3072
s_mov_b32 s52, s16
s_and_b32 s53, s17, 0xffff
s_mov_b32 s54, 2621440
s_mov_b32 s55, 0x20000
buffer_load_dwordx4 v[32:35], v28, s[52:55], s84 offen
buffer_load_dwordx4 v[36:39], v28, s[52:55], s84 offen offset:512
buffer_load_dwordx4 v[40:43], v29, s[52:55], s84 offen offset:1024
s_mov_b32 s52, s28
s_and_b32 s53, s29, 0xffff
s_mov_b32 s54, 1280
s_mov_b32 s55, 0x20000
buffer_load_dwordx4 v[44:47], v88, s[52:55], 0 offen offset:0
buffer_load_dwordx4 v[48:51], v88, s[52:55], 0 offen offset:512
buffer_load_dwordx4 v[52:55], v88, s[52:55], 0 offen offset:1024
s_mov_b32 s69, 0
s_cmp_eq_u32 s14, 2
s_cbranch_scc1 .LL_2%=
s_cmp_eq_u32 s14, 3
s_cbranch_scc1 .LL_3%=
s_lshl_b32 s80, s58, 3
s_add_u32 s80, s80, s57
s_lshl_b32 s80, s80, 15
s_lshl_b32 s82, s14, 7
s_add_u32 s80, s80, s82
s_add_u32 s80, s80, 256
v_lshl_add_u32 v104, v88, 6, s80
v_and_b32 v105, 512, v1
v_mul_u32_u24 v105, 3584, v105
v_xor_b32 v104, v104, v105
v_mov_b32 v140, s18
v_mov_b32 v141, s19
s_mov_b32 s68, 256
s_branch .LL_4%=
.LL_2%=:
v_lshrrev_b32 v104, 6, v1
v_lshlrev_b32 v104, 13, v104
v_and_b32 v105, 16, v1
v_lshl_add_u32 v104, v105, 3, v104
s_lshl_b32 s80, s57, 17
s_add_u32 s80, s80, s83
s_add_u32 s80, s80, 1048576
v_add_u32 v104, s80, v104
v_and_b32 v105, 32, v1
v_cmp_eq_u32 vcc, 0, v105
v_mov_b32 v140, s24
v_mov_b32 v141, s25
v_mov_b32 v105, s22
v_mov_b32 v106, s23
v_cndmask_b32 v140, v140, v105, vcc
v_cndmask_b32 v141, v141, v106, vcc
s_mov_b32 s68, 1048576
s_branch .LL_4%=
.LL_3%=:
v_lshrrev_b32 v104, 5, v1
v_lshlrev_b32 v104, 13, v104
v_and_b32 v105, 16, v1
v_lshl_add_u32 v104, v105, 3, v104
s_mul_i32 s80, s57, 655360
s_add_u32 s80, s80, s83
v_add_u32 v104, s80, v104
v_mov_b32 v140, s20
v_mov_b32 v141, s21
s_mov_b32 s68, 196608
v_mov_b32 v104, 0
s_mov_b32 s68, 0
.LL_4%=:
v_mov_b32 v105, 0
v_lshl_add_u64 v[140:141], v[104:105], 0, v[140:141]
s_add_u32 s64, s64, 128
s_and_b32 s64, s64, 1023
s_lshl_b32 s65, s64, 12
s_add_u32 m0, s62, 32768
s_nop 0
D 16,44,65,0
D 17,44,65,1024
D 18,44,65,2048
D 19,44,65,3072
s_add_u32 m0, s62, 36864
s_nop 0
D 20,44,65,0
D 21,44,65,1024
D 22,44,65,2048
D 23,44,65,3072
v_lshrrev_b32 v104, 1, v102
v_xor_b32 v104, v104, v101
v_lshlrev_b32 v104, 4, v104
v_lshl_add_u32 v104, v102, 7, v104
s_lshr_b32 s81, s14, 1
s_lshl_b32 s81, s81, 14
s_add_u32 s81, s81, 65536
v_add_u32 v26, s81, v104
v_xor_b32 v27, 64, v26
v_and_b32 v105, 1, v101
v_lshrrev_b32 v104, 2, v102
v_lshl_or_b32 v105, v105, 2, v104
v_lshl_add_u32 v104, v101, 3, v104
v_lshlrev_b32 v104, 8, v104
v_lshl_add_u32 v104, v105, 5, v104
v_and_b32 v105, 3, v102
v_lshl_add_u32 v10, v105, 3, v104
v_xor_b32 v11, 32, v10
v_xor_b32 v12, 64, v10
v_xor_b32 v13, 96, v10
v_xor_b32 v14, 128, v10
v_xor_b32 v15, 160, v10
v_xor_b32 v24, 192, v10
v_xor_b32 v25, 224, v10
.LL_8%=:
s_lshr_b32 s81, s15, 3
s_cmp_eq_u32 s14, 2
s_cbranch_scc1 .LL_5%=
s_cmp_eq_u32 s14, 3
s_cbranch_scc1 .LL_6%=
s_cmp_eq_u32 s14, 0
s_cselect_b64 s[76:77], s[30:31], s[32:33]
s_mov_b32 s72, 512
s_mov_b32 s73, 24
s_mul_i32 s74, s81, 69120
s_mul_i32 s82, s59, 8640
s_andn2_b32 s82, s82, 127
s_add_u32 s74, s74, s82
s_mov_b32 s75, 2211836
s_branch .LL_7%=
.LL_5%=:
s_mov_b64 s[76:77], s[34:35]
s_mov_b32 s72, 512
s_mov_b32 s73, 24
s_mul_i32 s74, s81, 8960
s_mul_i32 s82, s59, 286720
s_add_u32 s74, s74, s82
s_mov_b32 s75, 2211836
s_branch .LL_7%=
.LL_6%=:
s_mov_b64 s[76:77], s[26:27]
s_mov_b32 s72, 512
s_mov_b32 s73, 4
s_mul_i32 s74, s81, 20480
s_mul_i32 s82, s59, 655360
s_add_u32 s74, s74, s82
s_mov_b32 s75, 5242876
s_branch .LL_7%=
.LL_7%=:
s_mov_b32 s8, s36
s_and_b32 s9, s37, 0xffff
s_mov_b32 s10, 79691776
s_mov_b32 s11, 0x20000
s_add_u32 s48, s20, s83
s_addc_u32 s49, s21, 0
s_and_b32 s49, s49, 0xffff
s_mov_b32 s50, 5242880
s_mov_b32 s51, 0x20000
s_sub_u32 s50, s50, s83
s_lshl_b32 s80, s57, 1
s_and_b32 s81, s14, 1
s_add_u32 s80, s80, s81
s_sub_u32 s81, 31, s80
s_cmp_lt_u32 s14, 2
s_cselect_b32 s60, s80, s81
s_mul_i32 s28, s60, 81920
s_add_u32 s28, s28, 131072
s_add_u32 s4, s28, 10240
s_mov_b32 s80, 0
s_lshl_b32 s81, s80, 9
s_add_u32 s28, s28, s81
s_add_u32 s5, s80, s14
s_cmp_ge_u32 s5, 20
s_cselect_b32 s81, 20, 0
s_sub_u32 s5, s5, s81
s_and_b32 s80, s14, 1
s_mul_i32 s29, s80, 17172480
s_mul_i32 s80, s56, 536576
s_add_u32 s29, s29, s80
s_lshl_b32 s80, s57, 13
s_add_u32 s29, s29, s80
s_add_u32 s29, s29, 4194304
W 0
s_add_u32 s12, s12, 0x7f4a7c15
s_addc_u32 s13, s13, 0x9e3779b9
s_mov_b64 exec, 0
s_cmp_eq_u32 s14, 0
s_cbranch_scc1 .Lh_0_0%=
s_cmp_eq_u32 s14, 1
s_cbranch_scc1 .Lh_0_1%=
s_cmp_eq_u32 s14, 2
s_cbranch_scc1 .Lh_0_2%=
s_branch .Lh_0_3%=
.Lhr_9%=:
s_mov_b64 exec, -1
s_bitcmp1_b32 s14, 0
s_cbranch_scc1 .Lv_10%=
s_branch .LL_12%=
.p2align 12
.LL_12%=:
s_waitcnt vmcnt(14)
s_barrier
Ta 0,10,0
Ta 2,10,1024
Ra 32,26,0
Ta 4,10,8192
.Lh_0_0%=:
s_cbranch_execz .Lh_0_0_1%=
Ta 6,10,9216
Ra 128,27,0
Ra 132,27,2048
Ra 36,26,2048
Ra 40,26,4096
Ra 136,27,4096
Ra 140,27,6144
Ra 44,26,6144
Ra 48,26,8192
Ra 144,27,8192
W 6
Zvaa 160,0,32
)"
R"(s_add_u32 m0, s61, 32768
Ra 148,27,10240
Ra 52,26,10240
Ra 56,26,12288
Ra 152,27,12288
Ra 156,27,14336
Ra 60,26,14336
Mvaa 160,4,128
Ta 8,11,0
Ta 10,11,1024
Ta 12,11,8192
Ta 14,11,9216
Zvaa 192,4,132
Ta 16,12,0
Ta 18,12,1024
Mvaa 192,0,36
W 14
Ta 20,12,8192
Ta 22,12,9216
Zvaa 224,0,40
D 2,40,64,0
Mvaa 224,4,136
Zaaa 160,4,140
Maaa 160,0,44
W 12
Zaaa 192,0,48
D 3,40,64,1024
Maaa 192,4,144
Zaaa 224,4,148
s_waitcnt vmcnt(13)
Maaa 224,0,52
W 8
v_lshlrev_b32 v56, 16, v32
v_and_b32 v57, 0xffff0000, v32
v_lshlrev_b32 v58, 16, v33
v_and_b32 v59, 0xffff0000, v33
Zaaa 64,0,56
D 4,40,64,2048
v_lshlrev_b32 v60, 16, v34
v_and_b32 v61, 0xffff0000, v34
v_lshlrev_b32 v62, 16, v35
v_and_b32 v63, 0xffff0000, v35
Maaa 64,4,152
v_lshlrev_b32 v64, 16, v36
v_and_b32 v65, 0xffff0000, v36
v_lshlrev_b32 v66, 16, v37
v_and_b32 v67, 0xffff0000, v37
v_lshlrev_b32 v68, 16, v38
Zaaa 96,4,156
v_and_b32 v69, 0xffff0000, v38
v_lshlrev_b32 v70, 16, v39
v_and_b32 v71, 0xffff0000, v39
v_lshlrev_b32 v72, 16, v40
Maaa 96,0,60
W 4
v_and_b32 v73, 0xffff0000, v40
v_lshlrev_b32 v74, 16, v41
v_and_b32 v75, 0xffff0000, v41
v_lshlrev_b32 v76, 16, v42
v_and_b32 v77, 0xffff0000, v42
Zaaa 100,8,60
D 5,40,64,3072
v_lshlrev_b32 v78, 16, v43
v_and_b32 v79, 0xffff0000, v43
A 80,56,57
A 81,58,59
Maaa 100,12,156
s_add_u32 m0, s61, 36864
A 80,80,60
A 81,81,61
A 80,80,62
A 81,81,63
A 80,80,64
Zaaa 68,12,152
Ta 24,13,0
Ta 26,13,1024
A 81,81,65
A 80,80,66
A 81,81,67
A 80,80,68
Maaa 68,8,56
Ta 28,13,8192
Ta 30,13,9216
A 81,81,69
A 80,80,70
A 81,81,71
A 80,80,72
Zaaa 228,8,52
D 6,40,64,0
A 81,81,73
A 80,80,74
A 81,81,75
A 80,80,76
A 81,81,77
Maaa 228,12,148
A 80,80,78
A 81,81,79
A 80,80,81
s_nop 1
v_add_f32_dpp v80, v80, v80 quad_perm:[1,0,3,2] row_mask:0xf bank_mask:0xf
Zaaa 196,12,144
Maaa 196,8,48
v_add_f32_dpp v80, v80, v80 quad_perm:[2,3,0,1] row_mask:0xf bank_mask:0xf
s_nop 1
v_add_f32_dpp v80, v80, v80 row_half_mirror row_mask:0xf bank_mask:0xf
s_nop 1
v_add_f32_dpp v80, v80, v80 row_mirror row_mask:0xf bank_mask:0xf
v_mov_b32 v81, v80
s_nop 1
v_permlane16_swap_b32 v80, v81
A 80,80,81
Zaaa 164,8,44
D 7,40,64,1024
v_mul_f32 v84, 0x3acccccd, v80
s_mov_b32 s86, 0xffff
s_mov_b32 s87, 0xffff
v_sub_f32 v82, v56, v84
U 80,82,82
v_sub_f32 v83, v57, v84
Maaa 164,12,140
v_fmac_f32 v80, v83, v83
v_sub_f32 v82, v58, v84
v_fmac_f32 v80, v82, v82
v_sub_f32 v83, v59, v84
v_fmac_f32 v80, v83, v83
v_sub_f32 v82, v60, v84
Zvaa 228,12,136
v_fmac_f32 v80, v82, v82
v_sub_f32 v83, v61, v84
v_fmac_f32 v80, v83, v83
v_sub_f32 v82, v62, v84
v_fmac_f32 v80, v82, v82
Mvaa 228,8,40
v_sub_f32 v83, v63, v84
v_fmac_f32 v80, v83, v83
v_sub_f32 v82, v64, v84
v_fmac_f32 v80, v82, v82
Zvaa 196,8,36
D 8,40,64,2048
v_sub_f32 v83, v65, v84
v_fmac_f32 v80, v83, v83
v_sub_f32 v82, v66, v84
v_fmac_f32 v80, v82, v82
Mvaa 196,12,132
v_sub_f32 v83, v67, v84
v_fmac_f32 v80, v83, v83
v_sub_f32 v82, v68, v84
v_fmac_f32 v80, v82, v82
v_sub_f32 v83, v69, v84
Zvaa 164,12,128
v_fmac_f32 v80, v83, v83
v_sub_f32 v82, v70, v84
v_fmac_f32 v80, v82, v82
v_sub_f32 v83, v71, v84
Mvaa 164,8,32
W 4
v_fmac_f32 v80, v83, v83
v_sub_f32 v82, v72, v84
U 81,82,82
v_sub_f32 v83, v73, v84
v_fmac_f32 v81, v83, v83
Zvaa 168,16,32
D 9,40,64,3072
v_sub_f32 v82, v74, v84
v_fmac_f32 v81, v82, v82
v_sub_f32 v83, v75, v84
v_fmac_f32 v81, v83, v83
Mvaa 168,20,128
v_sub_f32 v82, v76, v84
v_fmac_f32 v81, v82, v82
v_sub_f32 v83, v77, v84
v_fmac_f32 v81, v83, v83
v_sub_f32 v82, v78, v84
Zvaa 200,20,132
Ta 0,14,0
Ta 2,14,1024
v_fmac_f32 v81, v82, v82
v_sub_f32 v83, v79, v84
v_fmac_f32 v81, v83, v83
v_cndmask_b32_e64 v81, 0, v81, s[86:87]
Mvaa 200,16,36
Ta 4,14,8192
Ta 6,14,9216
A 80,80,81
s_nop 1
v_add_f32_dpp v80, v80, v80 quad_perm:[1,0,3,2] row_mask:0xf bank_mask:0xf
s_nop 1
v_add_f32_dpp v80, v80, v80 quad_perm:[2,3,0,1] row_mask:0xf bank_mask:0xf
s_nop 1
v_add_f32_dpp v80, v80, v80 row_half_mirror row_mask:0xf bank_mask:0xf
Zvaa 232,16,40
global_load_dword v142, v[140:141], off
v_lshl_add_u64 v[140:141], v[140:141], 0, s[68:69]
Mvaa 232,20,136
v_add_f32_dpp v80, v80, v80 row_mirror row_mask:0xf bank_mask:0xf
v_mov_b32 v81, v80
s_nop 1
v_permlane16_swap_b32 v80, v81
A 80,80,81
v_mul_f32 v80, 0x3acccccd, v80
K 80,80,80
Zaaa 168,20,140
v_lshlrev_b32 v80, 16, v80
v_add_f32 v80, 0x3727c5ac, v80
K 80,80,80
v_lshlrev_b32 v80, 16, v80
v_sqrt_f32 v81, v80
Maaa 168,16,44
v_rcp_f32 v82, v81
v_fma_f32 v83, -v81, v81, v80
v_mul_f32 v82, 0.5, v82
v_fma_f32 v81, v83, v82, v81
K 86,81,81
Zaaa 200,16,48
v_lshlrev_b32 v86, 16, v86
v_rcp_f32 v87, v86
K 85,84,84
v_lshlrev_b32 v85, 16, v85
Maaa 200,20,144
s_waitcnt vmcnt(17)
v_sub_f32 v56, v56, v85
v_sub_f32 v57, v57, v85
K 80,56,57
Zaaa 232,20,148
v_lshlrev_b32 v56, 16, v80
v_and_b32 v57, 0xffff0000, v80
U 80,56,87
U 81,57,87
K 32,80,81
Maaa 232,16,52
v_lshlrev_b32 v80, 16, v32
v_and_b32 v81, 0xffff0000, v32
v_lshlrev_b32 v82, 16, v44
v_and_b32 v83, 0xffff0000, v44
Zaaa 72,16,56
U 84,80,82
v_fmac_f32 v84, v81, v83
v_sub_f32 v58, v58, v85
v_sub_f32 v59, v59, v85
Maaa 72,20,152
K 80,58,59
v_lshlrev_b32 v58, 16, v80
v_and_b32 v59, 0xffff0000, v80
U 80,58,87
U 81,59,87
Zaaa 104,20,156
K 33,80,81
v_lshlrev_b32 v80, 16, v33
v_and_b32 v81, 0xffff0000, v33
v_lshlrev_b32 v82, 16, v45
Maaa 104,16,60
W 4
v_and_b32 v83, 0xffff0000, v45
v_fmac_f32 v84, v80, v82
v_fmac_f32 v84, v81, v83
v_sub_f32 v60, v60, v85
v_sub_f32 v61, v61, v85
Zaaa 108,24,60
K 80,60,61
v_lshlrev_b32 v60, 16, v80
v_and_b32 v61, 0xffff0000, v80
U 80,60,87
Maaa 108,28,156
U 81,61,87
K 34,80,81
v_lshlrev_b32 v80, 16, v34
v_and_b32 v81, 0xffff0000, v34
v_lshlrev_b32 v82, 16, v46
Zaaa 76,28,152
Ta 8,15,0
Ta 10,15,1024
v_and_b32 v83, 0xffff0000, v46
v_fmac_f32 v84, v80, v82
v_fmac_f32 v84, v81, v83
v_sub_f32 v62, v62, v85
Maaa 76,24,56
Ta 12,15,8192
v_sub_f32 v63, v63, v85
K 80,62,63
v_lshlrev_b32 v62, 16, v80
v_and_b32 v63, 0xffff0000, v80
U 80,62,87
Zaaa 236,24,52
Ta 14,15,9216
U 81,63,87
K 35,80,81
v_lshlrev_b32 v80, 16, v35
v_and_b32 v81, 0xffff0000, v35
Maaa 236,28,148
v_lshlrev_b32 v82, 16, v47
v_and_b32 v83, 0xffff0000, v47
v_fmac_f32 v84, v80, v82
v_fmac_f32 v84, v81, v83
Zaaa 204,28,144
v_sub_f32 v64, v64, v85
v_sub_f32 v65, v65, v85
K 80,64,65
v_lshlrev_b32 v64, 16, v80
v_and_b32 v65, 0xffff0000, v80
Maaa 204,24,48
U 80,64,87
U 81,65,87
K 36,80,81
v_lshlrev_b32 v80, 16, v36
Zaaa 172,24,44
v_and_b32 v81, 0xffff0000, v36
v_lshlrev_b32 v82, 16, v48
v_and_b32 v83, 0xffff0000, v48
v_fmac_f32 v84, v80, v82
v_fmac_f32 v84, v81, v83
Maaa 172,28,140
v_sub_f32 v66, v66, v85
v_sub_f32 v67, v67, v85
K 80,66,67
v_lshlrev_b32 v66, 16, v80
Zvaa 236,28,136
v_and_b32 v67, 0xffff0000, v80
U 80,66,87
U 81,67,87
K 37,80,81
v_lshlrev_b32 v80, 16, v37
Mvaa 236,24,40
v_and_b32 v81, 0xffff0000, v37
v_lshlrev_b32 v82, 16, v49
v_and_b32 v83, 0xffff0000, v49
v_fmac_f32 v84, v80, v82
Zvaa 204,24,36
v_fmac_f32 v84, v81, v83
v_sub_f32 v68, v68, v85
v_sub_f32 v69, v69, v85
K 80,68,69
v_lshlrev_b32 v68, 16, v80
Mvaa 204,28,132
v_and_b32 v69, 0xffff0000, v80
U 80,68,87
U 81,69,87
K 38,80,81
Zvaa 172,28,128
v_lshlrev_b32 v80, 16, v38
v_and_b32 v81, 0xffff0000, v38
v_lshlrev_b32 v82, 16, v50
v_and_b32 v83, 0xffff0000, v50
Mvaa 172,24,32
W 4
v_fmac_f32 v84, v80, v82
v_fmac_f32 v84, v81, v83
v_sub_f32 v70, v70, v85
v_sub_f32 v71, v71, v85
K 80,70,71
Zvaa 176,0,32
v_lshlrev_b32 v70, 16, v80
v_and_b32 v71, 0xffff0000, v80
U 80,70,87
U 81,71,87
Mvaa 176,4,128
K 39,80,81
v_lshlrev_b32 v80, 16, v39
v_and_b32 v81, 0xffff0000, v39
v_lshlrev_b32 v82, 16, v51
v_and_b32 v83, 0xffff0000, v51
Zvaa 208,4,132
Ta 16,24,0
Ta 18,24,1024
v_fmac_f32 v84, v80, v82
v_fmac_f32 v84, v81, v83
v_sub_f32 v72, v72, v85
v_sub_f32 v73, v73, v85
Mvaa 208,0,36
Ta 20,24,8192
K 80,72,73
v_lshlrev_b32 v72, 16, v80
v_and_b32 v73, 0xffff0000, v80
U 80,72,87
U 81,73,87
Zvaa 240,0,40
Ta 22,24,9216
K 40,80,81
v_lshlrev_b32 v80, 16, v40
v_and_b32 v81, 0xffff0000, v40
v_lshlrev_b32 v82, 16, v52
Mvaa 240,4,136
v_and_b32 v83, 0xffff0000, v52
v_fmac_f32 v84, v80, v82
v_fmac_f32 v84, v81, v83
v_sub_f32 v74, v74, v85
Zaaa 176,4,140
v_sub_f32 v75, v75, v85
K 80,74,75
v_lshlrev_b32 v74, 16, v80
v_and_b32 v75, 0xffff0000, v80
U 80,74,87
Maaa 176,0,44
U 81,75,87
K 41,80,81
v_lshlrev_b32 v80, 16, v41
v_and_b32 v81, 0xffff0000, v41
Zaaa 208,0,48
v_lshlrev_b32 v82, 16, v53
v_and_b32 v83, 0xffff0000, v53
v_fmac_f32 v84, v80, v82
)"
R"(v_fmac_f32 v84, v81, v83
v_sub_f32 v76, v76, v85
Maaa 208,4,144
v_sub_f32 v77, v77, v85
K 80,76,77
v_lshlrev_b32 v76, 16, v80
v_and_b32 v77, 0xffff0000, v80
Zaaa 240,4,148
U 80,76,87
U 81,77,87
K 42,80,81
v_lshlrev_b32 v80, 16, v42
v_and_b32 v81, 0xffff0000, v42
Maaa 240,0,52
v_lshlrev_b32 v82, 16, v54
v_and_b32 v83, 0xffff0000, v54
v_fmac_f32 v84, v80, v82
v_fmac_f32 v84, v81, v83
Zaaa 80,0,56
v_sub_f32 v78, v78, v85
v_sub_f32 v79, v79, v85
K 80,78,79
v_lshlrev_b32 v78, 16, v80
v_and_b32 v79, 0xffff0000, v80
Maaa 80,4,152
U 80,78,87
U 81,79,87
K 43,80,81
v_lshlrev_b32 v80, 16, v43
Zaaa 112,4,156
v_and_b32 v81, 0xffff0000, v43
v_lshlrev_b32 v82, 16, v55
v_and_b32 v83, 0xffff0000, v55
v_fmac_f32 v84, v80, v82
Maaa 112,0,60
W 4
v_fmac_f32 v84, v81, v83
v_lshrrev_b32 v82, 9, v1
s_lshl_b32 s80, s14, 1
v_add_u32 v82, s80, v82
v_mul_u32_u24 v83, 1552, v82
Zaaa 116,8,60
Ta 24,25,0
Ta 26,25,1024
v_add_u32 v83, v83, v88
v_add_u32 v83, 131072, v83
ds_write_b128 v83, v[32:35] offset:0
ds_write_b128 v83, v[36:39] offset:512
Maaa 116,12,156
ds_write_b128 v83, v[40:43] offset:1024
v_add_f32_dpp v84, v84, v84 quad_perm:[1,0,3,2] row_mask:0xf bank_mask:0xf
s_nop 1
v_add_f32_dpp v84, v84, v84 quad_perm:[2,3,0,1] row_mask:0xf bank_mask:0xf
s_nop 1
v_add_f32_dpp v84, v84, v84 row_half_mirror row_mask:0xf bank_mask:0xf
s_nop 1
v_add_f32_dpp v84, v84, v84 row_mirror row_mask:0xf bank_mask:0xf
Zaaa 84,12,152
Ta 28,25,8192
Ta 30,25,9216
v_mov_b32 v81, v84
Maaa 84,8,56
Zaaa 244,8,52
v_permlane16_swap_b32 v84, v81
A 84,84,81
K 80,84,84
v_lshlrev_b32 v80, 16, v80
s_lshl_b32 s80, s15, 3
v_add_u32 v82, s80, v82
v_sub_u32 v83, 2047, v82
Maaa 244,12,148
v_ashrrev_i32 v81, 31, v80
v_or_b32 v81, 0x80000000, v81
v_xor_b32 v80, v80, v81
v_and_b32 v80, 0xffff0000, v80
v_or_b32 v80, v80, v83
v_and_b32 v82, 7, v82
Zaaa 212,12,144
v_lshlrev_b32 v82, 2, v82
v_add_u32 v82, 143488, v82
ds_write_b32 v82, v80
Maaa 212,8,48
Zaaa 180,8,44
Maaa 180,12,140
Zvaa 244,12,136
s_waitcnt vmcnt(1) lgkmcnt(0)
s_barrier
s_add_u32 s64, s64, 128
Mvaa 244,8,40
v_bfe_u32 v80, v1, 4, 3
Zvaa 212,8,36
v_mul_u32_u24 v81, 1552, v80
Mvaa 212,12,132
s_and_b32 s64, s64, 1023
v_lshrrev_b32 v82, 7, v1
Zvaa 180,12,128
v_lshl_add_u32 v81, v82, 4, v81
Mvaa 180,8,32
s_lshl_b32 s80, s14, 7
Zvaa 184,16,32
s_lshl_b32 s65, s64, 12
s_add_u32 s80, s80, 131072
Mvaa 184,20,128
v_add_u32 v81, s80, v81
Zvaa 216,20,132
ds_read_b128 v[32:35], v81
Ta 0,10,32768
Mvaa 216,16,36
s_add_u32 m0, s62, 0
Ta 2,10,33792
Rv 36,81,512
Zvaa 248,16,40
s_and_b32 s80, s14, 2
Ta 4,10,40960
Ta 6,10,41984
Mvaa 248,20,136
s_lshl_b32 s80, s80, 7
Zaaa 184,20,140
D 16,44,65,0
v_subrev_u32 v81, s80, v81
Maaa 184,16,44
Rv 40,81,1024
Zaaa 216,16,48
v_lshlrev_b32 v80, 2, v80
Maaa 216,20,144
v_add_u32 v80, 143488, v80
Zaaa 248,20,148
D 17,44,65,1024
ds_read_b32 v84, v80
Maaa 248,16,52
v_mov_b32 v85, s13
Zaaa 88,16,56
v_lshrrev_b32 v83, 1, v1
Maaa 88,20,152
s_mul_i32 s82, s15, 10240
Zaaa 120,20,156
s_add_u32 s82, s82, 131072
D 18,44,65,2048
Maaa 120,16,60
s_lshl_b32 s80, s14, 10
Zaaa 124,24,60
s_add_u32 s83, s82, s80
Ra 60,26,47104
Maaa 124,28,156
Ra 156,27,47104
Zaaa 92,28,152
D 19,44,65,3072
Ra 152,27,45056
Maaa 92,24,56
s_add_u32 m0, s62, 4096
Ta 8,11,32768
Ta 10,11,33792
Zaaa 252,24,52
Ra 56,26,45056
Ta 12,11,40960
Maaa 252,28,148
Ta 14,11,41984
Ra 52,26,43008
Zaaa 220,28,144
W 9
buffer_store_dwordx4 v[32:35], v1, s[8:11], s83 offen sc1
s_add_u32 s83, s83, 4096
Maaa 220,24,48
Ra 148,27,43008
Ra 144,27,40960
Zaaa 188,24,44
Ra 48,26,40960
Ra 44,26,38912
Maaa 188,28,140
Ra 140,27,38912
Zvaa 252,28,136
D 20,44,65,0
Ra 136,27,36864
Mvaa 252,24,40
Ra 40,26,36864
Zvaa 220,24,36
Ra 36,26,34816
Mvaa 220,28,132
Ra 132,27,34816
Zvaa 188,28,128
buffer_store_dwordx4 v[36:39], v1, s[8:11], s83 offen sc1
Ra 128,27,32768
Mvaa 188,24,32
W 13
s_and_b32 s80, s80, 1024
s_add_u32 s83, s82, s80
Maaa 96,0,60
s_add_u32 s83, s83, 8192
Ra 32,26,32768
Maaa 96,4,156
Maaa 64,4,152
D 21,44,65,1024
Ta 16,12,32768
Maaa 64,0,56
W 9
Ta 18,12,33792
Ta 20,12,40960
Maaa 224,0,52
Ta 22,12,41984
Maaa 224,4,148
Maaa 192,4,144
buffer_store_dwordx4 v[40:43], v1, s[8:11], s83 offen sc1
Maaa 192,0,48
W 8
Maaa 160,0,44
Maaa 160,4,140
Mvaa 224,4,136
D 22,44,65,2048
Mvaa 224,0,40
W 4
Mvaa 192,0,36
Mvaa 192,4,132
Mvaa 160,4,128
D 23,44,65,3072
Mvaa 160,0,32
s_add_u32 m0, s61, 0
Mvaa 164,8,32
Mvaa 164,12,128
Mvaa 196,12,132
D 2,40,64,0
Ta 24,13,32768
Mvaa 196,8,36
Ta 26,13,33792
Ta 28,13,40960
Mvaa 228,8,40
Ta 30,13,41984
Mvaa 228,12,136
.Lh_0_1%=:
s_cbranch_execz .Lh_0_1_1%=
Maaa 164,12,140
D 3,40,64,1024
Maaa 164,8,44
Maaa 196,8,48
Maaa 196,12,144
Maaa 228,12,148
D 4,40,64,2048
Maaa 228,8,52
Maaa 68,8,56
Maaa 68,12,152
Maaa 100,12,156
D 5,40,64,3072
Maaa 100,8,60
W 4
s_add_u32 m0, s61, 4096
Maaa 104,16,60
Maaa 104,20,156
Maaa 72,20,152
D 6,40,64,0
Ta 0,14,32768
Maaa 72,16,56
Ta 2,14,33792
Ta 4,14,40960
Maaa 232,16,52
Ta 6,14,41984
Maaa 232,20,148
Maaa 200,20,144
D 7,40,64,1024
Maaa 200,16,48
Maaa 168,16,44
Maaa 168,20,140
Mvaa 232,20,136
D 8,40,64,2048
Mvaa 232,16,40
Mvaa 200,16,36
Mvaa 200,20,132
Mvaa 168,20,128
D 9,40,64,3072
Mvaa 168,16,32
W 4
Mvaa 172,24,32
Mvaa 172,28,128
Mvaa 204,28,132
Ta 8,15,32768
Ta 10,15,33792
Mvaa 204,24,36
Ta 12,15,40960
Ta 14,15,41984
Mvaa 236,24,40
Mvaa 236,28,136
Maaa 172,28,140
Maaa 172,24,44
Maaa 204,24,48
Maaa 204,28,144
Maaa 236,28,148
Maaa 236,24,52
Maaa 76,24,56
Maaa 76,28,152
Maaa 108,28,156
Maaa 108,24,60
W 4
Maaa 112,0,60
Maaa 112,4,156
Maaa 80,4,152
Ta 16,24,32768
Ta 18,24,33792
Maaa 80,0,56
Ta 20,24,40960
Ta 22,24,41984
Maaa 240,0,52
Maaa 240,4,148
Maaa 208,4,144
Maaa 208,0,48
Maaa 176,0,44
Maaa 176,4,140
Mvaa 240,4,136
Mvaa 240,0,40
Mvaa 208,0,36
Mvaa 208,4,132
Mvaa 176,4,128
Mvaa 176,0,32
W 4
Mvaa 180,8,32
Ta 24,25,32768
Ta 26,25,33792
Mvaa 180,12,128
Mvaa 212,12,132
Ta 28,25,40960
Ta 30,25,41984
Mvaa 212,8,36
Mvaa 244,8,40
Mvaa 244,12,136
Maaa 180,12,140
Maaa 180,8,44
Maaa 212,8,48
Maaa 212,12,144
Maaa 244,12,148
s_waitcnt vmcnt(0) lgkmcnt(0)
s_barrier
s_cmp_eq_u32 s14, 0
s_cbranch_scc0 .Lpubr_14%=
s_lshl_b32 s80, s15, 6
s_add_u32 s80, s80, 40960
s_mov_b64 exec, 0xff
buffer_store_dwordx2 v[84:85], v83, s[8:11], s80 offen sc1
s_mov_b64 exec, -1
.Lpubr_14%=:
Maaa 244,8,52
s_add_u32 s64, s64, 128
Maaa 84,8,56
Maaa 84,12,152
s_and_b32 s64, s64, 1023
Maaa 116,12,156
Maaa 116,8,60
Maaa 120,16,60
s_lshl_b32 s65, s64, 12
Maaa 120,20,156
Maaa 88,20,152
s_add_u32 m0, s62, 32768
Ta 0,10,0
Ta 2,10,1024
Maaa 88,16,56
Ta 4,10,8192
Ta 6,10,9216
Maaa 248,16,52
Maaa 248,20,148
D 16,44,65,0
Maaa 216,20,144
Maaa 216,16,48
Maaa 184,16,44
Maaa 184,20,140
D 17,44,65,1024
Mvaa 248,20,136
Mvaa 248,16,40
Mvaa 216,16,36
Mvaa 216,20,132
D 18,44,65,2048
Mvaa 184,20,128
Mvaa 184,16,32
Mvaa 188,24,32
Ra 32,26,0
Mvaa 188,28,128
D 19,44,65,3072
Ra 128,27,0
Mvaa 220,28,132
s_add_u32 m0, s62, 36864
Ra 132,27,2048
Ta 8,11,0
Mvaa 220,24,36
Ta 10,11,1024
Ra 36,26,2048
Mvaa 252,24,40
Ta 12,11,8192
Ta 14,11,9216
Mvaa 252,28,136
D 20,44,65,0
Ra 40,26,4096
Maaa 188,28,140
Ra 136,27,4096
Ra 140,27,6144
Maaa 188,24,44
Ra 44,26,6144
Maaa 220,24,48
Ra 48,26,8192
Maaa 220,28,144
D 21,44,65,1024
Ra 144,27,8192
Maaa 252,28,148
Ra 148,27,10240
Maaa 252,24,52
Ra 52,26,10240
)"
R"(Maaa 92,24,56
Ra 56,26,12288
Maaa 92,28,152
D 22,44,65,2048
Ra 152,27,12288
Maaa 124,28,156
Ra 156,27,14336
Maaa 124,24,60
W 13
Ra 60,26,14336
Mvaa 160,0,32
Mvaa 160,4,128
D 23,44,65,3072
Mvaa 192,4,132
s_add_u32 m0, s61, 32768
Ta 16,12,0
Ta 18,12,1024
Mvaa 192,0,36
W 10
Ta 20,12,8192
Ta 22,12,9216
Mvaa 224,0,40
Mvaa 224,4,136
D 2,40,64,0
Maaa 160,4,140
Maaa 160,0,44
W 8
Maaa 192,0,48
Maaa 192,4,144
D 3,40,64,1024
Maaa 224,4,148
Maaa 224,0,52
W 4
Maaa 64,0,56
Maaa 64,4,152
D 4,40,64,2048
Maaa 96,4,156
Maaa 96,0,60
Maaa 100,8,60
Maaa 100,12,156
D 5,40,64,3072
Maaa 68,12,152
s_add_u32 m0, s61, 36864
Ta 24,13,0
Ta 26,13,1024
Maaa 68,8,56
Ta 28,13,8192
Ta 30,13,9216
Maaa 228,8,52
Maaa 228,12,148
D 6,40,64,0
Maaa 196,12,144
Maaa 196,8,48
Maaa 164,8,44
Maaa 164,12,140
D 7,40,64,1024
Mvaa 228,12,136
Mvaa 228,8,40
Mvaa 196,8,36
Mvaa 196,12,132
D 8,40,64,2048
Mvaa 164,12,128
Mvaa 164,8,32
W 4
Mvaa 168,16,32
Mvaa 168,20,128
D 9,40,64,3072
Mvaa 200,20,132
Ta 0,14,0
Ta 2,14,1024
Mvaa 200,16,36
Ta 4,14,8192
Ta 6,14,9216
Mvaa 232,16,40
Mvaa 232,20,136
global_load_dword v142, v[140:141], off
v_lshl_add_u64 v[140:141], v[140:141], 0, s[68:69]
Maaa 168,20,140
Maaa 168,16,44
Maaa 200,16,48
Maaa 200,20,144
Maaa 232,20,148
Maaa 232,16,52
Maaa 72,16,56
Maaa 72,20,152
Maaa 104,20,156
Maaa 104,16,60
W 4
Maaa 108,24,60
Maaa 108,28,156
Maaa 76,28,152
Ta 8,15,0
Ta 10,15,1024
Maaa 76,24,56
Ta 12,15,8192
Ta 14,15,9216
Maaa 236,24,52
Maaa 236,28,148
Maaa 204,28,144
Maaa 204,24,48
Maaa 172,24,44
Maaa 172,28,140
Mvaa 236,28,136
Mvaa 236,24,40
Mvaa 204,24,36
Mvaa 204,28,132
Mvaa 172,28,128
Mvaa 172,24,32
W 4
Mvaa 176,0,32
Mvaa 176,4,128
Mvaa 208,4,132
Ta 16,24,0
Ta 18,24,1024
Mvaa 208,0,36
Ta 20,24,8192
Ta 22,24,9216
Mvaa 240,0,40
Mvaa 240,4,136
Maaa 176,4,140
Maaa 176,0,44
Maaa 208,0,48
Maaa 208,4,144
Maaa 240,4,148
Maaa 240,0,52
Maaa 80,0,56
Maaa 80,4,152
Maaa 112,4,156
Maaa 112,0,60
W 4
Maaa 116,8,60
Ta 24,25,0
Ta 26,25,1024
Maaa 116,12,156
Maaa 84,12,152
Ta 28,25,8192
Ta 30,25,9216
Maaa 84,8,56
Maaa 244,8,52
Maaa 244,12,148
Maaa 212,12,144
Maaa 212,8,48
Maaa 180,8,44
Maaa 180,12,140
Mvaa 244,12,136
s_waitcnt vmcnt(1) lgkmcnt(0)
s_barrier
s_add_u32 s64, s64, 128
Mvaa 244,8,40
Mvaa 212,8,36
Mvaa 212,12,132
s_and_b32 s64, s64, 1023
Mvaa 180,12,128
Mvaa 180,8,32
Mvaa 184,16,32
s_lshl_b32 s65, s64, 12
Mvaa 184,20,128
Mvaa 216,20,132
Ta 0,10,32768
Ta 2,10,33792
Mvaa 216,16,36
s_add_u32 m0, s62, 0
Ta 4,10,40960
Ta 6,10,41984
Mvaa 248,16,40
Mvaa 248,20,136
Maaa 184,20,140
D 16,44,65,0
Maaa 184,16,44
Maaa 216,16,48
Maaa 216,20,144
Maaa 248,20,148
D 17,44,65,1024
Maaa 248,16,52
Maaa 88,16,56
Maaa 88,20,152
Maaa 120,20,156
D 18,44,65,2048
Maaa 120,16,60
Maaa 124,24,60
Ra 60,26,47104
Maaa 124,28,156
Ra 156,27,47104
Maaa 92,28,152
D 19,44,65,3072
Ra 152,27,45056
Maaa 92,24,56
s_add_u32 m0, s62, 4096
Ta 8,11,32768
Ta 10,11,33792
Maaa 252,24,52
Ra 56,26,45056
Ta 12,11,40960
Maaa 252,28,148
Ta 14,11,41984
Ra 52,26,43008
Maaa 220,28,144
D 20,44,65,0
Ra 148,27,43008
Maaa 220,24,48
Ra 144,27,40960
Ra 48,26,40960
Maaa 188,24,44
Ra 44,26,38912
Maaa 188,28,140
Ra 140,27,38912
Mvaa 252,28,136
D 21,44,65,1024
Ra 136,27,36864
Mvaa 252,24,40
Ra 40,26,36864
Mvaa 220,24,36
Ra 36,26,34816
Mvaa 220,28,132
Ra 132,27,34816
Mvaa 188,28,128
D 22,44,65,2048
Ra 128,27,32768
Mvaa 188,24,32
W 13
Ra 32,26,32768
Maaa 96,0,60
Maaa 96,4,156
Maaa 64,4,152
D 23,44,65,3072
Ta 16,12,32768
Maaa 64,0,56
W 9
s_add_u32 m0, s61, 0
Ta 18,12,33792
Maaa 224,0,52
Ta 20,12,40960
Ta 22,12,41984
Maaa 224,4,148
Maaa 192,4,144
D 2,40,64,0
Maaa 192,0,48
W 8
Maaa 160,0,44
Maaa 160,4,140
Mvaa 224,4,136
D 3,40,64,1024
Mvaa 224,0,40
W 4
Mvaa 192,0,36
Mvaa 192,4,132
Mvaa 160,4,128
D 4,40,64,2048
Mvaa 160,0,32
Mvaa 164,8,32
Mvaa 164,12,128
Mvaa 196,12,132
D 5,40,64,3072
Ta 24,13,32768
Mvaa 196,8,36
s_add_u32 m0, s61, 4096
Ta 26,13,33792
Ta 28,13,40960
Mvaa 228,8,40
Ta 30,13,41984
Mvaa 228,12,136
Maaa 164,12,140
D 6,40,64,0
Maaa 164,8,44
Maaa 196,8,48
Maaa 196,12,144
Maaa 228,12,148
D 7,40,64,1024
Maaa 228,8,52
Maaa 68,8,56
Maaa 68,12,152
Maaa 100,12,156
D 8,40,64,2048
Maaa 100,8,60
W 4
Maaa 104,16,60
Maaa 104,20,156
Maaa 72,20,152
D 9,40,64,3072
v_bfe_u32 v147, v1, 5, 3
Maaa 72,16,56
v_lshlrev_b32 v147, 13, v147
v_and_b32 v148, 16, v1
Maaa 232,16,52
Ta 0,14,32768
v_lshl_add_u32 v147, v148, 3, v147
Maaa 232,20,148
s_lshl_b32 s92, s57, 2
s_add_u32 s92, s92, s14
s_lshl_b32 s92, s92, 16
Maaa 200,20,144
buffer_load_dword v142, v147, s[48:51], s92 offen
Ta 2,14,33792
Maaa 200,16,48
Ta 4,14,40960
Ta 6,14,41984
Maaa 168,16,44
Maaa 168,20,140
Mvaa 232,20,136
Mvaa 232,16,40
Mvaa 200,16,36
Mvaa 200,20,132
Mvaa 168,20,128
Mvaa 168,16,32
W 4
Mvaa 172,24,32
Mvaa 172,28,128
Mvaa 204,28,132
Ta 8,15,32768
Ta 10,15,33792
Mvaa 204,24,36
Ta 12,15,40960
Ta 14,15,41984
Mvaa 236,24,40
Mvaa 236,28,136
Maaa 172,28,140
Maaa 172,24,44
Maaa 204,24,48
Maaa 204,28,144
Maaa 236,28,148
Maaa 236,24,52
Maaa 76,24,56
Maaa 76,28,152
Maaa 108,28,156
Maaa 108,24,60
W 4
Maaa 112,0,60
Maaa 112,4,156
Maaa 80,4,152
Ta 16,24,32768
Ta 18,24,33792
Maaa 80,0,56
Ta 20,24,40960
Ta 22,24,41984
Maaa 240,0,52
Maaa 240,4,148
Maaa 208,4,144
Maaa 208,0,48
Maaa 176,0,44
Maaa 176,4,140
Mvaa 240,4,136
Mvaa 240,0,40
Mvaa 208,0,36
Mvaa 208,4,132
Mvaa 176,4,128
Mvaa 176,0,32
W 4
Mvaa 180,8,32
Ta 24,25,32768
Ta 26,25,33792
Mvaa 180,12,128
Mvaa 212,12,132
Ta 28,25,40960
Ta 30,25,41984
Mvaa 212,8,36
Mvaa 244,8,40
Mvaa 244,12,136
Maaa 180,12,140
Maaa 180,8,44
Maaa 212,8,48
.Lh_0_2%=:
s_cbranch_execz .Lh_0_2_1%=
Maaa 212,12,144
Maaa 244,12,148
s_waitcnt vmcnt(1) lgkmcnt(0)
s_barrier
s_add_u32 s64, s64, 128
Maaa 244,8,52
Maaa 84,8,56
Maaa 84,12,152
s_and_b32 s64, s64, 1023
Maaa 116,12,156
Maaa 116,8,60
Maaa 120,16,60
s_lshl_b32 s65, s64, 12
Maaa 120,20,156
Maaa 88,20,152
s_add_u32 m0, s62, 32768
Ta 0,10,0
Ta 2,10,1024
Maaa 88,16,56
Ta 4,10,8192
Ta 6,10,9216
Maaa 248,16,52
Maaa 248,20,148
D 16,44,65,0
Maaa 216,20,144
Maaa 216,16,48
Maaa 184,16,44
Maaa 184,20,140
D 17,44,65,1024
Mvaa 248,20,136
Mvaa 248,16,40
Mvaa 216,16,36
Mvaa 216,20,132
D 18,44,65,2048
Mvaa 184,20,128
Mvaa 184,16,32
Mvaa 188,24,32
Ra 32,26,0
)"
R"(Mvaa 188,28,128
D 19,44,65,3072
Ra 128,27,0
Mvaa 220,28,132
s_add_u32 m0, s62, 36864
Ra 132,27,2048
Ta 8,11,0
Mvaa 220,24,36
Ta 10,11,1024
Ra 36,26,2048
Mvaa 252,24,40
Ta 12,11,8192
Ta 14,11,9216
Mvaa 252,28,136
D 20,44,65,0
Ra 40,26,4096
Maaa 188,28,140
Ra 136,27,4096
Ra 140,27,6144
Maaa 188,24,44
Ra 44,26,6144
Maaa 220,24,48
Ra 48,26,8192
Maaa 220,28,144
D 21,44,65,1024
Ra 144,27,8192
Maaa 252,28,148
Ra 148,27,10240
Maaa 252,24,52
Ra 52,26,10240
Maaa 92,24,56
Ra 56,26,12288
Maaa 92,28,152
D 22,44,65,2048
Ra 152,27,12288
Maaa 124,28,156
Ra 156,27,14336
Maaa 124,24,60
W 13
Ra 60,26,14336
Mvaa 160,0,32
Mvaa 160,4,128
D 23,44,65,3072
Mvaa 192,4,132
s_add_u32 m0, s61, 32768
Ta 16,12,0
Ta 18,12,1024
Mvaa 192,0,36
W 10
Ta 20,12,8192
Ta 22,12,9216
Mvaa 224,0,40
Mvaa 224,4,136
D 2,40,64,0
Maaa 160,4,140
Maaa 160,0,44
W 8
Maaa 192,0,48
Maaa 192,4,144
D 3,40,64,1024
Maaa 224,4,148
Maaa 224,0,52
W 4
Maaa 64,0,56
Maaa 64,4,152
D 4,40,64,2048
Maaa 96,4,156
Maaa 96,0,60
Maaa 100,8,60
Maaa 100,12,156
D 5,40,64,3072
Maaa 68,12,152
s_add_u32 m0, s61, 36864
Ta 24,13,0
Ta 26,13,1024
Maaa 68,8,56
Ta 28,13,8192
Ta 30,13,9216
Maaa 228,8,52
Maaa 228,12,148
D 6,40,64,0
Maaa 196,12,144
Maaa 196,8,48
Maaa 164,8,44
Maaa 164,12,140
D 7,40,64,1024
Mvaa 228,12,136
Mvaa 228,8,40
Mvaa 196,8,36
Mvaa 196,12,132
D 8,40,64,2048
Mvaa 164,12,128
Mvaa 164,8,32
W 4
Mvaa 168,16,32
Mvaa 168,20,128
D 9,40,64,3072
Mvaa 200,20,132
Ta 0,14,0
Ta 2,14,1024
Mvaa 200,16,36
Ta 4,14,8192
Ta 6,14,9216
Mvaa 232,16,40
Mvaa 232,20,136
global_load_dword v142, v[140:141], off
v_lshl_add_u64 v[140:141], v[140:141], 0, s[68:69]
Maaa 168,20,140
Maaa 168,16,44
Maaa 200,16,48
Maaa 200,20,144
Maaa 232,20,148
Maaa 232,16,52
Maaa 72,16,56
Maaa 72,20,152
Maaa 104,20,156
Maaa 104,16,60
W 4
Maaa 108,24,60
Maaa 108,28,156
Maaa 76,28,152
Ta 8,15,0
Ta 10,15,1024
Maaa 76,24,56
Ta 12,15,8192
Ta 14,15,9216
Maaa 236,24,52
Maaa 236,28,148
Maaa 204,28,144
Maaa 204,24,48
Maaa 172,24,44
Maaa 172,28,140
Mvaa 236,28,136
Mvaa 236,24,40
Mvaa 204,24,36
Mvaa 204,28,132
Mvaa 172,28,128
Mvaa 172,24,32
W 4
Mvaa 176,0,32
Mvaa 176,4,128
Mvaa 208,4,132
Ta 16,24,0
Ta 18,24,1024
Mvaa 208,0,36
Ta 20,24,8192
Ta 22,24,9216
Mvaa 240,0,40
Mvaa 240,4,136
Maaa 176,4,140
Maaa 176,0,44
Maaa 208,0,48
Maaa 208,4,144
Maaa 240,4,148
Maaa 240,0,52
Maaa 80,0,56
Maaa 80,4,152
Maaa 112,4,156
Maaa 112,0,60
W 4
Maaa 116,8,60
Ta 24,25,0
Ta 26,25,1024
Maaa 116,12,156
Maaa 84,12,152
Ta 28,25,8192
Ta 30,25,9216
Maaa 84,8,56
Maaa 244,8,52
Maaa 244,12,148
Maaa 212,12,144
Maaa 212,8,48
Maaa 180,8,44
Maaa 180,12,140
Mvaa 244,12,136
s_waitcnt vmcnt(1) lgkmcnt(0)
s_barrier
s_add_u32 s64, s64, 128
Mvaa 244,8,40
Mvaa 212,8,36
Mvaa 212,12,132
s_and_b32 s64, s64, 1023
Mvaa 180,12,128
Mvaa 180,8,32
Mvaa 184,16,32
s_lshl_b32 s65, s64, 12
Mvaa 184,20,128
Mvaa 216,20,132
Ta 0,10,32768
Ta 2,10,33792
Mvaa 216,16,36
s_add_u32 m0, s62, 0
Ta 4,10,40960
Ta 6,10,41984
Mvaa 248,16,40
Mvaa 248,20,136
Maaa 184,20,140
D 16,44,65,0
Maaa 184,16,44
Maaa 216,16,48
Maaa 216,20,144
Maaa 248,20,148
D 17,44,65,1024
v_lshrrev_b32 v28, 1, v1
Maaa 248,16,52
s_lshl_b32 s80, s60, 9
Maaa 88,16,56
s_add_u32 s80, s80, 40960
Maaa 88,20,152
Maaa 120,20,156
D 18,44,65,2048
Maaa 120,16,60
Maaa 124,24,60
Ra 60,26,47104
Maaa 124,28,156
Ra 156,27,47104
Maaa 92,28,152
buffer_load_dwordx2 v[28:29], v28, s[8:11], s80 offen sc1
Ra 152,27,45056
Maaa 92,24,56
Ta 8,11,32768
Ta 10,11,33792
Maaa 252,24,52
Ra 56,26,45056
Ta 12,11,40960
Maaa 252,28,148
Ta 14,11,41984
Ra 52,26,43008
Maaa 220,28,144
D 19,44,65,3072
Ra 148,27,43008
Maaa 220,24,48
s_add_u32 m0, s62, 4096
Ra 144,27,40960
Ra 48,26,40960
Maaa 188,24,44
Ra 44,26,38912
Maaa 188,28,140
Ra 140,27,38912
Mvaa 252,28,136
D 20,44,65,0
Ra 136,27,36864
Mvaa 252,24,40
Ra 40,26,36864
Mvaa 220,24,36
Ra 36,26,34816
Mvaa 220,28,132
Ra 132,27,34816
Mvaa 188,28,128
D 21,44,65,1024
Ra 128,27,32768
Mvaa 188,24,32
W 13
Ra 32,26,32768
Maaa 96,0,60
Maaa 96,4,156
Maaa 64,4,152
D 22,44,65,2048
Ta 16,12,32768
Maaa 64,0,56
W 9
Ta 18,12,33792
Ta 20,12,40960
Maaa 224,0,52
Ta 22,12,41984
Maaa 224,4,148
Maaa 192,4,144
D 23,44,65,3072
Maaa 192,0,48
W 8
s_add_u32 m0, s61, 0
Maaa 160,0,44
Maaa 160,4,140
Mvaa 224,4,136
D 2,40,64,0
Mvaa 224,0,40
W 4
Mvaa 192,0,36
Mvaa 192,4,132
Mvaa 160,4,128
D 3,40,64,1024
Mvaa 160,0,32
Mvaa 164,8,32
Mvaa 164,12,128
Mvaa 196,12,132
D 4,40,64,2048
Ta 24,13,32768
Mvaa 196,8,36
Ta 26,13,33792
Ta 28,13,40960
Mvaa 228,8,40
Ta 30,13,41984
Mvaa 228,12,136
Maaa 164,12,140
D 5,40,64,3072
Maaa 164,8,44
s_add_u32 m0, s61, 4096
Maaa 196,8,48
Maaa 196,12,144
Maaa 228,12,148
D 6,40,64,0
Maaa 228,8,52
Maaa 68,8,56
Maaa 68,12,152
Maaa 100,12,156
D 7,40,64,1024
Maaa 100,8,60
W 4
Maaa 104,16,60
s_waitcnt vmcnt(11)
v_cmp_eq_u32 vcc, s13, v29
s_cmp_eq_u64 vcc, -1
s_cbranch_scc0 .Lspin_15%=
.Lok_16%=:
Maaa 104,20,156
Maaa 72,20,152
D 8,40,64,2048
Ta 0,14,32768
Maaa 72,16,56
Ta 2,14,33792
v_bfe_u32 v144, v1, 8, 1
Maaa 232,16,52
Ta 4,14,40960
v_mul_u32_u24 v144, 10240, v144
Maaa 232,20,148
Ta 6,14,41984
v_and_b32 v145, 0xf0, v1
Maaa 200,20,144
D 9,40,64,3072
v_lshl_add_u32 v144, v145, 3, v144
Maaa 200,16,48
s_mul_i32 s91, s58, 20480
s_add_u32 s91, s91, s28
Maaa 168,16,44
Maaa 168,20,140
Mvaa 232,20,136
global_load_dword v142, v146, s[6:7]
Mvaa 232,16,40
s_add_u32 s92, s92, 2097152
Mvaa 200,16,36
Mvaa 200,20,132
Mvaa 168,20,128
buffer_load_dword v142, v147, s[48:51], s92 offen
Mvaa 168,16,32
W 4
s_add_u32 s92, s92, 2097152
Mvaa 172,24,32
Mvaa 172,28,128
Mvaa 204,28,132
buffer_load_dword v142, v147, s[48:51], s92 offen
Ta 8,15,32768
Mvaa 204,24,36
Ta 10,15,33792
Ta 12,15,40960
Mvaa 236,24,40
Ta 14,15,41984
Mvaa 236,28,136
Maaa 172,28,140
buffer_load_dword v142, v144, s[8:11], s91 offen offset:0 sc1
Maaa 172,24,44
Maaa 204,24,48
Maaa 204,28,144
Maaa 236,28,148
Maaa 236,24,52
Maaa 76,24,56
Maaa 76,28,152
Maaa 108,28,156
Maaa 108,24,60
W 4
Maaa 112,0,60
Maaa 112,4,156
Maaa 80,4,152
Ta 16,24,32768
Ta 18,24,33792
Maaa 80,0,56
Ta 20,24,40960
Ta 22,24,41984
Maaa 240,0,52
Maaa 240,4,148
Maaa 208,4,144
Maaa 208,0,48
Maaa 176,0,44
Maaa 176,4,140
Mvaa 240,4,136
Mvaa 240,0,40
Mvaa 208,0,36
Mvaa 208,4,132
Mvaa 176,4,128
Mvaa 176,0,32
W 4
Mvaa 180,8,32
Ta 24,25,32768
Ta 26,25,33792
Mvaa 180,12,128
Mvaa 212,12,132
Ta 28,25,40960
Ta 30,25,41984
Mvaa 212,8,36
Mvaa 244,8,40
Mvaa 244,12,136
)"
R"(Maaa 180,12,140
Maaa 180,8,44
Maaa 212,8,48
Maaa 212,12,144
Maaa 244,12,148
s_waitcnt vmcnt(4) lgkmcnt(0)
s_barrier
s_add_u32 s64, s64, 128
Maaa 244,8,52
Maaa 84,8,56
Maaa 84,12,152
s_and_b32 s64, s64, 1023
Maaa 116,12,156
Maaa 116,8,60
Maaa 120,16,60
s_lshl_b32 s65, s64, 12
Maaa 120,20,156
Maaa 88,20,152
Ta 0,10,0
Ta 2,10,1024
Maaa 88,16,56
s_add_u32 m0, s62, 32768
Ta 4,10,8192
Ta 6,10,9216
Maaa 248,16,52
Maaa 248,20,148
Maaa 216,20,144
D 16,44,65,0
Maaa 216,16,48
Maaa 184,16,44
Maaa 184,20,140
Mvaa 248,20,136
D 17,44,65,1024
Mvaa 248,16,40
Mvaa 216,16,36
Mvaa 216,20,132
Mvaa 184,20,128
D 18,44,65,2048
Mvaa 184,16,32
Mvaa 188,24,32
Ra 32,26,0
Mvaa 188,28,128
Ra 128,27,0
Mvaa 220,28,132
D 19,44,65,3072
Ra 132,27,2048
Mvaa 220,24,36
s_add_u32 m0, s62, 36864
Ta 8,11,0
Ta 10,11,1024
Mvaa 252,24,40
Ra 36,26,2048
Ta 12,11,8192
Mvaa 252,28,136
Ta 14,11,9216
Ra 40,26,4096
Maaa 188,28,140
D 20,44,65,0
Ra 136,27,4096
Maaa 188,24,44
Ra 140,27,6144
Ra 44,26,6144
Maaa 220,24,48
Ra 48,26,8192
Maaa 220,28,144
Ra 144,27,8192
Maaa 252,28,148
D 21,44,65,1024
Ra 148,27,10240
Maaa 252,24,52
Ra 52,26,10240
Maaa 92,24,56
Ra 56,26,12288
Maaa 92,28,152
Ra 152,27,12288
Maaa 124,28,156
D 22,44,65,2048
Ra 156,27,14336
Maaa 124,24,60
W 13
Ra 60,26,14336
Mvaa 160,0,32
Mvaa 160,4,128
Mvaa 192,4,132
D 23,44,65,3072
Ta 16,12,0
Mvaa 192,0,36
W 9
s_add_u32 m0, s61, 32768
Ta 18,12,1024
Mvaa 224,0,40
Ta 20,12,8192
Ta 22,12,9216
Mvaa 224,4,136
.Lh_0_3%=:
s_cbranch_execz .Lh_0_3_1%=
Maaa 160,4,140
D 2,40,64,0
Maaa 160,0,44
W 8
Maaa 192,0,48
Maaa 192,4,144
Maaa 224,4,148
D 3,40,64,1024
Maaa 224,0,52
W 4
Maaa 64,0,56
Maaa 64,4,152
Maaa 96,4,156
D 4,40,64,2048
Maaa 96,0,60
Maaa 100,8,60
Maaa 100,12,156
Maaa 68,12,152
D 5,40,64,3072
Ta 24,13,0
Maaa 68,8,56
s_add_u32 m0, s61, 36864
Ta 26,13,1024
Ta 28,13,8192
Maaa 228,8,52
Ta 30,13,9216
Maaa 228,12,148
Maaa 196,12,144
D 6,40,64,0
Maaa 196,8,48
Maaa 164,8,44
Maaa 164,12,140
Mvaa 228,12,136
D 7,40,64,1024
Mvaa 228,8,40
Mvaa 196,8,36
Mvaa 196,12,132
Mvaa 164,12,128
D 8,40,64,2048
Mvaa 164,8,32
W 4
Mvaa 168,16,32
Mvaa 168,20,128
Mvaa 200,20,132
D 9,40,64,3072
v_add_u32 v2, 131072, v10
Mvaa 200,16,36
Ta 0,14,0
v_add_u32 v3, 131072, v11
Mvaa 232,16,40
Ta 2,14,1024
v_add_u32 v4, 131072, v12
Mvaa 232,20,136
Ta 4,14,8192
Ta 6,14,9216
Maaa 168,20,140
buffer_load_dword v142, v144, s[8:11], s91 offen offset:2048 sc1
v_add_u32 v5, 131072, v13
Maaa 168,16,44
s_add_u32 s91, s91, 4096
v_add_u32 v6, 131072, v14
Maaa 200,16,48
v_add_u32 v7, 131072, v15
Maaa 200,20,144
Maaa 232,20,148
v_add_u32 v8, 131072, v24
Maaa 232,16,52
Maaa 72,16,56
v_add_u32 v9, 131072, v25
Maaa 72,20,152
s_add_u32 s65, s5, 0
s_cmp_ge_u32 s65, 20
s_cselect_b32 s90, 20, 0
s_sub_u32 s65, s65, s90
Maaa 104,20,156
s_lshl_b32 s65, s65, 18
s_add_u32 m0, s63, 131072
Maaa 104,16,60
W 4
Maaa 108,24,60
Maaa 108,28,156
Maaa 76,28,152
Ta 8,15,0
Ta 10,15,1024
Maaa 76,24,56
D 16,48,65,0
Ta 12,15,8192
Maaa 236,24,52
Ta 14,15,9216
Maaa 236,28,148
Maaa 204,28,144
Maaa 204,24,48
D 17,48,65,1024
Maaa 172,24,44
Maaa 172,28,140
Mvaa 236,28,136
Mvaa 236,24,40
Mvaa 204,24,36
D 18,48,65,2048
Mvaa 204,28,132
Mvaa 172,28,128
Mvaa 172,24,32
W 4
Mvaa 176,0,32
Mvaa 176,4,128
D 19,48,65,3072
Mvaa 208,4,132
s_add_u32 m0, s63, 135168
Ta 16,24,0
Ta 18,24,1024
Mvaa 208,0,36
Ta 20,24,8192
Ta 22,24,9216
Mvaa 240,0,40
Mvaa 240,4,136
Maaa 176,4,140
D 20,48,65,0
Maaa 176,0,44
Maaa 208,0,48
Maaa 208,4,144
Maaa 240,4,148
Maaa 240,0,52
D 21,48,65,1024
Maaa 80,0,56
Maaa 80,4,152
Maaa 112,4,156
Maaa 112,0,60
W 4
Maaa 116,8,60
D 22,48,65,2048
Ta 24,25,0
Maaa 116,12,156
Ta 26,25,1024
Maaa 84,12,152
Ta 28,25,8192
Ta 30,25,9216
Maaa 84,8,56
Maaa 244,8,52
D 23,48,65,3072
Maaa 244,12,148
Maaa 212,12,144
Maaa 212,8,48
Maaa 180,8,44
Maaa 180,12,140
Mvaa 244,12,136
s_waitcnt vmcnt(9) lgkmcnt(0)
s_barrier
s_add_u32 s65, s5, 4
Mvaa 244,8,40
Mvaa 212,8,36
Mvaa 212,12,132
s_cmp_ge_u32 s65, 20
s_cselect_b32 s90, 20, 0
Mvaa 180,12,128
Mvaa 180,8,32
s_sub_u32 s65, s65, s90
Mvaa 184,16,32
Mvaa 184,20,128
s_lshl_b32 s65, s65, 18
Mvaa 216,20,132
Ta 0,10,32768
Ta 2,10,33792
Mvaa 216,16,36
Mvaa 248,16,40
s_add_u32 m0, s63, 0
Mvaa 248,20,136
Maaa 184,20,140
D 16,48,65,0
Maaa 184,16,44
Maaa 216,16,48
Maaa 216,20,144
Maaa 248,20,148
D 17,48,65,1024
Ta 4,11,32768
Maaa 248,16,52
Ta 6,11,33792
Maaa 88,16,56
Maaa 88,20,152
Maaa 120,20,156
D 18,48,65,2048
Maaa 120,16,60
Maaa 124,24,60
Ra 60,26,47104
Maaa 124,28,156
Maaa 92,28,152
D 19,48,65,3072
Ta 8,12,32768
Maaa 92,24,56
s_add_u32 m0, s63, 4096
Ta 10,12,33792
Ra 56,26,45056
Maaa 252,24,52
Ra 52,26,43008
Maaa 252,28,148
Maaa 220,28,144
D 20,48,65,0
Maaa 220,24,48
Ra 48,26,40960
Maaa 188,24,44
Ra 44,26,38912
Maaa 188,28,140
Mvaa 252,28,136
D 21,48,65,1024
Ta 12,13,32768
Mvaa 252,24,40
Ta 14,13,33792
Ra 40,26,36864
Mvaa 220,24,36
Ra 36,26,34816
Mvaa 220,28,132
Mvaa 188,28,128
D 22,48,65,2048
Mvaa 188,24,32
Ra 32,26,32768
W 6
Maaa 96,0,60
Maaa 64,0,56
Maaa 224,0,52
D 23,48,65,3072
Ta 16,14,32768
Maaa 192,0,48
W 1
s_add_u32 s65, s5, 8
s_cmp_ge_u32 s65, 20
s_cselect_b32 s90, 20, 0
Maaa 160,0,44
s_sub_u32 s65, s65, s90
s_lshl_b32 s65, s65, 18
Ta 18,14,33792
Mvaa 224,0,40
s_add_u32 m0, s63, 65536
Mvaa 192,0,36
D 16,48,65,0
Mvaa 160,0,32
Mvaa 164,4,32
Mvaa 196,4,36
Mvaa 228,4,40
D 17,48,65,1024
Ta 20,15,32768
Maaa 164,4,44
Ta 22,15,33792
Maaa 196,4,48
Maaa 228,4,52
Maaa 68,4,56
D 18,48,65,2048
Maaa 100,4,60
Maaa 104,8,60
Maaa 72,8,56
Maaa 232,8,52
D 19,48,65,3072
Ta 24,24,32768
Maaa 200,8,48
s_add_u32 m0, s63, 69632
Ta 26,24,33792
Maaa 168,8,44
Mvaa 232,8,40
Mvaa 200,8,36
D 20,48,65,0
Mvaa 168,8,32
Mvaa 172,12,32
Mvaa 204,12,36
Mvaa 236,12,40
D 21,48,65,1024
Ta 28,25,32768
Maaa 172,12,44
Ta 30,25,33792
Maaa 204,12,48
Ra 156,27,47104
Maaa 236,12,52
Maaa 76,12,56
D 22,48,65,2048
Maaa 108,12,60
W 7
Ra 152,27,45056
Maaa 112,16,60
Maaa 80,16,56
Maaa 240,16,52
D 23,48,65,3072
Ra 148,27,43008
Maaa 208,16,48
Ta 0,10,40960
Ta 2,10,41984
Maaa 176,16,44
Mvaa 240,16,40
Ra 144,27,40960
Mvaa 208,16,36
buffer_load_dword v142, v144, s[8:11], s91 offen offset:0 sc1
Mvaa 176,16,32
W 10
Mvaa 180,20,32
Ra 140,27,38912
Mvaa 212,20,36
Mvaa 244,20,40
Ta 4,11,40960
Ta 6,11,41984
Maaa 180,20,44
Ra 136,27,36864
Maaa 212,20,48
Maaa 244,20,52
v_bfe_u32 v29, v1, 7, 1
Maaa 84,20,56
Ra 132,27,34816
Maaa 116,20,60
W 13
)"
R"(v_mul_u32_u24 v28, 10240, v29
Maaa 120,24,60
Maaa 88,24,56
Ra 128,27,32768
v_lshrrev_b32 v29, 8, v1
Maaa 248,24,52
Ta 8,12,40960
Ta 10,12,41984
Maaa 216,24,48
v_lshl_add_u32 v28, v29, 7, v28
Maaa 184,24,44
v_and_b32 v29, 0x70, v1
Mvaa 248,24,40
Mvaa 216,24,36
v_add_u32 v28, v28, v29
Mvaa 184,24,32
W 14
Mvaa 188,28,32
s_and_b32 s80, s14, 2
Mvaa 220,28,36
s_mul_i32 s80, s80, 20480
Mvaa 252,28,40
Ta 12,13,40960
Ta 14,13,41984
Maaa 188,28,44
s_sub_u32 s81, 40960, s80
Maaa 220,28,48
Maaa 252,28,52
s_add_u32 s93, s28, 20480
Maaa 92,28,56
Maaa 124,28,60
W 10
v_add_u32 v26, s80, v28
Maaa 96,0,156
Maaa 64,0,152
Maaa 224,0,148
Ta 16,14,40960
v_add_u32 v27, s81, v28
Maaa 192,0,144
W 5
Ta 18,14,41984
Maaa 160,0,140
buffer_load_dwordx4 a[32:35], v26, s[8:11], s28 offen offset:0 sc1
Mvaa 224,0,136
Mvaa 192,0,132
Mvaa 160,0,128
Mvaa 164,4,128
buffer_load_dwordx4 a[36:39], v26, s[8:11], s93 offen offset:0 sc1
Mvaa 196,4,132
Mvaa 228,4,136
Ta 20,15,40960
Ta 22,15,41984
Maaa 164,4,140
Maaa 196,4,144
buffer_load_dwordx4 a[40:43], v27, s[8:11], s28 offen offset:0 sc1
Maaa 228,4,148
Maaa 68,4,152
Maaa 100,4,156
W 6
Maaa 104,8,156
buffer_load_dwordx4 a[44:47], v27, s[8:11], s93 offen offset:0 sc1
Ta 24,24,40960
Maaa 72,8,152
s_add_u32 s28, s28, 512
s_cmp_eq_u32 s28, s4
s_cselect_b32 s93, 10240, 0
Ta 26,24,41984
Maaa 232,8,148
s_sub_u32 s28, s28, s93
Ta 28,25,40960
Ta 30,25,41984
Maaa 200,8,144
s_add_u32 s93, s28, 20480
Maaa 168,8,140
Mvaa 232,8,136
Mvaa 200,8,132
s_add_u32 s65, s5, 12
s_cmp_ge_u32 s65, 20
s_cselect_b32 s90, 20, 0
buffer_load_dwordx4 a[48:51], v26, s[8:11], s28 offen offset:0 sc1
Mvaa 168,8,128
W 8
s_sub_u32 s65, s65, s90
s_lshl_b32 s65, s65, 18
Mvaa 172,12,128
s_add_u32 m0, s63, 32768
Mvaa 204,12,132
Mvaa 236,12,136
s_waitcnt vmcnt(6) lgkmcnt(0)
s_barrier
buffer_load_dwordx4 a[52:55], v26, s[8:11], s93 offen offset:0 sc1
Maaa 172,12,140
Maaa 204,12,144
Maaa 236,12,148
Maaa 76,12,152
buffer_load_dwordx4 a[56:59], v27, s[8:11], s28 offen offset:0 sc1
Maaa 108,12,156
Maaa 112,16,156
Maaa 80,16,152
Maaa 240,16,148
buffer_load_dwordx4 a[60:63], v27, s[8:11], s93 offen offset:0 sc1
Maaa 208,16,144
Maaa 176,16,140
Mvaa 240,16,136
Mvaa 208,16,132
D 16,48,65,0
Mvaa 176,16,128
Mvaa 180,20,128
Mvaa 212,20,132
Mvaa 244,20,136
D 17,48,65,1024
Ta 0,2,0
Maaa 180,20,140
Ta 2,2,1024
Maaa 212,20,144
Maaa 244,20,148
Maaa 84,20,152
D 18,48,65,2048
Ta 4,2,8192
Maaa 116,20,156
Ta 6,2,9216
Maaa 120,24,156
Maaa 88,24,152
Maaa 248,24,148
Ta 8,3,0
Ta 10,3,1024
Maaa 216,24,144
D 19,48,65,3072
Maaa 184,24,140
s_add_u32 m0, s63, 36864
Mvaa 248,24,136
Mvaa 216,24,132
Ta 12,3,8192
Ta 14,3,9216
Mvaa 184,24,128
D 20,48,65,0
Mvaa 188,28,128
Mvaa 220,28,132
Mvaa 252,28,136
Ta 16,4,0
Ta 18,4,1024
Maaa 188,28,140
Maaa 220,28,144
D 21,48,65,1024
Maaa 252,28,148
Maaa 92,28,152
Maaa 124,28,156
s_branch .Lq_11%=
.p2align 12
.Lv_10%=:
s_waitcnt vmcnt(14)
s_barrier
Ta 0,10,16384
Ta 2,10,17408
Ra 32,26,0
Ta 4,10,24576
.Lh_0_0_1%=:
s_cbranch_execz .Lhr_9%=
Ta 6,10,25600
Ra 128,27,0
Ra 132,27,2048
Ra 36,26,2048
Ra 40,26,4096
Ra 136,27,4096
Ra 140,27,6144
Ra 44,26,6144
Ra 48,26,8192
Ra 144,27,8192
W 6
Zvaa 160,32,0
s_add_u32 m0, s61, 32768
Ra 148,27,10240
Ra 52,26,10240
Ra 56,26,12288
Ra 152,27,12288
Ra 156,27,14336
Ra 60,26,14336
Mvaa 160,128,4
Ta 8,11,16384
Ta 10,11,17408
Ta 12,11,24576
Ta 14,11,25600
Zvaa 164,132,4
Ta 16,12,16384
Ta 18,12,17408
Mvaa 164,36,0
W 14
Ta 20,12,24576
Ta 22,12,25600
Zvaa 224,40,0
D 2,40,64,0
Mvaa 224,136,4
Zvaa 228,140,4
Mvaa 228,44,0
W 12
Zaaa 192,48,0
D 3,40,64,1024
Maaa 192,144,4
Zaaa 196,148,4
s_waitcnt vmcnt(13)
Maaa 196,52,0
W 8
v_lshlrev_b32 v56, 16, v32
v_and_b32 v57, 0xffff0000, v32
v_lshlrev_b32 v58, 16, v33
v_and_b32 v59, 0xffff0000, v33
Zaaa 64,56,0
D 4,40,64,2048
v_lshlrev_b32 v60, 16, v34
v_and_b32 v61, 0xffff0000, v34
v_lshlrev_b32 v62, 16, v35
v_and_b32 v63, 0xffff0000, v35
Maaa 64,152,4
v_lshlrev_b32 v64, 16, v36
v_and_b32 v65, 0xffff0000, v36
v_lshlrev_b32 v66, 16, v37
v_and_b32 v67, 0xffff0000, v37
v_lshlrev_b32 v68, 16, v38
Zaaa 68,156,4
v_and_b32 v69, 0xffff0000, v38
v_lshlrev_b32 v70, 16, v39
v_and_b32 v71, 0xffff0000, v39
v_lshlrev_b32 v72, 16, v40
Maaa 68,60,0
W 4
v_and_b32 v73, 0xffff0000, v40
v_lshlrev_b32 v74, 16, v41
v_and_b32 v75, 0xffff0000, v41
v_lshlrev_b32 v76, 16, v42
v_and_b32 v77, 0xffff0000, v42
Zaaa 76,60,8
D 5,40,64,3072
v_lshlrev_b32 v78, 16, v43
v_and_b32 v79, 0xffff0000, v43
A 80,56,57
A 81,58,59
Maaa 76,156,12
s_add_u32 m0, s61, 36864
A 80,80,60
A 81,81,61
A 80,80,62
A 81,81,63
A 80,80,64
Zaaa 72,152,12
Ta 24,13,16384
Ta 26,13,17408
A 81,81,65
A 80,80,66
A 81,81,67
A 80,80,68
Maaa 72,56,8
Ta 28,13,24576
Ta 30,13,25600
A 81,81,69
A 80,80,70
A 81,81,71
A 80,80,72
Zaaa 204,52,8
D 6,40,64,0
A 81,81,73
A 80,80,74
A 81,81,75
A 80,80,76
A 81,81,77
Maaa 204,148,12
A 80,80,78
A 81,81,79
A 80,80,81
s_nop 1
v_add_f32_dpp v80, v80, v80 quad_perm:[1,0,3,2] row_mask:0xf bank_mask:0xf
Zaaa 200,144,12
Maaa 200,48,8
v_add_f32_dpp v80, v80, v80 quad_perm:[2,3,0,1] row_mask:0xf bank_mask:0xf
s_nop 1
v_add_f32_dpp v80, v80, v80 row_half_mirror row_mask:0xf bank_mask:0xf
s_nop 1
v_add_f32_dpp v80, v80, v80 row_mirror row_mask:0xf bank_mask:0xf
v_mov_b32 v81, v80
s_nop 1
v_permlane16_swap_b32 v80, v81
A 80,80,81
Zvaa 236,44,8
D 7,40,64,1024
v_mul_f32 v84, 0x3acccccd, v80
s_mov_b32 s86, 0xffff
s_mov_b32 s87, 0xffff
v_sub_f32 v82, v56, v84
U 80,82,82
v_sub_f32 v83, v57, v84
Mvaa 236,140,12
v_fmac_f32 v80, v83, v83
v_sub_f32 v82, v58, v84
v_fmac_f32 v80, v82, v82
v_sub_f32 v83, v59, v84
v_fmac_f32 v80, v83, v83
v_sub_f32 v82, v60, v84
Zvaa 232,136,12
v_fmac_f32 v80, v82, v82
v_sub_f32 v83, v61, v84
v_fmac_f32 v80, v83, v83
v_sub_f32 v82, v62, v84
v_fmac_f32 v80, v82, v82
Mvaa 232,40,8
v_sub_f32 v83, v63, v84
v_fmac_f32 v80, v83, v83
v_sub_f32 v82, v64, v84
v_fmac_f32 v80, v82, v82
Zvaa 172,36,8
D 8,40,64,2048
v_sub_f32 v83, v65, v84
v_fmac_f32 v80, v83, v83
v_sub_f32 v82, v66, v84
v_fmac_f32 v80, v82, v82
Mvaa 172,132,12
v_sub_f32 v83, v67, v84
v_fmac_f32 v80, v83, v83
v_sub_f32 v82, v68, v84
v_fmac_f32 v80, v82, v82
v_sub_f32 v83, v69, v84
Zvaa 168,128,12
v_fmac_f32 v80, v83, v83
v_sub_f32 v82, v70, v84
v_fmac_f32 v80, v82, v82
v_sub_f32 v83, v71, v84
Mvaa 168,32,8
W 4
v_fmac_f32 v80, v83, v83
v_sub_f32 v82, v72, v84
U 81,82,82
v_sub_f32 v83, v73, v84
v_fmac_f32 v81, v83, v83
Zvaa 176,32,16
D 9,40,64,3072
v_sub_f32 v82, v74, v84
v_fmac_f32 v81, v82, v82
v_sub_f32 v83, v75, v84
v_fmac_f32 v81, v83, v83
Mvaa 176,128,20
v_sub_f32 v82, v76, v84
v_fmac_f32 v81, v82, v82
v_sub_f32 v83, v77, v84
v_fmac_f32 v81, v83, v83
v_sub_f32 v82, v78, v84
Zvaa 180,132,20
Ta 0,14,16384
Ta 2,14,17408
v_fmac_f32 v81, v82, v82
v_sub_f32 v83, v79, v84
v_fmac_f32 v81, v83, v83
v_cndmask_b32_e64 v81, 0, v81, s[86:87]
Mvaa 180,36,16
Ta 4,14,24576
Ta 6,14,25600
A 80,80,81
s_nop 1
v_add_f32_dpp v80, v80, v80 quad_perm:[1,0,3,2] row_mask:0xf bank_mask:0xf
s_nop 1
v_add_f32_dpp v80, v80, v80 quad_perm:[2,3,0,1] row_mask:0xf bank_mask:0xf
s_nop 1
v_add_f32_dpp v80, v80, v80 row_half_mirror row_mask:0xf bank_mask:0xf
Zvaa 240,40,16
global_load_dword v142, v[140:141], off
v_lshl_add_u64 v[140:141], v[140:141], 0, s[68:69]
Mvaa 240,136,20
v_add_f32_dpp v80, v80, v80 row_mirror row_mask:0xf bank_mask:0xf
v_mov_b32 v81, v80
s_nop 1
v_permlane16_swap_b32 v80, v81
A 80,80,81
v_mul_f32 v80, 0x3acccccd, v80
K 80,80,80
Zvaa 244,140,20
v_lshlrev_b32 v80, 16, v80
v_add_f32 v80, 0x3727c5ac, v80
K 80,80,80
v_lshlrev_b32 v80, 16, v80
v_sqrt_f32 v81, v80
Mvaa 244,44,16
v_rcp_f32 v82, v81
v_fma_f32 v83, -v81, v81, v80
v_mul_f32 v82, 0.5, v82
v_fma_f32 v81, v83, v82, v81
K 86,81,81
Zaaa 208,48,16
v_lshlrev_b32 v86, 16, v86
v_rcp_f32 v87, v86
K 85,84,84
v_lshlrev_b32 v85, 16, v85
Maaa 208,144,20
s_waitcnt vmcnt(17)
v_sub_f32 v56, v56, v85
v_sub_f32 v57, v57, v85
K 80,56,57
Zaaa 212,148,20
v_lshlrev_b32 v56, 16, v80
v_and_b32 v57, 0xffff0000, v80
U 80,56,87
U 81,57,87
K 32,80,81
Maaa 212,52,16
)"
R"(v_lshlrev_b32 v80, 16, v32
v_and_b32 v81, 0xffff0000, v32
v_lshlrev_b32 v82, 16, v44
v_and_b32 v83, 0xffff0000, v44
Zaaa 80,56,16
U 84,80,82
v_fmac_f32 v84, v81, v83
v_sub_f32 v58, v58, v85
v_sub_f32 v59, v59, v85
Maaa 80,152,20
K 80,58,59
v_lshlrev_b32 v58, 16, v80
v_and_b32 v59, 0xffff0000, v80
U 80,58,87
U 81,59,87
Zaaa 84,156,20
K 33,80,81
v_lshlrev_b32 v80, 16, v33
v_and_b32 v81, 0xffff0000, v33
v_lshlrev_b32 v82, 16, v45
Maaa 84,60,16
W 4
v_and_b32 v83, 0xffff0000, v45
v_fmac_f32 v84, v80, v82
v_fmac_f32 v84, v81, v83
v_sub_f32 v60, v60, v85
v_sub_f32 v61, v61, v85
Zaaa 92,60,24
K 80,60,61
v_lshlrev_b32 v60, 16, v80
v_and_b32 v61, 0xffff0000, v80
U 80,60,87
Maaa 92,156,28
U 81,61,87
K 34,80,81
v_lshlrev_b32 v80, 16, v34
v_and_b32 v81, 0xffff0000, v34
v_lshlrev_b32 v82, 16, v46
Zaaa 88,152,28
Ta 8,15,16384
Ta 10,15,17408
v_and_b32 v83, 0xffff0000, v46
v_fmac_f32 v84, v80, v82
v_fmac_f32 v84, v81, v83
v_sub_f32 v62, v62, v85
Maaa 88,56,24
Ta 12,15,24576
v_sub_f32 v63, v63, v85
K 80,62,63
v_lshlrev_b32 v62, 16, v80
v_and_b32 v63, 0xffff0000, v80
U 80,62,87
Zaaa 220,52,24
Ta 14,15,25600
U 81,63,87
K 35,80,81
v_lshlrev_b32 v80, 16, v35
v_and_b32 v81, 0xffff0000, v35
Maaa 220,148,28
v_lshlrev_b32 v82, 16, v47
v_and_b32 v83, 0xffff0000, v47
v_fmac_f32 v84, v80, v82
v_fmac_f32 v84, v81, v83
Zaaa 216,144,28
v_sub_f32 v64, v64, v85
v_sub_f32 v65, v65, v85
K 80,64,65
v_lshlrev_b32 v64, 16, v80
v_and_b32 v65, 0xffff0000, v80
Maaa 216,48,24
U 80,64,87
U 81,65,87
K 36,80,81
v_lshlrev_b32 v80, 16, v36
Zvaa 252,44,24
v_and_b32 v81, 0xffff0000, v36
v_lshlrev_b32 v82, 16, v48
v_and_b32 v83, 0xffff0000, v48
v_fmac_f32 v84, v80, v82
v_fmac_f32 v84, v81, v83
Mvaa 252,140,28
v_sub_f32 v66, v66, v85
v_sub_f32 v67, v67, v85
K 80,66,67
v_lshlrev_b32 v66, 16, v80
Zvaa 248,136,28
v_and_b32 v67, 0xffff0000, v80
U 80,66,87
U 81,67,87
K 37,80,81
v_lshlrev_b32 v80, 16, v37
Mvaa 248,40,24
v_and_b32 v81, 0xffff0000, v37
v_lshlrev_b32 v82, 16, v49
v_and_b32 v83, 0xffff0000, v49
v_fmac_f32 v84, v80, v82
Zvaa 188,36,24
v_fmac_f32 v84, v81, v83
v_sub_f32 v68, v68, v85
v_sub_f32 v69, v69, v85
K 80,68,69
v_lshlrev_b32 v68, 16, v80
Mvaa 188,132,28
v_and_b32 v69, 0xffff0000, v80
U 80,68,87
U 81,69,87
K 38,80,81
Zvaa 184,128,28
v_lshlrev_b32 v80, 16, v38
v_and_b32 v81, 0xffff0000, v38
v_lshlrev_b32 v82, 16, v50
v_and_b32 v83, 0xffff0000, v50
Mvaa 184,32,24
W 4
v_fmac_f32 v84, v80, v82
v_fmac_f32 v84, v81, v83
v_sub_f32 v70, v70, v85
v_sub_f32 v71, v71, v85
K 80,70,71
Zvaa 192,32,0
v_lshlrev_b32 v70, 16, v80
v_and_b32 v71, 0xffff0000, v80
U 80,70,87
U 81,71,87
Mvaa 192,128,4
K 39,80,81
v_lshlrev_b32 v80, 16, v39
v_and_b32 v81, 0xffff0000, v39
v_lshlrev_b32 v82, 16, v51
v_and_b32 v83, 0xffff0000, v51
Zvaa 196,132,4
Ta 16,24,16384
Ta 18,24,17408
v_fmac_f32 v84, v80, v82
v_fmac_f32 v84, v81, v83
v_sub_f32 v72, v72, v85
v_sub_f32 v73, v73, v85
Mvaa 196,36,0
Ta 20,24,24576
K 80,72,73
v_lshlrev_b32 v72, 16, v80
v_and_b32 v73, 0xffff0000, v80
U 80,72,87
U 81,73,87
Zaaa 160,40,0
Ta 22,24,25600
K 40,80,81
v_lshlrev_b32 v80, 16, v40
v_and_b32 v81, 0xffff0000, v40
v_lshlrev_b32 v82, 16, v52
Maaa 160,136,4
v_and_b32 v83, 0xffff0000, v52
v_fmac_f32 v84, v80, v82
v_fmac_f32 v84, v81, v83
v_sub_f32 v74, v74, v85
Zaaa 164,140,4
v_sub_f32 v75, v75, v85
K 80,74,75
v_lshlrev_b32 v74, 16, v80
v_and_b32 v75, 0xffff0000, v80
U 80,74,87
Maaa 164,44,0
U 81,75,87
K 41,80,81
v_lshlrev_b32 v80, 16, v41
v_and_b32 v81, 0xffff0000, v41
Zaaa 224,48,0
v_lshlrev_b32 v82, 16, v53
v_and_b32 v83, 0xffff0000, v53
v_fmac_f32 v84, v80, v82
v_fmac_f32 v84, v81, v83
v_sub_f32 v76, v76, v85
Maaa 224,144,4
v_sub_f32 v77, v77, v85
K 80,76,77
v_lshlrev_b32 v76, 16, v80
v_and_b32 v77, 0xffff0000, v80
Zaaa 228,148,4
U 80,76,87
U 81,77,87
K 42,80,81
v_lshlrev_b32 v80, 16, v42
v_and_b32 v81, 0xffff0000, v42
Maaa 228,52,0
v_lshlrev_b32 v82, 16, v54
v_and_b32 v83, 0xffff0000, v54
v_fmac_f32 v84, v80, v82
v_fmac_f32 v84, v81, v83
Zaaa 96,56,0
v_sub_f32 v78, v78, v85
v_sub_f32 v79, v79, v85
K 80,78,79
v_lshlrev_b32 v78, 16, v80
v_and_b32 v79, 0xffff0000, v80
Maaa 96,152,4
U 80,78,87
U 81,79,87
K 43,80,81
v_lshlrev_b32 v80, 16, v43
Zaaa 100,156,4
v_and_b32 v81, 0xffff0000, v43
v_lshlrev_b32 v82, 16, v55
v_and_b32 v83, 0xffff0000, v55
v_fmac_f32 v84, v80, v82
Maaa 100,60,0
W 4
v_fmac_f32 v84, v81, v83
v_lshrrev_b32 v82, 9, v1
s_lshl_b32 s80, s14, 1
v_add_u32 v82, s80, v82
v_mul_u32_u24 v83, 1552, v82
Zaaa 108,60,8
Ta 24,25,16384
Ta 26,25,17408
v_add_u32 v83, v83, v88
v_add_u32 v83, 131072, v83
ds_write_b128 v83, v[32:35] offset:0
ds_write_b128 v83, v[36:39] offset:512
Maaa 108,156,12
ds_write_b128 v83, v[40:43] offset:1024
v_add_f32_dpp v84, v84, v84 quad_perm:[1,0,3,2] row_mask:0xf bank_mask:0xf
s_nop 1
v_add_f32_dpp v84, v84, v84 quad_perm:[2,3,0,1] row_mask:0xf bank_mask:0xf
s_nop 1
v_add_f32_dpp v84, v84, v84 row_half_mirror row_mask:0xf bank_mask:0xf
s_nop 1
v_add_f32_dpp v84, v84, v84 row_mirror row_mask:0xf bank_mask:0xf
Zaaa 104,152,12
Ta 28,25,24576
Ta 30,25,25600
v_mov_b32 v81, v84
Maaa 104,56,8
Zaaa 236,52,8
v_permlane16_swap_b32 v84, v81
A 84,84,81
K 80,84,84
v_lshlrev_b32 v80, 16, v80
s_lshl_b32 s80, s15, 3
v_add_u32 v82, s80, v82
v_sub_u32 v83, 2047, v82
Maaa 236,148,12
v_ashrrev_i32 v81, 31, v80
v_or_b32 v81, 0x80000000, v81
v_xor_b32 v80, v80, v81
v_and_b32 v80, 0xffff0000, v80
v_or_b32 v80, v80, v83
v_and_b32 v82, 7, v82
Zaaa 232,144,12
v_lshlrev_b32 v82, 2, v82
v_add_u32 v82, 143488, v82
ds_write_b32 v82, v80
Maaa 232,48,8
Zaaa 172,44,8
Maaa 172,140,12
Zaaa 168,136,12
s_waitcnt vmcnt(1) lgkmcnt(0)
s_barrier
s_add_u32 s64, s64, 128
Maaa 168,40,8
v_bfe_u32 v80, v1, 4, 3
Zvaa 204,36,8
v_mul_u32_u24 v81, 1552, v80
Mvaa 204,132,12
s_and_b32 s64, s64, 1023
v_lshrrev_b32 v82, 7, v1
Zvaa 200,128,12
v_lshl_add_u32 v81, v82, 4, v81
Mvaa 200,32,8
s_lshl_b32 s80, s14, 7
Zvaa 208,32,16
s_lshl_b32 s65, s64, 12
s_add_u32 s80, s80, 131072
Mvaa 208,128,20
v_add_u32 v81, s80, v81
Zvaa 212,132,20
s_add_u32 m0, s62, 0
ds_read_b128 v[32:35], v81
Ta 0,10,49152
Mvaa 212,36,16
Ta 2,10,50176
Rv 36,81,512
Zaaa 176,40,16
s_and_b32 s80, s14, 2
Ta 4,10,57344
Ta 6,10,58368
Maaa 176,136,20
s_lshl_b32 s80, s80, 7
D 16,44,65,0
Zaaa 180,140,20
v_subrev_u32 v81, s80, v81
Maaa 180,44,16
Rv 40,81,1024
Zaaa 240,48,16
v_lshlrev_b32 v80, 2, v80
Maaa 240,144,20
D 17,44,65,1024
v_add_u32 v80, 143488, v80
Zaaa 244,148,20
ds_read_b32 v84, v80
Maaa 244,52,16
v_mov_b32 v85, s13
Zaaa 112,56,16
v_lshrrev_b32 v83, 1, v1
Maaa 112,152,20
s_mul_i32 s82, s15, 10240
D 18,44,65,2048
Zaaa 116,156,20
s_add_u32 s82, s82, 131072
Maaa 116,60,16
s_lshl_b32 s80, s14, 10
Zaaa 124,60,24
s_add_u32 s83, s82, s80
Ra 60,26,47104
Maaa 124,156,28
D 19,44,65,3072
Ra 156,27,47104
Zaaa 120,152,28
s_add_u32 m0, s62, 4096
Ra 152,27,45056
Ta 8,11,49152
Maaa 120,56,24
Ta 10,11,50176
Ra 56,26,45056
Zaaa 252,52,24
Ta 12,11,57344
Ta 14,11,58368
Maaa 252,148,28
W 8
buffer_store_dwordx4 v[32:35], v1, s[8:11], s83 offen sc1
s_add_u32 s83, s83, 4096
Zaaa 248,144,28
Ra 52,26,43008
Ra 148,27,43008
Maaa 248,48,24
Ra 144,27,40960
Ra 48,26,40960
Zaaa 188,44,24
Ra 44,26,38912
Maaa 188,140,28
D 20,44,65,0
Ra 140,27,38912
Zaaa 184,136,28
Ra 136,27,36864
Maaa 184,40,24
Ra 40,26,36864
Zvaa 220,36,24
Ra 36,26,34816
Mvaa 220,132,28
buffer_store_dwordx4 v[36:39], v1, s[8:11], s83 offen sc1
Ra 132,27,34816
Zvaa 216,128,28
s_and_b32 s80, s80, 1024
s_add_u32 s83, s82, s80
s_add_u32 s83, s83, 8192
Mvaa 216,32,24
W 12
Ra 128,27,32768
Ra 32,26,32768
Maaa 68,60,0
Maaa 68,156,4
D 21,44,65,1024
Maaa 64,152,4
Ta 16,12,49152
Ta 18,12,50176
Maaa 64,56,0
W 10
Ta 20,12,57344
Ta 22,12,58368
Maaa 196,52,0
Maaa 196,148,4
D 22,44,65,2048
Maaa 192,144,4
Maaa 192,48,0
W 8
Mvaa 228,44,0
Mvaa 228,140,4
buffer_store_dwordx4 v[40:43], v1, s[8:11], s83 offen sc1
Mvaa 224,136,4
Mvaa 224,40,0
W 4
Mvaa 164,36,0
Mvaa 164,132,4
D 23,44,65,3072
Mvaa 160,128,4
s_add_u32 m0, s61, 0
Mvaa 160,32,0
Mvaa 168,32,8
Mvaa 168,128,12
D 2,40,64,0
Mvaa 172,132,12
Ta 24,13,49152
Ta 26,13,50176
Mvaa 172,36,8
Ta 28,13,57344
Ta 30,13,58368
Mvaa 232,40,8
Mvaa 232,136,12
.Lh_0_1_1%=:
s_cbranch_execz .Lhr_9%=
D 3,40,64,1024
)"
R"(Mvaa 236,140,12
Mvaa 236,44,8
Maaa 200,48,8
Maaa 200,144,12
D 4,40,64,2048
Maaa 204,148,12
Maaa 204,52,8
Maaa 72,56,8
Maaa 72,152,12
D 5,40,64,3072
Maaa 76,156,12
s_add_u32 m0, s61, 4096
Maaa 76,60,8
W 4
Maaa 84,60,16
Maaa 84,156,20
D 6,40,64,0
Maaa 80,152,20
Ta 0,14,49152
Ta 2,14,50176
Maaa 80,56,16
Ta 4,14,57344
Ta 6,14,58368
Maaa 212,52,16
Maaa 212,148,20
D 7,40,64,1024
Maaa 208,144,20
Maaa 208,48,16
Mvaa 244,44,16
Mvaa 244,140,20
D 8,40,64,2048
Mvaa 240,136,20
Mvaa 240,40,16
Mvaa 180,36,16
Mvaa 180,132,20
D 9,40,64,3072
Mvaa 176,128,20
Mvaa 176,32,16
W 4
Mvaa 184,32,24
Mvaa 184,128,28
global_load_dword v142, v[140:141], off
v_lshl_add_u64 v[140:141], v[140:141], 0, s[68:69]
Mvaa 188,132,28
Ta 8,15,49152
Ta 10,15,50176
Mvaa 188,36,24
Ta 12,15,57344
Ta 14,15,58368
Mvaa 248,40,24
Mvaa 248,136,28
Mvaa 252,140,28
Mvaa 252,44,24
Maaa 216,48,24
Maaa 216,144,28
Maaa 220,148,28
Maaa 220,52,24
Maaa 88,56,24
Maaa 88,152,28
Maaa 92,156,28
Maaa 92,60,24
W 4
Maaa 100,60,0
Maaa 100,156,4
Maaa 96,152,4
Ta 16,24,49152
Ta 18,24,50176
Maaa 96,56,0
Ta 20,24,57344
Ta 22,24,58368
Maaa 228,52,0
Maaa 228,148,4
Maaa 224,144,4
Maaa 224,48,0
Maaa 164,44,0
Maaa 164,140,4
Maaa 160,136,4
Maaa 160,40,0
Mvaa 196,36,0
Mvaa 196,132,4
Mvaa 192,128,4
Mvaa 192,32,0
W 4
Mvaa 200,32,8
Ta 24,25,49152
Ta 26,25,50176
Mvaa 200,128,12
Mvaa 204,132,12
Ta 28,25,57344
Ta 30,25,58368
Mvaa 204,36,8
Maaa 168,40,8
Maaa 168,136,12
Maaa 172,140,12
Maaa 172,44,8
Maaa 232,48,8
Maaa 232,144,12
Maaa 236,148,12
s_waitcnt vmcnt(1) lgkmcnt(0)
s_barrier
s_cmp_eq_u32 s14, 0
s_cbranch_scc0 .Lpubr_19%=
s_lshl_b32 s80, s15, 6
s_add_u32 s80, s80, 40960
s_mov_b64 exec, 0xff
buffer_store_dwordx2 v[84:85], v83, s[8:11], s80 offen sc1
s_mov_b64 exec, -1
.Lpubr_19%=:
Maaa 236,52,8
s_add_u32 s64, s64, 128
Maaa 104,56,8
Maaa 104,152,12
s_and_b32 s64, s64, 1023
Maaa 108,156,12
Maaa 108,60,8
Maaa 116,60,16
s_lshl_b32 s65, s64, 12
Maaa 116,156,20
Maaa 112,152,20
Ta 0,10,16384
Ta 2,10,17408
Maaa 112,56,16
s_add_u32 m0, s62, 32768
Ta 4,10,24576
Ta 6,10,25600
Maaa 244,52,16
Maaa 244,148,20
Maaa 240,144,20
D 16,44,65,0
Maaa 240,48,16
Maaa 180,44,16
Maaa 180,140,20
Maaa 176,136,20
D 17,44,65,1024
Maaa 176,40,16
Mvaa 212,36,16
Mvaa 212,132,20
Mvaa 208,128,20
D 18,44,65,2048
Mvaa 208,32,16
Mvaa 216,32,24
Ra 32,26,0
Mvaa 216,128,28
Ra 128,27,0
Mvaa 220,132,28
D 19,44,65,3072
Ra 132,27,2048
Mvaa 220,36,24
s_add_u32 m0, s62, 36864
Ta 8,11,16384
Ta 10,11,17408
Maaa 184,40,24
Ra 36,26,2048
Ta 12,11,24576
Maaa 184,136,28
Ta 14,11,25600
Ra 40,26,4096
Maaa 188,140,28
D 20,44,65,0
Ra 136,27,4096
Maaa 188,44,24
Ra 140,27,6144
Ra 44,26,6144
Maaa 248,48,24
Ra 48,26,8192
Maaa 248,144,28
Ra 144,27,8192
Maaa 252,148,28
D 21,44,65,1024
Ra 148,27,10240
Maaa 252,52,24
Ra 52,26,10240
Maaa 120,56,24
Ra 56,26,12288
Maaa 120,152,28
Ra 152,27,12288
Maaa 124,156,28
D 22,44,65,2048
Ra 156,27,14336
Maaa 124,60,24
W 13
Ra 60,26,14336
Mvaa 160,32,0
Mvaa 160,128,4
Mvaa 164,132,4
D 23,44,65,3072
Ta 16,12,16384
Mvaa 164,36,0
W 9
s_add_u32 m0, s61, 32768
Ta 18,12,17408
Mvaa 224,40,0
Ta 20,12,24576
Ta 22,12,25600
Mvaa 224,136,4
Mvaa 228,140,4
D 2,40,64,0
Mvaa 228,44,0
W 8
Maaa 192,48,0
Maaa 192,144,4
Maaa 196,148,4
D 3,40,64,1024
Maaa 196,52,0
W 4
Maaa 64,56,0
Maaa 64,152,4
Maaa 68,156,4
D 4,40,64,2048
Maaa 68,60,0
Maaa 76,60,8
Maaa 76,156,12
Maaa 72,152,12
D 5,40,64,3072
Ta 24,13,16384
Maaa 72,56,8
s_add_u32 m0, s61, 36864
Ta 26,13,17408
Ta 28,13,24576
Maaa 204,52,8
Ta 30,13,25600
Maaa 204,148,12
Maaa 200,144,12
D 6,40,64,0
Maaa 200,48,8
Mvaa 236,44,8
Mvaa 236,140,12
Mvaa 232,136,12
D 7,40,64,1024
Mvaa 232,40,8
Mvaa 172,36,8
Mvaa 172,132,12
Mvaa 168,128,12
D 8,40,64,2048
Mvaa 168,32,8
W 4
Mvaa 176,32,16
Mvaa 176,128,20
Mvaa 180,132,20
D 9,40,64,3072
Ta 0,14,16384
Mvaa 180,36,16
Ta 2,14,17408
Ta 4,14,24576
Mvaa 240,40,16
Ta 6,14,25600
Mvaa 240,136,20
Mvaa 244,140,20
Mvaa 244,44,16
Maaa 208,48,16
Maaa 208,144,20
Maaa 212,148,20
Maaa 212,52,16
Maaa 80,56,16
Maaa 80,152,20
Maaa 84,156,20
Maaa 84,60,16
W 4
Maaa 92,60,24
Maaa 92,156,28
Maaa 88,152,28
Ta 8,15,16384
Ta 10,15,17408
Maaa 88,56,24
Ta 12,15,24576
Ta 14,15,25600
Maaa 220,52,24
Maaa 220,148,28
Maaa 216,144,28
Maaa 216,48,24
Mvaa 252,44,24
Mvaa 252,140,28
Mvaa 248,136,28
Mvaa 248,40,24
Mvaa 188,36,24
Mvaa 188,132,28
Mvaa 184,128,28
Mvaa 184,32,24
W 4
Mvaa 192,32,0
Mvaa 192,128,4
Mvaa 196,132,4
Ta 16,24,16384
Ta 18,24,17408
Mvaa 196,36,0
Ta 20,24,24576
Ta 22,24,25600
Maaa 160,40,0
Maaa 160,136,4
Maaa 164,140,4
Maaa 164,44,0
Maaa 224,48,0
Maaa 224,144,4
Maaa 228,148,4
Maaa 228,52,0
Maaa 96,56,0
Maaa 96,152,4
Maaa 100,156,4
Maaa 100,60,0
W 4
Maaa 108,60,8
Ta 24,25,16384
Ta 26,25,17408
Maaa 108,156,12
Maaa 104,152,12
Ta 28,25,24576
Ta 30,25,25600
Maaa 104,56,8
Maaa 236,52,8
Maaa 236,148,12
Maaa 232,144,12
Maaa 232,48,8
Maaa 172,44,8
Maaa 172,140,12
Maaa 168,136,12
s_waitcnt vmcnt(0) lgkmcnt(0)
s_barrier
s_add_u32 s64, s64, 128
Maaa 168,40,8
Mvaa 204,36,8
Mvaa 204,132,12
s_and_b32 s64, s64, 1023
Mvaa 200,128,12
Mvaa 200,32,8
Mvaa 208,32,16
s_lshl_b32 s65, s64, 12
Mvaa 208,128,20
Mvaa 212,132,20
s_add_u32 m0, s62, 0
Ta 0,10,49152
Ta 2,10,50176
Mvaa 212,36,16
Ta 4,10,57344
Ta 6,10,58368
Maaa 176,40,16
Maaa 176,136,20
D 16,44,65,0
Maaa 180,140,20
Maaa 180,44,16
Maaa 240,48,16
Maaa 240,144,20
D 17,44,65,1024
Maaa 244,148,20
Maaa 244,52,16
Maaa 112,56,16
Maaa 112,152,20
D 18,44,65,2048
Maaa 116,156,20
Maaa 116,60,16
Maaa 124,60,24
Ra 60,26,47104
Maaa 124,156,28
D 19,44,65,3072
Ra 156,27,47104
Maaa 120,152,28
s_add_u32 m0, s62, 4096
Ra 152,27,45056
Ta 8,11,49152
Maaa 120,56,24
Ta 10,11,50176
Ra 56,26,45056
Maaa 252,52,24
Ta 12,11,57344
Ta 14,11,58368
Maaa 252,148,28
D 20,44,65,0
Ra 52,26,43008
Maaa 248,144,28
Ra 148,27,43008
Ra 144,27,40960
Maaa 248,48,24
Ra 48,26,40960
Maaa 188,44,24
Ra 44,26,38912
Maaa 188,140,28
D 21,44,65,1024
Ra 140,27,38912
Maaa 184,136,28
Ra 136,27,36864
Maaa 184,40,24
Ra 40,26,36864
Mvaa 220,36,24
Ra 36,26,34816
Mvaa 220,132,28
D 22,44,65,2048
Ra 132,27,34816
Mvaa 216,128,28
Ra 128,27,32768
Mvaa 216,32,24
W 13
Ra 32,26,32768
Maaa 68,60,0
Maaa 68,156,4
D 23,44,65,3072
Maaa 64,152,4
s_add_u32 m0, s61, 0
Ta 16,12,49152
Ta 18,12,50176
Maaa 64,56,0
W 10
)"
R"(Ta 20,12,57344
Ta 22,12,58368
Maaa 196,52,0
Maaa 196,148,4
D 2,40,64,0
Maaa 192,144,4
Maaa 192,48,0
W 8
Mvaa 228,44,0
Mvaa 228,140,4
D 3,40,64,1024
Mvaa 224,136,4
Mvaa 224,40,0
W 4
Mvaa 164,36,0
Mvaa 164,132,4
D 4,40,64,2048
Mvaa 160,128,4
Mvaa 160,32,0
Mvaa 168,32,8
Mvaa 168,128,12
D 5,40,64,3072
Mvaa 172,132,12
s_add_u32 m0, s61, 4096
Ta 24,13,49152
Ta 26,13,50176
Mvaa 172,36,8
Ta 28,13,57344
Ta 30,13,58368
Mvaa 232,40,8
Mvaa 232,136,12
D 6,40,64,0
Mvaa 236,140,12
Mvaa 236,44,8
Maaa 200,48,8
Maaa 200,144,12
D 7,40,64,1024
Maaa 204,148,12
Maaa 204,52,8
Maaa 72,56,8
Maaa 72,152,12
D 8,40,64,2048
Maaa 76,156,12
Maaa 76,60,8
W 4
Maaa 84,60,16
Maaa 84,156,20
D 9,40,64,3072
Maaa 80,152,20
Ta 0,14,49152
Ta 2,14,50176
Maaa 80,56,16
Ta 4,14,57344
Ta 6,14,58368
Maaa 212,52,16
Maaa 212,148,20
global_load_dword v142, v[140:141], off
v_lshl_add_u64 v[140:141], v[140:141], 0, s[68:69]
v_bfe_u32 v147, v1, 5, 3
Maaa 208,144,20
v_lshlrev_b32 v147, 13, v147
v_and_b32 v148, 16, v1
Maaa 208,48,16
v_lshl_add_u32 v147, v148, 3, v147
Mvaa 244,44,16
s_lshl_b32 s92, s57, 2
s_add_u32 s92, s92, s14
s_lshl_b32 s92, s92, 16
Mvaa 244,140,20
buffer_load_dword v142, v147, s[48:51], s92 offen
Mvaa 240,136,20
Mvaa 240,40,16
Mvaa 180,36,16
Mvaa 180,132,20
Mvaa 176,128,20
Mvaa 176,32,16
W 4
Mvaa 184,32,24
Mvaa 184,128,28
Mvaa 188,132,28
Ta 8,15,49152
Ta 10,15,50176
Mvaa 188,36,24
Ta 12,15,57344
Ta 14,15,58368
Mvaa 248,40,24
Mvaa 248,136,28
Mvaa 252,140,28
Mvaa 252,44,24
Maaa 216,48,24
Maaa 216,144,28
Maaa 220,148,28
Maaa 220,52,24
Maaa 88,56,24
Maaa 88,152,28
Maaa 92,156,28
Maaa 92,60,24
W 4
Maaa 100,60,0
Maaa 100,156,4
Maaa 96,152,4
Ta 16,24,49152
Ta 18,24,50176
Maaa 96,56,0
Ta 20,24,57344
Ta 22,24,58368
Maaa 228,52,0
Maaa 228,148,4
Maaa 224,144,4
Maaa 224,48,0
Maaa 164,44,0
Maaa 164,140,4
Maaa 160,136,4
Maaa 160,40,0
Mvaa 196,36,0
Mvaa 196,132,4
Mvaa 192,128,4
Mvaa 192,32,0
W 4
Mvaa 200,32,8
Ta 24,25,49152
Ta 26,25,50176
Mvaa 200,128,12
Mvaa 204,132,12
Ta 28,25,57344
Ta 30,25,58368
Mvaa 204,36,8
Maaa 168,40,8
Maaa 168,136,12
Maaa 172,140,12
.Lh_0_2_1%=:
s_cbranch_execz .Lhr_9%=
Maaa 172,44,8
Maaa 232,48,8
Maaa 232,144,12
Maaa 236,148,12
s_waitcnt vmcnt(2) lgkmcnt(0)
s_barrier
s_add_u32 s64, s64, 128
Maaa 236,52,8
Maaa 104,56,8
Maaa 104,152,12
s_and_b32 s64, s64, 1023
Maaa 108,156,12
Maaa 108,60,8
Maaa 116,60,16
s_lshl_b32 s65, s64, 12
Maaa 116,156,20
Maaa 112,152,20
Ta 0,10,16384
Ta 2,10,17408
Maaa 112,56,16
s_add_u32 m0, s62, 32768
Ta 4,10,24576
Ta 6,10,25600
Maaa 244,52,16
Maaa 244,148,20
Maaa 240,144,20
D 16,44,65,0
Maaa 240,48,16
Maaa 180,44,16
Maaa 180,140,20
Maaa 176,136,20
D 17,44,65,1024
Maaa 176,40,16
Mvaa 212,36,16
Mvaa 212,132,20
Mvaa 208,128,20
D 18,44,65,2048
Mvaa 208,32,16
Mvaa 216,32,24
Ra 32,26,0
Mvaa 216,128,28
Ra 128,27,0
Mvaa 220,132,28
D 19,44,65,3072
Ra 132,27,2048
Mvaa 220,36,24
s_add_u32 m0, s62, 36864
Ta 8,11,16384
Ta 10,11,17408
Maaa 184,40,24
Ra 36,26,2048
Ta 12,11,24576
Maaa 184,136,28
Ta 14,11,25600
Ra 40,26,4096
Maaa 188,140,28
D 20,44,65,0
Ra 136,27,4096
Maaa 188,44,24
Ra 140,27,6144
Ra 44,26,6144
Maaa 248,48,24
Ra 48,26,8192
Maaa 248,144,28
Ra 144,27,8192
Maaa 252,148,28
D 21,44,65,1024
Ra 148,27,10240
Maaa 252,52,24
Ra 52,26,10240
Maaa 120,56,24
Ra 56,26,12288
Maaa 120,152,28
Ra 152,27,12288
Maaa 124,156,28
D 22,44,65,2048
Ra 156,27,14336
Maaa 124,60,24
W 13
Ra 60,26,14336
Mvaa 160,32,0
Mvaa 160,128,4
Mvaa 164,132,4
D 23,44,65,3072
Ta 16,12,16384
Mvaa 164,36,0
W 9
s_add_u32 m0, s61, 32768
Ta 18,12,17408
Mvaa 224,40,0
Ta 20,12,24576
Ta 22,12,25600
Mvaa 224,136,4
Mvaa 228,140,4
D 2,40,64,0
Mvaa 228,44,0
W 8
Maaa 192,48,0
Maaa 192,144,4
Maaa 196,148,4
D 3,40,64,1024
Maaa 196,52,0
W 4
Maaa 64,56,0
Maaa 64,152,4
Maaa 68,156,4
D 4,40,64,2048
Maaa 68,60,0
Maaa 76,60,8
Maaa 76,156,12
Maaa 72,152,12
D 5,40,64,3072
Ta 24,13,16384
Maaa 72,56,8
s_add_u32 m0, s61, 36864
Ta 26,13,17408
Ta 28,13,24576
Maaa 204,52,8
Ta 30,13,25600
Maaa 204,148,12
Maaa 200,144,12
D 6,40,64,0
Maaa 200,48,8
Mvaa 236,44,8
Mvaa 236,140,12
Mvaa 232,136,12
D 7,40,64,1024
Mvaa 232,40,8
Mvaa 172,36,8
Mvaa 172,132,12
Mvaa 168,128,12
D 8,40,64,2048
Mvaa 168,32,8
W 4
Mvaa 176,32,16
Mvaa 176,128,20
Mvaa 180,132,20
D 9,40,64,3072
Ta 0,14,16384
Mvaa 180,36,16
Ta 2,14,17408
Ta 4,14,24576
Mvaa 240,40,16
Ta 6,14,25600
Mvaa 240,136,20
Mvaa 244,140,20
Mvaa 244,44,16
Maaa 208,48,16
Maaa 208,144,20
Maaa 212,148,20
Maaa 212,52,16
Maaa 80,56,16
Maaa 80,152,20
Maaa 84,156,20
Maaa 84,60,16
W 4
Maaa 92,60,24
Maaa 92,156,28
Maaa 88,152,28
Ta 8,15,16384
Ta 10,15,17408
Maaa 88,56,24
Ta 12,15,24576
Ta 14,15,25600
Maaa 220,52,24
Maaa 220,148,28
Maaa 216,144,28
Maaa 216,48,24
Mvaa 252,44,24
Mvaa 252,140,28
Mvaa 248,136,28
Mvaa 248,40,24
Mvaa 188,36,24
Mvaa 188,132,28
Mvaa 184,128,28
Mvaa 184,32,24
W 4
Mvaa 192,32,0
Mvaa 192,128,4
Mvaa 196,132,4
Ta 16,24,16384
Ta 18,24,17408
Mvaa 196,36,0
Ta 20,24,24576
Ta 22,24,25600
Maaa 160,40,0
Maaa 160,136,4
Maaa 164,140,4
Maaa 164,44,0
Maaa 224,48,0
Maaa 224,144,4
Maaa 228,148,4
Maaa 228,52,0
Maaa 96,56,0
Maaa 96,152,4
Maaa 100,156,4
Maaa 100,60,0
W 4
Maaa 108,60,8
Ta 24,25,16384
Ta 26,25,17408
Maaa 108,156,12
Maaa 104,152,12
Ta 28,25,24576
Ta 30,25,25600
Maaa 104,56,8
Maaa 236,52,8
Maaa 236,148,12
Maaa 232,144,12
Maaa 232,48,8
Maaa 172,44,8
Maaa 172,140,12
Maaa 168,136,12
s_waitcnt vmcnt(0) lgkmcnt(0)
s_barrier
s_add_u32 s64, s64, 128
Maaa 168,40,8
Mvaa 204,36,8
Mvaa 204,132,12
s_and_b32 s64, s64, 1023
Mvaa 200,128,12
Mvaa 200,32,8
Mvaa 208,32,16
s_lshl_b32 s65, s64, 12
Mvaa 208,128,20
Mvaa 212,132,20
Ta 0,10,49152
Ta 2,10,50176
Mvaa 212,36,16
s_add_u32 m0, s62, 0
Ta 4,10,57344
Ta 6,10,58368
Maaa 176,40,16
Maaa 176,136,20
Maaa 180,140,20
D 16,44,65,0
Maaa 180,44,16
Maaa 240,48,16
Maaa 240,144,20
Maaa 244,148,20
D 17,44,65,1024
v_lshrrev_b32 v28, 1, v1
Maaa 244,52,16
s_lshl_b32 s80, s60, 9
Maaa 112,56,16
s_add_u32 s80, s80, 40960
Maaa 112,152,20
Maaa 116,156,20
D 18,44,65,2048
Maaa 116,60,16
Maaa 124,60,24
Ra 60,26,47104
Maaa 124,156,28
Ra 156,27,47104
Maaa 120,152,28
buffer_load_dwordx2 v[28:29], v28, s[8:11], s80 offen sc1
Ra 152,27,45056
Maaa 120,56,24
Ta 8,11,49152
Ta 10,11,50176
Maaa 252,52,24
Ra 56,26,45056
Ta 12,11,57344
Maaa 252,148,28
Ta 14,11,58368
Ra 52,26,43008
Maaa 248,144,28
D 19,44,65,3072
)"
R"(Ra 148,27,43008
Maaa 248,48,24
s_add_u32 m0, s62, 4096
Ra 144,27,40960
Ra 48,26,40960
Maaa 188,44,24
Ra 44,26,38912
Maaa 188,140,28
Ra 140,27,38912
Maaa 184,136,28
D 20,44,65,0
Ra 136,27,36864
Maaa 184,40,24
Ra 40,26,36864
Mvaa 220,36,24
Ra 36,26,34816
Mvaa 220,132,28
Ra 132,27,34816
Mvaa 216,128,28
D 21,44,65,1024
Ra 128,27,32768
Mvaa 216,32,24
W 13
Ra 32,26,32768
Maaa 68,60,0
Maaa 68,156,4
Maaa 64,152,4
D 22,44,65,2048
Ta 16,12,49152
Maaa 64,56,0
W 9
Ta 18,12,50176
Ta 20,12,57344
Maaa 196,52,0
Ta 22,12,58368
Maaa 196,148,4
Maaa 192,144,4
D 23,44,65,3072
Maaa 192,48,0
W 8
s_add_u32 m0, s61, 0
Mvaa 228,44,0
Mvaa 228,140,4
Mvaa 224,136,4
D 2,40,64,0
Mvaa 224,40,0
W 4
Mvaa 164,36,0
Mvaa 164,132,4
Mvaa 160,128,4
D 3,40,64,1024
Mvaa 160,32,0
Mvaa 168,32,8
Mvaa 168,128,12
Mvaa 172,132,12
D 4,40,64,2048
Ta 24,13,49152
Mvaa 172,36,8
Ta 26,13,50176
Ta 28,13,57344
Mvaa 232,40,8
Ta 30,13,58368
Mvaa 232,136,12
Mvaa 236,140,12
D 5,40,64,3072
Mvaa 236,44,8
s_add_u32 m0, s61, 4096
Maaa 200,48,8
Maaa 200,144,12
Maaa 204,148,12
D 6,40,64,0
Maaa 204,52,8
Maaa 72,56,8
Maaa 72,152,12
Maaa 76,156,12
D 7,40,64,1024
Maaa 76,60,8
W 4
Maaa 84,60,16
s_waitcnt vmcnt(11)
v_cmp_eq_u32 vcc, s13, v29
s_cmp_eq_u64 vcc, -1
s_cbranch_scc0 .Lspin_20%=
.Lok_21%=:
Maaa 84,156,20
Maaa 80,152,20
D 8,40,64,2048
Ta 0,14,49152
Maaa 80,56,16
Ta 2,14,50176
v_bfe_u32 v144, v1, 8, 1
Maaa 212,52,16
Ta 4,14,57344
v_mul_u32_u24 v144, 10240, v144
Maaa 212,148,20
Ta 6,14,58368
v_and_b32 v145, 0xf0, v1
Maaa 208,144,20
D 9,40,64,3072
v_lshl_add_u32 v144, v145, 3, v144
Maaa 208,48,16
s_mul_i32 s91, s58, 20480
s_add_u32 s91, s91, s28
Mvaa 244,44,16
Mvaa 244,140,20
Mvaa 240,136,20
global_load_dword v142, v146, s[6:7]
Mvaa 240,40,16
s_add_u32 s92, s92, 2097152
Mvaa 180,36,16
Mvaa 180,132,20
Mvaa 176,128,20
buffer_load_dword v142, v147, s[48:51], s92 offen
Mvaa 176,32,16
W 4
s_add_u32 s92, s92, 2097152
Mvaa 184,32,24
Mvaa 184,128,28
Mvaa 188,132,28
buffer_load_dword v142, v147, s[48:51], s92 offen
Ta 8,15,49152
Mvaa 188,36,24
Ta 10,15,50176
Ta 12,15,57344
Mvaa 248,40,24
Ta 14,15,58368
Mvaa 248,136,28
Mvaa 252,140,28
buffer_load_dword v142, v144, s[8:11], s91 offen offset:0 sc1
Mvaa 252,44,24
Maaa 216,48,24
Maaa 216,144,28
Maaa 220,148,28
Maaa 220,52,24
Maaa 88,56,24
Maaa 88,152,28
Maaa 92,156,28
Maaa 92,60,24
W 4
Maaa 100,60,0
Maaa 100,156,4
Maaa 96,152,4
Ta 16,24,49152
Ta 18,24,50176
Maaa 96,56,0
Ta 20,24,57344
Ta 22,24,58368
Maaa 228,52,0
Maaa 228,148,4
Maaa 224,144,4
Maaa 224,48,0
Maaa 164,44,0
Maaa 164,140,4
Maaa 160,136,4
Maaa 160,40,0
Mvaa 196,36,0
Mvaa 196,132,4
Mvaa 192,128,4
Mvaa 192,32,0
W 4
Mvaa 200,32,8
Ta 24,25,49152
Ta 26,25,50176
Mvaa 200,128,12
Mvaa 204,132,12
Ta 28,25,57344
Ta 30,25,58368
Mvaa 204,36,8
Maaa 168,40,8
Maaa 168,136,12
Maaa 172,140,12
Maaa 172,44,8
Maaa 232,48,8
Maaa 232,144,12
Maaa 236,148,12
s_waitcnt vmcnt(4) lgkmcnt(0)
s_barrier
s_cmp_eq_u32 s85, 107
s_cbranch_scc1 .Lrt_23%=
.Lrtr_24%=:
Maaa 236,52,8
s_add_u32 s64, s64, 128
Maaa 104,56,8
Maaa 104,152,12
s_and_b32 s64, s64, 1023
Maaa 108,156,12
Maaa 108,60,8
Maaa 116,60,16
s_lshl_b32 s65, s64, 12
Maaa 116,156,20
Maaa 112,152,20
Ta 0,10,16384
Ta 2,10,17408
Maaa 112,56,16
s_add_u32 m0, s62, 32768
Ta 4,10,24576
Ta 6,10,25600
Maaa 244,52,16
Maaa 244,148,20
Maaa 240,144,20
D 16,44,65,0
Maaa 240,48,16
Maaa 180,44,16
Maaa 180,140,20
Maaa 176,136,20
D 17,44,65,1024
Maaa 176,40,16
Mvaa 212,36,16
Mvaa 212,132,20
Mvaa 208,128,20
D 18,44,65,2048
Mvaa 208,32,16
Mvaa 216,32,24
Ra 32,26,0
Mvaa 216,128,28
Ra 128,27,0
Mvaa 220,132,28
D 19,44,65,3072
Ra 132,27,2048
Mvaa 220,36,24
s_add_u32 m0, s62, 36864
Ta 8,11,16384
Ta 10,11,17408
Maaa 184,40,24
Ra 36,26,2048
Ta 12,11,24576
Maaa 184,136,28
Ta 14,11,25600
Ra 40,26,4096
Maaa 188,140,28
D 20,44,65,0
Ra 136,27,4096
Maaa 188,44,24
Ra 140,27,6144
Ra 44,26,6144
Maaa 248,48,24
Ra 48,26,8192
Maaa 248,144,28
Ra 144,27,8192
Maaa 252,148,28
D 21,44,65,1024
Ra 148,27,10240
Maaa 252,52,24
Ra 52,26,10240
Maaa 120,56,24
Ra 56,26,12288
Maaa 120,152,28
Ra 152,27,12288
Maaa 124,156,28
D 22,44,65,2048
Ra 156,27,14336
Maaa 124,60,24
W 13
Ra 60,26,14336
Mvaa 160,32,0
Mvaa 160,128,4
Mvaa 164,132,4
D 23,44,65,3072
Ta 16,12,16384
Mvaa 164,36,0
W 9
s_add_u32 m0, s61, 32768
Ta 18,12,17408
Mvaa 224,40,0
.Lh_0_3_1%=:
s_cbranch_execz .Lhr_9%=
Ta 20,12,24576
Ta 22,12,25600
Mvaa 224,136,4
Mvaa 228,140,4
D 2,40,64,0
Mvaa 228,44,0
W 8
Maaa 192,48,0
Maaa 192,144,4
Maaa 196,148,4
D 3,40,64,1024
Maaa 196,52,0
W 4
Maaa 64,56,0
Maaa 64,152,4
Maaa 68,156,4
D 4,40,64,2048
Maaa 68,60,0
Maaa 76,60,8
Maaa 76,156,12
Maaa 72,152,12
D 5,40,64,3072
Ta 24,13,16384
Maaa 72,56,8
s_add_u32 m0, s61, 36864
Ta 26,13,17408
Ta 28,13,24576
Maaa 204,52,8
Ta 30,13,25600
Maaa 204,148,12
Maaa 200,144,12
D 6,40,64,0
Maaa 200,48,8
Mvaa 236,44,8
Mvaa 236,140,12
Mvaa 232,136,12
D 7,40,64,1024
Mvaa 232,40,8
Mvaa 172,36,8
Mvaa 172,132,12
Mvaa 168,128,12
D 8,40,64,2048
Mvaa 168,32,8
W 4
Mvaa 176,32,16
Mvaa 176,128,20
Mvaa 180,132,20
D 9,40,64,3072
v_add_u32 v2, 131072, v10
Mvaa 180,36,16
Ta 0,14,16384
v_add_u32 v3, 131072, v11
Mvaa 240,40,16
Ta 2,14,17408
v_add_u32 v4, 131072, v12
Mvaa 240,136,20
Ta 4,14,24576
Ta 6,14,25600
Mvaa 244,140,20
buffer_load_dword v142, v144, s[8:11], s91 offen offset:2048 sc1
v_add_u32 v5, 131072, v13
Mvaa 244,44,16
s_add_u32 s91, s91, 4096
v_add_u32 v6, 131072, v14
Maaa 208,48,16
v_add_u32 v7, 131072, v15
Maaa 208,144,20
Maaa 212,148,20
v_add_u32 v8, 131072, v24
Maaa 212,52,16
Maaa 80,56,16
v_add_u32 v9, 131072, v25
Maaa 80,152,20
s_add_u32 s65, s5, 0
s_cmp_ge_u32 s65, 20
s_cselect_b32 s90, 20, 0
s_sub_u32 s65, s65, s90
Maaa 84,156,20
s_lshl_b32 s65, s65, 18
s_add_u32 m0, s63, 131072
Maaa 84,60,16
W 4
Maaa 92,60,24
Maaa 92,156,28
Maaa 88,152,28
Ta 8,15,16384
Ta 10,15,17408
Maaa 88,56,24
D 16,48,65,0
Ta 12,15,24576
Maaa 220,52,24
Ta 14,15,25600
Maaa 220,148,28
Maaa 216,144,28
Maaa 216,48,24
D 17,48,65,1024
Mvaa 252,44,24
Mvaa 252,140,28
Mvaa 248,136,28
Mvaa 248,40,24
Mvaa 188,36,24
D 18,48,65,2048
Mvaa 188,132,28
Mvaa 184,128,28
Mvaa 184,32,24
W 4
Mvaa 192,32,0
Mvaa 192,128,4
D 19,48,65,3072
Mvaa 196,132,4
s_add_u32 m0, s63, 135168
Ta 16,24,16384
Ta 18,24,17408
Mvaa 196,36,0
Ta 20,24,24576
Ta 22,24,25600
Maaa 160,40,0
Maaa 160,136,4
Maaa 164,140,4
D 20,48,65,0
Maaa 164,44,0
Maaa 224,48,0
Maaa 224,144,4
Maaa 228,148,4
Maaa 228,52,0
D 21,48,65,1024
Maaa 96,56,0
Maaa 96,152,4
Maaa 100,156,4
Maaa 100,60,0
W 4
Maaa 108,60,8
D 22,48,65,2048
Ta 24,25,16384
)"
R"(Maaa 108,156,12
Ta 26,25,17408
Maaa 104,152,12
Ta 28,25,24576
Ta 30,25,25600
Maaa 104,56,8
Maaa 236,52,8
D 23,48,65,3072
Maaa 236,148,12
Maaa 232,144,12
Maaa 232,48,8
Maaa 172,44,8
Maaa 172,140,12
Maaa 168,136,12
s_waitcnt vmcnt(9) lgkmcnt(0)
s_barrier
s_cmp_eq_u32 s85, 107
s_cbranch_scc1 .Lrt_25%=
.Lrtr_26%=:
Maaa 168,40,8
s_add_u32 s65, s5, 4
Mvaa 204,36,8
Mvaa 204,132,12
s_cmp_ge_u32 s65, 20
s_cselect_b32 s90, 20, 0
Mvaa 200,128,12
Mvaa 200,32,8
s_sub_u32 s65, s65, s90
Mvaa 208,32,16
Mvaa 208,128,20
s_lshl_b32 s65, s65, 18
Mvaa 212,132,20
Ta 0,10,49152
Ta 2,10,50176
Mvaa 212,36,16
Maaa 176,40,16
s_add_u32 m0, s63, 0
Maaa 176,136,20
Maaa 180,140,20
D 16,48,65,0
Maaa 180,44,16
Maaa 240,48,16
Maaa 240,144,20
Maaa 244,148,20
D 17,48,65,1024
Ta 4,11,49152
Maaa 244,52,16
Ta 6,11,50176
Maaa 112,56,16
Maaa 112,152,20
Maaa 116,156,20
D 18,48,65,2048
Maaa 116,60,16
Maaa 124,60,24
Ra 60,26,47104
Maaa 124,156,28
Maaa 120,152,28
D 19,48,65,3072
Ta 8,12,49152
Maaa 120,56,24
s_add_u32 m0, s63, 4096
Ta 10,12,50176
Ra 56,26,45056
Maaa 252,52,24
Ra 52,26,43008
Maaa 252,148,28
Maaa 248,144,28
D 20,48,65,0
Maaa 248,48,24
Ra 48,26,40960
Maaa 188,44,24
Ra 44,26,38912
Maaa 188,140,28
Maaa 184,136,28
D 21,48,65,1024
Ta 12,13,49152
Maaa 184,40,24
Ta 14,13,50176
Ra 40,26,36864
Mvaa 220,36,24
Ra 36,26,34816
Mvaa 220,132,28
Mvaa 216,128,28
D 22,48,65,2048
Mvaa 216,32,24
Ra 32,26,32768
W 6
Maaa 68,60,0
Maaa 64,56,0
Maaa 196,52,0
D 23,48,65,3072
Ta 16,14,49152
Maaa 192,48,0
W 1
s_add_u32 s65, s5, 8
s_cmp_ge_u32 s65, 20
s_cselect_b32 s90, 20, 0
Mvaa 228,44,0
s_sub_u32 s65, s65, s90
s_lshl_b32 s65, s65, 18
Ta 18,14,50176
Mvaa 224,40,0
s_add_u32 m0, s63, 65536
Mvaa 164,36,0
D 16,48,65,0
Mvaa 160,32,0
Mvaa 168,32,4
Mvaa 172,36,4
Mvaa 232,40,4
D 17,48,65,1024
Ta 20,15,49152
Mvaa 236,44,4
Ta 22,15,50176
Maaa 200,48,4
Maaa 204,52,4
Maaa 72,56,4
D 18,48,65,2048
Maaa 76,60,4
Maaa 84,60,8
Maaa 80,56,8
Maaa 212,52,8
D 19,48,65,3072
Ta 24,24,49152
Maaa 208,48,8
s_add_u32 m0, s63, 69632
Ta 26,24,50176
Mvaa 244,44,8
Mvaa 240,40,8
Mvaa 180,36,8
D 20,48,65,0
Mvaa 176,32,8
Mvaa 184,32,12
Mvaa 188,36,12
Mvaa 248,40,12
D 21,48,65,1024
Ta 28,25,49152
Mvaa 252,44,12
Ta 30,25,50176
Maaa 216,48,12
Ra 156,27,47104
Maaa 220,52,12
Maaa 88,56,12
D 22,48,65,2048
Maaa 92,60,12
W 7
Ra 152,27,45056
Maaa 100,60,16
Maaa 96,56,16
Maaa 228,52,16
D 23,48,65,3072
Ra 148,27,43008
Maaa 224,48,16
Ta 0,10,57344
Ta 2,10,58368
Maaa 164,44,16
Maaa 160,40,16
Ra 144,27,40960
Mvaa 196,36,16
buffer_load_dword v142, v144, s[8:11], s91 offen offset:0 sc1
Mvaa 192,32,16
W 10
Mvaa 200,32,20
Ra 140,27,38912
Mvaa 204,36,20
Maaa 168,40,20
Ta 4,11,57344
Ta 6,11,58368
Maaa 172,44,20
Ra 136,27,36864
Maaa 232,48,20
Maaa 236,52,20
v_bfe_u32 v29, v1, 7, 1
Maaa 104,56,20
Ra 132,27,34816
Maaa 108,60,20
W 13
v_mul_u32_u24 v28, 10240, v29
Maaa 116,60,24
Maaa 112,56,24
Ra 128,27,32768
v_lshrrev_b32 v29, 8, v1
Maaa 244,52,24
Ta 8,12,57344
Ta 10,12,58368
Maaa 240,48,24
v_lshl_add_u32 v28, v29, 7, v28
Maaa 180,44,24
v_and_b32 v29, 0x70, v1
Maaa 176,40,24
Mvaa 212,36,24
v_add_u32 v28, v28, v29
Mvaa 208,32,24
W 14
Mvaa 216,32,28
s_and_b32 s80, s14, 2
Mvaa 220,36,28
s_mul_i32 s80, s80, 20480
Maaa 184,40,28
Ta 12,13,57344
Ta 14,13,58368
Maaa 188,44,28
s_sub_u32 s81, 40960, s80
Maaa 248,48,28
Maaa 252,52,28
s_add_u32 s93, s28, 20480
Maaa 120,56,28
Maaa 124,60,28
W 10
v_add_u32 v26, s80, v28
Maaa 68,156,0
Maaa 64,152,0
Maaa 196,148,0
Ta 16,14,57344
v_add_u32 v27, s81, v28
Maaa 192,144,0
W 5
Ta 18,14,58368
Mvaa 228,140,0
buffer_load_dwordx4 a[32:35], v26, s[8:11], s28 offen offset:0 sc1
Mvaa 224,136,0
Mvaa 164,132,0
Mvaa 160,128,0
Mvaa 168,128,4
buffer_load_dwordx4 a[36:39], v26, s[8:11], s93 offen offset:0 sc1
Mvaa 172,132,4
Mvaa 232,136,4
Ta 20,15,57344
Ta 22,15,58368
Mvaa 236,140,4
Maaa 200,144,4
buffer_load_dwordx4 a[40:43], v27, s[8:11], s28 offen offset:0 sc1
Maaa 204,148,4
Maaa 72,152,4
Maaa 76,156,4
W 6
Maaa 84,156,8
buffer_load_dwordx4 a[44:47], v27, s[8:11], s93 offen offset:0 sc1
Ta 24,24,57344
Maaa 80,152,8
s_add_u32 s28, s28, 512
s_cmp_eq_u32 s28, s4
s_cselect_b32 s93, 10240, 0
Ta 26,24,58368
Maaa 212,148,8
s_sub_u32 s28, s28, s93
Ta 28,25,57344
Ta 30,25,58368
Maaa 208,144,8
s_add_u32 s93, s28, 20480
Mvaa 244,140,8
Mvaa 240,136,8
Mvaa 180,132,8
s_add_u32 s65, s5, 12
s_cmp_ge_u32 s65, 20
s_cselect_b32 s90, 20, 0
buffer_load_dwordx4 a[48:51], v26, s[8:11], s28 offen offset:0 sc1
Mvaa 176,128,8
W 8
s_sub_u32 s65, s65, s90
s_lshl_b32 s65, s65, 18
Mvaa 184,128,12
s_add_u32 m0, s63, 32768
Mvaa 188,132,12
Mvaa 248,136,12
s_waitcnt vmcnt(6) lgkmcnt(0)
s_barrier
buffer_load_dwordx4 a[52:55], v26, s[8:11], s93 offen offset:0 sc1
Mvaa 252,140,12
Maaa 216,144,12
Maaa 220,148,12
Maaa 88,152,12
buffer_load_dwordx4 a[56:59], v27, s[8:11], s28 offen offset:0 sc1
Maaa 92,156,12
Maaa 100,156,16
Maaa 96,152,16
Maaa 228,148,16
buffer_load_dwordx4 a[60:63], v27, s[8:11], s93 offen offset:0 sc1
Maaa 224,144,16
Maaa 164,140,16
Maaa 160,136,16
Mvaa 196,132,16
D 16,48,65,0
Mvaa 192,128,16
Mvaa 200,128,20
Mvaa 204,132,20
Maaa 168,136,20
D 17,48,65,1024
Ta 0,2,0
Maaa 172,140,20
Ta 2,2,1024
Maaa 232,144,20
Maaa 236,148,20
Maaa 104,152,20
D 18,48,65,2048
Ta 4,2,8192
Maaa 108,156,20
Ta 6,2,9216
Maaa 116,156,24
Maaa 112,152,24
Maaa 244,148,24
Ta 8,3,0
Ta 10,3,1024
Maaa 240,144,24
D 19,48,65,3072
Maaa 180,140,24
s_add_u32 m0, s63, 36864
Maaa 176,136,24
Mvaa 212,132,24
Ta 12,3,8192
Ta 14,3,9216
Mvaa 208,128,24
D 20,48,65,0
Mvaa 216,128,28
Mvaa 220,132,28
Maaa 184,136,28
Ta 16,4,0
Ta 18,4,1024
Maaa 188,140,28
Maaa 248,144,28
D 21,48,65,1024
Maaa 252,148,28
Maaa 120,152,28
Maaa 124,156,28
.Lq_11%=:
s_cmp_ge_u32 s14, 2
s_cbranch_scc1 .Lpb_31%=
s_waitcnt vmcnt(6) lgkmcnt(6)
Zvaa 32,0,32
s_add_u32 s28, s28, 512
Ta 20,4,8192
Ta 22,4,9216
Mvaa 32,4,48
s_cmp_eq_u32 s28, s4
s_cselect_b32 s93, 10240, 0
Ta 24,5,0
Ta 26,5,1024
Zvaa 64,4,52
s_sub_u32 s28, s28, s93
s_add_u32 s93, s28, 20480
buffer_load_dwordx4 a[128:131], v26, s[8:11], s28 offen offset:0 sc1
Mvaa 64,0,36
Ta 28,5,8192
Ta 30,5,9216
Zvaa 96,0,40
K 184,184,185
K 185,186,187
Mvaa 96,4,56
K 186,188,189
K 187,190,191
Zvaa 128,4,60
K 192,192,193
Mvaa 128,0,44
W 8
buffer_load_dwordx4 a[132:135], v26, s[8:11], s93 offen offset:0 sc1
K 193,194,195
Zvaa 132,8,44
Ta 0,6,0
Ta 2,6,1024
Mvaa 132,12,60
K 194,196,197
K 195,198,199
Zvaa 100,12,56
K 200,200,201
K 201,202,203
Mvaa 100,8,40
Ta 4,6,8192
Ta 6,6,9216
Zvaa 68,8,36
buffer_load_dwordx4 a[136:139], v27, s[8:11], s28 offen offset:0 sc1
K 202,204,205
Mvaa 68,12,52
K 203,206,207
K 208,208,209
Zvaa 36,12,48
K 209,210,211
K 210,212,213
Mvaa 36,8,32
W 8
Ta 8,7,0
K 211,214,215
Zvaa 40,16,32
Ta 10,7,1024
K 216,216,217
Mvaa 40,20,48
buffer_load_dwordx4 a[140:143], v27, s[8:11], s93 offen offset:0 sc1
K 217,218,219
Zvaa 72,20,52
s_add_u32 s28, s28, 512
s_cmp_eq_u32 s28, s4
s_cselect_b32 s93, 10240, 0
s_sub_u32 s28, s28, s93
Mvaa 72,16,36
s_add_u32 s93, s28, 20480
K 218,220,221
K 219,222,223
Zvaa 104,16,40
Ta 12,7,8192
Ta 14,7,9216
Mvaa 104,20,56
K 224,224,225
)"
R"(K 225,226,227
Zvaa 136,20,60
K 226,228,229
K 227,230,231
Mvaa 136,16,44
W 8
buffer_load_dwordx4 a[144:147], v26, s[8:11], s28 offen offset:0 sc1
K 232,232,233
Zvaa 140,24,44
K 233,234,235
K 234,236,237
Mvaa 140,28,60
Ta 16,8,0
K 235,238,239
Zvaa 108,28,56
Ta 18,8,1024
K 240,240,241
Mvaa 108,24,40
K 241,242,243
K 242,244,245
Zvaa 76,24,36
buffer_load_dwordx4 a[148:151], v26, s[8:11], s93 offen offset:0 sc1
K 243,246,247
Mvaa 76,28,52
Ta 20,8,8192
Ta 22,8,9216
Zvaa 44,28,48
K 248,248,249
K 249,250,251
Mvaa 44,24,32
K 250,252,253
K 251,254,255
W 8
Zvaa 48,0,32
Ta 24,9,0
Ta 26,9,1024
Mvaa 48,4,48
buffer_load_dwordx4 a[152:155], v27, s[8:11], s28 offen offset:0 sc1
K 160,160,161
Zvaa 80,4,52
Ta 28,9,8192
Ta 30,9,9216
Mvaa 80,0,36
K 161,162,163
Zvaa 112,0,40
K 162,164,165
Mvaa 112,4,56
Zvaa 144,4,60
buffer_load_dwordx4 a[156:159], v27, s[8:11], s93 offen offset:0 sc1
K 163,166,167
Mvaa 144,0,44
W 8
s_add_u32 s28, s28, 512
s_cmp_eq_u32 s28, s4
s_cselect_b32 s93, 10240, 0
Zvaa 148,8,44
s_sub_u32 s28, s28, s93
s_add_u32 s93, s28, 20480
Ta 0,2,16384
Mvaa 148,12,60
s_add_u32 s36, s29, 0
Ta 2,2,17408
Zvaa 116,12,56
Ta 4,2,24576
Ta 6,2,25600
Mvaa 116,8,40
buffer_load_dwordx4 v[188:191], v26, s[8:11], s28 offen offset:0 sc1
Zvaa 84,8,36
Mvaa 84,12,52
Zvaa 52,12,48
Mvaa 52,8,32
W 8
Ta 8,3,16384
Ta 10,3,17408
Zvaa 56,16,32
buffer_load_dwordx4 v[196:199], v26, s[8:11], s93 offen offset:0 sc1
Mvaa 56,20,48
Zvaa 88,20,52
Ta 12,3,24576
Ta 14,3,25600
Mvaa 88,16,36
Zvaa 120,16,40
buffer_store_dwordx4 v[160:163], v1, s[8:11], s36 offen offset:0 sc1
K 168,168,169
Mvaa 120,20,56
K 169,170,171
K 170,172,173
Zvaa 152,20,60
Mvaa 152,16,44
W 8
Ta 16,4,16384
K 171,174,175
Zvaa 156,24,44
Ta 18,4,17408
Mvaa 156,28,60
buffer_load_dwordx4 v[204:207], v27, s[8:11], s28 offen offset:0 sc1
Zvaa 124,28,56
Ta 20,4,24576
Ta 22,4,25600
Mvaa 124,24,40
Zvaa 92,24,36
Mvaa 92,28,52
Zvaa 60,28,48
buffer_load_dwordx4 v[212:215], v27, s[8:11], s93 offen offset:0 sc1
Mvaa 60,24,32
s_waitcnt vmcnt(5) lgkmcnt(8)
s_add_u32 s28, s28, 512
s_cmp_eq_u32 s28, s4
s_cselect_b32 s93, 10240, 0
Mvaa 32,0,128
s_sub_u32 s28, s28, s93
s_add_u32 s93, s28, 20480
Ta 24,5,16384
Mvaa 32,4,144
Ta 26,5,17408
Mvaa 64,4,148
Ta 28,5,24576
Ta 30,5,25600
Mvaa 64,0,132
buffer_store_dwordx4 v[168:171], v1, s[8:11], s36 offen offset:1024 sc1
K 176,176,177
Mvaa 96,0,136
K 177,178,179
K 178,180,181
Mvaa 96,4,152
Mvaa 128,4,156
Mvaa 128,0,140
W 8
Ta 0,6,16384
K 179,182,183
Mvaa 132,8,140
buffer_load_dwordx4 v[220:223], v26, s[8:11], s28 offen offset:0 sc1
Ta 2,6,17408
Mvaa 132,12,156
Mvaa 100,12,152
Ta 4,6,24576
Ta 6,6,25600
Mvaa 100,8,136
Mvaa 68,8,132
Mvaa 68,12,148
buffer_load_dwordx4 v[228:231], v26, s[8:11], s93 offen offset:0 sc1
Mvaa 36,12,144
Mvaa 36,8,128
W 8
Ta 8,7,16384
Ta 10,7,17408
Mvaa 40,16,128
Mvaa 40,20,144
Mvaa 72,20,148
buffer_load_dwordx4 v[236:239], v27, s[8:11], s28 offen offset:0 sc1
Ta 12,7,24576
Mvaa 72,16,132
buffer_store_dwordx4 v[176:179], v1, s[8:11], s36 offen offset:2048 sc1
Ta 14,7,25600
Mvaa 104,16,136
Mvaa 104,20,152
Mvaa 136,20,156
Mvaa 136,16,140
W 8
Ta 16,8,16384
Ta 18,8,17408
Mvaa 140,24,140
buffer_load_dwordx4 v[244:247], v27, s[8:11], s93 offen offset:0 sc1
Mvaa 140,28,156
s_add_u32 s28, s28, 512
s_cmp_eq_u32 s28, s4
s_cselect_b32 s93, 10240, 0
s_sub_u32 s28, s28, s93
Mvaa 108,28,152
s_add_u32 s93, s28, 20480
Ta 20,8,24576
Ta 22,8,25600
Mvaa 108,24,136
Mvaa 76,24,132
Mvaa 76,28,148
buffer_load_dwordx4 v[252:255], v26, s[8:11], s28 offen offset:0 sc1
Mvaa 44,28,144
Mvaa 44,24,128
W 8
Ta 24,9,16384
Ta 26,9,17408
Mvaa 48,0,128
Mvaa 48,4,144
Mvaa 80,4,148
D 22,48,65,2048
Ta 28,9,24576
Mvaa 80,0,132
buffer_store_dwordx4 v[184:187], v1, s[8:11], s36 offen offset:3072 sc1
Ta 30,9,25600
Mvaa 112,0,136
s_add_u32 s36, s29, 4096
s_lshl_b32 s80, s56, 9
Mvaa 112,4,152
Mvaa 144,4,156
s_lshl_b32 s81, s57, 4
Mvaa 144,0,140
W 8
s_add_u32 s80, s80, s81
Ta 0,10,0
Mvaa 148,8,140
buffer_load_dwordx4 a[32:35], v26, s[8:11], s93 offen offset:0 sc1
Ta 2,10,1024
Mvaa 148,12,156
s_and_b32 s81, s14, 1
Mvaa 116,12,152
s_lshl_b32 s81, s81, 3
Ta 4,10,8192
Ta 6,10,9216
Mvaa 116,8,136
Mvaa 84,8,132
s_add_u32 s80, s80, s81
Mvaa 84,12,148
s_add_u32 s71, s80, 4096
buffer_load_dwordx4 a[36:39], v27, s[8:11], s28 offen offset:0 sc1
Mvaa 52,12,144
Mvaa 52,8,128
W 8
Ta 8,11,0
Ta 10,11,1024
Mvaa 56,16,128
Mvaa 56,20,144
Mvaa 88,20,148
buffer_store_dwordx4 v[192:195], v1, s[8:11], s36 offen offset:0 sc1
Ta 12,11,8192
Mvaa 88,16,132
Ta 14,11,9216
Mvaa 120,16,136
Mvaa 120,20,152
Mvaa 152,20,156
Mvaa 152,16,140
W 8
buffer_load_dwordx4 a[40:43], v27, s[8:11], s93 offen offset:0 sc1
Ta 16,12,0
Mvaa 156,24,140
s_add_u32 s28, s28, 512
s_cmp_eq_u32 s28, s4
s_cselect_b32 s93, 10240, 0
s_sub_u32 s28, s28, s93
Mvaa 156,28,156
s_add_u32 s93, s28, 20480
Ta 18,12,1024
Mvaa 124,28,152
Ta 20,12,8192
Ta 22,12,9216
Mvaa 124,24,136
Mvaa 92,24,132
buffer_load_dwordx4 a[44:47], v26, s[8:11], s28 offen offset:0 sc1
Mvaa 92,28,148
Mvaa 60,28,144
Mvaa 60,24,128
s_waitcnt vmcnt(8) lgkmcnt(8)
Ta 24,13,0
Ta 26,13,1024
Mvav 32,0,188
Mvav 32,4,220
D 23,48,65,3072
Mvav 64,4,228
s_add_u32 s65, s5, 16
s_cmp_ge_u32 s65, 20
s_cselect_b32 s90, 20, 0
buffer_store_dwordx4 v[200:203], v1, s[8:11], s36 offen offset:1024 sc1
Mvav 64,0,196
s_sub_u32 s65, s65, s90
s_lshl_b32 s65, s65, 18
s_add_u32 m0, s63, 98304
Mvav 96,0,204
Ta 28,13,8192
Ta 30,13,9216
Mvav 96,4,236
Mvav 128,4,244
Mvav 128,0,212
W 8
buffer_load_dwordx4 a[48:51], v26, s[8:11], s93 offen offset:0 sc1
Ta 0,14,0
Mvav 132,8,212
Ta 2,14,1024
v_add_u32 v2, 65536, v10
Mvav 132,12,244
Mvav 100,12,236
Ta 4,14,8192
Ta 6,14,9216
Mvav 100,8,204
v_add_u32 v3, 65536, v11
Mvav 68,8,196
buffer_load_dwordx4 a[52:55], v27, s[8:11], s28 offen offset:0 sc1
Mvav 68,12,228
Mvav 36,12,220
Mvav 36,8,188
W 8
Ta 8,15,0
Ta 10,15,1024
Mvav 40,16,188
v_add_u32 v4, 65536, v12
Mvav 40,20,220
buffer_load_dwordx4 a[56:59], v27, s[8:11], s93 offen offset:0 sc1
Mvav 72,20,228
s_add_u32 s28, s28, 512
s_cmp_eq_u32 s28, s4
s_cselect_b32 s93, 10240, 0
buffer_store_dwordx4 v[208:211], v1, s[8:11], s36 offen offset:2048 sc1
Mvav 72,16,196
s_sub_u32 s28, s28, s93
s_add_u32 s93, s28, 20480
Ta 12,15,8192
Mvav 104,16,204
Ta 14,15,9216
v_add_u32 v5, 65536, v13
Mvav 104,20,236
Mvav 136,20,244
Mvav 136,16,212
W 8
D 16,48,65,0
Ta 16,24,0
Mvav 140,24,212
Ta 18,24,1024
v_add_u32 v6, 65536, v14
Mvav 140,28,244
Mvav 108,28,236
Ta 20,24,8192
Ta 22,24,9216
Mvav 108,24,204
v_add_u32 v7, 65536, v15
Mvav 76,24,196
buffer_load_dwordx4 a[60:63], v26, s[8:11], s28 offen offset:0 sc1
Mvav 76,28,228
Mvav 44,28,220
Mvav 44,24,188
W 8
Ta 24,25,0
Ta 26,25,1024
Mvav 48,0,188
v_add_u32 v8, 65536, v24
Mvav 48,4,220
buffer_load_dwordx4 v[184:187], v26, s[8:11], s93 offen offset:0 sc1
Mvav 80,4,228
buffer_store_dwordx4 v[216:219], v1, s[8:11], s36 offen offset:3072 sc1
Ta 28,25,8192
Mvav 80,0,196
s_add_u32 s36, s29, 458752
Ta 30,25,9216
v_add_u32 v9, 65536, v25
Mvav 112,0,204
Mvav 112,4,236
Mvav 144,4,244
Mvav 144,0,212
W 8
buffer_load_dwordx4 v[192:195], v27, s[8:11], s28 offen offset:0 sc1
Ta 0,10,16384
Mvav 148,8,212
Ta 2,10,17408
Mvav 148,12,244
Mvav 116,12,236
Ta 4,10,24576
Ta 6,10,25600
Mvav 116,8,204
Mvav 84,8,196
D 17,48,65,1024
Mvav 84,12,228
Mvav 52,12,220
Mvav 52,8,188
W 8
Ta 8,11,16384
Ta 10,11,17408
Mvav 56,16,188
Mvav 56,20,220
buffer_store_dwordx4 v[224:227], v1, s[8:11], s36 offen offset:0 sc1
Mvav 88,20,228
Ta 12,11,24576
Ta 14,11,25600
Mvav 88,16,196
Mvav 120,16,204
Mvav 120,20,236
Mvav 152,20,244
buffer_load_dwordx4 a[128:131], v27, s[8:11], s93 offen offset:0 sc1
Mvav 152,16,212
W 8
s_add_u32 s28, s28, 512
s_cmp_eq_u32 s28, s4
s_cselect_b32 s93, 10240, 0
Mvav 156,24,212
s_sub_u32 s28, s28, s93
s_add_u32 s93, s28, 20480
Ta 16,12,16384
Mvav 156,28,244
Ta 18,12,17408
Mvav 124,28,236
Ta 20,12,24576
Ta 22,12,25600
Mvav 124,24,204
buffer_load_dwordx4 a[132:135], v26, s[8:11], s28 offen offset:0 sc1
Mvav 92,24,196
Mvav 92,28,228
Mvav 60,28,220
Mvav 60,24,188
s_waitcnt vmcnt(10) lgkmcnt(8)
Ta 24,13,16384
Ta 26,13,17408
Mvav 32,0,252
buffer_load_dwordx4 a[136:139], v26, s[8:11], s93 offen offset:0 sc1
Mvaa 32,4,44
)"
R"(buffer_store_dwordx4 v[232:235], v1, s[8:11], s36 offen offset:1024 sc1
Mvaa 64,4,48
Ta 28,13,24576
Ta 30,13,25600
Mvaa 64,0,32
Mvaa 96,0,36
Mvaa 96,4,52
Mvaa 128,4,56
buffer_load_dwordx4 a[140:143], v27, s[8:11], s28 offen offset:0 sc1
Mvaa 128,0,40
W 8
Ta 0,14,16384
Ta 2,14,17408
Mvaa 132,8,40
Mvaa 132,12,56
Mvaa 100,12,52
Ta 4,14,24576
Ta 6,14,25600
Mvaa 100,8,36
D 18,48,65,2048
Mvaa 68,8,32
Mvaa 68,12,48
Mvaa 36,12,44
Mvav 36,8,252
W 8
Ta 8,15,16384
Ta 10,15,17408
Mvav 40,16,252
buffer_load_dwordx4 a[144:147], v27, s[8:11], s93 offen offset:0 sc1
Mvaa 40,20,44
s_add_u32 s28, s28, 512
s_cmp_eq_u32 s28, s4
s_cselect_b32 s93, 10240, 0
buffer_store_dwordx4 v[240:243], v1, s[8:11], s36 offen offset:2048 sc1
Mvaa 72,20,48
s_sub_u32 s28, s28, s93
s_add_u32 s93, s28, 20480
Ta 12,15,24576
Mvaa 72,16,32
Ta 14,15,25600
Mvaa 104,16,36
Mvaa 104,20,52
Mvaa 136,20,56
buffer_load_dwordx4 a[148:151], v26, s[8:11], s28 offen offset:0 sc1
Mvaa 136,16,40
W 8
Ta 16,24,16384
Ta 18,24,17408
Mvaa 140,24,40
Mvaa 140,28,56
Mvaa 108,28,52
Ta 20,24,24576
Ta 22,24,25600
Mvaa 108,24,36
buffer_load_dwordx4 a[152:155], v26, s[8:11], s93 offen offset:0 sc1
Mvaa 76,24,32
Mvaa 76,28,48
Mvaa 44,28,44
Mvav 44,24,252
W 8
Ta 24,25,16384
Ta 26,25,17408
Mvav 48,0,252
buffer_load_dwordx4 a[156:159], v27, s[8:11], s28 offen offset:0 sc1
Mvaa 48,4,44
buffer_store_dwordx4 v[248:251], v1, s[8:11], s36 offen offset:3072 sc1
G 160,160
Mvaa 80,4,48
G 161,161
G 162,162
Mvaa 80,0,32
G 163,163
G 164,164
Mvaa 112,0,36
G 165,165
G 166,166
Mvaa 112,4,52
Ta 28,25,24576
Ta 30,25,25600
Mvaa 144,4,56
D 19,48,65,3072
G 167,167
Mvaa 144,0,40
W 8
s_add_u32 m0, s63, 102400
K 160,160,161
Mvaa 148,8,40
K 161,162,163
K 162,164,165
Mvaa 148,12,56
Ta 0,2,0
K 163,166,167
Mvaa 116,12,52
s_add_u32 s36, s29, 462848
Ta 2,2,1024
Ta 4,2,8192
Mvaa 116,8,36
buffer_load_dwordx4 v[200:203], v27, s[8:11], s93 offen offset:0 sc1
Ta 6,2,9216
Mvaa 84,8,32
s_add_u32 s28, s28, 512
s_cmp_eq_u32 s28, s4
s_cselect_b32 s93, 10240, 0
s_sub_u32 s28, s28, s93
Mvaa 84,12,48
s_add_u32 s93, s28, 20480
Mvaa 52,12,44
Mvav 52,8,252
W 8
Ta 8,3,0
Ta 10,3,1024
Mvav 56,16,252
buffer_store_dwordx4 v[160:163], v1, s[8:11], s36 offen offset:0 sc1
G 168,168
Mvaa 56,20,44
G 169,169
G 170,170
Mvaa 88,20,48
G 171,171
G 172,172
Mvaa 88,16,32
G 173,173
G 174,174
Mvaa 120,16,36
Ta 12,3,8192
Ta 14,3,9216
Mvaa 120,20,52
buffer_load_dwordx4 v[208:211], v26, s[8:11], s28 offen offset:0 sc1
G 175,175
Mvaa 152,20,56
K 168,168,169
K 169,170,171
Mvaa 152,16,40
W 8
K 170,172,173
K 171,174,175
Mvaa 156,24,40
Ta 16,4,0
Ta 18,4,1024
Mvaa 156,28,56
Mvaa 124,28,52
buffer_load_dwordx4 v[216:219], v26, s[8:11], s93 offen offset:0 sc1
Ta 20,4,8192
Mvaa 124,24,36
Ta 22,4,9216
Mvaa 92,24,32
Mvaa 92,28,48
Mvaa 60,28,44
Mvav 60,24,252
s_waitcnt vmcnt(10) lgkmcnt(8)
D 20,48,65,0
Ta 24,5,0
Mvaa 32,0,60
buffer_store_dwordx4 v[168:171], v1, s[8:11], s36 offen offset:1024 sc1
G 176,176
Mvaa 32,4,132
G 177,177
G 178,178
Mvaa 64,4,136
Ta 26,5,1024
G 179,179
Mvav 64,0,184
G 180,180
G 181,181
Mvav 96,0,192
Ta 28,5,8192
G 182,182
Mvaa 96,4,140
buffer_load_dwordx4 v[224:227], v27, s[8:11], s28 offen offset:0 sc1
Ta 30,5,9216
Mvaa 128,4,144
G 183,183
K 176,176,177
Mvaa 128,0,128
W 8
K 177,178,179
K 178,180,181
Mvaa 132,8,128
Ta 0,6,0
K 179,182,183
Mvaa 132,12,144
Ta 2,6,1024
Mvaa 100,12,140
buffer_load_dwordx4 v[188:191], v27, s[8:11], s93 offen offset:0 sc1
Ta 4,6,8192
Mvav 100,8,192
s_add_u32 s28, s28, 512
s_cmp_eq_u32 s28, s4
s_cselect_b32 s93, 10240, 0
s_sub_u32 s28, s28, s93
Mvav 68,8,184
s_add_u32 s93, s28, 20480
Ta 6,6,9216
Mvaa 68,12,136
Mvaa 36,12,132
Mvaa 36,8,60
W 8
buffer_load_dwordx4 v[196:199], v26, s[8:11], s28 offen offset:0 sc1
Ta 8,7,0
Mvaa 40,16,60
buffer_store_dwordx4 v[176:179], v1, s[8:11], s36 offen offset:2048 sc1
G 160,184
Mvaa 40,20,132
G 161,185
G 162,186
Mvaa 72,20,136
Ta 10,7,1024
G 163,187
Mvav 72,16,184
G 164,188
G 165,189
Mvav 104,16,192
Ta 12,7,8192
G 166,190
Mvaa 104,20,140
buffer_load_dwordx4 v[204:207], v26, s[8:11], s93 offen offset:0 sc1
Ta 14,7,9216
Mvaa 136,20,144
G 167,191
K 160,160,161
Mvaa 136,16,128
W 8
K 161,162,163
K 162,164,165
Mvaa 140,24,128
Ta 16,8,0
K 163,166,167
Mvaa 140,28,144
Ta 18,8,1024
Mvaa 108,28,140
D 21,48,65,1024
Ta 20,8,8192
Mvav 108,24,192
Ta 22,8,9216
Mvav 76,24,184
Mvaa 76,28,136
Mvaa 44,28,132
Mvaa 44,24,60
W 8
buffer_load_dwordx4 v[212:215], v27, s[8:11], s28 offen offset:0 sc1
Ta 24,9,0
Mvaa 48,0,60
buffer_store_dwordx4 v[160:163], v1, s[8:11], s36 offen offset:3072 sc1
G 168,192
Mvaa 48,4,132
G 169,193
G 170,194
Mvaa 80,4,136
G 171,195
G 172,196
Mvav 80,0,184
Ta 26,9,1024
G 173,197
Mvav 112,0,192
G 174,198
G 175,199
Mvaa 112,4,140
buffer_load_dwordx4 v[220:223], v27, s[8:11], s93 offen offset:0 sc1
Ta 28,9,8192
Mvaa 144,4,144
s_add_u32 s28, s28, 512
s_cmp_eq_u32 s28, s4
s_cselect_b32 s93, 10240, 0
s_sub_u32 s28, s28, s93
Mvaa 144,0,128
W 7
s_add_u32 s93, s28, 20480
Ta 30,9,9216
Mvaa 148,8,128
K 168,168,169
K 169,170,171
Mvaa 148,12,144
K 170,172,173
K 171,174,175
Mvaa 116,12,140
buffer_load_dwordx4 v[228:231], v26, s[8:11], s28 offen offset:0 sc1
Ta 0,2,16384
Mvav 116,8,192
Ta 2,2,17408
v_accvgpr_write_b32 a192, v168
Mvav 84,8,184
v_accvgpr_write_b32 a193, v169
v_accvgpr_write_b32 a194, v170
Mvaa 84,12,136
Ta 4,2,24576
Ta 6,2,25600
Mvaa 52,12,132
v_accvgpr_write_b32 a195, v171
G 176,64
Mvaa 52,8,60
W 8
D 22,48,65,2048
G 177,65
Mvaa 56,16,60
D 23,48,65,3072
G 178,66
Mvaa 56,20,132
Ta 8,3,16384
Ta 10,3,17408
Mvaa 88,20,136
G 179,67
G 180,68
Mvav 88,16,184
Ta 12,3,24576
Ta 14,3,25600
Mvav 120,16,192
G 181,69
G 182,70
Mvaa 120,20,140
buffer_load_dwordx4 v[236:239], v26, s[8:11], s93 offen offset:0 sc1
G 183,71
Mvaa 152,20,144
v_lshrrev_b32 v20, 4, v1
K 176,176,177
Mvaa 152,16,128
W 8
Ta 16,4,16384
v_add_u32 v20, 0, v20
Mvaa 156,24,128
Ta 18,4,17408
K 177,178,179
Mvaa 156,28,144
v_mul_u32_u24 v22, 205, v20
K 178,180,181
Mvaa 124,28,140
buffer_load_dwordx4 v[232:235], v27, s[8:11], s28 offen offset:0 sc1
K 179,182,183
Mvav 124,24,192
Ta 20,4,24576
Ta 22,4,25600
Mvav 92,24,184
v_lshrrev_b32 v22, 12, v22
v_accvgpr_write_b32 a196, v176
Mvaa 92,28,136
v_mul_u32_u24 v23, 20, v22
v_accvgpr_write_b32 a197, v177
Mvaa 60,28,132
v_accvgpr_write_b32 a198, v178
v_sub_u32 v20, v20, v23
Mvaa 60,24,60
s_waitcnt vmcnt(12) lgkmcnt(8)
buffer_load_dwordx4 a[32:35], v27, s[8:11], s93 offen offset:0 sc1
v_accvgpr_write_b32 a199, v179
Mvaa 32,0,148
s_add_u32 s28, s28, 512
s_cmp_eq_u32 s28, s4
s_cselect_b32 s93, 10240, 0
s_sub_u32 s28, s28, s93
Mvav 32,4,208
s_add_u32 s93, s28, 20480
Ta 24,5,16384
Ta 26,5,17408
Mvav 64,4,216
v_lshrrev_b32 v23, 1, v22
G 168,200
Mvaa 64,0,152
v_and_b32 v23, 2, v23
G 169,201
Mvaa 96,0,156
buffer_load_dwordx4 a[36:39], v26, s[8:11], s28 offen offset:0 sc1
Ta 28,5,24576
Mvav 96,4,224
Ta 30,5,25600
G 170,202
Mvav 128,4,188
v_xor_b32 v20, v20, v23
G 171,203
Mvav 128,0,200
W 8
v_mul_u32_u24 v22, 80, v22
G 172,204
Mvav 132,8,200
v_add_lshl_u32 v16, v20, v22, 4
G 173,205
Mvav 132,12,188
buffer_load_dwordx4 a[40:43], v26, s[8:11], s93 offen offset:0 sc1
Ta 0,6,16384
Mvav 100,12,224
Ta 2,6,17408
G 174,206
Mvaa 100,8,156
v_lshrrev_b32 v20, 4, v1
G 175,207
Mvaa 68,8,152
Ta 4,6,24576
v_add_u32 v20, 64, v20
Mvav 68,12,216
Ta 6,6,25600
K 168,168,169
Mvav 36,12,208
buffer_load_dwordx4 a[44:47], v27, s[8:11], s28 offen offset:0 sc1
K 169,170,171
Mvaa 36,8,148
W 8
v_mul_u32_u24 v22, 205, v20
K 170,172,173
Mvaa 40,16,148
v_lshrrev_b32 v22, 12, v22
K 171,174,175
Mvav 40,20,208
)"
R"(Ta 8,7,16384
v_mul_u32_u24 v23, 20, v22
Mvav 72,20,216
Ta 10,7,17408
v_accvgpr_write_b32 a200, v168
Mvaa 72,16,152
buffer_load_dwordx4 a[48:51], v27, s[8:11], s93 offen offset:0 sc1
v_accvgpr_write_b32 a201, v169
Mvaa 104,16,156
s_add_u32 s28, s28, 512
s_cmp_eq_u32 s28, s4
s_cselect_b32 s93, 10240, 0
s_sub_u32 s28, s28, s93
Mvav 104,20,224
s_add_u32 s93, s28, 20480
v_sub_u32 v20, v20, v23
v_accvgpr_write_b32 a202, v170
Mvav 136,20,188
Ta 12,7,24576
Ta 14,7,25600
Mvav 136,16,200
W 8
v_lshrrev_b32 v23, 1, v22
v_accvgpr_write_b32 a203, v171
Mvav 140,24,200
buffer_load_dwordx4 a[52:55], v26, s[8:11], s28 offen offset:0 sc1
v_and_b32 v23, 2, v23
Mvav 140,28,188
G 176,72
G 177,73
Mvav 108,28,224
v_xor_b32 v20, v20, v23
G 178,74
Mvaa 108,24,156
Ta 16,8,16384
Ta 18,8,17408
Mvaa 76,24,152
v_mul_u32_u24 v22, 80, v22
G 179,75
Mvav 76,28,216
buffer_load_dwordx4 a[56:59], v26, s[8:11], s93 offen offset:0 sc1
G 180,76
Mvav 44,28,208
Ta 20,8,24576
v_add_lshl_u32 v17, v20, v22, 4
Mvaa 44,24,148
W 7
Ta 22,8,25600
G 181,77
Mvaa 48,0,148
v_subrev_u32 v17, 1024, v17
G 182,78
Mvav 48,4,208
v_lshrrev_b32 v20, 4, v1
G 183,79
Mvav 80,4,216
buffer_load_dwordx4 a[184:187], v27, s[8:11], s28 offen offset:0 sc1
K 176,176,177
Mvaa 80,0,152
Ta 24,9,16384
v_add_u32 v20, 128, v20
Mvaa 112,0,156
Ta 26,9,17408
K 177,178,179
Mvav 112,4,224
v_mul_u32_u24 v22, 205, v20
K 178,180,181
Mvav 144,4,188
v_lshrrev_b32 v22, 12, v22
K 179,182,183
Mvav 144,0,200
W 6
buffer_load_dwordx4 a[188:191], v27, s[8:11], s93 offen offset:0 sc1
Ta 28,9,24576
Mvav 148,8,200
s_add_u32 s28, s28, 512
s_cmp_eq_u32 s28, s4
s_cselect_b32 s93, 10240, 0
s_sub_u32 s28, s28, s93
Mvav 148,12,188
s_add_u32 s93, s28, 20480
Ta 30,9,25600
v_accvgpr_write_b32 a204, v176
Mvav 116,12,224
v_mul_u32_u24 v23, 20, v22
v_accvgpr_write_b32 a205, v177
Mvaa 116,8,156
s_waitcnt vmcnt(15)
v_mov_b64 v[28:29], s[12:13]
s_mov_b64 exec, 1
buffer_store_dwordx2 v[28:29], off, s[8:11], s71 sc1
s_mov_b64 exec, -1
v_sub_u32 v20, v20, v23
v_accvgpr_write_b32 a206, v178
Mvaa 84,8,152
s_waitcnt vmcnt(12) lgkmcnt(0)
s_barrier
v_accvgpr_write_b32 a207, v179
Mvav 84,12,216
v_lshrrev_b32 v23, 1, v22
G 168,208
Mvav 52,12,208
v_and_b32 v23, 2, v23
G 169,209
Mvaa 52,8,148
v_xor_b32 v20, v20, v23
G 170,210
Mvaa 56,16,148
buffer_load_dwordx4 a[60:63], v26, s[8:11], s28 offen offset:0 sc1
G 171,211
Mvav 56,20,208
Ta 0,10,32768
Ta 2,10,33792
Mvav 88,20,216
Ta 4,10,40960
Ta 6,10,41984
Mvaa 88,16,152
v_mul_u32_u24 v22, 80, v22
G 172,212
Mvaa 120,16,156
v_add_lshl_u32 v18, v20, v22, 4
G 173,213
Mvav 120,20,224
buffer_load_dwordx4 v[184:187], v26, s[8:11], s93 offen offset:0 sc1
Ta 8,11,32768
Mvav 152,20,188
Ta 10,11,33792
G 174,214
Mvav 152,16,200
v_subrev_u32 v18, 2048, v18
G 175,215
Mvav 156,24,200
Ta 12,11,40960
v_lshrrev_b32 v20, 4, v1
Mvav 156,28,188
Ta 14,11,41984
K 168,168,169
Mvav 124,28,224
buffer_load_dwordx4 v[192:195], v27, s[8:11], s28 offen offset:0 sc1
v_add_u32 v20, 192, v20
Mvaa 124,24,156
K 169,170,171
K 170,172,173
Mvaa 92,24,152
v_mul_u32_u24 v22, 205, v20
K 171,174,175
Mvav 92,28,216
Ta 16,12,32768
v_lshrrev_b32 v22, 12, v22
Mvav 60,28,208
Ta 18,12,33792
v_accvgpr_write_b32 a208, v168
Mvaa 60,24,148
s_waitcnt vmcnt(12) lgkmcnt(6)
buffer_load_dwordx4 a[128:131], v27, s[8:11], s93 offen offset:0 sc1
v_mul_u32_u24 v23, 20, v22
Mvav 32,0,196
s_add_u32 s28, s28, 512
v_accvgpr_write_b32 a209, v169
v_accvgpr_write_b32 a210, v170
Mvav 32,4,228
s_cmp_eq_u32 s28, s4
s_cselect_b32 s93, 10240, 0
Ta 20,12,40960
v_sub_u32 v20, v20, v23
Mvav 64,4,236
s_sub_u32 s28, s28, s93
Ta 22,12,41984
v_accvgpr_write_b32 a211, v171
Mvav 64,0,204
s_add_u32 s93, s28, 20480
v_lshrrev_b32 v23, 1, v22
G 176,80
Mvav 96,0,212
G 177,81
v_and_b32 v23, 2, v23
Mvav 96,4,232
G 178,82
v_xor_b32 v20, v20, v23
Mvaa 128,4,32
Ta 24,13,32768
Ta 26,13,33792
Mvav 128,0,220
W 6
buffer_load_dwordx4 a[132:135], v26, s[8:11], s28 offen offset:0 sc1
G 179,83
Mvav 132,8,220
v_mul_u32_u24 v22, 80, v22
G 180,84
Mvaa 132,12,32
Ta 28,13,40960
Ta 30,13,41984
Mvav 100,12,232
G 181,85
v_add_lshl_u32 v19, v20, v22, 4
Mvav 100,8,212
G 182,86
v_subrev_u32 v19, 3072, v19
Mvav 68,8,204
G 183,87
v_lshrrev_b32 v20, 4, v1
Mvav 68,12,236
Ta 0,14,32768
K 176,176,177
Mvav 36,12,228
Ta 2,14,33792
K 177,178,179
Mvav 36,8,196
W 6
buffer_load_dwordx4 a[136:139], v26, s[8:11], s93 offen offset:0 sc1
v_add_u32 v20, 256, v20
Mvav 40,16,196
K 178,180,181
v_mul_u32_u24 v22, 205, v20
Mvav 40,20,228
Ta 4,14,40960
K 179,182,183
Mvav 72,20,236
Ta 6,14,41984
v_accvgpr_write_b32 a212, v176
Mvav 72,16,204
v_lshrrev_b32 v22, 12, v22
v_accvgpr_write_b32 a213, v177
Mvav 104,16,212
v_mul_u32_u24 v23, 20, v22
v_accvgpr_write_b32 a214, v178
Mvav 104,20,232
v_sub_u32 v20, v20, v23
v_accvgpr_write_b32 a215, v179
Mvaa 136,20,32
Ta 8,15,32768
Ta 10,15,33792
Mvav 136,16,220
W 6
buffer_load_dwordx4 a[140:143], v27, s[8:11], s28 offen offset:0 sc1
G 168,216
Mvav 140,24,220
v_lshrrev_b32 v23, 1, v22
G 169,217
Mvaa 140,28,32
Ta 12,15,40960
v_and_b32 v23, 2, v23
Mvav 108,28,232
Ta 14,15,41984
G 170,218
Mvav 108,24,212
v_xor_b32 v20, v20, v23
G 171,219
Mvav 76,24,204
G 172,220
v_mul_u32_u24 v22, 80, v22
Mvav 76,28,236
Ta 16,24,32768
G 173,221
Mvav 44,28,228
Ta 18,24,33792
v_add_lshl_u32 v20, v20, v22, 4
Mvav 44,24,196
W 6
buffer_load_dwordx4 a[144:147], v27, s[8:11], s93 offen offset:0 sc1
G 174,222
Mvav 48,0,196
s_add_u32 s28, s28, 512
G 175,223
v_lshrrev_b32 v22, 6, v1
Mvav 48,4,228
s_cmp_eq_u32 s28, s4
s_cselect_b32 s93, 10240, 0
Ta 20,24,40960
Ta 22,24,41984
Mvav 80,4,236
s_sub_u32 s28, s28, s93
K 168,168,169
v_mul_u32_u24 v22, 320, v22
Mvav 80,0,204
s_add_u32 s93, s28, 20480
K 169,170,171
v_bfe_u32 v23, v1, 4, 2
Mvav 112,0,212
K 170,172,173
K 171,174,175
Mvav 112,4,232
Ta 24,25,32768
v_lshl_add_u32 v22, v23, 3, v22
Mvaa 144,4,32
Ta 26,25,33792
v_accvgpr_write_b32 a216, v168
Mvav 144,0,220
W 6
buffer_load_dwordx4 v[200:203], v26, s[8:11], s28 offen offset:0 sc1
v_bfe_u32 v23, v1, 8, 1
Mvav 148,8,220
v_accvgpr_write_b32 a217, v169
v_lshlrev_b32 v23, 5, v23
Mvaa 148,12,32
Ta 28,25,40960
v_accvgpr_write_b32 a218, v170
Mvav 116,12,232
Ta 30,25,41984
v_accvgpr_write_b32 a219, v171
Mvav 116,8,212
v_add_u32 v21, v22, v23
G 176,88
Mvav 84,8,204
v_sub_u32 v22, v22, v23
G 177,89
Mvav 84,12,236
s_mul_i32 s94, s59, 655360
Ta 0,10,49152
G 178,90
Mvav 52,12,228
s_mul_i32 s95, s58, 320
Ta 2,10,50176
G 179,91
Mvav 52,8,196
W 6
buffer_load_dwordx4 v[208:211], v26, s[8:11], s93 offen offset:0 sc1
G 180,92
Mvav 56,16,196
s_add_u32 s94, s94, s95
Ta 4,10,57344
Ta 6,10,58368
Mvav 56,20,228
G 181,93
G 182,94
Mvav 88,20,236
s_add_u32 s40, s26, s94
s_addc_u32 s41, s27, 0
G 183,95
Mvav 88,16,204
s_and_b32 s41, s41, 0xffff
Ta 8,11,49152
K 176,176,177
Mvav 120,16,212
s_mov_b32 s42, 5242880
Ta 10,11,50176
K 177,178,179
Mvav 120,20,232
s_mov_b32 s43, 0x20000
buffer_load_dwordx4 v[216:219], v27, s[8:11], s28 offen offset:0 sc1
K 178,180,181
Mvaa 152,20,32
s_sub_u32 s42, s42, s94
Ta 12,11,57344
K 179,182,183
Mvav 152,16,220
W 7
Ta 14,11,58368
v_accvgpr_write_b32 a220, v176
Mvav 156,24,220
s_mov_b64 s[44:45], s[8:9]
v_accvgpr_write_b32 a221, v177
v_accvgpr_write_b32 a222, v178
Mvaa 156,28,32
s_mov_b32 s47, 0x20000
Ta 16,12,49152
v_accvgpr_write_b32 a223, v179
Mvav 124,28,232
s_and_b32 s48, s14, 1
Ta 18,12,50176
buffer_load_dwordx4 v[224:227], v27, s[8:11], s93 offen offset:0 sc1
Mvav 124,24,212
G 168,224
G 169,225
Mvav 92,24,204
s_mul_i32 s48, s48, 5120
Ta 20,12,57344
Ta 22,12,58368
Mvav 92,28,236
s_lshr_b32 s51, s14, 1
G 170,226
G 171,227
Mvav 60,28,228
G 172,228
Mvav 60,24,196
s_waitcnt vmcnt(13) lgkmcnt(8)
s_and_b32 s94, s14, 1
G 173,229
Mvaa 32,0,36
s_add_u32 s28, s28, 512
Ta 24,13,49152
Ta 26,13,50176
Mvaa 32,4,52
s_cmp_eq_u32 s28, s4
s_cselect_b32 s93, 10240, 0
s_sub_u32 s28, s28, s93
G 174,230
Mvaa 64,4,56
s_add_u32 s93, s28, 20480
s_lshl_b32 s95, s57, 1
buffer_load_dwordx4 v[188:191], v26, s[8:11], s28 offen offset:0 sc1
Mvaa 64,0,40
s_add_u32 s94, s94, s95
Ta 28,13,57344
G 175,231
Mvaa 96,0,44
s_lshl_b32 s54, s94, 3
Ta 30,13,58368
K 168,168,169
Mvaa 96,4,184
)"
R"(K 169,170,171
K 170,172,173
Mvaa 128,4,188
s_add_u32 s54, s54, s59
K 171,174,175
Mvaa 128,0,48
W 8
Ta 0,14,49152
Ta 2,14,50176
Mvaa 132,8,48
s_lshl_b32 s54, s54, 6
buffer_load_dwordx4 v[196:199], v26, s[8:11], s93 offen offset:0 sc1
v_accvgpr_write_b32 a224, v168
Mvaa 132,12,188
s_xor_b32 s55, s54, 15872
v_accvgpr_write_b32 a225, v169
Mvaa 100,12,184
Ta 4,14,57344
v_accvgpr_write_b32 a226, v170
Mvaa 100,8,44
s_add_u32 s54, s54, 20480
Ta 6,14,58368
v_accvgpr_write_b32 a227, v171
Mvaa 68,8,40
s_and_b32 s81, s14, 1
G 176,96
Mvaa 68,12,56
s_add_u32 s55, s55, 20480
Mvaa 36,12,52
G 177,97
Mvaa 36,8,36
W 8
s_and_b32 s95, s14, 2
G 178,98
Mvaa 40,16,36
s_mul_i32 s91, s81, 17164288
Ta 8,15,49152
Ta 10,15,50176
Mvaa 40,20,52
s_lshl_b32 s94, s94, 2
buffer_load_dwordx4 v[204:207], v27, s[8:11], s28 offen offset:0 sc1
G 179,99
Mvaa 72,20,56
s_mul_i32 s82, s56, 536576
s_add_u32 s94, s94, s95
G 180,100
Mvaa 72,16,40
Ta 12,15,57344
Ta 14,15,58368
Mvaa 104,16,44
s_lshl_b32 s95, s95, 4
G 181,101
G 182,102
Mvaa 104,20,184
s_add_u32 s91, s91, s82
G 183,103
Mvaa 136,20,188
s_add_u32 s55, s55, s95
s_add_u32 s91, s91, 4194304
K 176,176,177
Mvaa 136,16,48
W 8
s_lshl_b32 s49, s94, 17
Ta 16,24,49152
Mvaa 140,24,48
Ta 18,24,50176
K 177,178,179
Mvaa 140,28,188
s_lshl_b32 s95, s59, 14
buffer_load_dwordx4 v[212:215], v27, s[8:11], s93 offen offset:0 sc1
K 178,180,181
Mvaa 108,28,184
s_lshr_b32 s82, s14, 1
Ta 20,24,57344
K 179,182,183
Mvaa 108,24,44
s_add_u32 s49, s49, s95
Ta 22,24,58368
v_accvgpr_write_b32 a228, v176
Mvaa 76,24,40
s_lshl_b32 s83, s82, 14
s_xor_b32 s50, s49, 16252928
Mvaa 76,28,56
v_accvgpr_write_b32 a229, v177
Mvaa 44,28,52
s_add_u32 s91, s91, s83
s_add_u32 s49, s49, 39845888
v_accvgpr_write_b32 a230, v178
Mvaa 44,24,36
W 8
Ta 24,25,49152
v_accvgpr_write_b32 a231, v179
Mvaa 48,0,36
s_add_u32 s50, s50, 39845888
Ta 26,25,50176
G 168,232
Mvaa 48,4,52
s_lshl_b32 s81, s81, 14
Mvaa 80,4,56
s_lshr_b32 s94, s15, 3
Ta 28,25,57344
G 169,233
Mvaa 80,0,40
s_lshl_b32 s82, s82, 16
Ta 30,25,58368
G 170,234
Mvaa 112,0,44
s_lshl_b32 s94, s94, 3
G 171,235
Mvaa 112,4,184
s_add_u32 s94, s94, s14
s_or_b32 s92, s81, s82
G 172,236
Mvaa 144,4,188
Mvaa 144,0,48
W 8
s_lshl_b32 s94, s94, 4
G 173,237
Mvaa 148,8,48
s_xor_b32 s92, s92, 0x10000
Ta 0,2,32768
Ta 2,2,33792
Mvaa 148,12,188
s_add_u32 s94, s94, s59
G 174,238
G 175,239
Mvaa 116,12,184
s_mul_i32 s52, s94, 5120
Ta 4,2,40960
Ta 6,2,41984
Mvaa 116,8,44
K 168,168,169
Mvaa 84,8,40
s_lshl_b32 s95, s59, 12
K 169,170,171
Mvaa 84,12,56
K 170,172,173
Mvaa 52,12,52
s_sub_u32 s52, s52, s95
K 171,174,175
Mvaa 52,8,36
W 8
s_add_u32 s52, s52, 56623104
Ta 8,3,32768
Mvaa 56,16,36
Ta 10,3,33792
v_accvgpr_write_b32 a232, v168
Mvaa 56,20,52
s_lshl_b32 s53, s94, 3
v_accvgpr_write_b32 a233, v169
Mvaa 88,20,56
Ta 12,3,40960
v_accvgpr_write_b32 a234, v170
Mvaa 88,16,40
s_add_u32 s53, s53, 65536
Ta 14,3,41984
v_accvgpr_write_b32 a235, v171
Mvaa 120,16,44
G 176,104
v_bfe_u32 v244, v1, 4, 1
Mvaa 120,20,184
Mvaa 152,20,188
G 177,105
v_mul_u32_u24 v244, 0xaaaa, v244
Mvaa 152,16,48
W 8
G 178,106
v_add_u32 v244, 0x4444, v244
Mvaa 156,24,48
Ta 16,4,32768
Ta 18,4,33792
Mvaa 156,28,188
G 179,107
v_lshl_or_b32 v240, v244, 16, v244
Mvaa 124,28,184
G 180,108
v_mov_b32 v241, 0xff800000
Mvaa 124,24,44
Ta 20,4,40960
Ta 22,4,41984
Mvaa 92,24,40
G 181,109
v_add_u32 v242, 0x10000, v1
Mvaa 92,28,56
G 182,110
v_bfe_u32 v244, v1, 4, 4
Mvaa 60,28,52
G 183,111
v_lshrrev_b32 v245, 8, v1
Mvaa 60,24,36
s_waitcnt vmcnt(8) lgkmcnt(8)
K 176,176,177
v_lshlrev_b32 v245, 2, v245
Mvaa 32,0,60
Ta 24,5,32768
Ta 26,5,33792
Mvaa 32,4,132
K 177,178,179
v_sub_u32 v244, v244, v245
Mvaa 64,4,136
K 178,180,181
v_cmp_le_i32_e64 s[72:73], 0, v244
Mvav 64,0,184
Ta 28,5,40960
K 179,182,183
Mvav 96,0,192
Ta 30,5,41984
v_accvgpr_write_b32 a236, v176
Mvaa 96,4,140
v_cmp_le_i32_e64 s[74:75], 1, v244
v_accvgpr_write_b32 a237, v177
Mvaa 128,4,144
v_cmp_le_i32_e64 s[76:77], 2, v244
v_accvgpr_write_b32 a238, v178
Mvaa 128,0,128
W 8
v_cmp_le_i32_e64 s[78:79], 3, v244
v_accvgpr_write_b32 a239, v179
Mvaa 132,8,128
Ta 0,6,32768
Ta 2,6,33792
Mvaa 132,12,144
v_add_u32 v243, 4096, v1
G 168,240
Mvaa 100,12,140
v_add_u32 v244, 8192, v1
G 169,241
Mvav 100,8,192
Ta 4,6,40960
v_add_u32 v245, 12288, v1
Mvav 68,8,184
s_mov_b32 s18, 0x3f803f80
Ta 6,6,41984
G 170,242
Mvaa 68,12,136
s_mov_b32 s19, 0x3f003f00
G 171,243
G 172,244
Mvaa 36,12,132
Mvaa 36,8,60
W 8
v_accvgpr_write_b32 a176, s18
G 173,245
Mvaa 40,16,60
Ta 8,7,32768
Ta 10,7,33792
Mvaa 40,20,132
v_accvgpr_write_b32 a180, s19
G 174,246
Mvaa 72,20,136
G 175,247
v_mov_b32 v252, v241
Mvav 72,16,184
Ta 12,7,40960
Ta 14,7,41984
Mvav 104,16,192
K 168,168,169
v_accvgpr_write_b32 a177, s18
Mvaa 104,20,140
K 169,170,171
v_accvgpr_write_b32 a181, s19
Mvaa 136,20,144
K 170,172,173
v_mov_b32 v253, v241
Mvaa 136,16,128
W 8
K 171,174,175
v_accvgpr_write_b32 a178, s18
Mvaa 140,24,128
Ta 16,8,32768
Ta 18,8,33792
Mvaa 140,28,144
v_accvgpr_write_b32 a240, v168
v_accvgpr_write_b32 a182, s19
Mvaa 108,28,140
v_accvgpr_write_b32 a241, v169
v_mov_b32 v254, v241
Mvav 108,24,192
Ta 20,8,40960
v_accvgpr_write_b32 a242, v170
Mvav 76,24,184
Ta 22,8,41984
v_accvgpr_write_b32 a179, s18
Mvaa 76,28,136
v_accvgpr_write_b32 a243, v171
v_accvgpr_write_b32 a183, s19
Mvaa 44,28,132
G 176,112
v_mov_b32 v255, v241
Mvaa 44,24,60
W 8
G 177,113
G 178,114
Mvaa 48,0,60
Ta 24,9,32768
v_accvgpr_write_b32 a160, 0
Mvaa 48,4,132
Ta 26,9,33792
G 179,115
Mvaa 80,4,136
v_accvgpr_write_b32 a161, 0
G 180,116
Mvav 80,0,184
Ta 28,9,40960
v_accvgpr_write_b32 a162, 0
Mvav 112,0,192
Ta 30,9,41984
G 181,117
Mvaa 112,4,140
v_accvgpr_write_b32 a163, 0
G 182,118
Mvaa 144,4,144
v_accvgpr_write_b32 a164, 0
G 183,119
Mvaa 144,0,128
W 8
v_accvgpr_write_b32 a165, 0
K 176,176,177
Mvaa 148,8,128
Ta 0,2,49152
v_accvgpr_write_b32 a166, 0
Mvaa 148,12,144
Ta 2,2,50176
K 177,178,179
Mvaa 116,12,140
v_accvgpr_write_b32 a167, 0
K 178,180,181
Mvav 116,8,192
v_accvgpr_write_b32 a168, 0
K 179,182,183
Mvav 84,8,184
Ta 4,2,57344
Ta 6,2,58368
Mvaa 84,12,136
v_accvgpr_write_b32 a169, 0
v_accvgpr_write_b32 a244, v176
Mvaa 52,12,132
v_accvgpr_write_b32 a245, v177
v_accvgpr_write_b32 a170, 0
Mvaa 52,8,60
W 8
v_accvgpr_write_b32 a246, v178
v_accvgpr_write_b32 a171, 0
Mvaa 56,16,60
Ta 8,3,49152
v_accvgpr_write_b32 a247, v179
Mvaa 56,20,132
Ta 10,3,50176
v_accvgpr_write_b32 a172, 0
Mvaa 88,20,136
G 168,248
v_accvgpr_write_b32 a173, 0
Mvav 88,16,184
G 169,249
v_accvgpr_write_b32 a174, 0
Mvav 120,16,192
Ta 12,3,57344
Ta 14,3,58368
Mvaa 120,20,140
G 170,250
v_accvgpr_write_b32 a175, 0
Mvaa 152,20,144
s_mov_b32 s90, 0x5d800000
G 171,251
G 172,252
Mvaa 152,16,128
W 8
s_mov_b64 s[88:89], 0
G 173,253
Mvaa 156,24,128
s_mov_b32 s64, 0
Ta 16,4,49152
Ta 18,4,50176
Mvaa 156,28,144
s_and_b32 s18, s14, 1
G 174,254
G 175,255
Mvaa 124,28,140
s_lshl_b32 s62, s18, 14
Ta 20,4,57344
Ta 22,4,58368
Mvav 124,24,192
K 168,168,169
Mvav 92,24,184
s_lshl_b32 s20, s57, 15
K 169,170,171
Mvaa 92,28,136
s_sub_u32 s20, 507904, s20
K 170,172,173
Mvaa 60,28,132
K 171,174,175
Mvaa 60,24,60
s_waitcnt vmcnt(0) lgkmcnt(8)
s_add_u32 s20, s20, s62
Ta 24,5,49152
Mvav 32,0,200
s_lshl_b32 s65, s57, 1
Ta 26,5,50176
v_accvgpr_write_b32 a248, v168
Mvav 32,4,188
)"
R"(s_sub_u32 s65, s65, 1
v_accvgpr_write_b32 a249, v169
Mvav 64,4,196
Ta 28,5,57344
v_accvgpr_write_b32 a250, v170
Mvav 64,0,208
s_max_i32 s65, s65, 0
Ta 30,5,58368
v_accvgpr_write_b32 a251, v171
Mvav 96,0,216
s_sub_u32 s66, s60, 1
G 176,120
Mvav 96,4,204
s_cmp_lt_u32 s14, 2
s_cselect_b32 s20, s20, 0
s_cselect_b32 s66, s66, 15
s_cselect_b32 s21, 32768, 0
Mvav 128,4,212
G 177,121
Mvav 128,0,224
W 8
s_add_u32 s61, s91, s20
G 178,122
Mvav 132,8,224
s_add_u32 s62, s62, s21
Ta 0,6,49152
Ta 2,6,50176
Mvav 132,12,212
G 179,123
G 180,124
Mvav 100,12,204
s_sub_u32 s63, 16384, s21
Ta 4,6,57344
Ta 6,6,58368
Mvav 100,8,216
s_lshl_b32 s67, s60, 7
G 181,125
Mvav 68,8,208
s_add_u32 s67, s67, s56
G 182,126
Mvav 68,12,196
G 183,127
Mvav 36,12,188
s_lshl_b32 s67, s67, 12
K 176,176,177
Mvav 36,8,200
W 8
s_add_u32 s67, s67, 39845888
Ta 8,7,49152
Mvav 40,16,200
s_lshl_b32 s69, s60, 3
Ta 10,7,50176
K 177,178,179
Mvav 40,20,188
K 178,180,181
Mvav 72,20,196
s_add_u32 s69, s69, s59
Ta 12,7,57344
K 179,182,183
Mvav 72,16,208
s_lshl_b32 s69, s69, 6
Ta 14,7,58368
v_accvgpr_write_b32 a252, v176
Mvav 104,16,216
Mvav 104,20,204
s_lshl_b32 s21, s58, 3
v_accvgpr_write_b32 a253, v177
Mvav 136,20,212
s_add_u32 s69, s69, s21
v_accvgpr_write_b32 a254, v178
Mvav 136,16,224
W 8
s_add_u32 s69, s69, 20480
v_accvgpr_write_b32 a255, v179
Mvav 140,24,224
Ta 16,8,49152
Ta 18,8,50176
Mvav 140,28,212
s_mov_b32 s70, 0
Mvav 108,28,204
s_mov_b32 s37, 0
Ta 20,8,57344
Ta 22,8,58368
Mvav 108,24,216
s_sub_u32 s18, s57, 4
Mvav 76,24,208
Mvav 76,28,196
s_lshl_b32 s18, s18, 2
Mvav 44,28,188
s_add_u32 s18, s18, s58
Mvav 44,24,200
W 8
Ta 24,9,49152
Ta 26,9,50176
Mvav 48,0,200
s_mul_i32 s85, s59, 7
Mvav 48,4,188
s_add_u32 s85, s85, s18
Mvav 80,4,196
s_cmp_lt_u32 s18, 7
s_cselect_b32 s85, s85, 99
Ta 28,9,57344
Ta 30,9,58368
Mvav 80,0,208
Mvav 112,0,216
s_cmp_lt_u32 s85, 54
s_cselect_b32 s65, 2, s65
Mvav 112,4,204
s_mov_b32 s68, 0
Mvav 144,4,212
s_mov_b64 s[24:25], -1
Mvav 144,0,224
W 8
Mvav 148,8,224
s_mov_b32 s22, 0x3e0293ee
Mvav 148,12,212
s_mov_b32 s23, 0x3e0293ee
Mvav 116,12,204
Mvav 116,8,216
Mvav 84,8,208
Mvav 84,12,196
Mvav 52,12,188
Mvav 52,8,200
W 4
Mvav 56,16,200
Mvav 56,20,188
Mvav 88,20,196
Mvav 88,16,208
Mvav 120,16,216
W 0
s_barrier
Ra 128,242,0
Mvav 120,20,204
Mvav 152,20,212
Ra 132,242,1024
Mvav 152,16,224
Mvav 156,24,224
Ra 136,242,2048
Mvav 156,28,212
Mvav 124,28,204
Ra 140,242,3072
Mvav 124,24,216
Mvav 92,24,208
Mvav 92,28,196
Mvav 60,28,188
Mvav 60,24,200
.Lago%=:
s_cmp_eq_u32 s60, 1
s_cbranch_scc1 .Lpe_48%=
s_cmp_eq_u32 s60, 0
s_cbranch_scc1 .Lpd_32%=
v_pk_mul_f32 v[32:33], v[32:33], s[22:23]
v_pk_mul_f32 v[34:35], v[34:35], s[22:23]
v_pk_mul_f32 v[36:37], v[36:37], s[22:23]
v_pk_mul_f32 v[38:39], v[38:39], s[22:23]
v_pk_mul_f32 v[64:65], v[64:65], s[22:23]
v_pk_mul_f32 v[66:67], v[66:67], s[22:23]
v_pk_mul_f32 v[68:69], v[68:69], s[22:23]
v_pk_mul_f32 v[70:71], v[70:71], s[22:23]
v_pk_mul_f32 v[96:97], v[96:97], s[22:23]
v_pk_mul_f32 v[98:99], v[98:99], s[22:23]
v_pk_mul_f32 v[100:101], v[100:101], s[22:23]
v_pk_mul_f32 v[102:103], v[102:103], s[22:23]
v_pk_mul_f32 v[128:129], v[128:129], s[22:23]
v_pk_mul_f32 v[130:131], v[130:131], s[22:23]
v_pk_mul_f32 v[132:133], v[132:133], s[22:23]
v_pk_mul_f32 v[134:135], v[134:135], s[22:23]
v_pk_mul_f32 v[40:41], v[40:41], s[22:23]
v_pk_mul_f32 v[42:43], v[42:43], s[22:23]
v_pk_mul_f32 v[44:45], v[44:45], s[22:23]
v_pk_mul_f32 v[46:47], v[46:47], s[22:23]
v_pk_mul_f32 v[72:73], v[72:73], s[22:23]
v_pk_mul_f32 v[74:75], v[74:75], s[22:23]
v_pk_mul_f32 v[76:77], v[76:77], s[22:23]
v_pk_mul_f32 v[78:79], v[78:79], s[22:23]
v_pk_mul_f32 v[104:105], v[104:105], s[22:23]
v_pk_mul_f32 v[106:107], v[106:107], s[22:23]
v_pk_mul_f32 v[108:109], v[108:109], s[22:23]
v_pk_mul_f32 v[110:111], v[110:111], s[22:23]
v_pk_mul_f32 v[136:137], v[136:137], s[22:23]
v_pk_mul_f32 v[138:139], v[138:139], s[22:23]
v_pk_mul_f32 v[140:141], v[140:141], s[22:23]
v_pk_mul_f32 v[142:143], v[142:143], s[22:23]
v_pk_mul_f32 v[48:49], v[48:49], s[22:23]
v_pk_mul_f32 v[50:51], v[50:51], s[22:23]
v_pk_mul_f32 v[52:53], v[52:53], s[22:23]
v_pk_mul_f32 v[54:55], v[54:55], s[22:23]
v_pk_mul_f32 v[80:81], v[80:81], s[22:23]
v_pk_mul_f32 v[82:83], v[82:83], s[22:23]
v_pk_mul_f32 v[84:85], v[84:85], s[22:23]
v_pk_mul_f32 v[86:87], v[86:87], s[22:23]
v_pk_mul_f32 v[112:113], v[112:113], s[22:23]
v_pk_mul_f32 v[114:115], v[114:115], s[22:23]
v_pk_mul_f32 v[116:117], v[116:117], s[22:23]
v_pk_mul_f32 v[118:119], v[118:119], s[22:23]
v_pk_mul_f32 v[144:145], v[144:145], s[22:23]
v_pk_mul_f32 v[146:147], v[146:147], s[22:23]
v_pk_mul_f32 v[148:149], v[148:149], s[22:23]
v_pk_mul_f32 v[150:151], v[150:151], s[22:23]
v_pk_mul_f32 v[56:57], v[56:57], s[22:23]
v_pk_mul_f32 v[58:59], v[58:59], s[22:23]
v_pk_mul_f32 v[60:61], v[60:61], s[22:23]
v_pk_mul_f32 v[62:63], v[62:63], s[22:23]
v_pk_mul_f32 v[88:89], v[88:89], s[22:23]
v_pk_mul_f32 v[90:91], v[90:91], s[22:23]
v_pk_mul_f32 v[92:93], v[92:93], s[22:23]
v_pk_mul_f32 v[94:95], v[94:95], s[22:23]
v_pk_mul_f32 v[120:121], v[120:121], s[22:23]
v_pk_mul_f32 v[122:123], v[122:123], s[22:23]
v_pk_mul_f32 v[124:125], v[124:125], s[22:23]
v_pk_mul_f32 v[126:127], v[126:127], s[22:23]
v_pk_mul_f32 v[152:153], v[152:153], s[22:23]
v_pk_mul_f32 v[154:155], v[154:155], s[22:23]
v_pk_mul_f32 v[156:157], v[156:157], s[22:23]
v_pk_mul_f32 v[158:159], v[158:159], s[22:23]
K 32,32,33
K 33,34,35
K 34,36,37
K 35,38,39
K 40,40,41
K 41,42,43
K 42,44,45
K 43,46,47
W 0
Zvav 196,128,32
s_add_u32 s36, s29, 65536
Ra 144,242,4096
Ra 148,242,5120
Ra 152,242,6144
Ra 156,242,7168
Mvav 196,132,40
K 64,64,65
K 65,66,67
K 66,68,69
K 67,70,71
K 72,72,73
K 73,74,75
K 74,76,77
K 75,78,79
s_nop 1
Zvav 204,132,72
buffer_store_dwordx4 a[192:195], v1, s[8:11], s36 offen offset:0 sc1
K 96,96,97
K 97,98,99
K 98,100,101
Mvav 204,128,64
K 99,102,103
K 104,104,105
K 105,106,107
K 106,108,109
K 107,110,111
Zvav 212,128,96
buffer_store_dwordx4 a[200:203], v1, s[8:11], s36 offen offset:1024 sc1
K 128,128,129
K 129,130,131
K 130,132,133
Mvav 212,132,104
K 131,134,135
K 136,136,137
K 137,138,139
K 138,140,141
K 139,142,143
s_nop 1
Zvav 220,132,136
K 48,48,49
K 49,50,51
K 50,52,53
K 51,54,55
Mvav 220,128,128
K 56,56,57
K 57,58,59
K 58,60,61
K 59,62,63
Mvav 196,136,48
buffer_store_dwordx4 a[208:211], v1, s[8:11], s36 offen offset:2048 sc1
Ra 128,242,8192
Ra 132,242,9216
K 80,80,81
Mvav 196,140,56
K 81,82,83
K 82,84,85
K 83,86,87
K 88,88,89
K 89,90,91
K 90,92,93
K 91,94,95
s_nop 1
Mvav 204,140,88
buffer_store_dwordx4 a[216:219], v1, s[8:11], s36 offen offset:3072 sc1
s_add_u32 s36, s29, 69632
K 112,112,113
K 113,114,115
K 114,116,117
Mvav 204,136,80
K 115,118,119
K 120,120,121
K 121,122,123
K 122,124,125
K 123,126,127
Mvav 212,136,112
K 144,144,145
K 145,146,147
K 146,148,149
K 147,150,151
Mvav 212,140,120
K 152,152,153
K 153,154,155
K 154,156,157
K 155,158,159
s_nop 1
Mvav 220,140,152
buffer_store_dwordx4 a[224:227], v1, s[8:11], s36 offen offset:0 sc1
v_accvgpr_write_b32 a0, v32
v_accvgpr_write_b32 a1, v33
v_accvgpr_write_b32 a2, v34
Mvav 220,136,144
W 2
Ra 136,242,10240
v_accvgpr_write_b32 a3, v35
v_accvgpr_write_b32 a4, v40
Zvav 36,144,32
Ra 140,242,11264
buffer_store_dwordx4 a[232:235], v1, s[8:11], s36 offen offset:1024 sc1
v_accvgpr_write_b32 a5, v41
Mvav 36,148,40
v_accvgpr_write_b32 a6, v42
v_accvgpr_write_b32 a7, v43
v_max3_f32 v4, v196, v197, v198
v_max3_f32 v4, v4, v199, v252
Zvav 68,148,72
v_max3_f32 v5, v204, v205, v206
v_max3_f32 v5, v5, v207, v253
v_max3_f32 v6, v212, v213, v214
v_max3_f32 v6, v6, v215, v254
Mvav 68,144,64
v_max3_f32 v7, v220, v221, v222
v_max3_f32 v7, v7, v223, v255
v_mov_b32 v8, v4
v_mov_b32 v9, v5
Zvav 100,144,96
v_mov_b32 v10, v6
v_mov_b32 v11, v7
v_permlane32_swap_b32 v4, v8
v_accvgpr_write_b32 a8, v48
Mvav 100,148,104
v_permlane32_swap_b32 v5, v9
v_permlane32_swap_b32 v6, v10
v_permlane32_swap_b32 v7, v11
v_max_f32 v4, v4, v8
Zvav 132,148,136
v_max_f32 v5, v5, v9
v_max_f32 v6, v6, v10
v_max_f32 v7, v7, v11
v_mov_b32 v8, v4
Mvav 132,144,128
v_mov_b32 v9, v5
v_mov_b32 v10, v6
v_mov_b32 v11, v7
v_accvgpr_write_b32 a9, v49
Mvav 36,152,48
v_permlane16_swap_b32 v4, v8
v_permlane16_swap_b32 v5, v9
v_permlane16_swap_b32 v6, v10
v_permlane16_swap_b32 v7, v11
Mvav 36,156,56
v_max_f32 v4, v4, v8
v_max_f32 v5, v5, v9
v_max_f32 v6, v6, v10
v_max_f32 v7, v7, v11
Mvav 68,156,88
v_sub_f32 v4, 0, v4
v_sub_f32 v5, 0, v5
v_sub_f32 v6, 0, v6
v_sub_f32 v7, 0, v7
Mvav 68,152,80
v_mov_b32 v224, v4
v_mov_b32 v225, v4
v_mov_b32 v226, v4
v_mov_b32 v227, v4
Mvav 100,152,112
v_mov_b32 v228, v5
v_mov_b32 v229, v5
v_mov_b32 v230, v5
v_mov_b32 v231, v5
Mvav 100,156,120
v_mov_b32 v232, v6
v_mov_b32 v233, v6
v_mov_b32 v234, v6
v_mov_b32 v235, v6
Mvav 132,156,152
v_mov_b32 v236, v7
v_mov_b32 v237, v7
v_mov_b32 v238, v7
v_mov_b32 v239, v7
Mvav 132,152,144
W 0
buffer_store_dwordx4 a[240:243], v1, s[8:11], s36 offen offset:2048 sc1
v_accvgpr_write_b32 a10, v50
v_accvgpr_write_b32 a11, v51
v_accvgpr_write_b32 a12, v56
)"
R"(Cvav 160,128,32,224
s_and_b32 s80, s14, 1
v_accvgpr_write_b32 a13, v57
v_accvgpr_write_b32 a14, v58
v_accvgpr_write_b32 a15, v59
v_accvgpr_write_b32 a16, v64
Mvav 160,132,40
buffer_store_dwordx4 a[248:251], v1, s[8:11], s36 offen offset:3072 sc1
s_add_u32 s36, s29, 393216
Ra 144,242,12288
Ra 148,242,13312
v_accvgpr_write_b32 a17, v65
Cvav 164,132,72,228
s_lshl_b32 s80, s80, 14
v_accvgpr_write_b32 a18, v66
v_accvgpr_write_b32 a19, v67
v_accvgpr_write_b32 a20, v72
v_accvgpr_write_b32 a21, v73
Mvav 164,128,64
s_add_u32 s80, s80, 98304
buffer_store_dwordx4 a[196:199], v1, s[8:11], s36 offen offset:0 sc1
v_accvgpr_write_b32 a22, v74
v_accvgpr_write_b32 a23, v75
v_accvgpr_write_b32 a24, v80
Cvav 168,128,96,232
v_accvgpr_write_b32 a25, v81
v_accvgpr_write_b32 a26, v82
A 196,196,224
v_accvgpr_write_b32 a27, v83
Mvav 168,132,104
buffer_store_dwordx4 a[204:207], v1, s[8:11], s36 offen offset:1024 sc1
A 197,197,224
A 198,198,224
v_accvgpr_write_b32 a28, v88
Cvav 172,132,136,236
Ra 152,242,14336
Ra 156,242,15360
A 199,199,224
v_accvgpr_write_b32 a29, v89
Mvav 172,128,128
buffer_store_dwordx4 a[212:215], v1, s[8:11], s36 offen offset:2048 sc1
X 196
v_accvgpr_write_b32 a30, v90
Mvav 160,136,48
X 197
v_accvgpr_write_b32 a31, v91
v_accvgpr_write_b32 a32, v96
Mvav 160,140,56
X 198
K 192,196,197
v_accvgpr_write_b32 a33, v97
Mvav 164,140,88
X 199
A 204,204,228
v_accvgpr_write_b32 a34, v98
Mvav 164,136,80
buffer_store_dwordx4 a[220:223], v1, s[8:11], s36 offen offset:3072 sc1
s_add_u32 s36, s29, 397312
K 193,198,199
v_accvgpr_write_b32 a35, v99
A 205,205,228
Mvav 168,136,112
A 206,206,228
v_accvgpr_write_b32 a36, v104
v_add_u32 v8, s80, v1
A 207,207,228
Mvav 168,140,120
X 204
v_accvgpr_write_b32 a37, v105
v_accvgpr_write_b32 a38, v106
Mvav 172,140,152
X 205
X 206
Mvav 172,136,144
W 0
buffer_store_dwordx4 a[228:231], v1, s[8:11], s36 offen offset:0 sc1
v_accvgpr_write_b32 a39, v107
v_accvgpr_write_b32 a40, v112
K 200,204,205
Cvav 176,144,32,224
Ra 128,242,24576
X 207
v_accvgpr_write_b32 a41, v113
Mvav 176,148,40
Ra 132,242,25600
A 212,212,232
v_accvgpr_write_b32 a42, v114
K 201,206,207
Cvav 180,148,72,228
A 213,213,232
v_accvgpr_write_b32 a43, v115
A 214,214,232
A 215,215,232
Mvav 180,144,64
buffer_store_dwordx4 a[236:239], v1, s[8:11], s36 offen offset:1024 sc1
v_accvgpr_write_b32 a44, v120
X 212
Cvav 184,144,96,232
v_accvgpr_write_b32 a45, v121
X 213
v_accvgpr_write_b32 a46, v122
Mvav 184,148,104
X 214
v_accvgpr_write_b32 a47, v123
K 208,212,213
Cvav 188,148,136,236
buffer_store_dwordx4 a[244:247], v1, s[8:11], s36 offen offset:2048 sc1
v_accvgpr_write_b32 a48, v128
X 215
Mvav 188,144,128
v_accvgpr_write_b32 a49, v129
A 220,220,236
v_accvgpr_write_b32 a50, v130
K 209,214,215
Mvav 176,152,48
A 221,221,236
v_accvgpr_write_b32 a51, v131
A 222,222,236
A 223,223,236
Mvav 176,156,56
Ra 136,242,26624
v_accvgpr_write_b32 a52, v136
X 220
Mvav 180,156,88
Ra 140,242,27648
v_accvgpr_write_b32 a53, v137
X 221
Mvav 180,152,80
buffer_store_dwordx4 a[252:255], v1, s[8:11], s36 offen offset:3072 sc1
v_accvgpr_write_b32 a54, v138
X 222
Mvav 184,152,112
v_accvgpr_write_b32 a55, v139
v_accvgpr_write_b32 a56, v144
v_accvgpr_write_b32 a57, v145
v_accvgpr_write_b32 a58, v146
v_accvgpr_write_b32 a59, v147
v_accvgpr_write_b32 a60, v152
v_accvgpr_write_b32 a61, v153
v_accvgpr_write_b32 a62, v154
v_accvgpr_write_b32 a63, v155
Mvav 184,156,120
s_barrier
K 216,220,221
X 223
A 36,36,224
Mvav 188,156,152
K 217,222,223
A 37,37,224
A 38,38,224
A 39,39,224
X 36
X 37
X 38
K 194,36,37
X 39
A 68,68,228
K 195,38,39
A 69,69,228
A 70,70,228
A 71,71,228
X 68
X 69
X 70
K 202,68,69
X 71
A 100,100,232
K 203,70,71
A 101,101,232
A 102,102,232
A 103,103,232
X 100
X 101
X 102
K 210,100,101
X 103
A 132,132,236
K 211,102,103
A 133,133,236
A 134,134,236
A 135,135,236
X 132
X 133
X 134
K 218,132,133
X 135
s_nop 0
K 219,134,135
Mvav 188,152,144
W 0
Ra 144,242,28672
Ra 148,242,29696
Ra 152,242,30720
Zvav 32,128,192
Ra 156,242,31744
Zvav 64,128,200
Zvav 96,128,208
s_and_b32 s80, s14, 1
s_xor_b32 s62, s62, 0x10000
Zvav 128,128,216
s_lshl_b32 s80, s80, 14
s_add_u32 s80, s80, 131072
v_add_u32 v23, 61440, v21
Zvav 132,132,216
v_add_u32 v12, s80, v1
Ra 64,12,8192
Ra 68,12,9216
v_add_u32 v24, 61440, v22
Zvav 100,132,208
Ra 72,12,10240
Ra 76,12,11264
Ra 80,12,12288
v_add_u32 v25, 122880, v21
Zvav 68,132,200
Ra 84,12,13312
Ra 88,12,14336
Ra 92,12,15360
v_add_u32 v26, 122880, v22
Zvav 36,132,192
Ra 96,12,0
Ra 100,12,1024
Ra 104,12,2048
Zvav 40,136,192
Ra 108,12,3072
Ra 112,12,4096
X 172
Zvav 72,136,200
Ra 116,12,5120
Ra 120,12,6144
X 173
Zvav 104,136,208
Ra 124,12,7168
X 174
K 220,172,173
Zvav 136,136,216
X 175
Zvav 140,140,216
X 168
K 221,174,175
Zvav 108,140,208
X 169
X 170
Zvav 76,140,200
s_waitcnt vmcnt(0) lgkmcnt(0)
s_barrier
v_xor_b32 v242, 0x10000, v242
ds_read_b128 v[248:251], v8
Zvav 44,140,192
s_add_u32 s80, s71, 128
v_mov_b64 v[28:29], s[12:13]
s_mov_b64 exec, 1
buffer_store_dwordx2 v[28:29], off, s[8:11], s80 sc1
s_mov_b64 exec, -1
Ra 128,242,0
K 212,168,169
X 171
Zvav 48,144,192
X 164
K 213,170,171
Zvav 80,144,200
X 165
X 166
Zvav 112,144,208
Ra 132,242,1024
K 204,164,165
X 167
Zvav 144,144,216
X 160
Zvav 148,148,216
K 205,166,167
X 161
Zvav 116,148,208
Ra 136,242,2048
X 162
Zvav 84,148,200
W 3
K 196,160,161
X 163
Zvav 52,148,192
X 176
K 197,162,163
Zvav 56,152,192
Ra 140,242,3072
X 177
Zvav 88,152,200
X 178
K 198,176,177
Zvav 120,152,208
X 179
X 180
Zvav 152,152,216
K 199,178,179
Zvav 156,156,216
X 181
X 182
Zvav 124,156,208
K 206,180,181
X 183
Zvav 92,156,200
K 207,182,183
Zvav 60,156,192
.Ll0t_46%=:
s_cmp_eq_u32 s64, s65
s_cbranch_scc1 .Ll1t_35%=
.Ll0_33%=:
W 0
Cvaa 160,128,0,224
Ra 144,242,4096
Ra 148,242,5120
Mvaa 160,132,4
s_xor_b32 s62, s62, 0x10000
Ra 152,242,6144
Cvaa 164,132,20,228
X 184
Mvaa 164,128,16
X 185
Cvaa 168,128,32,232
X 186
Mvaa 168,132,36
Ra 156,242,7168
K 214,184,185
Cvaa 172,132,52,236
X 187
Mvaa 172,128,48
X 188
Mvaa 160,136,8
Ra 128,242,16384
Mvaa 160,140,12
K 215,186,187
Mvaa 164,140,28
X 189
Mvaa 164,136,24
X 190
Mvaa 168,136,40
Ra 132,242,17408
Mvaa 168,140,44
K 222,188,189
Mvaa 172,140,60
X 191
Mvaa 172,136,56
K 223,190,191
v_smfmac_f32_16x16x64_bf16 a[160:163], a[176:179], v[192:199], v240
Ra 136,242,18432
v_smfmac_f32_16x16x64_bf16 a[164:167], a[176:179], v[200:207], v240
X 160
v_smfmac_f32_16x16x64_bf16 a[168:171], a[176:179], v[208:215], v240
X 161
v_smfmac_f32_16x16x64_bf16 a[172:175], a[176:179], v[216:223], v240
W 3
X 162
Cvaa 176,144,0,224
Ra 140,242,19456
Mvaa 176,148,4
K 192,160,161
Cvaa 180,148,20,228
X 163
Mvaa 180,144,16
X 164
Cvaa 184,144,32,232
Mvaa 184,148,36
K 193,162,163
Cvaa 188,148,52,236
X 165
Mvaa 188,144,48
X 166
Mvaa 176,152,8
Ra 144,242,20480
Ra 148,242,21504
Mvaa 176,156,12
K 200,164,165
Mvaa 180,156,28
X 167
Mvaa 180,152,24
X 168
Mvaa 184,152,40
Mvaa 184,156,44
K 201,166,167
Mvaa 188,156,60
X 169
Mvaa 188,152,56
W 2
Ra 152,242,22528
Mvav 32,128,196
X 170
Mvav 64,128,204
Ra 156,242,23552
K 208,168,169
Mvav 96,128,212
X 171
Mvav 128,128,220
Mvav 132,132,220
X 172
Mvav 100,132,212
s_add_u32 s64, s64, 1
Ra 128,242,8192
K 209,170,171
Mvav 68,132,204
X 173
Mvav 36,132,196
Mvav 40,136,196
)"
R"(X 174
Mvav 72,136,204
Ra 132,242,9216
K 216,172,173
Mvav 104,136,212
X 175
Mvav 136,136,220
Mvav 140,140,220
X 176
Mvav 108,140,212
Ra 136,242,10240
K 217,174,175
Mvav 76,140,204
X 177
Mvav 44,140,196
W 3
Mvav 48,144,196
X 178
Mvav 80,144,204
Ra 140,242,11264
Mvav 112,144,212
K 194,176,177
Mvav 144,144,220
X 179
Mvav 148,148,220
X 180
Mvav 116,148,212
Ra 144,242,12288
Mvav 84,148,204
K 195,178,179
Mvav 52,148,196
X 181
Mvav 56,152,196
X 182
Mvav 88,152,204
Ra 148,242,13312
Mvav 120,152,212
K 202,180,181
Mvav 152,152,220
X 183
Mvav 156,156,220
X 184
Mvav 124,156,212
Ra 152,242,14336
Mvav 92,156,204
K 203,182,183
Mvav 60,156,196
W 3
X 185
Cvaa 160,128,0,224
X 186
Mvaa 160,132,4
Ra 156,242,15360
Cvaa 164,132,20,228
K 210,184,185
Mvaa 164,128,16
X 187
Cvaa 168,128,32,232
X 188
Mvaa 168,132,36
Cvaa 172,132,52,236
K 211,186,187
Mvaa 172,128,48
X 189
Mvaa 160,136,8
Ra 128,242,24576
Ra 132,242,25600
Mvaa 160,140,12
X 190
Mvaa 164,140,28
K 218,188,189
Mvaa 164,136,24
X 191
Mvaa 168,136,40
Mvaa 168,140,44
Mvaa 172,140,60
X 160
Mvaa 172,136,56
W 2
Ra 136,242,26624
K 219,190,191
Cvaa 176,144,0,224
X 161
Mvaa 176,148,4
Ra 140,242,27648
Cvaa 180,148,20,228
X 162
Mvaa 180,144,16
K 196,160,161
Cvaa 184,144,32,232
X 163
Mvaa 184,148,36
X 164
Cvaa 188,148,52,236
K 197,162,163
Mvaa 188,144,48
X 165
Mvaa 176,152,8
Ra 144,242,28672
Ra 148,242,29696
Mvaa 176,156,12
X 166
Mvaa 180,156,28
K 204,164,165
Mvaa 180,152,24
X 167
Mvaa 184,152,40
Mvaa 184,156,44
X 168
Mvaa 188,156,60
Mvaa 188,152,56
W 2
Ra 152,242,30720
K 205,166,167
Mvav 32,128,192
X 169
Mvav 64,128,200
Ra 156,242,31744
Mvav 96,128,208
X 170
Mvav 128,128,216
K 212,168,169
Mvav 132,132,216
X 171
Mvav 100,132,208
X 172
Mvav 68,132,200
Mvav 36,132,192
K 213,170,171
Mvav 40,136,192
X 173
Mvav 72,136,200
X 174
Mvav 104,136,208
Mvav 136,136,216
K 220,172,173
Mvav 140,140,216
X 175
Mvav 108,140,208
X 176
Mvav 76,140,200
W 0
s_barrier
v_xor_b32 v242, 0x10000, v242
Mvav 44,140,192
Ra 128,242,0
K 221,174,175
Mvav 48,144,192
X 177
Mvav 80,144,200
X 178
Mvav 112,144,208
Ra 132,242,1024
Mvav 144,144,216
K 198,176,177
Mvav 148,148,216
X 179
Mvav 116,148,208
X 180
Mvav 84,148,200
Ra 136,242,2048
Mvav 52,148,192
K 199,178,179
Mvav 56,152,192
X 181
Mvav 88,152,200
Ra 140,242,3072
Mvav 120,152,208
X 182
Mvav 152,152,216
K 206,180,181
Mvav 156,156,216
X 183
Mvav 124,156,208
Mvav 92,156,200
K 207,182,183
Mvav 60,156,192
s_cmp_lg_u32 s64, s65
s_cbranch_scc1 .Ll0_33%=
s_cmp_lt_u32 s85, 54
s_cbranch_scc1 .Lmoe_44%=
.Ll1t_35%=:
s_cmp_eq_u32 s64, s66
s_cbranch_scc1 .Lsp1_36%=
.Ll1_34%=:
W 0
Cvaa 160,128,0,224
Ra 144,242,4096
Ra 148,242,5120
Mvaa 160,132,4
s_add_u32 m0, s62, 0
Ra 152,242,6144
Cvaa 164,132,20,228
X 184
Mvaa 164,128,16
X 185
Cvaa 168,128,32,232
X 186
Mvaa 168,132,36
Ra 156,242,7168
K 214,184,185
Cvaa 172,132,52,236
Dc 1,8,61,0
Mvaa 172,128,48
X 187
Mvaa 160,136,8
X 188
Mvaa 160,140,12
Ra 128,242,16384
K 215,186,187
Mvaa 164,140,28
Dc 1,8,61,1024
Mvaa 164,136,24
X 189
Mvaa 168,136,40
X 190
Mvaa 168,140,44
Ra 132,242,17408
K 222,188,189
Mvaa 172,140,60
Dc 1,8,61,2048
Mvaa 172,136,56
X 191
v_smfmac_f32_16x16x64_bf16 a[160:163], a[176:179], v[192:199], v240
Ra 136,242,18432
K 223,190,191
v_smfmac_f32_16x16x64_bf16 a[164:167], a[176:179], v[200:207], v240
X 160
v_smfmac_f32_16x16x64_bf16 a[168:171], a[176:179], v[208:215], v240
Dc 1,8,61,3072
v_smfmac_f32_16x16x64_bf16 a[172:175], a[176:179], v[216:223], v240
W 3
X 161
Cvaa 176,144,0,224
s_add_u32 m0, s62, 4096
X 162
Mvaa 176,148,4
Ra 140,242,19456
K 192,160,161
Cvaa 180,148,20,228
X 163
Mvaa 180,144,16
Dc 243,8,61,0
Cvaa 184,144,32,232
X 164
Mvaa 184,148,36
K 193,162,163
Cvaa 188,148,52,236
X 165
Mvaa 188,144,48
Dc 243,8,61,1024
Ra 144,242,20480
Mvaa 176,152,8
X 166
Mvaa 176,156,12
Ra 148,242,21504
K 200,164,165
Mvaa 180,156,28
X 167
Mvaa 180,152,24
Dc 243,8,61,2048
Mvaa 184,152,40
X 168
Mvaa 184,156,44
K 201,166,167
Mvaa 188,156,60
X 169
Mvaa 188,152,56
W 2
Dc 243,8,61,3072
Ra 152,242,22528
Mvav 32,128,196
s_add_u32 m0, s62, 8192
X 170
Mvav 64,128,204
Ra 156,242,23552
K 208,168,169
Mvav 96,128,212
X 171
Mvav 128,128,220
Dc 244,8,61,0
Mvav 132,132,220
X 172
Mvav 100,132,212
Ra 128,242,8192
K 209,170,171
Mvav 68,132,204
X 173
Mvav 36,132,196
Dc 244,8,61,1024
Mvav 40,136,196
X 174
Mvav 72,136,204
Ra 132,242,9216
K 216,172,173
Mvav 104,136,212
X 175
Mvav 136,136,220
Dc 244,8,61,2048
Mvav 140,140,220
X 176
Mvav 108,140,212
Ra 136,242,10240
K 217,174,175
Mvav 76,140,204
X 177
Mvav 44,140,196
W 3
Dc 244,8,61,3072
Mvav 48,144,196
X 178
Mvav 80,144,204
s_add_u32 m0, s62, 12288
Ra 140,242,11264
Mvav 112,144,212
K 194,176,177
Mvav 144,144,220
X 179
Mvav 148,148,220
X 180
Mvav 116,148,212
Dc 245,8,61,0
Ra 144,242,12288
Mvav 84,148,204
K 195,178,179
Mvav 52,148,196
X 181
Mvav 56,152,196
X 182
Mvav 88,152,204
Dc 245,8,61,1024
Ra 148,242,13312
Mvav 120,152,212
K 202,180,181
Mvav 152,152,220
X 183
Mvav 156,156,220
X 184
Mvav 124,156,212
Dc 245,8,61,2048
Ra 152,242,14336
Mvav 92,156,204
K 203,182,183
Mvav 60,156,196
W 3
X 185
Cvaa 160,128,0,224
X 186
Mvaa 160,132,4
Dc 245,8,61,3072
Ra 156,242,15360
Cvaa 164,132,20,228
s_add_u32 s61, s61, s63
K 210,184,185
Mvaa 164,128,16
X 187
Cvaa 168,128,32,232
X 188
Mvaa 168,132,36
Cvaa 172,132,52,236
s_xor_b32 s62, s62, 0x10000
K 211,186,187
Mvaa 172,128,48
X 189
Mvaa 160,136,8
Ra 128,242,24576
Ra 132,242,25600
Mvaa 160,140,12
s_add_u32 s64, s64, 1
X 190
Mvaa 164,140,28
K 218,188,189
Mvaa 164,136,24
X 191
Mvaa 168,136,40
Mvaa 168,140,44
X 160
Mvaa 172,140,60
K 219,190,191
Mvaa 172,136,56
W 2
X 161
Cvaa 176,144,0,224
Ra 136,242,26624
Ra 140,242,27648
Mvaa 176,148,4
X 162
Cvaa 180,148,20,228
K 196,160,161
Mvaa 180,144,16
X 163
Cvaa 184,144,32,232
Mvaa 184,148,36
X 164
Cvaa 188,148,52,236
K 197,162,163
Mvaa 188,144,48
X 165
Mvaa 176,152,8
Ra 144,242,28672
)"
R"(Ra 148,242,29696
Mvaa 176,156,12
X 166
Mvaa 180,156,28
K 204,164,165
Mvaa 180,152,24
X 167
Mvaa 184,152,40
Mvaa 184,156,44
X 168
Mvaa 188,156,60
Mvaa 188,152,56
W 2
Ra 152,242,30720
K 205,166,167
Mvav 32,128,192
X 169
Mvav 64,128,200
Ra 156,242,31744
Mvav 96,128,208
X 170
Mvav 128,128,216
K 212,168,169
Mvav 132,132,216
X 171
Mvav 100,132,208
X 172
Mvav 68,132,200
Mvav 36,132,192
K 213,170,171
Mvav 40,136,192
X 173
Mvav 72,136,200
X 174
Mvav 104,136,208
Mvav 136,136,216
K 220,172,173
Mvav 140,140,216
X 175
Mvav 108,140,208
X 176
Mvav 76,140,200
s_waitcnt vmcnt(0) lgkmcnt(0)
s_barrier
v_xor_b32 v242, 0x10000, v242
Mvav 44,140,192
Ra 128,242,0
K 221,174,175
Mvav 48,144,192
X 177
Mvav 80,144,200
X 178
Mvav 112,144,208
Ra 132,242,1024
Mvav 144,144,216
K 198,176,177
Mvav 148,148,216
X 179
Mvav 116,148,208
X 180
Mvav 84,148,200
Ra 136,242,2048
Mvav 52,148,192
K 199,178,179
Mvav 56,152,192
X 181
Mvav 88,152,200
Ra 140,242,3072
Mvav 120,152,208
X 182
Mvav 152,152,216
K 206,180,181
Mvav 156,156,216
X 183
Mvav 124,156,208
Mvav 92,156,200
K 207,182,183
Mvav 60,156,192
s_cmp_lg_u32 s64, s66
s_cbranch_scc1 .Ll1_34%=
s_cmp_ge_u32 s14, 2
s_cbranch_scc1 .Lf1r_47%=
.Lsp1_36%=:
W 0
Cvaa 160,128,0,224
Ra 144,242,4096
Ra 148,242,5120
Mvaa 160,132,4
s_add_u32 m0, s62, 0
Ra 152,242,6144
Cvaa 164,132,20,228
X 184
Mvaa 164,128,16
X 185
Cvaa 168,128,32,232
X 186
Mvaa 168,132,36
Ra 156,242,7168
K 214,184,185
Cvaa 172,132,52,236
Dc 1,8,61,0
Mvaa 172,128,48
X 187
Mvaa 160,136,8
X 188
Mvaa 160,140,12
Ra 128,242,16384
K 215,186,187
Mvaa 164,140,28
Dc 1,8,61,1024
Ra 132,242,17408
Mvaa 164,136,24
X 189
Mvaa 168,136,40
X 190
Mvaa 168,140,44
Dc 1,8,61,2048
K 222,188,189
Mvaa 172,140,60
X 191
Mvaa 172,136,56
Ra 136,242,18432
K 223,190,191
v_smfmac_f32_16x16x64_bf16 a[160:163], a[176:179], v[192:199], v240
Dc 1,8,61,3072
Ra 140,242,19456
v_smfmac_f32_16x16x64_bf16 a[164:167], a[176:179], v[200:207], v240
s_add_u32 m0, s62, 4096
v_cndmask_b32_e64 v160, v241, v160, s[72:73]
v_cndmask_b32_e64 v161, v241, v161, s[74:75]
v_smfmac_f32_16x16x64_bf16 a[168:171], a[176:179], v[208:215], v240
v_cndmask_b32_e64 v162, v241, v162, s[76:77]
v_cndmask_b32_e64 v163, v241, v163, s[78:79]
v_smfmac_f32_16x16x64_bf16 a[172:175], a[176:179], v[216:223], v240
W 4
X 160
Cvaa 180,144,16,228
X 161
Mvaa 180,148,20
Dc 243,8,61,0
Cvaa 184,148,36,232
X 162
Mvaa 184,144,32
K 192,160,161
Cvaa 188,144,48,236
X 163
Mvaa 188,148,52
Dc 243,8,61,1024
Ra 144,242,20480
Mvaa 180,152,24
X 164
Mvaa 180,156,28
Ra 148,242,21504
K 193,162,163
Mvaa 184,156,44
Dc 243,8,61,2048
Mvaa 184,152,40
X 165
Mvaa 188,152,56
X 166
Mvaa 188,156,60
W 2
Dc 243,8,61,3072
K 200,164,165
Mvav 32,128,196
s_add_u32 m0, s62, 8192
X 167
Mvav 64,128,204
Ra 152,242,22528
Ra 156,242,23552
Mvav 96,128,212
X 168
Mvav 128,128,220
Dc 244,8,61,0
K 201,166,167
Mvav 132,132,220
X 169
Mvav 100,132,212
X 170
Mvav 68,132,204
Ra 128,242,8192
K 208,168,169
Mvav 36,132,196
X 171
Mvav 40,136,196
Dc 244,8,61,1024
Ra 132,242,9216
Mvav 72,136,204
X 172
Mvav 104,136,212
K 209,170,171
Mvav 136,136,220
X 173
Mvav 140,140,220
Dc 244,8,61,2048
Ra 136,242,10240
Mvav 108,140,212
X 174
Mvav 76,140,204
K 216,172,173
Mvav 44,140,196
W 3
Dc 244,8,61,3072
Ra 140,242,11264
Mvav 48,144,196
s_add_u32 m0, s62, 12288
X 175
Mvav 80,144,204
v_mov_b32 v194, 0
K 217,174,175
Mvav 112,144,212
v_mov_b32 v195, 0
v_cndmask_b32_e64 v180, v241, v180, s[72:73]
Mvav 144,144,220
v_cndmask_b32_e64 v181, v241, v181, s[74:75]
v_cndmask_b32_e64 v182, v241, v182, s[76:77]
Mvav 148,148,220
Dc 245,8,61,0
v_cndmask_b32_e64 v183, v241, v183, s[78:79]
Mvav 116,148,212
X 180
Mvav 84,148,204
Ra 144,242,12288
Mvav 52,148,196
X 181
Mvav 56,152,196
Dc 245,8,61,1024
Ra 148,242,13312
Mvav 88,152,204
X 182
Mvav 120,152,212
K 202,180,181
Mvav 152,152,220
X 183
Mvav 156,156,220
Dc 245,8,61,2048
Ra 152,242,14336
Mvav 124,156,212
X 184
Mvav 92,156,204
K 203,182,183
Mvav 60,156,196
W 3
Dc 245,8,61,3072
Ra 156,242,15360
Cvaa 168,128,32,232
s_add_u32 s61, s61, s63
X 185
Mvaa 168,132,36
s_xor_b32 s62, s62, 0x10000
X 186
Cvaa 172,132,52,236
K 210,184,185
Mvaa 172,128,48
s_add_u32 s64, s64, 1
X 187
Mvaa 168,136,40
X 188
Mvaa 168,140,44
Ra 128,242,24576
K 211,186,187
Mvaa 172,140,60
Ra 132,242,25600
Mvaa 172,136,56
Ra 136,242,26624
Ra 140,242,27648
X 189
X 190
K 218,188,189
X 191
v_mov_b32 v196, 0
K 219,190,191
W 4
Cvaa 188,144,48,236
Ra 144,242,28672
Mvaa 188,148,52
v_mov_b32 v197, 0
v_mov_b32 v204, 0
Mvaa 188,152,56
Ra 148,242,29696
v_mov_b32 v205, 0
Mvaa 188,156,60
W 2
Ra 152,242,30720
v_cndmask_b32_e64 v168, v241, v168, s[72:73]
Mvav 32,128,192
v_cndmask_b32_e64 v169, v241, v169, s[74:75]
v_cndmask_b32_e64 v170, v241, v170, s[76:77]
Mvav 64,128,200
Ra 156,242,31744
v_cndmask_b32_e64 v171, v241, v171, s[78:79]
Mvav 96,128,208
X 168
Mvav 128,128,216
X 169
Mvav 132,132,216
X 170
Mvav 100,132,208
v_mov_b32 v224, v250
Mvav 68,132,200
K 212,168,169
v_mov_b32 v225, v250
Mvav 36,132,192
X 171
Mvav 40,136,192
v_mov_b32 v226, v250
Mvav 72,136,200
X 172
Mvav 104,136,208
v_mov_b32 v227, v250
K 213,170,171
Mvav 136,136,216
X 173
Mvav 140,140,216
v_mov_b32 v228, v251
v_mov_b32 v229, v251
Mvav 108,140,208
X 174
Mvav 76,140,200
K 220,172,173
v_mov_b32 v230, v251
Mvav 44,140,192
W 0
X 175
Mvav 48,144,192
v_mov_b32 v231, v251
v_mov_b32 v198, 0
Mvav 80,144,200
K 221,174,175
v_mov_b32 v232, v248
Mvav 112,144,208
v_mov_b32 v199, 0
v_mov_b32 v206, 0
Mvav 144,144,216
v_mov_b32 v233, v248
v_mov_b32 v207, 0
Mvav 148,148,216
v_mov_b32 v214, 0
v_mov_b32 v234, v248
Mvav 116,148,208
v_mov_b32 v215, 0
v_cndmask_b32_e64 v188, v241, v188, s[72:73]
Mvav 84,148,200
v_mov_b32 v235, v248
v_cndmask_b32_e64 v189, v241, v189, s[74:75]
Mvav 52,148,192
v_cndmask_b32_e64 v190, v241, v190, s[76:77]
v_mov_b32 v236, v249
Mvav 56,152,192
v_cndmask_b32_e64 v191, v241, v191, s[78:79]
v_mov_b32 v237, v249
Mvav 88,152,200
X 188
Mvav 120,152,208
X 189
Mvav 152,152,216
v_mov_b32 v238, v249
v_mov_b32 v239, v249
Mvav 156,156,216
X 190
Mvav 124,156,208
s_waitcnt vmcnt(0)
s_barrier
v_xor_b32 v242, 0x10000, v242
Mvav 92,156,200
Ra 128,242,40960
K 222,188,189
Mvav 60,156,192
Ra 132,242,41984
Ra 136,242,43008
Ra 140,242,44032
X 191
s_nop 0
K 223,190,191
s_cmp_eq_u32 s64, 15
s_cbranch_scc1 .Lfs_42%=
.Lsp2_37%=:
v_smfmac_f32_16x16x64_bf16 a[160:163], a[176:179], v[192:199], v240
s_add_u32 m0, s62, 0
Ra 144,242,45056
Ra 148,242,46080
Ra 152,242,47104
Ra 156,242,48128
v_smfmac_f32_16x16x64_bf16 a[164:167], a[176:179], v[200:207], v240
v_smfmac_f32_16x16x64_bf16 a[168:171], a[176:179], v[208:215], v240
v_smfmac_f32_16x16x64_bf16 a[172:175], a[176:179], v[216:223], v240
W 4
Dc 1,8,61,0
Cvaa 168,128,96,232
v_mov_b32 v192, 0
G 12,160
Mvaa 168,132,100
Dc 1,8,61,1024
G 4,161
A 12,12,4
v_cmp_nlt_f32_e64 s[86:87], v12, s90
v_mov_b32 v193, 0
Cvaa 172,132,116,236
v_rcp_f32 v12, v12
s_or_b64 s[88:89], s[88:89], s[86:87]
)"
R"(v_accvgpr_write_b32 a160, 0
v_mov_b32 v200, 0
v_accvgpr_write_b32 a161, 0
v_accvgpr_write_b32 a162, 0
Mvaa 172,128,112
Dc 1,8,61,2048
v_accvgpr_write_b32 a163, 0
G 13,164
v_mov_b32 v201, 0
G 5,165
A 13,13,5
Mvaa 168,136,104
Ra 128,242,16384
Ra 132,242,17408
v_cmp_nlt_f32_e64 s[86:87], v13, s90
v_rcp_f32 v13, v13
s_or_b64 s[88:89], s[88:89], s[86:87]
v_accvgpr_write_b32 a164, 0
Mvaa 168,140,108
Dc 1,8,61,3072
v_accvgpr_write_b32 a165, 0
v_accvgpr_write_b32 a166, 0
v_accvgpr_write_b32 a167, 0
U 32,32,12
U 33,33,12
Mvaa 172,140,124
U 34,34,12
U 35,35,12
U 36,36,12
U 37,37,12
U 38,38,12
U 39,39,12
Mvaa 172,136,120
s_add_u32 m0, s62, 4096
Ra 136,242,18432
Ra 140,242,19456
K 32,32,33
K 33,34,35
K 34,36,37
W 4
Cvaa 188,144,112,236
K 35,38,39
s_add_u32 s93, s67, 0
buffer_store_dwordx4 v[32:35], v1, s[8:11], s93 offen offset:0 sc1
U 40,40,12
U 41,41,12
U 42,42,12
U 43,43,12
Mvaa 188,148,116
Ra 144,242,20480
v_cndmask_b32_e64 v168, v241, v168, s[72:73]
v_cndmask_b32_e64 v169, v241, v169, s[74:75]
v_cndmask_b32_e64 v170, v241, v170, s[76:77]
v_cndmask_b32_e64 v171, v241, v171, s[78:79]
U 44,44,12
Mvaa 188,152,120
X 168
X 169
U 45,45,12
U 46,46,12
Mvaa 188,156,124
W 1
Ra 148,242,21504
Dc 243,8,61,0
U 47,47,12
K 40,40,41
K 41,42,43
K 42,44,45
Mvav 96,128,212
Ra 152,242,22528
Ra 156,242,23552
K 43,46,47
X 170
K 208,168,169
Mvav 128,128,220
buffer_store_dwordx4 v[40:43], v1, s[8:11], s93 offen offset:1024 sc1
U 48,48,12
U 49,49,12
U 50,50,12
U 51,51,12
U 52,52,12
Mvav 132,132,220
U 53,53,12
U 54,54,12
U 55,55,12
K 48,48,49
K 49,50,51
K 50,52,53
Mvav 100,132,212
Dc 243,8,61,1024
Ra 128,242,32768
Ra 132,242,33792
X 171
K 51,54,55
Mvav 104,136,212
X 172
K 209,170,171
X 173
Mvav 136,136,220
buffer_store_dwordx4 v[48:51], v1, s[8:11], s93 offen offset:2048 sc1
U 56,56,12
U 57,57,12
U 58,58,12
U 59,59,12
U 60,60,12
Mvav 140,140,220
U 61,61,12
U 62,62,12
U 63,63,12
K 56,56,57
K 57,58,59
K 58,60,61
Mvav 108,140,212
W 2
Dc 243,8,61,2048
Ra 136,242,34816
Ra 140,242,35840
K 59,62,63
X 174
Mvav 112,144,212
K 216,172,173
X 175
Mvav 144,144,220
buffer_store_dwordx4 v[56:59], v1, s[8:11], s93 offen offset:3072 sc1
Ra 144,242,36864
G 14,168
G 6,169
A 14,14,6
v_cmp_nlt_f32_e64 s[86:87], v14, s90
Mvav 148,148,220
v_rcp_f32 v14, v14
s_or_b64 s[88:89], s[88:89], s[86:87]
v_accvgpr_write_b32 a168, 0
v_accvgpr_write_b32 a169, 0
v_accvgpr_write_b32 a170, 0
v_accvgpr_write_b32 a171, 0
Mvav 116,148,212
Dc 243,8,61,3072
s_add_u32 m0, s62, 8192
G 15,172
G 7,173
A 15,15,7
v_cmp_nlt_f32_e64 s[86:87], v15, s90
v_mov_b32 v194, 0
Mvav 120,152,212
v_rcp_f32 v15, v15
s_or_b64 s[88:89], s[88:89], s[86:87]
v_accvgpr_write_b32 a172, 0
v_accvgpr_write_b32 a173, 0
v_accvgpr_write_b32 a174, 0
v_accvgpr_write_b32 a175, 0
Mvav 152,152,220
Dc 244,8,61,0
U 64,64,13
U 65,65,13
U 66,66,13
U 67,67,13
U 68,68,13
Mvav 156,156,220
U 69,69,13
U 70,70,13
U 71,71,13
K 64,64,65
K 65,66,67
K 66,68,69
Mvav 124,156,212
W 1
Dc 244,8,61,1024
Ra 148,242,37888
K 67,70,71
s_add_u32 s93, s67, 131072
K 217,174,175
v_mov_b32 v195, 0
Cvaa 160,128,64,224
Ra 152,242,38912
Ra 156,242,39936
v_mov_b32 v202, 0
v_mov_b32 v203, 0
v_mov_b32 v210, 0
v_mov_b32 v211, 0
Mvaa 160,132,68
buffer_store_dwordx4 v[64:67], v1, s[8:11], s93 offen offset:0 sc1
U 72,72,13
U 73,73,13
U 74,74,13
U 75,75,13
U 76,76,13
Cvaa 164,132,84,228
U 77,77,13
U 78,78,13
U 79,79,13
K 72,72,73
K 73,74,75
K 74,76,77
Mvaa 164,128,80
Dc 244,8,61,2048
K 75,78,79
v_cndmask_b32_e64 v188, v241, v188, s[72:73]
v_cndmask_b32_e64 v189, v241, v189, s[74:75]
v_cndmask_b32_e64 v190, v241, v190, s[76:77]
v_cndmask_b32_e64 v191, v241, v191, s[78:79]
Cvaa 168,128,96,232
X 188
X 189
Mvaa 168,132,100
buffer_store_dwordx4 v[72:75], v1, s[8:11], s93 offen offset:1024 sc1
U 80,80,13
U 81,81,13
U 82,82,13
U 83,83,13
U 84,84,13
Cvaa 172,132,116,236
U 85,85,13
U 86,86,13
U 87,87,13
K 80,80,81
K 81,82,83
K 82,84,85
Mvaa 172,128,112
Dc 244,8,61,3072
s_add_u32 m0, s62, 12288
Ra 128,242,49152
Ra 132,242,50176
K 83,86,87
X 190
Mvaa 160,136,72
K 218,188,189
X 191
Mvaa 160,140,76
buffer_store_dwordx4 v[80:83], v1, s[8:11], s93 offen offset:2048 sc1
U 88,88,13
U 89,89,13
U 90,90,13
U 91,91,13
U 92,92,13
Mvaa 164,140,92
U 93,93,13
U 94,94,13
U 95,95,13
K 88,88,89
K 89,90,91
K 90,92,93
Mvaa 164,136,88
K 91,94,95
buffer_store_dwordx4 v[88:91], v1, s[8:11], s93 offen offset:3072 sc1
U 96,96,14
U 97,97,14
U 98,98,14
U 99,99,14
Mvaa 168,136,104
U 100,100,14
U 101,101,14
U 102,102,14
U 103,103,14
K 96,96,97
K 97,98,99
Mvaa 168,140,108
Dc 245,8,61,0
K 98,100,101
K 99,102,103
s_add_u32 s93, s67, 262144
v_cndmask_b32_e64 v160, v241, v160, s[72:73]
K 219,190,191
v_cndmask_b32_e64 v161, v241, v161, s[74:75]
Mvaa 172,140,124
v_cndmask_b32_e64 v162, v241, v162, s[76:77]
v_cndmask_b32_e64 v163, v241, v163, s[78:79]
X 160
Mvaa 172,136,120
W 2
Dc 245,8,61,1024
Ra 136,242,51200
Ra 140,242,52224
X 161
Cvaa 180,144,80,228
Mvaa 180,148,84
buffer_store_dwordx4 v[96:99], v1, s[8:11], s93 offen offset:0 sc1
U 104,104,14
U 105,105,14
U 106,106,14
U 107,107,14
U 108,108,14
Cvaa 184,148,100,232
U 109,109,14
U 110,110,14
U 111,111,14
K 104,104,105
K 105,106,107
K 106,108,109
Mvaa 184,144,96
Dc 245,8,61,2048
K 107,110,111
X 162
K 196,160,161
Cvaa 188,144,112,236
Ra 144,242,53248
X 163
Mvaa 188,148,116
buffer_store_dwordx4 v[104:107], v1, s[8:11], s93 offen offset:1024 sc1
U 112,112,14
U 113,113,14
U 114,114,14
U 115,115,14
U 116,116,14
Mvaa 180,152,88
U 117,117,14
U 118,118,14
U 119,119,14
K 112,112,113
K 113,114,115
K 114,116,117
Mvaa 180,156,92
Dc 245,8,61,3072
s_add_u32 s61, s61, s63
s_xor_b32 s62, s62, 0x10000
s_add_u32 s64, s64, 1
K 115,118,119
X 164
Mvaa 184,156,108
Ra 148,242,54272
K 197,162,163
X 165
Mvaa 184,152,104
buffer_store_dwordx4 v[112:115], v1, s[8:11], s93 offen offset:2048 sc1
U 120,120,14
U 121,121,14
U 122,122,14
U 123,123,14
U 124,124,14
Mvaa 188,152,120
U 125,125,14
U 126,126,14
U 127,127,14
K 120,120,121
K 121,122,123
K 122,124,125
Mvaa 188,156,124
W 2
K 123,126,127
buffer_store_dwordx4 v[120:123], v1, s[8:11], s93 offen offset:3072 sc1
U 128,128,15
U 129,129,15
U 130,130,15
U 131,131,15
Zvav 32,128,192
U 132,132,15
U 133,133,15
U 134,134,15
U 135,135,15
K 128,128,129
K 129,130,131
Zvav 64,128,200
K 130,132,133
K 131,134,135
s_add_u32 s93, s67, 393216
buffer_store_dwordx4 v[128:131], v1, s[8:11], s93 offen offset:0 sc1
U 136,136,15
U 137,137,15
U 138,138,15
Zvav 96,128,208
U 139,139,15
U 140,140,15
U 141,141,15
U 142,142,15
U 143,143,15
K 136,136,137
Zvav 128,128,216
K 137,138,139
K 138,140,141
K 139,142,143
buffer_store_dwordx4 v[136:139], v1, s[8:11], s93 offen offset:1024 sc1
U 144,144,15
U 145,145,15
Zvav 132,132,216
U 146,146,15
U 147,147,15
U 148,148,15
U 149,149,15
U 150,150,15
U 151,151,15
Zvav 100,132,208
K 144,144,145
K 145,146,147
K 146,148,149
K 147,150,151
buffer_store_dwordx4 v[144:147], v1, s[8:11], s93 offen offset:2048 sc1
U 152,152,15
Zvav 68,132,200
U 153,153,15
U 154,154,15
U 155,155,15
U 156,156,15
U 157,157,15
U 158,158,15
Zvav 36,132,192
U 159,159,15
K 152,152,153
K 153,154,155
K 154,156,157
K 155,158,159
)"
R"(buffer_store_dwordx4 v[152:155], v1, s[8:11], s93 offen offset:3072 sc1
Zvav 40,136,192
Ra 152,242,55296
X 166
K 204,164,165
X 167
Zvav 72,136,200
Ra 156,242,56320
X 168
K 205,166,167
X 169
Zvav 104,136,208
X 170
K 212,168,169
X 171
Zvav 136,136,216
X 172
K 213,170,171
X 173
Zvav 140,140,216
Zvav 108,140,208
X 174
Zvav 76,140,200
s_waitcnt vmcnt(6) lgkmcnt(0)
s_barrier
v_xor_b32 v242, 0x10000, v242
Zvav 44,140,192
Ra 128,242,40960
K 220,172,173
X 175
Zvav 48,144,192
Zvav 80,144,200
v_mov_b32 v198, 0
Zvav 112,144,208
Ra 132,242,41984
K 221,174,175
Zvav 144,144,216
v_mov_b32 v199, 0
v_cndmask_b32_e64 v180, v241, v180, s[72:73]
Zvav 148,148,216
v_cndmask_b32_e64 v181, v241, v181, s[74:75]
Zvav 116,148,208
Ra 136,242,43008
v_cndmask_b32_e64 v182, v241, v182, s[76:77]
Zvav 84,148,200
v_cndmask_b32_e64 v183, v241, v183, s[78:79]
Zvav 52,148,192
X 180
Zvav 56,152,192
Ra 140,242,44032
X 181
Zvav 88,152,200
Zvav 120,152,208
X 182
Zvav 152,152,216
K 206,180,181
Zvav 156,156,216
X 183
Zvav 124,156,208
Zvav 92,156,200
s_waitcnt vmcnt(0)
s_and_b64 s[86:87], s[88:89], s[24:25]
s_cbranch_scc1 .LL_49%=
v_mov_b64 v[28:29], s[12:13]
s_mov_b64 exec, 1
buffer_store_dwordx2 v[28:29], off, s[8:11], s69 sc1
s_mov_b64 exec, -1
.LL_49%=:
K 207,182,183
Zvav 60,156,192
s_cmp_eq_u32 s64, 15
s_cbranch_scc1 .Lf2_41%=
.Ll2_38%=:
W 0
Cvaa 160,128,64,224
Ra 144,242,45056
Ra 148,242,46080
Mvaa 160,132,68
s_add_u32 m0, s62, 0
Ra 152,242,47104
Cvaa 164,132,84,228
X 184
Mvaa 164,128,80
X 185
Cvaa 168,128,96,232
X 186
Mvaa 168,132,100
Ra 156,242,48128
K 214,184,185
Cvaa 172,132,116,236
Dc 1,8,61,0
Mvaa 172,128,112
X 187
Mvaa 160,136,72
X 188
Mvaa 160,140,76
Ra 128,242,57344
K 215,186,187
Mvaa 164,140,92
Dc 1,8,61,1024
Mvaa 164,136,88
X 189
Mvaa 168,136,104
X 190
Mvaa 168,140,108
Ra 132,242,58368
K 222,188,189
Mvaa 172,140,124
Dc 1,8,61,2048
Mvaa 172,136,120
X 191
v_smfmac_f32_16x16x64_bf16 a[160:163], a[176:179], v[192:199], v240
Ra 136,242,59392
K 223,190,191
v_smfmac_f32_16x16x64_bf16 a[164:167], a[176:179], v[200:207], v240
X 160
v_smfmac_f32_16x16x64_bf16 a[168:171], a[176:179], v[208:215], v240
Dc 1,8,61,3072
v_smfmac_f32_16x16x64_bf16 a[172:175], a[176:179], v[216:223], v240
W 3
X 161
Cvaa 176,144,64,224
s_add_u32 m0, s62, 4096
X 162
Mvaa 176,148,68
Ra 140,242,60416
K 192,160,161
Cvaa 180,148,84,228
X 163
Mvaa 180,144,80
Dc 243,8,61,0
Cvaa 184,144,96,232
X 164
Mvaa 184,148,100
K 193,162,163
Cvaa 188,148,116,236
X 165
Mvaa 188,144,112
Dc 243,8,61,1024
Ra 144,242,61440
Mvaa 176,152,72
X 166
Mvaa 176,156,76
Ra 148,242,62464
K 200,164,165
Mvaa 180,156,92
X 167
Mvaa 180,152,88
Dc 243,8,61,2048
Mvaa 184,152,104
X 168
Mvaa 184,156,108
K 201,166,167
Mvaa 188,156,124
X 169
Mvaa 188,152,120
W 2
Dc 243,8,61,3072
Ra 152,242,63488
Mvav 32,128,196
s_add_u32 m0, s62, 8192
X 170
Mvav 64,128,204
Ra 156,242,64512
K 208,168,169
Mvav 96,128,212
X 171
Mvav 128,128,220
Dc 244,8,61,0
Mvav 132,132,220
X 172
Mvav 100,132,212
Ra 128,242,32768
K 209,170,171
Mvav 68,132,204
X 173
Mvav 36,132,196
Dc 244,8,61,1024
Mvav 40,136,196
X 174
Mvav 72,136,204
Ra 132,242,33792
K 216,172,173
Mvav 104,136,212
X 175
Mvav 136,136,220
Dc 244,8,61,2048
Mvav 140,140,220
X 176
Mvav 108,140,212
Ra 136,242,34816
K 217,174,175
Mvav 76,140,204
X 177
Mvav 44,140,196
W 3
Dc 244,8,61,3072
Mvav 48,144,196
X 178
Mvav 80,144,204
s_add_u32 m0, s62, 12288
Ra 140,242,35840
Mvav 112,144,212
K 194,176,177
Mvav 144,144,220
X 179
Mvav 148,148,220
X 180
Mvav 116,148,212
Dc 245,8,61,0
Ra 144,242,36864
Mvav 84,148,204
K 195,178,179
Mvav 52,148,196
X 181
Mvav 56,152,196
X 182
Mvav 88,152,204
Dc 245,8,61,1024
Ra 148,242,37888
Mvav 120,152,212
K 202,180,181
Mvav 152,152,220
X 183
Mvav 156,156,220
X 184
Mvav 124,156,212
Dc 245,8,61,2048
Ra 152,242,38912
Mvav 92,156,204
K 203,182,183
Mvav 60,156,196
W 3
X 185
Cvaa 160,128,64,224
X 186
Mvaa 160,132,68
Dc 245,8,61,3072
Ra 156,242,39936
Cvaa 164,132,84,228
s_add_u32 s61, s61, s63
K 210,184,185
Mvaa 164,128,80
X 187
Cvaa 168,128,96,232
X 188
Mvaa 168,132,100
Cvaa 172,132,116,236
s_xor_b32 s62, s62, 0x10000
K 211,186,187
Mvaa 172,128,112
X 189
Mvaa 160,136,72
Ra 128,242,49152
Ra 132,242,50176
Mvaa 160,140,76
s_add_u32 s64, s64, 1
X 190
Mvaa 164,140,92
K 218,188,189
Mvaa 164,136,88
X 191
Mvaa 168,136,104
Mvaa 168,140,108
X 160
Mvaa 172,140,124
K 219,190,191
Mvaa 172,136,120
W 2
X 161
Cvaa 176,144,64,224
Ra 136,242,51200
Ra 140,242,52224
Mvaa 176,148,68
X 162
Cvaa 180,148,84,228
K 196,160,161
Mvaa 180,144,80
X 163
Cvaa 184,144,96,232
Mvaa 184,148,100
X 164
Cvaa 188,148,116,236
K 197,162,163
Mvaa 188,144,112
X 165
Mvaa 176,152,72
Ra 144,242,53248
Ra 148,242,54272
Mvaa 176,156,76
X 166
Mvaa 180,156,92
K 204,164,165
Mvaa 180,152,88
X 167
Mvaa 184,152,104
Mvaa 184,156,108
X 168
Mvaa 188,156,124
Mvaa 188,152,120
W 2
Ra 152,242,55296
K 205,166,167
Mvav 32,128,192
X 169
Mvav 64,128,200
Ra 156,242,56320
Mvav 96,128,208
X 170
Mvav 128,128,216
K 212,168,169
Mvav 132,132,216
X 171
Mvav 100,132,208
X 172
Mvav 68,132,200
Mvav 36,132,192
K 213,170,171
Mvav 40,136,192
X 173
Mvav 72,136,200
X 174
Mvav 104,136,208
Mvav 136,136,216
K 220,172,173
Mvav 140,140,216
X 175
Mvav 108,140,208
X 176
Mvav 76,140,200
s_waitcnt vmcnt(0) lgkmcnt(0)
s_barrier
v_xor_b32 v242, 0x10000, v242
Mvav 44,140,192
Ra 128,242,40960
K 221,174,175
Mvav 48,144,192
X 177
Mvav 80,144,200
X 178
Mvav 112,144,208
Ra 132,242,41984
Mvav 144,144,216
K 198,176,177
Mvav 148,148,216
X 179
Mvav 116,148,208
X 180
Mvav 84,148,200
Ra 136,242,43008
Mvav 52,148,192
K 199,178,179
Mvav 56,152,192
X 181
Mvav 88,152,200
Ra 140,242,44032
Mvav 120,152,208
X 182
Mvav 152,152,216
K 206,180,181
Mvav 156,156,216
X 183
Mvav 124,156,208
Mvav 92,156,200
K 207,182,183
Mvav 60,156,192
s_cmp_lg_u32 s64, 15
s_cbranch_scc1 .Ll2_38%=
.Lf2_41%=:
W 0
Cvaa 160,128,64,224
v_bfe_u32 v28, v1, 4, 2
v_lshlrev_b32 v28, 3, v28
Mvaa 160,132,68
Ra 144,242,45056
Ra 148,242,46080
Cvaa 164,132,84,228
Ra 152,242,47104
buffer_load_dwordx2 v[28:29], v28, s[8:11], s54 offen sc1
Mvaa 164,128,80
s_lshl_b32 s92, s58, 2
s_add_u32 s91, s92, s51
s_add_u32 s95, s91, 3
Cvaa 168,128,96,232
s_and_b32 s95, s95, 15
s_mul_i32 s95, s95, 40960
s_mul_i32 s71, s51, 10240
Mvaa 168,132,100
s_add_u32 s71, s71, 0
s_add_u32 m0, s71, s48
s_lshl2_add_u32 s94, s48, s95
Cvaa 172,132,116,236
X 184
Mvaa 172,128,112
X 185
Mvaa 160,136,72
X 186
Mvaa 160,140,76
Ra 156,242,48128
K 214,184,185
)"
R"(Mvaa 164,140,92
X 187
Mvaa 164,136,88
D 16,40,94,0
Ra 128,242,57344
Mvaa 168,136,104
s_add_u32 s95, s91, 5
s_and_b32 s95, s95, 15
s_mul_i32 s95, s95, 40960
Mvaa 168,140,108
s_mul_i32 s71, s51, 10240
s_add_u32 s71, s71, 20480
Ra 132,242,58368
Mvaa 172,140,124
X 188
Mvaa 172,136,120
D 17,40,94,1024
K 215,186,187
v_smfmac_f32_16x16x64_bf16 a[160:163], a[176:179], v[192:199], v240
X 189
v_smfmac_f32_16x16x64_bf16 a[164:167], a[176:179], v[200:207], v240
X 190
K 222,188,189
X 191
s_nop 0
K 223,190,191
v_smfmac_f32_16x16x64_bf16 a[168:171], a[176:179], v[208:215], v240
D 18,40,94,2048
Ra 136,242,59392
v_smfmac_f32_16x16x64_bf16 a[172:175], a[176:179], v[216:223], v240
W 3
X 160
Cvaa 176,144,64,224
X 161
Mvaa 176,148,68
X 162
Cvaa 180,148,84,228
D 19,40,94,3072
K 192,160,161
Mvaa 180,144,80
s_add_u32 m0, m0, 4096
X 163
Cvaa 184,144,96,232
X 164
Mvaa 184,148,100
Ra 140,242,60416
K 193,162,163
Cvaa 188,148,116,236
buffer_load_dwordx4 v20, s[40:43], s94 offen lds
Mvaa 188,144,112
s_add_u32 m0, s71, s48
s_lshl2_add_u32 s94, s48, s95
Ra 144,242,61440
Mvaa 176,152,72
X 165
Mvaa 176,156,76
X 166
Mvaa 180,156,92
D 16,40,94,0
K 200,164,165
Mvaa 180,152,88
X 167
Mvaa 184,152,104
X 172
Mvaa 184,156,108
D 17,40,94,1024
K 201,166,167
Mvaa 188,156,124
X 173
Mvaa 188,152,120
W 1
Ra 148,242,62464
Ra 152,242,63488
Mvav 32,128,196
X 174
Mvav 64,128,204
D 18,40,94,2048
K 216,172,173
Mvav 96,128,212
X 175
Mvav 128,128,220
X 168
Mvav 132,132,220
s_mov_b32 s18, 40960
D 19,40,94,3072
K 217,174,175
Mvav 100,132,212
s_add_u32 m0, m0, 4096
X 169
Mvav 68,132,204
s_mov_b32 s19, 73728
X 170
Mvav 36,132,196
s_bitcmp1_b32 s14, 0
s_cselect_b32 s18, 49152, s18
s_cselect_b32 s19, 98304, s19
Ra 156,242,64512
K 208,168,169
Mvav 40,136,196
s_cmp_lt_u32 s14, 2
s_cselect_b32 s20, s18, s19
s_cselect_b32 s21, s19, s18
X 171
Mvav 72,136,204
X 184
Mvav 104,136,212
buffer_load_dwordx4 v20, s[40:43], s94 offen lds
K 209,170,171
Mvav 136,136,220
s_waitcnt vmcnt(10)
v_cmp_eq_u64 vcc, s[12:13], v[28:29]
s_cmp_eq_u64 vcc, -1
s_cselect_b32 s46, 79691776, 0
s_add_u32 s81, s58, 0
s_and_b32 s81, s81, 3
Mvav 140,140,220
s_lshl_b32 s81, s81, 12
s_add_u32 s81, s81, s49
Ra 128,242,49152
Mvav 108,140,212
X 185
Mvav 76,140,204
X 186
Mvav 44,140,196
W 1
buffer_load_dwordx4 a[184:187], v1, s[44:47], s81 offen offset:0 sc1
K 210,184,185
Mvav 48,144,196
s_add_u32 s82, s81, 131072
X 187
Mvav 80,144,204
v_add_u32 v2, s20, v1
v_add_u32 v3, s21, v1
Mvav 112,144,212
s_lshl_b32 s18, s14, 9
X 188
Mvav 144,144,220
Ra 132,242,50176
K 211,186,187
Mvav 148,148,220
buffer_load_dwordx4 a[200:203], v1, s[44:47], s82 offen offset:0 sc1
Ra 136,242,51200
Mvav 116,148,212
X 189
Mvav 84,148,204
v_lshrrev_b32 v232, 1, v1
v_add_u32 v232, s18, v232
Mvav 52,148,196
X 190
Mvav 56,152,196
K 218,188,189
v_bfe_u32 v12, v1, 6, 2
Mvav 88,152,204
buffer_load_dwordx4 a[188:191], v1, s[44:47], s81 offen offset:1024 sc1
Ra 140,242,52224
Mvav 120,152,212
X 191
Mvav 152,152,220
v_lshrrev_b32 v13, 8, v1
v_bfe_u32 v14, v1, 4, 1
Mvav 156,156,220
X 176
Mvav 124,156,212
K 219,190,191
v_lshlrev_b32 v14, 4, v14
Mvav 92,156,204
X 177
Mvav 60,156,196
buffer_load_dwordx4 a[204:207], v1, s[44:47], s82 offen offset:1024 sc1
v_mov_b32 v15, 0x3f80
Maav 168,180,208
X 178
K 194,176,177
X 179
X 180
K 195,178,179
Maav 172,180,216
X 181
X 182
K 202,180,181
X 183
s_nop 0
K 203,182,183
Maav 160,180,192
Ra 144,242,53248
v_lshlrev_b32 v15, v14, v15
Maav 164,180,200
W 1
buffer_load_dwordx4 a[192:195], v1, s[44:47], s81 offen offset:2048 sc1
v_bfe_u32 v14, v1, 5, 1
Mvav 96,128,208
Ra 148,242,54272
v_xor_b32 v27, v12, v13
v_lshl_or_b32 v27, v27, 1, v14
Mvav 128,128,216
v_cmp_eq_u32_e64 s[86:87], 0, v27
v_mov_b32 v226, 0
s_nop 0
v_cndmask_b32_e64 v224, 0, v15, s[86:87]
Mvav 132,132,216
Ra 152,242,55296
Ra 156,242,56320
buffer_load_dwordx4 a[208:211], v1, s[44:47], s82 offen offset:2048 sc1
Mvav 100,132,208
v_mov_b32 v228, 0
v_mov_b32 v230, v224
v_cmp_eq_u32_e64 s[86:87], 1, v27
Mvav 104,136,208
v_mov_b32 v227, 0
Mvav 136,136,216
buffer_load_dwordx4 a[196:199], v1, s[44:47], s81 offen offset:3072 sc1
v_cndmask_b32_e64 v225, 0, v15, s[86:87]
v_mov_b32 v229, 0
Mvav 140,140,216
v_mov_b32 v231, v225
G 4,160
G 8,161
Mvav 108,140,208
W 0
G 5,164
G 9,165
G 6,168
Mvav 112,144,208
buffer_load_dwordx4 a[212:215], v1, s[44:47], s82 offen offset:3072 sc1
s_add_u32 s81, s58, 1
s_and_b32 s81, s81, 3
s_lshl_b32 s81, s81, 12
Mvav 144,144,216
s_add_u32 s81, s81, s49
G 10,169
G 7,172
G 11,173
Mvav 148,148,216
A 4,4,8
A 5,5,9
A 6,6,10
Mvav 116,148,208
buffer_load_dwordx4 a[216:219], v1, s[44:47], s81 offen offset:0 sc1
s_add_u32 s82, s81, 131072
A 7,7,11
ds_write_b64 v232, v[6:7] offset:57344
Mvav 120,152,208
A 8,4,5
A 9,6,7
A 8,8,9
Mvav 152,152,216
v_cmp_ngt_f32_e64 s[86:87], s90, v8
s_lshl_b32 s19, s14, 2
v_mov_b32 v9, s19
s_or_b64 s[88:89], s[88:89], s[86:87]
Mvav 156,156,216
s_and_b64 s[88:89], s[88:89], s[24:25]
s_cselect_b32 s18, 1, 0
buffer_load_dwordx4 a[232:235], v1, s[44:47], s82 offen offset:0 sc1
v_mov_b32 v8, s18
ds_write_b32 v9, v8 offset:59392
Mvav 124,156,208
v_xor_b32 v10, 1024, v232
v_bfe_u32 v11, v1, 2, 4
K 96,96,97
Mvav 32,128,192
K 97,98,99
K 98,100,101
K 99,102,103
Mvav 64,128,200
ds_write_b128 v2, v[96:99] offset:0
K 128,128,129
K 129,130,131
Mvav 68,132,200
K 130,132,133
K 131,134,135
ds_write_b128 v2, v[128:131] offset:4096
Mvav 36,132,192
buffer_load_dwordx4 a[220:223], v1, s[44:47], s81 offen offset:1024 sc1
K 104,104,105
K 105,106,107
Mvav 40,136,192
K 106,108,109
K 107,110,111
ds_write_b128 v2, v[104:107] offset:1024
Mvav 72,136,200
K 136,136,137
K 137,138,139
K 138,140,141
Mvav 76,140,200
buffer_load_dwordx4 a[236:239], v1, s[44:47], s82 offen offset:1024 sc1
K 139,142,143
ds_write_b128 v2, v[136:139] offset:5120
Mvav 44,140,192
K 112,112,113
K 113,114,115
K 114,116,117
Mvav 48,144,192
K 115,118,119
ds_write_b128 v2, v[112:115] offset:2048
K 144,144,145
Mvav 80,144,200
K 145,146,147
K 146,148,149
K 147,150,151
Mvav 84,148,200
ds_write_b128 v2, v[144:147] offset:6144
buffer_load_dwordx4 a[224:227], v1, s[44:47], s81 offen offset:2048 sc1
K 120,120,121
Mvav 52,148,192
K 121,122,123
K 122,124,125
K 123,126,127
Mvav 56,152,192
ds_write_b128 v2, v[120:123] offset:3072
K 152,152,153
K 153,154,155
Mvav 88,152,200
buffer_load_dwordx4 a[240:243], v1, s[44:47], s82 offen offset:2048 sc1
K 154,156,157
K 155,158,159
Mvav 92,156,200
ds_write_b128 v2, v[152:155] offset:7168
Mvav 60,156,192
buffer_load_dwordx4 a[228:231], v1, s[44:47], s81 offen offset:3072 sc1
buffer_load_dwordx4 a[244:247], v1, s[44:47], s82 offen offset:3072 sc1
s_branch .Lend_43%=
.p2align 12
.Lfs_42%=:
v_smfmac_f32_16x16x64_bf16 a[160:163], a[176:179], v[192:199], v240
s_mov_b32 s46, 0
s_lshl_b32 s92, s58, 2
s_add_u32 s91, s92, s51
s_add_u32 s95, s91, 3
s_and_b32 s95, s95, 15
s_mul_i32 s95, s95, 40960
s_mul_i32 s71, s51, 10240
v_smfmac_f32_16x16x64_bf16 a[164:167], a[176:179], v[200:207], v240
s_add_u32 s71, s71, 0
s_add_u32 m0, s71, s48
s_lshl2_add_u32 s94, s48, s95
Ra 144,242,45056
Ra 148,242,46080
Ra 152,242,47104
Ra 156,242,48128
v_smfmac_f32_16x16x64_bf16 a[168:171], a[176:179], v[208:215], v240
D 16,40,94,0
s_add_u32 s95, s91, 5
s_and_b32 s95, s95, 15
s_mul_i32 s95, s95, 40960
s_mul_i32 s71, s51, 10240
s_add_u32 s71, s71, 20480
v_smfmac_f32_16x16x64_bf16 a[172:175], a[176:179], v[216:223], v240
W 4
Cvaa 168,128,96,232
v_mov_b32 v192, 0
G 12,160
Mvaa 168,132,100
D 17,40,94,1024
G 4,161
A 12,12,4
v_cmp_nlt_f32_e64 s[86:87], v12, s90
v_rcp_f32 v12, v12
s_or_b64 s[88:89], s[88:89], s[86:87]
Cvaa 172,132,116,236
v_accvgpr_write_b32 a160, 0
v_accvgpr_write_b32 a161, 0
v_accvgpr_write_b32 a162, 0
v_accvgpr_write_b32 a163, 0
G 13,164
G 5,165
Mvaa 172,128,112
v_mov_b32 v193, 0
A 13,13,5
v_cmp_nlt_f32_e64 s[86:87], v13, s90
v_rcp_f32 v13, v13
s_or_b64 s[88:89], s[88:89], s[86:87]
v_accvgpr_write_b32 a164, 0
Mvaa 168,136,104
v_accvgpr_write_b32 a165, 0
v_accvgpr_write_b32 a166, 0
v_accvgpr_write_b32 a167, 0
U 32,32,12
U 33,33,12
U 34,34,12
Mvaa 168,140,108
v_mov_b32 v200, 0
U 35,35,12
U 36,36,12
U 37,37,12
U 38,38,12
U 39,39,12
Mvaa 172,140,124
Ra 128,242,16384
Ra 132,242,17408
K 32,32,33
K 33,34,35
K 34,36,37
K 35,38,39
s_add_u32 s93, s67, 0
Mvaa 172,136,120
Ra 136,242,18432
Ra 140,242,19456
D 18,40,94,2048
v_mov_b32 v201, 0
)"
R"(W 4
Cvaa 188,144,112,236
Ra 144,242,20480
v_cndmask_b32_e64 v168, v241, v168, s[72:73]
v_cndmask_b32_e64 v169, v241, v169, s[74:75]
v_cndmask_b32_e64 v170, v241, v170, s[76:77]
Mvaa 188,148,116
buffer_store_dwordx4 v[32:35], v1, s[8:11], s93 offen offset:0 sc1
U 40,40,12
U 41,41,12
U 42,42,12
U 43,43,12
U 44,44,12
Mvaa 188,152,120
U 45,45,12
U 46,46,12
U 47,47,12
K 40,40,41
K 41,42,43
K 42,44,45
Mvaa 188,156,124
W 1
K 43,46,47
buffer_store_dwordx4 v[40:43], v1, s[8:11], s93 offen offset:1024 sc1
U 48,48,12
U 49,49,12
U 50,50,12
U 51,51,12
Mvav 96,128,212
U 52,52,12
U 53,53,12
U 54,54,12
U 55,55,12
K 48,48,49
K 49,50,51
Mvav 128,128,220
K 50,52,53
K 51,54,55
buffer_store_dwordx4 v[48:51], v1, s[8:11], s93 offen offset:2048 sc1
U 56,56,12
U 57,57,12
U 58,58,12
Mvav 132,132,220
Ra 148,242,21504
Ra 152,242,22528
Ra 156,242,23552
U 59,59,12
U 60,60,12
U 61,61,12
Mvav 100,132,212
D 19,40,94,3072
s_add_u32 m0, m0, 4096
U 62,62,12
U 63,63,12
K 56,56,57
K 57,58,59
K 58,60,61
Mvav 104,136,212
K 59,62,63
v_cndmask_b32_e64 v171, v241, v171, s[78:79]
X 168
X 169
Mvav 136,136,220
buffer_store_dwordx4 v[56:59], v1, s[8:11], s93 offen offset:3072 sc1
Ra 128,242,49152
G 14,168
G 6,169
A 14,14,6
v_cmp_nlt_f32_e64 s[86:87], v14, s90
Mvav 140,140,220
v_rcp_f32 v14, v14
s_or_b64 s[88:89], s[88:89], s[86:87]
v_accvgpr_write_b32 a168, 0
v_accvgpr_write_b32 a169, 0
v_accvgpr_write_b32 a170, 0
v_accvgpr_write_b32 a171, 0
Mvav 108,140,212
W 1
G 15,172
G 7,173
A 15,15,7
v_cmp_nlt_f32_e64 s[86:87], v15, s90
v_rcp_f32 v15, v15
Mvav 112,144,212
s_or_b64 s[88:89], s[88:89], s[86:87]
buffer_load_dwordx4 v20, s[40:43], s94 offen lds
v_accvgpr_write_b32 a172, 0
v_accvgpr_write_b32 a173, 0
v_accvgpr_write_b32 a174, 0
v_accvgpr_write_b32 a175, 0
U 64,64,13
Mvav 144,144,220
s_add_u32 m0, s71, s48
s_lshl2_add_u32 s94, s48, s95
U 65,65,13
U 66,66,13
U 67,67,13
U 68,68,13
U 69,69,13
Mvav 148,148,220
U 70,70,13
U 71,71,13
K 64,64,65
K 65,66,67
K 66,68,69
K 67,70,71
s_add_u32 s93, s67, 131072
Mvav 116,148,212
buffer_store_dwordx4 v[64:67], v1, s[8:11], s93 offen offset:0 sc1
U 72,72,13
U 73,73,13
U 74,74,13
U 75,75,13
U 76,76,13
Mvav 120,152,212
Ra 132,242,50176
Ra 136,242,51200
Ra 140,242,52224
U 77,77,13
U 78,78,13
U 79,79,13
Mvav 152,152,220
K 72,72,73
K 73,74,75
K 74,76,77
K 75,78,79
buffer_store_dwordx4 v[72:75], v1, s[8:11], s93 offen offset:1024 sc1
U 80,80,13
Mvav 156,156,220
X 170
K 208,168,169
X 171
X 172
K 209,170,171
X 173
X 174
K 216,172,173
X 175
v_mov_b32 v194, 0
K 217,174,175
v_mov_b32 v195, 0
v_mov_b32 v202, 0
v_mov_b32 v203, 0
v_mov_b32 v210, 0
v_mov_b32 v211, 0
Mvav 124,156,212
v_cndmask_b32_e64 v188, v241, v188, s[72:73]
v_cndmask_b32_e64 v189, v241, v189, s[74:75]
v_cndmask_b32_e64 v190, v241, v190, s[76:77]
v_cndmask_b32_e64 v191, v241, v191, s[78:79]
X 188
X 189
X 190
K 218,188,189
X 191
s_nop 0
K 219,190,191
Maav 168,180,208
D 16,40,94,0
U 81,81,13
U 82,82,13
U 83,83,13
U 84,84,13
U 85,85,13
Maav 172,180,216
W 0
U 86,86,13
U 87,87,13
K 80,80,81
K 81,82,83
K 82,84,85
K 83,86,87
Zvav 32,128,192
buffer_store_dwordx4 v[80:83], v1, s[8:11], s93 offen offset:2048 sc1
U 88,88,13
U 89,89,13
U 90,90,13
U 91,91,13
U 92,92,13
Zvav 64,128,200
U 93,93,13
U 94,94,13
U 95,95,13
K 88,88,89
K 89,90,91
K 90,92,93
K 91,94,95
buffer_store_dwordx4 v[88:91], v1, s[8:11], s93 offen offset:3072 sc1
U 96,96,14
U 97,97,14
U 98,98,14
U 99,99,14
U 100,100,14
U 101,101,14
U 102,102,14
U 103,103,14
K 96,96,97
K 97,98,99
K 98,100,101
K 99,102,103
s_add_u32 s93, s67, 262144
buffer_store_dwordx4 v[96:99], v1, s[8:11], s93 offen offset:0 sc1
s_nop 1
Zvav 96,128,208
U 104,104,14
U 105,105,14
U 106,106,14
U 107,107,14
U 108,108,14
U 109,109,14
U 110,110,14
U 111,111,14
K 104,104,105
K 105,106,107
K 106,108,109
K 107,110,111
buffer_store_dwordx4 v[104:107], v1, s[8:11], s93 offen offset:1024 sc1
U 112,112,14
U 113,113,14
U 114,114,14
U 115,115,14
U 116,116,14
U 117,117,14
U 118,118,14
U 119,119,14
K 112,112,113
K 113,114,115
K 114,116,117
K 115,118,119
buffer_store_dwordx4 v[112:115], v1, s[8:11], s93 offen offset:2048 sc1
U 120,120,14
U 121,121,14
U 122,122,14
U 123,123,14
U 124,124,14
U 125,125,14
U 126,126,14
U 127,127,14
K 120,120,121
K 121,122,123
K 122,124,125
K 123,126,127
buffer_store_dwordx4 v[120:123], v1, s[8:11], s93 offen offset:3072 sc1
U 128,128,15
U 129,129,15
U 130,130,15
U 131,131,15
U 132,132,15
U 133,133,15
U 134,134,15
U 135,135,15
K 128,128,129
K 129,130,131
K 130,132,133
K 131,134,135
s_add_u32 s93, s67, 393216
buffer_store_dwordx4 v[128:131], v1, s[8:11], s93 offen offset:0 sc1
s_nop 1
Zvav 128,128,216
Ra 144,242,53248
U 136,136,15
U 137,137,15
U 138,138,15
U 139,139,15
U 140,140,15
Zvav 132,132,216
D 17,40,94,1024
U 141,141,15
U 142,142,15
U 143,143,15
K 136,136,137
K 137,138,139
Zvav 100,132,208
Ra 148,242,54272
Ra 152,242,55296
Ra 156,242,56320
K 138,140,141
K 139,142,143
Zvav 68,132,200
D 18,40,94,2048
Zvav 36,132,192
Zvav 40,136,192
D 19,40,94,3072
s_add_u32 m0, m0, 4096
Zvav 72,136,200
Zvav 104,136,208
buffer_store_dwordx4 v[136:139], v1, s[8:11], s93 offen offset:1024 sc1
U 144,144,15
U 145,145,15
U 146,146,15
U 147,147,15
U 148,148,15
Zvav 136,136,216
U 149,149,15
U 150,150,15
U 151,151,15
K 144,144,145
K 145,146,147
K 146,148,149
Zvav 140,140,216
K 147,150,151
buffer_store_dwordx4 v[144:147], v1, s[8:11], s93 offen offset:2048 sc1
U 152,152,15
U 153,153,15
U 154,154,15
U 155,155,15
Zvav 108,140,208
U 156,156,15
U 157,157,15
U 158,158,15
U 159,159,15
K 152,152,153
K 153,154,155
Zvav 76,140,200
buffer_load_dwordx4 v20, s[40:43], s94 offen lds
K 154,156,157
K 155,158,159
Zvav 44,140,192
W 0
Zvav 48,144,192
buffer_store_dwordx4 v[152:155], v1, s[8:11], s93 offen offset:3072 sc1
s_mov_b32 s18, 40960
s_mov_b32 s19, 73728
s_bitcmp1_b32 s14, 0
s_cselect_b32 s18, 49152, s18
s_cselect_b32 s19, 98304, s19
s_cmp_lt_u32 s14, 2
s_cselect_b32 s20, s18, s19
s_cselect_b32 s21, s19, s18
v_add_u32 v2, s20, v1
v_add_u32 v3, s21, v1
Zvav 80,144,200
s_lshl_b32 s18, s14, 9
v_lshrrev_b32 v232, 1, v1
v_add_u32 v232, s18, v232
v_bfe_u32 v12, v1, 6, 2
v_lshrrev_b32 v13, 8, v1
v_bfe_u32 v14, v1, 4, 1
v_lshlrev_b32 v14, 4, v14
Zvav 112,144,208
v_mov_b32 v15, 0x3f80
v_lshlrev_b32 v15, v14, v15
v_bfe_u32 v14, v1, 5, 1
v_xor_b32 v27, v12, v13
v_lshl_or_b32 v27, v27, 1, v14
v_cmp_eq_u32_e64 s[86:87], 0, v27
Zvav 144,144,216
v_mov_b32 v226, 0
Zvav 148,148,216
v_cndmask_b32_e64 v224, 0, v15, s[86:87]
v_mov_b32 v228, 0
v_mov_b32 v230, v224
v_cmp_eq_u32_e64 s[86:87], 1, v27
v_mov_b32 v227, 0
s_nop 0
v_cndmask_b32_e64 v225, 0, v15, s[86:87]
Zvav 116,148,208
v_mov_b32 v229, 0
v_mov_b32 v231, v225
G 4,160
G 8,161
G 5,164
G 9,165
Zvav 84,148,200
G 6,168
G 10,169
G 7,172
G 11,173
A 4,4,8
A 5,5,9
Zvav 52,148,192
A 6,6,10
A 7,7,11
ds_write_b64 v232, v[6:7] offset:57344
A 8,4,5
A 9,6,7
A 8,8,9
Zvav 56,152,192
v_cmp_ngt_f32_e64 s[86:87], s90, v8
s_lshl_b32 s19, s14, 2
v_mov_b32 v9, s19
s_or_b64 s[88:89], s[88:89], s[86:87]
s_and_b64 s[88:89], s[88:89], s[24:25]
s_cselect_b32 s18, 1, 0
v_mov_b32 v8, s18
ds_write_b32 v9, v8 offset:59392
Zvav 88,152,200
v_xor_b32 v10, 1024, v232
v_bfe_u32 v11, v1, 2, 4
K 96,96,97
K 97,98,99
K 98,100,101
K 99,102,103
Zvav 120,152,208
ds_write_b128 v2, v[96:99] offset:0
K 128,128,129
K 129,130,131
K 130,132,133
K 131,134,135
ds_write_b128 v2, v[128:131] offset:4096
)"
R"(Zvav 152,152,216
K 104,104,105
K 105,106,107
K 106,108,109
K 107,110,111
ds_write_b128 v2, v[104:107] offset:1024
K 136,136,137
Zvav 156,156,216
K 137,138,139
K 138,140,141
K 139,142,143
ds_write_b128 v2, v[136:139] offset:5120
K 112,112,113
K 113,114,115
Zvav 124,156,208
K 114,116,117
K 115,118,119
ds_write_b128 v2, v[112:115] offset:2048
K 144,144,145
K 145,146,147
K 146,148,149
Zvav 92,156,200
K 147,150,151
ds_write_b128 v2, v[144:147] offset:6144
Zvav 60,156,192
K 120,120,121
K 121,122,123
K 122,124,125
K 123,126,127
ds_write_b128 v2, v[120:123] offset:3072
K 152,152,153
K 153,154,155
K 154,156,157
K 155,158,159
ds_write_b128 v2, v[152:155] offset:7168
s_waitcnt vmcnt(0)
s_and_b64 s[86:87], s[88:89], s[24:25]
s_cbranch_scc1 .LL_50%=
v_mov_b64 v[28:29], s[12:13]
s_mov_b64 exec, 1
buffer_store_dwordx2 v[28:29], off, s[8:11], s69 sc1
s_mov_b64 exec, -1
.LL_50%=:
s_mov_b32 s70, 0
s_branch .Lend_43%=
.p2align 12
.Lpd_32%=:
v_pk_mul_f32 v[32:33], v[32:33], s[22:23]
v_pk_mul_f32 v[34:35], v[34:35], s[22:23]
v_pk_mul_f32 v[36:37], v[36:37], s[22:23]
v_pk_mul_f32 v[38:39], v[38:39], s[22:23]
v_pk_mul_f32 v[64:65], v[64:65], s[22:23]
v_pk_mul_f32 v[66:67], v[66:67], s[22:23]
v_pk_mul_f32 v[68:69], v[68:69], s[22:23]
v_pk_mul_f32 v[70:71], v[70:71], s[22:23]
v_pk_mul_f32 v[96:97], v[96:97], s[22:23]
v_pk_mul_f32 v[98:99], v[98:99], s[22:23]
v_pk_mul_f32 v[100:101], v[100:101], s[22:23]
v_pk_mul_f32 v[102:103], v[102:103], s[22:23]
v_pk_mul_f32 v[128:129], v[128:129], s[22:23]
v_pk_mul_f32 v[130:131], v[130:131], s[22:23]
v_pk_mul_f32 v[132:133], v[132:133], s[22:23]
v_pk_mul_f32 v[134:135], v[134:135], s[22:23]
v_pk_mul_f32 v[40:41], v[40:41], s[22:23]
v_pk_mul_f32 v[42:43], v[42:43], s[22:23]
v_pk_mul_f32 v[44:45], v[44:45], s[22:23]
v_pk_mul_f32 v[46:47], v[46:47], s[22:23]
v_pk_mul_f32 v[72:73], v[72:73], s[22:23]
v_pk_mul_f32 v[74:75], v[74:75], s[22:23]
v_pk_mul_f32 v[76:77], v[76:77], s[22:23]
v_pk_mul_f32 v[78:79], v[78:79], s[22:23]
v_pk_mul_f32 v[104:105], v[104:105], s[22:23]
v_pk_mul_f32 v[106:107], v[106:107], s[22:23]
v_pk_mul_f32 v[108:109], v[108:109], s[22:23]
v_pk_mul_f32 v[110:111], v[110:111], s[22:23]
v_pk_mul_f32 v[136:137], v[136:137], s[22:23]
v_pk_mul_f32 v[138:139], v[138:139], s[22:23]
v_pk_mul_f32 v[140:141], v[140:141], s[22:23]
v_pk_mul_f32 v[142:143], v[142:143], s[22:23]
v_pk_mul_f32 v[48:49], v[48:49], s[22:23]
v_pk_mul_f32 v[50:51], v[50:51], s[22:23]
v_pk_mul_f32 v[52:53], v[52:53], s[22:23]
v_pk_mul_f32 v[54:55], v[54:55], s[22:23]
v_pk_mul_f32 v[80:81], v[80:81], s[22:23]
v_pk_mul_f32 v[82:83], v[82:83], s[22:23]
v_pk_mul_f32 v[84:85], v[84:85], s[22:23]
v_pk_mul_f32 v[86:87], v[86:87], s[22:23]
v_pk_mul_f32 v[112:113], v[112:113], s[22:23]
v_pk_mul_f32 v[114:115], v[114:115], s[22:23]
v_pk_mul_f32 v[116:117], v[116:117], s[22:23]
v_pk_mul_f32 v[118:119], v[118:119], s[22:23]
v_pk_mul_f32 v[144:145], v[144:145], s[22:23]
v_pk_mul_f32 v[146:147], v[146:147], s[22:23]
v_pk_mul_f32 v[148:149], v[148:149], s[22:23]
v_pk_mul_f32 v[150:151], v[150:151], s[22:23]
v_pk_mul_f32 v[56:57], v[56:57], s[22:23]
v_pk_mul_f32 v[58:59], v[58:59], s[22:23]
v_pk_mul_f32 v[60:61], v[60:61], s[22:23]
v_pk_mul_f32 v[62:63], v[62:63], s[22:23]
v_pk_mul_f32 v[88:89], v[88:89], s[22:23]
v_pk_mul_f32 v[90:91], v[90:91], s[22:23]
v_pk_mul_f32 v[92:93], v[92:93], s[22:23]
v_pk_mul_f32 v[94:95], v[94:95], s[22:23]
v_pk_mul_f32 v[120:121], v[120:121], s[22:23]
v_pk_mul_f32 v[122:123], v[122:123], s[22:23]
v_pk_mul_f32 v[124:125], v[124:125], s[22:23]
v_pk_mul_f32 v[126:127], v[126:127], s[22:23]
v_pk_mul_f32 v[152:153], v[152:153], s[22:23]
v_pk_mul_f32 v[154:155], v[154:155], s[22:23]
v_pk_mul_f32 v[156:157], v[156:157], s[22:23]
v_pk_mul_f32 v[158:159], v[158:159], s[22:23]
K 32,32,33
K 33,34,35
K 34,36,37
K 35,38,39
K 40,40,41
K 41,42,43
K 42,44,45
K 43,46,47
W 0
Zvav 196,128,32
s_add_u32 m0, s62, 0
Ra 144,242,4096
Ra 148,242,5120
Ra 152,242,6144
Ra 156,242,7168
Mvav 196,132,40
K 64,64,65
K 65,66,67
K 66,68,69
K 67,70,71
K 72,72,73
K 73,74,75
K 74,76,77
K 75,78,79
s_nop 1
Zvav 204,132,72
Dc 1,8,61,0
K 96,96,97
K 97,98,99
K 98,100,101
Mvav 204,128,64
K 99,102,103
K 104,104,105
K 105,106,107
K 106,108,109
K 107,110,111
Zvav 212,128,96
Dc 1,8,61,1024
K 128,128,129
K 129,130,131
K 130,132,133
Mvav 212,132,104
K 131,134,135
K 136,136,137
K 137,138,139
K 138,140,141
K 139,142,143
s_nop 1
Zvav 220,132,136
Dc 1,8,61,2048
K 48,48,49
K 49,50,51
K 50,52,53
Mvav 220,128,128
K 51,54,55
K 56,56,57
K 57,58,59
K 58,60,61
K 59,62,63
Mvav 196,136,48
Dc 1,8,61,3072
s_add_u32 m0, s62, 4096
Ra 128,242,8192
Ra 132,242,9216
K 80,80,81
Mvav 196,140,56
K 81,82,83
K 82,84,85
K 83,86,87
K 88,88,89
K 89,90,91
K 90,92,93
K 91,94,95
s_nop 1
Mvav 204,140,88
Dc 243,8,61,0
K 112,112,113
K 113,114,115
K 114,116,117
Mvav 204,136,80
K 115,118,119
K 120,120,121
K 121,122,123
K 122,124,125
K 123,126,127
Mvav 212,136,112
Dc 243,8,61,1024
K 144,144,145
K 145,146,147
K 146,148,149
Mvav 212,140,120
K 147,150,151
K 152,152,153
K 153,154,155
K 154,156,157
K 155,158,159
s_nop 1
Mvav 220,140,152
Dc 243,8,61,2048
v_accvgpr_write_b32 a0, v32
v_accvgpr_write_b32 a1, v33
v_accvgpr_write_b32 a2, v34
Mvav 220,136,144
W 2
Dc 243,8,61,3072
s_add_u32 m0, s62, 8192
v_accvgpr_write_b32 a3, v35
v_accvgpr_write_b32 a4, v40
Zvav 68,144,64
Dc 244,8,61,0
v_accvgpr_write_b32 a5, v41
v_accvgpr_write_b32 a6, v42
v_accvgpr_write_b32 a7, v43
Mvav 68,148,72
Dc 244,8,61,1024
v_accvgpr_write_b32 a8, v48
v_accvgpr_write_b32 a9, v49
v_accvgpr_write_b32 a10, v50
Zvav 100,148,104
Dc 244,8,61,2048
Ra 136,242,10240
Ra 140,242,11264
v_accvgpr_write_b32 a11, v51
Mvav 100,144,96
Dc 244,8,61,3072
s_add_u32 m0, s62, 12288
v_accvgpr_write_b32 a12, v56
v_accvgpr_write_b32 a13, v57
v_accvgpr_write_b32 a14, v58
Zvav 132,144,128
Dc 245,8,61,0
v_accvgpr_write_b32 a15, v59
v_accvgpr_write_b32 a16, v64
v_cndmask_b32_e64 v196, v241, v196, s[72:73]
Mvav 132,148,136
Dc 245,8,61,1024
v_cndmask_b32_e64 v197, v241, v197, s[74:75]
v_cndmask_b32_e64 v198, v241, v198, s[76:77]
v_cndmask_b32_e64 v199, v241, v199, s[78:79]
Mvav 68,152,80
Dc 245,8,61,2048
v_max3_f32 v4, v196, v197, v198
v_max3_f32 v4, v4, v199, v252
v_max3_f32 v5, v204, v205, v206
Mvav 68,156,88
v_max3_f32 v5, v5, v207, v253
v_max3_f32 v6, v212, v213, v214
v_max3_f32 v6, v6, v215, v254
v_max3_f32 v7, v220, v221, v222
Mvav 100,156,120
v_max3_f32 v7, v7, v223, v255
v_mov_b32 v8, v4
v_mov_b32 v9, v5
v_mov_b32 v10, v6
Mvav 100,152,112
v_mov_b32 v11, v7
v_permlane32_swap_b32 v4, v8
v_permlane32_swap_b32 v5, v9
v_accvgpr_write_b32 a17, v65
Mvav 132,152,144
v_permlane32_swap_b32 v6, v10
v_permlane32_swap_b32 v7, v11
v_max_f32 v4, v4, v8
v_max_f32 v5, v5, v9
v_max_f32 v6, v6, v10
v_max_f32 v7, v7, v11
v_mov_b32 v8, v4
v_mov_b32 v9, v5
v_mov_b32 v10, v6
v_mov_b32 v11, v7
v_permlane16_swap_b32 v4, v8
v_permlane16_swap_b32 v5, v9
v_permlane16_swap_b32 v6, v10
v_permlane16_swap_b32 v7, v11
v_max_f32 v4, v4, v8
v_max_f32 v5, v5, v9
v_max_f32 v6, v6, v10
v_max_f32 v7, v7, v11
v_sub_f32 v4, 0, v4
v_sub_f32 v5, 0, v5
v_sub_f32 v6, 0, v6
v_sub_f32 v7, 0, v7
v_mov_b32 v224, v4
v_mov_b32 v225, v4
v_mov_b32 v226, v4
v_mov_b32 v227, v4
v_mov_b32 v228, v5
v_mov_b32 v229, v5
v_mov_b32 v230, v5
v_mov_b32 v231, v5
v_mov_b32 v232, v6
v_mov_b32 v233, v6
v_mov_b32 v234, v6
v_mov_b32 v235, v6
v_mov_b32 v236, v7
v_mov_b32 v237, v7
v_mov_b32 v238, v7
v_mov_b32 v239, v7
Mvav 132,156,152
W 0
Dc 245,8,61,3072
v_accvgpr_write_b32 a18, v66
v_accvgpr_write_b32 a19, v67
v_accvgpr_write_b32 a20, v72
Cvav 168,128,96,232
s_add_u32 s61, s61, s63
s_add_u32 s36, s29, 65536
v_accvgpr_write_b32 a21, v73
v_accvgpr_write_b32 a22, v74
v_accvgpr_write_b32 a23, v75
Mvav 168,132,104
s_and_b32 s80, s14, 1
buffer_store_dwordx4 a[192:195], v1, s[8:11], s36 offen offset:0 sc1
v_accvgpr_write_b32 a24, v80
v_accvgpr_write_b32 a25, v81
v_accvgpr_write_b32 a26, v82
Cvav 172,132,136,236
s_lshl_b32 s80, s80, 14
Ra 144,242,12288
Ra 148,242,13312
Ra 152,242,14336
Ra 156,242,15360
Mvav 172,128,128
s_add_u32 s80, s80, 98304
v_accvgpr_write_b32 a27, v83
v_accvgpr_write_b32 a28, v88
v_accvgpr_write_b32 a29, v89
v_accvgpr_write_b32 a30, v90
Mvav 168,136,112
buffer_store_dwordx4 a[200:203], v1, s[8:11], s36 offen offset:1024 sc1
v_accvgpr_write_b32 a31, v91
v_accvgpr_write_b32 a32, v96
v_accvgpr_write_b32 a33, v97
Mvav 168,140,120
v_accvgpr_write_b32 a34, v98
v_accvgpr_write_b32 a35, v99
v_accvgpr_write_b32 a36, v104
v_accvgpr_write_b32 a37, v105
Mvav 172,140,152
Ra 128,242,24576
Ra 132,242,25600
buffer_store_dwordx4 a[208:211], v1, s[8:11], s36 offen offset:2048 sc1
v_accvgpr_write_b32 a38, v106
Mvav 172,136,144
Ra 136,242,26624
Ra 140,242,27648
v_accvgpr_write_b32 a39, v107
W 4
Cvav 188,144,128,236
v_accvgpr_write_b32 a40, v112
v_accvgpr_write_b32 a41, v113
v_accvgpr_write_b32 a42, v114
v_accvgpr_write_b32 a43, v115
v_accvgpr_write_b32 a44, v120
v_accvgpr_write_b32 a45, v121
v_accvgpr_write_b32 a46, v122
v_accvgpr_write_b32 a47, v123
v_accvgpr_write_b32 a48, v128
v_accvgpr_write_b32 a49, v129
v_accvgpr_write_b32 a50, v130
v_accvgpr_write_b32 a51, v131
v_accvgpr_write_b32 a52, v136
v_accvgpr_write_b32 a53, v137
v_accvgpr_write_b32 a54, v138
v_accvgpr_write_b32 a55, v139
v_accvgpr_write_b32 a56, v144
v_accvgpr_write_b32 a57, v145
v_accvgpr_write_b32 a58, v146
v_accvgpr_write_b32 a59, v147
v_accvgpr_write_b32 a60, v152
v_accvgpr_write_b32 a61, v153
v_accvgpr_write_b32 a62, v154
v_accvgpr_write_b32 a63, v155
Mvav 188,148,136
s_barrier
buffer_store_dwordx4 a[216:219], v1, s[8:11], s36 offen offset:3072 sc1
s_add_u32 s36, s29, 69632
v_add_u32 v8, s80, v1
A 196,196,224
Mvav 188,152,144
A 197,197,224
A 198,198,224
A 199,199,224
X 196
X 197
X 198
K 192,196,197
X 199
A 204,204,228
)"
R"(K 193,198,199
A 205,205,228
A 206,206,228
A 207,207,228
X 204
X 205
X 206
K 200,204,205
X 207
A 212,212,232
K 201,206,207
A 213,213,232
A 214,214,232
A 215,215,232
X 212
X 213
X 214
K 208,212,213
X 215
A 220,220,236
K 209,214,215
A 221,221,236
A 222,222,236
A 223,223,236
X 220
X 221
X 222
K 216,220,221
X 223
v_mov_b32 v194, 0
K 217,222,223
v_mov_b32 v195, 0
v_cndmask_b32_e64 v68, v241, v68, s[72:73]
v_cndmask_b32_e64 v69, v241, v69, s[74:75]
v_cndmask_b32_e64 v70, v241, v70, s[76:77]
v_cndmask_b32_e64 v71, v241, v71, s[78:79]
A 68,68,228
A 69,69,228
A 70,70,228
A 71,71,228
X 68
X 69
X 70
K 202,68,69
X 71
A 100,100,232
K 203,70,71
A 101,101,232
A 102,102,232
A 103,103,232
X 100
X 101
X 102
K 210,100,101
X 103
A 132,132,236
K 211,102,103
A 133,133,236
A 134,134,236
A 135,135,236
X 132
X 133
X 134
K 218,132,133
X 135
s_nop 0
K 219,134,135
Mvav 188,156,152
W 0
buffer_store_dwordx4 a[224:227], v1, s[8:11], s36 offen offset:0 sc1
Ra 144,242,28672
Ra 148,242,29696
Ra 152,242,30720
Zvav 32,128,192
buffer_store_dwordx4 a[232:235], v1, s[8:11], s36 offen offset:1024 sc1
Ra 156,242,31744
Zvav 64,128,200
buffer_store_dwordx4 a[240:243], v1, s[8:11], s36 offen offset:2048 sc1
Zvav 96,128,208
s_and_b32 s80, s14, 1
s_xor_b32 s62, s62, 0x10000
buffer_store_dwordx4 a[248:251], v1, s[8:11], s36 offen offset:3072 sc1
s_add_u32 s36, s29, 393216
Zvav 128,128,216
s_lshl_b32 s80, s80, 14
s_add_u32 s80, s80, 131072
buffer_store_dwordx4 a[196:199], v1, s[8:11], s36 offen offset:0 sc1
v_add_u32 v23, 61440, v21
Zvav 132,132,216
buffer_store_dwordx4 a[204:207], v1, s[8:11], s36 offen offset:1024 sc1
v_add_u32 v12, s80, v1
Ra 64,12,8192
v_add_u32 v24, 61440, v22
Zvav 100,132,208
buffer_store_dwordx4 a[212:215], v1, s[8:11], s36 offen offset:2048 sc1
Ra 68,12,9216
Ra 72,12,10240
v_add_u32 v25, 122880, v21
Zvav 68,132,200
buffer_store_dwordx4 a[220:223], v1, s[8:11], s36 offen offset:3072 sc1
s_add_u32 s36, s29, 397312
Ra 76,12,11264
Ra 80,12,12288
Ra 84,12,13312
Zvav 36,132,192
buffer_store_dwordx4 a[228:231], v1, s[8:11], s36 offen offset:0 sc1
Ra 88,12,14336
Ra 92,12,15360
v_add_u32 v26, 122880, v22
Zvav 40,136,192
buffer_store_dwordx4 a[236:239], v1, s[8:11], s36 offen offset:1024 sc1
Ra 96,12,0
Ra 100,12,1024
Ra 104,12,2048
Zvav 72,136,200
Ra 108,12,3072
Ra 112,12,4096
buffer_store_dwordx4 a[244:247], v1, s[8:11], s36 offen offset:2048 sc1
Ra 116,12,5120
Zvav 104,136,208
Ra 120,12,6144
Ra 124,12,7168
buffer_store_dwordx4 a[252:255], v1, s[8:11], s36 offen offset:3072 sc1
Zvav 136,136,216
Zvav 140,140,216
Zvav 108,140,208
Zvav 76,140,200
s_waitcnt vmcnt(16) lgkmcnt(0)
s_barrier
v_xor_b32 v242, 0x10000, v242
ds_read_b128 v[248:251], v8
Zvav 44,140,192
Ra 128,242,40960
Zvav 48,144,192
Zvav 80,144,200
X 172
X 173
Zvav 112,144,208
Ra 132,242,41984
X 174
K 220,172,173
Zvav 144,144,216
X 175
v_cndmask_b32_e64 v168, v241, v168, s[72:73]
K 221,174,175
Zvav 148,148,216
v_cndmask_b32_e64 v169, v241, v169, s[74:75]
v_cndmask_b32_e64 v170, v241, v170, s[76:77]
v_cndmask_b32_e64 v171, v241, v171, s[78:79]
Zvav 116,148,208
X 168
X 169
Zvav 84,148,200
W 2
Ra 136,242,43008
X 170
K 212,168,169
Zvav 52,148,192
v_mov_b32 v224, v250
v_mov_b32 v225, v250
v_mov_b32 v226, v250
v_mov_b32 v227, v250
Zvav 56,152,192
v_mov_b32 v228, v251
v_mov_b32 v229, v251
v_mov_b32 v230, v251
v_mov_b32 v231, v251
Zvav 88,152,200
v_mov_b32 v232, v248
v_mov_b32 v233, v248
v_mov_b32 v234, v248
v_mov_b32 v235, v248
Zvav 120,152,208
v_mov_b32 v236, v249
v_mov_b32 v237, v249
v_mov_b32 v238, v249
v_mov_b32 v239, v249
Zvav 152,152,216
X 171
v_mov_b32 v204, 0
K 213,170,171
Zvav 156,156,216
Ra 140,242,44032
v_mov_b32 v205, 0
v_mov_b32 v196, 0
v_mov_b32 v197, 0
Zvav 124,156,208
v_mov_b32 v198, 0
v_mov_b32 v199, 0
v_mov_b32 v206, 0
v_mov_b32 v207, 0
Zvav 92,156,200
v_mov_b32 v214, 0
v_mov_b32 v215, 0
v_cndmask_b32_e64 v188, v241, v188, s[72:73]
v_cndmask_b32_e64 v189, v241, v189, s[74:75]
Zvav 60,156,192
v_cndmask_b32_e64 v190, v241, v190, s[76:77]
v_cndmask_b32_e64 v191, v241, v191, s[78:79]
X 188
X 189
X 190
K 222,188,189
X 191
s_nop 0
K 223,190,191
s_waitcnt vmcnt(0)
s_add_u32 s80, s71, 128
v_mov_b64 v[28:29], s[12:13]
s_mov_b64 exec, 1
buffer_store_dwordx2 v[28:29], off, s[8:11], s80 sc1
s_mov_b64 exec, -1
s_nop 1
s_branch .Lsp2_37%=
.p2align 12
.Lpe_48%=:
v_pk_mul_f32 v[32:33], v[32:33], s[22:23]
v_pk_mul_f32 v[34:35], v[34:35], s[22:23]
v_pk_mul_f32 v[36:37], v[36:37], s[22:23]
v_pk_mul_f32 v[38:39], v[38:39], s[22:23]
v_pk_mul_f32 v[64:65], v[64:65], s[22:23]
v_pk_mul_f32 v[66:67], v[66:67], s[22:23]
v_pk_mul_f32 v[68:69], v[68:69], s[22:23]
v_pk_mul_f32 v[70:71], v[70:71], s[22:23]
v_pk_mul_f32 v[96:97], v[96:97], s[22:23]
v_pk_mul_f32 v[98:99], v[98:99], s[22:23]
v_pk_mul_f32 v[100:101], v[100:101], s[22:23]
v_pk_mul_f32 v[102:103], v[102:103], s[22:23]
v_pk_mul_f32 v[128:129], v[128:129], s[22:23]
v_pk_mul_f32 v[130:131], v[130:131], s[22:23]
v_pk_mul_f32 v[132:133], v[132:133], s[22:23]
v_pk_mul_f32 v[134:135], v[134:135], s[22:23]
v_pk_mul_f32 v[40:41], v[40:41], s[22:23]
v_pk_mul_f32 v[42:43], v[42:43], s[22:23]
v_pk_mul_f32 v[44:45], v[44:45], s[22:23]
v_pk_mul_f32 v[46:47], v[46:47], s[22:23]
v_pk_mul_f32 v[72:73], v[72:73], s[22:23]
v_pk_mul_f32 v[74:75], v[74:75], s[22:23]
v_pk_mul_f32 v[76:77], v[76:77], s[22:23]
v_pk_mul_f32 v[78:79], v[78:79], s[22:23]
v_pk_mul_f32 v[104:105], v[104:105], s[22:23]
v_pk_mul_f32 v[106:107], v[106:107], s[22:23]
v_pk_mul_f32 v[108:109], v[108:109], s[22:23]
v_pk_mul_f32 v[110:111], v[110:111], s[22:23]
v_pk_mul_f32 v[136:137], v[136:137], s[22:23]
v_pk_mul_f32 v[138:139], v[138:139], s[22:23]
v_pk_mul_f32 v[140:141], v[140:141], s[22:23]
v_pk_mul_f32 v[142:143], v[142:143], s[22:23]
v_pk_mul_f32 v[48:49], v[48:49], s[22:23]
v_pk_mul_f32 v[50:51], v[50:51], s[22:23]
v_pk_mul_f32 v[52:53], v[52:53], s[22:23]
v_pk_mul_f32 v[54:55], v[54:55], s[22:23]
v_pk_mul_f32 v[80:81], v[80:81], s[22:23]
v_pk_mul_f32 v[82:83], v[82:83], s[22:23]
v_pk_mul_f32 v[84:85], v[84:85], s[22:23]
v_pk_mul_f32 v[86:87], v[86:87], s[22:23]
v_pk_mul_f32 v[112:113], v[112:113], s[22:23]
v_pk_mul_f32 v[114:115], v[114:115], s[22:23]
v_pk_mul_f32 v[116:117], v[116:117], s[22:23]
v_pk_mul_f32 v[118:119], v[118:119], s[22:23]
v_pk_mul_f32 v[144:145], v[144:145], s[22:23]
v_pk_mul_f32 v[146:147], v[146:147], s[22:23]
v_pk_mul_f32 v[148:149], v[148:149], s[22:23]
v_pk_mul_f32 v[150:151], v[150:151], s[22:23]
v_pk_mul_f32 v[56:57], v[56:57], s[22:23]
v_pk_mul_f32 v[58:59], v[58:59], s[22:23]
v_pk_mul_f32 v[60:61], v[60:61], s[22:23]
v_pk_mul_f32 v[62:63], v[62:63], s[22:23]
v_pk_mul_f32 v[88:89], v[88:89], s[22:23]
v_pk_mul_f32 v[90:91], v[90:91], s[22:23]
v_pk_mul_f32 v[92:93], v[92:93], s[22:23]
v_pk_mul_f32 v[94:95], v[94:95], s[22:23]
v_pk_mul_f32 v[120:121], v[120:121], s[22:23]
v_pk_mul_f32 v[122:123], v[122:123], s[22:23]
v_pk_mul_f32 v[124:125], v[124:125], s[22:23]
v_pk_mul_f32 v[126:127], v[126:127], s[22:23]
v_pk_mul_f32 v[152:153], v[152:153], s[22:23]
v_pk_mul_f32 v[154:155], v[154:155], s[22:23]
v_pk_mul_f32 v[156:157], v[156:157], s[22:23]
v_pk_mul_f32 v[158:159], v[158:159], s[22:23]
K 32,32,33
K 33,34,35
K 34,36,37
K 35,38,39
K 40,40,41
K 41,42,43
K 42,44,45
K 43,46,47
W 0
Zvav 196,128,32
s_add_u32 m0, s62, 0
Ra 144,242,4096
Ra 148,242,5120
Ra 152,242,6144
Ra 156,242,7168
Mvav 196,132,40
K 64,64,65
K 65,66,67
K 66,68,69
K 67,70,71
K 72,72,73
K 73,74,75
K 74,76,77
K 75,78,79
s_nop 1
Zvav 204,132,72
Dc 1,8,61,0
K 96,96,97
K 97,98,99
K 98,100,101
Mvav 204,128,64
K 99,102,103
K 104,104,105
K 105,106,107
K 106,108,109
K 107,110,111
Zvav 212,128,96
Dc 1,8,61,1024
K 128,128,129
K 129,130,131
K 130,132,133
Mvav 212,132,104
K 131,134,135
K 136,136,137
K 137,138,139
K 138,140,141
K 139,142,143
s_nop 1
Zvav 220,132,136
Dc 1,8,61,2048
K 48,48,49
K 49,50,51
K 50,52,53
Mvav 220,128,128
K 51,54,55
K 56,56,57
K 57,58,59
K 58,60,61
K 59,62,63
Mvav 196,136,48
Dc 1,8,61,3072
s_add_u32 m0, s62, 4096
Ra 128,242,8192
Ra 132,242,9216
K 80,80,81
Mvav 196,140,56
K 81,82,83
K 82,84,85
K 83,86,87
K 88,88,89
K 89,90,91
K 90,92,93
K 91,94,95
s_nop 1
Mvav 204,140,88
Dc 243,8,61,0
K 112,112,113
K 113,114,115
K 114,116,117
Mvav 204,136,80
K 115,118,119
K 120,120,121
K 121,122,123
K 122,124,125
K 123,126,127
Mvav 212,136,112
Dc 243,8,61,1024
K 144,144,145
K 145,146,147
K 146,148,149
Mvav 212,140,120
K 147,150,151
K 152,152,153
K 153,154,155
K 154,156,157
K 155,158,159
s_nop 1
Mvav 220,140,152
Dc 243,8,61,2048
v_accvgpr_write_b32 a0, v32
v_accvgpr_write_b32 a1, v33
v_accvgpr_write_b32 a2, v34
Mvav 220,136,144
W 2
Dc 243,8,61,3072
s_add_u32 m0, s62, 8192
v_accvgpr_write_b32 a3, v35
v_accvgpr_write_b32 a4, v40
Zvav 36,144,32
Ra 136,242,10240
Ra 140,242,11264
Dc 244,8,61,0
v_accvgpr_write_b32 a5, v41
Mvav 36,148,40
v_accvgpr_write_b32 a6, v42
v_accvgpr_write_b32 a7, v43
v_max3_f32 v4, v196, v197, v198
v_max3_f32 v4, v4, v199, v252
Zvav 68,148,72
v_max3_f32 v5, v204, v205, v206
)"
R"(v_max3_f32 v5, v5, v207, v253
v_max3_f32 v6, v212, v213, v214
v_max3_f32 v6, v6, v215, v254
Mvav 68,144,64
v_max3_f32 v7, v220, v221, v222
v_max3_f32 v7, v7, v223, v255
v_mov_b32 v8, v4
v_mov_b32 v9, v5
Zvav 100,144,96
v_mov_b32 v10, v6
v_mov_b32 v11, v7
v_permlane32_swap_b32 v4, v8
v_accvgpr_write_b32 a8, v48
Mvav 100,148,104
v_permlane32_swap_b32 v5, v9
v_permlane32_swap_b32 v6, v10
v_permlane32_swap_b32 v7, v11
v_max_f32 v4, v4, v8
Zvav 132,148,136
v_max_f32 v5, v5, v9
v_max_f32 v6, v6, v10
v_max_f32 v7, v7, v11
v_mov_b32 v8, v4
Mvav 132,144,128
Dc 244,8,61,1024
v_mov_b32 v9, v5
v_mov_b32 v10, v6
v_mov_b32 v11, v7
Mvav 36,152,48
v_permlane16_swap_b32 v4, v8
v_permlane16_swap_b32 v5, v9
v_permlane16_swap_b32 v6, v10
v_accvgpr_write_b32 a9, v49
Mvav 36,156,56
v_permlane16_swap_b32 v7, v11
v_max_f32 v4, v4, v8
v_max_f32 v5, v5, v9
v_max_f32 v6, v6, v10
Mvav 68,156,88
v_max_f32 v7, v7, v11
v_sub_f32 v4, 0, v4
v_sub_f32 v5, 0, v5
v_sub_f32 v6, 0, v6
Mvav 68,152,80
v_sub_f32 v7, 0, v7
v_mov_b32 v224, v4
v_mov_b32 v225, v4
v_mov_b32 v226, v4
Mvav 100,152,112
v_mov_b32 v227, v4
v_mov_b32 v228, v5
v_mov_b32 v229, v5
v_mov_b32 v230, v5
Mvav 100,156,120
v_mov_b32 v231, v5
v_mov_b32 v232, v6
v_mov_b32 v233, v6
v_mov_b32 v234, v6
Mvav 132,156,152
v_mov_b32 v235, v6
v_mov_b32 v236, v7
v_mov_b32 v237, v7
v_mov_b32 v238, v7
v_mov_b32 v239, v7
Mvav 132,152,144
W 0
Dc 244,8,61,2048
v_accvgpr_write_b32 a10, v50
v_accvgpr_write_b32 a11, v51
v_accvgpr_write_b32 a12, v56
Cvav 160,128,32,224
Dc 244,8,61,3072
s_add_u32 m0, s62, 12288
v_accvgpr_write_b32 a13, v57
v_accvgpr_write_b32 a14, v58
v_accvgpr_write_b32 a15, v59
Mvav 160,132,40
s_and_b32 s80, s14, 1
Ra 144,242,12288
Ra 148,242,13312
v_accvgpr_write_b32 a16, v64
v_accvgpr_write_b32 a17, v65
Cvav 164,132,72,228
s_lshl_b32 s80, s80, 14
Dc 245,8,61,0
v_accvgpr_write_b32 a18, v66
v_accvgpr_write_b32 a19, v67
v_accvgpr_write_b32 a20, v72
Mvav 164,128,64
s_add_u32 s80, s80, 98304
Dc 245,8,61,1024
v_accvgpr_write_b32 a21, v73
v_accvgpr_write_b32 a22, v74
v_accvgpr_write_b32 a23, v75
Cvav 168,128,96,232
Dc 245,8,61,2048
v_accvgpr_write_b32 a24, v80
v_accvgpr_write_b32 a25, v81
v_accvgpr_write_b32 a26, v82
Mvav 168,132,104
Dc 245,8,61,3072
s_add_u32 s61, s61, s63
s_add_u32 s36, s29, 65536
A 196,196,224
v_accvgpr_write_b32 a27, v83
Cvav 172,132,136,236
Ra 152,242,14336
A 197,197,224
A 198,198,224
v_accvgpr_write_b32 a28, v88
Mvav 172,128,128
buffer_store_dwordx4 a[192:195], v1, s[8:11], s36 offen offset:0 sc1
Ra 156,242,15360
A 199,199,224
v_accvgpr_write_b32 a29, v89
Mvav 160,136,48
X 196
v_accvgpr_write_b32 a30, v90
v_accvgpr_write_b32 a31, v91
Mvav 160,140,56
X 197
X 198
Mvav 164,140,88
buffer_store_dwordx4 a[200:203], v1, s[8:11], s36 offen offset:1024 sc1
v_accvgpr_write_b32 a32, v96
K 192,196,197
v_accvgpr_write_b32 a33, v97
Mvav 164,136,80
X 199
A 204,204,228
v_accvgpr_write_b32 a34, v98
Mvav 168,136,112
K 193,198,199
v_accvgpr_write_b32 a35, v99
A 205,205,228
A 206,206,228
Mvav 168,140,120
buffer_store_dwordx4 a[208:211], v1, s[8:11], s36 offen offset:2048 sc1
v_accvgpr_write_b32 a36, v104
v_add_u32 v8, s80, v1
A 207,207,228
Mvav 172,140,152
X 204
v_accvgpr_write_b32 a37, v105
v_accvgpr_write_b32 a38, v106
Mvav 172,136,144
W 0
X 205
X 206
Cvav 176,144,32,224
Ra 128,242,24576
v_accvgpr_write_b32 a39, v107
v_accvgpr_write_b32 a40, v112
K 200,204,205
Mvav 176,148,40
buffer_store_dwordx4 a[216:219], v1, s[8:11], s36 offen offset:3072 sc1
s_add_u32 s36, s29, 69632
X 207
v_accvgpr_write_b32 a41, v113
Cvav 180,148,72,228
Ra 132,242,25600
A 212,212,232
v_accvgpr_write_b32 a42, v114
K 201,206,207
Mvav 180,144,64
A 213,213,232
v_accvgpr_write_b32 a43, v115
A 214,214,232
A 215,215,232
Cvav 184,144,96,232
v_accvgpr_write_b32 a44, v120
X 212
v_accvgpr_write_b32 a45, v121
Mvav 184,148,104
buffer_store_dwordx4 a[224:227], v1, s[8:11], s36 offen offset:0 sc1
X 213
v_accvgpr_write_b32 a46, v122
Cvav 188,148,136,236
X 214
v_accvgpr_write_b32 a47, v123
K 208,212,213
Mvav 188,144,128
v_accvgpr_write_b32 a48, v128
X 215
v_accvgpr_write_b32 a49, v129
Mvav 176,152,48
buffer_store_dwordx4 a[232:235], v1, s[8:11], s36 offen offset:1024 sc1
A 220,220,236
v_accvgpr_write_b32 a50, v130
K 209,214,215
Mvav 176,156,56
A 221,221,236
v_accvgpr_write_b32 a51, v131
A 222,222,236
A 223,223,236
Mvav 180,156,88
Ra 136,242,26624
Ra 140,242,27648
v_accvgpr_write_b32 a52, v136
v_accvgpr_write_b32 a53, v137
Mvav 180,152,80
X 220
X 221
Mvav 184,152,112
v_accvgpr_write_b32 a54, v138
v_accvgpr_write_b32 a55, v139
v_accvgpr_write_b32 a56, v144
v_accvgpr_write_b32 a57, v145
v_accvgpr_write_b32 a58, v146
v_accvgpr_write_b32 a59, v147
v_accvgpr_write_b32 a60, v152
v_accvgpr_write_b32 a61, v153
v_accvgpr_write_b32 a62, v154
v_accvgpr_write_b32 a63, v155
Mvav 184,156,120
s_barrier
buffer_store_dwordx4 a[240:243], v1, s[8:11], s36 offen offset:2048 sc1
X 222
K 216,220,221
Mvav 188,156,152
X 223
A 36,36,224
K 217,222,223
A 37,37,224
A 38,38,224
A 39,39,224
X 36
X 37
X 38
K 194,36,37
X 39
A 68,68,228
K 195,38,39
A 69,69,228
A 70,70,228
A 71,71,228
X 68
X 69
X 70
K 202,68,69
X 71
A 100,100,232
K 203,70,71
A 101,101,232
A 102,102,232
A 103,103,232
X 100
X 101
X 102
K 210,100,101
X 103
A 132,132,236
K 211,102,103
A 133,133,236
A 134,134,236
A 135,135,236
X 132
X 133
X 134
K 218,132,133
X 135
s_nop 0
K 219,134,135
Mvav 188,152,144
W 0
buffer_store_dwordx4 a[248:251], v1, s[8:11], s36 offen offset:3072 sc1
s_add_u32 s36, s29, 393216
Ra 144,242,28672
Ra 148,242,29696
Zvav 32,128,192
buffer_store_dwordx4 a[196:199], v1, s[8:11], s36 offen offset:0 sc1
Ra 152,242,30720
Ra 156,242,31744
Zvav 64,128,200
buffer_store_dwordx4 a[204:207], v1, s[8:11], s36 offen offset:1024 sc1
Zvav 96,128,208
s_and_b32 s80, s14, 1
s_xor_b32 s62, s62, 0x10000
buffer_store_dwordx4 a[212:215], v1, s[8:11], s36 offen offset:2048 sc1
Zvav 128,128,216
s_lshl_b32 s80, s80, 14
s_add_u32 s80, s80, 131072
buffer_store_dwordx4 a[220:223], v1, s[8:11], s36 offen offset:3072 sc1
s_add_u32 s36, s29, 397312
v_add_u32 v23, 61440, v21
Zvav 132,132,216
buffer_store_dwordx4 a[228:231], v1, s[8:11], s36 offen offset:0 sc1
v_add_u32 v12, s80, v1
Ra 64,12,8192
v_add_u32 v24, 61440, v22
Zvav 100,132,208
Ra 68,12,9216
Ra 72,12,10240
buffer_store_dwordx4 a[236:239], v1, s[8:11], s36 offen offset:1024 sc1
v_add_u32 v25, 122880, v21
Zvav 68,132,200
Ra 76,12,11264
Ra 80,12,12288
Ra 84,12,13312
v_add_u32 v26, 122880, v22
Zvav 36,132,192
Ra 88,12,14336
Ra 92,12,15360
Ra 96,12,0
Ra 100,12,1024
Zvav 40,136,192
Ra 104,12,2048
buffer_store_dwordx4 a[244:247], v1, s[8:11], s36 offen offset:2048 sc1
Ra 108,12,3072
Ra 112,12,4096
Zvav 72,136,200
Ra 116,12,5120
Ra 120,12,6144
X 172
Zvav 104,136,208
Ra 124,12,7168
buffer_store_dwordx4 a[252:255], v1, s[8:11], s36 offen offset:3072 sc1
X 173
Zvav 136,136,216
X 174
K 220,172,173
Zvav 140,140,216
X 175
X 168
Zvav 108,140,208
K 221,174,175
X 169
Zvav 76,140,200
s_waitcnt vmcnt(16) lgkmcnt(0)
s_barrier
v_xor_b32 v242, 0x10000, v242
ds_read_b128 v[248:251], v8
Zvav 44,140,192
Ra 128,242,0
X 170
K 212,168,169
Zvav 48,144,192
X 171
X 164
Zvav 80,144,200
K 213,170,171
X 165
Zvav 112,144,208
Ra 132,242,1024
X 166
K 204,164,165
Zvav 144,144,216
X 167
X 160
Zvav 148,148,216
K 205,166,167
X 161
Zvav 116,148,208
Ra 136,242,2048
X 162
Zvav 84,148,200
W 3
K 196,160,161
X 163
Zvav 52,148,192
X 176
K 197,162,163
Zvav 56,152,192
Ra 140,242,3072
X 177
Zvav 88,152,200
X 178
K 198,176,177
Zvav 120,152,208
X 179
X 180
Zvav 152,152,216
K 199,178,179
Zvav 156,156,216
X 181
X 182
Zvav 124,156,208
K 206,180,181
X 183
Zvav 92,156,200
K 207,182,183
Zvav 60,156,192
s_waitcnt vmcnt(0)
s_add_u32 s80, s71, 128
v_mov_b64 v[28:29], s[12:13]
s_mov_b64 exec, 1
buffer_store_dwordx2 v[28:29], off, s[8:11], s80 sc1
s_mov_b64 exec, -1
s_branch .Lsp1_36%=
.p2align 12
.Lmoe_44%=:
W 0
Cvaa 160,128,0,224
s_cmp_eq_u32 s14, 0
s_cselect_b32 s20, s30, s32
s_cselect_b32 s21, s31, s33
Ra 144,242,4096
Ra 148,242,5120
Mvaa 160,132,4
s_xor_b32 s62, s62, 0x10000
)"
R"(Ra 152,242,6144
Cvaa 164,132,20,228
X 184
Mvaa 164,128,16
s_and_b32 s21, s21, 0xffff
X 185
Cvaa 168,128,32,232
X 186
Mvaa 168,132,36
Ra 156,242,7168
K 214,184,185
Cvaa 172,132,52,236
s_mov_b32 s22, 2211840
X 187
Mvaa 172,128,48
X 188
Mvaa 160,136,8
Ra 128,242,16384
Mvaa 160,140,12
s_mov_b32 s23, 0x20000
K 215,186,187
Mvaa 164,140,28
X 189
Mvaa 164,136,24
X 190
Mvaa 168,136,40
Ra 132,242,17408
v_lshrrev_b32 v4, 6, v1
Mvaa 168,140,44
K 222,188,189
Mvaa 172,140,60
X 191
Mvaa 172,136,56
v_mul_u32_u24 v4, 3456, v4
K 223,190,191
v_smfmac_f32_16x16x64_bf16 a[160:163], a[176:179], v[192:199], v240
Ra 136,242,18432
v_smfmac_f32_16x16x64_bf16 a[164:167], a[176:179], v[200:207], v240
X 160
v_smfmac_f32_16x16x64_bf16 a[168:171], a[176:179], v[208:215], v240
v_lshrrev_b32 v5, 4, v1
v_smfmac_f32_16x16x64_bf16 a[172:175], a[176:179], v[216:223], v240
W 3
X 161
Cvaa 176,144,0,224
X 162
Mvaa 176,148,4
Ra 140,242,19456
v_xor_b32 v5, v5, v1
Cvaa 180,148,20,228
K 192,160,161
Mvaa 180,144,16
X 163
Cvaa 184,144,32,232
v_and_b32 v5, 32, v5
Mvaa 184,148,36
X 164
Cvaa 188,148,52,236
K 193,162,163
v_and_b32 v6, 16, v1
Mvaa 188,144,48
X 165
Mvaa 176,152,8
X 166
Mvaa 176,156,12
Ra 144,242,20480
Ra 148,242,21504
Mvaa 180,156,28
v_add3_u32 v4, v4, v5, v6
K 200,164,165
Mvaa 180,152,24
X 167
Mvaa 184,152,40
X 168
Mvaa 184,156,44
v_add_u32 v5, 54272, v4
K 201,166,167
Mvaa 188,156,60
X 169
Mvaa 188,152,56
W 2
Ra 152,242,22528
v_add_u32 v6, 108544, v4
Mvav 32,128,196
X 170
Mvav 64,128,204
Ra 156,242,23552
v_add_u32 v7, 162816, v4
Mvav 96,128,212
K 208,168,169
Mvav 128,128,220
X 171
Mvav 132,132,220
s_lshl_b32 s94, s85, 6
X 172
Mvav 100,132,212
s_add_u32 s64, s64, 1
Ra 128,242,8192
K 209,170,171
Mvav 68,132,204
X 173
Mvav 36,132,196
s_mul_i32 s95, s14, 20480
Mvav 40,136,196
X 174
Mvav 72,136,204
Ra 132,242,9216
K 216,172,173
Mvav 104,136,212
s_add_u32 s95, s95, 98304
X 175
Mvav 136,136,220
Mvav 140,140,220
s_add_u32 m0, s95, 0
X 176
Mvav 108,140,212
Ra 136,242,10240
K 217,174,175
Mvav 76,140,204
X 177
Mvav 44,140,196
W 3
D 4,20,94,0
Mvav 48,144,196
X 178
Mvav 80,144,204
Ra 140,242,11264
Mvav 112,144,212
D 5,20,94,1024
K 194,176,177
Mvav 144,144,220
X 179
Mvav 148,148,220
X 180
Mvav 116,148,212
Ra 144,242,12288
D 6,20,94,2048
Mvav 84,148,204
K 195,178,179
Mvav 52,148,196
X 181
Mvav 56,152,196
D 7,20,94,3072
Ra 148,242,13312
Mvav 88,152,204
X 182
Mvav 120,152,212
s_add_u32 s94, s94, 221184
K 202,180,181
Mvav 152,152,220
X 183
Mvav 156,156,220
X 184
Mvav 124,156,212
s_add_u32 m0, s95, 4096
Ra 152,242,14336
Mvav 92,156,204
K 203,182,183
Mvav 60,156,196
W 3
X 185
Cvaa 160,128,0,224
D 4,20,94,0
Ra 156,242,15360
Mvaa 160,132,4
X 186
Cvaa 164,132,20,228
K 210,184,185
Mvaa 164,128,16
X 187
Cvaa 168,128,32,232
D 5,20,94,1024
Mvaa 168,132,36
X 188
Cvaa 172,132,52,236
D 6,20,94,2048
K 211,186,187
Mvaa 172,128,48
X 189
Mvaa 160,136,8
Ra 128,242,24576
Ra 132,242,25600
Mvaa 160,140,12
X 190
Mvaa 164,140,28
D 7,20,94,3072
K 218,188,189
Mvaa 164,136,24
s_add_u32 s94, s94, 221184
X 191
Mvaa 168,136,40
Mvaa 168,140,44
X 160
Mvaa 172,140,60
s_add_u32 m0, s95, 8192
K 219,190,191
Mvaa 172,136,56
W 2
X 161
Cvaa 176,144,0,224
Ra 136,242,26624
Ra 140,242,27648
Mvaa 176,148,4
X 162
Cvaa 180,148,20,228
D 4,20,94,0
K 196,160,161
Mvaa 180,144,16
X 163
Cvaa 184,144,32,232
D 5,20,94,1024
Mvaa 184,148,36
X 164
Cvaa 188,148,52,236
K 197,162,163
Mvaa 188,144,48
D 6,20,94,2048
Ra 144,242,28672
Mvaa 176,152,8
X 165
Mvaa 176,156,12
Ra 148,242,29696
D 7,20,94,3072
Mvaa 180,156,28
X 166
Mvaa 180,152,24
K 204,164,165
Mvaa 184,152,40
s_add_u32 s94, s94, 221184
X 167
Mvaa 184,156,44
X 168
Mvaa 188,156,60
Mvaa 188,152,56
W 2
s_add_u32 m0, s95, 12288
K 205,166,167
Mvav 32,128,192
X 169
Mvav 64,128,200
Ra 152,242,30720
Ra 156,242,31744
Mvav 96,128,208
X 170
Mvav 128,128,216
D 4,20,94,0
K 212,168,169
Mvav 132,132,216
X 171
Mvav 100,132,208
D 5,20,94,1024
Mvav 68,132,200
X 172
Mvav 36,132,192
K 213,170,171
Mvav 40,136,192
X 173
Mvav 72,136,200
D 6,20,94,2048
Mvav 104,136,208
X 174
Mvav 136,136,216
D 7,20,94,3072
K 220,172,173
Mvav 140,140,216
X 175
Mvav 108,140,208
s_add_u32 s94, s94, 221184
X 176
Mvav 76,140,200
W 0
s_barrier
v_xor_b32 v242, 0x10000, v242
Mvav 44,140,192
Ra 128,242,0
K 221,174,175
Mvav 48,144,192
X 177
Mvav 80,144,200
v_bfe_u32 v11, v1, 4, 3
Mvav 112,144,208
X 178
Mvav 144,144,216
Ra 132,242,1024
v_lshlrev_b32 v11, 3, v11
Mvav 148,148,216
K 198,176,177
Mvav 116,148,208
X 179
Mvav 84,148,200
X 180
Mvav 52,148,192
Ra 136,242,2048
buffer_load_dwordx2 v[8:9], v11, s[8:11], 0 offen offset:3840 sc1
Mvav 56,152,192
Ra 140,242,3072
K 199,178,179
Mvav 88,152,200
X 181
Mvav 120,152,208
X 182
Mvav 152,152,216
K 206,180,181
Mvav 156,156,216
X 183
Mvav 124,156,208
Mvav 92,156,200
K 207,182,183
Mvav 60,156,192
W 0
Cvaa 160,128,0,224
s_add_u32 m0, s95, 16384
Ra 144,242,4096
Ra 148,242,5120
Mvaa 160,132,4
s_xor_b32 s62, s62, 0x10000
Ra 152,242,6144
Cvaa 164,132,20,228
X 184
Mvaa 164,128,16
X 185
Cvaa 168,128,32,232
X 186
Mvaa 168,132,36
D 4,20,94,0
K 214,184,185
Cvaa 172,132,52,236
Ra 156,242,7168
Mvaa 172,128,48
X 187
Mvaa 160,136,8
X 188
Mvaa 160,140,12
D 5,20,94,1024
Ra 128,242,16384
Mvaa 164,140,28
K 215,186,187
Mvaa 164,136,24
X 189
Mvaa 168,136,40
X 190
Mvaa 168,140,44
D 6,20,94,2048
Ra 132,242,17408
Mvaa 172,140,60
K 222,188,189
Mvaa 172,136,56
X 191
v_smfmac_f32_16x16x64_bf16 a[160:163], a[176:179], v[192:199], v240
Ra 136,242,18432
K 223,190,191
v_smfmac_f32_16x16x64_bf16 a[164:167], a[176:179], v[200:207], v240
D 7,20,94,3072
v_smfmac_f32_16x16x64_bf16 a[168:171], a[176:179], v[208:215], v240
s_add_u32 s94, s94, 221184
v_mul_u32_u24 v10, 160, v11
v_smfmac_f32_16x16x64_bf16 a[172:175], a[176:179], v[216:223], v240
W 3
X 160
Cvaa 176,144,0,224
X 161
Mvaa 176,148,4
Ra 140,242,19456
v_lshrrev_b32 v12, 7, v1
Cvaa 180,148,20,228
X 162
Mvaa 180,144,16
K 192,160,161
v_lshl_add_u32 v10, v12, 4, v10
Cvaa 184,144,32,232
s_mul_i32 s82, s14, 5120
X 163
Mvaa 184,148,36
X 164
Cvaa 188,148,52,236
K 193,162,163
Mvaa 188,144,48
s_mul_i32 s81, s14, 640
X 165
Mvaa 176,152,8
X 166
Mvaa 176,156,12
s_add_u32 s81, s81, 118784
Ra 144,242,20480
Ra 148,242,21504
Mvaa 180,156,28
K 200,164,165
Mvaa 180,152,24
X 167
Mvaa 184,152,40
s_waitcnt vmcnt(4)
v_cmp_eq_u64 vcc, s[12:13], v[8:9]
s_cmp_eq_u64 vcc, -1
s_cbranch_scc0 .Lspin_51%=
.Lok_52%=:
X 168
Mvaa 184,156,44
K 201,166,167
Mvaa 188,156,60
s_add_u32 m0, s82, 53248
)"
R"(X 169
Mvaa 188,152,56
W 2
Ra 152,242,22528
Mvav 32,128,196
X 170
Mvav 64,128,204
Ra 156,242,23552
Dc 10,8,81,0
Mvav 96,128,212
K 208,168,169
Mvav 128,128,220
X 171
Mvav 132,132,220
s_add_u32 m0, s82, 54144
X 172
Mvav 100,132,212
s_add_u32 s64, s64, 1
Ra 128,242,8192
K 209,170,171
Mvav 68,132,204
X 173
Mvav 36,132,196
Dc 10,8,81,128
Mvav 40,136,196
X 174
Mvav 72,136,204
s_add_u32 m0, s82, 55040
Ra 132,242,9216
K 216,172,173
Mvav 104,136,212
X 175
Mvav 136,136,220
Dc 10,8,81,256
Mvav 140,140,220
X 176
Mvav 108,140,212
Ra 136,242,10240
K 217,174,175
Mvav 76,140,204
s_add_u32 m0, s82, 55936
X 177
Mvav 44,140,196
W 3
Mvav 48,144,196
X 178
Mvav 80,144,204
Ra 140,242,11264
Dc 10,8,81,384
Mvav 112,144,212
K 194,176,177
Mvav 144,144,220
s_add_u32 m0, s82, 56832
X 179
Mvav 148,148,220
X 180
Mvav 116,148,212
Ra 144,242,12288
Mvav 84,148,204
Dc 10,8,81,512
K 195,178,179
Mvav 52,148,196
X 181
Mvav 56,152,196
X 182
Mvav 88,152,204
Ra 148,242,13312
Mvav 120,152,212
K 202,180,181
Mvav 152,152,220
X 183
Mvav 156,156,220
X 184
Mvav 124,156,212
Ra 152,242,14336
Mvav 92,156,204
K 203,182,183
Mvav 60,156,196
W 3
X 185
Cvaa 160,128,0,224
X 186
Mvaa 160,132,4
Ra 156,242,15360
Cvaa 164,132,20,228
K 210,184,185
Mvaa 164,128,16
X 187
Cvaa 168,128,32,232
X 188
Mvaa 168,132,36
Cvaa 172,132,52,236
K 211,186,187
Mvaa 172,128,48
X 189
Mvaa 160,136,8
Ra 128,242,24576
Ra 132,242,25600
Mvaa 160,140,12
X 190
Mvaa 164,140,28
K 218,188,189
Mvaa 164,136,24
X 191
Mvaa 168,136,40
Mvaa 168,140,44
Mvaa 172,140,60
X 160
Mvaa 172,136,56
W 2
Ra 136,242,26624
K 219,190,191
Cvaa 176,144,0,224
X 161
Mvaa 176,148,4
Ra 140,242,27648
v_lshrrev_b32 v8, 8, v1
Cvaa 180,148,20,228
X 162
Mvaa 180,144,16
K 196,160,161
Cvaa 184,144,32,232
X 163
Mvaa 184,148,36
v_bfe_u32 v9, v1, 6, 2
Cvaa 188,148,52,236
X 164
Mvaa 188,144,48
K 197,162,163
v_lshl_add_u32 v9, v8, 3, v9
Mvaa 176,152,8
X 165
Mvaa 176,156,12
Ra 144,242,28672
Ra 148,242,29696
Mvaa 180,156,28
X 166
Mvaa 180,152,24
K 204,164,165
v_lshlrev_b32 v11, 6, v9
Mvaa 184,152,40
X 167
Mvaa 184,156,44
X 168
Mvaa 188,156,60
v_xor_b32 v9, s14, v8
Mvaa 188,152,56
W 2
Ra 152,242,30720
K 205,166,167
Mvav 32,128,192
X 169
Mvav 64,128,200
Ra 156,242,31744
v_and_b32 v9, 1, v9
Mvav 96,128,208
X 170
Mvav 128,128,216
K 212,168,169
Mvav 132,132,216
X 171
Mvav 100,132,208
v_lshl_add_u32 v11, v9, 5, v11
Mvav 68,132,200
X 172
Mvav 36,132,192
K 213,170,171
Mvav 40,136,192
X 173
Mvav 72,136,200
v_bfe_u32 v9, v1, 4, 2
Mvav 104,136,208
X 174
Mvav 136,136,216
K 220,172,173
v_lshl_add_u32 v12, v9, 3, v11
Mvav 140,140,216
X 175
Mvav 108,140,208
X 176
Mvav 76,140,200
s_waitcnt vmcnt(0) lgkmcnt(0)
s_barrier
v_xor_b32 v242, 0x10000, v242
Mvav 44,140,192
v_add_u32 v11, 98304, v12
K 221,174,175
Mvav 48,144,192
Ra 128,242,0
Mvav 80,144,200
X 177
Mvav 112,144,208
X 178
Mvav 144,144,216
Ra 132,242,1024
v_and_b32 v13, 0x70, v1
Mvav 148,148,216
K 198,176,177
Mvav 116,148,208
X 179
Mvav 84,148,200
Ra 136,242,2048
v_lshl_add_u32 v13, v8, 7, v13
Mvav 52,148,192
X 180
Mvav 56,152,192
Ra 140,242,3072
K 199,178,179
Mvav 88,152,200
X 181
Mvav 120,152,208
X 182
Mvav 152,152,216
K 206,180,181
Mvav 156,156,216
X 183
Mvav 124,156,208
Mvav 92,156,200
K 207,182,183
Mvav 60,156,192
W 0
Cvaa 160,128,0,224
s_cmp_eq_u32 s14, 0
s_cselect_b32 s95, 106496, 0
Ra 144,242,4096
Ra 148,242,5120
Mvaa 160,132,4
s_xor_b32 s62, s62, 0x10000
X 184
Cvaa 164,132,20,228
Ta 184,11,0
Ta 186,11,256
Mvaa 164,128,16
s_add_u32 s95, s95, 32768
Ta 188,11,20480
Ta 190,11,20736
Cvaa 168,128,32,232
Ra 192,13,53248
Ta 196,11,2048
Mvaa 168,132,36
s_add_u32 m0, s95, 0
X 185
Cvaa 172,132,52,236
Ta 198,11,2304
Ta 200,11,22528
Mvaa 172,128,48
W 3
X 186
Zaaa 232,184,192
Ta 202,11,22784
K 214,184,185
Zaaa 236,188,192
Ra 204,13,53760
Ra 152,242,6144
Mvaa 160,136,8
X 187
Mvaa 160,140,12
Ta 208,11,4096
D 4,20,94,0
Mvaa 164,140,28
X 188
Mvaa 164,136,24
Ta 210,11,4352
D 5,20,94,1024
Mvaa 168,136,40
Ra 156,242,7168
K 215,186,187
Mvaa 168,140,44
X 189
Mvaa 172,140,60
Ta 212,11,24576
D 6,20,94,2048
Mvaa 172,136,56
X 190
v_smfmac_f32_16x16x64_bf16 a[160:163], a[176:179], v[192:199], v240
W 5
Ta 214,11,24832
D 7,20,94,3072
Maaa 232,196,204
s_add_u32 s94, s94, 221184
Ra 216,13,54272
K 222,188,189
Maaa 236,200,204
s_add_u32 m0, s95, 4096
X 191
v_smfmac_f32_16x16x64_bf16 a[164:167], a[176:179], v[200:207], v240
X 160
K 223,190,191
v_smfmac_f32_16x16x64_bf16 a[168:171], a[176:179], v[208:215], v240
Ra 128,242,16384
Ta 220,11,6144
v_smfmac_f32_16x16x64_bf16 a[172:175], a[176:179], v[216:223], v240
W 5
Ta 222,11,6400
Ra 132,242,17408
Cvaa 176,144,0,224
X 161
Mvaa 176,148,4
D 4,20,94,0
Ta 224,11,26624
Cvaa 180,148,20,228
X 162
Mvaa 180,144,16
D 5,20,94,1024
K 192,160,161
Cvaa 184,144,32,232
Ta 226,11,26880
Ra 228,13,54784
Mvaa 184,148,36
W 7
X 163
Maaa 232,208,216
D 6,20,94,2048
Ra 136,242,18432
Maaa 236,212,216
X 164
Cvaa 188,148,52,236
D 7,20,94,3072
Ta 184,11,8192
Mvaa 188,144,48
s_add_u32 s94, s94, 221184
Ra 140,242,19456
K 193,162,163
Mvaa 176,152,8
s_add_u32 m0, s95, 8192
X 165
Mvaa 176,156,12
Ta 186,11,8448
Ta 188,11,28672
Mvaa 180,156,28
X 166
Mvaa 180,152,24
Ra 144,242,20480
K 200,164,165
Mvaa 184,152,40
X 167
Mvaa 184,156,44
Ta 190,11,28928
D 4,20,94,0
Mvaa 188,156,60
W 7
X 168
Maaa 232,220,228
s_add_u32 s64, s64, 1
Ra 192,13,55296
D 5,20,94,1024
Maaa 236,224,228
Ta 196,11,10240
K 201,166,167
Mvaa 188,152,56
W 6
X 169
Mvav 32,128,196
D 6,20,94,2048
Ra 148,242,21504
Mvav 64,128,204
X 170
Mvav 96,128,212
Ta 198,11,10496
D 7,20,94,3072
Mvav 128,128,220
s_add_u32 s94, s94, 221184
Ta 200,11,30720
K 208,168,169
Mvav 132,132,220
s_add_u32 m0, s95, 12288
X 171
Mvav 100,132,212
Ra 152,242,22528
Ta 202,11,30976
Mvav 68,132,204
X 172
Mvav 36,132,196
W 6
Ra 204,13,55808
K 209,170,171
Maaa 232,184,192
X 173
Maaa 236,188,192
D 4,20,94,0
Ra 156,242,23552
Mvav 40,136,196
X 174
Mvav 72,136,204
Ta 208,11,12288
D 5,20,94,1024
Mvav 104,136,212
Ta 210,11,12544
K 216,172,173
Mvav 136,136,220
X 175
Mvav 140,140,220
D 6,20,94,2048
Ra 128,242,8192
Mvav 108,140,212
Ta 212,11,32768
Ta 214,11,33024
Mvav 76,140,204
X 176
Mvav 44,140,196
)"
R"(W 5
D 7,20,94,3072
K 217,174,175
Mvav 48,144,196
s_add_u32 s94, s94, 221184
X 177
Maaa 232,196,204
s_add_u32 m0, s95, 16384
Ra 132,242,9216
Ra 216,13,56320
Maaa 236,200,204
X 178
Mvav 80,144,204
Ta 220,11,14336
K 194,176,177
Mvav 112,144,212
X 179
Mvav 144,144,220
D 4,20,94,0
Ta 222,11,14592
Mvav 148,148,220
Ra 136,242,10240
Ta 224,11,34816
Mvav 116,148,212
X 180
Mvav 84,148,204
D 5,20,94,1024
K 195,178,179
Mvav 52,148,196
X 181
Mvav 56,152,196
D 6,20,94,2048
Ta 226,11,35072
Mvav 88,152,204
W 5
Ra 140,242,11264
Ra 228,13,56832
Maaa 232,208,216
X 182
Maaa 236,212,216
D 7,20,94,3072
K 202,180,181
Mvav 120,152,212
s_add_u32 s94, s94, 221184
X 183
Mvav 152,152,220
Ta 184,11,16384
Ra 144,242,12288
Mvav 156,156,220
X 184
Mvav 124,156,212
Ta 186,11,16640
K 203,182,183
Mvav 92,156,204
X 185
Mvav 60,156,196
W 4
Ta 188,11,36864
Ra 148,242,13312
Cvaa 160,128,0,224
X 186
Mvaa 160,132,4
Ta 190,11,37120
K 210,184,185
Cvaa 164,132,20,228
W 6
Ra 192,13,57344
Ta 196,11,18432
Maaa 232,220,228
X 187
Maaa 236,224,228
X 188
Mvaa 164,128,16
Ra 152,242,14336
K 211,186,187
Cvaa 168,128,32,232
Ta 198,11,18688
Ta 200,11,38912
Mvaa 168,132,36
X 189
Cvaa 172,132,52,236
X 190
Mvaa 172,128,48
Ra 156,242,15360
K 218,188,189
Mvaa 160,136,8
Ta 202,11,39168
Ra 204,13,57856
Mvaa 160,140,12
X 191
Mvaa 164,140,28
Ra 128,242,24576
Mvaa 164,136,24
W 8
Maaa 232,184,192
Ra 132,242,25600
Maaa 236,188,192
X 160
Mvaa 168,136,40
K 219,190,191
Mvaa 168,140,44
X 161
Mvaa 172,140,60
X 162
Mvaa 172,136,56
W 4
Ra 136,242,26624
K 196,160,161
Cvaa 176,144,0,224
X 163
Mvaa 176,148,4
X 164
Cvaa 180,148,20,228
Ra 140,242,27648
K 197,162,163
Mvaa 180,144,16
X 165
Cvaa 184,144,32,232
W 4
Maaa 232,196,204
X 166
Maaa 236,200,204
Mvaa 184,148,36
K 204,164,165
Cvaa 188,148,52,236
X 167
Mvaa 188,144,48
Ra 144,242,28672
Mvaa 176,152,8
X 168
Mvaa 176,156,12
Ra 148,242,29696
Mvaa 180,156,28
K 205,166,167
Mvaa 180,152,24
X 169
Mvaa 184,152,40
Mvaa 184,156,44
X 170
Mvaa 188,156,60
K 212,168,169
Mvaa 188,152,56
W 2
X 171
Mvav 32,128,192
Ra 152,242,30720
Ra 156,242,31744
Mvav 64,128,200
X 172
Mvav 96,128,208
Mvav 128,128,216
K 213,170,171
Mvav 132,132,216
X 173
Mvav 100,132,208
Mvav 68,132,200
X 174
Mvav 36,132,192
Mvav 40,136,192
K 220,172,173
Mvav 72,136,200
X 175
Mvav 104,136,208
Mvav 136,136,216
X 176
Mvav 140,140,216
Mvav 108,140,208
K 221,174,175
Mvav 76,140,200
s_waitcnt vmcnt(0) lgkmcnt(0)
s_barrier
v_xor_b32 v242, 0x10000, v242
Mvav 44,140,192
X 177
Mvav 48,144,192
X 178
Mvav 80,144,200
Ra 128,242,0
K 198,176,177
Mvav 112,144,208
X 179
Mvav 144,144,216
Ra 132,242,1024
Mvav 148,148,216
X 180
Mvav 116,148,208
Ra 136,242,2048
Mvav 84,148,200
K 199,178,179
Mvav 52,148,192
X 181
Mvav 56,152,192
Ra 140,242,3072
Mvav 88,152,200
X 182
Mvav 120,152,208
Mvav 152,152,216
K 206,180,181
Mvav 156,156,216
X 183
Mvav 124,156,208
Mvav 92,156,200
K 207,182,183
Mvav 60,156,192
W 0
Cvaa 160,128,0,224
s_mov_b32 s20, s34
Ra 144,242,4096
Ra 148,242,5120
Mvaa 160,132,4
s_xor_b32 s62, s62, 0x10000
X 184
Cvaa 164,132,20,228
s_and_b32 s21, s35, 0xffff
Ta 184,11,40960
Ta 186,11,41216
Mvaa 164,128,16
Ta 188,12,32768
Ta 190,12,33024
Cvaa 168,128,32,232
s_mov_b32 s22, 2211840
Ra 192,13,58368
Ta 196,11,43008
Mvaa 168,132,36
s_mov_b32 s23, 0x20000
X 185
Cvaa 172,132,52,236
Ta 198,11,43264
Ta 200,12,34816
Mvaa 172,128,48
W 3
X 186
Maaa 232,184,192
Ta 202,12,35072
K 214,184,185
Maaa 236,188,192
Ra 204,13,58880
Ra 152,242,6144
Mvaa 160,136,8
X 187
Mvaa 160,140,12
Ta 208,11,45056
v_lshrrev_b32 v4, 8, v1
Mvaa 164,140,28
X 188
Mvaa 164,136,24
Ta 210,11,45312
v_lshlrev_b32 v5, 5, v4
Mvaa 168,136,40
v_xor_b32 v5, v5, v1
K 215,186,187
Mvaa 168,140,44
Ra 156,242,7168
Ta 212,12,36864
Mvaa 172,140,60
X 189
Mvaa 172,136,56
v_and_b32 v5, 0xf0, v5
v_mul_u32_u24 v4, 1280, v4
v_smfmac_f32_16x16x64_bf16 a[160:163], a[176:179], v[192:199], v240
W 5
X 190
Maaa 232,196,204
Ta 214,12,37120
v_add_u32 v4, v4, v5
Maaa 236,200,204
Ra 216,13,59392
K 222,188,189
v_smfmac_f32_16x16x64_bf16 a[164:167], a[176:179], v[200:207], v240
X 191
X 160
K 223,190,191
v_smfmac_f32_16x16x64_bf16 a[168:171], a[176:179], v[208:215], v240
Ra 128,242,16384
v_xor_b32 v5, 128, v4
v_smfmac_f32_16x16x64_bf16 a[172:175], a[176:179], v[216:223], v240
W 4
Ta 220,11,47104
v_add_u32 v6, 8192, v4
Cvaa 176,144,0,224
Ta 222,11,47360
v_add_u32 v7, 12288, v5
Mvaa 176,148,4
Ra 132,242,17408
v_add_u32 v5, 4096, v5
Cvaa 180,148,20,228
s_mul_i32 s94, s85, 40960
X 161
Mvaa 180,144,16
s_mul_i32 s81, s14, 20480
Ta 224,12,38912
Ta 226,12,39168
Cvaa 184,144,32,232
s_add_u32 s94, s94, s81
X 162
Mvaa 184,148,36
W 6
s_lshl_b32 s95, s14, 12
K 192,160,161
Maaa 232,208,216
s_add_u32 s95, s95, 98304
X 163
Maaa 236,212,216
s_add_u32 m0, s95, 0
Ra 228,13,59904
Ra 136,242,18432
Cvaa 188,148,52,236
X 164
Mvaa 188,144,48
Ta 184,11,49152
K 193,162,163
Mvaa 176,152,8
X 165
Mvaa 176,156,12
Ra 140,242,19456
Ta 186,11,49408
Mvaa 180,156,28
D 4,20,94,0
Ta 188,12,40960
Mvaa 180,152,24
X 166
Mvaa 184,152,40
D 5,20,94,1024
K 200,164,165
Mvaa 184,156,44
X 167
Mvaa 188,156,60
W 5
D 6,20,94,2048
Ra 144,242,20480
Maaa 232,220,228
s_add_u32 s64, s64, 1
Ta 190,12,41216
Ra 192,13,60416
Maaa 236,224,228
D 7,20,94,3072
Ta 196,11,51200
Mvaa 188,152,56
W 6
X 168
Mvav 32,128,196
s_add_u32 s94, s94, 256
s_add_u32 m0, s95, 8192
K 201,166,167
Mvav 64,128,204
X 169
Mvav 96,128,212
D 4,20,94,0
Ra 148,242,21504
Mvav 128,128,220
X 170
Mvav 132,132,220
D 5,20,94,1024
Ta 198,11,51456
Mvav 100,132,212
Ta 200,12,43008
K 208,168,169
Mvav 68,132,204
D 6,20,94,2048
Ra 152,242,22528
Mvav 36,132,196
W 5
X 171
Maaa 232,184,192
D 7,20,94,3072
Ta 202,12,43264
Maaa 236,188,192
s_add_u32 s94, s94, 256
X 172
Mvav 40,136,196
s_add_u32 m0, s95, 16384
Ra 204,13,60928
K 209,170,171
Mvav 72,136,204
X 173
Mvav 104,136,212
Ra 156,242,23552
D 4,20,94,0
Mvav 136,136,220
X 174
Mvav 140,140,220
D 5,20,94,1024
Ta 208,11,53248
Mvav 108,140,212
Ta 210,11,53504
K 216,172,173
Mvav 76,140,204
D 6,20,94,2048
Ra 128,242,8192
Mvav 44,140,196
W 3
X 175
Mvav 48,144,196
D 7,20,94,3072
Ta 212,12,45056
Maaa 232,196,204
s_add_u32 s94, s94, 256
X 176
Maaa 236,200,204
s_add_u32 m0, s95, 24576
)"
R"(Ta 214,12,45312
K 217,174,175
Mvav 80,144,204
D 4,20,94,0
Ra 132,242,9216
Mvav 112,144,212
X 177
Mvav 144,144,220
Ra 216,13,61440
D 5,20,94,1024
Mvav 148,148,220
X 178
Mvav 116,148,212
D 6,20,94,2048
Ta 220,11,55296
Mvav 84,148,204
Ta 222,11,55552
K 194,176,177
Mvav 52,148,196
D 7,20,94,3072
Ra 136,242,10240
Mvav 56,152,196
s_add_u32 s94, s94, 256
X 179
Mvav 88,152,204
W 3
X 180
Maaa 232,208,216
s_add_u32 m0, s95, 32768
Ta 224,12,47104
K 195,178,179
Maaa 236,212,216
D 4,20,94,0
Ta 226,12,47360
Mvav 120,152,212
Ra 140,242,11264
Ra 228,13,61952
Mvav 152,152,220
X 181
Mvav 156,156,220
D 5,20,94,1024
Ta 184,11,57344
Mvav 124,156,212
X 182
Mvav 92,156,204
D 6,20,94,2048
K 202,180,181
Mvav 60,156,196
W 2
X 183
Cvaa 160,128,0,224
D 7,20,94,3072
Ra 144,242,12288
Mvaa 160,132,4
s_add_u32 s94, s94, 256
X 184
Cvaa 164,132,20,228
W 2
Ta 186,11,57600
K 203,182,183
Maaa 232,220,228
X 185
Maaa 236,224,228
Ta 188,12,49152
Ra 148,242,13312
Mvaa 164,128,16
X 186
Cvaa 168,128,32,232
Ta 190,12,49408
K 210,184,185
Mvaa 168,132,36
Ra 192,13,62464
Ta 196,11,59392
Cvaa 172,132,52,236
X 187
Mvaa 172,128,48
X 188
Mvaa 160,136,8
Ra 152,242,14336
K 211,186,187
Mvaa 160,140,12
Ta 198,11,59648
Ta 200,12,51200
Mvaa 164,140,28
X 189
Mvaa 164,136,24
W 4
X 190
Maaa 232,184,192
Ra 156,242,15360
K 218,188,189
Maaa 236,188,192
Ta 202,12,51456
Ra 204,13,62976
Mvaa 168,136,40
X 191
Mvaa 168,140,44
Ra 128,242,24576
Ra 132,242,25600
Mvaa 172,140,60
X 160
Mvaa 172,136,56
W 4
Ra 136,242,26624
K 219,190,191
Cvaa 176,144,0,224
X 161
Mvaa 176,148,4
X 162
Cvaa 180,148,20,228
Ra 140,242,27648
K 196,160,161
Mvaa 180,144,16
X 163
Cvaa 184,144,32,232
W 4
X 164
Maaa 232,196,204
K 197,162,163
Maaa 236,200,204
X 165
Mvaa 184,148,36
X 166
Cvaa 188,148,52,236
K 204,164,165
Mvaa 188,144,48
X 167
Mvaa 176,152,8
Ra 144,242,28672
G 252,232
Mvaa 176,156,12
G 253,233
G 254,234
Mvaa 180,156,28
X 168
Mvaa 180,152,24
G 255,235
G 8,236
Mvaa 184,152,40
Ra 148,242,29696
G 9,237
Mvaa 184,156,44
G 10,238
G 14,239
Mvaa 188,156,60
K 252,252,8
K 253,253,9
Mvaa 188,152,56
W 2
K 205,166,167
K 254,254,10
Mvav 32,128,192
K 255,255,14
v_and_b32 v8, 0xffff0000, v252
Mvav 64,128,200
v_and_b32 v9, 0xffff0000, v253
v_and_b32 v10, 0xffff0000, v254
Mvav 96,128,208
X 169
Mvav 128,128,216
v_and_b32 v14, 0xffff0000, v255
v_lshlrev_b32 v252, 16, v252
Mvav 132,132,216
v_lshlrev_b32 v253, 16, v253
v_lshlrev_b32 v254, 16, v254
Mvav 100,132,208
v_lshlrev_b32 v255, 16, v255
v_mul_f32 v15, 0xbfb8aa3b, v252
Mvav 68,132,200
v_mul_f32 v2, 0xbfb8aa3b, v253
v_mul_f32 v3, 0xbfb8aa3b, v254
Mvav 36,132,192
X 170
Mvav 40,136,192
v_mul_f32 v27, 0xbfb8aa3b, v255
K 212,168,169
Mvav 72,136,200
X 15
Mvav 104,136,208
Ra 152,242,30720
Ra 156,242,31744
Mvav 136,136,216
X 2
X 3
X 27
v_add_f32 v15, 1.0, v15
v_add_f32 v2, 1.0, v2
v_add_f32 v3, 1.0, v3
v_add_f32 v27, 1.0, v27
v_rcp_f32 v15, v15
v_rcp_f32 v2, v2
v_rcp_f32 v3, v3
v_rcp_f32 v27, v27
U 252,252,15
U 253,253,2
U 254,254,3
U 255,255,27
K 252,252,252
K 253,253,253
K 254,254,254
K 255,255,255
v_lshlrev_b32 v252, 16, v252
v_lshlrev_b32 v253, 16, v253
v_lshlrev_b32 v254, 16, v254
v_lshlrev_b32 v255, 16, v255
U 252,252,8
U 253,253,9
U 254,254,10
U 255,255,14
K 246,252,253
K 247,254,255
s_lshl_b32 s81, s14, 3
v_add_u32 v2, s81, v1
s_mov_b32 s86, 0x00ff00ff
s_mov_b32 s87, 0x00ff00ff
v_cndmask_b32_e64 v252, 0, v246, s[86:87]
v_cndmask_b32_e64 v253, 0, v247, s[86:87]
v_cndmask_b32_e64 v246, v246, 0, s[86:87]
v_cndmask_b32_e64 v247, v247, 0, s[86:87]
ds_write_b64 v2, v[252:253] offset:63488
ds_write_b64 v2, v[246:247] offset:64512
Mvav 140,140,216
X 171
Mvav 108,140,208
X 172
Mvav 76,140,200
s_waitcnt vmcnt(0) lgkmcnt(0)
s_barrier
v_xor_b32 v242, 0x10000, v242
Mvav 44,140,192
Ra 128,242,0
K 213,170,171
Mvav 48,144,192
X 173
Mvav 80,144,200
X 174
Mvav 112,144,208
Ra 132,242,1024
K 220,172,173
Mvav 144,144,216
X 175
Mvav 148,148,216
X 176
Mvav 116,148,208
Ra 136,242,2048
K 221,174,175
Mvav 84,148,200
X 177
Mvav 52,148,192
X 178
Mvav 56,152,192
Ra 140,242,3072
K 198,176,177
Mvav 88,152,200
X 179
Mvav 120,152,208
X 180
Mvav 152,152,216
K 199,178,179
Mvav 156,156,216
X 181
Mvav 124,156,208
X 182
Mvav 92,156,200
K 206,180,181
Mvav 60,156,192
X 183
s_nop 0
K 207,182,183
W 0
Cvaa 160,128,0,224
Ra 144,242,4096
Ra 148,242,5120
Mvaa 160,132,4
s_xor_b32 s62, s62, 0x10000
X 184
Cvaa 164,132,20,228
Ra 216,1,63488
Ra 220,1,64512
Mvaa 164,128,16
v_lshrrev_b32 v8, 6, v1
v_and_b32 v9, 7, v8
Cvaa 168,128,32,232
s_lshl_b32 s81, s14, 2
X 185
Mvaa 168,132,36
v_xor_b32 v9, s81, v9
v_lshlrev_b32 v9, 5, v9
Cvaa 172,132,52,236
X 186
Mvaa 172,128,48
v_lshl_add_u32 v4, v8, 8, v9
v_bfe_u32 v9, v1, 4, 2
Mvaa 160,136,8
v_lshl_add_u32 v4, v9, 3, v4
v_add_u32 v4, 98304, v4
Mvaa 160,140,12
K 214,184,185
v_xor_b32 v5, 32, v4
Mvaa 164,140,28
v_xor_b32 v6, 64, v4
v_xor_b32 v7, 96, v4
Ta 184,4,0
Ta 186,4,4096
Mvaa 164,136,24
Ta 188,5,0
Ta 190,5,4096
Mvaa 168,136,40
Ra 152,242,6144
Ta 192,6,0
Mvaa 168,140,44
W 4
X 187
Zaaa 232,184,216
Ta 194,6,4096
v_and_b32 v11, 0x70, v1
Mvaa 172,140,60
v_mul_u32_u24 v11, 160, v11
v_lshrrev_b32 v10, 8, v1
Mvaa 172,136,56
X 188
v_smfmac_f32_16x16x64_bf16 a[160:163], a[176:179], v[192:199], v240
Ra 156,242,7168
W 4
Maaa 232,188,220
K 215,186,187
v_lshl_add_u32 v11, v10, 4, v11
v_smfmac_f32_16x16x64_bf16 a[164:167], a[176:179], v[200:207], v240
X 189
X 190
K 222,188,189
X 191
X 160
K 223,190,191
v_smfmac_f32_16x16x64_bf16 a[168:171], a[176:179], v[208:215], v240
Ta 196,7,0
v_bfe_u32 v10, v1, 7, 1
v_smfmac_f32_16x16x64_bf16 a[172:175], a[176:179], v[216:223], v240
W 2
Ta 198,7,4096
v_lshl_add_u32 v10, v10, 6, v11
Zaaa 236,192,216
W 2
s_mul_i32 s94, s85, 20480
s_lshl_b32 s82, s14, 8
Cvaa 176,144,0,224
s_add_u32 s94, s94, s82
Ta 200,4,8192
Ta 202,4,12288
Mvaa 176,148,4
s_add_u32 s94, s94, 2752512
s_lshl_b32 s71, s85, 4
Ra 128,242,16384
Cvaa 180,148,20,228
W 3
s_lshl_b32 s82, s14, 3
Ta 204,5,8192
Maaa 236,196,220
s_add_u32 s71, s71, s82
s_add_u32 s71, s71, 2560
Ta 206,5,12288
Mvaa 180,144,16
X 161
Cvaa 184,144,32,232
Ra 132,242,17408
Ta 208,6,8192
Mvaa 184,148,36
W 5
X 162
Zaaa 240,200,216
Ta 210,6,12288
K 192,160,161
Cvaa 188,148,52,236
buffer_store_dwordx4 a[232:235], v10, s[8:11], s94 offen offset:0 sc1
Ta 212,7,8192
Mvaa 188,144,48
X 163
Mvaa 176,152,8
W 4
X 164
Maaa 240,204,220
Ra 136,242,18432
Ta 214,7,12288
Mvaa 176,156,12
Ta 184,4,16384
K 193,162,163
Mvaa 180,156,28
X 165
Mvaa 180,152,24
W 4
Ta 186,4,20480
Ra 140,242,19456
Zaaa 244,208,216
X 166
Mvaa 184,152,40
buffer_store_dwordx4 a[236:239], v10, s[8:11], s94 offen offset:128 sc1
K 200,164,165
Mvaa 184,156,44
X 167
Mvaa 188,156,60
)"
R"(W 3
Ta 188,5,16384
Ta 190,5,20480
Maaa 244,212,220
s_add_u32 s64, s64, 1
X 168
Mvaa 188,152,56
W 2
Ra 144,242,20480
K 201,166,167
Mvav 32,128,196
Ta 192,6,16384
Ta 194,6,20480
Mvav 64,128,204
X 169
Zaaa 248,184,216
buffer_store_dwordx4 a[240:243], v10, s[8:11], s94 offen offset:512 sc1
Ta 196,7,16384
Mvav 96,128,212
X 170
Mvav 128,128,220
Ra 148,242,21504
K 208,168,169
Mvav 132,132,220
W 5
Ta 198,7,20480
Ta 200,4,24576
Maaa 248,188,220
X 171
Mvav 100,132,212
X 172
Mvav 68,132,204
Ta 202,4,28672
K 209,170,171
Mvav 36,132,196
W 5
Ra 152,242,22528
buffer_store_dwordx4 a[244:247], v10, s[8:11], s94 offen offset:640 sc1
Zaaa 252,192,216
X 173
Mvav 40,136,196
X 174
Mvav 72,136,204
Ta 204,5,24576
Ta 206,5,28672
Mvav 104,136,212
W 5
Ra 156,242,23552
K 216,172,173
Maaa 252,196,220
X 175
Mvav 136,136,220
Ta 208,6,24576
Ta 210,6,28672
Mvav 140,140,220
X 176
Mvav 108,140,212
W 6
buffer_store_dwordx4 a[248:251], v10, s[8:11], s94 offen offset:1024 sc1
K 217,174,175
Zaaa 232,200,216
X 177
Mvav 76,140,204
Ta 212,7,24576
Ra 128,242,8192
Mvav 44,140,196
W 4
Ta 214,7,28672
Ta 184,4,32768
Mvav 48,144,196
X 178
Maaa 232,204,220
Ra 132,242,9216
K 194,176,177
Mvav 80,144,204
X 179
Mvav 112,144,212
Ta 186,4,36864
buffer_store_dwordx4 a[252:255], v10, s[8:11], s94 offen offset:1152 sc1
Mvav 144,144,220
W 6
X 180
Zaaa 236,208,216
Ta 188,5,32768
K 195,178,179
Mvav 148,148,220
X 181
Mvav 116,148,212
Ta 190,5,36864
Ra 136,242,10240
Mvav 84,148,204
W 6
X 182
Maaa 236,212,220
Ta 192,6,32768
K 202,180,181
Mvav 52,148,196
Ta 194,6,36864
Ra 140,242,11264
Mvav 56,152,196
X 183
Mvav 88,152,204
W 6
buffer_store_dwordx4 a[232:235], v10, s[8:11], s94 offen offset:1536 sc1
Ta 196,7,32768
Zaaa 240,184,216
X 184
Mvav 120,152,212
Ta 198,7,36864
K 203,182,183
Mvav 152,152,220
Ra 144,242,12288
Ra 148,242,13312
Mvav 156,156,220
W 8
X 185
Maaa 240,188,220
X 186
Mvav 124,156,212
buffer_store_dwordx4 a[236:239], v10, s[8:11], s94 offen offset:1664 sc1
K 210,184,185
Mvav 92,156,204
X 187
Mvav 60,156,196
W 5
X 188
Zaaa 244,192,216
W 4
Ra 152,242,14336
K 211,186,187
Cvaa 160,128,0,224
X 189
Mvaa 160,132,4
X 190
Cvaa 164,132,20,228
W 3
Ra 156,242,15360
buffer_store_dwordx4 a[240:243], v10, s[8:11], s94 offen offset:2048 sc1
Maaa 244,196,220
K 218,188,189
Mvaa 164,128,16
X 191
Cvaa 168,128,32,232
Mvaa 168,132,36
Cvaa 172,132,52,236
Mvaa 172,128,48
Ra 128,242,24576
Mvaa 160,136,8
Ra 132,242,25600
buffer_store_dwordx4 a[244:247], v10, s[8:11], s94 offen offset:2176 sc1
Mvaa 160,140,12
Mvaa 164,140,28
Mvaa 164,136,24
Mvaa 168,136,40
Mvaa 168,140,44
Mvaa 172,140,60
Mvaa 172,136,56
W 2
Ra 136,242,26624
Cvaa 176,144,0,224
X 160
Mvaa 176,148,4
Ra 140,242,27648
K 219,190,191
Cvaa 180,148,20,228
X 161
Mvaa 180,144,16
X 162
Cvaa 184,144,32,232
K 196,160,161
Mvaa 184,148,36
X 163
Cvaa 188,148,52,236
X 164
Mvaa 188,144,48
Ra 144,242,28672
K 197,162,163
Mvaa 176,152,8
X 165
Mvaa 176,156,12
X 166
Mvaa 180,156,28
Ra 148,242,29696
K 204,164,165
Mvaa 180,152,24
X 167
Mvaa 184,152,40
X 168
Mvaa 184,156,44
K 205,166,167
Mvaa 188,156,60
X 169
Mvaa 188,152,56
W 2
X 170
Mvav 32,128,192
Ra 152,242,30720
K 212,168,169
Mvav 64,128,200
X 171
Mvav 96,128,208
Ra 156,242,31744
Mvav 128,128,216
X 172
Mvav 132,132,216
K 213,170,171
Mvav 100,132,208
X 173
Mvav 68,132,200
X 174
Mvav 36,132,192
Mvav 40,136,192
K 220,172,173
Mvav 72,136,200
X 175
Mvav 104,136,208
Mvav 136,136,216
X 176
Mvav 140,140,216
Mvav 108,140,208
K 221,174,175
Mvav 76,140,200
W 0
s_barrier
v_xor_b32 v242, 0x10000, v242
Mvav 44,140,192
X 177
Mvav 48,144,192
X 178
Mvav 80,144,200
Ra 128,242,0
K 198,176,177
Mvav 112,144,208
X 179
Mvav 144,144,216
Ra 132,242,1024
Mvav 148,148,216
X 180
Mvav 116,148,208
Ra 136,242,2048
Mvav 84,148,200
K 199,178,179
Mvav 52,148,192
X 181
Mvav 56,152,192
Ra 140,242,3072
Mvav 88,152,200
X 182
Mvav 120,152,208
Mvav 152,152,216
K 206,180,181
Mvav 156,156,216
X 183
Mvav 124,156,208
Mvav 92,156,200
s_waitcnt vmcnt(0)
v_mov_b64 v[28:29], s[12:13]
s_mov_b64 exec, 1
buffer_store_dwordx2 v[28:29], off, s[8:11], s71 sc1
s_mov_b64 exec, -1
K 207,182,183
Mvav 60,156,192
s_mov_b32 s85, 99
s_lshl_b32 s65, s57, 1
s_sub_u32 s65, s65, 1
s_branch .Ll0t_46%=
.Lf1r_47%=:
s_lshl_b32 s92, s58, 2
s_add_u32 s91, s92, s51
s_add_u32 s95, s92, 0
s_and_b32 s95, s95, 15
s_mul_i32 s95, s95, 40960
s_mul_i32 s71, s51, 0
s_add_u32 s71, s71, 133120
s_add_u32 m0, s71, s48
s_lshl2_add_u32 s94, s48, s95
D 16,40,94,0
s_add_u32 s95, s92, 1
s_and_b32 s95, s95, 15
s_mul_i32 s95, s95, 40960
s_mul_i32 s71, s51, 0
s_add_u32 s71, s71, 143360
D 17,40,94,1024
D 18,40,94,2048
D 19,40,94,3072
s_add_u32 m0, m0, 4096
s_nop 0
buffer_load_dwordx4 v20, s[40:43], s94 offen lds
s_add_u32 m0, s71, s48
s_lshl2_add_u32 s94, s48, s95
D 16,40,94,0
s_add_u32 s95, s92, 2
s_and_b32 s95, s95, 15
s_mul_i32 s95, s95, 40960
s_mul_i32 s71, s51, 0
s_add_u32 s71, s71, 153600
D 17,40,94,1024
D 18,40,94,2048
D 19,40,94,3072
s_add_u32 m0, m0, 4096
s_nop 0
buffer_load_dwordx4 v20, s[40:43], s94 offen lds
s_add_u32 m0, s71, s48
s_lshl2_add_u32 s94, s48, s95
D 16,40,94,0
D 17,40,94,1024
D 18,40,94,2048
D 19,40,94,3072
s_add_u32 m0, m0, 4096
s_nop 0
buffer_load_dwordx4 v20, s[40:43], s94 offen lds
s_branch .Lf1_40%=
.Lspin_15%=:
s_mov_b32 s84, 0
.Llp_17%=:
s_sleep 2
v_lshrrev_b32 v28, 1, v1
s_lshl_b32 s80, s60, 9
s_add_u32 s80, s80, 40960
buffer_load_dwordx2 v[28:29], v28, s[8:11], s80 offen sc1
s_waitcnt vmcnt(0)
s_add_u32 s84, s84, 1
v_cmp_eq_u32 vcc, s13, v29
s_cmp_eq_u64 vcc, -1
s_cbranch_scc1 .Lok_16%=
s_cmp_lt_u32 s84, 1048576
s_cbranch_scc1 .Llp_17%=
s_branch .Lok_16%=
.Lspin_20%=:
s_mov_b32 s84, 0
.Llp_22%=:
s_sleep 2
v_lshrrev_b32 v28, 1, v1
s_lshl_b32 s80, s60, 9
s_add_u32 s80, s80, 40960
buffer_load_dwordx2 v[28:29], v28, s[8:11], s80 offen sc1
s_waitcnt vmcnt(0)
s_add_u32 s84, s84, 1
v_cmp_eq_u32 vcc, s13, v29
s_cmp_eq_u64 vcc, -1
s_cbranch_scc1 .Lok_21%=
s_cmp_lt_u32 s84, 1048576
s_cbranch_scc1 .Llp_22%=
s_branch .Lok_21%=
.Lrt_23%=:
s_waitcnt vmcnt(0)
s_mov_b32 s86, 40960
buffer_load_dwordx4 v[32:35], v1, s[8:11], s86 offen offset:0 sc1
buffer_load_dwordx4 v[36:39], v1, s[8:11], s86 offen offset:1024 sc1
buffer_load_dwordx4 v[40:43], v1, s[8:11], s86 offen offset:2048 sc1
buffer_load_dwordx4 v[44:47], v1, s[8:11], s86 offen offset:3072 sc1
s_mov_b32 s86, 45056
buffer_load_dwordx4 v[48:51], v1, s[8:11], s86 offen offset:0 sc1
buffer_load_dwordx4 v[52:55], v1, s[8:11], s86 offen offset:1024 sc1
buffer_load_dwordx4 v[56:59], v1, s[8:11], s86 offen offset:2048 sc1
buffer_load_dwordx4 v[60:63], v1, s[8:11], s86 offen offset:3072 sc1
s_mov_b32 s86, 49152
buffer_load_dwordx4 v[64:67], v1, s[8:11], s86 offen offset:0 sc1
buffer_load_dwordx4 v[68:71], v1, s[8:11], s86 offen offset:1024 sc1
buffer_load_dwordx4 v[72:75], v1, s[8:11], s86 offen offset:2048 sc1
buffer_load_dwordx4 v[76:79], v1, s[8:11], s86 offen offset:3072 sc1
s_mov_b32 s86, 53248
buffer_load_dwordx4 v[80:83], v1, s[8:11], s86 offen offset:0 sc1
buffer_load_dwordx4 v[84:87], v1, s[8:11], s86 offen offset:1024 sc1
buffer_load_dwordx4 v[88:91], v1, s[8:11], s86 offen offset:2048 sc1
buffer_load_dwordx4 v[92:95], v1, s[8:11], s86 offen offset:3072 sc1
s_branch .Lrtr_24%=
.Lrt_25%=:
v_lshrrev_b32 v136, 1, v1
s_mov_b32 s84, 0
.LL_27%=:
s_waitcnt vmcnt(0)
v_min3_u32 v96, v33, v35, v95
v_max3_u32 v97, v33, v35, v95
v_min3_u32 v96, v96, v37, v39
v_max3_u32 v97, v97, v37, v39
v_min3_u32 v96, v96, v41, v43
v_max3_u32 v97, v97, v41, v43
v_min3_u32 v96, v96, v45, v47
v_max3_u32 v97, v97, v45, v47
v_min3_u32 v96, v96, v49, v51
v_max3_u32 v97, v97, v49, v51
v_min3_u32 v96, v96, v53, v55
v_max3_u32 v97, v97, v53, v55
v_min3_u32 v96, v96, v57, v59
v_max3_u32 v97, v97, v57, v59
v_min3_u32 v96, v96, v61, v63
v_max3_u32 v97, v97, v61, v63
v_min3_u32 v96, v96, v65, v67
v_max3_u32 v97, v97, v65, v67
v_min3_u32 v96, v96, v69, v71
v_max3_u32 v97, v97, v69, v71
v_min3_u32 v96, v96, v73, v75
v_max3_u32 v97, v97, v73, v75
v_min3_u32 v96, v96, v77, v79
v_max3_u32 v97, v97, v77, v79
v_min3_u32 v96, v96, v81, v83
v_max3_u32 v97, v97, v81, v83
v_min3_u32 v96, v96, v85, v87
v_max3_u32 v97, v97, v85, v87
)"
R"(v_min3_u32 v96, v96, v89, v91
v_max3_u32 v97, v97, v89, v91
v_min_u32 v96, v96, v93
v_max_u32 v97, v97, v93
v_cmp_eq_u32 vcc, s13, v96
v_cmp_eq_u32_e64 s[86:87], s13, v97
s_and_b64 vcc, vcc, s[86:87]
s_cmp_eq_u64 vcc, -1
s_cbranch_scc1 .LL_28%=
s_sleep 2
s_mov_b32 s86, 40960
buffer_load_dwordx4 v[32:35], v1, s[8:11], s86 offen offset:0 sc1
buffer_load_dwordx4 v[36:39], v1, s[8:11], s86 offen offset:1024 sc1
buffer_load_dwordx4 v[40:43], v1, s[8:11], s86 offen offset:2048 sc1
buffer_load_dwordx4 v[44:47], v1, s[8:11], s86 offen offset:3072 sc1
s_mov_b32 s86, 45056
buffer_load_dwordx4 v[48:51], v1, s[8:11], s86 offen offset:0 sc1
buffer_load_dwordx4 v[52:55], v1, s[8:11], s86 offen offset:1024 sc1
buffer_load_dwordx4 v[56:59], v1, s[8:11], s86 offen offset:2048 sc1
buffer_load_dwordx4 v[60:63], v1, s[8:11], s86 offen offset:3072 sc1
s_mov_b32 s86, 49152
buffer_load_dwordx4 v[64:67], v1, s[8:11], s86 offen offset:0 sc1
buffer_load_dwordx4 v[68:71], v1, s[8:11], s86 offen offset:1024 sc1
buffer_load_dwordx4 v[72:75], v1, s[8:11], s86 offen offset:2048 sc1
buffer_load_dwordx4 v[76:79], v1, s[8:11], s86 offen offset:3072 sc1
s_mov_b32 s86, 53248
buffer_load_dwordx4 v[80:83], v1, s[8:11], s86 offen offset:0 sc1
buffer_load_dwordx4 v[84:87], v1, s[8:11], s86 offen offset:1024 sc1
buffer_load_dwordx4 v[88:91], v1, s[8:11], s86 offen offset:2048 sc1
buffer_load_dwordx4 v[92:95], v1, s[8:11], s86 offen offset:3072 sc1
s_add_u32 s84, s84, 1
s_cmp_lt_u32 s84, 1048576
s_cbranch_scc1 .LL_27%=
s_waitcnt vmcnt(0)
.LL_28%=:
v_mov_b32 v96, v32
v_mov_b32 v97, 0
v_mov_b32 v98, 0
v_mov_b32 v100, 0
v_med3_u32 v98, v97, v98, v34
v_med3_u32 v97, v96, v97, v34
v_max_u32 v96, v96, v34
v_med3_u32 v98, v97, v98, v36
v_med3_u32 v97, v96, v97, v36
v_max_u32 v96, v96, v36
v_med3_u32 v98, v97, v98, v38
v_med3_u32 v97, v96, v97, v38
v_max_u32 v96, v96, v38
v_med3_u32 v98, v97, v98, v40
v_med3_u32 v97, v96, v97, v40
v_max_u32 v96, v96, v40
v_med3_u32 v98, v97, v98, v42
v_med3_u32 v97, v96, v97, v42
v_max_u32 v96, v96, v42
v_med3_u32 v98, v97, v98, v44
v_med3_u32 v97, v96, v97, v44
v_max_u32 v96, v96, v44
v_med3_u32 v98, v97, v98, v46
v_med3_u32 v97, v96, v97, v46
v_max_u32 v96, v96, v46
v_med3_u32 v98, v97, v98, v48
v_med3_u32 v97, v96, v97, v48
v_max_u32 v96, v96, v48
v_med3_u32 v98, v97, v98, v50
v_med3_u32 v97, v96, v97, v50
v_max_u32 v96, v96, v50
v_med3_u32 v98, v97, v98, v52
v_med3_u32 v97, v96, v97, v52
v_max_u32 v96, v96, v52
v_med3_u32 v98, v97, v98, v54
v_med3_u32 v97, v96, v97, v54
v_max_u32 v96, v96, v54
v_med3_u32 v98, v97, v98, v56
v_med3_u32 v97, v96, v97, v56
v_max_u32 v96, v96, v56
v_med3_u32 v98, v97, v98, v58
v_med3_u32 v97, v96, v97, v58
v_max_u32 v96, v96, v58
v_med3_u32 v98, v97, v98, v60
v_med3_u32 v97, v96, v97, v60
v_max_u32 v96, v96, v60
v_med3_u32 v98, v97, v98, v62
v_med3_u32 v97, v96, v97, v62
v_max_u32 v96, v96, v62
v_med3_u32 v98, v97, v98, v64
v_med3_u32 v97, v96, v97, v64
v_max_u32 v96, v96, v64
v_med3_u32 v98, v97, v98, v66
v_med3_u32 v97, v96, v97, v66
v_max_u32 v96, v96, v66
v_med3_u32 v98, v97, v98, v68
v_med3_u32 v97, v96, v97, v68
v_max_u32 v96, v96, v68
v_med3_u32 v98, v97, v98, v70
v_med3_u32 v97, v96, v97, v70
v_max_u32 v96, v96, v70
v_med3_u32 v98, v97, v98, v72
v_med3_u32 v97, v96, v97, v72
v_max_u32 v96, v96, v72
v_med3_u32 v98, v97, v98, v74
v_med3_u32 v97, v96, v97, v74
v_max_u32 v96, v96, v74
v_med3_u32 v98, v97, v98, v76
v_med3_u32 v97, v96, v97, v76
v_max_u32 v96, v96, v76
v_med3_u32 v98, v97, v98, v78
v_med3_u32 v97, v96, v97, v78
v_max_u32 v96, v96, v78
v_med3_u32 v98, v97, v98, v80
v_med3_u32 v97, v96, v97, v80
v_max_u32 v96, v96, v80
v_med3_u32 v98, v97, v98, v82
v_med3_u32 v97, v96, v97, v82
v_max_u32 v96, v96, v82
v_med3_u32 v98, v97, v98, v84
v_med3_u32 v97, v96, v97, v84
v_max_u32 v96, v96, v84
v_med3_u32 v98, v97, v98, v86
v_med3_u32 v97, v96, v97, v86
v_max_u32 v96, v96, v86
v_med3_u32 v98, v97, v98, v88
v_med3_u32 v97, v96, v97, v88
v_max_u32 v96, v96, v88
v_med3_u32 v98, v97, v98, v90
v_med3_u32 v97, v96, v97, v90
v_max_u32 v96, v96, v90
v_med3_u32 v98, v97, v98, v92
v_med3_u32 v97, v96, v97, v92
v_max_u32 v96, v96, v92
v_med3_u32 v98, v97, v98, v94
v_med3_u32 v97, v96, v97, v94
v_max_u32 v96, v96, v94
v_mov_b32 v99, v96
s_nop 1
v_max_u32_dpp v99, v99, v99 row_shr:1 row_mask:0xf bank_mask:0xf
s_nop 1
v_max_u32_dpp v99, v99, v99 row_shr:2 row_mask:0xf bank_mask:0xf
s_nop 1
v_max_u32_dpp v99, v99, v99 row_shr:4 row_mask:0xf bank_mask:0xf
s_nop 1
v_max_u32_dpp v99, v99, v99 row_shr:8 row_mask:0xf bank_mask:0xf
s_nop 1
v_max_u32_dpp v99, v99, v99 row_bcast:15 row_mask:0xa bank_mask:0xf
s_nop 1
v_max_u32_dpp v99, v99, v99 row_bcast:31 row_mask:0xc bank_mask:0xf
s_nop 0
v_readlane_b32 s86, v99, 63
s_nop 1
v_cmp_eq_u32 vcc, s86, v96
s_and_b32 s87, s86, 0xffff
s_sub_u32 s87, 2047, s87
v_cndmask_b32 v96, v96, v97, vcc
v_cndmask_b32_e64 v97, v97, 0, vcc
v_writelane_b32 v100, s87, 0
v_mov_b32 v99, v96
s_nop 1
v_max_u32_dpp v99, v99, v99 row_shr:1 row_mask:0xf bank_mask:0xf
s_nop 1
v_max_u32_dpp v99, v99, v99 row_shr:2 row_mask:0xf bank_mask:0xf
s_nop 1
v_max_u32_dpp v99, v99, v99 row_shr:4 row_mask:0xf bank_mask:0xf
s_nop 1
v_max_u32_dpp v99, v99, v99 row_shr:8 row_mask:0xf bank_mask:0xf
s_nop 1
v_max_u32_dpp v99, v99, v99 row_bcast:15 row_mask:0xa bank_mask:0xf
s_nop 1
v_max_u32_dpp v99, v99, v99 row_bcast:31 row_mask:0xc bank_mask:0xf
s_nop 0
v_readlane_b32 s86, v99, 63
s_nop 1
v_cmp_eq_u32 vcc, s86, v96
s_and_b32 s87, s86, 0xffff
s_sub_u32 s87, 2047, s87
v_cndmask_b32 v96, v96, v97, vcc
v_cndmask_b32_e64 v97, v97, 0, vcc
v_writelane_b32 v100, s87, 1
v_mov_b32 v99, v96
s_nop 1
v_max_u32_dpp v99, v99, v99 row_shr:1 row_mask:0xf bank_mask:0xf
s_nop 1
v_max_u32_dpp v99, v99, v99 row_shr:2 row_mask:0xf bank_mask:0xf
s_nop 1
v_max_u32_dpp v99, v99, v99 row_shr:4 row_mask:0xf bank_mask:0xf
s_nop 1
v_max_u32_dpp v99, v99, v99 row_shr:8 row_mask:0xf bank_mask:0xf
s_nop 1
v_max_u32_dpp v99, v99, v99 row_bcast:15 row_mask:0xa bank_mask:0xf
s_nop 1
v_max_u32_dpp v99, v99, v99 row_bcast:31 row_mask:0xc bank_mask:0xf
s_nop 0
v_readlane_b32 s86, v99, 63
s_nop 1
v_cmp_eq_u32 vcc, s86, v96
s_and_b32 s87, s86, 0xffff
s_sub_u32 s87, 2047, s87
v_cndmask_b32 v96, v96, v97, vcc
v_cndmask_b32_e64 v97, v97, 0, vcc
v_writelane_b32 v100, s87, 2
v_mov_b32 v99, v96
s_nop 1
v_max_u32_dpp v99, v99, v99 row_shr:1 row_mask:0xf bank_mask:0xf
s_nop 1
v_max_u32_dpp v99, v99, v99 row_shr:2 row_mask:0xf bank_mask:0xf
s_nop 1
v_max_u32_dpp v99, v99, v99 row_shr:4 row_mask:0xf bank_mask:0xf
s_nop 1
v_max_u32_dpp v99, v99, v99 row_shr:8 row_mask:0xf bank_mask:0xf
s_nop 1
v_max_u32_dpp v99, v99, v99 row_bcast:15 row_mask:0xa bank_mask:0xf
s_nop 1
v_max_u32_dpp v99, v99, v99 row_bcast:31 row_mask:0xc bank_mask:0xf
s_nop 0
v_readlane_b32 s86, v99, 63
s_nop 1
v_cmp_eq_u32 vcc, s86, v96
s_and_b32 s87, s86, 0xffff
s_sub_u32 s87, 2047, s87
v_cndmask_b32 v96, v96, v97, vcc
v_cndmask_b32_e64 v97, v97, 0, vcc
v_writelane_b32 v100, s87, 3
v_mov_b32 v99, v96
s_nop 1
v_max_u32_dpp v99, v99, v99 row_shr:1 row_mask:0xf bank_mask:0xf
s_nop 1
v_max_u32_dpp v99, v99, v99 row_shr:2 row_mask:0xf bank_mask:0xf
s_nop 1
v_max_u32_dpp v99, v99, v99 row_shr:4 row_mask:0xf bank_mask:0xf
s_nop 1
v_max_u32_dpp v99, v99, v99 row_shr:8 row_mask:0xf bank_mask:0xf
s_nop 1
v_max_u32_dpp v99, v99, v99 row_bcast:15 row_mask:0xa bank_mask:0xf
s_nop 1
v_max_u32_dpp v99, v99, v99 row_bcast:31 row_mask:0xc bank_mask:0xf
s_nop 0
v_readlane_b32 s86, v99, 63
s_nop 1
v_cmp_eq_u32 vcc, s86, v96
s_and_b32 s87, s86, 0xffff
s_sub_u32 s87, 2047, s87
v_cndmask_b32 v96, v96, v97, vcc
v_cndmask_b32_e64 v97, v97, 0, vcc
v_writelane_b32 v100, s87, 4
v_mov_b32 v99, v96
s_nop 1
v_max_u32_dpp v99, v99, v99 row_shr:1 row_mask:0xf bank_mask:0xf
s_nop 1
v_max_u32_dpp v99, v99, v99 row_shr:2 row_mask:0xf bank_mask:0xf
s_nop 1
v_max_u32_dpp v99, v99, v99 row_shr:4 row_mask:0xf bank_mask:0xf
s_nop 1
v_max_u32_dpp v99, v99, v99 row_shr:8 row_mask:0xf bank_mask:0xf
s_nop 1
v_max_u32_dpp v99, v99, v99 row_bcast:15 row_mask:0xa bank_mask:0xf
s_nop 1
v_max_u32_dpp v99, v99, v99 row_bcast:31 row_mask:0xc bank_mask:0xf
s_nop 0
v_readlane_b32 s86, v99, 63
s_nop 1
v_cmp_eq_u32 vcc, s86, v96
s_and_b32 s87, s86, 0xffff
s_sub_u32 s87, 2047, s87
v_cndmask_b32 v96, v96, v97, vcc
v_cndmask_b32_e64 v97, v97, 0, vcc
v_writelane_b32 v100, s87, 5
v_mov_b32 v99, v96
s_nop 1
v_max_u32_dpp v99, v99, v99 row_shr:1 row_mask:0xf bank_mask:0xf
s_nop 1
v_max_u32_dpp v99, v99, v99 row_shr:2 row_mask:0xf bank_mask:0xf
s_nop 1
v_max_u32_dpp v99, v99, v99 row_shr:4 row_mask:0xf bank_mask:0xf
s_nop 1
v_max_u32_dpp v99, v99, v99 row_shr:8 row_mask:0xf bank_mask:0xf
s_nop 1
v_max_u32_dpp v99, v99, v99 row_bcast:15 row_mask:0xa bank_mask:0xf
s_nop 1
v_max_u32_dpp v99, v99, v99 row_bcast:31 row_mask:0xc bank_mask:0xf
s_nop 0
v_readlane_b32 s86, v99, 63
s_nop 1
v_cmp_eq_u32 vcc, s86, v96
s_and_b32 s87, s86, 0xffff
s_sub_u32 s87, 2047, s87
v_cndmask_b32 v96, v96, v97, vcc
v_cndmask_b32_e64 v97, v97, 0, vcc
v_writelane_b32 v100, s87, 6
v_mov_b32 v99, v96
s_nop 1
v_max_u32_dpp v99, v99, v99 row_shr:1 row_mask:0xf bank_mask:0xf
s_nop 1
v_max_u32_dpp v99, v99, v99 row_shr:2 row_mask:0xf bank_mask:0xf
s_nop 1
v_max_u32_dpp v99, v99, v99 row_shr:4 row_mask:0xf bank_mask:0xf
s_nop 1
v_max_u32_dpp v99, v99, v99 row_shr:8 row_mask:0xf bank_mask:0xf
s_nop 1
v_max_u32_dpp v99, v99, v99 row_bcast:15 row_mask:0xa bank_mask:0xf
s_nop 1
v_max_u32_dpp v99, v99, v99 row_bcast:31 row_mask:0xc bank_mask:0xf
s_nop 0
v_readlane_b32 s86, v99, 63
s_nop 1
v_cmp_eq_u32 vcc, s86, v96
s_and_b32 s87, s86, 0xffff
s_sub_u32 s87, 2047, s87
v_cndmask_b32 v96, v96, v97, vcc
v_cndmask_b32_e64 v97, v97, 0, vcc
v_writelane_b32 v100, s87, 7
v_mov_b32 v99, v98
s_nop 1
v_max_u32_dpp v99, v99, v99 row_shr:1 row_mask:0xf bank_mask:0xf
s_nop 1
v_max_u32_dpp v99, v99, v99 row_shr:2 row_mask:0xf bank_mask:0xf
s_nop 1
v_max_u32_dpp v99, v99, v99 row_shr:4 row_mask:0xf bank_mask:0xf
s_nop 1
v_max_u32_dpp v99, v99, v99 row_shr:8 row_mask:0xf bank_mask:0xf
s_nop 1
v_max_u32_dpp v99, v99, v99 row_bcast:15 row_mask:0xa bank_mask:0xf
s_nop 1
v_max_u32_dpp v99, v99, v99 row_bcast:31 row_mask:0xc bank_mask:0xf
s_nop 0
v_readlane_b32 s87, v99, 63
s_nop 0
s_cmp_gt_u32 s87, s86
s_cbranch_scc0 .LL_29%=
s_mov_b32 m0, 0
.LL_30%=:
v_max3_u32 v96, v32, v34, v36
v_max3_u32 v96, v96, v38, v40
v_max3_u32 v96, v96, v42, v44
v_max3_u32 v96, v96, v46, v48
v_max3_u32 v96, v96, v50, v52
v_max3_u32 v96, v96, v54, v56
v_max3_u32 v96, v96, v58, v60
v_max3_u32 v96, v96, v62, v64
v_max3_u32 v96, v96, v66, v68
v_max3_u32 v96, v96, v70, v72
v_max3_u32 v96, v96, v74, v76
v_max3_u32 v96, v96, v78, v80
v_max3_u32 v96, v96, v82, v84
v_max3_u32 v96, v96, v86, v88
v_max3_u32 v96, v96, v90, v92
v_max_u32 v96, v96, v94
v_mov_b32 v99, v96
s_nop 1
v_max_u32_dpp v99, v99, v99 row_shr:1 row_mask:0xf bank_mask:0xf
s_nop 1
v_max_u32_dpp v99, v99, v99 row_shr:2 row_mask:0xf bank_mask:0xf
s_nop 1
v_max_u32_dpp v99, v99, v99 row_shr:4 row_mask:0xf bank_mask:0xf
s_nop 1
v_max_u32_dpp v99, v99, v99 row_shr:8 row_mask:0xf bank_mask:0xf
s_nop 1
v_max_u32_dpp v99, v99, v99 row_bcast:15 row_mask:0xa bank_mask:0xf
s_nop 1
v_max_u32_dpp v99, v99, v99 row_bcast:31 row_mask:0xc bank_mask:0xf
s_nop 0
v_readlane_b32 s86, v99, 63
s_nop 1
v_cmp_eq_u32 vcc, s86, v32
s_nop 1
v_cndmask_b32_e64 v32, v32, 0, vcc
v_cmp_eq_u32 vcc, s86, v34
s_nop 1
v_cndmask_b32_e64 v34, v34, 0, vcc
v_cmp_eq_u32 vcc, s86, v36
s_nop 1
v_cndmask_b32_e64 v36, v36, 0, vcc
v_cmp_eq_u32 vcc, s86, v38
s_nop 1
v_cndmask_b32_e64 v38, v38, 0, vcc
v_cmp_eq_u32 vcc, s86, v40
s_nop 1
v_cndmask_b32_e64 v40, v40, 0, vcc
v_cmp_eq_u32 vcc, s86, v42
s_nop 1
v_cndmask_b32_e64 v42, v42, 0, vcc
v_cmp_eq_u32 vcc, s86, v44
s_nop 1
v_cndmask_b32_e64 v44, v44, 0, vcc
v_cmp_eq_u32 vcc, s86, v46
s_nop 1
v_cndmask_b32_e64 v46, v46, 0, vcc
v_cmp_eq_u32 vcc, s86, v48
s_nop 1
v_cndmask_b32_e64 v48, v48, 0, vcc
v_cmp_eq_u32 vcc, s86, v50
s_nop 1
v_cndmask_b32_e64 v50, v50, 0, vcc
v_cmp_eq_u32 vcc, s86, v52
s_nop 1
v_cndmask_b32_e64 v52, v52, 0, vcc
v_cmp_eq_u32 vcc, s86, v54
s_nop 1
v_cndmask_b32_e64 v54, v54, 0, vcc
v_cmp_eq_u32 vcc, s86, v56
s_nop 1
v_cndmask_b32_e64 v56, v56, 0, vcc
v_cmp_eq_u32 vcc, s86, v58
)"
R"(s_nop 1
v_cndmask_b32_e64 v58, v58, 0, vcc
v_cmp_eq_u32 vcc, s86, v60
s_nop 1
v_cndmask_b32_e64 v60, v60, 0, vcc
v_cmp_eq_u32 vcc, s86, v62
s_nop 1
v_cndmask_b32_e64 v62, v62, 0, vcc
v_cmp_eq_u32 vcc, s86, v64
s_nop 1
v_cndmask_b32_e64 v64, v64, 0, vcc
v_cmp_eq_u32 vcc, s86, v66
s_nop 1
v_cndmask_b32_e64 v66, v66, 0, vcc
v_cmp_eq_u32 vcc, s86, v68
s_nop 1
v_cndmask_b32_e64 v68, v68, 0, vcc
v_cmp_eq_u32 vcc, s86, v70
s_nop 1
v_cndmask_b32_e64 v70, v70, 0, vcc
v_cmp_eq_u32 vcc, s86, v72
s_nop 1
v_cndmask_b32_e64 v72, v72, 0, vcc
v_cmp_eq_u32 vcc, s86, v74
s_nop 1
v_cndmask_b32_e64 v74, v74, 0, vcc
v_cmp_eq_u32 vcc, s86, v76
s_nop 1
v_cndmask_b32_e64 v76, v76, 0, vcc
v_cmp_eq_u32 vcc, s86, v78
s_nop 1
v_cndmask_b32_e64 v78, v78, 0, vcc
v_cmp_eq_u32 vcc, s86, v80
s_nop 1
v_cndmask_b32_e64 v80, v80, 0, vcc
v_cmp_eq_u32 vcc, s86, v82
s_nop 1
v_cndmask_b32_e64 v82, v82, 0, vcc
v_cmp_eq_u32 vcc, s86, v84
s_nop 1
v_cndmask_b32_e64 v84, v84, 0, vcc
v_cmp_eq_u32 vcc, s86, v86
s_nop 1
v_cndmask_b32_e64 v86, v86, 0, vcc
v_cmp_eq_u32 vcc, s86, v88
s_nop 1
v_cndmask_b32_e64 v88, v88, 0, vcc
v_cmp_eq_u32 vcc, s86, v90
s_nop 1
v_cndmask_b32_e64 v90, v90, 0, vcc
v_cmp_eq_u32 vcc, s86, v92
s_nop 1
v_cndmask_b32_e64 v92, v92, 0, vcc
v_cmp_eq_u32 vcc, s86, v94
s_nop 1
v_cndmask_b32_e64 v94, v94, 0, vcc
s_and_b32 s87, s86, 0xffff
s_sub_u32 s87, 2047, s87
s_nop 0
v_writelane_b32 v100, s87, m0
s_add_u32 m0, m0, 1
s_cmp_lt_u32 m0, 8
s_cbranch_scc1 .LL_30%=
.LL_29%=:
v_xor_b32 v102, s12, v100
v_mov_b32 v103, s13
s_mov_b64 exec, 0xff
buffer_store_dwordx2 v[102:103], v136, s[8:11], 0 offen offset:3584 sc1
s_mov_b64 exec, -1
s_branch .Lrtr_26%=
.Lspin_51%=:
s_mov_b32 s84, 0
.Llp_53%=:
s_sleep 2
buffer_load_dwordx2 v[8:9], v11, s[8:11], 0 offen offset:3840 sc1
s_waitcnt vmcnt(0)
s_add_u32 s84, s84, 1
v_cmp_eq_u64 vcc, s[12:13], v[8:9]
s_cmp_eq_u64 vcc, -1
s_cbranch_scc1 .Lok_52%=
s_cmp_lt_u32 s84, 1048576
s_cbranch_scc1 .Llp_53%=
s_branch .Lok_52%=
.Lredo%=:
s_waitcnt vmcnt(0) lgkmcnt(0)
s_barrier
s_and_b32 s81, s14, 1
s_mul_i32 s91, s81, 17164288
s_mul_i32 s82, s56, 536576
s_add_u32 s91, s91, s82
s_add_u32 s91, s91, 4194304
s_lshr_b32 s82, s14, 1
s_lshl_b32 s83, s82, 14
s_add_u32 s91, s91, s83
s_lshl_b32 s81, s81, 14
s_lshl_b32 s82, s82, 16
s_or_b32 s92, s81, s82
s_xor_b32 s92, s92, 0x10000
s_add_u32 m0, s92, 0
s_add_u32 s81, s91, 0
Dc 1,8,81,0
Dc 1,8,81,1024
Dc 1,8,81,2048
Dc 1,8,81,3072
s_add_u32 m0, s92, 4096
s_add_u32 s81, s91, 4096
Dc 1,8,81,0
Dc 1,8,81,1024
Dc 1,8,81,2048
Dc 1,8,81,3072
s_add_u32 m0, s92, 8192
s_add_u32 s81, s91, 8192
Dc 1,8,81,0
Dc 1,8,81,1024
Dc 1,8,81,2048
Dc 1,8,81,3072
s_add_u32 m0, s92, 12288
s_add_u32 s81, s91, 12288
Dc 1,8,81,0
Dc 1,8,81,1024
Dc 1,8,81,2048
Dc 1,8,81,3072
v_bfe_u32 v244, v1, 4, 1
v_mul_u32_u24 v244, 0xaaaa, v244
v_add_u32 v244, 0x4444, v244
v_lshl_or_b32 v240, v244, 16, v244
v_mov_b32 v241, 0xff800000
v_add_u32 v242, 0x10000, v1
v_bfe_u32 v244, v1, 4, 4
v_lshrrev_b32 v245, 8, v1
v_lshlrev_b32 v245, 2, v245
v_sub_u32 v244, v244, v245
v_cmp_le_i32_e64 s[72:73], 0, v244
v_cmp_le_i32_e64 s[74:75], 1, v244
v_cmp_le_i32_e64 s[76:77], 2, v244
v_cmp_le_i32_e64 s[78:79], 3, v244
v_add_u32 v243, 4096, v1
v_add_u32 v244, 8192, v1
v_add_u32 v245, 12288, v1
s_mov_b32 s18, 0x3f803f80
s_mov_b32 s19, 0x3f003f00
v_accvgpr_write_b32 a176, s18
v_accvgpr_write_b32 a180, s19
v_mov_b32 v252, v241
v_accvgpr_write_b32 a177, s18
v_accvgpr_write_b32 a181, s19
v_mov_b32 v253, v241
v_accvgpr_write_b32 a178, s18
v_accvgpr_write_b32 a182, s19
v_mov_b32 v254, v241
v_accvgpr_write_b32 a179, s18
v_accvgpr_write_b32 a183, s19
v_mov_b32 v255, v241
v_accvgpr_write_b32 a160, 0
v_accvgpr_write_b32 a161, 0
v_accvgpr_write_b32 a162, 0
v_accvgpr_write_b32 a163, 0
v_accvgpr_write_b32 a164, 0
v_accvgpr_write_b32 a165, 0
v_accvgpr_write_b32 a166, 0
v_accvgpr_write_b32 a167, 0
v_accvgpr_write_b32 a168, 0
v_accvgpr_write_b32 a169, 0
v_accvgpr_write_b32 a170, 0
v_accvgpr_write_b32 a171, 0
v_accvgpr_write_b32 a172, 0
v_accvgpr_write_b32 a173, 0
v_accvgpr_write_b32 a174, 0
v_accvgpr_write_b32 a175, 0
s_mov_b32 s90, 0x5d800000
s_mov_b64 s[88:89], 0
s_mov_b32 s64, 0
s_and_b32 s18, s14, 1
s_lshl_b32 s62, s18, 14
s_lshl_b32 s20, s57, 15
s_sub_u32 s20, 507904, s20
s_add_u32 s20, s20, s62
s_lshl_b32 s65, s57, 1
s_sub_u32 s65, s65, 1
s_max_i32 s65, s65, 0
s_sub_u32 s66, s60, 1
s_cmp_lt_u32 s14, 2
s_cselect_b32 s20, s20, 0
s_cselect_b32 s66, s66, 15
s_cselect_b32 s21, 32768, 0
s_add_u32 s61, s91, s20
s_add_u32 s62, s62, s21
s_sub_u32 s63, 16384, s21
s_lshl_b32 s67, s60, 7
s_add_u32 s67, s67, s56
s_lshl_b32 s67, s67, 12
s_add_u32 s67, s67, 39845888
s_lshl_b32 s69, s60, 3
s_add_u32 s69, s69, s59
s_lshl_b32 s69, s69, 6
s_lshl_b32 s21, s58, 3
s_add_u32 s69, s69, s21
s_add_u32 s69, s69, 20480
s_mov_b32 s70, 0
s_mov_b32 s37, 0
v_bfe_u32 v28, v1, 4, 4
v_lshrrev_b32 v29, 8, v1
v_lshlrev_b32 v29, 2, v29
v_sub_u32 v28, v28, v29
s_mov_b64 s[24:25], 0
s_mov_b32 s68, 0
s_mov_b32 s85, 99
s_lshl_b32 s80, s60, 6
s_and_b32 s81, s14, 2
s_lshl_b32 s81, s81, 4
s_xor_b32 s82, s81, 0
s_add_u32 s82, s82, s80
v_add_u32 v4, s82, v28
s_xor_b32 s82, s81, 16
s_add_u32 s82, s82, s80
v_add_u32 v5, s82, v28
s_xor_b32 s82, s81, 32
s_add_u32 s82, s82, s80
v_add_u32 v6, s82, v28
s_xor_b32 s82, s81, 48
s_add_u32 s82, s82, s80
v_add_u32 v7, s82, v28
s_mul_i32 s82, s56, 536576
s_add_u32 s82, s82, 4194304
s_lshl_b32 s84, s60, 1
s_add_u32 s84, s84, 2
.LL_54%=:
buffer_load_dwordx4 a[128:131], v1, s[8:11], s82 offen offset:0 sc1
buffer_load_dwordx4 a[132:135], v1, s[8:11], s82 offen offset:1024 sc1
buffer_load_dwordx4 a[136:139], v1, s[8:11], s82 offen offset:2048 sc1
buffer_load_dwordx4 a[140:143], v1, s[8:11], s82 offen offset:3072 sc1
buffer_load_dwordx4 a[144:147], v243, s[8:11], s82 offen offset:0 sc1
buffer_load_dwordx4 a[148:151], v243, s[8:11], s82 offen offset:1024 sc1
buffer_load_dwordx4 a[152:155], v243, s[8:11], s82 offen offset:2048 sc1
buffer_load_dwordx4 a[156:159], v243, s[8:11], s82 offen offset:3072 sc1
s_waitcnt vmcnt(0)
Zvaa 160,128,0
Zvaa 164,128,16
Zvaa 168,128,32
Zvaa 172,128,48
Mvaa 160,132,4
Mvaa 164,132,20
Mvaa 168,132,36
Mvaa 172,132,52
Mvaa 160,136,8
Mvaa 164,136,24
Mvaa 168,136,40
Mvaa 172,136,56
Mvaa 160,140,12
Mvaa 164,140,28
Mvaa 168,140,44
Mvaa 172,140,60
Zvaa 176,144,0
Zvaa 180,144,16
Zvaa 184,144,32
Zvaa 188,144,48
Mvaa 176,148,4
Mvaa 180,148,20
Mvaa 184,148,36
Mvaa 188,148,52
Mvaa 176,152,8
Mvaa 180,152,24
Mvaa 184,152,40
Mvaa 188,152,56
Mvaa 176,156,12
Mvaa 180,156,28
Mvaa 184,156,44
Mvaa 188,156,60
v_cmp_le_i32 vcc, 0, v4
s_nop 1
v_cndmask_b32 v160, v241, v160, vcc
v_max_f32 v252, v252, v160
v_cmp_le_i32 vcc, 1, v4
s_nop 1
v_cndmask_b32 v161, v241, v161, vcc
v_max_f32 v252, v252, v161
v_cmp_le_i32 vcc, 2, v4
s_nop 1
v_cndmask_b32 v162, v241, v162, vcc
v_max_f32 v252, v252, v162
v_cmp_le_i32 vcc, 3, v4
s_nop 1
v_cndmask_b32 v163, v241, v163, vcc
v_max_f32 v252, v252, v163
v_cmp_le_i32 vcc, 0, v5
s_nop 1
v_cndmask_b32 v164, v241, v164, vcc
v_max_f32 v253, v253, v164
v_cmp_le_i32 vcc, 1, v5
s_nop 1
v_cndmask_b32 v165, v241, v165, vcc
v_max_f32 v253, v253, v165
v_cmp_le_i32 vcc, 2, v5
s_nop 1
v_cndmask_b32 v166, v241, v166, vcc
v_max_f32 v253, v253, v166
v_cmp_le_i32 vcc, 3, v5
s_nop 1
v_cndmask_b32 v167, v241, v167, vcc
v_max_f32 v253, v253, v167
v_cmp_le_i32 vcc, 0, v6
s_nop 1
v_cndmask_b32 v168, v241, v168, vcc
v_max_f32 v254, v254, v168
v_cmp_le_i32 vcc, 1, v6
s_nop 1
v_cndmask_b32 v169, v241, v169, vcc
v_max_f32 v254, v254, v169
v_cmp_le_i32 vcc, 2, v6
s_nop 1
v_cndmask_b32 v170, v241, v170, vcc
v_max_f32 v254, v254, v170
v_cmp_le_i32 vcc, 3, v6
s_nop 1
v_cndmask_b32 v171, v241, v171, vcc
v_max_f32 v254, v254, v171
v_cmp_le_i32 vcc, 0, v7
s_nop 1
v_cndmask_b32 v172, v241, v172, vcc
v_max_f32 v255, v255, v172
v_cmp_le_i32 vcc, 1, v7
s_nop 1
v_cndmask_b32 v173, v241, v173, vcc
v_max_f32 v255, v255, v173
v_cmp_le_i32 vcc, 2, v7
s_nop 1
v_cndmask_b32 v174, v241, v174, vcc
v_max_f32 v255, v255, v174
v_cmp_le_i32 vcc, 3, v7
s_nop 1
v_cndmask_b32 v175, v241, v175, vcc
v_max_f32 v255, v255, v175
v_cmp_le_i32 vcc, 16, v4
s_nop 1
v_cndmask_b32 v176, v241, v176, vcc
v_max_f32 v252, v252, v176
v_cmp_le_i32 vcc, 17, v4
s_nop 1
v_cndmask_b32 v177, v241, v177, vcc
v_max_f32 v252, v252, v177
v_cmp_le_i32 vcc, 18, v4
s_nop 1
v_cndmask_b32 v178, v241, v178, vcc
v_max_f32 v252, v252, v178
v_cmp_le_i32 vcc, 19, v4
s_nop 1
v_cndmask_b32 v179, v241, v179, vcc
v_max_f32 v252, v252, v179
v_cmp_le_i32 vcc, 16, v5
s_nop 1
v_cndmask_b32 v180, v241, v180, vcc
v_max_f32 v253, v253, v180
v_cmp_le_i32 vcc, 17, v5
s_nop 1
v_cndmask_b32 v181, v241, v181, vcc
v_max_f32 v253, v253, v181
v_cmp_le_i32 vcc, 18, v5
s_nop 1
v_cndmask_b32 v182, v241, v182, vcc
v_max_f32 v253, v253, v182
v_cmp_le_i32 vcc, 19, v5
s_nop 1
v_cndmask_b32 v183, v241, v183, vcc
v_max_f32 v253, v253, v183
v_cmp_le_i32 vcc, 16, v6
s_nop 1
v_cndmask_b32 v184, v241, v184, vcc
v_max_f32 v254, v254, v184
v_cmp_le_i32 vcc, 17, v6
s_nop 1
v_cndmask_b32 v185, v241, v185, vcc
v_max_f32 v254, v254, v185
v_cmp_le_i32 vcc, 18, v6
s_nop 1
v_cndmask_b32 v186, v241, v186, vcc
v_max_f32 v254, v254, v186
v_cmp_le_i32 vcc, 19, v6
s_nop 1
v_cndmask_b32 v187, v241, v187, vcc
v_max_f32 v254, v254, v187
v_cmp_le_i32 vcc, 16, v7
s_nop 1
v_cndmask_b32 v188, v241, v188, vcc
v_max_f32 v255, v255, v188
v_cmp_le_i32 vcc, 17, v7
s_nop 1
v_cndmask_b32 v189, v241, v189, vcc
v_max_f32 v255, v255, v189
v_cmp_le_i32 vcc, 18, v7
s_nop 1
v_cndmask_b32 v190, v241, v190, vcc
v_max_f32 v255, v255, v190
v_cmp_le_i32 vcc, 19, v7
s_nop 1
v_cndmask_b32 v191, v241, v191, vcc
v_max_f32 v255, v255, v191
v_subrev_u32 v4, 32, v4
v_subrev_u32 v5, 32, v5
v_subrev_u32 v6, 32, v6
v_subrev_u32 v7, 32, v7
)"
R"(s_add_u32 s82, s82, 8192
s_sub_u32 s84, s84, 1
s_cmp_lg_u32 s84, 0
s_cbranch_scc1 .LL_54%=
G 28,0
v_lshlrev_b32 v32, 16, v28
v_and_b32 v33, 0xffff0000, v28
G 28,1
v_lshlrev_b32 v34, 16, v28
v_and_b32 v35, 0xffff0000, v28
G 28,2
v_lshlrev_b32 v36, 16, v28
v_and_b32 v37, 0xffff0000, v28
G 28,3
v_lshlrev_b32 v38, 16, v28
v_and_b32 v39, 0xffff0000, v28
G 28,4
v_lshlrev_b32 v40, 16, v28
v_and_b32 v41, 0xffff0000, v28
G 28,5
v_lshlrev_b32 v42, 16, v28
v_and_b32 v43, 0xffff0000, v28
G 28,6
v_lshlrev_b32 v44, 16, v28
v_and_b32 v45, 0xffff0000, v28
G 28,7
v_lshlrev_b32 v46, 16, v28
v_and_b32 v47, 0xffff0000, v28
G 28,8
v_lshlrev_b32 v48, 16, v28
v_and_b32 v49, 0xffff0000, v28
G 28,9
v_lshlrev_b32 v50, 16, v28
v_and_b32 v51, 0xffff0000, v28
G 28,10
v_lshlrev_b32 v52, 16, v28
v_and_b32 v53, 0xffff0000, v28
G 28,11
v_lshlrev_b32 v54, 16, v28
v_and_b32 v55, 0xffff0000, v28
G 28,12
v_lshlrev_b32 v56, 16, v28
v_and_b32 v57, 0xffff0000, v28
G 28,13
v_lshlrev_b32 v58, 16, v28
v_and_b32 v59, 0xffff0000, v28
G 28,14
v_lshlrev_b32 v60, 16, v28
v_and_b32 v61, 0xffff0000, v28
G 28,15
v_lshlrev_b32 v62, 16, v28
v_and_b32 v63, 0xffff0000, v28
G 28,16
v_lshlrev_b32 v64, 16, v28
v_and_b32 v65, 0xffff0000, v28
G 28,17
v_lshlrev_b32 v66, 16, v28
v_and_b32 v67, 0xffff0000, v28
G 28,18
v_lshlrev_b32 v68, 16, v28
v_and_b32 v69, 0xffff0000, v28
G 28,19
v_lshlrev_b32 v70, 16, v28
v_and_b32 v71, 0xffff0000, v28
G 28,20
v_lshlrev_b32 v72, 16, v28
v_and_b32 v73, 0xffff0000, v28
G 28,21
v_lshlrev_b32 v74, 16, v28
v_and_b32 v75, 0xffff0000, v28
G 28,22
v_lshlrev_b32 v76, 16, v28
v_and_b32 v77, 0xffff0000, v28
G 28,23
v_lshlrev_b32 v78, 16, v28
v_and_b32 v79, 0xffff0000, v28
G 28,24
v_lshlrev_b32 v80, 16, v28
v_and_b32 v81, 0xffff0000, v28
G 28,25
v_lshlrev_b32 v82, 16, v28
v_and_b32 v83, 0xffff0000, v28
G 28,26
v_lshlrev_b32 v84, 16, v28
v_and_b32 v85, 0xffff0000, v28
G 28,27
v_lshlrev_b32 v86, 16, v28
v_and_b32 v87, 0xffff0000, v28
G 28,28
v_lshlrev_b32 v88, 16, v28
v_and_b32 v89, 0xffff0000, v28
G 28,29
v_lshlrev_b32 v90, 16, v28
v_and_b32 v91, 0xffff0000, v28
G 28,30
v_lshlrev_b32 v92, 16, v28
v_and_b32 v93, 0xffff0000, v28
G 28,31
v_lshlrev_b32 v94, 16, v28
v_and_b32 v95, 0xffff0000, v28
G 28,32
v_lshlrev_b32 v96, 16, v28
v_and_b32 v97, 0xffff0000, v28
G 28,33
v_lshlrev_b32 v98, 16, v28
v_and_b32 v99, 0xffff0000, v28
G 28,34
v_lshlrev_b32 v100, 16, v28
v_and_b32 v101, 0xffff0000, v28
G 28,35
v_lshlrev_b32 v102, 16, v28
v_and_b32 v103, 0xffff0000, v28
G 28,36
v_lshlrev_b32 v104, 16, v28
v_and_b32 v105, 0xffff0000, v28
G 28,37
v_lshlrev_b32 v106, 16, v28
v_and_b32 v107, 0xffff0000, v28
G 28,38
v_lshlrev_b32 v108, 16, v28
v_and_b32 v109, 0xffff0000, v28
G 28,39
v_lshlrev_b32 v110, 16, v28
v_and_b32 v111, 0xffff0000, v28
G 28,40
v_lshlrev_b32 v112, 16, v28
v_and_b32 v113, 0xffff0000, v28
G 28,41
v_lshlrev_b32 v114, 16, v28
v_and_b32 v115, 0xffff0000, v28
G 28,42
v_lshlrev_b32 v116, 16, v28
v_and_b32 v117, 0xffff0000, v28
G 28,43
v_lshlrev_b32 v118, 16, v28
v_and_b32 v119, 0xffff0000, v28
G 28,44
v_lshlrev_b32 v120, 16, v28
v_and_b32 v121, 0xffff0000, v28
G 28,45
v_lshlrev_b32 v122, 16, v28
v_and_b32 v123, 0xffff0000, v28
G 28,46
v_lshlrev_b32 v124, 16, v28
v_and_b32 v125, 0xffff0000, v28
G 28,47
v_lshlrev_b32 v126, 16, v28
v_and_b32 v127, 0xffff0000, v28
G 28,48
v_lshlrev_b32 v128, 16, v28
v_and_b32 v129, 0xffff0000, v28
G 28,49
v_lshlrev_b32 v130, 16, v28
v_and_b32 v131, 0xffff0000, v28
G 28,50
v_lshlrev_b32 v132, 16, v28
v_and_b32 v133, 0xffff0000, v28
G 28,51
v_lshlrev_b32 v134, 16, v28
v_and_b32 v135, 0xffff0000, v28
G 28,52
v_lshlrev_b32 v136, 16, v28
v_and_b32 v137, 0xffff0000, v28
G 28,53
v_lshlrev_b32 v138, 16, v28
v_and_b32 v139, 0xffff0000, v28
G 28,54
v_lshlrev_b32 v140, 16, v28
v_and_b32 v141, 0xffff0000, v28
G 28,55
v_lshlrev_b32 v142, 16, v28
v_and_b32 v143, 0xffff0000, v28
G 28,56
v_lshlrev_b32 v144, 16, v28
v_and_b32 v145, 0xffff0000, v28
G 28,57
v_lshlrev_b32 v146, 16, v28
v_and_b32 v147, 0xffff0000, v28
G 28,58
v_lshlrev_b32 v148, 16, v28
v_and_b32 v149, 0xffff0000, v28
G 28,59
v_lshlrev_b32 v150, 16, v28
v_and_b32 v151, 0xffff0000, v28
G 28,60
v_lshlrev_b32 v152, 16, v28
v_and_b32 v153, 0xffff0000, v28
G 28,61
v_lshlrev_b32 v154, 16, v28
v_and_b32 v155, 0xffff0000, v28
G 28,62
v_lshlrev_b32 v156, 16, v28
v_and_b32 v157, 0xffff0000, v28
G 28,63
v_lshlrev_b32 v158, 16, v28
v_and_b32 v159, 0xffff0000, v28
s_mov_b32 s22, 1.0
s_mov_b32 s23, 1.0
s_mov_b32 s29, 134217728
s_mov_b32 s71, 3584
s_barrier
Ra 128,242,0
Ra 132,242,1024
Ra 136,242,2048
Ra 140,242,3072
s_cmp_ge_u32 s14, 2
s_cbranch_scc1 .Lbgo%=
s_branch .Lago%=
.p2align 12
.Lpb_31%=:
s_waitcnt vmcnt(6) lgkmcnt(6)
Zvaa 32,0,32
s_add_u32 s28, s28, 512
Ta 20,4,8192
Ta 22,4,9216
Mvaa 32,4,48
s_cmp_eq_u32 s28, s4
s_cselect_b32 s93, 10240, 0
Ta 24,5,0
Ta 26,5,1024
Zvaa 64,4,52
s_sub_u32 s28, s28, s93
s_add_u32 s93, s28, 20480
buffer_load_dwordx4 a[128:131], v26, s[8:11], s28 offen offset:0 sc1
Mvaa 64,0,36
Ta 28,5,8192
Ta 30,5,9216
Zvaa 96,0,40
K 184,184,185
K 185,186,187
Mvaa 96,4,56
K 186,188,189
K 187,190,191
Zvaa 128,4,60
K 192,192,193
Mvaa 128,0,44
W 8
buffer_load_dwordx4 a[132:135], v26, s[8:11], s93 offen offset:0 sc1
K 193,194,195
Zvaa 132,8,44
Ta 0,6,0
Ta 2,6,1024
Mvaa 132,12,60
K 194,196,197
K 195,198,199
Zvaa 100,12,56
K 200,200,201
K 201,202,203
Mvaa 100,8,40
Ta 4,6,8192
Ta 6,6,9216
Zvaa 68,8,36
buffer_load_dwordx4 a[136:139], v27, s[8:11], s28 offen offset:0 sc1
K 202,204,205
Mvaa 68,12,52
K 203,206,207
K 208,208,209
Zvaa 36,12,48
K 209,210,211
K 210,212,213
Mvaa 36,8,32
W 8
Ta 8,7,0
K 211,214,215
Zvaa 40,16,32
Ta 10,7,1024
K 216,216,217
Mvaa 40,20,48
buffer_load_dwordx4 a[140:143], v27, s[8:11], s93 offen offset:0 sc1
K 217,218,219
Zvaa 72,20,52
s_add_u32 s28, s28, 512
s_cmp_eq_u32 s28, s4
s_cselect_b32 s93, 10240, 0
s_sub_u32 s28, s28, s93
Mvaa 72,16,36
s_add_u32 s93, s28, 20480
K 218,220,221
K 219,222,223
Zvaa 104,16,40
Ta 12,7,8192
Ta 14,7,9216
Mvaa 104,20,56
K 224,224,225
K 225,226,227
Zvaa 136,20,60
K 226,228,229
K 227,230,231
Mvaa 136,16,44
W 8
buffer_load_dwordx4 a[144:147], v26, s[8:11], s28 offen offset:0 sc1
K 232,232,233
Zvaa 140,24,44
K 233,234,235
K 234,236,237
Mvaa 140,28,60
Ta 16,8,0
K 235,238,239
Zvaa 108,28,56
Ta 18,8,1024
K 240,240,241
Mvaa 108,24,40
K 241,242,243
K 242,244,245
Zvaa 76,24,36
buffer_load_dwordx4 a[148:151], v26, s[8:11], s93 offen offset:0 sc1
K 243,246,247
Mvaa 76,28,52
Ta 20,8,8192
Ta 22,8,9216
Zvaa 44,28,48
K 248,248,249
K 249,250,251
Mvaa 44,24,32
K 250,252,253
K 251,254,255
W 8
Zvaa 48,0,32
Ta 24,9,0
Ta 26,9,1024
Mvaa 48,4,48
buffer_load_dwordx4 a[152:155], v27, s[8:11], s28 offen offset:0 sc1
K 160,160,161
Zvaa 80,4,52
K 161,162,163
K 162,164,165
Mvaa 80,0,36
Ta 28,9,8192
K 163,166,167
Zvaa 112,0,40
Ta 30,9,9216
K 168,168,169
Mvaa 112,4,56
K 169,170,171
K 170,172,173
Zvaa 144,4,60
buffer_load_dwordx4 a[156:159], v27, s[8:11], s93 offen offset:0 sc1
K 171,174,175
Mvaa 144,0,44
W 8
s_add_u32 s28, s28, 512
s_cmp_eq_u32 s28, s4
s_cselect_b32 s93, 10240, 0
Zvaa 148,8,44
s_sub_u32 s28, s28, s93
s_add_u32 s93, s28, 20480
G 164,184
Mvaa 148,12,60
G 165,185
G 166,186
Zvaa 116,12,56
Ta 0,2,16384
Ta 2,2,17408
Mvaa 116,8,40
buffer_load_dwordx4 v[188:191], v26, s[8:11], s28 offen offset:0 sc1
G 167,187
Zvaa 84,8,36
Ta 4,2,24576
G 172,188
Mvaa 84,12,52
Ta 6,2,25600
G 173,189
Zvaa 52,12,48
G 174,190
G 175,191
Mvaa 52,8,32
W 8
Ta 8,3,16384
K 164,164,165
Zvaa 56,16,32
buffer_load_dwordx4 v[196:199], v26, s[8:11], s93 offen offset:0 sc1
Ta 10,3,17408
Mvaa 56,20,48
K 165,166,167
K 166,172,173
Zvaa 88,20,52
Ta 12,3,24576
K 167,174,175
Mvaa 88,16,36
Ta 14,3,25600
v_accvgpr_write_b32 a184, v164
Zvaa 120,16,40
Mvaa 120,20,56
buffer_load_dwordx4 v[204:207], v27, s[8:11], s28 offen offset:0 sc1
v_accvgpr_write_b32 a185, v165
Zvaa 152,20,60
v_accvgpr_write_b32 a186, v166
Mvaa 152,16,44
W 8
Ta 16,4,16384
v_accvgpr_write_b32 a187, v167
Zvaa 156,24,44
Ta 18,4,17408
v_accvgpr_write_b32 a188, v240
Mvaa 156,28,60
Zvaa 124,28,56
buffer_load_dwordx4 v[212:215], v27, s[8:11], s93 offen offset:0 sc1
v_accvgpr_write_b32 a189, v241
Mvaa 124,24,40
s_add_u32 s28, s28, 512
)"
R"(s_cmp_eq_u32 s28, s4
s_cselect_b32 s93, 10240, 0
s_sub_u32 s28, s28, s93
Zvaa 92,24,36
s_add_u32 s93, s28, 20480
Ta 20,4,24576
Ta 22,4,25600
Mvaa 92,28,52
v_accvgpr_write_b32 a190, v242
v_accvgpr_write_b32 a191, v243
Zvaa 60,28,48
G 164,192
Mvaa 60,24,32
s_waitcnt vmcnt(4) lgkmcnt(8)
buffer_load_dwordx4 v[220:223], v26, s[8:11], s28 offen offset:0 sc1
G 165,193
Mvaa 32,0,128
Ta 24,5,16384
Ta 26,5,17408
Mvaa 32,4,144
G 166,194
G 167,195
Mvaa 64,4,148
Ta 28,5,24576
Ta 30,5,25600
Mvaa 64,0,132
G 172,196
Mvaa 96,0,136
buffer_load_dwordx4 v[228:231], v26, s[8:11], s93 offen offset:0 sc1
G 173,197
Mvaa 96,4,152
G 174,198
Mvaa 128,4,156
G 175,199
Mvaa 128,0,140
W 8
Ta 0,6,16384
Ta 2,6,17408
Mvaa 132,8,140
K 164,164,165
Mvaa 132,12,156
buffer_load_dwordx4 v[236:239], v27, s[8:11], s28 offen offset:0 sc1
K 165,166,167
Mvaa 100,12,152
Ta 4,6,24576
K 166,172,173
Mvaa 100,8,136
Ta 6,6,25600
K 167,174,175
Mvaa 68,8,132
v_accvgpr_write_b32 a192, v164
Mvaa 68,12,148
Mvaa 36,12,144
buffer_load_dwordx4 v[244:247], v27, s[8:11], s93 offen offset:0 sc1
v_accvgpr_write_b32 a193, v165
Mvaa 36,8,128
W 8
s_add_u32 s28, s28, 512
s_cmp_eq_u32 s28, s4
s_cselect_b32 s93, 10240, 0
Mvaa 40,16,128
s_sub_u32 s28, s28, s93
s_add_u32 s93, s28, 20480
v_accvgpr_write_b32 a194, v166
Mvaa 40,20,144
Ta 8,7,16384
Ta 10,7,17408
Mvaa 72,20,148
v_accvgpr_write_b32 a195, v167
v_accvgpr_write_b32 a196, v248
Mvaa 72,16,132
D 22,48,65,2048
Ta 12,7,24576
Mvaa 104,16,136
Ta 14,7,25600
v_accvgpr_write_b32 a197, v249
Mvaa 104,20,152
v_accvgpr_write_b32 a198, v250
v_accvgpr_write_b32 a199, v251
Mvaa 136,20,156
G 164,200
Mvaa 136,16,140
W 8
Ta 16,8,16384
Ta 18,8,17408
Mvaa 140,24,140
buffer_load_dwordx4 a[32:35], v26, s[8:11], s28 offen offset:0 sc1
G 165,201
Mvaa 140,28,156
G 166,202
Mvaa 108,28,152
Ta 20,8,24576
G 167,203
Mvaa 108,24,136
Ta 22,8,25600
G 172,204
Mvaa 76,24,132
Mvaa 76,28,148
buffer_load_dwordx4 a[36:39], v26, s[8:11], s93 offen offset:0 sc1
G 173,205
Mvaa 44,28,144
G 174,206
Mvaa 44,24,128
W 8
Ta 24,9,16384
G 175,207
Mvaa 48,0,128
Ta 26,9,17408
K 164,164,165
Mvaa 48,4,144
Mvaa 80,4,148
buffer_load_dwordx4 a[40:43], v27, s[8:11], s28 offen offset:0 sc1
K 165,166,167
Mvaa 80,0,132
Ta 28,9,24576
Ta 30,9,25600
Mvaa 112,0,136
s_lshl_b32 s80, s56, 9
K 166,172,173
K 167,174,175
Mvaa 112,4,152
v_accvgpr_write_b32 a200, v164
Mvaa 144,4,156
s_lshl_b32 s81, s57, 4
Mvaa 144,0,140
W 8
buffer_load_dwordx4 a[44:47], v27, s[8:11], s93 offen offset:0 sc1
v_accvgpr_write_b32 a201, v165
Mvaa 148,8,140
s_add_u32 s28, s28, 512
s_cmp_eq_u32 s28, s4
s_cselect_b32 s93, 10240, 0
s_sub_u32 s28, s28, s93
Mvaa 148,12,156
s_add_u32 s93, s28, 20480
s_add_u32 s80, s80, s81
Ta 0,10,0
Mvaa 116,12,152
s_and_b32 s81, s14, 1
Ta 2,10,1024
v_accvgpr_write_b32 a202, v166
Mvaa 116,8,136
s_lshl_b32 s81, s81, 3
Ta 4,10,8192
v_accvgpr_write_b32 a203, v167
Mvaa 84,8,132
s_add_u32 s80, s80, s81
buffer_load_dwordx4 a[48:51], v26, s[8:11], s28 offen offset:0 sc1
Ta 6,10,9216
Mvaa 84,12,148
s_add_u32 s71, s80, 4096
v_accvgpr_write_b32 a204, v184
v_accvgpr_write_b32 a205, v185
Mvaa 52,12,144
v_accvgpr_write_b32 a206, v186
v_accvgpr_write_b32 a207, v187
Mvaa 52,8,128
W 8
Ta 8,11,0
Ta 10,11,1024
Mvaa 56,16,128
G 164,208
Mvaa 56,20,144
D 23,48,65,3072
G 165,209
Mvaa 88,20,148
s_add_u32 s65, s5, 16
s_cmp_ge_u32 s65, 20
s_cselect_b32 s90, 20, 0
s_sub_u32 s65, s65, s90
Mvaa 88,16,132
s_lshl_b32 s65, s65, 18
s_add_u32 m0, s63, 98304
G 166,210
Mvaa 120,16,136
Ta 12,11,8192
Ta 14,11,9216
Mvaa 120,20,152
G 167,211
G 172,212
Mvaa 152,20,156
buffer_load_dwordx4 a[52:55], v26, s[8:11], s93 offen offset:0 sc1
G 173,213
Mvaa 152,16,140
W 8
Ta 16,12,0
G 174,214
Mvaa 156,24,140
s_lshr_b32 s80, s15, 3
Ta 18,12,1024
Mvaa 156,28,156
G 175,215
Mvaa 124,28,152
s_lshl_b32 s80, s80, 1
Ta 20,12,8192
K 164,164,165
Mvaa 124,24,136
s_add_u32 s80, s80, s14
buffer_load_dwordx4 a[56:59], v27, s[8:11], s28 offen offset:0 sc1
Ta 22,12,9216
Mvaa 92,24,132
s_sub_u32 s80, s80, 2
K 165,166,167
K 166,172,173
Mvaa 92,28,148
s_lshl_b32 s80, s80, 12
Mvaa 60,28,144
K 167,174,175
v_min_u32 v28, 496, v1
Mvaa 60,24,128
s_waitcnt vmcnt(9) lgkmcnt(8)
v_accvgpr_write_b32 a208, v164
v_lshl_add_u32 v28, v28, 3, s80
Mvav 32,0,188
buffer_load_dwordx4 a[60:63], v27, s[8:11], s93 offen offset:0 sc1
Ta 24,13,0
Mvav 32,4,220
s_add_u32 s28, s28, 512
s_cmp_eq_u32 s28, s4
s_cselect_b32 s93, 10240, 0
s_sub_u32 s28, s28, s93
Mvav 64,4,228
s_add_u32 s93, s28, 20480
Ta 26,13,1024
v_min_u32 v28, .Lend%=-.Lbeg%=-4, v28
Mvav 64,0,196
v_accvgpr_write_b32 a209, v165
v_accvgpr_write_b32 a210, v166
Mvav 96,0,204
Ta 28,13,8192
Ta 30,13,9216
Mvav 96,4,236
D 16,48,65,0
v_accvgpr_write_b32 a211, v167
Mvav 128,4,244
v_accvgpr_write_b32 a212, v192
v_accvgpr_write_b32 a213, v193
Mvav 128,0,212
W 8
Ta 0,14,0
v_accvgpr_write_b32 a214, v194
Mvav 132,8,212
Ta 2,14,1024
v_add_u32 v2, 65536, v10
Mvav 132,12,244
v_accvgpr_write_b32 a215, v195
G 164,216
Mvav 100,12,236
buffer_load_dwordx4 v[184:187], v26, s[8:11], s28 offen offset:0 sc1
Ta 4,14,8192
Mvav 100,8,204
Ta 6,14,9216
v_add_u32 v3, 65536, v11
Mvav 68,8,196
G 165,217
G 166,218
Mvav 68,12,228
G 167,219
Mvav 36,12,220
G 172,220
Mvav 36,8,188
W 8
D 17,48,65,1024
Ta 8,15,0
Mvav 40,16,188
Ta 10,15,1024
v_add_u32 v4, 65536, v12
Mvav 40,20,220
G 173,221
G 174,222
Mvav 72,20,228
Ta 12,15,8192
G 175,223
Mvav 72,16,196
Ta 14,15,9216
v_add_u32 v5, 65536, v13
Mvav 104,16,204
buffer_load_dwordx4 a[128:131], v26, s[8:11], s93 offen offset:0 sc1
K 164,164,165
Mvav 104,20,236
K 165,166,167
Mvav 136,20,244
K 166,172,173
Mvav 136,16,212
W 8
Ta 16,24,0
K 167,174,175
Mvav 140,24,212
Ta 18,24,1024
v_add_u32 v6, 65536, v14
Mvav 140,28,244
buffer_load_dwordx4 a[132:135], v27, s[8:11], s28 offen offset:0 sc1
v_accvgpr_write_b32 a216, v164
Mvav 108,28,236
Ta 20,24,8192
v_accvgpr_write_b32 a217, v165
Mvav 108,24,204
Ta 22,24,9216
v_add_u32 v7, 65536, v15
Mvav 76,24,196
v_accvgpr_write_b32 a218, v166
v_accvgpr_write_b32 a219, v167
Mvav 76,28,228
v_accvgpr_write_b32 a220, v200
Mvav 44,28,220
buffer_load_dwordx4 a[136:139], v27, s[8:11], s93 offen offset:0 sc1
Mvav 44,24,188
W 8
s_add_u32 s28, s28, 512
s_cmp_eq_u32 s28, s4
s_cselect_b32 s93, 10240, 0
Mvav 48,0,188
s_sub_u32 s28, s28, s93
s_add_u32 s93, s28, 20480
v_accvgpr_write_b32 a221, v201
Mvav 48,4,220
Ta 24,25,0
Ta 26,25,1024
Mvav 80,4,228
v_add_u32 v8, 65536, v24
v_accvgpr_write_b32 a222, v202
Mvav 80,0,196
buffer_load_dwordx4 a[140:143], v26, s[8:11], s28 offen offset:0 sc1
v_accvgpr_write_b32 a223, v203
Mvav 112,0,204
Ta 28,25,8192
Ta 30,25,9216
Mvav 112,4,236
v_add_u32 v9, 65536, v25
G 164,224
Mvav 144,4,244
G 165,225
G 166,226
Mvav 144,0,212
W 8
Ta 0,10,16384
G 167,227
Mvav 148,8,212
global_load_dword v29, v28, s[6:7]
Ta 2,10,17408
Mvav 148,12,244
s_sub_u32 s74, s85, 66
s_and_b32 s75, s74, 0xffffffe3
s_cselect_b64 s[72:73], -1, 0
s_and_b32 s74, s74, 28
Mvav 116,12,236
s_lshl_b32 s74, s74, 1
v_mov_b32 v28, s74
G 172,228
Mvav 116,8,204
G 173,229
G 174,230
Mvav 84,8,196
Ta 4,10,24576
Ta 6,10,25600
Mvav 84,12,228
buffer_load_dwordx4 a[144:147], v26, s[8:11], s93 offen offset:0 sc1
G 175,231
Mvav 52,12,220
K 164,164,165
K 165,166,167
Mvav 52,8,188
W 8
Ta 8,11,16384
K 166,172,173
Mvav 56,16,188
Ta 10,11,17408
K 167,174,175
Mvav 56,20,220
v_accvgpr_write_b32 a224, v164
Mvav 88,20,228
buffer_load_dwordx4 a[148:151], v27, s[8:11], s28 offen offset:0 sc1
Ta 12,11,24576
Mvav 88,16,196
Ta 14,11,25600
v_accvgpr_write_b32 a225, v165
Mvav 120,16,204
v_accvgpr_write_b32 a226, v166
Mvav 120,20,236
v_accvgpr_write_b32 a227, v167
Mvav 152,20,244
v_accvgpr_write_b32 a228, v208
Mvav 152,16,212
W 8
D 18,48,65,2048
Ta 16,12,16384
Mvav 156,24,212
Ta 18,12,17408
v_accvgpr_write_b32 a229, v209
Mvav 156,28,244
v_accvgpr_write_b32 a230, v210
Mvav 124,28,236
Ta 20,12,24576
v_accvgpr_write_b32 a231, v211
Mvav 124,24,204
Ta 22,12,25600
G 164,232
Mvav 92,24,196
buffer_load_dwordx4 a[152:155], v27, s[8:11], s93 offen offset:0 sc1
G 165,233
)"
R"(Mvav 92,28,228
s_add_u32 s28, s28, 512
s_cmp_eq_u32 s28, s4
s_cselect_b32 s93, 10240, 0
s_sub_u32 s28, s28, s93
Mvav 60,28,220
s_add_u32 s93, s28, 20480
G 166,234
G 167,235
Mvav 60,24,188
s_waitcnt vmcnt(12) lgkmcnt(8)
G 172,236
G 173,237
Mvaa 32,0,32
Ta 24,13,16384
Ta 26,13,17408
Mvaa 32,4,48
buffer_load_dwordx2 v[28:29], v28, s[8:11], 0 offen offset:3584 sc1
G 174,238
Mvaa 64,4,52
G 175,239
K 164,164,165
Mvaa 64,0,36
Ta 28,13,24576
Ta 30,13,25600
Mvaa 96,0,40
K 165,166,167
K 166,172,173
Mvaa 96,4,56
K 167,174,175
v_accvgpr_write_b32 a232, v164
Mvaa 128,4,60
buffer_load_dwordx4 a[156:159], v26, s[8:11], s28 offen offset:0 sc1
v_accvgpr_write_b32 a233, v165
Mvaa 128,0,44
W 8
v_accvgpr_write_b32 a234, v166
v_accvgpr_write_b32 a235, v167
Mvaa 132,8,44
Ta 0,14,16384
Ta 2,14,17408
Mvaa 132,12,60
G 164,160
G 165,161
Mvaa 100,12,56
G 166,162
G 167,163
Mvaa 100,8,40
buffer_load_dwordx4 v[192:195], v26, s[8:11], s93 offen offset:0 sc1
Ta 4,14,24576
Mvaa 68,8,36
Ta 6,14,25600
G 172,164
Mvaa 68,12,52
G 173,165
G 174,166
Mvaa 36,12,48
G 175,167
K 164,164,165
Mvaa 36,8,32
W 8
Ta 8,15,16384
K 165,166,167
Mvaa 40,16,32
buffer_load_dwordx4 v[200:203], v27, s[8:11], s28 offen offset:0 sc1
Ta 10,15,17408
Mvaa 40,20,48
K 166,172,173
K 167,174,175
Mvaa 72,20,52
s_cmp_lg_u32 s72, 0
s_cbranch_scc1 .LL_55%=
s_waitcnt vmcnt(3)
.LL_55%=:
v_xor_b32 v28, s12, v28
v_xor_b32 v29, s13, v29
v_lshrrev_b32 v248, 11, v28
v_or_b32 v29, v29, v248
v_cmp_eq_u32 vcc, 0, v29
s_or_b64 vcc, vcc, s[72:73]
s_cmp_eq_u64 vcc, -1
s_cbranch_scc0 .Lspin_56%=
.Lok_57%=:
v_accvgpr_write_b32 a236, v164
v_accvgpr_write_b32 a237, v165
Mvaa 72,16,36
Ta 12,15,24576
Ta 14,15,25600
Mvaa 104,16,40
v_accvgpr_write_b32 a238, v166
v_accvgpr_write_b32 a239, v167
Mvaa 104,20,56
D 19,48,65,3072
G 164,240
Mvaa 136,20,60
s_add_u32 m0, s63, 102400
G 165,241
G 166,242
Mvaa 136,16,44
W 8
v_bfe_u32 v248, v28, 3, 8
G 167,243
Mvaa 140,24,44
Ta 16,24,16384
Ta 18,24,17408
Mvaa 140,28,60
v_mul_u32_u24 v248, 10240, v248
G 172,244
Mvaa 108,28,56
buffer_load_dwordx4 v[188:191], v27, s[8:11], s93 offen offset:0 sc1
G 173,245
Mvaa 108,24,40
s_add_u32 s28, s28, 512
s_cmp_eq_u32 s28, s4
s_cselect_b32 s93, 10240, 0
s_sub_u32 s28, s28, s93
Mvaa 76,24,36
s_add_u32 s93, s28, 20480
v_and_b32 v28, 7, v28
G 174,246
Mvaa 76,28,52
v_lshl_add_u32 v248, v28, 4, v248
G 175,247
Mvaa 44,28,48
Ta 20,24,24576
Ta 22,24,25600
Mvaa 44,24,32
W 8
buffer_load_dwordx4 v[196:199], v26, s[8:11], s28 offen offset:0 sc1
K 164,164,165
Mvaa 48,0,32
v_lshl_add_u32 v248, v1, 3, v248
K 165,166,167
Mvaa 48,4,48
v_lshrrev_b32 v29, 8, v1
K 166,172,173
Mvaa 80,4,52
K 167,174,175
v_lshlrev_b32 v29, 28, v29
Mvaa 80,0,36
v_accvgpr_write_b32 a240, v164
v_or_b32 v249, v248, v29
Mvaa 112,0,40
s_and_b32 s75, s72, 134217728
buffer_load_dwordx4 v[204:207], v26, s[8:11], s93 offen offset:0 sc1
v_accvgpr_write_b32 a241, v165
Mvaa 112,4,56
s_add_u32 s76, s75, 131072
Ta 24,25,16384
Ta 26,25,17408
Mvaa 144,4,60
v_accvgpr_write_b32 a242, v166
v_accvgpr_write_b32 a243, v167
Mvaa 144,0,44
W 6
G 164,168
G 165,169
Mvaa 148,8,44
Ta 28,25,24576
Ta 30,25,25600
Mvaa 148,12,60
D 20,48,65,0
G 166,170
Mvaa 116,12,56
G 167,171
G 172,172
Mvaa 116,8,40
G 173,173
G 174,174
Mvaa 84,8,36
Ta 0,2,0
G 175,175
Mvaa 84,12,52
Ta 2,2,1024
K 164,164,165
Mvaa 52,12,48
buffer_load_dwordx4 v[212:215], v27, s[8:11], s28 offen offset:0 sc1
K 165,166,167
Mvaa 52,8,32
W 6
K 166,172,173
K 167,174,175
Mvaa 56,16,32
Ta 4,2,8192
Ta 6,2,9216
Mvaa 56,20,48
v_accvgpr_write_b32 a244, v164
v_accvgpr_write_b32 a245, v165
Mvaa 88,20,52
v_accvgpr_write_b32 a246, v166
v_accvgpr_write_b32 a247, v167
Mvaa 88,16,36
buffer_load_dwordx4 v[220:223], v27, s[8:11], s93 offen offset:0 sc1
G 164,64
Mvaa 120,16,40
s_add_u32 s28, s28, 512
s_cmp_eq_u32 s28, s4
s_cselect_b32 s93, 10240, 0
s_sub_u32 s28, s28, s93
Mvaa 120,20,56
s_add_u32 s93, s28, 20480
Ta 8,3,0
G 165,65
Mvaa 152,20,60
Ta 10,3,1024
G 166,66
Mvaa 152,16,44
W 6
G 167,67
G 172,68
Mvaa 156,24,44
buffer_load_dwordx4 v[228:231], v26, s[8:11], s28 offen offset:0 sc1
G 173,69
Mvaa 156,28,60
Ta 12,3,8192
Ta 14,3,9216
Mvaa 124,28,56
G 174,70
G 175,71
Mvaa 124,24,40
K 164,164,165
K 165,166,167
Mvaa 92,24,36
K 166,172,173
K 167,174,175
Mvaa 92,28,52
buffer_load_dwordx4 v[240:243], v248, s[8:11], s76 offen sc1
Ta 16,4,0
Mvaa 60,28,48
s_add_u32 s76, s76, 8192
Ta 18,4,1024
v_accvgpr_write_b32 a64, v164
Mvaa 60,24,32
s_waitcnt vmcnt(13) lgkmcnt(6)
v_accvgpr_write_b32 a65, v165
v_accvgpr_write_b32 a66, v166
Mvav 32,0,184
Ta 20,4,8192
v_accvgpr_write_b32 a67, v167
Mvaa 32,4,140
Ta 22,4,9216
G 164,176
Mvaa 64,4,144
buffer_load_dwordx4 v[244:247], v249, s[8:11], s76 offen sc1
v_or_b32 v249, v1, v29
Mvaa 64,0,128
s_mul_i32 s76, s74, 160
s_add_u32 s76, s76, s75
s_add_u32 s76, s76, 118784
Mvaa 96,0,132
G 165,177
G 166,178
Mvaa 96,4,148
G 167,179
G 172,180
Mvaa 128,4,152
Ta 24,5,0
G 173,181
Mvaa 128,0,136
W 5
buffer_load_dwordx4 v[236:239], v26, s[8:11], s93 offen offset:0 sc1
Ta 26,5,1024
Mvaa 132,8,136
G 174,182
G 175,183
Mvaa 132,12,152
K 164,164,165
K 165,166,167
Mvaa 100,12,148
Ta 28,5,8192
Ta 30,5,9216
Mvaa 100,8,132
K 166,172,173
K 167,174,175
Mvaa 68,8,128
D 21,48,65,1024
v_accvgpr_write_b32 a68, v164
Mvaa 68,12,144
v_accvgpr_write_b32 a69, v165
v_accvgpr_write_b32 a70, v166
Mvaa 36,12,140
Ta 0,6,0
v_accvgpr_write_b32 a71, v167
Mvav 36,8,184
W 5
Ta 2,6,1024
G 164,72
Mvav 40,16,184
G 165,73
G 166,74
Mvaa 40,20,140
buffer_load_dwordx4 v[208:211], v27, s[8:11], s28 offen offset:0 sc1
Ta 4,6,8192
Mvaa 72,20,144
Ta 6,6,9216
G 167,75
Mvaa 72,16,128
G 172,76
G 173,77
Mvaa 104,16,132
Ta 8,7,0
G 174,78
Mvaa 104,20,148
Ta 10,7,1024
G 175,79
Mvaa 136,20,152
buffer_load_dwordx4 a[32:35], v27, s[8:11], s93 offen offset:0 sc1
K 164,164,165
Mvaa 136,16,136
W 6
s_add_u32 s28, s28, 512
s_cmp_eq_u32 s28, s4
s_cselect_b32 s93, 10240, 0
Mvaa 140,24,136
s_sub_u32 s28, s28, s93
s_add_u32 s93, s28, 20480
Ta 12,7,8192
Mvaa 140,28,152
Ta 14,7,9216
K 165,166,167
Mvaa 108,28,148
K 166,172,173
K 167,174,175
Mvaa 108,24,132
buffer_load_dwordx4 a[36:39], v26, s[8:11], s28 offen offset:0 sc1
v_accvgpr_write_b32 a72, v164
Mvaa 76,24,128
Ta 16,8,0
Ta 18,8,1024
Mvaa 76,28,144
v_accvgpr_write_b32 a73, v165
v_accvgpr_write_b32 a74, v166
Mvaa 44,28,140
Ta 20,8,8192
v_accvgpr_write_b32 a75, v167
Mvav 44,24,184
W 7
Ta 22,8,9216
v_accvgpr_write_b32 a76, v216
Mvav 48,0,184
D 22,48,65,2048
v_accvgpr_write_b32 a77, v217
Mvaa 48,4,140
v_accvgpr_write_b32 a78, v218
v_accvgpr_write_b32 a79, v219
Mvaa 80,4,144
Ta 24,9,0
Ta 26,9,1024
Mvaa 80,0,128
G 164,80
G 165,81
Mvaa 112,0,132
Ta 28,9,8192
Ta 30,9,9216
Mvaa 112,4,148
buffer_load_dwordx4 a[40:43], v26, s[8:11], s93 offen offset:0 sc1
G 166,82
Mvaa 144,4,152
G 167,83
G 172,84
Mvaa 144,0,136
W 8
Ta 0,2,16384
G 173,85
Mvaa 148,8,136
s_cmp_eq_u32 s14, 2
s_cselect_b64 s[88:89], s[30:31], s[32:33]
Ta 2,2,17408
G 174,86
Mvaa 148,12,152
s_lshr_b32 s77, s15, 3
s_mul_i32 s78, s77, 69120
s_mul_i32 s79, s59, 448
Mvaa 116,12,148
s_andn2_b32 s79, s79, 127
s_add_u32 s78, s78, s79
buffer_load_dwordx4 a[44:47], v27, s[8:11], s28 offen offset:0 sc1
Mvaa 116,8,132
v_min_u32 v250, 624, v1
v_lshrrev_b32 v251, 6, v250
Mvaa 84,8,128
v_mul_u32_u24 v251, 3456, v251
v_and_b32 v250, 48, v250
Mvaa 84,12,144
v_lshl_add_u32 v250, v250, 3, v251
v_add_u32 v250, s78, v250
Mvaa 52,12,140
Ta 4,2,24576
G 175,87
Mvav 52,8,184
W 7
buffer_load_dwordx4 a[48:51], v27, s[8:11], s93 offen offset:0 sc1
Ta 6,2,25600
Mvav 56,16,184
s_add_u32 s28, s28, 512
s_cmp_eq_u32 s28, s4
s_cselect_b32 s93, 10240, 0
D 23,48,65,3072
Mvaa 56,20,140
s_sub_u32 s28, s28, s93
)"
R"(s_add_u32 s93, s28, 20480
K 164,164,165
Mvaa 88,20,144
K 165,166,167
K 166,172,173
Mvaa 88,16,128
Ta 8,3,16384
K 167,174,175
Mvaa 120,16,132
Ta 10,3,17408
v_accvgpr_write_b32 a80, v164
Mvaa 120,20,148
buffer_load_dwordx4 a[52:55], v26, s[8:11], s28 offen offset:0 sc1
v_accvgpr_write_b32 a81, v165
Mvaa 152,20,152
Ta 12,3,24576
v_accvgpr_write_b32 a82, v166
Mvaa 152,16,136
W 7
Ta 14,3,25600
v_accvgpr_write_b32 a83, v167
Mvaa 156,24,136
v_accvgpr_write_b32 a84, v224
v_lshrrev_b32 v20, 4, v1
Mvaa 156,28,152
v_accvgpr_write_b32 a85, v225
v_accvgpr_write_b32 a86, v226
Mvaa 124,28,148
buffer_load_dwordx4 a[56:59], v26, s[8:11], s93 offen offset:0 sc1
v_add_u32 v20, 0, v20
Mvaa 124,24,132
Ta 16,4,16384
Ta 18,4,17408
Mvaa 92,24,128
v_accvgpr_write_b32 a87, v227
v_mul_u32_u24 v22, 205, v20
Mvaa 92,28,144
Ta 20,4,24576
G 164,88
Mvaa 60,28,140
Ta 22,4,25600
v_lshrrev_b32 v22, 12, v22
Mvav 60,24,184
s_waitcnt vmcnt(15) lgkmcnt(8)
buffer_load_dwordx4 a[60:63], v27, s[8:11], s28 offen offset:0 sc1
G 165,89
Mvaa 32,0,156
G 166,90
v_mul_u32_u24 v23, 20, v22
Mvav 32,4,196
G 167,91
v_sub_u32 v20, v20, v23
Mvav 64,4,204
Ta 24,5,16384
G 172,92
Mvav 64,0,192
Ta 26,5,17408
v_lshrrev_b32 v23, 1, v22
Mvav 96,0,200
global_load_dword v251, v250, s[88:89]
v_add_u32 v250, 34560, v250
Mvav 96,4,212
v_min_u32 v250, 2211836, v250
G 173,93
Mvav 128,4,220
G 174,94
v_and_b32 v23, 2, v23
Mvav 128,0,188
W 6
Ta 28,5,24576
Ta 30,5,25600
Mvav 132,8,188
G 175,95
v_xor_b32 v20, v20, v23
Mvav 132,12,220
buffer_load_dwordx4 v[216:219], v27, s[8:11], s93 offen offset:0 sc1
K 164,164,165
Mvav 100,12,212
s_add_u32 s28, s28, 512
s_cmp_eq_u32 s28, s4
s_cselect_b32 s93, 10240, 0
s_sub_u32 s28, s28, s93
Mvav 100,8,200
s_add_u32 s93, s28, 20480
v_mul_u32_u24 v22, 80, v22
K 165,166,167
Mvav 68,8,192
K 166,172,173
v_add_lshl_u32 v16, v20, v22, 4
Mvav 68,12,204
Ta 0,6,16384
Ta 2,6,17408
Mvav 36,12,196
buffer_load_dwordx4 v[184:187], v26, s[8:11], s28 offen offset:0 sc1
K 167,174,175
Mvaa 36,8,156
W 6
v_lshrrev_b32 v20, 4, v1
v_accvgpr_write_b32 a88, v164
Mvaa 40,16,156
v_add_u32 v20, 64, v20
v_accvgpr_write_b32 a89, v165
Mvav 40,20,196
Ta 4,6,24576
Ta 6,6,25600
Mvav 72,20,204
v_accvgpr_write_b32 a90, v166
v_mul_u32_u24 v22, 205, v20
Mvav 72,16,192
s_cmp_lg_u32 s72, 0
s_cbranch_scc1 .LL_59%=
s_waitcnt vmcnt(16)
.LL_59%=:
buffer_store_dwordx4 v[240:243], v1, s[8:11], s76 offen sc1
v_accvgpr_write_b32 a91, v167
v_lshrrev_b32 v22, 12, v22
Mvav 104,16,200
v_accvgpr_write_b32 a92, v232
v_mul_u32_u24 v23, 20, v22
Mvav 104,20,212
Ta 8,7,16384
Ta 10,7,17408
Mvav 136,20,220
v_accvgpr_write_b32 a93, v233
v_accvgpr_write_b32 a94, v234
Mvav 136,16,188
W 6
v_sub_u32 v20, v20, v23
v_accvgpr_write_b32 a95, v235
Mvav 140,24,188
buffer_store_dwordx4 v[244:247], v249, s[8:11], s76 offen offset:1024 sc1
Ta 12,7,24576
Mvav 140,28,220
s_add_u32 s76, s74, s75
s_add_u32 s76, s76, 3840
Ta 14,7,25600
Mvav 108,28,212
v_lshrrev_b32 v23, 1, v22
G 164,96
Mvav 108,24,200
G 165,97
v_and_b32 v23, 2, v23
Mvav 76,24,192
G 166,98
G 167,99
Mvav 76,28,204
buffer_load_dwordx4 a[128:131], v26, s[8:11], s93 offen offset:0 sc1
v_xor_b32 v20, v20, v23
Mvav 44,28,196
G 172,100
G 173,101
Mvaa 44,24,156
W 4
Ta 16,8,16384
Ta 18,8,17408
Mvaa 48,0,156
v_mul_u32_u24 v22, 80, v22
G 174,102
Mvav 48,4,196
G 175,103
v_add_lshl_u32 v17, v20, v22, 4
Mvav 80,4,204
buffer_load_dwordx4 a[132:135], v27, s[8:11], s28 offen offset:0 sc1
K 164,164,165
Mvav 80,0,192
Ta 20,8,24576
Ta 22,8,25600
Mvav 112,0,200
K 165,166,167
K 166,172,173
Mvav 112,4,212
v_subrev_u32 v17, 1024, v17
K 167,174,175
Mvav 144,4,220
v_accvgpr_write_b32 a96, v164
v_lshrrev_b32 v20, 4, v1
Mvav 144,0,188
W 4
buffer_load_dwordx4 a[136:139], v27, s[8:11], s93 offen offset:0 sc1
v_accvgpr_write_b32 a97, v165
Mvav 148,8,188
s_add_u32 s28, s28, 512
v_accvgpr_write_b32 a98, v166
v_add_u32 v20, 128, v20
Mvav 148,12,220
Ta 24,9,16384
Ta 26,9,17408
Ta 28,9,24576
Ta 30,9,25600
Mvav 116,12,212
s_cmp_eq_u32 s28, s4
s_cselect_b32 s93, 10240, 0
v_accvgpr_write_b32 a99, v167
G 164,248
Mvav 116,8,200
s_sub_u32 s28, s28, s93
s_add_u32 s93, s28, 20480
v_mul_u32_u24 v22, 205, v20
Mvav 84,8,192
s_waitcnt vmcnt(11) lgkmcnt(0)
s_barrier
global_load_dword v251, v250, s[88:89]
Mvav 84,12,204
s_mov_b64 s[88:89], s[34:35]
s_lshr_b32 s77, s15, 3
s_lshl_b32 s78, s59, 5
Mvav 52,12,196
s_add_u32 s77, s77, s78
s_lshl_b32 s77, s77, 1
s_add_u32 s77, s77, s14
Mvaa 52,8,156
s_sub_u32 s77, s77, 2
s_mul_i32 s77, s77, 4480
v_min_u32 v250, 544, v1
Mvaa 56,16,156
v_lshl_add_u32 v250, v250, 3, s77
v_min_u32 v250, 2211836, v250
Mvav 56,20,196
buffer_load_dwordx4 a[140:143], v26, s[8:11], s28 offen offset:0 sc1
G 165,249
Mvav 88,20,204
G 166,250
v_lshrrev_b32 v22, 12, v22
Mvav 88,16,192
G 167,251
G 172,252
Mvav 120,16,200
G 173,253
G 174,254
G 175,255
K 164,164,165
K 165,166,167
K 166,172,173
K 167,174,175
v_accvgpr_write_b32 a100, v164
v_accvgpr_write_b32 a101, v165
v_accvgpr_write_b32 a102, v166
v_accvgpr_write_b32 a103, v167
Mvav 120,20,212
v_mul_u32_u24 v23, 20, v22
v_sub_u32 v20, v20, v23
Mvav 152,20,220
buffer_load_dwordx4 a[144:147], v26, s[8:11], s93 offen offset:0 sc1
v_lshrrev_b32 v23, 1, v22
Mvav 152,16,188
v_and_b32 v23, 2, v23
v_xor_b32 v20, v20, v23
Mvav 156,24,188
Ta 0,10,32768
Ta 2,10,33792
Mvav 156,28,220
Ta 4,10,40960
Ta 6,10,41984
Mvav 124,28,212
G 164,104
v_mul_u32_u24 v22, 80, v22
Mvav 124,24,200
G 165,105
v_add_lshl_u32 v18, v20, v22, 4
Mvav 92,24,192
Ta 8,11,32768
G 166,106
Mvav 92,28,204
Ta 10,11,33792
G 167,107
Mvav 60,28,196
buffer_load_dwordx4 a[148:151], v27, s[8:11], s28 offen offset:0 sc1
v_subrev_u32 v18, 2048, v18
Mvaa 60,24,156
W 2
G 172,108
G 173,109
Mvav 32,0,228
Ta 12,11,40960
v_lshrrev_b32 v20, 4, v1
Mvaa 32,4,36
Ta 14,11,41984
G 174,110
Mvaa 64,4,40
v_add_u32 v20, 192, v20
G 175,111
Mvav 64,0,236
K 164,164,165
v_mul_u32_u24 v22, 205, v20
Mvav 96,0,208
K 165,166,167
v_lshrrev_b32 v22, 12, v22
Mvaa 96,4,44
Ta 16,12,32768
K 166,172,173
Mvaa 128,4,48
Ta 18,12,33792
K 167,174,175
Mvaa 128,0,32
W 2
buffer_load_dwordx4 a[152:155], v27, s[8:11], s93 offen offset:0 sc1
v_mul_u32_u24 v23, 20, v22
Mvaa 132,8,32
s_add_u32 s28, s28, 512
v_accvgpr_write_b32 a104, v164
v_accvgpr_write_b32 a105, v165
Mvaa 132,12,48
s_cmp_eq_u32 s28, s4
s_cselect_b32 s93, 10240, 0
Ta 20,12,40960
v_sub_u32 v20, v20, v23
Mvaa 100,12,44
s_sub_u32 s28, s28, s93
Ta 22,12,41984
v_accvgpr_write_b32 a106, v166
Mvav 100,8,208
s_add_u32 s93, s28, 20480
v_lshrrev_b32 v23, 1, v22
v_accvgpr_write_b32 a107, v167
Mvav 68,8,236
K 176,176,177
v_and_b32 v23, 2, v23
Mvaa 68,12,40
K 177,178,179
K 178,180,181
K 179,182,183
v_accvgpr_write_b32 a108, v176
v_accvgpr_write_b32 a109, v177
v_accvgpr_write_b32 a110, v178
v_accvgpr_write_b32 a111, v179
Mvaa 36,12,36
Ta 24,13,32768
v_xor_b32 v20, v20, v23
Mvav 36,8,228
W 1
Ta 26,13,33792
buffer_load_dwordx4 v[224:227], v26, s[8:11], s28 offen offset:0 sc1
Mvav 40,16,228
Ta 28,13,40960
v_mul_u32_u24 v22, 80, v22
Mvaa 40,20,36
Ta 30,13,41984
v_add_lshl_u32 v19, v20, v22, 4
Mvaa 72,20,40
v_subrev_u32 v19, 3072, v19
G 164,112
Mvav 72,16,236
v_lshrrev_b32 v20, 4, v1
G 165,113
Mvav 104,16,208
s_cmp_lg_u32 s72, 0
s_cbranch_scc1 .LL_60%=
s_waitcnt vmcnt(9)
.LL_60%=:
v_mov_b64 v[28:29], s[12:13]
s_mov_b64 exec, 1
buffer_store_dwordx2 v[28:29], off, s[8:11], s76 sc1
s_mov_b64 exec, -1
Ta 0,14,32768
Ta 2,14,33792
Mvaa 104,20,44
G 166,114
v_add_u32 v20, 256, v20
Mvaa 136,20,48
G 167,115
v_mul_u32_u24 v22, 205, v20
Mvaa 136,16,32
W 2
Ta 4,14,40960
Ta 6,14,41984
Mvaa 140,24,32
G 172,116
v_lshrrev_b32 v22, 12, v22
Mvaa 140,28,48
global_load_dword v251, v250, s[88:89]
G 173,117
Mvaa 108,28,44
G 174,118
v_mul_u32_u24 v23, 20, v22
Mvav 108,24,208
G 175,119
v_sub_u32 v20, v20, v23
Mvav 76,24,236
Ta 8,15,32768
Ta 10,15,33792
Mvaa 76,28,40
K 164,164,165
v_lshrrev_b32 v23, 1, v22
Mvaa 44,28,36
buffer_load_dwordx4 v[232:235], v26, s[8:11], s93 offen offset:0 sc1
K 165,166,167
Mvav 44,24,228
W 2
K 166,172,173
v_and_b32 v23, 2, v23
Mvav 48,0,228
Ta 12,15,40960
Ta 14,15,41984
Mvaa 48,4,36
K 167,174,175
v_xor_b32 v20, v20, v23
Mvaa 80,4,40
v_accvgpr_write_b32 a112, v164
v_accvgpr_write_b32 a113, v165
Mvav 80,0,236
)"
R"(buffer_load_dwordx4 a[248:251], v27, s[8:11], s28 offen offset:0 sc1
v_mul_u32_u24 v22, 80, v22
Mvav 112,0,208
Ta 16,24,32768
v_accvgpr_write_b32 a114, v166
Mvaa 112,4,44
Ta 18,24,33792
v_add_lshl_u32 v20, v20, v22, 4
Mvaa 144,4,48
v_accvgpr_write_b32 a115, v167
v_accvgpr_write_b32 a116, v168
v_accvgpr_write_b32 a117, v169
v_accvgpr_write_b32 a118, v170
v_accvgpr_write_b32 a119, v171
Mvaa 144,0,32
W 2
Ta 20,24,40960
v_lshrrev_b32 v22, 6, v1
Mvaa 148,8,32
buffer_load_dwordx4 a[252:255], v27, s[8:11], s93 offen offset:0 sc1
Ta 22,24,41984
Mvaa 148,12,48
s_add_u32 s28, s28, 512
v_mul_u32_u24 v22, 320, v22
v_bfe_u32 v23, v1, 4, 2
Mvaa 116,12,44
s_cmp_eq_u32 s28, s4
s_cselect_b32 s93, 10240, 0
v_lshl_add_u32 v22, v23, 3, v22
G 164,120
Mvav 116,8,208
s_sub_u32 s28, s28, s93
Ta 24,25,32768
Ta 26,25,33792
Mvav 84,8,236
s_add_u32 s93, s28, 20480
G 165,121
v_bfe_u32 v23, v1, 8, 1
Mvaa 84,12,40
buffer_load_dwordx4 v[192:195], v26, s[8:11], s28 offen offset:0 sc1
G 166,122
Mvaa 52,12,36
v_lshlrev_b32 v23, 5, v23
G 167,123
Mvav 52,8,228
W 2
Ta 28,25,40960
Ta 30,25,41984
Mvav 56,16,228
v_add_u32 v21, v22, v23
G 172,124
Mvaa 56,20,36
G 173,125
v_sub_u32 v22, v22, v23
Mvaa 88,20,40
s_mul_i32 s94, s59, 655360
Ta 0,10,49152
G 174,126
Mvav 88,16,236
s_mul_i32 s95, s58, 320
Ta 2,10,50176
G 175,127
Mvav 120,16,208
s_add_u32 s94, s94, s95
buffer_load_dwordx4 v[200:203], v26, s[8:11], s93 offen offset:0 sc1
K 164,164,165
Mvaa 120,20,44
Ta 4,10,57344
K 165,166,167
Mvaa 152,20,48
Ta 6,10,58368
K 166,172,173
Mvaa 152,16,32
W 4
s_add_u32 s40, s26, s94
s_addc_u32 s41, s27, 0
Mvaa 156,24,32
s_and_b32 s41, s41, 0xffff
K 167,174,175
v_accvgpr_write_b32 a120, v164
Mvaa 156,28,48
s_mov_b32 s42, 5242880
Ta 8,11,49152
v_accvgpr_write_b32 a121, v165
Mvaa 124,28,44
s_mov_b32 s43, 0x20000
Ta 10,11,50176
buffer_load_dwordx4 v[188:191], v27, s[8:11], s28 offen offset:0 sc1
Mvav 124,24,208
s_sub_u32 s42, s42, s94
v_accvgpr_write_b32 a122, v166
v_accvgpr_write_b32 a123, v167
Mvav 92,24,236
s_mov_b64 s[44:45], s[8:9]
Ta 12,11,57344
Ta 14,11,58368
Mvaa 92,28,40
s_mov_b32 s47, 0x20000
v_accvgpr_write_b32 a124, v160
v_accvgpr_write_b32 a125, v161
Mvaa 60,28,36
s_and_b32 s48, s14, 1
v_accvgpr_write_b32 a126, v162
v_accvgpr_write_b32 a127, v163
Mvav 60,24,228
s_waitcnt vmcnt(14) lgkmcnt(4)
Ta 16,12,49152
Ta 18,12,50176
Mvaa 32,0,52
s_mul_i32 s48, s48, 5120
buffer_load_dwordx4 v[196:199], v27, s[8:11], s93 offen offset:0 sc1
v_bfe_u32 v28, v1, 4, 2
Mvav 32,4,184
s_lshr_b32 s51, s14, 1
Ta 20,12,57344
v_lshlrev_b32 v28, 3, v28
Mvaa 64,4,128
s_lshl_b32 s80, s56, 9
s_add_u32 s80, s80, 4096
Ta 22,12,58368
Mvaa 64,0,56
s_and_b32 s94, s14, 1
Ta 24,13,49152
Ta 26,13,50176
Mvaa 96,0,60
s_add_u32 s28, s28, 512
s_cmp_eq_u32 s28, s4
s_cselect_b32 s93, 10240, 0
s_sub_u32 s28, s28, s93
Mvaa 96,4,132
s_add_u32 s93, s28, 20480
s_lshl_b32 s95, s57, 1
buffer_load_dwordx2 v[28:29], v28, s[8:11], s80 offen sc1
Mvaa 128,4,136
s_add_u32 s94, s94, s95
Ta 28,13,57344
Ta 30,13,58368
Mvav 128,0,216
W 8
s_lshl_b32 s54, s94, 3
s_add_u32 s54, s54, s59
Mvav 132,8,216
s_lshl_b32 s54, s54, 6
Ta 0,14,49152
Ta 2,14,50176
Mvaa 132,12,136
s_xor_b32 s55, s54, 15872
Mvaa 100,12,132
buffer_load_dwordx4 v[204:207], v26, s[8:11], s28 offen offset:0 sc1
Ta 4,14,57344
Mvaa 100,8,60
s_add_u32 s54, s54, 20480
Ta 6,14,58368
Mvaa 68,8,56
s_and_b32 s81, s14, 1
Mvaa 68,12,128
s_add_u32 s55, s55, 20480
Mvav 36,12,184
Mvaa 36,8,52
W 8
s_and_b32 s95, s14, 2
buffer_load_dwordx4 v[212:215], v26, s[8:11], s93 offen offset:0 sc1
Mvaa 40,16,52
s_mul_i32 s91, s81, 17164288
Ta 8,15,49152
Ta 10,15,50176
Mvav 40,20,184
s_lshl_b32 s94, s94, 2
s_mul_i32 s82, s56, 536576
Mvaa 72,20,128
s_add_u32 s94, s94, s95
Ta 12,15,57344
Ta 14,15,58368
Mvaa 72,16,56
Mvaa 104,16,60
s_lshl_b32 s95, s95, 4
s_add_u32 s91, s91, s82
buffer_load_dwordx4 v[220:223], v27, s[8:11], s28 offen offset:0 sc1
Mvaa 104,20,132
Mvaa 136,20,136
s_add_u32 s55, s55, s95
s_add_u32 s91, s91, 4194304
Mvav 136,16,216
W 8
s_lshl_b32 s49, s94, 17
Ta 16,24,49152
Mvav 140,24,216
Ta 18,24,50176
Mvaa 140,28,136
s_lshl_b32 s95, s59, 14
s_lshr_b32 s82, s14, 1
buffer_load_dwordx4 v[176:179], v27, s[8:11], s93 offen offset:0 sc1
Mvaa 108,28,132
Ta 20,24,57344
Ta 22,24,58368
Mvaa 108,24,60
s_add_u32 s49, s49, s95
s_lshl_b32 s83, s82, 14
Mvaa 76,24,56
s_xor_b32 s50, s49, 16252928
Mvaa 76,28,128
Mvav 44,28,184
s_add_u32 s91, s91, s83
s_add_u32 s49, s49, 39845888
Mvaa 44,24,52
W 8
Ta 24,25,49152
Ta 26,25,50176
Mvaa 48,0,52
s_add_u32 s50, s50, 39845888
s_lshl_b32 s81, s81, 14
Mvav 48,4,184
Mvaa 80,4,128
s_lshr_b32 s94, s15, 3
Ta 28,25,57344
Ta 30,25,58368
Mvaa 80,0,56
s_lshl_b32 s82, s82, 16
s_lshl_b32 s94, s94, 3
Mvaa 112,0,60
Mvaa 112,4,132
s_add_u32 s94, s94, s14
s_or_b32 s92, s81, s82
Mvaa 144,4,136
Mvav 144,0,216
W 8
s_lshl_b32 s94, s94, 4
Ta 0,2,32768
Mvav 148,8,216
s_xor_b32 s92, s92, 0x10000
s_add_u32 s94, s94, s59
Ta 2,2,33792
Mvaa 148,12,136
Mvaa 116,12,132
s_mul_i32 s52, s94, 5120
Ta 4,2,40960
Ta 6,2,41984
Mvaa 116,8,60
Mvaa 84,8,56
s_lshl_b32 s95, s59, 12
Mvaa 84,12,128
Mvav 52,12,184
s_sub_u32 s52, s52, s95
Mvaa 52,8,52
W 8
s_add_u32 s52, s52, 56623104
Ta 8,3,32768
Mvaa 56,16,52
Ta 10,3,33792
Mvav 56,20,184
s_lshl_b32 s53, s94, 3
Mvaa 88,20,128
Ta 12,3,40960
Ta 14,3,41984
Mvaa 88,16,56
s_add_u32 s53, s53, 65536
Mvaa 120,16,60
s_waitcnt vmcnt(4)
v_cmp_eq_u64 vcc, s[12:13], v[28:29]
s_cmp_eq_u64 vcc, -1
s_cbranch_scc0 .Lspin_61%=
.Lok_62%=:
s_sub_u32 s82, s91, 16384
Mvaa 120,20,132
s_xor_b32 s83, s92, 0x10000
s_add_u32 m0, s83, 0
s_add_u32 s81, s82, 0
Mvaa 152,20,136
v_bfe_u32 v244, v1, 4, 1
v_mul_u32_u24 v244, 0xaaaa, v244
Mvav 152,16,216
W 8
Dc 1,8,81,0
v_add_u32 v244, 0x4444, v244
Mvav 156,24,216
Ta 16,4,32768
Ta 18,4,33792
Mvaa 156,28,136
v_lshl_or_b32 v240, v244, 16, v244
Mvaa 124,28,132
Ta 20,4,40960
v_mov_b32 v241, 0xff800000
Mvaa 124,24,60
Ta 22,4,41984
v_add_u32 v242, 0x10000, v1
Mvaa 92,24,56
Dc 1,8,81,1024
Mvaa 92,28,128
v_bfe_u32 v244, v1, 4, 4
Mvav 60,28,184
v_lshrrev_b32 v245, 8, v1
Mvaa 60,24,52
W 8
Ta 24,5,32768
v_lshlrev_b32 v245, 2, v245
Mvaa 32,0,140
Ta 26,5,33792
Mvav 32,4,224
Dc 1,8,81,2048
v_sub_u32 v244, v244, v245
Mvav 64,4,232
Ta 28,5,40960
v_cmp_le_i32_e64 s[72:73], 0, v244
Mvaa 64,0,144
Ta 30,5,41984
Mvaa 96,0,148
v_cmp_le_i32_e64 s[74:75], 1, v244
Mvaa 96,4,248
v_cmp_le_i32_e64 s[76:77], 2, v244
Mvaa 128,4,252
Dc 1,8,81,3072
v_cmp_le_i32_e64 s[78:79], 3, v244
Mvaa 128,0,152
W 8
s_add_u32 m0, s83, 4096
s_add_u32 s81, s82, 4096
Mvaa 132,8,152
Ta 0,6,32768
Ta 2,6,33792
Mvaa 132,12,252
v_add_u32 v243, 4096, v1
v_add_u32 v244, 8192, v1
Mvaa 100,12,248
Ta 4,6,40960
v_add_u32 v245, 12288, v1
Mvaa 100,8,148
Dc 1,8,81,0
Ta 6,6,41984
Mvaa 68,8,144
s_mov_b32 s18, 0x3f803f80
Mvav 68,12,232
s_mov_b32 s19, 0x3f003f00
Mvav 36,12,224
Mvaa 36,8,140
W 8
Ta 8,7,32768
v_accvgpr_write_b32 a176, s18
Mvaa 40,16,140
Dc 1,8,81,1024
Ta 10,7,33792
Mvav 40,20,224
v_accvgpr_write_b32 a180, s19
v_mov_b32 v252, v241
Mvav 72,20,232
Ta 12,7,40960
Ta 14,7,41984
Mvaa 72,16,144
v_accvgpr_write_b32 a177, s18
Mvaa 104,16,148
v_accvgpr_write_b32 a181, s19
Mvaa 104,20,248
Dc 1,8,81,2048
v_mov_b32 v253, v241
Mvaa 136,20,252
Mvaa 136,16,152
W 8
Ta 16,8,32768
v_accvgpr_write_b32 a178, s18
Mvaa 140,24,152
Ta 18,8,33792
v_accvgpr_write_b32 a182, s19
Mvaa 140,28,252
Mvaa 108,28,248
Dc 1,8,81,3072
v_mov_b32 v254, v241
Mvaa 108,24,148
s_add_u32 m0, s83, 8192
s_add_u32 s81, s82, 8192
Ta 20,8,40960
Mvaa 76,24,144
Ta 22,8,41984
v_accvgpr_write_b32 a179, s18
Mvav 76,28,232
v_accvgpr_write_b32 a183, s19
Mvav 44,28,224
v_mov_b32 v255, v241
Mvaa 44,24,140
W 8
Dc 1,8,81,0
v_accvgpr_write_b32 a160, 0
Mvaa 48,0,140
Ta 24,9,32768
Ta 26,9,33792
Mvav 48,4,224
v_accvgpr_write_b32 a161, 0
Mvav 80,4,232
Ta 28,9,40960
v_accvgpr_write_b32 a162, 0
Mvaa 80,0,144
Ta 30,9,41984
v_accvgpr_write_b32 a163, 0
Mvaa 112,0,148
Dc 1,8,81,1024
Mvaa 112,4,248
v_accvgpr_write_b32 a164, 0
Mvaa 144,4,252
v_accvgpr_write_b32 a165, 0
Mvaa 144,0,152
W 8
Ta 0,2,49152
)"
R"(v_accvgpr_write_b32 a166, 0
Mvaa 148,8,152
Ta 2,2,50176
Mvaa 148,12,252
Dc 1,8,81,2048
v_accvgpr_write_b32 a167, 0
Mvaa 116,12,248
Ta 4,2,57344
v_accvgpr_write_b32 a168, 0
Mvaa 116,8,148
Ta 6,2,58368
v_accvgpr_write_b32 a169, 0
Mvaa 84,8,144
Mvav 84,12,232
v_accvgpr_write_b32 a170, 0
Mvav 52,12,224
Dc 1,8,81,3072
v_accvgpr_write_b32 a171, 0
Mvaa 52,8,140
W 8
s_add_u32 m0, s83, 12288
s_add_u32 s81, s82, 12288
Mvaa 56,16,140
Ta 8,3,49152
Ta 10,3,50176
Mvav 56,20,224
v_accvgpr_write_b32 a172, 0
v_accvgpr_write_b32 a173, 0
Mvav 88,20,232
Ta 12,3,57344
v_accvgpr_write_b32 a174, 0
Mvaa 88,16,144
Dc 1,8,81,0
Ta 14,3,58368
Mvaa 120,16,148
v_accvgpr_write_b32 a175, 0
Mvaa 120,20,248
s_mov_b32 s90, 0x5d800000
Mvaa 152,20,252
s_mov_b64 s[88:89], 0
Mvaa 152,16,152
W 8
Ta 16,4,49152
Ta 18,4,50176
Mvaa 156,24,152
s_mov_b32 s64, 0
Dc 1,8,81,1024
Mvaa 156,28,252
s_and_b32 s18, s14, 1
Mvaa 124,28,248
s_lshl_b32 s62, s18, 14
Ta 20,4,57344
Ta 22,4,58368
Mvaa 124,24,148
Mvaa 92,24,144
s_lshl_b32 s20, s57, 15
Mvav 92,28,232
s_sub_u32 s20, 507904, s20
Dc 1,8,81,2048
Mvav 60,28,224
Mvaa 60,24,140
s_waitcnt vmcnt(15) lgkmcnt(8)
s_add_u32 s20, s20, s62
Ta 24,5,49152
Mvav 32,0,192
s_lshl_b32 s65, s57, 1
Ta 26,5,50176
Mvav 32,4,204
s_sub_u32 s65, s65, 1
Mvav 64,4,212
Dc 1,8,81,3072
Ta 28,5,57344
Mvav 64,0,200
s_max_i32 s65, s65, 0
Ta 30,5,58368
v_bfe_u32 v28, v1, 4, 4
Mvav 96,0,188
s_sub_u32 s66, s60, 1
v_lshlrev_b32 v28, 3, v28
Mvav 96,4,220
s_lshl_b32 s80, s56, 9
s_add_u32 s80, s80, 4096
s_cmp_lt_u32 s14, 2
s_cselect_b32 s20, s20, 0
s_cselect_b32 s66, s66, 15
s_cselect_b32 s21, 32768, 0
Mvav 128,4,176
Mvav 128,0,196
W 8
s_add_u32 s61, s91, s20
buffer_load_dwordx2 v[28:29], v28, s[8:11], s80 offen sc1
Mvav 132,8,196
s_add_u32 s62, s62, s21
Ta 0,6,49152
Ta 2,6,50176
Mvav 132,12,176
Mvav 100,12,220
s_sub_u32 s63, 16384, s21
Ta 4,6,57344
Ta 6,6,58368
Mvav 100,8,188
s_lshl_b32 s67, s60, 7
Mvav 68,8,200
s_add_u32 s67, s67, s56
Mvav 68,12,212
Mvav 36,12,204
s_lshl_b32 s67, s67, 12
Mvav 36,8,192
W 8
s_add_u32 s67, s67, 39845888
Ta 8,7,49152
Mvav 40,16,192
s_lshl_b32 s69, s60, 3
Ta 10,7,50176
Mvav 40,20,204
Mvav 72,20,212
s_add_u32 s69, s69, s59
Ta 12,7,57344
Ta 14,7,58368
Mvav 72,16,200
s_lshl_b32 s69, s69, 6
Mvav 104,16,188
Mvav 104,20,220
s_lshl_b32 s21, s58, 3
Mvav 136,20,176
s_add_u32 s69, s69, s21
Mvav 136,16,196
W 8
s_add_u32 s69, s69, 20480
Ta 16,8,49152
Mvav 140,24,196
Ta 18,8,50176
Mvav 140,28,176
s_mov_b32 s70, 0
Mvav 108,28,220
s_mov_b32 s37, 0
Ta 20,8,57344
Ta 22,8,58368
Mvav 108,24,188
s_sub_u32 s18, s57, 4
Mvav 76,24,200
Mvav 76,28,212
s_lshl_b32 s18, s18, 2
Mvav 44,28,204
s_add_u32 s18, s18, s58
Mvav 44,24,192
W 8
Ta 24,9,49152
Ta 26,9,50176
Mvav 48,0,192
s_mul_i32 s85, s59, 7
Mvav 48,4,204
s_add_u32 s85, s85, s18
Mvav 80,4,212
s_cmp_lt_u32 s18, 7
s_cselect_b32 s85, s85, 99
Ta 28,9,57344
Ta 30,9,58368
Mvav 80,0,200
Mvav 112,0,188
s_cmp_lt_u32 s85, 54
s_cselect_b32 s65, 2, s65
Mvav 112,4,220
s_mov_b32 s68, 0
Mvav 144,4,176
s_mov_b64 s[24:25], -1
Mvav 144,0,196
W 8
Mvav 148,8,196
s_mov_b32 s22, 0x3e0293ee
Mvav 148,12,176
s_mov_b32 s23, 0x3e0293ee
Mvav 116,12,220
Mvav 116,8,188
Mvav 84,8,200
Mvav 84,12,212
Mvav 52,12,204
Mvav 52,8,192
W 4
Mvav 56,16,192
Mvav 56,20,204
s_waitcnt vmcnt(0)
v_cmp_eq_u64 vcc, s[12:13], v[28:29]
s_cmp_eq_u64 vcc, -1
s_cbranch_scc0 .Lspin_64%=
.Lok_65%=:
Mvav 88,20,212
Mvav 88,16,200
Mvav 120,16,188
W 0
s_barrier
Ra 128,242,0
Mvav 120,20,220
Mvav 152,20,176
Ra 132,242,1024
Mvav 152,16,196
Mvav 156,24,196
Ra 136,242,2048
Mvav 156,28,176
Mvav 124,28,220
Ra 140,242,3072
Mvav 124,24,188
Mvav 92,24,200
Mvav 92,28,212
Mvav 60,28,204
Mvav 60,24,192
.Lbgo%=:
v_pk_mul_f32 v[32:33], v[32:33], s[22:23]
v_pk_mul_f32 v[34:35], v[34:35], s[22:23]
v_pk_mul_f32 v[36:37], v[36:37], s[22:23]
v_pk_mul_f32 v[38:39], v[38:39], s[22:23]
v_pk_mul_f32 v[64:65], v[64:65], s[22:23]
v_pk_mul_f32 v[66:67], v[66:67], s[22:23]
v_pk_mul_f32 v[68:69], v[68:69], s[22:23]
v_pk_mul_f32 v[70:71], v[70:71], s[22:23]
v_pk_mul_f32 v[96:97], v[96:97], s[22:23]
v_pk_mul_f32 v[98:99], v[98:99], s[22:23]
v_pk_mul_f32 v[100:101], v[100:101], s[22:23]
v_pk_mul_f32 v[102:103], v[102:103], s[22:23]
v_pk_mul_f32 v[128:129], v[128:129], s[22:23]
v_pk_mul_f32 v[130:131], v[130:131], s[22:23]
v_pk_mul_f32 v[132:133], v[132:133], s[22:23]
v_pk_mul_f32 v[134:135], v[134:135], s[22:23]
v_pk_mul_f32 v[40:41], v[40:41], s[22:23]
v_pk_mul_f32 v[42:43], v[42:43], s[22:23]
v_pk_mul_f32 v[44:45], v[44:45], s[22:23]
v_pk_mul_f32 v[46:47], v[46:47], s[22:23]
v_pk_mul_f32 v[72:73], v[72:73], s[22:23]
v_pk_mul_f32 v[74:75], v[74:75], s[22:23]
v_pk_mul_f32 v[76:77], v[76:77], s[22:23]
v_pk_mul_f32 v[78:79], v[78:79], s[22:23]
v_pk_mul_f32 v[104:105], v[104:105], s[22:23]
v_pk_mul_f32 v[106:107], v[106:107], s[22:23]
v_pk_mul_f32 v[108:109], v[108:109], s[22:23]
v_pk_mul_f32 v[110:111], v[110:111], s[22:23]
v_pk_mul_f32 v[136:137], v[136:137], s[22:23]
v_pk_mul_f32 v[138:139], v[138:139], s[22:23]
v_pk_mul_f32 v[140:141], v[140:141], s[22:23]
v_pk_mul_f32 v[142:143], v[142:143], s[22:23]
v_pk_mul_f32 v[48:49], v[48:49], s[22:23]
v_pk_mul_f32 v[50:51], v[50:51], s[22:23]
v_pk_mul_f32 v[52:53], v[52:53], s[22:23]
v_pk_mul_f32 v[54:55], v[54:55], s[22:23]
v_pk_mul_f32 v[80:81], v[80:81], s[22:23]
v_pk_mul_f32 v[82:83], v[82:83], s[22:23]
v_pk_mul_f32 v[84:85], v[84:85], s[22:23]
v_pk_mul_f32 v[86:87], v[86:87], s[22:23]
v_pk_mul_f32 v[112:113], v[112:113], s[22:23]
v_pk_mul_f32 v[114:115], v[114:115], s[22:23]
v_pk_mul_f32 v[116:117], v[116:117], s[22:23]
v_pk_mul_f32 v[118:119], v[118:119], s[22:23]
v_pk_mul_f32 v[144:145], v[144:145], s[22:23]
v_pk_mul_f32 v[146:147], v[146:147], s[22:23]
v_pk_mul_f32 v[148:149], v[148:149], s[22:23]
v_pk_mul_f32 v[150:151], v[150:151], s[22:23]
v_pk_mul_f32 v[56:57], v[56:57], s[22:23]
v_pk_mul_f32 v[58:59], v[58:59], s[22:23]
v_pk_mul_f32 v[60:61], v[60:61], s[22:23]
v_pk_mul_f32 v[62:63], v[62:63], s[22:23]
v_pk_mul_f32 v[88:89], v[88:89], s[22:23]
v_pk_mul_f32 v[90:91], v[90:91], s[22:23]
v_pk_mul_f32 v[92:93], v[92:93], s[22:23]
v_pk_mul_f32 v[94:95], v[94:95], s[22:23]
v_pk_mul_f32 v[120:121], v[120:121], s[22:23]
v_pk_mul_f32 v[122:123], v[122:123], s[22:23]
v_pk_mul_f32 v[124:125], v[124:125], s[22:23]
v_pk_mul_f32 v[126:127], v[126:127], s[22:23]
v_pk_mul_f32 v[152:153], v[152:153], s[22:23]
v_pk_mul_f32 v[154:155], v[154:155], s[22:23]
v_pk_mul_f32 v[156:157], v[156:157], s[22:23]
v_pk_mul_f32 v[158:159], v[158:159], s[22:23]
K 32,32,33
K 33,34,35
K 34,36,37
K 35,38,39
K 40,40,41
K 41,42,43
K 42,44,45
K 43,46,47
W 0
Zvav 196,128,32
s_add_u32 m0, s62, 0
Ra 144,242,4096
Ra 148,242,5120
Ra 152,242,6144
Ra 156,242,7168
Mvav 196,132,40
K 64,64,65
K 65,66,67
K 66,68,69
K 67,70,71
K 72,72,73
K 73,74,75
K 74,76,77
K 75,78,79
s_nop 1
Zvav 204,132,72
Dc 1,8,61,0
K 96,96,97
K 97,98,99
K 98,100,101
Mvav 204,128,64
K 99,102,103
K 104,104,105
K 105,106,107
K 106,108,109
K 107,110,111
Zvav 212,128,96
Dc 1,8,61,1024
K 128,128,129
K 129,130,131
K 130,132,133
Mvav 212,132,104
K 131,134,135
K 136,136,137
K 137,138,139
K 138,140,141
K 139,142,143
s_nop 1
Zvav 220,132,136
Dc 1,8,61,2048
K 48,48,49
K 49,50,51
K 50,52,53
Mvav 220,128,128
K 51,54,55
K 56,56,57
K 57,58,59
K 58,60,61
K 59,62,63
Mvav 196,136,48
Ra 128,242,8192
Ra 132,242,9216
K 80,80,81
K 81,82,83
Mvav 196,140,56
K 82,84,85
K 83,86,87
K 88,88,89
K 89,90,91
K 90,92,93
K 91,94,95
s_nop 1
Mvav 204,140,88
Dc 1,8,61,3072
s_add_u32 m0, s62, 4096
K 112,112,113
K 113,114,115
K 114,116,117
Mvav 204,136,80
K 115,118,119
K 120,120,121
K 121,122,123
K 122,124,125
K 123,126,127
Mvav 212,136,112
Dc 243,8,61,0
K 144,144,145
K 145,146,147
K 146,148,149
Mvav 212,140,120
K 147,150,151
K 152,152,153
K 153,154,155
K 154,156,157
K 155,158,159
s_nop 1
Mvav 220,140,152
s_add_u32 s80, s62, 131072
Dc 243,8,61,1024
v_add_u32 v12, s80, v1
Mvav 220,136,144
W 2
ds_write_b128 v12, v[32:35] offset:0
Ra 136,242,10240
Zvav 36,144,32
Ra 0,12,0
Ra 140,242,11264
Dc 243,8,61,2048
Mvav 36,148,40
v_max3_f32 v4, v196, v197, v198
v_max3_f32 v4, v4, v199, v252
v_max3_f32 v5, v204, v205, v206
v_max3_f32 v5, v5, v207, v253
Zvav 68,148,72
v_max3_f32 v6, v212, v213, v214
v_max3_f32 v6, v6, v215, v254
v_max3_f32 v7, v220, v221, v222
v_max3_f32 v7, v7, v223, v255
Mvav 68,144,64
v_mov_b32 v8, v4
v_mov_b32 v9, v5
v_mov_b32 v10, v6
v_mov_b32 v11, v7
Zvav 100,144,96
ds_write_b128 v12, v[40:43] offset:1024
v_permlane32_swap_b32 v4, v8
v_permlane32_swap_b32 v5, v9
v_permlane32_swap_b32 v6, v10
Mvav 100,148,104
)"
R"(v_permlane32_swap_b32 v7, v11
v_max_f32 v4, v4, v8
v_max_f32 v5, v5, v9
v_max_f32 v6, v6, v10
Zvav 132,148,136
v_max_f32 v7, v7, v11
v_mov_b32 v8, v4
v_mov_b32 v9, v5
v_mov_b32 v10, v6
Mvav 132,144,128
Ra 4,12,1024
v_mov_b32 v11, v7
v_permlane16_swap_b32 v4, v8
v_permlane16_swap_b32 v5, v9
Mvav 36,152,48
v_permlane16_swap_b32 v6, v10
v_permlane16_swap_b32 v7, v11
v_max_f32 v4, v4, v8
v_max_f32 v5, v5, v9
Mvav 36,156,56
v_max_f32 v6, v6, v10
v_max_f32 v7, v7, v11
v_sub_f32 v4, 0, v4
v_sub_f32 v5, 0, v5
Mvav 68,156,88
v_sub_f32 v6, 0, v6
v_sub_f32 v7, 0, v7
v_mov_b32 v224, v4
v_mov_b32 v225, v4
Mvav 68,152,80
v_mov_b32 v226, v4
v_mov_b32 v227, v4
v_mov_b32 v228, v5
v_mov_b32 v229, v5
Mvav 100,152,112
v_mov_b32 v230, v5
v_mov_b32 v231, v5
v_mov_b32 v232, v6
v_mov_b32 v233, v6
Mvav 100,156,120
v_mov_b32 v234, v6
v_mov_b32 v235, v6
v_mov_b32 v236, v7
v_mov_b32 v237, v7
Mvav 132,156,152
Dc 243,8,61,3072
s_add_u32 m0, s62, 8192
ds_write_b128 v12, v[48:51] offset:2048
v_mov_b32 v238, v7
v_mov_b32 v239, v7
Mvav 132,152,144
W 3
Ra 8,12,2048
ds_write_b128 v12, v[56:59] offset:3072
Ra 144,242,12288
Ra 12,12,3072
Cvav 160,128,32,224
s_and_b32 s80, s14, 1
Dc 244,8,61,0
Ra 148,242,13312
ds_write_b128 v12, v[64:67] offset:4096
Ra 16,12,4096
Mvav 160,132,40
s_lshl_b32 s80, s80, 14
ds_write_b128 v12, v[72:75] offset:5120
Ra 20,12,5120
ds_write_b128 v12, v[80:83] offset:6144
A 196,196,224
Cvav 164,132,72,228
s_add_u32 s80, s80, 98304
Dc 244,8,61,1024
Ra 24,12,6144
A 197,197,224
A 198,198,224
Mvav 164,128,64
Ra 152,242,14336
A 199,199,224
X 196
Cvav 168,128,96,232
Dc 244,8,61,2048
Ra 156,242,15360
X 197
Mvav 168,132,104
ds_write_b128 v12, v[88:91] offset:7168
Ra 28,12,7168
X 198
Cvav 172,132,136,236
Dc 244,8,61,3072
s_add_u32 m0, s62, 12288
K 192,196,197
X 199
Mvav 172,128,128
ds_write_b128 v12, v[96:99] offset:8192
A 204,204,228
v_add_u32 v8, s80, v1
K 193,198,199
Mvav 160,136,48
Dc 245,8,61,0
Ra 32,12,8192
A 205,205,228
A 206,206,228
Mvav 160,140,56
ds_write_b128 v8, v[4:7]
A 207,207,228
X 204
Mvav 164,140,88
Dc 245,8,61,1024
ds_write_b128 v12, v[104:107] offset:9216
X 205
Mvav 164,136,80
Ra 36,12,9216
X 206
K 200,204,205
Mvav 168,136,112
Dc 245,8,61,2048
Ra 128,242,24576
X 207
Mvav 168,140,120
ds_write_b128 v12, v[112:115] offset:10240
Ra 132,242,25600
A 212,212,232
K 201,206,207
Mvav 172,140,152
Dc 245,8,61,3072
s_add_u32 s61, s61, s63
Ra 40,12,10240
A 213,213,232
A 214,214,232
Mvav 172,136,144
W 11
ds_write_b128 v12, v[120:123] offset:11264
A 215,215,232
X 212
Cvav 176,144,32,224
X 213
X 214
Mvav 176,148,40
Ra 44,12,11264
K 208,212,213
X 215
Cvav 180,148,72,228
ds_write_b128 v12, v[128:131] offset:12288
A 220,220,236
K 209,214,215
A 221,221,236
Mvav 180,144,64
Ra 48,12,12288
Ra 136,242,26624
A 222,222,236
A 223,223,236
Cvav 184,144,96,232
ds_write_b128 v12, v[136:139] offset:13312
Ra 140,242,27648
X 220
Mvav 184,148,104
X 221
X 222
Cvav 188,148,136,236
Ra 52,12,13312
K 216,220,221
X 223
Mvav 188,144,128
ds_write_b128 v12, v[144:147] offset:14336
Ra 56,12,14336
A 36,36,224
K 217,222,223
Mvav 176,152,48
ds_write_b128 v12, v[152:155] offset:15360
A 37,37,224
A 38,38,224
A 39,39,224
Mvav 176,156,56
X 36
X 37
Mvav 180,156,88
Ra 60,12,15360
X 38
K 194,36,37
Mvav 180,152,80
Ra 144,242,28672
X 39
A 68,68,228
Mvav 184,152,112
Ra 148,242,29696
K 195,38,39
A 69,69,228
A 70,70,228
Mvav 184,156,120
W 3
s_barrier
A 71,71,228
X 68
Mvav 188,156,152
X 69
X 70
K 202,68,69
X 71
A 100,100,232
K 203,70,71
A 101,101,232
A 102,102,232
A 103,103,232
X 100
X 101
X 102
K 210,100,101
X 103
A 132,132,236
K 211,102,103
A 133,133,236
A 134,134,236
A 135,135,236
X 132
X 133
X 134
K 218,132,133
X 135
s_nop 0
K 219,134,135
Mvav 188,152,144
Ra 152,242,30720
Zvav 32,128,192
Ra 156,242,31744
Zvav 64,128,200
Zvav 96,128,208
s_xor_b32 s62, s62, 0x10000
Zvav 128,128,216
v_add_u32 v23, 61440, v21
Zvav 132,132,216
v_add_u32 v24, 61440, v22
Zvav 100,132,208
v_add_u32 v25, 122880, v21
Zvav 68,132,200
v_add_u32 v26, 122880, v22
Zvav 36,132,192
Zvav 40,136,192
X 172
Zvav 72,136,200
X 173
Zvav 104,136,208
X 174
K 220,172,173
Zvav 136,136,216
X 175
Zvav 140,140,216
X 168
K 221,174,175
Zvav 108,140,208
X 169
X 170
Zvav 76,140,200
s_waitcnt vmcnt(0) lgkmcnt(0)
s_barrier
v_xor_b32 v242, 0x10000, v242
K 212,168,169
Zvav 44,140,192
X 171
X 164
Zvav 48,144,192
Ra 128,242,0
K 213,170,171
X 165
Zvav 80,144,200
X 166
Zvav 112,144,208
Ra 132,242,1024
K 204,164,165
X 167
Zvav 144,144,216
X 160
Zvav 148,148,216
K 205,166,167
X 161
Zvav 116,148,208
Ra 136,242,2048
X 162
Zvav 84,148,200
K 196,160,161
X 163
Zvav 52,148,192
X 176
K 197,162,163
Zvav 56,152,192
Ra 140,242,3072
X 177
Zvav 88,152,200
X 178
K 198,176,177
Zvav 120,152,208
X 179
X 180
Zvav 152,152,216
K 199,178,179
Zvav 156,156,216
X 181
X 182
Zvav 124,156,208
K 206,180,181
X 183
Zvav 92,156,200
K 207,182,183
Zvav 60,156,192
s_cmp_eq_u64 s[24:25], 0
s_cbranch_scc1 .Ll1_34%=
W 0
Cvaa 160,128,0,224
Ra 144,242,4096
Ra 148,242,5120
Mvaa 160,132,4
s_add_u32 m0, s62, 0
Ra 152,242,6144
Cvaa 164,132,20,228
X 184
Mvaa 164,128,16
X 185
Cvaa 168,128,32,232
X 186
Mvaa 168,132,36
Ra 156,242,7168
K 214,184,185
Cvaa 172,132,52,236
Dc 1,8,61,0
Mvaa 172,128,48
X 187
Mvaa 160,136,8
X 188
Mvaa 160,140,12
Ra 128,242,16384
K 215,186,187
Mvaa 164,140,28
Dc 1,8,61,1024
Mvaa 164,136,24
X 189
Mvaa 168,136,40
X 190
Mvaa 168,140,44
Ra 132,242,17408
K 222,188,189
Mvaa 172,140,60
Dc 1,8,61,2048
Mvaa 172,136,56
X 191
v_smfmac_f32_16x16x64_bf16 a[160:163], a[176:179], v[192:199], v240
Ra 136,242,18432
K 223,190,191
v_smfmac_f32_16x16x64_bf16 a[164:167], a[176:179], v[200:207], v240
X 160
v_smfmac_f32_16x16x64_bf16 a[168:171], a[176:179], v[208:215], v240
Dc 1,8,61,3072
v_smfmac_f32_16x16x64_bf16 a[172:175], a[176:179], v[216:223], v240
W 3
X 161
Cvaa 176,144,0,224
s_add_u32 m0, s62, 4096
X 162
Mvaa 176,148,4
Ra 140,242,19456
K 192,160,161
Cvaa 180,148,20,228
X 163
Mvaa 180,144,16
Dc 243,8,61,0
Cvaa 184,144,32,232
X 164
Mvaa 184,148,36
K 193,162,163
Cvaa 188,148,52,236
X 165
Mvaa 188,144,48
Dc 243,8,61,1024
Ra 144,242,20480
Mvaa 176,152,8
X 166
Mvaa 176,156,12
Ra 148,242,21504
K 200,164,165
Mvaa 180,156,28
X 167
Mvaa 180,152,24
Dc 243,8,61,2048
Mvaa 184,152,40
X 168
Mvaa 184,156,44
K 201,166,167
Mvaa 188,156,60
X 169
Mvaa 188,152,56
W 2
Dc 243,8,61,3072
Ra 152,242,22528
Mvav 32,128,196
s_add_u32 m0, s62, 8192
X 170
Mvav 64,128,204
Ra 156,242,23552
K 208,168,169
Mvav 96,128,212
X 171
Mvav 128,128,220
Dc 244,8,61,0
)"
R"(Mvav 132,132,220
X 172
Mvav 100,132,212
Ra 128,242,8192
K 209,170,171
Mvav 68,132,204
X 173
Mvav 36,132,196
Dc 244,8,61,1024
Mvav 40,136,196
X 174
Mvav 72,136,204
Ra 132,242,9216
K 216,172,173
Mvav 104,136,212
X 175
Mvav 136,136,220
Dc 244,8,61,2048
Mvav 140,140,220
X 176
Mvav 108,140,212
Ra 136,242,10240
K 217,174,175
Mvav 76,140,204
X 177
Mvav 44,140,196
W 3
Dc 244,8,61,3072
Mvav 48,144,196
X 178
Mvav 80,144,204
s_add_u32 m0, s62, 12288
Ra 140,242,11264
Mvav 112,144,212
K 194,176,177
Mvav 144,144,220
X 179
Mvav 148,148,220
X 180
Mvav 116,148,212
Dc 245,8,61,0
Ra 144,242,12288
Mvav 84,148,204
K 195,178,179
Mvav 52,148,196
X 181
Mvav 56,152,196
X 182
Mvav 88,152,204
Dc 245,8,61,1024
Ra 148,242,13312
Mvav 120,152,212
K 202,180,181
Mvav 152,152,220
X 183
Mvav 156,156,220
X 184
Mvav 124,156,212
Dc 245,8,61,2048
Ra 152,242,14336
Mvav 92,156,204
K 203,182,183
Mvav 60,156,196
W 3
X 185
Cvaa 160,128,0,224
X 186
Mvaa 160,132,4
Dc 245,8,61,3072
Ra 156,242,15360
Cvaa 164,132,20,228
s_add_u32 s61, s61, s63
K 210,184,185
Mvaa 164,128,16
X 187
Cvaa 168,128,32,232
X 188
Mvaa 168,132,36
Cvaa 172,132,52,236
s_xor_b32 s62, s62, 0x10000
K 211,186,187
Mvaa 172,128,48
X 189
Mvaa 160,136,8
Ra 128,242,24576
Ra 132,242,25600
Mvaa 160,140,12
s_add_u32 s64, s64, 1
X 190
Mvaa 164,140,28
K 218,188,189
Mvaa 164,136,24
X 191
Mvaa 168,136,40
Mvaa 168,140,44
X 160
Mvaa 172,140,60
s_add_u32 s36, s29, 131072
K 219,190,191
Mvaa 172,136,56
W 2
X 161
Cvaa 176,144,0,224
Ra 136,242,26624
Ra 140,242,27648
Mvaa 176,148,4
X 162
Cvaa 180,148,20,228
K 196,160,161
Mvaa 180,144,16
X 163
Cvaa 184,144,32,232
buffer_store_dwordx4 a[124:127], v1, s[8:11], s36 offen offset:0 sc1
Mvaa 184,148,36
X 164
Cvaa 188,148,52,236
K 197,162,163
Mvaa 188,144,48
X 165
Mvaa 176,152,8
Ra 144,242,28672
Ra 148,242,29696
Mvaa 176,156,12
X 166
Mvaa 180,156,28
buffer_store_dwordx4 a[116:119], v1, s[8:11], s36 offen offset:1024 sc1
K 204,164,165
Mvaa 180,152,24
X 167
Mvaa 184,152,40
Mvaa 184,156,44
X 168
Mvaa 188,156,60
Mvaa 188,152,56
W 2
Ra 152,242,30720
K 205,166,167
Mvav 32,128,192
X 169
Mvav 64,128,200
buffer_store_dwordx4 a[108:111], v1, s[8:11], s36 offen offset:2048 sc1
Ra 156,242,31744
Mvav 96,128,208
X 170
Mvav 128,128,216
K 212,168,169
Mvav 132,132,216
X 171
Mvav 100,132,208
X 172
Mvav 68,132,200
buffer_store_dwordx4 a[204:207], v1, s[8:11], s36 offen offset:3072 sc1
Mvav 36,132,192
K 213,170,171
Mvav 40,136,192
X 173
Mvav 72,136,200
X 174
Mvav 104,136,208
Mvav 136,136,216
s_add_u32 s36, s29, 135168
K 220,172,173
Mvav 140,140,216
X 175
Mvav 108,140,208
X 176
Mvav 76,140,200
s_waitcnt vmcnt(4) lgkmcnt(0)
s_barrier
v_xor_b32 v242, 0x10000, v242
Mvav 44,140,192
Ra 128,242,0
K 221,174,175
Mvav 48,144,192
X 177
Mvav 80,144,200
buffer_store_dwordx4 a[212:215], v1, s[8:11], s36 offen offset:0 sc1
Mvav 112,144,208
X 178
Mvav 144,144,216
Ra 132,242,1024
K 198,176,177
Mvav 148,148,216
X 179
Mvav 116,148,208
X 180
Mvav 84,148,200
Ra 136,242,2048
Mvav 52,148,192
buffer_store_dwordx4 a[220:223], v1, s[8:11], s36 offen offset:1024 sc1
K 199,178,179
Mvav 56,152,192
X 181
Mvav 88,152,200
Ra 140,242,3072
Mvav 120,152,208
X 182
Mvav 152,152,216
K 206,180,181
Mvav 156,156,216
X 183
Mvav 124,156,208
Mvav 92,156,200
K 207,182,183
Mvav 60,156,192
W 0
Cvaa 160,128,0,224
Ra 144,242,4096
Ra 148,242,5120
Mvaa 160,132,4
s_add_u32 m0, s62, 0
Ra 152,242,6144
Cvaa 164,132,20,228
X 184
Mvaa 164,128,16
X 185
Cvaa 168,128,32,232
X 186
Mvaa 168,132,36
Ra 156,242,7168
K 214,184,185
Cvaa 172,132,52,236
Dc 1,8,61,0
Mvaa 172,128,48
X 187
Mvaa 160,136,8
X 188
Mvaa 160,140,12
Ra 128,242,16384
K 215,186,187
Mvaa 164,140,28
Dc 1,8,61,1024
Mvaa 164,136,24
X 189
Mvaa 168,136,40
X 190
Mvaa 168,140,44
Ra 132,242,17408
K 222,188,189
Mvaa 172,140,60
Dc 1,8,61,2048
Mvaa 172,136,56
X 191
v_smfmac_f32_16x16x64_bf16 a[160:163], a[176:179], v[192:199], v240
Ra 136,242,18432
K 223,190,191
v_smfmac_f32_16x16x64_bf16 a[164:167], a[176:179], v[200:207], v240
X 160
v_smfmac_f32_16x16x64_bf16 a[168:171], a[176:179], v[208:215], v240
Dc 1,8,61,3072
v_smfmac_f32_16x16x64_bf16 a[172:175], a[176:179], v[216:223], v240
W 3
X 161
Cvaa 176,144,0,224
s_add_u32 m0, s62, 4096
X 162
Mvaa 176,148,4
Ra 140,242,19456
K 192,160,161
Cvaa 180,148,20,228
X 163
Mvaa 180,144,16
Dc 243,8,61,0
Cvaa 184,144,32,232
X 164
Mvaa 184,148,36
K 193,162,163
Cvaa 188,148,52,236
X 165
Mvaa 188,144,48
Dc 243,8,61,1024
Ra 144,242,20480
Mvaa 176,152,8
X 166
Mvaa 176,156,12
Ra 148,242,21504
K 200,164,165
Mvaa 180,156,28
X 167
Mvaa 180,152,24
Dc 243,8,61,2048
Mvaa 184,152,40
X 168
Mvaa 184,156,44
K 201,166,167
Mvaa 188,156,60
X 169
Mvaa 188,152,56
W 2
Dc 243,8,61,3072
Ra 152,242,22528
Mvav 32,128,196
s_add_u32 m0, s62, 8192
X 170
Mvav 64,128,204
Ra 156,242,23552
K 208,168,169
Mvav 96,128,212
X 171
Mvav 128,128,220
Dc 244,8,61,0
Mvav 132,132,220
X 172
Mvav 100,132,212
Ra 128,242,8192
K 209,170,171
Mvav 68,132,204
X 173
Mvav 36,132,196
Dc 244,8,61,1024
Mvav 40,136,196
X 174
Mvav 72,136,204
Ra 132,242,9216
K 216,172,173
Mvav 104,136,212
X 175
Mvav 136,136,220
Dc 244,8,61,2048
Mvav 140,140,220
X 176
Mvav 108,140,212
Ra 136,242,10240
K 217,174,175
Mvav 76,140,204
X 177
Mvav 44,140,196
W 3
Dc 244,8,61,3072
Mvav 48,144,196
X 178
Mvav 80,144,204
s_add_u32 m0, s62, 12288
Ra 140,242,11264
Mvav 112,144,212
K 194,176,177
Mvav 144,144,220
X 179
Mvav 148,148,220
X 180
Mvav 116,148,212
Dc 245,8,61,0
Ra 144,242,12288
Mvav 84,148,204
K 195,178,179
Mvav 52,148,196
X 181
Mvav 56,152,196
X 182
Mvav 88,152,204
Dc 245,8,61,1024
Ra 148,242,13312
Mvav 120,152,212
K 202,180,181
Mvav 152,152,220
X 183
Mvav 156,156,220
X 184
Mvav 124,156,212
Dc 245,8,61,2048
Ra 152,242,14336
Mvav 92,156,204
K 203,182,183
v_bfe_u32 v28, v1, 4, 4
Mvav 60,156,196
W 3
X 185
Cvaa 160,128,0,224
Ra 156,242,15360
v_lshlrev_b32 v28, 3, v28
Mvaa 160,132,4
s_lshl_b32 s80, s56, 9
s_add_u32 s80, s80, 4224
Dc 245,8,61,3072
Cvaa 164,132,20,228
s_add_u32 s61, s61, s63
X 186
Mvaa 164,128,16
K 210,184,185
Cvaa 168,128,32,232
X 187
Mvaa 168,132,36
buffer_load_dwordx2 v[28:29], v28, s[8:11], s80 offen sc1
Cvaa 172,132,52,236
s_xor_b32 s62, s62, 0x10000
X 188
Mvaa 172,128,48
Ra 128,242,24576
K 211,186,187
Mvaa 160,136,8
X 189
Mvaa 160,140,12
s_add_u32 s64, s64, 1
X 190
Mvaa 164,140,28
Ra 132,242,25600
K 218,188,189
Mvaa 164,136,24
X 191
Mvaa 168,136,40
)"
R"(Mvaa 168,140,44
X 160
Mvaa 172,140,60
buffer_store_dwordx4 a[228:231], v1, s[8:11], s36 offen offset:2048 sc1
K 219,190,191
Mvaa 172,136,56
W 2
X 161
Cvaa 176,144,0,224
Ra 136,242,26624
Ra 140,242,27648
Mvaa 176,148,4
X 162
Cvaa 180,148,20,228
K 196,160,161
Mvaa 180,144,16
X 163
Cvaa 184,144,32,232
Mvaa 184,148,36
X 164
Cvaa 188,148,52,236
buffer_store_dwordx4 a[76:79], v1, s[8:11], s36 offen offset:3072 sc1
K 197,162,163
Mvaa 188,144,48
X 165
Mvaa 176,152,8
Ra 144,242,28672
Ra 148,242,29696
Mvaa 176,156,12
X 166
Mvaa 180,156,28
K 204,164,165
Mvaa 180,152,24
X 167
Mvaa 184,152,40
s_add_u32 s36, s29, 327680
Mvaa 184,156,44
X 168
Mvaa 188,156,60
Mvaa 188,152,56
W 2
Ra 152,242,30720
K 205,166,167
Mvav 32,128,192
X 169
Mvav 64,128,200
Ra 156,242,31744
Mvav 96,128,208
X 170
Mvav 128,128,216
buffer_store_dwordx4 a[84:87], v1, s[8:11], s36 offen offset:0 sc1
K 212,168,169
Mvav 132,132,216
X 171
Mvav 100,132,208
X 172
Mvav 68,132,200
Mvav 36,132,192
K 213,170,171
Mvav 40,136,192
X 173
Mvav 72,136,200
buffer_store_dwordx4 a[92:95], v1, s[8:11], s36 offen offset:1024 sc1
Mvav 104,136,208
X 174
Mvav 136,136,216
s_waitcnt vmcnt(4)
v_cmp_eq_u64 vcc, s[12:13], v[28:29]
s_cmp_eq_u64 vcc, -1
s_cbranch_scc0 .Lspin_67%=
.Lok_68%=:
K 220,172,173
Mvav 140,140,216
X 175
Mvav 108,140,208
X 176
Mvav 76,140,200
W 0
s_barrier
v_xor_b32 v242, 0x10000, v242
Mvav 44,140,192
buffer_store_dwordx4 a[188:191], v1, s[8:11], s36 offen offset:2048 sc1
K 221,174,175
Mvav 48,144,192
Ra 128,242,0
Mvav 80,144,200
X 177
Mvav 112,144,208
X 178
Mvav 144,144,216
Ra 132,242,1024
K 198,176,177
Mvav 148,148,216
X 179
Mvav 116,148,208
X 180
Mvav 84,148,200
Ra 136,242,2048
buffer_store_dwordx4 a[196:199], v1, s[8:11], s36 offen offset:3072 sc1
Mvav 52,148,192
K 199,178,179
Mvav 56,152,192
X 181
Mvav 88,152,200
Ra 140,242,3072
Mvav 120,152,208
X 182
Mvav 152,152,216
K 206,180,181
Mvav 156,156,216
X 183
Mvav 124,156,208
Mvav 92,156,200
K 207,182,183
Mvav 60,156,192
W 0
Cvaa 160,128,0,224
Ra 144,242,4096
Ra 148,242,5120
Mvaa 160,132,4
s_add_u32 m0, s62, 0
Ra 152,242,6144
Cvaa 164,132,20,228
X 184
Mvaa 164,128,16
X 185
Cvaa 168,128,32,232
X 186
Mvaa 168,132,36
Ra 156,242,7168
K 214,184,185
Cvaa 172,132,52,236
Dc 1,8,61,0
Mvaa 172,128,48
X 187
Mvaa 160,136,8
X 188
Mvaa 160,140,12
Ra 128,242,16384
K 215,186,187
Mvaa 164,140,28
Dc 1,8,61,1024
Mvaa 164,136,24
X 189
Mvaa 168,136,40
X 190
Mvaa 168,140,44
Ra 132,242,17408
K 222,188,189
Mvaa 172,140,60
Dc 1,8,61,2048
Mvaa 172,136,56
X 191
v_smfmac_f32_16x16x64_bf16 a[160:163], a[176:179], v[192:199], v240
Ra 136,242,18432
K 223,190,191
v_smfmac_f32_16x16x64_bf16 a[164:167], a[176:179], v[200:207], v240
X 160
v_smfmac_f32_16x16x64_bf16 a[168:171], a[176:179], v[208:215], v240
Dc 1,8,61,3072
v_smfmac_f32_16x16x64_bf16 a[172:175], a[176:179], v[216:223], v240
W 3
X 161
Cvaa 176,144,0,224
s_add_u32 m0, s62, 4096
X 162
Mvaa 176,148,4
Ra 140,242,19456
K 192,160,161
Cvaa 180,148,20,228
X 163
Mvaa 180,144,16
Dc 243,8,61,0
Cvaa 184,144,32,232
X 164
Mvaa 184,148,36
K 193,162,163
Cvaa 188,148,52,236
X 165
Mvaa 188,144,48
Dc 243,8,61,1024
Ra 144,242,20480
Mvaa 176,152,8
X 166
Mvaa 176,156,12
Ra 148,242,21504
K 200,164,165
Mvaa 180,156,28
X 167
Mvaa 180,152,24
Dc 243,8,61,2048
Mvaa 184,152,40
X 168
Mvaa 184,156,44
K 201,166,167
Mvaa 188,156,60
X 169
Mvaa 188,152,56
W 2
Dc 243,8,61,3072
Ra 152,242,22528
Mvav 32,128,196
s_add_u32 m0, s62, 8192
X 170
Mvav 64,128,204
Ra 156,242,23552
K 208,168,169
Mvav 96,128,212
X 171
Mvav 128,128,220
Dc 244,8,61,0
Mvav 132,132,220
X 172
Mvav 100,132,212
Ra 128,242,8192
K 209,170,171
Mvav 68,132,204
X 173
Mvav 36,132,196
Dc 244,8,61,1024
Mvav 40,136,196
X 174
Mvav 72,136,204
Ra 132,242,9216
K 216,172,173
Mvav 104,136,212
X 175
Mvav 136,136,220
Dc 244,8,61,2048
Mvav 140,140,220
X 176
Mvav 108,140,212
Ra 136,242,10240
K 217,174,175
Mvav 76,140,204
X 177
Mvav 44,140,196
W 3
Dc 244,8,61,3072
Mvav 48,144,196
X 178
Mvav 80,144,204
s_add_u32 m0, s62, 12288
Ra 140,242,11264
Mvav 112,144,212
K 194,176,177
Mvav 144,144,220
X 179
Mvav 148,148,220
X 180
Mvav 116,148,212
Dc 245,8,61,0
Ra 144,242,12288
Mvav 84,148,204
K 195,178,179
Mvav 52,148,196
X 181
Mvav 56,152,196
X 182
Mvav 88,152,204
Dc 245,8,61,1024
Ra 148,242,13312
Mvav 120,152,212
K 202,180,181
Mvav 152,152,220
X 183
Mvav 156,156,220
X 184
Mvav 124,156,212
Dc 245,8,61,2048
Ra 152,242,14336
Mvav 92,156,204
K 203,182,183
Mvav 60,156,196
W 3
X 185
Cvaa 160,128,0,224
X 186
Mvaa 160,132,4
Dc 245,8,61,3072
Ra 156,242,15360
Cvaa 164,132,20,228
s_add_u32 s61, s61, s63
K 210,184,185
Mvaa 164,128,16
X 187
Cvaa 168,128,32,232
X 188
Mvaa 168,132,36
Cvaa 172,132,52,236
s_xor_b32 s62, s62, 0x10000
K 211,186,187
Mvaa 172,128,48
X 189
Mvaa 160,136,8
Ra 128,242,24576
Ra 132,242,25600
Mvaa 160,140,12
s_add_u32 s64, s64, 1
X 190
Mvaa 164,140,28
K 218,188,189
Mvaa 164,136,24
X 191
Mvaa 168,136,40
Mvaa 168,140,44
X 160
Mvaa 172,140,60
s_add_u32 s36, s29, 331776
K 219,190,191
Mvaa 172,136,56
W 2
X 161
Cvaa 176,144,0,224
Ra 136,242,26624
Ra 140,242,27648
Mvaa 176,148,4
X 162
Cvaa 180,148,20,228
K 196,160,161
Mvaa 180,144,16
X 163
Cvaa 184,144,32,232
buffer_store_dwordx4 a[236:239], v1, s[8:11], s36 offen offset:0 sc1
Mvaa 184,148,36
X 164
Cvaa 188,148,52,236
K 197,162,163
Mvaa 188,144,48
X 165
Mvaa 176,152,8
Ra 144,242,28672
Ra 148,242,29696
Mvaa 176,156,12
X 166
Mvaa 180,156,28
buffer_store_dwordx4 a[244:247], v1, s[8:11], s36 offen offset:1024 sc1
K 204,164,165
Mvaa 180,152,24
X 167
Mvaa 184,152,40
Mvaa 184,156,44
X 168
Mvaa 188,156,60
Mvaa 188,152,56
W 2
Ra 152,242,30720
K 205,166,167
Mvav 32,128,192
X 169
Mvav 64,128,200
buffer_store_dwordx4 a[68:71], v1, s[8:11], s36 offen offset:2048 sc1
Ra 156,242,31744
Mvav 96,128,208
X 170
Mvav 128,128,216
K 212,168,169
Mvav 132,132,216
X 171
Mvav 100,132,208
X 172
Mvav 68,132,200
buffer_store_dwordx4 a[184:187], v1, s[8:11], s36 offen offset:3072 sc1
Mvav 36,132,192
K 213,170,171
Mvav 40,136,192
X 173
Mvav 72,136,200
X 174
Mvav 104,136,208
Mvav 136,136,216
s_add_u32 s36, s29, 196608
K 220,172,173
Mvav 140,140,216
X 175
Mvav 108,140,208
X 176
Mvav 76,140,200
s_waitcnt vmcnt(4) lgkmcnt(0)
s_barrier
v_xor_b32 v242, 0x10000, v242
Mvav 44,140,192
Ra 128,242,0
K 221,174,175
Mvav 48,144,192
X 177
Mvav 80,144,200
buffer_store_dwordx4 a[192:195], v1, s[8:11], s36 offen offset:0 sc1
Mvav 112,144,208
X 178
Mvav 144,144,216
Ra 132,242,1024
K 198,176,177
Mvav 148,148,216
X 179
Mvav 116,148,208
X 180
Mvav 84,148,200
Ra 136,242,2048
Mvav 52,148,192
)"
R"(buffer_store_dwordx4 a[200:203], v1, s[8:11], s36 offen offset:1024 sc1
K 199,178,179
Mvav 56,152,192
X 181
Mvav 88,152,200
Ra 140,242,3072
Mvav 120,152,208
X 182
Mvav 152,152,216
K 206,180,181
Mvav 156,156,216
X 183
Mvav 124,156,208
Mvav 92,156,200
K 207,182,183
Mvav 60,156,192
W 0
Cvaa 160,128,0,224
Ra 144,242,4096
Ra 148,242,5120
Mvaa 160,132,4
s_add_u32 m0, s62, 0
Ra 152,242,6144
Cvaa 164,132,20,228
X 184
Mvaa 164,128,16
X 185
Cvaa 168,128,32,232
X 186
Mvaa 168,132,36
Ra 156,242,7168
K 214,184,185
Cvaa 172,132,52,236
Dc 1,8,61,0
Mvaa 172,128,48
X 187
Mvaa 160,136,8
X 188
Mvaa 160,140,12
Ra 128,242,16384
K 215,186,187
Mvaa 164,140,28
Dc 1,8,61,1024
Mvaa 164,136,24
X 189
Mvaa 168,136,40
X 190
Mvaa 168,140,44
Ra 132,242,17408
K 222,188,189
Mvaa 172,140,60
Dc 1,8,61,2048
Mvaa 172,136,56
X 191
v_smfmac_f32_16x16x64_bf16 a[160:163], a[176:179], v[192:199], v240
Ra 136,242,18432
K 223,190,191
v_smfmac_f32_16x16x64_bf16 a[164:167], a[176:179], v[200:207], v240
X 160
v_smfmac_f32_16x16x64_bf16 a[168:171], a[176:179], v[208:215], v240
Dc 1,8,61,3072
v_smfmac_f32_16x16x64_bf16 a[172:175], a[176:179], v[216:223], v240
W 3
X 161
Cvaa 176,144,0,224
s_add_u32 m0, s62, 4096
X 162
Mvaa 176,148,4
Ra 140,242,19456
K 192,160,161
Cvaa 180,148,20,228
X 163
Mvaa 180,144,16
Dc 243,8,61,0
Cvaa 184,144,32,232
X 164
Mvaa 184,148,36
K 193,162,163
Cvaa 188,148,52,236
X 165
Mvaa 188,144,48
Dc 243,8,61,1024
Ra 144,242,20480
Mvaa 176,152,8
X 166
Mvaa 176,156,12
Ra 148,242,21504
K 200,164,165
Mvaa 180,156,28
X 167
Mvaa 180,152,24
Dc 243,8,61,2048
Mvaa 184,152,40
X 168
Mvaa 184,156,44
K 201,166,167
Mvaa 188,156,60
X 169
Mvaa 188,152,56
W 2
Dc 243,8,61,3072
Ra 152,242,22528
Mvav 32,128,196
s_add_u32 m0, s62, 8192
X 170
Mvav 64,128,204
Ra 156,242,23552
K 208,168,169
Mvav 96,128,212
X 171
Mvav 128,128,220
Dc 244,8,61,0
Mvav 132,132,220
X 172
Mvav 100,132,212
Ra 128,242,8192
K 209,170,171
Mvav 68,132,204
X 173
Mvav 36,132,196
Dc 244,8,61,1024
Mvav 40,136,196
X 174
Mvav 72,136,204
Ra 132,242,9216
K 216,172,173
Mvav 104,136,212
X 175
Mvav 136,136,220
Dc 244,8,61,2048
Mvav 140,140,220
X 176
Mvav 108,140,212
Ra 136,242,10240
K 217,174,175
Mvav 76,140,204
X 177
Mvav 44,140,196
W 3
Dc 244,8,61,3072
Mvav 48,144,196
X 178
Mvav 80,144,204
s_add_u32 m0, s62, 12288
Ra 140,242,11264
Mvav 112,144,212
K 194,176,177
Mvav 144,144,220
X 179
Mvav 148,148,220
X 180
Mvav 116,148,212
Dc 245,8,61,0
Ra 144,242,12288
Mvav 84,148,204
K 195,178,179
Mvav 52,148,196
X 181
Mvav 56,152,196
X 182
Mvav 88,152,204
Dc 245,8,61,1024
Ra 148,242,13312
Mvav 120,152,212
K 202,180,181
Mvav 152,152,220
X 183
Mvav 156,156,220
s_waitcnt vmcnt(16)
s_add_u32 s80, s71, 256
v_mov_b64 v[28:29], s[12:13]
s_mov_b64 exec, 1
buffer_store_dwordx2 v[28:29], off, s[8:11], s80 sc1
s_mov_b64 exec, -1
X 184
Mvav 124,156,212
Ra 152,242,14336
Mvav 92,156,204
K 203,182,183
Mvav 60,156,196
W 3
X 185
Cvaa 160,128,0,224
Dc 245,8,61,2048
Ra 156,242,15360
Mvaa 160,132,4
X 186
Cvaa 164,132,20,228
K 210,184,185
Mvaa 164,128,16
X 187
Cvaa 168,128,32,232
Dc 245,8,61,3072
Mvaa 168,132,36
s_add_u32 s61, s61, s63
X 188
Cvaa 172,132,52,236
s_xor_b32 s62, s62, 0x10000
K 211,186,187
Mvaa 172,128,48
X 189
Mvaa 160,136,8
Ra 128,242,24576
Ra 132,242,25600
Mvaa 160,140,12
s_add_u32 s64, s64, 1
X 190
Mvaa 164,140,28
K 218,188,189
Mvaa 164,136,24
X 191
Mvaa 168,136,40
Mvaa 168,140,44
X 160
Mvaa 172,140,60
buffer_store_dwordx4 a[208:211], v1, s[8:11], s36 offen offset:2048 sc1
K 219,190,191
Mvaa 172,136,56
W 2
X 161
Cvaa 176,144,0,224
Ra 136,242,26624
Ra 140,242,27648
Mvaa 176,148,4
X 162
Cvaa 180,148,20,228
K 196,160,161
Mvaa 180,144,16
X 163
Cvaa 184,144,32,232
Mvaa 184,148,36
X 164
Cvaa 188,148,52,236
buffer_store_dwordx4 a[216:219], v1, s[8:11], s36 offen offset:3072 sc1
K 197,162,163
Mvaa 188,144,48
X 165
Mvaa 176,152,8
Ra 144,242,28672
Ra 148,242,29696
Mvaa 176,156,12
X 166
Mvaa 180,156,28
K 204,164,165
Mvaa 180,152,24
X 167
Mvaa 184,152,40
s_add_u32 s36, s29, 200704
Mvaa 184,156,44
X 168
Mvaa 188,156,60
Mvaa 188,152,56
W 2
Ra 152,242,30720
K 205,166,167
Mvav 32,128,192
X 169
Mvav 64,128,200
Ra 156,242,31744
Mvav 96,128,208
X 170
Mvav 128,128,216
buffer_store_dwordx4 a[224:227], v1, s[8:11], s36 offen offset:0 sc1
K 212,168,169
Mvav 132,132,216
X 171
Mvav 100,132,208
X 172
Mvav 68,132,200
Mvav 36,132,192
K 213,170,171
Mvav 40,136,192
X 173
Mvav 72,136,200
buffer_store_dwordx4 a[232:235], v1, s[8:11], s36 offen offset:1024 sc1
Mvav 104,136,208
X 174
Mvav 136,136,216
K 220,172,173
Mvav 140,140,216
X 175
Mvav 108,140,208
X 176
Mvav 76,140,200
s_waitcnt vmcnt(4) lgkmcnt(0)
s_barrier
v_xor_b32 v242, 0x10000, v242
Mvav 44,140,192
buffer_store_dwordx4 a[240:243], v1, s[8:11], s36 offen offset:2048 sc1
K 221,174,175
Mvav 48,144,192
Ra 128,242,0
Mvav 80,144,200
X 177
Mvav 112,144,208
X 178
Mvav 144,144,216
Ra 132,242,1024
K 198,176,177
Mvav 148,148,216
X 179
Mvav 116,148,208
X 180
Mvav 84,148,200
Ra 136,242,2048
buffer_store_dwordx4 a[100:103], v1, s[8:11], s36 offen offset:3072 sc1
Mvav 52,148,192
K 199,178,179
Mvav 56,152,192
X 181
Mvav 88,152,200
Ra 140,242,3072
Mvav 120,152,208
X 182
Mvav 152,152,216
K 206,180,181
Mvav 156,156,216
X 183
Mvav 124,156,208
Mvav 92,156,200
K 207,182,183
Mvav 60,156,192
W 0
Cvaa 160,128,0,224
Ra 144,242,4096
Ra 148,242,5120
Mvaa 160,132,4
s_add_u32 m0, s62, 0
Ra 152,242,6144
Cvaa 164,132,20,228
X 184
Mvaa 164,128,16
X 185
Cvaa 168,128,32,232
X 186
Mvaa 168,132,36
Ra 156,242,7168
K 214,184,185
Cvaa 172,132,52,236
Dc 1,8,61,0
Mvaa 172,128,48
X 187
Mvaa 160,136,8
X 188
Mvaa 160,140,12
Ra 128,242,16384
K 215,186,187
Mvaa 164,140,28
Dc 1,8,61,1024
Mvaa 164,136,24
X 189
Mvaa 168,136,40
X 190
Mvaa 168,140,44
Ra 132,242,17408
K 222,188,189
Mvaa 172,140,60
Dc 1,8,61,2048
Mvaa 172,136,56
X 191
v_smfmac_f32_16x16x64_bf16 a[160:163], a[176:179], v[192:199], v240
Ra 136,242,18432
K 223,190,191
v_smfmac_f32_16x16x64_bf16 a[164:167], a[176:179], v[200:207], v240
X 160
v_smfmac_f32_16x16x64_bf16 a[168:171], a[176:179], v[208:215], v240
Dc 1,8,61,3072
v_smfmac_f32_16x16x64_bf16 a[172:175], a[176:179], v[216:223], v240
W 3
X 161
Cvaa 176,144,0,224
s_add_u32 m0, s62, 4096
X 162
Mvaa 176,148,4
Ra 140,242,19456
K 192,160,161
Cvaa 180,148,20,228
X 163
Mvaa 180,144,16
Dc 243,8,61,0
Cvaa 184,144,32,232
X 164
Mvaa 184,148,36
K 193,162,163
Cvaa 188,148,52,236
X 165
Mvaa 188,144,48
Dc 243,8,61,1024
Ra 144,242,20480
Mvaa 176,152,8
X 166
Mvaa 176,156,12
Ra 148,242,21504
K 200,164,165
Mvaa 180,156,28
X 167
Mvaa 180,152,24
Dc 243,8,61,2048
Mvaa 184,152,40
X 168
)"
R"(Mvaa 184,156,44
K 201,166,167
Mvaa 188,156,60
X 169
Mvaa 188,152,56
W 2
Dc 243,8,61,3072
Ra 152,242,22528
Mvav 32,128,196
s_add_u32 m0, s62, 8192
X 170
Mvav 64,128,204
Ra 156,242,23552
K 208,168,169
Mvav 96,128,212
X 171
Mvav 128,128,220
Dc 244,8,61,0
Mvav 132,132,220
X 172
Mvav 100,132,212
Ra 128,242,8192
K 209,170,171
Mvav 68,132,204
X 173
Mvav 36,132,196
Dc 244,8,61,1024
Mvav 40,136,196
X 174
Mvav 72,136,204
Ra 132,242,9216
K 216,172,173
Mvav 104,136,212
X 175
Mvav 136,136,220
Dc 244,8,61,2048
Mvav 140,140,220
X 176
Mvav 108,140,212
Ra 136,242,10240
K 217,174,175
Mvav 76,140,204
X 177
Mvav 44,140,196
W 3
Dc 244,8,61,3072
Mvav 48,144,196
X 178
Mvav 80,144,204
s_add_u32 m0, s62, 12288
Ra 140,242,11264
Mvav 112,144,212
K 194,176,177
Mvav 144,144,220
X 179
Mvav 148,148,220
X 180
Mvav 116,148,212
Dc 245,8,61,0
Ra 144,242,12288
Mvav 84,148,204
K 195,178,179
Mvav 52,148,196
X 181
Mvav 56,152,196
X 182
Mvav 88,152,204
Dc 245,8,61,1024
Ra 148,242,13312
Mvav 120,152,212
K 202,180,181
Mvav 152,152,220
X 183
Mvav 156,156,220
X 184
Mvav 124,156,212
Dc 245,8,61,2048
Ra 152,242,14336
Mvav 92,156,204
K 203,182,183
Mvav 60,156,196
W 3
X 185
Cvaa 160,128,0,224
X 186
Mvaa 160,132,4
Dc 245,8,61,3072
Ra 156,242,15360
Cvaa 164,132,20,228
s_add_u32 s61, s61, s63
K 210,184,185
Mvaa 164,128,16
X 187
Cvaa 168,128,32,232
X 188
Mvaa 168,132,36
Cvaa 172,132,52,236
s_xor_b32 s62, s62, 0x10000
K 211,186,187
Mvaa 172,128,48
X 189
Mvaa 160,136,8
Ra 128,242,24576
Ra 132,242,25600
Mvaa 160,140,12
s_add_u32 s64, s64, 1
X 190
Mvaa 164,140,28
K 218,188,189
Mvaa 164,136,24
X 191
Mvaa 168,136,40
Mvaa 168,140,44
X 160
Mvaa 172,140,60
s_add_u32 s36, s29, 262144
K 219,190,191
Mvaa 172,136,56
W 2
X 161
Cvaa 176,144,0,224
Ra 136,242,26624
Ra 140,242,27648
Mvaa 176,148,4
X 162
Cvaa 180,148,20,228
K 196,160,161
Mvaa 180,144,16
X 163
Cvaa 184,144,32,232
buffer_store_dwordx4 a[64:67], v1, s[8:11], s36 offen offset:0 sc1
Mvaa 184,148,36
X 164
Cvaa 188,148,52,236
K 197,162,163
Mvaa 188,144,48
X 165
Mvaa 176,152,8
Ra 144,242,28672
Ra 148,242,29696
Mvaa 176,156,12
X 166
Mvaa 180,156,28
buffer_store_dwordx4 a[72:75], v1, s[8:11], s36 offen offset:1024 sc1
K 204,164,165
Mvaa 180,152,24
X 167
Mvaa 184,152,40
Mvaa 184,156,44
X 168
Mvaa 188,156,60
Mvaa 188,152,56
W 2
Ra 152,242,30720
K 205,166,167
Mvav 32,128,192
X 169
Mvav 64,128,200
buffer_store_dwordx4 a[80:83], v1, s[8:11], s36 offen offset:2048 sc1
Ra 156,242,31744
Mvav 96,128,208
X 170
Mvav 128,128,216
K 212,168,169
Mvav 132,132,216
X 171
Mvav 100,132,208
X 172
Mvav 68,132,200
buffer_store_dwordx4 a[88:91], v1, s[8:11], s36 offen offset:3072 sc1
Mvav 36,132,192
K 213,170,171
Mvav 40,136,192
X 173
Mvav 72,136,200
X 174
Mvav 104,136,208
Mvav 136,136,216
s_add_u32 s36, s29, 266240
K 220,172,173
Mvav 140,140,216
X 175
Mvav 108,140,208
X 176
Mvav 76,140,200
s_waitcnt vmcnt(4) lgkmcnt(0)
s_barrier
v_xor_b32 v242, 0x10000, v242
Mvav 44,140,192
Ra 128,242,0
K 221,174,175
Mvav 48,144,192
X 177
Mvav 80,144,200
buffer_store_dwordx4 a[96:99], v1, s[8:11], s36 offen offset:0 sc1
Mvav 112,144,208
X 178
Mvav 144,144,216
Ra 132,242,1024
K 198,176,177
Mvav 148,148,216
X 179
Mvav 116,148,208
X 180
Mvav 84,148,200
Ra 136,242,2048
Mvav 52,148,192
buffer_store_dwordx4 a[104:107], v1, s[8:11], s36 offen offset:1024 sc1
K 199,178,179
Mvav 56,152,192
X 181
Mvav 88,152,200
Ra 140,242,3072
Mvav 120,152,208
X 182
Mvav 152,152,216
K 206,180,181
Mvav 156,156,216
X 183
Mvav 124,156,208
Mvav 92,156,200
K 207,182,183
Mvav 60,156,192
W 0
Cvaa 160,128,0,224
Ra 144,242,4096
Ra 148,242,5120
Mvaa 160,132,4
s_add_u32 m0, s62, 0
Ra 152,242,6144
Cvaa 164,132,20,228
X 184
Mvaa 164,128,16
X 185
Cvaa 168,128,32,232
X 186
Mvaa 168,132,36
Ra 156,242,7168
K 214,184,185
Cvaa 172,132,52,236
Dc 1,8,61,0
Mvaa 172,128,48
X 187
Mvaa 160,136,8
X 188
Mvaa 160,140,12
Ra 128,242,16384
K 215,186,187
Mvaa 164,140,28
Dc 1,8,61,1024
Mvaa 164,136,24
X 189
Mvaa 168,136,40
X 190
Mvaa 168,140,44
Ra 132,242,17408
K 222,188,189
Mvaa 172,140,60
Dc 1,8,61,2048
Mvaa 172,136,56
X 191
v_smfmac_f32_16x16x64_bf16 a[160:163], a[176:179], v[192:199], v240
Ra 136,242,18432
K 223,190,191
v_smfmac_f32_16x16x64_bf16 a[164:167], a[176:179], v[200:207], v240
X 160
v_smfmac_f32_16x16x64_bf16 a[168:171], a[176:179], v[208:215], v240
Dc 1,8,61,3072
v_smfmac_f32_16x16x64_bf16 a[172:175], a[176:179], v[216:223], v240
W 3
X 161
Cvaa 176,144,0,224
s_add_u32 m0, s62, 4096
X 162
Mvaa 176,148,4
Ra 140,242,19456
K 192,160,161
Cvaa 180,148,20,228
X 163
Mvaa 180,144,16
Dc 243,8,61,0
Cvaa 184,144,32,232
X 164
Mvaa 184,148,36
K 193,162,163
Cvaa 188,148,52,236
X 165
Mvaa 188,144,48
Dc 243,8,61,1024
Ra 144,242,20480
Mvaa 176,152,8
X 166
Mvaa 176,156,12
Ra 148,242,21504
K 200,164,165
Mvaa 180,156,28
X 167
Mvaa 180,152,24
Dc 243,8,61,2048
Mvaa 184,152,40
X 168
Mvaa 184,156,44
K 201,166,167
Mvaa 188,156,60
X 169
Mvaa 188,152,56
W 2
Dc 243,8,61,3072
Ra 152,242,22528
Mvav 32,128,196
s_add_u32 m0, s62, 8192
X 170
Mvav 64,128,204
Ra 156,242,23552
K 208,168,169
Mvav 96,128,212
X 171
Mvav 128,128,220
Dc 244,8,61,0
Mvav 132,132,220
X 172
Mvav 100,132,212
Ra 128,242,8192
K 209,170,171
Mvav 68,132,204
X 173
Mvav 36,132,196
Dc 244,8,61,1024
Mvav 40,136,196
X 174
Mvav 72,136,204
Ra 132,242,9216
K 216,172,173
Mvav 104,136,212
X 175
Mvav 136,136,220
Dc 244,8,61,2048
Mvav 140,140,220
X 176
Mvav 108,140,212
Ra 136,242,10240
K 217,174,175
Mvav 76,140,204
X 177
Mvav 44,140,196
W 3
Dc 244,8,61,3072
Mvav 48,144,196
X 178
Mvav 80,144,204
s_add_u32 m0, s62, 12288
Ra 140,242,11264
Mvav 112,144,212
K 194,176,177
Mvav 144,144,220
X 179
Mvav 148,148,220
X 180
Mvav 116,148,212
Dc 245,8,61,0
Ra 144,242,12288
Mvav 84,148,204
K 195,178,179
Mvav 52,148,196
X 181
Mvav 56,152,196
X 182
Mvav 88,152,204
Dc 245,8,61,1024
Ra 148,242,13312
Mvav 120,152,212
K 202,180,181
Mvav 152,152,220
X 183
Mvav 156,156,220
X 184
Mvav 124,156,212
Dc 245,8,61,2048
Ra 152,242,14336
Mvav 92,156,204
K 203,182,183
v_bfe_u32 v28, v1, 4, 4
Mvav 60,156,196
W 3
X 185
Cvaa 160,128,0,224
Ra 156,242,15360
v_lshlrev_b32 v28, 3, v28
Mvaa 160,132,4
s_lshl_b32 s80, s56, 9
s_add_u32 s80, s80, 4352
Dc 245,8,61,3072
Cvaa 164,132,20,228
s_add_u32 s61, s61, s63
X 186
Mvaa 164,128,16
K 210,184,185
Cvaa 168,128,32,232
X 187
Mvaa 168,132,36
)"
R"(buffer_load_dwordx2 v[28:29], v28, s[8:11], s80 offen sc1
Cvaa 172,132,52,236
s_xor_b32 s62, s62, 0x10000
X 188
Mvaa 172,128,48
Ra 128,242,24576
K 211,186,187
Mvaa 160,136,8
X 189
Mvaa 160,140,12
s_add_u32 s64, s64, 1
X 190
Mvaa 164,140,28
Ra 132,242,25600
K 218,188,189
Mvaa 164,136,24
X 191
Mvaa 168,136,40
Mvaa 168,140,44
X 160
Mvaa 172,140,60
buffer_store_dwordx4 a[112:115], v1, s[8:11], s36 offen offset:2048 sc1
K 219,190,191
Mvaa 172,136,56
W 2
X 161
Cvaa 176,144,0,224
Ra 136,242,26624
Ra 140,242,27648
Mvaa 176,148,4
X 162
Cvaa 180,148,20,228
K 196,160,161
Mvaa 180,144,16
X 163
Cvaa 184,144,32,232
Mvaa 184,148,36
X 164
Cvaa 188,148,52,236
K 197,162,163
Mvaa 188,144,48
X 165
Mvaa 176,152,8
Ra 144,242,28672
Ra 148,242,29696
Mvaa 176,156,12
X 166
Mvaa 180,156,28
K 204,164,165
Mvaa 180,152,24
X 167
Mvaa 184,152,40
Mvaa 184,156,44
X 168
Mvaa 188,156,60
Mvaa 188,152,56
W 2
Ra 152,242,30720
K 205,166,167
Mvav 32,128,192
X 169
Mvav 64,128,200
Ra 156,242,31744
Mvav 96,128,208
X 170
Mvav 128,128,216
K 212,168,169
Mvav 132,132,216
X 171
Mvav 100,132,208
X 172
Mvav 68,132,200
buffer_store_dwordx4 a[120:123], v1, s[8:11], s36 offen offset:3072 sc1
Mvav 36,132,192
K 213,170,171
Mvav 40,136,192
X 173
Mvav 72,136,200
X 174
Mvav 104,136,208
Mvav 136,136,216
s_waitcnt vmcnt(2)
v_cmp_eq_u64 vcc, s[12:13], v[28:29]
s_cmp_eq_u64 vcc, -1
s_cbranch_scc0 .Lspin_70%=
.Lok_71%=:
K 220,172,173
Mvav 140,140,216
X 175
Mvav 108,140,208
X 176
Mvav 76,140,200
W 0
s_barrier
v_xor_b32 v242, 0x10000, v242
Mvav 44,140,192
Ra 128,242,0
K 221,174,175
Mvav 48,144,192
X 177
Mvav 80,144,200
X 178
Mvav 112,144,208
Ra 132,242,1024
Mvav 144,144,216
K 198,176,177
Mvav 148,148,216
X 179
Mvav 116,148,208
X 180
Mvav 84,148,200
Ra 136,242,2048
Mvav 52,148,192
K 199,178,179
Mvav 56,152,192
X 181
Mvav 88,152,200
Ra 140,242,3072
Mvav 120,152,208
X 182
Mvav 152,152,216
K 206,180,181
Mvav 156,156,216
X 183
Mvav 124,156,208
Mvav 92,156,200
K 207,182,183
Mvav 60,156,192
W 0
Cvaa 160,128,0,224
Ra 144,242,4096
Ra 148,242,5120
Mvaa 160,132,4
s_add_u32 m0, s62, 0
Ra 152,242,6144
Cvaa 164,132,20,228
X 184
Mvaa 164,128,16
X 185
Cvaa 168,128,32,232
X 186
Mvaa 168,132,36
Ra 156,242,7168
K 214,184,185
Cvaa 172,132,52,236
Dc 1,8,61,0
Mvaa 172,128,48
X 187
Mvaa 160,136,8
X 188
Mvaa 160,140,12
Ra 128,242,16384
K 215,186,187
Mvaa 164,140,28
Dc 1,8,61,1024
Mvaa 164,136,24
X 189
Mvaa 168,136,40
X 190
Mvaa 168,140,44
Ra 132,242,17408
K 222,188,189
Mvaa 172,140,60
Dc 1,8,61,2048
Mvaa 172,136,56
X 191
v_smfmac_f32_16x16x64_bf16 a[160:163], a[176:179], v[192:199], v240
Ra 136,242,18432
K 223,190,191
v_smfmac_f32_16x16x64_bf16 a[164:167], a[176:179], v[200:207], v240
X 160
v_smfmac_f32_16x16x64_bf16 a[168:171], a[176:179], v[208:215], v240
Dc 1,8,61,3072
v_smfmac_f32_16x16x64_bf16 a[172:175], a[176:179], v[216:223], v240
W 3
X 161
Cvaa 176,144,0,224
s_add_u32 m0, s62, 4096
X 162
Mvaa 176,148,4
Ra 140,242,19456
K 192,160,161
Cvaa 180,148,20,228
X 163
Mvaa 180,144,16
Dc 243,8,61,0
Cvaa 184,144,32,232
X 164
Mvaa 184,148,36
K 193,162,163
Cvaa 188,148,52,236
X 165
Mvaa 188,144,48
Dc 243,8,61,1024
Ra 144,242,20480
Mvaa 176,152,8
X 166
Mvaa 176,156,12
Ra 148,242,21504
K 200,164,165
Mvaa 180,156,28
X 167
Mvaa 180,152,24
Dc 243,8,61,2048
Mvaa 184,152,40
X 168
Mvaa 184,156,44
K 201,166,167
Mvaa 188,156,60
X 169
Mvaa 188,152,56
W 2
Dc 243,8,61,3072
Ra 152,242,22528
Mvav 32,128,196
s_add_u32 m0, s62, 8192
X 170
Mvav 64,128,204
Ra 156,242,23552
K 208,168,169
Mvav 96,128,212
X 171
Mvav 128,128,220
Dc 244,8,61,0
Mvav 132,132,220
X 172
Mvav 100,132,212
Ra 128,242,8192
K 209,170,171
Mvav 68,132,204
X 173
Mvav 36,132,196
Dc 244,8,61,1024
Mvav 40,136,196
X 174
Mvav 72,136,204
Ra 132,242,9216
K 216,172,173
Mvav 104,136,212
X 175
Mvav 136,136,220
Dc 244,8,61,2048
Mvav 140,140,220
X 176
Mvav 108,140,212
Ra 136,242,10240
K 217,174,175
Mvav 76,140,204
X 177
Mvav 44,140,196
W 3
Dc 244,8,61,3072
Mvav 48,144,196
X 178
Mvav 80,144,204
s_add_u32 m0, s62, 12288
Ra 140,242,11264
Mvav 112,144,212
K 194,176,177
Mvav 144,144,220
X 179
Mvav 148,148,220
X 180
Mvav 116,148,212
Dc 245,8,61,0
Ra 144,242,12288
Mvav 84,148,204
K 195,178,179
Mvav 52,148,196
X 181
Mvav 56,152,196
X 182
Mvav 88,152,204
Dc 245,8,61,1024
Ra 148,242,13312
Mvav 120,152,212
K 202,180,181
Mvav 152,152,220
s_add_u32 s81, s57, 2
X 183
Mvav 156,156,220
s_waitcnt vmcnt(14)
s_add_u32 s80, s71, 384
v_mov_b64 v[28:29], s[12:13]
s_mov_b64 exec, 1
buffer_store_dwordx2 v[28:29], off, s[8:11], s80 sc1
s_mov_b64 exec, -1
X 184
Mvav 124,156,212
s_bitcmp0_b32 s81, 2
s_cselect_b32 s22, -1, 0
s_and_b32 s81, s81, 3
Ra 152,242,14336
Mvav 92,156,204
s_lshl_b32 s82, s56, 2
K 203,182,183
Mvav 60,156,196
W 3
X 185
Cvaa 160,128,0,224
s_add_u32 s81, s81, s82
Dc 245,8,61,2048
Ra 156,242,15360
Mvaa 160,132,4
s_lshl_b32 s81, s81, 1
X 186
Cvaa 164,132,20,228
s_add_u32 s81, s81, s14
s_sub_u32 s81, s81, 2
K 210,184,185
Mvaa 164,128,16
s_mul_i32 s81, s81, 20480
X 187
Cvaa 168,128,32,232
Dc 245,8,61,3072
v_min_u32 v2, 624, v1
Mvaa 168,132,36
s_add_u32 s61, s61, s63
X 188
Cvaa 172,132,52,236
s_xor_b32 s62, s62, 0x10000
v_lshl_add_u32 v2, v2, 3, s81
K 211,186,187
Mvaa 172,128,48
Ra 128,242,24576
v_and_b32 v2, s22, v2
Mvaa 160,136,8
s_and_b32 s22, s22, 5120
X 189
Mvaa 160,140,12
s_add_u32 s64, s64, 1
global_load_dword v3, v2, s[26:27]
Ra 132,242,25600
Mvaa 164,140,28
X 190
Mvaa 164,136,24
v_add_u32 v2, s22, v2
K 218,188,189
Mvaa 168,136,40
X 191
Mvaa 168,140,44
X 160
Mvaa 172,140,60
K 219,190,191
Mvaa 172,136,56
W 2
X 161
Cvaa 176,144,0,224
Ra 136,242,26624
Ra 140,242,27648
Mvaa 176,148,4
X 162
Cvaa 180,148,20,228
K 196,160,161
Mvaa 180,144,16
X 163
Cvaa 184,144,32,232
Mvaa 184,148,36
X 164
Cvaa 188,148,52,236
K 197,162,163
Mvaa 188,144,48
X 165
Mvaa 176,152,8
Ra 144,242,28672
Ra 148,242,29696
Mvaa 176,156,12
X 166
Mvaa 180,156,28
K 204,164,165
Mvaa 180,152,24
X 167
Mvaa 184,152,40
Mvaa 184,156,44
X 168
Mvaa 188,156,60
Mvaa 188,152,56
W 2
Ra 152,242,30720
K 205,166,167
Mvav 32,128,192
X 169
Mvav 64,128,200
Ra 156,242,31744
Mvav 96,128,208
X 170
Mvav 128,128,216
K 212,168,169
Mvav 132,132,216
X 171
Mvav 100,132,208
X 172
Mvav 68,132,200
Mvav 36,132,192
K 213,170,171
Mvav 40,136,192
X 173
Mvav 72,136,200
X 174
Mvav 104,136,208
)"
R"(Mvav 136,136,216
K 220,172,173
Mvav 140,140,216
X 175
Mvav 108,140,208
X 176
Mvav 76,140,200
s_waitcnt vmcnt(1) lgkmcnt(0)
s_barrier
v_xor_b32 v242, 0x10000, v242
Mvav 44,140,192
Ra 128,242,0
K 221,174,175
Mvav 48,144,192
X 177
Mvav 80,144,200
X 178
Mvav 112,144,208
Ra 132,242,1024
Mvav 144,144,216
K 198,176,177
Mvav 148,148,216
X 179
Mvav 116,148,208
X 180
Mvav 84,148,200
Ra 136,242,2048
Mvav 52,148,192
K 199,178,179
Mvav 56,152,192
X 181
Mvav 88,152,200
Ra 140,242,3072
Mvav 120,152,208
X 182
Mvav 152,152,216
K 206,180,181
Mvav 156,156,216
X 183
Mvav 124,156,208
Mvav 92,156,200
K 207,182,183
Mvav 60,156,192
W 0
Cvaa 160,128,0,224
Ra 144,242,4096
Ra 148,242,5120
Mvaa 160,132,4
s_add_u32 m0, s62, 0
Ra 152,242,6144
Cvaa 164,132,20,228
X 184
Mvaa 164,128,16
X 185
Cvaa 168,128,32,232
X 186
Mvaa 168,132,36
Ra 156,242,7168
K 214,184,185
Cvaa 172,132,52,236
Dc 1,8,61,0
Mvaa 172,128,48
X 187
Mvaa 160,136,8
X 188
Mvaa 160,140,12
Ra 128,242,16384
K 215,186,187
Mvaa 164,140,28
Dc 1,8,61,1024
Mvaa 164,136,24
X 189
Mvaa 168,136,40
X 190
Mvaa 168,140,44
Ra 132,242,17408
K 222,188,189
Mvaa 172,140,60
Dc 1,8,61,2048
Mvaa 172,136,56
X 191
v_smfmac_f32_16x16x64_bf16 a[160:163], a[176:179], v[192:199], v240
Ra 136,242,18432
K 223,190,191
v_smfmac_f32_16x16x64_bf16 a[164:167], a[176:179], v[200:207], v240
X 160
v_smfmac_f32_16x16x64_bf16 a[168:171], a[176:179], v[208:215], v240
Dc 1,8,61,3072
v_smfmac_f32_16x16x64_bf16 a[172:175], a[176:179], v[216:223], v240
W 3
X 161
Cvaa 176,144,0,224
s_add_u32 m0, s62, 4096
X 162
Mvaa 176,148,4
Ra 140,242,19456
K 192,160,161
Cvaa 180,148,20,228
X 163
Mvaa 180,144,16
Dc 243,8,61,0
Cvaa 184,144,32,232
X 164
Mvaa 184,148,36
K 193,162,163
Cvaa 188,148,52,236
X 165
Mvaa 188,144,48
Dc 243,8,61,1024
Ra 144,242,20480
Mvaa 176,152,8
X 166
Mvaa 176,156,12
Ra 148,242,21504
K 200,164,165
Mvaa 180,156,28
X 167
Mvaa 180,152,24
Dc 243,8,61,2048
Mvaa 184,152,40
X 168
Mvaa 184,156,44
K 201,166,167
Mvaa 188,156,60
X 169
Mvaa 188,152,56
W 2
Dc 243,8,61,3072
Ra 152,242,22528
Mvav 32,128,196
s_add_u32 m0, s62, 8192
X 170
Mvav 64,128,204
Ra 156,242,23552
K 208,168,169
Mvav 96,128,212
X 171
Mvav 128,128,220
Dc 244,8,61,0
Mvav 132,132,220
X 172
Mvav 100,132,212
Ra 128,242,8192
K 209,170,171
Mvav 68,132,204
X 173
Mvav 36,132,196
Dc 244,8,61,1024
Mvav 40,136,196
X 174
Mvav 72,136,204
Ra 132,242,9216
K 216,172,173
Mvav 104,136,212
X 175
Mvav 136,136,220
Dc 244,8,61,2048
Mvav 140,140,220
X 176
Mvav 108,140,212
Ra 136,242,10240
K 217,174,175
Mvav 76,140,204
X 177
Mvav 44,140,196
W 3
Dc 244,8,61,3072
Mvav 48,144,196
X 178
Mvav 80,144,204
s_add_u32 m0, s62, 12288
Ra 140,242,11264
Mvav 112,144,212
K 194,176,177
Mvav 144,144,220
X 179
Mvav 148,148,220
X 180
Mvav 116,148,212
Dc 245,8,61,0
Ra 144,242,12288
Mvav 84,148,204
K 195,178,179
Mvav 52,148,196
X 181
Mvav 56,152,196
X 182
Mvav 88,152,204
Dc 245,8,61,1024
Ra 148,242,13312
Mvav 120,152,212
K 202,180,181
Mvav 152,152,220
X 183
Mvav 156,156,220
X 184
Mvav 124,156,212
Dc 245,8,61,2048
Ra 152,242,14336
Mvav 92,156,204
K 203,182,183
Mvav 60,156,196
W 3
X 185
Cvaa 160,128,0,224
X 186
Mvaa 160,132,4
Dc 245,8,61,3072
Ra 156,242,15360
Cvaa 164,132,20,228
s_add_u32 s61, s61, s63
K 210,184,185
Mvaa 164,128,16
X 187
Cvaa 168,128,32,232
X 188
Mvaa 168,132,36
Cvaa 172,132,52,236
s_xor_b32 s62, s62, 0x10000
global_load_dword v3, v2, s[26:27]
K 211,186,187
Mvaa 172,128,48
X 189
Mvaa 160,136,8
Ra 128,242,24576
Ra 132,242,25600
Mvaa 160,140,12
s_add_u32 s64, s64, 1
X 190
Mvaa 164,140,28
v_add_u32 v2, s22, v2
K 218,188,189
Mvaa 164,136,24
X 191
Mvaa 168,136,40
Mvaa 168,140,44
X 160
Mvaa 172,140,60
K 219,190,191
Mvaa 172,136,56
W 2
X 161
Cvaa 176,144,0,224
Ra 136,242,26624
Ra 140,242,27648
Mvaa 176,148,4
X 162
Cvaa 180,148,20,228
K 196,160,161
Mvaa 180,144,16
X 163
Cvaa 184,144,32,232
Mvaa 184,148,36
X 164
Cvaa 188,148,52,236
K 197,162,163
Mvaa 188,144,48
X 165
Mvaa 176,152,8
Ra 144,242,28672
Ra 148,242,29696
Mvaa 176,156,12
X 166
Mvaa 180,156,28
K 204,164,165
Mvaa 180,152,24
X 167
Mvaa 184,152,40
Mvaa 184,156,44
X 168
Mvaa 188,156,60
Mvaa 188,152,56
W 2
Ra 152,242,30720
K 205,166,167
Mvav 32,128,192
X 169
Mvav 64,128,200
Ra 156,242,31744
Mvav 96,128,208
X 170
Mvav 128,128,216
K 212,168,169
Mvav 132,132,216
X 171
Mvav 100,132,208
X 172
Mvav 68,132,200
Mvav 36,132,192
K 213,170,171
Mvav 40,136,192
X 173
Mvav 72,136,200
X 174
Mvav 104,136,208
Mvav 136,136,216
K 220,172,173
Mvav 140,140,216
X 175
Mvav 108,140,208
X 176
Mvav 76,140,200
s_waitcnt vmcnt(1) lgkmcnt(0)
s_barrier
v_xor_b32 v242, 0x10000, v242
Mvav 44,140,192
Ra 128,242,0
K 221,174,175
Mvav 48,144,192
X 177
Mvav 80,144,200
X 178
Mvav 112,144,208
Ra 132,242,1024
Mvav 144,144,216
K 198,176,177
Mvav 148,148,216
X 179
Mvav 116,148,208
X 180
Mvav 84,148,200
Ra 136,242,2048
Mvav 52,148,192
K 199,178,179
Mvav 56,152,192
X 181
Mvav 88,152,200
Ra 140,242,3072
Mvav 120,152,208
X 182
Mvav 152,152,216
K 206,180,181
Mvav 156,156,216
X 183
Mvav 124,156,208
Mvav 92,156,200
K 207,182,183
Mvav 60,156,192
W 0
Cvaa 160,128,0,224
Ra 144,242,4096
Ra 148,242,5120
Mvaa 160,132,4
s_add_u32 m0, s62, 0
Ra 152,242,6144
Cvaa 164,132,20,228
X 184
Mvaa 164,128,16
X 185
Cvaa 168,128,32,232
X 186
Mvaa 168,132,36
Ra 156,242,7168
K 214,184,185
Cvaa 172,132,52,236
Dc 1,8,61,0
Mvaa 172,128,48
X 187
Mvaa 160,136,8
X 188
Mvaa 160,140,12
Ra 128,242,16384
K 215,186,187
Mvaa 164,140,28
Dc 1,8,61,1024
Mvaa 164,136,24
X 189
Mvaa 168,136,40
X 190
Mvaa 168,140,44
Ra 132,242,17408
K 222,188,189
Mvaa 172,140,60
Dc 1,8,61,2048
Mvaa 172,136,56
X 191
v_smfmac_f32_16x16x64_bf16 a[160:163], a[176:179], v[192:199], v240
Ra 136,242,18432
K 223,190,191
v_smfmac_f32_16x16x64_bf16 a[164:167], a[176:179], v[200:207], v240
X 160
v_smfmac_f32_16x16x64_bf16 a[168:171], a[176:179], v[208:215], v240
Dc 1,8,61,3072
v_smfmac_f32_16x16x64_bf16 a[172:175], a[176:179], v[216:223], v240
W 3
X 161
Cvaa 176,144,0,224
s_add_u32 m0, s62, 4096
X 162
Mvaa 176,148,4
Ra 140,242,19456
K 192,160,161
Cvaa 180,148,20,228
X 163
Mvaa 180,144,16
Dc 243,8,61,0
Cvaa 184,144,32,232
X 164
Mvaa 184,148,36
K 193,162,163
)"
R"(Cvaa 188,148,52,236
X 165
Mvaa 188,144,48
Dc 243,8,61,1024
Ra 144,242,20480
Mvaa 176,152,8
X 166
Mvaa 176,156,12
Ra 148,242,21504
K 200,164,165
Mvaa 180,156,28
X 167
Mvaa 180,152,24
Dc 243,8,61,2048
Mvaa 184,152,40
X 168
Mvaa 184,156,44
K 201,166,167
Mvaa 188,156,60
X 169
Mvaa 188,152,56
W 2
Dc 243,8,61,3072
Ra 152,242,22528
Mvav 32,128,196
s_add_u32 m0, s62, 8192
X 170
Mvav 64,128,204
Ra 156,242,23552
K 208,168,169
Mvav 96,128,212
X 171
Mvav 128,128,220
Dc 244,8,61,0
Mvav 132,132,220
X 172
Mvav 100,132,212
Ra 128,242,8192
K 209,170,171
Mvav 68,132,204
X 173
Mvav 36,132,196
Dc 244,8,61,1024
Mvav 40,136,196
X 174
Mvav 72,136,204
Ra 132,242,9216
K 216,172,173
Mvav 104,136,212
X 175
Mvav 136,136,220
Dc 244,8,61,2048
Mvav 140,140,220
X 176
Mvav 108,140,212
Ra 136,242,10240
K 217,174,175
Mvav 76,140,204
X 177
Mvav 44,140,196
W 3
Dc 244,8,61,3072
Mvav 48,144,196
X 178
Mvav 80,144,204
s_add_u32 m0, s62, 12288
Ra 140,242,11264
Mvav 112,144,212
K 194,176,177
Mvav 144,144,220
X 179
Mvav 148,148,220
X 180
Mvav 116,148,212
Dc 245,8,61,0
Ra 144,242,12288
Mvav 84,148,204
K 195,178,179
Mvav 52,148,196
X 181
Mvav 56,152,196
X 182
Mvav 88,152,204
Dc 245,8,61,1024
Ra 148,242,13312
Mvav 120,152,212
K 202,180,181
Mvav 152,152,220
X 183
Mvav 156,156,220
X 184
Mvav 124,156,212
Dc 245,8,61,2048
Ra 152,242,14336
Mvav 92,156,204
K 203,182,183
Mvav 60,156,196
W 3
X 185
Cvaa 160,128,0,224
X 186
Mvaa 160,132,4
Dc 245,8,61,3072
Ra 156,242,15360
Cvaa 164,132,20,228
s_add_u32 s61, s61, s63
K 210,184,185
Mvaa 164,128,16
X 187
Cvaa 168,128,32,232
X 188
Mvaa 168,132,36
Cvaa 172,132,52,236
s_xor_b32 s62, s62, 0x10000
global_load_dword v3, v2, s[26:27]
K 211,186,187
Mvaa 172,128,48
Ra 128,242,24576
v_add_u32 v2, s22, v2
Mvaa 160,136,8
s_lshr_b32 s80, s15, 3
s_lshl_b32 s80, s80, 1
s_add_u32 s80, s80, s14
Mvaa 160,140,12
s_sub_u32 s80, s80, 2
s_lshl_b32 s80, s80, 12
v_min_u32 v27, 496, v1
Mvaa 164,140,28
s_add_u32 s64, s64, 1
X 189
Mvaa 164,136,24
Ra 132,242,25600
v_lshl_add_u32 v27, v27, 3, s80
Mvaa 168,136,40
X 190
Mvaa 168,140,44
v_min_u32 v27, .Lend%=-.Lbeg%=-4, v27
K 218,188,189
Mvaa 172,140,60
global_load_dword v3, v27, s[6:7]
Mvaa 172,136,56
W 2
X 191
Cvaa 176,144,0,224
X 160
Mvaa 176,148,4
Ra 136,242,26624
K 219,190,191
Cvaa 180,148,20,228
X 161
Mvaa 180,144,16
Ra 140,242,27648
Cvaa 184,144,32,232
X 162
Mvaa 184,148,36
K 196,160,161
Cvaa 188,148,52,236
X 163
Mvaa 188,144,48
X 164
Mvaa 176,152,8
Ra 144,242,28672
K 197,162,163
Mvaa 176,156,12
X 165
Mvaa 180,156,28
Ra 148,242,29696
Mvaa 180,152,24
X 166
Mvaa 184,152,40
K 204,164,165
Mvaa 184,156,44
X 167
Mvaa 188,156,60
X 168
Mvaa 188,152,56
W 2
Ra 152,242,30720
K 205,166,167
Mvav 32,128,192
X 169
Mvav 64,128,200
Ra 156,242,31744
Mvav 96,128,208
X 170
Mvav 128,128,216
K 212,168,169
Mvav 132,132,216
X 171
Mvav 100,132,208
X 172
Mvav 68,132,200
Mvav 36,132,192
K 213,170,171
Mvav 40,136,192
X 173
Mvav 72,136,200
X 174
Mvav 104,136,208
Mvav 136,136,216
K 220,172,173
Mvav 140,140,216
X 175
Mvav 108,140,208
X 176
Mvav 76,140,200
s_waitcnt vmcnt(2) lgkmcnt(0)
s_barrier
v_xor_b32 v242, 0x10000, v242
Mvav 44,140,192
Ra 128,242,0
K 221,174,175
Mvav 48,144,192
X 177
Mvav 80,144,200
X 178
Mvav 112,144,208
Ra 132,242,1024
Mvav 144,144,216
K 198,176,177
Mvav 148,148,216
X 179
Mvav 116,148,208
X 180
Mvav 84,148,200
Ra 136,242,2048
Mvav 52,148,192
K 199,178,179
Mvav 56,152,192
X 181
Mvav 88,152,200
Ra 140,242,3072
Mvav 120,152,208
X 182
Mvav 152,152,216
K 206,180,181
Mvav 156,156,216
X 183
Mvav 124,156,208
Mvav 92,156,200
K 207,182,183
Mvav 60,156,192
W 0
Cvaa 160,128,0,224
Ra 144,242,4096
Ra 148,242,5120
Mvaa 160,132,4
s_add_u32 m0, s62, 0
Ra 152,242,6144
Cvaa 164,132,20,228
X 184
Mvaa 164,128,16
X 185
Cvaa 168,128,32,232
X 186
Mvaa 168,132,36
Ra 156,242,7168
K 214,184,185
Cvaa 172,132,52,236
Dc 1,8,61,0
Mvaa 172,128,48
X 187
Mvaa 160,136,8
X 188
Mvaa 160,140,12
Ra 128,242,16384
K 215,186,187
Mvaa 164,140,28
Dc 1,8,61,1024
Mvaa 164,136,24
X 189
Mvaa 168,136,40
X 190
Mvaa 168,140,44
Ra 132,242,17408
K 222,188,189
Mvaa 172,140,60
Dc 1,8,61,2048
Mvaa 172,136,56
X 191
v_smfmac_f32_16x16x64_bf16 a[160:163], a[176:179], v[192:199], v240
Ra 136,242,18432
K 223,190,191
v_smfmac_f32_16x16x64_bf16 a[164:167], a[176:179], v[200:207], v240
X 160
v_smfmac_f32_16x16x64_bf16 a[168:171], a[176:179], v[208:215], v240
Dc 1,8,61,3072
v_smfmac_f32_16x16x64_bf16 a[172:175], a[176:179], v[216:223], v240
W 3
X 161
Cvaa 176,144,0,224
s_add_u32 m0, s62, 4096
X 162
Mvaa 176,148,4
s_sub_u32 s81, s57, 1
Ra 140,242,19456
K 192,160,161
Cvaa 180,148,20,228
X 163
Mvaa 180,144,16
s_lshl_b32 s85, s81, 5
Dc 243,8,61,0
Cvaa 184,144,32,232
X 164
Mvaa 184,148,36
s_add_u32 s85, s85, s56
K 193,162,163
Cvaa 188,148,52,236
X 165
Mvaa 188,144,48
Dc 243,8,61,1024
Ra 144,242,20480
Mvaa 176,152,8
s_lshl_b32 s85, s85, 1
X 166
Mvaa 176,156,12
Ra 148,242,21504
K 200,164,165
Mvaa 180,156,28
s_add_u32 s85, s85, s14
X 167
Mvaa 180,152,24
Dc 243,8,61,2048
Mvaa 184,152,40
s_sub_u32 s85, s85, 2
X 168
Mvaa 184,156,44
K 201,166,167
Mvaa 188,156,60
X 169
Mvaa 188,152,56
W 2
s_cmp_lt_u32 s85, 160
s_cselect_b64 s[20:21], 0, -1
Dc 243,8,61,3072
Mvav 32,128,196
s_add_u32 m0, s62, 8192
Ra 152,242,22528
Ra 156,242,23552
Mvav 64,128,204
s_mul_i32 s18, s85, 205
X 170
Mvav 96,128,212
K 208,168,169
Mvav 128,128,220
s_lshr_b32 s18, s18, 12
X 171
Mvav 132,132,220
Dc 244,8,61,0
Ra 128,242,8192
Mvav 100,132,212
X 172
Mvav 68,132,204
s_and_b32 s18, s18, 7
K 209,170,171
Mvav 36,132,196
X 173
Mvav 40,136,196
Dc 244,8,61,1024
v_lshrrev_b32 v4, 1, v1
Mvav 72,136,204
X 174
Mvav 104,136,212
Ra 132,242,9216
K 216,172,173
Mvav 136,136,220
X 175
Mvav 140,140,220
Dc 244,8,61,2048
v_min_u32 v5, 344, v4
Mvav 108,140,212
s_lshl_b32 s81, s18, 3
X 176
Mvav 76,140,204
Ra 136,242,10240
K 217,174,175
Mvav 44,140,196
W 3
X 177
Mvav 48,144,196
Dc 244,8,61,3072
v_mov_b32 v6, s81
Mvav 80,144,204
s_add_u32 m0, s62, 12288
X 178
Mvav 112,144,212
Ra 140,242,11264
K 194,176,177
Mvav 144,144,220
X 179
Mvav 148,148,220
buffer_load_dwordx2 v[8:9], v4, s[8:11], 0 offen offset:2560 sc1
Ra 144,242,12288
Mvav 116,148,212
X 180
Mvav 84,148,204
K 195,178,179
Mvav 52,148,196
X 181
Mvav 56,152,196
)"
R"(buffer_load_dwordx2 v[10:11], v5, s[8:11], 0 offen offset:3072 sc1
Ra 148,242,13312
Mvav 88,152,204
X 182
Mvav 120,152,212
K 202,180,181
Mvav 152,152,220
X 183
Mvav 156,156,220
Dc 245,8,61,0
Ra 152,242,14336
Mvav 124,156,212
X 184
Mvav 92,156,204
K 203,182,183
v_bfe_u32 v28, v1, 4, 4
Mvav 60,156,196
W 3
X 185
Cvaa 160,128,0,224
buffer_load_dwordx2 v[12:13], v6, s[8:11], 0 offen offset:3584 sc1
v_lshlrev_b32 v28, 3, v28
Mvaa 160,132,4
s_lshl_b32 s80, s56, 9
X 186
Cvaa 164,132,20,228
s_add_u32 s80, s80, 4480
Ra 156,242,15360
K 210,184,185
Mvaa 164,128,16
X 187
Cvaa 168,128,32,232
Dc 245,8,61,1024
Mvaa 168,132,36
X 188
Cvaa 172,132,52,236
K 211,186,187
Mvaa 172,128,48
X 189
Mvaa 160,136,8
Dc 245,8,61,2048
Ra 128,242,24576
Mvaa 160,140,12
X 190
Mvaa 164,140,28
Ra 132,242,25600
K 218,188,189
Mvaa 164,136,24
X 191
Mvaa 168,136,40
buffer_load_dwordx2 v[28:29], v28, s[8:11], s80 offen sc1
Mvaa 168,140,44
Dc 245,8,61,3072
s_add_u32 s61, s61, s63
s_xor_b32 s62, s62, 0x10000
s_add_u32 s64, s64, 1
Mvaa 172,140,60
X 160
Mvaa 172,136,56
W 2
Ra 136,242,26624
K 219,190,191
Cvaa 176,144,0,224
X 161
Mvaa 176,148,4
global_load_dword v3, v2, s[26:27]
v_add_u32 v2, s22, v2
Cvaa 180,148,20,228
Ra 140,242,27648
Mvaa 180,144,16
X 162
Cvaa 184,144,32,232
K 196,160,161
Mvaa 184,148,36
X 163
Cvaa 188,148,52,236
X 164
Mvaa 188,144,48
Ra 144,242,28672
K 197,162,163
Mvaa 176,152,8
X 165
Mvaa 176,156,12
Ra 148,242,29696
Mvaa 180,156,28
X 166
Mvaa 180,152,24
K 204,164,165
Mvaa 184,152,40
X 167
Mvaa 184,156,44
X 168
Mvaa 188,156,60
Mvaa 188,152,56
W 2
Ra 152,242,30720
K 205,166,167
Mvav 32,128,192
X 169
Mvav 64,128,200
Ra 156,242,31744
Mvav 96,128,208
X 170
Mvav 128,128,216
K 212,168,169
Mvav 132,132,216
X 171
Mvav 100,132,208
X 172
Mvav 68,132,200
Mvav 36,132,192
K 213,170,171
Mvav 40,136,192
X 173
Mvav 72,136,200
X 174
Mvav 104,136,208
Mvav 136,136,216
s_waitcnt vmcnt(2)
v_cmp_eq_u64 vcc, s[12:13], v[28:29]
s_cmp_eq_u64 vcc, -1
s_cbranch_scc0 .Lspin_73%=
.Lok_74%=:
v_cmp_eq_u64 vcc, s[12:13], v[8:9]
v_cmp_eq_u64_e64 s[86:87], s[12:13], v[10:11]
s_and_b64 vcc, vcc, s[86:87]
s_or_b64 vcc, vcc, s[20:21]
s_cmp_eq_u64 vcc, -1
s_cbranch_scc0 .Lspin_76%=
.Lok_77%=:
K 220,172,173
Mvav 140,140,216
X 175
Mvav 108,140,208
X 176
Mvav 76,140,200
s_waitcnt vmcnt(1) lgkmcnt(0)
s_barrier
v_xor_b32 v242, 0x10000, v242
Mvav 44,140,192
Ra 128,242,0
K 221,174,175
Mvav 48,144,192
X 177
Mvav 80,144,200
X 178
Mvav 112,144,208
Ra 132,242,1024
Mvav 144,144,216
K 198,176,177
Mvav 148,148,216
X 179
Mvav 116,148,208
X 180
Mvav 84,148,200
Ra 136,242,2048
Mvav 52,148,192
K 199,178,179
Mvav 56,152,192
X 181
Mvav 88,152,200
Ra 140,242,3072
Mvav 120,152,208
X 182
Mvav 152,152,216
K 206,180,181
Mvav 156,156,216
X 183
Mvav 124,156,208
Mvav 92,156,200
K 207,182,183
Mvav 60,156,192
W 0
Cvaa 160,128,0,224
Ra 144,242,4096
Ra 148,242,5120
Mvaa 160,132,4
s_add_u32 m0, s62, 0
Ra 152,242,6144
Cvaa 164,132,20,228
X 184
Mvaa 164,128,16
X 185
Cvaa 168,128,32,232
X 186
Mvaa 168,132,36
Ra 156,242,7168
K 214,184,185
Cvaa 172,132,52,236
Dc 1,8,61,0
Mvaa 172,128,48
X 187
Mvaa 160,136,8
X 188
Mvaa 160,140,12
Ra 128,242,16384
K 215,186,187
Mvaa 164,140,28
Dc 1,8,61,1024
Mvaa 164,136,24
X 189
Mvaa 168,136,40
X 190
Mvaa 168,140,44
Ra 132,242,17408
K 222,188,189
Mvaa 172,140,60
Dc 1,8,61,2048
Mvaa 172,136,56
X 191
v_smfmac_f32_16x16x64_bf16 a[160:163], a[176:179], v[192:199], v240
Ra 136,242,18432
K 223,190,191
v_smfmac_f32_16x16x64_bf16 a[164:167], a[176:179], v[200:207], v240
X 160
v_smfmac_f32_16x16x64_bf16 a[168:171], a[176:179], v[208:215], v240
Dc 1,8,61,3072
v_smfmac_f32_16x16x64_bf16 a[172:175], a[176:179], v[216:223], v240
W 3
X 161
Cvaa 176,144,0,224
s_add_u32 m0, s62, 4096
X 162
Mvaa 176,148,4
Ra 140,242,19456
K 192,160,161
Cvaa 180,148,20,228
X 163
Mvaa 180,144,16
Dc 243,8,61,0
Cvaa 184,144,32,232
X 164
Mvaa 184,148,36
K 193,162,163
Cvaa 188,148,52,236
X 165
Mvaa 188,144,48
Dc 243,8,61,1024
Ra 144,242,20480
Mvaa 176,152,8
X 166
Mvaa 176,156,12
Ra 148,242,21504
K 200,164,165
Mvaa 180,156,28
X 167
Mvaa 180,152,24
Dc 243,8,61,2048
Mvaa 184,152,40
X 168
Mvaa 184,156,44
K 201,166,167
Mvaa 188,156,60
X 169
Mvaa 188,152,56
W 2
Dc 243,8,61,3072
Ra 152,242,22528
Mvav 32,128,196
s_add_u32 m0, s62, 8192
X 170
Mvav 64,128,204
Ra 156,242,23552
K 208,168,169
Mvav 96,128,212
X 171
Mvav 128,128,220
Dc 244,8,61,0
Mvav 132,132,220
X 172
Mvav 100,132,212
Ra 128,242,8192
K 209,170,171
Mvav 68,132,204
X 173
Mvav 36,132,196
Dc 244,8,61,1024
Mvav 40,136,196
X 174
Mvav 72,136,204
Ra 132,242,9216
K 216,172,173
Mvav 104,136,212
X 175
Mvav 136,136,220
Dc 244,8,61,2048
Mvav 140,140,220
X 176
Mvav 108,140,212
Ra 136,242,10240
K 217,174,175
Mvav 76,140,204
X 177
Mvav 44,140,196
W 3
Dc 244,8,61,3072
Mvav 48,144,196
X 178
Mvav 80,144,204
s_add_u32 m0, s62, 12288
Ra 140,242,11264
Mvav 112,144,212
K 194,176,177
Mvav 144,144,220
X 179
Mvav 148,148,220
X 180
Mvav 116,148,212
Dc 245,8,61,0
Ra 144,242,12288
Mvav 84,148,204
K 195,178,179
Mvav 52,148,196
X 181
Mvav 56,152,196
X 182
Mvav 88,152,204
Dc 245,8,61,1024
Ra 148,242,13312
Mvav 120,152,212
v_xor_b32 v6, s12, v12
K 202,180,181
Mvav 152,152,220
X 183
Mvav 156,156,220
Ra 152,242,14336
v_and_b32 v6, 2047, v6
Mvav 124,156,212
Dc 245,8,61,2048
v_lshlrev_b32 v6, 3, v6
Mvav 92,156,204
X 184
Mvav 60,156,196
W 3
K 203,182,183
Cvaa 160,128,0,224
s_mov_b32 s81, 40960
X 185
Mvaa 160,132,4
Dc 245,8,61,3072
Ra 156,242,15360
Cvaa 164,132,20,228
s_add_u32 s61, s61, s63
X 186
Mvaa 164,128,16
K 210,184,185
Cvaa 168,128,32,232
X 187
Mvaa 168,132,36
buffer_load_dword v7, v6, s[8:11], s81 offen sc1
v_lshrrev_b32 v5, 7, v1
Cvaa 172,132,52,236
s_xor_b32 s62, s62, 0x10000
X 188
Mvaa 172,128,48
K 211,186,187
v_mul_u32_u24 v4, 20480, v5
Mvaa 160,136,8
X 189
Mvaa 160,140,12
s_add_u32 s64, s64, 1
Ra 128,242,24576
Ra 132,242,25600
Mvaa 164,140,28
X 190
Mvaa 164,136,24
v_and_b32 v6, 0x70, v1
K 218,188,189
Mvaa 168,136,40
X 191
Mvaa 168,140,44
v_add_u32 v4, v4, v6
Mvaa 172,140,60
X 160
Mvaa 172,136,56
W 2
v_add_u32 v5, 2, v5
K 219,190,191
Cvaa 176,144,0,224
X 161
Mvaa 176,148,4
Ra 136,242,26624
v_lshrrev_b32 v5, 3, v5
Cvaa 180,148,20,228
Ra 140,242,27648
v_lshl_or_b32 v5, v5, 31, v4
Mvaa 180,144,16
X 162
Cvaa 184,144,32,232
K 196,160,161
Mvaa 184,148,36
s_lshl_b32 s19, s85, 7
X 163
Cvaa 188,148,52,236
X 164
Mvaa 188,144,48
s_add_u32 s19, s19, 2752512
Ra 144,242,28672
K 197,162,163
Mvaa 176,152,8
)"
R"(X 165
Mvaa 176,156,12
s_and_b32 s81, s20, 134217728
Ra 148,242,29696
Mvaa 180,156,28
X 166
Mvaa 180,152,24
K 204,164,165
Mvaa 184,152,40
s_or_b32 s19, s19, s81
X 167
Mvaa 184,156,44
X 168
Mvaa 188,156,60
buffer_load_dwordx4 a[64:67], v4, s[8:11], s19 offen sc1
Mvaa 188,152,56
W 2
Ra 152,242,30720
K 205,166,167
Mvav 32,128,192
s_add_u32 s19, s19, 163840
X 169
Mvav 64,128,200
Ra 156,242,31744
Mvav 96,128,208
X 170
Mvav 128,128,216
buffer_load_dwordx4 a[68:71], v4, s[8:11], s19 offen sc1
K 212,168,169
Mvav 132,132,216
X 171
Mvav 100,132,208
s_add_u32 s19, s19, 163840
X 172
Mvav 68,132,200
Mvav 36,132,192
buffer_load_dwordx4 a[72:75], v4, s[8:11], s19 offen sc1
K 213,170,171
Mvav 40,136,192
X 173
Mvav 72,136,200
X 174
Mvav 104,136,208
s_add_u32 s19, s19, 163840
Mvav 136,136,216
K 220,172,173
Mvav 140,140,216
X 175
Mvav 108,140,208
buffer_load_dwordx4 a[76:79], v4, s[8:11], s19 offen sc1
Mvav 76,140,200
s_waitcnt vmcnt(5) lgkmcnt(0)
s_barrier
v_xor_b32 v242, 0x10000, v242
Mvav 44,140,192
s_add_u32 s19, s19, 163840
X 176
Mvav 48,144,192
Ra 128,242,0
K 221,174,175
Mvav 80,144,200
X 177
Mvav 112,144,208
buffer_load_dwordx4 a[80:83], v4, s[8:11], s19 offen sc1
Ra 132,242,1024
Mvav 144,144,216
s_add_u32 s19, s19, 163840
X 178
Mvav 148,148,216
K 198,176,177
Mvav 116,148,208
X 179
Mvav 84,148,200
buffer_load_dwordx4 a[84:87], v4, s[8:11], s19 offen sc1
Ra 136,242,2048
Mvav 52,148,192
X 180
Mvav 56,152,192
s_add_u32 s19, s19, 163840
Ra 140,242,3072
K 199,178,179
Mvav 88,152,200
X 181
Mvav 120,152,208
X 182
Mvav 152,152,216
buffer_load_dwordx4 a[88:91], v5, s[8:11], s19 offen sc1
K 206,180,181
Mvav 156,156,216
X 183
Mvav 124,156,208
Mvav 92,156,200
K 207,182,183
Mvav 60,156,192
W 0
Cvaa 160,128,0,224
Ra 144,242,4096
Ra 148,242,5120
Mvaa 160,132,4
s_add_u32 m0, s62, 0
Ra 152,242,6144
Cvaa 164,132,20,228
X 184
Mvaa 164,128,16
X 185
Cvaa 168,128,32,232
X 186
Mvaa 168,132,36
Ra 156,242,7168
K 214,184,185
Cvaa 172,132,52,236
Dc 1,8,61,0
Mvaa 172,128,48
X 187
Mvaa 160,136,8
X 188
Mvaa 160,140,12
Ra 128,242,16384
K 215,186,187
Mvaa 164,140,28
Dc 1,8,61,1024
Mvaa 164,136,24
X 189
Mvaa 168,136,40
X 190
Mvaa 168,140,44
Ra 132,242,17408
K 222,188,189
Mvaa 172,140,60
Dc 1,8,61,2048
Mvaa 172,136,56
X 191
v_smfmac_f32_16x16x64_bf16 a[160:163], a[176:179], v[192:199], v240
Ra 136,242,18432
K 223,190,191
v_smfmac_f32_16x16x64_bf16 a[164:167], a[176:179], v[200:207], v240
X 160
v_smfmac_f32_16x16x64_bf16 a[168:171], a[176:179], v[208:215], v240
Dc 1,8,61,3072
v_smfmac_f32_16x16x64_bf16 a[172:175], a[176:179], v[216:223], v240
W 3
X 161
Cvaa 176,144,0,224
s_add_u32 m0, s62, 4096
X 162
Mvaa 176,148,4
Ra 140,242,19456
K 192,160,161
Cvaa 180,148,20,228
X 163
Mvaa 180,144,16
Dc 243,8,61,0
Cvaa 184,144,32,232
X 164
Mvaa 184,148,36
K 193,162,163
Cvaa 188,148,52,236
X 165
Mvaa 188,144,48
Dc 243,8,61,1024
Ra 144,242,20480
Mvaa 176,152,8
X 166
Mvaa 176,156,12
Ra 148,242,21504
K 200,164,165
Mvaa 180,156,28
X 167
Mvaa 180,152,24
Dc 243,8,61,2048
Mvaa 184,152,40
X 168
Mvaa 184,156,44
K 201,166,167
Mvaa 188,156,60
X 169
Mvaa 188,152,56
W 2
Dc 243,8,61,3072
Ra 152,242,22528
Mvav 32,128,196
s_add_u32 m0, s62, 8192
X 170
Mvav 64,128,204
Ra 156,242,23552
K 208,168,169
Mvav 96,128,212
X 171
Mvav 128,128,220
Dc 244,8,61,0
Mvav 132,132,220
X 172
Mvav 100,132,212
Ra 128,242,8192
K 209,170,171
Mvav 68,132,204
X 173
Mvav 36,132,196
Dc 244,8,61,1024
Mvav 40,136,196
X 174
Mvav 72,136,204
Ra 132,242,9216
K 216,172,173
Mvav 104,136,212
X 175
Mvav 136,136,220
Dc 244,8,61,2048
Mvav 140,140,220
s_bfe_u32 s81, s59, 0x10001
X 176
Mvav 108,140,212
s_lshl_b32 s82, s57, 1
Ra 136,242,10240
K 217,174,175
Mvav 76,140,204
s_add_u32 s81, s81, s82
X 177
Mvav 44,140,196
W 3
s_lshl_b32 s83, s81, 2
Dc 244,8,61,3072
Mvav 48,144,196
s_lshr_b32 s81, s59, 2
X 178
Mvav 80,144,204
s_add_u32 m0, s62, 12288
s_lshl_b32 s81, s81, 1
Ra 140,242,11264
Mvav 112,144,212
s_add_u32 s83, s83, s81
K 194,176,177
Mvav 144,144,220
s_and_b32 s81, s59, 1
X 179
Mvav 148,148,220
s_add_u32 s83, s83, s81
X 180
Mvav 116,148,212
s_and_b32 s81, s14, 1
Dc 245,8,61,0
Ra 144,242,12288
Mvav 84,148,204
s_mul_i32 s81, s81, 124
K 195,178,179
Mvav 52,148,196
s_xor_b32 s83, s83, s81
X 181
Mvav 56,152,196
s_mul_i32 s83, s83, 20480
X 182
Mvav 88,152,204
s_mul_i32 s81, s58, 320
Dc 245,8,61,1024
Ra 148,242,13312
Mvav 120,152,212
s_add_u32 s82, s83, s81
K 202,180,181
Mvav 152,152,220
X 183
Mvav 156,156,220
Ra 152,242,14336
v_bfe_u32 v246, v1, 4, 4
Mvav 124,156,212
Dc 245,8,61,2048
v_mul_u32_u24 v246, 1280, v246
Mvav 92,156,204
X 184
Mvav 60,156,196
W 3
v_lshrrev_b32 v247, 8, v1
K 203,182,183
Cvaa 160,128,0,224
v_mul_u32_u24 v247, 104, v247
v_add3_u32 v246, v246, v247, s82
Mvaa 160,132,4
X 185
Cvaa 164,132,20,228
Dc 245,8,61,3072
Ra 156,242,15360
Mvaa 164,128,16
s_add_u32 s61, s61, s63
X 186
Cvaa 168,128,32,232
K 210,184,185
Mvaa 168,132,36
X 187
Cvaa 172,132,52,236
s_xor_b32 s62, s62, 0x10000
X 188
Mvaa 172,128,48
global_load_dword v3, v246, s[16:17]
K 211,186,187
Mvaa 160,136,8
s_lshl_b32 s81, s81, 3
s_add_u32 s83, s83, s81
s_add_u32 s83, s83, 131072
Mvaa 160,140,12
s_add_u32 s64, s64, 1
v_lshrrev_b32 v246, 9, v1
v_mul_u32_u24 v246, 10240, v246
Mvaa 164,140,28
Ra 128,242,24576
v_and_b32 v247, 0x1f0, v1
Mvaa 164,136,24
X 189
Mvaa 168,136,40
Ra 132,242,25600
v_min_u32 v247, 304, v247
Mvaa 168,140,44
X 190
Mvaa 172,140,60
v_lshl_add_u32 v246, v247, 3, v246
K 218,188,189
Mvaa 172,136,56
W 2
buffer_load_dword v3, v246, s[8:11], s83 offen sc1
Ra 136,242,26624
Cvaa 176,144,0,224
X 191
Mvaa 176,148,4
X 160
Cvaa 180,148,20,228
Ra 140,242,27648
K 219,190,191
Mvaa 180,144,16
X 161
Cvaa 184,144,32,232
X 162
Mvaa 184,148,36
K 196,160,161
Cvaa 188,148,52,236
X 163
Mvaa 188,144,48
X 164
Mvaa 176,152,8
Ra 144,242,28672
K 197,162,163
Mvaa 176,156,12
X 165
Mvaa 180,156,28
Ra 148,242,29696
Mvaa 180,152,24
X 166
Mvaa 184,152,40
K 204,164,165
Mvaa 184,156,44
X 167
Mvaa 188,156,60
X 168
Mvaa 188,152,56
W 2
Ra 152,242,30720
K 205,166,167
Mvav 32,128,192
X 169
Mvav 64,128,200
Ra 156,242,31744
Mvav 96,128,208
X 170
Mvav 128,128,216
K 212,168,169
Mvav 132,132,216
X 171
Mvav 100,132,208
X 172
Mvav 68,132,200
Mvav 36,132,192
K 213,170,171
Mvav 40,136,192
X 173
Mvav 72,136,200
X 174
Mvav 104,136,208
Mvav 136,136,216
K 220,172,173
Mvav 140,140,216
X 175
Mvav 108,140,208
X 176
Mvav 76,140,200
s_waitcnt vmcnt(2) lgkmcnt(0)
s_barrier
v_xor_b32 v242, 0x10000, v242
Mvav 44,140,192
Ra 128,242,0
K 221,174,175
Mvav 48,144,192
X 177
Mvav 80,144,200
X 178
Mvav 112,144,208
Ra 132,242,1024
Mvav 144,144,216
K 198,176,177
Mvav 148,148,216
X 179
Mvav 116,148,208
X 180
Mvav 84,148,200
)"
R"(Ra 136,242,2048
Mvav 52,148,192
K 199,178,179
Mvav 56,152,192
X 181
Mvav 88,152,200
Ra 140,242,3072
Mvav 120,152,208
X 182
Mvav 152,152,216
K 206,180,181
Mvav 156,156,216
X 183
Mvav 124,156,208
Mvav 92,156,200
K 207,182,183
Mvav 60,156,192
W 0
Cvaa 160,128,0,224
Ra 144,242,4096
Ra 148,242,5120
Mvaa 160,132,4
s_add_u32 m0, s62, 0
Ra 152,242,6144
G 8,64
Cvaa 164,132,20,228
X 184
Mvaa 164,128,16
X 185
Cvaa 168,128,32,232
X 186
Mvaa 168,132,36
G 9,65
K 214,184,185
Cvaa 172,132,52,236
Ra 156,242,7168
Dc 1,8,61,0
Mvaa 172,128,48
X 187
Mvaa 160,136,8
X 188
Mvaa 160,140,12
Ra 128,242,16384
G 10,66
Mvaa 164,140,28
Dc 1,8,61,1024
K 215,186,187
Mvaa 164,136,24
X 189
Mvaa 168,136,40
Ra 132,242,17408
G 11,67
Mvaa 168,140,44
X 190
Mvaa 172,140,60
Dc 1,8,61,2048
K 222,188,189
Mvaa 172,136,56
X 191
v_smfmac_f32_16x16x64_bf16 a[160:163], a[176:179], v[192:199], v240
G 12,68
K 223,190,191
v_smfmac_f32_16x16x64_bf16 a[164:167], a[176:179], v[200:207], v240
Ra 136,242,18432
v_smfmac_f32_16x16x64_bf16 a[168:171], a[176:179], v[208:215], v240
Dc 1,8,61,3072
G 13,69
v_smfmac_f32_16x16x64_bf16 a[172:175], a[176:179], v[216:223], v240
W 3
X 160
Cvaa 176,144,0,224
s_add_u32 m0, s62, 4096
X 161
Mvaa 176,148,4
X 162
Cvaa 180,148,20,228
Ra 140,242,19456
K 192,160,161
Mvaa 180,144,16
Dc 243,8,61,0
G 14,70
Cvaa 184,144,32,232
X 163
Mvaa 184,148,36
X 164
Cvaa 188,148,52,236
G 15,71
K 193,162,163
Mvaa 188,144,48
Dc 243,8,61,1024
Ra 144,242,20480
Mvaa 176,152,8
X 165
Mvaa 176,156,12
X 166
Mvaa 180,156,28
Ra 148,242,21504
A 8,8,12
Mvaa 180,152,24
Dc 243,8,61,2048
K 200,164,165
Mvaa 184,152,40
X 167
Mvaa 184,156,44
A 9,9,13
Mvaa 188,156,60
X 168
Mvaa 188,152,56
W 2
Dc 243,8,61,3072
K 201,166,167
Mvav 32,128,196
s_add_u32 m0, s62, 8192
X 169
Mvav 64,128,204
Ra 152,242,22528
A 10,10,14
Mvav 96,128,212
X 170
Mvav 128,128,220
Ra 156,242,23552
K 208,168,169
Mvav 132,132,220
Dc 244,8,61,0
A 11,11,15
Mvav 100,132,212
X 171
Mvav 68,132,204
X 172
Mvav 36,132,196
Ra 128,242,8192
K 209,170,171
Mvav 40,136,196
Dc 244,8,61,1024
G 12,72
Mvav 72,136,204
X 173
Mvav 104,136,212
X 174
Mvav 136,136,220
Ra 132,242,9216
G 13,73
Mvav 140,140,220
Dc 244,8,61,2048
K 216,172,173
Mvav 108,140,212
X 175
Mvav 76,140,204
X 176
Mvav 44,140,196
W 2
Ra 136,242,10240
G 14,74
Mvav 48,144,196
Dc 244,8,61,3072
K 217,174,175
Mvav 80,144,204
s_add_u32 m0, s62, 12288
X 177
Mvav 112,144,212
Ra 140,242,11264
G 15,75
Mvav 144,144,220
X 178
Mvav 148,148,220
K 194,176,177
A 8,8,12
Mvav 116,148,212
X 179
Mvav 84,148,204
X 180
Mvav 52,148,196
Dc 245,8,61,0
Ra 144,242,12288
Mvav 56,152,196
K 195,178,179
A 9,9,13
Mvav 88,152,204
X 181
Mvav 120,152,212
X 182
Mvav 152,152,220
Dc 245,8,61,1024
Ra 148,242,13312
Mvav 156,156,220
K 202,180,181
A 10,10,14
Mvav 124,156,212
X 183
Mvav 92,156,204
X 184
Mvav 60,156,196
W 2
Dc 245,8,61,2048
Ra 152,242,14336
Cvaa 160,128,0,224
A 11,11,15
K 203,182,183
Mvaa 160,132,4
X 185
Cvaa 164,132,20,228
X 186
Mvaa 164,128,16
Dc 245,8,61,3072
Ra 156,242,15360
Cvaa 168,128,32,232
s_add_u32 s61, s61, s63
G 12,76
K 210,184,185
Mvaa 168,132,36
X 187
Cvaa 172,132,52,236
s_xor_b32 s62, s62, 0x10000
G 13,77
Mvaa 172,128,48
X 188
Mvaa 160,136,8
K 211,186,187
G 14,78
Mvaa 160,140,12
s_add_u32 s64, s64, 1
X 189
Mvaa 164,140,28
Ra 128,242,24576
Ra 132,242,25600
Mvaa 164,136,24
X 190
Mvaa 168,136,40
K 218,188,189
G 15,79
Mvaa 168,140,44
X 191
Mvaa 172,140,60
s_lshl_b32 s92, s58, 2
X 160
Mvaa 172,136,56
W 2
K 219,190,191
A 8,8,12
Cvaa 176,144,0,224
X 161
Mvaa 176,148,4
Ra 136,242,26624
Ra 140,242,27648
Cvaa 180,148,20,228
s_add_u32 s91, s92, s51
X 162
Mvaa 180,144,16
A 9,9,13
K 196,160,161
Cvaa 184,144,32,232
X 163
Mvaa 184,148,36
s_add_u32 s95, s92, 0
X 164
Cvaa 188,148,52,236
A 10,10,14
K 197,162,163
Mvaa 188,144,48
X 165
Mvaa 176,152,8
s_and_b32 s95, s95, 15
Ra 144,242,28672
A 11,11,15
Mvaa 176,156,12
Ra 148,242,29696
Mvaa 180,156,28
X 166
Mvaa 180,152,24
s_mul_i32 s95, s95, 40960
K 204,164,165
Mvaa 184,152,40
X 167
Mvaa 184,156,44
G 12,80
Mvaa 188,156,60
s_mul_i32 s71, s51, 0
X 168
Mvaa 188,152,56
W 2
K 205,166,167
G 13,81
Mvav 32,128,192
X 169
Mvav 64,128,200
s_add_u32 s71, s71, 133120
Ra 152,242,30720
Ra 156,242,31744
Mvav 96,128,208
X 170
Mvav 128,128,216
G 14,82
K 212,168,169
Mvav 132,132,216
s_add_u32 m0, s71, s48
X 171
Mvav 100,132,208
X 172
Mvav 68,132,200
G 15,83
Mvav 36,132,192
s_lshl2_add_u32 s94, s48, s95
K 213,170,171
Mvav 40,136,192
X 173
Mvav 72,136,200
X 174
Mvav 104,136,208
D 16,40,94,0
A 8,8,12
Mvav 136,136,216
K 220,172,173
Mvav 140,140,216
X 175
Mvav 108,140,208
A 9,9,13
Mvav 76,140,200
s_waitcnt vmcnt(1) lgkmcnt(0)
s_barrier
v_xor_b32 v242, 0x10000, v242
Mvav 44,140,192
D 17,40,94,1024
Ra 128,242,0
Mvav 48,144,192
X 176
Mvav 80,144,200
K 221,174,175
A 10,10,14
Mvav 112,144,208
X 177
Mvav 144,144,216
D 18,40,94,2048
Ra 132,242,1024
Mvav 148,148,216
X 178
Mvav 116,148,208
K 198,176,177
A 11,11,15
Mvav 84,148,200
X 179
Mvav 52,148,192
D 19,40,94,3072
Ra 136,242,2048
Mvav 56,152,192
s_add_u32 m0, m0, 4096
X 180
Mvav 88,152,200
G 12,84
K 199,178,179
Mvav 120,152,208
X 181
Mvav 152,152,216
Ra 140,242,3072
buffer_load_dwordx4 v20, s[40:43], s94 offen lds
Mvav 156,156,216
X 182
Mvav 124,156,208
K 206,180,181
Mvav 92,156,200
X 183
Mvav 60,156,192
K 207,182,183
W 0
Cvaa 160,128,0,224
Ra 144,242,4096
Ra 148,242,5120
Mvaa 160,132,4
s_add_u32 m0, s62, 0
Ra 152,242,6144
G 13,85
Cvaa 164,132,20,228
X 184
Mvaa 164,128,16
X 185
Cvaa 168,128,32,232
X 186
Mvaa 168,132,36
G 14,86
K 214,184,185
Cvaa 172,132,52,236
Ra 156,242,7168
Dc 1,8,61,0
Mvaa 172,128,48
X 187
Mvaa 160,136,8
X 188
Mvaa 160,140,12
Ra 128,242,16384
G 15,87
Mvaa 164,140,28
Dc 1,8,61,1024
K 215,186,187
Mvaa 164,136,24
X 189
Mvaa 168,136,40
Ra 132,242,17408
A 8,8,12
Mvaa 168,140,44
X 190
Mvaa 172,140,60
)"
R"(Dc 1,8,61,2048
K 222,188,189
Mvaa 172,136,56
X 191
v_smfmac_f32_16x16x64_bf16 a[160:163], a[176:179], v[192:199], v240
A 9,9,13
K 223,190,191
v_smfmac_f32_16x16x64_bf16 a[164:167], a[176:179], v[200:207], v240
Ra 136,242,18432
A 10,10,14
v_smfmac_f32_16x16x64_bf16 a[168:171], a[176:179], v[208:215], v240
Dc 1,8,61,3072
v_smfmac_f32_16x16x64_bf16 a[172:175], a[176:179], v[216:223], v240
W 3
X 160
Cvaa 176,144,0,224
s_add_u32 m0, s62, 4096
X 161
Mvaa 176,148,4
X 162
Cvaa 180,148,20,228
Ra 140,242,19456
A 11,11,15
Mvaa 180,144,16
Dc 243,8,61,0
K 192,160,161
Cvaa 184,144,32,232
X 163
Mvaa 184,148,36
X 164
Cvaa 188,148,52,236
G 12,88
K 193,162,163
Mvaa 188,144,48
Dc 243,8,61,1024
G 13,89
Mvaa 176,152,8
X 165
Mvaa 176,156,12
X 166
Mvaa 180,156,28
Ra 144,242,20480
Ra 148,242,21504
Mvaa 180,152,24
Dc 243,8,61,2048
K 200,164,165
Mvaa 184,152,40
X 167
Mvaa 184,156,44
G 14,90
Mvaa 188,156,60
X 168
Mvaa 188,152,56
W 2
Dc 243,8,61,3072
K 201,166,167
Mvav 32,128,196
s_add_u32 m0, s62, 8192
Ra 152,242,22528
G 15,91
Mvav 64,128,204
X 169
Mvav 96,128,212
X 170
Mvav 128,128,220
Ra 156,242,23552
A 8,8,12
Mvav 132,132,220
Dc 244,8,61,0
K 208,168,169
Mvav 100,132,212
X 171
Mvav 68,132,204
X 172
Mvav 36,132,196
Ra 128,242,8192
A 9,9,13
Mvav 40,136,196
Dc 244,8,61,1024
K 209,170,171
Mvav 72,136,204
X 173
Mvav 104,136,212
Ra 132,242,9216
A 10,10,14
Mvav 136,136,220
X 174
Mvav 140,140,220
Dc 244,8,61,2048
K 216,172,173
Mvav 108,140,212
X 175
Mvav 76,140,204
Ra 136,242,10240
A 11,11,15
Mvav 44,140,196
W 3
X 176
Mvav 48,144,196
Dc 244,8,61,3072
K 217,174,175
Mvav 80,144,204
s_add_u32 m0, s62, 12288
Ra 140,242,11264
v_add_f32_dpp v8, v8, v8 row_ror:8 row_mask:0xf bank_mask:0xf
Mvav 112,144,212
X 177
Mvav 144,144,220
X 178
Mvav 148,148,220
K 194,176,177
v_add_f32_dpp v9, v9, v9 row_ror:8 row_mask:0xf bank_mask:0xf
Mvav 116,148,212
X 179
Mvav 84,148,204
X 180
Mvav 52,148,196
Dc 245,8,61,0
Ra 144,242,12288
Mvav 56,152,196
v_add_f32_dpp v10, v10, v10 row_ror:8 row_mask:0xf bank_mask:0xf
K 195,178,179
Mvav 88,152,204
X 181
Mvav 120,152,212
X 182
Mvav 152,152,220
Dc 245,8,61,1024
v_add_f32_dpp v11, v11, v11 row_ror:8 row_mask:0xf bank_mask:0xf
Mvav 156,156,220
Ra 148,242,13312
K 202,180,181
Mvav 124,156,212
X 183
Mvav 92,156,204
Ra 152,242,14336
v_mov_b32 v12, v8
Mvav 60,156,196
W 3
Dc 245,8,61,2048
v_mov_b32 v13, v9
Cvaa 160,128,0,224
X 184
Mvaa 160,132,4
Ra 156,242,15360
K 203,182,183
Cvaa 164,132,20,228
X 185
Mvaa 164,128,16
Dc 245,8,61,3072
v_mov_b32 v14, v10
Cvaa 168,128,32,232
s_add_u32 s61, s61, s63
X 186
Mvaa 168,132,36
K 210,184,185
v_mov_b32 v15, v11
Cvaa 172,132,52,236
s_xor_b32 s62, s62, 0x10000
X 187
Mvaa 172,128,48
X 188
Mvaa 160,136,8
Ra 128,242,24576
K 211,186,187
Mvaa 160,140,12
s_add_u32 s64, s64, 1
X 189
Mvaa 164,140,28
X 190
Mvaa 164,136,24
Ra 132,242,25600
v_permlane16_swap_b32 v8, v12
Mvaa 168,136,40
K 218,188,189
v_permlane16_swap_b32 v9, v13
Mvaa 168,140,44
X 191
Mvaa 172,140,60
s_lshl_b32 s92, s58, 2
X 160
Mvaa 172,136,56
W 2
K 219,190,191
v_permlane16_swap_b32 v10, v14
Cvaa 176,144,0,224
X 161
Mvaa 176,148,4
Ra 136,242,26624
Ra 140,242,27648
Cvaa 180,148,20,228
s_add_u32 s91, s92, s51
X 162
Mvaa 180,144,16
K 196,160,161
v_permlane16_swap_b32 v11, v15
Cvaa 184,144,32,232
X 163
Mvaa 184,148,36
s_add_u32 s95, s92, 1
X 164
Cvaa 188,148,52,236
A 8,8,12
K 197,162,163
Mvaa 188,144,48
X 165
Mvaa 176,152,8
s_and_b32 s95, s95, 15
Ra 144,242,28672
Ra 148,242,29696
Mvaa 176,156,12
X 166
Mvaa 180,156,28
A 9,9,13
K 204,164,165
Mvaa 180,152,24
s_mul_i32 s95, s95, 40960
X 167
Mvaa 184,152,40
Mvaa 184,156,44
A 10,10,14
Mvaa 188,156,60
s_mul_i32 s71, s51, 0
X 168
Mvaa 188,152,56
W 2
Ra 152,242,30720
K 205,166,167
Mvav 32,128,192
X 169
Mvav 64,128,200
s_add_u32 s71, s71, 143360
Ra 156,242,31744
A 11,11,15
Mvav 96,128,208
X 170
Mvav 128,128,216
K 212,168,169
v_mov_b32 v12, v8
Mvav 132,132,216
s_add_u32 m0, s71, s48
X 171
Mvav 100,132,208
X 172
Mvav 68,132,200
Mvav 36,132,192
s_lshl2_add_u32 s94, s48, s95
v_mov_b32 v13, v9
K 213,170,171
Mvav 40,136,192
X 173
Mvav 72,136,200
X 174
Mvav 104,136,208
D 16,40,94,0
v_mov_b32 v14, v10
Mvav 136,136,216
K 220,172,173
Mvav 140,140,216
X 175
Mvav 108,140,208
v_mov_b32 v15, v11
Mvav 76,140,200
s_waitcnt vmcnt(1) lgkmcnt(0)
s_barrier
v_xor_b32 v242, 0x10000, v242
Mvav 44,140,192
D 17,40,94,1024
Ra 128,242,0
Mvav 48,144,192
X 176
Mvav 80,144,200
K 221,174,175
v_permlane32_swap_b32 v8, v12
Mvav 112,144,208
X 177
Mvav 144,144,216
D 18,40,94,2048
Ra 132,242,1024
Mvav 148,148,216
X 178
Mvav 116,148,208
K 198,176,177
v_permlane32_swap_b32 v9, v13
Mvav 84,148,200
X 179
Mvav 52,148,192
D 19,40,94,3072
Ra 136,242,2048
Mvav 56,152,192
s_add_u32 m0, m0, 4096
X 180
Mvav 88,152,200
K 199,178,179
v_permlane32_swap_b32 v10, v14
Mvav 120,152,208
X 181
Mvav 152,152,216
Ra 140,242,3072
buffer_load_dwordx4 v20, s[40:43], s94 offen lds
Mvav 156,156,216
X 182
Mvav 124,156,208
K 206,180,181
Mvav 92,156,200
X 183
Mvav 60,156,192
K 207,182,183
W 0
Cvaa 160,128,0,224
Ra 144,242,4096
Ra 148,242,5120
Mvaa 160,132,4
s_add_u32 m0, s62, 0
Ra 152,242,6144
v_permlane32_swap_b32 v11, v15
Cvaa 164,132,20,228
X 184
Mvaa 164,128,16
X 185
Cvaa 168,128,32,232
X 186
Mvaa 168,132,36
A 8,8,12
K 214,184,185
Cvaa 172,132,52,236
Ra 156,242,7168
Dc 1,8,61,0
Mvaa 172,128,48
X 187
Mvaa 160,136,8
Ra 128,242,16384
A 9,9,13
Mvaa 160,140,12
X 188
Mvaa 164,140,28
Dc 1,8,61,1024
K 215,186,187
Mvaa 164,136,24
X 189
Mvaa 168,136,40
Ra 132,242,17408
A 10,10,14
Mvaa 168,140,44
X 190
Mvaa 172,140,60
Dc 1,8,61,2048
K 222,188,189
Mvaa 172,136,56
A 11,11,15
v_smfmac_f32_16x16x64_bf16 a[160:163], a[176:179], v[192:199], v240
X 191
v_smfmac_f32_16x16x64_bf16 a[164:167], a[176:179], v[200:207], v240
K 223,190,191
v_and_b32 v7, 0xffff0000, v7
v_smfmac_f32_16x16x64_bf16 a[168:171], a[176:179], v[208:215], v240
Dc 1,8,61,3072
Ra 136,242,18432
v_smfmac_f32_16x16x64_bf16 a[172:175], a[176:179], v[216:223], v240
W 3
X 160
Cvaa 176,144,0,224
s_add_u32 m0, s62, 4096
X 161
Mvaa 176,148,4
X 162
Cvaa 180,148,20,228
Ra 140,242,19456
v_ashrrev_i32 v6, 31, v7
Mvaa 180,144,16
Dc 243,8,61,0
K 192,160,161
Cvaa 184,144,32,232
X 163
Mvaa 184,148,36
v_not_b32 v6, v6
Cvaa 188,148,52,236
X 164
Mvaa 188,144,48
Dc 243,8,61,1024
K 193,162,163
Mvaa 176,152,8
X 165
Mvaa 176,156,12
Ra 144,242,20480
v_or_b32 v6, 0x80000000, v6
Mvaa 180,156,28
X 166
Mvaa 180,152,24
Ra 148,242,21504
v_xor_b32 v7, v7, v6
Mvaa 184,152,40
Dc 243,8,61,2048
K 200,164,165
Mvaa 184,156,44
X 167
Mvaa 188,156,60
X 168
Mvaa 188,152,56
W 2
)"
R"(v_and_b32 v7, 0xffff0000, v7
K 201,166,167
Mvav 32,128,196
Dc 243,8,61,3072
v_mul_f32 v7, 0xbfb8aa3b, v7
Mvav 64,128,204
s_add_u32 m0, s62, 8192
X 169
Mvav 96,128,212
Ra 152,242,22528
Ra 156,242,23552
Mvav 128,128,220
X 170
Mvav 132,132,220
Dc 244,8,61,0
K 208,168,169
Mvav 100,132,212
X 171
Mvav 68,132,204
X 7
Mvav 36,132,196
X 172
Mvav 40,136,196
Ra 128,242,8192
K 209,170,171
Mvav 72,136,204
X 173
Mvav 104,136,212
Dc 244,8,61,1024
Ra 132,242,9216
Mvav 136,136,220
X 174
Mvav 140,140,220
v_add_f32 v7, 1.0, v7
K 216,172,173
Mvav 108,140,212
X 175
Mvav 76,140,204
Dc 244,8,61,2048
Ra 136,242,10240
Mvav 44,140,196
W 3
v_rcp_f32 v7, v7
Mvav 48,144,196
X 176
Mvav 80,144,204
Ra 140,242,11264
K 217,174,175
Mvav 112,144,212
Dc 244,8,61,3072
K 7,7,7
Mvav 144,144,220
s_add_u32 m0, s62, 12288
X 177
Mvav 148,148,220
X 178
Mvav 116,148,212
Ra 144,242,12288
K 194,176,177
Mvav 84,148,204
X 179
Mvav 52,148,196
X 180
Mvav 56,152,196
Dc 245,8,61,0
v_lshlrev_b32 v7, 16, v7
Mvav 88,152,204
K 195,178,179
K 12,8,9
Mvav 120,152,212
X 181
Mvav 152,152,220
X 182
Mvav 156,156,220
Dc 245,8,61,1024
Ra 148,242,13312
Mvav 124,156,212
K 202,180,181
K 13,10,11
Mvav 92,156,204
X 183
Mvav 60,156,196
W 2
X 184
Cvaa 160,128,0,224
Dc 245,8,61,2048
Ra 152,242,14336
Mvaa 160,132,4
K 203,182,183
v_lshlrev_b32 v8, 16, v12
Cvaa 164,132,20,228
X 185
Mvaa 164,128,16
X 186
Cvaa 168,128,32,232
Dc 245,8,61,3072
Ra 156,242,15360
Mvaa 168,132,36
s_add_u32 s61, s61, s63
v_and_b32 v9, 0xffff0000, v12
K 210,184,185
Cvaa 172,132,52,236
s_xor_b32 s62, s62, 0x10000
X 187
Mvaa 172,128,48
X 188
Mvaa 160,136,8
v_lshlrev_b32 v10, 16, v13
K 211,186,187
Mvaa 160,140,12
s_add_u32 s64, s64, 1
X 189
Mvaa 164,140,28
Ra 128,242,24576
v_and_b32 v11, 0xffff0000, v13
Mvaa 164,136,24
X 190
Mvaa 168,136,40
Ra 132,242,25600
K 218,188,189
Mvaa 168,140,44
X 191
Mvaa 172,140,60
s_lshl_b32 s92, s58, 2
U 8,8,7
U 9,9,7
Mvaa 172,136,56
W 2
X 160
Cvaa 176,144,0,224
Ra 136,242,26624
K 219,190,191
Mvaa 176,148,4
X 161
Cvaa 180,148,20,228
s_add_u32 s91, s92, s51
Ra 140,242,27648
U 10,10,7
Mvaa 180,144,16
X 162
Cvaa 184,144,32,232
K 196,160,161
Mvaa 184,148,36
s_add_u32 s95, s92, 2
X 163
Cvaa 188,148,52,236
U 11,11,7
Mvaa 188,144,48
X 164
Mvaa 176,152,8
s_and_b32 s95, s95, 15
Ra 144,242,28672
K 197,162,163
Mvaa 176,156,12
X 165
Mvaa 180,156,28
Ra 148,242,29696
K 12,8,9
Mvaa 180,152,24
s_mul_i32 s95, s95, 40960
X 166
Mvaa 184,152,40
K 204,164,165
K 14,10,11
Mvaa 184,156,44
X 167
Mvaa 188,156,60
s_mul_i32 s71, s51, 0
X 168
Mvaa 188,152,56
W 2
v_mov_b32 v13, s13
K 205,166,167
Mvav 32,128,192
X 169
Mvav 64,128,200
s_add_u32 s71, s71, 153600
Ra 152,242,30720
Ra 156,242,31744
Mvav 96,128,208
X 170
Mvav 128,128,216
v_mov_b32 v15, s13
K 212,168,169
Mvav 132,132,216
s_add_u32 m0, s71, s48
X 171
Mvav 100,132,208
v_lshrrev_b32 v6, 7, v1
Mvav 68,132,200
X 172
Mvav 36,132,192
s_lshl2_add_u32 s94, s48, s95
K 213,170,171
Mvav 40,136,192
X 173
Mvav 72,136,200
v_lshl_or_b32 v6, v6, 28, v1
Mvav 104,136,208
X 174
Mvav 136,136,216
s_lshl_b32 s19, s85, 7
D 16,40,94,0
K 220,172,173
Mvav 140,140,216
X 175
Mvav 108,140,208
X 176
Mvav 76,140,200
s_waitcnt vmcnt(1) lgkmcnt(0)
s_barrier
v_xor_b32 v242, 0x10000, v242
Mvav 44,140,192
s_add_u32 s19, s19, 98304
D 17,40,94,1024
K 221,174,175
Mvav 48,144,192
Ra 128,242,0
Mvav 80,144,200
s_and_b32 s21, s20, 134217728
X 177
Mvav 112,144,208
X 178
Mvav 144,144,216
D 18,40,94,2048
Ra 132,242,1024
Mvav 148,148,216
s_or_b32 s19, s19, s21
K 198,176,177
Mvav 116,148,208
X 179
Mvav 84,148,200
X 180
Mvav 52,148,192
D 19,40,94,3072
Ra 136,242,2048
Mvav 56,152,192
s_add_u32 m0, m0, 4096
Ra 140,242,3072
K 199,178,179
Mvav 88,152,200
X 181
Mvav 120,152,208
X 182
Mvav 152,152,216
buffer_store_dwordx4 v[12:15], v6, s[8:11], s19 offen sc1
K 206,180,181
Mvav 156,156,216
X 183
Mvav 124,156,208
Mvav 92,156,200
K 207,182,183
Mvav 60,156,192
buffer_load_dwordx4 v20, s[40:43], s94 offen lds
.Lf1_40%=:
W 0
Cvaa 160,128,0,224
v_bfe_u32 v28, v1, 4, 2
v_lshlrev_b32 v28, 3, v28
Mvaa 160,132,4
Ra 144,242,4096
Ra 148,242,5120
Cvaa 164,132,20,228
Ra 152,242,6144
buffer_load_dwordx2 v[28:29], v28, s[8:11], s54 offen sc1
Mvaa 164,128,16
s_lshl_b32 s92, s58, 2
s_add_u32 s91, s92, s51
s_add_u32 s95, s91, 3
Cvaa 168,128,32,232
s_and_b32 s95, s95, 15
s_mul_i32 s95, s95, 40960
s_mul_i32 s71, s51, 10240
Mvaa 168,132,36
s_add_u32 s71, s71, 0
s_add_u32 m0, s71, s48
s_lshl2_add_u32 s94, s48, s95
Cvaa 172,132,52,236
X 184
Mvaa 172,128,48
X 185
Mvaa 160,136,8
X 186
Mvaa 160,140,12
Ra 156,242,7168
K 214,184,185
Mvaa 164,140,28
X 187
Mvaa 164,136,24
D 16,40,94,0
Ra 128,242,16384
Mvaa 168,136,40
s_add_u32 s95, s91, 5
s_and_b32 s95, s95, 15
s_mul_i32 s95, s95, 40960
Mvaa 168,140,44
s_mul_i32 s71, s51, 10240
s_add_u32 s71, s71, 20480
Ra 132,242,17408
Mvaa 172,140,60
X 188
Mvaa 172,136,56
D 17,40,94,1024
K 215,186,187
v_smfmac_f32_16x16x64_bf16 a[160:163], a[176:179], v[192:199], v240
X 189
v_smfmac_f32_16x16x64_bf16 a[164:167], a[176:179], v[200:207], v240
X 190
K 222,188,189
X 191
s_nop 0
K 223,190,191
v_smfmac_f32_16x16x64_bf16 a[168:171], a[176:179], v[208:215], v240
D 18,40,94,2048
Ra 136,242,18432
v_smfmac_f32_16x16x64_bf16 a[172:175], a[176:179], v[216:223], v240
W 3
X 160
Cvaa 176,144,0,224
X 161
Mvaa 176,148,4
X 162
Cvaa 180,148,20,228
D 19,40,94,3072
K 192,160,161
Mvaa 180,144,16
s_add_u32 m0, m0, 4096
X 163
Cvaa 184,144,32,232
X 164
Mvaa 184,148,36
Ra 140,242,19456
K 193,162,163
Cvaa 188,148,52,236
buffer_load_dwordx4 v20, s[40:43], s94 offen lds
Mvaa 188,144,48
s_add_u32 m0, s71, s48
s_lshl2_add_u32 s94, s48, s95
Ra 144,242,20480
Mvaa 176,152,8
X 165
Mvaa 176,156,12
X 166
Mvaa 180,156,28
D 16,40,94,0
K 200,164,165
Mvaa 180,152,24
X 167
Mvaa 184,152,40
X 172
Mvaa 184,156,44
D 17,40,94,1024
K 201,166,167
Mvaa 188,156,60
X 173
Mvaa 188,152,56
s_cmp_eq_u32 s60, 16
s_cbranch_scc1 .Lmk_79%=
.Lmkr_80%=:
W 1
Mvav 32,128,196
Ra 148,242,21504
D 18,40,94,2048
Mvav 64,128,204
X 174
Mvav 96,128,212
Ra 152,242,22528
K 216,172,173
Mvav 128,128,220
X 175
Mvav 132,132,220
s_mov_b32 s18, 40960
X 168
Mvav 100,132,212
D 19,40,94,3072
K 217,174,175
Mvav 68,132,204
s_add_u32 m0, m0, 4096
X 169
Mvav 36,132,196
s_mov_b32 s19, 73728
X 170
Mvav 40,136,196
s_bitcmp1_b32 s14, 0
s_cselect_b32 s18, 49152, s18
s_cselect_b32 s19, 98304, s19
Ra 156,242,23552
K 208,168,169
Mvav 72,136,204
s_cmp_lt_u32 s14, 2
s_cselect_b32 s20, s18, s19
s_cselect_b32 s21, s19, s18
X 171
Mvav 104,136,212
X 184
Mvav 136,136,220
buffer_load_dwordx4 v20, s[40:43], s94 offen lds
K 209,170,171
Mvav 140,140,220
s_waitcnt vmcnt(10)
)"
R"(v_cmp_eq_u64 vcc, s[12:13], v[28:29]
s_cmp_eq_u64 vcc, -1
s_cselect_b32 s46, 79691776, 0
s_add_u32 s81, s58, 0
s_and_b32 s81, s81, 3
Mvav 108,140,212
s_lshl_b32 s81, s81, 12
s_add_u32 s81, s81, s49
Ra 128,242,24576
Mvav 76,140,204
X 185
Mvav 44,140,196
W 1
X 186
Mvav 48,144,196
buffer_load_dwordx4 a[184:187], v1, s[44:47], s81 offen offset:0 sc1
K 210,184,185
Mvav 80,144,204
s_add_u32 s82, s81, 131072
X 187
Mvav 112,144,212
v_add_u32 v2, s20, v1
v_add_u32 v3, s21, v1
Mvav 144,144,220
s_lshl_b32 s18, s14, 9
X 188
Mvav 148,148,220
Ra 132,242,25600
K 211,186,187
Mvav 116,148,212
buffer_load_dwordx4 a[200:203], v1, s[44:47], s82 offen offset:0 sc1
Ra 136,242,26624
Mvav 84,148,204
X 189
Mvav 52,148,196
v_lshrrev_b32 v232, 1, v1
v_add_u32 v232, s18, v232
Mvav 56,152,196
X 190
Mvav 88,152,204
K 218,188,189
v_bfe_u32 v12, v1, 6, 2
Mvav 120,152,212
buffer_load_dwordx4 a[188:191], v1, s[44:47], s81 offen offset:1024 sc1
Ra 140,242,27648
Mvav 152,152,220
X 191
Mvav 156,156,220
v_lshrrev_b32 v13, 8, v1
v_bfe_u32 v14, v1, 4, 1
Mvav 124,156,212
X 176
Mvav 92,156,204
K 219,190,191
v_lshlrev_b32 v14, 4, v14
Mvav 60,156,196
X 177
Maav 168,180,208
X 178
K 194,176,177
X 179
X 180
K 195,178,179
Maav 172,180,216
X 181
X 182
K 202,180,181
X 183
s_nop 0
K 203,182,183
Maav 160,180,192
buffer_load_dwordx4 a[204:207], v1, s[44:47], s82 offen offset:1024 sc1
v_mov_b32 v15, 0x3f80
Maav 164,180,200
W 0
Ra 144,242,28672
v_lshlrev_b32 v15, v14, v15
Mvav 96,128,208
Ra 148,242,29696
v_bfe_u32 v14, v1, 5, 1
v_xor_b32 v27, v12, v13
Mvav 128,128,216
buffer_load_dwordx4 a[192:195], v1, s[44:47], s81 offen offset:2048 sc1
v_lshl_or_b32 v27, v27, 1, v14
v_cmp_eq_u32_e64 s[86:87], 0, v27
Mvav 132,132,216
Ra 152,242,30720
Ra 156,242,31744
v_mov_b32 v226, 0
Mvav 100,132,208
v_cndmask_b32_e64 v224, 0, v15, s[86:87]
v_mov_b32 v228, 0
v_mov_b32 v230, v224
Mvav 104,136,208
buffer_load_dwordx4 a[208:211], v1, s[44:47], s82 offen offset:2048 sc1
v_cmp_eq_u32_e64 s[86:87], 1, v27
v_mov_b32 v227, 0
Mvav 136,136,216
v_cndmask_b32_e64 v225, 0, v15, s[86:87]
v_mov_b32 v229, 0
v_mov_b32 v231, v225
Mvav 140,140,216
G 4,160
G 8,161
G 5,164
Mvav 108,140,208
W 0
buffer_load_dwordx4 a[196:199], v1, s[44:47], s81 offen offset:3072 sc1
G 9,165
G 6,168
Mvav 112,144,208
G 10,169
G 7,172
G 11,173
Mvav 144,144,216
A 4,4,8
A 5,5,9
A 6,6,10
Mvav 148,148,216
buffer_load_dwordx4 a[212:215], v1, s[44:47], s82 offen offset:3072 sc1
s_add_u32 s81, s58, 1
s_and_b32 s81, s81, 3
s_lshl_b32 s81, s81, 12
Mvav 116,148,208
s_add_u32 s81, s81, s49
A 7,7,11
ds_write_b64 v232, v[6:7] offset:57344
A 8,4,5
Mvav 120,152,208
A 9,6,7
A 8,8,9
v_cmp_ngt_f32_e64 s[86:87], s90, v8
s_lshl_b32 s19, s14, 2
Mvav 152,152,216
buffer_load_dwordx4 a[216:219], v1, s[44:47], s81 offen offset:0 sc1
s_add_u32 s82, s81, 131072
v_mov_b32 v9, s19
s_or_b64 s[88:89], s[88:89], s[86:87]
Mvav 156,156,216
s_and_b64 s[88:89], s[88:89], s[24:25]
s_cselect_b32 s18, 1, 0
v_mov_b32 v8, s18
ds_write_b32 v9, v8 offset:59392
v_xor_b32 v10, 1024, v232
Mvav 124,156,208
v_bfe_u32 v11, v1, 2, 4
K 96,96,97
K 97,98,99
Mvav 32,128,192
buffer_load_dwordx4 a[232:235], v1, s[44:47], s82 offen offset:0 sc1
K 98,100,101
K 99,102,103
Mvav 64,128,200
ds_write_b128 v2, v[96:99] offset:0
K 128,128,129
K 129,130,131
Mvav 68,132,200
K 130,132,133
K 131,134,135
ds_write_b128 v2, v[128:131] offset:4096
Mvav 36,132,192
buffer_load_dwordx4 a[220:223], v1, s[44:47], s81 offen offset:1024 sc1
K 104,104,105
K 105,106,107
Mvav 40,136,192
K 106,108,109
K 107,110,111
ds_write_b128 v2, v[104:107] offset:1024
Mvav 72,136,200
K 136,136,137
K 137,138,139
K 138,140,141
Mvav 76,140,200
buffer_load_dwordx4 a[236:239], v1, s[44:47], s82 offen offset:1024 sc1
K 139,142,143
ds_write_b128 v2, v[136:139] offset:5120
Mvav 44,140,192
K 112,112,113
K 113,114,115
K 114,116,117
Mvav 48,144,192
K 115,118,119
ds_write_b128 v2, v[112:115] offset:2048
K 144,144,145
Mvav 80,144,200
K 145,146,147
K 146,148,149
K 147,150,151
Mvav 84,148,200
ds_write_b128 v2, v[144:147] offset:6144
buffer_load_dwordx4 a[224:227], v1, s[44:47], s81 offen offset:2048 sc1
K 120,120,121
Mvav 52,148,192
K 121,122,123
K 122,124,125
K 123,126,127
Mvav 56,152,192
ds_write_b128 v2, v[120:123] offset:3072
K 152,152,153
K 153,154,155
Mvav 88,152,200
buffer_load_dwordx4 a[240:243], v1, s[44:47], s82 offen offset:2048 sc1
K 154,156,157
K 155,158,159
Mvav 92,156,200
ds_write_b128 v2, v[152:155] offset:7168
Mvav 60,156,192
buffer_load_dwordx4 a[228:231], v1, s[44:47], s81 offen offset:3072 sc1
buffer_load_dwordx4 a[244:247], v1, s[44:47], s82 offen offset:3072 sc1
s_branch .LL_81%=
.p2align 12
.LL_81%=:
.Lend_43%=:
s_waitcnt vmcnt(8) lgkmcnt(0)
s_barrier
s_cmp_eq_u32 s46, 0
s_cbranch_scc1 .Llate_82%=
ds_read_b32 v11, v11 offset:59392
ds_read_b64 v[8:9], v10 offset:57344
Rv 176,3,0
Rv 180,3,1024
Rv 184,3,2048
Rv 188,3,3072
Rv 192,3,4096
Rv 196,3,5120
Rv 200,3,6144
Rv 204,3,7168
W 6
Mvvv 32,224,176
s_lshl_b32 s92, s58, 2
s_add_u32 s91, s92, s51
s_add_u32 s95, s91, 7
Mvvv 36,228,176
s_and_b32 s95, s95, 15
s_mul_i32 s95, s95, 40960
s_mul_i32 s71, s51, 20480
Mvvv 40,224,180
s_add_u32 s71, s71, 61440
s_add_u32 m0, s71, s48
s_lshl2_add_u32 s94, s48, s95
Mvvv 44,228,180
W 4
v_cmp_ne_u32 vcc, 0, v11
s_cmp_lg_u64 vcc, 0
s_cbranch_scc1 .Lredo%=
A 4,4,8
Mvvv 48,224,184
v_rcp_f32 v4, v4
Mvvv 52,228,184
Ta 128,25,10240
A 5,5,9
Mvvv 56,224,188
Ta 130,25,15360
D 16,40,94,0
Mvvv 60,228,188
W 4
s_add_u32 s95, s91, 9
s_and_b32 s95, s95, 15
Mvvv 64,224,192
s_mul_i32 s95, s95, 40960
s_mul_i32 s71, s51, 10240
s_add_u32 s71, s71, 112640
Mvvv 68,228,192
v_rcp_f32 v5, v5
Mvvv 72,224,196
s_lshl_b32 s93, s58, 12
s_add_u32 s93, s93, s50
Ta 132,25,20480
Mvvv 76,228,196
Ta 134,25,25600
W 4
Mvvv 80,224,200
U 32,32,4
U 33,33,4
Mvvv 84,228,200
Ta 136,26,10272
U 34,34,4
Mvvv 88,224,204
Ta 138,26,15392
U 35,35,4
Mvvv 92,228,204
Ta 140,26,20512
Ta 142,26,25632
W 4
Zvaa 96,128,184
U 36,36,4
U 37,37,4
Mvaa 96,132,188
D 17,40,94,1024
U 38,38,4
Zvaa 104,132,204
U 39,39,4
K 32,32,33
Mvaa 104,128,200
Ta 144,25,10304
Ta 146,25,15424
Ta 148,25,20544
Ta 150,25,25664
W 4
Zvaa 108,136,200
K 33,34,35
K 34,36,37
Mvaa 108,140,204
Ta 152,26,10336
K 35,38,39
Zvaa 100,140,188
buffer_store_dwordx4 v[32:35], v1, s[8:11], s93 offen offset:0 sc1
U 40,40,4
Mvaa 100,136,184
Ta 154,26,15456
Ta 156,26,20576
Ta 158,26,25696
W 4
Zvaa 112,144,184
U 41,41,4
U 42,42,4
Mvaa 112,148,188
U 43,43,4
U 44,44,4
Zvaa 120,148,204
D 18,40,94,2048
U 45,45,4
Mvaa 120,144,200
Ta 128,25,10368
Ta 130,25,15488
Ta 132,25,20608
Ta 134,25,25728
W 4
Zvaa 124,152,200
U 46,46,4
U 47,47,4
Mvaa 124,156,204
K 40,40,41
K 41,42,43
Zvaa 116,156,188
K 42,44,45
K 43,46,47
Mvaa 116,152,184
Ta 136,26,10400
Ta 138,26,15520
Ta 140,26,20640
Ta 142,26,25760
W 4
Zvaa 128,128,184
buffer_store_dwordx4 v[40:43], v1, s[8:11], s93 offen offset:1024 sc1
U 48,48,4
Mvaa 128,132,188
U 49,49,4
U 50,50,4
Zvaa 136,132,204
U 51,51,4
U 52,52,4
Mvaa 136,128,200
Ta 144,25,10432
Ta 146,25,15552
Ta 148,25,20672
Ta 150,25,25792
W 4
Zvaa 140,136,200
D 19,40,94,3072
U 53,53,4
Mvaa 140,140,204
s_add_u32 m0, m0, 4096
U 54,54,4
U 55,55,4
Zvaa 132,140,188
K 48,48,49
K 49,50,51
Mvaa 132,136,184
Ta 152,26,10464
Ta 154,26,15584
Ta 156,26,20704
Ta 158,26,25824
W 4
Zvaa 144,144,184
K 50,52,53
K 51,54,55
Mvaa 144,148,188
buffer_load_dwordx4 v20, s[40:43], s94 offen lds
Ta 128,25,10496
Zvaa 152,148,204
s_add_u32 m0, s71, s48
s_lshl2_add_u32 s94, s48, s95
Ta 130,25,15616
Mvaa 152,144,200
Ta 132,25,20736
Ta 134,25,25856
W 4
Zvaa 156,152,200
Ta 136,26,10528
Ta 138,26,15648
Mvaa 156,156,204
buffer_store_dwordx4 v[48:51], v1, s[8:11], s93 offen offset:2048 sc1
U 56,56,4
Zvaa 148,156,188
U 57,57,4
U 58,58,4
Mvaa 148,152,184
Ta 140,26,20768
Ta 142,26,25888
)"
R"(W 4
Zvaa 160,128,184
U 59,59,4
U 60,60,4
Mvaa 160,132,188
U 61,61,4
U 62,62,4
Zvaa 168,132,204
U 63,63,4
K 56,56,57
Mvaa 168,128,200
Ta 144,25,30720
Ta 146,25,35840
Ta 148,21,0
Ta 150,21,5120
W 4
Zvaa 172,136,200
D 16,40,94,0
K 57,58,59
Mvaa 172,140,204
K 58,60,61
K 59,62,63
Zvaa 164,140,188
Ta 152,26,30752
Ta 154,26,35872
Mvaa 164,136,184
Ta 156,22,32
Ta 158,22,5152
W 4
Mvaa 96,144,192
buffer_store_dwordx4 v[56:59], v1, s[8:11], s93 offen offset:3072 sc1
U 64,64,5
Mvaa 96,148,196
U 65,65,5
U 66,66,5
Mvaa 104,148,212
U 67,67,5
U 68,68,5
Mvaa 104,144,208
Ta 128,25,30784
Ta 130,25,35904
Ta 132,21,64
Ta 134,21,5184
W 4
Mvaa 108,152,208
D 17,40,94,1024
U 69,69,5
Mvaa 108,156,212
U 70,70,5
U 71,71,5
Mvaa 100,156,196
K 64,64,65
K 65,66,67
Mvaa 100,152,192
Ta 136,26,30816
Ta 138,26,35936
Ta 140,22,96
Ta 142,22,5216
W 4
Mvaa 112,128,192
K 66,68,69
K 67,70,71
Mvaa 112,132,196
s_add_u32 s67, s93, 131072
buffer_store_dwordx4 v[64:67], v1, s[8:11], s67 offen offset:0 sc1
U 72,72,5
Mvaa 120,132,212
U 73,73,5
U 74,74,5
Mvaa 120,128,208
Ta 144,25,30848
Ta 146,25,35968
Ta 148,21,128
Ta 150,21,5248
W 4
Mvaa 124,136,208
U 75,75,5
U 76,76,5
Mvaa 124,140,212
D 18,40,94,2048
U 77,77,5
Mvaa 116,140,196
U 78,78,5
U 79,79,5
Mvaa 116,136,192
Ta 152,26,30880
Ta 154,26,36000
Ta 156,22,160
Ta 158,22,5280
W 4
Mvaa 128,144,192
K 72,72,73
K 73,74,75
Mvaa 128,148,196
K 74,76,77
K 75,78,79
Mvaa 136,148,212
buffer_store_dwordx4 v[72:75], v1, s[8:11], s67 offen offset:1024 sc1
U 80,80,5
Mvaa 136,144,208
Ta 128,25,30912
Ta 130,25,36032
Ta 132,21,192
Ta 134,21,5312
W 4
Mvaa 140,152,208
D 19,40,94,3072
s_add_u32 m0, m0, 4096
s_nop 0
buffer_load_dwordx4 v20, s[40:43], s94 offen lds
Mvaa 140,156,212
s_add_u32 s81, s58, 2
s_and_b32 s81, s81, 3
s_lshl_b32 s81, s81, 12
Mvaa 132,156,196
s_add_u32 s81, s81, s49
U 81,81,5
U 82,82,5
Mvaa 132,152,192
Ta 136,26,30944
Ta 138,26,36064
Ta 140,22,224
Ta 142,22,5344
W 4
Mvaa 144,128,192
U 83,83,5
U 84,84,5
Mvaa 144,132,196
U 85,85,5
U 86,86,5
Mvaa 152,132,212
U 87,87,5
K 80,80,81
Mvaa 152,128,208
Ta 144,25,30976
Ta 146,25,36096
Ta 148,21,256
Ta 150,21,5376
W 4
Mvaa 156,136,208
K 81,82,83
K 82,84,85
Mvaa 156,140,212
buffer_load_dwordx4 a[0:3], v1, s[8:11], s81 offen offset:0 sc1
K 83,86,87
Mvaa 148,140,196
s_add_u32 s82, s81, 131072
Ta 152,26,31008
Ta 154,26,36128
Mvaa 148,136,192
Ta 156,22,288
Ta 158,22,5408
W 4
Mvaa 160,144,192
Ta 128,21,10240
Ta 130,21,15360
Mvaa 160,148,196
buffer_store_dwordx4 v[80:83], v1, s[8:11], s67 offen offset:2048 sc1
U 88,88,5
Mvaa 168,148,212
U 89,89,5
U 90,90,5
Mvaa 168,144,208
Ta 132,21,20480
Ta 134,21,25600
W 4
Mvaa 172,152,208
U 91,91,5
U 92,92,5
Mvaa 172,156,212
U 93,93,5
U 94,94,5
Mvaa 164,156,196
U 95,95,5
K 88,88,89
Mvaa 164,152,192
Ta 136,22,10272
Ta 138,22,15392
Ta 140,22,20512
Ta 142,22,25632
s_waitcnt vmcnt(22) lgkmcnt(4)
Mvaa 96,128,216
K 89,90,91
K 90,92,93
Mvaa 96,132,220
buffer_load_dwordx4 a[16:19], v1, s[8:11], s82 offen offset:0 sc1
K 91,94,95
Mvaa 104,132,236
Ta 144,21,10304
Ta 146,21,15424
Mvaa 104,128,232
Ta 148,21,20544
Ta 150,21,25664
W 4
Mvaa 108,136,232
Ta 152,22,10336
Ta 154,22,15456
Mvaa 108,140,236
buffer_store_dwordx4 v[88:91], v1, s[8:11], s67 offen offset:3072 sc1
Ta 156,22,20576
Mvaa 100,140,220
Ta 158,22,25696
Ta 128,21,10368
Mvaa 100,136,216
W 5
Ta 130,21,15488
Ta 132,21,20608
Mvaa 112,144,216
Ta 134,21,25728
Ta 136,22,10400
Mvaa 112,148,220
buffer_load_dwordx4 a[4:7], v1, s[8:11], s81 offen offset:1024 sc1
Ta 138,22,15520
Mvaa 120,148,236
Ta 140,22,20640
Ta 142,22,25760
Mvaa 120,144,232
W 8
Ta 144,21,10432
Ta 146,21,15552
Mvaa 124,152,232
Mvaa 124,156,236
buffer_load_dwordx4 a[20:23], v1, s[8:11], s82 offen offset:1024 sc1
Ta 148,21,20672
Mvaa 116,156,220
Ta 150,21,25792
Mvaa 116,152,216
W 8
Ta 152,22,10464
Ta 154,22,15584
Mvaa 128,128,216
Mvaa 128,132,220
buffer_load_dwordx4 a[8:11], v1, s[8:11], s81 offen offset:2048 sc1
Ta 156,22,20704
Mvaa 136,132,236
Ta 158,22,25824
Mvaa 136,128,232
W 8
Ta 128,21,10496
Ta 130,21,15616
Mvaa 140,136,232
s_waitcnt vmcnt(7)
s_barrier
Mvaa 140,140,236
buffer_load_dwordx4 a[24:27], v1, s[8:11], s82 offen offset:2048 sc1
Ta 132,21,20736
Mvaa 132,140,220
Ta 134,21,25856
Mvaa 132,136,216
W 8
Ta 136,22,10528
Ta 138,22,15648
Mvaa 144,144,216
Mvaa 144,148,220
buffer_load_dwordx4 a[12:15], v1, s[8:11], s81 offen offset:3072 sc1
Ta 140,22,20768
Mvaa 152,148,236
Ta 142,22,25888
Mvaa 152,144,232
W 8
Ta 144,21,30720
Ta 146,21,35840
Mvaa 156,152,232
Mvaa 156,156,236
buffer_load_dwordx4 a[28:31], v1, s[8:11], s82 offen offset:3072 sc1
Ta 148,23,0
Mvaa 148,156,220
s_lshl_b32 s92, s58, 2
s_add_u32 s91, s92, s51
s_add_u32 s95, s91, 11
Mvaa 148,152,216
W 7
s_and_b32 s95, s95, 15
s_mul_i32 s95, s95, 40960
Mvaa 160,128,216
s_mul_i32 s71, s51, 10240
s_add_u32 s71, s71, 40960
s_add_u32 m0, s71, s48
Mvaa 160,132,220
s_lshl2_add_u32 s94, s48, s95
D 16,40,94,0
Ta 150,23,5120
Mvaa 168,132,236
s_add_u32 s95, s91, 13
s_and_b32 s95, s95, 15
s_mul_i32 s95, s95, 40960
Mvaa 168,128,232
W 4
s_mul_i32 s71, s51, 20480
s_add_u32 s71, s71, 71680
Mvaa 172,136,232
Ta 152,22,30752
Ta 154,22,35872
Mvaa 172,140,236
D 17,40,94,1024
Ta 156,24,32
Mvaa 164,140,220
Ta 158,24,5152
Ta 128,21,30784
Mvaa 164,136,216
W 5
Ta 130,21,35904
Ta 132,23,64
Mvaa 96,144,224
Ta 134,23,5184
Ta 136,22,30816
Mvaa 96,148,228
D 18,40,94,2048
Ta 138,22,35936
Mvaa 104,148,244
Ta 140,24,96
Ta 142,24,5216
Mvaa 104,144,240
W 8
Ta 144,21,30848
Ta 146,21,35968
Mvaa 108,152,240
Mvaa 108,156,244
D 19,40,94,3072
Ta 148,23,128
Mvaa 100,156,228
s_add_u32 m0, m0, 4096
Ta 150,23,5248
Mvaa 100,152,224
W 8
Ta 152,22,30880
Ta 154,22,36000
Mvaa 112,128,224
Mvaa 112,132,228
buffer_load_dwordx4 v20, s[40:43], s94 offen lds
Ta 156,24,160
Mvaa 120,132,244
s_add_u32 m0, s71, s48
s_lshl2_add_u32 s94, s48, s95
Ta 158,24,5280
Mvaa 120,128,240
W 8
Ta 128,21,30912
Ta 130,21,36032
Mvaa 124,136,240
Mvaa 124,140,244
D 16,40,94,0
Ta 132,23,192
Mvaa 116,140,228
s_add_u32 s95, s92, 15
s_and_b32 s95, s95, 15
s_mul_i32 s95, s95, 40960
Mvaa 116,136,224
W 7
s_mul_i32 s71, s51, 0
s_add_u32 s71, s71, 102400
Mvaa 128,144,224
Ta 134,23,5312
Ta 136,22,30944
Mvaa 128,148,228
D 17,40,94,1024
Ta 138,22,36064
Mvaa 136,148,244
Ta 140,24,224
Ta 142,24,5344
Mvaa 136,144,240
W 8
Ta 144,21,30976
Ta 146,21,36096
Mvaa 140,152,240
Mvaa 140,156,244
D 18,40,94,2048
Ta 148,23,256
Mvaa 132,156,228
Ta 150,23,5376
Mvaa 132,152,224
W 8
Ta 152,22,31008
Ta 154,22,36128
Mvaa 144,128,224
s_lshl_b32 s80, s58, 3
s_add_u32 s80, s80, s55
Mvaa 144,132,228
D 19,40,94,3072
Ta 156,24,288
Mvaa 152,132,244
s_add_u32 m0, m0, 4096
Ta 158,24,5408
Mvaa 152,128,240
W 8
Ta 128,23,20480
Ta 130,23,25600
Mvaa 156,136,240
s_waitcnt vmcnt(15)
v_mov_b64 v[28:29], s[12:13]
s_mov_b64 exec, 1
buffer_store_dwordx2 v[28:29], off, s[8:11], s80 sc1
s_mov_b64 exec, -1
Mvaa 156,140,244
Ta 132,23,51200
Ta 134,23,56320
Mvaa 148,140,228
Mvaa 148,136,224
W 8
Ta 136,24,20512
)"
R"(Ta 138,24,25632
Mvaa 160,144,224
buffer_load_dwordx4 v20, s[40:43], s94 offen lds
Mvaa 160,148,228
s_add_u32 m0, s71, s48
s_lshl2_add_u32 s94, s48, s95
Ta 140,24,51232
Mvaa 168,148,244
Ta 142,24,56352
Mvaa 168,144,240
W 8
Ta 144,23,20544
Ta 146,23,25664
Mvaa 172,152,240
D 16,40,94,0
Mvaa 172,156,244
Ta 148,23,51264
Ta 150,23,56384
Mvaa 164,156,228
Mvaa 164,152,224
s_waitcnt vmcnt(16) lgkmcnt(8)
Ta 152,24,20576
Ta 154,24,25696
Mvaa 96,128,0
D 17,40,94,1024
Mvaa 96,132,4
Ta 156,24,51296
Ta 158,24,56416
Mvaa 104,132,20
Mvaa 104,128,16
W 8
Ta 128,23,20608
Ta 130,23,25728
Mvaa 108,136,16
D 18,40,94,2048
Mvaa 108,140,20
Ta 132,23,51328
Ta 134,23,56448
Mvaa 100,140,4
Mvaa 100,136,0
W 8
Ta 136,24,20640
Ta 138,24,25760
Mvaa 112,144,0
D 19,40,94,3072
Mvaa 112,148,4
s_add_u32 m0, m0, 4096
Ta 140,24,51360
Ta 142,24,56480
Mvaa 120,148,20
Mvaa 120,144,16
W 8
Ta 144,23,20672
Ta 146,23,25792
Mvaa 124,152,16
buffer_load_dwordx4 v20, s[40:43], s94 offen lds
Mvaa 124,156,20
s_add_u32 s81, s58, 3
s_and_b32 s81, s81, 3
s_lshl_b32 s81, s81, 12
Mvaa 116,156,4
s_add_u32 s81, s81, s49
Ta 148,23,51392
Ta 150,23,56512
Mvaa 116,152,0
W 8
Ta 152,24,20704
Ta 154,24,25824
Mvaa 128,128,0
buffer_load_dwordx4 a[32:35], v1, s[8:11], s81 offen offset:0 sc1
Mvaa 128,132,4
s_add_u32 s82, s81, 131072
Ta 156,24,51424
Ta 158,24,56544
Mvaa 136,132,20
Mvaa 136,128,16
W 8
Ta 128,23,20736
Ta 130,23,25856
Mvaa 140,136,16
s_waitcnt vmcnt(12)
s_barrier
buffer_load_dwordx4 a[48:51], v1, s[8:11], s82 offen offset:0 sc1
Mvaa 140,140,20
Ta 132,23,51456
v_and_b32 v27, 1, v12
Mvaa 132,140,4
Ta 134,23,56576
Mvaa 132,136,0
W 8
Ta 136,24,20768
v_lshlrev_b32 v27, 1, v27
Mvaa 144,144,0
buffer_load_dwordx4 a[36:39], v1, s[8:11], s81 offen offset:1024 sc1
Ta 138,24,25888
Mvaa 144,148,4
Ta 140,24,51488
v_xor_b32 v27, v27, v13
Mvaa 152,148,20
Ta 142,24,56608
Mvaa 152,144,16
W 8
Ta 144,25,0
v_and_b32 v12, 2, v12
Mvaa 156,152,16
buffer_load_dwordx4 a[52:55], v1, s[8:11], s82 offen offset:1024 sc1
Ta 146,25,5120
Mvaa 156,156,20
Ta 148,21,40960
Ta 150,21,46080
Mvaa 148,156,4
v_or_b32 v12, v12, v14
Mvaa 148,152,0
W 8
Ta 152,26,32
Ta 154,26,5152
Mvaa 160,128,0
buffer_load_dwordx4 a[40:43], v1, s[8:11], s81 offen offset:2048 sc1
v_lshl_or_b32 v12, v27, 2, v12
Mvaa 160,132,4
Ta 156,22,40992
Ta 158,22,46112
Mvaa 168,132,20
v_cmp_eq_u32_e64 s[86:87], 0, v12
Mvaa 168,128,16
W 8
Ta 128,25,64
Ta 130,25,5184
Mvaa 172,136,16
buffer_load_dwordx4 a[56:59], v1, s[8:11], s82 offen offset:2048 sc1
v_cndmask_b32_e64 v4, 0, v15, s[86:87]
Mvaa 172,140,20
Ta 132,21,41024
Ta 134,21,46144
Mvaa 164,140,4
Mvaa 164,136,0
W 8
Ta 136,26,96
v_cmp_eq_u32_e64 s[86:87], 1, v12
Mvaa 96,144,8
buffer_load_dwordx4 a[44:47], v1, s[8:11], s81 offen offset:3072 sc1
Ta 138,26,5216
Mvaa 96,148,12
Ta 140,22,41056
v_cndmask_b32_e64 v5, 0, v15, s[86:87]
Mvaa 104,148,28
Ta 142,22,46176
Mvaa 104,144,24
W 8
Ta 144,25,128
v_cmp_eq_u32_e64 s[86:87], 2, v12
Mvaa 108,152,24
buffer_load_dwordx4 a[60:63], v1, s[8:11], s82 offen offset:3072 sc1
Ta 146,25,5248
Mvaa 108,156,28
Ta 148,21,41088
Ta 150,21,46208
Mvaa 100,156,12
v_cndmask_b32_e64 v6, 0, v15, s[86:87]
Mvaa 100,152,8
W 8
Ta 152,26,160
Ta 154,26,5280
Mvaa 112,128,8
v_cmp_eq_u32_e64 s[86:87], 3, v12
Mvaa 112,132,12
Ta 156,22,41120
Ta 158,22,46240
Mvaa 120,132,28
v_cndmask_b32_e64 v7, 0, v15, s[86:87]
Mvaa 120,128,24
W 8
Ta 128,25,192
Ta 130,25,5312
Mvaa 124,136,24
v_cmp_eq_u32_e64 s[86:87], 4, v12
Mvaa 124,140,28
Ta 132,21,41152
Ta 134,21,46272
Mvaa 116,140,12
Mvaa 116,136,8
W 8
Ta 136,26,224
v_cndmask_b32_e64 v8, 0, v15, s[86:87]
Mvaa 128,144,8
Ta 138,26,5344
Mvaa 128,148,12
Ta 140,22,41184
v_cmp_eq_u32_e64 s[86:87], 5, v12
Mvaa 136,148,28
Ta 142,22,46304
Mvaa 136,144,24
W 8
Ta 144,25,256
v_cndmask_b32_e64 v9, 0, v15, s[86:87]
Mvaa 140,152,24
s_waitcnt vmcnt(8)
s_barrier
Ta 146,25,5376
Mvaa 140,156,28
Ta 148,21,41216
v_cmp_eq_u32_e64 s[86:87], 6, v12
Mvaa 132,156,12
Ta 150,21,46336
Mvaa 132,152,8
W 8
Ta 152,26,288
Ta 154,26,5408
Mvaa 144,128,8
v_cndmask_b32_e64 v10, 0, v15, s[86:87]
Mvaa 144,132,12
Ta 156,22,41248
Ta 158,22,46368
Mvaa 152,132,28
v_cmp_eq_u32_e64 s[86:87], 7, v12
Mvaa 152,128,24
W 8
Ta 128,21,51200
Ta 130,21,56320
Mvaa 156,136,24
v_cndmask_b32_e64 v11, 0, v15, s[86:87]
Mvaa 156,140,28
Ta 132,23,10240
Ta 134,23,15360
Mvaa 148,140,12
Mvaa 148,136,8
W 8
s_mov_b32 s20, s38
Ta 136,23,30720
Mvaa 160,144,8
Ta 138,23,35840
v_bfe_u32 v28, v1, 4, 2
Mvaa 160,148,12
s_and_b32 s21, s39, 0xffff
Ta 140,23,40960
v_lshlrev_b32 v28, 3, v28
Mvaa 168,148,28
Ta 142,23,46080
buffer_load_dwordx2 v[28:29], v28, s[8:11], s55 offen sc1
Mvaa 168,144,24
W 8
s_mov_b32 s22, 2621440
Ta 144,22,51232
Mvaa 172,152,24
Ta 146,22,56352
Mvaa 172,156,28
s_mov_b32 s23, 0x20000
Ta 148,24,10272
Ta 150,24,15392
Mvaa 164,156,12
Mvaa 164,152,8
s_waitcnt vmcnt(5) lgkmcnt(8)
Ta 152,24,30752
Ta 154,24,35872
Mvaa 96,128,32
s_and_b32 s17, s17, 0xffff
Mvaa 96,132,36
Ta 156,24,40992
Ta 158,24,46112
Mvaa 104,132,52
s_mov_b32 s18, 2621440
Mvaa 104,128,48
s_waitcnt vmcnt(1) lgkmcnt(8)
Ta 128,21,51264
Ta 130,21,56384
Mvaa 104,136,56
s_mov_b32 s19, 0x20000
Mvaa 104,140,60
Ta 132,23,10304
Ta 134,23,15424
Mvaa 96,140,44
Mvaa 96,136,40
W 8
s_lshr_b32 s66, s15, 3
Ta 136,23,30784
Mvaa 100,144,32
Ta 138,23,35904
Mvaa 100,148,36
s_lshl_b32 s66, s66, 4
Ta 140,23,41024
Ta 142,23,46144
Mvaa 108,148,52
Mvaa 108,144,48
W 8
s_add_u32 s66, s66, s59
Ta 144,22,51296
Mvaa 108,152,56
Ta 146,22,56416
Mvaa 108,156,60
s_lshl_b32 s63, s66, 6
Ta 148,24,10336
Ta 150,24,15456
Mvaa 100,156,44
Mvaa 100,152,40
W 8
Ta 152,24,30816
Ta 154,24,35936
Mvaa 112,128,32
s_add_u32 s63, s63, 65536
Mvaa 112,132,36
Ta 156,24,41056
Ta 158,24,46176
Mvaa 120,132,52
s_mul_i32 s62, s66, 40960
K 96,96,97
K 97,98,99
Mvaa 120,128,48
W 8
Ta 128,21,51328
Ta 130,21,56448
Mvaa 120,136,56
s_add_u32 s62, s62, 56623104
K 98,100,101
Mvaa 120,140,60
Ta 132,23,10368
Ta 134,23,15488
Mvaa 112,140,44
s_lshl_b32 s66, s14, 13
K 99,102,103
Mvaa 112,136,40
W 8
s_add_u32 s83, s52, 0
Ta 136,23,30848
Mvaa 116,144,32
Ta 138,23,35968
Mvaa 116,148,36
s_add_u32 s78, s62, s66
buffer_store_dwordx4 v[96:99], v1, s[8:11], s83 offen sc1
Ta 140,23,41088
Mvaa 124,148,52
Ta 142,23,46208
Mvaa 124,144,48
W 8
s_add_u32 s79, s78, 327680
K 104,104,105
Mvaa 124,152,56
Ta 144,22,51360
Ta 146,22,56480
Mvaa 124,156,60
s_and_b32 s66, s14, 1
Ta 148,24,10400
K 105,106,107
Mvaa 116,156,44
Ta 150,24,15520
K 106,108,109
Mvaa 116,152,40
W 8
Ta 152,24,30880
Ta 154,24,36000
Mvaa 128,128,32
s_mul_i32 s66, s66, 327680
K 107,110,111
Mvaa 128,132,36
Ta 156,24,41120
Ta 158,24,46240
Mvaa 136,132,52
s_add_u32 s83, s52, 40960
s_add_u32 s65, s62, s66
s_waitcnt vmcnt(1)
v_cmp_eq_u64 vcc, s[12:13], v[28:29]
s_cmp_eq_u64 vcc, -1
s_cbranch_scc0 .Lspin_84%=
.Lok_85%=:
Mvaa 136,128,48
W 8
s_add_u32 s81, s58, 1
s_and_b32 s81, s81, 3
Mvaa 136,136,56
s_lshl_b32 s81, s81, 12
s_add_u32 s81, s81, s50
buffer_load_dwordx4 a[64:67], v1, s[8:11], s81 offen offset:0 sc1
Mvaa 136,140,60
s_add_u32 s82, s81, 131072
Ta 128,21,51392
Ta 130,21,56512
Mvaa 128,140,44
s_add_u32 s65, s65, 32768
Ta 132,23,10432
Ta 134,23,15552
Mvaa 128,136,40
W 8
s_mov_b32 s64, 0
Ta 136,23,30912
Mvaa 132,144,32
buffer_store_dwordx4 v[104:107], v1, s[8:11], s83 offen sc1
K 112,112,113
Mvaa 132,148,36
Ta 138,23,36032
K 113,114,115
Mvaa 140,148,52
v_add_u32 v19, 4096, v1
K 114,116,117
Mvaa 140,144,48
W 6
Ta 140,23,41152
Ta 142,23,46272
Mvaa 140,152,56
buffer_load_dwordx4 a[80:83], v1, s[8:11], s82 offen offset:0 sc1
K 115,118,119
Mvaa 140,156,60
)"
R"(s_add_u32 s83, s52, 8192
Ta 144,22,51424
v_bfe_u32 v27, v1, 4, 3
Mvaa 132,156,44
Ta 146,22,56544
v_lshlrev_b32 v27, 3, v27
Mvaa 132,152,40
W 6
Ta 148,24,10464
Ta 150,24,15584
Mvaa 144,128,32
buffer_load_dwordx4 a[68:71], v1, s[8:11], s81 offen offset:1024 sc1
Ta 152,24,30944
Mvaa 144,132,36
Ta 154,24,36064
v_bfe_u32 v18, v1, 7, 1
Mvaa 152,132,52
Ta 156,24,41184
Ta 158,24,46304
Mvaa 152,128,48
W 8
Ta 128,21,51456
v_lshl_add_u32 v18, v18, 9, v27
Mvaa 152,136,56
s_mov_b32 s71, 0
buffer_store_dwordx4 v[112:115], v1, s[8:11], s83 offen sc1
K 120,120,121
Mvaa 152,140,60
K 121,122,123
K 122,124,125
Mvaa 144,140,44
Ta 130,21,56576
K 123,126,127
Mvaa 144,136,40
W 6
Ta 132,23,10496
Ta 134,23,15616
Mvaa 148,144,32
s_add_u32 s83, s52, 49152
buffer_load_dwordx4 a[84:87], v1, s[8:11], s82 offen offset:1024 sc1
v_bfe_u32 v20, v1, 4, 4
Mvaa 148,148,36
Ta 136,23,30976
Ta 138,23,36096
Mvaa 156,148,52
Ta 140,23,41216
v_mul_u32_u24 v20, 1280, v20
Mvaa 156,144,48
W 7
Ta 142,23,46336
v_lshrrev_b32 v12, 8, v1
Mvaa 156,152,56
buffer_load_dwordx4 a[72:75], v1, s[8:11], s81 offen offset:2048 sc1
Ta 144,22,51488
Mvaa 156,156,60
Ta 146,22,56608
v_lshl_add_u32 v20, v12, 4, v20
Mvaa 148,156,44
Ta 148,24,10528
Ta 150,24,15648
Mvaa 148,152,40
W 8
s_bfe_u32 s66, s59, 0x10001
Ta 152,24,31008
Mvaa 160,128,32
buffer_store_dwordx4 v[120:123], v1, s[8:11], s83 offen sc1
K 128,128,129
Mvaa 160,132,36
K 129,130,131
K 130,132,133
Mvaa 168,132,52
s_lshl_b32 s67, s57, 1
Ta 154,24,36128
K 131,134,135
Mvaa 168,128,48
W 6
s_add_u32 s83, s52, 16384
Ta 156,24,41248
Mvaa 168,136,56
s_add_u32 s66, s66, s67
buffer_load_dwordx4 a[88:91], v1, s[8:11], s82 offen offset:2048 sc1
Ta 158,24,46368
Mvaa 168,140,60
Ta 128,25,10240
Ta 130,25,15360
Mvaa 160,140,44
s_lshl_b32 s61, s66, 2
Ta 132,25,20480
Ta 134,25,25600
Mvaa 160,136,40
W 8
Ta 136,26,10272
Ta 138,26,15392
Mvaa 164,144,32
buffer_load_dwordx4 a[76:79], v1, s[8:11], s81 offen offset:3072 sc1
Mvaa 164,148,36
s_lshr_b32 s66, s59, 2
Ta 140,26,20512
Ta 142,26,25632
Mvaa 172,148,52
Mvaa 172,144,48
W 8
s_lshl_b32 s66, s66, 1
Ta 144,25,10304
Mvaa 172,152,56
buffer_store_dwordx4 v[128:131], v1, s[8:11], s83 offen sc1
K 136,136,137
Mvaa 172,156,60
s_add_u32 s61, s61, s66
K 137,138,139
K 138,140,141
Mvaa 164,156,44
Ta 146,25,15424
K 139,142,143
Mvaa 164,152,40
W 6
s_add_u32 s83, s52, 57344
Ta 148,25,20544
Zvav 176,128,32
s_and_b32 s66, s59, 1
buffer_load_dwordx4 a[92:95], v1, s[8:11], s82 offen offset:3072 sc1
Ta 150,25,25664
Mvav 176,132,40
s_add_u32 s81, s58, 2
s_and_b32 s81, s81, 3
s_lshl_b32 s81, s81, 12
Zvav 184,132,72
s_add_u32 s81, s81, s50
Ta 152,26,10336
Ta 154,26,15456
Mvav 184,128,64
W 6
Ta 156,26,20576
Ta 158,26,25696
Zvav 188,136,64
s_add_u32 s61, s61, s66
buffer_load_dwordx4 a[96:99], v1, s[8:11], s81 offen offset:0 sc1
Ta 128,25,10368
Mvav 188,140,72
s_add_u32 s82, s81, 131072
s_xor_b32 s67, s61, 0
Ta 130,25,15488
Zvav 180,140,40
s_mul_i32 s72, s67, 20480
Ta 132,25,20608
Ta 134,25,25728
Mvav 180,136,32
W 8
Ta 136,26,10400
Ta 138,26,15520
Zvav 192,144,32
s_mul_i32 s66, s58, 320
buffer_store_dwordx4 v[136:139], v1, s[8:11], s83 offen sc1
K 144,144,145
Mvav 192,148,40
K 145,146,147
K 146,148,149
Zvav 200,148,72
Ta 140,26,20640
K 147,150,151
Mvav 200,144,64
W 7
s_add_u32 s83, s52, 24576
Ta 142,26,25760
Zvav 204,152,64
s_add_u32 s72, s72, s66
buffer_load_dwordx4 a[112:115], v1, s[8:11], s82 offen offset:0 sc1
Ta 144,25,10432
Mvav 204,156,72
s_mul_i32 s75, s67, 20480
Ta 146,25,15552
Ta 148,25,20672
Zvav 196,156,40
Ta 150,25,25792
Mvav 196,152,32
W 8
s_mul_i32 s66, s58, 2560
Ta 152,26,10464
Zvav 208,128,32
buffer_load_dwordx4 a[100:103], v1, s[8:11], s81 offen offset:1024 sc1
Ta 154,26,15584
Mvav 208,132,40
Ta 156,26,20704
Ta 158,26,25824
Zvav 216,132,72
s_add_u32 s75, s75, s66
Mvav 216,128,64
W 8
Ta 128,25,10496
Ta 130,25,15616
Zvav 220,136,64
s_add_u32 s75, s75, 131072
buffer_store_dwordx4 v[144:147], v1, s[8:11], s83 offen sc1
K 152,152,153
Mvav 220,140,72
K 153,154,155
K 154,156,157
Zvav 212,140,40
s_xor_b32 s67, s61, 124
Ta 132,25,20736
K 155,158,159
Mvav 212,136,32
W 7
s_add_u32 s83, s52, 65536
Ta 134,25,25856
Zvav 224,144,32
s_mul_i32 s73, s67, 20480
buffer_load_dwordx4 a[116:119], v1, s[8:11], s82 offen offset:1024 sc1
Ta 136,26,10528
Mvav 224,148,40
Ta 138,26,15648
Ta 140,26,20768
Zvav 232,148,72
Ta 142,26,25888
Mvav 232,144,64
W 8
s_mul_i32 s66, s58, 320
Ta 144,25,30720
Zvav 236,152,64
buffer_load_dwordx4 a[104:107], v1, s[8:11], s81 offen offset:2048 sc1
Ta 146,25,35840
Mvav 236,156,72
s_add_u32 s73, s73, s66
Ta 148,21,0
Ta 150,21,5120
Zvav 228,156,40
Mvav 228,152,32
W 8
s_mul_i32 s76, s67, 20480
Ta 152,26,30752
Zvav 240,128,32
buffer_store_dwordx4 v[152:155], v1, s[8:11], s83 offen sc1
K 160,160,161
Mvav 240,132,40
K 161,162,163
K 162,164,165
Zvav 248,132,72
s_mul_i32 s66, s58, 2560
Ta 154,26,35872
K 163,166,167
Mvav 248,128,64
W 6
s_add_u32 s83, s52, 32768
Ta 156,22,32
Zvav 252,136,64
s_add_u32 s76, s76, s66
buffer_load_dwordx4 a[120:123], v1, s[8:11], s82 offen offset:2048 sc1
Ta 158,22,5152
Mvav 252,140,72
Ta 128,25,30784
Ta 130,25,35904
Zvav 244,140,40
s_add_u32 s76, s76, 131072
Ta 132,21,64
Ta 134,21,5184
Mvav 244,136,32
W 8
Ta 136,26,30816
Ta 138,26,35936
Mvav 176,144,48
buffer_load_dwordx4 a[108:111], v1, s[8:11], s81 offen offset:3072 sc1
v_bfe_u32 v15, v1, 7, 1
Mvav 176,148,56
Ta 140,22,96
Ta 142,22,5216
Mvav 184,148,88
Mvav 184,144,80
W 8
Ta 144,25,30848
v_mul_u32_u24 v14, 10240, v15
Mvav 188,152,80
buffer_store_dwordx4 v[160:163], v1, s[8:11], s83 offen sc1
K 168,168,169
Mvav 188,156,88
K 169,170,171
K 170,172,173
Mvav 180,156,56
Ta 146,25,35968
K 171,174,175
Mvav 180,152,48
W 6
s_add_u32 s83, s52, 73728
v_lshrrev_b32 v15, 8, v1
Mvav 192,128,48
buffer_load_dwordx4 a[124:127], v1, s[8:11], s82 offen offset:3072 sc1
Ta 148,21,128
Mvav 192,132,56
s_add_u32 s81, s58, 3
s_and_b32 s81, s81, 3
s_lshl_b32 s81, s81, 12
Mvav 200,132,88
s_add_u32 s81, s81, s50
Ta 150,21,5248
v_lshl_add_u32 v14, v15, 7, v14
Mvav 200,128,80
W 4
Ta 152,26,30880
Ta 154,26,36000
Mvav 204,136,80
buffer_load_dwordx4 a[184:187], v1, s[8:11], s81 offen offset:0 sc1
v_and_b32 v15, 0x70, v1
Mvav 204,140,88
s_add_u32 s82, s81, 131072
Ta 156,22,160
Ta 158,22,5280
Mvav 196,140,56
Ta 128,25,30912
Ta 130,25,36032
Mvav 196,136,48
W 6
Ta 132,21,192
v_add_u32 v14, v14, v15
Mvav 208,144,48
s_bitcmp1_b32 s14, 0
s_cselect_b32 s74, s73, s72
s_cselect_b32 s77, s76, s75
buffer_store_dwordx4 v[168:171], v1, s[8:11], s83 offen sc1
Ta 134,21,5312
Mvav 208,148,56
s_add_u32 s74, s74, 256
Ta 136,26,30944
Ta 138,26,36064
Mvav 216,148,88
Ta 140,22,224
Ta 142,22,5344
Mvav 216,144,80
W 8
s_add_u32 s77, s77, 2048
Ta 144,25,30976
Mvav 220,152,80
buffer_load_dwordx4 a[200:203], v1, s[8:11], s82 offen offset:0 sc1
Ta 146,25,36096
Mvav 220,156,88
s_lshl_b32 s66, s14, 6
Ta 148,21,256
Ta 150,21,5376
Mvav 212,156,56
Mvav 212,152,48
W 8
s_lshl_b32 s67, s14, 9
Ta 152,26,31008
Mvav 224,128,48
buffer_load_dwordx4 a[188:191], v1, s[8:11], s81 offen offset:1024 sc1
Ta 154,26,36128
Mvav 224,132,56
s_add_u32 s72, s72, s66
Ta 156,22,288
Ta 158,22,5408
Mvav 232,132,88
Mvav 232,128,80
W 8
Ta 128,21,10240
Ta 130,21,15360
Mvav 236,136,80
s_add_u32 s75, s75, s67
buffer_load_dwordx4 a[204:207], v1, s[8:11], s82 offen offset:1024 sc1
Mvav 236,140,88
Ta 132,21,20480
Ta 134,21,25600
Mvav 228,140,56
s_add_u32 s73, s73, s66
Mvav 228,136,48
W 8
Ta 136,22,10272
Ta 138,22,15392
Mvav 240,144,48
s_add_u32 s76, s76, s67
buffer_load_dwordx4 a[192:195], v1, s[8:11], s81 offen offset:2048 sc1
Mvav 240,148,56
Ta 140,22,20512
Ta 142,22,25632
Mvav 248,148,88
Mvav 248,144,80
W 8
Ta 144,21,10304
Ta 146,21,15424
Mvav 252,152,80
buffer_load_dwordx4 a[208:211], v1, s[8:11], s82 offen offset:2048 sc1
Mvav 252,156,88
Ta 148,21,20544
Ta 150,21,25664
Mvav 244,156,56
Mvav 244,152,48
s_waitcnt vmcnt(25) lgkmcnt(8)
Ta 152,22,10336
Ta 154,22,15456
Mvaa 176,128,64
buffer_load_dwordx4 a[196:199], v1, s[8:11], s81 offen offset:3072 sc1
Mvaa 176,132,68
Ta 156,22,20576
Ta 158,22,25696
Mvaa 184,132,84
Mvaa 184,128,80
W 8
Ta 128,21,10368
Ta 130,21,15488
Mvaa 188,136,80
buffer_load_dwordx4 a[212:215], v1, s[8:11], s82 offen offset:3072 sc1
Mvaa 188,140,84
Ta 132,21,20608
)"
R"(Ta 134,21,25728
Mvaa 180,140,68
Mvaa 180,136,64
W 8
Ta 136,22,10400
Ta 138,22,15520
Mvaa 192,144,64
buffer_load_dwordx4 v[36:39], v20, s[16:19], s72 offen
Mvaa 192,148,68
Ta 140,22,20640
Ta 142,22,25760
Mvaa 200,148,84
Mvaa 200,144,80
W 8
Ta 144,21,10432
Ta 146,21,15552
Mvaa 204,152,80
buffer_load_dwordx4 v[44:47], v14, s[8:11], s75 offen sc1
Mvaa 204,156,84
Ta 148,21,20672
Ta 150,21,25792
Mvaa 196,156,68
Mvaa 196,152,64
W 8
Ta 152,22,10464
Ta 154,22,15584
Mvaa 208,128,64
buffer_load_dwordx4 v[52:55], v20, s[16:19], s73 offen
Mvaa 208,132,68
Ta 156,22,20704
Ta 158,22,25824
Mvaa 216,132,84
Mvaa 216,128,80
W 8
Ta 128,21,10496
Ta 130,21,15616
Mvaa 220,136,80
buffer_load_dwordx4 v[60:63], v14, s[8:11], s76 offen sc1
Mvaa 220,140,84
Ta 132,21,20736
Ta 134,21,25856
Mvaa 212,140,68
Mvaa 212,136,64
s_waitcnt vmcnt(11)
v_lshlrev_b32 v2, 2, v1
v_mov_b64 v[28:29], s[12:13]
s_mov_b64 exec, 3
buffer_store_dwordx2 v[28:29], v2, s[8:11], s53 offen offset:0 sc1
s_mov_b64 exec, -1
Ta 136,22,10528
W 9
Mvaa 224,144,64
Ta 138,22,15648
Mvaa 224,148,68
Ta 140,22,20768
Ta 142,22,25888
Mvaa 232,148,84
Mvaa 232,144,80
W 8
buffer_load_dwordx4 v[68:71], v20, s[16:19], s74 offen
Ta 144,21,30720
Mvaa 236,152,80
Ta 146,21,35840
Mvaa 236,156,84
Ta 148,23,0
Ta 150,23,5120
Mvaa 228,156,68
Mvaa 228,152,64
W 8
buffer_load_dwordx4 v[76:79], v14, s[8:11], s77 offen sc1
Ta 152,22,30752
Mvaa 240,128,64
Ta 154,22,35872
Mvaa 240,132,68
Ta 156,24,32
Ta 158,24,5152
Mvaa 248,132,84
Mvaa 248,128,80
W 8
buffer_load_dwordx2 v[12:13], v27, s[8:11], 0 offen offset:3584 sc1
Ta 128,21,30784
Mvaa 252,136,80
Ta 130,21,35904
Mvaa 252,140,84
Ta 132,23,64
Ta 134,23,5184
Mvaa 244,140,68
Mvaa 244,136,64
W 8
Ta 136,22,30816
Ta 138,22,35936
Mvaa 176,144,72
Mvaa 176,148,76
Ta 140,24,96
Ta 142,24,5216
Mvaa 184,148,92
Mvaa 184,144,88
W 8
Ta 144,21,30848
Ta 146,21,35968
Mvaa 188,152,88
Mvaa 188,156,92
Ta 148,23,128
Ta 150,23,5248
Mvaa 180,156,76
Mvaa 180,152,72
W 8
Ta 152,22,30880
Ta 154,22,36000
Mvaa 192,128,72
Mvaa 192,132,76
Ta 156,24,160
Ta 158,24,5280
Mvaa 200,132,92
Mvaa 200,128,88
W 8
Ta 128,21,30912
Ta 130,21,36032
Mvaa 204,136,88
Mvaa 204,140,92
Ta 132,23,192
Ta 134,23,5312
Mvaa 196,140,76
Mvaa 196,136,72
W 8
Ta 136,22,30944
Ta 138,22,36064
Mvaa 208,144,72
Mvaa 208,148,76
Ta 140,24,224
Ta 142,24,5344
Mvaa 216,148,92
Mvaa 216,144,88
W 8
Ta 144,21,30976
Ta 146,21,36096
Mvaa 220,152,88
buffer_load_dwordx2 v[16:17], v18, s[8:11], s63 offen sc1
Mvaa 220,156,92
Ta 148,23,256
Ta 150,23,5376
Mvaa 212,156,76
Mvaa 212,152,72
W 8
Ta 152,22,31008
Ta 154,22,36128
Mvaa 224,128,72
Mvaa 224,132,76
Ta 156,24,288
Ta 158,24,5408
Mvaa 232,132,92
Mvaa 232,128,88
W 8
Ta 128,23,20480
Ta 130,23,25600
Mvaa 236,136,88
Mvaa 236,140,92
Ta 132,23,51200
Ta 134,23,56320
Mvaa 228,140,76
Mvaa 228,136,72
W 8
Ta 136,25,0
Ta 138,25,5120
Mvaa 240,144,72
Mvaa 240,148,76
Ta 140,21,40960
Ta 142,21,46080
Mvaa 248,148,92
Mvaa 248,144,88
W 8
Ta 144,21,51200
Ta 146,21,56320
Mvaa 252,152,88
Mvaa 252,156,92
Ta 148,23,10240
Ta 150,23,15360
Mvaa 244,156,76
Mvaa 244,152,72
W 8
Ta 152,23,30720
Ta 154,23,35840
Mvaa 176,128,96
Mvaa 176,132,100
Ta 156,23,40960
Ta 158,23,46080
Mvaa 184,132,116
Mvaa 184,128,112
W 8
Ta 128,24,20512
Ta 130,24,25632
Mvaa 184,136,120
Mvaa 184,140,124
Ta 132,24,51232
Ta 134,24,56352
Mvaa 176,140,108
Mvaa 176,136,104
s_waitcnt vmcnt(13) lgkmcnt(8)
Ta 136,26,32
Ta 138,26,5152
Mvaa 176,144,184
Mvaa 176,148,188
Ta 140,22,40992
Ta 142,22,46112
Mvaa 184,148,204
Mvaa 184,144,200
s_waitcnt vmcnt(9) lgkmcnt(8)
Ta 144,22,51232
Ta 146,22,56352
Mvaa 184,152,208
Mvaa 184,156,212
Ta 148,24,10272
Ta 150,24,15392
Mvaa 176,156,196
s_waitcnt vmcnt(1)
v_xor_b32 v12, s12, v12
v_xor_b32 v13, s13, v13
v_lshrrev_b32 v3, 11, v12
v_or_b32 v13, v13, v3
v_cmp_eq_u32 vcc, 0, v13
s_cmp_eq_u64 vcc, -1
s_cbranch_scc0 .LL_89%=
.LL_90%=:
v_bfe_u32 v13, v12, 4, 7
s_xor_b32 s66, s61, 124
v_cmp_eq_u32 vcc, s61, v13
v_cmp_eq_u32_e64 s[86:87], s66, v13
s_or_b32 s68, vcc_lo, s86
s_and_b32 s68, s68, 255
s_cbranch_scc1 .Lup_87%=
.Lupr_88%=:
Mvaa 176,152,192
W 8
Ta 152,24,30752
Ta 154,24,35872
Mvaa 180,128,96
v_lshlrev_b32 v32, 16, v36
Mvaa 180,132,100
Ta 156,24,40992
v_and_b32 v33, 0xffff0000, v36
Mvaa 188,132,116
Ta 158,24,46112
v_lshlrev_b32 v34, 16, v37
Mvaa 188,128,112
W 8
Ta 128,23,20544
v_and_b32 v35, 0xffff0000, v37
Mvaa 188,136,120
Ta 130,23,25664
v_lshlrev_b32 v36, 16, v38
Mvaa 188,140,124
Ta 132,23,51264
v_and_b32 v37, 0xffff0000, v38
Mvaa 180,140,108
Ta 134,23,56384
v_lshlrev_b32 v38, 16, v39
Mvaa 180,136,104
W 8
Ta 136,25,64
v_and_b32 v39, 0xffff0000, v39
Mvaa 180,144,184
s_waitcnt vmcnt(0)
v_cmp_eq_u64 vcc, s[12:13], v[16:17]
s_and_b32 s71, vcc_lo, 255
Ta 138,25,5184
v_lshlrev_b32 v40, 16, v44
Mvaa 180,148,188
s_lshl_b32 s92, s14, 3
s_lshr_b32 s92, s71, s92
s_mov_b32 s91, 134217728
Mvaa 188,148,204
s_bitcmp1_b32 s71, 0
s_cselect_b32 s94, s78, s91
v_and_b32 v41, 0xffff0000, v44
Mvaa 188,144,200
W 6
Ta 140,21,41024
Ta 142,21,46144
Mvaa 188,152,208
v_lshlrev_b32 v42, 16, v45
v_and_b32 v43, 0xffff0000, v45
Mvaa 188,156,212
Ta 144,21,51264
Ta 146,21,56384
Mvaa 180,156,196
buffer_load_dwordx4 a[0:3], v1, s[8:11], s94 offen offset:0 sc1
v_lshlrev_b32 v44, 16, v46
Mvaa 180,152,192
W 6
Ta 148,23,10304
v_and_b32 v45, 0xffff0000, v46
Mvaa 192,128,96
Ta 150,23,15424
v_lshlrev_b32 v46, 16, v47
Mvaa 192,132,100
Ta 152,23,30784
v_and_b32 v47, 0xffff0000, v47
Mvaa 200,132,116
Ta 154,23,35904
v_lshlrev_b32 v48, 16, v52
Mvaa 200,128,112
W 6
Ta 156,23,41024
v_and_b32 v49, 0xffff0000, v52
Mvaa 200,136,120
Ta 158,23,46144
v_lshlrev_b32 v50, 16, v53
Mvaa 200,140,124
K 176,176,177
v_and_b32 v51, 0xffff0000, v53
Mvaa 192,140,108
Ta 128,24,20576
Ta 130,24,25696
Mvaa 192,136,104
W 6
K 177,178,179
v_lshlrev_b32 v52, 16, v54
Mvaa 192,144,184
s_bitcmp1_b32 s92, 0
s_cbranch_scc0 .LL_92%=
buffer_load_dwordx4 a[216:219], v1, s[8:11], s65 offen offset:0 sc1
.LL_92%=:
v_and_b32 v53, 0xffff0000, v54
Mvaa 192,148,188
s_bitcmp1_b32 s71, 1
s_cselect_b32 s94, s78, s91
K 178,180,181
Mvaa 200,148,204
Ta 132,24,51296
Ta 134,24,56416
Mvaa 200,144,200
W 4
v_lshlrev_b32 v54, 16, v55
K 179,182,183
Mvaa 200,152,208
s_add_u32 s83, s52, 327680
Ta 136,26,96
v_and_b32 v55, 0xffff0000, v55
Mvaa 200,156,212
Ta 138,26,5216
v_lshlrev_b32 v56, 16, v60
Mvaa 192,156,196
buffer_store_dwordx4 v[176:179], v1, s[8:11], s83 offen sc1
K 184,184,185
K 185,186,187
K 186,188,189
K 187,190,191
s_add_u32 s83, s52, 368640
buffer_store_dwordx4 v[184:187], v1, s[8:11], s83 offen sc1
Mvaa 192,152,192
Ta 140,22,41056
Ta 142,22,46176
W 4
Mvaa 196,128,96
v_and_b32 v57, 0xffff0000, v60
v_lshlrev_b32 v58, 16, v61
Mvaa 196,132,100
Ta 144,22,51296
v_and_b32 v59, 0xffff0000, v61
Mvaa 204,132,116
Ta 146,22,56416
v_lshlrev_b32 v60, 16, v62
Mvaa 204,128,112
Ta 148,24,10336
Ta 150,24,15456
W 4
Mvaa 204,136,120
buffer_load_dwordx4 a[4:7], v1, s[8:11], s94 offen offset:1024 sc1
v_and_b32 v61, 0xffff0000, v62
Mvaa 204,140,124
v_lshlrev_b32 v62, 16, v63
v_and_b32 v63, 0xffff0000, v63
Mvaa 196,140,108
Ta 152,24,30816
Ta 154,24,35936
Mvaa 196,136,104
Ta 156,24,41056
Ta 158,24,46176
W 4
Mvaa 196,144,184
v_lshlrev_b32 v64, 16, v68
v_and_b32 v65, 0xffff0000, v68
Mvaa 196,148,188
v_lshlrev_b32 v66, 16, v69
v_and_b32 v67, 0xffff0000, v69
Mvaa 204,148,204
Ta 128,23,20608
Ta 130,23,25728
Mvaa 204,144,200
Ta 132,23,51328
Ta 134,23,56448
W 4
Mvaa 204,152,208
v_lshlrev_b32 v68, 16, v70
v_and_b32 v69, 0xffff0000, v70
Mvaa 204,156,212
s_bitcmp1_b32 s92, 1
s_cbranch_scc0 .LL_93%=
buffer_load_dwordx4 a[220:223], v1, s[8:11], s65 offen offset:1024 sc1
.LL_93%=:
)"
R"(v_lshlrev_b32 v70, 16, v71
Mvaa 196,156,196
s_bitcmp1_b32 s71, 2
s_cselect_b32 s94, s78, s91
v_and_b32 v71, 0xffff0000, v71
Mvaa 196,152,192
Ta 136,25,128
Ta 138,25,5248
Ta 140,21,41088
Ta 142,21,46208
W 4
Mvaa 208,128,96
v_lshlrev_b32 v72, 16, v76
v_and_b32 v73, 0xffff0000, v76
Mvaa 208,132,100
v_lshlrev_b32 v74, 16, v77
v_and_b32 v75, 0xffff0000, v77
Mvaa 216,132,116
Ta 144,21,51328
Ta 146,21,56448
Mvaa 216,128,112
Ta 148,23,10368
Ta 150,23,15488
W 4
Mvaa 216,136,120
v_lshlrev_b32 v76, 16, v78
v_and_b32 v77, 0xffff0000, v78
Mvaa 216,140,124
buffer_load_dwordx4 a[8:11], v1, s[8:11], s94 offen offset:2048 sc1
v_lshlrev_b32 v78, 16, v79
Mvaa 208,140,108
Ta 152,23,30848
v_and_b32 v79, 0xffff0000, v79
Mvaa 208,136,104
Ta 154,23,35968
Ta 156,23,41088
Ta 158,23,46208
W 4
Mvaa 208,144,184
Ta 128,24,20640
K 192,192,193
Mvaa 208,148,188
Ta 130,24,25760
K 193,194,195
Mvaa 216,148,204
Ta 132,24,51360
K 194,196,197
Mvaa 216,144,200
Ta 134,24,56480
W 4
Mvaa 216,152,208
s_bitcmp1_b32 s92, 2
s_cbranch_scc0 .LL_94%=
buffer_load_dwordx4 a[224:227], v1, s[8:11], s65 offen offset:2048 sc1
.LL_94%=:
K 195,198,199
Mvaa 216,156,212
s_bitcmp1_b32 s71, 3
s_cselect_b32 s94, s78, s91
Ta 136,26,160
Mvaa 208,156,196
s_add_u32 s83, s52, 335872
buffer_store_dwordx4 v[192:195], v1, s[8:11], s83 offen sc1
K 200,200,201
K 201,202,203
K 202,204,205
K 203,206,207
s_add_u32 s83, s52, 376832
buffer_store_dwordx4 v[200:203], v1, s[8:11], s83 offen sc1
Mvaa 208,152,192
Ta 138,26,5280
Ta 140,22,41120
Ta 142,22,46240
W 4
Mvaa 212,128,96
Ta 144,22,51360
Ta 146,22,56480
Mvaa 212,132,100
Ta 148,24,10400
Ta 150,24,15520
Mvaa 220,132,116
buffer_load_dwordx4 a[12:15], v1, s[8:11], s94 offen offset:3072 sc1
Ta 152,24,30880
Mvaa 220,128,112
W 5
Ta 154,24,36000
Ta 156,24,41120
Mvaa 220,136,120
Ta 158,24,46240
Ta 128,23,20672
Mvaa 220,140,124
Ta 130,23,25792
Ta 132,23,51392
Mvaa 212,140,108
s_bitcmp1_b32 s92, 3
s_cbranch_scc0 .LL_95%=
buffer_load_dwordx4 a[228:231], v1, s[8:11], s65 offen offset:3072 sc1
.LL_95%=:
Ta 134,23,56512
Mvaa 212,136,104
W 8
s_bitcmp1_b32 s71, 4
s_cselect_b32 s94, s78, s91
Mvaa 212,144,184
Ta 136,25,192
Ta 138,25,5312
Mvaa 212,148,188
Ta 140,21,41152
Ta 142,21,46272
Mvaa 220,148,204
Mvaa 220,144,200
W 8
Ta 144,21,51392
Ta 146,21,56512
Mvaa 220,152,208
Mvaa 220,156,212
Ta 148,23,10432
Ta 150,23,15552
Mvaa 212,156,196
Mvaa 212,152,192
W 8
buffer_load_dwordx4 a[16:19], v19, s[8:11], s94 offen offset:0 sc1
Ta 152,23,30912
Mvaa 224,128,96
Ta 154,23,36032
Mvaa 224,132,100
Ta 156,23,41152
Ta 158,23,46272
Mvaa 232,132,116
K 208,208,209
Mvaa 232,128,112
W 8
Ta 128,24,20704
Ta 130,24,25824
Mvaa 232,136,120
K 209,210,211
Mvaa 232,140,124
Ta 132,24,51424
K 210,212,213
Mvaa 224,140,108
Ta 134,24,56544
K 211,214,215
Mvaa 224,136,104
W 8
s_bitcmp1_b32 s92, 4
s_cbranch_scc0 .LL_96%=
buffer_load_dwordx4 a[232:235], v19, s[8:11], s65 offen offset:0 sc1
.LL_96%=:
Mvaa 224,144,184
s_bitcmp1_b32 s71, 5
s_cselect_b32 s94, s78, s91
Ta 136,26,224
Mvaa 224,148,188
s_add_u32 s83, s52, 344064
Ta 138,26,5344
Ta 140,22,41184
Mvaa 232,148,204
Ta 142,22,46304
Mvaa 232,144,200
W 8
buffer_store_dwordx4 v[208:211], v1, s[8:11], s83 offen sc1
K 216,216,217
Mvaa 232,152,208
K 217,218,219
K 218,220,221
Mvaa 232,156,212
Ta 144,22,51424
K 219,222,223
Mvaa 224,156,196
s_add_u32 s83, s52, 385024
buffer_store_dwordx4 v[216:219], v1, s[8:11], s83 offen sc1
Ta 146,22,56544
Mvaa 224,152,192
W 6
Ta 148,24,10464
Ta 150,24,15584
Mvaa 228,128,96
Ta 152,24,30944
Ta 154,24,36064
Mvaa 228,132,100
Ta 156,24,41184
Ta 158,24,46304
Mvaa 236,132,116
buffer_load_dwordx4 a[20:23], v19, s[8:11], s94 offen offset:1024 sc1
Mvaa 236,128,112
W 8
Ta 128,23,20736
Ta 130,23,25856
Mvaa 236,136,120
Mvaa 236,140,124
Ta 132,23,51456
Ta 134,23,56576
Mvaa 228,140,108
Mvaa 228,136,104
W 8
s_bitcmp1_b32 s92, 5
s_cbranch_scc0 .LL_97%=
buffer_load_dwordx4 a[236:239], v19, s[8:11], s65 offen offset:1024 sc1
.LL_97%=:
Mvaa 228,144,184
s_bitcmp1_b32 s71, 6
s_cselect_b32 s94, s78, s91
Ta 136,25,256
Mvaa 228,148,188
Ta 138,25,5376
Ta 140,21,41216
Mvaa 236,148,204
Ta 142,21,46336
Mvaa 236,144,200
W 8
Ta 144,21,51456
Ta 146,21,56576
Mvaa 236,152,208
Mvaa 236,156,212
Ta 148,23,10496
Ta 150,23,15616
Mvaa 228,156,196
Mvaa 228,152,192
W 8
Ta 152,23,30976
Ta 154,23,36096
Mvaa 240,128,96
buffer_load_dwordx4 a[24:27], v19, s[8:11], s94 offen offset:2048 sc1
Mvaa 240,132,100
Ta 156,23,41216
Ta 158,23,46336
Mvaa 248,132,116
K 224,224,225
Mvaa 248,128,112
W 8
Ta 128,24,20768
Ta 130,24,25888
Mvaa 248,136,120
K 225,226,227
Mvaa 248,140,124
Ta 132,24,51488
K 226,228,229
Mvaa 240,140,108
Ta 134,24,56608
K 227,230,231
Mvaa 240,136,104
W 8
Ta 136,26,288
Ta 138,26,5408
Mvaa 240,144,184
s_add_u32 s83, s52, 352256
s_bitcmp1_b32 s92, 6
s_cbranch_scc0 .LL_98%=
buffer_load_dwordx4 a[240:243], v19, s[8:11], s65 offen offset:2048 sc1
.LL_98%=:
Mvaa 240,148,188
s_bitcmp1_b32 s71, 7
s_cselect_b32 s94, s78, s91
Ta 140,22,41248
Mvaa 248,148,204
Ta 142,22,46368
Mvaa 248,144,200
W 8
Ta 144,22,51488
Ta 146,22,56608
Mvaa 248,152,208
buffer_store_dwordx4 v[224:227], v1, s[8:11], s83 offen sc1
K 232,232,233
Mvaa 248,156,212
K 233,234,235
K 234,236,237
Mvaa 240,156,196
K 235,238,239
s_add_u32 s83, s52, 393216
buffer_store_dwordx4 v[232:235], v1, s[8:11], s83 offen sc1
Mvaa 240,152,192
W 6
Ta 148,24,10528
Ta 150,24,15648
Mvaa 244,128,96
Ta 152,24,31008
Ta 154,24,36128
Mvaa 244,132,100
Ta 156,24,41248
Ta 158,24,46368
Mvaa 252,132,116
buffer_load_dwordx4 a[28:31], v19, s[8:11], s94 offen offset:3072 sc1
Mvaa 252,128,112
W 8
Mvaa 252,136,120
Mvaa 252,140,124
Mvaa 244,140,108
Mvaa 244,136,104
W 4
Mvaa 244,144,184
s_bitcmp1_b32 s92, 7
s_cbranch_scc0 .LL_99%=
buffer_load_dwordx4 a[244:247], v19, s[8:11], s65 offen offset:3072 sc1
.LL_99%=:
Mvaa 244,148,188
Mvaa 252,148,204
Mvaa 252,144,200
W 0
Mvaa 252,152,208
Mvaa 252,156,212
Mvaa 244,156,196
Mvaa 244,152,192
K 240,240,241
K 241,242,243
s_nop 5
K 242,244,245
K 243,246,247
s_add_u32 s83, s52, 360448
buffer_store_dwordx4 v[240:243], v1, s[8:11], s83 offen sc1
K 248,248,249
K 249,250,251
K 250,252,253
K 251,254,255
s_add_u32 s83, s52, 401408
buffer_store_dwordx4 v[248:251], v1, s[8:11], s83 offen sc1
s_branch .Ltend_83%=
.p2align 12
.Llate_82%=:
ds_read_b32 v11, v11 offset:59392
ds_read_b64 v[8:9], v10 offset:57344
Rv 176,3,0
Rv 180,3,1024
Rv 184,3,2048
Rv 188,3,3072
Rv 192,3,4096
Rv 196,3,5120
Rv 200,3,6144
Rv 204,3,7168
W 6
Mvvv 32,224,176
s_lshl_b32 s92, s58, 2
s_add_u32 s91, s92, s51
s_add_u32 s95, s91, 7
Mvvv 36,228,176
s_and_b32 s95, s95, 15
s_mul_i32 s95, s95, 40960
s_mul_i32 s71, s51, 20480
Mvvv 40,224,180
s_add_u32 s71, s71, 61440
s_add_u32 m0, s71, s48
s_lshl2_add_u32 s94, s48, s95
Mvvv 44,228,180
W 4
v_cmp_ne_u32 vcc, 0, v11
s_cmp_lg_u64 vcc, 0
s_cbranch_scc1 .Lredo%=
A 4,4,8
Mvvv 48,224,184
v_rcp_f32 v4, v4
Mvvv 52,228,184
Ta 128,25,10240
A 5,5,9
Mvvv 56,224,188
Ta 130,25,15360
D 16,40,94,0
Mvvv 60,228,188
W 4
s_add_u32 s95, s91, 9
s_and_b32 s95, s95, 15
Mvvv 64,224,192
s_mul_i32 s95, s95, 40960
s_mul_i32 s71, s51, 10240
s_add_u32 s71, s71, 112640
Mvvv 68,228,192
v_rcp_f32 v5, v5
Mvvv 72,224,196
s_lshl_b32 s93, s58, 12
s_add_u32 s93, s93, s50
Ta 132,25,20480
Mvvv 76,228,196
Ta 134,25,25600
W 4
Mvvv 80,224,200
U 32,32,4
U 33,33,4
Mvvv 84,228,200
Ta 136,26,10272
U 34,34,4
Mvvv 88,224,204
U 35,35,4
U 36,36,4
U 37,37,4
U 38,38,4
U 39,39,4
K 32,32,33
K 33,34,35
K 34,36,37
K 35,38,39
buffer_store_dwordx4 v[32:35], v1, s[8:11], s93 offen offset:0 sc1
U 40,40,4
U 41,41,4
U 42,42,4
U 43,43,4
U 44,44,4
U 45,45,4
U 46,46,4
U 47,47,4
K 40,40,41
K 41,42,43
K 42,44,45
)"
R"(K 43,46,47
buffer_store_dwordx4 v[40:43], v1, s[8:11], s93 offen offset:1024 sc1
U 64,64,5
U 65,65,5
U 66,66,5
U 67,67,5
U 68,68,5
U 69,69,5
U 70,70,5
U 71,71,5
K 64,64,65
K 65,66,67
K 66,68,69
K 67,70,71
s_add_u32 s67, s93, 131072
buffer_store_dwordx4 v[64:67], v1, s[8:11], s67 offen offset:0 sc1
U 72,72,5
U 73,73,5
U 74,74,5
U 75,75,5
U 76,76,5
U 77,77,5
U 78,78,5
U 79,79,5
K 72,72,73
K 73,74,75
K 74,76,77
K 75,78,79
Mvvv 92,228,204
Ta 138,26,15392
Ta 140,26,20512
Ta 142,26,25632
W 4
Zvav 176,128,32
Ta 144,25,10304
Ta 146,25,15424
Mvav 176,132,40
Ta 148,25,20544
Ta 150,25,25664
Zvav 184,132,72
D 17,40,94,1024
Ta 152,26,10336
Mvav 184,128,64
W 5
Ta 154,26,15456
Ta 156,26,20576
Zvav 188,136,64
Ta 158,26,25696
Ta 128,25,10368
Mvav 188,140,72
Ta 130,25,15488
Ta 132,25,20608
Zvav 180,140,40
D 18,40,94,2048
Ta 134,25,25728
Mvav 180,136,32
W 8
Ta 136,26,10400
Ta 138,26,15520
Zvav 192,144,32
Mvav 192,148,40
Ta 140,26,20640
Ta 142,26,25760
Zvav 200,148,72
D 19,40,94,3072
Mvav 200,144,64
W 8
s_add_u32 m0, m0, 4096
Ta 144,25,10432
Zvav 204,152,64
Ta 146,25,15552
Mvav 204,156,72
Ta 148,25,20672
Ta 150,25,25792
Zvav 196,156,40
buffer_load_dwordx4 v20, s[40:43], s94 offen lds
Mvav 196,152,32
W 8
s_add_u32 m0, s71, s48
s_lshl2_add_u32 s94, s48, s95
Zvav 208,128,32
Ta 152,26,10464
Ta 154,26,15584
Mvav 208,132,40
Ta 156,26,20704
Ta 158,26,25824
Zvav 216,132,72
D 16,40,94,0
v_bfe_u32 v28, v1, 4, 2
Mvav 216,128,64
W 8
Ta 128,25,10496
v_lshlrev_b32 v28, 3, v28
Zvav 220,136,64
Ta 130,25,15616
Mvav 220,140,72
Ta 132,25,20736
Ta 134,25,25856
Zvav 212,140,40
buffer_load_dwordx2 v[28:29], v28, s[8:11], s54 offen sc1
Mvav 212,136,32
W 8
Ta 136,26,10528
Ta 138,26,15648
Zvav 224,144,32
Mvav 224,148,40
Ta 140,26,20768
Ta 142,26,25888
Zvav 232,148,72
D 17,40,94,1024
Mvav 232,144,64
W 8
Ta 144,25,30720
Ta 146,25,35840
Zvav 236,152,64
Mvav 236,156,72
Ta 148,21,0
Ta 150,21,5120
Zvav 228,156,40
D 18,40,94,2048
Mvav 228,152,32
W 8
Ta 152,26,30752
Ta 154,26,35872
Zvav 240,128,32
Mvav 240,132,40
Ta 156,22,32
Ta 158,22,5152
Zvav 248,132,72
D 19,40,94,3072
Mvav 248,128,64
W 8
s_add_u32 m0, m0, 4096
Ta 128,25,30784
Zvav 252,136,64
Ta 130,25,35904
Mvav 252,140,72
Ta 132,21,64
Ta 134,21,5184
Zvav 244,140,40
buffer_store_dwordx4 v[72:75], v1, s[8:11], s67 offen offset:1024 sc1
U 48,48,4
U 49,49,4
U 50,50,4
U 51,51,4
U 52,52,4
U 53,53,4
U 54,54,4
U 55,55,4
K 48,48,49
K 49,50,51
K 50,52,53
K 51,54,55
buffer_store_dwordx4 v[48:51], v1, s[8:11], s93 offen offset:2048 sc1
U 56,56,4
U 57,57,4
U 58,58,4
U 59,59,4
U 60,60,4
U 61,61,4
U 62,62,4
U 63,63,4
K 56,56,57
K 57,58,59
K 58,60,61
K 59,62,63
buffer_store_dwordx4 v[56:59], v1, s[8:11], s93 offen offset:3072 sc1
U 80,80,5
U 81,81,5
U 82,82,5
U 83,83,5
U 84,84,5
U 85,85,5
U 86,86,5
U 87,87,5
K 80,80,81
K 81,82,83
K 82,84,85
K 83,86,87
buffer_store_dwordx4 v[80:83], v1, s[8:11], s67 offen offset:2048 sc1
U 88,88,5
U 89,89,5
U 90,90,5
U 91,91,5
U 92,92,5
U 93,93,5
U 94,94,5
U 95,95,5
K 88,88,89
K 89,90,91
K 90,92,93
K 91,94,95
Mvav 244,136,32
W 8
Ta 136,26,30816
Ta 138,26,35936
Mvav 176,144,48
Mvav 176,148,56
Ta 140,22,96
Ta 142,22,5216
Mvav 184,148,88
buffer_load_dwordx4 v20, s[40:43], s94 offen lds
Mvav 184,144,80
s_waitcnt vmcnt(8)
v_cmp_eq_u64 vcc, s[12:13], v[28:29]
s_cmp_eq_u64 vcc, -1
s_cbranch_scc0 .Lspin_100%=
.Lok_101%=:
s_add_u32 s81, s58, 0
W 8
Mvav 188,152,80
s_and_b32 s81, s81, 3
s_lshl_b32 s81, s81, 12
s_add_u32 s81, s81, s49
Mvav 188,156,88
Ta 144,25,30848
Ta 146,25,35968
Mvav 180,156,56
buffer_load_dwordx4 a[184:187], v1, s[8:11], s81 offen offset:0 sc1
Ta 148,21,128
Mvav 180,152,48
W 7
s_add_u32 s82, s81, 131072
Ta 150,21,5248
Mvav 192,128,48
Ta 152,26,30880
Ta 154,26,36000
Mvav 192,132,56
Ta 156,22,160
Ta 158,22,5280
Mvav 200,132,88
buffer_load_dwordx4 a[200:203], v1, s[8:11], s82 offen offset:0 sc1
Mvav 200,128,80
W 8
Ta 128,25,30912
Ta 130,25,36032
Mvav 204,136,80
Mvav 204,140,88
Ta 132,21,192
Ta 134,21,5312
Mvav 196,140,56
buffer_load_dwordx4 a[188:191], v1, s[8:11], s81 offen offset:1024 sc1
Mvav 196,136,48
W 8
Ta 136,26,30944
Ta 138,26,36064
Mvav 208,144,48
Mvav 208,148,56
Ta 140,22,224
Ta 142,22,5344
Mvav 216,148,88
buffer_load_dwordx4 a[204:207], v1, s[8:11], s82 offen offset:1024 sc1
Mvav 216,144,80
W 8
Ta 144,25,30976
Ta 146,25,36096
Mvav 220,152,80
Mvav 220,156,88
Ta 148,21,256
Ta 150,21,5376
Mvav 212,156,56
buffer_load_dwordx4 a[192:195], v1, s[8:11], s81 offen offset:2048 sc1
Mvav 212,152,48
W 8
Ta 152,26,31008
Ta 154,26,36128
Mvav 224,128,48
Mvav 224,132,56
Ta 156,22,288
Ta 158,22,5408
Mvav 232,132,88
buffer_store_dwordx4 v[88:91], v1, s[8:11], s67 offen offset:3072 sc1
Mvav 232,128,80
W 8
Ta 128,25,10240
Ta 130,25,15360
Mvav 236,136,80
Mvav 236,140,88
Ta 132,25,20480
Ta 134,25,25600
Mvav 228,140,56
buffer_load_dwordx4 a[208:211], v1, s[8:11], s82 offen offset:2048 sc1
Mvav 228,136,48
W 8
Ta 136,26,10272
Ta 138,26,15392
Mvav 240,144,48
Mvav 240,148,56
Ta 140,26,20512
Ta 142,26,25632
Mvav 248,148,88
buffer_load_dwordx4 a[196:199], v1, s[8:11], s81 offen offset:3072 sc1
Mvav 248,144,80
W 8
Ta 144,25,10304
Ta 146,25,15424
Mvav 252,152,80
Mvav 252,156,88
Ta 148,25,20544
Ta 150,25,25664
Mvav 244,156,56
buffer_load_dwordx4 a[212:215], v1, s[8:11], s82 offen offset:3072 sc1
Mvav 244,152,48
s_waitcnt vmcnt(5) lgkmcnt(8)
Ta 152,26,10336
Ta 154,26,15456
Zvaa 96,128,184
Mvaa 96,132,188
Ta 156,26,20576
Ta 158,26,25696
Zvaa 104,132,204
Mvaa 104,128,200
W 8
Ta 128,25,10368
Ta 130,25,15488
Zvaa 108,136,200
Mvaa 108,140,204
Ta 132,25,20608
Ta 134,25,25728
Zvaa 100,140,188
s_lshl_b32 s92, s58, 2
s_add_u32 s91, s92, s51
s_add_u32 s95, s91, 11
Mvaa 100,136,184
W 8
s_and_b32 s95, s95, 15
s_mul_i32 s95, s95, 40960
Zvaa 112,144,184
s_mul_i32 s71, s51, 10240
s_add_u32 s71, s71, 40960
s_add_u32 m0, s71, s48
Mvaa 112,148,188
s_lshl2_add_u32 s94, s48, s95
Ta 136,26,10400
Ta 138,26,15520
Zvaa 120,148,204
Ta 140,26,20640
Ta 142,26,25760
Mvaa 120,144,200
W 8
Ta 144,25,10432
Ta 146,25,15552
Zvaa 124,152,200
Mvaa 124,156,204
Ta 148,25,20672
Ta 150,25,25792
Zvaa 116,156,188
Mvaa 116,152,184
W 8
Ta 152,26,10464
Ta 154,26,15584
Zvaa 128,128,184
s_lshl_b32 s80, s58, 3
s_add_u32 s80, s80, s55
s_waitcnt vmcnt(3)
v_mov_b64 v[28:29], s[12:13]
s_mov_b64 exec, 1
buffer_store_dwordx2 v[28:29], off, s[8:11], s80 sc1
s_mov_b64 exec, -1
Mvaa 128,132,188
Ta 156,26,20704
Ta 158,26,25824
Zvaa 136,132,204
Mvaa 136,128,200
W 8
Ta 128,25,10496
Ta 130,25,15616
Zvaa 140,136,200
s_barrier
D 16,40,94,0
Mvaa 140,140,204
s_add_u32 s95, s91, 13
s_and_b32 s95, s95, 15
s_mul_i32 s95, s95, 40960
Zvaa 132,140,188
s_mul_i32 s71, s51, 20480
s_add_u32 s71, s71, 71680
Ta 132,25,20736
Mvaa 132,136,184
W 7
Ta 134,25,25856
Ta 136,26,10528
Zvaa 144,144,184
Ta 138,26,15648
D 17,40,94,1024
Mvaa 144,148,188
Ta 140,26,20768
Ta 142,26,25888
Zvaa 152,148,204
Mvaa 152,144,200
W 8
Ta 144,25,30720
Ta 146,25,35840
Zvaa 156,152,200
D 18,40,94,2048
Mvaa 156,156,204
Ta 148,21,0
Ta 150,21,5120
Zvaa 148,156,188
Mvaa 148,152,184
)"
R"(W 8
Ta 152,26,30752
Ta 154,26,35872
Zvaa 160,128,184
D 19,40,94,3072
Mvaa 160,132,188
s_add_u32 m0, m0, 4096
Ta 156,22,32
Ta 158,22,5152
Zvaa 168,132,204
Mvaa 168,128,200
W 8
Ta 128,25,30784
Ta 130,25,35904
Zvaa 172,136,200
buffer_load_dwordx4 v20, s[40:43], s94 offen lds
Mvaa 172,140,204
s_add_u32 s81, s58, 1
s_and_b32 s81, s81, 3
s_lshl_b32 s81, s81, 12
Zvaa 164,140,188
s_add_u32 s81, s81, s49
Ta 132,21,64
Ta 134,21,5184
Mvaa 164,136,184
s_waitcnt vmcnt(6) lgkmcnt(8)
Ta 136,26,30816
Ta 138,26,35936
Mvaa 96,144,192
buffer_load_dwordx4 a[216:219], v1, s[8:11], s81 offen offset:0 sc1
Mvaa 96,148,196
s_add_u32 s82, s81, 131072
Ta 140,22,96
Ta 142,22,5216
Mvaa 104,148,212
Mvaa 104,144,208
W 8
Ta 144,25,30848
Ta 146,25,35968
Mvaa 108,152,208
buffer_load_dwordx4 a[232:235], v1, s[8:11], s82 offen offset:0 sc1
Mvaa 108,156,212
Ta 148,21,128
Ta 150,21,5248
Mvaa 100,156,196
Mvaa 100,152,192
W 8
Ta 152,26,30880
Ta 154,26,36000
Mvaa 112,128,192
buffer_load_dwordx4 a[220:223], v1, s[8:11], s81 offen offset:1024 sc1
Mvaa 112,132,196
Ta 156,22,160
Ta 158,22,5280
Mvaa 120,132,212
Mvaa 120,128,208
W 8
Ta 128,25,30912
Ta 130,25,36032
Mvaa 124,136,208
buffer_load_dwordx4 a[236:239], v1, s[8:11], s82 offen offset:1024 sc1
Mvaa 124,140,212
Ta 132,21,192
Ta 134,21,5312
Mvaa 116,140,196
Mvaa 116,136,192
W 8
Ta 136,26,30944
Ta 138,26,36064
Mvaa 128,144,192
buffer_load_dwordx4 a[224:227], v1, s[8:11], s81 offen offset:2048 sc1
Mvaa 128,148,196
Ta 140,22,224
Ta 142,22,5344
Mvaa 136,148,212
Mvaa 136,144,208
W 8
Ta 144,25,30976
Ta 146,25,36096
Mvaa 140,152,208
buffer_load_dwordx4 a[240:243], v1, s[8:11], s82 offen offset:2048 sc1
Mvaa 140,156,212
Ta 148,21,256
Ta 150,21,5376
Mvaa 132,156,196
Mvaa 132,152,192
W 8
Ta 152,26,31008
Ta 154,26,36128
Mvaa 144,128,192
buffer_load_dwordx4 a[228:231], v1, s[8:11], s81 offen offset:3072 sc1
Mvaa 144,132,196
Ta 156,22,288
Ta 158,22,5408
Mvaa 152,132,212
Mvaa 152,128,208
W 8
Ta 128,21,10240
Ta 130,21,15360
Mvaa 156,136,208
buffer_load_dwordx4 a[244:247], v1, s[8:11], s82 offen offset:3072 sc1
Mvaa 156,140,212
s_add_u32 m0, s71, s48
s_lshl2_add_u32 s94, s48, s95
Ta 132,21,20480
Mvaa 148,140,196
Ta 134,21,25600
Mvaa 148,136,192
W 8
Ta 136,22,10272
Ta 138,22,15392
Mvaa 160,144,192
D 16,40,94,0
Mvaa 160,148,196
s_add_u32 s95, s92, 15
s_and_b32 s95, s95, 15
s_mul_i32 s95, s95, 40960
Mvaa 168,148,212
s_mul_i32 s71, s51, 0
s_add_u32 s71, s71, 102400
Ta 140,22,20512
Mvaa 168,144,208
W 7
Ta 142,22,25632
Ta 144,21,10304
Mvaa 172,152,208
D 17,40,94,1024
Ta 146,21,15424
Mvaa 172,156,212
Ta 148,21,20544
Ta 150,21,25664
Mvaa 164,156,196
Mvaa 164,152,192
s_waitcnt vmcnt(6) lgkmcnt(8)
Ta 152,22,10336
Ta 154,22,15456
Mvaa 96,128,216
D 18,40,94,2048
Mvaa 96,132,220
Ta 156,22,20576
Ta 158,22,25696
Mvaa 104,132,236
Mvaa 104,128,232
W 8
Ta 128,21,10368
Ta 130,21,15488
Mvaa 108,136,232
D 19,40,94,3072
Mvaa 108,140,236
s_add_u32 m0, m0, 4096
Ta 132,21,20608
Ta 134,21,25728
Mvaa 100,140,220
Mvaa 100,136,216
W 8
Ta 136,22,10400
Ta 138,22,15520
Mvaa 112,144,216
buffer_load_dwordx4 v20, s[40:43], s94 offen lds
Mvaa 112,148,220
s_add_u32 m0, s71, s48
s_lshl2_add_u32 s94, s48, s95
Ta 140,22,20640
Mvaa 120,148,236
Ta 142,22,25760
Mvaa 120,144,232
W 8
Ta 144,21,10432
Ta 146,21,15552
Mvaa 124,152,232
D 16,40,94,0
Mvaa 124,156,236
Ta 148,21,20672
Ta 150,21,25792
Mvaa 116,156,220
D 17,40,94,1024
D 18,40,94,2048
D 19,40,94,3072
s_add_u32 m0, m0, 4096
s_nop 0
buffer_load_dwordx4 v20, s[40:43], s94 offen lds
Mvaa 116,152,216
W 8
s_add_u32 s81, s58, 2
s_and_b32 s81, s81, 3
Mvaa 128,128,216
s_lshl_b32 s81, s81, 12
s_add_u32 s81, s81, s49
Ta 152,22,10464
Mvaa 128,132,220
Ta 154,22,15584
Ta 156,22,20704
Mvaa 136,132,236
Ta 158,22,25824
Mvaa 136,128,232
W 8
Ta 128,21,10496
Ta 130,21,15616
Mvaa 140,136,232
s_barrier
buffer_load_dwordx4 a[0:3], v1, s[8:11], s81 offen offset:0 sc1
v_and_b32 v27, 1, v12
Mvaa 140,140,236
s_add_u32 s82, s81, 131072
Ta 132,21,20736
Ta 134,21,25856
Mvaa 132,140,220
Mvaa 132,136,216
W 8
Ta 136,22,10528
v_lshlrev_b32 v27, 1, v27
Mvaa 144,144,216
Ta 138,22,15648
buffer_load_dwordx4 a[16:19], v1, s[8:11], s82 offen offset:0 sc1
Mvaa 144,148,220
Ta 140,22,20768
v_xor_b32 v27, v27, v13
Mvaa 152,148,236
Ta 142,22,25888
Mvaa 152,144,232
W 8
Ta 144,21,30720
Ta 146,21,35840
Mvaa 156,152,232
buffer_load_dwordx4 a[4:7], v1, s[8:11], s81 offen offset:1024 sc1
v_and_b32 v12, 2, v12
Mvaa 156,156,236
Ta 148,23,0
Ta 150,23,5120
Mvaa 148,156,220
v_or_b32 v12, v12, v14
Mvaa 148,152,216
W 8
Ta 152,22,30752
Ta 154,22,35872
Mvaa 160,128,216
buffer_load_dwordx4 a[20:23], v1, s[8:11], s82 offen offset:1024 sc1
Mvaa 160,132,220
Ta 156,24,32
v_lshl_or_b32 v12, v27, 2, v12
Mvaa 168,132,236
Ta 158,24,5152
Mvaa 168,128,232
W 8
Ta 128,21,30784
v_cmp_eq_u32_e64 s[86:87], 0, v12
Mvaa 172,136,232
Ta 130,21,35904
buffer_load_dwordx4 a[8:11], v1, s[8:11], s81 offen offset:2048 sc1
Mvaa 172,140,236
Ta 132,23,64
Ta 134,23,5184
Mvaa 164,140,220
v_cndmask_b32_e64 v4, 0, v15, s[86:87]
Mvaa 164,136,216
s_waitcnt vmcnt(15) lgkmcnt(8)
Ta 136,22,30816
Ta 138,22,35936
Mvaa 96,144,224
buffer_load_dwordx4 a[24:27], v1, s[8:11], s82 offen offset:2048 sc1
v_cmp_eq_u32_e64 s[86:87], 1, v12
Mvaa 96,148,228
Ta 140,24,96
Ta 142,24,5216
Mvaa 104,148,244
Mvaa 104,144,240
W 8
Ta 144,21,30848
v_cndmask_b32_e64 v5, 0, v15, s[86:87]
Mvaa 108,152,240
Ta 146,21,35968
buffer_load_dwordx4 a[12:15], v1, s[8:11], s81 offen offset:3072 sc1
Mvaa 108,156,244
Ta 148,23,128
v_cmp_eq_u32_e64 s[86:87], 2, v12
Mvaa 100,156,228
Ta 150,23,5248
Mvaa 100,152,224
W 8
Ta 152,22,30880
v_cndmask_b32_e64 v6, 0, v15, s[86:87]
Mvaa 112,128,224
Ta 154,22,36000
buffer_load_dwordx4 a[28:31], v1, s[8:11], s82 offen offset:3072 sc1
Mvaa 112,132,228
s_add_u32 s81, s58, 3
s_and_b32 s81, s81, 3
s_lshl_b32 s81, s81, 12
Mvaa 120,132,244
s_add_u32 s81, s81, s49
Ta 156,24,160
Ta 158,24,5280
Mvaa 120,128,240
W 8
Ta 128,21,30912
v_cmp_eq_u32_e64 s[86:87], 3, v12
Mvaa 124,136,240
Ta 130,21,36032
buffer_load_dwordx4 a[32:35], v1, s[8:11], s81 offen offset:0 sc1
Mvaa 124,140,244
s_add_u32 s82, s81, 131072
Ta 132,23,192
v_cndmask_b32_e64 v7, 0, v15, s[86:87]
Mvaa 116,140,228
Ta 134,23,5312
Mvaa 116,136,224
W 8
Ta 136,22,30944
v_cmp_eq_u32_e64 s[86:87], 4, v12
Mvaa 128,144,224
Ta 138,22,36064
buffer_load_dwordx4 a[48:51], v1, s[8:11], s82 offen offset:0 sc1
Mvaa 128,148,228
Ta 140,24,224
v_cndmask_b32_e64 v8, 0, v15, s[86:87]
Mvaa 136,148,244
Ta 142,24,5344
Mvaa 136,144,240
W 8
Ta 144,21,30976
Ta 146,21,36096
Mvaa 140,152,240
s_waitcnt vmcnt(10)
s_barrier
v_cmp_eq_u32_e64 s[86:87], 5, v12
Mvaa 140,156,244
buffer_load_dwordx4 a[36:39], v1, s[8:11], s81 offen offset:1024 sc1
Ta 148,23,256
Mvaa 132,156,228
Ta 150,23,5376
v_cndmask_b32_e64 v9, 0, v15, s[86:87]
Mvaa 132,152,224
W 8
Ta 152,22,31008
Ta 154,22,36128
Mvaa 144,128,224
Mvaa 144,132,228
buffer_load_dwordx4 a[52:55], v1, s[8:11], s82 offen offset:1024 sc1
v_cmp_eq_u32_e64 s[86:87], 6, v12
Mvaa 152,132,244
Ta 156,24,288
Ta 158,24,5408
Mvaa 152,128,240
W 8
Ta 128,23,20480
v_cndmask_b32_e64 v10, 0, v15, s[86:87]
Mvaa 156,136,240
Ta 130,23,25600
Mvaa 156,140,244
buffer_load_dwordx4 a[40:43], v1, s[8:11], s81 offen offset:2048 sc1
v_cmp_eq_u32_e64 s[86:87], 7, v12
Mvaa 148,140,228
Ta 132,23,51200
Ta 134,23,56320
Mvaa 148,136,224
W 8
Ta 136,24,20512
Ta 138,24,25632
Mvaa 160,144,224
v_cndmask_b32_e64 v11, 0, v15, s[86:87]
Mvaa 160,148,228
buffer_load_dwordx4 a[56:59], v1, s[8:11], s82 offen offset:2048 sc1
Ta 140,24,51232
Mvaa 168,148,244
s_mov_b32 s20, s38
Ta 142,24,56352
Mvaa 168,144,240
W 8
Ta 144,23,20544
Ta 146,23,25664
Mvaa 172,152,240
Mvaa 172,156,244
s_and_b32 s21, s39, 0xffff
buffer_load_dwordx4 a[44:47], v1, s[8:11], s81 offen offset:3072 sc1
Ta 148,23,51264
Mvaa 164,156,228
Ta 150,23,56384
Mvaa 164,152,224
s_waitcnt vmcnt(11) lgkmcnt(8)
s_mov_b32 s22, 2621440
Ta 152,24,20576
Mvaa 96,128,0
Ta 154,24,25696
Mvaa 96,132,4
buffer_load_dwordx4 a[60:63], v1, s[8:11], s82 offen offset:3072 sc1
Ta 156,24,51296
Mvaa 104,132,20
s_mov_b32 s23, 0x20000
Ta 158,24,56416
Mvaa 104,128,16
W 8
Ta 128,23,20608
Ta 130,23,25728
Mvaa 108,136,16
s_and_b32 s17, s17, 0xffff
Mvaa 108,140,20
Ta 132,23,51328
Ta 134,23,56448
Mvaa 100,140,4
Mvaa 100,136,0
)"
R"(W 8
s_mov_b32 s18, 2621440
Ta 136,24,20640
Mvaa 112,144,0
Ta 138,24,25760
v_bfe_u32 v28, v1, 4, 2
Mvaa 112,148,4
s_mov_b32 s19, 0x20000
Ta 140,24,51360
v_lshlrev_b32 v28, 3, v28
Mvaa 120,148,20
Ta 142,24,56480
buffer_load_dwordx2 v[28:29], v28, s[8:11], s55 offen sc1
Mvaa 120,144,16
W 8
s_lshr_b32 s66, s15, 3
Ta 144,23,20672
Mvaa 124,152,16
Ta 146,23,25792
Mvaa 124,156,20
Ta 148,23,51392
Ta 150,23,56512
Mvaa 116,156,4
s_lshl_b32 s66, s66, 4
Mvaa 116,152,0
W 8
Ta 152,24,20704
Ta 154,24,25824
Mvaa 128,128,0
s_add_u32 s66, s66, s59
Mvaa 128,132,4
Ta 156,24,51424
Ta 158,24,56544
Mvaa 136,132,20
Mvaa 136,128,16
W 8
s_lshl_b32 s63, s66, 6
Ta 128,23,20736
Mvaa 140,136,16
Ta 130,23,25856
Mvaa 140,140,20
s_add_u32 s63, s63, 65536
Ta 132,23,51456
Ta 134,23,56576
Mvaa 132,140,4
Mvaa 132,136,0
W 8
Ta 136,24,20768
Ta 138,24,25888
Mvaa 144,144,0
s_mul_i32 s62, s66, 40960
Mvaa 144,148,4
Ta 140,24,51488
Ta 142,24,56608
Mvaa 152,148,20
s_add_u32 s62, s62, 56623104
Mvaa 152,144,16
W 8
Ta 144,25,0
Ta 146,25,5120
Mvaa 156,152,16
Mvaa 156,156,20
s_lshl_b32 s66, s14, 13
Ta 148,21,40960
Ta 150,21,46080
Mvaa 148,156,4
Mvaa 148,152,0
W 8
s_add_u32 s78, s62, s66
Ta 152,26,32
Mvaa 160,128,0
Ta 154,26,5152
Mvaa 160,132,4
s_add_u32 s79, s78, 327680
Ta 156,22,40992
Ta 158,22,46112
Mvaa 168,132,20
Mvaa 168,128,16
W 8
Ta 128,25,64
Ta 130,25,5184
Mvaa 172,136,16
s_and_b32 s66, s14, 1
Mvaa 172,140,20
Ta 132,21,41024
Ta 134,21,46144
Mvaa 164,140,4
s_mul_i32 s66, s66, 327680
Mvaa 164,136,0
s_waitcnt vmcnt(9) lgkmcnt(8)
Ta 136,26,96
Ta 138,26,5216
Mvaa 96,144,8
Mvaa 96,148,12
s_add_u32 s65, s62, s66
Ta 140,22,41056
Ta 142,22,46176
Mvaa 104,148,28
Mvaa 104,144,24
W 8
s_add_u32 s65, s65, 32768
Ta 144,25,128
Mvaa 108,152,24
Ta 146,25,5248
Mvaa 108,156,28
Ta 148,21,41088
Ta 150,21,46208
Mvaa 100,156,12
s_mov_b32 s64, 0
Mvaa 100,152,8
W 8
Ta 152,26,160
Ta 154,26,5280
Mvaa 112,128,8
v_add_u32 v19, 4096, v1
Mvaa 112,132,12
Ta 156,22,41120
Ta 158,22,46240
Mvaa 120,132,28
s_waitcnt vmcnt(0)
v_cmp_eq_u64 vcc, s[12:13], v[28:29]
s_cmp_eq_u64 vcc, -1
s_cbranch_scc0 .Lspin_103%=
.Lok_104%=:
s_add_u32 s81, s58, 1
s_and_b32 s81, s81, 3
Mvaa 120,128,24
W 8
s_lshl_b32 s81, s81, 12
s_add_u32 s81, s81, s50
Mvaa 124,136,24
buffer_load_dwordx4 a[64:67], v1, s[8:11], s81 offen offset:0 sc1
v_bfe_u32 v27, v1, 4, 3
Mvaa 124,140,28
s_add_u32 s82, s81, 131072
Ta 128,25,192
Ta 130,25,5312
Mvaa 116,140,12
Ta 132,21,41152
v_lshlrev_b32 v27, 3, v27
Mvaa 116,136,8
W 7
Ta 134,21,46272
Ta 136,26,224
Mvaa 128,144,8
Ta 138,26,5344
v_bfe_u32 v18, v1, 7, 1
Mvaa 128,148,12
buffer_load_dwordx4 a[80:83], v1, s[8:11], s82 offen offset:0 sc1
Ta 140,22,41184
Mvaa 136,148,28
Ta 142,22,46304
v_lshl_add_u32 v18, v18, 9, v27
Mvaa 136,144,24
W 8
Ta 144,25,256
Ta 146,25,5376
Mvaa 140,152,24
s_mov_b32 s71, 0
Mvaa 140,156,28
buffer_load_dwordx4 a[68:71], v1, s[8:11], s81 offen offset:1024 sc1
Ta 148,21,41216
Mvaa 132,156,12
Ta 150,21,46336
Mvaa 132,152,8
W 8
Ta 152,26,288
v_bfe_u32 v20, v1, 4, 4
Mvaa 144,128,8
Ta 154,26,5408
Mvaa 144,132,12
Ta 156,22,41248
v_mul_u32_u24 v20, 1280, v20
Mvaa 152,132,28
Ta 158,22,46368
buffer_load_dwordx4 a[84:87], v1, s[8:11], s82 offen offset:1024 sc1
Mvaa 152,128,24
W 8
Ta 128,21,51200
Ta 130,21,56320
Mvaa 156,136,24
v_lshrrev_b32 v12, 8, v1
Mvaa 156,140,28
Ta 132,23,10240
Ta 134,23,15360
Mvaa 148,140,12
v_lshl_add_u32 v20, v12, 4, v20
Mvaa 148,136,8
W 8
buffer_load_dwordx4 a[72:75], v1, s[8:11], s81 offen offset:2048 sc1
Ta 136,23,30720
Mvaa 160,144,8
Ta 138,23,35840
Mvaa 160,148,12
s_bfe_u32 s66, s59, 0x10001
Ta 140,23,40960
Ta 142,23,46080
Mvaa 168,148,28
Mvaa 168,144,24
W 8
s_lshl_b32 s67, s57, 1
Ta 144,22,51232
Mvaa 172,152,24
Ta 146,22,56352
buffer_load_dwordx4 a[88:91], v1, s[8:11], s82 offen offset:2048 sc1
Mvaa 172,156,28
Ta 148,24,10272
Ta 150,24,15392
Mvaa 164,156,12
s_add_u32 s66, s66, s67
Mvaa 164,152,8
W 8
Ta 152,24,30752
Ta 154,24,35872
Mvaa 96,128,32
s_lshl_b32 s61, s66, 2
Mvaa 96,132,36
buffer_load_dwordx4 a[76:79], v1, s[8:11], s81 offen offset:3072 sc1
Ta 156,24,40992
Mvaa 104,132,52
s_lshr_b32 s66, s59, 2
Ta 158,24,46112
Mvaa 104,128,48
W 8
Ta 128,21,51264
Ta 130,21,56384
Mvaa 104,136,56
Mvaa 104,140,60
s_lshl_b32 s66, s66, 1
Ta 132,23,10304
Ta 134,23,15424
Mvaa 96,140,44
buffer_load_dwordx4 a[92:95], v1, s[8:11], s82 offen offset:3072 sc1
Mvaa 96,136,40
W 8
s_add_u32 s81, s58, 2
s_and_b32 s81, s81, 3
Mvaa 100,144,32
s_lshl_b32 s81, s81, 12
s_add_u32 s81, s81, s50
s_add_u32 s61, s61, s66
Mvaa 100,148,36
Ta 136,23,30784
Ta 138,23,35904
Mvaa 108,148,52
s_and_b32 s66, s59, 1
Ta 140,23,41024
Ta 142,23,46144
Mvaa 108,144,48
W 8
buffer_load_dwordx4 a[96:99], v1, s[8:11], s81 offen offset:0 sc1
Ta 144,22,51296
Mvaa 108,152,56
s_add_u32 s82, s81, 131072
s_add_u32 s61, s61, s66
Ta 146,22,56416
Mvaa 108,156,60
Ta 148,24,10336
Ta 150,24,15456
Mvaa 100,156,44
Mvaa 100,152,40
W 8
s_xor_b32 s67, s61, 0
Ta 152,24,30816
Mvaa 112,128,32
Ta 154,24,35936
buffer_load_dwordx4 a[112:115], v1, s[8:11], s82 offen offset:0 sc1
Mvaa 112,132,36
s_mul_i32 s72, s67, 20480
Ta 156,24,41056
Ta 158,24,46176
Mvaa 120,132,52
K 96,96,97
K 97,98,99
Mvaa 120,128,48
W 8
Ta 128,21,51328
Ta 130,21,56448
Mvaa 120,136,56
s_mul_i32 s66, s58, 320
K 98,100,101
Mvaa 120,140,60
buffer_load_dwordx4 a[100:103], v1, s[8:11], s81 offen offset:1024 sc1
K 99,102,103
Mvaa 112,140,44
s_add_u32 s72, s72, s66
Ta 132,23,10368
Ta 134,23,15488
Mvaa 112,136,40
W 8
s_add_u32 s83, s52, 0
Ta 136,23,30848
Mvaa 116,144,32
s_mul_i32 s75, s67, 20480
Ta 138,23,35968
Mvaa 116,148,36
buffer_store_dwordx4 v[96:99], v1, s[8:11], s83 offen sc1
Ta 140,23,41088
Mvaa 124,148,52
Ta 142,23,46208
K 104,104,105
Mvaa 124,144,48
W 8
s_mul_i32 s66, s58, 2560
K 105,106,107
Mvaa 124,152,56
Ta 144,22,51360
Ta 146,22,56480
Mvaa 124,156,60
s_add_u32 s75, s75, s66
buffer_load_dwordx4 a[116:119], v1, s[8:11], s82 offen offset:1024 sc1
K 106,108,109
Mvaa 116,156,44
Ta 148,24,10400
Ta 150,24,15520
Mvaa 116,152,40
W 8
Ta 152,24,30880
K 107,110,111
Mvaa 128,128,32
s_add_u32 s75, s75, 131072
s_add_u32 s83, s52, 40960
Ta 154,24,36000
Mvaa 128,132,36
buffer_load_dwordx4 a[104:107], v1, s[8:11], s81 offen offset:2048 sc1
Ta 156,24,41120
Mvaa 136,132,52
s_xor_b32 s67, s61, 124
Ta 158,24,46240
Mvaa 136,128,48
W 8
Ta 128,21,51392
Ta 130,21,56512
Mvaa 136,136,56
Mvaa 136,140,60
s_mul_i32 s73, s67, 20480
buffer_store_dwordx4 v[104:107], v1, s[8:11], s83 offen sc1
K 112,112,113
Mvaa 128,140,44
Ta 132,23,10432
K 113,114,115
Mvaa 128,136,40
W 7
Ta 134,23,15552
K 114,116,117
Mvaa 132,144,32
s_mul_i32 s66, s58, 320
Ta 136,23,30912
Ta 138,23,36032
Mvaa 132,148,36
buffer_load_dwordx4 a[120:123], v1, s[8:11], s82 offen offset:2048 sc1
K 115,118,119
Mvaa 140,148,52
s_add_u32 s73, s73, s66
Ta 140,23,41152
Ta 142,23,46272
Mvaa 140,144,48
W 8
s_add_u32 s83, s52, 8192
Ta 144,22,51424
Mvaa 140,152,56
s_mul_i32 s76, s67, 20480
Ta 146,22,56544
Mvaa 140,156,60
buffer_load_dwordx4 a[108:111], v1, s[8:11], s81 offen offset:3072 sc1
Ta 148,24,10464
Mvaa 132,156,44
s_mul_i32 s66, s58, 2560
Ta 150,24,15584
Mvaa 132,152,40
W 8
Ta 152,24,30944
Ta 154,24,36064
Mvaa 144,128,32
Mvaa 144,132,36
s_add_u32 s76, s76, s66
buffer_store_dwordx4 v[112:115], v1, s[8:11], s83 offen sc1
K 120,120,121
Mvaa 152,132,52
K 121,122,123
K 122,124,125
Mvaa 152,128,48
W 6
Ta 156,24,41184
K 123,126,127
Mvaa 152,136,56
s_add_u32 s83, s52, 49152
s_add_u32 s76, s76, 131072
Ta 158,24,46304
Mvaa 152,140,60
buffer_load_dwordx4 a[124:127], v1, s[8:11], s82 offen offset:3072 sc1
Ta 128,21,51456
Mvaa 144,140,44
s_add_u32 s81, s58, 3
s_and_b32 s81, s81, 3
s_lshl_b32 s81, s81, 12
Mvaa 144,136,40
W 5
s_add_u32 s81, s81, s50
Ta 130,21,56576
)"
R"(Mvaa 148,144,32
Ta 132,23,10496
Ta 134,23,15616
Mvaa 148,148,36
buffer_load_dwordx4 a[184:187], v1, s[8:11], s81 offen offset:0 sc1
v_bfe_u32 v15, v1, 7, 1
Mvaa 156,148,52
s_add_u32 s82, s81, 131072
Ta 136,23,30976
Ta 138,23,36096
Mvaa 156,144,48
W 6
Ta 140,23,41216
v_mul_u32_u24 v14, 10240, v15
Mvaa 156,152,56
Ta 142,23,46336
v_lshrrev_b32 v15, 8, v1
Mvaa 156,156,60
buffer_store_dwordx4 v[120:123], v1, s[8:11], s83 offen sc1
K 128,128,129
Mvaa 148,156,44
K 129,130,131
K 130,132,133
Mvaa 148,152,40
W 4
Ta 144,22,51488
K 131,134,135
Mvaa 160,128,32
s_add_u32 s83, s52, 16384
Ta 146,22,56608
v_lshl_add_u32 v14, v15, 7, v14
Mvaa 160,132,36
buffer_load_dwordx4 a[200:203], v1, s[8:11], s82 offen offset:0 sc1
Ta 148,24,10528
Mvaa 168,132,52
Ta 150,24,15648
Ta 152,24,31008
Mvaa 168,128,48
W 5
Ta 154,24,36128
v_and_b32 v15, 0x70, v1
Mvaa 168,136,56
Ta 156,24,41248
Ta 158,24,46368
Mvaa 168,140,60
buffer_load_dwordx4 a[188:191], v1, s[8:11], s81 offen offset:1024 sc1
v_add_u32 v14, v14, v15
Mvaa 160,140,44
s_bitcmp1_b32 s14, 0
s_cselect_b32 s74, s73, s72
s_cselect_b32 s77, s76, s75
Ta 128,21,10240
Ta 130,21,15360
Mvaa 160,136,40
W 6
Ta 132,21,20480
Ta 134,21,25600
Mvaa 164,144,32
s_add_u32 s74, s74, 256
Ta 136,22,10272
Ta 138,22,15392
Mvaa 164,148,36
s_add_u32 s77, s77, 2048
buffer_store_dwordx4 v[128:131], v1, s[8:11], s83 offen sc1
K 136,136,137
Mvaa 172,148,52
K 137,138,139
K 138,140,141
Mvaa 172,144,48
W 6
Ta 140,22,20512
K 139,142,143
Mvaa 172,152,56
s_add_u32 s83, s52, 57344
Ta 142,22,25632
Ta 144,21,10304
Mvaa 172,156,60
s_lshl_b32 s66, s14, 6
buffer_load_dwordx4 a[204:207], v1, s[8:11], s82 offen offset:1024 sc1
Ta 146,21,15424
Mvaa 164,156,44
s_lshl_b32 s67, s14, 9
Ta 148,21,20544
Ta 150,21,25664
Mvaa 164,152,40
s_waitcnt vmcnt(21) lgkmcnt(8)
Ta 152,22,10336
Ta 154,22,15456
Mvaa 176,128,64
Mvaa 176,132,68
s_add_u32 s72, s72, s66
buffer_load_dwordx4 a[192:195], v1, s[8:11], s81 offen offset:2048 sc1
Ta 156,22,20576
Mvaa 184,132,84
Ta 158,22,25696
Mvaa 184,128,80
W 8
s_add_u32 s75, s75, s67
Ta 128,21,10368
Mvaa 188,136,80
Ta 130,21,15488
Mvaa 188,140,84
buffer_store_dwordx4 v[136:139], v1, s[8:11], s83 offen sc1
K 144,144,145
Mvaa 180,140,68
s_add_u32 s73, s73, s66
K 145,146,147
K 146,148,149
Mvaa 180,136,64
W 6
Ta 132,21,20608
K 147,150,151
Mvaa 192,144,64
s_add_u32 s83, s52, 24576
Ta 134,21,25728
Ta 136,22,10400
Mvaa 192,148,68
s_add_u32 s76, s76, s67
buffer_load_dwordx4 a[208:211], v1, s[8:11], s82 offen offset:2048 sc1
Ta 138,22,15520
Mvaa 200,148,84
Ta 140,22,20640
Ta 142,22,25760
Mvaa 200,144,80
W 8
Ta 144,21,10432
Ta 146,21,15552
Mvaa 204,152,80
Mvaa 204,156,84
buffer_store_dwordx4 v[144:147], v1, s[8:11], s83 offen sc1
K 152,152,153
Mvaa 196,156,68
K 153,154,155
K 154,156,157
Mvaa 196,152,64
W 6
Ta 148,21,20672
K 155,158,159
Mvaa 208,128,64
s_add_u32 s83, s52, 65536
Ta 150,21,25792
Ta 152,22,10464
Mvaa 208,132,68
buffer_load_dwordx4 a[196:199], v1, s[8:11], s81 offen offset:3072 sc1
Ta 154,22,15584
Mvaa 216,132,84
Ta 156,22,20704
Ta 158,22,25824
Mvaa 216,128,80
W 8
Ta 128,21,10496
Ta 130,21,15616
Mvaa 220,136,80
Mvaa 220,140,84
buffer_load_dwordx4 a[212:215], v1, s[8:11], s82 offen offset:3072 sc1
Ta 132,21,20736
Mvaa 212,140,68
Ta 134,21,25856
Mvaa 212,136,64
W 8
Ta 136,22,10528
Ta 138,22,15648
Mvaa 224,144,64
Mvaa 224,148,68
buffer_store_dwordx4 v[152:155], v1, s[8:11], s83 offen sc1
K 160,160,161
Mvaa 232,148,84
K 161,162,163
K 162,164,165
Mvaa 232,144,80
W 6
Ta 140,22,20768
K 163,166,167
Mvaa 236,152,80
s_add_u32 s83, s52, 32768
Ta 142,22,25888
Ta 144,21,30720
Mvaa 236,156,84
buffer_load_dwordx4 v[36:39], v20, s[16:19], s72 offen
Ta 146,21,35840
Mvaa 228,156,68
Ta 148,23,0
Ta 150,23,5120
Mvaa 228,152,64
W 8
Ta 152,22,30752
Ta 154,22,35872
Mvaa 240,128,64
Mvaa 240,132,68
buffer_load_dwordx4 v[44:47], v14, s[8:11], s75 offen sc1
Ta 156,24,32
Mvaa 248,132,84
Ta 158,24,5152
Mvaa 248,128,80
W 8
Ta 128,21,30784
Ta 130,21,35904
Mvaa 252,136,80
Mvaa 252,140,84
buffer_store_dwordx4 v[160:163], v1, s[8:11], s83 offen sc1
K 168,168,169
Mvaa 244,140,68
K 169,170,171
K 170,172,173
Mvaa 244,136,64
s_waitcnt vmcnt(27) lgkmcnt(6)
Ta 132,23,64
K 171,174,175
Mvaa 176,144,72
s_add_u32 s83, s52, 73728
Ta 134,23,5184
Ta 136,22,30816
Mvaa 176,148,76
buffer_load_dwordx4 v[52:55], v20, s[16:19], s73 offen
Ta 138,22,35936
Mvaa 184,148,92
Ta 140,24,96
Ta 142,24,5216
Mvaa 184,144,88
W 8
Ta 144,21,30848
Ta 146,21,35968
Mvaa 188,152,88
Mvaa 188,156,92
buffer_store_dwordx4 v[168:171], v1, s[8:11], s83 offen sc1
Ta 148,23,128
Mvaa 180,156,76
Ta 150,23,5248
Mvaa 180,152,72
W 8
Ta 152,22,30880
Ta 154,22,36000
Mvaa 192,128,72
Mvaa 192,132,76
buffer_load_dwordx4 v[60:63], v14, s[8:11], s76 offen sc1
Ta 156,24,160
Mvaa 200,132,92
Ta 158,24,5280
Mvaa 200,128,88
W 8
Ta 128,21,30912
Ta 130,21,36032
Mvaa 204,136,88
Mvaa 204,140,92
buffer_load_dwordx4 v[68:71], v20, s[16:19], s74 offen
Ta 132,23,192
Mvaa 196,140,76
Ta 134,23,5312
Mvaa 196,136,72
W 8
Ta 136,22,30944
Ta 138,22,36064
Mvaa 208,144,72
Mvaa 208,148,76
buffer_load_dwordx4 v[76:79], v14, s[8:11], s77 offen sc1
Ta 140,24,224
Mvaa 216,148,92
Ta 142,24,5344
Mvaa 216,144,88
W 8
Ta 144,21,30976
Ta 146,21,36096
Mvaa 220,152,88
Mvaa 220,156,92
buffer_load_dwordx2 v[12:13], v27, s[8:11], 0 offen offset:3584 sc1
Ta 148,23,256
Mvaa 212,156,76
Ta 150,23,5376
Mvaa 212,152,72
W 8
Ta 152,22,31008
Ta 154,22,36128
Mvaa 224,128,72
Mvaa 224,132,76
Ta 156,24,288
Ta 158,24,5408
Mvaa 232,132,92
Mvaa 232,128,88
W 8
Ta 128,23,20480
Ta 130,23,25600
Mvaa 236,136,88
Mvaa 236,140,92
Ta 132,23,51200
Ta 134,23,56320
Mvaa 228,140,76
Mvaa 228,136,72
W 8
Ta 136,25,0
Ta 138,25,5120
Mvaa 240,144,72
Mvaa 240,148,76
Ta 140,21,40960
Ta 142,21,46080
Mvaa 248,148,92
Mvaa 248,144,88
W 8
Ta 144,21,51200
Ta 146,21,56320
Mvaa 252,152,88
Mvaa 252,156,92
Ta 148,23,10240
Ta 150,23,15360
Mvaa 244,156,76
Mvaa 244,152,72
s_waitcnt vmcnt(28) lgkmcnt(8)
Ta 152,23,30720
Ta 154,23,35840
Mvaa 176,128,96
Mvaa 176,132,100
Ta 156,23,40960
Ta 158,23,46080
Mvaa 184,132,116
Mvaa 184,128,112
s_waitcnt vmcnt(22) lgkmcnt(8)
Ta 128,24,20512
Ta 130,24,25632
Mvaa 184,136,120
Mvaa 184,140,124
Ta 132,24,51232
Ta 134,24,56352
Mvaa 176,140,108
Mvaa 176,136,104
s_waitcnt vmcnt(16) lgkmcnt(8)
Ta 136,26,32
Ta 138,26,5152
Mvaa 176,144,184
Mvaa 176,148,188
Ta 140,22,40992
Ta 142,22,46112
Mvaa 184,148,204
s_waitcnt vmcnt(4)
v_lshlrev_b32 v2, 2, v1
v_mov_b64 v[28:29], s[12:13]
s_mov_b64 exec, 3
buffer_store_dwordx2 v[28:29], v2, s[8:11], s53 offen offset:0 sc1
s_mov_b64 exec, -1
Mvaa 184,144,200
W 8
Ta 144,22,51232
Ta 146,22,56352
Mvaa 184,152,208
Mvaa 184,156,212
Ta 148,24,10272
Ta 150,24,15392
Mvaa 176,156,196
Mvaa 176,152,192
W 8
Ta 152,24,30752
Ta 154,24,35872
Mvaa 180,128,96
Mvaa 180,132,100
Ta 156,24,40992
Ta 158,24,46112
Mvaa 188,132,116
Mvaa 188,128,112
W 8
Ta 128,23,20544
Ta 130,23,25664
Mvaa 188,136,120
Mvaa 188,140,124
Ta 132,23,51264
Ta 134,23,56384
Mvaa 180,140,108
Mvaa 180,136,104
W 8
Ta 136,25,64
Ta 138,25,5184
Mvaa 180,144,184
Mvaa 180,148,188
Ta 140,21,41024
Ta 142,21,46144
Mvaa 188,148,204
Mvaa 188,144,200
W 8
Ta 144,21,51264
Ta 146,21,56384
Mvaa 188,152,208
Mvaa 188,156,212
Ta 148,23,10304
Ta 150,23,15424
Mvaa 180,156,196
s_waitcnt vmcnt(1)
v_xor_b32 v12, s12, v12
v_xor_b32 v13, s13, v13
v_lshrrev_b32 v3, 11, v12
v_or_b32 v13, v13, v3
v_cmp_eq_u32 vcc, 0, v13
s_cmp_eq_u64 vcc, -1
s_cbranch_scc0 .LL_108%=
.LL_109%=:
v_bfe_u32 v13, v12, 4, 7
s_xor_b32 s66, s61, 124
v_cmp_eq_u32 vcc, s61, v13
v_cmp_eq_u32_e64 s[86:87], s66, v13
s_or_b32 s68, vcc_lo, s86
s_and_b32 s68, s68, 255
s_cbranch_scc1 .Lup_106%=
.Lupr_107%=:
Mvaa 180,152,192
)"
R"(W 8
Ta 152,23,30784
Ta 154,23,35904
Mvaa 192,128,96
v_lshlrev_b32 v32, 16, v36
Mvaa 192,132,100
Ta 156,23,41024
v_and_b32 v33, 0xffff0000, v36
Mvaa 200,132,116
Ta 158,23,46144
v_lshlrev_b32 v34, 16, v37
Mvaa 200,128,112
W 8
K 176,176,177
v_and_b32 v35, 0xffff0000, v37
Mvaa 200,136,120
Ta 128,24,20576
Ta 130,24,25696
Mvaa 200,140,124
buffer_load_dwordx2 v[16:17], v18, s[8:11], s63 offen sc1
v_lshlrev_b32 v36, 16, v38
Mvaa 192,140,108
K 177,178,179
v_and_b32 v37, 0xffff0000, v38
Mvaa 192,136,104
W 6
Ta 132,24,51296
K 178,180,181
Mvaa 192,144,184
Ta 134,24,56416
v_lshlrev_b32 v38, 16, v39
Mvaa 192,148,188
K 179,182,183
v_and_b32 v39, 0xffff0000, v39
Mvaa 200,148,204
s_add_u32 s83, s52, 327680
Ta 136,26,96
Ta 138,26,5216
Mvaa 200,144,200
W 6
v_lshlrev_b32 v40, 16, v44
v_and_b32 v41, 0xffff0000, v44
Mvaa 200,152,208
buffer_store_dwordx4 v[176:179], v1, s[8:11], s83 offen sc1
K 184,184,185
Mvaa 200,156,212
K 185,186,187
K 186,188,189
Mvaa 192,156,196
K 187,190,191
s_add_u32 s83, s52, 368640
buffer_store_dwordx4 v[184:187], v1, s[8:11], s83 offen sc1
Mvaa 192,152,192
Ta 140,22,41056
Ta 142,22,46176
W 4
Mvaa 196,128,96
v_lshlrev_b32 v42, 16, v45
v_and_b32 v43, 0xffff0000, v45
Mvaa 196,132,100
Ta 144,22,51296
Ta 146,22,56416
Mvaa 204,132,116
v_lshlrev_b32 v44, 16, v46
v_and_b32 v45, 0xffff0000, v46
Mvaa 204,128,112
Ta 148,24,10336
Ta 150,24,15456
W 4
Mvaa 204,136,120
v_lshlrev_b32 v46, 16, v47
v_and_b32 v47, 0xffff0000, v47
Mvaa 204,140,124
Ta 152,24,30816
Ta 154,24,35936
Mvaa 196,140,108
v_lshlrev_b32 v48, 16, v52
v_and_b32 v49, 0xffff0000, v52
Mvaa 196,136,104
Ta 156,24,41056
Ta 158,24,46176
W 4
Mvaa 196,144,184
v_lshlrev_b32 v50, 16, v53
v_and_b32 v51, 0xffff0000, v53
Mvaa 196,148,188
Ta 128,23,20608
Ta 130,23,25728
Mvaa 204,148,204
v_lshlrev_b32 v52, 16, v54
v_and_b32 v53, 0xffff0000, v54
Mvaa 204,144,200
Ta 132,23,51328
Ta 134,23,56448
W 4
Mvaa 204,152,208
v_lshlrev_b32 v54, 16, v55
v_and_b32 v55, 0xffff0000, v55
Mvaa 204,156,212
Ta 136,25,128
Ta 138,25,5248
Mvaa 196,156,196
v_lshlrev_b32 v56, 16, v60
v_and_b32 v57, 0xffff0000, v60
Mvaa 196,152,192
Ta 140,21,41088
Ta 142,21,46208
W 4
Mvaa 208,128,96
v_lshlrev_b32 v58, 16, v61
v_and_b32 v59, 0xffff0000, v61
Mvaa 208,132,100
Ta 144,21,51328
Ta 146,21,56448
Mvaa 216,132,116
v_lshlrev_b32 v60, 16, v62
v_and_b32 v61, 0xffff0000, v62
Mvaa 216,128,112
Ta 148,23,10368
Ta 150,23,15488
W 4
Mvaa 216,136,120
v_lshlrev_b32 v62, 16, v63
v_and_b32 v63, 0xffff0000, v63
Mvaa 216,140,124
Ta 152,23,30848
Ta 154,23,35968
Mvaa 208,140,108
v_lshlrev_b32 v64, 16, v68
v_and_b32 v65, 0xffff0000, v68
Mvaa 208,136,104
Ta 156,23,41088
Ta 158,23,46208
W 4
Mvaa 208,144,184
v_lshlrev_b32 v66, 16, v69
K 192,192,193
Mvaa 208,148,188
Ta 128,24,20640
v_and_b32 v67, 0xffff0000, v69
Mvaa 216,148,204
Ta 130,24,25760
K 193,194,195
Mvaa 216,144,200
Ta 132,24,51360
Ta 134,24,56480
W 4
Mvaa 216,152,208
v_lshlrev_b32 v68, 16, v70
K 194,196,197
Mvaa 216,156,212
v_and_b32 v69, 0xffff0000, v70
v_lshlrev_b32 v70, 16, v71
Mvaa 208,156,196
K 195,198,199
s_add_u32 s83, s52, 335872
buffer_store_dwordx4 v[192:195], v1, s[8:11], s83 offen sc1
K 200,200,201
K 201,202,203
K 202,204,205
K 203,206,207
s_add_u32 s83, s52, 376832
buffer_store_dwordx4 v[200:203], v1, s[8:11], s83 offen sc1
Mvaa 208,152,192
Ta 136,26,160
Ta 138,26,5280
Ta 140,22,41120
Ta 142,22,46240
W 4
Mvaa 212,128,96
s_waitcnt vmcnt(4)
v_cmp_eq_u64 vcc, s[12:13], v[16:17]
s_and_b32 s71, vcc_lo, 255
v_and_b32 v71, 0xffff0000, v71
v_lshlrev_b32 v72, 16, v76
Mvaa 212,132,100
s_lshl_b32 s92, s14, 3
s_lshr_b32 s92, s71, s92
s_mov_b32 s91, 134217728
Mvaa 220,132,116
s_bitcmp1_b32 s71, 0
s_cselect_b32 s94, s78, s91
v_and_b32 v73, 0xffff0000, v76
Mvaa 220,128,112
Ta 144,22,51360
Ta 146,22,56480
Ta 148,24,10400
Ta 150,24,15520
W 4
Mvaa 220,136,120
v_lshlrev_b32 v74, 16, v77
v_and_b32 v75, 0xffff0000, v77
Mvaa 220,140,124
v_lshlrev_b32 v76, 16, v78
v_and_b32 v77, 0xffff0000, v78
Mvaa 212,140,108
v_lshlrev_b32 v78, 16, v79
v_and_b32 v79, 0xffff0000, v79
Mvaa 212,136,104
Ta 152,24,30880
Ta 154,24,36000
Ta 156,24,41120
Ta 158,24,46240
W 4
Mvaa 212,144,184
Ta 128,23,20672
Ta 130,23,25792
Mvaa 212,148,188
buffer_load_dwordx4 a[0:3], v1, s[8:11], s94 offen offset:0 sc1
Ta 132,23,51392
Mvaa 220,148,204
Ta 134,23,56512
Ta 136,25,192
Mvaa 220,144,200
W 5
Ta 138,25,5312
Ta 140,21,41152
Mvaa 220,152,208
Ta 142,21,46272
Ta 144,21,51392
Mvaa 220,156,212
s_bitcmp1_b32 s92, 0
s_cbranch_scc0 .LL_111%=
buffer_load_dwordx4 a[216:219], v1, s[8:11], s65 offen offset:0 sc1
.LL_111%=:
Ta 146,21,56512
Mvaa 212,156,196
s_bitcmp1_b32 s71, 1
s_cselect_b32 s94, s78, s91
Ta 148,23,10432
Mvaa 212,152,192
W 7
Ta 150,23,15552
Ta 152,23,30912
Mvaa 224,128,96
Ta 154,23,36032
Mvaa 224,132,100
buffer_load_dwordx4 a[4:7], v1, s[8:11], s94 offen offset:1024 sc1
Ta 156,23,41152
Mvaa 232,132,116
Ta 158,23,46272
K 208,208,209
Mvaa 232,128,112
W 8
Ta 128,24,20704
Ta 130,24,25824
Mvaa 232,136,120
K 209,210,211
Mvaa 232,140,124
s_bitcmp1_b32 s92, 1
s_cbranch_scc0 .LL_112%=
buffer_load_dwordx4 a[220:223], v1, s[8:11], s65 offen offset:1024 sc1
.LL_112%=:
K 210,212,213
Mvaa 224,140,108
s_bitcmp1_b32 s71, 2
s_cselect_b32 s94, s78, s91
Ta 132,24,51424
Mvaa 224,136,104
W 7
Ta 134,24,56544
K 211,214,215
Mvaa 224,144,184
s_add_u32 s83, s52, 344064
Ta 136,26,224
Ta 138,26,5344
Mvaa 224,148,188
buffer_load_dwordx4 a[8:11], v1, s[8:11], s94 offen offset:2048 sc1
Ta 140,22,41184
Mvaa 232,148,204
Ta 142,22,46304
Mvaa 232,144,200
W 8
Ta 144,22,51424
Ta 146,22,56544
Mvaa 232,152,208
Mvaa 232,156,212
buffer_store_dwordx4 v[208:211], v1, s[8:11], s83 offen sc1
K 216,216,217
Mvaa 224,156,196
K 217,218,219
K 218,220,221
K 219,222,223
s_add_u32 s83, s52, 385024
buffer_store_dwordx4 v[216:219], v1, s[8:11], s83 offen sc1
Mvaa 224,152,192
W 6
Ta 148,24,10464
Ta 150,24,15584
Mvaa 228,128,96
Ta 152,24,30944
Ta 154,24,36064
Mvaa 228,132,100
Ta 156,24,41184
Ta 158,24,46304
Mvaa 236,132,116
s_bitcmp1_b32 s92, 2
s_cbranch_scc0 .LL_113%=
buffer_load_dwordx4 a[224:227], v1, s[8:11], s65 offen offset:2048 sc1
.LL_113%=:
Mvaa 236,128,112
W 8
s_bitcmp1_b32 s71, 3
s_cselect_b32 s94, s78, s91
Mvaa 236,136,120
Ta 128,23,20736
Ta 130,23,25856
Mvaa 236,140,124
Ta 132,23,51456
Ta 134,23,56576
Mvaa 228,140,108
buffer_load_dwordx4 a[12:15], v1, s[8:11], s94 offen offset:3072 sc1
Mvaa 228,136,104
W 8
Ta 136,25,256
Ta 138,25,5376
Mvaa 228,144,184
Mvaa 228,148,188
Ta 140,21,41216
Ta 142,21,46336
Mvaa 236,148,204
s_bitcmp1_b32 s92, 3
s_cbranch_scc0 .LL_114%=
buffer_load_dwordx4 a[228:231], v1, s[8:11], s65 offen offset:3072 sc1
.LL_114%=:
Mvaa 236,144,200
W 8
s_bitcmp1_b32 s71, 4
s_cselect_b32 s94, s78, s91
Mvaa 236,152,208
Ta 144,21,51456
Ta 146,21,56576
Mvaa 236,156,212
Ta 148,23,10496
Ta 150,23,15616
Mvaa 228,156,196
buffer_load_dwordx4 a[16:19], v19, s[8:11], s94 offen offset:0 sc1
Mvaa 228,152,192
W 8
Ta 152,23,30976
Ta 154,23,36096
Mvaa 240,128,96
Mvaa 240,132,100
Ta 156,23,41216
Ta 158,23,46336
Mvaa 248,132,116
s_bitcmp1_b32 s92, 4
s_cbranch_scc0 .LL_115%=
buffer_load_dwordx4 a[232:235], v19, s[8:11], s65 offen offset:0 sc1
.LL_115%=:
K 224,224,225
Mvaa 248,128,112
W 8
s_bitcmp1_b32 s71, 5
s_cselect_b32 s94, s78, s91
Mvaa 248,136,120
Ta 128,24,20768
Ta 130,24,25888
Mvaa 248,140,124
K 225,226,227
K 226,228,229
Mvaa 240,140,108
buffer_load_dwordx4 a[20:23], v19, s[8:11], s94 offen offset:1024 sc1
Ta 132,24,51488
Mvaa 240,136,104
W 7
Ta 134,24,56608
K 227,230,231
Mvaa 240,144,184
s_add_u32 s83, s52, 352256
Ta 136,26,288
Ta 138,26,5408
Mvaa 240,148,188
Ta 140,22,41248
Ta 142,22,46368
Mvaa 248,148,204
s_bitcmp1_b32 s92, 5
s_cbranch_scc0 .LL_116%=
buffer_load_dwordx4 a[236:239], v19, s[8:11], s65 offen offset:1024 sc1
.LL_116%=:
Mvaa 248,144,200
W 8
s_bitcmp1_b32 s71, 6
s_cselect_b32 s94, s78, s91
Mvaa 248,152,208
Ta 144,22,51488
Ta 146,22,56608
Mvaa 248,156,212
Ta 148,24,10528
Ta 150,24,15648
Mvaa 240,156,196
buffer_store_dwordx4 v[224:227], v1, s[8:11], s83 offen sc1
K 232,232,233
K 233,234,235
K 234,236,237
K 235,238,239
s_add_u32 s83, s52, 393216
buffer_store_dwordx4 v[232:235], v1, s[8:11], s83 offen sc1
Mvaa 240,152,192
W 8
)"
R"(Ta 152,24,31008
Ta 154,24,36128
Mvaa 244,128,96
Mvaa 244,132,100
Ta 156,24,41248
Ta 158,24,46368
Mvaa 252,132,116
buffer_load_dwordx4 a[24:27], v19, s[8:11], s94 offen offset:2048 sc1
Mvaa 252,128,112
W 8
Mvaa 252,136,120
Mvaa 252,140,124
Mvaa 244,140,108
s_bitcmp1_b32 s92, 6
s_cbranch_scc0 .LL_117%=
buffer_load_dwordx4 a[240:243], v19, s[8:11], s65 offen offset:2048 sc1
.LL_117%=:
Mvaa 244,136,104
W 4
s_bitcmp1_b32 s71, 7
s_cselect_b32 s94, s78, s91
Mvaa 244,144,184
Mvaa 244,148,188
Mvaa 252,148,204
buffer_load_dwordx4 a[28:31], v19, s[8:11], s94 offen offset:3072 sc1
Mvaa 252,144,200
W 0
Mvaa 252,152,208
Mvaa 252,156,212
Mvaa 244,156,196
s_bitcmp1_b32 s92, 7
s_cbranch_scc0 .LL_118%=
buffer_load_dwordx4 a[244:247], v19, s[8:11], s65 offen offset:3072 sc1
.LL_118%=:
Mvaa 244,152,192
K 240,240,241
K 241,242,243
s_nop 5
K 242,244,245
K 243,246,247
s_add_u32 s83, s52, 360448
buffer_store_dwordx4 v[240:243], v1, s[8:11], s83 offen sc1
K 248,248,249
K 249,250,251
K 250,252,253
K 251,254,255
s_add_u32 s83, s52, 401408
buffer_store_dwordx4 v[248:251], v1, s[8:11], s83 offen sc1
.Ltend_83%=:
buffer_load_dwordx2 v[16:17], v18, s[8:11], s63 offen sc1
s_cmp_lg_u32 s68, 0
s_cbranch_scc1 .Lup_119%=
.Lupr_120%=:
s_lshl_b32 s66, 0x101, s59
s_waitcnt vmcnt(1)
s_cmp_eq_u32 s64, 0
s_cbranch_scc0 .LL_125%=
v_lshlrev_b32 v2, 2, v1
v_mov_b64 v[28:29], s[12:13]
s_mov_b64 exec, 3
buffer_store_dwordx2 v[28:29], v2, s[8:11], s53 offen offset:512 sc1
s_mov_b64 exec, -1
.LL_125%=:
s_barrier
s_waitcnt vmcnt(1)
v_cmp_eq_u64 vcc, s[12:13], v[16:17]
s_or_b32 s67, vcc_lo, s66
s_cmp_eq_u32 s14, 0
s_cbranch_scc1 .LL_127%=
s_cmp_eq_u32 s14, 1
s_cbranch_scc1 .LL_128%=
s_mov_b32 s84, 0
.LL_129%=:
s_mov_b32 s70, s71
s_andn2_b32 s69, s67, s71
s_and_b32 s69, s69, 0xffff
s_cbranch_scc0 .LL_132%=
s_and_b32 s80, s69, 255
s_cbranch_scc0 .LL_133%=
s_bitcmp1_b32 s69, 0
s_cbranch_scc0 .LL_134%=
buffer_load_dwordx4 a[0:3], v1, s[8:11], s78 offen offset:0 sc1
.LL_134%=:
s_bitcmp1_b32 s69, 1
s_cbranch_scc0 .LL_135%=
buffer_load_dwordx4 a[4:7], v1, s[8:11], s78 offen offset:1024 sc1
.LL_135%=:
s_bitcmp1_b32 s69, 2
s_cbranch_scc0 .LL_136%=
buffer_load_dwordx4 a[8:11], v1, s[8:11], s78 offen offset:2048 sc1
.LL_136%=:
s_bitcmp1_b32 s69, 3
s_cbranch_scc0 .LL_137%=
buffer_load_dwordx4 a[12:15], v1, s[8:11], s78 offen offset:3072 sc1
.LL_137%=:
s_bitcmp1_b32 s69, 4
s_cbranch_scc0 .LL_138%=
buffer_load_dwordx4 a[16:19], v19, s[8:11], s78 offen offset:0 sc1
.LL_138%=:
s_bitcmp1_b32 s69, 5
s_cbranch_scc0 .LL_139%=
buffer_load_dwordx4 a[20:23], v19, s[8:11], s78 offen offset:1024 sc1
.LL_139%=:
s_bitcmp1_b32 s69, 6
s_cbranch_scc0 .LL_140%=
buffer_load_dwordx4 a[24:27], v19, s[8:11], s78 offen offset:2048 sc1
.LL_140%=:
s_bitcmp1_b32 s69, 7
s_cbranch_scc0 .LL_141%=
buffer_load_dwordx4 a[28:31], v19, s[8:11], s78 offen offset:3072 sc1
.LL_141%=:
.LL_133%=:
s_and_b32 s80, s69, 65280
s_cbranch_scc0 .LL_142%=
s_bitcmp1_b32 s69, 8
s_cbranch_scc0 .LL_143%=
buffer_load_dwordx4 a[32:35], v1, s[8:11], s79 offen offset:0 sc1
.LL_143%=:
s_bitcmp1_b32 s69, 9
s_cbranch_scc0 .LL_144%=
buffer_load_dwordx4 a[36:39], v1, s[8:11], s79 offen offset:1024 sc1
.LL_144%=:
s_bitcmp1_b32 s69, 10
s_cbranch_scc0 .LL_145%=
buffer_load_dwordx4 a[40:43], v1, s[8:11], s79 offen offset:2048 sc1
.LL_145%=:
s_bitcmp1_b32 s69, 11
s_cbranch_scc0 .LL_146%=
buffer_load_dwordx4 a[44:47], v1, s[8:11], s79 offen offset:3072 sc1
.LL_146%=:
s_bitcmp1_b32 s69, 12
s_cbranch_scc0 .LL_147%=
buffer_load_dwordx4 a[48:51], v19, s[8:11], s79 offen offset:0 sc1
.LL_147%=:
s_bitcmp1_b32 s69, 13
s_cbranch_scc0 .LL_148%=
buffer_load_dwordx4 a[52:55], v19, s[8:11], s79 offen offset:1024 sc1
.LL_148%=:
s_bitcmp1_b32 s69, 14
s_cbranch_scc0 .LL_149%=
buffer_load_dwordx4 a[56:59], v19, s[8:11], s79 offen offset:2048 sc1
.LL_149%=:
s_bitcmp1_b32 s69, 15
s_cbranch_scc0 .LL_150%=
buffer_load_dwordx4 a[60:63], v19, s[8:11], s79 offen offset:3072 sc1
.LL_150%=:
.LL_142%=:
s_or_b32 s71, s71, s69
.LL_132%=:
s_andn2_b32 s80, 0xffff, s71
s_cbranch_scc0 .LL_131%=
s_sleep 4
buffer_load_dwordx2 v[16:17], v18, s[8:11], s63 offen sc1
s_and_b32 s80, s70, 0x100ff
s_cmp_eq_u32 s80, 0xff
s_cbranch_scc0 .LL_130%=
Zvva 80,4,0
Zvva 84,8,0
Mvva 80,4,4
Mvva 84,8,4
Mvva 80,4,8
Mvva 84,8,8
Mvva 80,4,12
Mvva 84,8,12
Mvva 80,4,16
Mvva 84,8,16
Mvva 80,4,20
Mvva 84,8,20
Mvva 80,4,24
Mvva 84,8,24
Mvva 80,4,28
Mvva 84,8,28
s_nop 6
K 160,80,81
K 161,82,83
K 162,84,85
K 163,86,87
v_lshlrev_b32 v80, 16, v160
v_and_b32 v81, 0xffff0000, v160
v_lshlrev_b32 v82, 16, v161
v_and_b32 v83, 0xffff0000, v161
v_lshlrev_b32 v84, 16, v162
v_and_b32 v85, 0xffff0000, v162
v_lshlrev_b32 v86, 16, v163
v_and_b32 v87, 0xffff0000, v163
v_pk_add_f32 v[80:81], v[32:33], v[80:81]
v_pk_add_f32 v[82:83], v[34:35], v[82:83]
v_pk_add_f32 v[84:85], v[36:37], v[84:85]
v_pk_add_f32 v[86:87], v[38:39], v[86:87]
K 160,80,81
K 161,82,83
K 162,84,85
K 163,86,87
v_lshlrev_b32 v80, 16, v160
v_and_b32 v81, 0xffff0000, v160
v_lshlrev_b32 v82, 16, v161
v_and_b32 v83, 0xffff0000, v161
v_lshlrev_b32 v84, 16, v162
v_and_b32 v85, 0xffff0000, v162
v_lshlrev_b32 v86, 16, v163
v_and_b32 v87, 0xffff0000, v163
v_pk_add_f32 v[80:81], v[40:41], v[80:81]
v_pk_add_f32 v[82:83], v[42:43], v[82:83]
v_pk_add_f32 v[84:85], v[44:45], v[84:85]
v_pk_add_f32 v[86:87], v[46:47], v[86:87]
K 80,80,81
K 81,82,83
K 82,84,85
K 83,86,87
buffer_store_dwordx4 v[80:83], v20, s[20:23], s72 offen nt
s_bitset1_b32 s71, 16
.LL_130%=:
s_add_u32 s84, s84, 1
s_waitcnt vmcnt(0)
v_cmp_eq_u64 vcc, s[12:13], v[16:17]
s_or_b32 s67, vcc_lo, s66
s_cmp_lt_u32 s84, 4194304
s_cbranch_scc1 .LL_129%=
.LL_131%=:
s_bitcmp1_b32 s71, 16
s_cbranch_scc1 .LL_151%=
s_and_b32 s80, s70, 0xff
s_cmp_eq_u32 s80, 0xff
s_cbranch_scc1 .LL_152%=
s_waitcnt vmcnt(0)
.LL_152%=:
Zvva 80,4,0
Zvva 84,8,0
Mvva 80,4,4
Mvva 84,8,4
Mvva 80,4,8
Mvva 84,8,8
Mvva 80,4,12
Mvva 84,8,12
Mvva 80,4,16
Mvva 84,8,16
Mvva 80,4,20
Mvva 84,8,20
Mvva 80,4,24
Mvva 84,8,24
Mvva 80,4,28
Mvva 84,8,28
s_nop 6
K 160,80,81
K 161,82,83
K 162,84,85
K 163,86,87
v_lshlrev_b32 v80, 16, v160
v_and_b32 v81, 0xffff0000, v160
v_lshlrev_b32 v82, 16, v161
v_and_b32 v83, 0xffff0000, v161
v_lshlrev_b32 v84, 16, v162
v_and_b32 v85, 0xffff0000, v162
v_lshlrev_b32 v86, 16, v163
v_and_b32 v87, 0xffff0000, v163
v_pk_add_f32 v[80:81], v[32:33], v[80:81]
v_pk_add_f32 v[82:83], v[34:35], v[82:83]
v_pk_add_f32 v[84:85], v[36:37], v[84:85]
v_pk_add_f32 v[86:87], v[38:39], v[86:87]
K 160,80,81
K 161,82,83
K 162,84,85
K 163,86,87
v_lshlrev_b32 v80, 16, v160
v_and_b32 v81, 0xffff0000, v160
v_lshlrev_b32 v82, 16, v161
v_and_b32 v83, 0xffff0000, v161
v_lshlrev_b32 v84, 16, v162
v_and_b32 v85, 0xffff0000, v162
v_lshlrev_b32 v86, 16, v163
v_and_b32 v87, 0xffff0000, v163
v_pk_add_f32 v[80:81], v[40:41], v[80:81]
v_pk_add_f32 v[82:83], v[42:43], v[82:83]
v_pk_add_f32 v[84:85], v[44:45], v[84:85]
v_pk_add_f32 v[86:87], v[46:47], v[86:87]
K 80,80,81
K 81,82,83
K 82,84,85
K 83,86,87
buffer_store_dwordx4 v[80:83], v20, s[20:23], s72 offen nt
s_waitcnt vmcnt(1)
s_nop 0
Zvva 80,4,32
Zvva 84,8,32
Mvva 80,4,36
Mvva 84,8,36
Mvva 80,4,40
Mvva 84,8,40
Mvva 80,4,44
Mvva 84,8,44
Mvva 80,4,48
Mvva 84,8,48
Mvva 80,4,52
Mvva 84,8,52
Mvva 80,4,56
Mvva 84,8,56
Mvva 80,4,60
Mvva 84,8,60
s_nop 6
K 160,80,81
K 161,82,83
K 162,84,85
K 163,86,87
v_lshlrev_b32 v80, 16, v160
v_and_b32 v81, 0xffff0000, v160
v_lshlrev_b32 v82, 16, v161
v_and_b32 v83, 0xffff0000, v161
v_lshlrev_b32 v84, 16, v162
v_and_b32 v85, 0xffff0000, v162
v_lshlrev_b32 v86, 16, v163
v_and_b32 v87, 0xffff0000, v163
v_pk_add_f32 v[80:81], v[48:49], v[80:81]
v_pk_add_f32 v[82:83], v[50:51], v[82:83]
v_pk_add_f32 v[84:85], v[52:53], v[84:85]
v_pk_add_f32 v[86:87], v[54:55], v[86:87]
K 160,80,81
K 161,82,83
K 162,84,85
K 163,86,87
v_lshlrev_b32 v80, 16, v160
v_and_b32 v81, 0xffff0000, v160
v_lshlrev_b32 v82, 16, v161
v_and_b32 v83, 0xffff0000, v161
v_lshlrev_b32 v84, 16, v162
v_and_b32 v85, 0xffff0000, v162
v_lshlrev_b32 v86, 16, v163
v_and_b32 v87, 0xffff0000, v163
v_pk_add_f32 v[80:81], v[56:57], v[80:81]
v_pk_add_f32 v[82:83], v[58:59], v[82:83]
v_pk_add_f32 v[84:85], v[60:61], v[84:85]
v_pk_add_f32 v[86:87], v[62:63], v[86:87]
K 80,80,81
K 81,82,83
K 82,84,85
K 83,86,87
buffer_store_dwordx4 v[80:83], v20, s[20:23], s73 offen nt
s_branch .LL_126%=
.LL_151%=:
s_waitcnt vmcnt(0)
Zvva 80,4,32
Zvva 84,8,32
Mvva 80,4,36
Mvva 84,8,36
Mvva 80,4,40
Mvva 84,8,40
Mvva 80,4,44
Mvva 84,8,44
Mvva 80,4,48
Mvva 84,8,48
Mvva 80,4,52
Mvva 84,8,52
Mvva 80,4,56
Mvva 84,8,56
Mvva 80,4,60
Mvva 84,8,60
s_nop 6
K 160,80,81
K 161,82,83
K 162,84,85
K 163,86,87
v_lshlrev_b32 v80, 16, v160
v_and_b32 v81, 0xffff0000, v160
v_lshlrev_b32 v82, 16, v161
v_and_b32 v83, 0xffff0000, v161
v_lshlrev_b32 v84, 16, v162
v_and_b32 v85, 0xffff0000, v162
v_lshlrev_b32 v86, 16, v163
v_and_b32 v87, 0xffff0000, v163
v_pk_add_f32 v[80:81], v[48:49], v[80:81]
v_pk_add_f32 v[82:83], v[50:51], v[82:83]
v_pk_add_f32 v[84:85], v[52:53], v[84:85]
v_pk_add_f32 v[86:87], v[54:55], v[86:87]
K 160,80,81
K 161,82,83
K 162,84,85
K 163,86,87
v_lshlrev_b32 v80, 16, v160
v_and_b32 v81, 0xffff0000, v160
v_lshlrev_b32 v82, 16, v161
v_and_b32 v83, 0xffff0000, v161
v_lshlrev_b32 v84, 16, v162
v_and_b32 v85, 0xffff0000, v162
v_lshlrev_b32 v86, 16, v163
v_and_b32 v87, 0xffff0000, v163
v_pk_add_f32 v[80:81], v[56:57], v[80:81]
v_pk_add_f32 v[82:83], v[58:59], v[82:83]
v_pk_add_f32 v[84:85], v[60:61], v[84:85]
v_pk_add_f32 v[86:87], v[62:63], v[86:87]
K 80,80,81
K 81,82,83
K 82,84,85
K 83,86,87
buffer_store_dwordx4 v[80:83], v20, s[20:23], s73 offen nt
s_branch .LL_126%=
.LL_127%=:
s_mov_b32 s84, 0
.LL_153%=:
s_mov_b32 s70, s71
s_andn2_b32 s69, s67, s71
s_and_b32 s69, s69, 0xffff
)"
R"(s_cbranch_scc0 .LL_156%=
s_and_b32 s80, s69, 255
s_cbranch_scc0 .LL_157%=
s_bitcmp1_b32 s69, 0
s_cbranch_scc0 .LL_158%=
buffer_load_dwordx4 a[0:3], v1, s[8:11], s78 offen offset:0 sc1
buffer_load_dwordx4 a[216:219], v1, s[8:11], s65 offen offset:0 sc1
.LL_158%=:
s_bitcmp1_b32 s69, 1
s_cbranch_scc0 .LL_159%=
buffer_load_dwordx4 a[4:7], v1, s[8:11], s78 offen offset:1024 sc1
buffer_load_dwordx4 a[220:223], v1, s[8:11], s65 offen offset:1024 sc1
.LL_159%=:
s_bitcmp1_b32 s69, 2
s_cbranch_scc0 .LL_160%=
buffer_load_dwordx4 a[8:11], v1, s[8:11], s78 offen offset:2048 sc1
buffer_load_dwordx4 a[224:227], v1, s[8:11], s65 offen offset:2048 sc1
.LL_160%=:
s_bitcmp1_b32 s69, 3
s_cbranch_scc0 .LL_161%=
buffer_load_dwordx4 a[12:15], v1, s[8:11], s78 offen offset:3072 sc1
buffer_load_dwordx4 a[228:231], v1, s[8:11], s65 offen offset:3072 sc1
.LL_161%=:
s_bitcmp1_b32 s69, 4
s_cbranch_scc0 .LL_162%=
buffer_load_dwordx4 a[16:19], v19, s[8:11], s78 offen offset:0 sc1
buffer_load_dwordx4 a[232:235], v19, s[8:11], s65 offen offset:0 sc1
.LL_162%=:
s_bitcmp1_b32 s69, 5
s_cbranch_scc0 .LL_163%=
buffer_load_dwordx4 a[20:23], v19, s[8:11], s78 offen offset:1024 sc1
buffer_load_dwordx4 a[236:239], v19, s[8:11], s65 offen offset:1024 sc1
.LL_163%=:
s_bitcmp1_b32 s69, 6
s_cbranch_scc0 .LL_164%=
buffer_load_dwordx4 a[24:27], v19, s[8:11], s78 offen offset:2048 sc1
buffer_load_dwordx4 a[240:243], v19, s[8:11], s65 offen offset:2048 sc1
.LL_164%=:
s_bitcmp1_b32 s69, 7
s_cbranch_scc0 .LL_165%=
buffer_load_dwordx4 a[28:31], v19, s[8:11], s78 offen offset:3072 sc1
buffer_load_dwordx4 a[244:247], v19, s[8:11], s65 offen offset:3072 sc1
.LL_165%=:
.LL_157%=:
s_and_b32 s80, s69, 65280
s_cbranch_scc0 .LL_166%=
s_bitcmp1_b32 s69, 8
s_cbranch_scc0 .LL_167%=
buffer_load_dwordx4 a[32:35], v1, s[8:11], s79 offen offset:0 sc1
.LL_167%=:
s_bitcmp1_b32 s69, 9
s_cbranch_scc0 .LL_168%=
buffer_load_dwordx4 a[36:39], v1, s[8:11], s79 offen offset:1024 sc1
.LL_168%=:
s_bitcmp1_b32 s69, 10
s_cbranch_scc0 .LL_169%=
buffer_load_dwordx4 a[40:43], v1, s[8:11], s79 offen offset:2048 sc1
.LL_169%=:
s_bitcmp1_b32 s69, 11
s_cbranch_scc0 .LL_170%=
buffer_load_dwordx4 a[44:47], v1, s[8:11], s79 offen offset:3072 sc1
.LL_170%=:
s_bitcmp1_b32 s69, 12
s_cbranch_scc0 .LL_171%=
buffer_load_dwordx4 a[48:51], v19, s[8:11], s79 offen offset:0 sc1
.LL_171%=:
s_bitcmp1_b32 s69, 13
s_cbranch_scc0 .LL_172%=
buffer_load_dwordx4 a[52:55], v19, s[8:11], s79 offen offset:1024 sc1
.LL_172%=:
s_bitcmp1_b32 s69, 14
s_cbranch_scc0 .LL_173%=
buffer_load_dwordx4 a[56:59], v19, s[8:11], s79 offen offset:2048 sc1
.LL_173%=:
s_bitcmp1_b32 s69, 15
s_cbranch_scc0 .LL_174%=
buffer_load_dwordx4 a[60:63], v19, s[8:11], s79 offen offset:3072 sc1
.LL_174%=:
.LL_166%=:
s_or_b32 s71, s71, s69
.LL_156%=:
s_andn2_b32 s80, 0xffff, s71
s_cbranch_scc0 .LL_155%=
s_sleep 4
buffer_load_dwordx2 v[16:17], v18, s[8:11], s63 offen sc1
s_and_b32 s80, s70, 0x100ff
s_cmp_eq_u32 s80, 0xff
s_cbranch_scc0 .LL_154%=
Zvva 80,4,0
Zvva 84,8,0
Zvva 88,4,216
Zvva 92,8,216
Mvva 80,4,4
Mvva 84,8,4
Mvva 88,4,220
Mvva 92,8,220
Mvva 80,4,8
Mvva 84,8,8
Mvva 88,4,224
Mvva 92,8,224
Mvva 80,4,12
Mvva 84,8,12
Mvva 88,4,228
Mvva 92,8,228
Mvva 80,4,16
Mvva 84,8,16
Mvva 88,4,232
Mvva 92,8,232
Mvva 80,4,20
Mvva 84,8,20
Mvva 88,4,236
Mvva 92,8,236
Mvva 80,4,24
Mvva 84,8,24
Mvva 88,4,240
Mvva 92,8,240
Mvva 80,4,28
Mvva 84,8,28
Mvva 88,4,244
Mvva 92,8,244
s_nop 4
K 160,80,81
K 161,82,83
K 162,84,85
K 163,86,87
v_lshlrev_b32 v80, 16, v160
v_and_b32 v81, 0xffff0000, v160
v_lshlrev_b32 v82, 16, v161
v_and_b32 v83, 0xffff0000, v161
v_lshlrev_b32 v84, 16, v162
v_and_b32 v85, 0xffff0000, v162
v_lshlrev_b32 v86, 16, v163
v_and_b32 v87, 0xffff0000, v163
v_pk_add_f32 v[80:81], v[32:33], v[80:81]
v_pk_add_f32 v[82:83], v[34:35], v[82:83]
v_pk_add_f32 v[84:85], v[36:37], v[84:85]
v_pk_add_f32 v[86:87], v[38:39], v[86:87]
K 160,80,81
K 161,82,83
K 162,84,85
K 163,86,87
v_lshlrev_b32 v80, 16, v160
v_and_b32 v81, 0xffff0000, v160
v_lshlrev_b32 v82, 16, v161
v_and_b32 v83, 0xffff0000, v161
v_lshlrev_b32 v84, 16, v162
v_and_b32 v85, 0xffff0000, v162
v_lshlrev_b32 v86, 16, v163
v_and_b32 v87, 0xffff0000, v163
v_pk_add_f32 v[80:81], v[40:41], v[80:81]
v_pk_add_f32 v[82:83], v[42:43], v[82:83]
v_pk_add_f32 v[84:85], v[44:45], v[84:85]
v_pk_add_f32 v[86:87], v[46:47], v[86:87]
K 80,80,81
K 81,82,83
K 82,84,85
K 83,86,87
buffer_store_dwordx4 v[80:83], v20, s[20:23], s72 offen nt
K 160,88,89
K 161,90,91
K 162,92,93
K 163,94,95
v_lshlrev_b32 v88, 16, v160
v_and_b32 v89, 0xffff0000, v160
v_lshlrev_b32 v90, 16, v161
v_and_b32 v91, 0xffff0000, v161
v_lshlrev_b32 v92, 16, v162
v_and_b32 v93, 0xffff0000, v162
v_lshlrev_b32 v94, 16, v163
v_and_b32 v95, 0xffff0000, v163
v_pk_add_f32 v[88:89], v[64:65], v[88:89]
v_pk_add_f32 v[90:91], v[66:67], v[90:91]
v_pk_add_f32 v[92:93], v[68:69], v[92:93]
v_pk_add_f32 v[94:95], v[70:71], v[94:95]
K 160,88,89
K 161,90,91
K 162,92,93
K 163,94,95
v_lshlrev_b32 v88, 16, v160
v_and_b32 v89, 0xffff0000, v160
v_lshlrev_b32 v90, 16, v161
v_and_b32 v91, 0xffff0000, v161
v_lshlrev_b32 v92, 16, v162
v_and_b32 v93, 0xffff0000, v162
v_lshlrev_b32 v94, 16, v163
v_and_b32 v95, 0xffff0000, v163
v_pk_add_f32 v[88:89], v[72:73], v[88:89]
v_pk_add_f32 v[90:91], v[74:75], v[90:91]
v_pk_add_f32 v[92:93], v[76:77], v[92:93]
v_pk_add_f32 v[94:95], v[78:79], v[94:95]
K 88,88,89
K 89,90,91
K 90,92,93
K 91,94,95
buffer_store_dwordx4 v[88:91], v20, s[20:23], s74 offen nt
s_bitset1_b32 s71, 16
.LL_154%=:
s_add_u32 s84, s84, 1
s_waitcnt vmcnt(0)
v_cmp_eq_u64 vcc, s[12:13], v[16:17]
s_or_b32 s67, vcc_lo, s66
s_cmp_lt_u32 s84, 4194304
s_cbranch_scc1 .LL_153%=
.LL_155%=:
s_bitcmp1_b32 s71, 16
s_cbranch_scc1 .LL_175%=
s_and_b32 s80, s70, 0xff
s_cmp_eq_u32 s80, 0xff
s_cbranch_scc1 .LL_176%=
s_waitcnt vmcnt(0)
.LL_176%=:
Zvva 80,4,0
Zvva 84,8,0
Zvva 88,4,216
Zvva 92,8,216
Mvva 80,4,4
Mvva 84,8,4
Mvva 88,4,220
Mvva 92,8,220
Mvva 80,4,8
Mvva 84,8,8
Mvva 88,4,224
Mvva 92,8,224
Mvva 80,4,12
Mvva 84,8,12
Mvva 88,4,228
Mvva 92,8,228
Mvva 80,4,16
Mvva 84,8,16
Mvva 88,4,232
Mvva 92,8,232
Mvva 80,4,20
Mvva 84,8,20
Mvva 88,4,236
Mvva 92,8,236
Mvva 80,4,24
Mvva 84,8,24
Mvva 88,4,240
Mvva 92,8,240
Mvva 80,4,28
Mvva 84,8,28
Mvva 88,4,244
Mvva 92,8,244
s_nop 4
K 160,80,81
K 161,82,83
K 162,84,85
K 163,86,87
v_lshlrev_b32 v80, 16, v160
v_and_b32 v81, 0xffff0000, v160
v_lshlrev_b32 v82, 16, v161
v_and_b32 v83, 0xffff0000, v161
v_lshlrev_b32 v84, 16, v162
v_and_b32 v85, 0xffff0000, v162
v_lshlrev_b32 v86, 16, v163
v_and_b32 v87, 0xffff0000, v163
v_pk_add_f32 v[80:81], v[32:33], v[80:81]
v_pk_add_f32 v[82:83], v[34:35], v[82:83]
v_pk_add_f32 v[84:85], v[36:37], v[84:85]
v_pk_add_f32 v[86:87], v[38:39], v[86:87]
K 160,80,81
K 161,82,83
K 162,84,85
K 163,86,87
v_lshlrev_b32 v80, 16, v160
v_and_b32 v81, 0xffff0000, v160
v_lshlrev_b32 v82, 16, v161
v_and_b32 v83, 0xffff0000, v161
v_lshlrev_b32 v84, 16, v162
v_and_b32 v85, 0xffff0000, v162
v_lshlrev_b32 v86, 16, v163
v_and_b32 v87, 0xffff0000, v163
v_pk_add_f32 v[80:81], v[40:41], v[80:81]
v_pk_add_f32 v[82:83], v[42:43], v[82:83]
v_pk_add_f32 v[84:85], v[44:45], v[84:85]
v_pk_add_f32 v[86:87], v[46:47], v[86:87]
K 80,80,81
K 81,82,83
K 82,84,85
K 83,86,87
buffer_store_dwordx4 v[80:83], v20, s[20:23], s72 offen nt
K 160,88,89
K 161,90,91
K 162,92,93
K 163,94,95
v_lshlrev_b32 v88, 16, v160
v_and_b32 v89, 0xffff0000, v160
v_lshlrev_b32 v90, 16, v161
v_and_b32 v91, 0xffff0000, v161
v_lshlrev_b32 v92, 16, v162
v_and_b32 v93, 0xffff0000, v162
v_lshlrev_b32 v94, 16, v163
v_and_b32 v95, 0xffff0000, v163
v_pk_add_f32 v[88:89], v[64:65], v[88:89]
v_pk_add_f32 v[90:91], v[66:67], v[90:91]
v_pk_add_f32 v[92:93], v[68:69], v[92:93]
v_pk_add_f32 v[94:95], v[70:71], v[94:95]
K 160,88,89
K 161,90,91
K 162,92,93
K 163,94,95
v_lshlrev_b32 v88, 16, v160
v_and_b32 v89, 0xffff0000, v160
v_lshlrev_b32 v90, 16, v161
v_and_b32 v91, 0xffff0000, v161
v_lshlrev_b32 v92, 16, v162
v_and_b32 v93, 0xffff0000, v162
v_lshlrev_b32 v94, 16, v163
v_and_b32 v95, 0xffff0000, v163
v_pk_add_f32 v[88:89], v[72:73], v[88:89]
v_pk_add_f32 v[90:91], v[74:75], v[90:91]
v_pk_add_f32 v[92:93], v[76:77], v[92:93]
v_pk_add_f32 v[94:95], v[78:79], v[94:95]
K 88,88,89
K 89,90,91
K 90,92,93
K 91,94,95
buffer_store_dwordx4 v[88:91], v20, s[20:23], s74 offen nt
s_waitcnt vmcnt(2)
Zvva 80,4,32
Zvva 84,8,32
Mvva 80,4,36
Mvva 84,8,36
Mvva 80,4,40
Mvva 84,8,40
Mvva 80,4,44
Mvva 84,8,44
Mvva 80,4,48
Mvva 84,8,48
Mvva 80,4,52
Mvva 84,8,52
Mvva 80,4,56
Mvva 84,8,56
Mvva 80,4,60
Mvva 84,8,60
s_nop 6
K 160,80,81
K 161,82,83
K 162,84,85
K 163,86,87
v_lshlrev_b32 v80, 16, v160
v_and_b32 v81, 0xffff0000, v160
v_lshlrev_b32 v82, 16, v161
v_and_b32 v83, 0xffff0000, v161
v_lshlrev_b32 v84, 16, v162
v_and_b32 v85, 0xffff0000, v162
v_lshlrev_b32 v86, 16, v163
v_and_b32 v87, 0xffff0000, v163
v_pk_add_f32 v[80:81], v[48:49], v[80:81]
v_pk_add_f32 v[82:83], v[50:51], v[82:83]
v_pk_add_f32 v[84:85], v[52:53], v[84:85]
v_pk_add_f32 v[86:87], v[54:55], v[86:87]
K 160,80,81
K 161,82,83
K 162,84,85
K 163,86,87
v_lshlrev_b32 v80, 16, v160
v_and_b32 v81, 0xffff0000, v160
v_lshlrev_b32 v82, 16, v161
v_and_b32 v83, 0xffff0000, v161
v_lshlrev_b32 v84, 16, v162
v_and_b32 v85, 0xffff0000, v162
v_lshlrev_b32 v86, 16, v163
v_and_b32 v87, 0xffff0000, v163
v_pk_add_f32 v[80:81], v[56:57], v[80:81]
v_pk_add_f32 v[82:83], v[58:59], v[82:83]
v_pk_add_f32 v[84:85], v[60:61], v[84:85]
v_pk_add_f32 v[86:87], v[62:63], v[86:87]
K 80,80,81
K 81,82,83
K 82,84,85
K 83,86,87
buffer_store_dwordx4 v[80:83], v20, s[20:23], s73 offen nt
s_branch .LL_126%=
.LL_175%=:
s_waitcnt vmcnt(0)
Zvva 80,4,32
Zvva 84,8,32
Mvva 80,4,36
Mvva 84,8,36
Mvva 80,4,40
Mvva 84,8,40
Mvva 80,4,44
Mvva 84,8,44
Mvva 80,4,48
Mvva 84,8,48
Mvva 80,4,52
Mvva 84,8,52
Mvva 80,4,56
Mvva 84,8,56
Mvva 80,4,60
Mvva 84,8,60
s_nop 6
K 160,80,81
K 161,82,83
K 162,84,85
K 163,86,87
v_lshlrev_b32 v80, 16, v160
v_and_b32 v81, 0xffff0000, v160
v_lshlrev_b32 v82, 16, v161
)"
R"(v_and_b32 v83, 0xffff0000, v161
v_lshlrev_b32 v84, 16, v162
v_and_b32 v85, 0xffff0000, v162
v_lshlrev_b32 v86, 16, v163
v_and_b32 v87, 0xffff0000, v163
v_pk_add_f32 v[80:81], v[48:49], v[80:81]
v_pk_add_f32 v[82:83], v[50:51], v[82:83]
v_pk_add_f32 v[84:85], v[52:53], v[84:85]
v_pk_add_f32 v[86:87], v[54:55], v[86:87]
K 160,80,81
K 161,82,83
K 162,84,85
K 163,86,87
v_lshlrev_b32 v80, 16, v160
v_and_b32 v81, 0xffff0000, v160
v_lshlrev_b32 v82, 16, v161
v_and_b32 v83, 0xffff0000, v161
v_lshlrev_b32 v84, 16, v162
v_and_b32 v85, 0xffff0000, v162
v_lshlrev_b32 v86, 16, v163
v_and_b32 v87, 0xffff0000, v163
v_pk_add_f32 v[80:81], v[56:57], v[80:81]
v_pk_add_f32 v[82:83], v[58:59], v[82:83]
v_pk_add_f32 v[84:85], v[60:61], v[84:85]
v_pk_add_f32 v[86:87], v[62:63], v[86:87]
K 80,80,81
K 81,82,83
K 82,84,85
K 83,86,87
buffer_store_dwordx4 v[80:83], v20, s[20:23], s73 offen nt
s_branch .LL_126%=
.LL_128%=:
s_mov_b32 s84, 0
.LL_177%=:
s_mov_b32 s70, s71
s_andn2_b32 s69, s67, s71
s_and_b32 s69, s69, 0xffff
s_cbranch_scc0 .LL_180%=
s_and_b32 s80, s69, 255
s_cbranch_scc0 .LL_181%=
s_bitcmp1_b32 s69, 0
s_cbranch_scc0 .LL_182%=
buffer_load_dwordx4 a[0:3], v1, s[8:11], s78 offen offset:0 sc1
.LL_182%=:
s_bitcmp1_b32 s69, 1
s_cbranch_scc0 .LL_183%=
buffer_load_dwordx4 a[4:7], v1, s[8:11], s78 offen offset:1024 sc1
.LL_183%=:
s_bitcmp1_b32 s69, 2
s_cbranch_scc0 .LL_184%=
buffer_load_dwordx4 a[8:11], v1, s[8:11], s78 offen offset:2048 sc1
.LL_184%=:
s_bitcmp1_b32 s69, 3
s_cbranch_scc0 .LL_185%=
buffer_load_dwordx4 a[12:15], v1, s[8:11], s78 offen offset:3072 sc1
.LL_185%=:
s_bitcmp1_b32 s69, 4
s_cbranch_scc0 .LL_186%=
buffer_load_dwordx4 a[16:19], v19, s[8:11], s78 offen offset:0 sc1
.LL_186%=:
s_bitcmp1_b32 s69, 5
s_cbranch_scc0 .LL_187%=
buffer_load_dwordx4 a[20:23], v19, s[8:11], s78 offen offset:1024 sc1
.LL_187%=:
s_bitcmp1_b32 s69, 6
s_cbranch_scc0 .LL_188%=
buffer_load_dwordx4 a[24:27], v19, s[8:11], s78 offen offset:2048 sc1
.LL_188%=:
s_bitcmp1_b32 s69, 7
s_cbranch_scc0 .LL_189%=
buffer_load_dwordx4 a[28:31], v19, s[8:11], s78 offen offset:3072 sc1
.LL_189%=:
.LL_181%=:
s_and_b32 s80, s69, 65280
s_cbranch_scc0 .LL_190%=
s_bitcmp1_b32 s69, 8
s_cbranch_scc0 .LL_191%=
buffer_load_dwordx4 a[32:35], v1, s[8:11], s79 offen offset:0 sc1
buffer_load_dwordx4 a[216:219], v1, s[8:11], s65 offen offset:0 sc1
.LL_191%=:
s_bitcmp1_b32 s69, 9
s_cbranch_scc0 .LL_192%=
buffer_load_dwordx4 a[36:39], v1, s[8:11], s79 offen offset:1024 sc1
buffer_load_dwordx4 a[220:223], v1, s[8:11], s65 offen offset:1024 sc1
.LL_192%=:
s_bitcmp1_b32 s69, 10
s_cbranch_scc0 .LL_193%=
buffer_load_dwordx4 a[40:43], v1, s[8:11], s79 offen offset:2048 sc1
buffer_load_dwordx4 a[224:227], v1, s[8:11], s65 offen offset:2048 sc1
.LL_193%=:
s_bitcmp1_b32 s69, 11
s_cbranch_scc0 .LL_194%=
buffer_load_dwordx4 a[44:47], v1, s[8:11], s79 offen offset:3072 sc1
buffer_load_dwordx4 a[228:231], v1, s[8:11], s65 offen offset:3072 sc1
.LL_194%=:
s_bitcmp1_b32 s69, 12
s_cbranch_scc0 .LL_195%=
buffer_load_dwordx4 a[48:51], v19, s[8:11], s79 offen offset:0 sc1
buffer_load_dwordx4 a[232:235], v19, s[8:11], s65 offen offset:0 sc1
.LL_195%=:
s_bitcmp1_b32 s69, 13
s_cbranch_scc0 .LL_196%=
buffer_load_dwordx4 a[52:55], v19, s[8:11], s79 offen offset:1024 sc1
buffer_load_dwordx4 a[236:239], v19, s[8:11], s65 offen offset:1024 sc1
.LL_196%=:
s_bitcmp1_b32 s69, 14
s_cbranch_scc0 .LL_197%=
buffer_load_dwordx4 a[56:59], v19, s[8:11], s79 offen offset:2048 sc1
buffer_load_dwordx4 a[240:243], v19, s[8:11], s65 offen offset:2048 sc1
.LL_197%=:
s_bitcmp1_b32 s69, 15
s_cbranch_scc0 .LL_198%=
buffer_load_dwordx4 a[60:63], v19, s[8:11], s79 offen offset:3072 sc1
buffer_load_dwordx4 a[244:247], v19, s[8:11], s65 offen offset:3072 sc1
.LL_198%=:
.LL_190%=:
s_or_b32 s71, s71, s69
.LL_180%=:
s_andn2_b32 s80, 0xffff, s71
s_cbranch_scc0 .LL_179%=
s_sleep 4
buffer_load_dwordx2 v[16:17], v18, s[8:11], s63 offen sc1
s_and_b32 s80, s70, 0x100ff
s_cmp_eq_u32 s80, 0xff
s_cbranch_scc0 .LL_178%=
Zvva 80,4,0
Zvva 84,8,0
Mvva 80,4,4
Mvva 84,8,4
Mvva 80,4,8
Mvva 84,8,8
Mvva 80,4,12
Mvva 84,8,12
Mvva 80,4,16
Mvva 84,8,16
Mvva 80,4,20
Mvva 84,8,20
Mvva 80,4,24
Mvva 84,8,24
Mvva 80,4,28
Mvva 84,8,28
s_nop 6
K 160,80,81
K 161,82,83
K 162,84,85
K 163,86,87
v_lshlrev_b32 v80, 16, v160
v_and_b32 v81, 0xffff0000, v160
v_lshlrev_b32 v82, 16, v161
v_and_b32 v83, 0xffff0000, v161
v_lshlrev_b32 v84, 16, v162
v_and_b32 v85, 0xffff0000, v162
v_lshlrev_b32 v86, 16, v163
v_and_b32 v87, 0xffff0000, v163
v_pk_add_f32 v[80:81], v[32:33], v[80:81]
v_pk_add_f32 v[82:83], v[34:35], v[82:83]
v_pk_add_f32 v[84:85], v[36:37], v[84:85]
v_pk_add_f32 v[86:87], v[38:39], v[86:87]
K 160,80,81
K 161,82,83
K 162,84,85
K 163,86,87
v_lshlrev_b32 v80, 16, v160
v_and_b32 v81, 0xffff0000, v160
v_lshlrev_b32 v82, 16, v161
v_and_b32 v83, 0xffff0000, v161
v_lshlrev_b32 v84, 16, v162
v_and_b32 v85, 0xffff0000, v162
v_lshlrev_b32 v86, 16, v163
v_and_b32 v87, 0xffff0000, v163
v_pk_add_f32 v[80:81], v[40:41], v[80:81]
v_pk_add_f32 v[82:83], v[42:43], v[82:83]
v_pk_add_f32 v[84:85], v[44:45], v[84:85]
v_pk_add_f32 v[86:87], v[46:47], v[86:87]
K 80,80,81
K 81,82,83
K 82,84,85
K 83,86,87
buffer_store_dwordx4 v[80:83], v20, s[20:23], s72 offen nt
s_bitset1_b32 s71, 16
.LL_178%=:
s_add_u32 s84, s84, 1
s_waitcnt vmcnt(0)
v_cmp_eq_u64 vcc, s[12:13], v[16:17]
s_or_b32 s67, vcc_lo, s66
s_cmp_lt_u32 s84, 4194304
s_cbranch_scc1 .LL_177%=
.LL_179%=:
s_bitcmp1_b32 s71, 16
s_cbranch_scc1 .LL_199%=
s_and_b32 s80, s70, 0xff
s_cmp_eq_u32 s80, 0xff
s_cbranch_scc1 .LL_200%=
s_waitcnt vmcnt(0)
.LL_200%=:
Zvva 80,4,0
Zvva 84,8,0
Mvva 80,4,4
Mvva 84,8,4
Mvva 80,4,8
Mvva 84,8,8
Mvva 80,4,12
Mvva 84,8,12
Mvva 80,4,16
Mvva 84,8,16
Mvva 80,4,20
Mvva 84,8,20
Mvva 80,4,24
Mvva 84,8,24
Mvva 80,4,28
Mvva 84,8,28
s_nop 6
K 160,80,81
K 161,82,83
K 162,84,85
K 163,86,87
v_lshlrev_b32 v80, 16, v160
v_and_b32 v81, 0xffff0000, v160
v_lshlrev_b32 v82, 16, v161
v_and_b32 v83, 0xffff0000, v161
v_lshlrev_b32 v84, 16, v162
v_and_b32 v85, 0xffff0000, v162
v_lshlrev_b32 v86, 16, v163
v_and_b32 v87, 0xffff0000, v163
v_pk_add_f32 v[80:81], v[32:33], v[80:81]
v_pk_add_f32 v[82:83], v[34:35], v[82:83]
v_pk_add_f32 v[84:85], v[36:37], v[84:85]
v_pk_add_f32 v[86:87], v[38:39], v[86:87]
K 160,80,81
K 161,82,83
K 162,84,85
K 163,86,87
v_lshlrev_b32 v80, 16, v160
v_and_b32 v81, 0xffff0000, v160
v_lshlrev_b32 v82, 16, v161
v_and_b32 v83, 0xffff0000, v161
v_lshlrev_b32 v84, 16, v162
v_and_b32 v85, 0xffff0000, v162
v_lshlrev_b32 v86, 16, v163
v_and_b32 v87, 0xffff0000, v163
v_pk_add_f32 v[80:81], v[40:41], v[80:81]
v_pk_add_f32 v[82:83], v[42:43], v[82:83]
v_pk_add_f32 v[84:85], v[44:45], v[84:85]
v_pk_add_f32 v[86:87], v[46:47], v[86:87]
K 80,80,81
K 81,82,83
K 82,84,85
K 83,86,87
buffer_store_dwordx4 v[80:83], v20, s[20:23], s72 offen nt
s_waitcnt vmcnt(1)
s_nop 0
Zvva 80,4,32
Zvva 84,8,32
Zvva 88,4,216
Zvva 92,8,216
Mvva 80,4,36
Mvva 84,8,36
Mvva 88,4,220
Mvva 92,8,220
Mvva 80,4,40
Mvva 84,8,40
Mvva 88,4,224
Mvva 92,8,224
Mvva 80,4,44
Mvva 84,8,44
Mvva 88,4,228
Mvva 92,8,228
Mvva 80,4,48
Mvva 84,8,48
Mvva 88,4,232
Mvva 92,8,232
Mvva 80,4,52
Mvva 84,8,52
Mvva 88,4,236
Mvva 92,8,236
Mvva 80,4,56
Mvva 84,8,56
Mvva 88,4,240
Mvva 92,8,240
Mvva 80,4,60
Mvva 84,8,60
Mvva 88,4,244
Mvva 92,8,244
s_nop 4
K 160,80,81
K 161,82,83
K 162,84,85
K 163,86,87
v_lshlrev_b32 v80, 16, v160
v_and_b32 v81, 0xffff0000, v160
v_lshlrev_b32 v82, 16, v161
v_and_b32 v83, 0xffff0000, v161
v_lshlrev_b32 v84, 16, v162
v_and_b32 v85, 0xffff0000, v162
v_lshlrev_b32 v86, 16, v163
v_and_b32 v87, 0xffff0000, v163
v_pk_add_f32 v[80:81], v[48:49], v[80:81]
v_pk_add_f32 v[82:83], v[50:51], v[82:83]
v_pk_add_f32 v[84:85], v[52:53], v[84:85]
v_pk_add_f32 v[86:87], v[54:55], v[86:87]
K 160,80,81
K 161,82,83
K 162,84,85
K 163,86,87
v_lshlrev_b32 v80, 16, v160
v_and_b32 v81, 0xffff0000, v160
v_lshlrev_b32 v82, 16, v161
v_and_b32 v83, 0xffff0000, v161
v_lshlrev_b32 v84, 16, v162
v_and_b32 v85, 0xffff0000, v162
v_lshlrev_b32 v86, 16, v163
v_and_b32 v87, 0xffff0000, v163
v_pk_add_f32 v[80:81], v[56:57], v[80:81]
v_pk_add_f32 v[82:83], v[58:59], v[82:83]
v_pk_add_f32 v[84:85], v[60:61], v[84:85]
v_pk_add_f32 v[86:87], v[62:63], v[86:87]
K 80,80,81
K 81,82,83
K 82,84,85
K 83,86,87
buffer_store_dwordx4 v[80:83], v20, s[20:23], s73 offen nt
K 160,88,89
K 161,90,91
K 162,92,93
K 163,94,95
v_lshlrev_b32 v88, 16, v160
v_and_b32 v89, 0xffff0000, v160
v_lshlrev_b32 v90, 16, v161
v_and_b32 v91, 0xffff0000, v161
v_lshlrev_b32 v92, 16, v162
v_and_b32 v93, 0xffff0000, v162
v_lshlrev_b32 v94, 16, v163
v_and_b32 v95, 0xffff0000, v163
v_pk_add_f32 v[88:89], v[64:65], v[88:89]
v_pk_add_f32 v[90:91], v[66:67], v[90:91]
v_pk_add_f32 v[92:93], v[68:69], v[92:93]
v_pk_add_f32 v[94:95], v[70:71], v[94:95]
K 160,88,89
K 161,90,91
K 162,92,93
K 163,94,95
v_lshlrev_b32 v88, 16, v160
v_and_b32 v89, 0xffff0000, v160
v_lshlrev_b32 v90, 16, v161
v_and_b32 v91, 0xffff0000, v161
v_lshlrev_b32 v92, 16, v162
v_and_b32 v93, 0xffff0000, v162
v_lshlrev_b32 v94, 16, v163
v_and_b32 v95, 0xffff0000, v163
v_pk_add_f32 v[88:89], v[72:73], v[88:89]
v_pk_add_f32 v[90:91], v[74:75], v[90:91]
v_pk_add_f32 v[92:93], v[76:77], v[92:93]
v_pk_add_f32 v[94:95], v[78:79], v[94:95]
K 88,88,89
K 89,90,91
K 90,92,93
K 91,94,95
buffer_store_dwordx4 v[88:91], v20, s[20:23], s74 offen nt
s_branch .LL_126%=
.LL_199%=:
s_waitcnt vmcnt(0)
Zvva 80,4,32
Zvva 84,8,32
Zvva 88,4,216
Zvva 92,8,216
Mvva 80,4,36
Mvva 84,8,36
Mvva 88,4,220
Mvva 92,8,220
Mvva 80,4,40
Mvva 84,8,40
Mvva 88,4,224
Mvva 92,8,224
Mvva 80,4,44
Mvva 84,8,44
Mvva 88,4,228
Mvva 92,8,228
Mvva 80,4,48
Mvva 84,8,48
Mvva 88,4,232
Mvva 92,8,232
Mvva 80,4,52
Mvva 84,8,52
Mvva 88,4,236
Mvva 92,8,236
Mvva 80,4,56
Mvva 84,8,56
Mvva 88,4,240
Mvva 92,8,240
Mvva 80,4,60
Mvva 84,8,60
Mvva 88,4,244
Mvva 92,8,244
s_nop 4
K 160,80,81
K 161,82,83
K 162,84,85
K 163,86,87
v_lshlrev_b32 v80, 16, v160
v_and_b32 v81, 0xffff0000, v160
)"
R"(v_lshlrev_b32 v82, 16, v161
v_and_b32 v83, 0xffff0000, v161
v_lshlrev_b32 v84, 16, v162
v_and_b32 v85, 0xffff0000, v162
v_lshlrev_b32 v86, 16, v163
v_and_b32 v87, 0xffff0000, v163
v_pk_add_f32 v[80:81], v[48:49], v[80:81]
v_pk_add_f32 v[82:83], v[50:51], v[82:83]
v_pk_add_f32 v[84:85], v[52:53], v[84:85]
v_pk_add_f32 v[86:87], v[54:55], v[86:87]
K 160,80,81
K 161,82,83
K 162,84,85
K 163,86,87
v_lshlrev_b32 v80, 16, v160
v_and_b32 v81, 0xffff0000, v160
v_lshlrev_b32 v82, 16, v161
v_and_b32 v83, 0xffff0000, v161
v_lshlrev_b32 v84, 16, v162
v_and_b32 v85, 0xffff0000, v162
v_lshlrev_b32 v86, 16, v163
v_and_b32 v87, 0xffff0000, v163
v_pk_add_f32 v[80:81], v[56:57], v[80:81]
v_pk_add_f32 v[82:83], v[58:59], v[82:83]
v_pk_add_f32 v[84:85], v[60:61], v[84:85]
v_pk_add_f32 v[86:87], v[62:63], v[86:87]
K 80,80,81
K 81,82,83
K 82,84,85
K 83,86,87
buffer_store_dwordx4 v[80:83], v20, s[20:23], s73 offen nt
K 160,88,89
K 161,90,91
K 162,92,93
K 163,94,95
v_lshlrev_b32 v88, 16, v160
v_and_b32 v89, 0xffff0000, v160
v_lshlrev_b32 v90, 16, v161
v_and_b32 v91, 0xffff0000, v161
v_lshlrev_b32 v92, 16, v162
v_and_b32 v93, 0xffff0000, v162
v_lshlrev_b32 v94, 16, v163
v_and_b32 v95, 0xffff0000, v163
v_pk_add_f32 v[88:89], v[64:65], v[88:89]
v_pk_add_f32 v[90:91], v[66:67], v[90:91]
v_pk_add_f32 v[92:93], v[68:69], v[92:93]
v_pk_add_f32 v[94:95], v[70:71], v[94:95]
K 160,88,89
K 161,90,91
K 162,92,93
K 163,94,95
v_lshlrev_b32 v88, 16, v160
v_and_b32 v89, 0xffff0000, v160
v_lshlrev_b32 v90, 16, v161
v_and_b32 v91, 0xffff0000, v161
v_lshlrev_b32 v92, 16, v162
v_and_b32 v93, 0xffff0000, v162
v_lshlrev_b32 v94, 16, v163
v_and_b32 v95, 0xffff0000, v163
v_pk_add_f32 v[88:89], v[72:73], v[88:89]
v_pk_add_f32 v[90:91], v[74:75], v[90:91]
v_pk_add_f32 v[92:93], v[76:77], v[92:93]
v_pk_add_f32 v[94:95], v[78:79], v[94:95]
K 88,88,89
K 89,90,91
K 90,92,93
K 91,94,95
buffer_store_dwordx4 v[88:91], v20, s[20:23], s74 offen nt
.LL_126%=:
s_lshl_b32 s80, s15, 2
s_add_u32 s80, s80, s14
s_cmp_eq_u32 s80, 4
s_cbranch_scc0 .LL_201%=
v_mov_b64 v[28:29], s[12:13]
s_mov_b64 exec, 1
buffer_store_dwordx2 v[28:29], off, s[8:11], 0 sc1
s_mov_b64 exec, -1
.LL_201%=:
s_branch .Lend_202%=
.Lup_119%=:
s_waitcnt vmcnt(3)
s_mov_b32 s84, 0
.LL_121%=:
v_min3_u32 v176, v161, v163, v165
v_max3_u32 v177, v161, v163, v165
v_min3_u32 v176, v176, v167, v169
v_max3_u32 v177, v177, v167, v169
v_min3_u32 v176, v176, v171, v173
v_max3_u32 v177, v177, v171, v173
v_min_u32 v176, v176, v175
v_max_u32 v177, v177, v175
v_cmp_eq_u32 vcc, s13, v176
v_cmp_eq_u32_e64 s[86:87], s13, v177
s_and_b64 vcc, vcc, s[86:87]
s_cmp_eq_u64 vcc, -1
s_cbranch_scc1 .LL_122%=
s_add_u32 s84, s84, 1
s_cmp_lt_u32 s84, 1048576
s_cbranch_scc1 .LL_123%=
.LL_122%=:
v_readlane_b32 s70, v12, s69
v_bfe_u32 v2, v1, 4, 4
s_nop 2
s_lshr_b32 s80, s70, 4
s_and_b32 s70, s70, 15
v_cmp_eq_u32 vcc, s70, v2
s_cmp_eq_u32 s80, s61
s_cselect_b64 s[86:87], vcc, 0
s_cselect_b64 s[88:89], 0, vcc
s_bitcmp1_b32 s14, 0
s_cselect_b64 vcc, s[88:89], s[86:87]
v_lshlrev_b32 v176, 16, v160
v_and_b32 v177, 0xffff0000, v160
v_lshlrev_b32 v178, 16, v162
v_and_b32 v179, 0xffff0000, v162
v_lshlrev_b32 v180, 16, v164
v_and_b32 v181, 0xffff0000, v164
v_lshlrev_b32 v182, 16, v166
v_and_b32 v183, 0xffff0000, v166
A 176,176,40
A 177,177,41
A 178,178,42
A 179,179,43
A 180,180,44
A 181,181,45
A 182,182,46
A 183,183,47
K 184,176,177
K 185,178,179
K 186,180,181
K 187,182,183
v_lshlrev_b32 v176, 16, v184
v_and_b32 v177, 0xffff0000, v184
v_lshlrev_b32 v178, 16, v185
v_and_b32 v179, 0xffff0000, v185
v_lshlrev_b32 v180, 16, v186
v_and_b32 v181, 0xffff0000, v186
v_lshlrev_b32 v182, 16, v187
v_and_b32 v183, 0xffff0000, v187
v_cndmask_b32_e64 v40, v40, v176, s[86:87]
v_cndmask_b32_e64 v41, v41, v177, s[86:87]
v_cndmask_b32_e64 v42, v42, v178, s[86:87]
v_cndmask_b32_e64 v43, v43, v179, s[86:87]
v_cndmask_b32_e64 v44, v44, v180, s[86:87]
v_cndmask_b32_e64 v45, v45, v181, s[86:87]
v_cndmask_b32_e64 v46, v46, v182, s[86:87]
v_cndmask_b32_e64 v47, v47, v183, s[86:87]
v_lshlrev_b32 v176, 16, v160
v_and_b32 v177, 0xffff0000, v160
v_lshlrev_b32 v178, 16, v162
v_and_b32 v179, 0xffff0000, v162
v_lshlrev_b32 v180, 16, v164
v_and_b32 v181, 0xffff0000, v164
v_lshlrev_b32 v182, 16, v166
v_and_b32 v183, 0xffff0000, v166
A 176,176,56
A 177,177,57
A 178,178,58
A 179,179,59
A 180,180,60
A 181,181,61
A 182,182,62
A 183,183,63
K 184,176,177
K 185,178,179
K 186,180,181
K 187,182,183
v_lshlrev_b32 v176, 16, v184
v_and_b32 v177, 0xffff0000, v184
v_lshlrev_b32 v178, 16, v185
v_and_b32 v179, 0xffff0000, v185
v_lshlrev_b32 v180, 16, v186
v_and_b32 v181, 0xffff0000, v186
v_lshlrev_b32 v182, 16, v187
v_and_b32 v183, 0xffff0000, v187
v_cndmask_b32_e64 v56, v56, v176, s[88:89]
v_cndmask_b32_e64 v57, v57, v177, s[88:89]
v_cndmask_b32_e64 v58, v58, v178, s[88:89]
v_cndmask_b32_e64 v59, v59, v179, s[88:89]
v_cndmask_b32_e64 v60, v60, v180, s[88:89]
v_cndmask_b32_e64 v61, v61, v181, s[88:89]
v_cndmask_b32_e64 v62, v62, v182, s[88:89]
v_cndmask_b32_e64 v63, v63, v183, s[88:89]
v_lshlrev_b32 v176, 16, v168
v_and_b32 v177, 0xffff0000, v168
v_lshlrev_b32 v178, 16, v170
v_and_b32 v179, 0xffff0000, v170
v_lshlrev_b32 v180, 16, v172
v_and_b32 v181, 0xffff0000, v172
v_lshlrev_b32 v182, 16, v174
v_and_b32 v183, 0xffff0000, v174
A 176,176,72
A 177,177,73
A 178,178,74
A 179,179,75
A 180,180,76
A 181,181,77
A 182,182,78
A 183,183,79
K 184,176,177
K 185,178,179
K 186,180,181
K 187,182,183
v_lshlrev_b32 v176, 16, v184
v_and_b32 v177, 0xffff0000, v184
v_lshlrev_b32 v178, 16, v185
v_and_b32 v179, 0xffff0000, v185
v_lshlrev_b32 v180, 16, v186
v_and_b32 v181, 0xffff0000, v186
v_lshlrev_b32 v182, 16, v187
v_and_b32 v183, 0xffff0000, v187
v_cndmask_b32_e64 v72, v72, v176, vcc
v_cndmask_b32_e64 v73, v73, v177, vcc
v_cndmask_b32_e64 v74, v74, v178, vcc
v_cndmask_b32_e64 v75, v75, v179, vcc
v_cndmask_b32_e64 v76, v76, v180, vcc
v_cndmask_b32_e64 v77, v77, v181, vcc
v_cndmask_b32_e64 v78, v78, v182, vcc
v_cndmask_b32_e64 v79, v79, v183, vcc
s_bitset0_b32 s68, s69
s_cmp_eq_u32 s68, 0
s_cbranch_scc1 .Lupr_120%=
s_ff1_i32_b32 s69, s68
.LL_123%=:
s_cmp_eq_u32 s64, 0
s_cbranch_scc0 .LL_124%=
s_waitcnt vmcnt(0)
v_lshlrev_b32 v2, 2, v1
v_mov_b64 v[28:29], s[12:13]
s_mov_b64 exec, 3
buffer_store_dwordx2 v[28:29], v2, s[8:11], s53 offen offset:512 sc1
s_mov_b64 exec, -1
s_mov_b32 s64, 1
.LL_124%=:
s_mul_i32 s66, s69, 20
s_mul_i32 s67, s58, 5
s_add_u32 s66, s66, s67
s_lshl_b32 s66, s66, 7
s_add_u32 s66, s66, 98304
s_lshl_b32 s67, s14, 7
s_add_u32 s67, s67, s66
buffer_load_dwordx4 v[160:163], v3, s[8:11], s67 offen offset:0 sc1
buffer_load_dwordx4 v[164:167], v3, s[8:11], s67 offen offset:16 sc1
buffer_load_dwordx4 v[168:171], v3, s[8:11], s66 offen offset:512 sc1
buffer_load_dwordx4 v[172:175], v3, s[8:11], s66 offen offset:528 sc1
s_waitcnt vmcnt(0)
s_branch .LL_121%=
.Lspin_56%=:
s_mov_b32 s84, 0
.Llp_58%=:
s_sleep 2
v_mov_b32 v28, s74
buffer_load_dwordx2 v[28:29], v28, s[8:11], 0 offen offset:3584 sc1
s_waitcnt vmcnt(0)
s_add_u32 s84, s84, 1
v_xor_b32 v28, s12, v28
v_xor_b32 v29, s13, v29
v_lshrrev_b32 v248, 11, v28
v_or_b32 v29, v29, v248
v_cmp_eq_u32 vcc, 0, v29
s_or_b64 vcc, vcc, s[72:73]
s_cmp_eq_u64 vcc, -1
s_cbranch_scc1 .Lok_57%=
s_cmp_lt_u32 s84, 1048576
s_cbranch_scc1 .Llp_58%=
s_branch .Lok_57%=
.Lspin_61%=:
s_mov_b32 s84, 0
.Llp_63%=:
s_sleep 2
v_bfe_u32 v28, v1, 4, 2
v_lshlrev_b32 v28, 3, v28
s_lshl_b32 s80, s56, 9
s_add_u32 s80, s80, 4096
buffer_load_dwordx2 v[28:29], v28, s[8:11], s80 offen sc1
s_waitcnt vmcnt(0)
s_add_u32 s84, s84, 1
v_cmp_eq_u64 vcc, s[12:13], v[28:29]
s_cmp_eq_u64 vcc, -1
s_cbranch_scc1 .Lok_62%=
s_cmp_lt_u32 s84, 1048576
s_cbranch_scc1 .Llp_63%=
s_branch .Lok_62%=
.Lspin_64%=:
s_mov_b32 s84, 0
.Llp_66%=:
s_sleep 2
v_bfe_u32 v28, v1, 4, 4
v_lshlrev_b32 v28, 3, v28
s_lshl_b32 s80, s56, 9
s_add_u32 s80, s80, 4096
buffer_load_dwordx2 v[28:29], v28, s[8:11], s80 offen sc1
s_waitcnt vmcnt(0)
s_add_u32 s84, s84, 1
v_cmp_eq_u64 vcc, s[12:13], v[28:29]
s_cmp_eq_u64 vcc, -1
s_cbranch_scc1 .Lok_65%=
s_cmp_lt_u32 s84, 1048576
s_cbranch_scc1 .Llp_66%=
s_branch .Lok_65%=
.Lspin_67%=:
s_mov_b32 s84, 0
.Llp_69%=:
s_sleep 2
v_bfe_u32 v28, v1, 4, 4
v_lshlrev_b32 v28, 3, v28
s_lshl_b32 s80, s56, 9
s_add_u32 s80, s80, 4224
buffer_load_dwordx2 v[28:29], v28, s[8:11], s80 offen sc1
s_waitcnt vmcnt(0)
s_add_u32 s84, s84, 1
v_cmp_eq_u64 vcc, s[12:13], v[28:29]
s_cmp_eq_u64 vcc, -1
s_cbranch_scc1 .Lok_68%=
s_cmp_lt_u32 s84, 1048576
s_cbranch_scc1 .Llp_69%=
s_branch .Lok_68%=
.Lspin_70%=:
s_mov_b32 s84, 0
.Llp_72%=:
s_sleep 2
v_bfe_u32 v28, v1, 4, 4
v_lshlrev_b32 v28, 3, v28
s_lshl_b32 s80, s56, 9
s_add_u32 s80, s80, 4352
buffer_load_dwordx2 v[28:29], v28, s[8:11], s80 offen sc1
s_waitcnt vmcnt(0)
s_add_u32 s84, s84, 1
v_cmp_eq_u64 vcc, s[12:13], v[28:29]
s_cmp_eq_u64 vcc, -1
s_cbranch_scc1 .Lok_71%=
s_cmp_lt_u32 s84, 1048576
s_cbranch_scc1 .Llp_72%=
s_branch .Lok_71%=
.Lspin_73%=:
s_mov_b32 s84, 0
.Llp_75%=:
s_sleep 2
v_bfe_u32 v28, v1, 4, 4
v_lshlrev_b32 v28, 3, v28
s_lshl_b32 s80, s56, 9
s_add_u32 s80, s80, 4480
buffer_load_dwordx2 v[28:29], v28, s[8:11], s80 offen sc1
s_waitcnt vmcnt(0)
s_add_u32 s84, s84, 1
v_cmp_eq_u64 vcc, s[12:13], v[28:29]
s_cmp_eq_u64 vcc, -1
s_cbranch_scc1 .Lok_74%=
s_cmp_lt_u32 s84, 1048576
s_cbranch_scc1 .Llp_75%=
s_branch .Lok_74%=
.Lspin_76%=:
s_mov_b32 s84, 0
.Llp_78%=:
s_sleep 2
buffer_load_dwordx2 v[8:9], v4, s[8:11], 0 offen offset:2560 sc1
buffer_load_dwordx2 v[10:11], v5, s[8:11], 0 offen offset:3072 sc1
buffer_load_dwordx2 v[12:13], v6, s[8:11], 0 offen offset:3584 sc1
s_waitcnt vmcnt(0)
s_add_u32 s84, s84, 1
v_cmp_eq_u64 vcc, s[12:13], v[8:9]
v_cmp_eq_u64_e64 s[86:87], s[12:13], v[10:11]
s_and_b64 vcc, vcc, s[86:87]
s_or_b64 vcc, vcc, s[20:21]
s_cmp_eq_u64 vcc, -1
s_cbranch_scc1 .Lok_77%=
s_cmp_lt_u32 s84, 1048576
s_cbranch_scc1 .Llp_78%=
s_branch .Lok_77%=
.Lmk_79%=:
s_nop 7
v_cndmask_b32_e64 v168, v241, v168, s[72:73]
v_cndmask_b32_e64 v169, v241, v169, s[74:75]
v_cndmask_b32_e64 v170, v241, v170, s[76:77]
v_cndmask_b32_e64 v171, v241, v171, s[78:79]
v_cndmask_b32_e64 v188, v241, v188, s[72:73]
v_cndmask_b32_e64 v189, v241, v189, s[74:75]
v_cndmask_b32_e64 v190, v241, v190, s[76:77]
v_cndmask_b32_e64 v191, v241, v191, s[78:79]
v_mov_b32 v184, v241
v_mov_b32 v185, v241
v_mov_b32 v186, v241
v_mov_b32 v187, v241
s_branch .Lmkr_80%=
.Lspin_84%=:
s_mov_b32 s84, 0
.Llp_86%=:
s_sleep 2
v_bfe_u32 v28, v1, 4, 2
v_lshlrev_b32 v28, 3, v28
buffer_load_dwordx2 v[28:29], v28, s[8:11], s55 offen sc1
s_waitcnt vmcnt(0)
s_add_u32 s84, s84, 1
v_cmp_eq_u64 vcc, s[12:13], v[28:29]
s_cmp_eq_u64 vcc, -1
s_cbranch_scc1 .Lok_85%=
s_cmp_lt_u32 s84, 1048576
s_cbranch_scc1 .Llp_86%=
s_branch .Lok_85%=
.LL_89%=:
)"
R"(s_mov_b32 s84, 0
.LL_91%=:
s_sleep 2
buffer_load_dwordx2 v[12:13], v27, s[8:11], 0 offen offset:3584 sc1
s_waitcnt vmcnt(0)
s_add_u32 s84, s84, 1
v_xor_b32 v12, s12, v12
v_xor_b32 v13, s13, v13
v_lshrrev_b32 v3, 11, v12
v_or_b32 v13, v13, v3
v_cmp_eq_u32 vcc, 0, v13
s_cmp_eq_u64 vcc, -1
s_cbranch_scc1 .LL_90%=
s_cmp_lt_u32 s84, 1048576
s_cbranch_scc1 .LL_91%=
s_branch .LL_90%=
.Lup_87%=:
v_lshrrev_b32 v3, 8, v1
v_lshlrev_b32 v3, 5, v3
s_ff1_i32_b32 s69, s68
s_mul_i32 s66, s69, 20
s_mul_i32 s67, s58, 5
s_add_u32 s66, s66, s67
s_lshl_b32 s66, s66, 7
s_add_u32 s66, s66, 98304
s_lshl_b32 s67, s14, 7
s_add_u32 s67, s67, s66
buffer_load_dwordx4 v[160:163], v3, s[8:11], s67 offen offset:0 sc1
buffer_load_dwordx4 v[164:167], v3, s[8:11], s67 offen offset:16 sc1
buffer_load_dwordx4 v[168:171], v3, s[8:11], s66 offen offset:512 sc1
buffer_load_dwordx4 v[172:175], v3, s[8:11], s66 offen offset:528 sc1
s_branch .Lupr_88%=
.Lspin_100%=:
s_mov_b32 s84, 0
.Llp_102%=:
s_sleep 2
v_bfe_u32 v28, v1, 4, 2
v_lshlrev_b32 v28, 3, v28
buffer_load_dwordx2 v[28:29], v28, s[8:11], s54 offen sc1
s_waitcnt vmcnt(0)
s_add_u32 s84, s84, 1
v_cmp_eq_u64 vcc, s[12:13], v[28:29]
s_cmp_eq_u64 vcc, -1
s_cbranch_scc1 .Lok_101%=
s_cmp_lt_u32 s84, 1048576
s_cbranch_scc1 .Llp_102%=
s_branch .Lok_101%=
.Lspin_103%=:
s_mov_b32 s84, 0
.Llp_105%=:
s_sleep 2
v_bfe_u32 v28, v1, 4, 2
v_lshlrev_b32 v28, 3, v28
buffer_load_dwordx2 v[28:29], v28, s[8:11], s55 offen sc1
s_waitcnt vmcnt(0)
s_add_u32 s84, s84, 1
v_cmp_eq_u64 vcc, s[12:13], v[28:29]
s_cmp_eq_u64 vcc, -1
s_cbranch_scc1 .Lok_104%=
s_cmp_lt_u32 s84, 1048576
s_cbranch_scc1 .Llp_105%=
s_branch .Lok_104%=
.LL_108%=:
s_mov_b32 s84, 0
.LL_110%=:
s_sleep 2
buffer_load_dwordx2 v[12:13], v27, s[8:11], 0 offen offset:3584 sc1
s_waitcnt vmcnt(0)
s_add_u32 s84, s84, 1
v_xor_b32 v12, s12, v12
v_xor_b32 v13, s13, v13
v_lshrrev_b32 v3, 11, v12
v_or_b32 v13, v13, v3
v_cmp_eq_u32 vcc, 0, v13
s_cmp_eq_u64 vcc, -1
s_cbranch_scc1 .LL_109%=
s_cmp_lt_u32 s84, 1048576
s_cbranch_scc1 .LL_110%=
s_branch .LL_109%=
.Lup_106%=:
v_lshrrev_b32 v3, 8, v1
v_lshlrev_b32 v3, 5, v3
s_ff1_i32_b32 s69, s68
s_mul_i32 s66, s69, 20
s_mul_i32 s67, s58, 5
s_add_u32 s66, s66, s67
s_lshl_b32 s66, s66, 7
s_add_u32 s66, s66, 98304
s_lshl_b32 s67, s14, 7
s_add_u32 s67, s67, s66
buffer_load_dwordx4 v[160:163], v3, s[8:11], s67 offen offset:0 sc1
buffer_load_dwordx4 v[164:167], v3, s[8:11], s67 offen offset:16 sc1
buffer_load_dwordx4 v[168:171], v3, s[8:11], s66 offen offset:512 sc1
buffer_load_dwordx4 v[172:175], v3, s[8:11], s66 offen offset:528 sc1
s_branch .Lupr_107%=
.Lend_202%=:
.Lend%=:
.purgem W
.purgem D
.purgem Ta
.purgem Ra
.purgem Zvaa
.purgem Mvaa
.purgem Zaaa
.purgem Maaa
.purgem A
.purgem U
.purgem K
.purgem Rv
.purgem Mvav
.purgem G
.purgem Zvav
.purgem Cvav
.purgem X
.purgem Cvaa
.purgem Dc
.purgem Maav
.purgem Mvvv
.purgem Zvva
.purgem Mvva
)"
    : : [ka] "s"(__builtin_amdgcn_kernarg_segment_ptr()), [bid] "s"(blockIdx.x), [tid] "v"(threadIdx.x)
    : "v1", "v2", "v3", "v4", "v5", "v6", "v7", "v8", "v9", "v10", "v11", "v12", "v13", "v14", "v15", "v16", "v17", "v18", "v19", "v20", "v21", "v22", "v23", "v24", "v25", "v26", "v27", "v28", "v29", "v30", "v31", "v32", "v33", "v34", "v35", "v36", "v37", "v38", "v39", "v40", "v41", "v42", "v43", "v44", "v45", "v46", "v47", "v48", "v49", "v50", "v51", "v52", "v53", "v54", "v55", "v56", "v57", "v58", "v59", "v60", "v61", "v62", "v63", "v64", "v65", "v66", "v67", "v68", "v69", "v70", "v71", "v72", "v73", "v74", "v75", "v76", "v77", "v78", "v79", "v80", "v81", "v82", "v83", "v84", "v85", "v86", "v87", "v88", "v89", "v90", "v91", "v92", "v93", "v94", "v95", "v96", "v97", "v98", "v99", "v100", "v101", "v102", "v103", "v104", "v105", "v106", "v107", "v108", "v109", "v110", "v111", "v112", "v113", "v114", "v115", "v116", "v117", "v118", "v119", "v120", "v121", "v122", "v123", "v124", "v125", "v126", "v127", "v128", "v129", "v130", "v131", "v132", "v133", "v134", "v135", "v136", "v137", "v138", "v139", "v140", "v141", "v142", "v143", "v144", "v145", "v146", "v147", "v148", "v149", "v150", "v151", "v152", "v153", "v154", "v155", "v156", "v157", "v158", "v159", "v160", "v161", "v162", "v163", "v164", "v165", "v166", "v167", "v168", "v169", "v170", "v171", "v172", "v173", "v174", "v175", "v176", "v177", "v178", "v179", "v180", "v181", "v182", "v183", "v184", "v185", "v186", "v187", "v188", "v189", "v190", "v191", "v192", "v193", "v194", "v195", "v196", "v197", "v198", "v199", "v200", "v201", "v202", "v203", "v204", "v205", "v206", "v207", "v208", "v209", "v210", "v211", "v212", "v213", "v214", "v215", "v216", "v217", "v218", "v219", "v220", "v221", "v222", "v223", "v224", "v225", "v226", "v227", "v228", "v229", "v230", "v231", "v232", "v233", "v234", "v235", "v236", "v237", "v238", "v239", "v240", "v241", "v242", "v243", "v244", "v245", "v246", "v247", "v248", "v249", "v250", "v251", "v252", "v253", "v254", "v255", "a0", "a1", "a2", "a3", "a4", "a5", "a6", "a7", "a8", "a9", "a10", "a11", "a12", "a13", "a14", "a15", "a16", "a17", "a18", "a19", "a20", "a21", "a22", "a23", "a24", "a25", "a26", "a27", "a28", "a29", "a30", "a31", "a32", "a33", "a34", "a35", "a36", "a37", "a38", "a39", "a40", "a41", "a42", "a43", "a44", "a45", "a46", "a47", "a48", "a49", "a50", "a51", "a52", "a53", "a54", "a55", "a56", "a57", "a58", "a59", "a60", "a61", "a62", "a63", "a64", "a65", "a66", "a67", "a68", "a69", "a70", "a71", "a72", "a73", "a74", "a75", "a76", "a77", "a78", "a79", "a80", "a81", "a82", "a83", "a84", "a85", "a86", "a87", "a88", "a89", "a90", "a91", "a92", "a93", "a94", "a95", "a96", "a97", "a98", "a99", "a100", "a101", "a102", "a103", "a104", "a105", "a106", "a107", "a108", "a109", "a110", "a111", "a112", "a113", "a114", "a115", "a116", "a117", "a118", "a119", "a120", "a121", "a122", "a123", "a124", "a125", "a126", "a127", "a128", "a129", "a130", "a131", "a132", "a133", "a134", "a135", "a136", "a137", "a138", "a139", "a140", "a141", "a142", "a143", "a144", "a145", "a146", "a147", "a148", "a149", "a150", "a151", "a152", "a153", "a154", "a155", "a156", "a157", "a158", "a159", "a160", "a161", "a162", "a163", "a164", "a165", "a166", "a167", "a168", "a169", "a170", "a171", "a172", "a173", "a174", "a175", "a176", "a177", "a178", "a179", "a180", "a181", "a182", "a183", "a184", "a185", "a186", "a187", "a188", "a189", "a190", "a191", "a192", "a193", "a194", "a195", "a196", "a197", "a198", "a199", "a200", "a201", "a202", "a203", "a204", "a205", "a206", "a207", "a208", "a209", "a210", "a211", "a212", "a213", "a214", "a215", "a216", "a217", "a218", "a219", "a220", "a221", "a222", "a223", "a224", "a225", "a226", "a227", "a228", "a229", "a230", "a231", "a232", "a233", "a234", "a235", "a236", "a237", "a238", "a239", "a240", "a241", "a242", "a243", "a244", "a245", "a246", "a247", "a248", "a249", "a250", "a251", "a252", "a253", "a254", "a255", "s4", "s5", "s6", "s7", "s8", "s9", "s10", "s11", "s12", "s13", "s14", "s15", "s16", "s17", "s18", "s19", "s20", "s21", "s22", "s23", "s24", "s25", "s26", "s27", "s28", "s29", "s30", "s31", "s32", "s33", "s34", "s35", "s36", "s37", "s38", "s39", "s40", "s41", "s42", "s43", "s44", "s45", "s46", "s47", "s48", "s49", "s50", "s51", "s52", "s53", "s54", "s55", "s56", "s57", "s58", "s59", "s60", "s61", "s62", "s63", "s64", "s65", "s66", "s67", "s68", "s69", "s70", "s71", "s72", "s73", "s74", "s75", "s76", "s77", "s78", "s79", "s80", "s81", "s82", "s83", "s84", "s85", "s86", "s87", "s88", "s89", "s90", "s91", "s92", "s93", "s94", "s95", "s96", "s97", "s98", "s99", "vcc", "scc", "memory"
  );
}
extern "C" void kernel_launch(void* const* d_in, const int* in_sizes, int n_in, void* d_out, int out_size, void* d_ws, size_t ws_size, hipStream_t stream) {
  (void)hipFuncSetAttribute((const void*)mega_kernel, hipFuncAttributeMaxDynamicSharedMemorySize, LDS_BYTES);
  mega_kernel<<<256, 256, LDS_BYTES, stream>>>((const bf16*)d_in[0], (const bf16*)d_in[1], (const bf16*)d_in[2], (const bf16*)d_in[3], (const bf16*)d_in[4], (const bf16*)d_in[5],
      (const bf16*)d_in[7], (const bf16*)d_in[8], (const bf16*)d_in[9], (const bf16*)d_in[10], (char*)d_ws + 0, (bf16*)d_out);
}
```

```cpp
#include <hip/hip_runtime.h>
#include <hip/hip_bf16.h>
typedef __hip_bfloat16 bf16;
constexpr int LDS_BYTES = 163840;
__global__ __launch_bounds__(256, 1) void mega_kernel(const bf16* x, const bf16* ckv, const bf16* wq, const bf16* wuk, const bf16* wuv, const bf16* wo,
    const bf16* w_r, const bf16* wg, const bf16* wu, const bf16* wd, char* ws, bf16* out) {
  asm volatile(
R"(.macro W n
s_waitcnt lgkmcnt(\n)
.endm
.macro D v,r,s,o
buffer_load_dwordx4 v\v, s[\r:\r+3], s\s offen offset:\o lds
.endm
.macro Ta d,a,o
ds_read_b64_tr_b16 a[\d:\d+1], v\a offset:\o
.endm
.macro Ra d,a,o
ds_read_b128 a[\d:\d+3], v\a offset:\o
.endm
.macro Zvaa d,a,b
v_mfma_f32_16x16x32_bf16 v[\d:\d+3], a[\a:\a+3], a[\b:\b+3], 0
.endm
.macro Mvaa d,a,b
v_mfma_f32_16x16x32_bf16 v[\d:\d+3], a[\a:\a+3], a[\b:\b+3], v[\d:\d+3]
.endm
.macro Zaaa d,a,b
v_mfma_f32_16x16x32_bf16 a[\d:\d+3], a[\a:\a+3], a[\b:\b+3], 0
.endm
.macro Maaa d,a,b
v_mfma_f32_16x16x32_bf16 a[\d:\d+3], a[\a:\a+3], a[\b:\b+3], a[\d:\d+3]
.endm
.macro A d,a,b
v_add_f32 v\d, v\a, v\b
.endm
.macro U d,a,b
v_mul_f32 v\d, v\a, v\b
.endm
.macro K d,a,b
v_cvt_pk_bf16_f32 v\d, v\a, v\b
.endm
.macro Rv d,a,o
ds_read_b128 v[\d:\d+3], v\a offset:\o
.endm
.macro Mvav d,a,b
v_mfma_f32_16x16x32_bf16 v[\d:\d+3], a[\a:\a+3], v[\b:\b+3], v[\d:\d+3]
.endm
.macro G d,a
v_accvgpr_read_b32 v\d, a\a
.endm
.macro Zvav d,a,b
v_mfma_f32_16x16x32_bf16 v[\d:\d+3], a[\a:\a+3], v[\b:\b+3], 0
.endm
.macro Cvav d,a,b,c
v_mfma_f32_16x16x32_bf16 v[\d:\d+3], a[\a:\a+3], v[\b:\b+3], v[\c:\c+3]
.endm
.macro X d
v_exp_f32 v\d, v\d
.endm
.macro Cvaa d,a,b,c
v_mfma_f32_16x16x32_bf16 v[\d:\d+3], a[\a:\a+3], a[\b:\b+3], v[\c:\c+3]
.endm
.macro Dc v,r,s,o
buffer_load_dwordx4 v\v, s[\r:\r+3], s\s offen offset:\o sc1 lds
.endm
.macro Maav d,a,b
v_mfma_f32_16x16x32_bf16 a[\d:\d+3], a[\a:\a+3], v[\b:\b+3], a[\d:\d+3]
.endm
.macro Mvvv d,a,b
v_mfma_f32_16x16x32_bf16 v[\d:\d+3], v[\a:\a+3], v[\b:\b+3], v[\d:\d+3]
.endm
.macro Zvva d,a,b
v_mfma_f32_16x16x32_bf16 v[\d:\d+3], v[\a:\a+3], a[\b:\b+3], 0
.endm
.macro Mvva d,a,b
v_mfma_f32_16x16x32_bf16 v[\d:\d+3], v[\a:\a+3], a[\b:\b+3], v[\d:\d+3]
.endm
s_load_dwordx16 s[16:31], %[ka], 0
s_load_dwordx8 s[32:39], %[ka], 64
s_getpc_b64 s[6:7]
.Lbeg%=:
s_mov_b32 s15, %[bid]
v_lshrrev_b32 v2, 6, %[tid]
v_and_b32 v100, 63, %[tid]
v_readfirstlane_b32 s14, v2
v_lshlrev_b32 v1, 4, v100
v_lshrrev_b32 v101, 4, v100
v_and_b32 v102, 15, v100
s_and_b32 s59, s15, 7
s_bfe_u32 s57, s15, 0x30003
s_lshr_b32 s58, s15, 6
s_lshl_b32 s56, s59, 2
s_add_u32 s56, s56, s58
s_lshr_b32 s80, s15, 3
s_lshl_b32 s80, s80, 2
s_add_u32 s80, s80, s14
s_lshl_b32 s80, s80, 11
v_min_u32 v146, 240, v1
v_lshl_add_u32 v146, v146, 3, s80
v_min_u32 v146, .Lend%=-.Lbeg%=-4, v146
global_load_dword v142, v146, s[6:7]
v_and_b32 v88, 0x1f0, v1
v_lshrrev_b32 v105, 1, v101
v_mul_u32_u24 v105, 1280, v105
v_add_u32 v28, v88, v105
s_lshl_b32 s85, s15, 2
s_add_u32 s85, s85, s14
s_mul_i32 s84, s85, 2560
v_and_b32 v105, 1, v101
v_lshl_or_b32 v29, v105, 31, v28
v_lshlrev_b32 v104, 1, v101
v_xor_b32 v104, v104, v102
v_lshlrev_b32 v104, 4, v104
v_lshl_add_u32 v16, v101, 13, v104
v_xor_b32 v105, 128, v16
v_add_u32 v17, 31744, v16
v_add_u32 v18, 63488, v105
v_add_u32 v19, 95232, v105
v_add_u32 v20, 131072, v16
v_add_u32 v21, 162816, v16
v_add_u32 v22, 194560, v105
v_add_u32 v23, 226304, v105
s_lshl_b32 s80, s14, 3
s_add_u32 s80, s80, s57
s_lshl_b32 s80, s80, 15
v_and_b32 v104, 7, v100
v_xor_b32 v104, v104, v101
v_lshlrev_b32 v104, 4, v104
v_lshrrev_b32 v105, 3, v100
v_lshl_add_u32 v104, v105, 10, v104
v_add_u32 v2, s80, v104
v_xor_b32 v105, 64, v2
v_add_u32 v3, 7168, v105
v_add_u32 v4, 14336, v2
v_add_u32 v5, 21504, v105
v_xor_b32 v6, 1835008, v2
v_xor_b32 v7, 1835008, v3
v_xor_b32 v8, 1835008, v4
v_xor_b32 v9, 1835008, v5
s_lshl_b32 s63, s14, 13
s_add_u32 s61, s63, 65536
s_and_b32 s80, s14, 1
s_lshr_b32 s81, s14, 1
s_lshl_b32 s62, s80, 14
s_lshl_b32 s82, s81, 13
s_add_u32 s62, s62, s82
s_lshl_b32 s81, s81, 18
s_lshl_b32 s83, s56, 8
s_add_u32 s81, s81, s83
s_mov_b32 s64, 0
s_cmp_eq_u32 s80, 0
W 0
s_cselect_b32 s44, s22, s24
s_cselect_b32 s45, s23, s25
s_cmp_eq_u32 s14, 3
s_cbranch_scc0 .LL_1%=
s_lshr_b32 s88, s15, 3
s_cmp_lt_u32 s88, 14
s_cbranch_scc0 .LL_1%=
v_lshlrev_b32 v104, 16, v1
s_mov_b64 s[86:87], s[36:37]
s_mov_b32 s89, 38797312
s_cmp_eq_u32 s88, 1
s_cselect_b32 s90, 39845888, 0
s_add_u32 s86, s86, s90
s_addc_u32 s87, s87, 0
s_cmp_eq_u32 s88, 2
s_cselect_b64 s[86:87], s[26:27], s[86:87]
s_cselect_b32 s89, 5242876, s89
s_cmp_eq_u32 s88, 3
s_cselect_b64 s[86:87], s[30:31], s[86:87]
s_cselect_b32 s89, 2211836, s89
s_cmp_eq_u32 s88, 4
s_cselect_b64 s[86:87], s[32:33], s[86:87]
s_cselect_b32 s89, 2211836, s89
s_cmp_eq_u32 s88, 5
s_cselect_b64 s[86:87], s[34:35], s[86:87]
s_cselect_b32 s89, 2211836, s89
s_cmp_eq_u32 s88, 6
s_cselect_b64 s[86:87], s[38:39], s[86:87]
s_cselect_b32 s89, 2621436, s89
s_cmp_eq_u32 s88, 7
s_cselect_b64 s[86:87], s[20:21], s[86:87]
s_cselect_b32 s89, 5242876, s89
s_cmp_eq_u32 s88, 8
s_cselect_b64 s[86:87], s[18:19], s[86:87]
s_cselect_b32 s89, 2097148, s89
s_cmp_eq_u32 s88, 9
s_cselect_b64 s[86:87], s[22:23], s[86:87]
s_cselect_b32 s89, 4194300, s89
s_cmp_eq_u32 s88, 10
s_cselect_b64 s[86:87], s[24:25], s[86:87]
s_cselect_b32 s89, 4194300, s89
s_cmp_eq_u32 s88, 11
s_cselect_b64 s[86:87], s[16:17], s[86:87]
s_cselect_b32 s89, 2621436, s89
s_cmp_eq_u32 s88, 12
s_cselect_b64 s[86:87], s[28:29], s[86:87]
s_cselect_b32 s89, 1276, s89
s_cmp_eq_u32 s88, 13
s_cselect_b64 s[86:87], s[6:7], s[86:87]
s_cselect_b32 s89, 0, s89
v_min_u32 v104, s89, v104
global_load_dword v142, v104, s[86:87] sc1
.LL_1%=:
s_mov_b32 s40, s18
s_and_b32 s41, s19, 0xffff
s_mov_b32 s42, 0x200000
s_mov_b32 s43, 0x20000
s_add_u32 s44, s44, s81
s_addc_u32 s45, s45, 0
s_and_b32 s45, s45, 0xffff
s_mov_b32 s46, 0x400000
s_mov_b32 s47, 0x20000
s_sub_u32 s46, s46, s81
s_load_dwordx2 s[12:13], s[36:37], 0
s_lshl_b32 s65, s64, 12
s_add_u32 m0, s62, 0
s_nop 0
D 16,44,65,0
D 17,44,65,1024
D 18,44,65,2048
D 19,44,65,3072
s_add_u32 m0, s62, 4096
s_nop 0
D 20,44,65,0
D 21,44,65,1024
D 22,44,65,2048
D 23,44,65,3072
s_add_u32 m0, s61, 0
s_nop 0
D 2,40,64,0
D 3,40,64,1024
D 4,40,64,2048
D 5,40,64,3072
s_add_u32 m0, s61, 4096
s_nop 0
D 6,40,64,0
D 7,40,64,1024
D 8,40,64,2048
D 9,40,64,3072
s_mov_b32 s52, s16
s_and_b32 s53, s17, 0xffff
s_mov_b32 s54, 2621440
s_mov_b32 s55, 0x20000
buffer_load_dwordx4 v[32:35], v28, s[52:55], s84 offen
buffer_load_dwordx4 v[36:39], v28, s[52:55], s84 offen offset:512
buffer_load_dwordx4 v[40:43], v29, s[52:55], s84 offen offset:1024
s_mov_b32 s52, s28
s_and_b32 s53, s29, 0xffff
s_mov_b32 s54, 1280
s_mov_b32 s55, 0x20000
buffer_load_dwordx4 v[44:47], v88, s[52:55], 0 offen offset:0
buffer_load_dwordx4 v[48:51], v88, s[52:55], 0 offen offset:512
buffer_load_dwordx4 v[52:55], v88, s[52:55], 0 offen offset:1024
s_mov_b32 s69, 0
s_cmp_eq_u32 s14, 2
s_cbranch_scc1 .LL_2%=
s_cmp_eq_u32 s14, 3
s_cbranch_scc1 .LL_3%=
s_lshl_b32 s80, s58, 3
s_add_u32 s80, s80, s57
s_lshl_b32 s80, s80, 15
s_lshl_b32 s82, s14, 7
s_add_u32 s80, s80, s82
s_add_u32 s80, s80, 256
v_lshl_add_u32 v104, v88, 6, s80
v_and_b32 v105, 512, v1
v_mul_u32_u24 v105, 3584, v105
v_xor_b32 v104, v104, v105
v_mov_b32 v140, s18
v_mov_b32 v141, s19
s_mov_b32 s68, 256
s_branch .LL_4%=
.LL_2%=:
v_lshrrev_b32 v104, 6, v1
v_lshlrev_b32 v104, 13, v104
v_and_b32 v105, 16, v1
v_lshl_add_u32 v104, v105, 3, v104
s_lshl_b32 s80, s57, 17
s_add_u32 s80, s80, s83
s_add_u32 s80, s80, 1048576
v_add_u32 v104, s80, v104
v_and_b32 v105, 32, v1
v_cmp_eq_u32 vcc, 0, v105
v_mov_b32 v140, s24
v_mov_b32 v141, s25
v_mov_b32 v105, s22
v_mov_b32 v106, s23
v_cndmask_b32 v140, v140, v105, vcc
v_cndmask_b32 v141, v141, v106, vcc
s_mov_b32 s68, 1048576
s_branch .LL_4%=
.LL_3%=:
v_lshrrev_b32 v104, 5, v1
v_lshlrev_b32 v104, 13, v104
v_and_b32 v105, 16, v1
v_lshl_add_u32 v104, v105, 3, v104
s_mul_i32 s80, s57, 655360
s_add_u32 s80, s80, s83
v_add_u32 v104, s80, v104
v_mov_b32 v140, s20
v_mov_b32 v141, s21
s_mov_b32 s68, 196608
v_mov_b32 v104, 0
s_mov_b32 s68, 0
.LL_4%=:
v_mov_b32 v105, 0
v_lshl_add_u64 v[140:141], v[104:105], 0, v[140:141]
s_add_u32 s64, s64, 128
s_and_b32 s64, s64, 1023
s_lshl_b32 s65, s64, 12
s_add_u32 m0, s62, 32768
s_nop 0
D 16,44,65,0
D 17,44,65,1024
D 18,44,65,2048
D 19,44,65,3072
s_add_u32 m0, s62, 36864
s_nop 0
D 20,44,65,0
D 21,44,65,1024
D 22,44,65,2048
D 23,44,65,3072
v_lshrrev_b32 v104, 1, v102
v_xor_b32 v104, v104, v101
v_lshlrev_b32 v104, 4, v104
v_lshl_add_u32 v104, v102, 7, v104
s_lshr_b32 s81, s14, 1
s_lshl_b32 s81, s81, 14
s_add_u32 s81, s81, 65536
v_add_u32 v26, s81, v104
v_xor_b32 v27, 64, v26
v_and_b32 v105, 1, v101
v_lshrrev_b32 v104, 2, v102
v_lshl_or_b32 v105, v105, 2, v104
v_lshl_add_u32 v104, v101, 3, v104
v_lshlrev_b32 v104, 8, v104
v_lshl_add_u32 v104, v105, 5, v104
v_and_b32 v105, 3, v102
v_lshl_add_u32 v10, v105, 3, v104
v_xor_b32 v11, 32, v10
v_xor_b32 v12, 64, v10
v_xor_b32 v13, 96, v10
v_xor_b32 v14, 128, v10
v_xor_b32 v15, 160, v10
v_xor_b32 v24, 192, v10
v_xor_b32 v25, 224, v10
.LL_8%=:
s_lshr_b32 s81, s15, 3
s_cmp_eq_u32 s14, 2
s_cbranch_scc1 .LL_5%=
s_cmp_eq_u32 s14, 3
s_cbranch_scc1 .LL_6%=
s_cmp_eq_u32 s14, 0
s_cselect_b64 s[76:77], s[30:31], s[32:33]
s_mov_b32 s72, 512
s_mov_b32 s73, 24
s_mul_i32 s74, s81, 69120
s_mul_i32 s82, s59, 8640
s_andn2_b32 s82, s82, 127
s_add_u32 s74, s74, s82
s_mov_b32 s75, 2211836
s_branch .LL_7%=
.LL_5%=:
s_mov_b64 s[76:77], s[34:35]
s_mov_b32 s72, 512
s_mov_b32 s73, 24
s_mul_i32 s74, s81, 8960
s_mul_i32 s82, s59, 286720
s_add_u32 s74, s74, s82
s_mov_b32 s75, 2211836
s_branch .LL_7%=
.LL_6%=:
s_mov_b64 s[76:77], s[26:27]
s_mov_b32 s72, 512
s_mov_b32 s73, 4
s_mul_i32 s74, s81, 20480
s_mul_i32 s82, s59, 655360
s_add_u32 s74, s74, s82
s_mov_b32 s75, 5242876
s_branch .LL_7%=
.LL_7%=:
s_mov_b32 s8, s36
s_and_b32 s9, s37, 0xffff
s_mov_b32 s10, 79691776
s_mov_b32 s11, 0x20000
s_add_u32 s48, s20, s83
s_addc_u32 s49, s21, 0
s_and_b32 s49, s49, 0xffff
s_mov_b32 s50, 5242880
s_mov_b32 s51, 0x20000
s_sub_u32 s50, s50, s83
s_lshl_b32 s80, s57, 1
s_and_b32 s81, s14, 1
s_add_u32 s80, s80, s81
s_sub_u32 s81, 31, s80
s_cmp_lt_u32 s14, 2
s_cselect_b32 s60, s80, s81
s_mul_i32 s28, s60, 81920
s_add_u32 s28, s28, 131072
s_add_u32 s4, s28, 10240
s_mov_b32 s80, 0
s_lshl_b32 s81, s80, 9
s_add_u32 s28, s28, s81
s_add_u32 s5, s80, s14
s_cmp_ge_u32 s5, 20
s_cselect_b32 s81, 20, 0
s_sub_u32 s5, s5, s81
s_and_b32 s80, s14, 1
s_mul_i32 s29, s80, 17172480
s_mul_i32 s80, s56, 536576
s_add_u32 s29, s29, s80
s_lshl_b32 s80, s57, 13
s_add_u32 s29, s29, s80
s_add_u32 s29, s29, 4194304
W 0
s_add_u32 s12, s12, 0x7f4a7c15
s_addc_u32 s13, s13, 0x9e3779b9
s_mov_b64 exec, 0
s_cmp_eq_u32 s14, 0
s_cbranch_scc1 .Lh_0_0%=
s_cmp_eq_u32 s14, 1
s_cbranch_scc1 .Lh_0_1%=
s_cmp_eq_u32 s14, 2
s_cbranch_scc1 .Lh_0_2%=
s_branch .Lh_0_3%=
.Lhr_9%=:
s_mov_b64 exec, -1
s_bitcmp1_b32 s14, 0
s_cbranch_scc1 .Lv_10%=
s_branch .LL_12%=
.p2align 12
.LL_12%=:
s_waitcnt vmcnt(14)
s_barrier
Ta 0,10,0
Ta 2,10,1024
Ra 32,26,0
Ta 4,10,8192
.Lh_0_0%=:
s_cbranch_execz .Lh_0_0_1%=
Ta 6,10,9216
Ra 128,27,0
Ra 132,27,2048
Ra 36,26,2048
Ra 40,26,4096
Ra 136,27,4096
Ra 140,27,6144
Ra 44,26,6144
Ra 48,26,8192
Ra 144,27,8192
W 6
Zvaa 160,0,32
)"
R"(s_add_u32 m0, s61, 32768
Ra 148,27,10240
Ra 52,26,10240
Ra 56,26,12288
Ra 152,27,12288
Ra 156,27,14336
Ra 60,26,14336
Mvaa 160,4,128
Ta 8,11,0
Ta 10,11,1024
Ta 12,11,8192
Ta 14,11,9216
Zvaa 192,4,132
Ta 16,12,0
Ta 18,12,1024
Mvaa 192,0,36
W 14
Ta 20,12,8192
Ta 22,12,9216
Zvaa 224,0,40
D 2,40,64,0
Mvaa 224,4,136
Zaaa 160,4,140
Maaa 160,0,44
W 12
Zaaa 192,0,48
D 3,40,64,1024
Maaa 192,4,144
Zaaa 224,4,148
Maaa 224,0,52
W 8
Zaaa 64,0,56
D 4,40,64,2048
Maaa 64,4,152
Zaaa 96,4,156
Maaa 96,0,60
W 4
Zaaa 100,8,60
D 5,40,64,3072
Maaa 100,12,156
s_add_u32 m0, s61, 36864
Zaaa 68,12,152
Ta 24,13,0
Ta 26,13,1024
Maaa 68,8,56
Ta 28,13,8192
Ta 30,13,9216
Zaaa 228,8,52
s_waitcnt vmcnt(15)
D 6,40,64,0
Maaa 228,12,148
v_lshlrev_b32 v56, 16, v32
v_and_b32 v57, 0xffff0000, v32
v_lshlrev_b32 v58, 16, v33
v_and_b32 v59, 0xffff0000, v33
v_lshlrev_b32 v60, 16, v34
Zaaa 196,12,144
v_and_b32 v61, 0xffff0000, v34
v_lshlrev_b32 v62, 16, v35
v_and_b32 v63, 0xffff0000, v35
v_lshlrev_b32 v64, 16, v36
v_and_b32 v65, 0xffff0000, v36
Maaa 196,8,48
v_lshlrev_b32 v66, 16, v37
v_and_b32 v67, 0xffff0000, v37
v_lshlrev_b32 v68, 16, v38
v_and_b32 v69, 0xffff0000, v38
v_lshlrev_b32 v70, 16, v39
Zaaa 164,8,44
D 7,40,64,1024
v_and_b32 v71, 0xffff0000, v39
v_lshlrev_b32 v72, 16, v40
v_and_b32 v73, 0xffff0000, v40
v_lshlrev_b32 v74, 16, v41
v_and_b32 v75, 0xffff0000, v41
Maaa 164,12,140
v_lshlrev_b32 v76, 16, v42
v_and_b32 v77, 0xffff0000, v42
v_lshlrev_b32 v78, 16, v43
v_and_b32 v79, 0xffff0000, v43
A 80,56,57
Zvaa 228,12,136
A 81,58,59
A 80,80,60
A 81,81,61
A 80,80,62
A 81,81,63
Mvaa 228,8,40
A 80,80,64
A 81,81,65
A 80,80,66
A 81,81,67
A 80,80,68
Zvaa 196,8,36
D 8,40,64,2048
A 81,81,69
A 80,80,70
A 81,81,71
A 80,80,72
A 81,81,73
Mvaa 196,12,132
A 80,80,74
A 81,81,75
A 80,80,76
A 81,81,77
A 80,80,78
A 81,81,79
Zvaa 164,12,128
A 80,80,81
s_nop 1
v_add_f32_dpp v80, v80, v80 quad_perm:[1,0,3,2] row_mask:0xf bank_mask:0xf
s_nop 1
v_add_f32_dpp v80, v80, v80 quad_perm:[2,3,0,1] row_mask:0xf bank_mask:0xf
s_nop 1
v_add_f32_dpp v80, v80, v80 row_half_mirror row_mask:0xf bank_mask:0xf
s_nop 1
v_add_f32_dpp v80, v80, v80 row_mirror row_mask:0xf bank_mask:0xf
Mvaa 164,8,32
W 4
v_mov_b32 v81, v80
s_nop 1
v_permlane16_swap_b32 v80, v81
A 80,80,81
v_mul_f32 v84, 0x3acccccd, v80
s_mov_b32 s86, 0xffff
Zvaa 168,16,32
s_mov_b32 s87, 0xffff
D 9,40,64,3072
v_sub_f32 v82, v56, v84
U 80,82,82
v_sub_f32 v83, v57, v84
v_fmac_f32 v80, v83, v83
Mvaa 168,20,128
v_sub_f32 v82, v58, v84
v_fmac_f32 v80, v82, v82
v_sub_f32 v83, v59, v84
v_fmac_f32 v80, v83, v83
v_sub_f32 v82, v60, v84
Zvaa 200,20,132
Ta 0,14,0
v_fmac_f32 v80, v82, v82
v_sub_f32 v83, v61, v84
v_fmac_f32 v80, v83, v83
v_sub_f32 v82, v62, v84
v_fmac_f32 v80, v82, v82
Mvaa 200,16,36
Ta 2,14,1024
v_sub_f32 v83, v63, v84
v_fmac_f32 v80, v83, v83
v_sub_f32 v82, v64, v84
v_fmac_f32 v80, v82, v82
v_sub_f32 v83, v65, v84
Zvaa 232,16,40
global_load_dword v142, v[140:141], off
v_lshl_add_u64 v[140:141], v[140:141], 0, s[68:69]
Ta 4,14,8192
Ta 6,14,9216
v_fmac_f32 v80, v83, v83
v_sub_f32 v82, v66, v84
v_fmac_f32 v80, v82, v82
Mvaa 232,20,136
v_sub_f32 v83, v67, v84
v_fmac_f32 v80, v83, v83
v_sub_f32 v82, v68, v84
v_fmac_f32 v80, v82, v82
v_sub_f32 v83, v69, v84
v_fmac_f32 v80, v83, v83
Zaaa 168,20,140
v_sub_f32 v82, v70, v84
v_fmac_f32 v80, v82, v82
v_sub_f32 v83, v71, v84
v_fmac_f32 v80, v83, v83
v_sub_f32 v82, v72, v84
U 81,82,82
Maaa 168,16,44
v_sub_f32 v83, v73, v84
v_fmac_f32 v81, v83, v83
v_sub_f32 v82, v74, v84
v_fmac_f32 v81, v82, v82
v_sub_f32 v83, v75, v84
v_fmac_f32 v81, v83, v83
Zaaa 200,16,48
v_sub_f32 v82, v76, v84
v_fmac_f32 v81, v82, v82
v_sub_f32 v83, v77, v84
v_fmac_f32 v81, v83, v83
v_sub_f32 v82, v78, v84
Maaa 200,20,144
v_fmac_f32 v81, v82, v82
v_sub_f32 v83, v79, v84
v_fmac_f32 v81, v83, v83
v_cndmask_b32_e64 v81, 0, v81, s[86:87]
A 80,80,81
Zaaa 232,20,148
Maaa 232,16,52
v_add_f32_dpp v80, v80, v80 quad_perm:[1,0,3,2] row_mask:0xf bank_mask:0xf
s_nop 1
v_add_f32_dpp v80, v80, v80 quad_perm:[2,3,0,1] row_mask:0xf bank_mask:0xf
s_nop 1
v_add_f32_dpp v80, v80, v80 row_half_mirror row_mask:0xf bank_mask:0xf
s_nop 1
v_add_f32_dpp v80, v80, v80 row_mirror row_mask:0xf bank_mask:0xf
v_mov_b32 v81, v80
s_nop 1
v_permlane16_swap_b32 v80, v81
Zaaa 72,16,56
A 80,80,81
v_mul_f32 v80, 0x3acccccd, v80
K 80,80,80
v_lshlrev_b32 v80, 16, v80
v_add_f32 v80, 0x3727c5ac, v80
K 80,80,80
Maaa 72,20,152
v_lshlrev_b32 v80, 16, v80
v_sqrt_f32 v81, v80
s_nop 0
v_rcp_f32 v82, v81
v_fma_f32 v83, -v81, v81, v80
Zaaa 104,20,156
v_mul_f32 v82, 0.5, v82
v_fma_f32 v81, v83, v82, v81
K 86,81,81
v_lshlrev_b32 v86, 16, v86
v_rcp_f32 v87, v86
Maaa 104,16,60
W 4
K 85,84,84
v_lshlrev_b32 v85, 16, v85
s_waitcnt vmcnt(17)
v_sub_f32 v56, v56, v85
v_sub_f32 v57, v57, v85
K 80,56,57
Zaaa 108,24,60
v_lshlrev_b32 v56, 16, v80
v_and_b32 v57, 0xffff0000, v80
U 80,56,87
U 81,57,87
K 32,80,81
v_lshlrev_b32 v80, 16, v32
Maaa 108,28,156
v_and_b32 v81, 0xffff0000, v32
v_lshlrev_b32 v82, 16, v44
v_and_b32 v83, 0xffff0000, v44
U 84,80,82
v_fmac_f32 v84, v81, v83
Zaaa 76,28,152
Ta 8,15,0
v_sub_f32 v58, v58, v85
v_sub_f32 v59, v59, v85
K 80,58,59
v_lshlrev_b32 v58, 16, v80
v_and_b32 v59, 0xffff0000, v80
Maaa 76,24,56
Ta 10,15,1024
U 80,58,87
U 81,59,87
K 33,80,81
v_lshlrev_b32 v80, 16, v33
v_and_b32 v81, 0xffff0000, v33
Zaaa 236,24,52
Ta 12,15,8192
Ta 14,15,9216
v_lshlrev_b32 v82, 16, v45
v_and_b32 v83, 0xffff0000, v45
v_fmac_f32 v84, v80, v82
v_fmac_f32 v84, v81, v83
Maaa 236,28,148
v_sub_f32 v60, v60, v85
v_sub_f32 v61, v61, v85
K 80,60,61
v_lshlrev_b32 v60, 16, v80
v_and_b32 v61, 0xffff0000, v80
U 80,60,87
Zaaa 204,28,144
U 81,61,87
K 34,80,81
v_lshlrev_b32 v80, 16, v34
v_and_b32 v81, 0xffff0000, v34
v_lshlrev_b32 v82, 16, v46
v_and_b32 v83, 0xffff0000, v46
Maaa 204,24,48
v_fmac_f32 v84, v80, v82
v_fmac_f32 v84, v81, v83
v_sub_f32 v62, v62, v85
v_sub_f32 v63, v63, v85
K 80,62,63
Zaaa 172,24,44
v_lshlrev_b32 v62, 16, v80
v_and_b32 v63, 0xffff0000, v80
U 80,62,87
U 81,63,87
K 35,80,81
Maaa 172,28,140
v_lshlrev_b32 v80, 16, v35
v_and_b32 v81, 0xffff0000, v35
v_lshlrev_b32 v82, 16, v47
v_and_b32 v83, 0xffff0000, v47
v_fmac_f32 v84, v80, v82
Zvaa 236,28,136
v_fmac_f32 v84, v81, v83
v_sub_f32 v64, v64, v85
v_sub_f32 v65, v65, v85
K 80,64,65
v_lshlrev_b32 v64, 16, v80
Mvaa 236,24,40
v_and_b32 v65, 0xffff0000, v80
U 80,64,87
U 81,65,87
K 36,80,81
v_lshlrev_b32 v80, 16, v36
Zvaa 204,24,36
v_and_b32 v81, 0xffff0000, v36
v_lshlrev_b32 v82, 16, v48
v_and_b32 v83, 0xffff0000, v48
v_fmac_f32 v84, v80, v82
v_fmac_f32 v84, v81, v83
Mvaa 204,28,132
v_sub_f32 v66, v66, v85
v_sub_f32 v67, v67, v85
K 80,66,67
v_lshlrev_b32 v66, 16, v80
v_and_b32 v67, 0xffff0000, v80
U 80,66,87
Zvaa 172,28,128
U 81,67,87
K 37,80,81
v_lshlrev_b32 v80, 16, v37
v_and_b32 v81, 0xffff0000, v37
v_lshlrev_b32 v82, 16, v49
Mvaa 172,24,32
W 4
v_and_b32 v83, 0xffff0000, v49
v_fmac_f32 v84, v80, v82
v_fmac_f32 v84, v81, v83
v_sub_f32 v68, v68, v85
v_sub_f32 v69, v69, v85
Zvaa 176,0,32
K 80,68,69
v_lshlrev_b32 v68, 16, v80
v_and_b32 v69, 0xffff0000, v80
U 80,68,87
U 81,69,87
Mvaa 176,4,128
K 38,80,81
v_lshlrev_b32 v80, 16, v38
v_and_b32 v81, 0xffff0000, v38
v_lshlrev_b32 v82, 16, v50
v_and_b32 v83, 0xffff0000, v50
Zvaa 208,4,132
Ta 16,24,0
v_fmac_f32 v84, v80, v82
v_fmac_f32 v84, v81, v83
v_sub_f32 v70, v70, v85
v_sub_f32 v71, v71, v85
K 80,70,71
Mvaa 208,0,36
Ta 18,24,1024
v_lshlrev_b32 v70, 16, v80
v_and_b32 v71, 0xffff0000, v80
U 80,70,87
U 81,71,87
K 39,80,81
Zvaa 240,0,40
Ta 20,24,8192
Ta 22,24,9216
v_lshlrev_b32 v80, 16, v39
v_and_b32 v81, 0xffff0000, v39
v_lshlrev_b32 v82, 16, v51
v_and_b32 v83, 0xffff0000, v51
Mvaa 240,4,136
v_fmac_f32 v84, v80, v82
v_fmac_f32 v84, v81, v83
v_sub_f32 v72, v72, v85
v_sub_f32 v73, v73, v85
K 80,72,73
v_lshlrev_b32 v72, 16, v80
Zaaa 176,4,140
v_and_b32 v73, 0xffff0000, v80
U 80,72,87
U 81,73,87
K 40,80,81
v_lshlrev_b32 v80, 16, v40
v_and_b32 v81, 0xffff0000, v40
Maaa 176,0,44
v_lshlrev_b32 v82, 16, v52
v_and_b32 v83, 0xffff0000, v52
v_fmac_f32 v84, v80, v82
v_fmac_f32 v84, v81, v83
v_sub_f32 v74, v74, v85
Zaaa 208,0,48
v_sub_f32 v75, v75, v85
K 80,74,75
v_lshlrev_b32 v74, 16, v80
v_and_b32 v75, 0xffff0000, v80
U 80,74,87
Maaa 208,4,144
U 81,75,87
K 41,80,81
v_lshlrev_b32 v80, 16, v41
v_and_b32 v81, 0xffff0000, v41
)"
R"(v_lshlrev_b32 v82, 16, v53
Zaaa 240,4,148
v_and_b32 v83, 0xffff0000, v53
v_fmac_f32 v84, v80, v82
v_fmac_f32 v84, v81, v83
v_sub_f32 v76, v76, v85
v_sub_f32 v77, v77, v85
Maaa 240,0,52
K 80,76,77
v_lshlrev_b32 v76, 16, v80
v_and_b32 v77, 0xffff0000, v80
U 80,76,87
U 81,77,87
Zaaa 80,0,56
K 42,80,81
v_lshlrev_b32 v80, 16, v42
v_and_b32 v81, 0xffff0000, v42
v_lshlrev_b32 v82, 16, v54
v_and_b32 v83, 0xffff0000, v54
Maaa 80,4,152
v_fmac_f32 v84, v80, v82
v_fmac_f32 v84, v81, v83
v_sub_f32 v78, v78, v85
v_sub_f32 v79, v79, v85
K 80,78,79
v_lshlrev_b32 v78, 16, v80
Zaaa 112,4,156
v_and_b32 v79, 0xffff0000, v80
U 80,78,87
U 81,79,87
K 43,80,81
v_lshlrev_b32 v80, 16, v43
Maaa 112,0,60
W 4
v_and_b32 v81, 0xffff0000, v43
v_lshlrev_b32 v82, 16, v55
v_and_b32 v83, 0xffff0000, v55
v_fmac_f32 v84, v80, v82
v_fmac_f32 v84, v81, v83
Zaaa 116,8,60
Ta 24,25,0
Ta 26,25,1024
v_lshrrev_b32 v82, 9, v1
s_lshl_b32 s80, s14, 1
v_add_u32 v82, s80, v82
v_mul_u32_u24 v83, 1552, v82
v_add_u32 v83, v83, v88
Maaa 116,12,156
v_add_u32 v83, 131072, v83
ds_write_b128 v83, v[32:35] offset:0
ds_write_b128 v83, v[36:39] offset:512
ds_write_b128 v83, v[40:43] offset:1024
v_add_f32_dpp v84, v84, v84 quad_perm:[1,0,3,2] row_mask:0xf bank_mask:0xf
Zaaa 84,12,152
Ta 28,25,8192
Ta 30,25,9216
Maaa 84,8,56
v_add_f32_dpp v84, v84, v84 quad_perm:[2,3,0,1] row_mask:0xf bank_mask:0xf
s_nop 1
v_add_f32_dpp v84, v84, v84 row_half_mirror row_mask:0xf bank_mask:0xf
s_nop 1
v_add_f32_dpp v84, v84, v84 row_mirror row_mask:0xf bank_mask:0xf
v_mov_b32 v81, v84
s_nop 1
v_permlane16_swap_b32 v84, v81
A 84,84,81
Zaaa 244,8,52
K 80,84,84
v_lshlrev_b32 v80, 16, v80
s_lshl_b32 s80, s15, 3
v_add_u32 v82, s80, v82
v_sub_u32 v83, 2047, v82
v_ashrrev_i32 v81, 31, v80
v_or_b32 v81, 0x80000000, v81
Maaa 244,12,148
v_xor_b32 v80, v80, v81
v_and_b32 v80, 0xffff0000, v80
v_or_b32 v80, v80, v83
v_and_b32 v82, 7, v82
v_lshlrev_b32 v82, 2, v82
v_add_u32 v82, 143488, v82
Zaaa 212,12,144
ds_write_b32 v82, v80
Maaa 212,8,48
Zaaa 180,8,44
Maaa 180,12,140
Zvaa 244,12,136
s_waitcnt vmcnt(1) lgkmcnt(0)
s_barrier
s_add_u32 s64, s64, 128
Mvaa 244,8,40
v_bfe_u32 v80, v1, 4, 3
Zvaa 212,8,36
v_mul_u32_u24 v81, 1552, v80
Mvaa 212,12,132
s_and_b32 s64, s64, 1023
v_lshrrev_b32 v82, 7, v1
Zvaa 180,12,128
v_lshl_add_u32 v81, v82, 4, v81
Mvaa 180,8,32
s_lshl_b32 s80, s14, 7
Zvaa 184,16,32
s_lshl_b32 s65, s64, 12
s_add_u32 s80, s80, 131072
Mvaa 184,20,128
v_add_u32 v81, s80, v81
Zvaa 216,20,132
ds_read_b128 v[32:35], v81
Ta 0,10,32768
Mvaa 216,16,36
s_add_u32 m0, s62, 0
Ta 2,10,33792
Rv 36,81,512
Zvaa 248,16,40
s_and_b32 s80, s14, 2
Ta 4,10,40960
Ta 6,10,41984
Mvaa 248,20,136
s_lshl_b32 s80, s80, 7
Zaaa 184,20,140
D 16,44,65,0
v_subrev_u32 v81, s80, v81
Maaa 184,16,44
Rv 40,81,1024
Zaaa 216,16,48
v_lshlrev_b32 v80, 2, v80
Maaa 216,20,144
v_add_u32 v80, 143488, v80
Zaaa 248,20,148
D 17,44,65,1024
ds_read_b32 v84, v80
Maaa 248,16,52
v_mov_b32 v85, s13
Zaaa 88,16,56
v_lshrrev_b32 v83, 1, v1
Maaa 88,20,152
s_mul_i32 s82, s15, 10240
Zaaa 120,20,156
s_add_u32 s82, s82, 131072
D 18,44,65,2048
Maaa 120,16,60
s_lshl_b32 s80, s14, 10
Zaaa 124,24,60
s_add_u32 s83, s82, s80
Ra 60,26,47104
Maaa 124,28,156
Ra 156,27,47104
Zaaa 92,28,152
D 19,44,65,3072
Ra 152,27,45056
Maaa 92,24,56
s_add_u32 m0, s62, 4096
Ta 8,11,32768
Ta 10,11,33792
Zaaa 252,24,52
Ra 56,26,45056
Ta 12,11,40960
Maaa 252,28,148
Ta 14,11,41984
Ra 52,26,43008
Zaaa 220,28,144
W 9
buffer_store_dwordx4 v[32:35], v1, s[8:11], s83 offen sc1
s_add_u32 s83, s83, 4096
Maaa 220,24,48
Ra 148,27,43008
Ra 144,27,40960
Zaaa 188,24,44
Ra 48,26,40960
Ra 44,26,38912
Maaa 188,28,140
Ra 140,27,38912
Zvaa 252,28,136
D 20,44,65,0
Ra 136,27,36864
Mvaa 252,24,40
Ra 40,26,36864
Zvaa 220,24,36
Ra 36,26,34816
Mvaa 220,28,132
Ra 132,27,34816
Zvaa 188,28,128
buffer_store_dwordx4 v[36:39], v1, s[8:11], s83 offen sc1
Ra 128,27,32768
Mvaa 188,24,32
W 13
s_and_b32 s80, s80, 1024
s_add_u32 s83, s82, s80
Maaa 96,0,60
s_add_u32 s83, s83, 8192
Ra 32,26,32768
Maaa 96,4,156
Maaa 64,4,152
D 21,44,65,1024
Ta 16,12,32768
Maaa 64,0,56
W 9
Ta 18,12,33792
Ta 20,12,40960
Maaa 224,0,52
Ta 22,12,41984
Maaa 224,4,148
Maaa 192,4,144
buffer_store_dwordx4 v[40:43], v1, s[8:11], s83 offen sc1
Maaa 192,0,48
W 8
Maaa 160,0,44
Maaa 160,4,140
Mvaa 224,4,136
D 22,44,65,2048
Mvaa 224,0,40
W 4
Mvaa 192,0,36
Mvaa 192,4,132
Mvaa 160,4,128
D 23,44,65,3072
Mvaa 160,0,32
s_add_u32 m0, s61, 0
Mvaa 164,8,32
Mvaa 164,12,128
Mvaa 196,12,132
D 2,40,64,0
Ta 24,13,32768
Mvaa 196,8,36
Ta 26,13,33792
Ta 28,13,40960
Mvaa 228,8,40
Ta 30,13,41984
Mvaa 228,12,136
.Lh_0_1%=:
s_cbranch_execz .Lh_0_1_1%=
Maaa 164,12,140
D 3,40,64,1024
Maaa 164,8,44
Maaa 196,8,48
Maaa 196,12,144
Maaa 228,12,148
D 4,40,64,2048
Maaa 228,8,52
Maaa 68,8,56
Maaa 68,12,152
Maaa 100,12,156
D 5,40,64,3072
Maaa 100,8,60
W 4
s_add_u32 m0, s61, 4096
Maaa 104,16,60
Maaa 104,20,156
Maaa 72,20,152
D 6,40,64,0
Ta 0,14,32768
Maaa 72,16,56
Ta 2,14,33792
Ta 4,14,40960
Maaa 232,16,52
Ta 6,14,41984
Maaa 232,20,148
Maaa 200,20,144
D 7,40,64,1024
Maaa 200,16,48
Maaa 168,16,44
Maaa 168,20,140
Mvaa 232,20,136
D 8,40,64,2048
Mvaa 232,16,40
Mvaa 200,16,36
Mvaa 200,20,132
Mvaa 168,20,128
D 9,40,64,3072
v_bfe_u32 v147, v1, 5, 3
Mvaa 168,16,32
W 4
v_lshlrev_b32 v147, 13, v147
v_and_b32 v148, 16, v1
Mvaa 172,24,32
v_lshl_add_u32 v147, v148, 3, v147
Mvaa 172,28,128
s_lshl_b32 s92, s57, 2
s_add_u32 s92, s92, s14
s_lshl_b32 s92, s92, 16
Mvaa 204,28,132
buffer_load_dword v142, v147, s[48:51], s92 offen
Ta 8,15,32768
Mvaa 204,24,36
Ta 10,15,33792
Ta 12,15,40960
Mvaa 236,24,40
Ta 14,15,41984
Mvaa 236,28,136
Maaa 172,28,140
Maaa 172,24,44
Maaa 204,24,48
Maaa 204,28,144
Maaa 236,28,148
Maaa 236,24,52
Maaa 76,24,56
Maaa 76,28,152
Maaa 108,28,156
Maaa 108,24,60
W 4
Maaa 112,0,60
Maaa 112,4,156
Maaa 80,4,152
Ta 16,24,32768
Ta 18,24,33792
Maaa 80,0,56
Ta 20,24,40960
Ta 22,24,41984
Maaa 240,0,52
Maaa 240,4,148
Maaa 208,4,144
Maaa 208,0,48
Maaa 176,0,44
Maaa 176,4,140
Mvaa 240,4,136
Mvaa 240,0,40
Mvaa 208,0,36
Mvaa 208,4,132
Mvaa 176,4,128
Mvaa 176,0,32
W 4
Mvaa 180,8,32
Ta 24,25,32768
Ta 26,25,33792
Mvaa 180,12,128
Mvaa 212,12,132
Ta 28,25,40960
Ta 30,25,41984
Mvaa 212,8,36
Mvaa 244,8,40
Mvaa 244,12,136
Maaa 180,12,140
Maaa 180,8,44
Maaa 212,8,48
Maaa 212,12,144
Maaa 244,12,148
s_waitcnt vmcnt(1) lgkmcnt(0)
s_barrier
s_cmp_eq_u32 s14, 0
s_cbranch_scc0 .Lpubr_14%=
s_lshl_b32 s80, s15, 6
s_add_u32 s80, s80, 40960
s_mov_b64 exec, 0xff
buffer_store_dwordx2 v[84:85], v83, s[8:11], s80 offen sc1
s_mov_b64 exec, -1
.Lpubr_14%=:
Maaa 244,8,52
s_add_u32 s64, s64, 128
Maaa 84,8,56
Maaa 84,12,152
s_and_b32 s64, s64, 1023
Maaa 116,12,156
Maaa 116,8,60
Maaa 120,16,60
s_lshl_b32 s65, s64, 12
Maaa 120,20,156
Maaa 88,20,152
s_add_u32 m0, s62, 32768
Ta 0,10,0
Ta 2,10,1024
Maaa 88,16,56
Ta 4,10,8192
Ta 6,10,9216
Maaa 248,16,52
Maaa 248,20,148
D 16,44,65,0
Maaa 216,20,144
Maaa 216,16,48
Maaa 184,16,44
Maaa 184,20,140
D 17,44,65,1024
Mvaa 248,20,136
Mvaa 248,16,40
Mvaa 216,16,36
Mvaa 216,20,132
D 18,44,65,2048
Mvaa 184,20,128
Mvaa 184,16,32
Mvaa 188,24,32
Ra 32,26,0
Mvaa 188,28,128
D 19,44,65,3072
Ra 128,27,0
Mvaa 220,28,132
s_add_u32 m0, s62, 36864
Ra 132,27,2048
Ta 8,11,0
Mvaa 220,24,36
Ta 10,11,1024
Ra 36,26,2048
Mvaa 252,24,40
Ta 12,11,8192
Ta 14,11,9216
Mvaa 252,28,136
D 20,44,65,0
Ra 40,26,4096
Maaa 188,28,140
Ra 136,27,4096
Ra 140,27,6144
Maaa 188,24,44
)"
R"(Ra 44,26,6144
Maaa 220,24,48
Ra 48,26,8192
Maaa 220,28,144
D 21,44,65,1024
Ra 144,27,8192
Maaa 252,28,148
Ra 148,27,10240
Maaa 252,24,52
Ra 52,26,10240
Maaa 92,24,56
Ra 56,26,12288
Maaa 92,28,152
D 22,44,65,2048
Ra 152,27,12288
Maaa 124,28,156
Ra 156,27,14336
Maaa 124,24,60
W 13
Ra 60,26,14336
Mvaa 160,0,32
Mvaa 160,4,128
D 23,44,65,3072
Mvaa 192,4,132
s_add_u32 m0, s61, 32768
Ta 16,12,0
Ta 18,12,1024
Mvaa 192,0,36
W 10
Ta 20,12,8192
Ta 22,12,9216
Mvaa 224,0,40
Mvaa 224,4,136
D 2,40,64,0
Maaa 160,4,140
Maaa 160,0,44
W 8
Maaa 192,0,48
Maaa 192,4,144
D 3,40,64,1024
Maaa 224,4,148
Maaa 224,0,52
W 4
Maaa 64,0,56
Maaa 64,4,152
D 4,40,64,2048
Maaa 96,4,156
Maaa 96,0,60
Maaa 100,8,60
Maaa 100,12,156
D 5,40,64,3072
Maaa 68,12,152
s_add_u32 m0, s61, 36864
Ta 24,13,0
Ta 26,13,1024
Maaa 68,8,56
Ta 28,13,8192
Ta 30,13,9216
Maaa 228,8,52
Maaa 228,12,148
D 6,40,64,0
Maaa 196,12,144
Maaa 196,8,48
Maaa 164,8,44
Maaa 164,12,140
D 7,40,64,1024
Mvaa 228,12,136
Mvaa 228,8,40
Mvaa 196,8,36
Mvaa 196,12,132
D 8,40,64,2048
Mvaa 164,12,128
Mvaa 164,8,32
W 4
Mvaa 168,16,32
Mvaa 168,20,128
D 9,40,64,3072
Mvaa 200,20,132
Ta 0,14,0
Ta 2,14,1024
Mvaa 200,16,36
Ta 4,14,8192
Ta 6,14,9216
Mvaa 232,16,40
Mvaa 232,20,136
global_load_dword v142, v[140:141], off
v_lshl_add_u64 v[140:141], v[140:141], 0, s[68:69]
Maaa 168,20,140
Maaa 168,16,44
Maaa 200,16,48
Maaa 200,20,144
Maaa 232,20,148
Maaa 232,16,52
Maaa 72,16,56
Maaa 72,20,152
Maaa 104,20,156
Maaa 104,16,60
W 4
Maaa 108,24,60
Maaa 108,28,156
Maaa 76,28,152
Ta 8,15,0
Ta 10,15,1024
Maaa 76,24,56
Ta 12,15,8192
Ta 14,15,9216
Maaa 236,24,52
Maaa 236,28,148
Maaa 204,28,144
Maaa 204,24,48
Maaa 172,24,44
Maaa 172,28,140
Mvaa 236,28,136
Mvaa 236,24,40
Mvaa 204,24,36
Mvaa 204,28,132
Mvaa 172,28,128
Mvaa 172,24,32
W 4
Mvaa 176,0,32
Mvaa 176,4,128
Mvaa 208,4,132
Ta 16,24,0
Ta 18,24,1024
Mvaa 208,0,36
Ta 20,24,8192
Ta 22,24,9216
Mvaa 240,0,40
Mvaa 240,4,136
Maaa 176,4,140
Maaa 176,0,44
Maaa 208,0,48
Maaa 208,4,144
Maaa 240,4,148
Maaa 240,0,52
Maaa 80,0,56
Maaa 80,4,152
Maaa 112,4,156
Maaa 112,0,60
W 4
Maaa 116,8,60
Ta 24,25,0
Ta 26,25,1024
Maaa 116,12,156
Maaa 84,12,152
Ta 28,25,8192
Ta 30,25,9216
Maaa 84,8,56
Maaa 244,8,52
Maaa 244,12,148
Maaa 212,12,144
Maaa 212,8,48
Maaa 180,8,44
Maaa 180,12,140
Mvaa 244,12,136
s_waitcnt vmcnt(1) lgkmcnt(0)
s_barrier
s_add_u32 s64, s64, 128
Mvaa 244,8,40
Mvaa 212,8,36
Mvaa 212,12,132
s_and_b32 s64, s64, 1023
Mvaa 180,12,128
Mvaa 180,8,32
Mvaa 184,16,32
s_lshl_b32 s65, s64, 12
Mvaa 184,20,128
Mvaa 216,20,132
s_add_u32 m0, s62, 0
Ta 0,10,32768
Ta 2,10,33792
Mvaa 216,16,36
Ta 4,10,40960
Ta 6,10,41984
Mvaa 248,16,40
Mvaa 248,20,136
D 16,44,65,0
Maaa 184,20,140
Maaa 184,16,44
Maaa 216,16,48
Maaa 216,20,144
D 17,44,65,1024
Maaa 248,20,148
Maaa 248,16,52
Maaa 88,16,56
Maaa 88,20,152
D 18,44,65,2048
Maaa 120,20,156
Maaa 120,16,60
Maaa 124,24,60
Ra 60,26,47104
Maaa 124,28,156
D 19,44,65,3072
Ra 156,27,47104
Maaa 92,28,152
s_add_u32 m0, s62, 4096
Ra 152,27,45056
Ta 8,11,32768
Maaa 92,24,56
Ta 10,11,33792
Ra 56,26,45056
Maaa 252,24,52
Ta 12,11,40960
Ta 14,11,41984
Maaa 252,28,148
D 20,44,65,0
Ra 52,26,43008
Maaa 220,28,144
Ra 148,27,43008
Ra 144,27,40960
Maaa 220,24,48
Ra 48,26,40960
Maaa 188,24,44
Ra 44,26,38912
Maaa 188,28,140
D 21,44,65,1024
Ra 140,27,38912
Mvaa 252,28,136
Ra 136,27,36864
Mvaa 252,24,40
Ra 40,26,36864
Mvaa 220,24,36
Ra 36,26,34816
Mvaa 220,28,132
D 22,44,65,2048
Ra 132,27,34816
Mvaa 188,28,128
Ra 128,27,32768
Mvaa 188,24,32
W 13
Ra 32,26,32768
Maaa 96,0,60
Maaa 96,4,156
D 23,44,65,3072
Maaa 64,4,152
s_add_u32 m0, s61, 0
Ta 16,12,32768
Ta 18,12,33792
Maaa 64,0,56
W 10
Ta 20,12,40960
Ta 22,12,41984
Maaa 224,0,52
Maaa 224,4,148
D 2,40,64,0
Maaa 192,4,144
Maaa 192,0,48
W 8
Maaa 160,0,44
Maaa 160,4,140
D 3,40,64,1024
Mvaa 224,4,136
Mvaa 224,0,40
W 4
Mvaa 192,0,36
Mvaa 192,4,132
D 4,40,64,2048
Mvaa 160,4,128
Mvaa 160,0,32
Mvaa 164,8,32
Mvaa 164,12,128
D 5,40,64,3072
Mvaa 196,12,132
s_add_u32 m0, s61, 4096
Ta 24,13,32768
Ta 26,13,33792
Mvaa 196,8,36
Ta 28,13,40960
Ta 30,13,41984
Mvaa 228,8,40
Mvaa 228,12,136
D 6,40,64,0
Maaa 164,12,140
Maaa 164,8,44
Maaa 196,8,48
Maaa 196,12,144
D 7,40,64,1024
Maaa 228,12,148
Maaa 228,8,52
Maaa 68,8,56
Maaa 68,12,152
D 8,40,64,2048
Maaa 100,12,156
Maaa 100,8,60
W 4
Maaa 104,16,60
Maaa 104,20,156
D 9,40,64,3072
Maaa 72,20,152
Ta 0,14,32768
Ta 2,14,33792
Maaa 72,16,56
Ta 4,14,40960
Ta 6,14,41984
Maaa 232,16,52
Maaa 232,20,148
global_load_dword v142, v[140:141], off
v_lshl_add_u64 v[140:141], v[140:141], 0, s[68:69]
Maaa 200,20,144
Maaa 200,16,48
Maaa 168,16,44
Maaa 168,20,140
Mvaa 232,20,136
Mvaa 232,16,40
Mvaa 200,16,36
Mvaa 200,20,132
Mvaa 168,20,128
Mvaa 168,16,32
W 4
Mvaa 172,24,32
Mvaa 172,28,128
Mvaa 204,28,132
Ta 8,15,32768
Ta 10,15,33792
Mvaa 204,24,36
Ta 12,15,40960
Ta 14,15,41984
Mvaa 236,24,40
Mvaa 236,28,136
Maaa 172,28,140
Maaa 172,24,44
Maaa 204,24,48
Maaa 204,28,144
Maaa 236,28,148
Maaa 236,24,52
Maaa 76,24,56
Maaa 76,28,152
Maaa 108,28,156
Maaa 108,24,60
W 4
Maaa 112,0,60
Maaa 112,4,156
Maaa 80,4,152
Ta 16,24,32768
Ta 18,24,33792
Maaa 80,0,56
Ta 20,24,40960
Ta 22,24,41984
Maaa 240,0,52
Maaa 240,4,148
Maaa 208,4,144
Maaa 208,0,48
Maaa 176,0,44
Maaa 176,4,140
Mvaa 240,4,136
Mvaa 240,0,40
Mvaa 208,0,36
Mvaa 208,4,132
Mvaa 176,4,128
Mvaa 176,0,32
W 4
Mvaa 180,8,32
Ta 24,25,32768
Ta 26,25,33792
Mvaa 180,12,128
Mvaa 212,12,132
Ta 28,25,40960
Ta 30,25,41984
Mvaa 212,8,36
Mvaa 244,8,40
Mvaa 244,12,136
.Lh_0_2%=:
s_cbranch_execz .Lh_0_2_1%=
Maaa 180,12,140
Maaa 180,8,44
Maaa 212,8,48
Maaa 212,12,144
Maaa 244,12,148
s_waitcnt vmcnt(1) lgkmcnt(0)
s_barrier
s_add_u32 s64, s64, 128
Maaa 244,8,52
Maaa 84,8,56
Maaa 84,12,152
s_and_b32 s64, s64, 1023
Maaa 116,12,156
Maaa 116,8,60
Maaa 120,16,60
s_lshl_b32 s65, s64, 12
Maaa 120,20,156
Maaa 88,20,152
Ta 0,10,0
Ta 2,10,1024
Maaa 88,16,56
s_add_u32 m0, s62, 32768
Ta 4,10,8192
Ta 6,10,9216
Maaa 248,16,52
Maaa 248,20,148
Maaa 216,20,144
D 16,44,65,0
Maaa 216,16,48
Maaa 184,16,44
Maaa 184,20,140
Mvaa 248,20,136
D 17,44,65,1024
Mvaa 248,16,40
Mvaa 216,16,36
Mvaa 216,20,132
Mvaa 184,20,128
)"
R"(D 18,44,65,2048
Mvaa 184,16,32
Mvaa 188,24,32
Ra 32,26,0
Mvaa 188,28,128
Ra 128,27,0
Mvaa 220,28,132
D 19,44,65,3072
Ra 132,27,2048
Mvaa 220,24,36
s_add_u32 m0, s62, 36864
Ta 8,11,0
Ta 10,11,1024
Mvaa 252,24,40
Ra 36,26,2048
Ta 12,11,8192
Mvaa 252,28,136
Ta 14,11,9216
Ra 40,26,4096
Maaa 188,28,140
D 20,44,65,0
Ra 136,27,4096
Maaa 188,24,44
Ra 140,27,6144
Ra 44,26,6144
Maaa 220,24,48
Ra 48,26,8192
Maaa 220,28,144
Ra 144,27,8192
Maaa 252,28,148
D 21,44,65,1024
Ra 148,27,10240
Maaa 252,24,52
Ra 52,26,10240
Maaa 92,24,56
Ra 56,26,12288
Maaa 92,28,152
Ra 152,27,12288
Maaa 124,28,156
D 22,44,65,2048
Ra 156,27,14336
Maaa 124,24,60
W 13
Ra 60,26,14336
Mvaa 160,0,32
Mvaa 160,4,128
Mvaa 192,4,132
D 23,44,65,3072
Ta 16,12,0
Mvaa 192,0,36
W 9
s_add_u32 m0, s61, 32768
Ta 18,12,1024
Mvaa 224,0,40
Ta 20,12,8192
Ta 22,12,9216
Mvaa 224,4,136
Maaa 160,4,140
D 2,40,64,0
Maaa 160,0,44
W 8
Maaa 192,0,48
Maaa 192,4,144
Maaa 224,4,148
D 3,40,64,1024
Maaa 224,0,52
W 4
Maaa 64,0,56
Maaa 64,4,152
Maaa 96,4,156
D 4,40,64,2048
Maaa 96,0,60
Maaa 100,8,60
Maaa 100,12,156
Maaa 68,12,152
D 5,40,64,3072
Ta 24,13,0
Maaa 68,8,56
s_add_u32 m0, s61, 36864
Ta 26,13,1024
Ta 28,13,8192
Maaa 228,8,52
Ta 30,13,9216
Maaa 228,12,148
Maaa 196,12,144
D 6,40,64,0
Maaa 196,8,48
Maaa 164,8,44
Maaa 164,12,140
Mvaa 228,12,136
D 7,40,64,1024
Mvaa 228,8,40
Mvaa 196,8,36
Mvaa 196,12,132
Mvaa 164,12,128
D 8,40,64,2048
Mvaa 164,8,32
W 4
Mvaa 168,16,32
Mvaa 168,20,128
Mvaa 200,20,132
D 9,40,64,3072
Ta 0,14,0
Mvaa 200,16,36
s_add_u32 s92, s92, 2097152
Ta 2,14,1024
Ta 4,14,8192
Mvaa 232,16,40
Ta 6,14,9216
Mvaa 232,20,136
Maaa 168,20,140
buffer_load_dword v142, v147, s[48:51], s92 offen
Maaa 168,16,44
s_add_u32 s92, s92, 2097152
Maaa 200,16,48
Maaa 200,20,144
Maaa 232,20,148
buffer_load_dword v142, v147, s[48:51], s92 offen
Maaa 232,16,52
Maaa 72,16,56
Maaa 72,20,152
Maaa 104,20,156
Maaa 104,16,60
W 4
Maaa 108,24,60
Maaa 108,28,156
Maaa 76,28,152
Ta 8,15,0
Ta 10,15,1024
Maaa 76,24,56
Ta 12,15,8192
Ta 14,15,9216
Maaa 236,24,52
Maaa 236,28,148
Maaa 204,28,144
Maaa 204,24,48
Maaa 172,24,44
Maaa 172,28,140
Mvaa 236,28,136
Mvaa 236,24,40
Mvaa 204,24,36
Mvaa 204,28,132
Mvaa 172,28,128
Mvaa 172,24,32
W 4
Mvaa 176,0,32
Mvaa 176,4,128
Mvaa 208,4,132
Ta 16,24,0
Ta 18,24,1024
Mvaa 208,0,36
Ta 20,24,8192
Ta 22,24,9216
Mvaa 240,0,40
Mvaa 240,4,136
Maaa 176,4,140
Maaa 176,0,44
Maaa 208,0,48
Maaa 208,4,144
Maaa 240,4,148
Maaa 240,0,52
Maaa 80,0,56
Maaa 80,4,152
Maaa 112,4,156
Maaa 112,0,60
W 4
Maaa 116,8,60
Ta 24,25,0
Ta 26,25,1024
Maaa 116,12,156
Maaa 84,12,152
Ta 28,25,8192
Ta 30,25,9216
Maaa 84,8,56
Maaa 244,8,52
Maaa 244,12,148
Maaa 212,12,144
Maaa 212,8,48
Maaa 180,8,44
Maaa 180,12,140
Mvaa 244,12,136
s_waitcnt vmcnt(2) lgkmcnt(0)
s_barrier
s_add_u32 s64, s64, 128
Mvaa 244,8,40
Mvaa 212,8,36
Mvaa 212,12,132
s_and_b32 s64, s64, 1023
Mvaa 180,12,128
Mvaa 180,8,32
Mvaa 184,16,32
s_lshl_b32 s65, s64, 12
Mvaa 184,20,128
Mvaa 216,20,132
Ta 0,10,32768
Ta 2,10,33792
Mvaa 216,16,36
s_add_u32 m0, s62, 0
Ta 4,10,40960
Ta 6,10,41984
Mvaa 248,16,40
Mvaa 248,20,136
Maaa 184,20,140
D 16,44,65,0
Maaa 184,16,44
Maaa 216,16,48
Maaa 216,20,144
Maaa 248,20,148
D 17,44,65,1024
Maaa 248,16,52
Maaa 88,16,56
Maaa 88,20,152
Maaa 120,20,156
D 18,44,65,2048
Maaa 120,16,60
Maaa 124,24,60
Ra 60,26,47104
Maaa 124,28,156
Ra 156,27,47104
Maaa 92,28,152
D 19,44,65,3072
Ra 152,27,45056
Maaa 92,24,56
s_add_u32 m0, s62, 4096
Ta 8,11,32768
Ta 10,11,33792
Maaa 252,24,52
Ra 56,26,45056
Ta 12,11,40960
Maaa 252,28,148
Ta 14,11,41984
Ra 52,26,43008
Maaa 220,28,144
D 20,44,65,0
v_lshrrev_b32 v28, 1, v1
Maaa 220,24,48
s_lshl_b32 s80, s60, 9
Ra 148,27,43008
Ra 144,27,40960
Maaa 188,24,44
s_add_u32 s80, s80, 40960
Ra 48,26,40960
Ra 44,26,38912
Maaa 188,28,140
Ra 140,27,38912
Mvaa 252,28,136
D 21,44,65,1024
Ra 136,27,36864
Mvaa 252,24,40
Ra 40,26,36864
Mvaa 220,24,36
Ra 36,26,34816
Mvaa 220,28,132
Ra 132,27,34816
Mvaa 188,28,128
buffer_load_dwordx2 v[28:29], v28, s[8:11], s80 offen sc1
Ra 128,27,32768
Mvaa 188,24,32
W 13
Ra 32,26,32768
Maaa 96,0,60
Maaa 96,4,156
Maaa 64,4,152
D 22,44,65,2048
Ta 16,12,32768
Maaa 64,0,56
W 9
Ta 18,12,33792
Ta 20,12,40960
Maaa 224,0,52
Ta 22,12,41984
Maaa 224,4,148
Maaa 192,4,144
D 23,44,65,3072
Maaa 192,0,48
W 8
s_add_u32 m0, s61, 0
Maaa 160,0,44
Maaa 160,4,140
Mvaa 224,4,136
D 2,40,64,0
Mvaa 224,0,40
W 4
Mvaa 192,0,36
Mvaa 192,4,132
Mvaa 160,4,128
D 3,40,64,1024
Mvaa 160,0,32
Mvaa 164,8,32
Mvaa 164,12,128
Mvaa 196,12,132
D 4,40,64,2048
Ta 24,13,32768
Mvaa 196,8,36
Ta 26,13,33792
Ta 28,13,40960
Mvaa 228,8,40
Ta 30,13,41984
Mvaa 228,12,136
Maaa 164,12,140
D 5,40,64,3072
Maaa 164,8,44
s_add_u32 m0, s61, 4096
Maaa 196,8,48
Maaa 196,12,144
Maaa 228,12,148
D 6,40,64,0
Maaa 228,8,52
Maaa 68,8,56
Maaa 68,12,152
Maaa 100,12,156
D 7,40,64,1024
Maaa 100,8,60
W 4
Maaa 104,16,60
Maaa 104,20,156
Maaa 72,20,152
D 8,40,64,2048
Ta 0,14,32768
Maaa 72,16,56
Ta 2,14,33792
Ta 4,14,40960
Maaa 232,16,52
Ta 6,14,41984
Maaa 232,20,148
Maaa 200,20,144
D 9,40,64,3072
Maaa 200,16,48
Maaa 168,16,44
Maaa 168,20,140
Mvaa 232,20,136
s_waitcnt vmcnt(10)
v_cmp_eq_u32 vcc, s13, v29
s_cmp_eq_u64 vcc, -1
s_cbranch_scc0 .Lspin_15%=
.Lok_16%=:
global_load_dword v142, v146, s[6:7]
Mvaa 232,16,40
Mvaa 200,16,36
v_bfe_u32 v144, v1, 8, 1
Mvaa 200,20,132
v_mul_u32_u24 v144, 10240, v144
Mvaa 168,20,128
v_and_b32 v145, 0xf0, v1
Mvaa 168,16,32
W 4
v_lshl_add_u32 v144, v145, 3, v144
Mvaa 172,24,32
s_mul_i32 s91, s58, 20480
Mvaa 172,28,128
s_add_u32 s91, s91, s28
Mvaa 204,28,132
buffer_load_dword v142, v144, s[8:11], s91 offen offset:0 sc1
Ta 8,15,32768
Mvaa 204,24,36
Ta 10,15,33792
Ta 12,15,40960
Mvaa 236,24,40
Ta 14,15,41984
Mvaa 236,28,136
Maaa 172,28,140
Maaa 172,24,44
Maaa 204,24,48
Maaa 204,28,144
Maaa 236,28,148
Maaa 236,24,52
Maaa 76,24,56
Maaa 76,28,152
Maaa 108,28,156
Maaa 108,24,60
W 4
Maaa 112,0,60
Maaa 112,4,156
Maaa 80,4,152
Ta 16,24,32768
Ta 18,24,33792
Maaa 80,0,56
Ta 20,24,40960
Ta 22,24,41984
Maaa 240,0,52
Maaa 240,4,148
Maaa 208,4,144
Maaa 208,0,48
Maaa 176,0,44
Maaa 176,4,140
Mvaa 240,4,136
Mvaa 240,0,40
Mvaa 208,0,36
Mvaa 208,4,132
Mvaa 176,4,128
Mvaa 176,0,32
W 4
Mvaa 180,8,32
Ta 24,25,32768
Ta 26,25,33792
Mvaa 180,12,128
Mvaa 212,12,132
Ta 28,25,40960
Ta 30,25,41984
Mvaa 212,8,36
)"
R"(Mvaa 244,8,40
Mvaa 244,12,136
Maaa 180,12,140
Maaa 180,8,44
Maaa 212,8,48
Maaa 212,12,144
Maaa 244,12,148
s_waitcnt vmcnt(2) lgkmcnt(0)
s_barrier
s_add_u32 s64, s64, 128
Maaa 244,8,52
Maaa 84,8,56
Maaa 84,12,152
s_and_b32 s64, s64, 1023
Maaa 116,12,156
Maaa 116,8,60
Maaa 120,16,60
s_lshl_b32 s65, s64, 12
Maaa 120,20,156
Maaa 88,20,152
Ta 0,10,0
Ta 2,10,1024
Maaa 88,16,56
s_add_u32 m0, s62, 32768
Ta 4,10,8192
Ta 6,10,9216
Maaa 248,16,52
Maaa 248,20,148
Maaa 216,20,144
D 16,44,65,0
Maaa 216,16,48
Maaa 184,16,44
Maaa 184,20,140
Mvaa 248,20,136
D 17,44,65,1024
Mvaa 248,16,40
Mvaa 216,16,36
Mvaa 216,20,132
Mvaa 184,20,128
D 18,44,65,2048
Mvaa 184,16,32
Mvaa 188,24,32
Ra 32,26,0
Mvaa 188,28,128
Ra 128,27,0
Mvaa 220,28,132
D 19,44,65,3072
Ra 132,27,2048
Mvaa 220,24,36
s_add_u32 m0, s62, 36864
Ta 8,11,0
Ta 10,11,1024
Mvaa 252,24,40
Ra 36,26,2048
Ta 12,11,8192
Mvaa 252,28,136
Ta 14,11,9216
Ra 40,26,4096
Maaa 188,28,140
D 20,44,65,0
Ra 136,27,4096
Maaa 188,24,44
Ra 140,27,6144
Ra 44,26,6144
Maaa 220,24,48
Ra 48,26,8192
Maaa 220,28,144
Ra 144,27,8192
Maaa 252,28,148
D 21,44,65,1024
Ra 148,27,10240
Maaa 252,24,52
Ra 52,26,10240
Maaa 92,24,56
Ra 56,26,12288
Maaa 92,28,152
Ra 152,27,12288
Maaa 124,28,156
D 22,44,65,2048
Ra 156,27,14336
Maaa 124,24,60
W 13
Ra 60,26,14336
Mvaa 160,0,32
Mvaa 160,4,128
Mvaa 192,4,132
D 23,44,65,3072
Ta 16,12,0
Mvaa 192,0,36
W 9
s_add_u32 m0, s61, 32768
Ta 18,12,1024
Mvaa 224,0,40
Ta 20,12,8192
Ta 22,12,9216
Mvaa 224,4,136
.Lh_0_3%=:
s_cbranch_execz .Lh_0_3_1%=
Maaa 160,4,140
D 2,40,64,0
Maaa 160,0,44
W 8
Maaa 192,0,48
Maaa 192,4,144
Maaa 224,4,148
D 3,40,64,1024
Maaa 224,0,52
W 4
Maaa 64,0,56
Maaa 64,4,152
Maaa 96,4,156
D 4,40,64,2048
Maaa 96,0,60
Maaa 100,8,60
Maaa 100,12,156
Maaa 68,12,152
D 5,40,64,3072
Ta 24,13,0
Maaa 68,8,56
s_add_u32 m0, s61, 36864
Ta 26,13,1024
Ta 28,13,8192
Maaa 228,8,52
Ta 30,13,9216
Maaa 228,12,148
Maaa 196,12,144
D 6,40,64,0
Maaa 196,8,48
Maaa 164,8,44
Maaa 164,12,140
Mvaa 228,12,136
D 7,40,64,1024
Mvaa 228,8,40
Mvaa 196,8,36
Mvaa 196,12,132
Mvaa 164,12,128
D 8,40,64,2048
Mvaa 164,8,32
W 4
Mvaa 168,16,32
Mvaa 168,20,128
Mvaa 200,20,132
D 9,40,64,3072
v_add_u32 v2, 131072, v10
Mvaa 200,16,36
Ta 0,14,0
v_add_u32 v3, 131072, v11
Mvaa 232,16,40
Ta 2,14,1024
v_add_u32 v4, 131072, v12
Mvaa 232,20,136
Ta 4,14,8192
Ta 6,14,9216
Maaa 168,20,140
buffer_load_dword v142, v144, s[8:11], s91 offen offset:2048 sc1
v_add_u32 v5, 131072, v13
Maaa 168,16,44
s_add_u32 s91, s91, 4096
v_add_u32 v6, 131072, v14
Maaa 200,16,48
v_add_u32 v7, 131072, v15
Maaa 200,20,144
Maaa 232,20,148
v_add_u32 v8, 131072, v24
Maaa 232,16,52
Maaa 72,16,56
v_add_u32 v9, 131072, v25
Maaa 72,20,152
s_add_u32 s65, s5, 0
s_cmp_ge_u32 s65, 20
s_cselect_b32 s90, 20, 0
s_sub_u32 s65, s65, s90
Maaa 104,20,156
s_lshl_b32 s65, s65, 18
s_add_u32 m0, s63, 131072
Maaa 104,16,60
W 4
Maaa 108,24,60
Maaa 108,28,156
Maaa 76,28,152
Ta 8,15,0
Ta 10,15,1024
Maaa 76,24,56
D 16,48,65,0
Ta 12,15,8192
Maaa 236,24,52
Ta 14,15,9216
Maaa 236,28,148
Maaa 204,28,144
Maaa 204,24,48
D 17,48,65,1024
Maaa 172,24,44
Maaa 172,28,140
Mvaa 236,28,136
Mvaa 236,24,40
Mvaa 204,24,36
D 18,48,65,2048
Mvaa 204,28,132
Mvaa 172,28,128
Mvaa 172,24,32
W 4
Mvaa 176,0,32
Mvaa 176,4,128
D 19,48,65,3072
Mvaa 208,4,132
s_add_u32 m0, s63, 135168
Ta 16,24,0
Ta 18,24,1024
Mvaa 208,0,36
Ta 20,24,8192
Ta 22,24,9216
Mvaa 240,0,40
Mvaa 240,4,136
Maaa 176,4,140
D 20,48,65,0
Maaa 176,0,44
Maaa 208,0,48
Maaa 208,4,144
Maaa 240,4,148
Maaa 240,0,52
D 21,48,65,1024
Maaa 80,0,56
Maaa 80,4,152
Maaa 112,4,156
Maaa 112,0,60
W 4
Maaa 116,8,60
D 22,48,65,2048
Ta 24,25,0
Maaa 116,12,156
Ta 26,25,1024
Maaa 84,12,152
Ta 28,25,8192
Ta 30,25,9216
Maaa 84,8,56
Maaa 244,8,52
D 23,48,65,3072
Maaa 244,12,148
Maaa 212,12,144
Maaa 212,8,48
Maaa 180,8,44
Maaa 180,12,140
Mvaa 244,12,136
s_waitcnt vmcnt(9) lgkmcnt(0)
s_barrier
s_add_u32 s65, s5, 4
Mvaa 244,8,40
Mvaa 212,8,36
Mvaa 212,12,132
s_cmp_ge_u32 s65, 20
s_cselect_b32 s90, 20, 0
Mvaa 180,12,128
Mvaa 180,8,32
s_sub_u32 s65, s65, s90
Mvaa 184,16,32
Mvaa 184,20,128
s_lshl_b32 s65, s65, 18
Mvaa 216,20,132
Ta 0,10,32768
Ta 2,10,33792
Mvaa 216,16,36
Mvaa 248,16,40
s_add_u32 m0, s63, 0
Mvaa 248,20,136
Maaa 184,20,140
D 16,48,65,0
Maaa 184,16,44
Maaa 216,16,48
Maaa 216,20,144
Maaa 248,20,148
D 17,48,65,1024
Ta 4,11,32768
Maaa 248,16,52
Ta 6,11,33792
Maaa 88,16,56
Maaa 88,20,152
Maaa 120,20,156
D 18,48,65,2048
Maaa 120,16,60
Maaa 124,24,60
Ra 60,26,47104
Maaa 124,28,156
Maaa 92,28,152
D 19,48,65,3072
Ta 8,12,32768
Maaa 92,24,56
s_add_u32 m0, s63, 4096
Ta 10,12,33792
Ra 56,26,45056
Maaa 252,24,52
Ra 52,26,43008
Maaa 252,28,148
Maaa 220,28,144
D 20,48,65,0
Maaa 220,24,48
Ra 48,26,40960
Maaa 188,24,44
Ra 44,26,38912
Maaa 188,28,140
Mvaa 252,28,136
D 21,48,65,1024
Ta 12,13,32768
Mvaa 252,24,40
Ta 14,13,33792
Ra 40,26,36864
Mvaa 220,24,36
Ra 36,26,34816
Mvaa 220,28,132
Mvaa 188,28,128
D 22,48,65,2048
Mvaa 188,24,32
Ra 32,26,32768
W 6
Maaa 96,0,60
Maaa 64,0,56
Maaa 224,0,52
D 23,48,65,3072
Ta 16,14,32768
Maaa 192,0,48
W 1
s_add_u32 s65, s5, 8
s_cmp_ge_u32 s65, 20
s_cselect_b32 s90, 20, 0
Maaa 160,0,44
s_sub_u32 s65, s65, s90
s_lshl_b32 s65, s65, 18
Ta 18,14,33792
Mvaa 224,0,40
s_add_u32 m0, s63, 65536
Mvaa 192,0,36
D 16,48,65,0
Mvaa 160,0,32
Mvaa 164,4,32
Mvaa 196,4,36
Mvaa 228,4,40
D 17,48,65,1024
Ta 20,15,32768
Maaa 164,4,44
Ta 22,15,33792
Maaa 196,4,48
Maaa 228,4,52
Maaa 68,4,56
D 18,48,65,2048
Maaa 100,4,60
Maaa 104,8,60
Maaa 72,8,56
Maaa 232,8,52
D 19,48,65,3072
Ta 24,24,32768
Maaa 200,8,48
s_add_u32 m0, s63, 69632
Ta 26,24,33792
Maaa 168,8,44
Mvaa 232,8,40
Mvaa 200,8,36
D 20,48,65,0
Mvaa 168,8,32
Mvaa 172,12,32
Mvaa 204,12,36
Mvaa 236,12,40
D 21,48,65,1024
Ta 28,25,32768
Maaa 172,12,44
Ta 30,25,33792
Maaa 204,12,48
Ra 156,27,47104
Maaa 236,12,52
Maaa 76,12,56
D 22,48,65,2048
Maaa 108,12,60
W 7
Ra 152,27,45056
Maaa 112,16,60
Maaa 80,16,56
Maaa 240,16,52
D 23,48,65,3072
Ra 148,27,43008
Maaa 208,16,48
Ta 0,10,40960
Ta 2,10,41984
Maaa 176,16,44
Mvaa 240,16,40
Ra 144,27,40960
Mvaa 208,16,36
buffer_load_dword v142, v144, s[8:11], s91 offen offset:0 sc1
Mvaa 176,16,32
W 10
Mvaa 180,20,32
Ra 140,27,38912
Mvaa 212,20,36
Mvaa 244,20,40
Ta 4,11,40960
Ta 6,11,41984
Maaa 180,20,44
Ra 136,27,36864
Maaa 212,20,48
Maaa 244,20,52
v_bfe_u32 v29, v1, 7, 1
Maaa 84,20,56
Ra 132,27,34816
)"
R"(Maaa 116,20,60
W 13
v_mul_u32_u24 v28, 10240, v29
Maaa 120,24,60
Maaa 88,24,56
Ra 128,27,32768
v_lshrrev_b32 v29, 8, v1
Maaa 248,24,52
Ta 8,12,40960
Ta 10,12,41984
Maaa 216,24,48
v_lshl_add_u32 v28, v29, 7, v28
Maaa 184,24,44
v_and_b32 v29, 0x70, v1
Mvaa 248,24,40
Mvaa 216,24,36
v_add_u32 v28, v28, v29
Mvaa 184,24,32
W 14
Mvaa 188,28,32
s_and_b32 s80, s14, 2
Mvaa 220,28,36
s_mul_i32 s80, s80, 20480
Mvaa 252,28,40
Ta 12,13,40960
Ta 14,13,41984
Maaa 188,28,44
s_sub_u32 s81, 40960, s80
Maaa 220,28,48
Maaa 252,28,52
s_add_u32 s93, s28, 20480
Maaa 92,28,56
Maaa 124,28,60
W 10
v_add_u32 v26, s80, v28
Maaa 96,0,156
Maaa 64,0,152
Maaa 224,0,148
Ta 16,14,40960
v_add_u32 v27, s81, v28
Maaa 192,0,144
W 5
Ta 18,14,41984
Maaa 160,0,140
buffer_load_dwordx4 a[32:35], v26, s[8:11], s28 offen offset:0 sc1
Mvaa 224,0,136
Mvaa 192,0,132
Mvaa 160,0,128
Mvaa 164,4,128
buffer_load_dwordx4 a[36:39], v26, s[8:11], s93 offen offset:0 sc1
Mvaa 196,4,132
Mvaa 228,4,136
Ta 20,15,40960
Ta 22,15,41984
Maaa 164,4,140
Maaa 196,4,144
buffer_load_dwordx4 a[40:43], v27, s[8:11], s28 offen offset:0 sc1
Maaa 228,4,148
Maaa 68,4,152
Maaa 100,4,156
W 6
Maaa 104,8,156
buffer_load_dwordx4 a[44:47], v27, s[8:11], s93 offen offset:0 sc1
Ta 24,24,40960
Maaa 72,8,152
s_add_u32 s28, s28, 512
s_cmp_eq_u32 s28, s4
s_cselect_b32 s93, 10240, 0
Ta 26,24,41984
Maaa 232,8,148
s_sub_u32 s28, s28, s93
Ta 28,25,40960
Ta 30,25,41984
Maaa 200,8,144
s_add_u32 s93, s28, 20480
Maaa 168,8,140
Mvaa 232,8,136
Mvaa 200,8,132
s_add_u32 s65, s5, 12
s_cmp_ge_u32 s65, 20
s_cselect_b32 s90, 20, 0
buffer_load_dwordx4 a[48:51], v26, s[8:11], s28 offen offset:0 sc1
Mvaa 168,8,128
W 8
s_sub_u32 s65, s65, s90
s_lshl_b32 s65, s65, 18
Mvaa 172,12,128
s_add_u32 m0, s63, 32768
Mvaa 204,12,132
Mvaa 236,12,136
s_waitcnt vmcnt(6) lgkmcnt(0)
s_barrier
buffer_load_dwordx4 a[52:55], v26, s[8:11], s93 offen offset:0 sc1
Maaa 172,12,140
Maaa 204,12,144
Maaa 236,12,148
Maaa 76,12,152
buffer_load_dwordx4 a[56:59], v27, s[8:11], s28 offen offset:0 sc1
Maaa 108,12,156
Maaa 112,16,156
Maaa 80,16,152
Maaa 240,16,148
buffer_load_dwordx4 a[60:63], v27, s[8:11], s93 offen offset:0 sc1
Maaa 208,16,144
Maaa 176,16,140
Mvaa 240,16,136
Mvaa 208,16,132
D 16,48,65,0
Mvaa 176,16,128
Mvaa 180,20,128
Mvaa 212,20,132
Mvaa 244,20,136
D 17,48,65,1024
Ta 0,2,0
Maaa 180,20,140
Ta 2,2,1024
Maaa 212,20,144
Maaa 244,20,148
Maaa 84,20,152
D 18,48,65,2048
Ta 4,2,8192
Maaa 116,20,156
Ta 6,2,9216
Maaa 120,24,156
Maaa 88,24,152
Maaa 248,24,148
Ta 8,3,0
Ta 10,3,1024
Maaa 216,24,144
D 19,48,65,3072
Maaa 184,24,140
s_add_u32 m0, s63, 36864
Mvaa 248,24,136
Mvaa 216,24,132
Ta 12,3,8192
Ta 14,3,9216
Mvaa 184,24,128
D 20,48,65,0
Mvaa 188,28,128
Mvaa 220,28,132
Mvaa 252,28,136
Ta 16,4,0
Ta 18,4,1024
Maaa 188,28,140
Maaa 220,28,144
D 21,48,65,1024
Maaa 252,28,148
Maaa 92,28,152
Maaa 124,28,156
s_branch .Lq_11%=
.p2align 12
.Lv_10%=:
s_waitcnt vmcnt(14)
s_barrier
Ta 0,10,16384
Ta 2,10,17408
Ra 32,26,0
Ta 4,10,24576
.Lh_0_0_1%=:
s_cbranch_execz .Lhr_9%=
Ta 6,10,25600
Ra 128,27,0
Ra 132,27,2048
Ra 36,26,2048
Ra 40,26,4096
Ra 136,27,4096
Ra 140,27,6144
Ra 44,26,6144
Ra 48,26,8192
Ra 144,27,8192
W 6
Zvaa 160,32,0
s_add_u32 m0, s61, 32768
Ra 148,27,10240
Ra 52,26,10240
Ra 56,26,12288
Ra 152,27,12288
Ra 156,27,14336
Ra 60,26,14336
Mvaa 160,128,4
Ta 8,11,16384
Ta 10,11,17408
Ta 12,11,24576
Ta 14,11,25600
Zvaa 164,132,4
Ta 16,12,16384
Ta 18,12,17408
Mvaa 164,36,0
W 14
Ta 20,12,24576
Ta 22,12,25600
Zvaa 224,40,0
D 2,40,64,0
Mvaa 224,136,4
Zvaa 228,140,4
Mvaa 228,44,0
W 12
Zaaa 192,48,0
D 3,40,64,1024
Maaa 192,144,4
Zaaa 196,148,4
Maaa 196,52,0
W 8
Zaaa 64,56,0
D 4,40,64,2048
Maaa 64,152,4
Zaaa 68,156,4
Maaa 68,60,0
W 4
Zaaa 76,60,8
D 5,40,64,3072
Maaa 76,156,12
s_add_u32 m0, s61, 36864
Zaaa 72,152,12
Ta 24,13,16384
Ta 26,13,17408
Maaa 72,56,8
Ta 28,13,24576
Ta 30,13,25600
Zaaa 204,52,8
s_waitcnt vmcnt(15)
D 6,40,64,0
Maaa 204,148,12
v_lshlrev_b32 v56, 16, v32
v_and_b32 v57, 0xffff0000, v32
v_lshlrev_b32 v58, 16, v33
v_and_b32 v59, 0xffff0000, v33
v_lshlrev_b32 v60, 16, v34
Zaaa 200,144,12
v_and_b32 v61, 0xffff0000, v34
v_lshlrev_b32 v62, 16, v35
v_and_b32 v63, 0xffff0000, v35
v_lshlrev_b32 v64, 16, v36
v_and_b32 v65, 0xffff0000, v36
Maaa 200,48,8
v_lshlrev_b32 v66, 16, v37
v_and_b32 v67, 0xffff0000, v37
v_lshlrev_b32 v68, 16, v38
v_and_b32 v69, 0xffff0000, v38
v_lshlrev_b32 v70, 16, v39
Zvaa 236,44,8
D 7,40,64,1024
v_and_b32 v71, 0xffff0000, v39
v_lshlrev_b32 v72, 16, v40
v_and_b32 v73, 0xffff0000, v40
v_lshlrev_b32 v74, 16, v41
v_and_b32 v75, 0xffff0000, v41
Mvaa 236,140,12
v_lshlrev_b32 v76, 16, v42
v_and_b32 v77, 0xffff0000, v42
v_lshlrev_b32 v78, 16, v43
v_and_b32 v79, 0xffff0000, v43
A 80,56,57
Zvaa 232,136,12
A 81,58,59
A 80,80,60
A 81,81,61
A 80,80,62
A 81,81,63
Mvaa 232,40,8
A 80,80,64
A 81,81,65
A 80,80,66
A 81,81,67
A 80,80,68
Zvaa 172,36,8
D 8,40,64,2048
A 81,81,69
A 80,80,70
A 81,81,71
A 80,80,72
A 81,81,73
Mvaa 172,132,12
A 80,80,74
A 81,81,75
A 80,80,76
A 81,81,77
A 80,80,78
A 81,81,79
Zvaa 168,128,12
A 80,80,81
s_nop 1
v_add_f32_dpp v80, v80, v80 quad_perm:[1,0,3,2] row_mask:0xf bank_mask:0xf
s_nop 1
v_add_f32_dpp v80, v80, v80 quad_perm:[2,3,0,1] row_mask:0xf bank_mask:0xf
s_nop 1
v_add_f32_dpp v80, v80, v80 row_half_mirror row_mask:0xf bank_mask:0xf
s_nop 1
v_add_f32_dpp v80, v80, v80 row_mirror row_mask:0xf bank_mask:0xf
Mvaa 168,32,8
W 4
v_mov_b32 v81, v80
s_nop 1
v_permlane16_swap_b32 v80, v81
A 80,80,81
v_mul_f32 v84, 0x3acccccd, v80
s_mov_b32 s86, 0xffff
Zvaa 176,32,16
s_mov_b32 s87, 0xffff
D 9,40,64,3072
v_sub_f32 v82, v56, v84
U 80,82,82
v_sub_f32 v83, v57, v84
v_fmac_f32 v80, v83, v83
Mvaa 176,128,20
v_sub_f32 v82, v58, v84
v_fmac_f32 v80, v82, v82
v_sub_f32 v83, v59, v84
v_fmac_f32 v80, v83, v83
v_sub_f32 v82, v60, v84
Zvaa 180,132,20
Ta 0,14,16384
v_fmac_f32 v80, v82, v82
v_sub_f32 v83, v61, v84
v_fmac_f32 v80, v83, v83
v_sub_f32 v82, v62, v84
v_fmac_f32 v80, v82, v82
Mvaa 180,36,16
Ta 2,14,17408
v_sub_f32 v83, v63, v84
v_fmac_f32 v80, v83, v83
v_sub_f32 v82, v64, v84
v_fmac_f32 v80, v82, v82
v_sub_f32 v83, v65, v84
Zvaa 240,40,16
global_load_dword v142, v[140:141], off
v_lshl_add_u64 v[140:141], v[140:141], 0, s[68:69]
Ta 4,14,24576
Ta 6,14,25600
v_fmac_f32 v80, v83, v83
v_sub_f32 v82, v66, v84
v_fmac_f32 v80, v82, v82
Mvaa 240,136,20
v_sub_f32 v83, v67, v84
v_fmac_f32 v80, v83, v83
v_sub_f32 v82, v68, v84
v_fmac_f32 v80, v82, v82
v_sub_f32 v83, v69, v84
v_fmac_f32 v80, v83, v83
Zvaa 244,140,20
v_sub_f32 v82, v70, v84
v_fmac_f32 v80, v82, v82
v_sub_f32 v83, v71, v84
v_fmac_f32 v80, v83, v83
v_sub_f32 v82, v72, v84
U 81,82,82
Mvaa 244,44,16
v_sub_f32 v83, v73, v84
v_fmac_f32 v81, v83, v83
v_sub_f32 v82, v74, v84
v_fmac_f32 v81, v82, v82
v_sub_f32 v83, v75, v84
v_fmac_f32 v81, v83, v83
Zaaa 208,48,16
v_sub_f32 v82, v76, v84
v_fmac_f32 v81, v82, v82
v_sub_f32 v83, v77, v84
v_fmac_f32 v81, v83, v83
v_sub_f32 v82, v78, v84
Maaa 208,144,20
v_fmac_f32 v81, v82, v82
v_sub_f32 v83, v79, v84
v_fmac_f32 v81, v83, v83
v_cndmask_b32_e64 v81, 0, v81, s[86:87]
A 80,80,81
Zaaa 212,148,20
Maaa 212,52,16
v_add_f32_dpp v80, v80, v80 quad_perm:[1,0,3,2] row_mask:0xf bank_mask:0xf
s_nop 1
v_add_f32_dpp v80, v80, v80 quad_perm:[2,3,0,1] row_mask:0xf bank_mask:0xf
s_nop 1
v_add_f32_dpp v80, v80, v80 row_half_mirror row_mask:0xf bank_mask:0xf
s_nop 1
v_add_f32_dpp v80, v80, v80 row_mirror row_mask:0xf bank_mask:0xf
v_mov_b32 v81, v80
s_nop 1
v_permlane16_swap_b32 v80, v81
Zaaa 80,56,16
A 80,80,81
v_mul_f32 v80, 0x3acccccd, v80
K 80,80,80
v_lshlrev_b32 v80, 16, v80
v_add_f32 v80, 0x3727c5ac, v80
K 80,80,80
Maaa 80,152,20
v_lshlrev_b32 v80, 16, v80
v_sqrt_f32 v81, v80
s_nop 0
v_rcp_f32 v82, v81
v_fma_f32 v83, -v81, v81, v80
Zaaa 84,156,20
v_mul_f32 v82, 0.5, v82
v_fma_f32 v81, v83, v82, v81
K 86,81,81
v_lshlrev_b32 v86, 16, v86
v_rcp_f32 v87, v86
Maaa 84,60,16
W 4
K 85,84,84
v_lshlrev_b32 v85, 16, v85
)"
R"(s_waitcnt vmcnt(17)
v_sub_f32 v56, v56, v85
v_sub_f32 v57, v57, v85
K 80,56,57
Zaaa 92,60,24
v_lshlrev_b32 v56, 16, v80
v_and_b32 v57, 0xffff0000, v80
U 80,56,87
U 81,57,87
K 32,80,81
v_lshlrev_b32 v80, 16, v32
Maaa 92,156,28
v_and_b32 v81, 0xffff0000, v32
v_lshlrev_b32 v82, 16, v44
v_and_b32 v83, 0xffff0000, v44
U 84,80,82
v_fmac_f32 v84, v81, v83
Zaaa 88,152,28
Ta 8,15,16384
v_sub_f32 v58, v58, v85
v_sub_f32 v59, v59, v85
K 80,58,59
v_lshlrev_b32 v58, 16, v80
v_and_b32 v59, 0xffff0000, v80
Maaa 88,56,24
Ta 10,15,17408
U 80,58,87
U 81,59,87
K 33,80,81
v_lshlrev_b32 v80, 16, v33
v_and_b32 v81, 0xffff0000, v33
Zaaa 220,52,24
Ta 12,15,24576
Ta 14,15,25600
v_lshlrev_b32 v82, 16, v45
v_and_b32 v83, 0xffff0000, v45
v_fmac_f32 v84, v80, v82
v_fmac_f32 v84, v81, v83
Maaa 220,148,28
v_sub_f32 v60, v60, v85
v_sub_f32 v61, v61, v85
K 80,60,61
v_lshlrev_b32 v60, 16, v80
v_and_b32 v61, 0xffff0000, v80
U 80,60,87
Zaaa 216,144,28
U 81,61,87
K 34,80,81
v_lshlrev_b32 v80, 16, v34
v_and_b32 v81, 0xffff0000, v34
v_lshlrev_b32 v82, 16, v46
v_and_b32 v83, 0xffff0000, v46
Maaa 216,48,24
v_fmac_f32 v84, v80, v82
v_fmac_f32 v84, v81, v83
v_sub_f32 v62, v62, v85
v_sub_f32 v63, v63, v85
K 80,62,63
Zvaa 252,44,24
v_lshlrev_b32 v62, 16, v80
v_and_b32 v63, 0xffff0000, v80
U 80,62,87
U 81,63,87
K 35,80,81
Mvaa 252,140,28
v_lshlrev_b32 v80, 16, v35
v_and_b32 v81, 0xffff0000, v35
v_lshlrev_b32 v82, 16, v47
v_and_b32 v83, 0xffff0000, v47
v_fmac_f32 v84, v80, v82
Zvaa 248,136,28
v_fmac_f32 v84, v81, v83
v_sub_f32 v64, v64, v85
v_sub_f32 v65, v65, v85
K 80,64,65
v_lshlrev_b32 v64, 16, v80
Mvaa 248,40,24
v_and_b32 v65, 0xffff0000, v80
U 80,64,87
U 81,65,87
K 36,80,81
v_lshlrev_b32 v80, 16, v36
Zvaa 188,36,24
v_and_b32 v81, 0xffff0000, v36
v_lshlrev_b32 v82, 16, v48
v_and_b32 v83, 0xffff0000, v48
v_fmac_f32 v84, v80, v82
v_fmac_f32 v84, v81, v83
Mvaa 188,132,28
v_sub_f32 v66, v66, v85
v_sub_f32 v67, v67, v85
K 80,66,67
v_lshlrev_b32 v66, 16, v80
v_and_b32 v67, 0xffff0000, v80
U 80,66,87
Zvaa 184,128,28
U 81,67,87
K 37,80,81
v_lshlrev_b32 v80, 16, v37
v_and_b32 v81, 0xffff0000, v37
v_lshlrev_b32 v82, 16, v49
Mvaa 184,32,24
W 4
v_and_b32 v83, 0xffff0000, v49
v_fmac_f32 v84, v80, v82
v_fmac_f32 v84, v81, v83
v_sub_f32 v68, v68, v85
v_sub_f32 v69, v69, v85
Zvaa 192,32,0
K 80,68,69
v_lshlrev_b32 v68, 16, v80
v_and_b32 v69, 0xffff0000, v80
U 80,68,87
U 81,69,87
Mvaa 192,128,4
K 38,80,81
v_lshlrev_b32 v80, 16, v38
v_and_b32 v81, 0xffff0000, v38
v_lshlrev_b32 v82, 16, v50
v_and_b32 v83, 0xffff0000, v50
Zvaa 196,132,4
Ta 16,24,16384
v_fmac_f32 v84, v80, v82
v_fmac_f32 v84, v81, v83
v_sub_f32 v70, v70, v85
v_sub_f32 v71, v71, v85
K 80,70,71
Mvaa 196,36,0
Ta 18,24,17408
v_lshlrev_b32 v70, 16, v80
v_and_b32 v71, 0xffff0000, v80
U 80,70,87
U 81,71,87
K 39,80,81
Zaaa 160,40,0
Ta 20,24,24576
Ta 22,24,25600
v_lshlrev_b32 v80, 16, v39
v_and_b32 v81, 0xffff0000, v39
v_lshlrev_b32 v82, 16, v51
v_and_b32 v83, 0xffff0000, v51
Maaa 160,136,4
v_fmac_f32 v84, v80, v82
v_fmac_f32 v84, v81, v83
v_sub_f32 v72, v72, v85
v_sub_f32 v73, v73, v85
K 80,72,73
v_lshlrev_b32 v72, 16, v80
Zaaa 164,140,4
v_and_b32 v73, 0xffff0000, v80
U 80,72,87
U 81,73,87
K 40,80,81
v_lshlrev_b32 v80, 16, v40
v_and_b32 v81, 0xffff0000, v40
Maaa 164,44,0
v_lshlrev_b32 v82, 16, v52
v_and_b32 v83, 0xffff0000, v52
v_fmac_f32 v84, v80, v82
v_fmac_f32 v84, v81, v83
v_sub_f32 v74, v74, v85
Zaaa 224,48,0
v_sub_f32 v75, v75, v85
K 80,74,75
v_lshlrev_b32 v74, 16, v80
v_and_b32 v75, 0xffff0000, v80
U 80,74,87
Maaa 224,144,4
U 81,75,87
K 41,80,81
v_lshlrev_b32 v80, 16, v41
v_and_b32 v81, 0xffff0000, v41
v_lshlrev_b32 v82, 16, v53
Zaaa 228,148,4
v_and_b32 v83, 0xffff0000, v53
v_fmac_f32 v84, v80, v82
v_fmac_f32 v84, v81, v83
v_sub_f32 v76, v76, v85
v_sub_f32 v77, v77, v85
Maaa 228,52,0
K 80,76,77
v_lshlrev_b32 v76, 16, v80
v_and_b32 v77, 0xffff0000, v80
U 80,76,87
U 81,77,87
Zaaa 96,56,0
K 42,80,81
v_lshlrev_b32 v80, 16, v42
v_and_b32 v81, 0xffff0000, v42
v_lshlrev_b32 v82, 16, v54
v_and_b32 v83, 0xffff0000, v54
Maaa 96,152,4
v_fmac_f32 v84, v80, v82
v_fmac_f32 v84, v81, v83
v_sub_f32 v78, v78, v85
v_sub_f32 v79, v79, v85
K 80,78,79
v_lshlrev_b32 v78, 16, v80
Zaaa 100,156,4
v_and_b32 v79, 0xffff0000, v80
U 80,78,87
U 81,79,87
K 43,80,81
v_lshlrev_b32 v80, 16, v43
Maaa 100,60,0
W 4
v_and_b32 v81, 0xffff0000, v43
v_lshlrev_b32 v82, 16, v55
v_and_b32 v83, 0xffff0000, v55
v_fmac_f32 v84, v80, v82
v_fmac_f32 v84, v81, v83
Zaaa 108,60,8
Ta 24,25,16384
Ta 26,25,17408
v_lshrrev_b32 v82, 9, v1
s_lshl_b32 s80, s14, 1
v_add_u32 v82, s80, v82
v_mul_u32_u24 v83, 1552, v82
v_add_u32 v83, v83, v88
Maaa 108,156,12
v_add_u32 v83, 131072, v83
ds_write_b128 v83, v[32:35] offset:0
ds_write_b128 v83, v[36:39] offset:512
ds_write_b128 v83, v[40:43] offset:1024
v_add_f32_dpp v84, v84, v84 quad_perm:[1,0,3,2] row_mask:0xf bank_mask:0xf
Zaaa 104,152,12
Ta 28,25,24576
Ta 30,25,25600
Maaa 104,56,8
v_add_f32_dpp v84, v84, v84 quad_perm:[2,3,0,1] row_mask:0xf bank_mask:0xf
s_nop 1
v_add_f32_dpp v84, v84, v84 row_half_mirror row_mask:0xf bank_mask:0xf
s_nop 1
v_add_f32_dpp v84, v84, v84 row_mirror row_mask:0xf bank_mask:0xf
v_mov_b32 v81, v84
s_nop 1
v_permlane16_swap_b32 v84, v81
A 84,84,81
Zaaa 236,52,8
K 80,84,84
v_lshlrev_b32 v80, 16, v80
s_lshl_b32 s80, s15, 3
v_add_u32 v82, s80, v82
v_sub_u32 v83, 2047, v82
v_ashrrev_i32 v81, 31, v80
v_or_b32 v81, 0x80000000, v81
Maaa 236,148,12
v_xor_b32 v80, v80, v81
v_and_b32 v80, 0xffff0000, v80
v_or_b32 v80, v80, v83
v_and_b32 v82, 7, v82
v_lshlrev_b32 v82, 2, v82
v_add_u32 v82, 143488, v82
Zaaa 232,144,12
ds_write_b32 v82, v80
Maaa 232,48,8
Zaaa 172,44,8
Maaa 172,140,12
Zaaa 168,136,12
s_waitcnt vmcnt(1) lgkmcnt(0)
s_barrier
s_add_u32 s64, s64, 128
Maaa 168,40,8
v_bfe_u32 v80, v1, 4, 3
Zvaa 204,36,8
v_mul_u32_u24 v81, 1552, v80
Mvaa 204,132,12
s_and_b32 s64, s64, 1023
v_lshrrev_b32 v82, 7, v1
Zvaa 200,128,12
v_lshl_add_u32 v81, v82, 4, v81
Mvaa 200,32,8
s_lshl_b32 s80, s14, 7
Zvaa 208,32,16
s_lshl_b32 s65, s64, 12
s_add_u32 s80, s80, 131072
Mvaa 208,128,20
v_add_u32 v81, s80, v81
Zvaa 212,132,20
s_add_u32 m0, s62, 0
ds_read_b128 v[32:35], v81
Ta 0,10,49152
Mvaa 212,36,16
Ta 2,10,50176
Rv 36,81,512
Zaaa 176,40,16
s_and_b32 s80, s14, 2
Ta 4,10,57344
Ta 6,10,58368
Maaa 176,136,20
s_lshl_b32 s80, s80, 7
D 16,44,65,0
Zaaa 180,140,20
v_subrev_u32 v81, s80, v81
Maaa 180,44,16
Rv 40,81,1024
Zaaa 240,48,16
v_lshlrev_b32 v80, 2, v80
Maaa 240,144,20
D 17,44,65,1024
v_add_u32 v80, 143488, v80
Zaaa 244,148,20
ds_read_b32 v84, v80
Maaa 244,52,16
v_mov_b32 v85, s13
Zaaa 112,56,16
v_lshrrev_b32 v83, 1, v1
Maaa 112,152,20
s_mul_i32 s82, s15, 10240
D 18,44,65,2048
Zaaa 116,156,20
s_add_u32 s82, s82, 131072
Maaa 116,60,16
s_lshl_b32 s80, s14, 10
Zaaa 124,60,24
s_add_u32 s83, s82, s80
Ra 60,26,47104
Maaa 124,156,28
D 19,44,65,3072
Ra 156,27,47104
Zaaa 120,152,28
s_add_u32 m0, s62, 4096
Ra 152,27,45056
Ta 8,11,49152
Maaa 120,56,24
Ta 10,11,50176
Ra 56,26,45056
Zaaa 252,52,24
Ta 12,11,57344
Ta 14,11,58368
Maaa 252,148,28
W 8
buffer_store_dwordx4 v[32:35], v1, s[8:11], s83 offen sc1
s_add_u32 s83, s83, 4096
Zaaa 248,144,28
Ra 52,26,43008
Ra 148,27,43008
Maaa 248,48,24
Ra 144,27,40960
Ra 48,26,40960
Zaaa 188,44,24
Ra 44,26,38912
Maaa 188,140,28
D 20,44,65,0
Ra 140,27,38912
Zaaa 184,136,28
Ra 136,27,36864
Maaa 184,40,24
Ra 40,26,36864
Zvaa 220,36,24
Ra 36,26,34816
Mvaa 220,132,28
buffer_store_dwordx4 v[36:39], v1, s[8:11], s83 offen sc1
Ra 132,27,34816
Zvaa 216,128,28
s_and_b32 s80, s80, 1024
s_add_u32 s83, s82, s80
s_add_u32 s83, s83, 8192
Mvaa 216,32,24
W 12
Ra 128,27,32768
Ra 32,26,32768
Maaa 68,60,0
Maaa 68,156,4
D 21,44,65,1024
Maaa 64,152,4
Ta 16,12,49152
Ta 18,12,50176
Maaa 64,56,0
W 10
Ta 20,12,57344
Ta 22,12,58368
Maaa 196,52,0
Maaa 196,148,4
D 22,44,65,2048
Maaa 192,144,4
Maaa 192,48,0
W 8
Mvaa 228,44,0
Mvaa 228,140,4
buffer_store_dwordx4 v[40:43], v1, s[8:11], s83 offen sc1
Mvaa 224,136,4
Mvaa 224,40,0
W 4
Mvaa 164,36,0
Mvaa 164,132,4
D 23,44,65,3072
Mvaa 160,128,4
s_add_u32 m0, s61, 0
Mvaa 160,32,0
Mvaa 168,32,8
Mvaa 168,128,12
D 2,40,64,0
Mvaa 172,132,12
Ta 24,13,49152
Ta 26,13,50176
Mvaa 172,36,8
Ta 28,13,57344
Ta 30,13,58368
Mvaa 232,40,8
)"
R"(.Lh_0_1_1%=:
s_cbranch_execz .Lhr_9%=
Mvaa 232,136,12
D 3,40,64,1024
Mvaa 236,140,12
Mvaa 236,44,8
Maaa 200,48,8
Maaa 200,144,12
D 4,40,64,2048
Maaa 204,148,12
Maaa 204,52,8
Maaa 72,56,8
Maaa 72,152,12
D 5,40,64,3072
Maaa 76,156,12
s_add_u32 m0, s61, 4096
Maaa 76,60,8
W 4
Maaa 84,60,16
Maaa 84,156,20
D 6,40,64,0
Maaa 80,152,20
Ta 0,14,49152
Ta 2,14,50176
Maaa 80,56,16
Ta 4,14,57344
Ta 6,14,58368
Maaa 212,52,16
Maaa 212,148,20
D 7,40,64,1024
Maaa 208,144,20
Maaa 208,48,16
Mvaa 244,44,16
Mvaa 244,140,20
D 8,40,64,2048
Mvaa 240,136,20
Mvaa 240,40,16
Mvaa 180,36,16
Mvaa 180,132,20
D 9,40,64,3072
Mvaa 176,128,20
Mvaa 176,32,16
W 4
Mvaa 184,32,24
Mvaa 184,128,28
global_load_dword v142, v[140:141], off
v_lshl_add_u64 v[140:141], v[140:141], 0, s[68:69]
v_bfe_u32 v147, v1, 5, 3
Mvaa 188,132,28
v_lshlrev_b32 v147, 13, v147
v_and_b32 v148, 16, v1
Mvaa 188,36,24
Ta 8,15,49152
v_lshl_add_u32 v147, v148, 3, v147
Mvaa 248,40,24
s_lshl_b32 s92, s57, 2
s_add_u32 s92, s92, s14
s_lshl_b32 s92, s92, 16
Mvaa 248,136,28
buffer_load_dword v142, v147, s[48:51], s92 offen
Ta 10,15,50176
Mvaa 252,140,28
Ta 12,15,57344
Ta 14,15,58368
Mvaa 252,44,24
Maaa 216,48,24
Maaa 216,144,28
Maaa 220,148,28
Maaa 220,52,24
Maaa 88,56,24
Maaa 88,152,28
Maaa 92,156,28
Maaa 92,60,24
W 4
Maaa 100,60,0
Maaa 100,156,4
Maaa 96,152,4
Ta 16,24,49152
Ta 18,24,50176
Maaa 96,56,0
Ta 20,24,57344
Ta 22,24,58368
Maaa 228,52,0
Maaa 228,148,4
Maaa 224,144,4
Maaa 224,48,0
Maaa 164,44,0
Maaa 164,140,4
Maaa 160,136,4
Maaa 160,40,0
Mvaa 196,36,0
Mvaa 196,132,4
Mvaa 192,128,4
Mvaa 192,32,0
W 4
Mvaa 200,32,8
Ta 24,25,49152
Ta 26,25,50176
Mvaa 200,128,12
Mvaa 204,132,12
Ta 28,25,57344
Ta 30,25,58368
Mvaa 204,36,8
Maaa 168,40,8
Maaa 168,136,12
Maaa 172,140,12
Maaa 172,44,8
Maaa 232,48,8
Maaa 232,144,12
Maaa 236,148,12
s_waitcnt vmcnt(2) lgkmcnt(0)
s_barrier
s_cmp_eq_u32 s14, 0
s_cbranch_scc0 .Lpubr_19%=
s_lshl_b32 s80, s15, 6
s_add_u32 s80, s80, 40960
s_mov_b64 exec, 0xff
buffer_store_dwordx2 v[84:85], v83, s[8:11], s80 offen sc1
s_mov_b64 exec, -1
.Lpubr_19%=:
Maaa 236,52,8
s_add_u32 s64, s64, 128
Maaa 104,56,8
Maaa 104,152,12
s_and_b32 s64, s64, 1023
Maaa 108,156,12
Maaa 108,60,8
Maaa 116,60,16
s_lshl_b32 s65, s64, 12
Maaa 116,156,20
Maaa 112,152,20
Ta 0,10,16384
Ta 2,10,17408
Maaa 112,56,16
s_add_u32 m0, s62, 32768
Ta 4,10,24576
Ta 6,10,25600
Maaa 244,52,16
Maaa 244,148,20
Maaa 240,144,20
D 16,44,65,0
Maaa 240,48,16
Maaa 180,44,16
Maaa 180,140,20
Maaa 176,136,20
D 17,44,65,1024
Maaa 176,40,16
Mvaa 212,36,16
Mvaa 212,132,20
Mvaa 208,128,20
D 18,44,65,2048
Mvaa 208,32,16
Mvaa 216,32,24
Ra 32,26,0
Mvaa 216,128,28
Ra 128,27,0
Mvaa 220,132,28
D 19,44,65,3072
Ra 132,27,2048
Mvaa 220,36,24
s_add_u32 m0, s62, 36864
Ta 8,11,16384
Ta 10,11,17408
Maaa 184,40,24
Ra 36,26,2048
Ta 12,11,24576
Maaa 184,136,28
Ta 14,11,25600
Ra 40,26,4096
Maaa 188,140,28
D 20,44,65,0
Ra 136,27,4096
Maaa 188,44,24
Ra 140,27,6144
Ra 44,26,6144
Maaa 248,48,24
Ra 48,26,8192
Maaa 248,144,28
Ra 144,27,8192
Maaa 252,148,28
D 21,44,65,1024
Ra 148,27,10240
Maaa 252,52,24
Ra 52,26,10240
Maaa 120,56,24
Ra 56,26,12288
Maaa 120,152,28
Ra 152,27,12288
Maaa 124,156,28
D 22,44,65,2048
Ra 156,27,14336
Maaa 124,60,24
W 13
Ra 60,26,14336
Mvaa 160,32,0
Mvaa 160,128,4
Mvaa 164,132,4
D 23,44,65,3072
Ta 16,12,16384
Mvaa 164,36,0
W 9
s_add_u32 m0, s61, 32768
Ta 18,12,17408
Mvaa 224,40,0
Ta 20,12,24576
Ta 22,12,25600
Mvaa 224,136,4
Mvaa 228,140,4
D 2,40,64,0
Mvaa 228,44,0
W 8
Maaa 192,48,0
Maaa 192,144,4
Maaa 196,148,4
D 3,40,64,1024
Maaa 196,52,0
W 4
Maaa 64,56,0
Maaa 64,152,4
Maaa 68,156,4
D 4,40,64,2048
Maaa 68,60,0
Maaa 76,60,8
Maaa 76,156,12
Maaa 72,152,12
D 5,40,64,3072
Ta 24,13,16384
Maaa 72,56,8
s_add_u32 m0, s61, 36864
Ta 26,13,17408
Ta 28,13,24576
Maaa 204,52,8
Ta 30,13,25600
Maaa 204,148,12
Maaa 200,144,12
D 6,40,64,0
Maaa 200,48,8
Mvaa 236,44,8
Mvaa 236,140,12
Mvaa 232,136,12
D 7,40,64,1024
Mvaa 232,40,8
Mvaa 172,36,8
Mvaa 172,132,12
Mvaa 168,128,12
D 8,40,64,2048
Mvaa 168,32,8
W 4
Mvaa 176,32,16
Mvaa 176,128,20
Mvaa 180,132,20
D 9,40,64,3072
Ta 0,14,16384
Mvaa 180,36,16
Ta 2,14,17408
Ta 4,14,24576
Mvaa 240,40,16
Ta 6,14,25600
Mvaa 240,136,20
Mvaa 244,140,20
Mvaa 244,44,16
Maaa 208,48,16
Maaa 208,144,20
Maaa 212,148,20
Maaa 212,52,16
Maaa 80,56,16
Maaa 80,152,20
Maaa 84,156,20
Maaa 84,60,16
W 4
Maaa 92,60,24
Maaa 92,156,28
Maaa 88,152,28
Ta 8,15,16384
Ta 10,15,17408
Maaa 88,56,24
Ta 12,15,24576
Ta 14,15,25600
Maaa 220,52,24
Maaa 220,148,28
Maaa 216,144,28
Maaa 216,48,24
Mvaa 252,44,24
Mvaa 252,140,28
Mvaa 248,136,28
Mvaa 248,40,24
Mvaa 188,36,24
Mvaa 188,132,28
Mvaa 184,128,28
Mvaa 184,32,24
W 4
Mvaa 192,32,0
Mvaa 192,128,4
Mvaa 196,132,4
Ta 16,24,16384
Ta 18,24,17408
Mvaa 196,36,0
Ta 20,24,24576
Ta 22,24,25600
Maaa 160,40,0
Maaa 160,136,4
Maaa 164,140,4
Maaa 164,44,0
Maaa 224,48,0
Maaa 224,144,4
Maaa 228,148,4
Maaa 228,52,0
Maaa 96,56,0
Maaa 96,152,4
Maaa 100,156,4
Maaa 100,60,0
W 4
Maaa 108,60,8
Ta 24,25,16384
Ta 26,25,17408
Maaa 108,156,12
Maaa 104,152,12
Ta 28,25,24576
Ta 30,25,25600
Maaa 104,56,8
Maaa 236,52,8
Maaa 236,148,12
Maaa 232,144,12
Maaa 232,48,8
Maaa 172,44,8
Maaa 172,140,12
Maaa 168,136,12
s_waitcnt vmcnt(0) lgkmcnt(0)
s_barrier
s_add_u32 s64, s64, 128
Maaa 168,40,8
Mvaa 204,36,8
Mvaa 204,132,12
s_and_b32 s64, s64, 1023
Mvaa 200,128,12
Mvaa 200,32,8
Mvaa 208,32,16
s_lshl_b32 s65, s64, 12
Mvaa 208,128,20
Mvaa 212,132,20
s_add_u32 m0, s62, 0
Ta 0,10,49152
Ta 2,10,50176
Mvaa 212,36,16
Ta 4,10,57344
Ta 6,10,58368
Maaa 176,40,16
Maaa 176,136,20
D 16,44,65,0
Maaa 180,140,20
Maaa 180,44,16
Maaa 240,48,16
Maaa 240,144,20
D 17,44,65,1024
Maaa 244,148,20
Maaa 244,52,16
Maaa 112,56,16
Maaa 112,152,20
D 18,44,65,2048
Maaa 116,156,20
Maaa 116,60,16
Maaa 124,60,24
Ra 60,26,47104
Maaa 124,156,28
D 19,44,65,3072
Ra 156,27,47104
Maaa 120,152,28
s_add_u32 m0, s62, 4096
Ra 152,27,45056
Ta 8,11,49152
Maaa 120,56,24
Ta 10,11,50176
Ra 56,26,45056
Maaa 252,52,24
Ta 12,11,57344
Ta 14,11,58368
Maaa 252,148,28
D 20,44,65,0
Ra 52,26,43008
Maaa 248,144,28
Ra 148,27,43008
Ra 144,27,40960
Maaa 248,48,24
Ra 48,26,40960
Maaa 188,44,24
Ra 44,26,38912
Maaa 188,140,28
D 21,44,65,1024
Ra 140,27,38912
Maaa 184,136,28
Ra 136,27,36864
Maaa 184,40,24
Ra 40,26,36864
Mvaa 220,36,24
Ra 36,26,34816
Mvaa 220,132,28
D 22,44,65,2048
Ra 132,27,34816
Mvaa 216,128,28
Ra 128,27,32768
)"
R"(Mvaa 216,32,24
W 13
Ra 32,26,32768
Maaa 68,60,0
Maaa 68,156,4
D 23,44,65,3072
Maaa 64,152,4
s_add_u32 m0, s61, 0
Ta 16,12,49152
Ta 18,12,50176
Maaa 64,56,0
W 10
Ta 20,12,57344
Ta 22,12,58368
Maaa 196,52,0
Maaa 196,148,4
D 2,40,64,0
Maaa 192,144,4
Maaa 192,48,0
W 8
Mvaa 228,44,0
Mvaa 228,140,4
D 3,40,64,1024
Mvaa 224,136,4
Mvaa 224,40,0
W 4
Mvaa 164,36,0
Mvaa 164,132,4
D 4,40,64,2048
Mvaa 160,128,4
Mvaa 160,32,0
Mvaa 168,32,8
Mvaa 168,128,12
D 5,40,64,3072
Mvaa 172,132,12
s_add_u32 m0, s61, 4096
Ta 24,13,49152
Ta 26,13,50176
Mvaa 172,36,8
Ta 28,13,57344
Ta 30,13,58368
Mvaa 232,40,8
Mvaa 232,136,12
D 6,40,64,0
Mvaa 236,140,12
Mvaa 236,44,8
Maaa 200,48,8
Maaa 200,144,12
D 7,40,64,1024
Maaa 204,148,12
Maaa 204,52,8
Maaa 72,56,8
Maaa 72,152,12
D 8,40,64,2048
Maaa 76,156,12
Maaa 76,60,8
W 4
Maaa 84,60,16
Maaa 84,156,20
D 9,40,64,3072
Maaa 80,152,20
Ta 0,14,49152
Ta 2,14,50176
Maaa 80,56,16
Ta 4,14,57344
Ta 6,14,58368
Maaa 212,52,16
Maaa 212,148,20
global_load_dword v142, v[140:141], off
v_lshl_add_u64 v[140:141], v[140:141], 0, s[68:69]
Maaa 208,144,20
Maaa 208,48,16
Mvaa 244,44,16
Mvaa 244,140,20
Mvaa 240,136,20
Mvaa 240,40,16
Mvaa 180,36,16
Mvaa 180,132,20
Mvaa 176,128,20
Mvaa 176,32,16
W 4
Mvaa 184,32,24
Mvaa 184,128,28
Mvaa 188,132,28
Ta 8,15,49152
Ta 10,15,50176
Mvaa 188,36,24
Ta 12,15,57344
Ta 14,15,58368
Mvaa 248,40,24
Mvaa 248,136,28
Mvaa 252,140,28
Mvaa 252,44,24
Maaa 216,48,24
Maaa 216,144,28
Maaa 220,148,28
Maaa 220,52,24
Maaa 88,56,24
Maaa 88,152,28
Maaa 92,156,28
Maaa 92,60,24
W 4
Maaa 100,60,0
Maaa 100,156,4
Maaa 96,152,4
Ta 16,24,49152
Ta 18,24,50176
Maaa 96,56,0
Ta 20,24,57344
Ta 22,24,58368
Maaa 228,52,0
Maaa 228,148,4
Maaa 224,144,4
Maaa 224,48,0
Maaa 164,44,0
Maaa 164,140,4
Maaa 160,136,4
Maaa 160,40,0
Mvaa 196,36,0
Mvaa 196,132,4
Mvaa 192,128,4
Mvaa 192,32,0
W 4
Mvaa 200,32,8
Ta 24,25,49152
Ta 26,25,50176
Mvaa 200,128,12
Mvaa 204,132,12
Ta 28,25,57344
Ta 30,25,58368
Mvaa 204,36,8
Maaa 168,40,8
Maaa 168,136,12
.Lh_0_2_1%=:
s_cbranch_execz .Lhr_9%=
Maaa 172,140,12
Maaa 172,44,8
Maaa 232,48,8
Maaa 232,144,12
Maaa 236,148,12
s_waitcnt vmcnt(1) lgkmcnt(0)
s_barrier
s_add_u32 s64, s64, 128
Maaa 236,52,8
Maaa 104,56,8
Maaa 104,152,12
s_and_b32 s64, s64, 1023
Maaa 108,156,12
Maaa 108,60,8
Maaa 116,60,16
s_lshl_b32 s65, s64, 12
Maaa 116,156,20
Maaa 112,152,20
Ta 0,10,16384
Ta 2,10,17408
Maaa 112,56,16
s_add_u32 m0, s62, 32768
Ta 4,10,24576
Ta 6,10,25600
Maaa 244,52,16
Maaa 244,148,20
Maaa 240,144,20
D 16,44,65,0
Maaa 240,48,16
Maaa 180,44,16
Maaa 180,140,20
Maaa 176,136,20
D 17,44,65,1024
Maaa 176,40,16
Mvaa 212,36,16
Mvaa 212,132,20
Mvaa 208,128,20
D 18,44,65,2048
Mvaa 208,32,16
Mvaa 216,32,24
Ra 32,26,0
Mvaa 216,128,28
Ra 128,27,0
Mvaa 220,132,28
D 19,44,65,3072
Ra 132,27,2048
Mvaa 220,36,24
s_add_u32 m0, s62, 36864
Ta 8,11,16384
Ta 10,11,17408
Maaa 184,40,24
Ra 36,26,2048
Ta 12,11,24576
Maaa 184,136,28
Ta 14,11,25600
Ra 40,26,4096
Maaa 188,140,28
D 20,44,65,0
Ra 136,27,4096
Maaa 188,44,24
Ra 140,27,6144
Ra 44,26,6144
Maaa 248,48,24
Ra 48,26,8192
Maaa 248,144,28
Ra 144,27,8192
Maaa 252,148,28
D 21,44,65,1024
Ra 148,27,10240
Maaa 252,52,24
Ra 52,26,10240
Maaa 120,56,24
Ra 56,26,12288
Maaa 120,152,28
Ra 152,27,12288
Maaa 124,156,28
D 22,44,65,2048
Ra 156,27,14336
Maaa 124,60,24
W 13
Ra 60,26,14336
Mvaa 160,32,0
Mvaa 160,128,4
Mvaa 164,132,4
D 23,44,65,3072
Ta 16,12,16384
Mvaa 164,36,0
W 9
s_add_u32 m0, s61, 32768
Ta 18,12,17408
Mvaa 224,40,0
Ta 20,12,24576
Ta 22,12,25600
Mvaa 224,136,4
Mvaa 228,140,4
D 2,40,64,0
Mvaa 228,44,0
W 8
Maaa 192,48,0
Maaa 192,144,4
Maaa 196,148,4
D 3,40,64,1024
Maaa 196,52,0
W 4
Maaa 64,56,0
Maaa 64,152,4
Maaa 68,156,4
D 4,40,64,2048
Maaa 68,60,0
Maaa 76,60,8
Maaa 76,156,12
Maaa 72,152,12
D 5,40,64,3072
Ta 24,13,16384
Maaa 72,56,8
s_add_u32 m0, s61, 36864
Ta 26,13,17408
Ta 28,13,24576
Maaa 204,52,8
Ta 30,13,25600
Maaa 204,148,12
Maaa 200,144,12
D 6,40,64,0
Maaa 200,48,8
Mvaa 236,44,8
Mvaa 236,140,12
Mvaa 232,136,12
D 7,40,64,1024
Mvaa 232,40,8
Mvaa 172,36,8
Mvaa 172,132,12
Mvaa 168,128,12
D 8,40,64,2048
Mvaa 168,32,8
W 4
Mvaa 176,32,16
Mvaa 176,128,20
Mvaa 180,132,20
D 9,40,64,3072
Ta 0,14,16384
Mvaa 180,36,16
s_add_u32 s92, s92, 2097152
Ta 2,14,17408
Ta 4,14,24576
Mvaa 240,40,16
Ta 6,14,25600
Mvaa 240,136,20
Mvaa 244,140,20
buffer_load_dword v142, v147, s[48:51], s92 offen
Mvaa 244,44,16
s_add_u32 s92, s92, 2097152
Maaa 208,48,16
Maaa 208,144,20
Maaa 212,148,20
buffer_load_dword v142, v147, s[48:51], s92 offen
Maaa 212,52,16
Maaa 80,56,16
Maaa 80,152,20
Maaa 84,156,20
Maaa 84,60,16
W 4
Maaa 92,60,24
Maaa 92,156,28
Maaa 88,152,28
Ta 8,15,16384
Ta 10,15,17408
Maaa 88,56,24
Ta 12,15,24576
Ta 14,15,25600
Maaa 220,52,24
Maaa 220,148,28
Maaa 216,144,28
Maaa 216,48,24
Mvaa 252,44,24
Mvaa 252,140,28
Mvaa 248,136,28
Mvaa 248,40,24
Mvaa 188,36,24
Mvaa 188,132,28
Mvaa 184,128,28
Mvaa 184,32,24
W 4
Mvaa 192,32,0
Mvaa 192,128,4
Mvaa 196,132,4
Ta 16,24,16384
Ta 18,24,17408
Mvaa 196,36,0
Ta 20,24,24576
Ta 22,24,25600
Maaa 160,40,0
Maaa 160,136,4
Maaa 164,140,4
Maaa 164,44,0
Maaa 224,48,0
Maaa 224,144,4
Maaa 228,148,4
Maaa 228,52,0
Maaa 96,56,0
Maaa 96,152,4
Maaa 100,156,4
Maaa 100,60,0
W 4
Maaa 108,60,8
Ta 24,25,16384
Ta 26,25,17408
Maaa 108,156,12
Maaa 104,152,12
Ta 28,25,24576
Ta 30,25,25600
Maaa 104,56,8
Maaa 236,52,8
Maaa 236,148,12
Maaa 232,144,12
Maaa 232,48,8
Maaa 172,44,8
Maaa 172,140,12
Maaa 168,136,12
s_waitcnt vmcnt(2) lgkmcnt(0)
s_barrier
s_add_u32 s64, s64, 128
Maaa 168,40,8
Mvaa 204,36,8
Mvaa 204,132,12
s_and_b32 s64, s64, 1023
Mvaa 200,128,12
Mvaa 200,32,8
Mvaa 208,32,16
s_lshl_b32 s65, s64, 12
Mvaa 208,128,20
Mvaa 212,132,20
Ta 0,10,49152
Ta 2,10,50176
Mvaa 212,36,16
s_add_u32 m0, s62, 0
Ta 4,10,57344
Ta 6,10,58368
Maaa 176,40,16
Maaa 176,136,20
Maaa 180,140,20
D 16,44,65,0
Maaa 180,44,16
Maaa 240,48,16
Maaa 240,144,20
Maaa 244,148,20
D 17,44,65,1024
Maaa 244,52,16
Maaa 112,56,16
Maaa 112,152,20
Maaa 116,156,20
D 18,44,65,2048
Maaa 116,60,16
Maaa 124,60,24
Ra 60,26,47104
Maaa 124,156,28
Ra 156,27,47104
Maaa 120,152,28
D 19,44,65,3072
Ra 152,27,45056
Maaa 120,56,24
s_add_u32 m0, s62, 4096
Ta 8,11,49152
Ta 10,11,50176
Maaa 252,52,24
Ra 56,26,45056
)"
R"(Ta 12,11,57344
Maaa 252,148,28
Ta 14,11,58368
Ra 52,26,43008
Maaa 248,144,28
D 20,44,65,0
v_lshrrev_b32 v28, 1, v1
Maaa 248,48,24
s_lshl_b32 s80, s60, 9
Ra 148,27,43008
Ra 144,27,40960
Maaa 188,44,24
s_add_u32 s80, s80, 40960
Ra 48,26,40960
Ra 44,26,38912
Maaa 188,140,28
Ra 140,27,38912
Maaa 184,136,28
D 21,44,65,1024
Ra 136,27,36864
Maaa 184,40,24
Ra 40,26,36864
Mvaa 220,36,24
Ra 36,26,34816
Mvaa 220,132,28
Ra 132,27,34816
Mvaa 216,128,28
buffer_load_dwordx2 v[28:29], v28, s[8:11], s80 offen sc1
Ra 128,27,32768
Mvaa 216,32,24
W 13
Ra 32,26,32768
Maaa 68,60,0
Maaa 68,156,4
Maaa 64,152,4
D 22,44,65,2048
Ta 16,12,49152
Maaa 64,56,0
W 9
Ta 18,12,50176
Ta 20,12,57344
Maaa 196,52,0
Ta 22,12,58368
Maaa 196,148,4
Maaa 192,144,4
D 23,44,65,3072
Maaa 192,48,0
W 8
s_add_u32 m0, s61, 0
Mvaa 228,44,0
Mvaa 228,140,4
Mvaa 224,136,4
D 2,40,64,0
Mvaa 224,40,0
W 4
Mvaa 164,36,0
Mvaa 164,132,4
Mvaa 160,128,4
D 3,40,64,1024
Mvaa 160,32,0
Mvaa 168,32,8
Mvaa 168,128,12
Mvaa 172,132,12
D 4,40,64,2048
Ta 24,13,49152
Mvaa 172,36,8
Ta 26,13,50176
Ta 28,13,57344
Mvaa 232,40,8
Ta 30,13,58368
Mvaa 232,136,12
Mvaa 236,140,12
D 5,40,64,3072
Mvaa 236,44,8
s_add_u32 m0, s61, 4096
Maaa 200,48,8
Maaa 200,144,12
Maaa 204,148,12
D 6,40,64,0
Maaa 204,52,8
Maaa 72,56,8
Maaa 72,152,12
Maaa 76,156,12
D 7,40,64,1024
Maaa 76,60,8
W 4
Maaa 84,60,16
Maaa 84,156,20
Maaa 80,152,20
D 8,40,64,2048
Ta 0,14,49152
Maaa 80,56,16
Ta 2,14,50176
Ta 4,14,57344
Maaa 212,52,16
Ta 6,14,58368
Maaa 212,148,20
Maaa 208,144,20
D 9,40,64,3072
Maaa 208,48,16
Mvaa 244,44,16
Mvaa 244,140,20
Mvaa 240,136,20
s_waitcnt vmcnt(10)
v_cmp_eq_u32 vcc, s13, v29
s_cmp_eq_u64 vcc, -1
s_cbranch_scc0 .Lspin_20%=
.Lok_21%=:
global_load_dword v142, v146, s[6:7]
Mvaa 240,40,16
Mvaa 180,36,16
v_bfe_u32 v144, v1, 8, 1
Mvaa 180,132,20
v_mul_u32_u24 v144, 10240, v144
Mvaa 176,128,20
v_and_b32 v145, 0xf0, v1
Mvaa 176,32,16
W 4
v_lshl_add_u32 v144, v145, 3, v144
Mvaa 184,32,24
s_mul_i32 s91, s58, 20480
Mvaa 184,128,28
s_add_u32 s91, s91, s28
Mvaa 188,132,28
buffer_load_dword v142, v144, s[8:11], s91 offen offset:0 sc1
Ta 8,15,49152
Mvaa 188,36,24
Ta 10,15,50176
Ta 12,15,57344
Mvaa 248,40,24
Ta 14,15,58368
Mvaa 248,136,28
Mvaa 252,140,28
Mvaa 252,44,24
Maaa 216,48,24
Maaa 216,144,28
Maaa 220,148,28
Maaa 220,52,24
Maaa 88,56,24
Maaa 88,152,28
Maaa 92,156,28
Maaa 92,60,24
W 4
Maaa 100,60,0
Maaa 100,156,4
Maaa 96,152,4
Ta 16,24,49152
Ta 18,24,50176
Maaa 96,56,0
Ta 20,24,57344
Ta 22,24,58368
Maaa 228,52,0
Maaa 228,148,4
Maaa 224,144,4
Maaa 224,48,0
Maaa 164,44,0
Maaa 164,140,4
Maaa 160,136,4
Maaa 160,40,0
Mvaa 196,36,0
Mvaa 196,132,4
Mvaa 192,128,4
Mvaa 192,32,0
W 4
Mvaa 200,32,8
Ta 24,25,49152
Ta 26,25,50176
Mvaa 200,128,12
Mvaa 204,132,12
Ta 28,25,57344
Ta 30,25,58368
Mvaa 204,36,8
Maaa 168,40,8
Maaa 168,136,12
Maaa 172,140,12
Maaa 172,44,8
Maaa 232,48,8
Maaa 232,144,12
Maaa 236,148,12
s_waitcnt vmcnt(2) lgkmcnt(0)
s_barrier
s_cmp_eq_u32 s85, 107
s_cbranch_scc1 .Lrt_23%=
.Lrtr_24%=:
Maaa 236,52,8
s_add_u32 s64, s64, 128
Maaa 104,56,8
Maaa 104,152,12
s_and_b32 s64, s64, 1023
Maaa 108,156,12
Maaa 108,60,8
Maaa 116,60,16
s_lshl_b32 s65, s64, 12
Maaa 116,156,20
Maaa 112,152,20
Ta 0,10,16384
Ta 2,10,17408
Maaa 112,56,16
s_add_u32 m0, s62, 32768
Ta 4,10,24576
Ta 6,10,25600
Maaa 244,52,16
Maaa 244,148,20
Maaa 240,144,20
D 16,44,65,0
Maaa 240,48,16
Maaa 180,44,16
Maaa 180,140,20
Maaa 176,136,20
D 17,44,65,1024
Maaa 176,40,16
Mvaa 212,36,16
Mvaa 212,132,20
Mvaa 208,128,20
D 18,44,65,2048
Mvaa 208,32,16
Mvaa 216,32,24
Ra 32,26,0
Mvaa 216,128,28
Ra 128,27,0
Mvaa 220,132,28
D 19,44,65,3072
Ra 132,27,2048
Mvaa 220,36,24
s_add_u32 m0, s62, 36864
Ta 8,11,16384
Ta 10,11,17408
Maaa 184,40,24
Ra 36,26,2048
Ta 12,11,24576
Maaa 184,136,28
Ta 14,11,25600
Ra 40,26,4096
Maaa 188,140,28
D 20,44,65,0
Ra 136,27,4096
Maaa 188,44,24
Ra 140,27,6144
Ra 44,26,6144
Maaa 248,48,24
Ra 48,26,8192
Maaa 248,144,28
Ra 144,27,8192
Maaa 252,148,28
D 21,44,65,1024
Ra 148,27,10240
Maaa 252,52,24
Ra 52,26,10240
Maaa 120,56,24
Ra 56,26,12288
Maaa 120,152,28
Ra 152,27,12288
Maaa 124,156,28
D 22,44,65,2048
Ra 156,27,14336
Maaa 124,60,24
W 13
Ra 60,26,14336
Mvaa 160,32,0
Mvaa 160,128,4
Mvaa 164,132,4
D 23,44,65,3072
Ta 16,12,16384
Mvaa 164,36,0
W 9
s_add_u32 m0, s61, 32768
Ta 18,12,17408
Mvaa 224,40,0
.Lh_0_3_1%=:
s_cbranch_execz .Lhr_9%=
Ta 20,12,24576
Ta 22,12,25600
Mvaa 224,136,4
Mvaa 228,140,4
D 2,40,64,0
Mvaa 228,44,0
W 8
Maaa 192,48,0
Maaa 192,144,4
Maaa 196,148,4
D 3,40,64,1024
Maaa 196,52,0
W 4
Maaa 64,56,0
Maaa 64,152,4
Maaa 68,156,4
D 4,40,64,2048
Maaa 68,60,0
Maaa 76,60,8
Maaa 76,156,12
Maaa 72,152,12
D 5,40,64,3072
Ta 24,13,16384
Maaa 72,56,8
s_add_u32 m0, s61, 36864
Ta 26,13,17408
Ta 28,13,24576
Maaa 204,52,8
Ta 30,13,25600
Maaa 204,148,12
Maaa 200,144,12
D 6,40,64,0
Maaa 200,48,8
Mvaa 236,44,8
Mvaa 236,140,12
Mvaa 232,136,12
D 7,40,64,1024
Mvaa 232,40,8
Mvaa 172,36,8
Mvaa 172,132,12
Mvaa 168,128,12
D 8,40,64,2048
Mvaa 168,32,8
W 4
Mvaa 176,32,16
Mvaa 176,128,20
Mvaa 180,132,20
D 9,40,64,3072
v_add_u32 v2, 131072, v10
Mvaa 180,36,16
Ta 0,14,16384
v_add_u32 v3, 131072, v11
Mvaa 240,40,16
Ta 2,14,17408
v_add_u32 v4, 131072, v12
Mvaa 240,136,20
Ta 4,14,24576
Ta 6,14,25600
Mvaa 244,140,20
buffer_load_dword v142, v144, s[8:11], s91 offen offset:2048 sc1
v_add_u32 v5, 131072, v13
Mvaa 244,44,16
s_add_u32 s91, s91, 4096
v_add_u32 v6, 131072, v14
Maaa 208,48,16
v_add_u32 v7, 131072, v15
Maaa 208,144,20
Maaa 212,148,20
v_add_u32 v8, 131072, v24
Maaa 212,52,16
Maaa 80,56,16
v_add_u32 v9, 131072, v25
Maaa 80,152,20
s_add_u32 s65, s5, 0
s_cmp_ge_u32 s65, 20
s_cselect_b32 s90, 20, 0
s_sub_u32 s65, s65, s90
Maaa 84,156,20
s_lshl_b32 s65, s65, 18
s_add_u32 m0, s63, 131072
Maaa 84,60,16
W 4
Maaa 92,60,24
Maaa 92,156,28
Maaa 88,152,28
Ta 8,15,16384
Ta 10,15,17408
Maaa 88,56,24
D 16,48,65,0
Ta 12,15,24576
Maaa 220,52,24
Ta 14,15,25600
Maaa 220,148,28
Maaa 216,144,28
Maaa 216,48,24
D 17,48,65,1024
Mvaa 252,44,24
Mvaa 252,140,28
Mvaa 248,136,28
Mvaa 248,40,24
Mvaa 188,36,24
D 18,48,65,2048
Mvaa 188,132,28
Mvaa 184,128,28
Mvaa 184,32,24
W 4
Mvaa 192,32,0
Mvaa 192,128,4
D 19,48,65,3072
Mvaa 196,132,4
s_add_u32 m0, s63, 135168
Ta 16,24,16384
Ta 18,24,17408
Mvaa 196,36,0
Ta 20,24,24576
Ta 22,24,25600
Maaa 160,40,0
Maaa 160,136,4
Maaa 164,140,4
D 20,48,65,0
Maaa 164,44,0
Maaa 224,48,0
Maaa 224,144,4
Maaa 228,148,4
Maaa 228,52,0
D 21,48,65,1024
Maaa 96,56,0
Maaa 96,152,4
Maaa 100,156,4
Maaa 100,60,0
)"
R"(W 4
Maaa 108,60,8
D 22,48,65,2048
Ta 24,25,16384
Maaa 108,156,12
Ta 26,25,17408
Maaa 104,152,12
Ta 28,25,24576
Ta 30,25,25600
Maaa 104,56,8
Maaa 236,52,8
D 23,48,65,3072
Maaa 236,148,12
Maaa 232,144,12
Maaa 232,48,8
Maaa 172,44,8
Maaa 172,140,12
Maaa 168,136,12
s_waitcnt vmcnt(9) lgkmcnt(0)
s_barrier
s_cmp_eq_u32 s85, 107
s_cbranch_scc1 .Lrt_25%=
.Lrtr_26%=:
Maaa 168,40,8
s_add_u32 s65, s5, 4
Mvaa 204,36,8
Mvaa 204,132,12
s_cmp_ge_u32 s65, 20
s_cselect_b32 s90, 20, 0
Mvaa 200,128,12
Mvaa 200,32,8
s_sub_u32 s65, s65, s90
Mvaa 208,32,16
Mvaa 208,128,20
s_lshl_b32 s65, s65, 18
Mvaa 212,132,20
Ta 0,10,49152
Ta 2,10,50176
Mvaa 212,36,16
Maaa 176,40,16
s_add_u32 m0, s63, 0
Maaa 176,136,20
Maaa 180,140,20
D 16,48,65,0
Maaa 180,44,16
Maaa 240,48,16
Maaa 240,144,20
Maaa 244,148,20
D 17,48,65,1024
Ta 4,11,49152
Maaa 244,52,16
Ta 6,11,50176
Maaa 112,56,16
Maaa 112,152,20
Maaa 116,156,20
D 18,48,65,2048
Maaa 116,60,16
Maaa 124,60,24
Ra 60,26,47104
Maaa 124,156,28
Maaa 120,152,28
D 19,48,65,3072
Ta 8,12,49152
Maaa 120,56,24
s_add_u32 m0, s63, 4096
Ta 10,12,50176
Ra 56,26,45056
Maaa 252,52,24
Ra 52,26,43008
Maaa 252,148,28
Maaa 248,144,28
D 20,48,65,0
Maaa 248,48,24
Ra 48,26,40960
Maaa 188,44,24
Ra 44,26,38912
Maaa 188,140,28
Maaa 184,136,28
D 21,48,65,1024
Ta 12,13,49152
Maaa 184,40,24
Ta 14,13,50176
Ra 40,26,36864
Mvaa 220,36,24
Ra 36,26,34816
Mvaa 220,132,28
Mvaa 216,128,28
D 22,48,65,2048
Mvaa 216,32,24
Ra 32,26,32768
W 6
Maaa 68,60,0
Maaa 64,56,0
Maaa 196,52,0
D 23,48,65,3072
Ta 16,14,49152
Maaa 192,48,0
W 1
s_add_u32 s65, s5, 8
s_cmp_ge_u32 s65, 20
s_cselect_b32 s90, 20, 0
Mvaa 228,44,0
s_sub_u32 s65, s65, s90
s_lshl_b32 s65, s65, 18
Ta 18,14,50176
Mvaa 224,40,0
s_add_u32 m0, s63, 65536
Mvaa 164,36,0
D 16,48,65,0
Mvaa 160,32,0
Mvaa 168,32,4
Mvaa 172,36,4
Mvaa 232,40,4
D 17,48,65,1024
Ta 20,15,49152
Mvaa 236,44,4
Ta 22,15,50176
Maaa 200,48,4
Maaa 204,52,4
Maaa 72,56,4
D 18,48,65,2048
Maaa 76,60,4
Maaa 84,60,8
Maaa 80,56,8
Maaa 212,52,8
D 19,48,65,3072
Ta 24,24,49152
Maaa 208,48,8
s_add_u32 m0, s63, 69632
Ta 26,24,50176
Mvaa 244,44,8
Mvaa 240,40,8
Mvaa 180,36,8
D 20,48,65,0
Mvaa 176,32,8
Mvaa 184,32,12
Mvaa 188,36,12
Mvaa 248,40,12
D 21,48,65,1024
Ta 28,25,49152
Mvaa 252,44,12
Ta 30,25,50176
Maaa 216,48,12
Ra 156,27,47104
Maaa 220,52,12
Maaa 88,56,12
D 22,48,65,2048
Maaa 92,60,12
W 7
Ra 152,27,45056
Maaa 100,60,16
Maaa 96,56,16
Maaa 228,52,16
D 23,48,65,3072
Ra 148,27,43008
Maaa 224,48,16
Ta 0,10,57344
Ta 2,10,58368
Maaa 164,44,16
Maaa 160,40,16
Ra 144,27,40960
Mvaa 196,36,16
buffer_load_dword v142, v144, s[8:11], s91 offen offset:0 sc1
Mvaa 192,32,16
W 10
Mvaa 200,32,20
Ra 140,27,38912
Mvaa 204,36,20
Maaa 168,40,20
Ta 4,11,57344
Ta 6,11,58368
Maaa 172,44,20
Ra 136,27,36864
Maaa 232,48,20
Maaa 236,52,20
v_bfe_u32 v29, v1, 7, 1
Maaa 104,56,20
Ra 132,27,34816
Maaa 108,60,20
W 13
v_mul_u32_u24 v28, 10240, v29
Maaa 116,60,24
Maaa 112,56,24
Ra 128,27,32768
v_lshrrev_b32 v29, 8, v1
Maaa 244,52,24
Ta 8,12,57344
Ta 10,12,58368
Maaa 240,48,24
v_lshl_add_u32 v28, v29, 7, v28
Maaa 180,44,24
v_and_b32 v29, 0x70, v1
Maaa 176,40,24
Mvaa 212,36,24
v_add_u32 v28, v28, v29
Mvaa 208,32,24
W 14
Mvaa 216,32,28
s_and_b32 s80, s14, 2
Mvaa 220,36,28
s_mul_i32 s80, s80, 20480
Maaa 184,40,28
Ta 12,13,57344
Ta 14,13,58368
Maaa 188,44,28
s_sub_u32 s81, 40960, s80
Maaa 248,48,28
Maaa 252,52,28
s_add_u32 s93, s28, 20480
Maaa 120,56,28
Maaa 124,60,28
W 10
v_add_u32 v26, s80, v28
Maaa 68,156,0
Maaa 64,152,0
Maaa 196,148,0
Ta 16,14,57344
v_add_u32 v27, s81, v28
Maaa 192,144,0
W 5
Ta 18,14,58368
Mvaa 228,140,0
buffer_load_dwordx4 a[32:35], v26, s[8:11], s28 offen offset:0 sc1
Mvaa 224,136,0
Mvaa 164,132,0
Mvaa 160,128,0
Mvaa 168,128,4
buffer_load_dwordx4 a[36:39], v26, s[8:11], s93 offen offset:0 sc1
Mvaa 172,132,4
Mvaa 232,136,4
Ta 20,15,57344
Ta 22,15,58368
Mvaa 236,140,4
Maaa 200,144,4
buffer_load_dwordx4 a[40:43], v27, s[8:11], s28 offen offset:0 sc1
Maaa 204,148,4
Maaa 72,152,4
Maaa 76,156,4
W 6
Maaa 84,156,8
buffer_load_dwordx4 a[44:47], v27, s[8:11], s93 offen offset:0 sc1
Ta 24,24,57344
Maaa 80,152,8
s_add_u32 s28, s28, 512
s_cmp_eq_u32 s28, s4
s_cselect_b32 s93, 10240, 0
Ta 26,24,58368
Maaa 212,148,8
s_sub_u32 s28, s28, s93
Ta 28,25,57344
Ta 30,25,58368
Maaa 208,144,8
s_add_u32 s93, s28, 20480
Mvaa 244,140,8
Mvaa 240,136,8
Mvaa 180,132,8
s_add_u32 s65, s5, 12
s_cmp_ge_u32 s65, 20
s_cselect_b32 s90, 20, 0
buffer_load_dwordx4 a[48:51], v26, s[8:11], s28 offen offset:0 sc1
Mvaa 176,128,8
W 8
s_sub_u32 s65, s65, s90
s_lshl_b32 s65, s65, 18
Mvaa 184,128,12
s_add_u32 m0, s63, 32768
Mvaa 188,132,12
Mvaa 248,136,12
s_waitcnt vmcnt(6) lgkmcnt(0)
s_barrier
buffer_load_dwordx4 a[52:55], v26, s[8:11], s93 offen offset:0 sc1
Mvaa 252,140,12
Maaa 216,144,12
Maaa 220,148,12
Maaa 88,152,12
buffer_load_dwordx4 a[56:59], v27, s[8:11], s28 offen offset:0 sc1
Maaa 92,156,12
Maaa 100,156,16
Maaa 96,152,16
Maaa 228,148,16
buffer_load_dwordx4 a[60:63], v27, s[8:11], s93 offen offset:0 sc1
Maaa 224,144,16
Maaa 164,140,16
Maaa 160,136,16
Mvaa 196,132,16
D 16,48,65,0
Mvaa 192,128,16
Mvaa 200,128,20
Mvaa 204,132,20
Maaa 168,136,20
D 17,48,65,1024
Ta 0,2,0
Maaa 172,140,20
Ta 2,2,1024
Maaa 232,144,20
Maaa 236,148,20
Maaa 104,152,20
D 18,48,65,2048
Ta 4,2,8192
Maaa 108,156,20
Ta 6,2,9216
Maaa 116,156,24
Maaa 112,152,24
Maaa 244,148,24
Ta 8,3,0
Ta 10,3,1024
Maaa 240,144,24
D 19,48,65,3072
Maaa 180,140,24
s_add_u32 m0, s63, 36864
Maaa 176,136,24
Mvaa 212,132,24
Ta 12,3,8192
Ta 14,3,9216
Mvaa 208,128,24
D 20,48,65,0
Mvaa 216,128,28
Mvaa 220,132,28
Maaa 184,136,28
Ta 16,4,0
Ta 18,4,1024
Maaa 188,140,28
Maaa 248,144,28
D 21,48,65,1024
Maaa 252,148,28
Maaa 120,152,28
Maaa 124,156,28
.Lq_11%=:
s_cmp_ge_u32 s14, 2
s_cbranch_scc1 .Lpb_31%=
s_waitcnt vmcnt(6) lgkmcnt(6)
Zvaa 32,0,32
s_add_u32 s28, s28, 512
Ta 20,4,8192
Ta 22,4,9216
Mvaa 32,4,48
s_cmp_eq_u32 s28, s4
s_cselect_b32 s93, 10240, 0
Ta 24,5,0
Ta 26,5,1024
Zvaa 64,4,52
s_sub_u32 s28, s28, s93
s_add_u32 s93, s28, 20480
buffer_load_dwordx4 a[128:131], v26, s[8:11], s28 offen offset:0 sc1
Mvaa 64,0,36
Ta 28,5,8192
Ta 30,5,9216
Zvaa 96,0,40
K 184,184,185
K 185,186,187
Mvaa 96,4,56
K 186,188,189
K 187,190,191
Zvaa 128,4,60
K 192,192,193
Mvaa 128,0,44
W 8
buffer_load_dwordx4 a[132:135], v26, s[8:11], s93 offen offset:0 sc1
K 193,194,195
Zvaa 132,8,44
Ta 0,6,0
Ta 2,6,1024
Mvaa 132,12,60
K 194,196,197
K 195,198,199
Zvaa 100,12,56
K 200,200,201
K 201,202,203
Mvaa 100,8,40
Ta 4,6,8192
Ta 6,6,9216
Zvaa 68,8,36
buffer_load_dwordx4 a[136:139], v27, s[8:11], s28 offen offset:0 sc1
K 202,204,205
Mvaa 68,12,52
K 203,206,207
K 208,208,209
Zvaa 36,12,48
K 209,210,211
K 210,212,213
Mvaa 36,8,32
W 8
Ta 8,7,0
K 211,214,215
Zvaa 40,16,32
Ta 10,7,1024
K 216,216,217
Mvaa 40,20,48
buffer_load_dwordx4 a[140:143], v27, s[8:11], s93 offen offset:0 sc1
K 217,218,219
Zvaa 72,20,52
s_add_u32 s28, s28, 512
s_cmp_eq_u32 s28, s4
s_cselect_b32 s93, 10240, 0
s_sub_u32 s28, s28, s93
Mvaa 72,16,36
s_add_u32 s93, s28, 20480
K 218,220,221
K 219,222,223
Zvaa 104,16,40
)"
R"(Ta 12,7,8192
Ta 14,7,9216
Mvaa 104,20,56
K 224,224,225
K 225,226,227
Zvaa 136,20,60
K 226,228,229
K 227,230,231
Mvaa 136,16,44
W 8
buffer_load_dwordx4 a[144:147], v26, s[8:11], s28 offen offset:0 sc1
K 232,232,233
Zvaa 140,24,44
K 233,234,235
K 234,236,237
Mvaa 140,28,60
Ta 16,8,0
K 235,238,239
Zvaa 108,28,56
Ta 18,8,1024
K 240,240,241
Mvaa 108,24,40
K 241,242,243
K 242,244,245
Zvaa 76,24,36
buffer_load_dwordx4 a[148:151], v26, s[8:11], s93 offen offset:0 sc1
K 243,246,247
Mvaa 76,28,52
Ta 20,8,8192
Ta 22,8,9216
Zvaa 44,28,48
K 248,248,249
K 249,250,251
Mvaa 44,24,32
K 250,252,253
K 251,254,255
W 8
Zvaa 48,0,32
Ta 24,9,0
Ta 26,9,1024
Mvaa 48,4,48
buffer_load_dwordx4 a[152:155], v27, s[8:11], s28 offen offset:0 sc1
K 160,160,161
Zvaa 80,4,52
Ta 28,9,8192
Ta 30,9,9216
Mvaa 80,0,36
K 161,162,163
Zvaa 112,0,40
K 162,164,165
Mvaa 112,4,56
Zvaa 144,4,60
buffer_load_dwordx4 a[156:159], v27, s[8:11], s93 offen offset:0 sc1
K 163,166,167
Mvaa 144,0,44
W 8
s_add_u32 s28, s28, 512
s_cmp_eq_u32 s28, s4
s_cselect_b32 s93, 10240, 0
Zvaa 148,8,44
s_sub_u32 s28, s28, s93
s_add_u32 s93, s28, 20480
Ta 0,2,16384
Mvaa 148,12,60
s_add_u32 s36, s29, 0
Ta 2,2,17408
Zvaa 116,12,56
Ta 4,2,24576
Ta 6,2,25600
Mvaa 116,8,40
buffer_load_dwordx4 v[188:191], v26, s[8:11], s28 offen offset:0 sc1
Zvaa 84,8,36
Mvaa 84,12,52
Zvaa 52,12,48
Mvaa 52,8,32
W 8
Ta 8,3,16384
Ta 10,3,17408
Zvaa 56,16,32
buffer_load_dwordx4 v[196:199], v26, s[8:11], s93 offen offset:0 sc1
Mvaa 56,20,48
Zvaa 88,20,52
Ta 12,3,24576
Ta 14,3,25600
Mvaa 88,16,36
Zvaa 120,16,40
buffer_store_dwordx4 v[160:163], v1, s[8:11], s36 offen offset:0 sc1
K 168,168,169
Mvaa 120,20,56
K 169,170,171
K 170,172,173
Zvaa 152,20,60
Mvaa 152,16,44
W 8
Ta 16,4,16384
K 171,174,175
Zvaa 156,24,44
Ta 18,4,17408
Mvaa 156,28,60
buffer_load_dwordx4 v[204:207], v27, s[8:11], s28 offen offset:0 sc1
Zvaa 124,28,56
Ta 20,4,24576
Ta 22,4,25600
Mvaa 124,24,40
Zvaa 92,24,36
Mvaa 92,28,52
Zvaa 60,28,48
buffer_load_dwordx4 v[212:215], v27, s[8:11], s93 offen offset:0 sc1
Mvaa 60,24,32
s_waitcnt vmcnt(5) lgkmcnt(8)
s_add_u32 s28, s28, 512
s_cmp_eq_u32 s28, s4
s_cselect_b32 s93, 10240, 0
Mvaa 32,0,128
s_sub_u32 s28, s28, s93
s_add_u32 s93, s28, 20480
Ta 24,5,16384
Mvaa 32,4,144
Ta 26,5,17408
Mvaa 64,4,148
Ta 28,5,24576
Ta 30,5,25600
Mvaa 64,0,132
buffer_store_dwordx4 v[168:171], v1, s[8:11], s36 offen offset:1024 sc1
K 176,176,177
Mvaa 96,0,136
K 177,178,179
K 178,180,181
Mvaa 96,4,152
Mvaa 128,4,156
Mvaa 128,0,140
W 8
Ta 0,6,16384
K 179,182,183
Mvaa 132,8,140
buffer_load_dwordx4 v[220:223], v26, s[8:11], s28 offen offset:0 sc1
Ta 2,6,17408
Mvaa 132,12,156
Mvaa 100,12,152
Ta 4,6,24576
Ta 6,6,25600
Mvaa 100,8,136
Mvaa 68,8,132
Mvaa 68,12,148
buffer_load_dwordx4 v[228:231], v26, s[8:11], s93 offen offset:0 sc1
Mvaa 36,12,144
Mvaa 36,8,128
W 8
Ta 8,7,16384
Ta 10,7,17408
Mvaa 40,16,128
Mvaa 40,20,144
Mvaa 72,20,148
buffer_load_dwordx4 v[236:239], v27, s[8:11], s28 offen offset:0 sc1
Ta 12,7,24576
Mvaa 72,16,132
buffer_store_dwordx4 v[176:179], v1, s[8:11], s36 offen offset:2048 sc1
Ta 14,7,25600
Mvaa 104,16,136
Mvaa 104,20,152
Mvaa 136,20,156
Mvaa 136,16,140
W 8
Ta 16,8,16384
Ta 18,8,17408
Mvaa 140,24,140
buffer_load_dwordx4 v[244:247], v27, s[8:11], s93 offen offset:0 sc1
Mvaa 140,28,156
s_add_u32 s28, s28, 512
s_cmp_eq_u32 s28, s4
s_cselect_b32 s93, 10240, 0
s_sub_u32 s28, s28, s93
Mvaa 108,28,152
s_add_u32 s93, s28, 20480
Ta 20,8,24576
Ta 22,8,25600
Mvaa 108,24,136
Mvaa 76,24,132
Mvaa 76,28,148
buffer_load_dwordx4 v[252:255], v26, s[8:11], s28 offen offset:0 sc1
Mvaa 44,28,144
Mvaa 44,24,128
W 8
Ta 24,9,16384
Ta 26,9,17408
Mvaa 48,0,128
Mvaa 48,4,144
Mvaa 80,4,148
D 22,48,65,2048
Ta 28,9,24576
Mvaa 80,0,132
buffer_store_dwordx4 v[184:187], v1, s[8:11], s36 offen offset:3072 sc1
Ta 30,9,25600
Mvaa 112,0,136
s_add_u32 s36, s29, 4096
s_lshl_b32 s80, s56, 9
Mvaa 112,4,152
Mvaa 144,4,156
s_lshl_b32 s81, s57, 4
Mvaa 144,0,140
W 8
s_add_u32 s80, s80, s81
Ta 0,10,0
Mvaa 148,8,140
buffer_load_dwordx4 a[32:35], v26, s[8:11], s93 offen offset:0 sc1
Ta 2,10,1024
Mvaa 148,12,156
s_and_b32 s81, s14, 1
Mvaa 116,12,152
s_lshl_b32 s81, s81, 3
Ta 4,10,8192
Ta 6,10,9216
Mvaa 116,8,136
Mvaa 84,8,132
s_add_u32 s80, s80, s81
Mvaa 84,12,148
s_add_u32 s71, s80, 4096
buffer_load_dwordx4 a[36:39], v27, s[8:11], s28 offen offset:0 sc1
Mvaa 52,12,144
Mvaa 52,8,128
W 8
Ta 8,11,0
Ta 10,11,1024
Mvaa 56,16,128
Mvaa 56,20,144
Mvaa 88,20,148
buffer_store_dwordx4 v[192:195], v1, s[8:11], s36 offen offset:0 sc1
Ta 12,11,8192
Mvaa 88,16,132
Ta 14,11,9216
Mvaa 120,16,136
Mvaa 120,20,152
Mvaa 152,20,156
Mvaa 152,16,140
W 8
buffer_load_dwordx4 a[40:43], v27, s[8:11], s93 offen offset:0 sc1
Ta 16,12,0
Mvaa 156,24,140
s_add_u32 s28, s28, 512
s_cmp_eq_u32 s28, s4
s_cselect_b32 s93, 10240, 0
s_sub_u32 s28, s28, s93
Mvaa 156,28,156
s_add_u32 s93, s28, 20480
Ta 18,12,1024
Mvaa 124,28,152
Ta 20,12,8192
Ta 22,12,9216
Mvaa 124,24,136
Mvaa 92,24,132
buffer_load_dwordx4 a[44:47], v26, s[8:11], s28 offen offset:0 sc1
Mvaa 92,28,148
Mvaa 60,28,144
Mvaa 60,24,128
s_waitcnt vmcnt(8) lgkmcnt(8)
Ta 24,13,0
Ta 26,13,1024
Mvav 32,0,188
Mvav 32,4,220
D 23,48,65,3072
Mvav 64,4,228
s_add_u32 s65, s5, 16
s_cmp_ge_u32 s65, 20
s_cselect_b32 s90, 20, 0
buffer_store_dwordx4 v[200:203], v1, s[8:11], s36 offen offset:1024 sc1
Mvav 64,0,196
s_sub_u32 s65, s65, s90
s_lshl_b32 s65, s65, 18
s_add_u32 m0, s63, 98304
Mvav 96,0,204
Ta 28,13,8192
Ta 30,13,9216
Mvav 96,4,236
Mvav 128,4,244
Mvav 128,0,212
W 8
buffer_load_dwordx4 a[48:51], v26, s[8:11], s93 offen offset:0 sc1
Ta 0,14,0
Mvav 132,8,212
Ta 2,14,1024
v_add_u32 v2, 65536, v10
Mvav 132,12,244
Mvav 100,12,236
Ta 4,14,8192
Ta 6,14,9216
Mvav 100,8,204
v_add_u32 v3, 65536, v11
Mvav 68,8,196
buffer_load_dwordx4 a[52:55], v27, s[8:11], s28 offen offset:0 sc1
Mvav 68,12,228
Mvav 36,12,220
Mvav 36,8,188
W 8
Ta 8,15,0
Ta 10,15,1024
Mvav 40,16,188
v_add_u32 v4, 65536, v12
Mvav 40,20,220
buffer_load_dwordx4 a[56:59], v27, s[8:11], s93 offen offset:0 sc1
Mvav 72,20,228
s_add_u32 s28, s28, 512
s_cmp_eq_u32 s28, s4
s_cselect_b32 s93, 10240, 0
buffer_store_dwordx4 v[208:211], v1, s[8:11], s36 offen offset:2048 sc1
Mvav 72,16,196
s_sub_u32 s28, s28, s93
s_add_u32 s93, s28, 20480
Ta 12,15,8192
Mvav 104,16,204
Ta 14,15,9216
v_add_u32 v5, 65536, v13
Mvav 104,20,236
Mvav 136,20,244
Mvav 136,16,212
W 8
D 16,48,65,0
Ta 16,24,0
Mvav 140,24,212
Ta 18,24,1024
v_add_u32 v6, 65536, v14
Mvav 140,28,244
Mvav 108,28,236
Ta 20,24,8192
Ta 22,24,9216
Mvav 108,24,204
v_add_u32 v7, 65536, v15
Mvav 76,24,196
buffer_load_dwordx4 a[60:63], v26, s[8:11], s28 offen offset:0 sc1
Mvav 76,28,228
Mvav 44,28,220
Mvav 44,24,188
W 8
Ta 24,25,0
Ta 26,25,1024
Mvav 48,0,188
v_add_u32 v8, 65536, v24
Mvav 48,4,220
buffer_load_dwordx4 v[184:187], v26, s[8:11], s93 offen offset:0 sc1
Mvav 80,4,228
buffer_store_dwordx4 v[216:219], v1, s[8:11], s36 offen offset:3072 sc1
Ta 28,25,8192
Mvav 80,0,196
s_add_u32 s36, s29, 458752
Ta 30,25,9216
v_add_u32 v9, 65536, v25
Mvav 112,0,204
Mvav 112,4,236
Mvav 144,4,244
Mvav 144,0,212
W 8
buffer_load_dwordx4 v[192:195], v27, s[8:11], s28 offen offset:0 sc1
Ta 0,10,16384
Mvav 148,8,212
Ta 2,10,17408
Mvav 148,12,244
Mvav 116,12,236
Ta 4,10,24576
Ta 6,10,25600
Mvav 116,8,204
Mvav 84,8,196
D 17,48,65,1024
Mvav 84,12,228
Mvav 52,12,220
Mvav 52,8,188
W 8
Ta 8,11,16384
Ta 10,11,17408
Mvav 56,16,188
Mvav 56,20,220
buffer_store_dwordx4 v[224:227], v1, s[8:11], s36 offen offset:0 sc1
Mvav 88,20,228
Ta 12,11,24576
Ta 14,11,25600
Mvav 88,16,196
Mvav 120,16,204
Mvav 120,20,236
Mvav 152,20,244
buffer_load_dwordx4 a[128:131], v27, s[8:11], s93 offen offset:0 sc1
Mvav 152,16,212
W 8
s_add_u32 s28, s28, 512
s_cmp_eq_u32 s28, s4
s_cselect_b32 s93, 10240, 0
Mvav 156,24,212
s_sub_u32 s28, s28, s93
s_add_u32 s93, s28, 20480
Ta 16,12,16384
Mvav 156,28,244
Ta 18,12,17408
Mvav 124,28,236
Ta 20,12,24576
Ta 22,12,25600
Mvav 124,24,204
buffer_load_dwordx4 a[132:135], v26, s[8:11], s28 offen offset:0 sc1
Mvav 92,24,196
Mvav 92,28,228
Mvav 60,28,220
Mvav 60,24,188
s_waitcnt vmcnt(10) lgkmcnt(8)
Ta 24,13,16384
)"
R"(Ta 26,13,17408
Mvav 32,0,252
buffer_load_dwordx4 a[136:139], v26, s[8:11], s93 offen offset:0 sc1
Mvaa 32,4,44
buffer_store_dwordx4 v[232:235], v1, s[8:11], s36 offen offset:1024 sc1
Mvaa 64,4,48
Ta 28,13,24576
Ta 30,13,25600
Mvaa 64,0,32
Mvaa 96,0,36
Mvaa 96,4,52
Mvaa 128,4,56
buffer_load_dwordx4 a[140:143], v27, s[8:11], s28 offen offset:0 sc1
Mvaa 128,0,40
W 8
Ta 0,14,16384
Ta 2,14,17408
Mvaa 132,8,40
Mvaa 132,12,56
Mvaa 100,12,52
Ta 4,14,24576
Ta 6,14,25600
Mvaa 100,8,36
D 18,48,65,2048
Mvaa 68,8,32
Mvaa 68,12,48
Mvaa 36,12,44
Mvav 36,8,252
W 8
Ta 8,15,16384
Ta 10,15,17408
Mvav 40,16,252
buffer_load_dwordx4 a[144:147], v27, s[8:11], s93 offen offset:0 sc1
Mvaa 40,20,44
s_add_u32 s28, s28, 512
s_cmp_eq_u32 s28, s4
s_cselect_b32 s93, 10240, 0
buffer_store_dwordx4 v[240:243], v1, s[8:11], s36 offen offset:2048 sc1
Mvaa 72,20,48
s_sub_u32 s28, s28, s93
s_add_u32 s93, s28, 20480
Ta 12,15,24576
Mvaa 72,16,32
Ta 14,15,25600
Mvaa 104,16,36
Mvaa 104,20,52
Mvaa 136,20,56
buffer_load_dwordx4 a[148:151], v26, s[8:11], s28 offen offset:0 sc1
Mvaa 136,16,40
W 8
Ta 16,24,16384
Ta 18,24,17408
Mvaa 140,24,40
Mvaa 140,28,56
Mvaa 108,28,52
Ta 20,24,24576
Ta 22,24,25600
Mvaa 108,24,36
buffer_load_dwordx4 a[152:155], v26, s[8:11], s93 offen offset:0 sc1
Mvaa 76,24,32
Mvaa 76,28,48
Mvaa 44,28,44
Mvav 44,24,252
W 8
Ta 24,25,16384
Ta 26,25,17408
Mvav 48,0,252
buffer_load_dwordx4 a[156:159], v27, s[8:11], s28 offen offset:0 sc1
Mvaa 48,4,44
buffer_store_dwordx4 v[248:251], v1, s[8:11], s36 offen offset:3072 sc1
G 160,160
Mvaa 80,4,48
G 161,161
G 162,162
Mvaa 80,0,32
G 163,163
G 164,164
Mvaa 112,0,36
G 165,165
G 166,166
Mvaa 112,4,52
Ta 28,25,24576
Ta 30,25,25600
Mvaa 144,4,56
D 19,48,65,3072
G 167,167
Mvaa 144,0,40
W 8
s_add_u32 m0, s63, 102400
K 160,160,161
Mvaa 148,8,40
K 161,162,163
K 162,164,165
Mvaa 148,12,56
Ta 0,2,0
K 163,166,167
Mvaa 116,12,52
s_add_u32 s36, s29, 462848
Ta 2,2,1024
Ta 4,2,8192
Mvaa 116,8,36
buffer_load_dwordx4 v[200:203], v27, s[8:11], s93 offen offset:0 sc1
Ta 6,2,9216
Mvaa 84,8,32
s_add_u32 s28, s28, 512
s_cmp_eq_u32 s28, s4
s_cselect_b32 s93, 10240, 0
s_sub_u32 s28, s28, s93
Mvaa 84,12,48
s_add_u32 s93, s28, 20480
Mvaa 52,12,44
Mvav 52,8,252
W 8
Ta 8,3,0
Ta 10,3,1024
Mvav 56,16,252
buffer_store_dwordx4 v[160:163], v1, s[8:11], s36 offen offset:0 sc1
G 168,168
Mvaa 56,20,44
G 169,169
G 170,170
Mvaa 88,20,48
G 171,171
G 172,172
Mvaa 88,16,32
G 173,173
G 174,174
Mvaa 120,16,36
Ta 12,3,8192
Ta 14,3,9216
Mvaa 120,20,52
buffer_load_dwordx4 v[208:211], v26, s[8:11], s28 offen offset:0 sc1
G 175,175
Mvaa 152,20,56
K 168,168,169
K 169,170,171
Mvaa 152,16,40
W 8
K 170,172,173
K 171,174,175
Mvaa 156,24,40
Ta 16,4,0
Ta 18,4,1024
Mvaa 156,28,56
Mvaa 124,28,52
buffer_load_dwordx4 v[216:219], v26, s[8:11], s93 offen offset:0 sc1
Ta 20,4,8192
Mvaa 124,24,36
Ta 22,4,9216
Mvaa 92,24,32
Mvaa 92,28,48
Mvaa 60,28,44
Mvav 60,24,252
s_waitcnt vmcnt(10) lgkmcnt(8)
D 20,48,65,0
Ta 24,5,0
Mvaa 32,0,60
buffer_store_dwordx4 v[168:171], v1, s[8:11], s36 offen offset:1024 sc1
G 176,176
Mvaa 32,4,132
G 177,177
G 178,178
Mvaa 64,4,136
Ta 26,5,1024
G 179,179
Mvav 64,0,184
G 180,180
G 181,181
Mvav 96,0,192
Ta 28,5,8192
G 182,182
Mvaa 96,4,140
buffer_load_dwordx4 v[224:227], v27, s[8:11], s28 offen offset:0 sc1
Ta 30,5,9216
Mvaa 128,4,144
G 183,183
K 176,176,177
Mvaa 128,0,128
W 8
K 177,178,179
K 178,180,181
Mvaa 132,8,128
Ta 0,6,0
K 179,182,183
Mvaa 132,12,144
Ta 2,6,1024
Mvaa 100,12,140
buffer_load_dwordx4 v[188:191], v27, s[8:11], s93 offen offset:0 sc1
Ta 4,6,8192
Mvav 100,8,192
s_add_u32 s28, s28, 512
s_cmp_eq_u32 s28, s4
s_cselect_b32 s93, 10240, 0
s_sub_u32 s28, s28, s93
Mvav 68,8,184
s_add_u32 s93, s28, 20480
Ta 6,6,9216
Mvaa 68,12,136
Mvaa 36,12,132
Mvaa 36,8,60
W 8
buffer_load_dwordx4 v[196:199], v26, s[8:11], s28 offen offset:0 sc1
Ta 8,7,0
Mvaa 40,16,60
buffer_store_dwordx4 v[176:179], v1, s[8:11], s36 offen offset:2048 sc1
G 160,184
Mvaa 40,20,132
G 161,185
G 162,186
Mvaa 72,20,136
Ta 10,7,1024
G 163,187
Mvav 72,16,184
G 164,188
G 165,189
Mvav 104,16,192
Ta 12,7,8192
G 166,190
Mvaa 104,20,140
buffer_load_dwordx4 v[204:207], v26, s[8:11], s93 offen offset:0 sc1
Ta 14,7,9216
Mvaa 136,20,144
G 167,191
K 160,160,161
Mvaa 136,16,128
W 8
K 161,162,163
K 162,164,165
Mvaa 140,24,128
Ta 16,8,0
K 163,166,167
Mvaa 140,28,144
Ta 18,8,1024
Mvaa 108,28,140
D 21,48,65,1024
Ta 20,8,8192
Mvav 108,24,192
Ta 22,8,9216
Mvav 76,24,184
Mvaa 76,28,136
Mvaa 44,28,132
Mvaa 44,24,60
W 8
buffer_load_dwordx4 v[212:215], v27, s[8:11], s28 offen offset:0 sc1
Ta 24,9,0
Mvaa 48,0,60
buffer_store_dwordx4 v[160:163], v1, s[8:11], s36 offen offset:3072 sc1
G 168,192
Mvaa 48,4,132
G 169,193
G 170,194
Mvaa 80,4,136
G 171,195
G 172,196
Mvav 80,0,184
Ta 26,9,1024
G 173,197
Mvav 112,0,192
G 174,198
G 175,199
Mvaa 112,4,140
buffer_load_dwordx4 v[220:223], v27, s[8:11], s93 offen offset:0 sc1
Ta 28,9,8192
Mvaa 144,4,144
s_add_u32 s28, s28, 512
s_cmp_eq_u32 s28, s4
s_cselect_b32 s93, 10240, 0
s_sub_u32 s28, s28, s93
Mvaa 144,0,128
W 7
s_add_u32 s93, s28, 20480
Ta 30,9,9216
Mvaa 148,8,128
K 168,168,169
K 169,170,171
Mvaa 148,12,144
K 170,172,173
K 171,174,175
Mvaa 116,12,140
buffer_load_dwordx4 v[228:231], v26, s[8:11], s28 offen offset:0 sc1
Ta 0,2,16384
Mvav 116,8,192
Ta 2,2,17408
v_accvgpr_write_b32 a192, v168
Mvav 84,8,184
v_accvgpr_write_b32 a193, v169
v_accvgpr_write_b32 a194, v170
Mvaa 84,12,136
Ta 4,2,24576
Ta 6,2,25600
Mvaa 52,12,132
v_accvgpr_write_b32 a195, v171
G 176,64
Mvaa 52,8,60
W 8
D 22,48,65,2048
G 177,65
Mvaa 56,16,60
D 23,48,65,3072
G 178,66
Mvaa 56,20,132
Ta 8,3,16384
Ta 10,3,17408
Mvaa 88,20,136
G 179,67
G 180,68
Mvav 88,16,184
Ta 12,3,24576
Ta 14,3,25600
Mvav 120,16,192
G 181,69
G 182,70
Mvaa 120,20,140
buffer_load_dwordx4 v[236:239], v26, s[8:11], s93 offen offset:0 sc1
G 183,71
Mvaa 152,20,144
v_lshrrev_b32 v20, 4, v1
K 176,176,177
Mvaa 152,16,128
W 8
Ta 16,4,16384
v_add_u32 v20, 0, v20
Mvaa 156,24,128
Ta 18,4,17408
K 177,178,179
Mvaa 156,28,144
v_mul_u32_u24 v22, 205, v20
K 178,180,181
Mvaa 124,28,140
buffer_load_dwordx4 v[232:235], v27, s[8:11], s28 offen offset:0 sc1
K 179,182,183
Mvav 124,24,192
Ta 20,4,24576
Ta 22,4,25600
Mvav 92,24,184
v_lshrrev_b32 v22, 12, v22
v_accvgpr_write_b32 a196, v176
Mvaa 92,28,136
v_mul_u32_u24 v23, 20, v22
v_accvgpr_write_b32 a197, v177
Mvaa 60,28,132
v_accvgpr_write_b32 a198, v178
v_sub_u32 v20, v20, v23
Mvaa 60,24,60
s_waitcnt vmcnt(12) lgkmcnt(8)
buffer_load_dwordx4 a[32:35], v27, s[8:11], s93 offen offset:0 sc1
v_accvgpr_write_b32 a199, v179
Mvaa 32,0,148
s_add_u32 s28, s28, 512
s_cmp_eq_u32 s28, s4
s_cselect_b32 s93, 10240, 0
s_sub_u32 s28, s28, s93
Mvav 32,4,208
s_add_u32 s93, s28, 20480
Ta 24,5,16384
Ta 26,5,17408
Mvav 64,4,216
v_lshrrev_b32 v23, 1, v22
G 168,200
Mvaa 64,0,152
v_and_b32 v23, 2, v23
G 169,201
Mvaa 96,0,156
buffer_load_dwordx4 a[36:39], v26, s[8:11], s28 offen offset:0 sc1
Ta 28,5,24576
Mvav 96,4,224
Ta 30,5,25600
G 170,202
Mvav 128,4,188
v_xor_b32 v20, v20, v23
G 171,203
Mvav 128,0,200
W 8
v_mul_u32_u24 v22, 80, v22
G 172,204
Mvav 132,8,200
v_add_lshl_u32 v16, v20, v22, 4
G 173,205
Mvav 132,12,188
buffer_load_dwordx4 a[40:43], v26, s[8:11], s93 offen offset:0 sc1
Ta 0,6,16384
Mvav 100,12,224
Ta 2,6,17408
G 174,206
Mvaa 100,8,156
v_lshrrev_b32 v20, 4, v1
G 175,207
Mvaa 68,8,152
Ta 4,6,24576
v_add_u32 v20, 64, v20
Mvav 68,12,216
Ta 6,6,25600
K 168,168,169
Mvav 36,12,208
buffer_load_dwordx4 a[44:47], v27, s[8:11], s28 offen offset:0 sc1
K 169,170,171
Mvaa 36,8,148
W 8
v_mul_u32_u24 v22, 205, v20
K 170,172,173
)"
R"(Mvaa 40,16,148
v_lshrrev_b32 v22, 12, v22
K 171,174,175
Mvav 40,20,208
Ta 8,7,16384
v_mul_u32_u24 v23, 20, v22
Mvav 72,20,216
Ta 10,7,17408
v_accvgpr_write_b32 a200, v168
Mvaa 72,16,152
buffer_load_dwordx4 a[48:51], v27, s[8:11], s93 offen offset:0 sc1
v_accvgpr_write_b32 a201, v169
Mvaa 104,16,156
s_add_u32 s28, s28, 512
s_cmp_eq_u32 s28, s4
s_cselect_b32 s93, 10240, 0
s_sub_u32 s28, s28, s93
Mvav 104,20,224
s_add_u32 s93, s28, 20480
v_sub_u32 v20, v20, v23
v_accvgpr_write_b32 a202, v170
Mvav 136,20,188
Ta 12,7,24576
Ta 14,7,25600
Mvav 136,16,200
W 8
v_lshrrev_b32 v23, 1, v22
v_accvgpr_write_b32 a203, v171
Mvav 140,24,200
buffer_load_dwordx4 a[52:55], v26, s[8:11], s28 offen offset:0 sc1
v_and_b32 v23, 2, v23
Mvav 140,28,188
G 176,72
G 177,73
Mvav 108,28,224
v_xor_b32 v20, v20, v23
G 178,74
Mvaa 108,24,156
Ta 16,8,16384
Ta 18,8,17408
Mvaa 76,24,152
v_mul_u32_u24 v22, 80, v22
G 179,75
Mvav 76,28,216
buffer_load_dwordx4 a[56:59], v26, s[8:11], s93 offen offset:0 sc1
G 180,76
Mvav 44,28,208
Ta 20,8,24576
v_add_lshl_u32 v17, v20, v22, 4
Mvaa 44,24,148
W 7
Ta 22,8,25600
G 181,77
Mvaa 48,0,148
v_subrev_u32 v17, 1024, v17
G 182,78
Mvav 48,4,208
v_lshrrev_b32 v20, 4, v1
G 183,79
Mvav 80,4,216
buffer_load_dwordx4 a[184:187], v27, s[8:11], s28 offen offset:0 sc1
K 176,176,177
Mvaa 80,0,152
Ta 24,9,16384
v_add_u32 v20, 128, v20
Mvaa 112,0,156
Ta 26,9,17408
K 177,178,179
Mvav 112,4,224
v_mul_u32_u24 v22, 205, v20
K 178,180,181
Mvav 144,4,188
v_lshrrev_b32 v22, 12, v22
K 179,182,183
Mvav 144,0,200
W 6
buffer_load_dwordx4 a[188:191], v27, s[8:11], s93 offen offset:0 sc1
Ta 28,9,24576
Mvav 148,8,200
s_add_u32 s28, s28, 512
s_cmp_eq_u32 s28, s4
s_cselect_b32 s93, 10240, 0
s_sub_u32 s28, s28, s93
Mvav 148,12,188
s_add_u32 s93, s28, 20480
Ta 30,9,25600
v_accvgpr_write_b32 a204, v176
Mvav 116,12,224
v_mul_u32_u24 v23, 20, v22
v_accvgpr_write_b32 a205, v177
Mvaa 116,8,156
s_waitcnt vmcnt(15)
v_mov_b64 v[28:29], s[12:13]
s_mov_b64 exec, 1
buffer_store_dwordx2 v[28:29], off, s[8:11], s71 sc1
s_mov_b64 exec, -1
v_sub_u32 v20, v20, v23
v_accvgpr_write_b32 a206, v178
Mvaa 84,8,152
s_waitcnt vmcnt(12) lgkmcnt(0)
s_barrier
v_accvgpr_write_b32 a207, v179
Mvav 84,12,216
v_lshrrev_b32 v23, 1, v22
G 168,208
Mvav 52,12,208
v_and_b32 v23, 2, v23
G 169,209
Mvaa 52,8,148
v_xor_b32 v20, v20, v23
G 170,210
Mvaa 56,16,148
buffer_load_dwordx4 a[60:63], v26, s[8:11], s28 offen offset:0 sc1
G 171,211
Mvav 56,20,208
Ta 0,10,32768
Ta 2,10,33792
Mvav 88,20,216
Ta 4,10,40960
Ta 6,10,41984
Mvaa 88,16,152
v_mul_u32_u24 v22, 80, v22
G 172,212
Mvaa 120,16,156
v_add_lshl_u32 v18, v20, v22, 4
G 173,213
Mvav 120,20,224
buffer_load_dwordx4 v[184:187], v26, s[8:11], s93 offen offset:0 sc1
Ta 8,11,32768
Mvav 152,20,188
Ta 10,11,33792
G 174,214
Mvav 152,16,200
v_subrev_u32 v18, 2048, v18
G 175,215
Mvav 156,24,200
Ta 12,11,40960
v_lshrrev_b32 v20, 4, v1
Mvav 156,28,188
Ta 14,11,41984
K 168,168,169
Mvav 124,28,224
buffer_load_dwordx4 v[192:195], v27, s[8:11], s28 offen offset:0 sc1
v_add_u32 v20, 192, v20
Mvaa 124,24,156
K 169,170,171
K 170,172,173
Mvaa 92,24,152
v_mul_u32_u24 v22, 205, v20
K 171,174,175
Mvav 92,28,216
Ta 16,12,32768
v_lshrrev_b32 v22, 12, v22
Mvav 60,28,208
Ta 18,12,33792
v_accvgpr_write_b32 a208, v168
Mvaa 60,24,148
s_waitcnt vmcnt(12) lgkmcnt(6)
buffer_load_dwordx4 a[128:131], v27, s[8:11], s93 offen offset:0 sc1
v_mul_u32_u24 v23, 20, v22
Mvav 32,0,196
s_add_u32 s28, s28, 512
v_accvgpr_write_b32 a209, v169
v_accvgpr_write_b32 a210, v170
Mvav 32,4,228
s_cmp_eq_u32 s28, s4
s_cselect_b32 s93, 10240, 0
Ta 20,12,40960
v_sub_u32 v20, v20, v23
Mvav 64,4,236
s_sub_u32 s28, s28, s93
Ta 22,12,41984
v_accvgpr_write_b32 a211, v171
Mvav 64,0,204
s_add_u32 s93, s28, 20480
v_lshrrev_b32 v23, 1, v22
G 176,80
Mvav 96,0,212
G 177,81
v_and_b32 v23, 2, v23
Mvav 96,4,232
G 178,82
v_xor_b32 v20, v20, v23
Mvaa 128,4,32
Ta 24,13,32768
Ta 26,13,33792
Mvav 128,0,220
W 6
buffer_load_dwordx4 a[132:135], v26, s[8:11], s28 offen offset:0 sc1
G 179,83
Mvav 132,8,220
v_mul_u32_u24 v22, 80, v22
G 180,84
Mvaa 132,12,32
Ta 28,13,40960
Ta 30,13,41984
Mvav 100,12,232
G 181,85
v_add_lshl_u32 v19, v20, v22, 4
Mvav 100,8,212
G 182,86
v_subrev_u32 v19, 3072, v19
Mvav 68,8,204
G 183,87
v_lshrrev_b32 v20, 4, v1
Mvav 68,12,236
Ta 0,14,32768
K 176,176,177
Mvav 36,12,228
Ta 2,14,33792
K 177,178,179
Mvav 36,8,196
W 6
buffer_load_dwordx4 a[136:139], v26, s[8:11], s93 offen offset:0 sc1
v_add_u32 v20, 256, v20
Mvav 40,16,196
K 178,180,181
v_mul_u32_u24 v22, 205, v20
Mvav 40,20,228
Ta 4,14,40960
K 179,182,183
Mvav 72,20,236
Ta 6,14,41984
v_accvgpr_write_b32 a212, v176
Mvav 72,16,204
v_lshrrev_b32 v22, 12, v22
v_accvgpr_write_b32 a213, v177
Mvav 104,16,212
v_mul_u32_u24 v23, 20, v22
v_accvgpr_write_b32 a214, v178
Mvav 104,20,232
v_sub_u32 v20, v20, v23
v_accvgpr_write_b32 a215, v179
Mvaa 136,20,32
Ta 8,15,32768
Ta 10,15,33792
Mvav 136,16,220
W 6
buffer_load_dwordx4 a[140:143], v27, s[8:11], s28 offen offset:0 sc1
G 168,216
Mvav 140,24,220
v_lshrrev_b32 v23, 1, v22
G 169,217
Mvaa 140,28,32
Ta 12,15,40960
v_and_b32 v23, 2, v23
Mvav 108,28,232
Ta 14,15,41984
G 170,218
Mvav 108,24,212
v_xor_b32 v20, v20, v23
G 171,219
Mvav 76,24,204
G 172,220
v_mul_u32_u24 v22, 80, v22
Mvav 76,28,236
Ta 16,24,32768
G 173,221
Mvav 44,28,228
Ta 18,24,33792
v_add_lshl_u32 v20, v20, v22, 4
Mvav 44,24,196
W 6
buffer_load_dwordx4 a[144:147], v27, s[8:11], s93 offen offset:0 sc1
G 174,222
Mvav 48,0,196
s_add_u32 s28, s28, 512
G 175,223
v_lshrrev_b32 v22, 6, v1
Mvav 48,4,228
s_cmp_eq_u32 s28, s4
s_cselect_b32 s93, 10240, 0
Ta 20,24,40960
Ta 22,24,41984
Mvav 80,4,236
s_sub_u32 s28, s28, s93
K 168,168,169
v_mul_u32_u24 v22, 320, v22
Mvav 80,0,204
s_add_u32 s93, s28, 20480
K 169,170,171
v_bfe_u32 v23, v1, 4, 2
Mvav 112,0,212
K 170,172,173
K 171,174,175
Mvav 112,4,232
Ta 24,25,32768
v_lshl_add_u32 v22, v23, 3, v22
Mvaa 144,4,32
Ta 26,25,33792
v_accvgpr_write_b32 a216, v168
Mvav 144,0,220
W 6
buffer_load_dwordx4 v[200:203], v26, s[8:11], s28 offen offset:0 sc1
v_bfe_u32 v23, v1, 8, 1
Mvav 148,8,220
v_accvgpr_write_b32 a217, v169
v_lshlrev_b32 v23, 5, v23
Mvaa 148,12,32
Ta 28,25,40960
v_accvgpr_write_b32 a218, v170
Mvav 116,12,232
Ta 30,25,41984
v_accvgpr_write_b32 a219, v171
Mvav 116,8,212
v_add_u32 v21, v22, v23
G 176,88
Mvav 84,8,204
v_sub_u32 v22, v22, v23
G 177,89
Mvav 84,12,236
s_mul_i32 s94, s59, 655360
Ta 0,10,49152
G 178,90
Mvav 52,12,228
s_mul_i32 s95, s58, 320
Ta 2,10,50176
G 179,91
Mvav 52,8,196
W 6
buffer_load_dwordx4 v[208:211], v26, s[8:11], s93 offen offset:0 sc1
G 180,92
Mvav 56,16,196
s_add_u32 s94, s94, s95
Ta 4,10,57344
Ta 6,10,58368
Mvav 56,20,228
G 181,93
G 182,94
Mvav 88,20,236
s_add_u32 s40, s26, s94
s_addc_u32 s41, s27, 0
G 183,95
Mvav 88,16,204
s_and_b32 s41, s41, 0xffff
Ta 8,11,49152
K 176,176,177
Mvav 120,16,212
s_mov_b32 s42, 5242880
Ta 10,11,50176
K 177,178,179
Mvav 120,20,232
s_mov_b32 s43, 0x20000
buffer_load_dwordx4 v[216:219], v27, s[8:11], s28 offen offset:0 sc1
K 178,180,181
Mvaa 152,20,32
s_sub_u32 s42, s42, s94
Ta 12,11,57344
K 179,182,183
Mvav 152,16,220
W 7
Ta 14,11,58368
v_accvgpr_write_b32 a220, v176
Mvav 156,24,220
s_mov_b64 s[44:45], s[8:9]
v_accvgpr_write_b32 a221, v177
v_accvgpr_write_b32 a222, v178
Mvaa 156,28,32
s_mov_b32 s47, 0x20000
Ta 16,12,49152
v_accvgpr_write_b32 a223, v179
Mvav 124,28,232
s_and_b32 s48, s14, 1
Ta 18,12,50176
buffer_load_dwordx4 v[224:227], v27, s[8:11], s93 offen offset:0 sc1
Mvav 124,24,212
G 168,224
G 169,225
Mvav 92,24,204
s_mul_i32 s48, s48, 5120
Ta 20,12,57344
Ta 22,12,58368
Mvav 92,28,236
s_lshr_b32 s51, s14, 1
G 170,226
G 171,227
Mvav 60,28,228
G 172,228
Mvav 60,24,196
s_waitcnt vmcnt(13) lgkmcnt(8)
s_and_b32 s94, s14, 1
G 173,229
Mvaa 32,0,36
s_add_u32 s28, s28, 512
Ta 24,13,49152
Ta 26,13,50176
Mvaa 32,4,52
s_cmp_eq_u32 s28, s4
s_cselect_b32 s93, 10240, 0
s_sub_u32 s28, s28, s93
G 174,230
Mvaa 64,4,56
s_add_u32 s93, s28, 20480
s_lshl_b32 s95, s57, 1
buffer_load_dwordx4 v[188:191], v26, s[8:11], s28 offen offset:0 sc1
Mvaa 64,0,40
s_add_u32 s94, s94, s95
Ta 28,13,57344
G 175,231
Mvaa 96,0,44
)"
R"(s_lshl_b32 s54, s94, 3
Ta 30,13,58368
K 168,168,169
Mvaa 96,4,184
K 169,170,171
K 170,172,173
Mvaa 128,4,188
s_add_u32 s54, s54, s59
K 171,174,175
Mvaa 128,0,48
W 8
Ta 0,14,49152
Ta 2,14,50176
Mvaa 132,8,48
s_lshl_b32 s54, s54, 6
buffer_load_dwordx4 v[196:199], v26, s[8:11], s93 offen offset:0 sc1
v_accvgpr_write_b32 a224, v168
Mvaa 132,12,188
s_xor_b32 s55, s54, 15872
v_accvgpr_write_b32 a225, v169
Mvaa 100,12,184
Ta 4,14,57344
v_accvgpr_write_b32 a226, v170
Mvaa 100,8,44
s_add_u32 s54, s54, 20480
Ta 6,14,58368
v_accvgpr_write_b32 a227, v171
Mvaa 68,8,40
s_and_b32 s81, s14, 1
G 176,96
Mvaa 68,12,56
s_add_u32 s55, s55, 20480
Mvaa 36,12,52
G 177,97
Mvaa 36,8,36
W 8
s_and_b32 s95, s14, 2
G 178,98
Mvaa 40,16,36
s_mul_i32 s91, s81, 17164288
Ta 8,15,49152
Ta 10,15,50176
Mvaa 40,20,52
s_lshl_b32 s94, s94, 2
buffer_load_dwordx4 v[204:207], v27, s[8:11], s28 offen offset:0 sc1
G 179,99
Mvaa 72,20,56
s_mul_i32 s82, s56, 536576
s_add_u32 s94, s94, s95
G 180,100
Mvaa 72,16,40
Ta 12,15,57344
Ta 14,15,58368
Mvaa 104,16,44
s_lshl_b32 s95, s95, 4
G 181,101
G 182,102
Mvaa 104,20,184
s_add_u32 s91, s91, s82
G 183,103
Mvaa 136,20,188
s_add_u32 s55, s55, s95
s_add_u32 s91, s91, 4194304
K 176,176,177
Mvaa 136,16,48
W 8
s_lshl_b32 s49, s94, 17
Ta 16,24,49152
Mvaa 140,24,48
Ta 18,24,50176
K 177,178,179
Mvaa 140,28,188
s_lshl_b32 s95, s59, 14
buffer_load_dwordx4 v[212:215], v27, s[8:11], s93 offen offset:0 sc1
K 178,180,181
Mvaa 108,28,184
s_lshr_b32 s82, s14, 1
Ta 20,24,57344
K 179,182,183
Mvaa 108,24,44
s_add_u32 s49, s49, s95
Ta 22,24,58368
v_accvgpr_write_b32 a228, v176
Mvaa 76,24,40
s_lshl_b32 s83, s82, 14
s_xor_b32 s50, s49, 16252928
Mvaa 76,28,56
v_accvgpr_write_b32 a229, v177
Mvaa 44,28,52
s_add_u32 s91, s91, s83
s_add_u32 s49, s49, 39845888
v_accvgpr_write_b32 a230, v178
Mvaa 44,24,36
W 8
Ta 24,25,49152
v_accvgpr_write_b32 a231, v179
Mvaa 48,0,36
s_add_u32 s50, s50, 39845888
Ta 26,25,50176
G 168,232
Mvaa 48,4,52
s_lshl_b32 s81, s81, 14
Mvaa 80,4,56
s_lshr_b32 s94, s15, 3
Ta 28,25,57344
G 169,233
Mvaa 80,0,40
s_lshl_b32 s82, s82, 16
Ta 30,25,58368
G 170,234
Mvaa 112,0,44
s_lshl_b32 s94, s94, 3
G 171,235
Mvaa 112,4,184
s_add_u32 s94, s94, s14
s_or_b32 s92, s81, s82
G 172,236
Mvaa 144,4,188
Mvaa 144,0,48
W 8
s_lshl_b32 s94, s94, 4
G 173,237
Mvaa 148,8,48
s_xor_b32 s92, s92, 0x10000
Ta 0,2,32768
Ta 2,2,33792
Mvaa 148,12,188
s_add_u32 s94, s94, s59
G 174,238
G 175,239
Mvaa 116,12,184
s_mul_i32 s52, s94, 5120
Ta 4,2,40960
Ta 6,2,41984
Mvaa 116,8,44
K 168,168,169
Mvaa 84,8,40
s_lshl_b32 s95, s59, 12
K 169,170,171
Mvaa 84,12,56
K 170,172,173
Mvaa 52,12,52
s_sub_u32 s52, s52, s95
K 171,174,175
Mvaa 52,8,36
W 8
s_add_u32 s52, s52, 56623104
Ta 8,3,32768
Mvaa 56,16,36
Ta 10,3,33792
v_accvgpr_write_b32 a232, v168
Mvaa 56,20,52
s_lshl_b32 s53, s94, 3
v_accvgpr_write_b32 a233, v169
Mvaa 88,20,56
Ta 12,3,40960
v_accvgpr_write_b32 a234, v170
Mvaa 88,16,40
s_add_u32 s53, s53, 65536
Ta 14,3,41984
v_accvgpr_write_b32 a235, v171
Mvaa 120,16,44
G 176,104
v_bfe_u32 v244, v1, 4, 1
Mvaa 120,20,184
Mvaa 152,20,188
G 177,105
v_mul_u32_u24 v244, 0xaaaa, v244
Mvaa 152,16,48
W 8
G 178,106
v_add_u32 v244, 0x4444, v244
Mvaa 156,24,48
Ta 16,4,32768
Ta 18,4,33792
Mvaa 156,28,188
G 179,107
v_lshl_or_b32 v240, v244, 16, v244
Mvaa 124,28,184
G 180,108
v_mov_b32 v241, 0xff800000
Mvaa 124,24,44
Ta 20,4,40960
Ta 22,4,41984
Mvaa 92,24,40
G 181,109
v_add_u32 v242, 0x10000, v1
Mvaa 92,28,56
G 182,110
v_bfe_u32 v244, v1, 4, 4
Mvaa 60,28,52
G 183,111
v_lshrrev_b32 v245, 8, v1
Mvaa 60,24,36
s_waitcnt vmcnt(8) lgkmcnt(8)
K 176,176,177
v_lshlrev_b32 v245, 2, v245
Mvaa 32,0,60
Ta 24,5,32768
Ta 26,5,33792
Mvaa 32,4,132
K 177,178,179
v_sub_u32 v244, v244, v245
Mvaa 64,4,136
K 178,180,181
v_cmp_le_i32_e64 s[72:73], 0, v244
Mvav 64,0,184
Ta 28,5,40960
K 179,182,183
Mvav 96,0,192
Ta 30,5,41984
v_accvgpr_write_b32 a236, v176
Mvaa 96,4,140
v_cmp_le_i32_e64 s[74:75], 1, v244
v_accvgpr_write_b32 a237, v177
Mvaa 128,4,144
v_cmp_le_i32_e64 s[76:77], 2, v244
v_accvgpr_write_b32 a238, v178
Mvaa 128,0,128
W 8
v_cmp_le_i32_e64 s[78:79], 3, v244
v_accvgpr_write_b32 a239, v179
Mvaa 132,8,128
Ta 0,6,32768
Ta 2,6,33792
Mvaa 132,12,144
v_add_u32 v243, 4096, v1
G 168,240
Mvaa 100,12,140
v_add_u32 v244, 8192, v1
G 169,241
Mvav 100,8,192
Ta 4,6,40960
v_add_u32 v245, 12288, v1
Mvav 68,8,184
s_mov_b32 s18, 0x3f803f80
Ta 6,6,41984
G 170,242
Mvaa 68,12,136
s_mov_b32 s19, 0x3f003f00
G 171,243
G 172,244
Mvaa 36,12,132
Mvaa 36,8,60
W 8
v_accvgpr_write_b32 a176, s18
G 173,245
Mvaa 40,16,60
Ta 8,7,32768
Ta 10,7,33792
Mvaa 40,20,132
v_accvgpr_write_b32 a180, s19
G 174,246
Mvaa 72,20,136
G 175,247
v_mov_b32 v252, v241
Mvav 72,16,184
Ta 12,7,40960
Ta 14,7,41984
Mvav 104,16,192
K 168,168,169
v_accvgpr_write_b32 a177, s18
Mvaa 104,20,140
K 169,170,171
v_accvgpr_write_b32 a181, s19
Mvaa 136,20,144
K 170,172,173
v_mov_b32 v253, v241
Mvaa 136,16,128
W 8
K 171,174,175
v_accvgpr_write_b32 a178, s18
Mvaa 140,24,128
Ta 16,8,32768
Ta 18,8,33792
Mvaa 140,28,144
v_accvgpr_write_b32 a240, v168
v_accvgpr_write_b32 a182, s19
Mvaa 108,28,140
v_accvgpr_write_b32 a241, v169
v_mov_b32 v254, v241
Mvav 108,24,192
Ta 20,8,40960
v_accvgpr_write_b32 a242, v170
Mvav 76,24,184
Ta 22,8,41984
v_accvgpr_write_b32 a179, s18
Mvaa 76,28,136
v_accvgpr_write_b32 a243, v171
v_accvgpr_write_b32 a183, s19
Mvaa 44,28,132
G 176,112
v_mov_b32 v255, v241
Mvaa 44,24,60
W 8
G 177,113
G 178,114
Mvaa 48,0,60
Ta 24,9,32768
v_accvgpr_write_b32 a160, 0
Mvaa 48,4,132
Ta 26,9,33792
G 179,115
Mvaa 80,4,136
v_accvgpr_write_b32 a161, 0
G 180,116
Mvav 80,0,184
Ta 28,9,40960
v_accvgpr_write_b32 a162, 0
Mvav 112,0,192
Ta 30,9,41984
G 181,117
Mvaa 112,4,140
v_accvgpr_write_b32 a163, 0
G 182,118
Mvaa 144,4,144
v_accvgpr_write_b32 a164, 0
G 183,119
Mvaa 144,0,128
W 8
v_accvgpr_write_b32 a165, 0
K 176,176,177
Mvaa 148,8,128
Ta 0,2,49152
v_accvgpr_write_b32 a166, 0
Mvaa 148,12,144
Ta 2,2,50176
K 177,178,179
Mvaa 116,12,140
v_accvgpr_write_b32 a167, 0
K 178,180,181
Mvav 116,8,192
v_accvgpr_write_b32 a168, 0
K 179,182,183
Mvav 84,8,184
Ta 4,2,57344
Ta 6,2,58368
Mvaa 84,12,136
v_accvgpr_write_b32 a169, 0
v_accvgpr_write_b32 a244, v176
Mvaa 52,12,132
v_accvgpr_write_b32 a245, v177
v_accvgpr_write_b32 a170, 0
Mvaa 52,8,60
W 8
v_accvgpr_write_b32 a246, v178
v_accvgpr_write_b32 a171, 0
Mvaa 56,16,60
Ta 8,3,49152
v_accvgpr_write_b32 a247, v179
Mvaa 56,20,132
Ta 10,3,50176
v_accvgpr_write_b32 a172, 0
Mvaa 88,20,136
G 168,248
v_accvgpr_write_b32 a173, 0
Mvav 88,16,184
G 169,249
v_accvgpr_write_b32 a174, 0
Mvav 120,16,192
Ta 12,3,57344
Ta 14,3,58368
Mvaa 120,20,140
G 170,250
v_accvgpr_write_b32 a175, 0
Mvaa 152,20,144
s_mov_b32 s90, 0x5d800000
G 171,251
G 172,252
Mvaa 152,16,128
W 8
s_mov_b64 s[88:89], 0
G 173,253
Mvaa 156,24,128
s_mov_b32 s64, 0
Ta 16,4,49152
Ta 18,4,50176
Mvaa 156,28,144
s_and_b32 s18, s14, 1
G 174,254
G 175,255
Mvaa 124,28,140
s_lshl_b32 s62, s18, 14
Ta 20,4,57344
Ta 22,4,58368
Mvav 124,24,192
K 168,168,169
Mvav 92,24,184
s_lshl_b32 s20, s57, 15
K 169,170,171
Mvaa 92,28,136
s_sub_u32 s20, 507904, s20
K 170,172,173
Mvaa 60,28,132
K 171,174,175
Mvaa 60,24,60
s_waitcnt vmcnt(0) lgkmcnt(8)
s_add_u32 s20, s20, s62
Ta 24,5,49152
Mvav 32,0,200
)"
R"(s_lshl_b32 s65, s57, 1
Ta 26,5,50176
v_accvgpr_write_b32 a248, v168
Mvav 32,4,188
s_sub_u32 s65, s65, 1
v_accvgpr_write_b32 a249, v169
Mvav 64,4,196
Ta 28,5,57344
v_accvgpr_write_b32 a250, v170
Mvav 64,0,208
s_max_i32 s65, s65, 0
Ta 30,5,58368
v_accvgpr_write_b32 a251, v171
Mvav 96,0,216
s_sub_u32 s66, s60, 1
G 176,120
Mvav 96,4,204
s_cmp_lt_u32 s14, 2
s_cselect_b32 s20, s20, 0
s_cselect_b32 s66, s66, 15
s_cselect_b32 s21, 32768, 0
Mvav 128,4,212
G 177,121
Mvav 128,0,224
W 8
s_add_u32 s61, s91, s20
G 178,122
Mvav 132,8,224
s_add_u32 s62, s62, s21
Ta 0,6,49152
Ta 2,6,50176
Mvav 132,12,212
G 179,123
G 180,124
Mvav 100,12,204
s_sub_u32 s63, 16384, s21
Ta 4,6,57344
Ta 6,6,58368
Mvav 100,8,216
s_lshl_b32 s67, s60, 7
G 181,125
Mvav 68,8,208
s_add_u32 s67, s67, s56
G 182,126
Mvav 68,12,196
G 183,127
Mvav 36,12,188
s_lshl_b32 s67, s67, 12
K 176,176,177
Mvav 36,8,200
W 8
s_add_u32 s67, s67, 39845888
Ta 8,7,49152
Mvav 40,16,200
s_lshl_b32 s69, s60, 3
Ta 10,7,50176
K 177,178,179
Mvav 40,20,188
K 178,180,181
Mvav 72,20,196
s_add_u32 s69, s69, s59
Ta 12,7,57344
K 179,182,183
Mvav 72,16,208
s_lshl_b32 s69, s69, 6
Ta 14,7,58368
v_accvgpr_write_b32 a252, v176
Mvav 104,16,216
Mvav 104,20,204
s_lshl_b32 s21, s58, 3
v_accvgpr_write_b32 a253, v177
Mvav 136,20,212
s_add_u32 s69, s69, s21
v_accvgpr_write_b32 a254, v178
Mvav 136,16,224
W 8
s_add_u32 s69, s69, 20480
v_accvgpr_write_b32 a255, v179
Mvav 140,24,224
Ta 16,8,49152
Ta 18,8,50176
Mvav 140,28,212
s_mov_b32 s70, 0
Mvav 108,28,204
s_mov_b32 s37, 0
Ta 20,8,57344
Ta 22,8,58368
Mvav 108,24,216
s_sub_u32 s18, s57, 4
Mvav 76,24,208
Mvav 76,28,196
s_lshl_b32 s18, s18, 2
Mvav 44,28,188
s_add_u32 s18, s18, s58
Mvav 44,24,200
W 8
Ta 24,9,49152
Ta 26,9,50176
Mvav 48,0,200
s_mul_i32 s85, s59, 7
Mvav 48,4,188
s_add_u32 s85, s85, s18
Mvav 80,4,196
s_cmp_lt_u32 s18, 7
s_cselect_b32 s85, s85, 99
Ta 28,9,57344
Ta 30,9,58368
Mvav 80,0,208
Mvav 112,0,216
s_cmp_lt_u32 s85, 54
s_cselect_b32 s65, 2, s65
Mvav 112,4,204
s_mov_b32 s68, 0
Mvav 144,4,212
s_mov_b64 s[24:25], -1
Mvav 144,0,224
W 8
Mvav 148,8,224
s_mov_b32 s22, 0x3e0293ee
Mvav 148,12,212
s_mov_b32 s23, 0x3e0293ee
Mvav 116,12,204
Mvav 116,8,216
Mvav 84,8,208
Mvav 84,12,196
Mvav 52,12,188
Mvav 52,8,200
W 4
Mvav 56,16,200
Mvav 56,20,188
Mvav 88,20,196
Mvav 88,16,208
Mvav 120,16,216
W 0
s_barrier
Ra 128,242,0
Mvav 120,20,204
Mvav 152,20,212
Ra 132,242,1024
Mvav 152,16,224
Mvav 156,24,224
Ra 136,242,2048
Mvav 156,28,212
Mvav 124,28,204
Ra 140,242,3072
Mvav 124,24,216
Mvav 92,24,208
Mvav 92,28,196
Mvav 60,28,188
Mvav 60,24,200
.Lago%=:
s_cmp_eq_u32 s60, 1
s_cbranch_scc1 .Lpe_48%=
s_cmp_eq_u32 s60, 0
s_cbranch_scc1 .Lpd_32%=
v_pk_mul_f32 v[32:33], v[32:33], s[22:23]
v_pk_mul_f32 v[34:35], v[34:35], s[22:23]
v_pk_mul_f32 v[36:37], v[36:37], s[22:23]
v_pk_mul_f32 v[38:39], v[38:39], s[22:23]
v_pk_mul_f32 v[64:65], v[64:65], s[22:23]
v_pk_mul_f32 v[66:67], v[66:67], s[22:23]
v_pk_mul_f32 v[68:69], v[68:69], s[22:23]
v_pk_mul_f32 v[70:71], v[70:71], s[22:23]
v_pk_mul_f32 v[96:97], v[96:97], s[22:23]
v_pk_mul_f32 v[98:99], v[98:99], s[22:23]
v_pk_mul_f32 v[100:101], v[100:101], s[22:23]
v_pk_mul_f32 v[102:103], v[102:103], s[22:23]
v_pk_mul_f32 v[128:129], v[128:129], s[22:23]
v_pk_mul_f32 v[130:131], v[130:131], s[22:23]
v_pk_mul_f32 v[132:133], v[132:133], s[22:23]
v_pk_mul_f32 v[134:135], v[134:135], s[22:23]
v_pk_mul_f32 v[40:41], v[40:41], s[22:23]
v_pk_mul_f32 v[42:43], v[42:43], s[22:23]
v_pk_mul_f32 v[44:45], v[44:45], s[22:23]
v_pk_mul_f32 v[46:47], v[46:47], s[22:23]
v_pk_mul_f32 v[72:73], v[72:73], s[22:23]
v_pk_mul_f32 v[74:75], v[74:75], s[22:23]
v_pk_mul_f32 v[76:77], v[76:77], s[22:23]
v_pk_mul_f32 v[78:79], v[78:79], s[22:23]
v_pk_mul_f32 v[104:105], v[104:105], s[22:23]
v_pk_mul_f32 v[106:107], v[106:107], s[22:23]
v_pk_mul_f32 v[108:109], v[108:109], s[22:23]
v_pk_mul_f32 v[110:111], v[110:111], s[22:23]
v_pk_mul_f32 v[136:137], v[136:137], s[22:23]
v_pk_mul_f32 v[138:139], v[138:139], s[22:23]
v_pk_mul_f32 v[140:141], v[140:141], s[22:23]
v_pk_mul_f32 v[142:143], v[142:143], s[22:23]
v_pk_mul_f32 v[48:49], v[48:49], s[22:23]
v_pk_mul_f32 v[50:51], v[50:51], s[22:23]
v_pk_mul_f32 v[52:53], v[52:53], s[22:23]
v_pk_mul_f32 v[54:55], v[54:55], s[22:23]
v_pk_mul_f32 v[80:81], v[80:81], s[22:23]
v_pk_mul_f32 v[82:83], v[82:83], s[22:23]
v_pk_mul_f32 v[84:85], v[84:85], s[22:23]
v_pk_mul_f32 v[86:87], v[86:87], s[22:23]
v_pk_mul_f32 v[112:113], v[112:113], s[22:23]
v_pk_mul_f32 v[114:115], v[114:115], s[22:23]
v_pk_mul_f32 v[116:117], v[116:117], s[22:23]
v_pk_mul_f32 v[118:119], v[118:119], s[22:23]
v_pk_mul_f32 v[144:145], v[144:145], s[22:23]
v_pk_mul_f32 v[146:147], v[146:147], s[22:23]
v_pk_mul_f32 v[148:149], v[148:149], s[22:23]
v_pk_mul_f32 v[150:151], v[150:151], s[22:23]
v_pk_mul_f32 v[56:57], v[56:57], s[22:23]
v_pk_mul_f32 v[58:59], v[58:59], s[22:23]
v_pk_mul_f32 v[60:61], v[60:61], s[22:23]
v_pk_mul_f32 v[62:63], v[62:63], s[22:23]
v_pk_mul_f32 v[88:89], v[88:89], s[22:23]
v_pk_mul_f32 v[90:91], v[90:91], s[22:23]
v_pk_mul_f32 v[92:93], v[92:93], s[22:23]
v_pk_mul_f32 v[94:95], v[94:95], s[22:23]
v_pk_mul_f32 v[120:121], v[120:121], s[22:23]
v_pk_mul_f32 v[122:123], v[122:123], s[22:23]
v_pk_mul_f32 v[124:125], v[124:125], s[22:23]
v_pk_mul_f32 v[126:127], v[126:127], s[22:23]
v_pk_mul_f32 v[152:153], v[152:153], s[22:23]
v_pk_mul_f32 v[154:155], v[154:155], s[22:23]
v_pk_mul_f32 v[156:157], v[156:157], s[22:23]
v_pk_mul_f32 v[158:159], v[158:159], s[22:23]
K 32,32,33
K 33,34,35
K 34,36,37
K 35,38,39
K 40,40,41
K 41,42,43
K 42,44,45
K 43,46,47
W 0
Zvav 196,128,32
s_add_u32 s36, s29, 65536
Ra 144,242,4096
Ra 148,242,5120
Ra 152,242,6144
Ra 156,242,7168
Mvav 196,132,40
K 64,64,65
K 65,66,67
K 66,68,69
K 67,70,71
K 72,72,73
K 73,74,75
K 74,76,77
K 75,78,79
s_nop 1
Zvav 204,132,72
buffer_store_dwordx4 a[192:195], v1, s[8:11], s36 offen offset:0 sc1
K 96,96,97
K 97,98,99
K 98,100,101
Mvav 204,128,64
K 99,102,103
K 104,104,105
K 105,106,107
K 106,108,109
K 107,110,111
Zvav 212,128,96
buffer_store_dwordx4 a[200:203], v1, s[8:11], s36 offen offset:1024 sc1
K 128,128,129
K 129,130,131
K 130,132,133
Mvav 212,132,104
K 131,134,135
K 136,136,137
K 137,138,139
K 138,140,141
K 139,142,143
s_nop 1
Zvav 220,132,136
K 48,48,49
K 49,50,51
K 50,52,53
K 51,54,55
Mvav 220,128,128
K 56,56,57
K 57,58,59
K 58,60,61
K 59,62,63
Mvav 196,136,48
buffer_store_dwordx4 a[208:211], v1, s[8:11], s36 offen offset:2048 sc1
Ra 128,242,8192
Ra 132,242,9216
K 80,80,81
Mvav 196,140,56
K 81,82,83
K 82,84,85
K 83,86,87
K 88,88,89
K 89,90,91
K 90,92,93
K 91,94,95
s_nop 1
Mvav 204,140,88
buffer_store_dwordx4 a[216:219], v1, s[8:11], s36 offen offset:3072 sc1
s_add_u32 s36, s29, 69632
K 112,112,113
K 113,114,115
K 114,116,117
Mvav 204,136,80
K 115,118,119
K 120,120,121
K 121,122,123
K 122,124,125
K 123,126,127
Mvav 212,136,112
K 144,144,145
K 145,146,147
K 146,148,149
K 147,150,151
Mvav 212,140,120
K 152,152,153
K 153,154,155
K 154,156,157
K 155,158,159
s_nop 1
Mvav 220,140,152
buffer_store_dwordx4 a[224:227], v1, s[8:11], s36 offen offset:0 sc1
v_accvgpr_write_b32 a0, v32
v_accvgpr_write_b32 a1, v33
v_accvgpr_write_b32 a2, v34
Mvav 220,136,144
W 2
Ra 136,242,10240
v_accvgpr_write_b32 a3, v35
v_accvgpr_write_b32 a4, v40
Zvav 36,144,32
Ra 140,242,11264
buffer_store_dwordx4 a[232:235], v1, s[8:11], s36 offen offset:1024 sc1
v_accvgpr_write_b32 a5, v41
Mvav 36,148,40
v_accvgpr_write_b32 a6, v42
v_accvgpr_write_b32 a7, v43
v_max3_f32 v4, v196, v197, v198
v_max3_f32 v4, v4, v199, v252
Zvav 68,148,72
v_max3_f32 v5, v204, v205, v206
v_max3_f32 v5, v5, v207, v253
v_max3_f32 v6, v212, v213, v214
v_max3_f32 v6, v6, v215, v254
Mvav 68,144,64
v_max3_f32 v7, v220, v221, v222
v_max3_f32 v7, v7, v223, v255
v_mov_b32 v8, v4
v_mov_b32 v9, v5
Zvav 100,144,96
v_mov_b32 v10, v6
v_mov_b32 v11, v7
v_permlane32_swap_b32 v4, v8
v_accvgpr_write_b32 a8, v48
Mvav 100,148,104
v_permlane32_swap_b32 v5, v9
v_permlane32_swap_b32 v6, v10
v_permlane32_swap_b32 v7, v11
v_max_f32 v4, v4, v8
Zvav 132,148,136
v_max_f32 v5, v5, v9
v_max_f32 v6, v6, v10
v_max_f32 v7, v7, v11
v_mov_b32 v8, v4
Mvav 132,144,128
v_mov_b32 v9, v5
v_mov_b32 v10, v6
v_mov_b32 v11, v7
v_accvgpr_write_b32 a9, v49
Mvav 36,152,48
v_permlane16_swap_b32 v4, v8
v_permlane16_swap_b32 v5, v9
v_permlane16_swap_b32 v6, v10
v_permlane16_swap_b32 v7, v11
Mvav 36,156,56
v_max_f32 v4, v4, v8
v_max_f32 v5, v5, v9
v_max_f32 v6, v6, v10
v_max_f32 v7, v7, v11
Mvav 68,156,88
v_sub_f32 v4, 0, v4
v_sub_f32 v5, 0, v5
v_sub_f32 v6, 0, v6
v_sub_f32 v7, 0, v7
Mvav 68,152,80
v_mov_b32 v224, v4
v_mov_b32 v225, v4
v_mov_b32 v226, v4
v_mov_b32 v227, v4
Mvav 100,152,112
v_mov_b32 v228, v5
v_mov_b32 v229, v5
v_mov_b32 v230, v5
v_mov_b32 v231, v5
Mvav 100,156,120
v_mov_b32 v232, v6
v_mov_b32 v233, v6
v_mov_b32 v234, v6
v_mov_b32 v235, v6
Mvav 132,156,152
v_mov_b32 v236, v7
v_mov_b32 v237, v7
v_mov_b32 v238, v7
v_mov_b32 v239, v7
Mvav 132,152,144
W 0
)"
R"(buffer_store_dwordx4 a[240:243], v1, s[8:11], s36 offen offset:2048 sc1
v_accvgpr_write_b32 a10, v50
v_accvgpr_write_b32 a11, v51
v_accvgpr_write_b32 a12, v56
Cvav 160,128,32,224
s_and_b32 s80, s14, 1
v_accvgpr_write_b32 a13, v57
v_accvgpr_write_b32 a14, v58
v_accvgpr_write_b32 a15, v59
v_accvgpr_write_b32 a16, v64
Mvav 160,132,40
buffer_store_dwordx4 a[248:251], v1, s[8:11], s36 offen offset:3072 sc1
s_add_u32 s36, s29, 393216
Ra 144,242,12288
Ra 148,242,13312
v_accvgpr_write_b32 a17, v65
Cvav 164,132,72,228
s_lshl_b32 s80, s80, 14
v_accvgpr_write_b32 a18, v66
v_accvgpr_write_b32 a19, v67
v_accvgpr_write_b32 a20, v72
v_accvgpr_write_b32 a21, v73
Mvav 164,128,64
s_add_u32 s80, s80, 98304
buffer_store_dwordx4 a[196:199], v1, s[8:11], s36 offen offset:0 sc1
v_accvgpr_write_b32 a22, v74
v_accvgpr_write_b32 a23, v75
v_accvgpr_write_b32 a24, v80
Cvav 168,128,96,232
v_accvgpr_write_b32 a25, v81
v_accvgpr_write_b32 a26, v82
A 196,196,224
v_accvgpr_write_b32 a27, v83
Mvav 168,132,104
buffer_store_dwordx4 a[204:207], v1, s[8:11], s36 offen offset:1024 sc1
A 197,197,224
A 198,198,224
v_accvgpr_write_b32 a28, v88
Cvav 172,132,136,236
Ra 152,242,14336
Ra 156,242,15360
A 199,199,224
v_accvgpr_write_b32 a29, v89
Mvav 172,128,128
buffer_store_dwordx4 a[212:215], v1, s[8:11], s36 offen offset:2048 sc1
X 196
v_accvgpr_write_b32 a30, v90
Mvav 160,136,48
X 197
v_accvgpr_write_b32 a31, v91
v_accvgpr_write_b32 a32, v96
Mvav 160,140,56
X 198
K 192,196,197
v_accvgpr_write_b32 a33, v97
Mvav 164,140,88
X 199
A 204,204,228
v_accvgpr_write_b32 a34, v98
Mvav 164,136,80
buffer_store_dwordx4 a[220:223], v1, s[8:11], s36 offen offset:3072 sc1
s_add_u32 s36, s29, 397312
K 193,198,199
v_accvgpr_write_b32 a35, v99
A 205,205,228
Mvav 168,136,112
A 206,206,228
v_accvgpr_write_b32 a36, v104
v_add_u32 v8, s80, v1
A 207,207,228
Mvav 168,140,120
X 204
v_accvgpr_write_b32 a37, v105
v_accvgpr_write_b32 a38, v106
Mvav 172,140,152
X 205
X 206
Mvav 172,136,144
W 0
buffer_store_dwordx4 a[228:231], v1, s[8:11], s36 offen offset:0 sc1
v_accvgpr_write_b32 a39, v107
v_accvgpr_write_b32 a40, v112
K 200,204,205
Cvav 176,144,32,224
Ra 128,242,24576
X 207
v_accvgpr_write_b32 a41, v113
Mvav 176,148,40
Ra 132,242,25600
A 212,212,232
v_accvgpr_write_b32 a42, v114
K 201,206,207
Cvav 180,148,72,228
A 213,213,232
v_accvgpr_write_b32 a43, v115
A 214,214,232
A 215,215,232
Mvav 180,144,64
buffer_store_dwordx4 a[236:239], v1, s[8:11], s36 offen offset:1024 sc1
v_accvgpr_write_b32 a44, v120
X 212
Cvav 184,144,96,232
v_accvgpr_write_b32 a45, v121
X 213
v_accvgpr_write_b32 a46, v122
Mvav 184,148,104
X 214
v_accvgpr_write_b32 a47, v123
K 208,212,213
Cvav 188,148,136,236
buffer_store_dwordx4 a[244:247], v1, s[8:11], s36 offen offset:2048 sc1
v_accvgpr_write_b32 a48, v128
X 215
Mvav 188,144,128
v_accvgpr_write_b32 a49, v129
A 220,220,236
v_accvgpr_write_b32 a50, v130
K 209,214,215
Mvav 176,152,48
A 221,221,236
v_accvgpr_write_b32 a51, v131
A 222,222,236
A 223,223,236
Mvav 176,156,56
Ra 136,242,26624
v_accvgpr_write_b32 a52, v136
X 220
Mvav 180,156,88
Ra 140,242,27648
v_accvgpr_write_b32 a53, v137
X 221
Mvav 180,152,80
buffer_store_dwordx4 a[252:255], v1, s[8:11], s36 offen offset:3072 sc1
v_accvgpr_write_b32 a54, v138
X 222
Mvav 184,152,112
v_accvgpr_write_b32 a55, v139
v_accvgpr_write_b32 a56, v144
v_accvgpr_write_b32 a57, v145
v_accvgpr_write_b32 a58, v146
v_accvgpr_write_b32 a59, v147
v_accvgpr_write_b32 a60, v152
v_accvgpr_write_b32 a61, v153
v_accvgpr_write_b32 a62, v154
v_accvgpr_write_b32 a63, v155
Mvav 184,156,120
s_barrier
K 216,220,221
X 223
A 36,36,224
Mvav 188,156,152
K 217,222,223
A 37,37,224
A 38,38,224
A 39,39,224
X 36
X 37
X 38
K 194,36,37
X 39
A 68,68,228
K 195,38,39
A 69,69,228
A 70,70,228
A 71,71,228
X 68
X 69
X 70
K 202,68,69
X 71
A 100,100,232
K 203,70,71
A 101,101,232
A 102,102,232
A 103,103,232
X 100
X 101
X 102
K 210,100,101
X 103
A 132,132,236
K 211,102,103
A 133,133,236
A 134,134,236
A 135,135,236
X 132
X 133
X 134
K 218,132,133
X 135
s_nop 0
K 219,134,135
Mvav 188,152,144
W 0
Ra 144,242,28672
Ra 148,242,29696
Ra 152,242,30720
Zvav 32,128,192
Ra 156,242,31744
Zvav 64,128,200
Zvav 96,128,208
s_and_b32 s80, s14, 1
s_xor_b32 s62, s62, 0x10000
Zvav 128,128,216
s_lshl_b32 s80, s80, 14
s_add_u32 s80, s80, 131072
v_add_u32 v23, 61440, v21
Zvav 132,132,216
v_add_u32 v12, s80, v1
Ra 64,12,8192
Ra 68,12,9216
v_add_u32 v24, 61440, v22
Zvav 100,132,208
Ra 72,12,10240
Ra 76,12,11264
Ra 80,12,12288
v_add_u32 v25, 122880, v21
Zvav 68,132,200
Ra 84,12,13312
Ra 88,12,14336
Ra 92,12,15360
v_add_u32 v26, 122880, v22
Zvav 36,132,192
Ra 96,12,0
Ra 100,12,1024
Ra 104,12,2048
Zvav 40,136,192
Ra 108,12,3072
Ra 112,12,4096
X 172
Zvav 72,136,200
Ra 116,12,5120
Ra 120,12,6144
X 173
Zvav 104,136,208
Ra 124,12,7168
X 174
K 220,172,173
Zvav 136,136,216
X 175
Zvav 140,140,216
X 168
K 221,174,175
Zvav 108,140,208
X 169
X 170
Zvav 76,140,200
s_waitcnt vmcnt(0) lgkmcnt(0)
s_barrier
v_xor_b32 v242, 0x10000, v242
ds_read_b128 v[248:251], v8
Zvav 44,140,192
s_add_u32 s80, s71, 128
v_mov_b64 v[28:29], s[12:13]
s_mov_b64 exec, 1
buffer_store_dwordx2 v[28:29], off, s[8:11], s80 sc1
s_mov_b64 exec, -1
Ra 128,242,0
K 212,168,169
X 171
Zvav 48,144,192
X 164
K 213,170,171
Zvav 80,144,200
X 165
X 166
Zvav 112,144,208
Ra 132,242,1024
K 204,164,165
X 167
Zvav 144,144,216
X 160
Zvav 148,148,216
K 205,166,167
X 161
Zvav 116,148,208
Ra 136,242,2048
X 162
Zvav 84,148,200
W 3
K 196,160,161
X 163
Zvav 52,148,192
X 176
K 197,162,163
Zvav 56,152,192
Ra 140,242,3072
X 177
Zvav 88,152,200
X 178
K 198,176,177
Zvav 120,152,208
X 179
X 180
Zvav 152,152,216
K 199,178,179
Zvav 156,156,216
X 181
X 182
Zvav 124,156,208
K 206,180,181
X 183
Zvav 92,156,200
K 207,182,183
Zvav 60,156,192
.Ll0t_46%=:
s_cmp_eq_u32 s64, s65
s_cbranch_scc1 .Ll1t_35%=
.Ll0_33%=:
W 0
Cvaa 160,128,0,224
Ra 144,242,4096
Ra 148,242,5120
Mvaa 160,132,4
s_xor_b32 s62, s62, 0x10000
Ra 152,242,6144
Cvaa 164,132,20,228
X 184
Mvaa 164,128,16
X 185
Cvaa 168,128,32,232
X 186
Mvaa 168,132,36
Ra 156,242,7168
K 214,184,185
Cvaa 172,132,52,236
X 187
Mvaa 172,128,48
X 188
Mvaa 160,136,8
Ra 128,242,16384
Mvaa 160,140,12
K 215,186,187
Mvaa 164,140,28
X 189
Mvaa 164,136,24
X 190
Mvaa 168,136,40
Ra 132,242,17408
Mvaa 168,140,44
K 222,188,189
Mvaa 172,140,60
X 191
Mvaa 172,136,56
K 223,190,191
v_smfmac_f32_16x16x64_bf16 a[160:163], a[176:179], v[192:199], v240
Ra 136,242,18432
v_smfmac_f32_16x16x64_bf16 a[164:167], a[176:179], v[200:207], v240
X 160
v_smfmac_f32_16x16x64_bf16 a[168:171], a[176:179], v[208:215], v240
X 161
v_smfmac_f32_16x16x64_bf16 a[172:175], a[176:179], v[216:223], v240
W 3
X 162
Cvaa 176,144,0,224
Ra 140,242,19456
Mvaa 176,148,4
K 192,160,161
Cvaa 180,148,20,228
X 163
Mvaa 180,144,16
X 164
Cvaa 184,144,32,232
Mvaa 184,148,36
K 193,162,163
Cvaa 188,148,52,236
X 165
Mvaa 188,144,48
X 166
Mvaa 176,152,8
Ra 144,242,20480
Ra 148,242,21504
Mvaa 176,156,12
K 200,164,165
Mvaa 180,156,28
X 167
Mvaa 180,152,24
X 168
Mvaa 184,152,40
Mvaa 184,156,44
K 201,166,167
Mvaa 188,156,60
X 169
Mvaa 188,152,56
W 2
Ra 152,242,22528
Mvav 32,128,196
X 170
Mvav 64,128,204
Ra 156,242,23552
K 208,168,169
Mvav 96,128,212
X 171
Mvav 128,128,220
Mvav 132,132,220
X 172
Mvav 100,132,212
s_add_u32 s64, s64, 1
Ra 128,242,8192
K 209,170,171
)"
R"(Mvav 68,132,204
X 173
Mvav 36,132,196
Mvav 40,136,196
X 174
Mvav 72,136,204
Ra 132,242,9216
K 216,172,173
Mvav 104,136,212
X 175
Mvav 136,136,220
Mvav 140,140,220
X 176
Mvav 108,140,212
Ra 136,242,10240
K 217,174,175
Mvav 76,140,204
X 177
Mvav 44,140,196
W 3
Mvav 48,144,196
X 178
Mvav 80,144,204
Ra 140,242,11264
Mvav 112,144,212
K 194,176,177
Mvav 144,144,220
X 179
Mvav 148,148,220
X 180
Mvav 116,148,212
Ra 144,242,12288
Mvav 84,148,204
K 195,178,179
Mvav 52,148,196
X 181
Mvav 56,152,196
X 182
Mvav 88,152,204
Ra 148,242,13312
Mvav 120,152,212
K 202,180,181
Mvav 152,152,220
X 183
Mvav 156,156,220
X 184
Mvav 124,156,212
Ra 152,242,14336
Mvav 92,156,204
K 203,182,183
Mvav 60,156,196
W 3
X 185
Cvaa 160,128,0,224
X 186
Mvaa 160,132,4
Ra 156,242,15360
Cvaa 164,132,20,228
K 210,184,185
Mvaa 164,128,16
X 187
Cvaa 168,128,32,232
X 188
Mvaa 168,132,36
Cvaa 172,132,52,236
K 211,186,187
Mvaa 172,128,48
X 189
Mvaa 160,136,8
Ra 128,242,24576
Ra 132,242,25600
Mvaa 160,140,12
X 190
Mvaa 164,140,28
K 218,188,189
Mvaa 164,136,24
X 191
Mvaa 168,136,40
Mvaa 168,140,44
Mvaa 172,140,60
X 160
Mvaa 172,136,56
W 2
Ra 136,242,26624
K 219,190,191
Cvaa 176,144,0,224
X 161
Mvaa 176,148,4
Ra 140,242,27648
Cvaa 180,148,20,228
X 162
Mvaa 180,144,16
K 196,160,161
Cvaa 184,144,32,232
X 163
Mvaa 184,148,36
X 164
Cvaa 188,148,52,236
K 197,162,163
Mvaa 188,144,48
X 165
Mvaa 176,152,8
Ra 144,242,28672
Ra 148,242,29696
Mvaa 176,156,12
X 166
Mvaa 180,156,28
K 204,164,165
Mvaa 180,152,24
X 167
Mvaa 184,152,40
Mvaa 184,156,44
X 168
Mvaa 188,156,60
Mvaa 188,152,56
W 2
Ra 152,242,30720
K 205,166,167
Mvav 32,128,192
X 169
Mvav 64,128,200
Ra 156,242,31744
Mvav 96,128,208
X 170
Mvav 128,128,216
K 212,168,169
Mvav 132,132,216
X 171
Mvav 100,132,208
X 172
Mvav 68,132,200
Mvav 36,132,192
K 213,170,171
Mvav 40,136,192
X 173
Mvav 72,136,200
X 174
Mvav 104,136,208
Mvav 136,136,216
K 220,172,173
Mvav 140,140,216
X 175
Mvav 108,140,208
X 176
Mvav 76,140,200
W 0
s_barrier
v_xor_b32 v242, 0x10000, v242
Mvav 44,140,192
Ra 128,242,0
K 221,174,175
Mvav 48,144,192
X 177
Mvav 80,144,200
X 178
Mvav 112,144,208
Ra 132,242,1024
Mvav 144,144,216
K 198,176,177
Mvav 148,148,216
X 179
Mvav 116,148,208
X 180
Mvav 84,148,200
Ra 136,242,2048
Mvav 52,148,192
K 199,178,179
Mvav 56,152,192
X 181
Mvav 88,152,200
Ra 140,242,3072
Mvav 120,152,208
X 182
Mvav 152,152,216
K 206,180,181
Mvav 156,156,216
X 183
Mvav 124,156,208
Mvav 92,156,200
K 207,182,183
Mvav 60,156,192
s_cmp_lg_u32 s64, s65
s_cbranch_scc1 .Ll0_33%=
s_cmp_lt_u32 s85, 54
s_cbranch_scc1 .Lmoe_44%=
.Ll1t_35%=:
s_cmp_eq_u32 s64, s66
s_cbranch_scc1 .Lsp1_36%=
.Ll1_34%=:
W 0
Cvaa 160,128,0,224
Ra 144,242,4096
Ra 148,242,5120
Mvaa 160,132,4
s_add_u32 m0, s62, 0
Ra 152,242,6144
Cvaa 164,132,20,228
X 184
Mvaa 164,128,16
X 185
Cvaa 168,128,32,232
X 186
Mvaa 168,132,36
Ra 156,242,7168
K 214,184,185
Cvaa 172,132,52,236
Dc 1,8,61,0
Mvaa 172,128,48
X 187
Mvaa 160,136,8
X 188
Mvaa 160,140,12
Ra 128,242,16384
K 215,186,187
Mvaa 164,140,28
Dc 1,8,61,1024
Mvaa 164,136,24
X 189
Mvaa 168,136,40
X 190
Mvaa 168,140,44
Ra 132,242,17408
K 222,188,189
Mvaa 172,140,60
Dc 1,8,61,2048
Mvaa 172,136,56
X 191
v_smfmac_f32_16x16x64_bf16 a[160:163], a[176:179], v[192:199], v240
Ra 136,242,18432
K 223,190,191
v_smfmac_f32_16x16x64_bf16 a[164:167], a[176:179], v[200:207], v240
X 160
v_smfmac_f32_16x16x64_bf16 a[168:171], a[176:179], v[208:215], v240
Dc 1,8,61,3072
v_smfmac_f32_16x16x64_bf16 a[172:175], a[176:179], v[216:223], v240
W 3
X 161
Cvaa 176,144,0,224
s_add_u32 m0, s62, 4096
X 162
Mvaa 176,148,4
Ra 140,242,19456
K 192,160,161
Cvaa 180,148,20,228
X 163
Mvaa 180,144,16
Dc 243,8,61,0
Cvaa 184,144,32,232
X 164
Mvaa 184,148,36
K 193,162,163
Cvaa 188,148,52,236
X 165
Mvaa 188,144,48
Dc 243,8,61,1024
Ra 144,242,20480
Mvaa 176,152,8
X 166
Mvaa 176,156,12
Ra 148,242,21504
K 200,164,165
Mvaa 180,156,28
X 167
Mvaa 180,152,24
Dc 243,8,61,2048
Mvaa 184,152,40
X 168
Mvaa 184,156,44
K 201,166,167
Mvaa 188,156,60
X 169
Mvaa 188,152,56
W 2
Dc 243,8,61,3072
Ra 152,242,22528
Mvav 32,128,196
s_add_u32 m0, s62, 8192
X 170
Mvav 64,128,204
Ra 156,242,23552
K 208,168,169
Mvav 96,128,212
X 171
Mvav 128,128,220
Dc 244,8,61,0
Mvav 132,132,220
X 172
Mvav 100,132,212
Ra 128,242,8192
K 209,170,171
Mvav 68,132,204
X 173
Mvav 36,132,196
Dc 244,8,61,1024
Mvav 40,136,196
X 174
Mvav 72,136,204
Ra 132,242,9216
K 216,172,173
Mvav 104,136,212
X 175
Mvav 136,136,220
Dc 244,8,61,2048
Mvav 140,140,220
X 176
Mvav 108,140,212
Ra 136,242,10240
K 217,174,175
Mvav 76,140,204
X 177
Mvav 44,140,196
W 3
Dc 244,8,61,3072
Mvav 48,144,196
X 178
Mvav 80,144,204
s_add_u32 m0, s62, 12288
Ra 140,242,11264
Mvav 112,144,212
K 194,176,177
Mvav 144,144,220
X 179
Mvav 148,148,220
X 180
Mvav 116,148,212
Dc 245,8,61,0
Ra 144,242,12288
Mvav 84,148,204
K 195,178,179
Mvav 52,148,196
X 181
Mvav 56,152,196
X 182
Mvav 88,152,204
Dc 245,8,61,1024
Ra 148,242,13312
Mvav 120,152,212
K 202,180,181
Mvav 152,152,220
X 183
Mvav 156,156,220
X 184
Mvav 124,156,212
Dc 245,8,61,2048
Ra 152,242,14336
Mvav 92,156,204
K 203,182,183
Mvav 60,156,196
W 3
X 185
Cvaa 160,128,0,224
X 186
Mvaa 160,132,4
Dc 245,8,61,3072
Ra 156,242,15360
Cvaa 164,132,20,228
s_add_u32 s61, s61, s63
K 210,184,185
Mvaa 164,128,16
X 187
Cvaa 168,128,32,232
X 188
Mvaa 168,132,36
Cvaa 172,132,52,236
s_xor_b32 s62, s62, 0x10000
K 211,186,187
Mvaa 172,128,48
X 189
Mvaa 160,136,8
Ra 128,242,24576
Ra 132,242,25600
Mvaa 160,140,12
s_add_u32 s64, s64, 1
X 190
Mvaa 164,140,28
K 218,188,189
Mvaa 164,136,24
X 191
Mvaa 168,136,40
Mvaa 168,140,44
X 160
Mvaa 172,140,60
K 219,190,191
Mvaa 172,136,56
W 2
X 161
Cvaa 176,144,0,224
Ra 136,242,26624
Ra 140,242,27648
Mvaa 176,148,4
X 162
Cvaa 180,148,20,228
K 196,160,161
Mvaa 180,144,16
X 163
Cvaa 184,144,32,232
Mvaa 184,148,36
X 164
Cvaa 188,148,52,236
K 197,162,163
)"
R"(Mvaa 188,144,48
X 165
Mvaa 176,152,8
Ra 144,242,28672
Ra 148,242,29696
Mvaa 176,156,12
X 166
Mvaa 180,156,28
K 204,164,165
Mvaa 180,152,24
X 167
Mvaa 184,152,40
Mvaa 184,156,44
X 168
Mvaa 188,156,60
Mvaa 188,152,56
W 2
Ra 152,242,30720
K 205,166,167
Mvav 32,128,192
X 169
Mvav 64,128,200
Ra 156,242,31744
Mvav 96,128,208
X 170
Mvav 128,128,216
K 212,168,169
Mvav 132,132,216
X 171
Mvav 100,132,208
X 172
Mvav 68,132,200
Mvav 36,132,192
K 213,170,171
Mvav 40,136,192
X 173
Mvav 72,136,200
X 174
Mvav 104,136,208
Mvav 136,136,216
K 220,172,173
Mvav 140,140,216
X 175
Mvav 108,140,208
X 176
Mvav 76,140,200
s_waitcnt vmcnt(0) lgkmcnt(0)
s_barrier
v_xor_b32 v242, 0x10000, v242
Mvav 44,140,192
Ra 128,242,0
K 221,174,175
Mvav 48,144,192
X 177
Mvav 80,144,200
X 178
Mvav 112,144,208
Ra 132,242,1024
Mvav 144,144,216
K 198,176,177
Mvav 148,148,216
X 179
Mvav 116,148,208
X 180
Mvav 84,148,200
Ra 136,242,2048
Mvav 52,148,192
K 199,178,179
Mvav 56,152,192
X 181
Mvav 88,152,200
Ra 140,242,3072
Mvav 120,152,208
X 182
Mvav 152,152,216
K 206,180,181
Mvav 156,156,216
X 183
Mvav 124,156,208
Mvav 92,156,200
K 207,182,183
Mvav 60,156,192
s_cmp_lg_u32 s64, s66
s_cbranch_scc1 .Ll1_34%=
s_cmp_ge_u32 s14, 2
s_cbranch_scc1 .Lf1r_47%=
.Lsp1_36%=:
W 0
Cvaa 160,128,0,224
Ra 144,242,4096
Ra 148,242,5120
Mvaa 160,132,4
s_add_u32 m0, s62, 0
Ra 152,242,6144
Cvaa 164,132,20,228
X 184
Mvaa 164,128,16
X 185
Cvaa 168,128,32,232
X 186
Mvaa 168,132,36
Ra 156,242,7168
K 214,184,185
Cvaa 172,132,52,236
Dc 1,8,61,0
Mvaa 172,128,48
X 187
Mvaa 160,136,8
X 188
Mvaa 160,140,12
Ra 128,242,16384
K 215,186,187
Mvaa 164,140,28
Dc 1,8,61,1024
Ra 132,242,17408
Mvaa 164,136,24
X 189
Mvaa 168,136,40
X 190
Mvaa 168,140,44
Dc 1,8,61,2048
K 222,188,189
Mvaa 172,140,60
X 191
Mvaa 172,136,56
Ra 136,242,18432
K 223,190,191
v_smfmac_f32_16x16x64_bf16 a[160:163], a[176:179], v[192:199], v240
Dc 1,8,61,3072
Ra 140,242,19456
v_smfmac_f32_16x16x64_bf16 a[164:167], a[176:179], v[200:207], v240
s_add_u32 m0, s62, 4096
v_cndmask_b32_e64 v160, v241, v160, s[72:73]
v_cndmask_b32_e64 v161, v241, v161, s[74:75]
v_smfmac_f32_16x16x64_bf16 a[168:171], a[176:179], v[208:215], v240
v_cndmask_b32_e64 v162, v241, v162, s[76:77]
v_cndmask_b32_e64 v163, v241, v163, s[78:79]
v_smfmac_f32_16x16x64_bf16 a[172:175], a[176:179], v[216:223], v240
W 4
X 160
Cvaa 180,144,16,228
X 161
Mvaa 180,148,20
Dc 243,8,61,0
Cvaa 184,148,36,232
X 162
Mvaa 184,144,32
K 192,160,161
Cvaa 188,144,48,236
X 163
Mvaa 188,148,52
Dc 243,8,61,1024
Ra 144,242,20480
Mvaa 180,152,24
X 164
Mvaa 180,156,28
Ra 148,242,21504
K 193,162,163
Mvaa 184,156,44
Dc 243,8,61,2048
Mvaa 184,152,40
X 165
Mvaa 188,152,56
X 166
Mvaa 188,156,60
W 2
Dc 243,8,61,3072
K 200,164,165
Mvav 32,128,196
s_add_u32 m0, s62, 8192
X 167
Mvav 64,128,204
Ra 152,242,22528
Ra 156,242,23552
Mvav 96,128,212
X 168
Mvav 128,128,220
Dc 244,8,61,0
K 201,166,167
Mvav 132,132,220
X 169
Mvav 100,132,212
X 170
Mvav 68,132,204
Ra 128,242,8192
K 208,168,169
Mvav 36,132,196
X 171
Mvav 40,136,196
Dc 244,8,61,1024
Ra 132,242,9216
Mvav 72,136,204
X 172
Mvav 104,136,212
K 209,170,171
Mvav 136,136,220
X 173
Mvav 140,140,220
Dc 244,8,61,2048
Ra 136,242,10240
Mvav 108,140,212
X 174
Mvav 76,140,204
K 216,172,173
Mvav 44,140,196
W 3
Dc 244,8,61,3072
Ra 140,242,11264
Mvav 48,144,196
s_add_u32 m0, s62, 12288
X 175
Mvav 80,144,204
v_mov_b32 v194, 0
K 217,174,175
Mvav 112,144,212
v_mov_b32 v195, 0
v_cndmask_b32_e64 v180, v241, v180, s[72:73]
Mvav 144,144,220
v_cndmask_b32_e64 v181, v241, v181, s[74:75]
v_cndmask_b32_e64 v182, v241, v182, s[76:77]
Mvav 148,148,220
Dc 245,8,61,0
v_cndmask_b32_e64 v183, v241, v183, s[78:79]
Mvav 116,148,212
X 180
Mvav 84,148,204
Ra 144,242,12288
Mvav 52,148,196
X 181
Mvav 56,152,196
Dc 245,8,61,1024
Ra 148,242,13312
Mvav 88,152,204
X 182
Mvav 120,152,212
K 202,180,181
Mvav 152,152,220
X 183
Mvav 156,156,220
Dc 245,8,61,2048
Ra 152,242,14336
Mvav 124,156,212
X 184
Mvav 92,156,204
K 203,182,183
Mvav 60,156,196
W 3
Dc 245,8,61,3072
Ra 156,242,15360
Cvaa 168,128,32,232
s_add_u32 s61, s61, s63
X 185
Mvaa 168,132,36
s_xor_b32 s62, s62, 0x10000
X 186
Cvaa 172,132,52,236
K 210,184,185
Mvaa 172,128,48
s_add_u32 s64, s64, 1
X 187
Mvaa 168,136,40
X 188
Mvaa 168,140,44
Ra 128,242,24576
K 211,186,187
Mvaa 172,140,60
Ra 132,242,25600
Mvaa 172,136,56
Ra 136,242,26624
Ra 140,242,27648
X 189
X 190
K 218,188,189
X 191
v_mov_b32 v196, 0
K 219,190,191
W 4
Cvaa 188,144,48,236
Ra 144,242,28672
Mvaa 188,148,52
v_mov_b32 v197, 0
v_mov_b32 v204, 0
Mvaa 188,152,56
Ra 148,242,29696
v_mov_b32 v205, 0
Mvaa 188,156,60
W 2
Ra 152,242,30720
v_cndmask_b32_e64 v168, v241, v168, s[72:73]
Mvav 32,128,192
v_cndmask_b32_e64 v169, v241, v169, s[74:75]
v_cndmask_b32_e64 v170, v241, v170, s[76:77]
Mvav 64,128,200
Ra 156,242,31744
v_cndmask_b32_e64 v171, v241, v171, s[78:79]
Mvav 96,128,208
X 168
Mvav 128,128,216
X 169
Mvav 132,132,216
X 170
Mvav 100,132,208
v_mov_b32 v224, v250
Mvav 68,132,200
K 212,168,169
v_mov_b32 v225, v250
Mvav 36,132,192
X 171
Mvav 40,136,192
v_mov_b32 v226, v250
Mvav 72,136,200
X 172
Mvav 104,136,208
v_mov_b32 v227, v250
K 213,170,171
Mvav 136,136,216
X 173
Mvav 140,140,216
v_mov_b32 v228, v251
v_mov_b32 v229, v251
Mvav 108,140,208
X 174
Mvav 76,140,200
K 220,172,173
v_mov_b32 v230, v251
Mvav 44,140,192
W 0
X 175
Mvav 48,144,192
v_mov_b32 v231, v251
v_mov_b32 v198, 0
Mvav 80,144,200
K 221,174,175
v_mov_b32 v232, v248
Mvav 112,144,208
v_mov_b32 v199, 0
v_mov_b32 v206, 0
Mvav 144,144,216
v_mov_b32 v233, v248
v_mov_b32 v207, 0
Mvav 148,148,216
v_mov_b32 v214, 0
v_mov_b32 v234, v248
Mvav 116,148,208
v_mov_b32 v215, 0
v_cndmask_b32_e64 v188, v241, v188, s[72:73]
Mvav 84,148,200
v_mov_b32 v235, v248
v_cndmask_b32_e64 v189, v241, v189, s[74:75]
Mvav 52,148,192
v_cndmask_b32_e64 v190, v241, v190, s[76:77]
v_mov_b32 v236, v249
Mvav 56,152,192
v_cndmask_b32_e64 v191, v241, v191, s[78:79]
v_mov_b32 v237, v249
Mvav 88,152,200
X 188
Mvav 120,152,208
X 189
Mvav 152,152,216
v_mov_b32 v238, v249
v_mov_b32 v239, v249
Mvav 156,156,216
X 190
Mvav 124,156,208
s_waitcnt vmcnt(0)
s_barrier
v_xor_b32 v242, 0x10000, v242
Mvav 92,156,200
Ra 128,242,40960
K 222,188,189
Mvav 60,156,192
Ra 132,242,41984
Ra 136,242,43008
Ra 140,242,44032
X 191
s_nop 0
K 223,190,191
s_cmp_eq_u32 s64, 15
s_cbranch_scc1 .Lfs_42%=
.Lsp2_37%=:
v_smfmac_f32_16x16x64_bf16 a[160:163], a[176:179], v[192:199], v240
s_add_u32 m0, s62, 0
Ra 144,242,45056
Ra 148,242,46080
Ra 152,242,47104
Ra 156,242,48128
v_smfmac_f32_16x16x64_bf16 a[164:167], a[176:179], v[200:207], v240
v_smfmac_f32_16x16x64_bf16 a[168:171], a[176:179], v[208:215], v240
v_smfmac_f32_16x16x64_bf16 a[172:175], a[176:179], v[216:223], v240
W 4
Dc 1,8,61,0
Cvaa 168,128,96,232
v_mov_b32 v192, 0
G 12,160
Mvaa 168,132,100
Dc 1,8,61,1024
G 4,161
A 12,12,4
v_cmp_nlt_f32_e64 s[86:87], v12, s90
)"
R"(v_mov_b32 v193, 0
Cvaa 172,132,116,236
v_rcp_f32 v12, v12
s_or_b64 s[88:89], s[88:89], s[86:87]
v_accvgpr_write_b32 a160, 0
v_mov_b32 v200, 0
v_accvgpr_write_b32 a161, 0
v_accvgpr_write_b32 a162, 0
Mvaa 172,128,112
Dc 1,8,61,2048
v_accvgpr_write_b32 a163, 0
G 13,164
v_mov_b32 v201, 0
G 5,165
A 13,13,5
Mvaa 168,136,104
Ra 128,242,16384
Ra 132,242,17408
v_cmp_nlt_f32_e64 s[86:87], v13, s90
v_rcp_f32 v13, v13
s_or_b64 s[88:89], s[88:89], s[86:87]
v_accvgpr_write_b32 a164, 0
Mvaa 168,140,108
Dc 1,8,61,3072
v_accvgpr_write_b32 a165, 0
v_accvgpr_write_b32 a166, 0
v_accvgpr_write_b32 a167, 0
U 32,32,12
U 33,33,12
Mvaa 172,140,124
U 34,34,12
U 35,35,12
U 36,36,12
U 37,37,12
U 38,38,12
U 39,39,12
Mvaa 172,136,120
s_add_u32 m0, s62, 4096
Ra 136,242,18432
Ra 140,242,19456
K 32,32,33
K 33,34,35
K 34,36,37
W 4
Cvaa 188,144,112,236
K 35,38,39
s_add_u32 s93, s67, 0
buffer_store_dwordx4 v[32:35], v1, s[8:11], s93 offen offset:0 sc1
U 40,40,12
U 41,41,12
U 42,42,12
U 43,43,12
Mvaa 188,148,116
Ra 144,242,20480
v_cndmask_b32_e64 v168, v241, v168, s[72:73]
v_cndmask_b32_e64 v169, v241, v169, s[74:75]
v_cndmask_b32_e64 v170, v241, v170, s[76:77]
v_cndmask_b32_e64 v171, v241, v171, s[78:79]
U 44,44,12
Mvaa 188,152,120
X 168
X 169
U 45,45,12
U 46,46,12
Mvaa 188,156,124
W 1
Ra 148,242,21504
Dc 243,8,61,0
U 47,47,12
K 40,40,41
K 41,42,43
K 42,44,45
Mvav 96,128,212
Ra 152,242,22528
Ra 156,242,23552
K 43,46,47
X 170
K 208,168,169
Mvav 128,128,220
buffer_store_dwordx4 v[40:43], v1, s[8:11], s93 offen offset:1024 sc1
U 48,48,12
U 49,49,12
U 50,50,12
U 51,51,12
U 52,52,12
Mvav 132,132,220
U 53,53,12
U 54,54,12
U 55,55,12
K 48,48,49
K 49,50,51
K 50,52,53
Mvav 100,132,212
Dc 243,8,61,1024
Ra 128,242,32768
Ra 132,242,33792
X 171
K 51,54,55
Mvav 104,136,212
X 172
K 209,170,171
X 173
Mvav 136,136,220
buffer_store_dwordx4 v[48:51], v1, s[8:11], s93 offen offset:2048 sc1
U 56,56,12
U 57,57,12
U 58,58,12
U 59,59,12
U 60,60,12
Mvav 140,140,220
U 61,61,12
U 62,62,12
U 63,63,12
K 56,56,57
K 57,58,59
K 58,60,61
Mvav 108,140,212
W 2
Dc 243,8,61,2048
Ra 136,242,34816
Ra 140,242,35840
K 59,62,63
X 174
Mvav 112,144,212
K 216,172,173
X 175
Mvav 144,144,220
buffer_store_dwordx4 v[56:59], v1, s[8:11], s93 offen offset:3072 sc1
Ra 144,242,36864
G 14,168
G 6,169
A 14,14,6
v_cmp_nlt_f32_e64 s[86:87], v14, s90
Mvav 148,148,220
v_rcp_f32 v14, v14
s_or_b64 s[88:89], s[88:89], s[86:87]
v_accvgpr_write_b32 a168, 0
v_accvgpr_write_b32 a169, 0
v_accvgpr_write_b32 a170, 0
v_accvgpr_write_b32 a171, 0
Mvav 116,148,212
Dc 243,8,61,3072
s_add_u32 m0, s62, 8192
G 15,172
G 7,173
A 15,15,7
v_cmp_nlt_f32_e64 s[86:87], v15, s90
v_mov_b32 v194, 0
Mvav 120,152,212
v_rcp_f32 v15, v15
s_or_b64 s[88:89], s[88:89], s[86:87]
v_accvgpr_write_b32 a172, 0
v_accvgpr_write_b32 a173, 0
v_accvgpr_write_b32 a174, 0
v_accvgpr_write_b32 a175, 0
Mvav 152,152,220
Dc 244,8,61,0
U 64,64,13
U 65,65,13
U 66,66,13
U 67,67,13
U 68,68,13
Mvav 156,156,220
U 69,69,13
U 70,70,13
U 71,71,13
K 64,64,65
K 65,66,67
K 66,68,69
Mvav 124,156,212
W 1
Dc 244,8,61,1024
Ra 148,242,37888
K 67,70,71
s_add_u32 s93, s67, 131072
K 217,174,175
v_mov_b32 v195, 0
Cvaa 160,128,64,224
Ra 152,242,38912
Ra 156,242,39936
v_mov_b32 v202, 0
v_mov_b32 v203, 0
v_mov_b32 v210, 0
v_mov_b32 v211, 0
Mvaa 160,132,68
buffer_store_dwordx4 v[64:67], v1, s[8:11], s93 offen offset:0 sc1
U 72,72,13
U 73,73,13
U 74,74,13
U 75,75,13
U 76,76,13
Cvaa 164,132,84,228
U 77,77,13
U 78,78,13
U 79,79,13
K 72,72,73
K 73,74,75
K 74,76,77
Mvaa 164,128,80
Dc 244,8,61,2048
K 75,78,79
v_cndmask_b32_e64 v188, v241, v188, s[72:73]
v_cndmask_b32_e64 v189, v241, v189, s[74:75]
v_cndmask_b32_e64 v190, v241, v190, s[76:77]
v_cndmask_b32_e64 v191, v241, v191, s[78:79]
Cvaa 168,128,96,232
X 188
X 189
Mvaa 168,132,100
buffer_store_dwordx4 v[72:75], v1, s[8:11], s93 offen offset:1024 sc1
U 80,80,13
U 81,81,13
U 82,82,13
U 83,83,13
U 84,84,13
Cvaa 172,132,116,236
U 85,85,13
U 86,86,13
U 87,87,13
K 80,80,81
K 81,82,83
K 82,84,85
Mvaa 172,128,112
Dc 244,8,61,3072
s_add_u32 m0, s62, 12288
Ra 128,242,49152
Ra 132,242,50176
K 83,86,87
X 190
Mvaa 160,136,72
K 218,188,189
X 191
Mvaa 160,140,76
buffer_store_dwordx4 v[80:83], v1, s[8:11], s93 offen offset:2048 sc1
U 88,88,13
U 89,89,13
U 90,90,13
U 91,91,13
U 92,92,13
Mvaa 164,140,92
U 93,93,13
U 94,94,13
U 95,95,13
K 88,88,89
K 89,90,91
K 90,92,93
Mvaa 164,136,88
K 91,94,95
buffer_store_dwordx4 v[88:91], v1, s[8:11], s93 offen offset:3072 sc1
U 96,96,14
U 97,97,14
U 98,98,14
U 99,99,14
Mvaa 168,136,104
U 100,100,14
U 101,101,14
U 102,102,14
U 103,103,14
K 96,96,97
K 97,98,99
Mvaa 168,140,108
Dc 245,8,61,0
K 98,100,101
K 99,102,103
s_add_u32 s93, s67, 262144
v_cndmask_b32_e64 v160, v241, v160, s[72:73]
K 219,190,191
v_cndmask_b32_e64 v161, v241, v161, s[74:75]
Mvaa 172,140,124
v_cndmask_b32_e64 v162, v241, v162, s[76:77]
v_cndmask_b32_e64 v163, v241, v163, s[78:79]
X 160
Mvaa 172,136,120
W 2
Dc 245,8,61,1024
Ra 136,242,51200
Ra 140,242,52224
X 161
Cvaa 180,144,80,228
Mvaa 180,148,84
buffer_store_dwordx4 v[96:99], v1, s[8:11], s93 offen offset:0 sc1
U 104,104,14
U 105,105,14
U 106,106,14
U 107,107,14
U 108,108,14
Cvaa 184,148,100,232
U 109,109,14
U 110,110,14
U 111,111,14
K 104,104,105
K 105,106,107
K 106,108,109
Mvaa 184,144,96
Dc 245,8,61,2048
K 107,110,111
X 162
K 196,160,161
Cvaa 188,144,112,236
Ra 144,242,53248
X 163
Mvaa 188,148,116
buffer_store_dwordx4 v[104:107], v1, s[8:11], s93 offen offset:1024 sc1
U 112,112,14
U 113,113,14
U 114,114,14
U 115,115,14
U 116,116,14
Mvaa 180,152,88
U 117,117,14
U 118,118,14
U 119,119,14
K 112,112,113
K 113,114,115
K 114,116,117
Mvaa 180,156,92
Dc 245,8,61,3072
s_add_u32 s61, s61, s63
s_xor_b32 s62, s62, 0x10000
s_add_u32 s64, s64, 1
K 115,118,119
X 164
Mvaa 184,156,108
Ra 148,242,54272
K 197,162,163
X 165
Mvaa 184,152,104
buffer_store_dwordx4 v[112:115], v1, s[8:11], s93 offen offset:2048 sc1
U 120,120,14
U 121,121,14
U 122,122,14
U 123,123,14
U 124,124,14
Mvaa 188,152,120
U 125,125,14
U 126,126,14
U 127,127,14
K 120,120,121
K 121,122,123
K 122,124,125
Mvaa 188,156,124
W 2
K 123,126,127
buffer_store_dwordx4 v[120:123], v1, s[8:11], s93 offen offset:3072 sc1
U 128,128,15
U 129,129,15
U 130,130,15
U 131,131,15
Zvav 32,128,192
U 132,132,15
U 133,133,15
U 134,134,15
U 135,135,15
K 128,128,129
K 129,130,131
Zvav 64,128,200
K 130,132,133
K 131,134,135
s_add_u32 s93, s67, 393216
buffer_store_dwordx4 v[128:131], v1, s[8:11], s93 offen offset:0 sc1
U 136,136,15
U 137,137,15
U 138,138,15
Zvav 96,128,208
U 139,139,15
U 140,140,15
U 141,141,15
U 142,142,15
U 143,143,15
K 136,136,137
Zvav 128,128,216
K 137,138,139
K 138,140,141
K 139,142,143
buffer_store_dwordx4 v[136:139], v1, s[8:11], s93 offen offset:1024 sc1
U 144,144,15
U 145,145,15
Zvav 132,132,216
U 146,146,15
U 147,147,15
U 148,148,15
U 149,149,15
U 150,150,15
U 151,151,15
Zvav 100,132,208
K 144,144,145
K 145,146,147
K 146,148,149
K 147,150,151
buffer_store_dwordx4 v[144:147], v1, s[8:11], s93 offen offset:2048 sc1
U 152,152,15
Zvav 68,132,200
U 153,153,15
U 154,154,15
U 155,155,15
U 156,156,15
U 157,157,15
U 158,158,15
Zvav 36,132,192
U 159,159,15
)"
R"(K 152,152,153
K 153,154,155
K 154,156,157
K 155,158,159
buffer_store_dwordx4 v[152:155], v1, s[8:11], s93 offen offset:3072 sc1
Zvav 40,136,192
Ra 152,242,55296
X 166
K 204,164,165
X 167
Zvav 72,136,200
Ra 156,242,56320
X 168
K 205,166,167
X 169
Zvav 104,136,208
X 170
K 212,168,169
X 171
Zvav 136,136,216
X 172
K 213,170,171
X 173
Zvav 140,140,216
Zvav 108,140,208
X 174
Zvav 76,140,200
s_waitcnt vmcnt(6) lgkmcnt(0)
s_barrier
v_xor_b32 v242, 0x10000, v242
Zvav 44,140,192
Ra 128,242,40960
K 220,172,173
X 175
Zvav 48,144,192
Zvav 80,144,200
v_mov_b32 v198, 0
Zvav 112,144,208
Ra 132,242,41984
K 221,174,175
Zvav 144,144,216
v_mov_b32 v199, 0
v_cndmask_b32_e64 v180, v241, v180, s[72:73]
Zvav 148,148,216
v_cndmask_b32_e64 v181, v241, v181, s[74:75]
Zvav 116,148,208
Ra 136,242,43008
v_cndmask_b32_e64 v182, v241, v182, s[76:77]
Zvav 84,148,200
v_cndmask_b32_e64 v183, v241, v183, s[78:79]
Zvav 52,148,192
X 180
Zvav 56,152,192
Ra 140,242,44032
X 181
Zvav 88,152,200
Zvav 120,152,208
X 182
Zvav 152,152,216
K 206,180,181
Zvav 156,156,216
X 183
Zvav 124,156,208
Zvav 92,156,200
s_waitcnt vmcnt(0)
s_and_b64 s[86:87], s[88:89], s[24:25]
s_cbranch_scc1 .LL_49%=
v_mov_b64 v[28:29], s[12:13]
s_mov_b64 exec, 1
buffer_store_dwordx2 v[28:29], off, s[8:11], s69 sc1
s_mov_b64 exec, -1
.LL_49%=:
K 207,182,183
Zvav 60,156,192
s_cmp_eq_u32 s64, 15
s_cbranch_scc1 .Lf2_41%=
.Ll2_38%=:
W 0
Cvaa 160,128,64,224
Ra 144,242,45056
Ra 148,242,46080
Mvaa 160,132,68
s_add_u32 m0, s62, 0
Ra 152,242,47104
Cvaa 164,132,84,228
X 184
Mvaa 164,128,80
X 185
Cvaa 168,128,96,232
X 186
Mvaa 168,132,100
Ra 156,242,48128
K 214,184,185
Cvaa 172,132,116,236
Dc 1,8,61,0
Mvaa 172,128,112
X 187
Mvaa 160,136,72
X 188
Mvaa 160,140,76
Ra 128,242,57344
K 215,186,187
Mvaa 164,140,92
Dc 1,8,61,1024
Mvaa 164,136,88
X 189
Mvaa 168,136,104
X 190
Mvaa 168,140,108
Ra 132,242,58368
K 222,188,189
Mvaa 172,140,124
Dc 1,8,61,2048
Mvaa 172,136,120
X 191
v_smfmac_f32_16x16x64_bf16 a[160:163], a[176:179], v[192:199], v240
Ra 136,242,59392
K 223,190,191
v_smfmac_f32_16x16x64_bf16 a[164:167], a[176:179], v[200:207], v240
X 160
v_smfmac_f32_16x16x64_bf16 a[168:171], a[176:179], v[208:215], v240
Dc 1,8,61,3072
v_smfmac_f32_16x16x64_bf16 a[172:175], a[176:179], v[216:223], v240
W 3
X 161
Cvaa 176,144,64,224
s_add_u32 m0, s62, 4096
X 162
Mvaa 176,148,68
Ra 140,242,60416
K 192,160,161
Cvaa 180,148,84,228
X 163
Mvaa 180,144,80
Dc 243,8,61,0
Cvaa 184,144,96,232
X 164
Mvaa 184,148,100
K 193,162,163
Cvaa 188,148,116,236
X 165
Mvaa 188,144,112
Dc 243,8,61,1024
Ra 144,242,61440
Mvaa 176,152,72
X 166
Mvaa 176,156,76
Ra 148,242,62464
K 200,164,165
Mvaa 180,156,92
X 167
Mvaa 180,152,88
Dc 243,8,61,2048
Mvaa 184,152,104
X 168
Mvaa 184,156,108
K 201,166,167
Mvaa 188,156,124
X 169
Mvaa 188,152,120
W 2
Dc 243,8,61,3072
Ra 152,242,63488
Mvav 32,128,196
s_add_u32 m0, s62, 8192
X 170
Mvav 64,128,204
Ra 156,242,64512
K 208,168,169
Mvav 96,128,212
X 171
Mvav 128,128,220
Dc 244,8,61,0
Mvav 132,132,220
X 172
Mvav 100,132,212
Ra 128,242,32768
K 209,170,171
Mvav 68,132,204
X 173
Mvav 36,132,196
Dc 244,8,61,1024
Mvav 40,136,196
X 174
Mvav 72,136,204
Ra 132,242,33792
K 216,172,173
Mvav 104,136,212
X 175
Mvav 136,136,220
Dc 244,8,61,2048
Mvav 140,140,220
X 176
Mvav 108,140,212
Ra 136,242,34816
K 217,174,175
Mvav 76,140,204
X 177
Mvav 44,140,196
W 3
Dc 244,8,61,3072
Mvav 48,144,196
X 178
Mvav 80,144,204
s_add_u32 m0, s62, 12288
Ra 140,242,35840
Mvav 112,144,212
K 194,176,177
Mvav 144,144,220
X 179
Mvav 148,148,220
X 180
Mvav 116,148,212
Dc 245,8,61,0
Ra 144,242,36864
Mvav 84,148,204
K 195,178,179
Mvav 52,148,196
X 181
Mvav 56,152,196
X 182
Mvav 88,152,204
Dc 245,8,61,1024
Ra 148,242,37888
Mvav 120,152,212
K 202,180,181
Mvav 152,152,220
X 183
Mvav 156,156,220
X 184
Mvav 124,156,212
Dc 245,8,61,2048
Ra 152,242,38912
Mvav 92,156,204
K 203,182,183
Mvav 60,156,196
W 3
X 185
Cvaa 160,128,64,224
X 186
Mvaa 160,132,68
Dc 245,8,61,3072
Ra 156,242,39936
Cvaa 164,132,84,228
s_add_u32 s61, s61, s63
K 210,184,185
Mvaa 164,128,80
X 187
Cvaa 168,128,96,232
X 188
Mvaa 168,132,100
Cvaa 172,132,116,236
s_xor_b32 s62, s62, 0x10000
K 211,186,187
Mvaa 172,128,112
X 189
Mvaa 160,136,72
Ra 128,242,49152
Ra 132,242,50176
Mvaa 160,140,76
s_add_u32 s64, s64, 1
X 190
Mvaa 164,140,92
K 218,188,189
Mvaa 164,136,88
X 191
Mvaa 168,136,104
Mvaa 168,140,108
X 160
Mvaa 172,140,124
K 219,190,191
Mvaa 172,136,120
W 2
X 161
Cvaa 176,144,64,224
Ra 136,242,51200
Ra 140,242,52224
Mvaa 176,148,68
X 162
Cvaa 180,148,84,228
K 196,160,161
Mvaa 180,144,80
X 163
Cvaa 184,144,96,232
Mvaa 184,148,100
X 164
Cvaa 188,148,116,236
K 197,162,163
Mvaa 188,144,112
X 165
Mvaa 176,152,72
Ra 144,242,53248
Ra 148,242,54272
Mvaa 176,156,76
X 166
Mvaa 180,156,92
K 204,164,165
Mvaa 180,152,88
X 167
Mvaa 184,152,104
Mvaa 184,156,108
X 168
Mvaa 188,156,124
Mvaa 188,152,120
W 2
Ra 152,242,55296
K 205,166,167
Mvav 32,128,192
X 169
Mvav 64,128,200
Ra 156,242,56320
Mvav 96,128,208
X 170
Mvav 128,128,216
K 212,168,169
Mvav 132,132,216
X 171
Mvav 100,132,208
X 172
Mvav 68,132,200
Mvav 36,132,192
K 213,170,171
Mvav 40,136,192
X 173
Mvav 72,136,200
X 174
Mvav 104,136,208
Mvav 136,136,216
K 220,172,173
Mvav 140,140,216
X 175
Mvav 108,140,208
X 176
Mvav 76,140,200
s_waitcnt vmcnt(0) lgkmcnt(0)
s_barrier
v_xor_b32 v242, 0x10000, v242
Mvav 44,140,192
Ra 128,242,40960
K 221,174,175
Mvav 48,144,192
X 177
Mvav 80,144,200
X 178
Mvav 112,144,208
Ra 132,242,41984
Mvav 144,144,216
K 198,176,177
Mvav 148,148,216
X 179
Mvav 116,148,208
X 180
Mvav 84,148,200
Ra 136,242,43008
Mvav 52,148,192
K 199,178,179
Mvav 56,152,192
X 181
Mvav 88,152,200
Ra 140,242,44032
Mvav 120,152,208
X 182
Mvav 152,152,216
K 206,180,181
Mvav 156,156,216
X 183
Mvav 124,156,208
Mvav 92,156,200
K 207,182,183
Mvav 60,156,192
s_cmp_lg_u32 s64, 15
s_cbranch_scc1 .Ll2_38%=
.Lf2_41%=:
W 0
Cvaa 160,128,64,224
v_bfe_u32 v28, v1, 4, 2
v_lshlrev_b32 v28, 3, v28
Mvaa 160,132,68
Ra 144,242,45056
Ra 148,242,46080
Cvaa 164,132,84,228
Ra 152,242,47104
buffer_load_dwordx2 v[28:29], v28, s[8:11], s54 offen sc1
Mvaa 164,128,80
s_lshl_b32 s92, s58, 2
s_add_u32 s91, s92, s51
s_add_u32 s95, s91, 3
Cvaa 168,128,96,232
s_and_b32 s95, s95, 15
s_mul_i32 s95, s95, 40960
s_mul_i32 s71, s51, 10240
Mvaa 168,132,100
s_add_u32 s71, s71, 0
s_add_u32 m0, s71, s48
s_lshl2_add_u32 s94, s48, s95
Cvaa 172,132,116,236
X 184
Mvaa 172,128,112
X 185
Mvaa 160,136,72
)"
R"(X 186
Mvaa 160,140,76
Ra 156,242,48128
K 214,184,185
Mvaa 164,140,92
X 187
Mvaa 164,136,88
D 16,40,94,0
Ra 128,242,57344
Mvaa 168,136,104
s_add_u32 s95, s91, 5
s_and_b32 s95, s95, 15
s_mul_i32 s95, s95, 40960
Mvaa 168,140,108
s_mul_i32 s71, s51, 10240
s_add_u32 s71, s71, 20480
Ra 132,242,58368
Mvaa 172,140,124
X 188
Mvaa 172,136,120
D 17,40,94,1024
K 215,186,187
v_smfmac_f32_16x16x64_bf16 a[160:163], a[176:179], v[192:199], v240
X 189
v_smfmac_f32_16x16x64_bf16 a[164:167], a[176:179], v[200:207], v240
X 190
K 222,188,189
X 191
s_nop 0
K 223,190,191
v_smfmac_f32_16x16x64_bf16 a[168:171], a[176:179], v[208:215], v240
D 18,40,94,2048
Ra 136,242,59392
v_smfmac_f32_16x16x64_bf16 a[172:175], a[176:179], v[216:223], v240
W 3
X 160
Cvaa 176,144,64,224
X 161
Mvaa 176,148,68
X 162
Cvaa 180,148,84,228
D 19,40,94,3072
K 192,160,161
Mvaa 180,144,80
s_add_u32 m0, m0, 4096
X 163
Cvaa 184,144,96,232
X 164
Mvaa 184,148,100
Ra 140,242,60416
K 193,162,163
Cvaa 188,148,116,236
buffer_load_dwordx4 v20, s[40:43], s94 offen lds
Mvaa 188,144,112
s_add_u32 m0, s71, s48
s_lshl2_add_u32 s94, s48, s95
Ra 144,242,61440
Mvaa 176,152,72
X 165
Mvaa 176,156,76
X 166
Mvaa 180,156,92
D 16,40,94,0
K 200,164,165
Mvaa 180,152,88
X 167
Mvaa 184,152,104
X 172
Mvaa 184,156,108
D 17,40,94,1024
K 201,166,167
Mvaa 188,156,124
X 173
Mvaa 188,152,120
W 1
Ra 148,242,62464
Ra 152,242,63488
Mvav 32,128,196
X 174
Mvav 64,128,204
D 18,40,94,2048
K 216,172,173
Mvav 96,128,212
X 175
Mvav 128,128,220
X 168
Mvav 132,132,220
s_mov_b32 s18, 40960
D 19,40,94,3072
K 217,174,175
Mvav 100,132,212
s_add_u32 m0, m0, 4096
X 169
Mvav 68,132,204
s_mov_b32 s19, 73728
X 170
Mvav 36,132,196
s_bitcmp1_b32 s14, 0
s_cselect_b32 s18, 49152, s18
s_cselect_b32 s19, 98304, s19
Ra 156,242,64512
K 208,168,169
Mvav 40,136,196
s_cmp_lt_u32 s14, 2
s_cselect_b32 s20, s18, s19
s_cselect_b32 s21, s19, s18
X 171
Mvav 72,136,204
X 184
Mvav 104,136,212
buffer_load_dwordx4 v20, s[40:43], s94 offen lds
K 209,170,171
Mvav 136,136,220
s_waitcnt vmcnt(10)
v_cmp_eq_u64 vcc, s[12:13], v[28:29]
s_cmp_eq_u64 vcc, -1
s_cselect_b32 s46, 79691776, 0
s_add_u32 s81, s58, 0
s_and_b32 s81, s81, 3
Mvav 140,140,220
s_lshl_b32 s81, s81, 12
s_add_u32 s81, s81, s49
Ra 128,242,49152
Mvav 108,140,212
X 185
Mvav 76,140,204
X 186
Mvav 44,140,196
W 1
buffer_load_dwordx4 a[184:187], v1, s[44:47], s81 offen offset:0 sc1
K 210,184,185
Mvav 48,144,196
s_add_u32 s82, s81, 131072
X 187
Mvav 80,144,204
v_add_u32 v2, s20, v1
v_add_u32 v3, s21, v1
Mvav 112,144,212
s_lshl_b32 s18, s14, 9
X 188
Mvav 144,144,220
Ra 132,242,50176
K 211,186,187
Mvav 148,148,220
buffer_load_dwordx4 a[200:203], v1, s[44:47], s82 offen offset:0 sc1
Ra 136,242,51200
Mvav 116,148,212
X 189
Mvav 84,148,204
v_lshrrev_b32 v232, 1, v1
v_add_u32 v232, s18, v232
Mvav 52,148,196
X 190
Mvav 56,152,196
K 218,188,189
v_bfe_u32 v12, v1, 6, 2
Mvav 88,152,204
buffer_load_dwordx4 a[188:191], v1, s[44:47], s81 offen offset:1024 sc1
Ra 140,242,52224
Mvav 120,152,212
X 191
Mvav 152,152,220
v_lshrrev_b32 v13, 8, v1
v_bfe_u32 v14, v1, 4, 1
Mvav 156,156,220
X 176
Mvav 124,156,212
K 219,190,191
v_lshlrev_b32 v14, 4, v14
Mvav 92,156,204
X 177
Mvav 60,156,196
buffer_load_dwordx4 a[204:207], v1, s[44:47], s82 offen offset:1024 sc1
v_mov_b32 v15, 0x3f80
Maav 168,180,208
X 178
K 194,176,177
X 179
X 180
K 195,178,179
Maav 172,180,216
X 181
X 182
K 202,180,181
X 183
s_nop 0
K 203,182,183
Maav 160,180,192
Ra 144,242,53248
v_lshlrev_b32 v15, v14, v15
Maav 164,180,200
W 1
buffer_load_dwordx4 a[192:195], v1, s[44:47], s81 offen offset:2048 sc1
v_bfe_u32 v14, v1, 5, 1
Mvav 96,128,208
Ra 148,242,54272
v_xor_b32 v27, v12, v13
v_lshl_or_b32 v27, v27, 1, v14
Mvav 128,128,216
v_cmp_eq_u32_e64 s[86:87], 0, v27
v_mov_b32 v226, 0
s_nop 0
v_cndmask_b32_e64 v224, 0, v15, s[86:87]
Mvav 132,132,216
Ra 152,242,55296
Ra 156,242,56320
buffer_load_dwordx4 a[208:211], v1, s[44:47], s82 offen offset:2048 sc1
Mvav 100,132,208
v_mov_b32 v228, 0
v_mov_b32 v230, v224
v_cmp_eq_u32_e64 s[86:87], 1, v27
Mvav 104,136,208
v_mov_b32 v227, 0
Mvav 136,136,216
buffer_load_dwordx4 a[196:199], v1, s[44:47], s81 offen offset:3072 sc1
v_cndmask_b32_e64 v225, 0, v15, s[86:87]
v_mov_b32 v229, 0
Mvav 140,140,216
v_mov_b32 v231, v225
G 4,160
G 8,161
Mvav 108,140,208
W 0
G 5,164
G 9,165
G 6,168
Mvav 112,144,208
buffer_load_dwordx4 a[212:215], v1, s[44:47], s82 offen offset:3072 sc1
s_add_u32 s81, s58, 1
s_and_b32 s81, s81, 3
s_lshl_b32 s81, s81, 12
Mvav 144,144,216
s_add_u32 s81, s81, s49
G 10,169
G 7,172
G 11,173
Mvav 148,148,216
A 4,4,8
A 5,5,9
A 6,6,10
Mvav 116,148,208
buffer_load_dwordx4 a[216:219], v1, s[44:47], s81 offen offset:0 sc1
s_add_u32 s82, s81, 131072
A 7,7,11
ds_write_b64 v232, v[6:7] offset:57344
Mvav 120,152,208
A 8,4,5
A 9,6,7
A 8,8,9
Mvav 152,152,216
v_cmp_ngt_f32_e64 s[86:87], s90, v8
s_lshl_b32 s19, s14, 2
v_mov_b32 v9, s19
s_or_b64 s[88:89], s[88:89], s[86:87]
Mvav 156,156,216
s_and_b64 s[88:89], s[88:89], s[24:25]
s_cselect_b32 s18, 1, 0
buffer_load_dwordx4 a[232:235], v1, s[44:47], s82 offen offset:0 sc1
v_mov_b32 v8, s18
ds_write_b32 v9, v8 offset:59392
Mvav 124,156,208
v_xor_b32 v10, 1024, v232
v_bfe_u32 v11, v1, 2, 4
K 96,96,97
Mvav 32,128,192
K 97,98,99
K 98,100,101
K 99,102,103
Mvav 64,128,200
ds_write_b128 v2, v[96:99] offset:0
K 128,128,129
K 129,130,131
Mvav 68,132,200
K 130,132,133
K 131,134,135
ds_write_b128 v2, v[128:131] offset:4096
Mvav 36,132,192
buffer_load_dwordx4 a[220:223], v1, s[44:47], s81 offen offset:1024 sc1
K 104,104,105
K 105,106,107
Mvav 40,136,192
K 106,108,109
K 107,110,111
ds_write_b128 v2, v[104:107] offset:1024
Mvav 72,136,200
K 136,136,137
K 137,138,139
K 138,140,141
Mvav 76,140,200
buffer_load_dwordx4 a[236:239], v1, s[44:47], s82 offen offset:1024 sc1
K 139,142,143
ds_write_b128 v2, v[136:139] offset:5120
Mvav 44,140,192
K 112,112,113
K 113,114,115
K 114,116,117
Mvav 48,144,192
K 115,118,119
ds_write_b128 v2, v[112:115] offset:2048
K 144,144,145
Mvav 80,144,200
K 145,146,147
K 146,148,149
K 147,150,151
Mvav 84,148,200
ds_write_b128 v2, v[144:147] offset:6144
buffer_load_dwordx4 a[224:227], v1, s[44:47], s81 offen offset:2048 sc1
K 120,120,121
Mvav 52,148,192
K 121,122,123
K 122,124,125
K 123,126,127
Mvav 56,152,192
ds_write_b128 v2, v[120:123] offset:3072
K 152,152,153
K 153,154,155
Mvav 88,152,200
buffer_load_dwordx4 a[240:243], v1, s[44:47], s82 offen offset:2048 sc1
K 154,156,157
K 155,158,159
Mvav 92,156,200
ds_write_b128 v2, v[152:155] offset:7168
Mvav 60,156,192
buffer_load_dwordx4 a[228:231], v1, s[44:47], s81 offen offset:3072 sc1
buffer_load_dwordx4 a[244:247], v1, s[44:47], s82 offen offset:3072 sc1
s_branch .Lend_43%=
.p2align 12
.Lfs_42%=:
v_smfmac_f32_16x16x64_bf16 a[160:163], a[176:179], v[192:199], v240
s_mov_b32 s46, 0
s_lshl_b32 s92, s58, 2
s_add_u32 s91, s92, s51
s_add_u32 s95, s91, 3
s_and_b32 s95, s95, 15
s_mul_i32 s95, s95, 40960
s_mul_i32 s71, s51, 10240
v_smfmac_f32_16x16x64_bf16 a[164:167], a[176:179], v[200:207], v240
s_add_u32 s71, s71, 0
s_add_u32 m0, s71, s48
s_lshl2_add_u32 s94, s48, s95
Ra 144,242,45056
Ra 148,242,46080
Ra 152,242,47104
Ra 156,242,48128
v_smfmac_f32_16x16x64_bf16 a[168:171], a[176:179], v[208:215], v240
D 16,40,94,0
s_add_u32 s95, s91, 5
s_and_b32 s95, s95, 15
s_mul_i32 s95, s95, 40960
s_mul_i32 s71, s51, 10240
s_add_u32 s71, s71, 20480
v_smfmac_f32_16x16x64_bf16 a[172:175], a[176:179], v[216:223], v240
W 4
Cvaa 168,128,96,232
v_mov_b32 v192, 0
G 12,160
Mvaa 168,132,100
D 17,40,94,1024
G 4,161
A 12,12,4
v_cmp_nlt_f32_e64 s[86:87], v12, s90
v_rcp_f32 v12, v12
s_or_b64 s[88:89], s[88:89], s[86:87]
Cvaa 172,132,116,236
v_accvgpr_write_b32 a160, 0
v_accvgpr_write_b32 a161, 0
v_accvgpr_write_b32 a162, 0
v_accvgpr_write_b32 a163, 0
G 13,164
G 5,165
Mvaa 172,128,112
v_mov_b32 v193, 0
A 13,13,5
v_cmp_nlt_f32_e64 s[86:87], v13, s90
v_rcp_f32 v13, v13
s_or_b64 s[88:89], s[88:89], s[86:87]
v_accvgpr_write_b32 a164, 0
Mvaa 168,136,104
v_accvgpr_write_b32 a165, 0
v_accvgpr_write_b32 a166, 0
v_accvgpr_write_b32 a167, 0
U 32,32,12
U 33,33,12
U 34,34,12
Mvaa 168,140,108
v_mov_b32 v200, 0
U 35,35,12
U 36,36,12
U 37,37,12
U 38,38,12
U 39,39,12
Mvaa 172,140,124
Ra 128,242,16384
Ra 132,242,17408
K 32,32,33
K 33,34,35
K 34,36,37
K 35,38,39
s_add_u32 s93, s67, 0
Mvaa 172,136,120
)"
R"(Ra 136,242,18432
Ra 140,242,19456
D 18,40,94,2048
v_mov_b32 v201, 0
W 4
Cvaa 188,144,112,236
Ra 144,242,20480
v_cndmask_b32_e64 v168, v241, v168, s[72:73]
v_cndmask_b32_e64 v169, v241, v169, s[74:75]
v_cndmask_b32_e64 v170, v241, v170, s[76:77]
Mvaa 188,148,116
buffer_store_dwordx4 v[32:35], v1, s[8:11], s93 offen offset:0 sc1
U 40,40,12
U 41,41,12
U 42,42,12
U 43,43,12
U 44,44,12
Mvaa 188,152,120
U 45,45,12
U 46,46,12
U 47,47,12
K 40,40,41
K 41,42,43
K 42,44,45
Mvaa 188,156,124
W 1
K 43,46,47
buffer_store_dwordx4 v[40:43], v1, s[8:11], s93 offen offset:1024 sc1
U 48,48,12
U 49,49,12
U 50,50,12
U 51,51,12
Mvav 96,128,212
U 52,52,12
U 53,53,12
U 54,54,12
U 55,55,12
K 48,48,49
K 49,50,51
Mvav 128,128,220
K 50,52,53
K 51,54,55
buffer_store_dwordx4 v[48:51], v1, s[8:11], s93 offen offset:2048 sc1
U 56,56,12
U 57,57,12
U 58,58,12
Mvav 132,132,220
Ra 148,242,21504
Ra 152,242,22528
Ra 156,242,23552
U 59,59,12
U 60,60,12
U 61,61,12
Mvav 100,132,212
D 19,40,94,3072
s_add_u32 m0, m0, 4096
U 62,62,12
U 63,63,12
K 56,56,57
K 57,58,59
K 58,60,61
Mvav 104,136,212
K 59,62,63
v_cndmask_b32_e64 v171, v241, v171, s[78:79]
X 168
X 169
Mvav 136,136,220
buffer_store_dwordx4 v[56:59], v1, s[8:11], s93 offen offset:3072 sc1
Ra 128,242,49152
G 14,168
G 6,169
A 14,14,6
v_cmp_nlt_f32_e64 s[86:87], v14, s90
Mvav 140,140,220
v_rcp_f32 v14, v14
s_or_b64 s[88:89], s[88:89], s[86:87]
v_accvgpr_write_b32 a168, 0
v_accvgpr_write_b32 a169, 0
v_accvgpr_write_b32 a170, 0
v_accvgpr_write_b32 a171, 0
Mvav 108,140,212
W 1
G 15,172
G 7,173
A 15,15,7
v_cmp_nlt_f32_e64 s[86:87], v15, s90
v_rcp_f32 v15, v15
Mvav 112,144,212
s_or_b64 s[88:89], s[88:89], s[86:87]
buffer_load_dwordx4 v20, s[40:43], s94 offen lds
v_accvgpr_write_b32 a172, 0
v_accvgpr_write_b32 a173, 0
v_accvgpr_write_b32 a174, 0
v_accvgpr_write_b32 a175, 0
U 64,64,13
Mvav 144,144,220
s_add_u32 m0, s71, s48
s_lshl2_add_u32 s94, s48, s95
U 65,65,13
U 66,66,13
U 67,67,13
U 68,68,13
U 69,69,13
Mvav 148,148,220
U 70,70,13
U 71,71,13
K 64,64,65
K 65,66,67
K 66,68,69
K 67,70,71
s_add_u32 s93, s67, 131072
Mvav 116,148,212
buffer_store_dwordx4 v[64:67], v1, s[8:11], s93 offen offset:0 sc1
U 72,72,13
U 73,73,13
U 74,74,13
U 75,75,13
U 76,76,13
Mvav 120,152,212
Ra 132,242,50176
Ra 136,242,51200
Ra 140,242,52224
U 77,77,13
U 78,78,13
U 79,79,13
Mvav 152,152,220
K 72,72,73
K 73,74,75
K 74,76,77
K 75,78,79
buffer_store_dwordx4 v[72:75], v1, s[8:11], s93 offen offset:1024 sc1
U 80,80,13
Mvav 156,156,220
X 170
K 208,168,169
X 171
X 172
K 209,170,171
X 173
X 174
K 216,172,173
X 175
v_mov_b32 v194, 0
K 217,174,175
v_mov_b32 v195, 0
v_mov_b32 v202, 0
v_mov_b32 v203, 0
v_mov_b32 v210, 0
v_mov_b32 v211, 0
Mvav 124,156,212
v_cndmask_b32_e64 v188, v241, v188, s[72:73]
v_cndmask_b32_e64 v189, v241, v189, s[74:75]
v_cndmask_b32_e64 v190, v241, v190, s[76:77]
v_cndmask_b32_e64 v191, v241, v191, s[78:79]
X 188
X 189
X 190
K 218,188,189
X 191
s_nop 0
K 219,190,191
Maav 168,180,208
D 16,40,94,0
U 81,81,13
U 82,82,13
U 83,83,13
U 84,84,13
U 85,85,13
Maav 172,180,216
W 0
U 86,86,13
U 87,87,13
K 80,80,81
K 81,82,83
K 82,84,85
K 83,86,87
Zvav 32,128,192
buffer_store_dwordx4 v[80:83], v1, s[8:11], s93 offen offset:2048 sc1
U 88,88,13
U 89,89,13
U 90,90,13
U 91,91,13
U 92,92,13
Zvav 64,128,200
U 93,93,13
U 94,94,13
U 95,95,13
K 88,88,89
K 89,90,91
K 90,92,93
K 91,94,95
buffer_store_dwordx4 v[88:91], v1, s[8:11], s93 offen offset:3072 sc1
U 96,96,14
U 97,97,14
U 98,98,14
U 99,99,14
U 100,100,14
U 101,101,14
U 102,102,14
U 103,103,14
K 96,96,97
K 97,98,99
K 98,100,101
K 99,102,103
s_add_u32 s93, s67, 262144
buffer_store_dwordx4 v[96:99], v1, s[8:11], s93 offen offset:0 sc1
s_nop 1
Zvav 96,128,208
U 104,104,14
U 105,105,14
U 106,106,14
U 107,107,14
U 108,108,14
U 109,109,14
U 110,110,14
U 111,111,14
K 104,104,105
K 105,106,107
K 106,108,109
K 107,110,111
buffer_store_dwordx4 v[104:107], v1, s[8:11], s93 offen offset:1024 sc1
U 112,112,14
U 113,113,14
U 114,114,14
U 115,115,14
U 116,116,14
U 117,117,14
U 118,118,14
U 119,119,14
K 112,112,113
K 113,114,115
K 114,116,117
K 115,118,119
buffer_store_dwordx4 v[112:115], v1, s[8:11], s93 offen offset:2048 sc1
U 120,120,14
U 121,121,14
U 122,122,14
U 123,123,14
U 124,124,14
U 125,125,14
U 126,126,14
U 127,127,14
K 120,120,121
K 121,122,123
K 122,124,125
K 123,126,127
buffer_store_dwordx4 v[120:123], v1, s[8:11], s93 offen offset:3072 sc1
U 128,128,15
U 129,129,15
U 130,130,15
U 131,131,15
U 132,132,15
U 133,133,15
U 134,134,15
U 135,135,15
K 128,128,129
K 129,130,131
K 130,132,133
K 131,134,135
s_add_u32 s93, s67, 393216
buffer_store_dwordx4 v[128:131], v1, s[8:11], s93 offen offset:0 sc1
s_nop 1
Zvav 128,128,216
Ra 144,242,53248
U 136,136,15
U 137,137,15
U 138,138,15
U 139,139,15
U 140,140,15
Zvav 132,132,216
D 17,40,94,1024
U 141,141,15
U 142,142,15
U 143,143,15
K 136,136,137
K 137,138,139
Zvav 100,132,208
Ra 148,242,54272
Ra 152,242,55296
Ra 156,242,56320
K 138,140,141
K 139,142,143
Zvav 68,132,200
D 18,40,94,2048
Zvav 36,132,192
Zvav 40,136,192
D 19,40,94,3072
s_add_u32 m0, m0, 4096
Zvav 72,136,200
Zvav 104,136,208
buffer_store_dwordx4 v[136:139], v1, s[8:11], s93 offen offset:1024 sc1
U 144,144,15
U 145,145,15
U 146,146,15
U 147,147,15
U 148,148,15
Zvav 136,136,216
U 149,149,15
U 150,150,15
U 151,151,15
K 144,144,145
K 145,146,147
K 146,148,149
Zvav 140,140,216
K 147,150,151
buffer_store_dwordx4 v[144:147], v1, s[8:11], s93 offen offset:2048 sc1
U 152,152,15
U 153,153,15
U 154,154,15
U 155,155,15
Zvav 108,140,208
U 156,156,15
U 157,157,15
U 158,158,15
U 159,159,15
K 152,152,153
K 153,154,155
Zvav 76,140,200
buffer_load_dwordx4 v20, s[40:43], s94 offen lds
K 154,156,157
K 155,158,159
Zvav 44,140,192
W 0
Zvav 48,144,192
buffer_store_dwordx4 v[152:155], v1, s[8:11], s93 offen offset:3072 sc1
s_mov_b32 s18, 40960
s_mov_b32 s19, 73728
s_bitcmp1_b32 s14, 0
s_cselect_b32 s18, 49152, s18
s_cselect_b32 s19, 98304, s19
s_cmp_lt_u32 s14, 2
s_cselect_b32 s20, s18, s19
s_cselect_b32 s21, s19, s18
v_add_u32 v2, s20, v1
v_add_u32 v3, s21, v1
Zvav 80,144,200
s_lshl_b32 s18, s14, 9
v_lshrrev_b32 v232, 1, v1
v_add_u32 v232, s18, v232
v_bfe_u32 v12, v1, 6, 2
v_lshrrev_b32 v13, 8, v1
v_bfe_u32 v14, v1, 4, 1
v_lshlrev_b32 v14, 4, v14
Zvav 112,144,208
v_mov_b32 v15, 0x3f80
v_lshlrev_b32 v15, v14, v15
v_bfe_u32 v14, v1, 5, 1
v_xor_b32 v27, v12, v13
v_lshl_or_b32 v27, v27, 1, v14
v_cmp_eq_u32_e64 s[86:87], 0, v27
Zvav 144,144,216
v_mov_b32 v226, 0
Zvav 148,148,216
v_cndmask_b32_e64 v224, 0, v15, s[86:87]
v_mov_b32 v228, 0
v_mov_b32 v230, v224
v_cmp_eq_u32_e64 s[86:87], 1, v27
v_mov_b32 v227, 0
s_nop 0
v_cndmask_b32_e64 v225, 0, v15, s[86:87]
Zvav 116,148,208
v_mov_b32 v229, 0
v_mov_b32 v231, v225
G 4,160
G 8,161
G 5,164
G 9,165
Zvav 84,148,200
G 6,168
G 10,169
G 7,172
G 11,173
A 4,4,8
A 5,5,9
Zvav 52,148,192
A 6,6,10
A 7,7,11
ds_write_b64 v232, v[6:7] offset:57344
A 8,4,5
A 9,6,7
A 8,8,9
Zvav 56,152,192
v_cmp_ngt_f32_e64 s[86:87], s90, v8
s_lshl_b32 s19, s14, 2
v_mov_b32 v9, s19
s_or_b64 s[88:89], s[88:89], s[86:87]
s_and_b64 s[88:89], s[88:89], s[24:25]
s_cselect_b32 s18, 1, 0
v_mov_b32 v8, s18
ds_write_b32 v9, v8 offset:59392
Zvav 88,152,200
v_xor_b32 v10, 1024, v232
v_bfe_u32 v11, v1, 2, 4
K 96,96,97
K 97,98,99
K 98,100,101
K 99,102,103
Zvav 120,152,208
ds_write_b128 v2, v[96:99] offset:0
K 128,128,129
)"
R"(K 129,130,131
K 130,132,133
K 131,134,135
ds_write_b128 v2, v[128:131] offset:4096
Zvav 152,152,216
K 104,104,105
K 105,106,107
K 106,108,109
K 107,110,111
ds_write_b128 v2, v[104:107] offset:1024
K 136,136,137
Zvav 156,156,216
K 137,138,139
K 138,140,141
K 139,142,143
ds_write_b128 v2, v[136:139] offset:5120
K 112,112,113
K 113,114,115
Zvav 124,156,208
K 114,116,117
K 115,118,119
ds_write_b128 v2, v[112:115] offset:2048
K 144,144,145
K 145,146,147
K 146,148,149
Zvav 92,156,200
K 147,150,151
ds_write_b128 v2, v[144:147] offset:6144
Zvav 60,156,192
K 120,120,121
K 121,122,123
K 122,124,125
K 123,126,127
ds_write_b128 v2, v[120:123] offset:3072
K 152,152,153
K 153,154,155
K 154,156,157
K 155,158,159
ds_write_b128 v2, v[152:155] offset:7168
s_waitcnt vmcnt(0)
s_and_b64 s[86:87], s[88:89], s[24:25]
s_cbranch_scc1 .LL_50%=
v_mov_b64 v[28:29], s[12:13]
s_mov_b64 exec, 1
buffer_store_dwordx2 v[28:29], off, s[8:11], s69 sc1
s_mov_b64 exec, -1
.LL_50%=:
s_mov_b32 s70, 0
s_branch .Lend_43%=
.p2align 12
.Lpd_32%=:
v_pk_mul_f32 v[32:33], v[32:33], s[22:23]
v_pk_mul_f32 v[34:35], v[34:35], s[22:23]
v_pk_mul_f32 v[36:37], v[36:37], s[22:23]
v_pk_mul_f32 v[38:39], v[38:39], s[22:23]
v_pk_mul_f32 v[64:65], v[64:65], s[22:23]
v_pk_mul_f32 v[66:67], v[66:67], s[22:23]
v_pk_mul_f32 v[68:69], v[68:69], s[22:23]
v_pk_mul_f32 v[70:71], v[70:71], s[22:23]
v_pk_mul_f32 v[96:97], v[96:97], s[22:23]
v_pk_mul_f32 v[98:99], v[98:99], s[22:23]
v_pk_mul_f32 v[100:101], v[100:101], s[22:23]
v_pk_mul_f32 v[102:103], v[102:103], s[22:23]
v_pk_mul_f32 v[128:129], v[128:129], s[22:23]
v_pk_mul_f32 v[130:131], v[130:131], s[22:23]
v_pk_mul_f32 v[132:133], v[132:133], s[22:23]
v_pk_mul_f32 v[134:135], v[134:135], s[22:23]
v_pk_mul_f32 v[40:41], v[40:41], s[22:23]
v_pk_mul_f32 v[42:43], v[42:43], s[22:23]
v_pk_mul_f32 v[44:45], v[44:45], s[22:23]
v_pk_mul_f32 v[46:47], v[46:47], s[22:23]
v_pk_mul_f32 v[72:73], v[72:73], s[22:23]
v_pk_mul_f32 v[74:75], v[74:75], s[22:23]
v_pk_mul_f32 v[76:77], v[76:77], s[22:23]
v_pk_mul_f32 v[78:79], v[78:79], s[22:23]
v_pk_mul_f32 v[104:105], v[104:105], s[22:23]
v_pk_mul_f32 v[106:107], v[106:107], s[22:23]
v_pk_mul_f32 v[108:109], v[108:109], s[22:23]
v_pk_mul_f32 v[110:111], v[110:111], s[22:23]
v_pk_mul_f32 v[136:137], v[136:137], s[22:23]
v_pk_mul_f32 v[138:139], v[138:139], s[22:23]
v_pk_mul_f32 v[140:141], v[140:141], s[22:23]
v_pk_mul_f32 v[142:143], v[142:143], s[22:23]
v_pk_mul_f32 v[48:49], v[48:49], s[22:23]
v_pk_mul_f32 v[50:51], v[50:51], s[22:23]
v_pk_mul_f32 v[52:53], v[52:53], s[22:23]
v_pk_mul_f32 v[54:55], v[54:55], s[22:23]
v_pk_mul_f32 v[80:81], v[80:81], s[22:23]
v_pk_mul_f32 v[82:83], v[82:83], s[22:23]
v_pk_mul_f32 v[84:85], v[84:85], s[22:23]
v_pk_mul_f32 v[86:87], v[86:87], s[22:23]
v_pk_mul_f32 v[112:113], v[112:113], s[22:23]
v_pk_mul_f32 v[114:115], v[114:115], s[22:23]
v_pk_mul_f32 v[116:117], v[116:117], s[22:23]
v_pk_mul_f32 v[118:119], v[118:119], s[22:23]
v_pk_mul_f32 v[144:145], v[144:145], s[22:23]
v_pk_mul_f32 v[146:147], v[146:147], s[22:23]
v_pk_mul_f32 v[148:149], v[148:149], s[22:23]
v_pk_mul_f32 v[150:151], v[150:151], s[22:23]
v_pk_mul_f32 v[56:57], v[56:57], s[22:23]
v_pk_mul_f32 v[58:59], v[58:59], s[22:23]
v_pk_mul_f32 v[60:61], v[60:61], s[22:23]
v_pk_mul_f32 v[62:63], v[62:63], s[22:23]
v_pk_mul_f32 v[88:89], v[88:89], s[22:23]
v_pk_mul_f32 v[90:91], v[90:91], s[22:23]
v_pk_mul_f32 v[92:93], v[92:93], s[22:23]
v_pk_mul_f32 v[94:95], v[94:95], s[22:23]
v_pk_mul_f32 v[120:121], v[120:121], s[22:23]
v_pk_mul_f32 v[122:123], v[122:123], s[22:23]
v_pk_mul_f32 v[124:125], v[124:125], s[22:23]
v_pk_mul_f32 v[126:127], v[126:127], s[22:23]
v_pk_mul_f32 v[152:153], v[152:153], s[22:23]
v_pk_mul_f32 v[154:155], v[154:155], s[22:23]
v_pk_mul_f32 v[156:157], v[156:157], s[22:23]
v_pk_mul_f32 v[158:159], v[158:159], s[22:23]
K 32,32,33
K 33,34,35
K 34,36,37
K 35,38,39
K 40,40,41
K 41,42,43
K 42,44,45
K 43,46,47
W 0
Zvav 196,128,32
s_add_u32 m0, s62, 0
Ra 144,242,4096
Ra 148,242,5120
Ra 152,242,6144
Ra 156,242,7168
Mvav 196,132,40
K 64,64,65
K 65,66,67
K 66,68,69
K 67,70,71
K 72,72,73
K 73,74,75
K 74,76,77
K 75,78,79
s_nop 1
Zvav 204,132,72
Dc 1,8,61,0
K 96,96,97
K 97,98,99
K 98,100,101
Mvav 204,128,64
K 99,102,103
K 104,104,105
K 105,106,107
K 106,108,109
K 107,110,111
Zvav 212,128,96
Dc 1,8,61,1024
K 128,128,129
K 129,130,131
K 130,132,133
Mvav 212,132,104
K 131,134,135
K 136,136,137
K 137,138,139
K 138,140,141
K 139,142,143
s_nop 1
Zvav 220,132,136
Dc 1,8,61,2048
K 48,48,49
K 49,50,51
K 50,52,53
Mvav 220,128,128
K 51,54,55
K 56,56,57
K 57,58,59
K 58,60,61
K 59,62,63
Mvav 196,136,48
Dc 1,8,61,3072
s_add_u32 m0, s62, 4096
Ra 128,242,8192
Ra 132,242,9216
K 80,80,81
Mvav 196,140,56
K 81,82,83
K 82,84,85
K 83,86,87
K 88,88,89
K 89,90,91
K 90,92,93
K 91,94,95
s_nop 1
Mvav 204,140,88
Dc 243,8,61,0
K 112,112,113
K 113,114,115
K 114,116,117
Mvav 204,136,80
K 115,118,119
K 120,120,121
K 121,122,123
K 122,124,125
K 123,126,127
Mvav 212,136,112
Dc 243,8,61,1024
K 144,144,145
K 145,146,147
K 146,148,149
Mvav 212,140,120
K 147,150,151
K 152,152,153
K 153,154,155
K 154,156,157
K 155,158,159
s_nop 1
Mvav 220,140,152
Dc 243,8,61,2048
v_accvgpr_write_b32 a0, v32
v_accvgpr_write_b32 a1, v33
v_accvgpr_write_b32 a2, v34
Mvav 220,136,144
W 2
Dc 243,8,61,3072
s_add_u32 m0, s62, 8192
v_accvgpr_write_b32 a3, v35
v_accvgpr_write_b32 a4, v40
Zvav 68,144,64
Dc 244,8,61,0
v_accvgpr_write_b32 a5, v41
v_accvgpr_write_b32 a6, v42
v_accvgpr_write_b32 a7, v43
Mvav 68,148,72
Dc 244,8,61,1024
v_accvgpr_write_b32 a8, v48
v_accvgpr_write_b32 a9, v49
v_accvgpr_write_b32 a10, v50
Zvav 100,148,104
Dc 244,8,61,2048
Ra 136,242,10240
Ra 140,242,11264
v_accvgpr_write_b32 a11, v51
Mvav 100,144,96
Dc 244,8,61,3072
s_add_u32 m0, s62, 12288
v_accvgpr_write_b32 a12, v56
v_accvgpr_write_b32 a13, v57
v_accvgpr_write_b32 a14, v58
Zvav 132,144,128
Dc 245,8,61,0
v_accvgpr_write_b32 a15, v59
v_accvgpr_write_b32 a16, v64
v_cndmask_b32_e64 v196, v241, v196, s[72:73]
Mvav 132,148,136
Dc 245,8,61,1024
v_cndmask_b32_e64 v197, v241, v197, s[74:75]
v_cndmask_b32_e64 v198, v241, v198, s[76:77]
v_cndmask_b32_e64 v199, v241, v199, s[78:79]
Mvav 68,152,80
Dc 245,8,61,2048
v_max3_f32 v4, v196, v197, v198
v_max3_f32 v4, v4, v199, v252
v_max3_f32 v5, v204, v205, v206
Mvav 68,156,88
v_max3_f32 v5, v5, v207, v253
v_max3_f32 v6, v212, v213, v214
v_max3_f32 v6, v6, v215, v254
v_max3_f32 v7, v220, v221, v222
Mvav 100,156,120
v_max3_f32 v7, v7, v223, v255
v_mov_b32 v8, v4
v_mov_b32 v9, v5
v_mov_b32 v10, v6
Mvav 100,152,112
v_mov_b32 v11, v7
v_permlane32_swap_b32 v4, v8
v_permlane32_swap_b32 v5, v9
v_accvgpr_write_b32 a17, v65
Mvav 132,152,144
v_permlane32_swap_b32 v6, v10
v_permlane32_swap_b32 v7, v11
v_max_f32 v4, v4, v8
v_max_f32 v5, v5, v9
v_max_f32 v6, v6, v10
v_max_f32 v7, v7, v11
v_mov_b32 v8, v4
v_mov_b32 v9, v5
v_mov_b32 v10, v6
v_mov_b32 v11, v7
v_permlane16_swap_b32 v4, v8
v_permlane16_swap_b32 v5, v9
v_permlane16_swap_b32 v6, v10
v_permlane16_swap_b32 v7, v11
v_max_f32 v4, v4, v8
v_max_f32 v5, v5, v9
v_max_f32 v6, v6, v10
v_max_f32 v7, v7, v11
v_sub_f32 v4, 0, v4
v_sub_f32 v5, 0, v5
v_sub_f32 v6, 0, v6
v_sub_f32 v7, 0, v7
v_mov_b32 v224, v4
v_mov_b32 v225, v4
v_mov_b32 v226, v4
v_mov_b32 v227, v4
v_mov_b32 v228, v5
v_mov_b32 v229, v5
v_mov_b32 v230, v5
v_mov_b32 v231, v5
v_mov_b32 v232, v6
v_mov_b32 v233, v6
v_mov_b32 v234, v6
v_mov_b32 v235, v6
v_mov_b32 v236, v7
v_mov_b32 v237, v7
v_mov_b32 v238, v7
v_mov_b32 v239, v7
Mvav 132,156,152
W 0
Dc 245,8,61,3072
v_accvgpr_write_b32 a18, v66
v_accvgpr_write_b32 a19, v67
v_accvgpr_write_b32 a20, v72
Cvav 168,128,96,232
s_add_u32 s61, s61, s63
s_add_u32 s36, s29, 65536
v_accvgpr_write_b32 a21, v73
v_accvgpr_write_b32 a22, v74
v_accvgpr_write_b32 a23, v75
Mvav 168,132,104
s_and_b32 s80, s14, 1
buffer_store_dwordx4 a[192:195], v1, s[8:11], s36 offen offset:0 sc1
v_accvgpr_write_b32 a24, v80
v_accvgpr_write_b32 a25, v81
v_accvgpr_write_b32 a26, v82
Cvav 172,132,136,236
s_lshl_b32 s80, s80, 14
Ra 144,242,12288
Ra 148,242,13312
Ra 152,242,14336
Ra 156,242,15360
Mvav 172,128,128
s_add_u32 s80, s80, 98304
v_accvgpr_write_b32 a27, v83
v_accvgpr_write_b32 a28, v88
v_accvgpr_write_b32 a29, v89
v_accvgpr_write_b32 a30, v90
Mvav 168,136,112
buffer_store_dwordx4 a[200:203], v1, s[8:11], s36 offen offset:1024 sc1
v_accvgpr_write_b32 a31, v91
v_accvgpr_write_b32 a32, v96
v_accvgpr_write_b32 a33, v97
Mvav 168,140,120
v_accvgpr_write_b32 a34, v98
v_accvgpr_write_b32 a35, v99
v_accvgpr_write_b32 a36, v104
v_accvgpr_write_b32 a37, v105
Mvav 172,140,152
Ra 128,242,24576
Ra 132,242,25600
buffer_store_dwordx4 a[208:211], v1, s[8:11], s36 offen offset:2048 sc1
v_accvgpr_write_b32 a38, v106
Mvav 172,136,144
Ra 136,242,26624
Ra 140,242,27648
v_accvgpr_write_b32 a39, v107
W 4
Cvav 188,144,128,236
v_accvgpr_write_b32 a40, v112
v_accvgpr_write_b32 a41, v113
v_accvgpr_write_b32 a42, v114
v_accvgpr_write_b32 a43, v115
v_accvgpr_write_b32 a44, v120
v_accvgpr_write_b32 a45, v121
v_accvgpr_write_b32 a46, v122
v_accvgpr_write_b32 a47, v123
v_accvgpr_write_b32 a48, v128
v_accvgpr_write_b32 a49, v129
v_accvgpr_write_b32 a50, v130
v_accvgpr_write_b32 a51, v131
v_accvgpr_write_b32 a52, v136
v_accvgpr_write_b32 a53, v137
v_accvgpr_write_b32 a54, v138
v_accvgpr_write_b32 a55, v139
v_accvgpr_write_b32 a56, v144
v_accvgpr_write_b32 a57, v145
v_accvgpr_write_b32 a58, v146
v_accvgpr_write_b32 a59, v147
v_accvgpr_write_b32 a60, v152
v_accvgpr_write_b32 a61, v153
v_accvgpr_write_b32 a62, v154
v_accvgpr_write_b32 a63, v155
Mvav 188,148,136
s_barrier
buffer_store_dwordx4 a[216:219], v1, s[8:11], s36 offen offset:3072 sc1
s_add_u32 s36, s29, 69632
v_add_u32 v8, s80, v1
A 196,196,224
Mvav 188,152,144
A 197,197,224
A 198,198,224
A 199,199,224
X 196
X 197
)"
R"(X 198
K 192,196,197
X 199
A 204,204,228
K 193,198,199
A 205,205,228
A 206,206,228
A 207,207,228
X 204
X 205
X 206
K 200,204,205
X 207
A 212,212,232
K 201,206,207
A 213,213,232
A 214,214,232
A 215,215,232
X 212
X 213
X 214
K 208,212,213
X 215
A 220,220,236
K 209,214,215
A 221,221,236
A 222,222,236
A 223,223,236
X 220
X 221
X 222
K 216,220,221
X 223
v_mov_b32 v194, 0
K 217,222,223
v_mov_b32 v195, 0
v_cndmask_b32_e64 v68, v241, v68, s[72:73]
v_cndmask_b32_e64 v69, v241, v69, s[74:75]
v_cndmask_b32_e64 v70, v241, v70, s[76:77]
v_cndmask_b32_e64 v71, v241, v71, s[78:79]
A 68,68,228
A 69,69,228
A 70,70,228
A 71,71,228
X 68
X 69
X 70
K 202,68,69
X 71
A 100,100,232
K 203,70,71
A 101,101,232
A 102,102,232
A 103,103,232
X 100
X 101
X 102
K 210,100,101
X 103
A 132,132,236
K 211,102,103
A 133,133,236
A 134,134,236
A 135,135,236
X 132
X 133
X 134
K 218,132,133
X 135
s_nop 0
K 219,134,135
Mvav 188,156,152
W 0
buffer_store_dwordx4 a[224:227], v1, s[8:11], s36 offen offset:0 sc1
Ra 144,242,28672
Ra 148,242,29696
Ra 152,242,30720
Zvav 32,128,192
buffer_store_dwordx4 a[232:235], v1, s[8:11], s36 offen offset:1024 sc1
Ra 156,242,31744
Zvav 64,128,200
buffer_store_dwordx4 a[240:243], v1, s[8:11], s36 offen offset:2048 sc1
Zvav 96,128,208
s_and_b32 s80, s14, 1
s_xor_b32 s62, s62, 0x10000
buffer_store_dwordx4 a[248:251], v1, s[8:11], s36 offen offset:3072 sc1
s_add_u32 s36, s29, 393216
Zvav 128,128,216
s_lshl_b32 s80, s80, 14
s_add_u32 s80, s80, 131072
buffer_store_dwordx4 a[196:199], v1, s[8:11], s36 offen offset:0 sc1
v_add_u32 v23, 61440, v21
Zvav 132,132,216
buffer_store_dwordx4 a[204:207], v1, s[8:11], s36 offen offset:1024 sc1
v_add_u32 v12, s80, v1
Ra 64,12,8192
v_add_u32 v24, 61440, v22
Zvav 100,132,208
buffer_store_dwordx4 a[212:215], v1, s[8:11], s36 offen offset:2048 sc1
Ra 68,12,9216
Ra 72,12,10240
v_add_u32 v25, 122880, v21
Zvav 68,132,200
buffer_store_dwordx4 a[220:223], v1, s[8:11], s36 offen offset:3072 sc1
s_add_u32 s36, s29, 397312
Ra 76,12,11264
Ra 80,12,12288
Ra 84,12,13312
Zvav 36,132,192
buffer_store_dwordx4 a[228:231], v1, s[8:11], s36 offen offset:0 sc1
Ra 88,12,14336
Ra 92,12,15360
v_add_u32 v26, 122880, v22
Zvav 40,136,192
buffer_store_dwordx4 a[236:239], v1, s[8:11], s36 offen offset:1024 sc1
Ra 96,12,0
Ra 100,12,1024
Ra 104,12,2048
Zvav 72,136,200
Ra 108,12,3072
Ra 112,12,4096
buffer_store_dwordx4 a[244:247], v1, s[8:11], s36 offen offset:2048 sc1
Ra 116,12,5120
Zvav 104,136,208
Ra 120,12,6144
Ra 124,12,7168
buffer_store_dwordx4 a[252:255], v1, s[8:11], s36 offen offset:3072 sc1
Zvav 136,136,216
Zvav 140,140,216
Zvav 108,140,208
Zvav 76,140,200
s_waitcnt vmcnt(16) lgkmcnt(0)
s_barrier
v_xor_b32 v242, 0x10000, v242
ds_read_b128 v[248:251], v8
Zvav 44,140,192
Ra 128,242,40960
Zvav 48,144,192
Zvav 80,144,200
X 172
X 173
Zvav 112,144,208
Ra 132,242,41984
X 174
K 220,172,173
Zvav 144,144,216
X 175
v_cndmask_b32_e64 v168, v241, v168, s[72:73]
K 221,174,175
Zvav 148,148,216
v_cndmask_b32_e64 v169, v241, v169, s[74:75]
v_cndmask_b32_e64 v170, v241, v170, s[76:77]
v_cndmask_b32_e64 v171, v241, v171, s[78:79]
Zvav 116,148,208
X 168
X 169
Zvav 84,148,200
W 2
Ra 136,242,43008
X 170
K 212,168,169
Zvav 52,148,192
v_mov_b32 v224, v250
v_mov_b32 v225, v250
v_mov_b32 v226, v250
v_mov_b32 v227, v250
Zvav 56,152,192
v_mov_b32 v228, v251
v_mov_b32 v229, v251
v_mov_b32 v230, v251
v_mov_b32 v231, v251
Zvav 88,152,200
v_mov_b32 v232, v248
v_mov_b32 v233, v248
v_mov_b32 v234, v248
v_mov_b32 v235, v248
Zvav 120,152,208
v_mov_b32 v236, v249
v_mov_b32 v237, v249
v_mov_b32 v238, v249
v_mov_b32 v239, v249
Zvav 152,152,216
X 171
v_mov_b32 v204, 0
K 213,170,171
Zvav 156,156,216
Ra 140,242,44032
v_mov_b32 v205, 0
v_mov_b32 v196, 0
v_mov_b32 v197, 0
Zvav 124,156,208
v_mov_b32 v198, 0
v_mov_b32 v199, 0
v_mov_b32 v206, 0
v_mov_b32 v207, 0
Zvav 92,156,200
v_mov_b32 v214, 0
v_mov_b32 v215, 0
v_cndmask_b32_e64 v188, v241, v188, s[72:73]
v_cndmask_b32_e64 v189, v241, v189, s[74:75]
Zvav 60,156,192
v_cndmask_b32_e64 v190, v241, v190, s[76:77]
v_cndmask_b32_e64 v191, v241, v191, s[78:79]
X 188
X 189
X 190
K 222,188,189
X 191
s_nop 0
K 223,190,191
s_waitcnt vmcnt(0)
s_add_u32 s80, s71, 128
v_mov_b64 v[28:29], s[12:13]
s_mov_b64 exec, 1
buffer_store_dwordx2 v[28:29], off, s[8:11], s80 sc1
s_mov_b64 exec, -1
s_nop 1
s_branch .Lsp2_37%=
.p2align 12
.Lpe_48%=:
v_pk_mul_f32 v[32:33], v[32:33], s[22:23]
v_pk_mul_f32 v[34:35], v[34:35], s[22:23]
v_pk_mul_f32 v[36:37], v[36:37], s[22:23]
v_pk_mul_f32 v[38:39], v[38:39], s[22:23]
v_pk_mul_f32 v[64:65], v[64:65], s[22:23]
v_pk_mul_f32 v[66:67], v[66:67], s[22:23]
v_pk_mul_f32 v[68:69], v[68:69], s[22:23]
v_pk_mul_f32 v[70:71], v[70:71], s[22:23]
v_pk_mul_f32 v[96:97], v[96:97], s[22:23]
v_pk_mul_f32 v[98:99], v[98:99], s[22:23]
v_pk_mul_f32 v[100:101], v[100:101], s[22:23]
v_pk_mul_f32 v[102:103], v[102:103], s[22:23]
v_pk_mul_f32 v[128:129], v[128:129], s[22:23]
v_pk_mul_f32 v[130:131], v[130:131], s[22:23]
v_pk_mul_f32 v[132:133], v[132:133], s[22:23]
v_pk_mul_f32 v[134:135], v[134:135], s[22:23]
v_pk_mul_f32 v[40:41], v[40:41], s[22:23]
v_pk_mul_f32 v[42:43], v[42:43], s[22:23]
v_pk_mul_f32 v[44:45], v[44:45], s[22:23]
v_pk_mul_f32 v[46:47], v[46:47], s[22:23]
v_pk_mul_f32 v[72:73], v[72:73], s[22:23]
v_pk_mul_f32 v[74:75], v[74:75], s[22:23]
v_pk_mul_f32 v[76:77], v[76:77], s[22:23]
v_pk_mul_f32 v[78:79], v[78:79], s[22:23]
v_pk_mul_f32 v[104:105], v[104:105], s[22:23]
v_pk_mul_f32 v[106:107], v[106:107], s[22:23]
v_pk_mul_f32 v[108:109], v[108:109], s[22:23]
v_pk_mul_f32 v[110:111], v[110:111], s[22:23]
v_pk_mul_f32 v[136:137], v[136:137], s[22:23]
v_pk_mul_f32 v[138:139], v[138:139], s[22:23]
v_pk_mul_f32 v[140:141], v[140:141], s[22:23]
v_pk_mul_f32 v[142:143], v[142:143], s[22:23]
v_pk_mul_f32 v[48:49], v[48:49], s[22:23]
v_pk_mul_f32 v[50:51], v[50:51], s[22:23]
v_pk_mul_f32 v[52:53], v[52:53], s[22:23]
v_pk_mul_f32 v[54:55], v[54:55], s[22:23]
v_pk_mul_f32 v[80:81], v[80:81], s[22:23]
v_pk_mul_f32 v[82:83], v[82:83], s[22:23]
v_pk_mul_f32 v[84:85], v[84:85], s[22:23]
v_pk_mul_f32 v[86:87], v[86:87], s[22:23]
v_pk_mul_f32 v[112:113], v[112:113], s[22:23]
v_pk_mul_f32 v[114:115], v[114:115], s[22:23]
v_pk_mul_f32 v[116:117], v[116:117], s[22:23]
v_pk_mul_f32 v[118:119], v[118:119], s[22:23]
v_pk_mul_f32 v[144:145], v[144:145], s[22:23]
v_pk_mul_f32 v[146:147], v[146:147], s[22:23]
v_pk_mul_f32 v[148:149], v[148:149], s[22:23]
v_pk_mul_f32 v[150:151], v[150:151], s[22:23]
v_pk_mul_f32 v[56:57], v[56:57], s[22:23]
v_pk_mul_f32 v[58:59], v[58:59], s[22:23]
v_pk_mul_f32 v[60:61], v[60:61], s[22:23]
v_pk_mul_f32 v[62:63], v[62:63], s[22:23]
v_pk_mul_f32 v[88:89], v[88:89], s[22:23]
v_pk_mul_f32 v[90:91], v[90:91], s[22:23]
v_pk_mul_f32 v[92:93], v[92:93], s[22:23]
v_pk_mul_f32 v[94:95], v[94:95], s[22:23]
v_pk_mul_f32 v[120:121], v[120:121], s[22:23]
v_pk_mul_f32 v[122:123], v[122:123], s[22:23]
v_pk_mul_f32 v[124:125], v[124:125], s[22:23]
v_pk_mul_f32 v[126:127], v[126:127], s[22:23]
v_pk_mul_f32 v[152:153], v[152:153], s[22:23]
v_pk_mul_f32 v[154:155], v[154:155], s[22:23]
v_pk_mul_f32 v[156:157], v[156:157], s[22:23]
v_pk_mul_f32 v[158:159], v[158:159], s[22:23]
K 32,32,33
K 33,34,35
K 34,36,37
K 35,38,39
K 40,40,41
K 41,42,43
K 42,44,45
K 43,46,47
W 0
Zvav 196,128,32
s_add_u32 m0, s62, 0
Ra 144,242,4096
Ra 148,242,5120
Ra 152,242,6144
Ra 156,242,7168
Mvav 196,132,40
K 64,64,65
K 65,66,67
K 66,68,69
K 67,70,71
K 72,72,73
K 73,74,75
K 74,76,77
K 75,78,79
s_nop 1
Zvav 204,132,72
Dc 1,8,61,0
K 96,96,97
K 97,98,99
K 98,100,101
Mvav 204,128,64
K 99,102,103
K 104,104,105
K 105,106,107
K 106,108,109
K 107,110,111
Zvav 212,128,96
Dc 1,8,61,1024
K 128,128,129
K 129,130,131
K 130,132,133
Mvav 212,132,104
K 131,134,135
K 136,136,137
K 137,138,139
K 138,140,141
K 139,142,143
s_nop 1
Zvav 220,132,136
Dc 1,8,61,2048
K 48,48,49
K 49,50,51
K 50,52,53
Mvav 220,128,128
K 51,54,55
K 56,56,57
K 57,58,59
K 58,60,61
K 59,62,63
Mvav 196,136,48
Dc 1,8,61,3072
s_add_u32 m0, s62, 4096
Ra 128,242,8192
Ra 132,242,9216
K 80,80,81
Mvav 196,140,56
K 81,82,83
K 82,84,85
K 83,86,87
K 88,88,89
K 89,90,91
K 90,92,93
K 91,94,95
s_nop 1
Mvav 204,140,88
Dc 243,8,61,0
K 112,112,113
K 113,114,115
K 114,116,117
Mvav 204,136,80
K 115,118,119
K 120,120,121
K 121,122,123
K 122,124,125
K 123,126,127
Mvav 212,136,112
Dc 243,8,61,1024
K 144,144,145
K 145,146,147
K 146,148,149
Mvav 212,140,120
K 147,150,151
K 152,152,153
K 153,154,155
K 154,156,157
K 155,158,159
s_nop 1
Mvav 220,140,152
Dc 243,8,61,2048
v_accvgpr_write_b32 a0, v32
v_accvgpr_write_b32 a1, v33
v_accvgpr_write_b32 a2, v34
Mvav 220,136,144
W 2
Dc 243,8,61,3072
s_add_u32 m0, s62, 8192
v_accvgpr_write_b32 a3, v35
v_accvgpr_write_b32 a4, v40
Zvav 36,144,32
Ra 136,242,10240
Ra 140,242,11264
Dc 244,8,61,0
v_accvgpr_write_b32 a5, v41
Mvav 36,148,40
v_accvgpr_write_b32 a6, v42
v_accvgpr_write_b32 a7, v43
)"
R"(v_max3_f32 v4, v196, v197, v198
v_max3_f32 v4, v4, v199, v252
Zvav 68,148,72
v_max3_f32 v5, v204, v205, v206
v_max3_f32 v5, v5, v207, v253
v_max3_f32 v6, v212, v213, v214
v_max3_f32 v6, v6, v215, v254
Mvav 68,144,64
v_max3_f32 v7, v220, v221, v222
v_max3_f32 v7, v7, v223, v255
v_mov_b32 v8, v4
v_mov_b32 v9, v5
Zvav 100,144,96
v_mov_b32 v10, v6
v_mov_b32 v11, v7
v_permlane32_swap_b32 v4, v8
v_accvgpr_write_b32 a8, v48
Mvav 100,148,104
v_permlane32_swap_b32 v5, v9
v_permlane32_swap_b32 v6, v10
v_permlane32_swap_b32 v7, v11
v_max_f32 v4, v4, v8
Zvav 132,148,136
v_max_f32 v5, v5, v9
v_max_f32 v6, v6, v10
v_max_f32 v7, v7, v11
v_mov_b32 v8, v4
Mvav 132,144,128
Dc 244,8,61,1024
v_mov_b32 v9, v5
v_mov_b32 v10, v6
v_mov_b32 v11, v7
Mvav 36,152,48
v_permlane16_swap_b32 v4, v8
v_permlane16_swap_b32 v5, v9
v_permlane16_swap_b32 v6, v10
v_accvgpr_write_b32 a9, v49
Mvav 36,156,56
v_permlane16_swap_b32 v7, v11
v_max_f32 v4, v4, v8
v_max_f32 v5, v5, v9
v_max_f32 v6, v6, v10
Mvav 68,156,88
v_max_f32 v7, v7, v11
v_sub_f32 v4, 0, v4
v_sub_f32 v5, 0, v5
v_sub_f32 v6, 0, v6
Mvav 68,152,80
v_sub_f32 v7, 0, v7
v_mov_b32 v224, v4
v_mov_b32 v225, v4
v_mov_b32 v226, v4
Mvav 100,152,112
v_mov_b32 v227, v4
v_mov_b32 v228, v5
v_mov_b32 v229, v5
v_mov_b32 v230, v5
Mvav 100,156,120
v_mov_b32 v231, v5
v_mov_b32 v232, v6
v_mov_b32 v233, v6
v_mov_b32 v234, v6
Mvav 132,156,152
v_mov_b32 v235, v6
v_mov_b32 v236, v7
v_mov_b32 v237, v7
v_mov_b32 v238, v7
v_mov_b32 v239, v7
Mvav 132,152,144
W 0
Dc 244,8,61,2048
v_accvgpr_write_b32 a10, v50
v_accvgpr_write_b32 a11, v51
v_accvgpr_write_b32 a12, v56
Cvav 160,128,32,224
Dc 244,8,61,3072
s_add_u32 m0, s62, 12288
v_accvgpr_write_b32 a13, v57
v_accvgpr_write_b32 a14, v58
v_accvgpr_write_b32 a15, v59
Mvav 160,132,40
s_and_b32 s80, s14, 1
Ra 144,242,12288
Ra 148,242,13312
v_accvgpr_write_b32 a16, v64
v_accvgpr_write_b32 a17, v65
Cvav 164,132,72,228
s_lshl_b32 s80, s80, 14
Dc 245,8,61,0
v_accvgpr_write_b32 a18, v66
v_accvgpr_write_b32 a19, v67
v_accvgpr_write_b32 a20, v72
Mvav 164,128,64
s_add_u32 s80, s80, 98304
Dc 245,8,61,1024
v_accvgpr_write_b32 a21, v73
v_accvgpr_write_b32 a22, v74
v_accvgpr_write_b32 a23, v75
Cvav 168,128,96,232
Dc 245,8,61,2048
v_accvgpr_write_b32 a24, v80
v_accvgpr_write_b32 a25, v81
v_accvgpr_write_b32 a26, v82
Mvav 168,132,104
Dc 245,8,61,3072
s_add_u32 s61, s61, s63
s_add_u32 s36, s29, 65536
A 196,196,224
v_accvgpr_write_b32 a27, v83
Cvav 172,132,136,236
Ra 152,242,14336
A 197,197,224
A 198,198,224
v_accvgpr_write_b32 a28, v88
Mvav 172,128,128
buffer_store_dwordx4 a[192:195], v1, s[8:11], s36 offen offset:0 sc1
Ra 156,242,15360
A 199,199,224
v_accvgpr_write_b32 a29, v89
Mvav 160,136,48
X 196
v_accvgpr_write_b32 a30, v90
v_accvgpr_write_b32 a31, v91
Mvav 160,140,56
X 197
X 198
Mvav 164,140,88
buffer_store_dwordx4 a[200:203], v1, s[8:11], s36 offen offset:1024 sc1
v_accvgpr_write_b32 a32, v96
K 192,196,197
v_accvgpr_write_b32 a33, v97
Mvav 164,136,80
X 199
A 204,204,228
v_accvgpr_write_b32 a34, v98
Mvav 168,136,112
K 193,198,199
v_accvgpr_write_b32 a35, v99
A 205,205,228
A 206,206,228
Mvav 168,140,120
buffer_store_dwordx4 a[208:211], v1, s[8:11], s36 offen offset:2048 sc1
v_accvgpr_write_b32 a36, v104
v_add_u32 v8, s80, v1
A 207,207,228
Mvav 172,140,152
X 204
v_accvgpr_write_b32 a37, v105
v_accvgpr_write_b32 a38, v106
Mvav 172,136,144
W 0
X 205
X 206
Cvav 176,144,32,224
Ra 128,242,24576
v_accvgpr_write_b32 a39, v107
v_accvgpr_write_b32 a40, v112
K 200,204,205
Mvav 176,148,40
buffer_store_dwordx4 a[216:219], v1, s[8:11], s36 offen offset:3072 sc1
s_add_u32 s36, s29, 69632
X 207
v_accvgpr_write_b32 a41, v113
Cvav 180,148,72,228
Ra 132,242,25600
A 212,212,232
v_accvgpr_write_b32 a42, v114
K 201,206,207
Mvav 180,144,64
A 213,213,232
v_accvgpr_write_b32 a43, v115
A 214,214,232
A 215,215,232
Cvav 184,144,96,232
v_accvgpr_write_b32 a44, v120
X 212
v_accvgpr_write_b32 a45, v121
Mvav 184,148,104
buffer_store_dwordx4 a[224:227], v1, s[8:11], s36 offen offset:0 sc1
X 213
v_accvgpr_write_b32 a46, v122
Cvav 188,148,136,236
X 214
v_accvgpr_write_b32 a47, v123
K 208,212,213
Mvav 188,144,128
v_accvgpr_write_b32 a48, v128
X 215
v_accvgpr_write_b32 a49, v129
Mvav 176,152,48
buffer_store_dwordx4 a[232:235], v1, s[8:11], s36 offen offset:1024 sc1
A 220,220,236
v_accvgpr_write_b32 a50, v130
K 209,214,215
Mvav 176,156,56
A 221,221,236
v_accvgpr_write_b32 a51, v131
A 222,222,236
A 223,223,236
Mvav 180,156,88
Ra 136,242,26624
Ra 140,242,27648
v_accvgpr_write_b32 a52, v136
v_accvgpr_write_b32 a53, v137
Mvav 180,152,80
X 220
X 221
Mvav 184,152,112
v_accvgpr_write_b32 a54, v138
v_accvgpr_write_b32 a55, v139
v_accvgpr_write_b32 a56, v144
v_accvgpr_write_b32 a57, v145
v_accvgpr_write_b32 a58, v146
v_accvgpr_write_b32 a59, v147
v_accvgpr_write_b32 a60, v152
v_accvgpr_write_b32 a61, v153
v_accvgpr_write_b32 a62, v154
v_accvgpr_write_b32 a63, v155
Mvav 184,156,120
s_barrier
buffer_store_dwordx4 a[240:243], v1, s[8:11], s36 offen offset:2048 sc1
X 222
K 216,220,221
Mvav 188,156,152
X 223
A 36,36,224
K 217,222,223
A 37,37,224
A 38,38,224
A 39,39,224
X 36
X 37
X 38
K 194,36,37
X 39
A 68,68,228
K 195,38,39
A 69,69,228
A 70,70,228
A 71,71,228
X 68
X 69
X 70
K 202,68,69
X 71
A 100,100,232
K 203,70,71
A 101,101,232
A 102,102,232
A 103,103,232
X 100
X 101
X 102
K 210,100,101
X 103
A 132,132,236
K 211,102,103
A 133,133,236
A 134,134,236
A 135,135,236
X 132
X 133
X 134
K 218,132,133
X 135
s_nop 0
K 219,134,135
Mvav 188,152,144
W 0
buffer_store_dwordx4 a[248:251], v1, s[8:11], s36 offen offset:3072 sc1
s_add_u32 s36, s29, 393216
Ra 144,242,28672
Ra 148,242,29696
Zvav 32,128,192
buffer_store_dwordx4 a[196:199], v1, s[8:11], s36 offen offset:0 sc1
Ra 152,242,30720
Ra 156,242,31744
Zvav 64,128,200
buffer_store_dwordx4 a[204:207], v1, s[8:11], s36 offen offset:1024 sc1
Zvav 96,128,208
s_and_b32 s80, s14, 1
s_xor_b32 s62, s62, 0x10000
buffer_store_dwordx4 a[212:215], v1, s[8:11], s36 offen offset:2048 sc1
Zvav 128,128,216
s_lshl_b32 s80, s80, 14
s_add_u32 s80, s80, 131072
buffer_store_dwordx4 a[220:223], v1, s[8:11], s36 offen offset:3072 sc1
s_add_u32 s36, s29, 397312
v_add_u32 v23, 61440, v21
Zvav 132,132,216
buffer_store_dwordx4 a[228:231], v1, s[8:11], s36 offen offset:0 sc1
v_add_u32 v12, s80, v1
Ra 64,12,8192
v_add_u32 v24, 61440, v22
Zvav 100,132,208
Ra 68,12,9216
Ra 72,12,10240
buffer_store_dwordx4 a[236:239], v1, s[8:11], s36 offen offset:1024 sc1
v_add_u32 v25, 122880, v21
Zvav 68,132,200
Ra 76,12,11264
Ra 80,12,12288
Ra 84,12,13312
v_add_u32 v26, 122880, v22
Zvav 36,132,192
Ra 88,12,14336
Ra 92,12,15360
Ra 96,12,0
Ra 100,12,1024
Zvav 40,136,192
Ra 104,12,2048
buffer_store_dwordx4 a[244:247], v1, s[8:11], s36 offen offset:2048 sc1
Ra 108,12,3072
Ra 112,12,4096
Zvav 72,136,200
Ra 116,12,5120
Ra 120,12,6144
X 172
Zvav 104,136,208
Ra 124,12,7168
buffer_store_dwordx4 a[252:255], v1, s[8:11], s36 offen offset:3072 sc1
X 173
Zvav 136,136,216
X 174
K 220,172,173
Zvav 140,140,216
X 175
X 168
Zvav 108,140,208
K 221,174,175
X 169
Zvav 76,140,200
s_waitcnt vmcnt(16) lgkmcnt(0)
s_barrier
v_xor_b32 v242, 0x10000, v242
ds_read_b128 v[248:251], v8
Zvav 44,140,192
Ra 128,242,0
X 170
K 212,168,169
Zvav 48,144,192
X 171
X 164
Zvav 80,144,200
K 213,170,171
X 165
Zvav 112,144,208
Ra 132,242,1024
X 166
K 204,164,165
Zvav 144,144,216
X 167
X 160
Zvav 148,148,216
K 205,166,167
X 161
Zvav 116,148,208
Ra 136,242,2048
X 162
Zvav 84,148,200
W 3
K 196,160,161
X 163
Zvav 52,148,192
X 176
K 197,162,163
Zvav 56,152,192
Ra 140,242,3072
X 177
Zvav 88,152,200
X 178
K 198,176,177
Zvav 120,152,208
X 179
X 180
Zvav 152,152,216
K 199,178,179
Zvav 156,156,216
X 181
X 182
Zvav 124,156,208
K 206,180,181
X 183
Zvav 92,156,200
K 207,182,183
Zvav 60,156,192
s_waitcnt vmcnt(0)
s_add_u32 s80, s71, 128
v_mov_b64 v[28:29], s[12:13]
s_mov_b64 exec, 1
buffer_store_dwordx2 v[28:29], off, s[8:11], s80 sc1
s_mov_b64 exec, -1
s_branch .Lsp1_36%=
.p2align 12
.Lmoe_44%=:
W 0
Cvaa 160,128,0,224
s_cmp_eq_u32 s14, 0
s_cselect_b32 s20, s30, s32
s_cselect_b32 s21, s31, s33
)"
R"(Ra 144,242,4096
Ra 148,242,5120
Mvaa 160,132,4
s_xor_b32 s62, s62, 0x10000
Ra 152,242,6144
Cvaa 164,132,20,228
X 184
Mvaa 164,128,16
s_and_b32 s21, s21, 0xffff
X 185
Cvaa 168,128,32,232
X 186
Mvaa 168,132,36
Ra 156,242,7168
K 214,184,185
Cvaa 172,132,52,236
s_mov_b32 s22, 2211840
X 187
Mvaa 172,128,48
X 188
Mvaa 160,136,8
Ra 128,242,16384
Mvaa 160,140,12
s_mov_b32 s23, 0x20000
K 215,186,187
Mvaa 164,140,28
X 189
Mvaa 164,136,24
X 190
Mvaa 168,136,40
Ra 132,242,17408
v_lshrrev_b32 v4, 6, v1
Mvaa 168,140,44
K 222,188,189
Mvaa 172,140,60
X 191
Mvaa 172,136,56
v_mul_u32_u24 v4, 3456, v4
K 223,190,191
v_smfmac_f32_16x16x64_bf16 a[160:163], a[176:179], v[192:199], v240
Ra 136,242,18432
v_smfmac_f32_16x16x64_bf16 a[164:167], a[176:179], v[200:207], v240
X 160
v_smfmac_f32_16x16x64_bf16 a[168:171], a[176:179], v[208:215], v240
v_lshrrev_b32 v5, 4, v1
v_smfmac_f32_16x16x64_bf16 a[172:175], a[176:179], v[216:223], v240
W 3
X 161
Cvaa 176,144,0,224
X 162
Mvaa 176,148,4
Ra 140,242,19456
v_xor_b32 v5, v5, v1
Cvaa 180,148,20,228
K 192,160,161
Mvaa 180,144,16
X 163
Cvaa 184,144,32,232
v_and_b32 v5, 32, v5
Mvaa 184,148,36
X 164
Cvaa 188,148,52,236
K 193,162,163
v_and_b32 v6, 16, v1
Mvaa 188,144,48
X 165
Mvaa 176,152,8
X 166
Mvaa 176,156,12
Ra 144,242,20480
Ra 148,242,21504
Mvaa 180,156,28
v_add3_u32 v4, v4, v5, v6
K 200,164,165
Mvaa 180,152,24
X 167
Mvaa 184,152,40
X 168
Mvaa 184,156,44
v_add_u32 v5, 54272, v4
K 201,166,167
Mvaa 188,156,60
X 169
Mvaa 188,152,56
W 2
Ra 152,242,22528
v_add_u32 v6, 108544, v4
Mvav 32,128,196
X 170
Mvav 64,128,204
Ra 156,242,23552
v_add_u32 v7, 162816, v4
Mvav 96,128,212
K 208,168,169
Mvav 128,128,220
X 171
Mvav 132,132,220
s_lshl_b32 s94, s85, 6
X 172
Mvav 100,132,212
s_add_u32 s64, s64, 1
Ra 128,242,8192
K 209,170,171
Mvav 68,132,204
X 173
Mvav 36,132,196
s_mul_i32 s95, s14, 20480
Mvav 40,136,196
X 174
Mvav 72,136,204
Ra 132,242,9216
K 216,172,173
Mvav 104,136,212
s_add_u32 s95, s95, 98304
X 175
Mvav 136,136,220
Mvav 140,140,220
s_add_u32 m0, s95, 0
X 176
Mvav 108,140,212
Ra 136,242,10240
K 217,174,175
Mvav 76,140,204
X 177
Mvav 44,140,196
W 3
D 4,20,94,0
Mvav 48,144,196
X 178
Mvav 80,144,204
Ra 140,242,11264
Mvav 112,144,212
D 5,20,94,1024
K 194,176,177
Mvav 144,144,220
X 179
Mvav 148,148,220
X 180
Mvav 116,148,212
Ra 144,242,12288
D 6,20,94,2048
Mvav 84,148,204
K 195,178,179
Mvav 52,148,196
X 181
Mvav 56,152,196
D 7,20,94,3072
Ra 148,242,13312
Mvav 88,152,204
X 182
Mvav 120,152,212
s_add_u32 s94, s94, 221184
K 202,180,181
Mvav 152,152,220
X 183
Mvav 156,156,220
X 184
Mvav 124,156,212
s_add_u32 m0, s95, 4096
Ra 152,242,14336
Mvav 92,156,204
K 203,182,183
Mvav 60,156,196
W 3
X 185
Cvaa 160,128,0,224
D 4,20,94,0
Ra 156,242,15360
Mvaa 160,132,4
X 186
Cvaa 164,132,20,228
K 210,184,185
Mvaa 164,128,16
X 187
Cvaa 168,128,32,232
D 5,20,94,1024
Mvaa 168,132,36
X 188
Cvaa 172,132,52,236
D 6,20,94,2048
K 211,186,187
Mvaa 172,128,48
X 189
Mvaa 160,136,8
Ra 128,242,24576
Ra 132,242,25600
Mvaa 160,140,12
X 190
Mvaa 164,140,28
D 7,20,94,3072
K 218,188,189
Mvaa 164,136,24
s_add_u32 s94, s94, 221184
X 191
Mvaa 168,136,40
Mvaa 168,140,44
X 160
Mvaa 172,140,60
s_add_u32 m0, s95, 8192
K 219,190,191
Mvaa 172,136,56
W 2
X 161
Cvaa 176,144,0,224
Ra 136,242,26624
Ra 140,242,27648
Mvaa 176,148,4
X 162
Cvaa 180,148,20,228
D 4,20,94,0
K 196,160,161
Mvaa 180,144,16
X 163
Cvaa 184,144,32,232
D 5,20,94,1024
Mvaa 184,148,36
X 164
Cvaa 188,148,52,236
K 197,162,163
Mvaa 188,144,48
D 6,20,94,2048
Ra 144,242,28672
Mvaa 176,152,8
X 165
Mvaa 176,156,12
Ra 148,242,29696
D 7,20,94,3072
Mvaa 180,156,28
X 166
Mvaa 180,152,24
K 204,164,165
Mvaa 184,152,40
s_add_u32 s94, s94, 221184
X 167
Mvaa 184,156,44
X 168
Mvaa 188,156,60
Mvaa 188,152,56
W 2
s_add_u32 m0, s95, 12288
K 205,166,167
Mvav 32,128,192
X 169
Mvav 64,128,200
Ra 152,242,30720
Ra 156,242,31744
Mvav 96,128,208
X 170
Mvav 128,128,216
D 4,20,94,0
K 212,168,169
Mvav 132,132,216
X 171
Mvav 100,132,208
D 5,20,94,1024
Mvav 68,132,200
X 172
Mvav 36,132,192
K 213,170,171
Mvav 40,136,192
X 173
Mvav 72,136,200
D 6,20,94,2048
Mvav 104,136,208
X 174
Mvav 136,136,216
D 7,20,94,3072
K 220,172,173
Mvav 140,140,216
X 175
Mvav 108,140,208
s_add_u32 s94, s94, 221184
X 176
Mvav 76,140,200
W 0
s_barrier
v_xor_b32 v242, 0x10000, v242
Mvav 44,140,192
Ra 128,242,0
K 221,174,175
Mvav 48,144,192
X 177
Mvav 80,144,200
v_bfe_u32 v11, v1, 4, 3
Mvav 112,144,208
X 178
Mvav 144,144,216
Ra 132,242,1024
v_lshlrev_b32 v11, 3, v11
Mvav 148,148,216
K 198,176,177
Mvav 116,148,208
X 179
Mvav 84,148,200
X 180
Mvav 52,148,192
Ra 136,242,2048
buffer_load_dwordx2 v[8:9], v11, s[8:11], 0 offen offset:3840 sc1
Mvav 56,152,192
Ra 140,242,3072
K 199,178,179
Mvav 88,152,200
X 181
Mvav 120,152,208
X 182
Mvav 152,152,216
K 206,180,181
Mvav 156,156,216
X 183
Mvav 124,156,208
Mvav 92,156,200
K 207,182,183
Mvav 60,156,192
W 0
Cvaa 160,128,0,224
s_add_u32 m0, s95, 16384
Ra 144,242,4096
Ra 148,242,5120
Mvaa 160,132,4
s_xor_b32 s62, s62, 0x10000
Ra 152,242,6144
Cvaa 164,132,20,228
X 184
Mvaa 164,128,16
X 185
Cvaa 168,128,32,232
X 186
Mvaa 168,132,36
D 4,20,94,0
K 214,184,185
Cvaa 172,132,52,236
Ra 156,242,7168
Mvaa 172,128,48
X 187
Mvaa 160,136,8
X 188
Mvaa 160,140,12
D 5,20,94,1024
Ra 128,242,16384
Mvaa 164,140,28
K 215,186,187
Mvaa 164,136,24
X 189
Mvaa 168,136,40
X 190
Mvaa 168,140,44
D 6,20,94,2048
Ra 132,242,17408
Mvaa 172,140,60
K 222,188,189
Mvaa 172,136,56
X 191
v_smfmac_f32_16x16x64_bf16 a[160:163], a[176:179], v[192:199], v240
Ra 136,242,18432
K 223,190,191
v_smfmac_f32_16x16x64_bf16 a[164:167], a[176:179], v[200:207], v240
D 7,20,94,3072
v_smfmac_f32_16x16x64_bf16 a[168:171], a[176:179], v[208:215], v240
s_add_u32 s94, s94, 221184
v_mul_u32_u24 v10, 160, v11
v_smfmac_f32_16x16x64_bf16 a[172:175], a[176:179], v[216:223], v240
W 3
X 160
Cvaa 176,144,0,224
X 161
Mvaa 176,148,4
Ra 140,242,19456
v_lshrrev_b32 v12, 7, v1
Cvaa 180,148,20,228
X 162
Mvaa 180,144,16
K 192,160,161
v_lshl_add_u32 v10, v12, 4, v10
Cvaa 184,144,32,232
s_mul_i32 s82, s14, 5120
X 163
Mvaa 184,148,36
X 164
Cvaa 188,148,52,236
K 193,162,163
Mvaa 188,144,48
s_mul_i32 s81, s14, 640
X 165
Mvaa 176,152,8
X 166
Mvaa 176,156,12
s_add_u32 s81, s81, 118784
Ra 144,242,20480
Ra 148,242,21504
Mvaa 180,156,28
K 200,164,165
Mvaa 180,152,24
X 167
Mvaa 184,152,40
s_waitcnt vmcnt(4)
v_cmp_eq_u64 vcc, s[12:13], v[8:9]
s_cmp_eq_u64 vcc, -1
s_cbranch_scc0 .Lspin_51%=
.Lok_52%=:
X 168
)"
R"(Mvaa 184,156,44
K 201,166,167
Mvaa 188,156,60
s_add_u32 m0, s82, 53248
X 169
Mvaa 188,152,56
W 2
Ra 152,242,22528
Mvav 32,128,196
X 170
Mvav 64,128,204
Ra 156,242,23552
Dc 10,8,81,0
Mvav 96,128,212
K 208,168,169
Mvav 128,128,220
X 171
Mvav 132,132,220
s_add_u32 m0, s82, 54144
X 172
Mvav 100,132,212
s_add_u32 s64, s64, 1
Ra 128,242,8192
K 209,170,171
Mvav 68,132,204
X 173
Mvav 36,132,196
Dc 10,8,81,128
Mvav 40,136,196
X 174
Mvav 72,136,204
s_add_u32 m0, s82, 55040
Ra 132,242,9216
K 216,172,173
Mvav 104,136,212
X 175
Mvav 136,136,220
Dc 10,8,81,256
Mvav 140,140,220
X 176
Mvav 108,140,212
Ra 136,242,10240
K 217,174,175
Mvav 76,140,204
s_add_u32 m0, s82, 55936
X 177
Mvav 44,140,196
W 3
Mvav 48,144,196
X 178
Mvav 80,144,204
Ra 140,242,11264
Dc 10,8,81,384
Mvav 112,144,212
K 194,176,177
Mvav 144,144,220
s_add_u32 m0, s82, 56832
X 179
Mvav 148,148,220
X 180
Mvav 116,148,212
Ra 144,242,12288
Mvav 84,148,204
Dc 10,8,81,512
K 195,178,179
Mvav 52,148,196
X 181
Mvav 56,152,196
X 182
Mvav 88,152,204
Ra 148,242,13312
Mvav 120,152,212
K 202,180,181
Mvav 152,152,220
X 183
Mvav 156,156,220
X 184
Mvav 124,156,212
Ra 152,242,14336
Mvav 92,156,204
K 203,182,183
Mvav 60,156,196
W 3
X 185
Cvaa 160,128,0,224
X 186
Mvaa 160,132,4
Ra 156,242,15360
Cvaa 164,132,20,228
K 210,184,185
Mvaa 164,128,16
X 187
Cvaa 168,128,32,232
X 188
Mvaa 168,132,36
Cvaa 172,132,52,236
K 211,186,187
Mvaa 172,128,48
X 189
Mvaa 160,136,8
Ra 128,242,24576
Ra 132,242,25600
Mvaa 160,140,12
X 190
Mvaa 164,140,28
K 218,188,189
Mvaa 164,136,24
X 191
Mvaa 168,136,40
Mvaa 168,140,44
Mvaa 172,140,60
X 160
Mvaa 172,136,56
W 2
Ra 136,242,26624
K 219,190,191
Cvaa 176,144,0,224
X 161
Mvaa 176,148,4
Ra 140,242,27648
v_lshrrev_b32 v8, 8, v1
Cvaa 180,148,20,228
X 162
Mvaa 180,144,16
K 196,160,161
Cvaa 184,144,32,232
X 163
Mvaa 184,148,36
v_bfe_u32 v9, v1, 6, 2
Cvaa 188,148,52,236
X 164
Mvaa 188,144,48
K 197,162,163
v_lshl_add_u32 v9, v8, 3, v9
Mvaa 176,152,8
X 165
Mvaa 176,156,12
Ra 144,242,28672
Ra 148,242,29696
Mvaa 180,156,28
X 166
Mvaa 180,152,24
K 204,164,165
v_lshlrev_b32 v11, 6, v9
Mvaa 184,152,40
X 167
Mvaa 184,156,44
X 168
Mvaa 188,156,60
v_xor_b32 v9, s14, v8
Mvaa 188,152,56
W 2
Ra 152,242,30720
K 205,166,167
Mvav 32,128,192
X 169
Mvav 64,128,200
Ra 156,242,31744
v_and_b32 v9, 1, v9
Mvav 96,128,208
X 170
Mvav 128,128,216
K 212,168,169
Mvav 132,132,216
X 171
Mvav 100,132,208
v_lshl_add_u32 v11, v9, 5, v11
Mvav 68,132,200
X 172
Mvav 36,132,192
K 213,170,171
Mvav 40,136,192
X 173
Mvav 72,136,200
v_bfe_u32 v9, v1, 4, 2
Mvav 104,136,208
X 174
Mvav 136,136,216
K 220,172,173
v_lshl_add_u32 v12, v9, 3, v11
Mvav 140,140,216
X 175
Mvav 108,140,208
X 176
Mvav 76,140,200
s_waitcnt vmcnt(0) lgkmcnt(0)
s_barrier
v_xor_b32 v242, 0x10000, v242
Mvav 44,140,192
v_add_u32 v11, 98304, v12
K 221,174,175
Mvav 48,144,192
Ra 128,242,0
Mvav 80,144,200
X 177
Mvav 112,144,208
X 178
Mvav 144,144,216
Ra 132,242,1024
v_and_b32 v13, 0x70, v1
Mvav 148,148,216
K 198,176,177
Mvav 116,148,208
X 179
Mvav 84,148,200
Ra 136,242,2048
v_lshl_add_u32 v13, v8, 7, v13
Mvav 52,148,192
X 180
Mvav 56,152,192
Ra 140,242,3072
K 199,178,179
Mvav 88,152,200
X 181
Mvav 120,152,208
X 182
Mvav 152,152,216
K 206,180,181
Mvav 156,156,216
X 183
Mvav 124,156,208
Mvav 92,156,200
K 207,182,183
Mvav 60,156,192
W 0
Cvaa 160,128,0,224
s_cmp_eq_u32 s14, 0
s_cselect_b32 s95, 106496, 0
Ra 144,242,4096
Ra 148,242,5120
Mvaa 160,132,4
s_xor_b32 s62, s62, 0x10000
X 184
Cvaa 164,132,20,228
Ta 184,11,0
Ta 186,11,256
Mvaa 164,128,16
s_add_u32 s95, s95, 32768
Ta 188,11,20480
Ta 190,11,20736
Cvaa 168,128,32,232
Ra 192,13,53248
Ta 196,11,2048
Mvaa 168,132,36
s_add_u32 m0, s95, 0
X 185
Cvaa 172,132,52,236
Ta 198,11,2304
Ta 200,11,22528
Mvaa 172,128,48
W 3
X 186
Zaaa 232,184,192
Ta 202,11,22784
K 214,184,185
Zaaa 236,188,192
Ra 204,13,53760
Ra 152,242,6144
Mvaa 160,136,8
X 187
Mvaa 160,140,12
Ta 208,11,4096
D 4,20,94,0
Mvaa 164,140,28
X 188
Mvaa 164,136,24
Ta 210,11,4352
D 5,20,94,1024
Mvaa 168,136,40
Ra 156,242,7168
K 215,186,187
Mvaa 168,140,44
X 189
Mvaa 172,140,60
Ta 212,11,24576
D 6,20,94,2048
Mvaa 172,136,56
X 190
v_smfmac_f32_16x16x64_bf16 a[160:163], a[176:179], v[192:199], v240
W 5
Ta 214,11,24832
D 7,20,94,3072
Maaa 232,196,204
s_add_u32 s94, s94, 221184
Ra 216,13,54272
K 222,188,189
Maaa 236,200,204
s_add_u32 m0, s95, 4096
X 191
v_smfmac_f32_16x16x64_bf16 a[164:167], a[176:179], v[200:207], v240
X 160
K 223,190,191
v_smfmac_f32_16x16x64_bf16 a[168:171], a[176:179], v[208:215], v240
Ra 128,242,16384
Ta 220,11,6144
v_smfmac_f32_16x16x64_bf16 a[172:175], a[176:179], v[216:223], v240
W 5
Ta 222,11,6400
Ra 132,242,17408
Cvaa 176,144,0,224
X 161
Mvaa 176,148,4
D 4,20,94,0
Ta 224,11,26624
Cvaa 180,148,20,228
X 162
Mvaa 180,144,16
D 5,20,94,1024
K 192,160,161
Cvaa 184,144,32,232
Ta 226,11,26880
Ra 228,13,54784
Mvaa 184,148,36
W 7
X 163
Maaa 232,208,216
D 6,20,94,2048
Ra 136,242,18432
Maaa 236,212,216
X 164
Cvaa 188,148,52,236
D 7,20,94,3072
Ta 184,11,8192
Mvaa 188,144,48
s_add_u32 s94, s94, 221184
Ra 140,242,19456
K 193,162,163
Mvaa 176,152,8
s_add_u32 m0, s95, 8192
X 165
Mvaa 176,156,12
Ta 186,11,8448
Ta 188,11,28672
Mvaa 180,156,28
X 166
Mvaa 180,152,24
Ra 144,242,20480
K 200,164,165
Mvaa 184,152,40
X 167
Mvaa 184,156,44
Ta 190,11,28928
D 4,20,94,0
Mvaa 188,156,60
W 7
X 168
Maaa 232,220,228
s_add_u32 s64, s64, 1
Ra 192,13,55296
D 5,20,94,1024
Maaa 236,224,228
Ta 196,11,10240
K 201,166,167
Mvaa 188,152,56
W 6
X 169
Mvav 32,128,196
D 6,20,94,2048
Ra 148,242,21504
Mvav 64,128,204
X 170
Mvav 96,128,212
Ta 198,11,10496
D 7,20,94,3072
Mvav 128,128,220
s_add_u32 s94, s94, 221184
Ta 200,11,30720
K 208,168,169
Mvav 132,132,220
s_add_u32 m0, s95, 12288
X 171
Mvav 100,132,212
Ra 152,242,22528
Ta 202,11,30976
Mvav 68,132,204
X 172
Mvav 36,132,196
W 6
Ra 204,13,55808
K 209,170,171
Maaa 232,184,192
X 173
Maaa 236,188,192
D 4,20,94,0
Ra 156,242,23552
Mvav 40,136,196
X 174
Mvav 72,136,204
Ta 208,11,12288
D 5,20,94,1024
Mvav 104,136,212
Ta 210,11,12544
K 216,172,173
Mvav 136,136,220
X 175
Mvav 140,140,220
D 6,20,94,2048
Ra 128,242,8192
Mvav 108,140,212
Ta 212,11,32768
)"
R"(Ta 214,11,33024
Mvav 76,140,204
X 176
Mvav 44,140,196
W 5
D 7,20,94,3072
K 217,174,175
Mvav 48,144,196
s_add_u32 s94, s94, 221184
X 177
Maaa 232,196,204
s_add_u32 m0, s95, 16384
Ra 132,242,9216
Ra 216,13,56320
Maaa 236,200,204
X 178
Mvav 80,144,204
Ta 220,11,14336
K 194,176,177
Mvav 112,144,212
X 179
Mvav 144,144,220
D 4,20,94,0
Ta 222,11,14592
Mvav 148,148,220
Ra 136,242,10240
Ta 224,11,34816
Mvav 116,148,212
X 180
Mvav 84,148,204
D 5,20,94,1024
K 195,178,179
Mvav 52,148,196
X 181
Mvav 56,152,196
D 6,20,94,2048
Ta 226,11,35072
Mvav 88,152,204
W 5
Ra 140,242,11264
Ra 228,13,56832
Maaa 232,208,216
X 182
Maaa 236,212,216
D 7,20,94,3072
K 202,180,181
Mvav 120,152,212
s_add_u32 s94, s94, 221184
X 183
Mvav 152,152,220
Ta 184,11,16384
Ra 144,242,12288
Mvav 156,156,220
X 184
Mvav 124,156,212
Ta 186,11,16640
K 203,182,183
Mvav 92,156,204
X 185
Mvav 60,156,196
W 4
Ta 188,11,36864
Ra 148,242,13312
Cvaa 160,128,0,224
X 186
Mvaa 160,132,4
Ta 190,11,37120
K 210,184,185
Cvaa 164,132,20,228
W 6
Ra 192,13,57344
Ta 196,11,18432
Maaa 232,220,228
X 187
Maaa 236,224,228
X 188
Mvaa 164,128,16
Ra 152,242,14336
K 211,186,187
Cvaa 168,128,32,232
Ta 198,11,18688
Ta 200,11,38912
Mvaa 168,132,36
X 189
Cvaa 172,132,52,236
X 190
Mvaa 172,128,48
Ra 156,242,15360
K 218,188,189
Mvaa 160,136,8
Ta 202,11,39168
Ra 204,13,57856
Mvaa 160,140,12
X 191
Mvaa 164,140,28
Ra 128,242,24576
Mvaa 164,136,24
W 8
Maaa 232,184,192
Ra 132,242,25600
Maaa 236,188,192
X 160
Mvaa 168,136,40
K 219,190,191
Mvaa 168,140,44
X 161
Mvaa 172,140,60
X 162
Mvaa 172,136,56
W 4
Ra 136,242,26624
K 196,160,161
Cvaa 176,144,0,224
X 163
Mvaa 176,148,4
X 164
Cvaa 180,148,20,228
Ra 140,242,27648
K 197,162,163
Mvaa 180,144,16
X 165
Cvaa 184,144,32,232
W 4
Maaa 232,196,204
X 166
Maaa 236,200,204
Mvaa 184,148,36
K 204,164,165
Cvaa 188,148,52,236
X 167
Mvaa 188,144,48
Ra 144,242,28672
Mvaa 176,152,8
X 168
Mvaa 176,156,12
Ra 148,242,29696
Mvaa 180,156,28
K 205,166,167
Mvaa 180,152,24
X 169
Mvaa 184,152,40
Mvaa 184,156,44
X 170
Mvaa 188,156,60
K 212,168,169
Mvaa 188,152,56
W 2
X 171
Mvav 32,128,192
Ra 152,242,30720
Ra 156,242,31744
Mvav 64,128,200
X 172
Mvav 96,128,208
Mvav 128,128,216
K 213,170,171
Mvav 132,132,216
X 173
Mvav 100,132,208
Mvav 68,132,200
X 174
Mvav 36,132,192
Mvav 40,136,192
K 220,172,173
Mvav 72,136,200
X 175
Mvav 104,136,208
Mvav 136,136,216
X 176
Mvav 140,140,216
Mvav 108,140,208
K 221,174,175
Mvav 76,140,200
s_waitcnt vmcnt(0) lgkmcnt(0)
s_barrier
v_xor_b32 v242, 0x10000, v242
Mvav 44,140,192
X 177
Mvav 48,144,192
X 178
Mvav 80,144,200
Ra 128,242,0
K 198,176,177
Mvav 112,144,208
X 179
Mvav 144,144,216
Ra 132,242,1024
Mvav 148,148,216
X 180
Mvav 116,148,208
Ra 136,242,2048
Mvav 84,148,200
K 199,178,179
Mvav 52,148,192
X 181
Mvav 56,152,192
Ra 140,242,3072
Mvav 88,152,200
X 182
Mvav 120,152,208
Mvav 152,152,216
K 206,180,181
Mvav 156,156,216
X 183
Mvav 124,156,208
Mvav 92,156,200
K 207,182,183
Mvav 60,156,192
W 0
Cvaa 160,128,0,224
s_mov_b32 s20, s34
Ra 144,242,4096
Ra 148,242,5120
Mvaa 160,132,4
s_xor_b32 s62, s62, 0x10000
X 184
Cvaa 164,132,20,228
s_and_b32 s21, s35, 0xffff
Ta 184,11,40960
Ta 186,11,41216
Mvaa 164,128,16
Ta 188,12,32768
Ta 190,12,33024
Cvaa 168,128,32,232
s_mov_b32 s22, 2211840
Ra 192,13,58368
Ta 196,11,43008
Mvaa 168,132,36
s_mov_b32 s23, 0x20000
X 185
Cvaa 172,132,52,236
Ta 198,11,43264
Ta 200,12,34816
Mvaa 172,128,48
W 3
X 186
Maaa 232,184,192
Ta 202,12,35072
K 214,184,185
Maaa 236,188,192
Ra 204,13,58880
Ra 152,242,6144
Mvaa 160,136,8
X 187
Mvaa 160,140,12
Ta 208,11,45056
v_lshrrev_b32 v4, 8, v1
Mvaa 164,140,28
X 188
Mvaa 164,136,24
Ta 210,11,45312
v_lshlrev_b32 v5, 5, v4
Mvaa 168,136,40
v_xor_b32 v5, v5, v1
K 215,186,187
Mvaa 168,140,44
Ra 156,242,7168
Ta 212,12,36864
Mvaa 172,140,60
X 189
Mvaa 172,136,56
v_and_b32 v5, 0xf0, v5
v_mul_u32_u24 v4, 1280, v4
v_smfmac_f32_16x16x64_bf16 a[160:163], a[176:179], v[192:199], v240
W 5
X 190
Maaa 232,196,204
Ta 214,12,37120
v_add_u32 v4, v4, v5
Maaa 236,200,204
Ra 216,13,59392
K 222,188,189
v_smfmac_f32_16x16x64_bf16 a[164:167], a[176:179], v[200:207], v240
X 191
X 160
K 223,190,191
v_smfmac_f32_16x16x64_bf16 a[168:171], a[176:179], v[208:215], v240
Ra 128,242,16384
v_xor_b32 v5, 128, v4
v_smfmac_f32_16x16x64_bf16 a[172:175], a[176:179], v[216:223], v240
W 4
Ta 220,11,47104
v_add_u32 v6, 8192, v4
Cvaa 176,144,0,224
Ta 222,11,47360
v_add_u32 v7, 12288, v5
Mvaa 176,148,4
Ra 132,242,17408
v_add_u32 v5, 4096, v5
Cvaa 180,148,20,228
s_mul_i32 s94, s85, 40960
X 161
Mvaa 180,144,16
s_mul_i32 s81, s14, 20480
Ta 224,12,38912
Ta 226,12,39168
Cvaa 184,144,32,232
s_add_u32 s94, s94, s81
X 162
Mvaa 184,148,36
W 6
s_lshl_b32 s95, s14, 12
K 192,160,161
Maaa 232,208,216
s_add_u32 s95, s95, 98304
X 163
Maaa 236,212,216
s_add_u32 m0, s95, 0
Ra 228,13,59904
Ra 136,242,18432
Cvaa 188,148,52,236
X 164
Mvaa 188,144,48
Ta 184,11,49152
K 193,162,163
Mvaa 176,152,8
X 165
Mvaa 176,156,12
Ra 140,242,19456
Ta 186,11,49408
Mvaa 180,156,28
D 4,20,94,0
Ta 188,12,40960
Mvaa 180,152,24
X 166
Mvaa 184,152,40
D 5,20,94,1024
K 200,164,165
Mvaa 184,156,44
X 167
Mvaa 188,156,60
W 5
D 6,20,94,2048
Ra 144,242,20480
Maaa 232,220,228
s_add_u32 s64, s64, 1
Ta 190,12,41216
Ra 192,13,60416
Maaa 236,224,228
D 7,20,94,3072
Ta 196,11,51200
Mvaa 188,152,56
W 6
X 168
Mvav 32,128,196
s_add_u32 s94, s94, 256
s_add_u32 m0, s95, 8192
K 201,166,167
Mvav 64,128,204
X 169
Mvav 96,128,212
D 4,20,94,0
Ra 148,242,21504
Mvav 128,128,220
X 170
Mvav 132,132,220
D 5,20,94,1024
Ta 198,11,51456
Mvav 100,132,212
Ta 200,12,43008
K 208,168,169
Mvav 68,132,204
D 6,20,94,2048
Ra 152,242,22528
Mvav 36,132,196
W 5
X 171
Maaa 232,184,192
D 7,20,94,3072
Ta 202,12,43264
Maaa 236,188,192
s_add_u32 s94, s94, 256
X 172
Mvav 40,136,196
s_add_u32 m0, s95, 16384
Ra 204,13,60928
K 209,170,171
Mvav 72,136,204
X 173
Mvav 104,136,212
Ra 156,242,23552
D 4,20,94,0
Mvav 136,136,220
X 174
Mvav 140,140,220
D 5,20,94,1024
Ta 208,11,53248
Mvav 108,140,212
Ta 210,11,53504
K 216,172,173
Mvav 76,140,204
D 6,20,94,2048
Ra 128,242,8192
Mvav 44,140,196
W 3
X 175
Mvav 48,144,196
D 7,20,94,3072
Ta 212,12,45056
Maaa 232,196,204
)"
R"(s_add_u32 s94, s94, 256
X 176
Maaa 236,200,204
s_add_u32 m0, s95, 24576
Ta 214,12,45312
K 217,174,175
Mvav 80,144,204
D 4,20,94,0
Ra 132,242,9216
Mvav 112,144,212
X 177
Mvav 144,144,220
Ra 216,13,61440
D 5,20,94,1024
Mvav 148,148,220
X 178
Mvav 116,148,212
D 6,20,94,2048
Ta 220,11,55296
Mvav 84,148,204
Ta 222,11,55552
K 194,176,177
Mvav 52,148,196
D 7,20,94,3072
Ra 136,242,10240
Mvav 56,152,196
s_add_u32 s94, s94, 256
X 179
Mvav 88,152,204
W 3
X 180
Maaa 232,208,216
s_add_u32 m0, s95, 32768
Ta 224,12,47104
K 195,178,179
Maaa 236,212,216
D 4,20,94,0
Ta 226,12,47360
Mvav 120,152,212
Ra 140,242,11264
Ra 228,13,61952
Mvav 152,152,220
X 181
Mvav 156,156,220
D 5,20,94,1024
Ta 184,11,57344
Mvav 124,156,212
X 182
Mvav 92,156,204
D 6,20,94,2048
K 202,180,181
Mvav 60,156,196
W 2
X 183
Cvaa 160,128,0,224
D 7,20,94,3072
Ra 144,242,12288
Mvaa 160,132,4
s_add_u32 s94, s94, 256
X 184
Cvaa 164,132,20,228
W 2
Ta 186,11,57600
K 203,182,183
Maaa 232,220,228
X 185
Maaa 236,224,228
Ta 188,12,49152
Ra 148,242,13312
Mvaa 164,128,16
X 186
Cvaa 168,128,32,232
Ta 190,12,49408
K 210,184,185
Mvaa 168,132,36
Ra 192,13,62464
Ta 196,11,59392
Cvaa 172,132,52,236
X 187
Mvaa 172,128,48
X 188
Mvaa 160,136,8
Ra 152,242,14336
K 211,186,187
Mvaa 160,140,12
Ta 198,11,59648
Ta 200,12,51200
Mvaa 164,140,28
X 189
Mvaa 164,136,24
W 4
X 190
Maaa 232,184,192
Ra 156,242,15360
K 218,188,189
Maaa 236,188,192
Ta 202,12,51456
Ra 204,13,62976
Mvaa 168,136,40
X 191
Mvaa 168,140,44
Ra 128,242,24576
Ra 132,242,25600
Mvaa 172,140,60
X 160
Mvaa 172,136,56
W 4
Ra 136,242,26624
K 219,190,191
Cvaa 176,144,0,224
X 161
Mvaa 176,148,4
X 162
Cvaa 180,148,20,228
Ra 140,242,27648
K 196,160,161
Mvaa 180,144,16
X 163
Cvaa 184,144,32,232
W 4
X 164
Maaa 232,196,204
K 197,162,163
Maaa 236,200,204
X 165
Mvaa 184,148,36
X 166
Cvaa 188,148,52,236
K 204,164,165
Mvaa 188,144,48
X 167
Mvaa 176,152,8
Ra 144,242,28672
G 252,232
Mvaa 176,156,12
G 253,233
G 254,234
Mvaa 180,156,28
X 168
Mvaa 180,152,24
G 255,235
G 8,236
Mvaa 184,152,40
Ra 148,242,29696
G 9,237
Mvaa 184,156,44
G 10,238
G 14,239
Mvaa 188,156,60
K 252,252,8
K 253,253,9
Mvaa 188,152,56
W 2
K 205,166,167
K 254,254,10
Mvav 32,128,192
K 255,255,14
v_and_b32 v8, 0xffff0000, v252
Mvav 64,128,200
v_and_b32 v9, 0xffff0000, v253
v_and_b32 v10, 0xffff0000, v254
Mvav 96,128,208
X 169
Mvav 128,128,216
v_and_b32 v14, 0xffff0000, v255
v_lshlrev_b32 v252, 16, v252
Mvav 132,132,216
v_lshlrev_b32 v253, 16, v253
v_lshlrev_b32 v254, 16, v254
Mvav 100,132,208
v_lshlrev_b32 v255, 16, v255
v_mul_f32 v15, 0xbfb8aa3b, v252
Mvav 68,132,200
v_mul_f32 v2, 0xbfb8aa3b, v253
v_mul_f32 v3, 0xbfb8aa3b, v254
Mvav 36,132,192
X 170
Mvav 40,136,192
v_mul_f32 v27, 0xbfb8aa3b, v255
K 212,168,169
Mvav 72,136,200
X 15
Mvav 104,136,208
Ra 152,242,30720
Ra 156,242,31744
Mvav 136,136,216
X 2
X 3
X 27
v_add_f32 v15, 1.0, v15
v_add_f32 v2, 1.0, v2
v_add_f32 v3, 1.0, v3
v_add_f32 v27, 1.0, v27
v_rcp_f32 v15, v15
v_rcp_f32 v2, v2
v_rcp_f32 v3, v3
v_rcp_f32 v27, v27
U 252,252,15
U 253,253,2
U 254,254,3
U 255,255,27
K 252,252,252
K 253,253,253
K 254,254,254
K 255,255,255
v_lshlrev_b32 v252, 16, v252
v_lshlrev_b32 v253, 16, v253
v_lshlrev_b32 v254, 16, v254
v_lshlrev_b32 v255, 16, v255
U 252,252,8
U 253,253,9
U 254,254,10
U 255,255,14
K 246,252,253
K 247,254,255
s_lshl_b32 s81, s14, 3
v_add_u32 v2, s81, v1
s_mov_b32 s86, 0x00ff00ff
s_mov_b32 s87, 0x00ff00ff
v_cndmask_b32_e64 v252, 0, v246, s[86:87]
v_cndmask_b32_e64 v253, 0, v247, s[86:87]
v_cndmask_b32_e64 v246, v246, 0, s[86:87]
v_cndmask_b32_e64 v247, v247, 0, s[86:87]
ds_write_b64 v2, v[252:253] offset:63488
ds_write_b64 v2, v[246:247] offset:64512
Mvav 140,140,216
X 171
Mvav 108,140,208
X 172
Mvav 76,140,200
s_waitcnt vmcnt(0) lgkmcnt(0)
s_barrier
v_xor_b32 v242, 0x10000, v242
Mvav 44,140,192
Ra 128,242,0
K 213,170,171
Mvav 48,144,192
X 173
Mvav 80,144,200
X 174
Mvav 112,144,208
Ra 132,242,1024
K 220,172,173
Mvav 144,144,216
X 175
Mvav 148,148,216
X 176
Mvav 116,148,208
Ra 136,242,2048
K 221,174,175
Mvav 84,148,200
X 177
Mvav 52,148,192
X 178
Mvav 56,152,192
Ra 140,242,3072
K 198,176,177
Mvav 88,152,200
X 179
Mvav 120,152,208
X 180
Mvav 152,152,216
K 199,178,179
Mvav 156,156,216
X 181
Mvav 124,156,208
X 182
Mvav 92,156,200
K 206,180,181
Mvav 60,156,192
X 183
s_nop 0
K 207,182,183
W 0
Cvaa 160,128,0,224
Ra 144,242,4096
Ra 148,242,5120
Mvaa 160,132,4
s_xor_b32 s62, s62, 0x10000
X 184
Cvaa 164,132,20,228
Ra 216,1,63488
Ra 220,1,64512
Mvaa 164,128,16
v_lshrrev_b32 v8, 6, v1
v_and_b32 v9, 7, v8
Cvaa 168,128,32,232
s_lshl_b32 s81, s14, 2
X 185
Mvaa 168,132,36
v_xor_b32 v9, s81, v9
v_lshlrev_b32 v9, 5, v9
Cvaa 172,132,52,236
X 186
Mvaa 172,128,48
v_lshl_add_u32 v4, v8, 8, v9
v_bfe_u32 v9, v1, 4, 2
Mvaa 160,136,8
v_lshl_add_u32 v4, v9, 3, v4
v_add_u32 v4, 98304, v4
Mvaa 160,140,12
K 214,184,185
v_xor_b32 v5, 32, v4
Mvaa 164,140,28
v_xor_b32 v6, 64, v4
v_xor_b32 v7, 96, v4
Ta 184,4,0
Ta 186,4,4096
Mvaa 164,136,24
Ta 188,5,0
Ta 190,5,4096
Mvaa 168,136,40
Ra 152,242,6144
Ta 192,6,0
Mvaa 168,140,44
W 4
X 187
Zaaa 232,184,216
Ta 194,6,4096
v_and_b32 v11, 0x70, v1
Mvaa 172,140,60
v_mul_u32_u24 v11, 160, v11
v_lshrrev_b32 v10, 8, v1
Mvaa 172,136,56
X 188
v_smfmac_f32_16x16x64_bf16 a[160:163], a[176:179], v[192:199], v240
Ra 156,242,7168
W 4
Maaa 232,188,220
K 215,186,187
v_lshl_add_u32 v11, v10, 4, v11
v_smfmac_f32_16x16x64_bf16 a[164:167], a[176:179], v[200:207], v240
X 189
X 190
K 222,188,189
X 191
X 160
K 223,190,191
v_smfmac_f32_16x16x64_bf16 a[168:171], a[176:179], v[208:215], v240
Ta 196,7,0
v_bfe_u32 v10, v1, 7, 1
v_smfmac_f32_16x16x64_bf16 a[172:175], a[176:179], v[216:223], v240
W 2
Ta 198,7,4096
v_lshl_add_u32 v10, v10, 6, v11
Zaaa 236,192,216
W 2
s_mul_i32 s94, s85, 20480
s_lshl_b32 s82, s14, 8
Cvaa 176,144,0,224
s_add_u32 s94, s94, s82
Ta 200,4,8192
Ta 202,4,12288
Mvaa 176,148,4
s_add_u32 s94, s94, 2752512
s_lshl_b32 s71, s85, 4
Ra 128,242,16384
Cvaa 180,148,20,228
W 3
s_lshl_b32 s82, s14, 3
Ta 204,5,8192
Maaa 236,196,220
s_add_u32 s71, s71, s82
s_add_u32 s71, s71, 2560
Ta 206,5,12288
Mvaa 180,144,16
X 161
Cvaa 184,144,32,232
Ra 132,242,17408
Ta 208,6,8192
Mvaa 184,148,36
W 5
X 162
Zaaa 240,200,216
Ta 210,6,12288
K 192,160,161
Cvaa 188,148,52,236
buffer_store_dwordx4 a[232:235], v10, s[8:11], s94 offen offset:0 sc1
Ta 212,7,8192
Mvaa 188,144,48
X 163
Mvaa 176,152,8
W 4
X 164
Maaa 240,204,220
Ra 136,242,18432
Ta 214,7,12288
Mvaa 176,156,12
Ta 184,4,16384
K 193,162,163
Mvaa 180,156,28
X 165
Mvaa 180,152,24
W 4
Ta 186,4,20480
Ra 140,242,19456
Zaaa 244,208,216
X 166
Mvaa 184,152,40
buffer_store_dwordx4 a[236:239], v10, s[8:11], s94 offen offset:128 sc1
)"
R"(K 200,164,165
Mvaa 184,156,44
X 167
Mvaa 188,156,60
W 3
Ta 188,5,16384
Ta 190,5,20480
Maaa 244,212,220
s_add_u32 s64, s64, 1
X 168
Mvaa 188,152,56
W 2
Ra 144,242,20480
K 201,166,167
Mvav 32,128,196
Ta 192,6,16384
Ta 194,6,20480
Mvav 64,128,204
X 169
Zaaa 248,184,216
buffer_store_dwordx4 a[240:243], v10, s[8:11], s94 offen offset:512 sc1
Ta 196,7,16384
Mvav 96,128,212
X 170
Mvav 128,128,220
Ra 148,242,21504
K 208,168,169
Mvav 132,132,220
W 5
Ta 198,7,20480
Ta 200,4,24576
Maaa 248,188,220
X 171
Mvav 100,132,212
X 172
Mvav 68,132,204
Ta 202,4,28672
K 209,170,171
Mvav 36,132,196
W 5
Ra 152,242,22528
buffer_store_dwordx4 a[244:247], v10, s[8:11], s94 offen offset:640 sc1
Zaaa 252,192,216
X 173
Mvav 40,136,196
X 174
Mvav 72,136,204
Ta 204,5,24576
Ta 206,5,28672
Mvav 104,136,212
W 5
Ra 156,242,23552
K 216,172,173
Maaa 252,196,220
X 175
Mvav 136,136,220
Ta 208,6,24576
Ta 210,6,28672
Mvav 140,140,220
X 176
Mvav 108,140,212
W 6
buffer_store_dwordx4 a[248:251], v10, s[8:11], s94 offen offset:1024 sc1
K 217,174,175
Zaaa 232,200,216
X 177
Mvav 76,140,204
Ta 212,7,24576
Ra 128,242,8192
Mvav 44,140,196
W 4
Ta 214,7,28672
Ta 184,4,32768
Mvav 48,144,196
X 178
Maaa 232,204,220
Ra 132,242,9216
K 194,176,177
Mvav 80,144,204
X 179
Mvav 112,144,212
Ta 186,4,36864
buffer_store_dwordx4 a[252:255], v10, s[8:11], s94 offen offset:1152 sc1
Mvav 144,144,220
W 6
X 180
Zaaa 236,208,216
Ta 188,5,32768
K 195,178,179
Mvav 148,148,220
X 181
Mvav 116,148,212
Ta 190,5,36864
Ra 136,242,10240
Mvav 84,148,204
W 6
X 182
Maaa 236,212,220
Ta 192,6,32768
K 202,180,181
Mvav 52,148,196
Ta 194,6,36864
Ra 140,242,11264
Mvav 56,152,196
X 183
Mvav 88,152,204
W 6
buffer_store_dwordx4 a[232:235], v10, s[8:11], s94 offen offset:1536 sc1
Ta 196,7,32768
Zaaa 240,184,216
X 184
Mvav 120,152,212
Ta 198,7,36864
K 203,182,183
Mvav 152,152,220
Ra 144,242,12288
Ra 148,242,13312
Mvav 156,156,220
W 8
X 185
Maaa 240,188,220
X 186
Mvav 124,156,212
buffer_store_dwordx4 a[236:239], v10, s[8:11], s94 offen offset:1664 sc1
K 210,184,185
Mvav 92,156,204
X 187
Mvav 60,156,196
W 5
X 188
Zaaa 244,192,216
W 4
Ra 152,242,14336
K 211,186,187
Cvaa 160,128,0,224
X 189
Mvaa 160,132,4
X 190
Cvaa 164,132,20,228
W 3
Ra 156,242,15360
buffer_store_dwordx4 a[240:243], v10, s[8:11], s94 offen offset:2048 sc1
Maaa 244,196,220
K 218,188,189
Mvaa 164,128,16
X 191
Cvaa 168,128,32,232
Mvaa 168,132,36
Cvaa 172,132,52,236
Mvaa 172,128,48
Ra 128,242,24576
Mvaa 160,136,8
Ra 132,242,25600
buffer_store_dwordx4 a[244:247], v10, s[8:11], s94 offen offset:2176 sc1
Mvaa 160,140,12
Mvaa 164,140,28
Mvaa 164,136,24
Mvaa 168,136,40
Mvaa 168,140,44
Mvaa 172,140,60
Mvaa 172,136,56
W 2
Ra 136,242,26624
Cvaa 176,144,0,224
X 160
Mvaa 176,148,4
Ra 140,242,27648
K 219,190,191
Cvaa 180,148,20,228
X 161
Mvaa 180,144,16
X 162
Cvaa 184,144,32,232
K 196,160,161
Mvaa 184,148,36
X 163
Cvaa 188,148,52,236
X 164
Mvaa 188,144,48
Ra 144,242,28672
K 197,162,163
Mvaa 176,152,8
X 165
Mvaa 176,156,12
X 166
Mvaa 180,156,28
Ra 148,242,29696
K 204,164,165
Mvaa 180,152,24
X 167
Mvaa 184,152,40
X 168
Mvaa 184,156,44
K 205,166,167
Mvaa 188,156,60
X 169
Mvaa 188,152,56
W 2
X 170
Mvav 32,128,192
Ra 152,242,30720
K 212,168,169
Mvav 64,128,200
X 171
Mvav 96,128,208
Ra 156,242,31744
Mvav 128,128,216
X 172
Mvav 132,132,216
K 213,170,171
Mvav 100,132,208
X 173
Mvav 68,132,200
X 174
Mvav 36,132,192
Mvav 40,136,192
K 220,172,173
Mvav 72,136,200
X 175
Mvav 104,136,208
Mvav 136,136,216
X 176
Mvav 140,140,216
Mvav 108,140,208
K 221,174,175
Mvav 76,140,200
W 0
s_barrier
v_xor_b32 v242, 0x10000, v242
Mvav 44,140,192
X 177
Mvav 48,144,192
X 178
Mvav 80,144,200
Ra 128,242,0
K 198,176,177
Mvav 112,144,208
X 179
Mvav 144,144,216
Ra 132,242,1024
Mvav 148,148,216
X 180
Mvav 116,148,208
Ra 136,242,2048
Mvav 84,148,200
K 199,178,179
Mvav 52,148,192
X 181
Mvav 56,152,192
Ra 140,242,3072
Mvav 88,152,200
X 182
Mvav 120,152,208
Mvav 152,152,216
K 206,180,181
Mvav 156,156,216
X 183
Mvav 124,156,208
Mvav 92,156,200
s_waitcnt vmcnt(0)
v_mov_b64 v[28:29], s[12:13]
s_mov_b64 exec, 1
buffer_store_dwordx2 v[28:29], off, s[8:11], s71 sc1
s_mov_b64 exec, -1
K 207,182,183
Mvav 60,156,192
s_mov_b32 s85, 99
s_lshl_b32 s65, s57, 1
s_sub_u32 s65, s65, 1
s_branch .Ll0t_46%=
.Lf1r_47%=:
s_lshl_b32 s92, s58, 2
s_add_u32 s91, s92, s51
s_add_u32 s95, s92, 0
s_and_b32 s95, s95, 15
s_mul_i32 s95, s95, 40960
s_mul_i32 s71, s51, 0
s_add_u32 s71, s71, 133120
s_add_u32 m0, s71, s48
s_lshl2_add_u32 s94, s48, s95
D 16,40,94,0
s_add_u32 s95, s92, 1
s_and_b32 s95, s95, 15
s_mul_i32 s95, s95, 40960
s_mul_i32 s71, s51, 0
s_add_u32 s71, s71, 143360
D 17,40,94,1024
D 18,40,94,2048
D 19,40,94,3072
s_add_u32 m0, m0, 4096
s_nop 0
buffer_load_dwordx4 v20, s[40:43], s94 offen lds
s_add_u32 m0, s71, s48
s_lshl2_add_u32 s94, s48, s95
D 16,40,94,0
s_add_u32 s95, s92, 2
s_and_b32 s95, s95, 15
s_mul_i32 s95, s95, 40960
s_mul_i32 s71, s51, 0
s_add_u32 s71, s71, 153600
D 17,40,94,1024
D 18,40,94,2048
D 19,40,94,3072
s_add_u32 m0, m0, 4096
s_nop 0
buffer_load_dwordx4 v20, s[40:43], s94 offen lds
s_add_u32 m0, s71, s48
s_lshl2_add_u32 s94, s48, s95
D 16,40,94,0
D 17,40,94,1024
D 18,40,94,2048
D 19,40,94,3072
s_add_u32 m0, m0, 4096
s_nop 0
buffer_load_dwordx4 v20, s[40:43], s94 offen lds
s_branch .Lf1_40%=
.Lspin_15%=:
s_mov_b32 s84, 0
.Llp_17%=:
s_sleep 2
v_lshrrev_b32 v28, 1, v1
s_lshl_b32 s80, s60, 9
s_add_u32 s80, s80, 40960
buffer_load_dwordx2 v[28:29], v28, s[8:11], s80 offen sc1
s_waitcnt vmcnt(0)
s_add_u32 s84, s84, 1
v_cmp_eq_u32 vcc, s13, v29
s_cmp_eq_u64 vcc, -1
s_cbranch_scc1 .Lok_16%=
s_cmp_lt_u32 s84, 1048576
s_cbranch_scc1 .Llp_17%=
s_branch .Lok_16%=
.Lspin_20%=:
s_mov_b32 s84, 0
.Llp_22%=:
s_sleep 2
v_lshrrev_b32 v28, 1, v1
s_lshl_b32 s80, s60, 9
s_add_u32 s80, s80, 40960
buffer_load_dwordx2 v[28:29], v28, s[8:11], s80 offen sc1
s_waitcnt vmcnt(0)
s_add_u32 s84, s84, 1
v_cmp_eq_u32 vcc, s13, v29
s_cmp_eq_u64 vcc, -1
s_cbranch_scc1 .Lok_21%=
s_cmp_lt_u32 s84, 1048576
s_cbranch_scc1 .Llp_22%=
s_branch .Lok_21%=
.Lrt_23%=:
s_waitcnt vmcnt(0)
s_mov_b32 s86, 40960
buffer_load_dwordx4 v[32:35], v1, s[8:11], s86 offen offset:0 sc1
buffer_load_dwordx4 v[36:39], v1, s[8:11], s86 offen offset:1024 sc1
buffer_load_dwordx4 v[40:43], v1, s[8:11], s86 offen offset:2048 sc1
buffer_load_dwordx4 v[44:47], v1, s[8:11], s86 offen offset:3072 sc1
s_mov_b32 s86, 45056
buffer_load_dwordx4 v[48:51], v1, s[8:11], s86 offen offset:0 sc1
buffer_load_dwordx4 v[52:55], v1, s[8:11], s86 offen offset:1024 sc1
buffer_load_dwordx4 v[56:59], v1, s[8:11], s86 offen offset:2048 sc1
buffer_load_dwordx4 v[60:63], v1, s[8:11], s86 offen offset:3072 sc1
s_mov_b32 s86, 49152
buffer_load_dwordx4 v[64:67], v1, s[8:11], s86 offen offset:0 sc1
buffer_load_dwordx4 v[68:71], v1, s[8:11], s86 offen offset:1024 sc1
buffer_load_dwordx4 v[72:75], v1, s[8:11], s86 offen offset:2048 sc1
buffer_load_dwordx4 v[76:79], v1, s[8:11], s86 offen offset:3072 sc1
s_mov_b32 s86, 53248
buffer_load_dwordx4 v[80:83], v1, s[8:11], s86 offen offset:0 sc1
buffer_load_dwordx4 v[84:87], v1, s[8:11], s86 offen offset:1024 sc1
buffer_load_dwordx4 v[88:91], v1, s[8:11], s86 offen offset:2048 sc1
buffer_load_dwordx4 v[92:95], v1, s[8:11], s86 offen offset:3072 sc1
s_branch .Lrtr_24%=
.Lrt_25%=:
v_lshrrev_b32 v136, 1, v1
s_mov_b32 s84, 0
.LL_27%=:
s_waitcnt vmcnt(0)
v_min3_u32 v96, v33, v35, v95
v_max3_u32 v97, v33, v35, v95
v_min3_u32 v96, v96, v37, v39
v_max3_u32 v97, v97, v37, v39
v_min3_u32 v96, v96, v41, v43
v_max3_u32 v97, v97, v41, v43
v_min3_u32 v96, v96, v45, v47
v_max3_u32 v97, v97, v45, v47
v_min3_u32 v96, v96, v49, v51
v_max3_u32 v97, v97, v49, v51
v_min3_u32 v96, v96, v53, v55
v_max3_u32 v97, v97, v53, v55
v_min3_u32 v96, v96, v57, v59
v_max3_u32 v97, v97, v57, v59
v_min3_u32 v96, v96, v61, v63
v_max3_u32 v97, v97, v61, v63
v_min3_u32 v96, v96, v65, v67
v_max3_u32 v97, v97, v65, v67
v_min3_u32 v96, v96, v69, v71
v_max3_u32 v97, v97, v69, v71
v_min3_u32 v96, v96, v73, v75
v_max3_u32 v97, v97, v73, v75
v_min3_u32 v96, v96, v77, v79
v_max3_u32 v97, v97, v77, v79
)"
R"(v_min3_u32 v96, v96, v81, v83
v_max3_u32 v97, v97, v81, v83
v_min3_u32 v96, v96, v85, v87
v_max3_u32 v97, v97, v85, v87
v_min3_u32 v96, v96, v89, v91
v_max3_u32 v97, v97, v89, v91
v_min_u32 v96, v96, v93
v_max_u32 v97, v97, v93
v_cmp_eq_u32 vcc, s13, v96
v_cmp_eq_u32_e64 s[86:87], s13, v97
s_and_b64 vcc, vcc, s[86:87]
s_cmp_eq_u64 vcc, -1
s_cbranch_scc1 .LL_28%=
s_sleep 2
s_mov_b32 s86, 40960
buffer_load_dwordx4 v[32:35], v1, s[8:11], s86 offen offset:0 sc1
buffer_load_dwordx4 v[36:39], v1, s[8:11], s86 offen offset:1024 sc1
buffer_load_dwordx4 v[40:43], v1, s[8:11], s86 offen offset:2048 sc1
buffer_load_dwordx4 v[44:47], v1, s[8:11], s86 offen offset:3072 sc1
s_mov_b32 s86, 45056
buffer_load_dwordx4 v[48:51], v1, s[8:11], s86 offen offset:0 sc1
buffer_load_dwordx4 v[52:55], v1, s[8:11], s86 offen offset:1024 sc1
buffer_load_dwordx4 v[56:59], v1, s[8:11], s86 offen offset:2048 sc1
buffer_load_dwordx4 v[60:63], v1, s[8:11], s86 offen offset:3072 sc1
s_mov_b32 s86, 49152
buffer_load_dwordx4 v[64:67], v1, s[8:11], s86 offen offset:0 sc1
buffer_load_dwordx4 v[68:71], v1, s[8:11], s86 offen offset:1024 sc1
buffer_load_dwordx4 v[72:75], v1, s[8:11], s86 offen offset:2048 sc1
buffer_load_dwordx4 v[76:79], v1, s[8:11], s86 offen offset:3072 sc1
s_mov_b32 s86, 53248
buffer_load_dwordx4 v[80:83], v1, s[8:11], s86 offen offset:0 sc1
buffer_load_dwordx4 v[84:87], v1, s[8:11], s86 offen offset:1024 sc1
buffer_load_dwordx4 v[88:91], v1, s[8:11], s86 offen offset:2048 sc1
buffer_load_dwordx4 v[92:95], v1, s[8:11], s86 offen offset:3072 sc1
s_add_u32 s84, s84, 1
s_cmp_lt_u32 s84, 1048576
s_cbranch_scc1 .LL_27%=
s_waitcnt vmcnt(0)
.LL_28%=:
v_mov_b32 v96, v32
v_mov_b32 v97, 0
v_mov_b32 v98, 0
v_mov_b32 v100, 0
v_med3_u32 v98, v97, v98, v34
v_med3_u32 v97, v96, v97, v34
v_max_u32 v96, v96, v34
v_med3_u32 v98, v97, v98, v36
v_med3_u32 v97, v96, v97, v36
v_max_u32 v96, v96, v36
v_med3_u32 v98, v97, v98, v38
v_med3_u32 v97, v96, v97, v38
v_max_u32 v96, v96, v38
v_med3_u32 v98, v97, v98, v40
v_med3_u32 v97, v96, v97, v40
v_max_u32 v96, v96, v40
v_med3_u32 v98, v97, v98, v42
v_med3_u32 v97, v96, v97, v42
v_max_u32 v96, v96, v42
v_med3_u32 v98, v97, v98, v44
v_med3_u32 v97, v96, v97, v44
v_max_u32 v96, v96, v44
v_med3_u32 v98, v97, v98, v46
v_med3_u32 v97, v96, v97, v46
v_max_u32 v96, v96, v46
v_med3_u32 v98, v97, v98, v48
v_med3_u32 v97, v96, v97, v48
v_max_u32 v96, v96, v48
v_med3_u32 v98, v97, v98, v50
v_med3_u32 v97, v96, v97, v50
v_max_u32 v96, v96, v50
v_med3_u32 v98, v97, v98, v52
v_med3_u32 v97, v96, v97, v52
v_max_u32 v96, v96, v52
v_med3_u32 v98, v97, v98, v54
v_med3_u32 v97, v96, v97, v54
v_max_u32 v96, v96, v54
v_med3_u32 v98, v97, v98, v56
v_med3_u32 v97, v96, v97, v56
v_max_u32 v96, v96, v56
v_med3_u32 v98, v97, v98, v58
v_med3_u32 v97, v96, v97, v58
v_max_u32 v96, v96, v58
v_med3_u32 v98, v97, v98, v60
v_med3_u32 v97, v96, v97, v60
v_max_u32 v96, v96, v60
v_med3_u32 v98, v97, v98, v62
v_med3_u32 v97, v96, v97, v62
v_max_u32 v96, v96, v62
v_med3_u32 v98, v97, v98, v64
v_med3_u32 v97, v96, v97, v64
v_max_u32 v96, v96, v64
v_med3_u32 v98, v97, v98, v66
v_med3_u32 v97, v96, v97, v66
v_max_u32 v96, v96, v66
v_med3_u32 v98, v97, v98, v68
v_med3_u32 v97, v96, v97, v68
v_max_u32 v96, v96, v68
v_med3_u32 v98, v97, v98, v70
v_med3_u32 v97, v96, v97, v70
v_max_u32 v96, v96, v70
v_med3_u32 v98, v97, v98, v72
v_med3_u32 v97, v96, v97, v72
v_max_u32 v96, v96, v72
v_med3_u32 v98, v97, v98, v74
v_med3_u32 v97, v96, v97, v74
v_max_u32 v96, v96, v74
v_med3_u32 v98, v97, v98, v76
v_med3_u32 v97, v96, v97, v76
v_max_u32 v96, v96, v76
v_med3_u32 v98, v97, v98, v78
v_med3_u32 v97, v96, v97, v78
v_max_u32 v96, v96, v78
v_med3_u32 v98, v97, v98, v80
v_med3_u32 v97, v96, v97, v80
v_max_u32 v96, v96, v80
v_med3_u32 v98, v97, v98, v82
v_med3_u32 v97, v96, v97, v82
v_max_u32 v96, v96, v82
v_med3_u32 v98, v97, v98, v84
v_med3_u32 v97, v96, v97, v84
v_max_u32 v96, v96, v84
v_med3_u32 v98, v97, v98, v86
v_med3_u32 v97, v96, v97, v86
v_max_u32 v96, v96, v86
v_med3_u32 v98, v97, v98, v88
v_med3_u32 v97, v96, v97, v88
v_max_u32 v96, v96, v88
v_med3_u32 v98, v97, v98, v90
v_med3_u32 v97, v96, v97, v90
v_max_u32 v96, v96, v90
v_med3_u32 v98, v97, v98, v92
v_med3_u32 v97, v96, v97, v92
v_max_u32 v96, v96, v92
v_med3_u32 v98, v97, v98, v94
v_med3_u32 v97, v96, v97, v94
v_max_u32 v96, v96, v94
v_mov_b32 v99, v96
s_nop 1
v_max_u32_dpp v99, v99, v99 row_shr:1 row_mask:0xf bank_mask:0xf
s_nop 1
v_max_u32_dpp v99, v99, v99 row_shr:2 row_mask:0xf bank_mask:0xf
s_nop 1
v_max_u32_dpp v99, v99, v99 row_shr:4 row_mask:0xf bank_mask:0xf
s_nop 1
v_max_u32_dpp v99, v99, v99 row_shr:8 row_mask:0xf bank_mask:0xf
s_nop 1
v_max_u32_dpp v99, v99, v99 row_bcast:15 row_mask:0xa bank_mask:0xf
s_nop 1
v_max_u32_dpp v99, v99, v99 row_bcast:31 row_mask:0xc bank_mask:0xf
s_nop 0
v_readlane_b32 s86, v99, 63
s_nop 1
v_cmp_eq_u32 vcc, s86, v96
s_and_b32 s87, s86, 0xffff
s_sub_u32 s87, 2047, s87
v_cndmask_b32 v96, v96, v97, vcc
v_cndmask_b32_e64 v97, v97, 0, vcc
v_writelane_b32 v100, s87, 0
v_mov_b32 v99, v96
s_nop 1
v_max_u32_dpp v99, v99, v99 row_shr:1 row_mask:0xf bank_mask:0xf
s_nop 1
v_max_u32_dpp v99, v99, v99 row_shr:2 row_mask:0xf bank_mask:0xf
s_nop 1
v_max_u32_dpp v99, v99, v99 row_shr:4 row_mask:0xf bank_mask:0xf
s_nop 1
v_max_u32_dpp v99, v99, v99 row_shr:8 row_mask:0xf bank_mask:0xf
s_nop 1
v_max_u32_dpp v99, v99, v99 row_bcast:15 row_mask:0xa bank_mask:0xf
s_nop 1
v_max_u32_dpp v99, v99, v99 row_bcast:31 row_mask:0xc bank_mask:0xf
s_nop 0
v_readlane_b32 s86, v99, 63
s_nop 1
v_cmp_eq_u32 vcc, s86, v96
s_and_b32 s87, s86, 0xffff
s_sub_u32 s87, 2047, s87
v_cndmask_b32 v96, v96, v97, vcc
v_cndmask_b32_e64 v97, v97, 0, vcc
v_writelane_b32 v100, s87, 1
v_mov_b32 v99, v96
s_nop 1
v_max_u32_dpp v99, v99, v99 row_shr:1 row_mask:0xf bank_mask:0xf
s_nop 1
v_max_u32_dpp v99, v99, v99 row_shr:2 row_mask:0xf bank_mask:0xf
s_nop 1
v_max_u32_dpp v99, v99, v99 row_shr:4 row_mask:0xf bank_mask:0xf
s_nop 1
v_max_u32_dpp v99, v99, v99 row_shr:8 row_mask:0xf bank_mask:0xf
s_nop 1
v_max_u32_dpp v99, v99, v99 row_bcast:15 row_mask:0xa bank_mask:0xf
s_nop 1
v_max_u32_dpp v99, v99, v99 row_bcast:31 row_mask:0xc bank_mask:0xf
s_nop 0
v_readlane_b32 s86, v99, 63
s_nop 1
v_cmp_eq_u32 vcc, s86, v96
s_and_b32 s87, s86, 0xffff
s_sub_u32 s87, 2047, s87
v_cndmask_b32 v96, v96, v97, vcc
v_cndmask_b32_e64 v97, v97, 0, vcc
v_writelane_b32 v100, s87, 2
v_mov_b32 v99, v96
s_nop 1
v_max_u32_dpp v99, v99, v99 row_shr:1 row_mask:0xf bank_mask:0xf
s_nop 1
v_max_u32_dpp v99, v99, v99 row_shr:2 row_mask:0xf bank_mask:0xf
s_nop 1
v_max_u32_dpp v99, v99, v99 row_shr:4 row_mask:0xf bank_mask:0xf
s_nop 1
v_max_u32_dpp v99, v99, v99 row_shr:8 row_mask:0xf bank_mask:0xf
s_nop 1
v_max_u32_dpp v99, v99, v99 row_bcast:15 row_mask:0xa bank_mask:0xf
s_nop 1
v_max_u32_dpp v99, v99, v99 row_bcast:31 row_mask:0xc bank_mask:0xf
s_nop 0
v_readlane_b32 s86, v99, 63
s_nop 1
v_cmp_eq_u32 vcc, s86, v96
s_and_b32 s87, s86, 0xffff
s_sub_u32 s87, 2047, s87
v_cndmask_b32 v96, v96, v97, vcc
v_cndmask_b32_e64 v97, v97, 0, vcc
v_writelane_b32 v100, s87, 3
v_mov_b32 v99, v96
s_nop 1
v_max_u32_dpp v99, v99, v99 row_shr:1 row_mask:0xf bank_mask:0xf
s_nop 1
v_max_u32_dpp v99, v99, v99 row_shr:2 row_mask:0xf bank_mask:0xf
s_nop 1
v_max_u32_dpp v99, v99, v99 row_shr:4 row_mask:0xf bank_mask:0xf
s_nop 1
v_max_u32_dpp v99, v99, v99 row_shr:8 row_mask:0xf bank_mask:0xf
s_nop 1
v_max_u32_dpp v99, v99, v99 row_bcast:15 row_mask:0xa bank_mask:0xf
s_nop 1
v_max_u32_dpp v99, v99, v99 row_bcast:31 row_mask:0xc bank_mask:0xf
s_nop 0
v_readlane_b32 s86, v99, 63
s_nop 1
v_cmp_eq_u32 vcc, s86, v96
s_and_b32 s87, s86, 0xffff
s_sub_u32 s87, 2047, s87
v_cndmask_b32 v96, v96, v97, vcc
v_cndmask_b32_e64 v97, v97, 0, vcc
v_writelane_b32 v100, s87, 4
v_mov_b32 v99, v96
s_nop 1
v_max_u32_dpp v99, v99, v99 row_shr:1 row_mask:0xf bank_mask:0xf
s_nop 1
v_max_u32_dpp v99, v99, v99 row_shr:2 row_mask:0xf bank_mask:0xf
s_nop 1
v_max_u32_dpp v99, v99, v99 row_shr:4 row_mask:0xf bank_mask:0xf
s_nop 1
v_max_u32_dpp v99, v99, v99 row_shr:8 row_mask:0xf bank_mask:0xf
s_nop 1
v_max_u32_dpp v99, v99, v99 row_bcast:15 row_mask:0xa bank_mask:0xf
s_nop 1
v_max_u32_dpp v99, v99, v99 row_bcast:31 row_mask:0xc bank_mask:0xf
s_nop 0
v_readlane_b32 s86, v99, 63
s_nop 1
v_cmp_eq_u32 vcc, s86, v96
s_and_b32 s87, s86, 0xffff
s_sub_u32 s87, 2047, s87
v_cndmask_b32 v96, v96, v97, vcc
v_cndmask_b32_e64 v97, v97, 0, vcc
v_writelane_b32 v100, s87, 5
v_mov_b32 v99, v96
s_nop 1
v_max_u32_dpp v99, v99, v99 row_shr:1 row_mask:0xf bank_mask:0xf
s_nop 1
v_max_u32_dpp v99, v99, v99 row_shr:2 row_mask:0xf bank_mask:0xf
s_nop 1
v_max_u32_dpp v99, v99, v99 row_shr:4 row_mask:0xf bank_mask:0xf
s_nop 1
v_max_u32_dpp v99, v99, v99 row_shr:8 row_mask:0xf bank_mask:0xf
s_nop 1
v_max_u32_dpp v99, v99, v99 row_bcast:15 row_mask:0xa bank_mask:0xf
s_nop 1
v_max_u32_dpp v99, v99, v99 row_bcast:31 row_mask:0xc bank_mask:0xf
s_nop 0
v_readlane_b32 s86, v99, 63
s_nop 1
v_cmp_eq_u32 vcc, s86, v96
s_and_b32 s87, s86, 0xffff
s_sub_u32 s87, 2047, s87
v_cndmask_b32 v96, v96, v97, vcc
v_cndmask_b32_e64 v97, v97, 0, vcc
v_writelane_b32 v100, s87, 6
v_mov_b32 v99, v96
s_nop 1
v_max_u32_dpp v99, v99, v99 row_shr:1 row_mask:0xf bank_mask:0xf
s_nop 1
v_max_u32_dpp v99, v99, v99 row_shr:2 row_mask:0xf bank_mask:0xf
s_nop 1
v_max_u32_dpp v99, v99, v99 row_shr:4 row_mask:0xf bank_mask:0xf
s_nop 1
v_max_u32_dpp v99, v99, v99 row_shr:8 row_mask:0xf bank_mask:0xf
s_nop 1
v_max_u32_dpp v99, v99, v99 row_bcast:15 row_mask:0xa bank_mask:0xf
s_nop 1
v_max_u32_dpp v99, v99, v99 row_bcast:31 row_mask:0xc bank_mask:0xf
s_nop 0
v_readlane_b32 s86, v99, 63
s_nop 1
v_cmp_eq_u32 vcc, s86, v96
s_and_b32 s87, s86, 0xffff
s_sub_u32 s87, 2047, s87
v_cndmask_b32 v96, v96, v97, vcc
v_cndmask_b32_e64 v97, v97, 0, vcc
v_writelane_b32 v100, s87, 7
v_mov_b32 v99, v98
s_nop 1
v_max_u32_dpp v99, v99, v99 row_shr:1 row_mask:0xf bank_mask:0xf
s_nop 1
v_max_u32_dpp v99, v99, v99 row_shr:2 row_mask:0xf bank_mask:0xf
s_nop 1
v_max_u32_dpp v99, v99, v99 row_shr:4 row_mask:0xf bank_mask:0xf
s_nop 1
v_max_u32_dpp v99, v99, v99 row_shr:8 row_mask:0xf bank_mask:0xf
s_nop 1
v_max_u32_dpp v99, v99, v99 row_bcast:15 row_mask:0xa bank_mask:0xf
s_nop 1
v_max_u32_dpp v99, v99, v99 row_bcast:31 row_mask:0xc bank_mask:0xf
s_nop 0
v_readlane_b32 s87, v99, 63
s_nop 0
s_cmp_gt_u32 s87, s86
s_cbranch_scc0 .LL_29%=
s_mov_b32 m0, 0
.LL_30%=:
v_max3_u32 v96, v32, v34, v36
v_max3_u32 v96, v96, v38, v40
v_max3_u32 v96, v96, v42, v44
v_max3_u32 v96, v96, v46, v48
v_max3_u32 v96, v96, v50, v52
v_max3_u32 v96, v96, v54, v56
v_max3_u32 v96, v96, v58, v60
v_max3_u32 v96, v96, v62, v64
v_max3_u32 v96, v96, v66, v68
v_max3_u32 v96, v96, v70, v72
v_max3_u32 v96, v96, v74, v76
v_max3_u32 v96, v96, v78, v80
v_max3_u32 v96, v96, v82, v84
v_max3_u32 v96, v96, v86, v88
v_max3_u32 v96, v96, v90, v92
v_max_u32 v96, v96, v94
v_mov_b32 v99, v96
s_nop 1
v_max_u32_dpp v99, v99, v99 row_shr:1 row_mask:0xf bank_mask:0xf
s_nop 1
v_max_u32_dpp v99, v99, v99 row_shr:2 row_mask:0xf bank_mask:0xf
s_nop 1
v_max_u32_dpp v99, v99, v99 row_shr:4 row_mask:0xf bank_mask:0xf
s_nop 1
v_max_u32_dpp v99, v99, v99 row_shr:8 row_mask:0xf bank_mask:0xf
s_nop 1
v_max_u32_dpp v99, v99, v99 row_bcast:15 row_mask:0xa bank_mask:0xf
s_nop 1
v_max_u32_dpp v99, v99, v99 row_bcast:31 row_mask:0xc bank_mask:0xf
s_nop 0
v_readlane_b32 s86, v99, 63
s_nop 1
v_cmp_eq_u32 vcc, s86, v32
s_nop 1
v_cndmask_b32_e64 v32, v32, 0, vcc
v_cmp_eq_u32 vcc, s86, v34
s_nop 1
v_cndmask_b32_e64 v34, v34, 0, vcc
v_cmp_eq_u32 vcc, s86, v36
s_nop 1
v_cndmask_b32_e64 v36, v36, 0, vcc
v_cmp_eq_u32 vcc, s86, v38
s_nop 1
v_cndmask_b32_e64 v38, v38, 0, vcc
v_cmp_eq_u32 vcc, s86, v40
s_nop 1
v_cndmask_b32_e64 v40, v40, 0, vcc
v_cmp_eq_u32 vcc, s86, v42
s_nop 1
v_cndmask_b32_e64 v42, v42, 0, vcc
v_cmp_eq_u32 vcc, s86, v44
s_nop 1
v_cndmask_b32_e64 v44, v44, 0, vcc
v_cmp_eq_u32 vcc, s86, v46
s_nop 1
v_cndmask_b32_e64 v46, v46, 0, vcc
v_cmp_eq_u32 vcc, s86, v48
s_nop 1
v_cndmask_b32_e64 v48, v48, 0, vcc
v_cmp_eq_u32 vcc, s86, v50
s_nop 1
v_cndmask_b32_e64 v50, v50, 0, vcc
v_cmp_eq_u32 vcc, s86, v52
s_nop 1
v_cndmask_b32_e64 v52, v52, 0, vcc
v_cmp_eq_u32 vcc, s86, v54
s_nop 1
v_cndmask_b32_e64 v54, v54, 0, vcc
)"
R"(v_cmp_eq_u32 vcc, s86, v56
s_nop 1
v_cndmask_b32_e64 v56, v56, 0, vcc
v_cmp_eq_u32 vcc, s86, v58
s_nop 1
v_cndmask_b32_e64 v58, v58, 0, vcc
v_cmp_eq_u32 vcc, s86, v60
s_nop 1
v_cndmask_b32_e64 v60, v60, 0, vcc
v_cmp_eq_u32 vcc, s86, v62
s_nop 1
v_cndmask_b32_e64 v62, v62, 0, vcc
v_cmp_eq_u32 vcc, s86, v64
s_nop 1
v_cndmask_b32_e64 v64, v64, 0, vcc
v_cmp_eq_u32 vcc, s86, v66
s_nop 1
v_cndmask_b32_e64 v66, v66, 0, vcc
v_cmp_eq_u32 vcc, s86, v68
s_nop 1
v_cndmask_b32_e64 v68, v68, 0, vcc
v_cmp_eq_u32 vcc, s86, v70
s_nop 1
v_cndmask_b32_e64 v70, v70, 0, vcc
v_cmp_eq_u32 vcc, s86, v72
s_nop 1
v_cndmask_b32_e64 v72, v72, 0, vcc
v_cmp_eq_u32 vcc, s86, v74
s_nop 1
v_cndmask_b32_e64 v74, v74, 0, vcc
v_cmp_eq_u32 vcc, s86, v76
s_nop 1
v_cndmask_b32_e64 v76, v76, 0, vcc
v_cmp_eq_u32 vcc, s86, v78
s_nop 1
v_cndmask_b32_e64 v78, v78, 0, vcc
v_cmp_eq_u32 vcc, s86, v80
s_nop 1
v_cndmask_b32_e64 v80, v80, 0, vcc
v_cmp_eq_u32 vcc, s86, v82
s_nop 1
v_cndmask_b32_e64 v82, v82, 0, vcc
v_cmp_eq_u32 vcc, s86, v84
s_nop 1
v_cndmask_b32_e64 v84, v84, 0, vcc
v_cmp_eq_u32 vcc, s86, v86
s_nop 1
v_cndmask_b32_e64 v86, v86, 0, vcc
v_cmp_eq_u32 vcc, s86, v88
s_nop 1
v_cndmask_b32_e64 v88, v88, 0, vcc
v_cmp_eq_u32 vcc, s86, v90
s_nop 1
v_cndmask_b32_e64 v90, v90, 0, vcc
v_cmp_eq_u32 vcc, s86, v92
s_nop 1
v_cndmask_b32_e64 v92, v92, 0, vcc
v_cmp_eq_u32 vcc, s86, v94
s_nop 1
v_cndmask_b32_e64 v94, v94, 0, vcc
s_and_b32 s87, s86, 0xffff
s_sub_u32 s87, 2047, s87
s_nop 0
v_writelane_b32 v100, s87, m0
s_add_u32 m0, m0, 1
s_cmp_lt_u32 m0, 8
s_cbranch_scc1 .LL_30%=
.LL_29%=:
v_xor_b32 v102, s12, v100
v_mov_b32 v103, s13
s_mov_b64 exec, 0xff
buffer_store_dwordx2 v[102:103], v136, s[8:11], 0 offen offset:3584 sc1
s_mov_b64 exec, -1
s_branch .Lrtr_26%=
.Lspin_51%=:
s_mov_b32 s84, 0
.Llp_53%=:
s_sleep 2
buffer_load_dwordx2 v[8:9], v11, s[8:11], 0 offen offset:3840 sc1
s_waitcnt vmcnt(0)
s_add_u32 s84, s84, 1
v_cmp_eq_u64 vcc, s[12:13], v[8:9]
s_cmp_eq_u64 vcc, -1
s_cbranch_scc1 .Lok_52%=
s_cmp_lt_u32 s84, 1048576
s_cbranch_scc1 .Llp_53%=
s_branch .Lok_52%=
.Lredo%=:
s_waitcnt vmcnt(0) lgkmcnt(0)
s_barrier
s_and_b32 s81, s14, 1
s_mul_i32 s91, s81, 17164288
s_mul_i32 s82, s56, 536576
s_add_u32 s91, s91, s82
s_add_u32 s91, s91, 4194304
s_lshr_b32 s82, s14, 1
s_lshl_b32 s83, s82, 14
s_add_u32 s91, s91, s83
s_lshl_b32 s81, s81, 14
s_lshl_b32 s82, s82, 16
s_or_b32 s92, s81, s82
s_xor_b32 s92, s92, 0x10000
s_add_u32 m0, s92, 0
s_add_u32 s81, s91, 0
Dc 1,8,81,0
Dc 1,8,81,1024
Dc 1,8,81,2048
Dc 1,8,81,3072
s_add_u32 m0, s92, 4096
s_add_u32 s81, s91, 4096
Dc 1,8,81,0
Dc 1,8,81,1024
Dc 1,8,81,2048
Dc 1,8,81,3072
s_add_u32 m0, s92, 8192
s_add_u32 s81, s91, 8192
Dc 1,8,81,0
Dc 1,8,81,1024
Dc 1,8,81,2048
Dc 1,8,81,3072
s_add_u32 m0, s92, 12288
s_add_u32 s81, s91, 12288
Dc 1,8,81,0
Dc 1,8,81,1024
Dc 1,8,81,2048
Dc 1,8,81,3072
v_bfe_u32 v244, v1, 4, 1
v_mul_u32_u24 v244, 0xaaaa, v244
v_add_u32 v244, 0x4444, v244
v_lshl_or_b32 v240, v244, 16, v244
v_mov_b32 v241, 0xff800000
v_add_u32 v242, 0x10000, v1
v_bfe_u32 v244, v1, 4, 4
v_lshrrev_b32 v245, 8, v1
v_lshlrev_b32 v245, 2, v245
v_sub_u32 v244, v244, v245
v_cmp_le_i32_e64 s[72:73], 0, v244
v_cmp_le_i32_e64 s[74:75], 1, v244
v_cmp_le_i32_e64 s[76:77], 2, v244
v_cmp_le_i32_e64 s[78:79], 3, v244
v_add_u32 v243, 4096, v1
v_add_u32 v244, 8192, v1
v_add_u32 v245, 12288, v1
s_mov_b32 s18, 0x3f803f80
s_mov_b32 s19, 0x3f003f00
v_accvgpr_write_b32 a176, s18
v_accvgpr_write_b32 a180, s19
v_mov_b32 v252, v241
v_accvgpr_write_b32 a177, s18
v_accvgpr_write_b32 a181, s19
v_mov_b32 v253, v241
v_accvgpr_write_b32 a178, s18
v_accvgpr_write_b32 a182, s19
v_mov_b32 v254, v241
v_accvgpr_write_b32 a179, s18
v_accvgpr_write_b32 a183, s19
v_mov_b32 v255, v241
v_accvgpr_write_b32 a160, 0
v_accvgpr_write_b32 a161, 0
v_accvgpr_write_b32 a162, 0
v_accvgpr_write_b32 a163, 0
v_accvgpr_write_b32 a164, 0
v_accvgpr_write_b32 a165, 0
v_accvgpr_write_b32 a166, 0
v_accvgpr_write_b32 a167, 0
v_accvgpr_write_b32 a168, 0
v_accvgpr_write_b32 a169, 0
v_accvgpr_write_b32 a170, 0
v_accvgpr_write_b32 a171, 0
v_accvgpr_write_b32 a172, 0
v_accvgpr_write_b32 a173, 0
v_accvgpr_write_b32 a174, 0
v_accvgpr_write_b32 a175, 0
s_mov_b32 s90, 0x5d800000
s_mov_b64 s[88:89], 0
s_mov_b32 s64, 0
s_and_b32 s18, s14, 1
s_lshl_b32 s62, s18, 14
s_lshl_b32 s20, s57, 15
s_sub_u32 s20, 507904, s20
s_add_u32 s20, s20, s62
s_lshl_b32 s65, s57, 1
s_sub_u32 s65, s65, 1
s_max_i32 s65, s65, 0
s_sub_u32 s66, s60, 1
s_cmp_lt_u32 s14, 2
s_cselect_b32 s20, s20, 0
s_cselect_b32 s66, s66, 15
s_cselect_b32 s21, 32768, 0
s_add_u32 s61, s91, s20
s_add_u32 s62, s62, s21
s_sub_u32 s63, 16384, s21
s_lshl_b32 s67, s60, 7
s_add_u32 s67, s67, s56
s_lshl_b32 s67, s67, 12
s_add_u32 s67, s67, 39845888
s_lshl_b32 s69, s60, 3
s_add_u32 s69, s69, s59
s_lshl_b32 s69, s69, 6
s_lshl_b32 s21, s58, 3
s_add_u32 s69, s69, s21
s_add_u32 s69, s69, 20480
s_mov_b32 s70, 0
s_mov_b32 s37, 0
v_bfe_u32 v28, v1, 4, 4
v_lshrrev_b32 v29, 8, v1
v_lshlrev_b32 v29, 2, v29
v_sub_u32 v28, v28, v29
s_mov_b64 s[24:25], 0
s_mov_b32 s68, 0
s_mov_b32 s85, 99
s_lshl_b32 s80, s60, 6
s_and_b32 s81, s14, 2
s_lshl_b32 s81, s81, 4
s_xor_b32 s82, s81, 0
s_add_u32 s82, s82, s80
v_add_u32 v4, s82, v28
s_xor_b32 s82, s81, 16
s_add_u32 s82, s82, s80
v_add_u32 v5, s82, v28
s_xor_b32 s82, s81, 32
s_add_u32 s82, s82, s80
v_add_u32 v6, s82, v28
s_xor_b32 s82, s81, 48
s_add_u32 s82, s82, s80
v_add_u32 v7, s82, v28
s_mul_i32 s82, s56, 536576
s_add_u32 s82, s82, 4194304
s_lshl_b32 s84, s60, 1
s_add_u32 s84, s84, 2
.LL_54%=:
buffer_load_dwordx4 a[128:131], v1, s[8:11], s82 offen offset:0 sc1
buffer_load_dwordx4 a[132:135], v1, s[8:11], s82 offen offset:1024 sc1
buffer_load_dwordx4 a[136:139], v1, s[8:11], s82 offen offset:2048 sc1
buffer_load_dwordx4 a[140:143], v1, s[8:11], s82 offen offset:3072 sc1
buffer_load_dwordx4 a[144:147], v243, s[8:11], s82 offen offset:0 sc1
buffer_load_dwordx4 a[148:151], v243, s[8:11], s82 offen offset:1024 sc1
buffer_load_dwordx4 a[152:155], v243, s[8:11], s82 offen offset:2048 sc1
buffer_load_dwordx4 a[156:159], v243, s[8:11], s82 offen offset:3072 sc1
s_waitcnt vmcnt(0)
Zvaa 160,128,0
Zvaa 164,128,16
Zvaa 168,128,32
Zvaa 172,128,48
Mvaa 160,132,4
Mvaa 164,132,20
Mvaa 168,132,36
Mvaa 172,132,52
Mvaa 160,136,8
Mvaa 164,136,24
Mvaa 168,136,40
Mvaa 172,136,56
Mvaa 160,140,12
Mvaa 164,140,28
Mvaa 168,140,44
Mvaa 172,140,60
Zvaa 176,144,0
Zvaa 180,144,16
Zvaa 184,144,32
Zvaa 188,144,48
Mvaa 176,148,4
Mvaa 180,148,20
Mvaa 184,148,36
Mvaa 188,148,52
Mvaa 176,152,8
Mvaa 180,152,24
Mvaa 184,152,40
Mvaa 188,152,56
Mvaa 176,156,12
Mvaa 180,156,28
Mvaa 184,156,44
Mvaa 188,156,60
v_cmp_le_i32 vcc, 0, v4
s_nop 1
v_cndmask_b32 v160, v241, v160, vcc
v_max_f32 v252, v252, v160
v_cmp_le_i32 vcc, 1, v4
s_nop 1
v_cndmask_b32 v161, v241, v161, vcc
v_max_f32 v252, v252, v161
v_cmp_le_i32 vcc, 2, v4
s_nop 1
v_cndmask_b32 v162, v241, v162, vcc
v_max_f32 v252, v252, v162
v_cmp_le_i32 vcc, 3, v4
s_nop 1
v_cndmask_b32 v163, v241, v163, vcc
v_max_f32 v252, v252, v163
v_cmp_le_i32 vcc, 0, v5
s_nop 1
v_cndmask_b32 v164, v241, v164, vcc
v_max_f32 v253, v253, v164
v_cmp_le_i32 vcc, 1, v5
s_nop 1
v_cndmask_b32 v165, v241, v165, vcc
v_max_f32 v253, v253, v165
v_cmp_le_i32 vcc, 2, v5
s_nop 1
v_cndmask_b32 v166, v241, v166, vcc
v_max_f32 v253, v253, v166
v_cmp_le_i32 vcc, 3, v5
s_nop 1
v_cndmask_b32 v167, v241, v167, vcc
v_max_f32 v253, v253, v167
v_cmp_le_i32 vcc, 0, v6
s_nop 1
v_cndmask_b32 v168, v241, v168, vcc
v_max_f32 v254, v254, v168
v_cmp_le_i32 vcc, 1, v6
s_nop 1
v_cndmask_b32 v169, v241, v169, vcc
v_max_f32 v254, v254, v169
v_cmp_le_i32 vcc, 2, v6
s_nop 1
v_cndmask_b32 v170, v241, v170, vcc
v_max_f32 v254, v254, v170
v_cmp_le_i32 vcc, 3, v6
s_nop 1
v_cndmask_b32 v171, v241, v171, vcc
v_max_f32 v254, v254, v171
v_cmp_le_i32 vcc, 0, v7
s_nop 1
v_cndmask_b32 v172, v241, v172, vcc
v_max_f32 v255, v255, v172
v_cmp_le_i32 vcc, 1, v7
s_nop 1
v_cndmask_b32 v173, v241, v173, vcc
v_max_f32 v255, v255, v173
v_cmp_le_i32 vcc, 2, v7
s_nop 1
v_cndmask_b32 v174, v241, v174, vcc
v_max_f32 v255, v255, v174
v_cmp_le_i32 vcc, 3, v7
s_nop 1
v_cndmask_b32 v175, v241, v175, vcc
v_max_f32 v255, v255, v175
v_cmp_le_i32 vcc, 16, v4
s_nop 1
v_cndmask_b32 v176, v241, v176, vcc
v_max_f32 v252, v252, v176
v_cmp_le_i32 vcc, 17, v4
s_nop 1
v_cndmask_b32 v177, v241, v177, vcc
v_max_f32 v252, v252, v177
v_cmp_le_i32 vcc, 18, v4
s_nop 1
v_cndmask_b32 v178, v241, v178, vcc
v_max_f32 v252, v252, v178
v_cmp_le_i32 vcc, 19, v4
s_nop 1
v_cndmask_b32 v179, v241, v179, vcc
v_max_f32 v252, v252, v179
v_cmp_le_i32 vcc, 16, v5
s_nop 1
v_cndmask_b32 v180, v241, v180, vcc
v_max_f32 v253, v253, v180
v_cmp_le_i32 vcc, 17, v5
s_nop 1
v_cndmask_b32 v181, v241, v181, vcc
v_max_f32 v253, v253, v181
v_cmp_le_i32 vcc, 18, v5
s_nop 1
v_cndmask_b32 v182, v241, v182, vcc
v_max_f32 v253, v253, v182
v_cmp_le_i32 vcc, 19, v5
s_nop 1
v_cndmask_b32 v183, v241, v183, vcc
v_max_f32 v253, v253, v183
v_cmp_le_i32 vcc, 16, v6
s_nop 1
v_cndmask_b32 v184, v241, v184, vcc
v_max_f32 v254, v254, v184
v_cmp_le_i32 vcc, 17, v6
s_nop 1
v_cndmask_b32 v185, v241, v185, vcc
v_max_f32 v254, v254, v185
v_cmp_le_i32 vcc, 18, v6
s_nop 1
v_cndmask_b32 v186, v241, v186, vcc
v_max_f32 v254, v254, v186
v_cmp_le_i32 vcc, 19, v6
s_nop 1
v_cndmask_b32 v187, v241, v187, vcc
v_max_f32 v254, v254, v187
v_cmp_le_i32 vcc, 16, v7
s_nop 1
v_cndmask_b32 v188, v241, v188, vcc
v_max_f32 v255, v255, v188
v_cmp_le_i32 vcc, 17, v7
s_nop 1
v_cndmask_b32 v189, v241, v189, vcc
v_max_f32 v255, v255, v189
v_cmp_le_i32 vcc, 18, v7
s_nop 1
v_cndmask_b32 v190, v241, v190, vcc
v_max_f32 v255, v255, v190
v_cmp_le_i32 vcc, 19, v7
s_nop 1
v_cndmask_b32 v191, v241, v191, vcc
v_max_f32 v255, v255, v191
)"
R"(v_subrev_u32 v4, 32, v4
v_subrev_u32 v5, 32, v5
v_subrev_u32 v6, 32, v6
v_subrev_u32 v7, 32, v7
s_add_u32 s82, s82, 8192
s_sub_u32 s84, s84, 1
s_cmp_lg_u32 s84, 0
s_cbranch_scc1 .LL_54%=
G 28,0
v_lshlrev_b32 v32, 16, v28
v_and_b32 v33, 0xffff0000, v28
G 28,1
v_lshlrev_b32 v34, 16, v28
v_and_b32 v35, 0xffff0000, v28
G 28,2
v_lshlrev_b32 v36, 16, v28
v_and_b32 v37, 0xffff0000, v28
G 28,3
v_lshlrev_b32 v38, 16, v28
v_and_b32 v39, 0xffff0000, v28
G 28,4
v_lshlrev_b32 v40, 16, v28
v_and_b32 v41, 0xffff0000, v28
G 28,5
v_lshlrev_b32 v42, 16, v28
v_and_b32 v43, 0xffff0000, v28
G 28,6
v_lshlrev_b32 v44, 16, v28
v_and_b32 v45, 0xffff0000, v28
G 28,7
v_lshlrev_b32 v46, 16, v28
v_and_b32 v47, 0xffff0000, v28
G 28,8
v_lshlrev_b32 v48, 16, v28
v_and_b32 v49, 0xffff0000, v28
G 28,9
v_lshlrev_b32 v50, 16, v28
v_and_b32 v51, 0xffff0000, v28
G 28,10
v_lshlrev_b32 v52, 16, v28
v_and_b32 v53, 0xffff0000, v28
G 28,11
v_lshlrev_b32 v54, 16, v28
v_and_b32 v55, 0xffff0000, v28
G 28,12
v_lshlrev_b32 v56, 16, v28
v_and_b32 v57, 0xffff0000, v28
G 28,13
v_lshlrev_b32 v58, 16, v28
v_and_b32 v59, 0xffff0000, v28
G 28,14
v_lshlrev_b32 v60, 16, v28
v_and_b32 v61, 0xffff0000, v28
G 28,15
v_lshlrev_b32 v62, 16, v28
v_and_b32 v63, 0xffff0000, v28
G 28,16
v_lshlrev_b32 v64, 16, v28
v_and_b32 v65, 0xffff0000, v28
G 28,17
v_lshlrev_b32 v66, 16, v28
v_and_b32 v67, 0xffff0000, v28
G 28,18
v_lshlrev_b32 v68, 16, v28
v_and_b32 v69, 0xffff0000, v28
G 28,19
v_lshlrev_b32 v70, 16, v28
v_and_b32 v71, 0xffff0000, v28
G 28,20
v_lshlrev_b32 v72, 16, v28
v_and_b32 v73, 0xffff0000, v28
G 28,21
v_lshlrev_b32 v74, 16, v28
v_and_b32 v75, 0xffff0000, v28
G 28,22
v_lshlrev_b32 v76, 16, v28
v_and_b32 v77, 0xffff0000, v28
G 28,23
v_lshlrev_b32 v78, 16, v28
v_and_b32 v79, 0xffff0000, v28
G 28,24
v_lshlrev_b32 v80, 16, v28
v_and_b32 v81, 0xffff0000, v28
G 28,25
v_lshlrev_b32 v82, 16, v28
v_and_b32 v83, 0xffff0000, v28
G 28,26
v_lshlrev_b32 v84, 16, v28
v_and_b32 v85, 0xffff0000, v28
G 28,27
v_lshlrev_b32 v86, 16, v28
v_and_b32 v87, 0xffff0000, v28
G 28,28
v_lshlrev_b32 v88, 16, v28
v_and_b32 v89, 0xffff0000, v28
G 28,29
v_lshlrev_b32 v90, 16, v28
v_and_b32 v91, 0xffff0000, v28
G 28,30
v_lshlrev_b32 v92, 16, v28
v_and_b32 v93, 0xffff0000, v28
G 28,31
v_lshlrev_b32 v94, 16, v28
v_and_b32 v95, 0xffff0000, v28
G 28,32
v_lshlrev_b32 v96, 16, v28
v_and_b32 v97, 0xffff0000, v28
G 28,33
v_lshlrev_b32 v98, 16, v28
v_and_b32 v99, 0xffff0000, v28
G 28,34
v_lshlrev_b32 v100, 16, v28
v_and_b32 v101, 0xffff0000, v28
G 28,35
v_lshlrev_b32 v102, 16, v28
v_and_b32 v103, 0xffff0000, v28
G 28,36
v_lshlrev_b32 v104, 16, v28
v_and_b32 v105, 0xffff0000, v28
G 28,37
v_lshlrev_b32 v106, 16, v28
v_and_b32 v107, 0xffff0000, v28
G 28,38
v_lshlrev_b32 v108, 16, v28
v_and_b32 v109, 0xffff0000, v28
G 28,39
v_lshlrev_b32 v110, 16, v28
v_and_b32 v111, 0xffff0000, v28
G 28,40
v_lshlrev_b32 v112, 16, v28
v_and_b32 v113, 0xffff0000, v28
G 28,41
v_lshlrev_b32 v114, 16, v28
v_and_b32 v115, 0xffff0000, v28
G 28,42
v_lshlrev_b32 v116, 16, v28
v_and_b32 v117, 0xffff0000, v28
G 28,43
v_lshlrev_b32 v118, 16, v28
v_and_b32 v119, 0xffff0000, v28
G 28,44
v_lshlrev_b32 v120, 16, v28
v_and_b32 v121, 0xffff0000, v28
G 28,45
v_lshlrev_b32 v122, 16, v28
v_and_b32 v123, 0xffff0000, v28
G 28,46
v_lshlrev_b32 v124, 16, v28
v_and_b32 v125, 0xffff0000, v28
G 28,47
v_lshlrev_b32 v126, 16, v28
v_and_b32 v127, 0xffff0000, v28
G 28,48
v_lshlrev_b32 v128, 16, v28
v_and_b32 v129, 0xffff0000, v28
G 28,49
v_lshlrev_b32 v130, 16, v28
v_and_b32 v131, 0xffff0000, v28
G 28,50
v_lshlrev_b32 v132, 16, v28
v_and_b32 v133, 0xffff0000, v28
G 28,51
v_lshlrev_b32 v134, 16, v28
v_and_b32 v135, 0xffff0000, v28
G 28,52
v_lshlrev_b32 v136, 16, v28
v_and_b32 v137, 0xffff0000, v28
G 28,53
v_lshlrev_b32 v138, 16, v28
v_and_b32 v139, 0xffff0000, v28
G 28,54
v_lshlrev_b32 v140, 16, v28
v_and_b32 v141, 0xffff0000, v28
G 28,55
v_lshlrev_b32 v142, 16, v28
v_and_b32 v143, 0xffff0000, v28
G 28,56
v_lshlrev_b32 v144, 16, v28
v_and_b32 v145, 0xffff0000, v28
G 28,57
v_lshlrev_b32 v146, 16, v28
v_and_b32 v147, 0xffff0000, v28
G 28,58
v_lshlrev_b32 v148, 16, v28
v_and_b32 v149, 0xffff0000, v28
G 28,59
v_lshlrev_b32 v150, 16, v28
v_and_b32 v151, 0xffff0000, v28
G 28,60
v_lshlrev_b32 v152, 16, v28
v_and_b32 v153, 0xffff0000, v28
G 28,61
v_lshlrev_b32 v154, 16, v28
v_and_b32 v155, 0xffff0000, v28
G 28,62
v_lshlrev_b32 v156, 16, v28
v_and_b32 v157, 0xffff0000, v28
G 28,63
v_lshlrev_b32 v158, 16, v28
v_and_b32 v159, 0xffff0000, v28
s_mov_b32 s22, 1.0
s_mov_b32 s23, 1.0
s_mov_b32 s29, 134217728
s_mov_b32 s71, 3584
s_barrier
Ra 128,242,0
Ra 132,242,1024
Ra 136,242,2048
Ra 140,242,3072
s_cmp_ge_u32 s14, 2
s_cbranch_scc1 .Lbgo%=
s_branch .Lago%=
.p2align 12
.Lpb_31%=:
s_waitcnt vmcnt(6) lgkmcnt(6)
Zvaa 32,0,32
s_add_u32 s28, s28, 512
Ta 20,4,8192
Ta 22,4,9216
Mvaa 32,4,48
s_cmp_eq_u32 s28, s4
s_cselect_b32 s93, 10240, 0
Ta 24,5,0
Ta 26,5,1024
Zvaa 64,4,52
s_sub_u32 s28, s28, s93
s_add_u32 s93, s28, 20480
buffer_load_dwordx4 a[128:131], v26, s[8:11], s28 offen offset:0 sc1
Mvaa 64,0,36
Ta 28,5,8192
Ta 30,5,9216
Zvaa 96,0,40
K 184,184,185
K 185,186,187
Mvaa 96,4,56
K 186,188,189
K 187,190,191
Zvaa 128,4,60
K 192,192,193
Mvaa 128,0,44
W 8
buffer_load_dwordx4 a[132:135], v26, s[8:11], s93 offen offset:0 sc1
K 193,194,195
Zvaa 132,8,44
Ta 0,6,0
Ta 2,6,1024
Mvaa 132,12,60
K 194,196,197
K 195,198,199
Zvaa 100,12,56
K 200,200,201
K 201,202,203
Mvaa 100,8,40
Ta 4,6,8192
Ta 6,6,9216
Zvaa 68,8,36
buffer_load_dwordx4 a[136:139], v27, s[8:11], s28 offen offset:0 sc1
K 202,204,205
Mvaa 68,12,52
K 203,206,207
K 208,208,209
Zvaa 36,12,48
K 209,210,211
K 210,212,213
Mvaa 36,8,32
W 8
Ta 8,7,0
K 211,214,215
Zvaa 40,16,32
Ta 10,7,1024
K 216,216,217
Mvaa 40,20,48
buffer_load_dwordx4 a[140:143], v27, s[8:11], s93 offen offset:0 sc1
K 217,218,219
Zvaa 72,20,52
s_add_u32 s28, s28, 512
s_cmp_eq_u32 s28, s4
s_cselect_b32 s93, 10240, 0
s_sub_u32 s28, s28, s93
Mvaa 72,16,36
s_add_u32 s93, s28, 20480
K 218,220,221
K 219,222,223
Zvaa 104,16,40
Ta 12,7,8192
Ta 14,7,9216
Mvaa 104,20,56
K 224,224,225
K 225,226,227
Zvaa 136,20,60
K 226,228,229
K 227,230,231
Mvaa 136,16,44
W 8
buffer_load_dwordx4 a[144:147], v26, s[8:11], s28 offen offset:0 sc1
K 232,232,233
Zvaa 140,24,44
K 233,234,235
K 234,236,237
Mvaa 140,28,60
Ta 16,8,0
K 235,238,239
Zvaa 108,28,56
Ta 18,8,1024
K 240,240,241
Mvaa 108,24,40
K 241,242,243
K 242,244,245
Zvaa 76,24,36
buffer_load_dwordx4 a[148:151], v26, s[8:11], s93 offen offset:0 sc1
K 243,246,247
Mvaa 76,28,52
Ta 20,8,8192
Ta 22,8,9216
Zvaa 44,28,48
K 248,248,249
K 249,250,251
Mvaa 44,24,32
K 250,252,253
K 251,254,255
W 8
Zvaa 48,0,32
Ta 24,9,0
Ta 26,9,1024
Mvaa 48,4,48
buffer_load_dwordx4 a[152:155], v27, s[8:11], s28 offen offset:0 sc1
K 160,160,161
Zvaa 80,4,52
K 161,162,163
K 162,164,165
Mvaa 80,0,36
Ta 28,9,8192
K 163,166,167
Zvaa 112,0,40
Ta 30,9,9216
K 168,168,169
Mvaa 112,4,56
K 169,170,171
K 170,172,173
Zvaa 144,4,60
buffer_load_dwordx4 a[156:159], v27, s[8:11], s93 offen offset:0 sc1
K 171,174,175
Mvaa 144,0,44
W 8
s_add_u32 s28, s28, 512
s_cmp_eq_u32 s28, s4
s_cselect_b32 s93, 10240, 0
Zvaa 148,8,44
s_sub_u32 s28, s28, s93
s_add_u32 s93, s28, 20480
G 164,184
Mvaa 148,12,60
G 165,185
G 166,186
Zvaa 116,12,56
Ta 0,2,16384
Ta 2,2,17408
Mvaa 116,8,40
buffer_load_dwordx4 v[188:191], v26, s[8:11], s28 offen offset:0 sc1
G 167,187
Zvaa 84,8,36
Ta 4,2,24576
G 172,188
Mvaa 84,12,52
Ta 6,2,25600
G 173,189
Zvaa 52,12,48
G 174,190
G 175,191
Mvaa 52,8,32
W 8
Ta 8,3,16384
K 164,164,165
Zvaa 56,16,32
buffer_load_dwordx4 v[196:199], v26, s[8:11], s93 offen offset:0 sc1
Ta 10,3,17408
Mvaa 56,20,48
K 165,166,167
K 166,172,173
Zvaa 88,20,52
Ta 12,3,24576
K 167,174,175
Mvaa 88,16,36
Ta 14,3,25600
v_accvgpr_write_b32 a184, v164
Zvaa 120,16,40
Mvaa 120,20,56
buffer_load_dwordx4 v[204:207], v27, s[8:11], s28 offen offset:0 sc1
v_accvgpr_write_b32 a185, v165
Zvaa 152,20,60
v_accvgpr_write_b32 a186, v166
Mvaa 152,16,44
W 8
Ta 16,4,16384
v_accvgpr_write_b32 a187, v167
Zvaa 156,24,44
Ta 18,4,17408
v_accvgpr_write_b32 a188, v240
Mvaa 156,28,60
Zvaa 124,28,56
)"
R"(buffer_load_dwordx4 v[212:215], v27, s[8:11], s93 offen offset:0 sc1
v_accvgpr_write_b32 a189, v241
Mvaa 124,24,40
s_add_u32 s28, s28, 512
s_cmp_eq_u32 s28, s4
s_cselect_b32 s93, 10240, 0
s_sub_u32 s28, s28, s93
Zvaa 92,24,36
s_add_u32 s93, s28, 20480
Ta 20,4,24576
Ta 22,4,25600
Mvaa 92,28,52
v_accvgpr_write_b32 a190, v242
v_accvgpr_write_b32 a191, v243
Zvaa 60,28,48
G 164,192
Mvaa 60,24,32
s_waitcnt vmcnt(4) lgkmcnt(8)
buffer_load_dwordx4 v[220:223], v26, s[8:11], s28 offen offset:0 sc1
G 165,193
Mvaa 32,0,128
Ta 24,5,16384
Ta 26,5,17408
Mvaa 32,4,144
G 166,194
G 167,195
Mvaa 64,4,148
Ta 28,5,24576
Ta 30,5,25600
Mvaa 64,0,132
G 172,196
Mvaa 96,0,136
buffer_load_dwordx4 v[228:231], v26, s[8:11], s93 offen offset:0 sc1
G 173,197
Mvaa 96,4,152
G 174,198
Mvaa 128,4,156
G 175,199
Mvaa 128,0,140
W 8
Ta 0,6,16384
Ta 2,6,17408
Mvaa 132,8,140
K 164,164,165
Mvaa 132,12,156
buffer_load_dwordx4 v[236:239], v27, s[8:11], s28 offen offset:0 sc1
K 165,166,167
Mvaa 100,12,152
Ta 4,6,24576
K 166,172,173
Mvaa 100,8,136
Ta 6,6,25600
K 167,174,175
Mvaa 68,8,132
v_accvgpr_write_b32 a192, v164
Mvaa 68,12,148
Mvaa 36,12,144
buffer_load_dwordx4 v[244:247], v27, s[8:11], s93 offen offset:0 sc1
v_accvgpr_write_b32 a193, v165
Mvaa 36,8,128
W 8
s_add_u32 s28, s28, 512
s_cmp_eq_u32 s28, s4
s_cselect_b32 s93, 10240, 0
Mvaa 40,16,128
s_sub_u32 s28, s28, s93
s_add_u32 s93, s28, 20480
v_accvgpr_write_b32 a194, v166
Mvaa 40,20,144
Ta 8,7,16384
Ta 10,7,17408
Mvaa 72,20,148
v_accvgpr_write_b32 a195, v167
v_accvgpr_write_b32 a196, v248
Mvaa 72,16,132
D 22,48,65,2048
Ta 12,7,24576
Mvaa 104,16,136
Ta 14,7,25600
v_accvgpr_write_b32 a197, v249
Mvaa 104,20,152
v_accvgpr_write_b32 a198, v250
v_accvgpr_write_b32 a199, v251
Mvaa 136,20,156
G 164,200
Mvaa 136,16,140
W 8
Ta 16,8,16384
Ta 18,8,17408
Mvaa 140,24,140
buffer_load_dwordx4 a[32:35], v26, s[8:11], s28 offen offset:0 sc1
G 165,201
Mvaa 140,28,156
G 166,202
Mvaa 108,28,152
Ta 20,8,24576
G 167,203
Mvaa 108,24,136
Ta 22,8,25600
G 172,204
Mvaa 76,24,132
Mvaa 76,28,148
buffer_load_dwordx4 a[36:39], v26, s[8:11], s93 offen offset:0 sc1
G 173,205
Mvaa 44,28,144
G 174,206
Mvaa 44,24,128
W 8
Ta 24,9,16384
G 175,207
Mvaa 48,0,128
Ta 26,9,17408
K 164,164,165
Mvaa 48,4,144
Mvaa 80,4,148
buffer_load_dwordx4 a[40:43], v27, s[8:11], s28 offen offset:0 sc1
K 165,166,167
Mvaa 80,0,132
Ta 28,9,24576
Ta 30,9,25600
Mvaa 112,0,136
s_lshl_b32 s80, s56, 9
K 166,172,173
K 167,174,175
Mvaa 112,4,152
v_accvgpr_write_b32 a200, v164
Mvaa 144,4,156
s_lshl_b32 s81, s57, 4
Mvaa 144,0,140
W 8
buffer_load_dwordx4 a[44:47], v27, s[8:11], s93 offen offset:0 sc1
v_accvgpr_write_b32 a201, v165
Mvaa 148,8,140
s_add_u32 s28, s28, 512
s_cmp_eq_u32 s28, s4
s_cselect_b32 s93, 10240, 0
s_sub_u32 s28, s28, s93
Mvaa 148,12,156
s_add_u32 s93, s28, 20480
s_add_u32 s80, s80, s81
Ta 0,10,0
Mvaa 116,12,152
s_and_b32 s81, s14, 1
Ta 2,10,1024
v_accvgpr_write_b32 a202, v166
Mvaa 116,8,136
s_lshl_b32 s81, s81, 3
Ta 4,10,8192
v_accvgpr_write_b32 a203, v167
Mvaa 84,8,132
s_add_u32 s80, s80, s81
buffer_load_dwordx4 a[48:51], v26, s[8:11], s28 offen offset:0 sc1
Ta 6,10,9216
Mvaa 84,12,148
s_add_u32 s71, s80, 4096
v_accvgpr_write_b32 a204, v184
v_accvgpr_write_b32 a205, v185
Mvaa 52,12,144
v_accvgpr_write_b32 a206, v186
v_accvgpr_write_b32 a207, v187
Mvaa 52,8,128
W 8
Ta 8,11,0
Ta 10,11,1024
Mvaa 56,16,128
G 164,208
Mvaa 56,20,144
D 23,48,65,3072
G 165,209
Mvaa 88,20,148
s_add_u32 s65, s5, 16
s_cmp_ge_u32 s65, 20
s_cselect_b32 s90, 20, 0
s_sub_u32 s65, s65, s90
Mvaa 88,16,132
s_lshl_b32 s65, s65, 18
s_add_u32 m0, s63, 98304
G 166,210
Mvaa 120,16,136
Ta 12,11,8192
Ta 14,11,9216
Mvaa 120,20,152
G 167,211
G 172,212
Mvaa 152,20,156
buffer_load_dwordx4 a[52:55], v26, s[8:11], s93 offen offset:0 sc1
G 173,213
Mvaa 152,16,140
W 8
Ta 16,12,0
G 174,214
Mvaa 156,24,140
s_lshr_b32 s80, s15, 3
Ta 18,12,1024
Mvaa 156,28,156
G 175,215
Mvaa 124,28,152
s_lshl_b32 s80, s80, 1
Ta 20,12,8192
K 164,164,165
Mvaa 124,24,136
s_add_u32 s80, s80, s14
buffer_load_dwordx4 a[56:59], v27, s[8:11], s28 offen offset:0 sc1
Ta 22,12,9216
Mvaa 92,24,132
s_sub_u32 s80, s80, 2
K 165,166,167
K 166,172,173
Mvaa 92,28,148
s_lshl_b32 s80, s80, 12
Mvaa 60,28,144
K 167,174,175
v_min_u32 v28, 496, v1
Mvaa 60,24,128
s_waitcnt vmcnt(9) lgkmcnt(8)
v_accvgpr_write_b32 a208, v164
v_lshl_add_u32 v28, v28, 3, s80
Mvav 32,0,188
buffer_load_dwordx4 a[60:63], v27, s[8:11], s93 offen offset:0 sc1
Ta 24,13,0
Mvav 32,4,220
s_add_u32 s28, s28, 512
s_cmp_eq_u32 s28, s4
s_cselect_b32 s93, 10240, 0
s_sub_u32 s28, s28, s93
Mvav 64,4,228
s_add_u32 s93, s28, 20480
Ta 26,13,1024
v_min_u32 v28, .Lend%=-.Lbeg%=-4, v28
Mvav 64,0,196
v_accvgpr_write_b32 a209, v165
v_accvgpr_write_b32 a210, v166
Mvav 96,0,204
Ta 28,13,8192
Ta 30,13,9216
Mvav 96,4,236
D 16,48,65,0
v_accvgpr_write_b32 a211, v167
Mvav 128,4,244
v_accvgpr_write_b32 a212, v192
v_accvgpr_write_b32 a213, v193
Mvav 128,0,212
W 8
Ta 0,14,0
v_accvgpr_write_b32 a214, v194
Mvav 132,8,212
Ta 2,14,1024
v_add_u32 v2, 65536, v10
Mvav 132,12,244
v_accvgpr_write_b32 a215, v195
G 164,216
Mvav 100,12,236
buffer_load_dwordx4 v[184:187], v26, s[8:11], s28 offen offset:0 sc1
Ta 4,14,8192
Mvav 100,8,204
Ta 6,14,9216
v_add_u32 v3, 65536, v11
Mvav 68,8,196
G 165,217
G 166,218
Mvav 68,12,228
G 167,219
Mvav 36,12,220
G 172,220
Mvav 36,8,188
W 8
D 17,48,65,1024
Ta 8,15,0
Mvav 40,16,188
Ta 10,15,1024
v_add_u32 v4, 65536, v12
Mvav 40,20,220
G 173,221
G 174,222
Mvav 72,20,228
Ta 12,15,8192
G 175,223
Mvav 72,16,196
Ta 14,15,9216
v_add_u32 v5, 65536, v13
Mvav 104,16,204
buffer_load_dwordx4 a[128:131], v26, s[8:11], s93 offen offset:0 sc1
K 164,164,165
Mvav 104,20,236
K 165,166,167
Mvav 136,20,244
K 166,172,173
Mvav 136,16,212
W 8
Ta 16,24,0
K 167,174,175
Mvav 140,24,212
Ta 18,24,1024
v_add_u32 v6, 65536, v14
Mvav 140,28,244
buffer_load_dwordx4 a[132:135], v27, s[8:11], s28 offen offset:0 sc1
v_accvgpr_write_b32 a216, v164
Mvav 108,28,236
Ta 20,24,8192
v_accvgpr_write_b32 a217, v165
Mvav 108,24,204
Ta 22,24,9216
v_add_u32 v7, 65536, v15
Mvav 76,24,196
v_accvgpr_write_b32 a218, v166
v_accvgpr_write_b32 a219, v167
Mvav 76,28,228
v_accvgpr_write_b32 a220, v200
Mvav 44,28,220
buffer_load_dwordx4 a[136:139], v27, s[8:11], s93 offen offset:0 sc1
Mvav 44,24,188
W 8
s_add_u32 s28, s28, 512
s_cmp_eq_u32 s28, s4
s_cselect_b32 s93, 10240, 0
Mvav 48,0,188
s_sub_u32 s28, s28, s93
s_add_u32 s93, s28, 20480
v_accvgpr_write_b32 a221, v201
Mvav 48,4,220
Ta 24,25,0
Ta 26,25,1024
Mvav 80,4,228
v_add_u32 v8, 65536, v24
v_accvgpr_write_b32 a222, v202
Mvav 80,0,196
buffer_load_dwordx4 a[140:143], v26, s[8:11], s28 offen offset:0 sc1
v_accvgpr_write_b32 a223, v203
Mvav 112,0,204
Ta 28,25,8192
Ta 30,25,9216
Mvav 112,4,236
v_add_u32 v9, 65536, v25
G 164,224
Mvav 144,4,244
G 165,225
G 166,226
Mvav 144,0,212
W 8
Ta 0,10,16384
G 167,227
Mvav 148,8,212
global_load_dword v29, v28, s[6:7]
Ta 2,10,17408
Mvav 148,12,244
s_sub_u32 s74, s85, 66
s_and_b32 s75, s74, 0xffffffe3
s_cselect_b64 s[72:73], -1, 0
s_and_b32 s74, s74, 28
Mvav 116,12,236
s_lshl_b32 s74, s74, 1
v_mov_b32 v28, s74
G 172,228
Mvav 116,8,204
G 173,229
G 174,230
Mvav 84,8,196
Ta 4,10,24576
Ta 6,10,25600
Mvav 84,12,228
buffer_load_dwordx4 a[144:147], v26, s[8:11], s93 offen offset:0 sc1
G 175,231
Mvav 52,12,220
K 164,164,165
K 165,166,167
Mvav 52,8,188
W 8
Ta 8,11,16384
K 166,172,173
Mvav 56,16,188
Ta 10,11,17408
K 167,174,175
Mvav 56,20,220
v_accvgpr_write_b32 a224, v164
Mvav 88,20,228
buffer_load_dwordx4 a[148:151], v27, s[8:11], s28 offen offset:0 sc1
Ta 12,11,24576
Mvav 88,16,196
Ta 14,11,25600
v_accvgpr_write_b32 a225, v165
Mvav 120,16,204
v_accvgpr_write_b32 a226, v166
Mvav 120,20,236
v_accvgpr_write_b32 a227, v167
Mvav 152,20,244
v_accvgpr_write_b32 a228, v208
Mvav 152,16,212
W 8
D 18,48,65,2048
Ta 16,12,16384
Mvav 156,24,212
Ta 18,12,17408
v_accvgpr_write_b32 a229, v209
Mvav 156,28,244
v_accvgpr_write_b32 a230, v210
Mvav 124,28,236
Ta 20,12,24576
v_accvgpr_write_b32 a231, v211
Mvav 124,24,204
Ta 22,12,25600
)"
R"(G 164,232
Mvav 92,24,196
buffer_load_dwordx4 a[152:155], v27, s[8:11], s93 offen offset:0 sc1
G 165,233
Mvav 92,28,228
s_add_u32 s28, s28, 512
s_cmp_eq_u32 s28, s4
s_cselect_b32 s93, 10240, 0
s_sub_u32 s28, s28, s93
Mvav 60,28,220
s_add_u32 s93, s28, 20480
G 166,234
G 167,235
Mvav 60,24,188
s_waitcnt vmcnt(12) lgkmcnt(8)
G 172,236
G 173,237
Mvaa 32,0,32
Ta 24,13,16384
Ta 26,13,17408
Mvaa 32,4,48
buffer_load_dwordx2 v[28:29], v28, s[8:11], 0 offen offset:3584 sc1
G 174,238
Mvaa 64,4,52
G 175,239
K 164,164,165
Mvaa 64,0,36
Ta 28,13,24576
Ta 30,13,25600
Mvaa 96,0,40
K 165,166,167
K 166,172,173
Mvaa 96,4,56
K 167,174,175
v_accvgpr_write_b32 a232, v164
Mvaa 128,4,60
buffer_load_dwordx4 a[156:159], v26, s[8:11], s28 offen offset:0 sc1
v_accvgpr_write_b32 a233, v165
Mvaa 128,0,44
W 8
v_accvgpr_write_b32 a234, v166
v_accvgpr_write_b32 a235, v167
Mvaa 132,8,44
Ta 0,14,16384
Ta 2,14,17408
Mvaa 132,12,60
G 164,160
G 165,161
Mvaa 100,12,56
G 166,162
G 167,163
Mvaa 100,8,40
buffer_load_dwordx4 v[192:195], v26, s[8:11], s93 offen offset:0 sc1
Ta 4,14,24576
Mvaa 68,8,36
Ta 6,14,25600
G 172,164
Mvaa 68,12,52
G 173,165
G 174,166
Mvaa 36,12,48
G 175,167
K 164,164,165
Mvaa 36,8,32
W 8
Ta 8,15,16384
K 165,166,167
Mvaa 40,16,32
buffer_load_dwordx4 v[200:203], v27, s[8:11], s28 offen offset:0 sc1
Ta 10,15,17408
Mvaa 40,20,48
K 166,172,173
K 167,174,175
Mvaa 72,20,52
s_cmp_lg_u32 s72, 0
s_cbranch_scc1 .LL_55%=
s_waitcnt vmcnt(3)
.LL_55%=:
v_xor_b32 v28, s12, v28
v_xor_b32 v29, s13, v29
v_lshrrev_b32 v248, 11, v28
v_or_b32 v29, v29, v248
v_cmp_eq_u32 vcc, 0, v29
s_or_b64 vcc, vcc, s[72:73]
s_cmp_eq_u64 vcc, -1
s_cbranch_scc0 .Lspin_56%=
.Lok_57%=:
v_accvgpr_write_b32 a236, v164
v_accvgpr_write_b32 a237, v165
Mvaa 72,16,36
Ta 12,15,24576
Ta 14,15,25600
Mvaa 104,16,40
v_accvgpr_write_b32 a238, v166
v_accvgpr_write_b32 a239, v167
Mvaa 104,20,56
D 19,48,65,3072
G 164,240
Mvaa 136,20,60
s_add_u32 m0, s63, 102400
G 165,241
G 166,242
Mvaa 136,16,44
W 8
v_bfe_u32 v248, v28, 3, 8
G 167,243
Mvaa 140,24,44
Ta 16,24,16384
Ta 18,24,17408
Mvaa 140,28,60
v_mul_u32_u24 v248, 10240, v248
G 172,244
Mvaa 108,28,56
buffer_load_dwordx4 v[188:191], v27, s[8:11], s93 offen offset:0 sc1
G 173,245
Mvaa 108,24,40
s_add_u32 s28, s28, 512
s_cmp_eq_u32 s28, s4
s_cselect_b32 s93, 10240, 0
s_sub_u32 s28, s28, s93
Mvaa 76,24,36
s_add_u32 s93, s28, 20480
v_and_b32 v28, 7, v28
G 174,246
Mvaa 76,28,52
v_lshl_add_u32 v248, v28, 4, v248
G 175,247
Mvaa 44,28,48
Ta 20,24,24576
Ta 22,24,25600
Mvaa 44,24,32
W 8
buffer_load_dwordx4 v[196:199], v26, s[8:11], s28 offen offset:0 sc1
K 164,164,165
Mvaa 48,0,32
v_lshl_add_u32 v248, v1, 3, v248
K 165,166,167
Mvaa 48,4,48
v_lshrrev_b32 v29, 8, v1
K 166,172,173
Mvaa 80,4,52
K 167,174,175
v_lshlrev_b32 v29, 28, v29
Mvaa 80,0,36
v_accvgpr_write_b32 a240, v164
v_or_b32 v249, v248, v29
Mvaa 112,0,40
s_and_b32 s75, s72, 134217728
buffer_load_dwordx4 v[204:207], v26, s[8:11], s93 offen offset:0 sc1
v_accvgpr_write_b32 a241, v165
Mvaa 112,4,56
s_add_u32 s76, s75, 131072
Ta 24,25,16384
Ta 26,25,17408
Mvaa 144,4,60
v_accvgpr_write_b32 a242, v166
v_accvgpr_write_b32 a243, v167
Mvaa 144,0,44
W 6
G 164,168
G 165,169
Mvaa 148,8,44
Ta 28,25,24576
Ta 30,25,25600
Mvaa 148,12,60
D 20,48,65,0
G 166,170
Mvaa 116,12,56
G 167,171
G 172,172
Mvaa 116,8,40
G 173,173
G 174,174
Mvaa 84,8,36
Ta 0,2,0
G 175,175
Mvaa 84,12,52
Ta 2,2,1024
K 164,164,165
Mvaa 52,12,48
buffer_load_dwordx4 v[212:215], v27, s[8:11], s28 offen offset:0 sc1
K 165,166,167
Mvaa 52,8,32
W 6
K 166,172,173
K 167,174,175
Mvaa 56,16,32
Ta 4,2,8192
Ta 6,2,9216
Mvaa 56,20,48
v_accvgpr_write_b32 a244, v164
v_accvgpr_write_b32 a245, v165
Mvaa 88,20,52
v_accvgpr_write_b32 a246, v166
v_accvgpr_write_b32 a247, v167
Mvaa 88,16,36
buffer_load_dwordx4 v[220:223], v27, s[8:11], s93 offen offset:0 sc1
G 164,64
Mvaa 120,16,40
s_add_u32 s28, s28, 512
s_cmp_eq_u32 s28, s4
s_cselect_b32 s93, 10240, 0
s_sub_u32 s28, s28, s93
Mvaa 120,20,56
s_add_u32 s93, s28, 20480
Ta 8,3,0
G 165,65
Mvaa 152,20,60
Ta 10,3,1024
G 166,66
Mvaa 152,16,44
W 6
G 167,67
G 172,68
Mvaa 156,24,44
buffer_load_dwordx4 v[228:231], v26, s[8:11], s28 offen offset:0 sc1
G 173,69
Mvaa 156,28,60
Ta 12,3,8192
Ta 14,3,9216
Mvaa 124,28,56
G 174,70
G 175,71
Mvaa 124,24,40
K 164,164,165
K 165,166,167
Mvaa 92,24,36
K 166,172,173
K 167,174,175
Mvaa 92,28,52
buffer_load_dwordx4 v[240:243], v248, s[8:11], s76 offen sc1
Ta 16,4,0
Mvaa 60,28,48
s_add_u32 s76, s76, 8192
Ta 18,4,1024
v_accvgpr_write_b32 a64, v164
Mvaa 60,24,32
s_waitcnt vmcnt(13) lgkmcnt(6)
v_accvgpr_write_b32 a65, v165
v_accvgpr_write_b32 a66, v166
Mvav 32,0,184
Ta 20,4,8192
v_accvgpr_write_b32 a67, v167
Mvaa 32,4,140
Ta 22,4,9216
G 164,176
Mvaa 64,4,144
buffer_load_dwordx4 v[244:247], v249, s[8:11], s76 offen sc1
v_or_b32 v249, v1, v29
Mvaa 64,0,128
s_mul_i32 s76, s74, 160
s_add_u32 s76, s76, s75
s_add_u32 s76, s76, 118784
Mvaa 96,0,132
G 165,177
G 166,178
Mvaa 96,4,148
G 167,179
G 172,180
Mvaa 128,4,152
Ta 24,5,0
G 173,181
Mvaa 128,0,136
W 5
buffer_load_dwordx4 v[236:239], v26, s[8:11], s93 offen offset:0 sc1
Ta 26,5,1024
Mvaa 132,8,136
G 174,182
G 175,183
Mvaa 132,12,152
K 164,164,165
K 165,166,167
Mvaa 100,12,148
Ta 28,5,8192
Ta 30,5,9216
Mvaa 100,8,132
K 166,172,173
K 167,174,175
Mvaa 68,8,128
D 21,48,65,1024
v_accvgpr_write_b32 a68, v164
Mvaa 68,12,144
v_accvgpr_write_b32 a69, v165
v_accvgpr_write_b32 a70, v166
Mvaa 36,12,140
Ta 0,6,0
v_accvgpr_write_b32 a71, v167
Mvav 36,8,184
W 5
Ta 2,6,1024
G 164,72
Mvav 40,16,184
G 165,73
G 166,74
Mvaa 40,20,140
buffer_load_dwordx4 v[208:211], v27, s[8:11], s28 offen offset:0 sc1
Ta 4,6,8192
Mvaa 72,20,144
Ta 6,6,9216
G 167,75
Mvaa 72,16,128
G 172,76
G 173,77
Mvaa 104,16,132
Ta 8,7,0
G 174,78
Mvaa 104,20,148
Ta 10,7,1024
G 175,79
Mvaa 136,20,152
buffer_load_dwordx4 a[32:35], v27, s[8:11], s93 offen offset:0 sc1
K 164,164,165
Mvaa 136,16,136
W 6
s_add_u32 s28, s28, 512
s_cmp_eq_u32 s28, s4
s_cselect_b32 s93, 10240, 0
Mvaa 140,24,136
s_sub_u32 s28, s28, s93
s_add_u32 s93, s28, 20480
Ta 12,7,8192
Mvaa 140,28,152
Ta 14,7,9216
K 165,166,167
Mvaa 108,28,148
K 166,172,173
K 167,174,175
Mvaa 108,24,132
buffer_load_dwordx4 a[36:39], v26, s[8:11], s28 offen offset:0 sc1
v_accvgpr_write_b32 a72, v164
Mvaa 76,24,128
Ta 16,8,0
Ta 18,8,1024
Mvaa 76,28,144
v_accvgpr_write_b32 a73, v165
v_accvgpr_write_b32 a74, v166
Mvaa 44,28,140
Ta 20,8,8192
v_accvgpr_write_b32 a75, v167
Mvav 44,24,184
W 7
Ta 22,8,9216
v_accvgpr_write_b32 a76, v216
Mvav 48,0,184
D 22,48,65,2048
v_accvgpr_write_b32 a77, v217
Mvaa 48,4,140
v_accvgpr_write_b32 a78, v218
v_accvgpr_write_b32 a79, v219
Mvaa 80,4,144
Ta 24,9,0
Ta 26,9,1024
Mvaa 80,0,128
G 164,80
G 165,81
Mvaa 112,0,132
Ta 28,9,8192
Ta 30,9,9216
Mvaa 112,4,148
buffer_load_dwordx4 a[40:43], v26, s[8:11], s93 offen offset:0 sc1
G 166,82
Mvaa 144,4,152
G 167,83
G 172,84
Mvaa 144,0,136
W 8
Ta 0,2,16384
G 173,85
Mvaa 148,8,136
Ta 2,2,17408
G 174,86
Mvaa 148,12,152
G 175,87
Mvaa 116,12,148
buffer_load_dwordx4 a[44:47], v27, s[8:11], s28 offen offset:0 sc1
Ta 4,2,24576
Mvaa 116,8,132
Ta 6,2,25600
K 164,164,165
Mvaa 84,8,128
K 165,166,167
Mvaa 84,12,144
K 166,172,173
Mvaa 52,12,140
K 167,174,175
Mvav 52,8,184
W 8
buffer_load_dwordx4 a[48:51], v27, s[8:11], s93 offen offset:0 sc1
Ta 8,3,16384
Mvav 56,16,184
s_add_u32 s28, s28, 512
s_cmp_eq_u32 s28, s4
s_cselect_b32 s93, 10240, 0
D 23,48,65,3072
Mvaa 56,20,140
s_sub_u32 s28, s28, s93
s_add_u32 s93, s28, 20480
Ta 10,3,17408
Mvaa 88,20,144
v_accvgpr_write_b32 a80, v164
)"
R"(v_accvgpr_write_b32 a81, v165
Mvaa 88,16,128
Ta 12,3,24576
v_accvgpr_write_b32 a82, v166
Mvaa 120,16,132
Ta 14,3,25600
v_accvgpr_write_b32 a83, v167
Mvaa 120,20,148
buffer_load_dwordx4 a[52:55], v26, s[8:11], s28 offen offset:0 sc1
v_accvgpr_write_b32 a84, v224
Mvaa 152,20,152
v_lshrrev_b32 v20, 4, v1
v_accvgpr_write_b32 a85, v225
Mvaa 152,16,136
W 8
v_accvgpr_write_b32 a86, v226
v_add_u32 v20, 0, v20
Mvaa 156,24,136
Ta 16,4,16384
Ta 18,4,17408
Mvaa 156,28,152
v_accvgpr_write_b32 a87, v227
v_mul_u32_u24 v22, 205, v20
Mvaa 124,28,148
buffer_load_dwordx4 a[56:59], v26, s[8:11], s93 offen offset:0 sc1
G 164,88
Mvaa 124,24,132
Ta 20,4,24576
Ta 22,4,25600
Mvaa 92,24,128
v_lshrrev_b32 v22, 12, v22
G 165,89
Mvaa 92,28,144
G 166,90
v_mul_u32_u24 v23, 20, v22
Mvaa 60,28,140
G 167,91
v_sub_u32 v20, v20, v23
Mvav 60,24,184
s_waitcnt vmcnt(15) lgkmcnt(8)
buffer_load_dwordx4 a[60:63], v27, s[8:11], s28 offen offset:0 sc1
G 172,92
Mvaa 32,0,156
Ta 24,5,16384
Ta 26,5,17408
Mvav 32,4,196
v_lshrrev_b32 v23, 1, v22
G 173,93
Mvav 64,4,204
G 174,94
v_and_b32 v23, 2, v23
Mvav 64,0,192
Ta 28,5,24576
Ta 30,5,25600
Mvav 96,0,200
buffer_load_dwordx4 v[216:219], v27, s[8:11], s93 offen offset:0 sc1
G 175,95
Mvav 96,4,212
s_add_u32 s28, s28, 512
s_cmp_eq_u32 s28, s4
s_cselect_b32 s93, 10240, 0
s_sub_u32 s28, s28, s93
Mvav 128,4,220
s_add_u32 s93, s28, 20480
v_xor_b32 v20, v20, v23
K 164,164,165
Mvav 128,0,188
W 8
v_mul_u32_u24 v22, 80, v22
K 165,166,167
Mvav 132,8,188
K 166,172,173
v_add_lshl_u32 v16, v20, v22, 4
Mvav 132,12,220
buffer_load_dwordx4 v[184:187], v26, s[8:11], s28 offen offset:0 sc1
Ta 0,6,16384
Mvav 100,12,212
Ta 2,6,17408
K 167,174,175
Mvav 100,8,200
v_lshrrev_b32 v20, 4, v1
v_accvgpr_write_b32 a88, v164
Mvav 68,8,192
v_add_u32 v20, 64, v20
v_accvgpr_write_b32 a89, v165
Mvav 68,12,204
Ta 4,6,24576
Ta 6,6,25600
Mvav 36,12,196
s_cmp_lg_u32 s72, 0
s_cbranch_scc1 .LL_59%=
s_waitcnt vmcnt(15)
.LL_59%=:
buffer_store_dwordx4 v[240:243], v1, s[8:11], s76 offen sc1
v_accvgpr_write_b32 a90, v166
v_mul_u32_u24 v22, 205, v20
Mvaa 36,8,156
W 8
v_accvgpr_write_b32 a91, v167
v_lshrrev_b32 v22, 12, v22
Mvaa 40,16,156
v_accvgpr_write_b32 a92, v232
v_mul_u32_u24 v23, 20, v22
Mvav 40,20,196
Ta 8,7,16384
Ta 10,7,17408
Mvav 72,20,204
v_accvgpr_write_b32 a93, v233
v_accvgpr_write_b32 a94, v234
Mvav 72,16,192
buffer_store_dwordx4 v[244:247], v249, s[8:11], s76 offen offset:1024 sc1
v_sub_u32 v20, v20, v23
Mvav 104,16,200
s_add_u32 s76, s74, s75
s_add_u32 s76, s76, 3840
v_accvgpr_write_b32 a95, v235
Mvav 104,20,212
Ta 12,7,24576
Ta 14,7,25600
Mvav 136,20,220
v_lshrrev_b32 v23, 1, v22
G 164,96
Mvav 136,16,188
W 8
G 165,97
v_and_b32 v23, 2, v23
Mvav 140,24,188
buffer_load_dwordx4 a[128:131], v26, s[8:11], s93 offen offset:0 sc1
G 166,98
Mvav 140,28,220
G 167,99
v_xor_b32 v20, v20, v23
Mvav 108,28,212
G 172,100
G 173,101
Mvav 108,24,200
Ta 16,8,16384
Ta 18,8,17408
Mvav 76,24,192
v_mul_u32_u24 v22, 80, v22
G 174,102
Mvav 76,28,204
buffer_load_dwordx4 a[132:135], v27, s[8:11], s28 offen offset:0 sc1
G 175,103
Mvav 44,28,196
v_add_lshl_u32 v17, v20, v22, 4
K 164,164,165
Mvaa 44,24,156
W 6
Ta 20,8,24576
Ta 22,8,25600
Mvaa 48,0,156
K 165,166,167
K 166,172,173
Mvav 48,4,196
v_subrev_u32 v17, 1024, v17
K 167,174,175
Mvav 80,4,204
buffer_load_dwordx4 a[136:139], v27, s[8:11], s93 offen offset:0 sc1
v_accvgpr_write_b32 a96, v164
Mvav 80,0,192
s_add_u32 s28, s28, 512
v_lshrrev_b32 v20, 4, v1
v_accvgpr_write_b32 a97, v165
Mvav 112,0,200
s_cmp_eq_u32 s28, s4
s_cselect_b32 s93, 10240, 0
v_accvgpr_write_b32 a98, v166
v_add_u32 v20, 128, v20
Mvav 112,4,212
s_sub_u32 s28, s28, s93
Ta 24,9,16384
v_accvgpr_write_b32 a99, v167
Mvav 144,4,220
s_add_u32 s93, s28, 20480
Ta 26,9,17408
G 164,248
Mvav 144,0,188
W 6
buffer_load_dwordx4 a[140:143], v26, s[8:11], s28 offen offset:0 sc1
v_mul_u32_u24 v22, 205, v20
Mvav 148,8,188
G 165,249
G 166,250
Mvav 148,12,220
Ta 28,9,24576
Ta 30,9,25600
Mvav 116,12,212
v_lshrrev_b32 v22, 12, v22
G 167,251
Mvav 116,8,200
G 172,252
G 173,253
Mvav 84,8,192
s_waitcnt vmcnt(11) lgkmcnt(0)
s_barrier
v_mul_u32_u24 v23, 20, v22
Mvav 84,12,204
G 174,254
G 175,255
Mvav 52,12,196
v_sub_u32 v20, v20, v23
K 164,164,165
Mvaa 52,8,156
K 165,166,167
v_lshrrev_b32 v23, 1, v22
Mvaa 56,16,156
buffer_load_dwordx4 a[144:147], v26, s[8:11], s93 offen offset:0 sc1
K 166,172,173
Mvav 56,20,196
K 167,174,175
v_and_b32 v23, 2, v23
Mvav 88,20,204
v_accvgpr_write_b32 a100, v164
v_accvgpr_write_b32 a101, v165
Mvav 88,16,192
v_xor_b32 v20, v20, v23
v_accvgpr_write_b32 a102, v166
Mvav 120,16,200
Ta 0,10,32768
v_accvgpr_write_b32 a103, v167
Mvav 120,20,212
Ta 2,10,33792
Ta 4,10,40960
Mvav 152,20,220
Ta 6,10,41984
G 164,104
Mvav 152,16,188
v_mul_u32_u24 v22, 80, v22
G 165,105
Mvav 156,24,188
v_add_lshl_u32 v18, v20, v22, 4
G 166,106
Mvav 156,28,220
Ta 8,11,32768
Ta 10,11,33792
Mvav 124,28,212
G 167,107
v_subrev_u32 v18, 2048, v18
Mvav 124,24,200
buffer_load_dwordx4 a[148:151], v27, s[8:11], s28 offen offset:0 sc1
G 172,108
Mvav 92,24,192
G 173,109
v_lshrrev_b32 v20, 4, v1
Mvav 92,28,204
Ta 12,11,40960
Ta 14,11,41984
Mvav 60,28,196
G 174,110
v_add_u32 v20, 192, v20
Mvaa 60,24,156
W 4
G 175,111
K 164,164,165
Mvav 32,0,228
v_mul_u32_u24 v22, 205, v20
K 165,166,167
Mvaa 32,4,36
v_lshrrev_b32 v22, 12, v22
K 166,172,173
Mvaa 64,4,40
Ta 16,12,32768
Ta 18,12,33792
Mvav 64,0,236
buffer_load_dwordx4 a[152:155], v27, s[8:11], s93 offen offset:0 sc1
K 167,174,175
Mvav 96,0,208
s_add_u32 s28, s28, 512
v_mul_u32_u24 v23, 20, v22
v_accvgpr_write_b32 a104, v164
Mvaa 96,4,44
s_cmp_eq_u32 s28, s4
s_cselect_b32 s93, 10240, 0
v_accvgpr_write_b32 a105, v165
v_sub_u32 v20, v20, v23
Mvaa 128,4,48
s_sub_u32 s28, s28, s93
Ta 20,12,40960
Ta 22,12,41984
Mvaa 128,0,32
W 4
v_accvgpr_write_b32 a106, v166
v_lshrrev_b32 v23, 1, v22
Mvaa 132,8,32
s_add_u32 s93, s28, 20480
v_accvgpr_write_b32 a107, v167
K 176,176,177
Mvaa 132,12,48
v_and_b32 v23, 2, v23
K 177,178,179
Mvaa 100,12,44
K 178,180,181
v_xor_b32 v20, v20, v23
Mvav 100,8,208
Ta 24,13,32768
Ta 26,13,33792
Mvav 68,8,236
buffer_load_dwordx4 v[224:227], v26, s[8:11], s28 offen offset:0 sc1
K 179,182,183
Mvaa 68,12,40
v_accvgpr_write_b32 a108, v176
v_accvgpr_write_b32 a109, v177
v_accvgpr_write_b32 a110, v178
v_accvgpr_write_b32 a111, v179
Mvaa 36,12,36
Ta 28,13,40960
v_mul_u32_u24 v22, 80, v22
Mvav 36,8,228
W 3
Ta 30,13,41984
v_add_lshl_u32 v19, v20, v22, 4
Mvav 40,16,228
v_subrev_u32 v19, 3072, v19
G 164,112
Mvaa 40,20,36
s_cmp_lg_u32 s72, 0
s_cbranch_scc1 .LL_60%=
s_waitcnt vmcnt(8)
.LL_60%=:
v_mov_b64 v[28:29], s[12:13]
s_mov_b64 exec, 1
buffer_store_dwordx2 v[28:29], off, s[8:11], s76 sc1
s_mov_b64 exec, -1
v_lshrrev_b32 v20, 4, v1
G 165,113
Mvaa 72,20,40
Ta 0,14,32768
Ta 2,14,33792
Mvav 72,16,236
G 166,114
v_add_u32 v20, 256, v20
Mvav 104,16,208
G 167,115
v_mul_u32_u24 v22, 205, v20
Mvaa 104,20,44
Ta 4,14,40960
Ta 6,14,41984
Mvaa 136,20,48
buffer_load_dwordx4 v[232:235], v26, s[8:11], s93 offen offset:0 sc1
G 172,116
Mvaa 136,16,32
W 4
v_lshrrev_b32 v22, 12, v22
G 173,117
Mvaa 140,24,32
G 174,118
v_mul_u32_u24 v23, 20, v22
Mvaa 140,28,48
G 175,119
v_sub_u32 v20, v20, v23
Mvaa 108,28,44
Ta 8,15,32768
Ta 10,15,33792
Mvav 108,24,208
buffer_load_dwordx4 a[248:251], v27, s[8:11], s28 offen offset:0 sc1
K 164,164,165
Mvav 76,24,236
v_lshrrev_b32 v23, 1, v22
K 165,166,167
Mvaa 76,28,40
K 166,172,173
v_and_b32 v23, 2, v23
Mvaa 44,28,36
Ta 12,15,40960
Ta 14,15,41984
Mvav 44,24,228
W 4
K 167,174,175
v_xor_b32 v20, v20, v23
Mvav 48,0,228
v_accvgpr_write_b32 a112, v164
v_accvgpr_write_b32 a113, v165
Mvaa 48,4,36
v_mul_u32_u24 v22, 80, v22
v_accvgpr_write_b32 a114, v166
Mvaa 80,4,40
Ta 16,24,32768
Ta 18,24,33792
Mvav 80,0,236
v_add_lshl_u32 v20, v20, v22, 4
v_accvgpr_write_b32 a115, v167
Mvav 112,0,208
buffer_load_dwordx4 a[252:255], v27, s[8:11], s93 offen offset:0 sc1
v_lshrrev_b32 v22, 6, v1
Mvaa 112,4,44
s_add_u32 s28, s28, 512
Ta 20,24,40960
v_accvgpr_write_b32 a116, v168
Mvaa 144,4,48
v_accvgpr_write_b32 a117, v169
v_accvgpr_write_b32 a118, v170
v_accvgpr_write_b32 a119, v171
Mvaa 144,0,32
W 3
Ta 22,24,41984
v_mul_u32_u24 v22, 320, v22
Mvaa 148,8,32
s_cmp_eq_u32 s28, s4
s_cselect_b32 s93, 10240, 0
)"
R"(v_bfe_u32 v23, v1, 4, 2
v_lshl_add_u32 v22, v23, 3, v22
Mvaa 148,12,48
s_sub_u32 s28, s28, s93
Ta 24,25,32768
G 164,120
Mvaa 116,12,44
s_add_u32 s93, s28, 20480
Ta 26,25,33792
G 165,121
Mvav 116,8,208
buffer_load_dwordx4 v[192:195], v26, s[8:11], s28 offen offset:0 sc1
v_bfe_u32 v23, v1, 8, 1
Mvav 84,8,236
G 166,122
v_lshlrev_b32 v23, 5, v23
Mvaa 84,12,40
Ta 28,25,40960
G 167,123
Mvaa 52,12,36
Ta 30,25,41984
v_add_u32 v21, v22, v23
Mvav 52,8,228
W 4
G 172,124
G 173,125
Mvav 56,16,228
v_sub_u32 v22, v22, v23
G 174,126
Mvaa 56,20,36
s_mul_i32 s94, s59, 655360
Ta 0,10,49152
Ta 2,10,50176
Mvaa 88,20,40
s_mul_i32 s95, s58, 320
buffer_load_dwordx4 v[200:203], v26, s[8:11], s93 offen offset:0 sc1
G 175,127
Mvav 88,16,236
s_add_u32 s94, s94, s95
K 164,164,165
K 165,166,167
Mvav 120,16,208
Ta 4,10,57344
Ta 6,10,58368
Mvaa 120,20,44
s_add_u32 s40, s26, s94
s_addc_u32 s41, s27, 0
K 166,172,173
Mvaa 152,20,48
s_and_b32 s41, s41, 0xffff
K 167,174,175
v_accvgpr_write_b32 a120, v164
Mvaa 152,16,32
W 4
Ta 8,11,49152
v_accvgpr_write_b32 a121, v165
Mvaa 156,24,32
s_mov_b32 s42, 5242880
Ta 10,11,50176
buffer_load_dwordx4 v[188:191], v27, s[8:11], s28 offen offset:0 sc1
Mvaa 156,28,48
s_mov_b32 s43, 0x20000
v_accvgpr_write_b32 a122, v166
v_accvgpr_write_b32 a123, v167
Mvaa 124,28,44
s_sub_u32 s42, s42, s94
Ta 12,11,57344
Ta 14,11,58368
Mvav 124,24,208
s_mov_b64 s[44:45], s[8:9]
v_accvgpr_write_b32 a124, v160
v_accvgpr_write_b32 a125, v161
Mvav 92,24,236
s_mov_b32 s47, 0x20000
v_accvgpr_write_b32 a126, v162
v_accvgpr_write_b32 a127, v163
Mvaa 92,28,40
s_and_b32 s48, s14, 1
Ta 16,12,49152
Ta 18,12,50176
Mvaa 60,28,36
s_mul_i32 s48, s48, 5120
buffer_load_dwordx4 v[196:199], v27, s[8:11], s93 offen offset:0 sc1
v_bfe_u32 v28, v1, 4, 2
Mvav 60,24,228
s_waitcnt vmcnt(13) lgkmcnt(6)
Ta 20,12,57344
v_lshlrev_b32 v28, 3, v28
Mvaa 32,0,52
s_lshl_b32 s80, s56, 9
s_add_u32 s80, s80, 4096
Ta 22,12,58368
Mvav 32,4,184
s_lshr_b32 s51, s14, 1
s_and_b32 s94, s14, 1
Ta 24,13,49152
Mvaa 64,4,128
s_add_u32 s28, s28, 512
s_cmp_eq_u32 s28, s4
s_cselect_b32 s93, 10240, 0
Ta 26,13,50176
Mvaa 64,0,56
s_sub_u32 s28, s28, s93
s_add_u32 s93, s28, 20480
buffer_load_dwordx2 v[28:29], v28, s[8:11], s80 offen sc1
Mvaa 96,0,60
s_lshl_b32 s95, s57, 1
Ta 28,13,57344
Ta 30,13,58368
Mvaa 96,4,132
s_add_u32 s94, s94, s95
s_lshl_b32 s54, s94, 3
Mvaa 128,4,136
s_add_u32 s54, s54, s59
Mvav 128,0,216
W 8
Ta 0,14,49152
Ta 2,14,50176
Mvav 132,8,216
s_lshl_b32 s54, s54, 6
buffer_load_dwordx4 v[204:207], v26, s[8:11], s28 offen offset:0 sc1
Mvaa 132,12,136
s_xor_b32 s55, s54, 15872
Mvaa 100,12,132
Ta 4,14,57344
Ta 6,14,58368
Mvaa 100,8,60
s_add_u32 s54, s54, 20480
Mvaa 68,8,56
s_and_b32 s81, s14, 1
Mvaa 68,12,128
s_add_u32 s55, s55, 20480
buffer_load_dwordx4 v[212:215], v26, s[8:11], s93 offen offset:0 sc1
Mvav 36,12,184
Mvaa 36,8,52
W 8
s_and_b32 s95, s14, 2
s_mul_i32 s91, s81, 17164288
Mvaa 40,16,52
s_lshl_b32 s94, s94, 2
Ta 8,15,49152
Ta 10,15,50176
Mvav 40,20,184
s_mul_i32 s82, s56, 536576
Mvaa 72,20,128
s_add_u32 s94, s94, s95
buffer_load_dwordx4 v[220:223], v27, s[8:11], s28 offen offset:0 sc1
Ta 12,15,57344
Mvaa 72,16,56
Ta 14,15,58368
Mvaa 104,16,60
s_lshl_b32 s95, s95, 4
s_add_u32 s91, s91, s82
Mvaa 104,20,132
Mvaa 136,20,136
s_add_u32 s55, s55, s95
s_add_u32 s91, s91, 4194304
Mvav 136,16,216
W 8
s_lshl_b32 s49, s94, 17
Ta 16,24,49152
Mvav 140,24,216
Ta 18,24,50176
buffer_load_dwordx4 v[176:179], v27, s[8:11], s93 offen offset:0 sc1
Mvaa 140,28,136
s_lshl_b32 s95, s59, 14
s_lshr_b32 s82, s14, 1
Mvaa 108,28,132
Ta 20,24,57344
Ta 22,24,58368
Mvaa 108,24,60
s_add_u32 s49, s49, s95
s_lshl_b32 s83, s82, 14
Mvaa 76,24,56
s_xor_b32 s50, s49, 16252928
Mvaa 76,28,128
Mvav 44,28,184
s_add_u32 s91, s91, s83
s_add_u32 s49, s49, 39845888
Mvaa 44,24,52
W 8
Ta 24,25,49152
Ta 26,25,50176
Mvaa 48,0,52
s_add_u32 s50, s50, 39845888
s_lshl_b32 s81, s81, 14
Mvav 48,4,184
Mvaa 80,4,128
s_lshr_b32 s94, s15, 3
Ta 28,25,57344
Ta 30,25,58368
Mvaa 80,0,56
s_lshl_b32 s82, s82, 16
s_lshl_b32 s94, s94, 3
Mvaa 112,0,60
Mvaa 112,4,132
s_add_u32 s94, s94, s14
s_or_b32 s92, s81, s82
Mvaa 144,4,136
Mvav 144,0,216
W 8
s_lshl_b32 s94, s94, 4
Ta 0,2,32768
Mvav 148,8,216
s_xor_b32 s92, s92, 0x10000
s_add_u32 s94, s94, s59
Ta 2,2,33792
Mvaa 148,12,136
Mvaa 116,12,132
s_mul_i32 s52, s94, 5120
Ta 4,2,40960
Ta 6,2,41984
Mvaa 116,8,60
Mvaa 84,8,56
s_lshl_b32 s95, s59, 12
Mvaa 84,12,128
Mvav 52,12,184
s_sub_u32 s52, s52, s95
Mvaa 52,8,52
W 8
s_add_u32 s52, s52, 56623104
Ta 8,3,32768
Mvaa 56,16,52
Ta 10,3,33792
Mvav 56,20,184
s_lshl_b32 s53, s94, 3
Mvaa 88,20,128
Ta 12,3,40960
Ta 14,3,41984
Mvaa 88,16,56
s_add_u32 s53, s53, 65536
Mvaa 120,16,60
s_waitcnt vmcnt(4)
v_cmp_eq_u64 vcc, s[12:13], v[28:29]
s_cmp_eq_u64 vcc, -1
s_cbranch_scc0 .Lspin_61%=
.Lok_62%=:
s_sub_u32 s82, s91, 16384
Mvaa 120,20,132
s_xor_b32 s83, s92, 0x10000
s_add_u32 m0, s83, 0
s_add_u32 s81, s82, 0
Mvaa 152,20,136
v_bfe_u32 v244, v1, 4, 1
v_mul_u32_u24 v244, 0xaaaa, v244
Mvav 152,16,216
W 8
Dc 1,8,81,0
v_add_u32 v244, 0x4444, v244
Mvav 156,24,216
Ta 16,4,32768
Ta 18,4,33792
Mvaa 156,28,136
v_lshl_or_b32 v240, v244, 16, v244
Mvaa 124,28,132
Ta 20,4,40960
v_mov_b32 v241, 0xff800000
Mvaa 124,24,60
Ta 22,4,41984
v_add_u32 v242, 0x10000, v1
Mvaa 92,24,56
Dc 1,8,81,1024
Mvaa 92,28,128
v_bfe_u32 v244, v1, 4, 4
Mvav 60,28,184
v_lshrrev_b32 v245, 8, v1
Mvaa 60,24,52
W 8
Ta 24,5,32768
v_lshlrev_b32 v245, 2, v245
Mvaa 32,0,140
Ta 26,5,33792
Mvav 32,4,224
Dc 1,8,81,2048
v_sub_u32 v244, v244, v245
Mvav 64,4,232
Ta 28,5,40960
v_cmp_le_i32_e64 s[72:73], 0, v244
Mvaa 64,0,144
Ta 30,5,41984
Mvaa 96,0,148
v_cmp_le_i32_e64 s[74:75], 1, v244
Mvaa 96,4,248
v_cmp_le_i32_e64 s[76:77], 2, v244
Mvaa 128,4,252
Dc 1,8,81,3072
v_cmp_le_i32_e64 s[78:79], 3, v244
Mvaa 128,0,152
W 8
s_add_u32 m0, s83, 4096
s_add_u32 s81, s82, 4096
Mvaa 132,8,152
Ta 0,6,32768
Ta 2,6,33792
Mvaa 132,12,252
v_add_u32 v243, 4096, v1
v_add_u32 v244, 8192, v1
Mvaa 100,12,248
Ta 4,6,40960
v_add_u32 v245, 12288, v1
Mvaa 100,8,148
Dc 1,8,81,0
Ta 6,6,41984
Mvaa 68,8,144
s_mov_b32 s18, 0x3f803f80
Mvav 68,12,232
s_mov_b32 s19, 0x3f003f00
Mvav 36,12,224
Mvaa 36,8,140
W 8
Ta 8,7,32768
v_accvgpr_write_b32 a176, s18
Mvaa 40,16,140
Dc 1,8,81,1024
Ta 10,7,33792
Mvav 40,20,224
v_accvgpr_write_b32 a180, s19
v_mov_b32 v252, v241
Mvav 72,20,232
Ta 12,7,40960
Ta 14,7,41984
Mvaa 72,16,144
v_accvgpr_write_b32 a177, s18
Mvaa 104,16,148
v_accvgpr_write_b32 a181, s19
Mvaa 104,20,248
Dc 1,8,81,2048
v_mov_b32 v253, v241
Mvaa 136,20,252
Mvaa 136,16,152
W 8
Ta 16,8,32768
v_accvgpr_write_b32 a178, s18
Mvaa 140,24,152
Ta 18,8,33792
v_accvgpr_write_b32 a182, s19
Mvaa 140,28,252
Mvaa 108,28,248
Dc 1,8,81,3072
v_mov_b32 v254, v241
Mvaa 108,24,148
s_add_u32 m0, s83, 8192
s_add_u32 s81, s82, 8192
Ta 20,8,40960
Mvaa 76,24,144
Ta 22,8,41984
v_accvgpr_write_b32 a179, s18
Mvav 76,28,232
v_accvgpr_write_b32 a183, s19
Mvav 44,28,224
v_mov_b32 v255, v241
Mvaa 44,24,140
W 8
Dc 1,8,81,0
v_accvgpr_write_b32 a160, 0
Mvaa 48,0,140
Ta 24,9,32768
Ta 26,9,33792
Mvav 48,4,224
v_accvgpr_write_b32 a161, 0
Mvav 80,4,232
Ta 28,9,40960
v_accvgpr_write_b32 a162, 0
Mvaa 80,0,144
Ta 30,9,41984
v_accvgpr_write_b32 a163, 0
Mvaa 112,0,148
Dc 1,8,81,1024
Mvaa 112,4,248
v_accvgpr_write_b32 a164, 0
Mvaa 144,4,252
v_accvgpr_write_b32 a165, 0
Mvaa 144,0,152
W 8
Ta 0,2,49152
v_accvgpr_write_b32 a166, 0
Mvaa 148,8,152
Ta 2,2,50176
Mvaa 148,12,252
Dc 1,8,81,2048
v_accvgpr_write_b32 a167, 0
Mvaa 116,12,248
Ta 4,2,57344
v_accvgpr_write_b32 a168, 0
Mvaa 116,8,148
Ta 6,2,58368
v_accvgpr_write_b32 a169, 0
Mvaa 84,8,144
Mvav 84,12,232
v_accvgpr_write_b32 a170, 0
Mvav 52,12,224
Dc 1,8,81,3072
v_accvgpr_write_b32 a171, 0
Mvaa 52,8,140
W 8
s_add_u32 m0, s83, 12288
s_add_u32 s81, s82, 12288
Mvaa 56,16,140
Ta 8,3,49152
Ta 10,3,50176
)"
R"(Mvav 56,20,224
v_accvgpr_write_b32 a172, 0
v_accvgpr_write_b32 a173, 0
Mvav 88,20,232
Ta 12,3,57344
v_accvgpr_write_b32 a174, 0
Mvaa 88,16,144
Dc 1,8,81,0
Ta 14,3,58368
Mvaa 120,16,148
v_accvgpr_write_b32 a175, 0
Mvaa 120,20,248
s_mov_b32 s90, 0x5d800000
Mvaa 152,20,252
s_mov_b64 s[88:89], 0
Mvaa 152,16,152
W 8
Ta 16,4,49152
Ta 18,4,50176
Mvaa 156,24,152
s_mov_b32 s64, 0
Dc 1,8,81,1024
Mvaa 156,28,252
s_and_b32 s18, s14, 1
Mvaa 124,28,248
s_lshl_b32 s62, s18, 14
Ta 20,4,57344
Ta 22,4,58368
Mvaa 124,24,148
Mvaa 92,24,144
s_lshl_b32 s20, s57, 15
Mvav 92,28,232
s_sub_u32 s20, 507904, s20
Dc 1,8,81,2048
Mvav 60,28,224
Mvaa 60,24,140
s_waitcnt vmcnt(15) lgkmcnt(8)
s_add_u32 s20, s20, s62
Ta 24,5,49152
Mvav 32,0,192
s_lshl_b32 s65, s57, 1
Ta 26,5,50176
Mvav 32,4,204
s_sub_u32 s65, s65, 1
Mvav 64,4,212
Dc 1,8,81,3072
Ta 28,5,57344
Mvav 64,0,200
s_max_i32 s65, s65, 0
Ta 30,5,58368
v_bfe_u32 v28, v1, 4, 4
Mvav 96,0,188
s_sub_u32 s66, s60, 1
v_lshlrev_b32 v28, 3, v28
Mvav 96,4,220
s_lshl_b32 s80, s56, 9
s_add_u32 s80, s80, 4096
s_cmp_lt_u32 s14, 2
s_cselect_b32 s20, s20, 0
s_cselect_b32 s66, s66, 15
s_cselect_b32 s21, 32768, 0
Mvav 128,4,176
Mvav 128,0,196
W 8
s_add_u32 s61, s91, s20
buffer_load_dwordx2 v[28:29], v28, s[8:11], s80 offen sc1
Mvav 132,8,196
s_add_u32 s62, s62, s21
Ta 0,6,49152
Ta 2,6,50176
Mvav 132,12,176
Mvav 100,12,220
s_sub_u32 s63, 16384, s21
Ta 4,6,57344
Ta 6,6,58368
Mvav 100,8,188
s_lshl_b32 s67, s60, 7
Mvav 68,8,200
s_add_u32 s67, s67, s56
Mvav 68,12,212
Mvav 36,12,204
s_lshl_b32 s67, s67, 12
Mvav 36,8,192
W 8
s_add_u32 s67, s67, 39845888
Ta 8,7,49152
Mvav 40,16,192
s_lshl_b32 s69, s60, 3
Ta 10,7,50176
Mvav 40,20,204
Mvav 72,20,212
s_add_u32 s69, s69, s59
Ta 12,7,57344
Ta 14,7,58368
Mvav 72,16,200
s_lshl_b32 s69, s69, 6
Mvav 104,16,188
Mvav 104,20,220
s_lshl_b32 s21, s58, 3
Mvav 136,20,176
s_add_u32 s69, s69, s21
Mvav 136,16,196
W 8
s_add_u32 s69, s69, 20480
Ta 16,8,49152
Mvav 140,24,196
Ta 18,8,50176
Mvav 140,28,176
s_mov_b32 s70, 0
Mvav 108,28,220
s_mov_b32 s37, 0
Ta 20,8,57344
Ta 22,8,58368
Mvav 108,24,188
s_sub_u32 s18, s57, 4
Mvav 76,24,200
Mvav 76,28,212
s_lshl_b32 s18, s18, 2
Mvav 44,28,204
s_add_u32 s18, s18, s58
Mvav 44,24,192
W 8
Ta 24,9,49152
Ta 26,9,50176
Mvav 48,0,192
s_mul_i32 s85, s59, 7
Mvav 48,4,204
s_add_u32 s85, s85, s18
Mvav 80,4,212
s_cmp_lt_u32 s18, 7
s_cselect_b32 s85, s85, 99
Ta 28,9,57344
Ta 30,9,58368
Mvav 80,0,200
Mvav 112,0,188
s_cmp_lt_u32 s85, 54
s_cselect_b32 s65, 2, s65
Mvav 112,4,220
s_mov_b32 s68, 0
Mvav 144,4,176
s_mov_b64 s[24:25], -1
Mvav 144,0,196
W 8
Mvav 148,8,196
s_mov_b32 s22, 0x3e0293ee
Mvav 148,12,176
s_mov_b32 s23, 0x3e0293ee
Mvav 116,12,220
Mvav 116,8,188
Mvav 84,8,200
Mvav 84,12,212
Mvav 52,12,204
Mvav 52,8,192
W 4
Mvav 56,16,192
Mvav 56,20,204
s_waitcnt vmcnt(0)
v_cmp_eq_u64 vcc, s[12:13], v[28:29]
s_cmp_eq_u64 vcc, -1
s_cbranch_scc0 .Lspin_64%=
.Lok_65%=:
Mvav 88,20,212
Mvav 88,16,200
Mvav 120,16,188
W 0
s_barrier
Ra 128,242,0
Mvav 120,20,220
Mvav 152,20,176
Ra 132,242,1024
Mvav 152,16,196
Mvav 156,24,196
Ra 136,242,2048
Mvav 156,28,176
Mvav 124,28,220
Ra 140,242,3072
Mvav 124,24,188
Mvav 92,24,200
Mvav 92,28,212
Mvav 60,28,204
Mvav 60,24,192
.Lbgo%=:
v_pk_mul_f32 v[32:33], v[32:33], s[22:23]
v_pk_mul_f32 v[34:35], v[34:35], s[22:23]
v_pk_mul_f32 v[36:37], v[36:37], s[22:23]
v_pk_mul_f32 v[38:39], v[38:39], s[22:23]
v_pk_mul_f32 v[64:65], v[64:65], s[22:23]
v_pk_mul_f32 v[66:67], v[66:67], s[22:23]
v_pk_mul_f32 v[68:69], v[68:69], s[22:23]
v_pk_mul_f32 v[70:71], v[70:71], s[22:23]
v_pk_mul_f32 v[96:97], v[96:97], s[22:23]
v_pk_mul_f32 v[98:99], v[98:99], s[22:23]
v_pk_mul_f32 v[100:101], v[100:101], s[22:23]
v_pk_mul_f32 v[102:103], v[102:103], s[22:23]
v_pk_mul_f32 v[128:129], v[128:129], s[22:23]
v_pk_mul_f32 v[130:131], v[130:131], s[22:23]
v_pk_mul_f32 v[132:133], v[132:133], s[22:23]
v_pk_mul_f32 v[134:135], v[134:135], s[22:23]
v_pk_mul_f32 v[40:41], v[40:41], s[22:23]
v_pk_mul_f32 v[42:43], v[42:43], s[22:23]
v_pk_mul_f32 v[44:45], v[44:45], s[22:23]
v_pk_mul_f32 v[46:47], v[46:47], s[22:23]
v_pk_mul_f32 v[72:73], v[72:73], s[22:23]
v_pk_mul_f32 v[74:75], v[74:75], s[22:23]
v_pk_mul_f32 v[76:77], v[76:77], s[22:23]
v_pk_mul_f32 v[78:79], v[78:79], s[22:23]
v_pk_mul_f32 v[104:105], v[104:105], s[22:23]
v_pk_mul_f32 v[106:107], v[106:107], s[22:23]
v_pk_mul_f32 v[108:109], v[108:109], s[22:23]
v_pk_mul_f32 v[110:111], v[110:111], s[22:23]
v_pk_mul_f32 v[136:137], v[136:137], s[22:23]
v_pk_mul_f32 v[138:139], v[138:139], s[22:23]
v_pk_mul_f32 v[140:141], v[140:141], s[22:23]
v_pk_mul_f32 v[142:143], v[142:143], s[22:23]
v_pk_mul_f32 v[48:49], v[48:49], s[22:23]
v_pk_mul_f32 v[50:51], v[50:51], s[22:23]
v_pk_mul_f32 v[52:53], v[52:53], s[22:23]
v_pk_mul_f32 v[54:55], v[54:55], s[22:23]
v_pk_mul_f32 v[80:81], v[80:81], s[22:23]
v_pk_mul_f32 v[82:83], v[82:83], s[22:23]
v_pk_mul_f32 v[84:85], v[84:85], s[22:23]
v_pk_mul_f32 v[86:87], v[86:87], s[22:23]
v_pk_mul_f32 v[112:113], v[112:113], s[22:23]
v_pk_mul_f32 v[114:115], v[114:115], s[22:23]
v_pk_mul_f32 v[116:117], v[116:117], s[22:23]
v_pk_mul_f32 v[118:119], v[118:119], s[22:23]
v_pk_mul_f32 v[144:145], v[144:145], s[22:23]
v_pk_mul_f32 v[146:147], v[146:147], s[22:23]
v_pk_mul_f32 v[148:149], v[148:149], s[22:23]
v_pk_mul_f32 v[150:151], v[150:151], s[22:23]
v_pk_mul_f32 v[56:57], v[56:57], s[22:23]
v_pk_mul_f32 v[58:59], v[58:59], s[22:23]
v_pk_mul_f32 v[60:61], v[60:61], s[22:23]
v_pk_mul_f32 v[62:63], v[62:63], s[22:23]
v_pk_mul_f32 v[88:89], v[88:89], s[22:23]
v_pk_mul_f32 v[90:91], v[90:91], s[22:23]
v_pk_mul_f32 v[92:93], v[92:93], s[22:23]
v_pk_mul_f32 v[94:95], v[94:95], s[22:23]
v_pk_mul_f32 v[120:121], v[120:121], s[22:23]
v_pk_mul_f32 v[122:123], v[122:123], s[22:23]
v_pk_mul_f32 v[124:125], v[124:125], s[22:23]
v_pk_mul_f32 v[126:127], v[126:127], s[22:23]
v_pk_mul_f32 v[152:153], v[152:153], s[22:23]
v_pk_mul_f32 v[154:155], v[154:155], s[22:23]
v_pk_mul_f32 v[156:157], v[156:157], s[22:23]
v_pk_mul_f32 v[158:159], v[158:159], s[22:23]
K 32,32,33
K 33,34,35
K 34,36,37
K 35,38,39
K 40,40,41
K 41,42,43
K 42,44,45
K 43,46,47
W 0
Zvav 196,128,32
s_add_u32 m0, s62, 0
Ra 144,242,4096
Ra 148,242,5120
Ra 152,242,6144
Ra 156,242,7168
Mvav 196,132,40
K 64,64,65
K 65,66,67
K 66,68,69
K 67,70,71
K 72,72,73
K 73,74,75
K 74,76,77
K 75,78,79
s_nop 1
Zvav 204,132,72
Dc 1,8,61,0
K 96,96,97
K 97,98,99
K 98,100,101
Mvav 204,128,64
K 99,102,103
K 104,104,105
K 105,106,107
K 106,108,109
K 107,110,111
Zvav 212,128,96
Dc 1,8,61,1024
K 128,128,129
K 129,130,131
K 130,132,133
Mvav 212,132,104
K 131,134,135
K 136,136,137
K 137,138,139
K 138,140,141
K 139,142,143
s_nop 1
Zvav 220,132,136
Dc 1,8,61,2048
K 48,48,49
K 49,50,51
K 50,52,53
Mvav 220,128,128
K 51,54,55
K 56,56,57
K 57,58,59
K 58,60,61
K 59,62,63
Mvav 196,136,48
Ra 128,242,8192
Ra 132,242,9216
K 80,80,81
K 81,82,83
Mvav 196,140,56
K 82,84,85
K 83,86,87
K 88,88,89
K 89,90,91
K 90,92,93
K 91,94,95
s_nop 1
Mvav 204,140,88
Dc 1,8,61,3072
s_add_u32 m0, s62, 4096
K 112,112,113
K 113,114,115
K 114,116,117
Mvav 204,136,80
K 115,118,119
K 120,120,121
K 121,122,123
K 122,124,125
K 123,126,127
Mvav 212,136,112
Dc 243,8,61,0
K 144,144,145
K 145,146,147
K 146,148,149
Mvav 212,140,120
K 147,150,151
K 152,152,153
K 153,154,155
K 154,156,157
K 155,158,159
s_nop 1
Mvav 220,140,152
s_add_u32 s80, s62, 131072
Dc 243,8,61,1024
v_add_u32 v12, s80, v1
Mvav 220,136,144
W 2
ds_write_b128 v12, v[32:35] offset:0
Ra 136,242,10240
Zvav 36,144,32
Ra 0,12,0
Ra 140,242,11264
Dc 243,8,61,2048
Mvav 36,148,40
v_max3_f32 v4, v196, v197, v198
v_max3_f32 v4, v4, v199, v252
v_max3_f32 v5, v204, v205, v206
v_max3_f32 v5, v5, v207, v253
Zvav 68,148,72
v_max3_f32 v6, v212, v213, v214
v_max3_f32 v6, v6, v215, v254
v_max3_f32 v7, v220, v221, v222
v_max3_f32 v7, v7, v223, v255
Mvav 68,144,64
v_mov_b32 v8, v4
v_mov_b32 v9, v5
v_mov_b32 v10, v6
v_mov_b32 v11, v7
Zvav 100,144,96
ds_write_b128 v12, v[40:43] offset:1024
v_permlane32_swap_b32 v4, v8
v_permlane32_swap_b32 v5, v9
v_permlane32_swap_b32 v6, v10
Mvav 100,148,104
v_permlane32_swap_b32 v7, v11
v_max_f32 v4, v4, v8
v_max_f32 v5, v5, v9
v_max_f32 v6, v6, v10
Zvav 132,148,136
v_max_f32 v7, v7, v11
v_mov_b32 v8, v4
v_mov_b32 v9, v5
v_mov_b32 v10, v6
Mvav 132,144,128
Ra 4,12,1024
v_mov_b32 v11, v7
v_permlane16_swap_b32 v4, v8
v_permlane16_swap_b32 v5, v9
Mvav 36,152,48
v_permlane16_swap_b32 v6, v10
v_permlane16_swap_b32 v7, v11
v_max_f32 v4, v4, v8
v_max_f32 v5, v5, v9
Mvav 36,156,56
v_max_f32 v6, v6, v10
v_max_f32 v7, v7, v11
v_sub_f32 v4, 0, v4
v_sub_f32 v5, 0, v5
Mvav 68,156,88
)"
R"(v_sub_f32 v6, 0, v6
v_sub_f32 v7, 0, v7
v_mov_b32 v224, v4
v_mov_b32 v225, v4
Mvav 68,152,80
v_mov_b32 v226, v4
v_mov_b32 v227, v4
v_mov_b32 v228, v5
v_mov_b32 v229, v5
Mvav 100,152,112
v_mov_b32 v230, v5
v_mov_b32 v231, v5
v_mov_b32 v232, v6
v_mov_b32 v233, v6
Mvav 100,156,120
v_mov_b32 v234, v6
v_mov_b32 v235, v6
v_mov_b32 v236, v7
v_mov_b32 v237, v7
Mvav 132,156,152
Dc 243,8,61,3072
s_add_u32 m0, s62, 8192
ds_write_b128 v12, v[48:51] offset:2048
v_mov_b32 v238, v7
v_mov_b32 v239, v7
Mvav 132,152,144
W 3
Ra 8,12,2048
ds_write_b128 v12, v[56:59] offset:3072
Ra 144,242,12288
Ra 12,12,3072
Cvav 160,128,32,224
s_and_b32 s80, s14, 1
Dc 244,8,61,0
Ra 148,242,13312
ds_write_b128 v12, v[64:67] offset:4096
Ra 16,12,4096
Mvav 160,132,40
s_lshl_b32 s80, s80, 14
ds_write_b128 v12, v[72:75] offset:5120
Ra 20,12,5120
ds_write_b128 v12, v[80:83] offset:6144
A 196,196,224
Cvav 164,132,72,228
s_add_u32 s80, s80, 98304
Dc 244,8,61,1024
Ra 24,12,6144
A 197,197,224
A 198,198,224
Mvav 164,128,64
Ra 152,242,14336
A 199,199,224
X 196
Cvav 168,128,96,232
Dc 244,8,61,2048
Ra 156,242,15360
X 197
Mvav 168,132,104
ds_write_b128 v12, v[88:91] offset:7168
Ra 28,12,7168
X 198
Cvav 172,132,136,236
Dc 244,8,61,3072
s_add_u32 m0, s62, 12288
K 192,196,197
X 199
Mvav 172,128,128
ds_write_b128 v12, v[96:99] offset:8192
A 204,204,228
v_add_u32 v8, s80, v1
K 193,198,199
Mvav 160,136,48
Dc 245,8,61,0
Ra 32,12,8192
A 205,205,228
A 206,206,228
Mvav 160,140,56
ds_write_b128 v8, v[4:7]
A 207,207,228
X 204
Mvav 164,140,88
Dc 245,8,61,1024
ds_write_b128 v12, v[104:107] offset:9216
X 205
Mvav 164,136,80
Ra 36,12,9216
X 206
K 200,204,205
Mvav 168,136,112
Dc 245,8,61,2048
Ra 128,242,24576
X 207
Mvav 168,140,120
ds_write_b128 v12, v[112:115] offset:10240
Ra 132,242,25600
A 212,212,232
K 201,206,207
Mvav 172,140,152
Dc 245,8,61,3072
s_add_u32 s61, s61, s63
Ra 40,12,10240
A 213,213,232
A 214,214,232
Mvav 172,136,144
W 11
ds_write_b128 v12, v[120:123] offset:11264
A 215,215,232
X 212
Cvav 176,144,32,224
X 213
X 214
Mvav 176,148,40
Ra 44,12,11264
K 208,212,213
X 215
Cvav 180,148,72,228
ds_write_b128 v12, v[128:131] offset:12288
A 220,220,236
K 209,214,215
A 221,221,236
Mvav 180,144,64
Ra 48,12,12288
Ra 136,242,26624
A 222,222,236
A 223,223,236
Cvav 184,144,96,232
ds_write_b128 v12, v[136:139] offset:13312
Ra 140,242,27648
X 220
Mvav 184,148,104
X 221
X 222
Cvav 188,148,136,236
Ra 52,12,13312
K 216,220,221
X 223
Mvav 188,144,128
ds_write_b128 v12, v[144:147] offset:14336
Ra 56,12,14336
A 36,36,224
K 217,222,223
Mvav 176,152,48
ds_write_b128 v12, v[152:155] offset:15360
A 37,37,224
A 38,38,224
A 39,39,224
Mvav 176,156,56
X 36
X 37
Mvav 180,156,88
Ra 60,12,15360
X 38
K 194,36,37
Mvav 180,152,80
Ra 144,242,28672
X 39
A 68,68,228
Mvav 184,152,112
Ra 148,242,29696
K 195,38,39
A 69,69,228
A 70,70,228
Mvav 184,156,120
W 3
s_barrier
A 71,71,228
X 68
Mvav 188,156,152
X 69
X 70
K 202,68,69
X 71
A 100,100,232
K 203,70,71
A 101,101,232
A 102,102,232
A 103,103,232
X 100
X 101
X 102
K 210,100,101
X 103
A 132,132,236
K 211,102,103
A 133,133,236
A 134,134,236
A 135,135,236
X 132
X 133
X 134
K 218,132,133
X 135
s_nop 0
K 219,134,135
Mvav 188,152,144
Ra 152,242,30720
Zvav 32,128,192
Ra 156,242,31744
Zvav 64,128,200
Zvav 96,128,208
s_xor_b32 s62, s62, 0x10000
Zvav 128,128,216
v_add_u32 v23, 61440, v21
Zvav 132,132,216
v_add_u32 v24, 61440, v22
Zvav 100,132,208
v_add_u32 v25, 122880, v21
Zvav 68,132,200
v_add_u32 v26, 122880, v22
Zvav 36,132,192
Zvav 40,136,192
X 172
Zvav 72,136,200
X 173
Zvav 104,136,208
X 174
K 220,172,173
Zvav 136,136,216
X 175
Zvav 140,140,216
X 168
K 221,174,175
Zvav 108,140,208
X 169
X 170
Zvav 76,140,200
s_waitcnt vmcnt(0) lgkmcnt(0)
s_barrier
v_xor_b32 v242, 0x10000, v242
K 212,168,169
Zvav 44,140,192
X 171
X 164
Zvav 48,144,192
Ra 128,242,0
K 213,170,171
X 165
Zvav 80,144,200
X 166
Zvav 112,144,208
Ra 132,242,1024
K 204,164,165
X 167
Zvav 144,144,216
X 160
Zvav 148,148,216
K 205,166,167
X 161
Zvav 116,148,208
Ra 136,242,2048
X 162
Zvav 84,148,200
K 196,160,161
X 163
Zvav 52,148,192
X 176
K 197,162,163
Zvav 56,152,192
Ra 140,242,3072
X 177
Zvav 88,152,200
X 178
K 198,176,177
Zvav 120,152,208
X 179
X 180
Zvav 152,152,216
K 199,178,179
Zvav 156,156,216
X 181
X 182
Zvav 124,156,208
K 206,180,181
X 183
Zvav 92,156,200
K 207,182,183
Zvav 60,156,192
s_cmp_eq_u64 s[24:25], 0
s_cbranch_scc1 .Ll1_34%=
W 0
Cvaa 160,128,0,224
Ra 144,242,4096
Ra 148,242,5120
Mvaa 160,132,4
s_add_u32 m0, s62, 0
Ra 152,242,6144
Cvaa 164,132,20,228
X 184
Mvaa 164,128,16
X 185
Cvaa 168,128,32,232
X 186
Mvaa 168,132,36
Ra 156,242,7168
K 214,184,185
Cvaa 172,132,52,236
Dc 1,8,61,0
Mvaa 172,128,48
X 187
Mvaa 160,136,8
X 188
Mvaa 160,140,12
Ra 128,242,16384
K 215,186,187
Mvaa 164,140,28
Dc 1,8,61,1024
Mvaa 164,136,24
X 189
Mvaa 168,136,40
X 190
Mvaa 168,140,44
Ra 132,242,17408
K 222,188,189
Mvaa 172,140,60
Dc 1,8,61,2048
Mvaa 172,136,56
X 191
v_smfmac_f32_16x16x64_bf16 a[160:163], a[176:179], v[192:199], v240
Ra 136,242,18432
K 223,190,191
v_smfmac_f32_16x16x64_bf16 a[164:167], a[176:179], v[200:207], v240
X 160
v_smfmac_f32_16x16x64_bf16 a[168:171], a[176:179], v[208:215], v240
Dc 1,8,61,3072
v_smfmac_f32_16x16x64_bf16 a[172:175], a[176:179], v[216:223], v240
W 3
X 161
Cvaa 176,144,0,224
s_add_u32 m0, s62, 4096
X 162
Mvaa 176,148,4
Ra 140,242,19456
K 192,160,161
Cvaa 180,148,20,228
X 163
Mvaa 180,144,16
Dc 243,8,61,0
Cvaa 184,144,32,232
X 164
Mvaa 184,148,36
K 193,162,163
Cvaa 188,148,52,236
X 165
Mvaa 188,144,48
Dc 243,8,61,1024
Ra 144,242,20480
Mvaa 176,152,8
X 166
Mvaa 176,156,12
Ra 148,242,21504
K 200,164,165
Mvaa 180,156,28
X 167
Mvaa 180,152,24
Dc 243,8,61,2048
Mvaa 184,152,40
X 168
Mvaa 184,156,44
K 201,166,167
Mvaa 188,156,60
X 169
Mvaa 188,152,56
W 2
Dc 243,8,61,3072
Ra 152,242,22528
Mvav 32,128,196
s_add_u32 m0, s62, 8192
X 170
Mvav 64,128,204
Ra 156,242,23552
K 208,168,169
Mvav 96,128,212
X 171
Mvav 128,128,220
Dc 244,8,61,0
Mvav 132,132,220
X 172
Mvav 100,132,212
Ra 128,242,8192
K 209,170,171
Mvav 68,132,204
X 173
Mvav 36,132,196
Dc 244,8,61,1024
Mvav 40,136,196
X 174
Mvav 72,136,204
Ra 132,242,9216
K 216,172,173
Mvav 104,136,212
X 175
Mvav 136,136,220
Dc 244,8,61,2048
Mvav 140,140,220
X 176
Mvav 108,140,212
Ra 136,242,10240
K 217,174,175
Mvav 76,140,204
X 177
)"
R"(Mvav 44,140,196
W 3
Dc 244,8,61,3072
Mvav 48,144,196
X 178
Mvav 80,144,204
s_add_u32 m0, s62, 12288
Ra 140,242,11264
Mvav 112,144,212
K 194,176,177
Mvav 144,144,220
X 179
Mvav 148,148,220
X 180
Mvav 116,148,212
Dc 245,8,61,0
Ra 144,242,12288
Mvav 84,148,204
K 195,178,179
Mvav 52,148,196
X 181
Mvav 56,152,196
X 182
Mvav 88,152,204
s_cmp_eq_u32 s14, 2
s_cselect_b64 s[18:19], s[30:31], s[32:33]
Dc 245,8,61,1024
Ra 148,242,13312
Mvav 120,152,212
s_mul_i32 s81, s58, 6
K 202,180,181
Mvav 152,152,220
s_add_u32 s81, s81, s57
X 183
Mvav 156,156,220
s_sub_u32 s81, s81, 2
X 184
Mvav 124,156,212
s_cmp_ge_u32 s57, 2
s_cselect_b32 s23, 2211836, 0
Dc 245,8,61,2048
Ra 152,242,14336
Mvav 92,156,204
s_mul_i32 s82, s81, 48384
K 203,182,183
Mvav 60,156,196
W 3
X 185
Cvaa 160,128,0,224
s_mul_i32 s83, s59, 448
X 186
Mvaa 160,132,4
s_andn2_b32 s83, s83, 127
Dc 245,8,61,3072
Ra 156,242,15360
Cvaa 164,132,20,228
s_add_u32 s82, s82, s83
s_add_u32 s61, s61, s63
K 210,184,185
Mvaa 164,128,16
v_min_u32 v246, 880, v1
v_lshrrev_b32 v247, 6, v246
Cvaa 168,128,32,232
X 187
Mvaa 168,132,36
X 188
Cvaa 172,132,52,236
s_xor_b32 s62, s62, 0x10000
v_mul_u32_u24 v247, 3456, v247
v_and_b32 v246, 48, v246
Mvaa 172,128,48
K 211,186,187
v_lshl_add_u32 v246, v246, 3, v247
Mvaa 160,136,8
X 189
Mvaa 160,140,12
s_add_u32 s64, s64, 1
Ra 128,242,24576
v_add_u32 v246, s82, v246
Mvaa 164,140,28
X 190
Mvaa 164,136,24
Ra 132,242,25600
v_min_u32 v246, s23, v246
Mvaa 168,136,40
global_load_dword v3, v246, s[18:19]
K 218,188,189
Mvaa 168,140,44
X 191
Mvaa 172,140,60
X 160
Mvaa 172,136,56
W 2
Ra 136,242,26624
K 219,190,191
Cvaa 176,144,0,224
X 161
Mvaa 176,148,4
Ra 140,242,27648
Cvaa 180,148,20,228
X 162
Mvaa 180,144,16
K 196,160,161
Cvaa 184,144,32,232
X 163
Mvaa 184,148,36
X 164
Cvaa 188,148,52,236
K 197,162,163
Mvaa 188,144,48
X 165
Mvaa 176,152,8
Ra 144,242,28672
Ra 148,242,29696
Mvaa 176,156,12
X 166
Mvaa 180,156,28
K 204,164,165
Mvaa 180,152,24
X 167
Mvaa 184,152,40
Mvaa 184,156,44
X 168
Mvaa 188,156,60
Mvaa 188,152,56
W 2
Ra 152,242,30720
K 205,166,167
Mvav 32,128,192
X 169
Mvav 64,128,200
Ra 156,242,31744
Mvav 96,128,208
X 170
Mvav 128,128,216
K 212,168,169
Mvav 132,132,216
X 171
Mvav 100,132,208
X 172
Mvav 68,132,200
Mvav 36,132,192
K 213,170,171
Mvav 40,136,192
X 173
Mvav 72,136,200
X 174
Mvav 104,136,208
Mvav 136,136,216
K 220,172,173
Mvav 140,140,216
X 175
Mvav 108,140,208
X 176
Mvav 76,140,200
s_waitcnt vmcnt(1) lgkmcnt(0)
s_barrier
v_xor_b32 v242, 0x10000, v242
Mvav 44,140,192
Ra 128,242,0
K 221,174,175
Mvav 48,144,192
X 177
Mvav 80,144,200
X 178
Mvav 112,144,208
Ra 132,242,1024
Mvav 144,144,216
K 198,176,177
Mvav 148,148,216
X 179
Mvav 116,148,208
X 180
Mvav 84,148,200
Ra 136,242,2048
Mvav 52,148,192
K 199,178,179
Mvav 56,152,192
X 181
Mvav 88,152,200
Ra 140,242,3072
Mvav 120,152,208
X 182
Mvav 152,152,216
K 206,180,181
Mvav 156,156,216
X 183
Mvav 124,156,208
Mvav 92,156,200
K 207,182,183
Mvav 60,156,192
W 0
Cvaa 160,128,0,224
Ra 144,242,4096
Ra 148,242,5120
Mvaa 160,132,4
s_add_u32 m0, s62, 0
Ra 152,242,6144
Cvaa 164,132,20,228
X 184
Mvaa 164,128,16
X 185
Cvaa 168,128,32,232
X 186
Mvaa 168,132,36
Ra 156,242,7168
K 214,184,185
Cvaa 172,132,52,236
Dc 1,8,61,0
Mvaa 172,128,48
X 187
Mvaa 160,136,8
X 188
Mvaa 160,140,12
Ra 128,242,16384
K 215,186,187
Mvaa 164,140,28
Dc 1,8,61,1024
Mvaa 164,136,24
X 189
Mvaa 168,136,40
X 190
Mvaa 168,140,44
Ra 132,242,17408
K 222,188,189
Mvaa 172,140,60
Dc 1,8,61,2048
Mvaa 172,136,56
X 191
v_smfmac_f32_16x16x64_bf16 a[160:163], a[176:179], v[192:199], v240
Ra 136,242,18432
K 223,190,191
v_smfmac_f32_16x16x64_bf16 a[164:167], a[176:179], v[200:207], v240
X 160
v_smfmac_f32_16x16x64_bf16 a[168:171], a[176:179], v[208:215], v240
Dc 1,8,61,3072
v_smfmac_f32_16x16x64_bf16 a[172:175], a[176:179], v[216:223], v240
W 3
X 161
Cvaa 176,144,0,224
s_add_u32 m0, s62, 4096
X 162
Mvaa 176,148,4
Ra 140,242,19456
K 192,160,161
Cvaa 180,148,20,228
X 163
Mvaa 180,144,16
Dc 243,8,61,0
Cvaa 184,144,32,232
X 164
Mvaa 184,148,36
K 193,162,163
Cvaa 188,148,52,236
X 165
Mvaa 188,144,48
Dc 243,8,61,1024
Ra 144,242,20480
Mvaa 176,152,8
X 166
Mvaa 176,156,12
Ra 148,242,21504
K 200,164,165
Mvaa 180,156,28
X 167
Mvaa 180,152,24
Dc 243,8,61,2048
Mvaa 184,152,40
X 168
Mvaa 184,156,44
K 201,166,167
Mvaa 188,156,60
X 169
Mvaa 188,152,56
W 2
Dc 243,8,61,3072
Ra 152,242,22528
Mvav 32,128,196
s_add_u32 m0, s62, 8192
X 170
Mvav 64,128,204
Ra 156,242,23552
K 208,168,169
Mvav 96,128,212
X 171
Mvav 128,128,220
Dc 244,8,61,0
Mvav 132,132,220
X 172
Mvav 100,132,212
Ra 128,242,8192
K 209,170,171
Mvav 68,132,204
X 173
Mvav 36,132,196
Dc 244,8,61,1024
Mvav 40,136,196
X 174
Mvav 72,136,204
Ra 132,242,9216
K 216,172,173
Mvav 104,136,212
X 175
Mvav 136,136,220
Dc 244,8,61,2048
Mvav 140,140,220
X 176
Mvav 108,140,212
Ra 136,242,10240
K 217,174,175
Mvav 76,140,204
X 177
Mvav 44,140,196
W 3
Dc 244,8,61,3072
Mvav 48,144,196
X 178
Mvav 80,144,204
s_add_u32 m0, s62, 12288
Ra 140,242,11264
Mvav 112,144,212
K 194,176,177
Mvav 144,144,220
X 179
Mvav 148,148,220
X 180
Mvav 116,148,212
Dc 245,8,61,0
Ra 144,242,12288
Mvav 84,148,204
K 195,178,179
Mvav 52,148,196
X 181
Mvav 56,152,196
X 182
Mvav 88,152,204
Dc 245,8,61,1024
Ra 148,242,13312
Mvav 120,152,212
v_add_u32 v246, 1161216, v246
v_min_u32 v246, s23, v246
Mvav 152,152,220
K 202,180,181
Mvav 156,156,220
X 183
Mvav 124,156,212
Dc 245,8,61,2048
Ra 152,242,14336
Mvav 92,156,204
X 184
Mvav 60,156,196
W 3
K 203,182,183
v_bfe_u32 v28, v1, 4, 4
Cvaa 160,128,0,224
X 185
Mvaa 160,132,4
Dc 245,8,61,3072
v_lshlrev_b32 v28, 3, v28
Cvaa 164,132,20,228
s_lshl_b32 s80, s56, 9
X 186
Mvaa 164,128,16
s_add_u32 s80, s80, 4224
Ra 156,242,15360
K 210,184,185
Cvaa 168,128,32,232
s_add_u32 s61, s61, s63
X 187
Mvaa 168,132,36
buffer_load_dwordx2 v[28:29], v28, s[8:11], s80 offen sc1
Cvaa 172,132,52,236
s_xor_b32 s62, s62, 0x10000
X 188
Mvaa 172,128,48
Ra 128,242,24576
K 211,186,187
Mvaa 160,136,8
X 189
Mvaa 160,140,12
s_add_u32 s64, s64, 1
global_load_dword v3, v246, s[18:19]
Ra 132,242,25600
Mvaa 164,140,28
s_mov_b64 s[18:19], s[34:35]
s_mul_i32 s81, s58, 6
s_add_u32 s81, s81, s57
Mvaa 164,136,24
s_sub_u32 s81, s81, 2
s_cmp_ge_u32 s57, 2
s_cselect_b32 s23, 2211836, 0
s_lshl_b32 s81, s81, 1
Mvaa 168,136,40
s_add_u32 s81, s81, s14
s_sub_u32 s81, s81, 2
s_mul_i32 s81, s81, 6016
Mvaa 168,140,44
s_mul_i32 s82, s59, 286720
s_add_u32 s81, s81, s82
v_min_u32 v246, 736, v1
)"
R"(Mvaa 172,140,60
v_lshl_add_u32 v246, v246, 3, s81
v_min_u32 v246, s23, v246
Mvaa 172,136,56
W 2
global_load_dword v3, v246, s[18:19]
Ra 136,242,26624
Cvaa 176,144,0,224
X 190
Mvaa 176,148,4
Ra 140,242,27648
K 218,188,189
Cvaa 180,148,20,228
X 191
Mvaa 180,144,16
X 160
Cvaa 184,144,32,232
K 219,190,191
Mvaa 184,148,36
X 161
Cvaa 188,148,52,236
X 162
Mvaa 188,144,48
Ra 144,242,28672
K 196,160,161
Mvaa 176,152,8
X 163
Mvaa 176,156,12
X 164
Mvaa 180,156,28
Ra 148,242,29696
K 197,162,163
Mvaa 180,152,24
X 165
Mvaa 184,152,40
X 166
Mvaa 184,156,44
K 204,164,165
Mvaa 188,156,60
X 167
Mvaa 188,152,56
W 2
X 168
Mvav 32,128,192
Ra 152,242,30720
K 205,166,167
Mvav 64,128,200
X 169
Mvav 96,128,208
Ra 156,242,31744
Mvav 128,128,216
X 170
Mvav 132,132,216
K 212,168,169
Mvav 100,132,208
X 171
Mvav 68,132,200
X 172
Mvav 36,132,192
K 213,170,171
Mvav 40,136,192
X 173
Mvav 72,136,200
X 174
Mvav 104,136,208
Mvav 136,136,216
s_waitcnt vmcnt(2)
v_cmp_eq_u64 vcc, s[12:13], v[28:29]
s_cmp_eq_u64 vcc, -1
s_cbranch_scc0 .Lspin_67%=
.Lok_68%=:
K 220,172,173
Mvav 140,140,216
X 175
Mvav 108,140,208
X 176
Mvav 76,140,200
W 0
s_barrier
v_xor_b32 v242, 0x10000, v242
Mvav 44,140,192
Ra 128,242,0
K 221,174,175
Mvav 48,144,192
X 177
Mvav 80,144,200
X 178
Mvav 112,144,208
Ra 132,242,1024
Mvav 144,144,216
K 198,176,177
Mvav 148,148,216
X 179
Mvav 116,148,208
X 180
Mvav 84,148,200
Ra 136,242,2048
Mvav 52,148,192
K 199,178,179
Mvav 56,152,192
X 181
Mvav 88,152,200
Ra 140,242,3072
Mvav 120,152,208
X 182
Mvav 152,152,216
K 206,180,181
Mvav 156,156,216
X 183
Mvav 124,156,208
Mvav 92,156,200
K 207,182,183
Mvav 60,156,192
W 0
Cvaa 160,128,0,224
Ra 144,242,4096
Ra 148,242,5120
Mvaa 160,132,4
s_add_u32 m0, s62, 0
Ra 152,242,6144
Cvaa 164,132,20,228
X 184
Mvaa 164,128,16
X 185
Cvaa 168,128,32,232
X 186
Mvaa 168,132,36
Ra 156,242,7168
K 214,184,185
Cvaa 172,132,52,236
Dc 1,8,61,0
Mvaa 172,128,48
X 187
Mvaa 160,136,8
X 188
Mvaa 160,140,12
Ra 128,242,16384
K 215,186,187
Mvaa 164,140,28
Dc 1,8,61,1024
Mvaa 164,136,24
X 189
Mvaa 168,136,40
X 190
Mvaa 168,140,44
Ra 132,242,17408
K 222,188,189
Mvaa 172,140,60
Dc 1,8,61,2048
Mvaa 172,136,56
X 191
v_smfmac_f32_16x16x64_bf16 a[160:163], a[176:179], v[192:199], v240
Ra 136,242,18432
K 223,190,191
v_smfmac_f32_16x16x64_bf16 a[164:167], a[176:179], v[200:207], v240
X 160
v_smfmac_f32_16x16x64_bf16 a[168:171], a[176:179], v[208:215], v240
Dc 1,8,61,3072
v_smfmac_f32_16x16x64_bf16 a[172:175], a[176:179], v[216:223], v240
W 3
X 161
Cvaa 176,144,0,224
s_add_u32 m0, s62, 4096
X 162
Mvaa 176,148,4
Ra 140,242,19456
K 192,160,161
Cvaa 180,148,20,228
X 163
Mvaa 180,144,16
Dc 243,8,61,0
Cvaa 184,144,32,232
X 164
Mvaa 184,148,36
K 193,162,163
Cvaa 188,148,52,236
X 165
Mvaa 188,144,48
Dc 243,8,61,1024
Ra 144,242,20480
Mvaa 176,152,8
X 166
Mvaa 176,156,12
Ra 148,242,21504
K 200,164,165
Mvaa 180,156,28
X 167
Mvaa 180,152,24
Dc 243,8,61,2048
Mvaa 184,152,40
X 168
Mvaa 184,156,44
K 201,166,167
Mvaa 188,156,60
X 169
Mvaa 188,152,56
W 2
Dc 243,8,61,3072
Ra 152,242,22528
Mvav 32,128,196
s_add_u32 m0, s62, 8192
X 170
Mvav 64,128,204
Ra 156,242,23552
K 208,168,169
Mvav 96,128,212
X 171
Mvav 128,128,220
Dc 244,8,61,0
Mvav 132,132,220
X 172
Mvav 100,132,212
Ra 128,242,8192
K 209,170,171
Mvav 68,132,204
X 173
Mvav 36,132,196
Dc 244,8,61,1024
Mvav 40,136,196
X 174
Mvav 72,136,204
Ra 132,242,9216
K 216,172,173
Mvav 104,136,212
X 175
Mvav 136,136,220
Dc 244,8,61,2048
Mvav 140,140,220
X 176
Mvav 108,140,212
Ra 136,242,10240
K 217,174,175
Mvav 76,140,204
X 177
Mvav 44,140,196
W 3
Dc 244,8,61,3072
Mvav 48,144,196
X 178
Mvav 80,144,204
s_add_u32 m0, s62, 12288
Ra 140,242,11264
Mvav 112,144,212
K 194,176,177
Mvav 144,144,220
X 179
Mvav 148,148,220
X 180
Mvav 116,148,212
Dc 245,8,61,0
Ra 144,242,12288
Mvav 84,148,204
K 195,178,179
Mvav 52,148,196
X 181
Mvav 56,152,196
X 182
Mvav 88,152,204
Dc 245,8,61,1024
Ra 148,242,13312
Mvav 120,152,212
K 202,180,181
Mvav 152,152,220
X 183
Mvav 156,156,220
X 184
Mvav 124,156,212
Dc 245,8,61,2048
Ra 152,242,14336
Mvav 92,156,204
K 203,182,183
Mvav 60,156,196
W 3
X 185
Cvaa 160,128,0,224
X 186
Mvaa 160,132,4
Dc 245,8,61,3072
Ra 156,242,15360
Cvaa 164,132,20,228
s_add_u32 s61, s61, s63
K 210,184,185
Mvaa 164,128,16
X 187
Cvaa 168,128,32,232
X 188
Mvaa 168,132,36
Cvaa 172,132,52,236
s_xor_b32 s62, s62, 0x10000
K 211,186,187
Mvaa 172,128,48
X 189
Mvaa 160,136,8
Ra 128,242,24576
Ra 132,242,25600
Mvaa 160,140,12
s_add_u32 s64, s64, 1
X 190
Mvaa 164,140,28
K 218,188,189
Mvaa 164,136,24
X 191
Mvaa 168,136,40
Mvaa 168,140,44
X 160
Mvaa 172,140,60
s_add_u32 s36, s29, 131072
K 219,190,191
Mvaa 172,136,56
W 2
X 161
Cvaa 176,144,0,224
Ra 136,242,26624
Ra 140,242,27648
Mvaa 176,148,4
X 162
Cvaa 180,148,20,228
K 196,160,161
Mvaa 180,144,16
buffer_store_dwordx4 a[124:127], v1, s[8:11], s36 offen offset:0 sc1
Cvaa 184,144,32,232
X 163
Mvaa 184,148,36
X 164
Cvaa 188,148,52,236
K 197,162,163
Mvaa 188,144,48
X 165
Mvaa 176,152,8
Ra 144,242,28672
buffer_store_dwordx4 a[116:119], v1, s[8:11], s36 offen offset:1024 sc1
Mvaa 176,156,12
Ra 148,242,29696
Mvaa 180,156,28
X 166
Mvaa 180,152,24
K 204,164,165
Mvaa 184,152,40
X 167
Mvaa 184,156,44
buffer_store_dwordx4 a[108:111], v1, s[8:11], s36 offen offset:2048 sc1
Mvaa 188,156,60
X 168
Mvaa 188,152,56
W 2
Ra 152,242,30720
K 205,166,167
Mvav 32,128,192
X 169
Mvav 64,128,200
Ra 156,242,31744
buffer_store_dwordx4 a[204:207], v1, s[8:11], s36 offen offset:3072 sc1
Mvav 96,128,208
X 170
Mvav 128,128,216
K 212,168,169
Mvav 132,132,216
X 171
Mvav 100,132,208
s_add_u32 s36, s29, 135168
X 172
Mvav 68,132,200
Mvav 36,132,192
K 213,170,171
Mvav 40,136,192
X 173
Mvav 72,136,200
X 174
Mvav 104,136,208
buffer_store_dwordx4 a[212:215], v1, s[8:11], s36 offen offset:0 sc1
Mvav 136,136,216
K 220,172,173
Mvav 140,140,216
X 175
Mvav 108,140,208
X 176
Mvav 76,140,200
s_waitcnt vmcnt(5) lgkmcnt(0)
s_barrier
v_xor_b32 v242, 0x10000, v242
Mvav 44,140,192
buffer_store_dwordx4 a[220:223], v1, s[8:11], s36 offen offset:1024 sc1
K 221,174,175
Mvav 48,144,192
Ra 128,242,0
Mvav 80,144,200
X 177
Mvav 112,144,208
X 178
Mvav 144,144,216
Ra 132,242,1024
K 198,176,177
Mvav 148,148,216
buffer_store_dwordx4 a[228:231], v1, s[8:11], s36 offen offset:2048 sc1
Mvav 116,148,208
X 179
Mvav 84,148,200
X 180
Mvav 52,148,192
Ra 136,242,2048
)"
R"(K 199,178,179
Mvav 56,152,192
buffer_store_dwordx4 a[76:79], v1, s[8:11], s36 offen offset:3072 sc1
Ra 140,242,3072
Mvav 88,152,200
X 181
Mvav 120,152,208
X 182
Mvav 152,152,216
K 206,180,181
Mvav 156,156,216
X 183
Mvav 124,156,208
Mvav 92,156,200
K 207,182,183
Mvav 60,156,192
W 0
Cvaa 160,128,0,224
Ra 144,242,4096
Ra 148,242,5120
Mvaa 160,132,4
s_add_u32 m0, s62, 0
Ra 152,242,6144
Cvaa 164,132,20,228
X 184
Mvaa 164,128,16
X 185
Cvaa 168,128,32,232
X 186
Mvaa 168,132,36
Ra 156,242,7168
K 214,184,185
Cvaa 172,132,52,236
Dc 1,8,61,0
Mvaa 172,128,48
X 187
Mvaa 160,136,8
X 188
Mvaa 160,140,12
Ra 128,242,16384
K 215,186,187
Mvaa 164,140,28
Dc 1,8,61,1024
Mvaa 164,136,24
X 189
Mvaa 168,136,40
X 190
Mvaa 168,140,44
Ra 132,242,17408
K 222,188,189
Mvaa 172,140,60
Dc 1,8,61,2048
Mvaa 172,136,56
X 191
v_smfmac_f32_16x16x64_bf16 a[160:163], a[176:179], v[192:199], v240
Ra 136,242,18432
K 223,190,191
v_smfmac_f32_16x16x64_bf16 a[164:167], a[176:179], v[200:207], v240
X 160
v_smfmac_f32_16x16x64_bf16 a[168:171], a[176:179], v[208:215], v240
Dc 1,8,61,3072
v_smfmac_f32_16x16x64_bf16 a[172:175], a[176:179], v[216:223], v240
W 3
X 161
Cvaa 176,144,0,224
s_add_u32 m0, s62, 4096
X 162
Mvaa 176,148,4
Ra 140,242,19456
K 192,160,161
Cvaa 180,148,20,228
X 163
Mvaa 180,144,16
Dc 243,8,61,0
Cvaa 184,144,32,232
X 164
Mvaa 184,148,36
K 193,162,163
Cvaa 188,148,52,236
X 165
Mvaa 188,144,48
Dc 243,8,61,1024
Ra 144,242,20480
Mvaa 176,152,8
X 166
Mvaa 176,156,12
Ra 148,242,21504
K 200,164,165
Mvaa 180,156,28
X 167
Mvaa 180,152,24
Dc 243,8,61,2048
Mvaa 184,152,40
X 168
Mvaa 184,156,44
K 201,166,167
Mvaa 188,156,60
X 169
Mvaa 188,152,56
W 2
Dc 243,8,61,3072
Ra 152,242,22528
Mvav 32,128,196
s_add_u32 m0, s62, 8192
X 170
Mvav 64,128,204
Ra 156,242,23552
K 208,168,169
Mvav 96,128,212
X 171
Mvav 128,128,220
Dc 244,8,61,0
Mvav 132,132,220
X 172
Mvav 100,132,212
Ra 128,242,8192
K 209,170,171
Mvav 68,132,204
X 173
Mvav 36,132,196
Dc 244,8,61,1024
Mvav 40,136,196
X 174
Mvav 72,136,204
Ra 132,242,9216
K 216,172,173
Mvav 104,136,212
X 175
Mvav 136,136,220
Dc 244,8,61,2048
Mvav 140,140,220
X 176
Mvav 108,140,212
Ra 136,242,10240
K 217,174,175
Mvav 76,140,204
X 177
Mvav 44,140,196
W 3
Dc 244,8,61,3072
Mvav 48,144,196
X 178
Mvav 80,144,204
s_add_u32 m0, s62, 12288
Ra 140,242,11264
Mvav 112,144,212
K 194,176,177
Mvav 144,144,220
X 179
Mvav 148,148,220
X 180
Mvav 116,148,212
Dc 245,8,61,0
Ra 144,242,12288
Mvav 84,148,204
K 195,178,179
Mvav 52,148,196
X 181
Mvav 56,152,196
X 182
Mvav 88,152,204
Dc 245,8,61,1024
Ra 148,242,13312
Mvav 120,152,212
K 202,180,181
Mvav 152,152,220
X 183
Mvav 156,156,220
X 184
Mvav 124,156,212
Dc 245,8,61,2048
Ra 152,242,14336
Mvav 92,156,204
K 203,182,183
Mvav 60,156,196
W 3
X 185
Cvaa 160,128,0,224
X 186
Mvaa 160,132,4
Dc 245,8,61,3072
Ra 156,242,15360
Cvaa 164,132,20,228
s_add_u32 s61, s61, s63
K 210,184,185
Mvaa 164,128,16
X 187
Cvaa 168,128,32,232
X 188
Mvaa 168,132,36
Cvaa 172,132,52,236
s_xor_b32 s62, s62, 0x10000
K 211,186,187
Mvaa 172,128,48
X 189
Mvaa 160,136,8
Ra 128,242,24576
Ra 132,242,25600
Mvaa 160,140,12
s_add_u32 s64, s64, 1
X 190
Mvaa 164,140,28
K 218,188,189
Mvaa 164,136,24
X 191
Mvaa 168,136,40
Mvaa 168,140,44
X 160
Mvaa 172,140,60
s_add_u32 s36, s29, 327680
K 219,190,191
Mvaa 172,136,56
W 2
X 161
Cvaa 176,144,0,224
Ra 136,242,26624
Ra 140,242,27648
Mvaa 176,148,4
X 162
Cvaa 180,148,20,228
K 196,160,161
Mvaa 180,144,16
buffer_store_dwordx4 a[84:87], v1, s[8:11], s36 offen offset:0 sc1
Cvaa 184,144,32,232
X 163
Mvaa 184,148,36
X 164
Cvaa 188,148,52,236
K 197,162,163
Mvaa 188,144,48
X 165
Mvaa 176,152,8
Ra 144,242,28672
buffer_store_dwordx4 a[92:95], v1, s[8:11], s36 offen offset:1024 sc1
Mvaa 176,156,12
Ra 148,242,29696
Mvaa 180,156,28
X 166
Mvaa 180,152,24
K 204,164,165
Mvaa 184,152,40
X 167
Mvaa 184,156,44
buffer_store_dwordx4 a[188:191], v1, s[8:11], s36 offen offset:2048 sc1
Mvaa 188,156,60
X 168
Mvaa 188,152,56
W 2
Ra 152,242,30720
K 205,166,167
Mvav 32,128,192
X 169
Mvav 64,128,200
Ra 156,242,31744
buffer_store_dwordx4 a[196:199], v1, s[8:11], s36 offen offset:3072 sc1
Mvav 96,128,208
X 170
Mvav 128,128,216
K 212,168,169
Mvav 132,132,216
X 171
Mvav 100,132,208
s_add_u32 s36, s29, 331776
X 172
Mvav 68,132,200
Mvav 36,132,192
K 213,170,171
Mvav 40,136,192
X 173
Mvav 72,136,200
X 174
Mvav 104,136,208
buffer_store_dwordx4 a[236:239], v1, s[8:11], s36 offen offset:0 sc1
Mvav 136,136,216
K 220,172,173
Mvav 140,140,216
X 175
Mvav 108,140,208
X 176
Mvav 76,140,200
s_waitcnt vmcnt(5) lgkmcnt(0)
s_barrier
v_xor_b32 v242, 0x10000, v242
Mvav 44,140,192
buffer_store_dwordx4 a[244:247], v1, s[8:11], s36 offen offset:1024 sc1
K 221,174,175
Mvav 48,144,192
Ra 128,242,0
Mvav 80,144,200
X 177
Mvav 112,144,208
X 178
Mvav 144,144,216
Ra 132,242,1024
K 198,176,177
Mvav 148,148,216
buffer_store_dwordx4 a[68:71], v1, s[8:11], s36 offen offset:2048 sc1
Mvav 116,148,208
X 179
Mvav 84,148,200
X 180
Mvav 52,148,192
Ra 136,242,2048
K 199,178,179
Mvav 56,152,192
buffer_store_dwordx4 a[184:187], v1, s[8:11], s36 offen offset:3072 sc1
Ra 140,242,3072
Mvav 88,152,200
X 181
Mvav 120,152,208
X 182
Mvav 152,152,216
K 206,180,181
Mvav 156,156,216
X 183
Mvav 124,156,208
Mvav 92,156,200
K 207,182,183
Mvav 60,156,192
W 0
Cvaa 160,128,0,224
Ra 144,242,4096
Ra 148,242,5120
Mvaa 160,132,4
s_add_u32 m0, s62, 0
Ra 152,242,6144
Cvaa 164,132,20,228
X 184
Mvaa 164,128,16
X 185
Cvaa 168,128,32,232
X 186
Mvaa 168,132,36
Ra 156,242,7168
K 214,184,185
Cvaa 172,132,52,236
Dc 1,8,61,0
Mvaa 172,128,48
X 187
Mvaa 160,136,8
X 188
Mvaa 160,140,12
Ra 128,242,16384
K 215,186,187
Mvaa 164,140,28
Dc 1,8,61,1024
Mvaa 164,136,24
X 189
Mvaa 168,136,40
X 190
Mvaa 168,140,44
Ra 132,242,17408
K 222,188,189
Mvaa 172,140,60
Dc 1,8,61,2048
Mvaa 172,136,56
X 191
v_smfmac_f32_16x16x64_bf16 a[160:163], a[176:179], v[192:199], v240
Ra 136,242,18432
K 223,190,191
v_smfmac_f32_16x16x64_bf16 a[164:167], a[176:179], v[200:207], v240
X 160
v_smfmac_f32_16x16x64_bf16 a[168:171], a[176:179], v[208:215], v240
Dc 1,8,61,3072
v_smfmac_f32_16x16x64_bf16 a[172:175], a[176:179], v[216:223], v240
W 3
X 161
Cvaa 176,144,0,224
s_add_u32 m0, s62, 4096
X 162
Mvaa 176,148,4
Ra 140,242,19456
K 192,160,161
Cvaa 180,148,20,228
X 163
Mvaa 180,144,16
Dc 243,8,61,0
Cvaa 184,144,32,232
X 164
Mvaa 184,148,36
K 193,162,163
Cvaa 188,148,52,236
X 165
Mvaa 188,144,48
Dc 243,8,61,1024
Ra 144,242,20480
Mvaa 176,152,8
X 166
Mvaa 176,156,12
Ra 148,242,21504
K 200,164,165
Mvaa 180,156,28
X 167
Mvaa 180,152,24
Dc 243,8,61,2048
Mvaa 184,152,40
X 168
Mvaa 184,156,44
K 201,166,167
Mvaa 188,156,60
)"
R"(X 169
Mvaa 188,152,56
W 2
Dc 243,8,61,3072
Ra 152,242,22528
Mvav 32,128,196
s_add_u32 m0, s62, 8192
X 170
Mvav 64,128,204
Ra 156,242,23552
K 208,168,169
Mvav 96,128,212
X 171
Mvav 128,128,220
Dc 244,8,61,0
Mvav 132,132,220
X 172
Mvav 100,132,212
Ra 128,242,8192
K 209,170,171
Mvav 68,132,204
X 173
Mvav 36,132,196
Dc 244,8,61,1024
Mvav 40,136,196
X 174
Mvav 72,136,204
Ra 132,242,9216
K 216,172,173
Mvav 104,136,212
X 175
Mvav 136,136,220
Dc 244,8,61,2048
Mvav 140,140,220
X 176
Mvav 108,140,212
Ra 136,242,10240
K 217,174,175
Mvav 76,140,204
X 177
Mvav 44,140,196
W 3
Dc 244,8,61,3072
Mvav 48,144,196
X 178
Mvav 80,144,204
s_add_u32 m0, s62, 12288
Ra 140,242,11264
Mvav 112,144,212
K 194,176,177
Mvav 144,144,220
X 179
Mvav 148,148,220
X 180
Mvav 116,148,212
Dc 245,8,61,0
Ra 144,242,12288
Mvav 84,148,204
K 195,178,179
Mvav 52,148,196
X 181
Mvav 56,152,196
X 182
Mvav 88,152,204
Dc 245,8,61,1024
Ra 148,242,13312
Mvav 120,152,212
K 202,180,181
Mvav 152,152,220
X 183
Mvav 156,156,220
s_waitcnt vmcnt(14)
s_add_u32 s80, s71, 256
v_mov_b64 v[28:29], s[12:13]
s_mov_b64 exec, 1
buffer_store_dwordx2 v[28:29], off, s[8:11], s80 sc1
s_mov_b64 exec, -1
X 184
Mvav 124,156,212
Ra 152,242,14336
Mvav 92,156,204
K 203,182,183
Mvav 60,156,196
W 3
X 185
Cvaa 160,128,0,224
Dc 245,8,61,2048
Ra 156,242,15360
Mvaa 160,132,4
X 186
Cvaa 164,132,20,228
K 210,184,185
Mvaa 164,128,16
X 187
Cvaa 168,128,32,232
Dc 245,8,61,3072
Mvaa 168,132,36
s_add_u32 s61, s61, s63
X 188
Cvaa 172,132,52,236
s_xor_b32 s62, s62, 0x10000
K 211,186,187
Mvaa 172,128,48
X 189
Mvaa 160,136,8
Ra 128,242,24576
Ra 132,242,25600
Mvaa 160,140,12
s_add_u32 s64, s64, 1
X 190
Mvaa 164,140,28
K 218,188,189
Mvaa 164,136,24
X 191
Mvaa 168,136,40
Mvaa 168,140,44
X 160
Mvaa 172,140,60
s_add_u32 s36, s29, 196608
K 219,190,191
Mvaa 172,136,56
W 2
X 161
Cvaa 176,144,0,224
Ra 136,242,26624
Ra 140,242,27648
Mvaa 176,148,4
X 162
Cvaa 180,148,20,228
K 196,160,161
Mvaa 180,144,16
buffer_store_dwordx4 a[192:195], v1, s[8:11], s36 offen offset:0 sc1
Cvaa 184,144,32,232
X 163
Mvaa 184,148,36
X 164
Cvaa 188,148,52,236
K 197,162,163
Mvaa 188,144,48
X 165
Mvaa 176,152,8
Ra 144,242,28672
buffer_store_dwordx4 a[200:203], v1, s[8:11], s36 offen offset:1024 sc1
Mvaa 176,156,12
Ra 148,242,29696
Mvaa 180,156,28
X 166
Mvaa 180,152,24
K 204,164,165
Mvaa 184,152,40
X 167
Mvaa 184,156,44
buffer_store_dwordx4 a[208:211], v1, s[8:11], s36 offen offset:2048 sc1
Mvaa 188,156,60
X 168
Mvaa 188,152,56
W 2
Ra 152,242,30720
K 205,166,167
Mvav 32,128,192
X 169
Mvav 64,128,200
Ra 156,242,31744
buffer_store_dwordx4 a[216:219], v1, s[8:11], s36 offen offset:3072 sc1
Mvav 96,128,208
X 170
Mvav 128,128,216
K 212,168,169
Mvav 132,132,216
X 171
Mvav 100,132,208
s_add_u32 s36, s29, 200704
X 172
Mvav 68,132,200
Mvav 36,132,192
K 213,170,171
Mvav 40,136,192
X 173
Mvav 72,136,200
X 174
Mvav 104,136,208
buffer_store_dwordx4 a[224:227], v1, s[8:11], s36 offen offset:0 sc1
Mvav 136,136,216
K 220,172,173
Mvav 140,140,216
X 175
Mvav 108,140,208
X 176
Mvav 76,140,200
s_waitcnt vmcnt(5) lgkmcnt(0)
s_barrier
v_xor_b32 v242, 0x10000, v242
Mvav 44,140,192
buffer_store_dwordx4 a[232:235], v1, s[8:11], s36 offen offset:1024 sc1
K 221,174,175
Mvav 48,144,192
Ra 128,242,0
Mvav 80,144,200
X 177
Mvav 112,144,208
X 178
Mvav 144,144,216
Ra 132,242,1024
K 198,176,177
Mvav 148,148,216
buffer_store_dwordx4 a[240:243], v1, s[8:11], s36 offen offset:2048 sc1
Mvav 116,148,208
X 179
Mvav 84,148,200
X 180
Mvav 52,148,192
Ra 136,242,2048
K 199,178,179
Mvav 56,152,192
buffer_store_dwordx4 a[100:103], v1, s[8:11], s36 offen offset:3072 sc1
Ra 140,242,3072
Mvav 88,152,200
X 181
Mvav 120,152,208
X 182
Mvav 152,152,216
K 206,180,181
Mvav 156,156,216
X 183
Mvav 124,156,208
Mvav 92,156,200
K 207,182,183
Mvav 60,156,192
W 0
Cvaa 160,128,0,224
Ra 144,242,4096
Ra 148,242,5120
Mvaa 160,132,4
s_add_u32 m0, s62, 0
Ra 152,242,6144
Cvaa 164,132,20,228
X 184
Mvaa 164,128,16
X 185
Cvaa 168,128,32,232
X 186
Mvaa 168,132,36
Ra 156,242,7168
K 214,184,185
Cvaa 172,132,52,236
Dc 1,8,61,0
Mvaa 172,128,48
X 187
Mvaa 160,136,8
X 188
Mvaa 160,140,12
Ra 128,242,16384
K 215,186,187
Mvaa 164,140,28
Dc 1,8,61,1024
Mvaa 164,136,24
X 189
Mvaa 168,136,40
X 190
Mvaa 168,140,44
Ra 132,242,17408
K 222,188,189
Mvaa 172,140,60
Dc 1,8,61,2048
Mvaa 172,136,56
X 191
v_smfmac_f32_16x16x64_bf16 a[160:163], a[176:179], v[192:199], v240
Ra 136,242,18432
K 223,190,191
v_smfmac_f32_16x16x64_bf16 a[164:167], a[176:179], v[200:207], v240
X 160
v_smfmac_f32_16x16x64_bf16 a[168:171], a[176:179], v[208:215], v240
Dc 1,8,61,3072
v_smfmac_f32_16x16x64_bf16 a[172:175], a[176:179], v[216:223], v240
W 3
X 161
Cvaa 176,144,0,224
s_add_u32 m0, s62, 4096
X 162
Mvaa 176,148,4
Ra 140,242,19456
K 192,160,161
Cvaa 180,148,20,228
X 163
Mvaa 180,144,16
Dc 243,8,61,0
Cvaa 184,144,32,232
X 164
Mvaa 184,148,36
K 193,162,163
Cvaa 188,148,52,236
X 165
Mvaa 188,144,48
Dc 243,8,61,1024
Ra 144,242,20480
Mvaa 176,152,8
X 166
Mvaa 176,156,12
Ra 148,242,21504
K 200,164,165
Mvaa 180,156,28
X 167
Mvaa 180,152,24
Dc 243,8,61,2048
Mvaa 184,152,40
X 168
Mvaa 184,156,44
K 201,166,167
Mvaa 188,156,60
X 169
Mvaa 188,152,56
W 2
Dc 243,8,61,3072
Ra 152,242,22528
Mvav 32,128,196
s_add_u32 m0, s62, 8192
X 170
Mvav 64,128,204
Ra 156,242,23552
K 208,168,169
Mvav 96,128,212
X 171
Mvav 128,128,220
Dc 244,8,61,0
Mvav 132,132,220
X 172
Mvav 100,132,212
Ra 128,242,8192
K 209,170,171
Mvav 68,132,204
X 173
Mvav 36,132,196
Dc 244,8,61,1024
Mvav 40,136,196
X 174
Mvav 72,136,204
Ra 132,242,9216
K 216,172,173
Mvav 104,136,212
X 175
Mvav 136,136,220
Dc 244,8,61,2048
Mvav 140,140,220
X 176
Mvav 108,140,212
Ra 136,242,10240
K 217,174,175
Mvav 76,140,204
X 177
Mvav 44,140,196
W 3
Dc 244,8,61,3072
Mvav 48,144,196
X 178
Mvav 80,144,204
s_add_u32 m0, s62, 12288
Ra 140,242,11264
Mvav 112,144,212
K 194,176,177
Mvav 144,144,220
X 179
Mvav 148,148,220
X 180
Mvav 116,148,212
Dc 245,8,61,0
Ra 144,242,12288
Mvav 84,148,204
K 195,178,179
Mvav 52,148,196
X 181
Mvav 56,152,196
X 182
Mvav 88,152,204
Dc 245,8,61,1024
Ra 148,242,13312
Mvav 120,152,212
K 202,180,181
Mvav 152,152,220
X 183
Mvav 156,156,220
X 184
Mvav 124,156,212
Dc 245,8,61,2048
Ra 152,242,14336
Mvav 92,156,204
K 203,182,183
v_bfe_u32 v28, v1, 4, 4
Mvav 60,156,196
W 3
X 185
Cvaa 160,128,0,224
Ra 156,242,15360
v_lshlrev_b32 v28, 3, v28
Mvaa 160,132,4
s_lshl_b32 s80, s56, 9
s_add_u32 s80, s80, 4352
Dc 245,8,61,3072
Cvaa 164,132,20,228
s_add_u32 s61, s61, s63
X 186
)"
R"(Mvaa 164,128,16
K 210,184,185
Cvaa 168,128,32,232
X 187
Mvaa 168,132,36
buffer_load_dwordx2 v[28:29], v28, s[8:11], s80 offen sc1
Cvaa 172,132,52,236
s_xor_b32 s62, s62, 0x10000
X 188
Mvaa 172,128,48
Ra 128,242,24576
K 211,186,187
Mvaa 160,136,8
X 189
Mvaa 160,140,12
s_add_u32 s64, s64, 1
X 190
Mvaa 164,140,28
Ra 132,242,25600
K 218,188,189
Mvaa 164,136,24
X 191
Mvaa 168,136,40
Mvaa 168,140,44
X 160
Mvaa 172,140,60
s_add_u32 s36, s29, 262144
K 219,190,191
Mvaa 172,136,56
W 2
X 161
Cvaa 176,144,0,224
Ra 136,242,26624
Ra 140,242,27648
Mvaa 176,148,4
X 162
Cvaa 180,148,20,228
K 196,160,161
Mvaa 180,144,16
buffer_store_dwordx4 a[64:67], v1, s[8:11], s36 offen offset:0 sc1
Cvaa 184,144,32,232
X 163
Mvaa 184,148,36
X 164
Cvaa 188,148,52,236
K 197,162,163
Mvaa 188,144,48
X 165
Mvaa 176,152,8
Ra 144,242,28672
buffer_store_dwordx4 a[72:75], v1, s[8:11], s36 offen offset:1024 sc1
Mvaa 176,156,12
Ra 148,242,29696
Mvaa 180,156,28
X 166
Mvaa 180,152,24
K 204,164,165
Mvaa 184,152,40
X 167
Mvaa 184,156,44
buffer_store_dwordx4 a[80:83], v1, s[8:11], s36 offen offset:2048 sc1
Mvaa 188,156,60
X 168
Mvaa 188,152,56
W 2
Ra 152,242,30720
K 205,166,167
Mvav 32,128,192
X 169
Mvav 64,128,200
Ra 156,242,31744
buffer_store_dwordx4 a[88:91], v1, s[8:11], s36 offen offset:3072 sc1
Mvav 96,128,208
X 170
Mvav 128,128,216
K 212,168,169
Mvav 132,132,216
X 171
Mvav 100,132,208
s_add_u32 s36, s29, 266240
X 172
Mvav 68,132,200
Mvav 36,132,192
K 213,170,171
Mvav 40,136,192
X 173
Mvav 72,136,200
X 174
Mvav 104,136,208
buffer_store_dwordx4 a[96:99], v1, s[8:11], s36 offen offset:0 sc1
Mvav 136,136,216
s_waitcnt vmcnt(5)
v_cmp_eq_u64 vcc, s[12:13], v[28:29]
s_cmp_eq_u64 vcc, -1
s_cbranch_scc0 .Lspin_70%=
.Lok_71%=:
K 220,172,173
Mvav 140,140,216
X 175
Mvav 108,140,208
X 176
Mvav 76,140,200
W 0
s_barrier
v_xor_b32 v242, 0x10000, v242
Mvav 44,140,192
buffer_store_dwordx4 a[104:107], v1, s[8:11], s36 offen offset:1024 sc1
K 221,174,175
Mvav 48,144,192
Ra 128,242,0
Mvav 80,144,200
X 177
Mvav 112,144,208
X 178
Mvav 144,144,216
Ra 132,242,1024
K 198,176,177
Mvav 148,148,216
buffer_store_dwordx4 a[112:115], v1, s[8:11], s36 offen offset:2048 sc1
Mvav 116,148,208
X 179
Mvav 84,148,200
X 180
Mvav 52,148,192
Ra 136,242,2048
K 199,178,179
Mvav 56,152,192
buffer_store_dwordx4 a[120:123], v1, s[8:11], s36 offen offset:3072 sc1
Ra 140,242,3072
Mvav 88,152,200
X 181
Mvav 120,152,208
X 182
Mvav 152,152,216
K 206,180,181
Mvav 156,156,216
X 183
Mvav 124,156,208
Mvav 92,156,200
K 207,182,183
Mvav 60,156,192
W 0
Cvaa 160,128,0,224
Ra 144,242,4096
Ra 148,242,5120
Mvaa 160,132,4
s_add_u32 m0, s62, 0
Ra 152,242,6144
Cvaa 164,132,20,228
X 184
Mvaa 164,128,16
X 185
Cvaa 168,128,32,232
X 186
Mvaa 168,132,36
Ra 156,242,7168
K 214,184,185
Cvaa 172,132,52,236
Dc 1,8,61,0
Mvaa 172,128,48
X 187
Mvaa 160,136,8
X 188
Mvaa 160,140,12
Ra 128,242,16384
K 215,186,187
Mvaa 164,140,28
Dc 1,8,61,1024
Mvaa 164,136,24
X 189
Mvaa 168,136,40
X 190
Mvaa 168,140,44
Ra 132,242,17408
K 222,188,189
Mvaa 172,140,60
Dc 1,8,61,2048
Mvaa 172,136,56
X 191
v_smfmac_f32_16x16x64_bf16 a[160:163], a[176:179], v[192:199], v240
Ra 136,242,18432
K 223,190,191
v_smfmac_f32_16x16x64_bf16 a[164:167], a[176:179], v[200:207], v240
X 160
v_smfmac_f32_16x16x64_bf16 a[168:171], a[176:179], v[208:215], v240
Dc 1,8,61,3072
v_smfmac_f32_16x16x64_bf16 a[172:175], a[176:179], v[216:223], v240
W 3
X 161
Cvaa 176,144,0,224
s_add_u32 m0, s62, 4096
X 162
Mvaa 176,148,4
Ra 140,242,19456
K 192,160,161
Cvaa 180,148,20,228
X 163
Mvaa 180,144,16
Dc 243,8,61,0
Cvaa 184,144,32,232
X 164
Mvaa 184,148,36
K 193,162,163
Cvaa 188,148,52,236
X 165
Mvaa 188,144,48
Dc 243,8,61,1024
Ra 144,242,20480
Mvaa 176,152,8
X 166
Mvaa 176,156,12
Ra 148,242,21504
K 200,164,165
Mvaa 180,156,28
X 167
Mvaa 180,152,24
Dc 243,8,61,2048
Mvaa 184,152,40
X 168
Mvaa 184,156,44
K 201,166,167
Mvaa 188,156,60
X 169
Mvaa 188,152,56
W 2
Dc 243,8,61,3072
Ra 152,242,22528
Mvav 32,128,196
s_add_u32 m0, s62, 8192
X 170
Mvav 64,128,204
Ra 156,242,23552
K 208,168,169
Mvav 96,128,212
X 171
Mvav 128,128,220
Dc 244,8,61,0
Mvav 132,132,220
X 172
Mvav 100,132,212
Ra 128,242,8192
K 209,170,171
Mvav 68,132,204
X 173
Mvav 36,132,196
Dc 244,8,61,1024
Mvav 40,136,196
X 174
Mvav 72,136,204
Ra 132,242,9216
K 216,172,173
Mvav 104,136,212
X 175
Mvav 136,136,220
Dc 244,8,61,2048
Mvav 140,140,220
X 176
Mvav 108,140,212
Ra 136,242,10240
K 217,174,175
Mvav 76,140,204
X 177
Mvav 44,140,196
W 3
Dc 244,8,61,3072
Mvav 48,144,196
X 178
Mvav 80,144,204
s_add_u32 m0, s62, 12288
Ra 140,242,11264
Mvav 112,144,212
K 194,176,177
Mvav 144,144,220
X 179
Mvav 148,148,220
X 180
Mvav 116,148,212
Dc 245,8,61,0
Ra 144,242,12288
Mvav 84,148,204
K 195,178,179
Mvav 52,148,196
X 181
Mvav 56,152,196
X 182
Mvav 88,152,204
Dc 245,8,61,1024
Ra 148,242,13312
Mvav 120,152,212
K 202,180,181
Mvav 152,152,220
s_add_u32 s81, s57, 2
X 183
Mvav 156,156,220
s_waitcnt vmcnt(14)
s_add_u32 s80, s71, 384
v_mov_b64 v[28:29], s[12:13]
s_mov_b64 exec, 1
buffer_store_dwordx2 v[28:29], off, s[8:11], s80 sc1
s_mov_b64 exec, -1
X 184
Mvav 124,156,212
s_bitcmp0_b32 s81, 2
s_cselect_b32 s22, -1, 0
s_and_b32 s81, s81, 3
Ra 152,242,14336
Mvav 92,156,204
s_lshl_b32 s82, s56, 2
K 203,182,183
Mvav 60,156,196
W 3
X 185
Cvaa 160,128,0,224
s_add_u32 s81, s81, s82
Dc 245,8,61,2048
Ra 156,242,15360
Mvaa 160,132,4
s_lshl_b32 s81, s81, 1
X 186
Cvaa 164,132,20,228
s_add_u32 s81, s81, s14
s_sub_u32 s81, s81, 2
K 210,184,185
Mvaa 164,128,16
s_mul_i32 s81, s81, 20480
X 187
Cvaa 168,128,32,232
Dc 245,8,61,3072
v_min_u32 v2, 624, v1
Mvaa 168,132,36
s_add_u32 s61, s61, s63
X 188
Cvaa 172,132,52,236
s_xor_b32 s62, s62, 0x10000
v_lshl_add_u32 v2, v2, 3, s81
K 211,186,187
Mvaa 172,128,48
Ra 128,242,24576
v_and_b32 v2, s22, v2
Mvaa 160,136,8
s_and_b32 s22, s22, 5120
X 189
Mvaa 160,140,12
s_add_u32 s64, s64, 1
global_load_dword v3, v2, s[26:27]
Ra 132,242,25600
Mvaa 164,140,28
X 190
Mvaa 164,136,24
v_add_u32 v2, s22, v2
K 218,188,189
Mvaa 168,136,40
X 191
Mvaa 168,140,44
X 160
Mvaa 172,140,60
K 219,190,191
Mvaa 172,136,56
W 2
X 161
Cvaa 176,144,0,224
Ra 136,242,26624
Ra 140,242,27648
Mvaa 176,148,4
X 162
Cvaa 180,148,20,228
K 196,160,161
Mvaa 180,144,16
X 163
Cvaa 184,144,32,232
Mvaa 184,148,36
X 164
Cvaa 188,148,52,236
K 197,162,163
Mvaa 188,144,48
X 165
Mvaa 176,152,8
Ra 144,242,28672
Ra 148,242,29696
Mvaa 176,156,12
X 166
Mvaa 180,156,28
K 204,164,165
Mvaa 180,152,24
X 167
Mvaa 184,152,40
Mvaa 184,156,44
X 168
Mvaa 188,156,60
Mvaa 188,152,56
W 2
Ra 152,242,30720
K 205,166,167
Mvav 32,128,192
X 169
Mvav 64,128,200
Ra 156,242,31744
Mvav 96,128,208
X 170
Mvav 128,128,216
)"
R"(K 212,168,169
Mvav 132,132,216
X 171
Mvav 100,132,208
X 172
Mvav 68,132,200
Mvav 36,132,192
K 213,170,171
Mvav 40,136,192
X 173
Mvav 72,136,200
X 174
Mvav 104,136,208
Mvav 136,136,216
K 220,172,173
Mvav 140,140,216
X 175
Mvav 108,140,208
X 176
Mvav 76,140,200
s_waitcnt vmcnt(1) lgkmcnt(0)
s_barrier
v_xor_b32 v242, 0x10000, v242
Mvav 44,140,192
Ra 128,242,0
K 221,174,175
Mvav 48,144,192
X 177
Mvav 80,144,200
X 178
Mvav 112,144,208
Ra 132,242,1024
Mvav 144,144,216
K 198,176,177
Mvav 148,148,216
X 179
Mvav 116,148,208
X 180
Mvav 84,148,200
Ra 136,242,2048
Mvav 52,148,192
K 199,178,179
Mvav 56,152,192
X 181
Mvav 88,152,200
Ra 140,242,3072
Mvav 120,152,208
X 182
Mvav 152,152,216
K 206,180,181
Mvav 156,156,216
X 183
Mvav 124,156,208
Mvav 92,156,200
K 207,182,183
Mvav 60,156,192
W 0
Cvaa 160,128,0,224
Ra 144,242,4096
Ra 148,242,5120
Mvaa 160,132,4
s_add_u32 m0, s62, 0
Ra 152,242,6144
Cvaa 164,132,20,228
X 184
Mvaa 164,128,16
X 185
Cvaa 168,128,32,232
X 186
Mvaa 168,132,36
Ra 156,242,7168
K 214,184,185
Cvaa 172,132,52,236
Dc 1,8,61,0
Mvaa 172,128,48
X 187
Mvaa 160,136,8
X 188
Mvaa 160,140,12
Ra 128,242,16384
K 215,186,187
Mvaa 164,140,28
Dc 1,8,61,1024
Mvaa 164,136,24
X 189
Mvaa 168,136,40
X 190
Mvaa 168,140,44
Ra 132,242,17408
K 222,188,189
Mvaa 172,140,60
Dc 1,8,61,2048
Mvaa 172,136,56
X 191
v_smfmac_f32_16x16x64_bf16 a[160:163], a[176:179], v[192:199], v240
Ra 136,242,18432
K 223,190,191
v_smfmac_f32_16x16x64_bf16 a[164:167], a[176:179], v[200:207], v240
X 160
v_smfmac_f32_16x16x64_bf16 a[168:171], a[176:179], v[208:215], v240
Dc 1,8,61,3072
v_smfmac_f32_16x16x64_bf16 a[172:175], a[176:179], v[216:223], v240
W 3
X 161
Cvaa 176,144,0,224
s_add_u32 m0, s62, 4096
X 162
Mvaa 176,148,4
Ra 140,242,19456
K 192,160,161
Cvaa 180,148,20,228
X 163
Mvaa 180,144,16
Dc 243,8,61,0
Cvaa 184,144,32,232
X 164
Mvaa 184,148,36
K 193,162,163
Cvaa 188,148,52,236
X 165
Mvaa 188,144,48
Dc 243,8,61,1024
Ra 144,242,20480
Mvaa 176,152,8
X 166
Mvaa 176,156,12
Ra 148,242,21504
K 200,164,165
Mvaa 180,156,28
X 167
Mvaa 180,152,24
Dc 243,8,61,2048
Mvaa 184,152,40
X 168
Mvaa 184,156,44
K 201,166,167
Mvaa 188,156,60
X 169
Mvaa 188,152,56
W 2
Dc 243,8,61,3072
Ra 152,242,22528
Mvav 32,128,196
s_add_u32 m0, s62, 8192
X 170
Mvav 64,128,204
Ra 156,242,23552
K 208,168,169
Mvav 96,128,212
X 171
Mvav 128,128,220
Dc 244,8,61,0
Mvav 132,132,220
X 172
Mvav 100,132,212
Ra 128,242,8192
K 209,170,171
Mvav 68,132,204
X 173
Mvav 36,132,196
Dc 244,8,61,1024
Mvav 40,136,196
X 174
Mvav 72,136,204
Ra 132,242,9216
K 216,172,173
Mvav 104,136,212
X 175
Mvav 136,136,220
Dc 244,8,61,2048
Mvav 140,140,220
X 176
Mvav 108,140,212
Ra 136,242,10240
K 217,174,175
Mvav 76,140,204
X 177
Mvav 44,140,196
W 3
Dc 244,8,61,3072
Mvav 48,144,196
X 178
Mvav 80,144,204
s_add_u32 m0, s62, 12288
Ra 140,242,11264
Mvav 112,144,212
K 194,176,177
Mvav 144,144,220
X 179
Mvav 148,148,220
X 180
Mvav 116,148,212
Dc 245,8,61,0
Ra 144,242,12288
Mvav 84,148,204
K 195,178,179
Mvav 52,148,196
X 181
Mvav 56,152,196
X 182
Mvav 88,152,204
Dc 245,8,61,1024
Ra 148,242,13312
Mvav 120,152,212
K 202,180,181
Mvav 152,152,220
X 183
Mvav 156,156,220
X 184
Mvav 124,156,212
Dc 245,8,61,2048
Ra 152,242,14336
Mvav 92,156,204
K 203,182,183
Mvav 60,156,196
W 3
X 185
Cvaa 160,128,0,224
X 186
Mvaa 160,132,4
Dc 245,8,61,3072
Ra 156,242,15360
Cvaa 164,132,20,228
s_add_u32 s61, s61, s63
K 210,184,185
Mvaa 164,128,16
X 187
Cvaa 168,128,32,232
X 188
Mvaa 168,132,36
Cvaa 172,132,52,236
s_xor_b32 s62, s62, 0x10000
global_load_dword v3, v2, s[26:27]
K 211,186,187
Mvaa 172,128,48
X 189
Mvaa 160,136,8
Ra 128,242,24576
Ra 132,242,25600
Mvaa 160,140,12
s_add_u32 s64, s64, 1
X 190
Mvaa 164,140,28
v_add_u32 v2, s22, v2
K 218,188,189
Mvaa 164,136,24
X 191
Mvaa 168,136,40
Mvaa 168,140,44
X 160
Mvaa 172,140,60
K 219,190,191
Mvaa 172,136,56
W 2
X 161
Cvaa 176,144,0,224
Ra 136,242,26624
Ra 140,242,27648
Mvaa 176,148,4
X 162
Cvaa 180,148,20,228
K 196,160,161
Mvaa 180,144,16
X 163
Cvaa 184,144,32,232
Mvaa 184,148,36
X 164
Cvaa 188,148,52,236
K 197,162,163
Mvaa 188,144,48
X 165
Mvaa 176,152,8
Ra 144,242,28672
Ra 148,242,29696
Mvaa 176,156,12
X 166
Mvaa 180,156,28
K 204,164,165
Mvaa 180,152,24
X 167
Mvaa 184,152,40
Mvaa 184,156,44
X 168
Mvaa 188,156,60
Mvaa 188,152,56
W 2
Ra 152,242,30720
K 205,166,167
Mvav 32,128,192
X 169
Mvav 64,128,200
Ra 156,242,31744
Mvav 96,128,208
X 170
Mvav 128,128,216
K 212,168,169
Mvav 132,132,216
X 171
Mvav 100,132,208
X 172
Mvav 68,132,200
Mvav 36,132,192
K 213,170,171
Mvav 40,136,192
X 173
Mvav 72,136,200
X 174
Mvav 104,136,208
Mvav 136,136,216
K 220,172,173
Mvav 140,140,216
X 175
Mvav 108,140,208
X 176
Mvav 76,140,200
s_waitcnt vmcnt(1) lgkmcnt(0)
s_barrier
v_xor_b32 v242, 0x10000, v242
Mvav 44,140,192
Ra 128,242,0
K 221,174,175
Mvav 48,144,192
X 177
Mvav 80,144,200
X 178
Mvav 112,144,208
Ra 132,242,1024
Mvav 144,144,216
K 198,176,177
Mvav 148,148,216
X 179
Mvav 116,148,208
X 180
Mvav 84,148,200
Ra 136,242,2048
Mvav 52,148,192
K 199,178,179
Mvav 56,152,192
X 181
Mvav 88,152,200
Ra 140,242,3072
Mvav 120,152,208
X 182
Mvav 152,152,216
K 206,180,181
Mvav 156,156,216
X 183
Mvav 124,156,208
Mvav 92,156,200
K 207,182,183
Mvav 60,156,192
W 0
Cvaa 160,128,0,224
Ra 144,242,4096
Ra 148,242,5120
Mvaa 160,132,4
s_add_u32 m0, s62, 0
Ra 152,242,6144
Cvaa 164,132,20,228
X 184
Mvaa 164,128,16
X 185
Cvaa 168,128,32,232
X 186
Mvaa 168,132,36
Ra 156,242,7168
K 214,184,185
Cvaa 172,132,52,236
Dc 1,8,61,0
Mvaa 172,128,48
X 187
Mvaa 160,136,8
X 188
Mvaa 160,140,12
Ra 128,242,16384
K 215,186,187
Mvaa 164,140,28
Dc 1,8,61,1024
Mvaa 164,136,24
X 189
Mvaa 168,136,40
X 190
Mvaa 168,140,44
Ra 132,242,17408
K 222,188,189
Mvaa 172,140,60
Dc 1,8,61,2048
Mvaa 172,136,56
X 191
v_smfmac_f32_16x16x64_bf16 a[160:163], a[176:179], v[192:199], v240
Ra 136,242,18432
K 223,190,191
v_smfmac_f32_16x16x64_bf16 a[164:167], a[176:179], v[200:207], v240
X 160
v_smfmac_f32_16x16x64_bf16 a[168:171], a[176:179], v[208:215], v240
Dc 1,8,61,3072
v_smfmac_f32_16x16x64_bf16 a[172:175], a[176:179], v[216:223], v240
W 3
X 161
Cvaa 176,144,0,224
)"
R"(s_add_u32 m0, s62, 4096
X 162
Mvaa 176,148,4
Ra 140,242,19456
K 192,160,161
Cvaa 180,148,20,228
X 163
Mvaa 180,144,16
Dc 243,8,61,0
Cvaa 184,144,32,232
X 164
Mvaa 184,148,36
K 193,162,163
Cvaa 188,148,52,236
X 165
Mvaa 188,144,48
Dc 243,8,61,1024
Ra 144,242,20480
Mvaa 176,152,8
X 166
Mvaa 176,156,12
Ra 148,242,21504
K 200,164,165
Mvaa 180,156,28
X 167
Mvaa 180,152,24
Dc 243,8,61,2048
Mvaa 184,152,40
X 168
Mvaa 184,156,44
K 201,166,167
Mvaa 188,156,60
X 169
Mvaa 188,152,56
W 2
Dc 243,8,61,3072
Ra 152,242,22528
Mvav 32,128,196
s_add_u32 m0, s62, 8192
X 170
Mvav 64,128,204
Ra 156,242,23552
K 208,168,169
Mvav 96,128,212
X 171
Mvav 128,128,220
Dc 244,8,61,0
Mvav 132,132,220
X 172
Mvav 100,132,212
Ra 128,242,8192
K 209,170,171
Mvav 68,132,204
X 173
Mvav 36,132,196
Dc 244,8,61,1024
Mvav 40,136,196
X 174
Mvav 72,136,204
Ra 132,242,9216
K 216,172,173
Mvav 104,136,212
X 175
Mvav 136,136,220
Dc 244,8,61,2048
Mvav 140,140,220
X 176
Mvav 108,140,212
Ra 136,242,10240
K 217,174,175
Mvav 76,140,204
X 177
Mvav 44,140,196
W 3
Dc 244,8,61,3072
Mvav 48,144,196
X 178
Mvav 80,144,204
s_add_u32 m0, s62, 12288
Ra 140,242,11264
Mvav 112,144,212
K 194,176,177
Mvav 144,144,220
X 179
Mvav 148,148,220
X 180
Mvav 116,148,212
Dc 245,8,61,0
Ra 144,242,12288
Mvav 84,148,204
K 195,178,179
Mvav 52,148,196
X 181
Mvav 56,152,196
X 182
Mvav 88,152,204
Dc 245,8,61,1024
Ra 148,242,13312
Mvav 120,152,212
K 202,180,181
Mvav 152,152,220
X 183
Mvav 156,156,220
X 184
Mvav 124,156,212
Dc 245,8,61,2048
Ra 152,242,14336
Mvav 92,156,204
K 203,182,183
Mvav 60,156,196
W 3
X 185
Cvaa 160,128,0,224
X 186
Mvaa 160,132,4
Dc 245,8,61,3072
Ra 156,242,15360
Cvaa 164,132,20,228
s_add_u32 s61, s61, s63
K 210,184,185
Mvaa 164,128,16
X 187
Cvaa 168,128,32,232
X 188
Mvaa 168,132,36
Cvaa 172,132,52,236
s_xor_b32 s62, s62, 0x10000
global_load_dword v3, v2, s[26:27]
K 211,186,187
Mvaa 172,128,48
Ra 128,242,24576
v_add_u32 v2, s22, v2
Mvaa 160,136,8
s_lshr_b32 s80, s15, 3
s_lshl_b32 s80, s80, 1
s_add_u32 s80, s80, s14
Mvaa 160,140,12
s_sub_u32 s80, s80, 2
s_lshl_b32 s80, s80, 12
v_min_u32 v27, 496, v1
Mvaa 164,140,28
s_add_u32 s64, s64, 1
X 189
Mvaa 164,136,24
Ra 132,242,25600
v_lshl_add_u32 v27, v27, 3, s80
Mvaa 168,136,40
X 190
Mvaa 168,140,44
v_min_u32 v27, .Lend%=-.Lbeg%=-4, v27
K 218,188,189
Mvaa 172,140,60
global_load_dword v3, v27, s[6:7]
Mvaa 172,136,56
W 2
X 191
Cvaa 176,144,0,224
X 160
Mvaa 176,148,4
Ra 136,242,26624
K 219,190,191
Cvaa 180,148,20,228
X 161
Mvaa 180,144,16
Ra 140,242,27648
Cvaa 184,144,32,232
X 162
Mvaa 184,148,36
K 196,160,161
Cvaa 188,148,52,236
X 163
Mvaa 188,144,48
X 164
Mvaa 176,152,8
Ra 144,242,28672
K 197,162,163
Mvaa 176,156,12
X 165
Mvaa 180,156,28
Ra 148,242,29696
Mvaa 180,152,24
X 166
Mvaa 184,152,40
K 204,164,165
Mvaa 184,156,44
X 167
Mvaa 188,156,60
X 168
Mvaa 188,152,56
W 2
Ra 152,242,30720
K 205,166,167
Mvav 32,128,192
X 169
Mvav 64,128,200
Ra 156,242,31744
Mvav 96,128,208
X 170
Mvav 128,128,216
K 212,168,169
Mvav 132,132,216
X 171
Mvav 100,132,208
X 172
Mvav 68,132,200
Mvav 36,132,192
K 213,170,171
Mvav 40,136,192
X 173
Mvav 72,136,200
X 174
Mvav 104,136,208
Mvav 136,136,216
K 220,172,173
Mvav 140,140,216
X 175
Mvav 108,140,208
X 176
Mvav 76,140,200
s_waitcnt vmcnt(2) lgkmcnt(0)
s_barrier
v_xor_b32 v242, 0x10000, v242
Mvav 44,140,192
Ra 128,242,0
K 221,174,175
Mvav 48,144,192
X 177
Mvav 80,144,200
X 178
Mvav 112,144,208
Ra 132,242,1024
Mvav 144,144,216
K 198,176,177
Mvav 148,148,216
X 179
Mvav 116,148,208
X 180
Mvav 84,148,200
Ra 136,242,2048
Mvav 52,148,192
K 199,178,179
Mvav 56,152,192
X 181
Mvav 88,152,200
Ra 140,242,3072
Mvav 120,152,208
X 182
Mvav 152,152,216
K 206,180,181
Mvav 156,156,216
X 183
Mvav 124,156,208
Mvav 92,156,200
K 207,182,183
Mvav 60,156,192
W 0
Cvaa 160,128,0,224
Ra 144,242,4096
Ra 148,242,5120
Mvaa 160,132,4
s_add_u32 m0, s62, 0
Ra 152,242,6144
Cvaa 164,132,20,228
X 184
Mvaa 164,128,16
X 185
Cvaa 168,128,32,232
X 186
Mvaa 168,132,36
Ra 156,242,7168
K 214,184,185
Cvaa 172,132,52,236
Dc 1,8,61,0
Mvaa 172,128,48
X 187
Mvaa 160,136,8
X 188
Mvaa 160,140,12
Ra 128,242,16384
K 215,186,187
Mvaa 164,140,28
Dc 1,8,61,1024
Mvaa 164,136,24
X 189
Mvaa 168,136,40
X 190
Mvaa 168,140,44
Ra 132,242,17408
K 222,188,189
Mvaa 172,140,60
Dc 1,8,61,2048
Mvaa 172,136,56
X 191
v_smfmac_f32_16x16x64_bf16 a[160:163], a[176:179], v[192:199], v240
Ra 136,242,18432
K 223,190,191
v_smfmac_f32_16x16x64_bf16 a[164:167], a[176:179], v[200:207], v240
X 160
v_smfmac_f32_16x16x64_bf16 a[168:171], a[176:179], v[208:215], v240
Dc 1,8,61,3072
v_smfmac_f32_16x16x64_bf16 a[172:175], a[176:179], v[216:223], v240
W 3
X 161
Cvaa 176,144,0,224
s_add_u32 m0, s62, 4096
X 162
Mvaa 176,148,4
s_sub_u32 s81, s57, 1
Ra 140,242,19456
K 192,160,161
Cvaa 180,148,20,228
X 163
Mvaa 180,144,16
s_lshl_b32 s85, s81, 5
Dc 243,8,61,0
Cvaa 184,144,32,232
X 164
Mvaa 184,148,36
s_add_u32 s85, s85, s56
K 193,162,163
Cvaa 188,148,52,236
X 165
Mvaa 188,144,48
Dc 243,8,61,1024
Ra 144,242,20480
Mvaa 176,152,8
s_lshl_b32 s85, s85, 1
X 166
Mvaa 176,156,12
Ra 148,242,21504
K 200,164,165
Mvaa 180,156,28
s_add_u32 s85, s85, s14
X 167
Mvaa 180,152,24
Dc 243,8,61,2048
Mvaa 184,152,40
s_sub_u32 s85, s85, 2
X 168
Mvaa 184,156,44
K 201,166,167
Mvaa 188,156,60
X 169
Mvaa 188,152,56
W 2
s_cmp_lt_u32 s85, 160
s_cselect_b64 s[20:21], 0, -1
Dc 243,8,61,3072
Mvav 32,128,196
s_add_u32 m0, s62, 8192
Ra 152,242,22528
Ra 156,242,23552
Mvav 64,128,204
s_mul_i32 s18, s85, 205
X 170
Mvav 96,128,212
K 208,168,169
Mvav 128,128,220
s_lshr_b32 s18, s18, 12
X 171
Mvav 132,132,220
Dc 244,8,61,0
Ra 128,242,8192
Mvav 100,132,212
X 172
Mvav 68,132,204
s_and_b32 s18, s18, 7
K 209,170,171
Mvav 36,132,196
X 173
Mvav 40,136,196
Dc 244,8,61,1024
v_lshrrev_b32 v4, 1, v1
Mvav 72,136,204
X 174
Mvav 104,136,212
Ra 132,242,9216
K 216,172,173
Mvav 136,136,220
X 175
Mvav 140,140,220
Dc 244,8,61,2048
v_min_u32 v5, 344, v4
Mvav 108,140,212
s_lshl_b32 s81, s18, 3
X 176
Mvav 76,140,204
Ra 136,242,10240
K 217,174,175
Mvav 44,140,196
W 3
X 177
Mvav 48,144,196
Dc 244,8,61,3072
v_mov_b32 v6, s81
Mvav 80,144,204
s_add_u32 m0, s62, 12288
X 178
Mvav 112,144,212
Ra 140,242,11264
)"
R"(K 194,176,177
Mvav 144,144,220
X 179
Mvav 148,148,220
buffer_load_dwordx2 v[8:9], v4, s[8:11], 0 offen offset:2560 sc1
Ra 144,242,12288
Mvav 116,148,212
X 180
Mvav 84,148,204
K 195,178,179
Mvav 52,148,196
X 181
Mvav 56,152,196
buffer_load_dwordx2 v[10:11], v5, s[8:11], 0 offen offset:3072 sc1
Ra 148,242,13312
Mvav 88,152,204
X 182
Mvav 120,152,212
K 202,180,181
Mvav 152,152,220
X 183
Mvav 156,156,220
Dc 245,8,61,0
Ra 152,242,14336
Mvav 124,156,212
X 184
Mvav 92,156,204
K 203,182,183
v_bfe_u32 v28, v1, 4, 4
Mvav 60,156,196
W 3
X 185
Cvaa 160,128,0,224
buffer_load_dwordx2 v[12:13], v6, s[8:11], 0 offen offset:3584 sc1
v_lshlrev_b32 v28, 3, v28
Mvaa 160,132,4
s_lshl_b32 s80, s56, 9
X 186
Cvaa 164,132,20,228
s_add_u32 s80, s80, 4480
Ra 156,242,15360
K 210,184,185
Mvaa 164,128,16
X 187
Cvaa 168,128,32,232
Dc 245,8,61,1024
Mvaa 168,132,36
X 188
Cvaa 172,132,52,236
K 211,186,187
Mvaa 172,128,48
X 189
Mvaa 160,136,8
Dc 245,8,61,2048
Ra 128,242,24576
Mvaa 160,140,12
X 190
Mvaa 164,140,28
Ra 132,242,25600
K 218,188,189
Mvaa 164,136,24
X 191
Mvaa 168,136,40
buffer_load_dwordx2 v[28:29], v28, s[8:11], s80 offen sc1
Mvaa 168,140,44
Dc 245,8,61,3072
s_add_u32 s61, s61, s63
s_xor_b32 s62, s62, 0x10000
s_add_u32 s64, s64, 1
Mvaa 172,140,60
X 160
Mvaa 172,136,56
W 2
Ra 136,242,26624
K 219,190,191
Cvaa 176,144,0,224
X 161
Mvaa 176,148,4
global_load_dword v3, v2, s[26:27]
v_add_u32 v2, s22, v2
Cvaa 180,148,20,228
Ra 140,242,27648
Mvaa 180,144,16
X 162
Cvaa 184,144,32,232
K 196,160,161
Mvaa 184,148,36
X 163
Cvaa 188,148,52,236
X 164
Mvaa 188,144,48
Ra 144,242,28672
K 197,162,163
Mvaa 176,152,8
X 165
Mvaa 176,156,12
Ra 148,242,29696
Mvaa 180,156,28
X 166
Mvaa 180,152,24
K 204,164,165
Mvaa 184,152,40
X 167
Mvaa 184,156,44
X 168
Mvaa 188,156,60
Mvaa 188,152,56
W 2
Ra 152,242,30720
K 205,166,167
Mvav 32,128,192
X 169
Mvav 64,128,200
Ra 156,242,31744
Mvav 96,128,208
X 170
Mvav 128,128,216
K 212,168,169
Mvav 132,132,216
X 171
Mvav 100,132,208
X 172
Mvav 68,132,200
Mvav 36,132,192
K 213,170,171
Mvav 40,136,192
X 173
Mvav 72,136,200
X 174
Mvav 104,136,208
Mvav 136,136,216
s_waitcnt vmcnt(2)
v_cmp_eq_u64 vcc, s[12:13], v[28:29]
s_cmp_eq_u64 vcc, -1
s_cbranch_scc0 .Lspin_73%=
.Lok_74%=:
v_cmp_eq_u64 vcc, s[12:13], v[8:9]
v_cmp_eq_u64_e64 s[86:87], s[12:13], v[10:11]
s_and_b64 vcc, vcc, s[86:87]
s_or_b64 vcc, vcc, s[20:21]
s_cmp_eq_u64 vcc, -1
s_cbranch_scc0 .Lspin_76%=
.Lok_77%=:
K 220,172,173
Mvav 140,140,216
X 175
Mvav 108,140,208
X 176
Mvav 76,140,200
s_waitcnt vmcnt(1) lgkmcnt(0)
s_barrier
v_xor_b32 v242, 0x10000, v242
Mvav 44,140,192
Ra 128,242,0
K 221,174,175
Mvav 48,144,192
X 177
Mvav 80,144,200
X 178
Mvav 112,144,208
Ra 132,242,1024
Mvav 144,144,216
K 198,176,177
Mvav 148,148,216
X 179
Mvav 116,148,208
X 180
Mvav 84,148,200
Ra 136,242,2048
Mvav 52,148,192
K 199,178,179
Mvav 56,152,192
X 181
Mvav 88,152,200
Ra 140,242,3072
Mvav 120,152,208
X 182
Mvav 152,152,216
K 206,180,181
Mvav 156,156,216
X 183
Mvav 124,156,208
Mvav 92,156,200
K 207,182,183
Mvav 60,156,192
W 0
Cvaa 160,128,0,224
Ra 144,242,4096
Ra 148,242,5120
Mvaa 160,132,4
s_add_u32 m0, s62, 0
Ra 152,242,6144
Cvaa 164,132,20,228
X 184
Mvaa 164,128,16
X 185
Cvaa 168,128,32,232
X 186
Mvaa 168,132,36
Ra 156,242,7168
K 214,184,185
Cvaa 172,132,52,236
Dc 1,8,61,0
Mvaa 172,128,48
X 187
Mvaa 160,136,8
X 188
Mvaa 160,140,12
Ra 128,242,16384
K 215,186,187
Mvaa 164,140,28
Dc 1,8,61,1024
Mvaa 164,136,24
X 189
Mvaa 168,136,40
X 190
Mvaa 168,140,44
Ra 132,242,17408
K 222,188,189
Mvaa 172,140,60
Dc 1,8,61,2048
Mvaa 172,136,56
X 191
v_smfmac_f32_16x16x64_bf16 a[160:163], a[176:179], v[192:199], v240
Ra 136,242,18432
K 223,190,191
v_smfmac_f32_16x16x64_bf16 a[164:167], a[176:179], v[200:207], v240
X 160
v_smfmac_f32_16x16x64_bf16 a[168:171], a[176:179], v[208:215], v240
Dc 1,8,61,3072
v_smfmac_f32_16x16x64_bf16 a[172:175], a[176:179], v[216:223], v240
W 3
X 161
Cvaa 176,144,0,224
s_add_u32 m0, s62, 4096
X 162
Mvaa 176,148,4
Ra 140,242,19456
K 192,160,161
Cvaa 180,148,20,228
X 163
Mvaa 180,144,16
Dc 243,8,61,0
Cvaa 184,144,32,232
X 164
Mvaa 184,148,36
K 193,162,163
Cvaa 188,148,52,236
X 165
Mvaa 188,144,48
Dc 243,8,61,1024
Ra 144,242,20480
Mvaa 176,152,8
X 166
Mvaa 176,156,12
Ra 148,242,21504
K 200,164,165
Mvaa 180,156,28
X 167
Mvaa 180,152,24
Dc 243,8,61,2048
Mvaa 184,152,40
X 168
Mvaa 184,156,44
K 201,166,167
Mvaa 188,156,60
X 169
Mvaa 188,152,56
W 2
Dc 243,8,61,3072
Ra 152,242,22528
Mvav 32,128,196
s_add_u32 m0, s62, 8192
X 170
Mvav 64,128,204
Ra 156,242,23552
K 208,168,169
Mvav 96,128,212
X 171
Mvav 128,128,220
Dc 244,8,61,0
Mvav 132,132,220
X 172
Mvav 100,132,212
Ra 128,242,8192
K 209,170,171
Mvav 68,132,204
X 173
Mvav 36,132,196
Dc 244,8,61,1024
Mvav 40,136,196
X 174
Mvav 72,136,204
Ra 132,242,9216
K 216,172,173
Mvav 104,136,212
X 175
Mvav 136,136,220
Dc 244,8,61,2048
Mvav 140,140,220
X 176
Mvav 108,140,212
Ra 136,242,10240
K 217,174,175
Mvav 76,140,204
X 177
Mvav 44,140,196
W 3
Dc 244,8,61,3072
Mvav 48,144,196
X 178
Mvav 80,144,204
s_add_u32 m0, s62, 12288
Ra 140,242,11264
Mvav 112,144,212
K 194,176,177
Mvav 144,144,220
X 179
Mvav 148,148,220
X 180
Mvav 116,148,212
Dc 245,8,61,0
Ra 144,242,12288
Mvav 84,148,204
K 195,178,179
Mvav 52,148,196
X 181
Mvav 56,152,196
X 182
Mvav 88,152,204
Dc 245,8,61,1024
Ra 148,242,13312
Mvav 120,152,212
v_xor_b32 v6, s12, v12
K 202,180,181
Mvav 152,152,220
X 183
Mvav 156,156,220
Ra 152,242,14336
v_and_b32 v6, 2047, v6
Mvav 124,156,212
Dc 245,8,61,2048
v_lshlrev_b32 v6, 3, v6
Mvav 92,156,204
X 184
Mvav 60,156,196
W 3
K 203,182,183
Cvaa 160,128,0,224
s_mov_b32 s81, 40960
X 185
Mvaa 160,132,4
Dc 245,8,61,3072
Ra 156,242,15360
Cvaa 164,132,20,228
s_add_u32 s61, s61, s63
X 186
Mvaa 164,128,16
K 210,184,185
Cvaa 168,128,32,232
X 187
Mvaa 168,132,36
buffer_load_dword v7, v6, s[8:11], s81 offen sc1
v_lshrrev_b32 v5, 7, v1
Cvaa 172,132,52,236
s_xor_b32 s62, s62, 0x10000
X 188
Mvaa 172,128,48
K 211,186,187
v_mul_u32_u24 v4, 20480, v5
Mvaa 160,136,8
X 189
Mvaa 160,140,12
s_add_u32 s64, s64, 1
Ra 128,242,24576
Ra 132,242,25600
Mvaa 164,140,28
X 190
Mvaa 164,136,24
v_and_b32 v6, 0x70, v1
K 218,188,189
Mvaa 168,136,40
X 191
Mvaa 168,140,44
v_add_u32 v4, v4, v6
Mvaa 172,140,60
X 160
Mvaa 172,136,56
W 2
v_add_u32 v5, 2, v5
K 219,190,191
Cvaa 176,144,0,224
X 161
Mvaa 176,148,4
Ra 136,242,26624
v_lshrrev_b32 v5, 3, v5
Cvaa 180,148,20,228
Ra 140,242,27648
v_lshl_or_b32 v5, v5, 31, v4
Mvaa 180,144,16
)"
R"(X 162
Cvaa 184,144,32,232
K 196,160,161
Mvaa 184,148,36
s_lshl_b32 s19, s85, 7
X 163
Cvaa 188,148,52,236
X 164
Mvaa 188,144,48
s_add_u32 s19, s19, 2752512
Ra 144,242,28672
K 197,162,163
Mvaa 176,152,8
X 165
Mvaa 176,156,12
s_and_b32 s81, s20, 134217728
Ra 148,242,29696
Mvaa 180,156,28
X 166
Mvaa 180,152,24
K 204,164,165
Mvaa 184,152,40
s_or_b32 s19, s19, s81
X 167
Mvaa 184,156,44
X 168
Mvaa 188,156,60
buffer_load_dwordx4 a[64:67], v4, s[8:11], s19 offen sc1
Mvaa 188,152,56
W 2
Ra 152,242,30720
K 205,166,167
Mvav 32,128,192
s_add_u32 s19, s19, 163840
X 169
Mvav 64,128,200
Ra 156,242,31744
Mvav 96,128,208
X 170
Mvav 128,128,216
buffer_load_dwordx4 a[68:71], v4, s[8:11], s19 offen sc1
K 212,168,169
Mvav 132,132,216
X 171
Mvav 100,132,208
s_add_u32 s19, s19, 163840
X 172
Mvav 68,132,200
Mvav 36,132,192
buffer_load_dwordx4 a[72:75], v4, s[8:11], s19 offen sc1
K 213,170,171
Mvav 40,136,192
X 173
Mvav 72,136,200
X 174
Mvav 104,136,208
s_add_u32 s19, s19, 163840
Mvav 136,136,216
K 220,172,173
Mvav 140,140,216
X 175
Mvav 108,140,208
buffer_load_dwordx4 a[76:79], v4, s[8:11], s19 offen sc1
Mvav 76,140,200
s_waitcnt vmcnt(5) lgkmcnt(0)
s_barrier
v_xor_b32 v242, 0x10000, v242
Mvav 44,140,192
s_add_u32 s19, s19, 163840
X 176
Mvav 48,144,192
Ra 128,242,0
K 221,174,175
Mvav 80,144,200
X 177
Mvav 112,144,208
buffer_load_dwordx4 a[80:83], v4, s[8:11], s19 offen sc1
Ra 132,242,1024
Mvav 144,144,216
s_add_u32 s19, s19, 163840
X 178
Mvav 148,148,216
K 198,176,177
Mvav 116,148,208
X 179
Mvav 84,148,200
buffer_load_dwordx4 a[84:87], v4, s[8:11], s19 offen sc1
Ra 136,242,2048
Mvav 52,148,192
X 180
Mvav 56,152,192
s_add_u32 s19, s19, 163840
Ra 140,242,3072
K 199,178,179
Mvav 88,152,200
X 181
Mvav 120,152,208
X 182
Mvav 152,152,216
buffer_load_dwordx4 a[88:91], v5, s[8:11], s19 offen sc1
K 206,180,181
Mvav 156,156,216
X 183
Mvav 124,156,208
Mvav 92,156,200
K 207,182,183
Mvav 60,156,192
W 0
Cvaa 160,128,0,224
Ra 144,242,4096
Ra 148,242,5120
Mvaa 160,132,4
s_add_u32 m0, s62, 0
Ra 152,242,6144
Cvaa 164,132,20,228
X 184
Mvaa 164,128,16
X 185
Cvaa 168,128,32,232
X 186
Mvaa 168,132,36
Ra 156,242,7168
K 214,184,185
Cvaa 172,132,52,236
Dc 1,8,61,0
Mvaa 172,128,48
X 187
Mvaa 160,136,8
X 188
Mvaa 160,140,12
Ra 128,242,16384
K 215,186,187
Mvaa 164,140,28
Dc 1,8,61,1024
Mvaa 164,136,24
X 189
Mvaa 168,136,40
X 190
Mvaa 168,140,44
Ra 132,242,17408
K 222,188,189
Mvaa 172,140,60
Dc 1,8,61,2048
Mvaa 172,136,56
X 191
v_smfmac_f32_16x16x64_bf16 a[160:163], a[176:179], v[192:199], v240
Ra 136,242,18432
K 223,190,191
v_smfmac_f32_16x16x64_bf16 a[164:167], a[176:179], v[200:207], v240
X 160
v_smfmac_f32_16x16x64_bf16 a[168:171], a[176:179], v[208:215], v240
Dc 1,8,61,3072
v_smfmac_f32_16x16x64_bf16 a[172:175], a[176:179], v[216:223], v240
W 3
X 161
Cvaa 176,144,0,224
s_add_u32 m0, s62, 4096
X 162
Mvaa 176,148,4
Ra 140,242,19456
K 192,160,161
Cvaa 180,148,20,228
X 163
Mvaa 180,144,16
Dc 243,8,61,0
Cvaa 184,144,32,232
X 164
Mvaa 184,148,36
K 193,162,163
Cvaa 188,148,52,236
X 165
Mvaa 188,144,48
Dc 243,8,61,1024
Ra 144,242,20480
Mvaa 176,152,8
X 166
Mvaa 176,156,12
Ra 148,242,21504
K 200,164,165
Mvaa 180,156,28
X 167
Mvaa 180,152,24
Dc 243,8,61,2048
Mvaa 184,152,40
X 168
Mvaa 184,156,44
K 201,166,167
Mvaa 188,156,60
X 169
Mvaa 188,152,56
W 2
Dc 243,8,61,3072
Ra 152,242,22528
Mvav 32,128,196
s_add_u32 m0, s62, 8192
X 170
Mvav 64,128,204
Ra 156,242,23552
K 208,168,169
Mvav 96,128,212
X 171
Mvav 128,128,220
Dc 244,8,61,0
Mvav 132,132,220
X 172
Mvav 100,132,212
Ra 128,242,8192
K 209,170,171
Mvav 68,132,204
X 173
Mvav 36,132,196
Dc 244,8,61,1024
Mvav 40,136,196
X 174
Mvav 72,136,204
Ra 132,242,9216
K 216,172,173
Mvav 104,136,212
X 175
Mvav 136,136,220
Dc 244,8,61,2048
Mvav 140,140,220
s_bfe_u32 s81, s59, 0x10001
X 176
Mvav 108,140,212
s_lshl_b32 s82, s57, 1
Ra 136,242,10240
K 217,174,175
Mvav 76,140,204
s_add_u32 s81, s81, s82
X 177
Mvav 44,140,196
W 3
s_lshl_b32 s83, s81, 2
Dc 244,8,61,3072
Mvav 48,144,196
s_lshr_b32 s81, s59, 2
X 178
Mvav 80,144,204
s_add_u32 m0, s62, 12288
s_lshl_b32 s81, s81, 1
Ra 140,242,11264
Mvav 112,144,212
s_add_u32 s83, s83, s81
K 194,176,177
Mvav 144,144,220
s_and_b32 s81, s59, 1
X 179
Mvav 148,148,220
s_add_u32 s83, s83, s81
X 180
Mvav 116,148,212
s_and_b32 s81, s14, 1
Dc 245,8,61,0
Ra 144,242,12288
Mvav 84,148,204
s_mul_i32 s81, s81, 124
K 195,178,179
Mvav 52,148,196
s_xor_b32 s83, s83, s81
X 181
Mvav 56,152,196
s_mul_i32 s83, s83, 20480
X 182
Mvav 88,152,204
s_mul_i32 s81, s58, 320
Dc 245,8,61,1024
Ra 148,242,13312
Mvav 120,152,212
s_add_u32 s82, s83, s81
K 202,180,181
Mvav 152,152,220
X 183
Mvav 156,156,220
Ra 152,242,14336
v_bfe_u32 v246, v1, 4, 4
Mvav 124,156,212
Dc 245,8,61,2048
v_mul_u32_u24 v246, 1280, v246
Mvav 92,156,204
X 184
Mvav 60,156,196
W 3
v_lshrrev_b32 v247, 8, v1
K 203,182,183
Cvaa 160,128,0,224
v_mul_u32_u24 v247, 104, v247
v_add3_u32 v246, v246, v247, s82
Mvaa 160,132,4
X 185
Cvaa 164,132,20,228
Dc 245,8,61,3072
Ra 156,242,15360
Mvaa 164,128,16
s_add_u32 s61, s61, s63
X 186
Cvaa 168,128,32,232
K 210,184,185
Mvaa 168,132,36
X 187
Cvaa 172,132,52,236
s_xor_b32 s62, s62, 0x10000
X 188
Mvaa 172,128,48
global_load_dword v3, v246, s[16:17]
K 211,186,187
Mvaa 160,136,8
s_lshl_b32 s81, s81, 3
s_add_u32 s83, s83, s81
s_add_u32 s83, s83, 131072
Mvaa 160,140,12
s_add_u32 s64, s64, 1
v_lshrrev_b32 v246, 9, v1
v_mul_u32_u24 v246, 10240, v246
Mvaa 164,140,28
Ra 128,242,24576
v_and_b32 v247, 0x1f0, v1
Mvaa 164,136,24
X 189
Mvaa 168,136,40
Ra 132,242,25600
v_min_u32 v247, 304, v247
Mvaa 168,140,44
X 190
Mvaa 172,140,60
v_lshl_add_u32 v246, v247, 3, v246
K 218,188,189
Mvaa 172,136,56
W 2
buffer_load_dword v3, v246, s[8:11], s83 offen sc1
Ra 136,242,26624
Cvaa 176,144,0,224
X 191
Mvaa 176,148,4
X 160
Cvaa 180,148,20,228
Ra 140,242,27648
K 219,190,191
Mvaa 180,144,16
X 161
Cvaa 184,144,32,232
X 162
Mvaa 184,148,36
K 196,160,161
Cvaa 188,148,52,236
X 163
Mvaa 188,144,48
X 164
Mvaa 176,152,8
Ra 144,242,28672
K 197,162,163
Mvaa 176,156,12
X 165
Mvaa 180,156,28
Ra 148,242,29696
Mvaa 180,152,24
X 166
Mvaa 184,152,40
K 204,164,165
Mvaa 184,156,44
X 167
Mvaa 188,156,60
X 168
Mvaa 188,152,56
W 2
Ra 152,242,30720
K 205,166,167
Mvav 32,128,192
X 169
Mvav 64,128,200
Ra 156,242,31744
Mvav 96,128,208
X 170
Mvav 128,128,216
K 212,168,169
Mvav 132,132,216
X 171
Mvav 100,132,208
X 172
Mvav 68,132,200
Mvav 36,132,192
K 213,170,171
Mvav 40,136,192
X 173
Mvav 72,136,200
X 174
Mvav 104,136,208
Mvav 136,136,216
K 220,172,173
Mvav 140,140,216
X 175
Mvav 108,140,208
X 176
Mvav 76,140,200
s_waitcnt vmcnt(2) lgkmcnt(0)
s_barrier
v_xor_b32 v242, 0x10000, v242
Mvav 44,140,192
Ra 128,242,0
K 221,174,175
)"
R"(Mvav 48,144,192
X 177
Mvav 80,144,200
X 178
Mvav 112,144,208
Ra 132,242,1024
Mvav 144,144,216
K 198,176,177
Mvav 148,148,216
X 179
Mvav 116,148,208
X 180
Mvav 84,148,200
Ra 136,242,2048
Mvav 52,148,192
K 199,178,179
Mvav 56,152,192
X 181
Mvav 88,152,200
Ra 140,242,3072
Mvav 120,152,208
X 182
Mvav 152,152,216
K 206,180,181
Mvav 156,156,216
X 183
Mvav 124,156,208
Mvav 92,156,200
K 207,182,183
Mvav 60,156,192
W 0
Cvaa 160,128,0,224
Ra 144,242,4096
Ra 148,242,5120
Mvaa 160,132,4
s_add_u32 m0, s62, 0
Ra 152,242,6144
G 8,64
Cvaa 164,132,20,228
X 184
Mvaa 164,128,16
X 185
Cvaa 168,128,32,232
X 186
Mvaa 168,132,36
G 9,65
K 214,184,185
Cvaa 172,132,52,236
Ra 156,242,7168
Dc 1,8,61,0
Mvaa 172,128,48
X 187
Mvaa 160,136,8
X 188
Mvaa 160,140,12
Ra 128,242,16384
G 10,66
Mvaa 164,140,28
Dc 1,8,61,1024
K 215,186,187
Mvaa 164,136,24
X 189
Mvaa 168,136,40
Ra 132,242,17408
G 11,67
Mvaa 168,140,44
X 190
Mvaa 172,140,60
Dc 1,8,61,2048
K 222,188,189
Mvaa 172,136,56
X 191
v_smfmac_f32_16x16x64_bf16 a[160:163], a[176:179], v[192:199], v240
G 12,68
K 223,190,191
v_smfmac_f32_16x16x64_bf16 a[164:167], a[176:179], v[200:207], v240
Ra 136,242,18432
v_smfmac_f32_16x16x64_bf16 a[168:171], a[176:179], v[208:215], v240
Dc 1,8,61,3072
G 13,69
v_smfmac_f32_16x16x64_bf16 a[172:175], a[176:179], v[216:223], v240
W 3
X 160
Cvaa 176,144,0,224
s_add_u32 m0, s62, 4096
X 161
Mvaa 176,148,4
X 162
Cvaa 180,148,20,228
Ra 140,242,19456
K 192,160,161
Mvaa 180,144,16
Dc 243,8,61,0
G 14,70
Cvaa 184,144,32,232
X 163
Mvaa 184,148,36
X 164
Cvaa 188,148,52,236
G 15,71
K 193,162,163
Mvaa 188,144,48
Dc 243,8,61,1024
Ra 144,242,20480
Mvaa 176,152,8
X 165
Mvaa 176,156,12
X 166
Mvaa 180,156,28
Ra 148,242,21504
A 8,8,12
Mvaa 180,152,24
Dc 243,8,61,2048
K 200,164,165
Mvaa 184,152,40
X 167
Mvaa 184,156,44
A 9,9,13
Mvaa 188,156,60
X 168
Mvaa 188,152,56
W 2
Dc 243,8,61,3072
K 201,166,167
Mvav 32,128,196
s_add_u32 m0, s62, 8192
X 169
Mvav 64,128,204
Ra 152,242,22528
A 10,10,14
Mvav 96,128,212
X 170
Mvav 128,128,220
Ra 156,242,23552
K 208,168,169
Mvav 132,132,220
Dc 244,8,61,0
A 11,11,15
Mvav 100,132,212
X 171
Mvav 68,132,204
X 172
Mvav 36,132,196
Ra 128,242,8192
K 209,170,171
Mvav 40,136,196
Dc 244,8,61,1024
G 12,72
Mvav 72,136,204
X 173
Mvav 104,136,212
X 174
Mvav 136,136,220
Ra 132,242,9216
G 13,73
Mvav 140,140,220
Dc 244,8,61,2048
K 216,172,173
Mvav 108,140,212
X 175
Mvav 76,140,204
X 176
Mvav 44,140,196
W 2
Ra 136,242,10240
G 14,74
Mvav 48,144,196
Dc 244,8,61,3072
K 217,174,175
Mvav 80,144,204
s_add_u32 m0, s62, 12288
X 177
Mvav 112,144,212
Ra 140,242,11264
G 15,75
Mvav 144,144,220
X 178
Mvav 148,148,220
K 194,176,177
A 8,8,12
Mvav 116,148,212
X 179
Mvav 84,148,204
X 180
Mvav 52,148,196
Dc 245,8,61,0
Ra 144,242,12288
Mvav 56,152,196
K 195,178,179
A 9,9,13
Mvav 88,152,204
X 181
Mvav 120,152,212
X 182
Mvav 152,152,220
Dc 245,8,61,1024
Ra 148,242,13312
Mvav 156,156,220
K 202,180,181
A 10,10,14
Mvav 124,156,212
X 183
Mvav 92,156,204
X 184
Mvav 60,156,196
W 2
Dc 245,8,61,2048
Ra 152,242,14336
Cvaa 160,128,0,224
A 11,11,15
K 203,182,183
Mvaa 160,132,4
X 185
Cvaa 164,132,20,228
X 186
Mvaa 164,128,16
Dc 245,8,61,3072
Ra 156,242,15360
Cvaa 168,128,32,232
s_add_u32 s61, s61, s63
G 12,76
K 210,184,185
Mvaa 168,132,36
X 187
Cvaa 172,132,52,236
s_xor_b32 s62, s62, 0x10000
G 13,77
Mvaa 172,128,48
X 188
Mvaa 160,136,8
K 211,186,187
G 14,78
Mvaa 160,140,12
s_add_u32 s64, s64, 1
X 189
Mvaa 164,140,28
Ra 128,242,24576
Ra 132,242,25600
Mvaa 164,136,24
X 190
Mvaa 168,136,40
K 218,188,189
G 15,79
Mvaa 168,140,44
X 191
Mvaa 172,140,60
s_lshl_b32 s92, s58, 2
X 160
Mvaa 172,136,56
W 2
K 219,190,191
A 8,8,12
Cvaa 176,144,0,224
X 161
Mvaa 176,148,4
Ra 136,242,26624
Ra 140,242,27648
Cvaa 180,148,20,228
s_add_u32 s91, s92, s51
X 162
Mvaa 180,144,16
A 9,9,13
K 196,160,161
Cvaa 184,144,32,232
X 163
Mvaa 184,148,36
s_add_u32 s95, s92, 0
X 164
Cvaa 188,148,52,236
A 10,10,14
K 197,162,163
Mvaa 188,144,48
X 165
Mvaa 176,152,8
s_and_b32 s95, s95, 15
Ra 144,242,28672
A 11,11,15
Mvaa 176,156,12
Ra 148,242,29696
Mvaa 180,156,28
X 166
Mvaa 180,152,24
s_mul_i32 s95, s95, 40960
K 204,164,165
Mvaa 184,152,40
X 167
Mvaa 184,156,44
G 12,80
Mvaa 188,156,60
s_mul_i32 s71, s51, 0
X 168
Mvaa 188,152,56
W 2
K 205,166,167
G 13,81
Mvav 32,128,192
X 169
Mvav 64,128,200
s_add_u32 s71, s71, 133120
Ra 152,242,30720
Ra 156,242,31744
Mvav 96,128,208
X 170
Mvav 128,128,216
G 14,82
K 212,168,169
Mvav 132,132,216
s_add_u32 m0, s71, s48
X 171
Mvav 100,132,208
X 172
Mvav 68,132,200
G 15,83
Mvav 36,132,192
s_lshl2_add_u32 s94, s48, s95
K 213,170,171
Mvav 40,136,192
X 173
Mvav 72,136,200
X 174
Mvav 104,136,208
D 16,40,94,0
A 8,8,12
Mvav 136,136,216
K 220,172,173
Mvav 140,140,216
X 175
Mvav 108,140,208
A 9,9,13
Mvav 76,140,200
s_waitcnt vmcnt(1) lgkmcnt(0)
s_barrier
v_xor_b32 v242, 0x10000, v242
Mvav 44,140,192
D 17,40,94,1024
Ra 128,242,0
Mvav 48,144,192
X 176
Mvav 80,144,200
K 221,174,175
A 10,10,14
Mvav 112,144,208
X 177
Mvav 144,144,216
D 18,40,94,2048
Ra 132,242,1024
Mvav 148,148,216
X 178
Mvav 116,148,208
K 198,176,177
A 11,11,15
Mvav 84,148,200
X 179
Mvav 52,148,192
D 19,40,94,3072
Ra 136,242,2048
Mvav 56,152,192
s_add_u32 m0, m0, 4096
X 180
Mvav 88,152,200
G 12,84
K 199,178,179
Mvav 120,152,208
X 181
Mvav 152,152,216
Ra 140,242,3072
buffer_load_dwordx4 v20, s[40:43], s94 offen lds
Mvav 156,156,216
X 182
Mvav 124,156,208
K 206,180,181
Mvav 92,156,200
X 183
Mvav 60,156,192
K 207,182,183
W 0
Cvaa 160,128,0,224
Ra 144,242,4096
Ra 148,242,5120
Mvaa 160,132,4
s_add_u32 m0, s62, 0
Ra 152,242,6144
G 13,85
Cvaa 164,132,20,228
X 184
Mvaa 164,128,16
X 185
Cvaa 168,128,32,232
X 186
Mvaa 168,132,36
G 14,86
K 214,184,185
Cvaa 172,132,52,236
Ra 156,242,7168
Dc 1,8,61,0
Mvaa 172,128,48
X 187
Mvaa 160,136,8
X 188
Mvaa 160,140,12
)"
R"(Ra 128,242,16384
G 15,87
Mvaa 164,140,28
Dc 1,8,61,1024
K 215,186,187
Mvaa 164,136,24
X 189
Mvaa 168,136,40
Ra 132,242,17408
A 8,8,12
Mvaa 168,140,44
X 190
Mvaa 172,140,60
Dc 1,8,61,2048
K 222,188,189
Mvaa 172,136,56
X 191
v_smfmac_f32_16x16x64_bf16 a[160:163], a[176:179], v[192:199], v240
A 9,9,13
K 223,190,191
v_smfmac_f32_16x16x64_bf16 a[164:167], a[176:179], v[200:207], v240
Ra 136,242,18432
A 10,10,14
v_smfmac_f32_16x16x64_bf16 a[168:171], a[176:179], v[208:215], v240
Dc 1,8,61,3072
v_smfmac_f32_16x16x64_bf16 a[172:175], a[176:179], v[216:223], v240
W 3
X 160
Cvaa 176,144,0,224
s_add_u32 m0, s62, 4096
X 161
Mvaa 176,148,4
X 162
Cvaa 180,148,20,228
Ra 140,242,19456
A 11,11,15
Mvaa 180,144,16
Dc 243,8,61,0
K 192,160,161
Cvaa 184,144,32,232
X 163
Mvaa 184,148,36
X 164
Cvaa 188,148,52,236
G 12,88
K 193,162,163
Mvaa 188,144,48
Dc 243,8,61,1024
G 13,89
Mvaa 176,152,8
X 165
Mvaa 176,156,12
X 166
Mvaa 180,156,28
Ra 144,242,20480
Ra 148,242,21504
Mvaa 180,152,24
Dc 243,8,61,2048
K 200,164,165
Mvaa 184,152,40
X 167
Mvaa 184,156,44
G 14,90
Mvaa 188,156,60
X 168
Mvaa 188,152,56
W 2
Dc 243,8,61,3072
K 201,166,167
Mvav 32,128,196
s_add_u32 m0, s62, 8192
Ra 152,242,22528
G 15,91
Mvav 64,128,204
X 169
Mvav 96,128,212
X 170
Mvav 128,128,220
Ra 156,242,23552
A 8,8,12
Mvav 132,132,220
Dc 244,8,61,0
K 208,168,169
Mvav 100,132,212
X 171
Mvav 68,132,204
X 172
Mvav 36,132,196
Ra 128,242,8192
A 9,9,13
Mvav 40,136,196
Dc 244,8,61,1024
K 209,170,171
Mvav 72,136,204
X 173
Mvav 104,136,212
Ra 132,242,9216
A 10,10,14
Mvav 136,136,220
X 174
Mvav 140,140,220
Dc 244,8,61,2048
K 216,172,173
Mvav 108,140,212
X 175
Mvav 76,140,204
Ra 136,242,10240
A 11,11,15
Mvav 44,140,196
W 3
X 176
Mvav 48,144,196
Dc 244,8,61,3072
K 217,174,175
Mvav 80,144,204
s_add_u32 m0, s62, 12288
Ra 140,242,11264
v_add_f32_dpp v8, v8, v8 row_ror:8 row_mask:0xf bank_mask:0xf
Mvav 112,144,212
X 177
Mvav 144,144,220
X 178
Mvav 148,148,220
K 194,176,177
v_add_f32_dpp v9, v9, v9 row_ror:8 row_mask:0xf bank_mask:0xf
Mvav 116,148,212
X 179
Mvav 84,148,204
X 180
Mvav 52,148,196
Dc 245,8,61,0
Ra 144,242,12288
Mvav 56,152,196
v_add_f32_dpp v10, v10, v10 row_ror:8 row_mask:0xf bank_mask:0xf
K 195,178,179
Mvav 88,152,204
X 181
Mvav 120,152,212
X 182
Mvav 152,152,220
Dc 245,8,61,1024
v_add_f32_dpp v11, v11, v11 row_ror:8 row_mask:0xf bank_mask:0xf
Mvav 156,156,220
Ra 148,242,13312
K 202,180,181
Mvav 124,156,212
X 183
Mvav 92,156,204
Ra 152,242,14336
v_mov_b32 v12, v8
Mvav 60,156,196
W 3
Dc 245,8,61,2048
v_mov_b32 v13, v9
Cvaa 160,128,0,224
X 184
Mvaa 160,132,4
Ra 156,242,15360
K 203,182,183
Cvaa 164,132,20,228
X 185
Mvaa 164,128,16
Dc 245,8,61,3072
v_mov_b32 v14, v10
Cvaa 168,128,32,232
s_add_u32 s61, s61, s63
X 186
Mvaa 168,132,36
K 210,184,185
v_mov_b32 v15, v11
Cvaa 172,132,52,236
s_xor_b32 s62, s62, 0x10000
X 187
Mvaa 172,128,48
X 188
Mvaa 160,136,8
Ra 128,242,24576
K 211,186,187
Mvaa 160,140,12
s_add_u32 s64, s64, 1
X 189
Mvaa 164,140,28
X 190
Mvaa 164,136,24
Ra 132,242,25600
v_permlane16_swap_b32 v8, v12
Mvaa 168,136,40
K 218,188,189
v_permlane16_swap_b32 v9, v13
Mvaa 168,140,44
X 191
Mvaa 172,140,60
s_lshl_b32 s92, s58, 2
X 160
Mvaa 172,136,56
W 2
K 219,190,191
v_permlane16_swap_b32 v10, v14
Cvaa 176,144,0,224
X 161
Mvaa 176,148,4
Ra 136,242,26624
Ra 140,242,27648
Cvaa 180,148,20,228
s_add_u32 s91, s92, s51
X 162
Mvaa 180,144,16
K 196,160,161
v_permlane16_swap_b32 v11, v15
Cvaa 184,144,32,232
X 163
Mvaa 184,148,36
s_add_u32 s95, s92, 1
X 164
Cvaa 188,148,52,236
A 8,8,12
K 197,162,163
Mvaa 188,144,48
X 165
Mvaa 176,152,8
s_and_b32 s95, s95, 15
Ra 144,242,28672
Ra 148,242,29696
Mvaa 176,156,12
X 166
Mvaa 180,156,28
A 9,9,13
K 204,164,165
Mvaa 180,152,24
s_mul_i32 s95, s95, 40960
X 167
Mvaa 184,152,40
Mvaa 184,156,44
A 10,10,14
Mvaa 188,156,60
s_mul_i32 s71, s51, 0
X 168
Mvaa 188,152,56
W 2
Ra 152,242,30720
K 205,166,167
Mvav 32,128,192
X 169
Mvav 64,128,200
s_add_u32 s71, s71, 143360
Ra 156,242,31744
A 11,11,15
Mvav 96,128,208
X 170
Mvav 128,128,216
K 212,168,169
v_mov_b32 v12, v8
Mvav 132,132,216
s_add_u32 m0, s71, s48
X 171
Mvav 100,132,208
X 172
Mvav 68,132,200
Mvav 36,132,192
s_lshl2_add_u32 s94, s48, s95
v_mov_b32 v13, v9
K 213,170,171
Mvav 40,136,192
X 173
Mvav 72,136,200
X 174
Mvav 104,136,208
D 16,40,94,0
v_mov_b32 v14, v10
Mvav 136,136,216
K 220,172,173
Mvav 140,140,216
X 175
Mvav 108,140,208
v_mov_b32 v15, v11
Mvav 76,140,200
s_waitcnt vmcnt(1) lgkmcnt(0)
s_barrier
v_xor_b32 v242, 0x10000, v242
Mvav 44,140,192
D 17,40,94,1024
Ra 128,242,0
Mvav 48,144,192
X 176
Mvav 80,144,200
K 221,174,175
v_permlane32_swap_b32 v8, v12
Mvav 112,144,208
X 177
Mvav 144,144,216
D 18,40,94,2048
Ra 132,242,1024
Mvav 148,148,216
X 178
Mvav 116,148,208
K 198,176,177
v_permlane32_swap_b32 v9, v13
Mvav 84,148,200
X 179
Mvav 52,148,192
D 19,40,94,3072
Ra 136,242,2048
Mvav 56,152,192
s_add_u32 m0, m0, 4096
X 180
Mvav 88,152,200
K 199,178,179
v_permlane32_swap_b32 v10, v14
Mvav 120,152,208
X 181
Mvav 152,152,216
Ra 140,242,3072
buffer_load_dwordx4 v20, s[40:43], s94 offen lds
Mvav 156,156,216
X 182
Mvav 124,156,208
K 206,180,181
Mvav 92,156,200
X 183
Mvav 60,156,192
K 207,182,183
W 0
Cvaa 160,128,0,224
Ra 144,242,4096
Ra 148,242,5120
Mvaa 160,132,4
s_add_u32 m0, s62, 0
Ra 152,242,6144
v_permlane32_swap_b32 v11, v15
Cvaa 164,132,20,228
X 184
Mvaa 164,128,16
X 185
Cvaa 168,128,32,232
X 186
Mvaa 168,132,36
A 8,8,12
K 214,184,185
Cvaa 172,132,52,236
Ra 156,242,7168
Dc 1,8,61,0
Mvaa 172,128,48
X 187
Mvaa 160,136,8
Ra 128,242,16384
A 9,9,13
Mvaa 160,140,12
X 188
Mvaa 164,140,28
Dc 1,8,61,1024
K 215,186,187
Mvaa 164,136,24
X 189
Mvaa 168,136,40
Ra 132,242,17408
A 10,10,14
Mvaa 168,140,44
X 190
Mvaa 172,140,60
Dc 1,8,61,2048
K 222,188,189
Mvaa 172,136,56
A 11,11,15
v_smfmac_f32_16x16x64_bf16 a[160:163], a[176:179], v[192:199], v240
X 191
v_smfmac_f32_16x16x64_bf16 a[164:167], a[176:179], v[200:207], v240
K 223,190,191
v_and_b32 v7, 0xffff0000, v7
v_smfmac_f32_16x16x64_bf16 a[168:171], a[176:179], v[208:215], v240
Dc 1,8,61,3072
Ra 136,242,18432
v_smfmac_f32_16x16x64_bf16 a[172:175], a[176:179], v[216:223], v240
W 3
X 160
Cvaa 176,144,0,224
s_add_u32 m0, s62, 4096
X 161
Mvaa 176,148,4
X 162
Cvaa 180,148,20,228
Ra 140,242,19456
v_ashrrev_i32 v6, 31, v7
Mvaa 180,144,16
Dc 243,8,61,0
K 192,160,161
Cvaa 184,144,32,232
X 163
Mvaa 184,148,36
v_not_b32 v6, v6
Cvaa 188,148,52,236
X 164
Mvaa 188,144,48
Dc 243,8,61,1024
K 193,162,163
Mvaa 176,152,8
X 165
Mvaa 176,156,12
Ra 144,242,20480
v_or_b32 v6, 0x80000000, v6
Mvaa 180,156,28
)"
R"(X 166
Mvaa 180,152,24
Ra 148,242,21504
v_xor_b32 v7, v7, v6
Mvaa 184,152,40
Dc 243,8,61,2048
K 200,164,165
Mvaa 184,156,44
X 167
Mvaa 188,156,60
X 168
Mvaa 188,152,56
W 2
v_and_b32 v7, 0xffff0000, v7
K 201,166,167
Mvav 32,128,196
Dc 243,8,61,3072
v_mul_f32 v7, 0xbfb8aa3b, v7
Mvav 64,128,204
s_add_u32 m0, s62, 8192
X 169
Mvav 96,128,212
Ra 152,242,22528
Ra 156,242,23552
Mvav 128,128,220
X 170
Mvav 132,132,220
Dc 244,8,61,0
K 208,168,169
Mvav 100,132,212
X 171
Mvav 68,132,204
X 7
Mvav 36,132,196
X 172
Mvav 40,136,196
Ra 128,242,8192
K 209,170,171
Mvav 72,136,204
X 173
Mvav 104,136,212
Dc 244,8,61,1024
Ra 132,242,9216
Mvav 136,136,220
X 174
Mvav 140,140,220
v_add_f32 v7, 1.0, v7
K 216,172,173
Mvav 108,140,212
X 175
Mvav 76,140,204
Dc 244,8,61,2048
Ra 136,242,10240
Mvav 44,140,196
W 3
v_rcp_f32 v7, v7
Mvav 48,144,196
X 176
Mvav 80,144,204
Ra 140,242,11264
K 217,174,175
Mvav 112,144,212
Dc 244,8,61,3072
K 7,7,7
Mvav 144,144,220
s_add_u32 m0, s62, 12288
X 177
Mvav 148,148,220
X 178
Mvav 116,148,212
Ra 144,242,12288
K 194,176,177
Mvav 84,148,204
X 179
Mvav 52,148,196
X 180
Mvav 56,152,196
Dc 245,8,61,0
v_lshlrev_b32 v7, 16, v7
Mvav 88,152,204
K 195,178,179
K 12,8,9
Mvav 120,152,212
X 181
Mvav 152,152,220
X 182
Mvav 156,156,220
Dc 245,8,61,1024
Ra 148,242,13312
Mvav 124,156,212
K 202,180,181
K 13,10,11
Mvav 92,156,204
X 183
Mvav 60,156,196
W 2
X 184
Cvaa 160,128,0,224
Dc 245,8,61,2048
Ra 152,242,14336
Mvaa 160,132,4
K 203,182,183
v_lshlrev_b32 v8, 16, v12
Cvaa 164,132,20,228
X 185
Mvaa 164,128,16
X 186
Cvaa 168,128,32,232
Dc 245,8,61,3072
Ra 156,242,15360
Mvaa 168,132,36
s_add_u32 s61, s61, s63
v_and_b32 v9, 0xffff0000, v12
K 210,184,185
Cvaa 172,132,52,236
s_xor_b32 s62, s62, 0x10000
X 187
Mvaa 172,128,48
X 188
Mvaa 160,136,8
v_lshlrev_b32 v10, 16, v13
K 211,186,187
Mvaa 160,140,12
s_add_u32 s64, s64, 1
X 189
Mvaa 164,140,28
Ra 128,242,24576
v_and_b32 v11, 0xffff0000, v13
Mvaa 164,136,24
X 190
Mvaa 168,136,40
Ra 132,242,25600
K 218,188,189
Mvaa 168,140,44
X 191
Mvaa 172,140,60
s_lshl_b32 s92, s58, 2
U 8,8,7
U 9,9,7
Mvaa 172,136,56
W 2
X 160
Cvaa 176,144,0,224
Ra 136,242,26624
K 219,190,191
Mvaa 176,148,4
X 161
Cvaa 180,148,20,228
s_add_u32 s91, s92, s51
Ra 140,242,27648
U 10,10,7
Mvaa 180,144,16
X 162
Cvaa 184,144,32,232
K 196,160,161
Mvaa 184,148,36
s_add_u32 s95, s92, 2
X 163
Cvaa 188,148,52,236
U 11,11,7
Mvaa 188,144,48
X 164
Mvaa 176,152,8
s_and_b32 s95, s95, 15
Ra 144,242,28672
K 197,162,163
Mvaa 176,156,12
X 165
Mvaa 180,156,28
Ra 148,242,29696
K 12,8,9
Mvaa 180,152,24
s_mul_i32 s95, s95, 40960
X 166
Mvaa 184,152,40
K 204,164,165
K 14,10,11
Mvaa 184,156,44
X 167
Mvaa 188,156,60
s_mul_i32 s71, s51, 0
X 168
Mvaa 188,152,56
W 2
v_mov_b32 v13, s13
K 205,166,167
Mvav 32,128,192
X 169
Mvav 64,128,200
s_add_u32 s71, s71, 153600
Ra 152,242,30720
Ra 156,242,31744
Mvav 96,128,208
X 170
Mvav 128,128,216
v_mov_b32 v15, s13
K 212,168,169
Mvav 132,132,216
s_add_u32 m0, s71, s48
X 171
Mvav 100,132,208
v_lshrrev_b32 v6, 7, v1
Mvav 68,132,200
X 172
Mvav 36,132,192
s_lshl2_add_u32 s94, s48, s95
K 213,170,171
Mvav 40,136,192
X 173
Mvav 72,136,200
v_lshl_or_b32 v6, v6, 28, v1
Mvav 104,136,208
X 174
Mvav 136,136,216
s_lshl_b32 s19, s85, 7
D 16,40,94,0
K 220,172,173
Mvav 140,140,216
X 175
Mvav 108,140,208
X 176
Mvav 76,140,200
s_waitcnt vmcnt(1) lgkmcnt(0)
s_barrier
v_xor_b32 v242, 0x10000, v242
Mvav 44,140,192
s_add_u32 s19, s19, 98304
D 17,40,94,1024
K 221,174,175
Mvav 48,144,192
Ra 128,242,0
Mvav 80,144,200
s_and_b32 s21, s20, 134217728
X 177
Mvav 112,144,208
X 178
Mvav 144,144,216
D 18,40,94,2048
Ra 132,242,1024
Mvav 148,148,216
s_or_b32 s19, s19, s21
K 198,176,177
Mvav 116,148,208
X 179
Mvav 84,148,200
X 180
Mvav 52,148,192
D 19,40,94,3072
Ra 136,242,2048
Mvav 56,152,192
s_add_u32 m0, m0, 4096
Ra 140,242,3072
K 199,178,179
Mvav 88,152,200
X 181
Mvav 120,152,208
X 182
Mvav 152,152,216
buffer_store_dwordx4 v[12:15], v6, s[8:11], s19 offen sc1
K 206,180,181
Mvav 156,156,216
X 183
Mvav 124,156,208
Mvav 92,156,200
K 207,182,183
Mvav 60,156,192
buffer_load_dwordx4 v20, s[40:43], s94 offen lds
.Lf1_40%=:
W 0
Cvaa 160,128,0,224
v_bfe_u32 v28, v1, 4, 2
v_lshlrev_b32 v28, 3, v28
Mvaa 160,132,4
Ra 144,242,4096
Ra 148,242,5120
Cvaa 164,132,20,228
Ra 152,242,6144
buffer_load_dwordx2 v[28:29], v28, s[8:11], s54 offen sc1
Mvaa 164,128,16
s_lshl_b32 s92, s58, 2
s_add_u32 s91, s92, s51
s_add_u32 s95, s91, 3
Cvaa 168,128,32,232
s_and_b32 s95, s95, 15
s_mul_i32 s95, s95, 40960
s_mul_i32 s71, s51, 10240
Mvaa 168,132,36
s_add_u32 s71, s71, 0
s_add_u32 m0, s71, s48
s_lshl2_add_u32 s94, s48, s95
Cvaa 172,132,52,236
X 184
Mvaa 172,128,48
X 185
Mvaa 160,136,8
X 186
Mvaa 160,140,12
Ra 156,242,7168
K 214,184,185
Mvaa 164,140,28
X 187
Mvaa 164,136,24
D 16,40,94,0
Ra 128,242,16384
Mvaa 168,136,40
s_add_u32 s95, s91, 5
s_and_b32 s95, s95, 15
s_mul_i32 s95, s95, 40960
Mvaa 168,140,44
s_mul_i32 s71, s51, 10240
s_add_u32 s71, s71, 20480
Ra 132,242,17408
Mvaa 172,140,60
X 188
Mvaa 172,136,56
D 17,40,94,1024
K 215,186,187
v_smfmac_f32_16x16x64_bf16 a[160:163], a[176:179], v[192:199], v240
X 189
v_smfmac_f32_16x16x64_bf16 a[164:167], a[176:179], v[200:207], v240
X 190
K 222,188,189
X 191
s_nop 0
K 223,190,191
v_smfmac_f32_16x16x64_bf16 a[168:171], a[176:179], v[208:215], v240
D 18,40,94,2048
Ra 136,242,18432
v_smfmac_f32_16x16x64_bf16 a[172:175], a[176:179], v[216:223], v240
W 3
X 160
Cvaa 176,144,0,224
X 161
Mvaa 176,148,4
X 162
Cvaa 180,148,20,228
D 19,40,94,3072
K 192,160,161
Mvaa 180,144,16
s_add_u32 m0, m0, 4096
X 163
Cvaa 184,144,32,232
X 164
Mvaa 184,148,36
Ra 140,242,19456
K 193,162,163
Cvaa 188,148,52,236
buffer_load_dwordx4 v20, s[40:43], s94 offen lds
Mvaa 188,144,48
s_add_u32 m0, s71, s48
s_lshl2_add_u32 s94, s48, s95
Ra 144,242,20480
Mvaa 176,152,8
X 165
Mvaa 176,156,12
X 166
Mvaa 180,156,28
D 16,40,94,0
K 200,164,165
Mvaa 180,152,24
X 167
Mvaa 184,152,40
X 172
Mvaa 184,156,44
D 17,40,94,1024
K 201,166,167
Mvaa 188,156,60
X 173
Mvaa 188,152,56
s_cmp_eq_u32 s60, 16
s_cbranch_scc1 .Lmk_79%=
.Lmkr_80%=:
W 1
Mvav 32,128,196
Ra 148,242,21504
D 18,40,94,2048
Mvav 64,128,204
X 174
Mvav 96,128,212
Ra 152,242,22528
K 216,172,173
Mvav 128,128,220
X 175
Mvav 132,132,220
s_mov_b32 s18, 40960
X 168
Mvav 100,132,212
D 19,40,94,3072
K 217,174,175
Mvav 68,132,204
s_add_u32 m0, m0, 4096
X 169
Mvav 36,132,196
s_mov_b32 s19, 73728
X 170
Mvav 40,136,196
s_bitcmp1_b32 s14, 0
s_cselect_b32 s18, 49152, s18
s_cselect_b32 s19, 98304, s19
Ra 156,242,23552
)"
R"(K 208,168,169
Mvav 72,136,204
s_cmp_lt_u32 s14, 2
s_cselect_b32 s20, s18, s19
s_cselect_b32 s21, s19, s18
X 171
Mvav 104,136,212
X 184
Mvav 136,136,220
buffer_load_dwordx4 v20, s[40:43], s94 offen lds
K 209,170,171
Mvav 140,140,220
s_waitcnt vmcnt(10)
v_cmp_eq_u64 vcc, s[12:13], v[28:29]
s_cmp_eq_u64 vcc, -1
s_cselect_b32 s46, 79691776, 0
s_add_u32 s81, s58, 0
s_and_b32 s81, s81, 3
Mvav 108,140,212
s_lshl_b32 s81, s81, 12
s_add_u32 s81, s81, s49
Ra 128,242,24576
Mvav 76,140,204
X 185
Mvav 44,140,196
W 1
X 186
Mvav 48,144,196
buffer_load_dwordx4 a[184:187], v1, s[44:47], s81 offen offset:0 sc1
K 210,184,185
Mvav 80,144,204
s_add_u32 s82, s81, 131072
X 187
Mvav 112,144,212
v_add_u32 v2, s20, v1
v_add_u32 v3, s21, v1
Mvav 144,144,220
s_lshl_b32 s18, s14, 9
X 188
Mvav 148,148,220
Ra 132,242,25600
K 211,186,187
Mvav 116,148,212
buffer_load_dwordx4 a[200:203], v1, s[44:47], s82 offen offset:0 sc1
Ra 136,242,26624
Mvav 84,148,204
X 189
Mvav 52,148,196
v_lshrrev_b32 v232, 1, v1
v_add_u32 v232, s18, v232
Mvav 56,152,196
X 190
Mvav 88,152,204
K 218,188,189
v_bfe_u32 v12, v1, 6, 2
Mvav 120,152,212
buffer_load_dwordx4 a[188:191], v1, s[44:47], s81 offen offset:1024 sc1
Ra 140,242,27648
Mvav 152,152,220
X 191
Mvav 156,156,220
v_lshrrev_b32 v13, 8, v1
v_bfe_u32 v14, v1, 4, 1
Mvav 124,156,212
X 176
Mvav 92,156,204
K 219,190,191
v_lshlrev_b32 v14, 4, v14
Mvav 60,156,196
X 177
Maav 168,180,208
X 178
K 194,176,177
X 179
X 180
K 195,178,179
Maav 172,180,216
X 181
X 182
K 202,180,181
X 183
s_nop 0
K 203,182,183
Maav 160,180,192
buffer_load_dwordx4 a[204:207], v1, s[44:47], s82 offen offset:1024 sc1
v_mov_b32 v15, 0x3f80
Maav 164,180,200
W 0
Ra 144,242,28672
v_lshlrev_b32 v15, v14, v15
Mvav 96,128,208
Ra 148,242,29696
v_bfe_u32 v14, v1, 5, 1
v_xor_b32 v27, v12, v13
Mvav 128,128,216
buffer_load_dwordx4 a[192:195], v1, s[44:47], s81 offen offset:2048 sc1
v_lshl_or_b32 v27, v27, 1, v14
v_cmp_eq_u32_e64 s[86:87], 0, v27
Mvav 132,132,216
Ra 152,242,30720
Ra 156,242,31744
v_mov_b32 v226, 0
Mvav 100,132,208
v_cndmask_b32_e64 v224, 0, v15, s[86:87]
v_mov_b32 v228, 0
v_mov_b32 v230, v224
Mvav 104,136,208
buffer_load_dwordx4 a[208:211], v1, s[44:47], s82 offen offset:2048 sc1
v_cmp_eq_u32_e64 s[86:87], 1, v27
v_mov_b32 v227, 0
Mvav 136,136,216
v_cndmask_b32_e64 v225, 0, v15, s[86:87]
v_mov_b32 v229, 0
v_mov_b32 v231, v225
Mvav 140,140,216
G 4,160
G 8,161
G 5,164
Mvav 108,140,208
W 0
buffer_load_dwordx4 a[196:199], v1, s[44:47], s81 offen offset:3072 sc1
G 9,165
G 6,168
Mvav 112,144,208
G 10,169
G 7,172
G 11,173
Mvav 144,144,216
A 4,4,8
A 5,5,9
A 6,6,10
Mvav 148,148,216
buffer_load_dwordx4 a[212:215], v1, s[44:47], s82 offen offset:3072 sc1
s_add_u32 s81, s58, 1
s_and_b32 s81, s81, 3
s_lshl_b32 s81, s81, 12
Mvav 116,148,208
s_add_u32 s81, s81, s49
A 7,7,11
ds_write_b64 v232, v[6:7] offset:57344
A 8,4,5
Mvav 120,152,208
A 9,6,7
A 8,8,9
v_cmp_ngt_f32_e64 s[86:87], s90, v8
s_lshl_b32 s19, s14, 2
Mvav 152,152,216
buffer_load_dwordx4 a[216:219], v1, s[44:47], s81 offen offset:0 sc1
s_add_u32 s82, s81, 131072
v_mov_b32 v9, s19
s_or_b64 s[88:89], s[88:89], s[86:87]
Mvav 156,156,216
s_and_b64 s[88:89], s[88:89], s[24:25]
s_cselect_b32 s18, 1, 0
v_mov_b32 v8, s18
ds_write_b32 v9, v8 offset:59392
v_xor_b32 v10, 1024, v232
Mvav 124,156,208
v_bfe_u32 v11, v1, 2, 4
K 96,96,97
K 97,98,99
Mvav 32,128,192
buffer_load_dwordx4 a[232:235], v1, s[44:47], s82 offen offset:0 sc1
K 98,100,101
K 99,102,103
Mvav 64,128,200
ds_write_b128 v2, v[96:99] offset:0
K 128,128,129
K 129,130,131
Mvav 68,132,200
K 130,132,133
K 131,134,135
ds_write_b128 v2, v[128:131] offset:4096
Mvav 36,132,192
buffer_load_dwordx4 a[220:223], v1, s[44:47], s81 offen offset:1024 sc1
K 104,104,105
K 105,106,107
Mvav 40,136,192
K 106,108,109
K 107,110,111
ds_write_b128 v2, v[104:107] offset:1024
Mvav 72,136,200
K 136,136,137
K 137,138,139
K 138,140,141
Mvav 76,140,200
buffer_load_dwordx4 a[236:239], v1, s[44:47], s82 offen offset:1024 sc1
K 139,142,143
ds_write_b128 v2, v[136:139] offset:5120
Mvav 44,140,192
K 112,112,113
K 113,114,115
K 114,116,117
Mvav 48,144,192
K 115,118,119
ds_write_b128 v2, v[112:115] offset:2048
K 144,144,145
Mvav 80,144,200
K 145,146,147
K 146,148,149
K 147,150,151
Mvav 84,148,200
ds_write_b128 v2, v[144:147] offset:6144
buffer_load_dwordx4 a[224:227], v1, s[44:47], s81 offen offset:2048 sc1
K 120,120,121
Mvav 52,148,192
K 121,122,123
K 122,124,125
K 123,126,127
Mvav 56,152,192
ds_write_b128 v2, v[120:123] offset:3072
K 152,152,153
K 153,154,155
Mvav 88,152,200
buffer_load_dwordx4 a[240:243], v1, s[44:47], s82 offen offset:2048 sc1
K 154,156,157
K 155,158,159
Mvav 92,156,200
ds_write_b128 v2, v[152:155] offset:7168
Mvav 60,156,192
buffer_load_dwordx4 a[228:231], v1, s[44:47], s81 offen offset:3072 sc1
buffer_load_dwordx4 a[244:247], v1, s[44:47], s82 offen offset:3072 sc1
s_branch .LL_81%=
.p2align 12
.LL_81%=:
.Lend_43%=:
s_waitcnt vmcnt(8) lgkmcnt(0)
s_barrier
s_cmp_eq_u32 s46, 0
s_cbranch_scc1 .Llate_82%=
ds_read_b32 v11, v11 offset:59392
ds_read_b64 v[8:9], v10 offset:57344
Rv 176,3,0
Rv 180,3,1024
Rv 184,3,2048
Rv 188,3,3072
Rv 192,3,4096
Rv 196,3,5120
Rv 200,3,6144
Rv 204,3,7168
W 6
Mvvv 32,224,176
s_lshl_b32 s92, s58, 2
s_add_u32 s91, s92, s51
s_add_u32 s95, s91, 7
Mvvv 36,228,176
s_and_b32 s95, s95, 15
s_mul_i32 s95, s95, 40960
s_mul_i32 s71, s51, 20480
Mvvv 40,224,180
s_add_u32 s71, s71, 61440
s_add_u32 m0, s71, s48
s_lshl2_add_u32 s94, s48, s95
Mvvv 44,228,180
W 4
v_cmp_ne_u32 vcc, 0, v11
s_cmp_lg_u64 vcc, 0
s_cbranch_scc1 .Lredo%=
A 4,4,8
Mvvv 48,224,184
v_rcp_f32 v4, v4
Mvvv 52,228,184
Ta 128,25,10240
A 5,5,9
Mvvv 56,224,188
Ta 130,25,15360
D 16,40,94,0
Mvvv 60,228,188
W 4
s_add_u32 s95, s91, 9
s_and_b32 s95, s95, 15
Mvvv 64,224,192
s_mul_i32 s95, s95, 40960
s_mul_i32 s71, s51, 10240
s_add_u32 s71, s71, 112640
Mvvv 68,228,192
v_rcp_f32 v5, v5
Mvvv 72,224,196
s_lshl_b32 s93, s58, 12
s_add_u32 s93, s93, s50
Ta 132,25,20480
Mvvv 76,228,196
Ta 134,25,25600
W 4
Mvvv 80,224,200
U 32,32,4
U 33,33,4
Mvvv 84,228,200
Ta 136,26,10272
U 34,34,4
Mvvv 88,224,204
Ta 138,26,15392
U 35,35,4
Mvvv 92,228,204
Ta 140,26,20512
Ta 142,26,25632
W 4
Zvaa 96,128,184
U 36,36,4
U 37,37,4
Mvaa 96,132,188
D 17,40,94,1024
U 38,38,4
Zvaa 104,132,204
U 39,39,4
K 32,32,33
Mvaa 104,128,200
Ta 144,25,10304
Ta 146,25,15424
Ta 148,25,20544
Ta 150,25,25664
W 4
Zvaa 108,136,200
K 33,34,35
K 34,36,37
Mvaa 108,140,204
Ta 152,26,10336
K 35,38,39
Zvaa 100,140,188
buffer_store_dwordx4 v[32:35], v1, s[8:11], s93 offen offset:0 sc1
U 40,40,4
Mvaa 100,136,184
Ta 154,26,15456
Ta 156,26,20576
Ta 158,26,25696
W 4
Zvaa 112,144,184
U 41,41,4
U 42,42,4
Mvaa 112,148,188
U 43,43,4
U 44,44,4
Zvaa 120,148,204
D 18,40,94,2048
U 45,45,4
Mvaa 120,144,200
Ta 128,25,10368
Ta 130,25,15488
Ta 132,25,20608
Ta 134,25,25728
W 4
Zvaa 124,152,200
U 46,46,4
U 47,47,4
Mvaa 124,156,204
K 40,40,41
K 41,42,43
Zvaa 116,156,188
K 42,44,45
K 43,46,47
Mvaa 116,152,184
Ta 136,26,10400
Ta 138,26,15520
Ta 140,26,20640
Ta 142,26,25760
W 4
Zvaa 128,128,184
buffer_store_dwordx4 v[40:43], v1, s[8:11], s93 offen offset:1024 sc1
U 48,48,4
Mvaa 128,132,188
U 49,49,4
U 50,50,4
Zvaa 136,132,204
U 51,51,4
U 52,52,4
Mvaa 136,128,200
Ta 144,25,10432
Ta 146,25,15552
Ta 148,25,20672
Ta 150,25,25792
W 4
Zvaa 140,136,200
D 19,40,94,3072
U 53,53,4
Mvaa 140,140,204
s_add_u32 m0, m0, 4096
U 54,54,4
U 55,55,4
Zvaa 132,140,188
K 48,48,49
K 49,50,51
Mvaa 132,136,184
Ta 152,26,10464
Ta 154,26,15584
Ta 156,26,20704
Ta 158,26,25824
W 4
Zvaa 144,144,184
K 50,52,53
K 51,54,55
Mvaa 144,148,188
buffer_load_dwordx4 v20, s[40:43], s94 offen lds
Ta 128,25,10496
Zvaa 152,148,204
s_add_u32 m0, s71, s48
s_lshl2_add_u32 s94, s48, s95
Ta 130,25,15616
Mvaa 152,144,200
Ta 132,25,20736
Ta 134,25,25856
)"
R"(W 4
Zvaa 156,152,200
Ta 136,26,10528
Ta 138,26,15648
Mvaa 156,156,204
buffer_store_dwordx4 v[48:51], v1, s[8:11], s93 offen offset:2048 sc1
U 56,56,4
Zvaa 148,156,188
U 57,57,4
U 58,58,4
Mvaa 148,152,184
Ta 140,26,20768
Ta 142,26,25888
W 4
Zvaa 160,128,184
U 59,59,4
U 60,60,4
Mvaa 160,132,188
U 61,61,4
U 62,62,4
Zvaa 168,132,204
U 63,63,4
K 56,56,57
Mvaa 168,128,200
Ta 144,25,30720
Ta 146,25,35840
Ta 148,21,0
Ta 150,21,5120
W 4
Zvaa 172,136,200
D 16,40,94,0
K 57,58,59
Mvaa 172,140,204
K 58,60,61
K 59,62,63
Zvaa 164,140,188
Ta 152,26,30752
Ta 154,26,35872
Mvaa 164,136,184
Ta 156,22,32
Ta 158,22,5152
W 4
Mvaa 96,144,192
buffer_store_dwordx4 v[56:59], v1, s[8:11], s93 offen offset:3072 sc1
U 64,64,5
Mvaa 96,148,196
U 65,65,5
U 66,66,5
Mvaa 104,148,212
U 67,67,5
U 68,68,5
Mvaa 104,144,208
Ta 128,25,30784
Ta 130,25,35904
Ta 132,21,64
Ta 134,21,5184
W 4
Mvaa 108,152,208
D 17,40,94,1024
U 69,69,5
Mvaa 108,156,212
U 70,70,5
U 71,71,5
Mvaa 100,156,196
K 64,64,65
K 65,66,67
Mvaa 100,152,192
Ta 136,26,30816
Ta 138,26,35936
Ta 140,22,96
Ta 142,22,5216
W 4
Mvaa 112,128,192
K 66,68,69
K 67,70,71
Mvaa 112,132,196
s_add_u32 s67, s93, 131072
buffer_store_dwordx4 v[64:67], v1, s[8:11], s67 offen offset:0 sc1
U 72,72,5
Mvaa 120,132,212
U 73,73,5
U 74,74,5
Mvaa 120,128,208
Ta 144,25,30848
Ta 146,25,35968
Ta 148,21,128
Ta 150,21,5248
W 4
Mvaa 124,136,208
U 75,75,5
U 76,76,5
Mvaa 124,140,212
D 18,40,94,2048
U 77,77,5
Mvaa 116,140,196
U 78,78,5
U 79,79,5
Mvaa 116,136,192
Ta 152,26,30880
Ta 154,26,36000
Ta 156,22,160
Ta 158,22,5280
W 4
Mvaa 128,144,192
K 72,72,73
K 73,74,75
Mvaa 128,148,196
K 74,76,77
K 75,78,79
Mvaa 136,148,212
buffer_store_dwordx4 v[72:75], v1, s[8:11], s67 offen offset:1024 sc1
U 80,80,5
Mvaa 136,144,208
Ta 128,25,30912
Ta 130,25,36032
Ta 132,21,192
Ta 134,21,5312
W 4
Mvaa 140,152,208
D 19,40,94,3072
s_add_u32 m0, m0, 4096
s_nop 0
buffer_load_dwordx4 v20, s[40:43], s94 offen lds
Mvaa 140,156,212
s_add_u32 s81, s58, 2
s_and_b32 s81, s81, 3
s_lshl_b32 s81, s81, 12
Mvaa 132,156,196
s_add_u32 s81, s81, s49
U 81,81,5
U 82,82,5
Mvaa 132,152,192
Ta 136,26,30944
Ta 138,26,36064
Ta 140,22,224
Ta 142,22,5344
W 4
Mvaa 144,128,192
U 83,83,5
U 84,84,5
Mvaa 144,132,196
U 85,85,5
U 86,86,5
Mvaa 152,132,212
U 87,87,5
K 80,80,81
Mvaa 152,128,208
Ta 144,25,30976
Ta 146,25,36096
Ta 148,21,256
Ta 150,21,5376
W 4
Mvaa 156,136,208
K 81,82,83
K 82,84,85
Mvaa 156,140,212
buffer_load_dwordx4 a[0:3], v1, s[8:11], s81 offen offset:0 sc1
K 83,86,87
Mvaa 148,140,196
s_add_u32 s82, s81, 131072
Ta 152,26,31008
Ta 154,26,36128
Mvaa 148,136,192
Ta 156,22,288
Ta 158,22,5408
W 4
Mvaa 160,144,192
Ta 128,21,10240
Ta 130,21,15360
Mvaa 160,148,196
buffer_store_dwordx4 v[80:83], v1, s[8:11], s67 offen offset:2048 sc1
U 88,88,5
Mvaa 168,148,212
U 89,89,5
U 90,90,5
Mvaa 168,144,208
Ta 132,21,20480
Ta 134,21,25600
W 4
Mvaa 172,152,208
U 91,91,5
U 92,92,5
Mvaa 172,156,212
U 93,93,5
U 94,94,5
Mvaa 164,156,196
U 95,95,5
K 88,88,89
Mvaa 164,152,192
Ta 136,22,10272
Ta 138,22,15392
Ta 140,22,20512
Ta 142,22,25632
s_waitcnt vmcnt(22) lgkmcnt(4)
Mvaa 96,128,216
K 89,90,91
K 90,92,93
Mvaa 96,132,220
buffer_load_dwordx4 a[16:19], v1, s[8:11], s82 offen offset:0 sc1
K 91,94,95
Mvaa 104,132,236
Ta 144,21,10304
Ta 146,21,15424
Mvaa 104,128,232
Ta 148,21,20544
Ta 150,21,25664
W 4
Mvaa 108,136,232
Ta 152,22,10336
Ta 154,22,15456
Mvaa 108,140,236
buffer_store_dwordx4 v[88:91], v1, s[8:11], s67 offen offset:3072 sc1
Ta 156,22,20576
Mvaa 100,140,220
Ta 158,22,25696
Ta 128,21,10368
Mvaa 100,136,216
W 5
Ta 130,21,15488
Ta 132,21,20608
Mvaa 112,144,216
Ta 134,21,25728
Ta 136,22,10400
Mvaa 112,148,220
buffer_load_dwordx4 a[4:7], v1, s[8:11], s81 offen offset:1024 sc1
Ta 138,22,15520
Mvaa 120,148,236
Ta 140,22,20640
Ta 142,22,25760
Mvaa 120,144,232
W 8
Ta 144,21,10432
Ta 146,21,15552
Mvaa 124,152,232
Mvaa 124,156,236
buffer_load_dwordx4 a[20:23], v1, s[8:11], s82 offen offset:1024 sc1
Ta 148,21,20672
Mvaa 116,156,220
Ta 150,21,25792
Mvaa 116,152,216
W 8
Ta 152,22,10464
Ta 154,22,15584
Mvaa 128,128,216
Mvaa 128,132,220
buffer_load_dwordx4 a[8:11], v1, s[8:11], s81 offen offset:2048 sc1
Ta 156,22,20704
Mvaa 136,132,236
Ta 158,22,25824
Mvaa 136,128,232
W 8
Ta 128,21,10496
Ta 130,21,15616
Mvaa 140,136,232
s_waitcnt vmcnt(7)
s_barrier
Mvaa 140,140,236
buffer_load_dwordx4 a[24:27], v1, s[8:11], s82 offen offset:2048 sc1
Ta 132,21,20736
Mvaa 132,140,220
Ta 134,21,25856
Mvaa 132,136,216
W 8
Ta 136,22,10528
Ta 138,22,15648
Mvaa 144,144,216
Mvaa 144,148,220
buffer_load_dwordx4 a[12:15], v1, s[8:11], s81 offen offset:3072 sc1
Ta 140,22,20768
Mvaa 152,148,236
Ta 142,22,25888
Mvaa 152,144,232
W 8
Ta 144,21,30720
Ta 146,21,35840
Mvaa 156,152,232
Mvaa 156,156,236
buffer_load_dwordx4 a[28:31], v1, s[8:11], s82 offen offset:3072 sc1
Ta 148,23,0
Mvaa 148,156,220
s_lshl_b32 s92, s58, 2
s_add_u32 s91, s92, s51
s_add_u32 s95, s91, 11
Mvaa 148,152,216
W 7
s_and_b32 s95, s95, 15
s_mul_i32 s95, s95, 40960
Mvaa 160,128,216
s_mul_i32 s71, s51, 10240
s_add_u32 s71, s71, 40960
s_add_u32 m0, s71, s48
Mvaa 160,132,220
s_lshl2_add_u32 s94, s48, s95
D 16,40,94,0
Ta 150,23,5120
Mvaa 168,132,236
s_add_u32 s95, s91, 13
s_and_b32 s95, s95, 15
s_mul_i32 s95, s95, 40960
Mvaa 168,128,232
W 4
s_mul_i32 s71, s51, 20480
s_add_u32 s71, s71, 71680
Mvaa 172,136,232
Ta 152,22,30752
Ta 154,22,35872
Mvaa 172,140,236
D 17,40,94,1024
Ta 156,24,32
Mvaa 164,140,220
Ta 158,24,5152
Ta 128,21,30784
Mvaa 164,136,216
W 5
Ta 130,21,35904
Ta 132,23,64
Mvaa 96,144,224
Ta 134,23,5184
Ta 136,22,30816
Mvaa 96,148,228
D 18,40,94,2048
Ta 138,22,35936
Mvaa 104,148,244
Ta 140,24,96
Ta 142,24,5216
Mvaa 104,144,240
W 8
Ta 144,21,30848
Ta 146,21,35968
Mvaa 108,152,240
Mvaa 108,156,244
D 19,40,94,3072
Ta 148,23,128
Mvaa 100,156,228
s_add_u32 m0, m0, 4096
Ta 150,23,5248
Mvaa 100,152,224
W 8
Ta 152,22,30880
Ta 154,22,36000
Mvaa 112,128,224
Mvaa 112,132,228
buffer_load_dwordx4 v20, s[40:43], s94 offen lds
Ta 156,24,160
Mvaa 120,132,244
s_add_u32 m0, s71, s48
s_lshl2_add_u32 s94, s48, s95
Ta 158,24,5280
Mvaa 120,128,240
W 8
Ta 128,21,30912
Ta 130,21,36032
Mvaa 124,136,240
Mvaa 124,140,244
D 16,40,94,0
Ta 132,23,192
Mvaa 116,140,228
s_add_u32 s95, s92, 15
s_and_b32 s95, s95, 15
s_mul_i32 s95, s95, 40960
Mvaa 116,136,224
W 7
s_mul_i32 s71, s51, 0
s_add_u32 s71, s71, 102400
Mvaa 128,144,224
Ta 134,23,5312
Ta 136,22,30944
Mvaa 128,148,228
D 17,40,94,1024
Ta 138,22,36064
Mvaa 136,148,244
Ta 140,24,224
Ta 142,24,5344
Mvaa 136,144,240
W 8
Ta 144,21,30976
Ta 146,21,36096
Mvaa 140,152,240
Mvaa 140,156,244
D 18,40,94,2048
Ta 148,23,256
Mvaa 132,156,228
Ta 150,23,5376
Mvaa 132,152,224
W 8
Ta 152,22,31008
Ta 154,22,36128
Mvaa 144,128,224
s_lshl_b32 s80, s58, 3
s_add_u32 s80, s80, s55
Mvaa 144,132,228
D 19,40,94,3072
Ta 156,24,288
Mvaa 152,132,244
s_add_u32 m0, m0, 4096
Ta 158,24,5408
Mvaa 152,128,240
W 8
Ta 128,23,20480
Ta 130,23,25600
)"
R"(Mvaa 156,136,240
s_waitcnt vmcnt(15)
v_mov_b64 v[28:29], s[12:13]
s_mov_b64 exec, 1
buffer_store_dwordx2 v[28:29], off, s[8:11], s80 sc1
s_mov_b64 exec, -1
Mvaa 156,140,244
Ta 132,23,51200
Ta 134,23,56320
Mvaa 148,140,228
Mvaa 148,136,224
W 8
Ta 136,24,20512
Ta 138,24,25632
Mvaa 160,144,224
buffer_load_dwordx4 v20, s[40:43], s94 offen lds
Mvaa 160,148,228
s_add_u32 m0, s71, s48
s_lshl2_add_u32 s94, s48, s95
Ta 140,24,51232
Mvaa 168,148,244
Ta 142,24,56352
Mvaa 168,144,240
W 8
Ta 144,23,20544
Ta 146,23,25664
Mvaa 172,152,240
D 16,40,94,0
Mvaa 172,156,244
Ta 148,23,51264
Ta 150,23,56384
Mvaa 164,156,228
Mvaa 164,152,224
s_waitcnt vmcnt(16) lgkmcnt(8)
Ta 152,24,20576
Ta 154,24,25696
Mvaa 96,128,0
D 17,40,94,1024
Mvaa 96,132,4
Ta 156,24,51296
Ta 158,24,56416
Mvaa 104,132,20
Mvaa 104,128,16
W 8
Ta 128,23,20608
Ta 130,23,25728
Mvaa 108,136,16
D 18,40,94,2048
Mvaa 108,140,20
Ta 132,23,51328
Ta 134,23,56448
Mvaa 100,140,4
Mvaa 100,136,0
W 8
Ta 136,24,20640
Ta 138,24,25760
Mvaa 112,144,0
D 19,40,94,3072
Mvaa 112,148,4
s_add_u32 m0, m0, 4096
Ta 140,24,51360
Ta 142,24,56480
Mvaa 120,148,20
Mvaa 120,144,16
W 8
Ta 144,23,20672
Ta 146,23,25792
Mvaa 124,152,16
buffer_load_dwordx4 v20, s[40:43], s94 offen lds
Mvaa 124,156,20
s_add_u32 s81, s58, 3
s_and_b32 s81, s81, 3
s_lshl_b32 s81, s81, 12
Mvaa 116,156,4
s_add_u32 s81, s81, s49
Ta 148,23,51392
Ta 150,23,56512
Mvaa 116,152,0
W 8
Ta 152,24,20704
Ta 154,24,25824
Mvaa 128,128,0
buffer_load_dwordx4 a[32:35], v1, s[8:11], s81 offen offset:0 sc1
Mvaa 128,132,4
s_add_u32 s82, s81, 131072
Ta 156,24,51424
Ta 158,24,56544
Mvaa 136,132,20
Mvaa 136,128,16
W 8
Ta 128,23,20736
Ta 130,23,25856
Mvaa 140,136,16
s_waitcnt vmcnt(12)
s_barrier
buffer_load_dwordx4 a[48:51], v1, s[8:11], s82 offen offset:0 sc1
Mvaa 140,140,20
Ta 132,23,51456
v_and_b32 v27, 1, v12
Mvaa 132,140,4
Ta 134,23,56576
Mvaa 132,136,0
W 8
Ta 136,24,20768
v_lshlrev_b32 v27, 1, v27
Mvaa 144,144,0
buffer_load_dwordx4 a[36:39], v1, s[8:11], s81 offen offset:1024 sc1
Ta 138,24,25888
Mvaa 144,148,4
Ta 140,24,51488
v_xor_b32 v27, v27, v13
Mvaa 152,148,20
Ta 142,24,56608
Mvaa 152,144,16
W 8
Ta 144,25,0
v_and_b32 v12, 2, v12
Mvaa 156,152,16
buffer_load_dwordx4 a[52:55], v1, s[8:11], s82 offen offset:1024 sc1
Ta 146,25,5120
Mvaa 156,156,20
Ta 148,21,40960
Ta 150,21,46080
Mvaa 148,156,4
v_or_b32 v12, v12, v14
Mvaa 148,152,0
W 8
Ta 152,26,32
Ta 154,26,5152
Mvaa 160,128,0
buffer_load_dwordx4 a[40:43], v1, s[8:11], s81 offen offset:2048 sc1
v_lshl_or_b32 v12, v27, 2, v12
Mvaa 160,132,4
Ta 156,22,40992
Ta 158,22,46112
Mvaa 168,132,20
v_cmp_eq_u32_e64 s[86:87], 0, v12
Mvaa 168,128,16
W 8
Ta 128,25,64
Ta 130,25,5184
Mvaa 172,136,16
buffer_load_dwordx4 a[56:59], v1, s[8:11], s82 offen offset:2048 sc1
v_cndmask_b32_e64 v4, 0, v15, s[86:87]
Mvaa 172,140,20
Ta 132,21,41024
Ta 134,21,46144
Mvaa 164,140,4
Mvaa 164,136,0
W 8
Ta 136,26,96
v_cmp_eq_u32_e64 s[86:87], 1, v12
Mvaa 96,144,8
buffer_load_dwordx4 a[44:47], v1, s[8:11], s81 offen offset:3072 sc1
Ta 138,26,5216
Mvaa 96,148,12
Ta 140,22,41056
v_cndmask_b32_e64 v5, 0, v15, s[86:87]
Mvaa 104,148,28
Ta 142,22,46176
Mvaa 104,144,24
W 8
Ta 144,25,128
v_cmp_eq_u32_e64 s[86:87], 2, v12
Mvaa 108,152,24
buffer_load_dwordx4 a[60:63], v1, s[8:11], s82 offen offset:3072 sc1
Ta 146,25,5248
Mvaa 108,156,28
Ta 148,21,41088
Ta 150,21,46208
Mvaa 100,156,12
v_cndmask_b32_e64 v6, 0, v15, s[86:87]
Mvaa 100,152,8
W 8
Ta 152,26,160
Ta 154,26,5280
Mvaa 112,128,8
v_cmp_eq_u32_e64 s[86:87], 3, v12
Mvaa 112,132,12
Ta 156,22,41120
Ta 158,22,46240
Mvaa 120,132,28
v_cndmask_b32_e64 v7, 0, v15, s[86:87]
Mvaa 120,128,24
W 8
Ta 128,25,192
Ta 130,25,5312
Mvaa 124,136,24
v_cmp_eq_u32_e64 s[86:87], 4, v12
Mvaa 124,140,28
Ta 132,21,41152
Ta 134,21,46272
Mvaa 116,140,12
Mvaa 116,136,8
W 8
Ta 136,26,224
v_cndmask_b32_e64 v8, 0, v15, s[86:87]
Mvaa 128,144,8
Ta 138,26,5344
Mvaa 128,148,12
Ta 140,22,41184
v_cmp_eq_u32_e64 s[86:87], 5, v12
Mvaa 136,148,28
Ta 142,22,46304
Mvaa 136,144,24
W 8
Ta 144,25,256
v_cndmask_b32_e64 v9, 0, v15, s[86:87]
Mvaa 140,152,24
s_waitcnt vmcnt(8)
s_barrier
Ta 146,25,5376
Mvaa 140,156,28
Ta 148,21,41216
v_cmp_eq_u32_e64 s[86:87], 6, v12
Mvaa 132,156,12
Ta 150,21,46336
Mvaa 132,152,8
W 8
Ta 152,26,288
Ta 154,26,5408
Mvaa 144,128,8
v_cndmask_b32_e64 v10, 0, v15, s[86:87]
Mvaa 144,132,12
Ta 156,22,41248
Ta 158,22,46368
Mvaa 152,132,28
v_cmp_eq_u32_e64 s[86:87], 7, v12
Mvaa 152,128,24
W 8
Ta 128,21,51200
Ta 130,21,56320
Mvaa 156,136,24
v_cndmask_b32_e64 v11, 0, v15, s[86:87]
Mvaa 156,140,28
Ta 132,23,10240
Ta 134,23,15360
Mvaa 148,140,12
Mvaa 148,136,8
W 8
s_mov_b32 s20, s38
Ta 136,23,30720
Mvaa 160,144,8
Ta 138,23,35840
v_bfe_u32 v28, v1, 4, 2
Mvaa 160,148,12
s_and_b32 s21, s39, 0xffff
Ta 140,23,40960
v_lshlrev_b32 v28, 3, v28
Mvaa 168,148,28
Ta 142,23,46080
buffer_load_dwordx2 v[28:29], v28, s[8:11], s55 offen sc1
Mvaa 168,144,24
W 8
s_mov_b32 s22, 2621440
Ta 144,22,51232
Mvaa 172,152,24
Ta 146,22,56352
Mvaa 172,156,28
s_mov_b32 s23, 0x20000
Ta 148,24,10272
Ta 150,24,15392
Mvaa 164,156,12
Mvaa 164,152,8
s_waitcnt vmcnt(5) lgkmcnt(8)
Ta 152,24,30752
Ta 154,24,35872
Mvaa 96,128,32
s_and_b32 s17, s17, 0xffff
Mvaa 96,132,36
Ta 156,24,40992
Ta 158,24,46112
Mvaa 104,132,52
s_mov_b32 s18, 2621440
Mvaa 104,128,48
s_waitcnt vmcnt(1) lgkmcnt(8)
Ta 128,21,51264
Ta 130,21,56384
Mvaa 104,136,56
s_mov_b32 s19, 0x20000
Mvaa 104,140,60
Ta 132,23,10304
Ta 134,23,15424
Mvaa 96,140,44
Mvaa 96,136,40
W 8
s_lshr_b32 s66, s15, 3
Ta 136,23,30784
Mvaa 100,144,32
Ta 138,23,35904
Mvaa 100,148,36
s_lshl_b32 s66, s66, 4
Ta 140,23,41024
Ta 142,23,46144
Mvaa 108,148,52
Mvaa 108,144,48
W 8
s_add_u32 s66, s66, s59
Ta 144,22,51296
Mvaa 108,152,56
Ta 146,22,56416
Mvaa 108,156,60
s_lshl_b32 s63, s66, 6
Ta 148,24,10336
Ta 150,24,15456
Mvaa 100,156,44
Mvaa 100,152,40
W 8
Ta 152,24,30816
Ta 154,24,35936
Mvaa 112,128,32
s_add_u32 s63, s63, 65536
Mvaa 112,132,36
Ta 156,24,41056
Ta 158,24,46176
Mvaa 120,132,52
s_mul_i32 s62, s66, 40960
K 96,96,97
K 97,98,99
Mvaa 120,128,48
W 8
Ta 128,21,51328
Ta 130,21,56448
Mvaa 120,136,56
s_add_u32 s62, s62, 56623104
K 98,100,101
Mvaa 120,140,60
Ta 132,23,10368
Ta 134,23,15488
Mvaa 112,140,44
s_lshl_b32 s66, s14, 13
K 99,102,103
Mvaa 112,136,40
W 8
s_add_u32 s83, s52, 0
Ta 136,23,30848
Mvaa 116,144,32
Ta 138,23,35968
Mvaa 116,148,36
s_add_u32 s78, s62, s66
buffer_store_dwordx4 v[96:99], v1, s[8:11], s83 offen sc1
Ta 140,23,41088
Mvaa 124,148,52
Ta 142,23,46208
Mvaa 124,144,48
W 8
s_add_u32 s79, s78, 327680
K 104,104,105
Mvaa 124,152,56
Ta 144,22,51360
Ta 146,22,56480
Mvaa 124,156,60
s_and_b32 s66, s14, 1
Ta 148,24,10400
K 105,106,107
Mvaa 116,156,44
Ta 150,24,15520
K 106,108,109
Mvaa 116,152,40
W 8
Ta 152,24,30880
Ta 154,24,36000
Mvaa 128,128,32
s_mul_i32 s66, s66, 327680
K 107,110,111
Mvaa 128,132,36
Ta 156,24,41120
Ta 158,24,46240
Mvaa 136,132,52
s_add_u32 s83, s52, 40960
s_add_u32 s65, s62, s66
s_waitcnt vmcnt(1)
v_cmp_eq_u64 vcc, s[12:13], v[28:29]
s_cmp_eq_u64 vcc, -1
s_cbranch_scc0 .Lspin_84%=
.Lok_85%=:
Mvaa 136,128,48
W 8
s_add_u32 s81, s58, 1
s_and_b32 s81, s81, 3
Mvaa 136,136,56
s_lshl_b32 s81, s81, 12
s_add_u32 s81, s81, s50
buffer_load_dwordx4 a[64:67], v1, s[8:11], s81 offen offset:0 sc1
Mvaa 136,140,60
s_add_u32 s82, s81, 131072
Ta 128,21,51392
Ta 130,21,56512
Mvaa 128,140,44
s_add_u32 s65, s65, 32768
Ta 132,23,10432
Ta 134,23,15552
Mvaa 128,136,40
W 8
s_mov_b32 s64, 0
Ta 136,23,30912
Mvaa 132,144,32
buffer_store_dwordx4 v[104:107], v1, s[8:11], s83 offen sc1
K 112,112,113
Mvaa 132,148,36
)"
R"(Ta 138,23,36032
K 113,114,115
Mvaa 140,148,52
v_add_u32 v19, 4096, v1
K 114,116,117
Mvaa 140,144,48
W 6
Ta 140,23,41152
Ta 142,23,46272
Mvaa 140,152,56
buffer_load_dwordx4 a[80:83], v1, s[8:11], s82 offen offset:0 sc1
K 115,118,119
Mvaa 140,156,60
s_add_u32 s83, s52, 8192
Ta 144,22,51424
v_bfe_u32 v27, v1, 4, 3
Mvaa 132,156,44
Ta 146,22,56544
v_lshlrev_b32 v27, 3, v27
Mvaa 132,152,40
W 6
Ta 148,24,10464
Ta 150,24,15584
Mvaa 144,128,32
buffer_load_dwordx4 a[68:71], v1, s[8:11], s81 offen offset:1024 sc1
Ta 152,24,30944
Mvaa 144,132,36
Ta 154,24,36064
v_bfe_u32 v18, v1, 7, 1
Mvaa 152,132,52
Ta 156,24,41184
Ta 158,24,46304
Mvaa 152,128,48
W 8
Ta 128,21,51456
v_lshl_add_u32 v18, v18, 9, v27
Mvaa 152,136,56
s_mov_b32 s71, 0
buffer_store_dwordx4 v[112:115], v1, s[8:11], s83 offen sc1
K 120,120,121
Mvaa 152,140,60
K 121,122,123
K 122,124,125
Mvaa 144,140,44
Ta 130,21,56576
K 123,126,127
Mvaa 144,136,40
W 6
Ta 132,23,10496
Ta 134,23,15616
Mvaa 148,144,32
s_add_u32 s83, s52, 49152
buffer_load_dwordx4 a[84:87], v1, s[8:11], s82 offen offset:1024 sc1
v_bfe_u32 v20, v1, 4, 4
Mvaa 148,148,36
Ta 136,23,30976
Ta 138,23,36096
Mvaa 156,148,52
Ta 140,23,41216
v_mul_u32_u24 v20, 1280, v20
Mvaa 156,144,48
W 7
Ta 142,23,46336
v_lshrrev_b32 v12, 8, v1
Mvaa 156,152,56
buffer_load_dwordx4 a[72:75], v1, s[8:11], s81 offen offset:2048 sc1
Ta 144,22,51488
Mvaa 156,156,60
Ta 146,22,56608
v_lshl_add_u32 v20, v12, 4, v20
Mvaa 148,156,44
Ta 148,24,10528
Ta 150,24,15648
Mvaa 148,152,40
W 8
s_bfe_u32 s66, s59, 0x10001
Ta 152,24,31008
Mvaa 160,128,32
buffer_store_dwordx4 v[120:123], v1, s[8:11], s83 offen sc1
K 128,128,129
Mvaa 160,132,36
K 129,130,131
K 130,132,133
Mvaa 168,132,52
s_lshl_b32 s67, s57, 1
Ta 154,24,36128
K 131,134,135
Mvaa 168,128,48
W 6
s_add_u32 s83, s52, 16384
Ta 156,24,41248
Mvaa 168,136,56
s_add_u32 s66, s66, s67
buffer_load_dwordx4 a[88:91], v1, s[8:11], s82 offen offset:2048 sc1
Ta 158,24,46368
Mvaa 168,140,60
Ta 128,25,10240
Ta 130,25,15360
Mvaa 160,140,44
s_lshl_b32 s61, s66, 2
Ta 132,25,20480
Ta 134,25,25600
Mvaa 160,136,40
W 8
Ta 136,26,10272
Ta 138,26,15392
Mvaa 164,144,32
buffer_load_dwordx4 a[76:79], v1, s[8:11], s81 offen offset:3072 sc1
Mvaa 164,148,36
s_lshr_b32 s66, s59, 2
Ta 140,26,20512
Ta 142,26,25632
Mvaa 172,148,52
Mvaa 172,144,48
W 8
s_lshl_b32 s66, s66, 1
Ta 144,25,10304
Mvaa 172,152,56
buffer_store_dwordx4 v[128:131], v1, s[8:11], s83 offen sc1
K 136,136,137
Mvaa 172,156,60
s_add_u32 s61, s61, s66
K 137,138,139
K 138,140,141
Mvaa 164,156,44
Ta 146,25,15424
K 139,142,143
Mvaa 164,152,40
W 6
s_add_u32 s83, s52, 57344
Ta 148,25,20544
Zvav 176,128,32
s_and_b32 s66, s59, 1
buffer_load_dwordx4 a[92:95], v1, s[8:11], s82 offen offset:3072 sc1
Ta 150,25,25664
Mvav 176,132,40
s_add_u32 s81, s58, 2
s_and_b32 s81, s81, 3
s_lshl_b32 s81, s81, 12
Zvav 184,132,72
s_add_u32 s81, s81, s50
Ta 152,26,10336
Ta 154,26,15456
Mvav 184,128,64
W 6
Ta 156,26,20576
Ta 158,26,25696
Zvav 188,136,64
s_add_u32 s61, s61, s66
buffer_load_dwordx4 a[96:99], v1, s[8:11], s81 offen offset:0 sc1
Ta 128,25,10368
Mvav 188,140,72
s_add_u32 s82, s81, 131072
s_xor_b32 s67, s61, 0
Ta 130,25,15488
Zvav 180,140,40
s_mul_i32 s72, s67, 20480
Ta 132,25,20608
Ta 134,25,25728
Mvav 180,136,32
W 8
Ta 136,26,10400
Ta 138,26,15520
Zvav 192,144,32
s_mul_i32 s66, s58, 320
buffer_store_dwordx4 v[136:139], v1, s[8:11], s83 offen sc1
K 144,144,145
Mvav 192,148,40
K 145,146,147
K 146,148,149
Zvav 200,148,72
Ta 140,26,20640
K 147,150,151
Mvav 200,144,64
W 7
s_add_u32 s83, s52, 24576
Ta 142,26,25760
Zvav 204,152,64
s_add_u32 s72, s72, s66
buffer_load_dwordx4 a[112:115], v1, s[8:11], s82 offen offset:0 sc1
Ta 144,25,10432
Mvav 204,156,72
s_mul_i32 s75, s67, 20480
Ta 146,25,15552
Ta 148,25,20672
Zvav 196,156,40
Ta 150,25,25792
Mvav 196,152,32
W 8
s_mul_i32 s66, s58, 2560
Ta 152,26,10464
Zvav 208,128,32
buffer_load_dwordx4 a[100:103], v1, s[8:11], s81 offen offset:1024 sc1
Ta 154,26,15584
Mvav 208,132,40
Ta 156,26,20704
Ta 158,26,25824
Zvav 216,132,72
s_add_u32 s75, s75, s66
Mvav 216,128,64
W 8
Ta 128,25,10496
Ta 130,25,15616
Zvav 220,136,64
s_add_u32 s75, s75, 131072
buffer_store_dwordx4 v[144:147], v1, s[8:11], s83 offen sc1
K 152,152,153
Mvav 220,140,72
K 153,154,155
K 154,156,157
Zvav 212,140,40
s_xor_b32 s67, s61, 124
Ta 132,25,20736
K 155,158,159
Mvav 212,136,32
W 7
s_add_u32 s83, s52, 65536
Ta 134,25,25856
Zvav 224,144,32
s_mul_i32 s73, s67, 20480
buffer_load_dwordx4 a[116:119], v1, s[8:11], s82 offen offset:1024 sc1
Ta 136,26,10528
Mvav 224,148,40
Ta 138,26,15648
Ta 140,26,20768
Zvav 232,148,72
Ta 142,26,25888
Mvav 232,144,64
W 8
s_mul_i32 s66, s58, 320
Ta 144,25,30720
Zvav 236,152,64
buffer_load_dwordx4 a[104:107], v1, s[8:11], s81 offen offset:2048 sc1
Ta 146,25,35840
Mvav 236,156,72
s_add_u32 s73, s73, s66
Ta 148,21,0
Ta 150,21,5120
Zvav 228,156,40
Mvav 228,152,32
W 8
s_mul_i32 s76, s67, 20480
Ta 152,26,30752
Zvav 240,128,32
buffer_store_dwordx4 v[152:155], v1, s[8:11], s83 offen sc1
K 160,160,161
Mvav 240,132,40
K 161,162,163
K 162,164,165
Zvav 248,132,72
s_mul_i32 s66, s58, 2560
Ta 154,26,35872
K 163,166,167
Mvav 248,128,64
W 6
s_add_u32 s83, s52, 32768
Ta 156,22,32
Zvav 252,136,64
s_add_u32 s76, s76, s66
buffer_load_dwordx4 a[120:123], v1, s[8:11], s82 offen offset:2048 sc1
Ta 158,22,5152
Mvav 252,140,72
Ta 128,25,30784
Ta 130,25,35904
Zvav 244,140,40
s_add_u32 s76, s76, 131072
Ta 132,21,64
Ta 134,21,5184
Mvav 244,136,32
W 8
Ta 136,26,30816
Ta 138,26,35936
Mvav 176,144,48
buffer_load_dwordx4 a[108:111], v1, s[8:11], s81 offen offset:3072 sc1
v_bfe_u32 v15, v1, 7, 1
Mvav 176,148,56
Ta 140,22,96
Ta 142,22,5216
Mvav 184,148,88
Mvav 184,144,80
W 8
Ta 144,25,30848
v_mul_u32_u24 v14, 10240, v15
Mvav 188,152,80
buffer_store_dwordx4 v[160:163], v1, s[8:11], s83 offen sc1
K 168,168,169
Mvav 188,156,88
K 169,170,171
K 170,172,173
Mvav 180,156,56
Ta 146,25,35968
K 171,174,175
Mvav 180,152,48
W 6
s_add_u32 s83, s52, 73728
v_lshrrev_b32 v15, 8, v1
Mvav 192,128,48
buffer_load_dwordx4 a[124:127], v1, s[8:11], s82 offen offset:3072 sc1
Ta 148,21,128
Mvav 192,132,56
s_add_u32 s81, s58, 3
s_and_b32 s81, s81, 3
s_lshl_b32 s81, s81, 12
Mvav 200,132,88
s_add_u32 s81, s81, s50
Ta 150,21,5248
v_lshl_add_u32 v14, v15, 7, v14
Mvav 200,128,80
W 4
Ta 152,26,30880
Ta 154,26,36000
Mvav 204,136,80
buffer_load_dwordx4 a[184:187], v1, s[8:11], s81 offen offset:0 sc1
v_and_b32 v15, 0x70, v1
Mvav 204,140,88
s_add_u32 s82, s81, 131072
Ta 156,22,160
Ta 158,22,5280
Mvav 196,140,56
Ta 128,25,30912
Ta 130,25,36032
Mvav 196,136,48
W 6
Ta 132,21,192
v_add_u32 v14, v14, v15
Mvav 208,144,48
s_bitcmp1_b32 s14, 0
s_cselect_b32 s74, s73, s72
s_cselect_b32 s77, s76, s75
buffer_store_dwordx4 v[168:171], v1, s[8:11], s83 offen sc1
Ta 134,21,5312
Mvav 208,148,56
s_add_u32 s74, s74, 256
Ta 136,26,30944
Ta 138,26,36064
Mvav 216,148,88
Ta 140,22,224
Ta 142,22,5344
Mvav 216,144,80
W 8
s_add_u32 s77, s77, 2048
Ta 144,25,30976
Mvav 220,152,80
buffer_load_dwordx4 a[200:203], v1, s[8:11], s82 offen offset:0 sc1
Ta 146,25,36096
Mvav 220,156,88
s_lshl_b32 s66, s14, 6
Ta 148,21,256
Ta 150,21,5376
Mvav 212,156,56
Mvav 212,152,48
W 8
s_lshl_b32 s67, s14, 9
Ta 152,26,31008
Mvav 224,128,48
buffer_load_dwordx4 a[188:191], v1, s[8:11], s81 offen offset:1024 sc1
Ta 154,26,36128
Mvav 224,132,56
s_add_u32 s72, s72, s66
Ta 156,22,288
Ta 158,22,5408
Mvav 232,132,88
Mvav 232,128,80
W 8
Ta 128,21,10240
Ta 130,21,15360
Mvav 236,136,80
s_add_u32 s75, s75, s67
buffer_load_dwordx4 a[204:207], v1, s[8:11], s82 offen offset:1024 sc1
Mvav 236,140,88
Ta 132,21,20480
Ta 134,21,25600
Mvav 228,140,56
s_add_u32 s73, s73, s66
Mvav 228,136,48
W 8
Ta 136,22,10272
Ta 138,22,15392
Mvav 240,144,48
s_add_u32 s76, s76, s67
buffer_load_dwordx4 a[192:195], v1, s[8:11], s81 offen offset:2048 sc1
Mvav 240,148,56
Ta 140,22,20512
Ta 142,22,25632
Mvav 248,148,88
Mvav 248,144,80
W 8
Ta 144,21,10304
Ta 146,21,15424
Mvav 252,152,80
buffer_load_dwordx4 a[208:211], v1, s[8:11], s82 offen offset:2048 sc1
Mvav 252,156,88
Ta 148,21,20544
Ta 150,21,25664
Mvav 244,156,56
Mvav 244,152,48
s_waitcnt vmcnt(25) lgkmcnt(8)
Ta 152,22,10336
Ta 154,22,15456
Mvaa 176,128,64
)"
R"(buffer_load_dwordx4 a[196:199], v1, s[8:11], s81 offen offset:3072 sc1
Mvaa 176,132,68
Ta 156,22,20576
Ta 158,22,25696
Mvaa 184,132,84
Mvaa 184,128,80
W 8
Ta 128,21,10368
Ta 130,21,15488
Mvaa 188,136,80
buffer_load_dwordx4 a[212:215], v1, s[8:11], s82 offen offset:3072 sc1
Mvaa 188,140,84
Ta 132,21,20608
Ta 134,21,25728
Mvaa 180,140,68
Mvaa 180,136,64
W 8
Ta 136,22,10400
Ta 138,22,15520
Mvaa 192,144,64
buffer_load_dwordx4 v[36:39], v20, s[16:19], s72 offen
Mvaa 192,148,68
Ta 140,22,20640
Ta 142,22,25760
Mvaa 200,148,84
Mvaa 200,144,80
W 8
Ta 144,21,10432
Ta 146,21,15552
Mvaa 204,152,80
buffer_load_dwordx4 v[44:47], v14, s[8:11], s75 offen sc1
Mvaa 204,156,84
Ta 148,21,20672
Ta 150,21,25792
Mvaa 196,156,68
Mvaa 196,152,64
W 8
Ta 152,22,10464
Ta 154,22,15584
Mvaa 208,128,64
buffer_load_dwordx4 v[52:55], v20, s[16:19], s73 offen
Mvaa 208,132,68
Ta 156,22,20704
Ta 158,22,25824
Mvaa 216,132,84
Mvaa 216,128,80
W 8
Ta 128,21,10496
Ta 130,21,15616
Mvaa 220,136,80
buffer_load_dwordx4 v[60:63], v14, s[8:11], s76 offen sc1
Mvaa 220,140,84
Ta 132,21,20736
Ta 134,21,25856
Mvaa 212,140,68
Mvaa 212,136,64
s_waitcnt vmcnt(11)
v_lshlrev_b32 v2, 2, v1
v_mov_b64 v[28:29], s[12:13]
s_mov_b64 exec, 3
buffer_store_dwordx2 v[28:29], v2, s[8:11], s53 offen offset:0 sc1
s_mov_b64 exec, -1
Ta 136,22,10528
W 9
Mvaa 224,144,64
Ta 138,22,15648
Mvaa 224,148,68
Ta 140,22,20768
Ta 142,22,25888
Mvaa 232,148,84
Mvaa 232,144,80
W 8
buffer_load_dwordx4 v[68:71], v20, s[16:19], s74 offen
Ta 144,21,30720
Mvaa 236,152,80
Ta 146,21,35840
Mvaa 236,156,84
Ta 148,23,0
Ta 150,23,5120
Mvaa 228,156,68
Mvaa 228,152,64
W 8
buffer_load_dwordx4 v[76:79], v14, s[8:11], s77 offen sc1
Ta 152,22,30752
Mvaa 240,128,64
Ta 154,22,35872
Mvaa 240,132,68
Ta 156,24,32
Ta 158,24,5152
Mvaa 248,132,84
Mvaa 248,128,80
W 8
buffer_load_dwordx2 v[12:13], v27, s[8:11], 0 offen offset:3584 sc1
Ta 128,21,30784
Mvaa 252,136,80
Ta 130,21,35904
Mvaa 252,140,84
Ta 132,23,64
Ta 134,23,5184
Mvaa 244,140,68
Mvaa 244,136,64
W 8
Ta 136,22,30816
Ta 138,22,35936
Mvaa 176,144,72
Mvaa 176,148,76
Ta 140,24,96
Ta 142,24,5216
Mvaa 184,148,92
Mvaa 184,144,88
W 8
Ta 144,21,30848
Ta 146,21,35968
Mvaa 188,152,88
Mvaa 188,156,92
Ta 148,23,128
Ta 150,23,5248
Mvaa 180,156,76
Mvaa 180,152,72
W 8
Ta 152,22,30880
Ta 154,22,36000
Mvaa 192,128,72
Mvaa 192,132,76
Ta 156,24,160
Ta 158,24,5280
Mvaa 200,132,92
Mvaa 200,128,88
W 8
Ta 128,21,30912
Ta 130,21,36032
Mvaa 204,136,88
Mvaa 204,140,92
Ta 132,23,192
Ta 134,23,5312
Mvaa 196,140,76
Mvaa 196,136,72
W 8
Ta 136,22,30944
Ta 138,22,36064
Mvaa 208,144,72
Mvaa 208,148,76
Ta 140,24,224
Ta 142,24,5344
Mvaa 216,148,92
Mvaa 216,144,88
W 8
Ta 144,21,30976
Ta 146,21,36096
Mvaa 220,152,88
buffer_load_dwordx2 v[16:17], v18, s[8:11], s63 offen sc1
Mvaa 220,156,92
Ta 148,23,256
Ta 150,23,5376
Mvaa 212,156,76
Mvaa 212,152,72
W 8
Ta 152,22,31008
Ta 154,22,36128
Mvaa 224,128,72
Mvaa 224,132,76
Ta 156,24,288
Ta 158,24,5408
Mvaa 232,132,92
Mvaa 232,128,88
W 8
Ta 128,23,20480
Ta 130,23,25600
Mvaa 236,136,88
Mvaa 236,140,92
Ta 132,23,51200
Ta 134,23,56320
Mvaa 228,140,76
Mvaa 228,136,72
W 8
Ta 136,25,0
Ta 138,25,5120
Mvaa 240,144,72
Mvaa 240,148,76
Ta 140,21,40960
Ta 142,21,46080
Mvaa 248,148,92
Mvaa 248,144,88
W 8
Ta 144,21,51200
Ta 146,21,56320
Mvaa 252,152,88
Mvaa 252,156,92
Ta 148,23,10240
Ta 150,23,15360
Mvaa 244,156,76
Mvaa 244,152,72
W 8
Ta 152,23,30720
Ta 154,23,35840
Mvaa 176,128,96
Mvaa 176,132,100
Ta 156,23,40960
Ta 158,23,46080
Mvaa 184,132,116
Mvaa 184,128,112
W 8
Ta 128,24,20512
Ta 130,24,25632
Mvaa 184,136,120
Mvaa 184,140,124
Ta 132,24,51232
Ta 134,24,56352
Mvaa 176,140,108
Mvaa 176,136,104
s_waitcnt vmcnt(13) lgkmcnt(8)
Ta 136,26,32
Ta 138,26,5152
Mvaa 176,144,184
Mvaa 176,148,188
Ta 140,22,40992
Ta 142,22,46112
Mvaa 184,148,204
Mvaa 184,144,200
s_waitcnt vmcnt(9) lgkmcnt(8)
Ta 144,22,51232
Ta 146,22,56352
Mvaa 184,152,208
Mvaa 184,156,212
Ta 148,24,10272
Ta 150,24,15392
Mvaa 176,156,196
s_waitcnt vmcnt(1)
v_xor_b32 v12, s12, v12
v_xor_b32 v13, s13, v13
v_lshrrev_b32 v3, 11, v12
v_or_b32 v13, v13, v3
v_cmp_eq_u32 vcc, 0, v13
s_cmp_eq_u64 vcc, -1
s_cbranch_scc0 .LL_89%=
.LL_90%=:
v_bfe_u32 v13, v12, 4, 7
s_xor_b32 s66, s61, 124
v_cmp_eq_u32 vcc, s61, v13
v_cmp_eq_u32_e64 s[86:87], s66, v13
s_or_b32 s68, vcc_lo, s86
s_and_b32 s68, s68, 255
s_cbranch_scc1 .Lup_87%=
.Lupr_88%=:
Mvaa 176,152,192
W 8
Ta 152,24,30752
Ta 154,24,35872
Mvaa 180,128,96
v_lshlrev_b32 v32, 16, v36
Mvaa 180,132,100
Ta 156,24,40992
v_and_b32 v33, 0xffff0000, v36
Mvaa 188,132,116
Ta 158,24,46112
v_lshlrev_b32 v34, 16, v37
Mvaa 188,128,112
W 8
Ta 128,23,20544
v_and_b32 v35, 0xffff0000, v37
Mvaa 188,136,120
Ta 130,23,25664
v_lshlrev_b32 v36, 16, v38
Mvaa 188,140,124
Ta 132,23,51264
v_and_b32 v37, 0xffff0000, v38
Mvaa 180,140,108
Ta 134,23,56384
v_lshlrev_b32 v38, 16, v39
Mvaa 180,136,104
W 8
Ta 136,25,64
v_and_b32 v39, 0xffff0000, v39
Mvaa 180,144,184
s_waitcnt vmcnt(0)
v_cmp_eq_u64 vcc, s[12:13], v[16:17]
s_and_b32 s71, vcc_lo, 255
Ta 138,25,5184
v_lshlrev_b32 v40, 16, v44
Mvaa 180,148,188
s_lshl_b32 s92, s14, 3
s_lshr_b32 s92, s71, s92
s_mov_b32 s91, 134217728
Mvaa 188,148,204
s_bitcmp1_b32 s71, 0
s_cselect_b32 s94, s78, s91
v_and_b32 v41, 0xffff0000, v44
Mvaa 188,144,200
W 6
Ta 140,21,41024
Ta 142,21,46144
Mvaa 188,152,208
v_lshlrev_b32 v42, 16, v45
v_and_b32 v43, 0xffff0000, v45
Mvaa 188,156,212
Ta 144,21,51264
Ta 146,21,56384
Mvaa 180,156,196
buffer_load_dwordx4 a[0:3], v1, s[8:11], s94 offen offset:0 sc1
v_lshlrev_b32 v44, 16, v46
Mvaa 180,152,192
W 6
Ta 148,23,10304
v_and_b32 v45, 0xffff0000, v46
Mvaa 192,128,96
Ta 150,23,15424
v_lshlrev_b32 v46, 16, v47
Mvaa 192,132,100
Ta 152,23,30784
v_and_b32 v47, 0xffff0000, v47
Mvaa 200,132,116
Ta 154,23,35904
v_lshlrev_b32 v48, 16, v52
Mvaa 200,128,112
W 6
Ta 156,23,41024
v_and_b32 v49, 0xffff0000, v52
Mvaa 200,136,120
Ta 158,23,46144
v_lshlrev_b32 v50, 16, v53
Mvaa 200,140,124
K 176,176,177
v_and_b32 v51, 0xffff0000, v53
Mvaa 192,140,108
Ta 128,24,20576
Ta 130,24,25696
Mvaa 192,136,104
W 6
K 177,178,179
v_lshlrev_b32 v52, 16, v54
Mvaa 192,144,184
s_bitcmp1_b32 s92, 0
s_cbranch_scc0 .LL_92%=
buffer_load_dwordx4 a[216:219], v1, s[8:11], s65 offen offset:0 sc1
.LL_92%=:
v_and_b32 v53, 0xffff0000, v54
Mvaa 192,148,188
s_bitcmp1_b32 s71, 1
s_cselect_b32 s94, s78, s91
K 178,180,181
Mvaa 200,148,204
Ta 132,24,51296
Ta 134,24,56416
Mvaa 200,144,200
W 4
v_lshlrev_b32 v54, 16, v55
K 179,182,183
Mvaa 200,152,208
s_add_u32 s83, s52, 327680
Ta 136,26,96
v_and_b32 v55, 0xffff0000, v55
Mvaa 200,156,212
Ta 138,26,5216
v_lshlrev_b32 v56, 16, v60
Mvaa 192,156,196
buffer_store_dwordx4 v[176:179], v1, s[8:11], s83 offen sc1
K 184,184,185
K 185,186,187
K 186,188,189
K 187,190,191
s_add_u32 s83, s52, 368640
buffer_store_dwordx4 v[184:187], v1, s[8:11], s83 offen sc1
Mvaa 192,152,192
Ta 140,22,41056
Ta 142,22,46176
W 4
Mvaa 196,128,96
v_and_b32 v57, 0xffff0000, v60
v_lshlrev_b32 v58, 16, v61
Mvaa 196,132,100
Ta 144,22,51296
v_and_b32 v59, 0xffff0000, v61
Mvaa 204,132,116
Ta 146,22,56416
v_lshlrev_b32 v60, 16, v62
Mvaa 204,128,112
Ta 148,24,10336
Ta 150,24,15456
W 4
Mvaa 204,136,120
buffer_load_dwordx4 a[4:7], v1, s[8:11], s94 offen offset:1024 sc1
v_and_b32 v61, 0xffff0000, v62
Mvaa 204,140,124
v_lshlrev_b32 v62, 16, v63
v_and_b32 v63, 0xffff0000, v63
Mvaa 196,140,108
Ta 152,24,30816
Ta 154,24,35936
Mvaa 196,136,104
Ta 156,24,41056
Ta 158,24,46176
W 4
Mvaa 196,144,184
v_lshlrev_b32 v64, 16, v68
v_and_b32 v65, 0xffff0000, v68
Mvaa 196,148,188
v_lshlrev_b32 v66, 16, v69
v_and_b32 v67, 0xffff0000, v69
Mvaa 204,148,204
Ta 128,23,20608
)"
R"(Ta 130,23,25728
Mvaa 204,144,200
Ta 132,23,51328
Ta 134,23,56448
W 4
Mvaa 204,152,208
v_lshlrev_b32 v68, 16, v70
v_and_b32 v69, 0xffff0000, v70
Mvaa 204,156,212
s_bitcmp1_b32 s92, 1
s_cbranch_scc0 .LL_93%=
buffer_load_dwordx4 a[220:223], v1, s[8:11], s65 offen offset:1024 sc1
.LL_93%=:
v_lshlrev_b32 v70, 16, v71
Mvaa 196,156,196
s_bitcmp1_b32 s71, 2
s_cselect_b32 s94, s78, s91
v_and_b32 v71, 0xffff0000, v71
Mvaa 196,152,192
Ta 136,25,128
Ta 138,25,5248
Ta 140,21,41088
Ta 142,21,46208
W 4
Mvaa 208,128,96
v_lshlrev_b32 v72, 16, v76
v_and_b32 v73, 0xffff0000, v76
Mvaa 208,132,100
v_lshlrev_b32 v74, 16, v77
v_and_b32 v75, 0xffff0000, v77
Mvaa 216,132,116
Ta 144,21,51328
Ta 146,21,56448
Mvaa 216,128,112
Ta 148,23,10368
Ta 150,23,15488
W 4
Mvaa 216,136,120
v_lshlrev_b32 v76, 16, v78
v_and_b32 v77, 0xffff0000, v78
Mvaa 216,140,124
buffer_load_dwordx4 a[8:11], v1, s[8:11], s94 offen offset:2048 sc1
v_lshlrev_b32 v78, 16, v79
Mvaa 208,140,108
Ta 152,23,30848
v_and_b32 v79, 0xffff0000, v79
Mvaa 208,136,104
Ta 154,23,35968
Ta 156,23,41088
Ta 158,23,46208
W 4
Mvaa 208,144,184
Ta 128,24,20640
K 192,192,193
Mvaa 208,148,188
Ta 130,24,25760
K 193,194,195
Mvaa 216,148,204
Ta 132,24,51360
K 194,196,197
Mvaa 216,144,200
Ta 134,24,56480
W 4
Mvaa 216,152,208
s_bitcmp1_b32 s92, 2
s_cbranch_scc0 .LL_94%=
buffer_load_dwordx4 a[224:227], v1, s[8:11], s65 offen offset:2048 sc1
.LL_94%=:
K 195,198,199
Mvaa 216,156,212
s_bitcmp1_b32 s71, 3
s_cselect_b32 s94, s78, s91
Ta 136,26,160
Mvaa 208,156,196
s_add_u32 s83, s52, 335872
buffer_store_dwordx4 v[192:195], v1, s[8:11], s83 offen sc1
K 200,200,201
K 201,202,203
K 202,204,205
K 203,206,207
s_add_u32 s83, s52, 376832
buffer_store_dwordx4 v[200:203], v1, s[8:11], s83 offen sc1
Mvaa 208,152,192
Ta 138,26,5280
Ta 140,22,41120
Ta 142,22,46240
W 4
Mvaa 212,128,96
Ta 144,22,51360
Ta 146,22,56480
Mvaa 212,132,100
Ta 148,24,10400
Ta 150,24,15520
Mvaa 220,132,116
buffer_load_dwordx4 a[12:15], v1, s[8:11], s94 offen offset:3072 sc1
Ta 152,24,30880
Mvaa 220,128,112
W 5
Ta 154,24,36000
Ta 156,24,41120
Mvaa 220,136,120
Ta 158,24,46240
Ta 128,23,20672
Mvaa 220,140,124
Ta 130,23,25792
Ta 132,23,51392
Mvaa 212,140,108
s_bitcmp1_b32 s92, 3
s_cbranch_scc0 .LL_95%=
buffer_load_dwordx4 a[228:231], v1, s[8:11], s65 offen offset:3072 sc1
.LL_95%=:
Ta 134,23,56512
Mvaa 212,136,104
W 8
s_bitcmp1_b32 s71, 4
s_cselect_b32 s94, s78, s91
Mvaa 212,144,184
Ta 136,25,192
Ta 138,25,5312
Mvaa 212,148,188
Ta 140,21,41152
Ta 142,21,46272
Mvaa 220,148,204
Mvaa 220,144,200
W 8
Ta 144,21,51392
Ta 146,21,56512
Mvaa 220,152,208
Mvaa 220,156,212
Ta 148,23,10432
Ta 150,23,15552
Mvaa 212,156,196
Mvaa 212,152,192
W 8
buffer_load_dwordx4 a[16:19], v19, s[8:11], s94 offen offset:0 sc1
Ta 152,23,30912
Mvaa 224,128,96
Ta 154,23,36032
Mvaa 224,132,100
Ta 156,23,41152
Ta 158,23,46272
Mvaa 232,132,116
K 208,208,209
Mvaa 232,128,112
W 8
Ta 128,24,20704
Ta 130,24,25824
Mvaa 232,136,120
K 209,210,211
Mvaa 232,140,124
Ta 132,24,51424
K 210,212,213
Mvaa 224,140,108
Ta 134,24,56544
K 211,214,215
Mvaa 224,136,104
W 8
s_bitcmp1_b32 s92, 4
s_cbranch_scc0 .LL_96%=
buffer_load_dwordx4 a[232:235], v19, s[8:11], s65 offen offset:0 sc1
.LL_96%=:
Mvaa 224,144,184
s_bitcmp1_b32 s71, 5
s_cselect_b32 s94, s78, s91
Ta 136,26,224
Mvaa 224,148,188
s_add_u32 s83, s52, 344064
Ta 138,26,5344
Ta 140,22,41184
Mvaa 232,148,204
Ta 142,22,46304
Mvaa 232,144,200
W 8
buffer_store_dwordx4 v[208:211], v1, s[8:11], s83 offen sc1
K 216,216,217
Mvaa 232,152,208
K 217,218,219
K 218,220,221
Mvaa 232,156,212
Ta 144,22,51424
K 219,222,223
Mvaa 224,156,196
s_add_u32 s83, s52, 385024
buffer_store_dwordx4 v[216:219], v1, s[8:11], s83 offen sc1
Ta 146,22,56544
Mvaa 224,152,192
W 6
Ta 148,24,10464
Ta 150,24,15584
Mvaa 228,128,96
Ta 152,24,30944
Ta 154,24,36064
Mvaa 228,132,100
Ta 156,24,41184
Ta 158,24,46304
Mvaa 236,132,116
buffer_load_dwordx4 a[20:23], v19, s[8:11], s94 offen offset:1024 sc1
Mvaa 236,128,112
W 8
Ta 128,23,20736
Ta 130,23,25856
Mvaa 236,136,120
Mvaa 236,140,124
Ta 132,23,51456
Ta 134,23,56576
Mvaa 228,140,108
Mvaa 228,136,104
W 8
s_bitcmp1_b32 s92, 5
s_cbranch_scc0 .LL_97%=
buffer_load_dwordx4 a[236:239], v19, s[8:11], s65 offen offset:1024 sc1
.LL_97%=:
Mvaa 228,144,184
s_bitcmp1_b32 s71, 6
s_cselect_b32 s94, s78, s91
Ta 136,25,256
Mvaa 228,148,188
Ta 138,25,5376
Ta 140,21,41216
Mvaa 236,148,204
Ta 142,21,46336
Mvaa 236,144,200
W 8
Ta 144,21,51456
Ta 146,21,56576
Mvaa 236,152,208
Mvaa 236,156,212
Ta 148,23,10496
Ta 150,23,15616
Mvaa 228,156,196
Mvaa 228,152,192
W 8
Ta 152,23,30976
Ta 154,23,36096
Mvaa 240,128,96
buffer_load_dwordx4 a[24:27], v19, s[8:11], s94 offen offset:2048 sc1
Mvaa 240,132,100
Ta 156,23,41216
Ta 158,23,46336
Mvaa 248,132,116
K 224,224,225
Mvaa 248,128,112
W 8
Ta 128,24,20768
Ta 130,24,25888
Mvaa 248,136,120
K 225,226,227
Mvaa 248,140,124
Ta 132,24,51488
K 226,228,229
Mvaa 240,140,108
Ta 134,24,56608
K 227,230,231
Mvaa 240,136,104
W 8
Ta 136,26,288
Ta 138,26,5408
Mvaa 240,144,184
s_add_u32 s83, s52, 352256
s_bitcmp1_b32 s92, 6
s_cbranch_scc0 .LL_98%=
buffer_load_dwordx4 a[240:243], v19, s[8:11], s65 offen offset:2048 sc1
.LL_98%=:
Mvaa 240,148,188
s_bitcmp1_b32 s71, 7
s_cselect_b32 s94, s78, s91
Ta 140,22,41248
Mvaa 248,148,204
Ta 142,22,46368
Mvaa 248,144,200
W 8
Ta 144,22,51488
Ta 146,22,56608
Mvaa 248,152,208
buffer_store_dwordx4 v[224:227], v1, s[8:11], s83 offen sc1
K 232,232,233
Mvaa 248,156,212
K 233,234,235
K 234,236,237
Mvaa 240,156,196
K 235,238,239
s_add_u32 s83, s52, 393216
buffer_store_dwordx4 v[232:235], v1, s[8:11], s83 offen sc1
Mvaa 240,152,192
W 6
Ta 148,24,10528
Ta 150,24,15648
Mvaa 244,128,96
Ta 152,24,31008
Ta 154,24,36128
Mvaa 244,132,100
Ta 156,24,41248
Ta 158,24,46368
Mvaa 252,132,116
buffer_load_dwordx4 a[28:31], v19, s[8:11], s94 offen offset:3072 sc1
Mvaa 252,128,112
W 8
Mvaa 252,136,120
Mvaa 252,140,124
Mvaa 244,140,108
Mvaa 244,136,104
W 4
Mvaa 244,144,184
s_bitcmp1_b32 s92, 7
s_cbranch_scc0 .LL_99%=
buffer_load_dwordx4 a[244:247], v19, s[8:11], s65 offen offset:3072 sc1
.LL_99%=:
Mvaa 244,148,188
Mvaa 252,148,204
Mvaa 252,144,200
W 0
Mvaa 252,152,208
Mvaa 252,156,212
Mvaa 244,156,196
Mvaa 244,152,192
K 240,240,241
K 241,242,243
s_nop 5
K 242,244,245
K 243,246,247
s_add_u32 s83, s52, 360448
buffer_store_dwordx4 v[240:243], v1, s[8:11], s83 offen sc1
K 248,248,249
K 249,250,251
K 250,252,253
K 251,254,255
s_add_u32 s83, s52, 401408
buffer_store_dwordx4 v[248:251], v1, s[8:11], s83 offen sc1
s_branch .Ltend_83%=
.p2align 12
.Llate_82%=:
ds_read_b32 v11, v11 offset:59392
ds_read_b64 v[8:9], v10 offset:57344
Rv 176,3,0
Rv 180,3,1024
Rv 184,3,2048
Rv 188,3,3072
Rv 192,3,4096
Rv 196,3,5120
Rv 200,3,6144
Rv 204,3,7168
W 6
Mvvv 32,224,176
s_lshl_b32 s92, s58, 2
s_add_u32 s91, s92, s51
s_add_u32 s95, s91, 7
Mvvv 36,228,176
s_and_b32 s95, s95, 15
s_mul_i32 s95, s95, 40960
s_mul_i32 s71, s51, 20480
Mvvv 40,224,180
s_add_u32 s71, s71, 61440
s_add_u32 m0, s71, s48
s_lshl2_add_u32 s94, s48, s95
Mvvv 44,228,180
W 4
v_cmp_ne_u32 vcc, 0, v11
s_cmp_lg_u64 vcc, 0
s_cbranch_scc1 .Lredo%=
A 4,4,8
Mvvv 48,224,184
v_rcp_f32 v4, v4
Mvvv 52,228,184
Ta 128,25,10240
A 5,5,9
Mvvv 56,224,188
Ta 130,25,15360
D 16,40,94,0
Mvvv 60,228,188
W 4
s_add_u32 s95, s91, 9
s_and_b32 s95, s95, 15
Mvvv 64,224,192
s_mul_i32 s95, s95, 40960
s_mul_i32 s71, s51, 10240
s_add_u32 s71, s71, 112640
Mvvv 68,228,192
v_rcp_f32 v5, v5
Mvvv 72,224,196
s_lshl_b32 s93, s58, 12
s_add_u32 s93, s93, s50
Ta 132,25,20480
Mvvv 76,228,196
Ta 134,25,25600
W 4
Mvvv 80,224,200
U 32,32,4
U 33,33,4
Mvvv 84,228,200
Ta 136,26,10272
U 34,34,4
Mvvv 88,224,204
U 35,35,4
U 36,36,4
U 37,37,4
U 38,38,4
U 39,39,4
K 32,32,33
K 33,34,35
K 34,36,37
)"
R"(K 35,38,39
buffer_store_dwordx4 v[32:35], v1, s[8:11], s93 offen offset:0 sc1
U 40,40,4
U 41,41,4
U 42,42,4
U 43,43,4
U 44,44,4
U 45,45,4
U 46,46,4
U 47,47,4
K 40,40,41
K 41,42,43
K 42,44,45
K 43,46,47
buffer_store_dwordx4 v[40:43], v1, s[8:11], s93 offen offset:1024 sc1
U 64,64,5
U 65,65,5
U 66,66,5
U 67,67,5
U 68,68,5
U 69,69,5
U 70,70,5
U 71,71,5
K 64,64,65
K 65,66,67
K 66,68,69
K 67,70,71
s_add_u32 s67, s93, 131072
buffer_store_dwordx4 v[64:67], v1, s[8:11], s67 offen offset:0 sc1
U 72,72,5
U 73,73,5
U 74,74,5
U 75,75,5
U 76,76,5
U 77,77,5
U 78,78,5
U 79,79,5
K 72,72,73
K 73,74,75
K 74,76,77
K 75,78,79
Mvvv 92,228,204
Ta 138,26,15392
Ta 140,26,20512
Ta 142,26,25632
W 4
Zvav 176,128,32
Ta 144,25,10304
Ta 146,25,15424
Mvav 176,132,40
Ta 148,25,20544
Ta 150,25,25664
Zvav 184,132,72
D 17,40,94,1024
Ta 152,26,10336
Mvav 184,128,64
W 5
Ta 154,26,15456
Ta 156,26,20576
Zvav 188,136,64
Ta 158,26,25696
Ta 128,25,10368
Mvav 188,140,72
Ta 130,25,15488
Ta 132,25,20608
Zvav 180,140,40
D 18,40,94,2048
Ta 134,25,25728
Mvav 180,136,32
W 8
Ta 136,26,10400
Ta 138,26,15520
Zvav 192,144,32
Mvav 192,148,40
Ta 140,26,20640
Ta 142,26,25760
Zvav 200,148,72
D 19,40,94,3072
Mvav 200,144,64
W 8
s_add_u32 m0, m0, 4096
Ta 144,25,10432
Zvav 204,152,64
Ta 146,25,15552
Mvav 204,156,72
Ta 148,25,20672
Ta 150,25,25792
Zvav 196,156,40
buffer_load_dwordx4 v20, s[40:43], s94 offen lds
Mvav 196,152,32
W 8
s_add_u32 m0, s71, s48
s_lshl2_add_u32 s94, s48, s95
Zvav 208,128,32
Ta 152,26,10464
Ta 154,26,15584
Mvav 208,132,40
Ta 156,26,20704
Ta 158,26,25824
Zvav 216,132,72
D 16,40,94,0
v_bfe_u32 v28, v1, 4, 2
Mvav 216,128,64
W 8
Ta 128,25,10496
v_lshlrev_b32 v28, 3, v28
Zvav 220,136,64
Ta 130,25,15616
Mvav 220,140,72
Ta 132,25,20736
Ta 134,25,25856
Zvav 212,140,40
buffer_load_dwordx2 v[28:29], v28, s[8:11], s54 offen sc1
Mvav 212,136,32
W 8
Ta 136,26,10528
Ta 138,26,15648
Zvav 224,144,32
Mvav 224,148,40
Ta 140,26,20768
Ta 142,26,25888
Zvav 232,148,72
D 17,40,94,1024
Mvav 232,144,64
W 8
Ta 144,25,30720
Ta 146,25,35840
Zvav 236,152,64
Mvav 236,156,72
Ta 148,21,0
Ta 150,21,5120
Zvav 228,156,40
D 18,40,94,2048
Mvav 228,152,32
W 8
Ta 152,26,30752
Ta 154,26,35872
Zvav 240,128,32
Mvav 240,132,40
Ta 156,22,32
Ta 158,22,5152
Zvav 248,132,72
D 19,40,94,3072
Mvav 248,128,64
W 8
s_add_u32 m0, m0, 4096
Ta 128,25,30784
Zvav 252,136,64
Ta 130,25,35904
Mvav 252,140,72
Ta 132,21,64
Ta 134,21,5184
Zvav 244,140,40
buffer_store_dwordx4 v[72:75], v1, s[8:11], s67 offen offset:1024 sc1
U 48,48,4
U 49,49,4
U 50,50,4
U 51,51,4
U 52,52,4
U 53,53,4
U 54,54,4
U 55,55,4
K 48,48,49
K 49,50,51
K 50,52,53
K 51,54,55
buffer_store_dwordx4 v[48:51], v1, s[8:11], s93 offen offset:2048 sc1
U 56,56,4
U 57,57,4
U 58,58,4
U 59,59,4
U 60,60,4
U 61,61,4
U 62,62,4
U 63,63,4
K 56,56,57
K 57,58,59
K 58,60,61
K 59,62,63
buffer_store_dwordx4 v[56:59], v1, s[8:11], s93 offen offset:3072 sc1
U 80,80,5
U 81,81,5
U 82,82,5
U 83,83,5
U 84,84,5
U 85,85,5
U 86,86,5
U 87,87,5
K 80,80,81
K 81,82,83
K 82,84,85
K 83,86,87
buffer_store_dwordx4 v[80:83], v1, s[8:11], s67 offen offset:2048 sc1
U 88,88,5
U 89,89,5
U 90,90,5
U 91,91,5
U 92,92,5
U 93,93,5
U 94,94,5
U 95,95,5
K 88,88,89
K 89,90,91
K 90,92,93
K 91,94,95
Mvav 244,136,32
W 8
Ta 136,26,30816
Ta 138,26,35936
Mvav 176,144,48
Mvav 176,148,56
Ta 140,22,96
Ta 142,22,5216
Mvav 184,148,88
buffer_load_dwordx4 v20, s[40:43], s94 offen lds
Mvav 184,144,80
s_waitcnt vmcnt(8)
v_cmp_eq_u64 vcc, s[12:13], v[28:29]
s_cmp_eq_u64 vcc, -1
s_cbranch_scc0 .Lspin_100%=
.Lok_101%=:
s_add_u32 s81, s58, 0
W 8
Mvav 188,152,80
s_and_b32 s81, s81, 3
s_lshl_b32 s81, s81, 12
s_add_u32 s81, s81, s49
Mvav 188,156,88
Ta 144,25,30848
Ta 146,25,35968
Mvav 180,156,56
buffer_load_dwordx4 a[184:187], v1, s[8:11], s81 offen offset:0 sc1
Ta 148,21,128
Mvav 180,152,48
W 7
s_add_u32 s82, s81, 131072
Ta 150,21,5248
Mvav 192,128,48
Ta 152,26,30880
Ta 154,26,36000
Mvav 192,132,56
Ta 156,22,160
Ta 158,22,5280
Mvav 200,132,88
buffer_load_dwordx4 a[200:203], v1, s[8:11], s82 offen offset:0 sc1
Mvav 200,128,80
W 8
Ta 128,25,30912
Ta 130,25,36032
Mvav 204,136,80
Mvav 204,140,88
Ta 132,21,192
Ta 134,21,5312
Mvav 196,140,56
buffer_load_dwordx4 a[188:191], v1, s[8:11], s81 offen offset:1024 sc1
Mvav 196,136,48
W 8
Ta 136,26,30944
Ta 138,26,36064
Mvav 208,144,48
Mvav 208,148,56
Ta 140,22,224
Ta 142,22,5344
Mvav 216,148,88
buffer_load_dwordx4 a[204:207], v1, s[8:11], s82 offen offset:1024 sc1
Mvav 216,144,80
W 8
Ta 144,25,30976
Ta 146,25,36096
Mvav 220,152,80
Mvav 220,156,88
Ta 148,21,256
Ta 150,21,5376
Mvav 212,156,56
buffer_load_dwordx4 a[192:195], v1, s[8:11], s81 offen offset:2048 sc1
Mvav 212,152,48
W 8
Ta 152,26,31008
Ta 154,26,36128
Mvav 224,128,48
Mvav 224,132,56
Ta 156,22,288
Ta 158,22,5408
Mvav 232,132,88
buffer_store_dwordx4 v[88:91], v1, s[8:11], s67 offen offset:3072 sc1
Mvav 232,128,80
W 8
Ta 128,25,10240
Ta 130,25,15360
Mvav 236,136,80
Mvav 236,140,88
Ta 132,25,20480
Ta 134,25,25600
Mvav 228,140,56
buffer_load_dwordx4 a[208:211], v1, s[8:11], s82 offen offset:2048 sc1
Mvav 228,136,48
W 8
Ta 136,26,10272
Ta 138,26,15392
Mvav 240,144,48
Mvav 240,148,56
Ta 140,26,20512
Ta 142,26,25632
Mvav 248,148,88
buffer_load_dwordx4 a[196:199], v1, s[8:11], s81 offen offset:3072 sc1
Mvav 248,144,80
W 8
Ta 144,25,10304
Ta 146,25,15424
Mvav 252,152,80
Mvav 252,156,88
Ta 148,25,20544
Ta 150,25,25664
Mvav 244,156,56
buffer_load_dwordx4 a[212:215], v1, s[8:11], s82 offen offset:3072 sc1
Mvav 244,152,48
s_waitcnt vmcnt(5) lgkmcnt(8)
Ta 152,26,10336
Ta 154,26,15456
Zvaa 96,128,184
Mvaa 96,132,188
Ta 156,26,20576
Ta 158,26,25696
Zvaa 104,132,204
Mvaa 104,128,200
W 8
Ta 128,25,10368
Ta 130,25,15488
Zvaa 108,136,200
Mvaa 108,140,204
Ta 132,25,20608
Ta 134,25,25728
Zvaa 100,140,188
s_lshl_b32 s92, s58, 2
s_add_u32 s91, s92, s51
s_add_u32 s95, s91, 11
Mvaa 100,136,184
W 8
s_and_b32 s95, s95, 15
s_mul_i32 s95, s95, 40960
Zvaa 112,144,184
s_mul_i32 s71, s51, 10240
s_add_u32 s71, s71, 40960
s_add_u32 m0, s71, s48
Mvaa 112,148,188
s_lshl2_add_u32 s94, s48, s95
Ta 136,26,10400
Ta 138,26,15520
Zvaa 120,148,204
Ta 140,26,20640
Ta 142,26,25760
Mvaa 120,144,200
W 8
Ta 144,25,10432
Ta 146,25,15552
Zvaa 124,152,200
Mvaa 124,156,204
Ta 148,25,20672
Ta 150,25,25792
Zvaa 116,156,188
Mvaa 116,152,184
W 8
Ta 152,26,10464
Ta 154,26,15584
Zvaa 128,128,184
s_lshl_b32 s80, s58, 3
s_add_u32 s80, s80, s55
s_waitcnt vmcnt(3)
v_mov_b64 v[28:29], s[12:13]
s_mov_b64 exec, 1
buffer_store_dwordx2 v[28:29], off, s[8:11], s80 sc1
s_mov_b64 exec, -1
Mvaa 128,132,188
Ta 156,26,20704
Ta 158,26,25824
Zvaa 136,132,204
Mvaa 136,128,200
W 8
Ta 128,25,10496
Ta 130,25,15616
Zvaa 140,136,200
s_barrier
D 16,40,94,0
Mvaa 140,140,204
s_add_u32 s95, s91, 13
s_and_b32 s95, s95, 15
s_mul_i32 s95, s95, 40960
Zvaa 132,140,188
s_mul_i32 s71, s51, 20480
s_add_u32 s71, s71, 71680
Ta 132,25,20736
Mvaa 132,136,184
W 7
Ta 134,25,25856
Ta 136,26,10528
Zvaa 144,144,184
Ta 138,26,15648
D 17,40,94,1024
Mvaa 144,148,188
Ta 140,26,20768
)"
R"(Ta 142,26,25888
Zvaa 152,148,204
Mvaa 152,144,200
W 8
Ta 144,25,30720
Ta 146,25,35840
Zvaa 156,152,200
D 18,40,94,2048
Mvaa 156,156,204
Ta 148,21,0
Ta 150,21,5120
Zvaa 148,156,188
Mvaa 148,152,184
W 8
Ta 152,26,30752
Ta 154,26,35872
Zvaa 160,128,184
D 19,40,94,3072
Mvaa 160,132,188
s_add_u32 m0, m0, 4096
Ta 156,22,32
Ta 158,22,5152
Zvaa 168,132,204
Mvaa 168,128,200
W 8
Ta 128,25,30784
Ta 130,25,35904
Zvaa 172,136,200
buffer_load_dwordx4 v20, s[40:43], s94 offen lds
Mvaa 172,140,204
s_add_u32 s81, s58, 1
s_and_b32 s81, s81, 3
s_lshl_b32 s81, s81, 12
Zvaa 164,140,188
s_add_u32 s81, s81, s49
Ta 132,21,64
Ta 134,21,5184
Mvaa 164,136,184
s_waitcnt vmcnt(6) lgkmcnt(8)
Ta 136,26,30816
Ta 138,26,35936
Mvaa 96,144,192
buffer_load_dwordx4 a[216:219], v1, s[8:11], s81 offen offset:0 sc1
Mvaa 96,148,196
s_add_u32 s82, s81, 131072
Ta 140,22,96
Ta 142,22,5216
Mvaa 104,148,212
Mvaa 104,144,208
W 8
Ta 144,25,30848
Ta 146,25,35968
Mvaa 108,152,208
buffer_load_dwordx4 a[232:235], v1, s[8:11], s82 offen offset:0 sc1
Mvaa 108,156,212
Ta 148,21,128
Ta 150,21,5248
Mvaa 100,156,196
Mvaa 100,152,192
W 8
Ta 152,26,30880
Ta 154,26,36000
Mvaa 112,128,192
buffer_load_dwordx4 a[220:223], v1, s[8:11], s81 offen offset:1024 sc1
Mvaa 112,132,196
Ta 156,22,160
Ta 158,22,5280
Mvaa 120,132,212
Mvaa 120,128,208
W 8
Ta 128,25,30912
Ta 130,25,36032
Mvaa 124,136,208
buffer_load_dwordx4 a[236:239], v1, s[8:11], s82 offen offset:1024 sc1
Mvaa 124,140,212
Ta 132,21,192
Ta 134,21,5312
Mvaa 116,140,196
Mvaa 116,136,192
W 8
Ta 136,26,30944
Ta 138,26,36064
Mvaa 128,144,192
buffer_load_dwordx4 a[224:227], v1, s[8:11], s81 offen offset:2048 sc1
Mvaa 128,148,196
Ta 140,22,224
Ta 142,22,5344
Mvaa 136,148,212
Mvaa 136,144,208
W 8
Ta 144,25,30976
Ta 146,25,36096
Mvaa 140,152,208
buffer_load_dwordx4 a[240:243], v1, s[8:11], s82 offen offset:2048 sc1
Mvaa 140,156,212
Ta 148,21,256
Ta 150,21,5376
Mvaa 132,156,196
Mvaa 132,152,192
W 8
Ta 152,26,31008
Ta 154,26,36128
Mvaa 144,128,192
buffer_load_dwordx4 a[228:231], v1, s[8:11], s81 offen offset:3072 sc1
Mvaa 144,132,196
Ta 156,22,288
Ta 158,22,5408
Mvaa 152,132,212
Mvaa 152,128,208
W 8
Ta 128,21,10240
Ta 130,21,15360
Mvaa 156,136,208
buffer_load_dwordx4 a[244:247], v1, s[8:11], s82 offen offset:3072 sc1
Mvaa 156,140,212
s_add_u32 m0, s71, s48
s_lshl2_add_u32 s94, s48, s95
Ta 132,21,20480
Mvaa 148,140,196
Ta 134,21,25600
Mvaa 148,136,192
W 8
Ta 136,22,10272
Ta 138,22,15392
Mvaa 160,144,192
D 16,40,94,0
Mvaa 160,148,196
s_add_u32 s95, s92, 15
s_and_b32 s95, s95, 15
s_mul_i32 s95, s95, 40960
Mvaa 168,148,212
s_mul_i32 s71, s51, 0
s_add_u32 s71, s71, 102400
Ta 140,22,20512
Mvaa 168,144,208
W 7
Ta 142,22,25632
Ta 144,21,10304
Mvaa 172,152,208
D 17,40,94,1024
Ta 146,21,15424
Mvaa 172,156,212
Ta 148,21,20544
Ta 150,21,25664
Mvaa 164,156,196
Mvaa 164,152,192
s_waitcnt vmcnt(6) lgkmcnt(8)
Ta 152,22,10336
Ta 154,22,15456
Mvaa 96,128,216
D 18,40,94,2048
Mvaa 96,132,220
Ta 156,22,20576
Ta 158,22,25696
Mvaa 104,132,236
Mvaa 104,128,232
W 8
Ta 128,21,10368
Ta 130,21,15488
Mvaa 108,136,232
D 19,40,94,3072
Mvaa 108,140,236
s_add_u32 m0, m0, 4096
Ta 132,21,20608
Ta 134,21,25728
Mvaa 100,140,220
Mvaa 100,136,216
W 8
Ta 136,22,10400
Ta 138,22,15520
Mvaa 112,144,216
buffer_load_dwordx4 v20, s[40:43], s94 offen lds
Mvaa 112,148,220
s_add_u32 m0, s71, s48
s_lshl2_add_u32 s94, s48, s95
Ta 140,22,20640
Mvaa 120,148,236
Ta 142,22,25760
Mvaa 120,144,232
W 8
Ta 144,21,10432
Ta 146,21,15552
Mvaa 124,152,232
D 16,40,94,0
Mvaa 124,156,236
Ta 148,21,20672
Ta 150,21,25792
Mvaa 116,156,220
D 17,40,94,1024
D 18,40,94,2048
D 19,40,94,3072
s_add_u32 m0, m0, 4096
s_nop 0
buffer_load_dwordx4 v20, s[40:43], s94 offen lds
Mvaa 116,152,216
W 8
s_add_u32 s81, s58, 2
s_and_b32 s81, s81, 3
Mvaa 128,128,216
s_lshl_b32 s81, s81, 12
s_add_u32 s81, s81, s49
Ta 152,22,10464
Mvaa 128,132,220
Ta 154,22,15584
Ta 156,22,20704
Mvaa 136,132,236
Ta 158,22,25824
Mvaa 136,128,232
W 8
Ta 128,21,10496
Ta 130,21,15616
Mvaa 140,136,232
s_barrier
buffer_load_dwordx4 a[0:3], v1, s[8:11], s81 offen offset:0 sc1
v_and_b32 v27, 1, v12
Mvaa 140,140,236
s_add_u32 s82, s81, 131072
Ta 132,21,20736
Ta 134,21,25856
Mvaa 132,140,220
Mvaa 132,136,216
W 8
Ta 136,22,10528
v_lshlrev_b32 v27, 1, v27
Mvaa 144,144,216
Ta 138,22,15648
buffer_load_dwordx4 a[16:19], v1, s[8:11], s82 offen offset:0 sc1
Mvaa 144,148,220
Ta 140,22,20768
v_xor_b32 v27, v27, v13
Mvaa 152,148,236
Ta 142,22,25888
Mvaa 152,144,232
W 8
Ta 144,21,30720
Ta 146,21,35840
Mvaa 156,152,232
buffer_load_dwordx4 a[4:7], v1, s[8:11], s81 offen offset:1024 sc1
v_and_b32 v12, 2, v12
Mvaa 156,156,236
Ta 148,23,0
Ta 150,23,5120
Mvaa 148,156,220
v_or_b32 v12, v12, v14
Mvaa 148,152,216
W 8
Ta 152,22,30752
Ta 154,22,35872
Mvaa 160,128,216
buffer_load_dwordx4 a[20:23], v1, s[8:11], s82 offen offset:1024 sc1
Mvaa 160,132,220
Ta 156,24,32
v_lshl_or_b32 v12, v27, 2, v12
Mvaa 168,132,236
Ta 158,24,5152
Mvaa 168,128,232
W 8
Ta 128,21,30784
v_cmp_eq_u32_e64 s[86:87], 0, v12
Mvaa 172,136,232
Ta 130,21,35904
buffer_load_dwordx4 a[8:11], v1, s[8:11], s81 offen offset:2048 sc1
Mvaa 172,140,236
Ta 132,23,64
Ta 134,23,5184
Mvaa 164,140,220
v_cndmask_b32_e64 v4, 0, v15, s[86:87]
Mvaa 164,136,216
s_waitcnt vmcnt(15) lgkmcnt(8)
Ta 136,22,30816
Ta 138,22,35936
Mvaa 96,144,224
buffer_load_dwordx4 a[24:27], v1, s[8:11], s82 offen offset:2048 sc1
v_cmp_eq_u32_e64 s[86:87], 1, v12
Mvaa 96,148,228
Ta 140,24,96
Ta 142,24,5216
Mvaa 104,148,244
Mvaa 104,144,240
W 8
Ta 144,21,30848
v_cndmask_b32_e64 v5, 0, v15, s[86:87]
Mvaa 108,152,240
Ta 146,21,35968
buffer_load_dwordx4 a[12:15], v1, s[8:11], s81 offen offset:3072 sc1
Mvaa 108,156,244
Ta 148,23,128
v_cmp_eq_u32_e64 s[86:87], 2, v12
Mvaa 100,156,228
Ta 150,23,5248
Mvaa 100,152,224
W 8
Ta 152,22,30880
v_cndmask_b32_e64 v6, 0, v15, s[86:87]
Mvaa 112,128,224
Ta 154,22,36000
buffer_load_dwordx4 a[28:31], v1, s[8:11], s82 offen offset:3072 sc1
Mvaa 112,132,228
s_add_u32 s81, s58, 3
s_and_b32 s81, s81, 3
s_lshl_b32 s81, s81, 12
Mvaa 120,132,244
s_add_u32 s81, s81, s49
Ta 156,24,160
Ta 158,24,5280
Mvaa 120,128,240
W 8
Ta 128,21,30912
v_cmp_eq_u32_e64 s[86:87], 3, v12
Mvaa 124,136,240
Ta 130,21,36032
buffer_load_dwordx4 a[32:35], v1, s[8:11], s81 offen offset:0 sc1
Mvaa 124,140,244
s_add_u32 s82, s81, 131072
Ta 132,23,192
v_cndmask_b32_e64 v7, 0, v15, s[86:87]
Mvaa 116,140,228
Ta 134,23,5312
Mvaa 116,136,224
W 8
Ta 136,22,30944
v_cmp_eq_u32_e64 s[86:87], 4, v12
Mvaa 128,144,224
Ta 138,22,36064
buffer_load_dwordx4 a[48:51], v1, s[8:11], s82 offen offset:0 sc1
Mvaa 128,148,228
Ta 140,24,224
v_cndmask_b32_e64 v8, 0, v15, s[86:87]
Mvaa 136,148,244
Ta 142,24,5344
Mvaa 136,144,240
W 8
Ta 144,21,30976
Ta 146,21,36096
Mvaa 140,152,240
s_waitcnt vmcnt(10)
s_barrier
v_cmp_eq_u32_e64 s[86:87], 5, v12
Mvaa 140,156,244
buffer_load_dwordx4 a[36:39], v1, s[8:11], s81 offen offset:1024 sc1
Ta 148,23,256
Mvaa 132,156,228
Ta 150,23,5376
v_cndmask_b32_e64 v9, 0, v15, s[86:87]
Mvaa 132,152,224
W 8
Ta 152,22,31008
Ta 154,22,36128
Mvaa 144,128,224
Mvaa 144,132,228
buffer_load_dwordx4 a[52:55], v1, s[8:11], s82 offen offset:1024 sc1
v_cmp_eq_u32_e64 s[86:87], 6, v12
Mvaa 152,132,244
Ta 156,24,288
Ta 158,24,5408
Mvaa 152,128,240
W 8
Ta 128,23,20480
v_cndmask_b32_e64 v10, 0, v15, s[86:87]
Mvaa 156,136,240
Ta 130,23,25600
Mvaa 156,140,244
buffer_load_dwordx4 a[40:43], v1, s[8:11], s81 offen offset:2048 sc1
v_cmp_eq_u32_e64 s[86:87], 7, v12
Mvaa 148,140,228
Ta 132,23,51200
Ta 134,23,56320
Mvaa 148,136,224
W 8
Ta 136,24,20512
Ta 138,24,25632
Mvaa 160,144,224
v_cndmask_b32_e64 v11, 0, v15, s[86:87]
Mvaa 160,148,228
buffer_load_dwordx4 a[56:59], v1, s[8:11], s82 offen offset:2048 sc1
Ta 140,24,51232
Mvaa 168,148,244
s_mov_b32 s20, s38
Ta 142,24,56352
Mvaa 168,144,240
W 8
Ta 144,23,20544
Ta 146,23,25664
Mvaa 172,152,240
Mvaa 172,156,244
s_and_b32 s21, s39, 0xffff
buffer_load_dwordx4 a[44:47], v1, s[8:11], s81 offen offset:3072 sc1
Ta 148,23,51264
Mvaa 164,156,228
Ta 150,23,56384
Mvaa 164,152,224
s_waitcnt vmcnt(11) lgkmcnt(8)
s_mov_b32 s22, 2621440
Ta 152,24,20576
Mvaa 96,128,0
Ta 154,24,25696
Mvaa 96,132,4
buffer_load_dwordx4 a[60:63], v1, s[8:11], s82 offen offset:3072 sc1
Ta 156,24,51296
Mvaa 104,132,20
)"
R"(s_mov_b32 s23, 0x20000
Ta 158,24,56416
Mvaa 104,128,16
W 8
Ta 128,23,20608
Ta 130,23,25728
Mvaa 108,136,16
s_and_b32 s17, s17, 0xffff
Mvaa 108,140,20
Ta 132,23,51328
Ta 134,23,56448
Mvaa 100,140,4
Mvaa 100,136,0
W 8
s_mov_b32 s18, 2621440
Ta 136,24,20640
Mvaa 112,144,0
Ta 138,24,25760
v_bfe_u32 v28, v1, 4, 2
Mvaa 112,148,4
s_mov_b32 s19, 0x20000
Ta 140,24,51360
v_lshlrev_b32 v28, 3, v28
Mvaa 120,148,20
Ta 142,24,56480
buffer_load_dwordx2 v[28:29], v28, s[8:11], s55 offen sc1
Mvaa 120,144,16
W 8
s_lshr_b32 s66, s15, 3
Ta 144,23,20672
Mvaa 124,152,16
Ta 146,23,25792
Mvaa 124,156,20
Ta 148,23,51392
Ta 150,23,56512
Mvaa 116,156,4
s_lshl_b32 s66, s66, 4
Mvaa 116,152,0
W 8
Ta 152,24,20704
Ta 154,24,25824
Mvaa 128,128,0
s_add_u32 s66, s66, s59
Mvaa 128,132,4
Ta 156,24,51424
Ta 158,24,56544
Mvaa 136,132,20
Mvaa 136,128,16
W 8
s_lshl_b32 s63, s66, 6
Ta 128,23,20736
Mvaa 140,136,16
Ta 130,23,25856
Mvaa 140,140,20
s_add_u32 s63, s63, 65536
Ta 132,23,51456
Ta 134,23,56576
Mvaa 132,140,4
Mvaa 132,136,0
W 8
Ta 136,24,20768
Ta 138,24,25888
Mvaa 144,144,0
s_mul_i32 s62, s66, 40960
Mvaa 144,148,4
Ta 140,24,51488
Ta 142,24,56608
Mvaa 152,148,20
s_add_u32 s62, s62, 56623104
Mvaa 152,144,16
W 8
Ta 144,25,0
Ta 146,25,5120
Mvaa 156,152,16
Mvaa 156,156,20
s_lshl_b32 s66, s14, 13
Ta 148,21,40960
Ta 150,21,46080
Mvaa 148,156,4
Mvaa 148,152,0
W 8
s_add_u32 s78, s62, s66
Ta 152,26,32
Mvaa 160,128,0
Ta 154,26,5152
Mvaa 160,132,4
s_add_u32 s79, s78, 327680
Ta 156,22,40992
Ta 158,22,46112
Mvaa 168,132,20
Mvaa 168,128,16
W 8
Ta 128,25,64
Ta 130,25,5184
Mvaa 172,136,16
s_and_b32 s66, s14, 1
Mvaa 172,140,20
Ta 132,21,41024
Ta 134,21,46144
Mvaa 164,140,4
s_mul_i32 s66, s66, 327680
Mvaa 164,136,0
s_waitcnt vmcnt(9) lgkmcnt(8)
Ta 136,26,96
Ta 138,26,5216
Mvaa 96,144,8
Mvaa 96,148,12
s_add_u32 s65, s62, s66
Ta 140,22,41056
Ta 142,22,46176
Mvaa 104,148,28
Mvaa 104,144,24
W 8
s_add_u32 s65, s65, 32768
Ta 144,25,128
Mvaa 108,152,24
Ta 146,25,5248
Mvaa 108,156,28
Ta 148,21,41088
Ta 150,21,46208
Mvaa 100,156,12
s_mov_b32 s64, 0
Mvaa 100,152,8
W 8
Ta 152,26,160
Ta 154,26,5280
Mvaa 112,128,8
v_add_u32 v19, 4096, v1
Mvaa 112,132,12
Ta 156,22,41120
Ta 158,22,46240
Mvaa 120,132,28
s_waitcnt vmcnt(0)
v_cmp_eq_u64 vcc, s[12:13], v[28:29]
s_cmp_eq_u64 vcc, -1
s_cbranch_scc0 .Lspin_103%=
.Lok_104%=:
s_add_u32 s81, s58, 1
s_and_b32 s81, s81, 3
Mvaa 120,128,24
W 8
s_lshl_b32 s81, s81, 12
s_add_u32 s81, s81, s50
Mvaa 124,136,24
buffer_load_dwordx4 a[64:67], v1, s[8:11], s81 offen offset:0 sc1
v_bfe_u32 v27, v1, 4, 3
Mvaa 124,140,28
s_add_u32 s82, s81, 131072
Ta 128,25,192
Ta 130,25,5312
Mvaa 116,140,12
Ta 132,21,41152
v_lshlrev_b32 v27, 3, v27
Mvaa 116,136,8
W 7
Ta 134,21,46272
Ta 136,26,224
Mvaa 128,144,8
Ta 138,26,5344
v_bfe_u32 v18, v1, 7, 1
Mvaa 128,148,12
buffer_load_dwordx4 a[80:83], v1, s[8:11], s82 offen offset:0 sc1
Ta 140,22,41184
Mvaa 136,148,28
Ta 142,22,46304
v_lshl_add_u32 v18, v18, 9, v27
Mvaa 136,144,24
W 8
Ta 144,25,256
Ta 146,25,5376
Mvaa 140,152,24
s_mov_b32 s71, 0
Mvaa 140,156,28
buffer_load_dwordx4 a[68:71], v1, s[8:11], s81 offen offset:1024 sc1
Ta 148,21,41216
Mvaa 132,156,12
Ta 150,21,46336
Mvaa 132,152,8
W 8
Ta 152,26,288
v_bfe_u32 v20, v1, 4, 4
Mvaa 144,128,8
Ta 154,26,5408
Mvaa 144,132,12
Ta 156,22,41248
v_mul_u32_u24 v20, 1280, v20
Mvaa 152,132,28
Ta 158,22,46368
buffer_load_dwordx4 a[84:87], v1, s[8:11], s82 offen offset:1024 sc1
Mvaa 152,128,24
W 8
Ta 128,21,51200
Ta 130,21,56320
Mvaa 156,136,24
v_lshrrev_b32 v12, 8, v1
Mvaa 156,140,28
Ta 132,23,10240
Ta 134,23,15360
Mvaa 148,140,12
v_lshl_add_u32 v20, v12, 4, v20
Mvaa 148,136,8
W 8
buffer_load_dwordx4 a[72:75], v1, s[8:11], s81 offen offset:2048 sc1
Ta 136,23,30720
Mvaa 160,144,8
Ta 138,23,35840
Mvaa 160,148,12
s_bfe_u32 s66, s59, 0x10001
Ta 140,23,40960
Ta 142,23,46080
Mvaa 168,148,28
Mvaa 168,144,24
W 8
s_lshl_b32 s67, s57, 1
Ta 144,22,51232
Mvaa 172,152,24
Ta 146,22,56352
buffer_load_dwordx4 a[88:91], v1, s[8:11], s82 offen offset:2048 sc1
Mvaa 172,156,28
Ta 148,24,10272
Ta 150,24,15392
Mvaa 164,156,12
s_add_u32 s66, s66, s67
Mvaa 164,152,8
W 8
Ta 152,24,30752
Ta 154,24,35872
Mvaa 96,128,32
s_lshl_b32 s61, s66, 2
Mvaa 96,132,36
buffer_load_dwordx4 a[76:79], v1, s[8:11], s81 offen offset:3072 sc1
Ta 156,24,40992
Mvaa 104,132,52
s_lshr_b32 s66, s59, 2
Ta 158,24,46112
Mvaa 104,128,48
W 8
Ta 128,21,51264
Ta 130,21,56384
Mvaa 104,136,56
Mvaa 104,140,60
s_lshl_b32 s66, s66, 1
Ta 132,23,10304
Ta 134,23,15424
Mvaa 96,140,44
buffer_load_dwordx4 a[92:95], v1, s[8:11], s82 offen offset:3072 sc1
Mvaa 96,136,40
W 8
s_add_u32 s81, s58, 2
s_and_b32 s81, s81, 3
Mvaa 100,144,32
s_lshl_b32 s81, s81, 12
s_add_u32 s81, s81, s50
s_add_u32 s61, s61, s66
Mvaa 100,148,36
Ta 136,23,30784
Ta 138,23,35904
Mvaa 108,148,52
s_and_b32 s66, s59, 1
Ta 140,23,41024
Ta 142,23,46144
Mvaa 108,144,48
W 8
buffer_load_dwordx4 a[96:99], v1, s[8:11], s81 offen offset:0 sc1
Ta 144,22,51296
Mvaa 108,152,56
s_add_u32 s82, s81, 131072
s_add_u32 s61, s61, s66
Ta 146,22,56416
Mvaa 108,156,60
Ta 148,24,10336
Ta 150,24,15456
Mvaa 100,156,44
Mvaa 100,152,40
W 8
s_xor_b32 s67, s61, 0
Ta 152,24,30816
Mvaa 112,128,32
Ta 154,24,35936
buffer_load_dwordx4 a[112:115], v1, s[8:11], s82 offen offset:0 sc1
Mvaa 112,132,36
s_mul_i32 s72, s67, 20480
Ta 156,24,41056
Ta 158,24,46176
Mvaa 120,132,52
K 96,96,97
K 97,98,99
Mvaa 120,128,48
W 8
Ta 128,21,51328
Ta 130,21,56448
Mvaa 120,136,56
s_mul_i32 s66, s58, 320
K 98,100,101
Mvaa 120,140,60
buffer_load_dwordx4 a[100:103], v1, s[8:11], s81 offen offset:1024 sc1
K 99,102,103
Mvaa 112,140,44
s_add_u32 s72, s72, s66
Ta 132,23,10368
Ta 134,23,15488
Mvaa 112,136,40
W 8
s_add_u32 s83, s52, 0
Ta 136,23,30848
Mvaa 116,144,32
s_mul_i32 s75, s67, 20480
Ta 138,23,35968
Mvaa 116,148,36
buffer_store_dwordx4 v[96:99], v1, s[8:11], s83 offen sc1
Ta 140,23,41088
Mvaa 124,148,52
Ta 142,23,46208
K 104,104,105
Mvaa 124,144,48
W 8
s_mul_i32 s66, s58, 2560
K 105,106,107
Mvaa 124,152,56
Ta 144,22,51360
Ta 146,22,56480
Mvaa 124,156,60
s_add_u32 s75, s75, s66
buffer_load_dwordx4 a[116:119], v1, s[8:11], s82 offen offset:1024 sc1
K 106,108,109
Mvaa 116,156,44
Ta 148,24,10400
Ta 150,24,15520
Mvaa 116,152,40
W 8
Ta 152,24,30880
K 107,110,111
Mvaa 128,128,32
s_add_u32 s75, s75, 131072
s_add_u32 s83, s52, 40960
Ta 154,24,36000
Mvaa 128,132,36
buffer_load_dwordx4 a[104:107], v1, s[8:11], s81 offen offset:2048 sc1
Ta 156,24,41120
Mvaa 136,132,52
s_xor_b32 s67, s61, 124
Ta 158,24,46240
Mvaa 136,128,48
W 8
Ta 128,21,51392
Ta 130,21,56512
Mvaa 136,136,56
Mvaa 136,140,60
s_mul_i32 s73, s67, 20480
buffer_store_dwordx4 v[104:107], v1, s[8:11], s83 offen sc1
K 112,112,113
Mvaa 128,140,44
Ta 132,23,10432
K 113,114,115
Mvaa 128,136,40
W 7
Ta 134,23,15552
K 114,116,117
Mvaa 132,144,32
s_mul_i32 s66, s58, 320
Ta 136,23,30912
Ta 138,23,36032
Mvaa 132,148,36
buffer_load_dwordx4 a[120:123], v1, s[8:11], s82 offen offset:2048 sc1
K 115,118,119
Mvaa 140,148,52
s_add_u32 s73, s73, s66
Ta 140,23,41152
Ta 142,23,46272
Mvaa 140,144,48
W 8
s_add_u32 s83, s52, 8192
Ta 144,22,51424
Mvaa 140,152,56
s_mul_i32 s76, s67, 20480
Ta 146,22,56544
Mvaa 140,156,60
buffer_load_dwordx4 a[108:111], v1, s[8:11], s81 offen offset:3072 sc1
Ta 148,24,10464
Mvaa 132,156,44
s_mul_i32 s66, s58, 2560
Ta 150,24,15584
Mvaa 132,152,40
W 8
Ta 152,24,30944
Ta 154,24,36064
Mvaa 144,128,32
Mvaa 144,132,36
s_add_u32 s76, s76, s66
buffer_store_dwordx4 v[112:115], v1, s[8:11], s83 offen sc1
K 120,120,121
Mvaa 152,132,52
K 121,122,123
K 122,124,125
Mvaa 152,128,48
W 6
Ta 156,24,41184
K 123,126,127
Mvaa 152,136,56
s_add_u32 s83, s52, 49152
)"
R"(s_add_u32 s76, s76, 131072
Ta 158,24,46304
Mvaa 152,140,60
buffer_load_dwordx4 a[124:127], v1, s[8:11], s82 offen offset:3072 sc1
Ta 128,21,51456
Mvaa 144,140,44
s_add_u32 s81, s58, 3
s_and_b32 s81, s81, 3
s_lshl_b32 s81, s81, 12
Mvaa 144,136,40
W 5
s_add_u32 s81, s81, s50
Ta 130,21,56576
Mvaa 148,144,32
Ta 132,23,10496
Ta 134,23,15616
Mvaa 148,148,36
buffer_load_dwordx4 a[184:187], v1, s[8:11], s81 offen offset:0 sc1
v_bfe_u32 v15, v1, 7, 1
Mvaa 156,148,52
s_add_u32 s82, s81, 131072
Ta 136,23,30976
Ta 138,23,36096
Mvaa 156,144,48
W 6
Ta 140,23,41216
v_mul_u32_u24 v14, 10240, v15
Mvaa 156,152,56
Ta 142,23,46336
v_lshrrev_b32 v15, 8, v1
Mvaa 156,156,60
buffer_store_dwordx4 v[120:123], v1, s[8:11], s83 offen sc1
K 128,128,129
Mvaa 148,156,44
K 129,130,131
K 130,132,133
Mvaa 148,152,40
W 4
Ta 144,22,51488
K 131,134,135
Mvaa 160,128,32
s_add_u32 s83, s52, 16384
Ta 146,22,56608
v_lshl_add_u32 v14, v15, 7, v14
Mvaa 160,132,36
buffer_load_dwordx4 a[200:203], v1, s[8:11], s82 offen offset:0 sc1
Ta 148,24,10528
Mvaa 168,132,52
Ta 150,24,15648
Ta 152,24,31008
Mvaa 168,128,48
W 5
Ta 154,24,36128
v_and_b32 v15, 0x70, v1
Mvaa 168,136,56
Ta 156,24,41248
Ta 158,24,46368
Mvaa 168,140,60
buffer_load_dwordx4 a[188:191], v1, s[8:11], s81 offen offset:1024 sc1
v_add_u32 v14, v14, v15
Mvaa 160,140,44
s_bitcmp1_b32 s14, 0
s_cselect_b32 s74, s73, s72
s_cselect_b32 s77, s76, s75
Ta 128,21,10240
Ta 130,21,15360
Mvaa 160,136,40
W 6
Ta 132,21,20480
Ta 134,21,25600
Mvaa 164,144,32
s_add_u32 s74, s74, 256
Ta 136,22,10272
Ta 138,22,15392
Mvaa 164,148,36
s_add_u32 s77, s77, 2048
buffer_store_dwordx4 v[128:131], v1, s[8:11], s83 offen sc1
K 136,136,137
Mvaa 172,148,52
K 137,138,139
K 138,140,141
Mvaa 172,144,48
W 6
Ta 140,22,20512
K 139,142,143
Mvaa 172,152,56
s_add_u32 s83, s52, 57344
Ta 142,22,25632
Ta 144,21,10304
Mvaa 172,156,60
s_lshl_b32 s66, s14, 6
buffer_load_dwordx4 a[204:207], v1, s[8:11], s82 offen offset:1024 sc1
Ta 146,21,15424
Mvaa 164,156,44
s_lshl_b32 s67, s14, 9
Ta 148,21,20544
Ta 150,21,25664
Mvaa 164,152,40
s_waitcnt vmcnt(21) lgkmcnt(8)
Ta 152,22,10336
Ta 154,22,15456
Mvaa 176,128,64
Mvaa 176,132,68
s_add_u32 s72, s72, s66
buffer_load_dwordx4 a[192:195], v1, s[8:11], s81 offen offset:2048 sc1
Ta 156,22,20576
Mvaa 184,132,84
Ta 158,22,25696
Mvaa 184,128,80
W 8
s_add_u32 s75, s75, s67
Ta 128,21,10368
Mvaa 188,136,80
Ta 130,21,15488
Mvaa 188,140,84
buffer_store_dwordx4 v[136:139], v1, s[8:11], s83 offen sc1
K 144,144,145
Mvaa 180,140,68
s_add_u32 s73, s73, s66
K 145,146,147
K 146,148,149
Mvaa 180,136,64
W 6
Ta 132,21,20608
K 147,150,151
Mvaa 192,144,64
s_add_u32 s83, s52, 24576
Ta 134,21,25728
Ta 136,22,10400
Mvaa 192,148,68
s_add_u32 s76, s76, s67
buffer_load_dwordx4 a[208:211], v1, s[8:11], s82 offen offset:2048 sc1
Ta 138,22,15520
Mvaa 200,148,84
Ta 140,22,20640
Ta 142,22,25760
Mvaa 200,144,80
W 8
Ta 144,21,10432
Ta 146,21,15552
Mvaa 204,152,80
Mvaa 204,156,84
buffer_store_dwordx4 v[144:147], v1, s[8:11], s83 offen sc1
K 152,152,153
Mvaa 196,156,68
K 153,154,155
K 154,156,157
Mvaa 196,152,64
W 6
Ta 148,21,20672
K 155,158,159
Mvaa 208,128,64
s_add_u32 s83, s52, 65536
Ta 150,21,25792
Ta 152,22,10464
Mvaa 208,132,68
buffer_load_dwordx4 a[196:199], v1, s[8:11], s81 offen offset:3072 sc1
Ta 154,22,15584
Mvaa 216,132,84
Ta 156,22,20704
Ta 158,22,25824
Mvaa 216,128,80
W 8
Ta 128,21,10496
Ta 130,21,15616
Mvaa 220,136,80
Mvaa 220,140,84
buffer_load_dwordx4 a[212:215], v1, s[8:11], s82 offen offset:3072 sc1
Ta 132,21,20736
Mvaa 212,140,68
Ta 134,21,25856
Mvaa 212,136,64
W 8
Ta 136,22,10528
Ta 138,22,15648
Mvaa 224,144,64
Mvaa 224,148,68
buffer_store_dwordx4 v[152:155], v1, s[8:11], s83 offen sc1
K 160,160,161
Mvaa 232,148,84
K 161,162,163
K 162,164,165
Mvaa 232,144,80
W 6
Ta 140,22,20768
K 163,166,167
Mvaa 236,152,80
s_add_u32 s83, s52, 32768
Ta 142,22,25888
Ta 144,21,30720
Mvaa 236,156,84
buffer_load_dwordx4 v[36:39], v20, s[16:19], s72 offen
Ta 146,21,35840
Mvaa 228,156,68
Ta 148,23,0
Ta 150,23,5120
Mvaa 228,152,64
W 8
Ta 152,22,30752
Ta 154,22,35872
Mvaa 240,128,64
Mvaa 240,132,68
buffer_load_dwordx4 v[44:47], v14, s[8:11], s75 offen sc1
Ta 156,24,32
Mvaa 248,132,84
Ta 158,24,5152
Mvaa 248,128,80
W 8
Ta 128,21,30784
Ta 130,21,35904
Mvaa 252,136,80
Mvaa 252,140,84
buffer_store_dwordx4 v[160:163], v1, s[8:11], s83 offen sc1
K 168,168,169
Mvaa 244,140,68
K 169,170,171
K 170,172,173
Mvaa 244,136,64
s_waitcnt vmcnt(27) lgkmcnt(6)
Ta 132,23,64
K 171,174,175
Mvaa 176,144,72
s_add_u32 s83, s52, 73728
Ta 134,23,5184
Ta 136,22,30816
Mvaa 176,148,76
buffer_load_dwordx4 v[52:55], v20, s[16:19], s73 offen
Ta 138,22,35936
Mvaa 184,148,92
Ta 140,24,96
Ta 142,24,5216
Mvaa 184,144,88
W 8
Ta 144,21,30848
Ta 146,21,35968
Mvaa 188,152,88
Mvaa 188,156,92
buffer_store_dwordx4 v[168:171], v1, s[8:11], s83 offen sc1
Ta 148,23,128
Mvaa 180,156,76
Ta 150,23,5248
Mvaa 180,152,72
W 8
Ta 152,22,30880
Ta 154,22,36000
Mvaa 192,128,72
Mvaa 192,132,76
buffer_load_dwordx4 v[60:63], v14, s[8:11], s76 offen sc1
Ta 156,24,160
Mvaa 200,132,92
Ta 158,24,5280
Mvaa 200,128,88
W 8
Ta 128,21,30912
Ta 130,21,36032
Mvaa 204,136,88
Mvaa 204,140,92
buffer_load_dwordx4 v[68:71], v20, s[16:19], s74 offen
Ta 132,23,192
Mvaa 196,140,76
Ta 134,23,5312
Mvaa 196,136,72
W 8
Ta 136,22,30944
Ta 138,22,36064
Mvaa 208,144,72
Mvaa 208,148,76
buffer_load_dwordx4 v[76:79], v14, s[8:11], s77 offen sc1
Ta 140,24,224
Mvaa 216,148,92
Ta 142,24,5344
Mvaa 216,144,88
W 8
Ta 144,21,30976
Ta 146,21,36096
Mvaa 220,152,88
Mvaa 220,156,92
buffer_load_dwordx2 v[12:13], v27, s[8:11], 0 offen offset:3584 sc1
Ta 148,23,256
Mvaa 212,156,76
Ta 150,23,5376
Mvaa 212,152,72
W 8
Ta 152,22,31008
Ta 154,22,36128
Mvaa 224,128,72
Mvaa 224,132,76
Ta 156,24,288
Ta 158,24,5408
Mvaa 232,132,92
Mvaa 232,128,88
W 8
Ta 128,23,20480
Ta 130,23,25600
Mvaa 236,136,88
Mvaa 236,140,92
Ta 132,23,51200
Ta 134,23,56320
Mvaa 228,140,76
Mvaa 228,136,72
W 8
Ta 136,25,0
Ta 138,25,5120
Mvaa 240,144,72
Mvaa 240,148,76
Ta 140,21,40960
Ta 142,21,46080
Mvaa 248,148,92
Mvaa 248,144,88
W 8
Ta 144,21,51200
Ta 146,21,56320
Mvaa 252,152,88
Mvaa 252,156,92
Ta 148,23,10240
Ta 150,23,15360
Mvaa 244,156,76
Mvaa 244,152,72
s_waitcnt vmcnt(28) lgkmcnt(8)
Ta 152,23,30720
Ta 154,23,35840
Mvaa 176,128,96
Mvaa 176,132,100
Ta 156,23,40960
Ta 158,23,46080
Mvaa 184,132,116
Mvaa 184,128,112
s_waitcnt vmcnt(22) lgkmcnt(8)
Ta 128,24,20512
Ta 130,24,25632
Mvaa 184,136,120
Mvaa 184,140,124
Ta 132,24,51232
Ta 134,24,56352
Mvaa 176,140,108
Mvaa 176,136,104
s_waitcnt vmcnt(16) lgkmcnt(8)
Ta 136,26,32
Ta 138,26,5152
Mvaa 176,144,184
Mvaa 176,148,188
Ta 140,22,40992
Ta 142,22,46112
Mvaa 184,148,204
s_waitcnt vmcnt(4)
v_lshlrev_b32 v2, 2, v1
v_mov_b64 v[28:29], s[12:13]
s_mov_b64 exec, 3
buffer_store_dwordx2 v[28:29], v2, s[8:11], s53 offen offset:0 sc1
s_mov_b64 exec, -1
Mvaa 184,144,200
W 8
Ta 144,22,51232
Ta 146,22,56352
Mvaa 184,152,208
Mvaa 184,156,212
Ta 148,24,10272
Ta 150,24,15392
Mvaa 176,156,196
Mvaa 176,152,192
W 8
Ta 152,24,30752
Ta 154,24,35872
Mvaa 180,128,96
Mvaa 180,132,100
Ta 156,24,40992
Ta 158,24,46112
Mvaa 188,132,116
Mvaa 188,128,112
W 8
Ta 128,23,20544
Ta 130,23,25664
Mvaa 188,136,120
Mvaa 188,140,124
Ta 132,23,51264
Ta 134,23,56384
Mvaa 180,140,108
Mvaa 180,136,104
W 8
Ta 136,25,64
Ta 138,25,5184
Mvaa 180,144,184
Mvaa 180,148,188
Ta 140,21,41024
Ta 142,21,46144
Mvaa 188,148,204
Mvaa 188,144,200
W 8
Ta 144,21,51264
Ta 146,21,56384
Mvaa 188,152,208
Mvaa 188,156,212
Ta 148,23,10304
Ta 150,23,15424
Mvaa 180,156,196
s_waitcnt vmcnt(1)
v_xor_b32 v12, s12, v12
v_xor_b32 v13, s13, v13
v_lshrrev_b32 v3, 11, v12
v_or_b32 v13, v13, v3
)"
R"(v_cmp_eq_u32 vcc, 0, v13
s_cmp_eq_u64 vcc, -1
s_cbranch_scc0 .LL_108%=
.LL_109%=:
v_bfe_u32 v13, v12, 4, 7
s_xor_b32 s66, s61, 124
v_cmp_eq_u32 vcc, s61, v13
v_cmp_eq_u32_e64 s[86:87], s66, v13
s_or_b32 s68, vcc_lo, s86
s_and_b32 s68, s68, 255
s_cbranch_scc1 .Lup_106%=
.Lupr_107%=:
Mvaa 180,152,192
W 8
Ta 152,23,30784
Ta 154,23,35904
Mvaa 192,128,96
v_lshlrev_b32 v32, 16, v36
Mvaa 192,132,100
Ta 156,23,41024
v_and_b32 v33, 0xffff0000, v36
Mvaa 200,132,116
Ta 158,23,46144
v_lshlrev_b32 v34, 16, v37
Mvaa 200,128,112
W 8
K 176,176,177
v_and_b32 v35, 0xffff0000, v37
Mvaa 200,136,120
Ta 128,24,20576
Ta 130,24,25696
Mvaa 200,140,124
buffer_load_dwordx2 v[16:17], v18, s[8:11], s63 offen sc1
v_lshlrev_b32 v36, 16, v38
Mvaa 192,140,108
K 177,178,179
v_and_b32 v37, 0xffff0000, v38
Mvaa 192,136,104
W 6
Ta 132,24,51296
K 178,180,181
Mvaa 192,144,184
Ta 134,24,56416
v_lshlrev_b32 v38, 16, v39
Mvaa 192,148,188
K 179,182,183
v_and_b32 v39, 0xffff0000, v39
Mvaa 200,148,204
s_add_u32 s83, s52, 327680
Ta 136,26,96
Ta 138,26,5216
Mvaa 200,144,200
W 6
v_lshlrev_b32 v40, 16, v44
v_and_b32 v41, 0xffff0000, v44
Mvaa 200,152,208
buffer_store_dwordx4 v[176:179], v1, s[8:11], s83 offen sc1
K 184,184,185
Mvaa 200,156,212
K 185,186,187
K 186,188,189
Mvaa 192,156,196
K 187,190,191
s_add_u32 s83, s52, 368640
buffer_store_dwordx4 v[184:187], v1, s[8:11], s83 offen sc1
Mvaa 192,152,192
Ta 140,22,41056
Ta 142,22,46176
W 4
Mvaa 196,128,96
v_lshlrev_b32 v42, 16, v45
v_and_b32 v43, 0xffff0000, v45
Mvaa 196,132,100
Ta 144,22,51296
Ta 146,22,56416
Mvaa 204,132,116
v_lshlrev_b32 v44, 16, v46
v_and_b32 v45, 0xffff0000, v46
Mvaa 204,128,112
Ta 148,24,10336
Ta 150,24,15456
W 4
Mvaa 204,136,120
v_lshlrev_b32 v46, 16, v47
v_and_b32 v47, 0xffff0000, v47
Mvaa 204,140,124
Ta 152,24,30816
Ta 154,24,35936
Mvaa 196,140,108
v_lshlrev_b32 v48, 16, v52
v_and_b32 v49, 0xffff0000, v52
Mvaa 196,136,104
Ta 156,24,41056
Ta 158,24,46176
W 4
Mvaa 196,144,184
v_lshlrev_b32 v50, 16, v53
v_and_b32 v51, 0xffff0000, v53
Mvaa 196,148,188
Ta 128,23,20608
Ta 130,23,25728
Mvaa 204,148,204
v_lshlrev_b32 v52, 16, v54
v_and_b32 v53, 0xffff0000, v54
Mvaa 204,144,200
Ta 132,23,51328
Ta 134,23,56448
W 4
Mvaa 204,152,208
v_lshlrev_b32 v54, 16, v55
v_and_b32 v55, 0xffff0000, v55
Mvaa 204,156,212
Ta 136,25,128
Ta 138,25,5248
Mvaa 196,156,196
v_lshlrev_b32 v56, 16, v60
v_and_b32 v57, 0xffff0000, v60
Mvaa 196,152,192
Ta 140,21,41088
Ta 142,21,46208
W 4
Mvaa 208,128,96
v_lshlrev_b32 v58, 16, v61
v_and_b32 v59, 0xffff0000, v61
Mvaa 208,132,100
Ta 144,21,51328
Ta 146,21,56448
Mvaa 216,132,116
v_lshlrev_b32 v60, 16, v62
v_and_b32 v61, 0xffff0000, v62
Mvaa 216,128,112
Ta 148,23,10368
Ta 150,23,15488
W 4
Mvaa 216,136,120
v_lshlrev_b32 v62, 16, v63
v_and_b32 v63, 0xffff0000, v63
Mvaa 216,140,124
Ta 152,23,30848
Ta 154,23,35968
Mvaa 208,140,108
v_lshlrev_b32 v64, 16, v68
v_and_b32 v65, 0xffff0000, v68
Mvaa 208,136,104
Ta 156,23,41088
Ta 158,23,46208
W 4
Mvaa 208,144,184
v_lshlrev_b32 v66, 16, v69
K 192,192,193
Mvaa 208,148,188
Ta 128,24,20640
v_and_b32 v67, 0xffff0000, v69
Mvaa 216,148,204
Ta 130,24,25760
K 193,194,195
Mvaa 216,144,200
Ta 132,24,51360
Ta 134,24,56480
W 4
Mvaa 216,152,208
v_lshlrev_b32 v68, 16, v70
K 194,196,197
Mvaa 216,156,212
v_and_b32 v69, 0xffff0000, v70
v_lshlrev_b32 v70, 16, v71
Mvaa 208,156,196
K 195,198,199
s_add_u32 s83, s52, 335872
buffer_store_dwordx4 v[192:195], v1, s[8:11], s83 offen sc1
K 200,200,201
K 201,202,203
K 202,204,205
K 203,206,207
s_add_u32 s83, s52, 376832
buffer_store_dwordx4 v[200:203], v1, s[8:11], s83 offen sc1
Mvaa 208,152,192
Ta 136,26,160
Ta 138,26,5280
Ta 140,22,41120
Ta 142,22,46240
W 4
Mvaa 212,128,96
s_waitcnt vmcnt(4)
v_cmp_eq_u64 vcc, s[12:13], v[16:17]
s_and_b32 s71, vcc_lo, 255
v_and_b32 v71, 0xffff0000, v71
v_lshlrev_b32 v72, 16, v76
Mvaa 212,132,100
s_lshl_b32 s92, s14, 3
s_lshr_b32 s92, s71, s92
s_mov_b32 s91, 134217728
Mvaa 220,132,116
s_bitcmp1_b32 s71, 0
s_cselect_b32 s94, s78, s91
v_and_b32 v73, 0xffff0000, v76
Mvaa 220,128,112
Ta 144,22,51360
Ta 146,22,56480
Ta 148,24,10400
Ta 150,24,15520
W 4
Mvaa 220,136,120
v_lshlrev_b32 v74, 16, v77
v_and_b32 v75, 0xffff0000, v77
Mvaa 220,140,124
v_lshlrev_b32 v76, 16, v78
v_and_b32 v77, 0xffff0000, v78
Mvaa 212,140,108
v_lshlrev_b32 v78, 16, v79
v_and_b32 v79, 0xffff0000, v79
Mvaa 212,136,104
Ta 152,24,30880
Ta 154,24,36000
Ta 156,24,41120
Ta 158,24,46240
W 4
Mvaa 212,144,184
Ta 128,23,20672
Ta 130,23,25792
Mvaa 212,148,188
buffer_load_dwordx4 a[0:3], v1, s[8:11], s94 offen offset:0 sc1
Ta 132,23,51392
Mvaa 220,148,204
Ta 134,23,56512
Ta 136,25,192
Mvaa 220,144,200
W 5
Ta 138,25,5312
Ta 140,21,41152
Mvaa 220,152,208
Ta 142,21,46272
Ta 144,21,51392
Mvaa 220,156,212
s_bitcmp1_b32 s92, 0
s_cbranch_scc0 .LL_111%=
buffer_load_dwordx4 a[216:219], v1, s[8:11], s65 offen offset:0 sc1
.LL_111%=:
Ta 146,21,56512
Mvaa 212,156,196
s_bitcmp1_b32 s71, 1
s_cselect_b32 s94, s78, s91
Ta 148,23,10432
Mvaa 212,152,192
W 7
Ta 150,23,15552
Ta 152,23,30912
Mvaa 224,128,96
Ta 154,23,36032
Mvaa 224,132,100
buffer_load_dwordx4 a[4:7], v1, s[8:11], s94 offen offset:1024 sc1
Ta 156,23,41152
Mvaa 232,132,116
Ta 158,23,46272
K 208,208,209
Mvaa 232,128,112
W 8
Ta 128,24,20704
Ta 130,24,25824
Mvaa 232,136,120
K 209,210,211
Mvaa 232,140,124
s_bitcmp1_b32 s92, 1
s_cbranch_scc0 .LL_112%=
buffer_load_dwordx4 a[220:223], v1, s[8:11], s65 offen offset:1024 sc1
.LL_112%=:
K 210,212,213
Mvaa 224,140,108
s_bitcmp1_b32 s71, 2
s_cselect_b32 s94, s78, s91
Ta 132,24,51424
Mvaa 224,136,104
W 7
Ta 134,24,56544
K 211,214,215
Mvaa 224,144,184
s_add_u32 s83, s52, 344064
Ta 136,26,224
Ta 138,26,5344
Mvaa 224,148,188
buffer_load_dwordx4 a[8:11], v1, s[8:11], s94 offen offset:2048 sc1
Ta 140,22,41184
Mvaa 232,148,204
Ta 142,22,46304
Mvaa 232,144,200
W 8
Ta 144,22,51424
Ta 146,22,56544
Mvaa 232,152,208
Mvaa 232,156,212
buffer_store_dwordx4 v[208:211], v1, s[8:11], s83 offen sc1
K 216,216,217
Mvaa 224,156,196
K 217,218,219
K 218,220,221
K 219,222,223
s_add_u32 s83, s52, 385024
buffer_store_dwordx4 v[216:219], v1, s[8:11], s83 offen sc1
Mvaa 224,152,192
W 6
Ta 148,24,10464
Ta 150,24,15584
Mvaa 228,128,96
Ta 152,24,30944
Ta 154,24,36064
Mvaa 228,132,100
Ta 156,24,41184
Ta 158,24,46304
Mvaa 236,132,116
s_bitcmp1_b32 s92, 2
s_cbranch_scc0 .LL_113%=
buffer_load_dwordx4 a[224:227], v1, s[8:11], s65 offen offset:2048 sc1
.LL_113%=:
Mvaa 236,128,112
W 8
s_bitcmp1_b32 s71, 3
s_cselect_b32 s94, s78, s91
Mvaa 236,136,120
Ta 128,23,20736
Ta 130,23,25856
Mvaa 236,140,124
Ta 132,23,51456
Ta 134,23,56576
Mvaa 228,140,108
buffer_load_dwordx4 a[12:15], v1, s[8:11], s94 offen offset:3072 sc1
Mvaa 228,136,104
W 8
Ta 136,25,256
Ta 138,25,5376
Mvaa 228,144,184
Mvaa 228,148,188
Ta 140,21,41216
Ta 142,21,46336
Mvaa 236,148,204
s_bitcmp1_b32 s92, 3
s_cbranch_scc0 .LL_114%=
buffer_load_dwordx4 a[228:231], v1, s[8:11], s65 offen offset:3072 sc1
.LL_114%=:
Mvaa 236,144,200
W 8
s_bitcmp1_b32 s71, 4
s_cselect_b32 s94, s78, s91
Mvaa 236,152,208
Ta 144,21,51456
Ta 146,21,56576
Mvaa 236,156,212
Ta 148,23,10496
Ta 150,23,15616
Mvaa 228,156,196
buffer_load_dwordx4 a[16:19], v19, s[8:11], s94 offen offset:0 sc1
Mvaa 228,152,192
W 8
Ta 152,23,30976
Ta 154,23,36096
Mvaa 240,128,96
Mvaa 240,132,100
Ta 156,23,41216
Ta 158,23,46336
Mvaa 248,132,116
s_bitcmp1_b32 s92, 4
s_cbranch_scc0 .LL_115%=
buffer_load_dwordx4 a[232:235], v19, s[8:11], s65 offen offset:0 sc1
.LL_115%=:
K 224,224,225
Mvaa 248,128,112
W 8
s_bitcmp1_b32 s71, 5
s_cselect_b32 s94, s78, s91
Mvaa 248,136,120
Ta 128,24,20768
Ta 130,24,25888
Mvaa 248,140,124
K 225,226,227
K 226,228,229
Mvaa 240,140,108
buffer_load_dwordx4 a[20:23], v19, s[8:11], s94 offen offset:1024 sc1
Ta 132,24,51488
Mvaa 240,136,104
W 7
Ta 134,24,56608
K 227,230,231
Mvaa 240,144,184
s_add_u32 s83, s52, 352256
Ta 136,26,288
Ta 138,26,5408
Mvaa 240,148,188
Ta 140,22,41248
Ta 142,22,46368
Mvaa 248,148,204
s_bitcmp1_b32 s92, 5
s_cbranch_scc0 .LL_116%=
buffer_load_dwordx4 a[236:239], v19, s[8:11], s65 offen offset:1024 sc1
.LL_116%=:
Mvaa 248,144,200
W 8
s_bitcmp1_b32 s71, 6
s_cselect_b32 s94, s78, s91
Mvaa 248,152,208
Ta 144,22,51488
Ta 146,22,56608
)"
R"(Mvaa 248,156,212
Ta 148,24,10528
Ta 150,24,15648
Mvaa 240,156,196
buffer_store_dwordx4 v[224:227], v1, s[8:11], s83 offen sc1
K 232,232,233
K 233,234,235
K 234,236,237
K 235,238,239
s_add_u32 s83, s52, 393216
buffer_store_dwordx4 v[232:235], v1, s[8:11], s83 offen sc1
Mvaa 240,152,192
W 8
Ta 152,24,31008
Ta 154,24,36128
Mvaa 244,128,96
Mvaa 244,132,100
Ta 156,24,41248
Ta 158,24,46368
Mvaa 252,132,116
buffer_load_dwordx4 a[24:27], v19, s[8:11], s94 offen offset:2048 sc1
Mvaa 252,128,112
W 8
Mvaa 252,136,120
Mvaa 252,140,124
Mvaa 244,140,108
s_bitcmp1_b32 s92, 6
s_cbranch_scc0 .LL_117%=
buffer_load_dwordx4 a[240:243], v19, s[8:11], s65 offen offset:2048 sc1
.LL_117%=:
Mvaa 244,136,104
W 4
s_bitcmp1_b32 s71, 7
s_cselect_b32 s94, s78, s91
Mvaa 244,144,184
Mvaa 244,148,188
Mvaa 252,148,204
buffer_load_dwordx4 a[28:31], v19, s[8:11], s94 offen offset:3072 sc1
Mvaa 252,144,200
W 0
Mvaa 252,152,208
Mvaa 252,156,212
Mvaa 244,156,196
s_bitcmp1_b32 s92, 7
s_cbranch_scc0 .LL_118%=
buffer_load_dwordx4 a[244:247], v19, s[8:11], s65 offen offset:3072 sc1
.LL_118%=:
Mvaa 244,152,192
K 240,240,241
K 241,242,243
s_nop 5
K 242,244,245
K 243,246,247
s_add_u32 s83, s52, 360448
buffer_store_dwordx4 v[240:243], v1, s[8:11], s83 offen sc1
K 248,248,249
K 249,250,251
K 250,252,253
K 251,254,255
s_add_u32 s83, s52, 401408
buffer_store_dwordx4 v[248:251], v1, s[8:11], s83 offen sc1
.Ltend_83%=:
buffer_load_dwordx2 v[16:17], v18, s[8:11], s63 offen sc1
s_cmp_lg_u32 s68, 0
s_cbranch_scc1 .Lup_119%=
.Lupr_120%=:
s_lshl_b32 s66, 0x101, s59
s_waitcnt vmcnt(1)
s_cmp_eq_u32 s64, 0
s_cbranch_scc0 .LL_125%=
v_lshlrev_b32 v2, 2, v1
v_mov_b64 v[28:29], s[12:13]
s_mov_b64 exec, 3
buffer_store_dwordx2 v[28:29], v2, s[8:11], s53 offen offset:512 sc1
s_mov_b64 exec, -1
.LL_125%=:
s_barrier
s_waitcnt vmcnt(1)
v_cmp_eq_u64 vcc, s[12:13], v[16:17]
s_or_b32 s67, vcc_lo, s66
s_cmp_eq_u32 s14, 0
s_cbranch_scc1 .LL_127%=
s_cmp_eq_u32 s14, 1
s_cbranch_scc1 .LL_128%=
s_mov_b32 s84, 0
.LL_129%=:
s_mov_b32 s70, s71
s_andn2_b32 s69, s67, s71
s_and_b32 s69, s69, 0xffff
s_cbranch_scc0 .LL_132%=
s_and_b32 s80, s69, 255
s_cbranch_scc0 .LL_133%=
s_bitcmp1_b32 s69, 0
s_cbranch_scc0 .LL_134%=
buffer_load_dwordx4 a[0:3], v1, s[8:11], s78 offen offset:0 sc1
.LL_134%=:
s_bitcmp1_b32 s69, 1
s_cbranch_scc0 .LL_135%=
buffer_load_dwordx4 a[4:7], v1, s[8:11], s78 offen offset:1024 sc1
.LL_135%=:
s_bitcmp1_b32 s69, 2
s_cbranch_scc0 .LL_136%=
buffer_load_dwordx4 a[8:11], v1, s[8:11], s78 offen offset:2048 sc1
.LL_136%=:
s_bitcmp1_b32 s69, 3
s_cbranch_scc0 .LL_137%=
buffer_load_dwordx4 a[12:15], v1, s[8:11], s78 offen offset:3072 sc1
.LL_137%=:
s_bitcmp1_b32 s69, 4
s_cbranch_scc0 .LL_138%=
buffer_load_dwordx4 a[16:19], v19, s[8:11], s78 offen offset:0 sc1
.LL_138%=:
s_bitcmp1_b32 s69, 5
s_cbranch_scc0 .LL_139%=
buffer_load_dwordx4 a[20:23], v19, s[8:11], s78 offen offset:1024 sc1
.LL_139%=:
s_bitcmp1_b32 s69, 6
s_cbranch_scc0 .LL_140%=
buffer_load_dwordx4 a[24:27], v19, s[8:11], s78 offen offset:2048 sc1
.LL_140%=:
s_bitcmp1_b32 s69, 7
s_cbranch_scc0 .LL_141%=
buffer_load_dwordx4 a[28:31], v19, s[8:11], s78 offen offset:3072 sc1
.LL_141%=:
.LL_133%=:
s_and_b32 s80, s69, 65280
s_cbranch_scc0 .LL_142%=
s_bitcmp1_b32 s69, 8
s_cbranch_scc0 .LL_143%=
buffer_load_dwordx4 a[32:35], v1, s[8:11], s79 offen offset:0 sc1
.LL_143%=:
s_bitcmp1_b32 s69, 9
s_cbranch_scc0 .LL_144%=
buffer_load_dwordx4 a[36:39], v1, s[8:11], s79 offen offset:1024 sc1
.LL_144%=:
s_bitcmp1_b32 s69, 10
s_cbranch_scc0 .LL_145%=
buffer_load_dwordx4 a[40:43], v1, s[8:11], s79 offen offset:2048 sc1
.LL_145%=:
s_bitcmp1_b32 s69, 11
s_cbranch_scc0 .LL_146%=
buffer_load_dwordx4 a[44:47], v1, s[8:11], s79 offen offset:3072 sc1
.LL_146%=:
s_bitcmp1_b32 s69, 12
s_cbranch_scc0 .LL_147%=
buffer_load_dwordx4 a[48:51], v19, s[8:11], s79 offen offset:0 sc1
.LL_147%=:
s_bitcmp1_b32 s69, 13
s_cbranch_scc0 .LL_148%=
buffer_load_dwordx4 a[52:55], v19, s[8:11], s79 offen offset:1024 sc1
.LL_148%=:
s_bitcmp1_b32 s69, 14
s_cbranch_scc0 .LL_149%=
buffer_load_dwordx4 a[56:59], v19, s[8:11], s79 offen offset:2048 sc1
.LL_149%=:
s_bitcmp1_b32 s69, 15
s_cbranch_scc0 .LL_150%=
buffer_load_dwordx4 a[60:63], v19, s[8:11], s79 offen offset:3072 sc1
.LL_150%=:
.LL_142%=:
s_or_b32 s71, s71, s69
.LL_132%=:
s_andn2_b32 s80, 0xffff, s71
s_cbranch_scc0 .LL_131%=
s_sleep 4
buffer_load_dwordx2 v[16:17], v18, s[8:11], s63 offen sc1
s_and_b32 s80, s70, 0x100ff
s_cmp_eq_u32 s80, 0xff
s_cbranch_scc0 .LL_130%=
Zvva 80,4,0
Zvva 84,8,0
Mvva 80,4,4
Mvva 84,8,4
Mvva 80,4,8
Mvva 84,8,8
Mvva 80,4,12
Mvva 84,8,12
Mvva 80,4,16
Mvva 84,8,16
Mvva 80,4,20
Mvva 84,8,20
Mvva 80,4,24
Mvva 84,8,24
Mvva 80,4,28
Mvva 84,8,28
s_nop 6
K 160,80,81
K 161,82,83
K 162,84,85
K 163,86,87
v_lshlrev_b32 v80, 16, v160
v_and_b32 v81, 0xffff0000, v160
v_lshlrev_b32 v82, 16, v161
v_and_b32 v83, 0xffff0000, v161
v_lshlrev_b32 v84, 16, v162
v_and_b32 v85, 0xffff0000, v162
v_lshlrev_b32 v86, 16, v163
v_and_b32 v87, 0xffff0000, v163
v_pk_add_f32 v[80:81], v[32:33], v[80:81]
v_pk_add_f32 v[82:83], v[34:35], v[82:83]
v_pk_add_f32 v[84:85], v[36:37], v[84:85]
v_pk_add_f32 v[86:87], v[38:39], v[86:87]
K 160,80,81
K 161,82,83
K 162,84,85
K 163,86,87
v_lshlrev_b32 v80, 16, v160
v_and_b32 v81, 0xffff0000, v160
v_lshlrev_b32 v82, 16, v161
v_and_b32 v83, 0xffff0000, v161
v_lshlrev_b32 v84, 16, v162
v_and_b32 v85, 0xffff0000, v162
v_lshlrev_b32 v86, 16, v163
v_and_b32 v87, 0xffff0000, v163
v_pk_add_f32 v[80:81], v[40:41], v[80:81]
v_pk_add_f32 v[82:83], v[42:43], v[82:83]
v_pk_add_f32 v[84:85], v[44:45], v[84:85]
v_pk_add_f32 v[86:87], v[46:47], v[86:87]
K 80,80,81
K 81,82,83
K 82,84,85
K 83,86,87
buffer_store_dwordx4 v[80:83], v20, s[20:23], s72 offen nt
s_bitset1_b32 s71, 16
.LL_130%=:
s_add_u32 s84, s84, 1
s_waitcnt vmcnt(0)
v_cmp_eq_u64 vcc, s[12:13], v[16:17]
s_or_b32 s67, vcc_lo, s66
s_cmp_lt_u32 s84, 4194304
s_cbranch_scc1 .LL_129%=
.LL_131%=:
s_bitcmp1_b32 s71, 16
s_cbranch_scc1 .LL_151%=
s_and_b32 s80, s70, 0xff
s_cmp_eq_u32 s80, 0xff
s_cbranch_scc1 .LL_152%=
s_waitcnt vmcnt(0)
.LL_152%=:
Zvva 80,4,0
Zvva 84,8,0
Mvva 80,4,4
Mvva 84,8,4
Mvva 80,4,8
Mvva 84,8,8
Mvva 80,4,12
Mvva 84,8,12
Mvva 80,4,16
Mvva 84,8,16
Mvva 80,4,20
Mvva 84,8,20
Mvva 80,4,24
Mvva 84,8,24
Mvva 80,4,28
Mvva 84,8,28
s_nop 6
K 160,80,81
K 161,82,83
K 162,84,85
K 163,86,87
v_lshlrev_b32 v80, 16, v160
v_and_b32 v81, 0xffff0000, v160
v_lshlrev_b32 v82, 16, v161
v_and_b32 v83, 0xffff0000, v161
v_lshlrev_b32 v84, 16, v162
v_and_b32 v85, 0xffff0000, v162
v_lshlrev_b32 v86, 16, v163
v_and_b32 v87, 0xffff0000, v163
v_pk_add_f32 v[80:81], v[32:33], v[80:81]
v_pk_add_f32 v[82:83], v[34:35], v[82:83]
v_pk_add_f32 v[84:85], v[36:37], v[84:85]
v_pk_add_f32 v[86:87], v[38:39], v[86:87]
K 160,80,81
K 161,82,83
K 162,84,85
K 163,86,87
v_lshlrev_b32 v80, 16, v160
v_and_b32 v81, 0xffff0000, v160
v_lshlrev_b32 v82, 16, v161
v_and_b32 v83, 0xffff0000, v161
v_lshlrev_b32 v84, 16, v162
v_and_b32 v85, 0xffff0000, v162
v_lshlrev_b32 v86, 16, v163
v_and_b32 v87, 0xffff0000, v163
v_pk_add_f32 v[80:81], v[40:41], v[80:81]
v_pk_add_f32 v[82:83], v[42:43], v[82:83]
v_pk_add_f32 v[84:85], v[44:45], v[84:85]
v_pk_add_f32 v[86:87], v[46:47], v[86:87]
K 80,80,81
K 81,82,83
K 82,84,85
K 83,86,87
buffer_store_dwordx4 v[80:83], v20, s[20:23], s72 offen nt
s_waitcnt vmcnt(1)
s_nop 0
Zvva 80,4,32
Zvva 84,8,32
Mvva 80,4,36
Mvva 84,8,36
Mvva 80,4,40
Mvva 84,8,40
Mvva 80,4,44
Mvva 84,8,44
Mvva 80,4,48
Mvva 84,8,48
Mvva 80,4,52
Mvva 84,8,52
Mvva 80,4,56
Mvva 84,8,56
Mvva 80,4,60
Mvva 84,8,60
s_nop 6
K 160,80,81
K 161,82,83
K 162,84,85
K 163,86,87
v_lshlrev_b32 v80, 16, v160
v_and_b32 v81, 0xffff0000, v160
v_lshlrev_b32 v82, 16, v161
v_and_b32 v83, 0xffff0000, v161
v_lshlrev_b32 v84, 16, v162
v_and_b32 v85, 0xffff0000, v162
v_lshlrev_b32 v86, 16, v163
v_and_b32 v87, 0xffff0000, v163
v_pk_add_f32 v[80:81], v[48:49], v[80:81]
v_pk_add_f32 v[82:83], v[50:51], v[82:83]
v_pk_add_f32 v[84:85], v[52:53], v[84:85]
v_pk_add_f32 v[86:87], v[54:55], v[86:87]
K 160,80,81
K 161,82,83
K 162,84,85
K 163,86,87
v_lshlrev_b32 v80, 16, v160
v_and_b32 v81, 0xffff0000, v160
v_lshlrev_b32 v82, 16, v161
v_and_b32 v83, 0xffff0000, v161
v_lshlrev_b32 v84, 16, v162
v_and_b32 v85, 0xffff0000, v162
v_lshlrev_b32 v86, 16, v163
v_and_b32 v87, 0xffff0000, v163
v_pk_add_f32 v[80:81], v[56:57], v[80:81]
v_pk_add_f32 v[82:83], v[58:59], v[82:83]
v_pk_add_f32 v[84:85], v[60:61], v[84:85]
v_pk_add_f32 v[86:87], v[62:63], v[86:87]
K 80,80,81
K 81,82,83
K 82,84,85
K 83,86,87
buffer_store_dwordx4 v[80:83], v20, s[20:23], s73 offen nt
s_branch .LL_126%=
.LL_151%=:
s_waitcnt vmcnt(0)
Zvva 80,4,32
Zvva 84,8,32
Mvva 80,4,36
Mvva 84,8,36
Mvva 80,4,40
Mvva 84,8,40
Mvva 80,4,44
Mvva 84,8,44
Mvva 80,4,48
Mvva 84,8,48
Mvva 80,4,52
Mvva 84,8,52
Mvva 80,4,56
Mvva 84,8,56
Mvva 80,4,60
Mvva 84,8,60
s_nop 6
K 160,80,81
K 161,82,83
K 162,84,85
K 163,86,87
v_lshlrev_b32 v80, 16, v160
v_and_b32 v81, 0xffff0000, v160
v_lshlrev_b32 v82, 16, v161
v_and_b32 v83, 0xffff0000, v161
v_lshlrev_b32 v84, 16, v162
v_and_b32 v85, 0xffff0000, v162
v_lshlrev_b32 v86, 16, v163
v_and_b32 v87, 0xffff0000, v163
v_pk_add_f32 v[80:81], v[48:49], v[80:81]
v_pk_add_f32 v[82:83], v[50:51], v[82:83]
v_pk_add_f32 v[84:85], v[52:53], v[84:85]
v_pk_add_f32 v[86:87], v[54:55], v[86:87]
K 160,80,81
K 161,82,83
K 162,84,85
K 163,86,87
v_lshlrev_b32 v80, 16, v160
v_and_b32 v81, 0xffff0000, v160
v_lshlrev_b32 v82, 16, v161
v_and_b32 v83, 0xffff0000, v161
v_lshlrev_b32 v84, 16, v162
v_and_b32 v85, 0xffff0000, v162
v_lshlrev_b32 v86, 16, v163
v_and_b32 v87, 0xffff0000, v163
v_pk_add_f32 v[80:81], v[56:57], v[80:81]
v_pk_add_f32 v[82:83], v[58:59], v[82:83]
v_pk_add_f32 v[84:85], v[60:61], v[84:85]
)"
R"(v_pk_add_f32 v[86:87], v[62:63], v[86:87]
K 80,80,81
K 81,82,83
K 82,84,85
K 83,86,87
buffer_store_dwordx4 v[80:83], v20, s[20:23], s73 offen nt
s_branch .LL_126%=
.LL_127%=:
s_mov_b32 s84, 0
.LL_153%=:
s_mov_b32 s70, s71
s_andn2_b32 s69, s67, s71
s_and_b32 s69, s69, 0xffff
s_cbranch_scc0 .LL_156%=
s_and_b32 s80, s69, 255
s_cbranch_scc0 .LL_157%=
s_bitcmp1_b32 s69, 0
s_cbranch_scc0 .LL_158%=
buffer_load_dwordx4 a[0:3], v1, s[8:11], s78 offen offset:0 sc1
buffer_load_dwordx4 a[216:219], v1, s[8:11], s65 offen offset:0 sc1
.LL_158%=:
s_bitcmp1_b32 s69, 1
s_cbranch_scc0 .LL_159%=
buffer_load_dwordx4 a[4:7], v1, s[8:11], s78 offen offset:1024 sc1
buffer_load_dwordx4 a[220:223], v1, s[8:11], s65 offen offset:1024 sc1
.LL_159%=:
s_bitcmp1_b32 s69, 2
s_cbranch_scc0 .LL_160%=
buffer_load_dwordx4 a[8:11], v1, s[8:11], s78 offen offset:2048 sc1
buffer_load_dwordx4 a[224:227], v1, s[8:11], s65 offen offset:2048 sc1
.LL_160%=:
s_bitcmp1_b32 s69, 3
s_cbranch_scc0 .LL_161%=
buffer_load_dwordx4 a[12:15], v1, s[8:11], s78 offen offset:3072 sc1
buffer_load_dwordx4 a[228:231], v1, s[8:11], s65 offen offset:3072 sc1
.LL_161%=:
s_bitcmp1_b32 s69, 4
s_cbranch_scc0 .LL_162%=
buffer_load_dwordx4 a[16:19], v19, s[8:11], s78 offen offset:0 sc1
buffer_load_dwordx4 a[232:235], v19, s[8:11], s65 offen offset:0 sc1
.LL_162%=:
s_bitcmp1_b32 s69, 5
s_cbranch_scc0 .LL_163%=
buffer_load_dwordx4 a[20:23], v19, s[8:11], s78 offen offset:1024 sc1
buffer_load_dwordx4 a[236:239], v19, s[8:11], s65 offen offset:1024 sc1
.LL_163%=:
s_bitcmp1_b32 s69, 6
s_cbranch_scc0 .LL_164%=
buffer_load_dwordx4 a[24:27], v19, s[8:11], s78 offen offset:2048 sc1
buffer_load_dwordx4 a[240:243], v19, s[8:11], s65 offen offset:2048 sc1
.LL_164%=:
s_bitcmp1_b32 s69, 7
s_cbranch_scc0 .LL_165%=
buffer_load_dwordx4 a[28:31], v19, s[8:11], s78 offen offset:3072 sc1
buffer_load_dwordx4 a[244:247], v19, s[8:11], s65 offen offset:3072 sc1
.LL_165%=:
.LL_157%=:
s_and_b32 s80, s69, 65280
s_cbranch_scc0 .LL_166%=
s_bitcmp1_b32 s69, 8
s_cbranch_scc0 .LL_167%=
buffer_load_dwordx4 a[32:35], v1, s[8:11], s79 offen offset:0 sc1
.LL_167%=:
s_bitcmp1_b32 s69, 9
s_cbranch_scc0 .LL_168%=
buffer_load_dwordx4 a[36:39], v1, s[8:11], s79 offen offset:1024 sc1
.LL_168%=:
s_bitcmp1_b32 s69, 10
s_cbranch_scc0 .LL_169%=
buffer_load_dwordx4 a[40:43], v1, s[8:11], s79 offen offset:2048 sc1
.LL_169%=:
s_bitcmp1_b32 s69, 11
s_cbranch_scc0 .LL_170%=
buffer_load_dwordx4 a[44:47], v1, s[8:11], s79 offen offset:3072 sc1
.LL_170%=:
s_bitcmp1_b32 s69, 12
s_cbranch_scc0 .LL_171%=
buffer_load_dwordx4 a[48:51], v19, s[8:11], s79 offen offset:0 sc1
.LL_171%=:
s_bitcmp1_b32 s69, 13
s_cbranch_scc0 .LL_172%=
buffer_load_dwordx4 a[52:55], v19, s[8:11], s79 offen offset:1024 sc1
.LL_172%=:
s_bitcmp1_b32 s69, 14
s_cbranch_scc0 .LL_173%=
buffer_load_dwordx4 a[56:59], v19, s[8:11], s79 offen offset:2048 sc1
.LL_173%=:
s_bitcmp1_b32 s69, 15
s_cbranch_scc0 .LL_174%=
buffer_load_dwordx4 a[60:63], v19, s[8:11], s79 offen offset:3072 sc1
.LL_174%=:
.LL_166%=:
s_or_b32 s71, s71, s69
.LL_156%=:
s_andn2_b32 s80, 0xffff, s71
s_cbranch_scc0 .LL_155%=
s_sleep 4
buffer_load_dwordx2 v[16:17], v18, s[8:11], s63 offen sc1
s_and_b32 s80, s70, 0x100ff
s_cmp_eq_u32 s80, 0xff
s_cbranch_scc0 .LL_154%=
Zvva 80,4,0
Zvva 84,8,0
Zvva 88,4,216
Zvva 92,8,216
Mvva 80,4,4
Mvva 84,8,4
Mvva 88,4,220
Mvva 92,8,220
Mvva 80,4,8
Mvva 84,8,8
Mvva 88,4,224
Mvva 92,8,224
Mvva 80,4,12
Mvva 84,8,12
Mvva 88,4,228
Mvva 92,8,228
Mvva 80,4,16
Mvva 84,8,16
Mvva 88,4,232
Mvva 92,8,232
Mvva 80,4,20
Mvva 84,8,20
Mvva 88,4,236
Mvva 92,8,236
Mvva 80,4,24
Mvva 84,8,24
Mvva 88,4,240
Mvva 92,8,240
Mvva 80,4,28
Mvva 84,8,28
Mvva 88,4,244
Mvva 92,8,244
s_nop 4
K 160,80,81
K 161,82,83
K 162,84,85
K 163,86,87
v_lshlrev_b32 v80, 16, v160
v_and_b32 v81, 0xffff0000, v160
v_lshlrev_b32 v82, 16, v161
v_and_b32 v83, 0xffff0000, v161
v_lshlrev_b32 v84, 16, v162
v_and_b32 v85, 0xffff0000, v162
v_lshlrev_b32 v86, 16, v163
v_and_b32 v87, 0xffff0000, v163
v_pk_add_f32 v[80:81], v[32:33], v[80:81]
v_pk_add_f32 v[82:83], v[34:35], v[82:83]
v_pk_add_f32 v[84:85], v[36:37], v[84:85]
v_pk_add_f32 v[86:87], v[38:39], v[86:87]
K 160,80,81
K 161,82,83
K 162,84,85
K 163,86,87
v_lshlrev_b32 v80, 16, v160
v_and_b32 v81, 0xffff0000, v160
v_lshlrev_b32 v82, 16, v161
v_and_b32 v83, 0xffff0000, v161
v_lshlrev_b32 v84, 16, v162
v_and_b32 v85, 0xffff0000, v162
v_lshlrev_b32 v86, 16, v163
v_and_b32 v87, 0xffff0000, v163
v_pk_add_f32 v[80:81], v[40:41], v[80:81]
v_pk_add_f32 v[82:83], v[42:43], v[82:83]
v_pk_add_f32 v[84:85], v[44:45], v[84:85]
v_pk_add_f32 v[86:87], v[46:47], v[86:87]
K 80,80,81
K 81,82,83
K 82,84,85
K 83,86,87
buffer_store_dwordx4 v[80:83], v20, s[20:23], s72 offen nt
K 160,88,89
K 161,90,91
K 162,92,93
K 163,94,95
v_lshlrev_b32 v88, 16, v160
v_and_b32 v89, 0xffff0000, v160
v_lshlrev_b32 v90, 16, v161
v_and_b32 v91, 0xffff0000, v161
v_lshlrev_b32 v92, 16, v162
v_and_b32 v93, 0xffff0000, v162
v_lshlrev_b32 v94, 16, v163
v_and_b32 v95, 0xffff0000, v163
v_pk_add_f32 v[88:89], v[64:65], v[88:89]
v_pk_add_f32 v[90:91], v[66:67], v[90:91]
v_pk_add_f32 v[92:93], v[68:69], v[92:93]
v_pk_add_f32 v[94:95], v[70:71], v[94:95]
K 160,88,89
K 161,90,91
K 162,92,93
K 163,94,95
v_lshlrev_b32 v88, 16, v160
v_and_b32 v89, 0xffff0000, v160
v_lshlrev_b32 v90, 16, v161
v_and_b32 v91, 0xffff0000, v161
v_lshlrev_b32 v92, 16, v162
v_and_b32 v93, 0xffff0000, v162
v_lshlrev_b32 v94, 16, v163
v_and_b32 v95, 0xffff0000, v163
v_pk_add_f32 v[88:89], v[72:73], v[88:89]
v_pk_add_f32 v[90:91], v[74:75], v[90:91]
v_pk_add_f32 v[92:93], v[76:77], v[92:93]
v_pk_add_f32 v[94:95], v[78:79], v[94:95]
K 88,88,89
K 89,90,91
K 90,92,93
K 91,94,95
buffer_store_dwordx4 v[88:91], v20, s[20:23], s74 offen nt
s_bitset1_b32 s71, 16
.LL_154%=:
s_add_u32 s84, s84, 1
s_waitcnt vmcnt(0)
v_cmp_eq_u64 vcc, s[12:13], v[16:17]
s_or_b32 s67, vcc_lo, s66
s_cmp_lt_u32 s84, 4194304
s_cbranch_scc1 .LL_153%=
.LL_155%=:
s_bitcmp1_b32 s71, 16
s_cbranch_scc1 .LL_175%=
s_and_b32 s80, s70, 0xff
s_cmp_eq_u32 s80, 0xff
s_cbranch_scc1 .LL_176%=
s_waitcnt vmcnt(0)
.LL_176%=:
Zvva 80,4,0
Zvva 84,8,0
Zvva 88,4,216
Zvva 92,8,216
Mvva 80,4,4
Mvva 84,8,4
Mvva 88,4,220
Mvva 92,8,220
Mvva 80,4,8
Mvva 84,8,8
Mvva 88,4,224
Mvva 92,8,224
Mvva 80,4,12
Mvva 84,8,12
Mvva 88,4,228
Mvva 92,8,228
Mvva 80,4,16
Mvva 84,8,16
Mvva 88,4,232
Mvva 92,8,232
Mvva 80,4,20
Mvva 84,8,20
Mvva 88,4,236
Mvva 92,8,236
Mvva 80,4,24
Mvva 84,8,24
Mvva 88,4,240
Mvva 92,8,240
Mvva 80,4,28
Mvva 84,8,28
Mvva 88,4,244
Mvva 92,8,244
s_nop 4
K 160,80,81
K 161,82,83
K 162,84,85
K 163,86,87
v_lshlrev_b32 v80, 16, v160
v_and_b32 v81, 0xffff0000, v160
v_lshlrev_b32 v82, 16, v161
v_and_b32 v83, 0xffff0000, v161
v_lshlrev_b32 v84, 16, v162
v_and_b32 v85, 0xffff0000, v162
v_lshlrev_b32 v86, 16, v163
v_and_b32 v87, 0xffff0000, v163
v_pk_add_f32 v[80:81], v[32:33], v[80:81]
v_pk_add_f32 v[82:83], v[34:35], v[82:83]
v_pk_add_f32 v[84:85], v[36:37], v[84:85]
v_pk_add_f32 v[86:87], v[38:39], v[86:87]
K 160,80,81
K 161,82,83
K 162,84,85
K 163,86,87
v_lshlrev_b32 v80, 16, v160
v_and_b32 v81, 0xffff0000, v160
v_lshlrev_b32 v82, 16, v161
v_and_b32 v83, 0xffff0000, v161
v_lshlrev_b32 v84, 16, v162
v_and_b32 v85, 0xffff0000, v162
v_lshlrev_b32 v86, 16, v163
v_and_b32 v87, 0xffff0000, v163
v_pk_add_f32 v[80:81], v[40:41], v[80:81]
v_pk_add_f32 v[82:83], v[42:43], v[82:83]
v_pk_add_f32 v[84:85], v[44:45], v[84:85]
v_pk_add_f32 v[86:87], v[46:47], v[86:87]
K 80,80,81
K 81,82,83
K 82,84,85
K 83,86,87
buffer_store_dwordx4 v[80:83], v20, s[20:23], s72 offen nt
K 160,88,89
K 161,90,91
K 162,92,93
K 163,94,95
v_lshlrev_b32 v88, 16, v160
v_and_b32 v89, 0xffff0000, v160
v_lshlrev_b32 v90, 16, v161
v_and_b32 v91, 0xffff0000, v161
v_lshlrev_b32 v92, 16, v162
v_and_b32 v93, 0xffff0000, v162
v_lshlrev_b32 v94, 16, v163
v_and_b32 v95, 0xffff0000, v163
v_pk_add_f32 v[88:89], v[64:65], v[88:89]
v_pk_add_f32 v[90:91], v[66:67], v[90:91]
v_pk_add_f32 v[92:93], v[68:69], v[92:93]
v_pk_add_f32 v[94:95], v[70:71], v[94:95]
K 160,88,89
K 161,90,91
K 162,92,93
K 163,94,95
v_lshlrev_b32 v88, 16, v160
v_and_b32 v89, 0xffff0000, v160
v_lshlrev_b32 v90, 16, v161
v_and_b32 v91, 0xffff0000, v161
v_lshlrev_b32 v92, 16, v162
v_and_b32 v93, 0xffff0000, v162
v_lshlrev_b32 v94, 16, v163
v_and_b32 v95, 0xffff0000, v163
v_pk_add_f32 v[88:89], v[72:73], v[88:89]
v_pk_add_f32 v[90:91], v[74:75], v[90:91]
v_pk_add_f32 v[92:93], v[76:77], v[92:93]
v_pk_add_f32 v[94:95], v[78:79], v[94:95]
K 88,88,89
K 89,90,91
K 90,92,93
K 91,94,95
buffer_store_dwordx4 v[88:91], v20, s[20:23], s74 offen nt
s_waitcnt vmcnt(2)
Zvva 80,4,32
Zvva 84,8,32
Mvva 80,4,36
Mvva 84,8,36
Mvva 80,4,40
Mvva 84,8,40
Mvva 80,4,44
Mvva 84,8,44
Mvva 80,4,48
Mvva 84,8,48
Mvva 80,4,52
Mvva 84,8,52
Mvva 80,4,56
Mvva 84,8,56
Mvva 80,4,60
Mvva 84,8,60
s_nop 6
K 160,80,81
K 161,82,83
K 162,84,85
K 163,86,87
v_lshlrev_b32 v80, 16, v160
v_and_b32 v81, 0xffff0000, v160
v_lshlrev_b32 v82, 16, v161
v_and_b32 v83, 0xffff0000, v161
v_lshlrev_b32 v84, 16, v162
v_and_b32 v85, 0xffff0000, v162
v_lshlrev_b32 v86, 16, v163
v_and_b32 v87, 0xffff0000, v163
v_pk_add_f32 v[80:81], v[48:49], v[80:81]
v_pk_add_f32 v[82:83], v[50:51], v[82:83]
v_pk_add_f32 v[84:85], v[52:53], v[84:85]
v_pk_add_f32 v[86:87], v[54:55], v[86:87]
K 160,80,81
K 161,82,83
K 162,84,85
K 163,86,87
v_lshlrev_b32 v80, 16, v160
v_and_b32 v81, 0xffff0000, v160
v_lshlrev_b32 v82, 16, v161
v_and_b32 v83, 0xffff0000, v161
v_lshlrev_b32 v84, 16, v162
v_and_b32 v85, 0xffff0000, v162
v_lshlrev_b32 v86, 16, v163
v_and_b32 v87, 0xffff0000, v163
v_pk_add_f32 v[80:81], v[56:57], v[80:81]
v_pk_add_f32 v[82:83], v[58:59], v[82:83]
v_pk_add_f32 v[84:85], v[60:61], v[84:85]
v_pk_add_f32 v[86:87], v[62:63], v[86:87]
K 80,80,81
K 81,82,83
K 82,84,85
K 83,86,87
buffer_store_dwordx4 v[80:83], v20, s[20:23], s73 offen nt
s_branch .LL_126%=
.LL_175%=:
s_waitcnt vmcnt(0)
Zvva 80,4,32
Zvva 84,8,32
Mvva 80,4,36
Mvva 84,8,36
Mvva 80,4,40
Mvva 84,8,40
Mvva 80,4,44
Mvva 84,8,44
Mvva 80,4,48
Mvva 84,8,48
Mvva 80,4,52
)"
R"(Mvva 84,8,52
Mvva 80,4,56
Mvva 84,8,56
Mvva 80,4,60
Mvva 84,8,60
s_nop 6
K 160,80,81
K 161,82,83
K 162,84,85
K 163,86,87
v_lshlrev_b32 v80, 16, v160
v_and_b32 v81, 0xffff0000, v160
v_lshlrev_b32 v82, 16, v161
v_and_b32 v83, 0xffff0000, v161
v_lshlrev_b32 v84, 16, v162
v_and_b32 v85, 0xffff0000, v162
v_lshlrev_b32 v86, 16, v163
v_and_b32 v87, 0xffff0000, v163
v_pk_add_f32 v[80:81], v[48:49], v[80:81]
v_pk_add_f32 v[82:83], v[50:51], v[82:83]
v_pk_add_f32 v[84:85], v[52:53], v[84:85]
v_pk_add_f32 v[86:87], v[54:55], v[86:87]
K 160,80,81
K 161,82,83
K 162,84,85
K 163,86,87
v_lshlrev_b32 v80, 16, v160
v_and_b32 v81, 0xffff0000, v160
v_lshlrev_b32 v82, 16, v161
v_and_b32 v83, 0xffff0000, v161
v_lshlrev_b32 v84, 16, v162
v_and_b32 v85, 0xffff0000, v162
v_lshlrev_b32 v86, 16, v163
v_and_b32 v87, 0xffff0000, v163
v_pk_add_f32 v[80:81], v[56:57], v[80:81]
v_pk_add_f32 v[82:83], v[58:59], v[82:83]
v_pk_add_f32 v[84:85], v[60:61], v[84:85]
v_pk_add_f32 v[86:87], v[62:63], v[86:87]
K 80,80,81
K 81,82,83
K 82,84,85
K 83,86,87
buffer_store_dwordx4 v[80:83], v20, s[20:23], s73 offen nt
s_branch .LL_126%=
.LL_128%=:
s_mov_b32 s84, 0
.LL_177%=:
s_mov_b32 s70, s71
s_andn2_b32 s69, s67, s71
s_and_b32 s69, s69, 0xffff
s_cbranch_scc0 .LL_180%=
s_and_b32 s80, s69, 255
s_cbranch_scc0 .LL_181%=
s_bitcmp1_b32 s69, 0
s_cbranch_scc0 .LL_182%=
buffer_load_dwordx4 a[0:3], v1, s[8:11], s78 offen offset:0 sc1
.LL_182%=:
s_bitcmp1_b32 s69, 1
s_cbranch_scc0 .LL_183%=
buffer_load_dwordx4 a[4:7], v1, s[8:11], s78 offen offset:1024 sc1
.LL_183%=:
s_bitcmp1_b32 s69, 2
s_cbranch_scc0 .LL_184%=
buffer_load_dwordx4 a[8:11], v1, s[8:11], s78 offen offset:2048 sc1
.LL_184%=:
s_bitcmp1_b32 s69, 3
s_cbranch_scc0 .LL_185%=
buffer_load_dwordx4 a[12:15], v1, s[8:11], s78 offen offset:3072 sc1
.LL_185%=:
s_bitcmp1_b32 s69, 4
s_cbranch_scc0 .LL_186%=
buffer_load_dwordx4 a[16:19], v19, s[8:11], s78 offen offset:0 sc1
.LL_186%=:
s_bitcmp1_b32 s69, 5
s_cbranch_scc0 .LL_187%=
buffer_load_dwordx4 a[20:23], v19, s[8:11], s78 offen offset:1024 sc1
.LL_187%=:
s_bitcmp1_b32 s69, 6
s_cbranch_scc0 .LL_188%=
buffer_load_dwordx4 a[24:27], v19, s[8:11], s78 offen offset:2048 sc1
.LL_188%=:
s_bitcmp1_b32 s69, 7
s_cbranch_scc0 .LL_189%=
buffer_load_dwordx4 a[28:31], v19, s[8:11], s78 offen offset:3072 sc1
.LL_189%=:
.LL_181%=:
s_and_b32 s80, s69, 65280
s_cbranch_scc0 .LL_190%=
s_bitcmp1_b32 s69, 8
s_cbranch_scc0 .LL_191%=
buffer_load_dwordx4 a[32:35], v1, s[8:11], s79 offen offset:0 sc1
buffer_load_dwordx4 a[216:219], v1, s[8:11], s65 offen offset:0 sc1
.LL_191%=:
s_bitcmp1_b32 s69, 9
s_cbranch_scc0 .LL_192%=
buffer_load_dwordx4 a[36:39], v1, s[8:11], s79 offen offset:1024 sc1
buffer_load_dwordx4 a[220:223], v1, s[8:11], s65 offen offset:1024 sc1
.LL_192%=:
s_bitcmp1_b32 s69, 10
s_cbranch_scc0 .LL_193%=
buffer_load_dwordx4 a[40:43], v1, s[8:11], s79 offen offset:2048 sc1
buffer_load_dwordx4 a[224:227], v1, s[8:11], s65 offen offset:2048 sc1
.LL_193%=:
s_bitcmp1_b32 s69, 11
s_cbranch_scc0 .LL_194%=
buffer_load_dwordx4 a[44:47], v1, s[8:11], s79 offen offset:3072 sc1
buffer_load_dwordx4 a[228:231], v1, s[8:11], s65 offen offset:3072 sc1
.LL_194%=:
s_bitcmp1_b32 s69, 12
s_cbranch_scc0 .LL_195%=
buffer_load_dwordx4 a[48:51], v19, s[8:11], s79 offen offset:0 sc1
buffer_load_dwordx4 a[232:235], v19, s[8:11], s65 offen offset:0 sc1
.LL_195%=:
s_bitcmp1_b32 s69, 13
s_cbranch_scc0 .LL_196%=
buffer_load_dwordx4 a[52:55], v19, s[8:11], s79 offen offset:1024 sc1
buffer_load_dwordx4 a[236:239], v19, s[8:11], s65 offen offset:1024 sc1
.LL_196%=:
s_bitcmp1_b32 s69, 14
s_cbranch_scc0 .LL_197%=
buffer_load_dwordx4 a[56:59], v19, s[8:11], s79 offen offset:2048 sc1
buffer_load_dwordx4 a[240:243], v19, s[8:11], s65 offen offset:2048 sc1
.LL_197%=:
s_bitcmp1_b32 s69, 15
s_cbranch_scc0 .LL_198%=
buffer_load_dwordx4 a[60:63], v19, s[8:11], s79 offen offset:3072 sc1
buffer_load_dwordx4 a[244:247], v19, s[8:11], s65 offen offset:3072 sc1
.LL_198%=:
.LL_190%=:
s_or_b32 s71, s71, s69
.LL_180%=:
s_andn2_b32 s80, 0xffff, s71
s_cbranch_scc0 .LL_179%=
s_sleep 4
buffer_load_dwordx2 v[16:17], v18, s[8:11], s63 offen sc1
s_and_b32 s80, s70, 0x100ff
s_cmp_eq_u32 s80, 0xff
s_cbranch_scc0 .LL_178%=
Zvva 80,4,0
Zvva 84,8,0
Mvva 80,4,4
Mvva 84,8,4
Mvva 80,4,8
Mvva 84,8,8
Mvva 80,4,12
Mvva 84,8,12
Mvva 80,4,16
Mvva 84,8,16
Mvva 80,4,20
Mvva 84,8,20
Mvva 80,4,24
Mvva 84,8,24
Mvva 80,4,28
Mvva 84,8,28
s_nop 6
K 160,80,81
K 161,82,83
K 162,84,85
K 163,86,87
v_lshlrev_b32 v80, 16, v160
v_and_b32 v81, 0xffff0000, v160
v_lshlrev_b32 v82, 16, v161
v_and_b32 v83, 0xffff0000, v161
v_lshlrev_b32 v84, 16, v162
v_and_b32 v85, 0xffff0000, v162
v_lshlrev_b32 v86, 16, v163
v_and_b32 v87, 0xffff0000, v163
v_pk_add_f32 v[80:81], v[32:33], v[80:81]
v_pk_add_f32 v[82:83], v[34:35], v[82:83]
v_pk_add_f32 v[84:85], v[36:37], v[84:85]
v_pk_add_f32 v[86:87], v[38:39], v[86:87]
K 160,80,81
K 161,82,83
K 162,84,85
K 163,86,87
v_lshlrev_b32 v80, 16, v160
v_and_b32 v81, 0xffff0000, v160
v_lshlrev_b32 v82, 16, v161
v_and_b32 v83, 0xffff0000, v161
v_lshlrev_b32 v84, 16, v162
v_and_b32 v85, 0xffff0000, v162
v_lshlrev_b32 v86, 16, v163
v_and_b32 v87, 0xffff0000, v163
v_pk_add_f32 v[80:81], v[40:41], v[80:81]
v_pk_add_f32 v[82:83], v[42:43], v[82:83]
v_pk_add_f32 v[84:85], v[44:45], v[84:85]
v_pk_add_f32 v[86:87], v[46:47], v[86:87]
K 80,80,81
K 81,82,83
K 82,84,85
K 83,86,87
buffer_store_dwordx4 v[80:83], v20, s[20:23], s72 offen nt
s_bitset1_b32 s71, 16
.LL_178%=:
s_add_u32 s84, s84, 1
s_waitcnt vmcnt(0)
v_cmp_eq_u64 vcc, s[12:13], v[16:17]
s_or_b32 s67, vcc_lo, s66
s_cmp_lt_u32 s84, 4194304
s_cbranch_scc1 .LL_177%=
.LL_179%=:
s_bitcmp1_b32 s71, 16
s_cbranch_scc1 .LL_199%=
s_and_b32 s80, s70, 0xff
s_cmp_eq_u32 s80, 0xff
s_cbranch_scc1 .LL_200%=
s_waitcnt vmcnt(0)
.LL_200%=:
Zvva 80,4,0
Zvva 84,8,0
Mvva 80,4,4
Mvva 84,8,4
Mvva 80,4,8
Mvva 84,8,8
Mvva 80,4,12
Mvva 84,8,12
Mvva 80,4,16
Mvva 84,8,16
Mvva 80,4,20
Mvva 84,8,20
Mvva 80,4,24
Mvva 84,8,24
Mvva 80,4,28
Mvva 84,8,28
s_nop 6
K 160,80,81
K 161,82,83
K 162,84,85
K 163,86,87
v_lshlrev_b32 v80, 16, v160
v_and_b32 v81, 0xffff0000, v160
v_lshlrev_b32 v82, 16, v161
v_and_b32 v83, 0xffff0000, v161
v_lshlrev_b32 v84, 16, v162
v_and_b32 v85, 0xffff0000, v162
v_lshlrev_b32 v86, 16, v163
v_and_b32 v87, 0xffff0000, v163
v_pk_add_f32 v[80:81], v[32:33], v[80:81]
v_pk_add_f32 v[82:83], v[34:35], v[82:83]
v_pk_add_f32 v[84:85], v[36:37], v[84:85]
v_pk_add_f32 v[86:87], v[38:39], v[86:87]
K 160,80,81
K 161,82,83
K 162,84,85
K 163,86,87
v_lshlrev_b32 v80, 16, v160
v_and_b32 v81, 0xffff0000, v160
v_lshlrev_b32 v82, 16, v161
v_and_b32 v83, 0xffff0000, v161
v_lshlrev_b32 v84, 16, v162
v_and_b32 v85, 0xffff0000, v162
v_lshlrev_b32 v86, 16, v163
v_and_b32 v87, 0xffff0000, v163
v_pk_add_f32 v[80:81], v[40:41], v[80:81]
v_pk_add_f32 v[82:83], v[42:43], v[82:83]
v_pk_add_f32 v[84:85], v[44:45], v[84:85]
v_pk_add_f32 v[86:87], v[46:47], v[86:87]
K 80,80,81
K 81,82,83
K 82,84,85
K 83,86,87
buffer_store_dwordx4 v[80:83], v20, s[20:23], s72 offen nt
s_waitcnt vmcnt(1)
s_nop 0
Zvva 80,4,32
Zvva 84,8,32
Zvva 88,4,216
Zvva 92,8,216
Mvva 80,4,36
Mvva 84,8,36
Mvva 88,4,220
Mvva 92,8,220
Mvva 80,4,40
Mvva 84,8,40
Mvva 88,4,224
Mvva 92,8,224
Mvva 80,4,44
Mvva 84,8,44
Mvva 88,4,228
Mvva 92,8,228
Mvva 80,4,48
Mvva 84,8,48
Mvva 88,4,232
Mvva 92,8,232
Mvva 80,4,52
Mvva 84,8,52
Mvva 88,4,236
Mvva 92,8,236
Mvva 80,4,56
Mvva 84,8,56
Mvva 88,4,240
Mvva 92,8,240
Mvva 80,4,60
Mvva 84,8,60
Mvva 88,4,244
Mvva 92,8,244
s_nop 4
K 160,80,81
K 161,82,83
K 162,84,85
K 163,86,87
v_lshlrev_b32 v80, 16, v160
v_and_b32 v81, 0xffff0000, v160
v_lshlrev_b32 v82, 16, v161
v_and_b32 v83, 0xffff0000, v161
v_lshlrev_b32 v84, 16, v162
v_and_b32 v85, 0xffff0000, v162
v_lshlrev_b32 v86, 16, v163
v_and_b32 v87, 0xffff0000, v163
v_pk_add_f32 v[80:81], v[48:49], v[80:81]
v_pk_add_f32 v[82:83], v[50:51], v[82:83]
v_pk_add_f32 v[84:85], v[52:53], v[84:85]
v_pk_add_f32 v[86:87], v[54:55], v[86:87]
K 160,80,81
K 161,82,83
K 162,84,85
K 163,86,87
v_lshlrev_b32 v80, 16, v160
v_and_b32 v81, 0xffff0000, v160
v_lshlrev_b32 v82, 16, v161
v_and_b32 v83, 0xffff0000, v161
v_lshlrev_b32 v84, 16, v162
v_and_b32 v85, 0xffff0000, v162
v_lshlrev_b32 v86, 16, v163
v_and_b32 v87, 0xffff0000, v163
v_pk_add_f32 v[80:81], v[56:57], v[80:81]
v_pk_add_f32 v[82:83], v[58:59], v[82:83]
v_pk_add_f32 v[84:85], v[60:61], v[84:85]
v_pk_add_f32 v[86:87], v[62:63], v[86:87]
K 80,80,81
K 81,82,83
K 82,84,85
K 83,86,87
buffer_store_dwordx4 v[80:83], v20, s[20:23], s73 offen nt
K 160,88,89
K 161,90,91
K 162,92,93
K 163,94,95
v_lshlrev_b32 v88, 16, v160
v_and_b32 v89, 0xffff0000, v160
v_lshlrev_b32 v90, 16, v161
v_and_b32 v91, 0xffff0000, v161
v_lshlrev_b32 v92, 16, v162
v_and_b32 v93, 0xffff0000, v162
v_lshlrev_b32 v94, 16, v163
v_and_b32 v95, 0xffff0000, v163
v_pk_add_f32 v[88:89], v[64:65], v[88:89]
v_pk_add_f32 v[90:91], v[66:67], v[90:91]
v_pk_add_f32 v[92:93], v[68:69], v[92:93]
v_pk_add_f32 v[94:95], v[70:71], v[94:95]
K 160,88,89
K 161,90,91
K 162,92,93
K 163,94,95
v_lshlrev_b32 v88, 16, v160
v_and_b32 v89, 0xffff0000, v160
v_lshlrev_b32 v90, 16, v161
v_and_b32 v91, 0xffff0000, v161
v_lshlrev_b32 v92, 16, v162
v_and_b32 v93, 0xffff0000, v162
v_lshlrev_b32 v94, 16, v163
v_and_b32 v95, 0xffff0000, v163
v_pk_add_f32 v[88:89], v[72:73], v[88:89]
v_pk_add_f32 v[90:91], v[74:75], v[90:91]
v_pk_add_f32 v[92:93], v[76:77], v[92:93]
v_pk_add_f32 v[94:95], v[78:79], v[94:95]
K 88,88,89
K 89,90,91
K 90,92,93
K 91,94,95
buffer_store_dwordx4 v[88:91], v20, s[20:23], s74 offen nt
s_branch .LL_126%=
.LL_199%=:
s_waitcnt vmcnt(0)
Zvva 80,4,32
Zvva 84,8,32
Zvva 88,4,216
Zvva 92,8,216
Mvva 80,4,36
Mvva 84,8,36
Mvva 88,4,220
Mvva 92,8,220
Mvva 80,4,40
Mvva 84,8,40
Mvva 88,4,224
Mvva 92,8,224
Mvva 80,4,44
Mvva 84,8,44
Mvva 88,4,228
Mvva 92,8,228
Mvva 80,4,48
Mvva 84,8,48
Mvva 88,4,232
Mvva 92,8,232
Mvva 80,4,52
Mvva 84,8,52
Mvva 88,4,236
Mvva 92,8,236
Mvva 80,4,56
Mvva 84,8,56
)"
R"(Mvva 88,4,240
Mvva 92,8,240
Mvva 80,4,60
Mvva 84,8,60
Mvva 88,4,244
Mvva 92,8,244
s_nop 4
K 160,80,81
K 161,82,83
K 162,84,85
K 163,86,87
v_lshlrev_b32 v80, 16, v160
v_and_b32 v81, 0xffff0000, v160
v_lshlrev_b32 v82, 16, v161
v_and_b32 v83, 0xffff0000, v161
v_lshlrev_b32 v84, 16, v162
v_and_b32 v85, 0xffff0000, v162
v_lshlrev_b32 v86, 16, v163
v_and_b32 v87, 0xffff0000, v163
v_pk_add_f32 v[80:81], v[48:49], v[80:81]
v_pk_add_f32 v[82:83], v[50:51], v[82:83]
v_pk_add_f32 v[84:85], v[52:53], v[84:85]
v_pk_add_f32 v[86:87], v[54:55], v[86:87]
K 160,80,81
K 161,82,83
K 162,84,85
K 163,86,87
v_lshlrev_b32 v80, 16, v160
v_and_b32 v81, 0xffff0000, v160
v_lshlrev_b32 v82, 16, v161
v_and_b32 v83, 0xffff0000, v161
v_lshlrev_b32 v84, 16, v162
v_and_b32 v85, 0xffff0000, v162
v_lshlrev_b32 v86, 16, v163
v_and_b32 v87, 0xffff0000, v163
v_pk_add_f32 v[80:81], v[56:57], v[80:81]
v_pk_add_f32 v[82:83], v[58:59], v[82:83]
v_pk_add_f32 v[84:85], v[60:61], v[84:85]
v_pk_add_f32 v[86:87], v[62:63], v[86:87]
K 80,80,81
K 81,82,83
K 82,84,85
K 83,86,87
buffer_store_dwordx4 v[80:83], v20, s[20:23], s73 offen nt
K 160,88,89
K 161,90,91
K 162,92,93
K 163,94,95
v_lshlrev_b32 v88, 16, v160
v_and_b32 v89, 0xffff0000, v160
v_lshlrev_b32 v90, 16, v161
v_and_b32 v91, 0xffff0000, v161
v_lshlrev_b32 v92, 16, v162
v_and_b32 v93, 0xffff0000, v162
v_lshlrev_b32 v94, 16, v163
v_and_b32 v95, 0xffff0000, v163
v_pk_add_f32 v[88:89], v[64:65], v[88:89]
v_pk_add_f32 v[90:91], v[66:67], v[90:91]
v_pk_add_f32 v[92:93], v[68:69], v[92:93]
v_pk_add_f32 v[94:95], v[70:71], v[94:95]
K 160,88,89
K 161,90,91
K 162,92,93
K 163,94,95
v_lshlrev_b32 v88, 16, v160
v_and_b32 v89, 0xffff0000, v160
v_lshlrev_b32 v90, 16, v161
v_and_b32 v91, 0xffff0000, v161
v_lshlrev_b32 v92, 16, v162
v_and_b32 v93, 0xffff0000, v162
v_lshlrev_b32 v94, 16, v163
v_and_b32 v95, 0xffff0000, v163
v_pk_add_f32 v[88:89], v[72:73], v[88:89]
v_pk_add_f32 v[90:91], v[74:75], v[90:91]
v_pk_add_f32 v[92:93], v[76:77], v[92:93]
v_pk_add_f32 v[94:95], v[78:79], v[94:95]
K 88,88,89
K 89,90,91
K 90,92,93
K 91,94,95
buffer_store_dwordx4 v[88:91], v20, s[20:23], s74 offen nt
.LL_126%=:
s_lshl_b32 s80, s15, 2
s_add_u32 s80, s80, s14
s_cmp_eq_u32 s80, 4
s_cbranch_scc0 .LL_201%=
v_mov_b64 v[28:29], s[12:13]
s_mov_b64 exec, 1
buffer_store_dwordx2 v[28:29], off, s[8:11], 0 sc1
s_mov_b64 exec, -1
.LL_201%=:
s_branch .Lend_202%=
.Lup_119%=:
s_waitcnt vmcnt(3)
s_mov_b32 s84, 0
.LL_121%=:
v_min3_u32 v176, v161, v163, v165
v_max3_u32 v177, v161, v163, v165
v_min3_u32 v176, v176, v167, v169
v_max3_u32 v177, v177, v167, v169
v_min3_u32 v176, v176, v171, v173
v_max3_u32 v177, v177, v171, v173
v_min_u32 v176, v176, v175
v_max_u32 v177, v177, v175
v_cmp_eq_u32 vcc, s13, v176
v_cmp_eq_u32_e64 s[86:87], s13, v177
s_and_b64 vcc, vcc, s[86:87]
s_cmp_eq_u64 vcc, -1
s_cbranch_scc1 .LL_122%=
s_add_u32 s84, s84, 1
s_cmp_lt_u32 s84, 1048576
s_cbranch_scc1 .LL_123%=
.LL_122%=:
v_readlane_b32 s70, v12, s69
v_bfe_u32 v2, v1, 4, 4
s_nop 2
s_lshr_b32 s80, s70, 4
s_and_b32 s70, s70, 15
v_cmp_eq_u32 vcc, s70, v2
s_cmp_eq_u32 s80, s61
s_cselect_b64 s[86:87], vcc, 0
s_cselect_b64 s[88:89], 0, vcc
s_bitcmp1_b32 s14, 0
s_cselect_b64 vcc, s[88:89], s[86:87]
v_lshlrev_b32 v176, 16, v160
v_and_b32 v177, 0xffff0000, v160
v_lshlrev_b32 v178, 16, v162
v_and_b32 v179, 0xffff0000, v162
v_lshlrev_b32 v180, 16, v164
v_and_b32 v181, 0xffff0000, v164
v_lshlrev_b32 v182, 16, v166
v_and_b32 v183, 0xffff0000, v166
A 176,176,40
A 177,177,41
A 178,178,42
A 179,179,43
A 180,180,44
A 181,181,45
A 182,182,46
A 183,183,47
K 184,176,177
K 185,178,179
K 186,180,181
K 187,182,183
v_lshlrev_b32 v176, 16, v184
v_and_b32 v177, 0xffff0000, v184
v_lshlrev_b32 v178, 16, v185
v_and_b32 v179, 0xffff0000, v185
v_lshlrev_b32 v180, 16, v186
v_and_b32 v181, 0xffff0000, v186
v_lshlrev_b32 v182, 16, v187
v_and_b32 v183, 0xffff0000, v187
v_cndmask_b32_e64 v40, v40, v176, s[86:87]
v_cndmask_b32_e64 v41, v41, v177, s[86:87]
v_cndmask_b32_e64 v42, v42, v178, s[86:87]
v_cndmask_b32_e64 v43, v43, v179, s[86:87]
v_cndmask_b32_e64 v44, v44, v180, s[86:87]
v_cndmask_b32_e64 v45, v45, v181, s[86:87]
v_cndmask_b32_e64 v46, v46, v182, s[86:87]
v_cndmask_b32_e64 v47, v47, v183, s[86:87]
v_lshlrev_b32 v176, 16, v160
v_and_b32 v177, 0xffff0000, v160
v_lshlrev_b32 v178, 16, v162
v_and_b32 v179, 0xffff0000, v162
v_lshlrev_b32 v180, 16, v164
v_and_b32 v181, 0xffff0000, v164
v_lshlrev_b32 v182, 16, v166
v_and_b32 v183, 0xffff0000, v166
A 176,176,56
A 177,177,57
A 178,178,58
A 179,179,59
A 180,180,60
A 181,181,61
A 182,182,62
A 183,183,63
K 184,176,177
K 185,178,179
K 186,180,181
K 187,182,183
v_lshlrev_b32 v176, 16, v184
v_and_b32 v177, 0xffff0000, v184
v_lshlrev_b32 v178, 16, v185
v_and_b32 v179, 0xffff0000, v185
v_lshlrev_b32 v180, 16, v186
v_and_b32 v181, 0xffff0000, v186
v_lshlrev_b32 v182, 16, v187
v_and_b32 v183, 0xffff0000, v187
v_cndmask_b32_e64 v56, v56, v176, s[88:89]
v_cndmask_b32_e64 v57, v57, v177, s[88:89]
v_cndmask_b32_e64 v58, v58, v178, s[88:89]
v_cndmask_b32_e64 v59, v59, v179, s[88:89]
v_cndmask_b32_e64 v60, v60, v180, s[88:89]
v_cndmask_b32_e64 v61, v61, v181, s[88:89]
v_cndmask_b32_e64 v62, v62, v182, s[88:89]
v_cndmask_b32_e64 v63, v63, v183, s[88:89]
v_lshlrev_b32 v176, 16, v168
v_and_b32 v177, 0xffff0000, v168
v_lshlrev_b32 v178, 16, v170
v_and_b32 v179, 0xffff0000, v170
v_lshlrev_b32 v180, 16, v172
v_and_b32 v181, 0xffff0000, v172
v_lshlrev_b32 v182, 16, v174
v_and_b32 v183, 0xffff0000, v174
A 176,176,72
A 177,177,73
A 178,178,74
A 179,179,75
A 180,180,76
A 181,181,77
A 182,182,78
A 183,183,79
K 184,176,177
K 185,178,179
K 186,180,181
K 187,182,183
v_lshlrev_b32 v176, 16, v184
v_and_b32 v177, 0xffff0000, v184
v_lshlrev_b32 v178, 16, v185
v_and_b32 v179, 0xffff0000, v185
v_lshlrev_b32 v180, 16, v186
v_and_b32 v181, 0xffff0000, v186
v_lshlrev_b32 v182, 16, v187
v_and_b32 v183, 0xffff0000, v187
v_cndmask_b32_e64 v72, v72, v176, vcc
v_cndmask_b32_e64 v73, v73, v177, vcc
v_cndmask_b32_e64 v74, v74, v178, vcc
v_cndmask_b32_e64 v75, v75, v179, vcc
v_cndmask_b32_e64 v76, v76, v180, vcc
v_cndmask_b32_e64 v77, v77, v181, vcc
v_cndmask_b32_e64 v78, v78, v182, vcc
v_cndmask_b32_e64 v79, v79, v183, vcc
s_bitset0_b32 s68, s69
s_cmp_eq_u32 s68, 0
s_cbranch_scc1 .Lupr_120%=
s_ff1_i32_b32 s69, s68
.LL_123%=:
s_cmp_eq_u32 s64, 0
s_cbranch_scc0 .LL_124%=
s_waitcnt vmcnt(0)
v_lshlrev_b32 v2, 2, v1
v_mov_b64 v[28:29], s[12:13]
s_mov_b64 exec, 3
buffer_store_dwordx2 v[28:29], v2, s[8:11], s53 offen offset:512 sc1
s_mov_b64 exec, -1
s_mov_b32 s64, 1
.LL_124%=:
s_mul_i32 s66, s69, 20
s_mul_i32 s67, s58, 5
s_add_u32 s66, s66, s67
s_lshl_b32 s66, s66, 7
s_add_u32 s66, s66, 98304
s_lshl_b32 s67, s14, 7
s_add_u32 s67, s67, s66
buffer_load_dwordx4 v[160:163], v3, s[8:11], s67 offen offset:0 sc1
buffer_load_dwordx4 v[164:167], v3, s[8:11], s67 offen offset:16 sc1
buffer_load_dwordx4 v[168:171], v3, s[8:11], s66 offen offset:512 sc1
buffer_load_dwordx4 v[172:175], v3, s[8:11], s66 offen offset:528 sc1
s_waitcnt vmcnt(0)
s_branch .LL_121%=
.Lspin_56%=:
s_mov_b32 s84, 0
.Llp_58%=:
s_sleep 2
v_mov_b32 v28, s74
buffer_load_dwordx2 v[28:29], v28, s[8:11], 0 offen offset:3584 sc1
s_waitcnt vmcnt(0)
s_add_u32 s84, s84, 1
v_xor_b32 v28, s12, v28
v_xor_b32 v29, s13, v29
v_lshrrev_b32 v248, 11, v28
v_or_b32 v29, v29, v248
v_cmp_eq_u32 vcc, 0, v29
s_or_b64 vcc, vcc, s[72:73]
s_cmp_eq_u64 vcc, -1
s_cbranch_scc1 .Lok_57%=
s_cmp_lt_u32 s84, 1048576
s_cbranch_scc1 .Llp_58%=
s_branch .Lok_57%=
.Lspin_61%=:
s_mov_b32 s84, 0
.Llp_63%=:
s_sleep 2
v_bfe_u32 v28, v1, 4, 2
v_lshlrev_b32 v28, 3, v28
s_lshl_b32 s80, s56, 9
s_add_u32 s80, s80, 4096
buffer_load_dwordx2 v[28:29], v28, s[8:11], s80 offen sc1
s_waitcnt vmcnt(0)
s_add_u32 s84, s84, 1
v_cmp_eq_u64 vcc, s[12:13], v[28:29]
s_cmp_eq_u64 vcc, -1
s_cbranch_scc1 .Lok_62%=
s_cmp_lt_u32 s84, 1048576
s_cbranch_scc1 .Llp_63%=
s_branch .Lok_62%=
.Lspin_64%=:
s_mov_b32 s84, 0
.Llp_66%=:
s_sleep 2
v_bfe_u32 v28, v1, 4, 4
v_lshlrev_b32 v28, 3, v28
s_lshl_b32 s80, s56, 9
s_add_u32 s80, s80, 4096
buffer_load_dwordx2 v[28:29], v28, s[8:11], s80 offen sc1
s_waitcnt vmcnt(0)
s_add_u32 s84, s84, 1
v_cmp_eq_u64 vcc, s[12:13], v[28:29]
s_cmp_eq_u64 vcc, -1
s_cbranch_scc1 .Lok_65%=
s_cmp_lt_u32 s84, 1048576
s_cbranch_scc1 .Llp_66%=
s_branch .Lok_65%=
.Lspin_67%=:
s_mov_b32 s84, 0
.Llp_69%=:
s_sleep 2
v_bfe_u32 v28, v1, 4, 4
v_lshlrev_b32 v28, 3, v28
s_lshl_b32 s80, s56, 9
s_add_u32 s80, s80, 4224
buffer_load_dwordx2 v[28:29], v28, s[8:11], s80 offen sc1
s_waitcnt vmcnt(0)
s_add_u32 s84, s84, 1
v_cmp_eq_u64 vcc, s[12:13], v[28:29]
s_cmp_eq_u64 vcc, -1
s_cbranch_scc1 .Lok_68%=
s_cmp_lt_u32 s84, 1048576
s_cbranch_scc1 .Llp_69%=
s_branch .Lok_68%=
.Lspin_70%=:
s_mov_b32 s84, 0
.Llp_72%=:
s_sleep 2
v_bfe_u32 v28, v1, 4, 4
v_lshlrev_b32 v28, 3, v28
s_lshl_b32 s80, s56, 9
s_add_u32 s80, s80, 4352
buffer_load_dwordx2 v[28:29], v28, s[8:11], s80 offen sc1
s_waitcnt vmcnt(0)
s_add_u32 s84, s84, 1
v_cmp_eq_u64 vcc, s[12:13], v[28:29]
s_cmp_eq_u64 vcc, -1
s_cbranch_scc1 .Lok_71%=
s_cmp_lt_u32 s84, 1048576
s_cbranch_scc1 .Llp_72%=
s_branch .Lok_71%=
.Lspin_73%=:
s_mov_b32 s84, 0
.Llp_75%=:
s_sleep 2
v_bfe_u32 v28, v1, 4, 4
v_lshlrev_b32 v28, 3, v28
s_lshl_b32 s80, s56, 9
s_add_u32 s80, s80, 4480
buffer_load_dwordx2 v[28:29], v28, s[8:11], s80 offen sc1
s_waitcnt vmcnt(0)
s_add_u32 s84, s84, 1
v_cmp_eq_u64 vcc, s[12:13], v[28:29]
s_cmp_eq_u64 vcc, -1
s_cbranch_scc1 .Lok_74%=
s_cmp_lt_u32 s84, 1048576
s_cbranch_scc1 .Llp_75%=
s_branch .Lok_74%=
.Lspin_76%=:
s_mov_b32 s84, 0
.Llp_78%=:
s_sleep 2
buffer_load_dwordx2 v[8:9], v4, s[8:11], 0 offen offset:2560 sc1
buffer_load_dwordx2 v[10:11], v5, s[8:11], 0 offen offset:3072 sc1
buffer_load_dwordx2 v[12:13], v6, s[8:11], 0 offen offset:3584 sc1
s_waitcnt vmcnt(0)
s_add_u32 s84, s84, 1
v_cmp_eq_u64 vcc, s[12:13], v[8:9]
v_cmp_eq_u64_e64 s[86:87], s[12:13], v[10:11]
s_and_b64 vcc, vcc, s[86:87]
s_or_b64 vcc, vcc, s[20:21]
s_cmp_eq_u64 vcc, -1
s_cbranch_scc1 .Lok_77%=
s_cmp_lt_u32 s84, 1048576
s_cbranch_scc1 .Llp_78%=
s_branch .Lok_77%=
.Lmk_79%=:
s_nop 7
v_cndmask_b32_e64 v168, v241, v168, s[72:73]
v_cndmask_b32_e64 v169, v241, v169, s[74:75]
v_cndmask_b32_e64 v170, v241, v170, s[76:77]
v_cndmask_b32_e64 v171, v241, v171, s[78:79]
v_cndmask_b32_e64 v188, v241, v188, s[72:73]
v_cndmask_b32_e64 v189, v241, v189, s[74:75]
v_cndmask_b32_e64 v190, v241, v190, s[76:77]
v_cndmask_b32_e64 v191, v241, v191, s[78:79]
v_mov_b32 v184, v241
v_mov_b32 v185, v241
v_mov_b32 v186, v241
v_mov_b32 v187, v241
s_branch .Lmkr_80%=
.Lspin_84%=:
s_mov_b32 s84, 0
.Llp_86%=:
)"
R"(s_sleep 2
v_bfe_u32 v28, v1, 4, 2
v_lshlrev_b32 v28, 3, v28
buffer_load_dwordx2 v[28:29], v28, s[8:11], s55 offen sc1
s_waitcnt vmcnt(0)
s_add_u32 s84, s84, 1
v_cmp_eq_u64 vcc, s[12:13], v[28:29]
s_cmp_eq_u64 vcc, -1
s_cbranch_scc1 .Lok_85%=
s_cmp_lt_u32 s84, 1048576
s_cbranch_scc1 .Llp_86%=
s_branch .Lok_85%=
.LL_89%=:
s_mov_b32 s84, 0
.LL_91%=:
s_sleep 2
buffer_load_dwordx2 v[12:13], v27, s[8:11], 0 offen offset:3584 sc1
s_waitcnt vmcnt(0)
s_add_u32 s84, s84, 1
v_xor_b32 v12, s12, v12
v_xor_b32 v13, s13, v13
v_lshrrev_b32 v3, 11, v12
v_or_b32 v13, v13, v3
v_cmp_eq_u32 vcc, 0, v13
s_cmp_eq_u64 vcc, -1
s_cbranch_scc1 .LL_90%=
s_cmp_lt_u32 s84, 1048576
s_cbranch_scc1 .LL_91%=
s_branch .LL_90%=
.Lup_87%=:
v_lshrrev_b32 v3, 8, v1
v_lshlrev_b32 v3, 5, v3
s_ff1_i32_b32 s69, s68
s_mul_i32 s66, s69, 20
s_mul_i32 s67, s58, 5
s_add_u32 s66, s66, s67
s_lshl_b32 s66, s66, 7
s_add_u32 s66, s66, 98304
s_lshl_b32 s67, s14, 7
s_add_u32 s67, s67, s66
buffer_load_dwordx4 v[160:163], v3, s[8:11], s67 offen offset:0 sc1
buffer_load_dwordx4 v[164:167], v3, s[8:11], s67 offen offset:16 sc1
buffer_load_dwordx4 v[168:171], v3, s[8:11], s66 offen offset:512 sc1
buffer_load_dwordx4 v[172:175], v3, s[8:11], s66 offen offset:528 sc1
s_branch .Lupr_88%=
.Lspin_100%=:
s_mov_b32 s84, 0
.Llp_102%=:
s_sleep 2
v_bfe_u32 v28, v1, 4, 2
v_lshlrev_b32 v28, 3, v28
buffer_load_dwordx2 v[28:29], v28, s[8:11], s54 offen sc1
s_waitcnt vmcnt(0)
s_add_u32 s84, s84, 1
v_cmp_eq_u64 vcc, s[12:13], v[28:29]
s_cmp_eq_u64 vcc, -1
s_cbranch_scc1 .Lok_101%=
s_cmp_lt_u32 s84, 1048576
s_cbranch_scc1 .Llp_102%=
s_branch .Lok_101%=
.Lspin_103%=:
s_mov_b32 s84, 0
.Llp_105%=:
s_sleep 2
v_bfe_u32 v28, v1, 4, 2
v_lshlrev_b32 v28, 3, v28
buffer_load_dwordx2 v[28:29], v28, s[8:11], s55 offen sc1
s_waitcnt vmcnt(0)
s_add_u32 s84, s84, 1
v_cmp_eq_u64 vcc, s[12:13], v[28:29]
s_cmp_eq_u64 vcc, -1
s_cbranch_scc1 .Lok_104%=
s_cmp_lt_u32 s84, 1048576
s_cbranch_scc1 .Llp_105%=
s_branch .Lok_104%=
.LL_108%=:
s_mov_b32 s84, 0
.LL_110%=:
s_sleep 2
buffer_load_dwordx2 v[12:13], v27, s[8:11], 0 offen offset:3584 sc1
s_waitcnt vmcnt(0)
s_add_u32 s84, s84, 1
v_xor_b32 v12, s12, v12
v_xor_b32 v13, s13, v13
v_lshrrev_b32 v3, 11, v12
v_or_b32 v13, v13, v3
v_cmp_eq_u32 vcc, 0, v13
s_cmp_eq_u64 vcc, -1
s_cbranch_scc1 .LL_109%=
s_cmp_lt_u32 s84, 1048576
s_cbranch_scc1 .LL_110%=
s_branch .LL_109%=
.Lup_106%=:
v_lshrrev_b32 v3, 8, v1
v_lshlrev_b32 v3, 5, v3
s_ff1_i32_b32 s69, s68
s_mul_i32 s66, s69, 20
s_mul_i32 s67, s58, 5
s_add_u32 s66, s66, s67
s_lshl_b32 s66, s66, 7
s_add_u32 s66, s66, 98304
s_lshl_b32 s67, s14, 7
s_add_u32 s67, s67, s66
buffer_load_dwordx4 v[160:163], v3, s[8:11], s67 offen offset:0 sc1
buffer_load_dwordx4 v[164:167], v3, s[8:11], s67 offen offset:16 sc1
buffer_load_dwordx4 v[168:171], v3, s[8:11], s66 offen offset:512 sc1
buffer_load_dwordx4 v[172:175], v3, s[8:11], s66 offen offset:528 sc1
s_branch .Lupr_107%=
.Lend_202%=:
.Lend%=:
.purgem W
.purgem D
.purgem Ta
.purgem Ra
.purgem Zvaa
.purgem Mvaa
.purgem Zaaa
.purgem Maaa
.purgem A
.purgem U
.purgem K
.purgem Rv
.purgem Mvav
.purgem G
.purgem Zvav
.purgem Cvav
.purgem X
.purgem Cvaa
.purgem Dc
.purgem Maav
.purgem Mvvv
.purgem Zvva
.purgem Mvva
)"
    : : [ka] "s"(__builtin_amdgcn_kernarg_segment_ptr()), [bid] "s"(blockIdx.x), [tid] "v"(threadIdx.x)
    : "v1", "v2", "v3", "v4", "v5", "v6", "v7", "v8", "v9", "v10", "v11", "v12", "v13", "v14", "v15", "v16", "v17", "v18", "v19", "v20", "v21", "v22", "v23", "v24", "v25", "v26", "v27", "v28", "v29", "v30", "v31", "v32", "v33", "v34", "v35", "v36", "v37", "v38", "v39", "v40", "v41", "v42", "v43", "v44", "v45", "v46", "v47", "v48", "v49", "v50", "v51", "v52", "v53", "v54", "v55", "v56", "v57", "v58", "v59", "v60", "v61", "v62", "v63", "v64", "v65", "v66", "v67", "v68", "v69", "v70", "v71", "v72", "v73", "v74", "v75", "v76", "v77", "v78", "v79", "v80", "v81", "v82", "v83", "v84", "v85", "v86", "v87", "v88", "v89", "v90", "v91", "v92", "v93", "v94", "v95", "v96", "v97", "v98", "v99", "v100", "v101", "v102", "v103", "v104", "v105", "v106", "v107", "v108", "v109", "v110", "v111", "v112", "v113", "v114", "v115", "v116", "v117", "v118", "v119", "v120", "v121", "v122", "v123", "v124", "v125", "v126", "v127", "v128", "v129", "v130", "v131", "v132", "v133", "v134", "v135", "v136", "v137", "v138", "v139", "v140", "v141", "v142", "v143", "v144", "v145", "v146", "v147", "v148", "v149", "v150", "v151", "v152", "v153", "v154", "v155", "v156", "v157", "v158", "v159", "v160", "v161", "v162", "v163", "v164", "v165", "v166", "v167", "v168", "v169", "v170", "v171", "v172", "v173", "v174", "v175", "v176", "v177", "v178", "v179", "v180", "v181", "v182", "v183", "v184", "v185", "v186", "v187", "v188", "v189", "v190", "v191", "v192", "v193", "v194", "v195", "v196", "v197", "v198", "v199", "v200", "v201", "v202", "v203", "v204", "v205", "v206", "v207", "v208", "v209", "v210", "v211", "v212", "v213", "v214", "v215", "v216", "v217", "v218", "v219", "v220", "v221", "v222", "v223", "v224", "v225", "v226", "v227", "v228", "v229", "v230", "v231", "v232", "v233", "v234", "v235", "v236", "v237", "v238", "v239", "v240", "v241", "v242", "v243", "v244", "v245", "v246", "v247", "v248", "v249", "v250", "v251", "v252", "v253", "v254", "v255", "a0", "a1", "a2", "a3", "a4", "a5", "a6", "a7", "a8", "a9", "a10", "a11", "a12", "a13", "a14", "a15", "a16", "a17", "a18", "a19", "a20", "a21", "a22", "a23", "a24", "a25", "a26", "a27", "a28", "a29", "a30", "a31", "a32", "a33", "a34", "a35", "a36", "a37", "a38", "a39", "a40", "a41", "a42", "a43", "a44", "a45", "a46", "a47", "a48", "a49", "a50", "a51", "a52", "a53", "a54", "a55", "a56", "a57", "a58", "a59", "a60", "a61", "a62", "a63", "a64", "a65", "a66", "a67", "a68", "a69", "a70", "a71", "a72", "a73", "a74", "a75", "a76", "a77", "a78", "a79", "a80", "a81", "a82", "a83", "a84", "a85", "a86", "a87", "a88", "a89", "a90", "a91", "a92", "a93", "a94", "a95", "a96", "a97", "a98", "a99", "a100", "a101", "a102", "a103", "a104", "a105", "a106", "a107", "a108", "a109", "a110", "a111", "a112", "a113", "a114", "a115", "a116", "a117", "a118", "a119", "a120", "a121", "a122", "a123", "a124", "a125", "a126", "a127", "a128", "a129", "a130", "a131", "a132", "a133", "a134", "a135", "a136", "a137", "a138", "a139", "a140", "a141", "a142", "a143", "a144", "a145", "a146", "a147", "a148", "a149", "a150", "a151", "a152", "a153", "a154", "a155", "a156", "a157", "a158", "a159", "a160", "a161", "a162", "a163", "a164", "a165", "a166", "a167", "a168", "a169", "a170", "a171", "a172", "a173", "a174", "a175", "a176", "a177", "a178", "a179", "a180", "a181", "a182", "a183", "a184", "a185", "a186", "a187", "a188", "a189", "a190", "a191", "a192", "a193", "a194", "a195", "a196", "a197", "a198", "a199", "a200", "a201", "a202", "a203", "a204", "a205", "a206", "a207", "a208", "a209", "a210", "a211", "a212", "a213", "a214", "a215", "a216", "a217", "a218", "a219", "a220", "a221", "a222", "a223", "a224", "a225", "a226", "a227", "a228", "a229", "a230", "a231", "a232", "a233", "a234", "a235", "a236", "a237", "a238", "a239", "a240", "a241", "a242", "a243", "a244", "a245", "a246", "a247", "a248", "a249", "a250", "a251", "a252", "a253", "a254", "a255", "s4", "s5", "s6", "s7", "s8", "s9", "s10", "s11", "s12", "s13", "s14", "s15", "s16", "s17", "s18", "s19", "s20", "s21", "s22", "s23", "s24", "s25", "s26", "s27", "s28", "s29", "s30", "s31", "s32", "s33", "s34", "s35", "s36", "s37", "s38", "s39", "s40", "s41", "s42", "s43", "s44", "s45", "s46", "s47", "s48", "s49", "s50", "s51", "s52", "s53", "s54", "s55", "s56", "s57", "s58", "s59", "s60", "s61", "s62", "s63", "s64", "s65", "s66", "s67", "s68", "s69", "s70", "s71", "s72", "s73", "s74", "s75", "s76", "s77", "s78", "s79", "s80", "s81", "s82", "s83", "s84", "s85", "s86", "s87", "s88", "s89", "s90", "s91", "s92", "s93", "s94", "s95", "s96", "s97", "s98", "s99", "vcc", "scc", "memory"
  );
}
extern "C" void kernel_launch(void* const* d_in, const int* in_sizes, int n_in, void* d_out, int out_size, void* d_ws, size_t ws_size, hipStream_t stream) {
  (void)hipFuncSetAttribute((const void*)mega_kernel, hipFuncAttributeMaxDynamicSharedMemorySize, LDS_BYTES);
  mega_kernel<<<256, 256, LDS_BYTES, stream>>>((const bf16*)d_in[0], (const bf16*)d_in[1], (const bf16*)d_in[2], (const bf16*)d_in[3], (const bf16*)d_in[4], (const bf16*)d_in[5],
      (const bf16*)d_in[7], (const bf16*)d_in[8], (const bf16*)d_in[9], (const bf16*)d_in[10], (char*)d_ws + 0, (bf16*)d_out);
}
```

```cpp
#include <hip/hip_runtime.h>
#include <hip/hip_bf16.h>
typedef __hip_bfloat16 bf16;
constexpr int LDS_BYTES = 163840;
__global__ __launch_bounds__(256, 1) void mega_kernel(const bf16* x, const bf16* ckv, const bf16* wq, const bf16* wuk, const bf16* wuv, const bf16* wo,
    const bf16* w_r, const bf16* wg, const bf16* wu, const bf16* wd, char* ws, bf16* out) {
  asm volatile(
R"(.macro W n
s_waitcnt lgkmcnt(\n)
.endm
.macro D v,r,s,o
buffer_load_dwordx4 v\v, s[\r:\r+3], s\s offen offset:\o lds
.endm
.macro Ta d,a,o
ds_read_b64_tr_b16 a[\d:\d+1], v\a offset:\o
.endm
.macro Ra d,a,o
ds_read_b128 a[\d:\d+3], v\a offset:\o
.endm
.macro Zvaa d,a,b
v_mfma_f32_16x16x32_bf16 v[\d:\d+3], a[\a:\a+3], a[\b:\b+3], 0
.endm
.macro Mvaa d,a,b
v_mfma_f32_16x16x32_bf16 v[\d:\d+3], a[\a:\a+3], a[\b:\b+3], v[\d:\d+3]
.endm
.macro Zaaa d,a,b
v_mfma_f32_16x16x32_bf16 a[\d:\d+3], a[\a:\a+3], a[\b:\b+3], 0
.endm
.macro Maaa d,a,b
v_mfma_f32_16x16x32_bf16 a[\d:\d+3], a[\a:\a+3], a[\b:\b+3], a[\d:\d+3]
.endm
.macro A d,a,b
v_add_f32 v\d, v\a, v\b
.endm
.macro U d,a,b
v_mul_f32 v\d, v\a, v\b
.endm
.macro K d,a,b
v_cvt_pk_bf16_f32 v\d, v\a, v\b
.endm
.macro Rv d,a,o
ds_read_b128 v[\d:\d+3], v\a offset:\o
.endm
.macro Mvav d,a,b
v_mfma_f32_16x16x32_bf16 v[\d:\d+3], a[\a:\a+3], v[\b:\b+3], v[\d:\d+3]
.endm
.macro G d,a
v_accvgpr_read_b32 v\d, a\a
.endm
.macro Zvav d,a,b
v_mfma_f32_16x16x32_bf16 v[\d:\d+3], a[\a:\a+3], v[\b:\b+3], 0
.endm
.macro Cvav d,a,b,c
v_mfma_f32_16x16x32_bf16 v[\d:\d+3], a[\a:\a+3], v[\b:\b+3], v[\c:\c+3]
.endm
.macro X d
v_exp_f32 v\d, v\d
.endm
.macro Cvaa d,a,b,c
v_mfma_f32_16x16x32_bf16 v[\d:\d+3], a[\a:\a+3], a[\b:\b+3], v[\c:\c+3]
.endm
.macro Dc v,r,s,o
buffer_load_dwordx4 v\v, s[\r:\r+3], s\s offen offset:\o sc1 lds
.endm
.macro Maav d,a,b
v_mfma_f32_16x16x32_bf16 a[\d:\d+3], a[\a:\a+3], v[\b:\b+3], a[\d:\d+3]
.endm
.macro Mvvv d,a,b
v_mfma_f32_16x16x32_bf16 v[\d:\d+3], v[\a:\a+3], v[\b:\b+3], v[\d:\d+3]
.endm
.macro Zvva d,a,b
v_mfma_f32_16x16x32_bf16 v[\d:\d+3], v[\a:\a+3], a[\b:\b+3], 0
.endm
.macro Mvva d,a,b
v_mfma_f32_16x16x32_bf16 v[\d:\d+3], v[\a:\a+3], a[\b:\b+3], v[\d:\d+3]
.endm
s_load_dwordx16 s[16:31], %[ka], 0
s_load_dwordx8 s[32:39], %[ka], 64
s_getpc_b64 s[6:7]
.Lbeg%=:
s_mov_b32 s15, %[bid]
v_lshrrev_b32 v2, 6, %[tid]
v_and_b32 v100, 63, %[tid]
v_readfirstlane_b32 s14, v2
v_lshlrev_b32 v1, 4, v100
v_lshrrev_b32 v101, 4, v100
v_and_b32 v102, 15, v100
s_and_b32 s59, s15, 7
s_bfe_u32 s57, s15, 0x30003
s_lshr_b32 s58, s15, 6
s_lshl_b32 s56, s59, 2
s_add_u32 s56, s56, s58
s_lshr_b32 s80, s15, 3
s_lshl_b32 s80, s80, 2
s_add_u32 s80, s80, s14
s_lshl_b32 s80, s80, 11
v_min_u32 v146, 240, v1
v_lshl_add_u32 v146, v146, 3, s80
v_min_u32 v146, .Lend%=-.Lbeg%=-4, v146
global_load_dword v142, v146, s[6:7]
v_and_b32 v88, 0x1f0, v1
v_lshrrev_b32 v105, 1, v101
v_mul_u32_u24 v105, 1280, v105
v_add_u32 v28, v88, v105
s_lshl_b32 s85, s15, 2
s_add_u32 s85, s85, s14
s_mul_i32 s84, s85, 2560
v_and_b32 v105, 1, v101
v_lshl_or_b32 v29, v105, 31, v28
v_lshlrev_b32 v104, 1, v101
v_xor_b32 v104, v104, v102
v_lshlrev_b32 v104, 4, v104
v_lshl_add_u32 v16, v101, 13, v104
v_xor_b32 v105, 128, v16
v_add_u32 v17, 31744, v16
v_add_u32 v18, 63488, v105
v_add_u32 v19, 95232, v105
v_add_u32 v20, 131072, v16
v_add_u32 v21, 162816, v16
v_add_u32 v22, 194560, v105
v_add_u32 v23, 226304, v105
s_lshl_b32 s80, s14, 3
s_add_u32 s80, s80, s57
s_lshl_b32 s80, s80, 15
v_and_b32 v104, 7, v100
v_xor_b32 v104, v104, v101
v_lshlrev_b32 v104, 4, v104
v_lshrrev_b32 v105, 3, v100
v_lshl_add_u32 v104, v105, 10, v104
v_add_u32 v2, s80, v104
v_xor_b32 v105, 64, v2
v_add_u32 v3, 7168, v105
v_add_u32 v4, 14336, v2
v_add_u32 v5, 21504, v105
v_xor_b32 v6, 1835008, v2
v_xor_b32 v7, 1835008, v3
v_xor_b32 v8, 1835008, v4
v_xor_b32 v9, 1835008, v5
s_lshl_b32 s63, s14, 13
s_add_u32 s61, s63, 65536
s_and_b32 s80, s14, 1
s_lshr_b32 s81, s14, 1
s_lshl_b32 s62, s80, 14
s_lshl_b32 s82, s81, 13
s_add_u32 s62, s62, s82
s_lshl_b32 s81, s81, 18
s_lshl_b32 s83, s56, 8
s_add_u32 s81, s81, s83
s_mov_b32 s64, 0
s_cmp_eq_u32 s80, 0
W 0
s_cselect_b32 s44, s22, s24
s_cselect_b32 s45, s23, s25
s_cmp_eq_u32 s14, 3
s_cbranch_scc0 .LL_1%=
s_lshr_b32 s88, s15, 3
s_cmp_lt_u32 s88, 14
s_cbranch_scc0 .LL_1%=
v_lshlrev_b32 v104, 16, v1
s_mov_b64 s[86:87], s[36:37]
s_mov_b32 s89, 38797312
s_cmp_eq_u32 s88, 1
s_cselect_b32 s90, 39845888, 0
s_add_u32 s86, s86, s90
s_addc_u32 s87, s87, 0
s_cmp_eq_u32 s88, 2
s_cselect_b64 s[86:87], s[26:27], s[86:87]
s_cselect_b32 s89, 5242876, s89
s_cmp_eq_u32 s88, 3
s_cselect_b64 s[86:87], s[30:31], s[86:87]
s_cselect_b32 s89, 2211836, s89
s_cmp_eq_u32 s88, 4
s_cselect_b64 s[86:87], s[32:33], s[86:87]
s_cselect_b32 s89, 2211836, s89
s_cmp_eq_u32 s88, 5
s_cselect_b64 s[86:87], s[34:35], s[86:87]
s_cselect_b32 s89, 2211836, s89
s_cmp_eq_u32 s88, 6
s_cselect_b64 s[86:87], s[38:39], s[86:87]
s_cselect_b32 s89, 2621436, s89
s_cmp_eq_u32 s88, 7
s_cselect_b64 s[86:87], s[20:21], s[86:87]
s_cselect_b32 s89, 5242876, s89
s_cmp_eq_u32 s88, 8
s_cselect_b64 s[86:87], s[18:19], s[86:87]
s_cselect_b32 s89, 2097148, s89
s_cmp_eq_u32 s88, 9
s_cselect_b64 s[86:87], s[22:23], s[86:87]
s_cselect_b32 s89, 4194300, s89
s_cmp_eq_u32 s88, 10
s_cselect_b64 s[86:87], s[24:25], s[86:87]
s_cselect_b32 s89, 4194300, s89
s_cmp_eq_u32 s88, 11
s_cselect_b64 s[86:87], s[16:17], s[86:87]
s_cselect_b32 s89, 2621436, s89
s_cmp_eq_u32 s88, 12
s_cselect_b64 s[86:87], s[28:29], s[86:87]
s_cselect_b32 s89, 1276, s89
s_cmp_eq_u32 s88, 13
s_cselect_b64 s[86:87], s[6:7], s[86:87]
s_cselect_b32 s89, 0, s89
v_min_u32 v104, s89, v104
global_load_dword v142, v104, s[86:87] sc1
.LL_1%=:
s_mov_b32 s40, s18
s_and_b32 s41, s19, 0xffff
s_mov_b32 s42, 0x200000
s_mov_b32 s43, 0x20000
s_add_u32 s44, s44, s81
s_addc_u32 s45, s45, 0
s_and_b32 s45, s45, 0xffff
s_mov_b32 s46, 0x400000
s_mov_b32 s47, 0x20000
s_sub_u32 s46, s46, s81
s_load_dwordx2 s[12:13], s[36:37], 0
s_lshl_b32 s65, s64, 12
s_add_u32 m0, s62, 0
s_nop 0
D 16,44,65,0
D 17,44,65,1024
D 18,44,65,2048
D 19,44,65,3072
s_add_u32 m0, s62, 4096
s_nop 0
D 20,44,65,0
D 21,44,65,1024
D 22,44,65,2048
D 23,44,65,3072
s_add_u32 m0, s61, 0
s_nop 0
D 2,40,64,0
D 3,40,64,1024
D 4,40,64,2048
D 5,40,64,3072
s_add_u32 m0, s61, 4096
s_nop 0
D 6,40,64,0
D 7,40,64,1024
D 8,40,64,2048
D 9,40,64,3072
s_mov_b32 s52, s16
s_and_b32 s53, s17, 0xffff
s_mov_b32 s54, 2621440
s_mov_b32 s55, 0x20000
buffer_load_dwordx4 v[32:35], v28, s[52:55], s84 offen
buffer_load_dwordx4 v[36:39], v28, s[52:55], s84 offen offset:512
buffer_load_dwordx4 v[40:43], v29, s[52:55], s84 offen offset:1024
s_mov_b32 s52, s28
s_and_b32 s53, s29, 0xffff
s_mov_b32 s54, 1280
s_mov_b32 s55, 0x20000
buffer_load_dwordx4 v[44:47], v88, s[52:55], 0 offen offset:0
buffer_load_dwordx4 v[48:51], v88, s[52:55], 0 offen offset:512
buffer_load_dwordx4 v[52:55], v88, s[52:55], 0 offen offset:1024
s_mov_b32 s69, 0
s_cmp_eq_u32 s14, 2
s_cbranch_scc1 .LL_2%=
s_cmp_eq_u32 s14, 3
s_cbranch_scc1 .LL_3%=
s_lshl_b32 s80, s58, 3
s_add_u32 s80, s80, s57
s_lshl_b32 s80, s80, 15
s_lshl_b32 s82, s14, 7
s_add_u32 s80, s80, s82
s_add_u32 s80, s80, 256
v_lshl_add_u32 v104, v88, 6, s80
v_and_b32 v105, 512, v1
v_mul_u32_u24 v105, 3584, v105
v_xor_b32 v104, v104, v105
v_mov_b32 v140, s18
v_mov_b32 v141, s19
s_mov_b32 s68, 256
s_branch .LL_4%=
.LL_2%=:
v_lshrrev_b32 v104, 6, v1
v_lshlrev_b32 v104, 13, v104
v_and_b32 v105, 16, v1
v_lshl_add_u32 v104, v105, 3, v104
s_lshl_b32 s80, s57, 17
s_add_u32 s80, s80, s83
s_add_u32 s80, s80, 1048576
v_add_u32 v104, s80, v104
v_and_b32 v105, 32, v1
v_cmp_eq_u32 vcc, 0, v105
v_mov_b32 v140, s24
v_mov_b32 v141, s25
v_mov_b32 v105, s22
v_mov_b32 v106, s23
v_cndmask_b32 v140, v140, v105, vcc
v_cndmask_b32 v141, v141, v106, vcc
s_mov_b32 s68, 1048576
s_branch .LL_4%=
.LL_3%=:
v_lshrrev_b32 v104, 5, v1
v_lshlrev_b32 v104, 13, v104
v_and_b32 v105, 16, v1
v_lshl_add_u32 v104, v105, 3, v104
s_mul_i32 s80, s57, 655360
s_add_u32 s80, s80, s83
v_add_u32 v104, s80, v104
v_mov_b32 v140, s20
v_mov_b32 v141, s21
s_mov_b32 s68, 196608
v_mov_b32 v104, 0
s_mov_b32 s68, 0
.LL_4%=:
v_mov_b32 v105, 0
v_lshl_add_u64 v[140:141], v[104:105], 0, v[140:141]
s_add_u32 s64, s64, 128
s_and_b32 s64, s64, 1023
s_lshl_b32 s65, s64, 12
s_add_u32 m0, s62, 32768
s_nop 0
D 16,44,65,0
D 17,44,65,1024
D 18,44,65,2048
D 19,44,65,3072
s_add_u32 m0, s62, 36864
s_nop 0
D 20,44,65,0
D 21,44,65,1024
D 22,44,65,2048
D 23,44,65,3072
v_lshrrev_b32 v104, 1, v102
v_xor_b32 v104, v104, v101
v_lshlrev_b32 v104, 4, v104
v_lshl_add_u32 v104, v102, 7, v104
s_lshr_b32 s81, s14, 1
s_lshl_b32 s81, s81, 14
s_add_u32 s81, s81, 65536
v_add_u32 v26, s81, v104
v_xor_b32 v27, 64, v26
v_and_b32 v105, 1, v101
v_lshrrev_b32 v104, 2, v102
v_lshl_or_b32 v105, v105, 2, v104
v_lshl_add_u32 v104, v101, 3, v104
v_lshlrev_b32 v104, 8, v104
v_lshl_add_u32 v104, v105, 5, v104
v_and_b32 v105, 3, v102
v_lshl_add_u32 v10, v105, 3, v104
v_xor_b32 v11, 32, v10
v_xor_b32 v12, 64, v10
v_xor_b32 v13, 96, v10
v_xor_b32 v14, 128, v10
v_xor_b32 v15, 160, v10
v_xor_b32 v24, 192, v10
v_xor_b32 v25, 224, v10
.LL_8%=:
s_lshr_b32 s81, s15, 3
s_cmp_eq_u32 s14, 2
s_cbranch_scc1 .LL_5%=
s_cmp_eq_u32 s14, 3
s_cbranch_scc1 .LL_6%=
s_cmp_eq_u32 s14, 0
s_cselect_b64 s[76:77], s[30:31], s[32:33]
s_mov_b32 s72, 512
s_mov_b32 s73, 24
s_mul_i32 s74, s81, 69120
s_mul_i32 s82, s59, 8640
s_andn2_b32 s82, s82, 127
s_add_u32 s74, s74, s82
s_mov_b32 s75, 2211836
s_branch .LL_7%=
.LL_5%=:
s_mov_b64 s[76:77], s[34:35]
s_mov_b32 s72, 512
s_mov_b32 s73, 24
s_mul_i32 s74, s81, 8960
s_mul_i32 s82, s59, 286720
s_add_u32 s74, s74, s82
s_mov_b32 s75, 2211836
s_branch .LL_7%=
.LL_6%=:
s_mov_b64 s[76:77], s[26:27]
s_mov_b32 s72, 512
s_mov_b32 s73, 4
s_mul_i32 s74, s81, 20480
s_mul_i32 s82, s59, 655360
s_add_u32 s74, s74, s82
s_mov_b32 s75, 5242876
s_branch .LL_7%=
.LL_7%=:
s_mov_b32 s8, s36
s_and_b32 s9, s37, 0xffff
s_mov_b32 s10, 79691776
s_mov_b32 s11, 0x20000
s_add_u32 s48, s20, s83
s_addc_u32 s49, s21, 0
s_and_b32 s49, s49, 0xffff
s_mov_b32 s50, 5242880
s_mov_b32 s51, 0x20000
s_sub_u32 s50, s50, s83
s_lshl_b32 s80, s57, 1
s_and_b32 s81, s14, 1
s_add_u32 s80, s80, s81
s_sub_u32 s81, 31, s80
s_cmp_lt_u32 s14, 2
s_cselect_b32 s60, s80, s81
s_mul_i32 s28, s60, 81920
s_add_u32 s28, s28, 131072
s_add_u32 s4, s28, 10240
s_mov_b32 s80, 0
s_lshl_b32 s81, s80, 9
s_add_u32 s28, s28, s81
s_add_u32 s5, s80, s14
s_cmp_ge_u32 s5, 20
s_cselect_b32 s81, 20, 0
s_sub_u32 s5, s5, s81
s_and_b32 s80, s14, 1
s_mul_i32 s29, s80, 17172480
s_mul_i32 s80, s56, 536576
s_add_u32 s29, s29, s80
s_lshl_b32 s80, s57, 13
s_add_u32 s29, s29, s80
s_add_u32 s29, s29, 4194304
W 0
s_add_u32 s12, s12, 0x7f4a7c15
s_addc_u32 s13, s13, 0x9e3779b9
s_mov_b64 exec, 0
s_cmp_eq_u32 s14, 0
s_cbranch_scc1 .Lh_0_0%=
s_cmp_eq_u32 s14, 1
s_cbranch_scc1 .Lh_0_1%=
s_cmp_eq_u32 s14, 2
s_cbranch_scc1 .Lh_0_2%=
s_branch .Lh_0_3%=
.Lhr_9%=:
s_mov_b64 exec, -1
s_bitcmp1_b32 s14, 0
s_cbranch_scc1 .Lv_10%=
s_branch .LL_12%=
.p2align 12
.LL_12%=:
s_waitcnt vmcnt(14)
s_barrier
Ta 0,10,0
Ta 2,10,1024
Ra 32,26,0
Ta 4,10,8192
.Lh_0_0%=:
s_cbranch_execz .Lh_0_0_1%=
Ta 6,10,9216
Ra 128,27,0
Ra 132,27,2048
Ra 36,26,2048
Ra 40,26,4096
Ra 136,27,4096
Ra 140,27,6144
Ra 44,26,6144
Ra 48,26,8192
Ra 144,27,8192
W 6
Zvaa 160,0,32
)"
R"(s_add_u32 m0, s61, 32768
Ra 148,27,10240
Ra 52,26,10240
Ra 56,26,12288
Ra 152,27,12288
Ra 156,27,14336
Ra 60,26,14336
Mvaa 160,4,128
Ta 8,11,0
Ta 10,11,1024
Ta 12,11,8192
Ta 14,11,9216
Zvaa 192,4,132
Ta 16,12,0
Ta 18,12,1024
Mvaa 192,0,36
W 14
Ta 20,12,8192
Ta 22,12,9216
Zvaa 224,0,40
D 2,40,64,0
Mvaa 224,4,136
Zaaa 160,4,140
Maaa 160,0,44
W 12
Zaaa 192,0,48
D 3,40,64,1024
Maaa 192,4,144
Zaaa 224,4,148
Maaa 224,0,52
W 8
Zaaa 64,0,56
D 4,40,64,2048
Maaa 64,4,152
Zaaa 96,4,156
Maaa 96,0,60
W 4
Zaaa 100,8,60
D 5,40,64,3072
Maaa 100,12,156
s_add_u32 m0, s61, 36864
Zaaa 68,12,152
Ta 24,13,0
Ta 26,13,1024
Maaa 68,8,56
Ta 28,13,8192
Ta 30,13,9216
Zaaa 228,8,52
s_waitcnt vmcnt(15)
D 6,40,64,0
Maaa 228,12,148
v_lshlrev_b32 v56, 16, v32
v_and_b32 v57, 0xffff0000, v32
v_lshlrev_b32 v58, 16, v33
v_and_b32 v59, 0xffff0000, v33
v_lshlrev_b32 v60, 16, v34
Zaaa 196,12,144
v_and_b32 v61, 0xffff0000, v34
v_lshlrev_b32 v62, 16, v35
v_and_b32 v63, 0xffff0000, v35
v_lshlrev_b32 v64, 16, v36
v_and_b32 v65, 0xffff0000, v36
Maaa 196,8,48
v_lshlrev_b32 v66, 16, v37
v_and_b32 v67, 0xffff0000, v37
v_lshlrev_b32 v68, 16, v38
v_and_b32 v69, 0xffff0000, v38
v_lshlrev_b32 v70, 16, v39
Zaaa 164,8,44
D 7,40,64,1024
v_and_b32 v71, 0xffff0000, v39
v_lshlrev_b32 v72, 16, v40
v_and_b32 v73, 0xffff0000, v40
v_lshlrev_b32 v74, 16, v41
v_and_b32 v75, 0xffff0000, v41
Maaa 164,12,140
v_lshlrev_b32 v76, 16, v42
v_and_b32 v77, 0xffff0000, v42
v_lshlrev_b32 v78, 16, v43
v_and_b32 v79, 0xffff0000, v43
A 80,56,57
Zvaa 228,12,136
A 81,58,59
A 80,80,60
A 81,81,61
A 80,80,62
A 81,81,63
Mvaa 228,8,40
A 80,80,64
A 81,81,65
A 80,80,66
A 81,81,67
A 80,80,68
Zvaa 196,8,36
D 8,40,64,2048
A 81,81,69
A 80,80,70
A 81,81,71
A 80,80,72
A 81,81,73
Mvaa 196,12,132
A 80,80,74
A 81,81,75
A 80,80,76
A 81,81,77
A 80,80,78
A 81,81,79
Zvaa 164,12,128
A 80,80,81
s_nop 1
v_add_f32_dpp v80, v80, v80 quad_perm:[1,0,3,2] row_mask:0xf bank_mask:0xf
s_nop 1
v_add_f32_dpp v80, v80, v80 quad_perm:[2,3,0,1] row_mask:0xf bank_mask:0xf
s_nop 1
v_add_f32_dpp v80, v80, v80 row_half_mirror row_mask:0xf bank_mask:0xf
s_nop 1
v_add_f32_dpp v80, v80, v80 row_mirror row_mask:0xf bank_mask:0xf
Mvaa 164,8,32
W 4
v_mov_b32 v81, v80
s_nop 1
v_permlane16_swap_b32 v80, v81
A 80,80,81
v_mul_f32 v84, 0x3acccccd, v80
s_mov_b32 s86, 0xffff
Zvaa 168,16,32
s_mov_b32 s87, 0xffff
D 9,40,64,3072
v_sub_f32 v82, v56, v84
U 80,82,82
v_sub_f32 v83, v57, v84
v_fmac_f32 v80, v83, v83
Mvaa 168,20,128
v_sub_f32 v82, v58, v84
v_fmac_f32 v80, v82, v82
v_sub_f32 v83, v59, v84
v_fmac_f32 v80, v83, v83
v_sub_f32 v82, v60, v84
Zvaa 200,20,132
Ta 0,14,0
v_fmac_f32 v80, v82, v82
v_sub_f32 v83, v61, v84
v_fmac_f32 v80, v83, v83
v_sub_f32 v82, v62, v84
v_fmac_f32 v80, v82, v82
Mvaa 200,16,36
Ta 2,14,1024
v_sub_f32 v83, v63, v84
v_fmac_f32 v80, v83, v83
v_sub_f32 v82, v64, v84
v_fmac_f32 v80, v82, v82
v_sub_f32 v83, v65, v84
Zvaa 232,16,40
global_load_dword v142, v[140:141], off
v_lshl_add_u64 v[140:141], v[140:141], 0, s[68:69]
Ta 4,14,8192
Ta 6,14,9216
v_fmac_f32 v80, v83, v83
v_sub_f32 v82, v66, v84
v_fmac_f32 v80, v82, v82
Mvaa 232,20,136
v_sub_f32 v83, v67, v84
v_fmac_f32 v80, v83, v83
v_sub_f32 v82, v68, v84
v_fmac_f32 v80, v82, v82
v_sub_f32 v83, v69, v84
v_fmac_f32 v80, v83, v83
Zaaa 168,20,140
v_sub_f32 v82, v70, v84
v_fmac_f32 v80, v82, v82
v_sub_f32 v83, v71, v84
v_fmac_f32 v80, v83, v83
v_sub_f32 v82, v72, v84
U 81,82,82
Maaa 168,16,44
v_sub_f32 v83, v73, v84
v_fmac_f32 v81, v83, v83
v_sub_f32 v82, v74, v84
v_fmac_f32 v81, v82, v82
v_sub_f32 v83, v75, v84
v_fmac_f32 v81, v83, v83
Zaaa 200,16,48
v_sub_f32 v82, v76, v84
v_fmac_f32 v81, v82, v82
v_sub_f32 v83, v77, v84
v_fmac_f32 v81, v83, v83
v_sub_f32 v82, v78, v84
Maaa 200,20,144
v_fmac_f32 v81, v82, v82
v_sub_f32 v83, v79, v84
v_fmac_f32 v81, v83, v83
v_cndmask_b32_e64 v81, 0, v81, s[86:87]
A 80,80,81
Zaaa 232,20,148
Maaa 232,16,52
v_add_f32_dpp v80, v80, v80 quad_perm:[1,0,3,2] row_mask:0xf bank_mask:0xf
s_nop 1
v_add_f32_dpp v80, v80, v80 quad_perm:[2,3,0,1] row_mask:0xf bank_mask:0xf
s_nop 1
v_add_f32_dpp v80, v80, v80 row_half_mirror row_mask:0xf bank_mask:0xf
s_nop 1
v_add_f32_dpp v80, v80, v80 row_mirror row_mask:0xf bank_mask:0xf
v_mov_b32 v81, v80
s_nop 1
v_permlane16_swap_b32 v80, v81
Zaaa 72,16,56
A 80,80,81
v_mul_f32 v80, 0x3acccccd, v80
K 80,80,80
v_lshlrev_b32 v80, 16, v80
v_add_f32 v80, 0x3727c5ac, v80
K 80,80,80
Maaa 72,20,152
v_lshlrev_b32 v80, 16, v80
v_sqrt_f32 v81, v80
s_nop 0
v_rcp_f32 v82, v81
v_fma_f32 v83, -v81, v81, v80
Zaaa 104,20,156
v_mul_f32 v82, 0.5, v82
v_fma_f32 v81, v83, v82, v81
K 86,81,81
v_lshlrev_b32 v86, 16, v86
v_rcp_f32 v87, v86
Maaa 104,16,60
W 4
K 85,84,84
v_lshlrev_b32 v85, 16, v85
s_waitcnt vmcnt(17)
v_sub_f32 v56, v56, v85
v_sub_f32 v57, v57, v85
K 80,56,57
Zaaa 108,24,60
v_lshlrev_b32 v56, 16, v80
v_and_b32 v57, 0xffff0000, v80
U 80,56,87
U 81,57,87
K 32,80,81
v_lshlrev_b32 v80, 16, v32
Maaa 108,28,156
v_and_b32 v81, 0xffff0000, v32
v_lshlrev_b32 v82, 16, v44
v_and_b32 v83, 0xffff0000, v44
U 84,80,82
v_fmac_f32 v84, v81, v83
Zaaa 76,28,152
Ta 8,15,0
v_sub_f32 v58, v58, v85
v_sub_f32 v59, v59, v85
K 80,58,59
v_lshlrev_b32 v58, 16, v80
v_and_b32 v59, 0xffff0000, v80
Maaa 76,24,56
Ta 10,15,1024
U 80,58,87
U 81,59,87
K 33,80,81
v_lshlrev_b32 v80, 16, v33
v_and_b32 v81, 0xffff0000, v33
Zaaa 236,24,52
Ta 12,15,8192
Ta 14,15,9216
v_lshlrev_b32 v82, 16, v45
v_and_b32 v83, 0xffff0000, v45
v_fmac_f32 v84, v80, v82
v_fmac_f32 v84, v81, v83
Maaa 236,28,148
v_sub_f32 v60, v60, v85
v_sub_f32 v61, v61, v85
K 80,60,61
v_lshlrev_b32 v60, 16, v80
v_and_b32 v61, 0xffff0000, v80
U 80,60,87
Zaaa 204,28,144
U 81,61,87
K 34,80,81
v_lshlrev_b32 v80, 16, v34
v_and_b32 v81, 0xffff0000, v34
v_lshlrev_b32 v82, 16, v46
v_and_b32 v83, 0xffff0000, v46
Maaa 204,24,48
v_fmac_f32 v84, v80, v82
v_fmac_f32 v84, v81, v83
v_sub_f32 v62, v62, v85
v_sub_f32 v63, v63, v85
K 80,62,63
Zaaa 172,24,44
v_lshlrev_b32 v62, 16, v80
v_and_b32 v63, 0xffff0000, v80
U 80,62,87
U 81,63,87
K 35,80,81
Maaa 172,28,140
v_lshlrev_b32 v80, 16, v35
v_and_b32 v81, 0xffff0000, v35
v_lshlrev_b32 v82, 16, v47
v_and_b32 v83, 0xffff0000, v47
v_fmac_f32 v84, v80, v82
Zvaa 236,28,136
v_fmac_f32 v84, v81, v83
v_sub_f32 v64, v64, v85
v_sub_f32 v65, v65, v85
K 80,64,65
v_lshlrev_b32 v64, 16, v80
Mvaa 236,24,40
v_and_b32 v65, 0xffff0000, v80
U 80,64,87
U 81,65,87
K 36,80,81
v_lshlrev_b32 v80, 16, v36
Zvaa 204,24,36
v_and_b32 v81, 0xffff0000, v36
v_lshlrev_b32 v82, 16, v48
v_and_b32 v83, 0xffff0000, v48
v_fmac_f32 v84, v80, v82
v_fmac_f32 v84, v81, v83
Mvaa 204,28,132
v_sub_f32 v66, v66, v85
v_sub_f32 v67, v67, v85
K 80,66,67
v_lshlrev_b32 v66, 16, v80
v_and_b32 v67, 0xffff0000, v80
U 80,66,87
Zvaa 172,28,128
U 81,67,87
K 37,80,81
v_lshlrev_b32 v80, 16, v37
v_and_b32 v81, 0xffff0000, v37
v_lshlrev_b32 v82, 16, v49
Mvaa 172,24,32
W 4
v_and_b32 v83, 0xffff0000, v49
v_fmac_f32 v84, v80, v82
v_fmac_f32 v84, v81, v83
v_sub_f32 v68, v68, v85
v_sub_f32 v69, v69, v85
Zvaa 176,0,32
K 80,68,69
v_lshlrev_b32 v68, 16, v80
v_and_b32 v69, 0xffff0000, v80
U 80,68,87
U 81,69,87
Mvaa 176,4,128
K 38,80,81
v_lshlrev_b32 v80, 16, v38
v_and_b32 v81, 0xffff0000, v38
v_lshlrev_b32 v82, 16, v50
v_and_b32 v83, 0xffff0000, v50
Zvaa 208,4,132
Ta 16,24,0
v_fmac_f32 v84, v80, v82
v_fmac_f32 v84, v81, v83
v_sub_f32 v70, v70, v85
v_sub_f32 v71, v71, v85
K 80,70,71
Mvaa 208,0,36
Ta 18,24,1024
v_lshlrev_b32 v70, 16, v80
v_and_b32 v71, 0xffff0000, v80
U 80,70,87
U 81,71,87
K 39,80,81
Zvaa 240,0,40
Ta 20,24,8192
Ta 22,24,9216
v_lshlrev_b32 v80, 16, v39
v_and_b32 v81, 0xffff0000, v39
v_lshlrev_b32 v82, 16, v51
v_and_b32 v83, 0xffff0000, v51
Mvaa 240,4,136
v_fmac_f32 v84, v80, v82
v_fmac_f32 v84, v81, v83
v_sub_f32 v72, v72, v85
v_sub_f32 v73, v73, v85
K 80,72,73
v_lshlrev_b32 v72, 16, v80
Zaaa 176,4,140
v_and_b32 v73, 0xffff0000, v80
U 80,72,87
U 81,73,87
K 40,80,81
v_lshlrev_b32 v80, 16, v40
v_and_b32 v81, 0xffff0000, v40
Maaa 176,0,44
v_lshlrev_b32 v82, 16, v52
v_and_b32 v83, 0xffff0000, v52
v_fmac_f32 v84, v80, v82
v_fmac_f32 v84, v81, v83
v_sub_f32 v74, v74, v85
Zaaa 208,0,48
v_sub_f32 v75, v75, v85
K 80,74,75
v_lshlrev_b32 v74, 16, v80
v_and_b32 v75, 0xffff0000, v80
U 80,74,87
Maaa 208,4,144
U 81,75,87
K 41,80,81
v_lshlrev_b32 v80, 16, v41
v_and_b32 v81, 0xffff0000, v41
)"
R"(v_lshlrev_b32 v82, 16, v53
Zaaa 240,4,148
v_and_b32 v83, 0xffff0000, v53
v_fmac_f32 v84, v80, v82
v_fmac_f32 v84, v81, v83
v_sub_f32 v76, v76, v85
v_sub_f32 v77, v77, v85
Maaa 240,0,52
K 80,76,77
v_lshlrev_b32 v76, 16, v80
v_and_b32 v77, 0xffff0000, v80
U 80,76,87
U 81,77,87
Zaaa 80,0,56
K 42,80,81
v_lshlrev_b32 v80, 16, v42
v_and_b32 v81, 0xffff0000, v42
v_lshlrev_b32 v82, 16, v54
v_and_b32 v83, 0xffff0000, v54
Maaa 80,4,152
v_fmac_f32 v84, v80, v82
v_fmac_f32 v84, v81, v83
v_sub_f32 v78, v78, v85
v_sub_f32 v79, v79, v85
K 80,78,79
v_lshlrev_b32 v78, 16, v80
Zaaa 112,4,156
v_and_b32 v79, 0xffff0000, v80
U 80,78,87
U 81,79,87
K 43,80,81
v_lshlrev_b32 v80, 16, v43
Maaa 112,0,60
W 4
v_and_b32 v81, 0xffff0000, v43
v_lshlrev_b32 v82, 16, v55
v_and_b32 v83, 0xffff0000, v55
v_fmac_f32 v84, v80, v82
v_fmac_f32 v84, v81, v83
Zaaa 116,8,60
Ta 24,25,0
Ta 26,25,1024
v_lshrrev_b32 v82, 9, v1
s_lshl_b32 s80, s14, 1
v_add_u32 v82, s80, v82
v_mul_u32_u24 v83, 1552, v82
v_add_u32 v83, v83, v88
Maaa 116,12,156
v_add_u32 v83, 131072, v83
ds_write_b128 v83, v[32:35] offset:0
ds_write_b128 v83, v[36:39] offset:512
ds_write_b128 v83, v[40:43] offset:1024
v_add_f32_dpp v84, v84, v84 quad_perm:[1,0,3,2] row_mask:0xf bank_mask:0xf
Zaaa 84,12,152
Ta 28,25,8192
Ta 30,25,9216
Maaa 84,8,56
v_add_f32_dpp v84, v84, v84 quad_perm:[2,3,0,1] row_mask:0xf bank_mask:0xf
s_nop 1
v_add_f32_dpp v84, v84, v84 row_half_mirror row_mask:0xf bank_mask:0xf
s_nop 1
v_add_f32_dpp v84, v84, v84 row_mirror row_mask:0xf bank_mask:0xf
v_mov_b32 v81, v84
s_nop 1
v_permlane16_swap_b32 v84, v81
A 84,84,81
Zaaa 244,8,52
K 80,84,84
v_lshlrev_b32 v80, 16, v80
s_lshl_b32 s80, s15, 3
v_add_u32 v82, s80, v82
v_sub_u32 v83, 2047, v82
v_ashrrev_i32 v81, 31, v80
v_or_b32 v81, 0x80000000, v81
Maaa 244,12,148
v_xor_b32 v80, v80, v81
v_and_b32 v80, 0xffff0000, v80
v_or_b32 v80, v80, v83
v_and_b32 v82, 7, v82
v_lshlrev_b32 v82, 2, v82
v_add_u32 v82, 143488, v82
Zaaa 212,12,144
ds_write_b32 v82, v80
Maaa 212,8,48
Zaaa 180,8,44
Maaa 180,12,140
Zvaa 244,12,136
s_waitcnt vmcnt(1) lgkmcnt(0)
s_barrier
s_add_u32 s64, s64, 128
Mvaa 244,8,40
v_bfe_u32 v80, v1, 4, 3
Zvaa 212,8,36
v_mul_u32_u24 v81, 1552, v80
Mvaa 212,12,132
s_and_b32 s64, s64, 1023
v_lshrrev_b32 v82, 7, v1
Zvaa 180,12,128
v_lshl_add_u32 v81, v82, 4, v81
Mvaa 180,8,32
s_lshl_b32 s80, s14, 7
Zvaa 184,16,32
s_lshl_b32 s65, s64, 12
s_add_u32 s80, s80, 131072
Mvaa 184,20,128
v_add_u32 v81, s80, v81
Zvaa 216,20,132
ds_read_b128 v[32:35], v81
Ta 0,10,32768
Mvaa 216,16,36
s_add_u32 m0, s62, 0
Ta 2,10,33792
Rv 36,81,512
Zvaa 248,16,40
s_and_b32 s80, s14, 2
Ta 4,10,40960
Ta 6,10,41984
Mvaa 248,20,136
s_lshl_b32 s80, s80, 7
Zaaa 184,20,140
D 16,44,65,0
v_subrev_u32 v81, s80, v81
Maaa 184,16,44
Rv 40,81,1024
Zaaa 216,16,48
v_lshlrev_b32 v80, 2, v80
Maaa 216,20,144
v_add_u32 v80, 143488, v80
Zaaa 248,20,148
D 17,44,65,1024
ds_read_b32 v84, v80
Maaa 248,16,52
v_mov_b32 v85, s13
Zaaa 88,16,56
v_lshrrev_b32 v83, 1, v1
Maaa 88,20,152
s_mul_i32 s82, s15, 10240
Zaaa 120,20,156
s_add_u32 s82, s82, 131072
D 18,44,65,2048
Maaa 120,16,60
s_lshl_b32 s80, s14, 10
Zaaa 124,24,60
s_add_u32 s83, s82, s80
Ra 60,26,47104
Maaa 124,28,156
Ra 156,27,47104
Zaaa 92,28,152
D 19,44,65,3072
Ra 152,27,45056
Maaa 92,24,56
s_add_u32 m0, s62, 4096
Ta 8,11,32768
Ta 10,11,33792
Zaaa 252,24,52
Ra 56,26,45056
Ta 12,11,40960
Maaa 252,28,148
Ta 14,11,41984
Ra 52,26,43008
Zaaa 220,28,144
W 9
buffer_store_dwordx4 v[32:35], v1, s[8:11], s83 offen sc1
s_add_u32 s83, s83, 4096
Maaa 220,24,48
Ra 148,27,43008
Ra 144,27,40960
Zaaa 188,24,44
Ra 48,26,40960
Ra 44,26,38912
Maaa 188,28,140
Ra 140,27,38912
Zvaa 252,28,136
D 20,44,65,0
Ra 136,27,36864
Mvaa 252,24,40
Ra 40,26,36864
Zvaa 220,24,36
Ra 36,26,34816
Mvaa 220,28,132
Ra 132,27,34816
Zvaa 188,28,128
buffer_store_dwordx4 v[36:39], v1, s[8:11], s83 offen sc1
Ra 128,27,32768
Mvaa 188,24,32
W 13
s_and_b32 s80, s80, 1024
s_add_u32 s83, s82, s80
Maaa 96,0,60
s_add_u32 s83, s83, 8192
Ra 32,26,32768
Maaa 96,4,156
Maaa 64,4,152
D 21,44,65,1024
Ta 16,12,32768
Maaa 64,0,56
W 9
Ta 18,12,33792
Ta 20,12,40960
Maaa 224,0,52
Ta 22,12,41984
Maaa 224,4,148
Maaa 192,4,144
buffer_store_dwordx4 v[40:43], v1, s[8:11], s83 offen sc1
Maaa 192,0,48
W 8
Maaa 160,0,44
Maaa 160,4,140
Mvaa 224,4,136
D 22,44,65,2048
Mvaa 224,0,40
W 4
Mvaa 192,0,36
Mvaa 192,4,132
Mvaa 160,4,128
D 23,44,65,3072
Mvaa 160,0,32
s_add_u32 m0, s61, 0
Mvaa 164,8,32
Mvaa 164,12,128
Mvaa 196,12,132
D 2,40,64,0
Ta 24,13,32768
Mvaa 196,8,36
Ta 26,13,33792
Ta 28,13,40960
Mvaa 228,8,40
Ta 30,13,41984
Mvaa 228,12,136
.Lh_0_1%=:
s_cbranch_execz .Lh_0_1_1%=
Maaa 164,12,140
D 3,40,64,1024
Maaa 164,8,44
Maaa 196,8,48
Maaa 196,12,144
Maaa 228,12,148
D 4,40,64,2048
Maaa 228,8,52
Maaa 68,8,56
Maaa 68,12,152
Maaa 100,12,156
D 5,40,64,3072
Maaa 100,8,60
W 4
s_add_u32 m0, s61, 4096
Maaa 104,16,60
Maaa 104,20,156
Maaa 72,20,152
D 6,40,64,0
Ta 0,14,32768
Maaa 72,16,56
Ta 2,14,33792
Ta 4,14,40960
Maaa 232,16,52
Ta 6,14,41984
Maaa 232,20,148
Maaa 200,20,144
D 7,40,64,1024
Maaa 200,16,48
Maaa 168,16,44
Maaa 168,20,140
Mvaa 232,20,136
D 8,40,64,2048
Mvaa 232,16,40
Mvaa 200,16,36
Mvaa 200,20,132
Mvaa 168,20,128
D 9,40,64,3072
v_bfe_u32 v147, v1, 5, 3
Mvaa 168,16,32
W 4
v_lshlrev_b32 v147, 13, v147
v_and_b32 v148, 16, v1
Mvaa 172,24,32
v_lshl_add_u32 v147, v148, 3, v147
Mvaa 172,28,128
s_lshl_b32 s92, s57, 2
s_add_u32 s92, s92, s14
s_lshl_b32 s92, s92, 16
Mvaa 204,28,132
buffer_load_dword v142, v147, s[48:51], s92 offen
Ta 8,15,32768
Mvaa 204,24,36
Ta 10,15,33792
Ta 12,15,40960
Mvaa 236,24,40
Ta 14,15,41984
Mvaa 236,28,136
Maaa 172,28,140
Maaa 172,24,44
Maaa 204,24,48
Maaa 204,28,144
Maaa 236,28,148
Maaa 236,24,52
Maaa 76,24,56
Maaa 76,28,152
Maaa 108,28,156
Maaa 108,24,60
W 4
Maaa 112,0,60
Maaa 112,4,156
Maaa 80,4,152
Ta 16,24,32768
Ta 18,24,33792
Maaa 80,0,56
Ta 20,24,40960
Ta 22,24,41984
Maaa 240,0,52
Maaa 240,4,148
Maaa 208,4,144
Maaa 208,0,48
Maaa 176,0,44
Maaa 176,4,140
Mvaa 240,4,136
Mvaa 240,0,40
Mvaa 208,0,36
Mvaa 208,4,132
Mvaa 176,4,128
Mvaa 176,0,32
W 4
Mvaa 180,8,32
Ta 24,25,32768
Ta 26,25,33792
Mvaa 180,12,128
Mvaa 212,12,132
Ta 28,25,40960
Ta 30,25,41984
Mvaa 212,8,36
Mvaa 244,8,40
Mvaa 244,12,136
Maaa 180,12,140
Maaa 180,8,44
Maaa 212,8,48
Maaa 212,12,144
Maaa 244,12,148
s_waitcnt vmcnt(1) lgkmcnt(0)
s_barrier
s_cmp_eq_u32 s14, 0
s_cbranch_scc0 .Lpubr_14%=
s_lshl_b32 s80, s15, 6
s_add_u32 s80, s80, 40960
s_mov_b64 exec, 0xff
buffer_store_dwordx2 v[84:85], v83, s[8:11], s80 offen sc1
s_mov_b64 exec, -1
.Lpubr_14%=:
Maaa 244,8,52
s_add_u32 s64, s64, 128
Maaa 84,8,56
Maaa 84,12,152
s_and_b32 s64, s64, 1023
Maaa 116,12,156
Maaa 116,8,60
Maaa 120,16,60
s_lshl_b32 s65, s64, 12
Maaa 120,20,156
Maaa 88,20,152
s_add_u32 m0, s62, 32768
Ta 0,10,0
Ta 2,10,1024
Maaa 88,16,56
Ta 4,10,8192
Ta 6,10,9216
Maaa 248,16,52
Maaa 248,20,148
D 16,44,65,0
Maaa 216,20,144
Maaa 216,16,48
Maaa 184,16,44
Maaa 184,20,140
D 17,44,65,1024
Mvaa 248,20,136
Mvaa 248,16,40
Mvaa 216,16,36
Mvaa 216,20,132
D 18,44,65,2048
Mvaa 184,20,128
Mvaa 184,16,32
Mvaa 188,24,32
Ra 32,26,0
Mvaa 188,28,128
D 19,44,65,3072
Ra 128,27,0
Mvaa 220,28,132
s_add_u32 m0, s62, 36864
Ra 132,27,2048
Ta 8,11,0
Mvaa 220,24,36
Ta 10,11,1024
Ra 36,26,2048
Mvaa 252,24,40
Ta 12,11,8192
Ta 14,11,9216
Mvaa 252,28,136
D 20,44,65,0
Ra 40,26,4096
Maaa 188,28,140
Ra 136,27,4096
Ra 140,27,6144
Maaa 188,24,44
)"
R"(Ra 44,26,6144
Maaa 220,24,48
Ra 48,26,8192
Maaa 220,28,144
D 21,44,65,1024
Ra 144,27,8192
Maaa 252,28,148
Ra 148,27,10240
Maaa 252,24,52
Ra 52,26,10240
Maaa 92,24,56
Ra 56,26,12288
Maaa 92,28,152
D 22,44,65,2048
Ra 152,27,12288
Maaa 124,28,156
Ra 156,27,14336
Maaa 124,24,60
W 13
Ra 60,26,14336
Mvaa 160,0,32
Mvaa 160,4,128
D 23,44,65,3072
Mvaa 192,4,132
s_add_u32 m0, s61, 32768
Ta 16,12,0
Ta 18,12,1024
Mvaa 192,0,36
W 10
Ta 20,12,8192
Ta 22,12,9216
Mvaa 224,0,40
Mvaa 224,4,136
D 2,40,64,0
Maaa 160,4,140
Maaa 160,0,44
W 8
Maaa 192,0,48
Maaa 192,4,144
D 3,40,64,1024
Maaa 224,4,148
Maaa 224,0,52
W 4
Maaa 64,0,56
Maaa 64,4,152
D 4,40,64,2048
Maaa 96,4,156
Maaa 96,0,60
Maaa 100,8,60
Maaa 100,12,156
D 5,40,64,3072
Maaa 68,12,152
s_add_u32 m0, s61, 36864
Ta 24,13,0
Ta 26,13,1024
Maaa 68,8,56
Ta 28,13,8192
Ta 30,13,9216
Maaa 228,8,52
Maaa 228,12,148
D 6,40,64,0
Maaa 196,12,144
Maaa 196,8,48
Maaa 164,8,44
Maaa 164,12,140
D 7,40,64,1024
Mvaa 228,12,136
Mvaa 228,8,40
Mvaa 196,8,36
Mvaa 196,12,132
D 8,40,64,2048
Mvaa 164,12,128
Mvaa 164,8,32
W 4
Mvaa 168,16,32
Mvaa 168,20,128
D 9,40,64,3072
Mvaa 200,20,132
Ta 0,14,0
Ta 2,14,1024
Mvaa 200,16,36
Ta 4,14,8192
Ta 6,14,9216
Mvaa 232,16,40
Mvaa 232,20,136
global_load_dword v142, v[140:141], off
v_lshl_add_u64 v[140:141], v[140:141], 0, s[68:69]
Maaa 168,20,140
Maaa 168,16,44
Maaa 200,16,48
Maaa 200,20,144
Maaa 232,20,148
Maaa 232,16,52
Maaa 72,16,56
Maaa 72,20,152
Maaa 104,20,156
Maaa 104,16,60
W 4
Maaa 108,24,60
Maaa 108,28,156
Maaa 76,28,152
Ta 8,15,0
Ta 10,15,1024
Maaa 76,24,56
Ta 12,15,8192
Ta 14,15,9216
Maaa 236,24,52
Maaa 236,28,148
Maaa 204,28,144
Maaa 204,24,48
Maaa 172,24,44
Maaa 172,28,140
Mvaa 236,28,136
Mvaa 236,24,40
Mvaa 204,24,36
Mvaa 204,28,132
Mvaa 172,28,128
Mvaa 172,24,32
W 4
Mvaa 176,0,32
Mvaa 176,4,128
Mvaa 208,4,132
Ta 16,24,0
Ta 18,24,1024
Mvaa 208,0,36
Ta 20,24,8192
Ta 22,24,9216
Mvaa 240,0,40
Mvaa 240,4,136
Maaa 176,4,140
Maaa 176,0,44
Maaa 208,0,48
Maaa 208,4,144
Maaa 240,4,148
Maaa 240,0,52
Maaa 80,0,56
Maaa 80,4,152
Maaa 112,4,156
Maaa 112,0,60
W 4
Maaa 116,8,60
Ta 24,25,0
Ta 26,25,1024
Maaa 116,12,156
Maaa 84,12,152
Ta 28,25,8192
Ta 30,25,9216
Maaa 84,8,56
Maaa 244,8,52
Maaa 244,12,148
Maaa 212,12,144
Maaa 212,8,48
Maaa 180,8,44
Maaa 180,12,140
Mvaa 244,12,136
s_waitcnt vmcnt(1) lgkmcnt(0)
s_barrier
s_add_u32 s64, s64, 128
Mvaa 244,8,40
Mvaa 212,8,36
Mvaa 212,12,132
s_and_b32 s64, s64, 1023
Mvaa 180,12,128
Mvaa 180,8,32
Mvaa 184,16,32
s_lshl_b32 s65, s64, 12
Mvaa 184,20,128
Mvaa 216,20,132
s_add_u32 m0, s62, 0
Ta 0,10,32768
Ta 2,10,33792
Mvaa 216,16,36
Ta 4,10,40960
Ta 6,10,41984
Mvaa 248,16,40
Mvaa 248,20,136
D 16,44,65,0
Maaa 184,20,140
Maaa 184,16,44
Maaa 216,16,48
Maaa 216,20,144
D 17,44,65,1024
Maaa 248,20,148
Maaa 248,16,52
Maaa 88,16,56
Maaa 88,20,152
D 18,44,65,2048
Maaa 120,20,156
Maaa 120,16,60
Maaa 124,24,60
Ra 60,26,47104
Maaa 124,28,156
D 19,44,65,3072
Ra 156,27,47104
Maaa 92,28,152
s_add_u32 m0, s62, 4096
Ra 152,27,45056
Ta 8,11,32768
Maaa 92,24,56
Ta 10,11,33792
Ra 56,26,45056
Maaa 252,24,52
Ta 12,11,40960
Ta 14,11,41984
Maaa 252,28,148
D 20,44,65,0
Ra 52,26,43008
Maaa 220,28,144
Ra 148,27,43008
Ra 144,27,40960
Maaa 220,24,48
Ra 48,26,40960
Maaa 188,24,44
Ra 44,26,38912
Maaa 188,28,140
D 21,44,65,1024
Ra 140,27,38912
Mvaa 252,28,136
Ra 136,27,36864
Mvaa 252,24,40
Ra 40,26,36864
Mvaa 220,24,36
Ra 36,26,34816
Mvaa 220,28,132
D 22,44,65,2048
Ra 132,27,34816
Mvaa 188,28,128
Ra 128,27,32768
Mvaa 188,24,32
W 13
Ra 32,26,32768
Maaa 96,0,60
Maaa 96,4,156
D 23,44,65,3072
Maaa 64,4,152
s_add_u32 m0, s61, 0
Ta 16,12,32768
Ta 18,12,33792
Maaa 64,0,56
W 10
Ta 20,12,40960
Ta 22,12,41984
Maaa 224,0,52
Maaa 224,4,148
D 2,40,64,0
Maaa 192,4,144
Maaa 192,0,48
W 8
Maaa 160,0,44
Maaa 160,4,140
D 3,40,64,1024
Mvaa 224,4,136
Mvaa 224,0,40
W 4
Mvaa 192,0,36
Mvaa 192,4,132
D 4,40,64,2048
Mvaa 160,4,128
Mvaa 160,0,32
Mvaa 164,8,32
Mvaa 164,12,128
D 5,40,64,3072
Mvaa 196,12,132
s_add_u32 m0, s61, 4096
Ta 24,13,32768
Ta 26,13,33792
Mvaa 196,8,36
Ta 28,13,40960
Ta 30,13,41984
Mvaa 228,8,40
Mvaa 228,12,136
D 6,40,64,0
Maaa 164,12,140
Maaa 164,8,44
Maaa 196,8,48
Maaa 196,12,144
D 7,40,64,1024
Maaa 228,12,148
Maaa 228,8,52
Maaa 68,8,56
Maaa 68,12,152
D 8,40,64,2048
Maaa 100,12,156
Maaa 100,8,60
W 4
Maaa 104,16,60
Maaa 104,20,156
D 9,40,64,3072
Maaa 72,20,152
Ta 0,14,32768
Ta 2,14,33792
Maaa 72,16,56
Ta 4,14,40960
Ta 6,14,41984
Maaa 232,16,52
Maaa 232,20,148
global_load_dword v142, v[140:141], off
v_lshl_add_u64 v[140:141], v[140:141], 0, s[68:69]
Maaa 200,20,144
Maaa 200,16,48
Maaa 168,16,44
Maaa 168,20,140
Mvaa 232,20,136
Mvaa 232,16,40
Mvaa 200,16,36
Mvaa 200,20,132
Mvaa 168,20,128
Mvaa 168,16,32
W 4
Mvaa 172,24,32
Mvaa 172,28,128
Mvaa 204,28,132
Ta 8,15,32768
Ta 10,15,33792
Mvaa 204,24,36
Ta 12,15,40960
Ta 14,15,41984
Mvaa 236,24,40
Mvaa 236,28,136
Maaa 172,28,140
Maaa 172,24,44
Maaa 204,24,48
Maaa 204,28,144
Maaa 236,28,148
Maaa 236,24,52
Maaa 76,24,56
Maaa 76,28,152
Maaa 108,28,156
Maaa 108,24,60
W 4
Maaa 112,0,60
Maaa 112,4,156
Maaa 80,4,152
Ta 16,24,32768
Ta 18,24,33792
Maaa 80,0,56
Ta 20,24,40960
Ta 22,24,41984
Maaa 240,0,52
Maaa 240,4,148
Maaa 208,4,144
Maaa 208,0,48
Maaa 176,0,44
Maaa 176,4,140
Mvaa 240,4,136
Mvaa 240,0,40
Mvaa 208,0,36
Mvaa 208,4,132
Mvaa 176,4,128
Mvaa 176,0,32
W 4
Mvaa 180,8,32
Ta 24,25,32768
Ta 26,25,33792
Mvaa 180,12,128
Mvaa 212,12,132
Ta 28,25,40960
Ta 30,25,41984
Mvaa 212,8,36
Mvaa 244,8,40
Mvaa 244,12,136
.Lh_0_2%=:
s_cbranch_execz .Lh_0_2_1%=
Maaa 180,12,140
Maaa 180,8,44
Maaa 212,8,48
Maaa 212,12,144
Maaa 244,12,148
s_waitcnt vmcnt(1) lgkmcnt(0)
s_barrier
s_add_u32 s64, s64, 128
Maaa 244,8,52
Maaa 84,8,56
Maaa 84,12,152
s_and_b32 s64, s64, 1023
Maaa 116,12,156
Maaa 116,8,60
Maaa 120,16,60
s_lshl_b32 s65, s64, 12
Maaa 120,20,156
Maaa 88,20,152
Ta 0,10,0
Ta 2,10,1024
Maaa 88,16,56
s_add_u32 m0, s62, 32768
Ta 4,10,8192
Ta 6,10,9216
Maaa 248,16,52
Maaa 248,20,148
Maaa 216,20,144
D 16,44,65,0
Maaa 216,16,48
Maaa 184,16,44
Maaa 184,20,140
Mvaa 248,20,136
D 17,44,65,1024
Mvaa 248,16,40
Mvaa 216,16,36
Mvaa 216,20,132
Mvaa 184,20,128
)"
R"(D 18,44,65,2048
Mvaa 184,16,32
Mvaa 188,24,32
Ra 32,26,0
Mvaa 188,28,128
Ra 128,27,0
Mvaa 220,28,132
D 19,44,65,3072
Ra 132,27,2048
Mvaa 220,24,36
s_add_u32 m0, s62, 36864
Ta 8,11,0
Ta 10,11,1024
Mvaa 252,24,40
Ra 36,26,2048
Ta 12,11,8192
Mvaa 252,28,136
Ta 14,11,9216
Ra 40,26,4096
Maaa 188,28,140
D 20,44,65,0
Ra 136,27,4096
Maaa 188,24,44
Ra 140,27,6144
Ra 44,26,6144
Maaa 220,24,48
Ra 48,26,8192
Maaa 220,28,144
Ra 144,27,8192
Maaa 252,28,148
D 21,44,65,1024
Ra 148,27,10240
Maaa 252,24,52
Ra 52,26,10240
Maaa 92,24,56
Ra 56,26,12288
Maaa 92,28,152
Ra 152,27,12288
Maaa 124,28,156
D 22,44,65,2048
Ra 156,27,14336
Maaa 124,24,60
W 13
Ra 60,26,14336
Mvaa 160,0,32
Mvaa 160,4,128
Mvaa 192,4,132
D 23,44,65,3072
Ta 16,12,0
Mvaa 192,0,36
W 9
s_add_u32 m0, s61, 32768
Ta 18,12,1024
Mvaa 224,0,40
Ta 20,12,8192
Ta 22,12,9216
Mvaa 224,4,136
Maaa 160,4,140
D 2,40,64,0
Maaa 160,0,44
W 8
Maaa 192,0,48
Maaa 192,4,144
Maaa 224,4,148
D 3,40,64,1024
Maaa 224,0,52
W 4
Maaa 64,0,56
Maaa 64,4,152
Maaa 96,4,156
D 4,40,64,2048
Maaa 96,0,60
Maaa 100,8,60
Maaa 100,12,156
Maaa 68,12,152
D 5,40,64,3072
Ta 24,13,0
Maaa 68,8,56
s_add_u32 m0, s61, 36864
Ta 26,13,1024
Ta 28,13,8192
Maaa 228,8,52
Ta 30,13,9216
Maaa 228,12,148
Maaa 196,12,144
D 6,40,64,0
Maaa 196,8,48
Maaa 164,8,44
Maaa 164,12,140
Mvaa 228,12,136
D 7,40,64,1024
Mvaa 228,8,40
Mvaa 196,8,36
Mvaa 196,12,132
Mvaa 164,12,128
D 8,40,64,2048
Mvaa 164,8,32
W 4
Mvaa 168,16,32
Mvaa 168,20,128
Mvaa 200,20,132
D 9,40,64,3072
Ta 0,14,0
Mvaa 200,16,36
s_add_u32 s92, s92, 2097152
Ta 2,14,1024
Ta 4,14,8192
Mvaa 232,16,40
Ta 6,14,9216
Mvaa 232,20,136
Maaa 168,20,140
buffer_load_dword v142, v147, s[48:51], s92 offen
Maaa 168,16,44
s_add_u32 s92, s92, 2097152
Maaa 200,16,48
Maaa 200,20,144
Maaa 232,20,148
buffer_load_dword v142, v147, s[48:51], s92 offen
Maaa 232,16,52
Maaa 72,16,56
Maaa 72,20,152
Maaa 104,20,156
Maaa 104,16,60
W 4
Maaa 108,24,60
Maaa 108,28,156
Maaa 76,28,152
Ta 8,15,0
Ta 10,15,1024
Maaa 76,24,56
Ta 12,15,8192
Ta 14,15,9216
Maaa 236,24,52
Maaa 236,28,148
Maaa 204,28,144
Maaa 204,24,48
Maaa 172,24,44
Maaa 172,28,140
Mvaa 236,28,136
Mvaa 236,24,40
Mvaa 204,24,36
Mvaa 204,28,132
Mvaa 172,28,128
Mvaa 172,24,32
W 4
Mvaa 176,0,32
Mvaa 176,4,128
Mvaa 208,4,132
Ta 16,24,0
Ta 18,24,1024
Mvaa 208,0,36
Ta 20,24,8192
Ta 22,24,9216
Mvaa 240,0,40
Mvaa 240,4,136
Maaa 176,4,140
Maaa 176,0,44
Maaa 208,0,48
Maaa 208,4,144
Maaa 240,4,148
Maaa 240,0,52
Maaa 80,0,56
Maaa 80,4,152
Maaa 112,4,156
Maaa 112,0,60
W 4
Maaa 116,8,60
Ta 24,25,0
Ta 26,25,1024
Maaa 116,12,156
Maaa 84,12,152
Ta 28,25,8192
Ta 30,25,9216
Maaa 84,8,56
Maaa 244,8,52
Maaa 244,12,148
Maaa 212,12,144
Maaa 212,8,48
Maaa 180,8,44
Maaa 180,12,140
Mvaa 244,12,136
s_waitcnt vmcnt(2) lgkmcnt(0)
s_barrier
s_add_u32 s64, s64, 128
Mvaa 244,8,40
Mvaa 212,8,36
Mvaa 212,12,132
s_and_b32 s64, s64, 1023
Mvaa 180,12,128
Mvaa 180,8,32
Mvaa 184,16,32
s_lshl_b32 s65, s64, 12
Mvaa 184,20,128
Mvaa 216,20,132
Ta 0,10,32768
Ta 2,10,33792
Mvaa 216,16,36
s_add_u32 m0, s62, 0
Ta 4,10,40960
Ta 6,10,41984
Mvaa 248,16,40
Mvaa 248,20,136
Maaa 184,20,140
D 16,44,65,0
Maaa 184,16,44
Maaa 216,16,48
Maaa 216,20,144
Maaa 248,20,148
D 17,44,65,1024
Maaa 248,16,52
Maaa 88,16,56
Maaa 88,20,152
Maaa 120,20,156
D 18,44,65,2048
Maaa 120,16,60
Maaa 124,24,60
Ra 60,26,47104
Maaa 124,28,156
Ra 156,27,47104
Maaa 92,28,152
D 19,44,65,3072
Ra 152,27,45056
Maaa 92,24,56
s_add_u32 m0, s62, 4096
Ta 8,11,32768
Ta 10,11,33792
Maaa 252,24,52
Ra 56,26,45056
Ta 12,11,40960
Maaa 252,28,148
Ta 14,11,41984
Ra 52,26,43008
Maaa 220,28,144
D 20,44,65,0
v_lshrrev_b32 v28, 1, v1
Maaa 220,24,48
s_lshl_b32 s80, s60, 9
Ra 148,27,43008
Ra 144,27,40960
Maaa 188,24,44
s_add_u32 s80, s80, 40960
Ra 48,26,40960
Ra 44,26,38912
Maaa 188,28,140
Ra 140,27,38912
Mvaa 252,28,136
D 21,44,65,1024
Ra 136,27,36864
Mvaa 252,24,40
Ra 40,26,36864
Mvaa 220,24,36
Ra 36,26,34816
Mvaa 220,28,132
Ra 132,27,34816
Mvaa 188,28,128
buffer_load_dwordx2 v[28:29], v28, s[8:11], s80 offen sc1
Ra 128,27,32768
Mvaa 188,24,32
W 13
Ra 32,26,32768
Maaa 96,0,60
Maaa 96,4,156
Maaa 64,4,152
D 22,44,65,2048
Ta 16,12,32768
Maaa 64,0,56
W 9
Ta 18,12,33792
Ta 20,12,40960
Maaa 224,0,52
Ta 22,12,41984
Maaa 224,4,148
Maaa 192,4,144
D 23,44,65,3072
Maaa 192,0,48
W 8
s_add_u32 m0, s61, 0
Maaa 160,0,44
Maaa 160,4,140
Mvaa 224,4,136
D 2,40,64,0
Mvaa 224,0,40
W 4
Mvaa 192,0,36
Mvaa 192,4,132
Mvaa 160,4,128
D 3,40,64,1024
Mvaa 160,0,32
Mvaa 164,8,32
Mvaa 164,12,128
Mvaa 196,12,132
D 4,40,64,2048
Ta 24,13,32768
Mvaa 196,8,36
Ta 26,13,33792
Ta 28,13,40960
Mvaa 228,8,40
Ta 30,13,41984
Mvaa 228,12,136
Maaa 164,12,140
D 5,40,64,3072
Maaa 164,8,44
s_add_u32 m0, s61, 4096
Maaa 196,8,48
Maaa 196,12,144
Maaa 228,12,148
D 6,40,64,0
Maaa 228,8,52
Maaa 68,8,56
Maaa 68,12,152
Maaa 100,12,156
D 7,40,64,1024
Maaa 100,8,60
W 4
Maaa 104,16,60
Maaa 104,20,156
Maaa 72,20,152
D 8,40,64,2048
Ta 0,14,32768
Maaa 72,16,56
Ta 2,14,33792
Ta 4,14,40960
Maaa 232,16,52
Ta 6,14,41984
Maaa 232,20,148
Maaa 200,20,144
D 9,40,64,3072
Maaa 200,16,48
Maaa 168,16,44
Maaa 168,20,140
Mvaa 232,20,136
s_waitcnt vmcnt(10)
v_cmp_eq_u32 vcc, s13, v29
s_cmp_eq_u64 vcc, -1
s_cbranch_scc0 .Lspin_15%=
.Lok_16%=:
global_load_dword v142, v146, s[6:7]
Mvaa 232,16,40
Mvaa 200,16,36
v_bfe_u32 v144, v1, 8, 1
Mvaa 200,20,132
v_mul_u32_u24 v144, 10240, v144
Mvaa 168,20,128
v_and_b32 v145, 0xf0, v1
Mvaa 168,16,32
W 4
v_lshl_add_u32 v144, v145, 3, v144
Mvaa 172,24,32
s_mul_i32 s91, s58, 20480
Mvaa 172,28,128
s_add_u32 s91, s91, s28
Mvaa 204,28,132
buffer_load_dword v142, v144, s[8:11], s91 offen offset:0 sc1
Ta 8,15,32768
Mvaa 204,24,36
Ta 10,15,33792
Ta 12,15,40960
Mvaa 236,24,40
Ta 14,15,41984
Mvaa 236,28,136
Maaa 172,28,140
Maaa 172,24,44
Maaa 204,24,48
Maaa 204,28,144
Maaa 236,28,148
Maaa 236,24,52
Maaa 76,24,56
Maaa 76,28,152
Maaa 108,28,156
Maaa 108,24,60
W 4
Maaa 112,0,60
Maaa 112,4,156
Maaa 80,4,152
Ta 16,24,32768
Ta 18,24,33792
Maaa 80,0,56
Ta 20,24,40960
Ta 22,24,41984
Maaa 240,0,52
Maaa 240,4,148
Maaa 208,4,144
Maaa 208,0,48
Maaa 176,0,44
Maaa 176,4,140
Mvaa 240,4,136
Mvaa 240,0,40
Mvaa 208,0,36
Mvaa 208,4,132
Mvaa 176,4,128
Mvaa 176,0,32
W 4
Mvaa 180,8,32
Ta 24,25,32768
Ta 26,25,33792
Mvaa 180,12,128
Mvaa 212,12,132
Ta 28,25,40960
Ta 30,25,41984
Mvaa 212,8,36
)"
R"(Mvaa 244,8,40
Mvaa 244,12,136
Maaa 180,12,140
Maaa 180,8,44
Maaa 212,8,48
Maaa 212,12,144
Maaa 244,12,148
s_waitcnt vmcnt(2) lgkmcnt(0)
s_barrier
s_add_u32 s64, s64, 128
Maaa 244,8,52
Maaa 84,8,56
Maaa 84,12,152
s_and_b32 s64, s64, 1023
Maaa 116,12,156
Maaa 116,8,60
Maaa 120,16,60
s_lshl_b32 s65, s64, 12
Maaa 120,20,156
Maaa 88,20,152
Ta 0,10,0
Ta 2,10,1024
Maaa 88,16,56
s_add_u32 m0, s62, 32768
Ta 4,10,8192
Ta 6,10,9216
Maaa 248,16,52
Maaa 248,20,148
Maaa 216,20,144
D 16,44,65,0
Maaa 216,16,48
Maaa 184,16,44
Maaa 184,20,140
Mvaa 248,20,136
D 17,44,65,1024
Mvaa 248,16,40
Mvaa 216,16,36
Mvaa 216,20,132
Mvaa 184,20,128
D 18,44,65,2048
Mvaa 184,16,32
Mvaa 188,24,32
Ra 32,26,0
Mvaa 188,28,128
Ra 128,27,0
Mvaa 220,28,132
D 19,44,65,3072
Ra 132,27,2048
Mvaa 220,24,36
s_add_u32 m0, s62, 36864
Ta 8,11,0
Ta 10,11,1024
Mvaa 252,24,40
Ra 36,26,2048
Ta 12,11,8192
Mvaa 252,28,136
Ta 14,11,9216
Ra 40,26,4096
Maaa 188,28,140
D 20,44,65,0
Ra 136,27,4096
Maaa 188,24,44
Ra 140,27,6144
Ra 44,26,6144
Maaa 220,24,48
Ra 48,26,8192
Maaa 220,28,144
Ra 144,27,8192
Maaa 252,28,148
D 21,44,65,1024
Ra 148,27,10240
Maaa 252,24,52
Ra 52,26,10240
Maaa 92,24,56
Ra 56,26,12288
Maaa 92,28,152
Ra 152,27,12288
Maaa 124,28,156
D 22,44,65,2048
Ra 156,27,14336
Maaa 124,24,60
W 13
Ra 60,26,14336
Mvaa 160,0,32
Mvaa 160,4,128
Mvaa 192,4,132
D 23,44,65,3072
Ta 16,12,0
Mvaa 192,0,36
W 9
s_add_u32 m0, s61, 32768
Ta 18,12,1024
Mvaa 224,0,40
Ta 20,12,8192
Ta 22,12,9216
Mvaa 224,4,136
.Lh_0_3%=:
s_cbranch_execz .Lh_0_3_1%=
Maaa 160,4,140
D 2,40,64,0
Maaa 160,0,44
W 8
Maaa 192,0,48
Maaa 192,4,144
Maaa 224,4,148
D 3,40,64,1024
Maaa 224,0,52
W 4
Maaa 64,0,56
Maaa 64,4,152
Maaa 96,4,156
D 4,40,64,2048
Maaa 96,0,60
Maaa 100,8,60
Maaa 100,12,156
Maaa 68,12,152
D 5,40,64,3072
Ta 24,13,0
Maaa 68,8,56
s_add_u32 m0, s61, 36864
Ta 26,13,1024
Ta 28,13,8192
Maaa 228,8,52
Ta 30,13,9216
Maaa 228,12,148
Maaa 196,12,144
D 6,40,64,0
Maaa 196,8,48
Maaa 164,8,44
Maaa 164,12,140
Mvaa 228,12,136
D 7,40,64,1024
Mvaa 228,8,40
Mvaa 196,8,36
Mvaa 196,12,132
Mvaa 164,12,128
D 8,40,64,2048
Mvaa 164,8,32
W 4
Mvaa 168,16,32
Mvaa 168,20,128
Mvaa 200,20,132
D 9,40,64,3072
v_add_u32 v2, 131072, v10
Mvaa 200,16,36
Ta 0,14,0
v_add_u32 v3, 131072, v11
Mvaa 232,16,40
Ta 2,14,1024
v_add_u32 v4, 131072, v12
Mvaa 232,20,136
Ta 4,14,8192
Ta 6,14,9216
Maaa 168,20,140
buffer_load_dword v142, v144, s[8:11], s91 offen offset:2048 sc1
v_add_u32 v5, 131072, v13
Maaa 168,16,44
s_add_u32 s91, s91, 4096
v_add_u32 v6, 131072, v14
Maaa 200,16,48
v_add_u32 v7, 131072, v15
Maaa 200,20,144
Maaa 232,20,148
v_add_u32 v8, 131072, v24
Maaa 232,16,52
Maaa 72,16,56
v_add_u32 v9, 131072, v25
Maaa 72,20,152
s_add_u32 s65, s5, 0
s_cmp_ge_u32 s65, 20
s_cselect_b32 s90, 20, 0
s_sub_u32 s65, s65, s90
Maaa 104,20,156
s_lshl_b32 s65, s65, 18
s_add_u32 m0, s63, 131072
Maaa 104,16,60
W 4
Maaa 108,24,60
Maaa 108,28,156
Maaa 76,28,152
Ta 8,15,0
Ta 10,15,1024
Maaa 76,24,56
D 16,48,65,0
Ta 12,15,8192
Maaa 236,24,52
Ta 14,15,9216
Maaa 236,28,148
Maaa 204,28,144
Maaa 204,24,48
D 17,48,65,1024
Maaa 172,24,44
Maaa 172,28,140
Mvaa 236,28,136
Mvaa 236,24,40
Mvaa 204,24,36
D 18,48,65,2048
Mvaa 204,28,132
Mvaa 172,28,128
Mvaa 172,24,32
W 4
Mvaa 176,0,32
Mvaa 176,4,128
D 19,48,65,3072
Mvaa 208,4,132
s_add_u32 m0, s63, 135168
Ta 16,24,0
Ta 18,24,1024
Mvaa 208,0,36
Ta 20,24,8192
Ta 22,24,9216
Mvaa 240,0,40
Mvaa 240,4,136
Maaa 176,4,140
D 20,48,65,0
Maaa 176,0,44
Maaa 208,0,48
Maaa 208,4,144
Maaa 240,4,148
Maaa 240,0,52
D 21,48,65,1024
Maaa 80,0,56
Maaa 80,4,152
Maaa 112,4,156
Maaa 112,0,60
W 4
Maaa 116,8,60
D 22,48,65,2048
Ta 24,25,0
Maaa 116,12,156
Ta 26,25,1024
Maaa 84,12,152
Ta 28,25,8192
Ta 30,25,9216
Maaa 84,8,56
Maaa 244,8,52
D 23,48,65,3072
Maaa 244,12,148
Maaa 212,12,144
Maaa 212,8,48
Maaa 180,8,44
Maaa 180,12,140
Mvaa 244,12,136
s_waitcnt vmcnt(9) lgkmcnt(0)
s_barrier
s_add_u32 s65, s5, 4
Mvaa 244,8,40
Mvaa 212,8,36
Mvaa 212,12,132
s_cmp_ge_u32 s65, 20
s_cselect_b32 s90, 20, 0
Mvaa 180,12,128
Mvaa 180,8,32
s_sub_u32 s65, s65, s90
Mvaa 184,16,32
Mvaa 184,20,128
s_lshl_b32 s65, s65, 18
Mvaa 216,20,132
Ta 0,10,32768
Ta 2,10,33792
Mvaa 216,16,36
Mvaa 248,16,40
s_add_u32 m0, s63, 0
Mvaa 248,20,136
Maaa 184,20,140
D 16,48,65,0
Maaa 184,16,44
Maaa 216,16,48
Maaa 216,20,144
Maaa 248,20,148
D 17,48,65,1024
Ta 4,11,32768
Maaa 248,16,52
Ta 6,11,33792
Maaa 88,16,56
Maaa 88,20,152
Maaa 120,20,156
D 18,48,65,2048
Maaa 120,16,60
Maaa 124,24,60
Ra 60,26,47104
Maaa 124,28,156
Maaa 92,28,152
D 19,48,65,3072
Ta 8,12,32768
Maaa 92,24,56
s_add_u32 m0, s63, 4096
Ta 10,12,33792
Ra 56,26,45056
Maaa 252,24,52
Ra 52,26,43008
Maaa 252,28,148
Maaa 220,28,144
D 20,48,65,0
Maaa 220,24,48
Ra 48,26,40960
Maaa 188,24,44
Ra 44,26,38912
Maaa 188,28,140
Mvaa 252,28,136
D 21,48,65,1024
Ta 12,13,32768
Mvaa 252,24,40
Ta 14,13,33792
Ra 40,26,36864
Mvaa 220,24,36
Ra 36,26,34816
Mvaa 220,28,132
Mvaa 188,28,128
D 22,48,65,2048
Mvaa 188,24,32
Ra 32,26,32768
W 6
Maaa 96,0,60
Maaa 64,0,56
Maaa 224,0,52
D 23,48,65,3072
Ta 16,14,32768
Maaa 192,0,48
W 1
s_add_u32 s65, s5, 8
s_cmp_ge_u32 s65, 20
s_cselect_b32 s90, 20, 0
Maaa 160,0,44
s_sub_u32 s65, s65, s90
s_lshl_b32 s65, s65, 18
Ta 18,14,33792
Mvaa 224,0,40
s_add_u32 m0, s63, 65536
Mvaa 192,0,36
D 16,48,65,0
Mvaa 160,0,32
Mvaa 164,4,32
Mvaa 196,4,36
Mvaa 228,4,40
D 17,48,65,1024
Ta 20,15,32768
Maaa 164,4,44
Ta 22,15,33792
Maaa 196,4,48
Maaa 228,4,52
Maaa 68,4,56
D 18,48,65,2048
Maaa 100,4,60
Maaa 104,8,60
Maaa 72,8,56
Maaa 232,8,52
D 19,48,65,3072
Ta 24,24,32768
Maaa 200,8,48
s_add_u32 m0, s63, 69632
Ta 26,24,33792
Maaa 168,8,44
Mvaa 232,8,40
Mvaa 200,8,36
D 20,48,65,0
Mvaa 168,8,32
Mvaa 172,12,32
Mvaa 204,12,36
Mvaa 236,12,40
D 21,48,65,1024
Ta 28,25,32768
Maaa 172,12,44
Ta 30,25,33792
Maaa 204,12,48
Ra 156,27,47104
Maaa 236,12,52
Maaa 76,12,56
D 22,48,65,2048
Maaa 108,12,60
W 7
Ra 152,27,45056
Maaa 112,16,60
Maaa 80,16,56
Maaa 240,16,52
D 23,48,65,3072
Ra 148,27,43008
Maaa 208,16,48
Ta 0,10,40960
Ta 2,10,41984
Maaa 176,16,44
Mvaa 240,16,40
Ra 144,27,40960
Mvaa 208,16,36
buffer_load_dword v142, v144, s[8:11], s91 offen offset:0 sc1
Mvaa 176,16,32
W 10
Mvaa 180,20,32
Ra 140,27,38912
Mvaa 212,20,36
Mvaa 244,20,40
Ta 4,11,40960
Ta 6,11,41984
Maaa 180,20,44
Ra 136,27,36864
Maaa 212,20,48
Maaa 244,20,52
v_bfe_u32 v29, v1, 7, 1
Maaa 84,20,56
Ra 132,27,34816
)"
R"(Maaa 116,20,60
W 13
v_mul_u32_u24 v28, 10240, v29
Maaa 120,24,60
Maaa 88,24,56
Ra 128,27,32768
v_lshrrev_b32 v29, 8, v1
Maaa 248,24,52
Ta 8,12,40960
Ta 10,12,41984
Maaa 216,24,48
v_lshl_add_u32 v28, v29, 7, v28
Maaa 184,24,44
v_and_b32 v29, 0x70, v1
Mvaa 248,24,40
Mvaa 216,24,36
v_add_u32 v28, v28, v29
Mvaa 184,24,32
W 14
Mvaa 188,28,32
s_and_b32 s80, s14, 2
Mvaa 220,28,36
s_mul_i32 s80, s80, 20480
Mvaa 252,28,40
Ta 12,13,40960
Ta 14,13,41984
Maaa 188,28,44
s_sub_u32 s81, 40960, s80
Maaa 220,28,48
Maaa 252,28,52
s_add_u32 s93, s28, 20480
Maaa 92,28,56
Maaa 124,28,60
W 10
v_add_u32 v26, s80, v28
Maaa 96,0,156
Maaa 64,0,152
Maaa 224,0,148
Ta 16,14,40960
v_add_u32 v27, s81, v28
Maaa 192,0,144
W 5
Ta 18,14,41984
Maaa 160,0,140
buffer_load_dwordx4 a[32:35], v26, s[8:11], s28 offen offset:0 sc1
Mvaa 224,0,136
Mvaa 192,0,132
Mvaa 160,0,128
Mvaa 164,4,128
buffer_load_dwordx4 a[36:39], v26, s[8:11], s93 offen offset:0 sc1
Mvaa 196,4,132
Mvaa 228,4,136
Ta 20,15,40960
Ta 22,15,41984
Maaa 164,4,140
Maaa 196,4,144
buffer_load_dwordx4 a[40:43], v27, s[8:11], s28 offen offset:0 sc1
Maaa 228,4,148
Maaa 68,4,152
Maaa 100,4,156
W 6
Maaa 104,8,156
buffer_load_dwordx4 a[44:47], v27, s[8:11], s93 offen offset:0 sc1
Ta 24,24,40960
Maaa 72,8,152
s_add_u32 s28, s28, 512
s_cmp_eq_u32 s28, s4
s_cselect_b32 s93, 10240, 0
Ta 26,24,41984
Maaa 232,8,148
s_sub_u32 s28, s28, s93
Ta 28,25,40960
Ta 30,25,41984
Maaa 200,8,144
s_add_u32 s93, s28, 20480
Maaa 168,8,140
Mvaa 232,8,136
Mvaa 200,8,132
s_add_u32 s65, s5, 12
s_cmp_ge_u32 s65, 20
s_cselect_b32 s90, 20, 0
buffer_load_dwordx4 a[48:51], v26, s[8:11], s28 offen offset:0 sc1
Mvaa 168,8,128
W 8
s_sub_u32 s65, s65, s90
s_lshl_b32 s65, s65, 18
Mvaa 172,12,128
s_add_u32 m0, s63, 32768
Mvaa 204,12,132
Mvaa 236,12,136
s_waitcnt vmcnt(6) lgkmcnt(0)
s_barrier
buffer_load_dwordx4 a[52:55], v26, s[8:11], s93 offen offset:0 sc1
Maaa 172,12,140
Maaa 204,12,144
Maaa 236,12,148
Maaa 76,12,152
buffer_load_dwordx4 a[56:59], v27, s[8:11], s28 offen offset:0 sc1
Maaa 108,12,156
Maaa 112,16,156
Maaa 80,16,152
Maaa 240,16,148
buffer_load_dwordx4 a[60:63], v27, s[8:11], s93 offen offset:0 sc1
Maaa 208,16,144
Maaa 176,16,140
Mvaa 240,16,136
Mvaa 208,16,132
D 16,48,65,0
Mvaa 176,16,128
Mvaa 180,20,128
Mvaa 212,20,132
Mvaa 244,20,136
D 17,48,65,1024
Ta 0,2,0
Maaa 180,20,140
Ta 2,2,1024
Maaa 212,20,144
Maaa 244,20,148
Maaa 84,20,152
D 18,48,65,2048
Ta 4,2,8192
Maaa 116,20,156
Ta 6,2,9216
Maaa 120,24,156
Maaa 88,24,152
Maaa 248,24,148
Ta 8,3,0
Ta 10,3,1024
Maaa 216,24,144
D 19,48,65,3072
Maaa 184,24,140
s_add_u32 m0, s63, 36864
Mvaa 248,24,136
Mvaa 216,24,132
Ta 12,3,8192
Ta 14,3,9216
Mvaa 184,24,128
D 20,48,65,0
Mvaa 188,28,128
Mvaa 220,28,132
Mvaa 252,28,136
Ta 16,4,0
Ta 18,4,1024
Maaa 188,28,140
Maaa 220,28,144
D 21,48,65,1024
Maaa 252,28,148
Maaa 92,28,152
Maaa 124,28,156
s_branch .Lq_11%=
.p2align 12
.Lv_10%=:
s_waitcnt vmcnt(14)
s_barrier
Ta 0,10,16384
Ta 2,10,17408
Ra 32,26,0
Ta 4,10,24576
.Lh_0_0_1%=:
s_cbranch_execz .Lhr_9%=
Ta 6,10,25600
Ra 128,27,0
Ra 132,27,2048
Ra 36,26,2048
Ra 40,26,4096
Ra 136,27,4096
Ra 140,27,6144
Ra 44,26,6144
Ra 48,26,8192
Ra 144,27,8192
W 6
Zvaa 160,32,0
s_add_u32 m0, s61, 32768
Ra 148,27,10240
Ra 52,26,10240
Ra 56,26,12288
Ra 152,27,12288
Ra 156,27,14336
Ra 60,26,14336
Mvaa 160,128,4
Ta 8,11,16384
Ta 10,11,17408
Ta 12,11,24576
Ta 14,11,25600
Zvaa 164,132,4
Ta 16,12,16384
Ta 18,12,17408
Mvaa 164,36,0
W 14
Ta 20,12,24576
Ta 22,12,25600
Zvaa 224,40,0
D 2,40,64,0
Mvaa 224,136,4
Zvaa 228,140,4
Mvaa 228,44,0
W 12
Zaaa 192,48,0
D 3,40,64,1024
Maaa 192,144,4
Zaaa 196,148,4
Maaa 196,52,0
W 8
Zaaa 64,56,0
D 4,40,64,2048
Maaa 64,152,4
Zaaa 68,156,4
Maaa 68,60,0
W 4
Zaaa 76,60,8
D 5,40,64,3072
Maaa 76,156,12
s_add_u32 m0, s61, 36864
Zaaa 72,152,12
Ta 24,13,16384
Ta 26,13,17408
Maaa 72,56,8
Ta 28,13,24576
Ta 30,13,25600
Zaaa 204,52,8
s_waitcnt vmcnt(15)
D 6,40,64,0
Maaa 204,148,12
v_lshlrev_b32 v56, 16, v32
v_and_b32 v57, 0xffff0000, v32
v_lshlrev_b32 v58, 16, v33
v_and_b32 v59, 0xffff0000, v33
v_lshlrev_b32 v60, 16, v34
Zaaa 200,144,12
v_and_b32 v61, 0xffff0000, v34
v_lshlrev_b32 v62, 16, v35
v_and_b32 v63, 0xffff0000, v35
v_lshlrev_b32 v64, 16, v36
v_and_b32 v65, 0xffff0000, v36
Maaa 200,48,8
v_lshlrev_b32 v66, 16, v37
v_and_b32 v67, 0xffff0000, v37
v_lshlrev_b32 v68, 16, v38
v_and_b32 v69, 0xffff0000, v38
v_lshlrev_b32 v70, 16, v39
Zvaa 236,44,8
D 7,40,64,1024
v_and_b32 v71, 0xffff0000, v39
v_lshlrev_b32 v72, 16, v40
v_and_b32 v73, 0xffff0000, v40
v_lshlrev_b32 v74, 16, v41
v_and_b32 v75, 0xffff0000, v41
Mvaa 236,140,12
v_lshlrev_b32 v76, 16, v42
v_and_b32 v77, 0xffff0000, v42
v_lshlrev_b32 v78, 16, v43
v_and_b32 v79, 0xffff0000, v43
A 80,56,57
Zvaa 232,136,12
A 81,58,59
A 80,80,60
A 81,81,61
A 80,80,62
A 81,81,63
Mvaa 232,40,8
A 80,80,64
A 81,81,65
A 80,80,66
A 81,81,67
A 80,80,68
Zvaa 172,36,8
D 8,40,64,2048
A 81,81,69
A 80,80,70
A 81,81,71
A 80,80,72
A 81,81,73
Mvaa 172,132,12
A 80,80,74
A 81,81,75
A 80,80,76
A 81,81,77
A 80,80,78
A 81,81,79
Zvaa 168,128,12
A 80,80,81
s_nop 1
v_add_f32_dpp v80, v80, v80 quad_perm:[1,0,3,2] row_mask:0xf bank_mask:0xf
s_nop 1
v_add_f32_dpp v80, v80, v80 quad_perm:[2,3,0,1] row_mask:0xf bank_mask:0xf
s_nop 1
v_add_f32_dpp v80, v80, v80 row_half_mirror row_mask:0xf bank_mask:0xf
s_nop 1
v_add_f32_dpp v80, v80, v80 row_mirror row_mask:0xf bank_mask:0xf
Mvaa 168,32,8
W 4
v_mov_b32 v81, v80
s_nop 1
v_permlane16_swap_b32 v80, v81
A 80,80,81
v_mul_f32 v84, 0x3acccccd, v80
s_mov_b32 s86, 0xffff
Zvaa 176,32,16
s_mov_b32 s87, 0xffff
D 9,40,64,3072
v_sub_f32 v82, v56, v84
U 80,82,82
v_sub_f32 v83, v57, v84
v_fmac_f32 v80, v83, v83
Mvaa 176,128,20
v_sub_f32 v82, v58, v84
v_fmac_f32 v80, v82, v82
v_sub_f32 v83, v59, v84
v_fmac_f32 v80, v83, v83
v_sub_f32 v82, v60, v84
Zvaa 180,132,20
Ta 0,14,16384
v_fmac_f32 v80, v82, v82
v_sub_f32 v83, v61, v84
v_fmac_f32 v80, v83, v83
v_sub_f32 v82, v62, v84
v_fmac_f32 v80, v82, v82
Mvaa 180,36,16
Ta 2,14,17408
v_sub_f32 v83, v63, v84
v_fmac_f32 v80, v83, v83
v_sub_f32 v82, v64, v84
v_fmac_f32 v80, v82, v82
v_sub_f32 v83, v65, v84
Zvaa 240,40,16
global_load_dword v142, v[140:141], off
v_lshl_add_u64 v[140:141], v[140:141], 0, s[68:69]
Ta 4,14,24576
Ta 6,14,25600
v_fmac_f32 v80, v83, v83
v_sub_f32 v82, v66, v84
v_fmac_f32 v80, v82, v82
Mvaa 240,136,20
v_sub_f32 v83, v67, v84
v_fmac_f32 v80, v83, v83
v_sub_f32 v82, v68, v84
v_fmac_f32 v80, v82, v82
v_sub_f32 v83, v69, v84
v_fmac_f32 v80, v83, v83
Zvaa 244,140,20
v_sub_f32 v82, v70, v84
v_fmac_f32 v80, v82, v82
v_sub_f32 v83, v71, v84
v_fmac_f32 v80, v83, v83
v_sub_f32 v82, v72, v84
U 81,82,82
Mvaa 244,44,16
v_sub_f32 v83, v73, v84
v_fmac_f32 v81, v83, v83
v_sub_f32 v82, v74, v84
v_fmac_f32 v81, v82, v82
v_sub_f32 v83, v75, v84
v_fmac_f32 v81, v83, v83
Zaaa 208,48,16
v_sub_f32 v82, v76, v84
v_fmac_f32 v81, v82, v82
v_sub_f32 v83, v77, v84
v_fmac_f32 v81, v83, v83
v_sub_f32 v82, v78, v84
Maaa 208,144,20
v_fmac_f32 v81, v82, v82
v_sub_f32 v83, v79, v84
v_fmac_f32 v81, v83, v83
v_cndmask_b32_e64 v81, 0, v81, s[86:87]
A 80,80,81
Zaaa 212,148,20
Maaa 212,52,16
v_add_f32_dpp v80, v80, v80 quad_perm:[1,0,3,2] row_mask:0xf bank_mask:0xf
s_nop 1
v_add_f32_dpp v80, v80, v80 quad_perm:[2,3,0,1] row_mask:0xf bank_mask:0xf
s_nop 1
v_add_f32_dpp v80, v80, v80 row_half_mirror row_mask:0xf bank_mask:0xf
s_nop 1
v_add_f32_dpp v80, v80, v80 row_mirror row_mask:0xf bank_mask:0xf
v_mov_b32 v81, v80
s_nop 1
v_permlane16_swap_b32 v80, v81
Zaaa 80,56,16
A 80,80,81
v_mul_f32 v80, 0x3acccccd, v80
K 80,80,80
v_lshlrev_b32 v80, 16, v80
v_add_f32 v80, 0x3727c5ac, v80
K 80,80,80
Maaa 80,152,20
v_lshlrev_b32 v80, 16, v80
v_sqrt_f32 v81, v80
s_nop 0
v_rcp_f32 v82, v81
v_fma_f32 v83, -v81, v81, v80
Zaaa 84,156,20
v_mul_f32 v82, 0.5, v82
v_fma_f32 v81, v83, v82, v81
K 86,81,81
v_lshlrev_b32 v86, 16, v86
v_rcp_f32 v87, v86
Maaa 84,60,16
W 4
K 85,84,84
v_lshlrev_b32 v85, 16, v85
)"
R"(s_waitcnt vmcnt(17)
v_sub_f32 v56, v56, v85
v_sub_f32 v57, v57, v85
K 80,56,57
Zaaa 92,60,24
v_lshlrev_b32 v56, 16, v80
v_and_b32 v57, 0xffff0000, v80
U 80,56,87
U 81,57,87
K 32,80,81
v_lshlrev_b32 v80, 16, v32
Maaa 92,156,28
v_and_b32 v81, 0xffff0000, v32
v_lshlrev_b32 v82, 16, v44
v_and_b32 v83, 0xffff0000, v44
U 84,80,82
v_fmac_f32 v84, v81, v83
Zaaa 88,152,28
Ta 8,15,16384
v_sub_f32 v58, v58, v85
v_sub_f32 v59, v59, v85
K 80,58,59
v_lshlrev_b32 v58, 16, v80
v_and_b32 v59, 0xffff0000, v80
Maaa 88,56,24
Ta 10,15,17408
U 80,58,87
U 81,59,87
K 33,80,81
v_lshlrev_b32 v80, 16, v33
v_and_b32 v81, 0xffff0000, v33
Zaaa 220,52,24
Ta 12,15,24576
Ta 14,15,25600
v_lshlrev_b32 v82, 16, v45
v_and_b32 v83, 0xffff0000, v45
v_fmac_f32 v84, v80, v82
v_fmac_f32 v84, v81, v83
Maaa 220,148,28
v_sub_f32 v60, v60, v85
v_sub_f32 v61, v61, v85
K 80,60,61
v_lshlrev_b32 v60, 16, v80
v_and_b32 v61, 0xffff0000, v80
U 80,60,87
Zaaa 216,144,28
U 81,61,87
K 34,80,81
v_lshlrev_b32 v80, 16, v34
v_and_b32 v81, 0xffff0000, v34
v_lshlrev_b32 v82, 16, v46
v_and_b32 v83, 0xffff0000, v46
Maaa 216,48,24
v_fmac_f32 v84, v80, v82
v_fmac_f32 v84, v81, v83
v_sub_f32 v62, v62, v85
v_sub_f32 v63, v63, v85
K 80,62,63
Zvaa 252,44,24
v_lshlrev_b32 v62, 16, v80
v_and_b32 v63, 0xffff0000, v80
U 80,62,87
U 81,63,87
K 35,80,81
Mvaa 252,140,28
v_lshlrev_b32 v80, 16, v35
v_and_b32 v81, 0xffff0000, v35
v_lshlrev_b32 v82, 16, v47
v_and_b32 v83, 0xffff0000, v47
v_fmac_f32 v84, v80, v82
Zvaa 248,136,28
v_fmac_f32 v84, v81, v83
v_sub_f32 v64, v64, v85
v_sub_f32 v65, v65, v85
K 80,64,65
v_lshlrev_b32 v64, 16, v80
Mvaa 248,40,24
v_and_b32 v65, 0xffff0000, v80
U 80,64,87
U 81,65,87
K 36,80,81
v_lshlrev_b32 v80, 16, v36
Zvaa 188,36,24
v_and_b32 v81, 0xffff0000, v36
v_lshlrev_b32 v82, 16, v48
v_and_b32 v83, 0xffff0000, v48
v_fmac_f32 v84, v80, v82
v_fmac_f32 v84, v81, v83
Mvaa 188,132,28
v_sub_f32 v66, v66, v85
v_sub_f32 v67, v67, v85
K 80,66,67
v_lshlrev_b32 v66, 16, v80
v_and_b32 v67, 0xffff0000, v80
U 80,66,87
Zvaa 184,128,28
U 81,67,87
K 37,80,81
v_lshlrev_b32 v80, 16, v37
v_and_b32 v81, 0xffff0000, v37
v_lshlrev_b32 v82, 16, v49
Mvaa 184,32,24
W 4
v_and_b32 v83, 0xffff0000, v49
v_fmac_f32 v84, v80, v82
v_fmac_f32 v84, v81, v83
v_sub_f32 v68, v68, v85
v_sub_f32 v69, v69, v85
Zvaa 192,32,0
K 80,68,69
v_lshlrev_b32 v68, 16, v80
v_and_b32 v69, 0xffff0000, v80
U 80,68,87
U 81,69,87
Mvaa 192,128,4
K 38,80,81
v_lshlrev_b32 v80, 16, v38
v_and_b32 v81, 0xffff0000, v38
v_lshlrev_b32 v82, 16, v50
v_and_b32 v83, 0xffff0000, v50
Zvaa 196,132,4
Ta 16,24,16384
v_fmac_f32 v84, v80, v82
v_fmac_f32 v84, v81, v83
v_sub_f32 v70, v70, v85
v_sub_f32 v71, v71, v85
K 80,70,71
Mvaa 196,36,0
Ta 18,24,17408
v_lshlrev_b32 v70, 16, v80
v_and_b32 v71, 0xffff0000, v80
U 80,70,87
U 81,71,87
K 39,80,81
Zaaa 160,40,0
Ta 20,24,24576
Ta 22,24,25600
v_lshlrev_b32 v80, 16, v39
v_and_b32 v81, 0xffff0000, v39
v_lshlrev_b32 v82, 16, v51
v_and_b32 v83, 0xffff0000, v51
Maaa 160,136,4
v_fmac_f32 v84, v80, v82
v_fmac_f32 v84, v81, v83
v_sub_f32 v72, v72, v85
v_sub_f32 v73, v73, v85
K 80,72,73
v_lshlrev_b32 v72, 16, v80
Zaaa 164,140,4
v_and_b32 v73, 0xffff0000, v80
U 80,72,87
U 81,73,87
K 40,80,81
v_lshlrev_b32 v80, 16, v40
v_and_b32 v81, 0xffff0000, v40
Maaa 164,44,0
v_lshlrev_b32 v82, 16, v52
v_and_b32 v83, 0xffff0000, v52
v_fmac_f32 v84, v80, v82
v_fmac_f32 v84, v81, v83
v_sub_f32 v74, v74, v85
Zaaa 224,48,0
v_sub_f32 v75, v75, v85
K 80,74,75
v_lshlrev_b32 v74, 16, v80
v_and_b32 v75, 0xffff0000, v80
U 80,74,87
Maaa 224,144,4
U 81,75,87
K 41,80,81
v_lshlrev_b32 v80, 16, v41
v_and_b32 v81, 0xffff0000, v41
v_lshlrev_b32 v82, 16, v53
Zaaa 228,148,4
v_and_b32 v83, 0xffff0000, v53
v_fmac_f32 v84, v80, v82
v_fmac_f32 v84, v81, v83
v_sub_f32 v76, v76, v85
v_sub_f32 v77, v77, v85
Maaa 228,52,0
K 80,76,77
v_lshlrev_b32 v76, 16, v80
v_and_b32 v77, 0xffff0000, v80
U 80,76,87
U 81,77,87
Zaaa 96,56,0
K 42,80,81
v_lshlrev_b32 v80, 16, v42
v_and_b32 v81, 0xffff0000, v42
v_lshlrev_b32 v82, 16, v54
v_and_b32 v83, 0xffff0000, v54
Maaa 96,152,4
v_fmac_f32 v84, v80, v82
v_fmac_f32 v84, v81, v83
v_sub_f32 v78, v78, v85
v_sub_f32 v79, v79, v85
K 80,78,79
v_lshlrev_b32 v78, 16, v80
Zaaa 100,156,4
v_and_b32 v79, 0xffff0000, v80
U 80,78,87
U 81,79,87
K 43,80,81
v_lshlrev_b32 v80, 16, v43
Maaa 100,60,0
W 4
v_and_b32 v81, 0xffff0000, v43
v_lshlrev_b32 v82, 16, v55
v_and_b32 v83, 0xffff0000, v55
v_fmac_f32 v84, v80, v82
v_fmac_f32 v84, v81, v83
Zaaa 108,60,8
Ta 24,25,16384
Ta 26,25,17408
v_lshrrev_b32 v82, 9, v1
s_lshl_b32 s80, s14, 1
v_add_u32 v82, s80, v82
v_mul_u32_u24 v83, 1552, v82
v_add_u32 v83, v83, v88
Maaa 108,156,12
v_add_u32 v83, 131072, v83
ds_write_b128 v83, v[32:35] offset:0
ds_write_b128 v83, v[36:39] offset:512
ds_write_b128 v83, v[40:43] offset:1024
v_add_f32_dpp v84, v84, v84 quad_perm:[1,0,3,2] row_mask:0xf bank_mask:0xf
Zaaa 104,152,12
Ta 28,25,24576
Ta 30,25,25600
Maaa 104,56,8
v_add_f32_dpp v84, v84, v84 quad_perm:[2,3,0,1] row_mask:0xf bank_mask:0xf
s_nop 1
v_add_f32_dpp v84, v84, v84 row_half_mirror row_mask:0xf bank_mask:0xf
s_nop 1
v_add_f32_dpp v84, v84, v84 row_mirror row_mask:0xf bank_mask:0xf
v_mov_b32 v81, v84
s_nop 1
v_permlane16_swap_b32 v84, v81
A 84,84,81
Zaaa 236,52,8
K 80,84,84
v_lshlrev_b32 v80, 16, v80
s_lshl_b32 s80, s15, 3
v_add_u32 v82, s80, v82
v_sub_u32 v83, 2047, v82
v_ashrrev_i32 v81, 31, v80
v_or_b32 v81, 0x80000000, v81
Maaa 236,148,12
v_xor_b32 v80, v80, v81
v_and_b32 v80, 0xffff0000, v80
v_or_b32 v80, v80, v83
v_and_b32 v82, 7, v82
v_lshlrev_b32 v82, 2, v82
v_add_u32 v82, 143488, v82
Zaaa 232,144,12
ds_write_b32 v82, v80
Maaa 232,48,8
Zaaa 172,44,8
Maaa 172,140,12
Zaaa 168,136,12
s_waitcnt vmcnt(1) lgkmcnt(0)
s_barrier
s_add_u32 s64, s64, 128
Maaa 168,40,8
v_bfe_u32 v80, v1, 4, 3
Zvaa 204,36,8
v_mul_u32_u24 v81, 1552, v80
Mvaa 204,132,12
s_and_b32 s64, s64, 1023
v_lshrrev_b32 v82, 7, v1
Zvaa 200,128,12
v_lshl_add_u32 v81, v82, 4, v81
Mvaa 200,32,8
s_lshl_b32 s80, s14, 7
Zvaa 208,32,16
s_lshl_b32 s65, s64, 12
s_add_u32 s80, s80, 131072
Mvaa 208,128,20
v_add_u32 v81, s80, v81
Zvaa 212,132,20
s_add_u32 m0, s62, 0
ds_read_b128 v[32:35], v81
Ta 0,10,49152
Mvaa 212,36,16
Ta 2,10,50176
Rv 36,81,512
Zaaa 176,40,16
s_and_b32 s80, s14, 2
Ta 4,10,57344
Ta 6,10,58368
Maaa 176,136,20
s_lshl_b32 s80, s80, 7
D 16,44,65,0
Zaaa 180,140,20
v_subrev_u32 v81, s80, v81
Maaa 180,44,16
Rv 40,81,1024
Zaaa 240,48,16
v_lshlrev_b32 v80, 2, v80
Maaa 240,144,20
D 17,44,65,1024
v_add_u32 v80, 143488, v80
Zaaa 244,148,20
ds_read_b32 v84, v80
Maaa 244,52,16
v_mov_b32 v85, s13
Zaaa 112,56,16
v_lshrrev_b32 v83, 1, v1
Maaa 112,152,20
s_mul_i32 s82, s15, 10240
D 18,44,65,2048
Zaaa 116,156,20
s_add_u32 s82, s82, 131072
Maaa 116,60,16
s_lshl_b32 s80, s14, 10
Zaaa 124,60,24
s_add_u32 s83, s82, s80
Ra 60,26,47104
Maaa 124,156,28
D 19,44,65,3072
Ra 156,27,47104
Zaaa 120,152,28
s_add_u32 m0, s62, 4096
Ra 152,27,45056
Ta 8,11,49152
Maaa 120,56,24
Ta 10,11,50176
Ra 56,26,45056
Zaaa 252,52,24
Ta 12,11,57344
Ta 14,11,58368
Maaa 252,148,28
W 8
buffer_store_dwordx4 v[32:35], v1, s[8:11], s83 offen sc1
s_add_u32 s83, s83, 4096
Zaaa 248,144,28
Ra 52,26,43008
Ra 148,27,43008
Maaa 248,48,24
Ra 144,27,40960
Ra 48,26,40960
Zaaa 188,44,24
Ra 44,26,38912
Maaa 188,140,28
D 20,44,65,0
Ra 140,27,38912
Zaaa 184,136,28
Ra 136,27,36864
Maaa 184,40,24
Ra 40,26,36864
Zvaa 220,36,24
Ra 36,26,34816
Mvaa 220,132,28
buffer_store_dwordx4 v[36:39], v1, s[8:11], s83 offen sc1
Ra 132,27,34816
Zvaa 216,128,28
s_and_b32 s80, s80, 1024
s_add_u32 s83, s82, s80
s_add_u32 s83, s83, 8192
Mvaa 216,32,24
W 12
Ra 128,27,32768
Ra 32,26,32768
Maaa 68,60,0
Maaa 68,156,4
D 21,44,65,1024
Maaa 64,152,4
Ta 16,12,49152
Ta 18,12,50176
Maaa 64,56,0
W 10
Ta 20,12,57344
Ta 22,12,58368
Maaa 196,52,0
Maaa 196,148,4
D 22,44,65,2048
Maaa 192,144,4
Maaa 192,48,0
W 8
Mvaa 228,44,0
Mvaa 228,140,4
buffer_store_dwordx4 v[40:43], v1, s[8:11], s83 offen sc1
Mvaa 224,136,4
Mvaa 224,40,0
W 4
Mvaa 164,36,0
Mvaa 164,132,4
D 23,44,65,3072
Mvaa 160,128,4
s_add_u32 m0, s61, 0
Mvaa 160,32,0
Mvaa 168,32,8
Mvaa 168,128,12
D 2,40,64,0
Mvaa 172,132,12
Ta 24,13,49152
Ta 26,13,50176
Mvaa 172,36,8
Ta 28,13,57344
Ta 30,13,58368
Mvaa 232,40,8
)"
R"(.Lh_0_1_1%=:
s_cbranch_execz .Lhr_9%=
Mvaa 232,136,12
D 3,40,64,1024
Mvaa 236,140,12
Mvaa 236,44,8
Maaa 200,48,8
Maaa 200,144,12
D 4,40,64,2048
Maaa 204,148,12
Maaa 204,52,8
Maaa 72,56,8
Maaa 72,152,12
D 5,40,64,3072
Maaa 76,156,12
s_add_u32 m0, s61, 4096
Maaa 76,60,8
W 4
Maaa 84,60,16
Maaa 84,156,20
D 6,40,64,0
Maaa 80,152,20
Ta 0,14,49152
Ta 2,14,50176
Maaa 80,56,16
Ta 4,14,57344
Ta 6,14,58368
Maaa 212,52,16
Maaa 212,148,20
D 7,40,64,1024
Maaa 208,144,20
Maaa 208,48,16
Mvaa 244,44,16
Mvaa 244,140,20
D 8,40,64,2048
Mvaa 240,136,20
Mvaa 240,40,16
Mvaa 180,36,16
Mvaa 180,132,20
D 9,40,64,3072
Mvaa 176,128,20
Mvaa 176,32,16
W 4
Mvaa 184,32,24
Mvaa 184,128,28
global_load_dword v142, v[140:141], off
v_lshl_add_u64 v[140:141], v[140:141], 0, s[68:69]
v_bfe_u32 v147, v1, 5, 3
Mvaa 188,132,28
v_lshlrev_b32 v147, 13, v147
v_and_b32 v148, 16, v1
Mvaa 188,36,24
Ta 8,15,49152
v_lshl_add_u32 v147, v148, 3, v147
Mvaa 248,40,24
s_lshl_b32 s92, s57, 2
s_add_u32 s92, s92, s14
s_lshl_b32 s92, s92, 16
Mvaa 248,136,28
buffer_load_dword v142, v147, s[48:51], s92 offen
Ta 10,15,50176
Mvaa 252,140,28
Ta 12,15,57344
Ta 14,15,58368
Mvaa 252,44,24
Maaa 216,48,24
Maaa 216,144,28
Maaa 220,148,28
Maaa 220,52,24
Maaa 88,56,24
Maaa 88,152,28
Maaa 92,156,28
Maaa 92,60,24
W 4
Maaa 100,60,0
Maaa 100,156,4
Maaa 96,152,4
Ta 16,24,49152
Ta 18,24,50176
Maaa 96,56,0
Ta 20,24,57344
Ta 22,24,58368
Maaa 228,52,0
Maaa 228,148,4
Maaa 224,144,4
Maaa 224,48,0
Maaa 164,44,0
Maaa 164,140,4
Maaa 160,136,4
Maaa 160,40,0
Mvaa 196,36,0
Mvaa 196,132,4
Mvaa 192,128,4
Mvaa 192,32,0
W 4
Mvaa 200,32,8
Ta 24,25,49152
Ta 26,25,50176
Mvaa 200,128,12
Mvaa 204,132,12
Ta 28,25,57344
Ta 30,25,58368
Mvaa 204,36,8
Maaa 168,40,8
Maaa 168,136,12
Maaa 172,140,12
Maaa 172,44,8
Maaa 232,48,8
Maaa 232,144,12
Maaa 236,148,12
s_waitcnt vmcnt(2) lgkmcnt(0)
s_barrier
s_cmp_eq_u32 s14, 0
s_cbranch_scc0 .Lpubr_19%=
s_lshl_b32 s80, s15, 6
s_add_u32 s80, s80, 40960
s_mov_b64 exec, 0xff
buffer_store_dwordx2 v[84:85], v83, s[8:11], s80 offen sc1
s_mov_b64 exec, -1
.Lpubr_19%=:
Maaa 236,52,8
s_add_u32 s64, s64, 128
Maaa 104,56,8
Maaa 104,152,12
s_and_b32 s64, s64, 1023
Maaa 108,156,12
Maaa 108,60,8
Maaa 116,60,16
s_lshl_b32 s65, s64, 12
Maaa 116,156,20
Maaa 112,152,20
Ta 0,10,16384
Ta 2,10,17408
Maaa 112,56,16
s_add_u32 m0, s62, 32768
Ta 4,10,24576
Ta 6,10,25600
Maaa 244,52,16
Maaa 244,148,20
Maaa 240,144,20
D 16,44,65,0
Maaa 240,48,16
Maaa 180,44,16
Maaa 180,140,20
Maaa 176,136,20
D 17,44,65,1024
Maaa 176,40,16
Mvaa 212,36,16
Mvaa 212,132,20
Mvaa 208,128,20
D 18,44,65,2048
Mvaa 208,32,16
Mvaa 216,32,24
Ra 32,26,0
Mvaa 216,128,28
Ra 128,27,0
Mvaa 220,132,28
D 19,44,65,3072
Ra 132,27,2048
Mvaa 220,36,24
s_add_u32 m0, s62, 36864
Ta 8,11,16384
Ta 10,11,17408
Maaa 184,40,24
Ra 36,26,2048
Ta 12,11,24576
Maaa 184,136,28
Ta 14,11,25600
Ra 40,26,4096
Maaa 188,140,28
D 20,44,65,0
Ra 136,27,4096
Maaa 188,44,24
Ra 140,27,6144
Ra 44,26,6144
Maaa 248,48,24
Ra 48,26,8192
Maaa 248,144,28
Ra 144,27,8192
Maaa 252,148,28
D 21,44,65,1024
Ra 148,27,10240
Maaa 252,52,24
Ra 52,26,10240
Maaa 120,56,24
Ra 56,26,12288
Maaa 120,152,28
Ra 152,27,12288
Maaa 124,156,28
D 22,44,65,2048
Ra 156,27,14336
Maaa 124,60,24
W 13
Ra 60,26,14336
Mvaa 160,32,0
Mvaa 160,128,4
Mvaa 164,132,4
D 23,44,65,3072
Ta 16,12,16384
Mvaa 164,36,0
W 9
s_add_u32 m0, s61, 32768
Ta 18,12,17408
Mvaa 224,40,0
Ta 20,12,24576
Ta 22,12,25600
Mvaa 224,136,4
Mvaa 228,140,4
D 2,40,64,0
Mvaa 228,44,0
W 8
Maaa 192,48,0
Maaa 192,144,4
Maaa 196,148,4
D 3,40,64,1024
Maaa 196,52,0
W 4
Maaa 64,56,0
Maaa 64,152,4
Maaa 68,156,4
D 4,40,64,2048
Maaa 68,60,0
Maaa 76,60,8
Maaa 76,156,12
Maaa 72,152,12
D 5,40,64,3072
Ta 24,13,16384
Maaa 72,56,8
s_add_u32 m0, s61, 36864
Ta 26,13,17408
Ta 28,13,24576
Maaa 204,52,8
Ta 30,13,25600
Maaa 204,148,12
Maaa 200,144,12
D 6,40,64,0
Maaa 200,48,8
Mvaa 236,44,8
Mvaa 236,140,12
Mvaa 232,136,12
D 7,40,64,1024
Mvaa 232,40,8
Mvaa 172,36,8
Mvaa 172,132,12
Mvaa 168,128,12
D 8,40,64,2048
Mvaa 168,32,8
W 4
Mvaa 176,32,16
Mvaa 176,128,20
Mvaa 180,132,20
D 9,40,64,3072
Ta 0,14,16384
Mvaa 180,36,16
Ta 2,14,17408
Ta 4,14,24576
Mvaa 240,40,16
Ta 6,14,25600
Mvaa 240,136,20
Mvaa 244,140,20
Mvaa 244,44,16
Maaa 208,48,16
Maaa 208,144,20
Maaa 212,148,20
Maaa 212,52,16
Maaa 80,56,16
Maaa 80,152,20
Maaa 84,156,20
Maaa 84,60,16
W 4
Maaa 92,60,24
Maaa 92,156,28
Maaa 88,152,28
Ta 8,15,16384
Ta 10,15,17408
Maaa 88,56,24
Ta 12,15,24576
Ta 14,15,25600
Maaa 220,52,24
Maaa 220,148,28
Maaa 216,144,28
Maaa 216,48,24
Mvaa 252,44,24
Mvaa 252,140,28
Mvaa 248,136,28
Mvaa 248,40,24
Mvaa 188,36,24
Mvaa 188,132,28
Mvaa 184,128,28
Mvaa 184,32,24
W 4
Mvaa 192,32,0
Mvaa 192,128,4
Mvaa 196,132,4
Ta 16,24,16384
Ta 18,24,17408
Mvaa 196,36,0
Ta 20,24,24576
Ta 22,24,25600
Maaa 160,40,0
Maaa 160,136,4
Maaa 164,140,4
Maaa 164,44,0
Maaa 224,48,0
Maaa 224,144,4
Maaa 228,148,4
Maaa 228,52,0
Maaa 96,56,0
Maaa 96,152,4
Maaa 100,156,4
Maaa 100,60,0
W 4
Maaa 108,60,8
Ta 24,25,16384
Ta 26,25,17408
Maaa 108,156,12
Maaa 104,152,12
Ta 28,25,24576
Ta 30,25,25600
Maaa 104,56,8
Maaa 236,52,8
Maaa 236,148,12
Maaa 232,144,12
Maaa 232,48,8
Maaa 172,44,8
Maaa 172,140,12
Maaa 168,136,12
s_waitcnt vmcnt(0) lgkmcnt(0)
s_barrier
s_add_u32 s64, s64, 128
Maaa 168,40,8
Mvaa 204,36,8
Mvaa 204,132,12
s_and_b32 s64, s64, 1023
Mvaa 200,128,12
Mvaa 200,32,8
Mvaa 208,32,16
s_lshl_b32 s65, s64, 12
Mvaa 208,128,20
Mvaa 212,132,20
s_add_u32 m0, s62, 0
Ta 0,10,49152
Ta 2,10,50176
Mvaa 212,36,16
Ta 4,10,57344
Ta 6,10,58368
Maaa 176,40,16
Maaa 176,136,20
D 16,44,65,0
Maaa 180,140,20
Maaa 180,44,16
Maaa 240,48,16
Maaa 240,144,20
D 17,44,65,1024
Maaa 244,148,20
Maaa 244,52,16
Maaa 112,56,16
Maaa 112,152,20
D 18,44,65,2048
Maaa 116,156,20
Maaa 116,60,16
Maaa 124,60,24
Ra 60,26,47104
Maaa 124,156,28
D 19,44,65,3072
Ra 156,27,47104
Maaa 120,152,28
s_add_u32 m0, s62, 4096
Ra 152,27,45056
Ta 8,11,49152
Maaa 120,56,24
Ta 10,11,50176
Ra 56,26,45056
Maaa 252,52,24
Ta 12,11,57344
Ta 14,11,58368
Maaa 252,148,28
D 20,44,65,0
Ra 52,26,43008
Maaa 248,144,28
Ra 148,27,43008
Ra 144,27,40960
Maaa 248,48,24
Ra 48,26,40960
Maaa 188,44,24
Ra 44,26,38912
Maaa 188,140,28
D 21,44,65,1024
Ra 140,27,38912
Maaa 184,136,28
Ra 136,27,36864
Maaa 184,40,24
Ra 40,26,36864
Mvaa 220,36,24
Ra 36,26,34816
Mvaa 220,132,28
D 22,44,65,2048
Ra 132,27,34816
Mvaa 216,128,28
Ra 128,27,32768
)"
R"(Mvaa 216,32,24
W 13
Ra 32,26,32768
Maaa 68,60,0
Maaa 68,156,4
D 23,44,65,3072
Maaa 64,152,4
s_add_u32 m0, s61, 0
Ta 16,12,49152
Ta 18,12,50176
Maaa 64,56,0
W 10
Ta 20,12,57344
Ta 22,12,58368
Maaa 196,52,0
Maaa 196,148,4
D 2,40,64,0
Maaa 192,144,4
Maaa 192,48,0
W 8
Mvaa 228,44,0
Mvaa 228,140,4
D 3,40,64,1024
Mvaa 224,136,4
Mvaa 224,40,0
W 4
Mvaa 164,36,0
Mvaa 164,132,4
D 4,40,64,2048
Mvaa 160,128,4
Mvaa 160,32,0
Mvaa 168,32,8
Mvaa 168,128,12
D 5,40,64,3072
Mvaa 172,132,12
s_add_u32 m0, s61, 4096
Ta 24,13,49152
Ta 26,13,50176
Mvaa 172,36,8
Ta 28,13,57344
Ta 30,13,58368
Mvaa 232,40,8
Mvaa 232,136,12
D 6,40,64,0
Mvaa 236,140,12
Mvaa 236,44,8
Maaa 200,48,8
Maaa 200,144,12
D 7,40,64,1024
Maaa 204,148,12
Maaa 204,52,8
Maaa 72,56,8
Maaa 72,152,12
D 8,40,64,2048
Maaa 76,156,12
Maaa 76,60,8
W 4
Maaa 84,60,16
Maaa 84,156,20
D 9,40,64,3072
Maaa 80,152,20
Ta 0,14,49152
Ta 2,14,50176
Maaa 80,56,16
Ta 4,14,57344
Ta 6,14,58368
Maaa 212,52,16
Maaa 212,148,20
global_load_dword v142, v[140:141], off
v_lshl_add_u64 v[140:141], v[140:141], 0, s[68:69]
Maaa 208,144,20
Maaa 208,48,16
Mvaa 244,44,16
Mvaa 244,140,20
Mvaa 240,136,20
Mvaa 240,40,16
Mvaa 180,36,16
Mvaa 180,132,20
Mvaa 176,128,20
Mvaa 176,32,16
W 4
Mvaa 184,32,24
Mvaa 184,128,28
Mvaa 188,132,28
Ta 8,15,49152
Ta 10,15,50176
Mvaa 188,36,24
Ta 12,15,57344
Ta 14,15,58368
Mvaa 248,40,24
Mvaa 248,136,28
Mvaa 252,140,28
Mvaa 252,44,24
Maaa 216,48,24
Maaa 216,144,28
Maaa 220,148,28
Maaa 220,52,24
Maaa 88,56,24
Maaa 88,152,28
Maaa 92,156,28
Maaa 92,60,24
W 4
Maaa 100,60,0
Maaa 100,156,4
Maaa 96,152,4
Ta 16,24,49152
Ta 18,24,50176
Maaa 96,56,0
Ta 20,24,57344
Ta 22,24,58368
Maaa 228,52,0
Maaa 228,148,4
Maaa 224,144,4
Maaa 224,48,0
Maaa 164,44,0
Maaa 164,140,4
Maaa 160,136,4
Maaa 160,40,0
Mvaa 196,36,0
Mvaa 196,132,4
Mvaa 192,128,4
Mvaa 192,32,0
W 4
Mvaa 200,32,8
Ta 24,25,49152
Ta 26,25,50176
Mvaa 200,128,12
Mvaa 204,132,12
Ta 28,25,57344
Ta 30,25,58368
Mvaa 204,36,8
Maaa 168,40,8
Maaa 168,136,12
.Lh_0_2_1%=:
s_cbranch_execz .Lhr_9%=
Maaa 172,140,12
Maaa 172,44,8
Maaa 232,48,8
Maaa 232,144,12
Maaa 236,148,12
s_waitcnt vmcnt(1) lgkmcnt(0)
s_barrier
s_add_u32 s64, s64, 128
Maaa 236,52,8
Maaa 104,56,8
Maaa 104,152,12
s_and_b32 s64, s64, 1023
Maaa 108,156,12
Maaa 108,60,8
Maaa 116,60,16
s_lshl_b32 s65, s64, 12
Maaa 116,156,20
Maaa 112,152,20
Ta 0,10,16384
Ta 2,10,17408
Maaa 112,56,16
s_add_u32 m0, s62, 32768
Ta 4,10,24576
Ta 6,10,25600
Maaa 244,52,16
Maaa 244,148,20
Maaa 240,144,20
D 16,44,65,0
Maaa 240,48,16
Maaa 180,44,16
Maaa 180,140,20
Maaa 176,136,20
D 17,44,65,1024
Maaa 176,40,16
Mvaa 212,36,16
Mvaa 212,132,20
Mvaa 208,128,20
D 18,44,65,2048
Mvaa 208,32,16
Mvaa 216,32,24
Ra 32,26,0
Mvaa 216,128,28
Ra 128,27,0
Mvaa 220,132,28
D 19,44,65,3072
Ra 132,27,2048
Mvaa 220,36,24
s_add_u32 m0, s62, 36864
Ta 8,11,16384
Ta 10,11,17408
Maaa 184,40,24
Ra 36,26,2048
Ta 12,11,24576
Maaa 184,136,28
Ta 14,11,25600
Ra 40,26,4096
Maaa 188,140,28
D 20,44,65,0
Ra 136,27,4096
Maaa 188,44,24
Ra 140,27,6144
Ra 44,26,6144
Maaa 248,48,24
Ra 48,26,8192
Maaa 248,144,28
Ra 144,27,8192
Maaa 252,148,28
D 21,44,65,1024
Ra 148,27,10240
Maaa 252,52,24
Ra 52,26,10240
Maaa 120,56,24
Ra 56,26,12288
Maaa 120,152,28
Ra 152,27,12288
Maaa 124,156,28
D 22,44,65,2048
Ra 156,27,14336
Maaa 124,60,24
W 13
Ra 60,26,14336
Mvaa 160,32,0
Mvaa 160,128,4
Mvaa 164,132,4
D 23,44,65,3072
Ta 16,12,16384
Mvaa 164,36,0
W 9
s_add_u32 m0, s61, 32768
Ta 18,12,17408
Mvaa 224,40,0
Ta 20,12,24576
Ta 22,12,25600
Mvaa 224,136,4
Mvaa 228,140,4
D 2,40,64,0
Mvaa 228,44,0
W 8
Maaa 192,48,0
Maaa 192,144,4
Maaa 196,148,4
D 3,40,64,1024
Maaa 196,52,0
W 4
Maaa 64,56,0
Maaa 64,152,4
Maaa 68,156,4
D 4,40,64,2048
Maaa 68,60,0
Maaa 76,60,8
Maaa 76,156,12
Maaa 72,152,12
D 5,40,64,3072
Ta 24,13,16384
Maaa 72,56,8
s_add_u32 m0, s61, 36864
Ta 26,13,17408
Ta 28,13,24576
Maaa 204,52,8
Ta 30,13,25600
Maaa 204,148,12
Maaa 200,144,12
D 6,40,64,0
Maaa 200,48,8
Mvaa 236,44,8
Mvaa 236,140,12
Mvaa 232,136,12
D 7,40,64,1024
Mvaa 232,40,8
Mvaa 172,36,8
Mvaa 172,132,12
Mvaa 168,128,12
D 8,40,64,2048
Mvaa 168,32,8
W 4
Mvaa 176,32,16
Mvaa 176,128,20
Mvaa 180,132,20
D 9,40,64,3072
Ta 0,14,16384
Mvaa 180,36,16
s_add_u32 s92, s92, 2097152
Ta 2,14,17408
Ta 4,14,24576
Mvaa 240,40,16
Ta 6,14,25600
Mvaa 240,136,20
Mvaa 244,140,20
buffer_load_dword v142, v147, s[48:51], s92 offen
Mvaa 244,44,16
s_add_u32 s92, s92, 2097152
Maaa 208,48,16
Maaa 208,144,20
Maaa 212,148,20
buffer_load_dword v142, v147, s[48:51], s92 offen
Maaa 212,52,16
Maaa 80,56,16
Maaa 80,152,20
Maaa 84,156,20
Maaa 84,60,16
W 4
Maaa 92,60,24
Maaa 92,156,28
Maaa 88,152,28
Ta 8,15,16384
Ta 10,15,17408
Maaa 88,56,24
Ta 12,15,24576
Ta 14,15,25600
Maaa 220,52,24
Maaa 220,148,28
Maaa 216,144,28
Maaa 216,48,24
Mvaa 252,44,24
Mvaa 252,140,28
Mvaa 248,136,28
Mvaa 248,40,24
Mvaa 188,36,24
Mvaa 188,132,28
Mvaa 184,128,28
Mvaa 184,32,24
W 4
Mvaa 192,32,0
Mvaa 192,128,4
Mvaa 196,132,4
Ta 16,24,16384
Ta 18,24,17408
Mvaa 196,36,0
Ta 20,24,24576
Ta 22,24,25600
Maaa 160,40,0
Maaa 160,136,4
Maaa 164,140,4
Maaa 164,44,0
Maaa 224,48,0
Maaa 224,144,4
Maaa 228,148,4
Maaa 228,52,0
Maaa 96,56,0
Maaa 96,152,4
Maaa 100,156,4
Maaa 100,60,0
W 4
Maaa 108,60,8
Ta 24,25,16384
Ta 26,25,17408
Maaa 108,156,12
Maaa 104,152,12
Ta 28,25,24576
Ta 30,25,25600
Maaa 104,56,8
Maaa 236,52,8
Maaa 236,148,12
Maaa 232,144,12
Maaa 232,48,8
Maaa 172,44,8
Maaa 172,140,12
Maaa 168,136,12
s_waitcnt vmcnt(2) lgkmcnt(0)
s_barrier
s_add_u32 s64, s64, 128
Maaa 168,40,8
Mvaa 204,36,8
Mvaa 204,132,12
s_and_b32 s64, s64, 1023
Mvaa 200,128,12
Mvaa 200,32,8
Mvaa 208,32,16
s_lshl_b32 s65, s64, 12
Mvaa 208,128,20
Mvaa 212,132,20
Ta 0,10,49152
Ta 2,10,50176
Mvaa 212,36,16
s_add_u32 m0, s62, 0
Ta 4,10,57344
Ta 6,10,58368
Maaa 176,40,16
Maaa 176,136,20
Maaa 180,140,20
D 16,44,65,0
Maaa 180,44,16
Maaa 240,48,16
Maaa 240,144,20
Maaa 244,148,20
D 17,44,65,1024
Maaa 244,52,16
Maaa 112,56,16
Maaa 112,152,20
Maaa 116,156,20
D 18,44,65,2048
Maaa 116,60,16
Maaa 124,60,24
Ra 60,26,47104
Maaa 124,156,28
Ra 156,27,47104
Maaa 120,152,28
D 19,44,65,3072
Ra 152,27,45056
Maaa 120,56,24
s_add_u32 m0, s62, 4096
Ta 8,11,49152
Ta 10,11,50176
Maaa 252,52,24
Ra 56,26,45056
)"
R"(Ta 12,11,57344
Maaa 252,148,28
Ta 14,11,58368
Ra 52,26,43008
Maaa 248,144,28
D 20,44,65,0
v_lshrrev_b32 v28, 1, v1
Maaa 248,48,24
s_lshl_b32 s80, s60, 9
Ra 148,27,43008
Ra 144,27,40960
Maaa 188,44,24
s_add_u32 s80, s80, 40960
Ra 48,26,40960
Ra 44,26,38912
Maaa 188,140,28
Ra 140,27,38912
Maaa 184,136,28
D 21,44,65,1024
Ra 136,27,36864
Maaa 184,40,24
Ra 40,26,36864
Mvaa 220,36,24
Ra 36,26,34816
Mvaa 220,132,28
Ra 132,27,34816
Mvaa 216,128,28
buffer_load_dwordx2 v[28:29], v28, s[8:11], s80 offen sc1
Ra 128,27,32768
Mvaa 216,32,24
W 13
Ra 32,26,32768
Maaa 68,60,0
Maaa 68,156,4
Maaa 64,152,4
D 22,44,65,2048
Ta 16,12,49152
Maaa 64,56,0
W 9
Ta 18,12,50176
Ta 20,12,57344
Maaa 196,52,0
Ta 22,12,58368
Maaa 196,148,4
Maaa 192,144,4
D 23,44,65,3072
Maaa 192,48,0
W 8
s_add_u32 m0, s61, 0
Mvaa 228,44,0
Mvaa 228,140,4
Mvaa 224,136,4
D 2,40,64,0
Mvaa 224,40,0
W 4
Mvaa 164,36,0
Mvaa 164,132,4
Mvaa 160,128,4
D 3,40,64,1024
Mvaa 160,32,0
Mvaa 168,32,8
Mvaa 168,128,12
Mvaa 172,132,12
D 4,40,64,2048
Ta 24,13,49152
Mvaa 172,36,8
Ta 26,13,50176
Ta 28,13,57344
Mvaa 232,40,8
Ta 30,13,58368
Mvaa 232,136,12
Mvaa 236,140,12
D 5,40,64,3072
Mvaa 236,44,8
s_add_u32 m0, s61, 4096
Maaa 200,48,8
Maaa 200,144,12
Maaa 204,148,12
D 6,40,64,0
Maaa 204,52,8
Maaa 72,56,8
Maaa 72,152,12
Maaa 76,156,12
D 7,40,64,1024
Maaa 76,60,8
W 4
Maaa 84,60,16
Maaa 84,156,20
Maaa 80,152,20
D 8,40,64,2048
Ta 0,14,49152
Maaa 80,56,16
Ta 2,14,50176
Ta 4,14,57344
Maaa 212,52,16
Ta 6,14,58368
Maaa 212,148,20
Maaa 208,144,20
D 9,40,64,3072
Maaa 208,48,16
Mvaa 244,44,16
Mvaa 244,140,20
Mvaa 240,136,20
s_waitcnt vmcnt(10)
v_cmp_eq_u32 vcc, s13, v29
s_cmp_eq_u64 vcc, -1
s_cbranch_scc0 .Lspin_20%=
.Lok_21%=:
global_load_dword v142, v146, s[6:7]
Mvaa 240,40,16
Mvaa 180,36,16
v_bfe_u32 v144, v1, 8, 1
Mvaa 180,132,20
v_mul_u32_u24 v144, 10240, v144
Mvaa 176,128,20
v_and_b32 v145, 0xf0, v1
Mvaa 176,32,16
W 4
v_lshl_add_u32 v144, v145, 3, v144
Mvaa 184,32,24
s_mul_i32 s91, s58, 20480
Mvaa 184,128,28
s_add_u32 s91, s91, s28
Mvaa 188,132,28
buffer_load_dword v142, v144, s[8:11], s91 offen offset:0 sc1
Ta 8,15,49152
Mvaa 188,36,24
Ta 10,15,50176
Ta 12,15,57344
Mvaa 248,40,24
Ta 14,15,58368
Mvaa 248,136,28
Mvaa 252,140,28
Mvaa 252,44,24
Maaa 216,48,24
Maaa 216,144,28
Maaa 220,148,28
Maaa 220,52,24
Maaa 88,56,24
Maaa 88,152,28
Maaa 92,156,28
Maaa 92,60,24
W 4
Maaa 100,60,0
Maaa 100,156,4
Maaa 96,152,4
Ta 16,24,49152
Ta 18,24,50176
Maaa 96,56,0
Ta 20,24,57344
Ta 22,24,58368
Maaa 228,52,0
Maaa 228,148,4
Maaa 224,144,4
Maaa 224,48,0
Maaa 164,44,0
Maaa 164,140,4
Maaa 160,136,4
Maaa 160,40,0
Mvaa 196,36,0
Mvaa 196,132,4
Mvaa 192,128,4
Mvaa 192,32,0
W 4
Mvaa 200,32,8
Ta 24,25,49152
Ta 26,25,50176
Mvaa 200,128,12
Mvaa 204,132,12
Ta 28,25,57344
Ta 30,25,58368
Mvaa 204,36,8
Maaa 168,40,8
Maaa 168,136,12
Maaa 172,140,12
Maaa 172,44,8
Maaa 232,48,8
Maaa 232,144,12
Maaa 236,148,12
s_waitcnt vmcnt(2) lgkmcnt(0)
s_barrier
s_cmp_eq_u32 s85, 107
s_cbranch_scc1 .Lrt_23%=
.Lrtr_24%=:
Maaa 236,52,8
s_add_u32 s64, s64, 128
Maaa 104,56,8
Maaa 104,152,12
s_and_b32 s64, s64, 1023
Maaa 108,156,12
Maaa 108,60,8
Maaa 116,60,16
s_lshl_b32 s65, s64, 12
Maaa 116,156,20
Maaa 112,152,20
Ta 0,10,16384
Ta 2,10,17408
Maaa 112,56,16
s_add_u32 m0, s62, 32768
Ta 4,10,24576
Ta 6,10,25600
Maaa 244,52,16
Maaa 244,148,20
Maaa 240,144,20
D 16,44,65,0
Maaa 240,48,16
Maaa 180,44,16
Maaa 180,140,20
Maaa 176,136,20
D 17,44,65,1024
Maaa 176,40,16
Mvaa 212,36,16
Mvaa 212,132,20
Mvaa 208,128,20
D 18,44,65,2048
Mvaa 208,32,16
Mvaa 216,32,24
Ra 32,26,0
Mvaa 216,128,28
Ra 128,27,0
Mvaa 220,132,28
D 19,44,65,3072
Ra 132,27,2048
Mvaa 220,36,24
s_add_u32 m0, s62, 36864
Ta 8,11,16384
Ta 10,11,17408
Maaa 184,40,24
Ra 36,26,2048
Ta 12,11,24576
Maaa 184,136,28
Ta 14,11,25600
Ra 40,26,4096
Maaa 188,140,28
D 20,44,65,0
Ra 136,27,4096
Maaa 188,44,24
Ra 140,27,6144
Ra 44,26,6144
Maaa 248,48,24
Ra 48,26,8192
Maaa 248,144,28
Ra 144,27,8192
Maaa 252,148,28
D 21,44,65,1024
Ra 148,27,10240
Maaa 252,52,24
Ra 52,26,10240
Maaa 120,56,24
Ra 56,26,12288
Maaa 120,152,28
Ra 152,27,12288
Maaa 124,156,28
D 22,44,65,2048
Ra 156,27,14336
Maaa 124,60,24
W 13
Ra 60,26,14336
Mvaa 160,32,0
Mvaa 160,128,4
Mvaa 164,132,4
D 23,44,65,3072
Ta 16,12,16384
Mvaa 164,36,0
W 9
s_add_u32 m0, s61, 32768
Ta 18,12,17408
Mvaa 224,40,0
.Lh_0_3_1%=:
s_cbranch_execz .Lhr_9%=
Ta 20,12,24576
Ta 22,12,25600
Mvaa 224,136,4
Mvaa 228,140,4
D 2,40,64,0
Mvaa 228,44,0
W 8
Maaa 192,48,0
Maaa 192,144,4
Maaa 196,148,4
D 3,40,64,1024
Maaa 196,52,0
W 4
Maaa 64,56,0
Maaa 64,152,4
Maaa 68,156,4
D 4,40,64,2048
Maaa 68,60,0
Maaa 76,60,8
Maaa 76,156,12
Maaa 72,152,12
D 5,40,64,3072
Ta 24,13,16384
Maaa 72,56,8
s_add_u32 m0, s61, 36864
Ta 26,13,17408
Ta 28,13,24576
Maaa 204,52,8
Ta 30,13,25600
Maaa 204,148,12
Maaa 200,144,12
D 6,40,64,0
Maaa 200,48,8
Mvaa 236,44,8
Mvaa 236,140,12
Mvaa 232,136,12
D 7,40,64,1024
Mvaa 232,40,8
Mvaa 172,36,8
Mvaa 172,132,12
Mvaa 168,128,12
D 8,40,64,2048
Mvaa 168,32,8
W 4
Mvaa 176,32,16
Mvaa 176,128,20
Mvaa 180,132,20
D 9,40,64,3072
v_add_u32 v2, 131072, v10
Mvaa 180,36,16
Ta 0,14,16384
v_add_u32 v3, 131072, v11
Mvaa 240,40,16
Ta 2,14,17408
v_add_u32 v4, 131072, v12
Mvaa 240,136,20
Ta 4,14,24576
Ta 6,14,25600
Mvaa 244,140,20
buffer_load_dword v142, v144, s[8:11], s91 offen offset:2048 sc1
v_add_u32 v5, 131072, v13
Mvaa 244,44,16
s_add_u32 s91, s91, 4096
v_add_u32 v6, 131072, v14
Maaa 208,48,16
v_add_u32 v7, 131072, v15
Maaa 208,144,20
Maaa 212,148,20
v_add_u32 v8, 131072, v24
Maaa 212,52,16
Maaa 80,56,16
v_add_u32 v9, 131072, v25
Maaa 80,152,20
s_add_u32 s65, s5, 0
s_cmp_ge_u32 s65, 20
s_cselect_b32 s90, 20, 0
s_sub_u32 s65, s65, s90
Maaa 84,156,20
s_lshl_b32 s65, s65, 18
s_add_u32 m0, s63, 131072
Maaa 84,60,16
W 4
Maaa 92,60,24
Maaa 92,156,28
Maaa 88,152,28
Ta 8,15,16384
Ta 10,15,17408
Maaa 88,56,24
D 16,48,65,0
Ta 12,15,24576
Maaa 220,52,24
Ta 14,15,25600
Maaa 220,148,28
Maaa 216,144,28
Maaa 216,48,24
D 17,48,65,1024
Mvaa 252,44,24
Mvaa 252,140,28
Mvaa 248,136,28
Mvaa 248,40,24
Mvaa 188,36,24
D 18,48,65,2048
Mvaa 188,132,28
Mvaa 184,128,28
Mvaa 184,32,24
W 4
Mvaa 192,32,0
Mvaa 192,128,4
D 19,48,65,3072
Mvaa 196,132,4
s_add_u32 m0, s63, 135168
Ta 16,24,16384
Ta 18,24,17408
Mvaa 196,36,0
Ta 20,24,24576
Ta 22,24,25600
Maaa 160,40,0
Maaa 160,136,4
Maaa 164,140,4
D 20,48,65,0
Maaa 164,44,0
Maaa 224,48,0
Maaa 224,144,4
Maaa 228,148,4
Maaa 228,52,0
D 21,48,65,1024
Maaa 96,56,0
Maaa 96,152,4
Maaa 100,156,4
Maaa 100,60,0
)"
R"(W 4
Maaa 108,60,8
D 22,48,65,2048
Ta 24,25,16384
Maaa 108,156,12
Ta 26,25,17408
Maaa 104,152,12
Ta 28,25,24576
Ta 30,25,25600
Maaa 104,56,8
Maaa 236,52,8
D 23,48,65,3072
Maaa 236,148,12
Maaa 232,144,12
Maaa 232,48,8
Maaa 172,44,8
Maaa 172,140,12
Maaa 168,136,12
s_waitcnt vmcnt(9) lgkmcnt(0)
s_barrier
s_cmp_eq_u32 s85, 107
s_cbranch_scc1 .Lrt_25%=
.Lrtr_26%=:
Maaa 168,40,8
s_add_u32 s65, s5, 4
Mvaa 204,36,8
Mvaa 204,132,12
s_cmp_ge_u32 s65, 20
s_cselect_b32 s90, 20, 0
Mvaa 200,128,12
Mvaa 200,32,8
s_sub_u32 s65, s65, s90
Mvaa 208,32,16
Mvaa 208,128,20
s_lshl_b32 s65, s65, 18
Mvaa 212,132,20
Ta 0,10,49152
Ta 2,10,50176
Mvaa 212,36,16
Maaa 176,40,16
s_add_u32 m0, s63, 0
Maaa 176,136,20
Maaa 180,140,20
D 16,48,65,0
Maaa 180,44,16
Maaa 240,48,16
Maaa 240,144,20
Maaa 244,148,20
D 17,48,65,1024
Ta 4,11,49152
Maaa 244,52,16
Ta 6,11,50176
Maaa 112,56,16
Maaa 112,152,20
Maaa 116,156,20
D 18,48,65,2048
Maaa 116,60,16
Maaa 124,60,24
Ra 60,26,47104
Maaa 124,156,28
Maaa 120,152,28
D 19,48,65,3072
Ta 8,12,49152
Maaa 120,56,24
s_add_u32 m0, s63, 4096
Ta 10,12,50176
Ra 56,26,45056
Maaa 252,52,24
Ra 52,26,43008
Maaa 252,148,28
Maaa 248,144,28
D 20,48,65,0
Maaa 248,48,24
Ra 48,26,40960
Maaa 188,44,24
Ra 44,26,38912
Maaa 188,140,28
Maaa 184,136,28
D 21,48,65,1024
Ta 12,13,49152
Maaa 184,40,24
Ta 14,13,50176
Ra 40,26,36864
Mvaa 220,36,24
Ra 36,26,34816
Mvaa 220,132,28
Mvaa 216,128,28
D 22,48,65,2048
Mvaa 216,32,24
Ra 32,26,32768
W 6
Maaa 68,60,0
Maaa 64,56,0
Maaa 196,52,0
D 23,48,65,3072
Ta 16,14,49152
Maaa 192,48,0
W 1
s_add_u32 s65, s5, 8
s_cmp_ge_u32 s65, 20
s_cselect_b32 s90, 20, 0
Mvaa 228,44,0
s_sub_u32 s65, s65, s90
s_lshl_b32 s65, s65, 18
Ta 18,14,50176
Mvaa 224,40,0
s_add_u32 m0, s63, 65536
Mvaa 164,36,0
D 16,48,65,0
Mvaa 160,32,0
Mvaa 168,32,4
Mvaa 172,36,4
Mvaa 232,40,4
D 17,48,65,1024
Ta 20,15,49152
Mvaa 236,44,4
Ta 22,15,50176
Maaa 200,48,4
Maaa 204,52,4
Maaa 72,56,4
D 18,48,65,2048
Maaa 76,60,4
Maaa 84,60,8
Maaa 80,56,8
Maaa 212,52,8
D 19,48,65,3072
Ta 24,24,49152
Maaa 208,48,8
s_add_u32 m0, s63, 69632
Ta 26,24,50176
Mvaa 244,44,8
Mvaa 240,40,8
Mvaa 180,36,8
D 20,48,65,0
Mvaa 176,32,8
Mvaa 184,32,12
Mvaa 188,36,12
Mvaa 248,40,12
D 21,48,65,1024
Ta 28,25,49152
Mvaa 252,44,12
Ta 30,25,50176
Maaa 216,48,12
Ra 156,27,47104
Maaa 220,52,12
Maaa 88,56,12
D 22,48,65,2048
Maaa 92,60,12
W 7
Ra 152,27,45056
Maaa 100,60,16
Maaa 96,56,16
Maaa 228,52,16
D 23,48,65,3072
Ra 148,27,43008
Maaa 224,48,16
Ta 0,10,57344
Ta 2,10,58368
Maaa 164,44,16
Maaa 160,40,16
Ra 144,27,40960
Mvaa 196,36,16
buffer_load_dword v142, v144, s[8:11], s91 offen offset:0 sc1
Mvaa 192,32,16
W 10
Mvaa 200,32,20
Ra 140,27,38912
Mvaa 204,36,20
Maaa 168,40,20
Ta 4,11,57344
Ta 6,11,58368
Maaa 172,44,20
Ra 136,27,36864
Maaa 232,48,20
Maaa 236,52,20
v_bfe_u32 v29, v1, 7, 1
Maaa 104,56,20
Ra 132,27,34816
Maaa 108,60,20
W 13
v_mul_u32_u24 v28, 10240, v29
Maaa 116,60,24
Maaa 112,56,24
Ra 128,27,32768
v_lshrrev_b32 v29, 8, v1
Maaa 244,52,24
Ta 8,12,57344
Ta 10,12,58368
Maaa 240,48,24
v_lshl_add_u32 v28, v29, 7, v28
Maaa 180,44,24
v_and_b32 v29, 0x70, v1
Maaa 176,40,24
Mvaa 212,36,24
v_add_u32 v28, v28, v29
Mvaa 208,32,24
W 14
Mvaa 216,32,28
s_and_b32 s80, s14, 2
Mvaa 220,36,28
s_mul_i32 s80, s80, 20480
Maaa 184,40,28
Ta 12,13,57344
Ta 14,13,58368
Maaa 188,44,28
s_sub_u32 s81, 40960, s80
Maaa 248,48,28
Maaa 252,52,28
s_add_u32 s93, s28, 20480
Maaa 120,56,28
Maaa 124,60,28
W 10
v_add_u32 v26, s80, v28
Maaa 68,156,0
Maaa 64,152,0
Maaa 196,148,0
Ta 16,14,57344
v_add_u32 v27, s81, v28
Maaa 192,144,0
W 5
Ta 18,14,58368
Mvaa 228,140,0
buffer_load_dwordx4 a[32:35], v26, s[8:11], s28 offen offset:0 sc1
Mvaa 224,136,0
Mvaa 164,132,0
Mvaa 160,128,0
Mvaa 168,128,4
buffer_load_dwordx4 a[36:39], v26, s[8:11], s93 offen offset:0 sc1
Mvaa 172,132,4
Mvaa 232,136,4
Ta 20,15,57344
Ta 22,15,58368
Mvaa 236,140,4
Maaa 200,144,4
buffer_load_dwordx4 a[40:43], v27, s[8:11], s28 offen offset:0 sc1
Maaa 204,148,4
Maaa 72,152,4
Maaa 76,156,4
W 6
Maaa 84,156,8
buffer_load_dwordx4 a[44:47], v27, s[8:11], s93 offen offset:0 sc1
Ta 24,24,57344
Maaa 80,152,8
s_add_u32 s28, s28, 512
s_cmp_eq_u32 s28, s4
s_cselect_b32 s93, 10240, 0
Ta 26,24,58368
Maaa 212,148,8
s_sub_u32 s28, s28, s93
Ta 28,25,57344
Ta 30,25,58368
Maaa 208,144,8
s_add_u32 s93, s28, 20480
Mvaa 244,140,8
Mvaa 240,136,8
Mvaa 180,132,8
s_add_u32 s65, s5, 12
s_cmp_ge_u32 s65, 20
s_cselect_b32 s90, 20, 0
buffer_load_dwordx4 a[48:51], v26, s[8:11], s28 offen offset:0 sc1
Mvaa 176,128,8
W 8
s_sub_u32 s65, s65, s90
s_lshl_b32 s65, s65, 18
Mvaa 184,128,12
s_add_u32 m0, s63, 32768
Mvaa 188,132,12
Mvaa 248,136,12
s_waitcnt vmcnt(6) lgkmcnt(0)
s_barrier
buffer_load_dwordx4 a[52:55], v26, s[8:11], s93 offen offset:0 sc1
Mvaa 252,140,12
Maaa 216,144,12
Maaa 220,148,12
Maaa 88,152,12
buffer_load_dwordx4 a[56:59], v27, s[8:11], s28 offen offset:0 sc1
Maaa 92,156,12
Maaa 100,156,16
Maaa 96,152,16
Maaa 228,148,16
buffer_load_dwordx4 a[60:63], v27, s[8:11], s93 offen offset:0 sc1
Maaa 224,144,16
Maaa 164,140,16
Maaa 160,136,16
Mvaa 196,132,16
D 16,48,65,0
Mvaa 192,128,16
Mvaa 200,128,20
Mvaa 204,132,20
Maaa 168,136,20
D 17,48,65,1024
Ta 0,2,0
Maaa 172,140,20
Ta 2,2,1024
Maaa 232,144,20
Maaa 236,148,20
Maaa 104,152,20
D 18,48,65,2048
Ta 4,2,8192
Maaa 108,156,20
Ta 6,2,9216
Maaa 116,156,24
Maaa 112,152,24
Maaa 244,148,24
Ta 8,3,0
Ta 10,3,1024
Maaa 240,144,24
D 19,48,65,3072
Maaa 180,140,24
s_add_u32 m0, s63, 36864
Maaa 176,136,24
Mvaa 212,132,24
Ta 12,3,8192
Ta 14,3,9216
Mvaa 208,128,24
D 20,48,65,0
Mvaa 216,128,28
Mvaa 220,132,28
Maaa 184,136,28
Ta 16,4,0
Ta 18,4,1024
Maaa 188,140,28
Maaa 248,144,28
D 21,48,65,1024
Maaa 252,148,28
Maaa 120,152,28
Maaa 124,156,28
.Lq_11%=:
s_cmp_ge_u32 s14, 2
s_cbranch_scc1 .Lpb_31%=
s_waitcnt vmcnt(6) lgkmcnt(6)
Zvaa 32,0,32
s_add_u32 s28, s28, 512
Ta 20,4,8192
Ta 22,4,9216
Mvaa 32,4,48
s_cmp_eq_u32 s28, s4
s_cselect_b32 s93, 10240, 0
Ta 24,5,0
Ta 26,5,1024
Zvaa 64,4,52
s_sub_u32 s28, s28, s93
s_add_u32 s93, s28, 20480
buffer_load_dwordx4 a[128:131], v26, s[8:11], s28 offen offset:0 sc1
Mvaa 64,0,36
Ta 28,5,8192
Ta 30,5,9216
Zvaa 96,0,40
K 184,184,185
K 185,186,187
Mvaa 96,4,56
K 186,188,189
K 187,190,191
Zvaa 128,4,60
K 192,192,193
Mvaa 128,0,44
W 8
buffer_load_dwordx4 a[132:135], v26, s[8:11], s93 offen offset:0 sc1
K 193,194,195
Zvaa 132,8,44
Ta 0,6,0
Ta 2,6,1024
Mvaa 132,12,60
K 194,196,197
K 195,198,199
Zvaa 100,12,56
K 200,200,201
K 201,202,203
Mvaa 100,8,40
Ta 4,6,8192
Ta 6,6,9216
Zvaa 68,8,36
buffer_load_dwordx4 a[136:139], v27, s[8:11], s28 offen offset:0 sc1
K 202,204,205
Mvaa 68,12,52
K 203,206,207
K 208,208,209
Zvaa 36,12,48
K 209,210,211
K 210,212,213
Mvaa 36,8,32
W 8
Ta 8,7,0
K 211,214,215
Zvaa 40,16,32
Ta 10,7,1024
K 216,216,217
Mvaa 40,20,48
buffer_load_dwordx4 a[140:143], v27, s[8:11], s93 offen offset:0 sc1
K 217,218,219
Zvaa 72,20,52
s_add_u32 s28, s28, 512
s_cmp_eq_u32 s28, s4
s_cselect_b32 s93, 10240, 0
s_sub_u32 s28, s28, s93
Mvaa 72,16,36
s_add_u32 s93, s28, 20480
K 218,220,221
K 219,222,223
Zvaa 104,16,40
)"
R"(Ta 12,7,8192
Ta 14,7,9216
Mvaa 104,20,56
K 224,224,225
K 225,226,227
Zvaa 136,20,60
K 226,228,229
K 227,230,231
Mvaa 136,16,44
W 8
buffer_load_dwordx4 a[144:147], v26, s[8:11], s28 offen offset:0 sc1
K 232,232,233
Zvaa 140,24,44
K 233,234,235
K 234,236,237
Mvaa 140,28,60
Ta 16,8,0
K 235,238,239
Zvaa 108,28,56
Ta 18,8,1024
K 240,240,241
Mvaa 108,24,40
K 241,242,243
K 242,244,245
Zvaa 76,24,36
buffer_load_dwordx4 a[148:151], v26, s[8:11], s93 offen offset:0 sc1
K 243,246,247
Mvaa 76,28,52
Ta 20,8,8192
Ta 22,8,9216
Zvaa 44,28,48
K 248,248,249
K 249,250,251
Mvaa 44,24,32
K 250,252,253
K 251,254,255
W 8
Zvaa 48,0,32
Ta 24,9,0
Ta 26,9,1024
Mvaa 48,4,48
buffer_load_dwordx4 a[152:155], v27, s[8:11], s28 offen offset:0 sc1
K 160,160,161
Zvaa 80,4,52
Ta 28,9,8192
Ta 30,9,9216
Mvaa 80,0,36
K 161,162,163
Zvaa 112,0,40
K 162,164,165
Mvaa 112,4,56
Zvaa 144,4,60
buffer_load_dwordx4 a[156:159], v27, s[8:11], s93 offen offset:0 sc1
K 163,166,167
Mvaa 144,0,44
W 8
s_add_u32 s28, s28, 512
s_cmp_eq_u32 s28, s4
s_cselect_b32 s93, 10240, 0
Zvaa 148,8,44
s_sub_u32 s28, s28, s93
s_add_u32 s93, s28, 20480
Ta 0,2,16384
Mvaa 148,12,60
s_add_u32 s36, s29, 0
Ta 2,2,17408
Zvaa 116,12,56
Ta 4,2,24576
Ta 6,2,25600
Mvaa 116,8,40
buffer_load_dwordx4 v[188:191], v26, s[8:11], s28 offen offset:0 sc1
Zvaa 84,8,36
Mvaa 84,12,52
Zvaa 52,12,48
Mvaa 52,8,32
W 8
Ta 8,3,16384
Ta 10,3,17408
Zvaa 56,16,32
buffer_load_dwordx4 v[196:199], v26, s[8:11], s93 offen offset:0 sc1
Mvaa 56,20,48
Zvaa 88,20,52
Ta 12,3,24576
Ta 14,3,25600
Mvaa 88,16,36
Zvaa 120,16,40
buffer_store_dwordx4 v[160:163], v1, s[8:11], s36 offen offset:0 sc1
K 168,168,169
Mvaa 120,20,56
K 169,170,171
K 170,172,173
Zvaa 152,20,60
Mvaa 152,16,44
W 8
Ta 16,4,16384
K 171,174,175
Zvaa 156,24,44
Ta 18,4,17408
Mvaa 156,28,60
buffer_load_dwordx4 v[204:207], v27, s[8:11], s28 offen offset:0 sc1
Zvaa 124,28,56
Ta 20,4,24576
Ta 22,4,25600
Mvaa 124,24,40
Zvaa 92,24,36
Mvaa 92,28,52
Zvaa 60,28,48
buffer_load_dwordx4 v[212:215], v27, s[8:11], s93 offen offset:0 sc1
Mvaa 60,24,32
s_waitcnt vmcnt(5) lgkmcnt(8)
s_add_u32 s28, s28, 512
s_cmp_eq_u32 s28, s4
s_cselect_b32 s93, 10240, 0
Mvaa 32,0,128
s_sub_u32 s28, s28, s93
s_add_u32 s93, s28, 20480
Ta 24,5,16384
Mvaa 32,4,144
Ta 26,5,17408
Mvaa 64,4,148
Ta 28,5,24576
Ta 30,5,25600
Mvaa 64,0,132
buffer_store_dwordx4 v[168:171], v1, s[8:11], s36 offen offset:1024 sc1
K 176,176,177
Mvaa 96,0,136
K 177,178,179
K 178,180,181
Mvaa 96,4,152
Mvaa 128,4,156
Mvaa 128,0,140
W 8
Ta 0,6,16384
K 179,182,183
Mvaa 132,8,140
buffer_load_dwordx4 v[220:223], v26, s[8:11], s28 offen offset:0 sc1
Ta 2,6,17408
Mvaa 132,12,156
Mvaa 100,12,152
Ta 4,6,24576
Ta 6,6,25600
Mvaa 100,8,136
Mvaa 68,8,132
Mvaa 68,12,148
buffer_load_dwordx4 v[228:231], v26, s[8:11], s93 offen offset:0 sc1
Mvaa 36,12,144
Mvaa 36,8,128
W 8
Ta 8,7,16384
Ta 10,7,17408
Mvaa 40,16,128
Mvaa 40,20,144
Mvaa 72,20,148
buffer_load_dwordx4 v[236:239], v27, s[8:11], s28 offen offset:0 sc1
Ta 12,7,24576
Mvaa 72,16,132
buffer_store_dwordx4 v[176:179], v1, s[8:11], s36 offen offset:2048 sc1
Ta 14,7,25600
Mvaa 104,16,136
Mvaa 104,20,152
Mvaa 136,20,156
Mvaa 136,16,140
W 8
Ta 16,8,16384
Ta 18,8,17408
Mvaa 140,24,140
buffer_load_dwordx4 v[244:247], v27, s[8:11], s93 offen offset:0 sc1
Mvaa 140,28,156
s_add_u32 s28, s28, 512
s_cmp_eq_u32 s28, s4
s_cselect_b32 s93, 10240, 0
s_sub_u32 s28, s28, s93
Mvaa 108,28,152
s_add_u32 s93, s28, 20480
Ta 20,8,24576
Ta 22,8,25600
Mvaa 108,24,136
Mvaa 76,24,132
Mvaa 76,28,148
buffer_load_dwordx4 v[252:255], v26, s[8:11], s28 offen offset:0 sc1
Mvaa 44,28,144
Mvaa 44,24,128
W 8
Ta 24,9,16384
Ta 26,9,17408
Mvaa 48,0,128
Mvaa 48,4,144
Mvaa 80,4,148
D 22,48,65,2048
Ta 28,9,24576
Mvaa 80,0,132
buffer_store_dwordx4 v[184:187], v1, s[8:11], s36 offen offset:3072 sc1
Ta 30,9,25600
Mvaa 112,0,136
s_add_u32 s36, s29, 4096
s_lshl_b32 s80, s56, 9
Mvaa 112,4,152
Mvaa 144,4,156
s_lshl_b32 s81, s57, 4
Mvaa 144,0,140
W 8
s_add_u32 s80, s80, s81
Ta 0,10,0
Mvaa 148,8,140
buffer_load_dwordx4 a[32:35], v26, s[8:11], s93 offen offset:0 sc1
Ta 2,10,1024
Mvaa 148,12,156
s_and_b32 s81, s14, 1
Mvaa 116,12,152
s_lshl_b32 s81, s81, 3
Ta 4,10,8192
Ta 6,10,9216
Mvaa 116,8,136
Mvaa 84,8,132
s_add_u32 s80, s80, s81
Mvaa 84,12,148
s_add_u32 s71, s80, 4096
buffer_load_dwordx4 a[36:39], v27, s[8:11], s28 offen offset:0 sc1
Mvaa 52,12,144
Mvaa 52,8,128
W 8
Ta 8,11,0
Ta 10,11,1024
Mvaa 56,16,128
Mvaa 56,20,144
Mvaa 88,20,148
buffer_store_dwordx4 v[192:195], v1, s[8:11], s36 offen offset:0 sc1
Ta 12,11,8192
Mvaa 88,16,132
Ta 14,11,9216
Mvaa 120,16,136
Mvaa 120,20,152
Mvaa 152,20,156
Mvaa 152,16,140
W 8
buffer_load_dwordx4 a[40:43], v27, s[8:11], s93 offen offset:0 sc1
Ta 16,12,0
Mvaa 156,24,140
s_add_u32 s28, s28, 512
s_cmp_eq_u32 s28, s4
s_cselect_b32 s93, 10240, 0
s_sub_u32 s28, s28, s93
Mvaa 156,28,156
s_add_u32 s93, s28, 20480
Ta 18,12,1024
Mvaa 124,28,152
Ta 20,12,8192
Ta 22,12,9216
Mvaa 124,24,136
Mvaa 92,24,132
buffer_load_dwordx4 a[44:47], v26, s[8:11], s28 offen offset:0 sc1
Mvaa 92,28,148
Mvaa 60,28,144
Mvaa 60,24,128
s_waitcnt vmcnt(8) lgkmcnt(8)
Ta 24,13,0
Ta 26,13,1024
Mvav 32,0,188
Mvav 32,4,220
D 23,48,65,3072
Mvav 64,4,228
s_add_u32 s65, s5, 16
s_cmp_ge_u32 s65, 20
s_cselect_b32 s90, 20, 0
buffer_store_dwordx4 v[200:203], v1, s[8:11], s36 offen offset:1024 sc1
Mvav 64,0,196
s_sub_u32 s65, s65, s90
s_lshl_b32 s65, s65, 18
s_add_u32 m0, s63, 98304
Mvav 96,0,204
Ta 28,13,8192
Ta 30,13,9216
Mvav 96,4,236
Mvav 128,4,244
Mvav 128,0,212
W 8
buffer_load_dwordx4 a[48:51], v26, s[8:11], s93 offen offset:0 sc1
Ta 0,14,0
Mvav 132,8,212
Ta 2,14,1024
v_add_u32 v2, 65536, v10
Mvav 132,12,244
Mvav 100,12,236
Ta 4,14,8192
Ta 6,14,9216
Mvav 100,8,204
v_add_u32 v3, 65536, v11
Mvav 68,8,196
buffer_load_dwordx4 a[52:55], v27, s[8:11], s28 offen offset:0 sc1
Mvav 68,12,228
Mvav 36,12,220
Mvav 36,8,188
W 8
Ta 8,15,0
Ta 10,15,1024
Mvav 40,16,188
v_add_u32 v4, 65536, v12
Mvav 40,20,220
buffer_load_dwordx4 a[56:59], v27, s[8:11], s93 offen offset:0 sc1
Mvav 72,20,228
s_add_u32 s28, s28, 512
s_cmp_eq_u32 s28, s4
s_cselect_b32 s93, 10240, 0
buffer_store_dwordx4 v[208:211], v1, s[8:11], s36 offen offset:2048 sc1
Mvav 72,16,196
s_sub_u32 s28, s28, s93
s_add_u32 s93, s28, 20480
Ta 12,15,8192
Mvav 104,16,204
Ta 14,15,9216
v_add_u32 v5, 65536, v13
Mvav 104,20,236
Mvav 136,20,244
Mvav 136,16,212
W 8
D 16,48,65,0
Ta 16,24,0
Mvav 140,24,212
Ta 18,24,1024
v_add_u32 v6, 65536, v14
Mvav 140,28,244
Mvav 108,28,236
Ta 20,24,8192
Ta 22,24,9216
Mvav 108,24,204
v_add_u32 v7, 65536, v15
Mvav 76,24,196
buffer_load_dwordx4 a[60:63], v26, s[8:11], s28 offen offset:0 sc1
Mvav 76,28,228
Mvav 44,28,220
Mvav 44,24,188
W 8
Ta 24,25,0
Ta 26,25,1024
Mvav 48,0,188
v_add_u32 v8, 65536, v24
Mvav 48,4,220
buffer_load_dwordx4 v[184:187], v26, s[8:11], s93 offen offset:0 sc1
Mvav 80,4,228
buffer_store_dwordx4 v[216:219], v1, s[8:11], s36 offen offset:3072 sc1
Ta 28,25,8192
Mvav 80,0,196
s_add_u32 s36, s29, 458752
Ta 30,25,9216
v_add_u32 v9, 65536, v25
Mvav 112,0,204
Mvav 112,4,236
Mvav 144,4,244
Mvav 144,0,212
W 8
buffer_load_dwordx4 v[192:195], v27, s[8:11], s28 offen offset:0 sc1
Ta 0,10,16384
Mvav 148,8,212
Ta 2,10,17408
Mvav 148,12,244
Mvav 116,12,236
Ta 4,10,24576
Ta 6,10,25600
Mvav 116,8,204
Mvav 84,8,196
D 17,48,65,1024
Mvav 84,12,228
Mvav 52,12,220
Mvav 52,8,188
W 8
Ta 8,11,16384
Ta 10,11,17408
Mvav 56,16,188
Mvav 56,20,220
buffer_store_dwordx4 v[224:227], v1, s[8:11], s36 offen offset:0 sc1
Mvav 88,20,228
Ta 12,11,24576
Ta 14,11,25600
Mvav 88,16,196
Mvav 120,16,204
Mvav 120,20,236
Mvav 152,20,244
buffer_load_dwordx4 a[128:131], v27, s[8:11], s93 offen offset:0 sc1
Mvav 152,16,212
W 8
s_add_u32 s28, s28, 512
s_cmp_eq_u32 s28, s4
s_cselect_b32 s93, 10240, 0
Mvav 156,24,212
s_sub_u32 s28, s28, s93
s_add_u32 s93, s28, 20480
Ta 16,12,16384
Mvav 156,28,244
Ta 18,12,17408
Mvav 124,28,236
Ta 20,12,24576
Ta 22,12,25600
Mvav 124,24,204
buffer_load_dwordx4 a[132:135], v26, s[8:11], s28 offen offset:0 sc1
Mvav 92,24,196
Mvav 92,28,228
Mvav 60,28,220
Mvav 60,24,188
s_waitcnt vmcnt(10) lgkmcnt(8)
Ta 24,13,16384
)"
R"(Ta 26,13,17408
Mvav 32,0,252
buffer_load_dwordx4 a[136:139], v26, s[8:11], s93 offen offset:0 sc1
Mvaa 32,4,44
buffer_store_dwordx4 v[232:235], v1, s[8:11], s36 offen offset:1024 sc1
Mvaa 64,4,48
Ta 28,13,24576
Ta 30,13,25600
Mvaa 64,0,32
Mvaa 96,0,36
Mvaa 96,4,52
Mvaa 128,4,56
buffer_load_dwordx4 a[140:143], v27, s[8:11], s28 offen offset:0 sc1
Mvaa 128,0,40
W 8
Ta 0,14,16384
Ta 2,14,17408
Mvaa 132,8,40
Mvaa 132,12,56
Mvaa 100,12,52
Ta 4,14,24576
Ta 6,14,25600
Mvaa 100,8,36
D 18,48,65,2048
Mvaa 68,8,32
Mvaa 68,12,48
Mvaa 36,12,44
Mvav 36,8,252
W 8
Ta 8,15,16384
Ta 10,15,17408
Mvav 40,16,252
buffer_load_dwordx4 a[144:147], v27, s[8:11], s93 offen offset:0 sc1
Mvaa 40,20,44
s_add_u32 s28, s28, 512
s_cmp_eq_u32 s28, s4
s_cselect_b32 s93, 10240, 0
buffer_store_dwordx4 v[240:243], v1, s[8:11], s36 offen offset:2048 sc1
Mvaa 72,20,48
s_sub_u32 s28, s28, s93
s_add_u32 s93, s28, 20480
Ta 12,15,24576
Mvaa 72,16,32
Ta 14,15,25600
Mvaa 104,16,36
Mvaa 104,20,52
Mvaa 136,20,56
buffer_load_dwordx4 a[148:151], v26, s[8:11], s28 offen offset:0 sc1
Mvaa 136,16,40
W 8
Ta 16,24,16384
Ta 18,24,17408
Mvaa 140,24,40
Mvaa 140,28,56
Mvaa 108,28,52
Ta 20,24,24576
Ta 22,24,25600
Mvaa 108,24,36
buffer_load_dwordx4 a[152:155], v26, s[8:11], s93 offen offset:0 sc1
Mvaa 76,24,32
Mvaa 76,28,48
Mvaa 44,28,44
Mvav 44,24,252
W 8
Ta 24,25,16384
Ta 26,25,17408
Mvav 48,0,252
buffer_load_dwordx4 a[156:159], v27, s[8:11], s28 offen offset:0 sc1
Mvaa 48,4,44
buffer_store_dwordx4 v[248:251], v1, s[8:11], s36 offen offset:3072 sc1
G 160,160
Mvaa 80,4,48
G 161,161
G 162,162
Mvaa 80,0,32
G 163,163
G 164,164
Mvaa 112,0,36
G 165,165
G 166,166
Mvaa 112,4,52
Ta 28,25,24576
Ta 30,25,25600
Mvaa 144,4,56
D 19,48,65,3072
G 167,167
Mvaa 144,0,40
W 8
s_add_u32 m0, s63, 102400
K 160,160,161
Mvaa 148,8,40
K 161,162,163
K 162,164,165
Mvaa 148,12,56
Ta 0,2,0
K 163,166,167
Mvaa 116,12,52
s_add_u32 s36, s29, 462848
Ta 2,2,1024
Ta 4,2,8192
Mvaa 116,8,36
buffer_load_dwordx4 v[200:203], v27, s[8:11], s93 offen offset:0 sc1
Ta 6,2,9216
Mvaa 84,8,32
s_add_u32 s28, s28, 512
s_cmp_eq_u32 s28, s4
s_cselect_b32 s93, 10240, 0
s_sub_u32 s28, s28, s93
Mvaa 84,12,48
s_add_u32 s93, s28, 20480
Mvaa 52,12,44
Mvav 52,8,252
W 8
Ta 8,3,0
Ta 10,3,1024
Mvav 56,16,252
buffer_store_dwordx4 v[160:163], v1, s[8:11], s36 offen offset:0 sc1
G 168,168
Mvaa 56,20,44
G 169,169
G 170,170
Mvaa 88,20,48
G 171,171
G 172,172
Mvaa 88,16,32
G 173,173
G 174,174
Mvaa 120,16,36
Ta 12,3,8192
Ta 14,3,9216
Mvaa 120,20,52
buffer_load_dwordx4 v[208:211], v26, s[8:11], s28 offen offset:0 sc1
G 175,175
Mvaa 152,20,56
K 168,168,169
K 169,170,171
Mvaa 152,16,40
W 8
K 170,172,173
K 171,174,175
Mvaa 156,24,40
Ta 16,4,0
Ta 18,4,1024
Mvaa 156,28,56
Mvaa 124,28,52
buffer_load_dwordx4 v[216:219], v26, s[8:11], s93 offen offset:0 sc1
Ta 20,4,8192
Mvaa 124,24,36
Ta 22,4,9216
Mvaa 92,24,32
Mvaa 92,28,48
Mvaa 60,28,44
Mvav 60,24,252
s_waitcnt vmcnt(10) lgkmcnt(8)
D 20,48,65,0
Ta 24,5,0
Mvaa 32,0,60
buffer_store_dwordx4 v[168:171], v1, s[8:11], s36 offen offset:1024 sc1
G 176,176
Mvaa 32,4,132
G 177,177
G 178,178
Mvaa 64,4,136
Ta 26,5,1024
G 179,179
Mvav 64,0,184
G 180,180
G 181,181
Mvav 96,0,192
Ta 28,5,8192
G 182,182
Mvaa 96,4,140
buffer_load_dwordx4 v[224:227], v27, s[8:11], s28 offen offset:0 sc1
Ta 30,5,9216
Mvaa 128,4,144
G 183,183
K 176,176,177
Mvaa 128,0,128
W 8
K 177,178,179
K 178,180,181
Mvaa 132,8,128
Ta 0,6,0
K 179,182,183
Mvaa 132,12,144
Ta 2,6,1024
Mvaa 100,12,140
buffer_load_dwordx4 v[188:191], v27, s[8:11], s93 offen offset:0 sc1
Ta 4,6,8192
Mvav 100,8,192
s_add_u32 s28, s28, 512
s_cmp_eq_u32 s28, s4
s_cselect_b32 s93, 10240, 0
s_sub_u32 s28, s28, s93
Mvav 68,8,184
s_add_u32 s93, s28, 20480
Ta 6,6,9216
Mvaa 68,12,136
Mvaa 36,12,132
Mvaa 36,8,60
W 8
buffer_load_dwordx4 v[196:199], v26, s[8:11], s28 offen offset:0 sc1
Ta 8,7,0
Mvaa 40,16,60
buffer_store_dwordx4 v[176:179], v1, s[8:11], s36 offen offset:2048 sc1
G 160,184
Mvaa 40,20,132
G 161,185
G 162,186
Mvaa 72,20,136
Ta 10,7,1024
G 163,187
Mvav 72,16,184
G 164,188
G 165,189
Mvav 104,16,192
Ta 12,7,8192
G 166,190
Mvaa 104,20,140
buffer_load_dwordx4 v[204:207], v26, s[8:11], s93 offen offset:0 sc1
Ta 14,7,9216
Mvaa 136,20,144
G 167,191
K 160,160,161
Mvaa 136,16,128
W 8
K 161,162,163
K 162,164,165
Mvaa 140,24,128
Ta 16,8,0
K 163,166,167
Mvaa 140,28,144
Ta 18,8,1024
Mvaa 108,28,140
D 21,48,65,1024
Ta 20,8,8192
Mvav 108,24,192
Ta 22,8,9216
Mvav 76,24,184
Mvaa 76,28,136
Mvaa 44,28,132
Mvaa 44,24,60
W 8
buffer_load_dwordx4 v[212:215], v27, s[8:11], s28 offen offset:0 sc1
Ta 24,9,0
Mvaa 48,0,60
buffer_store_dwordx4 v[160:163], v1, s[8:11], s36 offen offset:3072 sc1
G 168,192
Mvaa 48,4,132
G 169,193
G 170,194
Mvaa 80,4,136
G 171,195
G 172,196
Mvav 80,0,184
Ta 26,9,1024
G 173,197
Mvav 112,0,192
G 174,198
G 175,199
Mvaa 112,4,140
buffer_load_dwordx4 v[220:223], v27, s[8:11], s93 offen offset:0 sc1
Ta 28,9,8192
Mvaa 144,4,144
s_add_u32 s28, s28, 512
s_cmp_eq_u32 s28, s4
s_cselect_b32 s93, 10240, 0
s_sub_u32 s28, s28, s93
Mvaa 144,0,128
W 7
s_add_u32 s93, s28, 20480
Ta 30,9,9216
Mvaa 148,8,128
K 168,168,169
K 169,170,171
Mvaa 148,12,144
K 170,172,173
K 171,174,175
Mvaa 116,12,140
buffer_load_dwordx4 v[228:231], v26, s[8:11], s28 offen offset:0 sc1
Ta 0,2,16384
Mvav 116,8,192
Ta 2,2,17408
v_accvgpr_write_b32 a192, v168
Mvav 84,8,184
v_accvgpr_write_b32 a193, v169
v_accvgpr_write_b32 a194, v170
Mvaa 84,12,136
Ta 4,2,24576
Ta 6,2,25600
Mvaa 52,12,132
v_accvgpr_write_b32 a195, v171
G 176,64
Mvaa 52,8,60
W 8
D 22,48,65,2048
G 177,65
Mvaa 56,16,60
D 23,48,65,3072
G 178,66
Mvaa 56,20,132
Ta 8,3,16384
Ta 10,3,17408
Mvaa 88,20,136
G 179,67
G 180,68
Mvav 88,16,184
Ta 12,3,24576
Ta 14,3,25600
Mvav 120,16,192
G 181,69
G 182,70
Mvaa 120,20,140
buffer_load_dwordx4 v[236:239], v26, s[8:11], s93 offen offset:0 sc1
G 183,71
Mvaa 152,20,144
v_lshrrev_b32 v20, 4, v1
K 176,176,177
Mvaa 152,16,128
W 8
Ta 16,4,16384
v_add_u32 v20, 0, v20
Mvaa 156,24,128
Ta 18,4,17408
K 177,178,179
Mvaa 156,28,144
v_mul_u32_u24 v22, 205, v20
K 178,180,181
Mvaa 124,28,140
buffer_load_dwordx4 v[232:235], v27, s[8:11], s28 offen offset:0 sc1
K 179,182,183
Mvav 124,24,192
Ta 20,4,24576
Ta 22,4,25600
Mvav 92,24,184
v_lshrrev_b32 v22, 12, v22
v_accvgpr_write_b32 a196, v176
Mvaa 92,28,136
v_mul_u32_u24 v23, 20, v22
v_accvgpr_write_b32 a197, v177
Mvaa 60,28,132
v_accvgpr_write_b32 a198, v178
v_sub_u32 v20, v20, v23
Mvaa 60,24,60
s_waitcnt vmcnt(12) lgkmcnt(8)
buffer_load_dwordx4 a[32:35], v27, s[8:11], s93 offen offset:0 sc1
v_accvgpr_write_b32 a199, v179
Mvaa 32,0,148
s_add_u32 s28, s28, 512
s_cmp_eq_u32 s28, s4
s_cselect_b32 s93, 10240, 0
s_sub_u32 s28, s28, s93
Mvav 32,4,208
s_add_u32 s93, s28, 20480
Ta 24,5,16384
Ta 26,5,17408
Mvav 64,4,216
v_lshrrev_b32 v23, 1, v22
G 168,200
Mvaa 64,0,152
v_and_b32 v23, 2, v23
G 169,201
Mvaa 96,0,156
buffer_load_dwordx4 a[36:39], v26, s[8:11], s28 offen offset:0 sc1
Ta 28,5,24576
Mvav 96,4,224
Ta 30,5,25600
G 170,202
Mvav 128,4,188
v_xor_b32 v20, v20, v23
G 171,203
Mvav 128,0,200
W 8
v_mul_u32_u24 v22, 80, v22
G 172,204
Mvav 132,8,200
v_add_lshl_u32 v16, v20, v22, 4
G 173,205
Mvav 132,12,188
buffer_load_dwordx4 a[40:43], v26, s[8:11], s93 offen offset:0 sc1
Ta 0,6,16384
Mvav 100,12,224
Ta 2,6,17408
G 174,206
Mvaa 100,8,156
v_lshrrev_b32 v20, 4, v1
G 175,207
Mvaa 68,8,152
Ta 4,6,24576
v_add_u32 v20, 64, v20
Mvav 68,12,216
Ta 6,6,25600
K 168,168,169
Mvav 36,12,208
buffer_load_dwordx4 a[44:47], v27, s[8:11], s28 offen offset:0 sc1
K 169,170,171
Mvaa 36,8,148
W 8
v_mul_u32_u24 v22, 205, v20
K 170,172,173
)"
R"(Mvaa 40,16,148
v_lshrrev_b32 v22, 12, v22
K 171,174,175
Mvav 40,20,208
Ta 8,7,16384
v_mul_u32_u24 v23, 20, v22
Mvav 72,20,216
Ta 10,7,17408
v_accvgpr_write_b32 a200, v168
Mvaa 72,16,152
buffer_load_dwordx4 a[48:51], v27, s[8:11], s93 offen offset:0 sc1
v_accvgpr_write_b32 a201, v169
Mvaa 104,16,156
s_add_u32 s28, s28, 512
s_cmp_eq_u32 s28, s4
s_cselect_b32 s93, 10240, 0
s_sub_u32 s28, s28, s93
Mvav 104,20,224
s_add_u32 s93, s28, 20480
v_sub_u32 v20, v20, v23
v_accvgpr_write_b32 a202, v170
Mvav 136,20,188
Ta 12,7,24576
Ta 14,7,25600
Mvav 136,16,200
W 8
v_lshrrev_b32 v23, 1, v22
v_accvgpr_write_b32 a203, v171
Mvav 140,24,200
buffer_load_dwordx4 a[52:55], v26, s[8:11], s28 offen offset:0 sc1
v_and_b32 v23, 2, v23
Mvav 140,28,188
G 176,72
G 177,73
Mvav 108,28,224
v_xor_b32 v20, v20, v23
G 178,74
Mvaa 108,24,156
Ta 16,8,16384
Ta 18,8,17408
Mvaa 76,24,152
v_mul_u32_u24 v22, 80, v22
G 179,75
Mvav 76,28,216
buffer_load_dwordx4 a[56:59], v26, s[8:11], s93 offen offset:0 sc1
G 180,76
Mvav 44,28,208
Ta 20,8,24576
v_add_lshl_u32 v17, v20, v22, 4
Mvaa 44,24,148
W 7
Ta 22,8,25600
G 181,77
Mvaa 48,0,148
v_subrev_u32 v17, 1024, v17
G 182,78
Mvav 48,4,208
v_lshrrev_b32 v20, 4, v1
G 183,79
Mvav 80,4,216
buffer_load_dwordx4 a[184:187], v27, s[8:11], s28 offen offset:0 sc1
K 176,176,177
Mvaa 80,0,152
Ta 24,9,16384
v_add_u32 v20, 128, v20
Mvaa 112,0,156
Ta 26,9,17408
K 177,178,179
Mvav 112,4,224
v_mul_u32_u24 v22, 205, v20
K 178,180,181
Mvav 144,4,188
v_lshrrev_b32 v22, 12, v22
K 179,182,183
Mvav 144,0,200
W 6
buffer_load_dwordx4 a[188:191], v27, s[8:11], s93 offen offset:0 sc1
Ta 28,9,24576
Mvav 148,8,200
s_add_u32 s28, s28, 512
s_cmp_eq_u32 s28, s4
s_cselect_b32 s93, 10240, 0
s_sub_u32 s28, s28, s93
Mvav 148,12,188
s_add_u32 s93, s28, 20480
Ta 30,9,25600
v_accvgpr_write_b32 a204, v176
Mvav 116,12,224
v_mul_u32_u24 v23, 20, v22
v_accvgpr_write_b32 a205, v177
Mvaa 116,8,156
s_waitcnt vmcnt(15)
v_mov_b64 v[28:29], s[12:13]
s_mov_b64 exec, 1
buffer_store_dwordx2 v[28:29], off, s[8:11], s71 sc1
s_mov_b64 exec, -1
v_sub_u32 v20, v20, v23
v_accvgpr_write_b32 a206, v178
Mvaa 84,8,152
s_waitcnt vmcnt(12) lgkmcnt(0)
s_barrier
v_accvgpr_write_b32 a207, v179
Mvav 84,12,216
v_lshrrev_b32 v23, 1, v22
G 168,208
Mvav 52,12,208
v_and_b32 v23, 2, v23
G 169,209
Mvaa 52,8,148
v_xor_b32 v20, v20, v23
G 170,210
Mvaa 56,16,148
buffer_load_dwordx4 a[60:63], v26, s[8:11], s28 offen offset:0 sc1
G 171,211
Mvav 56,20,208
Ta 0,10,32768
Ta 2,10,33792
Mvav 88,20,216
Ta 4,10,40960
Ta 6,10,41984
Mvaa 88,16,152
v_mul_u32_u24 v22, 80, v22
G 172,212
Mvaa 120,16,156
v_add_lshl_u32 v18, v20, v22, 4
G 173,213
Mvav 120,20,224
buffer_load_dwordx4 v[184:187], v26, s[8:11], s93 offen offset:0 sc1
Ta 8,11,32768
Mvav 152,20,188
Ta 10,11,33792
G 174,214
Mvav 152,16,200
v_subrev_u32 v18, 2048, v18
G 175,215
Mvav 156,24,200
Ta 12,11,40960
v_lshrrev_b32 v20, 4, v1
Mvav 156,28,188
Ta 14,11,41984
K 168,168,169
Mvav 124,28,224
buffer_load_dwordx4 v[192:195], v27, s[8:11], s28 offen offset:0 sc1
v_add_u32 v20, 192, v20
Mvaa 124,24,156
K 169,170,171
K 170,172,173
Mvaa 92,24,152
v_mul_u32_u24 v22, 205, v20
K 171,174,175
Mvav 92,28,216
Ta 16,12,32768
v_lshrrev_b32 v22, 12, v22
Mvav 60,28,208
Ta 18,12,33792
v_accvgpr_write_b32 a208, v168
Mvaa 60,24,148
s_waitcnt vmcnt(12) lgkmcnt(6)
buffer_load_dwordx4 a[128:131], v27, s[8:11], s93 offen offset:0 sc1
v_mul_u32_u24 v23, 20, v22
Mvav 32,0,196
s_add_u32 s28, s28, 512
v_accvgpr_write_b32 a209, v169
v_accvgpr_write_b32 a210, v170
Mvav 32,4,228
s_cmp_eq_u32 s28, s4
s_cselect_b32 s93, 10240, 0
Ta 20,12,40960
v_sub_u32 v20, v20, v23
Mvav 64,4,236
s_sub_u32 s28, s28, s93
Ta 22,12,41984
v_accvgpr_write_b32 a211, v171
Mvav 64,0,204
s_add_u32 s93, s28, 20480
v_lshrrev_b32 v23, 1, v22
G 176,80
Mvav 96,0,212
G 177,81
v_and_b32 v23, 2, v23
Mvav 96,4,232
G 178,82
v_xor_b32 v20, v20, v23
Mvaa 128,4,32
Ta 24,13,32768
Ta 26,13,33792
Mvav 128,0,220
W 6
buffer_load_dwordx4 a[132:135], v26, s[8:11], s28 offen offset:0 sc1
G 179,83
Mvav 132,8,220
v_mul_u32_u24 v22, 80, v22
G 180,84
Mvaa 132,12,32
Ta 28,13,40960
Ta 30,13,41984
Mvav 100,12,232
G 181,85
v_add_lshl_u32 v19, v20, v22, 4
Mvav 100,8,212
G 182,86
v_subrev_u32 v19, 3072, v19
Mvav 68,8,204
G 183,87
v_lshrrev_b32 v20, 4, v1
Mvav 68,12,236
Ta 0,14,32768
K 176,176,177
Mvav 36,12,228
Ta 2,14,33792
K 177,178,179
Mvav 36,8,196
W 6
buffer_load_dwordx4 a[136:139], v26, s[8:11], s93 offen offset:0 sc1
v_add_u32 v20, 256, v20
Mvav 40,16,196
K 178,180,181
v_mul_u32_u24 v22, 205, v20
Mvav 40,20,228
Ta 4,14,40960
K 179,182,183
Mvav 72,20,236
Ta 6,14,41984
v_accvgpr_write_b32 a212, v176
Mvav 72,16,204
v_lshrrev_b32 v22, 12, v22
v_accvgpr_write_b32 a213, v177
Mvav 104,16,212
v_mul_u32_u24 v23, 20, v22
v_accvgpr_write_b32 a214, v178
Mvav 104,20,232
v_sub_u32 v20, v20, v23
v_accvgpr_write_b32 a215, v179
Mvaa 136,20,32
Ta 8,15,32768
Ta 10,15,33792
Mvav 136,16,220
W 6
buffer_load_dwordx4 a[140:143], v27, s[8:11], s28 offen offset:0 sc1
G 168,216
Mvav 140,24,220
v_lshrrev_b32 v23, 1, v22
G 169,217
Mvaa 140,28,32
Ta 12,15,40960
v_and_b32 v23, 2, v23
Mvav 108,28,232
Ta 14,15,41984
G 170,218
Mvav 108,24,212
v_xor_b32 v20, v20, v23
G 171,219
Mvav 76,24,204
G 172,220
v_mul_u32_u24 v22, 80, v22
Mvav 76,28,236
Ta 16,24,32768
G 173,221
Mvav 44,28,228
Ta 18,24,33792
v_add_lshl_u32 v20, v20, v22, 4
Mvav 44,24,196
W 6
buffer_load_dwordx4 a[144:147], v27, s[8:11], s93 offen offset:0 sc1
G 174,222
Mvav 48,0,196
s_add_u32 s28, s28, 512
G 175,223
v_lshrrev_b32 v22, 6, v1
Mvav 48,4,228
s_cmp_eq_u32 s28, s4
s_cselect_b32 s93, 10240, 0
Ta 20,24,40960
Ta 22,24,41984
Mvav 80,4,236
s_sub_u32 s28, s28, s93
K 168,168,169
v_mul_u32_u24 v22, 320, v22
Mvav 80,0,204
s_add_u32 s93, s28, 20480
K 169,170,171
v_bfe_u32 v23, v1, 4, 2
Mvav 112,0,212
K 170,172,173
K 171,174,175
Mvav 112,4,232
Ta 24,25,32768
v_lshl_add_u32 v22, v23, 3, v22
Mvaa 144,4,32
Ta 26,25,33792
v_accvgpr_write_b32 a216, v168
Mvav 144,0,220
W 6
buffer_load_dwordx4 v[200:203], v26, s[8:11], s28 offen offset:0 sc1
v_bfe_u32 v23, v1, 8, 1
Mvav 148,8,220
v_accvgpr_write_b32 a217, v169
v_lshlrev_b32 v23, 5, v23
Mvaa 148,12,32
Ta 28,25,40960
v_accvgpr_write_b32 a218, v170
Mvav 116,12,232
Ta 30,25,41984
v_accvgpr_write_b32 a219, v171
Mvav 116,8,212
v_add_u32 v21, v22, v23
G 176,88
Mvav 84,8,204
v_sub_u32 v22, v22, v23
G 177,89
Mvav 84,12,236
s_mul_i32 s94, s59, 655360
Ta 0,10,49152
G 178,90
Mvav 52,12,228
s_mul_i32 s95, s58, 320
Ta 2,10,50176
G 179,91
Mvav 52,8,196
W 6
buffer_load_dwordx4 v[208:211], v26, s[8:11], s93 offen offset:0 sc1
G 180,92
Mvav 56,16,196
s_add_u32 s94, s94, s95
Ta 4,10,57344
Ta 6,10,58368
Mvav 56,20,228
G 181,93
G 182,94
Mvav 88,20,236
s_add_u32 s40, s26, s94
s_addc_u32 s41, s27, 0
G 183,95
Mvav 88,16,204
s_and_b32 s41, s41, 0xffff
Ta 8,11,49152
K 176,176,177
Mvav 120,16,212
s_mov_b32 s42, 5242880
Ta 10,11,50176
K 177,178,179
Mvav 120,20,232
s_mov_b32 s43, 0x20000
buffer_load_dwordx4 v[216:219], v27, s[8:11], s28 offen offset:0 sc1
K 178,180,181
Mvaa 152,20,32
s_sub_u32 s42, s42, s94
Ta 12,11,57344
K 179,182,183
Mvav 152,16,220
W 7
Ta 14,11,58368
v_accvgpr_write_b32 a220, v176
Mvav 156,24,220
s_mov_b64 s[44:45], s[8:9]
v_accvgpr_write_b32 a221, v177
v_accvgpr_write_b32 a222, v178
Mvaa 156,28,32
s_mov_b32 s47, 0x20000
Ta 16,12,49152
v_accvgpr_write_b32 a223, v179
Mvav 124,28,232
s_and_b32 s48, s14, 1
Ta 18,12,50176
buffer_load_dwordx4 v[224:227], v27, s[8:11], s93 offen offset:0 sc1
Mvav 124,24,212
G 168,224
G 169,225
Mvav 92,24,204
s_mul_i32 s48, s48, 5120
Ta 20,12,57344
Ta 22,12,58368
Mvav 92,28,236
s_lshr_b32 s51, s14, 1
G 170,226
G 171,227
Mvav 60,28,228
G 172,228
Mvav 60,24,196
s_waitcnt vmcnt(13) lgkmcnt(8)
s_and_b32 s94, s14, 1
G 173,229
Mvaa 32,0,36
s_add_u32 s28, s28, 512
Ta 24,13,49152
Ta 26,13,50176
Mvaa 32,4,52
s_cmp_eq_u32 s28, s4
s_cselect_b32 s93, 10240, 0
s_sub_u32 s28, s28, s93
G 174,230
Mvaa 64,4,56
s_add_u32 s93, s28, 20480
s_lshl_b32 s95, s57, 1
buffer_load_dwordx4 v[188:191], v26, s[8:11], s28 offen offset:0 sc1
Mvaa 64,0,40
s_add_u32 s94, s94, s95
Ta 28,13,57344
G 175,231
Mvaa 96,0,44
)"
R"(s_lshl_b32 s54, s94, 3
Ta 30,13,58368
K 168,168,169
Mvaa 96,4,184
K 169,170,171
K 170,172,173
Mvaa 128,4,188
s_add_u32 s54, s54, s59
K 171,174,175
Mvaa 128,0,48
W 8
Ta 0,14,49152
Ta 2,14,50176
Mvaa 132,8,48
s_lshl_b32 s54, s54, 6
buffer_load_dwordx4 v[196:199], v26, s[8:11], s93 offen offset:0 sc1
v_accvgpr_write_b32 a224, v168
Mvaa 132,12,188
s_xor_b32 s55, s54, 15872
v_accvgpr_write_b32 a225, v169
Mvaa 100,12,184
Ta 4,14,57344
v_accvgpr_write_b32 a226, v170
Mvaa 100,8,44
s_add_u32 s54, s54, 20480
Ta 6,14,58368
v_accvgpr_write_b32 a227, v171
Mvaa 68,8,40
s_and_b32 s81, s14, 1
G 176,96
Mvaa 68,12,56
s_add_u32 s55, s55, 20480
Mvaa 36,12,52
G 177,97
Mvaa 36,8,36
W 8
s_and_b32 s95, s14, 2
G 178,98
Mvaa 40,16,36
s_mul_i32 s91, s81, 17164288
Ta 8,15,49152
Ta 10,15,50176
Mvaa 40,20,52
s_lshl_b32 s94, s94, 2
buffer_load_dwordx4 v[204:207], v27, s[8:11], s28 offen offset:0 sc1
G 179,99
Mvaa 72,20,56
s_mul_i32 s82, s56, 536576
s_add_u32 s94, s94, s95
G 180,100
Mvaa 72,16,40
Ta 12,15,57344
Ta 14,15,58368
Mvaa 104,16,44
s_lshl_b32 s95, s95, 4
G 181,101
G 182,102
Mvaa 104,20,184
s_add_u32 s91, s91, s82
G 183,103
Mvaa 136,20,188
s_add_u32 s55, s55, s95
s_add_u32 s91, s91, 4194304
K 176,176,177
Mvaa 136,16,48
W 8
s_lshl_b32 s49, s94, 17
Ta 16,24,49152
Mvaa 140,24,48
Ta 18,24,50176
K 177,178,179
Mvaa 140,28,188
s_lshl_b32 s95, s59, 14
buffer_load_dwordx4 v[212:215], v27, s[8:11], s93 offen offset:0 sc1
K 178,180,181
Mvaa 108,28,184
s_lshr_b32 s82, s14, 1
Ta 20,24,57344
K 179,182,183
Mvaa 108,24,44
s_add_u32 s49, s49, s95
Ta 22,24,58368
v_accvgpr_write_b32 a228, v176
Mvaa 76,24,40
s_lshl_b32 s83, s82, 14
s_xor_b32 s50, s49, 16252928
Mvaa 76,28,56
v_accvgpr_write_b32 a229, v177
Mvaa 44,28,52
s_add_u32 s91, s91, s83
s_add_u32 s49, s49, 39845888
v_accvgpr_write_b32 a230, v178
Mvaa 44,24,36
W 8
Ta 24,25,49152
v_accvgpr_write_b32 a231, v179
Mvaa 48,0,36
s_add_u32 s50, s50, 39845888
Ta 26,25,50176
G 168,232
Mvaa 48,4,52
s_lshl_b32 s81, s81, 14
Mvaa 80,4,56
s_lshr_b32 s94, s15, 3
Ta 28,25,57344
G 169,233
Mvaa 80,0,40
s_lshl_b32 s82, s82, 16
Ta 30,25,58368
G 170,234
Mvaa 112,0,44
s_lshl_b32 s94, s94, 3
G 171,235
Mvaa 112,4,184
s_add_u32 s94, s94, s14
s_or_b32 s92, s81, s82
G 172,236
Mvaa 144,4,188
Mvaa 144,0,48
W 8
s_lshl_b32 s94, s94, 4
G 173,237
Mvaa 148,8,48
s_xor_b32 s92, s92, 0x10000
Ta 0,2,32768
Ta 2,2,33792
Mvaa 148,12,188
s_add_u32 s94, s94, s59
G 174,238
G 175,239
Mvaa 116,12,184
s_mul_i32 s52, s94, 5120
Ta 4,2,40960
Ta 6,2,41984
Mvaa 116,8,44
K 168,168,169
Mvaa 84,8,40
s_lshl_b32 s95, s59, 12
K 169,170,171
Mvaa 84,12,56
K 170,172,173
Mvaa 52,12,52
s_sub_u32 s52, s52, s95
K 171,174,175
Mvaa 52,8,36
W 8
s_add_u32 s52, s52, 56623104
Ta 8,3,32768
Mvaa 56,16,36
Ta 10,3,33792
v_accvgpr_write_b32 a232, v168
Mvaa 56,20,52
s_lshl_b32 s53, s94, 3
v_accvgpr_write_b32 a233, v169
Mvaa 88,20,56
Ta 12,3,40960
v_accvgpr_write_b32 a234, v170
Mvaa 88,16,40
s_add_u32 s53, s53, 65536
Ta 14,3,41984
v_accvgpr_write_b32 a235, v171
Mvaa 120,16,44
G 176,104
v_bfe_u32 v244, v1, 4, 1
Mvaa 120,20,184
Mvaa 152,20,188
G 177,105
v_mul_u32_u24 v244, 0xaaaa, v244
Mvaa 152,16,48
W 8
G 178,106
v_add_u32 v244, 0x4444, v244
Mvaa 156,24,48
Ta 16,4,32768
Ta 18,4,33792
Mvaa 156,28,188
G 179,107
v_lshl_or_b32 v240, v244, 16, v244
Mvaa 124,28,184
G 180,108
v_mov_b32 v241, 0xff800000
Mvaa 124,24,44
Ta 20,4,40960
Ta 22,4,41984
Mvaa 92,24,40
G 181,109
v_add_u32 v242, 0x10000, v1
Mvaa 92,28,56
G 182,110
v_bfe_u32 v244, v1, 4, 4
Mvaa 60,28,52
G 183,111
v_lshrrev_b32 v245, 8, v1
Mvaa 60,24,36
s_waitcnt vmcnt(8) lgkmcnt(8)
K 176,176,177
v_lshlrev_b32 v245, 2, v245
Mvaa 32,0,60
Ta 24,5,32768
Ta 26,5,33792
Mvaa 32,4,132
K 177,178,179
v_sub_u32 v244, v244, v245
Mvaa 64,4,136
K 178,180,181
v_cmp_le_i32_e64 s[72:73], 0, v244
Mvav 64,0,184
Ta 28,5,40960
K 179,182,183
Mvav 96,0,192
Ta 30,5,41984
v_accvgpr_write_b32 a236, v176
Mvaa 96,4,140
v_cmp_le_i32_e64 s[74:75], 1, v244
v_accvgpr_write_b32 a237, v177
Mvaa 128,4,144
v_cmp_le_i32_e64 s[76:77], 2, v244
v_accvgpr_write_b32 a238, v178
Mvaa 128,0,128
W 8
v_cmp_le_i32_e64 s[78:79], 3, v244
v_accvgpr_write_b32 a239, v179
Mvaa 132,8,128
Ta 0,6,32768
Ta 2,6,33792
Mvaa 132,12,144
v_add_u32 v243, 4096, v1
G 168,240
Mvaa 100,12,140
v_add_u32 v244, 8192, v1
G 169,241
Mvav 100,8,192
Ta 4,6,40960
v_add_u32 v245, 12288, v1
Mvav 68,8,184
s_mov_b32 s18, 0x3f803f80
Ta 6,6,41984
G 170,242
Mvaa 68,12,136
s_mov_b32 s19, 0x3f003f00
G 171,243
G 172,244
Mvaa 36,12,132
Mvaa 36,8,60
W 8
v_accvgpr_write_b32 a176, s18
G 173,245
Mvaa 40,16,60
Ta 8,7,32768
Ta 10,7,33792
Mvaa 40,20,132
v_accvgpr_write_b32 a180, s19
G 174,246
Mvaa 72,20,136
G 175,247
v_mov_b32 v252, v241
Mvav 72,16,184
Ta 12,7,40960
Ta 14,7,41984
Mvav 104,16,192
K 168,168,169
v_accvgpr_write_b32 a177, s18
Mvaa 104,20,140
K 169,170,171
v_accvgpr_write_b32 a181, s19
Mvaa 136,20,144
K 170,172,173
v_mov_b32 v253, v241
Mvaa 136,16,128
W 8
K 171,174,175
v_accvgpr_write_b32 a178, s18
Mvaa 140,24,128
Ta 16,8,32768
Ta 18,8,33792
Mvaa 140,28,144
v_accvgpr_write_b32 a240, v168
v_accvgpr_write_b32 a182, s19
Mvaa 108,28,140
v_accvgpr_write_b32 a241, v169
v_mov_b32 v254, v241
Mvav 108,24,192
Ta 20,8,40960
v_accvgpr_write_b32 a242, v170
Mvav 76,24,184
Ta 22,8,41984
v_accvgpr_write_b32 a179, s18
Mvaa 76,28,136
v_accvgpr_write_b32 a243, v171
v_accvgpr_write_b32 a183, s19
Mvaa 44,28,132
G 176,112
v_mov_b32 v255, v241
Mvaa 44,24,60
W 8
G 177,113
G 178,114
Mvaa 48,0,60
Ta 24,9,32768
v_accvgpr_write_b32 a160, 0
Mvaa 48,4,132
Ta 26,9,33792
G 179,115
Mvaa 80,4,136
v_accvgpr_write_b32 a161, 0
G 180,116
Mvav 80,0,184
Ta 28,9,40960
v_accvgpr_write_b32 a162, 0
Mvav 112,0,192
Ta 30,9,41984
G 181,117
Mvaa 112,4,140
v_accvgpr_write_b32 a163, 0
G 182,118
Mvaa 144,4,144
v_accvgpr_write_b32 a164, 0
G 183,119
Mvaa 144,0,128
W 8
v_accvgpr_write_b32 a165, 0
K 176,176,177
Mvaa 148,8,128
Ta 0,2,49152
v_accvgpr_write_b32 a166, 0
Mvaa 148,12,144
Ta 2,2,50176
K 177,178,179
Mvaa 116,12,140
v_accvgpr_write_b32 a167, 0
K 178,180,181
Mvav 116,8,192
v_accvgpr_write_b32 a168, 0
K 179,182,183
Mvav 84,8,184
Ta 4,2,57344
Ta 6,2,58368
Mvaa 84,12,136
v_accvgpr_write_b32 a169, 0
v_accvgpr_write_b32 a244, v176
Mvaa 52,12,132
v_accvgpr_write_b32 a245, v177
v_accvgpr_write_b32 a170, 0
Mvaa 52,8,60
W 8
v_accvgpr_write_b32 a246, v178
v_accvgpr_write_b32 a171, 0
Mvaa 56,16,60
Ta 8,3,49152
v_accvgpr_write_b32 a247, v179
Mvaa 56,20,132
Ta 10,3,50176
v_accvgpr_write_b32 a172, 0
Mvaa 88,20,136
G 168,248
v_accvgpr_write_b32 a173, 0
Mvav 88,16,184
G 169,249
v_accvgpr_write_b32 a174, 0
Mvav 120,16,192
Ta 12,3,57344
Ta 14,3,58368
Mvaa 120,20,140
G 170,250
v_accvgpr_write_b32 a175, 0
Mvaa 152,20,144
s_mov_b32 s90, 0x5d800000
G 171,251
G 172,252
Mvaa 152,16,128
W 8
s_mov_b64 s[88:89], 0
G 173,253
Mvaa 156,24,128
s_mov_b32 s64, 0
Ta 16,4,49152
Ta 18,4,50176
Mvaa 156,28,144
s_and_b32 s18, s14, 1
G 174,254
G 175,255
Mvaa 124,28,140
s_lshl_b32 s62, s18, 14
Ta 20,4,57344
Ta 22,4,58368
Mvav 124,24,192
K 168,168,169
Mvav 92,24,184
s_lshl_b32 s20, s57, 15
K 169,170,171
Mvaa 92,28,136
s_sub_u32 s20, 507904, s20
K 170,172,173
Mvaa 60,28,132
K 171,174,175
Mvaa 60,24,60
s_waitcnt vmcnt(0) lgkmcnt(8)
s_add_u32 s20, s20, s62
Ta 24,5,49152
Mvav 32,0,200
)"
R"(s_lshl_b32 s65, s57, 1
Ta 26,5,50176
v_accvgpr_write_b32 a248, v168
Mvav 32,4,188
s_sub_u32 s65, s65, 1
v_accvgpr_write_b32 a249, v169
Mvav 64,4,196
Ta 28,5,57344
v_accvgpr_write_b32 a250, v170
Mvav 64,0,208
s_max_i32 s65, s65, 0
Ta 30,5,58368
v_accvgpr_write_b32 a251, v171
Mvav 96,0,216
s_sub_u32 s66, s60, 1
G 176,120
Mvav 96,4,204
s_cmp_lt_u32 s14, 2
s_cselect_b32 s20, s20, 0
s_cselect_b32 s66, s66, 15
s_cselect_b32 s21, 32768, 0
Mvav 128,4,212
G 177,121
Mvav 128,0,224
W 8
s_add_u32 s61, s91, s20
G 178,122
Mvav 132,8,224
s_add_u32 s62, s62, s21
Ta 0,6,49152
Ta 2,6,50176
Mvav 132,12,212
G 179,123
G 180,124
Mvav 100,12,204
s_sub_u32 s63, 16384, s21
Ta 4,6,57344
Ta 6,6,58368
Mvav 100,8,216
s_lshl_b32 s67, s60, 7
G 181,125
Mvav 68,8,208
s_add_u32 s67, s67, s56
G 182,126
Mvav 68,12,196
G 183,127
Mvav 36,12,188
s_lshl_b32 s67, s67, 12
K 176,176,177
Mvav 36,8,200
W 8
s_add_u32 s67, s67, 39845888
Ta 8,7,49152
Mvav 40,16,200
s_lshl_b32 s69, s60, 3
Ta 10,7,50176
K 177,178,179
Mvav 40,20,188
K 178,180,181
Mvav 72,20,196
s_add_u32 s69, s69, s59
Ta 12,7,57344
K 179,182,183
Mvav 72,16,208
s_lshl_b32 s69, s69, 6
Ta 14,7,58368
v_accvgpr_write_b32 a252, v176
Mvav 104,16,216
Mvav 104,20,204
s_lshl_b32 s21, s58, 3
v_accvgpr_write_b32 a253, v177
Mvav 136,20,212
s_add_u32 s69, s69, s21
v_accvgpr_write_b32 a254, v178
Mvav 136,16,224
W 8
s_add_u32 s69, s69, 20480
v_accvgpr_write_b32 a255, v179
Mvav 140,24,224
Ta 16,8,49152
Ta 18,8,50176
Mvav 140,28,212
s_mov_b32 s70, 0
Mvav 108,28,204
s_mov_b32 s37, 0
Ta 20,8,57344
Ta 22,8,58368
Mvav 108,24,216
s_sub_u32 s18, s57, 4
Mvav 76,24,208
Mvav 76,28,196
s_lshl_b32 s18, s18, 2
Mvav 44,28,188
s_add_u32 s18, s18, s58
Mvav 44,24,200
W 8
Ta 24,9,49152
Ta 26,9,50176
Mvav 48,0,200
s_mul_i32 s85, s59, 7
Mvav 48,4,188
s_add_u32 s85, s85, s18
Mvav 80,4,196
s_cmp_lt_u32 s18, 7
s_cselect_b32 s85, s85, 99
Ta 28,9,57344
Ta 30,9,58368
Mvav 80,0,208
Mvav 112,0,216
s_cmp_lt_u32 s85, 54
s_cselect_b32 s65, 2, s65
Mvav 112,4,204
s_mov_b32 s68, 0
Mvav 144,4,212
s_mov_b64 s[24:25], -1
Mvav 144,0,224
W 8
Mvav 148,8,224
s_mov_b32 s22, 0x3e0293ee
Mvav 148,12,212
s_mov_b32 s23, 0x3e0293ee
Mvav 116,12,204
Mvav 116,8,216
Mvav 84,8,208
Mvav 84,12,196
Mvav 52,12,188
Mvav 52,8,200
W 4
Mvav 56,16,200
Mvav 56,20,188
Mvav 88,20,196
Mvav 88,16,208
Mvav 120,16,216
W 0
s_barrier
Ra 128,242,0
Mvav 120,20,204
Mvav 152,20,212
Ra 132,242,1024
Mvav 152,16,224
Mvav 156,24,224
Ra 136,242,2048
Mvav 156,28,212
Mvav 124,28,204
Ra 140,242,3072
Mvav 124,24,216
Mvav 92,24,208
Mvav 92,28,196
Mvav 60,28,188
Mvav 60,24,200
.Lago%=:
s_cmp_eq_u32 s60, 1
s_cbranch_scc1 .Lpe_48%=
s_cmp_eq_u32 s60, 0
s_cbranch_scc1 .Lpd_32%=
v_pk_mul_f32 v[32:33], v[32:33], s[22:23]
v_pk_mul_f32 v[34:35], v[34:35], s[22:23]
v_pk_mul_f32 v[36:37], v[36:37], s[22:23]
v_pk_mul_f32 v[38:39], v[38:39], s[22:23]
v_pk_mul_f32 v[64:65], v[64:65], s[22:23]
v_pk_mul_f32 v[66:67], v[66:67], s[22:23]
v_pk_mul_f32 v[68:69], v[68:69], s[22:23]
v_pk_mul_f32 v[70:71], v[70:71], s[22:23]
v_pk_mul_f32 v[96:97], v[96:97], s[22:23]
v_pk_mul_f32 v[98:99], v[98:99], s[22:23]
v_pk_mul_f32 v[100:101], v[100:101], s[22:23]
v_pk_mul_f32 v[102:103], v[102:103], s[22:23]
v_pk_mul_f32 v[128:129], v[128:129], s[22:23]
v_pk_mul_f32 v[130:131], v[130:131], s[22:23]
v_pk_mul_f32 v[132:133], v[132:133], s[22:23]
v_pk_mul_f32 v[134:135], v[134:135], s[22:23]
v_pk_mul_f32 v[40:41], v[40:41], s[22:23]
v_pk_mul_f32 v[42:43], v[42:43], s[22:23]
v_pk_mul_f32 v[44:45], v[44:45], s[22:23]
v_pk_mul_f32 v[46:47], v[46:47], s[22:23]
v_pk_mul_f32 v[72:73], v[72:73], s[22:23]
v_pk_mul_f32 v[74:75], v[74:75], s[22:23]
v_pk_mul_f32 v[76:77], v[76:77], s[22:23]
v_pk_mul_f32 v[78:79], v[78:79], s[22:23]
v_pk_mul_f32 v[104:105], v[104:105], s[22:23]
v_pk_mul_f32 v[106:107], v[106:107], s[22:23]
v_pk_mul_f32 v[108:109], v[108:109], s[22:23]
v_pk_mul_f32 v[110:111], v[110:111], s[22:23]
v_pk_mul_f32 v[136:137], v[136:137], s[22:23]
v_pk_mul_f32 v[138:139], v[138:139], s[22:23]
v_pk_mul_f32 v[140:141], v[140:141], s[22:23]
v_pk_mul_f32 v[142:143], v[142:143], s[22:23]
v_pk_mul_f32 v[48:49], v[48:49], s[22:23]
v_pk_mul_f32 v[50:51], v[50:51], s[22:23]
v_pk_mul_f32 v[52:53], v[52:53], s[22:23]
v_pk_mul_f32 v[54:55], v[54:55], s[22:23]
v_pk_mul_f32 v[80:81], v[80:81], s[22:23]
v_pk_mul_f32 v[82:83], v[82:83], s[22:23]
v_pk_mul_f32 v[84:85], v[84:85], s[22:23]
v_pk_mul_f32 v[86:87], v[86:87], s[22:23]
v_pk_mul_f32 v[112:113], v[112:113], s[22:23]
v_pk_mul_f32 v[114:115], v[114:115], s[22:23]
v_pk_mul_f32 v[116:117], v[116:117], s[22:23]
v_pk_mul_f32 v[118:119], v[118:119], s[22:23]
v_pk_mul_f32 v[144:145], v[144:145], s[22:23]
v_pk_mul_f32 v[146:147], v[146:147], s[22:23]
v_pk_mul_f32 v[148:149], v[148:149], s[22:23]
v_pk_mul_f32 v[150:151], v[150:151], s[22:23]
v_pk_mul_f32 v[56:57], v[56:57], s[22:23]
v_pk_mul_f32 v[58:59], v[58:59], s[22:23]
v_pk_mul_f32 v[60:61], v[60:61], s[22:23]
v_pk_mul_f32 v[62:63], v[62:63], s[22:23]
v_pk_mul_f32 v[88:89], v[88:89], s[22:23]
v_pk_mul_f32 v[90:91], v[90:91], s[22:23]
v_pk_mul_f32 v[92:93], v[92:93], s[22:23]
v_pk_mul_f32 v[94:95], v[94:95], s[22:23]
v_pk_mul_f32 v[120:121], v[120:121], s[22:23]
v_pk_mul_f32 v[122:123], v[122:123], s[22:23]
v_pk_mul_f32 v[124:125], v[124:125], s[22:23]
v_pk_mul_f32 v[126:127], v[126:127], s[22:23]
v_pk_mul_f32 v[152:153], v[152:153], s[22:23]
v_pk_mul_f32 v[154:155], v[154:155], s[22:23]
v_pk_mul_f32 v[156:157], v[156:157], s[22:23]
v_pk_mul_f32 v[158:159], v[158:159], s[22:23]
K 32,32,33
K 33,34,35
K 34,36,37
K 35,38,39
K 40,40,41
K 41,42,43
K 42,44,45
K 43,46,47
W 0
Zvav 196,128,32
s_add_u32 s36, s29, 65536
Ra 144,242,4096
Ra 148,242,5120
Ra 152,242,6144
Ra 156,242,7168
Mvav 196,132,40
K 64,64,65
K 65,66,67
K 66,68,69
K 67,70,71
K 72,72,73
K 73,74,75
K 74,76,77
K 75,78,79
s_nop 1
Zvav 204,132,72
buffer_store_dwordx4 a[192:195], v1, s[8:11], s36 offen offset:0 sc1
K 96,96,97
K 97,98,99
K 98,100,101
Mvav 204,128,64
K 99,102,103
K 104,104,105
K 105,106,107
K 106,108,109
K 107,110,111
Zvav 212,128,96
buffer_store_dwordx4 a[200:203], v1, s[8:11], s36 offen offset:1024 sc1
K 128,128,129
K 129,130,131
K 130,132,133
Mvav 212,132,104
K 131,134,135
K 136,136,137
K 137,138,139
K 138,140,141
K 139,142,143
s_nop 1
Zvav 220,132,136
K 48,48,49
K 49,50,51
K 50,52,53
K 51,54,55
Mvav 220,128,128
K 56,56,57
K 57,58,59
K 58,60,61
K 59,62,63
Mvav 196,136,48
buffer_store_dwordx4 a[208:211], v1, s[8:11], s36 offen offset:2048 sc1
Ra 128,242,8192
Ra 132,242,9216
K 80,80,81
Mvav 196,140,56
K 81,82,83
K 82,84,85
K 83,86,87
K 88,88,89
K 89,90,91
K 90,92,93
K 91,94,95
s_nop 1
Mvav 204,140,88
buffer_store_dwordx4 a[216:219], v1, s[8:11], s36 offen offset:3072 sc1
s_add_u32 s36, s29, 69632
K 112,112,113
K 113,114,115
K 114,116,117
Mvav 204,136,80
K 115,118,119
K 120,120,121
K 121,122,123
K 122,124,125
K 123,126,127
Mvav 212,136,112
K 144,144,145
K 145,146,147
K 146,148,149
K 147,150,151
Mvav 212,140,120
K 152,152,153
K 153,154,155
K 154,156,157
K 155,158,159
s_nop 1
Mvav 220,140,152
buffer_store_dwordx4 a[224:227], v1, s[8:11], s36 offen offset:0 sc1
v_accvgpr_write_b32 a0, v32
v_accvgpr_write_b32 a1, v33
v_accvgpr_write_b32 a2, v34
Mvav 220,136,144
W 2
Ra 136,242,10240
v_accvgpr_write_b32 a3, v35
v_accvgpr_write_b32 a4, v40
Zvav 36,144,32
Ra 140,242,11264
buffer_store_dwordx4 a[232:235], v1, s[8:11], s36 offen offset:1024 sc1
v_accvgpr_write_b32 a5, v41
Mvav 36,148,40
v_accvgpr_write_b32 a6, v42
v_accvgpr_write_b32 a7, v43
v_max3_f32 v4, v196, v197, v198
v_max3_f32 v4, v4, v199, v252
Zvav 68,148,72
v_max3_f32 v5, v204, v205, v206
v_max3_f32 v5, v5, v207, v253
v_max3_f32 v6, v212, v213, v214
v_max3_f32 v6, v6, v215, v254
Mvav 68,144,64
v_max3_f32 v7, v220, v221, v222
v_max3_f32 v7, v7, v223, v255
v_mov_b32 v8, v4
v_mov_b32 v9, v5
Zvav 100,144,96
v_mov_b32 v10, v6
v_mov_b32 v11, v7
v_permlane32_swap_b32 v4, v8
v_accvgpr_write_b32 a8, v48
Mvav 100,148,104
v_permlane32_swap_b32 v5, v9
v_permlane32_swap_b32 v6, v10
v_permlane32_swap_b32 v7, v11
v_max_f32 v4, v4, v8
Zvav 132,148,136
v_max_f32 v5, v5, v9
v_max_f32 v6, v6, v10
v_max_f32 v7, v7, v11
v_mov_b32 v8, v4
Mvav 132,144,128
v_mov_b32 v9, v5
v_mov_b32 v10, v6
v_mov_b32 v11, v7
v_accvgpr_write_b32 a9, v49
Mvav 36,152,48
v_permlane16_swap_b32 v4, v8
v_permlane16_swap_b32 v5, v9
v_permlane16_swap_b32 v6, v10
v_permlane16_swap_b32 v7, v11
Mvav 36,156,56
v_max_f32 v4, v4, v8
v_max_f32 v5, v5, v9
v_max_f32 v6, v6, v10
v_max_f32 v7, v7, v11
Mvav 68,156,88
v_sub_f32 v4, 0, v4
v_sub_f32 v5, 0, v5
v_sub_f32 v6, 0, v6
v_sub_f32 v7, 0, v7
Mvav 68,152,80
v_mov_b32 v224, v4
v_mov_b32 v225, v4
v_mov_b32 v226, v4
v_mov_b32 v227, v4
Mvav 100,152,112
v_mov_b32 v228, v5
v_mov_b32 v229, v5
v_mov_b32 v230, v5
v_mov_b32 v231, v5
Mvav 100,156,120
v_mov_b32 v232, v6
v_mov_b32 v233, v6
v_mov_b32 v234, v6
v_mov_b32 v235, v6
Mvav 132,156,152
v_mov_b32 v236, v7
v_mov_b32 v237, v7
v_mov_b32 v238, v7
v_mov_b32 v239, v7
Mvav 132,152,144
W 0
)"
R"(buffer_store_dwordx4 a[240:243], v1, s[8:11], s36 offen offset:2048 sc1
v_accvgpr_write_b32 a10, v50
v_accvgpr_write_b32 a11, v51
v_accvgpr_write_b32 a12, v56
Cvav 160,128,32,224
s_and_b32 s80, s14, 1
v_accvgpr_write_b32 a13, v57
v_accvgpr_write_b32 a14, v58
v_accvgpr_write_b32 a15, v59
v_accvgpr_write_b32 a16, v64
Mvav 160,132,40
buffer_store_dwordx4 a[248:251], v1, s[8:11], s36 offen offset:3072 sc1
s_add_u32 s36, s29, 393216
Ra 144,242,12288
Ra 148,242,13312
v_accvgpr_write_b32 a17, v65
Cvav 164,132,72,228
s_lshl_b32 s80, s80, 14
v_accvgpr_write_b32 a18, v66
v_accvgpr_write_b32 a19, v67
v_accvgpr_write_b32 a20, v72
v_accvgpr_write_b32 a21, v73
Mvav 164,128,64
s_add_u32 s80, s80, 98304
buffer_store_dwordx4 a[196:199], v1, s[8:11], s36 offen offset:0 sc1
v_accvgpr_write_b32 a22, v74
v_accvgpr_write_b32 a23, v75
v_accvgpr_write_b32 a24, v80
Cvav 168,128,96,232
v_accvgpr_write_b32 a25, v81
v_accvgpr_write_b32 a26, v82
A 196,196,224
v_accvgpr_write_b32 a27, v83
Mvav 168,132,104
buffer_store_dwordx4 a[204:207], v1, s[8:11], s36 offen offset:1024 sc1
A 197,197,224
A 198,198,224
v_accvgpr_write_b32 a28, v88
Cvav 172,132,136,236
Ra 152,242,14336
Ra 156,242,15360
A 199,199,224
v_accvgpr_write_b32 a29, v89
Mvav 172,128,128
buffer_store_dwordx4 a[212:215], v1, s[8:11], s36 offen offset:2048 sc1
X 196
v_accvgpr_write_b32 a30, v90
Mvav 160,136,48
X 197
v_accvgpr_write_b32 a31, v91
v_accvgpr_write_b32 a32, v96
Mvav 160,140,56
X 198
K 192,196,197
v_accvgpr_write_b32 a33, v97
Mvav 164,140,88
X 199
A 204,204,228
v_accvgpr_write_b32 a34, v98
Mvav 164,136,80
buffer_store_dwordx4 a[220:223], v1, s[8:11], s36 offen offset:3072 sc1
s_add_u32 s36, s29, 397312
K 193,198,199
v_accvgpr_write_b32 a35, v99
A 205,205,228
Mvav 168,136,112
A 206,206,228
v_accvgpr_write_b32 a36, v104
v_add_u32 v8, s80, v1
A 207,207,228
Mvav 168,140,120
X 204
v_accvgpr_write_b32 a37, v105
v_accvgpr_write_b32 a38, v106
Mvav 172,140,152
X 205
X 206
Mvav 172,136,144
W 0
buffer_store_dwordx4 a[228:231], v1, s[8:11], s36 offen offset:0 sc1
v_accvgpr_write_b32 a39, v107
v_accvgpr_write_b32 a40, v112
K 200,204,205
Cvav 176,144,32,224
Ra 128,242,24576
X 207
v_accvgpr_write_b32 a41, v113
Mvav 176,148,40
Ra 132,242,25600
A 212,212,232
v_accvgpr_write_b32 a42, v114
K 201,206,207
Cvav 180,148,72,228
A 213,213,232
v_accvgpr_write_b32 a43, v115
A 214,214,232
A 215,215,232
Mvav 180,144,64
buffer_store_dwordx4 a[236:239], v1, s[8:11], s36 offen offset:1024 sc1
v_accvgpr_write_b32 a44, v120
X 212
Cvav 184,144,96,232
v_accvgpr_write_b32 a45, v121
X 213
v_accvgpr_write_b32 a46, v122
Mvav 184,148,104
X 214
v_accvgpr_write_b32 a47, v123
K 208,212,213
Cvav 188,148,136,236
buffer_store_dwordx4 a[244:247], v1, s[8:11], s36 offen offset:2048 sc1
v_accvgpr_write_b32 a48, v128
X 215
Mvav 188,144,128
v_accvgpr_write_b32 a49, v129
A 220,220,236
v_accvgpr_write_b32 a50, v130
K 209,214,215
Mvav 176,152,48
A 221,221,236
v_accvgpr_write_b32 a51, v131
A 222,222,236
A 223,223,236
Mvav 176,156,56
Ra 136,242,26624
v_accvgpr_write_b32 a52, v136
X 220
Mvav 180,156,88
Ra 140,242,27648
v_accvgpr_write_b32 a53, v137
X 221
Mvav 180,152,80
buffer_store_dwordx4 a[252:255], v1, s[8:11], s36 offen offset:3072 sc1
v_accvgpr_write_b32 a54, v138
X 222
Mvav 184,152,112
v_accvgpr_write_b32 a55, v139
v_accvgpr_write_b32 a56, v144
v_accvgpr_write_b32 a57, v145
v_accvgpr_write_b32 a58, v146
v_accvgpr_write_b32 a59, v147
v_accvgpr_write_b32 a60, v152
v_accvgpr_write_b32 a61, v153
v_accvgpr_write_b32 a62, v154
v_accvgpr_write_b32 a63, v155
Mvav 184,156,120
s_barrier
K 216,220,221
X 223
A 36,36,224
Mvav 188,156,152
K 217,222,223
A 37,37,224
A 38,38,224
A 39,39,224
X 36
X 37
X 38
K 194,36,37
X 39
A 68,68,228
K 195,38,39
A 69,69,228
A 70,70,228
A 71,71,228
X 68
X 69
X 70
K 202,68,69
X 71
A 100,100,232
K 203,70,71
A 101,101,232
A 102,102,232
A 103,103,232
X 100
X 101
X 102
K 210,100,101
X 103
A 132,132,236
K 211,102,103
A 133,133,236
A 134,134,236
A 135,135,236
X 132
X 133
X 134
K 218,132,133
X 135
s_nop 0
K 219,134,135
Mvav 188,152,144
W 0
Ra 144,242,28672
Ra 148,242,29696
Ra 152,242,30720
Zvav 32,128,192
Ra 156,242,31744
Zvav 64,128,200
Zvav 96,128,208
s_and_b32 s80, s14, 1
s_xor_b32 s62, s62, 0x10000
Zvav 128,128,216
s_lshl_b32 s80, s80, 14
s_add_u32 s80, s80, 131072
v_add_u32 v23, 61440, v21
Zvav 132,132,216
v_add_u32 v12, s80, v1
Ra 64,12,8192
Ra 68,12,9216
v_add_u32 v24, 61440, v22
Zvav 100,132,208
Ra 72,12,10240
Ra 76,12,11264
Ra 80,12,12288
v_add_u32 v25, 122880, v21
Zvav 68,132,200
Ra 84,12,13312
Ra 88,12,14336
Ra 92,12,15360
v_add_u32 v26, 122880, v22
Zvav 36,132,192
Ra 96,12,0
Ra 100,12,1024
Ra 104,12,2048
Zvav 40,136,192
Ra 108,12,3072
Ra 112,12,4096
X 172
Zvav 72,136,200
Ra 116,12,5120
Ra 120,12,6144
X 173
Zvav 104,136,208
Ra 124,12,7168
X 174
K 220,172,173
Zvav 136,136,216
X 175
Zvav 140,140,216
X 168
K 221,174,175
Zvav 108,140,208
X 169
X 170
Zvav 76,140,200
s_waitcnt vmcnt(0) lgkmcnt(0)
s_barrier
v_xor_b32 v242, 0x10000, v242
ds_read_b128 v[248:251], v8
Zvav 44,140,192
s_add_u32 s80, s71, 128
v_mov_b64 v[28:29], s[12:13]
s_mov_b64 exec, 1
buffer_store_dwordx2 v[28:29], off, s[8:11], s80 sc1
s_mov_b64 exec, -1
Ra 128,242,0
K 212,168,169
X 171
Zvav 48,144,192
X 164
K 213,170,171
Zvav 80,144,200
X 165
X 166
Zvav 112,144,208
Ra 132,242,1024
K 204,164,165
X 167
Zvav 144,144,216
X 160
Zvav 148,148,216
K 205,166,167
X 161
Zvav 116,148,208
Ra 136,242,2048
X 162
Zvav 84,148,200
W 3
K 196,160,161
X 163
Zvav 52,148,192
X 176
K 197,162,163
Zvav 56,152,192
Ra 140,242,3072
X 177
Zvav 88,152,200
X 178
K 198,176,177
Zvav 120,152,208
X 179
X 180
Zvav 152,152,216
K 199,178,179
Zvav 156,156,216
X 181
X 182
Zvav 124,156,208
K 206,180,181
X 183
Zvav 92,156,200
K 207,182,183
Zvav 60,156,192
.Ll0t_46%=:
s_cmp_eq_u32 s64, s65
s_cbranch_scc1 .Ll1t_35%=
.Ll0_33%=:
W 0
Cvaa 160,128,0,224
Ra 144,242,4096
Ra 148,242,5120
Mvaa 160,132,4
s_xor_b32 s62, s62, 0x10000
Ra 152,242,6144
Cvaa 164,132,20,228
X 184
Mvaa 164,128,16
X 185
Cvaa 168,128,32,232
X 186
Mvaa 168,132,36
Ra 156,242,7168
K 214,184,185
Cvaa 172,132,52,236
X 187
Mvaa 172,128,48
X 188
Mvaa 160,136,8
Ra 128,242,16384
Mvaa 160,140,12
K 215,186,187
Mvaa 164,140,28
X 189
Mvaa 164,136,24
X 190
Mvaa 168,136,40
Ra 132,242,17408
Mvaa 168,140,44
K 222,188,189
Mvaa 172,140,60
X 191
Mvaa 172,136,56
K 223,190,191
v_smfmac_f32_16x16x64_bf16 a[160:163], a[176:179], v[192:199], v240
Ra 136,242,18432
v_smfmac_f32_16x16x64_bf16 a[164:167], a[176:179], v[200:207], v240
X 160
v_smfmac_f32_16x16x64_bf16 a[168:171], a[176:179], v[208:215], v240
X 161
v_smfmac_f32_16x16x64_bf16 a[172:175], a[176:179], v[216:223], v240
W 3
X 162
Cvaa 176,144,0,224
Ra 140,242,19456
Mvaa 176,148,4
K 192,160,161
Cvaa 180,148,20,228
X 163
Mvaa 180,144,16
X 164
Cvaa 184,144,32,232
Mvaa 184,148,36
K 193,162,163
Cvaa 188,148,52,236
X 165
Mvaa 188,144,48
X 166
Mvaa 176,152,8
Ra 144,242,20480
Ra 148,242,21504
Mvaa 176,156,12
K 200,164,165
Mvaa 180,156,28
X 167
Mvaa 180,152,24
X 168
Mvaa 184,152,40
Mvaa 184,156,44
K 201,166,167
Mvaa 188,156,60
X 169
Mvaa 188,152,56
W 2
Ra 152,242,22528
Mvav 32,128,196
X 170
Mvav 64,128,204
Ra 156,242,23552
K 208,168,169
Mvav 96,128,212
X 171
Mvav 128,128,220
Mvav 132,132,220
X 172
Mvav 100,132,212
s_add_u32 s64, s64, 1
Ra 128,242,8192
K 209,170,171
)"
R"(Mvav 68,132,204
X 173
Mvav 36,132,196
Mvav 40,136,196
X 174
Mvav 72,136,204
Ra 132,242,9216
K 216,172,173
Mvav 104,136,212
X 175
Mvav 136,136,220
Mvav 140,140,220
X 176
Mvav 108,140,212
Ra 136,242,10240
K 217,174,175
Mvav 76,140,204
X 177
Mvav 44,140,196
W 3
Mvav 48,144,196
X 178
Mvav 80,144,204
Ra 140,242,11264
Mvav 112,144,212
K 194,176,177
Mvav 144,144,220
X 179
Mvav 148,148,220
X 180
Mvav 116,148,212
Ra 144,242,12288
Mvav 84,148,204
K 195,178,179
Mvav 52,148,196
X 181
Mvav 56,152,196
X 182
Mvav 88,152,204
Ra 148,242,13312
Mvav 120,152,212
K 202,180,181
Mvav 152,152,220
X 183
Mvav 156,156,220
X 184
Mvav 124,156,212
Ra 152,242,14336
Mvav 92,156,204
K 203,182,183
Mvav 60,156,196
W 3
X 185
Cvaa 160,128,0,224
X 186
Mvaa 160,132,4
Ra 156,242,15360
Cvaa 164,132,20,228
K 210,184,185
Mvaa 164,128,16
X 187
Cvaa 168,128,32,232
X 188
Mvaa 168,132,36
Cvaa 172,132,52,236
K 211,186,187
Mvaa 172,128,48
X 189
Mvaa 160,136,8
Ra 128,242,24576
Ra 132,242,25600
Mvaa 160,140,12
X 190
Mvaa 164,140,28
K 218,188,189
Mvaa 164,136,24
X 191
Mvaa 168,136,40
Mvaa 168,140,44
Mvaa 172,140,60
X 160
Mvaa 172,136,56
W 2
Ra 136,242,26624
K 219,190,191
Cvaa 176,144,0,224
X 161
Mvaa 176,148,4
Ra 140,242,27648
Cvaa 180,148,20,228
X 162
Mvaa 180,144,16
K 196,160,161
Cvaa 184,144,32,232
X 163
Mvaa 184,148,36
X 164
Cvaa 188,148,52,236
K 197,162,163
Mvaa 188,144,48
X 165
Mvaa 176,152,8
Ra 144,242,28672
Ra 148,242,29696
Mvaa 176,156,12
X 166
Mvaa 180,156,28
K 204,164,165
Mvaa 180,152,24
X 167
Mvaa 184,152,40
Mvaa 184,156,44
X 168
Mvaa 188,156,60
Mvaa 188,152,56
W 2
Ra 152,242,30720
K 205,166,167
Mvav 32,128,192
X 169
Mvav 64,128,200
Ra 156,242,31744
Mvav 96,128,208
X 170
Mvav 128,128,216
K 212,168,169
Mvav 132,132,216
X 171
Mvav 100,132,208
X 172
Mvav 68,132,200
Mvav 36,132,192
K 213,170,171
Mvav 40,136,192
X 173
Mvav 72,136,200
X 174
Mvav 104,136,208
Mvav 136,136,216
K 220,172,173
Mvav 140,140,216
X 175
Mvav 108,140,208
X 176
Mvav 76,140,200
W 0
s_barrier
v_xor_b32 v242, 0x10000, v242
Mvav 44,140,192
Ra 128,242,0
K 221,174,175
Mvav 48,144,192
X 177
Mvav 80,144,200
X 178
Mvav 112,144,208
Ra 132,242,1024
Mvav 144,144,216
K 198,176,177
Mvav 148,148,216
X 179
Mvav 116,148,208
X 180
Mvav 84,148,200
Ra 136,242,2048
Mvav 52,148,192
K 199,178,179
Mvav 56,152,192
X 181
Mvav 88,152,200
Ra 140,242,3072
Mvav 120,152,208
X 182
Mvav 152,152,216
K 206,180,181
Mvav 156,156,216
X 183
Mvav 124,156,208
Mvav 92,156,200
K 207,182,183
Mvav 60,156,192
s_cmp_lg_u32 s64, s65
s_cbranch_scc1 .Ll0_33%=
s_cmp_lt_u32 s85, 54
s_cbranch_scc1 .Lmoe_44%=
.Ll1t_35%=:
s_cmp_eq_u32 s64, s66
s_cbranch_scc1 .Lsp1_36%=
.Ll1_34%=:
W 0
Cvaa 160,128,0,224
Ra 144,242,4096
Ra 148,242,5120
Mvaa 160,132,4
s_add_u32 m0, s62, 0
Ra 152,242,6144
Cvaa 164,132,20,228
X 184
Mvaa 164,128,16
X 185
Cvaa 168,128,32,232
X 186
Mvaa 168,132,36
Ra 156,242,7168
K 214,184,185
Cvaa 172,132,52,236
Dc 1,8,61,0
Mvaa 172,128,48
X 187
Mvaa 160,136,8
X 188
Mvaa 160,140,12
Ra 128,242,16384
K 215,186,187
Mvaa 164,140,28
Dc 1,8,61,1024
Mvaa 164,136,24
X 189
Mvaa 168,136,40
X 190
Mvaa 168,140,44
Ra 132,242,17408
K 222,188,189
Mvaa 172,140,60
Dc 1,8,61,2048
Mvaa 172,136,56
X 191
v_smfmac_f32_16x16x64_bf16 a[160:163], a[176:179], v[192:199], v240
Ra 136,242,18432
K 223,190,191
v_smfmac_f32_16x16x64_bf16 a[164:167], a[176:179], v[200:207], v240
X 160
v_smfmac_f32_16x16x64_bf16 a[168:171], a[176:179], v[208:215], v240
Dc 1,8,61,3072
v_smfmac_f32_16x16x64_bf16 a[172:175], a[176:179], v[216:223], v240
W 3
X 161
Cvaa 176,144,0,224
s_add_u32 m0, s62, 4096
X 162
Mvaa 176,148,4
Ra 140,242,19456
K 192,160,161
Cvaa 180,148,20,228
X 163
Mvaa 180,144,16
Dc 243,8,61,0
Cvaa 184,144,32,232
X 164
Mvaa 184,148,36
K 193,162,163
Cvaa 188,148,52,236
X 165
Mvaa 188,144,48
Dc 243,8,61,1024
Ra 144,242,20480
Mvaa 176,152,8
X 166
Mvaa 176,156,12
Ra 148,242,21504
K 200,164,165
Mvaa 180,156,28
X 167
Mvaa 180,152,24
Dc 243,8,61,2048
Mvaa 184,152,40
X 168
Mvaa 184,156,44
K 201,166,167
Mvaa 188,156,60
X 169
Mvaa 188,152,56
W 2
Dc 243,8,61,3072
Ra 152,242,22528
Mvav 32,128,196
s_add_u32 m0, s62, 8192
X 170
Mvav 64,128,204
Ra 156,242,23552
K 208,168,169
Mvav 96,128,212
X 171
Mvav 128,128,220
Dc 244,8,61,0
Mvav 132,132,220
X 172
Mvav 100,132,212
Ra 128,242,8192
K 209,170,171
Mvav 68,132,204
X 173
Mvav 36,132,196
Dc 244,8,61,1024
Mvav 40,136,196
X 174
Mvav 72,136,204
Ra 132,242,9216
K 216,172,173
Mvav 104,136,212
X 175
Mvav 136,136,220
Dc 244,8,61,2048
Mvav 140,140,220
X 176
Mvav 108,140,212
Ra 136,242,10240
K 217,174,175
Mvav 76,140,204
X 177
Mvav 44,140,196
W 3
Dc 244,8,61,3072
Mvav 48,144,196
X 178
Mvav 80,144,204
s_add_u32 m0, s62, 12288
Ra 140,242,11264
Mvav 112,144,212
K 194,176,177
Mvav 144,144,220
X 179
Mvav 148,148,220
X 180
Mvav 116,148,212
Dc 245,8,61,0
Ra 144,242,12288
Mvav 84,148,204
K 195,178,179
Mvav 52,148,196
X 181
Mvav 56,152,196
X 182
Mvav 88,152,204
Dc 245,8,61,1024
Ra 148,242,13312
Mvav 120,152,212
K 202,180,181
Mvav 152,152,220
X 183
Mvav 156,156,220
X 184
Mvav 124,156,212
Dc 245,8,61,2048
Ra 152,242,14336
Mvav 92,156,204
K 203,182,183
Mvav 60,156,196
W 3
X 185
Cvaa 160,128,0,224
X 186
Mvaa 160,132,4
Dc 245,8,61,3072
Ra 156,242,15360
Cvaa 164,132,20,228
s_add_u32 s61, s61, s63
K 210,184,185
Mvaa 164,128,16
X 187
Cvaa 168,128,32,232
X 188
Mvaa 168,132,36
Cvaa 172,132,52,236
s_xor_b32 s62, s62, 0x10000
K 211,186,187
Mvaa 172,128,48
X 189
Mvaa 160,136,8
Ra 128,242,24576
Ra 132,242,25600
Mvaa 160,140,12
s_add_u32 s64, s64, 1
X 190
Mvaa 164,140,28
K 218,188,189
Mvaa 164,136,24
X 191
Mvaa 168,136,40
Mvaa 168,140,44
X 160
Mvaa 172,140,60
K 219,190,191
Mvaa 172,136,56
W 2
X 161
Cvaa 176,144,0,224
Ra 136,242,26624
Ra 140,242,27648
Mvaa 176,148,4
X 162
Cvaa 180,148,20,228
K 196,160,161
Mvaa 180,144,16
X 163
Cvaa 184,144,32,232
Mvaa 184,148,36
X 164
Cvaa 188,148,52,236
K 197,162,163
)"
R"(Mvaa 188,144,48
X 165
Mvaa 176,152,8
Ra 144,242,28672
Ra 148,242,29696
Mvaa 176,156,12
X 166
Mvaa 180,156,28
K 204,164,165
Mvaa 180,152,24
X 167
Mvaa 184,152,40
Mvaa 184,156,44
X 168
Mvaa 188,156,60
Mvaa 188,152,56
W 2
Ra 152,242,30720
K 205,166,167
Mvav 32,128,192
X 169
Mvav 64,128,200
Ra 156,242,31744
Mvav 96,128,208
X 170
Mvav 128,128,216
K 212,168,169
Mvav 132,132,216
X 171
Mvav 100,132,208
X 172
Mvav 68,132,200
Mvav 36,132,192
K 213,170,171
Mvav 40,136,192
X 173
Mvav 72,136,200
X 174
Mvav 104,136,208
Mvav 136,136,216
K 220,172,173
Mvav 140,140,216
X 175
Mvav 108,140,208
X 176
Mvav 76,140,200
s_waitcnt vmcnt(0) lgkmcnt(0)
s_barrier
v_xor_b32 v242, 0x10000, v242
Mvav 44,140,192
Ra 128,242,0
K 221,174,175
Mvav 48,144,192
X 177
Mvav 80,144,200
X 178
Mvav 112,144,208
Ra 132,242,1024
Mvav 144,144,216
K 198,176,177
Mvav 148,148,216
X 179
Mvav 116,148,208
X 180
Mvav 84,148,200
Ra 136,242,2048
Mvav 52,148,192
K 199,178,179
Mvav 56,152,192
X 181
Mvav 88,152,200
Ra 140,242,3072
Mvav 120,152,208
X 182
Mvav 152,152,216
K 206,180,181
Mvav 156,156,216
X 183
Mvav 124,156,208
Mvav 92,156,200
K 207,182,183
Mvav 60,156,192
s_cmp_lg_u32 s64, s66
s_cbranch_scc1 .Ll1_34%=
s_cmp_ge_u32 s14, 2
s_cbranch_scc1 .Lf1r_47%=
.Lsp1_36%=:
W 0
Cvaa 160,128,0,224
Ra 144,242,4096
Ra 148,242,5120
Mvaa 160,132,4
s_add_u32 m0, s62, 0
Ra 152,242,6144
Cvaa 164,132,20,228
X 184
Mvaa 164,128,16
X 185
Cvaa 168,128,32,232
X 186
Mvaa 168,132,36
Ra 156,242,7168
K 214,184,185
Cvaa 172,132,52,236
Dc 1,8,61,0
Mvaa 172,128,48
X 187
Mvaa 160,136,8
X 188
Mvaa 160,140,12
Ra 128,242,16384
K 215,186,187
Mvaa 164,140,28
Dc 1,8,61,1024
Ra 132,242,17408
Mvaa 164,136,24
X 189
Mvaa 168,136,40
X 190
Mvaa 168,140,44
Dc 1,8,61,2048
K 222,188,189
Mvaa 172,140,60
X 191
Mvaa 172,136,56
Ra 136,242,18432
K 223,190,191
v_smfmac_f32_16x16x64_bf16 a[160:163], a[176:179], v[192:199], v240
Dc 1,8,61,3072
Ra 140,242,19456
v_smfmac_f32_16x16x64_bf16 a[164:167], a[176:179], v[200:207], v240
s_add_u32 m0, s62, 4096
v_cndmask_b32_e64 v160, v241, v160, s[72:73]
v_cndmask_b32_e64 v161, v241, v161, s[74:75]
v_smfmac_f32_16x16x64_bf16 a[168:171], a[176:179], v[208:215], v240
v_cndmask_b32_e64 v162, v241, v162, s[76:77]
v_cndmask_b32_e64 v163, v241, v163, s[78:79]
v_smfmac_f32_16x16x64_bf16 a[172:175], a[176:179], v[216:223], v240
W 4
X 160
Cvaa 180,144,16,228
X 161
Mvaa 180,148,20
Dc 243,8,61,0
Cvaa 184,148,36,232
X 162
Mvaa 184,144,32
K 192,160,161
Cvaa 188,144,48,236
X 163
Mvaa 188,148,52
Dc 243,8,61,1024
Ra 144,242,20480
Mvaa 180,152,24
X 164
Mvaa 180,156,28
Ra 148,242,21504
K 193,162,163
Mvaa 184,156,44
Dc 243,8,61,2048
Mvaa 184,152,40
X 165
Mvaa 188,152,56
X 166
Mvaa 188,156,60
W 2
Dc 243,8,61,3072
K 200,164,165
Mvav 32,128,196
s_add_u32 m0, s62, 8192
X 167
Mvav 64,128,204
Ra 152,242,22528
Ra 156,242,23552
Mvav 96,128,212
X 168
Mvav 128,128,220
Dc 244,8,61,0
K 201,166,167
Mvav 132,132,220
X 169
Mvav 100,132,212
X 170
Mvav 68,132,204
Ra 128,242,8192
K 208,168,169
Mvav 36,132,196
X 171
Mvav 40,136,196
Dc 244,8,61,1024
Ra 132,242,9216
Mvav 72,136,204
X 172
Mvav 104,136,212
K 209,170,171
Mvav 136,136,220
X 173
Mvav 140,140,220
Dc 244,8,61,2048
Ra 136,242,10240
Mvav 108,140,212
X 174
Mvav 76,140,204
K 216,172,173
Mvav 44,140,196
W 3
Dc 244,8,61,3072
Ra 140,242,11264
Mvav 48,144,196
s_add_u32 m0, s62, 12288
X 175
Mvav 80,144,204
v_mov_b32 v194, 0
K 217,174,175
Mvav 112,144,212
v_mov_b32 v195, 0
v_cndmask_b32_e64 v180, v241, v180, s[72:73]
Mvav 144,144,220
v_cndmask_b32_e64 v181, v241, v181, s[74:75]
v_cndmask_b32_e64 v182, v241, v182, s[76:77]
Mvav 148,148,220
Dc 245,8,61,0
v_cndmask_b32_e64 v183, v241, v183, s[78:79]
Mvav 116,148,212
X 180
Mvav 84,148,204
Ra 144,242,12288
Mvav 52,148,196
X 181
Mvav 56,152,196
Dc 245,8,61,1024
Ra 148,242,13312
Mvav 88,152,204
X 182
Mvav 120,152,212
K 202,180,181
Mvav 152,152,220
X 183
Mvav 156,156,220
Dc 245,8,61,2048
Ra 152,242,14336
Mvav 124,156,212
X 184
Mvav 92,156,204
K 203,182,183
Mvav 60,156,196
W 3
Dc 245,8,61,3072
Ra 156,242,15360
Cvaa 168,128,32,232
s_add_u32 s61, s61, s63
X 185
Mvaa 168,132,36
s_xor_b32 s62, s62, 0x10000
X 186
Cvaa 172,132,52,236
K 210,184,185
Mvaa 172,128,48
s_add_u32 s64, s64, 1
X 187
Mvaa 168,136,40
X 188
Mvaa 168,140,44
Ra 128,242,24576
K 211,186,187
Mvaa 172,140,60
Ra 132,242,25600
Mvaa 172,136,56
Ra 136,242,26624
Ra 140,242,27648
X 189
X 190
K 218,188,189
X 191
v_mov_b32 v196, 0
K 219,190,191
W 4
Cvaa 188,144,48,236
Ra 144,242,28672
Mvaa 188,148,52
v_mov_b32 v197, 0
v_mov_b32 v204, 0
Mvaa 188,152,56
Ra 148,242,29696
v_mov_b32 v205, 0
Mvaa 188,156,60
W 2
Ra 152,242,30720
v_cndmask_b32_e64 v168, v241, v168, s[72:73]
Mvav 32,128,192
v_cndmask_b32_e64 v169, v241, v169, s[74:75]
v_cndmask_b32_e64 v170, v241, v170, s[76:77]
Mvav 64,128,200
Ra 156,242,31744
v_cndmask_b32_e64 v171, v241, v171, s[78:79]
Mvav 96,128,208
X 168
Mvav 128,128,216
X 169
Mvav 132,132,216
X 170
Mvav 100,132,208
v_mov_b32 v224, v250
Mvav 68,132,200
K 212,168,169
v_mov_b32 v225, v250
Mvav 36,132,192
X 171
Mvav 40,136,192
v_mov_b32 v226, v250
Mvav 72,136,200
X 172
Mvav 104,136,208
v_mov_b32 v227, v250
K 213,170,171
Mvav 136,136,216
X 173
Mvav 140,140,216
v_mov_b32 v228, v251
v_mov_b32 v229, v251
Mvav 108,140,208
X 174
Mvav 76,140,200
K 220,172,173
v_mov_b32 v230, v251
Mvav 44,140,192
W 0
X 175
Mvav 48,144,192
v_mov_b32 v231, v251
v_mov_b32 v198, 0
Mvav 80,144,200
K 221,174,175
v_mov_b32 v232, v248
Mvav 112,144,208
v_mov_b32 v199, 0
v_mov_b32 v206, 0
Mvav 144,144,216
v_mov_b32 v233, v248
v_mov_b32 v207, 0
Mvav 148,148,216
v_mov_b32 v214, 0
v_mov_b32 v234, v248
Mvav 116,148,208
v_mov_b32 v215, 0
v_cndmask_b32_e64 v188, v241, v188, s[72:73]
Mvav 84,148,200
v_mov_b32 v235, v248
v_cndmask_b32_e64 v189, v241, v189, s[74:75]
Mvav 52,148,192
v_cndmask_b32_e64 v190, v241, v190, s[76:77]
v_mov_b32 v236, v249
Mvav 56,152,192
v_cndmask_b32_e64 v191, v241, v191, s[78:79]
v_mov_b32 v237, v249
Mvav 88,152,200
X 188
Mvav 120,152,208
X 189
Mvav 152,152,216
v_mov_b32 v238, v249
v_mov_b32 v239, v249
Mvav 156,156,216
X 190
Mvav 124,156,208
s_waitcnt vmcnt(0)
s_barrier
v_xor_b32 v242, 0x10000, v242
Mvav 92,156,200
Ra 128,242,40960
K 222,188,189
Mvav 60,156,192
Ra 132,242,41984
Ra 136,242,43008
Ra 140,242,44032
X 191
s_nop 0
K 223,190,191
s_cmp_eq_u32 s64, 15
s_cbranch_scc1 .Lfs_42%=
.Lsp2_37%=:
v_smfmac_f32_16x16x64_bf16 a[160:163], a[176:179], v[192:199], v240
s_add_u32 m0, s62, 0
Ra 144,242,45056
Ra 148,242,46080
Ra 152,242,47104
Ra 156,242,48128
v_smfmac_f32_16x16x64_bf16 a[164:167], a[176:179], v[200:207], v240
v_smfmac_f32_16x16x64_bf16 a[168:171], a[176:179], v[208:215], v240
v_smfmac_f32_16x16x64_bf16 a[172:175], a[176:179], v[216:223], v240
W 4
Dc 1,8,61,0
Cvaa 168,128,96,232
v_mov_b32 v192, 0
G 12,160
Mvaa 168,132,100
Dc 1,8,61,1024
G 4,161
A 12,12,4
v_cmp_nlt_f32_e64 s[86:87], v12, s90
)"
R"(v_mov_b32 v193, 0
Cvaa 172,132,116,236
v_rcp_f32 v12, v12
s_or_b64 s[88:89], s[88:89], s[86:87]
v_accvgpr_write_b32 a160, 0
v_mov_b32 v200, 0
v_accvgpr_write_b32 a161, 0
v_accvgpr_write_b32 a162, 0
Mvaa 172,128,112
Dc 1,8,61,2048
v_accvgpr_write_b32 a163, 0
G 13,164
v_mov_b32 v201, 0
G 5,165
A 13,13,5
Mvaa 168,136,104
Ra 128,242,16384
Ra 132,242,17408
v_cmp_nlt_f32_e64 s[86:87], v13, s90
v_rcp_f32 v13, v13
s_or_b64 s[88:89], s[88:89], s[86:87]
v_accvgpr_write_b32 a164, 0
Mvaa 168,140,108
Dc 1,8,61,3072
v_accvgpr_write_b32 a165, 0
v_accvgpr_write_b32 a166, 0
v_accvgpr_write_b32 a167, 0
U 32,32,12
U 33,33,12
Mvaa 172,140,124
U 34,34,12
U 35,35,12
U 36,36,12
U 37,37,12
U 38,38,12
U 39,39,12
Mvaa 172,136,120
s_add_u32 m0, s62, 4096
Ra 136,242,18432
Ra 140,242,19456
K 32,32,33
K 33,34,35
K 34,36,37
W 4
Cvaa 188,144,112,236
K 35,38,39
s_add_u32 s93, s67, 0
buffer_store_dwordx4 v[32:35], v1, s[8:11], s93 offen offset:0 sc1
U 40,40,12
U 41,41,12
U 42,42,12
U 43,43,12
Mvaa 188,148,116
Ra 144,242,20480
v_cndmask_b32_e64 v168, v241, v168, s[72:73]
v_cndmask_b32_e64 v169, v241, v169, s[74:75]
v_cndmask_b32_e64 v170, v241, v170, s[76:77]
v_cndmask_b32_e64 v171, v241, v171, s[78:79]
U 44,44,12
Mvaa 188,152,120
X 168
X 169
U 45,45,12
U 46,46,12
Mvaa 188,156,124
W 1
Ra 148,242,21504
Dc 243,8,61,0
U 47,47,12
K 40,40,41
K 41,42,43
K 42,44,45
Mvav 96,128,212
Ra 152,242,22528
Ra 156,242,23552
K 43,46,47
X 170
K 208,168,169
Mvav 128,128,220
buffer_store_dwordx4 v[40:43], v1, s[8:11], s93 offen offset:1024 sc1
U 48,48,12
U 49,49,12
U 50,50,12
U 51,51,12
U 52,52,12
Mvav 132,132,220
U 53,53,12
U 54,54,12
U 55,55,12
K 48,48,49
K 49,50,51
K 50,52,53
Mvav 100,132,212
Dc 243,8,61,1024
Ra 128,242,32768
Ra 132,242,33792
X 171
K 51,54,55
Mvav 104,136,212
X 172
K 209,170,171
X 173
Mvav 136,136,220
buffer_store_dwordx4 v[48:51], v1, s[8:11], s93 offen offset:2048 sc1
U 56,56,12
U 57,57,12
U 58,58,12
U 59,59,12
U 60,60,12
Mvav 140,140,220
U 61,61,12
U 62,62,12
U 63,63,12
K 56,56,57
K 57,58,59
K 58,60,61
Mvav 108,140,212
W 2
Dc 243,8,61,2048
Ra 136,242,34816
Ra 140,242,35840
K 59,62,63
X 174
Mvav 112,144,212
K 216,172,173
X 175
Mvav 144,144,220
buffer_store_dwordx4 v[56:59], v1, s[8:11], s93 offen offset:3072 sc1
Ra 144,242,36864
G 14,168
G 6,169
A 14,14,6
v_cmp_nlt_f32_e64 s[86:87], v14, s90
Mvav 148,148,220
v_rcp_f32 v14, v14
s_or_b64 s[88:89], s[88:89], s[86:87]
v_accvgpr_write_b32 a168, 0
v_accvgpr_write_b32 a169, 0
v_accvgpr_write_b32 a170, 0
v_accvgpr_write_b32 a171, 0
Mvav 116,148,212
Dc 243,8,61,3072
s_add_u32 m0, s62, 8192
G 15,172
G 7,173
A 15,15,7
v_cmp_nlt_f32_e64 s[86:87], v15, s90
v_mov_b32 v194, 0
Mvav 120,152,212
v_rcp_f32 v15, v15
s_or_b64 s[88:89], s[88:89], s[86:87]
v_accvgpr_write_b32 a172, 0
v_accvgpr_write_b32 a173, 0
v_accvgpr_write_b32 a174, 0
v_accvgpr_write_b32 a175, 0
Mvav 152,152,220
Dc 244,8,61,0
U 64,64,13
U 65,65,13
U 66,66,13
U 67,67,13
U 68,68,13
Mvav 156,156,220
U 69,69,13
U 70,70,13
U 71,71,13
K 64,64,65
K 65,66,67
K 66,68,69
Mvav 124,156,212
W 1
Dc 244,8,61,1024
Ra 148,242,37888
K 67,70,71
s_add_u32 s93, s67, 131072
K 217,174,175
v_mov_b32 v195, 0
Cvaa 160,128,64,224
Ra 152,242,38912
Ra 156,242,39936
v_mov_b32 v202, 0
v_mov_b32 v203, 0
v_mov_b32 v210, 0
v_mov_b32 v211, 0
Mvaa 160,132,68
buffer_store_dwordx4 v[64:67], v1, s[8:11], s93 offen offset:0 sc1
U 72,72,13
U 73,73,13
U 74,74,13
U 75,75,13
U 76,76,13
Cvaa 164,132,84,228
U 77,77,13
U 78,78,13
U 79,79,13
K 72,72,73
K 73,74,75
K 74,76,77
Mvaa 164,128,80
Dc 244,8,61,2048
K 75,78,79
v_cndmask_b32_e64 v188, v241, v188, s[72:73]
v_cndmask_b32_e64 v189, v241, v189, s[74:75]
v_cndmask_b32_e64 v190, v241, v190, s[76:77]
v_cndmask_b32_e64 v191, v241, v191, s[78:79]
Cvaa 168,128,96,232
X 188
X 189
Mvaa 168,132,100
buffer_store_dwordx4 v[72:75], v1, s[8:11], s93 offen offset:1024 sc1
U 80,80,13
U 81,81,13
U 82,82,13
U 83,83,13
U 84,84,13
Cvaa 172,132,116,236
U 85,85,13
U 86,86,13
U 87,87,13
K 80,80,81
K 81,82,83
K 82,84,85
Mvaa 172,128,112
Dc 244,8,61,3072
s_add_u32 m0, s62, 12288
Ra 128,242,49152
Ra 132,242,50176
K 83,86,87
X 190
Mvaa 160,136,72
K 218,188,189
X 191
Mvaa 160,140,76
buffer_store_dwordx4 v[80:83], v1, s[8:11], s93 offen offset:2048 sc1
U 88,88,13
U 89,89,13
U 90,90,13
U 91,91,13
U 92,92,13
Mvaa 164,140,92
U 93,93,13
U 94,94,13
U 95,95,13
K 88,88,89
K 89,90,91
K 90,92,93
Mvaa 164,136,88
K 91,94,95
buffer_store_dwordx4 v[88:91], v1, s[8:11], s93 offen offset:3072 sc1
U 96,96,14
U 97,97,14
U 98,98,14
U 99,99,14
Mvaa 168,136,104
U 100,100,14
U 101,101,14
U 102,102,14
U 103,103,14
K 96,96,97
K 97,98,99
Mvaa 168,140,108
Dc 245,8,61,0
K 98,100,101
K 99,102,103
s_add_u32 s93, s67, 262144
v_cndmask_b32_e64 v160, v241, v160, s[72:73]
K 219,190,191
v_cndmask_b32_e64 v161, v241, v161, s[74:75]
Mvaa 172,140,124
v_cndmask_b32_e64 v162, v241, v162, s[76:77]
v_cndmask_b32_e64 v163, v241, v163, s[78:79]
X 160
Mvaa 172,136,120
W 2
Dc 245,8,61,1024
Ra 136,242,51200
Ra 140,242,52224
X 161
Cvaa 180,144,80,228
Mvaa 180,148,84
buffer_store_dwordx4 v[96:99], v1, s[8:11], s93 offen offset:0 sc1
U 104,104,14
U 105,105,14
U 106,106,14
U 107,107,14
U 108,108,14
Cvaa 184,148,100,232
U 109,109,14
U 110,110,14
U 111,111,14
K 104,104,105
K 105,106,107
K 106,108,109
Mvaa 184,144,96
Dc 245,8,61,2048
K 107,110,111
X 162
K 196,160,161
Cvaa 188,144,112,236
Ra 144,242,53248
X 163
Mvaa 188,148,116
buffer_store_dwordx4 v[104:107], v1, s[8:11], s93 offen offset:1024 sc1
U 112,112,14
U 113,113,14
U 114,114,14
U 115,115,14
U 116,116,14
Mvaa 180,152,88
U 117,117,14
U 118,118,14
U 119,119,14
K 112,112,113
K 113,114,115
K 114,116,117
Mvaa 180,156,92
Dc 245,8,61,3072
s_add_u32 s61, s61, s63
s_xor_b32 s62, s62, 0x10000
s_add_u32 s64, s64, 1
K 115,118,119
X 164
Mvaa 184,156,108
Ra 148,242,54272
K 197,162,163
X 165
Mvaa 184,152,104
buffer_store_dwordx4 v[112:115], v1, s[8:11], s93 offen offset:2048 sc1
U 120,120,14
U 121,121,14
U 122,122,14
U 123,123,14
U 124,124,14
Mvaa 188,152,120
U 125,125,14
U 126,126,14
U 127,127,14
K 120,120,121
K 121,122,123
K 122,124,125
Mvaa 188,156,124
W 2
K 123,126,127
buffer_store_dwordx4 v[120:123], v1, s[8:11], s93 offen offset:3072 sc1
U 128,128,15
U 129,129,15
U 130,130,15
U 131,131,15
Zvav 32,128,192
U 132,132,15
U 133,133,15
U 134,134,15
U 135,135,15
K 128,128,129
K 129,130,131
Zvav 64,128,200
K 130,132,133
K 131,134,135
s_add_u32 s93, s67, 393216
buffer_store_dwordx4 v[128:131], v1, s[8:11], s93 offen offset:0 sc1
U 136,136,15
U 137,137,15
U 138,138,15
Zvav 96,128,208
U 139,139,15
U 140,140,15
U 141,141,15
U 142,142,15
U 143,143,15
K 136,136,137
Zvav 128,128,216
K 137,138,139
K 138,140,141
K 139,142,143
buffer_store_dwordx4 v[136:139], v1, s[8:11], s93 offen offset:1024 sc1
U 144,144,15
U 145,145,15
Zvav 132,132,216
U 146,146,15
U 147,147,15
U 148,148,15
U 149,149,15
U 150,150,15
U 151,151,15
Zvav 100,132,208
K 144,144,145
K 145,146,147
K 146,148,149
K 147,150,151
buffer_store_dwordx4 v[144:147], v1, s[8:11], s93 offen offset:2048 sc1
U 152,152,15
Zvav 68,132,200
U 153,153,15
U 154,154,15
U 155,155,15
U 156,156,15
U 157,157,15
U 158,158,15
Zvav 36,132,192
U 159,159,15
)"
R"(K 152,152,153
K 153,154,155
K 154,156,157
K 155,158,159
buffer_store_dwordx4 v[152:155], v1, s[8:11], s93 offen offset:3072 sc1
Zvav 40,136,192
Ra 152,242,55296
X 166
K 204,164,165
X 167
Zvav 72,136,200
Ra 156,242,56320
X 168
K 205,166,167
X 169
Zvav 104,136,208
X 170
K 212,168,169
X 171
Zvav 136,136,216
X 172
K 213,170,171
X 173
Zvav 140,140,216
Zvav 108,140,208
X 174
Zvav 76,140,200
s_waitcnt vmcnt(6) lgkmcnt(0)
s_barrier
v_xor_b32 v242, 0x10000, v242
Zvav 44,140,192
Ra 128,242,40960
K 220,172,173
X 175
Zvav 48,144,192
Zvav 80,144,200
v_mov_b32 v198, 0
Zvav 112,144,208
Ra 132,242,41984
K 221,174,175
Zvav 144,144,216
v_mov_b32 v199, 0
v_cndmask_b32_e64 v180, v241, v180, s[72:73]
Zvav 148,148,216
v_cndmask_b32_e64 v181, v241, v181, s[74:75]
Zvav 116,148,208
Ra 136,242,43008
v_cndmask_b32_e64 v182, v241, v182, s[76:77]
Zvav 84,148,200
v_cndmask_b32_e64 v183, v241, v183, s[78:79]
Zvav 52,148,192
X 180
Zvav 56,152,192
Ra 140,242,44032
X 181
Zvav 88,152,200
Zvav 120,152,208
X 182
Zvav 152,152,216
K 206,180,181
Zvav 156,156,216
X 183
Zvav 124,156,208
Zvav 92,156,200
s_waitcnt vmcnt(0)
s_and_b64 s[86:87], s[88:89], s[24:25]
s_cbranch_scc1 .LL_49%=
v_mov_b64 v[28:29], s[12:13]
s_mov_b64 exec, 1
buffer_store_dwordx2 v[28:29], off, s[8:11], s69 sc1
s_mov_b64 exec, -1
.LL_49%=:
K 207,182,183
Zvav 60,156,192
s_cmp_eq_u32 s64, 15
s_cbranch_scc1 .Lf2_41%=
.Ll2_38%=:
W 0
Cvaa 160,128,64,224
Ra 144,242,45056
Ra 148,242,46080
Mvaa 160,132,68
s_add_u32 m0, s62, 0
Ra 152,242,47104
Cvaa 164,132,84,228
X 184
Mvaa 164,128,80
X 185
Cvaa 168,128,96,232
X 186
Mvaa 168,132,100
Ra 156,242,48128
K 214,184,185
Cvaa 172,132,116,236
Dc 1,8,61,0
Mvaa 172,128,112
X 187
Mvaa 160,136,72
X 188
Mvaa 160,140,76
Ra 128,242,57344
K 215,186,187
Mvaa 164,140,92
Dc 1,8,61,1024
Mvaa 164,136,88
X 189
Mvaa 168,136,104
X 190
Mvaa 168,140,108
Ra 132,242,58368
K 222,188,189
Mvaa 172,140,124
Dc 1,8,61,2048
Mvaa 172,136,120
X 191
v_smfmac_f32_16x16x64_bf16 a[160:163], a[176:179], v[192:199], v240
Ra 136,242,59392
K 223,190,191
v_smfmac_f32_16x16x64_bf16 a[164:167], a[176:179], v[200:207], v240
X 160
v_smfmac_f32_16x16x64_bf16 a[168:171], a[176:179], v[208:215], v240
Dc 1,8,61,3072
v_smfmac_f32_16x16x64_bf16 a[172:175], a[176:179], v[216:223], v240
W 3
X 161
Cvaa 176,144,64,224
s_add_u32 m0, s62, 4096
X 162
Mvaa 176,148,68
Ra 140,242,60416
K 192,160,161
Cvaa 180,148,84,228
X 163
Mvaa 180,144,80
Dc 243,8,61,0
Cvaa 184,144,96,232
X 164
Mvaa 184,148,100
K 193,162,163
Cvaa 188,148,116,236
X 165
Mvaa 188,144,112
Dc 243,8,61,1024
Ra 144,242,61440
Mvaa 176,152,72
X 166
Mvaa 176,156,76
Ra 148,242,62464
K 200,164,165
Mvaa 180,156,92
X 167
Mvaa 180,152,88
Dc 243,8,61,2048
Mvaa 184,152,104
X 168
Mvaa 184,156,108
K 201,166,167
Mvaa 188,156,124
X 169
Mvaa 188,152,120
W 2
Dc 243,8,61,3072
Ra 152,242,63488
Mvav 32,128,196
s_add_u32 m0, s62, 8192
X 170
Mvav 64,128,204
Ra 156,242,64512
K 208,168,169
Mvav 96,128,212
X 171
Mvav 128,128,220
Dc 244,8,61,0
Mvav 132,132,220
X 172
Mvav 100,132,212
Ra 128,242,32768
K 209,170,171
Mvav 68,132,204
X 173
Mvav 36,132,196
Dc 244,8,61,1024
Mvav 40,136,196
X 174
Mvav 72,136,204
Ra 132,242,33792
K 216,172,173
Mvav 104,136,212
X 175
Mvav 136,136,220
Dc 244,8,61,2048
Mvav 140,140,220
X 176
Mvav 108,140,212
Ra 136,242,34816
K 217,174,175
Mvav 76,140,204
X 177
Mvav 44,140,196
W 3
Dc 244,8,61,3072
Mvav 48,144,196
X 178
Mvav 80,144,204
s_add_u32 m0, s62, 12288
Ra 140,242,35840
Mvav 112,144,212
K 194,176,177
Mvav 144,144,220
X 179
Mvav 148,148,220
X 180
Mvav 116,148,212
Dc 245,8,61,0
Ra 144,242,36864
Mvav 84,148,204
K 195,178,179
Mvav 52,148,196
X 181
Mvav 56,152,196
X 182
Mvav 88,152,204
Dc 245,8,61,1024
Ra 148,242,37888
Mvav 120,152,212
K 202,180,181
Mvav 152,152,220
X 183
Mvav 156,156,220
X 184
Mvav 124,156,212
Dc 245,8,61,2048
Ra 152,242,38912
Mvav 92,156,204
K 203,182,183
Mvav 60,156,196
W 3
X 185
Cvaa 160,128,64,224
X 186
Mvaa 160,132,68
Dc 245,8,61,3072
Ra 156,242,39936
Cvaa 164,132,84,228
s_add_u32 s61, s61, s63
K 210,184,185
Mvaa 164,128,80
X 187
Cvaa 168,128,96,232
X 188
Mvaa 168,132,100
Cvaa 172,132,116,236
s_xor_b32 s62, s62, 0x10000
K 211,186,187
Mvaa 172,128,112
X 189
Mvaa 160,136,72
Ra 128,242,49152
Ra 132,242,50176
Mvaa 160,140,76
s_add_u32 s64, s64, 1
X 190
Mvaa 164,140,92
K 218,188,189
Mvaa 164,136,88
X 191
Mvaa 168,136,104
Mvaa 168,140,108
X 160
Mvaa 172,140,124
K 219,190,191
Mvaa 172,136,120
W 2
X 161
Cvaa 176,144,64,224
Ra 136,242,51200
Ra 140,242,52224
Mvaa 176,148,68
X 162
Cvaa 180,148,84,228
K 196,160,161
Mvaa 180,144,80
X 163
Cvaa 184,144,96,232
Mvaa 184,148,100
X 164
Cvaa 188,148,116,236
K 197,162,163
Mvaa 188,144,112
X 165
Mvaa 176,152,72
Ra 144,242,53248
Ra 148,242,54272
Mvaa 176,156,76
X 166
Mvaa 180,156,92
K 204,164,165
Mvaa 180,152,88
X 167
Mvaa 184,152,104
Mvaa 184,156,108
X 168
Mvaa 188,156,124
Mvaa 188,152,120
W 2
Ra 152,242,55296
K 205,166,167
Mvav 32,128,192
X 169
Mvav 64,128,200
Ra 156,242,56320
Mvav 96,128,208
X 170
Mvav 128,128,216
K 212,168,169
Mvav 132,132,216
X 171
Mvav 100,132,208
X 172
Mvav 68,132,200
Mvav 36,132,192
K 213,170,171
Mvav 40,136,192
X 173
Mvav 72,136,200
X 174
Mvav 104,136,208
Mvav 136,136,216
K 220,172,173
Mvav 140,140,216
X 175
Mvav 108,140,208
X 176
Mvav 76,140,200
s_waitcnt vmcnt(0) lgkmcnt(0)
s_barrier
v_xor_b32 v242, 0x10000, v242
Mvav 44,140,192
Ra 128,242,40960
K 221,174,175
Mvav 48,144,192
X 177
Mvav 80,144,200
X 178
Mvav 112,144,208
Ra 132,242,41984
Mvav 144,144,216
K 198,176,177
Mvav 148,148,216
X 179
Mvav 116,148,208
X 180
Mvav 84,148,200
Ra 136,242,43008
Mvav 52,148,192
K 199,178,179
Mvav 56,152,192
X 181
Mvav 88,152,200
Ra 140,242,44032
Mvav 120,152,208
X 182
Mvav 152,152,216
K 206,180,181
Mvav 156,156,216
X 183
Mvav 124,156,208
Mvav 92,156,200
K 207,182,183
Mvav 60,156,192
s_cmp_lg_u32 s64, 15
s_cbranch_scc1 .Ll2_38%=
.Lf2_41%=:
W 0
Cvaa 160,128,64,224
v_bfe_u32 v28, v1, 4, 2
v_lshlrev_b32 v28, 3, v28
Mvaa 160,132,68
Ra 144,242,45056
Ra 148,242,46080
Cvaa 164,132,84,228
Ra 152,242,47104
buffer_load_dwordx2 v[28:29], v28, s[8:11], s54 offen sc1
Mvaa 164,128,80
s_lshl_b32 s92, s58, 2
s_add_u32 s91, s92, s51
s_add_u32 s95, s91, 3
Cvaa 168,128,96,232
s_and_b32 s95, s95, 15
s_mul_i32 s95, s95, 40960
s_mul_i32 s71, s51, 10240
Mvaa 168,132,100
s_add_u32 s71, s71, 0
s_add_u32 m0, s71, s48
s_lshl2_add_u32 s94, s48, s95
Cvaa 172,132,116,236
X 184
Mvaa 172,128,112
X 185
Mvaa 160,136,72
)"
R"(X 186
Mvaa 160,140,76
Ra 156,242,48128
K 214,184,185
Mvaa 164,140,92
X 187
Mvaa 164,136,88
D 16,40,94,0
Ra 128,242,57344
Mvaa 168,136,104
s_add_u32 s95, s91, 5
s_and_b32 s95, s95, 15
s_mul_i32 s95, s95, 40960
Mvaa 168,140,108
s_mul_i32 s71, s51, 10240
s_add_u32 s71, s71, 20480
Ra 132,242,58368
Mvaa 172,140,124
X 188
Mvaa 172,136,120
D 17,40,94,1024
K 215,186,187
v_smfmac_f32_16x16x64_bf16 a[160:163], a[176:179], v[192:199], v240
X 189
v_smfmac_f32_16x16x64_bf16 a[164:167], a[176:179], v[200:207], v240
X 190
K 222,188,189
X 191
s_nop 0
K 223,190,191
v_smfmac_f32_16x16x64_bf16 a[168:171], a[176:179], v[208:215], v240
D 18,40,94,2048
Ra 136,242,59392
v_smfmac_f32_16x16x64_bf16 a[172:175], a[176:179], v[216:223], v240
W 3
X 160
Cvaa 176,144,64,224
X 161
Mvaa 176,148,68
X 162
Cvaa 180,148,84,228
D 19,40,94,3072
K 192,160,161
Mvaa 180,144,80
s_add_u32 m0, m0, 4096
X 163
Cvaa 184,144,96,232
X 164
Mvaa 184,148,100
Ra 140,242,60416
K 193,162,163
Cvaa 188,148,116,236
buffer_load_dwordx4 v20, s[40:43], s94 offen lds
Mvaa 188,144,112
s_add_u32 m0, s71, s48
s_lshl2_add_u32 s94, s48, s95
Ra 144,242,61440
Mvaa 176,152,72
X 165
Mvaa 176,156,76
X 166
Mvaa 180,156,92
D 16,40,94,0
K 200,164,165
Mvaa 180,152,88
X 167
Mvaa 184,152,104
X 172
Mvaa 184,156,108
D 17,40,94,1024
K 201,166,167
Mvaa 188,156,124
X 173
Mvaa 188,152,120
W 1
Ra 148,242,62464
Ra 152,242,63488
Mvav 32,128,196
X 174
Mvav 64,128,204
D 18,40,94,2048
K 216,172,173
Mvav 96,128,212
X 175
Mvav 128,128,220
X 168
Mvav 132,132,220
s_mov_b32 s18, 40960
D 19,40,94,3072
K 217,174,175
Mvav 100,132,212
s_add_u32 m0, m0, 4096
X 169
Mvav 68,132,204
s_mov_b32 s19, 73728
X 170
Mvav 36,132,196
s_bitcmp1_b32 s14, 0
s_cselect_b32 s18, 49152, s18
s_cselect_b32 s19, 98304, s19
Ra 156,242,64512
K 208,168,169
Mvav 40,136,196
s_cmp_lt_u32 s14, 2
s_cselect_b32 s20, s18, s19
s_cselect_b32 s21, s19, s18
X 171
Mvav 72,136,204
X 184
Mvav 104,136,212
buffer_load_dwordx4 v20, s[40:43], s94 offen lds
K 209,170,171
Mvav 136,136,220
s_waitcnt vmcnt(10)
v_cmp_eq_u64 vcc, s[12:13], v[28:29]
s_cmp_eq_u64 vcc, -1
s_cselect_b32 s46, 79691776, 0
s_add_u32 s81, s58, 0
s_and_b32 s81, s81, 3
Mvav 140,140,220
s_lshl_b32 s81, s81, 12
s_add_u32 s81, s81, s49
Ra 128,242,49152
Mvav 108,140,212
X 185
Mvav 76,140,204
X 186
Mvav 44,140,196
W 1
buffer_load_dwordx4 a[184:187], v1, s[44:47], s81 offen offset:0 sc1
K 210,184,185
Mvav 48,144,196
s_add_u32 s82, s81, 131072
X 187
Mvav 80,144,204
v_add_u32 v2, s20, v1
v_add_u32 v3, s21, v1
Mvav 112,144,212
s_lshl_b32 s18, s14, 9
X 188
Mvav 144,144,220
Ra 132,242,50176
K 211,186,187
Mvav 148,148,220
buffer_load_dwordx4 a[200:203], v1, s[44:47], s82 offen offset:0 sc1
Ra 136,242,51200
Mvav 116,148,212
X 189
Mvav 84,148,204
v_lshrrev_b32 v232, 1, v1
v_add_u32 v232, s18, v232
Mvav 52,148,196
X 190
Mvav 56,152,196
K 218,188,189
v_bfe_u32 v12, v1, 6, 2
Mvav 88,152,204
buffer_load_dwordx4 a[188:191], v1, s[44:47], s81 offen offset:1024 sc1
Ra 140,242,52224
Mvav 120,152,212
X 191
Mvav 152,152,220
v_lshrrev_b32 v13, 8, v1
v_bfe_u32 v14, v1, 4, 1
Mvav 156,156,220
X 176
Mvav 124,156,212
K 219,190,191
v_lshlrev_b32 v14, 4, v14
Mvav 92,156,204
X 177
Mvav 60,156,196
buffer_load_dwordx4 a[204:207], v1, s[44:47], s82 offen offset:1024 sc1
v_mov_b32 v15, 0x3f80
Maav 168,180,208
X 178
K 194,176,177
X 179
X 180
K 195,178,179
Maav 172,180,216
X 181
X 182
K 202,180,181
X 183
s_nop 0
K 203,182,183
Maav 160,180,192
Ra 144,242,53248
v_lshlrev_b32 v15, v14, v15
Maav 164,180,200
W 1
buffer_load_dwordx4 a[192:195], v1, s[44:47], s81 offen offset:2048 sc1
v_bfe_u32 v14, v1, 5, 1
Mvav 96,128,208
Ra 148,242,54272
v_xor_b32 v27, v12, v13
v_lshl_or_b32 v27, v27, 1, v14
Mvav 128,128,216
v_cmp_eq_u32_e64 s[86:87], 0, v27
v_mov_b32 v226, 0
s_nop 0
v_cndmask_b32_e64 v224, 0, v15, s[86:87]
Mvav 132,132,216
Ra 152,242,55296
Ra 156,242,56320
buffer_load_dwordx4 a[208:211], v1, s[44:47], s82 offen offset:2048 sc1
Mvav 100,132,208
v_mov_b32 v228, 0
v_mov_b32 v230, v224
v_cmp_eq_u32_e64 s[86:87], 1, v27
Mvav 104,136,208
v_mov_b32 v227, 0
Mvav 136,136,216
buffer_load_dwordx4 a[196:199], v1, s[44:47], s81 offen offset:3072 sc1
v_cndmask_b32_e64 v225, 0, v15, s[86:87]
v_mov_b32 v229, 0
Mvav 140,140,216
v_mov_b32 v231, v225
G 4,160
G 8,161
Mvav 108,140,208
W 0
G 5,164
G 9,165
G 6,168
Mvav 112,144,208
buffer_load_dwordx4 a[212:215], v1, s[44:47], s82 offen offset:3072 sc1
s_add_u32 s81, s58, 1
s_and_b32 s81, s81, 3
s_lshl_b32 s81, s81, 12
Mvav 144,144,216
s_add_u32 s81, s81, s49
G 10,169
G 7,172
G 11,173
Mvav 148,148,216
A 4,4,8
A 5,5,9
A 6,6,10
Mvav 116,148,208
buffer_load_dwordx4 a[216:219], v1, s[44:47], s81 offen offset:0 sc1
s_add_u32 s82, s81, 131072
A 7,7,11
ds_write_b64 v232, v[6:7] offset:57344
Mvav 120,152,208
A 8,4,5
A 9,6,7
A 8,8,9
Mvav 152,152,216
v_cmp_ngt_f32_e64 s[86:87], s90, v8
s_lshl_b32 s19, s14, 2
v_mov_b32 v9, s19
s_or_b64 s[88:89], s[88:89], s[86:87]
Mvav 156,156,216
s_and_b64 s[88:89], s[88:89], s[24:25]
s_cselect_b32 s18, 1, 0
buffer_load_dwordx4 a[232:235], v1, s[44:47], s82 offen offset:0 sc1
v_mov_b32 v8, s18
ds_write_b32 v9, v8 offset:59392
Mvav 124,156,208
v_xor_b32 v10, 1024, v232
v_bfe_u32 v11, v1, 2, 4
K 96,96,97
Mvav 32,128,192
K 97,98,99
K 98,100,101
K 99,102,103
Mvav 64,128,200
ds_write_b128 v2, v[96:99] offset:0
K 128,128,129
K 129,130,131
Mvav 68,132,200
K 130,132,133
K 131,134,135
ds_write_b128 v2, v[128:131] offset:4096
Mvav 36,132,192
buffer_load_dwordx4 a[220:223], v1, s[44:47], s81 offen offset:1024 sc1
K 104,104,105
K 105,106,107
Mvav 40,136,192
K 106,108,109
K 107,110,111
ds_write_b128 v2, v[104:107] offset:1024
Mvav 72,136,200
K 136,136,137
K 137,138,139
K 138,140,141
Mvav 76,140,200
buffer_load_dwordx4 a[236:239], v1, s[44:47], s82 offen offset:1024 sc1
K 139,142,143
ds_write_b128 v2, v[136:139] offset:5120
Mvav 44,140,192
K 112,112,113
K 113,114,115
K 114,116,117
Mvav 48,144,192
K 115,118,119
ds_write_b128 v2, v[112:115] offset:2048
K 144,144,145
Mvav 80,144,200
K 145,146,147
K 146,148,149
K 147,150,151
Mvav 84,148,200
ds_write_b128 v2, v[144:147] offset:6144
buffer_load_dwordx4 a[224:227], v1, s[44:47], s81 offen offset:2048 sc1
K 120,120,121
Mvav 52,148,192
K 121,122,123
K 122,124,125
K 123,126,127
Mvav 56,152,192
ds_write_b128 v2, v[120:123] offset:3072
K 152,152,153
K 153,154,155
Mvav 88,152,200
buffer_load_dwordx4 a[240:243], v1, s[44:47], s82 offen offset:2048 sc1
K 154,156,157
K 155,158,159
Mvav 92,156,200
ds_write_b128 v2, v[152:155] offset:7168
Mvav 60,156,192
buffer_load_dwordx4 a[228:231], v1, s[44:47], s81 offen offset:3072 sc1
buffer_load_dwordx4 a[244:247], v1, s[44:47], s82 offen offset:3072 sc1
s_branch .Lend_43%=
.p2align 12
.Lfs_42%=:
v_smfmac_f32_16x16x64_bf16 a[160:163], a[176:179], v[192:199], v240
s_mov_b32 s46, 0
s_lshl_b32 s92, s58, 2
s_add_u32 s91, s92, s51
s_add_u32 s95, s91, 3
s_and_b32 s95, s95, 15
s_mul_i32 s95, s95, 40960
s_mul_i32 s71, s51, 10240
v_smfmac_f32_16x16x64_bf16 a[164:167], a[176:179], v[200:207], v240
s_add_u32 s71, s71, 0
s_add_u32 m0, s71, s48
s_lshl2_add_u32 s94, s48, s95
Ra 144,242,45056
Ra 148,242,46080
Ra 152,242,47104
Ra 156,242,48128
v_smfmac_f32_16x16x64_bf16 a[168:171], a[176:179], v[208:215], v240
D 16,40,94,0
s_add_u32 s95, s91, 5
s_and_b32 s95, s95, 15
s_mul_i32 s95, s95, 40960
s_mul_i32 s71, s51, 10240
s_add_u32 s71, s71, 20480
v_smfmac_f32_16x16x64_bf16 a[172:175], a[176:179], v[216:223], v240
W 4
Cvaa 168,128,96,232
v_mov_b32 v192, 0
G 12,160
Mvaa 168,132,100
D 17,40,94,1024
G 4,161
A 12,12,4
v_cmp_nlt_f32_e64 s[86:87], v12, s90
v_rcp_f32 v12, v12
s_or_b64 s[88:89], s[88:89], s[86:87]
Cvaa 172,132,116,236
v_accvgpr_write_b32 a160, 0
v_accvgpr_write_b32 a161, 0
v_accvgpr_write_b32 a162, 0
v_accvgpr_write_b32 a163, 0
G 13,164
G 5,165
Mvaa 172,128,112
v_mov_b32 v193, 0
A 13,13,5
v_cmp_nlt_f32_e64 s[86:87], v13, s90
v_rcp_f32 v13, v13
s_or_b64 s[88:89], s[88:89], s[86:87]
v_accvgpr_write_b32 a164, 0
Mvaa 168,136,104
v_accvgpr_write_b32 a165, 0
v_accvgpr_write_b32 a166, 0
v_accvgpr_write_b32 a167, 0
U 32,32,12
U 33,33,12
U 34,34,12
Mvaa 168,140,108
v_mov_b32 v200, 0
U 35,35,12
U 36,36,12
U 37,37,12
U 38,38,12
U 39,39,12
Mvaa 172,140,124
Ra 128,242,16384
Ra 132,242,17408
K 32,32,33
K 33,34,35
K 34,36,37
K 35,38,39
s_add_u32 s93, s67, 0
Mvaa 172,136,120
)"
R"(Ra 136,242,18432
Ra 140,242,19456
D 18,40,94,2048
v_mov_b32 v201, 0
W 4
Cvaa 188,144,112,236
Ra 144,242,20480
v_cndmask_b32_e64 v168, v241, v168, s[72:73]
v_cndmask_b32_e64 v169, v241, v169, s[74:75]
v_cndmask_b32_e64 v170, v241, v170, s[76:77]
Mvaa 188,148,116
buffer_store_dwordx4 v[32:35], v1, s[8:11], s93 offen offset:0 sc1
U 40,40,12
U 41,41,12
U 42,42,12
U 43,43,12
U 44,44,12
Mvaa 188,152,120
U 45,45,12
U 46,46,12
U 47,47,12
K 40,40,41
K 41,42,43
K 42,44,45
Mvaa 188,156,124
W 1
K 43,46,47
buffer_store_dwordx4 v[40:43], v1, s[8:11], s93 offen offset:1024 sc1
U 48,48,12
U 49,49,12
U 50,50,12
U 51,51,12
Mvav 96,128,212
U 52,52,12
U 53,53,12
U 54,54,12
U 55,55,12
K 48,48,49
K 49,50,51
Mvav 128,128,220
K 50,52,53
K 51,54,55
buffer_store_dwordx4 v[48:51], v1, s[8:11], s93 offen offset:2048 sc1
U 56,56,12
U 57,57,12
U 58,58,12
Mvav 132,132,220
Ra 148,242,21504
Ra 152,242,22528
Ra 156,242,23552
U 59,59,12
U 60,60,12
U 61,61,12
Mvav 100,132,212
D 19,40,94,3072
s_add_u32 m0, m0, 4096
U 62,62,12
U 63,63,12
K 56,56,57
K 57,58,59
K 58,60,61
Mvav 104,136,212
K 59,62,63
v_cndmask_b32_e64 v171, v241, v171, s[78:79]
X 168
X 169
Mvav 136,136,220
buffer_store_dwordx4 v[56:59], v1, s[8:11], s93 offen offset:3072 sc1
Ra 128,242,49152
G 14,168
G 6,169
A 14,14,6
v_cmp_nlt_f32_e64 s[86:87], v14, s90
Mvav 140,140,220
v_rcp_f32 v14, v14
s_or_b64 s[88:89], s[88:89], s[86:87]
v_accvgpr_write_b32 a168, 0
v_accvgpr_write_b32 a169, 0
v_accvgpr_write_b32 a170, 0
v_accvgpr_write_b32 a171, 0
Mvav 108,140,212
W 1
G 15,172
G 7,173
A 15,15,7
v_cmp_nlt_f32_e64 s[86:87], v15, s90
v_rcp_f32 v15, v15
Mvav 112,144,212
s_or_b64 s[88:89], s[88:89], s[86:87]
buffer_load_dwordx4 v20, s[40:43], s94 offen lds
v_accvgpr_write_b32 a172, 0
v_accvgpr_write_b32 a173, 0
v_accvgpr_write_b32 a174, 0
v_accvgpr_write_b32 a175, 0
U 64,64,13
Mvav 144,144,220
s_add_u32 m0, s71, s48
s_lshl2_add_u32 s94, s48, s95
U 65,65,13
U 66,66,13
U 67,67,13
U 68,68,13
U 69,69,13
Mvav 148,148,220
U 70,70,13
U 71,71,13
K 64,64,65
K 65,66,67
K 66,68,69
K 67,70,71
s_add_u32 s93, s67, 131072
Mvav 116,148,212
buffer_store_dwordx4 v[64:67], v1, s[8:11], s93 offen offset:0 sc1
U 72,72,13
U 73,73,13
U 74,74,13
U 75,75,13
U 76,76,13
Mvav 120,152,212
Ra 132,242,50176
Ra 136,242,51200
Ra 140,242,52224
U 77,77,13
U 78,78,13
U 79,79,13
Mvav 152,152,220
K 72,72,73
K 73,74,75
K 74,76,77
K 75,78,79
buffer_store_dwordx4 v[72:75], v1, s[8:11], s93 offen offset:1024 sc1
U 80,80,13
Mvav 156,156,220
X 170
K 208,168,169
X 171
X 172
K 209,170,171
X 173
X 174
K 216,172,173
X 175
v_mov_b32 v194, 0
K 217,174,175
v_mov_b32 v195, 0
v_mov_b32 v202, 0
v_mov_b32 v203, 0
v_mov_b32 v210, 0
v_mov_b32 v211, 0
Mvav 124,156,212
v_cndmask_b32_e64 v188, v241, v188, s[72:73]
v_cndmask_b32_e64 v189, v241, v189, s[74:75]
v_cndmask_b32_e64 v190, v241, v190, s[76:77]
v_cndmask_b32_e64 v191, v241, v191, s[78:79]
X 188
X 189
X 190
K 218,188,189
X 191
s_nop 0
K 219,190,191
Maav 168,180,208
D 16,40,94,0
U 81,81,13
U 82,82,13
U 83,83,13
U 84,84,13
U 85,85,13
Maav 172,180,216
W 0
U 86,86,13
U 87,87,13
K 80,80,81
K 81,82,83
K 82,84,85
K 83,86,87
Zvav 32,128,192
buffer_store_dwordx4 v[80:83], v1, s[8:11], s93 offen offset:2048 sc1
U 88,88,13
U 89,89,13
U 90,90,13
U 91,91,13
U 92,92,13
Zvav 64,128,200
U 93,93,13
U 94,94,13
U 95,95,13
K 88,88,89
K 89,90,91
K 90,92,93
K 91,94,95
buffer_store_dwordx4 v[88:91], v1, s[8:11], s93 offen offset:3072 sc1
U 96,96,14
U 97,97,14
U 98,98,14
U 99,99,14
U 100,100,14
U 101,101,14
U 102,102,14
U 103,103,14
K 96,96,97
K 97,98,99
K 98,100,101
K 99,102,103
s_add_u32 s93, s67, 262144
buffer_store_dwordx4 v[96:99], v1, s[8:11], s93 offen offset:0 sc1
s_nop 1
Zvav 96,128,208
U 104,104,14
U 105,105,14
U 106,106,14
U 107,107,14
U 108,108,14
U 109,109,14
U 110,110,14
U 111,111,14
K 104,104,105
K 105,106,107
K 106,108,109
K 107,110,111
buffer_store_dwordx4 v[104:107], v1, s[8:11], s93 offen offset:1024 sc1
U 112,112,14
U 113,113,14
U 114,114,14
U 115,115,14
U 116,116,14
U 117,117,14
U 118,118,14
U 119,119,14
K 112,112,113
K 113,114,115
K 114,116,117
K 115,118,119
buffer_store_dwordx4 v[112:115], v1, s[8:11], s93 offen offset:2048 sc1
U 120,120,14
U 121,121,14
U 122,122,14
U 123,123,14
U 124,124,14
U 125,125,14
U 126,126,14
U 127,127,14
K 120,120,121
K 121,122,123
K 122,124,125
K 123,126,127
buffer_store_dwordx4 v[120:123], v1, s[8:11], s93 offen offset:3072 sc1
U 128,128,15
U 129,129,15
U 130,130,15
U 131,131,15
U 132,132,15
U 133,133,15
U 134,134,15
U 135,135,15
K 128,128,129
K 129,130,131
K 130,132,133
K 131,134,135
s_add_u32 s93, s67, 393216
buffer_store_dwordx4 v[128:131], v1, s[8:11], s93 offen offset:0 sc1
s_nop 1
Zvav 128,128,216
Ra 144,242,53248
U 136,136,15
U 137,137,15
U 138,138,15
U 139,139,15
U 140,140,15
Zvav 132,132,216
D 17,40,94,1024
U 141,141,15
U 142,142,15
U 143,143,15
K 136,136,137
K 137,138,139
Zvav 100,132,208
Ra 148,242,54272
Ra 152,242,55296
Ra 156,242,56320
K 138,140,141
K 139,142,143
Zvav 68,132,200
D 18,40,94,2048
Zvav 36,132,192
Zvav 40,136,192
D 19,40,94,3072
s_add_u32 m0, m0, 4096
Zvav 72,136,200
Zvav 104,136,208
buffer_store_dwordx4 v[136:139], v1, s[8:11], s93 offen offset:1024 sc1
U 144,144,15
U 145,145,15
U 146,146,15
U 147,147,15
U 148,148,15
Zvav 136,136,216
U 149,149,15
U 150,150,15
U 151,151,15
K 144,144,145
K 145,146,147
K 146,148,149
Zvav 140,140,216
K 147,150,151
buffer_store_dwordx4 v[144:147], v1, s[8:11], s93 offen offset:2048 sc1
U 152,152,15
U 153,153,15
U 154,154,15
U 155,155,15
Zvav 108,140,208
U 156,156,15
U 157,157,15
U 158,158,15
U 159,159,15
K 152,152,153
K 153,154,155
Zvav 76,140,200
buffer_load_dwordx4 v20, s[40:43], s94 offen lds
K 154,156,157
K 155,158,159
Zvav 44,140,192
W 0
Zvav 48,144,192
buffer_store_dwordx4 v[152:155], v1, s[8:11], s93 offen offset:3072 sc1
s_mov_b32 s18, 40960
s_mov_b32 s19, 73728
s_bitcmp1_b32 s14, 0
s_cselect_b32 s18, 49152, s18
s_cselect_b32 s19, 98304, s19
s_cmp_lt_u32 s14, 2
s_cselect_b32 s20, s18, s19
s_cselect_b32 s21, s19, s18
v_add_u32 v2, s20, v1
v_add_u32 v3, s21, v1
Zvav 80,144,200
s_lshl_b32 s18, s14, 9
v_lshrrev_b32 v232, 1, v1
v_add_u32 v232, s18, v232
v_bfe_u32 v12, v1, 6, 2
v_lshrrev_b32 v13, 8, v1
v_bfe_u32 v14, v1, 4, 1
v_lshlrev_b32 v14, 4, v14
Zvav 112,144,208
v_mov_b32 v15, 0x3f80
v_lshlrev_b32 v15, v14, v15
v_bfe_u32 v14, v1, 5, 1
v_xor_b32 v27, v12, v13
v_lshl_or_b32 v27, v27, 1, v14
v_cmp_eq_u32_e64 s[86:87], 0, v27
Zvav 144,144,216
v_mov_b32 v226, 0
Zvav 148,148,216
v_cndmask_b32_e64 v224, 0, v15, s[86:87]
v_mov_b32 v228, 0
v_mov_b32 v230, v224
v_cmp_eq_u32_e64 s[86:87], 1, v27
v_mov_b32 v227, 0
s_nop 0
v_cndmask_b32_e64 v225, 0, v15, s[86:87]
Zvav 116,148,208
v_mov_b32 v229, 0
v_mov_b32 v231, v225
G 4,160
G 8,161
G 5,164
G 9,165
Zvav 84,148,200
G 6,168
G 10,169
G 7,172
G 11,173
A 4,4,8
A 5,5,9
Zvav 52,148,192
A 6,6,10
A 7,7,11
ds_write_b64 v232, v[6:7] offset:57344
A 8,4,5
A 9,6,7
A 8,8,9
Zvav 56,152,192
v_cmp_ngt_f32_e64 s[86:87], s90, v8
s_lshl_b32 s19, s14, 2
v_mov_b32 v9, s19
s_or_b64 s[88:89], s[88:89], s[86:87]
s_and_b64 s[88:89], s[88:89], s[24:25]
s_cselect_b32 s18, 1, 0
v_mov_b32 v8, s18
ds_write_b32 v9, v8 offset:59392
Zvav 88,152,200
v_xor_b32 v10, 1024, v232
v_bfe_u32 v11, v1, 2, 4
K 96,96,97
K 97,98,99
K 98,100,101
K 99,102,103
Zvav 120,152,208
ds_write_b128 v2, v[96:99] offset:0
K 128,128,129
)"
R"(K 129,130,131
K 130,132,133
K 131,134,135
ds_write_b128 v2, v[128:131] offset:4096
Zvav 152,152,216
K 104,104,105
K 105,106,107
K 106,108,109
K 107,110,111
ds_write_b128 v2, v[104:107] offset:1024
K 136,136,137
Zvav 156,156,216
K 137,138,139
K 138,140,141
K 139,142,143
ds_write_b128 v2, v[136:139] offset:5120
K 112,112,113
K 113,114,115
Zvav 124,156,208
K 114,116,117
K 115,118,119
ds_write_b128 v2, v[112:115] offset:2048
K 144,144,145
K 145,146,147
K 146,148,149
Zvav 92,156,200
K 147,150,151
ds_write_b128 v2, v[144:147] offset:6144
Zvav 60,156,192
K 120,120,121
K 121,122,123
K 122,124,125
K 123,126,127
ds_write_b128 v2, v[120:123] offset:3072
K 152,152,153
K 153,154,155
K 154,156,157
K 155,158,159
ds_write_b128 v2, v[152:155] offset:7168
s_waitcnt vmcnt(0)
s_and_b64 s[86:87], s[88:89], s[24:25]
s_cbranch_scc1 .LL_50%=
v_mov_b64 v[28:29], s[12:13]
s_mov_b64 exec, 1
buffer_store_dwordx2 v[28:29], off, s[8:11], s69 sc1
s_mov_b64 exec, -1
.LL_50%=:
s_mov_b32 s70, 0
s_branch .Lend_43%=
.p2align 12
.Lpd_32%=:
v_pk_mul_f32 v[32:33], v[32:33], s[22:23]
v_pk_mul_f32 v[34:35], v[34:35], s[22:23]
v_pk_mul_f32 v[36:37], v[36:37], s[22:23]
v_pk_mul_f32 v[38:39], v[38:39], s[22:23]
v_pk_mul_f32 v[64:65], v[64:65], s[22:23]
v_pk_mul_f32 v[66:67], v[66:67], s[22:23]
v_pk_mul_f32 v[68:69], v[68:69], s[22:23]
v_pk_mul_f32 v[70:71], v[70:71], s[22:23]
v_pk_mul_f32 v[96:97], v[96:97], s[22:23]
v_pk_mul_f32 v[98:99], v[98:99], s[22:23]
v_pk_mul_f32 v[100:101], v[100:101], s[22:23]
v_pk_mul_f32 v[102:103], v[102:103], s[22:23]
v_pk_mul_f32 v[128:129], v[128:129], s[22:23]
v_pk_mul_f32 v[130:131], v[130:131], s[22:23]
v_pk_mul_f32 v[132:133], v[132:133], s[22:23]
v_pk_mul_f32 v[134:135], v[134:135], s[22:23]
v_pk_mul_f32 v[40:41], v[40:41], s[22:23]
v_pk_mul_f32 v[42:43], v[42:43], s[22:23]
v_pk_mul_f32 v[44:45], v[44:45], s[22:23]
v_pk_mul_f32 v[46:47], v[46:47], s[22:23]
v_pk_mul_f32 v[72:73], v[72:73], s[22:23]
v_pk_mul_f32 v[74:75], v[74:75], s[22:23]
v_pk_mul_f32 v[76:77], v[76:77], s[22:23]
v_pk_mul_f32 v[78:79], v[78:79], s[22:23]
v_pk_mul_f32 v[104:105], v[104:105], s[22:23]
v_pk_mul_f32 v[106:107], v[106:107], s[22:23]
v_pk_mul_f32 v[108:109], v[108:109], s[22:23]
v_pk_mul_f32 v[110:111], v[110:111], s[22:23]
v_pk_mul_f32 v[136:137], v[136:137], s[22:23]
v_pk_mul_f32 v[138:139], v[138:139], s[22:23]
v_pk_mul_f32 v[140:141], v[140:141], s[22:23]
v_pk_mul_f32 v[142:143], v[142:143], s[22:23]
v_pk_mul_f32 v[48:49], v[48:49], s[22:23]
v_pk_mul_f32 v[50:51], v[50:51], s[22:23]
v_pk_mul_f32 v[52:53], v[52:53], s[22:23]
v_pk_mul_f32 v[54:55], v[54:55], s[22:23]
v_pk_mul_f32 v[80:81], v[80:81], s[22:23]
v_pk_mul_f32 v[82:83], v[82:83], s[22:23]
v_pk_mul_f32 v[84:85], v[84:85], s[22:23]
v_pk_mul_f32 v[86:87], v[86:87], s[22:23]
v_pk_mul_f32 v[112:113], v[112:113], s[22:23]
v_pk_mul_f32 v[114:115], v[114:115], s[22:23]
v_pk_mul_f32 v[116:117], v[116:117], s[22:23]
v_pk_mul_f32 v[118:119], v[118:119], s[22:23]
v_pk_mul_f32 v[144:145], v[144:145], s[22:23]
v_pk_mul_f32 v[146:147], v[146:147], s[22:23]
v_pk_mul_f32 v[148:149], v[148:149], s[22:23]
v_pk_mul_f32 v[150:151], v[150:151], s[22:23]
v_pk_mul_f32 v[56:57], v[56:57], s[22:23]
v_pk_mul_f32 v[58:59], v[58:59], s[22:23]
v_pk_mul_f32 v[60:61], v[60:61], s[22:23]
v_pk_mul_f32 v[62:63], v[62:63], s[22:23]
v_pk_mul_f32 v[88:89], v[88:89], s[22:23]
v_pk_mul_f32 v[90:91], v[90:91], s[22:23]
v_pk_mul_f32 v[92:93], v[92:93], s[22:23]
v_pk_mul_f32 v[94:95], v[94:95], s[22:23]
v_pk_mul_f32 v[120:121], v[120:121], s[22:23]
v_pk_mul_f32 v[122:123], v[122:123], s[22:23]
v_pk_mul_f32 v[124:125], v[124:125], s[22:23]
v_pk_mul_f32 v[126:127], v[126:127], s[22:23]
v_pk_mul_f32 v[152:153], v[152:153], s[22:23]
v_pk_mul_f32 v[154:155], v[154:155], s[22:23]
v_pk_mul_f32 v[156:157], v[156:157], s[22:23]
v_pk_mul_f32 v[158:159], v[158:159], s[22:23]
K 32,32,33
K 33,34,35
K 34,36,37
K 35,38,39
K 40,40,41
K 41,42,43
K 42,44,45
K 43,46,47
W 0
Zvav 196,128,32
s_add_u32 m0, s62, 0
Ra 144,242,4096
Ra 148,242,5120
Ra 152,242,6144
Ra 156,242,7168
Mvav 196,132,40
K 64,64,65
K 65,66,67
K 66,68,69
K 67,70,71
K 72,72,73
K 73,74,75
K 74,76,77
K 75,78,79
s_nop 1
Zvav 204,132,72
Dc 1,8,61,0
K 96,96,97
K 97,98,99
K 98,100,101
Mvav 204,128,64
K 99,102,103
K 104,104,105
K 105,106,107
K 106,108,109
K 107,110,111
Zvav 212,128,96
Dc 1,8,61,1024
K 128,128,129
K 129,130,131
K 130,132,133
Mvav 212,132,104
K 131,134,135
K 136,136,137
K 137,138,139
K 138,140,141
K 139,142,143
s_nop 1
Zvav 220,132,136
Dc 1,8,61,2048
K 48,48,49
K 49,50,51
K 50,52,53
Mvav 220,128,128
K 51,54,55
K 56,56,57
K 57,58,59
K 58,60,61
K 59,62,63
Mvav 196,136,48
Dc 1,8,61,3072
s_add_u32 m0, s62, 4096
Ra 128,242,8192
Ra 132,242,9216
K 80,80,81
Mvav 196,140,56
K 81,82,83
K 82,84,85
K 83,86,87
K 88,88,89
K 89,90,91
K 90,92,93
K 91,94,95
s_nop 1
Mvav 204,140,88
Dc 243,8,61,0
K 112,112,113
K 113,114,115
K 114,116,117
Mvav 204,136,80
K 115,118,119
K 120,120,121
K 121,122,123
K 122,124,125
K 123,126,127
Mvav 212,136,112
Dc 243,8,61,1024
K 144,144,145
K 145,146,147
K 146,148,149
Mvav 212,140,120
K 147,150,151
K 152,152,153
K 153,154,155
K 154,156,157
K 155,158,159
s_nop 1
Mvav 220,140,152
Dc 243,8,61,2048
v_accvgpr_write_b32 a0, v32
v_accvgpr_write_b32 a1, v33
v_accvgpr_write_b32 a2, v34
Mvav 220,136,144
W 2
Dc 243,8,61,3072
s_add_u32 m0, s62, 8192
v_accvgpr_write_b32 a3, v35
v_accvgpr_write_b32 a4, v40
Zvav 68,144,64
Dc 244,8,61,0
v_accvgpr_write_b32 a5, v41
v_accvgpr_write_b32 a6, v42
v_accvgpr_write_b32 a7, v43
Mvav 68,148,72
Dc 244,8,61,1024
v_accvgpr_write_b32 a8, v48
v_accvgpr_write_b32 a9, v49
v_accvgpr_write_b32 a10, v50
Zvav 100,148,104
Dc 244,8,61,2048
Ra 136,242,10240
Ra 140,242,11264
v_accvgpr_write_b32 a11, v51
Mvav 100,144,96
Dc 244,8,61,3072
s_add_u32 m0, s62, 12288
v_accvgpr_write_b32 a12, v56
v_accvgpr_write_b32 a13, v57
v_accvgpr_write_b32 a14, v58
Zvav 132,144,128
Dc 245,8,61,0
v_accvgpr_write_b32 a15, v59
v_accvgpr_write_b32 a16, v64
v_cndmask_b32_e64 v196, v241, v196, s[72:73]
Mvav 132,148,136
Dc 245,8,61,1024
v_cndmask_b32_e64 v197, v241, v197, s[74:75]
v_cndmask_b32_e64 v198, v241, v198, s[76:77]
v_cndmask_b32_e64 v199, v241, v199, s[78:79]
Mvav 68,152,80
Dc 245,8,61,2048
v_max3_f32 v4, v196, v197, v198
v_max3_f32 v4, v4, v199, v252
v_max3_f32 v5, v204, v205, v206
Mvav 68,156,88
v_max3_f32 v5, v5, v207, v253
v_max3_f32 v6, v212, v213, v214
v_max3_f32 v6, v6, v215, v254
v_max3_f32 v7, v220, v221, v222
Mvav 100,156,120
v_max3_f32 v7, v7, v223, v255
v_mov_b32 v8, v4
v_mov_b32 v9, v5
v_mov_b32 v10, v6
Mvav 100,152,112
v_mov_b32 v11, v7
v_permlane32_swap_b32 v4, v8
v_permlane32_swap_b32 v5, v9
v_accvgpr_write_b32 a17, v65
Mvav 132,152,144
v_permlane32_swap_b32 v6, v10
v_permlane32_swap_b32 v7, v11
v_max_f32 v4, v4, v8
v_max_f32 v5, v5, v9
v_max_f32 v6, v6, v10
v_max_f32 v7, v7, v11
v_mov_b32 v8, v4
v_mov_b32 v9, v5
v_mov_b32 v10, v6
v_mov_b32 v11, v7
v_permlane16_swap_b32 v4, v8
v_permlane16_swap_b32 v5, v9
v_permlane16_swap_b32 v6, v10
v_permlane16_swap_b32 v7, v11
v_max_f32 v4, v4, v8
v_max_f32 v5, v5, v9
v_max_f32 v6, v6, v10
v_max_f32 v7, v7, v11
v_sub_f32 v4, 0, v4
v_sub_f32 v5, 0, v5
v_sub_f32 v6, 0, v6
v_sub_f32 v7, 0, v7
v_mov_b32 v224, v4
v_mov_b32 v225, v4
v_mov_b32 v226, v4
v_mov_b32 v227, v4
v_mov_b32 v228, v5
v_mov_b32 v229, v5
v_mov_b32 v230, v5
v_mov_b32 v231, v5
v_mov_b32 v232, v6
v_mov_b32 v233, v6
v_mov_b32 v234, v6
v_mov_b32 v235, v6
v_mov_b32 v236, v7
v_mov_b32 v237, v7
v_mov_b32 v238, v7
v_mov_b32 v239, v7
Mvav 132,156,152
W 0
Dc 245,8,61,3072
v_accvgpr_write_b32 a18, v66
v_accvgpr_write_b32 a19, v67
v_accvgpr_write_b32 a20, v72
Cvav 168,128,96,232
s_add_u32 s61, s61, s63
s_add_u32 s36, s29, 65536
v_accvgpr_write_b32 a21, v73
v_accvgpr_write_b32 a22, v74
v_accvgpr_write_b32 a23, v75
Mvav 168,132,104
s_and_b32 s80, s14, 1
buffer_store_dwordx4 a[192:195], v1, s[8:11], s36 offen offset:0 sc1
v_accvgpr_write_b32 a24, v80
v_accvgpr_write_b32 a25, v81
v_accvgpr_write_b32 a26, v82
Cvav 172,132,136,236
s_lshl_b32 s80, s80, 14
Ra 144,242,12288
Ra 148,242,13312
Ra 152,242,14336
Ra 156,242,15360
Mvav 172,128,128
s_add_u32 s80, s80, 98304
v_accvgpr_write_b32 a27, v83
v_accvgpr_write_b32 a28, v88
v_accvgpr_write_b32 a29, v89
v_accvgpr_write_b32 a30, v90
Mvav 168,136,112
buffer_store_dwordx4 a[200:203], v1, s[8:11], s36 offen offset:1024 sc1
v_accvgpr_write_b32 a31, v91
v_accvgpr_write_b32 a32, v96
v_accvgpr_write_b32 a33, v97
Mvav 168,140,120
v_accvgpr_write_b32 a34, v98
v_accvgpr_write_b32 a35, v99
v_accvgpr_write_b32 a36, v104
v_accvgpr_write_b32 a37, v105
Mvav 172,140,152
Ra 128,242,24576
Ra 132,242,25600
buffer_store_dwordx4 a[208:211], v1, s[8:11], s36 offen offset:2048 sc1
v_accvgpr_write_b32 a38, v106
Mvav 172,136,144
Ra 136,242,26624
Ra 140,242,27648
v_accvgpr_write_b32 a39, v107
W 4
Cvav 188,144,128,236
v_accvgpr_write_b32 a40, v112
v_accvgpr_write_b32 a41, v113
v_accvgpr_write_b32 a42, v114
v_accvgpr_write_b32 a43, v115
v_accvgpr_write_b32 a44, v120
v_accvgpr_write_b32 a45, v121
v_accvgpr_write_b32 a46, v122
v_accvgpr_write_b32 a47, v123
v_accvgpr_write_b32 a48, v128
v_accvgpr_write_b32 a49, v129
v_accvgpr_write_b32 a50, v130
v_accvgpr_write_b32 a51, v131
v_accvgpr_write_b32 a52, v136
v_accvgpr_write_b32 a53, v137
v_accvgpr_write_b32 a54, v138
v_accvgpr_write_b32 a55, v139
v_accvgpr_write_b32 a56, v144
v_accvgpr_write_b32 a57, v145
v_accvgpr_write_b32 a58, v146
v_accvgpr_write_b32 a59, v147
v_accvgpr_write_b32 a60, v152
v_accvgpr_write_b32 a61, v153
v_accvgpr_write_b32 a62, v154
v_accvgpr_write_b32 a63, v155
Mvav 188,148,136
s_barrier
buffer_store_dwordx4 a[216:219], v1, s[8:11], s36 offen offset:3072 sc1
s_add_u32 s36, s29, 69632
v_add_u32 v8, s80, v1
A 196,196,224
Mvav 188,152,144
A 197,197,224
A 198,198,224
A 199,199,224
X 196
X 197
)"
R"(X 198
K 192,196,197
X 199
A 204,204,228
K 193,198,199
A 205,205,228
A 206,206,228
A 207,207,228
X 204
X 205
X 206
K 200,204,205
X 207
A 212,212,232
K 201,206,207
A 213,213,232
A 214,214,232
A 215,215,232
X 212
X 213
X 214
K 208,212,213
X 215
A 220,220,236
K 209,214,215
A 221,221,236
A 222,222,236
A 223,223,236
X 220
X 221
X 222
K 216,220,221
X 223
v_mov_b32 v194, 0
K 217,222,223
v_mov_b32 v195, 0
v_cndmask_b32_e64 v68, v241, v68, s[72:73]
v_cndmask_b32_e64 v69, v241, v69, s[74:75]
v_cndmask_b32_e64 v70, v241, v70, s[76:77]
v_cndmask_b32_e64 v71, v241, v71, s[78:79]
A 68,68,228
A 69,69,228
A 70,70,228
A 71,71,228
X 68
X 69
X 70
K 202,68,69
X 71
A 100,100,232
K 203,70,71
A 101,101,232
A 102,102,232
A 103,103,232
X 100
X 101
X 102
K 210,100,101
X 103
A 132,132,236
K 211,102,103
A 133,133,236
A 134,134,236
A 135,135,236
X 132
X 133
X 134
K 218,132,133
X 135
s_nop 0
K 219,134,135
Mvav 188,156,152
W 0
buffer_store_dwordx4 a[224:227], v1, s[8:11], s36 offen offset:0 sc1
Ra 144,242,28672
Ra 148,242,29696
Ra 152,242,30720
Zvav 32,128,192
buffer_store_dwordx4 a[232:235], v1, s[8:11], s36 offen offset:1024 sc1
Ra 156,242,31744
Zvav 64,128,200
buffer_store_dwordx4 a[240:243], v1, s[8:11], s36 offen offset:2048 sc1
Zvav 96,128,208
s_and_b32 s80, s14, 1
s_xor_b32 s62, s62, 0x10000
buffer_store_dwordx4 a[248:251], v1, s[8:11], s36 offen offset:3072 sc1
s_add_u32 s36, s29, 393216
Zvav 128,128,216
s_lshl_b32 s80, s80, 14
s_add_u32 s80, s80, 131072
buffer_store_dwordx4 a[196:199], v1, s[8:11], s36 offen offset:0 sc1
v_add_u32 v23, 61440, v21
Zvav 132,132,216
buffer_store_dwordx4 a[204:207], v1, s[8:11], s36 offen offset:1024 sc1
v_add_u32 v12, s80, v1
Ra 64,12,8192
v_add_u32 v24, 61440, v22
Zvav 100,132,208
buffer_store_dwordx4 a[212:215], v1, s[8:11], s36 offen offset:2048 sc1
Ra 68,12,9216
Ra 72,12,10240
v_add_u32 v25, 122880, v21
Zvav 68,132,200
buffer_store_dwordx4 a[220:223], v1, s[8:11], s36 offen offset:3072 sc1
s_add_u32 s36, s29, 397312
Ra 76,12,11264
Ra 80,12,12288
Ra 84,12,13312
Zvav 36,132,192
buffer_store_dwordx4 a[228:231], v1, s[8:11], s36 offen offset:0 sc1
Ra 88,12,14336
Ra 92,12,15360
v_add_u32 v26, 122880, v22
Zvav 40,136,192
buffer_store_dwordx4 a[236:239], v1, s[8:11], s36 offen offset:1024 sc1
Ra 96,12,0
Ra 100,12,1024
Ra 104,12,2048
Zvav 72,136,200
Ra 108,12,3072
Ra 112,12,4096
buffer_store_dwordx4 a[244:247], v1, s[8:11], s36 offen offset:2048 sc1
Ra 116,12,5120
Zvav 104,136,208
Ra 120,12,6144
Ra 124,12,7168
buffer_store_dwordx4 a[252:255], v1, s[8:11], s36 offen offset:3072 sc1
Zvav 136,136,216
Zvav 140,140,216
Zvav 108,140,208
Zvav 76,140,200
s_waitcnt vmcnt(16) lgkmcnt(0)
s_barrier
v_xor_b32 v242, 0x10000, v242
ds_read_b128 v[248:251], v8
Zvav 44,140,192
Ra 128,242,40960
Zvav 48,144,192
Zvav 80,144,200
X 172
X 173
Zvav 112,144,208
Ra 132,242,41984
X 174
K 220,172,173
Zvav 144,144,216
X 175
v_cndmask_b32_e64 v168, v241, v168, s[72:73]
K 221,174,175
Zvav 148,148,216
v_cndmask_b32_e64 v169, v241, v169, s[74:75]
v_cndmask_b32_e64 v170, v241, v170, s[76:77]
v_cndmask_b32_e64 v171, v241, v171, s[78:79]
Zvav 116,148,208
X 168
X 169
Zvav 84,148,200
W 2
Ra 136,242,43008
X 170
K 212,168,169
Zvav 52,148,192
v_mov_b32 v224, v250
v_mov_b32 v225, v250
v_mov_b32 v226, v250
v_mov_b32 v227, v250
Zvav 56,152,192
v_mov_b32 v228, v251
v_mov_b32 v229, v251
v_mov_b32 v230, v251
v_mov_b32 v231, v251
Zvav 88,152,200
v_mov_b32 v232, v248
v_mov_b32 v233, v248
v_mov_b32 v234, v248
v_mov_b32 v235, v248
Zvav 120,152,208
v_mov_b32 v236, v249
v_mov_b32 v237, v249
v_mov_b32 v238, v249
v_mov_b32 v239, v249
Zvav 152,152,216
X 171
v_mov_b32 v204, 0
K 213,170,171
Zvav 156,156,216
Ra 140,242,44032
v_mov_b32 v205, 0
v_mov_b32 v196, 0
v_mov_b32 v197, 0
Zvav 124,156,208
v_mov_b32 v198, 0
v_mov_b32 v199, 0
v_mov_b32 v206, 0
v_mov_b32 v207, 0
Zvav 92,156,200
v_mov_b32 v214, 0
v_mov_b32 v215, 0
v_cndmask_b32_e64 v188, v241, v188, s[72:73]
v_cndmask_b32_e64 v189, v241, v189, s[74:75]
Zvav 60,156,192
v_cndmask_b32_e64 v190, v241, v190, s[76:77]
v_cndmask_b32_e64 v191, v241, v191, s[78:79]
X 188
X 189
X 190
K 222,188,189
X 191
s_nop 0
K 223,190,191
s_waitcnt vmcnt(0)
s_add_u32 s80, s71, 128
v_mov_b64 v[28:29], s[12:13]
s_mov_b64 exec, 1
buffer_store_dwordx2 v[28:29], off, s[8:11], s80 sc1
s_mov_b64 exec, -1
s_nop 1
s_branch .Lsp2_37%=
.p2align 12
.Lpe_48%=:
v_pk_mul_f32 v[32:33], v[32:33], s[22:23]
v_pk_mul_f32 v[34:35], v[34:35], s[22:23]
v_pk_mul_f32 v[36:37], v[36:37], s[22:23]
v_pk_mul_f32 v[38:39], v[38:39], s[22:23]
v_pk_mul_f32 v[64:65], v[64:65], s[22:23]
v_pk_mul_f32 v[66:67], v[66:67], s[22:23]
v_pk_mul_f32 v[68:69], v[68:69], s[22:23]
v_pk_mul_f32 v[70:71], v[70:71], s[22:23]
v_pk_mul_f32 v[96:97], v[96:97], s[22:23]
v_pk_mul_f32 v[98:99], v[98:99], s[22:23]
v_pk_mul_f32 v[100:101], v[100:101], s[22:23]
v_pk_mul_f32 v[102:103], v[102:103], s[22:23]
v_pk_mul_f32 v[128:129], v[128:129], s[22:23]
v_pk_mul_f32 v[130:131], v[130:131], s[22:23]
v_pk_mul_f32 v[132:133], v[132:133], s[22:23]
v_pk_mul_f32 v[134:135], v[134:135], s[22:23]
v_pk_mul_f32 v[40:41], v[40:41], s[22:23]
v_pk_mul_f32 v[42:43], v[42:43], s[22:23]
v_pk_mul_f32 v[44:45], v[44:45], s[22:23]
v_pk_mul_f32 v[46:47], v[46:47], s[22:23]
v_pk_mul_f32 v[72:73], v[72:73], s[22:23]
v_pk_mul_f32 v[74:75], v[74:75], s[22:23]
v_pk_mul_f32 v[76:77], v[76:77], s[22:23]
v_pk_mul_f32 v[78:79], v[78:79], s[22:23]
v_pk_mul_f32 v[104:105], v[104:105], s[22:23]
v_pk_mul_f32 v[106:107], v[106:107], s[22:23]
v_pk_mul_f32 v[108:109], v[108:109], s[22:23]
v_pk_mul_f32 v[110:111], v[110:111], s[22:23]
v_pk_mul_f32 v[136:137], v[136:137], s[22:23]
v_pk_mul_f32 v[138:139], v[138:139], s[22:23]
v_pk_mul_f32 v[140:141], v[140:141], s[22:23]
v_pk_mul_f32 v[142:143], v[142:143], s[22:23]
v_pk_mul_f32 v[48:49], v[48:49], s[22:23]
v_pk_mul_f32 v[50:51], v[50:51], s[22:23]
v_pk_mul_f32 v[52:53], v[52:53], s[22:23]
v_pk_mul_f32 v[54:55], v[54:55], s[22:23]
v_pk_mul_f32 v[80:81], v[80:81], s[22:23]
v_pk_mul_f32 v[82:83], v[82:83], s[22:23]
v_pk_mul_f32 v[84:85], v[84:85], s[22:23]
v_pk_mul_f32 v[86:87], v[86:87], s[22:23]
v_pk_mul_f32 v[112:113], v[112:113], s[22:23]
v_pk_mul_f32 v[114:115], v[114:115], s[22:23]
v_pk_mul_f32 v[116:117], v[116:117], s[22:23]
v_pk_mul_f32 v[118:119], v[118:119], s[22:23]
v_pk_mul_f32 v[144:145], v[144:145], s[22:23]
v_pk_mul_f32 v[146:147], v[146:147], s[22:23]
v_pk_mul_f32 v[148:149], v[148:149], s[22:23]
v_pk_mul_f32 v[150:151], v[150:151], s[22:23]
v_pk_mul_f32 v[56:57], v[56:57], s[22:23]
v_pk_mul_f32 v[58:59], v[58:59], s[22:23]
v_pk_mul_f32 v[60:61], v[60:61], s[22:23]
v_pk_mul_f32 v[62:63], v[62:63], s[22:23]
v_pk_mul_f32 v[88:89], v[88:89], s[22:23]
v_pk_mul_f32 v[90:91], v[90:91], s[22:23]
v_pk_mul_f32 v[92:93], v[92:93], s[22:23]
v_pk_mul_f32 v[94:95], v[94:95], s[22:23]
v_pk_mul_f32 v[120:121], v[120:121], s[22:23]
v_pk_mul_f32 v[122:123], v[122:123], s[22:23]
v_pk_mul_f32 v[124:125], v[124:125], s[22:23]
v_pk_mul_f32 v[126:127], v[126:127], s[22:23]
v_pk_mul_f32 v[152:153], v[152:153], s[22:23]
v_pk_mul_f32 v[154:155], v[154:155], s[22:23]
v_pk_mul_f32 v[156:157], v[156:157], s[22:23]
v_pk_mul_f32 v[158:159], v[158:159], s[22:23]
K 32,32,33
K 33,34,35
K 34,36,37
K 35,38,39
K 40,40,41
K 41,42,43
K 42,44,45
K 43,46,47
W 0
Zvav 196,128,32
s_add_u32 m0, s62, 0
Ra 144,242,4096
Ra 148,242,5120
Ra 152,242,6144
Ra 156,242,7168
Mvav 196,132,40
K 64,64,65
K 65,66,67
K 66,68,69
K 67,70,71
K 72,72,73
K 73,74,75
K 74,76,77
K 75,78,79
s_nop 1
Zvav 204,132,72
Dc 1,8,61,0
K 96,96,97
K 97,98,99
K 98,100,101
Mvav 204,128,64
K 99,102,103
K 104,104,105
K 105,106,107
K 106,108,109
K 107,110,111
Zvav 212,128,96
Dc 1,8,61,1024
K 128,128,129
K 129,130,131
K 130,132,133
Mvav 212,132,104
K 131,134,135
K 136,136,137
K 137,138,139
K 138,140,141
K 139,142,143
s_nop 1
Zvav 220,132,136
Dc 1,8,61,2048
K 48,48,49
K 49,50,51
K 50,52,53
Mvav 220,128,128
K 51,54,55
K 56,56,57
K 57,58,59
K 58,60,61
K 59,62,63
Mvav 196,136,48
Dc 1,8,61,3072
s_add_u32 m0, s62, 4096
Ra 128,242,8192
Ra 132,242,9216
K 80,80,81
Mvav 196,140,56
K 81,82,83
K 82,84,85
K 83,86,87
K 88,88,89
K 89,90,91
K 90,92,93
K 91,94,95
s_nop 1
Mvav 204,140,88
Dc 243,8,61,0
K 112,112,113
K 113,114,115
K 114,116,117
Mvav 204,136,80
K 115,118,119
K 120,120,121
K 121,122,123
K 122,124,125
K 123,126,127
Mvav 212,136,112
Dc 243,8,61,1024
K 144,144,145
K 145,146,147
K 146,148,149
Mvav 212,140,120
K 147,150,151
K 152,152,153
K 153,154,155
K 154,156,157
K 155,158,159
s_nop 1
Mvav 220,140,152
Dc 243,8,61,2048
v_accvgpr_write_b32 a0, v32
v_accvgpr_write_b32 a1, v33
v_accvgpr_write_b32 a2, v34
Mvav 220,136,144
W 2
Dc 243,8,61,3072
s_add_u32 m0, s62, 8192
v_accvgpr_write_b32 a3, v35
v_accvgpr_write_b32 a4, v40
Zvav 36,144,32
Ra 136,242,10240
Ra 140,242,11264
Dc 244,8,61,0
v_accvgpr_write_b32 a5, v41
Mvav 36,148,40
v_accvgpr_write_b32 a6, v42
v_accvgpr_write_b32 a7, v43
)"
R"(v_max3_f32 v4, v196, v197, v198
v_max3_f32 v4, v4, v199, v252
Zvav 68,148,72
v_max3_f32 v5, v204, v205, v206
v_max3_f32 v5, v5, v207, v253
v_max3_f32 v6, v212, v213, v214
v_max3_f32 v6, v6, v215, v254
Mvav 68,144,64
v_max3_f32 v7, v220, v221, v222
v_max3_f32 v7, v7, v223, v255
v_mov_b32 v8, v4
v_mov_b32 v9, v5
Zvav 100,144,96
v_mov_b32 v10, v6
v_mov_b32 v11, v7
v_permlane32_swap_b32 v4, v8
v_accvgpr_write_b32 a8, v48
Mvav 100,148,104
v_permlane32_swap_b32 v5, v9
v_permlane32_swap_b32 v6, v10
v_permlane32_swap_b32 v7, v11
v_max_f32 v4, v4, v8
Zvav 132,148,136
v_max_f32 v5, v5, v9
v_max_f32 v6, v6, v10
v_max_f32 v7, v7, v11
v_mov_b32 v8, v4
Mvav 132,144,128
Dc 244,8,61,1024
v_mov_b32 v9, v5
v_mov_b32 v10, v6
v_mov_b32 v11, v7
Mvav 36,152,48
v_permlane16_swap_b32 v4, v8
v_permlane16_swap_b32 v5, v9
v_permlane16_swap_b32 v6, v10
v_accvgpr_write_b32 a9, v49
Mvav 36,156,56
v_permlane16_swap_b32 v7, v11
v_max_f32 v4, v4, v8
v_max_f32 v5, v5, v9
v_max_f32 v6, v6, v10
Mvav 68,156,88
v_max_f32 v7, v7, v11
v_sub_f32 v4, 0, v4
v_sub_f32 v5, 0, v5
v_sub_f32 v6, 0, v6
Mvav 68,152,80
v_sub_f32 v7, 0, v7
v_mov_b32 v224, v4
v_mov_b32 v225, v4
v_mov_b32 v226, v4
Mvav 100,152,112
v_mov_b32 v227, v4
v_mov_b32 v228, v5
v_mov_b32 v229, v5
v_mov_b32 v230, v5
Mvav 100,156,120
v_mov_b32 v231, v5
v_mov_b32 v232, v6
v_mov_b32 v233, v6
v_mov_b32 v234, v6
Mvav 132,156,152
v_mov_b32 v235, v6
v_mov_b32 v236, v7
v_mov_b32 v237, v7
v_mov_b32 v238, v7
v_mov_b32 v239, v7
Mvav 132,152,144
W 0
Dc 244,8,61,2048
v_accvgpr_write_b32 a10, v50
v_accvgpr_write_b32 a11, v51
v_accvgpr_write_b32 a12, v56
Cvav 160,128,32,224
Dc 244,8,61,3072
s_add_u32 m0, s62, 12288
v_accvgpr_write_b32 a13, v57
v_accvgpr_write_b32 a14, v58
v_accvgpr_write_b32 a15, v59
Mvav 160,132,40
s_and_b32 s80, s14, 1
Ra 144,242,12288
Ra 148,242,13312
v_accvgpr_write_b32 a16, v64
v_accvgpr_write_b32 a17, v65
Cvav 164,132,72,228
s_lshl_b32 s80, s80, 14
Dc 245,8,61,0
v_accvgpr_write_b32 a18, v66
v_accvgpr_write_b32 a19, v67
v_accvgpr_write_b32 a20, v72
Mvav 164,128,64
s_add_u32 s80, s80, 98304
Dc 245,8,61,1024
v_accvgpr_write_b32 a21, v73
v_accvgpr_write_b32 a22, v74
v_accvgpr_write_b32 a23, v75
Cvav 168,128,96,232
Dc 245,8,61,2048
v_accvgpr_write_b32 a24, v80
v_accvgpr_write_b32 a25, v81
v_accvgpr_write_b32 a26, v82
Mvav 168,132,104
Dc 245,8,61,3072
s_add_u32 s61, s61, s63
s_add_u32 s36, s29, 65536
A 196,196,224
v_accvgpr_write_b32 a27, v83
Cvav 172,132,136,236
Ra 152,242,14336
A 197,197,224
A 198,198,224
v_accvgpr_write_b32 a28, v88
Mvav 172,128,128
buffer_store_dwordx4 a[192:195], v1, s[8:11], s36 offen offset:0 sc1
Ra 156,242,15360
A 199,199,224
v_accvgpr_write_b32 a29, v89
Mvav 160,136,48
X 196
v_accvgpr_write_b32 a30, v90
v_accvgpr_write_b32 a31, v91
Mvav 160,140,56
X 197
X 198
Mvav 164,140,88
buffer_store_dwordx4 a[200:203], v1, s[8:11], s36 offen offset:1024 sc1
v_accvgpr_write_b32 a32, v96
K 192,196,197
v_accvgpr_write_b32 a33, v97
Mvav 164,136,80
X 199
A 204,204,228
v_accvgpr_write_b32 a34, v98
Mvav 168,136,112
K 193,198,199
v_accvgpr_write_b32 a35, v99
A 205,205,228
A 206,206,228
Mvav 168,140,120
buffer_store_dwordx4 a[208:211], v1, s[8:11], s36 offen offset:2048 sc1
v_accvgpr_write_b32 a36, v104
v_add_u32 v8, s80, v1
A 207,207,228
Mvav 172,140,152
X 204
v_accvgpr_write_b32 a37, v105
v_accvgpr_write_b32 a38, v106
Mvav 172,136,144
W 0
X 205
X 206
Cvav 176,144,32,224
Ra 128,242,24576
v_accvgpr_write_b32 a39, v107
v_accvgpr_write_b32 a40, v112
K 200,204,205
Mvav 176,148,40
buffer_store_dwordx4 a[216:219], v1, s[8:11], s36 offen offset:3072 sc1
s_add_u32 s36, s29, 69632
X 207
v_accvgpr_write_b32 a41, v113
Cvav 180,148,72,228
Ra 132,242,25600
A 212,212,232
v_accvgpr_write_b32 a42, v114
K 201,206,207
Mvav 180,144,64
A 213,213,232
v_accvgpr_write_b32 a43, v115
A 214,214,232
A 215,215,232
Cvav 184,144,96,232
v_accvgpr_write_b32 a44, v120
X 212
v_accvgpr_write_b32 a45, v121
Mvav 184,148,104
buffer_store_dwordx4 a[224:227], v1, s[8:11], s36 offen offset:0 sc1
X 213
v_accvgpr_write_b32 a46, v122
Cvav 188,148,136,236
X 214
v_accvgpr_write_b32 a47, v123
K 208,212,213
Mvav 188,144,128
v_accvgpr_write_b32 a48, v128
X 215
v_accvgpr_write_b32 a49, v129
Mvav 176,152,48
buffer_store_dwordx4 a[232:235], v1, s[8:11], s36 offen offset:1024 sc1
A 220,220,236
v_accvgpr_write_b32 a50, v130
K 209,214,215
Mvav 176,156,56
A 221,221,236
v_accvgpr_write_b32 a51, v131
A 222,222,236
A 223,223,236
Mvav 180,156,88
Ra 136,242,26624
Ra 140,242,27648
v_accvgpr_write_b32 a52, v136
v_accvgpr_write_b32 a53, v137
Mvav 180,152,80
X 220
X 221
Mvav 184,152,112
v_accvgpr_write_b32 a54, v138
v_accvgpr_write_b32 a55, v139
v_accvgpr_write_b32 a56, v144
v_accvgpr_write_b32 a57, v145
v_accvgpr_write_b32 a58, v146
v_accvgpr_write_b32 a59, v147
v_accvgpr_write_b32 a60, v152
v_accvgpr_write_b32 a61, v153
v_accvgpr_write_b32 a62, v154
v_accvgpr_write_b32 a63, v155
Mvav 184,156,120
s_barrier
buffer_store_dwordx4 a[240:243], v1, s[8:11], s36 offen offset:2048 sc1
X 222
K 216,220,221
Mvav 188,156,152
X 223
A 36,36,224
K 217,222,223
A 37,37,224
A 38,38,224
A 39,39,224
X 36
X 37
X 38
K 194,36,37
X 39
A 68,68,228
K 195,38,39
A 69,69,228
A 70,70,228
A 71,71,228
X 68
X 69
X 70
K 202,68,69
X 71
A 100,100,232
K 203,70,71
A 101,101,232
A 102,102,232
A 103,103,232
X 100
X 101
X 102
K 210,100,101
X 103
A 132,132,236
K 211,102,103
A 133,133,236
A 134,134,236
A 135,135,236
X 132
X 133
X 134
K 218,132,133
X 135
s_nop 0
K 219,134,135
Mvav 188,152,144
W 0
buffer_store_dwordx4 a[248:251], v1, s[8:11], s36 offen offset:3072 sc1
s_add_u32 s36, s29, 393216
Ra 144,242,28672
Ra 148,242,29696
Zvav 32,128,192
buffer_store_dwordx4 a[196:199], v1, s[8:11], s36 offen offset:0 sc1
Ra 152,242,30720
Ra 156,242,31744
Zvav 64,128,200
buffer_store_dwordx4 a[204:207], v1, s[8:11], s36 offen offset:1024 sc1
Zvav 96,128,208
s_and_b32 s80, s14, 1
s_xor_b32 s62, s62, 0x10000
buffer_store_dwordx4 a[212:215], v1, s[8:11], s36 offen offset:2048 sc1
Zvav 128,128,216
s_lshl_b32 s80, s80, 14
s_add_u32 s80, s80, 131072
buffer_store_dwordx4 a[220:223], v1, s[8:11], s36 offen offset:3072 sc1
s_add_u32 s36, s29, 397312
v_add_u32 v23, 61440, v21
Zvav 132,132,216
buffer_store_dwordx4 a[228:231], v1, s[8:11], s36 offen offset:0 sc1
v_add_u32 v12, s80, v1
Ra 64,12,8192
v_add_u32 v24, 61440, v22
Zvav 100,132,208
Ra 68,12,9216
Ra 72,12,10240
buffer_store_dwordx4 a[236:239], v1, s[8:11], s36 offen offset:1024 sc1
v_add_u32 v25, 122880, v21
Zvav 68,132,200
Ra 76,12,11264
Ra 80,12,12288
Ra 84,12,13312
v_add_u32 v26, 122880, v22
Zvav 36,132,192
Ra 88,12,14336
Ra 92,12,15360
Ra 96,12,0
Ra 100,12,1024
Zvav 40,136,192
Ra 104,12,2048
buffer_store_dwordx4 a[244:247], v1, s[8:11], s36 offen offset:2048 sc1
Ra 108,12,3072
Ra 112,12,4096
Zvav 72,136,200
Ra 116,12,5120
Ra 120,12,6144
X 172
Zvav 104,136,208
Ra 124,12,7168
buffer_store_dwordx4 a[252:255], v1, s[8:11], s36 offen offset:3072 sc1
X 173
Zvav 136,136,216
X 174
K 220,172,173
Zvav 140,140,216
X 175
X 168
Zvav 108,140,208
K 221,174,175
X 169
Zvav 76,140,200
s_waitcnt vmcnt(16) lgkmcnt(0)
s_barrier
v_xor_b32 v242, 0x10000, v242
ds_read_b128 v[248:251], v8
Zvav 44,140,192
Ra 128,242,0
X 170
K 212,168,169
Zvav 48,144,192
X 171
X 164
Zvav 80,144,200
K 213,170,171
X 165
Zvav 112,144,208
Ra 132,242,1024
X 166
K 204,164,165
Zvav 144,144,216
X 167
X 160
Zvav 148,148,216
K 205,166,167
X 161
Zvav 116,148,208
Ra 136,242,2048
X 162
Zvav 84,148,200
W 3
K 196,160,161
X 163
Zvav 52,148,192
X 176
K 197,162,163
Zvav 56,152,192
Ra 140,242,3072
X 177
Zvav 88,152,200
X 178
K 198,176,177
Zvav 120,152,208
X 179
X 180
Zvav 152,152,216
K 199,178,179
Zvav 156,156,216
X 181
X 182
Zvav 124,156,208
K 206,180,181
X 183
Zvav 92,156,200
K 207,182,183
Zvav 60,156,192
s_waitcnt vmcnt(0)
s_add_u32 s80, s71, 128
v_mov_b64 v[28:29], s[12:13]
s_mov_b64 exec, 1
buffer_store_dwordx2 v[28:29], off, s[8:11], s80 sc1
s_mov_b64 exec, -1
s_branch .Lsp1_36%=
.p2align 12
.Lmoe_44%=:
W 0
Cvaa 160,128,0,224
s_cmp_eq_u32 s14, 0
s_cselect_b32 s20, s30, s32
s_cselect_b32 s21, s31, s33
)"
R"(Ra 144,242,4096
Ra 148,242,5120
Mvaa 160,132,4
s_xor_b32 s62, s62, 0x10000
Ra 152,242,6144
Cvaa 164,132,20,228
X 184
Mvaa 164,128,16
s_and_b32 s21, s21, 0xffff
X 185
Cvaa 168,128,32,232
X 186
Mvaa 168,132,36
Ra 156,242,7168
K 214,184,185
Cvaa 172,132,52,236
s_mov_b32 s22, 2211840
X 187
Mvaa 172,128,48
X 188
Mvaa 160,136,8
Ra 128,242,16384
Mvaa 160,140,12
s_mov_b32 s23, 0x20000
K 215,186,187
Mvaa 164,140,28
X 189
Mvaa 164,136,24
X 190
Mvaa 168,136,40
Ra 132,242,17408
v_lshrrev_b32 v4, 6, v1
Mvaa 168,140,44
K 222,188,189
Mvaa 172,140,60
X 191
Mvaa 172,136,56
v_mul_u32_u24 v4, 3456, v4
K 223,190,191
v_smfmac_f32_16x16x64_bf16 a[160:163], a[176:179], v[192:199], v240
Ra 136,242,18432
v_smfmac_f32_16x16x64_bf16 a[164:167], a[176:179], v[200:207], v240
X 160
v_smfmac_f32_16x16x64_bf16 a[168:171], a[176:179], v[208:215], v240
v_lshrrev_b32 v5, 4, v1
v_smfmac_f32_16x16x64_bf16 a[172:175], a[176:179], v[216:223], v240
W 3
X 161
Cvaa 176,144,0,224
X 162
Mvaa 176,148,4
Ra 140,242,19456
v_xor_b32 v5, v5, v1
Cvaa 180,148,20,228
K 192,160,161
Mvaa 180,144,16
X 163
Cvaa 184,144,32,232
v_and_b32 v5, 32, v5
Mvaa 184,148,36
X 164
Cvaa 188,148,52,236
K 193,162,163
v_and_b32 v6, 16, v1
Mvaa 188,144,48
X 165
Mvaa 176,152,8
X 166
Mvaa 176,156,12
Ra 144,242,20480
Ra 148,242,21504
Mvaa 180,156,28
v_add3_u32 v4, v4, v5, v6
K 200,164,165
Mvaa 180,152,24
X 167
Mvaa 184,152,40
X 168
Mvaa 184,156,44
v_add_u32 v5, 54272, v4
K 201,166,167
Mvaa 188,156,60
X 169
Mvaa 188,152,56
W 2
Ra 152,242,22528
v_add_u32 v6, 108544, v4
Mvav 32,128,196
X 170
Mvav 64,128,204
Ra 156,242,23552
v_add_u32 v7, 162816, v4
Mvav 96,128,212
K 208,168,169
Mvav 128,128,220
X 171
Mvav 132,132,220
s_lshl_b32 s94, s85, 6
X 172
Mvav 100,132,212
s_add_u32 s64, s64, 1
Ra 128,242,8192
K 209,170,171
Mvav 68,132,204
X 173
Mvav 36,132,196
s_mul_i32 s95, s14, 20480
Mvav 40,136,196
X 174
Mvav 72,136,204
Ra 132,242,9216
K 216,172,173
Mvav 104,136,212
s_add_u32 s95, s95, 98304
X 175
Mvav 136,136,220
Mvav 140,140,220
s_add_u32 m0, s95, 0
X 176
Mvav 108,140,212
Ra 136,242,10240
K 217,174,175
Mvav 76,140,204
X 177
Mvav 44,140,196
W 3
D 4,20,94,0
Mvav 48,144,196
X 178
Mvav 80,144,204
Ra 140,242,11264
Mvav 112,144,212
D 5,20,94,1024
K 194,176,177
Mvav 144,144,220
X 179
Mvav 148,148,220
X 180
Mvav 116,148,212
Ra 144,242,12288
D 6,20,94,2048
Mvav 84,148,204
K 195,178,179
Mvav 52,148,196
X 181
Mvav 56,152,196
D 7,20,94,3072
Ra 148,242,13312
Mvav 88,152,204
X 182
Mvav 120,152,212
s_add_u32 s94, s94, 221184
K 202,180,181
Mvav 152,152,220
X 183
Mvav 156,156,220
X 184
Mvav 124,156,212
s_add_u32 m0, s95, 4096
Ra 152,242,14336
Mvav 92,156,204
K 203,182,183
Mvav 60,156,196
W 3
X 185
Cvaa 160,128,0,224
D 4,20,94,0
Ra 156,242,15360
Mvaa 160,132,4
X 186
Cvaa 164,132,20,228
K 210,184,185
Mvaa 164,128,16
X 187
Cvaa 168,128,32,232
D 5,20,94,1024
Mvaa 168,132,36
X 188
Cvaa 172,132,52,236
D 6,20,94,2048
K 211,186,187
Mvaa 172,128,48
X 189
Mvaa 160,136,8
Ra 128,242,24576
Ra 132,242,25600
Mvaa 160,140,12
X 190
Mvaa 164,140,28
D 7,20,94,3072
K 218,188,189
Mvaa 164,136,24
s_add_u32 s94, s94, 221184
X 191
Mvaa 168,136,40
Mvaa 168,140,44
X 160
Mvaa 172,140,60
s_add_u32 m0, s95, 8192
K 219,190,191
Mvaa 172,136,56
W 2
X 161
Cvaa 176,144,0,224
Ra 136,242,26624
Ra 140,242,27648
Mvaa 176,148,4
X 162
Cvaa 180,148,20,228
D 4,20,94,0
K 196,160,161
Mvaa 180,144,16
X 163
Cvaa 184,144,32,232
D 5,20,94,1024
Mvaa 184,148,36
X 164
Cvaa 188,148,52,236
K 197,162,163
Mvaa 188,144,48
D 6,20,94,2048
Ra 144,242,28672
Mvaa 176,152,8
X 165
Mvaa 176,156,12
Ra 148,242,29696
D 7,20,94,3072
Mvaa 180,156,28
X 166
Mvaa 180,152,24
K 204,164,165
Mvaa 184,152,40
s_add_u32 s94, s94, 221184
X 167
Mvaa 184,156,44
X 168
Mvaa 188,156,60
Mvaa 188,152,56
W 2
s_add_u32 m0, s95, 12288
K 205,166,167
Mvav 32,128,192
X 169
Mvav 64,128,200
Ra 152,242,30720
Ra 156,242,31744
Mvav 96,128,208
X 170
Mvav 128,128,216
D 4,20,94,0
K 212,168,169
Mvav 132,132,216
X 171
Mvav 100,132,208
D 5,20,94,1024
Mvav 68,132,200
X 172
Mvav 36,132,192
K 213,170,171
Mvav 40,136,192
X 173
Mvav 72,136,200
D 6,20,94,2048
Mvav 104,136,208
X 174
Mvav 136,136,216
D 7,20,94,3072
K 220,172,173
Mvav 140,140,216
X 175
Mvav 108,140,208
s_add_u32 s94, s94, 221184
X 176
Mvav 76,140,200
W 0
s_barrier
v_xor_b32 v242, 0x10000, v242
Mvav 44,140,192
Ra 128,242,0
K 221,174,175
Mvav 48,144,192
X 177
Mvav 80,144,200
v_bfe_u32 v11, v1, 4, 3
Mvav 112,144,208
X 178
Mvav 144,144,216
Ra 132,242,1024
v_lshlrev_b32 v11, 3, v11
Mvav 148,148,216
K 198,176,177
Mvav 116,148,208
X 179
Mvav 84,148,200
X 180
Mvav 52,148,192
Ra 136,242,2048
buffer_load_dwordx2 v[8:9], v11, s[8:11], 0 offen offset:3840 sc1
Mvav 56,152,192
Ra 140,242,3072
K 199,178,179
Mvav 88,152,200
X 181
Mvav 120,152,208
X 182
Mvav 152,152,216
K 206,180,181
Mvav 156,156,216
X 183
Mvav 124,156,208
Mvav 92,156,200
K 207,182,183
Mvav 60,156,192
W 0
Cvaa 160,128,0,224
s_add_u32 m0, s95, 16384
Ra 144,242,4096
Ra 148,242,5120
Mvaa 160,132,4
s_xor_b32 s62, s62, 0x10000
Ra 152,242,6144
Cvaa 164,132,20,228
X 184
Mvaa 164,128,16
X 185
Cvaa 168,128,32,232
X 186
Mvaa 168,132,36
D 4,20,94,0
K 214,184,185
Cvaa 172,132,52,236
Ra 156,242,7168
Mvaa 172,128,48
X 187
Mvaa 160,136,8
X 188
Mvaa 160,140,12
D 5,20,94,1024
Ra 128,242,16384
Mvaa 164,140,28
K 215,186,187
Mvaa 164,136,24
X 189
Mvaa 168,136,40
X 190
Mvaa 168,140,44
D 6,20,94,2048
Ra 132,242,17408
Mvaa 172,140,60
K 222,188,189
Mvaa 172,136,56
X 191
v_smfmac_f32_16x16x64_bf16 a[160:163], a[176:179], v[192:199], v240
Ra 136,242,18432
K 223,190,191
v_smfmac_f32_16x16x64_bf16 a[164:167], a[176:179], v[200:207], v240
D 7,20,94,3072
v_smfmac_f32_16x16x64_bf16 a[168:171], a[176:179], v[208:215], v240
s_add_u32 s94, s94, 221184
v_mul_u32_u24 v10, 160, v11
v_smfmac_f32_16x16x64_bf16 a[172:175], a[176:179], v[216:223], v240
W 3
X 160
Cvaa 176,144,0,224
X 161
Mvaa 176,148,4
Ra 140,242,19456
v_lshrrev_b32 v12, 7, v1
Cvaa 180,148,20,228
X 162
Mvaa 180,144,16
K 192,160,161
v_lshl_add_u32 v10, v12, 4, v10
Cvaa 184,144,32,232
s_mul_i32 s82, s14, 5120
X 163
Mvaa 184,148,36
X 164
Cvaa 188,148,52,236
K 193,162,163
Mvaa 188,144,48
s_mul_i32 s81, s14, 640
X 165
Mvaa 176,152,8
X 166
Mvaa 176,156,12
s_add_u32 s81, s81, 118784
Ra 144,242,20480
Ra 148,242,21504
Mvaa 180,156,28
K 200,164,165
Mvaa 180,152,24
X 167
Mvaa 184,152,40
s_waitcnt vmcnt(4)
v_cmp_eq_u64 vcc, s[12:13], v[8:9]
s_cmp_eq_u64 vcc, -1
s_cbranch_scc0 .Lspin_51%=
.Lok_52%=:
X 168
)"
R"(Mvaa 184,156,44
K 201,166,167
Mvaa 188,156,60
s_add_u32 m0, s82, 53248
X 169
Mvaa 188,152,56
W 2
Ra 152,242,22528
Mvav 32,128,196
X 170
Mvav 64,128,204
Ra 156,242,23552
Dc 10,8,81,0
Mvav 96,128,212
K 208,168,169
Mvav 128,128,220
X 171
Mvav 132,132,220
s_add_u32 m0, s82, 54144
X 172
Mvav 100,132,212
s_add_u32 s64, s64, 1
Ra 128,242,8192
K 209,170,171
Mvav 68,132,204
X 173
Mvav 36,132,196
Dc 10,8,81,128
Mvav 40,136,196
X 174
Mvav 72,136,204
s_add_u32 m0, s82, 55040
Ra 132,242,9216
K 216,172,173
Mvav 104,136,212
X 175
Mvav 136,136,220
Dc 10,8,81,256
Mvav 140,140,220
X 176
Mvav 108,140,212
Ra 136,242,10240
K 217,174,175
Mvav 76,140,204
s_add_u32 m0, s82, 55936
X 177
Mvav 44,140,196
W 3
Mvav 48,144,196
X 178
Mvav 80,144,204
Ra 140,242,11264
Dc 10,8,81,384
Mvav 112,144,212
K 194,176,177
Mvav 144,144,220
s_add_u32 m0, s82, 56832
X 179
Mvav 148,148,220
X 180
Mvav 116,148,212
Ra 144,242,12288
Mvav 84,148,204
Dc 10,8,81,512
K 195,178,179
Mvav 52,148,196
X 181
Mvav 56,152,196
X 182
Mvav 88,152,204
Ra 148,242,13312
Mvav 120,152,212
K 202,180,181
Mvav 152,152,220
X 183
Mvav 156,156,220
X 184
Mvav 124,156,212
Ra 152,242,14336
Mvav 92,156,204
K 203,182,183
Mvav 60,156,196
W 3
X 185
Cvaa 160,128,0,224
X 186
Mvaa 160,132,4
Ra 156,242,15360
Cvaa 164,132,20,228
K 210,184,185
Mvaa 164,128,16
X 187
Cvaa 168,128,32,232
X 188
Mvaa 168,132,36
Cvaa 172,132,52,236
K 211,186,187
Mvaa 172,128,48
X 189
Mvaa 160,136,8
Ra 128,242,24576
Ra 132,242,25600
Mvaa 160,140,12
X 190
Mvaa 164,140,28
K 218,188,189
Mvaa 164,136,24
X 191
Mvaa 168,136,40
Mvaa 168,140,44
Mvaa 172,140,60
X 160
Mvaa 172,136,56
W 2
Ra 136,242,26624
K 219,190,191
Cvaa 176,144,0,224
X 161
Mvaa 176,148,4
Ra 140,242,27648
v_lshrrev_b32 v8, 8, v1
Cvaa 180,148,20,228
X 162
Mvaa 180,144,16
K 196,160,161
Cvaa 184,144,32,232
X 163
Mvaa 184,148,36
v_bfe_u32 v9, v1, 6, 2
Cvaa 188,148,52,236
X 164
Mvaa 188,144,48
K 197,162,163
v_lshl_add_u32 v9, v8, 3, v9
Mvaa 176,152,8
X 165
Mvaa 176,156,12
Ra 144,242,28672
Ra 148,242,29696
Mvaa 180,156,28
X 166
Mvaa 180,152,24
K 204,164,165
v_lshlrev_b32 v11, 6, v9
Mvaa 184,152,40
X 167
Mvaa 184,156,44
X 168
Mvaa 188,156,60
v_xor_b32 v9, s14, v8
Mvaa 188,152,56
W 2
Ra 152,242,30720
K 205,166,167
Mvav 32,128,192
X 169
Mvav 64,128,200
Ra 156,242,31744
v_and_b32 v9, 1, v9
Mvav 96,128,208
X 170
Mvav 128,128,216
K 212,168,169
Mvav 132,132,216
X 171
Mvav 100,132,208
v_lshl_add_u32 v11, v9, 5, v11
Mvav 68,132,200
X 172
Mvav 36,132,192
K 213,170,171
Mvav 40,136,192
X 173
Mvav 72,136,200
v_bfe_u32 v9, v1, 4, 2
Mvav 104,136,208
X 174
Mvav 136,136,216
K 220,172,173
v_lshl_add_u32 v12, v9, 3, v11
Mvav 140,140,216
X 175
Mvav 108,140,208
X 176
Mvav 76,140,200
s_waitcnt vmcnt(0) lgkmcnt(0)
s_barrier
v_xor_b32 v242, 0x10000, v242
Mvav 44,140,192
v_add_u32 v11, 98304, v12
K 221,174,175
Mvav 48,144,192
Ra 128,242,0
Mvav 80,144,200
X 177
Mvav 112,144,208
X 178
Mvav 144,144,216
Ra 132,242,1024
v_and_b32 v13, 0x70, v1
Mvav 148,148,216
K 198,176,177
Mvav 116,148,208
X 179
Mvav 84,148,200
Ra 136,242,2048
v_lshl_add_u32 v13, v8, 7, v13
Mvav 52,148,192
X 180
Mvav 56,152,192
Ra 140,242,3072
K 199,178,179
Mvav 88,152,200
X 181
Mvav 120,152,208
X 182
Mvav 152,152,216
K 206,180,181
Mvav 156,156,216
X 183
Mvav 124,156,208
Mvav 92,156,200
K 207,182,183
Mvav 60,156,192
W 0
Cvaa 160,128,0,224
s_cmp_eq_u32 s14, 0
s_cselect_b32 s95, 106496, 0
Ra 144,242,4096
Ra 148,242,5120
Mvaa 160,132,4
s_xor_b32 s62, s62, 0x10000
X 184
Cvaa 164,132,20,228
Ta 184,11,0
Ta 186,11,256
Mvaa 164,128,16
s_add_u32 s95, s95, 32768
Ta 188,11,20480
Ta 190,11,20736
Cvaa 168,128,32,232
Ra 192,13,53248
Ta 196,11,2048
Mvaa 168,132,36
s_add_u32 m0, s95, 0
X 185
Cvaa 172,132,52,236
Ta 198,11,2304
Ta 200,11,22528
Mvaa 172,128,48
W 3
X 186
Zaaa 232,184,192
Ta 202,11,22784
K 214,184,185
Zaaa 236,188,192
Ra 204,13,53760
Ra 152,242,6144
Mvaa 160,136,8
X 187
Mvaa 160,140,12
Ta 208,11,4096
D 4,20,94,0
Mvaa 164,140,28
X 188
Mvaa 164,136,24
Ta 210,11,4352
D 5,20,94,1024
Mvaa 168,136,40
Ra 156,242,7168
K 215,186,187
Mvaa 168,140,44
X 189
Mvaa 172,140,60
Ta 212,11,24576
D 6,20,94,2048
Mvaa 172,136,56
X 190
v_smfmac_f32_16x16x64_bf16 a[160:163], a[176:179], v[192:199], v240
W 5
Ta 214,11,24832
D 7,20,94,3072
Maaa 232,196,204
s_add_u32 s94, s94, 221184
Ra 216,13,54272
K 222,188,189
Maaa 236,200,204
s_add_u32 m0, s95, 4096
X 191
v_smfmac_f32_16x16x64_bf16 a[164:167], a[176:179], v[200:207], v240
X 160
K 223,190,191
v_smfmac_f32_16x16x64_bf16 a[168:171], a[176:179], v[208:215], v240
Ra 128,242,16384
Ta 220,11,6144
v_smfmac_f32_16x16x64_bf16 a[172:175], a[176:179], v[216:223], v240
W 5
Ta 222,11,6400
Ra 132,242,17408
Cvaa 176,144,0,224
X 161
Mvaa 176,148,4
D 4,20,94,0
Ta 224,11,26624
Cvaa 180,148,20,228
X 162
Mvaa 180,144,16
D 5,20,94,1024
K 192,160,161
Cvaa 184,144,32,232
Ta 226,11,26880
Ra 228,13,54784
Mvaa 184,148,36
W 7
X 163
Maaa 232,208,216
D 6,20,94,2048
Ra 136,242,18432
Maaa 236,212,216
X 164
Cvaa 188,148,52,236
D 7,20,94,3072
Ta 184,11,8192
Mvaa 188,144,48
s_add_u32 s94, s94, 221184
Ra 140,242,19456
K 193,162,163
Mvaa 176,152,8
s_add_u32 m0, s95, 8192
X 165
Mvaa 176,156,12
Ta 186,11,8448
Ta 188,11,28672
Mvaa 180,156,28
X 166
Mvaa 180,152,24
Ra 144,242,20480
K 200,164,165
Mvaa 184,152,40
X 167
Mvaa 184,156,44
Ta 190,11,28928
D 4,20,94,0
Mvaa 188,156,60
W 7
X 168
Maaa 232,220,228
s_add_u32 s64, s64, 1
Ra 192,13,55296
D 5,20,94,1024
Maaa 236,224,228
Ta 196,11,10240
K 201,166,167
Mvaa 188,152,56
W 6
X 169
Mvav 32,128,196
D 6,20,94,2048
Ra 148,242,21504
Mvav 64,128,204
X 170
Mvav 96,128,212
Ta 198,11,10496
D 7,20,94,3072
Mvav 128,128,220
s_add_u32 s94, s94, 221184
Ta 200,11,30720
K 208,168,169
Mvav 132,132,220
s_add_u32 m0, s95, 12288
X 171
Mvav 100,132,212
Ra 152,242,22528
Ta 202,11,30976
Mvav 68,132,204
X 172
Mvav 36,132,196
W 6
Ra 204,13,55808
K 209,170,171
Maaa 232,184,192
X 173
Maaa 236,188,192
D 4,20,94,0
Ra 156,242,23552
Mvav 40,136,196
X 174
Mvav 72,136,204
Ta 208,11,12288
D 5,20,94,1024
Mvav 104,136,212
Ta 210,11,12544
K 216,172,173
Mvav 136,136,220
X 175
Mvav 140,140,220
D 6,20,94,2048
Ra 128,242,8192
Mvav 108,140,212
Ta 212,11,32768
)"
R"(Ta 214,11,33024
Mvav 76,140,204
X 176
Mvav 44,140,196
W 5
D 7,20,94,3072
K 217,174,175
Mvav 48,144,196
s_add_u32 s94, s94, 221184
X 177
Maaa 232,196,204
s_add_u32 m0, s95, 16384
Ra 132,242,9216
Ra 216,13,56320
Maaa 236,200,204
X 178
Mvav 80,144,204
Ta 220,11,14336
K 194,176,177
Mvav 112,144,212
X 179
Mvav 144,144,220
D 4,20,94,0
Ta 222,11,14592
Mvav 148,148,220
Ra 136,242,10240
Ta 224,11,34816
Mvav 116,148,212
X 180
Mvav 84,148,204
D 5,20,94,1024
K 195,178,179
Mvav 52,148,196
X 181
Mvav 56,152,196
D 6,20,94,2048
Ta 226,11,35072
Mvav 88,152,204
W 5
Ra 140,242,11264
Ra 228,13,56832
Maaa 232,208,216
X 182
Maaa 236,212,216
D 7,20,94,3072
K 202,180,181
Mvav 120,152,212
s_add_u32 s94, s94, 221184
X 183
Mvav 152,152,220
Ta 184,11,16384
Ra 144,242,12288
Mvav 156,156,220
X 184
Mvav 124,156,212
Ta 186,11,16640
K 203,182,183
Mvav 92,156,204
X 185
Mvav 60,156,196
W 4
Ta 188,11,36864
Ra 148,242,13312
Cvaa 160,128,0,224
X 186
Mvaa 160,132,4
Ta 190,11,37120
K 210,184,185
Cvaa 164,132,20,228
W 6
Ra 192,13,57344
Ta 196,11,18432
Maaa 232,220,228
X 187
Maaa 236,224,228
X 188
Mvaa 164,128,16
Ra 152,242,14336
K 211,186,187
Cvaa 168,128,32,232
Ta 198,11,18688
Ta 200,11,38912
Mvaa 168,132,36
X 189
Cvaa 172,132,52,236
X 190
Mvaa 172,128,48
Ra 156,242,15360
K 218,188,189
Mvaa 160,136,8
Ta 202,11,39168
Ra 204,13,57856
Mvaa 160,140,12
X 191
Mvaa 164,140,28
Ra 128,242,24576
Mvaa 164,136,24
W 8
Maaa 232,184,192
Ra 132,242,25600
Maaa 236,188,192
X 160
Mvaa 168,136,40
K 219,190,191
Mvaa 168,140,44
X 161
Mvaa 172,140,60
X 162
Mvaa 172,136,56
W 4
Ra 136,242,26624
K 196,160,161
Cvaa 176,144,0,224
X 163
Mvaa 176,148,4
X 164
Cvaa 180,148,20,228
Ra 140,242,27648
K 197,162,163
Mvaa 180,144,16
X 165
Cvaa 184,144,32,232
W 4
Maaa 232,196,204
X 166
Maaa 236,200,204
Mvaa 184,148,36
K 204,164,165
Cvaa 188,148,52,236
X 167
Mvaa 188,144,48
Ra 144,242,28672
Mvaa 176,152,8
X 168
Mvaa 176,156,12
Ra 148,242,29696
Mvaa 180,156,28
K 205,166,167
Mvaa 180,152,24
X 169
Mvaa 184,152,40
Mvaa 184,156,44
X 170
Mvaa 188,156,60
K 212,168,169
Mvaa 188,152,56
W 2
X 171
Mvav 32,128,192
Ra 152,242,30720
Ra 156,242,31744
Mvav 64,128,200
X 172
Mvav 96,128,208
Mvav 128,128,216
K 213,170,171
Mvav 132,132,216
X 173
Mvav 100,132,208
Mvav 68,132,200
X 174
Mvav 36,132,192
Mvav 40,136,192
K 220,172,173
Mvav 72,136,200
X 175
Mvav 104,136,208
Mvav 136,136,216
X 176
Mvav 140,140,216
Mvav 108,140,208
K 221,174,175
Mvav 76,140,200
s_waitcnt vmcnt(0) lgkmcnt(0)
s_barrier
v_xor_b32 v242, 0x10000, v242
Mvav 44,140,192
X 177
Mvav 48,144,192
X 178
Mvav 80,144,200
Ra 128,242,0
K 198,176,177
Mvav 112,144,208
X 179
Mvav 144,144,216
Ra 132,242,1024
Mvav 148,148,216
X 180
Mvav 116,148,208
Ra 136,242,2048
Mvav 84,148,200
K 199,178,179
Mvav 52,148,192
X 181
Mvav 56,152,192
Ra 140,242,3072
Mvav 88,152,200
X 182
Mvav 120,152,208
Mvav 152,152,216
K 206,180,181
Mvav 156,156,216
X 183
Mvav 124,156,208
Mvav 92,156,200
K 207,182,183
Mvav 60,156,192
W 0
Cvaa 160,128,0,224
s_mov_b32 s20, s34
Ra 144,242,4096
Ra 148,242,5120
Mvaa 160,132,4
s_xor_b32 s62, s62, 0x10000
X 184
Cvaa 164,132,20,228
s_and_b32 s21, s35, 0xffff
Ta 184,11,40960
Ta 186,11,41216
Mvaa 164,128,16
Ta 188,12,32768
Ta 190,12,33024
Cvaa 168,128,32,232
s_mov_b32 s22, 2211840
Ra 192,13,58368
Ta 196,11,43008
Mvaa 168,132,36
s_mov_b32 s23, 0x20000
X 185
Cvaa 172,132,52,236
Ta 198,11,43264
Ta 200,12,34816
Mvaa 172,128,48
W 3
X 186
Maaa 232,184,192
Ta 202,12,35072
K 214,184,185
Maaa 236,188,192
Ra 204,13,58880
Ra 152,242,6144
Mvaa 160,136,8
X 187
Mvaa 160,140,12
Ta 208,11,45056
v_lshrrev_b32 v4, 8, v1
Mvaa 164,140,28
X 188
Mvaa 164,136,24
Ta 210,11,45312
v_lshlrev_b32 v5, 5, v4
Mvaa 168,136,40
v_xor_b32 v5, v5, v1
K 215,186,187
Mvaa 168,140,44
Ra 156,242,7168
Ta 212,12,36864
Mvaa 172,140,60
X 189
Mvaa 172,136,56
v_and_b32 v5, 0xf0, v5
v_mul_u32_u24 v4, 1280, v4
v_smfmac_f32_16x16x64_bf16 a[160:163], a[176:179], v[192:199], v240
W 5
X 190
Maaa 232,196,204
Ta 214,12,37120
v_add_u32 v4, v4, v5
Maaa 236,200,204
Ra 216,13,59392
K 222,188,189
v_smfmac_f32_16x16x64_bf16 a[164:167], a[176:179], v[200:207], v240
X 191
X 160
K 223,190,191
v_smfmac_f32_16x16x64_bf16 a[168:171], a[176:179], v[208:215], v240
Ra 128,242,16384
v_xor_b32 v5, 128, v4
v_smfmac_f32_16x16x64_bf16 a[172:175], a[176:179], v[216:223], v240
W 4
Ta 220,11,47104
v_add_u32 v6, 8192, v4
Cvaa 176,144,0,224
Ta 222,11,47360
v_add_u32 v7, 12288, v5
Mvaa 176,148,4
Ra 132,242,17408
v_add_u32 v5, 4096, v5
Cvaa 180,148,20,228
s_mul_i32 s94, s85, 40960
X 161
Mvaa 180,144,16
s_mul_i32 s81, s14, 20480
Ta 224,12,38912
Ta 226,12,39168
Cvaa 184,144,32,232
s_add_u32 s94, s94, s81
X 162
Mvaa 184,148,36
W 6
s_lshl_b32 s95, s14, 12
K 192,160,161
Maaa 232,208,216
s_add_u32 s95, s95, 98304
X 163
Maaa 236,212,216
s_add_u32 m0, s95, 0
Ra 228,13,59904
Ra 136,242,18432
Cvaa 188,148,52,236
X 164
Mvaa 188,144,48
Ta 184,11,49152
K 193,162,163
Mvaa 176,152,8
X 165
Mvaa 176,156,12
Ra 140,242,19456
Ta 186,11,49408
Mvaa 180,156,28
D 4,20,94,0
Ta 188,12,40960
Mvaa 180,152,24
X 166
Mvaa 184,152,40
D 5,20,94,1024
K 200,164,165
Mvaa 184,156,44
X 167
Mvaa 188,156,60
W 5
D 6,20,94,2048
Ra 144,242,20480
Maaa 232,220,228
s_add_u32 s64, s64, 1
Ta 190,12,41216
Ra 192,13,60416
Maaa 236,224,228
D 7,20,94,3072
Ta 196,11,51200
Mvaa 188,152,56
W 6
X 168
Mvav 32,128,196
s_add_u32 s94, s94, 256
s_add_u32 m0, s95, 8192
K 201,166,167
Mvav 64,128,204
X 169
Mvav 96,128,212
D 4,20,94,0
Ra 148,242,21504
Mvav 128,128,220
X 170
Mvav 132,132,220
D 5,20,94,1024
Ta 198,11,51456
Mvav 100,132,212
Ta 200,12,43008
K 208,168,169
Mvav 68,132,204
D 6,20,94,2048
Ra 152,242,22528
Mvav 36,132,196
W 5
X 171
Maaa 232,184,192
D 7,20,94,3072
Ta 202,12,43264
Maaa 236,188,192
s_add_u32 s94, s94, 256
X 172
Mvav 40,136,196
s_add_u32 m0, s95, 16384
Ra 204,13,60928
K 209,170,171
Mvav 72,136,204
X 173
Mvav 104,136,212
Ra 156,242,23552
D 4,20,94,0
Mvav 136,136,220
X 174
Mvav 140,140,220
D 5,20,94,1024
Ta 208,11,53248
Mvav 108,140,212
Ta 210,11,53504
K 216,172,173
Mvav 76,140,204
D 6,20,94,2048
Ra 128,242,8192
Mvav 44,140,196
W 3
X 175
Mvav 48,144,196
D 7,20,94,3072
Ta 212,12,45056
Maaa 232,196,204
)"
R"(s_add_u32 s94, s94, 256
X 176
Maaa 236,200,204
s_add_u32 m0, s95, 24576
Ta 214,12,45312
K 217,174,175
Mvav 80,144,204
D 4,20,94,0
Ra 132,242,9216
Mvav 112,144,212
X 177
Mvav 144,144,220
Ra 216,13,61440
D 5,20,94,1024
Mvav 148,148,220
X 178
Mvav 116,148,212
D 6,20,94,2048
Ta 220,11,55296
Mvav 84,148,204
Ta 222,11,55552
K 194,176,177
Mvav 52,148,196
D 7,20,94,3072
Ra 136,242,10240
Mvav 56,152,196
s_add_u32 s94, s94, 256
X 179
Mvav 88,152,204
W 3
X 180
Maaa 232,208,216
s_add_u32 m0, s95, 32768
Ta 224,12,47104
K 195,178,179
Maaa 236,212,216
D 4,20,94,0
Ta 226,12,47360
Mvav 120,152,212
Ra 140,242,11264
Ra 228,13,61952
Mvav 152,152,220
X 181
Mvav 156,156,220
D 5,20,94,1024
Ta 184,11,57344
Mvav 124,156,212
X 182
Mvav 92,156,204
D 6,20,94,2048
K 202,180,181
Mvav 60,156,196
W 2
X 183
Cvaa 160,128,0,224
D 7,20,94,3072
Ra 144,242,12288
Mvaa 160,132,4
s_add_u32 s94, s94, 256
X 184
Cvaa 164,132,20,228
W 2
Ta 186,11,57600
K 203,182,183
Maaa 232,220,228
X 185
Maaa 236,224,228
Ta 188,12,49152
Ra 148,242,13312
Mvaa 164,128,16
X 186
Cvaa 168,128,32,232
Ta 190,12,49408
K 210,184,185
Mvaa 168,132,36
Ra 192,13,62464
Ta 196,11,59392
Cvaa 172,132,52,236
X 187
Mvaa 172,128,48
X 188
Mvaa 160,136,8
Ra 152,242,14336
K 211,186,187
Mvaa 160,140,12
Ta 198,11,59648
Ta 200,12,51200
Mvaa 164,140,28
X 189
Mvaa 164,136,24
W 4
X 190
Maaa 232,184,192
Ra 156,242,15360
K 218,188,189
Maaa 236,188,192
Ta 202,12,51456
Ra 204,13,62976
Mvaa 168,136,40
X 191
Mvaa 168,140,44
Ra 128,242,24576
Ra 132,242,25600
Mvaa 172,140,60
X 160
Mvaa 172,136,56
W 4
Ra 136,242,26624
K 219,190,191
Cvaa 176,144,0,224
X 161
Mvaa 176,148,4
X 162
Cvaa 180,148,20,228
Ra 140,242,27648
K 196,160,161
Mvaa 180,144,16
X 163
Cvaa 184,144,32,232
W 4
X 164
Maaa 232,196,204
K 197,162,163
Maaa 236,200,204
X 165
Mvaa 184,148,36
X 166
Cvaa 188,148,52,236
K 204,164,165
Mvaa 188,144,48
X 167
Mvaa 176,152,8
Ra 144,242,28672
G 252,232
Mvaa 176,156,12
G 253,233
G 254,234
Mvaa 180,156,28
X 168
Mvaa 180,152,24
G 255,235
G 8,236
Mvaa 184,152,40
Ra 148,242,29696
G 9,237
Mvaa 184,156,44
G 10,238
G 14,239
Mvaa 188,156,60
K 252,252,8
K 253,253,9
Mvaa 188,152,56
W 2
K 205,166,167
K 254,254,10
Mvav 32,128,192
K 255,255,14
v_and_b32 v8, 0xffff0000, v252
Mvav 64,128,200
v_and_b32 v9, 0xffff0000, v253
v_and_b32 v10, 0xffff0000, v254
Mvav 96,128,208
X 169
Mvav 128,128,216
v_and_b32 v14, 0xffff0000, v255
v_lshlrev_b32 v252, 16, v252
Mvav 132,132,216
v_lshlrev_b32 v253, 16, v253
v_lshlrev_b32 v254, 16, v254
Mvav 100,132,208
v_lshlrev_b32 v255, 16, v255
v_mul_f32 v15, 0xbfb8aa3b, v252
Mvav 68,132,200
v_mul_f32 v2, 0xbfb8aa3b, v253
v_mul_f32 v3, 0xbfb8aa3b, v254
Mvav 36,132,192
X 170
Mvav 40,136,192
v_mul_f32 v27, 0xbfb8aa3b, v255
K 212,168,169
Mvav 72,136,200
X 15
Mvav 104,136,208
Ra 152,242,30720
Ra 156,242,31744
Mvav 136,136,216
X 2
X 3
X 27
v_add_f32 v15, 1.0, v15
v_add_f32 v2, 1.0, v2
v_add_f32 v3, 1.0, v3
v_add_f32 v27, 1.0, v27
v_rcp_f32 v15, v15
v_rcp_f32 v2, v2
v_rcp_f32 v3, v3
v_rcp_f32 v27, v27
U 252,252,15
U 253,253,2
U 254,254,3
U 255,255,27
K 252,252,252
K 253,253,253
K 254,254,254
K 255,255,255
v_lshlrev_b32 v252, 16, v252
v_lshlrev_b32 v253, 16, v253
v_lshlrev_b32 v254, 16, v254
v_lshlrev_b32 v255, 16, v255
U 252,252,8
U 253,253,9
U 254,254,10
U 255,255,14
K 246,252,253
K 247,254,255
s_lshl_b32 s81, s14, 3
v_add_u32 v2, s81, v1
s_mov_b32 s86, 0x00ff00ff
s_mov_b32 s87, 0x00ff00ff
v_cndmask_b32_e64 v252, 0, v246, s[86:87]
v_cndmask_b32_e64 v253, 0, v247, s[86:87]
v_cndmask_b32_e64 v246, v246, 0, s[86:87]
v_cndmask_b32_e64 v247, v247, 0, s[86:87]
ds_write_b64 v2, v[252:253] offset:63488
ds_write_b64 v2, v[246:247] offset:64512
Mvav 140,140,216
X 171
Mvav 108,140,208
X 172
Mvav 76,140,200
s_waitcnt vmcnt(0) lgkmcnt(0)
s_barrier
v_xor_b32 v242, 0x10000, v242
Mvav 44,140,192
Ra 128,242,0
K 213,170,171
Mvav 48,144,192
X 173
Mvav 80,144,200
X 174
Mvav 112,144,208
Ra 132,242,1024
K 220,172,173
Mvav 144,144,216
X 175
Mvav 148,148,216
X 176
Mvav 116,148,208
Ra 136,242,2048
K 221,174,175
Mvav 84,148,200
X 177
Mvav 52,148,192
X 178
Mvav 56,152,192
Ra 140,242,3072
K 198,176,177
Mvav 88,152,200
X 179
Mvav 120,152,208
X 180
Mvav 152,152,216
K 199,178,179
Mvav 156,156,216
X 181
Mvav 124,156,208
X 182
Mvav 92,156,200
K 206,180,181
Mvav 60,156,192
X 183
s_nop 0
K 207,182,183
W 0
Cvaa 160,128,0,224
Ra 144,242,4096
Ra 148,242,5120
Mvaa 160,132,4
s_xor_b32 s62, s62, 0x10000
X 184
Cvaa 164,132,20,228
Ra 216,1,63488
Ra 220,1,64512
Mvaa 164,128,16
v_lshrrev_b32 v8, 6, v1
v_and_b32 v9, 7, v8
Cvaa 168,128,32,232
s_lshl_b32 s81, s14, 2
X 185
Mvaa 168,132,36
v_xor_b32 v9, s81, v9
v_lshlrev_b32 v9, 5, v9
Cvaa 172,132,52,236
X 186
Mvaa 172,128,48
v_lshl_add_u32 v4, v8, 8, v9
v_bfe_u32 v9, v1, 4, 2
Mvaa 160,136,8
v_lshl_add_u32 v4, v9, 3, v4
v_add_u32 v4, 98304, v4
Mvaa 160,140,12
K 214,184,185
v_xor_b32 v5, 32, v4
Mvaa 164,140,28
v_xor_b32 v6, 64, v4
v_xor_b32 v7, 96, v4
Ta 184,4,0
Ta 186,4,4096
Mvaa 164,136,24
Ta 188,5,0
Ta 190,5,4096
Mvaa 168,136,40
Ra 152,242,6144
Ta 192,6,0
Mvaa 168,140,44
W 4
X 187
Zaaa 232,184,216
Ta 194,6,4096
v_and_b32 v11, 0x70, v1
Mvaa 172,140,60
v_mul_u32_u24 v11, 160, v11
v_lshrrev_b32 v10, 8, v1
Mvaa 172,136,56
X 188
v_smfmac_f32_16x16x64_bf16 a[160:163], a[176:179], v[192:199], v240
Ra 156,242,7168
W 4
Maaa 232,188,220
K 215,186,187
v_lshl_add_u32 v11, v10, 4, v11
v_smfmac_f32_16x16x64_bf16 a[164:167], a[176:179], v[200:207], v240
X 189
X 190
K 222,188,189
X 191
X 160
K 223,190,191
v_smfmac_f32_16x16x64_bf16 a[168:171], a[176:179], v[208:215], v240
Ta 196,7,0
v_bfe_u32 v10, v1, 7, 1
v_smfmac_f32_16x16x64_bf16 a[172:175], a[176:179], v[216:223], v240
W 2
Ta 198,7,4096
v_lshl_add_u32 v10, v10, 6, v11
Zaaa 236,192,216
W 2
s_mul_i32 s94, s85, 20480
s_lshl_b32 s82, s14, 8
Cvaa 176,144,0,224
s_add_u32 s94, s94, s82
Ta 200,4,8192
Ta 202,4,12288
Mvaa 176,148,4
s_add_u32 s94, s94, 2752512
s_lshl_b32 s71, s85, 4
Ra 128,242,16384
Cvaa 180,148,20,228
W 3
s_lshl_b32 s82, s14, 3
Ta 204,5,8192
Maaa 236,196,220
s_add_u32 s71, s71, s82
s_add_u32 s71, s71, 2560
Ta 206,5,12288
Mvaa 180,144,16
X 161
Cvaa 184,144,32,232
Ra 132,242,17408
Ta 208,6,8192
Mvaa 184,148,36
W 5
X 162
Zaaa 240,200,216
Ta 210,6,12288
K 192,160,161
Cvaa 188,148,52,236
buffer_store_dwordx4 a[232:235], v10, s[8:11], s94 offen offset:0 sc1
Ta 212,7,8192
Mvaa 188,144,48
X 163
Mvaa 176,152,8
W 4
X 164
Maaa 240,204,220
Ra 136,242,18432
Ta 214,7,12288
Mvaa 176,156,12
Ta 184,4,16384
K 193,162,163
Mvaa 180,156,28
X 165
Mvaa 180,152,24
W 4
Ta 186,4,20480
Ra 140,242,19456
Zaaa 244,208,216
X 166
Mvaa 184,152,40
buffer_store_dwordx4 a[236:239], v10, s[8:11], s94 offen offset:128 sc1
)"
R"(K 200,164,165
Mvaa 184,156,44
X 167
Mvaa 188,156,60
W 3
Ta 188,5,16384
Ta 190,5,20480
Maaa 244,212,220
s_add_u32 s64, s64, 1
X 168
Mvaa 188,152,56
W 2
Ra 144,242,20480
K 201,166,167
Mvav 32,128,196
Ta 192,6,16384
Ta 194,6,20480
Mvav 64,128,204
X 169
Zaaa 248,184,216
buffer_store_dwordx4 a[240:243], v10, s[8:11], s94 offen offset:512 sc1
Ta 196,7,16384
Mvav 96,128,212
X 170
Mvav 128,128,220
Ra 148,242,21504
K 208,168,169
Mvav 132,132,220
W 5
Ta 198,7,20480
Ta 200,4,24576
Maaa 248,188,220
X 171
Mvav 100,132,212
X 172
Mvav 68,132,204
Ta 202,4,28672
K 209,170,171
Mvav 36,132,196
W 5
Ra 152,242,22528
buffer_store_dwordx4 a[244:247], v10, s[8:11], s94 offen offset:640 sc1
Zaaa 252,192,216
X 173
Mvav 40,136,196
X 174
Mvav 72,136,204
Ta 204,5,24576
Ta 206,5,28672
Mvav 104,136,212
W 5
Ra 156,242,23552
K 216,172,173
Maaa 252,196,220
X 175
Mvav 136,136,220
Ta 208,6,24576
Ta 210,6,28672
Mvav 140,140,220
X 176
Mvav 108,140,212
W 6
buffer_store_dwordx4 a[248:251], v10, s[8:11], s94 offen offset:1024 sc1
K 217,174,175
Zaaa 232,200,216
X 177
Mvav 76,140,204
Ta 212,7,24576
Ra 128,242,8192
Mvav 44,140,196
W 4
Ta 214,7,28672
Ta 184,4,32768
Mvav 48,144,196
X 178
Maaa 232,204,220
Ra 132,242,9216
K 194,176,177
Mvav 80,144,204
X 179
Mvav 112,144,212
Ta 186,4,36864
buffer_store_dwordx4 a[252:255], v10, s[8:11], s94 offen offset:1152 sc1
Mvav 144,144,220
W 6
X 180
Zaaa 236,208,216
Ta 188,5,32768
K 195,178,179
Mvav 148,148,220
X 181
Mvav 116,148,212
Ta 190,5,36864
Ra 136,242,10240
Mvav 84,148,204
W 6
X 182
Maaa 236,212,220
Ta 192,6,32768
K 202,180,181
Mvav 52,148,196
Ta 194,6,36864
Ra 140,242,11264
Mvav 56,152,196
X 183
Mvav 88,152,204
W 6
buffer_store_dwordx4 a[232:235], v10, s[8:11], s94 offen offset:1536 sc1
Ta 196,7,32768
Zaaa 240,184,216
X 184
Mvav 120,152,212
Ta 198,7,36864
K 203,182,183
Mvav 152,152,220
Ra 144,242,12288
Ra 148,242,13312
Mvav 156,156,220
W 8
X 185
Maaa 240,188,220
X 186
Mvav 124,156,212
buffer_store_dwordx4 a[236:239], v10, s[8:11], s94 offen offset:1664 sc1
K 210,184,185
Mvav 92,156,204
X 187
Mvav 60,156,196
W 5
X 188
Zaaa 244,192,216
W 4
Ra 152,242,14336
K 211,186,187
Cvaa 160,128,0,224
X 189
Mvaa 160,132,4
X 190
Cvaa 164,132,20,228
W 3
Ra 156,242,15360
buffer_store_dwordx4 a[240:243], v10, s[8:11], s94 offen offset:2048 sc1
Maaa 244,196,220
K 218,188,189
Mvaa 164,128,16
X 191
Cvaa 168,128,32,232
Mvaa 168,132,36
Cvaa 172,132,52,236
Mvaa 172,128,48
Ra 128,242,24576
Mvaa 160,136,8
Ra 132,242,25600
buffer_store_dwordx4 a[244:247], v10, s[8:11], s94 offen offset:2176 sc1
Mvaa 160,140,12
Mvaa 164,140,28
Mvaa 164,136,24
Mvaa 168,136,40
Mvaa 168,140,44
Mvaa 172,140,60
Mvaa 172,136,56
W 2
Ra 136,242,26624
Cvaa 176,144,0,224
X 160
Mvaa 176,148,4
Ra 140,242,27648
K 219,190,191
Cvaa 180,148,20,228
X 161
Mvaa 180,144,16
X 162
Cvaa 184,144,32,232
K 196,160,161
Mvaa 184,148,36
X 163
Cvaa 188,148,52,236
X 164
Mvaa 188,144,48
Ra 144,242,28672
K 197,162,163
Mvaa 176,152,8
X 165
Mvaa 176,156,12
X 166
Mvaa 180,156,28
Ra 148,242,29696
K 204,164,165
Mvaa 180,152,24
X 167
Mvaa 184,152,40
X 168
Mvaa 184,156,44
K 205,166,167
Mvaa 188,156,60
X 169
Mvaa 188,152,56
W 2
X 170
Mvav 32,128,192
Ra 152,242,30720
K 212,168,169
Mvav 64,128,200
X 171
Mvav 96,128,208
Ra 156,242,31744
Mvav 128,128,216
X 172
Mvav 132,132,216
K 213,170,171
Mvav 100,132,208
X 173
Mvav 68,132,200
X 174
Mvav 36,132,192
Mvav 40,136,192
K 220,172,173
Mvav 72,136,200
X 175
Mvav 104,136,208
Mvav 136,136,216
X 176
Mvav 140,140,216
Mvav 108,140,208
K 221,174,175
Mvav 76,140,200
W 0
s_barrier
v_xor_b32 v242, 0x10000, v242
Mvav 44,140,192
X 177
Mvav 48,144,192
X 178
Mvav 80,144,200
Ra 128,242,0
K 198,176,177
Mvav 112,144,208
X 179
Mvav 144,144,216
Ra 132,242,1024
Mvav 148,148,216
X 180
Mvav 116,148,208
Ra 136,242,2048
Mvav 84,148,200
K 199,178,179
Mvav 52,148,192
X 181
Mvav 56,152,192
Ra 140,242,3072
Mvav 88,152,200
X 182
Mvav 120,152,208
Mvav 152,152,216
K 206,180,181
Mvav 156,156,216
X 183
Mvav 124,156,208
Mvav 92,156,200
s_waitcnt vmcnt(0)
v_mov_b64 v[28:29], s[12:13]
s_mov_b64 exec, 1
buffer_store_dwordx2 v[28:29], off, s[8:11], s71 sc1
s_mov_b64 exec, -1
K 207,182,183
Mvav 60,156,192
s_mov_b32 s85, 99
s_lshl_b32 s65, s57, 1
s_sub_u32 s65, s65, 1
s_branch .Ll0t_46%=
.Lf1r_47%=:
s_lshl_b32 s92, s58, 2
s_add_u32 s91, s92, s51
s_add_u32 s95, s92, 0
s_and_b32 s95, s95, 15
s_mul_i32 s95, s95, 40960
s_mul_i32 s71, s51, 0
s_add_u32 s71, s71, 133120
s_add_u32 m0, s71, s48
s_lshl2_add_u32 s94, s48, s95
D 16,40,94,0
s_add_u32 s95, s92, 1
s_and_b32 s95, s95, 15
s_mul_i32 s95, s95, 40960
s_mul_i32 s71, s51, 0
s_add_u32 s71, s71, 143360
D 17,40,94,1024
D 18,40,94,2048
D 19,40,94,3072
s_add_u32 m0, m0, 4096
s_nop 0
buffer_load_dwordx4 v20, s[40:43], s94 offen lds
s_add_u32 m0, s71, s48
s_lshl2_add_u32 s94, s48, s95
D 16,40,94,0
s_add_u32 s95, s92, 2
s_and_b32 s95, s95, 15
s_mul_i32 s95, s95, 40960
s_mul_i32 s71, s51, 0
s_add_u32 s71, s71, 153600
D 17,40,94,1024
D 18,40,94,2048
D 19,40,94,3072
s_add_u32 m0, m0, 4096
s_nop 0
buffer_load_dwordx4 v20, s[40:43], s94 offen lds
s_add_u32 m0, s71, s48
s_lshl2_add_u32 s94, s48, s95
D 16,40,94,0
D 17,40,94,1024
D 18,40,94,2048
D 19,40,94,3072
s_add_u32 m0, m0, 4096
s_nop 0
buffer_load_dwordx4 v20, s[40:43], s94 offen lds
s_branch .Lf1_40%=
.Lspin_15%=:
s_mov_b32 s84, 0
.Llp_17%=:
s_sleep 2
v_lshrrev_b32 v28, 1, v1
s_lshl_b32 s80, s60, 9
s_add_u32 s80, s80, 40960
buffer_load_dwordx2 v[28:29], v28, s[8:11], s80 offen sc1
s_waitcnt vmcnt(0)
s_add_u32 s84, s84, 1
v_cmp_eq_u32 vcc, s13, v29
s_cmp_eq_u64 vcc, -1
s_cbranch_scc1 .Lok_16%=
s_cmp_lt_u32 s84, 1048576
s_cbranch_scc1 .Llp_17%=
s_branch .Lok_16%=
.Lspin_20%=:
s_mov_b32 s84, 0
.Llp_22%=:
s_sleep 2
v_lshrrev_b32 v28, 1, v1
s_lshl_b32 s80, s60, 9
s_add_u32 s80, s80, 40960
buffer_load_dwordx2 v[28:29], v28, s[8:11], s80 offen sc1
s_waitcnt vmcnt(0)
s_add_u32 s84, s84, 1
v_cmp_eq_u32 vcc, s13, v29
s_cmp_eq_u64 vcc, -1
s_cbranch_scc1 .Lok_21%=
s_cmp_lt_u32 s84, 1048576
s_cbranch_scc1 .Llp_22%=
s_branch .Lok_21%=
.Lrt_23%=:
s_waitcnt vmcnt(0)
s_mov_b32 s86, 40960
buffer_load_dwordx4 v[32:35], v1, s[8:11], s86 offen offset:0 sc1
buffer_load_dwordx4 v[36:39], v1, s[8:11], s86 offen offset:1024 sc1
buffer_load_dwordx4 v[40:43], v1, s[8:11], s86 offen offset:2048 sc1
buffer_load_dwordx4 v[44:47], v1, s[8:11], s86 offen offset:3072 sc1
s_mov_b32 s86, 45056
buffer_load_dwordx4 v[48:51], v1, s[8:11], s86 offen offset:0 sc1
buffer_load_dwordx4 v[52:55], v1, s[8:11], s86 offen offset:1024 sc1
buffer_load_dwordx4 v[56:59], v1, s[8:11], s86 offen offset:2048 sc1
buffer_load_dwordx4 v[60:63], v1, s[8:11], s86 offen offset:3072 sc1
s_mov_b32 s86, 49152
buffer_load_dwordx4 v[64:67], v1, s[8:11], s86 offen offset:0 sc1
buffer_load_dwordx4 v[68:71], v1, s[8:11], s86 offen offset:1024 sc1
buffer_load_dwordx4 v[72:75], v1, s[8:11], s86 offen offset:2048 sc1
buffer_load_dwordx4 v[76:79], v1, s[8:11], s86 offen offset:3072 sc1
s_mov_b32 s86, 53248
buffer_load_dwordx4 v[80:83], v1, s[8:11], s86 offen offset:0 sc1
buffer_load_dwordx4 v[84:87], v1, s[8:11], s86 offen offset:1024 sc1
buffer_load_dwordx4 v[88:91], v1, s[8:11], s86 offen offset:2048 sc1
buffer_load_dwordx4 v[92:95], v1, s[8:11], s86 offen offset:3072 sc1
s_branch .Lrtr_24%=
.Lrt_25%=:
v_lshrrev_b32 v136, 1, v1
s_mov_b32 s84, 0
.LL_27%=:
s_waitcnt vmcnt(0)
v_min3_u32 v96, v33, v35, v95
v_max3_u32 v97, v33, v35, v95
v_min3_u32 v96, v96, v37, v39
v_max3_u32 v97, v97, v37, v39
v_min3_u32 v96, v96, v41, v43
v_max3_u32 v97, v97, v41, v43
v_min3_u32 v96, v96, v45, v47
v_max3_u32 v97, v97, v45, v47
v_min3_u32 v96, v96, v49, v51
v_max3_u32 v97, v97, v49, v51
v_min3_u32 v96, v96, v53, v55
v_max3_u32 v97, v97, v53, v55
v_min3_u32 v96, v96, v57, v59
v_max3_u32 v97, v97, v57, v59
v_min3_u32 v96, v96, v61, v63
v_max3_u32 v97, v97, v61, v63
v_min3_u32 v96, v96, v65, v67
v_max3_u32 v97, v97, v65, v67
v_min3_u32 v96, v96, v69, v71
v_max3_u32 v97, v97, v69, v71
v_min3_u32 v96, v96, v73, v75
v_max3_u32 v97, v97, v73, v75
v_min3_u32 v96, v96, v77, v79
v_max3_u32 v97, v97, v77, v79
)"
R"(v_min3_u32 v96, v96, v81, v83
v_max3_u32 v97, v97, v81, v83
v_min3_u32 v96, v96, v85, v87
v_max3_u32 v97, v97, v85, v87
v_min3_u32 v96, v96, v89, v91
v_max3_u32 v97, v97, v89, v91
v_min_u32 v96, v96, v93
v_max_u32 v97, v97, v93
v_cmp_eq_u32 vcc, s13, v96
v_cmp_eq_u32_e64 s[86:87], s13, v97
s_and_b64 vcc, vcc, s[86:87]
s_cmp_eq_u64 vcc, -1
s_cbranch_scc1 .LL_28%=
s_sleep 2
s_mov_b32 s86, 40960
buffer_load_dwordx4 v[32:35], v1, s[8:11], s86 offen offset:0 sc1
buffer_load_dwordx4 v[36:39], v1, s[8:11], s86 offen offset:1024 sc1
buffer_load_dwordx4 v[40:43], v1, s[8:11], s86 offen offset:2048 sc1
buffer_load_dwordx4 v[44:47], v1, s[8:11], s86 offen offset:3072 sc1
s_mov_b32 s86, 45056
buffer_load_dwordx4 v[48:51], v1, s[8:11], s86 offen offset:0 sc1
buffer_load_dwordx4 v[52:55], v1, s[8:11], s86 offen offset:1024 sc1
buffer_load_dwordx4 v[56:59], v1, s[8:11], s86 offen offset:2048 sc1
buffer_load_dwordx4 v[60:63], v1, s[8:11], s86 offen offset:3072 sc1
s_mov_b32 s86, 49152
buffer_load_dwordx4 v[64:67], v1, s[8:11], s86 offen offset:0 sc1
buffer_load_dwordx4 v[68:71], v1, s[8:11], s86 offen offset:1024 sc1
buffer_load_dwordx4 v[72:75], v1, s[8:11], s86 offen offset:2048 sc1
buffer_load_dwordx4 v[76:79], v1, s[8:11], s86 offen offset:3072 sc1
s_mov_b32 s86, 53248
buffer_load_dwordx4 v[80:83], v1, s[8:11], s86 offen offset:0 sc1
buffer_load_dwordx4 v[84:87], v1, s[8:11], s86 offen offset:1024 sc1
buffer_load_dwordx4 v[88:91], v1, s[8:11], s86 offen offset:2048 sc1
buffer_load_dwordx4 v[92:95], v1, s[8:11], s86 offen offset:3072 sc1
s_add_u32 s84, s84, 1
s_cmp_lt_u32 s84, 1048576
s_cbranch_scc1 .LL_27%=
s_waitcnt vmcnt(0)
.LL_28%=:
v_mov_b32 v96, v32
v_mov_b32 v97, 0
v_mov_b32 v98, 0
v_mov_b32 v100, 0
v_med3_u32 v98, v97, v98, v34
v_med3_u32 v97, v96, v97, v34
v_max_u32 v96, v96, v34
v_med3_u32 v98, v97, v98, v36
v_med3_u32 v97, v96, v97, v36
v_max_u32 v96, v96, v36
v_med3_u32 v98, v97, v98, v38
v_med3_u32 v97, v96, v97, v38
v_max_u32 v96, v96, v38
v_med3_u32 v98, v97, v98, v40
v_med3_u32 v97, v96, v97, v40
v_max_u32 v96, v96, v40
v_med3_u32 v98, v97, v98, v42
v_med3_u32 v97, v96, v97, v42
v_max_u32 v96, v96, v42
v_med3_u32 v98, v97, v98, v44
v_med3_u32 v97, v96, v97, v44
v_max_u32 v96, v96, v44
v_med3_u32 v98, v97, v98, v46
v_med3_u32 v97, v96, v97, v46
v_max_u32 v96, v96, v46
v_med3_u32 v98, v97, v98, v48
v_med3_u32 v97, v96, v97, v48
v_max_u32 v96, v96, v48
v_med3_u32 v98, v97, v98, v50
v_med3_u32 v97, v96, v97, v50
v_max_u32 v96, v96, v50
v_med3_u32 v98, v97, v98, v52
v_med3_u32 v97, v96, v97, v52
v_max_u32 v96, v96, v52
v_med3_u32 v98, v97, v98, v54
v_med3_u32 v97, v96, v97, v54
v_max_u32 v96, v96, v54
v_med3_u32 v98, v97, v98, v56
v_med3_u32 v97, v96, v97, v56
v_max_u32 v96, v96, v56
v_med3_u32 v98, v97, v98, v58
v_med3_u32 v97, v96, v97, v58
v_max_u32 v96, v96, v58
v_med3_u32 v98, v97, v98, v60
v_med3_u32 v97, v96, v97, v60
v_max_u32 v96, v96, v60
v_med3_u32 v98, v97, v98, v62
v_med3_u32 v97, v96, v97, v62
v_max_u32 v96, v96, v62
v_med3_u32 v98, v97, v98, v64
v_med3_u32 v97, v96, v97, v64
v_max_u32 v96, v96, v64
v_med3_u32 v98, v97, v98, v66
v_med3_u32 v97, v96, v97, v66
v_max_u32 v96, v96, v66
v_med3_u32 v98, v97, v98, v68
v_med3_u32 v97, v96, v97, v68
v_max_u32 v96, v96, v68
v_med3_u32 v98, v97, v98, v70
v_med3_u32 v97, v96, v97, v70
v_max_u32 v96, v96, v70
v_med3_u32 v98, v97, v98, v72
v_med3_u32 v97, v96, v97, v72
v_max_u32 v96, v96, v72
v_med3_u32 v98, v97, v98, v74
v_med3_u32 v97, v96, v97, v74
v_max_u32 v96, v96, v74
v_med3_u32 v98, v97, v98, v76
v_med3_u32 v97, v96, v97, v76
v_max_u32 v96, v96, v76
v_med3_u32 v98, v97, v98, v78
v_med3_u32 v97, v96, v97, v78
v_max_u32 v96, v96, v78
v_med3_u32 v98, v97, v98, v80
v_med3_u32 v97, v96, v97, v80
v_max_u32 v96, v96, v80
v_med3_u32 v98, v97, v98, v82
v_med3_u32 v97, v96, v97, v82
v_max_u32 v96, v96, v82
v_med3_u32 v98, v97, v98, v84
v_med3_u32 v97, v96, v97, v84
v_max_u32 v96, v96, v84
v_med3_u32 v98, v97, v98, v86
v_med3_u32 v97, v96, v97, v86
v_max_u32 v96, v96, v86
v_med3_u32 v98, v97, v98, v88
v_med3_u32 v97, v96, v97, v88
v_max_u32 v96, v96, v88
v_med3_u32 v98, v97, v98, v90
v_med3_u32 v97, v96, v97, v90
v_max_u32 v96, v96, v90
v_med3_u32 v98, v97, v98, v92
v_med3_u32 v97, v96, v97, v92
v_max_u32 v96, v96, v92
v_med3_u32 v98, v97, v98, v94
v_med3_u32 v97, v96, v97, v94
v_max_u32 v96, v96, v94
v_mov_b32 v99, v96
s_nop 1
v_max_u32_dpp v99, v99, v99 row_shr:1 row_mask:0xf bank_mask:0xf
s_nop 1
v_max_u32_dpp v99, v99, v99 row_shr:2 row_mask:0xf bank_mask:0xf
s_nop 1
v_max_u32_dpp v99, v99, v99 row_shr:4 row_mask:0xf bank_mask:0xf
s_nop 1
v_max_u32_dpp v99, v99, v99 row_shr:8 row_mask:0xf bank_mask:0xf
s_nop 1
v_max_u32_dpp v99, v99, v99 row_bcast:15 row_mask:0xa bank_mask:0xf
s_nop 1
v_max_u32_dpp v99, v99, v99 row_bcast:31 row_mask:0xc bank_mask:0xf
s_nop 0
v_readlane_b32 s86, v99, 63
s_nop 1
v_cmp_eq_u32 vcc, s86, v96
s_and_b32 s87, s86, 0xffff
s_sub_u32 s87, 2047, s87
v_cndmask_b32 v96, v96, v97, vcc
v_cndmask_b32_e64 v97, v97, 0, vcc
v_writelane_b32 v100, s87, 0
v_mov_b32 v99, v96
s_nop 1
v_max_u32_dpp v99, v99, v99 row_shr:1 row_mask:0xf bank_mask:0xf
s_nop 1
v_max_u32_dpp v99, v99, v99 row_shr:2 row_mask:0xf bank_mask:0xf
s_nop 1
v_max_u32_dpp v99, v99, v99 row_shr:4 row_mask:0xf bank_mask:0xf
s_nop 1
v_max_u32_dpp v99, v99, v99 row_shr:8 row_mask:0xf bank_mask:0xf
s_nop 1
v_max_u32_dpp v99, v99, v99 row_bcast:15 row_mask:0xa bank_mask:0xf
s_nop 1
v_max_u32_dpp v99, v99, v99 row_bcast:31 row_mask:0xc bank_mask:0xf
s_nop 0
v_readlane_b32 s86, v99, 63
s_nop 1
v_cmp_eq_u32 vcc, s86, v96
s_and_b32 s87, s86, 0xffff
s_sub_u32 s87, 2047, s87
v_cndmask_b32 v96, v96, v97, vcc
v_cndmask_b32_e64 v97, v97, 0, vcc
v_writelane_b32 v100, s87, 1
v_mov_b32 v99, v96
s_nop 1
v_max_u32_dpp v99, v99, v99 row_shr:1 row_mask:0xf bank_mask:0xf
s_nop 1
v_max_u32_dpp v99, v99, v99 row_shr:2 row_mask:0xf bank_mask:0xf
s_nop 1
v_max_u32_dpp v99, v99, v99 row_shr:4 row_mask:0xf bank_mask:0xf
s_nop 1
v_max_u32_dpp v99, v99, v99 row_shr:8 row_mask:0xf bank_mask:0xf
s_nop 1
v_max_u32_dpp v99, v99, v99 row_bcast:15 row_mask:0xa bank_mask:0xf
s_nop 1
v_max_u32_dpp v99, v99, v99 row_bcast:31 row_mask:0xc bank_mask:0xf
s_nop 0
v_readlane_b32 s86, v99, 63
s_nop 1
v_cmp_eq_u32 vcc, s86, v96
s_and_b32 s87, s86, 0xffff
s_sub_u32 s87, 2047, s87
v_cndmask_b32 v96, v96, v97, vcc
v_cndmask_b32_e64 v97, v97, 0, vcc
v_writelane_b32 v100, s87, 2
v_mov_b32 v99, v96
s_nop 1
v_max_u32_dpp v99, v99, v99 row_shr:1 row_mask:0xf bank_mask:0xf
s_nop 1
v_max_u32_dpp v99, v99, v99 row_shr:2 row_mask:0xf bank_mask:0xf
s_nop 1
v_max_u32_dpp v99, v99, v99 row_shr:4 row_mask:0xf bank_mask:0xf
s_nop 1
v_max_u32_dpp v99, v99, v99 row_shr:8 row_mask:0xf bank_mask:0xf
s_nop 1
v_max_u32_dpp v99, v99, v99 row_bcast:15 row_mask:0xa bank_mask:0xf
s_nop 1
v_max_u32_dpp v99, v99, v99 row_bcast:31 row_mask:0xc bank_mask:0xf
s_nop 0
v_readlane_b32 s86, v99, 63
s_nop 1
v_cmp_eq_u32 vcc, s86, v96
s_and_b32 s87, s86, 0xffff
s_sub_u32 s87, 2047, s87
v_cndmask_b32 v96, v96, v97, vcc
v_cndmask_b32_e64 v97, v97, 0, vcc
v_writelane_b32 v100, s87, 3
v_mov_b32 v99, v96
s_nop 1
v_max_u32_dpp v99, v99, v99 row_shr:1 row_mask:0xf bank_mask:0xf
s_nop 1
v_max_u32_dpp v99, v99, v99 row_shr:2 row_mask:0xf bank_mask:0xf
s_nop 1
v_max_u32_dpp v99, v99, v99 row_shr:4 row_mask:0xf bank_mask:0xf
s_nop 1
v_max_u32_dpp v99, v99, v99 row_shr:8 row_mask:0xf bank_mask:0xf
s_nop 1
v_max_u32_dpp v99, v99, v99 row_bcast:15 row_mask:0xa bank_mask:0xf
s_nop 1
v_max_u32_dpp v99, v99, v99 row_bcast:31 row_mask:0xc bank_mask:0xf
s_nop 0
v_readlane_b32 s86, v99, 63
s_nop 1
v_cmp_eq_u32 vcc, s86, v96
s_and_b32 s87, s86, 0xffff
s_sub_u32 s87, 2047, s87
v_cndmask_b32 v96, v96, v97, vcc
v_cndmask_b32_e64 v97, v97, 0, vcc
v_writelane_b32 v100, s87, 4
v_mov_b32 v99, v96
s_nop 1
v_max_u32_dpp v99, v99, v99 row_shr:1 row_mask:0xf bank_mask:0xf
s_nop 1
v_max_u32_dpp v99, v99, v99 row_shr:2 row_mask:0xf bank_mask:0xf
s_nop 1
v_max_u32_dpp v99, v99, v99 row_shr:4 row_mask:0xf bank_mask:0xf
s_nop 1
v_max_u32_dpp v99, v99, v99 row_shr:8 row_mask:0xf bank_mask:0xf
s_nop 1
v_max_u32_dpp v99, v99, v99 row_bcast:15 row_mask:0xa bank_mask:0xf
s_nop 1
v_max_u32_dpp v99, v99, v99 row_bcast:31 row_mask:0xc bank_mask:0xf
s_nop 0
v_readlane_b32 s86, v99, 63
s_nop 1
v_cmp_eq_u32 vcc, s86, v96
s_and_b32 s87, s86, 0xffff
s_sub_u32 s87, 2047, s87
v_cndmask_b32 v96, v96, v97, vcc
v_cndmask_b32_e64 v97, v97, 0, vcc
v_writelane_b32 v100, s87, 5
v_mov_b32 v99, v96
s_nop 1
v_max_u32_dpp v99, v99, v99 row_shr:1 row_mask:0xf bank_mask:0xf
s_nop 1
v_max_u32_dpp v99, v99, v99 row_shr:2 row_mask:0xf bank_mask:0xf
s_nop 1
v_max_u32_dpp v99, v99, v99 row_shr:4 row_mask:0xf bank_mask:0xf
s_nop 1
v_max_u32_dpp v99, v99, v99 row_shr:8 row_mask:0xf bank_mask:0xf
s_nop 1
v_max_u32_dpp v99, v99, v99 row_bcast:15 row_mask:0xa bank_mask:0xf
s_nop 1
v_max_u32_dpp v99, v99, v99 row_bcast:31 row_mask:0xc bank_mask:0xf
s_nop 0
v_readlane_b32 s86, v99, 63
s_nop 1
v_cmp_eq_u32 vcc, s86, v96
s_and_b32 s87, s86, 0xffff
s_sub_u32 s87, 2047, s87
v_cndmask_b32 v96, v96, v97, vcc
v_cndmask_b32_e64 v97, v97, 0, vcc
v_writelane_b32 v100, s87, 6
v_mov_b32 v99, v96
s_nop 1
v_max_u32_dpp v99, v99, v99 row_shr:1 row_mask:0xf bank_mask:0xf
s_nop 1
v_max_u32_dpp v99, v99, v99 row_shr:2 row_mask:0xf bank_mask:0xf
s_nop 1
v_max_u32_dpp v99, v99, v99 row_shr:4 row_mask:0xf bank_mask:0xf
s_nop 1
v_max_u32_dpp v99, v99, v99 row_shr:8 row_mask:0xf bank_mask:0xf
s_nop 1
v_max_u32_dpp v99, v99, v99 row_bcast:15 row_mask:0xa bank_mask:0xf
s_nop 1
v_max_u32_dpp v99, v99, v99 row_bcast:31 row_mask:0xc bank_mask:0xf
s_nop 0
v_readlane_b32 s86, v99, 63
s_nop 1
v_cmp_eq_u32 vcc, s86, v96
s_and_b32 s87, s86, 0xffff
s_sub_u32 s87, 2047, s87
v_cndmask_b32 v96, v96, v97, vcc
v_cndmask_b32_e64 v97, v97, 0, vcc
v_writelane_b32 v100, s87, 7
v_mov_b32 v99, v98
s_nop 1
v_max_u32_dpp v99, v99, v99 row_shr:1 row_mask:0xf bank_mask:0xf
s_nop 1
v_max_u32_dpp v99, v99, v99 row_shr:2 row_mask:0xf bank_mask:0xf
s_nop 1
v_max_u32_dpp v99, v99, v99 row_shr:4 row_mask:0xf bank_mask:0xf
s_nop 1
v_max_u32_dpp v99, v99, v99 row_shr:8 row_mask:0xf bank_mask:0xf
s_nop 1
v_max_u32_dpp v99, v99, v99 row_bcast:15 row_mask:0xa bank_mask:0xf
s_nop 1
v_max_u32_dpp v99, v99, v99 row_bcast:31 row_mask:0xc bank_mask:0xf
s_nop 0
v_readlane_b32 s87, v99, 63
s_nop 0
s_cmp_gt_u32 s87, s86
s_cbranch_scc0 .LL_29%=
s_mov_b32 m0, 0
.LL_30%=:
v_max3_u32 v96, v32, v34, v36
v_max3_u32 v96, v96, v38, v40
v_max3_u32 v96, v96, v42, v44
v_max3_u32 v96, v96, v46, v48
v_max3_u32 v96, v96, v50, v52
v_max3_u32 v96, v96, v54, v56
v_max3_u32 v96, v96, v58, v60
v_max3_u32 v96, v96, v62, v64
v_max3_u32 v96, v96, v66, v68
v_max3_u32 v96, v96, v70, v72
v_max3_u32 v96, v96, v74, v76
v_max3_u32 v96, v96, v78, v80
v_max3_u32 v96, v96, v82, v84
v_max3_u32 v96, v96, v86, v88
v_max3_u32 v96, v96, v90, v92
v_max_u32 v96, v96, v94
v_mov_b32 v99, v96
s_nop 1
v_max_u32_dpp v99, v99, v99 row_shr:1 row_mask:0xf bank_mask:0xf
s_nop 1
v_max_u32_dpp v99, v99, v99 row_shr:2 row_mask:0xf bank_mask:0xf
s_nop 1
v_max_u32_dpp v99, v99, v99 row_shr:4 row_mask:0xf bank_mask:0xf
s_nop 1
v_max_u32_dpp v99, v99, v99 row_shr:8 row_mask:0xf bank_mask:0xf
s_nop 1
v_max_u32_dpp v99, v99, v99 row_bcast:15 row_mask:0xa bank_mask:0xf
s_nop 1
v_max_u32_dpp v99, v99, v99 row_bcast:31 row_mask:0xc bank_mask:0xf
s_nop 0
v_readlane_b32 s86, v99, 63
s_nop 1
v_cmp_eq_u32 vcc, s86, v32
s_nop 1
v_cndmask_b32_e64 v32, v32, 0, vcc
v_cmp_eq_u32 vcc, s86, v34
s_nop 1
v_cndmask_b32_e64 v34, v34, 0, vcc
v_cmp_eq_u32 vcc, s86, v36
s_nop 1
v_cndmask_b32_e64 v36, v36, 0, vcc
v_cmp_eq_u32 vcc, s86, v38
s_nop 1
v_cndmask_b32_e64 v38, v38, 0, vcc
v_cmp_eq_u32 vcc, s86, v40
s_nop 1
v_cndmask_b32_e64 v40, v40, 0, vcc
v_cmp_eq_u32 vcc, s86, v42
s_nop 1
v_cndmask_b32_e64 v42, v42, 0, vcc
v_cmp_eq_u32 vcc, s86, v44
s_nop 1
v_cndmask_b32_e64 v44, v44, 0, vcc
v_cmp_eq_u32 vcc, s86, v46
s_nop 1
v_cndmask_b32_e64 v46, v46, 0, vcc
v_cmp_eq_u32 vcc, s86, v48
s_nop 1
v_cndmask_b32_e64 v48, v48, 0, vcc
v_cmp_eq_u32 vcc, s86, v50
s_nop 1
v_cndmask_b32_e64 v50, v50, 0, vcc
v_cmp_eq_u32 vcc, s86, v52
s_nop 1
v_cndmask_b32_e64 v52, v52, 0, vcc
v_cmp_eq_u32 vcc, s86, v54
s_nop 1
v_cndmask_b32_e64 v54, v54, 0, vcc
)"
R"(v_cmp_eq_u32 vcc, s86, v56
s_nop 1
v_cndmask_b32_e64 v56, v56, 0, vcc
v_cmp_eq_u32 vcc, s86, v58
s_nop 1
v_cndmask_b32_e64 v58, v58, 0, vcc
v_cmp_eq_u32 vcc, s86, v60
s_nop 1
v_cndmask_b32_e64 v60, v60, 0, vcc
v_cmp_eq_u32 vcc, s86, v62
s_nop 1
v_cndmask_b32_e64 v62, v62, 0, vcc
v_cmp_eq_u32 vcc, s86, v64
s_nop 1
v_cndmask_b32_e64 v64, v64, 0, vcc
v_cmp_eq_u32 vcc, s86, v66
s_nop 1
v_cndmask_b32_e64 v66, v66, 0, vcc
v_cmp_eq_u32 vcc, s86, v68
s_nop 1
v_cndmask_b32_e64 v68, v68, 0, vcc
v_cmp_eq_u32 vcc, s86, v70
s_nop 1
v_cndmask_b32_e64 v70, v70, 0, vcc
v_cmp_eq_u32 vcc, s86, v72
s_nop 1
v_cndmask_b32_e64 v72, v72, 0, vcc
v_cmp_eq_u32 vcc, s86, v74
s_nop 1
v_cndmask_b32_e64 v74, v74, 0, vcc
v_cmp_eq_u32 vcc, s86, v76
s_nop 1
v_cndmask_b32_e64 v76, v76, 0, vcc
v_cmp_eq_u32 vcc, s86, v78
s_nop 1
v_cndmask_b32_e64 v78, v78, 0, vcc
v_cmp_eq_u32 vcc, s86, v80
s_nop 1
v_cndmask_b32_e64 v80, v80, 0, vcc
v_cmp_eq_u32 vcc, s86, v82
s_nop 1
v_cndmask_b32_e64 v82, v82, 0, vcc
v_cmp_eq_u32 vcc, s86, v84
s_nop 1
v_cndmask_b32_e64 v84, v84, 0, vcc
v_cmp_eq_u32 vcc, s86, v86
s_nop 1
v_cndmask_b32_e64 v86, v86, 0, vcc
v_cmp_eq_u32 vcc, s86, v88
s_nop 1
v_cndmask_b32_e64 v88, v88, 0, vcc
v_cmp_eq_u32 vcc, s86, v90
s_nop 1
v_cndmask_b32_e64 v90, v90, 0, vcc
v_cmp_eq_u32 vcc, s86, v92
s_nop 1
v_cndmask_b32_e64 v92, v92, 0, vcc
v_cmp_eq_u32 vcc, s86, v94
s_nop 1
v_cndmask_b32_e64 v94, v94, 0, vcc
s_and_b32 s87, s86, 0xffff
s_sub_u32 s87, 2047, s87
s_nop 0
v_writelane_b32 v100, s87, m0
s_add_u32 m0, m0, 1
s_cmp_lt_u32 m0, 8
s_cbranch_scc1 .LL_30%=
.LL_29%=:
v_xor_b32 v102, s12, v100
v_mov_b32 v103, s13
s_mov_b64 exec, 0xff
buffer_store_dwordx2 v[102:103], v136, s[8:11], 0 offen offset:3584 sc1
s_mov_b64 exec, -1
s_branch .Lrtr_26%=
.Lspin_51%=:
s_mov_b32 s84, 0
.Llp_53%=:
s_sleep 2
buffer_load_dwordx2 v[8:9], v11, s[8:11], 0 offen offset:3840 sc1
s_waitcnt vmcnt(0)
s_add_u32 s84, s84, 1
v_cmp_eq_u64 vcc, s[12:13], v[8:9]
s_cmp_eq_u64 vcc, -1
s_cbranch_scc1 .Lok_52%=
s_cmp_lt_u32 s84, 1048576
s_cbranch_scc1 .Llp_53%=
s_branch .Lok_52%=
.Lredo%=:
s_waitcnt vmcnt(0) lgkmcnt(0)
s_barrier
s_and_b32 s81, s14, 1
s_mul_i32 s91, s81, 17164288
s_mul_i32 s82, s56, 536576
s_add_u32 s91, s91, s82
s_add_u32 s91, s91, 4194304
s_lshr_b32 s82, s14, 1
s_lshl_b32 s83, s82, 14
s_add_u32 s91, s91, s83
s_lshl_b32 s81, s81, 14
s_lshl_b32 s82, s82, 16
s_or_b32 s92, s81, s82
s_xor_b32 s92, s92, 0x10000
s_add_u32 m0, s92, 0
s_add_u32 s81, s91, 0
Dc 1,8,81,0
Dc 1,8,81,1024
Dc 1,8,81,2048
Dc 1,8,81,3072
s_add_u32 m0, s92, 4096
s_add_u32 s81, s91, 4096
Dc 1,8,81,0
Dc 1,8,81,1024
Dc 1,8,81,2048
Dc 1,8,81,3072
s_add_u32 m0, s92, 8192
s_add_u32 s81, s91, 8192
Dc 1,8,81,0
Dc 1,8,81,1024
Dc 1,8,81,2048
Dc 1,8,81,3072
s_add_u32 m0, s92, 12288
s_add_u32 s81, s91, 12288
Dc 1,8,81,0
Dc 1,8,81,1024
Dc 1,8,81,2048
Dc 1,8,81,3072
v_bfe_u32 v244, v1, 4, 1
v_mul_u32_u24 v244, 0xaaaa, v244
v_add_u32 v244, 0x4444, v244
v_lshl_or_b32 v240, v244, 16, v244
v_mov_b32 v241, 0xff800000
v_add_u32 v242, 0x10000, v1
v_bfe_u32 v244, v1, 4, 4
v_lshrrev_b32 v245, 8, v1
v_lshlrev_b32 v245, 2, v245
v_sub_u32 v244, v244, v245
v_cmp_le_i32_e64 s[72:73], 0, v244
v_cmp_le_i32_e64 s[74:75], 1, v244
v_cmp_le_i32_e64 s[76:77], 2, v244
v_cmp_le_i32_e64 s[78:79], 3, v244
v_add_u32 v243, 4096, v1
v_add_u32 v244, 8192, v1
v_add_u32 v245, 12288, v1
s_mov_b32 s18, 0x3f803f80
s_mov_b32 s19, 0x3f003f00
v_accvgpr_write_b32 a176, s18
v_accvgpr_write_b32 a180, s19
v_mov_b32 v252, v241
v_accvgpr_write_b32 a177, s18
v_accvgpr_write_b32 a181, s19
v_mov_b32 v253, v241
v_accvgpr_write_b32 a178, s18
v_accvgpr_write_b32 a182, s19
v_mov_b32 v254, v241
v_accvgpr_write_b32 a179, s18
v_accvgpr_write_b32 a183, s19
v_mov_b32 v255, v241
v_accvgpr_write_b32 a160, 0
v_accvgpr_write_b32 a161, 0
v_accvgpr_write_b32 a162, 0
v_accvgpr_write_b32 a163, 0
v_accvgpr_write_b32 a164, 0
v_accvgpr_write_b32 a165, 0
v_accvgpr_write_b32 a166, 0
v_accvgpr_write_b32 a167, 0
v_accvgpr_write_b32 a168, 0
v_accvgpr_write_b32 a169, 0
v_accvgpr_write_b32 a170, 0
v_accvgpr_write_b32 a171, 0
v_accvgpr_write_b32 a172, 0
v_accvgpr_write_b32 a173, 0
v_accvgpr_write_b32 a174, 0
v_accvgpr_write_b32 a175, 0
s_mov_b32 s90, 0x5d800000
s_mov_b64 s[88:89], 0
s_mov_b32 s64, 0
s_and_b32 s18, s14, 1
s_lshl_b32 s62, s18, 14
s_lshl_b32 s20, s57, 15
s_sub_u32 s20, 507904, s20
s_add_u32 s20, s20, s62
s_lshl_b32 s65, s57, 1
s_sub_u32 s65, s65, 1
s_max_i32 s65, s65, 0
s_sub_u32 s66, s60, 1
s_cmp_lt_u32 s14, 2
s_cselect_b32 s20, s20, 0
s_cselect_b32 s66, s66, 15
s_cselect_b32 s21, 32768, 0
s_add_u32 s61, s91, s20
s_add_u32 s62, s62, s21
s_sub_u32 s63, 16384, s21
s_lshl_b32 s67, s60, 7
s_add_u32 s67, s67, s56
s_lshl_b32 s67, s67, 12
s_add_u32 s67, s67, 39845888
s_lshl_b32 s69, s60, 3
s_add_u32 s69, s69, s59
s_lshl_b32 s69, s69, 6
s_lshl_b32 s21, s58, 3
s_add_u32 s69, s69, s21
s_add_u32 s69, s69, 20480
s_mov_b32 s70, 0
s_mov_b32 s37, 0
v_bfe_u32 v28, v1, 4, 4
v_lshrrev_b32 v29, 8, v1
v_lshlrev_b32 v29, 2, v29
v_sub_u32 v28, v28, v29
s_mov_b64 s[24:25], 0
s_mov_b32 s68, 0
s_mov_b32 s85, 99
s_lshl_b32 s80, s60, 6
s_and_b32 s81, s14, 2
s_lshl_b32 s81, s81, 4
s_xor_b32 s82, s81, 0
s_add_u32 s82, s82, s80
v_add_u32 v4, s82, v28
s_xor_b32 s82, s81, 16
s_add_u32 s82, s82, s80
v_add_u32 v5, s82, v28
s_xor_b32 s82, s81, 32
s_add_u32 s82, s82, s80
v_add_u32 v6, s82, v28
s_xor_b32 s82, s81, 48
s_add_u32 s82, s82, s80
v_add_u32 v7, s82, v28
s_mul_i32 s82, s56, 536576
s_add_u32 s82, s82, 4194304
s_lshl_b32 s84, s60, 1
s_add_u32 s84, s84, 2
.LL_54%=:
buffer_load_dwordx4 a[128:131], v1, s[8:11], s82 offen offset:0 sc1
buffer_load_dwordx4 a[132:135], v1, s[8:11], s82 offen offset:1024 sc1
buffer_load_dwordx4 a[136:139], v1, s[8:11], s82 offen offset:2048 sc1
buffer_load_dwordx4 a[140:143], v1, s[8:11], s82 offen offset:3072 sc1
buffer_load_dwordx4 a[144:147], v243, s[8:11], s82 offen offset:0 sc1
buffer_load_dwordx4 a[148:151], v243, s[8:11], s82 offen offset:1024 sc1
buffer_load_dwordx4 a[152:155], v243, s[8:11], s82 offen offset:2048 sc1
buffer_load_dwordx4 a[156:159], v243, s[8:11], s82 offen offset:3072 sc1
s_waitcnt vmcnt(0)
Zvaa 160,128,0
Zvaa 164,128,16
Zvaa 168,128,32
Zvaa 172,128,48
Mvaa 160,132,4
Mvaa 164,132,20
Mvaa 168,132,36
Mvaa 172,132,52
Mvaa 160,136,8
Mvaa 164,136,24
Mvaa 168,136,40
Mvaa 172,136,56
Mvaa 160,140,12
Mvaa 164,140,28
Mvaa 168,140,44
Mvaa 172,140,60
Zvaa 176,144,0
Zvaa 180,144,16
Zvaa 184,144,32
Zvaa 188,144,48
Mvaa 176,148,4
Mvaa 180,148,20
Mvaa 184,148,36
Mvaa 188,148,52
Mvaa 176,152,8
Mvaa 180,152,24
Mvaa 184,152,40
Mvaa 188,152,56
Mvaa 176,156,12
Mvaa 180,156,28
Mvaa 184,156,44
Mvaa 188,156,60
v_cmp_le_i32 vcc, 0, v4
s_nop 1
v_cndmask_b32 v160, v241, v160, vcc
v_max_f32 v252, v252, v160
v_cmp_le_i32 vcc, 1, v4
s_nop 1
v_cndmask_b32 v161, v241, v161, vcc
v_max_f32 v252, v252, v161
v_cmp_le_i32 vcc, 2, v4
s_nop 1
v_cndmask_b32 v162, v241, v162, vcc
v_max_f32 v252, v252, v162
v_cmp_le_i32 vcc, 3, v4
s_nop 1
v_cndmask_b32 v163, v241, v163, vcc
v_max_f32 v252, v252, v163
v_cmp_le_i32 vcc, 0, v5
s_nop 1
v_cndmask_b32 v164, v241, v164, vcc
v_max_f32 v253, v253, v164
v_cmp_le_i32 vcc, 1, v5
s_nop 1
v_cndmask_b32 v165, v241, v165, vcc
v_max_f32 v253, v253, v165
v_cmp_le_i32 vcc, 2, v5
s_nop 1
v_cndmask_b32 v166, v241, v166, vcc
v_max_f32 v253, v253, v166
v_cmp_le_i32 vcc, 3, v5
s_nop 1
v_cndmask_b32 v167, v241, v167, vcc
v_max_f32 v253, v253, v167
v_cmp_le_i32 vcc, 0, v6
s_nop 1
v_cndmask_b32 v168, v241, v168, vcc
v_max_f32 v254, v254, v168
v_cmp_le_i32 vcc, 1, v6
s_nop 1
v_cndmask_b32 v169, v241, v169, vcc
v_max_f32 v254, v254, v169
v_cmp_le_i32 vcc, 2, v6
s_nop 1
v_cndmask_b32 v170, v241, v170, vcc
v_max_f32 v254, v254, v170
v_cmp_le_i32 vcc, 3, v6
s_nop 1
v_cndmask_b32 v171, v241, v171, vcc
v_max_f32 v254, v254, v171
v_cmp_le_i32 vcc, 0, v7
s_nop 1
v_cndmask_b32 v172, v241, v172, vcc
v_max_f32 v255, v255, v172
v_cmp_le_i32 vcc, 1, v7
s_nop 1
v_cndmask_b32 v173, v241, v173, vcc
v_max_f32 v255, v255, v173
v_cmp_le_i32 vcc, 2, v7
s_nop 1
v_cndmask_b32 v174, v241, v174, vcc
v_max_f32 v255, v255, v174
v_cmp_le_i32 vcc, 3, v7
s_nop 1
v_cndmask_b32 v175, v241, v175, vcc
v_max_f32 v255, v255, v175
v_cmp_le_i32 vcc, 16, v4
s_nop 1
v_cndmask_b32 v176, v241, v176, vcc
v_max_f32 v252, v252, v176
v_cmp_le_i32 vcc, 17, v4
s_nop 1
v_cndmask_b32 v177, v241, v177, vcc
v_max_f32 v252, v252, v177
v_cmp_le_i32 vcc, 18, v4
s_nop 1
v_cndmask_b32 v178, v241, v178, vcc
v_max_f32 v252, v252, v178
v_cmp_le_i32 vcc, 19, v4
s_nop 1
v_cndmask_b32 v179, v241, v179, vcc
v_max_f32 v252, v252, v179
v_cmp_le_i32 vcc, 16, v5
s_nop 1
v_cndmask_b32 v180, v241, v180, vcc
v_max_f32 v253, v253, v180
v_cmp_le_i32 vcc, 17, v5
s_nop 1
v_cndmask_b32 v181, v241, v181, vcc
v_max_f32 v253, v253, v181
v_cmp_le_i32 vcc, 18, v5
s_nop 1
v_cndmask_b32 v182, v241, v182, vcc
v_max_f32 v253, v253, v182
v_cmp_le_i32 vcc, 19, v5
s_nop 1
v_cndmask_b32 v183, v241, v183, vcc
v_max_f32 v253, v253, v183
v_cmp_le_i32 vcc, 16, v6
s_nop 1
v_cndmask_b32 v184, v241, v184, vcc
v_max_f32 v254, v254, v184
v_cmp_le_i32 vcc, 17, v6
s_nop 1
v_cndmask_b32 v185, v241, v185, vcc
v_max_f32 v254, v254, v185
v_cmp_le_i32 vcc, 18, v6
s_nop 1
v_cndmask_b32 v186, v241, v186, vcc
v_max_f32 v254, v254, v186
v_cmp_le_i32 vcc, 19, v6
s_nop 1
v_cndmask_b32 v187, v241, v187, vcc
v_max_f32 v254, v254, v187
v_cmp_le_i32 vcc, 16, v7
s_nop 1
v_cndmask_b32 v188, v241, v188, vcc
v_max_f32 v255, v255, v188
v_cmp_le_i32 vcc, 17, v7
s_nop 1
v_cndmask_b32 v189, v241, v189, vcc
v_max_f32 v255, v255, v189
v_cmp_le_i32 vcc, 18, v7
s_nop 1
v_cndmask_b32 v190, v241, v190, vcc
v_max_f32 v255, v255, v190
v_cmp_le_i32 vcc, 19, v7
s_nop 1
v_cndmask_b32 v191, v241, v191, vcc
v_max_f32 v255, v255, v191
)"
R"(v_subrev_u32 v4, 32, v4
v_subrev_u32 v5, 32, v5
v_subrev_u32 v6, 32, v6
v_subrev_u32 v7, 32, v7
s_add_u32 s82, s82, 8192
s_sub_u32 s84, s84, 1
s_cmp_lg_u32 s84, 0
s_cbranch_scc1 .LL_54%=
G 28,0
v_lshlrev_b32 v32, 16, v28
v_and_b32 v33, 0xffff0000, v28
G 28,1
v_lshlrev_b32 v34, 16, v28
v_and_b32 v35, 0xffff0000, v28
G 28,2
v_lshlrev_b32 v36, 16, v28
v_and_b32 v37, 0xffff0000, v28
G 28,3
v_lshlrev_b32 v38, 16, v28
v_and_b32 v39, 0xffff0000, v28
G 28,4
v_lshlrev_b32 v40, 16, v28
v_and_b32 v41, 0xffff0000, v28
G 28,5
v_lshlrev_b32 v42, 16, v28
v_and_b32 v43, 0xffff0000, v28
G 28,6
v_lshlrev_b32 v44, 16, v28
v_and_b32 v45, 0xffff0000, v28
G 28,7
v_lshlrev_b32 v46, 16, v28
v_and_b32 v47, 0xffff0000, v28
G 28,8
v_lshlrev_b32 v48, 16, v28
v_and_b32 v49, 0xffff0000, v28
G 28,9
v_lshlrev_b32 v50, 16, v28
v_and_b32 v51, 0xffff0000, v28
G 28,10
v_lshlrev_b32 v52, 16, v28
v_and_b32 v53, 0xffff0000, v28
G 28,11
v_lshlrev_b32 v54, 16, v28
v_and_b32 v55, 0xffff0000, v28
G 28,12
v_lshlrev_b32 v56, 16, v28
v_and_b32 v57, 0xffff0000, v28
G 28,13
v_lshlrev_b32 v58, 16, v28
v_and_b32 v59, 0xffff0000, v28
G 28,14
v_lshlrev_b32 v60, 16, v28
v_and_b32 v61, 0xffff0000, v28
G 28,15
v_lshlrev_b32 v62, 16, v28
v_and_b32 v63, 0xffff0000, v28
G 28,16
v_lshlrev_b32 v64, 16, v28
v_and_b32 v65, 0xffff0000, v28
G 28,17
v_lshlrev_b32 v66, 16, v28
v_and_b32 v67, 0xffff0000, v28
G 28,18
v_lshlrev_b32 v68, 16, v28
v_and_b32 v69, 0xffff0000, v28
G 28,19
v_lshlrev_b32 v70, 16, v28
v_and_b32 v71, 0xffff0000, v28
G 28,20
v_lshlrev_b32 v72, 16, v28
v_and_b32 v73, 0xffff0000, v28
G 28,21
v_lshlrev_b32 v74, 16, v28
v_and_b32 v75, 0xffff0000, v28
G 28,22
v_lshlrev_b32 v76, 16, v28
v_and_b32 v77, 0xffff0000, v28
G 28,23
v_lshlrev_b32 v78, 16, v28
v_and_b32 v79, 0xffff0000, v28
G 28,24
v_lshlrev_b32 v80, 16, v28
v_and_b32 v81, 0xffff0000, v28
G 28,25
v_lshlrev_b32 v82, 16, v28
v_and_b32 v83, 0xffff0000, v28
G 28,26
v_lshlrev_b32 v84, 16, v28
v_and_b32 v85, 0xffff0000, v28
G 28,27
v_lshlrev_b32 v86, 16, v28
v_and_b32 v87, 0xffff0000, v28
G 28,28
v_lshlrev_b32 v88, 16, v28
v_and_b32 v89, 0xffff0000, v28
G 28,29
v_lshlrev_b32 v90, 16, v28
v_and_b32 v91, 0xffff0000, v28
G 28,30
v_lshlrev_b32 v92, 16, v28
v_and_b32 v93, 0xffff0000, v28
G 28,31
v_lshlrev_b32 v94, 16, v28
v_and_b32 v95, 0xffff0000, v28
G 28,32
v_lshlrev_b32 v96, 16, v28
v_and_b32 v97, 0xffff0000, v28
G 28,33
v_lshlrev_b32 v98, 16, v28
v_and_b32 v99, 0xffff0000, v28
G 28,34
v_lshlrev_b32 v100, 16, v28
v_and_b32 v101, 0xffff0000, v28
G 28,35
v_lshlrev_b32 v102, 16, v28
v_and_b32 v103, 0xffff0000, v28
G 28,36
v_lshlrev_b32 v104, 16, v28
v_and_b32 v105, 0xffff0000, v28
G 28,37
v_lshlrev_b32 v106, 16, v28
v_and_b32 v107, 0xffff0000, v28
G 28,38
v_lshlrev_b32 v108, 16, v28
v_and_b32 v109, 0xffff0000, v28
G 28,39
v_lshlrev_b32 v110, 16, v28
v_and_b32 v111, 0xffff0000, v28
G 28,40
v_lshlrev_b32 v112, 16, v28
v_and_b32 v113, 0xffff0000, v28
G 28,41
v_lshlrev_b32 v114, 16, v28
v_and_b32 v115, 0xffff0000, v28
G 28,42
v_lshlrev_b32 v116, 16, v28
v_and_b32 v117, 0xffff0000, v28
G 28,43
v_lshlrev_b32 v118, 16, v28
v_and_b32 v119, 0xffff0000, v28
G 28,44
v_lshlrev_b32 v120, 16, v28
v_and_b32 v121, 0xffff0000, v28
G 28,45
v_lshlrev_b32 v122, 16, v28
v_and_b32 v123, 0xffff0000, v28
G 28,46
v_lshlrev_b32 v124, 16, v28
v_and_b32 v125, 0xffff0000, v28
G 28,47
v_lshlrev_b32 v126, 16, v28
v_and_b32 v127, 0xffff0000, v28
G 28,48
v_lshlrev_b32 v128, 16, v28
v_and_b32 v129, 0xffff0000, v28
G 28,49
v_lshlrev_b32 v130, 16, v28
v_and_b32 v131, 0xffff0000, v28
G 28,50
v_lshlrev_b32 v132, 16, v28
v_and_b32 v133, 0xffff0000, v28
G 28,51
v_lshlrev_b32 v134, 16, v28
v_and_b32 v135, 0xffff0000, v28
G 28,52
v_lshlrev_b32 v136, 16, v28
v_and_b32 v137, 0xffff0000, v28
G 28,53
v_lshlrev_b32 v138, 16, v28
v_and_b32 v139, 0xffff0000, v28
G 28,54
v_lshlrev_b32 v140, 16, v28
v_and_b32 v141, 0xffff0000, v28
G 28,55
v_lshlrev_b32 v142, 16, v28
v_and_b32 v143, 0xffff0000, v28
G 28,56
v_lshlrev_b32 v144, 16, v28
v_and_b32 v145, 0xffff0000, v28
G 28,57
v_lshlrev_b32 v146, 16, v28
v_and_b32 v147, 0xffff0000, v28
G 28,58
v_lshlrev_b32 v148, 16, v28
v_and_b32 v149, 0xffff0000, v28
G 28,59
v_lshlrev_b32 v150, 16, v28
v_and_b32 v151, 0xffff0000, v28
G 28,60
v_lshlrev_b32 v152, 16, v28
v_and_b32 v153, 0xffff0000, v28
G 28,61
v_lshlrev_b32 v154, 16, v28
v_and_b32 v155, 0xffff0000, v28
G 28,62
v_lshlrev_b32 v156, 16, v28
v_and_b32 v157, 0xffff0000, v28
G 28,63
v_lshlrev_b32 v158, 16, v28
v_and_b32 v159, 0xffff0000, v28
s_mov_b32 s22, 1.0
s_mov_b32 s23, 1.0
s_mov_b32 s29, 134217728
s_mov_b32 s71, 3584
s_barrier
Ra 128,242,0
Ra 132,242,1024
Ra 136,242,2048
Ra 140,242,3072
s_cmp_ge_u32 s14, 2
s_cbranch_scc1 .Lbgo%=
s_branch .Lago%=
.p2align 12
.Lpb_31%=:
s_waitcnt vmcnt(6) lgkmcnt(6)
Zvaa 32,0,32
s_add_u32 s28, s28, 512
Ta 20,4,8192
Ta 22,4,9216
Mvaa 32,4,48
s_cmp_eq_u32 s28, s4
s_cselect_b32 s93, 10240, 0
Ta 24,5,0
Ta 26,5,1024
Zvaa 64,4,52
s_sub_u32 s28, s28, s93
s_add_u32 s93, s28, 20480
buffer_load_dwordx4 a[128:131], v26, s[8:11], s28 offen offset:0 sc1
Mvaa 64,0,36
Ta 28,5,8192
Ta 30,5,9216
Zvaa 96,0,40
K 184,184,185
K 185,186,187
Mvaa 96,4,56
K 186,188,189
K 187,190,191
Zvaa 128,4,60
K 192,192,193
Mvaa 128,0,44
W 8
buffer_load_dwordx4 a[132:135], v26, s[8:11], s93 offen offset:0 sc1
K 193,194,195
Zvaa 132,8,44
Ta 0,6,0
Ta 2,6,1024
Mvaa 132,12,60
K 194,196,197
K 195,198,199
Zvaa 100,12,56
K 200,200,201
K 201,202,203
Mvaa 100,8,40
Ta 4,6,8192
Ta 6,6,9216
Zvaa 68,8,36
buffer_load_dwordx4 a[136:139], v27, s[8:11], s28 offen offset:0 sc1
K 202,204,205
Mvaa 68,12,52
K 203,206,207
K 208,208,209
Zvaa 36,12,48
K 209,210,211
K 210,212,213
Mvaa 36,8,32
W 8
Ta 8,7,0
K 211,214,215
Zvaa 40,16,32
Ta 10,7,1024
K 216,216,217
Mvaa 40,20,48
buffer_load_dwordx4 a[140:143], v27, s[8:11], s93 offen offset:0 sc1
K 217,218,219
Zvaa 72,20,52
s_add_u32 s28, s28, 512
s_cmp_eq_u32 s28, s4
s_cselect_b32 s93, 10240, 0
s_sub_u32 s28, s28, s93
Mvaa 72,16,36
s_add_u32 s93, s28, 20480
K 218,220,221
K 219,222,223
Zvaa 104,16,40
Ta 12,7,8192
Ta 14,7,9216
Mvaa 104,20,56
K 224,224,225
K 225,226,227
Zvaa 136,20,60
K 226,228,229
K 227,230,231
Mvaa 136,16,44
W 8
buffer_load_dwordx4 a[144:147], v26, s[8:11], s28 offen offset:0 sc1
K 232,232,233
Zvaa 140,24,44
K 233,234,235
K 234,236,237
Mvaa 140,28,60
Ta 16,8,0
K 235,238,239
Zvaa 108,28,56
Ta 18,8,1024
K 240,240,241
Mvaa 108,24,40
K 241,242,243
K 242,244,245
Zvaa 76,24,36
buffer_load_dwordx4 a[148:151], v26, s[8:11], s93 offen offset:0 sc1
K 243,246,247
Mvaa 76,28,52
Ta 20,8,8192
Ta 22,8,9216
Zvaa 44,28,48
K 248,248,249
K 249,250,251
Mvaa 44,24,32
K 250,252,253
K 251,254,255
W 8
Zvaa 48,0,32
Ta 24,9,0
Ta 26,9,1024
Mvaa 48,4,48
buffer_load_dwordx4 a[152:155], v27, s[8:11], s28 offen offset:0 sc1
K 160,160,161
Zvaa 80,4,52
K 161,162,163
K 162,164,165
Mvaa 80,0,36
Ta 28,9,8192
K 163,166,167
Zvaa 112,0,40
Ta 30,9,9216
K 168,168,169
Mvaa 112,4,56
K 169,170,171
K 170,172,173
Zvaa 144,4,60
buffer_load_dwordx4 a[156:159], v27, s[8:11], s93 offen offset:0 sc1
K 171,174,175
Mvaa 144,0,44
W 8
s_add_u32 s28, s28, 512
s_cmp_eq_u32 s28, s4
s_cselect_b32 s93, 10240, 0
Zvaa 148,8,44
s_sub_u32 s28, s28, s93
s_add_u32 s93, s28, 20480
G 164,184
Mvaa 148,12,60
G 165,185
G 166,186
Zvaa 116,12,56
Ta 0,2,16384
Ta 2,2,17408
Mvaa 116,8,40
buffer_load_dwordx4 v[188:191], v26, s[8:11], s28 offen offset:0 sc1
G 167,187
Zvaa 84,8,36
Ta 4,2,24576
G 172,188
Mvaa 84,12,52
Ta 6,2,25600
G 173,189
Zvaa 52,12,48
G 174,190
G 175,191
Mvaa 52,8,32
W 8
Ta 8,3,16384
K 164,164,165
Zvaa 56,16,32
buffer_load_dwordx4 v[196:199], v26, s[8:11], s93 offen offset:0 sc1
Ta 10,3,17408
Mvaa 56,20,48
K 165,166,167
K 166,172,173
Zvaa 88,20,52
Ta 12,3,24576
K 167,174,175
Mvaa 88,16,36
Ta 14,3,25600
v_accvgpr_write_b32 a184, v164
Zvaa 120,16,40
Mvaa 120,20,56
buffer_load_dwordx4 v[204:207], v27, s[8:11], s28 offen offset:0 sc1
v_accvgpr_write_b32 a185, v165
Zvaa 152,20,60
v_accvgpr_write_b32 a186, v166
Mvaa 152,16,44
W 8
Ta 16,4,16384
v_accvgpr_write_b32 a187, v167
Zvaa 156,24,44
Ta 18,4,17408
v_accvgpr_write_b32 a188, v240
Mvaa 156,28,60
Zvaa 124,28,56
)"
R"(buffer_load_dwordx4 v[212:215], v27, s[8:11], s93 offen offset:0 sc1
v_accvgpr_write_b32 a189, v241
Mvaa 124,24,40
s_add_u32 s28, s28, 512
s_cmp_eq_u32 s28, s4
s_cselect_b32 s93, 10240, 0
s_sub_u32 s28, s28, s93
Zvaa 92,24,36
s_add_u32 s93, s28, 20480
Ta 20,4,24576
Ta 22,4,25600
Mvaa 92,28,52
v_accvgpr_write_b32 a190, v242
v_accvgpr_write_b32 a191, v243
Zvaa 60,28,48
G 164,192
Mvaa 60,24,32
s_waitcnt vmcnt(4) lgkmcnt(8)
buffer_load_dwordx4 v[220:223], v26, s[8:11], s28 offen offset:0 sc1
G 165,193
Mvaa 32,0,128
Ta 24,5,16384
Ta 26,5,17408
Mvaa 32,4,144
G 166,194
G 167,195
Mvaa 64,4,148
Ta 28,5,24576
Ta 30,5,25600
Mvaa 64,0,132
G 172,196
Mvaa 96,0,136
buffer_load_dwordx4 v[228:231], v26, s[8:11], s93 offen offset:0 sc1
G 173,197
Mvaa 96,4,152
G 174,198
Mvaa 128,4,156
G 175,199
Mvaa 128,0,140
W 8
Ta 0,6,16384
Ta 2,6,17408
Mvaa 132,8,140
K 164,164,165
Mvaa 132,12,156
buffer_load_dwordx4 v[236:239], v27, s[8:11], s28 offen offset:0 sc1
K 165,166,167
Mvaa 100,12,152
Ta 4,6,24576
K 166,172,173
Mvaa 100,8,136
Ta 6,6,25600
K 167,174,175
Mvaa 68,8,132
v_accvgpr_write_b32 a192, v164
Mvaa 68,12,148
Mvaa 36,12,144
buffer_load_dwordx4 v[244:247], v27, s[8:11], s93 offen offset:0 sc1
v_accvgpr_write_b32 a193, v165
Mvaa 36,8,128
W 8
s_add_u32 s28, s28, 512
s_cmp_eq_u32 s28, s4
s_cselect_b32 s93, 10240, 0
Mvaa 40,16,128
s_sub_u32 s28, s28, s93
s_add_u32 s93, s28, 20480
v_accvgpr_write_b32 a194, v166
Mvaa 40,20,144
Ta 8,7,16384
Ta 10,7,17408
Mvaa 72,20,148
v_accvgpr_write_b32 a195, v167
v_accvgpr_write_b32 a196, v248
Mvaa 72,16,132
D 22,48,65,2048
Ta 12,7,24576
Mvaa 104,16,136
Ta 14,7,25600
v_accvgpr_write_b32 a197, v249
Mvaa 104,20,152
v_accvgpr_write_b32 a198, v250
v_accvgpr_write_b32 a199, v251
Mvaa 136,20,156
G 164,200
Mvaa 136,16,140
W 8
Ta 16,8,16384
Ta 18,8,17408
Mvaa 140,24,140
buffer_load_dwordx4 a[32:35], v26, s[8:11], s28 offen offset:0 sc1
G 165,201
Mvaa 140,28,156
G 166,202
Mvaa 108,28,152
Ta 20,8,24576
G 167,203
Mvaa 108,24,136
Ta 22,8,25600
G 172,204
Mvaa 76,24,132
Mvaa 76,28,148
buffer_load_dwordx4 a[36:39], v26, s[8:11], s93 offen offset:0 sc1
G 173,205
Mvaa 44,28,144
G 174,206
Mvaa 44,24,128
W 8
Ta 24,9,16384
G 175,207
Mvaa 48,0,128
Ta 26,9,17408
K 164,164,165
Mvaa 48,4,144
Mvaa 80,4,148
buffer_load_dwordx4 a[40:43], v27, s[8:11], s28 offen offset:0 sc1
K 165,166,167
Mvaa 80,0,132
Ta 28,9,24576
Ta 30,9,25600
Mvaa 112,0,136
s_lshl_b32 s80, s56, 9
K 166,172,173
K 167,174,175
Mvaa 112,4,152
v_accvgpr_write_b32 a200, v164
Mvaa 144,4,156
s_lshl_b32 s81, s57, 4
Mvaa 144,0,140
W 8
buffer_load_dwordx4 a[44:47], v27, s[8:11], s93 offen offset:0 sc1
v_accvgpr_write_b32 a201, v165
Mvaa 148,8,140
s_add_u32 s28, s28, 512
s_cmp_eq_u32 s28, s4
s_cselect_b32 s93, 10240, 0
s_sub_u32 s28, s28, s93
Mvaa 148,12,156
s_add_u32 s93, s28, 20480
s_add_u32 s80, s80, s81
Ta 0,10,0
Mvaa 116,12,152
s_and_b32 s81, s14, 1
Ta 2,10,1024
v_accvgpr_write_b32 a202, v166
Mvaa 116,8,136
s_lshl_b32 s81, s81, 3
Ta 4,10,8192
v_accvgpr_write_b32 a203, v167
Mvaa 84,8,132
s_add_u32 s80, s80, s81
buffer_load_dwordx4 a[48:51], v26, s[8:11], s28 offen offset:0 sc1
Ta 6,10,9216
Mvaa 84,12,148
s_add_u32 s71, s80, 4096
v_accvgpr_write_b32 a204, v184
v_accvgpr_write_b32 a205, v185
Mvaa 52,12,144
v_accvgpr_write_b32 a206, v186
v_accvgpr_write_b32 a207, v187
Mvaa 52,8,128
W 8
Ta 8,11,0
Ta 10,11,1024
Mvaa 56,16,128
G 164,208
Mvaa 56,20,144
D 23,48,65,3072
G 165,209
Mvaa 88,20,148
s_add_u32 s65, s5, 16
s_cmp_ge_u32 s65, 20
s_cselect_b32 s90, 20, 0
s_sub_u32 s65, s65, s90
Mvaa 88,16,132
s_lshl_b32 s65, s65, 18
s_add_u32 m0, s63, 98304
G 166,210
Mvaa 120,16,136
Ta 12,11,8192
Ta 14,11,9216
Mvaa 120,20,152
G 167,211
G 172,212
Mvaa 152,20,156
buffer_load_dwordx4 a[52:55], v26, s[8:11], s93 offen offset:0 sc1
G 173,213
Mvaa 152,16,140
W 8
Ta 16,12,0
G 174,214
Mvaa 156,24,140
s_lshr_b32 s80, s15, 3
Ta 18,12,1024
Mvaa 156,28,156
G 175,215
Mvaa 124,28,152
s_lshl_b32 s80, s80, 1
Ta 20,12,8192
K 164,164,165
Mvaa 124,24,136
s_add_u32 s80, s80, s14
buffer_load_dwordx4 a[56:59], v27, s[8:11], s28 offen offset:0 sc1
Ta 22,12,9216
Mvaa 92,24,132
s_sub_u32 s80, s80, 2
K 165,166,167
K 166,172,173
Mvaa 92,28,148
s_lshl_b32 s80, s80, 12
Mvaa 60,28,144
K 167,174,175
v_min_u32 v28, 496, v1
Mvaa 60,24,128
s_waitcnt vmcnt(9) lgkmcnt(8)
v_accvgpr_write_b32 a208, v164
v_lshl_add_u32 v28, v28, 3, s80
Mvav 32,0,188
buffer_load_dwordx4 a[60:63], v27, s[8:11], s93 offen offset:0 sc1
Ta 24,13,0
Mvav 32,4,220
s_add_u32 s28, s28, 512
s_cmp_eq_u32 s28, s4
s_cselect_b32 s93, 10240, 0
s_sub_u32 s28, s28, s93
Mvav 64,4,228
s_add_u32 s93, s28, 20480
Ta 26,13,1024
v_min_u32 v28, .Lend%=-.Lbeg%=-4, v28
Mvav 64,0,196
v_accvgpr_write_b32 a209, v165
v_accvgpr_write_b32 a210, v166
Mvav 96,0,204
Ta 28,13,8192
Ta 30,13,9216
Mvav 96,4,236
D 16,48,65,0
v_accvgpr_write_b32 a211, v167
Mvav 128,4,244
v_accvgpr_write_b32 a212, v192
v_accvgpr_write_b32 a213, v193
Mvav 128,0,212
W 8
Ta 0,14,0
v_accvgpr_write_b32 a214, v194
Mvav 132,8,212
Ta 2,14,1024
v_add_u32 v2, 65536, v10
Mvav 132,12,244
v_accvgpr_write_b32 a215, v195
G 164,216
Mvav 100,12,236
buffer_load_dwordx4 v[184:187], v26, s[8:11], s28 offen offset:0 sc1
Ta 4,14,8192
Mvav 100,8,204
Ta 6,14,9216
v_add_u32 v3, 65536, v11
Mvav 68,8,196
G 165,217
G 166,218
Mvav 68,12,228
G 167,219
Mvav 36,12,220
G 172,220
Mvav 36,8,188
W 8
D 17,48,65,1024
Ta 8,15,0
Mvav 40,16,188
Ta 10,15,1024
v_add_u32 v4, 65536, v12
Mvav 40,20,220
G 173,221
G 174,222
Mvav 72,20,228
Ta 12,15,8192
G 175,223
Mvav 72,16,196
Ta 14,15,9216
v_add_u32 v5, 65536, v13
Mvav 104,16,204
buffer_load_dwordx4 a[128:131], v26, s[8:11], s93 offen offset:0 sc1
K 164,164,165
Mvav 104,20,236
K 165,166,167
Mvav 136,20,244
K 166,172,173
Mvav 136,16,212
W 8
Ta 16,24,0
K 167,174,175
Mvav 140,24,212
Ta 18,24,1024
v_add_u32 v6, 65536, v14
Mvav 140,28,244
buffer_load_dwordx4 a[132:135], v27, s[8:11], s28 offen offset:0 sc1
v_accvgpr_write_b32 a216, v164
Mvav 108,28,236
Ta 20,24,8192
v_accvgpr_write_b32 a217, v165
Mvav 108,24,204
Ta 22,24,9216
v_add_u32 v7, 65536, v15
Mvav 76,24,196
v_accvgpr_write_b32 a218, v166
v_accvgpr_write_b32 a219, v167
Mvav 76,28,228
v_accvgpr_write_b32 a220, v200
Mvav 44,28,220
buffer_load_dwordx4 a[136:139], v27, s[8:11], s93 offen offset:0 sc1
Mvav 44,24,188
W 8
s_add_u32 s28, s28, 512
s_cmp_eq_u32 s28, s4
s_cselect_b32 s93, 10240, 0
Mvav 48,0,188
s_sub_u32 s28, s28, s93
s_add_u32 s93, s28, 20480
v_accvgpr_write_b32 a221, v201
Mvav 48,4,220
Ta 24,25,0
Ta 26,25,1024
Mvav 80,4,228
v_add_u32 v8, 65536, v24
v_accvgpr_write_b32 a222, v202
Mvav 80,0,196
buffer_load_dwordx4 a[140:143], v26, s[8:11], s28 offen offset:0 sc1
v_accvgpr_write_b32 a223, v203
Mvav 112,0,204
Ta 28,25,8192
Ta 30,25,9216
Mvav 112,4,236
v_add_u32 v9, 65536, v25
G 164,224
Mvav 144,4,244
G 165,225
G 166,226
Mvav 144,0,212
W 8
Ta 0,10,16384
G 167,227
Mvav 148,8,212
global_load_dword v29, v28, s[6:7]
Ta 2,10,17408
Mvav 148,12,244
s_sub_u32 s74, s85, 66
s_and_b32 s75, s74, 0xffffffe3
s_cselect_b64 s[72:73], -1, 0
s_and_b32 s74, s74, 28
Mvav 116,12,236
s_lshl_b32 s74, s74, 1
v_mov_b32 v28, s74
G 172,228
Mvav 116,8,204
G 173,229
G 174,230
Mvav 84,8,196
Ta 4,10,24576
Ta 6,10,25600
Mvav 84,12,228
buffer_load_dwordx4 a[144:147], v26, s[8:11], s93 offen offset:0 sc1
G 175,231
Mvav 52,12,220
K 164,164,165
K 165,166,167
Mvav 52,8,188
W 8
Ta 8,11,16384
K 166,172,173
Mvav 56,16,188
Ta 10,11,17408
K 167,174,175
Mvav 56,20,220
v_accvgpr_write_b32 a224, v164
Mvav 88,20,228
buffer_load_dwordx4 a[148:151], v27, s[8:11], s28 offen offset:0 sc1
Ta 12,11,24576
Mvav 88,16,196
Ta 14,11,25600
v_accvgpr_write_b32 a225, v165
Mvav 120,16,204
v_accvgpr_write_b32 a226, v166
Mvav 120,20,236
v_accvgpr_write_b32 a227, v167
Mvav 152,20,244
v_accvgpr_write_b32 a228, v208
Mvav 152,16,212
W 8
D 18,48,65,2048
Ta 16,12,16384
Mvav 156,24,212
Ta 18,12,17408
v_accvgpr_write_b32 a229, v209
Mvav 156,28,244
v_accvgpr_write_b32 a230, v210
Mvav 124,28,236
Ta 20,12,24576
v_accvgpr_write_b32 a231, v211
Mvav 124,24,204
Ta 22,12,25600
)"
R"(G 164,232
Mvav 92,24,196
buffer_load_dwordx4 a[152:155], v27, s[8:11], s93 offen offset:0 sc1
G 165,233
Mvav 92,28,228
s_add_u32 s28, s28, 512
s_cmp_eq_u32 s28, s4
s_cselect_b32 s93, 10240, 0
s_sub_u32 s28, s28, s93
Mvav 60,28,220
s_add_u32 s93, s28, 20480
G 166,234
G 167,235
Mvav 60,24,188
s_waitcnt vmcnt(12) lgkmcnt(8)
G 172,236
G 173,237
Mvaa 32,0,32
Ta 24,13,16384
Ta 26,13,17408
Mvaa 32,4,48
buffer_load_dwordx2 v[28:29], v28, s[8:11], 0 offen offset:3584 sc1
G 174,238
Mvaa 64,4,52
G 175,239
K 164,164,165
Mvaa 64,0,36
Ta 28,13,24576
Ta 30,13,25600
Mvaa 96,0,40
K 165,166,167
K 166,172,173
Mvaa 96,4,56
K 167,174,175
v_accvgpr_write_b32 a232, v164
Mvaa 128,4,60
buffer_load_dwordx4 a[156:159], v26, s[8:11], s28 offen offset:0 sc1
v_accvgpr_write_b32 a233, v165
Mvaa 128,0,44
W 8
v_accvgpr_write_b32 a234, v166
v_accvgpr_write_b32 a235, v167
Mvaa 132,8,44
Ta 0,14,16384
Ta 2,14,17408
Mvaa 132,12,60
G 164,160
G 165,161
Mvaa 100,12,56
G 166,162
G 167,163
Mvaa 100,8,40
buffer_load_dwordx4 v[192:195], v26, s[8:11], s93 offen offset:0 sc1
Ta 4,14,24576
Mvaa 68,8,36
Ta 6,14,25600
G 172,164
Mvaa 68,12,52
G 173,165
G 174,166
Mvaa 36,12,48
G 175,167
K 164,164,165
Mvaa 36,8,32
W 8
Ta 8,15,16384
K 165,166,167
Mvaa 40,16,32
buffer_load_dwordx4 v[200:203], v27, s[8:11], s28 offen offset:0 sc1
Ta 10,15,17408
Mvaa 40,20,48
K 166,172,173
K 167,174,175
Mvaa 72,20,52
s_cmp_lg_u32 s72, 0
s_cbranch_scc1 .LL_55%=
s_waitcnt vmcnt(3)
.LL_55%=:
v_xor_b32 v28, s12, v28
v_xor_b32 v29, s13, v29
v_lshrrev_b32 v248, 11, v28
v_or_b32 v29, v29, v248
v_cmp_eq_u32 vcc, 0, v29
s_or_b64 vcc, vcc, s[72:73]
s_cmp_eq_u64 vcc, -1
s_cbranch_scc0 .Lspin_56%=
.Lok_57%=:
v_accvgpr_write_b32 a236, v164
v_accvgpr_write_b32 a237, v165
Mvaa 72,16,36
Ta 12,15,24576
Ta 14,15,25600
Mvaa 104,16,40
v_accvgpr_write_b32 a238, v166
v_accvgpr_write_b32 a239, v167
Mvaa 104,20,56
D 19,48,65,3072
G 164,240
Mvaa 136,20,60
s_add_u32 m0, s63, 102400
G 165,241
G 166,242
Mvaa 136,16,44
W 8
v_bfe_u32 v248, v28, 3, 8
G 167,243
Mvaa 140,24,44
Ta 16,24,16384
Ta 18,24,17408
Mvaa 140,28,60
v_mul_u32_u24 v248, 10240, v248
G 172,244
Mvaa 108,28,56
buffer_load_dwordx4 v[188:191], v27, s[8:11], s93 offen offset:0 sc1
G 173,245
Mvaa 108,24,40
s_add_u32 s28, s28, 512
s_cmp_eq_u32 s28, s4
s_cselect_b32 s93, 10240, 0
s_sub_u32 s28, s28, s93
Mvaa 76,24,36
s_add_u32 s93, s28, 20480
v_and_b32 v28, 7, v28
G 174,246
Mvaa 76,28,52
v_lshl_add_u32 v248, v28, 4, v248
G 175,247
Mvaa 44,28,48
Ta 20,24,24576
Ta 22,24,25600
Mvaa 44,24,32
W 8
buffer_load_dwordx4 v[196:199], v26, s[8:11], s28 offen offset:0 sc1
K 164,164,165
Mvaa 48,0,32
v_lshl_add_u32 v248, v1, 3, v248
K 165,166,167
Mvaa 48,4,48
v_lshrrev_b32 v29, 8, v1
K 166,172,173
Mvaa 80,4,52
K 167,174,175
v_lshlrev_b32 v29, 28, v29
Mvaa 80,0,36
v_accvgpr_write_b32 a240, v164
v_or_b32 v249, v248, v29
Mvaa 112,0,40
s_and_b32 s75, s72, 134217728
buffer_load_dwordx4 v[204:207], v26, s[8:11], s93 offen offset:0 sc1
v_accvgpr_write_b32 a241, v165
Mvaa 112,4,56
s_add_u32 s76, s75, 131072
Ta 24,25,16384
Ta 26,25,17408
Mvaa 144,4,60
v_accvgpr_write_b32 a242, v166
v_accvgpr_write_b32 a243, v167
Mvaa 144,0,44
W 6
G 164,168
G 165,169
Mvaa 148,8,44
Ta 28,25,24576
Ta 30,25,25600
Mvaa 148,12,60
D 20,48,65,0
G 166,170
Mvaa 116,12,56
G 167,171
G 172,172
Mvaa 116,8,40
G 173,173
G 174,174
Mvaa 84,8,36
Ta 0,2,0
G 175,175
Mvaa 84,12,52
Ta 2,2,1024
K 164,164,165
Mvaa 52,12,48
buffer_load_dwordx4 v[212:215], v27, s[8:11], s28 offen offset:0 sc1
K 165,166,167
Mvaa 52,8,32
W 6
K 166,172,173
K 167,174,175
Mvaa 56,16,32
Ta 4,2,8192
Ta 6,2,9216
Mvaa 56,20,48
v_accvgpr_write_b32 a244, v164
v_accvgpr_write_b32 a245, v165
Mvaa 88,20,52
v_accvgpr_write_b32 a246, v166
v_accvgpr_write_b32 a247, v167
Mvaa 88,16,36
buffer_load_dwordx4 v[220:223], v27, s[8:11], s93 offen offset:0 sc1
G 164,64
Mvaa 120,16,40
s_add_u32 s28, s28, 512
s_cmp_eq_u32 s28, s4
s_cselect_b32 s93, 10240, 0
s_sub_u32 s28, s28, s93
Mvaa 120,20,56
s_add_u32 s93, s28, 20480
Ta 8,3,0
G 165,65
Mvaa 152,20,60
Ta 10,3,1024
G 166,66
Mvaa 152,16,44
W 6
G 167,67
G 172,68
Mvaa 156,24,44
buffer_load_dwordx4 v[228:231], v26, s[8:11], s28 offen offset:0 sc1
G 173,69
Mvaa 156,28,60
Ta 12,3,8192
Ta 14,3,9216
Mvaa 124,28,56
G 174,70
G 175,71
Mvaa 124,24,40
K 164,164,165
K 165,166,167
Mvaa 92,24,36
K 166,172,173
K 167,174,175
Mvaa 92,28,52
buffer_load_dwordx4 v[240:243], v248, s[8:11], s76 offen sc1
Ta 16,4,0
Mvaa 60,28,48
s_add_u32 s76, s76, 8192
Ta 18,4,1024
v_accvgpr_write_b32 a64, v164
Mvaa 60,24,32
s_waitcnt vmcnt(13) lgkmcnt(6)
v_accvgpr_write_b32 a65, v165
v_accvgpr_write_b32 a66, v166
Mvav 32,0,184
Ta 20,4,8192
v_accvgpr_write_b32 a67, v167
Mvaa 32,4,140
Ta 22,4,9216
G 164,176
Mvaa 64,4,144
buffer_load_dwordx4 v[244:247], v249, s[8:11], s76 offen sc1
v_or_b32 v249, v1, v29
Mvaa 64,0,128
s_mul_i32 s76, s74, 160
s_add_u32 s76, s76, s75
s_add_u32 s76, s76, 118784
Mvaa 96,0,132
G 165,177
G 166,178
Mvaa 96,4,148
G 167,179
G 172,180
Mvaa 128,4,152
Ta 24,5,0
G 173,181
Mvaa 128,0,136
W 5
buffer_load_dwordx4 v[236:239], v26, s[8:11], s93 offen offset:0 sc1
Ta 26,5,1024
Mvaa 132,8,136
G 174,182
G 175,183
Mvaa 132,12,152
K 164,164,165
K 165,166,167
Mvaa 100,12,148
Ta 28,5,8192
Ta 30,5,9216
Mvaa 100,8,132
K 166,172,173
K 167,174,175
Mvaa 68,8,128
D 21,48,65,1024
v_accvgpr_write_b32 a68, v164
Mvaa 68,12,144
v_accvgpr_write_b32 a69, v165
v_accvgpr_write_b32 a70, v166
Mvaa 36,12,140
Ta 0,6,0
v_accvgpr_write_b32 a71, v167
Mvav 36,8,184
W 5
Ta 2,6,1024
G 164,72
Mvav 40,16,184
G 165,73
G 166,74
Mvaa 40,20,140
buffer_load_dwordx4 v[208:211], v27, s[8:11], s28 offen offset:0 sc1
Ta 4,6,8192
Mvaa 72,20,144
Ta 6,6,9216
G 167,75
Mvaa 72,16,128
G 172,76
G 173,77
Mvaa 104,16,132
Ta 8,7,0
G 174,78
Mvaa 104,20,148
Ta 10,7,1024
G 175,79
Mvaa 136,20,152
buffer_load_dwordx4 a[32:35], v27, s[8:11], s93 offen offset:0 sc1
K 164,164,165
Mvaa 136,16,136
W 6
s_add_u32 s28, s28, 512
s_cmp_eq_u32 s28, s4
s_cselect_b32 s93, 10240, 0
Mvaa 140,24,136
s_sub_u32 s28, s28, s93
s_add_u32 s93, s28, 20480
Ta 12,7,8192
Mvaa 140,28,152
Ta 14,7,9216
K 165,166,167
Mvaa 108,28,148
K 166,172,173
K 167,174,175
Mvaa 108,24,132
buffer_load_dwordx4 a[36:39], v26, s[8:11], s28 offen offset:0 sc1
v_accvgpr_write_b32 a72, v164
Mvaa 76,24,128
Ta 16,8,0
Ta 18,8,1024
Mvaa 76,28,144
v_accvgpr_write_b32 a73, v165
v_accvgpr_write_b32 a74, v166
Mvaa 44,28,140
Ta 20,8,8192
v_accvgpr_write_b32 a75, v167
Mvav 44,24,184
W 7
Ta 22,8,9216
v_accvgpr_write_b32 a76, v216
Mvav 48,0,184
D 22,48,65,2048
v_accvgpr_write_b32 a77, v217
Mvaa 48,4,140
v_accvgpr_write_b32 a78, v218
v_accvgpr_write_b32 a79, v219
Mvaa 80,4,144
Ta 24,9,0
Ta 26,9,1024
Mvaa 80,0,128
G 164,80
G 165,81
Mvaa 112,0,132
Ta 28,9,8192
Ta 30,9,9216
Mvaa 112,4,148
buffer_load_dwordx4 a[40:43], v26, s[8:11], s93 offen offset:0 sc1
G 166,82
Mvaa 144,4,152
G 167,83
G 172,84
Mvaa 144,0,136
W 8
Ta 0,2,16384
G 173,85
Mvaa 148,8,136
Ta 2,2,17408
G 174,86
Mvaa 148,12,152
G 175,87
Mvaa 116,12,148
buffer_load_dwordx4 a[44:47], v27, s[8:11], s28 offen offset:0 sc1
Ta 4,2,24576
Mvaa 116,8,132
Ta 6,2,25600
K 164,164,165
Mvaa 84,8,128
K 165,166,167
Mvaa 84,12,144
K 166,172,173
Mvaa 52,12,140
K 167,174,175
Mvav 52,8,184
W 8
buffer_load_dwordx4 a[48:51], v27, s[8:11], s93 offen offset:0 sc1
Ta 8,3,16384
Mvav 56,16,184
s_add_u32 s28, s28, 512
s_cmp_eq_u32 s28, s4
s_cselect_b32 s93, 10240, 0
D 23,48,65,3072
Mvaa 56,20,140
s_sub_u32 s28, s28, s93
s_add_u32 s93, s28, 20480
Ta 10,3,17408
Mvaa 88,20,144
v_accvgpr_write_b32 a80, v164
)"
R"(v_accvgpr_write_b32 a81, v165
Mvaa 88,16,128
Ta 12,3,24576
v_accvgpr_write_b32 a82, v166
Mvaa 120,16,132
Ta 14,3,25600
v_accvgpr_write_b32 a83, v167
Mvaa 120,20,148
buffer_load_dwordx4 a[52:55], v26, s[8:11], s28 offen offset:0 sc1
v_accvgpr_write_b32 a84, v224
Mvaa 152,20,152
v_lshrrev_b32 v20, 4, v1
v_accvgpr_write_b32 a85, v225
Mvaa 152,16,136
W 8
v_accvgpr_write_b32 a86, v226
v_add_u32 v20, 0, v20
Mvaa 156,24,136
Ta 16,4,16384
Ta 18,4,17408
Mvaa 156,28,152
v_accvgpr_write_b32 a87, v227
v_mul_u32_u24 v22, 205, v20
Mvaa 124,28,148
buffer_load_dwordx4 a[56:59], v26, s[8:11], s93 offen offset:0 sc1
G 164,88
Mvaa 124,24,132
Ta 20,4,24576
Ta 22,4,25600
Mvaa 92,24,128
v_lshrrev_b32 v22, 12, v22
G 165,89
Mvaa 92,28,144
G 166,90
v_mul_u32_u24 v23, 20, v22
Mvaa 60,28,140
G 167,91
v_sub_u32 v20, v20, v23
Mvav 60,24,184
s_waitcnt vmcnt(15) lgkmcnt(8)
buffer_load_dwordx4 a[60:63], v27, s[8:11], s28 offen offset:0 sc1
G 172,92
Mvaa 32,0,156
Ta 24,5,16384
Ta 26,5,17408
Mvav 32,4,196
v_lshrrev_b32 v23, 1, v22
G 173,93
Mvav 64,4,204
G 174,94
v_and_b32 v23, 2, v23
Mvav 64,0,192
Ta 28,5,24576
Ta 30,5,25600
Mvav 96,0,200
buffer_load_dwordx4 v[216:219], v27, s[8:11], s93 offen offset:0 sc1
G 175,95
Mvav 96,4,212
s_add_u32 s28, s28, 512
s_cmp_eq_u32 s28, s4
s_cselect_b32 s93, 10240, 0
s_sub_u32 s28, s28, s93
Mvav 128,4,220
s_add_u32 s93, s28, 20480
v_xor_b32 v20, v20, v23
K 164,164,165
Mvav 128,0,188
W 8
v_mul_u32_u24 v22, 80, v22
K 165,166,167
Mvav 132,8,188
K 166,172,173
v_add_lshl_u32 v16, v20, v22, 4
Mvav 132,12,220
buffer_load_dwordx4 v[184:187], v26, s[8:11], s28 offen offset:0 sc1
Ta 0,6,16384
Mvav 100,12,212
Ta 2,6,17408
K 167,174,175
Mvav 100,8,200
v_lshrrev_b32 v20, 4, v1
v_accvgpr_write_b32 a88, v164
Mvav 68,8,192
v_add_u32 v20, 64, v20
v_accvgpr_write_b32 a89, v165
Mvav 68,12,204
Ta 4,6,24576
Ta 6,6,25600
Mvav 36,12,196
s_cmp_lg_u32 s72, 0
s_cbranch_scc1 .LL_59%=
s_waitcnt vmcnt(15)
.LL_59%=:
buffer_store_dwordx4 v[240:243], v1, s[8:11], s76 offen sc1
v_accvgpr_write_b32 a90, v166
v_mul_u32_u24 v22, 205, v20
Mvaa 36,8,156
W 8
v_accvgpr_write_b32 a91, v167
v_lshrrev_b32 v22, 12, v22
Mvaa 40,16,156
v_accvgpr_write_b32 a92, v232
v_mul_u32_u24 v23, 20, v22
Mvav 40,20,196
Ta 8,7,16384
Ta 10,7,17408
Mvav 72,20,204
v_accvgpr_write_b32 a93, v233
v_accvgpr_write_b32 a94, v234
Mvav 72,16,192
buffer_store_dwordx4 v[244:247], v249, s[8:11], s76 offen offset:1024 sc1
v_sub_u32 v20, v20, v23
Mvav 104,16,200
s_add_u32 s76, s74, s75
s_add_u32 s76, s76, 3840
v_accvgpr_write_b32 a95, v235
Mvav 104,20,212
Ta 12,7,24576
Ta 14,7,25600
Mvav 136,20,220
v_lshrrev_b32 v23, 1, v22
G 164,96
Mvav 136,16,188
W 8
G 165,97
v_and_b32 v23, 2, v23
Mvav 140,24,188
buffer_load_dwordx4 a[128:131], v26, s[8:11], s93 offen offset:0 sc1
G 166,98
Mvav 140,28,220
G 167,99
v_xor_b32 v20, v20, v23
Mvav 108,28,212
G 172,100
G 173,101
Mvav 108,24,200
Ta 16,8,16384
Ta 18,8,17408
Mvav 76,24,192
v_mul_u32_u24 v22, 80, v22
G 174,102
Mvav 76,28,204
buffer_load_dwordx4 a[132:135], v27, s[8:11], s28 offen offset:0 sc1
G 175,103
Mvav 44,28,196
v_add_lshl_u32 v17, v20, v22, 4
K 164,164,165
Mvaa 44,24,156
W 6
Ta 20,8,24576
Ta 22,8,25600
Mvaa 48,0,156
K 165,166,167
K 166,172,173
Mvav 48,4,196
v_subrev_u32 v17, 1024, v17
K 167,174,175
Mvav 80,4,204
buffer_load_dwordx4 a[136:139], v27, s[8:11], s93 offen offset:0 sc1
v_accvgpr_write_b32 a96, v164
Mvav 80,0,192
s_add_u32 s28, s28, 512
v_lshrrev_b32 v20, 4, v1
v_accvgpr_write_b32 a97, v165
Mvav 112,0,200
s_cmp_eq_u32 s28, s4
s_cselect_b32 s93, 10240, 0
v_accvgpr_write_b32 a98, v166
v_add_u32 v20, 128, v20
Mvav 112,4,212
s_sub_u32 s28, s28, s93
Ta 24,9,16384
v_accvgpr_write_b32 a99, v167
Mvav 144,4,220
s_add_u32 s93, s28, 20480
Ta 26,9,17408
G 164,248
Mvav 144,0,188
W 6
buffer_load_dwordx4 a[140:143], v26, s[8:11], s28 offen offset:0 sc1
v_mul_u32_u24 v22, 205, v20
Mvav 148,8,188
G 165,249
G 166,250
Mvav 148,12,220
Ta 28,9,24576
Ta 30,9,25600
Mvav 116,12,212
v_lshrrev_b32 v22, 12, v22
G 167,251
Mvav 116,8,200
G 172,252
G 173,253
Mvav 84,8,192
s_waitcnt vmcnt(11) lgkmcnt(0)
s_barrier
v_mul_u32_u24 v23, 20, v22
Mvav 84,12,204
G 174,254
G 175,255
Mvav 52,12,196
v_sub_u32 v20, v20, v23
K 164,164,165
Mvaa 52,8,156
K 165,166,167
v_lshrrev_b32 v23, 1, v22
Mvaa 56,16,156
buffer_load_dwordx4 a[144:147], v26, s[8:11], s93 offen offset:0 sc1
K 166,172,173
Mvav 56,20,196
K 167,174,175
v_and_b32 v23, 2, v23
Mvav 88,20,204
v_accvgpr_write_b32 a100, v164
v_accvgpr_write_b32 a101, v165
Mvav 88,16,192
v_xor_b32 v20, v20, v23
v_accvgpr_write_b32 a102, v166
Mvav 120,16,200
Ta 0,10,32768
v_accvgpr_write_b32 a103, v167
Mvav 120,20,212
Ta 2,10,33792
Ta 4,10,40960
Mvav 152,20,220
Ta 6,10,41984
G 164,104
Mvav 152,16,188
v_mul_u32_u24 v22, 80, v22
G 165,105
Mvav 156,24,188
v_add_lshl_u32 v18, v20, v22, 4
G 166,106
Mvav 156,28,220
Ta 8,11,32768
Ta 10,11,33792
Mvav 124,28,212
G 167,107
v_subrev_u32 v18, 2048, v18
Mvav 124,24,200
buffer_load_dwordx4 a[148:151], v27, s[8:11], s28 offen offset:0 sc1
G 172,108
Mvav 92,24,192
G 173,109
v_lshrrev_b32 v20, 4, v1
Mvav 92,28,204
Ta 12,11,40960
Ta 14,11,41984
Mvav 60,28,196
G 174,110
v_add_u32 v20, 192, v20
Mvaa 60,24,156
W 4
G 175,111
K 164,164,165
Mvav 32,0,228
v_mul_u32_u24 v22, 205, v20
K 165,166,167
Mvaa 32,4,36
v_lshrrev_b32 v22, 12, v22
K 166,172,173
Mvaa 64,4,40
Ta 16,12,32768
Ta 18,12,33792
Mvav 64,0,236
buffer_load_dwordx4 a[152:155], v27, s[8:11], s93 offen offset:0 sc1
K 167,174,175
Mvav 96,0,208
s_add_u32 s28, s28, 512
v_mul_u32_u24 v23, 20, v22
v_accvgpr_write_b32 a104, v164
Mvaa 96,4,44
s_cmp_eq_u32 s28, s4
s_cselect_b32 s93, 10240, 0
v_accvgpr_write_b32 a105, v165
v_sub_u32 v20, v20, v23
Mvaa 128,4,48
s_sub_u32 s28, s28, s93
Ta 20,12,40960
Ta 22,12,41984
Mvaa 128,0,32
W 4
v_accvgpr_write_b32 a106, v166
v_lshrrev_b32 v23, 1, v22
Mvaa 132,8,32
s_add_u32 s93, s28, 20480
v_accvgpr_write_b32 a107, v167
K 176,176,177
Mvaa 132,12,48
v_and_b32 v23, 2, v23
K 177,178,179
Mvaa 100,12,44
K 178,180,181
v_xor_b32 v20, v20, v23
Mvav 100,8,208
Ta 24,13,32768
Ta 26,13,33792
Mvav 68,8,236
buffer_load_dwordx4 v[224:227], v26, s[8:11], s28 offen offset:0 sc1
K 179,182,183
Mvaa 68,12,40
v_accvgpr_write_b32 a108, v176
v_accvgpr_write_b32 a109, v177
v_accvgpr_write_b32 a110, v178
v_accvgpr_write_b32 a111, v179
Mvaa 36,12,36
Ta 28,13,40960
v_mul_u32_u24 v22, 80, v22
Mvav 36,8,228
W 3
Ta 30,13,41984
v_add_lshl_u32 v19, v20, v22, 4
Mvav 40,16,228
v_subrev_u32 v19, 3072, v19
G 164,112
Mvaa 40,20,36
s_cmp_lg_u32 s72, 0
s_cbranch_scc1 .LL_60%=
s_waitcnt vmcnt(8)
.LL_60%=:
v_mov_b64 v[28:29], s[12:13]
s_mov_b64 exec, 1
buffer_store_dwordx2 v[28:29], off, s[8:11], s76 sc1
s_mov_b64 exec, -1
v_lshrrev_b32 v20, 4, v1
G 165,113
Mvaa 72,20,40
Ta 0,14,32768
Ta 2,14,33792
Mvav 72,16,236
G 166,114
v_add_u32 v20, 256, v20
Mvav 104,16,208
G 167,115
v_mul_u32_u24 v22, 205, v20
Mvaa 104,20,44
Ta 4,14,40960
Ta 6,14,41984
Mvaa 136,20,48
buffer_load_dwordx4 v[232:235], v26, s[8:11], s93 offen offset:0 sc1
G 172,116
Mvaa 136,16,32
W 4
v_lshrrev_b32 v22, 12, v22
G 173,117
Mvaa 140,24,32
G 174,118
v_mul_u32_u24 v23, 20, v22
Mvaa 140,28,48
G 175,119
v_sub_u32 v20, v20, v23
Mvaa 108,28,44
Ta 8,15,32768
Ta 10,15,33792
Mvav 108,24,208
buffer_load_dwordx4 a[248:251], v27, s[8:11], s28 offen offset:0 sc1
K 164,164,165
Mvav 76,24,236
v_lshrrev_b32 v23, 1, v22
K 165,166,167
Mvaa 76,28,40
K 166,172,173
v_and_b32 v23, 2, v23
Mvaa 44,28,36
Ta 12,15,40960
Ta 14,15,41984
Mvav 44,24,228
W 4
K 167,174,175
v_xor_b32 v20, v20, v23
Mvav 48,0,228
v_accvgpr_write_b32 a112, v164
v_accvgpr_write_b32 a113, v165
Mvaa 48,4,36
v_mul_u32_u24 v22, 80, v22
v_accvgpr_write_b32 a114, v166
Mvaa 80,4,40
Ta 16,24,32768
Ta 18,24,33792
Mvav 80,0,236
v_add_lshl_u32 v20, v20, v22, 4
v_accvgpr_write_b32 a115, v167
Mvav 112,0,208
buffer_load_dwordx4 a[252:255], v27, s[8:11], s93 offen offset:0 sc1
v_lshrrev_b32 v22, 6, v1
Mvaa 112,4,44
s_add_u32 s28, s28, 512
Ta 20,24,40960
v_accvgpr_write_b32 a116, v168
Mvaa 144,4,48
v_accvgpr_write_b32 a117, v169
v_accvgpr_write_b32 a118, v170
v_accvgpr_write_b32 a119, v171
Mvaa 144,0,32
W 3
Ta 22,24,41984
v_mul_u32_u24 v22, 320, v22
Mvaa 148,8,32
s_cmp_eq_u32 s28, s4
s_cselect_b32 s93, 10240, 0
)"
R"(v_bfe_u32 v23, v1, 4, 2
v_lshl_add_u32 v22, v23, 3, v22
Mvaa 148,12,48
s_sub_u32 s28, s28, s93
Ta 24,25,32768
G 164,120
Mvaa 116,12,44
s_add_u32 s93, s28, 20480
Ta 26,25,33792
G 165,121
Mvav 116,8,208
buffer_load_dwordx4 v[192:195], v26, s[8:11], s28 offen offset:0 sc1
v_bfe_u32 v23, v1, 8, 1
Mvav 84,8,236
G 166,122
v_lshlrev_b32 v23, 5, v23
Mvaa 84,12,40
Ta 28,25,40960
G 167,123
Mvaa 52,12,36
Ta 30,25,41984
v_add_u32 v21, v22, v23
Mvav 52,8,228
W 4
G 172,124
G 173,125
Mvav 56,16,228
v_sub_u32 v22, v22, v23
G 174,126
Mvaa 56,20,36
s_mul_i32 s94, s59, 655360
Ta 0,10,49152
Ta 2,10,50176
Mvaa 88,20,40
s_mul_i32 s95, s58, 320
buffer_load_dwordx4 v[200:203], v26, s[8:11], s93 offen offset:0 sc1
G 175,127
Mvav 88,16,236
s_add_u32 s94, s94, s95
K 164,164,165
K 165,166,167
Mvav 120,16,208
Ta 4,10,57344
Ta 6,10,58368
Mvaa 120,20,44
s_add_u32 s40, s26, s94
s_addc_u32 s41, s27, 0
K 166,172,173
Mvaa 152,20,48
s_and_b32 s41, s41, 0xffff
K 167,174,175
v_accvgpr_write_b32 a120, v164
Mvaa 152,16,32
W 4
Ta 8,11,49152
v_accvgpr_write_b32 a121, v165
Mvaa 156,24,32
s_mov_b32 s42, 5242880
Ta 10,11,50176
buffer_load_dwordx4 v[188:191], v27, s[8:11], s28 offen offset:0 sc1
Mvaa 156,28,48
s_mov_b32 s43, 0x20000
v_accvgpr_write_b32 a122, v166
v_accvgpr_write_b32 a123, v167
Mvaa 124,28,44
s_sub_u32 s42, s42, s94
Ta 12,11,57344
Ta 14,11,58368
Mvav 124,24,208
s_mov_b64 s[44:45], s[8:9]
v_accvgpr_write_b32 a124, v160
v_accvgpr_write_b32 a125, v161
Mvav 92,24,236
s_mov_b32 s47, 0x20000
v_accvgpr_write_b32 a126, v162
v_accvgpr_write_b32 a127, v163
Mvaa 92,28,40
s_and_b32 s48, s14, 1
Ta 16,12,49152
Ta 18,12,50176
Mvaa 60,28,36
s_mul_i32 s48, s48, 5120
buffer_load_dwordx4 v[196:199], v27, s[8:11], s93 offen offset:0 sc1
v_bfe_u32 v28, v1, 4, 2
Mvav 60,24,228
s_waitcnt vmcnt(13) lgkmcnt(6)
Ta 20,12,57344
v_lshlrev_b32 v28, 3, v28
Mvaa 32,0,52
s_lshl_b32 s80, s56, 9
s_add_u32 s80, s80, 4096
Ta 22,12,58368
Mvav 32,4,184
s_lshr_b32 s51, s14, 1
s_and_b32 s94, s14, 1
Ta 24,13,49152
Mvaa 64,4,128
s_add_u32 s28, s28, 512
s_cmp_eq_u32 s28, s4
s_cselect_b32 s93, 10240, 0
Ta 26,13,50176
Mvaa 64,0,56
s_sub_u32 s28, s28, s93
s_add_u32 s93, s28, 20480
buffer_load_dwordx2 v[28:29], v28, s[8:11], s80 offen sc1
Mvaa 96,0,60
s_lshl_b32 s95, s57, 1
Ta 28,13,57344
Ta 30,13,58368
Mvaa 96,4,132
s_add_u32 s94, s94, s95
s_lshl_b32 s54, s94, 3
Mvaa 128,4,136
s_add_u32 s54, s54, s59
Mvav 128,0,216
W 8
Ta 0,14,49152
Ta 2,14,50176
Mvav 132,8,216
s_lshl_b32 s54, s54, 6
buffer_load_dwordx4 v[204:207], v26, s[8:11], s28 offen offset:0 sc1
Mvaa 132,12,136
s_xor_b32 s55, s54, 15872
Mvaa 100,12,132
Ta 4,14,57344
Ta 6,14,58368
Mvaa 100,8,60
s_add_u32 s54, s54, 20480
Mvaa 68,8,56
s_and_b32 s81, s14, 1
Mvaa 68,12,128
s_add_u32 s55, s55, 20480
buffer_load_dwordx4 v[212:215], v26, s[8:11], s93 offen offset:0 sc1
Mvav 36,12,184
Mvaa 36,8,52
W 8
s_and_b32 s95, s14, 2
s_mul_i32 s91, s81, 17164288
Mvaa 40,16,52
s_lshl_b32 s94, s94, 2
Ta 8,15,49152
Ta 10,15,50176
Mvav 40,20,184
s_mul_i32 s82, s56, 536576
Mvaa 72,20,128
s_add_u32 s94, s94, s95
buffer_load_dwordx4 v[220:223], v27, s[8:11], s28 offen offset:0 sc1
Ta 12,15,57344
Mvaa 72,16,56
Ta 14,15,58368
Mvaa 104,16,60
s_lshl_b32 s95, s95, 4
s_add_u32 s91, s91, s82
Mvaa 104,20,132
Mvaa 136,20,136
s_add_u32 s55, s55, s95
s_add_u32 s91, s91, 4194304
Mvav 136,16,216
W 8
s_lshl_b32 s49, s94, 17
Ta 16,24,49152
Mvav 140,24,216
Ta 18,24,50176
buffer_load_dwordx4 v[176:179], v27, s[8:11], s93 offen offset:0 sc1
Mvaa 140,28,136
s_lshl_b32 s95, s59, 14
s_lshr_b32 s82, s14, 1
Mvaa 108,28,132
Ta 20,24,57344
Ta 22,24,58368
Mvaa 108,24,60
s_add_u32 s49, s49, s95
s_lshl_b32 s83, s82, 14
Mvaa 76,24,56
s_xor_b32 s50, s49, 16252928
Mvaa 76,28,128
Mvav 44,28,184
s_add_u32 s91, s91, s83
s_add_u32 s49, s49, 39845888
Mvaa 44,24,52
W 8
Ta 24,25,49152
Ta 26,25,50176
Mvaa 48,0,52
s_add_u32 s50, s50, 39845888
s_lshl_b32 s81, s81, 14
Mvav 48,4,184
Mvaa 80,4,128
s_lshr_b32 s94, s15, 3
Ta 28,25,57344
Ta 30,25,58368
Mvaa 80,0,56
s_lshl_b32 s82, s82, 16
s_lshl_b32 s94, s94, 3
Mvaa 112,0,60
Mvaa 112,4,132
s_add_u32 s94, s94, s14
s_or_b32 s92, s81, s82
Mvaa 144,4,136
Mvav 144,0,216
W 8
s_lshl_b32 s94, s94, 4
Ta 0,2,32768
Mvav 148,8,216
s_xor_b32 s92, s92, 0x10000
s_add_u32 s94, s94, s59
Ta 2,2,33792
Mvaa 148,12,136
Mvaa 116,12,132
s_mul_i32 s52, s94, 5120
Ta 4,2,40960
Ta 6,2,41984
Mvaa 116,8,60
Mvaa 84,8,56
s_lshl_b32 s95, s59, 12
Mvaa 84,12,128
Mvav 52,12,184
s_sub_u32 s52, s52, s95
Mvaa 52,8,52
W 8
s_add_u32 s52, s52, 56623104
Ta 8,3,32768
Mvaa 56,16,52
Ta 10,3,33792
Mvav 56,20,184
s_lshl_b32 s53, s94, 3
Mvaa 88,20,128
Ta 12,3,40960
Ta 14,3,41984
Mvaa 88,16,56
s_add_u32 s53, s53, 65536
Mvaa 120,16,60
s_waitcnt vmcnt(4)
v_cmp_eq_u64 vcc, s[12:13], v[28:29]
s_cmp_eq_u64 vcc, -1
s_cbranch_scc0 .Lspin_61%=
.Lok_62%=:
s_sub_u32 s82, s91, 16384
Mvaa 120,20,132
s_xor_b32 s83, s92, 0x10000
s_add_u32 m0, s83, 0
s_add_u32 s81, s82, 0
Mvaa 152,20,136
v_bfe_u32 v244, v1, 4, 1
v_mul_u32_u24 v244, 0xaaaa, v244
Mvav 152,16,216
W 8
Dc 1,8,81,0
v_add_u32 v244, 0x4444, v244
Mvav 156,24,216
Ta 16,4,32768
Ta 18,4,33792
Mvaa 156,28,136
v_lshl_or_b32 v240, v244, 16, v244
Mvaa 124,28,132
Ta 20,4,40960
v_mov_b32 v241, 0xff800000
Mvaa 124,24,60
Ta 22,4,41984
v_add_u32 v242, 0x10000, v1
Mvaa 92,24,56
Dc 1,8,81,1024
Mvaa 92,28,128
v_bfe_u32 v244, v1, 4, 4
Mvav 60,28,184
v_lshrrev_b32 v245, 8, v1
Mvaa 60,24,52
W 8
Ta 24,5,32768
v_lshlrev_b32 v245, 2, v245
Mvaa 32,0,140
Ta 26,5,33792
Mvav 32,4,224
Dc 1,8,81,2048
v_sub_u32 v244, v244, v245
Mvav 64,4,232
Ta 28,5,40960
v_cmp_le_i32_e64 s[72:73], 0, v244
Mvaa 64,0,144
Ta 30,5,41984
Mvaa 96,0,148
v_cmp_le_i32_e64 s[74:75], 1, v244
Mvaa 96,4,248
v_cmp_le_i32_e64 s[76:77], 2, v244
Mvaa 128,4,252
Dc 1,8,81,3072
v_cmp_le_i32_e64 s[78:79], 3, v244
Mvaa 128,0,152
W 8
s_add_u32 m0, s83, 4096
s_add_u32 s81, s82, 4096
Mvaa 132,8,152
Ta 0,6,32768
Ta 2,6,33792
Mvaa 132,12,252
v_add_u32 v243, 4096, v1
v_add_u32 v244, 8192, v1
Mvaa 100,12,248
Ta 4,6,40960
v_add_u32 v245, 12288, v1
Mvaa 100,8,148
Dc 1,8,81,0
Ta 6,6,41984
Mvaa 68,8,144
s_mov_b32 s18, 0x3f803f80
Mvav 68,12,232
s_mov_b32 s19, 0x3f003f00
Mvav 36,12,224
Mvaa 36,8,140
W 8
Ta 8,7,32768
v_accvgpr_write_b32 a176, s18
Mvaa 40,16,140
Dc 1,8,81,1024
Ta 10,7,33792
Mvav 40,20,224
v_accvgpr_write_b32 a180, s19
v_mov_b32 v252, v241
Mvav 72,20,232
Ta 12,7,40960
Ta 14,7,41984
Mvaa 72,16,144
v_accvgpr_write_b32 a177, s18
Mvaa 104,16,148
v_accvgpr_write_b32 a181, s19
Mvaa 104,20,248
Dc 1,8,81,2048
v_mov_b32 v253, v241
Mvaa 136,20,252
Mvaa 136,16,152
W 8
Ta 16,8,32768
v_accvgpr_write_b32 a178, s18
Mvaa 140,24,152
Ta 18,8,33792
v_accvgpr_write_b32 a182, s19
Mvaa 140,28,252
Mvaa 108,28,248
Dc 1,8,81,3072
v_mov_b32 v254, v241
Mvaa 108,24,148
s_add_u32 m0, s83, 8192
s_add_u32 s81, s82, 8192
Ta 20,8,40960
Mvaa 76,24,144
Ta 22,8,41984
v_accvgpr_write_b32 a179, s18
Mvav 76,28,232
v_accvgpr_write_b32 a183, s19
Mvav 44,28,224
v_mov_b32 v255, v241
Mvaa 44,24,140
W 8
Dc 1,8,81,0
v_accvgpr_write_b32 a160, 0
Mvaa 48,0,140
Ta 24,9,32768
Ta 26,9,33792
Mvav 48,4,224
v_accvgpr_write_b32 a161, 0
Mvav 80,4,232
Ta 28,9,40960
v_accvgpr_write_b32 a162, 0
Mvaa 80,0,144
Ta 30,9,41984
v_accvgpr_write_b32 a163, 0
Mvaa 112,0,148
Dc 1,8,81,1024
Mvaa 112,4,248
v_accvgpr_write_b32 a164, 0
Mvaa 144,4,252
v_accvgpr_write_b32 a165, 0
Mvaa 144,0,152
W 8
Ta 0,2,49152
v_accvgpr_write_b32 a166, 0
Mvaa 148,8,152
Ta 2,2,50176
Mvaa 148,12,252
Dc 1,8,81,2048
v_accvgpr_write_b32 a167, 0
Mvaa 116,12,248
Ta 4,2,57344
v_accvgpr_write_b32 a168, 0
Mvaa 116,8,148
Ta 6,2,58368
v_accvgpr_write_b32 a169, 0
Mvaa 84,8,144
Mvav 84,12,232
v_accvgpr_write_b32 a170, 0
Mvav 52,12,224
Dc 1,8,81,3072
v_accvgpr_write_b32 a171, 0
Mvaa 52,8,140
W 8
s_add_u32 m0, s83, 12288
s_add_u32 s81, s82, 12288
Mvaa 56,16,140
Ta 8,3,49152
Ta 10,3,50176
)"
R"(Mvav 56,20,224
v_accvgpr_write_b32 a172, 0
v_accvgpr_write_b32 a173, 0
Mvav 88,20,232
Ta 12,3,57344
v_accvgpr_write_b32 a174, 0
Mvaa 88,16,144
Dc 1,8,81,0
Ta 14,3,58368
Mvaa 120,16,148
v_accvgpr_write_b32 a175, 0
Mvaa 120,20,248
s_mov_b32 s90, 0x5d800000
Mvaa 152,20,252
s_mov_b64 s[88:89], 0
Mvaa 152,16,152
W 8
Ta 16,4,49152
Ta 18,4,50176
Mvaa 156,24,152
s_mov_b32 s64, 0
Dc 1,8,81,1024
Mvaa 156,28,252
s_and_b32 s18, s14, 1
Mvaa 124,28,248
s_lshl_b32 s62, s18, 14
Ta 20,4,57344
Ta 22,4,58368
Mvaa 124,24,148
Mvaa 92,24,144
s_lshl_b32 s20, s57, 15
Mvav 92,28,232
s_sub_u32 s20, 507904, s20
Dc 1,8,81,2048
Mvav 60,28,224
Mvaa 60,24,140
s_waitcnt vmcnt(15) lgkmcnt(8)
s_add_u32 s20, s20, s62
Ta 24,5,49152
Mvav 32,0,192
s_lshl_b32 s65, s57, 1
Ta 26,5,50176
Mvav 32,4,204
s_sub_u32 s65, s65, 1
Mvav 64,4,212
Dc 1,8,81,3072
Ta 28,5,57344
Mvav 64,0,200
s_max_i32 s65, s65, 0
Ta 30,5,58368
v_bfe_u32 v28, v1, 4, 4
Mvav 96,0,188
s_sub_u32 s66, s60, 1
v_lshlrev_b32 v28, 3, v28
Mvav 96,4,220
s_lshl_b32 s80, s56, 9
s_add_u32 s80, s80, 4096
s_cmp_lt_u32 s14, 2
s_cselect_b32 s20, s20, 0
s_cselect_b32 s66, s66, 15
s_cselect_b32 s21, 32768, 0
Mvav 128,4,176
Mvav 128,0,196
W 8
s_add_u32 s61, s91, s20
buffer_load_dwordx2 v[28:29], v28, s[8:11], s80 offen sc1
Mvav 132,8,196
s_add_u32 s62, s62, s21
Ta 0,6,49152
Ta 2,6,50176
Mvav 132,12,176
Mvav 100,12,220
s_sub_u32 s63, 16384, s21
Ta 4,6,57344
Ta 6,6,58368
Mvav 100,8,188
s_lshl_b32 s67, s60, 7
Mvav 68,8,200
s_add_u32 s67, s67, s56
Mvav 68,12,212
Mvav 36,12,204
s_lshl_b32 s67, s67, 12
Mvav 36,8,192
W 8
s_add_u32 s67, s67, 39845888
Ta 8,7,49152
Mvav 40,16,192
s_lshl_b32 s69, s60, 3
Ta 10,7,50176
Mvav 40,20,204
Mvav 72,20,212
s_add_u32 s69, s69, s59
Ta 12,7,57344
Ta 14,7,58368
Mvav 72,16,200
s_lshl_b32 s69, s69, 6
Mvav 104,16,188
Mvav 104,20,220
s_lshl_b32 s21, s58, 3
Mvav 136,20,176
s_add_u32 s69, s69, s21
Mvav 136,16,196
W 8
s_add_u32 s69, s69, 20480
Ta 16,8,49152
Mvav 140,24,196
Ta 18,8,50176
Mvav 140,28,176
s_mov_b32 s70, 0
Mvav 108,28,220
s_mov_b32 s37, 0
Ta 20,8,57344
Ta 22,8,58368
Mvav 108,24,188
s_sub_u32 s18, s57, 4
Mvav 76,24,200
Mvav 76,28,212
s_lshl_b32 s18, s18, 2
Mvav 44,28,204
s_add_u32 s18, s18, s58
Mvav 44,24,192
W 8
Ta 24,9,49152
Ta 26,9,50176
Mvav 48,0,192
s_mul_i32 s85, s59, 7
Mvav 48,4,204
s_add_u32 s85, s85, s18
Mvav 80,4,212
s_cmp_lt_u32 s18, 7
s_cselect_b32 s85, s85, 99
Ta 28,9,57344
Ta 30,9,58368
Mvav 80,0,200
Mvav 112,0,188
s_cmp_lt_u32 s85, 54
s_cselect_b32 s65, 2, s65
Mvav 112,4,220
s_mov_b32 s68, 0
Mvav 144,4,176
s_mov_b64 s[24:25], -1
Mvav 144,0,196
W 8
Mvav 148,8,196
s_mov_b32 s22, 0x3e0293ee
Mvav 148,12,176
s_mov_b32 s23, 0x3e0293ee
Mvav 116,12,220
Mvav 116,8,188
Mvav 84,8,200
Mvav 84,12,212
Mvav 52,12,204
Mvav 52,8,192
W 4
Mvav 56,16,192
Mvav 56,20,204
s_waitcnt vmcnt(0)
v_cmp_eq_u64 vcc, s[12:13], v[28:29]
s_cmp_eq_u64 vcc, -1
s_cbranch_scc0 .Lspin_64%=
.Lok_65%=:
Mvav 88,20,212
Mvav 88,16,200
Mvav 120,16,188
W 0
s_barrier
Ra 128,242,0
Mvav 120,20,220
Mvav 152,20,176
Ra 132,242,1024
Mvav 152,16,196
Mvav 156,24,196
Ra 136,242,2048
Mvav 156,28,176
Mvav 124,28,220
Ra 140,242,3072
Mvav 124,24,188
Mvav 92,24,200
Mvav 92,28,212
Mvav 60,28,204
Mvav 60,24,192
.Lbgo%=:
v_pk_mul_f32 v[32:33], v[32:33], s[22:23]
v_pk_mul_f32 v[34:35], v[34:35], s[22:23]
v_pk_mul_f32 v[36:37], v[36:37], s[22:23]
v_pk_mul_f32 v[38:39], v[38:39], s[22:23]
v_pk_mul_f32 v[64:65], v[64:65], s[22:23]
v_pk_mul_f32 v[66:67], v[66:67], s[22:23]
v_pk_mul_f32 v[68:69], v[68:69], s[22:23]
v_pk_mul_f32 v[70:71], v[70:71], s[22:23]
v_pk_mul_f32 v[96:97], v[96:97], s[22:23]
v_pk_mul_f32 v[98:99], v[98:99], s[22:23]
v_pk_mul_f32 v[100:101], v[100:101], s[22:23]
v_pk_mul_f32 v[102:103], v[102:103], s[22:23]
v_pk_mul_f32 v[128:129], v[128:129], s[22:23]
v_pk_mul_f32 v[130:131], v[130:131], s[22:23]
v_pk_mul_f32 v[132:133], v[132:133], s[22:23]
v_pk_mul_f32 v[134:135], v[134:135], s[22:23]
v_pk_mul_f32 v[40:41], v[40:41], s[22:23]
v_pk_mul_f32 v[42:43], v[42:43], s[22:23]
v_pk_mul_f32 v[44:45], v[44:45], s[22:23]
v_pk_mul_f32 v[46:47], v[46:47], s[22:23]
v_pk_mul_f32 v[72:73], v[72:73], s[22:23]
v_pk_mul_f32 v[74:75], v[74:75], s[22:23]
v_pk_mul_f32 v[76:77], v[76:77], s[22:23]
v_pk_mul_f32 v[78:79], v[78:79], s[22:23]
v_pk_mul_f32 v[104:105], v[104:105], s[22:23]
v_pk_mul_f32 v[106:107], v[106:107], s[22:23]
v_pk_mul_f32 v[108:109], v[108:109], s[22:23]
v_pk_mul_f32 v[110:111], v[110:111], s[22:23]
v_pk_mul_f32 v[136:137], v[136:137], s[22:23]
v_pk_mul_f32 v[138:139], v[138:139], s[22:23]
v_pk_mul_f32 v[140:141], v[140:141], s[22:23]
v_pk_mul_f32 v[142:143], v[142:143], s[22:23]
v_pk_mul_f32 v[48:49], v[48:49], s[22:23]
v_pk_mul_f32 v[50:51], v[50:51], s[22:23]
v_pk_mul_f32 v[52:53], v[52:53], s[22:23]
v_pk_mul_f32 v[54:55], v[54:55], s[22:23]
v_pk_mul_f32 v[80:81], v[80:81], s[22:23]
v_pk_mul_f32 v[82:83], v[82:83], s[22:23]
v_pk_mul_f32 v[84:85], v[84:85], s[22:23]
v_pk_mul_f32 v[86:87], v[86:87], s[22:23]
v_pk_mul_f32 v[112:113], v[112:113], s[22:23]
v_pk_mul_f32 v[114:115], v[114:115], s[22:23]
v_pk_mul_f32 v[116:117], v[116:117], s[22:23]
v_pk_mul_f32 v[118:119], v[118:119], s[22:23]
v_pk_mul_f32 v[144:145], v[144:145], s[22:23]
v_pk_mul_f32 v[146:147], v[146:147], s[22:23]
v_pk_mul_f32 v[148:149], v[148:149], s[22:23]
v_pk_mul_f32 v[150:151], v[150:151], s[22:23]
v_pk_mul_f32 v[56:57], v[56:57], s[22:23]
v_pk_mul_f32 v[58:59], v[58:59], s[22:23]
v_pk_mul_f32 v[60:61], v[60:61], s[22:23]
v_pk_mul_f32 v[62:63], v[62:63], s[22:23]
v_pk_mul_f32 v[88:89], v[88:89], s[22:23]
v_pk_mul_f32 v[90:91], v[90:91], s[22:23]
v_pk_mul_f32 v[92:93], v[92:93], s[22:23]
v_pk_mul_f32 v[94:95], v[94:95], s[22:23]
v_pk_mul_f32 v[120:121], v[120:121], s[22:23]
v_pk_mul_f32 v[122:123], v[122:123], s[22:23]
v_pk_mul_f32 v[124:125], v[124:125], s[22:23]
v_pk_mul_f32 v[126:127], v[126:127], s[22:23]
v_pk_mul_f32 v[152:153], v[152:153], s[22:23]
v_pk_mul_f32 v[154:155], v[154:155], s[22:23]
v_pk_mul_f32 v[156:157], v[156:157], s[22:23]
v_pk_mul_f32 v[158:159], v[158:159], s[22:23]
K 32,32,33
K 33,34,35
K 34,36,37
K 35,38,39
K 40,40,41
K 41,42,43
K 42,44,45
K 43,46,47
W 0
Zvav 196,128,32
s_add_u32 m0, s62, 0
Ra 144,242,4096
Ra 148,242,5120
Ra 152,242,6144
Ra 156,242,7168
Mvav 196,132,40
K 64,64,65
K 65,66,67
K 66,68,69
K 67,70,71
K 72,72,73
K 73,74,75
K 74,76,77
K 75,78,79
s_nop 1
Zvav 204,132,72
Dc 1,8,61,0
K 96,96,97
K 97,98,99
K 98,100,101
Mvav 204,128,64
K 99,102,103
K 104,104,105
K 105,106,107
K 106,108,109
K 107,110,111
Zvav 212,128,96
Dc 1,8,61,1024
K 128,128,129
K 129,130,131
K 130,132,133
Mvav 212,132,104
K 131,134,135
K 136,136,137
K 137,138,139
K 138,140,141
K 139,142,143
s_nop 1
Zvav 220,132,136
Dc 1,8,61,2048
K 48,48,49
K 49,50,51
K 50,52,53
Mvav 220,128,128
K 51,54,55
K 56,56,57
K 57,58,59
K 58,60,61
K 59,62,63
Mvav 196,136,48
Ra 128,242,8192
Ra 132,242,9216
K 80,80,81
K 81,82,83
Mvav 196,140,56
K 82,84,85
K 83,86,87
K 88,88,89
K 89,90,91
K 90,92,93
K 91,94,95
s_nop 1
Mvav 204,140,88
Dc 1,8,61,3072
s_add_u32 m0, s62, 4096
K 112,112,113
K 113,114,115
K 114,116,117
Mvav 204,136,80
K 115,118,119
K 120,120,121
K 121,122,123
K 122,124,125
K 123,126,127
Mvav 212,136,112
Dc 243,8,61,0
K 144,144,145
K 145,146,147
K 146,148,149
Mvav 212,140,120
K 147,150,151
K 152,152,153
K 153,154,155
K 154,156,157
K 155,158,159
s_nop 1
Mvav 220,140,152
s_add_u32 s80, s62, 131072
Dc 243,8,61,1024
v_add_u32 v12, s80, v1
Mvav 220,136,144
W 2
ds_write_b128 v12, v[32:35] offset:0
Ra 136,242,10240
Zvav 36,144,32
Ra 0,12,0
Ra 140,242,11264
Dc 243,8,61,2048
Mvav 36,148,40
v_max3_f32 v4, v196, v197, v198
v_max3_f32 v4, v4, v199, v252
v_max3_f32 v5, v204, v205, v206
v_max3_f32 v5, v5, v207, v253
Zvav 68,148,72
v_max3_f32 v6, v212, v213, v214
v_max3_f32 v6, v6, v215, v254
v_max3_f32 v7, v220, v221, v222
v_max3_f32 v7, v7, v223, v255
Mvav 68,144,64
v_mov_b32 v8, v4
v_mov_b32 v9, v5
v_mov_b32 v10, v6
v_mov_b32 v11, v7
Zvav 100,144,96
ds_write_b128 v12, v[40:43] offset:1024
v_permlane32_swap_b32 v4, v8
v_permlane32_swap_b32 v5, v9
v_permlane32_swap_b32 v6, v10
Mvav 100,148,104
v_permlane32_swap_b32 v7, v11
v_max_f32 v4, v4, v8
v_max_f32 v5, v5, v9
v_max_f32 v6, v6, v10
Zvav 132,148,136
v_max_f32 v7, v7, v11
v_mov_b32 v8, v4
v_mov_b32 v9, v5
v_mov_b32 v10, v6
Mvav 132,144,128
Ra 4,12,1024
v_mov_b32 v11, v7
v_permlane16_swap_b32 v4, v8
v_permlane16_swap_b32 v5, v9
Mvav 36,152,48
v_permlane16_swap_b32 v6, v10
v_permlane16_swap_b32 v7, v11
v_max_f32 v4, v4, v8
v_max_f32 v5, v5, v9
Mvav 36,156,56
v_max_f32 v6, v6, v10
v_max_f32 v7, v7, v11
v_sub_f32 v4, 0, v4
v_sub_f32 v5, 0, v5
Mvav 68,156,88
)"
R"(v_sub_f32 v6, 0, v6
v_sub_f32 v7, 0, v7
v_mov_b32 v224, v4
v_mov_b32 v225, v4
Mvav 68,152,80
v_mov_b32 v226, v4
v_mov_b32 v227, v4
v_mov_b32 v228, v5
v_mov_b32 v229, v5
Mvav 100,152,112
v_mov_b32 v230, v5
v_mov_b32 v231, v5
v_mov_b32 v232, v6
v_mov_b32 v233, v6
Mvav 100,156,120
v_mov_b32 v234, v6
v_mov_b32 v235, v6
v_mov_b32 v236, v7
v_mov_b32 v237, v7
Mvav 132,156,152
Dc 243,8,61,3072
s_add_u32 m0, s62, 8192
ds_write_b128 v12, v[48:51] offset:2048
v_mov_b32 v238, v7
v_mov_b32 v239, v7
Mvav 132,152,144
W 3
Ra 8,12,2048
ds_write_b128 v12, v[56:59] offset:3072
Ra 144,242,12288
Ra 12,12,3072
Cvav 160,128,32,224
s_and_b32 s80, s14, 1
Dc 244,8,61,0
Ra 148,242,13312
ds_write_b128 v12, v[64:67] offset:4096
Ra 16,12,4096
Mvav 160,132,40
s_lshl_b32 s80, s80, 14
ds_write_b128 v12, v[72:75] offset:5120
Ra 20,12,5120
ds_write_b128 v12, v[80:83] offset:6144
A 196,196,224
Cvav 164,132,72,228
s_add_u32 s80, s80, 98304
Dc 244,8,61,1024
Ra 24,12,6144
A 197,197,224
A 198,198,224
Mvav 164,128,64
Ra 152,242,14336
A 199,199,224
X 196
Cvav 168,128,96,232
Dc 244,8,61,2048
Ra 156,242,15360
X 197
Mvav 168,132,104
ds_write_b128 v12, v[88:91] offset:7168
Ra 28,12,7168
X 198
Cvav 172,132,136,236
Dc 244,8,61,3072
s_add_u32 m0, s62, 12288
K 192,196,197
X 199
Mvav 172,128,128
ds_write_b128 v12, v[96:99] offset:8192
A 204,204,228
v_add_u32 v8, s80, v1
K 193,198,199
Mvav 160,136,48
Dc 245,8,61,0
Ra 32,12,8192
A 205,205,228
A 206,206,228
Mvav 160,140,56
ds_write_b128 v8, v[4:7]
A 207,207,228
X 204
Mvav 164,140,88
Dc 245,8,61,1024
ds_write_b128 v12, v[104:107] offset:9216
X 205
Mvav 164,136,80
Ra 36,12,9216
X 206
K 200,204,205
Mvav 168,136,112
Dc 245,8,61,2048
Ra 128,242,24576
X 207
Mvav 168,140,120
ds_write_b128 v12, v[112:115] offset:10240
Ra 132,242,25600
A 212,212,232
K 201,206,207
Mvav 172,140,152
Dc 245,8,61,3072
s_add_u32 s61, s61, s63
Ra 40,12,10240
A 213,213,232
A 214,214,232
Mvav 172,136,144
W 11
ds_write_b128 v12, v[120:123] offset:11264
A 215,215,232
X 212
Cvav 176,144,32,224
X 213
X 214
Mvav 176,148,40
Ra 44,12,11264
K 208,212,213
X 215
Cvav 180,148,72,228
ds_write_b128 v12, v[128:131] offset:12288
A 220,220,236
K 209,214,215
A 221,221,236
Mvav 180,144,64
Ra 48,12,12288
Ra 136,242,26624
A 222,222,236
A 223,223,236
Cvav 184,144,96,232
ds_write_b128 v12, v[136:139] offset:13312
Ra 140,242,27648
X 220
Mvav 184,148,104
X 221
X 222
Cvav 188,148,136,236
Ra 52,12,13312
K 216,220,221
X 223
Mvav 188,144,128
ds_write_b128 v12, v[144:147] offset:14336
Ra 56,12,14336
A 36,36,224
K 217,222,223
Mvav 176,152,48
ds_write_b128 v12, v[152:155] offset:15360
A 37,37,224
A 38,38,224
A 39,39,224
Mvav 176,156,56
X 36
X 37
Mvav 180,156,88
Ra 60,12,15360
X 38
K 194,36,37
Mvav 180,152,80
Ra 144,242,28672
X 39
A 68,68,228
Mvav 184,152,112
Ra 148,242,29696
K 195,38,39
A 69,69,228
A 70,70,228
Mvav 184,156,120
W 3
s_barrier
A 71,71,228
X 68
Mvav 188,156,152
X 69
X 70
K 202,68,69
X 71
A 100,100,232
K 203,70,71
A 101,101,232
A 102,102,232
A 103,103,232
X 100
X 101
X 102
K 210,100,101
X 103
A 132,132,236
K 211,102,103
A 133,133,236
A 134,134,236
A 135,135,236
X 132
X 133
X 134
K 218,132,133
X 135
s_nop 0
K 219,134,135
Mvav 188,152,144
Ra 152,242,30720
Zvav 32,128,192
Ra 156,242,31744
Zvav 64,128,200
Zvav 96,128,208
s_xor_b32 s62, s62, 0x10000
Zvav 128,128,216
v_add_u32 v23, 61440, v21
Zvav 132,132,216
v_add_u32 v24, 61440, v22
Zvav 100,132,208
v_add_u32 v25, 122880, v21
Zvav 68,132,200
v_add_u32 v26, 122880, v22
Zvav 36,132,192
Zvav 40,136,192
X 172
Zvav 72,136,200
X 173
Zvav 104,136,208
X 174
K 220,172,173
Zvav 136,136,216
X 175
Zvav 140,140,216
X 168
K 221,174,175
Zvav 108,140,208
X 169
X 170
Zvav 76,140,200
s_waitcnt vmcnt(0) lgkmcnt(0)
s_barrier
v_xor_b32 v242, 0x10000, v242
K 212,168,169
Zvav 44,140,192
X 171
X 164
Zvav 48,144,192
Ra 128,242,0
K 213,170,171
X 165
Zvav 80,144,200
X 166
Zvav 112,144,208
Ra 132,242,1024
K 204,164,165
X 167
Zvav 144,144,216
X 160
Zvav 148,148,216
K 205,166,167
X 161
Zvav 116,148,208
Ra 136,242,2048
X 162
Zvav 84,148,200
K 196,160,161
X 163
Zvav 52,148,192
X 176
K 197,162,163
Zvav 56,152,192
Ra 140,242,3072
X 177
Zvav 88,152,200
X 178
K 198,176,177
Zvav 120,152,208
X 179
X 180
Zvav 152,152,216
K 199,178,179
Zvav 156,156,216
X 181
X 182
Zvav 124,156,208
K 206,180,181
X 183
Zvav 92,156,200
K 207,182,183
Zvav 60,156,192
s_cmp_eq_u64 s[24:25], 0
s_cbranch_scc1 .Ll1_34%=
W 0
Cvaa 160,128,0,224
Ra 144,242,4096
Ra 148,242,5120
Mvaa 160,132,4
s_add_u32 m0, s62, 0
Ra 152,242,6144
Cvaa 164,132,20,228
X 184
Mvaa 164,128,16
X 185
Cvaa 168,128,32,232
X 186
Mvaa 168,132,36
Ra 156,242,7168
K 214,184,185
Cvaa 172,132,52,236
Dc 1,8,61,0
Mvaa 172,128,48
X 187
Mvaa 160,136,8
X 188
Mvaa 160,140,12
Ra 128,242,16384
K 215,186,187
Mvaa 164,140,28
Dc 1,8,61,1024
Mvaa 164,136,24
X 189
Mvaa 168,136,40
X 190
Mvaa 168,140,44
Ra 132,242,17408
K 222,188,189
Mvaa 172,140,60
Dc 1,8,61,2048
Mvaa 172,136,56
X 191
v_smfmac_f32_16x16x64_bf16 a[160:163], a[176:179], v[192:199], v240
Ra 136,242,18432
K 223,190,191
v_smfmac_f32_16x16x64_bf16 a[164:167], a[176:179], v[200:207], v240
X 160
v_smfmac_f32_16x16x64_bf16 a[168:171], a[176:179], v[208:215], v240
Dc 1,8,61,3072
v_smfmac_f32_16x16x64_bf16 a[172:175], a[176:179], v[216:223], v240
W 3
X 161
Cvaa 176,144,0,224
s_add_u32 m0, s62, 4096
X 162
Mvaa 176,148,4
Ra 140,242,19456
K 192,160,161
Cvaa 180,148,20,228
X 163
Mvaa 180,144,16
Dc 243,8,61,0
Cvaa 184,144,32,232
X 164
Mvaa 184,148,36
K 193,162,163
Cvaa 188,148,52,236
X 165
Mvaa 188,144,48
Dc 243,8,61,1024
Ra 144,242,20480
Mvaa 176,152,8
X 166
Mvaa 176,156,12
Ra 148,242,21504
K 200,164,165
Mvaa 180,156,28
X 167
Mvaa 180,152,24
Dc 243,8,61,2048
Mvaa 184,152,40
X 168
Mvaa 184,156,44
K 201,166,167
Mvaa 188,156,60
X 169
Mvaa 188,152,56
W 2
Dc 243,8,61,3072
Ra 152,242,22528
Mvav 32,128,196
s_add_u32 m0, s62, 8192
X 170
Mvav 64,128,204
Ra 156,242,23552
K 208,168,169
Mvav 96,128,212
X 171
Mvav 128,128,220
Dc 244,8,61,0
Mvav 132,132,220
X 172
Mvav 100,132,212
Ra 128,242,8192
K 209,170,171
Mvav 68,132,204
X 173
Mvav 36,132,196
Dc 244,8,61,1024
Mvav 40,136,196
X 174
Mvav 72,136,204
Ra 132,242,9216
K 216,172,173
Mvav 104,136,212
X 175
Mvav 136,136,220
Dc 244,8,61,2048
Mvav 140,140,220
X 176
Mvav 108,140,212
Ra 136,242,10240
K 217,174,175
Mvav 76,140,204
X 177
)"
R"(Mvav 44,140,196
W 3
Dc 244,8,61,3072
Mvav 48,144,196
X 178
Mvav 80,144,204
s_add_u32 m0, s62, 12288
Ra 140,242,11264
Mvav 112,144,212
K 194,176,177
Mvav 144,144,220
X 179
Mvav 148,148,220
X 180
Mvav 116,148,212
Dc 245,8,61,0
Ra 144,242,12288
Mvav 84,148,204
K 195,178,179
Mvav 52,148,196
X 181
Mvav 56,152,196
X 182
Mvav 88,152,204
s_cmp_eq_u32 s14, 2
s_cselect_b64 s[18:19], s[30:31], s[32:33]
Dc 245,8,61,1024
Ra 148,242,13312
Mvav 120,152,212
s_mul_i32 s81, s58, 6
K 202,180,181
Mvav 152,152,220
s_add_u32 s81, s81, s57
X 183
Mvav 156,156,220
s_sub_u32 s81, s81, 2
X 184
Mvav 124,156,212
s_cmp_ge_u32 s57, 2
s_cselect_b32 s23, 2211836, 0
Dc 245,8,61,2048
Ra 152,242,14336
Mvav 92,156,204
s_mul_i32 s82, s81, 48384
K 203,182,183
Mvav 60,156,196
W 3
X 185
Cvaa 160,128,0,224
s_mul_i32 s83, s59, 448
X 186
Mvaa 160,132,4
s_andn2_b32 s83, s83, 127
Dc 245,8,61,3072
Ra 156,242,15360
Cvaa 164,132,20,228
s_add_u32 s82, s82, s83
s_add_u32 s61, s61, s63
K 210,184,185
Mvaa 164,128,16
v_min_u32 v246, 880, v1
v_lshrrev_b32 v247, 6, v246
Cvaa 168,128,32,232
X 187
Mvaa 168,132,36
X 188
Cvaa 172,132,52,236
s_xor_b32 s62, s62, 0x10000
v_mul_u32_u24 v247, 3456, v247
v_and_b32 v246, 48, v246
Mvaa 172,128,48
K 211,186,187
v_lshl_add_u32 v246, v246, 3, v247
Mvaa 160,136,8
X 189
Mvaa 160,140,12
s_add_u32 s64, s64, 1
Ra 128,242,24576
v_add_u32 v246, s82, v246
Mvaa 164,140,28
X 190
Mvaa 164,136,24
Ra 132,242,25600
v_min_u32 v246, s23, v246
Mvaa 168,136,40
global_load_dword v3, v246, s[18:19]
K 218,188,189
Mvaa 168,140,44
X 191
Mvaa 172,140,60
X 160
Mvaa 172,136,56
W 2
Ra 136,242,26624
K 219,190,191
Cvaa 176,144,0,224
X 161
Mvaa 176,148,4
Ra 140,242,27648
Cvaa 180,148,20,228
X 162
Mvaa 180,144,16
K 196,160,161
Cvaa 184,144,32,232
X 163
Mvaa 184,148,36
X 164
Cvaa 188,148,52,236
K 197,162,163
Mvaa 188,144,48
X 165
Mvaa 176,152,8
Ra 144,242,28672
Ra 148,242,29696
Mvaa 176,156,12
X 166
Mvaa 180,156,28
K 204,164,165
Mvaa 180,152,24
X 167
Mvaa 184,152,40
Mvaa 184,156,44
X 168
Mvaa 188,156,60
Mvaa 188,152,56
W 2
Ra 152,242,30720
K 205,166,167
Mvav 32,128,192
X 169
Mvav 64,128,200
Ra 156,242,31744
Mvav 96,128,208
X 170
Mvav 128,128,216
K 212,168,169
Mvav 132,132,216
X 171
Mvav 100,132,208
X 172
Mvav 68,132,200
Mvav 36,132,192
K 213,170,171
Mvav 40,136,192
X 173
Mvav 72,136,200
X 174
Mvav 104,136,208
Mvav 136,136,216
K 220,172,173
Mvav 140,140,216
X 175
Mvav 108,140,208
X 176
Mvav 76,140,200
s_waitcnt vmcnt(1) lgkmcnt(0)
s_barrier
v_xor_b32 v242, 0x10000, v242
Mvav 44,140,192
Ra 128,242,0
K 221,174,175
Mvav 48,144,192
X 177
Mvav 80,144,200
X 178
Mvav 112,144,208
Ra 132,242,1024
Mvav 144,144,216
K 198,176,177
Mvav 148,148,216
X 179
Mvav 116,148,208
X 180
Mvav 84,148,200
Ra 136,242,2048
Mvav 52,148,192
K 199,178,179
Mvav 56,152,192
X 181
Mvav 88,152,200
Ra 140,242,3072
Mvav 120,152,208
X 182
Mvav 152,152,216
K 206,180,181
Mvav 156,156,216
X 183
Mvav 124,156,208
Mvav 92,156,200
K 207,182,183
Mvav 60,156,192
W 0
Cvaa 160,128,0,224
Ra 144,242,4096
Ra 148,242,5120
Mvaa 160,132,4
s_add_u32 m0, s62, 0
Ra 152,242,6144
Cvaa 164,132,20,228
X 184
Mvaa 164,128,16
X 185
Cvaa 168,128,32,232
X 186
Mvaa 168,132,36
Ra 156,242,7168
K 214,184,185
Cvaa 172,132,52,236
Dc 1,8,61,0
Mvaa 172,128,48
X 187
Mvaa 160,136,8
X 188
Mvaa 160,140,12
Ra 128,242,16384
K 215,186,187
Mvaa 164,140,28
Dc 1,8,61,1024
Mvaa 164,136,24
X 189
Mvaa 168,136,40
X 190
Mvaa 168,140,44
Ra 132,242,17408
K 222,188,189
Mvaa 172,140,60
Dc 1,8,61,2048
Mvaa 172,136,56
X 191
v_smfmac_f32_16x16x64_bf16 a[160:163], a[176:179], v[192:199], v240
Ra 136,242,18432
K 223,190,191
v_smfmac_f32_16x16x64_bf16 a[164:167], a[176:179], v[200:207], v240
X 160
v_smfmac_f32_16x16x64_bf16 a[168:171], a[176:179], v[208:215], v240
Dc 1,8,61,3072
v_smfmac_f32_16x16x64_bf16 a[172:175], a[176:179], v[216:223], v240
W 3
X 161
Cvaa 176,144,0,224
s_add_u32 m0, s62, 4096
X 162
Mvaa 176,148,4
Ra 140,242,19456
K 192,160,161
Cvaa 180,148,20,228
X 163
Mvaa 180,144,16
Dc 243,8,61,0
Cvaa 184,144,32,232
X 164
Mvaa 184,148,36
K 193,162,163
Cvaa 188,148,52,236
X 165
Mvaa 188,144,48
Dc 243,8,61,1024
Ra 144,242,20480
Mvaa 176,152,8
X 166
Mvaa 176,156,12
Ra 148,242,21504
K 200,164,165
Mvaa 180,156,28
X 167
Mvaa 180,152,24
Dc 243,8,61,2048
Mvaa 184,152,40
X 168
Mvaa 184,156,44
K 201,166,167
Mvaa 188,156,60
X 169
Mvaa 188,152,56
W 2
Dc 243,8,61,3072
Ra 152,242,22528
Mvav 32,128,196
s_add_u32 m0, s62, 8192
X 170
Mvav 64,128,204
Ra 156,242,23552
K 208,168,169
Mvav 96,128,212
X 171
Mvav 128,128,220
Dc 244,8,61,0
Mvav 132,132,220
X 172
Mvav 100,132,212
Ra 128,242,8192
K 209,170,171
Mvav 68,132,204
X 173
Mvav 36,132,196
Dc 244,8,61,1024
Mvav 40,136,196
X 174
Mvav 72,136,204
Ra 132,242,9216
K 216,172,173
Mvav 104,136,212
X 175
Mvav 136,136,220
Dc 244,8,61,2048
Mvav 140,140,220
X 176
Mvav 108,140,212
Ra 136,242,10240
K 217,174,175
Mvav 76,140,204
X 177
Mvav 44,140,196
W 3
Dc 244,8,61,3072
Mvav 48,144,196
X 178
Mvav 80,144,204
s_add_u32 m0, s62, 12288
Ra 140,242,11264
Mvav 112,144,212
K 194,176,177
Mvav 144,144,220
X 179
Mvav 148,148,220
X 180
Mvav 116,148,212
Dc 245,8,61,0
Ra 144,242,12288
Mvav 84,148,204
K 195,178,179
Mvav 52,148,196
X 181
Mvav 56,152,196
X 182
Mvav 88,152,204
Dc 245,8,61,1024
Ra 148,242,13312
Mvav 120,152,212
v_add_u32 v246, 1161216, v246
v_min_u32 v246, s23, v246
Mvav 152,152,220
K 202,180,181
Mvav 156,156,220
X 183
Mvav 124,156,212
Dc 245,8,61,2048
Ra 152,242,14336
Mvav 92,156,204
X 184
Mvav 60,156,196
W 3
K 203,182,183
v_bfe_u32 v28, v1, 4, 4
Cvaa 160,128,0,224
X 185
Mvaa 160,132,4
Dc 245,8,61,3072
v_lshlrev_b32 v28, 3, v28
Cvaa 164,132,20,228
s_lshl_b32 s80, s56, 9
X 186
Mvaa 164,128,16
s_add_u32 s80, s80, 4224
Ra 156,242,15360
K 210,184,185
Cvaa 168,128,32,232
s_add_u32 s61, s61, s63
X 187
Mvaa 168,132,36
buffer_load_dwordx2 v[28:29], v28, s[8:11], s80 offen sc1
Cvaa 172,132,52,236
s_xor_b32 s62, s62, 0x10000
X 188
Mvaa 172,128,48
Ra 128,242,24576
K 211,186,187
Mvaa 160,136,8
X 189
Mvaa 160,140,12
s_add_u32 s64, s64, 1
global_load_dword v3, v246, s[18:19]
Ra 132,242,25600
Mvaa 164,140,28
s_mov_b64 s[18:19], s[34:35]
s_mul_i32 s81, s58, 6
s_add_u32 s81, s81, s57
Mvaa 164,136,24
s_sub_u32 s81, s81, 2
s_cmp_ge_u32 s57, 2
s_cselect_b32 s23, 2211836, 0
s_lshl_b32 s81, s81, 1
Mvaa 168,136,40
s_add_u32 s81, s81, s14
s_sub_u32 s81, s81, 2
s_mul_i32 s81, s81, 6016
Mvaa 168,140,44
s_mul_i32 s82, s59, 286720
s_add_u32 s81, s81, s82
v_min_u32 v246, 736, v1
)"
R"(Mvaa 172,140,60
v_lshl_add_u32 v246, v246, 3, s81
v_min_u32 v246, s23, v246
Mvaa 172,136,56
W 2
global_load_dword v3, v246, s[18:19]
Ra 136,242,26624
Cvaa 176,144,0,224
X 190
Mvaa 176,148,4
Ra 140,242,27648
K 218,188,189
Cvaa 180,148,20,228
X 191
Mvaa 180,144,16
X 160
Cvaa 184,144,32,232
K 219,190,191
Mvaa 184,148,36
X 161
Cvaa 188,148,52,236
X 162
Mvaa 188,144,48
Ra 144,242,28672
K 196,160,161
Mvaa 176,152,8
X 163
Mvaa 176,156,12
X 164
Mvaa 180,156,28
Ra 148,242,29696
K 197,162,163
Mvaa 180,152,24
X 165
Mvaa 184,152,40
X 166
Mvaa 184,156,44
K 204,164,165
Mvaa 188,156,60
X 167
Mvaa 188,152,56
W 2
X 168
Mvav 32,128,192
Ra 152,242,30720
K 205,166,167
Mvav 64,128,200
X 169
Mvav 96,128,208
Ra 156,242,31744
Mvav 128,128,216
X 170
Mvav 132,132,216
K 212,168,169
Mvav 100,132,208
X 171
Mvav 68,132,200
X 172
Mvav 36,132,192
K 213,170,171
Mvav 40,136,192
X 173
Mvav 72,136,200
X 174
Mvav 104,136,208
Mvav 136,136,216
s_waitcnt vmcnt(2)
v_cmp_eq_u64 vcc, s[12:13], v[28:29]
s_cmp_eq_u64 vcc, -1
s_cbranch_scc0 .Lspin_67%=
.Lok_68%=:
K 220,172,173
Mvav 140,140,216
X 175
Mvav 108,140,208
X 176
Mvav 76,140,200
W 0
s_barrier
v_xor_b32 v242, 0x10000, v242
Mvav 44,140,192
Ra 128,242,0
K 221,174,175
Mvav 48,144,192
X 177
Mvav 80,144,200
X 178
Mvav 112,144,208
Ra 132,242,1024
Mvav 144,144,216
K 198,176,177
Mvav 148,148,216
X 179
Mvav 116,148,208
X 180
Mvav 84,148,200
Ra 136,242,2048
Mvav 52,148,192
K 199,178,179
Mvav 56,152,192
X 181
Mvav 88,152,200
Ra 140,242,3072
Mvav 120,152,208
X 182
Mvav 152,152,216
K 206,180,181
Mvav 156,156,216
X 183
Mvav 124,156,208
Mvav 92,156,200
K 207,182,183
Mvav 60,156,192
W 0
Cvaa 160,128,0,224
Ra 144,242,4096
Ra 148,242,5120
Mvaa 160,132,4
s_add_u32 m0, s62, 0
Ra 152,242,6144
Cvaa 164,132,20,228
X 184
Mvaa 164,128,16
X 185
Cvaa 168,128,32,232
X 186
Mvaa 168,132,36
Ra 156,242,7168
K 214,184,185
Cvaa 172,132,52,236
Dc 1,8,61,0
Mvaa 172,128,48
X 187
Mvaa 160,136,8
X 188
Mvaa 160,140,12
Ra 128,242,16384
K 215,186,187
Mvaa 164,140,28
Dc 1,8,61,1024
Mvaa 164,136,24
X 189
Mvaa 168,136,40
X 190
Mvaa 168,140,44
Ra 132,242,17408
K 222,188,189
Mvaa 172,140,60
Dc 1,8,61,2048
Mvaa 172,136,56
X 191
v_smfmac_f32_16x16x64_bf16 a[160:163], a[176:179], v[192:199], v240
Ra 136,242,18432
K 223,190,191
v_smfmac_f32_16x16x64_bf16 a[164:167], a[176:179], v[200:207], v240
X 160
v_smfmac_f32_16x16x64_bf16 a[168:171], a[176:179], v[208:215], v240
Dc 1,8,61,3072
v_smfmac_f32_16x16x64_bf16 a[172:175], a[176:179], v[216:223], v240
W 3
X 161
Cvaa 176,144,0,224
s_add_u32 m0, s62, 4096
X 162
Mvaa 176,148,4
Ra 140,242,19456
K 192,160,161
Cvaa 180,148,20,228
X 163
Mvaa 180,144,16
Dc 243,8,61,0
Cvaa 184,144,32,232
X 164
Mvaa 184,148,36
K 193,162,163
Cvaa 188,148,52,236
X 165
Mvaa 188,144,48
Dc 243,8,61,1024
Ra 144,242,20480
Mvaa 176,152,8
X 166
Mvaa 176,156,12
Ra 148,242,21504
K 200,164,165
Mvaa 180,156,28
X 167
Mvaa 180,152,24
Dc 243,8,61,2048
Mvaa 184,152,40
X 168
Mvaa 184,156,44
K 201,166,167
Mvaa 188,156,60
X 169
Mvaa 188,152,56
W 2
Dc 243,8,61,3072
Ra 152,242,22528
Mvav 32,128,196
s_add_u32 m0, s62, 8192
X 170
Mvav 64,128,204
Ra 156,242,23552
K 208,168,169
Mvav 96,128,212
X 171
Mvav 128,128,220
Dc 244,8,61,0
Mvav 132,132,220
X 172
Mvav 100,132,212
Ra 128,242,8192
K 209,170,171
Mvav 68,132,204
X 173
Mvav 36,132,196
Dc 244,8,61,1024
Mvav 40,136,196
X 174
Mvav 72,136,204
Ra 132,242,9216
K 216,172,173
Mvav 104,136,212
X 175
Mvav 136,136,220
Dc 244,8,61,2048
Mvav 140,140,220
X 176
Mvav 108,140,212
Ra 136,242,10240
K 217,174,175
Mvav 76,140,204
X 177
Mvav 44,140,196
W 3
Dc 244,8,61,3072
Mvav 48,144,196
X 178
Mvav 80,144,204
s_add_u32 m0, s62, 12288
Ra 140,242,11264
Mvav 112,144,212
K 194,176,177
Mvav 144,144,220
X 179
Mvav 148,148,220
X 180
Mvav 116,148,212
Dc 245,8,61,0
Ra 144,242,12288
Mvav 84,148,204
K 195,178,179
Mvav 52,148,196
X 181
Mvav 56,152,196
X 182
Mvav 88,152,204
Dc 245,8,61,1024
Ra 148,242,13312
Mvav 120,152,212
K 202,180,181
Mvav 152,152,220
X 183
Mvav 156,156,220
X 184
Mvav 124,156,212
Dc 245,8,61,2048
Ra 152,242,14336
Mvav 92,156,204
K 203,182,183
Mvav 60,156,196
W 3
X 185
Cvaa 160,128,0,224
X 186
Mvaa 160,132,4
Dc 245,8,61,3072
Ra 156,242,15360
Cvaa 164,132,20,228
s_add_u32 s61, s61, s63
K 210,184,185
Mvaa 164,128,16
X 187
Cvaa 168,128,32,232
X 188
Mvaa 168,132,36
Cvaa 172,132,52,236
s_xor_b32 s62, s62, 0x10000
K 211,186,187
Mvaa 172,128,48
X 189
Mvaa 160,136,8
Ra 128,242,24576
Ra 132,242,25600
Mvaa 160,140,12
s_add_u32 s64, s64, 1
X 190
Mvaa 164,140,28
K 218,188,189
Mvaa 164,136,24
X 191
Mvaa 168,136,40
Mvaa 168,140,44
X 160
Mvaa 172,140,60
s_add_u32 s36, s29, 131072
K 219,190,191
Mvaa 172,136,56
W 2
X 161
Cvaa 176,144,0,224
Ra 136,242,26624
Ra 140,242,27648
Mvaa 176,148,4
X 162
Cvaa 180,148,20,228
K 196,160,161
Mvaa 180,144,16
buffer_store_dwordx4 a[124:127], v1, s[8:11], s36 offen offset:0 sc1
Cvaa 184,144,32,232
X 163
Mvaa 184,148,36
X 164
Cvaa 188,148,52,236
K 197,162,163
Mvaa 188,144,48
X 165
Mvaa 176,152,8
Ra 144,242,28672
buffer_store_dwordx4 a[116:119], v1, s[8:11], s36 offen offset:1024 sc1
Mvaa 176,156,12
Ra 148,242,29696
Mvaa 180,156,28
X 166
Mvaa 180,152,24
K 204,164,165
Mvaa 184,152,40
X 167
Mvaa 184,156,44
buffer_store_dwordx4 a[108:111], v1, s[8:11], s36 offen offset:2048 sc1
Mvaa 188,156,60
X 168
Mvaa 188,152,56
W 2
Ra 152,242,30720
K 205,166,167
Mvav 32,128,192
X 169
Mvav 64,128,200
Ra 156,242,31744
buffer_store_dwordx4 a[204:207], v1, s[8:11], s36 offen offset:3072 sc1
Mvav 96,128,208
X 170
Mvav 128,128,216
K 212,168,169
Mvav 132,132,216
X 171
Mvav 100,132,208
s_add_u32 s36, s29, 135168
X 172
Mvav 68,132,200
Mvav 36,132,192
K 213,170,171
Mvav 40,136,192
X 173
Mvav 72,136,200
X 174
Mvav 104,136,208
buffer_store_dwordx4 a[212:215], v1, s[8:11], s36 offen offset:0 sc1
Mvav 136,136,216
K 220,172,173
Mvav 140,140,216
X 175
Mvav 108,140,208
X 176
Mvav 76,140,200
s_waitcnt vmcnt(5) lgkmcnt(0)
s_barrier
v_xor_b32 v242, 0x10000, v242
Mvav 44,140,192
buffer_store_dwordx4 a[220:223], v1, s[8:11], s36 offen offset:1024 sc1
K 221,174,175
Mvav 48,144,192
Ra 128,242,0
Mvav 80,144,200
X 177
Mvav 112,144,208
X 178
Mvav 144,144,216
Ra 132,242,1024
K 198,176,177
Mvav 148,148,216
buffer_store_dwordx4 a[228:231], v1, s[8:11], s36 offen offset:2048 sc1
Mvav 116,148,208
X 179
Mvav 84,148,200
X 180
Mvav 52,148,192
Ra 136,242,2048
)"
R"(K 199,178,179
Mvav 56,152,192
buffer_store_dwordx4 a[76:79], v1, s[8:11], s36 offen offset:3072 sc1
Ra 140,242,3072
Mvav 88,152,200
X 181
Mvav 120,152,208
X 182
Mvav 152,152,216
K 206,180,181
Mvav 156,156,216
X 183
Mvav 124,156,208
Mvav 92,156,200
K 207,182,183
Mvav 60,156,192
W 0
Cvaa 160,128,0,224
Ra 144,242,4096
Ra 148,242,5120
Mvaa 160,132,4
s_add_u32 m0, s62, 0
Ra 152,242,6144
Cvaa 164,132,20,228
X 184
Mvaa 164,128,16
X 185
Cvaa 168,128,32,232
X 186
Mvaa 168,132,36
Ra 156,242,7168
K 214,184,185
Cvaa 172,132,52,236
Dc 1,8,61,0
Mvaa 172,128,48
X 187
Mvaa 160,136,8
X 188
Mvaa 160,140,12
Ra 128,242,16384
K 215,186,187
Mvaa 164,140,28
Dc 1,8,61,1024
Mvaa 164,136,24
X 189
Mvaa 168,136,40
X 190
Mvaa 168,140,44
Ra 132,242,17408
K 222,188,189
Mvaa 172,140,60
Dc 1,8,61,2048
Mvaa 172,136,56
X 191
v_smfmac_f32_16x16x64_bf16 a[160:163], a[176:179], v[192:199], v240
Ra 136,242,18432
K 223,190,191
v_smfmac_f32_16x16x64_bf16 a[164:167], a[176:179], v[200:207], v240
X 160
v_smfmac_f32_16x16x64_bf16 a[168:171], a[176:179], v[208:215], v240
Dc 1,8,61,3072
v_smfmac_f32_16x16x64_bf16 a[172:175], a[176:179], v[216:223], v240
W 3
X 161
Cvaa 176,144,0,224
s_add_u32 m0, s62, 4096
X 162
Mvaa 176,148,4
Ra 140,242,19456
K 192,160,161
Cvaa 180,148,20,228
X 163
Mvaa 180,144,16
Dc 243,8,61,0
Cvaa 184,144,32,232
X 164
Mvaa 184,148,36
K 193,162,163
Cvaa 188,148,52,236
X 165
Mvaa 188,144,48
Dc 243,8,61,1024
Ra 144,242,20480
Mvaa 176,152,8
X 166
Mvaa 176,156,12
Ra 148,242,21504
K 200,164,165
Mvaa 180,156,28
X 167
Mvaa 180,152,24
Dc 243,8,61,2048
Mvaa 184,152,40
X 168
Mvaa 184,156,44
K 201,166,167
Mvaa 188,156,60
X 169
Mvaa 188,152,56
W 2
Dc 243,8,61,3072
Ra 152,242,22528
Mvav 32,128,196
s_add_u32 m0, s62, 8192
X 170
Mvav 64,128,204
Ra 156,242,23552
K 208,168,169
Mvav 96,128,212
X 171
Mvav 128,128,220
Dc 244,8,61,0
Mvav 132,132,220
X 172
Mvav 100,132,212
Ra 128,242,8192
K 209,170,171
Mvav 68,132,204
X 173
Mvav 36,132,196
Dc 244,8,61,1024
Mvav 40,136,196
X 174
Mvav 72,136,204
Ra 132,242,9216
K 216,172,173
Mvav 104,136,212
X 175
Mvav 136,136,220
Dc 244,8,61,2048
Mvav 140,140,220
X 176
Mvav 108,140,212
Ra 136,242,10240
K 217,174,175
Mvav 76,140,204
X 177
Mvav 44,140,196
W 3
Dc 244,8,61,3072
Mvav 48,144,196
X 178
Mvav 80,144,204
s_add_u32 m0, s62, 12288
Ra 140,242,11264
Mvav 112,144,212
K 194,176,177
Mvav 144,144,220
X 179
Mvav 148,148,220
X 180
Mvav 116,148,212
Dc 245,8,61,0
Ra 144,242,12288
Mvav 84,148,204
K 195,178,179
Mvav 52,148,196
X 181
Mvav 56,152,196
X 182
Mvav 88,152,204
Dc 245,8,61,1024
Ra 148,242,13312
Mvav 120,152,212
K 202,180,181
Mvav 152,152,220
X 183
Mvav 156,156,220
X 184
Mvav 124,156,212
Dc 245,8,61,2048
Ra 152,242,14336
Mvav 92,156,204
K 203,182,183
Mvav 60,156,196
W 3
X 185
Cvaa 160,128,0,224
X 186
Mvaa 160,132,4
Dc 245,8,61,3072
Ra 156,242,15360
Cvaa 164,132,20,228
s_add_u32 s61, s61, s63
K 210,184,185
Mvaa 164,128,16
X 187
Cvaa 168,128,32,232
X 188
Mvaa 168,132,36
Cvaa 172,132,52,236
s_xor_b32 s62, s62, 0x10000
K 211,186,187
Mvaa 172,128,48
X 189
Mvaa 160,136,8
Ra 128,242,24576
Ra 132,242,25600
Mvaa 160,140,12
s_add_u32 s64, s64, 1
X 190
Mvaa 164,140,28
K 218,188,189
Mvaa 164,136,24
X 191
Mvaa 168,136,40
Mvaa 168,140,44
X 160
Mvaa 172,140,60
s_add_u32 s36, s29, 327680
K 219,190,191
Mvaa 172,136,56
W 2
X 161
Cvaa 176,144,0,224
Ra 136,242,26624
Ra 140,242,27648
Mvaa 176,148,4
X 162
Cvaa 180,148,20,228
K 196,160,161
Mvaa 180,144,16
buffer_store_dwordx4 a[84:87], v1, s[8:11], s36 offen offset:0 sc1
Cvaa 184,144,32,232
X 163
Mvaa 184,148,36
X 164
Cvaa 188,148,52,236
K 197,162,163
Mvaa 188,144,48
X 165
Mvaa 176,152,8
Ra 144,242,28672
buffer_store_dwordx4 a[92:95], v1, s[8:11], s36 offen offset:1024 sc1
Mvaa 176,156,12
Ra 148,242,29696
Mvaa 180,156,28
X 166
Mvaa 180,152,24
K 204,164,165
Mvaa 184,152,40
X 167
Mvaa 184,156,44
buffer_store_dwordx4 a[188:191], v1, s[8:11], s36 offen offset:2048 sc1
Mvaa 188,156,60
X 168
Mvaa 188,152,56
W 2
Ra 152,242,30720
K 205,166,167
Mvav 32,128,192
X 169
Mvav 64,128,200
Ra 156,242,31744
buffer_store_dwordx4 a[196:199], v1, s[8:11], s36 offen offset:3072 sc1
Mvav 96,128,208
X 170
Mvav 128,128,216
K 212,168,169
Mvav 132,132,216
X 171
Mvav 100,132,208
s_add_u32 s36, s29, 331776
X 172
Mvav 68,132,200
Mvav 36,132,192
K 213,170,171
Mvav 40,136,192
X 173
Mvav 72,136,200
X 174
Mvav 104,136,208
buffer_store_dwordx4 a[236:239], v1, s[8:11], s36 offen offset:0 sc1
Mvav 136,136,216
K 220,172,173
Mvav 140,140,216
X 175
Mvav 108,140,208
X 176
Mvav 76,140,200
s_waitcnt vmcnt(5) lgkmcnt(0)
s_barrier
v_xor_b32 v242, 0x10000, v242
Mvav 44,140,192
buffer_store_dwordx4 a[244:247], v1, s[8:11], s36 offen offset:1024 sc1
K 221,174,175
Mvav 48,144,192
Ra 128,242,0
Mvav 80,144,200
X 177
Mvav 112,144,208
X 178
Mvav 144,144,216
Ra 132,242,1024
K 198,176,177
Mvav 148,148,216
buffer_store_dwordx4 a[68:71], v1, s[8:11], s36 offen offset:2048 sc1
Mvav 116,148,208
X 179
Mvav 84,148,200
X 180
Mvav 52,148,192
Ra 136,242,2048
K 199,178,179
Mvav 56,152,192
buffer_store_dwordx4 a[184:187], v1, s[8:11], s36 offen offset:3072 sc1
Ra 140,242,3072
Mvav 88,152,200
X 181
Mvav 120,152,208
X 182
Mvav 152,152,216
K 206,180,181
Mvav 156,156,216
X 183
Mvav 124,156,208
Mvav 92,156,200
K 207,182,183
Mvav 60,156,192
W 0
Cvaa 160,128,0,224
Ra 144,242,4096
Ra 148,242,5120
Mvaa 160,132,4
s_add_u32 m0, s62, 0
Ra 152,242,6144
Cvaa 164,132,20,228
X 184
Mvaa 164,128,16
X 185
Cvaa 168,128,32,232
X 186
Mvaa 168,132,36
Ra 156,242,7168
K 214,184,185
Cvaa 172,132,52,236
Dc 1,8,61,0
Mvaa 172,128,48
X 187
Mvaa 160,136,8
X 188
Mvaa 160,140,12
Ra 128,242,16384
K 215,186,187
Mvaa 164,140,28
Dc 1,8,61,1024
Mvaa 164,136,24
X 189
Mvaa 168,136,40
X 190
Mvaa 168,140,44
Ra 132,242,17408
K 222,188,189
Mvaa 172,140,60
Dc 1,8,61,2048
Mvaa 172,136,56
X 191
v_smfmac_f32_16x16x64_bf16 a[160:163], a[176:179], v[192:199], v240
Ra 136,242,18432
K 223,190,191
v_smfmac_f32_16x16x64_bf16 a[164:167], a[176:179], v[200:207], v240
X 160
v_smfmac_f32_16x16x64_bf16 a[168:171], a[176:179], v[208:215], v240
Dc 1,8,61,3072
v_smfmac_f32_16x16x64_bf16 a[172:175], a[176:179], v[216:223], v240
W 3
X 161
Cvaa 176,144,0,224
s_add_u32 m0, s62, 4096
X 162
Mvaa 176,148,4
Ra 140,242,19456
K 192,160,161
Cvaa 180,148,20,228
X 163
Mvaa 180,144,16
Dc 243,8,61,0
Cvaa 184,144,32,232
X 164
Mvaa 184,148,36
K 193,162,163
Cvaa 188,148,52,236
X 165
Mvaa 188,144,48
Dc 243,8,61,1024
Ra 144,242,20480
Mvaa 176,152,8
X 166
Mvaa 176,156,12
Ra 148,242,21504
K 200,164,165
Mvaa 180,156,28
X 167
Mvaa 180,152,24
Dc 243,8,61,2048
Mvaa 184,152,40
X 168
Mvaa 184,156,44
K 201,166,167
Mvaa 188,156,60
)"
R"(X 169
Mvaa 188,152,56
W 2
Dc 243,8,61,3072
Ra 152,242,22528
Mvav 32,128,196
s_add_u32 m0, s62, 8192
X 170
Mvav 64,128,204
Ra 156,242,23552
K 208,168,169
Mvav 96,128,212
X 171
Mvav 128,128,220
Dc 244,8,61,0
Mvav 132,132,220
X 172
Mvav 100,132,212
Ra 128,242,8192
K 209,170,171
Mvav 68,132,204
X 173
Mvav 36,132,196
Dc 244,8,61,1024
Mvav 40,136,196
X 174
Mvav 72,136,204
Ra 132,242,9216
K 216,172,173
Mvav 104,136,212
X 175
Mvav 136,136,220
Dc 244,8,61,2048
Mvav 140,140,220
X 176
Mvav 108,140,212
Ra 136,242,10240
K 217,174,175
Mvav 76,140,204
X 177
Mvav 44,140,196
W 3
Dc 244,8,61,3072
Mvav 48,144,196
X 178
Mvav 80,144,204
s_add_u32 m0, s62, 12288
Ra 140,242,11264
Mvav 112,144,212
K 194,176,177
Mvav 144,144,220
X 179
Mvav 148,148,220
X 180
Mvav 116,148,212
Dc 245,8,61,0
Ra 144,242,12288
Mvav 84,148,204
K 195,178,179
Mvav 52,148,196
X 181
Mvav 56,152,196
X 182
Mvav 88,152,204
Dc 245,8,61,1024
Ra 148,242,13312
Mvav 120,152,212
K 202,180,181
Mvav 152,152,220
X 183
Mvav 156,156,220
s_waitcnt vmcnt(14)
s_add_u32 s80, s71, 256
v_mov_b64 v[28:29], s[12:13]
s_mov_b64 exec, 1
buffer_store_dwordx2 v[28:29], off, s[8:11], s80 sc1
s_mov_b64 exec, -1
X 184
Mvav 124,156,212
Ra 152,242,14336
Mvav 92,156,204
K 203,182,183
Mvav 60,156,196
W 3
X 185
Cvaa 160,128,0,224
Dc 245,8,61,2048
Ra 156,242,15360
Mvaa 160,132,4
X 186
Cvaa 164,132,20,228
K 210,184,185
Mvaa 164,128,16
X 187
Cvaa 168,128,32,232
Dc 245,8,61,3072
Mvaa 168,132,36
s_add_u32 s61, s61, s63
X 188
Cvaa 172,132,52,236
s_xor_b32 s62, s62, 0x10000
K 211,186,187
Mvaa 172,128,48
X 189
Mvaa 160,136,8
Ra 128,242,24576
Ra 132,242,25600
Mvaa 160,140,12
s_add_u32 s64, s64, 1
X 190
Mvaa 164,140,28
K 218,188,189
Mvaa 164,136,24
X 191
Mvaa 168,136,40
Mvaa 168,140,44
X 160
Mvaa 172,140,60
s_add_u32 s36, s29, 196608
K 219,190,191
Mvaa 172,136,56
W 2
X 161
Cvaa 176,144,0,224
Ra 136,242,26624
Ra 140,242,27648
Mvaa 176,148,4
X 162
Cvaa 180,148,20,228
K 196,160,161
Mvaa 180,144,16
buffer_store_dwordx4 a[192:195], v1, s[8:11], s36 offen offset:0 sc1
Cvaa 184,144,32,232
X 163
Mvaa 184,148,36
X 164
Cvaa 188,148,52,236
K 197,162,163
Mvaa 188,144,48
X 165
Mvaa 176,152,8
Ra 144,242,28672
buffer_store_dwordx4 a[200:203], v1, s[8:11], s36 offen offset:1024 sc1
Mvaa 176,156,12
Ra 148,242,29696
Mvaa 180,156,28
X 166
Mvaa 180,152,24
K 204,164,165
Mvaa 184,152,40
X 167
Mvaa 184,156,44
buffer_store_dwordx4 a[208:211], v1, s[8:11], s36 offen offset:2048 sc1
Mvaa 188,156,60
X 168
Mvaa 188,152,56
W 2
Ra 152,242,30720
K 205,166,167
Mvav 32,128,192
X 169
Mvav 64,128,200
Ra 156,242,31744
buffer_store_dwordx4 a[216:219], v1, s[8:11], s36 offen offset:3072 sc1
Mvav 96,128,208
X 170
Mvav 128,128,216
K 212,168,169
Mvav 132,132,216
X 171
Mvav 100,132,208
s_add_u32 s36, s29, 200704
X 172
Mvav 68,132,200
Mvav 36,132,192
K 213,170,171
Mvav 40,136,192
X 173
Mvav 72,136,200
X 174
Mvav 104,136,208
buffer_store_dwordx4 a[224:227], v1, s[8:11], s36 offen offset:0 sc1
Mvav 136,136,216
K 220,172,173
Mvav 140,140,216
X 175
Mvav 108,140,208
X 176
Mvav 76,140,200
s_waitcnt vmcnt(5) lgkmcnt(0)
s_barrier
v_xor_b32 v242, 0x10000, v242
Mvav 44,140,192
buffer_store_dwordx4 a[232:235], v1, s[8:11], s36 offen offset:1024 sc1
K 221,174,175
Mvav 48,144,192
Ra 128,242,0
Mvav 80,144,200
X 177
Mvav 112,144,208
X 178
Mvav 144,144,216
Ra 132,242,1024
K 198,176,177
Mvav 148,148,216
buffer_store_dwordx4 a[240:243], v1, s[8:11], s36 offen offset:2048 sc1
Mvav 116,148,208
X 179
Mvav 84,148,200
X 180
Mvav 52,148,192
Ra 136,242,2048
K 199,178,179
Mvav 56,152,192
buffer_store_dwordx4 a[100:103], v1, s[8:11], s36 offen offset:3072 sc1
Ra 140,242,3072
Mvav 88,152,200
X 181
Mvav 120,152,208
X 182
Mvav 152,152,216
K 206,180,181
Mvav 156,156,216
X 183
Mvav 124,156,208
Mvav 92,156,200
K 207,182,183
Mvav 60,156,192
W 0
Cvaa 160,128,0,224
Ra 144,242,4096
Ra 148,242,5120
Mvaa 160,132,4
s_add_u32 m0, s62, 0
Ra 152,242,6144
Cvaa 164,132,20,228
X 184
Mvaa 164,128,16
X 185
Cvaa 168,128,32,232
X 186
Mvaa 168,132,36
Ra 156,242,7168
K 214,184,185
Cvaa 172,132,52,236
Dc 1,8,61,0
Mvaa 172,128,48
X 187
Mvaa 160,136,8
X 188
Mvaa 160,140,12
Ra 128,242,16384
K 215,186,187
Mvaa 164,140,28
Dc 1,8,61,1024
Mvaa 164,136,24
X 189
Mvaa 168,136,40
X 190
Mvaa 168,140,44
Ra 132,242,17408
K 222,188,189
Mvaa 172,140,60
Dc 1,8,61,2048
Mvaa 172,136,56
X 191
v_smfmac_f32_16x16x64_bf16 a[160:163], a[176:179], v[192:199], v240
Ra 136,242,18432
K 223,190,191
v_smfmac_f32_16x16x64_bf16 a[164:167], a[176:179], v[200:207], v240
X 160
v_smfmac_f32_16x16x64_bf16 a[168:171], a[176:179], v[208:215], v240
Dc 1,8,61,3072
v_smfmac_f32_16x16x64_bf16 a[172:175], a[176:179], v[216:223], v240
W 3
X 161
Cvaa 176,144,0,224
s_add_u32 m0, s62, 4096
X 162
Mvaa 176,148,4
Ra 140,242,19456
K 192,160,161
Cvaa 180,148,20,228
X 163
Mvaa 180,144,16
Dc 243,8,61,0
Cvaa 184,144,32,232
X 164
Mvaa 184,148,36
K 193,162,163
Cvaa 188,148,52,236
X 165
Mvaa 188,144,48
Dc 243,8,61,1024
Ra 144,242,20480
Mvaa 176,152,8
X 166
Mvaa 176,156,12
Ra 148,242,21504
K 200,164,165
Mvaa 180,156,28
X 167
Mvaa 180,152,24
Dc 243,8,61,2048
Mvaa 184,152,40
X 168
Mvaa 184,156,44
K 201,166,167
Mvaa 188,156,60
X 169
Mvaa 188,152,56
W 2
Dc 243,8,61,3072
Ra 152,242,22528
Mvav 32,128,196
s_add_u32 m0, s62, 8192
X 170
Mvav 64,128,204
Ra 156,242,23552
K 208,168,169
Mvav 96,128,212
X 171
Mvav 128,128,220
Dc 244,8,61,0
Mvav 132,132,220
X 172
Mvav 100,132,212
Ra 128,242,8192
K 209,170,171
Mvav 68,132,204
X 173
Mvav 36,132,196
Dc 244,8,61,1024
Mvav 40,136,196
X 174
Mvav 72,136,204
Ra 132,242,9216
K 216,172,173
Mvav 104,136,212
X 175
Mvav 136,136,220
Dc 244,8,61,2048
Mvav 140,140,220
X 176
Mvav 108,140,212
Ra 136,242,10240
K 217,174,175
Mvav 76,140,204
X 177
Mvav 44,140,196
W 3
Dc 244,8,61,3072
Mvav 48,144,196
X 178
Mvav 80,144,204
s_add_u32 m0, s62, 12288
Ra 140,242,11264
Mvav 112,144,212
K 194,176,177
Mvav 144,144,220
X 179
Mvav 148,148,220
X 180
Mvav 116,148,212
Dc 245,8,61,0
Ra 144,242,12288
Mvav 84,148,204
K 195,178,179
Mvav 52,148,196
X 181
Mvav 56,152,196
X 182
Mvav 88,152,204
Dc 245,8,61,1024
Ra 148,242,13312
Mvav 120,152,212
K 202,180,181
Mvav 152,152,220
X 183
Mvav 156,156,220
X 184
Mvav 124,156,212
Dc 245,8,61,2048
Ra 152,242,14336
Mvav 92,156,204
K 203,182,183
v_bfe_u32 v28, v1, 4, 4
Mvav 60,156,196
W 3
X 185
Cvaa 160,128,0,224
Ra 156,242,15360
v_lshlrev_b32 v28, 3, v28
Mvaa 160,132,4
s_lshl_b32 s80, s56, 9
s_add_u32 s80, s80, 4352
Dc 245,8,61,3072
Cvaa 164,132,20,228
s_add_u32 s61, s61, s63
X 186
)"
R"(Mvaa 164,128,16
K 210,184,185
Cvaa 168,128,32,232
X 187
Mvaa 168,132,36
buffer_load_dwordx2 v[28:29], v28, s[8:11], s80 offen sc1
Cvaa 172,132,52,236
s_xor_b32 s62, s62, 0x10000
X 188
Mvaa 172,128,48
Ra 128,242,24576
K 211,186,187
Mvaa 160,136,8
X 189
Mvaa 160,140,12
s_add_u32 s64, s64, 1
X 190
Mvaa 164,140,28
Ra 132,242,25600
K 218,188,189
Mvaa 164,136,24
X 191
Mvaa 168,136,40
Mvaa 168,140,44
X 160
Mvaa 172,140,60
s_add_u32 s36, s29, 262144
K 219,190,191
Mvaa 172,136,56
W 2
X 161
Cvaa 176,144,0,224
Ra 136,242,26624
Ra 140,242,27648
Mvaa 176,148,4
X 162
Cvaa 180,148,20,228
K 196,160,161
Mvaa 180,144,16
buffer_store_dwordx4 a[64:67], v1, s[8:11], s36 offen offset:0 sc1
Cvaa 184,144,32,232
X 163
Mvaa 184,148,36
X 164
Cvaa 188,148,52,236
K 197,162,163
Mvaa 188,144,48
X 165
Mvaa 176,152,8
Ra 144,242,28672
buffer_store_dwordx4 a[72:75], v1, s[8:11], s36 offen offset:1024 sc1
Mvaa 176,156,12
Ra 148,242,29696
Mvaa 180,156,28
X 166
Mvaa 180,152,24
K 204,164,165
Mvaa 184,152,40
X 167
Mvaa 184,156,44
buffer_store_dwordx4 a[80:83], v1, s[8:11], s36 offen offset:2048 sc1
Mvaa 188,156,60
X 168
Mvaa 188,152,56
W 2
Ra 152,242,30720
K 205,166,167
Mvav 32,128,192
X 169
Mvav 64,128,200
Ra 156,242,31744
buffer_store_dwordx4 a[88:91], v1, s[8:11], s36 offen offset:3072 sc1
Mvav 96,128,208
X 170
Mvav 128,128,216
K 212,168,169
Mvav 132,132,216
X 171
Mvav 100,132,208
s_add_u32 s36, s29, 266240
X 172
Mvav 68,132,200
Mvav 36,132,192
K 213,170,171
Mvav 40,136,192
X 173
Mvav 72,136,200
X 174
Mvav 104,136,208
buffer_store_dwordx4 a[96:99], v1, s[8:11], s36 offen offset:0 sc1
Mvav 136,136,216
s_waitcnt vmcnt(5)
v_cmp_eq_u64 vcc, s[12:13], v[28:29]
s_cmp_eq_u64 vcc, -1
s_cbranch_scc0 .Lspin_70%=
.Lok_71%=:
K 220,172,173
Mvav 140,140,216
X 175
Mvav 108,140,208
X 176
Mvav 76,140,200
W 0
s_barrier
v_xor_b32 v242, 0x10000, v242
Mvav 44,140,192
buffer_store_dwordx4 a[104:107], v1, s[8:11], s36 offen offset:1024 sc1
K 221,174,175
Mvav 48,144,192
Ra 128,242,0
Mvav 80,144,200
X 177
Mvav 112,144,208
X 178
Mvav 144,144,216
Ra 132,242,1024
K 198,176,177
Mvav 148,148,216
buffer_store_dwordx4 a[112:115], v1, s[8:11], s36 offen offset:2048 sc1
Mvav 116,148,208
X 179
Mvav 84,148,200
X 180
Mvav 52,148,192
Ra 136,242,2048
K 199,178,179
Mvav 56,152,192
buffer_store_dwordx4 a[120:123], v1, s[8:11], s36 offen offset:3072 sc1
Ra 140,242,3072
Mvav 88,152,200
X 181
Mvav 120,152,208
X 182
Mvav 152,152,216
K 206,180,181
Mvav 156,156,216
X 183
Mvav 124,156,208
Mvav 92,156,200
K 207,182,183
Mvav 60,156,192
W 0
Cvaa 160,128,0,224
Ra 144,242,4096
Ra 148,242,5120
Mvaa 160,132,4
s_add_u32 m0, s62, 0
Ra 152,242,6144
Cvaa 164,132,20,228
X 184
Mvaa 164,128,16
X 185
Cvaa 168,128,32,232
X 186
Mvaa 168,132,36
Ra 156,242,7168
K 214,184,185
Cvaa 172,132,52,236
Dc 1,8,61,0
Mvaa 172,128,48
X 187
Mvaa 160,136,8
X 188
Mvaa 160,140,12
Ra 128,242,16384
K 215,186,187
Mvaa 164,140,28
Dc 1,8,61,1024
Mvaa 164,136,24
X 189
Mvaa 168,136,40
X 190
Mvaa 168,140,44
Ra 132,242,17408
K 222,188,189
Mvaa 172,140,60
Dc 1,8,61,2048
Mvaa 172,136,56
X 191
v_smfmac_f32_16x16x64_bf16 a[160:163], a[176:179], v[192:199], v240
Ra 136,242,18432
K 223,190,191
v_smfmac_f32_16x16x64_bf16 a[164:167], a[176:179], v[200:207], v240
X 160
v_smfmac_f32_16x16x64_bf16 a[168:171], a[176:179], v[208:215], v240
Dc 1,8,61,3072
v_smfmac_f32_16x16x64_bf16 a[172:175], a[176:179], v[216:223], v240
W 3
X 161
Cvaa 176,144,0,224
s_add_u32 m0, s62, 4096
X 162
Mvaa 176,148,4
Ra 140,242,19456
K 192,160,161
Cvaa 180,148,20,228
X 163
Mvaa 180,144,16
Dc 243,8,61,0
Cvaa 184,144,32,232
X 164
Mvaa 184,148,36
K 193,162,163
Cvaa 188,148,52,236
X 165
Mvaa 188,144,48
Dc 243,8,61,1024
Ra 144,242,20480
Mvaa 176,152,8
X 166
Mvaa 176,156,12
Ra 148,242,21504
K 200,164,165
Mvaa 180,156,28
X 167
Mvaa 180,152,24
Dc 243,8,61,2048
Mvaa 184,152,40
X 168
Mvaa 184,156,44
K 201,166,167
Mvaa 188,156,60
X 169
Mvaa 188,152,56
W 2
Dc 243,8,61,3072
Ra 152,242,22528
Mvav 32,128,196
s_add_u32 m0, s62, 8192
X 170
Mvav 64,128,204
Ra 156,242,23552
K 208,168,169
Mvav 96,128,212
X 171
Mvav 128,128,220
Dc 244,8,61,0
Mvav 132,132,220
X 172
Mvav 100,132,212
Ra 128,242,8192
K 209,170,171
Mvav 68,132,204
X 173
Mvav 36,132,196
Dc 244,8,61,1024
Mvav 40,136,196
X 174
Mvav 72,136,204
Ra 132,242,9216
K 216,172,173
Mvav 104,136,212
X 175
Mvav 136,136,220
Dc 244,8,61,2048
Mvav 140,140,220
X 176
Mvav 108,140,212
Ra 136,242,10240
K 217,174,175
Mvav 76,140,204
X 177
Mvav 44,140,196
W 3
Dc 244,8,61,3072
Mvav 48,144,196
X 178
Mvav 80,144,204
s_add_u32 m0, s62, 12288
Ra 140,242,11264
Mvav 112,144,212
K 194,176,177
Mvav 144,144,220
X 179
Mvav 148,148,220
X 180
Mvav 116,148,212
Dc 245,8,61,0
Ra 144,242,12288
Mvav 84,148,204
K 195,178,179
Mvav 52,148,196
X 181
Mvav 56,152,196
X 182
Mvav 88,152,204
Dc 245,8,61,1024
Ra 148,242,13312
Mvav 120,152,212
K 202,180,181
Mvav 152,152,220
s_add_u32 s81, s57, 2
X 183
Mvav 156,156,220
s_waitcnt vmcnt(14)
s_add_u32 s80, s71, 384
v_mov_b64 v[28:29], s[12:13]
s_mov_b64 exec, 1
buffer_store_dwordx2 v[28:29], off, s[8:11], s80 sc1
s_mov_b64 exec, -1
X 184
Mvav 124,156,212
s_bitcmp0_b32 s81, 2
s_cselect_b32 s22, -1, 0
s_and_b32 s81, s81, 3
Ra 152,242,14336
Mvav 92,156,204
s_lshl_b32 s82, s56, 2
K 203,182,183
Mvav 60,156,196
W 3
X 185
Cvaa 160,128,0,224
s_add_u32 s81, s81, s82
Dc 245,8,61,2048
Ra 156,242,15360
Mvaa 160,132,4
s_lshl_b32 s81, s81, 1
X 186
Cvaa 164,132,20,228
s_add_u32 s81, s81, s14
s_sub_u32 s81, s81, 2
K 210,184,185
Mvaa 164,128,16
s_mul_i32 s81, s81, 20480
X 187
Cvaa 168,128,32,232
Dc 245,8,61,3072
v_min_u32 v2, 624, v1
Mvaa 168,132,36
s_add_u32 s61, s61, s63
X 188
Cvaa 172,132,52,236
s_xor_b32 s62, s62, 0x10000
v_lshl_add_u32 v2, v2, 3, s81
K 211,186,187
Mvaa 172,128,48
Ra 128,242,24576
v_and_b32 v2, s22, v2
Mvaa 160,136,8
s_and_b32 s22, s22, 5120
X 189
Mvaa 160,140,12
s_add_u32 s64, s64, 1
global_load_dword v3, v2, s[26:27]
Ra 132,242,25600
Mvaa 164,140,28
X 190
Mvaa 164,136,24
v_add_u32 v2, s22, v2
K 218,188,189
Mvaa 168,136,40
X 191
Mvaa 168,140,44
X 160
Mvaa 172,140,60
K 219,190,191
Mvaa 172,136,56
W 2
X 161
Cvaa 176,144,0,224
Ra 136,242,26624
Ra 140,242,27648
Mvaa 176,148,4
X 162
Cvaa 180,148,20,228
K 196,160,161
Mvaa 180,144,16
X 163
Cvaa 184,144,32,232
Mvaa 184,148,36
X 164
Cvaa 188,148,52,236
K 197,162,163
Mvaa 188,144,48
X 165
Mvaa 176,152,8
Ra 144,242,28672
Ra 148,242,29696
Mvaa 176,156,12
X 166
Mvaa 180,156,28
K 204,164,165
Mvaa 180,152,24
X 167
Mvaa 184,152,40
Mvaa 184,156,44
X 168
Mvaa 188,156,60
Mvaa 188,152,56
W 2
Ra 152,242,30720
K 205,166,167
Mvav 32,128,192
X 169
Mvav 64,128,200
Ra 156,242,31744
Mvav 96,128,208
X 170
Mvav 128,128,216
)"
R"(K 212,168,169
Mvav 132,132,216
X 171
Mvav 100,132,208
X 172
Mvav 68,132,200
Mvav 36,132,192
K 213,170,171
Mvav 40,136,192
X 173
Mvav 72,136,200
X 174
Mvav 104,136,208
Mvav 136,136,216
K 220,172,173
Mvav 140,140,216
X 175
Mvav 108,140,208
X 176
Mvav 76,140,200
s_waitcnt vmcnt(1) lgkmcnt(0)
s_barrier
v_xor_b32 v242, 0x10000, v242
Mvav 44,140,192
Ra 128,242,0
K 221,174,175
Mvav 48,144,192
X 177
Mvav 80,144,200
X 178
Mvav 112,144,208
Ra 132,242,1024
Mvav 144,144,216
K 198,176,177
Mvav 148,148,216
X 179
Mvav 116,148,208
X 180
Mvav 84,148,200
Ra 136,242,2048
Mvav 52,148,192
K 199,178,179
Mvav 56,152,192
X 181
Mvav 88,152,200
Ra 140,242,3072
Mvav 120,152,208
X 182
Mvav 152,152,216
K 206,180,181
Mvav 156,156,216
X 183
Mvav 124,156,208
Mvav 92,156,200
K 207,182,183
Mvav 60,156,192
W 0
Cvaa 160,128,0,224
Ra 144,242,4096
Ra 148,242,5120
Mvaa 160,132,4
s_add_u32 m0, s62, 0
Ra 152,242,6144
Cvaa 164,132,20,228
X 184
Mvaa 164,128,16
X 185
Cvaa 168,128,32,232
X 186
Mvaa 168,132,36
Ra 156,242,7168
K 214,184,185
Cvaa 172,132,52,236
Dc 1,8,61,0
Mvaa 172,128,48
X 187
Mvaa 160,136,8
X 188
Mvaa 160,140,12
Ra 128,242,16384
K 215,186,187
Mvaa 164,140,28
Dc 1,8,61,1024
Mvaa 164,136,24
X 189
Mvaa 168,136,40
X 190
Mvaa 168,140,44
Ra 132,242,17408
K 222,188,189
Mvaa 172,140,60
Dc 1,8,61,2048
Mvaa 172,136,56
X 191
v_smfmac_f32_16x16x64_bf16 a[160:163], a[176:179], v[192:199], v240
Ra 136,242,18432
K 223,190,191
v_smfmac_f32_16x16x64_bf16 a[164:167], a[176:179], v[200:207], v240
X 160
v_smfmac_f32_16x16x64_bf16 a[168:171], a[176:179], v[208:215], v240
Dc 1,8,61,3072
v_smfmac_f32_16x16x64_bf16 a[172:175], a[176:179], v[216:223], v240
W 3
X 161
Cvaa 176,144,0,224
s_add_u32 m0, s62, 4096
X 162
Mvaa 176,148,4
Ra 140,242,19456
K 192,160,161
Cvaa 180,148,20,228
X 163
Mvaa 180,144,16
Dc 243,8,61,0
Cvaa 184,144,32,232
X 164
Mvaa 184,148,36
K 193,162,163
Cvaa 188,148,52,236
X 165
Mvaa 188,144,48
Dc 243,8,61,1024
Ra 144,242,20480
Mvaa 176,152,8
X 166
Mvaa 176,156,12
Ra 148,242,21504
K 200,164,165
Mvaa 180,156,28
X 167
Mvaa 180,152,24
Dc 243,8,61,2048
Mvaa 184,152,40
X 168
Mvaa 184,156,44
K 201,166,167
Mvaa 188,156,60
X 169
Mvaa 188,152,56
W 2
Dc 243,8,61,3072
Ra 152,242,22528
Mvav 32,128,196
s_add_u32 m0, s62, 8192
X 170
Mvav 64,128,204
Ra 156,242,23552
K 208,168,169
Mvav 96,128,212
X 171
Mvav 128,128,220
Dc 244,8,61,0
Mvav 132,132,220
X 172
Mvav 100,132,212
Ra 128,242,8192
K 209,170,171
Mvav 68,132,204
X 173
Mvav 36,132,196
Dc 244,8,61,1024
Mvav 40,136,196
X 174
Mvav 72,136,204
Ra 132,242,9216
K 216,172,173
Mvav 104,136,212
X 175
Mvav 136,136,220
Dc 244,8,61,2048
Mvav 140,140,220
X 176
Mvav 108,140,212
Ra 136,242,10240
K 217,174,175
Mvav 76,140,204
X 177
Mvav 44,140,196
W 3
Dc 244,8,61,3072
Mvav 48,144,196
X 178
Mvav 80,144,204
s_add_u32 m0, s62, 12288
Ra 140,242,11264
Mvav 112,144,212
K 194,176,177
Mvav 144,144,220
X 179
Mvav 148,148,220
X 180
Mvav 116,148,212
Dc 245,8,61,0
Ra 144,242,12288
Mvav 84,148,204
K 195,178,179
Mvav 52,148,196
X 181
Mvav 56,152,196
X 182
Mvav 88,152,204
Dc 245,8,61,1024
Ra 148,242,13312
Mvav 120,152,212
K 202,180,181
Mvav 152,152,220
X 183
Mvav 156,156,220
X 184
Mvav 124,156,212
Dc 245,8,61,2048
Ra 152,242,14336
Mvav 92,156,204
K 203,182,183
Mvav 60,156,196
W 3
X 185
Cvaa 160,128,0,224
X 186
Mvaa 160,132,4
Dc 245,8,61,3072
Ra 156,242,15360
Cvaa 164,132,20,228
s_add_u32 s61, s61, s63
K 210,184,185
Mvaa 164,128,16
X 187
Cvaa 168,128,32,232
X 188
Mvaa 168,132,36
Cvaa 172,132,52,236
s_xor_b32 s62, s62, 0x10000
global_load_dword v3, v2, s[26:27]
K 211,186,187
Mvaa 172,128,48
X 189
Mvaa 160,136,8
Ra 128,242,24576
Ra 132,242,25600
Mvaa 160,140,12
s_add_u32 s64, s64, 1
X 190
Mvaa 164,140,28
v_add_u32 v2, s22, v2
K 218,188,189
Mvaa 164,136,24
X 191
Mvaa 168,136,40
Mvaa 168,140,44
X 160
Mvaa 172,140,60
K 219,190,191
Mvaa 172,136,56
W 2
X 161
Cvaa 176,144,0,224
Ra 136,242,26624
Ra 140,242,27648
Mvaa 176,148,4
X 162
Cvaa 180,148,20,228
K 196,160,161
Mvaa 180,144,16
X 163
Cvaa 184,144,32,232
Mvaa 184,148,36
X 164
Cvaa 188,148,52,236
K 197,162,163
Mvaa 188,144,48
X 165
Mvaa 176,152,8
Ra 144,242,28672
Ra 148,242,29696
Mvaa 176,156,12
X 166
Mvaa 180,156,28
K 204,164,165
Mvaa 180,152,24
X 167
Mvaa 184,152,40
Mvaa 184,156,44
X 168
Mvaa 188,156,60
Mvaa 188,152,56
W 2
Ra 152,242,30720
K 205,166,167
Mvav 32,128,192
X 169
Mvav 64,128,200
Ra 156,242,31744
Mvav 96,128,208
X 170
Mvav 128,128,216
K 212,168,169
Mvav 132,132,216
X 171
Mvav 100,132,208
X 172
Mvav 68,132,200
Mvav 36,132,192
K 213,170,171
Mvav 40,136,192
X 173
Mvav 72,136,200
X 174
Mvav 104,136,208
Mvav 136,136,216
K 220,172,173
Mvav 140,140,216
X 175
Mvav 108,140,208
X 176
Mvav 76,140,200
s_waitcnt vmcnt(1) lgkmcnt(0)
s_barrier
v_xor_b32 v242, 0x10000, v242
Mvav 44,140,192
Ra 128,242,0
K 221,174,175
Mvav 48,144,192
X 177
Mvav 80,144,200
X 178
Mvav 112,144,208
Ra 132,242,1024
Mvav 144,144,216
K 198,176,177
Mvav 148,148,216
X 179
Mvav 116,148,208
X 180
Mvav 84,148,200
Ra 136,242,2048
Mvav 52,148,192
K 199,178,179
Mvav 56,152,192
X 181
Mvav 88,152,200
Ra 140,242,3072
Mvav 120,152,208
X 182
Mvav 152,152,216
K 206,180,181
Mvav 156,156,216
X 183
Mvav 124,156,208
Mvav 92,156,200
K 207,182,183
Mvav 60,156,192
W 0
Cvaa 160,128,0,224
Ra 144,242,4096
Ra 148,242,5120
Mvaa 160,132,4
s_add_u32 m0, s62, 0
Ra 152,242,6144
Cvaa 164,132,20,228
X 184
Mvaa 164,128,16
X 185
Cvaa 168,128,32,232
X 186
Mvaa 168,132,36
Ra 156,242,7168
K 214,184,185
Cvaa 172,132,52,236
Dc 1,8,61,0
Mvaa 172,128,48
X 187
Mvaa 160,136,8
X 188
Mvaa 160,140,12
Ra 128,242,16384
K 215,186,187
Mvaa 164,140,28
Dc 1,8,61,1024
Mvaa 164,136,24
X 189
Mvaa 168,136,40
X 190
Mvaa 168,140,44
Ra 132,242,17408
K 222,188,189
Mvaa 172,140,60
Dc 1,8,61,2048
Mvaa 172,136,56
X 191
v_smfmac_f32_16x16x64_bf16 a[160:163], a[176:179], v[192:199], v240
Ra 136,242,18432
K 223,190,191
v_smfmac_f32_16x16x64_bf16 a[164:167], a[176:179], v[200:207], v240
X 160
v_smfmac_f32_16x16x64_bf16 a[168:171], a[176:179], v[208:215], v240
Dc 1,8,61,3072
v_smfmac_f32_16x16x64_bf16 a[172:175], a[176:179], v[216:223], v240
W 3
X 161
Cvaa 176,144,0,224
)"
R"(s_add_u32 m0, s62, 4096
X 162
Mvaa 176,148,4
Ra 140,242,19456
K 192,160,161
Cvaa 180,148,20,228
X 163
Mvaa 180,144,16
Dc 243,8,61,0
Cvaa 184,144,32,232
X 164
Mvaa 184,148,36
K 193,162,163
Cvaa 188,148,52,236
X 165
Mvaa 188,144,48
Dc 243,8,61,1024
Ra 144,242,20480
Mvaa 176,152,8
X 166
Mvaa 176,156,12
Ra 148,242,21504
K 200,164,165
Mvaa 180,156,28
X 167
Mvaa 180,152,24
Dc 243,8,61,2048
Mvaa 184,152,40
X 168
Mvaa 184,156,44
K 201,166,167
Mvaa 188,156,60
X 169
Mvaa 188,152,56
W 2
Dc 243,8,61,3072
Ra 152,242,22528
Mvav 32,128,196
s_add_u32 m0, s62, 8192
X 170
Mvav 64,128,204
Ra 156,242,23552
K 208,168,169
Mvav 96,128,212
X 171
Mvav 128,128,220
Dc 244,8,61,0
Mvav 132,132,220
X 172
Mvav 100,132,212
Ra 128,242,8192
K 209,170,171
Mvav 68,132,204
X 173
Mvav 36,132,196
Dc 244,8,61,1024
Mvav 40,136,196
X 174
Mvav 72,136,204
Ra 132,242,9216
K 216,172,173
Mvav 104,136,212
X 175
Mvav 136,136,220
Dc 244,8,61,2048
Mvav 140,140,220
X 176
Mvav 108,140,212
Ra 136,242,10240
K 217,174,175
Mvav 76,140,204
X 177
Mvav 44,140,196
W 3
Dc 244,8,61,3072
Mvav 48,144,196
X 178
Mvav 80,144,204
s_add_u32 m0, s62, 12288
Ra 140,242,11264
Mvav 112,144,212
K 194,176,177
Mvav 144,144,220
X 179
Mvav 148,148,220
X 180
Mvav 116,148,212
Dc 245,8,61,0
Ra 144,242,12288
Mvav 84,148,204
K 195,178,179
Mvav 52,148,196
X 181
Mvav 56,152,196
X 182
Mvav 88,152,204
Dc 245,8,61,1024
Ra 148,242,13312
Mvav 120,152,212
K 202,180,181
Mvav 152,152,220
X 183
Mvav 156,156,220
X 184
Mvav 124,156,212
Dc 245,8,61,2048
Ra 152,242,14336
Mvav 92,156,204
K 203,182,183
Mvav 60,156,196
W 3
X 185
Cvaa 160,128,0,224
X 186
Mvaa 160,132,4
Dc 245,8,61,3072
Ra 156,242,15360
Cvaa 164,132,20,228
s_add_u32 s61, s61, s63
K 210,184,185
Mvaa 164,128,16
X 187
Cvaa 168,128,32,232
X 188
Mvaa 168,132,36
Cvaa 172,132,52,236
s_xor_b32 s62, s62, 0x10000
global_load_dword v3, v2, s[26:27]
K 211,186,187
Mvaa 172,128,48
Ra 128,242,24576
v_add_u32 v2, s22, v2
Mvaa 160,136,8
s_lshr_b32 s80, s15, 3
s_lshl_b32 s80, s80, 1
s_add_u32 s80, s80, s14
Mvaa 160,140,12
s_sub_u32 s80, s80, 2
s_lshl_b32 s80, s80, 12
v_min_u32 v27, 496, v1
Mvaa 164,140,28
s_add_u32 s64, s64, 1
X 189
Mvaa 164,136,24
Ra 132,242,25600
v_lshl_add_u32 v27, v27, 3, s80
Mvaa 168,136,40
X 190
Mvaa 168,140,44
v_min_u32 v27, .Lend%=-.Lbeg%=-4, v27
K 218,188,189
Mvaa 172,140,60
global_load_dword v3, v27, s[6:7]
Mvaa 172,136,56
W 2
X 191
Cvaa 176,144,0,224
X 160
Mvaa 176,148,4
Ra 136,242,26624
K 219,190,191
Cvaa 180,148,20,228
X 161
Mvaa 180,144,16
Ra 140,242,27648
Cvaa 184,144,32,232
X 162
Mvaa 184,148,36
K 196,160,161
Cvaa 188,148,52,236
X 163
Mvaa 188,144,48
X 164
Mvaa 176,152,8
Ra 144,242,28672
K 197,162,163
Mvaa 176,156,12
X 165
Mvaa 180,156,28
Ra 148,242,29696
Mvaa 180,152,24
X 166
Mvaa 184,152,40
K 204,164,165
Mvaa 184,156,44
X 167
Mvaa 188,156,60
X 168
Mvaa 188,152,56
W 2
Ra 152,242,30720
K 205,166,167
Mvav 32,128,192
X 169
Mvav 64,128,200
Ra 156,242,31744
Mvav 96,128,208
X 170
Mvav 128,128,216
K 212,168,169
Mvav 132,132,216
X 171
Mvav 100,132,208
X 172
Mvav 68,132,200
Mvav 36,132,192
K 213,170,171
Mvav 40,136,192
X 173
Mvav 72,136,200
X 174
Mvav 104,136,208
Mvav 136,136,216
K 220,172,173
Mvav 140,140,216
X 175
Mvav 108,140,208
X 176
Mvav 76,140,200
s_waitcnt vmcnt(2) lgkmcnt(0)
s_barrier
v_xor_b32 v242, 0x10000, v242
Mvav 44,140,192
Ra 128,242,0
K 221,174,175
Mvav 48,144,192
X 177
Mvav 80,144,200
X 178
Mvav 112,144,208
Ra 132,242,1024
Mvav 144,144,216
K 198,176,177
Mvav 148,148,216
X 179
Mvav 116,148,208
X 180
Mvav 84,148,200
Ra 136,242,2048
Mvav 52,148,192
K 199,178,179
Mvav 56,152,192
X 181
Mvav 88,152,200
Ra 140,242,3072
Mvav 120,152,208
X 182
Mvav 152,152,216
K 206,180,181
Mvav 156,156,216
X 183
Mvav 124,156,208
Mvav 92,156,200
K 207,182,183
Mvav 60,156,192
W 0
Cvaa 160,128,0,224
Ra 144,242,4096
Ra 148,242,5120
Mvaa 160,132,4
s_add_u32 m0, s62, 0
Ra 152,242,6144
Cvaa 164,132,20,228
X 184
Mvaa 164,128,16
X 185
Cvaa 168,128,32,232
X 186
Mvaa 168,132,36
Ra 156,242,7168
K 214,184,185
Cvaa 172,132,52,236
Dc 1,8,61,0
Mvaa 172,128,48
X 187
Mvaa 160,136,8
X 188
Mvaa 160,140,12
Ra 128,242,16384
K 215,186,187
Mvaa 164,140,28
Dc 1,8,61,1024
Mvaa 164,136,24
X 189
Mvaa 168,136,40
X 190
Mvaa 168,140,44
Ra 132,242,17408
K 222,188,189
Mvaa 172,140,60
Dc 1,8,61,2048
Mvaa 172,136,56
X 191
v_smfmac_f32_16x16x64_bf16 a[160:163], a[176:179], v[192:199], v240
Ra 136,242,18432
K 223,190,191
v_smfmac_f32_16x16x64_bf16 a[164:167], a[176:179], v[200:207], v240
X 160
v_smfmac_f32_16x16x64_bf16 a[168:171], a[176:179], v[208:215], v240
Dc 1,8,61,3072
v_smfmac_f32_16x16x64_bf16 a[172:175], a[176:179], v[216:223], v240
W 3
X 161
Cvaa 176,144,0,224
s_add_u32 m0, s62, 4096
X 162
Mvaa 176,148,4
s_sub_u32 s81, s57, 1
Ra 140,242,19456
K 192,160,161
Cvaa 180,148,20,228
X 163
Mvaa 180,144,16
s_lshl_b32 s85, s81, 5
Dc 243,8,61,0
Cvaa 184,144,32,232
X 164
Mvaa 184,148,36
s_add_u32 s85, s85, s56
K 193,162,163
Cvaa 188,148,52,236
X 165
Mvaa 188,144,48
Dc 243,8,61,1024
Ra 144,242,20480
Mvaa 176,152,8
s_lshl_b32 s85, s85, 1
X 166
Mvaa 176,156,12
Ra 148,242,21504
K 200,164,165
Mvaa 180,156,28
s_add_u32 s85, s85, s14
X 167
Mvaa 180,152,24
Dc 243,8,61,2048
Mvaa 184,152,40
s_sub_u32 s85, s85, 2
X 168
Mvaa 184,156,44
K 201,166,167
Mvaa 188,156,60
X 169
Mvaa 188,152,56
W 2
s_cmp_lt_u32 s85, 160
s_cselect_b64 s[20:21], 0, -1
Dc 243,8,61,3072
Mvav 32,128,196
s_add_u32 m0, s62, 8192
Ra 152,242,22528
Ra 156,242,23552
Mvav 64,128,204
s_mul_i32 s18, s85, 205
X 170
Mvav 96,128,212
K 208,168,169
Mvav 128,128,220
s_lshr_b32 s18, s18, 12
X 171
Mvav 132,132,220
Dc 244,8,61,0
Ra 128,242,8192
Mvav 100,132,212
X 172
Mvav 68,132,204
s_and_b32 s18, s18, 7
K 209,170,171
Mvav 36,132,196
X 173
Mvav 40,136,196
Dc 244,8,61,1024
v_lshrrev_b32 v4, 1, v1
Mvav 72,136,204
X 174
Mvav 104,136,212
Ra 132,242,9216
K 216,172,173
Mvav 136,136,220
X 175
Mvav 140,140,220
Dc 244,8,61,2048
v_min_u32 v5, 344, v4
Mvav 108,140,212
s_lshl_b32 s81, s18, 3
X 176
Mvav 76,140,204
Ra 136,242,10240
K 217,174,175
Mvav 44,140,196
W 3
X 177
Mvav 48,144,196
Dc 244,8,61,3072
v_mov_b32 v6, s81
Mvav 80,144,204
s_add_u32 m0, s62, 12288
X 178
Mvav 112,144,212
Ra 140,242,11264
)"
R"(K 194,176,177
Mvav 144,144,220
X 179
Mvav 148,148,220
buffer_load_dwordx2 v[8:9], v4, s[8:11], 0 offen offset:2560 sc1
Ra 144,242,12288
Mvav 116,148,212
X 180
Mvav 84,148,204
K 195,178,179
Mvav 52,148,196
X 181
Mvav 56,152,196
buffer_load_dwordx2 v[10:11], v5, s[8:11], 0 offen offset:3072 sc1
Ra 148,242,13312
Mvav 88,152,204
X 182
Mvav 120,152,212
K 202,180,181
Mvav 152,152,220
X 183
Mvav 156,156,220
Dc 245,8,61,0
Ra 152,242,14336
Mvav 124,156,212
X 184
Mvav 92,156,204
K 203,182,183
v_bfe_u32 v28, v1, 4, 4
Mvav 60,156,196
W 3
X 185
Cvaa 160,128,0,224
buffer_load_dwordx2 v[12:13], v6, s[8:11], 0 offen offset:3584 sc1
v_lshlrev_b32 v28, 3, v28
Mvaa 160,132,4
s_lshl_b32 s80, s56, 9
X 186
Cvaa 164,132,20,228
s_add_u32 s80, s80, 4480
Ra 156,242,15360
K 210,184,185
Mvaa 164,128,16
X 187
Cvaa 168,128,32,232
Dc 245,8,61,1024
Mvaa 168,132,36
X 188
Cvaa 172,132,52,236
K 211,186,187
Mvaa 172,128,48
X 189
Mvaa 160,136,8
Dc 245,8,61,2048
Ra 128,242,24576
Mvaa 160,140,12
X 190
Mvaa 164,140,28
Ra 132,242,25600
K 218,188,189
Mvaa 164,136,24
X 191
Mvaa 168,136,40
buffer_load_dwordx2 v[28:29], v28, s[8:11], s80 offen sc1
Mvaa 168,140,44
Dc 245,8,61,3072
s_add_u32 s61, s61, s63
s_xor_b32 s62, s62, 0x10000
s_add_u32 s64, s64, 1
Mvaa 172,140,60
X 160
Mvaa 172,136,56
W 2
Ra 136,242,26624
K 219,190,191
Cvaa 176,144,0,224
X 161
Mvaa 176,148,4
global_load_dword v3, v2, s[26:27]
v_add_u32 v2, s22, v2
Cvaa 180,148,20,228
Ra 140,242,27648
Mvaa 180,144,16
X 162
Cvaa 184,144,32,232
K 196,160,161
Mvaa 184,148,36
X 163
Cvaa 188,148,52,236
X 164
Mvaa 188,144,48
Ra 144,242,28672
K 197,162,163
Mvaa 176,152,8
X 165
Mvaa 176,156,12
Ra 148,242,29696
Mvaa 180,156,28
X 166
Mvaa 180,152,24
K 204,164,165
Mvaa 184,152,40
X 167
Mvaa 184,156,44
X 168
Mvaa 188,156,60
Mvaa 188,152,56
W 2
Ra 152,242,30720
K 205,166,167
Mvav 32,128,192
X 169
Mvav 64,128,200
Ra 156,242,31744
Mvav 96,128,208
X 170
Mvav 128,128,216
K 212,168,169
Mvav 132,132,216
X 171
Mvav 100,132,208
X 172
Mvav 68,132,200
Mvav 36,132,192
K 213,170,171
Mvav 40,136,192
X 173
Mvav 72,136,200
X 174
Mvav 104,136,208
Mvav 136,136,216
s_waitcnt vmcnt(2)
v_cmp_eq_u64 vcc, s[12:13], v[28:29]
s_cmp_eq_u64 vcc, -1
s_cbranch_scc0 .Lspin_73%=
.Lok_74%=:
v_cmp_eq_u64 vcc, s[12:13], v[8:9]
v_cmp_eq_u64_e64 s[86:87], s[12:13], v[10:11]
s_and_b64 vcc, vcc, s[86:87]
s_or_b64 vcc, vcc, s[20:21]
s_cmp_eq_u64 vcc, -1
s_cbranch_scc0 .Lspin_76%=
.Lok_77%=:
K 220,172,173
Mvav 140,140,216
X 175
Mvav 108,140,208
X 176
Mvav 76,140,200
s_waitcnt vmcnt(1) lgkmcnt(0)
s_barrier
v_xor_b32 v242, 0x10000, v242
Mvav 44,140,192
Ra 128,242,0
K 221,174,175
Mvav 48,144,192
X 177
Mvav 80,144,200
X 178
Mvav 112,144,208
Ra 132,242,1024
Mvav 144,144,216
K 198,176,177
Mvav 148,148,216
X 179
Mvav 116,148,208
X 180
Mvav 84,148,200
Ra 136,242,2048
Mvav 52,148,192
K 199,178,179
Mvav 56,152,192
X 181
Mvav 88,152,200
Ra 140,242,3072
Mvav 120,152,208
X 182
Mvav 152,152,216
K 206,180,181
Mvav 156,156,216
X 183
Mvav 124,156,208
Mvav 92,156,200
K 207,182,183
Mvav 60,156,192
W 0
Cvaa 160,128,0,224
Ra 144,242,4096
Ra 148,242,5120
Mvaa 160,132,4
s_add_u32 m0, s62, 0
Ra 152,242,6144
Cvaa 164,132,20,228
X 184
Mvaa 164,128,16
X 185
Cvaa 168,128,32,232
X 186
Mvaa 168,132,36
Ra 156,242,7168
K 214,184,185
Cvaa 172,132,52,236
Dc 1,8,61,0
Mvaa 172,128,48
X 187
Mvaa 160,136,8
X 188
Mvaa 160,140,12
Ra 128,242,16384
K 215,186,187
Mvaa 164,140,28
Dc 1,8,61,1024
Mvaa 164,136,24
X 189
Mvaa 168,136,40
X 190
Mvaa 168,140,44
Ra 132,242,17408
K 222,188,189
Mvaa 172,140,60
Dc 1,8,61,2048
Mvaa 172,136,56
X 191
v_smfmac_f32_16x16x64_bf16 a[160:163], a[176:179], v[192:199], v240
Ra 136,242,18432
K 223,190,191
v_smfmac_f32_16x16x64_bf16 a[164:167], a[176:179], v[200:207], v240
X 160
v_smfmac_f32_16x16x64_bf16 a[168:171], a[176:179], v[208:215], v240
Dc 1,8,61,3072
v_smfmac_f32_16x16x64_bf16 a[172:175], a[176:179], v[216:223], v240
W 3
X 161
Cvaa 176,144,0,224
s_add_u32 m0, s62, 4096
X 162
Mvaa 176,148,4
Ra 140,242,19456
K 192,160,161
Cvaa 180,148,20,228
X 163
Mvaa 180,144,16
Dc 243,8,61,0
Cvaa 184,144,32,232
X 164
Mvaa 184,148,36
K 193,162,163
Cvaa 188,148,52,236
X 165
Mvaa 188,144,48
Dc 243,8,61,1024
Ra 144,242,20480
Mvaa 176,152,8
X 166
Mvaa 176,156,12
Ra 148,242,21504
K 200,164,165
Mvaa 180,156,28
X 167
Mvaa 180,152,24
Dc 243,8,61,2048
Mvaa 184,152,40
X 168
Mvaa 184,156,44
K 201,166,167
Mvaa 188,156,60
X 169
Mvaa 188,152,56
W 2
Dc 243,8,61,3072
Ra 152,242,22528
Mvav 32,128,196
s_add_u32 m0, s62, 8192
X 170
Mvav 64,128,204
Ra 156,242,23552
K 208,168,169
Mvav 96,128,212
X 171
Mvav 128,128,220
Dc 244,8,61,0
Mvav 132,132,220
X 172
Mvav 100,132,212
Ra 128,242,8192
K 209,170,171
Mvav 68,132,204
X 173
Mvav 36,132,196
Dc 244,8,61,1024
Mvav 40,136,196
X 174
Mvav 72,136,204
Ra 132,242,9216
K 216,172,173
Mvav 104,136,212
X 175
Mvav 136,136,220
Dc 244,8,61,2048
Mvav 140,140,220
X 176
Mvav 108,140,212
Ra 136,242,10240
K 217,174,175
Mvav 76,140,204
X 177
Mvav 44,140,196
W 3
Dc 244,8,61,3072
Mvav 48,144,196
X 178
Mvav 80,144,204
s_add_u32 m0, s62, 12288
Ra 140,242,11264
Mvav 112,144,212
K 194,176,177
Mvav 144,144,220
X 179
Mvav 148,148,220
X 180
Mvav 116,148,212
Dc 245,8,61,0
Ra 144,242,12288
Mvav 84,148,204
K 195,178,179
Mvav 52,148,196
X 181
Mvav 56,152,196
X 182
Mvav 88,152,204
Dc 245,8,61,1024
Ra 148,242,13312
Mvav 120,152,212
v_xor_b32 v6, s12, v12
K 202,180,181
Mvav 152,152,220
X 183
Mvav 156,156,220
Ra 152,242,14336
v_and_b32 v6, 2047, v6
Mvav 124,156,212
Dc 245,8,61,2048
v_lshlrev_b32 v6, 3, v6
Mvav 92,156,204
X 184
Mvav 60,156,196
W 3
K 203,182,183
Cvaa 160,128,0,224
s_mov_b32 s81, 40960
X 185
Mvaa 160,132,4
Dc 245,8,61,3072
Ra 156,242,15360
Cvaa 164,132,20,228
s_add_u32 s61, s61, s63
X 186
Mvaa 164,128,16
K 210,184,185
Cvaa 168,128,32,232
X 187
Mvaa 168,132,36
buffer_load_dword v7, v6, s[8:11], s81 offen sc1
v_lshrrev_b32 v5, 7, v1
Cvaa 172,132,52,236
s_xor_b32 s62, s62, 0x10000
X 188
Mvaa 172,128,48
K 211,186,187
v_mul_u32_u24 v4, 20480, v5
Mvaa 160,136,8
X 189
Mvaa 160,140,12
s_add_u32 s64, s64, 1
Ra 128,242,24576
Ra 132,242,25600
Mvaa 164,140,28
X 190
Mvaa 164,136,24
v_and_b32 v6, 0x70, v1
K 218,188,189
Mvaa 168,136,40
X 191
Mvaa 168,140,44
v_add_u32 v4, v4, v6
Mvaa 172,140,60
X 160
Mvaa 172,136,56
W 2
v_add_u32 v5, 2, v5
K 219,190,191
Cvaa 176,144,0,224
X 161
Mvaa 176,148,4
Ra 136,242,26624
v_lshrrev_b32 v5, 3, v5
Cvaa 180,148,20,228
Ra 140,242,27648
v_lshl_or_b32 v5, v5, 31, v4
Mvaa 180,144,16
)"
R"(X 162
Cvaa 184,144,32,232
K 196,160,161
Mvaa 184,148,36
s_lshl_b32 s19, s85, 7
X 163
Cvaa 188,148,52,236
X 164
Mvaa 188,144,48
s_add_u32 s19, s19, 2752512
Ra 144,242,28672
K 197,162,163
Mvaa 176,152,8
X 165
Mvaa 176,156,12
s_and_b32 s81, s20, 134217728
Ra 148,242,29696
Mvaa 180,156,28
X 166
Mvaa 180,152,24
K 204,164,165
Mvaa 184,152,40
s_or_b32 s19, s19, s81
X 167
Mvaa 184,156,44
X 168
Mvaa 188,156,60
buffer_load_dwordx4 a[64:67], v4, s[8:11], s19 offen sc1
Mvaa 188,152,56
W 2
Ra 152,242,30720
K 205,166,167
Mvav 32,128,192
s_add_u32 s19, s19, 163840
X 169
Mvav 64,128,200
Ra 156,242,31744
Mvav 96,128,208
X 170
Mvav 128,128,216
buffer_load_dwordx4 a[68:71], v4, s[8:11], s19 offen sc1
K 212,168,169
Mvav 132,132,216
X 171
Mvav 100,132,208
s_add_u32 s19, s19, 163840
X 172
Mvav 68,132,200
Mvav 36,132,192
buffer_load_dwordx4 a[72:75], v4, s[8:11], s19 offen sc1
K 213,170,171
Mvav 40,136,192
X 173
Mvav 72,136,200
X 174
Mvav 104,136,208
s_add_u32 s19, s19, 163840
Mvav 136,136,216
K 220,172,173
Mvav 140,140,216
X 175
Mvav 108,140,208
buffer_load_dwordx4 a[76:79], v4, s[8:11], s19 offen sc1
Mvav 76,140,200
s_waitcnt vmcnt(5) lgkmcnt(0)
s_barrier
v_xor_b32 v242, 0x10000, v242
Mvav 44,140,192
s_add_u32 s19, s19, 163840
X 176
Mvav 48,144,192
Ra 128,242,0
K 221,174,175
Mvav 80,144,200
X 177
Mvav 112,144,208
buffer_load_dwordx4 a[80:83], v4, s[8:11], s19 offen sc1
Ra 132,242,1024
Mvav 144,144,216
s_add_u32 s19, s19, 163840
X 178
Mvav 148,148,216
K 198,176,177
Mvav 116,148,208
X 179
Mvav 84,148,200
buffer_load_dwordx4 a[84:87], v4, s[8:11], s19 offen sc1
Ra 136,242,2048
Mvav 52,148,192
X 180
Mvav 56,152,192
s_add_u32 s19, s19, 163840
Ra 140,242,3072
K 199,178,179
Mvav 88,152,200
X 181
Mvav 120,152,208
X 182
Mvav 152,152,216
buffer_load_dwordx4 a[88:91], v5, s[8:11], s19 offen sc1
K 206,180,181
Mvav 156,156,216
X 183
Mvav 124,156,208
Mvav 92,156,200
K 207,182,183
Mvav 60,156,192
W 0
Cvaa 160,128,0,224
Ra 144,242,4096
Ra 148,242,5120
Mvaa 160,132,4
s_add_u32 m0, s62, 0
Ra 152,242,6144
Cvaa 164,132,20,228
X 184
Mvaa 164,128,16
X 185
Cvaa 168,128,32,232
X 186
Mvaa 168,132,36
Ra 156,242,7168
K 214,184,185
Cvaa 172,132,52,236
Dc 1,8,61,0
Mvaa 172,128,48
X 187
Mvaa 160,136,8
X 188
Mvaa 160,140,12
Ra 128,242,16384
K 215,186,187
Mvaa 164,140,28
Dc 1,8,61,1024
Mvaa 164,136,24
X 189
Mvaa 168,136,40
X 190
Mvaa 168,140,44
Ra 132,242,17408
K 222,188,189
Mvaa 172,140,60
Dc 1,8,61,2048
Mvaa 172,136,56
X 191
v_smfmac_f32_16x16x64_bf16 a[160:163], a[176:179], v[192:199], v240
Ra 136,242,18432
K 223,190,191
v_smfmac_f32_16x16x64_bf16 a[164:167], a[176:179], v[200:207], v240
X 160
v_smfmac_f32_16x16x64_bf16 a[168:171], a[176:179], v[208:215], v240
Dc 1,8,61,3072
v_smfmac_f32_16x16x64_bf16 a[172:175], a[176:179], v[216:223], v240
W 3
X 161
Cvaa 176,144,0,224
s_add_u32 m0, s62, 4096
X 162
Mvaa 176,148,4
Ra 140,242,19456
K 192,160,161
Cvaa 180,148,20,228
X 163
Mvaa 180,144,16
Dc 243,8,61,0
Cvaa 184,144,32,232
X 164
Mvaa 184,148,36
K 193,162,163
Cvaa 188,148,52,236
X 165
Mvaa 188,144,48
Dc 243,8,61,1024
Ra 144,242,20480
Mvaa 176,152,8
X 166
Mvaa 176,156,12
Ra 148,242,21504
K 200,164,165
Mvaa 180,156,28
X 167
Mvaa 180,152,24
Dc 243,8,61,2048
Mvaa 184,152,40
X 168
Mvaa 184,156,44
K 201,166,167
Mvaa 188,156,60
X 169
Mvaa 188,152,56
W 2
Dc 243,8,61,3072
Ra 152,242,22528
Mvav 32,128,196
s_add_u32 m0, s62, 8192
X 170
Mvav 64,128,204
Ra 156,242,23552
K 208,168,169
Mvav 96,128,212
X 171
Mvav 128,128,220
Dc 244,8,61,0
Mvav 132,132,220
X 172
Mvav 100,132,212
Ra 128,242,8192
K 209,170,171
Mvav 68,132,204
X 173
Mvav 36,132,196
Dc 244,8,61,1024
Mvav 40,136,196
X 174
Mvav 72,136,204
Ra 132,242,9216
K 216,172,173
Mvav 104,136,212
X 175
Mvav 136,136,220
Dc 244,8,61,2048
Mvav 140,140,220
s_bfe_u32 s81, s59, 0x10001
X 176
Mvav 108,140,212
s_lshl_b32 s82, s57, 1
Ra 136,242,10240
K 217,174,175
Mvav 76,140,204
s_add_u32 s81, s81, s82
X 177
Mvav 44,140,196
W 3
s_lshl_b32 s83, s81, 2
Dc 244,8,61,3072
Mvav 48,144,196
s_lshr_b32 s81, s59, 2
X 178
Mvav 80,144,204
s_add_u32 m0, s62, 12288
s_lshl_b32 s81, s81, 1
Ra 140,242,11264
Mvav 112,144,212
s_add_u32 s83, s83, s81
K 194,176,177
Mvav 144,144,220
s_and_b32 s81, s59, 1
X 179
Mvav 148,148,220
s_add_u32 s83, s83, s81
X 180
Mvav 116,148,212
s_and_b32 s81, s14, 1
Dc 245,8,61,0
Ra 144,242,12288
Mvav 84,148,204
s_mul_i32 s81, s81, 124
K 195,178,179
Mvav 52,148,196
s_xor_b32 s83, s83, s81
X 181
Mvav 56,152,196
s_mul_i32 s83, s83, 20480
X 182
Mvav 88,152,204
s_mul_i32 s81, s58, 320
Dc 245,8,61,1024
Ra 148,242,13312
Mvav 120,152,212
s_add_u32 s82, s83, s81
K 202,180,181
Mvav 152,152,220
X 183
Mvav 156,156,220
Ra 152,242,14336
v_bfe_u32 v246, v1, 4, 4
Mvav 124,156,212
Dc 245,8,61,2048
v_mul_u32_u24 v246, 1280, v246
Mvav 92,156,204
X 184
Mvav 60,156,196
W 3
v_lshrrev_b32 v247, 8, v1
K 203,182,183
Cvaa 160,128,0,224
v_mul_u32_u24 v247, 104, v247
v_add3_u32 v246, v246, v247, s82
Mvaa 160,132,4
X 185
Cvaa 164,132,20,228
Dc 245,8,61,3072
Ra 156,242,15360
Mvaa 164,128,16
s_add_u32 s61, s61, s63
X 186
Cvaa 168,128,32,232
K 210,184,185
Mvaa 168,132,36
X 187
Cvaa 172,132,52,236
s_xor_b32 s62, s62, 0x10000
X 188
Mvaa 172,128,48
global_load_dword v3, v246, s[16:17]
K 211,186,187
Mvaa 160,136,8
s_lshl_b32 s81, s81, 3
s_add_u32 s83, s83, s81
s_add_u32 s83, s83, 131072
Mvaa 160,140,12
s_add_u32 s64, s64, 1
v_lshrrev_b32 v246, 9, v1
v_mul_u32_u24 v246, 10240, v246
Mvaa 164,140,28
Ra 128,242,24576
v_and_b32 v247, 0x1f0, v1
Mvaa 164,136,24
X 189
Mvaa 168,136,40
Ra 132,242,25600
v_min_u32 v247, 304, v247
Mvaa 168,140,44
X 190
Mvaa 172,140,60
v_lshl_add_u32 v246, v247, 3, v246
K 218,188,189
Mvaa 172,136,56
W 2
buffer_load_dword v3, v246, s[8:11], s83 offen sc1
Ra 136,242,26624
Cvaa 176,144,0,224
X 191
Mvaa 176,148,4
X 160
Cvaa 180,148,20,228
Ra 140,242,27648
K 219,190,191
Mvaa 180,144,16
X 161
Cvaa 184,144,32,232
X 162
Mvaa 184,148,36
K 196,160,161
Cvaa 188,148,52,236
X 163
Mvaa 188,144,48
X 164
Mvaa 176,152,8
Ra 144,242,28672
K 197,162,163
Mvaa 176,156,12
X 165
Mvaa 180,156,28
Ra 148,242,29696
Mvaa 180,152,24
X 166
Mvaa 184,152,40
K 204,164,165
Mvaa 184,156,44
X 167
Mvaa 188,156,60
X 168
Mvaa 188,152,56
W 2
Ra 152,242,30720
K 205,166,167
Mvav 32,128,192
X 169
Mvav 64,128,200
Ra 156,242,31744
Mvav 96,128,208
X 170
Mvav 128,128,216
K 212,168,169
Mvav 132,132,216
X 171
Mvav 100,132,208
X 172
Mvav 68,132,200
Mvav 36,132,192
K 213,170,171
Mvav 40,136,192
X 173
Mvav 72,136,200
X 174
Mvav 104,136,208
Mvav 136,136,216
K 220,172,173
Mvav 140,140,216
X 175
Mvav 108,140,208
X 176
Mvav 76,140,200
s_waitcnt vmcnt(2) lgkmcnt(0)
s_barrier
v_xor_b32 v242, 0x10000, v242
Mvav 44,140,192
Ra 128,242,0
K 221,174,175
)"
R"(Mvav 48,144,192
X 177
Mvav 80,144,200
X 178
Mvav 112,144,208
Ra 132,242,1024
Mvav 144,144,216
K 198,176,177
Mvav 148,148,216
X 179
Mvav 116,148,208
X 180
Mvav 84,148,200
Ra 136,242,2048
Mvav 52,148,192
K 199,178,179
Mvav 56,152,192
X 181
Mvav 88,152,200
Ra 140,242,3072
Mvav 120,152,208
X 182
Mvav 152,152,216
K 206,180,181
Mvav 156,156,216
X 183
Mvav 124,156,208
Mvav 92,156,200
K 207,182,183
Mvav 60,156,192
W 0
Cvaa 160,128,0,224
Ra 144,242,4096
Ra 148,242,5120
Mvaa 160,132,4
s_add_u32 m0, s62, 0
Ra 152,242,6144
G 8,64
Cvaa 164,132,20,228
X 184
Mvaa 164,128,16
X 185
Cvaa 168,128,32,232
X 186
Mvaa 168,132,36
G 9,65
K 214,184,185
Cvaa 172,132,52,236
Ra 156,242,7168
Dc 1,8,61,0
Mvaa 172,128,48
X 187
Mvaa 160,136,8
X 188
Mvaa 160,140,12
Ra 128,242,16384
G 10,66
Mvaa 164,140,28
Dc 1,8,61,1024
K 215,186,187
Mvaa 164,136,24
X 189
Mvaa 168,136,40
Ra 132,242,17408
G 11,67
Mvaa 168,140,44
X 190
Mvaa 172,140,60
Dc 1,8,61,2048
K 222,188,189
Mvaa 172,136,56
X 191
v_smfmac_f32_16x16x64_bf16 a[160:163], a[176:179], v[192:199], v240
G 12,68
K 223,190,191
v_smfmac_f32_16x16x64_bf16 a[164:167], a[176:179], v[200:207], v240
Ra 136,242,18432
v_smfmac_f32_16x16x64_bf16 a[168:171], a[176:179], v[208:215], v240
Dc 1,8,61,3072
G 13,69
v_smfmac_f32_16x16x64_bf16 a[172:175], a[176:179], v[216:223], v240
W 3
X 160
Cvaa 176,144,0,224
s_add_u32 m0, s62, 4096
X 161
Mvaa 176,148,4
X 162
Cvaa 180,148,20,228
Ra 140,242,19456
K 192,160,161
Mvaa 180,144,16
Dc 243,8,61,0
G 14,70
Cvaa 184,144,32,232
X 163
Mvaa 184,148,36
X 164
Cvaa 188,148,52,236
G 15,71
K 193,162,163
Mvaa 188,144,48
Dc 243,8,61,1024
Ra 144,242,20480
Mvaa 176,152,8
X 165
Mvaa 176,156,12
X 166
Mvaa 180,156,28
Ra 148,242,21504
A 8,8,12
Mvaa 180,152,24
Dc 243,8,61,2048
K 200,164,165
Mvaa 184,152,40
X 167
Mvaa 184,156,44
A 9,9,13
Mvaa 188,156,60
X 168
Mvaa 188,152,56
W 2
Dc 243,8,61,3072
K 201,166,167
Mvav 32,128,196
s_add_u32 m0, s62, 8192
X 169
Mvav 64,128,204
Ra 152,242,22528
A 10,10,14
Mvav 96,128,212
X 170
Mvav 128,128,220
Ra 156,242,23552
K 208,168,169
Mvav 132,132,220
Dc 244,8,61,0
A 11,11,15
Mvav 100,132,212
X 171
Mvav 68,132,204
X 172
Mvav 36,132,196
Ra 128,242,8192
K 209,170,171
Mvav 40,136,196
Dc 244,8,61,1024
G 12,72
Mvav 72,136,204
X 173
Mvav 104,136,212
X 174
Mvav 136,136,220
Ra 132,242,9216
G 13,73
Mvav 140,140,220
Dc 244,8,61,2048
K 216,172,173
Mvav 108,140,212
X 175
Mvav 76,140,204
X 176
Mvav 44,140,196
W 2
Ra 136,242,10240
G 14,74
Mvav 48,144,196
Dc 244,8,61,3072
K 217,174,175
Mvav 80,144,204
s_add_u32 m0, s62, 12288
X 177
Mvav 112,144,212
Ra 140,242,11264
G 15,75
Mvav 144,144,220
X 178
Mvav 148,148,220
K 194,176,177
A 8,8,12
Mvav 116,148,212
X 179
Mvav 84,148,204
X 180
Mvav 52,148,196
Dc 245,8,61,0
Ra 144,242,12288
Mvav 56,152,196
K 195,178,179
A 9,9,13
Mvav 88,152,204
X 181
Mvav 120,152,212
X 182
Mvav 152,152,220
Dc 245,8,61,1024
Ra 148,242,13312
Mvav 156,156,220
K 202,180,181
A 10,10,14
Mvav 124,156,212
X 183
Mvav 92,156,204
X 184
Mvav 60,156,196
W 2
Dc 245,8,61,2048
Ra 152,242,14336
Cvaa 160,128,0,224
A 11,11,15
K 203,182,183
Mvaa 160,132,4
X 185
Cvaa 164,132,20,228
X 186
Mvaa 164,128,16
Dc 245,8,61,3072
Ra 156,242,15360
Cvaa 168,128,32,232
s_add_u32 s61, s61, s63
G 12,76
K 210,184,185
Mvaa 168,132,36
X 187
Cvaa 172,132,52,236
s_xor_b32 s62, s62, 0x10000
G 13,77
Mvaa 172,128,48
X 188
Mvaa 160,136,8
K 211,186,187
G 14,78
Mvaa 160,140,12
s_add_u32 s64, s64, 1
X 189
Mvaa 164,140,28
Ra 128,242,24576
Ra 132,242,25600
Mvaa 164,136,24
X 190
Mvaa 168,136,40
K 218,188,189
G 15,79
Mvaa 168,140,44
X 191
Mvaa 172,140,60
s_lshl_b32 s92, s58, 2
X 160
Mvaa 172,136,56
W 2
K 219,190,191
A 8,8,12
Cvaa 176,144,0,224
X 161
Mvaa 176,148,4
Ra 136,242,26624
Ra 140,242,27648
Cvaa 180,148,20,228
s_add_u32 s91, s92, s51
X 162
Mvaa 180,144,16
A 9,9,13
K 196,160,161
Cvaa 184,144,32,232
X 163
Mvaa 184,148,36
s_add_u32 s95, s92, 0
X 164
Cvaa 188,148,52,236
A 10,10,14
K 197,162,163
Mvaa 188,144,48
X 165
Mvaa 176,152,8
s_and_b32 s95, s95, 15
Ra 144,242,28672
A 11,11,15
Mvaa 176,156,12
Ra 148,242,29696
Mvaa 180,156,28
X 166
Mvaa 180,152,24
s_mul_i32 s95, s95, 40960
K 204,164,165
Mvaa 184,152,40
X 167
Mvaa 184,156,44
G 12,80
Mvaa 188,156,60
s_mul_i32 s71, s51, 0
X 168
Mvaa 188,152,56
W 2
K 205,166,167
G 13,81
Mvav 32,128,192
X 169
Mvav 64,128,200
s_add_u32 s71, s71, 133120
Ra 152,242,30720
Ra 156,242,31744
Mvav 96,128,208
X 170
Mvav 128,128,216
G 14,82
K 212,168,169
Mvav 132,132,216
s_add_u32 m0, s71, s48
X 171
Mvav 100,132,208
X 172
Mvav 68,132,200
G 15,83
Mvav 36,132,192
s_lshl2_add_u32 s94, s48, s95
K 213,170,171
Mvav 40,136,192
X 173
Mvav 72,136,200
X 174
Mvav 104,136,208
D 16,40,94,0
A 8,8,12
Mvav 136,136,216
K 220,172,173
Mvav 140,140,216
X 175
Mvav 108,140,208
A 9,9,13
Mvav 76,140,200
s_waitcnt vmcnt(1) lgkmcnt(0)
s_barrier
v_xor_b32 v242, 0x10000, v242
Mvav 44,140,192
D 17,40,94,1024
Ra 128,242,0
Mvav 48,144,192
X 176
Mvav 80,144,200
K 221,174,175
A 10,10,14
Mvav 112,144,208
X 177
Mvav 144,144,216
D 18,40,94,2048
Ra 132,242,1024
Mvav 148,148,216
X 178
Mvav 116,148,208
K 198,176,177
A 11,11,15
Mvav 84,148,200
X 179
Mvav 52,148,192
D 19,40,94,3072
Ra 136,242,2048
Mvav 56,152,192
s_add_u32 m0, m0, 4096
X 180
Mvav 88,152,200
G 12,84
K 199,178,179
Mvav 120,152,208
X 181
Mvav 152,152,216
Ra 140,242,3072
buffer_load_dwordx4 v20, s[40:43], s94 offen lds
Mvav 156,156,216
X 182
Mvav 124,156,208
K 206,180,181
Mvav 92,156,200
X 183
Mvav 60,156,192
K 207,182,183
W 0
Cvaa 160,128,0,224
Ra 144,242,4096
Ra 148,242,5120
Mvaa 160,132,4
s_add_u32 m0, s62, 0
Ra 152,242,6144
G 13,85
Cvaa 164,132,20,228
X 184
Mvaa 164,128,16
X 185
Cvaa 168,128,32,232
X 186
Mvaa 168,132,36
G 14,86
K 214,184,185
Cvaa 172,132,52,236
Ra 156,242,7168
Dc 1,8,61,0
Mvaa 172,128,48
X 187
Mvaa 160,136,8
X 188
Mvaa 160,140,12
)"
R"(Ra 128,242,16384
G 15,87
Mvaa 164,140,28
Dc 1,8,61,1024
K 215,186,187
Mvaa 164,136,24
X 189
Mvaa 168,136,40
Ra 132,242,17408
A 8,8,12
Mvaa 168,140,44
X 190
Mvaa 172,140,60
Dc 1,8,61,2048
K 222,188,189
Mvaa 172,136,56
X 191
v_smfmac_f32_16x16x64_bf16 a[160:163], a[176:179], v[192:199], v240
A 9,9,13
K 223,190,191
v_smfmac_f32_16x16x64_bf16 a[164:167], a[176:179], v[200:207], v240
Ra 136,242,18432
A 10,10,14
v_smfmac_f32_16x16x64_bf16 a[168:171], a[176:179], v[208:215], v240
Dc 1,8,61,3072
v_smfmac_f32_16x16x64_bf16 a[172:175], a[176:179], v[216:223], v240
W 3
X 160
Cvaa 176,144,0,224
s_add_u32 m0, s62, 4096
X 161
Mvaa 176,148,4
X 162
Cvaa 180,148,20,228
Ra 140,242,19456
A 11,11,15
Mvaa 180,144,16
Dc 243,8,61,0
K 192,160,161
Cvaa 184,144,32,232
X 163
Mvaa 184,148,36
X 164
Cvaa 188,148,52,236
G 12,88
K 193,162,163
Mvaa 188,144,48
Dc 243,8,61,1024
G 13,89
Mvaa 176,152,8
X 165
Mvaa 176,156,12
X 166
Mvaa 180,156,28
Ra 144,242,20480
Ra 148,242,21504
Mvaa 180,152,24
Dc 243,8,61,2048
K 200,164,165
Mvaa 184,152,40
X 167
Mvaa 184,156,44
G 14,90
Mvaa 188,156,60
X 168
Mvaa 188,152,56
W 2
Dc 243,8,61,3072
K 201,166,167
Mvav 32,128,196
s_add_u32 m0, s62, 8192
Ra 152,242,22528
G 15,91
Mvav 64,128,204
X 169
Mvav 96,128,212
X 170
Mvav 128,128,220
Ra 156,242,23552
A 8,8,12
Mvav 132,132,220
Dc 244,8,61,0
K 208,168,169
Mvav 100,132,212
X 171
Mvav 68,132,204
X 172
Mvav 36,132,196
Ra 128,242,8192
A 9,9,13
Mvav 40,136,196
Dc 244,8,61,1024
K 209,170,171
Mvav 72,136,204
X 173
Mvav 104,136,212
Ra 132,242,9216
A 10,10,14
Mvav 136,136,220
X 174
Mvav 140,140,220
Dc 244,8,61,2048
K 216,172,173
Mvav 108,140,212
X 175
Mvav 76,140,204
Ra 136,242,10240
A 11,11,15
Mvav 44,140,196
W 3
X 176
Mvav 48,144,196
Dc 244,8,61,3072
K 217,174,175
Mvav 80,144,204
s_add_u32 m0, s62, 12288
Ra 140,242,11264
v_add_f32_dpp v8, v8, v8 row_ror:8 row_mask:0xf bank_mask:0xf
Mvav 112,144,212
X 177
Mvav 144,144,220
X 178
Mvav 148,148,220
K 194,176,177
v_add_f32_dpp v9, v9, v9 row_ror:8 row_mask:0xf bank_mask:0xf
Mvav 116,148,212
X 179
Mvav 84,148,204
X 180
Mvav 52,148,196
Dc 245,8,61,0
Ra 144,242,12288
Mvav 56,152,196
v_add_f32_dpp v10, v10, v10 row_ror:8 row_mask:0xf bank_mask:0xf
K 195,178,179
Mvav 88,152,204
X 181
Mvav 120,152,212
X 182
Mvav 152,152,220
Dc 245,8,61,1024
v_add_f32_dpp v11, v11, v11 row_ror:8 row_mask:0xf bank_mask:0xf
Mvav 156,156,220
Ra 148,242,13312
K 202,180,181
Mvav 124,156,212
X 183
Mvav 92,156,204
Ra 152,242,14336
v_mov_b32 v12, v8
Mvav 60,156,196
W 3
Dc 245,8,61,2048
v_mov_b32 v13, v9
Cvaa 160,128,0,224
X 184
Mvaa 160,132,4
Ra 156,242,15360
K 203,182,183
Cvaa 164,132,20,228
X 185
Mvaa 164,128,16
Dc 245,8,61,3072
v_mov_b32 v14, v10
Cvaa 168,128,32,232
s_add_u32 s61, s61, s63
X 186
Mvaa 168,132,36
K 210,184,185
v_mov_b32 v15, v11
Cvaa 172,132,52,236
s_xor_b32 s62, s62, 0x10000
X 187
Mvaa 172,128,48
X 188
Mvaa 160,136,8
Ra 128,242,24576
K 211,186,187
Mvaa 160,140,12
s_add_u32 s64, s64, 1
X 189
Mvaa 164,140,28
X 190
Mvaa 164,136,24
Ra 132,242,25600
v_permlane16_swap_b32 v8, v12
Mvaa 168,136,40
K 218,188,189
v_permlane16_swap_b32 v9, v13
Mvaa 168,140,44
X 191
Mvaa 172,140,60
s_lshl_b32 s92, s58, 2
X 160
Mvaa 172,136,56
W 2
K 219,190,191
v_permlane16_swap_b32 v10, v14
Cvaa 176,144,0,224
X 161
Mvaa 176,148,4
Ra 136,242,26624
Ra 140,242,27648
Cvaa 180,148,20,228
s_add_u32 s91, s92, s51
X 162
Mvaa 180,144,16
K 196,160,161
v_permlane16_swap_b32 v11, v15
Cvaa 184,144,32,232
X 163
Mvaa 184,148,36
s_add_u32 s95, s92, 1
X 164
Cvaa 188,148,52,236
A 8,8,12
K 197,162,163
Mvaa 188,144,48
X 165
Mvaa 176,152,8
s_and_b32 s95, s95, 15
Ra 144,242,28672
Ra 148,242,29696
Mvaa 176,156,12
X 166
Mvaa 180,156,28
A 9,9,13
K 204,164,165
Mvaa 180,152,24
s_mul_i32 s95, s95, 40960
X 167
Mvaa 184,152,40
Mvaa 184,156,44
A 10,10,14
Mvaa 188,156,60
s_mul_i32 s71, s51, 0
X 168
Mvaa 188,152,56
W 2
Ra 152,242,30720
K 205,166,167
Mvav 32,128,192
X 169
Mvav 64,128,200
s_add_u32 s71, s71, 143360
Ra 156,242,31744
A 11,11,15
Mvav 96,128,208
X 170
Mvav 128,128,216
K 212,168,169
v_mov_b32 v12, v8
Mvav 132,132,216
s_add_u32 m0, s71, s48
X 171
Mvav 100,132,208
X 172
Mvav 68,132,200
Mvav 36,132,192
s_lshl2_add_u32 s94, s48, s95
v_mov_b32 v13, v9
K 213,170,171
Mvav 40,136,192
X 173
Mvav 72,136,200
X 174
Mvav 104,136,208
D 16,40,94,0
v_mov_b32 v14, v10
Mvav 136,136,216
K 220,172,173
Mvav 140,140,216
X 175
Mvav 108,140,208
v_mov_b32 v15, v11
Mvav 76,140,200
s_waitcnt vmcnt(1) lgkmcnt(0)
s_barrier
v_xor_b32 v242, 0x10000, v242
Mvav 44,140,192
D 17,40,94,1024
Ra 128,242,0
Mvav 48,144,192
X 176
Mvav 80,144,200
K 221,174,175
v_permlane32_swap_b32 v8, v12
Mvav 112,144,208
X 177
Mvav 144,144,216
D 18,40,94,2048
Ra 132,242,1024
Mvav 148,148,216
X 178
Mvav 116,148,208
K 198,176,177
v_permlane32_swap_b32 v9, v13
Mvav 84,148,200
X 179
Mvav 52,148,192
D 19,40,94,3072
Ra 136,242,2048
Mvav 56,152,192
s_add_u32 m0, m0, 4096
X 180
Mvav 88,152,200
K 199,178,179
v_permlane32_swap_b32 v10, v14
Mvav 120,152,208
X 181
Mvav 152,152,216
Ra 140,242,3072
buffer_load_dwordx4 v20, s[40:43], s94 offen lds
Mvav 156,156,216
X 182
Mvav 124,156,208
K 206,180,181
Mvav 92,156,200
X 183
Mvav 60,156,192
K 207,182,183
W 0
Cvaa 160,128,0,224
Ra 144,242,4096
Ra 148,242,5120
Mvaa 160,132,4
s_add_u32 m0, s62, 0
Ra 152,242,6144
v_permlane32_swap_b32 v11, v15
Cvaa 164,132,20,228
X 184
Mvaa 164,128,16
X 185
Cvaa 168,128,32,232
X 186
Mvaa 168,132,36
A 8,8,12
K 214,184,185
Cvaa 172,132,52,236
Ra 156,242,7168
Dc 1,8,61,0
Mvaa 172,128,48
X 187
Mvaa 160,136,8
Ra 128,242,16384
A 9,9,13
Mvaa 160,140,12
X 188
Mvaa 164,140,28
Dc 1,8,61,1024
K 215,186,187
Mvaa 164,136,24
X 189
Mvaa 168,136,40
Ra 132,242,17408
A 10,10,14
Mvaa 168,140,44
X 190
Mvaa 172,140,60
Dc 1,8,61,2048
K 222,188,189
Mvaa 172,136,56
A 11,11,15
v_smfmac_f32_16x16x64_bf16 a[160:163], a[176:179], v[192:199], v240
X 191
v_smfmac_f32_16x16x64_bf16 a[164:167], a[176:179], v[200:207], v240
K 223,190,191
v_and_b32 v7, 0xffff0000, v7
v_smfmac_f32_16x16x64_bf16 a[168:171], a[176:179], v[208:215], v240
Dc 1,8,61,3072
Ra 136,242,18432
v_smfmac_f32_16x16x64_bf16 a[172:175], a[176:179], v[216:223], v240
W 3
X 160
Cvaa 176,144,0,224
s_add_u32 m0, s62, 4096
X 161
Mvaa 176,148,4
X 162
Cvaa 180,148,20,228
Ra 140,242,19456
v_ashrrev_i32 v6, 31, v7
Mvaa 180,144,16
Dc 243,8,61,0
K 192,160,161
Cvaa 184,144,32,232
X 163
Mvaa 184,148,36
v_not_b32 v6, v6
Cvaa 188,148,52,236
X 164
Mvaa 188,144,48
Dc 243,8,61,1024
K 193,162,163
Mvaa 176,152,8
X 165
Mvaa 176,156,12
Ra 144,242,20480
v_or_b32 v6, 0x80000000, v6
Mvaa 180,156,28
)"
R"(X 166
Mvaa 180,152,24
Ra 148,242,21504
v_xor_b32 v7, v7, v6
Mvaa 184,152,40
Dc 243,8,61,2048
K 200,164,165
Mvaa 184,156,44
X 167
Mvaa 188,156,60
X 168
Mvaa 188,152,56
W 2
v_and_b32 v7, 0xffff0000, v7
K 201,166,167
Mvav 32,128,196
Dc 243,8,61,3072
v_mul_f32 v7, 0xbfb8aa3b, v7
Mvav 64,128,204
s_add_u32 m0, s62, 8192
X 169
Mvav 96,128,212
Ra 152,242,22528
Ra 156,242,23552
Mvav 128,128,220
X 170
Mvav 132,132,220
Dc 244,8,61,0
K 208,168,169
Mvav 100,132,212
X 171
Mvav 68,132,204
X 7
Mvav 36,132,196
X 172
Mvav 40,136,196
Ra 128,242,8192
K 209,170,171
Mvav 72,136,204
X 173
Mvav 104,136,212
Dc 244,8,61,1024
Ra 132,242,9216
Mvav 136,136,220
X 174
Mvav 140,140,220
v_add_f32 v7, 1.0, v7
K 216,172,173
Mvav 108,140,212
X 175
Mvav 76,140,204
Dc 244,8,61,2048
Ra 136,242,10240
Mvav 44,140,196
W 3
v_rcp_f32 v7, v7
Mvav 48,144,196
X 176
Mvav 80,144,204
Ra 140,242,11264
K 217,174,175
Mvav 112,144,212
Dc 244,8,61,3072
K 7,7,7
Mvav 144,144,220
s_add_u32 m0, s62, 12288
X 177
Mvav 148,148,220
X 178
Mvav 116,148,212
Ra 144,242,12288
K 194,176,177
Mvav 84,148,204
X 179
Mvav 52,148,196
X 180
Mvav 56,152,196
Dc 245,8,61,0
v_lshlrev_b32 v7, 16, v7
Mvav 88,152,204
K 195,178,179
K 12,8,9
Mvav 120,152,212
X 181
Mvav 152,152,220
X 182
Mvav 156,156,220
Dc 245,8,61,1024
Ra 148,242,13312
Mvav 124,156,212
K 202,180,181
K 13,10,11
Mvav 92,156,204
X 183
Mvav 60,156,196
W 2
X 184
Cvaa 160,128,0,224
Dc 245,8,61,2048
Ra 152,242,14336
Mvaa 160,132,4
K 203,182,183
v_lshlrev_b32 v8, 16, v12
Cvaa 164,132,20,228
X 185
Mvaa 164,128,16
X 186
Cvaa 168,128,32,232
Dc 245,8,61,3072
Ra 156,242,15360
Mvaa 168,132,36
s_add_u32 s61, s61, s63
v_and_b32 v9, 0xffff0000, v12
K 210,184,185
Cvaa 172,132,52,236
s_xor_b32 s62, s62, 0x10000
X 187
Mvaa 172,128,48
X 188
Mvaa 160,136,8
v_lshlrev_b32 v10, 16, v13
K 211,186,187
Mvaa 160,140,12
s_add_u32 s64, s64, 1
X 189
Mvaa 164,140,28
Ra 128,242,24576
v_and_b32 v11, 0xffff0000, v13
Mvaa 164,136,24
X 190
Mvaa 168,136,40
Ra 132,242,25600
K 218,188,189
Mvaa 168,140,44
X 191
Mvaa 172,140,60
s_lshl_b32 s92, s58, 2
U 8,8,7
U 9,9,7
Mvaa 172,136,56
W 2
X 160
Cvaa 176,144,0,224
Ra 136,242,26624
K 219,190,191
Mvaa 176,148,4
X 161
Cvaa 180,148,20,228
s_add_u32 s91, s92, s51
Ra 140,242,27648
U 10,10,7
Mvaa 180,144,16
X 162
Cvaa 184,144,32,232
K 196,160,161
Mvaa 184,148,36
s_add_u32 s95, s92, 2
X 163
Cvaa 188,148,52,236
U 11,11,7
Mvaa 188,144,48
X 164
Mvaa 176,152,8
s_and_b32 s95, s95, 15
Ra 144,242,28672
K 197,162,163
Mvaa 176,156,12
X 165
Mvaa 180,156,28
Ra 148,242,29696
K 12,8,9
Mvaa 180,152,24
s_mul_i32 s95, s95, 40960
X 166
Mvaa 184,152,40
K 204,164,165
K 14,10,11
Mvaa 184,156,44
X 167
Mvaa 188,156,60
s_mul_i32 s71, s51, 0
X 168
Mvaa 188,152,56
W 2
v_mov_b32 v13, s13
K 205,166,167
Mvav 32,128,192
X 169
Mvav 64,128,200
s_add_u32 s71, s71, 153600
Ra 152,242,30720
Ra 156,242,31744
Mvav 96,128,208
X 170
Mvav 128,128,216
v_mov_b32 v15, s13
K 212,168,169
Mvav 132,132,216
s_add_u32 m0, s71, s48
X 171
Mvav 100,132,208
v_lshrrev_b32 v6, 7, v1
Mvav 68,132,200
X 172
Mvav 36,132,192
s_lshl2_add_u32 s94, s48, s95
K 213,170,171
Mvav 40,136,192
X 173
Mvav 72,136,200
v_lshl_or_b32 v6, v6, 28, v1
Mvav 104,136,208
X 174
Mvav 136,136,216
s_lshl_b32 s19, s85, 7
D 16,40,94,0
K 220,172,173
Mvav 140,140,216
X 175
Mvav 108,140,208
X 176
Mvav 76,140,200
s_waitcnt vmcnt(1) lgkmcnt(0)
s_barrier
v_xor_b32 v242, 0x10000, v242
Mvav 44,140,192
s_add_u32 s19, s19, 98304
D 17,40,94,1024
K 221,174,175
Mvav 48,144,192
Ra 128,242,0
Mvav 80,144,200
s_and_b32 s21, s20, 134217728
X 177
Mvav 112,144,208
X 178
Mvav 144,144,216
D 18,40,94,2048
Ra 132,242,1024
Mvav 148,148,216
s_or_b32 s19, s19, s21
K 198,176,177
Mvav 116,148,208
X 179
Mvav 84,148,200
X 180
Mvav 52,148,192
D 19,40,94,3072
Ra 136,242,2048
Mvav 56,152,192
s_add_u32 m0, m0, 4096
Ra 140,242,3072
K 199,178,179
Mvav 88,152,200
X 181
Mvav 120,152,208
X 182
Mvav 152,152,216
buffer_store_dwordx4 v[12:15], v6, s[8:11], s19 offen sc1
K 206,180,181
Mvav 156,156,216
X 183
Mvav 124,156,208
Mvav 92,156,200
K 207,182,183
Mvav 60,156,192
buffer_load_dwordx4 v20, s[40:43], s94 offen lds
.Lf1_40%=:
W 0
Cvaa 160,128,0,224
v_bfe_u32 v28, v1, 4, 2
v_lshlrev_b32 v28, 3, v28
Mvaa 160,132,4
Ra 144,242,4096
Ra 148,242,5120
Cvaa 164,132,20,228
Ra 152,242,6144
buffer_load_dwordx2 v[28:29], v28, s[8:11], s54 offen sc1
Mvaa 164,128,16
s_lshl_b32 s92, s58, 2
s_add_u32 s91, s92, s51
s_add_u32 s95, s91, 3
Cvaa 168,128,32,232
s_and_b32 s95, s95, 15
s_mul_i32 s95, s95, 40960
s_mul_i32 s71, s51, 10240
Mvaa 168,132,36
s_add_u32 s71, s71, 0
s_add_u32 m0, s71, s48
s_lshl2_add_u32 s94, s48, s95
Cvaa 172,132,52,236
X 184
Mvaa 172,128,48
X 185
Mvaa 160,136,8
X 186
Mvaa 160,140,12
Ra 156,242,7168
K 214,184,185
Mvaa 164,140,28
X 187
Mvaa 164,136,24
D 16,40,94,0
Ra 128,242,16384
Mvaa 168,136,40
s_add_u32 s95, s91, 5
s_and_b32 s95, s95, 15
s_mul_i32 s95, s95, 40960
Mvaa 168,140,44
s_mul_i32 s71, s51, 10240
s_add_u32 s71, s71, 20480
Ra 132,242,17408
Mvaa 172,140,60
X 188
Mvaa 172,136,56
D 17,40,94,1024
K 215,186,187
v_smfmac_f32_16x16x64_bf16 a[160:163], a[176:179], v[192:199], v240
X 189
v_smfmac_f32_16x16x64_bf16 a[164:167], a[176:179], v[200:207], v240
X 190
K 222,188,189
X 191
s_nop 0
K 223,190,191
v_smfmac_f32_16x16x64_bf16 a[168:171], a[176:179], v[208:215], v240
D 18,40,94,2048
Ra 136,242,18432
v_smfmac_f32_16x16x64_bf16 a[172:175], a[176:179], v[216:223], v240
W 3
X 160
Cvaa 176,144,0,224
X 161
Mvaa 176,148,4
X 162
Cvaa 180,148,20,228
D 19,40,94,3072
K 192,160,161
Mvaa 180,144,16
s_add_u32 m0, m0, 4096
X 163
Cvaa 184,144,32,232
X 164
Mvaa 184,148,36
Ra 140,242,19456
K 193,162,163
Cvaa 188,148,52,236
buffer_load_dwordx4 v20, s[40:43], s94 offen lds
Mvaa 188,144,48
s_add_u32 m0, s71, s48
s_lshl2_add_u32 s94, s48, s95
Ra 144,242,20480
Mvaa 176,152,8
X 165
Mvaa 176,156,12
X 166
Mvaa 180,156,28
D 16,40,94,0
K 200,164,165
Mvaa 180,152,24
X 167
Mvaa 184,152,40
X 172
Mvaa 184,156,44
D 17,40,94,1024
K 201,166,167
Mvaa 188,156,60
X 173
Mvaa 188,152,56
s_cmp_eq_u32 s60, 16
s_cbranch_scc1 .Lmk_79%=
.Lmkr_80%=:
W 1
Mvav 32,128,196
Ra 148,242,21504
D 18,40,94,2048
Mvav 64,128,204
X 174
Mvav 96,128,212
Ra 152,242,22528
K 216,172,173
Mvav 128,128,220
X 175
Mvav 132,132,220
s_mov_b32 s18, 40960
X 168
Mvav 100,132,212
D 19,40,94,3072
K 217,174,175
Mvav 68,132,204
s_add_u32 m0, m0, 4096
X 169
Mvav 36,132,196
s_mov_b32 s19, 73728
X 170
Mvav 40,136,196
s_bitcmp1_b32 s14, 0
s_cselect_b32 s18, 49152, s18
s_cselect_b32 s19, 98304, s19
Ra 156,242,23552
)"
R"(K 208,168,169
Mvav 72,136,204
s_cmp_lt_u32 s14, 2
s_cselect_b32 s20, s18, s19
s_cselect_b32 s21, s19, s18
X 171
Mvav 104,136,212
X 184
Mvav 136,136,220
buffer_load_dwordx4 v20, s[40:43], s94 offen lds
K 209,170,171
Mvav 140,140,220
s_waitcnt vmcnt(10)
v_cmp_eq_u64 vcc, s[12:13], v[28:29]
s_cmp_eq_u64 vcc, -1
s_cselect_b32 s46, 79691776, 0
s_add_u32 s81, s58, 0
s_and_b32 s81, s81, 3
Mvav 108,140,212
s_lshl_b32 s81, s81, 12
s_add_u32 s81, s81, s49
Ra 128,242,24576
Mvav 76,140,204
X 185
Mvav 44,140,196
W 1
X 186
Mvav 48,144,196
buffer_load_dwordx4 a[184:187], v1, s[44:47], s81 offen offset:0 sc1
K 210,184,185
Mvav 80,144,204
s_add_u32 s82, s81, 131072
X 187
Mvav 112,144,212
v_add_u32 v2, s20, v1
v_add_u32 v3, s21, v1
Mvav 144,144,220
s_lshl_b32 s18, s14, 9
X 188
Mvav 148,148,220
Ra 132,242,25600
K 211,186,187
Mvav 116,148,212
buffer_load_dwordx4 a[200:203], v1, s[44:47], s82 offen offset:0 sc1
Ra 136,242,26624
Mvav 84,148,204
X 189
Mvav 52,148,196
v_lshrrev_b32 v232, 1, v1
v_add_u32 v232, s18, v232
Mvav 56,152,196
X 190
Mvav 88,152,204
K 218,188,189
v_bfe_u32 v12, v1, 6, 2
Mvav 120,152,212
buffer_load_dwordx4 a[188:191], v1, s[44:47], s81 offen offset:1024 sc1
Ra 140,242,27648
Mvav 152,152,220
X 191
Mvav 156,156,220
v_lshrrev_b32 v13, 8, v1
v_bfe_u32 v14, v1, 4, 1
Mvav 124,156,212
X 176
Mvav 92,156,204
K 219,190,191
v_lshlrev_b32 v14, 4, v14
Mvav 60,156,196
X 177
Maav 168,180,208
X 178
K 194,176,177
X 179
X 180
K 195,178,179
Maav 172,180,216
X 181
X 182
K 202,180,181
X 183
s_nop 0
K 203,182,183
Maav 160,180,192
buffer_load_dwordx4 a[204:207], v1, s[44:47], s82 offen offset:1024 sc1
v_mov_b32 v15, 0x3f80
Maav 164,180,200
W 0
Ra 144,242,28672
v_lshlrev_b32 v15, v14, v15
Mvav 96,128,208
Ra 148,242,29696
v_bfe_u32 v14, v1, 5, 1
v_xor_b32 v27, v12, v13
Mvav 128,128,216
buffer_load_dwordx4 a[192:195], v1, s[44:47], s81 offen offset:2048 sc1
v_lshl_or_b32 v27, v27, 1, v14
v_cmp_eq_u32_e64 s[86:87], 0, v27
Mvav 132,132,216
Ra 152,242,30720
Ra 156,242,31744
v_mov_b32 v226, 0
Mvav 100,132,208
v_cndmask_b32_e64 v224, 0, v15, s[86:87]
v_mov_b32 v228, 0
v_mov_b32 v230, v224
Mvav 104,136,208
buffer_load_dwordx4 a[208:211], v1, s[44:47], s82 offen offset:2048 sc1
v_cmp_eq_u32_e64 s[86:87], 1, v27
v_mov_b32 v227, 0
Mvav 136,136,216
v_cndmask_b32_e64 v225, 0, v15, s[86:87]
v_mov_b32 v229, 0
v_mov_b32 v231, v225
Mvav 140,140,216
G 4,160
G 8,161
G 5,164
Mvav 108,140,208
W 0
buffer_load_dwordx4 a[196:199], v1, s[44:47], s81 offen offset:3072 sc1
G 9,165
G 6,168
Mvav 112,144,208
G 10,169
G 7,172
G 11,173
Mvav 144,144,216
A 4,4,8
A 5,5,9
A 6,6,10
Mvav 148,148,216
buffer_load_dwordx4 a[212:215], v1, s[44:47], s82 offen offset:3072 sc1
s_add_u32 s81, s58, 1
s_and_b32 s81, s81, 3
s_lshl_b32 s81, s81, 12
Mvav 116,148,208
s_add_u32 s81, s81, s49
A 7,7,11
ds_write_b64 v232, v[6:7] offset:57344
A 8,4,5
Mvav 120,152,208
A 9,6,7
A 8,8,9
v_cmp_ngt_f32_e64 s[86:87], s90, v8
s_lshl_b32 s19, s14, 2
Mvav 152,152,216
buffer_load_dwordx4 a[216:219], v1, s[44:47], s81 offen offset:0 sc1
s_add_u32 s82, s81, 131072
v_mov_b32 v9, s19
s_or_b64 s[88:89], s[88:89], s[86:87]
Mvav 156,156,216
s_and_b64 s[88:89], s[88:89], s[24:25]
s_cselect_b32 s18, 1, 0
v_mov_b32 v8, s18
ds_write_b32 v9, v8 offset:59392
v_xor_b32 v10, 1024, v232
Mvav 124,156,208
v_bfe_u32 v11, v1, 2, 4
K 96,96,97
K 97,98,99
Mvav 32,128,192
buffer_load_dwordx4 a[232:235], v1, s[44:47], s82 offen offset:0 sc1
K 98,100,101
K 99,102,103
Mvav 64,128,200
ds_write_b128 v2, v[96:99] offset:0
K 128,128,129
K 129,130,131
Mvav 68,132,200
K 130,132,133
K 131,134,135
ds_write_b128 v2, v[128:131] offset:4096
Mvav 36,132,192
buffer_load_dwordx4 a[220:223], v1, s[44:47], s81 offen offset:1024 sc1
K 104,104,105
K 105,106,107
Mvav 40,136,192
K 106,108,109
K 107,110,111
ds_write_b128 v2, v[104:107] offset:1024
Mvav 72,136,200
K 136,136,137
K 137,138,139
K 138,140,141
Mvav 76,140,200
buffer_load_dwordx4 a[236:239], v1, s[44:47], s82 offen offset:1024 sc1
K 139,142,143
ds_write_b128 v2, v[136:139] offset:5120
Mvav 44,140,192
K 112,112,113
K 113,114,115
K 114,116,117
Mvav 48,144,192
K 115,118,119
ds_write_b128 v2, v[112:115] offset:2048
K 144,144,145
Mvav 80,144,200
K 145,146,147
K 146,148,149
K 147,150,151
Mvav 84,148,200
ds_write_b128 v2, v[144:147] offset:6144
buffer_load_dwordx4 a[224:227], v1, s[44:47], s81 offen offset:2048 sc1
K 120,120,121
Mvav 52,148,192
K 121,122,123
K 122,124,125
K 123,126,127
Mvav 56,152,192
ds_write_b128 v2, v[120:123] offset:3072
K 152,152,153
K 153,154,155
Mvav 88,152,200
buffer_load_dwordx4 a[240:243], v1, s[44:47], s82 offen offset:2048 sc1
K 154,156,157
K 155,158,159
Mvav 92,156,200
ds_write_b128 v2, v[152:155] offset:7168
Mvav 60,156,192
buffer_load_dwordx4 a[228:231], v1, s[44:47], s81 offen offset:3072 sc1
buffer_load_dwordx4 a[244:247], v1, s[44:47], s82 offen offset:3072 sc1
s_branch .LL_81%=
.p2align 12
.LL_81%=:
.Lend_43%=:
s_waitcnt vmcnt(8) lgkmcnt(0)
s_barrier
s_cmp_eq_u32 s46, 0
s_cbranch_scc1 .Llate_82%=
ds_read_b32 v11, v11 offset:59392
ds_read_b64 v[8:9], v10 offset:57344
Rv 176,3,0
Rv 180,3,1024
Rv 184,3,2048
Rv 188,3,3072
Rv 192,3,4096
Rv 196,3,5120
Rv 200,3,6144
Rv 204,3,7168
W 6
Mvvv 32,224,176
s_lshl_b32 s92, s58, 2
s_add_u32 s91, s92, s51
s_add_u32 s95, s91, 7
Mvvv 36,228,176
s_and_b32 s95, s95, 15
s_mul_i32 s95, s95, 40960
s_mul_i32 s71, s51, 20480
Mvvv 40,224,180
s_add_u32 s71, s71, 61440
s_add_u32 m0, s71, s48
s_lshl2_add_u32 s94, s48, s95
Mvvv 44,228,180
W 4
v_cmp_ne_u32 vcc, 0, v11
s_cmp_lg_u64 vcc, 0
s_cbranch_scc1 .Lredo%=
A 4,4,8
Mvvv 48,224,184
v_rcp_f32 v4, v4
Mvvv 52,228,184
Ta 128,25,10240
A 5,5,9
Mvvv 56,224,188
v_rcp_f32 v5, v5
Mvvv 60,228,188
W 3
Ta 130,25,15360
D 16,40,94,0
Mvvv 64,224,192
s_add_u32 s95, s91, 9
s_and_b32 s95, s95, 15
s_mul_i32 s95, s95, 40960
Mvvv 68,228,192
s_mul_i32 s71, s51, 10240
s_add_u32 s71, s71, 112640
s_lshl_b32 s93, s58, 12
Mvvv 72,224,196
s_add_u32 s93, s93, s50
U 32,32,4
U 33,33,4
Mvvv 76,228,196
Ta 132,25,20480
Ta 134,25,25600
W 4
Mvvv 80,224,200
U 34,34,4
U 35,35,4
Mvvv 84,228,200
U 36,36,4
U 37,37,4
Mvvv 88,224,204
Ta 136,26,10272
Ta 138,26,15392
Mvvv 92,228,204
Ta 140,26,20512
Ta 142,26,25632
W 4
Zvaa 96,128,184
U 38,38,4
U 39,39,4
Mvaa 96,132,188
K 32,32,33
K 33,34,35
Zvaa 104,132,204
D 17,40,94,1024
K 34,36,37
Mvaa 104,128,200
Ta 144,25,10304
Ta 146,25,15424
Ta 148,25,20544
Ta 150,25,25664
W 4
Zvaa 108,136,200
Ta 152,26,10336
K 35,38,39
Mvaa 108,140,204
Ta 154,26,15456
Ta 156,26,20576
Zvaa 100,140,188
buffer_store_dwordx4 v[32:35], v1, s[8:11], s93 offen offset:0 sc1
U 40,40,4
Mvaa 100,136,184
Ta 158,26,25696
W 4
Zvaa 112,144,184
U 41,41,4
U 42,42,4
Mvaa 112,148,188
U 43,43,4
U 44,44,4
Zvaa 120,148,204
U 45,45,4
U 46,46,4
Mvaa 120,144,200
Ta 128,25,10368
Ta 130,25,15488
Ta 132,25,20608
Ta 134,25,25728
W 4
Zvaa 124,152,200
U 47,47,4
K 40,40,41
Mvaa 124,156,204
D 18,40,94,2048
K 41,42,43
Zvaa 116,156,188
K 42,44,45
K 43,46,47
Mvaa 116,152,184
Ta 136,26,10400
Ta 138,26,15520
Ta 140,26,20640
Ta 142,26,25760
W 4
Zvaa 128,128,184
Ta 144,25,10432
Ta 146,25,15552
Mvaa 128,132,188
buffer_store_dwordx4 v[40:43], v1, s[8:11], s93 offen offset:1024 sc1
U 48,48,4
Zvaa 136,132,204
U 49,49,4
U 50,50,4
Mvaa 136,128,200
Ta 148,25,20672
Ta 150,25,25792
W 4
Zvaa 140,136,200
U 51,51,4
U 52,52,4
Mvaa 140,140,204
U 53,53,4
U 54,54,4
Zvaa 132,140,188
U 55,55,4
K 48,48,49
Mvaa 132,136,184
Ta 152,26,10464
Ta 154,26,15584
Ta 156,26,20704
Ta 158,26,25824
W 4
Zvaa 144,144,184
D 19,40,94,3072
K 49,50,51
Mvaa 144,148,188
s_add_u32 m0, m0, 4096
K 50,52,53
K 51,54,55
Zvaa 152,148,204
Ta 128,25,10496
Ta 130,25,15616
Mvaa 152,144,200
Ta 132,25,20736
Ta 134,25,25856
W 4
Zvaa 156,152,200
buffer_store_dwordx4 v[48:51], v1, s[8:11], s93 offen offset:2048 sc1
)"
R"(U 56,56,4
Mvaa 156,156,204
U 57,57,4
U 58,58,4
Zvaa 148,156,188
U 59,59,4
U 60,60,4
Mvaa 148,152,184
Ta 136,26,10528
Ta 138,26,15648
Ta 140,26,20768
Ta 142,26,25888
W 4
Zvaa 160,128,184
U 61,61,4
U 62,62,4
Mvaa 160,132,188
U 63,63,4
K 56,56,57
Zvaa 168,132,204
buffer_load_dwordx4 v20, s[40:43], s94 offen lds
K 57,58,59
Mvaa 168,128,200
Ta 144,25,30720
Ta 146,25,35840
Ta 148,21,0
Ta 150,21,5120
W 4
Zvaa 172,136,200
s_add_u32 m0, s71, s48
s_lshl2_add_u32 s94, s48, s95
K 58,60,61
Mvaa 172,140,204
Ta 152,26,30752
K 59,62,63
Zvaa 164,140,188
buffer_store_dwordx4 v[56:59], v1, s[8:11], s93 offen offset:3072 sc1
U 64,64,5
Mvaa 164,136,184
Ta 154,26,35872
Ta 156,22,32
Ta 158,22,5152
W 4
Mvaa 96,144,192
U 65,65,5
U 66,66,5
Mvaa 96,148,196
U 67,67,5
U 68,68,5
Mvaa 104,148,212
U 69,69,5
U 70,70,5
Mvaa 104,144,208
Ta 128,25,30784
Ta 130,25,35904
Ta 132,21,64
Ta 134,21,5184
W 4
Mvaa 108,152,208
s_lshl_b32 s66, s14, 1
U 71,71,5
K 64,64,65
Mvaa 108,156,212
s_add_u32 s66, s66, 0
D 16,40,94,0
K 65,66,67
Mvaa 100,156,196
s_cmp_eq_u32 s66, s59
s_cselect_b32 s66, 134217728, 0
K 66,68,69
K 67,70,71
Mvaa 100,152,192
Ta 136,26,30816
Ta 138,26,35936
Ta 140,22,96
Ta 142,22,5216
W 4
Mvaa 112,128,192
s_add_u32 s67, s93, 131072
Ta 144,25,30848
Ta 146,25,35968
Mvaa 112,132,196
s_add_u32 s28, s52, s66
buffer_store_dwordx4 v[64:67], v1, s[8:11], s67 offen offset:0 sc1
U 72,72,5
Mvaa 120,132,212
s_lshl_b32 s66, s14, 1
U 73,73,5
U 74,74,5
Mvaa 120,128,208
Ta 148,21,128
Ta 150,21,5248
W 4
Mvaa 124,136,208
s_add_u32 s66, s66, 1
U 75,75,5
U 76,76,5
Mvaa 124,140,212
s_cmp_eq_u32 s66, s59
s_cselect_b32 s66, 134217728, 0
U 77,77,5
U 78,78,5
Mvaa 116,140,196
s_add_u32 s29, s52, s66
U 79,79,5
K 72,72,73
Mvaa 116,136,192
Ta 152,26,30880
Ta 154,26,36000
Ta 156,22,160
Ta 158,22,5280
W 4
Mvaa 128,144,192
D 17,40,94,1024
K 73,74,75
Mvaa 128,148,196
K 74,76,77
K 75,78,79
Mvaa 136,148,212
Ta 128,25,30912
Ta 130,25,36032
Mvaa 136,144,208
Ta 132,21,192
Ta 134,21,5312
W 4
Mvaa 140,152,208
D 18,40,94,2048
D 19,40,94,3072
s_add_u32 m0, m0, 4096
s_nop 0
buffer_load_dwordx4 v20, s[40:43], s94 offen lds
Mvaa 140,156,212
s_add_u32 s81, s58, 2
s_and_b32 s81, s81, 3
s_lshl_b32 s81, s81, 12
Mvaa 132,156,196
s_add_u32 s81, s81, s49
Ta 136,26,30944
Ta 138,26,36064
Mvaa 132,152,192
Ta 140,22,224
Ta 142,22,5344
W 4
Mvaa 144,128,192
buffer_store_dwordx4 v[72:75], v1, s[8:11], s67 offen offset:1024 sc1
U 80,80,5
Mvaa 144,132,196
U 81,81,5
U 82,82,5
Mvaa 152,132,212
U 83,83,5
U 84,84,5
Mvaa 152,128,208
Ta 144,25,30976
Ta 146,25,36096
Ta 148,21,256
Ta 150,21,5376
W 4
Mvaa 156,136,208
U 85,85,5
U 86,86,5
Mvaa 156,140,212
U 87,87,5
K 80,80,81
Mvaa 148,140,196
K 81,82,83
K 82,84,85
Mvaa 148,136,192
Ta 152,26,31008
Ta 154,26,36128
Ta 156,22,288
Ta 158,22,5408
W 4
Mvaa 160,144,192
buffer_load_dwordx4 a[0:3], v1, s[8:11], s81 offen offset:0 sc1
K 83,86,87
Mvaa 160,148,196
s_add_u32 s82, s81, 131072
Ta 128,21,10240
Ta 130,21,15360
Mvaa 168,148,212
Ta 132,21,20480
Ta 134,21,25600
Mvaa 168,144,208
W 4
Ta 136,22,10272
Ta 138,22,15392
Mvaa 172,152,208
buffer_store_dwordx4 v[80:83], v1, s[8:11], s67 offen offset:2048 sc1
U 88,88,5
Mvaa 172,156,212
U 89,89,5
U 90,90,5
Mvaa 164,156,196
U 91,91,5
U 92,92,5
Mvaa 164,152,192
Ta 140,22,20512
Ta 142,22,25632
s_waitcnt vmcnt(22) lgkmcnt(4)
Mvaa 96,128,216
U 93,93,5
U 94,94,5
Mvaa 96,132,220
U 95,95,5
K 88,88,89
Mvaa 104,132,236
K 89,90,91
K 90,92,93
Mvaa 104,128,232
Ta 144,21,10304
Ta 146,21,15424
Ta 148,21,20544
Ta 150,21,25664
W 4
Mvaa 108,136,232
buffer_load_dwordx4 a[16:19], v1, s[8:11], s82 offen offset:0 sc1
K 91,94,95
Mvaa 108,140,236
Ta 152,22,10336
Ta 154,22,15456
Mvaa 100,140,220
Ta 156,22,20576
Ta 158,22,25696
Mvaa 100,136,216
W 4
Ta 128,21,10368
Ta 130,21,15488
Mvaa 112,144,216
buffer_store_dwordx4 v[88:91], v1, s[8:11], s67 offen offset:3072 sc1
Ta 132,21,20608
Mvaa 112,148,220
Ta 134,21,25728
Ta 136,22,10400
Mvaa 120,148,236
Ta 138,22,15520
Ta 140,22,20640
Mvaa 120,144,232
W 7
Ta 142,22,25760
Ta 144,21,10432
Mvaa 124,152,232
buffer_load_dwordx4 a[4:7], v1, s[8:11], s81 offen offset:1024 sc1
Ta 146,21,15552
Mvaa 124,156,236
Ta 148,21,20672
Ta 150,21,25792
Mvaa 116,156,220
Mvaa 116,152,216
W 8
Ta 152,22,10464
Ta 154,22,15584
Mvaa 128,128,216
buffer_load_dwordx4 a[20:23], v1, s[8:11], s82 offen offset:1024 sc1
Mvaa 128,132,220
Ta 156,22,20704
Ta 158,22,25824
Mvaa 136,132,236
Mvaa 136,128,232
W 8
Ta 128,21,10496
Ta 130,21,15616
Mvaa 140,136,232
s_waitcnt vmcnt(7)
s_barrier
buffer_load_dwordx4 a[8:11], v1, s[8:11], s81 offen offset:2048 sc1
Mvaa 140,140,236
Ta 132,21,20736
Ta 134,21,25856
Mvaa 132,140,220
Mvaa 132,136,216
W 8
Ta 136,22,10528
Ta 138,22,15648
Mvaa 144,144,216
buffer_load_dwordx4 a[24:27], v1, s[8:11], s82 offen offset:2048 sc1
Mvaa 144,148,220
Ta 140,22,20768
Ta 142,22,25888
Mvaa 152,148,236
Mvaa 152,144,232
W 8
Ta 144,21,30720
Ta 146,21,35840
Mvaa 156,152,232
buffer_load_dwordx4 a[12:15], v1, s[8:11], s81 offen offset:3072 sc1
Mvaa 156,156,236
Ta 148,23,0
Ta 150,23,5120
Mvaa 148,156,220
Mvaa 148,152,216
W 8
Ta 152,22,30752
Ta 154,22,35872
Mvaa 160,128,216
buffer_load_dwordx4 a[28:31], v1, s[8:11], s82 offen offset:3072 sc1
Mvaa 160,132,220
s_lshl_b32 s92, s58, 2
s_add_u32 s91, s92, s51
s_add_u32 s95, s91, 11
Mvaa 168,132,236
s_and_b32 s95, s95, 15
s_mul_i32 s95, s95, 40960
s_mul_i32 s71, s51, 10240
Mvaa 168,128,232
W 6
s_add_u32 s71, s71, 40960
s_add_u32 m0, s71, s48
Mvaa 172,136,232
s_lshl2_add_u32 s94, s48, s95
D 16,40,94,0
Ta 156,24,32
Mvaa 172,140,236
s_add_u32 s95, s91, 13
s_and_b32 s95, s95, 15
s_mul_i32 s95, s95, 40960
Mvaa 164,140,220
s_mul_i32 s71, s51, 20480
s_add_u32 s71, s71, 71680
Ta 158,24,5152
Mvaa 164,136,216
W 4
Ta 128,21,30784
Ta 130,21,35904
Mvaa 96,144,224
s_lshl_b32 s80, s58, 3
D 17,40,94,1024
Ta 132,23,64
Mvaa 96,148,228
s_add_u32 s80, s80, s55
Ta 134,23,5184
Ta 136,22,30816
Mvaa 104,148,244
Ta 138,22,35936
Ta 140,24,96
Mvaa 104,144,240
W 7
Ta 142,24,5216
Ta 144,21,30848
Mvaa 108,152,240
s_waitcnt vmcnt(8)
v_mov_b64 v[28:29], s[12:13]
s_mov_b64 exec, 1
buffer_store_dwordx2 v[28:29], off, s[8:11], s80 sc1
s_mov_b64 exec, -1
Ta 146,21,35968
Mvaa 108,156,244
Ta 148,23,128
Ta 150,23,5248
Mvaa 100,156,228
Mvaa 100,152,224
W 8
Ta 152,22,30880
Ta 154,22,36000
Mvaa 112,128,224
D 18,40,94,2048
Mvaa 112,132,228
Ta 156,24,160
Ta 158,24,5280
Mvaa 120,132,244
Mvaa 120,128,240
W 8
Ta 128,21,30912
Ta 130,21,36032
Mvaa 124,136,240
D 19,40,94,3072
Mvaa 124,140,244
s_add_u32 m0, m0, 4096
Ta 132,23,192
Ta 134,23,5312
Mvaa 116,140,228
Mvaa 116,136,224
W 8
Ta 136,22,30944
Ta 138,22,36064
Mvaa 128,144,224
buffer_load_dwordx4 v20, s[40:43], s94 offen lds
Mvaa 128,148,228
s_add_u32 m0, s71, s48
s_lshl2_add_u32 s94, s48, s95
Ta 140,24,224
Mvaa 136,148,244
Ta 142,24,5344
Mvaa 136,144,240
W 8
Ta 144,21,30976
Ta 146,21,36096
Mvaa 140,152,240
D 16,40,94,0
Mvaa 140,156,244
s_add_u32 s95, s92, 15
s_and_b32 s95, s95, 15
s_mul_i32 s95, s95, 40960
Mvaa 132,156,228
s_mul_i32 s71, s51, 0
s_add_u32 s71, s71, 102400
Ta 148,23,256
Mvaa 132,152,224
W 7
Ta 150,23,5376
)"
R"(Ta 152,22,31008
Mvaa 144,128,224
D 17,40,94,1024
Ta 154,22,36128
Mvaa 144,132,228
Ta 156,24,288
Ta 158,24,5408
Mvaa 152,132,244
Mvaa 152,128,240
W 8
Ta 128,23,20480
Ta 130,23,25600
Mvaa 156,136,240
D 18,40,94,2048
Mvaa 156,140,244
Ta 132,23,51200
Ta 134,23,56320
Mvaa 148,140,228
Mvaa 148,136,224
W 8
Ta 136,24,20512
Ta 138,24,25632
Mvaa 160,144,224
D 19,40,94,3072
Mvaa 160,148,228
s_add_u32 m0, m0, 4096
Ta 140,24,51232
Ta 142,24,56352
Mvaa 168,148,244
Mvaa 168,144,240
W 8
Ta 144,23,20544
Ta 146,23,25664
Mvaa 172,152,240
buffer_load_dwordx4 v20, s[40:43], s94 offen lds
Mvaa 172,156,244
s_add_u32 m0, s71, s48
s_lshl2_add_u32 s94, s48, s95
Ta 148,23,51264
Mvaa 164,156,228
Ta 150,23,56384
Mvaa 164,152,224
s_waitcnt vmcnt(15) lgkmcnt(8)
Ta 152,24,20576
Ta 154,24,25696
Mvaa 96,128,0
D 16,40,94,0
Mvaa 96,132,4
Ta 156,24,51296
Ta 158,24,56416
Mvaa 104,132,20
Mvaa 104,128,16
W 8
Ta 128,23,20608
Ta 130,23,25728
Mvaa 108,136,16
D 17,40,94,1024
Mvaa 108,140,20
Ta 132,23,51328
Ta 134,23,56448
Mvaa 100,140,4
Mvaa 100,136,0
W 8
Ta 136,24,20640
Ta 138,24,25760
Mvaa 112,144,0
D 18,40,94,2048
Mvaa 112,148,4
Ta 140,24,51360
Ta 142,24,56480
Mvaa 120,148,20
Mvaa 120,144,16
W 8
Ta 144,23,20672
Ta 146,23,25792
Mvaa 124,152,16
D 19,40,94,3072
Mvaa 124,156,20
s_add_u32 m0, m0, 4096
Ta 148,23,51392
Ta 150,23,56512
Mvaa 116,156,4
buffer_load_dwordx4 v20, s[40:43], s94 offen lds
Mvaa 116,152,0
W 8
s_add_u32 s81, s58, 3
s_and_b32 s81, s81, 3
Mvaa 128,128,0
s_lshl_b32 s81, s81, 12
s_add_u32 s81, s81, s49
Ta 152,24,20704
Mvaa 128,132,4
Ta 154,24,25824
Ta 156,24,51424
Mvaa 136,132,20
buffer_load_dwordx4 a[32:35], v1, s[8:11], s81 offen offset:0 sc1
Ta 158,24,56544
Mvaa 136,128,16
W 8
s_add_u32 s82, s81, 131072
Ta 128,23,20736
Mvaa 140,136,16
s_waitcnt vmcnt(11)
s_barrier
Ta 130,23,25856
Mvaa 140,140,20
Ta 132,23,51456
v_and_b32 v27, 1, v12
Mvaa 132,140,4
buffer_load_dwordx4 a[48:51], v1, s[8:11], s82 offen offset:0 sc1
Ta 134,23,56576
Mvaa 132,136,0
W 8
Ta 136,24,20768
v_lshlrev_b32 v27, 1, v27
Mvaa 144,144,0
Ta 138,24,25888
v_xor_b32 v27, v27, v13
Mvaa 144,148,4
Ta 140,24,51488
Ta 142,24,56608
Mvaa 152,148,20
buffer_load_dwordx4 a[36:39], v1, s[8:11], s81 offen offset:1024 sc1
v_and_b32 v12, 2, v12
Mvaa 152,144,16
W 8
Ta 144,25,0
Ta 146,25,5120
Mvaa 156,152,16
v_or_b32 v12, v12, v14
Mvaa 156,156,20
Ta 148,21,40960
Ta 150,21,46080
Mvaa 148,156,4
buffer_load_dwordx4 a[52:55], v1, s[8:11], s82 offen offset:1024 sc1
v_lshl_or_b32 v12, v27, 2, v12
Mvaa 148,152,0
W 8
Ta 152,26,32
Ta 154,26,5152
Mvaa 160,128,0
v_cmp_eq_u32_e64 s[86:87], 0, v12
Mvaa 160,132,4
Ta 156,22,40992
Ta 158,22,46112
Mvaa 168,132,20
buffer_load_dwordx4 a[40:43], v1, s[8:11], s81 offen offset:2048 sc1
v_cndmask_b32_e64 v4, 0, v15, s[86:87]
Mvaa 168,128,16
W 8
Ta 128,25,64
Ta 130,25,5184
Mvaa 172,136,16
v_cmp_eq_u32_e64 s[86:87], 1, v12
Mvaa 172,140,20
Ta 132,21,41024
Ta 134,21,46144
Mvaa 164,140,4
buffer_load_dwordx4 a[56:59], v1, s[8:11], s82 offen offset:2048 sc1
v_cndmask_b32_e64 v5, 0, v15, s[86:87]
Mvaa 164,136,0
W 8
Ta 136,26,96
Ta 138,26,5216
Mvaa 96,144,8
v_cmp_eq_u32_e64 s[86:87], 2, v12
Mvaa 96,148,12
Ta 140,22,41056
Ta 142,22,46176
Mvaa 104,148,28
buffer_load_dwordx4 a[44:47], v1, s[8:11], s81 offen offset:3072 sc1
v_cndmask_b32_e64 v6, 0, v15, s[86:87]
Mvaa 104,144,24
W 8
Ta 144,25,128
Ta 146,25,5248
Mvaa 108,152,24
v_cmp_eq_u32_e64 s[86:87], 3, v12
Mvaa 108,156,28
Ta 148,21,41088
Ta 150,21,46208
Mvaa 100,156,12
buffer_load_dwordx4 a[60:63], v1, s[8:11], s82 offen offset:3072 sc1
v_cndmask_b32_e64 v7, 0, v15, s[86:87]
Mvaa 100,152,8
W 8
Ta 152,26,160
Ta 154,26,5280
Mvaa 112,128,8
v_cmp_eq_u32_e64 s[86:87], 4, v12
Mvaa 112,132,12
Ta 156,22,41120
Ta 158,22,46240
Mvaa 120,132,28
v_cndmask_b32_e64 v8, 0, v15, s[86:87]
Mvaa 120,128,24
W 8
Ta 128,25,192
Ta 130,25,5312
Mvaa 124,136,24
v_cmp_eq_u32_e64 s[86:87], 5, v12
v_bfe_u32 v28, v1, 4, 2
Mvaa 124,140,28
Ta 132,21,41152
v_lshlrev_b32 v28, 3, v28
Mvaa 116,140,12
Ta 134,21,46272
v_cndmask_b32_e64 v9, 0, v15, s[86:87]
Mvaa 116,136,8
W 8
buffer_load_dwordx2 v[28:29], v28, s[8:11], s55 offen sc1
Ta 136,26,224
Mvaa 128,144,8
Ta 138,26,5344
v_cmp_eq_u32_e64 s[86:87], 6, v12
Mvaa 128,148,12
Ta 140,22,41184
Ta 142,22,46304
Mvaa 136,148,28
v_cndmask_b32_e64 v10, 0, v15, s[86:87]
Mvaa 136,144,24
W 8
Ta 144,25,256
Ta 146,25,5376
Mvaa 140,152,24
s_waitcnt vmcnt(9)
s_barrier
v_cmp_eq_u32_e64 s[86:87], 7, v12
Mvaa 140,156,28
Ta 148,21,41216
Ta 150,21,46336
Mvaa 132,156,12
v_cndmask_b32_e64 v11, 0, v15, s[86:87]
Mvaa 132,152,8
W 8
Ta 152,26,288
Ta 154,26,5408
Mvaa 144,128,8
s_mov_b32 s20, s38
Mvaa 144,132,12
Ta 156,22,41248
Ta 158,22,46368
Mvaa 152,132,28
s_and_b32 s21, s39, 0xffff
Mvaa 152,128,24
W 8
Ta 128,21,51200
Ta 130,21,56320
Mvaa 156,136,24
s_mov_b32 s22, 2621440
Mvaa 156,140,28
s_mov_b32 s23, 0x20000
Ta 132,23,10240
Ta 134,23,15360
Mvaa 148,140,12
Mvaa 148,136,8
W 8
s_and_b32 s17, s17, 0xffff
Ta 136,23,30720
Mvaa 160,144,8
Ta 138,23,35840
Mvaa 160,148,12
s_mov_b32 s18, 2621440
Ta 140,23,40960
Ta 142,23,46080
Mvaa 168,148,28
Mvaa 168,144,24
W 8
s_mov_b32 s19, 0x20000
Ta 144,22,51232
Mvaa 172,152,24
Ta 146,22,56352
Mvaa 172,156,28
s_lshr_b32 s66, s15, 3
Ta 148,24,10272
Ta 150,24,15392
Mvaa 164,156,12
Mvaa 164,152,8
s_waitcnt vmcnt(5) lgkmcnt(8)
s_lshl_b32 s66, s66, 4
Ta 152,24,30752
Mvaa 96,128,32
Ta 154,24,35872
Mvaa 96,132,36
s_add_u32 s66, s66, s59
Ta 156,24,40992
Ta 158,24,46112
Mvaa 104,132,52
Mvaa 104,128,48
s_waitcnt vmcnt(1) lgkmcnt(8)
s_lshl_b32 s63, s66, 6
Ta 128,21,51264
Mvaa 104,136,56
Ta 130,21,56384
Mvaa 104,140,60
s_add_u32 s63, s63, 65536
Ta 132,23,10304
Ta 134,23,15424
Mvaa 96,140,44
Mvaa 96,136,40
W 8
s_mul_i32 s62, s66, 40960
Ta 136,23,30784
Mvaa 100,144,32
Ta 138,23,35904
Mvaa 100,148,36
s_add_u32 s62, s62, 56623104
Ta 140,23,41024
Ta 142,23,46144
Mvaa 108,148,52
Mvaa 108,144,48
W 8
s_lshl_b32 s66, s14, 13
Ta 144,22,51296
Mvaa 108,152,56
Ta 146,22,56416
Mvaa 108,156,60
s_add_u32 s78, s62, s66
Ta 148,24,10336
Ta 150,24,15456
Mvaa 100,156,44
s_waitcnt vmcnt(0)
v_cmp_eq_u64 vcc, s[12:13], v[28:29]
s_cmp_eq_u64 vcc, -1
s_cbranch_scc0 .Lspin_84%=
.Lok_85%=:
s_add_u32 s81, s58, 1
s_and_b32 s81, s81, 3
Mvaa 100,152,40
W 8
s_lshl_b32 s81, s81, 12
s_add_u32 s81, s81, s50
Mvaa 112,128,32
s_add_u32 s79, s78, 327680
buffer_load_dwordx4 a[64:67], v1, s[8:11], s81 offen offset:0 sc1
Ta 152,24,30816
Mvaa 112,132,36
s_add_u32 s82, s81, 131072
s_and_b32 s66, s14, 1
Ta 154,24,35936
Mvaa 120,132,52
Ta 156,24,41056
Ta 158,24,46176
Mvaa 120,128,48
W 8
K 96,96,97
K 97,98,99
Mvaa 120,136,56
s_mul_i32 s66, s66, 327680
Ta 128,21,51328
Ta 130,21,56448
Mvaa 120,140,60
s_add_u32 s65, s62, s66
buffer_load_dwordx4 a[80:83], v1, s[8:11], s82 offen offset:0 sc1
K 98,100,101
Mvaa 112,140,44
Ta 132,23,10368
Ta 134,23,15488
Mvaa 112,136,40
W 8
s_add_u32 s65, s65, 32768
K 99,102,103
Mvaa 116,144,32
s_add_u32 s83, s28, 0
Ta 136,23,30848
Ta 138,23,35968
Mvaa 116,148,36
s_mov_b32 s64, 0
buffer_load_dwordx4 a[68:71], v1, s[8:11], s81 offen offset:1024 sc1
Ta 140,23,41088
Mvaa 124,148,52
Ta 142,23,46208
Mvaa 124,144,48
W 8
Ta 144,22,51360
v_add_u32 v19, 4096, v1
Mvaa 124,152,56
Ta 146,22,56480
Mvaa 124,156,60
buffer_store_dwordx4 v[96:99], v1, s[8:11], s83 offen sc1
K 104,104,105
Mvaa 116,156,44
K 105,106,107
v_bfe_u32 v27, v1, 4, 3
Mvaa 116,152,40
W 6
Ta 148,24,10400
Ta 150,24,15520
Mvaa 128,128,32
K 106,108,109
v_lshlrev_b32 v27, 3, v27
Mvaa 128,132,36
buffer_load_dwordx4 a[84:87], v1, s[8:11], s82 offen offset:1024 sc1
Ta 152,24,30880
Mvaa 136,132,52
Ta 154,24,36000
K 107,110,111
Mvaa 136,128,48
W 6
Ta 156,24,41120
)"
R"(v_bfe_u32 v18, v1, 7, 1
Mvaa 136,136,56
s_add_u32 s83, s29, 40960
s_add_u32 s66, s14, 1
Ta 158,24,46240
Mvaa 136,140,60
s_lshr_b32 s66, s66, 2
buffer_load_dwordx4 a[72:75], v1, s[8:11], s81 offen offset:2048 sc1
Ta 128,21,51392
Mvaa 128,140,44
s_mul_i32 s66, s66, 0
Ta 130,21,56512
Ta 132,23,10432
Mvaa 128,136,40
W 7
Ta 134,23,15552
Ta 136,23,30912
Mvaa 132,144,32
s_sub_u32 s66, 512, s66
Ta 138,23,36032
Mvaa 132,148,36
buffer_store_dwordx4 v[104:107], v1, s[8:11], s83 offen sc1
K 112,112,113
Mvaa 140,148,52
K 113,114,115
K 114,116,117
Mvaa 140,144,48
W 6
Ta 140,23,41152
Ta 142,23,46272
Mvaa 140,152,56
v_mul_u32_u24 v18, s66, v18
K 115,118,119
Mvaa 140,156,60
s_add_u32 s83, s28, 8192
buffer_load_dwordx4 a[88:91], v1, s[8:11], s82 offen offset:2048 sc1
Ta 144,22,51424
Mvaa 132,156,44
Ta 146,22,56544
v_add_u32 v18, v18, v27
Mvaa 132,152,40
W 6
Ta 148,24,10464
Ta 150,24,15584
Mvaa 144,128,32
s_mov_b32 s71, 0
Ta 152,24,30944
Ta 154,24,36064
Mvaa 144,132,36
buffer_load_dwordx4 a[76:79], v1, s[8:11], s81 offen offset:3072 sc1
v_bfe_u32 v20, v1, 4, 4
Mvaa 152,132,52
Ta 156,24,41184
Ta 158,24,46304
Mvaa 152,128,48
W 8
Ta 128,21,51456
v_mul_u32_u24 v20, 1280, v20
Mvaa 152,136,56
Ta 130,21,56576
v_lshrrev_b32 v12, 8, v1
Mvaa 152,140,60
buffer_store_dwordx4 v[112:115], v1, s[8:11], s83 offen sc1
K 120,120,121
Mvaa 144,140,44
K 121,122,123
K 122,124,125
Mvaa 144,136,40
W 6
Ta 132,23,10496
K 123,126,127
Mvaa 148,144,32
s_add_u32 s83, s29, 49152
Ta 134,23,15616
v_lshl_add_u32 v20, v12, 4, v20
Mvaa 148,148,36
s_bfe_u32 s66, s59, 0x10001
buffer_load_dwordx4 a[92:95], v1, s[8:11], s82 offen offset:3072 sc1
Ta 136,23,30976
Mvaa 156,148,52
s_add_u32 s81, s58, 2
s_and_b32 s81, s81, 3
s_lshl_b32 s81, s81, 12
Mvaa 156,144,48
W 5
s_add_u32 s81, s81, s50
Ta 138,23,36096
Mvaa 156,152,56
s_lshl_b32 s67, s57, 1
Ta 140,23,41216
Ta 142,23,46336
Mvaa 156,156,60
s_add_u32 s66, s66, s67
buffer_load_dwordx4 a[96:99], v1, s[8:11], s81 offen offset:0 sc1
Ta 144,22,51488
Mvaa 148,156,44
s_add_u32 s82, s81, 131072
Ta 146,22,56608
Ta 148,24,10528
Mvaa 148,152,40
W 7
s_lshl_b32 s61, s66, 2
Ta 150,24,15648
Mvaa 160,128,32
s_lshr_b32 s66, s59, 2
Ta 152,24,31008
Ta 154,24,36128
Mvaa 160,132,36
buffer_store_dwordx4 v[120:123], v1, s[8:11], s83 offen sc1
K 128,128,129
Mvaa 168,132,52
s_lshl_b32 s66, s66, 1
K 129,130,131
K 130,132,133
Mvaa 168,128,48
W 6
Ta 156,24,41248
K 131,134,135
Mvaa 168,136,56
s_add_u32 s83, s28, 16384
Ta 158,24,46368
Ta 128,25,10240
Mvaa 168,140,60
s_add_u32 s61, s61, s66
buffer_load_dwordx4 a[112:115], v1, s[8:11], s82 offen offset:0 sc1
Ta 130,25,15360
Mvaa 160,140,44
s_and_b32 s66, s59, 1
Ta 132,25,20480
Ta 134,25,25600
Mvaa 160,136,40
W 8
Ta 136,26,10272
Ta 138,26,15392
Mvaa 164,144,32
s_add_u32 s61, s61, s66
Mvaa 164,148,36
buffer_load_dwordx4 a[100:103], v1, s[8:11], s81 offen offset:1024 sc1
Ta 140,26,20512
Mvaa 172,148,52
s_xor_b32 s67, s61, 0
Ta 142,26,25632
Mvaa 172,144,48
W 8
Ta 144,25,10304
Ta 146,25,15424
Mvaa 172,152,56
s_mul_i32 s72, s67, 20480
Mvaa 172,156,60
buffer_store_dwordx4 v[128:131], v1, s[8:11], s83 offen sc1
K 136,136,137
Mvaa 164,156,44
s_mul_i32 s66, s58, 320
K 137,138,139
K 138,140,141
Mvaa 164,152,40
W 6
Ta 148,25,20544
K 139,142,143
Zvav 176,128,32
s_add_u32 s83, s29, 57344
Ta 150,25,25664
Ta 152,26,10336
Mvav 176,132,40
s_add_u32 s72, s72, s66
buffer_load_dwordx4 a[116:119], v1, s[8:11], s82 offen offset:1024 sc1
Ta 154,26,15456
Zvav 184,132,72
s_mul_i32 s75, s67, 20480
Ta 156,26,20576
Ta 158,26,25696
Mvav 184,128,64
W 8
Ta 128,25,10368
Ta 130,25,15488
Zvav 188,136,64
s_mul_i32 s66, s58, 2560
Mvav 188,140,72
buffer_load_dwordx4 a[104:107], v1, s[8:11], s81 offen offset:2048 sc1
Ta 132,25,20608
Zvav 180,140,40
s_add_u32 s75, s75, s66
Ta 134,25,25728
Mvav 180,136,32
W 8
s_add_u32 s75, s75, 131072
Ta 136,26,10400
Zvav 192,144,32
Ta 138,26,15520
Mvav 192,148,40
s_xor_b32 s67, s61, 124
buffer_store_dwordx4 v[136:139], v1, s[8:11], s83 offen sc1
K 144,144,145
Zvav 200,148,72
K 145,146,147
K 146,148,149
Mvav 200,144,64
W 6
Ta 140,26,20640
K 147,150,151
Zvav 204,152,64
s_add_u32 s83, s28, 24576
s_mul_i32 s73, s67, 20480
Ta 142,26,25760
Mvav 204,156,72
s_mul_i32 s66, s58, 320
buffer_load_dwordx4 a[120:123], v1, s[8:11], s82 offen offset:2048 sc1
Ta 144,25,10432
Zvav 196,156,40
Ta 146,25,15552
Ta 148,25,20672
Mvav 196,152,32
W 7
s_add_u32 s73, s73, s66
Ta 150,25,25792
Zvav 208,128,32
Ta 152,26,10464
Ta 154,26,15584
Mvav 208,132,40
s_mul_i32 s76, s67, 20480
buffer_load_dwordx4 a[108:111], v1, s[8:11], s81 offen offset:3072 sc1
Ta 156,26,20704
Zvav 216,132,72
Ta 158,26,25824
Mvav 216,128,64
W 8
s_mul_i32 s66, s58, 2560
Ta 128,25,10496
Zvav 220,136,64
Ta 130,25,15616
Mvav 220,140,72
s_add_u32 s76, s76, s66
buffer_store_dwordx4 v[144:147], v1, s[8:11], s83 offen sc1
K 152,152,153
Zvav 212,140,40
K 153,154,155
K 154,156,157
Mvav 212,136,32
W 6
Ta 132,25,20736
K 155,158,159
Zvav 224,144,32
s_add_u32 s83, s29, 65536
s_add_u32 s76, s76, 131072
Ta 134,25,25856
Mvav 224,148,40
buffer_load_dwordx4 a[124:127], v1, s[8:11], s82 offen offset:3072 sc1
Ta 136,26,10528
Zvav 232,148,72
s_add_u32 s81, s58, 3
s_and_b32 s81, s81, 3
s_lshl_b32 s81, s81, 12
Mvav 232,144,64
W 5
s_add_u32 s81, s81, s50
Ta 138,26,15648
Zvav 236,152,64
Ta 140,26,20768
v_bfe_u32 v15, v1, 7, 1
Mvav 236,156,72
buffer_load_dwordx4 a[184:187], v1, s[8:11], s81 offen offset:0 sc1
Ta 142,26,25888
Zvav 228,156,40
s_add_u32 s82, s81, 131072
Ta 144,25,30720
v_mul_u32_u24 v14, 10240, v15
Mvav 228,152,32
W 5
Ta 146,25,35840
v_lshrrev_b32 v15, 8, v1
Zvav 240,128,32
Ta 148,21,0
Ta 150,21,5120
Mvav 240,132,40
buffer_store_dwordx4 v[152:155], v1, s[8:11], s83 offen sc1
K 160,160,161
Zvav 248,132,72
K 161,162,163
K 162,164,165
Mvav 248,128,64
W 4
K 163,166,167
v_lshl_add_u32 v14, v15, 7, v14
Zvav 252,136,64
s_add_u32 s83, s28, 32768
Ta 152,26,30752
Ta 154,26,35872
Mvav 252,140,72
buffer_load_dwordx4 a[200:203], v1, s[8:11], s82 offen offset:0 sc1
v_and_b32 v15, 0x70, v1
Zvav 244,140,40
Ta 156,22,32
Ta 158,22,5152
Mvav 244,136,32
W 4
Ta 128,25,30784
v_add_u32 v14, v14, v15
Mvav 176,144,48
s_bitcmp1_b32 s14, 0
s_cselect_b32 s74, s73, s72
s_cselect_b32 s77, s76, s75
Ta 130,25,35904
Ta 132,21,64
Mvav 176,148,56
s_add_u32 s74, s74, 256
buffer_load_dwordx4 a[188:191], v1, s[8:11], s81 offen offset:1024 sc1
Ta 134,21,5184
Mvav 184,148,88
s_add_u32 s77, s77, 2048
Ta 136,26,30816
Ta 138,26,35936
Mvav 184,144,80
W 6
Ta 140,22,96
Ta 142,22,5216
Mvav 188,152,80
s_lshl_b32 s66, s14, 6
Ta 144,25,30848
Ta 146,25,35968
Mvav 188,156,88
s_lshl_b32 s67, s14, 9
buffer_store_dwordx4 v[160:163], v1, s[8:11], s83 offen sc1
K 168,168,169
Mvav 180,156,56
K 169,170,171
K 170,172,173
Mvav 180,152,48
W 6
Ta 148,21,128
K 171,174,175
Mvav 192,128,48
s_add_u32 s83, s29, 73728
s_add_u32 s72, s72, s66
Ta 150,21,5248
Mvav 192,132,56
s_add_u32 s75, s75, s67
buffer_load_dwordx4 a[204:207], v1, s[8:11], s82 offen offset:1024 sc1
Ta 152,26,30880
Mvav 200,132,88
Ta 154,26,36000
Ta 156,22,160
Mvav 200,128,80
W 7
s_add_u32 s73, s73, s66
Ta 158,22,5280
Mvav 204,136,80
Ta 128,25,30912
Ta 130,25,36032
Mvav 204,140,88
s_add_u32 s76, s76, s67
buffer_load_dwordx4 a[192:195], v1, s[8:11], s81 offen offset:2048 sc1
Ta 132,21,192
Mvav 196,140,56
Ta 134,21,5312
Mvav 196,136,48
W 8
Ta 136,26,30944
Ta 138,26,36064
Mvav 208,144,48
Mvav 208,148,56
buffer_store_dwordx4 v[168:171], v1, s[8:11], s83 offen sc1
Ta 140,22,224
Mvav 216,148,88
Ta 142,22,5344
Mvav 216,144,80
W 8
Ta 144,25,30976
Ta 146,25,36096
Mvav 220,152,80
Mvav 220,156,88
buffer_load_dwordx4 a[208:211], v1, s[8:11], s82 offen offset:2048 sc1
Ta 148,21,256
Mvav 212,156,56
Ta 150,21,5376
Mvav 212,152,48
W 8
Ta 152,26,31008
Ta 154,26,36128
Mvav 224,128,48
Mvav 224,132,56
buffer_load_dwordx4 a[196:199], v1, s[8:11], s81 offen offset:3072 sc1
Ta 156,22,288
Mvav 232,132,88
Ta 158,22,5408
Mvav 232,128,80
W 8
Ta 128,21,10240
Ta 130,21,15360
Mvav 236,136,80
Mvav 236,140,88
buffer_load_dwordx4 a[212:215], v1, s[8:11], s82 offen offset:3072 sc1
Ta 132,21,20480
Mvav 228,140,56
Ta 134,21,25600
Mvav 228,136,48
W 8
Ta 136,22,10272
Ta 138,22,15392
Mvav 240,144,48
Mvav 240,148,56
)"
R"(buffer_load_dwordx4 v[36:39], v20, s[16:19], s72 offen
Ta 140,22,20512
Mvav 248,148,88
Ta 142,22,25632
Mvav 248,144,80
W 8
Ta 144,21,10304
Ta 146,21,15424
Mvav 252,152,80
Mvav 252,156,88
buffer_load_dwordx4 v[44:47], v14, s[8:11], s75 offen sc1
Ta 148,21,20544
Mvav 244,156,56
Ta 150,21,25664
Mvav 244,152,48
s_waitcnt vmcnt(31) lgkmcnt(8)
Ta 152,22,10336
Ta 154,22,15456
Mvaa 176,128,64
Mvaa 176,132,68
buffer_load_dwordx4 v[52:55], v20, s[16:19], s73 offen
Ta 156,22,20576
Mvaa 184,132,84
Ta 158,22,25696
Mvaa 184,128,80
W 8
Ta 128,21,10368
Ta 130,21,15488
Mvaa 188,136,80
Mvaa 188,140,84
buffer_load_dwordx4 v[60:63], v14, s[8:11], s76 offen sc1
Ta 132,21,20608
Mvaa 180,140,68
Ta 134,21,25728
Mvaa 180,136,64
W 8
Ta 136,22,10400
Ta 138,22,15520
Mvaa 192,144,64
Mvaa 192,148,68
buffer_load_dwordx4 v[68:71], v20, s[16:19], s74 offen
Ta 140,22,20640
Mvaa 200,148,84
Ta 142,22,25760
Mvaa 200,144,80
W 8
Ta 144,21,10432
Ta 146,21,15552
Mvaa 204,152,80
Mvaa 204,156,84
buffer_load_dwordx4 v[76:79], v14, s[8:11], s77 offen sc1
Ta 148,21,20672
Mvaa 196,156,68
Ta 150,21,25792
Mvaa 196,152,64
W 8
Ta 152,22,10464
Ta 154,22,15584
Mvaa 208,128,64
Mvaa 208,132,68
buffer_load_dwordx2 v[12:13], v27, s[8:11], 0 offen offset:3584 sc1
Ta 156,22,20704
Mvaa 216,132,84
Ta 158,22,25824
Mvaa 216,128,80
W 8
Ta 128,21,10496
Ta 130,21,15616
Mvaa 220,136,80
Mvaa 220,140,84
s_waitcnt vmcnt(10)
v_lshlrev_b32 v2, 2, v1
v_mov_b64 v[28:29], s[12:13]
s_mov_b64 exec, 3
buffer_store_dwordx2 v[28:29], v2, s[8:11], s53 offen offset:0 sc1
s_mov_b64 exec, -1
Ta 132,21,20736
Ta 134,21,25856
Mvaa 212,140,68
Mvaa 212,136,64
W 8
Ta 136,22,10528
Ta 138,22,15648
Mvaa 224,144,64
Mvaa 224,148,68
Ta 140,22,20768
Ta 142,22,25888
Mvaa 232,148,84
Mvaa 232,144,80
W 8
Ta 144,21,30720
Ta 146,21,35840
Mvaa 236,152,80
Mvaa 236,156,84
Ta 148,23,0
Ta 150,23,5120
Mvaa 228,156,68
Mvaa 228,152,64
W 8
Ta 152,22,30752
Ta 154,22,35872
Mvaa 240,128,64
Mvaa 240,132,68
Ta 156,24,32
Ta 158,24,5152
Mvaa 248,132,84
Mvaa 248,128,80
W 8
Ta 128,21,30784
Ta 130,21,35904
Mvaa 252,136,80
Mvaa 252,140,84
Ta 132,23,64
Ta 134,23,5184
Mvaa 244,140,68
Mvaa 244,136,64
W 8
Ta 136,22,30816
Ta 138,22,35936
Mvaa 176,144,72
Mvaa 176,148,76
Ta 140,24,96
Ta 142,24,5216
Mvaa 184,148,92
Mvaa 184,144,88
W 8
Ta 144,21,30848
Ta 146,21,35968
Mvaa 188,152,88
Mvaa 188,156,92
Ta 148,23,128
Ta 150,23,5248
Mvaa 180,156,76
Mvaa 180,152,72
W 8
Ta 152,22,30880
Ta 154,22,36000
Mvaa 192,128,72
Mvaa 192,132,76
Ta 156,24,160
Ta 158,24,5280
Mvaa 200,132,92
Mvaa 200,128,88
W 8
Ta 128,21,30912
Ta 130,21,36032
Mvaa 204,136,88
Mvaa 204,140,92
Ta 132,23,192
Ta 134,23,5312
Mvaa 196,140,76
Mvaa 196,136,72
W 8
Ta 136,22,30944
Ta 138,22,36064
Mvaa 208,144,72
Mvaa 208,148,76
Ta 140,24,224
Ta 142,24,5344
Mvaa 216,148,92
Mvaa 216,144,88
W 8
Ta 144,21,30976
Ta 146,21,36096
Mvaa 220,152,88
Mvaa 220,156,92
Ta 148,23,256
Ta 150,23,5376
Mvaa 212,156,76
Mvaa 212,152,72
W 8
Ta 152,22,31008
Ta 154,22,36128
Mvaa 224,128,72
Mvaa 224,132,76
Ta 156,24,288
Ta 158,24,5408
Mvaa 232,132,92
Mvaa 232,128,88
W 8
Ta 128,23,20480
Ta 130,23,25600
Mvaa 236,136,88
Mvaa 236,140,92
Ta 132,23,51200
Ta 134,23,56320
Mvaa 228,140,76
Mvaa 228,136,72
W 8
Ta 136,25,0
Ta 138,25,5120
Mvaa 240,144,72
Mvaa 240,148,76
Ta 140,21,40960
Ta 142,21,46080
Mvaa 248,148,92
s_waitcnt vmcnt(1)
v_xor_b32 v12, s12, v12
v_xor_b32 v13, s13, v13
v_lshrrev_b32 v3, 11, v12
v_or_b32 v13, v13, v3
v_cmp_eq_u32 vcc, 0, v13
s_cmp_eq_u64 vcc, -1
s_cbranch_scc0 .LL_89%=
.LL_90%=:
v_bfe_u32 v13, v12, 4, 7
s_xor_b32 s66, s61, 124
v_cmp_eq_u32 vcc, s61, v13
v_cmp_eq_u32_e64 s[86:87], s66, v13
s_or_b32 s68, vcc_lo, s86
s_and_b32 s68, s68, 255
s_cbranch_scc1 .Lup_87%=
.Lupr_88%=:
Mvaa 248,144,88
W 8
Ta 144,21,51200
Ta 146,21,56320
Mvaa 252,152,88
v_lshlrev_b32 v32, 16, v36
Mvaa 252,156,92
Ta 148,23,10240
v_and_b32 v33, 0xffff0000, v36
Mvaa 244,156,76
Ta 150,23,15360
v_lshlrev_b32 v34, 16, v37
Mvaa 244,152,72
W 8
Ta 152,23,30720
v_and_b32 v35, 0xffff0000, v37
Mvaa 176,128,96
Ta 154,23,35840
v_lshlrev_b32 v36, 16, v38
Mvaa 176,132,100
buffer_load_dwordx2 v[16:17], v18, s[8:11], s63 offen sc1
v_and_b32 v37, 0xffff0000, v38
Mvaa 184,132,116
Ta 156,23,40960
Ta 158,23,46080
Mvaa 184,128,112
W 8
v_lshlrev_b32 v38, 16, v39
v_and_b32 v39, 0xffff0000, v39
Mvaa 184,136,120
Ta 128,24,20512
Ta 130,24,25632
Mvaa 184,140,124
v_lshlrev_b32 v40, 16, v44
v_and_b32 v41, 0xffff0000, v44
Mvaa 176,140,108
s_barrier
Ta 132,24,51232
Ta 134,24,56352
Mvaa 176,136,104
W 8
v_lshlrev_b32 v42, 16, v45
v_and_b32 v43, 0xffff0000, v45
Mvaa 176,144,184
Ta 136,26,32
Ta 138,26,5152
Mvaa 176,148,188
v_lshlrev_b32 v44, 16, v46
v_and_b32 v45, 0xffff0000, v46
Mvaa 184,148,204
Ta 140,22,40992
Ta 142,22,46112
Mvaa 184,144,200
W 8
v_lshlrev_b32 v46, 16, v47
v_and_b32 v47, 0xffff0000, v47
Mvaa 184,152,208
Ta 144,22,51232
Ta 146,22,56352
Mvaa 184,156,212
v_lshlrev_b32 v48, 16, v52
v_and_b32 v49, 0xffff0000, v52
Mvaa 176,156,196
Ta 148,24,10272
Ta 150,24,15392
Mvaa 176,152,192
W 8
v_lshlrev_b32 v50, 16, v53
v_and_b32 v51, 0xffff0000, v53
Mvaa 180,128,96
Ta 152,24,30752
Ta 154,24,35872
Mvaa 180,132,100
v_lshlrev_b32 v52, 16, v54
v_and_b32 v53, 0xffff0000, v54
Mvaa 188,132,116
Ta 156,24,40992
Ta 158,24,46112
Mvaa 188,128,112
W 8
v_lshlrev_b32 v54, 16, v55
v_and_b32 v55, 0xffff0000, v55
Mvaa 188,136,120
Ta 128,23,20544
Ta 130,23,25664
Mvaa 188,140,124
v_lshlrev_b32 v56, 16, v60
v_and_b32 v57, 0xffff0000, v60
Mvaa 180,140,108
Ta 132,23,51264
Ta 134,23,56384
Mvaa 180,136,104
W 8
v_lshlrev_b32 v58, 16, v61
v_and_b32 v59, 0xffff0000, v61
Mvaa 180,144,184
Ta 136,25,64
Ta 138,25,5184
Mvaa 180,148,188
v_lshlrev_b32 v60, 16, v62
v_and_b32 v61, 0xffff0000, v62
Mvaa 188,148,204
Ta 140,21,41024
Ta 142,21,46144
Mvaa 188,144,200
W 8
v_lshlrev_b32 v62, 16, v63
v_and_b32 v63, 0xffff0000, v63
Mvaa 188,152,208
Ta 144,21,51264
Ta 146,21,56384
Mvaa 188,156,212
v_lshlrev_b32 v64, 16, v68
v_and_b32 v65, 0xffff0000, v68
Mvaa 180,156,196
Ta 148,23,10304
Ta 150,23,15424
Mvaa 180,152,192
W 8
v_lshlrev_b32 v66, 16, v69
v_and_b32 v67, 0xffff0000, v69
Mvaa 192,128,96
Ta 152,23,30784
Ta 154,23,35904
Mvaa 192,132,100
v_lshlrev_b32 v68, 16, v70
v_and_b32 v69, 0xffff0000, v70
Mvaa 200,132,116
Ta 156,23,41024
Ta 158,23,46144
Mvaa 200,128,112
W 8
v_lshlrev_b32 v70, 16, v71
K 176,176,177
Mvaa 200,136,120
Ta 128,24,20576
v_and_b32 v71, 0xffff0000, v71
Mvaa 200,140,124
Ta 130,24,25696
K 177,178,179
Mvaa 192,140,108
v_lshlrev_b32 v72, 16, v76
K 178,180,181
Mvaa 192,136,104
W 6
Ta 132,24,51296
v_and_b32 v73, 0xffff0000, v76
Mvaa 192,144,184
Ta 134,24,56416
v_lshlrev_b32 v74, 16, v77
Mvaa 192,148,188
K 179,182,183
v_and_b32 v75, 0xffff0000, v77
Mvaa 200,148,204
s_add_u32 s83, s28, 327680
Ta 136,26,96
Ta 138,26,5216
Mvaa 200,144,200
W 6
buffer_store_dwordx4 v[176:179], v1, s[8:11], s83 offen sc1
v_lshlrev_b32 v76, 16, v78
Mvaa 200,152,208
s_waitcnt vmcnt(1)
v_cmp_eq_u64 vcc, s[12:13], v[16:17]
s_and_b32 s71, vcc_lo, 255
s_bitset0_b32 s71, s59
K 184,184,185
K 185,186,187
Mvaa 200,156,212
s_lshl_b32 s92, s14, 3
s_lshr_b32 s92, s71, s92
s_mov_b32 s91, 134217728
Mvaa 192,156,196
K 186,188,189
K 187,190,191
s_add_u32 s83, s29, 368640
buffer_store_dwordx4 v[184:187], v1, s[8:11], s83 offen sc1
Mvaa 192,152,192
Ta 140,22,41056
Ta 142,22,46176
W 4
Mvaa 196,128,96
s_bitcmp1_b32 s71, 0
s_cselect_b32 s94, s78, s91
v_and_b32 v77, 0xffff0000, v78
Mvaa 196,132,100
v_lshlrev_b32 v78, 16, v79
)"
R"(v_and_b32 v79, 0xffff0000, v79
Mvaa 204,132,116
Ta 144,22,51296
Ta 146,22,56416
Mvaa 204,128,112
Ta 148,24,10336
Ta 150,24,15456
W 4
Mvaa 204,136,120
Ta 152,24,30816
Ta 154,24,35936
Mvaa 204,140,124
buffer_load_dwordx4 a[0:3], v1, s[8:11], s94 offen offset:0 sc1
Ta 156,24,41056
Mvaa 196,140,108
Ta 158,24,46176
Ta 128,23,20608
Mvaa 196,136,104
W 5
Ta 130,23,25728
Ta 132,23,51328
Mvaa 196,144,184
Ta 134,23,56448
Ta 136,25,128
Mvaa 196,148,188
s_bitcmp1_b32 s92, 0
s_cbranch_scc0 .LL_92%=
buffer_load_dwordx4 a[216:219], v1, s[8:11], s65 offen offset:0 sc1
.LL_92%=:
Ta 138,25,5248
Mvaa 204,148,204
s_bitcmp1_b32 s71, 1
s_cselect_b32 s94, s78, s91
Ta 140,21,41088
Mvaa 204,144,200
W 7
Ta 142,21,46208
Ta 144,21,51328
Mvaa 204,152,208
Ta 146,21,56448
Mvaa 204,156,212
Ta 148,23,10368
Ta 150,23,15488
Mvaa 196,156,196
buffer_load_dwordx4 a[4:7], v1, s[8:11], s94 offen offset:1024 sc1
Mvaa 196,152,192
W 8
Ta 152,23,30848
Ta 154,23,35968
Mvaa 208,128,96
Mvaa 208,132,100
Ta 156,23,41088
Ta 158,23,46208
Mvaa 216,132,116
K 192,192,193
Mvaa 216,128,112
W 8
Ta 128,24,20640
Ta 130,24,25760
Mvaa 216,136,120
K 193,194,195
Mvaa 216,140,124
s_bitcmp1_b32 s92, 1
s_cbranch_scc0 .LL_93%=
buffer_load_dwordx4 a[220:223], v1, s[8:11], s65 offen offset:1024 sc1
.LL_93%=:
K 194,196,197
Mvaa 208,140,108
s_bitcmp1_b32 s71, 2
s_cselect_b32 s94, s78, s91
Ta 132,24,51360
Mvaa 208,136,104
W 7
Ta 134,24,56480
K 195,198,199
Mvaa 208,144,184
s_add_u32 s83, s28, 335872
Ta 136,26,160
Ta 138,26,5280
Mvaa 208,148,188
buffer_store_dwordx4 v[192:195], v1, s[8:11], s83 offen sc1
K 200,200,201
Mvaa 216,148,204
K 201,202,203
K 202,204,205
Mvaa 216,144,200
W 6
Ta 140,22,41120
K 203,206,207
Mvaa 216,152,208
s_add_u32 s83, s29, 376832
Ta 142,22,46240
Ta 144,22,51360
Mvaa 216,156,212
buffer_store_dwordx4 v[200:203], v1, s[8:11], s83 offen sc1
Ta 146,22,56480
Mvaa 208,156,196
Ta 148,24,10400
Ta 150,24,15520
Mvaa 208,152,192
W 8
Ta 152,24,30880
Ta 154,24,36000
Mvaa 212,128,96
Mvaa 212,132,100
buffer_load_dwordx4 a[8:11], v1, s[8:11], s94 offen offset:2048 sc1
Ta 156,24,41120
Mvaa 220,132,116
Ta 158,24,46240
Mvaa 220,128,112
W 8
Ta 128,23,20672
Ta 130,23,25792
Mvaa 220,136,120
Mvaa 220,140,124
s_bitcmp1_b32 s92, 2
s_cbranch_scc0 .LL_94%=
buffer_load_dwordx4 a[224:227], v1, s[8:11], s65 offen offset:2048 sc1
.LL_94%=:
Ta 132,23,51392
Mvaa 212,140,108
s_bitcmp1_b32 s71, 3
s_cselect_b32 s94, s78, s91
Ta 134,23,56512
Mvaa 212,136,104
W 8
Ta 136,25,192
Ta 138,25,5312
Mvaa 212,144,184
Mvaa 212,148,188
Ta 140,21,41152
Ta 142,21,46272
Mvaa 220,148,204
buffer_load_dwordx4 a[12:15], v1, s[8:11], s94 offen offset:3072 sc1
Mvaa 220,144,200
W 8
Ta 144,21,51392
Ta 146,21,56512
Mvaa 220,152,208
Mvaa 220,156,212
Ta 148,23,10432
Ta 150,23,15552
Mvaa 212,156,196
Mvaa 212,152,192
W 8
Ta 152,23,30912
Ta 154,23,36032
Mvaa 224,128,96
Mvaa 224,132,100
s_bitcmp1_b32 s92, 3
s_cbranch_scc0 .LL_95%=
buffer_load_dwordx4 a[228:231], v1, s[8:11], s65 offen offset:3072 sc1
.LL_95%=:
Ta 156,23,41152
Mvaa 232,132,116
s_bitcmp1_b32 s71, 4
s_cselect_b32 s94, s78, s91
Ta 158,23,46272
Mvaa 232,128,112
W 8
Ta 128,24,20704
K 208,208,209
Mvaa 232,136,120
Ta 130,24,25824
K 209,210,211
Mvaa 232,140,124
Ta 132,24,51424
K 210,212,213
Mvaa 224,140,108
Ta 134,24,56544
K 211,214,215
Mvaa 224,136,104
W 8
Ta 136,26,224
Ta 138,26,5344
Mvaa 224,144,184
s_add_u32 s83, s28, 344064
buffer_load_dwordx4 a[16:19], v19, s[8:11], s94 offen offset:0 sc1
Mvaa 224,148,188
Ta 140,22,41184
Ta 142,22,46304
Mvaa 232,148,204
Mvaa 232,144,200
W 8
Ta 144,22,51424
Ta 146,22,56544
Mvaa 232,152,208
buffer_store_dwordx4 v[208:211], v1, s[8:11], s83 offen sc1
K 216,216,217
Mvaa 232,156,212
K 217,218,219
K 218,220,221
Mvaa 224,156,196
K 219,222,223
s_add_u32 s83, s29, 385024
buffer_store_dwordx4 v[216:219], v1, s[8:11], s83 offen sc1
Mvaa 224,152,192
W 6
Ta 148,24,10464
Ta 150,24,15584
Mvaa 228,128,96
Ta 152,24,30944
Ta 154,24,36064
Mvaa 228,132,100
Ta 156,24,41184
Ta 158,24,46304
Mvaa 236,132,116
s_bitcmp1_b32 s92, 4
s_cbranch_scc0 .LL_96%=
buffer_load_dwordx4 a[232:235], v19, s[8:11], s65 offen offset:0 sc1
.LL_96%=:
Mvaa 236,128,112
W 8
s_bitcmp1_b32 s71, 5
s_cselect_b32 s94, s78, s91
Mvaa 236,136,120
Ta 128,23,20736
Ta 130,23,25856
Mvaa 236,140,124
Ta 132,23,51456
Ta 134,23,56576
Mvaa 228,140,108
buffer_load_dwordx4 a[20:23], v19, s[8:11], s94 offen offset:1024 sc1
Mvaa 228,136,104
W 8
Ta 136,25,256
Ta 138,25,5376
Mvaa 228,144,184
Mvaa 228,148,188
Ta 140,21,41216
Ta 142,21,46336
Mvaa 236,148,204
Mvaa 236,144,200
W 8
Ta 144,21,51456
Ta 146,21,56576
Mvaa 236,152,208
Mvaa 236,156,212
s_bitcmp1_b32 s92, 5
s_cbranch_scc0 .LL_97%=
buffer_load_dwordx4 a[236:239], v19, s[8:11], s65 offen offset:1024 sc1
.LL_97%=:
Ta 148,23,10496
Mvaa 228,156,196
s_bitcmp1_b32 s71, 6
s_cselect_b32 s94, s78, s91
Ta 150,23,15616
Mvaa 228,152,192
W 8
Ta 152,23,30976
Ta 154,23,36096
Mvaa 240,128,96
Mvaa 240,132,100
Ta 156,23,41216
Ta 158,23,46336
Mvaa 248,132,116
K 224,224,225
Mvaa 248,128,112
W 8
Ta 128,24,20768
Ta 130,24,25888
Mvaa 248,136,120
buffer_load_dwordx4 a[24:27], v19, s[8:11], s94 offen offset:2048 sc1
K 225,226,227
Mvaa 248,140,124
Ta 132,24,51488
K 226,228,229
Mvaa 240,140,108
Ta 134,24,56608
K 227,230,231
Mvaa 240,136,104
W 8
Ta 136,26,288
Ta 138,26,5408
Mvaa 240,144,184
s_add_u32 s83, s28, 352256
Mvaa 240,148,188
buffer_store_dwordx4 v[224:227], v1, s[8:11], s83 offen sc1
K 232,232,233
Mvaa 248,148,204
K 233,234,235
K 234,236,237
Mvaa 248,144,200
W 6
Ta 140,22,41248
K 235,238,239
Mvaa 248,152,208
s_add_u32 s83, s29, 393216
Ta 142,22,46368
Ta 144,22,51488
Mvaa 248,156,212
buffer_store_dwordx4 v[232:235], v1, s[8:11], s83 offen sc1
Ta 146,22,56608
Mvaa 240,156,196
Ta 148,24,10528
Ta 150,24,15648
Mvaa 240,152,192
W 8
Ta 152,24,31008
Ta 154,24,36128
Mvaa 244,128,96
Mvaa 244,132,100
s_bitcmp1_b32 s92, 6
s_cbranch_scc0 .LL_98%=
buffer_load_dwordx4 a[240:243], v19, s[8:11], s65 offen offset:2048 sc1
.LL_98%=:
Ta 156,24,41248
Mvaa 252,132,116
s_bitcmp1_b32 s71, 7
s_cselect_b32 s94, s78, s91
Ta 158,24,46368
Mvaa 252,128,112
W 8
Mvaa 252,136,120
Mvaa 252,140,124
buffer_load_dwordx4 a[28:31], v19, s[8:11], s94 offen offset:3072 sc1
Mvaa 244,140,108
Mvaa 244,136,104
W 4
Mvaa 244,144,184
Mvaa 244,148,188
s_bitcmp1_b32 s92, 7
s_cbranch_scc0 .LL_99%=
buffer_load_dwordx4 a[244:247], v19, s[8:11], s65 offen offset:3072 sc1
.LL_99%=:
Mvaa 252,148,204
Mvaa 252,144,200
W 0
Mvaa 252,152,208
Mvaa 252,156,212
Mvaa 244,156,196
Mvaa 244,152,192
K 240,240,241
K 241,242,243
s_nop 5
K 242,244,245
K 243,246,247
s_add_u32 s83, s28, 360448
buffer_store_dwordx4 v[240:243], v1, s[8:11], s83 offen sc1
K 248,248,249
K 249,250,251
K 250,252,253
K 251,254,255
s_add_u32 s83, s29, 401408
buffer_store_dwordx4 v[248:251], v1, s[8:11], s83 offen sc1
s_branch .Ltend_83%=
.p2align 12
.Llate_82%=:
ds_read_b32 v11, v11 offset:59392
ds_read_b64 v[8:9], v10 offset:57344
Rv 176,3,0
Rv 180,3,1024
Rv 184,3,2048
Rv 188,3,3072
Rv 192,3,4096
Rv 196,3,5120
Rv 200,3,6144
Rv 204,3,7168
W 6
Mvvv 32,224,176
s_lshl_b32 s92, s58, 2
s_add_u32 s91, s92, s51
s_add_u32 s95, s91, 7
Mvvv 36,228,176
s_and_b32 s95, s95, 15
s_mul_i32 s95, s95, 40960
s_mul_i32 s71, s51, 20480
Mvvv 40,224,180
s_add_u32 s71, s71, 61440
s_add_u32 m0, s71, s48
s_lshl2_add_u32 s94, s48, s95
Mvvv 44,228,180
W 4
v_cmp_ne_u32 vcc, 0, v11
s_cmp_lg_u64 vcc, 0
s_cbranch_scc1 .Lredo%=
A 4,4,8
Mvvv 48,224,184
v_rcp_f32 v4, v4
Mvvv 52,228,184
Ta 128,25,10240
A 5,5,9
Mvvv 56,224,188
v_rcp_f32 v5, v5
Mvvv 60,228,188
W 3
Ta 130,25,15360
D 16,40,94,0
Mvvv 64,224,192
s_add_u32 s95, s91, 9
s_and_b32 s95, s95, 15
s_mul_i32 s95, s95, 40960
Mvvv 68,228,192
s_mul_i32 s71, s51, 10240
s_add_u32 s71, s71, 112640
s_lshl_b32 s93, s58, 12
Mvvv 72,224,196
s_add_u32 s93, s93, s50
U 32,32,4
U 33,33,4
)"
R"(Mvvv 76,228,196
Ta 132,25,20480
Ta 134,25,25600
W 4
Mvvv 80,224,200
U 34,34,4
U 35,35,4
Mvvv 84,228,200
U 36,36,4
U 37,37,4
Mvvv 88,224,204
U 38,38,4
U 39,39,4
K 32,32,33
K 33,34,35
K 34,36,37
K 35,38,39
buffer_store_dwordx4 v[32:35], v1, s[8:11], s93 offen offset:0 sc1
U 40,40,4
U 41,41,4
U 42,42,4
U 43,43,4
U 44,44,4
U 45,45,4
U 46,46,4
U 47,47,4
K 40,40,41
K 41,42,43
K 42,44,45
K 43,46,47
buffer_store_dwordx4 v[40:43], v1, s[8:11], s93 offen offset:1024 sc1
U 64,64,5
U 65,65,5
U 66,66,5
U 67,67,5
U 68,68,5
U 69,69,5
U 70,70,5
U 71,71,5
K 64,64,65
K 65,66,67
K 66,68,69
K 67,70,71
s_add_u32 s67, s93, 131072
buffer_store_dwordx4 v[64:67], v1, s[8:11], s67 offen offset:0 sc1
U 72,72,5
U 73,73,5
U 74,74,5
U 75,75,5
U 76,76,5
U 77,77,5
U 78,78,5
U 79,79,5
K 72,72,73
K 73,74,75
K 74,76,77
K 75,78,79
Mvvv 92,228,204
Ta 136,26,10272
Ta 138,26,15392
Ta 140,26,20512
Ta 142,26,25632
W 4
Zvav 176,128,32
Ta 144,25,10304
Ta 146,25,15424
Mvav 176,132,40
Ta 148,25,20544
Ta 150,25,25664
Zvav 184,132,72
D 17,40,94,1024
Ta 152,26,10336
Mvav 184,128,64
W 5
Ta 154,26,15456
Ta 156,26,20576
Zvav 188,136,64
Ta 158,26,25696
Ta 128,25,10368
Mvav 188,140,72
Ta 130,25,15488
Ta 132,25,20608
Zvav 180,140,40
D 18,40,94,2048
Ta 134,25,25728
Mvav 180,136,32
W 8
Ta 136,26,10400
Ta 138,26,15520
Zvav 192,144,32
Mvav 192,148,40
Ta 140,26,20640
Ta 142,26,25760
Zvav 200,148,72
D 19,40,94,3072
Mvav 200,144,64
W 8
s_add_u32 m0, m0, 4096
Ta 144,25,10432
Zvav 204,152,64
Ta 146,25,15552
Mvav 204,156,72
Ta 148,25,20672
Ta 150,25,25792
Zvav 196,156,40
buffer_load_dwordx4 v20, s[40:43], s94 offen lds
Mvav 196,152,32
W 8
s_add_u32 m0, s71, s48
s_lshl2_add_u32 s94, s48, s95
Zvav 208,128,32
Ta 152,26,10464
Ta 154,26,15584
Mvav 208,132,40
Ta 156,26,20704
Ta 158,26,25824
Zvav 216,132,72
D 16,40,94,0
v_bfe_u32 v28, v1, 4, 2
Mvav 216,128,64
W 8
Ta 128,25,10496
v_lshlrev_b32 v28, 3, v28
Zvav 220,136,64
Ta 130,25,15616
Mvav 220,140,72
Ta 132,25,20736
Ta 134,25,25856
Zvav 212,140,40
buffer_load_dwordx2 v[28:29], v28, s[8:11], s54 offen sc1
Mvav 212,136,32
W 8
Ta 136,26,10528
Ta 138,26,15648
Zvav 224,144,32
Mvav 224,148,40
Ta 140,26,20768
Ta 142,26,25888
Zvav 232,148,72
D 17,40,94,1024
Mvav 232,144,64
W 8
Ta 144,25,30720
Ta 146,25,35840
Zvav 236,152,64
Mvav 236,156,72
Ta 148,21,0
Ta 150,21,5120
Zvav 228,156,40
buffer_store_dwordx4 v[72:75], v1, s[8:11], s67 offen offset:1024 sc1
U 48,48,4
Mvav 228,152,32
W 8
U 49,49,4
U 50,50,4
Zvav 240,128,32
U 51,51,4
U 52,52,4
Mvav 240,132,40
U 53,53,4
U 54,54,4
Zvav 248,132,72
D 18,40,94,2048
U 55,55,4
Mvav 248,128,64
W 4
K 48,48,49
K 49,50,51
Zvav 252,136,64
K 50,52,53
K 51,54,55
Mvav 252,140,72
Ta 152,26,30752
Ta 154,26,35872
Zvav 244,140,40
buffer_store_dwordx4 v[48:51], v1, s[8:11], s93 offen offset:2048 sc1
U 56,56,4
U 57,57,4
U 58,58,4
U 59,59,4
U 60,60,4
U 61,61,4
U 62,62,4
U 63,63,4
K 56,56,57
K 57,58,59
K 58,60,61
K 59,62,63
buffer_store_dwordx4 v[56:59], v1, s[8:11], s93 offen offset:3072 sc1
U 80,80,5
U 81,81,5
U 82,82,5
U 83,83,5
U 84,84,5
U 85,85,5
U 86,86,5
U 87,87,5
K 80,80,81
K 81,82,83
K 82,84,85
K 83,86,87
buffer_store_dwordx4 v[80:83], v1, s[8:11], s67 offen offset:2048 sc1
U 88,88,5
U 89,89,5
U 90,90,5
U 91,91,5
U 92,92,5
U 93,93,5
U 94,94,5
U 95,95,5
K 88,88,89
K 89,90,91
K 90,92,93
K 91,94,95
Mvav 244,136,32
Ta 156,22,32
Ta 158,22,5152
W 4
Mvav 176,144,48
Ta 128,25,30784
Ta 130,25,35904
Mvav 176,148,56
Ta 132,21,64
Ta 134,21,5184
Mvav 184,148,88
D 19,40,94,3072
Ta 136,26,30816
Mvav 184,144,80
s_add_u32 m0, m0, 4096
s_nop 0
buffer_load_dwordx4 v20, s[40:43], s94 offen lds
s_waitcnt vmcnt(8)
v_cmp_eq_u64 vcc, s[12:13], v[28:29]
s_cmp_eq_u64 vcc, -1
s_cbranch_scc0 .Lspin_100%=
.Lok_101%=:
W 5
Mvav 188,152,80
s_add_u32 s81, s58, 0
s_and_b32 s81, s81, 3
s_lshl_b32 s81, s81, 12
Mvav 188,156,88
s_add_u32 s81, s81, s49
Ta 138,26,35936
Ta 140,22,96
Mvav 180,156,56
s_lshl_b32 s66, s14, 1
Ta 142,22,5216
Ta 144,25,30848
Mvav 180,152,48
W 5
buffer_store_dwordx4 v[88:91], v1, s[8:11], s67 offen offset:3072 sc1
Ta 146,25,35968
Mvav 192,128,48
s_add_u32 s66, s66, 0
Ta 148,21,128
Ta 150,21,5248
Mvav 192,132,56
s_cmp_eq_u32 s66, s59
s_cselect_b32 s66, 134217728, 0
Ta 152,26,30880
Ta 154,26,36000
Mvav 200,132,88
s_add_u32 s28, s52, s66
Ta 156,22,160
Ta 158,22,5280
Mvav 200,128,80
W 8
s_lshl_b32 s66, s14, 1
buffer_load_dwordx4 a[184:187], v1, s[8:11], s81 offen offset:0 sc1
Mvav 204,136,80
s_add_u32 s82, s81, 131072
Ta 128,25,30912
Ta 130,25,36032
Mvav 204,140,88
s_add_u32 s66, s66, 1
s_cmp_eq_u32 s66, s59
s_cselect_b32 s66, 134217728, 0
Ta 132,21,192
Mvav 196,140,56
s_add_u32 s29, s52, s66
Ta 134,21,5312
Mvav 196,136,48
W 8
buffer_load_dwordx4 a[200:203], v1, s[8:11], s82 offen offset:0 sc1
Ta 136,26,30944
Mvav 208,144,48
Ta 138,26,36064
Mvav 208,148,56
Ta 140,22,224
Ta 142,22,5344
Mvav 216,148,88
Mvav 216,144,80
W 8
buffer_load_dwordx4 a[188:191], v1, s[8:11], s81 offen offset:1024 sc1
Ta 144,25,30976
Mvav 220,152,80
Ta 146,25,36096
Mvav 220,156,88
Ta 148,21,256
Ta 150,21,5376
Mvav 212,156,56
Mvav 212,152,48
W 8
buffer_load_dwordx4 a[204:207], v1, s[8:11], s82 offen offset:1024 sc1
Ta 152,26,31008
Mvav 224,128,48
Ta 154,26,36128
Mvav 224,132,56
Ta 156,22,288
Ta 158,22,5408
Mvav 232,132,88
Mvav 232,128,80
W 8
buffer_load_dwordx4 a[192:195], v1, s[8:11], s81 offen offset:2048 sc1
Ta 128,25,10240
Mvav 236,136,80
Ta 130,25,15360
Mvav 236,140,88
Ta 132,25,20480
Ta 134,25,25600
Mvav 228,140,56
Mvav 228,136,48
W 8
buffer_load_dwordx4 a[208:211], v1, s[8:11], s82 offen offset:2048 sc1
Ta 136,26,10272
Mvav 240,144,48
Ta 138,26,15392
Mvav 240,148,56
Ta 140,26,20512
Ta 142,26,25632
Mvav 248,148,88
Mvav 248,144,80
W 8
buffer_load_dwordx4 a[196:199], v1, s[8:11], s81 offen offset:3072 sc1
Ta 144,25,10304
Mvav 252,152,80
Ta 146,25,15424
Mvav 252,156,88
Ta 148,25,20544
Ta 150,25,25664
Mvav 244,156,56
Mvav 244,152,48
s_waitcnt vmcnt(3) lgkmcnt(8)
buffer_load_dwordx4 a[212:215], v1, s[8:11], s82 offen offset:3072 sc1
Ta 152,26,10336
Zvaa 96,128,184
Ta 154,26,15456
Mvaa 96,132,188
Ta 156,26,20576
Ta 158,26,25696
Zvaa 104,132,204
Mvaa 104,128,200
W 8
Ta 128,25,10368
Ta 130,25,15488
Zvaa 108,136,200
Mvaa 108,140,204
Ta 132,25,20608
Ta 134,25,25728
Zvaa 100,140,188
s_lshl_b32 s92, s58, 2
s_add_u32 s91, s92, s51
s_add_u32 s95, s91, 11
Mvaa 100,136,184
W 8
s_and_b32 s95, s95, 15
s_mul_i32 s95, s95, 40960
Zvaa 112,144,184
s_mul_i32 s71, s51, 10240
s_add_u32 s71, s71, 40960
s_add_u32 m0, s71, s48
Mvaa 112,148,188
s_lshl2_add_u32 s94, s48, s95
Ta 136,26,10400
Ta 138,26,15520
Zvaa 120,148,204
Ta 140,26,20640
Ta 142,26,25760
Mvaa 120,144,200
W 8
Ta 144,25,10432
Ta 146,25,15552
Zvaa 124,152,200
Mvaa 124,156,204
Ta 148,25,20672
Ta 150,25,25792
Zvaa 116,156,188
Mvaa 116,152,184
W 8
Ta 152,26,10464
Ta 154,26,15584
Zvaa 128,128,184
s_lshl_b32 s80, s58, 3
s_add_u32 s80, s80, s55
v_mov_b64 v[28:29], s[12:13]
s_mov_b64 exec, 1
buffer_store_dwordx2 v[28:29], off, s[8:11], s80 sc1
s_mov_b64 exec, -1
Mvaa 128,132,188
)"
R"(Ta 156,26,20704
Ta 158,26,25824
Zvaa 136,132,204
Mvaa 136,128,200
W 8
Ta 128,25,10496
Ta 130,25,15616
Zvaa 140,136,200
s_barrier
D 16,40,94,0
Mvaa 140,140,204
s_add_u32 s95, s91, 13
s_and_b32 s95, s95, 15
s_mul_i32 s95, s95, 40960
Zvaa 132,140,188
s_mul_i32 s71, s51, 20480
s_add_u32 s71, s71, 71680
Ta 132,25,20736
Mvaa 132,136,184
W 7
Ta 134,25,25856
Ta 136,26,10528
Zvaa 144,144,184
Ta 138,26,15648
D 17,40,94,1024
Mvaa 144,148,188
Ta 140,26,20768
Ta 142,26,25888
Zvaa 152,148,204
Mvaa 152,144,200
W 8
Ta 144,25,30720
Ta 146,25,35840
Zvaa 156,152,200
D 18,40,94,2048
Mvaa 156,156,204
Ta 148,21,0
Ta 150,21,5120
Zvaa 148,156,188
Mvaa 148,152,184
W 8
Ta 152,26,30752
Ta 154,26,35872
Zvaa 160,128,184
D 19,40,94,3072
Mvaa 160,132,188
s_add_u32 m0, m0, 4096
Ta 156,22,32
Ta 158,22,5152
Zvaa 168,132,204
Mvaa 168,128,200
W 8
Ta 128,25,30784
Ta 130,25,35904
Zvaa 172,136,200
buffer_load_dwordx4 v20, s[40:43], s94 offen lds
Mvaa 172,140,204
s_add_u32 s81, s58, 1
s_and_b32 s81, s81, 3
s_lshl_b32 s81, s81, 12
Zvaa 164,140,188
s_add_u32 s81, s81, s49
Ta 132,21,64
Ta 134,21,5184
Mvaa 164,136,184
s_waitcnt vmcnt(6) lgkmcnt(8)
Ta 136,26,30816
Ta 138,26,35936
Mvaa 96,144,192
buffer_load_dwordx4 a[216:219], v1, s[8:11], s81 offen offset:0 sc1
Mvaa 96,148,196
s_add_u32 s82, s81, 131072
Ta 140,22,96
Ta 142,22,5216
Mvaa 104,148,212
Mvaa 104,144,208
W 8
Ta 144,25,30848
Ta 146,25,35968
Mvaa 108,152,208
buffer_load_dwordx4 a[232:235], v1, s[8:11], s82 offen offset:0 sc1
Mvaa 108,156,212
Ta 148,21,128
Ta 150,21,5248
Mvaa 100,156,196
Mvaa 100,152,192
W 8
Ta 152,26,30880
Ta 154,26,36000
Mvaa 112,128,192
buffer_load_dwordx4 a[220:223], v1, s[8:11], s81 offen offset:1024 sc1
Mvaa 112,132,196
Ta 156,22,160
Ta 158,22,5280
Mvaa 120,132,212
Mvaa 120,128,208
W 8
Ta 128,25,30912
Ta 130,25,36032
Mvaa 124,136,208
buffer_load_dwordx4 a[236:239], v1, s[8:11], s82 offen offset:1024 sc1
Mvaa 124,140,212
Ta 132,21,192
Ta 134,21,5312
Mvaa 116,140,196
Mvaa 116,136,192
W 8
Ta 136,26,30944
Ta 138,26,36064
Mvaa 128,144,192
buffer_load_dwordx4 a[224:227], v1, s[8:11], s81 offen offset:2048 sc1
Mvaa 128,148,196
Ta 140,22,224
Ta 142,22,5344
Mvaa 136,148,212
Mvaa 136,144,208
W 8
Ta 144,25,30976
Ta 146,25,36096
Mvaa 140,152,208
buffer_load_dwordx4 a[240:243], v1, s[8:11], s82 offen offset:2048 sc1
Mvaa 140,156,212
Ta 148,21,256
Ta 150,21,5376
Mvaa 132,156,196
Mvaa 132,152,192
W 8
Ta 152,26,31008
Ta 154,26,36128
Mvaa 144,128,192
buffer_load_dwordx4 a[228:231], v1, s[8:11], s81 offen offset:3072 sc1
Mvaa 144,132,196
Ta 156,22,288
Ta 158,22,5408
Mvaa 152,132,212
Mvaa 152,128,208
W 8
Ta 128,21,10240
Ta 130,21,15360
Mvaa 156,136,208
buffer_load_dwordx4 a[244:247], v1, s[8:11], s82 offen offset:3072 sc1
Mvaa 156,140,212
s_add_u32 m0, s71, s48
s_lshl2_add_u32 s94, s48, s95
Ta 132,21,20480
Mvaa 148,140,196
Ta 134,21,25600
Mvaa 148,136,192
W 8
Ta 136,22,10272
Ta 138,22,15392
Mvaa 160,144,192
D 16,40,94,0
Mvaa 160,148,196
s_add_u32 s95, s92, 15
s_and_b32 s95, s95, 15
s_mul_i32 s95, s95, 40960
Mvaa 168,148,212
s_mul_i32 s71, s51, 0
s_add_u32 s71, s71, 102400
Ta 140,22,20512
Mvaa 168,144,208
W 7
Ta 142,22,25632
Ta 144,21,10304
Mvaa 172,152,208
D 17,40,94,1024
Ta 146,21,15424
Mvaa 172,156,212
Ta 148,21,20544
Ta 150,21,25664
Mvaa 164,156,196
Mvaa 164,152,192
s_waitcnt vmcnt(6) lgkmcnt(8)
Ta 152,22,10336
Ta 154,22,15456
Mvaa 96,128,216
D 18,40,94,2048
Mvaa 96,132,220
Ta 156,22,20576
Ta 158,22,25696
Mvaa 104,132,236
Mvaa 104,128,232
W 8
Ta 128,21,10368
Ta 130,21,15488
Mvaa 108,136,232
D 19,40,94,3072
Mvaa 108,140,236
s_add_u32 m0, m0, 4096
Ta 132,21,20608
Ta 134,21,25728
Mvaa 100,140,220
Mvaa 100,136,216
W 8
Ta 136,22,10400
Ta 138,22,15520
Mvaa 112,144,216
buffer_load_dwordx4 v20, s[40:43], s94 offen lds
Mvaa 112,148,220
s_add_u32 m0, s71, s48
s_lshl2_add_u32 s94, s48, s95
Ta 140,22,20640
Mvaa 120,148,236
Ta 142,22,25760
Mvaa 120,144,232
W 8
Ta 144,21,10432
Ta 146,21,15552
Mvaa 124,152,232
D 16,40,94,0
Mvaa 124,156,236
Ta 148,21,20672
Ta 150,21,25792
Mvaa 116,156,220
D 17,40,94,1024
D 18,40,94,2048
D 19,40,94,3072
s_add_u32 m0, m0, 4096
s_nop 0
buffer_load_dwordx4 v20, s[40:43], s94 offen lds
Mvaa 116,152,216
W 8
s_add_u32 s81, s58, 2
s_and_b32 s81, s81, 3
Mvaa 128,128,216
s_lshl_b32 s81, s81, 12
s_add_u32 s81, s81, s49
Ta 152,22,10464
Mvaa 128,132,220
Ta 154,22,15584
Ta 156,22,20704
Mvaa 136,132,236
Ta 158,22,25824
Mvaa 136,128,232
W 8
Ta 128,21,10496
Ta 130,21,15616
Mvaa 140,136,232
s_barrier
buffer_load_dwordx4 a[0:3], v1, s[8:11], s81 offen offset:0 sc1
v_and_b32 v27, 1, v12
Mvaa 140,140,236
s_add_u32 s82, s81, 131072
Ta 132,21,20736
Ta 134,21,25856
Mvaa 132,140,220
Mvaa 132,136,216
W 8
Ta 136,22,10528
v_lshlrev_b32 v27, 1, v27
Mvaa 144,144,216
Ta 138,22,15648
buffer_load_dwordx4 a[16:19], v1, s[8:11], s82 offen offset:0 sc1
Mvaa 144,148,220
Ta 140,22,20768
v_xor_b32 v27, v27, v13
Mvaa 152,148,236
Ta 142,22,25888
Mvaa 152,144,232
W 8
Ta 144,21,30720
v_and_b32 v12, 2, v12
Mvaa 156,152,232
Ta 146,21,35840
buffer_load_dwordx4 a[4:7], v1, s[8:11], s81 offen offset:1024 sc1
Mvaa 156,156,236
Ta 148,23,0
Ta 150,23,5120
Mvaa 148,156,220
v_or_b32 v12, v12, v14
Mvaa 148,152,216
W 8
Ta 152,22,30752
Ta 154,22,35872
Mvaa 160,128,216
buffer_load_dwordx4 a[20:23], v1, s[8:11], s82 offen offset:1024 sc1
v_lshl_or_b32 v12, v27, 2, v12
Mvaa 160,132,220
Ta 156,24,32
Ta 158,24,5152
Mvaa 168,132,236
v_cmp_eq_u32_e64 s[86:87], 0, v12
Mvaa 168,128,232
W 8
Ta 128,21,30784
Ta 130,21,35904
Mvaa 172,136,232
buffer_load_dwordx4 a[8:11], v1, s[8:11], s81 offen offset:2048 sc1
Mvaa 172,140,236
Ta 132,23,64
v_cndmask_b32_e64 v4, 0, v15, s[86:87]
Mvaa 164,140,220
Ta 134,23,5184
Mvaa 164,136,216
s_waitcnt vmcnt(15) lgkmcnt(8)
Ta 136,22,30816
v_cmp_eq_u32_e64 s[86:87], 1, v12
Mvaa 96,144,224
Ta 138,22,35936
buffer_load_dwordx4 a[24:27], v1, s[8:11], s82 offen offset:2048 sc1
Mvaa 96,148,228
Ta 140,24,96
v_cndmask_b32_e64 v5, 0, v15, s[86:87]
Mvaa 104,148,244
Ta 142,24,5216
Mvaa 104,144,240
W 8
Ta 144,21,30848
Ta 146,21,35968
Mvaa 108,152,240
buffer_load_dwordx4 a[12:15], v1, s[8:11], s81 offen offset:3072 sc1
v_cmp_eq_u32_e64 s[86:87], 2, v12
Mvaa 108,156,244
Ta 148,23,128
Ta 150,23,5248
Mvaa 100,156,228
v_cndmask_b32_e64 v6, 0, v15, s[86:87]
Mvaa 100,152,224
W 8
Ta 152,22,30880
Ta 154,22,36000
Mvaa 112,128,224
buffer_load_dwordx4 a[28:31], v1, s[8:11], s82 offen offset:3072 sc1
v_cmp_eq_u32_e64 s[86:87], 3, v12
Mvaa 112,132,228
s_add_u32 s81, s58, 3
s_and_b32 s81, s81, 3
s_lshl_b32 s81, s81, 12
Mvaa 120,132,244
s_add_u32 s81, s81, s49
Ta 156,24,160
Ta 158,24,5280
Mvaa 120,128,240
W 8
Ta 128,21,30912
v_cndmask_b32_e64 v7, 0, v15, s[86:87]
Mvaa 124,136,240
Ta 130,21,36032
buffer_load_dwordx4 a[32:35], v1, s[8:11], s81 offen offset:0 sc1
Mvaa 124,140,244
s_add_u32 s82, s81, 131072
Ta 132,23,192
v_cmp_eq_u32_e64 s[86:87], 4, v12
Mvaa 116,140,228
Ta 134,23,5312
Mvaa 116,136,224
W 8
Ta 136,22,30944
v_cndmask_b32_e64 v8, 0, v15, s[86:87]
Mvaa 128,144,224
Ta 138,22,36064
buffer_load_dwordx4 a[48:51], v1, s[8:11], s82 offen offset:0 sc1
Mvaa 128,148,228
Ta 140,24,224
Ta 142,24,5344
Mvaa 136,148,244
v_cmp_eq_u32_e64 s[86:87], 5, v12
Mvaa 136,144,240
W 8
Ta 144,21,30976
Ta 146,21,36096
Mvaa 140,152,240
s_waitcnt vmcnt(10)
s_barrier
v_cndmask_b32_e64 v9, 0, v15, s[86:87]
Mvaa 140,156,244
buffer_load_dwordx4 a[36:39], v1, s[8:11], s81 offen offset:1024 sc1
Ta 148,23,256
Mvaa 132,156,228
Ta 150,23,5376
v_cmp_eq_u32_e64 s[86:87], 6, v12
Mvaa 132,152,224
W 8
Ta 152,22,31008
Ta 154,22,36128
Mvaa 144,128,224
Mvaa 144,132,228
buffer_load_dwordx4 a[52:55], v1, s[8:11], s82 offen offset:1024 sc1
v_cndmask_b32_e64 v10, 0, v15, s[86:87]
Mvaa 152,132,244
Ta 156,24,288
Ta 158,24,5408
Mvaa 152,128,240
W 8
Ta 128,23,20480
v_cmp_eq_u32_e64 s[86:87], 7, v12
Mvaa 156,136,240
Ta 130,23,25600
Mvaa 156,140,244
buffer_load_dwordx4 a[40:43], v1, s[8:11], s81 offen offset:2048 sc1
v_cndmask_b32_e64 v11, 0, v15, s[86:87]
Mvaa 148,140,228
Ta 132,23,51200
Ta 134,23,56320
Mvaa 148,136,224
W 8
Ta 136,24,20512
Ta 138,24,25632
Mvaa 160,144,224
)"
R"(s_mov_b32 s20, s38
Mvaa 160,148,228
buffer_load_dwordx4 a[56:59], v1, s[8:11], s82 offen offset:2048 sc1
Ta 140,24,51232
Mvaa 168,148,244
s_and_b32 s21, s39, 0xffff
Ta 142,24,56352
Mvaa 168,144,240
W 8
Ta 144,23,20544
Ta 146,23,25664
Mvaa 172,152,240
s_mov_b32 s22, 2621440
Mvaa 172,156,244
buffer_load_dwordx4 a[44:47], v1, s[8:11], s81 offen offset:3072 sc1
Ta 148,23,51264
Mvaa 164,156,228
Ta 150,23,56384
Mvaa 164,152,224
s_waitcnt vmcnt(11) lgkmcnt(8)
s_mov_b32 s23, 0x20000
Ta 152,24,20576
Mvaa 96,128,0
Ta 154,24,25696
Mvaa 96,132,4
s_and_b32 s17, s17, 0xffff
buffer_load_dwordx4 a[60:63], v1, s[8:11], s82 offen offset:3072 sc1
Ta 156,24,51296
Mvaa 104,132,20
Ta 158,24,56416
Mvaa 104,128,16
W 8
s_mov_b32 s18, 2621440
Ta 128,23,20608
Mvaa 108,136,16
Ta 130,23,25728
Mvaa 108,140,20
Ta 132,23,51328
Ta 134,23,56448
Mvaa 100,140,4
s_mov_b32 s19, 0x20000
Mvaa 100,136,0
W 8
Ta 136,24,20640
Ta 138,24,25760
Mvaa 112,144,0
s_lshr_b32 s66, s15, 3
v_bfe_u32 v28, v1, 4, 2
Mvaa 112,148,4
Ta 140,24,51360
v_lshlrev_b32 v28, 3, v28
Mvaa 120,148,20
s_lshl_b32 s66, s66, 4
Ta 142,24,56480
buffer_load_dwordx2 v[28:29], v28, s[8:11], s55 offen sc1
Mvaa 120,144,16
W 8
Ta 144,23,20672
Ta 146,23,25792
Mvaa 124,152,16
Mvaa 124,156,20
s_add_u32 s66, s66, s59
Ta 148,23,51392
Ta 150,23,56512
Mvaa 116,156,4
Mvaa 116,152,0
W 8
s_lshl_b32 s63, s66, 6
Ta 152,24,20704
Mvaa 128,128,0
Ta 154,24,25824
Mvaa 128,132,4
s_add_u32 s63, s63, 65536
Ta 156,24,51424
Ta 158,24,56544
Mvaa 136,132,20
Mvaa 136,128,16
W 8
s_mul_i32 s62, s66, 40960
Ta 128,23,20736
Mvaa 140,136,16
Ta 130,23,25856
Mvaa 140,140,20
Ta 132,23,51456
Ta 134,23,56576
Mvaa 132,140,4
s_add_u32 s62, s62, 56623104
Mvaa 132,136,0
W 8
Ta 136,24,20768
Ta 138,24,25888
Mvaa 144,144,0
s_lshl_b32 s66, s14, 13
Mvaa 144,148,4
Ta 140,24,51488
Ta 142,24,56608
Mvaa 152,148,20
s_add_u32 s78, s62, s66
Mvaa 152,144,16
W 8
Ta 144,25,0
Ta 146,25,5120
Mvaa 156,152,16
Mvaa 156,156,20
s_add_u32 s79, s78, 327680
Ta 148,21,40960
Ta 150,21,46080
Mvaa 148,156,4
Mvaa 148,152,0
W 8
s_and_b32 s66, s14, 1
Ta 152,26,32
Mvaa 160,128,0
Ta 154,26,5152
Mvaa 160,132,4
s_mul_i32 s66, s66, 327680
Ta 156,22,40992
Ta 158,22,46112
Mvaa 168,132,20
Mvaa 168,128,16
W 8
Ta 128,25,64
Ta 130,25,5184
Mvaa 172,136,16
s_add_u32 s65, s62, s66
Mvaa 172,140,20
Ta 132,21,41024
Ta 134,21,46144
Mvaa 164,140,4
s_add_u32 s65, s65, 32768
Mvaa 164,136,0
s_waitcnt vmcnt(9) lgkmcnt(8)
Ta 136,26,96
Ta 138,26,5216
Mvaa 96,144,8
s_mov_b32 s64, 0
Mvaa 96,148,12
Ta 140,22,41056
Ta 142,22,46176
Mvaa 104,148,28
Mvaa 104,144,24
W 8
Ta 144,25,128
v_add_u32 v19, 4096, v1
Mvaa 108,152,24
Ta 146,25,5248
Mvaa 108,156,28
Ta 148,21,41088
v_bfe_u32 v27, v1, 4, 3
Mvaa 100,156,12
Ta 150,21,46208
Mvaa 100,152,8
W 8
Ta 152,26,160
v_lshlrev_b32 v27, 3, v27
Mvaa 112,128,8
Ta 154,26,5280
Mvaa 112,132,12
Ta 156,22,41120
Ta 158,22,46240
Mvaa 120,132,28
s_waitcnt vmcnt(0)
v_cmp_eq_u64 vcc, s[12:13], v[28:29]
s_cmp_eq_u64 vcc, -1
s_cbranch_scc0 .Lspin_103%=
.Lok_104%=:
s_add_u32 s81, s58, 1
v_bfe_u32 v18, v1, 7, 1
Mvaa 120,128,24
W 8
s_and_b32 s81, s81, 3
s_lshl_b32 s81, s81, 12
Mvaa 124,136,24
s_add_u32 s81, s81, s50
buffer_load_dwordx4 a[64:67], v1, s[8:11], s81 offen offset:0 sc1
Ta 128,25,192
Mvaa 124,140,28
s_add_u32 s82, s81, 131072
s_add_u32 s66, s14, 1
Ta 130,25,5312
Mvaa 116,140,12
s_lshr_b32 s66, s66, 2
Ta 132,21,41152
Ta 134,21,46272
Mvaa 116,136,8
W 8
Ta 136,26,224
Ta 138,26,5344
Mvaa 128,144,8
buffer_load_dwordx4 a[80:83], v1, s[8:11], s82 offen offset:0 sc1
Mvaa 128,148,12
s_mul_i32 s66, s66, 0
Ta 140,22,41184
Ta 142,22,46304
Mvaa 136,148,28
Mvaa 136,144,24
W 8
s_sub_u32 s66, 512, s66
Ta 144,25,256
Mvaa 140,152,24
Ta 146,25,5376
Mvaa 140,156,28
buffer_load_dwordx4 a[68:71], v1, s[8:11], s81 offen offset:1024 sc1
v_mul_u32_u24 v18, s66, v18
Mvaa 132,156,12
Ta 148,21,41216
Ta 150,21,46336
Mvaa 132,152,8
W 8
Ta 152,26,288
Ta 154,26,5408
Mvaa 144,128,8
v_add_u32 v18, v18, v27
Mvaa 144,132,12
Ta 156,22,41248
Ta 158,22,46368
Mvaa 152,132,28
s_mov_b32 s71, 0
buffer_load_dwordx4 a[84:87], v1, s[8:11], s82 offen offset:1024 sc1
Mvaa 152,128,24
W 8
Ta 128,21,51200
Ta 130,21,56320
Mvaa 156,136,24
v_bfe_u32 v20, v1, 4, 4
Mvaa 156,140,28
Ta 132,23,10240
Ta 134,23,15360
Mvaa 148,140,12
Mvaa 148,136,8
W 8
buffer_load_dwordx4 a[72:75], v1, s[8:11], s81 offen offset:2048 sc1
v_mul_u32_u24 v20, 1280, v20
Mvaa 160,144,8
Ta 136,23,30720
Ta 138,23,35840
Mvaa 160,148,12
Ta 140,23,40960
v_lshrrev_b32 v12, 8, v1
Mvaa 168,148,28
Ta 142,23,46080
Mvaa 168,144,24
W 8
Ta 144,22,51232
v_lshl_add_u32 v20, v12, 4, v20
Mvaa 172,152,24
Ta 146,22,56352
buffer_load_dwordx4 a[88:91], v1, s[8:11], s82 offen offset:2048 sc1
Mvaa 172,156,28
Ta 148,24,10272
Ta 150,24,15392
Mvaa 164,156,12
s_bfe_u32 s66, s59, 0x10001
Mvaa 164,152,8
W 8
Ta 152,24,30752
Ta 154,24,35872
Mvaa 96,128,32
s_lshl_b32 s67, s57, 1
Mvaa 96,132,36
buffer_load_dwordx4 a[76:79], v1, s[8:11], s81 offen offset:3072 sc1
Ta 156,24,40992
Mvaa 104,132,52
s_add_u32 s66, s66, s67
Ta 158,24,46112
Mvaa 104,128,48
W 8
Ta 128,21,51264
Ta 130,21,56384
Mvaa 104,136,56
Mvaa 104,140,60
s_lshl_b32 s61, s66, 2
Ta 132,23,10304
Ta 134,23,15424
Mvaa 96,140,44
buffer_load_dwordx4 a[92:95], v1, s[8:11], s82 offen offset:3072 sc1
Mvaa 96,136,40
W 8
s_add_u32 s81, s58, 2
s_and_b32 s81, s81, 3
Mvaa 100,144,32
s_lshl_b32 s81, s81, 12
s_add_u32 s81, s81, s50
s_lshr_b32 s66, s59, 2
Mvaa 100,148,36
s_lshl_b32 s66, s66, 1
Ta 136,23,30784
Ta 138,23,35904
Mvaa 108,148,52
Ta 140,23,41024
Ta 142,23,46144
Mvaa 108,144,48
W 8
buffer_load_dwordx4 a[96:99], v1, s[8:11], s81 offen offset:0 sc1
Ta 144,22,51296
Mvaa 108,152,56
s_add_u32 s82, s81, 131072
s_add_u32 s61, s61, s66
Ta 146,22,56416
Mvaa 108,156,60
Ta 148,24,10336
Ta 150,24,15456
Mvaa 100,156,44
s_and_b32 s66, s59, 1
Mvaa 100,152,40
W 8
Ta 152,24,30816
Ta 154,24,35936
Mvaa 112,128,32
s_add_u32 s61, s61, s66
buffer_load_dwordx4 a[112:115], v1, s[8:11], s82 offen offset:0 sc1
Mvaa 112,132,36
Ta 156,24,41056
Ta 158,24,46176
Mvaa 120,132,52
s_xor_b32 s67, s61, 0
K 96,96,97
K 97,98,99
Mvaa 120,128,48
W 8
Ta 128,21,51328
Ta 130,21,56448
Mvaa 120,136,56
K 98,100,101
Mvaa 120,140,60
s_mul_i32 s72, s67, 20480
buffer_load_dwordx4 a[100:103], v1, s[8:11], s81 offen offset:1024 sc1
K 99,102,103
Mvaa 112,140,44
Ta 132,23,10368
Ta 134,23,15488
Mvaa 112,136,40
W 8
s_add_u32 s83, s28, 0
s_mul_i32 s66, s58, 320
Mvaa 116,144,32
Ta 136,23,30848
Ta 138,23,35968
Mvaa 116,148,36
s_add_u32 s72, s72, s66
buffer_store_dwordx4 v[96:99], v1, s[8:11], s83 offen sc1
Ta 140,23,41088
Mvaa 124,148,52
Ta 142,23,46208
K 104,104,105
Mvaa 124,144,48
W 8
Ta 144,22,51360
K 105,106,107
Mvaa 124,152,56
s_mul_i32 s75, s67, 20480
Ta 146,22,56480
Mvaa 124,156,60
buffer_load_dwordx4 a[116:119], v1, s[8:11], s82 offen offset:1024 sc1
K 106,108,109
Mvaa 116,156,44
s_mul_i32 s66, s58, 2560
Ta 148,24,10400
Ta 150,24,15520
Mvaa 116,152,40
W 8
Ta 152,24,30880
K 107,110,111
Mvaa 128,128,32
s_add_u32 s83, s29, 40960
s_add_u32 s75, s75, s66
Ta 154,24,36000
Mvaa 128,132,36
buffer_load_dwordx4 a[104:107], v1, s[8:11], s81 offen offset:2048 sc1
Ta 156,24,41120
Mvaa 136,132,52
Ta 158,24,46240
Mvaa 136,128,48
W 8
s_add_u32 s75, s75, 131072
Ta 128,21,51392
Mvaa 136,136,56
Ta 130,21,56512
Mvaa 136,140,60
s_xor_b32 s67, s61, 124
buffer_store_dwordx4 v[104:107], v1, s[8:11], s83 offen sc1
K 112,112,113
Mvaa 128,140,44
Ta 132,23,10432
K 113,114,115
Mvaa 128,136,40
W 7
Ta 134,23,15552
K 114,116,117
Mvaa 132,144,32
s_mul_i32 s73, s67, 20480
Ta 136,23,30912
Ta 138,23,36032
Mvaa 132,148,36
buffer_load_dwordx4 a[120:123], v1, s[8:11], s82 offen offset:2048 sc1
K 115,118,119
Mvaa 140,148,52
s_add_u32 s83, s28, 8192
Ta 140,23,41152
Ta 142,23,46272
)"
R"(Mvaa 140,144,48
W 8
s_mul_i32 s66, s58, 320
Ta 144,22,51424
Mvaa 140,152,56
s_add_u32 s73, s73, s66
Ta 146,22,56544
Mvaa 140,156,60
buffer_load_dwordx4 a[108:111], v1, s[8:11], s81 offen offset:3072 sc1
Ta 148,24,10464
Mvaa 132,156,44
s_mul_i32 s76, s67, 20480
Ta 150,24,15584
Mvaa 132,152,40
W 8
Ta 152,24,30944
Ta 154,24,36064
Mvaa 144,128,32
Mvaa 144,132,36
s_mul_i32 s66, s58, 2560
buffer_store_dwordx4 v[112:115], v1, s[8:11], s83 offen sc1
K 120,120,121
Mvaa 152,132,52
K 121,122,123
K 122,124,125
Mvaa 152,128,48
W 6
Ta 156,24,41184
K 123,126,127
Mvaa 152,136,56
s_add_u32 s83, s29, 49152
s_add_u32 s76, s76, s66
Ta 158,24,46304
Mvaa 152,140,60
s_add_u32 s76, s76, 131072
buffer_load_dwordx4 a[124:127], v1, s[8:11], s82 offen offset:3072 sc1
Ta 128,21,51456
Mvaa 144,140,44
s_add_u32 s81, s58, 3
s_and_b32 s81, s81, 3
s_lshl_b32 s81, s81, 12
Mvaa 144,136,40
W 5
s_add_u32 s81, s81, s50
Ta 130,21,56576
Mvaa 148,144,32
Ta 132,23,10496
Ta 134,23,15616
Mvaa 148,148,36
buffer_load_dwordx4 a[184:187], v1, s[8:11], s81 offen offset:0 sc1
Ta 136,23,30976
Mvaa 156,148,52
s_add_u32 s82, s81, 131072
Ta 138,23,36096
v_bfe_u32 v15, v1, 7, 1
Mvaa 156,144,48
W 6
Ta 140,23,41216
Ta 142,23,46336
Mvaa 156,152,56
Ta 144,22,51488
v_mul_u32_u24 v14, 10240, v15
Mvaa 156,156,60
buffer_store_dwordx4 v[120:123], v1, s[8:11], s83 offen sc1
K 128,128,129
Mvaa 148,156,44
K 129,130,131
K 130,132,133
Mvaa 148,152,40
W 5
Ta 146,22,56608
K 131,134,135
Mvaa 160,128,32
s_add_u32 s83, s28, 16384
Ta 148,24,10528
v_lshrrev_b32 v15, 8, v1
Mvaa 160,132,36
buffer_load_dwordx4 a[200:203], v1, s[8:11], s82 offen offset:0 sc1
Ta 150,24,15648
Mvaa 168,132,52
Ta 152,24,31008
v_lshl_add_u32 v14, v15, 7, v14
Mvaa 168,128,48
W 5
Ta 154,24,36128
v_and_b32 v15, 0x70, v1
Mvaa 168,136,56
Ta 156,24,41248
Ta 158,24,46368
Mvaa 168,140,60
buffer_load_dwordx4 a[188:191], v1, s[8:11], s81 offen offset:1024 sc1
v_add_u32 v14, v14, v15
Mvaa 160,140,44
s_bitcmp1_b32 s14, 0
s_cselect_b32 s74, s73, s72
s_cselect_b32 s77, s76, s75
Ta 128,21,10240
Ta 130,21,15360
Mvaa 160,136,40
W 6
Ta 132,21,20480
Ta 134,21,25600
Mvaa 164,144,32
s_add_u32 s74, s74, 256
Ta 136,22,10272
Ta 138,22,15392
Mvaa 164,148,36
buffer_store_dwordx4 v[128:131], v1, s[8:11], s83 offen sc1
K 136,136,137
Mvaa 172,148,52
s_add_u32 s77, s77, 2048
K 137,138,139
K 138,140,141
Mvaa 172,144,48
W 6
Ta 140,22,20512
K 139,142,143
Mvaa 172,152,56
s_add_u32 s83, s29, 57344
Ta 142,22,25632
Ta 144,21,10304
Mvaa 172,156,60
s_lshl_b32 s66, s14, 6
buffer_load_dwordx4 a[204:207], v1, s[8:11], s82 offen offset:1024 sc1
Ta 146,21,15424
Mvaa 164,156,44
Ta 148,21,20544
Ta 150,21,25664
Mvaa 164,152,40
s_waitcnt vmcnt(21) lgkmcnt(8)
s_lshl_b32 s67, s14, 9
Ta 152,22,10336
Mvaa 176,128,64
Ta 154,22,15456
Mvaa 176,132,68
s_add_u32 s72, s72, s66
buffer_load_dwordx4 a[192:195], v1, s[8:11], s81 offen offset:2048 sc1
Ta 156,22,20576
Mvaa 184,132,84
Ta 158,22,25696
Mvaa 184,128,80
W 8
Ta 128,21,10368
Ta 130,21,15488
Mvaa 188,136,80
s_add_u32 s75, s75, s67
Mvaa 188,140,84
buffer_store_dwordx4 v[136:139], v1, s[8:11], s83 offen sc1
K 144,144,145
Mvaa 180,140,68
s_add_u32 s73, s73, s66
K 145,146,147
K 146,148,149
Mvaa 180,136,64
W 6
Ta 132,21,20608
K 147,150,151
Mvaa 192,144,64
s_add_u32 s83, s28, 24576
Ta 134,21,25728
Ta 136,22,10400
Mvaa 192,148,68
s_add_u32 s76, s76, s67
buffer_load_dwordx4 a[208:211], v1, s[8:11], s82 offen offset:2048 sc1
Ta 138,22,15520
Mvaa 200,148,84
Ta 140,22,20640
Ta 142,22,25760
Mvaa 200,144,80
W 8
Ta 144,21,10432
Ta 146,21,15552
Mvaa 204,152,80
Mvaa 204,156,84
buffer_store_dwordx4 v[144:147], v1, s[8:11], s83 offen sc1
K 152,152,153
Mvaa 196,156,68
K 153,154,155
K 154,156,157
Mvaa 196,152,64
W 6
Ta 148,21,20672
K 155,158,159
Mvaa 208,128,64
s_add_u32 s83, s29, 65536
Ta 150,21,25792
Ta 152,22,10464
Mvaa 208,132,68
buffer_load_dwordx4 a[196:199], v1, s[8:11], s81 offen offset:3072 sc1
Ta 154,22,15584
Mvaa 216,132,84
Ta 156,22,20704
Ta 158,22,25824
Mvaa 216,128,80
W 8
Ta 128,21,10496
Ta 130,21,15616
Mvaa 220,136,80
Mvaa 220,140,84
buffer_load_dwordx4 a[212:215], v1, s[8:11], s82 offen offset:3072 sc1
Ta 132,21,20736
Mvaa 212,140,68
Ta 134,21,25856
Mvaa 212,136,64
W 8
Ta 136,22,10528
Ta 138,22,15648
Mvaa 224,144,64
Mvaa 224,148,68
buffer_store_dwordx4 v[152:155], v1, s[8:11], s83 offen sc1
K 160,160,161
Mvaa 232,148,84
K 161,162,163
K 162,164,165
Mvaa 232,144,80
W 6
Ta 140,22,20768
K 163,166,167
Mvaa 236,152,80
s_add_u32 s83, s28, 32768
Ta 142,22,25888
Ta 144,21,30720
Mvaa 236,156,84
buffer_load_dwordx4 v[36:39], v20, s[16:19], s72 offen
Ta 146,21,35840
Mvaa 228,156,68
Ta 148,23,0
Ta 150,23,5120
Mvaa 228,152,64
W 8
Ta 152,22,30752
Ta 154,22,35872
Mvaa 240,128,64
Mvaa 240,132,68
buffer_load_dwordx4 v[44:47], v14, s[8:11], s75 offen sc1
Ta 156,24,32
Mvaa 248,132,84
Ta 158,24,5152
Mvaa 248,128,80
W 8
Ta 128,21,30784
Ta 130,21,35904
Mvaa 252,136,80
Mvaa 252,140,84
buffer_store_dwordx4 v[160:163], v1, s[8:11], s83 offen sc1
K 168,168,169
Mvaa 244,140,68
K 169,170,171
K 170,172,173
Mvaa 244,136,64
s_waitcnt vmcnt(27) lgkmcnt(6)
Ta 132,23,64
K 171,174,175
Mvaa 176,144,72
s_add_u32 s83, s29, 73728
Ta 134,23,5184
Ta 136,22,30816
Mvaa 176,148,76
buffer_load_dwordx4 v[52:55], v20, s[16:19], s73 offen
Ta 138,22,35936
Mvaa 184,148,92
Ta 140,24,96
Ta 142,24,5216
Mvaa 184,144,88
W 8
Ta 144,21,30848
Ta 146,21,35968
Mvaa 188,152,88
Mvaa 188,156,92
buffer_store_dwordx4 v[168:171], v1, s[8:11], s83 offen sc1
Ta 148,23,128
Mvaa 180,156,76
Ta 150,23,5248
Mvaa 180,152,72
W 8
Ta 152,22,30880
Ta 154,22,36000
Mvaa 192,128,72
Mvaa 192,132,76
buffer_load_dwordx4 v[60:63], v14, s[8:11], s76 offen sc1
Ta 156,24,160
Mvaa 200,132,92
Ta 158,24,5280
Mvaa 200,128,88
W 8
Ta 128,21,30912
Ta 130,21,36032
Mvaa 204,136,88
Mvaa 204,140,92
buffer_load_dwordx4 v[68:71], v20, s[16:19], s74 offen
Ta 132,23,192
Mvaa 196,140,76
Ta 134,23,5312
Mvaa 196,136,72
W 8
Ta 136,22,30944
Ta 138,22,36064
Mvaa 208,144,72
Mvaa 208,148,76
buffer_load_dwordx4 v[76:79], v14, s[8:11], s77 offen sc1
Ta 140,24,224
Mvaa 216,148,92
Ta 142,24,5344
Mvaa 216,144,88
W 8
Ta 144,21,30976
Ta 146,21,36096
Mvaa 220,152,88
Mvaa 220,156,92
buffer_load_dwordx2 v[12:13], v27, s[8:11], 0 offen offset:3584 sc1
Ta 148,23,256
Mvaa 212,156,76
Ta 150,23,5376
Mvaa 212,152,72
W 8
Ta 152,22,31008
Ta 154,22,36128
Mvaa 224,128,72
Mvaa 224,132,76
Ta 156,24,288
Ta 158,24,5408
Mvaa 232,132,92
Mvaa 232,128,88
W 8
Ta 128,23,20480
Ta 130,23,25600
Mvaa 236,136,88
Mvaa 236,140,92
Ta 132,23,51200
Ta 134,23,56320
Mvaa 228,140,76
Mvaa 228,136,72
W 8
Ta 136,25,0
Ta 138,25,5120
Mvaa 240,144,72
Mvaa 240,148,76
Ta 140,21,40960
Ta 142,21,46080
Mvaa 248,148,92
Mvaa 248,144,88
W 8
Ta 144,21,51200
Ta 146,21,56320
Mvaa 252,152,88
Mvaa 252,156,92
Ta 148,23,10240
Ta 150,23,15360
Mvaa 244,156,76
Mvaa 244,152,72
s_waitcnt vmcnt(28) lgkmcnt(8)
Ta 152,23,30720
Ta 154,23,35840
Mvaa 176,128,96
Mvaa 176,132,100
Ta 156,23,40960
Ta 158,23,46080
Mvaa 184,132,116
Mvaa 184,128,112
s_waitcnt vmcnt(22) lgkmcnt(8)
Ta 128,24,20512
Ta 130,24,25632
Mvaa 184,136,120
Mvaa 184,140,124
Ta 132,24,51232
Ta 134,24,56352
Mvaa 176,140,108
s_barrier
Mvaa 176,136,104
s_waitcnt vmcnt(16) lgkmcnt(8)
Ta 136,26,32
Ta 138,26,5152
Mvaa 176,144,184
Mvaa 176,148,188
Ta 140,22,40992
Ta 142,22,46112
Mvaa 184,148,204
s_waitcnt vmcnt(4)
v_lshlrev_b32 v2, 2, v1
v_mov_b64 v[28:29], s[12:13]
s_mov_b64 exec, 3
buffer_store_dwordx2 v[28:29], v2, s[8:11], s53 offen offset:0 sc1
s_mov_b64 exec, -1
Mvaa 184,144,200
W 8
Ta 144,22,51232
Ta 146,22,56352
Mvaa 184,152,208
Mvaa 184,156,212
Ta 148,24,10272
Ta 150,24,15392
Mvaa 176,156,196
Mvaa 176,152,192
W 8
Ta 152,24,30752
Ta 154,24,35872
Mvaa 180,128,96
Mvaa 180,132,100
Ta 156,24,40992
Ta 158,24,46112
)"
R"(Mvaa 188,132,116
Mvaa 188,128,112
W 8
Ta 128,23,20544
Ta 130,23,25664
Mvaa 188,136,120
Mvaa 188,140,124
Ta 132,23,51264
Ta 134,23,56384
Mvaa 180,140,108
Mvaa 180,136,104
W 8
Ta 136,25,64
Ta 138,25,5184
Mvaa 180,144,184
Mvaa 180,148,188
Ta 140,21,41024
Ta 142,21,46144
Mvaa 188,148,204
Mvaa 188,144,200
W 8
Ta 144,21,51264
Ta 146,21,56384
Mvaa 188,152,208
Mvaa 188,156,212
Ta 148,23,10304
Ta 150,23,15424
Mvaa 180,156,196
s_waitcnt vmcnt(1)
v_xor_b32 v12, s12, v12
v_xor_b32 v13, s13, v13
v_lshrrev_b32 v3, 11, v12
v_or_b32 v13, v13, v3
v_cmp_eq_u32 vcc, 0, v13
s_cmp_eq_u64 vcc, -1
s_cbranch_scc0 .LL_108%=
.LL_109%=:
v_bfe_u32 v13, v12, 4, 7
s_xor_b32 s66, s61, 124
v_cmp_eq_u32 vcc, s61, v13
v_cmp_eq_u32_e64 s[86:87], s66, v13
s_or_b32 s68, vcc_lo, s86
s_and_b32 s68, s68, 255
s_cbranch_scc1 .Lup_106%=
.Lupr_107%=:
Mvaa 180,152,192
W 8
Ta 152,23,30784
Ta 154,23,35904
Mvaa 192,128,96
v_lshlrev_b32 v32, 16, v36
Mvaa 192,132,100
Ta 156,23,41024
v_and_b32 v33, 0xffff0000, v36
Mvaa 200,132,116
Ta 158,23,46144
v_lshlrev_b32 v34, 16, v37
Mvaa 200,128,112
W 8
K 176,176,177
v_and_b32 v35, 0xffff0000, v37
Mvaa 200,136,120
Ta 128,24,20576
Ta 130,24,25696
Mvaa 200,140,124
buffer_load_dwordx2 v[16:17], v18, s[8:11], s63 offen sc1
v_lshlrev_b32 v36, 16, v38
Mvaa 192,140,108
K 177,178,179
v_and_b32 v37, 0xffff0000, v38
Mvaa 192,136,104
W 6
Ta 132,24,51296
K 178,180,181
Mvaa 192,144,184
Ta 134,24,56416
v_lshlrev_b32 v38, 16, v39
Mvaa 192,148,188
K 179,182,183
v_and_b32 v39, 0xffff0000, v39
Mvaa 200,148,204
s_add_u32 s83, s28, 327680
Ta 136,26,96
Ta 138,26,5216
Mvaa 200,144,200
W 6
v_lshlrev_b32 v40, 16, v44
v_and_b32 v41, 0xffff0000, v44
Mvaa 200,152,208
buffer_store_dwordx4 v[176:179], v1, s[8:11], s83 offen sc1
K 184,184,185
Mvaa 200,156,212
K 185,186,187
K 186,188,189
Mvaa 192,156,196
K 187,190,191
s_add_u32 s83, s29, 368640
buffer_store_dwordx4 v[184:187], v1, s[8:11], s83 offen sc1
Mvaa 192,152,192
Ta 140,22,41056
Ta 142,22,46176
W 4
Mvaa 196,128,96
v_lshlrev_b32 v42, 16, v45
v_and_b32 v43, 0xffff0000, v45
Mvaa 196,132,100
Ta 144,22,51296
Ta 146,22,56416
Mvaa 204,132,116
v_lshlrev_b32 v44, 16, v46
v_and_b32 v45, 0xffff0000, v46
Mvaa 204,128,112
Ta 148,24,10336
Ta 150,24,15456
W 4
Mvaa 204,136,120
v_lshlrev_b32 v46, 16, v47
v_and_b32 v47, 0xffff0000, v47
Mvaa 204,140,124
Ta 152,24,30816
Ta 154,24,35936
Mvaa 196,140,108
v_lshlrev_b32 v48, 16, v52
v_and_b32 v49, 0xffff0000, v52
Mvaa 196,136,104
Ta 156,24,41056
Ta 158,24,46176
W 4
Mvaa 196,144,184
v_lshlrev_b32 v50, 16, v53
v_and_b32 v51, 0xffff0000, v53
Mvaa 196,148,188
Ta 128,23,20608
Ta 130,23,25728
Mvaa 204,148,204
v_lshlrev_b32 v52, 16, v54
v_and_b32 v53, 0xffff0000, v54
Mvaa 204,144,200
Ta 132,23,51328
Ta 134,23,56448
W 4
Mvaa 204,152,208
v_lshlrev_b32 v54, 16, v55
v_and_b32 v55, 0xffff0000, v55
Mvaa 204,156,212
Ta 136,25,128
Ta 138,25,5248
Mvaa 196,156,196
v_lshlrev_b32 v56, 16, v60
v_and_b32 v57, 0xffff0000, v60
Mvaa 196,152,192
Ta 140,21,41088
Ta 142,21,46208
W 4
Mvaa 208,128,96
v_lshlrev_b32 v58, 16, v61
v_and_b32 v59, 0xffff0000, v61
Mvaa 208,132,100
Ta 144,21,51328
Ta 146,21,56448
Mvaa 216,132,116
v_lshlrev_b32 v60, 16, v62
v_and_b32 v61, 0xffff0000, v62
Mvaa 216,128,112
Ta 148,23,10368
Ta 150,23,15488
W 4
Mvaa 216,136,120
v_lshlrev_b32 v62, 16, v63
v_and_b32 v63, 0xffff0000, v63
Mvaa 216,140,124
Ta 152,23,30848
Ta 154,23,35968
Mvaa 208,140,108
v_lshlrev_b32 v64, 16, v68
v_and_b32 v65, 0xffff0000, v68
Mvaa 208,136,104
Ta 156,23,41088
Ta 158,23,46208
W 4
Mvaa 208,144,184
v_lshlrev_b32 v66, 16, v69
K 192,192,193
Mvaa 208,148,188
Ta 128,24,20640
v_and_b32 v67, 0xffff0000, v69
Mvaa 216,148,204
Ta 130,24,25760
K 193,194,195
Mvaa 216,144,200
Ta 132,24,51360
Ta 134,24,56480
W 4
Mvaa 216,152,208
v_lshlrev_b32 v68, 16, v70
K 194,196,197
Mvaa 216,156,212
v_and_b32 v69, 0xffff0000, v70
v_lshlrev_b32 v70, 16, v71
Mvaa 208,156,196
K 195,198,199
s_add_u32 s83, s28, 335872
buffer_store_dwordx4 v[192:195], v1, s[8:11], s83 offen sc1
K 200,200,201
K 201,202,203
K 202,204,205
K 203,206,207
s_add_u32 s83, s29, 376832
buffer_store_dwordx4 v[200:203], v1, s[8:11], s83 offen sc1
Mvaa 208,152,192
Ta 136,26,160
Ta 138,26,5280
Ta 140,22,41120
Ta 142,22,46240
W 4
Mvaa 212,128,96
s_waitcnt vmcnt(4)
v_cmp_eq_u64 vcc, s[12:13], v[16:17]
s_and_b32 s71, vcc_lo, 255
s_bitset0_b32 s71, s59
v_and_b32 v71, 0xffff0000, v71
v_lshlrev_b32 v72, 16, v76
Mvaa 212,132,100
s_lshl_b32 s92, s14, 3
s_lshr_b32 s92, s71, s92
s_mov_b32 s91, 134217728
Mvaa 220,132,116
s_bitcmp1_b32 s71, 0
s_cselect_b32 s94, s78, s91
v_and_b32 v73, 0xffff0000, v76
Mvaa 220,128,112
Ta 144,22,51360
Ta 146,22,56480
Ta 148,24,10400
Ta 150,24,15520
W 4
Mvaa 220,136,120
v_lshlrev_b32 v74, 16, v77
v_and_b32 v75, 0xffff0000, v77
Mvaa 220,140,124
v_lshlrev_b32 v76, 16, v78
v_and_b32 v77, 0xffff0000, v78
Mvaa 212,140,108
v_lshlrev_b32 v78, 16, v79
v_and_b32 v79, 0xffff0000, v79
Mvaa 212,136,104
Ta 152,24,30880
Ta 154,24,36000
Ta 156,24,41120
Ta 158,24,46240
W 4
Mvaa 212,144,184
Ta 128,23,20672
Ta 130,23,25792
Mvaa 212,148,188
buffer_load_dwordx4 a[0:3], v1, s[8:11], s94 offen offset:0 sc1
Ta 132,23,51392
Mvaa 220,148,204
Ta 134,23,56512
Ta 136,25,192
Mvaa 220,144,200
W 5
Ta 138,25,5312
Ta 140,21,41152
Mvaa 220,152,208
Ta 142,21,46272
Ta 144,21,51392
Mvaa 220,156,212
s_bitcmp1_b32 s92, 0
s_cbranch_scc0 .LL_111%=
buffer_load_dwordx4 a[216:219], v1, s[8:11], s65 offen offset:0 sc1
.LL_111%=:
Ta 146,21,56512
Mvaa 212,156,196
s_bitcmp1_b32 s71, 1
s_cselect_b32 s94, s78, s91
Ta 148,23,10432
Mvaa 212,152,192
W 7
Ta 150,23,15552
Ta 152,23,30912
Mvaa 224,128,96
Ta 154,23,36032
Mvaa 224,132,100
buffer_load_dwordx4 a[4:7], v1, s[8:11], s94 offen offset:1024 sc1
Ta 156,23,41152
Mvaa 232,132,116
Ta 158,23,46272
K 208,208,209
Mvaa 232,128,112
W 8
Ta 128,24,20704
Ta 130,24,25824
Mvaa 232,136,120
K 209,210,211
Mvaa 232,140,124
s_bitcmp1_b32 s92, 1
s_cbranch_scc0 .LL_112%=
buffer_load_dwordx4 a[220:223], v1, s[8:11], s65 offen offset:1024 sc1
.LL_112%=:
K 210,212,213
Mvaa 224,140,108
s_bitcmp1_b32 s71, 2
s_cselect_b32 s94, s78, s91
Ta 132,24,51424
Mvaa 224,136,104
W 7
Ta 134,24,56544
K 211,214,215
Mvaa 224,144,184
s_add_u32 s83, s28, 344064
Ta 136,26,224
Ta 138,26,5344
Mvaa 224,148,188
buffer_load_dwordx4 a[8:11], v1, s[8:11], s94 offen offset:2048 sc1
Ta 140,22,41184
Mvaa 232,148,204
Ta 142,22,46304
Mvaa 232,144,200
W 8
Ta 144,22,51424
Ta 146,22,56544
Mvaa 232,152,208
Mvaa 232,156,212
buffer_store_dwordx4 v[208:211], v1, s[8:11], s83 offen sc1
K 216,216,217
Mvaa 224,156,196
K 217,218,219
K 218,220,221
K 219,222,223
s_add_u32 s83, s29, 385024
buffer_store_dwordx4 v[216:219], v1, s[8:11], s83 offen sc1
Mvaa 224,152,192
W 6
Ta 148,24,10464
Ta 150,24,15584
Mvaa 228,128,96
Ta 152,24,30944
Ta 154,24,36064
Mvaa 228,132,100
Ta 156,24,41184
Ta 158,24,46304
Mvaa 236,132,116
s_bitcmp1_b32 s92, 2
s_cbranch_scc0 .LL_113%=
buffer_load_dwordx4 a[224:227], v1, s[8:11], s65 offen offset:2048 sc1
.LL_113%=:
Mvaa 236,128,112
W 8
s_bitcmp1_b32 s71, 3
s_cselect_b32 s94, s78, s91
Mvaa 236,136,120
Ta 128,23,20736
Ta 130,23,25856
Mvaa 236,140,124
Ta 132,23,51456
Ta 134,23,56576
Mvaa 228,140,108
buffer_load_dwordx4 a[12:15], v1, s[8:11], s94 offen offset:3072 sc1
Mvaa 228,136,104
W 8
Ta 136,25,256
Ta 138,25,5376
Mvaa 228,144,184
Mvaa 228,148,188
Ta 140,21,41216
Ta 142,21,46336
Mvaa 236,148,204
s_bitcmp1_b32 s92, 3
s_cbranch_scc0 .LL_114%=
buffer_load_dwordx4 a[228:231], v1, s[8:11], s65 offen offset:3072 sc1
.LL_114%=:
Mvaa 236,144,200
W 8
s_bitcmp1_b32 s71, 4
s_cselect_b32 s94, s78, s91
Mvaa 236,152,208
Ta 144,21,51456
Ta 146,21,56576
Mvaa 236,156,212
Ta 148,23,10496
Ta 150,23,15616
Mvaa 228,156,196
buffer_load_dwordx4 a[16:19], v19, s[8:11], s94 offen offset:0 sc1
Mvaa 228,152,192
W 8
Ta 152,23,30976
Ta 154,23,36096
Mvaa 240,128,96
Mvaa 240,132,100
Ta 156,23,41216
Ta 158,23,46336
Mvaa 248,132,116
s_bitcmp1_b32 s92, 4
s_cbranch_scc0 .LL_115%=
buffer_load_dwordx4 a[232:235], v19, s[8:11], s65 offen offset:0 sc1
.LL_115%=:
K 224,224,225
Mvaa 248,128,112
W 8
)"
R"(s_bitcmp1_b32 s71, 5
s_cselect_b32 s94, s78, s91
Mvaa 248,136,120
Ta 128,24,20768
Ta 130,24,25888
Mvaa 248,140,124
K 225,226,227
K 226,228,229
Mvaa 240,140,108
buffer_load_dwordx4 a[20:23], v19, s[8:11], s94 offen offset:1024 sc1
Ta 132,24,51488
Mvaa 240,136,104
W 7
Ta 134,24,56608
K 227,230,231
Mvaa 240,144,184
s_add_u32 s83, s28, 352256
Ta 136,26,288
Ta 138,26,5408
Mvaa 240,148,188
Ta 140,22,41248
Ta 142,22,46368
Mvaa 248,148,204
s_bitcmp1_b32 s92, 5
s_cbranch_scc0 .LL_116%=
buffer_load_dwordx4 a[236:239], v19, s[8:11], s65 offen offset:1024 sc1
.LL_116%=:
Mvaa 248,144,200
W 8
s_bitcmp1_b32 s71, 6
s_cselect_b32 s94, s78, s91
Mvaa 248,152,208
Ta 144,22,51488
Ta 146,22,56608
Mvaa 248,156,212
Ta 148,24,10528
Ta 150,24,15648
Mvaa 240,156,196
buffer_store_dwordx4 v[224:227], v1, s[8:11], s83 offen sc1
K 232,232,233
K 233,234,235
K 234,236,237
K 235,238,239
s_add_u32 s83, s29, 393216
buffer_store_dwordx4 v[232:235], v1, s[8:11], s83 offen sc1
Mvaa 240,152,192
W 8
Ta 152,24,31008
Ta 154,24,36128
Mvaa 244,128,96
Mvaa 244,132,100
Ta 156,24,41248
Ta 158,24,46368
Mvaa 252,132,116
buffer_load_dwordx4 a[24:27], v19, s[8:11], s94 offen offset:2048 sc1
Mvaa 252,128,112
W 8
Mvaa 252,136,120
Mvaa 252,140,124
Mvaa 244,140,108
s_bitcmp1_b32 s92, 6
s_cbranch_scc0 .LL_117%=
buffer_load_dwordx4 a[240:243], v19, s[8:11], s65 offen offset:2048 sc1
.LL_117%=:
Mvaa 244,136,104
W 4
s_bitcmp1_b32 s71, 7
s_cselect_b32 s94, s78, s91
Mvaa 244,144,184
Mvaa 244,148,188
Mvaa 252,148,204
buffer_load_dwordx4 a[28:31], v19, s[8:11], s94 offen offset:3072 sc1
Mvaa 252,144,200
W 0
Mvaa 252,152,208
Mvaa 252,156,212
Mvaa 244,156,196
s_bitcmp1_b32 s92, 7
s_cbranch_scc0 .LL_118%=
buffer_load_dwordx4 a[244:247], v19, s[8:11], s65 offen offset:3072 sc1
.LL_118%=:
Mvaa 244,152,192
K 240,240,241
K 241,242,243
s_nop 5
K 242,244,245
K 243,246,247
s_add_u32 s83, s28, 360448
buffer_store_dwordx4 v[240:243], v1, s[8:11], s83 offen sc1
K 248,248,249
K 249,250,251
K 250,252,253
K 251,254,255
s_add_u32 s83, s29, 401408
buffer_store_dwordx4 v[248:251], v1, s[8:11], s83 offen sc1
.Ltend_83%=:
buffer_load_dwordx2 v[16:17], v18, s[8:11], s63 offen sc1
s_bitcmp1_b32 s28, 27
s_cbranch_scc0 .LL_119%=
ds_write_b128 v1, v[96:99] offset:0
ds_write_b128 v1, v[112:115] offset:1024
ds_write_b128 v1, v[128:131] offset:2048
ds_write_b128 v1, v[144:147] offset:3072
ds_write_b128 v1, v[160:163] offset:4096
ds_write_b128 v1, v[176:179] offset:5120
ds_write_b128 v1, v[192:195] offset:6144
ds_write_b128 v1, v[208:211] offset:7168
ds_write_b128 v1, v[224:227] offset:8192
ds_write_b128 v1, v[240:243] offset:9216
W 0
.LL_119%=:
s_bitcmp1_b32 s29, 27
s_cbranch_scc0 .LL_120%=
ds_write_b128 v1, v[104:107] offset:0
ds_write_b128 v1, v[120:123] offset:1024
ds_write_b128 v1, v[136:139] offset:2048
ds_write_b128 v1, v[152:155] offset:3072
ds_write_b128 v1, v[168:171] offset:4096
ds_write_b128 v1, v[184:187] offset:5120
ds_write_b128 v1, v[200:203] offset:6144
ds_write_b128 v1, v[216:219] offset:7168
ds_write_b128 v1, v[232:235] offset:8192
ds_write_b128 v1, v[248:251] offset:9216
W 0
.LL_120%=:
s_lshl_b32 s80, s14, 10
v_add_u32 v21, s80, v1
s_cmp_lg_u32 s68, 0
s_cbranch_scc1 .Lup_121%=
.Lupr_122%=:
s_lshl_b32 s66, 0x101, s59
s_waitcnt vmcnt(1)
s_cmp_eq_u32 s64, 0
s_cbranch_scc0 .LL_127%=
v_lshlrev_b32 v2, 2, v1
v_mov_b64 v[28:29], s[12:13]
s_mov_b64 exec, 3
buffer_store_dwordx2 v[28:29], v2, s[8:11], s53 offen offset:512 sc1
s_mov_b64 exec, -1
.LL_127%=:
s_barrier
s_waitcnt vmcnt(1)
v_cmp_eq_u64 vcc, s[12:13], v[16:17]
s_or_b32 s67, vcc_lo, s66
s_cmp_eq_u32 s14, 0
s_cbranch_scc1 .LL_129%=
s_cmp_eq_u32 s14, 1
s_cbranch_scc1 .LL_130%=
s_mov_b32 s84, 0
.LL_131%=:
s_or_b32 s70, s71, s66
s_andn2_b32 s69, s67, s71
s_and_b32 s69, s69, 0xffff
s_cbranch_scc0 .LL_134%=
s_and_b32 s80, s69, 255
s_cbranch_scc0 .LL_135%=
s_bitcmp1_b32 s69, 0
s_cbranch_scc0 .LL_136%=
s_cmp_eq_u32 s59, 0
s_cbranch_scc0 .LL_137%=
Ra 0,21,0
s_branch .LL_136%=
.LL_137%=:
buffer_load_dwordx4 a[0:3], v1, s[8:11], s78 offen offset:0 sc1
.LL_136%=:
s_bitcmp1_b32 s69, 1
s_cbranch_scc0 .LL_138%=
s_cmp_eq_u32 s59, 1
s_cbranch_scc0 .LL_139%=
Ra 4,21,0
s_branch .LL_138%=
.LL_139%=:
buffer_load_dwordx4 a[4:7], v1, s[8:11], s78 offen offset:1024 sc1
.LL_138%=:
s_bitcmp1_b32 s69, 2
s_cbranch_scc0 .LL_140%=
s_cmp_eq_u32 s59, 2
s_cbranch_scc0 .LL_141%=
Ra 8,21,0
s_branch .LL_140%=
.LL_141%=:
buffer_load_dwordx4 a[8:11], v1, s[8:11], s78 offen offset:2048 sc1
.LL_140%=:
s_bitcmp1_b32 s69, 3
s_cbranch_scc0 .LL_142%=
s_cmp_eq_u32 s59, 3
s_cbranch_scc0 .LL_143%=
Ra 12,21,0
s_branch .LL_142%=
.LL_143%=:
buffer_load_dwordx4 a[12:15], v1, s[8:11], s78 offen offset:3072 sc1
.LL_142%=:
s_bitcmp1_b32 s69, 4
s_cbranch_scc0 .LL_144%=
s_cmp_eq_u32 s59, 4
s_cbranch_scc0 .LL_145%=
Ra 16,21,0
s_branch .LL_144%=
.LL_145%=:
buffer_load_dwordx4 a[16:19], v19, s[8:11], s78 offen offset:0 sc1
.LL_144%=:
s_bitcmp1_b32 s69, 5
s_cbranch_scc0 .LL_146%=
s_cmp_eq_u32 s59, 5
s_cbranch_scc0 .LL_147%=
Ra 20,21,0
s_branch .LL_146%=
.LL_147%=:
buffer_load_dwordx4 a[20:23], v19, s[8:11], s78 offen offset:1024 sc1
.LL_146%=:
s_bitcmp1_b32 s69, 6
s_cbranch_scc0 .LL_148%=
s_cmp_eq_u32 s59, 6
s_cbranch_scc0 .LL_149%=
Ra 24,21,0
s_branch .LL_148%=
.LL_149%=:
buffer_load_dwordx4 a[24:27], v19, s[8:11], s78 offen offset:2048 sc1
.LL_148%=:
s_bitcmp1_b32 s69, 7
s_cbranch_scc0 .LL_150%=
s_cmp_eq_u32 s59, 7
s_cbranch_scc0 .LL_151%=
Ra 28,21,0
s_branch .LL_150%=
.LL_151%=:
buffer_load_dwordx4 a[28:31], v19, s[8:11], s78 offen offset:3072 sc1
.LL_150%=:
.LL_135%=:
s_and_b32 s80, s69, 65280
s_cbranch_scc0 .LL_152%=
s_bitcmp1_b32 s69, 8
s_cbranch_scc0 .LL_153%=
s_cmp_eq_u32 s59, 0
s_cbranch_scc0 .LL_154%=
Ra 32,21,5120
s_branch .LL_153%=
.LL_154%=:
buffer_load_dwordx4 a[32:35], v1, s[8:11], s79 offen offset:0 sc1
.LL_153%=:
s_bitcmp1_b32 s69, 9
s_cbranch_scc0 .LL_155%=
s_cmp_eq_u32 s59, 1
s_cbranch_scc0 .LL_156%=
Ra 36,21,5120
s_branch .LL_155%=
.LL_156%=:
buffer_load_dwordx4 a[36:39], v1, s[8:11], s79 offen offset:1024 sc1
.LL_155%=:
s_bitcmp1_b32 s69, 10
s_cbranch_scc0 .LL_157%=
s_cmp_eq_u32 s59, 2
s_cbranch_scc0 .LL_158%=
Ra 40,21,5120
s_branch .LL_157%=
.LL_158%=:
buffer_load_dwordx4 a[40:43], v1, s[8:11], s79 offen offset:2048 sc1
.LL_157%=:
s_bitcmp1_b32 s69, 11
s_cbranch_scc0 .LL_159%=
s_cmp_eq_u32 s59, 3
s_cbranch_scc0 .LL_160%=
Ra 44,21,5120
s_branch .LL_159%=
.LL_160%=:
buffer_load_dwordx4 a[44:47], v1, s[8:11], s79 offen offset:3072 sc1
.LL_159%=:
s_bitcmp1_b32 s69, 12
s_cbranch_scc0 .LL_161%=
s_cmp_eq_u32 s59, 4
s_cbranch_scc0 .LL_162%=
Ra 48,21,5120
s_branch .LL_161%=
.LL_162%=:
buffer_load_dwordx4 a[48:51], v19, s[8:11], s79 offen offset:0 sc1
.LL_161%=:
s_bitcmp1_b32 s69, 13
s_cbranch_scc0 .LL_163%=
s_cmp_eq_u32 s59, 5
s_cbranch_scc0 .LL_164%=
Ra 52,21,5120
s_branch .LL_163%=
.LL_164%=:
buffer_load_dwordx4 a[52:55], v19, s[8:11], s79 offen offset:1024 sc1
.LL_163%=:
s_bitcmp1_b32 s69, 14
s_cbranch_scc0 .LL_165%=
s_cmp_eq_u32 s59, 6
s_cbranch_scc0 .LL_166%=
Ra 56,21,5120
s_branch .LL_165%=
.LL_166%=:
buffer_load_dwordx4 a[56:59], v19, s[8:11], s79 offen offset:2048 sc1
.LL_165%=:
s_bitcmp1_b32 s69, 15
s_cbranch_scc0 .LL_167%=
s_cmp_eq_u32 s59, 7
s_cbranch_scc0 .LL_168%=
Ra 60,21,5120
s_branch .LL_167%=
.LL_168%=:
buffer_load_dwordx4 a[60:63], v19, s[8:11], s79 offen offset:3072 sc1
.LL_167%=:
.LL_152%=:
s_or_b32 s71, s71, s69
.LL_134%=:
s_andn2_b32 s80, 0xffff, s71
s_cbranch_scc0 .LL_133%=
s_sleep 4
buffer_load_dwordx2 v[16:17], v18, s[8:11], s63 offen sc1
s_and_b32 s80, s70, 0x100ff
s_cmp_eq_u32 s80, 0xff
s_cbranch_scc0 .LL_132%=
W 0
Zvva 80,4,0
Zvva 84,8,0
Mvva 80,4,4
Mvva 84,8,4
Mvva 80,4,8
Mvva 84,8,8
Mvva 80,4,12
Mvva 84,8,12
Mvva 80,4,16
Mvva 84,8,16
Mvva 80,4,20
Mvva 84,8,20
Mvva 80,4,24
Mvva 84,8,24
Mvva 80,4,28
Mvva 84,8,28
s_nop 6
K 160,80,81
K 161,82,83
K 162,84,85
K 163,86,87
v_lshlrev_b32 v80, 16, v160
v_and_b32 v81, 0xffff0000, v160
v_lshlrev_b32 v82, 16, v161
v_and_b32 v83, 0xffff0000, v161
v_lshlrev_b32 v84, 16, v162
v_and_b32 v85, 0xffff0000, v162
v_lshlrev_b32 v86, 16, v163
v_and_b32 v87, 0xffff0000, v163
v_pk_add_f32 v[80:81], v[32:33], v[80:81]
v_pk_add_f32 v[82:83], v[34:35], v[82:83]
v_pk_add_f32 v[84:85], v[36:37], v[84:85]
v_pk_add_f32 v[86:87], v[38:39], v[86:87]
K 160,80,81
K 161,82,83
K 162,84,85
K 163,86,87
v_lshlrev_b32 v80, 16, v160
v_and_b32 v81, 0xffff0000, v160
v_lshlrev_b32 v82, 16, v161
v_and_b32 v83, 0xffff0000, v161
v_lshlrev_b32 v84, 16, v162
v_and_b32 v85, 0xffff0000, v162
v_lshlrev_b32 v86, 16, v163
v_and_b32 v87, 0xffff0000, v163
v_pk_add_f32 v[80:81], v[40:41], v[80:81]
v_pk_add_f32 v[82:83], v[42:43], v[82:83]
v_pk_add_f32 v[84:85], v[44:45], v[84:85]
v_pk_add_f32 v[86:87], v[46:47], v[86:87]
K 80,80,81
K 81,82,83
K 82,84,85
K 83,86,87
buffer_store_dwordx4 v[80:83], v20, s[20:23], s72 offen nt
s_bitset1_b32 s71, 16
.LL_132%=:
s_add_u32 s84, s84, 1
s_waitcnt vmcnt(0)
v_cmp_eq_u64 vcc, s[12:13], v[16:17]
s_or_b32 s67, vcc_lo, s66
s_cmp_lt_u32 s84, 4194304
s_cbranch_scc1 .LL_131%=
.LL_133%=:
s_bitcmp1_b32 s71, 16
s_cbranch_scc1 .LL_169%=
s_and_b32 s80, s70, 0xff
s_cmp_eq_u32 s80, 0xff
s_cbranch_scc1 .LL_170%=
s_waitcnt vmcnt(0)
.LL_170%=:
W 0
Zvva 80,4,0
Zvva 84,8,0
Mvva 80,4,4
Mvva 84,8,4
Mvva 80,4,8
Mvva 84,8,8
Mvva 80,4,12
Mvva 84,8,12
Mvva 80,4,16
Mvva 84,8,16
Mvva 80,4,20
Mvva 84,8,20
Mvva 80,4,24
Mvva 84,8,24
Mvva 80,4,28
)"
R"(Mvva 84,8,28
s_nop 6
K 160,80,81
K 161,82,83
K 162,84,85
K 163,86,87
v_lshlrev_b32 v80, 16, v160
v_and_b32 v81, 0xffff0000, v160
v_lshlrev_b32 v82, 16, v161
v_and_b32 v83, 0xffff0000, v161
v_lshlrev_b32 v84, 16, v162
v_and_b32 v85, 0xffff0000, v162
v_lshlrev_b32 v86, 16, v163
v_and_b32 v87, 0xffff0000, v163
v_pk_add_f32 v[80:81], v[32:33], v[80:81]
v_pk_add_f32 v[82:83], v[34:35], v[82:83]
v_pk_add_f32 v[84:85], v[36:37], v[84:85]
v_pk_add_f32 v[86:87], v[38:39], v[86:87]
K 160,80,81
K 161,82,83
K 162,84,85
K 163,86,87
v_lshlrev_b32 v80, 16, v160
v_and_b32 v81, 0xffff0000, v160
v_lshlrev_b32 v82, 16, v161
v_and_b32 v83, 0xffff0000, v161
v_lshlrev_b32 v84, 16, v162
v_and_b32 v85, 0xffff0000, v162
v_lshlrev_b32 v86, 16, v163
v_and_b32 v87, 0xffff0000, v163
v_pk_add_f32 v[80:81], v[40:41], v[80:81]
v_pk_add_f32 v[82:83], v[42:43], v[82:83]
v_pk_add_f32 v[84:85], v[44:45], v[84:85]
v_pk_add_f32 v[86:87], v[46:47], v[86:87]
K 80,80,81
K 81,82,83
K 82,84,85
K 83,86,87
buffer_store_dwordx4 v[80:83], v20, s[20:23], s72 offen nt
s_waitcnt vmcnt(1)
W 0
Zvva 80,4,32
Zvva 84,8,32
Mvva 80,4,36
Mvva 84,8,36
Mvva 80,4,40
Mvva 84,8,40
Mvva 80,4,44
Mvva 84,8,44
Mvva 80,4,48
Mvva 84,8,48
Mvva 80,4,52
Mvva 84,8,52
Mvva 80,4,56
Mvva 84,8,56
Mvva 80,4,60
Mvva 84,8,60
s_nop 6
K 160,80,81
K 161,82,83
K 162,84,85
K 163,86,87
v_lshlrev_b32 v80, 16, v160
v_and_b32 v81, 0xffff0000, v160
v_lshlrev_b32 v82, 16, v161
v_and_b32 v83, 0xffff0000, v161
v_lshlrev_b32 v84, 16, v162
v_and_b32 v85, 0xffff0000, v162
v_lshlrev_b32 v86, 16, v163
v_and_b32 v87, 0xffff0000, v163
v_pk_add_f32 v[80:81], v[48:49], v[80:81]
v_pk_add_f32 v[82:83], v[50:51], v[82:83]
v_pk_add_f32 v[84:85], v[52:53], v[84:85]
v_pk_add_f32 v[86:87], v[54:55], v[86:87]
K 160,80,81
K 161,82,83
K 162,84,85
K 163,86,87
v_lshlrev_b32 v80, 16, v160
v_and_b32 v81, 0xffff0000, v160
v_lshlrev_b32 v82, 16, v161
v_and_b32 v83, 0xffff0000, v161
v_lshlrev_b32 v84, 16, v162
v_and_b32 v85, 0xffff0000, v162
v_lshlrev_b32 v86, 16, v163
v_and_b32 v87, 0xffff0000, v163
v_pk_add_f32 v[80:81], v[56:57], v[80:81]
v_pk_add_f32 v[82:83], v[58:59], v[82:83]
v_pk_add_f32 v[84:85], v[60:61], v[84:85]
v_pk_add_f32 v[86:87], v[62:63], v[86:87]
K 80,80,81
K 81,82,83
K 82,84,85
K 83,86,87
buffer_store_dwordx4 v[80:83], v20, s[20:23], s73 offen nt
s_branch .LL_128%=
.LL_169%=:
s_waitcnt vmcnt(0)
W 0
Zvva 80,4,32
Zvva 84,8,32
Mvva 80,4,36
Mvva 84,8,36
Mvva 80,4,40
Mvva 84,8,40
Mvva 80,4,44
Mvva 84,8,44
Mvva 80,4,48
Mvva 84,8,48
Mvva 80,4,52
Mvva 84,8,52
Mvva 80,4,56
Mvva 84,8,56
Mvva 80,4,60
Mvva 84,8,60
s_nop 6
K 160,80,81
K 161,82,83
K 162,84,85
K 163,86,87
v_lshlrev_b32 v80, 16, v160
v_and_b32 v81, 0xffff0000, v160
v_lshlrev_b32 v82, 16, v161
v_and_b32 v83, 0xffff0000, v161
v_lshlrev_b32 v84, 16, v162
v_and_b32 v85, 0xffff0000, v162
v_lshlrev_b32 v86, 16, v163
v_and_b32 v87, 0xffff0000, v163
v_pk_add_f32 v[80:81], v[48:49], v[80:81]
v_pk_add_f32 v[82:83], v[50:51], v[82:83]
v_pk_add_f32 v[84:85], v[52:53], v[84:85]
v_pk_add_f32 v[86:87], v[54:55], v[86:87]
K 160,80,81
K 161,82,83
K 162,84,85
K 163,86,87
v_lshlrev_b32 v80, 16, v160
v_and_b32 v81, 0xffff0000, v160
v_lshlrev_b32 v82, 16, v161
v_and_b32 v83, 0xffff0000, v161
v_lshlrev_b32 v84, 16, v162
v_and_b32 v85, 0xffff0000, v162
v_lshlrev_b32 v86, 16, v163
v_and_b32 v87, 0xffff0000, v163
v_pk_add_f32 v[80:81], v[56:57], v[80:81]
v_pk_add_f32 v[82:83], v[58:59], v[82:83]
v_pk_add_f32 v[84:85], v[60:61], v[84:85]
v_pk_add_f32 v[86:87], v[62:63], v[86:87]
K 80,80,81
K 81,82,83
K 82,84,85
K 83,86,87
buffer_store_dwordx4 v[80:83], v20, s[20:23], s73 offen nt
s_branch .LL_128%=
.LL_129%=:
s_mov_b32 s84, 0
.LL_171%=:
s_or_b32 s70, s71, s66
s_andn2_b32 s69, s67, s71
s_and_b32 s69, s69, 0xffff
s_cbranch_scc0 .LL_174%=
s_and_b32 s80, s69, 255
s_cbranch_scc0 .LL_175%=
s_bitcmp1_b32 s69, 0
s_cbranch_scc0 .LL_176%=
s_cmp_eq_u32 s59, 0
s_cbranch_scc0 .LL_177%=
Ra 0,21,0
Ra 216,1,4096
s_branch .LL_176%=
.LL_177%=:
buffer_load_dwordx4 a[0:3], v1, s[8:11], s78 offen offset:0 sc1
buffer_load_dwordx4 a[216:219], v1, s[8:11], s65 offen offset:0 sc1
.LL_176%=:
s_bitcmp1_b32 s69, 1
s_cbranch_scc0 .LL_178%=
s_cmp_eq_u32 s59, 1
s_cbranch_scc0 .LL_179%=
Ra 4,21,0
Ra 220,1,4096
s_branch .LL_178%=
.LL_179%=:
buffer_load_dwordx4 a[4:7], v1, s[8:11], s78 offen offset:1024 sc1
buffer_load_dwordx4 a[220:223], v1, s[8:11], s65 offen offset:1024 sc1
.LL_178%=:
s_bitcmp1_b32 s69, 2
s_cbranch_scc0 .LL_180%=
s_cmp_eq_u32 s59, 2
s_cbranch_scc0 .LL_181%=
Ra 8,21,0
Ra 224,1,4096
s_branch .LL_180%=
.LL_181%=:
buffer_load_dwordx4 a[8:11], v1, s[8:11], s78 offen offset:2048 sc1
buffer_load_dwordx4 a[224:227], v1, s[8:11], s65 offen offset:2048 sc1
.LL_180%=:
s_bitcmp1_b32 s69, 3
s_cbranch_scc0 .LL_182%=
s_cmp_eq_u32 s59, 3
s_cbranch_scc0 .LL_183%=
Ra 12,21,0
Ra 228,1,4096
s_branch .LL_182%=
.LL_183%=:
buffer_load_dwordx4 a[12:15], v1, s[8:11], s78 offen offset:3072 sc1
buffer_load_dwordx4 a[228:231], v1, s[8:11], s65 offen offset:3072 sc1
.LL_182%=:
s_bitcmp1_b32 s69, 4
s_cbranch_scc0 .LL_184%=
s_cmp_eq_u32 s59, 4
s_cbranch_scc0 .LL_185%=
Ra 16,21,0
Ra 232,1,4096
s_branch .LL_184%=
.LL_185%=:
buffer_load_dwordx4 a[16:19], v19, s[8:11], s78 offen offset:0 sc1
buffer_load_dwordx4 a[232:235], v19, s[8:11], s65 offen offset:0 sc1
.LL_184%=:
s_bitcmp1_b32 s69, 5
s_cbranch_scc0 .LL_186%=
s_cmp_eq_u32 s59, 5
s_cbranch_scc0 .LL_187%=
Ra 20,21,0
Ra 236,1,4096
s_branch .LL_186%=
.LL_187%=:
buffer_load_dwordx4 a[20:23], v19, s[8:11], s78 offen offset:1024 sc1
buffer_load_dwordx4 a[236:239], v19, s[8:11], s65 offen offset:1024 sc1
.LL_186%=:
s_bitcmp1_b32 s69, 6
s_cbranch_scc0 .LL_188%=
s_cmp_eq_u32 s59, 6
s_cbranch_scc0 .LL_189%=
Ra 24,21,0
Ra 240,1,4096
s_branch .LL_188%=
.LL_189%=:
buffer_load_dwordx4 a[24:27], v19, s[8:11], s78 offen offset:2048 sc1
buffer_load_dwordx4 a[240:243], v19, s[8:11], s65 offen offset:2048 sc1
.LL_188%=:
s_bitcmp1_b32 s69, 7
s_cbranch_scc0 .LL_190%=
s_cmp_eq_u32 s59, 7
s_cbranch_scc0 .LL_191%=
Ra 28,21,0
Ra 244,1,4096
s_branch .LL_190%=
.LL_191%=:
buffer_load_dwordx4 a[28:31], v19, s[8:11], s78 offen offset:3072 sc1
buffer_load_dwordx4 a[244:247], v19, s[8:11], s65 offen offset:3072 sc1
.LL_190%=:
.LL_175%=:
s_and_b32 s80, s69, 65280
s_cbranch_scc0 .LL_192%=
s_bitcmp1_b32 s69, 8
s_cbranch_scc0 .LL_193%=
s_cmp_eq_u32 s59, 0
s_cbranch_scc0 .LL_194%=
Ra 32,21,5120
s_branch .LL_193%=
.LL_194%=:
buffer_load_dwordx4 a[32:35], v1, s[8:11], s79 offen offset:0 sc1
.LL_193%=:
s_bitcmp1_b32 s69, 9
s_cbranch_scc0 .LL_195%=
s_cmp_eq_u32 s59, 1
s_cbranch_scc0 .LL_196%=
Ra 36,21,5120
s_branch .LL_195%=
.LL_196%=:
buffer_load_dwordx4 a[36:39], v1, s[8:11], s79 offen offset:1024 sc1
.LL_195%=:
s_bitcmp1_b32 s69, 10
s_cbranch_scc0 .LL_197%=
s_cmp_eq_u32 s59, 2
s_cbranch_scc0 .LL_198%=
Ra 40,21,5120
s_branch .LL_197%=
.LL_198%=:
buffer_load_dwordx4 a[40:43], v1, s[8:11], s79 offen offset:2048 sc1
.LL_197%=:
s_bitcmp1_b32 s69, 11
s_cbranch_scc0 .LL_199%=
s_cmp_eq_u32 s59, 3
s_cbranch_scc0 .LL_200%=
Ra 44,21,5120
s_branch .LL_199%=
.LL_200%=:
buffer_load_dwordx4 a[44:47], v1, s[8:11], s79 offen offset:3072 sc1
.LL_199%=:
s_bitcmp1_b32 s69, 12
s_cbranch_scc0 .LL_201%=
s_cmp_eq_u32 s59, 4
s_cbranch_scc0 .LL_202%=
Ra 48,21,5120
s_branch .LL_201%=
.LL_202%=:
buffer_load_dwordx4 a[48:51], v19, s[8:11], s79 offen offset:0 sc1
.LL_201%=:
s_bitcmp1_b32 s69, 13
s_cbranch_scc0 .LL_203%=
s_cmp_eq_u32 s59, 5
s_cbranch_scc0 .LL_204%=
Ra 52,21,5120
s_branch .LL_203%=
.LL_204%=:
buffer_load_dwordx4 a[52:55], v19, s[8:11], s79 offen offset:1024 sc1
.LL_203%=:
s_bitcmp1_b32 s69, 14
s_cbranch_scc0 .LL_205%=
s_cmp_eq_u32 s59, 6
s_cbranch_scc0 .LL_206%=
Ra 56,21,5120
s_branch .LL_205%=
.LL_206%=:
buffer_load_dwordx4 a[56:59], v19, s[8:11], s79 offen offset:2048 sc1
.LL_205%=:
s_bitcmp1_b32 s69, 15
s_cbranch_scc0 .LL_207%=
s_cmp_eq_u32 s59, 7
s_cbranch_scc0 .LL_208%=
Ra 60,21,5120
s_branch .LL_207%=
.LL_208%=:
buffer_load_dwordx4 a[60:63], v19, s[8:11], s79 offen offset:3072 sc1
.LL_207%=:
.LL_192%=:
s_or_b32 s71, s71, s69
.LL_174%=:
s_andn2_b32 s80, 0xffff, s71
s_cbranch_scc0 .LL_173%=
s_sleep 4
buffer_load_dwordx2 v[16:17], v18, s[8:11], s63 offen sc1
s_and_b32 s80, s70, 0x100ff
s_cmp_eq_u32 s80, 0xff
s_cbranch_scc0 .LL_172%=
W 0
Zvva 80,4,0
Zvva 84,8,0
Zvva 88,4,216
Zvva 92,8,216
Mvva 80,4,4
Mvva 84,8,4
Mvva 88,4,220
Mvva 92,8,220
Mvva 80,4,8
Mvva 84,8,8
Mvva 88,4,224
Mvva 92,8,224
Mvva 80,4,12
Mvva 84,8,12
Mvva 88,4,228
Mvva 92,8,228
Mvva 80,4,16
Mvva 84,8,16
Mvva 88,4,232
Mvva 92,8,232
Mvva 80,4,20
Mvva 84,8,20
Mvva 88,4,236
Mvva 92,8,236
Mvva 80,4,24
Mvva 84,8,24
Mvva 88,4,240
Mvva 92,8,240
Mvva 80,4,28
Mvva 84,8,28
Mvva 88,4,244
Mvva 92,8,244
s_nop 4
K 160,80,81
K 161,82,83
K 162,84,85
K 163,86,87
v_lshlrev_b32 v80, 16, v160
v_and_b32 v81, 0xffff0000, v160
v_lshlrev_b32 v82, 16, v161
v_and_b32 v83, 0xffff0000, v161
v_lshlrev_b32 v84, 16, v162
v_and_b32 v85, 0xffff0000, v162
v_lshlrev_b32 v86, 16, v163
v_and_b32 v87, 0xffff0000, v163
v_pk_add_f32 v[80:81], v[32:33], v[80:81]
v_pk_add_f32 v[82:83], v[34:35], v[82:83]
v_pk_add_f32 v[84:85], v[36:37], v[84:85]
v_pk_add_f32 v[86:87], v[38:39], v[86:87]
K 160,80,81
K 161,82,83
K 162,84,85
K 163,86,87
v_lshlrev_b32 v80, 16, v160
v_and_b32 v81, 0xffff0000, v160
v_lshlrev_b32 v82, 16, v161
v_and_b32 v83, 0xffff0000, v161
v_lshlrev_b32 v84, 16, v162
v_and_b32 v85, 0xffff0000, v162
v_lshlrev_b32 v86, 16, v163
v_and_b32 v87, 0xffff0000, v163
v_pk_add_f32 v[80:81], v[40:41], v[80:81]
v_pk_add_f32 v[82:83], v[42:43], v[82:83]
)"
R"(v_pk_add_f32 v[84:85], v[44:45], v[84:85]
v_pk_add_f32 v[86:87], v[46:47], v[86:87]
K 80,80,81
K 81,82,83
K 82,84,85
K 83,86,87
buffer_store_dwordx4 v[80:83], v20, s[20:23], s72 offen nt
K 160,88,89
K 161,90,91
K 162,92,93
K 163,94,95
v_lshlrev_b32 v88, 16, v160
v_and_b32 v89, 0xffff0000, v160
v_lshlrev_b32 v90, 16, v161
v_and_b32 v91, 0xffff0000, v161
v_lshlrev_b32 v92, 16, v162
v_and_b32 v93, 0xffff0000, v162
v_lshlrev_b32 v94, 16, v163
v_and_b32 v95, 0xffff0000, v163
v_pk_add_f32 v[88:89], v[64:65], v[88:89]
v_pk_add_f32 v[90:91], v[66:67], v[90:91]
v_pk_add_f32 v[92:93], v[68:69], v[92:93]
v_pk_add_f32 v[94:95], v[70:71], v[94:95]
K 160,88,89
K 161,90,91
K 162,92,93
K 163,94,95
v_lshlrev_b32 v88, 16, v160
v_and_b32 v89, 0xffff0000, v160
v_lshlrev_b32 v90, 16, v161
v_and_b32 v91, 0xffff0000, v161
v_lshlrev_b32 v92, 16, v162
v_and_b32 v93, 0xffff0000, v162
v_lshlrev_b32 v94, 16, v163
v_and_b32 v95, 0xffff0000, v163
v_pk_add_f32 v[88:89], v[72:73], v[88:89]
v_pk_add_f32 v[90:91], v[74:75], v[90:91]
v_pk_add_f32 v[92:93], v[76:77], v[92:93]
v_pk_add_f32 v[94:95], v[78:79], v[94:95]
K 88,88,89
K 89,90,91
K 90,92,93
K 91,94,95
buffer_store_dwordx4 v[88:91], v20, s[20:23], s74 offen nt
s_bitset1_b32 s71, 16
.LL_172%=:
s_add_u32 s84, s84, 1
s_waitcnt vmcnt(0)
v_cmp_eq_u64 vcc, s[12:13], v[16:17]
s_or_b32 s67, vcc_lo, s66
s_cmp_lt_u32 s84, 4194304
s_cbranch_scc1 .LL_171%=
.LL_173%=:
s_bitcmp1_b32 s71, 16
s_cbranch_scc1 .LL_209%=
s_and_b32 s80, s70, 0xff
s_cmp_eq_u32 s80, 0xff
s_cbranch_scc1 .LL_210%=
s_waitcnt vmcnt(0)
.LL_210%=:
W 0
Zvva 80,4,0
Zvva 84,8,0
Zvva 88,4,216
Zvva 92,8,216
Mvva 80,4,4
Mvva 84,8,4
Mvva 88,4,220
Mvva 92,8,220
Mvva 80,4,8
Mvva 84,8,8
Mvva 88,4,224
Mvva 92,8,224
Mvva 80,4,12
Mvva 84,8,12
Mvva 88,4,228
Mvva 92,8,228
Mvva 80,4,16
Mvva 84,8,16
Mvva 88,4,232
Mvva 92,8,232
Mvva 80,4,20
Mvva 84,8,20
Mvva 88,4,236
Mvva 92,8,236
Mvva 80,4,24
Mvva 84,8,24
Mvva 88,4,240
Mvva 92,8,240
Mvva 80,4,28
Mvva 84,8,28
Mvva 88,4,244
Mvva 92,8,244
s_nop 4
K 160,80,81
K 161,82,83
K 162,84,85
K 163,86,87
v_lshlrev_b32 v80, 16, v160
v_and_b32 v81, 0xffff0000, v160
v_lshlrev_b32 v82, 16, v161
v_and_b32 v83, 0xffff0000, v161
v_lshlrev_b32 v84, 16, v162
v_and_b32 v85, 0xffff0000, v162
v_lshlrev_b32 v86, 16, v163
v_and_b32 v87, 0xffff0000, v163
v_pk_add_f32 v[80:81], v[32:33], v[80:81]
v_pk_add_f32 v[82:83], v[34:35], v[82:83]
v_pk_add_f32 v[84:85], v[36:37], v[84:85]
v_pk_add_f32 v[86:87], v[38:39], v[86:87]
K 160,80,81
K 161,82,83
K 162,84,85
K 163,86,87
v_lshlrev_b32 v80, 16, v160
v_and_b32 v81, 0xffff0000, v160
v_lshlrev_b32 v82, 16, v161
v_and_b32 v83, 0xffff0000, v161
v_lshlrev_b32 v84, 16, v162
v_and_b32 v85, 0xffff0000, v162
v_lshlrev_b32 v86, 16, v163
v_and_b32 v87, 0xffff0000, v163
v_pk_add_f32 v[80:81], v[40:41], v[80:81]
v_pk_add_f32 v[82:83], v[42:43], v[82:83]
v_pk_add_f32 v[84:85], v[44:45], v[84:85]
v_pk_add_f32 v[86:87], v[46:47], v[86:87]
K 80,80,81
K 81,82,83
K 82,84,85
K 83,86,87
buffer_store_dwordx4 v[80:83], v20, s[20:23], s72 offen nt
K 160,88,89
K 161,90,91
K 162,92,93
K 163,94,95
v_lshlrev_b32 v88, 16, v160
v_and_b32 v89, 0xffff0000, v160
v_lshlrev_b32 v90, 16, v161
v_and_b32 v91, 0xffff0000, v161
v_lshlrev_b32 v92, 16, v162
v_and_b32 v93, 0xffff0000, v162
v_lshlrev_b32 v94, 16, v163
v_and_b32 v95, 0xffff0000, v163
v_pk_add_f32 v[88:89], v[64:65], v[88:89]
v_pk_add_f32 v[90:91], v[66:67], v[90:91]
v_pk_add_f32 v[92:93], v[68:69], v[92:93]
v_pk_add_f32 v[94:95], v[70:71], v[94:95]
K 160,88,89
K 161,90,91
K 162,92,93
K 163,94,95
v_lshlrev_b32 v88, 16, v160
v_and_b32 v89, 0xffff0000, v160
v_lshlrev_b32 v90, 16, v161
v_and_b32 v91, 0xffff0000, v161
v_lshlrev_b32 v92, 16, v162
v_and_b32 v93, 0xffff0000, v162
v_lshlrev_b32 v94, 16, v163
v_and_b32 v95, 0xffff0000, v163
v_pk_add_f32 v[88:89], v[72:73], v[88:89]
v_pk_add_f32 v[90:91], v[74:75], v[90:91]
v_pk_add_f32 v[92:93], v[76:77], v[92:93]
v_pk_add_f32 v[94:95], v[78:79], v[94:95]
K 88,88,89
K 89,90,91
K 90,92,93
K 91,94,95
buffer_store_dwordx4 v[88:91], v20, s[20:23], s74 offen nt
s_waitcnt vmcnt(2)
W 0
Zvva 80,4,32
Zvva 84,8,32
Mvva 80,4,36
Mvva 84,8,36
Mvva 80,4,40
Mvva 84,8,40
Mvva 80,4,44
Mvva 84,8,44
Mvva 80,4,48
Mvva 84,8,48
Mvva 80,4,52
Mvva 84,8,52
Mvva 80,4,56
Mvva 84,8,56
Mvva 80,4,60
Mvva 84,8,60
s_nop 6
K 160,80,81
K 161,82,83
K 162,84,85
K 163,86,87
v_lshlrev_b32 v80, 16, v160
v_and_b32 v81, 0xffff0000, v160
v_lshlrev_b32 v82, 16, v161
v_and_b32 v83, 0xffff0000, v161
v_lshlrev_b32 v84, 16, v162
v_and_b32 v85, 0xffff0000, v162
v_lshlrev_b32 v86, 16, v163
v_and_b32 v87, 0xffff0000, v163
v_pk_add_f32 v[80:81], v[48:49], v[80:81]
v_pk_add_f32 v[82:83], v[50:51], v[82:83]
v_pk_add_f32 v[84:85], v[52:53], v[84:85]
v_pk_add_f32 v[86:87], v[54:55], v[86:87]
K 160,80,81
K 161,82,83
K 162,84,85
K 163,86,87
v_lshlrev_b32 v80, 16, v160
v_and_b32 v81, 0xffff0000, v160
v_lshlrev_b32 v82, 16, v161
v_and_b32 v83, 0xffff0000, v161
v_lshlrev_b32 v84, 16, v162
v_and_b32 v85, 0xffff0000, v162
v_lshlrev_b32 v86, 16, v163
v_and_b32 v87, 0xffff0000, v163
v_pk_add_f32 v[80:81], v[56:57], v[80:81]
v_pk_add_f32 v[82:83], v[58:59], v[82:83]
v_pk_add_f32 v[84:85], v[60:61], v[84:85]
v_pk_add_f32 v[86:87], v[62:63], v[86:87]
K 80,80,81
K 81,82,83
K 82,84,85
K 83,86,87
buffer_store_dwordx4 v[80:83], v20, s[20:23], s73 offen nt
s_branch .LL_128%=
.LL_209%=:
s_waitcnt vmcnt(0)
W 0
Zvva 80,4,32
Zvva 84,8,32
Mvva 80,4,36
Mvva 84,8,36
Mvva 80,4,40
Mvva 84,8,40
Mvva 80,4,44
Mvva 84,8,44
Mvva 80,4,48
Mvva 84,8,48
Mvva 80,4,52
Mvva 84,8,52
Mvva 80,4,56
Mvva 84,8,56
Mvva 80,4,60
Mvva 84,8,60
s_nop 6
K 160,80,81
K 161,82,83
K 162,84,85
K 163,86,87
v_lshlrev_b32 v80, 16, v160
v_and_b32 v81, 0xffff0000, v160
v_lshlrev_b32 v82, 16, v161
v_and_b32 v83, 0xffff0000, v161
v_lshlrev_b32 v84, 16, v162
v_and_b32 v85, 0xffff0000, v162
v_lshlrev_b32 v86, 16, v163
v_and_b32 v87, 0xffff0000, v163
v_pk_add_f32 v[80:81], v[48:49], v[80:81]
v_pk_add_f32 v[82:83], v[50:51], v[82:83]
v_pk_add_f32 v[84:85], v[52:53], v[84:85]
v_pk_add_f32 v[86:87], v[54:55], v[86:87]
K 160,80,81
K 161,82,83
K 162,84,85
K 163,86,87
v_lshlrev_b32 v80, 16, v160
v_and_b32 v81, 0xffff0000, v160
v_lshlrev_b32 v82, 16, v161
v_and_b32 v83, 0xffff0000, v161
v_lshlrev_b32 v84, 16, v162
v_and_b32 v85, 0xffff0000, v162
v_lshlrev_b32 v86, 16, v163
v_and_b32 v87, 0xffff0000, v163
v_pk_add_f32 v[80:81], v[56:57], v[80:81]
v_pk_add_f32 v[82:83], v[58:59], v[82:83]
v_pk_add_f32 v[84:85], v[60:61], v[84:85]
v_pk_add_f32 v[86:87], v[62:63], v[86:87]
K 80,80,81
K 81,82,83
K 82,84,85
K 83,86,87
buffer_store_dwordx4 v[80:83], v20, s[20:23], s73 offen nt
s_branch .LL_128%=
.LL_130%=:
s_mov_b32 s84, 0
.LL_211%=:
s_or_b32 s70, s71, s66
s_andn2_b32 s69, s67, s71
s_and_b32 s69, s69, 0xffff
s_cbranch_scc0 .LL_214%=
s_and_b32 s80, s69, 255
s_cbranch_scc0 .LL_215%=
s_bitcmp1_b32 s69, 0
s_cbranch_scc0 .LL_216%=
s_cmp_eq_u32 s59, 0
s_cbranch_scc0 .LL_217%=
Ra 0,21,0
s_branch .LL_216%=
.LL_217%=:
buffer_load_dwordx4 a[0:3], v1, s[8:11], s78 offen offset:0 sc1
.LL_216%=:
s_bitcmp1_b32 s69, 1
s_cbranch_scc0 .LL_218%=
s_cmp_eq_u32 s59, 1
s_cbranch_scc0 .LL_219%=
Ra 4,21,0
s_branch .LL_218%=
.LL_219%=:
buffer_load_dwordx4 a[4:7], v1, s[8:11], s78 offen offset:1024 sc1
.LL_218%=:
s_bitcmp1_b32 s69, 2
s_cbranch_scc0 .LL_220%=
s_cmp_eq_u32 s59, 2
s_cbranch_scc0 .LL_221%=
Ra 8,21,0
s_branch .LL_220%=
.LL_221%=:
buffer_load_dwordx4 a[8:11], v1, s[8:11], s78 offen offset:2048 sc1
.LL_220%=:
s_bitcmp1_b32 s69, 3
s_cbranch_scc0 .LL_222%=
s_cmp_eq_u32 s59, 3
s_cbranch_scc0 .LL_223%=
Ra 12,21,0
s_branch .LL_222%=
.LL_223%=:
buffer_load_dwordx4 a[12:15], v1, s[8:11], s78 offen offset:3072 sc1
.LL_222%=:
s_bitcmp1_b32 s69, 4
s_cbranch_scc0 .LL_224%=
s_cmp_eq_u32 s59, 4
s_cbranch_scc0 .LL_225%=
Ra 16,21,0
s_branch .LL_224%=
.LL_225%=:
buffer_load_dwordx4 a[16:19], v19, s[8:11], s78 offen offset:0 sc1
.LL_224%=:
s_bitcmp1_b32 s69, 5
s_cbranch_scc0 .LL_226%=
s_cmp_eq_u32 s59, 5
s_cbranch_scc0 .LL_227%=
Ra 20,21,0
s_branch .LL_226%=
.LL_227%=:
buffer_load_dwordx4 a[20:23], v19, s[8:11], s78 offen offset:1024 sc1
.LL_226%=:
s_bitcmp1_b32 s69, 6
s_cbranch_scc0 .LL_228%=
s_cmp_eq_u32 s59, 6
s_cbranch_scc0 .LL_229%=
Ra 24,21,0
s_branch .LL_228%=
.LL_229%=:
buffer_load_dwordx4 a[24:27], v19, s[8:11], s78 offen offset:2048 sc1
.LL_228%=:
s_bitcmp1_b32 s69, 7
s_cbranch_scc0 .LL_230%=
s_cmp_eq_u32 s59, 7
s_cbranch_scc0 .LL_231%=
Ra 28,21,0
s_branch .LL_230%=
.LL_231%=:
buffer_load_dwordx4 a[28:31], v19, s[8:11], s78 offen offset:3072 sc1
.LL_230%=:
.LL_215%=:
s_and_b32 s80, s69, 65280
s_cbranch_scc0 .LL_232%=
s_bitcmp1_b32 s69, 8
s_cbranch_scc0 .LL_233%=
s_cmp_eq_u32 s59, 0
s_cbranch_scc0 .LL_234%=
Ra 32,21,5120
Ra 216,1,9216
s_branch .LL_233%=
.LL_234%=:
buffer_load_dwordx4 a[32:35], v1, s[8:11], s79 offen offset:0 sc1
buffer_load_dwordx4 a[216:219], v1, s[8:11], s65 offen offset:0 sc1
.LL_233%=:
s_bitcmp1_b32 s69, 9
s_cbranch_scc0 .LL_235%=
s_cmp_eq_u32 s59, 1
s_cbranch_scc0 .LL_236%=
Ra 36,21,5120
Ra 220,1,9216
s_branch .LL_235%=
.LL_236%=:
buffer_load_dwordx4 a[36:39], v1, s[8:11], s79 offen offset:1024 sc1
buffer_load_dwordx4 a[220:223], v1, s[8:11], s65 offen offset:1024 sc1
.LL_235%=:
s_bitcmp1_b32 s69, 10
s_cbranch_scc0 .LL_237%=
s_cmp_eq_u32 s59, 2
s_cbranch_scc0 .LL_238%=
Ra 40,21,5120
Ra 224,1,9216
s_branch .LL_237%=
.LL_238%=:
buffer_load_dwordx4 a[40:43], v1, s[8:11], s79 offen offset:2048 sc1
buffer_load_dwordx4 a[224:227], v1, s[8:11], s65 offen offset:2048 sc1
.LL_237%=:
)"
R"(s_bitcmp1_b32 s69, 11
s_cbranch_scc0 .LL_239%=
s_cmp_eq_u32 s59, 3
s_cbranch_scc0 .LL_240%=
Ra 44,21,5120
Ra 228,1,9216
s_branch .LL_239%=
.LL_240%=:
buffer_load_dwordx4 a[44:47], v1, s[8:11], s79 offen offset:3072 sc1
buffer_load_dwordx4 a[228:231], v1, s[8:11], s65 offen offset:3072 sc1
.LL_239%=:
s_bitcmp1_b32 s69, 12
s_cbranch_scc0 .LL_241%=
s_cmp_eq_u32 s59, 4
s_cbranch_scc0 .LL_242%=
Ra 48,21,5120
Ra 232,1,9216
s_branch .LL_241%=
.LL_242%=:
buffer_load_dwordx4 a[48:51], v19, s[8:11], s79 offen offset:0 sc1
buffer_load_dwordx4 a[232:235], v19, s[8:11], s65 offen offset:0 sc1
.LL_241%=:
s_bitcmp1_b32 s69, 13
s_cbranch_scc0 .LL_243%=
s_cmp_eq_u32 s59, 5
s_cbranch_scc0 .LL_244%=
Ra 52,21,5120
Ra 236,1,9216
s_branch .LL_243%=
.LL_244%=:
buffer_load_dwordx4 a[52:55], v19, s[8:11], s79 offen offset:1024 sc1
buffer_load_dwordx4 a[236:239], v19, s[8:11], s65 offen offset:1024 sc1
.LL_243%=:
s_bitcmp1_b32 s69, 14
s_cbranch_scc0 .LL_245%=
s_cmp_eq_u32 s59, 6
s_cbranch_scc0 .LL_246%=
Ra 56,21,5120
Ra 240,1,9216
s_branch .LL_245%=
.LL_246%=:
buffer_load_dwordx4 a[56:59], v19, s[8:11], s79 offen offset:2048 sc1
buffer_load_dwordx4 a[240:243], v19, s[8:11], s65 offen offset:2048 sc1
.LL_245%=:
s_bitcmp1_b32 s69, 15
s_cbranch_scc0 .LL_247%=
s_cmp_eq_u32 s59, 7
s_cbranch_scc0 .LL_248%=
Ra 60,21,5120
Ra 244,1,9216
s_branch .LL_247%=
.LL_248%=:
buffer_load_dwordx4 a[60:63], v19, s[8:11], s79 offen offset:3072 sc1
buffer_load_dwordx4 a[244:247], v19, s[8:11], s65 offen offset:3072 sc1
.LL_247%=:
.LL_232%=:
s_or_b32 s71, s71, s69
.LL_214%=:
s_andn2_b32 s80, 0xffff, s71
s_cbranch_scc0 .LL_213%=
s_sleep 4
buffer_load_dwordx2 v[16:17], v18, s[8:11], s63 offen sc1
s_and_b32 s80, s70, 0x100ff
s_cmp_eq_u32 s80, 0xff
s_cbranch_scc0 .LL_212%=
W 0
Zvva 80,4,0
Zvva 84,8,0
Mvva 80,4,4
Mvva 84,8,4
Mvva 80,4,8
Mvva 84,8,8
Mvva 80,4,12
Mvva 84,8,12
Mvva 80,4,16
Mvva 84,8,16
Mvva 80,4,20
Mvva 84,8,20
Mvva 80,4,24
Mvva 84,8,24
Mvva 80,4,28
Mvva 84,8,28
s_nop 6
K 160,80,81
K 161,82,83
K 162,84,85
K 163,86,87
v_lshlrev_b32 v80, 16, v160
v_and_b32 v81, 0xffff0000, v160
v_lshlrev_b32 v82, 16, v161
v_and_b32 v83, 0xffff0000, v161
v_lshlrev_b32 v84, 16, v162
v_and_b32 v85, 0xffff0000, v162
v_lshlrev_b32 v86, 16, v163
v_and_b32 v87, 0xffff0000, v163
v_pk_add_f32 v[80:81], v[32:33], v[80:81]
v_pk_add_f32 v[82:83], v[34:35], v[82:83]
v_pk_add_f32 v[84:85], v[36:37], v[84:85]
v_pk_add_f32 v[86:87], v[38:39], v[86:87]
K 160,80,81
K 161,82,83
K 162,84,85
K 163,86,87
v_lshlrev_b32 v80, 16, v160
v_and_b32 v81, 0xffff0000, v160
v_lshlrev_b32 v82, 16, v161
v_and_b32 v83, 0xffff0000, v161
v_lshlrev_b32 v84, 16, v162
v_and_b32 v85, 0xffff0000, v162
v_lshlrev_b32 v86, 16, v163
v_and_b32 v87, 0xffff0000, v163
v_pk_add_f32 v[80:81], v[40:41], v[80:81]
v_pk_add_f32 v[82:83], v[42:43], v[82:83]
v_pk_add_f32 v[84:85], v[44:45], v[84:85]
v_pk_add_f32 v[86:87], v[46:47], v[86:87]
K 80,80,81
K 81,82,83
K 82,84,85
K 83,86,87
buffer_store_dwordx4 v[80:83], v20, s[20:23], s72 offen nt
s_bitset1_b32 s71, 16
.LL_212%=:
s_add_u32 s84, s84, 1
s_waitcnt vmcnt(0)
v_cmp_eq_u64 vcc, s[12:13], v[16:17]
s_or_b32 s67, vcc_lo, s66
s_cmp_lt_u32 s84, 4194304
s_cbranch_scc1 .LL_211%=
.LL_213%=:
s_bitcmp1_b32 s71, 16
s_cbranch_scc1 .LL_249%=
s_and_b32 s80, s70, 0xff
s_cmp_eq_u32 s80, 0xff
s_cbranch_scc1 .LL_250%=
s_waitcnt vmcnt(0)
.LL_250%=:
W 0
Zvva 80,4,0
Zvva 84,8,0
Mvva 80,4,4
Mvva 84,8,4
Mvva 80,4,8
Mvva 84,8,8
Mvva 80,4,12
Mvva 84,8,12
Mvva 80,4,16
Mvva 84,8,16
Mvva 80,4,20
Mvva 84,8,20
Mvva 80,4,24
Mvva 84,8,24
Mvva 80,4,28
Mvva 84,8,28
s_nop 6
K 160,80,81
K 161,82,83
K 162,84,85
K 163,86,87
v_lshlrev_b32 v80, 16, v160
v_and_b32 v81, 0xffff0000, v160
v_lshlrev_b32 v82, 16, v161
v_and_b32 v83, 0xffff0000, v161
v_lshlrev_b32 v84, 16, v162
v_and_b32 v85, 0xffff0000, v162
v_lshlrev_b32 v86, 16, v163
v_and_b32 v87, 0xffff0000, v163
v_pk_add_f32 v[80:81], v[32:33], v[80:81]
v_pk_add_f32 v[82:83], v[34:35], v[82:83]
v_pk_add_f32 v[84:85], v[36:37], v[84:85]
v_pk_add_f32 v[86:87], v[38:39], v[86:87]
K 160,80,81
K 161,82,83
K 162,84,85
K 163,86,87
v_lshlrev_b32 v80, 16, v160
v_and_b32 v81, 0xffff0000, v160
v_lshlrev_b32 v82, 16, v161
v_and_b32 v83, 0xffff0000, v161
v_lshlrev_b32 v84, 16, v162
v_and_b32 v85, 0xffff0000, v162
v_lshlrev_b32 v86, 16, v163
v_and_b32 v87, 0xffff0000, v163
v_pk_add_f32 v[80:81], v[40:41], v[80:81]
v_pk_add_f32 v[82:83], v[42:43], v[82:83]
v_pk_add_f32 v[84:85], v[44:45], v[84:85]
v_pk_add_f32 v[86:87], v[46:47], v[86:87]
K 80,80,81
K 81,82,83
K 82,84,85
K 83,86,87
buffer_store_dwordx4 v[80:83], v20, s[20:23], s72 offen nt
s_waitcnt vmcnt(1)
W 0
Zvva 80,4,32
Zvva 84,8,32
Zvva 88,4,216
Zvva 92,8,216
Mvva 80,4,36
Mvva 84,8,36
Mvva 88,4,220
Mvva 92,8,220
Mvva 80,4,40
Mvva 84,8,40
Mvva 88,4,224
Mvva 92,8,224
Mvva 80,4,44
Mvva 84,8,44
Mvva 88,4,228
Mvva 92,8,228
Mvva 80,4,48
Mvva 84,8,48
Mvva 88,4,232
Mvva 92,8,232
Mvva 80,4,52
Mvva 84,8,52
Mvva 88,4,236
Mvva 92,8,236
Mvva 80,4,56
Mvva 84,8,56
Mvva 88,4,240
Mvva 92,8,240
Mvva 80,4,60
Mvva 84,8,60
Mvva 88,4,244
Mvva 92,8,244
s_nop 4
K 160,80,81
K 161,82,83
K 162,84,85
K 163,86,87
v_lshlrev_b32 v80, 16, v160
v_and_b32 v81, 0xffff0000, v160
v_lshlrev_b32 v82, 16, v161
v_and_b32 v83, 0xffff0000, v161
v_lshlrev_b32 v84, 16, v162
v_and_b32 v85, 0xffff0000, v162
v_lshlrev_b32 v86, 16, v163
v_and_b32 v87, 0xffff0000, v163
v_pk_add_f32 v[80:81], v[48:49], v[80:81]
v_pk_add_f32 v[82:83], v[50:51], v[82:83]
v_pk_add_f32 v[84:85], v[52:53], v[84:85]
v_pk_add_f32 v[86:87], v[54:55], v[86:87]
K 160,80,81
K 161,82,83
K 162,84,85
K 163,86,87
v_lshlrev_b32 v80, 16, v160
v_and_b32 v81, 0xffff0000, v160
v_lshlrev_b32 v82, 16, v161
v_and_b32 v83, 0xffff0000, v161
v_lshlrev_b32 v84, 16, v162
v_and_b32 v85, 0xffff0000, v162
v_lshlrev_b32 v86, 16, v163
v_and_b32 v87, 0xffff0000, v163
v_pk_add_f32 v[80:81], v[56:57], v[80:81]
v_pk_add_f32 v[82:83], v[58:59], v[82:83]
v_pk_add_f32 v[84:85], v[60:61], v[84:85]
v_pk_add_f32 v[86:87], v[62:63], v[86:87]
K 80,80,81
K 81,82,83
K 82,84,85
K 83,86,87
buffer_store_dwordx4 v[80:83], v20, s[20:23], s73 offen nt
K 160,88,89
K 161,90,91
K 162,92,93
K 163,94,95
v_lshlrev_b32 v88, 16, v160
v_and_b32 v89, 0xffff0000, v160
v_lshlrev_b32 v90, 16, v161
v_and_b32 v91, 0xffff0000, v161
v_lshlrev_b32 v92, 16, v162
v_and_b32 v93, 0xffff0000, v162
v_lshlrev_b32 v94, 16, v163
v_and_b32 v95, 0xffff0000, v163
v_pk_add_f32 v[88:89], v[64:65], v[88:89]
v_pk_add_f32 v[90:91], v[66:67], v[90:91]
v_pk_add_f32 v[92:93], v[68:69], v[92:93]
v_pk_add_f32 v[94:95], v[70:71], v[94:95]
K 160,88,89
K 161,90,91
K 162,92,93
K 163,94,95
v_lshlrev_b32 v88, 16, v160
v_and_b32 v89, 0xffff0000, v160
v_lshlrev_b32 v90, 16, v161
v_and_b32 v91, 0xffff0000, v161
v_lshlrev_b32 v92, 16, v162
v_and_b32 v93, 0xffff0000, v162
v_lshlrev_b32 v94, 16, v163
v_and_b32 v95, 0xffff0000, v163
v_pk_add_f32 v[88:89], v[72:73], v[88:89]
v_pk_add_f32 v[90:91], v[74:75], v[90:91]
v_pk_add_f32 v[92:93], v[76:77], v[92:93]
v_pk_add_f32 v[94:95], v[78:79], v[94:95]
K 88,88,89
K 89,90,91
K 90,92,93
K 91,94,95
buffer_store_dwordx4 v[88:91], v20, s[20:23], s74 offen nt
s_branch .LL_128%=
.LL_249%=:
s_waitcnt vmcnt(0)
W 0
Zvva 80,4,32
Zvva 84,8,32
Zvva 88,4,216
Zvva 92,8,216
Mvva 80,4,36
Mvva 84,8,36
Mvva 88,4,220
Mvva 92,8,220
Mvva 80,4,40
Mvva 84,8,40
Mvva 88,4,224
Mvva 92,8,224
Mvva 80,4,44
Mvva 84,8,44
Mvva 88,4,228
Mvva 92,8,228
Mvva 80,4,48
Mvva 84,8,48
Mvva 88,4,232
Mvva 92,8,232
Mvva 80,4,52
Mvva 84,8,52
Mvva 88,4,236
Mvva 92,8,236
Mvva 80,4,56
Mvva 84,8,56
Mvva 88,4,240
Mvva 92,8,240
Mvva 80,4,60
Mvva 84,8,60
Mvva 88,4,244
Mvva 92,8,244
s_nop 4
K 160,80,81
K 161,82,83
K 162,84,85
K 163,86,87
v_lshlrev_b32 v80, 16, v160
v_and_b32 v81, 0xffff0000, v160
v_lshlrev_b32 v82, 16, v161
v_and_b32 v83, 0xffff0000, v161
v_lshlrev_b32 v84, 16, v162
v_and_b32 v85, 0xffff0000, v162
v_lshlrev_b32 v86, 16, v163
v_and_b32 v87, 0xffff0000, v163
v_pk_add_f32 v[80:81], v[48:49], v[80:81]
v_pk_add_f32 v[82:83], v[50:51], v[82:83]
v_pk_add_f32 v[84:85], v[52:53], v[84:85]
v_pk_add_f32 v[86:87], v[54:55], v[86:87]
K 160,80,81
K 161,82,83
K 162,84,85
K 163,86,87
v_lshlrev_b32 v80, 16, v160
v_and_b32 v81, 0xffff0000, v160
v_lshlrev_b32 v82, 16, v161
v_and_b32 v83, 0xffff0000, v161
v_lshlrev_b32 v84, 16, v162
v_and_b32 v85, 0xffff0000, v162
v_lshlrev_b32 v86, 16, v163
v_and_b32 v87, 0xffff0000, v163
v_pk_add_f32 v[80:81], v[56:57], v[80:81]
v_pk_add_f32 v[82:83], v[58:59], v[82:83]
v_pk_add_f32 v[84:85], v[60:61], v[84:85]
v_pk_add_f32 v[86:87], v[62:63], v[86:87]
K 80,80,81
K 81,82,83
K 82,84,85
K 83,86,87
buffer_store_dwordx4 v[80:83], v20, s[20:23], s73 offen nt
K 160,88,89
K 161,90,91
K 162,92,93
K 163,94,95
v_lshlrev_b32 v88, 16, v160
v_and_b32 v89, 0xffff0000, v160
v_lshlrev_b32 v90, 16, v161
v_and_b32 v91, 0xffff0000, v161
v_lshlrev_b32 v92, 16, v162
v_and_b32 v93, 0xffff0000, v162
v_lshlrev_b32 v94, 16, v163
v_and_b32 v95, 0xffff0000, v163
v_pk_add_f32 v[88:89], v[64:65], v[88:89]
v_pk_add_f32 v[90:91], v[66:67], v[90:91]
v_pk_add_f32 v[92:93], v[68:69], v[92:93]
v_pk_add_f32 v[94:95], v[70:71], v[94:95]
K 160,88,89
K 161,90,91
K 162,92,93
K 163,94,95
v_lshlrev_b32 v88, 16, v160
v_and_b32 v89, 0xffff0000, v160
v_lshlrev_b32 v90, 16, v161
v_and_b32 v91, 0xffff0000, v161
v_lshlrev_b32 v92, 16, v162
v_and_b32 v93, 0xffff0000, v162
)"
R"(v_lshlrev_b32 v94, 16, v163
v_and_b32 v95, 0xffff0000, v163
v_pk_add_f32 v[88:89], v[72:73], v[88:89]
v_pk_add_f32 v[90:91], v[74:75], v[90:91]
v_pk_add_f32 v[92:93], v[76:77], v[92:93]
v_pk_add_f32 v[94:95], v[78:79], v[94:95]
K 88,88,89
K 89,90,91
K 90,92,93
K 91,94,95
buffer_store_dwordx4 v[88:91], v20, s[20:23], s74 offen nt
.LL_128%=:
s_lshl_b32 s80, s15, 2
s_add_u32 s80, s80, s14
s_cmp_eq_u32 s80, 4
s_cbranch_scc0 .LL_251%=
v_mov_b64 v[28:29], s[12:13]
s_mov_b64 exec, 1
buffer_store_dwordx2 v[28:29], off, s[8:11], 0 sc1
s_mov_b64 exec, -1
.LL_251%=:
s_branch .Lend_252%=
.Lup_121%=:
s_waitcnt vmcnt(3)
s_mov_b32 s84, 0
.LL_123%=:
v_min3_u32 v176, v101, v103, v109
v_max3_u32 v177, v101, v103, v109
v_min3_u32 v176, v176, v111, v117
v_max3_u32 v177, v177, v111, v117
v_min3_u32 v176, v176, v119, v125
v_max3_u32 v177, v177, v119, v125
v_min_u32 v176, v176, v127
v_max_u32 v177, v177, v127
v_cmp_eq_u32 vcc, s13, v176
v_cmp_eq_u32_e64 s[86:87], s13, v177
s_and_b64 vcc, vcc, s[86:87]
s_cmp_eq_u64 vcc, -1
s_cbranch_scc1 .LL_124%=
s_add_u32 s84, s84, 1
s_cmp_lt_u32 s84, 1048576
s_cbranch_scc1 .LL_125%=
.LL_124%=:
v_readlane_b32 s70, v12, s69
v_bfe_u32 v2, v1, 4, 4
s_nop 2
s_lshr_b32 s80, s70, 4
s_and_b32 s70, s70, 15
v_cmp_eq_u32 vcc, s70, v2
s_cmp_eq_u32 s80, s61
s_cselect_b64 s[86:87], vcc, 0
s_cselect_b64 s[88:89], 0, vcc
s_bitcmp1_b32 s14, 0
s_cselect_b64 vcc, s[88:89], s[86:87]
v_lshlrev_b32 v176, 16, v100
v_and_b32 v177, 0xffff0000, v100
v_lshlrev_b32 v178, 16, v102
v_and_b32 v179, 0xffff0000, v102
v_lshlrev_b32 v180, 16, v108
v_and_b32 v181, 0xffff0000, v108
v_lshlrev_b32 v182, 16, v110
v_and_b32 v183, 0xffff0000, v110
A 176,176,40
A 177,177,41
A 178,178,42
A 179,179,43
A 180,180,44
A 181,181,45
A 182,182,46
A 183,183,47
K 184,176,177
K 185,178,179
K 186,180,181
K 187,182,183
v_lshlrev_b32 v176, 16, v184
v_and_b32 v177, 0xffff0000, v184
v_lshlrev_b32 v178, 16, v185
v_and_b32 v179, 0xffff0000, v185
v_lshlrev_b32 v180, 16, v186
v_and_b32 v181, 0xffff0000, v186
v_lshlrev_b32 v182, 16, v187
v_and_b32 v183, 0xffff0000, v187
v_cndmask_b32_e64 v40, v40, v176, s[86:87]
v_cndmask_b32_e64 v41, v41, v177, s[86:87]
v_cndmask_b32_e64 v42, v42, v178, s[86:87]
v_cndmask_b32_e64 v43, v43, v179, s[86:87]
v_cndmask_b32_e64 v44, v44, v180, s[86:87]
v_cndmask_b32_e64 v45, v45, v181, s[86:87]
v_cndmask_b32_e64 v46, v46, v182, s[86:87]
v_cndmask_b32_e64 v47, v47, v183, s[86:87]
v_lshlrev_b32 v176, 16, v100
v_and_b32 v177, 0xffff0000, v100
v_lshlrev_b32 v178, 16, v102
v_and_b32 v179, 0xffff0000, v102
v_lshlrev_b32 v180, 16, v108
v_and_b32 v181, 0xffff0000, v108
v_lshlrev_b32 v182, 16, v110
v_and_b32 v183, 0xffff0000, v110
A 176,176,56
A 177,177,57
A 178,178,58
A 179,179,59
A 180,180,60
A 181,181,61
A 182,182,62
A 183,183,63
K 184,176,177
K 185,178,179
K 186,180,181
K 187,182,183
v_lshlrev_b32 v176, 16, v184
v_and_b32 v177, 0xffff0000, v184
v_lshlrev_b32 v178, 16, v185
v_and_b32 v179, 0xffff0000, v185
v_lshlrev_b32 v180, 16, v186
v_and_b32 v181, 0xffff0000, v186
v_lshlrev_b32 v182, 16, v187
v_and_b32 v183, 0xffff0000, v187
v_cndmask_b32_e64 v56, v56, v176, s[88:89]
v_cndmask_b32_e64 v57, v57, v177, s[88:89]
v_cndmask_b32_e64 v58, v58, v178, s[88:89]
v_cndmask_b32_e64 v59, v59, v179, s[88:89]
v_cndmask_b32_e64 v60, v60, v180, s[88:89]
v_cndmask_b32_e64 v61, v61, v181, s[88:89]
v_cndmask_b32_e64 v62, v62, v182, s[88:89]
v_cndmask_b32_e64 v63, v63, v183, s[88:89]
v_lshlrev_b32 v176, 16, v116
v_and_b32 v177, 0xffff0000, v116
v_lshlrev_b32 v178, 16, v118
v_and_b32 v179, 0xffff0000, v118
v_lshlrev_b32 v180, 16, v124
v_and_b32 v181, 0xffff0000, v124
v_lshlrev_b32 v182, 16, v126
v_and_b32 v183, 0xffff0000, v126
A 176,176,72
A 177,177,73
A 178,178,74
A 179,179,75
A 180,180,76
A 181,181,77
A 182,182,78
A 183,183,79
K 184,176,177
K 185,178,179
K 186,180,181
K 187,182,183
v_lshlrev_b32 v176, 16, v184
v_and_b32 v177, 0xffff0000, v184
v_lshlrev_b32 v178, 16, v185
v_and_b32 v179, 0xffff0000, v185
v_lshlrev_b32 v180, 16, v186
v_and_b32 v181, 0xffff0000, v186
v_lshlrev_b32 v182, 16, v187
v_and_b32 v183, 0xffff0000, v187
v_cndmask_b32_e64 v72, v72, v176, vcc
v_cndmask_b32_e64 v73, v73, v177, vcc
v_cndmask_b32_e64 v74, v74, v178, vcc
v_cndmask_b32_e64 v75, v75, v179, vcc
v_cndmask_b32_e64 v76, v76, v180, vcc
v_cndmask_b32_e64 v77, v77, v181, vcc
v_cndmask_b32_e64 v78, v78, v182, vcc
v_cndmask_b32_e64 v79, v79, v183, vcc
s_bitset0_b32 s68, s69
s_cmp_eq_u32 s68, 0
s_cbranch_scc1 .Lupr_122%=
s_ff1_i32_b32 s69, s68
.LL_125%=:
s_cmp_eq_u32 s64, 0
s_cbranch_scc0 .LL_126%=
s_waitcnt vmcnt(0)
v_lshlrev_b32 v2, 2, v1
v_mov_b64 v[28:29], s[12:13]
s_mov_b64 exec, 3
buffer_store_dwordx2 v[28:29], v2, s[8:11], s53 offen offset:512 sc1
s_mov_b64 exec, -1
s_mov_b32 s64, 1
.LL_126%=:
s_mul_i32 s66, s69, 20
s_mul_i32 s67, s58, 5
s_add_u32 s66, s66, s67
s_lshl_b32 s66, s66, 7
s_add_u32 s66, s66, 98304
s_lshl_b32 s67, s14, 7
s_add_u32 s67, s67, s66
buffer_load_dwordx4 v[100:103], v3, s[8:11], s67 offen offset:0 sc1
buffer_load_dwordx4 v[108:111], v3, s[8:11], s67 offen offset:16 sc1
buffer_load_dwordx4 v[116:119], v3, s[8:11], s66 offen offset:512 sc1
buffer_load_dwordx4 v[124:127], v3, s[8:11], s66 offen offset:528 sc1
s_waitcnt vmcnt(0)
s_branch .LL_123%=
.Lspin_56%=:
s_mov_b32 s84, 0
.Llp_58%=:
s_sleep 2
v_mov_b32 v28, s74
buffer_load_dwordx2 v[28:29], v28, s[8:11], 0 offen offset:3584 sc1
s_waitcnt vmcnt(0)
s_add_u32 s84, s84, 1
v_xor_b32 v28, s12, v28
v_xor_b32 v29, s13, v29
v_lshrrev_b32 v248, 11, v28
v_or_b32 v29, v29, v248
v_cmp_eq_u32 vcc, 0, v29
s_or_b64 vcc, vcc, s[72:73]
s_cmp_eq_u64 vcc, -1
s_cbranch_scc1 .Lok_57%=
s_cmp_lt_u32 s84, 1048576
s_cbranch_scc1 .Llp_58%=
s_branch .Lok_57%=
.Lspin_61%=:
s_mov_b32 s84, 0
.Llp_63%=:
s_sleep 2
v_bfe_u32 v28, v1, 4, 2
v_lshlrev_b32 v28, 3, v28
s_lshl_b32 s80, s56, 9
s_add_u32 s80, s80, 4096
buffer_load_dwordx2 v[28:29], v28, s[8:11], s80 offen sc1
s_waitcnt vmcnt(0)
s_add_u32 s84, s84, 1
v_cmp_eq_u64 vcc, s[12:13], v[28:29]
s_cmp_eq_u64 vcc, -1
s_cbranch_scc1 .Lok_62%=
s_cmp_lt_u32 s84, 1048576
s_cbranch_scc1 .Llp_63%=
s_branch .Lok_62%=
.Lspin_64%=:
s_mov_b32 s84, 0
.Llp_66%=:
s_sleep 2
v_bfe_u32 v28, v1, 4, 4
v_lshlrev_b32 v28, 3, v28
s_lshl_b32 s80, s56, 9
s_add_u32 s80, s80, 4096
buffer_load_dwordx2 v[28:29], v28, s[8:11], s80 offen sc1
s_waitcnt vmcnt(0)
s_add_u32 s84, s84, 1
v_cmp_eq_u64 vcc, s[12:13], v[28:29]
s_cmp_eq_u64 vcc, -1
s_cbranch_scc1 .Lok_65%=
s_cmp_lt_u32 s84, 1048576
s_cbranch_scc1 .Llp_66%=
s_branch .Lok_65%=
.Lspin_67%=:
s_mov_b32 s84, 0
.Llp_69%=:
s_sleep 2
v_bfe_u32 v28, v1, 4, 4
v_lshlrev_b32 v28, 3, v28
s_lshl_b32 s80, s56, 9
s_add_u32 s80, s80, 4224
buffer_load_dwordx2 v[28:29], v28, s[8:11], s80 offen sc1
s_waitcnt vmcnt(0)
s_add_u32 s84, s84, 1
v_cmp_eq_u64 vcc, s[12:13], v[28:29]
s_cmp_eq_u64 vcc, -1
s_cbranch_scc1 .Lok_68%=
s_cmp_lt_u32 s84, 1048576
s_cbranch_scc1 .Llp_69%=
s_branch .Lok_68%=
.Lspin_70%=:
s_mov_b32 s84, 0
.Llp_72%=:
s_sleep 2
v_bfe_u32 v28, v1, 4, 4
v_lshlrev_b32 v28, 3, v28
s_lshl_b32 s80, s56, 9
s_add_u32 s80, s80, 4352
buffer_load_dwordx2 v[28:29], v28, s[8:11], s80 offen sc1
s_waitcnt vmcnt(0)
s_add_u32 s84, s84, 1
v_cmp_eq_u64 vcc, s[12:13], v[28:29]
s_cmp_eq_u64 vcc, -1
s_cbranch_scc1 .Lok_71%=
s_cmp_lt_u32 s84, 1048576
s_cbranch_scc1 .Llp_72%=
s_branch .Lok_71%=
.Lspin_73%=:
s_mov_b32 s84, 0
.Llp_75%=:
s_sleep 2
v_bfe_u32 v28, v1, 4, 4
v_lshlrev_b32 v28, 3, v28
s_lshl_b32 s80, s56, 9
s_add_u32 s80, s80, 4480
buffer_load_dwordx2 v[28:29], v28, s[8:11], s80 offen sc1
s_waitcnt vmcnt(0)
s_add_u32 s84, s84, 1
v_cmp_eq_u64 vcc, s[12:13], v[28:29]
s_cmp_eq_u64 vcc, -1
s_cbranch_scc1 .Lok_74%=
s_cmp_lt_u32 s84, 1048576
s_cbranch_scc1 .Llp_75%=
s_branch .Lok_74%=
.Lspin_76%=:
s_mov_b32 s84, 0
.Llp_78%=:
s_sleep 2
buffer_load_dwordx2 v[8:9], v4, s[8:11], 0 offen offset:2560 sc1
buffer_load_dwordx2 v[10:11], v5, s[8:11], 0 offen offset:3072 sc1
buffer_load_dwordx2 v[12:13], v6, s[8:11], 0 offen offset:3584 sc1
s_waitcnt vmcnt(0)
s_add_u32 s84, s84, 1
v_cmp_eq_u64 vcc, s[12:13], v[8:9]
v_cmp_eq_u64_e64 s[86:87], s[12:13], v[10:11]
s_and_b64 vcc, vcc, s[86:87]
s_or_b64 vcc, vcc, s[20:21]
s_cmp_eq_u64 vcc, -1
s_cbranch_scc1 .Lok_77%=
s_cmp_lt_u32 s84, 1048576
s_cbranch_scc1 .Llp_78%=
s_branch .Lok_77%=
.Lmk_79%=:
s_nop 7
v_cndmask_b32_e64 v168, v241, v168, s[72:73]
v_cndmask_b32_e64 v169, v241, v169, s[74:75]
v_cndmask_b32_e64 v170, v241, v170, s[76:77]
v_cndmask_b32_e64 v171, v241, v171, s[78:79]
v_cndmask_b32_e64 v188, v241, v188, s[72:73]
v_cndmask_b32_e64 v189, v241, v189, s[74:75]
v_cndmask_b32_e64 v190, v241, v190, s[76:77]
v_cndmask_b32_e64 v191, v241, v191, s[78:79]
v_mov_b32 v184, v241
v_mov_b32 v185, v241
v_mov_b32 v186, v241
v_mov_b32 v187, v241
s_branch .Lmkr_80%=
.Lspin_84%=:
s_mov_b32 s84, 0
.Llp_86%=:
s_sleep 2
v_bfe_u32 v28, v1, 4, 2
v_lshlrev_b32 v28, 3, v28
buffer_load_dwordx2 v[28:29], v28, s[8:11], s55 offen sc1
s_waitcnt vmcnt(0)
s_add_u32 s84, s84, 1
v_cmp_eq_u64 vcc, s[12:13], v[28:29]
s_cmp_eq_u64 vcc, -1
s_cbranch_scc1 .Lok_85%=
s_cmp_lt_u32 s84, 1048576
s_cbranch_scc1 .Llp_86%=
s_branch .Lok_85%=
.LL_89%=:
s_mov_b32 s84, 0
.LL_91%=:
s_sleep 2
buffer_load_dwordx2 v[12:13], v27, s[8:11], 0 offen offset:3584 sc1
s_waitcnt vmcnt(0)
s_add_u32 s84, s84, 1
v_xor_b32 v12, s12, v12
v_xor_b32 v13, s13, v13
v_lshrrev_b32 v3, 11, v12
v_or_b32 v13, v13, v3
v_cmp_eq_u32 vcc, 0, v13
s_cmp_eq_u64 vcc, -1
s_cbranch_scc1 .LL_90%=
s_cmp_lt_u32 s84, 1048576
s_cbranch_scc1 .LL_91%=
s_branch .LL_90%=
.Lup_87%=:
v_lshrrev_b32 v3, 8, v1
v_lshlrev_b32 v3, 5, v3
s_ff1_i32_b32 s69, s68
s_mul_i32 s66, s69, 20
s_mul_i32 s67, s58, 5
s_add_u32 s66, s66, s67
s_lshl_b32 s66, s66, 7
s_add_u32 s66, s66, 98304
s_lshl_b32 s67, s14, 7
s_add_u32 s67, s67, s66
buffer_load_dwordx4 v[100:103], v3, s[8:11], s67 offen offset:0 sc1
buffer_load_dwordx4 v[108:111], v3, s[8:11], s67 offen offset:16 sc1
buffer_load_dwordx4 v[116:119], v3, s[8:11], s66 offen offset:512 sc1
buffer_load_dwordx4 v[124:127], v3, s[8:11], s66 offen offset:528 sc1
s_branch .Lupr_88%=
.Lspin_100%=:
s_mov_b32 s84, 0
.Llp_102%=:
s_sleep 2
v_bfe_u32 v28, v1, 4, 2
v_lshlrev_b32 v28, 3, v28
buffer_load_dwordx2 v[28:29], v28, s[8:11], s54 offen sc1
s_waitcnt vmcnt(0)
s_add_u32 s84, s84, 1
v_cmp_eq_u64 vcc, s[12:13], v[28:29]
s_cmp_eq_u64 vcc, -1
s_cbranch_scc1 .Lok_101%=
s_cmp_lt_u32 s84, 1048576
s_cbranch_scc1 .Llp_102%=
s_branch .Lok_101%=
.Lspin_103%=:
s_mov_b32 s84, 0
.Llp_105%=:
s_sleep 2
v_bfe_u32 v28, v1, 4, 2
v_lshlrev_b32 v28, 3, v28
buffer_load_dwordx2 v[28:29], v28, s[8:11], s55 offen sc1
s_waitcnt vmcnt(0)
s_add_u32 s84, s84, 1
v_cmp_eq_u64 vcc, s[12:13], v[28:29]
)"
R"(s_cmp_eq_u64 vcc, -1
s_cbranch_scc1 .Lok_104%=
s_cmp_lt_u32 s84, 1048576
s_cbranch_scc1 .Llp_105%=
s_branch .Lok_104%=
.LL_108%=:
s_mov_b32 s84, 0
.LL_110%=:
s_sleep 2
buffer_load_dwordx2 v[12:13], v27, s[8:11], 0 offen offset:3584 sc1
s_waitcnt vmcnt(0)
s_add_u32 s84, s84, 1
v_xor_b32 v12, s12, v12
v_xor_b32 v13, s13, v13
v_lshrrev_b32 v3, 11, v12
v_or_b32 v13, v13, v3
v_cmp_eq_u32 vcc, 0, v13
s_cmp_eq_u64 vcc, -1
s_cbranch_scc1 .LL_109%=
s_cmp_lt_u32 s84, 1048576
s_cbranch_scc1 .LL_110%=
s_branch .LL_109%=
.Lup_106%=:
v_lshrrev_b32 v3, 8, v1
v_lshlrev_b32 v3, 5, v3
s_ff1_i32_b32 s69, s68
s_mul_i32 s66, s69, 20
s_mul_i32 s67, s58, 5
s_add_u32 s66, s66, s67
s_lshl_b32 s66, s66, 7
s_add_u32 s66, s66, 98304
s_lshl_b32 s67, s14, 7
s_add_u32 s67, s67, s66
buffer_load_dwordx4 v[100:103], v3, s[8:11], s67 offen offset:0 sc1
buffer_load_dwordx4 v[108:111], v3, s[8:11], s67 offen offset:16 sc1
buffer_load_dwordx4 v[116:119], v3, s[8:11], s66 offen offset:512 sc1
buffer_load_dwordx4 v[124:127], v3, s[8:11], s66 offen offset:528 sc1
s_branch .Lupr_107%=
.Lend_252%=:
.Lend%=:
.purgem W
.purgem D
.purgem Ta
.purgem Ra
.purgem Zvaa
.purgem Mvaa
.purgem Zaaa
.purgem Maaa
.purgem A
.purgem U
.purgem K
.purgem Rv
.purgem Mvav
.purgem G
.purgem Zvav
.purgem Cvav
.purgem X
.purgem Cvaa
.purgem Dc
.purgem Maav
.purgem Mvvv
.purgem Zvva
.purgem Mvva
)"
    : : [ka] "s"(__builtin_amdgcn_kernarg_segment_ptr()), [bid] "s"(blockIdx.x), [tid] "v"(threadIdx.x)
    : "v1", "v2", "v3", "v4", "v5", "v6", "v7", "v8", "v9", "v10", "v11", "v12", "v13", "v14", "v15", "v16", "v17", "v18", "v19", "v20", "v21", "v22", "v23", "v24", "v25", "v26", "v27", "v28", "v29", "v30", "v31", "v32", "v33", "v34", "v35", "v36", "v37", "v38", "v39", "v40", "v41", "v42", "v43", "v44", "v45", "v46", "v47", "v48", "v49", "v50", "v51", "v52", "v53", "v54", "v55", "v56", "v57", "v58", "v59", "v60", "v61", "v62", "v63", "v64", "v65", "v66", "v67", "v68", "v69", "v70", "v71", "v72", "v73", "v74", "v75", "v76", "v77", "v78", "v79", "v80", "v81", "v82", "v83", "v84", "v85", "v86", "v87", "v88", "v89", "v90", "v91", "v92", "v93", "v94", "v95", "v96", "v97", "v98", "v99", "v100", "v101", "v102", "v103", "v104", "v105", "v106", "v107", "v108", "v109", "v110", "v111", "v112", "v113", "v114", "v115", "v116", "v117", "v118", "v119", "v120", "v121", "v122", "v123", "v124", "v125", "v126", "v127", "v128", "v129", "v130", "v131", "v132", "v133", "v134", "v135", "v136", "v137", "v138", "v139", "v140", "v141", "v142", "v143", "v144", "v145", "v146", "v147", "v148", "v149", "v150", "v151", "v152", "v153", "v154", "v155", "v156", "v157", "v158", "v159", "v160", "v161", "v162", "v163", "v164", "v165", "v166", "v167", "v168", "v169", "v170", "v171", "v172", "v173", "v174", "v175", "v176", "v177", "v178", "v179", "v180", "v181", "v182", "v183", "v184", "v185", "v186", "v187", "v188", "v189", "v190", "v191", "v192", "v193", "v194", "v195", "v196", "v197", "v198", "v199", "v200", "v201", "v202", "v203", "v204", "v205", "v206", "v207", "v208", "v209", "v210", "v211", "v212", "v213", "v214", "v215", "v216", "v217", "v218", "v219", "v220", "v221", "v222", "v223", "v224", "v225", "v226", "v227", "v228", "v229", "v230", "v231", "v232", "v233", "v234", "v235", "v236", "v237", "v238", "v239", "v240", "v241", "v242", "v243", "v244", "v245", "v246", "v247", "v248", "v249", "v250", "v251", "v252", "v253", "v254", "v255", "a0", "a1", "a2", "a3", "a4", "a5", "a6", "a7", "a8", "a9", "a10", "a11", "a12", "a13", "a14", "a15", "a16", "a17", "a18", "a19", "a20", "a21", "a22", "a23", "a24", "a25", "a26", "a27", "a28", "a29", "a30", "a31", "a32", "a33", "a34", "a35", "a36", "a37", "a38", "a39", "a40", "a41", "a42", "a43", "a44", "a45", "a46", "a47", "a48", "a49", "a50", "a51", "a52", "a53", "a54", "a55", "a56", "a57", "a58", "a59", "a60", "a61", "a62", "a63", "a64", "a65", "a66", "a67", "a68", "a69", "a70", "a71", "a72", "a73", "a74", "a75", "a76", "a77", "a78", "a79", "a80", "a81", "a82", "a83", "a84", "a85", "a86", "a87", "a88", "a89", "a90", "a91", "a92", "a93", "a94", "a95", "a96", "a97", "a98", "a99", "a100", "a101", "a102", "a103", "a104", "a105", "a106", "a107", "a108", "a109", "a110", "a111", "a112", "a113", "a114", "a115", "a116", "a117", "a118", "a119", "a120", "a121", "a122", "a123", "a124", "a125", "a126", "a127", "a128", "a129", "a130", "a131", "a132", "a133", "a134", "a135", "a136", "a137", "a138", "a139", "a140", "a141", "a142", "a143", "a144", "a145", "a146", "a147", "a148", "a149", "a150", "a151", "a152", "a153", "a154", "a155", "a156", "a157", "a158", "a159", "a160", "a161", "a162", "a163", "a164", "a165", "a166", "a167", "a168", "a169", "a170", "a171", "a172", "a173", "a174", "a175", "a176", "a177", "a178", "a179", "a180", "a181", "a182", "a183", "a184", "a185", "a186", "a187", "a188", "a189", "a190", "a191", "a192", "a193", "a194", "a195", "a196", "a197", "a198", "a199", "a200", "a201", "a202", "a203", "a204", "a205", "a206", "a207", "a208", "a209", "a210", "a211", "a212", "a213", "a214", "a215", "a216", "a217", "a218", "a219", "a220", "a221", "a222", "a223", "a224", "a225", "a226", "a227", "a228", "a229", "a230", "a231", "a232", "a233", "a234", "a235", "a236", "a237", "a238", "a239", "a240", "a241", "a242", "a243", "a244", "a245", "a246", "a247", "a248", "a249", "a250", "a251", "a252", "a253", "a254", "a255", "s4", "s5", "s6", "s7", "s8", "s9", "s10", "s11", "s12", "s13", "s14", "s15", "s16", "s17", "s18", "s19", "s20", "s21", "s22", "s23", "s24", "s25", "s26", "s27", "s28", "s29", "s30", "s31", "s32", "s33", "s34", "s35", "s36", "s37", "s38", "s39", "s40", "s41", "s42", "s43", "s44", "s45", "s46", "s47", "s48", "s49", "s50", "s51", "s52", "s53", "s54", "s55", "s56", "s57", "s58", "s59", "s60", "s61", "s62", "s63", "s64", "s65", "s66", "s67", "s68", "s69", "s70", "s71", "s72", "s73", "s74", "s75", "s76", "s77", "s78", "s79", "s80", "s81", "s82", "s83", "s84", "s85", "s86", "s87", "s88", "s89", "s90", "s91", "s92", "s93", "s94", "s95", "s96", "s97", "s98", "s99", "vcc", "scc", "memory"
  );
}
extern "C" void kernel_launch(void* const* d_in, const int* in_sizes, int n_in, void* d_out, int out_size, void* d_ws, size_t ws_size, hipStream_t stream) {
  (void)hipFuncSetAttribute((const void*)mega_kernel, hipFuncAttributeMaxDynamicSharedMemorySize, LDS_BYTES);
  mega_kernel<<<256, 256, LDS_BYTES, stream>>>((const bf16*)d_in[0], (const bf16*)d_in[1], (const bf16*)d_in[2], (const bf16*)d_in[3], (const bf16*)d_in[4], (const bf16*)d_in[5],
      (const bf16*)d_in[7], (const bf16*)d_in[8], (const bf16*)d_in[9], (const bf16*)d_in[10], (char*)d_ws + 0, (bf16*)d_out);
}
```

```cpp
#include <hip/hip_runtime.h>
#include <hip/hip_bf16.h>
typedef __hip_bfloat16 bf16;
constexpr int LDS_BYTES = 163840;
__global__ __launch_bounds__(256, 1) void mega_kernel(const bf16* x, const bf16* ckv, const bf16* wq, const bf16* wuk, const bf16* wuv, const bf16* wo,
    const bf16* w_r, const bf16* wg, const bf16* wu, const bf16* wd, char* ws, bf16* out) {
  asm volatile(
R"(.macro W n
s_waitcnt lgkmcnt(\n)
.endm
.macro D v,r,s,o
buffer_load_dwordx4 v\v, s[\r:\r+3], s\s offen offset:\o lds
.endm
.macro Ta d,a,o
ds_read_b64_tr_b16 a[\d:\d+1], v\a offset:\o
.endm
.macro Ra d,a,o
ds_read_b128 a[\d:\d+3], v\a offset:\o
.endm
.macro Zvaa d,a,b
v_mfma_f32_16x16x32_bf16 v[\d:\d+3], a[\a:\a+3], a[\b:\b+3], 0
.endm
.macro Mvaa d,a,b
v_mfma_f32_16x16x32_bf16 v[\d:\d+3], a[\a:\a+3], a[\b:\b+3], v[\d:\d+3]
.endm
.macro Zaaa d,a,b
v_mfma_f32_16x16x32_bf16 a[\d:\d+3], a[\a:\a+3], a[\b:\b+3], 0
.endm
.macro Maaa d,a,b
v_mfma_f32_16x16x32_bf16 a[\d:\d+3], a[\a:\a+3], a[\b:\b+3], a[\d:\d+3]
.endm
.macro A d,a,b
v_add_f32 v\d, v\a, v\b
.endm
.macro U d,a,b
v_mul_f32 v\d, v\a, v\b
.endm
.macro K d,a,b
v_cvt_pk_bf16_f32 v\d, v\a, v\b
.endm
.macro Rv d,a,o
ds_read_b128 v[\d:\d+3], v\a offset:\o
.endm
.macro Mvav d,a,b
v_mfma_f32_16x16x32_bf16 v[\d:\d+3], a[\a:\a+3], v[\b:\b+3], v[\d:\d+3]
.endm
.macro G d,a
v_accvgpr_read_b32 v\d, a\a
.endm
.macro Zvav d,a,b
v_mfma_f32_16x16x32_bf16 v[\d:\d+3], a[\a:\a+3], v[\b:\b+3], 0
.endm
.macro Cvav d,a,b,c
v_mfma_f32_16x16x32_bf16 v[\d:\d+3], a[\a:\a+3], v[\b:\b+3], v[\c:\c+3]
.endm
.macro X d
v_exp_f32 v\d, v\d
.endm
.macro Cvaa d,a,b,c
v_mfma_f32_16x16x32_bf16 v[\d:\d+3], a[\a:\a+3], a[\b:\b+3], v[\c:\c+3]
.endm
.macro Dc v,r,s,o
buffer_load_dwordx4 v\v, s[\r:\r+3], s\s offen offset:\o sc1 lds
.endm
.macro Maav d,a,b
v_mfma_f32_16x16x32_bf16 a[\d:\d+3], a[\a:\a+3], v[\b:\b+3], a[\d:\d+3]
.endm
.macro Mvvv d,a,b
v_mfma_f32_16x16x32_bf16 v[\d:\d+3], v[\a:\a+3], v[\b:\b+3], v[\d:\d+3]
.endm
.macro Zvva d,a,b
v_mfma_f32_16x16x32_bf16 v[\d:\d+3], v[\a:\a+3], a[\b:\b+3], 0
.endm
.macro Mvva d,a,b
v_mfma_f32_16x16x32_bf16 v[\d:\d+3], v[\a:\a+3], a[\b:\b+3], v[\d:\d+3]
.endm
s_load_dwordx16 s[16:31], %[ka], 0
s_load_dwordx8 s[32:39], %[ka], 64
s_getpc_b64 s[6:7]
.Lbeg%=:
s_mov_b32 s15, %[bid]
v_lshrrev_b32 v2, 6, %[tid]
v_and_b32 v100, 63, %[tid]
v_readfirstlane_b32 s14, v2
v_lshlrev_b32 v1, 4, v100
v_lshrrev_b32 v101, 4, v100
v_and_b32 v102, 15, v100
s_and_b32 s59, s15, 7
s_bfe_u32 s57, s15, 0x30003
s_lshr_b32 s58, s15, 6
s_lshl_b32 s56, s59, 2
s_add_u32 s56, s56, s58
s_lshr_b32 s80, s15, 3
s_lshl_b32 s80, s80, 2
s_add_u32 s80, s80, s14
s_lshl_b32 s80, s80, 11
v_min_u32 v146, 240, v1
v_lshl_add_u32 v146, v146, 3, s80
v_min_u32 v146, .Lend%=-.Lbeg%=-4, v146
global_load_dword v142, v146, s[6:7]
v_and_b32 v88, 0x1f0, v1
v_lshrrev_b32 v105, 1, v101
v_mul_u32_u24 v105, 1280, v105
v_add_u32 v28, v88, v105
s_lshl_b32 s85, s15, 2
s_add_u32 s85, s85, s14
s_mul_i32 s84, s85, 2560
v_and_b32 v105, 1, v101
v_lshl_or_b32 v29, v105, 31, v28
v_lshlrev_b32 v104, 1, v101
v_xor_b32 v104, v104, v102
v_lshlrev_b32 v104, 4, v104
v_lshl_add_u32 v16, v101, 13, v104
v_xor_b32 v105, 128, v16
v_add_u32 v17, 31744, v16
v_add_u32 v18, 63488, v105
v_add_u32 v19, 95232, v105
v_add_u32 v20, 131072, v16
v_add_u32 v21, 162816, v16
v_add_u32 v22, 194560, v105
v_add_u32 v23, 226304, v105
s_lshl_b32 s80, s14, 3
s_add_u32 s80, s80, s57
s_lshl_b32 s80, s80, 15
v_and_b32 v104, 7, v100
v_xor_b32 v104, v104, v101
v_lshlrev_b32 v104, 4, v104
v_lshrrev_b32 v105, 3, v100
v_lshl_add_u32 v104, v105, 10, v104
v_add_u32 v2, s80, v104
v_xor_b32 v105, 64, v2
v_add_u32 v3, 7168, v105
v_add_u32 v4, 14336, v2
v_add_u32 v5, 21504, v105
v_xor_b32 v6, 1835008, v2
v_xor_b32 v7, 1835008, v3
v_xor_b32 v8, 1835008, v4
v_xor_b32 v9, 1835008, v5
s_lshl_b32 s63, s14, 13
s_add_u32 s61, s63, 65536
s_and_b32 s80, s14, 1
s_lshr_b32 s81, s14, 1
s_lshl_b32 s62, s80, 14
s_lshl_b32 s82, s81, 13
s_add_u32 s62, s62, s82
s_lshl_b32 s81, s81, 18
s_lshl_b32 s83, s56, 8
s_add_u32 s81, s81, s83
s_mov_b32 s64, 0
s_cmp_eq_u32 s80, 0
W 0
s_cselect_b32 s44, s22, s24
s_cselect_b32 s45, s23, s25
s_cmp_eq_u32 s14, 3
s_cbranch_scc0 .LL_1%=
s_lshr_b32 s88, s15, 3
s_cmp_lt_u32 s88, 14
s_cbranch_scc0 .LL_1%=
v_lshlrev_b32 v104, 16, v1
s_mov_b64 s[86:87], s[36:37]
s_mov_b32 s89, 38797312
s_cmp_eq_u32 s88, 1
s_cselect_b32 s90, 39845888, 0
s_add_u32 s86, s86, s90
s_addc_u32 s87, s87, 0
s_cmp_eq_u32 s88, 2
s_cselect_b64 s[86:87], s[26:27], s[86:87]
s_cselect_b32 s89, 5242876, s89
s_cmp_eq_u32 s88, 3
s_cselect_b64 s[86:87], s[30:31], s[86:87]
s_cselect_b32 s89, 2211836, s89
s_cmp_eq_u32 s88, 4
s_cselect_b64 s[86:87], s[32:33], s[86:87]
s_cselect_b32 s89, 2211836, s89
s_cmp_eq_u32 s88, 5
s_cselect_b64 s[86:87], s[34:35], s[86:87]
s_cselect_b32 s89, 2211836, s89
s_cmp_eq_u32 s88, 6
s_cselect_b64 s[86:87], s[38:39], s[86:87]
s_cselect_b32 s89, 2621436, s89
s_cmp_eq_u32 s88, 7
s_cselect_b64 s[86:87], s[20:21], s[86:87]
s_cselect_b32 s89, 5242876, s89
s_cmp_eq_u32 s88, 8
s_cselect_b64 s[86:87], s[18:19], s[86:87]
s_cselect_b32 s89, 2097148, s89
s_cmp_eq_u32 s88, 9
s_cselect_b64 s[86:87], s[22:23], s[86:87]
s_cselect_b32 s89, 4194300, s89
s_cmp_eq_u32 s88, 10
s_cselect_b64 s[86:87], s[24:25], s[86:87]
s_cselect_b32 s89, 4194300, s89
s_cmp_eq_u32 s88, 11
s_cselect_b64 s[86:87], s[16:17], s[86:87]
s_cselect_b32 s89, 2621436, s89
s_cmp_eq_u32 s88, 12
s_cselect_b64 s[86:87], s[28:29], s[86:87]
s_cselect_b32 s89, 1276, s89
s_cmp_eq_u32 s88, 13
s_cselect_b64 s[86:87], s[6:7], s[86:87]
s_cselect_b32 s89, 0, s89
v_min_u32 v104, s89, v104
global_load_dword v142, v104, s[86:87] sc1
.LL_1%=:
s_mov_b32 s40, s18
s_and_b32 s41, s19, 0xffff
s_mov_b32 s42, 0x200000
s_mov_b32 s43, 0x20000
s_add_u32 s44, s44, s81
s_addc_u32 s45, s45, 0
s_and_b32 s45, s45, 0xffff
s_mov_b32 s46, 0x400000
s_mov_b32 s47, 0x20000
s_sub_u32 s46, s46, s81
s_load_dwordx2 s[12:13], s[36:37], 0
s_lshl_b32 s65, s64, 12
s_add_u32 m0, s62, 0
s_nop 0
D 16,44,65,0
D 17,44,65,1024
D 18,44,65,2048
D 19,44,65,3072
s_add_u32 m0, s62, 4096
s_nop 0
D 20,44,65,0
D 21,44,65,1024
D 22,44,65,2048
D 23,44,65,3072
s_add_u32 m0, s61, 0
s_nop 0
D 2,40,64,0
D 3,40,64,1024
D 4,40,64,2048
D 5,40,64,3072
s_add_u32 m0, s61, 4096
s_nop 0
D 6,40,64,0
D 7,40,64,1024
D 8,40,64,2048
D 9,40,64,3072
s_mov_b32 s52, s16
s_and_b32 s53, s17, 0xffff
s_mov_b32 s54, 2621440
s_mov_b32 s55, 0x20000
buffer_load_dwordx4 v[32:35], v28, s[52:55], s84 offen
buffer_load_dwordx4 v[36:39], v28, s[52:55], s84 offen offset:512
buffer_load_dwordx4 v[40:43], v29, s[52:55], s84 offen offset:1024
s_mov_b32 s52, s28
s_and_b32 s53, s29, 0xffff
s_mov_b32 s54, 1280
s_mov_b32 s55, 0x20000
buffer_load_dwordx4 v[44:47], v88, s[52:55], 0 offen offset:0
buffer_load_dwordx4 v[48:51], v88, s[52:55], 0 offen offset:512
buffer_load_dwordx4 v[52:55], v88, s[52:55], 0 offen offset:1024
s_mov_b32 s69, 0
s_cmp_eq_u32 s14, 2
s_cbranch_scc1 .LL_2%=
s_cmp_eq_u32 s14, 3
s_cbranch_scc1 .LL_3%=
s_lshl_b32 s80, s58, 3
s_add_u32 s80, s80, s57
s_lshl_b32 s80, s80, 15
s_lshl_b32 s82, s14, 7
s_add_u32 s80, s80, s82
s_add_u32 s80, s80, 256
v_lshl_add_u32 v104, v88, 6, s80
v_and_b32 v105, 512, v1
v_mul_u32_u24 v105, 3584, v105
v_xor_b32 v104, v104, v105
v_mov_b32 v140, s18
v_mov_b32 v141, s19
s_mov_b32 s68, 256
s_branch .LL_4%=
.LL_2%=:
v_lshrrev_b32 v104, 6, v1
v_lshlrev_b32 v104, 13, v104
v_and_b32 v105, 16, v1
v_lshl_add_u32 v104, v105, 3, v104
s_lshl_b32 s80, s57, 17
s_add_u32 s80, s80, s83
s_add_u32 s80, s80, 1048576
v_add_u32 v104, s80, v104
v_and_b32 v105, 32, v1
v_cmp_eq_u32 vcc, 0, v105
v_mov_b32 v140, s24
v_mov_b32 v141, s25
v_mov_b32 v105, s22
v_mov_b32 v106, s23
v_cndmask_b32 v140, v140, v105, vcc
v_cndmask_b32 v141, v141, v106, vcc
s_mov_b32 s68, 1048576
s_branch .LL_4%=
.LL_3%=:
v_lshrrev_b32 v104, 5, v1
v_lshlrev_b32 v104, 13, v104
v_and_b32 v105, 16, v1
v_lshl_add_u32 v104, v105, 3, v104
s_mul_i32 s80, s57, 655360
s_add_u32 s80, s80, s83
v_add_u32 v104, s80, v104
v_mov_b32 v140, s20
v_mov_b32 v141, s21
s_mov_b32 s68, 196608
v_mov_b32 v104, 0
s_mov_b32 s68, 0
.LL_4%=:
v_mov_b32 v105, 0
v_lshl_add_u64 v[140:141], v[104:105], 0, v[140:141]
s_add_u32 s64, s64, 128
s_and_b32 s64, s64, 1023
s_lshl_b32 s65, s64, 12
s_add_u32 m0, s62, 32768
s_nop 0
D 16,44,65,0
D 17,44,65,1024
D 18,44,65,2048
D 19,44,65,3072
s_add_u32 m0, s62, 36864
s_nop 0
D 20,44,65,0
D 21,44,65,1024
D 22,44,65,2048
D 23,44,65,3072
v_lshrrev_b32 v104, 1, v102
v_xor_b32 v104, v104, v101
v_lshlrev_b32 v104, 4, v104
v_lshl_add_u32 v104, v102, 7, v104
s_lshr_b32 s81, s14, 1
s_lshl_b32 s81, s81, 14
s_add_u32 s81, s81, 65536
v_add_u32 v26, s81, v104
v_xor_b32 v27, 64, v26
v_and_b32 v105, 1, v101
v_lshrrev_b32 v104, 2, v102
v_lshl_or_b32 v105, v105, 2, v104
v_lshl_add_u32 v104, v101, 3, v104
v_lshlrev_b32 v104, 8, v104
v_lshl_add_u32 v104, v105, 5, v104
v_and_b32 v105, 3, v102
v_lshl_add_u32 v10, v105, 3, v104
v_xor_b32 v11, 32, v10
v_xor_b32 v12, 64, v10
v_xor_b32 v13, 96, v10
v_xor_b32 v14, 128, v10
v_xor_b32 v15, 160, v10
v_xor_b32 v24, 192, v10
v_xor_b32 v25, 224, v10
.LL_8%=:
s_lshr_b32 s81, s15, 3
s_cmp_eq_u32 s14, 2
s_cbranch_scc1 .LL_5%=
s_cmp_eq_u32 s14, 3
s_cbranch_scc1 .LL_6%=
s_cmp_eq_u32 s14, 0
s_cselect_b64 s[76:77], s[30:31], s[32:33]
s_mov_b32 s72, 512
s_mov_b32 s73, 24
s_mul_i32 s74, s81, 69120
s_mul_i32 s82, s59, 8640
s_andn2_b32 s82, s82, 127
s_add_u32 s74, s74, s82
s_mov_b32 s75, 2211836
s_branch .LL_7%=
.LL_5%=:
s_mov_b64 s[76:77], s[34:35]
s_mov_b32 s72, 512
s_mov_b32 s73, 24
s_mul_i32 s74, s81, 8960
s_mul_i32 s82, s59, 286720
s_add_u32 s74, s74, s82
s_mov_b32 s75, 2211836
s_branch .LL_7%=
.LL_6%=:
s_mov_b64 s[76:77], s[26:27]
s_mov_b32 s72, 512
s_mov_b32 s73, 4
s_mul_i32 s74, s81, 20480
s_mul_i32 s82, s59, 655360
s_add_u32 s74, s74, s82
s_mov_b32 s75, 5242876
s_branch .LL_7%=
.LL_7%=:
s_mov_b32 s8, s36
s_and_b32 s9, s37, 0xffff
s_mov_b32 s10, 79691776
s_mov_b32 s11, 0x20000
s_add_u32 s48, s20, s83
s_addc_u32 s49, s21, 0
s_and_b32 s49, s49, 0xffff
s_mov_b32 s50, 5242880
s_mov_b32 s51, 0x20000
s_sub_u32 s50, s50, s83
s_lshl_b32 s80, s57, 1
s_and_b32 s81, s14, 1
s_add_u32 s80, s80, s81
s_sub_u32 s81, 31, s80
s_cmp_lt_u32 s14, 2
s_cselect_b32 s60, s80, s81
s_mul_i32 s28, s60, 81920
s_add_u32 s28, s28, 131072
s_add_u32 s4, s28, 10240
s_mov_b32 s80, 0
s_lshl_b32 s81, s80, 9
s_add_u32 s28, s28, s81
s_add_u32 s5, s80, s14
s_cmp_ge_u32 s5, 20
s_cselect_b32 s81, 20, 0
s_sub_u32 s5, s5, s81
s_and_b32 s80, s14, 1
s_mul_i32 s29, s80, 17172480
s_mul_i32 s80, s56, 536576
s_add_u32 s29, s29, s80
s_lshl_b32 s80, s57, 13
s_add_u32 s29, s29, s80
s_add_u32 s29, s29, 4194304
W 0
s_add_u32 s12, s12, 0x7f4a7c15
s_addc_u32 s13, s13, 0x9e3779b9
s_mov_b64 exec, 0
s_cmp_eq_u32 s14, 0
s_cbranch_scc1 .Lh_0_0%=
s_cmp_eq_u32 s14, 1
s_cbranch_scc1 .Lh_0_1%=
s_cmp_eq_u32 s14, 2
s_cbranch_scc1 .Lh_0_2%=
s_branch .Lh_0_3%=
.Lhr_9%=:
s_mov_b64 exec, -1
s_bitcmp1_b32 s14, 0
s_cbranch_scc1 .Lv_10%=
s_branch .LL_12%=
.p2align 12
.LL_12%=:
s_waitcnt vmcnt(14)
s_barrier
Ta 0,10,0
Ta 2,10,1024
Ra 32,26,0
Ta 4,10,8192
.Lh_0_0%=:
s_cbranch_execz .Lh_0_0_1%=
Ta 6,10,9216
Ra 128,27,0
Ra 132,27,2048
Ra 36,26,2048
Ra 40,26,4096
Ra 136,27,4096
Ra 140,27,6144
Ra 44,26,6144
Ra 48,26,8192
Ra 144,27,8192
W 6
Zvaa 160,0,32
)"
R"(s_add_u32 m0, s61, 32768
Ra 148,27,10240
Ra 52,26,10240
Ra 56,26,12288
Ra 152,27,12288
Ra 156,27,14336
Ra 60,26,14336
Mvaa 160,4,128
Ta 8,11,0
Ta 10,11,1024
Ta 12,11,8192
Ta 14,11,9216
Zvaa 192,4,132
Ta 16,12,0
Ta 18,12,1024
Mvaa 192,0,36
W 14
Ta 20,12,8192
Ta 22,12,9216
Zvaa 224,0,40
D 2,40,64,0
Mvaa 224,4,136
Zaaa 160,4,140
Maaa 160,0,44
W 12
Zaaa 192,0,48
D 3,40,64,1024
Maaa 192,4,144
Zaaa 224,4,148
Maaa 224,0,52
W 8
Zaaa 64,0,56
D 4,40,64,2048
Maaa 64,4,152
Zaaa 96,4,156
Maaa 96,0,60
W 4
Zaaa 100,8,60
D 5,40,64,3072
Maaa 100,12,156
s_add_u32 m0, s61, 36864
Zaaa 68,12,152
Ta 24,13,0
Ta 26,13,1024
Maaa 68,8,56
Ta 28,13,8192
Ta 30,13,9216
Zaaa 228,8,52
s_waitcnt vmcnt(15)
D 6,40,64,0
Maaa 228,12,148
v_lshlrev_b32 v56, 16, v32
v_and_b32 v57, 0xffff0000, v32
v_lshlrev_b32 v58, 16, v33
v_and_b32 v59, 0xffff0000, v33
v_lshlrev_b32 v60, 16, v34
Zaaa 196,12,144
v_and_b32 v61, 0xffff0000, v34
v_lshlrev_b32 v62, 16, v35
v_and_b32 v63, 0xffff0000, v35
v_lshlrev_b32 v64, 16, v36
v_and_b32 v65, 0xffff0000, v36
Maaa 196,8,48
v_lshlrev_b32 v66, 16, v37
v_and_b32 v67, 0xffff0000, v37
v_lshlrev_b32 v68, 16, v38
v_and_b32 v69, 0xffff0000, v38
v_lshlrev_b32 v70, 16, v39
Zaaa 164,8,44
D 7,40,64,1024
v_and_b32 v71, 0xffff0000, v39
v_lshlrev_b32 v72, 16, v40
v_and_b32 v73, 0xffff0000, v40
v_lshlrev_b32 v74, 16, v41
v_and_b32 v75, 0xffff0000, v41
Maaa 164,12,140
v_lshlrev_b32 v76, 16, v42
v_and_b32 v77, 0xffff0000, v42
v_lshlrev_b32 v78, 16, v43
v_and_b32 v79, 0xffff0000, v43
A 80,56,57
Zvaa 228,12,136
A 81,58,59
A 80,80,60
A 81,81,61
A 80,80,62
A 81,81,63
Mvaa 228,8,40
A 80,80,64
A 81,81,65
A 80,80,66
A 81,81,67
A 80,80,68
Zvaa 196,8,36
D 8,40,64,2048
A 81,81,69
A 80,80,70
A 81,81,71
A 80,80,72
A 81,81,73
Mvaa 196,12,132
A 80,80,74
A 81,81,75
A 80,80,76
A 81,81,77
A 80,80,78
A 81,81,79
Zvaa 164,12,128
A 80,80,81
s_nop 1
v_add_f32_dpp v80, v80, v80 quad_perm:[1,0,3,2] row_mask:0xf bank_mask:0xf
s_nop 1
v_add_f32_dpp v80, v80, v80 quad_perm:[2,3,0,1] row_mask:0xf bank_mask:0xf
s_nop 1
v_add_f32_dpp v80, v80, v80 row_half_mirror row_mask:0xf bank_mask:0xf
s_nop 1
v_add_f32_dpp v80, v80, v80 row_mirror row_mask:0xf bank_mask:0xf
Mvaa 164,8,32
W 4
v_mov_b32 v81, v80
s_nop 1
v_permlane16_swap_b32 v80, v81
A 80,80,81
v_mul_f32 v84, 0x3acccccd, v80
s_mov_b32 s86, 0xffff
Zvaa 168,16,32
s_mov_b32 s87, 0xffff
D 9,40,64,3072
v_sub_f32 v82, v56, v84
U 80,82,82
v_sub_f32 v83, v57, v84
v_fmac_f32 v80, v83, v83
Mvaa 168,20,128
v_sub_f32 v82, v58, v84
v_fmac_f32 v80, v82, v82
v_sub_f32 v83, v59, v84
v_fmac_f32 v80, v83, v83
v_sub_f32 v82, v60, v84
Zvaa 200,20,132
Ta 0,14,0
v_fmac_f32 v80, v82, v82
v_sub_f32 v83, v61, v84
v_fmac_f32 v80, v83, v83
v_sub_f32 v82, v62, v84
v_fmac_f32 v80, v82, v82
Mvaa 200,16,36
Ta 2,14,1024
v_sub_f32 v83, v63, v84
v_fmac_f32 v80, v83, v83
v_sub_f32 v82, v64, v84
v_fmac_f32 v80, v82, v82
v_sub_f32 v83, v65, v84
Zvaa 232,16,40
global_load_dword v142, v[140:141], off
v_lshl_add_u64 v[140:141], v[140:141], 0, s[68:69]
Ta 4,14,8192
Ta 6,14,9216
v_fmac_f32 v80, v83, v83
v_sub_f32 v82, v66, v84
v_fmac_f32 v80, v82, v82
Mvaa 232,20,136
v_sub_f32 v83, v67, v84
v_fmac_f32 v80, v83, v83
v_sub_f32 v82, v68, v84
v_fmac_f32 v80, v82, v82
v_sub_f32 v83, v69, v84
v_fmac_f32 v80, v83, v83
Zaaa 168,20,140
v_sub_f32 v82, v70, v84
v_fmac_f32 v80, v82, v82
v_sub_f32 v83, v71, v84
v_fmac_f32 v80, v83, v83
v_sub_f32 v82, v72, v84
U 81,82,82
Maaa 168,16,44
v_sub_f32 v83, v73, v84
v_fmac_f32 v81, v83, v83
v_sub_f32 v82, v74, v84
v_fmac_f32 v81, v82, v82
v_sub_f32 v83, v75, v84
v_fmac_f32 v81, v83, v83
Zaaa 200,16,48
v_sub_f32 v82, v76, v84
v_fmac_f32 v81, v82, v82
v_sub_f32 v83, v77, v84
v_fmac_f32 v81, v83, v83
v_sub_f32 v82, v78, v84
Maaa 200,20,144
v_fmac_f32 v81, v82, v82
v_sub_f32 v83, v79, v84
v_fmac_f32 v81, v83, v83
v_cndmask_b32_e64 v81, 0, v81, s[86:87]
A 80,80,81
Zaaa 232,20,148
Maaa 232,16,52
v_add_f32_dpp v80, v80, v80 quad_perm:[1,0,3,2] row_mask:0xf bank_mask:0xf
s_nop 1
v_add_f32_dpp v80, v80, v80 quad_perm:[2,3,0,1] row_mask:0xf bank_mask:0xf
s_nop 1
v_add_f32_dpp v80, v80, v80 row_half_mirror row_mask:0xf bank_mask:0xf
s_nop 1
v_add_f32_dpp v80, v80, v80 row_mirror row_mask:0xf bank_mask:0xf
v_mov_b32 v81, v80
s_nop 1
v_permlane16_swap_b32 v80, v81
Zaaa 72,16,56
A 80,80,81
v_mul_f32 v80, 0x3acccccd, v80
K 80,80,80
v_lshlrev_b32 v80, 16, v80
v_add_f32 v80, 0x3727c5ac, v80
K 80,80,80
Maaa 72,20,152
v_lshlrev_b32 v80, 16, v80
v_sqrt_f32 v81, v80
s_nop 0
v_rcp_f32 v82, v81
v_fma_f32 v83, -v81, v81, v80
Zaaa 104,20,156
v_mul_f32 v82, 0.5, v82
v_fma_f32 v81, v83, v82, v81
K 86,81,81
v_lshlrev_b32 v86, 16, v86
v_rcp_f32 v87, v86
Maaa 104,16,60
W 4
K 85,84,84
v_lshlrev_b32 v85, 16, v85
s_waitcnt vmcnt(17)
v_sub_f32 v56, v56, v85
v_sub_f32 v57, v57, v85
K 80,56,57
Zaaa 108,24,60
v_lshlrev_b32 v56, 16, v80
v_and_b32 v57, 0xffff0000, v80
U 80,56,87
U 81,57,87
K 32,80,81
v_lshlrev_b32 v80, 16, v32
Maaa 108,28,156
v_and_b32 v81, 0xffff0000, v32
v_lshlrev_b32 v82, 16, v44
v_and_b32 v83, 0xffff0000, v44
U 84,80,82
v_fmac_f32 v84, v81, v83
Zaaa 76,28,152
Ta 8,15,0
v_sub_f32 v58, v58, v85
v_sub_f32 v59, v59, v85
K 80,58,59
v_lshlrev_b32 v58, 16, v80
v_and_b32 v59, 0xffff0000, v80
Maaa 76,24,56
Ta 10,15,1024
U 80,58,87
U 81,59,87
K 33,80,81
v_lshlrev_b32 v80, 16, v33
v_and_b32 v81, 0xffff0000, v33
Zaaa 236,24,52
Ta 12,15,8192
Ta 14,15,9216
v_lshlrev_b32 v82, 16, v45
v_and_b32 v83, 0xffff0000, v45
v_fmac_f32 v84, v80, v82
v_fmac_f32 v84, v81, v83
Maaa 236,28,148
v_sub_f32 v60, v60, v85
v_sub_f32 v61, v61, v85
K 80,60,61
v_lshlrev_b32 v60, 16, v80
v_and_b32 v61, 0xffff0000, v80
U 80,60,87
Zaaa 204,28,144
U 81,61,87
K 34,80,81
v_lshlrev_b32 v80, 16, v34
v_and_b32 v81, 0xffff0000, v34
v_lshlrev_b32 v82, 16, v46
v_and_b32 v83, 0xffff0000, v46
Maaa 204,24,48
v_fmac_f32 v84, v80, v82
v_fmac_f32 v84, v81, v83
v_sub_f32 v62, v62, v85
v_sub_f32 v63, v63, v85
K 80,62,63
Zaaa 172,24,44
v_lshlrev_b32 v62, 16, v80
v_and_b32 v63, 0xffff0000, v80
U 80,62,87
U 81,63,87
K 35,80,81
Maaa 172,28,140
v_lshlrev_b32 v80, 16, v35
v_and_b32 v81, 0xffff0000, v35
v_lshlrev_b32 v82, 16, v47
v_and_b32 v83, 0xffff0000, v47
v_fmac_f32 v84, v80, v82
Zvaa 236,28,136
v_fmac_f32 v84, v81, v83
v_sub_f32 v64, v64, v85
v_sub_f32 v65, v65, v85
K 80,64,65
v_lshlrev_b32 v64, 16, v80
Mvaa 236,24,40
v_and_b32 v65, 0xffff0000, v80
U 80,64,87
U 81,65,87
K 36,80,81
v_lshlrev_b32 v80, 16, v36
Zvaa 204,24,36
v_and_b32 v81, 0xffff0000, v36
v_lshlrev_b32 v82, 16, v48
v_and_b32 v83, 0xffff0000, v48
v_fmac_f32 v84, v80, v82
v_fmac_f32 v84, v81, v83
Mvaa 204,28,132
v_sub_f32 v66, v66, v85
v_sub_f32 v67, v67, v85
K 80,66,67
v_lshlrev_b32 v66, 16, v80
v_and_b32 v67, 0xffff0000, v80
U 80,66,87
Zvaa 172,28,128
U 81,67,87
K 37,80,81
v_lshlrev_b32 v80, 16, v37
v_and_b32 v81, 0xffff0000, v37
v_lshlrev_b32 v82, 16, v49
Mvaa 172,24,32
W 4
v_and_b32 v83, 0xffff0000, v49
v_fmac_f32 v84, v80, v82
v_fmac_f32 v84, v81, v83
v_sub_f32 v68, v68, v85
v_sub_f32 v69, v69, v85
Zvaa 176,0,32
K 80,68,69
v_lshlrev_b32 v68, 16, v80
v_and_b32 v69, 0xffff0000, v80
U 80,68,87
U 81,69,87
Mvaa 176,4,128
K 38,80,81
v_lshlrev_b32 v80, 16, v38
v_and_b32 v81, 0xffff0000, v38
v_lshlrev_b32 v82, 16, v50
v_and_b32 v83, 0xffff0000, v50
Zvaa 208,4,132
Ta 16,24,0
v_fmac_f32 v84, v80, v82
v_fmac_f32 v84, v81, v83
v_sub_f32 v70, v70, v85
v_sub_f32 v71, v71, v85
K 80,70,71
Mvaa 208,0,36
Ta 18,24,1024
v_lshlrev_b32 v70, 16, v80
v_and_b32 v71, 0xffff0000, v80
U 80,70,87
U 81,71,87
K 39,80,81
Zvaa 240,0,40
Ta 20,24,8192
Ta 22,24,9216
v_lshlrev_b32 v80, 16, v39
v_and_b32 v81, 0xffff0000, v39
v_lshlrev_b32 v82, 16, v51
v_and_b32 v83, 0xffff0000, v51
Mvaa 240,4,136
v_fmac_f32 v84, v80, v82
v_fmac_f32 v84, v81, v83
v_sub_f32 v72, v72, v85
v_sub_f32 v73, v73, v85
K 80,72,73
v_lshlrev_b32 v72, 16, v80
Zaaa 176,4,140
v_and_b32 v73, 0xffff0000, v80
U 80,72,87
U 81,73,87
K 40,80,81
v_lshlrev_b32 v80, 16, v40
v_and_b32 v81, 0xffff0000, v40
Maaa 176,0,44
v_lshlrev_b32 v82, 16, v52
v_and_b32 v83, 0xffff0000, v52
v_fmac_f32 v84, v80, v82
v_fmac_f32 v84, v81, v83
v_sub_f32 v74, v74, v85
Zaaa 208,0,48
v_sub_f32 v75, v75, v85
K 80,74,75
v_lshlrev_b32 v74, 16, v80
v_and_b32 v75, 0xffff0000, v80
U 80,74,87
Maaa 208,4,144
U 81,75,87
K 41,80,81
v_lshlrev_b32 v80, 16, v41
v_and_b32 v81, 0xffff0000, v41
)"
R"(v_lshlrev_b32 v82, 16, v53
Zaaa 240,4,148
v_and_b32 v83, 0xffff0000, v53
v_fmac_f32 v84, v80, v82
v_fmac_f32 v84, v81, v83
v_sub_f32 v76, v76, v85
v_sub_f32 v77, v77, v85
Maaa 240,0,52
K 80,76,77
v_lshlrev_b32 v76, 16, v80
v_and_b32 v77, 0xffff0000, v80
U 80,76,87
U 81,77,87
Zaaa 80,0,56
K 42,80,81
v_lshlrev_b32 v80, 16, v42
v_and_b32 v81, 0xffff0000, v42
v_lshlrev_b32 v82, 16, v54
v_and_b32 v83, 0xffff0000, v54
Maaa 80,4,152
v_fmac_f32 v84, v80, v82
v_fmac_f32 v84, v81, v83
v_sub_f32 v78, v78, v85
v_sub_f32 v79, v79, v85
K 80,78,79
v_lshlrev_b32 v78, 16, v80
Zaaa 112,4,156
v_and_b32 v79, 0xffff0000, v80
U 80,78,87
U 81,79,87
K 43,80,81
v_lshlrev_b32 v80, 16, v43
Maaa 112,0,60
W 4
v_and_b32 v81, 0xffff0000, v43
v_lshlrev_b32 v82, 16, v55
v_and_b32 v83, 0xffff0000, v55
v_fmac_f32 v84, v80, v82
v_fmac_f32 v84, v81, v83
Zaaa 116,8,60
Ta 24,25,0
Ta 26,25,1024
v_lshrrev_b32 v82, 9, v1
s_lshl_b32 s80, s14, 1
v_add_u32 v82, s80, v82
v_mul_u32_u24 v83, 1552, v82
v_add_u32 v83, v83, v88
Maaa 116,12,156
v_add_u32 v83, 131072, v83
ds_write_b128 v83, v[32:35] offset:0
ds_write_b128 v83, v[36:39] offset:512
ds_write_b128 v83, v[40:43] offset:1024
v_add_f32_dpp v84, v84, v84 quad_perm:[1,0,3,2] row_mask:0xf bank_mask:0xf
Zaaa 84,12,152
Ta 28,25,8192
Ta 30,25,9216
Maaa 84,8,56
v_add_f32_dpp v84, v84, v84 quad_perm:[2,3,0,1] row_mask:0xf bank_mask:0xf
s_nop 1
v_add_f32_dpp v84, v84, v84 row_half_mirror row_mask:0xf bank_mask:0xf
s_nop 1
v_add_f32_dpp v84, v84, v84 row_mirror row_mask:0xf bank_mask:0xf
v_mov_b32 v81, v84
s_nop 1
v_permlane16_swap_b32 v84, v81
A 84,84,81
Zaaa 244,8,52
K 80,84,84
v_lshlrev_b32 v80, 16, v80
s_lshl_b32 s80, s15, 3
v_add_u32 v82, s80, v82
v_sub_u32 v83, 2047, v82
v_ashrrev_i32 v81, 31, v80
v_or_b32 v81, 0x80000000, v81
Maaa 244,12,148
v_xor_b32 v80, v80, v81
v_and_b32 v80, 0xffff0000, v80
v_or_b32 v80, v80, v83
v_and_b32 v82, 7, v82
v_lshlrev_b32 v82, 2, v82
v_add_u32 v82, 143488, v82
Zaaa 212,12,144
ds_write_b32 v82, v80
Maaa 212,8,48
Zaaa 180,8,44
Maaa 180,12,140
Zvaa 244,12,136
s_waitcnt vmcnt(1) lgkmcnt(0)
s_barrier
s_add_u32 s64, s64, 128
Mvaa 244,8,40
v_bfe_u32 v80, v1, 4, 3
Zvaa 212,8,36
v_mul_u32_u24 v81, 1552, v80
Mvaa 212,12,132
s_and_b32 s64, s64, 1023
v_lshrrev_b32 v82, 7, v1
Zvaa 180,12,128
v_lshl_add_u32 v81, v82, 4, v81
Mvaa 180,8,32
s_lshl_b32 s80, s14, 7
Zvaa 184,16,32
s_lshl_b32 s65, s64, 12
s_add_u32 s80, s80, 131072
Mvaa 184,20,128
v_add_u32 v81, s80, v81
Zvaa 216,20,132
ds_read_b128 v[32:35], v81
Ta 0,10,32768
Mvaa 216,16,36
s_add_u32 m0, s62, 0
Ta 2,10,33792
Rv 36,81,512
Zvaa 248,16,40
s_and_b32 s80, s14, 2
Ta 4,10,40960
Ta 6,10,41984
Mvaa 248,20,136
s_lshl_b32 s80, s80, 7
Zaaa 184,20,140
D 16,44,65,0
v_subrev_u32 v81, s80, v81
Maaa 184,16,44
Rv 40,81,1024
Zaaa 216,16,48
v_lshlrev_b32 v80, 2, v80
Maaa 216,20,144
v_add_u32 v80, 143488, v80
Zaaa 248,20,148
D 17,44,65,1024
ds_read_b32 v84, v80
Maaa 248,16,52
v_mov_b32 v85, s13
Zaaa 88,16,56
v_lshrrev_b32 v83, 1, v1
Maaa 88,20,152
s_mul_i32 s82, s15, 10240
Zaaa 120,20,156
s_add_u32 s82, s82, 131072
D 18,44,65,2048
Maaa 120,16,60
s_lshl_b32 s80, s14, 10
Zaaa 124,24,60
s_add_u32 s83, s82, s80
Ra 60,26,47104
Maaa 124,28,156
Ra 156,27,47104
Zaaa 92,28,152
D 19,44,65,3072
Ra 152,27,45056
Maaa 92,24,56
s_add_u32 m0, s62, 4096
Ta 8,11,32768
Ta 10,11,33792
Zaaa 252,24,52
Ra 56,26,45056
Ta 12,11,40960
Maaa 252,28,148
Ta 14,11,41984
Ra 52,26,43008
Zaaa 220,28,144
W 9
buffer_store_dwordx4 v[32:35], v1, s[8:11], s83 offen sc1
s_add_u32 s83, s83, 4096
Maaa 220,24,48
Ra 148,27,43008
Ra 144,27,40960
Zaaa 188,24,44
Ra 48,26,40960
Ra 44,26,38912
Maaa 188,28,140
Ra 140,27,38912
Zvaa 252,28,136
D 20,44,65,0
Ra 136,27,36864
Mvaa 252,24,40
Ra 40,26,36864
Zvaa 220,24,36
Ra 36,26,34816
Mvaa 220,28,132
Ra 132,27,34816
Zvaa 188,28,128
buffer_store_dwordx4 v[36:39], v1, s[8:11], s83 offen sc1
Ra 128,27,32768
Mvaa 188,24,32
W 13
s_and_b32 s80, s80, 1024
s_add_u32 s83, s82, s80
Maaa 96,0,60
s_add_u32 s83, s83, 8192
Ra 32,26,32768
Maaa 96,4,156
Maaa 64,4,152
D 21,44,65,1024
Ta 16,12,32768
Maaa 64,0,56
W 9
Ta 18,12,33792
Ta 20,12,40960
Maaa 224,0,52
Ta 22,12,41984
Maaa 224,4,148
Maaa 192,4,144
buffer_store_dwordx4 v[40:43], v1, s[8:11], s83 offen sc1
Maaa 192,0,48
W 8
Maaa 160,0,44
Maaa 160,4,140
Mvaa 224,4,136
D 22,44,65,2048
Mvaa 224,0,40
W 4
Mvaa 192,0,36
Mvaa 192,4,132
Mvaa 160,4,128
D 23,44,65,3072
Mvaa 160,0,32
s_add_u32 m0, s61, 0
Mvaa 164,8,32
Mvaa 164,12,128
Mvaa 196,12,132
D 2,40,64,0
Ta 24,13,32768
Mvaa 196,8,36
Ta 26,13,33792
Ta 28,13,40960
Mvaa 228,8,40
Ta 30,13,41984
Mvaa 228,12,136
.Lh_0_1%=:
s_cbranch_execz .Lh_0_1_1%=
Maaa 164,12,140
D 3,40,64,1024
Maaa 164,8,44
Maaa 196,8,48
Maaa 196,12,144
Maaa 228,12,148
D 4,40,64,2048
Maaa 228,8,52
Maaa 68,8,56
Maaa 68,12,152
Maaa 100,12,156
D 5,40,64,3072
Maaa 100,8,60
W 4
s_add_u32 m0, s61, 4096
Maaa 104,16,60
Maaa 104,20,156
Maaa 72,20,152
D 6,40,64,0
Ta 0,14,32768
Maaa 72,16,56
Ta 2,14,33792
Ta 4,14,40960
Maaa 232,16,52
Ta 6,14,41984
Maaa 232,20,148
Maaa 200,20,144
D 7,40,64,1024
Maaa 200,16,48
Maaa 168,16,44
Maaa 168,20,140
Mvaa 232,20,136
D 8,40,64,2048
Mvaa 232,16,40
Mvaa 200,16,36
Mvaa 200,20,132
Mvaa 168,20,128
D 9,40,64,3072
v_bfe_u32 v147, v1, 5, 3
Mvaa 168,16,32
W 4
v_lshlrev_b32 v147, 13, v147
v_and_b32 v148, 16, v1
Mvaa 172,24,32
v_lshl_add_u32 v147, v148, 3, v147
Mvaa 172,28,128
s_lshl_b32 s92, s57, 2
s_add_u32 s92, s92, s14
s_lshl_b32 s92, s92, 16
Mvaa 204,28,132
buffer_load_dword v142, v147, s[48:51], s92 offen
Ta 8,15,32768
Mvaa 204,24,36
Ta 10,15,33792
Ta 12,15,40960
Mvaa 236,24,40
Ta 14,15,41984
Mvaa 236,28,136
Maaa 172,28,140
Maaa 172,24,44
Maaa 204,24,48
Maaa 204,28,144
Maaa 236,28,148
Maaa 236,24,52
Maaa 76,24,56
Maaa 76,28,152
Maaa 108,28,156
Maaa 108,24,60
W 4
Maaa 112,0,60
Maaa 112,4,156
Maaa 80,4,152
Ta 16,24,32768
Ta 18,24,33792
Maaa 80,0,56
Ta 20,24,40960
Ta 22,24,41984
Maaa 240,0,52
Maaa 240,4,148
Maaa 208,4,144
Maaa 208,0,48
Maaa 176,0,44
Maaa 176,4,140
Mvaa 240,4,136
Mvaa 240,0,40
Mvaa 208,0,36
Mvaa 208,4,132
Mvaa 176,4,128
Mvaa 176,0,32
W 4
Mvaa 180,8,32
Ta 24,25,32768
Ta 26,25,33792
Mvaa 180,12,128
Mvaa 212,12,132
Ta 28,25,40960
Ta 30,25,41984
Mvaa 212,8,36
Mvaa 244,8,40
Mvaa 244,12,136
Maaa 180,12,140
Maaa 180,8,44
Maaa 212,8,48
Maaa 212,12,144
Maaa 244,12,148
s_waitcnt vmcnt(1) lgkmcnt(0)
s_barrier
s_cmp_eq_u32 s14, 0
s_cbranch_scc0 .Lpubr_14%=
s_lshl_b32 s80, s15, 6
s_add_u32 s80, s80, 40960
s_mov_b64 exec, 0xff
buffer_store_dwordx2 v[84:85], v83, s[8:11], s80 offen sc1
s_mov_b64 exec, -1
.Lpubr_14%=:
Maaa 244,8,52
s_add_u32 s64, s64, 128
Maaa 84,8,56
Maaa 84,12,152
s_and_b32 s64, s64, 1023
Maaa 116,12,156
Maaa 116,8,60
Maaa 120,16,60
s_lshl_b32 s65, s64, 12
Maaa 120,20,156
Maaa 88,20,152
s_add_u32 m0, s62, 32768
Ta 0,10,0
Ta 2,10,1024
Maaa 88,16,56
Ta 4,10,8192
Ta 6,10,9216
Maaa 248,16,52
Maaa 248,20,148
D 16,44,65,0
Maaa 216,20,144
Maaa 216,16,48
Maaa 184,16,44
Maaa 184,20,140
D 17,44,65,1024
Mvaa 248,20,136
Mvaa 248,16,40
Mvaa 216,16,36
Mvaa 216,20,132
D 18,44,65,2048
Mvaa 184,20,128
Mvaa 184,16,32
Mvaa 188,24,32
Ra 32,26,0
Mvaa 188,28,128
D 19,44,65,3072
Ra 128,27,0
Mvaa 220,28,132
s_add_u32 m0, s62, 36864
Ra 132,27,2048
Ta 8,11,0
Mvaa 220,24,36
Ta 10,11,1024
Ra 36,26,2048
Mvaa 252,24,40
Ta 12,11,8192
Ta 14,11,9216
Mvaa 252,28,136
D 20,44,65,0
Ra 40,26,4096
Maaa 188,28,140
Ra 136,27,4096
Ra 140,27,6144
Maaa 188,24,44
)"
R"(Ra 44,26,6144
Maaa 220,24,48
Ra 48,26,8192
Maaa 220,28,144
D 21,44,65,1024
Ra 144,27,8192
Maaa 252,28,148
Ra 148,27,10240
Maaa 252,24,52
Ra 52,26,10240
Maaa 92,24,56
Ra 56,26,12288
Maaa 92,28,152
D 22,44,65,2048
Ra 152,27,12288
Maaa 124,28,156
Ra 156,27,14336
Maaa 124,24,60
W 13
Ra 60,26,14336
Mvaa 160,0,32
Mvaa 160,4,128
D 23,44,65,3072
Mvaa 192,4,132
s_add_u32 m0, s61, 32768
Ta 16,12,0
Ta 18,12,1024
Mvaa 192,0,36
W 10
Ta 20,12,8192
Ta 22,12,9216
Mvaa 224,0,40
Mvaa 224,4,136
D 2,40,64,0
Maaa 160,4,140
Maaa 160,0,44
W 8
Maaa 192,0,48
Maaa 192,4,144
D 3,40,64,1024
Maaa 224,4,148
Maaa 224,0,52
W 4
Maaa 64,0,56
Maaa 64,4,152
D 4,40,64,2048
Maaa 96,4,156
Maaa 96,0,60
Maaa 100,8,60
Maaa 100,12,156
D 5,40,64,3072
Maaa 68,12,152
s_add_u32 m0, s61, 36864
Ta 24,13,0
Ta 26,13,1024
Maaa 68,8,56
Ta 28,13,8192
Ta 30,13,9216
Maaa 228,8,52
Maaa 228,12,148
D 6,40,64,0
Maaa 196,12,144
Maaa 196,8,48
Maaa 164,8,44
Maaa 164,12,140
D 7,40,64,1024
Mvaa 228,12,136
Mvaa 228,8,40
Mvaa 196,8,36
Mvaa 196,12,132
D 8,40,64,2048
Mvaa 164,12,128
Mvaa 164,8,32
W 4
Mvaa 168,16,32
Mvaa 168,20,128
D 9,40,64,3072
Mvaa 200,20,132
Ta 0,14,0
Ta 2,14,1024
Mvaa 200,16,36
Ta 4,14,8192
Ta 6,14,9216
Mvaa 232,16,40
Mvaa 232,20,136
global_load_dword v142, v[140:141], off
v_lshl_add_u64 v[140:141], v[140:141], 0, s[68:69]
Maaa 168,20,140
Maaa 168,16,44
Maaa 200,16,48
Maaa 200,20,144
Maaa 232,20,148
Maaa 232,16,52
Maaa 72,16,56
Maaa 72,20,152
Maaa 104,20,156
Maaa 104,16,60
W 4
Maaa 108,24,60
Maaa 108,28,156
Maaa 76,28,152
Ta 8,15,0
Ta 10,15,1024
Maaa 76,24,56
Ta 12,15,8192
Ta 14,15,9216
Maaa 236,24,52
Maaa 236,28,148
Maaa 204,28,144
Maaa 204,24,48
Maaa 172,24,44
Maaa 172,28,140
Mvaa 236,28,136
Mvaa 236,24,40
Mvaa 204,24,36
Mvaa 204,28,132
Mvaa 172,28,128
Mvaa 172,24,32
W 4
Mvaa 176,0,32
Mvaa 176,4,128
Mvaa 208,4,132
Ta 16,24,0
Ta 18,24,1024
Mvaa 208,0,36
Ta 20,24,8192
Ta 22,24,9216
Mvaa 240,0,40
Mvaa 240,4,136
Maaa 176,4,140
Maaa 176,0,44
Maaa 208,0,48
Maaa 208,4,144
Maaa 240,4,148
Maaa 240,0,52
Maaa 80,0,56
Maaa 80,4,152
Maaa 112,4,156
Maaa 112,0,60
W 4
Maaa 116,8,60
Ta 24,25,0
Ta 26,25,1024
Maaa 116,12,156
Maaa 84,12,152
Ta 28,25,8192
Ta 30,25,9216
Maaa 84,8,56
Maaa 244,8,52
Maaa 244,12,148
Maaa 212,12,144
Maaa 212,8,48
Maaa 180,8,44
Maaa 180,12,140
Mvaa 244,12,136
s_waitcnt vmcnt(1) lgkmcnt(0)
s_barrier
s_add_u32 s64, s64, 128
Mvaa 244,8,40
Mvaa 212,8,36
Mvaa 212,12,132
s_and_b32 s64, s64, 1023
Mvaa 180,12,128
Mvaa 180,8,32
Mvaa 184,16,32
s_lshl_b32 s65, s64, 12
Mvaa 184,20,128
Mvaa 216,20,132
s_add_u32 m0, s62, 0
Ta 0,10,32768
Ta 2,10,33792
Mvaa 216,16,36
Ta 4,10,40960
Ta 6,10,41984
Mvaa 248,16,40
Mvaa 248,20,136
D 16,44,65,0
Maaa 184,20,140
Maaa 184,16,44
Maaa 216,16,48
Maaa 216,20,144
D 17,44,65,1024
Maaa 248,20,148
Maaa 248,16,52
Maaa 88,16,56
Maaa 88,20,152
D 18,44,65,2048
Maaa 120,20,156
Maaa 120,16,60
Maaa 124,24,60
Ra 60,26,47104
Maaa 124,28,156
D 19,44,65,3072
Ra 156,27,47104
Maaa 92,28,152
s_add_u32 m0, s62, 4096
Ra 152,27,45056
Ta 8,11,32768
Maaa 92,24,56
Ta 10,11,33792
Ra 56,26,45056
Maaa 252,24,52
Ta 12,11,40960
Ta 14,11,41984
Maaa 252,28,148
D 20,44,65,0
Ra 52,26,43008
Maaa 220,28,144
Ra 148,27,43008
Ra 144,27,40960
Maaa 220,24,48
Ra 48,26,40960
Maaa 188,24,44
Ra 44,26,38912
Maaa 188,28,140
D 21,44,65,1024
Ra 140,27,38912
Mvaa 252,28,136
Ra 136,27,36864
Mvaa 252,24,40
Ra 40,26,36864
Mvaa 220,24,36
Ra 36,26,34816
Mvaa 220,28,132
D 22,44,65,2048
Ra 132,27,34816
Mvaa 188,28,128
Ra 128,27,32768
Mvaa 188,24,32
W 13
Ra 32,26,32768
Maaa 96,0,60
Maaa 96,4,156
D 23,44,65,3072
Maaa 64,4,152
s_add_u32 m0, s61, 0
Ta 16,12,32768
Ta 18,12,33792
Maaa 64,0,56
W 10
Ta 20,12,40960
Ta 22,12,41984
Maaa 224,0,52
Maaa 224,4,148
D 2,40,64,0
Maaa 192,4,144
Maaa 192,0,48
W 8
Maaa 160,0,44
Maaa 160,4,140
D 3,40,64,1024
Mvaa 224,4,136
Mvaa 224,0,40
W 4
Mvaa 192,0,36
Mvaa 192,4,132
D 4,40,64,2048
Mvaa 160,4,128
Mvaa 160,0,32
Mvaa 164,8,32
Mvaa 164,12,128
D 5,40,64,3072
Mvaa 196,12,132
s_add_u32 m0, s61, 4096
Ta 24,13,32768
Ta 26,13,33792
Mvaa 196,8,36
Ta 28,13,40960
Ta 30,13,41984
Mvaa 228,8,40
Mvaa 228,12,136
D 6,40,64,0
Maaa 164,12,140
Maaa 164,8,44
Maaa 196,8,48
Maaa 196,12,144
D 7,40,64,1024
Maaa 228,12,148
Maaa 228,8,52
Maaa 68,8,56
Maaa 68,12,152
D 8,40,64,2048
Maaa 100,12,156
Maaa 100,8,60
W 4
Maaa 104,16,60
Maaa 104,20,156
D 9,40,64,3072
Maaa 72,20,152
Ta 0,14,32768
Ta 2,14,33792
Maaa 72,16,56
Ta 4,14,40960
Ta 6,14,41984
Maaa 232,16,52
Maaa 232,20,148
global_load_dword v142, v[140:141], off
v_lshl_add_u64 v[140:141], v[140:141], 0, s[68:69]
Maaa 200,20,144
Maaa 200,16,48
Maaa 168,16,44
Maaa 168,20,140
Mvaa 232,20,136
Mvaa 232,16,40
Mvaa 200,16,36
Mvaa 200,20,132
Mvaa 168,20,128
Mvaa 168,16,32
W 4
Mvaa 172,24,32
Mvaa 172,28,128
Mvaa 204,28,132
Ta 8,15,32768
Ta 10,15,33792
Mvaa 204,24,36
Ta 12,15,40960
Ta 14,15,41984
Mvaa 236,24,40
Mvaa 236,28,136
Maaa 172,28,140
Maaa 172,24,44
Maaa 204,24,48
Maaa 204,28,144
Maaa 236,28,148
Maaa 236,24,52
Maaa 76,24,56
Maaa 76,28,152
Maaa 108,28,156
Maaa 108,24,60
W 4
Maaa 112,0,60
Maaa 112,4,156
Maaa 80,4,152
Ta 16,24,32768
Ta 18,24,33792
Maaa 80,0,56
Ta 20,24,40960
Ta 22,24,41984
Maaa 240,0,52
Maaa 240,4,148
Maaa 208,4,144
Maaa 208,0,48
Maaa 176,0,44
Maaa 176,4,140
Mvaa 240,4,136
Mvaa 240,0,40
Mvaa 208,0,36
Mvaa 208,4,132
Mvaa 176,4,128
Mvaa 176,0,32
W 4
Mvaa 180,8,32
Ta 24,25,32768
Ta 26,25,33792
Mvaa 180,12,128
Mvaa 212,12,132
Ta 28,25,40960
Ta 30,25,41984
Mvaa 212,8,36
Mvaa 244,8,40
Mvaa 244,12,136
.Lh_0_2%=:
s_cbranch_execz .Lh_0_2_1%=
Maaa 180,12,140
Maaa 180,8,44
Maaa 212,8,48
Maaa 212,12,144
Maaa 244,12,148
s_waitcnt vmcnt(1) lgkmcnt(0)
s_barrier
s_add_u32 s64, s64, 128
Maaa 244,8,52
Maaa 84,8,56
Maaa 84,12,152
s_and_b32 s64, s64, 1023
Maaa 116,12,156
Maaa 116,8,60
Maaa 120,16,60
s_lshl_b32 s65, s64, 12
Maaa 120,20,156
Maaa 88,20,152
Ta 0,10,0
Ta 2,10,1024
Maaa 88,16,56
s_add_u32 m0, s62, 32768
Ta 4,10,8192
Ta 6,10,9216
Maaa 248,16,52
Maaa 248,20,148
Maaa 216,20,144
D 16,44,65,0
Maaa 216,16,48
Maaa 184,16,44
Maaa 184,20,140
Mvaa 248,20,136
D 17,44,65,1024
Mvaa 248,16,40
Mvaa 216,16,36
Mvaa 216,20,132
Mvaa 184,20,128
)"
R"(D 18,44,65,2048
Mvaa 184,16,32
Mvaa 188,24,32
Ra 32,26,0
Mvaa 188,28,128
Ra 128,27,0
Mvaa 220,28,132
D 19,44,65,3072
Ra 132,27,2048
Mvaa 220,24,36
s_add_u32 m0, s62, 36864
Ta 8,11,0
Ta 10,11,1024
Mvaa 252,24,40
Ra 36,26,2048
Ta 12,11,8192
Mvaa 252,28,136
Ta 14,11,9216
Ra 40,26,4096
Maaa 188,28,140
D 20,44,65,0
Ra 136,27,4096
Maaa 188,24,44
Ra 140,27,6144
Ra 44,26,6144
Maaa 220,24,48
Ra 48,26,8192
Maaa 220,28,144
Ra 144,27,8192
Maaa 252,28,148
D 21,44,65,1024
Ra 148,27,10240
Maaa 252,24,52
Ra 52,26,10240
Maaa 92,24,56
Ra 56,26,12288
Maaa 92,28,152
Ra 152,27,12288
Maaa 124,28,156
D 22,44,65,2048
Ra 156,27,14336
Maaa 124,24,60
W 13
Ra 60,26,14336
Mvaa 160,0,32
Mvaa 160,4,128
Mvaa 192,4,132
D 23,44,65,3072
Ta 16,12,0
Mvaa 192,0,36
W 9
s_add_u32 m0, s61, 32768
Ta 18,12,1024
Mvaa 224,0,40
Ta 20,12,8192
Ta 22,12,9216
Mvaa 224,4,136
Maaa 160,4,140
D 2,40,64,0
Maaa 160,0,44
W 8
Maaa 192,0,48
Maaa 192,4,144
Maaa 224,4,148
D 3,40,64,1024
Maaa 224,0,52
W 4
Maaa 64,0,56
Maaa 64,4,152
Maaa 96,4,156
D 4,40,64,2048
Maaa 96,0,60
Maaa 100,8,60
Maaa 100,12,156
Maaa 68,12,152
D 5,40,64,3072
Ta 24,13,0
Maaa 68,8,56
s_add_u32 m0, s61, 36864
Ta 26,13,1024
Ta 28,13,8192
Maaa 228,8,52
Ta 30,13,9216
Maaa 228,12,148
Maaa 196,12,144
D 6,40,64,0
Maaa 196,8,48
Maaa 164,8,44
Maaa 164,12,140
Mvaa 228,12,136
D 7,40,64,1024
Mvaa 228,8,40
Mvaa 196,8,36
Mvaa 196,12,132
Mvaa 164,12,128
D 8,40,64,2048
Mvaa 164,8,32
W 4
Mvaa 168,16,32
Mvaa 168,20,128
Mvaa 200,20,132
D 9,40,64,3072
Ta 0,14,0
Mvaa 200,16,36
s_add_u32 s92, s92, 2097152
Ta 2,14,1024
Ta 4,14,8192
Mvaa 232,16,40
Ta 6,14,9216
Mvaa 232,20,136
Maaa 168,20,140
buffer_load_dword v142, v147, s[48:51], s92 offen
Maaa 168,16,44
s_add_u32 s92, s92, 2097152
Maaa 200,16,48
Maaa 200,20,144
Maaa 232,20,148
buffer_load_dword v142, v147, s[48:51], s92 offen
Maaa 232,16,52
Maaa 72,16,56
Maaa 72,20,152
Maaa 104,20,156
Maaa 104,16,60
W 4
Maaa 108,24,60
Maaa 108,28,156
Maaa 76,28,152
Ta 8,15,0
Ta 10,15,1024
Maaa 76,24,56
Ta 12,15,8192
Ta 14,15,9216
Maaa 236,24,52
Maaa 236,28,148
Maaa 204,28,144
Maaa 204,24,48
Maaa 172,24,44
Maaa 172,28,140
Mvaa 236,28,136
Mvaa 236,24,40
Mvaa 204,24,36
Mvaa 204,28,132
Mvaa 172,28,128
Mvaa 172,24,32
W 4
Mvaa 176,0,32
Mvaa 176,4,128
Mvaa 208,4,132
Ta 16,24,0
Ta 18,24,1024
Mvaa 208,0,36
Ta 20,24,8192
Ta 22,24,9216
Mvaa 240,0,40
Mvaa 240,4,136
Maaa 176,4,140
Maaa 176,0,44
Maaa 208,0,48
Maaa 208,4,144
Maaa 240,4,148
Maaa 240,0,52
Maaa 80,0,56
Maaa 80,4,152
Maaa 112,4,156
Maaa 112,0,60
W 4
Maaa 116,8,60
Ta 24,25,0
Ta 26,25,1024
Maaa 116,12,156
Maaa 84,12,152
Ta 28,25,8192
Ta 30,25,9216
Maaa 84,8,56
Maaa 244,8,52
Maaa 244,12,148
Maaa 212,12,144
Maaa 212,8,48
Maaa 180,8,44
Maaa 180,12,140
Mvaa 244,12,136
s_waitcnt vmcnt(2) lgkmcnt(0)
s_barrier
s_add_u32 s64, s64, 128
Mvaa 244,8,40
Mvaa 212,8,36
Mvaa 212,12,132
s_and_b32 s64, s64, 1023
Mvaa 180,12,128
Mvaa 180,8,32
Mvaa 184,16,32
s_lshl_b32 s65, s64, 12
Mvaa 184,20,128
Mvaa 216,20,132
Ta 0,10,32768
Ta 2,10,33792
Mvaa 216,16,36
s_add_u32 m0, s62, 0
Ta 4,10,40960
Ta 6,10,41984
Mvaa 248,16,40
Mvaa 248,20,136
Maaa 184,20,140
D 16,44,65,0
Maaa 184,16,44
Maaa 216,16,48
Maaa 216,20,144
Maaa 248,20,148
D 17,44,65,1024
Maaa 248,16,52
Maaa 88,16,56
Maaa 88,20,152
Maaa 120,20,156
D 18,44,65,2048
Maaa 120,16,60
Maaa 124,24,60
Ra 60,26,47104
Maaa 124,28,156
Ra 156,27,47104
Maaa 92,28,152
D 19,44,65,3072
Ra 152,27,45056
Maaa 92,24,56
s_add_u32 m0, s62, 4096
Ta 8,11,32768
Ta 10,11,33792
Maaa 252,24,52
Ra 56,26,45056
Ta 12,11,40960
Maaa 252,28,148
Ta 14,11,41984
Ra 52,26,43008
Maaa 220,28,144
D 20,44,65,0
v_lshrrev_b32 v28, 1, v1
Maaa 220,24,48
s_lshl_b32 s80, s60, 9
Ra 148,27,43008
Ra 144,27,40960
Maaa 188,24,44
s_add_u32 s80, s80, 40960
Ra 48,26,40960
Ra 44,26,38912
Maaa 188,28,140
Ra 140,27,38912
Mvaa 252,28,136
D 21,44,65,1024
Ra 136,27,36864
Mvaa 252,24,40
Ra 40,26,36864
Mvaa 220,24,36
Ra 36,26,34816
Mvaa 220,28,132
Ra 132,27,34816
Mvaa 188,28,128
buffer_load_dwordx2 v[28:29], v28, s[8:11], s80 offen sc1
Ra 128,27,32768
Mvaa 188,24,32
W 13
Ra 32,26,32768
Maaa 96,0,60
Maaa 96,4,156
Maaa 64,4,152
D 22,44,65,2048
Ta 16,12,32768
Maaa 64,0,56
W 9
Ta 18,12,33792
Ta 20,12,40960
Maaa 224,0,52
Ta 22,12,41984
Maaa 224,4,148
Maaa 192,4,144
D 23,44,65,3072
Maaa 192,0,48
W 8
s_add_u32 m0, s61, 0
Maaa 160,0,44
Maaa 160,4,140
Mvaa 224,4,136
D 2,40,64,0
Mvaa 224,0,40
W 4
Mvaa 192,0,36
Mvaa 192,4,132
Mvaa 160,4,128
D 3,40,64,1024
Mvaa 160,0,32
Mvaa 164,8,32
Mvaa 164,12,128
Mvaa 196,12,132
D 4,40,64,2048
Ta 24,13,32768
Mvaa 196,8,36
Ta 26,13,33792
Ta 28,13,40960
Mvaa 228,8,40
Ta 30,13,41984
Mvaa 228,12,136
Maaa 164,12,140
D 5,40,64,3072
Maaa 164,8,44
s_add_u32 m0, s61, 4096
Maaa 196,8,48
Maaa 196,12,144
Maaa 228,12,148
D 6,40,64,0
Maaa 228,8,52
Maaa 68,8,56
Maaa 68,12,152
Maaa 100,12,156
D 7,40,64,1024
Maaa 100,8,60
W 4
Maaa 104,16,60
Maaa 104,20,156
Maaa 72,20,152
D 8,40,64,2048
Ta 0,14,32768
Maaa 72,16,56
Ta 2,14,33792
Ta 4,14,40960
Maaa 232,16,52
Ta 6,14,41984
Maaa 232,20,148
Maaa 200,20,144
D 9,40,64,3072
Maaa 200,16,48
Maaa 168,16,44
Maaa 168,20,140
Mvaa 232,20,136
s_waitcnt vmcnt(10)
v_cmp_eq_u32 vcc, s13, v29
s_cmp_eq_u64 vcc, -1
s_cbranch_scc0 .Lspin_15%=
.Lok_16%=:
global_load_dword v142, v146, s[6:7]
Mvaa 232,16,40
Mvaa 200,16,36
v_bfe_u32 v144, v1, 8, 1
Mvaa 200,20,132
v_mul_u32_u24 v144, 10240, v144
Mvaa 168,20,128
v_and_b32 v145, 0xf0, v1
Mvaa 168,16,32
W 4
v_lshl_add_u32 v144, v145, 3, v144
Mvaa 172,24,32
s_mul_i32 s91, s58, 20480
Mvaa 172,28,128
s_add_u32 s91, s91, s28
Mvaa 204,28,132
buffer_load_dword v142, v144, s[8:11], s91 offen offset:0 sc1
Ta 8,15,32768
Mvaa 204,24,36
Ta 10,15,33792
Ta 12,15,40960
Mvaa 236,24,40
Ta 14,15,41984
Mvaa 236,28,136
Maaa 172,28,140
Maaa 172,24,44
Maaa 204,24,48
Maaa 204,28,144
Maaa 236,28,148
Maaa 236,24,52
Maaa 76,24,56
Maaa 76,28,152
Maaa 108,28,156
Maaa 108,24,60
W 4
Maaa 112,0,60
Maaa 112,4,156
Maaa 80,4,152
Ta 16,24,32768
Ta 18,24,33792
Maaa 80,0,56
Ta 20,24,40960
Ta 22,24,41984
Maaa 240,0,52
Maaa 240,4,148
Maaa 208,4,144
Maaa 208,0,48
Maaa 176,0,44
Maaa 176,4,140
Mvaa 240,4,136
Mvaa 240,0,40
Mvaa 208,0,36
Mvaa 208,4,132
Mvaa 176,4,128
Mvaa 176,0,32
W 4
Mvaa 180,8,32
Ta 24,25,32768
Ta 26,25,33792
Mvaa 180,12,128
Mvaa 212,12,132
Ta 28,25,40960
Ta 30,25,41984
Mvaa 212,8,36
)"
R"(Mvaa 244,8,40
Mvaa 244,12,136
Maaa 180,12,140
Maaa 180,8,44
Maaa 212,8,48
Maaa 212,12,144
Maaa 244,12,148
s_waitcnt vmcnt(2) lgkmcnt(0)
s_barrier
s_add_u32 s64, s64, 128
Maaa 244,8,52
Maaa 84,8,56
Maaa 84,12,152
s_and_b32 s64, s64, 1023
Maaa 116,12,156
Maaa 116,8,60
Maaa 120,16,60
s_lshl_b32 s65, s64, 12
Maaa 120,20,156
Maaa 88,20,152
Ta 0,10,0
Ta 2,10,1024
Maaa 88,16,56
s_add_u32 m0, s62, 32768
Ta 4,10,8192
Ta 6,10,9216
Maaa 248,16,52
Maaa 248,20,148
Maaa 216,20,144
D 16,44,65,0
Maaa 216,16,48
Maaa 184,16,44
Maaa 184,20,140
Mvaa 248,20,136
D 17,44,65,1024
Mvaa 248,16,40
Mvaa 216,16,36
Mvaa 216,20,132
Mvaa 184,20,128
D 18,44,65,2048
Mvaa 184,16,32
Mvaa 188,24,32
Ra 32,26,0
Mvaa 188,28,128
Ra 128,27,0
Mvaa 220,28,132
D 19,44,65,3072
Ra 132,27,2048
Mvaa 220,24,36
s_add_u32 m0, s62, 36864
Ta 8,11,0
Ta 10,11,1024
Mvaa 252,24,40
Ra 36,26,2048
Ta 12,11,8192
Mvaa 252,28,136
Ta 14,11,9216
Ra 40,26,4096
Maaa 188,28,140
D 20,44,65,0
Ra 136,27,4096
Maaa 188,24,44
Ra 140,27,6144
Ra 44,26,6144
Maaa 220,24,48
Ra 48,26,8192
Maaa 220,28,144
Ra 144,27,8192
Maaa 252,28,148
D 21,44,65,1024
Ra 148,27,10240
Maaa 252,24,52
Ra 52,26,10240
Maaa 92,24,56
Ra 56,26,12288
Maaa 92,28,152
Ra 152,27,12288
Maaa 124,28,156
D 22,44,65,2048
Ra 156,27,14336
Maaa 124,24,60
W 13
Ra 60,26,14336
Mvaa 160,0,32
Mvaa 160,4,128
Mvaa 192,4,132
D 23,44,65,3072
Ta 16,12,0
Mvaa 192,0,36
W 9
s_add_u32 m0, s61, 32768
Ta 18,12,1024
Mvaa 224,0,40
Ta 20,12,8192
Ta 22,12,9216
Mvaa 224,4,136
.Lh_0_3%=:
s_cbranch_execz .Lh_0_3_1%=
Maaa 160,4,140
D 2,40,64,0
Maaa 160,0,44
W 8
Maaa 192,0,48
Maaa 192,4,144
Maaa 224,4,148
D 3,40,64,1024
Maaa 224,0,52
W 4
Maaa 64,0,56
Maaa 64,4,152
Maaa 96,4,156
D 4,40,64,2048
Maaa 96,0,60
Maaa 100,8,60
Maaa 100,12,156
Maaa 68,12,152
D 5,40,64,3072
Ta 24,13,0
Maaa 68,8,56
s_add_u32 m0, s61, 36864
Ta 26,13,1024
Ta 28,13,8192
Maaa 228,8,52
Ta 30,13,9216
Maaa 228,12,148
Maaa 196,12,144
D 6,40,64,0
Maaa 196,8,48
Maaa 164,8,44
Maaa 164,12,140
Mvaa 228,12,136
D 7,40,64,1024
Mvaa 228,8,40
Mvaa 196,8,36
Mvaa 196,12,132
Mvaa 164,12,128
D 8,40,64,2048
Mvaa 164,8,32
W 4
Mvaa 168,16,32
Mvaa 168,20,128
Mvaa 200,20,132
D 9,40,64,3072
v_add_u32 v2, 131072, v10
Mvaa 200,16,36
Ta 0,14,0
v_add_u32 v3, 131072, v11
Mvaa 232,16,40
Ta 2,14,1024
v_add_u32 v4, 131072, v12
Mvaa 232,20,136
Ta 4,14,8192
Ta 6,14,9216
Maaa 168,20,140
buffer_load_dword v142, v144, s[8:11], s91 offen offset:2048 sc1
v_add_u32 v5, 131072, v13
Maaa 168,16,44
s_add_u32 s91, s91, 4096
v_add_u32 v6, 131072, v14
Maaa 200,16,48
v_add_u32 v7, 131072, v15
Maaa 200,20,144
Maaa 232,20,148
v_add_u32 v8, 131072, v24
Maaa 232,16,52
Maaa 72,16,56
v_add_u32 v9, 131072, v25
Maaa 72,20,152
s_add_u32 s65, s5, 0
s_cmp_ge_u32 s65, 20
s_cselect_b32 s90, 20, 0
s_sub_u32 s65, s65, s90
Maaa 104,20,156
s_lshl_b32 s65, s65, 18
s_add_u32 m0, s63, 131072
Maaa 104,16,60
W 4
Maaa 108,24,60
Maaa 108,28,156
Maaa 76,28,152
Ta 8,15,0
Ta 10,15,1024
Maaa 76,24,56
D 16,48,65,0
Ta 12,15,8192
Maaa 236,24,52
Ta 14,15,9216
Maaa 236,28,148
Maaa 204,28,144
Maaa 204,24,48
D 17,48,65,1024
Maaa 172,24,44
Maaa 172,28,140
Mvaa 236,28,136
Mvaa 236,24,40
Mvaa 204,24,36
D 18,48,65,2048
Mvaa 204,28,132
Mvaa 172,28,128
Mvaa 172,24,32
W 4
Mvaa 176,0,32
Mvaa 176,4,128
D 19,48,65,3072
Mvaa 208,4,132
s_add_u32 m0, s63, 135168
Ta 16,24,0
Ta 18,24,1024
Mvaa 208,0,36
Ta 20,24,8192
Ta 22,24,9216
Mvaa 240,0,40
Mvaa 240,4,136
Maaa 176,4,140
D 20,48,65,0
Maaa 176,0,44
Maaa 208,0,48
Maaa 208,4,144
Maaa 240,4,148
Maaa 240,0,52
D 21,48,65,1024
Maaa 80,0,56
Maaa 80,4,152
Maaa 112,4,156
Maaa 112,0,60
W 4
Maaa 116,8,60
D 22,48,65,2048
Ta 24,25,0
Maaa 116,12,156
Ta 26,25,1024
Maaa 84,12,152
Ta 28,25,8192
Ta 30,25,9216
Maaa 84,8,56
Maaa 244,8,52
D 23,48,65,3072
Maaa 244,12,148
Maaa 212,12,144
Maaa 212,8,48
Maaa 180,8,44
Maaa 180,12,140
Mvaa 244,12,136
s_waitcnt vmcnt(9) lgkmcnt(0)
s_barrier
s_add_u32 s65, s5, 4
Mvaa 244,8,40
Mvaa 212,8,36
Mvaa 212,12,132
s_cmp_ge_u32 s65, 20
s_cselect_b32 s90, 20, 0
Mvaa 180,12,128
Mvaa 180,8,32
s_sub_u32 s65, s65, s90
Mvaa 184,16,32
Mvaa 184,20,128
s_lshl_b32 s65, s65, 18
Mvaa 216,20,132
Ta 0,10,32768
Ta 2,10,33792
Mvaa 216,16,36
Mvaa 248,16,40
s_add_u32 m0, s63, 0
Mvaa 248,20,136
Maaa 184,20,140
D 16,48,65,0
Maaa 184,16,44
Maaa 216,16,48
Maaa 216,20,144
Maaa 248,20,148
D 17,48,65,1024
Ta 4,11,32768
Maaa 248,16,52
Ta 6,11,33792
Maaa 88,16,56
Maaa 88,20,152
Maaa 120,20,156
D 18,48,65,2048
Maaa 120,16,60
Maaa 124,24,60
Ra 60,26,47104
Maaa 124,28,156
Maaa 92,28,152
D 19,48,65,3072
Ta 8,12,32768
Maaa 92,24,56
s_add_u32 m0, s63, 4096
Ta 10,12,33792
Ra 56,26,45056
Maaa 252,24,52
Ra 52,26,43008
Maaa 252,28,148
Maaa 220,28,144
D 20,48,65,0
Maaa 220,24,48
Ra 48,26,40960
Maaa 188,24,44
Ra 44,26,38912
Maaa 188,28,140
Mvaa 252,28,136
D 21,48,65,1024
Ta 12,13,32768
Mvaa 252,24,40
Ta 14,13,33792
Ra 40,26,36864
Mvaa 220,24,36
Ra 36,26,34816
Mvaa 220,28,132
Mvaa 188,28,128
D 22,48,65,2048
Mvaa 188,24,32
Ra 32,26,32768
W 6
Maaa 96,0,60
Maaa 64,0,56
Maaa 224,0,52
D 23,48,65,3072
Ta 16,14,32768
Maaa 192,0,48
W 1
s_add_u32 s65, s5, 8
s_cmp_ge_u32 s65, 20
s_cselect_b32 s90, 20, 0
Maaa 160,0,44
s_sub_u32 s65, s65, s90
s_lshl_b32 s65, s65, 18
Ta 18,14,33792
Mvaa 224,0,40
s_add_u32 m0, s63, 65536
Mvaa 192,0,36
D 16,48,65,0
Mvaa 160,0,32
Mvaa 164,4,32
Mvaa 196,4,36
Mvaa 228,4,40
D 17,48,65,1024
Ta 20,15,32768
Maaa 164,4,44
Ta 22,15,33792
Maaa 196,4,48
Maaa 228,4,52
Maaa 68,4,56
D 18,48,65,2048
Maaa 100,4,60
Maaa 104,8,60
Maaa 72,8,56
Maaa 232,8,52
D 19,48,65,3072
Ta 24,24,32768
Maaa 200,8,48
s_add_u32 m0, s63, 69632
Ta 26,24,33792
Maaa 168,8,44
Mvaa 232,8,40
Mvaa 200,8,36
D 20,48,65,0
Mvaa 168,8,32
Mvaa 172,12,32
Mvaa 204,12,36
Mvaa 236,12,40
D 21,48,65,1024
Ta 28,25,32768
Maaa 172,12,44
Ta 30,25,33792
Maaa 204,12,48
Ra 156,27,47104
Maaa 236,12,52
Maaa 76,12,56
D 22,48,65,2048
Maaa 108,12,60
W 7
Ra 152,27,45056
Maaa 112,16,60
Maaa 80,16,56
Maaa 240,16,52
D 23,48,65,3072
Ra 148,27,43008
Maaa 208,16,48
Ta 0,10,40960
Ta 2,10,41984
Maaa 176,16,44
Mvaa 240,16,40
Ra 144,27,40960
Mvaa 208,16,36
buffer_load_dword v142, v144, s[8:11], s91 offen offset:0 sc1
Mvaa 176,16,32
W 10
Mvaa 180,20,32
Ra 140,27,38912
Mvaa 212,20,36
Mvaa 244,20,40
Ta 4,11,40960
Ta 6,11,41984
Maaa 180,20,44
Ra 136,27,36864
Maaa 212,20,48
Maaa 244,20,52
v_bfe_u32 v29, v1, 7, 1
Maaa 84,20,56
Ra 132,27,34816
)"
R"(Maaa 116,20,60
W 13
v_mul_u32_u24 v28, 10240, v29
Maaa 120,24,60
Maaa 88,24,56
Ra 128,27,32768
v_lshrrev_b32 v29, 8, v1
Maaa 248,24,52
Ta 8,12,40960
Ta 10,12,41984
Maaa 216,24,48
v_lshl_add_u32 v28, v29, 7, v28
Maaa 184,24,44
v_and_b32 v29, 0x70, v1
Mvaa 248,24,40
Mvaa 216,24,36
v_add_u32 v28, v28, v29
Mvaa 184,24,32
W 14
Mvaa 188,28,32
s_and_b32 s80, s14, 2
Mvaa 220,28,36
s_mul_i32 s80, s80, 20480
Mvaa 252,28,40
Ta 12,13,40960
Ta 14,13,41984
Maaa 188,28,44
s_sub_u32 s81, 40960, s80
Maaa 220,28,48
Maaa 252,28,52
s_add_u32 s93, s28, 20480
Maaa 92,28,56
Maaa 124,28,60
W 10
v_add_u32 v26, s80, v28
Maaa 96,0,156
Maaa 64,0,152
Maaa 224,0,148
Ta 16,14,40960
v_add_u32 v27, s81, v28
Maaa 192,0,144
W 5
Ta 18,14,41984
Maaa 160,0,140
buffer_load_dwordx4 a[32:35], v26, s[8:11], s28 offen offset:0 sc1
Mvaa 224,0,136
Mvaa 192,0,132
Mvaa 160,0,128
Mvaa 164,4,128
buffer_load_dwordx4 a[36:39], v26, s[8:11], s93 offen offset:0 sc1
Mvaa 196,4,132
Mvaa 228,4,136
Ta 20,15,40960
Ta 22,15,41984
Maaa 164,4,140
Maaa 196,4,144
buffer_load_dwordx4 a[40:43], v27, s[8:11], s28 offen offset:0 sc1
Maaa 228,4,148
Maaa 68,4,152
Maaa 100,4,156
W 6
Maaa 104,8,156
buffer_load_dwordx4 a[44:47], v27, s[8:11], s93 offen offset:0 sc1
Ta 24,24,40960
Maaa 72,8,152
s_add_u32 s28, s28, 512
s_cmp_eq_u32 s28, s4
s_cselect_b32 s93, 10240, 0
Ta 26,24,41984
Maaa 232,8,148
s_sub_u32 s28, s28, s93
Ta 28,25,40960
Ta 30,25,41984
Maaa 200,8,144
s_add_u32 s93, s28, 20480
Maaa 168,8,140
Mvaa 232,8,136
Mvaa 200,8,132
s_add_u32 s65, s5, 12
s_cmp_ge_u32 s65, 20
s_cselect_b32 s90, 20, 0
buffer_load_dwordx4 a[48:51], v26, s[8:11], s28 offen offset:0 sc1
Mvaa 168,8,128
W 8
s_sub_u32 s65, s65, s90
s_lshl_b32 s65, s65, 18
Mvaa 172,12,128
s_add_u32 m0, s63, 32768
Mvaa 204,12,132
Mvaa 236,12,136
s_waitcnt vmcnt(6) lgkmcnt(0)
s_barrier
buffer_load_dwordx4 a[52:55], v26, s[8:11], s93 offen offset:0 sc1
Maaa 172,12,140
Maaa 204,12,144
Maaa 236,12,148
Maaa 76,12,152
buffer_load_dwordx4 a[56:59], v27, s[8:11], s28 offen offset:0 sc1
Maaa 108,12,156
Maaa 112,16,156
Maaa 80,16,152
Maaa 240,16,148
buffer_load_dwordx4 a[60:63], v27, s[8:11], s93 offen offset:0 sc1
Maaa 208,16,144
Maaa 176,16,140
Mvaa 240,16,136
Mvaa 208,16,132
D 16,48,65,0
Mvaa 176,16,128
Mvaa 180,20,128
Mvaa 212,20,132
Mvaa 244,20,136
D 17,48,65,1024
Ta 0,2,0
Maaa 180,20,140
Ta 2,2,1024
Maaa 212,20,144
Maaa 244,20,148
Maaa 84,20,152
D 18,48,65,2048
Ta 4,2,8192
Maaa 116,20,156
Ta 6,2,9216
Maaa 120,24,156
Maaa 88,24,152
Maaa 248,24,148
Ta 8,3,0
Ta 10,3,1024
Maaa 216,24,144
D 19,48,65,3072
Maaa 184,24,140
s_add_u32 m0, s63, 36864
Mvaa 248,24,136
Mvaa 216,24,132
Ta 12,3,8192
Ta 14,3,9216
Mvaa 184,24,128
D 20,48,65,0
Mvaa 188,28,128
Mvaa 220,28,132
Mvaa 252,28,136
Ta 16,4,0
Ta 18,4,1024
Maaa 188,28,140
Maaa 220,28,144
D 21,48,65,1024
Maaa 252,28,148
Maaa 92,28,152
Maaa 124,28,156
s_branch .Lq_11%=
.p2align 12
.Lv_10%=:
s_waitcnt vmcnt(14)
s_barrier
Ta 0,10,16384
Ta 2,10,17408
Ra 32,26,0
Ta 4,10,24576
.Lh_0_0_1%=:
s_cbranch_execz .Lhr_9%=
Ta 6,10,25600
Ra 128,27,0
Ra 132,27,2048
Ra 36,26,2048
Ra 40,26,4096
Ra 136,27,4096
Ra 140,27,6144
Ra 44,26,6144
Ra 48,26,8192
Ra 144,27,8192
W 6
Zvaa 160,32,0
s_add_u32 m0, s61, 32768
Ra 148,27,10240
Ra 52,26,10240
Ra 56,26,12288
Ra 152,27,12288
Ra 156,27,14336
Ra 60,26,14336
Mvaa 160,128,4
Ta 8,11,16384
Ta 10,11,17408
Ta 12,11,24576
Ta 14,11,25600
Zvaa 164,132,4
Ta 16,12,16384
Ta 18,12,17408
Mvaa 164,36,0
W 14
Ta 20,12,24576
Ta 22,12,25600
Zvaa 224,40,0
D 2,40,64,0
Mvaa 224,136,4
Zvaa 228,140,4
Mvaa 228,44,0
W 12
Zaaa 192,48,0
D 3,40,64,1024
Maaa 192,144,4
Zaaa 196,148,4
Maaa 196,52,0
W 8
Zaaa 64,56,0
D 4,40,64,2048
Maaa 64,152,4
Zaaa 68,156,4
Maaa 68,60,0
W 4
Zaaa 76,60,8
D 5,40,64,3072
Maaa 76,156,12
s_add_u32 m0, s61, 36864
Zaaa 72,152,12
Ta 24,13,16384
Ta 26,13,17408
Maaa 72,56,8
Ta 28,13,24576
Ta 30,13,25600
Zaaa 204,52,8
s_waitcnt vmcnt(15)
D 6,40,64,0
Maaa 204,148,12
v_lshlrev_b32 v56, 16, v32
v_and_b32 v57, 0xffff0000, v32
v_lshlrev_b32 v58, 16, v33
v_and_b32 v59, 0xffff0000, v33
v_lshlrev_b32 v60, 16, v34
Zaaa 200,144,12
v_and_b32 v61, 0xffff0000, v34
v_lshlrev_b32 v62, 16, v35
v_and_b32 v63, 0xffff0000, v35
v_lshlrev_b32 v64, 16, v36
v_and_b32 v65, 0xffff0000, v36
Maaa 200,48,8
v_lshlrev_b32 v66, 16, v37
v_and_b32 v67, 0xffff0000, v37
v_lshlrev_b32 v68, 16, v38
v_and_b32 v69, 0xffff0000, v38
v_lshlrev_b32 v70, 16, v39
Zvaa 236,44,8
D 7,40,64,1024
v_and_b32 v71, 0xffff0000, v39
v_lshlrev_b32 v72, 16, v40
v_and_b32 v73, 0xffff0000, v40
v_lshlrev_b32 v74, 16, v41
v_and_b32 v75, 0xffff0000, v41
Mvaa 236,140,12
v_lshlrev_b32 v76, 16, v42
v_and_b32 v77, 0xffff0000, v42
v_lshlrev_b32 v78, 16, v43
v_and_b32 v79, 0xffff0000, v43
A 80,56,57
Zvaa 232,136,12
A 81,58,59
A 80,80,60
A 81,81,61
A 80,80,62
A 81,81,63
Mvaa 232,40,8
A 80,80,64
A 81,81,65
A 80,80,66
A 81,81,67
A 80,80,68
Zvaa 172,36,8
D 8,40,64,2048
A 81,81,69
A 80,80,70
A 81,81,71
A 80,80,72
A 81,81,73
Mvaa 172,132,12
A 80,80,74
A 81,81,75
A 80,80,76
A 81,81,77
A 80,80,78
A 81,81,79
Zvaa 168,128,12
A 80,80,81
s_nop 1
v_add_f32_dpp v80, v80, v80 quad_perm:[1,0,3,2] row_mask:0xf bank_mask:0xf
s_nop 1
v_add_f32_dpp v80, v80, v80 quad_perm:[2,3,0,1] row_mask:0xf bank_mask:0xf
s_nop 1
v_add_f32_dpp v80, v80, v80 row_half_mirror row_mask:0xf bank_mask:0xf
s_nop 1
v_add_f32_dpp v80, v80, v80 row_mirror row_mask:0xf bank_mask:0xf
Mvaa 168,32,8
W 4
v_mov_b32 v81, v80
s_nop 1
v_permlane16_swap_b32 v80, v81
A 80,80,81
v_mul_f32 v84, 0x3acccccd, v80
s_mov_b32 s86, 0xffff
Zvaa 176,32,16
s_mov_b32 s87, 0xffff
D 9,40,64,3072
v_sub_f32 v82, v56, v84
U 80,82,82
v_sub_f32 v83, v57, v84
v_fmac_f32 v80, v83, v83
Mvaa 176,128,20
v_sub_f32 v82, v58, v84
v_fmac_f32 v80, v82, v82
v_sub_f32 v83, v59, v84
v_fmac_f32 v80, v83, v83
v_sub_f32 v82, v60, v84
Zvaa 180,132,20
Ta 0,14,16384
v_fmac_f32 v80, v82, v82
v_sub_f32 v83, v61, v84
v_fmac_f32 v80, v83, v83
v_sub_f32 v82, v62, v84
v_fmac_f32 v80, v82, v82
Mvaa 180,36,16
Ta 2,14,17408
v_sub_f32 v83, v63, v84
v_fmac_f32 v80, v83, v83
v_sub_f32 v82, v64, v84
v_fmac_f32 v80, v82, v82
v_sub_f32 v83, v65, v84
Zvaa 240,40,16
global_load_dword v142, v[140:141], off
v_lshl_add_u64 v[140:141], v[140:141], 0, s[68:69]
Ta 4,14,24576
Ta 6,14,25600
v_fmac_f32 v80, v83, v83
v_sub_f32 v82, v66, v84
v_fmac_f32 v80, v82, v82
Mvaa 240,136,20
v_sub_f32 v83, v67, v84
v_fmac_f32 v80, v83, v83
v_sub_f32 v82, v68, v84
v_fmac_f32 v80, v82, v82
v_sub_f32 v83, v69, v84
v_fmac_f32 v80, v83, v83
Zvaa 244,140,20
v_sub_f32 v82, v70, v84
v_fmac_f32 v80, v82, v82
v_sub_f32 v83, v71, v84
v_fmac_f32 v80, v83, v83
v_sub_f32 v82, v72, v84
U 81,82,82
Mvaa 244,44,16
v_sub_f32 v83, v73, v84
v_fmac_f32 v81, v83, v83
v_sub_f32 v82, v74, v84
v_fmac_f32 v81, v82, v82
v_sub_f32 v83, v75, v84
v_fmac_f32 v81, v83, v83
Zaaa 208,48,16
v_sub_f32 v82, v76, v84
v_fmac_f32 v81, v82, v82
v_sub_f32 v83, v77, v84
v_fmac_f32 v81, v83, v83
v_sub_f32 v82, v78, v84
Maaa 208,144,20
v_fmac_f32 v81, v82, v82
v_sub_f32 v83, v79, v84
v_fmac_f32 v81, v83, v83
v_cndmask_b32_e64 v81, 0, v81, s[86:87]
A 80,80,81
Zaaa 212,148,20
Maaa 212,52,16
v_add_f32_dpp v80, v80, v80 quad_perm:[1,0,3,2] row_mask:0xf bank_mask:0xf
s_nop 1
v_add_f32_dpp v80, v80, v80 quad_perm:[2,3,0,1] row_mask:0xf bank_mask:0xf
s_nop 1
v_add_f32_dpp v80, v80, v80 row_half_mirror row_mask:0xf bank_mask:0xf
s_nop 1
v_add_f32_dpp v80, v80, v80 row_mirror row_mask:0xf bank_mask:0xf
v_mov_b32 v81, v80
s_nop 1
v_permlane16_swap_b32 v80, v81
Zaaa 80,56,16
A 80,80,81
v_mul_f32 v80, 0x3acccccd, v80
K 80,80,80
v_lshlrev_b32 v80, 16, v80
v_add_f32 v80, 0x3727c5ac, v80
K 80,80,80
Maaa 80,152,20
v_lshlrev_b32 v80, 16, v80
v_sqrt_f32 v81, v80
s_nop 0
v_rcp_f32 v82, v81
v_fma_f32 v83, -v81, v81, v80
Zaaa 84,156,20
v_mul_f32 v82, 0.5, v82
v_fma_f32 v81, v83, v82, v81
K 86,81,81
v_lshlrev_b32 v86, 16, v86
v_rcp_f32 v87, v86
Maaa 84,60,16
W 4
K 85,84,84
v_lshlrev_b32 v85, 16, v85
)"
R"(s_waitcnt vmcnt(17)
v_sub_f32 v56, v56, v85
v_sub_f32 v57, v57, v85
K 80,56,57
Zaaa 92,60,24
v_lshlrev_b32 v56, 16, v80
v_and_b32 v57, 0xffff0000, v80
U 80,56,87
U 81,57,87
K 32,80,81
v_lshlrev_b32 v80, 16, v32
Maaa 92,156,28
v_and_b32 v81, 0xffff0000, v32
v_lshlrev_b32 v82, 16, v44
v_and_b32 v83, 0xffff0000, v44
U 84,80,82
v_fmac_f32 v84, v81, v83
Zaaa 88,152,28
Ta 8,15,16384
v_sub_f32 v58, v58, v85
v_sub_f32 v59, v59, v85
K 80,58,59
v_lshlrev_b32 v58, 16, v80
v_and_b32 v59, 0xffff0000, v80
Maaa 88,56,24
Ta 10,15,17408
U 80,58,87
U 81,59,87
K 33,80,81
v_lshlrev_b32 v80, 16, v33
v_and_b32 v81, 0xffff0000, v33
Zaaa 220,52,24
Ta 12,15,24576
Ta 14,15,25600
v_lshlrev_b32 v82, 16, v45
v_and_b32 v83, 0xffff0000, v45
v_fmac_f32 v84, v80, v82
v_fmac_f32 v84, v81, v83
Maaa 220,148,28
v_sub_f32 v60, v60, v85
v_sub_f32 v61, v61, v85
K 80,60,61
v_lshlrev_b32 v60, 16, v80
v_and_b32 v61, 0xffff0000, v80
U 80,60,87
Zaaa 216,144,28
U 81,61,87
K 34,80,81
v_lshlrev_b32 v80, 16, v34
v_and_b32 v81, 0xffff0000, v34
v_lshlrev_b32 v82, 16, v46
v_and_b32 v83, 0xffff0000, v46
Maaa 216,48,24
v_fmac_f32 v84, v80, v82
v_fmac_f32 v84, v81, v83
v_sub_f32 v62, v62, v85
v_sub_f32 v63, v63, v85
K 80,62,63
Zvaa 252,44,24
v_lshlrev_b32 v62, 16, v80
v_and_b32 v63, 0xffff0000, v80
U 80,62,87
U 81,63,87
K 35,80,81
Mvaa 252,140,28
v_lshlrev_b32 v80, 16, v35
v_and_b32 v81, 0xffff0000, v35
v_lshlrev_b32 v82, 16, v47
v_and_b32 v83, 0xffff0000, v47
v_fmac_f32 v84, v80, v82
Zvaa 248,136,28
v_fmac_f32 v84, v81, v83
v_sub_f32 v64, v64, v85
v_sub_f32 v65, v65, v85
K 80,64,65
v_lshlrev_b32 v64, 16, v80
Mvaa 248,40,24
v_and_b32 v65, 0xffff0000, v80
U 80,64,87
U 81,65,87
K 36,80,81
v_lshlrev_b32 v80, 16, v36
Zvaa 188,36,24
v_and_b32 v81, 0xffff0000, v36
v_lshlrev_b32 v82, 16, v48
v_and_b32 v83, 0xffff0000, v48
v_fmac_f32 v84, v80, v82
v_fmac_f32 v84, v81, v83
Mvaa 188,132,28
v_sub_f32 v66, v66, v85
v_sub_f32 v67, v67, v85
K 80,66,67
v_lshlrev_b32 v66, 16, v80
v_and_b32 v67, 0xffff0000, v80
U 80,66,87
Zvaa 184,128,28
U 81,67,87
K 37,80,81
v_lshlrev_b32 v80, 16, v37
v_and_b32 v81, 0xffff0000, v37
v_lshlrev_b32 v82, 16, v49
Mvaa 184,32,24
W 4
v_and_b32 v83, 0xffff0000, v49
v_fmac_f32 v84, v80, v82
v_fmac_f32 v84, v81, v83
v_sub_f32 v68, v68, v85
v_sub_f32 v69, v69, v85
Zvaa 192,32,0
K 80,68,69
v_lshlrev_b32 v68, 16, v80
v_and_b32 v69, 0xffff0000, v80
U 80,68,87
U 81,69,87
Mvaa 192,128,4
K 38,80,81
v_lshlrev_b32 v80, 16, v38
v_and_b32 v81, 0xffff0000, v38
v_lshlrev_b32 v82, 16, v50
v_and_b32 v83, 0xffff0000, v50
Zvaa 196,132,4
Ta 16,24,16384
v_fmac_f32 v84, v80, v82
v_fmac_f32 v84, v81, v83
v_sub_f32 v70, v70, v85
v_sub_f32 v71, v71, v85
K 80,70,71
Mvaa 196,36,0
Ta 18,24,17408
v_lshlrev_b32 v70, 16, v80
v_and_b32 v71, 0xffff0000, v80
U 80,70,87
U 81,71,87
K 39,80,81
Zaaa 160,40,0
Ta 20,24,24576
Ta 22,24,25600
v_lshlrev_b32 v80, 16, v39
v_and_b32 v81, 0xffff0000, v39
v_lshlrev_b32 v82, 16, v51
v_and_b32 v83, 0xffff0000, v51
Maaa 160,136,4
v_fmac_f32 v84, v80, v82
v_fmac_f32 v84, v81, v83
v_sub_f32 v72, v72, v85
v_sub_f32 v73, v73, v85
K 80,72,73
v_lshlrev_b32 v72, 16, v80
Zaaa 164,140,4
v_and_b32 v73, 0xffff0000, v80
U 80,72,87
U 81,73,87
K 40,80,81
v_lshlrev_b32 v80, 16, v40
v_and_b32 v81, 0xffff0000, v40
Maaa 164,44,0
v_lshlrev_b32 v82, 16, v52
v_and_b32 v83, 0xffff0000, v52
v_fmac_f32 v84, v80, v82
v_fmac_f32 v84, v81, v83
v_sub_f32 v74, v74, v85
Zaaa 224,48,0
v_sub_f32 v75, v75, v85
K 80,74,75
v_lshlrev_b32 v74, 16, v80
v_and_b32 v75, 0xffff0000, v80
U 80,74,87
Maaa 224,144,4
U 81,75,87
K 41,80,81
v_lshlrev_b32 v80, 16, v41
v_and_b32 v81, 0xffff0000, v41
v_lshlrev_b32 v82, 16, v53
Zaaa 228,148,4
v_and_b32 v83, 0xffff0000, v53
v_fmac_f32 v84, v80, v82
v_fmac_f32 v84, v81, v83
v_sub_f32 v76, v76, v85
v_sub_f32 v77, v77, v85
Maaa 228,52,0
K 80,76,77
v_lshlrev_b32 v76, 16, v80
v_and_b32 v77, 0xffff0000, v80
U 80,76,87
U 81,77,87
Zaaa 96,56,0
K 42,80,81
v_lshlrev_b32 v80, 16, v42
v_and_b32 v81, 0xffff0000, v42
v_lshlrev_b32 v82, 16, v54
v_and_b32 v83, 0xffff0000, v54
Maaa 96,152,4
v_fmac_f32 v84, v80, v82
v_fmac_f32 v84, v81, v83
v_sub_f32 v78, v78, v85
v_sub_f32 v79, v79, v85
K 80,78,79
v_lshlrev_b32 v78, 16, v80
Zaaa 100,156,4
v_and_b32 v79, 0xffff0000, v80
U 80,78,87
U 81,79,87
K 43,80,81
v_lshlrev_b32 v80, 16, v43
Maaa 100,60,0
W 4
v_and_b32 v81, 0xffff0000, v43
v_lshlrev_b32 v82, 16, v55
v_and_b32 v83, 0xffff0000, v55
v_fmac_f32 v84, v80, v82
v_fmac_f32 v84, v81, v83
Zaaa 108,60,8
Ta 24,25,16384
Ta 26,25,17408
v_lshrrev_b32 v82, 9, v1
s_lshl_b32 s80, s14, 1
v_add_u32 v82, s80, v82
v_mul_u32_u24 v83, 1552, v82
v_add_u32 v83, v83, v88
Maaa 108,156,12
v_add_u32 v83, 131072, v83
ds_write_b128 v83, v[32:35] offset:0
ds_write_b128 v83, v[36:39] offset:512
ds_write_b128 v83, v[40:43] offset:1024
v_add_f32_dpp v84, v84, v84 quad_perm:[1,0,3,2] row_mask:0xf bank_mask:0xf
Zaaa 104,152,12
Ta 28,25,24576
Ta 30,25,25600
Maaa 104,56,8
v_add_f32_dpp v84, v84, v84 quad_perm:[2,3,0,1] row_mask:0xf bank_mask:0xf
s_nop 1
v_add_f32_dpp v84, v84, v84 row_half_mirror row_mask:0xf bank_mask:0xf
s_nop 1
v_add_f32_dpp v84, v84, v84 row_mirror row_mask:0xf bank_mask:0xf
v_mov_b32 v81, v84
s_nop 1
v_permlane16_swap_b32 v84, v81
A 84,84,81
Zaaa 236,52,8
K 80,84,84
v_lshlrev_b32 v80, 16, v80
s_lshl_b32 s80, s15, 3
v_add_u32 v82, s80, v82
v_sub_u32 v83, 2047, v82
v_ashrrev_i32 v81, 31, v80
v_or_b32 v81, 0x80000000, v81
Maaa 236,148,12
v_xor_b32 v80, v80, v81
v_and_b32 v80, 0xffff0000, v80
v_or_b32 v80, v80, v83
v_and_b32 v82, 7, v82
v_lshlrev_b32 v82, 2, v82
v_add_u32 v82, 143488, v82
Zaaa 232,144,12
ds_write_b32 v82, v80
Maaa 232,48,8
Zaaa 172,44,8
Maaa 172,140,12
Zaaa 168,136,12
s_waitcnt vmcnt(1) lgkmcnt(0)
s_barrier
s_add_u32 s64, s64, 128
Maaa 168,40,8
v_bfe_u32 v80, v1, 4, 3
Zvaa 204,36,8
v_mul_u32_u24 v81, 1552, v80
Mvaa 204,132,12
s_and_b32 s64, s64, 1023
v_lshrrev_b32 v82, 7, v1
Zvaa 200,128,12
v_lshl_add_u32 v81, v82, 4, v81
Mvaa 200,32,8
s_lshl_b32 s80, s14, 7
Zvaa 208,32,16
s_lshl_b32 s65, s64, 12
s_add_u32 s80, s80, 131072
Mvaa 208,128,20
v_add_u32 v81, s80, v81
Zvaa 212,132,20
s_add_u32 m0, s62, 0
ds_read_b128 v[32:35], v81
Ta 0,10,49152
Mvaa 212,36,16
Ta 2,10,50176
Rv 36,81,512
Zaaa 176,40,16
s_and_b32 s80, s14, 2
Ta 4,10,57344
Ta 6,10,58368
Maaa 176,136,20
s_lshl_b32 s80, s80, 7
D 16,44,65,0
Zaaa 180,140,20
v_subrev_u32 v81, s80, v81
Maaa 180,44,16
Rv 40,81,1024
Zaaa 240,48,16
v_lshlrev_b32 v80, 2, v80
Maaa 240,144,20
D 17,44,65,1024
v_add_u32 v80, 143488, v80
Zaaa 244,148,20
ds_read_b32 v84, v80
Maaa 244,52,16
v_mov_b32 v85, s13
Zaaa 112,56,16
v_lshrrev_b32 v83, 1, v1
Maaa 112,152,20
s_mul_i32 s82, s15, 10240
D 18,44,65,2048
Zaaa 116,156,20
s_add_u32 s82, s82, 131072
Maaa 116,60,16
s_lshl_b32 s80, s14, 10
Zaaa 124,60,24
s_add_u32 s83, s82, s80
Ra 60,26,47104
Maaa 124,156,28
D 19,44,65,3072
Ra 156,27,47104
Zaaa 120,152,28
s_add_u32 m0, s62, 4096
Ra 152,27,45056
Ta 8,11,49152
Maaa 120,56,24
Ta 10,11,50176
Ra 56,26,45056
Zaaa 252,52,24
Ta 12,11,57344
Ta 14,11,58368
Maaa 252,148,28
W 8
buffer_store_dwordx4 v[32:35], v1, s[8:11], s83 offen sc1
s_add_u32 s83, s83, 4096
Zaaa 248,144,28
Ra 52,26,43008
Ra 148,27,43008
Maaa 248,48,24
Ra 144,27,40960
Ra 48,26,40960
Zaaa 188,44,24
Ra 44,26,38912
Maaa 188,140,28
D 20,44,65,0
Ra 140,27,38912
Zaaa 184,136,28
Ra 136,27,36864
Maaa 184,40,24
Ra 40,26,36864
Zvaa 220,36,24
Ra 36,26,34816
Mvaa 220,132,28
buffer_store_dwordx4 v[36:39], v1, s[8:11], s83 offen sc1
Ra 132,27,34816
Zvaa 216,128,28
s_and_b32 s80, s80, 1024
s_add_u32 s83, s82, s80
s_add_u32 s83, s83, 8192
Mvaa 216,32,24
W 12
Ra 128,27,32768
Ra 32,26,32768
Maaa 68,60,0
Maaa 68,156,4
D 21,44,65,1024
Maaa 64,152,4
Ta 16,12,49152
Ta 18,12,50176
Maaa 64,56,0
W 10
Ta 20,12,57344
Ta 22,12,58368
Maaa 196,52,0
Maaa 196,148,4
D 22,44,65,2048
Maaa 192,144,4
Maaa 192,48,0
W 8
Mvaa 228,44,0
Mvaa 228,140,4
buffer_store_dwordx4 v[40:43], v1, s[8:11], s83 offen sc1
Mvaa 224,136,4
Mvaa 224,40,0
W 4
Mvaa 164,36,0
Mvaa 164,132,4
D 23,44,65,3072
Mvaa 160,128,4
s_add_u32 m0, s61, 0
Mvaa 160,32,0
Mvaa 168,32,8
Mvaa 168,128,12
D 2,40,64,0
Mvaa 172,132,12
Ta 24,13,49152
Ta 26,13,50176
Mvaa 172,36,8
Ta 28,13,57344
Ta 30,13,58368
Mvaa 232,40,8
)"
R"(.Lh_0_1_1%=:
s_cbranch_execz .Lhr_9%=
Mvaa 232,136,12
D 3,40,64,1024
Mvaa 236,140,12
Mvaa 236,44,8
Maaa 200,48,8
Maaa 200,144,12
D 4,40,64,2048
Maaa 204,148,12
Maaa 204,52,8
Maaa 72,56,8
Maaa 72,152,12
D 5,40,64,3072
Maaa 76,156,12
s_add_u32 m0, s61, 4096
Maaa 76,60,8
W 4
Maaa 84,60,16
Maaa 84,156,20
D 6,40,64,0
Maaa 80,152,20
Ta 0,14,49152
Ta 2,14,50176
Maaa 80,56,16
Ta 4,14,57344
Ta 6,14,58368
Maaa 212,52,16
Maaa 212,148,20
D 7,40,64,1024
Maaa 208,144,20
Maaa 208,48,16
Mvaa 244,44,16
Mvaa 244,140,20
D 8,40,64,2048
Mvaa 240,136,20
Mvaa 240,40,16
Mvaa 180,36,16
Mvaa 180,132,20
D 9,40,64,3072
Mvaa 176,128,20
Mvaa 176,32,16
W 4
Mvaa 184,32,24
Mvaa 184,128,28
global_load_dword v142, v[140:141], off
v_lshl_add_u64 v[140:141], v[140:141], 0, s[68:69]
v_bfe_u32 v147, v1, 5, 3
Mvaa 188,132,28
v_lshlrev_b32 v147, 13, v147
v_and_b32 v148, 16, v1
Mvaa 188,36,24
Ta 8,15,49152
v_lshl_add_u32 v147, v148, 3, v147
Mvaa 248,40,24
s_lshl_b32 s92, s57, 2
s_add_u32 s92, s92, s14
s_lshl_b32 s92, s92, 16
Mvaa 248,136,28
buffer_load_dword v142, v147, s[48:51], s92 offen
Ta 10,15,50176
Mvaa 252,140,28
Ta 12,15,57344
Ta 14,15,58368
Mvaa 252,44,24
Maaa 216,48,24
Maaa 216,144,28
Maaa 220,148,28
Maaa 220,52,24
Maaa 88,56,24
Maaa 88,152,28
Maaa 92,156,28
Maaa 92,60,24
W 4
Maaa 100,60,0
Maaa 100,156,4
Maaa 96,152,4
Ta 16,24,49152
Ta 18,24,50176
Maaa 96,56,0
Ta 20,24,57344
Ta 22,24,58368
Maaa 228,52,0
Maaa 228,148,4
Maaa 224,144,4
Maaa 224,48,0
Maaa 164,44,0
Maaa 164,140,4
Maaa 160,136,4
Maaa 160,40,0
Mvaa 196,36,0
Mvaa 196,132,4
Mvaa 192,128,4
Mvaa 192,32,0
W 4
Mvaa 200,32,8
Ta 24,25,49152
Ta 26,25,50176
Mvaa 200,128,12
Mvaa 204,132,12
Ta 28,25,57344
Ta 30,25,58368
Mvaa 204,36,8
Maaa 168,40,8
Maaa 168,136,12
Maaa 172,140,12
Maaa 172,44,8
Maaa 232,48,8
Maaa 232,144,12
Maaa 236,148,12
s_waitcnt vmcnt(2) lgkmcnt(0)
s_barrier
s_cmp_eq_u32 s14, 0
s_cbranch_scc0 .Lpubr_19%=
s_lshl_b32 s80, s15, 6
s_add_u32 s80, s80, 40960
s_mov_b64 exec, 0xff
buffer_store_dwordx2 v[84:85], v83, s[8:11], s80 offen sc1
s_mov_b64 exec, -1
.Lpubr_19%=:
Maaa 236,52,8
s_add_u32 s64, s64, 128
Maaa 104,56,8
Maaa 104,152,12
s_and_b32 s64, s64, 1023
Maaa 108,156,12
Maaa 108,60,8
Maaa 116,60,16
s_lshl_b32 s65, s64, 12
Maaa 116,156,20
Maaa 112,152,20
Ta 0,10,16384
Ta 2,10,17408
Maaa 112,56,16
s_add_u32 m0, s62, 32768
Ta 4,10,24576
Ta 6,10,25600
Maaa 244,52,16
Maaa 244,148,20
Maaa 240,144,20
D 16,44,65,0
Maaa 240,48,16
Maaa 180,44,16
Maaa 180,140,20
Maaa 176,136,20
D 17,44,65,1024
Maaa 176,40,16
Mvaa 212,36,16
Mvaa 212,132,20
Mvaa 208,128,20
D 18,44,65,2048
Mvaa 208,32,16
Mvaa 216,32,24
Ra 32,26,0
Mvaa 216,128,28
Ra 128,27,0
Mvaa 220,132,28
D 19,44,65,3072
Ra 132,27,2048
Mvaa 220,36,24
s_add_u32 m0, s62, 36864
Ta 8,11,16384
Ta 10,11,17408
Maaa 184,40,24
Ra 36,26,2048
Ta 12,11,24576
Maaa 184,136,28
Ta 14,11,25600
Ra 40,26,4096
Maaa 188,140,28
D 20,44,65,0
Ra 136,27,4096
Maaa 188,44,24
Ra 140,27,6144
Ra 44,26,6144
Maaa 248,48,24
Ra 48,26,8192
Maaa 248,144,28
Ra 144,27,8192
Maaa 252,148,28
D 21,44,65,1024
Ra 148,27,10240
Maaa 252,52,24
Ra 52,26,10240
Maaa 120,56,24
Ra 56,26,12288
Maaa 120,152,28
Ra 152,27,12288
Maaa 124,156,28
D 22,44,65,2048
Ra 156,27,14336
Maaa 124,60,24
W 13
Ra 60,26,14336
Mvaa 160,32,0
Mvaa 160,128,4
Mvaa 164,132,4
D 23,44,65,3072
Ta 16,12,16384
Mvaa 164,36,0
W 9
s_add_u32 m0, s61, 32768
Ta 18,12,17408
Mvaa 224,40,0
Ta 20,12,24576
Ta 22,12,25600
Mvaa 224,136,4
Mvaa 228,140,4
D 2,40,64,0
Mvaa 228,44,0
W 8
Maaa 192,48,0
Maaa 192,144,4
Maaa 196,148,4
D 3,40,64,1024
Maaa 196,52,0
W 4
Maaa 64,56,0
Maaa 64,152,4
Maaa 68,156,4
D 4,40,64,2048
Maaa 68,60,0
Maaa 76,60,8
Maaa 76,156,12
Maaa 72,152,12
D 5,40,64,3072
Ta 24,13,16384
Maaa 72,56,8
s_add_u32 m0, s61, 36864
Ta 26,13,17408
Ta 28,13,24576
Maaa 204,52,8
Ta 30,13,25600
Maaa 204,148,12
Maaa 200,144,12
D 6,40,64,0
Maaa 200,48,8
Mvaa 236,44,8
Mvaa 236,140,12
Mvaa 232,136,12
D 7,40,64,1024
Mvaa 232,40,8
Mvaa 172,36,8
Mvaa 172,132,12
Mvaa 168,128,12
D 8,40,64,2048
Mvaa 168,32,8
W 4
Mvaa 176,32,16
Mvaa 176,128,20
Mvaa 180,132,20
D 9,40,64,3072
Ta 0,14,16384
Mvaa 180,36,16
Ta 2,14,17408
Ta 4,14,24576
Mvaa 240,40,16
Ta 6,14,25600
Mvaa 240,136,20
Mvaa 244,140,20
Mvaa 244,44,16
Maaa 208,48,16
Maaa 208,144,20
Maaa 212,148,20
Maaa 212,52,16
Maaa 80,56,16
Maaa 80,152,20
Maaa 84,156,20
Maaa 84,60,16
W 4
Maaa 92,60,24
Maaa 92,156,28
Maaa 88,152,28
Ta 8,15,16384
Ta 10,15,17408
Maaa 88,56,24
Ta 12,15,24576
Ta 14,15,25600
Maaa 220,52,24
Maaa 220,148,28
Maaa 216,144,28
Maaa 216,48,24
Mvaa 252,44,24
Mvaa 252,140,28
Mvaa 248,136,28
Mvaa 248,40,24
Mvaa 188,36,24
Mvaa 188,132,28
Mvaa 184,128,28
Mvaa 184,32,24
W 4
Mvaa 192,32,0
Mvaa 192,128,4
Mvaa 196,132,4
Ta 16,24,16384
Ta 18,24,17408
Mvaa 196,36,0
Ta 20,24,24576
Ta 22,24,25600
Maaa 160,40,0
Maaa 160,136,4
Maaa 164,140,4
Maaa 164,44,0
Maaa 224,48,0
Maaa 224,144,4
Maaa 228,148,4
Maaa 228,52,0
Maaa 96,56,0
Maaa 96,152,4
Maaa 100,156,4
Maaa 100,60,0
W 4
Maaa 108,60,8
Ta 24,25,16384
Ta 26,25,17408
Maaa 108,156,12
Maaa 104,152,12
Ta 28,25,24576
Ta 30,25,25600
Maaa 104,56,8
Maaa 236,52,8
Maaa 236,148,12
Maaa 232,144,12
Maaa 232,48,8
Maaa 172,44,8
Maaa 172,140,12
Maaa 168,136,12
s_waitcnt vmcnt(0) lgkmcnt(0)
s_barrier
s_add_u32 s64, s64, 128
Maaa 168,40,8
Mvaa 204,36,8
Mvaa 204,132,12
s_and_b32 s64, s64, 1023
Mvaa 200,128,12
Mvaa 200,32,8
Mvaa 208,32,16
s_lshl_b32 s65, s64, 12
Mvaa 208,128,20
Mvaa 212,132,20
s_add_u32 m0, s62, 0
Ta 0,10,49152
Ta 2,10,50176
Mvaa 212,36,16
Ta 4,10,57344
Ta 6,10,58368
Maaa 176,40,16
Maaa 176,136,20
D 16,44,65,0
Maaa 180,140,20
Maaa 180,44,16
Maaa 240,48,16
Maaa 240,144,20
D 17,44,65,1024
Maaa 244,148,20
Maaa 244,52,16
Maaa 112,56,16
Maaa 112,152,20
D 18,44,65,2048
Maaa 116,156,20
Maaa 116,60,16
Maaa 124,60,24
Ra 60,26,47104
Maaa 124,156,28
D 19,44,65,3072
Ra 156,27,47104
Maaa 120,152,28
s_add_u32 m0, s62, 4096
Ra 152,27,45056
Ta 8,11,49152
Maaa 120,56,24
Ta 10,11,50176
Ra 56,26,45056
Maaa 252,52,24
Ta 12,11,57344
Ta 14,11,58368
Maaa 252,148,28
D 20,44,65,0
Ra 52,26,43008
Maaa 248,144,28
Ra 148,27,43008
Ra 144,27,40960
Maaa 248,48,24
Ra 48,26,40960
Maaa 188,44,24
Ra 44,26,38912
Maaa 188,140,28
D 21,44,65,1024
Ra 140,27,38912
Maaa 184,136,28
Ra 136,27,36864
Maaa 184,40,24
Ra 40,26,36864
Mvaa 220,36,24
Ra 36,26,34816
Mvaa 220,132,28
D 22,44,65,2048
Ra 132,27,34816
Mvaa 216,128,28
Ra 128,27,32768
)"
R"(Mvaa 216,32,24
W 13
Ra 32,26,32768
Maaa 68,60,0
Maaa 68,156,4
D 23,44,65,3072
Maaa 64,152,4
s_add_u32 m0, s61, 0
Ta 16,12,49152
Ta 18,12,50176
Maaa 64,56,0
W 10
Ta 20,12,57344
Ta 22,12,58368
Maaa 196,52,0
Maaa 196,148,4
D 2,40,64,0
Maaa 192,144,4
Maaa 192,48,0
W 8
Mvaa 228,44,0
Mvaa 228,140,4
D 3,40,64,1024
Mvaa 224,136,4
Mvaa 224,40,0
W 4
Mvaa 164,36,0
Mvaa 164,132,4
D 4,40,64,2048
Mvaa 160,128,4
Mvaa 160,32,0
Mvaa 168,32,8
Mvaa 168,128,12
D 5,40,64,3072
Mvaa 172,132,12
s_add_u32 m0, s61, 4096
Ta 24,13,49152
Ta 26,13,50176
Mvaa 172,36,8
Ta 28,13,57344
Ta 30,13,58368
Mvaa 232,40,8
Mvaa 232,136,12
D 6,40,64,0
Mvaa 236,140,12
Mvaa 236,44,8
Maaa 200,48,8
Maaa 200,144,12
D 7,40,64,1024
Maaa 204,148,12
Maaa 204,52,8
Maaa 72,56,8
Maaa 72,152,12
D 8,40,64,2048
Maaa 76,156,12
Maaa 76,60,8
W 4
Maaa 84,60,16
Maaa 84,156,20
D 9,40,64,3072
Maaa 80,152,20
Ta 0,14,49152
Ta 2,14,50176
Maaa 80,56,16
Ta 4,14,57344
Ta 6,14,58368
Maaa 212,52,16
Maaa 212,148,20
global_load_dword v142, v[140:141], off
v_lshl_add_u64 v[140:141], v[140:141], 0, s[68:69]
Maaa 208,144,20
Maaa 208,48,16
Mvaa 244,44,16
Mvaa 244,140,20
Mvaa 240,136,20
Mvaa 240,40,16
Mvaa 180,36,16
Mvaa 180,132,20
Mvaa 176,128,20
Mvaa 176,32,16
W 4
Mvaa 184,32,24
Mvaa 184,128,28
Mvaa 188,132,28
Ta 8,15,49152
Ta 10,15,50176
Mvaa 188,36,24
Ta 12,15,57344
Ta 14,15,58368
Mvaa 248,40,24
Mvaa 248,136,28
Mvaa 252,140,28
Mvaa 252,44,24
Maaa 216,48,24
Maaa 216,144,28
Maaa 220,148,28
Maaa 220,52,24
Maaa 88,56,24
Maaa 88,152,28
Maaa 92,156,28
Maaa 92,60,24
W 4
Maaa 100,60,0
Maaa 100,156,4
Maaa 96,152,4
Ta 16,24,49152
Ta 18,24,50176
Maaa 96,56,0
Ta 20,24,57344
Ta 22,24,58368
Maaa 228,52,0
Maaa 228,148,4
Maaa 224,144,4
Maaa 224,48,0
Maaa 164,44,0
Maaa 164,140,4
Maaa 160,136,4
Maaa 160,40,0
Mvaa 196,36,0
Mvaa 196,132,4
Mvaa 192,128,4
Mvaa 192,32,0
W 4
Mvaa 200,32,8
Ta 24,25,49152
Ta 26,25,50176
Mvaa 200,128,12
Mvaa 204,132,12
Ta 28,25,57344
Ta 30,25,58368
Mvaa 204,36,8
Maaa 168,40,8
Maaa 168,136,12
.Lh_0_2_1%=:
s_cbranch_execz .Lhr_9%=
Maaa 172,140,12
Maaa 172,44,8
Maaa 232,48,8
Maaa 232,144,12
Maaa 236,148,12
s_waitcnt vmcnt(1) lgkmcnt(0)
s_barrier
s_add_u32 s64, s64, 128
Maaa 236,52,8
Maaa 104,56,8
Maaa 104,152,12
s_and_b32 s64, s64, 1023
Maaa 108,156,12
Maaa 108,60,8
Maaa 116,60,16
s_lshl_b32 s65, s64, 12
Maaa 116,156,20
Maaa 112,152,20
Ta 0,10,16384
Ta 2,10,17408
Maaa 112,56,16
s_add_u32 m0, s62, 32768
Ta 4,10,24576
Ta 6,10,25600
Maaa 244,52,16
Maaa 244,148,20
Maaa 240,144,20
D 16,44,65,0
Maaa 240,48,16
Maaa 180,44,16
Maaa 180,140,20
Maaa 176,136,20
D 17,44,65,1024
Maaa 176,40,16
Mvaa 212,36,16
Mvaa 212,132,20
Mvaa 208,128,20
D 18,44,65,2048
Mvaa 208,32,16
Mvaa 216,32,24
Ra 32,26,0
Mvaa 216,128,28
Ra 128,27,0
Mvaa 220,132,28
D 19,44,65,3072
Ra 132,27,2048
Mvaa 220,36,24
s_add_u32 m0, s62, 36864
Ta 8,11,16384
Ta 10,11,17408
Maaa 184,40,24
Ra 36,26,2048
Ta 12,11,24576
Maaa 184,136,28
Ta 14,11,25600
Ra 40,26,4096
Maaa 188,140,28
D 20,44,65,0
Ra 136,27,4096
Maaa 188,44,24
Ra 140,27,6144
Ra 44,26,6144
Maaa 248,48,24
Ra 48,26,8192
Maaa 248,144,28
Ra 144,27,8192
Maaa 252,148,28
D 21,44,65,1024
Ra 148,27,10240
Maaa 252,52,24
Ra 52,26,10240
Maaa 120,56,24
Ra 56,26,12288
Maaa 120,152,28
Ra 152,27,12288
Maaa 124,156,28
D 22,44,65,2048
Ra 156,27,14336
Maaa 124,60,24
W 13
Ra 60,26,14336
Mvaa 160,32,0
Mvaa 160,128,4
Mvaa 164,132,4
D 23,44,65,3072
Ta 16,12,16384
Mvaa 164,36,0
W 9
s_add_u32 m0, s61, 32768
Ta 18,12,17408
Mvaa 224,40,0
Ta 20,12,24576
Ta 22,12,25600
Mvaa 224,136,4
Mvaa 228,140,4
D 2,40,64,0
Mvaa 228,44,0
W 8
Maaa 192,48,0
Maaa 192,144,4
Maaa 196,148,4
D 3,40,64,1024
Maaa 196,52,0
W 4
Maaa 64,56,0
Maaa 64,152,4
Maaa 68,156,4
D 4,40,64,2048
Maaa 68,60,0
Maaa 76,60,8
Maaa 76,156,12
Maaa 72,152,12
D 5,40,64,3072
Ta 24,13,16384
Maaa 72,56,8
s_add_u32 m0, s61, 36864
Ta 26,13,17408
Ta 28,13,24576
Maaa 204,52,8
Ta 30,13,25600
Maaa 204,148,12
Maaa 200,144,12
D 6,40,64,0
Maaa 200,48,8
Mvaa 236,44,8
Mvaa 236,140,12
Mvaa 232,136,12
D 7,40,64,1024
Mvaa 232,40,8
Mvaa 172,36,8
Mvaa 172,132,12
Mvaa 168,128,12
D 8,40,64,2048
Mvaa 168,32,8
W 4
Mvaa 176,32,16
Mvaa 176,128,20
Mvaa 180,132,20
D 9,40,64,3072
Ta 0,14,16384
Mvaa 180,36,16
s_add_u32 s92, s92, 2097152
Ta 2,14,17408
Ta 4,14,24576
Mvaa 240,40,16
Ta 6,14,25600
Mvaa 240,136,20
Mvaa 244,140,20
buffer_load_dword v142, v147, s[48:51], s92 offen
Mvaa 244,44,16
s_add_u32 s92, s92, 2097152
Maaa 208,48,16
Maaa 208,144,20
Maaa 212,148,20
buffer_load_dword v142, v147, s[48:51], s92 offen
Maaa 212,52,16
Maaa 80,56,16
Maaa 80,152,20
Maaa 84,156,20
Maaa 84,60,16
W 4
Maaa 92,60,24
Maaa 92,156,28
Maaa 88,152,28
Ta 8,15,16384
Ta 10,15,17408
Maaa 88,56,24
Ta 12,15,24576
Ta 14,15,25600
Maaa 220,52,24
Maaa 220,148,28
Maaa 216,144,28
Maaa 216,48,24
Mvaa 252,44,24
Mvaa 252,140,28
Mvaa 248,136,28
Mvaa 248,40,24
Mvaa 188,36,24
Mvaa 188,132,28
Mvaa 184,128,28
Mvaa 184,32,24
W 4
Mvaa 192,32,0
Mvaa 192,128,4
Mvaa 196,132,4
Ta 16,24,16384
Ta 18,24,17408
Mvaa 196,36,0
Ta 20,24,24576
Ta 22,24,25600
Maaa 160,40,0
Maaa 160,136,4
Maaa 164,140,4
Maaa 164,44,0
Maaa 224,48,0
Maaa 224,144,4
Maaa 228,148,4
Maaa 228,52,0
Maaa 96,56,0
Maaa 96,152,4
Maaa 100,156,4
Maaa 100,60,0
W 4
Maaa 108,60,8
Ta 24,25,16384
Ta 26,25,17408
Maaa 108,156,12
Maaa 104,152,12
Ta 28,25,24576
Ta 30,25,25600
Maaa 104,56,8
Maaa 236,52,8
Maaa 236,148,12
Maaa 232,144,12
Maaa 232,48,8
Maaa 172,44,8
Maaa 172,140,12
Maaa 168,136,12
s_waitcnt vmcnt(2) lgkmcnt(0)
s_barrier
s_add_u32 s64, s64, 128
Maaa 168,40,8
Mvaa 204,36,8
Mvaa 204,132,12
s_and_b32 s64, s64, 1023
Mvaa 200,128,12
Mvaa 200,32,8
Mvaa 208,32,16
s_lshl_b32 s65, s64, 12
Mvaa 208,128,20
Mvaa 212,132,20
Ta 0,10,49152
Ta 2,10,50176
Mvaa 212,36,16
s_add_u32 m0, s62, 0
Ta 4,10,57344
Ta 6,10,58368
Maaa 176,40,16
Maaa 176,136,20
Maaa 180,140,20
D 16,44,65,0
Maaa 180,44,16
Maaa 240,48,16
Maaa 240,144,20
Maaa 244,148,20
D 17,44,65,1024
Maaa 244,52,16
Maaa 112,56,16
Maaa 112,152,20
Maaa 116,156,20
D 18,44,65,2048
Maaa 116,60,16
Maaa 124,60,24
Ra 60,26,47104
Maaa 124,156,28
Ra 156,27,47104
Maaa 120,152,28
D 19,44,65,3072
Ra 152,27,45056
Maaa 120,56,24
s_add_u32 m0, s62, 4096
Ta 8,11,49152
Ta 10,11,50176
Maaa 252,52,24
Ra 56,26,45056
)"
R"(Ta 12,11,57344
Maaa 252,148,28
Ta 14,11,58368
Ra 52,26,43008
Maaa 248,144,28
D 20,44,65,0
v_lshrrev_b32 v28, 1, v1
Maaa 248,48,24
s_lshl_b32 s80, s60, 9
Ra 148,27,43008
Ra 144,27,40960
Maaa 188,44,24
s_add_u32 s80, s80, 40960
Ra 48,26,40960
Ra 44,26,38912
Maaa 188,140,28
Ra 140,27,38912
Maaa 184,136,28
D 21,44,65,1024
Ra 136,27,36864
Maaa 184,40,24
Ra 40,26,36864
Mvaa 220,36,24
Ra 36,26,34816
Mvaa 220,132,28
Ra 132,27,34816
Mvaa 216,128,28
buffer_load_dwordx2 v[28:29], v28, s[8:11], s80 offen sc1
Ra 128,27,32768
Mvaa 216,32,24
W 13
Ra 32,26,32768
Maaa 68,60,0
Maaa 68,156,4
Maaa 64,152,4
D 22,44,65,2048
Ta 16,12,49152
Maaa 64,56,0
W 9
Ta 18,12,50176
Ta 20,12,57344
Maaa 196,52,0
Ta 22,12,58368
Maaa 196,148,4
Maaa 192,144,4
D 23,44,65,3072
Maaa 192,48,0
W 8
s_add_u32 m0, s61, 0
Mvaa 228,44,0
Mvaa 228,140,4
Mvaa 224,136,4
D 2,40,64,0
Mvaa 224,40,0
W 4
Mvaa 164,36,0
Mvaa 164,132,4
Mvaa 160,128,4
D 3,40,64,1024
Mvaa 160,32,0
Mvaa 168,32,8
Mvaa 168,128,12
Mvaa 172,132,12
D 4,40,64,2048
Ta 24,13,49152
Mvaa 172,36,8
Ta 26,13,50176
Ta 28,13,57344
Mvaa 232,40,8
Ta 30,13,58368
Mvaa 232,136,12
Mvaa 236,140,12
D 5,40,64,3072
Mvaa 236,44,8
s_add_u32 m0, s61, 4096
Maaa 200,48,8
Maaa 200,144,12
Maaa 204,148,12
D 6,40,64,0
Maaa 204,52,8
Maaa 72,56,8
Maaa 72,152,12
Maaa 76,156,12
D 7,40,64,1024
Maaa 76,60,8
W 4
Maaa 84,60,16
Maaa 84,156,20
Maaa 80,152,20
D 8,40,64,2048
Ta 0,14,49152
Maaa 80,56,16
Ta 2,14,50176
Ta 4,14,57344
Maaa 212,52,16
Ta 6,14,58368
Maaa 212,148,20
Maaa 208,144,20
D 9,40,64,3072
Maaa 208,48,16
Mvaa 244,44,16
Mvaa 244,140,20
Mvaa 240,136,20
s_waitcnt vmcnt(10)
v_cmp_eq_u32 vcc, s13, v29
s_cmp_eq_u64 vcc, -1
s_cbranch_scc0 .Lspin_20%=
.Lok_21%=:
global_load_dword v142, v146, s[6:7]
Mvaa 240,40,16
Mvaa 180,36,16
v_bfe_u32 v144, v1, 8, 1
Mvaa 180,132,20
v_mul_u32_u24 v144, 10240, v144
Mvaa 176,128,20
v_and_b32 v145, 0xf0, v1
Mvaa 176,32,16
W 4
v_lshl_add_u32 v144, v145, 3, v144
Mvaa 184,32,24
s_mul_i32 s91, s58, 20480
Mvaa 184,128,28
s_add_u32 s91, s91, s28
Mvaa 188,132,28
buffer_load_dword v142, v144, s[8:11], s91 offen offset:0 sc1
Ta 8,15,49152
Mvaa 188,36,24
Ta 10,15,50176
Ta 12,15,57344
Mvaa 248,40,24
Ta 14,15,58368
Mvaa 248,136,28
Mvaa 252,140,28
Mvaa 252,44,24
Maaa 216,48,24
Maaa 216,144,28
Maaa 220,148,28
Maaa 220,52,24
Maaa 88,56,24
Maaa 88,152,28
Maaa 92,156,28
Maaa 92,60,24
W 4
Maaa 100,60,0
Maaa 100,156,4
Maaa 96,152,4
Ta 16,24,49152
Ta 18,24,50176
Maaa 96,56,0
Ta 20,24,57344
Ta 22,24,58368
Maaa 228,52,0
Maaa 228,148,4
Maaa 224,144,4
Maaa 224,48,0
Maaa 164,44,0
Maaa 164,140,4
Maaa 160,136,4
Maaa 160,40,0
Mvaa 196,36,0
Mvaa 196,132,4
Mvaa 192,128,4
Mvaa 192,32,0
W 4
Mvaa 200,32,8
Ta 24,25,49152
Ta 26,25,50176
Mvaa 200,128,12
Mvaa 204,132,12
Ta 28,25,57344
Ta 30,25,58368
Mvaa 204,36,8
Maaa 168,40,8
Maaa 168,136,12
Maaa 172,140,12
Maaa 172,44,8
Maaa 232,48,8
Maaa 232,144,12
Maaa 236,148,12
s_waitcnt vmcnt(2) lgkmcnt(0)
s_barrier
s_cmp_eq_u32 s85, 107
s_cbranch_scc1 .Lrt_23%=
.Lrtr_24%=:
Maaa 236,52,8
s_add_u32 s64, s64, 128
Maaa 104,56,8
Maaa 104,152,12
s_and_b32 s64, s64, 1023
Maaa 108,156,12
Maaa 108,60,8
Maaa 116,60,16
s_lshl_b32 s65, s64, 12
Maaa 116,156,20
Maaa 112,152,20
Ta 0,10,16384
Ta 2,10,17408
Maaa 112,56,16
s_add_u32 m0, s62, 32768
Ta 4,10,24576
Ta 6,10,25600
Maaa 244,52,16
Maaa 244,148,20
Maaa 240,144,20
D 16,44,65,0
Maaa 240,48,16
Maaa 180,44,16
Maaa 180,140,20
Maaa 176,136,20
D 17,44,65,1024
Maaa 176,40,16
Mvaa 212,36,16
Mvaa 212,132,20
Mvaa 208,128,20
D 18,44,65,2048
Mvaa 208,32,16
Mvaa 216,32,24
Ra 32,26,0
Mvaa 216,128,28
Ra 128,27,0
Mvaa 220,132,28
D 19,44,65,3072
Ra 132,27,2048
Mvaa 220,36,24
s_add_u32 m0, s62, 36864
Ta 8,11,16384
Ta 10,11,17408
Maaa 184,40,24
Ra 36,26,2048
Ta 12,11,24576
Maaa 184,136,28
Ta 14,11,25600
Ra 40,26,4096
Maaa 188,140,28
D 20,44,65,0
Ra 136,27,4096
Maaa 188,44,24
Ra 140,27,6144
Ra 44,26,6144
Maaa 248,48,24
Ra 48,26,8192
Maaa 248,144,28
Ra 144,27,8192
Maaa 252,148,28
D 21,44,65,1024
Ra 148,27,10240
Maaa 252,52,24
Ra 52,26,10240
Maaa 120,56,24
Ra 56,26,12288
Maaa 120,152,28
Ra 152,27,12288
Maaa 124,156,28
D 22,44,65,2048
Ra 156,27,14336
Maaa 124,60,24
W 13
Ra 60,26,14336
Mvaa 160,32,0
Mvaa 160,128,4
Mvaa 164,132,4
D 23,44,65,3072
Ta 16,12,16384
Mvaa 164,36,0
W 9
s_add_u32 m0, s61, 32768
Ta 18,12,17408
Mvaa 224,40,0
.Lh_0_3_1%=:
s_cbranch_execz .Lhr_9%=
Ta 20,12,24576
Ta 22,12,25600
Mvaa 224,136,4
Mvaa 228,140,4
D 2,40,64,0
Mvaa 228,44,0
W 8
Maaa 192,48,0
Maaa 192,144,4
Maaa 196,148,4
D 3,40,64,1024
Maaa 196,52,0
W 4
Maaa 64,56,0
Maaa 64,152,4
Maaa 68,156,4
D 4,40,64,2048
Maaa 68,60,0
Maaa 76,60,8
Maaa 76,156,12
Maaa 72,152,12
D 5,40,64,3072
Ta 24,13,16384
Maaa 72,56,8
s_add_u32 m0, s61, 36864
Ta 26,13,17408
Ta 28,13,24576
Maaa 204,52,8
Ta 30,13,25600
Maaa 204,148,12
Maaa 200,144,12
D 6,40,64,0
Maaa 200,48,8
Mvaa 236,44,8
Mvaa 236,140,12
Mvaa 232,136,12
D 7,40,64,1024
Mvaa 232,40,8
Mvaa 172,36,8
Mvaa 172,132,12
Mvaa 168,128,12
D 8,40,64,2048
Mvaa 168,32,8
W 4
Mvaa 176,32,16
Mvaa 176,128,20
Mvaa 180,132,20
D 9,40,64,3072
v_add_u32 v2, 131072, v10
Mvaa 180,36,16
Ta 0,14,16384
v_add_u32 v3, 131072, v11
Mvaa 240,40,16
Ta 2,14,17408
v_add_u32 v4, 131072, v12
Mvaa 240,136,20
Ta 4,14,24576
Ta 6,14,25600
Mvaa 244,140,20
buffer_load_dword v142, v144, s[8:11], s91 offen offset:2048 sc1
v_add_u32 v5, 131072, v13
Mvaa 244,44,16
s_add_u32 s91, s91, 4096
v_add_u32 v6, 131072, v14
Maaa 208,48,16
v_add_u32 v7, 131072, v15
Maaa 208,144,20
Maaa 212,148,20
v_add_u32 v8, 131072, v24
Maaa 212,52,16
Maaa 80,56,16
v_add_u32 v9, 131072, v25
Maaa 80,152,20
s_add_u32 s65, s5, 0
s_cmp_ge_u32 s65, 20
s_cselect_b32 s90, 20, 0
s_sub_u32 s65, s65, s90
Maaa 84,156,20
s_lshl_b32 s65, s65, 18
s_add_u32 m0, s63, 131072
Maaa 84,60,16
W 4
Maaa 92,60,24
Maaa 92,156,28
Maaa 88,152,28
Ta 8,15,16384
Ta 10,15,17408
Maaa 88,56,24
D 16,48,65,0
Ta 12,15,24576
Maaa 220,52,24
Ta 14,15,25600
Maaa 220,148,28
Maaa 216,144,28
Maaa 216,48,24
D 17,48,65,1024
Mvaa 252,44,24
Mvaa 252,140,28
Mvaa 248,136,28
Mvaa 248,40,24
Mvaa 188,36,24
D 18,48,65,2048
Mvaa 188,132,28
Mvaa 184,128,28
Mvaa 184,32,24
W 4
Mvaa 192,32,0
Mvaa 192,128,4
D 19,48,65,3072
Mvaa 196,132,4
s_add_u32 m0, s63, 135168
Ta 16,24,16384
Ta 18,24,17408
Mvaa 196,36,0
Ta 20,24,24576
Ta 22,24,25600
Maaa 160,40,0
Maaa 160,136,4
Maaa 164,140,4
D 20,48,65,0
Maaa 164,44,0
Maaa 224,48,0
Maaa 224,144,4
Maaa 228,148,4
Maaa 228,52,0
D 21,48,65,1024
Maaa 96,56,0
Maaa 96,152,4
Maaa 100,156,4
Maaa 100,60,0
)"
R"(W 4
Maaa 108,60,8
D 22,48,65,2048
Ta 24,25,16384
Maaa 108,156,12
Ta 26,25,17408
Maaa 104,152,12
Ta 28,25,24576
Ta 30,25,25600
Maaa 104,56,8
Maaa 236,52,8
D 23,48,65,3072
Maaa 236,148,12
Maaa 232,144,12
Maaa 232,48,8
Maaa 172,44,8
Maaa 172,140,12
Maaa 168,136,12
s_waitcnt vmcnt(9) lgkmcnt(0)
s_barrier
s_cmp_eq_u32 s85, 107
s_cbranch_scc1 .Lrt_25%=
.Lrtr_26%=:
Maaa 168,40,8
s_add_u32 s65, s5, 4
Mvaa 204,36,8
Mvaa 204,132,12
s_cmp_ge_u32 s65, 20
s_cselect_b32 s90, 20, 0
Mvaa 200,128,12
Mvaa 200,32,8
s_sub_u32 s65, s65, s90
Mvaa 208,32,16
Mvaa 208,128,20
s_lshl_b32 s65, s65, 18
Mvaa 212,132,20
Ta 0,10,49152
Ta 2,10,50176
Mvaa 212,36,16
Maaa 176,40,16
s_add_u32 m0, s63, 0
Maaa 176,136,20
Maaa 180,140,20
D 16,48,65,0
Maaa 180,44,16
Maaa 240,48,16
Maaa 240,144,20
Maaa 244,148,20
D 17,48,65,1024
Ta 4,11,49152
Maaa 244,52,16
Ta 6,11,50176
Maaa 112,56,16
Maaa 112,152,20
Maaa 116,156,20
D 18,48,65,2048
Maaa 116,60,16
Maaa 124,60,24
Ra 60,26,47104
Maaa 124,156,28
Maaa 120,152,28
D 19,48,65,3072
Ta 8,12,49152
Maaa 120,56,24
s_add_u32 m0, s63, 4096
Ta 10,12,50176
Ra 56,26,45056
Maaa 252,52,24
Ra 52,26,43008
Maaa 252,148,28
Maaa 248,144,28
D 20,48,65,0
Maaa 248,48,24
Ra 48,26,40960
Maaa 188,44,24
Ra 44,26,38912
Maaa 188,140,28
Maaa 184,136,28
D 21,48,65,1024
Ta 12,13,49152
Maaa 184,40,24
Ta 14,13,50176
Ra 40,26,36864
Mvaa 220,36,24
Ra 36,26,34816
Mvaa 220,132,28
Mvaa 216,128,28
D 22,48,65,2048
Mvaa 216,32,24
Ra 32,26,32768
W 6
Maaa 68,60,0
Maaa 64,56,0
Maaa 196,52,0
D 23,48,65,3072
Ta 16,14,49152
Maaa 192,48,0
W 1
s_add_u32 s65, s5, 8
s_cmp_ge_u32 s65, 20
s_cselect_b32 s90, 20, 0
Mvaa 228,44,0
s_sub_u32 s65, s65, s90
s_lshl_b32 s65, s65, 18
Ta 18,14,50176
Mvaa 224,40,0
s_add_u32 m0, s63, 65536
Mvaa 164,36,0
D 16,48,65,0
Mvaa 160,32,0
Mvaa 168,32,4
Mvaa 172,36,4
Mvaa 232,40,4
D 17,48,65,1024
Ta 20,15,49152
Mvaa 236,44,4
Ta 22,15,50176
Maaa 200,48,4
Maaa 204,52,4
Maaa 72,56,4
D 18,48,65,2048
Maaa 76,60,4
Maaa 84,60,8
Maaa 80,56,8
Maaa 212,52,8
D 19,48,65,3072
Ta 24,24,49152
Maaa 208,48,8
s_add_u32 m0, s63, 69632
Ta 26,24,50176
Mvaa 244,44,8
Mvaa 240,40,8
Mvaa 180,36,8
D 20,48,65,0
Mvaa 176,32,8
Mvaa 184,32,12
Mvaa 188,36,12
Mvaa 248,40,12
D 21,48,65,1024
Ta 28,25,49152
Mvaa 252,44,12
Ta 30,25,50176
Maaa 216,48,12
Ra 156,27,47104
Maaa 220,52,12
Maaa 88,56,12
D 22,48,65,2048
Maaa 92,60,12
W 7
Ra 152,27,45056
Maaa 100,60,16
Maaa 96,56,16
Maaa 228,52,16
D 23,48,65,3072
Ra 148,27,43008
Maaa 224,48,16
Ta 0,10,57344
Ta 2,10,58368
Maaa 164,44,16
Maaa 160,40,16
Ra 144,27,40960
Mvaa 196,36,16
buffer_load_dword v142, v144, s[8:11], s91 offen offset:0 sc1
Mvaa 192,32,16
W 10
Mvaa 200,32,20
Ra 140,27,38912
Mvaa 204,36,20
Maaa 168,40,20
Ta 4,11,57344
Ta 6,11,58368
Maaa 172,44,20
Ra 136,27,36864
Maaa 232,48,20
Maaa 236,52,20
v_bfe_u32 v29, v1, 7, 1
Maaa 104,56,20
Ra 132,27,34816
Maaa 108,60,20
W 13
v_mul_u32_u24 v28, 10240, v29
Maaa 116,60,24
Maaa 112,56,24
Ra 128,27,32768
v_lshrrev_b32 v29, 8, v1
Maaa 244,52,24
Ta 8,12,57344
Ta 10,12,58368
Maaa 240,48,24
v_lshl_add_u32 v28, v29, 7, v28
Maaa 180,44,24
v_and_b32 v29, 0x70, v1
Maaa 176,40,24
Mvaa 212,36,24
v_add_u32 v28, v28, v29
Mvaa 208,32,24
W 14
Mvaa 216,32,28
s_and_b32 s80, s14, 2
Mvaa 220,36,28
s_mul_i32 s80, s80, 20480
Maaa 184,40,28
Ta 12,13,57344
Ta 14,13,58368
Maaa 188,44,28
s_sub_u32 s81, 40960, s80
Maaa 248,48,28
Maaa 252,52,28
s_add_u32 s93, s28, 20480
Maaa 120,56,28
Maaa 124,60,28
W 10
v_add_u32 v26, s80, v28
Maaa 68,156,0
Maaa 64,152,0
Maaa 196,148,0
Ta 16,14,57344
v_add_u32 v27, s81, v28
Maaa 192,144,0
W 5
Ta 18,14,58368
Mvaa 228,140,0
buffer_load_dwordx4 a[32:35], v26, s[8:11], s28 offen offset:0 sc1
Mvaa 224,136,0
Mvaa 164,132,0
Mvaa 160,128,0
Mvaa 168,128,4
buffer_load_dwordx4 a[36:39], v26, s[8:11], s93 offen offset:0 sc1
Mvaa 172,132,4
Mvaa 232,136,4
Ta 20,15,57344
Ta 22,15,58368
Mvaa 236,140,4
Maaa 200,144,4
buffer_load_dwordx4 a[40:43], v27, s[8:11], s28 offen offset:0 sc1
Maaa 204,148,4
Maaa 72,152,4
Maaa 76,156,4
W 6
Maaa 84,156,8
buffer_load_dwordx4 a[44:47], v27, s[8:11], s93 offen offset:0 sc1
Ta 24,24,57344
Maaa 80,152,8
s_add_u32 s28, s28, 512
s_cmp_eq_u32 s28, s4
s_cselect_b32 s93, 10240, 0
Ta 26,24,58368
Maaa 212,148,8
s_sub_u32 s28, s28, s93
Ta 28,25,57344
Ta 30,25,58368
Maaa 208,144,8
s_add_u32 s93, s28, 20480
Mvaa 244,140,8
Mvaa 240,136,8
Mvaa 180,132,8
s_add_u32 s65, s5, 12
s_cmp_ge_u32 s65, 20
s_cselect_b32 s90, 20, 0
buffer_load_dwordx4 a[48:51], v26, s[8:11], s28 offen offset:0 sc1
Mvaa 176,128,8
W 8
s_sub_u32 s65, s65, s90
s_lshl_b32 s65, s65, 18
Mvaa 184,128,12
s_add_u32 m0, s63, 32768
Mvaa 188,132,12
Mvaa 248,136,12
s_waitcnt vmcnt(6) lgkmcnt(0)
s_barrier
buffer_load_dwordx4 a[52:55], v26, s[8:11], s93 offen offset:0 sc1
Mvaa 252,140,12
Maaa 216,144,12
Maaa 220,148,12
Maaa 88,152,12
buffer_load_dwordx4 a[56:59], v27, s[8:11], s28 offen offset:0 sc1
Maaa 92,156,12
Maaa 100,156,16
Maaa 96,152,16
Maaa 228,148,16
buffer_load_dwordx4 a[60:63], v27, s[8:11], s93 offen offset:0 sc1
Maaa 224,144,16
Maaa 164,140,16
Maaa 160,136,16
Mvaa 196,132,16
D 16,48,65,0
Mvaa 192,128,16
Mvaa 200,128,20
Mvaa 204,132,20
Maaa 168,136,20
D 17,48,65,1024
Ta 0,2,0
Maaa 172,140,20
Ta 2,2,1024
Maaa 232,144,20
Maaa 236,148,20
Maaa 104,152,20
D 18,48,65,2048
Ta 4,2,8192
Maaa 108,156,20
Ta 6,2,9216
Maaa 116,156,24
Maaa 112,152,24
Maaa 244,148,24
Ta 8,3,0
Ta 10,3,1024
Maaa 240,144,24
D 19,48,65,3072
Maaa 180,140,24
s_add_u32 m0, s63, 36864
Maaa 176,136,24
Mvaa 212,132,24
Ta 12,3,8192
Ta 14,3,9216
Mvaa 208,128,24
D 20,48,65,0
Mvaa 216,128,28
Mvaa 220,132,28
Maaa 184,136,28
Ta 16,4,0
Ta 18,4,1024
Maaa 188,140,28
Maaa 248,144,28
D 21,48,65,1024
Maaa 252,148,28
Maaa 120,152,28
Maaa 124,156,28
.Lq_11%=:
s_cmp_ge_u32 s14, 2
s_cbranch_scc1 .Lpb_31%=
s_waitcnt vmcnt(6) lgkmcnt(6)
Zvaa 32,0,32
s_add_u32 s28, s28, 512
Ta 20,4,8192
Ta 22,4,9216
Mvaa 32,4,48
s_cmp_eq_u32 s28, s4
s_cselect_b32 s93, 10240, 0
Ta 24,5,0
Ta 26,5,1024
Zvaa 64,4,52
s_sub_u32 s28, s28, s93
s_add_u32 s93, s28, 20480
buffer_load_dwordx4 a[128:131], v26, s[8:11], s28 offen offset:0 sc1
Mvaa 64,0,36
Ta 28,5,8192
Ta 30,5,9216
Zvaa 96,0,40
K 184,184,185
K 185,186,187
Mvaa 96,4,56
K 186,188,189
K 187,190,191
Zvaa 128,4,60
K 192,192,193
Mvaa 128,0,44
W 8
buffer_load_dwordx4 a[132:135], v26, s[8:11], s93 offen offset:0 sc1
K 193,194,195
Zvaa 132,8,44
Ta 0,6,0
Ta 2,6,1024
Mvaa 132,12,60
K 194,196,197
K 195,198,199
Zvaa 100,12,56
K 200,200,201
K 201,202,203
Mvaa 100,8,40
Ta 4,6,8192
Ta 6,6,9216
Zvaa 68,8,36
buffer_load_dwordx4 a[136:139], v27, s[8:11], s28 offen offset:0 sc1
K 202,204,205
Mvaa 68,12,52
K 203,206,207
K 208,208,209
Zvaa 36,12,48
K 209,210,211
K 210,212,213
Mvaa 36,8,32
W 8
Ta 8,7,0
K 211,214,215
Zvaa 40,16,32
Ta 10,7,1024
K 216,216,217
Mvaa 40,20,48
buffer_load_dwordx4 a[140:143], v27, s[8:11], s93 offen offset:0 sc1
K 217,218,219
Zvaa 72,20,52
s_add_u32 s28, s28, 512
s_cmp_eq_u32 s28, s4
s_cselect_b32 s93, 10240, 0
s_sub_u32 s28, s28, s93
Mvaa 72,16,36
s_add_u32 s93, s28, 20480
K 218,220,221
K 219,222,223
Zvaa 104,16,40
)"
R"(Ta 12,7,8192
Ta 14,7,9216
Mvaa 104,20,56
K 224,224,225
K 225,226,227
Zvaa 136,20,60
K 226,228,229
K 227,230,231
Mvaa 136,16,44
W 8
buffer_load_dwordx4 a[144:147], v26, s[8:11], s28 offen offset:0 sc1
K 232,232,233
Zvaa 140,24,44
K 233,234,235
K 234,236,237
Mvaa 140,28,60
Ta 16,8,0
K 235,238,239
Zvaa 108,28,56
Ta 18,8,1024
K 240,240,241
Mvaa 108,24,40
K 241,242,243
K 242,244,245
Zvaa 76,24,36
buffer_load_dwordx4 a[148:151], v26, s[8:11], s93 offen offset:0 sc1
K 243,246,247
Mvaa 76,28,52
Ta 20,8,8192
Ta 22,8,9216
Zvaa 44,28,48
K 248,248,249
K 249,250,251
Mvaa 44,24,32
K 250,252,253
K 251,254,255
W 8
Zvaa 48,0,32
Ta 24,9,0
Ta 26,9,1024
Mvaa 48,4,48
buffer_load_dwordx4 a[152:155], v27, s[8:11], s28 offen offset:0 sc1
K 160,160,161
Zvaa 80,4,52
Ta 28,9,8192
Ta 30,9,9216
Mvaa 80,0,36
K 161,162,163
Zvaa 112,0,40
K 162,164,165
Mvaa 112,4,56
Zvaa 144,4,60
buffer_load_dwordx4 a[156:159], v27, s[8:11], s93 offen offset:0 sc1
K 163,166,167
Mvaa 144,0,44
W 8
s_add_u32 s28, s28, 512
s_cmp_eq_u32 s28, s4
s_cselect_b32 s93, 10240, 0
Zvaa 148,8,44
s_sub_u32 s28, s28, s93
s_add_u32 s93, s28, 20480
Ta 0,2,16384
Mvaa 148,12,60
s_add_u32 s36, s29, 0
Ta 2,2,17408
Zvaa 116,12,56
Ta 4,2,24576
Ta 6,2,25600
Mvaa 116,8,40
buffer_load_dwordx4 v[188:191], v26, s[8:11], s28 offen offset:0 sc1
Zvaa 84,8,36
Mvaa 84,12,52
Zvaa 52,12,48
Mvaa 52,8,32
W 8
Ta 8,3,16384
Ta 10,3,17408
Zvaa 56,16,32
buffer_load_dwordx4 v[196:199], v26, s[8:11], s93 offen offset:0 sc1
Mvaa 56,20,48
Zvaa 88,20,52
Ta 12,3,24576
Ta 14,3,25600
Mvaa 88,16,36
Zvaa 120,16,40
buffer_store_dwordx4 v[160:163], v1, s[8:11], s36 offen offset:0 sc1
K 168,168,169
Mvaa 120,20,56
K 169,170,171
K 170,172,173
Zvaa 152,20,60
Mvaa 152,16,44
W 8
Ta 16,4,16384
K 171,174,175
Zvaa 156,24,44
Ta 18,4,17408
Mvaa 156,28,60
buffer_load_dwordx4 v[204:207], v27, s[8:11], s28 offen offset:0 sc1
Zvaa 124,28,56
Ta 20,4,24576
Ta 22,4,25600
Mvaa 124,24,40
Zvaa 92,24,36
Mvaa 92,28,52
Zvaa 60,28,48
buffer_load_dwordx4 v[212:215], v27, s[8:11], s93 offen offset:0 sc1
Mvaa 60,24,32
s_waitcnt vmcnt(5) lgkmcnt(8)
s_add_u32 s28, s28, 512
s_cmp_eq_u32 s28, s4
s_cselect_b32 s93, 10240, 0
Mvaa 32,0,128
s_sub_u32 s28, s28, s93
s_add_u32 s93, s28, 20480
Ta 24,5,16384
Mvaa 32,4,144
Ta 26,5,17408
Mvaa 64,4,148
Ta 28,5,24576
Ta 30,5,25600
Mvaa 64,0,132
buffer_store_dwordx4 v[168:171], v1, s[8:11], s36 offen offset:1024 sc1
K 176,176,177
Mvaa 96,0,136
K 177,178,179
K 178,180,181
Mvaa 96,4,152
Mvaa 128,4,156
Mvaa 128,0,140
W 8
Ta 0,6,16384
K 179,182,183
Mvaa 132,8,140
buffer_load_dwordx4 v[220:223], v26, s[8:11], s28 offen offset:0 sc1
Ta 2,6,17408
Mvaa 132,12,156
Mvaa 100,12,152
Ta 4,6,24576
Ta 6,6,25600
Mvaa 100,8,136
Mvaa 68,8,132
Mvaa 68,12,148
buffer_load_dwordx4 v[228:231], v26, s[8:11], s93 offen offset:0 sc1
Mvaa 36,12,144
Mvaa 36,8,128
W 8
Ta 8,7,16384
Ta 10,7,17408
Mvaa 40,16,128
Mvaa 40,20,144
Mvaa 72,20,148
buffer_load_dwordx4 v[236:239], v27, s[8:11], s28 offen offset:0 sc1
Ta 12,7,24576
Mvaa 72,16,132
buffer_store_dwordx4 v[176:179], v1, s[8:11], s36 offen offset:2048 sc1
Ta 14,7,25600
Mvaa 104,16,136
Mvaa 104,20,152
Mvaa 136,20,156
Mvaa 136,16,140
W 8
Ta 16,8,16384
Ta 18,8,17408
Mvaa 140,24,140
buffer_load_dwordx4 v[244:247], v27, s[8:11], s93 offen offset:0 sc1
Mvaa 140,28,156
s_add_u32 s28, s28, 512
s_cmp_eq_u32 s28, s4
s_cselect_b32 s93, 10240, 0
s_sub_u32 s28, s28, s93
Mvaa 108,28,152
s_add_u32 s93, s28, 20480
Ta 20,8,24576
Ta 22,8,25600
Mvaa 108,24,136
Mvaa 76,24,132
Mvaa 76,28,148
buffer_load_dwordx4 v[252:255], v26, s[8:11], s28 offen offset:0 sc1
Mvaa 44,28,144
Mvaa 44,24,128
W 8
Ta 24,9,16384
Ta 26,9,17408
Mvaa 48,0,128
Mvaa 48,4,144
Mvaa 80,4,148
D 22,48,65,2048
Ta 28,9,24576
Mvaa 80,0,132
buffer_store_dwordx4 v[184:187], v1, s[8:11], s36 offen offset:3072 sc1
Ta 30,9,25600
Mvaa 112,0,136
s_add_u32 s36, s29, 4096
s_lshl_b32 s80, s56, 9
Mvaa 112,4,152
Mvaa 144,4,156
s_lshl_b32 s81, s57, 4
Mvaa 144,0,140
W 8
s_add_u32 s80, s80, s81
Ta 0,10,0
Mvaa 148,8,140
buffer_load_dwordx4 a[32:35], v26, s[8:11], s93 offen offset:0 sc1
Ta 2,10,1024
Mvaa 148,12,156
s_and_b32 s81, s14, 1
Mvaa 116,12,152
s_lshl_b32 s81, s81, 3
Ta 4,10,8192
Ta 6,10,9216
Mvaa 116,8,136
Mvaa 84,8,132
s_add_u32 s80, s80, s81
Mvaa 84,12,148
s_add_u32 s71, s80, 4096
buffer_load_dwordx4 a[36:39], v27, s[8:11], s28 offen offset:0 sc1
Mvaa 52,12,144
Mvaa 52,8,128
W 8
Ta 8,11,0
Ta 10,11,1024
Mvaa 56,16,128
Mvaa 56,20,144
Mvaa 88,20,148
buffer_store_dwordx4 v[192:195], v1, s[8:11], s36 offen offset:0 sc1
Ta 12,11,8192
Mvaa 88,16,132
Ta 14,11,9216
Mvaa 120,16,136
Mvaa 120,20,152
Mvaa 152,20,156
Mvaa 152,16,140
W 8
buffer_load_dwordx4 a[40:43], v27, s[8:11], s93 offen offset:0 sc1
Ta 16,12,0
Mvaa 156,24,140
s_add_u32 s28, s28, 512
s_cmp_eq_u32 s28, s4
s_cselect_b32 s93, 10240, 0
s_sub_u32 s28, s28, s93
Mvaa 156,28,156
s_add_u32 s93, s28, 20480
Ta 18,12,1024
Mvaa 124,28,152
Ta 20,12,8192
Ta 22,12,9216
Mvaa 124,24,136
Mvaa 92,24,132
buffer_load_dwordx4 a[44:47], v26, s[8:11], s28 offen offset:0 sc1
Mvaa 92,28,148
Mvaa 60,28,144
Mvaa 60,24,128
s_waitcnt vmcnt(8) lgkmcnt(8)
Ta 24,13,0
Ta 26,13,1024
Mvav 32,0,188
Mvav 32,4,220
D 23,48,65,3072
Mvav 64,4,228
s_add_u32 s65, s5, 16
s_cmp_ge_u32 s65, 20
s_cselect_b32 s90, 20, 0
buffer_store_dwordx4 v[200:203], v1, s[8:11], s36 offen offset:1024 sc1
Mvav 64,0,196
s_sub_u32 s65, s65, s90
s_lshl_b32 s65, s65, 18
s_add_u32 m0, s63, 98304
Mvav 96,0,204
Ta 28,13,8192
Ta 30,13,9216
Mvav 96,4,236
Mvav 128,4,244
Mvav 128,0,212
W 8
buffer_load_dwordx4 a[48:51], v26, s[8:11], s93 offen offset:0 sc1
Ta 0,14,0
Mvav 132,8,212
Ta 2,14,1024
v_add_u32 v2, 65536, v10
Mvav 132,12,244
Mvav 100,12,236
Ta 4,14,8192
Ta 6,14,9216
Mvav 100,8,204
v_add_u32 v3, 65536, v11
Mvav 68,8,196
buffer_load_dwordx4 a[52:55], v27, s[8:11], s28 offen offset:0 sc1
Mvav 68,12,228
Mvav 36,12,220
Mvav 36,8,188
W 8
Ta 8,15,0
Ta 10,15,1024
Mvav 40,16,188
v_add_u32 v4, 65536, v12
Mvav 40,20,220
buffer_load_dwordx4 a[56:59], v27, s[8:11], s93 offen offset:0 sc1
Mvav 72,20,228
s_add_u32 s28, s28, 512
s_cmp_eq_u32 s28, s4
s_cselect_b32 s93, 10240, 0
buffer_store_dwordx4 v[208:211], v1, s[8:11], s36 offen offset:2048 sc1
Mvav 72,16,196
s_sub_u32 s28, s28, s93
s_add_u32 s93, s28, 20480
Ta 12,15,8192
Mvav 104,16,204
Ta 14,15,9216
v_add_u32 v5, 65536, v13
Mvav 104,20,236
Mvav 136,20,244
Mvav 136,16,212
W 8
D 16,48,65,0
Ta 16,24,0
Mvav 140,24,212
Ta 18,24,1024
v_add_u32 v6, 65536, v14
Mvav 140,28,244
Mvav 108,28,236
Ta 20,24,8192
Ta 22,24,9216
Mvav 108,24,204
v_add_u32 v7, 65536, v15
Mvav 76,24,196
buffer_load_dwordx4 a[60:63], v26, s[8:11], s28 offen offset:0 sc1
Mvav 76,28,228
Mvav 44,28,220
Mvav 44,24,188
W 8
Ta 24,25,0
Ta 26,25,1024
Mvav 48,0,188
v_add_u32 v8, 65536, v24
Mvav 48,4,220
buffer_load_dwordx4 v[184:187], v26, s[8:11], s93 offen offset:0 sc1
Mvav 80,4,228
buffer_store_dwordx4 v[216:219], v1, s[8:11], s36 offen offset:3072 sc1
Ta 28,25,8192
Mvav 80,0,196
s_add_u32 s36, s29, 458752
Ta 30,25,9216
v_add_u32 v9, 65536, v25
Mvav 112,0,204
Mvav 112,4,236
Mvav 144,4,244
Mvav 144,0,212
W 8
buffer_load_dwordx4 v[192:195], v27, s[8:11], s28 offen offset:0 sc1
Ta 0,10,16384
Mvav 148,8,212
Ta 2,10,17408
Mvav 148,12,244
Mvav 116,12,236
Ta 4,10,24576
Ta 6,10,25600
Mvav 116,8,204
Mvav 84,8,196
D 17,48,65,1024
Mvav 84,12,228
Mvav 52,12,220
Mvav 52,8,188
W 8
Ta 8,11,16384
Ta 10,11,17408
Mvav 56,16,188
Mvav 56,20,220
buffer_store_dwordx4 v[224:227], v1, s[8:11], s36 offen offset:0 sc1
Mvav 88,20,228
Ta 12,11,24576
Ta 14,11,25600
Mvav 88,16,196
Mvav 120,16,204
Mvav 120,20,236
Mvav 152,20,244
buffer_load_dwordx4 a[128:131], v27, s[8:11], s93 offen offset:0 sc1
Mvav 152,16,212
W 8
s_add_u32 s28, s28, 512
s_cmp_eq_u32 s28, s4
s_cselect_b32 s93, 10240, 0
Mvav 156,24,212
s_sub_u32 s28, s28, s93
s_add_u32 s93, s28, 20480
Ta 16,12,16384
Mvav 156,28,244
Ta 18,12,17408
Mvav 124,28,236
Ta 20,12,24576
Ta 22,12,25600
Mvav 124,24,204
buffer_load_dwordx4 a[132:135], v26, s[8:11], s28 offen offset:0 sc1
Mvav 92,24,196
Mvav 92,28,228
Mvav 60,28,220
Mvav 60,24,188
s_waitcnt vmcnt(10) lgkmcnt(8)
Ta 24,13,16384
)"
R"(Ta 26,13,17408
Mvav 32,0,252
buffer_load_dwordx4 a[136:139], v26, s[8:11], s93 offen offset:0 sc1
Mvaa 32,4,44
buffer_store_dwordx4 v[232:235], v1, s[8:11], s36 offen offset:1024 sc1
Mvaa 64,4,48
Ta 28,13,24576
Ta 30,13,25600
Mvaa 64,0,32
Mvaa 96,0,36
Mvaa 96,4,52
Mvaa 128,4,56
buffer_load_dwordx4 a[140:143], v27, s[8:11], s28 offen offset:0 sc1
Mvaa 128,0,40
W 8
Ta 0,14,16384
Ta 2,14,17408
Mvaa 132,8,40
Mvaa 132,12,56
Mvaa 100,12,52
Ta 4,14,24576
Ta 6,14,25600
Mvaa 100,8,36
D 18,48,65,2048
Mvaa 68,8,32
Mvaa 68,12,48
Mvaa 36,12,44
Mvav 36,8,252
W 8
Ta 8,15,16384
Ta 10,15,17408
Mvav 40,16,252
buffer_load_dwordx4 a[144:147], v27, s[8:11], s93 offen offset:0 sc1
Mvaa 40,20,44
s_add_u32 s28, s28, 512
s_cmp_eq_u32 s28, s4
s_cselect_b32 s93, 10240, 0
buffer_store_dwordx4 v[240:243], v1, s[8:11], s36 offen offset:2048 sc1
Mvaa 72,20,48
s_sub_u32 s28, s28, s93
s_add_u32 s93, s28, 20480
Ta 12,15,24576
Mvaa 72,16,32
Ta 14,15,25600
Mvaa 104,16,36
Mvaa 104,20,52
Mvaa 136,20,56
buffer_load_dwordx4 a[148:151], v26, s[8:11], s28 offen offset:0 sc1
Mvaa 136,16,40
W 8
Ta 16,24,16384
Ta 18,24,17408
Mvaa 140,24,40
Mvaa 140,28,56
Mvaa 108,28,52
Ta 20,24,24576
Ta 22,24,25600
Mvaa 108,24,36
buffer_load_dwordx4 a[152:155], v26, s[8:11], s93 offen offset:0 sc1
Mvaa 76,24,32
Mvaa 76,28,48
Mvaa 44,28,44
Mvav 44,24,252
W 8
Ta 24,25,16384
Ta 26,25,17408
Mvav 48,0,252
buffer_load_dwordx4 a[156:159], v27, s[8:11], s28 offen offset:0 sc1
Mvaa 48,4,44
buffer_store_dwordx4 v[248:251], v1, s[8:11], s36 offen offset:3072 sc1
G 160,160
Mvaa 80,4,48
G 161,161
G 162,162
Mvaa 80,0,32
G 163,163
G 164,164
Mvaa 112,0,36
G 165,165
G 166,166
Mvaa 112,4,52
Ta 28,25,24576
Ta 30,25,25600
Mvaa 144,4,56
D 19,48,65,3072
G 167,167
Mvaa 144,0,40
W 8
s_add_u32 m0, s63, 102400
K 160,160,161
Mvaa 148,8,40
K 161,162,163
K 162,164,165
Mvaa 148,12,56
Ta 0,2,0
K 163,166,167
Mvaa 116,12,52
s_add_u32 s36, s29, 462848
Ta 2,2,1024
Ta 4,2,8192
Mvaa 116,8,36
buffer_load_dwordx4 v[200:203], v27, s[8:11], s93 offen offset:0 sc1
Ta 6,2,9216
Mvaa 84,8,32
s_add_u32 s28, s28, 512
s_cmp_eq_u32 s28, s4
s_cselect_b32 s93, 10240, 0
s_sub_u32 s28, s28, s93
Mvaa 84,12,48
s_add_u32 s93, s28, 20480
Mvaa 52,12,44
Mvav 52,8,252
W 8
Ta 8,3,0
Ta 10,3,1024
Mvav 56,16,252
buffer_store_dwordx4 v[160:163], v1, s[8:11], s36 offen offset:0 sc1
G 168,168
Mvaa 56,20,44
G 169,169
G 170,170
Mvaa 88,20,48
G 171,171
G 172,172
Mvaa 88,16,32
G 173,173
G 174,174
Mvaa 120,16,36
Ta 12,3,8192
Ta 14,3,9216
Mvaa 120,20,52
buffer_load_dwordx4 v[208:211], v26, s[8:11], s28 offen offset:0 sc1
G 175,175
Mvaa 152,20,56
K 168,168,169
K 169,170,171
Mvaa 152,16,40
W 8
K 170,172,173
K 171,174,175
Mvaa 156,24,40
Ta 16,4,0
Ta 18,4,1024
Mvaa 156,28,56
Mvaa 124,28,52
buffer_load_dwordx4 v[216:219], v26, s[8:11], s93 offen offset:0 sc1
Ta 20,4,8192
Mvaa 124,24,36
Ta 22,4,9216
Mvaa 92,24,32
Mvaa 92,28,48
Mvaa 60,28,44
Mvav 60,24,252
s_waitcnt vmcnt(10) lgkmcnt(8)
D 20,48,65,0
Ta 24,5,0
Mvaa 32,0,60
buffer_store_dwordx4 v[168:171], v1, s[8:11], s36 offen offset:1024 sc1
G 176,176
Mvaa 32,4,132
G 177,177
G 178,178
Mvaa 64,4,136
Ta 26,5,1024
G 179,179
Mvav 64,0,184
G 180,180
G 181,181
Mvav 96,0,192
Ta 28,5,8192
G 182,182
Mvaa 96,4,140
buffer_load_dwordx4 v[224:227], v27, s[8:11], s28 offen offset:0 sc1
Ta 30,5,9216
Mvaa 128,4,144
G 183,183
K 176,176,177
Mvaa 128,0,128
W 8
K 177,178,179
K 178,180,181
Mvaa 132,8,128
Ta 0,6,0
K 179,182,183
Mvaa 132,12,144
Ta 2,6,1024
Mvaa 100,12,140
buffer_load_dwordx4 v[188:191], v27, s[8:11], s93 offen offset:0 sc1
Ta 4,6,8192
Mvav 100,8,192
s_add_u32 s28, s28, 512
s_cmp_eq_u32 s28, s4
s_cselect_b32 s93, 10240, 0
s_sub_u32 s28, s28, s93
Mvav 68,8,184
s_add_u32 s93, s28, 20480
Ta 6,6,9216
Mvaa 68,12,136
Mvaa 36,12,132
Mvaa 36,8,60
W 8
buffer_load_dwordx4 v[196:199], v26, s[8:11], s28 offen offset:0 sc1
Ta 8,7,0
Mvaa 40,16,60
buffer_store_dwordx4 v[176:179], v1, s[8:11], s36 offen offset:2048 sc1
G 160,184
Mvaa 40,20,132
G 161,185
G 162,186
Mvaa 72,20,136
Ta 10,7,1024
G 163,187
Mvav 72,16,184
G 164,188
G 165,189
Mvav 104,16,192
Ta 12,7,8192
G 166,190
Mvaa 104,20,140
buffer_load_dwordx4 v[204:207], v26, s[8:11], s93 offen offset:0 sc1
Ta 14,7,9216
Mvaa 136,20,144
G 167,191
K 160,160,161
Mvaa 136,16,128
W 8
K 161,162,163
K 162,164,165
Mvaa 140,24,128
Ta 16,8,0
K 163,166,167
Mvaa 140,28,144
Ta 18,8,1024
Mvaa 108,28,140
D 21,48,65,1024
Ta 20,8,8192
Mvav 108,24,192
Ta 22,8,9216
Mvav 76,24,184
Mvaa 76,28,136
Mvaa 44,28,132
Mvaa 44,24,60
W 8
buffer_load_dwordx4 v[212:215], v27, s[8:11], s28 offen offset:0 sc1
Ta 24,9,0
Mvaa 48,0,60
buffer_store_dwordx4 v[160:163], v1, s[8:11], s36 offen offset:3072 sc1
G 168,192
Mvaa 48,4,132
G 169,193
G 170,194
Mvaa 80,4,136
G 171,195
G 172,196
Mvav 80,0,184
Ta 26,9,1024
G 173,197
Mvav 112,0,192
G 174,198
G 175,199
Mvaa 112,4,140
buffer_load_dwordx4 v[220:223], v27, s[8:11], s93 offen offset:0 sc1
Ta 28,9,8192
Mvaa 144,4,144
s_add_u32 s28, s28, 512
s_cmp_eq_u32 s28, s4
s_cselect_b32 s93, 10240, 0
s_sub_u32 s28, s28, s93
Mvaa 144,0,128
W 7
s_add_u32 s93, s28, 20480
Ta 30,9,9216
Mvaa 148,8,128
K 168,168,169
K 169,170,171
Mvaa 148,12,144
K 170,172,173
K 171,174,175
Mvaa 116,12,140
buffer_load_dwordx4 v[228:231], v26, s[8:11], s28 offen offset:0 sc1
Ta 0,2,16384
Mvav 116,8,192
Ta 2,2,17408
v_accvgpr_write_b32 a192, v168
Mvav 84,8,184
v_accvgpr_write_b32 a193, v169
v_accvgpr_write_b32 a194, v170
Mvaa 84,12,136
Ta 4,2,24576
Ta 6,2,25600
Mvaa 52,12,132
v_accvgpr_write_b32 a195, v171
G 176,64
Mvaa 52,8,60
W 8
D 22,48,65,2048
G 177,65
Mvaa 56,16,60
D 23,48,65,3072
G 178,66
Mvaa 56,20,132
Ta 8,3,16384
Ta 10,3,17408
Mvaa 88,20,136
G 179,67
G 180,68
Mvav 88,16,184
Ta 12,3,24576
Ta 14,3,25600
Mvav 120,16,192
G 181,69
G 182,70
Mvaa 120,20,140
buffer_load_dwordx4 v[236:239], v26, s[8:11], s93 offen offset:0 sc1
G 183,71
Mvaa 152,20,144
v_lshrrev_b32 v20, 4, v1
K 176,176,177
Mvaa 152,16,128
W 8
Ta 16,4,16384
v_add_u32 v20, 0, v20
Mvaa 156,24,128
Ta 18,4,17408
K 177,178,179
Mvaa 156,28,144
v_mul_u32_u24 v22, 205, v20
K 178,180,181
Mvaa 124,28,140
buffer_load_dwordx4 v[232:235], v27, s[8:11], s28 offen offset:0 sc1
K 179,182,183
Mvav 124,24,192
Ta 20,4,24576
Ta 22,4,25600
Mvav 92,24,184
v_lshrrev_b32 v22, 12, v22
v_accvgpr_write_b32 a196, v176
Mvaa 92,28,136
v_mul_u32_u24 v23, 20, v22
v_accvgpr_write_b32 a197, v177
Mvaa 60,28,132
v_accvgpr_write_b32 a198, v178
v_sub_u32 v20, v20, v23
Mvaa 60,24,60
s_waitcnt vmcnt(12) lgkmcnt(8)
buffer_load_dwordx4 a[32:35], v27, s[8:11], s93 offen offset:0 sc1
v_accvgpr_write_b32 a199, v179
Mvaa 32,0,148
s_add_u32 s28, s28, 512
s_cmp_eq_u32 s28, s4
s_cselect_b32 s93, 10240, 0
s_sub_u32 s28, s28, s93
Mvav 32,4,208
s_add_u32 s93, s28, 20480
Ta 24,5,16384
Ta 26,5,17408
Mvav 64,4,216
v_lshrrev_b32 v23, 1, v22
G 168,200
Mvaa 64,0,152
v_and_b32 v23, 2, v23
G 169,201
Mvaa 96,0,156
buffer_load_dwordx4 a[36:39], v26, s[8:11], s28 offen offset:0 sc1
Ta 28,5,24576
Mvav 96,4,224
Ta 30,5,25600
G 170,202
Mvav 128,4,188
v_xor_b32 v20, v20, v23
G 171,203
Mvav 128,0,200
W 8
v_mul_u32_u24 v22, 80, v22
G 172,204
Mvav 132,8,200
v_add_lshl_u32 v16, v20, v22, 4
G 173,205
Mvav 132,12,188
buffer_load_dwordx4 a[40:43], v26, s[8:11], s93 offen offset:0 sc1
Ta 0,6,16384
Mvav 100,12,224
Ta 2,6,17408
G 174,206
Mvaa 100,8,156
v_lshrrev_b32 v20, 4, v1
G 175,207
Mvaa 68,8,152
Ta 4,6,24576
v_add_u32 v20, 64, v20
Mvav 68,12,216
Ta 6,6,25600
K 168,168,169
Mvav 36,12,208
buffer_load_dwordx4 a[44:47], v27, s[8:11], s28 offen offset:0 sc1
K 169,170,171
Mvaa 36,8,148
W 8
v_mul_u32_u24 v22, 205, v20
K 170,172,173
)"
R"(Mvaa 40,16,148
v_lshrrev_b32 v22, 12, v22
K 171,174,175
Mvav 40,20,208
Ta 8,7,16384
v_mul_u32_u24 v23, 20, v22
Mvav 72,20,216
Ta 10,7,17408
v_accvgpr_write_b32 a200, v168
Mvaa 72,16,152
buffer_load_dwordx4 a[48:51], v27, s[8:11], s93 offen offset:0 sc1
v_accvgpr_write_b32 a201, v169
Mvaa 104,16,156
s_add_u32 s28, s28, 512
s_cmp_eq_u32 s28, s4
s_cselect_b32 s93, 10240, 0
s_sub_u32 s28, s28, s93
Mvav 104,20,224
s_add_u32 s93, s28, 20480
v_sub_u32 v20, v20, v23
v_accvgpr_write_b32 a202, v170
Mvav 136,20,188
Ta 12,7,24576
Ta 14,7,25600
Mvav 136,16,200
W 8
v_lshrrev_b32 v23, 1, v22
v_accvgpr_write_b32 a203, v171
Mvav 140,24,200
buffer_load_dwordx4 a[52:55], v26, s[8:11], s28 offen offset:0 sc1
v_and_b32 v23, 2, v23
Mvav 140,28,188
G 176,72
G 177,73
Mvav 108,28,224
v_xor_b32 v20, v20, v23
G 178,74
Mvaa 108,24,156
Ta 16,8,16384
Ta 18,8,17408
Mvaa 76,24,152
v_mul_u32_u24 v22, 80, v22
G 179,75
Mvav 76,28,216
buffer_load_dwordx4 a[56:59], v26, s[8:11], s93 offen offset:0 sc1
G 180,76
Mvav 44,28,208
Ta 20,8,24576
v_add_lshl_u32 v17, v20, v22, 4
Mvaa 44,24,148
W 7
Ta 22,8,25600
G 181,77
Mvaa 48,0,148
v_subrev_u32 v17, 1024, v17
G 182,78
Mvav 48,4,208
v_lshrrev_b32 v20, 4, v1
G 183,79
Mvav 80,4,216
buffer_load_dwordx4 a[184:187], v27, s[8:11], s28 offen offset:0 sc1
K 176,176,177
Mvaa 80,0,152
Ta 24,9,16384
v_add_u32 v20, 128, v20
Mvaa 112,0,156
Ta 26,9,17408
K 177,178,179
Mvav 112,4,224
v_mul_u32_u24 v22, 205, v20
K 178,180,181
Mvav 144,4,188
v_lshrrev_b32 v22, 12, v22
K 179,182,183
Mvav 144,0,200
W 6
buffer_load_dwordx4 a[188:191], v27, s[8:11], s93 offen offset:0 sc1
Ta 28,9,24576
Mvav 148,8,200
s_add_u32 s28, s28, 512
s_cmp_eq_u32 s28, s4
s_cselect_b32 s93, 10240, 0
s_sub_u32 s28, s28, s93
Mvav 148,12,188
s_add_u32 s93, s28, 20480
Ta 30,9,25600
v_accvgpr_write_b32 a204, v176
Mvav 116,12,224
v_mul_u32_u24 v23, 20, v22
v_accvgpr_write_b32 a205, v177
Mvaa 116,8,156
s_waitcnt vmcnt(15)
v_mov_b64 v[28:29], s[12:13]
s_mov_b64 exec, 1
buffer_store_dwordx2 v[28:29], off, s[8:11], s71 sc1
s_mov_b64 exec, -1
v_sub_u32 v20, v20, v23
v_accvgpr_write_b32 a206, v178
Mvaa 84,8,152
s_waitcnt vmcnt(12) lgkmcnt(0)
s_barrier
v_accvgpr_write_b32 a207, v179
Mvav 84,12,216
v_lshrrev_b32 v23, 1, v22
G 168,208
Mvav 52,12,208
v_and_b32 v23, 2, v23
G 169,209
Mvaa 52,8,148
v_xor_b32 v20, v20, v23
G 170,210
Mvaa 56,16,148
buffer_load_dwordx4 a[60:63], v26, s[8:11], s28 offen offset:0 sc1
G 171,211
Mvav 56,20,208
Ta 0,10,32768
Ta 2,10,33792
Mvav 88,20,216
Ta 4,10,40960
Ta 6,10,41984
Mvaa 88,16,152
v_mul_u32_u24 v22, 80, v22
G 172,212
Mvaa 120,16,156
v_add_lshl_u32 v18, v20, v22, 4
G 173,213
Mvav 120,20,224
buffer_load_dwordx4 v[184:187], v26, s[8:11], s93 offen offset:0 sc1
Ta 8,11,32768
Mvav 152,20,188
Ta 10,11,33792
G 174,214
Mvav 152,16,200
v_subrev_u32 v18, 2048, v18
G 175,215
Mvav 156,24,200
Ta 12,11,40960
v_lshrrev_b32 v20, 4, v1
Mvav 156,28,188
Ta 14,11,41984
K 168,168,169
Mvav 124,28,224
buffer_load_dwordx4 v[192:195], v27, s[8:11], s28 offen offset:0 sc1
v_add_u32 v20, 192, v20
Mvaa 124,24,156
K 169,170,171
K 170,172,173
Mvaa 92,24,152
v_mul_u32_u24 v22, 205, v20
K 171,174,175
Mvav 92,28,216
Ta 16,12,32768
v_lshrrev_b32 v22, 12, v22
Mvav 60,28,208
Ta 18,12,33792
v_accvgpr_write_b32 a208, v168
Mvaa 60,24,148
s_waitcnt vmcnt(12) lgkmcnt(6)
buffer_load_dwordx4 a[128:131], v27, s[8:11], s93 offen offset:0 sc1
v_mul_u32_u24 v23, 20, v22
Mvav 32,0,196
s_add_u32 s28, s28, 512
v_accvgpr_write_b32 a209, v169
v_accvgpr_write_b32 a210, v170
Mvav 32,4,228
s_cmp_eq_u32 s28, s4
s_cselect_b32 s93, 10240, 0
Ta 20,12,40960
v_sub_u32 v20, v20, v23
Mvav 64,4,236
s_sub_u32 s28, s28, s93
Ta 22,12,41984
v_accvgpr_write_b32 a211, v171
Mvav 64,0,204
s_add_u32 s93, s28, 20480
v_lshrrev_b32 v23, 1, v22
G 176,80
Mvav 96,0,212
G 177,81
v_and_b32 v23, 2, v23
Mvav 96,4,232
G 178,82
v_xor_b32 v20, v20, v23
Mvaa 128,4,32
Ta 24,13,32768
Ta 26,13,33792
Mvav 128,0,220
W 6
buffer_load_dwordx4 a[132:135], v26, s[8:11], s28 offen offset:0 sc1
G 179,83
Mvav 132,8,220
v_mul_u32_u24 v22, 80, v22
G 180,84
Mvaa 132,12,32
Ta 28,13,40960
Ta 30,13,41984
Mvav 100,12,232
G 181,85
v_add_lshl_u32 v19, v20, v22, 4
Mvav 100,8,212
G 182,86
v_subrev_u32 v19, 3072, v19
Mvav 68,8,204
G 183,87
v_lshrrev_b32 v20, 4, v1
Mvav 68,12,236
Ta 0,14,32768
K 176,176,177
Mvav 36,12,228
Ta 2,14,33792
K 177,178,179
Mvav 36,8,196
W 6
buffer_load_dwordx4 a[136:139], v26, s[8:11], s93 offen offset:0 sc1
v_add_u32 v20, 256, v20
Mvav 40,16,196
K 178,180,181
v_mul_u32_u24 v22, 205, v20
Mvav 40,20,228
Ta 4,14,40960
K 179,182,183
Mvav 72,20,236
Ta 6,14,41984
v_accvgpr_write_b32 a212, v176
Mvav 72,16,204
v_lshrrev_b32 v22, 12, v22
v_accvgpr_write_b32 a213, v177
Mvav 104,16,212
v_mul_u32_u24 v23, 20, v22
v_accvgpr_write_b32 a214, v178
Mvav 104,20,232
v_sub_u32 v20, v20, v23
v_accvgpr_write_b32 a215, v179
Mvaa 136,20,32
Ta 8,15,32768
Ta 10,15,33792
Mvav 136,16,220
W 6
buffer_load_dwordx4 a[140:143], v27, s[8:11], s28 offen offset:0 sc1
G 168,216
Mvav 140,24,220
v_lshrrev_b32 v23, 1, v22
G 169,217
Mvaa 140,28,32
Ta 12,15,40960
v_and_b32 v23, 2, v23
Mvav 108,28,232
Ta 14,15,41984
G 170,218
Mvav 108,24,212
v_xor_b32 v20, v20, v23
G 171,219
Mvav 76,24,204
G 172,220
v_mul_u32_u24 v22, 80, v22
Mvav 76,28,236
Ta 16,24,32768
G 173,221
Mvav 44,28,228
Ta 18,24,33792
v_add_lshl_u32 v20, v20, v22, 4
Mvav 44,24,196
W 6
buffer_load_dwordx4 a[144:147], v27, s[8:11], s93 offen offset:0 sc1
G 174,222
Mvav 48,0,196
s_add_u32 s28, s28, 512
G 175,223
v_lshrrev_b32 v22, 6, v1
Mvav 48,4,228
s_cmp_eq_u32 s28, s4
s_cselect_b32 s93, 10240, 0
Ta 20,24,40960
Ta 22,24,41984
Mvav 80,4,236
s_sub_u32 s28, s28, s93
K 168,168,169
v_mul_u32_u24 v22, 320, v22
Mvav 80,0,204
s_add_u32 s93, s28, 20480
K 169,170,171
v_bfe_u32 v23, v1, 4, 2
Mvav 112,0,212
K 170,172,173
K 171,174,175
Mvav 112,4,232
Ta 24,25,32768
v_lshl_add_u32 v22, v23, 3, v22
Mvaa 144,4,32
Ta 26,25,33792
v_accvgpr_write_b32 a216, v168
Mvav 144,0,220
W 6
buffer_load_dwordx4 v[200:203], v26, s[8:11], s28 offen offset:0 sc1
v_bfe_u32 v23, v1, 8, 1
Mvav 148,8,220
v_accvgpr_write_b32 a217, v169
v_lshlrev_b32 v23, 5, v23
Mvaa 148,12,32
Ta 28,25,40960
v_accvgpr_write_b32 a218, v170
Mvav 116,12,232
Ta 30,25,41984
v_accvgpr_write_b32 a219, v171
Mvav 116,8,212
v_add_u32 v21, v22, v23
G 176,88
Mvav 84,8,204
v_sub_u32 v22, v22, v23
G 177,89
Mvav 84,12,236
s_mul_i32 s94, s59, 655360
Ta 0,10,49152
G 178,90
Mvav 52,12,228
s_mul_i32 s95, s58, 320
Ta 2,10,50176
G 179,91
Mvav 52,8,196
W 6
buffer_load_dwordx4 v[208:211], v26, s[8:11], s93 offen offset:0 sc1
G 180,92
Mvav 56,16,196
s_add_u32 s94, s94, s95
Ta 4,10,57344
Ta 6,10,58368
Mvav 56,20,228
G 181,93
G 182,94
Mvav 88,20,236
s_add_u32 s40, s26, s94
s_addc_u32 s41, s27, 0
G 183,95
Mvav 88,16,204
s_and_b32 s41, s41, 0xffff
Ta 8,11,49152
K 176,176,177
Mvav 120,16,212
s_mov_b32 s42, 5242880
Ta 10,11,50176
K 177,178,179
Mvav 120,20,232
s_mov_b32 s43, 0x20000
buffer_load_dwordx4 v[216:219], v27, s[8:11], s28 offen offset:0 sc1
K 178,180,181
Mvaa 152,20,32
s_sub_u32 s42, s42, s94
Ta 12,11,57344
K 179,182,183
Mvav 152,16,220
W 7
Ta 14,11,58368
v_accvgpr_write_b32 a220, v176
Mvav 156,24,220
s_mov_b64 s[44:45], s[8:9]
v_accvgpr_write_b32 a221, v177
v_accvgpr_write_b32 a222, v178
Mvaa 156,28,32
s_mov_b32 s47, 0x20000
Ta 16,12,49152
v_accvgpr_write_b32 a223, v179
Mvav 124,28,232
s_and_b32 s48, s14, 1
Ta 18,12,50176
buffer_load_dwordx4 v[224:227], v27, s[8:11], s93 offen offset:0 sc1
Mvav 124,24,212
G 168,224
G 169,225
Mvav 92,24,204
s_mul_i32 s48, s48, 5120
Ta 20,12,57344
Ta 22,12,58368
Mvav 92,28,236
s_lshr_b32 s51, s14, 1
G 170,226
G 171,227
Mvav 60,28,228
G 172,228
Mvav 60,24,196
s_waitcnt vmcnt(13) lgkmcnt(8)
s_and_b32 s94, s14, 1
G 173,229
Mvaa 32,0,36
s_add_u32 s28, s28, 512
Ta 24,13,49152
Ta 26,13,50176
Mvaa 32,4,52
s_cmp_eq_u32 s28, s4
s_cselect_b32 s93, 10240, 0
s_sub_u32 s28, s28, s93
G 174,230
Mvaa 64,4,56
s_add_u32 s93, s28, 20480
s_lshl_b32 s95, s57, 1
buffer_load_dwordx4 v[188:191], v26, s[8:11], s28 offen offset:0 sc1
Mvaa 64,0,40
s_add_u32 s94, s94, s95
Ta 28,13,57344
G 175,231
Mvaa 96,0,44
)"
R"(s_lshl_b32 s54, s94, 3
Ta 30,13,58368
K 168,168,169
Mvaa 96,4,184
K 169,170,171
K 170,172,173
Mvaa 128,4,188
s_add_u32 s54, s54, s59
K 171,174,175
Mvaa 128,0,48
W 8
Ta 0,14,49152
Ta 2,14,50176
Mvaa 132,8,48
s_lshl_b32 s54, s54, 6
buffer_load_dwordx4 v[196:199], v26, s[8:11], s93 offen offset:0 sc1
v_accvgpr_write_b32 a224, v168
Mvaa 132,12,188
s_xor_b32 s55, s54, 15872
v_accvgpr_write_b32 a225, v169
Mvaa 100,12,184
Ta 4,14,57344
v_accvgpr_write_b32 a226, v170
Mvaa 100,8,44
s_add_u32 s54, s54, 20480
Ta 6,14,58368
v_accvgpr_write_b32 a227, v171
Mvaa 68,8,40
s_and_b32 s81, s14, 1
G 176,96
Mvaa 68,12,56
s_add_u32 s55, s55, 20480
Mvaa 36,12,52
G 177,97
Mvaa 36,8,36
W 8
s_and_b32 s95, s14, 2
G 178,98
Mvaa 40,16,36
s_mul_i32 s91, s81, 17164288
Ta 8,15,49152
Ta 10,15,50176
Mvaa 40,20,52
s_lshl_b32 s94, s94, 2
buffer_load_dwordx4 v[204:207], v27, s[8:11], s28 offen offset:0 sc1
G 179,99
Mvaa 72,20,56
s_mul_i32 s82, s56, 536576
s_add_u32 s94, s94, s95
G 180,100
Mvaa 72,16,40
Ta 12,15,57344
Ta 14,15,58368
Mvaa 104,16,44
s_lshl_b32 s95, s95, 4
G 181,101
G 182,102
Mvaa 104,20,184
s_add_u32 s91, s91, s82
G 183,103
Mvaa 136,20,188
s_add_u32 s55, s55, s95
s_add_u32 s91, s91, 4194304
K 176,176,177
Mvaa 136,16,48
W 8
s_lshl_b32 s49, s94, 17
Ta 16,24,49152
Mvaa 140,24,48
Ta 18,24,50176
K 177,178,179
Mvaa 140,28,188
s_lshl_b32 s95, s59, 14
buffer_load_dwordx4 v[212:215], v27, s[8:11], s93 offen offset:0 sc1
K 178,180,181
Mvaa 108,28,184
s_lshr_b32 s82, s14, 1
Ta 20,24,57344
K 179,182,183
Mvaa 108,24,44
s_add_u32 s49, s49, s95
Ta 22,24,58368
v_accvgpr_write_b32 a228, v176
Mvaa 76,24,40
s_lshl_b32 s83, s82, 14
s_xor_b32 s50, s49, 16252928
Mvaa 76,28,56
v_accvgpr_write_b32 a229, v177
Mvaa 44,28,52
s_add_u32 s91, s91, s83
s_add_u32 s49, s49, 39845888
v_accvgpr_write_b32 a230, v178
Mvaa 44,24,36
W 8
Ta 24,25,49152
v_accvgpr_write_b32 a231, v179
Mvaa 48,0,36
s_add_u32 s50, s50, 39845888
Ta 26,25,50176
G 168,232
Mvaa 48,4,52
s_lshl_b32 s81, s81, 14
Mvaa 80,4,56
s_lshr_b32 s94, s15, 3
Ta 28,25,57344
G 169,233
Mvaa 80,0,40
s_lshl_b32 s82, s82, 16
Ta 30,25,58368
G 170,234
Mvaa 112,0,44
s_lshl_b32 s94, s94, 3
G 171,235
Mvaa 112,4,184
s_add_u32 s94, s94, s14
s_or_b32 s92, s81, s82
G 172,236
Mvaa 144,4,188
Mvaa 144,0,48
W 8
s_lshl_b32 s94, s94, 4
G 173,237
Mvaa 148,8,48
s_xor_b32 s92, s92, 0x10000
Ta 0,2,32768
Ta 2,2,33792
Mvaa 148,12,188
s_add_u32 s94, s94, s59
G 174,238
G 175,239
Mvaa 116,12,184
s_mul_i32 s52, s94, 5120
Ta 4,2,40960
Ta 6,2,41984
Mvaa 116,8,44
K 168,168,169
Mvaa 84,8,40
s_lshl_b32 s95, s59, 12
K 169,170,171
Mvaa 84,12,56
K 170,172,173
Mvaa 52,12,52
s_sub_u32 s52, s52, s95
K 171,174,175
Mvaa 52,8,36
W 8
s_add_u32 s52, s52, 56623104
Ta 8,3,32768
Mvaa 56,16,36
Ta 10,3,33792
v_accvgpr_write_b32 a232, v168
Mvaa 56,20,52
s_lshl_b32 s53, s94, 3
v_accvgpr_write_b32 a233, v169
Mvaa 88,20,56
Ta 12,3,40960
v_accvgpr_write_b32 a234, v170
Mvaa 88,16,40
s_add_u32 s53, s53, 65536
Ta 14,3,41984
v_accvgpr_write_b32 a235, v171
Mvaa 120,16,44
G 176,104
v_bfe_u32 v244, v1, 4, 1
Mvaa 120,20,184
Mvaa 152,20,188
G 177,105
v_mul_u32_u24 v244, 0xaaaa, v244
Mvaa 152,16,48
W 8
G 178,106
v_add_u32 v244, 0x4444, v244
Mvaa 156,24,48
Ta 16,4,32768
Ta 18,4,33792
Mvaa 156,28,188
G 179,107
v_lshl_or_b32 v240, v244, 16, v244
Mvaa 124,28,184
G 180,108
v_mov_b32 v241, 0xff800000
Mvaa 124,24,44
Ta 20,4,40960
Ta 22,4,41984
Mvaa 92,24,40
G 181,109
v_add_u32 v242, 0x10000, v1
Mvaa 92,28,56
G 182,110
v_bfe_u32 v244, v1, 4, 4
Mvaa 60,28,52
G 183,111
v_lshrrev_b32 v245, 8, v1
Mvaa 60,24,36
s_waitcnt vmcnt(8) lgkmcnt(8)
K 176,176,177
v_lshlrev_b32 v245, 2, v245
Mvaa 32,0,60
Ta 24,5,32768
Ta 26,5,33792
Mvaa 32,4,132
K 177,178,179
v_sub_u32 v244, v244, v245
Mvaa 64,4,136
K 178,180,181
v_cmp_le_i32_e64 s[72:73], 0, v244
Mvav 64,0,184
Ta 28,5,40960
K 179,182,183
Mvav 96,0,192
Ta 30,5,41984
v_accvgpr_write_b32 a236, v176
Mvaa 96,4,140
v_cmp_le_i32_e64 s[74:75], 1, v244
v_accvgpr_write_b32 a237, v177
Mvaa 128,4,144
v_cmp_le_i32_e64 s[76:77], 2, v244
v_accvgpr_write_b32 a238, v178
Mvaa 128,0,128
W 8
v_cmp_le_i32_e64 s[78:79], 3, v244
v_accvgpr_write_b32 a239, v179
Mvaa 132,8,128
Ta 0,6,32768
Ta 2,6,33792
Mvaa 132,12,144
v_add_u32 v243, 4096, v1
G 168,240
Mvaa 100,12,140
v_add_u32 v244, 8192, v1
G 169,241
Mvav 100,8,192
Ta 4,6,40960
v_add_u32 v245, 12288, v1
Mvav 68,8,184
s_mov_b32 s18, 0x3f803f80
Ta 6,6,41984
G 170,242
Mvaa 68,12,136
s_mov_b32 s19, 0x3f003f00
G 171,243
G 172,244
Mvaa 36,12,132
Mvaa 36,8,60
W 8
v_accvgpr_write_b32 a176, s18
G 173,245
Mvaa 40,16,60
Ta 8,7,32768
Ta 10,7,33792
Mvaa 40,20,132
v_accvgpr_write_b32 a180, s19
G 174,246
Mvaa 72,20,136
G 175,247
v_mov_b32 v252, v241
Mvav 72,16,184
Ta 12,7,40960
Ta 14,7,41984
Mvav 104,16,192
K 168,168,169
v_accvgpr_write_b32 a177, s18
Mvaa 104,20,140
K 169,170,171
v_accvgpr_write_b32 a181, s19
Mvaa 136,20,144
K 170,172,173
v_mov_b32 v253, v241
Mvaa 136,16,128
W 8
K 171,174,175
v_accvgpr_write_b32 a178, s18
Mvaa 140,24,128
Ta 16,8,32768
Ta 18,8,33792
Mvaa 140,28,144
v_accvgpr_write_b32 a240, v168
v_accvgpr_write_b32 a182, s19
Mvaa 108,28,140
v_accvgpr_write_b32 a241, v169
v_mov_b32 v254, v241
Mvav 108,24,192
Ta 20,8,40960
v_accvgpr_write_b32 a242, v170
Mvav 76,24,184
Ta 22,8,41984
v_accvgpr_write_b32 a179, s18
Mvaa 76,28,136
v_accvgpr_write_b32 a243, v171
v_accvgpr_write_b32 a183, s19
Mvaa 44,28,132
G 176,112
v_mov_b32 v255, v241
Mvaa 44,24,60
W 8
G 177,113
G 178,114
Mvaa 48,0,60
Ta 24,9,32768
v_accvgpr_write_b32 a160, 0
Mvaa 48,4,132
Ta 26,9,33792
G 179,115
Mvaa 80,4,136
v_accvgpr_write_b32 a161, 0
G 180,116
Mvav 80,0,184
Ta 28,9,40960
v_accvgpr_write_b32 a162, 0
Mvav 112,0,192
Ta 30,9,41984
G 181,117
Mvaa 112,4,140
v_accvgpr_write_b32 a163, 0
G 182,118
Mvaa 144,4,144
v_accvgpr_write_b32 a164, 0
G 183,119
Mvaa 144,0,128
W 8
v_accvgpr_write_b32 a165, 0
K 176,176,177
Mvaa 148,8,128
Ta 0,2,49152
v_accvgpr_write_b32 a166, 0
Mvaa 148,12,144
Ta 2,2,50176
K 177,178,179
Mvaa 116,12,140
v_accvgpr_write_b32 a167, 0
K 178,180,181
Mvav 116,8,192
v_accvgpr_write_b32 a168, 0
K 179,182,183
Mvav 84,8,184
Ta 4,2,57344
Ta 6,2,58368
Mvaa 84,12,136
v_accvgpr_write_b32 a169, 0
v_accvgpr_write_b32 a244, v176
Mvaa 52,12,132
v_accvgpr_write_b32 a245, v177
v_accvgpr_write_b32 a170, 0
Mvaa 52,8,60
W 8
v_accvgpr_write_b32 a246, v178
v_accvgpr_write_b32 a171, 0
Mvaa 56,16,60
Ta 8,3,49152
v_accvgpr_write_b32 a247, v179
Mvaa 56,20,132
Ta 10,3,50176
v_accvgpr_write_b32 a172, 0
Mvaa 88,20,136
G 168,248
v_accvgpr_write_b32 a173, 0
Mvav 88,16,184
G 169,249
v_accvgpr_write_b32 a174, 0
Mvav 120,16,192
Ta 12,3,57344
Ta 14,3,58368
Mvaa 120,20,140
G 170,250
v_accvgpr_write_b32 a175, 0
Mvaa 152,20,144
s_mov_b32 s90, 0x5d800000
G 171,251
G 172,252
Mvaa 152,16,128
W 8
s_mov_b64 s[88:89], 0
G 173,253
Mvaa 156,24,128
s_mov_b32 s64, 0
Ta 16,4,49152
Ta 18,4,50176
Mvaa 156,28,144
s_and_b32 s18, s14, 1
G 174,254
G 175,255
Mvaa 124,28,140
s_lshl_b32 s62, s18, 14
Ta 20,4,57344
Ta 22,4,58368
Mvav 124,24,192
K 168,168,169
Mvav 92,24,184
s_lshl_b32 s20, s57, 15
K 169,170,171
Mvaa 92,28,136
s_sub_u32 s20, 507904, s20
K 170,172,173
Mvaa 60,28,132
K 171,174,175
Mvaa 60,24,60
s_waitcnt vmcnt(0) lgkmcnt(8)
s_add_u32 s20, s20, s62
Ta 24,5,49152
Mvav 32,0,200
)"
R"(s_lshl_b32 s65, s57, 1
Ta 26,5,50176
v_accvgpr_write_b32 a248, v168
Mvav 32,4,188
s_sub_u32 s65, s65, 1
v_accvgpr_write_b32 a249, v169
Mvav 64,4,196
Ta 28,5,57344
v_accvgpr_write_b32 a250, v170
Mvav 64,0,208
s_max_i32 s65, s65, 0
Ta 30,5,58368
v_accvgpr_write_b32 a251, v171
Mvav 96,0,216
s_sub_u32 s66, s60, 1
G 176,120
Mvav 96,4,204
s_cmp_lt_u32 s14, 2
s_cselect_b32 s20, s20, 0
s_cselect_b32 s66, s66, 15
s_cselect_b32 s21, 32768, 0
Mvav 128,4,212
G 177,121
Mvav 128,0,224
W 8
s_add_u32 s61, s91, s20
G 178,122
Mvav 132,8,224
s_add_u32 s62, s62, s21
Ta 0,6,49152
Ta 2,6,50176
Mvav 132,12,212
G 179,123
G 180,124
Mvav 100,12,204
s_sub_u32 s63, 16384, s21
Ta 4,6,57344
Ta 6,6,58368
Mvav 100,8,216
s_lshl_b32 s67, s60, 7
G 181,125
Mvav 68,8,208
s_add_u32 s67, s67, s56
G 182,126
Mvav 68,12,196
G 183,127
Mvav 36,12,188
s_lshl_b32 s67, s67, 12
K 176,176,177
Mvav 36,8,200
W 8
s_add_u32 s67, s67, 39845888
Ta 8,7,49152
Mvav 40,16,200
s_lshl_b32 s69, s60, 3
Ta 10,7,50176
K 177,178,179
Mvav 40,20,188
K 178,180,181
Mvav 72,20,196
s_add_u32 s69, s69, s59
Ta 12,7,57344
K 179,182,183
Mvav 72,16,208
s_lshl_b32 s69, s69, 6
Ta 14,7,58368
v_accvgpr_write_b32 a252, v176
Mvav 104,16,216
Mvav 104,20,204
s_lshl_b32 s21, s58, 3
v_accvgpr_write_b32 a253, v177
Mvav 136,20,212
s_add_u32 s69, s69, s21
v_accvgpr_write_b32 a254, v178
Mvav 136,16,224
W 8
s_add_u32 s69, s69, 20480
v_accvgpr_write_b32 a255, v179
Mvav 140,24,224
Ta 16,8,49152
Ta 18,8,50176
Mvav 140,28,212
s_mov_b32 s70, 0
Mvav 108,28,204
s_mov_b32 s37, 0
Ta 20,8,57344
Ta 22,8,58368
Mvav 108,24,216
s_sub_u32 s18, s57, 4
Mvav 76,24,208
Mvav 76,28,196
s_lshl_b32 s18, s18, 2
Mvav 44,28,188
s_add_u32 s18, s18, s58
Mvav 44,24,200
W 8
Ta 24,9,49152
Ta 26,9,50176
Mvav 48,0,200
s_mul_i32 s85, s59, 7
Mvav 48,4,188
s_add_u32 s85, s85, s18
Mvav 80,4,196
s_cmp_lt_u32 s18, 7
s_cselect_b32 s85, s85, 99
Ta 28,9,57344
Ta 30,9,58368
Mvav 80,0,208
Mvav 112,0,216
s_cmp_lt_u32 s85, 54
s_cselect_b32 s65, 2, s65
Mvav 112,4,204
s_mov_b32 s68, 0
Mvav 144,4,212
s_mov_b64 s[24:25], -1
Mvav 144,0,224
W 8
Mvav 148,8,224
s_mov_b32 s22, 0x3e0293ee
Mvav 148,12,212
s_mov_b32 s23, 0x3e0293ee
Mvav 116,12,204
Mvav 116,8,216
Mvav 84,8,208
Mvav 84,12,196
Mvav 52,12,188
Mvav 52,8,200
W 4
Mvav 56,16,200
Mvav 56,20,188
Mvav 88,20,196
Mvav 88,16,208
Mvav 120,16,216
W 0
s_barrier
Ra 128,242,0
Mvav 120,20,204
Mvav 152,20,212
Ra 132,242,1024
Mvav 152,16,224
Mvav 156,24,224
Ra 136,242,2048
Mvav 156,28,212
Mvav 124,28,204
Ra 140,242,3072
Mvav 124,24,216
Mvav 92,24,208
Mvav 92,28,196
Mvav 60,28,188
Mvav 60,24,200
.Lago%=:
s_cmp_eq_u32 s60, 1
s_cbranch_scc1 .Lpe_48%=
s_cmp_eq_u32 s60, 0
s_cbranch_scc1 .Lpd_32%=
v_pk_mul_f32 v[32:33], v[32:33], s[22:23]
v_pk_mul_f32 v[34:35], v[34:35], s[22:23]
v_pk_mul_f32 v[36:37], v[36:37], s[22:23]
v_pk_mul_f32 v[38:39], v[38:39], s[22:23]
v_pk_mul_f32 v[64:65], v[64:65], s[22:23]
v_pk_mul_f32 v[66:67], v[66:67], s[22:23]
v_pk_mul_f32 v[68:69], v[68:69], s[22:23]
v_pk_mul_f32 v[70:71], v[70:71], s[22:23]
v_pk_mul_f32 v[96:97], v[96:97], s[22:23]
v_pk_mul_f32 v[98:99], v[98:99], s[22:23]
v_pk_mul_f32 v[100:101], v[100:101], s[22:23]
v_pk_mul_f32 v[102:103], v[102:103], s[22:23]
v_pk_mul_f32 v[128:129], v[128:129], s[22:23]
v_pk_mul_f32 v[130:131], v[130:131], s[22:23]
v_pk_mul_f32 v[132:133], v[132:133], s[22:23]
v_pk_mul_f32 v[134:135], v[134:135], s[22:23]
v_pk_mul_f32 v[40:41], v[40:41], s[22:23]
v_pk_mul_f32 v[42:43], v[42:43], s[22:23]
v_pk_mul_f32 v[44:45], v[44:45], s[22:23]
v_pk_mul_f32 v[46:47], v[46:47], s[22:23]
v_pk_mul_f32 v[72:73], v[72:73], s[22:23]
v_pk_mul_f32 v[74:75], v[74:75], s[22:23]
v_pk_mul_f32 v[76:77], v[76:77], s[22:23]
v_pk_mul_f32 v[78:79], v[78:79], s[22:23]
v_pk_mul_f32 v[104:105], v[104:105], s[22:23]
v_pk_mul_f32 v[106:107], v[106:107], s[22:23]
v_pk_mul_f32 v[108:109], v[108:109], s[22:23]
v_pk_mul_f32 v[110:111], v[110:111], s[22:23]
v_pk_mul_f32 v[136:137], v[136:137], s[22:23]
v_pk_mul_f32 v[138:139], v[138:139], s[22:23]
v_pk_mul_f32 v[140:141], v[140:141], s[22:23]
v_pk_mul_f32 v[142:143], v[142:143], s[22:23]
v_pk_mul_f32 v[48:49], v[48:49], s[22:23]
v_pk_mul_f32 v[50:51], v[50:51], s[22:23]
v_pk_mul_f32 v[52:53], v[52:53], s[22:23]
v_pk_mul_f32 v[54:55], v[54:55], s[22:23]
v_pk_mul_f32 v[80:81], v[80:81], s[22:23]
v_pk_mul_f32 v[82:83], v[82:83], s[22:23]
v_pk_mul_f32 v[84:85], v[84:85], s[22:23]
v_pk_mul_f32 v[86:87], v[86:87], s[22:23]
v_pk_mul_f32 v[112:113], v[112:113], s[22:23]
v_pk_mul_f32 v[114:115], v[114:115], s[22:23]
v_pk_mul_f32 v[116:117], v[116:117], s[22:23]
v_pk_mul_f32 v[118:119], v[118:119], s[22:23]
v_pk_mul_f32 v[144:145], v[144:145], s[22:23]
v_pk_mul_f32 v[146:147], v[146:147], s[22:23]
v_pk_mul_f32 v[148:149], v[148:149], s[22:23]
v_pk_mul_f32 v[150:151], v[150:151], s[22:23]
v_pk_mul_f32 v[56:57], v[56:57], s[22:23]
v_pk_mul_f32 v[58:59], v[58:59], s[22:23]
v_pk_mul_f32 v[60:61], v[60:61], s[22:23]
v_pk_mul_f32 v[62:63], v[62:63], s[22:23]
v_pk_mul_f32 v[88:89], v[88:89], s[22:23]
v_pk_mul_f32 v[90:91], v[90:91], s[22:23]
v_pk_mul_f32 v[92:93], v[92:93], s[22:23]
v_pk_mul_f32 v[94:95], v[94:95], s[22:23]
v_pk_mul_f32 v[120:121], v[120:121], s[22:23]
v_pk_mul_f32 v[122:123], v[122:123], s[22:23]
v_pk_mul_f32 v[124:125], v[124:125], s[22:23]
v_pk_mul_f32 v[126:127], v[126:127], s[22:23]
v_pk_mul_f32 v[152:153], v[152:153], s[22:23]
v_pk_mul_f32 v[154:155], v[154:155], s[22:23]
v_pk_mul_f32 v[156:157], v[156:157], s[22:23]
v_pk_mul_f32 v[158:159], v[158:159], s[22:23]
K 32,32,33
K 33,34,35
K 34,36,37
K 35,38,39
K 40,40,41
K 41,42,43
K 42,44,45
K 43,46,47
W 0
Zvav 196,128,32
s_add_u32 s36, s29, 65536
Ra 144,242,4096
Ra 148,242,5120
Ra 152,242,6144
Ra 156,242,7168
Mvav 196,132,40
K 64,64,65
K 65,66,67
K 66,68,69
K 67,70,71
K 72,72,73
K 73,74,75
K 74,76,77
K 75,78,79
s_nop 1
Zvav 204,132,72
buffer_store_dwordx4 a[192:195], v1, s[8:11], s36 offen offset:0 sc1
K 96,96,97
K 97,98,99
K 98,100,101
Mvav 204,128,64
K 99,102,103
K 104,104,105
K 105,106,107
K 106,108,109
K 107,110,111
Zvav 212,128,96
buffer_store_dwordx4 a[200:203], v1, s[8:11], s36 offen offset:1024 sc1
K 128,128,129
K 129,130,131
K 130,132,133
Mvav 212,132,104
K 131,134,135
K 136,136,137
K 137,138,139
K 138,140,141
K 139,142,143
s_nop 1
Zvav 220,132,136
K 48,48,49
K 49,50,51
K 50,52,53
K 51,54,55
Mvav 220,128,128
K 56,56,57
K 57,58,59
K 58,60,61
K 59,62,63
Mvav 196,136,48
buffer_store_dwordx4 a[208:211], v1, s[8:11], s36 offen offset:2048 sc1
Ra 128,242,8192
Ra 132,242,9216
K 80,80,81
Mvav 196,140,56
K 81,82,83
K 82,84,85
K 83,86,87
K 88,88,89
K 89,90,91
K 90,92,93
K 91,94,95
s_nop 1
Mvav 204,140,88
buffer_store_dwordx4 a[216:219], v1, s[8:11], s36 offen offset:3072 sc1
s_add_u32 s36, s29, 69632
K 112,112,113
K 113,114,115
K 114,116,117
Mvav 204,136,80
K 115,118,119
K 120,120,121
K 121,122,123
K 122,124,125
K 123,126,127
Mvav 212,136,112
K 144,144,145
K 145,146,147
K 146,148,149
K 147,150,151
Mvav 212,140,120
K 152,152,153
K 153,154,155
K 154,156,157
K 155,158,159
s_nop 1
Mvav 220,140,152
buffer_store_dwordx4 a[224:227], v1, s[8:11], s36 offen offset:0 sc1
v_accvgpr_write_b32 a0, v32
v_accvgpr_write_b32 a1, v33
v_accvgpr_write_b32 a2, v34
Mvav 220,136,144
W 2
Ra 136,242,10240
v_accvgpr_write_b32 a3, v35
v_accvgpr_write_b32 a4, v40
Zvav 36,144,32
Ra 140,242,11264
buffer_store_dwordx4 a[232:235], v1, s[8:11], s36 offen offset:1024 sc1
v_accvgpr_write_b32 a5, v41
Mvav 36,148,40
v_accvgpr_write_b32 a6, v42
v_accvgpr_write_b32 a7, v43
v_max3_f32 v4, v196, v197, v198
v_max3_f32 v4, v4, v199, v252
Zvav 68,148,72
v_max3_f32 v5, v204, v205, v206
v_max3_f32 v5, v5, v207, v253
v_max3_f32 v6, v212, v213, v214
v_max3_f32 v6, v6, v215, v254
Mvav 68,144,64
v_max3_f32 v7, v220, v221, v222
v_max3_f32 v7, v7, v223, v255
v_mov_b32 v8, v4
v_mov_b32 v9, v5
Zvav 100,144,96
v_mov_b32 v10, v6
v_mov_b32 v11, v7
v_permlane32_swap_b32 v4, v8
v_accvgpr_write_b32 a8, v48
Mvav 100,148,104
v_permlane32_swap_b32 v5, v9
v_permlane32_swap_b32 v6, v10
v_permlane32_swap_b32 v7, v11
v_max_f32 v4, v4, v8
Zvav 132,148,136
v_max_f32 v5, v5, v9
v_max_f32 v6, v6, v10
v_max_f32 v7, v7, v11
v_mov_b32 v8, v4
Mvav 132,144,128
v_mov_b32 v9, v5
v_mov_b32 v10, v6
v_mov_b32 v11, v7
v_accvgpr_write_b32 a9, v49
Mvav 36,152,48
v_permlane16_swap_b32 v4, v8
v_permlane16_swap_b32 v5, v9
v_permlane16_swap_b32 v6, v10
v_permlane16_swap_b32 v7, v11
Mvav 36,156,56
v_max_f32 v4, v4, v8
v_max_f32 v5, v5, v9
v_max_f32 v6, v6, v10
v_max_f32 v7, v7, v11
Mvav 68,156,88
v_sub_f32 v4, 0, v4
v_sub_f32 v5, 0, v5
v_sub_f32 v6, 0, v6
v_sub_f32 v7, 0, v7
Mvav 68,152,80
v_mov_b32 v224, v4
v_mov_b32 v225, v4
v_mov_b32 v226, v4
v_mov_b32 v227, v4
Mvav 100,152,112
v_mov_b32 v228, v5
v_mov_b32 v229, v5
v_mov_b32 v230, v5
v_mov_b32 v231, v5
Mvav 100,156,120
v_mov_b32 v232, v6
v_mov_b32 v233, v6
v_mov_b32 v234, v6
v_mov_b32 v235, v6
Mvav 132,156,152
v_mov_b32 v236, v7
v_mov_b32 v237, v7
v_mov_b32 v238, v7
v_mov_b32 v239, v7
Mvav 132,152,144
W 0
)"
R"(buffer_store_dwordx4 a[240:243], v1, s[8:11], s36 offen offset:2048 sc1
v_accvgpr_write_b32 a10, v50
v_accvgpr_write_b32 a11, v51
v_accvgpr_write_b32 a12, v56
Cvav 160,128,32,224
s_and_b32 s80, s14, 1
v_accvgpr_write_b32 a13, v57
v_accvgpr_write_b32 a14, v58
v_accvgpr_write_b32 a15, v59
v_accvgpr_write_b32 a16, v64
Mvav 160,132,40
buffer_store_dwordx4 a[248:251], v1, s[8:11], s36 offen offset:3072 sc1
s_add_u32 s36, s29, 393216
Ra 144,242,12288
Ra 148,242,13312
v_accvgpr_write_b32 a17, v65
Cvav 164,132,72,228
s_lshl_b32 s80, s80, 14
v_accvgpr_write_b32 a18, v66
v_accvgpr_write_b32 a19, v67
v_accvgpr_write_b32 a20, v72
v_accvgpr_write_b32 a21, v73
Mvav 164,128,64
s_add_u32 s80, s80, 98304
buffer_store_dwordx4 a[196:199], v1, s[8:11], s36 offen offset:0 sc1
v_accvgpr_write_b32 a22, v74
v_accvgpr_write_b32 a23, v75
v_accvgpr_write_b32 a24, v80
Cvav 168,128,96,232
v_accvgpr_write_b32 a25, v81
v_accvgpr_write_b32 a26, v82
A 196,196,224
v_accvgpr_write_b32 a27, v83
Mvav 168,132,104
buffer_store_dwordx4 a[204:207], v1, s[8:11], s36 offen offset:1024 sc1
A 197,197,224
A 198,198,224
v_accvgpr_write_b32 a28, v88
Cvav 172,132,136,236
Ra 152,242,14336
Ra 156,242,15360
A 199,199,224
v_accvgpr_write_b32 a29, v89
Mvav 172,128,128
buffer_store_dwordx4 a[212:215], v1, s[8:11], s36 offen offset:2048 sc1
X 196
v_accvgpr_write_b32 a30, v90
Mvav 160,136,48
X 197
v_accvgpr_write_b32 a31, v91
v_accvgpr_write_b32 a32, v96
Mvav 160,140,56
X 198
K 192,196,197
v_accvgpr_write_b32 a33, v97
Mvav 164,140,88
X 199
A 204,204,228
v_accvgpr_write_b32 a34, v98
Mvav 164,136,80
buffer_store_dwordx4 a[220:223], v1, s[8:11], s36 offen offset:3072 sc1
s_add_u32 s36, s29, 397312
K 193,198,199
v_accvgpr_write_b32 a35, v99
A 205,205,228
Mvav 168,136,112
A 206,206,228
v_accvgpr_write_b32 a36, v104
v_add_u32 v8, s80, v1
A 207,207,228
Mvav 168,140,120
X 204
v_accvgpr_write_b32 a37, v105
v_accvgpr_write_b32 a38, v106
Mvav 172,140,152
X 205
X 206
Mvav 172,136,144
W 0
buffer_store_dwordx4 a[228:231], v1, s[8:11], s36 offen offset:0 sc1
v_accvgpr_write_b32 a39, v107
v_accvgpr_write_b32 a40, v112
K 200,204,205
Cvav 176,144,32,224
Ra 128,242,24576
X 207
v_accvgpr_write_b32 a41, v113
Mvav 176,148,40
Ra 132,242,25600
A 212,212,232
v_accvgpr_write_b32 a42, v114
K 201,206,207
Cvav 180,148,72,228
A 213,213,232
v_accvgpr_write_b32 a43, v115
A 214,214,232
A 215,215,232
Mvav 180,144,64
buffer_store_dwordx4 a[236:239], v1, s[8:11], s36 offen offset:1024 sc1
v_accvgpr_write_b32 a44, v120
X 212
Cvav 184,144,96,232
v_accvgpr_write_b32 a45, v121
X 213
v_accvgpr_write_b32 a46, v122
Mvav 184,148,104
X 214
v_accvgpr_write_b32 a47, v123
K 208,212,213
Cvav 188,148,136,236
buffer_store_dwordx4 a[244:247], v1, s[8:11], s36 offen offset:2048 sc1
v_accvgpr_write_b32 a48, v128
X 215
Mvav 188,144,128
v_accvgpr_write_b32 a49, v129
A 220,220,236
v_accvgpr_write_b32 a50, v130
K 209,214,215
Mvav 176,152,48
A 221,221,236
v_accvgpr_write_b32 a51, v131
A 222,222,236
A 223,223,236
Mvav 176,156,56
Ra 136,242,26624
v_accvgpr_write_b32 a52, v136
X 220
Mvav 180,156,88
Ra 140,242,27648
v_accvgpr_write_b32 a53, v137
X 221
Mvav 180,152,80
buffer_store_dwordx4 a[252:255], v1, s[8:11], s36 offen offset:3072 sc1
v_accvgpr_write_b32 a54, v138
X 222
Mvav 184,152,112
v_accvgpr_write_b32 a55, v139
v_accvgpr_write_b32 a56, v144
v_accvgpr_write_b32 a57, v145
v_accvgpr_write_b32 a58, v146
v_accvgpr_write_b32 a59, v147
v_accvgpr_write_b32 a60, v152
v_accvgpr_write_b32 a61, v153
v_accvgpr_write_b32 a62, v154
v_accvgpr_write_b32 a63, v155
Mvav 184,156,120
s_barrier
K 216,220,221
X 223
A 36,36,224
Mvav 188,156,152
K 217,222,223
A 37,37,224
A 38,38,224
A 39,39,224
X 36
X 37
X 38
K 194,36,37
X 39
A 68,68,228
K 195,38,39
A 69,69,228
A 70,70,228
A 71,71,228
X 68
X 69
X 70
K 202,68,69
X 71
A 100,100,232
K 203,70,71
A 101,101,232
A 102,102,232
A 103,103,232
X 100
X 101
X 102
K 210,100,101
X 103
A 132,132,236
K 211,102,103
A 133,133,236
A 134,134,236
A 135,135,236
X 132
X 133
X 134
K 218,132,133
X 135
s_nop 0
K 219,134,135
Mvav 188,152,144
W 0
Ra 144,242,28672
Ra 148,242,29696
Ra 152,242,30720
Zvav 32,128,192
Ra 156,242,31744
Zvav 64,128,200
Zvav 96,128,208
s_and_b32 s80, s14, 1
s_xor_b32 s62, s62, 0x10000
Zvav 128,128,216
s_lshl_b32 s80, s80, 14
s_add_u32 s80, s80, 131072
v_add_u32 v23, 61440, v21
Zvav 132,132,216
v_add_u32 v12, s80, v1
Ra 64,12,8192
Ra 68,12,9216
v_add_u32 v24, 61440, v22
Zvav 100,132,208
Ra 72,12,10240
Ra 76,12,11264
Ra 80,12,12288
v_add_u32 v25, 122880, v21
Zvav 68,132,200
Ra 84,12,13312
Ra 88,12,14336
Ra 92,12,15360
v_add_u32 v26, 122880, v22
Zvav 36,132,192
Ra 96,12,0
Ra 100,12,1024
Ra 104,12,2048
Zvav 40,136,192
Ra 108,12,3072
Ra 112,12,4096
X 172
Zvav 72,136,200
Ra 116,12,5120
Ra 120,12,6144
X 173
Zvav 104,136,208
Ra 124,12,7168
X 174
K 220,172,173
Zvav 136,136,216
X 175
Zvav 140,140,216
X 168
K 221,174,175
Zvav 108,140,208
X 169
X 170
Zvav 76,140,200
s_waitcnt vmcnt(0) lgkmcnt(0)
s_barrier
v_xor_b32 v242, 0x10000, v242
ds_read_b128 v[248:251], v8
Zvav 44,140,192
s_add_u32 s80, s71, 128
v_mov_b64 v[28:29], s[12:13]
s_mov_b64 exec, 1
buffer_store_dwordx2 v[28:29], off, s[8:11], s80 sc1
s_mov_b64 exec, -1
Ra 128,242,0
K 212,168,169
X 171
Zvav 48,144,192
X 164
K 213,170,171
Zvav 80,144,200
X 165
X 166
Zvav 112,144,208
Ra 132,242,1024
K 204,164,165
X 167
Zvav 144,144,216
X 160
Zvav 148,148,216
K 205,166,167
X 161
Zvav 116,148,208
Ra 136,242,2048
X 162
Zvav 84,148,200
W 3
K 196,160,161
X 163
Zvav 52,148,192
X 176
K 197,162,163
Zvav 56,152,192
Ra 140,242,3072
X 177
Zvav 88,152,200
X 178
K 198,176,177
Zvav 120,152,208
X 179
X 180
Zvav 152,152,216
K 199,178,179
Zvav 156,156,216
X 181
X 182
Zvav 124,156,208
K 206,180,181
X 183
Zvav 92,156,200
K 207,182,183
Zvav 60,156,192
.Ll0t_46%=:
s_cmp_eq_u32 s64, s65
s_cbranch_scc1 .Ll1t_35%=
.Ll0_33%=:
W 0
Cvaa 160,128,0,224
Ra 144,242,4096
Ra 148,242,5120
Mvaa 160,132,4
s_xor_b32 s62, s62, 0x10000
Ra 152,242,6144
Cvaa 164,132,20,228
X 184
Mvaa 164,128,16
X 185
Cvaa 168,128,32,232
X 186
Mvaa 168,132,36
Ra 156,242,7168
K 214,184,185
Cvaa 172,132,52,236
X 187
Mvaa 172,128,48
X 188
Mvaa 160,136,8
Ra 128,242,16384
Mvaa 160,140,12
K 215,186,187
Mvaa 164,140,28
X 189
Mvaa 164,136,24
X 190
Mvaa 168,136,40
Ra 132,242,17408
Mvaa 168,140,44
K 222,188,189
Mvaa 172,140,60
X 191
Mvaa 172,136,56
K 223,190,191
v_smfmac_f32_16x16x64_bf16 a[160:163], a[176:179], v[192:199], v240
Ra 136,242,18432
v_smfmac_f32_16x16x64_bf16 a[164:167], a[176:179], v[200:207], v240
X 160
v_smfmac_f32_16x16x64_bf16 a[168:171], a[176:179], v[208:215], v240
X 161
v_smfmac_f32_16x16x64_bf16 a[172:175], a[176:179], v[216:223], v240
W 3
X 162
Cvaa 176,144,0,224
Ra 140,242,19456
Mvaa 176,148,4
K 192,160,161
Cvaa 180,148,20,228
X 163
Mvaa 180,144,16
X 164
Cvaa 184,144,32,232
Mvaa 184,148,36
K 193,162,163
Cvaa 188,148,52,236
X 165
Mvaa 188,144,48
X 166
Mvaa 176,152,8
Ra 144,242,20480
Ra 148,242,21504
Mvaa 176,156,12
K 200,164,165
Mvaa 180,156,28
X 167
Mvaa 180,152,24
X 168
Mvaa 184,152,40
Mvaa 184,156,44
K 201,166,167
Mvaa 188,156,60
X 169
Mvaa 188,152,56
W 2
Ra 152,242,22528
Mvav 32,128,196
X 170
Mvav 64,128,204
Ra 156,242,23552
K 208,168,169
Mvav 96,128,212
X 171
Mvav 128,128,220
Mvav 132,132,220
X 172
Mvav 100,132,212
s_add_u32 s64, s64, 1
Ra 128,242,8192
K 209,170,171
)"
R"(Mvav 68,132,204
X 173
Mvav 36,132,196
Mvav 40,136,196
X 174
Mvav 72,136,204
Ra 132,242,9216
K 216,172,173
Mvav 104,136,212
X 175
Mvav 136,136,220
Mvav 140,140,220
X 176
Mvav 108,140,212
Ra 136,242,10240
K 217,174,175
Mvav 76,140,204
X 177
Mvav 44,140,196
W 3
Mvav 48,144,196
X 178
Mvav 80,144,204
Ra 140,242,11264
Mvav 112,144,212
K 194,176,177
Mvav 144,144,220
X 179
Mvav 148,148,220
X 180
Mvav 116,148,212
Ra 144,242,12288
Mvav 84,148,204
K 195,178,179
Mvav 52,148,196
X 181
Mvav 56,152,196
X 182
Mvav 88,152,204
Ra 148,242,13312
Mvav 120,152,212
K 202,180,181
Mvav 152,152,220
X 183
Mvav 156,156,220
X 184
Mvav 124,156,212
Ra 152,242,14336
Mvav 92,156,204
K 203,182,183
Mvav 60,156,196
W 3
X 185
Cvaa 160,128,0,224
X 186
Mvaa 160,132,4
Ra 156,242,15360
Cvaa 164,132,20,228
K 210,184,185
Mvaa 164,128,16
X 187
Cvaa 168,128,32,232
X 188
Mvaa 168,132,36
Cvaa 172,132,52,236
K 211,186,187
Mvaa 172,128,48
X 189
Mvaa 160,136,8
Ra 128,242,24576
Ra 132,242,25600
Mvaa 160,140,12
X 190
Mvaa 164,140,28
K 218,188,189
Mvaa 164,136,24
X 191
Mvaa 168,136,40
Mvaa 168,140,44
Mvaa 172,140,60
X 160
Mvaa 172,136,56
W 2
Ra 136,242,26624
K 219,190,191
Cvaa 176,144,0,224
X 161
Mvaa 176,148,4
Ra 140,242,27648
Cvaa 180,148,20,228
X 162
Mvaa 180,144,16
K 196,160,161
Cvaa 184,144,32,232
X 163
Mvaa 184,148,36
X 164
Cvaa 188,148,52,236
K 197,162,163
Mvaa 188,144,48
X 165
Mvaa 176,152,8
Ra 144,242,28672
Ra 148,242,29696
Mvaa 176,156,12
X 166
Mvaa 180,156,28
K 204,164,165
Mvaa 180,152,24
X 167
Mvaa 184,152,40
Mvaa 184,156,44
X 168
Mvaa 188,156,60
Mvaa 188,152,56
W 2
Ra 152,242,30720
K 205,166,167
Mvav 32,128,192
X 169
Mvav 64,128,200
Ra 156,242,31744
Mvav 96,128,208
X 170
Mvav 128,128,216
K 212,168,169
Mvav 132,132,216
X 171
Mvav 100,132,208
X 172
Mvav 68,132,200
Mvav 36,132,192
K 213,170,171
Mvav 40,136,192
X 173
Mvav 72,136,200
X 174
Mvav 104,136,208
Mvav 136,136,216
K 220,172,173
Mvav 140,140,216
X 175
Mvav 108,140,208
X 176
Mvav 76,140,200
W 0
s_barrier
v_xor_b32 v242, 0x10000, v242
Mvav 44,140,192
Ra 128,242,0
K 221,174,175
Mvav 48,144,192
X 177
Mvav 80,144,200
X 178
Mvav 112,144,208
Ra 132,242,1024
Mvav 144,144,216
K 198,176,177
Mvav 148,148,216
X 179
Mvav 116,148,208
X 180
Mvav 84,148,200
Ra 136,242,2048
Mvav 52,148,192
K 199,178,179
Mvav 56,152,192
X 181
Mvav 88,152,200
Ra 140,242,3072
Mvav 120,152,208
X 182
Mvav 152,152,216
K 206,180,181
Mvav 156,156,216
X 183
Mvav 124,156,208
Mvav 92,156,200
K 207,182,183
Mvav 60,156,192
s_cmp_lg_u32 s64, s65
s_cbranch_scc1 .Ll0_33%=
s_cmp_lt_u32 s85, 54
s_cbranch_scc1 .Lmoe_44%=
.Ll1t_35%=:
s_cmp_eq_u32 s64, s66
s_cbranch_scc1 .Lsp1_36%=
.Ll1_34%=:
W 0
Cvaa 160,128,0,224
Ra 144,242,4096
Ra 148,242,5120
Mvaa 160,132,4
s_add_u32 m0, s62, 0
Ra 152,242,6144
Cvaa 164,132,20,228
X 184
Mvaa 164,128,16
X 185
Cvaa 168,128,32,232
X 186
Mvaa 168,132,36
Ra 156,242,7168
K 214,184,185
Cvaa 172,132,52,236
Dc 1,8,61,0
Mvaa 172,128,48
X 187
Mvaa 160,136,8
X 188
Mvaa 160,140,12
Ra 128,242,16384
K 215,186,187
Mvaa 164,140,28
Dc 1,8,61,1024
Mvaa 164,136,24
X 189
Mvaa 168,136,40
X 190
Mvaa 168,140,44
Ra 132,242,17408
K 222,188,189
Mvaa 172,140,60
Dc 1,8,61,2048
Mvaa 172,136,56
X 191
v_smfmac_f32_16x16x64_bf16 a[160:163], a[176:179], v[192:199], v240
Ra 136,242,18432
K 223,190,191
v_smfmac_f32_16x16x64_bf16 a[164:167], a[176:179], v[200:207], v240
X 160
v_smfmac_f32_16x16x64_bf16 a[168:171], a[176:179], v[208:215], v240
Dc 1,8,61,3072
v_smfmac_f32_16x16x64_bf16 a[172:175], a[176:179], v[216:223], v240
W 3
X 161
Cvaa 176,144,0,224
s_add_u32 m0, s62, 4096
X 162
Mvaa 176,148,4
Ra 140,242,19456
K 192,160,161
Cvaa 180,148,20,228
X 163
Mvaa 180,144,16
Dc 243,8,61,0
Cvaa 184,144,32,232
X 164
Mvaa 184,148,36
K 193,162,163
Cvaa 188,148,52,236
X 165
Mvaa 188,144,48
Dc 243,8,61,1024
Ra 144,242,20480
Mvaa 176,152,8
X 166
Mvaa 176,156,12
Ra 148,242,21504
K 200,164,165
Mvaa 180,156,28
X 167
Mvaa 180,152,24
Dc 243,8,61,2048
Mvaa 184,152,40
X 168
Mvaa 184,156,44
K 201,166,167
Mvaa 188,156,60
X 169
Mvaa 188,152,56
W 2
Dc 243,8,61,3072
Ra 152,242,22528
Mvav 32,128,196
s_add_u32 m0, s62, 8192
X 170
Mvav 64,128,204
Ra 156,242,23552
K 208,168,169
Mvav 96,128,212
X 171
Mvav 128,128,220
Dc 244,8,61,0
Mvav 132,132,220
X 172
Mvav 100,132,212
Ra 128,242,8192
K 209,170,171
Mvav 68,132,204
X 173
Mvav 36,132,196
Dc 244,8,61,1024
Mvav 40,136,196
X 174
Mvav 72,136,204
Ra 132,242,9216
K 216,172,173
Mvav 104,136,212
X 175
Mvav 136,136,220
Dc 244,8,61,2048
Mvav 140,140,220
X 176
Mvav 108,140,212
Ra 136,242,10240
K 217,174,175
Mvav 76,140,204
X 177
Mvav 44,140,196
W 3
Dc 244,8,61,3072
Mvav 48,144,196
X 178
Mvav 80,144,204
s_add_u32 m0, s62, 12288
Ra 140,242,11264
Mvav 112,144,212
K 194,176,177
Mvav 144,144,220
X 179
Mvav 148,148,220
X 180
Mvav 116,148,212
Dc 245,8,61,0
Ra 144,242,12288
Mvav 84,148,204
K 195,178,179
Mvav 52,148,196
X 181
Mvav 56,152,196
X 182
Mvav 88,152,204
Dc 245,8,61,1024
Ra 148,242,13312
Mvav 120,152,212
K 202,180,181
Mvav 152,152,220
X 183
Mvav 156,156,220
X 184
Mvav 124,156,212
Dc 245,8,61,2048
Ra 152,242,14336
Mvav 92,156,204
K 203,182,183
Mvav 60,156,196
W 3
X 185
Cvaa 160,128,0,224
X 186
Mvaa 160,132,4
Dc 245,8,61,3072
Ra 156,242,15360
Cvaa 164,132,20,228
s_add_u32 s61, s61, s63
K 210,184,185
Mvaa 164,128,16
X 187
Cvaa 168,128,32,232
X 188
Mvaa 168,132,36
Cvaa 172,132,52,236
s_xor_b32 s62, s62, 0x10000
K 211,186,187
Mvaa 172,128,48
X 189
Mvaa 160,136,8
Ra 128,242,24576
Ra 132,242,25600
Mvaa 160,140,12
s_add_u32 s64, s64, 1
X 190
Mvaa 164,140,28
K 218,188,189
Mvaa 164,136,24
X 191
Mvaa 168,136,40
Mvaa 168,140,44
X 160
Mvaa 172,140,60
K 219,190,191
Mvaa 172,136,56
W 2
X 161
Cvaa 176,144,0,224
Ra 136,242,26624
Ra 140,242,27648
Mvaa 176,148,4
X 162
Cvaa 180,148,20,228
K 196,160,161
Mvaa 180,144,16
X 163
Cvaa 184,144,32,232
Mvaa 184,148,36
X 164
Cvaa 188,148,52,236
K 197,162,163
)"
R"(Mvaa 188,144,48
X 165
Mvaa 176,152,8
Ra 144,242,28672
Ra 148,242,29696
Mvaa 176,156,12
X 166
Mvaa 180,156,28
K 204,164,165
Mvaa 180,152,24
X 167
Mvaa 184,152,40
Mvaa 184,156,44
X 168
Mvaa 188,156,60
Mvaa 188,152,56
W 2
Ra 152,242,30720
K 205,166,167
Mvav 32,128,192
X 169
Mvav 64,128,200
Ra 156,242,31744
Mvav 96,128,208
X 170
Mvav 128,128,216
K 212,168,169
Mvav 132,132,216
X 171
Mvav 100,132,208
X 172
Mvav 68,132,200
Mvav 36,132,192
K 213,170,171
Mvav 40,136,192
X 173
Mvav 72,136,200
X 174
Mvav 104,136,208
Mvav 136,136,216
K 220,172,173
Mvav 140,140,216
X 175
Mvav 108,140,208
X 176
Mvav 76,140,200
s_waitcnt vmcnt(0) lgkmcnt(0)
s_barrier
v_xor_b32 v242, 0x10000, v242
Mvav 44,140,192
Ra 128,242,0
K 221,174,175
Mvav 48,144,192
X 177
Mvav 80,144,200
X 178
Mvav 112,144,208
Ra 132,242,1024
Mvav 144,144,216
K 198,176,177
Mvav 148,148,216
X 179
Mvav 116,148,208
X 180
Mvav 84,148,200
Ra 136,242,2048
Mvav 52,148,192
K 199,178,179
Mvav 56,152,192
X 181
Mvav 88,152,200
Ra 140,242,3072
Mvav 120,152,208
X 182
Mvav 152,152,216
K 206,180,181
Mvav 156,156,216
X 183
Mvav 124,156,208
Mvav 92,156,200
K 207,182,183
Mvav 60,156,192
s_cmp_lg_u32 s64, s66
s_cbranch_scc1 .Ll1_34%=
s_cmp_ge_u32 s14, 2
s_cbranch_scc1 .Lf1r_47%=
.Lsp1_36%=:
W 0
Cvaa 160,128,0,224
Ra 144,242,4096
Ra 148,242,5120
Mvaa 160,132,4
s_add_u32 m0, s62, 0
Ra 152,242,6144
Cvaa 164,132,20,228
X 184
Mvaa 164,128,16
X 185
Cvaa 168,128,32,232
X 186
Mvaa 168,132,36
Ra 156,242,7168
K 214,184,185
Cvaa 172,132,52,236
Dc 1,8,61,0
Mvaa 172,128,48
X 187
Mvaa 160,136,8
X 188
Mvaa 160,140,12
Ra 128,242,16384
K 215,186,187
Mvaa 164,140,28
Dc 1,8,61,1024
Ra 132,242,17408
Mvaa 164,136,24
X 189
Mvaa 168,136,40
X 190
Mvaa 168,140,44
Dc 1,8,61,2048
K 222,188,189
Mvaa 172,140,60
X 191
Mvaa 172,136,56
Ra 136,242,18432
K 223,190,191
v_smfmac_f32_16x16x64_bf16 a[160:163], a[176:179], v[192:199], v240
Dc 1,8,61,3072
Ra 140,242,19456
v_smfmac_f32_16x16x64_bf16 a[164:167], a[176:179], v[200:207], v240
s_add_u32 m0, s62, 4096
v_cndmask_b32_e64 v160, v241, v160, s[72:73]
v_cndmask_b32_e64 v161, v241, v161, s[74:75]
v_smfmac_f32_16x16x64_bf16 a[168:171], a[176:179], v[208:215], v240
v_cndmask_b32_e64 v162, v241, v162, s[76:77]
v_cndmask_b32_e64 v163, v241, v163, s[78:79]
v_smfmac_f32_16x16x64_bf16 a[172:175], a[176:179], v[216:223], v240
W 4
X 160
Cvaa 180,144,16,228
X 161
Mvaa 180,148,20
Dc 243,8,61,0
Cvaa 184,148,36,232
X 162
Mvaa 184,144,32
K 192,160,161
Cvaa 188,144,48,236
X 163
Mvaa 188,148,52
Dc 243,8,61,1024
Ra 144,242,20480
Mvaa 180,152,24
X 164
Mvaa 180,156,28
Ra 148,242,21504
K 193,162,163
Mvaa 184,156,44
Dc 243,8,61,2048
Mvaa 184,152,40
X 165
Mvaa 188,152,56
X 166
Mvaa 188,156,60
W 2
Dc 243,8,61,3072
K 200,164,165
Mvav 32,128,196
s_add_u32 m0, s62, 8192
X 167
Mvav 64,128,204
Ra 152,242,22528
Ra 156,242,23552
Mvav 96,128,212
X 168
Mvav 128,128,220
Dc 244,8,61,0
K 201,166,167
Mvav 132,132,220
X 169
Mvav 100,132,212
X 170
Mvav 68,132,204
Ra 128,242,8192
K 208,168,169
Mvav 36,132,196
X 171
Mvav 40,136,196
Dc 244,8,61,1024
Ra 132,242,9216
Mvav 72,136,204
X 172
Mvav 104,136,212
K 209,170,171
Mvav 136,136,220
X 173
Mvav 140,140,220
Dc 244,8,61,2048
Ra 136,242,10240
Mvav 108,140,212
X 174
Mvav 76,140,204
K 216,172,173
Mvav 44,140,196
W 3
Dc 244,8,61,3072
Ra 140,242,11264
Mvav 48,144,196
s_add_u32 m0, s62, 12288
X 175
Mvav 80,144,204
v_mov_b32 v194, 0
K 217,174,175
Mvav 112,144,212
v_mov_b32 v195, 0
v_cndmask_b32_e64 v180, v241, v180, s[72:73]
Mvav 144,144,220
v_cndmask_b32_e64 v181, v241, v181, s[74:75]
v_cndmask_b32_e64 v182, v241, v182, s[76:77]
Mvav 148,148,220
Dc 245,8,61,0
v_cndmask_b32_e64 v183, v241, v183, s[78:79]
Mvav 116,148,212
X 180
Mvav 84,148,204
Ra 144,242,12288
Mvav 52,148,196
X 181
Mvav 56,152,196
Dc 245,8,61,1024
Ra 148,242,13312
Mvav 88,152,204
X 182
Mvav 120,152,212
K 202,180,181
Mvav 152,152,220
X 183
Mvav 156,156,220
Dc 245,8,61,2048
Ra 152,242,14336
Mvav 124,156,212
X 184
Mvav 92,156,204
K 203,182,183
Mvav 60,156,196
W 3
Dc 245,8,61,3072
Ra 156,242,15360
Cvaa 168,128,32,232
s_add_u32 s61, s61, s63
X 185
Mvaa 168,132,36
s_xor_b32 s62, s62, 0x10000
X 186
Cvaa 172,132,52,236
K 210,184,185
Mvaa 172,128,48
s_add_u32 s64, s64, 1
X 187
Mvaa 168,136,40
X 188
Mvaa 168,140,44
Ra 128,242,24576
K 211,186,187
Mvaa 172,140,60
Ra 132,242,25600
Mvaa 172,136,56
Ra 136,242,26624
Ra 140,242,27648
X 189
X 190
K 218,188,189
X 191
v_mov_b32 v196, 0
K 219,190,191
W 4
Cvaa 188,144,48,236
Ra 144,242,28672
Mvaa 188,148,52
v_mov_b32 v197, 0
v_mov_b32 v204, 0
Mvaa 188,152,56
Ra 148,242,29696
v_mov_b32 v205, 0
Mvaa 188,156,60
W 2
Ra 152,242,30720
v_cndmask_b32_e64 v168, v241, v168, s[72:73]
Mvav 32,128,192
v_cndmask_b32_e64 v169, v241, v169, s[74:75]
v_cndmask_b32_e64 v170, v241, v170, s[76:77]
Mvav 64,128,200
Ra 156,242,31744
v_cndmask_b32_e64 v171, v241, v171, s[78:79]
Mvav 96,128,208
X 168
Mvav 128,128,216
X 169
Mvav 132,132,216
X 170
Mvav 100,132,208
v_mov_b32 v224, v250
Mvav 68,132,200
K 212,168,169
v_mov_b32 v225, v250
Mvav 36,132,192
X 171
Mvav 40,136,192
v_mov_b32 v226, v250
Mvav 72,136,200
X 172
Mvav 104,136,208
v_mov_b32 v227, v250
K 213,170,171
Mvav 136,136,216
X 173
Mvav 140,140,216
v_mov_b32 v228, v251
v_mov_b32 v229, v251
Mvav 108,140,208
X 174
Mvav 76,140,200
K 220,172,173
v_mov_b32 v230, v251
Mvav 44,140,192
W 0
X 175
Mvav 48,144,192
v_mov_b32 v231, v251
v_mov_b32 v198, 0
Mvav 80,144,200
K 221,174,175
v_mov_b32 v232, v248
Mvav 112,144,208
v_mov_b32 v199, 0
v_mov_b32 v206, 0
Mvav 144,144,216
v_mov_b32 v233, v248
v_mov_b32 v207, 0
Mvav 148,148,216
v_mov_b32 v214, 0
v_mov_b32 v234, v248
Mvav 116,148,208
v_mov_b32 v215, 0
v_cndmask_b32_e64 v188, v241, v188, s[72:73]
Mvav 84,148,200
v_mov_b32 v235, v248
v_cndmask_b32_e64 v189, v241, v189, s[74:75]
Mvav 52,148,192
v_cndmask_b32_e64 v190, v241, v190, s[76:77]
v_mov_b32 v236, v249
Mvav 56,152,192
v_cndmask_b32_e64 v191, v241, v191, s[78:79]
v_mov_b32 v237, v249
Mvav 88,152,200
X 188
Mvav 120,152,208
X 189
Mvav 152,152,216
v_mov_b32 v238, v249
v_mov_b32 v239, v249
Mvav 156,156,216
X 190
Mvav 124,156,208
s_waitcnt vmcnt(0)
s_barrier
v_xor_b32 v242, 0x10000, v242
Mvav 92,156,200
Ra 128,242,40960
K 222,188,189
Mvav 60,156,192
Ra 132,242,41984
Ra 136,242,43008
Ra 140,242,44032
X 191
s_nop 0
K 223,190,191
s_cmp_eq_u32 s64, 15
s_cbranch_scc1 .Lfs_42%=
.Lsp2_37%=:
v_smfmac_f32_16x16x64_bf16 a[160:163], a[176:179], v[192:199], v240
s_add_u32 m0, s62, 0
Ra 144,242,45056
Ra 148,242,46080
Ra 152,242,47104
Ra 156,242,48128
v_smfmac_f32_16x16x64_bf16 a[164:167], a[176:179], v[200:207], v240
v_smfmac_f32_16x16x64_bf16 a[168:171], a[176:179], v[208:215], v240
v_smfmac_f32_16x16x64_bf16 a[172:175], a[176:179], v[216:223], v240
W 4
Dc 1,8,61,0
Cvaa 168,128,96,232
v_mov_b32 v192, 0
G 12,160
Mvaa 168,132,100
Dc 1,8,61,1024
G 4,161
A 12,12,4
v_cmp_nlt_f32_e64 s[86:87], v12, s90
)"
R"(v_mov_b32 v193, 0
Cvaa 172,132,116,236
v_rcp_f32 v12, v12
s_or_b64 s[88:89], s[88:89], s[86:87]
v_accvgpr_write_b32 a160, 0
v_mov_b32 v200, 0
v_accvgpr_write_b32 a161, 0
v_accvgpr_write_b32 a162, 0
Mvaa 172,128,112
Dc 1,8,61,2048
v_accvgpr_write_b32 a163, 0
G 13,164
v_mov_b32 v201, 0
G 5,165
A 13,13,5
Mvaa 168,136,104
Ra 128,242,16384
Ra 132,242,17408
v_cmp_nlt_f32_e64 s[86:87], v13, s90
v_rcp_f32 v13, v13
s_or_b64 s[88:89], s[88:89], s[86:87]
v_accvgpr_write_b32 a164, 0
Mvaa 168,140,108
Dc 1,8,61,3072
v_accvgpr_write_b32 a165, 0
v_accvgpr_write_b32 a166, 0
v_accvgpr_write_b32 a167, 0
U 32,32,12
U 33,33,12
Mvaa 172,140,124
U 34,34,12
U 35,35,12
U 36,36,12
U 37,37,12
U 38,38,12
U 39,39,12
Mvaa 172,136,120
s_add_u32 m0, s62, 4096
Ra 136,242,18432
Ra 140,242,19456
K 32,32,33
K 33,34,35
K 34,36,37
W 4
Cvaa 188,144,112,236
K 35,38,39
s_add_u32 s93, s67, 0
buffer_store_dwordx4 v[32:35], v1, s[8:11], s93 offen offset:0 sc1
U 40,40,12
U 41,41,12
U 42,42,12
U 43,43,12
Mvaa 188,148,116
Ra 144,242,20480
v_cndmask_b32_e64 v168, v241, v168, s[72:73]
v_cndmask_b32_e64 v169, v241, v169, s[74:75]
v_cndmask_b32_e64 v170, v241, v170, s[76:77]
v_cndmask_b32_e64 v171, v241, v171, s[78:79]
U 44,44,12
Mvaa 188,152,120
X 168
X 169
U 45,45,12
U 46,46,12
Mvaa 188,156,124
W 1
Ra 148,242,21504
Dc 243,8,61,0
U 47,47,12
K 40,40,41
K 41,42,43
K 42,44,45
Mvav 96,128,212
Ra 152,242,22528
Ra 156,242,23552
K 43,46,47
X 170
K 208,168,169
Mvav 128,128,220
buffer_store_dwordx4 v[40:43], v1, s[8:11], s93 offen offset:1024 sc1
U 48,48,12
U 49,49,12
U 50,50,12
U 51,51,12
U 52,52,12
Mvav 132,132,220
U 53,53,12
U 54,54,12
U 55,55,12
K 48,48,49
K 49,50,51
K 50,52,53
Mvav 100,132,212
Dc 243,8,61,1024
Ra 128,242,32768
Ra 132,242,33792
X 171
K 51,54,55
Mvav 104,136,212
X 172
K 209,170,171
X 173
Mvav 136,136,220
buffer_store_dwordx4 v[48:51], v1, s[8:11], s93 offen offset:2048 sc1
U 56,56,12
U 57,57,12
U 58,58,12
U 59,59,12
U 60,60,12
Mvav 140,140,220
U 61,61,12
U 62,62,12
U 63,63,12
K 56,56,57
K 57,58,59
K 58,60,61
Mvav 108,140,212
W 2
Dc 243,8,61,2048
Ra 136,242,34816
Ra 140,242,35840
K 59,62,63
X 174
Mvav 112,144,212
K 216,172,173
X 175
Mvav 144,144,220
buffer_store_dwordx4 v[56:59], v1, s[8:11], s93 offen offset:3072 sc1
Ra 144,242,36864
G 14,168
G 6,169
A 14,14,6
v_cmp_nlt_f32_e64 s[86:87], v14, s90
Mvav 148,148,220
v_rcp_f32 v14, v14
s_or_b64 s[88:89], s[88:89], s[86:87]
v_accvgpr_write_b32 a168, 0
v_accvgpr_write_b32 a169, 0
v_accvgpr_write_b32 a170, 0
v_accvgpr_write_b32 a171, 0
Mvav 116,148,212
Dc 243,8,61,3072
s_add_u32 m0, s62, 8192
G 15,172
G 7,173
A 15,15,7
v_cmp_nlt_f32_e64 s[86:87], v15, s90
v_mov_b32 v194, 0
Mvav 120,152,212
v_rcp_f32 v15, v15
s_or_b64 s[88:89], s[88:89], s[86:87]
v_accvgpr_write_b32 a172, 0
v_accvgpr_write_b32 a173, 0
v_accvgpr_write_b32 a174, 0
v_accvgpr_write_b32 a175, 0
Mvav 152,152,220
Dc 244,8,61,0
U 64,64,13
U 65,65,13
U 66,66,13
U 67,67,13
U 68,68,13
Mvav 156,156,220
U 69,69,13
U 70,70,13
U 71,71,13
K 64,64,65
K 65,66,67
K 66,68,69
Mvav 124,156,212
W 1
Dc 244,8,61,1024
Ra 148,242,37888
K 67,70,71
s_add_u32 s93, s67, 131072
K 217,174,175
v_mov_b32 v195, 0
Cvaa 160,128,64,224
Ra 152,242,38912
Ra 156,242,39936
v_mov_b32 v202, 0
v_mov_b32 v203, 0
v_mov_b32 v210, 0
v_mov_b32 v211, 0
Mvaa 160,132,68
buffer_store_dwordx4 v[64:67], v1, s[8:11], s93 offen offset:0 sc1
U 72,72,13
U 73,73,13
U 74,74,13
U 75,75,13
U 76,76,13
Cvaa 164,132,84,228
U 77,77,13
U 78,78,13
U 79,79,13
K 72,72,73
K 73,74,75
K 74,76,77
Mvaa 164,128,80
Dc 244,8,61,2048
K 75,78,79
v_cndmask_b32_e64 v188, v241, v188, s[72:73]
v_cndmask_b32_e64 v189, v241, v189, s[74:75]
v_cndmask_b32_e64 v190, v241, v190, s[76:77]
v_cndmask_b32_e64 v191, v241, v191, s[78:79]
Cvaa 168,128,96,232
X 188
X 189
Mvaa 168,132,100
buffer_store_dwordx4 v[72:75], v1, s[8:11], s93 offen offset:1024 sc1
U 80,80,13
U 81,81,13
U 82,82,13
U 83,83,13
U 84,84,13
Cvaa 172,132,116,236
U 85,85,13
U 86,86,13
U 87,87,13
K 80,80,81
K 81,82,83
K 82,84,85
Mvaa 172,128,112
Dc 244,8,61,3072
s_add_u32 m0, s62, 12288
Ra 128,242,49152
Ra 132,242,50176
K 83,86,87
X 190
Mvaa 160,136,72
K 218,188,189
X 191
Mvaa 160,140,76
buffer_store_dwordx4 v[80:83], v1, s[8:11], s93 offen offset:2048 sc1
U 88,88,13
U 89,89,13
U 90,90,13
U 91,91,13
U 92,92,13
Mvaa 164,140,92
U 93,93,13
U 94,94,13
U 95,95,13
K 88,88,89
K 89,90,91
K 90,92,93
Mvaa 164,136,88
K 91,94,95
buffer_store_dwordx4 v[88:91], v1, s[8:11], s93 offen offset:3072 sc1
U 96,96,14
U 97,97,14
U 98,98,14
U 99,99,14
Mvaa 168,136,104
U 100,100,14
U 101,101,14
U 102,102,14
U 103,103,14
K 96,96,97
K 97,98,99
Mvaa 168,140,108
Dc 245,8,61,0
K 98,100,101
K 99,102,103
s_add_u32 s93, s67, 262144
v_cndmask_b32_e64 v160, v241, v160, s[72:73]
K 219,190,191
v_cndmask_b32_e64 v161, v241, v161, s[74:75]
Mvaa 172,140,124
v_cndmask_b32_e64 v162, v241, v162, s[76:77]
v_cndmask_b32_e64 v163, v241, v163, s[78:79]
X 160
Mvaa 172,136,120
W 2
Dc 245,8,61,1024
Ra 136,242,51200
Ra 140,242,52224
X 161
Cvaa 180,144,80,228
Mvaa 180,148,84
buffer_store_dwordx4 v[96:99], v1, s[8:11], s93 offen offset:0 sc1
U 104,104,14
U 105,105,14
U 106,106,14
U 107,107,14
U 108,108,14
Cvaa 184,148,100,232
U 109,109,14
U 110,110,14
U 111,111,14
K 104,104,105
K 105,106,107
K 106,108,109
Mvaa 184,144,96
Dc 245,8,61,2048
K 107,110,111
X 162
K 196,160,161
Cvaa 188,144,112,236
Ra 144,242,53248
X 163
Mvaa 188,148,116
buffer_store_dwordx4 v[104:107], v1, s[8:11], s93 offen offset:1024 sc1
U 112,112,14
U 113,113,14
U 114,114,14
U 115,115,14
U 116,116,14
Mvaa 180,152,88
U 117,117,14
U 118,118,14
U 119,119,14
K 112,112,113
K 113,114,115
K 114,116,117
Mvaa 180,156,92
Dc 245,8,61,3072
s_add_u32 s61, s61, s63
s_xor_b32 s62, s62, 0x10000
s_add_u32 s64, s64, 1
K 115,118,119
X 164
Mvaa 184,156,108
Ra 148,242,54272
K 197,162,163
X 165
Mvaa 184,152,104
buffer_store_dwordx4 v[112:115], v1, s[8:11], s93 offen offset:2048 sc1
U 120,120,14
U 121,121,14
U 122,122,14
U 123,123,14
U 124,124,14
Mvaa 188,152,120
U 125,125,14
U 126,126,14
U 127,127,14
K 120,120,121
K 121,122,123
K 122,124,125
Mvaa 188,156,124
W 2
K 123,126,127
buffer_store_dwordx4 v[120:123], v1, s[8:11], s93 offen offset:3072 sc1
U 128,128,15
U 129,129,15
U 130,130,15
U 131,131,15
Zvav 32,128,192
U 132,132,15
U 133,133,15
U 134,134,15
U 135,135,15
K 128,128,129
K 129,130,131
Zvav 64,128,200
K 130,132,133
K 131,134,135
s_add_u32 s93, s67, 393216
buffer_store_dwordx4 v[128:131], v1, s[8:11], s93 offen offset:0 sc1
U 136,136,15
U 137,137,15
U 138,138,15
Zvav 96,128,208
U 139,139,15
U 140,140,15
U 141,141,15
U 142,142,15
U 143,143,15
K 136,136,137
Zvav 128,128,216
K 137,138,139
K 138,140,141
K 139,142,143
buffer_store_dwordx4 v[136:139], v1, s[8:11], s93 offen offset:1024 sc1
U 144,144,15
U 145,145,15
Zvav 132,132,216
U 146,146,15
U 147,147,15
U 148,148,15
U 149,149,15
U 150,150,15
U 151,151,15
Zvav 100,132,208
K 144,144,145
K 145,146,147
K 146,148,149
K 147,150,151
buffer_store_dwordx4 v[144:147], v1, s[8:11], s93 offen offset:2048 sc1
U 152,152,15
Zvav 68,132,200
U 153,153,15
U 154,154,15
U 155,155,15
U 156,156,15
U 157,157,15
U 158,158,15
Zvav 36,132,192
U 159,159,15
)"
R"(K 152,152,153
K 153,154,155
K 154,156,157
K 155,158,159
buffer_store_dwordx4 v[152:155], v1, s[8:11], s93 offen offset:3072 sc1
Zvav 40,136,192
Ra 152,242,55296
X 166
K 204,164,165
X 167
Zvav 72,136,200
Ra 156,242,56320
X 168
K 205,166,167
X 169
Zvav 104,136,208
X 170
K 212,168,169
X 171
Zvav 136,136,216
X 172
K 213,170,171
X 173
Zvav 140,140,216
Zvav 108,140,208
X 174
Zvav 76,140,200
s_waitcnt vmcnt(6) lgkmcnt(0)
s_barrier
v_xor_b32 v242, 0x10000, v242
Zvav 44,140,192
Ra 128,242,40960
K 220,172,173
X 175
Zvav 48,144,192
Zvav 80,144,200
v_mov_b32 v198, 0
Zvav 112,144,208
Ra 132,242,41984
K 221,174,175
Zvav 144,144,216
v_mov_b32 v199, 0
v_cndmask_b32_e64 v180, v241, v180, s[72:73]
Zvav 148,148,216
v_cndmask_b32_e64 v181, v241, v181, s[74:75]
Zvav 116,148,208
Ra 136,242,43008
v_cndmask_b32_e64 v182, v241, v182, s[76:77]
Zvav 84,148,200
v_cndmask_b32_e64 v183, v241, v183, s[78:79]
Zvav 52,148,192
X 180
Zvav 56,152,192
Ra 140,242,44032
X 181
Zvav 88,152,200
Zvav 120,152,208
X 182
Zvav 152,152,216
K 206,180,181
Zvav 156,156,216
X 183
Zvav 124,156,208
Zvav 92,156,200
s_waitcnt vmcnt(0)
s_and_b64 s[86:87], s[88:89], s[24:25]
s_cbranch_scc1 .LL_49%=
v_mov_b64 v[28:29], s[12:13]
s_mov_b64 exec, 1
buffer_store_dwordx2 v[28:29], off, s[8:11], s69 sc1
s_mov_b64 exec, -1
.LL_49%=:
K 207,182,183
Zvav 60,156,192
s_cmp_eq_u32 s64, 15
s_cbranch_scc1 .Lf2_41%=
.Ll2_38%=:
W 0
Cvaa 160,128,64,224
Ra 144,242,45056
Ra 148,242,46080
Mvaa 160,132,68
s_add_u32 m0, s62, 0
Ra 152,242,47104
Cvaa 164,132,84,228
X 184
Mvaa 164,128,80
X 185
Cvaa 168,128,96,232
X 186
Mvaa 168,132,100
Ra 156,242,48128
K 214,184,185
Cvaa 172,132,116,236
Dc 1,8,61,0
Mvaa 172,128,112
X 187
Mvaa 160,136,72
X 188
Mvaa 160,140,76
Ra 128,242,57344
K 215,186,187
Mvaa 164,140,92
Dc 1,8,61,1024
Mvaa 164,136,88
X 189
Mvaa 168,136,104
X 190
Mvaa 168,140,108
Ra 132,242,58368
K 222,188,189
Mvaa 172,140,124
Dc 1,8,61,2048
Mvaa 172,136,120
X 191
v_smfmac_f32_16x16x64_bf16 a[160:163], a[176:179], v[192:199], v240
Ra 136,242,59392
K 223,190,191
v_smfmac_f32_16x16x64_bf16 a[164:167], a[176:179], v[200:207], v240
X 160
v_smfmac_f32_16x16x64_bf16 a[168:171], a[176:179], v[208:215], v240
Dc 1,8,61,3072
v_smfmac_f32_16x16x64_bf16 a[172:175], a[176:179], v[216:223], v240
W 3
X 161
Cvaa 176,144,64,224
s_add_u32 m0, s62, 4096
X 162
Mvaa 176,148,68
Ra 140,242,60416
K 192,160,161
Cvaa 180,148,84,228
X 163
Mvaa 180,144,80
Dc 243,8,61,0
Cvaa 184,144,96,232
X 164
Mvaa 184,148,100
K 193,162,163
Cvaa 188,148,116,236
X 165
Mvaa 188,144,112
Dc 243,8,61,1024
Ra 144,242,61440
Mvaa 176,152,72
X 166
Mvaa 176,156,76
Ra 148,242,62464
K 200,164,165
Mvaa 180,156,92
X 167
Mvaa 180,152,88
Dc 243,8,61,2048
Mvaa 184,152,104
X 168
Mvaa 184,156,108
K 201,166,167
Mvaa 188,156,124
X 169
Mvaa 188,152,120
W 2
Dc 243,8,61,3072
Ra 152,242,63488
Mvav 32,128,196
s_add_u32 m0, s62, 8192
X 170
Mvav 64,128,204
Ra 156,242,64512
K 208,168,169
Mvav 96,128,212
X 171
Mvav 128,128,220
Dc 244,8,61,0
Mvav 132,132,220
X 172
Mvav 100,132,212
Ra 128,242,32768
K 209,170,171
Mvav 68,132,204
X 173
Mvav 36,132,196
Dc 244,8,61,1024
Mvav 40,136,196
X 174
Mvav 72,136,204
Ra 132,242,33792
K 216,172,173
Mvav 104,136,212
X 175
Mvav 136,136,220
Dc 244,8,61,2048
Mvav 140,140,220
X 176
Mvav 108,140,212
Ra 136,242,34816
K 217,174,175
Mvav 76,140,204
X 177
Mvav 44,140,196
W 3
Dc 244,8,61,3072
Mvav 48,144,196
X 178
Mvav 80,144,204
s_add_u32 m0, s62, 12288
Ra 140,242,35840
Mvav 112,144,212
K 194,176,177
Mvav 144,144,220
X 179
Mvav 148,148,220
X 180
Mvav 116,148,212
Dc 245,8,61,0
Ra 144,242,36864
Mvav 84,148,204
K 195,178,179
Mvav 52,148,196
X 181
Mvav 56,152,196
X 182
Mvav 88,152,204
Dc 245,8,61,1024
Ra 148,242,37888
Mvav 120,152,212
K 202,180,181
Mvav 152,152,220
X 183
Mvav 156,156,220
X 184
Mvav 124,156,212
Dc 245,8,61,2048
Ra 152,242,38912
Mvav 92,156,204
K 203,182,183
Mvav 60,156,196
W 3
X 185
Cvaa 160,128,64,224
X 186
Mvaa 160,132,68
Dc 245,8,61,3072
Ra 156,242,39936
Cvaa 164,132,84,228
s_add_u32 s61, s61, s63
K 210,184,185
Mvaa 164,128,80
X 187
Cvaa 168,128,96,232
X 188
Mvaa 168,132,100
Cvaa 172,132,116,236
s_xor_b32 s62, s62, 0x10000
K 211,186,187
Mvaa 172,128,112
X 189
Mvaa 160,136,72
Ra 128,242,49152
Ra 132,242,50176
Mvaa 160,140,76
s_add_u32 s64, s64, 1
X 190
Mvaa 164,140,92
K 218,188,189
Mvaa 164,136,88
X 191
Mvaa 168,136,104
Mvaa 168,140,108
X 160
Mvaa 172,140,124
K 219,190,191
Mvaa 172,136,120
W 2
X 161
Cvaa 176,144,64,224
Ra 136,242,51200
Ra 140,242,52224
Mvaa 176,148,68
X 162
Cvaa 180,148,84,228
K 196,160,161
Mvaa 180,144,80
X 163
Cvaa 184,144,96,232
Mvaa 184,148,100
X 164
Cvaa 188,148,116,236
K 197,162,163
Mvaa 188,144,112
X 165
Mvaa 176,152,72
Ra 144,242,53248
Ra 148,242,54272
Mvaa 176,156,76
X 166
Mvaa 180,156,92
K 204,164,165
Mvaa 180,152,88
X 167
Mvaa 184,152,104
Mvaa 184,156,108
X 168
Mvaa 188,156,124
Mvaa 188,152,120
W 2
Ra 152,242,55296
K 205,166,167
Mvav 32,128,192
X 169
Mvav 64,128,200
Ra 156,242,56320
Mvav 96,128,208
X 170
Mvav 128,128,216
K 212,168,169
Mvav 132,132,216
X 171
Mvav 100,132,208
X 172
Mvav 68,132,200
Mvav 36,132,192
K 213,170,171
Mvav 40,136,192
X 173
Mvav 72,136,200
X 174
Mvav 104,136,208
Mvav 136,136,216
K 220,172,173
Mvav 140,140,216
X 175
Mvav 108,140,208
X 176
Mvav 76,140,200
s_waitcnt vmcnt(0) lgkmcnt(0)
s_barrier
v_xor_b32 v242, 0x10000, v242
Mvav 44,140,192
Ra 128,242,40960
K 221,174,175
Mvav 48,144,192
X 177
Mvav 80,144,200
X 178
Mvav 112,144,208
Ra 132,242,41984
Mvav 144,144,216
K 198,176,177
Mvav 148,148,216
X 179
Mvav 116,148,208
X 180
Mvav 84,148,200
Ra 136,242,43008
Mvav 52,148,192
K 199,178,179
Mvav 56,152,192
X 181
Mvav 88,152,200
Ra 140,242,44032
Mvav 120,152,208
X 182
Mvav 152,152,216
K 206,180,181
Mvav 156,156,216
X 183
Mvav 124,156,208
Mvav 92,156,200
K 207,182,183
Mvav 60,156,192
s_cmp_lg_u32 s64, 15
s_cbranch_scc1 .Ll2_38%=
.Lf2_41%=:
W 0
Cvaa 160,128,64,224
v_bfe_u32 v28, v1, 4, 2
v_lshlrev_b32 v28, 3, v28
Mvaa 160,132,68
Ra 144,242,45056
Ra 148,242,46080
Cvaa 164,132,84,228
Ra 152,242,47104
buffer_load_dwordx2 v[28:29], v28, s[8:11], s54 offen sc1
Mvaa 164,128,80
s_lshl_b32 s92, s58, 2
s_add_u32 s91, s92, s51
s_add_u32 s95, s91, 3
Cvaa 168,128,96,232
s_and_b32 s95, s95, 15
s_mul_i32 s95, s95, 40960
s_mul_i32 s71, s51, 10240
Mvaa 168,132,100
s_add_u32 s71, s71, 0
s_add_u32 m0, s71, s48
s_lshl2_add_u32 s94, s48, s95
Cvaa 172,132,116,236
X 184
Mvaa 172,128,112
X 185
Mvaa 160,136,72
)"
R"(X 186
Mvaa 160,140,76
Ra 156,242,48128
K 214,184,185
Mvaa 164,140,92
X 187
Mvaa 164,136,88
D 16,40,94,0
Ra 128,242,57344
Mvaa 168,136,104
s_add_u32 s95, s91, 5
s_and_b32 s95, s95, 15
s_mul_i32 s95, s95, 40960
Mvaa 168,140,108
s_mul_i32 s71, s51, 10240
s_add_u32 s71, s71, 20480
Ra 132,242,58368
Mvaa 172,140,124
X 188
Mvaa 172,136,120
D 17,40,94,1024
K 215,186,187
v_smfmac_f32_16x16x64_bf16 a[160:163], a[176:179], v[192:199], v240
X 189
v_smfmac_f32_16x16x64_bf16 a[164:167], a[176:179], v[200:207], v240
X 190
K 222,188,189
X 191
s_nop 0
K 223,190,191
v_smfmac_f32_16x16x64_bf16 a[168:171], a[176:179], v[208:215], v240
D 18,40,94,2048
Ra 136,242,59392
v_smfmac_f32_16x16x64_bf16 a[172:175], a[176:179], v[216:223], v240
W 3
X 160
Cvaa 176,144,64,224
X 161
Mvaa 176,148,68
X 162
Cvaa 180,148,84,228
D 19,40,94,3072
K 192,160,161
Mvaa 180,144,80
s_add_u32 m0, m0, 4096
X 163
Cvaa 184,144,96,232
X 164
Mvaa 184,148,100
Ra 140,242,60416
K 193,162,163
Cvaa 188,148,116,236
buffer_load_dwordx4 v20, s[40:43], s94 offen lds
Mvaa 188,144,112
s_add_u32 m0, s71, s48
s_lshl2_add_u32 s94, s48, s95
Ra 144,242,61440
Mvaa 176,152,72
X 165
Mvaa 176,156,76
X 166
Mvaa 180,156,92
D 16,40,94,0
K 200,164,165
Mvaa 180,152,88
X 167
Mvaa 184,152,104
X 172
Mvaa 184,156,108
D 17,40,94,1024
K 201,166,167
Mvaa 188,156,124
X 173
Mvaa 188,152,120
W 1
Ra 148,242,62464
Ra 152,242,63488
Mvav 32,128,196
X 174
Mvav 64,128,204
D 18,40,94,2048
K 216,172,173
Mvav 96,128,212
X 175
Mvav 128,128,220
X 168
Mvav 132,132,220
s_mov_b32 s18, 40960
D 19,40,94,3072
K 217,174,175
Mvav 100,132,212
s_add_u32 m0, m0, 4096
X 169
Mvav 68,132,204
s_mov_b32 s19, 73728
X 170
Mvav 36,132,196
s_bitcmp1_b32 s14, 0
s_cselect_b32 s18, 49152, s18
s_cselect_b32 s19, 98304, s19
Ra 156,242,64512
K 208,168,169
Mvav 40,136,196
s_cmp_lt_u32 s14, 2
s_cselect_b32 s20, s18, s19
s_cselect_b32 s21, s19, s18
X 171
Mvav 72,136,204
X 184
Mvav 104,136,212
buffer_load_dwordx4 v20, s[40:43], s94 offen lds
K 209,170,171
Mvav 136,136,220
s_waitcnt vmcnt(10)
v_cmp_eq_u64 vcc, s[12:13], v[28:29]
s_cmp_eq_u64 vcc, -1
s_cselect_b32 s46, 79691776, 0
s_add_u32 s81, s58, 0
s_and_b32 s81, s81, 3
Mvav 140,140,220
s_lshl_b32 s81, s81, 12
s_add_u32 s81, s81, s49
Ra 128,242,49152
Mvav 108,140,212
X 185
Mvav 76,140,204
X 186
Mvav 44,140,196
W 1
buffer_load_dwordx4 a[184:187], v1, s[44:47], s81 offen offset:0 sc1
K 210,184,185
Mvav 48,144,196
s_add_u32 s82, s81, 131072
X 187
Mvav 80,144,204
v_add_u32 v2, s20, v1
v_add_u32 v3, s21, v1
Mvav 112,144,212
s_lshl_b32 s18, s14, 9
X 188
Mvav 144,144,220
Ra 132,242,50176
K 211,186,187
Mvav 148,148,220
buffer_load_dwordx4 a[200:203], v1, s[44:47], s82 offen offset:0 sc1
Ra 136,242,51200
Mvav 116,148,212
X 189
Mvav 84,148,204
v_lshrrev_b32 v232, 1, v1
v_add_u32 v232, s18, v232
Mvav 52,148,196
X 190
Mvav 56,152,196
K 218,188,189
v_bfe_u32 v12, v1, 6, 2
Mvav 88,152,204
buffer_load_dwordx4 a[188:191], v1, s[44:47], s81 offen offset:1024 sc1
Ra 140,242,52224
Mvav 120,152,212
X 191
Mvav 152,152,220
v_lshrrev_b32 v13, 8, v1
v_bfe_u32 v14, v1, 4, 1
Mvav 156,156,220
X 176
Mvav 124,156,212
K 219,190,191
v_lshlrev_b32 v14, 4, v14
Mvav 92,156,204
X 177
Mvav 60,156,196
buffer_load_dwordx4 a[204:207], v1, s[44:47], s82 offen offset:1024 sc1
v_mov_b32 v15, 0x3f80
Maav 168,180,208
X 178
K 194,176,177
X 179
X 180
K 195,178,179
Maav 172,180,216
X 181
X 182
K 202,180,181
X 183
s_nop 0
K 203,182,183
Maav 160,180,192
Ra 144,242,53248
v_lshlrev_b32 v15, v14, v15
Maav 164,180,200
W 1
buffer_load_dwordx4 a[192:195], v1, s[44:47], s81 offen offset:2048 sc1
v_bfe_u32 v14, v1, 5, 1
Mvav 96,128,208
Ra 148,242,54272
v_xor_b32 v27, v12, v13
v_lshl_or_b32 v27, v27, 1, v14
Mvav 128,128,216
v_cmp_eq_u32_e64 s[86:87], 0, v27
v_mov_b32 v226, 0
s_nop 0
v_cndmask_b32_e64 v224, 0, v15, s[86:87]
Mvav 132,132,216
Ra 152,242,55296
Ra 156,242,56320
buffer_load_dwordx4 a[208:211], v1, s[44:47], s82 offen offset:2048 sc1
Mvav 100,132,208
v_mov_b32 v228, 0
v_mov_b32 v230, v224
v_cmp_eq_u32_e64 s[86:87], 1, v27
Mvav 104,136,208
v_mov_b32 v227, 0
Mvav 136,136,216
buffer_load_dwordx4 a[196:199], v1, s[44:47], s81 offen offset:3072 sc1
v_cndmask_b32_e64 v225, 0, v15, s[86:87]
v_mov_b32 v229, 0
Mvav 140,140,216
v_mov_b32 v231, v225
G 4,160
G 8,161
Mvav 108,140,208
W 0
G 5,164
G 9,165
G 6,168
Mvav 112,144,208
buffer_load_dwordx4 a[212:215], v1, s[44:47], s82 offen offset:3072 sc1
s_add_u32 s81, s58, 1
s_and_b32 s81, s81, 3
s_lshl_b32 s81, s81, 12
Mvav 144,144,216
s_add_u32 s81, s81, s49
G 10,169
G 7,172
G 11,173
Mvav 148,148,216
A 4,4,8
A 5,5,9
A 6,6,10
Mvav 116,148,208
buffer_load_dwordx4 a[216:219], v1, s[44:47], s81 offen offset:0 sc1
s_add_u32 s82, s81, 131072
A 7,7,11
ds_write_b64 v232, v[6:7] offset:57344
Mvav 120,152,208
A 8,4,5
A 9,6,7
A 8,8,9
Mvav 152,152,216
v_cmp_ngt_f32_e64 s[86:87], s90, v8
s_lshl_b32 s19, s14, 2
v_mov_b32 v9, s19
s_or_b64 s[88:89], s[88:89], s[86:87]
Mvav 156,156,216
s_and_b64 s[88:89], s[88:89], s[24:25]
s_cselect_b32 s18, 1, 0
buffer_load_dwordx4 a[232:235], v1, s[44:47], s82 offen offset:0 sc1
v_mov_b32 v8, s18
ds_write_b32 v9, v8 offset:59392
Mvav 124,156,208
v_xor_b32 v10, 1024, v232
v_bfe_u32 v11, v1, 2, 4
K 96,96,97
Mvav 32,128,192
K 97,98,99
K 98,100,101
K 99,102,103
Mvav 64,128,200
ds_write_b128 v2, v[96:99] offset:0
K 128,128,129
K 129,130,131
Mvav 68,132,200
K 130,132,133
K 131,134,135
ds_write_b128 v2, v[128:131] offset:4096
Mvav 36,132,192
buffer_load_dwordx4 a[220:223], v1, s[44:47], s81 offen offset:1024 sc1
K 104,104,105
K 105,106,107
Mvav 40,136,192
K 106,108,109
K 107,110,111
ds_write_b128 v2, v[104:107] offset:1024
Mvav 72,136,200
K 136,136,137
K 137,138,139
K 138,140,141
Mvav 76,140,200
buffer_load_dwordx4 a[236:239], v1, s[44:47], s82 offen offset:1024 sc1
K 139,142,143
ds_write_b128 v2, v[136:139] offset:5120
Mvav 44,140,192
K 112,112,113
K 113,114,115
K 114,116,117
Mvav 48,144,192
K 115,118,119
ds_write_b128 v2, v[112:115] offset:2048
K 144,144,145
Mvav 80,144,200
K 145,146,147
K 146,148,149
K 147,150,151
Mvav 84,148,200
ds_write_b128 v2, v[144:147] offset:6144
buffer_load_dwordx4 a[224:227], v1, s[44:47], s81 offen offset:2048 sc1
K 120,120,121
Mvav 52,148,192
K 121,122,123
K 122,124,125
K 123,126,127
Mvav 56,152,192
ds_write_b128 v2, v[120:123] offset:3072
K 152,152,153
K 153,154,155
Mvav 88,152,200
buffer_load_dwordx4 a[240:243], v1, s[44:47], s82 offen offset:2048 sc1
K 154,156,157
K 155,158,159
Mvav 92,156,200
ds_write_b128 v2, v[152:155] offset:7168
Mvav 60,156,192
buffer_load_dwordx4 a[228:231], v1, s[44:47], s81 offen offset:3072 sc1
buffer_load_dwordx4 a[244:247], v1, s[44:47], s82 offen offset:3072 sc1
s_branch .Lend_43%=
.p2align 12
.Lfs_42%=:
v_smfmac_f32_16x16x64_bf16 a[160:163], a[176:179], v[192:199], v240
s_mov_b32 s46, 0
s_lshl_b32 s92, s58, 2
s_add_u32 s91, s92, s51
s_add_u32 s95, s91, 3
s_and_b32 s95, s95, 15
s_mul_i32 s95, s95, 40960
s_mul_i32 s71, s51, 10240
v_smfmac_f32_16x16x64_bf16 a[164:167], a[176:179], v[200:207], v240
s_add_u32 s71, s71, 0
s_add_u32 m0, s71, s48
s_lshl2_add_u32 s94, s48, s95
Ra 144,242,45056
Ra 148,242,46080
Ra 152,242,47104
Ra 156,242,48128
v_smfmac_f32_16x16x64_bf16 a[168:171], a[176:179], v[208:215], v240
D 16,40,94,0
s_add_u32 s95, s91, 5
s_and_b32 s95, s95, 15
s_mul_i32 s95, s95, 40960
s_mul_i32 s71, s51, 10240
s_add_u32 s71, s71, 20480
v_smfmac_f32_16x16x64_bf16 a[172:175], a[176:179], v[216:223], v240
W 4
Cvaa 168,128,96,232
v_mov_b32 v192, 0
G 12,160
Mvaa 168,132,100
D 17,40,94,1024
G 4,161
A 12,12,4
v_cmp_nlt_f32_e64 s[86:87], v12, s90
v_rcp_f32 v12, v12
s_or_b64 s[88:89], s[88:89], s[86:87]
Cvaa 172,132,116,236
v_accvgpr_write_b32 a160, 0
v_accvgpr_write_b32 a161, 0
v_accvgpr_write_b32 a162, 0
v_accvgpr_write_b32 a163, 0
G 13,164
G 5,165
Mvaa 172,128,112
v_mov_b32 v193, 0
A 13,13,5
v_cmp_nlt_f32_e64 s[86:87], v13, s90
v_rcp_f32 v13, v13
s_or_b64 s[88:89], s[88:89], s[86:87]
v_accvgpr_write_b32 a164, 0
Mvaa 168,136,104
v_accvgpr_write_b32 a165, 0
v_accvgpr_write_b32 a166, 0
v_accvgpr_write_b32 a167, 0
U 32,32,12
U 33,33,12
U 34,34,12
Mvaa 168,140,108
v_mov_b32 v200, 0
U 35,35,12
U 36,36,12
U 37,37,12
U 38,38,12
U 39,39,12
Mvaa 172,140,124
Ra 128,242,16384
Ra 132,242,17408
K 32,32,33
K 33,34,35
K 34,36,37
K 35,38,39
s_add_u32 s93, s67, 0
Mvaa 172,136,120
)"
R"(Ra 136,242,18432
Ra 140,242,19456
D 18,40,94,2048
v_mov_b32 v201, 0
W 4
Cvaa 188,144,112,236
Ra 144,242,20480
v_cndmask_b32_e64 v168, v241, v168, s[72:73]
v_cndmask_b32_e64 v169, v241, v169, s[74:75]
v_cndmask_b32_e64 v170, v241, v170, s[76:77]
Mvaa 188,148,116
buffer_store_dwordx4 v[32:35], v1, s[8:11], s93 offen offset:0 sc1
U 40,40,12
U 41,41,12
U 42,42,12
U 43,43,12
U 44,44,12
Mvaa 188,152,120
U 45,45,12
U 46,46,12
U 47,47,12
K 40,40,41
K 41,42,43
K 42,44,45
Mvaa 188,156,124
W 1
K 43,46,47
buffer_store_dwordx4 v[40:43], v1, s[8:11], s93 offen offset:1024 sc1
U 48,48,12
U 49,49,12
U 50,50,12
U 51,51,12
Mvav 96,128,212
U 52,52,12
U 53,53,12
U 54,54,12
U 55,55,12
K 48,48,49
K 49,50,51
Mvav 128,128,220
K 50,52,53
K 51,54,55
buffer_store_dwordx4 v[48:51], v1, s[8:11], s93 offen offset:2048 sc1
U 56,56,12
U 57,57,12
U 58,58,12
Mvav 132,132,220
Ra 148,242,21504
Ra 152,242,22528
Ra 156,242,23552
U 59,59,12
U 60,60,12
U 61,61,12
Mvav 100,132,212
D 19,40,94,3072
s_add_u32 m0, m0, 4096
U 62,62,12
U 63,63,12
K 56,56,57
K 57,58,59
K 58,60,61
Mvav 104,136,212
K 59,62,63
v_cndmask_b32_e64 v171, v241, v171, s[78:79]
X 168
X 169
Mvav 136,136,220
buffer_store_dwordx4 v[56:59], v1, s[8:11], s93 offen offset:3072 sc1
Ra 128,242,49152
G 14,168
G 6,169
A 14,14,6
v_cmp_nlt_f32_e64 s[86:87], v14, s90
Mvav 140,140,220
v_rcp_f32 v14, v14
s_or_b64 s[88:89], s[88:89], s[86:87]
v_accvgpr_write_b32 a168, 0
v_accvgpr_write_b32 a169, 0
v_accvgpr_write_b32 a170, 0
v_accvgpr_write_b32 a171, 0
Mvav 108,140,212
W 1
G 15,172
G 7,173
A 15,15,7
v_cmp_nlt_f32_e64 s[86:87], v15, s90
v_rcp_f32 v15, v15
Mvav 112,144,212
s_or_b64 s[88:89], s[88:89], s[86:87]
buffer_load_dwordx4 v20, s[40:43], s94 offen lds
v_accvgpr_write_b32 a172, 0
v_accvgpr_write_b32 a173, 0
v_accvgpr_write_b32 a174, 0
v_accvgpr_write_b32 a175, 0
U 64,64,13
Mvav 144,144,220
s_add_u32 m0, s71, s48
s_lshl2_add_u32 s94, s48, s95
U 65,65,13
U 66,66,13
U 67,67,13
U 68,68,13
U 69,69,13
Mvav 148,148,220
U 70,70,13
U 71,71,13
K 64,64,65
K 65,66,67
K 66,68,69
K 67,70,71
s_add_u32 s93, s67, 131072
Mvav 116,148,212
buffer_store_dwordx4 v[64:67], v1, s[8:11], s93 offen offset:0 sc1
U 72,72,13
U 73,73,13
U 74,74,13
U 75,75,13
U 76,76,13
Mvav 120,152,212
Ra 132,242,50176
Ra 136,242,51200
Ra 140,242,52224
U 77,77,13
U 78,78,13
U 79,79,13
Mvav 152,152,220
K 72,72,73
K 73,74,75
K 74,76,77
K 75,78,79
buffer_store_dwordx4 v[72:75], v1, s[8:11], s93 offen offset:1024 sc1
U 80,80,13
Mvav 156,156,220
X 170
K 208,168,169
X 171
X 172
K 209,170,171
X 173
X 174
K 216,172,173
X 175
v_mov_b32 v194, 0
K 217,174,175
v_mov_b32 v195, 0
v_mov_b32 v202, 0
v_mov_b32 v203, 0
v_mov_b32 v210, 0
v_mov_b32 v211, 0
Mvav 124,156,212
v_cndmask_b32_e64 v188, v241, v188, s[72:73]
v_cndmask_b32_e64 v189, v241, v189, s[74:75]
v_cndmask_b32_e64 v190, v241, v190, s[76:77]
v_cndmask_b32_e64 v191, v241, v191, s[78:79]
X 188
X 189
X 190
K 218,188,189
X 191
s_nop 0
K 219,190,191
Maav 168,180,208
D 16,40,94,0
U 81,81,13
U 82,82,13
U 83,83,13
U 84,84,13
U 85,85,13
Maav 172,180,216
W 0
U 86,86,13
U 87,87,13
K 80,80,81
K 81,82,83
K 82,84,85
K 83,86,87
Zvav 32,128,192
buffer_store_dwordx4 v[80:83], v1, s[8:11], s93 offen offset:2048 sc1
U 88,88,13
U 89,89,13
U 90,90,13
U 91,91,13
U 92,92,13
Zvav 64,128,200
U 93,93,13
U 94,94,13
U 95,95,13
K 88,88,89
K 89,90,91
K 90,92,93
K 91,94,95
buffer_store_dwordx4 v[88:91], v1, s[8:11], s93 offen offset:3072 sc1
U 96,96,14
U 97,97,14
U 98,98,14
U 99,99,14
U 100,100,14
U 101,101,14
U 102,102,14
U 103,103,14
K 96,96,97
K 97,98,99
K 98,100,101
K 99,102,103
s_add_u32 s93, s67, 262144
buffer_store_dwordx4 v[96:99], v1, s[8:11], s93 offen offset:0 sc1
s_nop 1
Zvav 96,128,208
U 104,104,14
U 105,105,14
U 106,106,14
U 107,107,14
U 108,108,14
U 109,109,14
U 110,110,14
U 111,111,14
K 104,104,105
K 105,106,107
K 106,108,109
K 107,110,111
buffer_store_dwordx4 v[104:107], v1, s[8:11], s93 offen offset:1024 sc1
U 112,112,14
U 113,113,14
U 114,114,14
U 115,115,14
U 116,116,14
U 117,117,14
U 118,118,14
U 119,119,14
K 112,112,113
K 113,114,115
K 114,116,117
K 115,118,119
buffer_store_dwordx4 v[112:115], v1, s[8:11], s93 offen offset:2048 sc1
U 120,120,14
U 121,121,14
U 122,122,14
U 123,123,14
U 124,124,14
U 125,125,14
U 126,126,14
U 127,127,14
K 120,120,121
K 121,122,123
K 122,124,125
K 123,126,127
buffer_store_dwordx4 v[120:123], v1, s[8:11], s93 offen offset:3072 sc1
U 128,128,15
U 129,129,15
U 130,130,15
U 131,131,15
U 132,132,15
U 133,133,15
U 134,134,15
U 135,135,15
K 128,128,129
K 129,130,131
K 130,132,133
K 131,134,135
s_add_u32 s93, s67, 393216
buffer_store_dwordx4 v[128:131], v1, s[8:11], s93 offen offset:0 sc1
s_nop 1
Zvav 128,128,216
Ra 144,242,53248
U 136,136,15
U 137,137,15
U 138,138,15
U 139,139,15
U 140,140,15
Zvav 132,132,216
D 17,40,94,1024
U 141,141,15
U 142,142,15
U 143,143,15
K 136,136,137
K 137,138,139
Zvav 100,132,208
Ra 148,242,54272
Ra 152,242,55296
Ra 156,242,56320
K 138,140,141
K 139,142,143
Zvav 68,132,200
D 18,40,94,2048
Zvav 36,132,192
Zvav 40,136,192
D 19,40,94,3072
s_add_u32 m0, m0, 4096
Zvav 72,136,200
Zvav 104,136,208
buffer_store_dwordx4 v[136:139], v1, s[8:11], s93 offen offset:1024 sc1
U 144,144,15
U 145,145,15
U 146,146,15
U 147,147,15
U 148,148,15
Zvav 136,136,216
U 149,149,15
U 150,150,15
U 151,151,15
K 144,144,145
K 145,146,147
K 146,148,149
Zvav 140,140,216
K 147,150,151
buffer_store_dwordx4 v[144:147], v1, s[8:11], s93 offen offset:2048 sc1
U 152,152,15
U 153,153,15
U 154,154,15
U 155,155,15
Zvav 108,140,208
U 156,156,15
U 157,157,15
U 158,158,15
U 159,159,15
K 152,152,153
K 153,154,155
Zvav 76,140,200
buffer_load_dwordx4 v20, s[40:43], s94 offen lds
K 154,156,157
K 155,158,159
Zvav 44,140,192
W 0
Zvav 48,144,192
buffer_store_dwordx4 v[152:155], v1, s[8:11], s93 offen offset:3072 sc1
s_mov_b32 s18, 40960
s_mov_b32 s19, 73728
s_bitcmp1_b32 s14, 0
s_cselect_b32 s18, 49152, s18
s_cselect_b32 s19, 98304, s19
s_cmp_lt_u32 s14, 2
s_cselect_b32 s20, s18, s19
s_cselect_b32 s21, s19, s18
v_add_u32 v2, s20, v1
v_add_u32 v3, s21, v1
Zvav 80,144,200
s_lshl_b32 s18, s14, 9
v_lshrrev_b32 v232, 1, v1
v_add_u32 v232, s18, v232
v_bfe_u32 v12, v1, 6, 2
v_lshrrev_b32 v13, 8, v1
v_bfe_u32 v14, v1, 4, 1
v_lshlrev_b32 v14, 4, v14
Zvav 112,144,208
v_mov_b32 v15, 0x3f80
v_lshlrev_b32 v15, v14, v15
v_bfe_u32 v14, v1, 5, 1
v_xor_b32 v27, v12, v13
v_lshl_or_b32 v27, v27, 1, v14
v_cmp_eq_u32_e64 s[86:87], 0, v27
Zvav 144,144,216
v_mov_b32 v226, 0
Zvav 148,148,216
v_cndmask_b32_e64 v224, 0, v15, s[86:87]
v_mov_b32 v228, 0
v_mov_b32 v230, v224
v_cmp_eq_u32_e64 s[86:87], 1, v27
v_mov_b32 v227, 0
s_nop 0
v_cndmask_b32_e64 v225, 0, v15, s[86:87]
Zvav 116,148,208
v_mov_b32 v229, 0
v_mov_b32 v231, v225
G 4,160
G 8,161
G 5,164
G 9,165
Zvav 84,148,200
G 6,168
G 10,169
G 7,172
G 11,173
A 4,4,8
A 5,5,9
Zvav 52,148,192
A 6,6,10
A 7,7,11
ds_write_b64 v232, v[6:7] offset:57344
A 8,4,5
A 9,6,7
A 8,8,9
Zvav 56,152,192
v_cmp_ngt_f32_e64 s[86:87], s90, v8
s_lshl_b32 s19, s14, 2
v_mov_b32 v9, s19
s_or_b64 s[88:89], s[88:89], s[86:87]
s_and_b64 s[88:89], s[88:89], s[24:25]
s_cselect_b32 s18, 1, 0
v_mov_b32 v8, s18
ds_write_b32 v9, v8 offset:59392
Zvav 88,152,200
v_xor_b32 v10, 1024, v232
v_bfe_u32 v11, v1, 2, 4
K 96,96,97
K 97,98,99
K 98,100,101
K 99,102,103
Zvav 120,152,208
ds_write_b128 v2, v[96:99] offset:0
K 128,128,129
)"
R"(K 129,130,131
K 130,132,133
K 131,134,135
ds_write_b128 v2, v[128:131] offset:4096
Zvav 152,152,216
K 104,104,105
K 105,106,107
K 106,108,109
K 107,110,111
ds_write_b128 v2, v[104:107] offset:1024
K 136,136,137
Zvav 156,156,216
K 137,138,139
K 138,140,141
K 139,142,143
ds_write_b128 v2, v[136:139] offset:5120
K 112,112,113
K 113,114,115
Zvav 124,156,208
K 114,116,117
K 115,118,119
ds_write_b128 v2, v[112:115] offset:2048
K 144,144,145
K 145,146,147
K 146,148,149
Zvav 92,156,200
K 147,150,151
ds_write_b128 v2, v[144:147] offset:6144
Zvav 60,156,192
K 120,120,121
K 121,122,123
K 122,124,125
K 123,126,127
ds_write_b128 v2, v[120:123] offset:3072
K 152,152,153
K 153,154,155
K 154,156,157
K 155,158,159
ds_write_b128 v2, v[152:155] offset:7168
s_waitcnt vmcnt(0)
s_and_b64 s[86:87], s[88:89], s[24:25]
s_cbranch_scc1 .LL_50%=
v_mov_b64 v[28:29], s[12:13]
s_mov_b64 exec, 1
buffer_store_dwordx2 v[28:29], off, s[8:11], s69 sc1
s_mov_b64 exec, -1
.LL_50%=:
s_mov_b32 s70, 0
s_branch .Lend_43%=
.p2align 12
.Lpd_32%=:
v_pk_mul_f32 v[32:33], v[32:33], s[22:23]
v_pk_mul_f32 v[34:35], v[34:35], s[22:23]
v_pk_mul_f32 v[36:37], v[36:37], s[22:23]
v_pk_mul_f32 v[38:39], v[38:39], s[22:23]
v_pk_mul_f32 v[64:65], v[64:65], s[22:23]
v_pk_mul_f32 v[66:67], v[66:67], s[22:23]
v_pk_mul_f32 v[68:69], v[68:69], s[22:23]
v_pk_mul_f32 v[70:71], v[70:71], s[22:23]
v_pk_mul_f32 v[96:97], v[96:97], s[22:23]
v_pk_mul_f32 v[98:99], v[98:99], s[22:23]
v_pk_mul_f32 v[100:101], v[100:101], s[22:23]
v_pk_mul_f32 v[102:103], v[102:103], s[22:23]
v_pk_mul_f32 v[128:129], v[128:129], s[22:23]
v_pk_mul_f32 v[130:131], v[130:131], s[22:23]
v_pk_mul_f32 v[132:133], v[132:133], s[22:23]
v_pk_mul_f32 v[134:135], v[134:135], s[22:23]
v_pk_mul_f32 v[40:41], v[40:41], s[22:23]
v_pk_mul_f32 v[42:43], v[42:43], s[22:23]
v_pk_mul_f32 v[44:45], v[44:45], s[22:23]
v_pk_mul_f32 v[46:47], v[46:47], s[22:23]
v_pk_mul_f32 v[72:73], v[72:73], s[22:23]
v_pk_mul_f32 v[74:75], v[74:75], s[22:23]
v_pk_mul_f32 v[76:77], v[76:77], s[22:23]
v_pk_mul_f32 v[78:79], v[78:79], s[22:23]
v_pk_mul_f32 v[104:105], v[104:105], s[22:23]
v_pk_mul_f32 v[106:107], v[106:107], s[22:23]
v_pk_mul_f32 v[108:109], v[108:109], s[22:23]
v_pk_mul_f32 v[110:111], v[110:111], s[22:23]
v_pk_mul_f32 v[136:137], v[136:137], s[22:23]
v_pk_mul_f32 v[138:139], v[138:139], s[22:23]
v_pk_mul_f32 v[140:141], v[140:141], s[22:23]
v_pk_mul_f32 v[142:143], v[142:143], s[22:23]
v_pk_mul_f32 v[48:49], v[48:49], s[22:23]
v_pk_mul_f32 v[50:51], v[50:51], s[22:23]
v_pk_mul_f32 v[52:53], v[52:53], s[22:23]
v_pk_mul_f32 v[54:55], v[54:55], s[22:23]
v_pk_mul_f32 v[80:81], v[80:81], s[22:23]
v_pk_mul_f32 v[82:83], v[82:83], s[22:23]
v_pk_mul_f32 v[84:85], v[84:85], s[22:23]
v_pk_mul_f32 v[86:87], v[86:87], s[22:23]
v_pk_mul_f32 v[112:113], v[112:113], s[22:23]
v_pk_mul_f32 v[114:115], v[114:115], s[22:23]
v_pk_mul_f32 v[116:117], v[116:117], s[22:23]
v_pk_mul_f32 v[118:119], v[118:119], s[22:23]
v_pk_mul_f32 v[144:145], v[144:145], s[22:23]
v_pk_mul_f32 v[146:147], v[146:147], s[22:23]
v_pk_mul_f32 v[148:149], v[148:149], s[22:23]
v_pk_mul_f32 v[150:151], v[150:151], s[22:23]
v_pk_mul_f32 v[56:57], v[56:57], s[22:23]
v_pk_mul_f32 v[58:59], v[58:59], s[22:23]
v_pk_mul_f32 v[60:61], v[60:61], s[22:23]
v_pk_mul_f32 v[62:63], v[62:63], s[22:23]
v_pk_mul_f32 v[88:89], v[88:89], s[22:23]
v_pk_mul_f32 v[90:91], v[90:91], s[22:23]
v_pk_mul_f32 v[92:93], v[92:93], s[22:23]
v_pk_mul_f32 v[94:95], v[94:95], s[22:23]
v_pk_mul_f32 v[120:121], v[120:121], s[22:23]
v_pk_mul_f32 v[122:123], v[122:123], s[22:23]
v_pk_mul_f32 v[124:125], v[124:125], s[22:23]
v_pk_mul_f32 v[126:127], v[126:127], s[22:23]
v_pk_mul_f32 v[152:153], v[152:153], s[22:23]
v_pk_mul_f32 v[154:155], v[154:155], s[22:23]
v_pk_mul_f32 v[156:157], v[156:157], s[22:23]
v_pk_mul_f32 v[158:159], v[158:159], s[22:23]
K 32,32,33
K 33,34,35
K 34,36,37
K 35,38,39
K 40,40,41
K 41,42,43
K 42,44,45
K 43,46,47
W 0
Zvav 196,128,32
s_add_u32 m0, s62, 0
Ra 144,242,4096
Ra 148,242,5120
Ra 152,242,6144
Ra 156,242,7168
Mvav 196,132,40
K 64,64,65
K 65,66,67
K 66,68,69
K 67,70,71
K 72,72,73
K 73,74,75
K 74,76,77
K 75,78,79
s_nop 1
Zvav 204,132,72
Dc 1,8,61,0
K 96,96,97
K 97,98,99
K 98,100,101
Mvav 204,128,64
K 99,102,103
K 104,104,105
K 105,106,107
K 106,108,109
K 107,110,111
Zvav 212,128,96
Dc 1,8,61,1024
K 128,128,129
K 129,130,131
K 130,132,133
Mvav 212,132,104
K 131,134,135
K 136,136,137
K 137,138,139
K 138,140,141
K 139,142,143
s_nop 1
Zvav 220,132,136
Dc 1,8,61,2048
K 48,48,49
K 49,50,51
K 50,52,53
Mvav 220,128,128
K 51,54,55
K 56,56,57
K 57,58,59
K 58,60,61
K 59,62,63
Mvav 196,136,48
Dc 1,8,61,3072
s_add_u32 m0, s62, 4096
Ra 128,242,8192
Ra 132,242,9216
K 80,80,81
Mvav 196,140,56
K 81,82,83
K 82,84,85
K 83,86,87
K 88,88,89
K 89,90,91
K 90,92,93
K 91,94,95
s_nop 1
Mvav 204,140,88
Dc 243,8,61,0
K 112,112,113
K 113,114,115
K 114,116,117
Mvav 204,136,80
K 115,118,119
K 120,120,121
K 121,122,123
K 122,124,125
K 123,126,127
Mvav 212,136,112
Dc 243,8,61,1024
K 144,144,145
K 145,146,147
K 146,148,149
Mvav 212,140,120
K 147,150,151
K 152,152,153
K 153,154,155
K 154,156,157
K 155,158,159
s_nop 1
Mvav 220,140,152
Dc 243,8,61,2048
v_accvgpr_write_b32 a0, v32
v_accvgpr_write_b32 a1, v33
v_accvgpr_write_b32 a2, v34
Mvav 220,136,144
W 2
Dc 243,8,61,3072
s_add_u32 m0, s62, 8192
v_accvgpr_write_b32 a3, v35
v_accvgpr_write_b32 a4, v40
Zvav 68,144,64
Dc 244,8,61,0
v_accvgpr_write_b32 a5, v41
v_accvgpr_write_b32 a6, v42
v_accvgpr_write_b32 a7, v43
Mvav 68,148,72
Dc 244,8,61,1024
v_accvgpr_write_b32 a8, v48
v_accvgpr_write_b32 a9, v49
v_accvgpr_write_b32 a10, v50
Zvav 100,148,104
Dc 244,8,61,2048
Ra 136,242,10240
Ra 140,242,11264
v_accvgpr_write_b32 a11, v51
Mvav 100,144,96
Dc 244,8,61,3072
s_add_u32 m0, s62, 12288
v_accvgpr_write_b32 a12, v56
v_accvgpr_write_b32 a13, v57
v_accvgpr_write_b32 a14, v58
Zvav 132,144,128
Dc 245,8,61,0
v_accvgpr_write_b32 a15, v59
v_accvgpr_write_b32 a16, v64
v_cndmask_b32_e64 v196, v241, v196, s[72:73]
Mvav 132,148,136
Dc 245,8,61,1024
v_cndmask_b32_e64 v197, v241, v197, s[74:75]
v_cndmask_b32_e64 v198, v241, v198, s[76:77]
v_cndmask_b32_e64 v199, v241, v199, s[78:79]
Mvav 68,152,80
Dc 245,8,61,2048
v_max3_f32 v4, v196, v197, v198
v_max3_f32 v4, v4, v199, v252
v_max3_f32 v5, v204, v205, v206
Mvav 68,156,88
v_max3_f32 v5, v5, v207, v253
v_max3_f32 v6, v212, v213, v214
v_max3_f32 v6, v6, v215, v254
v_max3_f32 v7, v220, v221, v222
Mvav 100,156,120
v_max3_f32 v7, v7, v223, v255
v_mov_b32 v8, v4
v_mov_b32 v9, v5
v_mov_b32 v10, v6
Mvav 100,152,112
v_mov_b32 v11, v7
v_permlane32_swap_b32 v4, v8
v_permlane32_swap_b32 v5, v9
v_accvgpr_write_b32 a17, v65
Mvav 132,152,144
v_permlane32_swap_b32 v6, v10
v_permlane32_swap_b32 v7, v11
v_max_f32 v4, v4, v8
v_max_f32 v5, v5, v9
v_max_f32 v6, v6, v10
v_max_f32 v7, v7, v11
v_mov_b32 v8, v4
v_mov_b32 v9, v5
v_mov_b32 v10, v6
v_mov_b32 v11, v7
v_permlane16_swap_b32 v4, v8
v_permlane16_swap_b32 v5, v9
v_permlane16_swap_b32 v6, v10
v_permlane16_swap_b32 v7, v11
v_max_f32 v4, v4, v8
v_max_f32 v5, v5, v9
v_max_f32 v6, v6, v10
v_max_f32 v7, v7, v11
v_sub_f32 v4, 0, v4
v_sub_f32 v5, 0, v5
v_sub_f32 v6, 0, v6
v_sub_f32 v7, 0, v7
v_mov_b32 v224, v4
v_mov_b32 v225, v4
v_mov_b32 v226, v4
v_mov_b32 v227, v4
v_mov_b32 v228, v5
v_mov_b32 v229, v5
v_mov_b32 v230, v5
v_mov_b32 v231, v5
v_mov_b32 v232, v6
v_mov_b32 v233, v6
v_mov_b32 v234, v6
v_mov_b32 v235, v6
v_mov_b32 v236, v7
v_mov_b32 v237, v7
v_mov_b32 v238, v7
v_mov_b32 v239, v7
Mvav 132,156,152
W 0
Dc 245,8,61,3072
v_accvgpr_write_b32 a18, v66
v_accvgpr_write_b32 a19, v67
v_accvgpr_write_b32 a20, v72
Cvav 168,128,96,232
s_add_u32 s61, s61, s63
s_add_u32 s36, s29, 65536
v_accvgpr_write_b32 a21, v73
v_accvgpr_write_b32 a22, v74
v_accvgpr_write_b32 a23, v75
Mvav 168,132,104
s_and_b32 s80, s14, 1
buffer_store_dwordx4 a[192:195], v1, s[8:11], s36 offen offset:0 sc1
v_accvgpr_write_b32 a24, v80
v_accvgpr_write_b32 a25, v81
v_accvgpr_write_b32 a26, v82
Cvav 172,132,136,236
s_lshl_b32 s80, s80, 14
Ra 144,242,12288
Ra 148,242,13312
Ra 152,242,14336
Ra 156,242,15360
Mvav 172,128,128
s_add_u32 s80, s80, 98304
v_accvgpr_write_b32 a27, v83
v_accvgpr_write_b32 a28, v88
v_accvgpr_write_b32 a29, v89
v_accvgpr_write_b32 a30, v90
Mvav 168,136,112
buffer_store_dwordx4 a[200:203], v1, s[8:11], s36 offen offset:1024 sc1
v_accvgpr_write_b32 a31, v91
v_accvgpr_write_b32 a32, v96
v_accvgpr_write_b32 a33, v97
Mvav 168,140,120
v_accvgpr_write_b32 a34, v98
v_accvgpr_write_b32 a35, v99
v_accvgpr_write_b32 a36, v104
v_accvgpr_write_b32 a37, v105
Mvav 172,140,152
Ra 128,242,24576
Ra 132,242,25600
buffer_store_dwordx4 a[208:211], v1, s[8:11], s36 offen offset:2048 sc1
v_accvgpr_write_b32 a38, v106
Mvav 172,136,144
Ra 136,242,26624
Ra 140,242,27648
v_accvgpr_write_b32 a39, v107
W 4
Cvav 188,144,128,236
v_accvgpr_write_b32 a40, v112
v_accvgpr_write_b32 a41, v113
v_accvgpr_write_b32 a42, v114
v_accvgpr_write_b32 a43, v115
v_accvgpr_write_b32 a44, v120
v_accvgpr_write_b32 a45, v121
v_accvgpr_write_b32 a46, v122
v_accvgpr_write_b32 a47, v123
v_accvgpr_write_b32 a48, v128
v_accvgpr_write_b32 a49, v129
v_accvgpr_write_b32 a50, v130
v_accvgpr_write_b32 a51, v131
v_accvgpr_write_b32 a52, v136
v_accvgpr_write_b32 a53, v137
v_accvgpr_write_b32 a54, v138
v_accvgpr_write_b32 a55, v139
v_accvgpr_write_b32 a56, v144
v_accvgpr_write_b32 a57, v145
v_accvgpr_write_b32 a58, v146
v_accvgpr_write_b32 a59, v147
v_accvgpr_write_b32 a60, v152
v_accvgpr_write_b32 a61, v153
v_accvgpr_write_b32 a62, v154
v_accvgpr_write_b32 a63, v155
Mvav 188,148,136
s_barrier
buffer_store_dwordx4 a[216:219], v1, s[8:11], s36 offen offset:3072 sc1
s_add_u32 s36, s29, 69632
v_add_u32 v8, s80, v1
A 196,196,224
Mvav 188,152,144
A 197,197,224
A 198,198,224
A 199,199,224
X 196
X 197
)"
R"(X 198
K 192,196,197
X 199
A 204,204,228
K 193,198,199
A 205,205,228
A 206,206,228
A 207,207,228
X 204
X 205
X 206
K 200,204,205
X 207
A 212,212,232
K 201,206,207
A 213,213,232
A 214,214,232
A 215,215,232
X 212
X 213
X 214
K 208,212,213
X 215
A 220,220,236
K 209,214,215
A 221,221,236
A 222,222,236
A 223,223,236
X 220
X 221
X 222
K 216,220,221
X 223
v_mov_b32 v194, 0
K 217,222,223
v_mov_b32 v195, 0
v_cndmask_b32_e64 v68, v241, v68, s[72:73]
v_cndmask_b32_e64 v69, v241, v69, s[74:75]
v_cndmask_b32_e64 v70, v241, v70, s[76:77]
v_cndmask_b32_e64 v71, v241, v71, s[78:79]
A 68,68,228
A 69,69,228
A 70,70,228
A 71,71,228
X 68
X 69
X 70
K 202,68,69
X 71
A 100,100,232
K 203,70,71
A 101,101,232
A 102,102,232
A 103,103,232
X 100
X 101
X 102
K 210,100,101
X 103
A 132,132,236
K 211,102,103
A 133,133,236
A 134,134,236
A 135,135,236
X 132
X 133
X 134
K 218,132,133
X 135
s_nop 0
K 219,134,135
Mvav 188,156,152
W 0
buffer_store_dwordx4 a[224:227], v1, s[8:11], s36 offen offset:0 sc1
Ra 144,242,28672
Ra 148,242,29696
Ra 152,242,30720
Zvav 32,128,192
buffer_store_dwordx4 a[232:235], v1, s[8:11], s36 offen offset:1024 sc1
Ra 156,242,31744
Zvav 64,128,200
buffer_store_dwordx4 a[240:243], v1, s[8:11], s36 offen offset:2048 sc1
Zvav 96,128,208
s_and_b32 s80, s14, 1
s_xor_b32 s62, s62, 0x10000
buffer_store_dwordx4 a[248:251], v1, s[8:11], s36 offen offset:3072 sc1
s_add_u32 s36, s29, 393216
Zvav 128,128,216
s_lshl_b32 s80, s80, 14
s_add_u32 s80, s80, 131072
buffer_store_dwordx4 a[196:199], v1, s[8:11], s36 offen offset:0 sc1
v_add_u32 v23, 61440, v21
Zvav 132,132,216
buffer_store_dwordx4 a[204:207], v1, s[8:11], s36 offen offset:1024 sc1
v_add_u32 v12, s80, v1
Ra 64,12,8192
v_add_u32 v24, 61440, v22
Zvav 100,132,208
buffer_store_dwordx4 a[212:215], v1, s[8:11], s36 offen offset:2048 sc1
Ra 68,12,9216
Ra 72,12,10240
v_add_u32 v25, 122880, v21
Zvav 68,132,200
buffer_store_dwordx4 a[220:223], v1, s[8:11], s36 offen offset:3072 sc1
s_add_u32 s36, s29, 397312
Ra 76,12,11264
Ra 80,12,12288
Ra 84,12,13312
Zvav 36,132,192
buffer_store_dwordx4 a[228:231], v1, s[8:11], s36 offen offset:0 sc1
Ra 88,12,14336
Ra 92,12,15360
v_add_u32 v26, 122880, v22
Zvav 40,136,192
buffer_store_dwordx4 a[236:239], v1, s[8:11], s36 offen offset:1024 sc1
Ra 96,12,0
Ra 100,12,1024
Ra 104,12,2048
Zvav 72,136,200
Ra 108,12,3072
Ra 112,12,4096
buffer_store_dwordx4 a[244:247], v1, s[8:11], s36 offen offset:2048 sc1
Ra 116,12,5120
Zvav 104,136,208
Ra 120,12,6144
Ra 124,12,7168
buffer_store_dwordx4 a[252:255], v1, s[8:11], s36 offen offset:3072 sc1
Zvav 136,136,216
Zvav 140,140,216
Zvav 108,140,208
Zvav 76,140,200
s_waitcnt vmcnt(16) lgkmcnt(0)
s_barrier
v_xor_b32 v242, 0x10000, v242
ds_read_b128 v[248:251], v8
Zvav 44,140,192
Ra 128,242,40960
Zvav 48,144,192
Zvav 80,144,200
X 172
X 173
Zvav 112,144,208
Ra 132,242,41984
X 174
K 220,172,173
Zvav 144,144,216
X 175
v_cndmask_b32_e64 v168, v241, v168, s[72:73]
K 221,174,175
Zvav 148,148,216
v_cndmask_b32_e64 v169, v241, v169, s[74:75]
v_cndmask_b32_e64 v170, v241, v170, s[76:77]
v_cndmask_b32_e64 v171, v241, v171, s[78:79]
Zvav 116,148,208
X 168
X 169
Zvav 84,148,200
W 2
Ra 136,242,43008
X 170
K 212,168,169
Zvav 52,148,192
v_mov_b32 v224, v250
v_mov_b32 v225, v250
v_mov_b32 v226, v250
v_mov_b32 v227, v250
Zvav 56,152,192
v_mov_b32 v228, v251
v_mov_b32 v229, v251
v_mov_b32 v230, v251
v_mov_b32 v231, v251
Zvav 88,152,200
v_mov_b32 v232, v248
v_mov_b32 v233, v248
v_mov_b32 v234, v248
v_mov_b32 v235, v248
Zvav 120,152,208
v_mov_b32 v236, v249
v_mov_b32 v237, v249
v_mov_b32 v238, v249
v_mov_b32 v239, v249
Zvav 152,152,216
X 171
v_mov_b32 v204, 0
K 213,170,171
Zvav 156,156,216
Ra 140,242,44032
v_mov_b32 v205, 0
v_mov_b32 v196, 0
v_mov_b32 v197, 0
Zvav 124,156,208
v_mov_b32 v198, 0
v_mov_b32 v199, 0
v_mov_b32 v206, 0
v_mov_b32 v207, 0
Zvav 92,156,200
v_mov_b32 v214, 0
v_mov_b32 v215, 0
v_cndmask_b32_e64 v188, v241, v188, s[72:73]
v_cndmask_b32_e64 v189, v241, v189, s[74:75]
Zvav 60,156,192
v_cndmask_b32_e64 v190, v241, v190, s[76:77]
v_cndmask_b32_e64 v191, v241, v191, s[78:79]
X 188
X 189
X 190
K 222,188,189
X 191
s_nop 0
K 223,190,191
s_waitcnt vmcnt(0)
s_add_u32 s80, s71, 128
v_mov_b64 v[28:29], s[12:13]
s_mov_b64 exec, 1
buffer_store_dwordx2 v[28:29], off, s[8:11], s80 sc1
s_mov_b64 exec, -1
s_nop 1
s_branch .Lsp2_37%=
.p2align 12
.Lpe_48%=:
v_pk_mul_f32 v[32:33], v[32:33], s[22:23]
v_pk_mul_f32 v[34:35], v[34:35], s[22:23]
v_pk_mul_f32 v[36:37], v[36:37], s[22:23]
v_pk_mul_f32 v[38:39], v[38:39], s[22:23]
v_pk_mul_f32 v[64:65], v[64:65], s[22:23]
v_pk_mul_f32 v[66:67], v[66:67], s[22:23]
v_pk_mul_f32 v[68:69], v[68:69], s[22:23]
v_pk_mul_f32 v[70:71], v[70:71], s[22:23]
v_pk_mul_f32 v[96:97], v[96:97], s[22:23]
v_pk_mul_f32 v[98:99], v[98:99], s[22:23]
v_pk_mul_f32 v[100:101], v[100:101], s[22:23]
v_pk_mul_f32 v[102:103], v[102:103], s[22:23]
v_pk_mul_f32 v[128:129], v[128:129], s[22:23]
v_pk_mul_f32 v[130:131], v[130:131], s[22:23]
v_pk_mul_f32 v[132:133], v[132:133], s[22:23]
v_pk_mul_f32 v[134:135], v[134:135], s[22:23]
v_pk_mul_f32 v[40:41], v[40:41], s[22:23]
v_pk_mul_f32 v[42:43], v[42:43], s[22:23]
v_pk_mul_f32 v[44:45], v[44:45], s[22:23]
v_pk_mul_f32 v[46:47], v[46:47], s[22:23]
v_pk_mul_f32 v[72:73], v[72:73], s[22:23]
v_pk_mul_f32 v[74:75], v[74:75], s[22:23]
v_pk_mul_f32 v[76:77], v[76:77], s[22:23]
v_pk_mul_f32 v[78:79], v[78:79], s[22:23]
v_pk_mul_f32 v[104:105], v[104:105], s[22:23]
v_pk_mul_f32 v[106:107], v[106:107], s[22:23]
v_pk_mul_f32 v[108:109], v[108:109], s[22:23]
v_pk_mul_f32 v[110:111], v[110:111], s[22:23]
v_pk_mul_f32 v[136:137], v[136:137], s[22:23]
v_pk_mul_f32 v[138:139], v[138:139], s[22:23]
v_pk_mul_f32 v[140:141], v[140:141], s[22:23]
v_pk_mul_f32 v[142:143], v[142:143], s[22:23]
v_pk_mul_f32 v[48:49], v[48:49], s[22:23]
v_pk_mul_f32 v[50:51], v[50:51], s[22:23]
v_pk_mul_f32 v[52:53], v[52:53], s[22:23]
v_pk_mul_f32 v[54:55], v[54:55], s[22:23]
v_pk_mul_f32 v[80:81], v[80:81], s[22:23]
v_pk_mul_f32 v[82:83], v[82:83], s[22:23]
v_pk_mul_f32 v[84:85], v[84:85], s[22:23]
v_pk_mul_f32 v[86:87], v[86:87], s[22:23]
v_pk_mul_f32 v[112:113], v[112:113], s[22:23]
v_pk_mul_f32 v[114:115], v[114:115], s[22:23]
v_pk_mul_f32 v[116:117], v[116:117], s[22:23]
v_pk_mul_f32 v[118:119], v[118:119], s[22:23]
v_pk_mul_f32 v[144:145], v[144:145], s[22:23]
v_pk_mul_f32 v[146:147], v[146:147], s[22:23]
v_pk_mul_f32 v[148:149], v[148:149], s[22:23]
v_pk_mul_f32 v[150:151], v[150:151], s[22:23]
v_pk_mul_f32 v[56:57], v[56:57], s[22:23]
v_pk_mul_f32 v[58:59], v[58:59], s[22:23]
v_pk_mul_f32 v[60:61], v[60:61], s[22:23]
v_pk_mul_f32 v[62:63], v[62:63], s[22:23]
v_pk_mul_f32 v[88:89], v[88:89], s[22:23]
v_pk_mul_f32 v[90:91], v[90:91], s[22:23]
v_pk_mul_f32 v[92:93], v[92:93], s[22:23]
v_pk_mul_f32 v[94:95], v[94:95], s[22:23]
v_pk_mul_f32 v[120:121], v[120:121], s[22:23]
v_pk_mul_f32 v[122:123], v[122:123], s[22:23]
v_pk_mul_f32 v[124:125], v[124:125], s[22:23]
v_pk_mul_f32 v[126:127], v[126:127], s[22:23]
v_pk_mul_f32 v[152:153], v[152:153], s[22:23]
v_pk_mul_f32 v[154:155], v[154:155], s[22:23]
v_pk_mul_f32 v[156:157], v[156:157], s[22:23]
v_pk_mul_f32 v[158:159], v[158:159], s[22:23]
K 32,32,33
K 33,34,35
K 34,36,37
K 35,38,39
K 40,40,41
K 41,42,43
K 42,44,45
K 43,46,47
W 0
Zvav 196,128,32
s_add_u32 m0, s62, 0
Ra 144,242,4096
Ra 148,242,5120
Ra 152,242,6144
Ra 156,242,7168
Mvav 196,132,40
K 64,64,65
K 65,66,67
K 66,68,69
K 67,70,71
K 72,72,73
K 73,74,75
K 74,76,77
K 75,78,79
s_nop 1
Zvav 204,132,72
Dc 1,8,61,0
K 96,96,97
K 97,98,99
K 98,100,101
Mvav 204,128,64
K 99,102,103
K 104,104,105
K 105,106,107
K 106,108,109
K 107,110,111
Zvav 212,128,96
K 128,128,129
K 129,130,131
K 130,132,133
K 131,134,135
Mvav 212,132,104
K 136,136,137
K 137,138,139
K 138,140,141
K 139,142,143
s_nop 1
Zvav 220,132,136
Dc 1,8,61,1024
K 48,48,49
K 49,50,51
K 50,52,53
Mvav 220,128,128
K 51,54,55
K 56,56,57
K 57,58,59
K 58,60,61
K 59,62,63
Mvav 196,136,48
Ra 128,242,8192
Ra 132,242,9216
K 80,80,81
K 81,82,83
Mvav 196,140,56
K 82,84,85
K 83,86,87
K 88,88,89
K 89,90,91
K 90,92,93
K 91,94,95
s_nop 1
Mvav 204,140,88
Dc 1,8,61,2048
K 112,112,113
K 113,114,115
K 114,116,117
Mvav 204,136,80
K 115,118,119
K 120,120,121
K 121,122,123
K 122,124,125
K 123,126,127
Mvav 212,136,112
K 144,144,145
K 145,146,147
K 146,148,149
K 147,150,151
Mvav 212,140,120
K 152,152,153
K 153,154,155
K 154,156,157
K 155,158,159
s_nop 1
Mvav 220,140,152
Dc 1,8,61,3072
s_add_u32 m0, s62, 4096
v_accvgpr_write_b32 a0, v32
v_accvgpr_write_b32 a1, v33
v_accvgpr_write_b32 a2, v34
Mvav 220,136,144
W 2
Ra 136,242,10240
v_accvgpr_write_b32 a3, v35
v_accvgpr_write_b32 a4, v40
Zvav 36,144,32
Ra 140,242,11264
v_accvgpr_write_b32 a5, v41
Mvav 36,148,40
v_accvgpr_write_b32 a6, v42
v_accvgpr_write_b32 a7, v43
v_max3_f32 v4, v196, v197, v198
v_max3_f32 v4, v4, v199, v252
Zvav 68,148,72
v_max3_f32 v5, v204, v205, v206
v_max3_f32 v5, v5, v207, v253
v_max3_f32 v6, v212, v213, v214
)"
R"(v_max3_f32 v6, v6, v215, v254
Mvav 68,144,64
v_max3_f32 v7, v220, v221, v222
v_max3_f32 v7, v7, v223, v255
v_mov_b32 v8, v4
v_mov_b32 v9, v5
Zvav 100,144,96
Dc 243,8,61,0
v_mov_b32 v10, v6
v_mov_b32 v11, v7
v_permlane32_swap_b32 v4, v8
Mvav 100,148,104
v_permlane32_swap_b32 v5, v9
v_permlane32_swap_b32 v6, v10
v_permlane32_swap_b32 v7, v11
v_max_f32 v4, v4, v8
Zvav 132,148,136
v_max_f32 v5, v5, v9
v_max_f32 v6, v6, v10
v_max_f32 v7, v7, v11
v_mov_b32 v8, v4
Mvav 132,144,128
v_mov_b32 v9, v5
v_mov_b32 v10, v6
v_mov_b32 v11, v7
v_accvgpr_write_b32 a8, v48
Mvav 36,152,48
v_permlane16_swap_b32 v4, v8
v_permlane16_swap_b32 v5, v9
v_permlane16_swap_b32 v6, v10
v_permlane16_swap_b32 v7, v11
Mvav 36,156,56
v_max_f32 v4, v4, v8
v_max_f32 v5, v5, v9
v_max_f32 v6, v6, v10
v_max_f32 v7, v7, v11
Mvav 68,156,88
v_sub_f32 v4, 0, v4
v_sub_f32 v5, 0, v5
v_sub_f32 v6, 0, v6
v_sub_f32 v7, 0, v7
Mvav 68,152,80
v_mov_b32 v224, v4
v_mov_b32 v225, v4
v_mov_b32 v226, v4
v_mov_b32 v227, v4
Mvav 100,152,112
v_mov_b32 v228, v5
v_mov_b32 v229, v5
v_mov_b32 v230, v5
v_mov_b32 v231, v5
Mvav 100,156,120
v_mov_b32 v232, v6
v_mov_b32 v233, v6
v_mov_b32 v234, v6
v_mov_b32 v235, v6
Mvav 132,156,152
v_mov_b32 v236, v7
v_mov_b32 v237, v7
v_mov_b32 v238, v7
v_mov_b32 v239, v7
Mvav 132,152,144
W 0
v_accvgpr_write_b32 a9, v49
v_accvgpr_write_b32 a10, v50
v_accvgpr_write_b32 a11, v51
v_accvgpr_write_b32 a12, v56
Cvav 160,128,32,224
s_and_b32 s80, s14, 1
Dc 243,8,61,1024
v_accvgpr_write_b32 a13, v57
v_accvgpr_write_b32 a14, v58
v_accvgpr_write_b32 a15, v59
Mvav 160,132,40
s_lshl_b32 s80, s80, 14
Ra 144,242,12288
Ra 148,242,13312
v_accvgpr_write_b32 a16, v64
v_accvgpr_write_b32 a17, v65
Cvav 164,132,72,228
Dc 243,8,61,2048
v_accvgpr_write_b32 a18, v66
v_accvgpr_write_b32 a19, v67
v_accvgpr_write_b32 a20, v72
Mvav 164,128,64
s_add_u32 s80, s80, 98304
v_accvgpr_write_b32 a21, v73
v_accvgpr_write_b32 a22, v74
v_accvgpr_write_b32 a23, v75
v_accvgpr_write_b32 a24, v80
Cvav 168,128,96,232
Dc 243,8,61,3072
s_add_u32 s61, s61, s63
v_accvgpr_write_b32 a25, v81
v_accvgpr_write_b32 a26, v82
A 196,196,224
Mvav 168,132,104
s_add_u32 s36, s29, 65536
v_accvgpr_write_b32 a27, v83
A 197,197,224
A 198,198,224
v_accvgpr_write_b32 a28, v88
Cvav 172,132,136,236
Ra 152,242,14336
buffer_store_dwordx4 a[192:195], v1, s[8:11], s36 offen offset:0 sc1
A 199,199,224
v_accvgpr_write_b32 a29, v89
Mvav 172,128,128
Ra 156,242,15360
X 196
v_accvgpr_write_b32 a30, v90
Mvav 160,136,48
X 197
v_accvgpr_write_b32 a31, v91
v_accvgpr_write_b32 a32, v96
Mvav 160,140,56
buffer_store_dwordx4 a[200:203], v1, s[8:11], s36 offen offset:1024 sc1
X 198
K 192,196,197
Mvav 164,140,88
X 199
v_accvgpr_write_b32 a33, v97
A 204,204,228
Mvav 164,136,80
v_accvgpr_write_b32 a34, v98
K 193,198,199
v_accvgpr_write_b32 a35, v99
A 205,205,228
Mvav 168,136,112
A 206,206,228
v_accvgpr_write_b32 a36, v104
v_add_u32 v8, s80, v1
A 207,207,228
Mvav 168,140,120
buffer_store_dwordx4 a[208:211], v1, s[8:11], s36 offen offset:2048 sc1
X 204
v_accvgpr_write_b32 a37, v105
Mvav 172,140,152
X 205
v_accvgpr_write_b32 a38, v106
v_accvgpr_write_b32 a39, v107
Mvav 172,136,144
W 0
X 206
v_accvgpr_write_b32 a40, v112
K 200,204,205
Cvav 176,144,32,224
Ra 128,242,24576
X 207
v_accvgpr_write_b32 a41, v113
Mvav 176,148,40
buffer_store_dwordx4 a[216:219], v1, s[8:11], s36 offen offset:3072 sc1
s_add_u32 s36, s29, 69632
Ra 132,242,25600
A 212,212,232
v_accvgpr_write_b32 a42, v114
Cvav 180,148,72,228
K 201,206,207
A 213,213,232
v_accvgpr_write_b32 a43, v115
A 214,214,232
Mvav 180,144,64
A 215,215,232
v_accvgpr_write_b32 a44, v120
X 212
Cvav 184,144,96,232
buffer_store_dwordx4 a[224:227], v1, s[8:11], s36 offen offset:0 sc1
v_accvgpr_write_b32 a45, v121
X 213
Mvav 184,148,104
v_accvgpr_write_b32 a46, v122
X 214
v_accvgpr_write_b32 a47, v123
Cvav 188,148,136,236
K 208,212,213
v_accvgpr_write_b32 a48, v128
X 215
Mvav 188,144,128
buffer_store_dwordx4 a[232:235], v1, s[8:11], s36 offen offset:1024 sc1
v_accvgpr_write_b32 a49, v129
A 220,220,236
v_accvgpr_write_b32 a50, v130
Mvav 176,152,48
K 209,214,215
A 221,221,236
v_accvgpr_write_b32 a51, v131
A 222,222,236
Mvav 176,156,56
Ra 136,242,26624
A 223,223,236
v_accvgpr_write_b32 a52, v136
v_accvgpr_write_b32 a53, v137
Mvav 180,156,88
Ra 140,242,27648
buffer_store_dwordx4 a[240:243], v1, s[8:11], s36 offen offset:2048 sc1
X 220
Mvav 180,152,80
X 221
v_accvgpr_write_b32 a54, v138
v_accvgpr_write_b32 a55, v139
Mvav 184,152,112
v_accvgpr_write_b32 a56, v144
v_accvgpr_write_b32 a57, v145
v_accvgpr_write_b32 a58, v146
v_accvgpr_write_b32 a59, v147
v_accvgpr_write_b32 a60, v152
v_accvgpr_write_b32 a61, v153
v_accvgpr_write_b32 a62, v154
v_accvgpr_write_b32 a63, v155
Mvav 184,156,120
s_barrier
buffer_store_dwordx4 a[248:251], v1, s[8:11], s36 offen offset:3072 sc1
X 222
K 216,220,221
Mvav 188,156,152
X 223
A 36,36,224
K 217,222,223
A 37,37,224
A 38,38,224
A 39,39,224
X 36
X 37
X 38
K 194,36,37
X 39
A 68,68,228
K 195,38,39
A 69,69,228
A 70,70,228
A 71,71,228
X 68
X 69
X 70
K 202,68,69
X 71
A 100,100,232
K 203,70,71
A 101,101,232
A 102,102,232
A 103,103,232
X 100
X 101
X 102
K 210,100,101
X 103
A 132,132,236
K 211,102,103
A 133,133,236
A 134,134,236
A 135,135,236
X 132
X 133
X 134
K 218,132,133
X 135
s_nop 0
K 219,134,135
Mvav 188,152,144
W 0
s_add_u32 s36, s29, 393216
buffer_store_dwordx4 a[196:199], v1, s[8:11], s36 offen offset:0 sc1
Ra 144,242,28672
Ra 148,242,29696
Zvav 32,128,192
buffer_store_dwordx4 a[204:207], v1, s[8:11], s36 offen offset:1024 sc1
Ra 152,242,30720
Ra 156,242,31744
Zvav 64,128,200
buffer_store_dwordx4 a[212:215], v1, s[8:11], s36 offen offset:2048 sc1
Zvav 96,128,208
s_and_b32 s80, s14, 1
s_xor_b32 s62, s62, 0x10000
buffer_store_dwordx4 a[220:223], v1, s[8:11], s36 offen offset:3072 sc1
s_add_u32 s36, s29, 397312
Zvav 128,128,216
s_lshl_b32 s80, s80, 14
s_add_u32 s80, s80, 131072
buffer_store_dwordx4 a[228:231], v1, s[8:11], s36 offen offset:0 sc1
v_add_u32 v23, 61440, v21
Zvav 132,132,216
v_add_u32 v12, s80, v1
Ra 64,12,8192
Ra 68,12,9216
v_add_u32 v24, 61440, v22
Zvav 100,132,208
Ra 72,12,10240
buffer_store_dwordx4 a[236:239], v1, s[8:11], s36 offen offset:1024 sc1
Ra 76,12,11264
v_add_u32 v25, 122880, v21
Zvav 68,132,200
Ra 80,12,12288
Ra 84,12,13312
Ra 88,12,14336
v_add_u32 v26, 122880, v22
Zvav 36,132,192
Ra 92,12,15360
Ra 96,12,0
Ra 100,12,1024
Ra 104,12,2048
Zvav 40,136,192
buffer_store_dwordx4 a[244:247], v1, s[8:11], s36 offen offset:2048 sc1
Ra 108,12,3072
Ra 112,12,4096
Ra 116,12,5120
Zvav 72,136,200
Ra 120,12,6144
Ra 124,12,7168
X 172
Zvav 104,136,208
X 173
X 174
Zvav 136,136,216
buffer_store_dwordx4 a[252:255], v1, s[8:11], s36 offen offset:3072 sc1
K 220,172,173
X 175
Zvav 140,140,216
X 168
K 221,174,175
Zvav 108,140,208
X 169
X 170
Zvav 76,140,200
s_waitcnt vmcnt(16) lgkmcnt(0)
s_barrier
v_xor_b32 v242, 0x10000, v242
ds_read_b128 v[248:251], v8
Zvav 44,140,192
Ra 128,242,0
K 212,168,169
X 171
Zvav 48,144,192
X 164
K 213,170,171
Zvav 80,144,200
X 165
X 166
Zvav 112,144,208
Ra 132,242,1024
K 204,164,165
X 167
Zvav 144,144,216
X 160
Zvav 148,148,216
K 205,166,167
X 161
Zvav 116,148,208
Ra 136,242,2048
X 162
Zvav 84,148,200
W 3
K 196,160,161
X 163
Zvav 52,148,192
X 176
K 197,162,163
Zvav 56,152,192
Ra 140,242,3072
X 177
Zvav 88,152,200
X 178
K 198,176,177
Zvav 120,152,208
X 179
X 180
Zvav 152,152,216
K 199,178,179
Zvav 156,156,216
X 181
X 182
Zvav 124,156,208
K 206,180,181
X 183
Zvav 92,156,200
K 207,182,183
Zvav 60,156,192
s_waitcnt vmcnt(0)
s_add_u32 s80, s71, 128
v_mov_b64 v[28:29], s[12:13]
s_mov_b64 exec, 1
buffer_store_dwordx2 v[28:29], off, s[8:11], s80 sc1
s_mov_b64 exec, -1
s_branch .Lsp1_36%=
.p2align 12
.Lmoe_44%=:
W 0
Cvaa 160,128,0,224
s_cmp_eq_u32 s14, 0
s_cselect_b32 s20, s30, s32
s_cselect_b32 s21, s31, s33
Ra 144,242,4096
Ra 148,242,5120
Mvaa 160,132,4
s_xor_b32 s62, s62, 0x10000
Ra 152,242,6144
Cvaa 164,132,20,228
X 184
Mvaa 164,128,16
s_and_b32 s21, s21, 0xffff
X 185
)"
R"(Cvaa 168,128,32,232
X 186
Mvaa 168,132,36
Ra 156,242,7168
K 214,184,185
Cvaa 172,132,52,236
s_mov_b32 s22, 2211840
X 187
Mvaa 172,128,48
X 188
Mvaa 160,136,8
Ra 128,242,16384
Mvaa 160,140,12
s_mov_b32 s23, 0x20000
K 215,186,187
Mvaa 164,140,28
X 189
Mvaa 164,136,24
X 190
Mvaa 168,136,40
Ra 132,242,17408
v_lshrrev_b32 v4, 6, v1
Mvaa 168,140,44
K 222,188,189
Mvaa 172,140,60
X 191
Mvaa 172,136,56
v_mul_u32_u24 v4, 3456, v4
K 223,190,191
v_smfmac_f32_16x16x64_bf16 a[160:163], a[176:179], v[192:199], v240
Ra 136,242,18432
v_smfmac_f32_16x16x64_bf16 a[164:167], a[176:179], v[200:207], v240
X 160
v_smfmac_f32_16x16x64_bf16 a[168:171], a[176:179], v[208:215], v240
v_lshrrev_b32 v5, 4, v1
v_smfmac_f32_16x16x64_bf16 a[172:175], a[176:179], v[216:223], v240
W 3
X 161
Cvaa 176,144,0,224
X 162
Mvaa 176,148,4
Ra 140,242,19456
v_xor_b32 v5, v5, v1
Cvaa 180,148,20,228
K 192,160,161
Mvaa 180,144,16
X 163
Cvaa 184,144,32,232
v_and_b32 v5, 32, v5
Mvaa 184,148,36
X 164
Cvaa 188,148,52,236
K 193,162,163
v_and_b32 v6, 16, v1
Mvaa 188,144,48
X 165
Mvaa 176,152,8
X 166
Mvaa 176,156,12
Ra 144,242,20480
Ra 148,242,21504
Mvaa 180,156,28
v_add3_u32 v4, v4, v5, v6
K 200,164,165
Mvaa 180,152,24
X 167
Mvaa 184,152,40
X 168
Mvaa 184,156,44
v_add_u32 v5, 54272, v4
K 201,166,167
Mvaa 188,156,60
X 169
Mvaa 188,152,56
W 2
Ra 152,242,22528
v_add_u32 v6, 108544, v4
Mvav 32,128,196
X 170
Mvav 64,128,204
Ra 156,242,23552
v_add_u32 v7, 162816, v4
Mvav 96,128,212
K 208,168,169
Mvav 128,128,220
X 171
Mvav 132,132,220
s_lshl_b32 s94, s85, 6
X 172
Mvav 100,132,212
s_add_u32 s64, s64, 1
Ra 128,242,8192
K 209,170,171
Mvav 68,132,204
X 173
Mvav 36,132,196
s_mul_i32 s95, s14, 20480
Mvav 40,136,196
X 174
Mvav 72,136,204
Ra 132,242,9216
K 216,172,173
Mvav 104,136,212
s_add_u32 s95, s95, 98304
X 175
Mvav 136,136,220
Mvav 140,140,220
s_add_u32 m0, s95, 0
X 176
Mvav 108,140,212
Ra 136,242,10240
K 217,174,175
Mvav 76,140,204
X 177
Mvav 44,140,196
W 3
D 4,20,94,0
Mvav 48,144,196
X 178
Mvav 80,144,204
Ra 140,242,11264
Mvav 112,144,212
D 5,20,94,1024
K 194,176,177
Mvav 144,144,220
X 179
Mvav 148,148,220
X 180
Mvav 116,148,212
Ra 144,242,12288
D 6,20,94,2048
Mvav 84,148,204
K 195,178,179
Mvav 52,148,196
X 181
Mvav 56,152,196
D 7,20,94,3072
Ra 148,242,13312
Mvav 88,152,204
X 182
Mvav 120,152,212
s_add_u32 s94, s94, 221184
K 202,180,181
Mvav 152,152,220
X 183
Mvav 156,156,220
X 184
Mvav 124,156,212
s_add_u32 m0, s95, 4096
Ra 152,242,14336
Mvav 92,156,204
K 203,182,183
Mvav 60,156,196
W 3
X 185
Cvaa 160,128,0,224
D 4,20,94,0
Ra 156,242,15360
Mvaa 160,132,4
X 186
Cvaa 164,132,20,228
K 210,184,185
Mvaa 164,128,16
X 187
Cvaa 168,128,32,232
D 5,20,94,1024
Mvaa 168,132,36
X 188
Cvaa 172,132,52,236
D 6,20,94,2048
K 211,186,187
Mvaa 172,128,48
X 189
Mvaa 160,136,8
Ra 128,242,24576
Ra 132,242,25600
Mvaa 160,140,12
X 190
Mvaa 164,140,28
D 7,20,94,3072
K 218,188,189
Mvaa 164,136,24
s_add_u32 s94, s94, 221184
X 191
Mvaa 168,136,40
Mvaa 168,140,44
X 160
Mvaa 172,140,60
s_add_u32 m0, s95, 8192
K 219,190,191
Mvaa 172,136,56
W 2
X 161
Cvaa 176,144,0,224
Ra 136,242,26624
Ra 140,242,27648
Mvaa 176,148,4
X 162
Cvaa 180,148,20,228
D 4,20,94,0
K 196,160,161
Mvaa 180,144,16
X 163
Cvaa 184,144,32,232
D 5,20,94,1024
Mvaa 184,148,36
X 164
Cvaa 188,148,52,236
K 197,162,163
Mvaa 188,144,48
D 6,20,94,2048
Ra 144,242,28672
Mvaa 176,152,8
X 165
Mvaa 176,156,12
Ra 148,242,29696
D 7,20,94,3072
Mvaa 180,156,28
X 166
Mvaa 180,152,24
K 204,164,165
Mvaa 184,152,40
s_add_u32 s94, s94, 221184
X 167
Mvaa 184,156,44
X 168
Mvaa 188,156,60
Mvaa 188,152,56
W 2
s_add_u32 m0, s95, 12288
K 205,166,167
Mvav 32,128,192
X 169
Mvav 64,128,200
Ra 152,242,30720
Ra 156,242,31744
Mvav 96,128,208
X 170
Mvav 128,128,216
D 4,20,94,0
K 212,168,169
Mvav 132,132,216
X 171
Mvav 100,132,208
D 5,20,94,1024
Mvav 68,132,200
X 172
Mvav 36,132,192
K 213,170,171
Mvav 40,136,192
X 173
Mvav 72,136,200
D 6,20,94,2048
Mvav 104,136,208
X 174
Mvav 136,136,216
D 7,20,94,3072
K 220,172,173
Mvav 140,140,216
X 175
Mvav 108,140,208
s_add_u32 s94, s94, 221184
X 176
Mvav 76,140,200
W 0
s_barrier
v_xor_b32 v242, 0x10000, v242
Mvav 44,140,192
Ra 128,242,0
K 221,174,175
Mvav 48,144,192
X 177
Mvav 80,144,200
v_bfe_u32 v11, v1, 4, 3
Mvav 112,144,208
X 178
Mvav 144,144,216
Ra 132,242,1024
v_lshlrev_b32 v11, 3, v11
Mvav 148,148,216
K 198,176,177
Mvav 116,148,208
X 179
Mvav 84,148,200
X 180
Mvav 52,148,192
Ra 136,242,2048
buffer_load_dwordx2 v[8:9], v11, s[8:11], 0 offen offset:3840 sc1
Mvav 56,152,192
Ra 140,242,3072
K 199,178,179
Mvav 88,152,200
X 181
Mvav 120,152,208
X 182
Mvav 152,152,216
K 206,180,181
Mvav 156,156,216
X 183
Mvav 124,156,208
Mvav 92,156,200
K 207,182,183
Mvav 60,156,192
W 0
Cvaa 160,128,0,224
s_add_u32 m0, s95, 16384
Ra 144,242,4096
Ra 148,242,5120
Mvaa 160,132,4
s_xor_b32 s62, s62, 0x10000
Ra 152,242,6144
Cvaa 164,132,20,228
X 184
Mvaa 164,128,16
X 185
Cvaa 168,128,32,232
X 186
Mvaa 168,132,36
D 4,20,94,0
K 214,184,185
Cvaa 172,132,52,236
Ra 156,242,7168
Mvaa 172,128,48
X 187
Mvaa 160,136,8
X 188
Mvaa 160,140,12
D 5,20,94,1024
Ra 128,242,16384
Mvaa 164,140,28
K 215,186,187
Mvaa 164,136,24
X 189
Mvaa 168,136,40
X 190
Mvaa 168,140,44
D 6,20,94,2048
Ra 132,242,17408
Mvaa 172,140,60
K 222,188,189
Mvaa 172,136,56
X 191
v_smfmac_f32_16x16x64_bf16 a[160:163], a[176:179], v[192:199], v240
Ra 136,242,18432
K 223,190,191
v_smfmac_f32_16x16x64_bf16 a[164:167], a[176:179], v[200:207], v240
D 7,20,94,3072
v_smfmac_f32_16x16x64_bf16 a[168:171], a[176:179], v[208:215], v240
s_add_u32 s94, s94, 221184
v_mul_u32_u24 v10, 160, v11
v_smfmac_f32_16x16x64_bf16 a[172:175], a[176:179], v[216:223], v240
W 3
X 160
Cvaa 176,144,0,224
X 161
Mvaa 176,148,4
Ra 140,242,19456
v_lshrrev_b32 v12, 7, v1
Cvaa 180,148,20,228
X 162
Mvaa 180,144,16
K 192,160,161
v_lshl_add_u32 v10, v12, 4, v10
Cvaa 184,144,32,232
s_mul_i32 s82, s14, 5120
X 163
Mvaa 184,148,36
X 164
Cvaa 188,148,52,236
K 193,162,163
Mvaa 188,144,48
s_mul_i32 s81, s14, 640
X 165
Mvaa 176,152,8
X 166
Mvaa 176,156,12
s_add_u32 s81, s81, 118784
Ra 144,242,20480
Ra 148,242,21504
Mvaa 180,156,28
K 200,164,165
Mvaa 180,152,24
X 167
Mvaa 184,152,40
s_waitcnt vmcnt(4)
v_cmp_eq_u64 vcc, s[12:13], v[8:9]
s_cmp_eq_u64 vcc, -1
s_cbranch_scc0 .Lspin_51%=
.Lok_52%=:
X 168
Mvaa 184,156,44
K 201,166,167
Mvaa 188,156,60
s_add_u32 m0, s82, 53248
X 169
Mvaa 188,152,56
W 2
Ra 152,242,22528
Mvav 32,128,196
X 170
)"
R"(Mvav 64,128,204
Ra 156,242,23552
Dc 10,8,81,0
Mvav 96,128,212
K 208,168,169
Mvav 128,128,220
X 171
Mvav 132,132,220
s_add_u32 m0, s82, 54144
X 172
Mvav 100,132,212
s_add_u32 s64, s64, 1
Ra 128,242,8192
K 209,170,171
Mvav 68,132,204
X 173
Mvav 36,132,196
Dc 10,8,81,128
Mvav 40,136,196
X 174
Mvav 72,136,204
s_add_u32 m0, s82, 55040
Ra 132,242,9216
K 216,172,173
Mvav 104,136,212
X 175
Mvav 136,136,220
Dc 10,8,81,256
Mvav 140,140,220
X 176
Mvav 108,140,212
Ra 136,242,10240
K 217,174,175
Mvav 76,140,204
s_add_u32 m0, s82, 55936
X 177
Mvav 44,140,196
W 3
Mvav 48,144,196
X 178
Mvav 80,144,204
Ra 140,242,11264
Dc 10,8,81,384
Mvav 112,144,212
K 194,176,177
Mvav 144,144,220
s_add_u32 m0, s82, 56832
X 179
Mvav 148,148,220
X 180
Mvav 116,148,212
Ra 144,242,12288
Mvav 84,148,204
Dc 10,8,81,512
K 195,178,179
Mvav 52,148,196
X 181
Mvav 56,152,196
X 182
Mvav 88,152,204
Ra 148,242,13312
Mvav 120,152,212
K 202,180,181
Mvav 152,152,220
X 183
Mvav 156,156,220
X 184
Mvav 124,156,212
Ra 152,242,14336
Mvav 92,156,204
K 203,182,183
Mvav 60,156,196
W 3
X 185
Cvaa 160,128,0,224
X 186
Mvaa 160,132,4
Ra 156,242,15360
Cvaa 164,132,20,228
K 210,184,185
Mvaa 164,128,16
X 187
Cvaa 168,128,32,232
X 188
Mvaa 168,132,36
Cvaa 172,132,52,236
K 211,186,187
Mvaa 172,128,48
X 189
Mvaa 160,136,8
Ra 128,242,24576
Ra 132,242,25600
Mvaa 160,140,12
X 190
Mvaa 164,140,28
K 218,188,189
Mvaa 164,136,24
X 191
Mvaa 168,136,40
Mvaa 168,140,44
Mvaa 172,140,60
X 160
Mvaa 172,136,56
W 2
Ra 136,242,26624
K 219,190,191
Cvaa 176,144,0,224
X 161
Mvaa 176,148,4
Ra 140,242,27648
v_lshrrev_b32 v8, 8, v1
Cvaa 180,148,20,228
X 162
Mvaa 180,144,16
K 196,160,161
Cvaa 184,144,32,232
X 163
Mvaa 184,148,36
v_bfe_u32 v9, v1, 6, 2
Cvaa 188,148,52,236
X 164
Mvaa 188,144,48
K 197,162,163
v_lshl_add_u32 v9, v8, 3, v9
Mvaa 176,152,8
X 165
Mvaa 176,156,12
Ra 144,242,28672
Ra 148,242,29696
Mvaa 180,156,28
X 166
Mvaa 180,152,24
K 204,164,165
v_lshlrev_b32 v11, 6, v9
Mvaa 184,152,40
X 167
Mvaa 184,156,44
X 168
Mvaa 188,156,60
v_xor_b32 v9, s14, v8
Mvaa 188,152,56
W 2
Ra 152,242,30720
K 205,166,167
Mvav 32,128,192
X 169
Mvav 64,128,200
Ra 156,242,31744
v_and_b32 v9, 1, v9
Mvav 96,128,208
X 170
Mvav 128,128,216
K 212,168,169
Mvav 132,132,216
X 171
Mvav 100,132,208
v_lshl_add_u32 v11, v9, 5, v11
Mvav 68,132,200
X 172
Mvav 36,132,192
K 213,170,171
Mvav 40,136,192
X 173
Mvav 72,136,200
v_bfe_u32 v9, v1, 4, 2
Mvav 104,136,208
X 174
Mvav 136,136,216
K 220,172,173
v_lshl_add_u32 v12, v9, 3, v11
Mvav 140,140,216
X 175
Mvav 108,140,208
X 176
Mvav 76,140,200
s_waitcnt vmcnt(0) lgkmcnt(0)
s_barrier
v_xor_b32 v242, 0x10000, v242
Mvav 44,140,192
v_add_u32 v11, 98304, v12
K 221,174,175
Mvav 48,144,192
Ra 128,242,0
Mvav 80,144,200
X 177
Mvav 112,144,208
X 178
Mvav 144,144,216
Ra 132,242,1024
v_and_b32 v13, 0x70, v1
Mvav 148,148,216
K 198,176,177
Mvav 116,148,208
X 179
Mvav 84,148,200
Ra 136,242,2048
v_lshl_add_u32 v13, v8, 7, v13
Mvav 52,148,192
X 180
Mvav 56,152,192
Ra 140,242,3072
K 199,178,179
Mvav 88,152,200
X 181
Mvav 120,152,208
X 182
Mvav 152,152,216
K 206,180,181
Mvav 156,156,216
X 183
Mvav 124,156,208
Mvav 92,156,200
K 207,182,183
Mvav 60,156,192
W 0
Cvaa 160,128,0,224
s_cmp_eq_u32 s14, 0
s_cselect_b32 s95, 106496, 0
Ra 144,242,4096
Ra 148,242,5120
Mvaa 160,132,4
s_xor_b32 s62, s62, 0x10000
X 184
Cvaa 164,132,20,228
Ta 184,11,0
Ta 186,11,256
Mvaa 164,128,16
s_add_u32 s95, s95, 32768
Ta 188,11,20480
Ta 190,11,20736
Cvaa 168,128,32,232
Ra 192,13,53248
Ta 196,11,2048
Mvaa 168,132,36
s_add_u32 m0, s95, 0
X 185
Cvaa 172,132,52,236
Ta 198,11,2304
Ta 200,11,22528
Mvaa 172,128,48
W 3
X 186
Zaaa 232,184,192
Ta 202,11,22784
K 214,184,185
Zaaa 236,188,192
Ra 204,13,53760
Ra 152,242,6144
Mvaa 160,136,8
X 187
Mvaa 160,140,12
Ta 208,11,4096
D 4,20,94,0
Mvaa 164,140,28
X 188
Mvaa 164,136,24
Ta 210,11,4352
D 5,20,94,1024
Mvaa 168,136,40
Ra 156,242,7168
K 215,186,187
Mvaa 168,140,44
X 189
Mvaa 172,140,60
Ta 212,11,24576
D 6,20,94,2048
Mvaa 172,136,56
X 190
v_smfmac_f32_16x16x64_bf16 a[160:163], a[176:179], v[192:199], v240
W 5
Ta 214,11,24832
D 7,20,94,3072
Maaa 232,196,204
s_add_u32 s94, s94, 221184
Ra 216,13,54272
K 222,188,189
Maaa 236,200,204
s_add_u32 m0, s95, 4096
X 191
v_smfmac_f32_16x16x64_bf16 a[164:167], a[176:179], v[200:207], v240
X 160
K 223,190,191
v_smfmac_f32_16x16x64_bf16 a[168:171], a[176:179], v[208:215], v240
Ra 128,242,16384
Ta 220,11,6144
v_smfmac_f32_16x16x64_bf16 a[172:175], a[176:179], v[216:223], v240
W 5
Ta 222,11,6400
Ra 132,242,17408
Cvaa 176,144,0,224
X 161
Mvaa 176,148,4
D 4,20,94,0
Ta 224,11,26624
Cvaa 180,148,20,228
X 162
Mvaa 180,144,16
D 5,20,94,1024
K 192,160,161
Cvaa 184,144,32,232
Ta 226,11,26880
Ra 228,13,54784
Mvaa 184,148,36
W 7
X 163
Maaa 232,208,216
D 6,20,94,2048
Ra 136,242,18432
Maaa 236,212,216
X 164
Cvaa 188,148,52,236
D 7,20,94,3072
Ta 184,11,8192
Mvaa 188,144,48
s_add_u32 s94, s94, 221184
Ra 140,242,19456
K 193,162,163
Mvaa 176,152,8
s_add_u32 m0, s95, 8192
X 165
Mvaa 176,156,12
Ta 186,11,8448
Ta 188,11,28672
Mvaa 180,156,28
X 166
Mvaa 180,152,24
Ra 144,242,20480
K 200,164,165
Mvaa 184,152,40
X 167
Mvaa 184,156,44
Ta 190,11,28928
D 4,20,94,0
Mvaa 188,156,60
W 7
X 168
Maaa 232,220,228
s_add_u32 s64, s64, 1
Ra 192,13,55296
D 5,20,94,1024
Maaa 236,224,228
Ta 196,11,10240
K 201,166,167
Mvaa 188,152,56
W 6
X 169
Mvav 32,128,196
D 6,20,94,2048
Ra 148,242,21504
Mvav 64,128,204
X 170
Mvav 96,128,212
Ta 198,11,10496
D 7,20,94,3072
Mvav 128,128,220
s_add_u32 s94, s94, 221184
Ta 200,11,30720
K 208,168,169
Mvav 132,132,220
s_add_u32 m0, s95, 12288
X 171
Mvav 100,132,212
Ra 152,242,22528
Ta 202,11,30976
Mvav 68,132,204
X 172
Mvav 36,132,196
W 6
Ra 204,13,55808
K 209,170,171
Maaa 232,184,192
X 173
Maaa 236,188,192
D 4,20,94,0
Ra 156,242,23552
Mvav 40,136,196
X 174
Mvav 72,136,204
Ta 208,11,12288
D 5,20,94,1024
Mvav 104,136,212
Ta 210,11,12544
K 216,172,173
Mvav 136,136,220
X 175
Mvav 140,140,220
D 6,20,94,2048
Ra 128,242,8192
Mvav 108,140,212
Ta 212,11,32768
Ta 214,11,33024
Mvav 76,140,204
X 176
Mvav 44,140,196
W 5
D 7,20,94,3072
K 217,174,175
Mvav 48,144,196
s_add_u32 s94, s94, 221184
X 177
)"
R"(Maaa 232,196,204
s_add_u32 m0, s95, 16384
Ra 132,242,9216
Ra 216,13,56320
Maaa 236,200,204
X 178
Mvav 80,144,204
Ta 220,11,14336
K 194,176,177
Mvav 112,144,212
X 179
Mvav 144,144,220
D 4,20,94,0
Ta 222,11,14592
Mvav 148,148,220
Ra 136,242,10240
Ta 224,11,34816
Mvav 116,148,212
X 180
Mvav 84,148,204
D 5,20,94,1024
K 195,178,179
Mvav 52,148,196
X 181
Mvav 56,152,196
D 6,20,94,2048
Ta 226,11,35072
Mvav 88,152,204
W 5
Ra 140,242,11264
Ra 228,13,56832
Maaa 232,208,216
X 182
Maaa 236,212,216
D 7,20,94,3072
K 202,180,181
Mvav 120,152,212
s_add_u32 s94, s94, 221184
X 183
Mvav 152,152,220
Ta 184,11,16384
Ra 144,242,12288
Mvav 156,156,220
X 184
Mvav 124,156,212
Ta 186,11,16640
K 203,182,183
Mvav 92,156,204
X 185
Mvav 60,156,196
W 4
Ta 188,11,36864
Ra 148,242,13312
Cvaa 160,128,0,224
X 186
Mvaa 160,132,4
Ta 190,11,37120
K 210,184,185
Cvaa 164,132,20,228
W 6
Ra 192,13,57344
Ta 196,11,18432
Maaa 232,220,228
X 187
Maaa 236,224,228
X 188
Mvaa 164,128,16
Ra 152,242,14336
K 211,186,187
Cvaa 168,128,32,232
Ta 198,11,18688
Ta 200,11,38912
Mvaa 168,132,36
X 189
Cvaa 172,132,52,236
X 190
Mvaa 172,128,48
Ra 156,242,15360
K 218,188,189
Mvaa 160,136,8
Ta 202,11,39168
Ra 204,13,57856
Mvaa 160,140,12
X 191
Mvaa 164,140,28
Ra 128,242,24576
Mvaa 164,136,24
W 8
Maaa 232,184,192
Ra 132,242,25600
Maaa 236,188,192
X 160
Mvaa 168,136,40
K 219,190,191
Mvaa 168,140,44
X 161
Mvaa 172,140,60
X 162
Mvaa 172,136,56
W 4
Ra 136,242,26624
K 196,160,161
Cvaa 176,144,0,224
X 163
Mvaa 176,148,4
X 164
Cvaa 180,148,20,228
Ra 140,242,27648
K 197,162,163
Mvaa 180,144,16
X 165
Cvaa 184,144,32,232
W 4
Maaa 232,196,204
X 166
Maaa 236,200,204
Mvaa 184,148,36
K 204,164,165
Cvaa 188,148,52,236
X 167
Mvaa 188,144,48
Ra 144,242,28672
Mvaa 176,152,8
X 168
Mvaa 176,156,12
Ra 148,242,29696
Mvaa 180,156,28
K 205,166,167
Mvaa 180,152,24
X 169
Mvaa 184,152,40
Mvaa 184,156,44
X 170
Mvaa 188,156,60
K 212,168,169
Mvaa 188,152,56
W 2
X 171
Mvav 32,128,192
Ra 152,242,30720
Ra 156,242,31744
Mvav 64,128,200
X 172
Mvav 96,128,208
Mvav 128,128,216
K 213,170,171
Mvav 132,132,216
X 173
Mvav 100,132,208
Mvav 68,132,200
X 174
Mvav 36,132,192
Mvav 40,136,192
K 220,172,173
Mvav 72,136,200
X 175
Mvav 104,136,208
Mvav 136,136,216
X 176
Mvav 140,140,216
Mvav 108,140,208
K 221,174,175
Mvav 76,140,200
s_waitcnt vmcnt(0) lgkmcnt(0)
s_barrier
v_xor_b32 v242, 0x10000, v242
Mvav 44,140,192
X 177
Mvav 48,144,192
X 178
Mvav 80,144,200
Ra 128,242,0
K 198,176,177
Mvav 112,144,208
X 179
Mvav 144,144,216
Ra 132,242,1024
Mvav 148,148,216
X 180
Mvav 116,148,208
Ra 136,242,2048
Mvav 84,148,200
K 199,178,179
Mvav 52,148,192
X 181
Mvav 56,152,192
Ra 140,242,3072
Mvav 88,152,200
X 182
Mvav 120,152,208
Mvav 152,152,216
K 206,180,181
Mvav 156,156,216
X 183
Mvav 124,156,208
Mvav 92,156,200
K 207,182,183
Mvav 60,156,192
W 0
Cvaa 160,128,0,224
s_mov_b32 s20, s34
Ra 144,242,4096
Ra 148,242,5120
Mvaa 160,132,4
s_xor_b32 s62, s62, 0x10000
X 184
Cvaa 164,132,20,228
s_and_b32 s21, s35, 0xffff
Ta 184,11,40960
Ta 186,11,41216
Mvaa 164,128,16
Ta 188,12,32768
Ta 190,12,33024
Cvaa 168,128,32,232
s_mov_b32 s22, 2211840
Ra 192,13,58368
Ta 196,11,43008
Mvaa 168,132,36
s_mov_b32 s23, 0x20000
X 185
Cvaa 172,132,52,236
Ta 198,11,43264
Ta 200,12,34816
Mvaa 172,128,48
W 3
X 186
Maaa 232,184,192
Ta 202,12,35072
K 214,184,185
Maaa 236,188,192
Ra 204,13,58880
Ra 152,242,6144
Mvaa 160,136,8
X 187
Mvaa 160,140,12
Ta 208,11,45056
v_lshrrev_b32 v4, 8, v1
Mvaa 164,140,28
X 188
Mvaa 164,136,24
Ta 210,11,45312
v_lshlrev_b32 v5, 5, v4
Mvaa 168,136,40
v_xor_b32 v5, v5, v1
K 215,186,187
Mvaa 168,140,44
Ra 156,242,7168
Ta 212,12,36864
Mvaa 172,140,60
X 189
Mvaa 172,136,56
v_and_b32 v5, 0xf0, v5
v_mul_u32_u24 v4, 1280, v4
v_smfmac_f32_16x16x64_bf16 a[160:163], a[176:179], v[192:199], v240
W 5
X 190
Maaa 232,196,204
Ta 214,12,37120
v_add_u32 v4, v4, v5
Maaa 236,200,204
Ra 216,13,59392
K 222,188,189
v_smfmac_f32_16x16x64_bf16 a[164:167], a[176:179], v[200:207], v240
X 191
X 160
K 223,190,191
v_smfmac_f32_16x16x64_bf16 a[168:171], a[176:179], v[208:215], v240
Ra 128,242,16384
v_xor_b32 v5, 128, v4
v_smfmac_f32_16x16x64_bf16 a[172:175], a[176:179], v[216:223], v240
W 4
Ta 220,11,47104
v_add_u32 v6, 8192, v4
Cvaa 176,144,0,224
Ta 222,11,47360
v_add_u32 v7, 12288, v5
Mvaa 176,148,4
Ra 132,242,17408
v_add_u32 v5, 4096, v5
Cvaa 180,148,20,228
s_mul_i32 s94, s85, 40960
X 161
Mvaa 180,144,16
s_mul_i32 s81, s14, 20480
Ta 224,12,38912
Ta 226,12,39168
Cvaa 184,144,32,232
s_add_u32 s94, s94, s81
X 162
Mvaa 184,148,36
W 6
s_lshl_b32 s95, s14, 12
K 192,160,161
Maaa 232,208,216
s_add_u32 s95, s95, 98304
X 163
Maaa 236,212,216
s_add_u32 m0, s95, 0
Ra 228,13,59904
Ra 136,242,18432
Cvaa 188,148,52,236
X 164
Mvaa 188,144,48
Ta 184,11,49152
K 193,162,163
Mvaa 176,152,8
X 165
Mvaa 176,156,12
Ra 140,242,19456
Ta 186,11,49408
Mvaa 180,156,28
D 4,20,94,0
Ta 188,12,40960
Mvaa 180,152,24
X 166
Mvaa 184,152,40
D 5,20,94,1024
K 200,164,165
Mvaa 184,156,44
X 167
Mvaa 188,156,60
W 5
D 6,20,94,2048
Ra 144,242,20480
Maaa 232,220,228
s_add_u32 s64, s64, 1
Ta 190,12,41216
Ra 192,13,60416
Maaa 236,224,228
D 7,20,94,3072
Ta 196,11,51200
Mvaa 188,152,56
W 6
X 168
Mvav 32,128,196
s_add_u32 s94, s94, 256
s_add_u32 m0, s95, 8192
K 201,166,167
Mvav 64,128,204
X 169
Mvav 96,128,212
D 4,20,94,0
Ra 148,242,21504
Mvav 128,128,220
X 170
Mvav 132,132,220
D 5,20,94,1024
Ta 198,11,51456
Mvav 100,132,212
Ta 200,12,43008
K 208,168,169
Mvav 68,132,204
D 6,20,94,2048
Ra 152,242,22528
Mvav 36,132,196
W 5
X 171
Maaa 232,184,192
D 7,20,94,3072
Ta 202,12,43264
Maaa 236,188,192
s_add_u32 s94, s94, 256
X 172
Mvav 40,136,196
s_add_u32 m0, s95, 16384
Ra 204,13,60928
K 209,170,171
Mvav 72,136,204
X 173
Mvav 104,136,212
Ra 156,242,23552
D 4,20,94,0
Mvav 136,136,220
X 174
Mvav 140,140,220
D 5,20,94,1024
Ta 208,11,53248
Mvav 108,140,212
Ta 210,11,53504
K 216,172,173
Mvav 76,140,204
D 6,20,94,2048
Ra 128,242,8192
Mvav 44,140,196
W 3
X 175
Mvav 48,144,196
D 7,20,94,3072
Ta 212,12,45056
Maaa 232,196,204
s_add_u32 s94, s94, 256
X 176
Maaa 236,200,204
s_add_u32 m0, s95, 24576
Ta 214,12,45312
K 217,174,175
Mvav 80,144,204
D 4,20,94,0
Ra 132,242,9216
Mvav 112,144,212
)"
R"(X 177
Mvav 144,144,220
Ra 216,13,61440
D 5,20,94,1024
Mvav 148,148,220
X 178
Mvav 116,148,212
D 6,20,94,2048
Ta 220,11,55296
Mvav 84,148,204
Ta 222,11,55552
K 194,176,177
Mvav 52,148,196
D 7,20,94,3072
Ra 136,242,10240
Mvav 56,152,196
s_add_u32 s94, s94, 256
X 179
Mvav 88,152,204
W 3
X 180
Maaa 232,208,216
s_add_u32 m0, s95, 32768
Ta 224,12,47104
K 195,178,179
Maaa 236,212,216
D 4,20,94,0
Ta 226,12,47360
Mvav 120,152,212
Ra 140,242,11264
Ra 228,13,61952
Mvav 152,152,220
X 181
Mvav 156,156,220
D 5,20,94,1024
Ta 184,11,57344
Mvav 124,156,212
X 182
Mvav 92,156,204
D 6,20,94,2048
K 202,180,181
Mvav 60,156,196
W 2
X 183
Cvaa 160,128,0,224
D 7,20,94,3072
Ra 144,242,12288
Mvaa 160,132,4
s_add_u32 s94, s94, 256
X 184
Cvaa 164,132,20,228
W 2
Ta 186,11,57600
K 203,182,183
Maaa 232,220,228
X 185
Maaa 236,224,228
Ta 188,12,49152
Ra 148,242,13312
Mvaa 164,128,16
X 186
Cvaa 168,128,32,232
Ta 190,12,49408
K 210,184,185
Mvaa 168,132,36
Ra 192,13,62464
Ta 196,11,59392
Cvaa 172,132,52,236
X 187
Mvaa 172,128,48
X 188
Mvaa 160,136,8
Ra 152,242,14336
K 211,186,187
Mvaa 160,140,12
Ta 198,11,59648
Ta 200,12,51200
Mvaa 164,140,28
X 189
Mvaa 164,136,24
W 4
X 190
Maaa 232,184,192
Ra 156,242,15360
K 218,188,189
Maaa 236,188,192
Ta 202,12,51456
Ra 204,13,62976
Mvaa 168,136,40
X 191
Mvaa 168,140,44
Ra 128,242,24576
Ra 132,242,25600
Mvaa 172,140,60
X 160
Mvaa 172,136,56
W 4
Ra 136,242,26624
K 219,190,191
Cvaa 176,144,0,224
X 161
Mvaa 176,148,4
X 162
Cvaa 180,148,20,228
Ra 140,242,27648
K 196,160,161
Mvaa 180,144,16
X 163
Cvaa 184,144,32,232
W 4
X 164
Maaa 232,196,204
K 197,162,163
Maaa 236,200,204
X 165
Mvaa 184,148,36
X 166
Cvaa 188,148,52,236
K 204,164,165
Mvaa 188,144,48
X 167
Mvaa 176,152,8
Ra 144,242,28672
G 252,232
Mvaa 176,156,12
G 253,233
G 254,234
Mvaa 180,156,28
X 168
Mvaa 180,152,24
G 255,235
G 8,236
Mvaa 184,152,40
Ra 148,242,29696
G 9,237
Mvaa 184,156,44
G 10,238
G 14,239
Mvaa 188,156,60
K 252,252,8
K 253,253,9
Mvaa 188,152,56
W 2
K 205,166,167
K 254,254,10
Mvav 32,128,192
K 255,255,14
v_and_b32 v8, 0xffff0000, v252
Mvav 64,128,200
v_and_b32 v9, 0xffff0000, v253
v_and_b32 v10, 0xffff0000, v254
Mvav 96,128,208
X 169
Mvav 128,128,216
v_and_b32 v14, 0xffff0000, v255
v_lshlrev_b32 v252, 16, v252
Mvav 132,132,216
v_lshlrev_b32 v253, 16, v253
v_lshlrev_b32 v254, 16, v254
Mvav 100,132,208
v_lshlrev_b32 v255, 16, v255
v_mul_f32 v15, 0xbfb8aa3b, v252
Mvav 68,132,200
v_mul_f32 v2, 0xbfb8aa3b, v253
v_mul_f32 v3, 0xbfb8aa3b, v254
Mvav 36,132,192
X 170
Mvav 40,136,192
v_mul_f32 v27, 0xbfb8aa3b, v255
K 212,168,169
Mvav 72,136,200
X 15
Mvav 104,136,208
Ra 152,242,30720
Ra 156,242,31744
Mvav 136,136,216
X 2
X 3
X 27
v_add_f32 v15, 1.0, v15
v_add_f32 v2, 1.0, v2
v_add_f32 v3, 1.0, v3
v_add_f32 v27, 1.0, v27
v_rcp_f32 v15, v15
v_rcp_f32 v2, v2
v_rcp_f32 v3, v3
v_rcp_f32 v27, v27
U 252,252,15
U 253,253,2
U 254,254,3
U 255,255,27
K 252,252,252
K 253,253,253
K 254,254,254
K 255,255,255
v_lshlrev_b32 v252, 16, v252
v_lshlrev_b32 v253, 16, v253
v_lshlrev_b32 v254, 16, v254
v_lshlrev_b32 v255, 16, v255
U 252,252,8
U 253,253,9
U 254,254,10
U 255,255,14
K 246,252,253
K 247,254,255
s_lshl_b32 s81, s14, 3
v_add_u32 v2, s81, v1
s_mov_b32 s86, 0x00ff00ff
s_mov_b32 s87, 0x00ff00ff
v_cndmask_b32_e64 v252, 0, v246, s[86:87]
v_cndmask_b32_e64 v253, 0, v247, s[86:87]
v_cndmask_b32_e64 v246, v246, 0, s[86:87]
v_cndmask_b32_e64 v247, v247, 0, s[86:87]
ds_write_b64 v2, v[252:253] offset:63488
ds_write_b64 v2, v[246:247] offset:64512
Mvav 140,140,216
X 171
Mvav 108,140,208
X 172
Mvav 76,140,200
s_waitcnt vmcnt(0) lgkmcnt(0)
s_barrier
v_xor_b32 v242, 0x10000, v242
Mvav 44,140,192
Ra 128,242,0
K 213,170,171
Mvav 48,144,192
X 173
Mvav 80,144,200
X 174
Mvav 112,144,208
Ra 132,242,1024
K 220,172,173
Mvav 144,144,216
X 175
Mvav 148,148,216
X 176
Mvav 116,148,208
Ra 136,242,2048
K 221,174,175
Mvav 84,148,200
X 177
Mvav 52,148,192
X 178
Mvav 56,152,192
Ra 140,242,3072
K 198,176,177
Mvav 88,152,200
X 179
Mvav 120,152,208
X 180
Mvav 152,152,216
K 199,178,179
Mvav 156,156,216
X 181
Mvav 124,156,208
X 182
Mvav 92,156,200
K 206,180,181
Mvav 60,156,192
X 183
s_nop 0
K 207,182,183
W 0
Cvaa 160,128,0,224
Ra 144,242,4096
Ra 148,242,5120
Mvaa 160,132,4
s_xor_b32 s62, s62, 0x10000
X 184
Cvaa 164,132,20,228
Ra 216,1,63488
Ra 220,1,64512
Mvaa 164,128,16
v_lshrrev_b32 v8, 6, v1
v_and_b32 v9, 7, v8
Cvaa 168,128,32,232
s_lshl_b32 s81, s14, 2
X 185
Mvaa 168,132,36
v_xor_b32 v9, s81, v9
v_lshlrev_b32 v9, 5, v9
Cvaa 172,132,52,236
X 186
Mvaa 172,128,48
v_lshl_add_u32 v4, v8, 8, v9
v_bfe_u32 v9, v1, 4, 2
Mvaa 160,136,8
v_lshl_add_u32 v4, v9, 3, v4
v_add_u32 v4, 98304, v4
Mvaa 160,140,12
K 214,184,185
v_xor_b32 v5, 32, v4
Mvaa 164,140,28
v_xor_b32 v6, 64, v4
v_xor_b32 v7, 96, v4
Ta 184,4,0
Ta 186,4,4096
Mvaa 164,136,24
Ta 188,5,0
Ta 190,5,4096
Mvaa 168,136,40
Ra 152,242,6144
Ta 192,6,0
Mvaa 168,140,44
W 4
X 187
Zaaa 232,184,216
Ta 194,6,4096
v_and_b32 v11, 0x70, v1
Mvaa 172,140,60
v_mul_u32_u24 v11, 160, v11
v_lshrrev_b32 v10, 8, v1
Mvaa 172,136,56
X 188
v_smfmac_f32_16x16x64_bf16 a[160:163], a[176:179], v[192:199], v240
Ra 156,242,7168
W 4
Maaa 232,188,220
K 215,186,187
v_lshl_add_u32 v11, v10, 4, v11
v_smfmac_f32_16x16x64_bf16 a[164:167], a[176:179], v[200:207], v240
X 189
X 190
K 222,188,189
X 191
X 160
K 223,190,191
v_smfmac_f32_16x16x64_bf16 a[168:171], a[176:179], v[208:215], v240
Ta 196,7,0
v_bfe_u32 v10, v1, 7, 1
v_smfmac_f32_16x16x64_bf16 a[172:175], a[176:179], v[216:223], v240
W 2
Ta 198,7,4096
v_lshl_add_u32 v10, v10, 6, v11
Zaaa 236,192,216
W 2
s_mul_i32 s94, s85, 20480
s_lshl_b32 s82, s14, 8
Cvaa 176,144,0,224
s_add_u32 s94, s94, s82
Ta 200,4,8192
Ta 202,4,12288
Mvaa 176,148,4
s_add_u32 s94, s94, 2752512
s_lshl_b32 s71, s85, 4
Ra 128,242,16384
Cvaa 180,148,20,228
W 3
s_lshl_b32 s82, s14, 3
Ta 204,5,8192
Maaa 236,196,220
s_add_u32 s71, s71, s82
s_add_u32 s71, s71, 2560
Ta 206,5,12288
Mvaa 180,144,16
X 161
Cvaa 184,144,32,232
Ra 132,242,17408
Ta 208,6,8192
Mvaa 184,148,36
W 5
X 162
Zaaa 240,200,216
Ta 210,6,12288
K 192,160,161
Cvaa 188,148,52,236
buffer_store_dwordx4 a[232:235], v10, s[8:11], s94 offen offset:0 sc1
Ta 212,7,8192
Mvaa 188,144,48
X 163
Mvaa 176,152,8
W 4
X 164
Maaa 240,204,220
Ra 136,242,18432
Ta 214,7,12288
Mvaa 176,156,12
Ta 184,4,16384
K 193,162,163
Mvaa 180,156,28
X 165
Mvaa 180,152,24
W 4
Ta 186,4,20480
Ra 140,242,19456
Zaaa 244,208,216
X 166
Mvaa 184,152,40
buffer_store_dwordx4 a[236:239], v10, s[8:11], s94 offen offset:128 sc1
K 200,164,165
Mvaa 184,156,44
X 167
Mvaa 188,156,60
W 3
Ta 188,5,16384
Ta 190,5,20480
Maaa 244,212,220
s_add_u32 s64, s64, 1
X 168
)"
R"(Mvaa 188,152,56
W 2
Ra 144,242,20480
K 201,166,167
Mvav 32,128,196
Ta 192,6,16384
Ta 194,6,20480
Mvav 64,128,204
X 169
Zaaa 248,184,216
buffer_store_dwordx4 a[240:243], v10, s[8:11], s94 offen offset:512 sc1
Ta 196,7,16384
Mvav 96,128,212
X 170
Mvav 128,128,220
Ra 148,242,21504
K 208,168,169
Mvav 132,132,220
W 5
Ta 198,7,20480
Ta 200,4,24576
Maaa 248,188,220
X 171
Mvav 100,132,212
X 172
Mvav 68,132,204
Ta 202,4,28672
K 209,170,171
Mvav 36,132,196
W 5
Ra 152,242,22528
buffer_store_dwordx4 a[244:247], v10, s[8:11], s94 offen offset:640 sc1
Zaaa 252,192,216
X 173
Mvav 40,136,196
X 174
Mvav 72,136,204
Ta 204,5,24576
Ta 206,5,28672
Mvav 104,136,212
W 5
Ra 156,242,23552
K 216,172,173
Maaa 252,196,220
X 175
Mvav 136,136,220
Ta 208,6,24576
Ta 210,6,28672
Mvav 140,140,220
X 176
Mvav 108,140,212
W 6
buffer_store_dwordx4 a[248:251], v10, s[8:11], s94 offen offset:1024 sc1
K 217,174,175
Zaaa 232,200,216
X 177
Mvav 76,140,204
Ta 212,7,24576
Ra 128,242,8192
Mvav 44,140,196
W 4
Ta 214,7,28672
Ta 184,4,32768
Mvav 48,144,196
X 178
Maaa 232,204,220
Ra 132,242,9216
K 194,176,177
Mvav 80,144,204
X 179
Mvav 112,144,212
Ta 186,4,36864
buffer_store_dwordx4 a[252:255], v10, s[8:11], s94 offen offset:1152 sc1
Mvav 144,144,220
W 6
X 180
Zaaa 236,208,216
Ta 188,5,32768
K 195,178,179
Mvav 148,148,220
X 181
Mvav 116,148,212
Ta 190,5,36864
Ra 136,242,10240
Mvav 84,148,204
W 6
X 182
Maaa 236,212,220
Ta 192,6,32768
K 202,180,181
Mvav 52,148,196
Ta 194,6,36864
Ra 140,242,11264
Mvav 56,152,196
X 183
Mvav 88,152,204
W 6
buffer_store_dwordx4 a[232:235], v10, s[8:11], s94 offen offset:1536 sc1
Ta 196,7,32768
Zaaa 240,184,216
X 184
Mvav 120,152,212
Ta 198,7,36864
K 203,182,183
Mvav 152,152,220
Ra 144,242,12288
Ra 148,242,13312
Mvav 156,156,220
W 8
X 185
Maaa 240,188,220
X 186
Mvav 124,156,212
buffer_store_dwordx4 a[236:239], v10, s[8:11], s94 offen offset:1664 sc1
K 210,184,185
Mvav 92,156,204
X 187
Mvav 60,156,196
W 5
X 188
Zaaa 244,192,216
W 4
Ra 152,242,14336
K 211,186,187
Cvaa 160,128,0,224
X 189
Mvaa 160,132,4
X 190
Cvaa 164,132,20,228
W 3
Ra 156,242,15360
buffer_store_dwordx4 a[240:243], v10, s[8:11], s94 offen offset:2048 sc1
Maaa 244,196,220
K 218,188,189
Mvaa 164,128,16
X 191
Cvaa 168,128,32,232
Mvaa 168,132,36
Cvaa 172,132,52,236
Mvaa 172,128,48
Ra 128,242,24576
Mvaa 160,136,8
Ra 132,242,25600
buffer_store_dwordx4 a[244:247], v10, s[8:11], s94 offen offset:2176 sc1
Mvaa 160,140,12
Mvaa 164,140,28
Mvaa 164,136,24
Mvaa 168,136,40
Mvaa 168,140,44
Mvaa 172,140,60
Mvaa 172,136,56
W 2
Ra 136,242,26624
Cvaa 176,144,0,224
X 160
Mvaa 176,148,4
Ra 140,242,27648
K 219,190,191
Cvaa 180,148,20,228
X 161
Mvaa 180,144,16
X 162
Cvaa 184,144,32,232
K 196,160,161
Mvaa 184,148,36
X 163
Cvaa 188,148,52,236
X 164
Mvaa 188,144,48
Ra 144,242,28672
K 197,162,163
Mvaa 176,152,8
X 165
Mvaa 176,156,12
X 166
Mvaa 180,156,28
Ra 148,242,29696
K 204,164,165
Mvaa 180,152,24
X 167
Mvaa 184,152,40
X 168
Mvaa 184,156,44
K 205,166,167
Mvaa 188,156,60
X 169
Mvaa 188,152,56
W 2
X 170
Mvav 32,128,192
Ra 152,242,30720
K 212,168,169
Mvav 64,128,200
X 171
Mvav 96,128,208
Ra 156,242,31744
Mvav 128,128,216
X 172
Mvav 132,132,216
K 213,170,171
Mvav 100,132,208
X 173
Mvav 68,132,200
X 174
Mvav 36,132,192
Mvav 40,136,192
K 220,172,173
Mvav 72,136,200
X 175
Mvav 104,136,208
Mvav 136,136,216
X 176
Mvav 140,140,216
Mvav 108,140,208
K 221,174,175
Mvav 76,140,200
W 0
s_barrier
v_xor_b32 v242, 0x10000, v242
Mvav 44,140,192
X 177
Mvav 48,144,192
X 178
Mvav 80,144,200
Ra 128,242,0
K 198,176,177
Mvav 112,144,208
X 179
Mvav 144,144,216
Ra 132,242,1024
Mvav 148,148,216
X 180
Mvav 116,148,208
Ra 136,242,2048
Mvav 84,148,200
K 199,178,179
Mvav 52,148,192
X 181
Mvav 56,152,192
Ra 140,242,3072
Mvav 88,152,200
X 182
Mvav 120,152,208
Mvav 152,152,216
K 206,180,181
Mvav 156,156,216
X 183
Mvav 124,156,208
Mvav 92,156,200
s_waitcnt vmcnt(0)
v_mov_b64 v[28:29], s[12:13]
s_mov_b64 exec, 1
buffer_store_dwordx2 v[28:29], off, s[8:11], s71 sc1
s_mov_b64 exec, -1
K 207,182,183
Mvav 60,156,192
s_mov_b32 s85, 99
s_lshl_b32 s65, s57, 1
s_sub_u32 s65, s65, 1
s_branch .Ll0t_46%=
.Lf1r_47%=:
s_lshl_b32 s92, s58, 2
s_add_u32 s91, s92, s51
s_add_u32 s95, s92, 0
s_and_b32 s95, s95, 15
s_mul_i32 s95, s95, 40960
s_mul_i32 s71, s51, 0
s_add_u32 s71, s71, 133120
s_add_u32 m0, s71, s48
s_lshl2_add_u32 s94, s48, s95
D 16,40,94,0
s_add_u32 s95, s92, 1
s_and_b32 s95, s95, 15
s_mul_i32 s95, s95, 40960
s_mul_i32 s71, s51, 0
s_add_u32 s71, s71, 143360
D 17,40,94,1024
D 18,40,94,2048
D 19,40,94,3072
s_add_u32 m0, m0, 4096
s_nop 0
buffer_load_dwordx4 v20, s[40:43], s94 offen lds
s_add_u32 m0, s71, s48
s_lshl2_add_u32 s94, s48, s95
D 16,40,94,0
s_add_u32 s95, s92, 2
s_and_b32 s95, s95, 15
s_mul_i32 s95, s95, 40960
s_mul_i32 s71, s51, 0
s_add_u32 s71, s71, 153600
D 17,40,94,1024
D 18,40,94,2048
D 19,40,94,3072
s_add_u32 m0, m0, 4096
s_nop 0
buffer_load_dwordx4 v20, s[40:43], s94 offen lds
s_add_u32 m0, s71, s48
s_lshl2_add_u32 s94, s48, s95
D 16,40,94,0
D 17,40,94,1024
D 18,40,94,2048
D 19,40,94,3072
s_add_u32 m0, m0, 4096
s_nop 0
buffer_load_dwordx4 v20, s[40:43], s94 offen lds
s_branch .Lf1_40%=
.Lspin_15%=:
s_mov_b32 s84, 0
.Llp_17%=:
s_sleep 2
v_lshrrev_b32 v28, 1, v1
s_lshl_b32 s80, s60, 9
s_add_u32 s80, s80, 40960
buffer_load_dwordx2 v[28:29], v28, s[8:11], s80 offen sc1
s_waitcnt vmcnt(0)
s_add_u32 s84, s84, 1
v_cmp_eq_u32 vcc, s13, v29
s_cmp_eq_u64 vcc, -1
s_cbranch_scc1 .Lok_16%=
s_cmp_lt_u32 s84, 1048576
s_cbranch_scc1 .Llp_17%=
s_branch .Lok_16%=
.Lspin_20%=:
s_mov_b32 s84, 0
.Llp_22%=:
s_sleep 2
v_lshrrev_b32 v28, 1, v1
s_lshl_b32 s80, s60, 9
s_add_u32 s80, s80, 40960
buffer_load_dwordx2 v[28:29], v28, s[8:11], s80 offen sc1
s_waitcnt vmcnt(0)
s_add_u32 s84, s84, 1
v_cmp_eq_u32 vcc, s13, v29
s_cmp_eq_u64 vcc, -1
s_cbranch_scc1 .Lok_21%=
s_cmp_lt_u32 s84, 1048576
s_cbranch_scc1 .Llp_22%=
s_branch .Lok_21%=
.Lrt_23%=:
s_waitcnt vmcnt(0)
s_mov_b32 s86, 40960
buffer_load_dwordx4 v[32:35], v1, s[8:11], s86 offen offset:0 sc1
buffer_load_dwordx4 v[36:39], v1, s[8:11], s86 offen offset:1024 sc1
buffer_load_dwordx4 v[40:43], v1, s[8:11], s86 offen offset:2048 sc1
buffer_load_dwordx4 v[44:47], v1, s[8:11], s86 offen offset:3072 sc1
s_mov_b32 s86, 45056
buffer_load_dwordx4 v[48:51], v1, s[8:11], s86 offen offset:0 sc1
buffer_load_dwordx4 v[52:55], v1, s[8:11], s86 offen offset:1024 sc1
buffer_load_dwordx4 v[56:59], v1, s[8:11], s86 offen offset:2048 sc1
buffer_load_dwordx4 v[60:63], v1, s[8:11], s86 offen offset:3072 sc1
s_mov_b32 s86, 49152
buffer_load_dwordx4 v[64:67], v1, s[8:11], s86 offen offset:0 sc1
buffer_load_dwordx4 v[68:71], v1, s[8:11], s86 offen offset:1024 sc1
buffer_load_dwordx4 v[72:75], v1, s[8:11], s86 offen offset:2048 sc1
buffer_load_dwordx4 v[76:79], v1, s[8:11], s86 offen offset:3072 sc1
s_mov_b32 s86, 53248
buffer_load_dwordx4 v[80:83], v1, s[8:11], s86 offen offset:0 sc1
buffer_load_dwordx4 v[84:87], v1, s[8:11], s86 offen offset:1024 sc1
buffer_load_dwordx4 v[88:91], v1, s[8:11], s86 offen offset:2048 sc1
buffer_load_dwordx4 v[92:95], v1, s[8:11], s86 offen offset:3072 sc1
s_branch .Lrtr_24%=
.Lrt_25%=:
v_lshrrev_b32 v136, 1, v1
s_mov_b32 s84, 0
.LL_27%=:
s_waitcnt vmcnt(0)
v_min3_u32 v96, v33, v35, v95
v_max3_u32 v97, v33, v35, v95
v_min3_u32 v96, v96, v37, v39
v_max3_u32 v97, v97, v37, v39
v_min3_u32 v96, v96, v41, v43
v_max3_u32 v97, v97, v41, v43
v_min3_u32 v96, v96, v45, v47
v_max3_u32 v97, v97, v45, v47
v_min3_u32 v96, v96, v49, v51
v_max3_u32 v97, v97, v49, v51
v_min3_u32 v96, v96, v53, v55
v_max3_u32 v97, v97, v53, v55
v_min3_u32 v96, v96, v57, v59
v_max3_u32 v97, v97, v57, v59
v_min3_u32 v96, v96, v61, v63
v_max3_u32 v97, v97, v61, v63
v_min3_u32 v96, v96, v65, v67
v_max3_u32 v97, v97, v65, v67
v_min3_u32 v96, v96, v69, v71
v_max3_u32 v97, v97, v69, v71
v_min3_u32 v96, v96, v73, v75
v_max3_u32 v97, v97, v73, v75
v_min3_u32 v96, v96, v77, v79
v_max3_u32 v97, v97, v77, v79
v_min3_u32 v96, v96, v81, v83
v_max3_u32 v97, v97, v81, v83
v_min3_u32 v96, v96, v85, v87
v_max3_u32 v97, v97, v85, v87
v_min3_u32 v96, v96, v89, v91
v_max3_u32 v97, v97, v89, v91
v_min_u32 v96, v96, v93
v_max_u32 v97, v97, v93
v_cmp_eq_u32 vcc, s13, v96
v_cmp_eq_u32_e64 s[86:87], s13, v97
)"
R"(s_and_b64 vcc, vcc, s[86:87]
s_cmp_eq_u64 vcc, -1
s_cbranch_scc1 .LL_28%=
s_sleep 2
s_mov_b32 s86, 40960
buffer_load_dwordx4 v[32:35], v1, s[8:11], s86 offen offset:0 sc1
buffer_load_dwordx4 v[36:39], v1, s[8:11], s86 offen offset:1024 sc1
buffer_load_dwordx4 v[40:43], v1, s[8:11], s86 offen offset:2048 sc1
buffer_load_dwordx4 v[44:47], v1, s[8:11], s86 offen offset:3072 sc1
s_mov_b32 s86, 45056
buffer_load_dwordx4 v[48:51], v1, s[8:11], s86 offen offset:0 sc1
buffer_load_dwordx4 v[52:55], v1, s[8:11], s86 offen offset:1024 sc1
buffer_load_dwordx4 v[56:59], v1, s[8:11], s86 offen offset:2048 sc1
buffer_load_dwordx4 v[60:63], v1, s[8:11], s86 offen offset:3072 sc1
s_mov_b32 s86, 49152
buffer_load_dwordx4 v[64:67], v1, s[8:11], s86 offen offset:0 sc1
buffer_load_dwordx4 v[68:71], v1, s[8:11], s86 offen offset:1024 sc1
buffer_load_dwordx4 v[72:75], v1, s[8:11], s86 offen offset:2048 sc1
buffer_load_dwordx4 v[76:79], v1, s[8:11], s86 offen offset:3072 sc1
s_mov_b32 s86, 53248
buffer_load_dwordx4 v[80:83], v1, s[8:11], s86 offen offset:0 sc1
buffer_load_dwordx4 v[84:87], v1, s[8:11], s86 offen offset:1024 sc1
buffer_load_dwordx4 v[88:91], v1, s[8:11], s86 offen offset:2048 sc1
buffer_load_dwordx4 v[92:95], v1, s[8:11], s86 offen offset:3072 sc1
s_add_u32 s84, s84, 1
s_cmp_lt_u32 s84, 1048576
s_cbranch_scc1 .LL_27%=
s_waitcnt vmcnt(0)
.LL_28%=:
v_mov_b32 v96, v32
v_mov_b32 v97, 0
v_mov_b32 v98, 0
v_mov_b32 v100, 0
v_med3_u32 v98, v97, v98, v34
v_med3_u32 v97, v96, v97, v34
v_max_u32 v96, v96, v34
v_med3_u32 v98, v97, v98, v36
v_med3_u32 v97, v96, v97, v36
v_max_u32 v96, v96, v36
v_med3_u32 v98, v97, v98, v38
v_med3_u32 v97, v96, v97, v38
v_max_u32 v96, v96, v38
v_med3_u32 v98, v97, v98, v40
v_med3_u32 v97, v96, v97, v40
v_max_u32 v96, v96, v40
v_med3_u32 v98, v97, v98, v42
v_med3_u32 v97, v96, v97, v42
v_max_u32 v96, v96, v42
v_med3_u32 v98, v97, v98, v44
v_med3_u32 v97, v96, v97, v44
v_max_u32 v96, v96, v44
v_med3_u32 v98, v97, v98, v46
v_med3_u32 v97, v96, v97, v46
v_max_u32 v96, v96, v46
v_med3_u32 v98, v97, v98, v48
v_med3_u32 v97, v96, v97, v48
v_max_u32 v96, v96, v48
v_med3_u32 v98, v97, v98, v50
v_med3_u32 v97, v96, v97, v50
v_max_u32 v96, v96, v50
v_med3_u32 v98, v97, v98, v52
v_med3_u32 v97, v96, v97, v52
v_max_u32 v96, v96, v52
v_med3_u32 v98, v97, v98, v54
v_med3_u32 v97, v96, v97, v54
v_max_u32 v96, v96, v54
v_med3_u32 v98, v97, v98, v56
v_med3_u32 v97, v96, v97, v56
v_max_u32 v96, v96, v56
v_med3_u32 v98, v97, v98, v58
v_med3_u32 v97, v96, v97, v58
v_max_u32 v96, v96, v58
v_med3_u32 v98, v97, v98, v60
v_med3_u32 v97, v96, v97, v60
v_max_u32 v96, v96, v60
v_med3_u32 v98, v97, v98, v62
v_med3_u32 v97, v96, v97, v62
v_max_u32 v96, v96, v62
v_med3_u32 v98, v97, v98, v64
v_med3_u32 v97, v96, v97, v64
v_max_u32 v96, v96, v64
v_med3_u32 v98, v97, v98, v66
v_med3_u32 v97, v96, v97, v66
v_max_u32 v96, v96, v66
v_med3_u32 v98, v97, v98, v68
v_med3_u32 v97, v96, v97, v68
v_max_u32 v96, v96, v68
v_med3_u32 v98, v97, v98, v70
v_med3_u32 v97, v96, v97, v70
v_max_u32 v96, v96, v70
v_med3_u32 v98, v97, v98, v72
v_med3_u32 v97, v96, v97, v72
v_max_u32 v96, v96, v72
v_med3_u32 v98, v97, v98, v74
v_med3_u32 v97, v96, v97, v74
v_max_u32 v96, v96, v74
v_med3_u32 v98, v97, v98, v76
v_med3_u32 v97, v96, v97, v76
v_max_u32 v96, v96, v76
v_med3_u32 v98, v97, v98, v78
v_med3_u32 v97, v96, v97, v78
v_max_u32 v96, v96, v78
v_med3_u32 v98, v97, v98, v80
v_med3_u32 v97, v96, v97, v80
v_max_u32 v96, v96, v80
v_med3_u32 v98, v97, v98, v82
v_med3_u32 v97, v96, v97, v82
v_max_u32 v96, v96, v82
v_med3_u32 v98, v97, v98, v84
v_med3_u32 v97, v96, v97, v84
v_max_u32 v96, v96, v84
v_med3_u32 v98, v97, v98, v86
v_med3_u32 v97, v96, v97, v86
v_max_u32 v96, v96, v86
v_med3_u32 v98, v97, v98, v88
v_med3_u32 v97, v96, v97, v88
v_max_u32 v96, v96, v88
v_med3_u32 v98, v97, v98, v90
v_med3_u32 v97, v96, v97, v90
v_max_u32 v96, v96, v90
v_med3_u32 v98, v97, v98, v92
v_med3_u32 v97, v96, v97, v92
v_max_u32 v96, v96, v92
v_med3_u32 v98, v97, v98, v94
v_med3_u32 v97, v96, v97, v94
v_max_u32 v96, v96, v94
v_mov_b32 v99, v96
s_nop 1
v_max_u32_dpp v99, v99, v99 row_shr:1 row_mask:0xf bank_mask:0xf
s_nop 1
v_max_u32_dpp v99, v99, v99 row_shr:2 row_mask:0xf bank_mask:0xf
s_nop 1
v_max_u32_dpp v99, v99, v99 row_shr:4 row_mask:0xf bank_mask:0xf
s_nop 1
v_max_u32_dpp v99, v99, v99 row_shr:8 row_mask:0xf bank_mask:0xf
s_nop 1
v_max_u32_dpp v99, v99, v99 row_bcast:15 row_mask:0xa bank_mask:0xf
s_nop 1
v_max_u32_dpp v99, v99, v99 row_bcast:31 row_mask:0xc bank_mask:0xf
s_nop 0
v_readlane_b32 s86, v99, 63
s_nop 1
v_cmp_eq_u32 vcc, s86, v96
s_and_b32 s87, s86, 0xffff
s_sub_u32 s87, 2047, s87
v_cndmask_b32 v96, v96, v97, vcc
v_cndmask_b32_e64 v97, v97, 0, vcc
v_writelane_b32 v100, s87, 0
v_mov_b32 v99, v96
s_nop 1
v_max_u32_dpp v99, v99, v99 row_shr:1 row_mask:0xf bank_mask:0xf
s_nop 1
v_max_u32_dpp v99, v99, v99 row_shr:2 row_mask:0xf bank_mask:0xf
s_nop 1
v_max_u32_dpp v99, v99, v99 row_shr:4 row_mask:0xf bank_mask:0xf
s_nop 1
v_max_u32_dpp v99, v99, v99 row_shr:8 row_mask:0xf bank_mask:0xf
s_nop 1
v_max_u32_dpp v99, v99, v99 row_bcast:15 row_mask:0xa bank_mask:0xf
s_nop 1
v_max_u32_dpp v99, v99, v99 row_bcast:31 row_mask:0xc bank_mask:0xf
s_nop 0
v_readlane_b32 s86, v99, 63
s_nop 1
v_cmp_eq_u32 vcc, s86, v96
s_and_b32 s87, s86, 0xffff
s_sub_u32 s87, 2047, s87
v_cndmask_b32 v96, v96, v97, vcc
v_cndmask_b32_e64 v97, v97, 0, vcc
v_writelane_b32 v100, s87, 1
v_mov_b32 v99, v96
s_nop 1
v_max_u32_dpp v99, v99, v99 row_shr:1 row_mask:0xf bank_mask:0xf
s_nop 1
v_max_u32_dpp v99, v99, v99 row_shr:2 row_mask:0xf bank_mask:0xf
s_nop 1
v_max_u32_dpp v99, v99, v99 row_shr:4 row_mask:0xf bank_mask:0xf
s_nop 1
v_max_u32_dpp v99, v99, v99 row_shr:8 row_mask:0xf bank_mask:0xf
s_nop 1
v_max_u32_dpp v99, v99, v99 row_bcast:15 row_mask:0xa bank_mask:0xf
s_nop 1
v_max_u32_dpp v99, v99, v99 row_bcast:31 row_mask:0xc bank_mask:0xf
s_nop 0
v_readlane_b32 s86, v99, 63
s_nop 1
v_cmp_eq_u32 vcc, s86, v96
s_and_b32 s87, s86, 0xffff
s_sub_u32 s87, 2047, s87
v_cndmask_b32 v96, v96, v97, vcc
v_cndmask_b32_e64 v97, v97, 0, vcc
v_writelane_b32 v100, s87, 2
v_mov_b32 v99, v96
s_nop 1
v_max_u32_dpp v99, v99, v99 row_shr:1 row_mask:0xf bank_mask:0xf
s_nop 1
v_max_u32_dpp v99, v99, v99 row_shr:2 row_mask:0xf bank_mask:0xf
s_nop 1
v_max_u32_dpp v99, v99, v99 row_shr:4 row_mask:0xf bank_mask:0xf
s_nop 1
v_max_u32_dpp v99, v99, v99 row_shr:8 row_mask:0xf bank_mask:0xf
s_nop 1
v_max_u32_dpp v99, v99, v99 row_bcast:15 row_mask:0xa bank_mask:0xf
s_nop 1
v_max_u32_dpp v99, v99, v99 row_bcast:31 row_mask:0xc bank_mask:0xf
s_nop 0
v_readlane_b32 s86, v99, 63
s_nop 1
v_cmp_eq_u32 vcc, s86, v96
s_and_b32 s87, s86, 0xffff
s_sub_u32 s87, 2047, s87
v_cndmask_b32 v96, v96, v97, vcc
v_cndmask_b32_e64 v97, v97, 0, vcc
v_writelane_b32 v100, s87, 3
v_mov_b32 v99, v96
s_nop 1
v_max_u32_dpp v99, v99, v99 row_shr:1 row_mask:0xf bank_mask:0xf
s_nop 1
v_max_u32_dpp v99, v99, v99 row_shr:2 row_mask:0xf bank_mask:0xf
s_nop 1
v_max_u32_dpp v99, v99, v99 row_shr:4 row_mask:0xf bank_mask:0xf
s_nop 1
v_max_u32_dpp v99, v99, v99 row_shr:8 row_mask:0xf bank_mask:0xf
s_nop 1
v_max_u32_dpp v99, v99, v99 row_bcast:15 row_mask:0xa bank_mask:0xf
s_nop 1
v_max_u32_dpp v99, v99, v99 row_bcast:31 row_mask:0xc bank_mask:0xf
s_nop 0
v_readlane_b32 s86, v99, 63
s_nop 1
v_cmp_eq_u32 vcc, s86, v96
s_and_b32 s87, s86, 0xffff
s_sub_u32 s87, 2047, s87
v_cndmask_b32 v96, v96, v97, vcc
v_cndmask_b32_e64 v97, v97, 0, vcc
v_writelane_b32 v100, s87, 4
v_mov_b32 v99, v96
s_nop 1
v_max_u32_dpp v99, v99, v99 row_shr:1 row_mask:0xf bank_mask:0xf
s_nop 1
v_max_u32_dpp v99, v99, v99 row_shr:2 row_mask:0xf bank_mask:0xf
s_nop 1
v_max_u32_dpp v99, v99, v99 row_shr:4 row_mask:0xf bank_mask:0xf
s_nop 1
v_max_u32_dpp v99, v99, v99 row_shr:8 row_mask:0xf bank_mask:0xf
s_nop 1
v_max_u32_dpp v99, v99, v99 row_bcast:15 row_mask:0xa bank_mask:0xf
s_nop 1
v_max_u32_dpp v99, v99, v99 row_bcast:31 row_mask:0xc bank_mask:0xf
s_nop 0
v_readlane_b32 s86, v99, 63
s_nop 1
v_cmp_eq_u32 vcc, s86, v96
s_and_b32 s87, s86, 0xffff
s_sub_u32 s87, 2047, s87
v_cndmask_b32 v96, v96, v97, vcc
v_cndmask_b32_e64 v97, v97, 0, vcc
v_writelane_b32 v100, s87, 5
v_mov_b32 v99, v96
s_nop 1
v_max_u32_dpp v99, v99, v99 row_shr:1 row_mask:0xf bank_mask:0xf
s_nop 1
v_max_u32_dpp v99, v99, v99 row_shr:2 row_mask:0xf bank_mask:0xf
s_nop 1
v_max_u32_dpp v99, v99, v99 row_shr:4 row_mask:0xf bank_mask:0xf
s_nop 1
v_max_u32_dpp v99, v99, v99 row_shr:8 row_mask:0xf bank_mask:0xf
s_nop 1
v_max_u32_dpp v99, v99, v99 row_bcast:15 row_mask:0xa bank_mask:0xf
s_nop 1
v_max_u32_dpp v99, v99, v99 row_bcast:31 row_mask:0xc bank_mask:0xf
s_nop 0
v_readlane_b32 s86, v99, 63
s_nop 1
v_cmp_eq_u32 vcc, s86, v96
s_and_b32 s87, s86, 0xffff
s_sub_u32 s87, 2047, s87
v_cndmask_b32 v96, v96, v97, vcc
v_cndmask_b32_e64 v97, v97, 0, vcc
v_writelane_b32 v100, s87, 6
v_mov_b32 v99, v96
s_nop 1
v_max_u32_dpp v99, v99, v99 row_shr:1 row_mask:0xf bank_mask:0xf
s_nop 1
v_max_u32_dpp v99, v99, v99 row_shr:2 row_mask:0xf bank_mask:0xf
s_nop 1
v_max_u32_dpp v99, v99, v99 row_shr:4 row_mask:0xf bank_mask:0xf
s_nop 1
v_max_u32_dpp v99, v99, v99 row_shr:8 row_mask:0xf bank_mask:0xf
s_nop 1
v_max_u32_dpp v99, v99, v99 row_bcast:15 row_mask:0xa bank_mask:0xf
s_nop 1
v_max_u32_dpp v99, v99, v99 row_bcast:31 row_mask:0xc bank_mask:0xf
s_nop 0
v_readlane_b32 s86, v99, 63
s_nop 1
v_cmp_eq_u32 vcc, s86, v96
s_and_b32 s87, s86, 0xffff
s_sub_u32 s87, 2047, s87
v_cndmask_b32 v96, v96, v97, vcc
v_cndmask_b32_e64 v97, v97, 0, vcc
v_writelane_b32 v100, s87, 7
v_mov_b32 v99, v98
s_nop 1
v_max_u32_dpp v99, v99, v99 row_shr:1 row_mask:0xf bank_mask:0xf
s_nop 1
v_max_u32_dpp v99, v99, v99 row_shr:2 row_mask:0xf bank_mask:0xf
s_nop 1
v_max_u32_dpp v99, v99, v99 row_shr:4 row_mask:0xf bank_mask:0xf
s_nop 1
v_max_u32_dpp v99, v99, v99 row_shr:8 row_mask:0xf bank_mask:0xf
s_nop 1
v_max_u32_dpp v99, v99, v99 row_bcast:15 row_mask:0xa bank_mask:0xf
s_nop 1
v_max_u32_dpp v99, v99, v99 row_bcast:31 row_mask:0xc bank_mask:0xf
s_nop 0
v_readlane_b32 s87, v99, 63
s_nop 0
s_cmp_gt_u32 s87, s86
s_cbranch_scc0 .LL_29%=
s_mov_b32 m0, 0
.LL_30%=:
v_max3_u32 v96, v32, v34, v36
v_max3_u32 v96, v96, v38, v40
v_max3_u32 v96, v96, v42, v44
v_max3_u32 v96, v96, v46, v48
v_max3_u32 v96, v96, v50, v52
v_max3_u32 v96, v96, v54, v56
v_max3_u32 v96, v96, v58, v60
v_max3_u32 v96, v96, v62, v64
v_max3_u32 v96, v96, v66, v68
v_max3_u32 v96, v96, v70, v72
v_max3_u32 v96, v96, v74, v76
v_max3_u32 v96, v96, v78, v80
v_max3_u32 v96, v96, v82, v84
v_max3_u32 v96, v96, v86, v88
v_max3_u32 v96, v96, v90, v92
v_max_u32 v96, v96, v94
v_mov_b32 v99, v96
s_nop 1
v_max_u32_dpp v99, v99, v99 row_shr:1 row_mask:0xf bank_mask:0xf
s_nop 1
v_max_u32_dpp v99, v99, v99 row_shr:2 row_mask:0xf bank_mask:0xf
s_nop 1
v_max_u32_dpp v99, v99, v99 row_shr:4 row_mask:0xf bank_mask:0xf
s_nop 1
v_max_u32_dpp v99, v99, v99 row_shr:8 row_mask:0xf bank_mask:0xf
s_nop 1
v_max_u32_dpp v99, v99, v99 row_bcast:15 row_mask:0xa bank_mask:0xf
s_nop 1
v_max_u32_dpp v99, v99, v99 row_bcast:31 row_mask:0xc bank_mask:0xf
s_nop 0
v_readlane_b32 s86, v99, 63
s_nop 1
v_cmp_eq_u32 vcc, s86, v32
s_nop 1
v_cndmask_b32_e64 v32, v32, 0, vcc
v_cmp_eq_u32 vcc, s86, v34
s_nop 1
v_cndmask_b32_e64 v34, v34, 0, vcc
v_cmp_eq_u32 vcc, s86, v36
s_nop 1
v_cndmask_b32_e64 v36, v36, 0, vcc
v_cmp_eq_u32 vcc, s86, v38
s_nop 1
v_cndmask_b32_e64 v38, v38, 0, vcc
v_cmp_eq_u32 vcc, s86, v40
s_nop 1
v_cndmask_b32_e64 v40, v40, 0, vcc
v_cmp_eq_u32 vcc, s86, v42
s_nop 1
v_cndmask_b32_e64 v42, v42, 0, vcc
v_cmp_eq_u32 vcc, s86, v44
s_nop 1
v_cndmask_b32_e64 v44, v44, 0, vcc
v_cmp_eq_u32 vcc, s86, v46
s_nop 1
v_cndmask_b32_e64 v46, v46, 0, vcc
v_cmp_eq_u32 vcc, s86, v48
s_nop 1
v_cndmask_b32_e64 v48, v48, 0, vcc
v_cmp_eq_u32 vcc, s86, v50
s_nop 1
v_cndmask_b32_e64 v50, v50, 0, vcc
v_cmp_eq_u32 vcc, s86, v52
s_nop 1
v_cndmask_b32_e64 v52, v52, 0, vcc
v_cmp_eq_u32 vcc, s86, v54
s_nop 1
v_cndmask_b32_e64 v54, v54, 0, vcc
v_cmp_eq_u32 vcc, s86, v56
s_nop 1
v_cndmask_b32_e64 v56, v56, 0, vcc
v_cmp_eq_u32 vcc, s86, v58
s_nop 1
v_cndmask_b32_e64 v58, v58, 0, vcc
v_cmp_eq_u32 vcc, s86, v60
s_nop 1
v_cndmask_b32_e64 v60, v60, 0, vcc
v_cmp_eq_u32 vcc, s86, v62
)"
R"(s_nop 1
v_cndmask_b32_e64 v62, v62, 0, vcc
v_cmp_eq_u32 vcc, s86, v64
s_nop 1
v_cndmask_b32_e64 v64, v64, 0, vcc
v_cmp_eq_u32 vcc, s86, v66
s_nop 1
v_cndmask_b32_e64 v66, v66, 0, vcc
v_cmp_eq_u32 vcc, s86, v68
s_nop 1
v_cndmask_b32_e64 v68, v68, 0, vcc
v_cmp_eq_u32 vcc, s86, v70
s_nop 1
v_cndmask_b32_e64 v70, v70, 0, vcc
v_cmp_eq_u32 vcc, s86, v72
s_nop 1
v_cndmask_b32_e64 v72, v72, 0, vcc
v_cmp_eq_u32 vcc, s86, v74
s_nop 1
v_cndmask_b32_e64 v74, v74, 0, vcc
v_cmp_eq_u32 vcc, s86, v76
s_nop 1
v_cndmask_b32_e64 v76, v76, 0, vcc
v_cmp_eq_u32 vcc, s86, v78
s_nop 1
v_cndmask_b32_e64 v78, v78, 0, vcc
v_cmp_eq_u32 vcc, s86, v80
s_nop 1
v_cndmask_b32_e64 v80, v80, 0, vcc
v_cmp_eq_u32 vcc, s86, v82
s_nop 1
v_cndmask_b32_e64 v82, v82, 0, vcc
v_cmp_eq_u32 vcc, s86, v84
s_nop 1
v_cndmask_b32_e64 v84, v84, 0, vcc
v_cmp_eq_u32 vcc, s86, v86
s_nop 1
v_cndmask_b32_e64 v86, v86, 0, vcc
v_cmp_eq_u32 vcc, s86, v88
s_nop 1
v_cndmask_b32_e64 v88, v88, 0, vcc
v_cmp_eq_u32 vcc, s86, v90
s_nop 1
v_cndmask_b32_e64 v90, v90, 0, vcc
v_cmp_eq_u32 vcc, s86, v92
s_nop 1
v_cndmask_b32_e64 v92, v92, 0, vcc
v_cmp_eq_u32 vcc, s86, v94
s_nop 1
v_cndmask_b32_e64 v94, v94, 0, vcc
s_and_b32 s87, s86, 0xffff
s_sub_u32 s87, 2047, s87
s_nop 0
v_writelane_b32 v100, s87, m0
s_add_u32 m0, m0, 1
s_cmp_lt_u32 m0, 8
s_cbranch_scc1 .LL_30%=
.LL_29%=:
v_xor_b32 v102, s12, v100
v_mov_b32 v103, s13
s_mov_b64 exec, 0xff
buffer_store_dwordx2 v[102:103], v136, s[8:11], 0 offen offset:3584 sc1
s_mov_b64 exec, -1
s_branch .Lrtr_26%=
.Lspin_51%=:
s_mov_b32 s84, 0
.Llp_53%=:
s_sleep 2
buffer_load_dwordx2 v[8:9], v11, s[8:11], 0 offen offset:3840 sc1
s_waitcnt vmcnt(0)
s_add_u32 s84, s84, 1
v_cmp_eq_u64 vcc, s[12:13], v[8:9]
s_cmp_eq_u64 vcc, -1
s_cbranch_scc1 .Lok_52%=
s_cmp_lt_u32 s84, 1048576
s_cbranch_scc1 .Llp_53%=
s_branch .Lok_52%=
.Lredo%=:
s_waitcnt vmcnt(0) lgkmcnt(0)
s_barrier
s_and_b32 s81, s14, 1
s_mul_i32 s91, s81, 17164288
s_mul_i32 s82, s56, 536576
s_add_u32 s91, s91, s82
s_add_u32 s91, s91, 4194304
s_lshr_b32 s82, s14, 1
s_lshl_b32 s83, s82, 14
s_add_u32 s91, s91, s83
s_lshl_b32 s81, s81, 14
s_lshl_b32 s82, s82, 16
s_or_b32 s92, s81, s82
s_xor_b32 s92, s92, 0x10000
s_add_u32 m0, s92, 0
s_add_u32 s81, s91, 0
Dc 1,8,81,0
Dc 1,8,81,1024
Dc 1,8,81,2048
Dc 1,8,81,3072
s_add_u32 m0, s92, 4096
s_add_u32 s81, s91, 4096
Dc 1,8,81,0
Dc 1,8,81,1024
Dc 1,8,81,2048
Dc 1,8,81,3072
s_add_u32 m0, s92, 8192
s_add_u32 s81, s91, 8192
Dc 1,8,81,0
Dc 1,8,81,1024
Dc 1,8,81,2048
Dc 1,8,81,3072
s_add_u32 m0, s92, 12288
s_add_u32 s81, s91, 12288
Dc 1,8,81,0
Dc 1,8,81,1024
Dc 1,8,81,2048
Dc 1,8,81,3072
v_bfe_u32 v244, v1, 4, 1
v_mul_u32_u24 v244, 0xaaaa, v244
v_add_u32 v244, 0x4444, v244
v_lshl_or_b32 v240, v244, 16, v244
v_mov_b32 v241, 0xff800000
v_add_u32 v242, 0x10000, v1
v_bfe_u32 v244, v1, 4, 4
v_lshrrev_b32 v245, 8, v1
v_lshlrev_b32 v245, 2, v245
v_sub_u32 v244, v244, v245
v_cmp_le_i32_e64 s[72:73], 0, v244
v_cmp_le_i32_e64 s[74:75], 1, v244
v_cmp_le_i32_e64 s[76:77], 2, v244
v_cmp_le_i32_e64 s[78:79], 3, v244
v_add_u32 v243, 4096, v1
v_add_u32 v244, 8192, v1
v_add_u32 v245, 12288, v1
s_mov_b32 s18, 0x3f803f80
s_mov_b32 s19, 0x3f003f00
v_accvgpr_write_b32 a176, s18
v_accvgpr_write_b32 a180, s19
v_mov_b32 v252, v241
v_accvgpr_write_b32 a177, s18
v_accvgpr_write_b32 a181, s19
v_mov_b32 v253, v241
v_accvgpr_write_b32 a178, s18
v_accvgpr_write_b32 a182, s19
v_mov_b32 v254, v241
v_accvgpr_write_b32 a179, s18
v_accvgpr_write_b32 a183, s19
v_mov_b32 v255, v241
v_accvgpr_write_b32 a160, 0
v_accvgpr_write_b32 a161, 0
v_accvgpr_write_b32 a162, 0
v_accvgpr_write_b32 a163, 0
v_accvgpr_write_b32 a164, 0
v_accvgpr_write_b32 a165, 0
v_accvgpr_write_b32 a166, 0
v_accvgpr_write_b32 a167, 0
v_accvgpr_write_b32 a168, 0
v_accvgpr_write_b32 a169, 0
v_accvgpr_write_b32 a170, 0
v_accvgpr_write_b32 a171, 0
v_accvgpr_write_b32 a172, 0
v_accvgpr_write_b32 a173, 0
v_accvgpr_write_b32 a174, 0
v_accvgpr_write_b32 a175, 0
s_mov_b32 s90, 0x5d800000
s_mov_b64 s[88:89], 0
s_mov_b32 s64, 0
s_and_b32 s18, s14, 1
s_lshl_b32 s62, s18, 14
s_lshl_b32 s20, s57, 15
s_sub_u32 s20, 507904, s20
s_add_u32 s20, s20, s62
s_lshl_b32 s65, s57, 1
s_sub_u32 s65, s65, 1
s_max_i32 s65, s65, 0
s_sub_u32 s66, s60, 1
s_cmp_lt_u32 s14, 2
s_cselect_b32 s20, s20, 0
s_cselect_b32 s66, s66, 15
s_cselect_b32 s21, 32768, 0
s_add_u32 s61, s91, s20
s_add_u32 s62, s62, s21
s_sub_u32 s63, 16384, s21
s_lshl_b32 s67, s60, 7
s_add_u32 s67, s67, s56
s_lshl_b32 s67, s67, 12
s_add_u32 s67, s67, 39845888
s_lshl_b32 s69, s60, 3
s_add_u32 s69, s69, s59
s_lshl_b32 s69, s69, 6
s_lshl_b32 s21, s58, 3
s_add_u32 s69, s69, s21
s_add_u32 s69, s69, 20480
s_mov_b32 s70, 0
s_mov_b32 s37, 0
v_bfe_u32 v28, v1, 4, 4
v_lshrrev_b32 v29, 8, v1
v_lshlrev_b32 v29, 2, v29
v_sub_u32 v28, v28, v29
s_mov_b64 s[24:25], 0
s_mov_b32 s68, 0
s_mov_b32 s85, 99
s_lshl_b32 s80, s60, 6
s_and_b32 s81, s14, 2
s_lshl_b32 s81, s81, 4
s_xor_b32 s82, s81, 0
s_add_u32 s82, s82, s80
v_add_u32 v4, s82, v28
s_xor_b32 s82, s81, 16
s_add_u32 s82, s82, s80
v_add_u32 v5, s82, v28
s_xor_b32 s82, s81, 32
s_add_u32 s82, s82, s80
v_add_u32 v6, s82, v28
s_xor_b32 s82, s81, 48
s_add_u32 s82, s82, s80
v_add_u32 v7, s82, v28
s_mul_i32 s82, s56, 536576
s_add_u32 s82, s82, 4194304
s_lshl_b32 s84, s60, 1
s_add_u32 s84, s84, 2
.LL_54%=:
buffer_load_dwordx4 a[128:131], v1, s[8:11], s82 offen offset:0 sc1
buffer_load_dwordx4 a[132:135], v1, s[8:11], s82 offen offset:1024 sc1
buffer_load_dwordx4 a[136:139], v1, s[8:11], s82 offen offset:2048 sc1
buffer_load_dwordx4 a[140:143], v1, s[8:11], s82 offen offset:3072 sc1
buffer_load_dwordx4 a[144:147], v243, s[8:11], s82 offen offset:0 sc1
buffer_load_dwordx4 a[148:151], v243, s[8:11], s82 offen offset:1024 sc1
buffer_load_dwordx4 a[152:155], v243, s[8:11], s82 offen offset:2048 sc1
buffer_load_dwordx4 a[156:159], v243, s[8:11], s82 offen offset:3072 sc1
s_waitcnt vmcnt(0)
Zvaa 160,128,0
Zvaa 164,128,16
Zvaa 168,128,32
Zvaa 172,128,48
Mvaa 160,132,4
Mvaa 164,132,20
Mvaa 168,132,36
Mvaa 172,132,52
Mvaa 160,136,8
Mvaa 164,136,24
Mvaa 168,136,40
Mvaa 172,136,56
Mvaa 160,140,12
Mvaa 164,140,28
Mvaa 168,140,44
Mvaa 172,140,60
Zvaa 176,144,0
Zvaa 180,144,16
Zvaa 184,144,32
Zvaa 188,144,48
Mvaa 176,148,4
Mvaa 180,148,20
Mvaa 184,148,36
Mvaa 188,148,52
Mvaa 176,152,8
Mvaa 180,152,24
Mvaa 184,152,40
Mvaa 188,152,56
Mvaa 176,156,12
Mvaa 180,156,28
Mvaa 184,156,44
Mvaa 188,156,60
v_cmp_le_i32 vcc, 0, v4
s_nop 1
v_cndmask_b32 v160, v241, v160, vcc
v_max_f32 v252, v252, v160
v_cmp_le_i32 vcc, 1, v4
s_nop 1
v_cndmask_b32 v161, v241, v161, vcc
v_max_f32 v252, v252, v161
v_cmp_le_i32 vcc, 2, v4
s_nop 1
v_cndmask_b32 v162, v241, v162, vcc
v_max_f32 v252, v252, v162
v_cmp_le_i32 vcc, 3, v4
s_nop 1
v_cndmask_b32 v163, v241, v163, vcc
v_max_f32 v252, v252, v163
v_cmp_le_i32 vcc, 0, v5
s_nop 1
v_cndmask_b32 v164, v241, v164, vcc
v_max_f32 v253, v253, v164
v_cmp_le_i32 vcc, 1, v5
s_nop 1
v_cndmask_b32 v165, v241, v165, vcc
v_max_f32 v253, v253, v165
v_cmp_le_i32 vcc, 2, v5
s_nop 1
v_cndmask_b32 v166, v241, v166, vcc
v_max_f32 v253, v253, v166
v_cmp_le_i32 vcc, 3, v5
s_nop 1
v_cndmask_b32 v167, v241, v167, vcc
v_max_f32 v253, v253, v167
v_cmp_le_i32 vcc, 0, v6
s_nop 1
v_cndmask_b32 v168, v241, v168, vcc
v_max_f32 v254, v254, v168
v_cmp_le_i32 vcc, 1, v6
s_nop 1
v_cndmask_b32 v169, v241, v169, vcc
v_max_f32 v254, v254, v169
v_cmp_le_i32 vcc, 2, v6
s_nop 1
v_cndmask_b32 v170, v241, v170, vcc
v_max_f32 v254, v254, v170
v_cmp_le_i32 vcc, 3, v6
s_nop 1
v_cndmask_b32 v171, v241, v171, vcc
v_max_f32 v254, v254, v171
v_cmp_le_i32 vcc, 0, v7
s_nop 1
v_cndmask_b32 v172, v241, v172, vcc
v_max_f32 v255, v255, v172
v_cmp_le_i32 vcc, 1, v7
s_nop 1
v_cndmask_b32 v173, v241, v173, vcc
v_max_f32 v255, v255, v173
v_cmp_le_i32 vcc, 2, v7
s_nop 1
v_cndmask_b32 v174, v241, v174, vcc
v_max_f32 v255, v255, v174
v_cmp_le_i32 vcc, 3, v7
s_nop 1
v_cndmask_b32 v175, v241, v175, vcc
v_max_f32 v255, v255, v175
v_cmp_le_i32 vcc, 16, v4
s_nop 1
v_cndmask_b32 v176, v241, v176, vcc
v_max_f32 v252, v252, v176
v_cmp_le_i32 vcc, 17, v4
s_nop 1
v_cndmask_b32 v177, v241, v177, vcc
v_max_f32 v252, v252, v177
v_cmp_le_i32 vcc, 18, v4
s_nop 1
v_cndmask_b32 v178, v241, v178, vcc
v_max_f32 v252, v252, v178
v_cmp_le_i32 vcc, 19, v4
s_nop 1
v_cndmask_b32 v179, v241, v179, vcc
v_max_f32 v252, v252, v179
v_cmp_le_i32 vcc, 16, v5
s_nop 1
v_cndmask_b32 v180, v241, v180, vcc
v_max_f32 v253, v253, v180
v_cmp_le_i32 vcc, 17, v5
s_nop 1
v_cndmask_b32 v181, v241, v181, vcc
v_max_f32 v253, v253, v181
v_cmp_le_i32 vcc, 18, v5
s_nop 1
v_cndmask_b32 v182, v241, v182, vcc
v_max_f32 v253, v253, v182
v_cmp_le_i32 vcc, 19, v5
s_nop 1
v_cndmask_b32 v183, v241, v183, vcc
v_max_f32 v253, v253, v183
v_cmp_le_i32 vcc, 16, v6
s_nop 1
v_cndmask_b32 v184, v241, v184, vcc
v_max_f32 v254, v254, v184
v_cmp_le_i32 vcc, 17, v6
s_nop 1
v_cndmask_b32 v185, v241, v185, vcc
v_max_f32 v254, v254, v185
v_cmp_le_i32 vcc, 18, v6
s_nop 1
v_cndmask_b32 v186, v241, v186, vcc
v_max_f32 v254, v254, v186
v_cmp_le_i32 vcc, 19, v6
s_nop 1
v_cndmask_b32 v187, v241, v187, vcc
v_max_f32 v254, v254, v187
v_cmp_le_i32 vcc, 16, v7
s_nop 1
v_cndmask_b32 v188, v241, v188, vcc
v_max_f32 v255, v255, v188
v_cmp_le_i32 vcc, 17, v7
s_nop 1
v_cndmask_b32 v189, v241, v189, vcc
v_max_f32 v255, v255, v189
v_cmp_le_i32 vcc, 18, v7
s_nop 1
v_cndmask_b32 v190, v241, v190, vcc
v_max_f32 v255, v255, v190
v_cmp_le_i32 vcc, 19, v7
s_nop 1
v_cndmask_b32 v191, v241, v191, vcc
v_max_f32 v255, v255, v191
v_subrev_u32 v4, 32, v4
v_subrev_u32 v5, 32, v5
v_subrev_u32 v6, 32, v6
v_subrev_u32 v7, 32, v7
s_add_u32 s82, s82, 8192
s_sub_u32 s84, s84, 1
s_cmp_lg_u32 s84, 0
s_cbranch_scc1 .LL_54%=
G 28,0
v_lshlrev_b32 v32, 16, v28
)"
R"(v_and_b32 v33, 0xffff0000, v28
G 28,1
v_lshlrev_b32 v34, 16, v28
v_and_b32 v35, 0xffff0000, v28
G 28,2
v_lshlrev_b32 v36, 16, v28
v_and_b32 v37, 0xffff0000, v28
G 28,3
v_lshlrev_b32 v38, 16, v28
v_and_b32 v39, 0xffff0000, v28
G 28,4
v_lshlrev_b32 v40, 16, v28
v_and_b32 v41, 0xffff0000, v28
G 28,5
v_lshlrev_b32 v42, 16, v28
v_and_b32 v43, 0xffff0000, v28
G 28,6
v_lshlrev_b32 v44, 16, v28
v_and_b32 v45, 0xffff0000, v28
G 28,7
v_lshlrev_b32 v46, 16, v28
v_and_b32 v47, 0xffff0000, v28
G 28,8
v_lshlrev_b32 v48, 16, v28
v_and_b32 v49, 0xffff0000, v28
G 28,9
v_lshlrev_b32 v50, 16, v28
v_and_b32 v51, 0xffff0000, v28
G 28,10
v_lshlrev_b32 v52, 16, v28
v_and_b32 v53, 0xffff0000, v28
G 28,11
v_lshlrev_b32 v54, 16, v28
v_and_b32 v55, 0xffff0000, v28
G 28,12
v_lshlrev_b32 v56, 16, v28
v_and_b32 v57, 0xffff0000, v28
G 28,13
v_lshlrev_b32 v58, 16, v28
v_and_b32 v59, 0xffff0000, v28
G 28,14
v_lshlrev_b32 v60, 16, v28
v_and_b32 v61, 0xffff0000, v28
G 28,15
v_lshlrev_b32 v62, 16, v28
v_and_b32 v63, 0xffff0000, v28
G 28,16
v_lshlrev_b32 v64, 16, v28
v_and_b32 v65, 0xffff0000, v28
G 28,17
v_lshlrev_b32 v66, 16, v28
v_and_b32 v67, 0xffff0000, v28
G 28,18
v_lshlrev_b32 v68, 16, v28
v_and_b32 v69, 0xffff0000, v28
G 28,19
v_lshlrev_b32 v70, 16, v28
v_and_b32 v71, 0xffff0000, v28
G 28,20
v_lshlrev_b32 v72, 16, v28
v_and_b32 v73, 0xffff0000, v28
G 28,21
v_lshlrev_b32 v74, 16, v28
v_and_b32 v75, 0xffff0000, v28
G 28,22
v_lshlrev_b32 v76, 16, v28
v_and_b32 v77, 0xffff0000, v28
G 28,23
v_lshlrev_b32 v78, 16, v28
v_and_b32 v79, 0xffff0000, v28
G 28,24
v_lshlrev_b32 v80, 16, v28
v_and_b32 v81, 0xffff0000, v28
G 28,25
v_lshlrev_b32 v82, 16, v28
v_and_b32 v83, 0xffff0000, v28
G 28,26
v_lshlrev_b32 v84, 16, v28
v_and_b32 v85, 0xffff0000, v28
G 28,27
v_lshlrev_b32 v86, 16, v28
v_and_b32 v87, 0xffff0000, v28
G 28,28
v_lshlrev_b32 v88, 16, v28
v_and_b32 v89, 0xffff0000, v28
G 28,29
v_lshlrev_b32 v90, 16, v28
v_and_b32 v91, 0xffff0000, v28
G 28,30
v_lshlrev_b32 v92, 16, v28
v_and_b32 v93, 0xffff0000, v28
G 28,31
v_lshlrev_b32 v94, 16, v28
v_and_b32 v95, 0xffff0000, v28
G 28,32
v_lshlrev_b32 v96, 16, v28
v_and_b32 v97, 0xffff0000, v28
G 28,33
v_lshlrev_b32 v98, 16, v28
v_and_b32 v99, 0xffff0000, v28
G 28,34
v_lshlrev_b32 v100, 16, v28
v_and_b32 v101, 0xffff0000, v28
G 28,35
v_lshlrev_b32 v102, 16, v28
v_and_b32 v103, 0xffff0000, v28
G 28,36
v_lshlrev_b32 v104, 16, v28
v_and_b32 v105, 0xffff0000, v28
G 28,37
v_lshlrev_b32 v106, 16, v28
v_and_b32 v107, 0xffff0000, v28
G 28,38
v_lshlrev_b32 v108, 16, v28
v_and_b32 v109, 0xffff0000, v28
G 28,39
v_lshlrev_b32 v110, 16, v28
v_and_b32 v111, 0xffff0000, v28
G 28,40
v_lshlrev_b32 v112, 16, v28
v_and_b32 v113, 0xffff0000, v28
G 28,41
v_lshlrev_b32 v114, 16, v28
v_and_b32 v115, 0xffff0000, v28
G 28,42
v_lshlrev_b32 v116, 16, v28
v_and_b32 v117, 0xffff0000, v28
G 28,43
v_lshlrev_b32 v118, 16, v28
v_and_b32 v119, 0xffff0000, v28
G 28,44
v_lshlrev_b32 v120, 16, v28
v_and_b32 v121, 0xffff0000, v28
G 28,45
v_lshlrev_b32 v122, 16, v28
v_and_b32 v123, 0xffff0000, v28
G 28,46
v_lshlrev_b32 v124, 16, v28
v_and_b32 v125, 0xffff0000, v28
G 28,47
v_lshlrev_b32 v126, 16, v28
v_and_b32 v127, 0xffff0000, v28
G 28,48
v_lshlrev_b32 v128, 16, v28
v_and_b32 v129, 0xffff0000, v28
G 28,49
v_lshlrev_b32 v130, 16, v28
v_and_b32 v131, 0xffff0000, v28
G 28,50
v_lshlrev_b32 v132, 16, v28
v_and_b32 v133, 0xffff0000, v28
G 28,51
v_lshlrev_b32 v134, 16, v28
v_and_b32 v135, 0xffff0000, v28
G 28,52
v_lshlrev_b32 v136, 16, v28
v_and_b32 v137, 0xffff0000, v28
G 28,53
v_lshlrev_b32 v138, 16, v28
v_and_b32 v139, 0xffff0000, v28
G 28,54
v_lshlrev_b32 v140, 16, v28
v_and_b32 v141, 0xffff0000, v28
G 28,55
v_lshlrev_b32 v142, 16, v28
v_and_b32 v143, 0xffff0000, v28
G 28,56
v_lshlrev_b32 v144, 16, v28
v_and_b32 v145, 0xffff0000, v28
G 28,57
v_lshlrev_b32 v146, 16, v28
v_and_b32 v147, 0xffff0000, v28
G 28,58
v_lshlrev_b32 v148, 16, v28
v_and_b32 v149, 0xffff0000, v28
G 28,59
v_lshlrev_b32 v150, 16, v28
v_and_b32 v151, 0xffff0000, v28
G 28,60
v_lshlrev_b32 v152, 16, v28
v_and_b32 v153, 0xffff0000, v28
G 28,61
v_lshlrev_b32 v154, 16, v28
v_and_b32 v155, 0xffff0000, v28
G 28,62
v_lshlrev_b32 v156, 16, v28
v_and_b32 v157, 0xffff0000, v28
G 28,63
v_lshlrev_b32 v158, 16, v28
v_and_b32 v159, 0xffff0000, v28
s_mov_b32 s22, 1.0
s_mov_b32 s23, 1.0
s_mov_b32 s29, 134217728
s_mov_b32 s71, 3584
s_barrier
Ra 128,242,0
Ra 132,242,1024
Ra 136,242,2048
Ra 140,242,3072
s_cmp_ge_u32 s14, 2
s_cbranch_scc1 .Lbgo%=
s_branch .Lago%=
.p2align 12
.Lpb_31%=:
s_waitcnt vmcnt(6) lgkmcnt(6)
Zvaa 32,0,32
s_add_u32 s28, s28, 512
Ta 20,4,8192
Ta 22,4,9216
Mvaa 32,4,48
s_cmp_eq_u32 s28, s4
s_cselect_b32 s93, 10240, 0
Ta 24,5,0
Ta 26,5,1024
Zvaa 64,4,52
s_sub_u32 s28, s28, s93
s_add_u32 s93, s28, 20480
buffer_load_dwordx4 a[128:131], v26, s[8:11], s28 offen offset:0 sc1
Mvaa 64,0,36
Ta 28,5,8192
Ta 30,5,9216
Zvaa 96,0,40
K 184,184,185
K 185,186,187
Mvaa 96,4,56
K 186,188,189
K 187,190,191
Zvaa 128,4,60
K 192,192,193
Mvaa 128,0,44
W 8
buffer_load_dwordx4 a[132:135], v26, s[8:11], s93 offen offset:0 sc1
K 193,194,195
Zvaa 132,8,44
Ta 0,6,0
Ta 2,6,1024
Mvaa 132,12,60
K 194,196,197
K 195,198,199
Zvaa 100,12,56
K 200,200,201
K 201,202,203
Mvaa 100,8,40
Ta 4,6,8192
Ta 6,6,9216
Zvaa 68,8,36
buffer_load_dwordx4 a[136:139], v27, s[8:11], s28 offen offset:0 sc1
K 202,204,205
Mvaa 68,12,52
K 203,206,207
K 208,208,209
Zvaa 36,12,48
K 209,210,211
K 210,212,213
Mvaa 36,8,32
W 8
Ta 8,7,0
K 211,214,215
Zvaa 40,16,32
Ta 10,7,1024
K 216,216,217
Mvaa 40,20,48
buffer_load_dwordx4 a[140:143], v27, s[8:11], s93 offen offset:0 sc1
K 217,218,219
Zvaa 72,20,52
s_add_u32 s28, s28, 512
s_cmp_eq_u32 s28, s4
s_cselect_b32 s93, 10240, 0
s_sub_u32 s28, s28, s93
Mvaa 72,16,36
s_add_u32 s93, s28, 20480
K 218,220,221
K 219,222,223
Zvaa 104,16,40
Ta 12,7,8192
Ta 14,7,9216
Mvaa 104,20,56
K 224,224,225
K 225,226,227
Zvaa 136,20,60
K 226,228,229
K 227,230,231
Mvaa 136,16,44
W 8
buffer_load_dwordx4 a[144:147], v26, s[8:11], s28 offen offset:0 sc1
K 232,232,233
Zvaa 140,24,44
K 233,234,235
K 234,236,237
Mvaa 140,28,60
Ta 16,8,0
K 235,238,239
Zvaa 108,28,56
Ta 18,8,1024
K 240,240,241
Mvaa 108,24,40
K 241,242,243
K 242,244,245
Zvaa 76,24,36
buffer_load_dwordx4 a[148:151], v26, s[8:11], s93 offen offset:0 sc1
K 243,246,247
Mvaa 76,28,52
Ta 20,8,8192
Ta 22,8,9216
Zvaa 44,28,48
K 248,248,249
K 249,250,251
Mvaa 44,24,32
K 250,252,253
K 251,254,255
W 8
Zvaa 48,0,32
Ta 24,9,0
Ta 26,9,1024
Mvaa 48,4,48
buffer_load_dwordx4 a[152:155], v27, s[8:11], s28 offen offset:0 sc1
K 160,160,161
Zvaa 80,4,52
K 161,162,163
K 162,164,165
Mvaa 80,0,36
Ta 28,9,8192
K 163,166,167
Zvaa 112,0,40
Ta 30,9,9216
K 168,168,169
Mvaa 112,4,56
K 169,170,171
K 170,172,173
Zvaa 144,4,60
buffer_load_dwordx4 a[156:159], v27, s[8:11], s93 offen offset:0 sc1
K 171,174,175
Mvaa 144,0,44
W 8
s_add_u32 s28, s28, 512
s_cmp_eq_u32 s28, s4
s_cselect_b32 s93, 10240, 0
Zvaa 148,8,44
s_sub_u32 s28, s28, s93
s_add_u32 s93, s28, 20480
G 164,184
Mvaa 148,12,60
G 165,185
G 166,186
Zvaa 116,12,56
Ta 0,2,16384
Ta 2,2,17408
Mvaa 116,8,40
buffer_load_dwordx4 v[188:191], v26, s[8:11], s28 offen offset:0 sc1
G 167,187
Zvaa 84,8,36
Ta 4,2,24576
G 172,188
Mvaa 84,12,52
Ta 6,2,25600
G 173,189
Zvaa 52,12,48
G 174,190
G 175,191
Mvaa 52,8,32
W 8
Ta 8,3,16384
K 164,164,165
Zvaa 56,16,32
buffer_load_dwordx4 v[196:199], v26, s[8:11], s93 offen offset:0 sc1
Ta 10,3,17408
Mvaa 56,20,48
K 165,166,167
K 166,172,173
Zvaa 88,20,52
Ta 12,3,24576
K 167,174,175
Mvaa 88,16,36
Ta 14,3,25600
v_accvgpr_write_b32 a184, v164
Zvaa 120,16,40
Mvaa 120,20,56
buffer_load_dwordx4 v[204:207], v27, s[8:11], s28 offen offset:0 sc1
v_accvgpr_write_b32 a185, v165
Zvaa 152,20,60
v_accvgpr_write_b32 a186, v166
Mvaa 152,16,44
W 8
Ta 16,4,16384
v_accvgpr_write_b32 a187, v167
Zvaa 156,24,44
Ta 18,4,17408
v_accvgpr_write_b32 a188, v240
Mvaa 156,28,60
Zvaa 124,28,56
buffer_load_dwordx4 v[212:215], v27, s[8:11], s93 offen offset:0 sc1
v_accvgpr_write_b32 a189, v241
Mvaa 124,24,40
s_add_u32 s28, s28, 512
s_cmp_eq_u32 s28, s4
s_cselect_b32 s93, 10240, 0
s_sub_u32 s28, s28, s93
Zvaa 92,24,36
s_add_u32 s93, s28, 20480
Ta 20,4,24576
)"
R"(Ta 22,4,25600
Mvaa 92,28,52
v_accvgpr_write_b32 a190, v242
v_accvgpr_write_b32 a191, v243
Zvaa 60,28,48
G 164,192
Mvaa 60,24,32
s_waitcnt vmcnt(4) lgkmcnt(8)
buffer_load_dwordx4 v[220:223], v26, s[8:11], s28 offen offset:0 sc1
G 165,193
Mvaa 32,0,128
Ta 24,5,16384
Ta 26,5,17408
Mvaa 32,4,144
G 166,194
G 167,195
Mvaa 64,4,148
Ta 28,5,24576
Ta 30,5,25600
Mvaa 64,0,132
G 172,196
Mvaa 96,0,136
buffer_load_dwordx4 v[228:231], v26, s[8:11], s93 offen offset:0 sc1
G 173,197
Mvaa 96,4,152
G 174,198
Mvaa 128,4,156
G 175,199
Mvaa 128,0,140
W 8
Ta 0,6,16384
Ta 2,6,17408
Mvaa 132,8,140
K 164,164,165
Mvaa 132,12,156
buffer_load_dwordx4 v[236:239], v27, s[8:11], s28 offen offset:0 sc1
K 165,166,167
Mvaa 100,12,152
Ta 4,6,24576
K 166,172,173
Mvaa 100,8,136
Ta 6,6,25600
K 167,174,175
Mvaa 68,8,132
v_accvgpr_write_b32 a192, v164
Mvaa 68,12,148
Mvaa 36,12,144
buffer_load_dwordx4 v[244:247], v27, s[8:11], s93 offen offset:0 sc1
v_accvgpr_write_b32 a193, v165
Mvaa 36,8,128
W 8
s_add_u32 s28, s28, 512
s_cmp_eq_u32 s28, s4
s_cselect_b32 s93, 10240, 0
Mvaa 40,16,128
s_sub_u32 s28, s28, s93
s_add_u32 s93, s28, 20480
v_accvgpr_write_b32 a194, v166
Mvaa 40,20,144
Ta 8,7,16384
Ta 10,7,17408
Mvaa 72,20,148
v_accvgpr_write_b32 a195, v167
v_accvgpr_write_b32 a196, v248
Mvaa 72,16,132
D 22,48,65,2048
Ta 12,7,24576
Mvaa 104,16,136
Ta 14,7,25600
v_accvgpr_write_b32 a197, v249
Mvaa 104,20,152
v_accvgpr_write_b32 a198, v250
v_accvgpr_write_b32 a199, v251
Mvaa 136,20,156
G 164,200
Mvaa 136,16,140
W 8
Ta 16,8,16384
Ta 18,8,17408
Mvaa 140,24,140
buffer_load_dwordx4 a[32:35], v26, s[8:11], s28 offen offset:0 sc1
G 165,201
Mvaa 140,28,156
G 166,202
Mvaa 108,28,152
Ta 20,8,24576
G 167,203
Mvaa 108,24,136
Ta 22,8,25600
G 172,204
Mvaa 76,24,132
Mvaa 76,28,148
buffer_load_dwordx4 a[36:39], v26, s[8:11], s93 offen offset:0 sc1
G 173,205
Mvaa 44,28,144
G 174,206
Mvaa 44,24,128
W 8
Ta 24,9,16384
G 175,207
Mvaa 48,0,128
Ta 26,9,17408
K 164,164,165
Mvaa 48,4,144
Mvaa 80,4,148
buffer_load_dwordx4 a[40:43], v27, s[8:11], s28 offen offset:0 sc1
K 165,166,167
Mvaa 80,0,132
Ta 28,9,24576
Ta 30,9,25600
Mvaa 112,0,136
s_lshl_b32 s80, s56, 9
K 166,172,173
K 167,174,175
Mvaa 112,4,152
v_accvgpr_write_b32 a200, v164
Mvaa 144,4,156
s_lshl_b32 s81, s57, 4
Mvaa 144,0,140
W 8
buffer_load_dwordx4 a[44:47], v27, s[8:11], s93 offen offset:0 sc1
v_accvgpr_write_b32 a201, v165
Mvaa 148,8,140
s_add_u32 s28, s28, 512
s_cmp_eq_u32 s28, s4
s_cselect_b32 s93, 10240, 0
s_sub_u32 s28, s28, s93
Mvaa 148,12,156
s_add_u32 s93, s28, 20480
s_add_u32 s80, s80, s81
Ta 0,10,0
Mvaa 116,12,152
s_and_b32 s81, s14, 1
Ta 2,10,1024
v_accvgpr_write_b32 a202, v166
Mvaa 116,8,136
s_lshl_b32 s81, s81, 3
Ta 4,10,8192
v_accvgpr_write_b32 a203, v167
Mvaa 84,8,132
s_add_u32 s80, s80, s81
buffer_load_dwordx4 a[48:51], v26, s[8:11], s28 offen offset:0 sc1
Ta 6,10,9216
Mvaa 84,12,148
s_add_u32 s71, s80, 4096
v_accvgpr_write_b32 a204, v184
v_accvgpr_write_b32 a205, v185
Mvaa 52,12,144
v_accvgpr_write_b32 a206, v186
v_accvgpr_write_b32 a207, v187
Mvaa 52,8,128
W 8
Ta 8,11,0
Ta 10,11,1024
Mvaa 56,16,128
G 164,208
Mvaa 56,20,144
D 23,48,65,3072
G 165,209
Mvaa 88,20,148
s_add_u32 s65, s5, 16
s_cmp_ge_u32 s65, 20
s_cselect_b32 s90, 20, 0
s_sub_u32 s65, s65, s90
Mvaa 88,16,132
s_lshl_b32 s65, s65, 18
s_add_u32 m0, s63, 98304
G 166,210
Mvaa 120,16,136
Ta 12,11,8192
Ta 14,11,9216
Mvaa 120,20,152
G 167,211
G 172,212
Mvaa 152,20,156
buffer_load_dwordx4 a[52:55], v26, s[8:11], s93 offen offset:0 sc1
G 173,213
Mvaa 152,16,140
W 8
Ta 16,12,0
G 174,214
Mvaa 156,24,140
s_lshr_b32 s80, s15, 3
Ta 18,12,1024
Mvaa 156,28,156
G 175,215
Mvaa 124,28,152
s_lshl_b32 s80, s80, 1
Ta 20,12,8192
K 164,164,165
Mvaa 124,24,136
s_add_u32 s80, s80, s14
buffer_load_dwordx4 a[56:59], v27, s[8:11], s28 offen offset:0 sc1
Ta 22,12,9216
Mvaa 92,24,132
s_sub_u32 s80, s80, 2
K 165,166,167
K 166,172,173
Mvaa 92,28,148
s_lshl_b32 s80, s80, 12
Mvaa 60,28,144
K 167,174,175
v_min_u32 v28, 496, v1
Mvaa 60,24,128
s_waitcnt vmcnt(9) lgkmcnt(8)
v_accvgpr_write_b32 a208, v164
v_lshl_add_u32 v28, v28, 3, s80
Mvav 32,0,188
buffer_load_dwordx4 a[60:63], v27, s[8:11], s93 offen offset:0 sc1
Ta 24,13,0
Mvav 32,4,220
s_add_u32 s28, s28, 512
s_cmp_eq_u32 s28, s4
s_cselect_b32 s93, 10240, 0
s_sub_u32 s28, s28, s93
Mvav 64,4,228
s_add_u32 s93, s28, 20480
Ta 26,13,1024
v_min_u32 v28, .Lend%=-.Lbeg%=-4, v28
Mvav 64,0,196
v_accvgpr_write_b32 a209, v165
v_accvgpr_write_b32 a210, v166
Mvav 96,0,204
Ta 28,13,8192
Ta 30,13,9216
Mvav 96,4,236
D 16,48,65,0
v_accvgpr_write_b32 a211, v167
Mvav 128,4,244
v_accvgpr_write_b32 a212, v192
v_accvgpr_write_b32 a213, v193
Mvav 128,0,212
W 8
Ta 0,14,0
v_accvgpr_write_b32 a214, v194
Mvav 132,8,212
Ta 2,14,1024
v_add_u32 v2, 65536, v10
Mvav 132,12,244
v_accvgpr_write_b32 a215, v195
G 164,216
Mvav 100,12,236
buffer_load_dwordx4 v[184:187], v26, s[8:11], s28 offen offset:0 sc1
Ta 4,14,8192
Mvav 100,8,204
Ta 6,14,9216
v_add_u32 v3, 65536, v11
Mvav 68,8,196
G 165,217
G 166,218
Mvav 68,12,228
G 167,219
Mvav 36,12,220
G 172,220
Mvav 36,8,188
W 8
D 17,48,65,1024
Ta 8,15,0
Mvav 40,16,188
Ta 10,15,1024
v_add_u32 v4, 65536, v12
Mvav 40,20,220
G 173,221
G 174,222
Mvav 72,20,228
Ta 12,15,8192
G 175,223
Mvav 72,16,196
Ta 14,15,9216
v_add_u32 v5, 65536, v13
Mvav 104,16,204
buffer_load_dwordx4 a[128:131], v26, s[8:11], s93 offen offset:0 sc1
K 164,164,165
Mvav 104,20,236
K 165,166,167
Mvav 136,20,244
K 166,172,173
Mvav 136,16,212
W 8
Ta 16,24,0
K 167,174,175
Mvav 140,24,212
Ta 18,24,1024
v_add_u32 v6, 65536, v14
Mvav 140,28,244
buffer_load_dwordx4 a[132:135], v27, s[8:11], s28 offen offset:0 sc1
v_accvgpr_write_b32 a216, v164
Mvav 108,28,236
Ta 20,24,8192
v_accvgpr_write_b32 a217, v165
Mvav 108,24,204
Ta 22,24,9216
v_add_u32 v7, 65536, v15
Mvav 76,24,196
v_accvgpr_write_b32 a218, v166
v_accvgpr_write_b32 a219, v167
Mvav 76,28,228
v_accvgpr_write_b32 a220, v200
Mvav 44,28,220
buffer_load_dwordx4 a[136:139], v27, s[8:11], s93 offen offset:0 sc1
Mvav 44,24,188
W 8
s_add_u32 s28, s28, 512
s_cmp_eq_u32 s28, s4
s_cselect_b32 s93, 10240, 0
Mvav 48,0,188
s_sub_u32 s28, s28, s93
s_add_u32 s93, s28, 20480
v_accvgpr_write_b32 a221, v201
Mvav 48,4,220
Ta 24,25,0
Ta 26,25,1024
Mvav 80,4,228
v_add_u32 v8, 65536, v24
v_accvgpr_write_b32 a222, v202
Mvav 80,0,196
buffer_load_dwordx4 a[140:143], v26, s[8:11], s28 offen offset:0 sc1
v_accvgpr_write_b32 a223, v203
Mvav 112,0,204
Ta 28,25,8192
Ta 30,25,9216
Mvav 112,4,236
v_add_u32 v9, 65536, v25
G 164,224
Mvav 144,4,244
G 165,225
G 166,226
Mvav 144,0,212
W 8
Ta 0,10,16384
G 167,227
Mvav 148,8,212
global_load_dword v29, v28, s[6:7]
Ta 2,10,17408
Mvav 148,12,244
s_sub_u32 s74, s85, 66
s_and_b32 s75, s74, 0xffffffe3
s_cselect_b64 s[72:73], -1, 0
s_and_b32 s74, s74, 28
Mvav 116,12,236
s_lshl_b32 s74, s74, 1
v_mov_b32 v28, s74
G 172,228
Mvav 116,8,204
G 173,229
G 174,230
Mvav 84,8,196
Ta 4,10,24576
Ta 6,10,25600
Mvav 84,12,228
buffer_load_dwordx4 a[144:147], v26, s[8:11], s93 offen offset:0 sc1
G 175,231
Mvav 52,12,220
K 164,164,165
K 165,166,167
Mvav 52,8,188
W 8
Ta 8,11,16384
K 166,172,173
Mvav 56,16,188
Ta 10,11,17408
K 167,174,175
Mvav 56,20,220
v_accvgpr_write_b32 a224, v164
Mvav 88,20,228
buffer_load_dwordx4 a[148:151], v27, s[8:11], s28 offen offset:0 sc1
Ta 12,11,24576
Mvav 88,16,196
Ta 14,11,25600
v_accvgpr_write_b32 a225, v165
Mvav 120,16,204
v_accvgpr_write_b32 a226, v166
Mvav 120,20,236
v_accvgpr_write_b32 a227, v167
Mvav 152,20,244
v_accvgpr_write_b32 a228, v208
Mvav 152,16,212
W 8
D 18,48,65,2048
Ta 16,12,16384
Mvav 156,24,212
Ta 18,12,17408
v_accvgpr_write_b32 a229, v209
Mvav 156,28,244
v_accvgpr_write_b32 a230, v210
Mvav 124,28,236
Ta 20,12,24576
v_accvgpr_write_b32 a231, v211
Mvav 124,24,204
Ta 22,12,25600
G 164,232
Mvav 92,24,196
buffer_load_dwordx4 a[152:155], v27, s[8:11], s93 offen offset:0 sc1
G 165,233
Mvav 92,28,228
s_add_u32 s28, s28, 512
s_cmp_eq_u32 s28, s4
s_cselect_b32 s93, 10240, 0
s_sub_u32 s28, s28, s93
Mvav 60,28,220
)"
R"(s_add_u32 s93, s28, 20480
G 166,234
G 167,235
Mvav 60,24,188
s_waitcnt vmcnt(12) lgkmcnt(8)
G 172,236
G 173,237
Mvaa 32,0,32
Ta 24,13,16384
Ta 26,13,17408
Mvaa 32,4,48
buffer_load_dwordx2 v[28:29], v28, s[8:11], 0 offen offset:3584 sc1
G 174,238
Mvaa 64,4,52
G 175,239
K 164,164,165
Mvaa 64,0,36
Ta 28,13,24576
Ta 30,13,25600
Mvaa 96,0,40
K 165,166,167
K 166,172,173
Mvaa 96,4,56
K 167,174,175
v_accvgpr_write_b32 a232, v164
Mvaa 128,4,60
buffer_load_dwordx4 a[156:159], v26, s[8:11], s28 offen offset:0 sc1
v_accvgpr_write_b32 a233, v165
Mvaa 128,0,44
W 8
v_accvgpr_write_b32 a234, v166
v_accvgpr_write_b32 a235, v167
Mvaa 132,8,44
Ta 0,14,16384
Ta 2,14,17408
Mvaa 132,12,60
G 164,160
G 165,161
Mvaa 100,12,56
G 166,162
G 167,163
Mvaa 100,8,40
buffer_load_dwordx4 v[192:195], v26, s[8:11], s93 offen offset:0 sc1
Ta 4,14,24576
Mvaa 68,8,36
Ta 6,14,25600
G 172,164
Mvaa 68,12,52
G 173,165
G 174,166
Mvaa 36,12,48
G 175,167
K 164,164,165
Mvaa 36,8,32
W 8
Ta 8,15,16384
K 165,166,167
Mvaa 40,16,32
buffer_load_dwordx4 v[200:203], v27, s[8:11], s28 offen offset:0 sc1
Ta 10,15,17408
Mvaa 40,20,48
K 166,172,173
K 167,174,175
Mvaa 72,20,52
s_cmp_lg_u32 s72, 0
s_cbranch_scc1 .LL_55%=
s_waitcnt vmcnt(3)
.LL_55%=:
v_xor_b32 v28, s12, v28
v_xor_b32 v29, s13, v29
v_lshrrev_b32 v248, 11, v28
v_or_b32 v29, v29, v248
v_cmp_eq_u32 vcc, 0, v29
s_or_b64 vcc, vcc, s[72:73]
s_cmp_eq_u64 vcc, -1
s_cbranch_scc0 .Lspin_56%=
.Lok_57%=:
v_accvgpr_write_b32 a236, v164
v_accvgpr_write_b32 a237, v165
Mvaa 72,16,36
Ta 12,15,24576
Ta 14,15,25600
Mvaa 104,16,40
v_accvgpr_write_b32 a238, v166
v_accvgpr_write_b32 a239, v167
Mvaa 104,20,56
D 19,48,65,3072
G 164,240
Mvaa 136,20,60
s_add_u32 m0, s63, 102400
G 165,241
G 166,242
Mvaa 136,16,44
W 8
v_bfe_u32 v248, v28, 3, 8
G 167,243
Mvaa 140,24,44
Ta 16,24,16384
Ta 18,24,17408
Mvaa 140,28,60
v_mul_u32_u24 v248, 10240, v248
G 172,244
Mvaa 108,28,56
buffer_load_dwordx4 v[188:191], v27, s[8:11], s93 offen offset:0 sc1
G 173,245
Mvaa 108,24,40
s_add_u32 s28, s28, 512
s_cmp_eq_u32 s28, s4
s_cselect_b32 s93, 10240, 0
s_sub_u32 s28, s28, s93
Mvaa 76,24,36
s_add_u32 s93, s28, 20480
v_and_b32 v28, 7, v28
G 174,246
Mvaa 76,28,52
v_lshl_add_u32 v248, v28, 4, v248
G 175,247
Mvaa 44,28,48
Ta 20,24,24576
Ta 22,24,25600
Mvaa 44,24,32
W 8
buffer_load_dwordx4 v[196:199], v26, s[8:11], s28 offen offset:0 sc1
K 164,164,165
Mvaa 48,0,32
v_lshl_add_u32 v248, v1, 3, v248
K 165,166,167
Mvaa 48,4,48
v_lshrrev_b32 v29, 8, v1
K 166,172,173
Mvaa 80,4,52
K 167,174,175
v_lshlrev_b32 v29, 28, v29
Mvaa 80,0,36
v_accvgpr_write_b32 a240, v164
v_or_b32 v249, v248, v29
Mvaa 112,0,40
s_and_b32 s75, s72, 134217728
buffer_load_dwordx4 v[204:207], v26, s[8:11], s93 offen offset:0 sc1
v_accvgpr_write_b32 a241, v165
Mvaa 112,4,56
s_add_u32 s76, s75, 131072
Ta 24,25,16384
Ta 26,25,17408
Mvaa 144,4,60
v_accvgpr_write_b32 a242, v166
v_accvgpr_write_b32 a243, v167
Mvaa 144,0,44
W 6
G 164,168
G 165,169
Mvaa 148,8,44
Ta 28,25,24576
Ta 30,25,25600
Mvaa 148,12,60
D 20,48,65,0
G 166,170
Mvaa 116,12,56
G 167,171
G 172,172
Mvaa 116,8,40
G 173,173
G 174,174
Mvaa 84,8,36
Ta 0,2,0
G 175,175
Mvaa 84,12,52
Ta 2,2,1024
K 164,164,165
Mvaa 52,12,48
buffer_load_dwordx4 v[212:215], v27, s[8:11], s28 offen offset:0 sc1
K 165,166,167
Mvaa 52,8,32
W 6
K 166,172,173
K 167,174,175
Mvaa 56,16,32
Ta 4,2,8192
Ta 6,2,9216
Mvaa 56,20,48
v_accvgpr_write_b32 a244, v164
v_accvgpr_write_b32 a245, v165
Mvaa 88,20,52
v_accvgpr_write_b32 a246, v166
v_accvgpr_write_b32 a247, v167
Mvaa 88,16,36
buffer_load_dwordx4 v[220:223], v27, s[8:11], s93 offen offset:0 sc1
G 164,64
Mvaa 120,16,40
s_add_u32 s28, s28, 512
s_cmp_eq_u32 s28, s4
s_cselect_b32 s93, 10240, 0
s_sub_u32 s28, s28, s93
Mvaa 120,20,56
s_add_u32 s93, s28, 20480
Ta 8,3,0
G 165,65
Mvaa 152,20,60
Ta 10,3,1024
G 166,66
Mvaa 152,16,44
W 6
G 167,67
G 172,68
Mvaa 156,24,44
buffer_load_dwordx4 v[228:231], v26, s[8:11], s28 offen offset:0 sc1
G 173,69
Mvaa 156,28,60
Ta 12,3,8192
Ta 14,3,9216
Mvaa 124,28,56
G 174,70
G 175,71
Mvaa 124,24,40
K 164,164,165
K 165,166,167
Mvaa 92,24,36
K 166,172,173
K 167,174,175
Mvaa 92,28,52
buffer_load_dwordx4 v[240:243], v248, s[8:11], s76 offen sc1
Ta 16,4,0
Mvaa 60,28,48
s_add_u32 s76, s76, 8192
Ta 18,4,1024
v_accvgpr_write_b32 a64, v164
Mvaa 60,24,32
s_waitcnt vmcnt(13) lgkmcnt(6)
v_accvgpr_write_b32 a65, v165
v_accvgpr_write_b32 a66, v166
Mvav 32,0,184
Ta 20,4,8192
v_accvgpr_write_b32 a67, v167
Mvaa 32,4,140
Ta 22,4,9216
G 164,176
Mvaa 64,4,144
buffer_load_dwordx4 v[244:247], v249, s[8:11], s76 offen sc1
v_or_b32 v249, v1, v29
Mvaa 64,0,128
s_mul_i32 s76, s74, 160
s_add_u32 s76, s76, s75
s_add_u32 s76, s76, 118784
Mvaa 96,0,132
G 165,177
G 166,178
Mvaa 96,4,148
G 167,179
G 172,180
Mvaa 128,4,152
Ta 24,5,0
G 173,181
Mvaa 128,0,136
W 5
buffer_load_dwordx4 v[236:239], v26, s[8:11], s93 offen offset:0 sc1
Ta 26,5,1024
Mvaa 132,8,136
G 174,182
G 175,183
Mvaa 132,12,152
K 164,164,165
K 165,166,167
Mvaa 100,12,148
Ta 28,5,8192
Ta 30,5,9216
Mvaa 100,8,132
K 166,172,173
K 167,174,175
Mvaa 68,8,128
D 21,48,65,1024
v_accvgpr_write_b32 a68, v164
Mvaa 68,12,144
v_accvgpr_write_b32 a69, v165
v_accvgpr_write_b32 a70, v166
Mvaa 36,12,140
Ta 0,6,0
v_accvgpr_write_b32 a71, v167
Mvav 36,8,184
W 5
Ta 2,6,1024
G 164,72
Mvav 40,16,184
G 165,73
G 166,74
Mvaa 40,20,140
buffer_load_dwordx4 v[208:211], v27, s[8:11], s28 offen offset:0 sc1
Ta 4,6,8192
Mvaa 72,20,144
Ta 6,6,9216
G 167,75
Mvaa 72,16,128
G 172,76
G 173,77
Mvaa 104,16,132
Ta 8,7,0
G 174,78
Mvaa 104,20,148
Ta 10,7,1024
G 175,79
Mvaa 136,20,152
buffer_load_dwordx4 a[32:35], v27, s[8:11], s93 offen offset:0 sc1
K 164,164,165
Mvaa 136,16,136
W 6
s_add_u32 s28, s28, 512
s_cmp_eq_u32 s28, s4
s_cselect_b32 s93, 10240, 0
Mvaa 140,24,136
s_sub_u32 s28, s28, s93
s_add_u32 s93, s28, 20480
Ta 12,7,8192
Mvaa 140,28,152
Ta 14,7,9216
K 165,166,167
Mvaa 108,28,148
K 166,172,173
K 167,174,175
Mvaa 108,24,132
buffer_load_dwordx4 a[36:39], v26, s[8:11], s28 offen offset:0 sc1
v_accvgpr_write_b32 a72, v164
Mvaa 76,24,128
Ta 16,8,0
Ta 18,8,1024
Mvaa 76,28,144
v_accvgpr_write_b32 a73, v165
v_accvgpr_write_b32 a74, v166
Mvaa 44,28,140
Ta 20,8,8192
v_accvgpr_write_b32 a75, v167
Mvav 44,24,184
W 7
Ta 22,8,9216
v_accvgpr_write_b32 a76, v216
Mvav 48,0,184
D 22,48,65,2048
v_accvgpr_write_b32 a77, v217
Mvaa 48,4,140
v_accvgpr_write_b32 a78, v218
v_accvgpr_write_b32 a79, v219
Mvaa 80,4,144
Ta 24,9,0
Ta 26,9,1024
Mvaa 80,0,128
G 164,80
G 165,81
Mvaa 112,0,132
Ta 28,9,8192
Ta 30,9,9216
Mvaa 112,4,148
buffer_load_dwordx4 a[40:43], v26, s[8:11], s93 offen offset:0 sc1
G 166,82
Mvaa 144,4,152
G 167,83
G 172,84
Mvaa 144,0,136
W 8
Ta 0,2,16384
G 173,85
Mvaa 148,8,136
Ta 2,2,17408
G 174,86
Mvaa 148,12,152
G 175,87
Mvaa 116,12,148
buffer_load_dwordx4 a[44:47], v27, s[8:11], s28 offen offset:0 sc1
Ta 4,2,24576
Mvaa 116,8,132
Ta 6,2,25600
K 164,164,165
Mvaa 84,8,128
K 165,166,167
Mvaa 84,12,144
K 166,172,173
Mvaa 52,12,140
K 167,174,175
Mvav 52,8,184
W 8
buffer_load_dwordx4 a[48:51], v27, s[8:11], s93 offen offset:0 sc1
Ta 8,3,16384
Mvav 56,16,184
s_add_u32 s28, s28, 512
s_cmp_eq_u32 s28, s4
s_cselect_b32 s93, 10240, 0
D 23,48,65,3072
Mvaa 56,20,140
s_sub_u32 s28, s28, s93
s_add_u32 s93, s28, 20480
Ta 10,3,17408
Mvaa 88,20,144
v_accvgpr_write_b32 a80, v164
v_accvgpr_write_b32 a81, v165
Mvaa 88,16,128
Ta 12,3,24576
v_accvgpr_write_b32 a82, v166
Mvaa 120,16,132
Ta 14,3,25600
v_accvgpr_write_b32 a83, v167
Mvaa 120,20,148
buffer_load_dwordx4 a[52:55], v26, s[8:11], s28 offen offset:0 sc1
v_accvgpr_write_b32 a84, v224
)"
R"(Mvaa 152,20,152
v_lshrrev_b32 v20, 4, v1
v_accvgpr_write_b32 a85, v225
Mvaa 152,16,136
W 8
v_accvgpr_write_b32 a86, v226
v_add_u32 v20, 0, v20
Mvaa 156,24,136
Ta 16,4,16384
Ta 18,4,17408
Mvaa 156,28,152
v_accvgpr_write_b32 a87, v227
v_mul_u32_u24 v22, 205, v20
Mvaa 124,28,148
buffer_load_dwordx4 a[56:59], v26, s[8:11], s93 offen offset:0 sc1
G 164,88
Mvaa 124,24,132
Ta 20,4,24576
Ta 22,4,25600
Mvaa 92,24,128
v_lshrrev_b32 v22, 12, v22
G 165,89
Mvaa 92,28,144
G 166,90
v_mul_u32_u24 v23, 20, v22
Mvaa 60,28,140
G 167,91
v_sub_u32 v20, v20, v23
Mvav 60,24,184
s_waitcnt vmcnt(15) lgkmcnt(8)
buffer_load_dwordx4 a[60:63], v27, s[8:11], s28 offen offset:0 sc1
G 172,92
Mvaa 32,0,156
Ta 24,5,16384
Ta 26,5,17408
Mvav 32,4,196
v_lshrrev_b32 v23, 1, v22
G 173,93
Mvav 64,4,204
G 174,94
v_and_b32 v23, 2, v23
Mvav 64,0,192
Ta 28,5,24576
Ta 30,5,25600
Mvav 96,0,200
buffer_load_dwordx4 v[216:219], v27, s[8:11], s93 offen offset:0 sc1
G 175,95
Mvav 96,4,212
s_add_u32 s28, s28, 512
s_cmp_eq_u32 s28, s4
s_cselect_b32 s93, 10240, 0
s_sub_u32 s28, s28, s93
Mvav 128,4,220
s_add_u32 s93, s28, 20480
v_xor_b32 v20, v20, v23
K 164,164,165
Mvav 128,0,188
W 8
v_mul_u32_u24 v22, 80, v22
K 165,166,167
Mvav 132,8,188
K 166,172,173
v_add_lshl_u32 v16, v20, v22, 4
Mvav 132,12,220
buffer_load_dwordx4 v[184:187], v26, s[8:11], s28 offen offset:0 sc1
Ta 0,6,16384
Mvav 100,12,212
Ta 2,6,17408
K 167,174,175
Mvav 100,8,200
v_lshrrev_b32 v20, 4, v1
v_accvgpr_write_b32 a88, v164
Mvav 68,8,192
v_add_u32 v20, 64, v20
v_accvgpr_write_b32 a89, v165
Mvav 68,12,204
Ta 4,6,24576
Ta 6,6,25600
Mvav 36,12,196
s_cmp_lg_u32 s72, 0
s_cbranch_scc1 .LL_59%=
s_waitcnt vmcnt(15)
.LL_59%=:
buffer_store_dwordx4 v[240:243], v1, s[8:11], s76 offen sc1
v_accvgpr_write_b32 a90, v166
v_mul_u32_u24 v22, 205, v20
Mvaa 36,8,156
W 8
v_accvgpr_write_b32 a91, v167
v_lshrrev_b32 v22, 12, v22
Mvaa 40,16,156
v_accvgpr_write_b32 a92, v232
v_mul_u32_u24 v23, 20, v22
Mvav 40,20,196
Ta 8,7,16384
Ta 10,7,17408
Mvav 72,20,204
v_accvgpr_write_b32 a93, v233
v_accvgpr_write_b32 a94, v234
Mvav 72,16,192
buffer_store_dwordx4 v[244:247], v249, s[8:11], s76 offen offset:1024 sc1
v_sub_u32 v20, v20, v23
Mvav 104,16,200
s_add_u32 s76, s74, s75
s_add_u32 s76, s76, 3840
v_accvgpr_write_b32 a95, v235
Mvav 104,20,212
Ta 12,7,24576
Ta 14,7,25600
Mvav 136,20,220
v_lshrrev_b32 v23, 1, v22
G 164,96
Mvav 136,16,188
W 8
G 165,97
v_and_b32 v23, 2, v23
Mvav 140,24,188
buffer_load_dwordx4 a[128:131], v26, s[8:11], s93 offen offset:0 sc1
G 166,98
Mvav 140,28,220
G 167,99
v_xor_b32 v20, v20, v23
Mvav 108,28,212
G 172,100
G 173,101
Mvav 108,24,200
Ta 16,8,16384
Ta 18,8,17408
Mvav 76,24,192
v_mul_u32_u24 v22, 80, v22
G 174,102
Mvav 76,28,204
buffer_load_dwordx4 a[132:135], v27, s[8:11], s28 offen offset:0 sc1
G 175,103
Mvav 44,28,196
v_add_lshl_u32 v17, v20, v22, 4
K 164,164,165
Mvaa 44,24,156
W 6
Ta 20,8,24576
Ta 22,8,25600
Mvaa 48,0,156
K 165,166,167
K 166,172,173
Mvav 48,4,196
v_subrev_u32 v17, 1024, v17
K 167,174,175
Mvav 80,4,204
buffer_load_dwordx4 a[136:139], v27, s[8:11], s93 offen offset:0 sc1
v_accvgpr_write_b32 a96, v164
Mvav 80,0,192
s_add_u32 s28, s28, 512
v_lshrrev_b32 v20, 4, v1
v_accvgpr_write_b32 a97, v165
Mvav 112,0,200
s_cmp_eq_u32 s28, s4
s_cselect_b32 s93, 10240, 0
v_accvgpr_write_b32 a98, v166
v_add_u32 v20, 128, v20
Mvav 112,4,212
s_sub_u32 s28, s28, s93
Ta 24,9,16384
v_accvgpr_write_b32 a99, v167
Mvav 144,4,220
s_add_u32 s93, s28, 20480
Ta 26,9,17408
G 164,248
Mvav 144,0,188
W 6
buffer_load_dwordx4 a[140:143], v26, s[8:11], s28 offen offset:0 sc1
v_mul_u32_u24 v22, 205, v20
Mvav 148,8,188
G 165,249
G 166,250
Mvav 148,12,220
Ta 28,9,24576
Ta 30,9,25600
Mvav 116,12,212
v_lshrrev_b32 v22, 12, v22
G 167,251
Mvav 116,8,200
G 172,252
G 173,253
Mvav 84,8,192
s_waitcnt vmcnt(11) lgkmcnt(0)
s_barrier
v_mul_u32_u24 v23, 20, v22
Mvav 84,12,204
G 174,254
G 175,255
Mvav 52,12,196
v_sub_u32 v20, v20, v23
K 164,164,165
Mvaa 52,8,156
K 165,166,167
v_lshrrev_b32 v23, 1, v22
Mvaa 56,16,156
buffer_load_dwordx4 a[144:147], v26, s[8:11], s93 offen offset:0 sc1
K 166,172,173
Mvav 56,20,196
K 167,174,175
v_and_b32 v23, 2, v23
Mvav 88,20,204
v_accvgpr_write_b32 a100, v164
v_accvgpr_write_b32 a101, v165
Mvav 88,16,192
v_xor_b32 v20, v20, v23
v_accvgpr_write_b32 a102, v166
Mvav 120,16,200
Ta 0,10,32768
v_accvgpr_write_b32 a103, v167
Mvav 120,20,212
Ta 2,10,33792
Ta 4,10,40960
Mvav 152,20,220
Ta 6,10,41984
G 164,104
Mvav 152,16,188
v_mul_u32_u24 v22, 80, v22
G 165,105
Mvav 156,24,188
v_add_lshl_u32 v18, v20, v22, 4
G 166,106
Mvav 156,28,220
Ta 8,11,32768
Ta 10,11,33792
Mvav 124,28,212
G 167,107
v_subrev_u32 v18, 2048, v18
Mvav 124,24,200
buffer_load_dwordx4 a[148:151], v27, s[8:11], s28 offen offset:0 sc1
G 172,108
Mvav 92,24,192
G 173,109
v_lshrrev_b32 v20, 4, v1
Mvav 92,28,204
Ta 12,11,40960
Ta 14,11,41984
Mvav 60,28,196
G 174,110
v_add_u32 v20, 192, v20
Mvaa 60,24,156
W 4
G 175,111
K 164,164,165
Mvav 32,0,228
v_mul_u32_u24 v22, 205, v20
K 165,166,167
Mvaa 32,4,36
v_lshrrev_b32 v22, 12, v22
K 166,172,173
Mvaa 64,4,40
Ta 16,12,32768
Ta 18,12,33792
Mvav 64,0,236
buffer_load_dwordx4 a[152:155], v27, s[8:11], s93 offen offset:0 sc1
K 167,174,175
Mvav 96,0,208
s_add_u32 s28, s28, 512
v_mul_u32_u24 v23, 20, v22
v_accvgpr_write_b32 a104, v164
Mvaa 96,4,44
s_cmp_eq_u32 s28, s4
s_cselect_b32 s93, 10240, 0
v_accvgpr_write_b32 a105, v165
v_sub_u32 v20, v20, v23
Mvaa 128,4,48
s_sub_u32 s28, s28, s93
Ta 20,12,40960
Ta 22,12,41984
Mvaa 128,0,32
W 4
v_accvgpr_write_b32 a106, v166
v_lshrrev_b32 v23, 1, v22
Mvaa 132,8,32
s_add_u32 s93, s28, 20480
v_accvgpr_write_b32 a107, v167
K 176,176,177
Mvaa 132,12,48
v_and_b32 v23, 2, v23
K 177,178,179
Mvaa 100,12,44
K 178,180,181
v_xor_b32 v20, v20, v23
Mvav 100,8,208
Ta 24,13,32768
Ta 26,13,33792
Mvav 68,8,236
buffer_load_dwordx4 v[224:227], v26, s[8:11], s28 offen offset:0 sc1
K 179,182,183
Mvaa 68,12,40
v_accvgpr_write_b32 a108, v176
v_accvgpr_write_b32 a109, v177
v_accvgpr_write_b32 a110, v178
v_accvgpr_write_b32 a111, v179
Mvaa 36,12,36
Ta 28,13,40960
v_mul_u32_u24 v22, 80, v22
Mvav 36,8,228
W 3
Ta 30,13,41984
v_add_lshl_u32 v19, v20, v22, 4
Mvav 40,16,228
v_subrev_u32 v19, 3072, v19
G 164,112
Mvaa 40,20,36
s_cmp_lg_u32 s72, 0
s_cbranch_scc1 .LL_60%=
s_waitcnt vmcnt(8)
.LL_60%=:
v_mov_b64 v[28:29], s[12:13]
s_mov_b64 exec, 1
buffer_store_dwordx2 v[28:29], off, s[8:11], s76 sc1
s_mov_b64 exec, -1
v_lshrrev_b32 v20, 4, v1
G 165,113
Mvaa 72,20,40
Ta 0,14,32768
Ta 2,14,33792
Mvav 72,16,236
G 166,114
v_add_u32 v20, 256, v20
Mvav 104,16,208
G 167,115
v_mul_u32_u24 v22, 205, v20
Mvaa 104,20,44
Ta 4,14,40960
Ta 6,14,41984
Mvaa 136,20,48
buffer_load_dwordx4 v[232:235], v26, s[8:11], s93 offen offset:0 sc1
G 172,116
Mvaa 136,16,32
W 4
v_lshrrev_b32 v22, 12, v22
G 173,117
Mvaa 140,24,32
G 174,118
v_mul_u32_u24 v23, 20, v22
Mvaa 140,28,48
G 175,119
v_sub_u32 v20, v20, v23
Mvaa 108,28,44
Ta 8,15,32768
Ta 10,15,33792
Mvav 108,24,208
buffer_load_dwordx4 a[248:251], v27, s[8:11], s28 offen offset:0 sc1
K 164,164,165
Mvav 76,24,236
v_lshrrev_b32 v23, 1, v22
K 165,166,167
Mvaa 76,28,40
K 166,172,173
v_and_b32 v23, 2, v23
Mvaa 44,28,36
Ta 12,15,40960
Ta 14,15,41984
Mvav 44,24,228
W 4
K 167,174,175
v_xor_b32 v20, v20, v23
Mvav 48,0,228
v_accvgpr_write_b32 a112, v164
v_accvgpr_write_b32 a113, v165
Mvaa 48,4,36
v_mul_u32_u24 v22, 80, v22
v_accvgpr_write_b32 a114, v166
Mvaa 80,4,40
Ta 16,24,32768
Ta 18,24,33792
Mvav 80,0,236
v_add_lshl_u32 v20, v20, v22, 4
v_accvgpr_write_b32 a115, v167
Mvav 112,0,208
buffer_load_dwordx4 a[252:255], v27, s[8:11], s93 offen offset:0 sc1
v_lshrrev_b32 v22, 6, v1
Mvaa 112,4,44
s_add_u32 s28, s28, 512
Ta 20,24,40960
v_accvgpr_write_b32 a116, v168
Mvaa 144,4,48
v_accvgpr_write_b32 a117, v169
v_accvgpr_write_b32 a118, v170
v_accvgpr_write_b32 a119, v171
Mvaa 144,0,32
W 3
Ta 22,24,41984
v_mul_u32_u24 v22, 320, v22
Mvaa 148,8,32
s_cmp_eq_u32 s28, s4
s_cselect_b32 s93, 10240, 0
v_bfe_u32 v23, v1, 4, 2
v_lshl_add_u32 v22, v23, 3, v22
Mvaa 148,12,48
s_sub_u32 s28, s28, s93
Ta 24,25,32768
G 164,120
Mvaa 116,12,44
s_add_u32 s93, s28, 20480
Ta 26,25,33792
G 165,121
)"
R"(Mvav 116,8,208
buffer_load_dwordx4 v[192:195], v26, s[8:11], s28 offen offset:0 sc1
v_bfe_u32 v23, v1, 8, 1
Mvav 84,8,236
G 166,122
v_lshlrev_b32 v23, 5, v23
Mvaa 84,12,40
Ta 28,25,40960
G 167,123
Mvaa 52,12,36
Ta 30,25,41984
v_add_u32 v21, v22, v23
Mvav 52,8,228
W 4
G 172,124
G 173,125
Mvav 56,16,228
v_sub_u32 v22, v22, v23
G 174,126
Mvaa 56,20,36
s_mul_i32 s94, s59, 655360
Ta 0,10,49152
Ta 2,10,50176
Mvaa 88,20,40
s_mul_i32 s95, s58, 320
buffer_load_dwordx4 v[200:203], v26, s[8:11], s93 offen offset:0 sc1
G 175,127
Mvav 88,16,236
s_add_u32 s94, s94, s95
K 164,164,165
K 165,166,167
Mvav 120,16,208
Ta 4,10,57344
Ta 6,10,58368
Mvaa 120,20,44
s_add_u32 s40, s26, s94
s_addc_u32 s41, s27, 0
K 166,172,173
Mvaa 152,20,48
s_and_b32 s41, s41, 0xffff
K 167,174,175
v_accvgpr_write_b32 a120, v164
Mvaa 152,16,32
W 4
Ta 8,11,49152
v_accvgpr_write_b32 a121, v165
Mvaa 156,24,32
s_mov_b32 s42, 5242880
Ta 10,11,50176
buffer_load_dwordx4 v[188:191], v27, s[8:11], s28 offen offset:0 sc1
Mvaa 156,28,48
s_mov_b32 s43, 0x20000
v_accvgpr_write_b32 a122, v166
v_accvgpr_write_b32 a123, v167
Mvaa 124,28,44
s_sub_u32 s42, s42, s94
Ta 12,11,57344
Ta 14,11,58368
Mvav 124,24,208
s_mov_b64 s[44:45], s[8:9]
v_accvgpr_write_b32 a124, v160
v_accvgpr_write_b32 a125, v161
Mvav 92,24,236
s_mov_b32 s47, 0x20000
v_accvgpr_write_b32 a126, v162
v_accvgpr_write_b32 a127, v163
Mvaa 92,28,40
s_and_b32 s48, s14, 1
Ta 16,12,49152
Ta 18,12,50176
Mvaa 60,28,36
s_mul_i32 s48, s48, 5120
buffer_load_dwordx4 v[196:199], v27, s[8:11], s93 offen offset:0 sc1
v_bfe_u32 v28, v1, 4, 2
Mvav 60,24,228
s_waitcnt vmcnt(13) lgkmcnt(6)
Ta 20,12,57344
v_lshlrev_b32 v28, 3, v28
Mvaa 32,0,52
s_lshl_b32 s80, s56, 9
s_add_u32 s80, s80, 4096
Ta 22,12,58368
Mvav 32,4,184
s_lshr_b32 s51, s14, 1
s_and_b32 s94, s14, 1
Ta 24,13,49152
Mvaa 64,4,128
s_add_u32 s28, s28, 512
s_cmp_eq_u32 s28, s4
s_cselect_b32 s93, 10240, 0
Ta 26,13,50176
Mvaa 64,0,56
s_sub_u32 s28, s28, s93
s_add_u32 s93, s28, 20480
buffer_load_dwordx2 v[28:29], v28, s[8:11], s80 offen sc1
Mvaa 96,0,60
s_lshl_b32 s95, s57, 1
Ta 28,13,57344
Ta 30,13,58368
Mvaa 96,4,132
s_add_u32 s94, s94, s95
s_lshl_b32 s54, s94, 3
Mvaa 128,4,136
s_add_u32 s54, s54, s59
Mvav 128,0,216
W 8
Ta 0,14,49152
Ta 2,14,50176
Mvav 132,8,216
s_lshl_b32 s54, s54, 6
buffer_load_dwordx4 v[204:207], v26, s[8:11], s28 offen offset:0 sc1
Mvaa 132,12,136
s_xor_b32 s55, s54, 15872
Mvaa 100,12,132
Ta 4,14,57344
Ta 6,14,58368
Mvaa 100,8,60
s_add_u32 s54, s54, 20480
Mvaa 68,8,56
s_and_b32 s81, s14, 1
Mvaa 68,12,128
s_add_u32 s55, s55, 20480
buffer_load_dwordx4 v[212:215], v26, s[8:11], s93 offen offset:0 sc1
Mvav 36,12,184
Mvaa 36,8,52
W 8
s_and_b32 s95, s14, 2
s_mul_i32 s91, s81, 17164288
Mvaa 40,16,52
s_lshl_b32 s94, s94, 2
Ta 8,15,49152
Ta 10,15,50176
Mvav 40,20,184
s_mul_i32 s82, s56, 536576
Mvaa 72,20,128
s_add_u32 s94, s94, s95
buffer_load_dwordx4 v[220:223], v27, s[8:11], s28 offen offset:0 sc1
Ta 12,15,57344
Mvaa 72,16,56
Ta 14,15,58368
Mvaa 104,16,60
s_lshl_b32 s95, s95, 4
s_add_u32 s91, s91, s82
Mvaa 104,20,132
Mvaa 136,20,136
s_add_u32 s55, s55, s95
s_add_u32 s91, s91, 4194304
Mvav 136,16,216
W 8
s_lshl_b32 s49, s94, 17
Ta 16,24,49152
Mvav 140,24,216
Ta 18,24,50176
buffer_load_dwordx4 v[176:179], v27, s[8:11], s93 offen offset:0 sc1
Mvaa 140,28,136
s_lshl_b32 s95, s59, 14
s_lshr_b32 s82, s14, 1
Mvaa 108,28,132
Ta 20,24,57344
Ta 22,24,58368
Mvaa 108,24,60
s_add_u32 s49, s49, s95
s_lshl_b32 s83, s82, 14
Mvaa 76,24,56
s_xor_b32 s50, s49, 16252928
Mvaa 76,28,128
Mvav 44,28,184
s_add_u32 s91, s91, s83
s_add_u32 s49, s49, 39845888
Mvaa 44,24,52
W 8
Ta 24,25,49152
Ta 26,25,50176
Mvaa 48,0,52
s_add_u32 s50, s50, 39845888
s_lshl_b32 s81, s81, 14
Mvav 48,4,184
Mvaa 80,4,128
s_lshr_b32 s94, s15, 3
Ta 28,25,57344
Ta 30,25,58368
Mvaa 80,0,56
s_lshl_b32 s82, s82, 16
s_lshl_b32 s94, s94, 3
Mvaa 112,0,60
Mvaa 112,4,132
s_add_u32 s94, s94, s14
s_or_b32 s92, s81, s82
Mvaa 144,4,136
Mvav 144,0,216
W 8
s_lshl_b32 s94, s94, 4
Ta 0,2,32768
Mvav 148,8,216
s_xor_b32 s92, s92, 0x10000
s_add_u32 s94, s94, s59
Ta 2,2,33792
Mvaa 148,12,136
Mvaa 116,12,132
s_mul_i32 s52, s94, 5120
Ta 4,2,40960
Ta 6,2,41984
Mvaa 116,8,60
Mvaa 84,8,56
s_lshl_b32 s95, s59, 12
Mvaa 84,12,128
Mvav 52,12,184
s_sub_u32 s52, s52, s95
Mvaa 52,8,52
W 8
s_add_u32 s52, s52, 56623104
Ta 8,3,32768
Mvaa 56,16,52
Ta 10,3,33792
Mvav 56,20,184
s_lshl_b32 s53, s94, 3
Mvaa 88,20,128
Ta 12,3,40960
Ta 14,3,41984
Mvaa 88,16,56
s_add_u32 s53, s53, 65536
Mvaa 120,16,60
s_waitcnt vmcnt(4)
v_cmp_eq_u64 vcc, s[12:13], v[28:29]
s_cmp_eq_u64 vcc, -1
s_cbranch_scc0 .Lspin_61%=
.Lok_62%=:
s_sub_u32 s82, s91, 16384
Mvaa 120,20,132
s_xor_b32 s83, s92, 0x10000
s_and_b32 s5, s14, 1
s_lshl_b32 s5, s5, 13
Mvaa 152,20,136
s_add_u32 s5, s82, s5
s_add_u32 m0, s83, 0
s_add_u32 s81, s5, 0
Mvav 152,16,216
W 8
v_bfe_u32 v244, v1, 4, 1
v_mul_u32_u24 v244, 0xaaaa, v244
Mvav 156,24,216
Dc 1,8,81,0
v_add_u32 v244, 0x4444, v244
Mvaa 156,28,136
Ta 16,4,32768
Ta 18,4,33792
Mvaa 124,28,132
v_lshl_or_b32 v240, v244, 16, v244
v_mov_b32 v241, 0xff800000
Mvaa 124,24,60
Ta 20,4,40960
Ta 22,4,41984
Mvaa 92,24,56
v_add_u32 v242, 0x10000, v1
Mvaa 92,28,128
Dc 1,8,81,1024
v_bfe_u32 v244, v1, 4, 4
Mvav 60,28,184
v_lshrrev_b32 v245, 8, v1
Mvaa 60,24,52
W 8
Ta 24,5,32768
v_lshlrev_b32 v245, 2, v245
Mvaa 32,0,140
Ta 26,5,33792
Mvav 32,4,224
v_sub_u32 v244, v244, v245
Mvav 64,4,232
Dc 1,8,81,2048
v_cmp_le_i32_e64 s[72:73], 0, v244
Mvaa 64,0,144
Ta 28,5,40960
Ta 30,5,41984
Mvaa 96,0,148
v_cmp_le_i32_e64 s[74:75], 1, v244
Mvaa 96,4,248
v_cmp_le_i32_e64 s[76:77], 2, v244
Mvaa 128,4,252
v_cmp_le_i32_e64 s[78:79], 3, v244
Mvaa 128,0,152
W 8
Dc 1,8,81,3072
Ta 0,6,32768
Mvaa 132,8,152
s_add_u32 m0, s83, 4096
s_add_u32 s81, s5, 4096
Ta 2,6,33792
Mvaa 132,12,252
v_add_u32 v243, 4096, v1
v_add_u32 v244, 8192, v1
Mvaa 100,12,248
Ta 4,6,40960
v_add_u32 v245, 12288, v1
Mvaa 100,8,148
Ta 6,6,41984
Mvaa 68,8,144
s_mov_b32 s18, 0x3f803f80
Dc 1,8,81,0
Mvav 68,12,232
s_mov_b32 s19, 0x3f003f00
Mvav 36,12,224
Mvaa 36,8,140
W 8
Ta 8,7,32768
v_accvgpr_write_b32 a176, s18
Mvaa 40,16,140
Ta 10,7,33792
v_accvgpr_write_b32 a180, s19
Mvav 40,20,224
Dc 1,8,81,1024
v_mov_b32 v252, v241
Mvav 72,20,232
Ta 12,7,40960
Ta 14,7,41984
Mvaa 72,16,144
v_accvgpr_write_b32 a177, s18
Mvaa 104,16,148
v_accvgpr_write_b32 a181, s19
Mvaa 104,20,248
v_mov_b32 v253, v241
Mvaa 136,20,252
Dc 1,8,81,2048
Mvaa 136,16,152
W 8
Ta 16,8,32768
v_accvgpr_write_b32 a178, s18
Mvaa 140,24,152
Ta 18,8,33792
v_accvgpr_write_b32 a182, s19
Mvaa 140,28,252
Mvaa 108,28,248
Ta 20,8,40960
v_mov_b32 v254, v241
Mvaa 108,24,148
Dc 1,8,81,3072
Ta 22,8,41984
Mvaa 76,24,144
s_add_u32 m0, s83, 8192
s_add_u32 s81, s82, 8192
v_accvgpr_write_b32 a179, s18
Mvav 76,28,232
v_accvgpr_write_b32 a183, s19
Mvav 44,28,224
v_mov_b32 v255, v241
Mvaa 44,24,140
W 8
Ta 24,9,32768
v_accvgpr_write_b32 a160, 0
Mvaa 48,0,140
Dc 1,8,81,0
Ta 26,9,33792
Mvav 48,4,224
v_accvgpr_write_b32 a161, 0
Mvav 80,4,232
Ta 28,9,40960
v_accvgpr_write_b32 a162, 0
Mvaa 80,0,144
Ta 30,9,41984
v_accvgpr_write_b32 a163, 0
Mvaa 112,0,148
Mvaa 112,4,248
Dc 1,8,81,1024
v_accvgpr_write_b32 a164, 0
Mvaa 144,4,252
v_accvgpr_write_b32 a165, 0
Mvaa 144,0,152
W 8
Ta 0,2,49152
v_accvgpr_write_b32 a166, 0
Mvaa 148,8,152
Ta 2,2,50176
Mvaa 148,12,252
v_accvgpr_write_b32 a167, 0
Mvaa 116,12,248
Dc 1,8,81,2048
v_accvgpr_write_b32 a168, 0
Mvaa 116,8,148
Ta 4,2,57344
Ta 6,2,58368
Mvaa 84,8,144
v_accvgpr_write_b32 a169, 0
Mvav 84,12,232
v_accvgpr_write_b32 a170, 0
Mvav 52,12,224
v_accvgpr_write_b32 a171, 0
Mvaa 52,8,140
W 8
Dc 1,8,81,3072
Ta 8,3,49152
Mvaa 56,16,140
s_add_u32 m0, s83, 12288
s_add_u32 s81, s82, 12288
Ta 10,3,50176
Mvav 56,20,224
v_accvgpr_write_b32 a172, 0
v_accvgpr_write_b32 a173, 0
Mvav 88,20,232
Ta 12,3,57344
v_accvgpr_write_b32 a174, 0
Mvaa 88,16,144
)"
R"(Ta 14,3,58368
Mvaa 120,16,148
Dc 1,8,81,0
v_accvgpr_write_b32 a175, 0
Mvaa 120,20,248
s_mov_b32 s90, 0x5d800000
Mvaa 152,20,252
s_mov_b64 s[88:89], 0
Mvaa 152,16,152
W 8
Ta 16,4,49152
Ta 18,4,50176
Mvaa 156,24,152
s_mov_b32 s64, 0
Mvaa 156,28,252
s_and_b32 s18, s14, 1
Dc 1,8,81,1024
Mvaa 124,28,248
s_lshl_b32 s62, s18, 14
Ta 20,4,57344
Ta 22,4,58368
Mvaa 124,24,148
Mvaa 92,24,144
s_lshl_b32 s20, s57, 15
Mvav 92,28,232
s_sub_u32 s20, 507904, s20
Mvav 60,28,224
Dc 1,8,81,2048
Mvaa 60,24,140
s_waitcnt vmcnt(15) lgkmcnt(8)
s_add_u32 s20, s20, s62
Ta 24,5,49152
Mvav 32,0,192
s_lshl_b32 s65, s57, 1
Ta 26,5,50176
Mvav 32,4,204
s_sub_u32 s65, s65, 1
Mvav 64,4,212
Ta 28,5,57344
Ta 30,5,58368
Mvav 64,0,200
s_max_i32 s65, s65, 0
Dc 1,8,81,3072
v_bfe_u32 v28, v1, 4, 4
Mvav 96,0,188
s_sub_u32 s66, s60, 1
v_lshlrev_b32 v28, 3, v28
Mvav 96,4,220
s_lshl_b32 s80, s56, 9
s_add_u32 s80, s80, 4096
s_cmp_lt_u32 s14, 2
s_cselect_b32 s20, s20, 0
s_cselect_b32 s66, s66, 15
s_cselect_b32 s21, 32768, 0
Mvav 128,4,176
Mvav 128,0,196
W 8
s_add_u32 s61, s91, s20
Ta 0,6,49152
Mvav 132,8,196
s_add_u32 s62, s62, s21
buffer_load_dwordx2 v[28:29], v28, s[8:11], s80 offen sc1
Ta 2,6,50176
Mvav 132,12,176
Mvav 100,12,220
s_sub_u32 s63, 16384, s21
Ta 4,6,57344
Ta 6,6,58368
Mvav 100,8,188
s_lshl_b32 s67, s60, 7
Mvav 68,8,200
s_add_u32 s67, s67, s56
Mvav 68,12,212
Mvav 36,12,204
s_lshl_b32 s67, s67, 12
Mvav 36,8,192
W 8
s_add_u32 s67, s67, 39845888
Ta 8,7,49152
Mvav 40,16,192
s_lshl_b32 s69, s60, 3
Ta 10,7,50176
Mvav 40,20,204
Mvav 72,20,212
s_add_u32 s69, s69, s59
Ta 12,7,57344
Ta 14,7,58368
Mvav 72,16,200
s_lshl_b32 s69, s69, 6
Mvav 104,16,188
Mvav 104,20,220
s_lshl_b32 s21, s58, 3
Mvav 136,20,176
s_add_u32 s69, s69, s21
Mvav 136,16,196
W 8
s_add_u32 s69, s69, 20480
Ta 16,8,49152
Mvav 140,24,196
Ta 18,8,50176
Mvav 140,28,176
s_mov_b32 s70, 0
Mvav 108,28,220
s_mov_b32 s37, 0
Ta 20,8,57344
Ta 22,8,58368
Mvav 108,24,188
s_sub_u32 s18, s57, 4
Mvav 76,24,200
Mvav 76,28,212
s_lshl_b32 s18, s18, 2
Mvav 44,28,204
s_add_u32 s18, s18, s58
Mvav 44,24,192
W 8
Ta 24,9,49152
Ta 26,9,50176
Mvav 48,0,192
s_mul_i32 s85, s59, 7
Mvav 48,4,204
s_add_u32 s85, s85, s18
Mvav 80,4,212
s_cmp_lt_u32 s18, 7
s_cselect_b32 s85, s85, 99
Ta 28,9,57344
Ta 30,9,58368
Mvav 80,0,200
Mvav 112,0,188
s_cmp_lt_u32 s85, 54
s_cselect_b32 s65, 2, s65
Mvav 112,4,220
s_mov_b32 s68, 0
Mvav 144,4,176
s_mov_b64 s[24:25], -1
Mvav 144,0,196
W 8
Mvav 148,8,196
s_mov_b32 s22, 0x3e0293ee
Mvav 148,12,176
s_mov_b32 s23, 0x3e0293ee
Mvav 116,12,220
Mvav 116,8,188
Mvav 84,8,200
Mvav 84,12,212
Mvav 52,12,204
Mvav 52,8,192
W 4
Mvav 56,16,192
Mvav 56,20,204
s_waitcnt vmcnt(0)
v_cmp_eq_u64 vcc, s[12:13], v[28:29]
s_cmp_eq_u64 vcc, -1
s_cbranch_scc0 .Lspin_64%=
.Lok_65%=:
Mvav 88,20,212
Mvav 88,16,200
Mvav 120,16,188
W 0
s_barrier
Ra 128,242,0
Mvav 120,20,220
Mvav 152,20,176
Ra 132,242,1024
Mvav 152,16,196
Mvav 156,24,196
Ra 136,242,2048
Mvav 156,28,176
Mvav 124,28,220
Ra 140,242,3072
Mvav 124,24,188
Mvav 92,24,200
Mvav 92,28,212
Mvav 60,28,204
Mvav 60,24,192
.Lbgo%=:
v_pk_mul_f32 v[32:33], v[32:33], s[22:23]
v_pk_mul_f32 v[34:35], v[34:35], s[22:23]
v_pk_mul_f32 v[36:37], v[36:37], s[22:23]
v_pk_mul_f32 v[38:39], v[38:39], s[22:23]
v_pk_mul_f32 v[64:65], v[64:65], s[22:23]
v_pk_mul_f32 v[66:67], v[66:67], s[22:23]
v_pk_mul_f32 v[68:69], v[68:69], s[22:23]
v_pk_mul_f32 v[70:71], v[70:71], s[22:23]
v_pk_mul_f32 v[96:97], v[96:97], s[22:23]
v_pk_mul_f32 v[98:99], v[98:99], s[22:23]
v_pk_mul_f32 v[100:101], v[100:101], s[22:23]
v_pk_mul_f32 v[102:103], v[102:103], s[22:23]
v_pk_mul_f32 v[128:129], v[128:129], s[22:23]
v_pk_mul_f32 v[130:131], v[130:131], s[22:23]
v_pk_mul_f32 v[132:133], v[132:133], s[22:23]
v_pk_mul_f32 v[134:135], v[134:135], s[22:23]
v_pk_mul_f32 v[40:41], v[40:41], s[22:23]
v_pk_mul_f32 v[42:43], v[42:43], s[22:23]
v_pk_mul_f32 v[44:45], v[44:45], s[22:23]
v_pk_mul_f32 v[46:47], v[46:47], s[22:23]
v_pk_mul_f32 v[72:73], v[72:73], s[22:23]
v_pk_mul_f32 v[74:75], v[74:75], s[22:23]
v_pk_mul_f32 v[76:77], v[76:77], s[22:23]
v_pk_mul_f32 v[78:79], v[78:79], s[22:23]
v_pk_mul_f32 v[104:105], v[104:105], s[22:23]
v_pk_mul_f32 v[106:107], v[106:107], s[22:23]
v_pk_mul_f32 v[108:109], v[108:109], s[22:23]
v_pk_mul_f32 v[110:111], v[110:111], s[22:23]
v_pk_mul_f32 v[136:137], v[136:137], s[22:23]
v_pk_mul_f32 v[138:139], v[138:139], s[22:23]
v_pk_mul_f32 v[140:141], v[140:141], s[22:23]
v_pk_mul_f32 v[142:143], v[142:143], s[22:23]
v_pk_mul_f32 v[48:49], v[48:49], s[22:23]
v_pk_mul_f32 v[50:51], v[50:51], s[22:23]
v_pk_mul_f32 v[52:53], v[52:53], s[22:23]
v_pk_mul_f32 v[54:55], v[54:55], s[22:23]
v_pk_mul_f32 v[80:81], v[80:81], s[22:23]
v_pk_mul_f32 v[82:83], v[82:83], s[22:23]
v_pk_mul_f32 v[84:85], v[84:85], s[22:23]
v_pk_mul_f32 v[86:87], v[86:87], s[22:23]
v_pk_mul_f32 v[112:113], v[112:113], s[22:23]
v_pk_mul_f32 v[114:115], v[114:115], s[22:23]
v_pk_mul_f32 v[116:117], v[116:117], s[22:23]
v_pk_mul_f32 v[118:119], v[118:119], s[22:23]
v_pk_mul_f32 v[144:145], v[144:145], s[22:23]
v_pk_mul_f32 v[146:147], v[146:147], s[22:23]
v_pk_mul_f32 v[148:149], v[148:149], s[22:23]
v_pk_mul_f32 v[150:151], v[150:151], s[22:23]
v_pk_mul_f32 v[56:57], v[56:57], s[22:23]
v_pk_mul_f32 v[58:59], v[58:59], s[22:23]
v_pk_mul_f32 v[60:61], v[60:61], s[22:23]
v_pk_mul_f32 v[62:63], v[62:63], s[22:23]
v_pk_mul_f32 v[88:89], v[88:89], s[22:23]
v_pk_mul_f32 v[90:91], v[90:91], s[22:23]
v_pk_mul_f32 v[92:93], v[92:93], s[22:23]
v_pk_mul_f32 v[94:95], v[94:95], s[22:23]
v_pk_mul_f32 v[120:121], v[120:121], s[22:23]
v_pk_mul_f32 v[122:123], v[122:123], s[22:23]
v_pk_mul_f32 v[124:125], v[124:125], s[22:23]
v_pk_mul_f32 v[126:127], v[126:127], s[22:23]
v_pk_mul_f32 v[152:153], v[152:153], s[22:23]
v_pk_mul_f32 v[154:155], v[154:155], s[22:23]
v_pk_mul_f32 v[156:157], v[156:157], s[22:23]
v_pk_mul_f32 v[158:159], v[158:159], s[22:23]
K 32,32,33
K 33,34,35
K 34,36,37
K 35,38,39
K 40,40,41
K 41,42,43
K 42,44,45
K 43,46,47
W 0
Zvav 196,128,32
s_add_u32 m0, s62, 0
Ra 144,242,4096
Ra 148,242,5120
Ra 152,242,6144
Ra 156,242,7168
Mvav 196,132,40
K 64,64,65
K 65,66,67
K 66,68,69
K 67,70,71
K 72,72,73
K 73,74,75
K 74,76,77
K 75,78,79
s_nop 1
Zvav 204,132,72
Dc 1,8,61,0
K 96,96,97
K 97,98,99
K 98,100,101
Mvav 204,128,64
K 99,102,103
K 104,104,105
K 105,106,107
K 106,108,109
K 107,110,111
Zvav 212,128,96
Dc 1,8,61,1024
K 128,128,129
K 129,130,131
K 130,132,133
Mvav 212,132,104
K 131,134,135
K 136,136,137
K 137,138,139
K 138,140,141
K 139,142,143
s_nop 1
Zvav 220,132,136
Dc 1,8,61,2048
K 48,48,49
K 49,50,51
K 50,52,53
Mvav 220,128,128
K 51,54,55
K 56,56,57
K 57,58,59
K 58,60,61
K 59,62,63
Mvav 196,136,48
Ra 128,242,8192
Ra 132,242,9216
K 80,80,81
K 81,82,83
Mvav 196,140,56
K 82,84,85
K 83,86,87
K 88,88,89
K 89,90,91
K 90,92,93
K 91,94,95
s_nop 1
Mvav 204,140,88
Dc 1,8,61,3072
s_add_u32 m0, s62, 4096
K 112,112,113
K 113,114,115
K 114,116,117
Mvav 204,136,80
K 115,118,119
K 120,120,121
K 121,122,123
K 122,124,125
K 123,126,127
Mvav 212,136,112
Dc 243,8,61,0
K 144,144,145
K 145,146,147
K 146,148,149
Mvav 212,140,120
K 147,150,151
K 152,152,153
K 153,154,155
K 154,156,157
K 155,158,159
s_nop 1
Mvav 220,140,152
s_add_u32 s80, s62, 131072
Dc 243,8,61,1024
v_add_u32 v12, s80, v1
Mvav 220,136,144
W 2
ds_write_b128 v12, v[32:35] offset:0
Ra 136,242,10240
Zvav 36,144,32
Ra 0,12,0
Ra 140,242,11264
Dc 243,8,61,2048
Mvav 36,148,40
v_max3_f32 v4, v196, v197, v198
v_max3_f32 v4, v4, v199, v252
v_max3_f32 v5, v204, v205, v206
v_max3_f32 v5, v5, v207, v253
Zvav 68,148,72
v_max3_f32 v6, v212, v213, v214
v_max3_f32 v6, v6, v215, v254
v_max3_f32 v7, v220, v221, v222
v_max3_f32 v7, v7, v223, v255
Mvav 68,144,64
v_mov_b32 v8, v4
v_mov_b32 v9, v5
v_mov_b32 v10, v6
v_mov_b32 v11, v7
Zvav 100,144,96
ds_write_b128 v12, v[40:43] offset:1024
v_permlane32_swap_b32 v4, v8
v_permlane32_swap_b32 v5, v9
v_permlane32_swap_b32 v6, v10
Mvav 100,148,104
v_permlane32_swap_b32 v7, v11
v_max_f32 v4, v4, v8
v_max_f32 v5, v5, v9
v_max_f32 v6, v6, v10
Zvav 132,148,136
v_max_f32 v7, v7, v11
v_mov_b32 v8, v4
v_mov_b32 v9, v5
v_mov_b32 v10, v6
Mvav 132,144,128
Ra 4,12,1024
v_mov_b32 v11, v7
v_permlane16_swap_b32 v4, v8
v_permlane16_swap_b32 v5, v9
Mvav 36,152,48
v_permlane16_swap_b32 v6, v10
v_permlane16_swap_b32 v7, v11
v_max_f32 v4, v4, v8
v_max_f32 v5, v5, v9
Mvav 36,156,56
v_max_f32 v6, v6, v10
v_max_f32 v7, v7, v11
v_sub_f32 v4, 0, v4
v_sub_f32 v5, 0, v5
Mvav 68,156,88
v_sub_f32 v6, 0, v6
v_sub_f32 v7, 0, v7
v_mov_b32 v224, v4
v_mov_b32 v225, v4
Mvav 68,152,80
v_mov_b32 v226, v4
v_mov_b32 v227, v4
)"
R"(v_mov_b32 v228, v5
v_mov_b32 v229, v5
Mvav 100,152,112
v_mov_b32 v230, v5
v_mov_b32 v231, v5
v_mov_b32 v232, v6
v_mov_b32 v233, v6
Mvav 100,156,120
v_mov_b32 v234, v6
v_mov_b32 v235, v6
v_mov_b32 v236, v7
v_mov_b32 v237, v7
Mvav 132,156,152
Dc 243,8,61,3072
s_add_u32 m0, s62, 8192
ds_write_b128 v12, v[48:51] offset:2048
v_mov_b32 v238, v7
v_mov_b32 v239, v7
Mvav 132,152,144
W 3
Ra 8,12,2048
ds_write_b128 v12, v[56:59] offset:3072
Ra 144,242,12288
Ra 12,12,3072
Cvav 160,128,32,224
s_and_b32 s80, s14, 1
Dc 244,8,61,0
Ra 148,242,13312
ds_write_b128 v12, v[64:67] offset:4096
Ra 16,12,4096
Mvav 160,132,40
s_lshl_b32 s80, s80, 14
ds_write_b128 v12, v[72:75] offset:5120
Ra 20,12,5120
ds_write_b128 v12, v[80:83] offset:6144
A 196,196,224
Cvav 164,132,72,228
s_add_u32 s80, s80, 98304
Dc 244,8,61,1024
Ra 24,12,6144
A 197,197,224
A 198,198,224
Mvav 164,128,64
Ra 152,242,14336
A 199,199,224
X 196
Cvav 168,128,96,232
Dc 244,8,61,2048
Ra 156,242,15360
X 197
Mvav 168,132,104
ds_write_b128 v12, v[88:91] offset:7168
Ra 28,12,7168
X 198
Cvav 172,132,136,236
Dc 244,8,61,3072
s_add_u32 m0, s62, 12288
K 192,196,197
X 199
Mvav 172,128,128
ds_write_b128 v12, v[96:99] offset:8192
A 204,204,228
v_add_u32 v8, s80, v1
K 193,198,199
Mvav 160,136,48
Dc 245,8,61,0
Ra 32,12,8192
A 205,205,228
A 206,206,228
Mvav 160,140,56
ds_write_b128 v8, v[4:7]
A 207,207,228
X 204
Mvav 164,140,88
Dc 245,8,61,1024
ds_write_b128 v12, v[104:107] offset:9216
X 205
Mvav 164,136,80
Ra 36,12,9216
X 206
K 200,204,205
Mvav 168,136,112
Dc 245,8,61,2048
Ra 128,242,24576
X 207
Mvav 168,140,120
ds_write_b128 v12, v[112:115] offset:10240
Ra 132,242,25600
A 212,212,232
K 201,206,207
Mvav 172,140,152
Dc 245,8,61,3072
s_add_u32 s61, s61, s63
Ra 40,12,10240
A 213,213,232
A 214,214,232
Mvav 172,136,144
W 11
ds_write_b128 v12, v[120:123] offset:11264
A 215,215,232
X 212
Cvav 176,144,32,224
X 213
X 214
Mvav 176,148,40
Ra 44,12,11264
K 208,212,213
X 215
Cvav 180,148,72,228
ds_write_b128 v12, v[128:131] offset:12288
A 220,220,236
K 209,214,215
A 221,221,236
Mvav 180,144,64
Ra 48,12,12288
Ra 136,242,26624
A 222,222,236
A 223,223,236
Cvav 184,144,96,232
ds_write_b128 v12, v[136:139] offset:13312
Ra 140,242,27648
X 220
Mvav 184,148,104
X 221
X 222
Cvav 188,148,136,236
Ra 52,12,13312
K 216,220,221
X 223
Mvav 188,144,128
ds_write_b128 v12, v[144:147] offset:14336
Ra 56,12,14336
A 36,36,224
K 217,222,223
Mvav 176,152,48
ds_write_b128 v12, v[152:155] offset:15360
A 37,37,224
A 38,38,224
A 39,39,224
Mvav 176,156,56
X 36
X 37
Mvav 180,156,88
Ra 60,12,15360
X 38
K 194,36,37
Mvav 180,152,80
Ra 144,242,28672
X 39
A 68,68,228
Mvav 184,152,112
Ra 148,242,29696
K 195,38,39
A 69,69,228
A 70,70,228
Mvav 184,156,120
W 3
s_barrier
A 71,71,228
X 68
Mvav 188,156,152
X 69
X 70
K 202,68,69
X 71
A 100,100,232
K 203,70,71
A 101,101,232
A 102,102,232
A 103,103,232
X 100
X 101
X 102
K 210,100,101
X 103
A 132,132,236
K 211,102,103
A 133,133,236
A 134,134,236
A 135,135,236
X 132
X 133
X 134
K 218,132,133
X 135
s_nop 0
K 219,134,135
Mvav 188,152,144
Ra 152,242,30720
Zvav 32,128,192
Ra 156,242,31744
Zvav 64,128,200
Zvav 96,128,208
s_xor_b32 s62, s62, 0x10000
Zvav 128,128,216
v_add_u32 v23, 61440, v21
Zvav 132,132,216
v_add_u32 v24, 61440, v22
Zvav 100,132,208
v_add_u32 v25, 122880, v21
Zvav 68,132,200
v_add_u32 v26, 122880, v22
Zvav 36,132,192
Zvav 40,136,192
X 172
Zvav 72,136,200
X 173
Zvav 104,136,208
X 174
K 220,172,173
Zvav 136,136,216
X 175
Zvav 140,140,216
X 168
K 221,174,175
Zvav 108,140,208
X 169
X 170
Zvav 76,140,200
s_waitcnt vmcnt(0) lgkmcnt(0)
s_barrier
v_xor_b32 v242, 0x10000, v242
K 212,168,169
Zvav 44,140,192
X 171
X 164
Zvav 48,144,192
Ra 128,242,0
K 213,170,171
X 165
Zvav 80,144,200
X 166
Zvav 112,144,208
Ra 132,242,1024
K 204,164,165
X 167
Zvav 144,144,216
X 160
Zvav 148,148,216
K 205,166,167
X 161
Zvav 116,148,208
Ra 136,242,2048
X 162
Zvav 84,148,200
K 196,160,161
X 163
Zvav 52,148,192
X 176
K 197,162,163
Zvav 56,152,192
Ra 140,242,3072
X 177
Zvav 88,152,200
X 178
K 198,176,177
Zvav 120,152,208
X 179
X 180
Zvav 152,152,216
K 199,178,179
Zvav 156,156,216
X 181
X 182
Zvav 124,156,208
K 206,180,181
X 183
Zvav 92,156,200
K 207,182,183
Zvav 60,156,192
s_cmp_eq_u64 s[24:25], 0
s_cbranch_scc1 .Ll1_34%=
W 0
Cvaa 160,128,0,224
Ra 144,242,4096
Ra 148,242,5120
Mvaa 160,132,4
s_add_u32 m0, s62, 0
Ra 152,242,6144
Cvaa 164,132,20,228
X 184
Mvaa 164,128,16
X 185
Cvaa 168,128,32,232
X 186
Mvaa 168,132,36
Ra 156,242,7168
K 214,184,185
Cvaa 172,132,52,236
Dc 1,8,61,0
Mvaa 172,128,48
X 187
Mvaa 160,136,8
X 188
Mvaa 160,140,12
Ra 128,242,16384
K 215,186,187
Mvaa 164,140,28
Dc 1,8,61,1024
Mvaa 164,136,24
X 189
Mvaa 168,136,40
X 190
Mvaa 168,140,44
Ra 132,242,17408
K 222,188,189
Mvaa 172,140,60
Dc 1,8,61,2048
Mvaa 172,136,56
X 191
v_smfmac_f32_16x16x64_bf16 a[160:163], a[176:179], v[192:199], v240
Ra 136,242,18432
K 223,190,191
v_smfmac_f32_16x16x64_bf16 a[164:167], a[176:179], v[200:207], v240
X 160
v_smfmac_f32_16x16x64_bf16 a[168:171], a[176:179], v[208:215], v240
Dc 1,8,61,3072
v_smfmac_f32_16x16x64_bf16 a[172:175], a[176:179], v[216:223], v240
W 3
X 161
Cvaa 176,144,0,224
s_add_u32 m0, s62, 4096
X 162
Mvaa 176,148,4
Ra 140,242,19456
K 192,160,161
Cvaa 180,148,20,228
X 163
Mvaa 180,144,16
Dc 243,8,61,0
Cvaa 184,144,32,232
X 164
Mvaa 184,148,36
K 193,162,163
Cvaa 188,148,52,236
X 165
Mvaa 188,144,48
Dc 243,8,61,1024
Ra 144,242,20480
Mvaa 176,152,8
X 166
Mvaa 176,156,12
Ra 148,242,21504
K 200,164,165
Mvaa 180,156,28
X 167
Mvaa 180,152,24
Dc 243,8,61,2048
Mvaa 184,152,40
X 168
Mvaa 184,156,44
K 201,166,167
Mvaa 188,156,60
X 169
Mvaa 188,152,56
W 2
Dc 243,8,61,3072
Ra 152,242,22528
Mvav 32,128,196
s_add_u32 m0, s62, 8192
X 170
Mvav 64,128,204
Ra 156,242,23552
K 208,168,169
Mvav 96,128,212
X 171
Mvav 128,128,220
Dc 244,8,61,0
Mvav 132,132,220
X 172
Mvav 100,132,212
Ra 128,242,8192
K 209,170,171
Mvav 68,132,204
X 173
Mvav 36,132,196
Dc 244,8,61,1024
Mvav 40,136,196
X 174
Mvav 72,136,204
Ra 132,242,9216
K 216,172,173
Mvav 104,136,212
X 175
Mvav 136,136,220
Dc 244,8,61,2048
Mvav 140,140,220
X 176
Mvav 108,140,212
Ra 136,242,10240
K 217,174,175
Mvav 76,140,204
X 177
Mvav 44,140,196
W 3
Dc 244,8,61,3072
Mvav 48,144,196
X 178
Mvav 80,144,204
s_add_u32 m0, s62, 12288
)"
R"(Ra 140,242,11264
Mvav 112,144,212
K 194,176,177
Mvav 144,144,220
X 179
Mvav 148,148,220
X 180
Mvav 116,148,212
Dc 245,8,61,0
Ra 144,242,12288
Mvav 84,148,204
K 195,178,179
Mvav 52,148,196
X 181
Mvav 56,152,196
X 182
Mvav 88,152,204
s_cmp_eq_u32 s14, 2
s_cselect_b64 s[18:19], s[30:31], s[32:33]
Dc 245,8,61,1024
Ra 148,242,13312
Mvav 120,152,212
s_mul_i32 s81, s58, 6
K 202,180,181
Mvav 152,152,220
s_add_u32 s81, s81, s57
X 183
Mvav 156,156,220
s_sub_u32 s81, s81, 2
X 184
Mvav 124,156,212
s_cmp_ge_u32 s57, 2
s_cselect_b32 s23, 2211836, 0
Dc 245,8,61,2048
Ra 152,242,14336
Mvav 92,156,204
s_mul_i32 s82, s81, 48384
K 203,182,183
Mvav 60,156,196
W 3
X 185
Cvaa 160,128,0,224
s_mul_i32 s83, s59, 448
X 186
Mvaa 160,132,4
s_andn2_b32 s83, s83, 127
Dc 245,8,61,3072
Ra 156,242,15360
Cvaa 164,132,20,228
s_add_u32 s82, s82, s83
s_add_u32 s61, s61, s63
K 210,184,185
Mvaa 164,128,16
v_min_u32 v246, 880, v1
v_lshrrev_b32 v247, 6, v246
Cvaa 168,128,32,232
X 187
Mvaa 168,132,36
X 188
Cvaa 172,132,52,236
s_xor_b32 s62, s62, 0x10000
v_mul_u32_u24 v247, 3456, v247
v_and_b32 v246, 48, v246
Mvaa 172,128,48
K 211,186,187
v_lshl_add_u32 v246, v246, 3, v247
Mvaa 160,136,8
X 189
Mvaa 160,140,12
s_add_u32 s64, s64, 1
Ra 128,242,24576
v_add_u32 v246, s82, v246
Mvaa 164,140,28
X 190
Mvaa 164,136,24
Ra 132,242,25600
v_min_u32 v246, s23, v246
Mvaa 168,136,40
global_load_dword v3, v246, s[18:19]
K 218,188,189
Mvaa 168,140,44
X 191
Mvaa 172,140,60
X 160
Mvaa 172,136,56
W 2
Ra 136,242,26624
K 219,190,191
Cvaa 176,144,0,224
X 161
Mvaa 176,148,4
Ra 140,242,27648
Cvaa 180,148,20,228
X 162
Mvaa 180,144,16
K 196,160,161
Cvaa 184,144,32,232
X 163
Mvaa 184,148,36
X 164
Cvaa 188,148,52,236
K 197,162,163
Mvaa 188,144,48
X 165
Mvaa 176,152,8
Ra 144,242,28672
Ra 148,242,29696
Mvaa 176,156,12
X 166
Mvaa 180,156,28
K 204,164,165
Mvaa 180,152,24
X 167
Mvaa 184,152,40
Mvaa 184,156,44
X 168
Mvaa 188,156,60
Mvaa 188,152,56
W 2
Ra 152,242,30720
K 205,166,167
Mvav 32,128,192
X 169
Mvav 64,128,200
Ra 156,242,31744
Mvav 96,128,208
X 170
Mvav 128,128,216
K 212,168,169
Mvav 132,132,216
X 171
Mvav 100,132,208
X 172
Mvav 68,132,200
Mvav 36,132,192
K 213,170,171
Mvav 40,136,192
X 173
Mvav 72,136,200
X 174
Mvav 104,136,208
Mvav 136,136,216
K 220,172,173
Mvav 140,140,216
X 175
Mvav 108,140,208
X 176
Mvav 76,140,200
s_waitcnt vmcnt(1) lgkmcnt(0)
s_barrier
v_xor_b32 v242, 0x10000, v242
Mvav 44,140,192
Ra 128,242,0
K 221,174,175
Mvav 48,144,192
X 177
Mvav 80,144,200
X 178
Mvav 112,144,208
Ra 132,242,1024
Mvav 144,144,216
K 198,176,177
Mvav 148,148,216
X 179
Mvav 116,148,208
X 180
Mvav 84,148,200
Ra 136,242,2048
Mvav 52,148,192
K 199,178,179
Mvav 56,152,192
X 181
Mvav 88,152,200
Ra 140,242,3072
Mvav 120,152,208
X 182
Mvav 152,152,216
K 206,180,181
Mvav 156,156,216
X 183
Mvav 124,156,208
Mvav 92,156,200
K 207,182,183
Mvav 60,156,192
W 0
Cvaa 160,128,0,224
Ra 144,242,4096
Ra 148,242,5120
Mvaa 160,132,4
s_add_u32 m0, s62, 0
Ra 152,242,6144
Cvaa 164,132,20,228
X 184
Mvaa 164,128,16
X 185
Cvaa 168,128,32,232
X 186
Mvaa 168,132,36
Ra 156,242,7168
K 214,184,185
Cvaa 172,132,52,236
Dc 1,8,61,0
Mvaa 172,128,48
X 187
Mvaa 160,136,8
X 188
Mvaa 160,140,12
Ra 128,242,16384
K 215,186,187
Mvaa 164,140,28
Dc 1,8,61,1024
Mvaa 164,136,24
X 189
Mvaa 168,136,40
X 190
Mvaa 168,140,44
Ra 132,242,17408
K 222,188,189
Mvaa 172,140,60
Dc 1,8,61,2048
Mvaa 172,136,56
X 191
v_smfmac_f32_16x16x64_bf16 a[160:163], a[176:179], v[192:199], v240
Ra 136,242,18432
K 223,190,191
v_smfmac_f32_16x16x64_bf16 a[164:167], a[176:179], v[200:207], v240
X 160
v_smfmac_f32_16x16x64_bf16 a[168:171], a[176:179], v[208:215], v240
Dc 1,8,61,3072
v_smfmac_f32_16x16x64_bf16 a[172:175], a[176:179], v[216:223], v240
W 3
X 161
Cvaa 176,144,0,224
s_add_u32 m0, s62, 4096
X 162
Mvaa 176,148,4
Ra 140,242,19456
K 192,160,161
Cvaa 180,148,20,228
X 163
Mvaa 180,144,16
Dc 243,8,61,0
Cvaa 184,144,32,232
X 164
Mvaa 184,148,36
K 193,162,163
Cvaa 188,148,52,236
X 165
Mvaa 188,144,48
Dc 243,8,61,1024
Ra 144,242,20480
Mvaa 176,152,8
X 166
Mvaa 176,156,12
Ra 148,242,21504
K 200,164,165
Mvaa 180,156,28
X 167
Mvaa 180,152,24
Dc 243,8,61,2048
Mvaa 184,152,40
X 168
Mvaa 184,156,44
K 201,166,167
Mvaa 188,156,60
X 169
Mvaa 188,152,56
W 2
Dc 243,8,61,3072
Ra 152,242,22528
Mvav 32,128,196
s_add_u32 m0, s62, 8192
X 170
Mvav 64,128,204
Ra 156,242,23552
K 208,168,169
Mvav 96,128,212
X 171
Mvav 128,128,220
Dc 244,8,61,0
Mvav 132,132,220
X 172
Mvav 100,132,212
Ra 128,242,8192
K 209,170,171
Mvav 68,132,204
X 173
Mvav 36,132,196
Dc 244,8,61,1024
Mvav 40,136,196
X 174
Mvav 72,136,204
Ra 132,242,9216
K 216,172,173
Mvav 104,136,212
X 175
Mvav 136,136,220
Dc 244,8,61,2048
Mvav 140,140,220
X 176
Mvav 108,140,212
Ra 136,242,10240
K 217,174,175
Mvav 76,140,204
X 177
Mvav 44,140,196
W 3
Dc 244,8,61,3072
Mvav 48,144,196
X 178
Mvav 80,144,204
s_add_u32 m0, s62, 12288
Ra 140,242,11264
Mvav 112,144,212
K 194,176,177
Mvav 144,144,220
X 179
Mvav 148,148,220
X 180
Mvav 116,148,212
Dc 245,8,61,0
Ra 144,242,12288
Mvav 84,148,204
K 195,178,179
Mvav 52,148,196
X 181
Mvav 56,152,196
X 182
Mvav 88,152,204
Dc 245,8,61,1024
Ra 148,242,13312
Mvav 120,152,212
v_add_u32 v246, 1161216, v246
v_min_u32 v246, s23, v246
Mvav 152,152,220
K 202,180,181
Mvav 156,156,220
X 183
Mvav 124,156,212
Dc 245,8,61,2048
Ra 152,242,14336
Mvav 92,156,204
X 184
Mvav 60,156,196
W 3
K 203,182,183
v_bfe_u32 v28, v1, 4, 4
Cvaa 160,128,0,224
X 185
Mvaa 160,132,4
Dc 245,8,61,3072
v_lshlrev_b32 v28, 3, v28
Cvaa 164,132,20,228
s_lshl_b32 s80, s56, 9
X 186
Mvaa 164,128,16
s_add_u32 s80, s80, 4224
Ra 156,242,15360
K 210,184,185
Cvaa 168,128,32,232
s_add_u32 s61, s61, s63
X 187
Mvaa 168,132,36
buffer_load_dwordx2 v[28:29], v28, s[8:11], s80 offen sc1
Cvaa 172,132,52,236
s_xor_b32 s62, s62, 0x10000
X 188
Mvaa 172,128,48
Ra 128,242,24576
K 211,186,187
Mvaa 160,136,8
X 189
Mvaa 160,140,12
s_add_u32 s64, s64, 1
global_load_dword v3, v246, s[18:19]
Ra 132,242,25600
Mvaa 164,140,28
s_mov_b64 s[18:19], s[34:35]
s_mul_i32 s81, s58, 6
s_add_u32 s81, s81, s57
Mvaa 164,136,24
s_sub_u32 s81, s81, 2
s_cmp_ge_u32 s57, 2
s_cselect_b32 s23, 2211836, 0
s_lshl_b32 s81, s81, 1
Mvaa 168,136,40
s_add_u32 s81, s81, s14
s_sub_u32 s81, s81, 2
s_mul_i32 s81, s81, 6016
Mvaa 168,140,44
s_mul_i32 s82, s59, 286720
s_add_u32 s81, s81, s82
v_min_u32 v246, 736, v1
Mvaa 172,140,60
v_lshl_add_u32 v246, v246, 3, s81
v_min_u32 v246, s23, v246
Mvaa 172,136,56
W 2
global_load_dword v3, v246, s[18:19]
Ra 136,242,26624
)"
R"(Cvaa 176,144,0,224
X 190
Mvaa 176,148,4
Ra 140,242,27648
K 218,188,189
Cvaa 180,148,20,228
X 191
Mvaa 180,144,16
X 160
Cvaa 184,144,32,232
K 219,190,191
Mvaa 184,148,36
X 161
Cvaa 188,148,52,236
X 162
Mvaa 188,144,48
Ra 144,242,28672
K 196,160,161
Mvaa 176,152,8
X 163
Mvaa 176,156,12
X 164
Mvaa 180,156,28
Ra 148,242,29696
K 197,162,163
Mvaa 180,152,24
X 165
Mvaa 184,152,40
X 166
Mvaa 184,156,44
K 204,164,165
Mvaa 188,156,60
X 167
Mvaa 188,152,56
W 2
X 168
Mvav 32,128,192
Ra 152,242,30720
K 205,166,167
Mvav 64,128,200
X 169
Mvav 96,128,208
Ra 156,242,31744
Mvav 128,128,216
X 170
Mvav 132,132,216
K 212,168,169
Mvav 100,132,208
X 171
Mvav 68,132,200
X 172
Mvav 36,132,192
K 213,170,171
Mvav 40,136,192
X 173
Mvav 72,136,200
X 174
Mvav 104,136,208
Mvav 136,136,216
s_waitcnt vmcnt(2)
v_cmp_eq_u64 vcc, s[12:13], v[28:29]
s_cmp_eq_u64 vcc, -1
s_cbranch_scc0 .Lspin_67%=
.Lok_68%=:
K 220,172,173
Mvav 140,140,216
X 175
Mvav 108,140,208
X 176
Mvav 76,140,200
W 0
s_barrier
v_xor_b32 v242, 0x10000, v242
Mvav 44,140,192
Ra 128,242,0
K 221,174,175
Mvav 48,144,192
X 177
Mvav 80,144,200
X 178
Mvav 112,144,208
Ra 132,242,1024
Mvav 144,144,216
K 198,176,177
Mvav 148,148,216
X 179
Mvav 116,148,208
X 180
Mvav 84,148,200
Ra 136,242,2048
Mvav 52,148,192
K 199,178,179
Mvav 56,152,192
X 181
Mvav 88,152,200
Ra 140,242,3072
Mvav 120,152,208
X 182
Mvav 152,152,216
K 206,180,181
Mvav 156,156,216
X 183
Mvav 124,156,208
Mvav 92,156,200
K 207,182,183
Mvav 60,156,192
W 0
Cvaa 160,128,0,224
Ra 144,242,4096
Ra 148,242,5120
Mvaa 160,132,4
s_add_u32 m0, s62, 0
Ra 152,242,6144
Cvaa 164,132,20,228
X 184
Mvaa 164,128,16
X 185
Cvaa 168,128,32,232
X 186
Mvaa 168,132,36
Ra 156,242,7168
K 214,184,185
Cvaa 172,132,52,236
Dc 1,8,61,0
Mvaa 172,128,48
X 187
Mvaa 160,136,8
X 188
Mvaa 160,140,12
Ra 128,242,16384
K 215,186,187
Mvaa 164,140,28
Dc 1,8,61,1024
Mvaa 164,136,24
X 189
Mvaa 168,136,40
X 190
Mvaa 168,140,44
Ra 132,242,17408
K 222,188,189
Mvaa 172,140,60
Dc 1,8,61,2048
Mvaa 172,136,56
X 191
v_smfmac_f32_16x16x64_bf16 a[160:163], a[176:179], v[192:199], v240
Ra 136,242,18432
K 223,190,191
v_smfmac_f32_16x16x64_bf16 a[164:167], a[176:179], v[200:207], v240
X 160
v_smfmac_f32_16x16x64_bf16 a[168:171], a[176:179], v[208:215], v240
Dc 1,8,61,3072
v_smfmac_f32_16x16x64_bf16 a[172:175], a[176:179], v[216:223], v240
W 3
X 161
Cvaa 176,144,0,224
s_add_u32 m0, s62, 4096
X 162
Mvaa 176,148,4
Ra 140,242,19456
K 192,160,161
Cvaa 180,148,20,228
X 163
Mvaa 180,144,16
Dc 243,8,61,0
Cvaa 184,144,32,232
X 164
Mvaa 184,148,36
K 193,162,163
Cvaa 188,148,52,236
X 165
Mvaa 188,144,48
Dc 243,8,61,1024
Ra 144,242,20480
Mvaa 176,152,8
X 166
Mvaa 176,156,12
Ra 148,242,21504
K 200,164,165
Mvaa 180,156,28
X 167
Mvaa 180,152,24
Dc 243,8,61,2048
Mvaa 184,152,40
X 168
Mvaa 184,156,44
K 201,166,167
Mvaa 188,156,60
X 169
Mvaa 188,152,56
W 2
Dc 243,8,61,3072
Ra 152,242,22528
Mvav 32,128,196
s_add_u32 m0, s62, 8192
X 170
Mvav 64,128,204
Ra 156,242,23552
K 208,168,169
Mvav 96,128,212
X 171
Mvav 128,128,220
Dc 244,8,61,0
Mvav 132,132,220
X 172
Mvav 100,132,212
Ra 128,242,8192
K 209,170,171
Mvav 68,132,204
X 173
Mvav 36,132,196
Dc 244,8,61,1024
Mvav 40,136,196
X 174
Mvav 72,136,204
Ra 132,242,9216
K 216,172,173
Mvav 104,136,212
X 175
Mvav 136,136,220
Dc 244,8,61,2048
Mvav 140,140,220
X 176
Mvav 108,140,212
Ra 136,242,10240
K 217,174,175
Mvav 76,140,204
X 177
Mvav 44,140,196
W 3
Dc 244,8,61,3072
Mvav 48,144,196
X 178
Mvav 80,144,204
s_add_u32 m0, s62, 12288
Ra 140,242,11264
Mvav 112,144,212
K 194,176,177
Mvav 144,144,220
X 179
Mvav 148,148,220
X 180
Mvav 116,148,212
Dc 245,8,61,0
Ra 144,242,12288
Mvav 84,148,204
K 195,178,179
Mvav 52,148,196
X 181
Mvav 56,152,196
X 182
Mvav 88,152,204
Dc 245,8,61,1024
Ra 148,242,13312
Mvav 120,152,212
K 202,180,181
Mvav 152,152,220
X 183
Mvav 156,156,220
X 184
Mvav 124,156,212
Dc 245,8,61,2048
Ra 152,242,14336
Mvav 92,156,204
K 203,182,183
Mvav 60,156,196
W 3
X 185
Cvaa 160,128,0,224
X 186
Mvaa 160,132,4
Dc 245,8,61,3072
Ra 156,242,15360
Cvaa 164,132,20,228
s_add_u32 s61, s61, s63
K 210,184,185
Mvaa 164,128,16
X 187
Cvaa 168,128,32,232
X 188
Mvaa 168,132,36
Cvaa 172,132,52,236
s_xor_b32 s62, s62, 0x10000
K 211,186,187
Mvaa 172,128,48
X 189
Mvaa 160,136,8
Ra 128,242,24576
Ra 132,242,25600
Mvaa 160,140,12
s_add_u32 s64, s64, 1
X 190
Mvaa 164,140,28
K 218,188,189
Mvaa 164,136,24
X 191
Mvaa 168,136,40
Mvaa 168,140,44
X 160
Mvaa 172,140,60
s_add_u32 s36, s29, 131072
K 219,190,191
Mvaa 172,136,56
W 2
X 161
Cvaa 176,144,0,224
Ra 136,242,26624
Ra 140,242,27648
Mvaa 176,148,4
X 162
Cvaa 180,148,20,228
K 196,160,161
Mvaa 180,144,16
buffer_store_dwordx4 a[124:127], v1, s[8:11], s36 offen offset:0 sc1
Cvaa 184,144,32,232
X 163
Mvaa 184,148,36
X 164
Cvaa 188,148,52,236
K 197,162,163
Mvaa 188,144,48
X 165
Mvaa 176,152,8
Ra 144,242,28672
buffer_store_dwordx4 a[116:119], v1, s[8:11], s36 offen offset:1024 sc1
Mvaa 176,156,12
Ra 148,242,29696
Mvaa 180,156,28
X 166
Mvaa 180,152,24
K 204,164,165
Mvaa 184,152,40
X 167
Mvaa 184,156,44
buffer_store_dwordx4 a[108:111], v1, s[8:11], s36 offen offset:2048 sc1
Mvaa 188,156,60
X 168
Mvaa 188,152,56
W 2
Ra 152,242,30720
K 205,166,167
Mvav 32,128,192
X 169
Mvav 64,128,200
Ra 156,242,31744
buffer_store_dwordx4 a[204:207], v1, s[8:11], s36 offen offset:3072 sc1
Mvav 96,128,208
X 170
Mvav 128,128,216
K 212,168,169
Mvav 132,132,216
X 171
Mvav 100,132,208
s_add_u32 s36, s29, 135168
X 172
Mvav 68,132,200
Mvav 36,132,192
K 213,170,171
Mvav 40,136,192
X 173
Mvav 72,136,200
X 174
Mvav 104,136,208
buffer_store_dwordx4 a[212:215], v1, s[8:11], s36 offen offset:0 sc1
Mvav 136,136,216
K 220,172,173
Mvav 140,140,216
X 175
Mvav 108,140,208
X 176
Mvav 76,140,200
s_waitcnt vmcnt(5) lgkmcnt(0)
s_barrier
v_xor_b32 v242, 0x10000, v242
Mvav 44,140,192
buffer_store_dwordx4 a[220:223], v1, s[8:11], s36 offen offset:1024 sc1
K 221,174,175
Mvav 48,144,192
Ra 128,242,0
Mvav 80,144,200
X 177
Mvav 112,144,208
X 178
Mvav 144,144,216
Ra 132,242,1024
K 198,176,177
Mvav 148,148,216
buffer_store_dwordx4 a[228:231], v1, s[8:11], s36 offen offset:2048 sc1
Mvav 116,148,208
X 179
Mvav 84,148,200
X 180
Mvav 52,148,192
Ra 136,242,2048
K 199,178,179
Mvav 56,152,192
buffer_store_dwordx4 a[76:79], v1, s[8:11], s36 offen offset:3072 sc1
Ra 140,242,3072
Mvav 88,152,200
X 181
Mvav 120,152,208
)"
R"(X 182
Mvav 152,152,216
K 206,180,181
Mvav 156,156,216
X 183
Mvav 124,156,208
Mvav 92,156,200
K 207,182,183
Mvav 60,156,192
W 0
Cvaa 160,128,0,224
Ra 144,242,4096
Ra 148,242,5120
Mvaa 160,132,4
s_add_u32 m0, s62, 0
Ra 152,242,6144
Cvaa 164,132,20,228
X 184
Mvaa 164,128,16
X 185
Cvaa 168,128,32,232
X 186
Mvaa 168,132,36
Ra 156,242,7168
K 214,184,185
Cvaa 172,132,52,236
Dc 1,8,61,0
Mvaa 172,128,48
X 187
Mvaa 160,136,8
X 188
Mvaa 160,140,12
Ra 128,242,16384
K 215,186,187
Mvaa 164,140,28
Dc 1,8,61,1024
Mvaa 164,136,24
X 189
Mvaa 168,136,40
X 190
Mvaa 168,140,44
Ra 132,242,17408
K 222,188,189
Mvaa 172,140,60
Dc 1,8,61,2048
Mvaa 172,136,56
X 191
v_smfmac_f32_16x16x64_bf16 a[160:163], a[176:179], v[192:199], v240
Ra 136,242,18432
K 223,190,191
v_smfmac_f32_16x16x64_bf16 a[164:167], a[176:179], v[200:207], v240
X 160
v_smfmac_f32_16x16x64_bf16 a[168:171], a[176:179], v[208:215], v240
Dc 1,8,61,3072
v_smfmac_f32_16x16x64_bf16 a[172:175], a[176:179], v[216:223], v240
W 3
X 161
Cvaa 176,144,0,224
s_add_u32 m0, s62, 4096
X 162
Mvaa 176,148,4
Ra 140,242,19456
K 192,160,161
Cvaa 180,148,20,228
X 163
Mvaa 180,144,16
Dc 243,8,61,0
Cvaa 184,144,32,232
X 164
Mvaa 184,148,36
K 193,162,163
Cvaa 188,148,52,236
X 165
Mvaa 188,144,48
Dc 243,8,61,1024
Ra 144,242,20480
Mvaa 176,152,8
X 166
Mvaa 176,156,12
Ra 148,242,21504
K 200,164,165
Mvaa 180,156,28
X 167
Mvaa 180,152,24
Dc 243,8,61,2048
Mvaa 184,152,40
X 168
Mvaa 184,156,44
K 201,166,167
Mvaa 188,156,60
X 169
Mvaa 188,152,56
W 2
Dc 243,8,61,3072
Ra 152,242,22528
Mvav 32,128,196
s_add_u32 m0, s62, 8192
X 170
Mvav 64,128,204
Ra 156,242,23552
K 208,168,169
Mvav 96,128,212
X 171
Mvav 128,128,220
Dc 244,8,61,0
Mvav 132,132,220
X 172
Mvav 100,132,212
Ra 128,242,8192
K 209,170,171
Mvav 68,132,204
X 173
Mvav 36,132,196
Dc 244,8,61,1024
Mvav 40,136,196
X 174
Mvav 72,136,204
Ra 132,242,9216
K 216,172,173
Mvav 104,136,212
X 175
Mvav 136,136,220
Dc 244,8,61,2048
Mvav 140,140,220
X 176
Mvav 108,140,212
Ra 136,242,10240
K 217,174,175
Mvav 76,140,204
X 177
Mvav 44,140,196
W 3
Dc 244,8,61,3072
Mvav 48,144,196
X 178
Mvav 80,144,204
s_add_u32 m0, s62, 12288
Ra 140,242,11264
Mvav 112,144,212
K 194,176,177
Mvav 144,144,220
X 179
Mvav 148,148,220
X 180
Mvav 116,148,212
Dc 245,8,61,0
Ra 144,242,12288
Mvav 84,148,204
K 195,178,179
Mvav 52,148,196
X 181
Mvav 56,152,196
X 182
Mvav 88,152,204
Dc 245,8,61,1024
Ra 148,242,13312
Mvav 120,152,212
K 202,180,181
Mvav 152,152,220
X 183
Mvav 156,156,220
X 184
Mvav 124,156,212
Dc 245,8,61,2048
Ra 152,242,14336
Mvav 92,156,204
K 203,182,183
Mvav 60,156,196
W 3
X 185
Cvaa 160,128,0,224
X 186
Mvaa 160,132,4
Dc 245,8,61,3072
Ra 156,242,15360
Cvaa 164,132,20,228
s_add_u32 s61, s61, s63
K 210,184,185
Mvaa 164,128,16
X 187
Cvaa 168,128,32,232
X 188
Mvaa 168,132,36
Cvaa 172,132,52,236
s_xor_b32 s62, s62, 0x10000
K 211,186,187
Mvaa 172,128,48
X 189
Mvaa 160,136,8
Ra 128,242,24576
Ra 132,242,25600
Mvaa 160,140,12
s_add_u32 s64, s64, 1
X 190
Mvaa 164,140,28
K 218,188,189
Mvaa 164,136,24
X 191
Mvaa 168,136,40
Mvaa 168,140,44
X 160
Mvaa 172,140,60
s_add_u32 s36, s29, 327680
K 219,190,191
Mvaa 172,136,56
W 2
X 161
Cvaa 176,144,0,224
Ra 136,242,26624
Ra 140,242,27648
Mvaa 176,148,4
X 162
Cvaa 180,148,20,228
K 196,160,161
Mvaa 180,144,16
buffer_store_dwordx4 a[84:87], v1, s[8:11], s36 offen offset:0 sc1
Cvaa 184,144,32,232
X 163
Mvaa 184,148,36
X 164
Cvaa 188,148,52,236
K 197,162,163
Mvaa 188,144,48
X 165
Mvaa 176,152,8
Ra 144,242,28672
buffer_store_dwordx4 a[92:95], v1, s[8:11], s36 offen offset:1024 sc1
Mvaa 176,156,12
Ra 148,242,29696
Mvaa 180,156,28
X 166
Mvaa 180,152,24
K 204,164,165
Mvaa 184,152,40
X 167
Mvaa 184,156,44
buffer_store_dwordx4 a[188:191], v1, s[8:11], s36 offen offset:2048 sc1
Mvaa 188,156,60
X 168
Mvaa 188,152,56
W 2
Ra 152,242,30720
K 205,166,167
Mvav 32,128,192
X 169
Mvav 64,128,200
Ra 156,242,31744
buffer_store_dwordx4 a[196:199], v1, s[8:11], s36 offen offset:3072 sc1
Mvav 96,128,208
X 170
Mvav 128,128,216
K 212,168,169
Mvav 132,132,216
X 171
Mvav 100,132,208
s_add_u32 s36, s29, 331776
X 172
Mvav 68,132,200
Mvav 36,132,192
K 213,170,171
Mvav 40,136,192
X 173
Mvav 72,136,200
X 174
Mvav 104,136,208
buffer_store_dwordx4 a[236:239], v1, s[8:11], s36 offen offset:0 sc1
Mvav 136,136,216
K 220,172,173
Mvav 140,140,216
X 175
Mvav 108,140,208
X 176
Mvav 76,140,200
s_waitcnt vmcnt(5) lgkmcnt(0)
s_barrier
v_xor_b32 v242, 0x10000, v242
Mvav 44,140,192
buffer_store_dwordx4 a[244:247], v1, s[8:11], s36 offen offset:1024 sc1
K 221,174,175
Mvav 48,144,192
Ra 128,242,0
Mvav 80,144,200
X 177
Mvav 112,144,208
X 178
Mvav 144,144,216
Ra 132,242,1024
K 198,176,177
Mvav 148,148,216
buffer_store_dwordx4 a[68:71], v1, s[8:11], s36 offen offset:2048 sc1
Mvav 116,148,208
X 179
Mvav 84,148,200
X 180
Mvav 52,148,192
Ra 136,242,2048
K 199,178,179
Mvav 56,152,192
buffer_store_dwordx4 a[184:187], v1, s[8:11], s36 offen offset:3072 sc1
Ra 140,242,3072
Mvav 88,152,200
X 181
Mvav 120,152,208
X 182
Mvav 152,152,216
K 206,180,181
Mvav 156,156,216
X 183
Mvav 124,156,208
Mvav 92,156,200
K 207,182,183
Mvav 60,156,192
W 0
Cvaa 160,128,0,224
Ra 144,242,4096
Ra 148,242,5120
Mvaa 160,132,4
s_add_u32 m0, s62, 0
Ra 152,242,6144
Cvaa 164,132,20,228
X 184
Mvaa 164,128,16
X 185
Cvaa 168,128,32,232
X 186
Mvaa 168,132,36
Ra 156,242,7168
K 214,184,185
Cvaa 172,132,52,236
Dc 1,8,61,0
Mvaa 172,128,48
X 187
Mvaa 160,136,8
X 188
Mvaa 160,140,12
Ra 128,242,16384
K 215,186,187
Mvaa 164,140,28
Dc 1,8,61,1024
Mvaa 164,136,24
X 189
Mvaa 168,136,40
X 190
Mvaa 168,140,44
Ra 132,242,17408
K 222,188,189
Mvaa 172,140,60
Dc 1,8,61,2048
Mvaa 172,136,56
X 191
v_smfmac_f32_16x16x64_bf16 a[160:163], a[176:179], v[192:199], v240
Ra 136,242,18432
K 223,190,191
v_smfmac_f32_16x16x64_bf16 a[164:167], a[176:179], v[200:207], v240
X 160
v_smfmac_f32_16x16x64_bf16 a[168:171], a[176:179], v[208:215], v240
Dc 1,8,61,3072
v_smfmac_f32_16x16x64_bf16 a[172:175], a[176:179], v[216:223], v240
W 3
X 161
Cvaa 176,144,0,224
s_add_u32 m0, s62, 4096
X 162
Mvaa 176,148,4
Ra 140,242,19456
K 192,160,161
Cvaa 180,148,20,228
X 163
Mvaa 180,144,16
Dc 243,8,61,0
Cvaa 184,144,32,232
X 164
Mvaa 184,148,36
K 193,162,163
Cvaa 188,148,52,236
X 165
Mvaa 188,144,48
Dc 243,8,61,1024
Ra 144,242,20480
Mvaa 176,152,8
X 166
Mvaa 176,156,12
Ra 148,242,21504
K 200,164,165
Mvaa 180,156,28
X 167
Mvaa 180,152,24
Dc 243,8,61,2048
Mvaa 184,152,40
X 168
Mvaa 184,156,44
K 201,166,167
Mvaa 188,156,60
X 169
Mvaa 188,152,56
W 2
Dc 243,8,61,3072
Ra 152,242,22528
Mvav 32,128,196
s_add_u32 m0, s62, 8192
)"
R"(X 170
Mvav 64,128,204
Ra 156,242,23552
K 208,168,169
Mvav 96,128,212
X 171
Mvav 128,128,220
Dc 244,8,61,0
Mvav 132,132,220
X 172
Mvav 100,132,212
Ra 128,242,8192
K 209,170,171
Mvav 68,132,204
X 173
Mvav 36,132,196
Dc 244,8,61,1024
Mvav 40,136,196
X 174
Mvav 72,136,204
Ra 132,242,9216
K 216,172,173
Mvav 104,136,212
X 175
Mvav 136,136,220
Dc 244,8,61,2048
Mvav 140,140,220
X 176
Mvav 108,140,212
Ra 136,242,10240
K 217,174,175
Mvav 76,140,204
X 177
Mvav 44,140,196
W 3
Dc 244,8,61,3072
Mvav 48,144,196
X 178
Mvav 80,144,204
s_add_u32 m0, s62, 12288
Ra 140,242,11264
Mvav 112,144,212
K 194,176,177
Mvav 144,144,220
X 179
Mvav 148,148,220
X 180
Mvav 116,148,212
Dc 245,8,61,0
Ra 144,242,12288
Mvav 84,148,204
K 195,178,179
Mvav 52,148,196
X 181
Mvav 56,152,196
X 182
Mvav 88,152,204
Dc 245,8,61,1024
Ra 148,242,13312
Mvav 120,152,212
K 202,180,181
Mvav 152,152,220
X 183
Mvav 156,156,220
s_waitcnt vmcnt(14)
s_add_u32 s80, s71, 256
v_mov_b64 v[28:29], s[12:13]
s_mov_b64 exec, 1
buffer_store_dwordx2 v[28:29], off, s[8:11], s80 sc1
s_mov_b64 exec, -1
X 184
Mvav 124,156,212
Ra 152,242,14336
Mvav 92,156,204
K 203,182,183
Mvav 60,156,196
W 3
X 185
Cvaa 160,128,0,224
Dc 245,8,61,2048
Ra 156,242,15360
Mvaa 160,132,4
X 186
Cvaa 164,132,20,228
K 210,184,185
Mvaa 164,128,16
X 187
Cvaa 168,128,32,232
Dc 245,8,61,3072
Mvaa 168,132,36
s_add_u32 s61, s61, s63
X 188
Cvaa 172,132,52,236
s_xor_b32 s62, s62, 0x10000
K 211,186,187
Mvaa 172,128,48
X 189
Mvaa 160,136,8
Ra 128,242,24576
Ra 132,242,25600
Mvaa 160,140,12
s_add_u32 s64, s64, 1
X 190
Mvaa 164,140,28
K 218,188,189
Mvaa 164,136,24
X 191
Mvaa 168,136,40
Mvaa 168,140,44
X 160
Mvaa 172,140,60
s_add_u32 s36, s29, 196608
K 219,190,191
Mvaa 172,136,56
W 2
X 161
Cvaa 176,144,0,224
Ra 136,242,26624
Ra 140,242,27648
Mvaa 176,148,4
X 162
Cvaa 180,148,20,228
K 196,160,161
Mvaa 180,144,16
buffer_store_dwordx4 a[192:195], v1, s[8:11], s36 offen offset:0 sc1
Cvaa 184,144,32,232
X 163
Mvaa 184,148,36
X 164
Cvaa 188,148,52,236
K 197,162,163
Mvaa 188,144,48
X 165
Mvaa 176,152,8
Ra 144,242,28672
buffer_store_dwordx4 a[200:203], v1, s[8:11], s36 offen offset:1024 sc1
Mvaa 176,156,12
Ra 148,242,29696
Mvaa 180,156,28
X 166
Mvaa 180,152,24
K 204,164,165
Mvaa 184,152,40
X 167
Mvaa 184,156,44
buffer_store_dwordx4 a[208:211], v1, s[8:11], s36 offen offset:2048 sc1
Mvaa 188,156,60
X 168
Mvaa 188,152,56
W 2
Ra 152,242,30720
K 205,166,167
Mvav 32,128,192
X 169
Mvav 64,128,200
Ra 156,242,31744
buffer_store_dwordx4 a[216:219], v1, s[8:11], s36 offen offset:3072 sc1
Mvav 96,128,208
X 170
Mvav 128,128,216
K 212,168,169
Mvav 132,132,216
X 171
Mvav 100,132,208
s_add_u32 s36, s29, 200704
X 172
Mvav 68,132,200
Mvav 36,132,192
K 213,170,171
Mvav 40,136,192
X 173
Mvav 72,136,200
X 174
Mvav 104,136,208
buffer_store_dwordx4 a[224:227], v1, s[8:11], s36 offen offset:0 sc1
Mvav 136,136,216
K 220,172,173
Mvav 140,140,216
X 175
Mvav 108,140,208
X 176
Mvav 76,140,200
s_waitcnt vmcnt(5) lgkmcnt(0)
s_barrier
v_xor_b32 v242, 0x10000, v242
Mvav 44,140,192
buffer_store_dwordx4 a[232:235], v1, s[8:11], s36 offen offset:1024 sc1
K 221,174,175
Mvav 48,144,192
Ra 128,242,0
Mvav 80,144,200
X 177
Mvav 112,144,208
X 178
Mvav 144,144,216
Ra 132,242,1024
K 198,176,177
Mvav 148,148,216
buffer_store_dwordx4 a[240:243], v1, s[8:11], s36 offen offset:2048 sc1
Mvav 116,148,208
X 179
Mvav 84,148,200
X 180
Mvav 52,148,192
Ra 136,242,2048
K 199,178,179
Mvav 56,152,192
buffer_store_dwordx4 a[100:103], v1, s[8:11], s36 offen offset:3072 sc1
Ra 140,242,3072
Mvav 88,152,200
X 181
Mvav 120,152,208
X 182
Mvav 152,152,216
K 206,180,181
Mvav 156,156,216
X 183
Mvav 124,156,208
Mvav 92,156,200
K 207,182,183
Mvav 60,156,192
W 0
Cvaa 160,128,0,224
Ra 144,242,4096
Ra 148,242,5120
Mvaa 160,132,4
s_add_u32 m0, s62, 0
Ra 152,242,6144
Cvaa 164,132,20,228
X 184
Mvaa 164,128,16
X 185
Cvaa 168,128,32,232
X 186
Mvaa 168,132,36
Ra 156,242,7168
K 214,184,185
Cvaa 172,132,52,236
Dc 1,8,61,0
Mvaa 172,128,48
X 187
Mvaa 160,136,8
X 188
Mvaa 160,140,12
Ra 128,242,16384
K 215,186,187
Mvaa 164,140,28
Dc 1,8,61,1024
Mvaa 164,136,24
X 189
Mvaa 168,136,40
X 190
Mvaa 168,140,44
Ra 132,242,17408
K 222,188,189
Mvaa 172,140,60
Dc 1,8,61,2048
Mvaa 172,136,56
X 191
v_smfmac_f32_16x16x64_bf16 a[160:163], a[176:179], v[192:199], v240
Ra 136,242,18432
K 223,190,191
v_smfmac_f32_16x16x64_bf16 a[164:167], a[176:179], v[200:207], v240
X 160
v_smfmac_f32_16x16x64_bf16 a[168:171], a[176:179], v[208:215], v240
Dc 1,8,61,3072
v_smfmac_f32_16x16x64_bf16 a[172:175], a[176:179], v[216:223], v240
W 3
X 161
Cvaa 176,144,0,224
s_add_u32 m0, s62, 4096
X 162
Mvaa 176,148,4
Ra 140,242,19456
K 192,160,161
Cvaa 180,148,20,228
X 163
Mvaa 180,144,16
Dc 243,8,61,0
Cvaa 184,144,32,232
X 164
Mvaa 184,148,36
K 193,162,163
Cvaa 188,148,52,236
X 165
Mvaa 188,144,48
Dc 243,8,61,1024
Ra 144,242,20480
Mvaa 176,152,8
X 166
Mvaa 176,156,12
Ra 148,242,21504
K 200,164,165
Mvaa 180,156,28
X 167
Mvaa 180,152,24
Dc 243,8,61,2048
Mvaa 184,152,40
X 168
Mvaa 184,156,44
K 201,166,167
Mvaa 188,156,60
X 169
Mvaa 188,152,56
W 2
Dc 243,8,61,3072
Ra 152,242,22528
Mvav 32,128,196
s_add_u32 m0, s62, 8192
X 170
Mvav 64,128,204
Ra 156,242,23552
K 208,168,169
Mvav 96,128,212
X 171
Mvav 128,128,220
Dc 244,8,61,0
Mvav 132,132,220
X 172
Mvav 100,132,212
Ra 128,242,8192
K 209,170,171
Mvav 68,132,204
X 173
Mvav 36,132,196
Dc 244,8,61,1024
Mvav 40,136,196
X 174
Mvav 72,136,204
Ra 132,242,9216
K 216,172,173
Mvav 104,136,212
X 175
Mvav 136,136,220
Dc 244,8,61,2048
Mvav 140,140,220
X 176
Mvav 108,140,212
Ra 136,242,10240
K 217,174,175
Mvav 76,140,204
X 177
Mvav 44,140,196
W 3
Dc 244,8,61,3072
Mvav 48,144,196
X 178
Mvav 80,144,204
s_add_u32 m0, s62, 12288
Ra 140,242,11264
Mvav 112,144,212
K 194,176,177
Mvav 144,144,220
X 179
Mvav 148,148,220
X 180
Mvav 116,148,212
Dc 245,8,61,0
Ra 144,242,12288
Mvav 84,148,204
K 195,178,179
Mvav 52,148,196
X 181
Mvav 56,152,196
X 182
Mvav 88,152,204
Dc 245,8,61,1024
Ra 148,242,13312
Mvav 120,152,212
K 202,180,181
Mvav 152,152,220
X 183
Mvav 156,156,220
X 184
Mvav 124,156,212
Dc 245,8,61,2048
Ra 152,242,14336
Mvav 92,156,204
K 203,182,183
v_bfe_u32 v28, v1, 4, 4
Mvav 60,156,196
W 3
X 185
Cvaa 160,128,0,224
Ra 156,242,15360
v_lshlrev_b32 v28, 3, v28
Mvaa 160,132,4
s_lshl_b32 s80, s56, 9
s_add_u32 s80, s80, 4352
Dc 245,8,61,3072
Cvaa 164,132,20,228
s_add_u32 s61, s61, s63
X 186
Mvaa 164,128,16
K 210,184,185
Cvaa 168,128,32,232
X 187
Mvaa 168,132,36
buffer_load_dwordx2 v[28:29], v28, s[8:11], s80 offen sc1
Cvaa 172,132,52,236
)"
R"(s_xor_b32 s62, s62, 0x10000
X 188
Mvaa 172,128,48
Ra 128,242,24576
K 211,186,187
Mvaa 160,136,8
X 189
Mvaa 160,140,12
s_add_u32 s64, s64, 1
X 190
Mvaa 164,140,28
Ra 132,242,25600
K 218,188,189
Mvaa 164,136,24
X 191
Mvaa 168,136,40
Mvaa 168,140,44
X 160
Mvaa 172,140,60
s_add_u32 s36, s29, 262144
K 219,190,191
Mvaa 172,136,56
W 2
X 161
Cvaa 176,144,0,224
Ra 136,242,26624
Ra 140,242,27648
Mvaa 176,148,4
X 162
Cvaa 180,148,20,228
K 196,160,161
Mvaa 180,144,16
buffer_store_dwordx4 a[64:67], v1, s[8:11], s36 offen offset:0 sc1
Cvaa 184,144,32,232
X 163
Mvaa 184,148,36
X 164
Cvaa 188,148,52,236
K 197,162,163
Mvaa 188,144,48
X 165
Mvaa 176,152,8
Ra 144,242,28672
buffer_store_dwordx4 a[72:75], v1, s[8:11], s36 offen offset:1024 sc1
Mvaa 176,156,12
Ra 148,242,29696
Mvaa 180,156,28
X 166
Mvaa 180,152,24
K 204,164,165
Mvaa 184,152,40
X 167
Mvaa 184,156,44
buffer_store_dwordx4 a[80:83], v1, s[8:11], s36 offen offset:2048 sc1
Mvaa 188,156,60
X 168
Mvaa 188,152,56
W 2
Ra 152,242,30720
K 205,166,167
Mvav 32,128,192
X 169
Mvav 64,128,200
Ra 156,242,31744
buffer_store_dwordx4 a[88:91], v1, s[8:11], s36 offen offset:3072 sc1
Mvav 96,128,208
X 170
Mvav 128,128,216
K 212,168,169
Mvav 132,132,216
X 171
Mvav 100,132,208
s_add_u32 s36, s29, 266240
X 172
Mvav 68,132,200
Mvav 36,132,192
K 213,170,171
Mvav 40,136,192
X 173
Mvav 72,136,200
X 174
Mvav 104,136,208
buffer_store_dwordx4 a[96:99], v1, s[8:11], s36 offen offset:0 sc1
Mvav 136,136,216
s_waitcnt vmcnt(5)
v_cmp_eq_u64 vcc, s[12:13], v[28:29]
s_cmp_eq_u64 vcc, -1
s_cbranch_scc0 .Lspin_70%=
.Lok_71%=:
K 220,172,173
Mvav 140,140,216
X 175
Mvav 108,140,208
X 176
Mvav 76,140,200
W 0
s_barrier
v_xor_b32 v242, 0x10000, v242
Mvav 44,140,192
buffer_store_dwordx4 a[104:107], v1, s[8:11], s36 offen offset:1024 sc1
K 221,174,175
Mvav 48,144,192
Ra 128,242,0
Mvav 80,144,200
X 177
Mvav 112,144,208
X 178
Mvav 144,144,216
Ra 132,242,1024
K 198,176,177
Mvav 148,148,216
buffer_store_dwordx4 a[112:115], v1, s[8:11], s36 offen offset:2048 sc1
Mvav 116,148,208
X 179
Mvav 84,148,200
X 180
Mvav 52,148,192
Ra 136,242,2048
K 199,178,179
Mvav 56,152,192
buffer_store_dwordx4 a[120:123], v1, s[8:11], s36 offen offset:3072 sc1
Ra 140,242,3072
Mvav 88,152,200
X 181
Mvav 120,152,208
X 182
Mvav 152,152,216
K 206,180,181
Mvav 156,156,216
X 183
Mvav 124,156,208
Mvav 92,156,200
K 207,182,183
Mvav 60,156,192
W 0
Cvaa 160,128,0,224
Ra 144,242,4096
Ra 148,242,5120
Mvaa 160,132,4
s_add_u32 m0, s62, 0
Ra 152,242,6144
Cvaa 164,132,20,228
X 184
Mvaa 164,128,16
X 185
Cvaa 168,128,32,232
X 186
Mvaa 168,132,36
Ra 156,242,7168
K 214,184,185
Cvaa 172,132,52,236
Dc 1,8,61,0
Mvaa 172,128,48
X 187
Mvaa 160,136,8
X 188
Mvaa 160,140,12
Ra 128,242,16384
K 215,186,187
Mvaa 164,140,28
Dc 1,8,61,1024
Mvaa 164,136,24
X 189
Mvaa 168,136,40
X 190
Mvaa 168,140,44
Ra 132,242,17408
K 222,188,189
Mvaa 172,140,60
Dc 1,8,61,2048
Mvaa 172,136,56
X 191
v_smfmac_f32_16x16x64_bf16 a[160:163], a[176:179], v[192:199], v240
Ra 136,242,18432
K 223,190,191
v_smfmac_f32_16x16x64_bf16 a[164:167], a[176:179], v[200:207], v240
X 160
v_smfmac_f32_16x16x64_bf16 a[168:171], a[176:179], v[208:215], v240
Dc 1,8,61,3072
v_smfmac_f32_16x16x64_bf16 a[172:175], a[176:179], v[216:223], v240
W 3
X 161
Cvaa 176,144,0,224
s_add_u32 m0, s62, 4096
X 162
Mvaa 176,148,4
Ra 140,242,19456
K 192,160,161
Cvaa 180,148,20,228
X 163
Mvaa 180,144,16
Dc 243,8,61,0
Cvaa 184,144,32,232
X 164
Mvaa 184,148,36
K 193,162,163
Cvaa 188,148,52,236
X 165
Mvaa 188,144,48
Dc 243,8,61,1024
Ra 144,242,20480
Mvaa 176,152,8
X 166
Mvaa 176,156,12
Ra 148,242,21504
K 200,164,165
Mvaa 180,156,28
X 167
Mvaa 180,152,24
Dc 243,8,61,2048
Mvaa 184,152,40
X 168
Mvaa 184,156,44
K 201,166,167
Mvaa 188,156,60
X 169
Mvaa 188,152,56
W 2
Dc 243,8,61,3072
Ra 152,242,22528
Mvav 32,128,196
s_add_u32 m0, s62, 8192
X 170
Mvav 64,128,204
Ra 156,242,23552
K 208,168,169
Mvav 96,128,212
X 171
Mvav 128,128,220
Dc 244,8,61,0
Mvav 132,132,220
X 172
Mvav 100,132,212
Ra 128,242,8192
K 209,170,171
Mvav 68,132,204
X 173
Mvav 36,132,196
Dc 244,8,61,1024
Mvav 40,136,196
X 174
Mvav 72,136,204
Ra 132,242,9216
K 216,172,173
Mvav 104,136,212
X 175
Mvav 136,136,220
Dc 244,8,61,2048
Mvav 140,140,220
X 176
Mvav 108,140,212
Ra 136,242,10240
K 217,174,175
Mvav 76,140,204
X 177
Mvav 44,140,196
W 3
Dc 244,8,61,3072
Mvav 48,144,196
X 178
Mvav 80,144,204
s_add_u32 m0, s62, 12288
Ra 140,242,11264
Mvav 112,144,212
K 194,176,177
Mvav 144,144,220
X 179
Mvav 148,148,220
X 180
Mvav 116,148,212
Dc 245,8,61,0
Ra 144,242,12288
Mvav 84,148,204
K 195,178,179
Mvav 52,148,196
X 181
Mvav 56,152,196
X 182
Mvav 88,152,204
Dc 245,8,61,1024
Ra 148,242,13312
Mvav 120,152,212
K 202,180,181
Mvav 152,152,220
s_add_u32 s81, s57, 2
X 183
Mvav 156,156,220
s_waitcnt vmcnt(14)
s_add_u32 s80, s71, 384
v_mov_b64 v[28:29], s[12:13]
s_mov_b64 exec, 1
buffer_store_dwordx2 v[28:29], off, s[8:11], s80 sc1
s_mov_b64 exec, -1
X 184
Mvav 124,156,212
s_bitcmp0_b32 s81, 2
s_cselect_b32 s22, -1, 0
s_and_b32 s81, s81, 3
Ra 152,242,14336
Mvav 92,156,204
s_lshl_b32 s82, s56, 2
K 203,182,183
Mvav 60,156,196
W 3
X 185
Cvaa 160,128,0,224
s_add_u32 s81, s81, s82
Dc 245,8,61,2048
Ra 156,242,15360
Mvaa 160,132,4
s_lshl_b32 s81, s81, 1
X 186
Cvaa 164,132,20,228
s_add_u32 s81, s81, s14
s_sub_u32 s81, s81, 2
K 210,184,185
Mvaa 164,128,16
s_mul_i32 s81, s81, 20480
X 187
Cvaa 168,128,32,232
Dc 245,8,61,3072
v_min_u32 v2, 624, v1
Mvaa 168,132,36
s_add_u32 s61, s61, s63
X 188
Cvaa 172,132,52,236
s_xor_b32 s62, s62, 0x10000
v_lshl_add_u32 v2, v2, 3, s81
K 211,186,187
Mvaa 172,128,48
Ra 128,242,24576
v_and_b32 v2, s22, v2
Mvaa 160,136,8
s_and_b32 s22, s22, 5120
X 189
Mvaa 160,140,12
s_add_u32 s64, s64, 1
global_load_dword v3, v2, s[26:27]
Ra 132,242,25600
Mvaa 164,140,28
X 190
Mvaa 164,136,24
v_add_u32 v2, s22, v2
K 218,188,189
Mvaa 168,136,40
X 191
Mvaa 168,140,44
X 160
Mvaa 172,140,60
K 219,190,191
Mvaa 172,136,56
W 2
X 161
Cvaa 176,144,0,224
Ra 136,242,26624
Ra 140,242,27648
Mvaa 176,148,4
X 162
Cvaa 180,148,20,228
K 196,160,161
Mvaa 180,144,16
X 163
Cvaa 184,144,32,232
Mvaa 184,148,36
X 164
Cvaa 188,148,52,236
K 197,162,163
Mvaa 188,144,48
X 165
Mvaa 176,152,8
Ra 144,242,28672
Ra 148,242,29696
Mvaa 176,156,12
X 166
Mvaa 180,156,28
K 204,164,165
Mvaa 180,152,24
X 167
Mvaa 184,152,40
Mvaa 184,156,44
X 168
Mvaa 188,156,60
Mvaa 188,152,56
W 2
Ra 152,242,30720
K 205,166,167
Mvav 32,128,192
X 169
Mvav 64,128,200
Ra 156,242,31744
Mvav 96,128,208
X 170
Mvav 128,128,216
K 212,168,169
Mvav 132,132,216
X 171
Mvav 100,132,208
X 172
Mvav 68,132,200
Mvav 36,132,192
)"
R"(K 213,170,171
Mvav 40,136,192
X 173
Mvav 72,136,200
X 174
Mvav 104,136,208
Mvav 136,136,216
K 220,172,173
Mvav 140,140,216
X 175
Mvav 108,140,208
X 176
Mvav 76,140,200
s_waitcnt vmcnt(1) lgkmcnt(0)
s_barrier
v_xor_b32 v242, 0x10000, v242
Mvav 44,140,192
Ra 128,242,0
K 221,174,175
Mvav 48,144,192
X 177
Mvav 80,144,200
X 178
Mvav 112,144,208
Ra 132,242,1024
Mvav 144,144,216
K 198,176,177
Mvav 148,148,216
X 179
Mvav 116,148,208
X 180
Mvav 84,148,200
Ra 136,242,2048
Mvav 52,148,192
K 199,178,179
Mvav 56,152,192
X 181
Mvav 88,152,200
Ra 140,242,3072
Mvav 120,152,208
X 182
Mvav 152,152,216
K 206,180,181
Mvav 156,156,216
X 183
Mvav 124,156,208
Mvav 92,156,200
K 207,182,183
Mvav 60,156,192
W 0
Cvaa 160,128,0,224
Ra 144,242,4096
Ra 148,242,5120
Mvaa 160,132,4
s_add_u32 m0, s62, 0
Ra 152,242,6144
Cvaa 164,132,20,228
X 184
Mvaa 164,128,16
X 185
Cvaa 168,128,32,232
X 186
Mvaa 168,132,36
Ra 156,242,7168
K 214,184,185
Cvaa 172,132,52,236
Dc 1,8,61,0
Mvaa 172,128,48
X 187
Mvaa 160,136,8
X 188
Mvaa 160,140,12
Ra 128,242,16384
K 215,186,187
Mvaa 164,140,28
Dc 1,8,61,1024
Mvaa 164,136,24
X 189
Mvaa 168,136,40
X 190
Mvaa 168,140,44
Ra 132,242,17408
K 222,188,189
Mvaa 172,140,60
Dc 1,8,61,2048
Mvaa 172,136,56
X 191
v_smfmac_f32_16x16x64_bf16 a[160:163], a[176:179], v[192:199], v240
Ra 136,242,18432
K 223,190,191
v_smfmac_f32_16x16x64_bf16 a[164:167], a[176:179], v[200:207], v240
X 160
v_smfmac_f32_16x16x64_bf16 a[168:171], a[176:179], v[208:215], v240
Dc 1,8,61,3072
v_smfmac_f32_16x16x64_bf16 a[172:175], a[176:179], v[216:223], v240
W 3
X 161
Cvaa 176,144,0,224
s_add_u32 m0, s62, 4096
X 162
Mvaa 176,148,4
Ra 140,242,19456
K 192,160,161
Cvaa 180,148,20,228
X 163
Mvaa 180,144,16
Dc 243,8,61,0
Cvaa 184,144,32,232
X 164
Mvaa 184,148,36
K 193,162,163
Cvaa 188,148,52,236
X 165
Mvaa 188,144,48
Dc 243,8,61,1024
Ra 144,242,20480
Mvaa 176,152,8
X 166
Mvaa 176,156,12
Ra 148,242,21504
K 200,164,165
Mvaa 180,156,28
X 167
Mvaa 180,152,24
Dc 243,8,61,2048
Mvaa 184,152,40
X 168
Mvaa 184,156,44
K 201,166,167
Mvaa 188,156,60
X 169
Mvaa 188,152,56
W 2
Dc 243,8,61,3072
Ra 152,242,22528
Mvav 32,128,196
s_add_u32 m0, s62, 8192
X 170
Mvav 64,128,204
Ra 156,242,23552
K 208,168,169
Mvav 96,128,212
X 171
Mvav 128,128,220
Dc 244,8,61,0
Mvav 132,132,220
X 172
Mvav 100,132,212
Ra 128,242,8192
K 209,170,171
Mvav 68,132,204
X 173
Mvav 36,132,196
Dc 244,8,61,1024
Mvav 40,136,196
X 174
Mvav 72,136,204
Ra 132,242,9216
K 216,172,173
Mvav 104,136,212
X 175
Mvav 136,136,220
Dc 244,8,61,2048
Mvav 140,140,220
X 176
Mvav 108,140,212
Ra 136,242,10240
K 217,174,175
Mvav 76,140,204
X 177
Mvav 44,140,196
W 3
Dc 244,8,61,3072
Mvav 48,144,196
X 178
Mvav 80,144,204
s_add_u32 m0, s62, 12288
Ra 140,242,11264
Mvav 112,144,212
K 194,176,177
Mvav 144,144,220
X 179
Mvav 148,148,220
X 180
Mvav 116,148,212
Dc 245,8,61,0
Ra 144,242,12288
Mvav 84,148,204
K 195,178,179
Mvav 52,148,196
X 181
Mvav 56,152,196
X 182
Mvav 88,152,204
Dc 245,8,61,1024
Ra 148,242,13312
Mvav 120,152,212
K 202,180,181
Mvav 152,152,220
X 183
Mvav 156,156,220
X 184
Mvav 124,156,212
Dc 245,8,61,2048
Ra 152,242,14336
Mvav 92,156,204
K 203,182,183
Mvav 60,156,196
W 3
X 185
Cvaa 160,128,0,224
X 186
Mvaa 160,132,4
Dc 245,8,61,3072
Ra 156,242,15360
Cvaa 164,132,20,228
s_add_u32 s61, s61, s63
K 210,184,185
Mvaa 164,128,16
X 187
Cvaa 168,128,32,232
X 188
Mvaa 168,132,36
Cvaa 172,132,52,236
s_xor_b32 s62, s62, 0x10000
global_load_dword v3, v2, s[26:27]
K 211,186,187
Mvaa 172,128,48
X 189
Mvaa 160,136,8
Ra 128,242,24576
Ra 132,242,25600
Mvaa 160,140,12
s_add_u32 s64, s64, 1
X 190
Mvaa 164,140,28
v_add_u32 v2, s22, v2
K 218,188,189
Mvaa 164,136,24
X 191
Mvaa 168,136,40
Mvaa 168,140,44
X 160
Mvaa 172,140,60
K 219,190,191
Mvaa 172,136,56
W 2
X 161
Cvaa 176,144,0,224
Ra 136,242,26624
Ra 140,242,27648
Mvaa 176,148,4
X 162
Cvaa 180,148,20,228
K 196,160,161
Mvaa 180,144,16
X 163
Cvaa 184,144,32,232
Mvaa 184,148,36
X 164
Cvaa 188,148,52,236
K 197,162,163
Mvaa 188,144,48
X 165
Mvaa 176,152,8
Ra 144,242,28672
Ra 148,242,29696
Mvaa 176,156,12
X 166
Mvaa 180,156,28
K 204,164,165
Mvaa 180,152,24
X 167
Mvaa 184,152,40
Mvaa 184,156,44
X 168
Mvaa 188,156,60
Mvaa 188,152,56
W 2
Ra 152,242,30720
K 205,166,167
Mvav 32,128,192
X 169
Mvav 64,128,200
Ra 156,242,31744
Mvav 96,128,208
X 170
Mvav 128,128,216
K 212,168,169
Mvav 132,132,216
X 171
Mvav 100,132,208
X 172
Mvav 68,132,200
Mvav 36,132,192
K 213,170,171
Mvav 40,136,192
X 173
Mvav 72,136,200
X 174
Mvav 104,136,208
Mvav 136,136,216
K 220,172,173
Mvav 140,140,216
X 175
Mvav 108,140,208
X 176
Mvav 76,140,200
s_waitcnt vmcnt(1) lgkmcnt(0)
s_barrier
v_xor_b32 v242, 0x10000, v242
Mvav 44,140,192
Ra 128,242,0
K 221,174,175
Mvav 48,144,192
X 177
Mvav 80,144,200
X 178
Mvav 112,144,208
Ra 132,242,1024
Mvav 144,144,216
K 198,176,177
Mvav 148,148,216
X 179
Mvav 116,148,208
X 180
Mvav 84,148,200
Ra 136,242,2048
Mvav 52,148,192
K 199,178,179
Mvav 56,152,192
X 181
Mvav 88,152,200
Ra 140,242,3072
Mvav 120,152,208
X 182
Mvav 152,152,216
K 206,180,181
Mvav 156,156,216
X 183
Mvav 124,156,208
Mvav 92,156,200
K 207,182,183
Mvav 60,156,192
W 0
Cvaa 160,128,0,224
Ra 144,242,4096
Ra 148,242,5120
Mvaa 160,132,4
s_add_u32 m0, s62, 0
Ra 152,242,6144
Cvaa 164,132,20,228
X 184
Mvaa 164,128,16
X 185
Cvaa 168,128,32,232
X 186
Mvaa 168,132,36
Ra 156,242,7168
K 214,184,185
Cvaa 172,132,52,236
Dc 1,8,61,0
Mvaa 172,128,48
X 187
Mvaa 160,136,8
X 188
Mvaa 160,140,12
Ra 128,242,16384
K 215,186,187
Mvaa 164,140,28
Dc 1,8,61,1024
Mvaa 164,136,24
X 189
Mvaa 168,136,40
X 190
Mvaa 168,140,44
Ra 132,242,17408
K 222,188,189
Mvaa 172,140,60
Dc 1,8,61,2048
Mvaa 172,136,56
X 191
v_smfmac_f32_16x16x64_bf16 a[160:163], a[176:179], v[192:199], v240
Ra 136,242,18432
K 223,190,191
v_smfmac_f32_16x16x64_bf16 a[164:167], a[176:179], v[200:207], v240
X 160
v_smfmac_f32_16x16x64_bf16 a[168:171], a[176:179], v[208:215], v240
Dc 1,8,61,3072
v_smfmac_f32_16x16x64_bf16 a[172:175], a[176:179], v[216:223], v240
W 3
X 161
Cvaa 176,144,0,224
s_add_u32 m0, s62, 4096
X 162
Mvaa 176,148,4
Ra 140,242,19456
K 192,160,161
Cvaa 180,148,20,228
X 163
)"
R"(Mvaa 180,144,16
Dc 243,8,61,0
Cvaa 184,144,32,232
X 164
Mvaa 184,148,36
K 193,162,163
Cvaa 188,148,52,236
X 165
Mvaa 188,144,48
Dc 243,8,61,1024
Ra 144,242,20480
Mvaa 176,152,8
X 166
Mvaa 176,156,12
Ra 148,242,21504
K 200,164,165
Mvaa 180,156,28
X 167
Mvaa 180,152,24
Dc 243,8,61,2048
Mvaa 184,152,40
X 168
Mvaa 184,156,44
K 201,166,167
Mvaa 188,156,60
X 169
Mvaa 188,152,56
W 2
Dc 243,8,61,3072
Ra 152,242,22528
Mvav 32,128,196
s_add_u32 m0, s62, 8192
X 170
Mvav 64,128,204
Ra 156,242,23552
K 208,168,169
Mvav 96,128,212
X 171
Mvav 128,128,220
Dc 244,8,61,0
Mvav 132,132,220
X 172
Mvav 100,132,212
Ra 128,242,8192
K 209,170,171
Mvav 68,132,204
X 173
Mvav 36,132,196
Dc 244,8,61,1024
Mvav 40,136,196
X 174
Mvav 72,136,204
Ra 132,242,9216
K 216,172,173
Mvav 104,136,212
X 175
Mvav 136,136,220
Dc 244,8,61,2048
Mvav 140,140,220
X 176
Mvav 108,140,212
Ra 136,242,10240
K 217,174,175
Mvav 76,140,204
X 177
Mvav 44,140,196
W 3
Dc 244,8,61,3072
Mvav 48,144,196
X 178
Mvav 80,144,204
s_add_u32 m0, s62, 12288
Ra 140,242,11264
Mvav 112,144,212
K 194,176,177
Mvav 144,144,220
X 179
Mvav 148,148,220
X 180
Mvav 116,148,212
Dc 245,8,61,0
Ra 144,242,12288
Mvav 84,148,204
K 195,178,179
Mvav 52,148,196
X 181
Mvav 56,152,196
X 182
Mvav 88,152,204
Dc 245,8,61,1024
Ra 148,242,13312
Mvav 120,152,212
K 202,180,181
Mvav 152,152,220
X 183
Mvav 156,156,220
X 184
Mvav 124,156,212
Dc 245,8,61,2048
Ra 152,242,14336
Mvav 92,156,204
K 203,182,183
Mvav 60,156,196
W 3
X 185
Cvaa 160,128,0,224
X 186
Mvaa 160,132,4
Dc 245,8,61,3072
Ra 156,242,15360
Cvaa 164,132,20,228
s_add_u32 s61, s61, s63
K 210,184,185
Mvaa 164,128,16
X 187
Cvaa 168,128,32,232
X 188
Mvaa 168,132,36
Cvaa 172,132,52,236
s_xor_b32 s62, s62, 0x10000
global_load_dword v3, v2, s[26:27]
K 211,186,187
Mvaa 172,128,48
Ra 128,242,24576
v_add_u32 v2, s22, v2
Mvaa 160,136,8
s_lshr_b32 s80, s15, 3
s_lshl_b32 s80, s80, 1
s_add_u32 s80, s80, s14
Mvaa 160,140,12
s_sub_u32 s80, s80, 2
s_lshl_b32 s80, s80, 12
v_min_u32 v27, 496, v1
Mvaa 164,140,28
s_add_u32 s64, s64, 1
X 189
Mvaa 164,136,24
Ra 132,242,25600
v_lshl_add_u32 v27, v27, 3, s80
Mvaa 168,136,40
X 190
Mvaa 168,140,44
v_min_u32 v27, .Lend%=-.Lbeg%=-4, v27
K 218,188,189
Mvaa 172,140,60
global_load_dword v3, v27, s[6:7]
Mvaa 172,136,56
W 2
X 191
Cvaa 176,144,0,224
X 160
Mvaa 176,148,4
Ra 136,242,26624
K 219,190,191
Cvaa 180,148,20,228
X 161
Mvaa 180,144,16
Ra 140,242,27648
Cvaa 184,144,32,232
X 162
Mvaa 184,148,36
K 196,160,161
Cvaa 188,148,52,236
X 163
Mvaa 188,144,48
X 164
Mvaa 176,152,8
Ra 144,242,28672
K 197,162,163
Mvaa 176,156,12
X 165
Mvaa 180,156,28
Ra 148,242,29696
Mvaa 180,152,24
X 166
Mvaa 184,152,40
K 204,164,165
Mvaa 184,156,44
X 167
Mvaa 188,156,60
X 168
Mvaa 188,152,56
W 2
Ra 152,242,30720
K 205,166,167
Mvav 32,128,192
X 169
Mvav 64,128,200
Ra 156,242,31744
Mvav 96,128,208
X 170
Mvav 128,128,216
K 212,168,169
Mvav 132,132,216
X 171
Mvav 100,132,208
X 172
Mvav 68,132,200
Mvav 36,132,192
K 213,170,171
Mvav 40,136,192
X 173
Mvav 72,136,200
X 174
Mvav 104,136,208
Mvav 136,136,216
K 220,172,173
Mvav 140,140,216
X 175
Mvav 108,140,208
X 176
Mvav 76,140,200
s_waitcnt vmcnt(2) lgkmcnt(0)
s_barrier
v_xor_b32 v242, 0x10000, v242
Mvav 44,140,192
Ra 128,242,0
K 221,174,175
Mvav 48,144,192
X 177
Mvav 80,144,200
X 178
Mvav 112,144,208
Ra 132,242,1024
Mvav 144,144,216
K 198,176,177
Mvav 148,148,216
X 179
Mvav 116,148,208
X 180
Mvav 84,148,200
Ra 136,242,2048
Mvav 52,148,192
K 199,178,179
Mvav 56,152,192
X 181
Mvav 88,152,200
Ra 140,242,3072
Mvav 120,152,208
X 182
Mvav 152,152,216
K 206,180,181
Mvav 156,156,216
X 183
Mvav 124,156,208
Mvav 92,156,200
K 207,182,183
Mvav 60,156,192
W 0
Cvaa 160,128,0,224
Ra 144,242,4096
Ra 148,242,5120
Mvaa 160,132,4
s_add_u32 m0, s62, 0
Ra 152,242,6144
Cvaa 164,132,20,228
X 184
Mvaa 164,128,16
X 185
Cvaa 168,128,32,232
X 186
Mvaa 168,132,36
Ra 156,242,7168
K 214,184,185
Cvaa 172,132,52,236
Dc 1,8,61,0
Mvaa 172,128,48
X 187
Mvaa 160,136,8
X 188
Mvaa 160,140,12
Ra 128,242,16384
K 215,186,187
Mvaa 164,140,28
Dc 1,8,61,1024
Mvaa 164,136,24
X 189
Mvaa 168,136,40
X 190
Mvaa 168,140,44
Ra 132,242,17408
K 222,188,189
Mvaa 172,140,60
Dc 1,8,61,2048
Mvaa 172,136,56
X 191
v_smfmac_f32_16x16x64_bf16 a[160:163], a[176:179], v[192:199], v240
Ra 136,242,18432
K 223,190,191
v_smfmac_f32_16x16x64_bf16 a[164:167], a[176:179], v[200:207], v240
X 160
v_smfmac_f32_16x16x64_bf16 a[168:171], a[176:179], v[208:215], v240
Dc 1,8,61,3072
v_smfmac_f32_16x16x64_bf16 a[172:175], a[176:179], v[216:223], v240
W 3
X 161
Cvaa 176,144,0,224
s_add_u32 m0, s62, 4096
X 162
Mvaa 176,148,4
s_sub_u32 s81, s57, 1
Ra 140,242,19456
K 192,160,161
Cvaa 180,148,20,228
X 163
Mvaa 180,144,16
s_lshl_b32 s85, s81, 5
Dc 243,8,61,0
Cvaa 184,144,32,232
X 164
Mvaa 184,148,36
s_add_u32 s85, s85, s56
K 193,162,163
Cvaa 188,148,52,236
X 165
Mvaa 188,144,48
Dc 243,8,61,1024
Ra 144,242,20480
Mvaa 176,152,8
s_lshl_b32 s85, s85, 1
X 166
Mvaa 176,156,12
Ra 148,242,21504
K 200,164,165
Mvaa 180,156,28
s_add_u32 s85, s85, s14
X 167
Mvaa 180,152,24
Dc 243,8,61,2048
Mvaa 184,152,40
s_sub_u32 s85, s85, 2
X 168
Mvaa 184,156,44
K 201,166,167
Mvaa 188,156,60
X 169
Mvaa 188,152,56
W 2
s_cmp_lt_u32 s85, 160
s_cselect_b64 s[20:21], 0, -1
Dc 243,8,61,3072
Mvav 32,128,196
s_add_u32 m0, s62, 8192
Ra 152,242,22528
Ra 156,242,23552
Mvav 64,128,204
s_mul_i32 s18, s85, 205
X 170
Mvav 96,128,212
K 208,168,169
Mvav 128,128,220
s_lshr_b32 s18, s18, 12
X 171
Mvav 132,132,220
Dc 244,8,61,0
Ra 128,242,8192
Mvav 100,132,212
X 172
Mvav 68,132,204
s_and_b32 s18, s18, 7
K 209,170,171
Mvav 36,132,196
X 173
Mvav 40,136,196
Dc 244,8,61,1024
v_lshrrev_b32 v4, 1, v1
Mvav 72,136,204
X 174
Mvav 104,136,212
Ra 132,242,9216
K 216,172,173
Mvav 136,136,220
X 175
Mvav 140,140,220
Dc 244,8,61,2048
v_min_u32 v5, 344, v4
Mvav 108,140,212
s_lshl_b32 s81, s18, 3
X 176
Mvav 76,140,204
Ra 136,242,10240
K 217,174,175
Mvav 44,140,196
W 3
X 177
Mvav 48,144,196
Dc 244,8,61,3072
v_mov_b32 v6, s81
Mvav 80,144,204
s_add_u32 m0, s62, 12288
X 178
Mvav 112,144,212
Ra 140,242,11264
K 194,176,177
Mvav 144,144,220
X 179
Mvav 148,148,220
buffer_load_dwordx2 v[8:9], v4, s[8:11], 0 offen offset:2560 sc1
Ra 144,242,12288
Mvav 116,148,212
)"
R"(X 180
Mvav 84,148,204
K 195,178,179
Mvav 52,148,196
X 181
Mvav 56,152,196
buffer_load_dwordx2 v[10:11], v5, s[8:11], 0 offen offset:3072 sc1
Ra 148,242,13312
Mvav 88,152,204
X 182
Mvav 120,152,212
K 202,180,181
Mvav 152,152,220
X 183
Mvav 156,156,220
Dc 245,8,61,0
Ra 152,242,14336
Mvav 124,156,212
X 184
Mvav 92,156,204
K 203,182,183
v_bfe_u32 v28, v1, 4, 4
Mvav 60,156,196
W 3
X 185
Cvaa 160,128,0,224
buffer_load_dwordx2 v[12:13], v6, s[8:11], 0 offen offset:3584 sc1
v_lshlrev_b32 v28, 3, v28
Mvaa 160,132,4
s_lshl_b32 s80, s56, 9
X 186
Cvaa 164,132,20,228
s_add_u32 s80, s80, 4480
Ra 156,242,15360
K 210,184,185
Mvaa 164,128,16
X 187
Cvaa 168,128,32,232
Dc 245,8,61,1024
Mvaa 168,132,36
X 188
Cvaa 172,132,52,236
K 211,186,187
Mvaa 172,128,48
X 189
Mvaa 160,136,8
Dc 245,8,61,2048
Ra 128,242,24576
Mvaa 160,140,12
X 190
Mvaa 164,140,28
Ra 132,242,25600
K 218,188,189
Mvaa 164,136,24
X 191
Mvaa 168,136,40
buffer_load_dwordx2 v[28:29], v28, s[8:11], s80 offen sc1
Mvaa 168,140,44
Dc 245,8,61,3072
s_add_u32 s61, s61, s63
s_xor_b32 s62, s62, 0x10000
s_add_u32 s64, s64, 1
Mvaa 172,140,60
X 160
Mvaa 172,136,56
W 2
Ra 136,242,26624
K 219,190,191
Cvaa 176,144,0,224
X 161
Mvaa 176,148,4
global_load_dword v3, v2, s[26:27]
v_add_u32 v2, s22, v2
Cvaa 180,148,20,228
Ra 140,242,27648
Mvaa 180,144,16
X 162
Cvaa 184,144,32,232
K 196,160,161
Mvaa 184,148,36
X 163
Cvaa 188,148,52,236
X 164
Mvaa 188,144,48
Ra 144,242,28672
K 197,162,163
Mvaa 176,152,8
X 165
Mvaa 176,156,12
Ra 148,242,29696
Mvaa 180,156,28
X 166
Mvaa 180,152,24
K 204,164,165
Mvaa 184,152,40
X 167
Mvaa 184,156,44
X 168
Mvaa 188,156,60
Mvaa 188,152,56
W 2
Ra 152,242,30720
K 205,166,167
Mvav 32,128,192
X 169
Mvav 64,128,200
Ra 156,242,31744
Mvav 96,128,208
X 170
Mvav 128,128,216
K 212,168,169
Mvav 132,132,216
X 171
Mvav 100,132,208
X 172
Mvav 68,132,200
Mvav 36,132,192
K 213,170,171
Mvav 40,136,192
X 173
Mvav 72,136,200
X 174
Mvav 104,136,208
Mvav 136,136,216
s_waitcnt vmcnt(2)
v_cmp_eq_u64 vcc, s[12:13], v[28:29]
s_cmp_eq_u64 vcc, -1
s_cbranch_scc0 .Lspin_73%=
.Lok_74%=:
v_cmp_eq_u64 vcc, s[12:13], v[8:9]
v_cmp_eq_u64_e64 s[86:87], s[12:13], v[10:11]
s_and_b64 vcc, vcc, s[86:87]
s_or_b64 vcc, vcc, s[20:21]
s_cmp_eq_u64 vcc, -1
s_cbranch_scc0 .Lspin_76%=
.Lok_77%=:
K 220,172,173
Mvav 140,140,216
X 175
Mvav 108,140,208
X 176
Mvav 76,140,200
s_waitcnt vmcnt(1) lgkmcnt(0)
s_barrier
v_xor_b32 v242, 0x10000, v242
Mvav 44,140,192
Ra 128,242,0
K 221,174,175
Mvav 48,144,192
X 177
Mvav 80,144,200
X 178
Mvav 112,144,208
Ra 132,242,1024
Mvav 144,144,216
K 198,176,177
Mvav 148,148,216
X 179
Mvav 116,148,208
X 180
Mvav 84,148,200
Ra 136,242,2048
Mvav 52,148,192
K 199,178,179
Mvav 56,152,192
X 181
Mvav 88,152,200
Ra 140,242,3072
Mvav 120,152,208
X 182
Mvav 152,152,216
K 206,180,181
Mvav 156,156,216
X 183
Mvav 124,156,208
Mvav 92,156,200
K 207,182,183
Mvav 60,156,192
W 0
Cvaa 160,128,0,224
Ra 144,242,4096
Ra 148,242,5120
Mvaa 160,132,4
s_add_u32 m0, s62, 0
Ra 152,242,6144
Cvaa 164,132,20,228
X 184
Mvaa 164,128,16
X 185
Cvaa 168,128,32,232
X 186
Mvaa 168,132,36
Ra 156,242,7168
K 214,184,185
Cvaa 172,132,52,236
Dc 1,8,61,0
Mvaa 172,128,48
X 187
Mvaa 160,136,8
X 188
Mvaa 160,140,12
Ra 128,242,16384
K 215,186,187
Mvaa 164,140,28
Dc 1,8,61,1024
Mvaa 164,136,24
X 189
Mvaa 168,136,40
X 190
Mvaa 168,140,44
Ra 132,242,17408
K 222,188,189
Mvaa 172,140,60
Dc 1,8,61,2048
Mvaa 172,136,56
X 191
v_smfmac_f32_16x16x64_bf16 a[160:163], a[176:179], v[192:199], v240
Ra 136,242,18432
K 223,190,191
v_smfmac_f32_16x16x64_bf16 a[164:167], a[176:179], v[200:207], v240
X 160
v_smfmac_f32_16x16x64_bf16 a[168:171], a[176:179], v[208:215], v240
Dc 1,8,61,3072
v_smfmac_f32_16x16x64_bf16 a[172:175], a[176:179], v[216:223], v240
W 3
X 161
Cvaa 176,144,0,224
s_add_u32 m0, s62, 4096
X 162
Mvaa 176,148,4
Ra 140,242,19456
K 192,160,161
Cvaa 180,148,20,228
X 163
Mvaa 180,144,16
Dc 243,8,61,0
Cvaa 184,144,32,232
X 164
Mvaa 184,148,36
K 193,162,163
Cvaa 188,148,52,236
X 165
Mvaa 188,144,48
Dc 243,8,61,1024
Ra 144,242,20480
Mvaa 176,152,8
X 166
Mvaa 176,156,12
Ra 148,242,21504
K 200,164,165
Mvaa 180,156,28
X 167
Mvaa 180,152,24
Dc 243,8,61,2048
Mvaa 184,152,40
X 168
Mvaa 184,156,44
K 201,166,167
Mvaa 188,156,60
X 169
Mvaa 188,152,56
W 2
Dc 243,8,61,3072
Ra 152,242,22528
Mvav 32,128,196
s_add_u32 m0, s62, 8192
X 170
Mvav 64,128,204
Ra 156,242,23552
K 208,168,169
Mvav 96,128,212
X 171
Mvav 128,128,220
Dc 244,8,61,0
Mvav 132,132,220
X 172
Mvav 100,132,212
Ra 128,242,8192
K 209,170,171
Mvav 68,132,204
X 173
Mvav 36,132,196
Dc 244,8,61,1024
Mvav 40,136,196
X 174
Mvav 72,136,204
Ra 132,242,9216
K 216,172,173
Mvav 104,136,212
X 175
Mvav 136,136,220
Dc 244,8,61,2048
Mvav 140,140,220
X 176
Mvav 108,140,212
Ra 136,242,10240
K 217,174,175
Mvav 76,140,204
X 177
Mvav 44,140,196
W 3
Dc 244,8,61,3072
Mvav 48,144,196
X 178
Mvav 80,144,204
s_add_u32 m0, s62, 12288
Ra 140,242,11264
Mvav 112,144,212
K 194,176,177
Mvav 144,144,220
X 179
Mvav 148,148,220
X 180
Mvav 116,148,212
Dc 245,8,61,0
Ra 144,242,12288
Mvav 84,148,204
K 195,178,179
Mvav 52,148,196
X 181
Mvav 56,152,196
X 182
Mvav 88,152,204
Dc 245,8,61,1024
Ra 148,242,13312
Mvav 120,152,212
v_xor_b32 v6, s12, v12
K 202,180,181
Mvav 152,152,220
X 183
Mvav 156,156,220
Ra 152,242,14336
v_and_b32 v6, 2047, v6
Mvav 124,156,212
Dc 245,8,61,2048
v_lshlrev_b32 v6, 3, v6
Mvav 92,156,204
X 184
Mvav 60,156,196
W 3
K 203,182,183
Cvaa 160,128,0,224
s_mov_b32 s81, 40960
X 185
Mvaa 160,132,4
Dc 245,8,61,3072
Ra 156,242,15360
Cvaa 164,132,20,228
s_add_u32 s61, s61, s63
X 186
Mvaa 164,128,16
K 210,184,185
Cvaa 168,128,32,232
X 187
Mvaa 168,132,36
buffer_load_dword v7, v6, s[8:11], s81 offen sc1
v_lshrrev_b32 v5, 7, v1
Cvaa 172,132,52,236
s_xor_b32 s62, s62, 0x10000
X 188
Mvaa 172,128,48
K 211,186,187
v_mul_u32_u24 v4, 20480, v5
Mvaa 160,136,8
X 189
Mvaa 160,140,12
s_add_u32 s64, s64, 1
Ra 128,242,24576
Ra 132,242,25600
Mvaa 164,140,28
X 190
Mvaa 164,136,24
v_and_b32 v6, 0x70, v1
K 218,188,189
Mvaa 168,136,40
X 191
Mvaa 168,140,44
v_add_u32 v4, v4, v6
Mvaa 172,140,60
X 160
Mvaa 172,136,56
W 2
v_add_u32 v5, 2, v5
K 219,190,191
Cvaa 176,144,0,224
X 161
Mvaa 176,148,4
Ra 136,242,26624
v_lshrrev_b32 v5, 3, v5
Cvaa 180,148,20,228
Ra 140,242,27648
v_lshl_or_b32 v5, v5, 31, v4
Mvaa 180,144,16
X 162
Cvaa 184,144,32,232
K 196,160,161
Mvaa 184,148,36
s_lshl_b32 s19, s85, 7
X 163
Cvaa 188,148,52,236
)"
R"(X 164
Mvaa 188,144,48
s_add_u32 s19, s19, 2752512
Ra 144,242,28672
K 197,162,163
Mvaa 176,152,8
X 165
Mvaa 176,156,12
s_and_b32 s81, s20, 134217728
Ra 148,242,29696
Mvaa 180,156,28
X 166
Mvaa 180,152,24
K 204,164,165
Mvaa 184,152,40
s_or_b32 s19, s19, s81
X 167
Mvaa 184,156,44
X 168
Mvaa 188,156,60
buffer_load_dwordx4 a[64:67], v4, s[8:11], s19 offen sc1
Mvaa 188,152,56
W 2
Ra 152,242,30720
K 205,166,167
Mvav 32,128,192
s_add_u32 s19, s19, 163840
X 169
Mvav 64,128,200
Ra 156,242,31744
Mvav 96,128,208
X 170
Mvav 128,128,216
buffer_load_dwordx4 a[68:71], v4, s[8:11], s19 offen sc1
K 212,168,169
Mvav 132,132,216
X 171
Mvav 100,132,208
s_add_u32 s19, s19, 163840
X 172
Mvav 68,132,200
Mvav 36,132,192
buffer_load_dwordx4 a[72:75], v4, s[8:11], s19 offen sc1
K 213,170,171
Mvav 40,136,192
X 173
Mvav 72,136,200
X 174
Mvav 104,136,208
s_add_u32 s19, s19, 163840
Mvav 136,136,216
K 220,172,173
Mvav 140,140,216
X 175
Mvav 108,140,208
buffer_load_dwordx4 a[76:79], v4, s[8:11], s19 offen sc1
Mvav 76,140,200
s_waitcnt vmcnt(5) lgkmcnt(0)
s_barrier
v_xor_b32 v242, 0x10000, v242
Mvav 44,140,192
s_add_u32 s19, s19, 163840
X 176
Mvav 48,144,192
Ra 128,242,0
K 221,174,175
Mvav 80,144,200
X 177
Mvav 112,144,208
buffer_load_dwordx4 a[80:83], v4, s[8:11], s19 offen sc1
Ra 132,242,1024
Mvav 144,144,216
s_add_u32 s19, s19, 163840
X 178
Mvav 148,148,216
K 198,176,177
Mvav 116,148,208
X 179
Mvav 84,148,200
buffer_load_dwordx4 a[84:87], v4, s[8:11], s19 offen sc1
Ra 136,242,2048
Mvav 52,148,192
X 180
Mvav 56,152,192
s_add_u32 s19, s19, 163840
Ra 140,242,3072
K 199,178,179
Mvav 88,152,200
X 181
Mvav 120,152,208
X 182
Mvav 152,152,216
buffer_load_dwordx4 a[88:91], v5, s[8:11], s19 offen sc1
K 206,180,181
Mvav 156,156,216
X 183
Mvav 124,156,208
Mvav 92,156,200
K 207,182,183
Mvav 60,156,192
W 0
Cvaa 160,128,0,224
Ra 144,242,4096
Ra 148,242,5120
Mvaa 160,132,4
s_add_u32 m0, s62, 0
Ra 152,242,6144
Cvaa 164,132,20,228
X 184
Mvaa 164,128,16
X 185
Cvaa 168,128,32,232
X 186
Mvaa 168,132,36
Ra 156,242,7168
K 214,184,185
Cvaa 172,132,52,236
Dc 1,8,61,0
Mvaa 172,128,48
X 187
Mvaa 160,136,8
X 188
Mvaa 160,140,12
Ra 128,242,16384
K 215,186,187
Mvaa 164,140,28
Dc 1,8,61,1024
Mvaa 164,136,24
X 189
Mvaa 168,136,40
X 190
Mvaa 168,140,44
Ra 132,242,17408
K 222,188,189
Mvaa 172,140,60
Dc 1,8,61,2048
Mvaa 172,136,56
X 191
v_smfmac_f32_16x16x64_bf16 a[160:163], a[176:179], v[192:199], v240
Ra 136,242,18432
K 223,190,191
v_smfmac_f32_16x16x64_bf16 a[164:167], a[176:179], v[200:207], v240
X 160
v_smfmac_f32_16x16x64_bf16 a[168:171], a[176:179], v[208:215], v240
Dc 1,8,61,3072
v_smfmac_f32_16x16x64_bf16 a[172:175], a[176:179], v[216:223], v240
W 3
X 161
Cvaa 176,144,0,224
s_add_u32 m0, s62, 4096
X 162
Mvaa 176,148,4
Ra 140,242,19456
K 192,160,161
Cvaa 180,148,20,228
X 163
Mvaa 180,144,16
Dc 243,8,61,0
Cvaa 184,144,32,232
X 164
Mvaa 184,148,36
K 193,162,163
Cvaa 188,148,52,236
X 165
Mvaa 188,144,48
Dc 243,8,61,1024
Ra 144,242,20480
Mvaa 176,152,8
X 166
Mvaa 176,156,12
Ra 148,242,21504
K 200,164,165
Mvaa 180,156,28
X 167
Mvaa 180,152,24
Dc 243,8,61,2048
Mvaa 184,152,40
X 168
Mvaa 184,156,44
K 201,166,167
Mvaa 188,156,60
X 169
Mvaa 188,152,56
W 2
Dc 243,8,61,3072
Ra 152,242,22528
Mvav 32,128,196
s_add_u32 m0, s62, 8192
X 170
Mvav 64,128,204
Ra 156,242,23552
K 208,168,169
Mvav 96,128,212
X 171
Mvav 128,128,220
Dc 244,8,61,0
Mvav 132,132,220
X 172
Mvav 100,132,212
Ra 128,242,8192
K 209,170,171
Mvav 68,132,204
X 173
Mvav 36,132,196
Dc 244,8,61,1024
Mvav 40,136,196
X 174
Mvav 72,136,204
Ra 132,242,9216
K 216,172,173
Mvav 104,136,212
X 175
Mvav 136,136,220
Dc 244,8,61,2048
Mvav 140,140,220
s_bfe_u32 s81, s59, 0x10001
X 176
Mvav 108,140,212
s_lshl_b32 s82, s57, 1
Ra 136,242,10240
K 217,174,175
Mvav 76,140,204
s_add_u32 s81, s81, s82
X 177
Mvav 44,140,196
W 3
s_lshl_b32 s83, s81, 2
Dc 244,8,61,3072
Mvav 48,144,196
s_lshr_b32 s81, s59, 2
X 178
Mvav 80,144,204
s_add_u32 m0, s62, 12288
s_lshl_b32 s81, s81, 1
Ra 140,242,11264
Mvav 112,144,212
s_add_u32 s83, s83, s81
K 194,176,177
Mvav 144,144,220
s_and_b32 s81, s59, 1
X 179
Mvav 148,148,220
s_add_u32 s83, s83, s81
X 180
Mvav 116,148,212
s_and_b32 s81, s14, 1
Dc 245,8,61,0
Ra 144,242,12288
Mvav 84,148,204
s_mul_i32 s81, s81, 124
K 195,178,179
Mvav 52,148,196
s_xor_b32 s83, s83, s81
X 181
Mvav 56,152,196
s_mul_i32 s83, s83, 20480
X 182
Mvav 88,152,204
s_mul_i32 s81, s58, 320
Dc 245,8,61,1024
Ra 148,242,13312
Mvav 120,152,212
s_add_u32 s82, s83, s81
K 202,180,181
Mvav 152,152,220
X 183
Mvav 156,156,220
Ra 152,242,14336
v_bfe_u32 v246, v1, 4, 4
Mvav 124,156,212
Dc 245,8,61,2048
v_mul_u32_u24 v246, 1280, v246
Mvav 92,156,204
X 184
Mvav 60,156,196
W 3
v_lshrrev_b32 v247, 8, v1
K 203,182,183
Cvaa 160,128,0,224
v_mul_u32_u24 v247, 104, v247
v_add3_u32 v246, v246, v247, s82
Mvaa 160,132,4
X 185
Cvaa 164,132,20,228
Dc 245,8,61,3072
Ra 156,242,15360
Mvaa 164,128,16
s_add_u32 s61, s61, s63
X 186
Cvaa 168,128,32,232
K 210,184,185
Mvaa 168,132,36
X 187
Cvaa 172,132,52,236
s_xor_b32 s62, s62, 0x10000
X 188
Mvaa 172,128,48
global_load_dword v3, v246, s[16:17]
K 211,186,187
Mvaa 160,136,8
s_lshl_b32 s81, s81, 3
s_add_u32 s83, s83, s81
s_add_u32 s83, s83, 131072
Mvaa 160,140,12
s_add_u32 s64, s64, 1
v_lshrrev_b32 v246, 9, v1
v_mul_u32_u24 v246, 10240, v246
Mvaa 164,140,28
Ra 128,242,24576
v_and_b32 v247, 0x1f0, v1
Mvaa 164,136,24
X 189
Mvaa 168,136,40
Ra 132,242,25600
v_min_u32 v247, 304, v247
Mvaa 168,140,44
X 190
Mvaa 172,140,60
v_lshl_add_u32 v246, v247, 3, v246
K 218,188,189
Mvaa 172,136,56
W 2
buffer_load_dword v3, v246, s[8:11], s83 offen sc1
Ra 136,242,26624
Cvaa 176,144,0,224
X 191
Mvaa 176,148,4
X 160
Cvaa 180,148,20,228
Ra 140,242,27648
K 219,190,191
Mvaa 180,144,16
X 161
Cvaa 184,144,32,232
X 162
Mvaa 184,148,36
K 196,160,161
Cvaa 188,148,52,236
X 163
Mvaa 188,144,48
X 164
Mvaa 176,152,8
Ra 144,242,28672
K 197,162,163
Mvaa 176,156,12
X 165
Mvaa 180,156,28
Ra 148,242,29696
Mvaa 180,152,24
X 166
Mvaa 184,152,40
K 204,164,165
Mvaa 184,156,44
X 167
Mvaa 188,156,60
X 168
Mvaa 188,152,56
W 2
Ra 152,242,30720
K 205,166,167
Mvav 32,128,192
X 169
Mvav 64,128,200
Ra 156,242,31744
Mvav 96,128,208
X 170
Mvav 128,128,216
K 212,168,169
Mvav 132,132,216
X 171
Mvav 100,132,208
X 172
Mvav 68,132,200
Mvav 36,132,192
K 213,170,171
Mvav 40,136,192
X 173
Mvav 72,136,200
X 174
Mvav 104,136,208
Mvav 136,136,216
K 220,172,173
Mvav 140,140,216
X 175
Mvav 108,140,208
X 176
Mvav 76,140,200
s_waitcnt vmcnt(2) lgkmcnt(0)
s_barrier
v_xor_b32 v242, 0x10000, v242
Mvav 44,140,192
Ra 128,242,0
K 221,174,175
Mvav 48,144,192
X 177
Mvav 80,144,200
X 178
Mvav 112,144,208
Ra 132,242,1024
Mvav 144,144,216
)"
R"(K 198,176,177
Mvav 148,148,216
X 179
Mvav 116,148,208
X 180
Mvav 84,148,200
Ra 136,242,2048
Mvav 52,148,192
K 199,178,179
Mvav 56,152,192
X 181
Mvav 88,152,200
Ra 140,242,3072
Mvav 120,152,208
X 182
Mvav 152,152,216
K 206,180,181
Mvav 156,156,216
X 183
Mvav 124,156,208
Mvav 92,156,200
K 207,182,183
Mvav 60,156,192
W 0
Cvaa 160,128,0,224
Ra 144,242,4096
Ra 148,242,5120
Mvaa 160,132,4
s_add_u32 m0, s62, 0
Ra 152,242,6144
G 8,64
Cvaa 164,132,20,228
X 184
Mvaa 164,128,16
X 185
Cvaa 168,128,32,232
X 186
Mvaa 168,132,36
G 9,65
K 214,184,185
Cvaa 172,132,52,236
Ra 156,242,7168
Dc 1,8,61,0
Mvaa 172,128,48
X 187
Mvaa 160,136,8
X 188
Mvaa 160,140,12
Ra 128,242,16384
G 10,66
Mvaa 164,140,28
Dc 1,8,61,1024
K 215,186,187
Mvaa 164,136,24
X 189
Mvaa 168,136,40
Ra 132,242,17408
G 11,67
Mvaa 168,140,44
X 190
Mvaa 172,140,60
Dc 1,8,61,2048
K 222,188,189
Mvaa 172,136,56
X 191
v_smfmac_f32_16x16x64_bf16 a[160:163], a[176:179], v[192:199], v240
G 12,68
K 223,190,191
v_smfmac_f32_16x16x64_bf16 a[164:167], a[176:179], v[200:207], v240
Ra 136,242,18432
v_smfmac_f32_16x16x64_bf16 a[168:171], a[176:179], v[208:215], v240
Dc 1,8,61,3072
G 13,69
v_smfmac_f32_16x16x64_bf16 a[172:175], a[176:179], v[216:223], v240
W 3
X 160
Cvaa 176,144,0,224
s_add_u32 m0, s62, 4096
X 161
Mvaa 176,148,4
X 162
Cvaa 180,148,20,228
Ra 140,242,19456
K 192,160,161
Mvaa 180,144,16
Dc 243,8,61,0
G 14,70
Cvaa 184,144,32,232
X 163
Mvaa 184,148,36
X 164
Cvaa 188,148,52,236
G 15,71
K 193,162,163
Mvaa 188,144,48
Dc 243,8,61,1024
Ra 144,242,20480
Mvaa 176,152,8
X 165
Mvaa 176,156,12
X 166
Mvaa 180,156,28
Ra 148,242,21504
A 8,8,12
Mvaa 180,152,24
Dc 243,8,61,2048
K 200,164,165
Mvaa 184,152,40
X 167
Mvaa 184,156,44
A 9,9,13
Mvaa 188,156,60
X 168
Mvaa 188,152,56
W 2
Dc 243,8,61,3072
K 201,166,167
Mvav 32,128,196
s_add_u32 m0, s62, 8192
X 169
Mvav 64,128,204
Ra 152,242,22528
A 10,10,14
Mvav 96,128,212
X 170
Mvav 128,128,220
Ra 156,242,23552
K 208,168,169
Mvav 132,132,220
Dc 244,8,61,0
A 11,11,15
Mvav 100,132,212
X 171
Mvav 68,132,204
X 172
Mvav 36,132,196
Ra 128,242,8192
K 209,170,171
Mvav 40,136,196
Dc 244,8,61,1024
G 12,72
Mvav 72,136,204
X 173
Mvav 104,136,212
X 174
Mvav 136,136,220
Ra 132,242,9216
G 13,73
Mvav 140,140,220
Dc 244,8,61,2048
K 216,172,173
Mvav 108,140,212
X 175
Mvav 76,140,204
X 176
Mvav 44,140,196
W 2
Ra 136,242,10240
G 14,74
Mvav 48,144,196
Dc 244,8,61,3072
K 217,174,175
Mvav 80,144,204
s_add_u32 m0, s62, 12288
X 177
Mvav 112,144,212
Ra 140,242,11264
G 15,75
Mvav 144,144,220
X 178
Mvav 148,148,220
K 194,176,177
A 8,8,12
Mvav 116,148,212
X 179
Mvav 84,148,204
X 180
Mvav 52,148,196
Dc 245,8,61,0
Ra 144,242,12288
Mvav 56,152,196
K 195,178,179
A 9,9,13
Mvav 88,152,204
X 181
Mvav 120,152,212
X 182
Mvav 152,152,220
Dc 245,8,61,1024
Ra 148,242,13312
Mvav 156,156,220
K 202,180,181
A 10,10,14
Mvav 124,156,212
X 183
Mvav 92,156,204
X 184
Mvav 60,156,196
W 2
Dc 245,8,61,2048
Ra 152,242,14336
Cvaa 160,128,0,224
A 11,11,15
K 203,182,183
Mvaa 160,132,4
X 185
Cvaa 164,132,20,228
X 186
Mvaa 164,128,16
Dc 245,8,61,3072
Ra 156,242,15360
Cvaa 168,128,32,232
s_add_u32 s61, s61, s63
G 12,76
K 210,184,185
Mvaa 168,132,36
X 187
Cvaa 172,132,52,236
s_xor_b32 s62, s62, 0x10000
G 13,77
Mvaa 172,128,48
X 188
Mvaa 160,136,8
K 211,186,187
G 14,78
Mvaa 160,140,12
s_add_u32 s64, s64, 1
X 189
Mvaa 164,140,28
Ra 128,242,24576
Ra 132,242,25600
Mvaa 164,136,24
X 190
Mvaa 168,136,40
K 218,188,189
G 15,79
Mvaa 168,140,44
X 191
Mvaa 172,140,60
s_lshl_b32 s92, s58, 2
X 160
Mvaa 172,136,56
W 2
K 219,190,191
A 8,8,12
Cvaa 176,144,0,224
X 161
Mvaa 176,148,4
Ra 136,242,26624
Ra 140,242,27648
Cvaa 180,148,20,228
s_add_u32 s91, s92, s51
X 162
Mvaa 180,144,16
A 9,9,13
K 196,160,161
Cvaa 184,144,32,232
X 163
Mvaa 184,148,36
s_add_u32 s95, s92, 0
X 164
Cvaa 188,148,52,236
A 10,10,14
K 197,162,163
Mvaa 188,144,48
X 165
Mvaa 176,152,8
s_and_b32 s95, s95, 15
Ra 144,242,28672
A 11,11,15
Mvaa 176,156,12
Ra 148,242,29696
Mvaa 180,156,28
X 166
Mvaa 180,152,24
s_mul_i32 s95, s95, 40960
K 204,164,165
Mvaa 184,152,40
X 167
Mvaa 184,156,44
G 12,80
Mvaa 188,156,60
s_mul_i32 s71, s51, 0
X 168
Mvaa 188,152,56
W 2
K 205,166,167
G 13,81
Mvav 32,128,192
X 169
Mvav 64,128,200
s_add_u32 s71, s71, 133120
Ra 152,242,30720
Ra 156,242,31744
Mvav 96,128,208
X 170
Mvav 128,128,216
G 14,82
K 212,168,169
Mvav 132,132,216
s_add_u32 m0, s71, s48
X 171
Mvav 100,132,208
X 172
Mvav 68,132,200
G 15,83
Mvav 36,132,192
s_lshl2_add_u32 s94, s48, s95
K 213,170,171
Mvav 40,136,192
X 173
Mvav 72,136,200
X 174
Mvav 104,136,208
D 16,40,94,0
A 8,8,12
Mvav 136,136,216
K 220,172,173
Mvav 140,140,216
X 175
Mvav 108,140,208
A 9,9,13
Mvav 76,140,200
s_waitcnt vmcnt(1) lgkmcnt(0)
s_barrier
v_xor_b32 v242, 0x10000, v242
Mvav 44,140,192
D 17,40,94,1024
Ra 128,242,0
Mvav 48,144,192
X 176
Mvav 80,144,200
K 221,174,175
A 10,10,14
Mvav 112,144,208
X 177
Mvav 144,144,216
D 18,40,94,2048
Ra 132,242,1024
Mvav 148,148,216
X 178
Mvav 116,148,208
K 198,176,177
A 11,11,15
Mvav 84,148,200
X 179
Mvav 52,148,192
D 19,40,94,3072
Ra 136,242,2048
Mvav 56,152,192
s_add_u32 m0, m0, 4096
X 180
Mvav 88,152,200
G 12,84
K 199,178,179
Mvav 120,152,208
X 181
Mvav 152,152,216
Ra 140,242,3072
buffer_load_dwordx4 v20, s[40:43], s94 offen lds
Mvav 156,156,216
X 182
Mvav 124,156,208
K 206,180,181
Mvav 92,156,200
X 183
Mvav 60,156,192
K 207,182,183
W 0
Cvaa 160,128,0,224
Ra 144,242,4096
Ra 148,242,5120
Mvaa 160,132,4
s_add_u32 m0, s62, 0
Ra 152,242,6144
G 13,85
Cvaa 164,132,20,228
X 184
Mvaa 164,128,16
X 185
Cvaa 168,128,32,232
X 186
Mvaa 168,132,36
G 14,86
K 214,184,185
Cvaa 172,132,52,236
Ra 156,242,7168
Dc 1,8,61,0
Mvaa 172,128,48
X 187
Mvaa 160,136,8
X 188
Mvaa 160,140,12
Ra 128,242,16384
G 15,87
Mvaa 164,140,28
Dc 1,8,61,1024
K 215,186,187
Mvaa 164,136,24
X 189
)"
R"(Mvaa 168,136,40
Ra 132,242,17408
A 8,8,12
Mvaa 168,140,44
X 190
Mvaa 172,140,60
Dc 1,8,61,2048
K 222,188,189
Mvaa 172,136,56
X 191
v_smfmac_f32_16x16x64_bf16 a[160:163], a[176:179], v[192:199], v240
A 9,9,13
K 223,190,191
v_smfmac_f32_16x16x64_bf16 a[164:167], a[176:179], v[200:207], v240
Ra 136,242,18432
A 10,10,14
v_smfmac_f32_16x16x64_bf16 a[168:171], a[176:179], v[208:215], v240
Dc 1,8,61,3072
v_smfmac_f32_16x16x64_bf16 a[172:175], a[176:179], v[216:223], v240
W 3
X 160
Cvaa 176,144,0,224
s_add_u32 m0, s62, 4096
X 161
Mvaa 176,148,4
X 162
Cvaa 180,148,20,228
Ra 140,242,19456
A 11,11,15
Mvaa 180,144,16
Dc 243,8,61,0
K 192,160,161
Cvaa 184,144,32,232
X 163
Mvaa 184,148,36
X 164
Cvaa 188,148,52,236
G 12,88
K 193,162,163
Mvaa 188,144,48
Dc 243,8,61,1024
G 13,89
Mvaa 176,152,8
X 165
Mvaa 176,156,12
X 166
Mvaa 180,156,28
Ra 144,242,20480
Ra 148,242,21504
Mvaa 180,152,24
Dc 243,8,61,2048
K 200,164,165
Mvaa 184,152,40
X 167
Mvaa 184,156,44
G 14,90
Mvaa 188,156,60
X 168
Mvaa 188,152,56
W 2
Dc 243,8,61,3072
K 201,166,167
Mvav 32,128,196
s_add_u32 m0, s62, 8192
Ra 152,242,22528
G 15,91
Mvav 64,128,204
X 169
Mvav 96,128,212
X 170
Mvav 128,128,220
Ra 156,242,23552
A 8,8,12
Mvav 132,132,220
Dc 244,8,61,0
K 208,168,169
Mvav 100,132,212
X 171
Mvav 68,132,204
X 172
Mvav 36,132,196
Ra 128,242,8192
A 9,9,13
Mvav 40,136,196
Dc 244,8,61,1024
K 209,170,171
Mvav 72,136,204
X 173
Mvav 104,136,212
Ra 132,242,9216
A 10,10,14
Mvav 136,136,220
X 174
Mvav 140,140,220
Dc 244,8,61,2048
K 216,172,173
Mvav 108,140,212
X 175
Mvav 76,140,204
Ra 136,242,10240
A 11,11,15
Mvav 44,140,196
W 3
X 176
Mvav 48,144,196
Dc 244,8,61,3072
K 217,174,175
Mvav 80,144,204
s_add_u32 m0, s62, 12288
Ra 140,242,11264
v_add_f32_dpp v8, v8, v8 row_ror:8 row_mask:0xf bank_mask:0xf
Mvav 112,144,212
X 177
Mvav 144,144,220
X 178
Mvav 148,148,220
K 194,176,177
v_add_f32_dpp v9, v9, v9 row_ror:8 row_mask:0xf bank_mask:0xf
Mvav 116,148,212
X 179
Mvav 84,148,204
X 180
Mvav 52,148,196
Dc 245,8,61,0
Ra 144,242,12288
Mvav 56,152,196
v_add_f32_dpp v10, v10, v10 row_ror:8 row_mask:0xf bank_mask:0xf
K 195,178,179
Mvav 88,152,204
X 181
Mvav 120,152,212
X 182
Mvav 152,152,220
Dc 245,8,61,1024
v_add_f32_dpp v11, v11, v11 row_ror:8 row_mask:0xf bank_mask:0xf
Mvav 156,156,220
Ra 148,242,13312
K 202,180,181
Mvav 124,156,212
X 183
Mvav 92,156,204
Ra 152,242,14336
v_mov_b32 v12, v8
Mvav 60,156,196
W 3
Dc 245,8,61,2048
v_mov_b32 v13, v9
Cvaa 160,128,0,224
X 184
Mvaa 160,132,4
Ra 156,242,15360
K 203,182,183
Cvaa 164,132,20,228
X 185
Mvaa 164,128,16
Dc 245,8,61,3072
v_mov_b32 v14, v10
Cvaa 168,128,32,232
s_add_u32 s61, s61, s63
X 186
Mvaa 168,132,36
K 210,184,185
v_mov_b32 v15, v11
Cvaa 172,132,52,236
s_xor_b32 s62, s62, 0x10000
X 187
Mvaa 172,128,48
X 188
Mvaa 160,136,8
Ra 128,242,24576
K 211,186,187
Mvaa 160,140,12
s_add_u32 s64, s64, 1
X 189
Mvaa 164,140,28
X 190
Mvaa 164,136,24
Ra 132,242,25600
v_permlane16_swap_b32 v8, v12
Mvaa 168,136,40
K 218,188,189
v_permlane16_swap_b32 v9, v13
Mvaa 168,140,44
X 191
Mvaa 172,140,60
s_lshl_b32 s92, s58, 2
X 160
Mvaa 172,136,56
W 2
K 219,190,191
v_permlane16_swap_b32 v10, v14
Cvaa 176,144,0,224
X 161
Mvaa 176,148,4
Ra 136,242,26624
Ra 140,242,27648
Cvaa 180,148,20,228
s_add_u32 s91, s92, s51
X 162
Mvaa 180,144,16
K 196,160,161
v_permlane16_swap_b32 v11, v15
Cvaa 184,144,32,232
X 163
Mvaa 184,148,36
s_add_u32 s95, s92, 1
X 164
Cvaa 188,148,52,236
A 8,8,12
K 197,162,163
Mvaa 188,144,48
X 165
Mvaa 176,152,8
s_and_b32 s95, s95, 15
Ra 144,242,28672
Ra 148,242,29696
Mvaa 176,156,12
X 166
Mvaa 180,156,28
A 9,9,13
K 204,164,165
Mvaa 180,152,24
s_mul_i32 s95, s95, 40960
X 167
Mvaa 184,152,40
Mvaa 184,156,44
A 10,10,14
Mvaa 188,156,60
s_mul_i32 s71, s51, 0
X 168
Mvaa 188,152,56
W 2
Ra 152,242,30720
K 205,166,167
Mvav 32,128,192
X 169
Mvav 64,128,200
s_add_u32 s71, s71, 143360
Ra 156,242,31744
A 11,11,15
Mvav 96,128,208
X 170
Mvav 128,128,216
K 212,168,169
v_mov_b32 v12, v8
Mvav 132,132,216
s_add_u32 m0, s71, s48
X 171
Mvav 100,132,208
X 172
Mvav 68,132,200
Mvav 36,132,192
s_lshl2_add_u32 s94, s48, s95
v_mov_b32 v13, v9
K 213,170,171
Mvav 40,136,192
X 173
Mvav 72,136,200
X 174
Mvav 104,136,208
D 16,40,94,0
v_mov_b32 v14, v10
Mvav 136,136,216
K 220,172,173
Mvav 140,140,216
X 175
Mvav 108,140,208
v_mov_b32 v15, v11
Mvav 76,140,200
s_waitcnt vmcnt(1) lgkmcnt(0)
s_barrier
v_xor_b32 v242, 0x10000, v242
Mvav 44,140,192
D 17,40,94,1024
Ra 128,242,0
Mvav 48,144,192
X 176
Mvav 80,144,200
K 221,174,175
v_permlane32_swap_b32 v8, v12
Mvav 112,144,208
X 177
Mvav 144,144,216
D 18,40,94,2048
Ra 132,242,1024
Mvav 148,148,216
X 178
Mvav 116,148,208
K 198,176,177
v_permlane32_swap_b32 v9, v13
Mvav 84,148,200
X 179
Mvav 52,148,192
D 19,40,94,3072
Ra 136,242,2048
Mvav 56,152,192
s_add_u32 m0, m0, 4096
X 180
Mvav 88,152,200
K 199,178,179
v_permlane32_swap_b32 v10, v14
Mvav 120,152,208
X 181
Mvav 152,152,216
Ra 140,242,3072
buffer_load_dwordx4 v20, s[40:43], s94 offen lds
Mvav 156,156,216
X 182
Mvav 124,156,208
K 206,180,181
Mvav 92,156,200
X 183
Mvav 60,156,192
K 207,182,183
W 0
Cvaa 160,128,0,224
Ra 144,242,4096
Ra 148,242,5120
Mvaa 160,132,4
s_add_u32 m0, s62, 0
Ra 152,242,6144
v_permlane32_swap_b32 v11, v15
Cvaa 164,132,20,228
X 184
Mvaa 164,128,16
X 185
Cvaa 168,128,32,232
X 186
Mvaa 168,132,36
A 8,8,12
K 214,184,185
Cvaa 172,132,52,236
Ra 156,242,7168
Dc 1,8,61,0
Mvaa 172,128,48
X 187
Mvaa 160,136,8
Ra 128,242,16384
A 9,9,13
Mvaa 160,140,12
X 188
Mvaa 164,140,28
Dc 1,8,61,1024
K 215,186,187
Mvaa 164,136,24
X 189
Mvaa 168,136,40
Ra 132,242,17408
A 10,10,14
Mvaa 168,140,44
X 190
Mvaa 172,140,60
Dc 1,8,61,2048
K 222,188,189
Mvaa 172,136,56
A 11,11,15
v_smfmac_f32_16x16x64_bf16 a[160:163], a[176:179], v[192:199], v240
X 191
v_smfmac_f32_16x16x64_bf16 a[164:167], a[176:179], v[200:207], v240
K 223,190,191
v_and_b32 v7, 0xffff0000, v7
v_smfmac_f32_16x16x64_bf16 a[168:171], a[176:179], v[208:215], v240
Dc 1,8,61,3072
Ra 136,242,18432
v_smfmac_f32_16x16x64_bf16 a[172:175], a[176:179], v[216:223], v240
W 3
X 160
Cvaa 176,144,0,224
s_add_u32 m0, s62, 4096
X 161
Mvaa 176,148,4
X 162
Cvaa 180,148,20,228
Ra 140,242,19456
v_ashrrev_i32 v6, 31, v7
Mvaa 180,144,16
Dc 243,8,61,0
K 192,160,161
Cvaa 184,144,32,232
X 163
Mvaa 184,148,36
v_not_b32 v6, v6
Cvaa 188,148,52,236
X 164
Mvaa 188,144,48
Dc 243,8,61,1024
K 193,162,163
Mvaa 176,152,8
X 165
Mvaa 176,156,12
Ra 144,242,20480
v_or_b32 v6, 0x80000000, v6
Mvaa 180,156,28
X 166
Mvaa 180,152,24
Ra 148,242,21504
v_xor_b32 v7, v7, v6
Mvaa 184,152,40
Dc 243,8,61,2048
K 200,164,165
)"
R"(Mvaa 184,156,44
X 167
Mvaa 188,156,60
X 168
Mvaa 188,152,56
W 2
v_and_b32 v7, 0xffff0000, v7
K 201,166,167
Mvav 32,128,196
Dc 243,8,61,3072
v_mul_f32 v7, 0xbfb8aa3b, v7
Mvav 64,128,204
s_add_u32 m0, s62, 8192
X 169
Mvav 96,128,212
Ra 152,242,22528
Ra 156,242,23552
Mvav 128,128,220
X 170
Mvav 132,132,220
Dc 244,8,61,0
K 208,168,169
Mvav 100,132,212
X 171
Mvav 68,132,204
X 7
Mvav 36,132,196
X 172
Mvav 40,136,196
Ra 128,242,8192
K 209,170,171
Mvav 72,136,204
X 173
Mvav 104,136,212
Dc 244,8,61,1024
Ra 132,242,9216
Mvav 136,136,220
X 174
Mvav 140,140,220
v_add_f32 v7, 1.0, v7
K 216,172,173
Mvav 108,140,212
X 175
Mvav 76,140,204
Dc 244,8,61,2048
Ra 136,242,10240
Mvav 44,140,196
W 3
v_rcp_f32 v7, v7
Mvav 48,144,196
X 176
Mvav 80,144,204
Ra 140,242,11264
K 217,174,175
Mvav 112,144,212
Dc 244,8,61,3072
K 7,7,7
Mvav 144,144,220
s_add_u32 m0, s62, 12288
X 177
Mvav 148,148,220
X 178
Mvav 116,148,212
Ra 144,242,12288
K 194,176,177
Mvav 84,148,204
X 179
Mvav 52,148,196
X 180
Mvav 56,152,196
Dc 245,8,61,0
v_lshlrev_b32 v7, 16, v7
Mvav 88,152,204
K 195,178,179
K 12,8,9
Mvav 120,152,212
X 181
Mvav 152,152,220
X 182
Mvav 156,156,220
Dc 245,8,61,1024
Ra 148,242,13312
Mvav 124,156,212
K 202,180,181
K 13,10,11
Mvav 92,156,204
X 183
Mvav 60,156,196
W 2
X 184
Cvaa 160,128,0,224
Dc 245,8,61,2048
Ra 152,242,14336
Mvaa 160,132,4
K 203,182,183
v_lshlrev_b32 v8, 16, v12
Cvaa 164,132,20,228
X 185
Mvaa 164,128,16
X 186
Cvaa 168,128,32,232
Dc 245,8,61,3072
Ra 156,242,15360
Mvaa 168,132,36
s_add_u32 s61, s61, s63
v_and_b32 v9, 0xffff0000, v12
K 210,184,185
Cvaa 172,132,52,236
s_xor_b32 s62, s62, 0x10000
X 187
Mvaa 172,128,48
X 188
Mvaa 160,136,8
v_lshlrev_b32 v10, 16, v13
K 211,186,187
Mvaa 160,140,12
s_add_u32 s64, s64, 1
X 189
Mvaa 164,140,28
Ra 128,242,24576
v_and_b32 v11, 0xffff0000, v13
Mvaa 164,136,24
X 190
Mvaa 168,136,40
Ra 132,242,25600
K 218,188,189
Mvaa 168,140,44
X 191
Mvaa 172,140,60
s_lshl_b32 s92, s58, 2
U 8,8,7
U 9,9,7
Mvaa 172,136,56
W 2
X 160
Cvaa 176,144,0,224
Ra 136,242,26624
K 219,190,191
Mvaa 176,148,4
X 161
Cvaa 180,148,20,228
s_add_u32 s91, s92, s51
Ra 140,242,27648
U 10,10,7
Mvaa 180,144,16
X 162
Cvaa 184,144,32,232
K 196,160,161
Mvaa 184,148,36
s_add_u32 s95, s92, 2
X 163
Cvaa 188,148,52,236
U 11,11,7
Mvaa 188,144,48
X 164
Mvaa 176,152,8
s_and_b32 s95, s95, 15
Ra 144,242,28672
K 197,162,163
Mvaa 176,156,12
X 165
Mvaa 180,156,28
Ra 148,242,29696
K 12,8,9
Mvaa 180,152,24
s_mul_i32 s95, s95, 40960
X 166
Mvaa 184,152,40
K 204,164,165
K 14,10,11
Mvaa 184,156,44
X 167
Mvaa 188,156,60
s_mul_i32 s71, s51, 0
X 168
Mvaa 188,152,56
W 2
v_mov_b32 v13, s13
K 205,166,167
Mvav 32,128,192
X 169
Mvav 64,128,200
s_add_u32 s71, s71, 153600
Ra 152,242,30720
Ra 156,242,31744
Mvav 96,128,208
X 170
Mvav 128,128,216
v_mov_b32 v15, s13
K 212,168,169
Mvav 132,132,216
s_add_u32 m0, s71, s48
X 171
Mvav 100,132,208
v_lshrrev_b32 v6, 7, v1
Mvav 68,132,200
X 172
Mvav 36,132,192
s_lshl2_add_u32 s94, s48, s95
K 213,170,171
Mvav 40,136,192
X 173
Mvav 72,136,200
v_lshl_or_b32 v6, v6, 28, v1
Mvav 104,136,208
X 174
Mvav 136,136,216
s_lshl_b32 s19, s85, 7
D 16,40,94,0
K 220,172,173
Mvav 140,140,216
X 175
Mvav 108,140,208
X 176
Mvav 76,140,200
s_waitcnt vmcnt(1) lgkmcnt(0)
s_barrier
v_xor_b32 v242, 0x10000, v242
Mvav 44,140,192
s_add_u32 s19, s19, 98304
D 17,40,94,1024
K 221,174,175
Mvav 48,144,192
Ra 128,242,0
Mvav 80,144,200
s_and_b32 s21, s20, 134217728
X 177
Mvav 112,144,208
X 178
Mvav 144,144,216
D 18,40,94,2048
Ra 132,242,1024
Mvav 148,148,216
s_or_b32 s19, s19, s21
K 198,176,177
Mvav 116,148,208
X 179
Mvav 84,148,200
X 180
Mvav 52,148,192
D 19,40,94,3072
Ra 136,242,2048
Mvav 56,152,192
s_add_u32 m0, m0, 4096
Ra 140,242,3072
K 199,178,179
Mvav 88,152,200
X 181
Mvav 120,152,208
X 182
Mvav 152,152,216
buffer_store_dwordx4 v[12:15], v6, s[8:11], s19 offen sc1
K 206,180,181
Mvav 156,156,216
X 183
Mvav 124,156,208
Mvav 92,156,200
K 207,182,183
Mvav 60,156,192
buffer_load_dwordx4 v20, s[40:43], s94 offen lds
.Lf1_40%=:
W 0
Cvaa 160,128,0,224
v_bfe_u32 v28, v1, 4, 2
v_lshlrev_b32 v28, 3, v28
Mvaa 160,132,4
Ra 144,242,4096
Ra 148,242,5120
Cvaa 164,132,20,228
Ra 152,242,6144
buffer_load_dwordx2 v[28:29], v28, s[8:11], s54 offen sc1
Mvaa 164,128,16
s_lshl_b32 s92, s58, 2
s_add_u32 s91, s92, s51
s_add_u32 s95, s91, 3
Cvaa 168,128,32,232
s_and_b32 s95, s95, 15
s_mul_i32 s95, s95, 40960
s_mul_i32 s71, s51, 10240
Mvaa 168,132,36
s_add_u32 s71, s71, 0
s_add_u32 m0, s71, s48
s_lshl2_add_u32 s94, s48, s95
Cvaa 172,132,52,236
X 184
Mvaa 172,128,48
X 185
Mvaa 160,136,8
X 186
Mvaa 160,140,12
Ra 156,242,7168
K 214,184,185
Mvaa 164,140,28
X 187
Mvaa 164,136,24
D 16,40,94,0
Ra 128,242,16384
Mvaa 168,136,40
s_add_u32 s95, s91, 5
s_and_b32 s95, s95, 15
s_mul_i32 s95, s95, 40960
Mvaa 168,140,44
s_mul_i32 s71, s51, 10240
s_add_u32 s71, s71, 20480
Ra 132,242,17408
Mvaa 172,140,60
X 188
Mvaa 172,136,56
D 17,40,94,1024
K 215,186,187
v_smfmac_f32_16x16x64_bf16 a[160:163], a[176:179], v[192:199], v240
X 189
v_smfmac_f32_16x16x64_bf16 a[164:167], a[176:179], v[200:207], v240
X 190
K 222,188,189
X 191
s_nop 0
K 223,190,191
v_smfmac_f32_16x16x64_bf16 a[168:171], a[176:179], v[208:215], v240
D 18,40,94,2048
Ra 136,242,18432
v_smfmac_f32_16x16x64_bf16 a[172:175], a[176:179], v[216:223], v240
W 3
X 160
Cvaa 176,144,0,224
X 161
Mvaa 176,148,4
X 162
Cvaa 180,148,20,228
D 19,40,94,3072
K 192,160,161
Mvaa 180,144,16
s_add_u32 m0, m0, 4096
X 163
Cvaa 184,144,32,232
X 164
Mvaa 184,148,36
Ra 140,242,19456
K 193,162,163
Cvaa 188,148,52,236
buffer_load_dwordx4 v20, s[40:43], s94 offen lds
Mvaa 188,144,48
s_add_u32 m0, s71, s48
s_lshl2_add_u32 s94, s48, s95
Ra 144,242,20480
Mvaa 176,152,8
X 165
Mvaa 176,156,12
X 166
Mvaa 180,156,28
D 16,40,94,0
K 200,164,165
Mvaa 180,152,24
X 167
Mvaa 184,152,40
X 172
Mvaa 184,156,44
D 17,40,94,1024
K 201,166,167
Mvaa 188,156,60
X 173
Mvaa 188,152,56
s_cmp_eq_u32 s60, 16
s_cbranch_scc1 .Lmk_79%=
.Lmkr_80%=:
W 1
Mvav 32,128,196
Ra 148,242,21504
D 18,40,94,2048
Mvav 64,128,204
X 174
Mvav 96,128,212
Ra 152,242,22528
K 216,172,173
Mvav 128,128,220
X 175
Mvav 132,132,220
s_mov_b32 s18, 40960
X 168
Mvav 100,132,212
D 19,40,94,3072
K 217,174,175
Mvav 68,132,204
s_add_u32 m0, m0, 4096
X 169
Mvav 36,132,196
s_mov_b32 s19, 73728
X 170
Mvav 40,136,196
s_bitcmp1_b32 s14, 0
s_cselect_b32 s18, 49152, s18
s_cselect_b32 s19, 98304, s19
Ra 156,242,23552
K 208,168,169
Mvav 72,136,204
s_cmp_lt_u32 s14, 2
s_cselect_b32 s20, s18, s19
s_cselect_b32 s21, s19, s18
X 171
Mvav 104,136,212
)"
R"(X 184
Mvav 136,136,220
buffer_load_dwordx4 v20, s[40:43], s94 offen lds
K 209,170,171
Mvav 140,140,220
s_waitcnt vmcnt(10)
v_cmp_eq_u64 vcc, s[12:13], v[28:29]
s_cmp_eq_u64 vcc, -1
s_cselect_b32 s46, 79691776, 0
s_add_u32 s81, s58, 0
s_and_b32 s81, s81, 3
Mvav 108,140,212
s_lshl_b32 s81, s81, 12
s_add_u32 s81, s81, s49
Ra 128,242,24576
Mvav 76,140,204
X 185
Mvav 44,140,196
W 1
X 186
Mvav 48,144,196
buffer_load_dwordx4 a[184:187], v1, s[44:47], s81 offen offset:0 sc1
K 210,184,185
Mvav 80,144,204
s_add_u32 s82, s81, 131072
X 187
Mvav 112,144,212
v_add_u32 v2, s20, v1
v_add_u32 v3, s21, v1
Mvav 144,144,220
s_lshl_b32 s18, s14, 9
X 188
Mvav 148,148,220
Ra 132,242,25600
K 211,186,187
Mvav 116,148,212
buffer_load_dwordx4 a[200:203], v1, s[44:47], s82 offen offset:0 sc1
Ra 136,242,26624
Mvav 84,148,204
X 189
Mvav 52,148,196
v_lshrrev_b32 v232, 1, v1
v_add_u32 v232, s18, v232
Mvav 56,152,196
X 190
Mvav 88,152,204
K 218,188,189
v_bfe_u32 v12, v1, 6, 2
Mvav 120,152,212
buffer_load_dwordx4 a[188:191], v1, s[44:47], s81 offen offset:1024 sc1
Ra 140,242,27648
Mvav 152,152,220
X 191
Mvav 156,156,220
v_lshrrev_b32 v13, 8, v1
v_bfe_u32 v14, v1, 4, 1
Mvav 124,156,212
X 176
Mvav 92,156,204
K 219,190,191
v_lshlrev_b32 v14, 4, v14
Mvav 60,156,196
X 177
Maav 168,180,208
X 178
K 194,176,177
X 179
X 180
K 195,178,179
Maav 172,180,216
X 181
X 182
K 202,180,181
X 183
s_nop 0
K 203,182,183
Maav 160,180,192
buffer_load_dwordx4 a[204:207], v1, s[44:47], s82 offen offset:1024 sc1
v_mov_b32 v15, 0x3f80
Maav 164,180,200
W 0
Ra 144,242,28672
v_lshlrev_b32 v15, v14, v15
Mvav 96,128,208
Ra 148,242,29696
v_bfe_u32 v14, v1, 5, 1
v_xor_b32 v27, v12, v13
Mvav 128,128,216
buffer_load_dwordx4 a[192:195], v1, s[44:47], s81 offen offset:2048 sc1
v_lshl_or_b32 v27, v27, 1, v14
v_cmp_eq_u32_e64 s[86:87], 0, v27
Mvav 132,132,216
Ra 152,242,30720
Ra 156,242,31744
v_mov_b32 v226, 0
Mvav 100,132,208
v_cndmask_b32_e64 v224, 0, v15, s[86:87]
v_mov_b32 v228, 0
v_mov_b32 v230, v224
Mvav 104,136,208
buffer_load_dwordx4 a[208:211], v1, s[44:47], s82 offen offset:2048 sc1
v_cmp_eq_u32_e64 s[86:87], 1, v27
v_mov_b32 v227, 0
Mvav 136,136,216
v_cndmask_b32_e64 v225, 0, v15, s[86:87]
v_mov_b32 v229, 0
v_mov_b32 v231, v225
Mvav 140,140,216
G 4,160
G 8,161
G 5,164
Mvav 108,140,208
W 0
buffer_load_dwordx4 a[196:199], v1, s[44:47], s81 offen offset:3072 sc1
G 9,165
G 6,168
Mvav 112,144,208
G 10,169
G 7,172
G 11,173
Mvav 144,144,216
A 4,4,8
A 5,5,9
A 6,6,10
Mvav 148,148,216
buffer_load_dwordx4 a[212:215], v1, s[44:47], s82 offen offset:3072 sc1
s_add_u32 s81, s58, 1
s_and_b32 s81, s81, 3
s_lshl_b32 s81, s81, 12
Mvav 116,148,208
s_add_u32 s81, s81, s49
A 7,7,11
ds_write_b64 v232, v[6:7] offset:57344
A 8,4,5
Mvav 120,152,208
A 9,6,7
A 8,8,9
v_cmp_ngt_f32_e64 s[86:87], s90, v8
s_lshl_b32 s19, s14, 2
Mvav 152,152,216
buffer_load_dwordx4 a[216:219], v1, s[44:47], s81 offen offset:0 sc1
s_add_u32 s82, s81, 131072
v_mov_b32 v9, s19
s_or_b64 s[88:89], s[88:89], s[86:87]
Mvav 156,156,216
s_and_b64 s[88:89], s[88:89], s[24:25]
s_cselect_b32 s18, 1, 0
v_mov_b32 v8, s18
ds_write_b32 v9, v8 offset:59392
v_xor_b32 v10, 1024, v232
Mvav 124,156,208
v_bfe_u32 v11, v1, 2, 4
K 96,96,97
K 97,98,99
Mvav 32,128,192
buffer_load_dwordx4 a[232:235], v1, s[44:47], s82 offen offset:0 sc1
K 98,100,101
K 99,102,103
Mvav 64,128,200
ds_write_b128 v2, v[96:99] offset:0
K 128,128,129
K 129,130,131
Mvav 68,132,200
K 130,132,133
K 131,134,135
ds_write_b128 v2, v[128:131] offset:4096
Mvav 36,132,192
buffer_load_dwordx4 a[220:223], v1, s[44:47], s81 offen offset:1024 sc1
K 104,104,105
K 105,106,107
Mvav 40,136,192
K 106,108,109
K 107,110,111
ds_write_b128 v2, v[104:107] offset:1024
Mvav 72,136,200
K 136,136,137
K 137,138,139
K 138,140,141
Mvav 76,140,200
buffer_load_dwordx4 a[236:239], v1, s[44:47], s82 offen offset:1024 sc1
K 139,142,143
ds_write_b128 v2, v[136:139] offset:5120
Mvav 44,140,192
K 112,112,113
K 113,114,115
K 114,116,117
Mvav 48,144,192
K 115,118,119
ds_write_b128 v2, v[112:115] offset:2048
K 144,144,145
Mvav 80,144,200
K 145,146,147
K 146,148,149
K 147,150,151
Mvav 84,148,200
ds_write_b128 v2, v[144:147] offset:6144
buffer_load_dwordx4 a[224:227], v1, s[44:47], s81 offen offset:2048 sc1
K 120,120,121
Mvav 52,148,192
K 121,122,123
K 122,124,125
K 123,126,127
Mvav 56,152,192
ds_write_b128 v2, v[120:123] offset:3072
K 152,152,153
K 153,154,155
Mvav 88,152,200
buffer_load_dwordx4 a[240:243], v1, s[44:47], s82 offen offset:2048 sc1
K 154,156,157
K 155,158,159
Mvav 92,156,200
ds_write_b128 v2, v[152:155] offset:7168
Mvav 60,156,192
buffer_load_dwordx4 a[228:231], v1, s[44:47], s81 offen offset:3072 sc1
buffer_load_dwordx4 a[244:247], v1, s[44:47], s82 offen offset:3072 sc1
s_branch .LL_81%=
.p2align 12
.LL_81%=:
.Lend_43%=:
s_waitcnt vmcnt(8) lgkmcnt(0)
s_barrier
s_cmp_eq_u32 s46, 0
s_cbranch_scc1 .Llate_82%=
ds_read_b32 v11, v11 offset:59392
ds_read_b64 v[8:9], v10 offset:57344
Rv 176,3,0
Rv 180,3,1024
Rv 184,3,2048
Rv 188,3,3072
Rv 192,3,4096
Rv 196,3,5120
Rv 200,3,6144
Rv 204,3,7168
W 6
Mvvv 32,224,176
s_lshl_b32 s92, s58, 2
s_add_u32 s91, s92, s51
s_add_u32 s95, s91, 7
Mvvv 36,228,176
s_and_b32 s95, s95, 15
s_mul_i32 s95, s95, 40960
s_mul_i32 s71, s51, 20480
Mvvv 40,224,180
s_add_u32 s71, s71, 61440
s_add_u32 m0, s71, s48
s_lshl2_add_u32 s94, s48, s95
Mvvv 44,228,180
W 4
v_cmp_ne_u32 vcc, 0, v11
s_cmp_lg_u64 vcc, 0
s_cbranch_scc1 .Lredo%=
A 4,4,8
Mvvv 48,224,184
v_rcp_f32 v4, v4
Mvvv 52,228,184
Ta 128,25,10240
A 5,5,9
Mvvv 56,224,188
v_rcp_f32 v5, v5
Mvvv 60,228,188
W 3
Ta 130,25,15360
D 16,40,94,0
Mvvv 64,224,192
s_add_u32 s95, s91, 9
s_and_b32 s95, s95, 15
s_mul_i32 s95, s95, 40960
Mvvv 68,228,192
s_mul_i32 s71, s51, 10240
s_add_u32 s71, s71, 112640
s_lshl_b32 s93, s58, 12
Mvvv 72,224,196
s_add_u32 s93, s93, s50
U 32,32,4
U 33,33,4
Mvvv 76,228,196
Ta 132,25,20480
Ta 134,25,25600
W 4
Mvvv 80,224,200
U 34,34,4
U 35,35,4
Mvvv 84,228,200
U 36,36,4
U 37,37,4
Mvvv 88,224,204
Ta 136,26,10272
Ta 138,26,15392
Mvvv 92,228,204
Ta 140,26,20512
Ta 142,26,25632
W 4
Zvaa 96,128,184
U 38,38,4
U 39,39,4
Mvaa 96,132,188
K 32,32,33
K 33,34,35
Zvaa 104,132,204
D 17,40,94,1024
K 34,36,37
Mvaa 104,128,200
Ta 144,25,10304
Ta 146,25,15424
Ta 148,25,20544
Ta 150,25,25664
W 4
Zvaa 108,136,200
Ta 152,26,10336
K 35,38,39
Mvaa 108,140,204
Ta 154,26,15456
Ta 156,26,20576
Zvaa 100,140,188
buffer_store_dwordx4 v[32:35], v1, s[8:11], s93 offen offset:0 sc1
U 40,40,4
Mvaa 100,136,184
Ta 158,26,25696
W 4
Zvaa 112,144,184
U 41,41,4
U 42,42,4
Mvaa 112,148,188
U 43,43,4
U 44,44,4
Zvaa 120,148,204
U 45,45,4
U 46,46,4
Mvaa 120,144,200
Ta 128,25,10368
Ta 130,25,15488
Ta 132,25,20608
Ta 134,25,25728
W 4
Zvaa 124,152,200
U 47,47,4
K 40,40,41
Mvaa 124,156,204
D 18,40,94,2048
K 41,42,43
Zvaa 116,156,188
K 42,44,45
K 43,46,47
Mvaa 116,152,184
Ta 136,26,10400
Ta 138,26,15520
Ta 140,26,20640
Ta 142,26,25760
W 4
Zvaa 128,128,184
Ta 144,25,10432
Ta 146,25,15552
Mvaa 128,132,188
buffer_store_dwordx4 v[40:43], v1, s[8:11], s93 offen offset:1024 sc1
U 48,48,4
Zvaa 136,132,204
U 49,49,4
U 50,50,4
Mvaa 136,128,200
Ta 148,25,20672
Ta 150,25,25792
W 4
Zvaa 140,136,200
U 51,51,4
U 52,52,4
Mvaa 140,140,204
U 53,53,4
U 54,54,4
Zvaa 132,140,188
U 55,55,4
K 48,48,49
Mvaa 132,136,184
Ta 152,26,10464
Ta 154,26,15584
Ta 156,26,20704
Ta 158,26,25824
W 4
Zvaa 144,144,184
D 19,40,94,3072
K 49,50,51
Mvaa 144,148,188
s_add_u32 m0, m0, 4096
K 50,52,53
K 51,54,55
Zvaa 152,148,204
Ta 128,25,10496
Ta 130,25,15616
Mvaa 152,144,200
Ta 132,25,20736
Ta 134,25,25856
W 4
Zvaa 156,152,200
buffer_store_dwordx4 v[48:51], v1, s[8:11], s93 offen offset:2048 sc1
U 56,56,4
Mvaa 156,156,204
U 57,57,4
U 58,58,4
Zvaa 148,156,188
U 59,59,4
U 60,60,4
)"
R"(Mvaa 148,152,184
Ta 136,26,10528
Ta 138,26,15648
Ta 140,26,20768
Ta 142,26,25888
W 4
Zvaa 160,128,184
U 61,61,4
U 62,62,4
Mvaa 160,132,188
U 63,63,4
K 56,56,57
Zvaa 168,132,204
buffer_load_dwordx4 v20, s[40:43], s94 offen lds
K 57,58,59
Mvaa 168,128,200
Ta 144,25,30720
Ta 146,25,35840
Ta 148,21,0
Ta 150,21,5120
W 4
Zvaa 172,136,200
s_add_u32 m0, s71, s48
s_lshl2_add_u32 s94, s48, s95
K 58,60,61
Mvaa 172,140,204
Ta 152,26,30752
K 59,62,63
Zvaa 164,140,188
buffer_store_dwordx4 v[56:59], v1, s[8:11], s93 offen offset:3072 sc1
U 64,64,5
Mvaa 164,136,184
Ta 154,26,35872
Ta 156,22,32
Ta 158,22,5152
W 4
Mvaa 96,144,192
U 65,65,5
U 66,66,5
Mvaa 96,148,196
U 67,67,5
U 68,68,5
Mvaa 104,148,212
U 69,69,5
U 70,70,5
Mvaa 104,144,208
Ta 128,25,30784
Ta 130,25,35904
Ta 132,21,64
Ta 134,21,5184
W 4
Mvaa 108,152,208
s_lshl_b32 s66, s14, 1
U 71,71,5
K 64,64,65
Mvaa 108,156,212
s_add_u32 s66, s66, 0
D 16,40,94,0
K 65,66,67
Mvaa 100,156,196
s_cmp_eq_u32 s66, s59
s_cselect_b32 s66, 134217728, 0
K 66,68,69
K 67,70,71
Mvaa 100,152,192
Ta 136,26,30816
Ta 138,26,35936
Ta 140,22,96
Ta 142,22,5216
W 4
Mvaa 112,128,192
s_add_u32 s67, s93, 131072
Ta 144,25,30848
Ta 146,25,35968
Mvaa 112,132,196
s_add_u32 s28, s52, s66
buffer_store_dwordx4 v[64:67], v1, s[8:11], s67 offen offset:0 sc1
U 72,72,5
Mvaa 120,132,212
s_lshl_b32 s66, s14, 1
U 73,73,5
U 74,74,5
Mvaa 120,128,208
Ta 148,21,128
Ta 150,21,5248
W 4
Mvaa 124,136,208
s_add_u32 s66, s66, 1
U 75,75,5
U 76,76,5
Mvaa 124,140,212
s_cmp_eq_u32 s66, s59
s_cselect_b32 s66, 134217728, 0
U 77,77,5
U 78,78,5
Mvaa 116,140,196
s_add_u32 s29, s52, s66
U 79,79,5
K 72,72,73
Mvaa 116,136,192
Ta 152,26,30880
Ta 154,26,36000
Ta 156,22,160
Ta 158,22,5280
W 4
Mvaa 128,144,192
D 17,40,94,1024
K 73,74,75
Mvaa 128,148,196
K 74,76,77
K 75,78,79
Mvaa 136,148,212
Ta 128,25,30912
Ta 130,25,36032
Mvaa 136,144,208
Ta 132,21,192
Ta 134,21,5312
W 4
Mvaa 140,152,208
D 18,40,94,2048
D 19,40,94,3072
s_add_u32 m0, m0, 4096
s_nop 0
buffer_load_dwordx4 v20, s[40:43], s94 offen lds
Mvaa 140,156,212
s_add_u32 s81, s58, 2
s_and_b32 s81, s81, 3
s_lshl_b32 s81, s81, 12
Mvaa 132,156,196
s_add_u32 s81, s81, s49
Ta 136,26,30944
Ta 138,26,36064
Mvaa 132,152,192
Ta 140,22,224
Ta 142,22,5344
W 4
Mvaa 144,128,192
buffer_store_dwordx4 v[72:75], v1, s[8:11], s67 offen offset:1024 sc1
U 80,80,5
Mvaa 144,132,196
U 81,81,5
U 82,82,5
Mvaa 152,132,212
U 83,83,5
U 84,84,5
Mvaa 152,128,208
Ta 144,25,30976
Ta 146,25,36096
Ta 148,21,256
Ta 150,21,5376
W 4
Mvaa 156,136,208
U 85,85,5
U 86,86,5
Mvaa 156,140,212
U 87,87,5
K 80,80,81
Mvaa 148,140,196
K 81,82,83
K 82,84,85
Mvaa 148,136,192
Ta 152,26,31008
Ta 154,26,36128
Ta 156,22,288
Ta 158,22,5408
W 4
Mvaa 160,144,192
buffer_load_dwordx4 a[0:3], v1, s[8:11], s81 offen offset:0 sc1
K 83,86,87
Mvaa 160,148,196
s_add_u32 s82, s81, 131072
Ta 128,21,10240
Ta 130,21,15360
Mvaa 168,148,212
Ta 132,21,20480
Ta 134,21,25600
Mvaa 168,144,208
W 4
Ta 136,22,10272
Ta 138,22,15392
Mvaa 172,152,208
buffer_store_dwordx4 v[80:83], v1, s[8:11], s67 offen offset:2048 sc1
U 88,88,5
Mvaa 172,156,212
U 89,89,5
U 90,90,5
Mvaa 164,156,196
U 91,91,5
U 92,92,5
Mvaa 164,152,192
Ta 140,22,20512
Ta 142,22,25632
s_waitcnt vmcnt(22) lgkmcnt(4)
Mvaa 96,128,216
U 93,93,5
U 94,94,5
Mvaa 96,132,220
U 95,95,5
K 88,88,89
Mvaa 104,132,236
K 89,90,91
K 90,92,93
Mvaa 104,128,232
Ta 144,21,10304
Ta 146,21,15424
Ta 148,21,20544
Ta 150,21,25664
W 4
Mvaa 108,136,232
buffer_load_dwordx4 a[16:19], v1, s[8:11], s82 offen offset:0 sc1
K 91,94,95
Mvaa 108,140,236
Ta 152,22,10336
Ta 154,22,15456
Mvaa 100,140,220
Ta 156,22,20576
Ta 158,22,25696
Mvaa 100,136,216
W 4
Ta 128,21,10368
Ta 130,21,15488
Mvaa 112,144,216
buffer_store_dwordx4 v[88:91], v1, s[8:11], s67 offen offset:3072 sc1
Ta 132,21,20608
Mvaa 112,148,220
Ta 134,21,25728
Ta 136,22,10400
Mvaa 120,148,236
Ta 138,22,15520
Ta 140,22,20640
Mvaa 120,144,232
W 7
Ta 142,22,25760
Ta 144,21,10432
Mvaa 124,152,232
buffer_load_dwordx4 a[4:7], v1, s[8:11], s81 offen offset:1024 sc1
Ta 146,21,15552
Mvaa 124,156,236
Ta 148,21,20672
Ta 150,21,25792
Mvaa 116,156,220
Mvaa 116,152,216
W 8
Ta 152,22,10464
Ta 154,22,15584
Mvaa 128,128,216
buffer_load_dwordx4 a[20:23], v1, s[8:11], s82 offen offset:1024 sc1
Mvaa 128,132,220
Ta 156,22,20704
Ta 158,22,25824
Mvaa 136,132,236
Mvaa 136,128,232
W 8
Ta 128,21,10496
Ta 130,21,15616
Mvaa 140,136,232
s_waitcnt vmcnt(7)
s_barrier
buffer_load_dwordx4 a[8:11], v1, s[8:11], s81 offen offset:2048 sc1
Mvaa 140,140,236
Ta 132,21,20736
Ta 134,21,25856
Mvaa 132,140,220
Mvaa 132,136,216
W 8
Ta 136,22,10528
Ta 138,22,15648
Mvaa 144,144,216
buffer_load_dwordx4 a[24:27], v1, s[8:11], s82 offen offset:2048 sc1
Mvaa 144,148,220
Ta 140,22,20768
Ta 142,22,25888
Mvaa 152,148,236
Mvaa 152,144,232
W 8
Ta 144,21,30720
Ta 146,21,35840
Mvaa 156,152,232
buffer_load_dwordx4 a[12:15], v1, s[8:11], s81 offen offset:3072 sc1
Mvaa 156,156,236
Ta 148,23,0
Ta 150,23,5120
Mvaa 148,156,220
Mvaa 148,152,216
W 8
Ta 152,22,30752
Ta 154,22,35872
Mvaa 160,128,216
buffer_load_dwordx4 a[28:31], v1, s[8:11], s82 offen offset:3072 sc1
Mvaa 160,132,220
s_lshl_b32 s92, s58, 2
s_add_u32 s91, s92, s51
s_add_u32 s95, s91, 11
Mvaa 168,132,236
s_and_b32 s95, s95, 15
s_mul_i32 s95, s95, 40960
s_mul_i32 s71, s51, 10240
Mvaa 168,128,232
W 6
s_add_u32 s71, s71, 40960
s_add_u32 m0, s71, s48
Mvaa 172,136,232
s_lshl2_add_u32 s94, s48, s95
D 16,40,94,0
Ta 156,24,32
Mvaa 172,140,236
s_add_u32 s95, s91, 13
s_and_b32 s95, s95, 15
s_mul_i32 s95, s95, 40960
Mvaa 164,140,220
s_mul_i32 s71, s51, 20480
s_add_u32 s71, s71, 71680
Ta 158,24,5152
Mvaa 164,136,216
W 4
Ta 128,21,30784
Ta 130,21,35904
Mvaa 96,144,224
s_lshl_b32 s80, s58, 3
D 17,40,94,1024
Ta 132,23,64
Mvaa 96,148,228
s_add_u32 s80, s80, s55
Ta 134,23,5184
Ta 136,22,30816
Mvaa 104,148,244
Ta 138,22,35936
Ta 140,24,96
Mvaa 104,144,240
W 7
Ta 142,24,5216
Ta 144,21,30848
Mvaa 108,152,240
s_waitcnt vmcnt(8)
v_mov_b64 v[28:29], s[12:13]
s_mov_b64 exec, 1
buffer_store_dwordx2 v[28:29], off, s[8:11], s80 sc1
s_mov_b64 exec, -1
Ta 146,21,35968
Mvaa 108,156,244
Ta 148,23,128
Ta 150,23,5248
Mvaa 100,156,228
Mvaa 100,152,224
W 8
Ta 152,22,30880
Ta 154,22,36000
Mvaa 112,128,224
D 18,40,94,2048
Mvaa 112,132,228
Ta 156,24,160
Ta 158,24,5280
Mvaa 120,132,244
Mvaa 120,128,240
W 8
Ta 128,21,30912
Ta 130,21,36032
Mvaa 124,136,240
D 19,40,94,3072
Mvaa 124,140,244
s_add_u32 m0, m0, 4096
Ta 132,23,192
Ta 134,23,5312
Mvaa 116,140,228
Mvaa 116,136,224
W 8
Ta 136,22,30944
Ta 138,22,36064
Mvaa 128,144,224
buffer_load_dwordx4 v20, s[40:43], s94 offen lds
Mvaa 128,148,228
s_add_u32 m0, s71, s48
s_lshl2_add_u32 s94, s48, s95
Ta 140,24,224
Mvaa 136,148,244
Ta 142,24,5344
Mvaa 136,144,240
W 8
Ta 144,21,30976
Ta 146,21,36096
Mvaa 140,152,240
D 16,40,94,0
Mvaa 140,156,244
s_add_u32 s95, s92, 15
s_and_b32 s95, s95, 15
s_mul_i32 s95, s95, 40960
Mvaa 132,156,228
s_mul_i32 s71, s51, 0
s_add_u32 s71, s71, 102400
Ta 148,23,256
Mvaa 132,152,224
W 7
Ta 150,23,5376
Ta 152,22,31008
Mvaa 144,128,224
D 17,40,94,1024
Ta 154,22,36128
Mvaa 144,132,228
Ta 156,24,288
Ta 158,24,5408
)"
R"(Mvaa 152,132,244
Mvaa 152,128,240
W 8
Ta 128,23,20480
Ta 130,23,25600
Mvaa 156,136,240
D 18,40,94,2048
Mvaa 156,140,244
Ta 132,23,51200
Ta 134,23,56320
Mvaa 148,140,228
Mvaa 148,136,224
W 8
Ta 136,24,20512
Ta 138,24,25632
Mvaa 160,144,224
D 19,40,94,3072
Mvaa 160,148,228
s_add_u32 m0, m0, 4096
Ta 140,24,51232
Ta 142,24,56352
Mvaa 168,148,244
Mvaa 168,144,240
W 8
Ta 144,23,20544
Ta 146,23,25664
Mvaa 172,152,240
buffer_load_dwordx4 v20, s[40:43], s94 offen lds
Mvaa 172,156,244
s_add_u32 m0, s71, s48
s_lshl2_add_u32 s94, s48, s95
Ta 148,23,51264
Mvaa 164,156,228
Ta 150,23,56384
Mvaa 164,152,224
s_waitcnt vmcnt(15) lgkmcnt(8)
Ta 152,24,20576
Ta 154,24,25696
Mvaa 96,128,0
D 16,40,94,0
Mvaa 96,132,4
Ta 156,24,51296
Ta 158,24,56416
Mvaa 104,132,20
Mvaa 104,128,16
W 8
Ta 128,23,20608
Ta 130,23,25728
Mvaa 108,136,16
D 17,40,94,1024
Mvaa 108,140,20
Ta 132,23,51328
Ta 134,23,56448
Mvaa 100,140,4
Mvaa 100,136,0
W 8
Ta 136,24,20640
Ta 138,24,25760
Mvaa 112,144,0
D 18,40,94,2048
Mvaa 112,148,4
Ta 140,24,51360
Ta 142,24,56480
Mvaa 120,148,20
Mvaa 120,144,16
W 8
Ta 144,23,20672
Ta 146,23,25792
Mvaa 124,152,16
D 19,40,94,3072
Mvaa 124,156,20
s_add_u32 m0, m0, 4096
Ta 148,23,51392
Ta 150,23,56512
Mvaa 116,156,4
buffer_load_dwordx4 v20, s[40:43], s94 offen lds
Mvaa 116,152,0
W 8
s_add_u32 s81, s58, 3
s_and_b32 s81, s81, 3
Mvaa 128,128,0
s_lshl_b32 s81, s81, 12
s_add_u32 s81, s81, s49
Ta 152,24,20704
Mvaa 128,132,4
Ta 154,24,25824
Ta 156,24,51424
Mvaa 136,132,20
buffer_load_dwordx4 a[32:35], v1, s[8:11], s81 offen offset:0 sc1
Ta 158,24,56544
Mvaa 136,128,16
W 8
s_add_u32 s82, s81, 131072
Ta 128,23,20736
Mvaa 140,136,16
s_waitcnt vmcnt(11)
s_barrier
Ta 130,23,25856
Mvaa 140,140,20
Ta 132,23,51456
v_and_b32 v27, 1, v12
Mvaa 132,140,4
buffer_load_dwordx4 a[48:51], v1, s[8:11], s82 offen offset:0 sc1
Ta 134,23,56576
Mvaa 132,136,0
W 8
Ta 136,24,20768
v_lshlrev_b32 v27, 1, v27
Mvaa 144,144,0
Ta 138,24,25888
v_xor_b32 v27, v27, v13
Mvaa 144,148,4
Ta 140,24,51488
Ta 142,24,56608
Mvaa 152,148,20
buffer_load_dwordx4 a[36:39], v1, s[8:11], s81 offen offset:1024 sc1
v_and_b32 v12, 2, v12
Mvaa 152,144,16
W 8
Ta 144,25,0
Ta 146,25,5120
Mvaa 156,152,16
v_or_b32 v12, v12, v14
Mvaa 156,156,20
Ta 148,21,40960
Ta 150,21,46080
Mvaa 148,156,4
buffer_load_dwordx4 a[52:55], v1, s[8:11], s82 offen offset:1024 sc1
v_lshl_or_b32 v12, v27, 2, v12
Mvaa 148,152,0
W 8
Ta 152,26,32
Ta 154,26,5152
Mvaa 160,128,0
v_cmp_eq_u32_e64 s[86:87], 0, v12
Mvaa 160,132,4
Ta 156,22,40992
Ta 158,22,46112
Mvaa 168,132,20
buffer_load_dwordx4 a[40:43], v1, s[8:11], s81 offen offset:2048 sc1
v_cndmask_b32_e64 v4, 0, v15, s[86:87]
Mvaa 168,128,16
W 8
Ta 128,25,64
Ta 130,25,5184
Mvaa 172,136,16
v_cmp_eq_u32_e64 s[86:87], 1, v12
Mvaa 172,140,20
Ta 132,21,41024
Ta 134,21,46144
Mvaa 164,140,4
buffer_load_dwordx4 a[56:59], v1, s[8:11], s82 offen offset:2048 sc1
v_cndmask_b32_e64 v5, 0, v15, s[86:87]
Mvaa 164,136,0
W 8
Ta 136,26,96
Ta 138,26,5216
Mvaa 96,144,8
v_cmp_eq_u32_e64 s[86:87], 2, v12
Mvaa 96,148,12
Ta 140,22,41056
Ta 142,22,46176
Mvaa 104,148,28
buffer_load_dwordx4 a[44:47], v1, s[8:11], s81 offen offset:3072 sc1
v_cndmask_b32_e64 v6, 0, v15, s[86:87]
Mvaa 104,144,24
W 8
Ta 144,25,128
Ta 146,25,5248
Mvaa 108,152,24
v_cmp_eq_u32_e64 s[86:87], 3, v12
Mvaa 108,156,28
Ta 148,21,41088
Ta 150,21,46208
Mvaa 100,156,12
buffer_load_dwordx4 a[60:63], v1, s[8:11], s82 offen offset:3072 sc1
v_cndmask_b32_e64 v7, 0, v15, s[86:87]
Mvaa 100,152,8
W 8
Ta 152,26,160
Ta 154,26,5280
Mvaa 112,128,8
v_cmp_eq_u32_e64 s[86:87], 4, v12
Mvaa 112,132,12
Ta 156,22,41120
Ta 158,22,46240
Mvaa 120,132,28
v_cndmask_b32_e64 v8, 0, v15, s[86:87]
Mvaa 120,128,24
W 8
Ta 128,25,192
Ta 130,25,5312
Mvaa 124,136,24
v_cmp_eq_u32_e64 s[86:87], 5, v12
v_bfe_u32 v28, v1, 4, 2
Mvaa 124,140,28
Ta 132,21,41152
v_lshlrev_b32 v28, 3, v28
Mvaa 116,140,12
Ta 134,21,46272
v_cndmask_b32_e64 v9, 0, v15, s[86:87]
Mvaa 116,136,8
W 8
buffer_load_dwordx2 v[28:29], v28, s[8:11], s55 offen sc1
Ta 136,26,224
Mvaa 128,144,8
Ta 138,26,5344
v_cmp_eq_u32_e64 s[86:87], 6, v12
Mvaa 128,148,12
Ta 140,22,41184
Ta 142,22,46304
Mvaa 136,148,28
v_cndmask_b32_e64 v10, 0, v15, s[86:87]
Mvaa 136,144,24
W 8
Ta 144,25,256
Ta 146,25,5376
Mvaa 140,152,24
s_waitcnt vmcnt(9)
s_barrier
v_cmp_eq_u32_e64 s[86:87], 7, v12
Mvaa 140,156,28
Ta 148,21,41216
Ta 150,21,46336
Mvaa 132,156,12
v_cndmask_b32_e64 v11, 0, v15, s[86:87]
Mvaa 132,152,8
W 8
Ta 152,26,288
Ta 154,26,5408
Mvaa 144,128,8
s_mov_b32 s20, s38
Mvaa 144,132,12
Ta 156,22,41248
Ta 158,22,46368
Mvaa 152,132,28
s_and_b32 s21, s39, 0xffff
Mvaa 152,128,24
W 8
Ta 128,21,51200
Ta 130,21,56320
Mvaa 156,136,24
s_mov_b32 s22, 2621440
Mvaa 156,140,28
s_mov_b32 s23, 0x20000
Ta 132,23,10240
Ta 134,23,15360
Mvaa 148,140,12
Mvaa 148,136,8
W 8
s_and_b32 s17, s17, 0xffff
Ta 136,23,30720
Mvaa 160,144,8
Ta 138,23,35840
Mvaa 160,148,12
s_mov_b32 s18, 2621440
Ta 140,23,40960
Ta 142,23,46080
Mvaa 168,148,28
Mvaa 168,144,24
W 8
s_mov_b32 s19, 0x20000
Ta 144,22,51232
Mvaa 172,152,24
Ta 146,22,56352
Mvaa 172,156,28
s_lshr_b32 s66, s15, 3
Ta 148,24,10272
Ta 150,24,15392
Mvaa 164,156,12
Mvaa 164,152,8
s_waitcnt vmcnt(5) lgkmcnt(8)
s_lshl_b32 s66, s66, 4
Ta 152,24,30752
Mvaa 96,128,32
Ta 154,24,35872
Mvaa 96,132,36
s_add_u32 s66, s66, s59
Ta 156,24,40992
Ta 158,24,46112
Mvaa 104,132,52
Mvaa 104,128,48
s_waitcnt vmcnt(1) lgkmcnt(8)
s_lshl_b32 s63, s66, 6
Ta 128,21,51264
Mvaa 104,136,56
Ta 130,21,56384
Mvaa 104,140,60
s_add_u32 s63, s63, 65536
Ta 132,23,10304
Ta 134,23,15424
Mvaa 96,140,44
Mvaa 96,136,40
W 8
s_mul_i32 s62, s66, 40960
Ta 136,23,30784
Mvaa 100,144,32
Ta 138,23,35904
Mvaa 100,148,36
s_add_u32 s62, s62, 56623104
Ta 140,23,41024
Ta 142,23,46144
Mvaa 108,148,52
Mvaa 108,144,48
W 8
s_lshl_b32 s66, s14, 13
Ta 144,22,51296
Mvaa 108,152,56
Ta 146,22,56416
Mvaa 108,156,60
s_add_u32 s78, s62, s66
Ta 148,24,10336
Ta 150,24,15456
Mvaa 100,156,44
s_waitcnt vmcnt(0)
v_cmp_eq_u64 vcc, s[12:13], v[28:29]
s_cmp_eq_u64 vcc, -1
s_cbranch_scc0 .Lspin_84%=
.Lok_85%=:
s_add_u32 s81, s58, 1
s_and_b32 s81, s81, 3
Mvaa 100,152,40
W 8
s_lshl_b32 s81, s81, 12
s_add_u32 s81, s81, s50
Mvaa 112,128,32
s_add_u32 s79, s78, 327680
buffer_load_dwordx4 a[64:67], v1, s[8:11], s81 offen offset:0 sc1
Ta 152,24,30816
Mvaa 112,132,36
s_add_u32 s82, s81, 131072
s_and_b32 s66, s14, 1
Ta 154,24,35936
Mvaa 120,132,52
Ta 156,24,41056
Ta 158,24,46176
Mvaa 120,128,48
W 8
K 96,96,97
K 97,98,99
Mvaa 120,136,56
s_mul_i32 s66, s66, 327680
Ta 128,21,51328
Ta 130,21,56448
Mvaa 120,140,60
s_add_u32 s65, s62, s66
buffer_load_dwordx4 a[80:83], v1, s[8:11], s82 offen offset:0 sc1
K 98,100,101
Mvaa 112,140,44
Ta 132,23,10368
Ta 134,23,15488
Mvaa 112,136,40
W 8
s_add_u32 s65, s65, 32768
K 99,102,103
Mvaa 116,144,32
s_add_u32 s83, s28, 0
Ta 136,23,30848
Ta 138,23,35968
Mvaa 116,148,36
s_mov_b32 s64, 0
buffer_load_dwordx4 a[68:71], v1, s[8:11], s81 offen offset:1024 sc1
Ta 140,23,41088
Mvaa 124,148,52
Ta 142,23,46208
Mvaa 124,144,48
W 8
Ta 144,22,51360
v_add_u32 v19, 4096, v1
Mvaa 124,152,56
Ta 146,22,56480
Mvaa 124,156,60
buffer_store_dwordx4 v[96:99], v1, s[8:11], s83 offen sc1
K 104,104,105
Mvaa 116,156,44
K 105,106,107
v_bfe_u32 v27, v1, 4, 3
Mvaa 116,152,40
W 6
Ta 148,24,10400
Ta 150,24,15520
Mvaa 128,128,32
K 106,108,109
v_lshlrev_b32 v27, 3, v27
Mvaa 128,132,36
buffer_load_dwordx4 a[84:87], v1, s[8:11], s82 offen offset:1024 sc1
Ta 152,24,30880
Mvaa 136,132,52
Ta 154,24,36000
K 107,110,111
Mvaa 136,128,48
W 6
Ta 156,24,41120
v_bfe_u32 v18, v1, 7, 1
Mvaa 136,136,56
s_add_u32 s83, s29, 40960
s_add_u32 s66, s14, 1
Ta 158,24,46240
Mvaa 136,140,60
s_lshr_b32 s66, s66, 2
)"
R"(buffer_load_dwordx4 a[72:75], v1, s[8:11], s81 offen offset:2048 sc1
Ta 128,21,51392
Mvaa 128,140,44
s_mul_i32 s66, s66, 0
Ta 130,21,56512
Ta 132,23,10432
Mvaa 128,136,40
W 7
Ta 134,23,15552
Ta 136,23,30912
Mvaa 132,144,32
s_sub_u32 s66, 512, s66
Ta 138,23,36032
Mvaa 132,148,36
buffer_store_dwordx4 v[104:107], v1, s[8:11], s83 offen sc1
K 112,112,113
Mvaa 140,148,52
K 113,114,115
K 114,116,117
Mvaa 140,144,48
W 6
Ta 140,23,41152
Ta 142,23,46272
Mvaa 140,152,56
v_mul_u32_u24 v18, s66, v18
K 115,118,119
Mvaa 140,156,60
s_add_u32 s83, s28, 8192
buffer_load_dwordx4 a[88:91], v1, s[8:11], s82 offen offset:2048 sc1
Ta 144,22,51424
Mvaa 132,156,44
Ta 146,22,56544
v_add_u32 v18, v18, v27
Mvaa 132,152,40
W 6
Ta 148,24,10464
Ta 150,24,15584
Mvaa 144,128,32
s_mov_b32 s71, 0
Ta 152,24,30944
Ta 154,24,36064
Mvaa 144,132,36
buffer_load_dwordx4 a[76:79], v1, s[8:11], s81 offen offset:3072 sc1
v_bfe_u32 v20, v1, 4, 4
Mvaa 152,132,52
Ta 156,24,41184
Ta 158,24,46304
Mvaa 152,128,48
W 8
Ta 128,21,51456
v_mul_u32_u24 v20, 1280, v20
Mvaa 152,136,56
Ta 130,21,56576
v_lshrrev_b32 v12, 8, v1
Mvaa 152,140,60
buffer_store_dwordx4 v[112:115], v1, s[8:11], s83 offen sc1
K 120,120,121
Mvaa 144,140,44
K 121,122,123
K 122,124,125
Mvaa 144,136,40
W 6
Ta 132,23,10496
K 123,126,127
Mvaa 148,144,32
s_add_u32 s83, s29, 49152
Ta 134,23,15616
v_lshl_add_u32 v20, v12, 4, v20
Mvaa 148,148,36
s_bfe_u32 s66, s59, 0x10001
buffer_load_dwordx4 a[92:95], v1, s[8:11], s82 offen offset:3072 sc1
Ta 136,23,30976
Mvaa 156,148,52
s_add_u32 s81, s58, 2
s_and_b32 s81, s81, 3
s_lshl_b32 s81, s81, 12
Mvaa 156,144,48
W 5
s_add_u32 s81, s81, s50
Ta 138,23,36096
Mvaa 156,152,56
s_lshl_b32 s67, s57, 1
Ta 140,23,41216
Ta 142,23,46336
Mvaa 156,156,60
s_add_u32 s66, s66, s67
buffer_load_dwordx4 a[96:99], v1, s[8:11], s81 offen offset:0 sc1
Ta 144,22,51488
Mvaa 148,156,44
s_add_u32 s82, s81, 131072
Ta 146,22,56608
Ta 148,24,10528
Mvaa 148,152,40
W 7
s_lshl_b32 s61, s66, 2
Ta 150,24,15648
Mvaa 160,128,32
s_lshr_b32 s66, s59, 2
Ta 152,24,31008
Ta 154,24,36128
Mvaa 160,132,36
buffer_store_dwordx4 v[120:123], v1, s[8:11], s83 offen sc1
K 128,128,129
Mvaa 168,132,52
s_lshl_b32 s66, s66, 1
K 129,130,131
K 130,132,133
Mvaa 168,128,48
W 6
Ta 156,24,41248
K 131,134,135
Mvaa 168,136,56
s_add_u32 s83, s28, 16384
Ta 158,24,46368
Ta 128,25,10240
Mvaa 168,140,60
s_add_u32 s61, s61, s66
buffer_load_dwordx4 a[112:115], v1, s[8:11], s82 offen offset:0 sc1
Ta 130,25,15360
Mvaa 160,140,44
s_and_b32 s66, s59, 1
Ta 132,25,20480
Ta 134,25,25600
Mvaa 160,136,40
W 8
Ta 136,26,10272
Ta 138,26,15392
Mvaa 164,144,32
s_add_u32 s61, s61, s66
Mvaa 164,148,36
buffer_load_dwordx4 a[100:103], v1, s[8:11], s81 offen offset:1024 sc1
Ta 140,26,20512
Mvaa 172,148,52
s_xor_b32 s67, s61, 0
Ta 142,26,25632
Mvaa 172,144,48
W 8
Ta 144,25,10304
Ta 146,25,15424
Mvaa 172,152,56
s_mul_i32 s72, s67, 20480
Mvaa 172,156,60
buffer_store_dwordx4 v[128:131], v1, s[8:11], s83 offen sc1
K 136,136,137
Mvaa 164,156,44
s_mul_i32 s66, s58, 320
K 137,138,139
K 138,140,141
Mvaa 164,152,40
W 6
Ta 148,25,20544
K 139,142,143
Zvav 176,128,32
s_add_u32 s83, s29, 57344
Ta 150,25,25664
Ta 152,26,10336
Mvav 176,132,40
s_add_u32 s72, s72, s66
buffer_load_dwordx4 a[116:119], v1, s[8:11], s82 offen offset:1024 sc1
Ta 154,26,15456
Zvav 184,132,72
s_mul_i32 s75, s67, 20480
Ta 156,26,20576
Ta 158,26,25696
Mvav 184,128,64
W 8
Ta 128,25,10368
Ta 130,25,15488
Zvav 188,136,64
s_mul_i32 s66, s58, 2560
Mvav 188,140,72
buffer_load_dwordx4 a[104:107], v1, s[8:11], s81 offen offset:2048 sc1
Ta 132,25,20608
Zvav 180,140,40
s_add_u32 s75, s75, s66
Ta 134,25,25728
Mvav 180,136,32
W 8
s_add_u32 s75, s75, 131072
Ta 136,26,10400
Zvav 192,144,32
Ta 138,26,15520
Mvav 192,148,40
s_xor_b32 s67, s61, 124
buffer_store_dwordx4 v[136:139], v1, s[8:11], s83 offen sc1
K 144,144,145
Zvav 200,148,72
K 145,146,147
K 146,148,149
Mvav 200,144,64
W 6
Ta 140,26,20640
K 147,150,151
Zvav 204,152,64
s_add_u32 s83, s28, 24576
s_mul_i32 s73, s67, 20480
Ta 142,26,25760
Mvav 204,156,72
s_mul_i32 s66, s58, 320
buffer_load_dwordx4 a[120:123], v1, s[8:11], s82 offen offset:2048 sc1
Ta 144,25,10432
Zvav 196,156,40
Ta 146,25,15552
Ta 148,25,20672
Mvav 196,152,32
W 7
s_add_u32 s73, s73, s66
Ta 150,25,25792
Zvav 208,128,32
Ta 152,26,10464
Ta 154,26,15584
Mvav 208,132,40
s_mul_i32 s76, s67, 20480
buffer_load_dwordx4 a[108:111], v1, s[8:11], s81 offen offset:3072 sc1
Ta 156,26,20704
Zvav 216,132,72
Ta 158,26,25824
Mvav 216,128,64
W 8
s_mul_i32 s66, s58, 2560
Ta 128,25,10496
Zvav 220,136,64
Ta 130,25,15616
Mvav 220,140,72
s_add_u32 s76, s76, s66
buffer_store_dwordx4 v[144:147], v1, s[8:11], s83 offen sc1
K 152,152,153
Zvav 212,140,40
K 153,154,155
K 154,156,157
Mvav 212,136,32
W 6
Ta 132,25,20736
K 155,158,159
Zvav 224,144,32
s_add_u32 s83, s29, 65536
s_add_u32 s76, s76, 131072
Ta 134,25,25856
Mvav 224,148,40
buffer_load_dwordx4 a[124:127], v1, s[8:11], s82 offen offset:3072 sc1
Ta 136,26,10528
Zvav 232,148,72
s_add_u32 s81, s58, 3
s_and_b32 s81, s81, 3
s_lshl_b32 s81, s81, 12
Mvav 232,144,64
W 5
s_add_u32 s81, s81, s50
Ta 138,26,15648
Zvav 236,152,64
Ta 140,26,20768
v_bfe_u32 v15, v1, 7, 1
Mvav 236,156,72
buffer_load_dwordx4 a[184:187], v1, s[8:11], s81 offen offset:0 sc1
Ta 142,26,25888
Zvav 228,156,40
s_add_u32 s82, s81, 131072
Ta 144,25,30720
v_mul_u32_u24 v14, 10240, v15
Mvav 228,152,32
W 5
Ta 146,25,35840
v_lshrrev_b32 v15, 8, v1
Zvav 240,128,32
Ta 148,21,0
Ta 150,21,5120
Mvav 240,132,40
buffer_store_dwordx4 v[152:155], v1, s[8:11], s83 offen sc1
K 160,160,161
Zvav 248,132,72
K 161,162,163
K 162,164,165
Mvav 248,128,64
W 4
K 163,166,167
v_lshl_add_u32 v14, v15, 7, v14
Zvav 252,136,64
s_add_u32 s83, s28, 32768
Ta 152,26,30752
Ta 154,26,35872
Mvav 252,140,72
buffer_load_dwordx4 a[200:203], v1, s[8:11], s82 offen offset:0 sc1
v_and_b32 v15, 0x70, v1
Zvav 244,140,40
Ta 156,22,32
Ta 158,22,5152
Mvav 244,136,32
W 4
Ta 128,25,30784
v_add_u32 v14, v14, v15
Mvav 176,144,48
s_bitcmp1_b32 s14, 0
s_cselect_b32 s74, s73, s72
s_cselect_b32 s77, s76, s75
Ta 130,25,35904
Ta 132,21,64
Mvav 176,148,56
s_add_u32 s74, s74, 256
buffer_load_dwordx4 a[188:191], v1, s[8:11], s81 offen offset:1024 sc1
Ta 134,21,5184
Mvav 184,148,88
s_add_u32 s77, s77, 2048
Ta 136,26,30816
Ta 138,26,35936
Mvav 184,144,80
W 6
Ta 140,22,96
Ta 142,22,5216
Mvav 188,152,80
s_lshl_b32 s66, s14, 6
Ta 144,25,30848
Ta 146,25,35968
Mvav 188,156,88
s_lshl_b32 s67, s14, 9
buffer_store_dwordx4 v[160:163], v1, s[8:11], s83 offen sc1
K 168,168,169
Mvav 180,156,56
K 169,170,171
K 170,172,173
Mvav 180,152,48
W 6
Ta 148,21,128
K 171,174,175
Mvav 192,128,48
s_add_u32 s83, s29, 73728
s_add_u32 s72, s72, s66
Ta 150,21,5248
Mvav 192,132,56
s_add_u32 s75, s75, s67
buffer_load_dwordx4 a[204:207], v1, s[8:11], s82 offen offset:1024 sc1
Ta 152,26,30880
Mvav 200,132,88
Ta 154,26,36000
Ta 156,22,160
Mvav 200,128,80
W 7
s_add_u32 s73, s73, s66
Ta 158,22,5280
Mvav 204,136,80
Ta 128,25,30912
Ta 130,25,36032
Mvav 204,140,88
s_add_u32 s76, s76, s67
buffer_load_dwordx4 a[192:195], v1, s[8:11], s81 offen offset:2048 sc1
Ta 132,21,192
Mvav 196,140,56
Ta 134,21,5312
Mvav 196,136,48
W 8
Ta 136,26,30944
Ta 138,26,36064
Mvav 208,144,48
Mvav 208,148,56
buffer_store_dwordx4 v[168:171], v1, s[8:11], s83 offen sc1
Ta 140,22,224
Mvav 216,148,88
Ta 142,22,5344
Mvav 216,144,80
W 8
Ta 144,25,30976
Ta 146,25,36096
Mvav 220,152,80
Mvav 220,156,88
buffer_load_dwordx4 a[208:211], v1, s[8:11], s82 offen offset:2048 sc1
Ta 148,21,256
Mvav 212,156,56
Ta 150,21,5376
Mvav 212,152,48
W 8
Ta 152,26,31008
Ta 154,26,36128
Mvav 224,128,48
Mvav 224,132,56
buffer_load_dwordx4 a[196:199], v1, s[8:11], s81 offen offset:3072 sc1
Ta 156,22,288
Mvav 232,132,88
Ta 158,22,5408
Mvav 232,128,80
W 8
Ta 128,21,10240
Ta 130,21,15360
Mvav 236,136,80
Mvav 236,140,88
buffer_load_dwordx4 a[212:215], v1, s[8:11], s82 offen offset:3072 sc1
Ta 132,21,20480
Mvav 228,140,56
Ta 134,21,25600
Mvav 228,136,48
W 8
Ta 136,22,10272
Ta 138,22,15392
Mvav 240,144,48
Mvav 240,148,56
buffer_load_dwordx4 v[36:39], v20, s[16:19], s72 offen
Ta 140,22,20512
Mvav 248,148,88
Ta 142,22,25632
Mvav 248,144,80
W 8
Ta 144,21,10304
)"
R"(Ta 146,21,15424
Mvav 252,152,80
Mvav 252,156,88
buffer_load_dwordx4 v[44:47], v14, s[8:11], s75 offen sc1
Ta 148,21,20544
Mvav 244,156,56
Ta 150,21,25664
Mvav 244,152,48
s_waitcnt vmcnt(31) lgkmcnt(8)
Ta 152,22,10336
Ta 154,22,15456
Mvaa 176,128,64
Mvaa 176,132,68
buffer_load_dwordx4 v[52:55], v20, s[16:19], s73 offen
Ta 156,22,20576
Mvaa 184,132,84
Ta 158,22,25696
Mvaa 184,128,80
W 8
Ta 128,21,10368
Ta 130,21,15488
Mvaa 188,136,80
Mvaa 188,140,84
buffer_load_dwordx4 v[60:63], v14, s[8:11], s76 offen sc1
Ta 132,21,20608
Mvaa 180,140,68
Ta 134,21,25728
Mvaa 180,136,64
W 8
Ta 136,22,10400
Ta 138,22,15520
Mvaa 192,144,64
Mvaa 192,148,68
buffer_load_dwordx4 v[68:71], v20, s[16:19], s74 offen
Ta 140,22,20640
Mvaa 200,148,84
Ta 142,22,25760
Mvaa 200,144,80
W 8
Ta 144,21,10432
Ta 146,21,15552
Mvaa 204,152,80
Mvaa 204,156,84
buffer_load_dwordx4 v[76:79], v14, s[8:11], s77 offen sc1
Ta 148,21,20672
Mvaa 196,156,68
Ta 150,21,25792
Mvaa 196,152,64
W 8
Ta 152,22,10464
Ta 154,22,15584
Mvaa 208,128,64
Mvaa 208,132,68
buffer_load_dwordx2 v[12:13], v27, s[8:11], 0 offen offset:3584 sc1
Ta 156,22,20704
Mvaa 216,132,84
Ta 158,22,25824
Mvaa 216,128,80
W 8
Ta 128,21,10496
Ta 130,21,15616
Mvaa 220,136,80
Mvaa 220,140,84
s_waitcnt vmcnt(10)
v_lshlrev_b32 v2, 2, v1
v_mov_b64 v[28:29], s[12:13]
s_mov_b64 exec, 3
buffer_store_dwordx2 v[28:29], v2, s[8:11], s53 offen offset:0 sc1
s_mov_b64 exec, -1
Ta 132,21,20736
Ta 134,21,25856
Mvaa 212,140,68
Mvaa 212,136,64
W 8
Ta 136,22,10528
Ta 138,22,15648
Mvaa 224,144,64
Mvaa 224,148,68
Ta 140,22,20768
Ta 142,22,25888
Mvaa 232,148,84
Mvaa 232,144,80
W 8
Ta 144,21,30720
Ta 146,21,35840
Mvaa 236,152,80
Mvaa 236,156,84
Ta 148,23,0
Ta 150,23,5120
Mvaa 228,156,68
Mvaa 228,152,64
W 8
Ta 152,22,30752
Ta 154,22,35872
Mvaa 240,128,64
Mvaa 240,132,68
Ta 156,24,32
Ta 158,24,5152
Mvaa 248,132,84
Mvaa 248,128,80
W 8
Ta 128,21,30784
Ta 130,21,35904
Mvaa 252,136,80
Mvaa 252,140,84
Ta 132,23,64
Ta 134,23,5184
Mvaa 244,140,68
Mvaa 244,136,64
W 8
Ta 136,22,30816
Ta 138,22,35936
Mvaa 176,144,72
Mvaa 176,148,76
Ta 140,24,96
Ta 142,24,5216
Mvaa 184,148,92
Mvaa 184,144,88
W 8
Ta 144,21,30848
Ta 146,21,35968
Mvaa 188,152,88
Mvaa 188,156,92
Ta 148,23,128
Ta 150,23,5248
Mvaa 180,156,76
Mvaa 180,152,72
W 8
Ta 152,22,30880
Ta 154,22,36000
Mvaa 192,128,72
Mvaa 192,132,76
Ta 156,24,160
Ta 158,24,5280
Mvaa 200,132,92
Mvaa 200,128,88
W 8
Ta 128,21,30912
Ta 130,21,36032
Mvaa 204,136,88
Mvaa 204,140,92
Ta 132,23,192
Ta 134,23,5312
Mvaa 196,140,76
Mvaa 196,136,72
W 8
Ta 136,22,30944
Ta 138,22,36064
Mvaa 208,144,72
Mvaa 208,148,76
Ta 140,24,224
Ta 142,24,5344
Mvaa 216,148,92
Mvaa 216,144,88
W 8
Ta 144,21,30976
Ta 146,21,36096
Mvaa 220,152,88
Mvaa 220,156,92
Ta 148,23,256
Ta 150,23,5376
Mvaa 212,156,76
Mvaa 212,152,72
W 8
Ta 152,22,31008
Ta 154,22,36128
Mvaa 224,128,72
Mvaa 224,132,76
Ta 156,24,288
Ta 158,24,5408
Mvaa 232,132,92
Mvaa 232,128,88
W 8
Ta 128,23,20480
Ta 130,23,25600
Mvaa 236,136,88
Mvaa 236,140,92
Ta 132,23,51200
Ta 134,23,56320
Mvaa 228,140,76
Mvaa 228,136,72
W 8
Ta 136,25,0
Ta 138,25,5120
Mvaa 240,144,72
Mvaa 240,148,76
Ta 140,21,40960
Ta 142,21,46080
Mvaa 248,148,92
s_waitcnt vmcnt(1)
v_xor_b32 v12, s12, v12
v_xor_b32 v13, s13, v13
v_lshrrev_b32 v3, 11, v12
v_or_b32 v13, v13, v3
v_cmp_eq_u32 vcc, 0, v13
s_cmp_eq_u64 vcc, -1
s_cbranch_scc0 .LL_89%=
.LL_90%=:
v_bfe_u32 v13, v12, 4, 7
s_xor_b32 s66, s61, 124
v_cmp_eq_u32 vcc, s61, v13
v_cmp_eq_u32_e64 s[86:87], s66, v13
s_or_b32 s68, vcc_lo, s86
s_and_b32 s68, s68, 255
s_cbranch_scc1 .Lup_87%=
.Lupr_88%=:
Mvaa 248,144,88
W 8
Ta 144,21,51200
Ta 146,21,56320
Mvaa 252,152,88
v_lshlrev_b32 v32, 16, v36
Mvaa 252,156,92
Ta 148,23,10240
v_and_b32 v33, 0xffff0000, v36
Mvaa 244,156,76
Ta 150,23,15360
v_lshlrev_b32 v34, 16, v37
Mvaa 244,152,72
W 8
Ta 152,23,30720
v_and_b32 v35, 0xffff0000, v37
Mvaa 176,128,96
Ta 154,23,35840
v_lshlrev_b32 v36, 16, v38
Mvaa 176,132,100
buffer_load_dwordx2 v[16:17], v18, s[8:11], s63 offen sc1
v_and_b32 v37, 0xffff0000, v38
Mvaa 184,132,116
Ta 156,23,40960
Ta 158,23,46080
Mvaa 184,128,112
W 8
v_lshlrev_b32 v38, 16, v39
v_and_b32 v39, 0xffff0000, v39
Mvaa 184,136,120
Ta 128,24,20512
Ta 130,24,25632
Mvaa 184,140,124
v_lshlrev_b32 v40, 16, v44
v_and_b32 v41, 0xffff0000, v44
Mvaa 176,140,108
s_barrier
Ta 132,24,51232
Ta 134,24,56352
Mvaa 176,136,104
W 8
v_lshlrev_b32 v42, 16, v45
v_and_b32 v43, 0xffff0000, v45
Mvaa 176,144,184
Ta 136,26,32
Ta 138,26,5152
Mvaa 176,148,188
v_lshlrev_b32 v44, 16, v46
v_and_b32 v45, 0xffff0000, v46
Mvaa 184,148,204
Ta 140,22,40992
Ta 142,22,46112
Mvaa 184,144,200
W 8
v_lshlrev_b32 v46, 16, v47
v_and_b32 v47, 0xffff0000, v47
Mvaa 184,152,208
Ta 144,22,51232
Ta 146,22,56352
Mvaa 184,156,212
v_lshlrev_b32 v48, 16, v52
v_and_b32 v49, 0xffff0000, v52
Mvaa 176,156,196
Ta 148,24,10272
Ta 150,24,15392
Mvaa 176,152,192
W 8
v_lshlrev_b32 v50, 16, v53
v_and_b32 v51, 0xffff0000, v53
Mvaa 180,128,96
Ta 152,24,30752
Ta 154,24,35872
Mvaa 180,132,100
v_lshlrev_b32 v52, 16, v54
v_and_b32 v53, 0xffff0000, v54
Mvaa 188,132,116
Ta 156,24,40992
Ta 158,24,46112
Mvaa 188,128,112
W 8
v_lshlrev_b32 v54, 16, v55
v_and_b32 v55, 0xffff0000, v55
Mvaa 188,136,120
Ta 128,23,20544
Ta 130,23,25664
Mvaa 188,140,124
v_lshlrev_b32 v56, 16, v60
v_and_b32 v57, 0xffff0000, v60
Mvaa 180,140,108
Ta 132,23,51264
Ta 134,23,56384
Mvaa 180,136,104
W 8
v_lshlrev_b32 v58, 16, v61
v_and_b32 v59, 0xffff0000, v61
Mvaa 180,144,184
Ta 136,25,64
Ta 138,25,5184
Mvaa 180,148,188
v_lshlrev_b32 v60, 16, v62
v_and_b32 v61, 0xffff0000, v62
Mvaa 188,148,204
Ta 140,21,41024
Ta 142,21,46144
Mvaa 188,144,200
W 8
v_lshlrev_b32 v62, 16, v63
v_and_b32 v63, 0xffff0000, v63
Mvaa 188,152,208
Ta 144,21,51264
Ta 146,21,56384
Mvaa 188,156,212
v_lshlrev_b32 v64, 16, v68
v_and_b32 v65, 0xffff0000, v68
Mvaa 180,156,196
Ta 148,23,10304
Ta 150,23,15424
Mvaa 180,152,192
W 8
v_lshlrev_b32 v66, 16, v69
v_and_b32 v67, 0xffff0000, v69
Mvaa 192,128,96
Ta 152,23,30784
Ta 154,23,35904
Mvaa 192,132,100
v_lshlrev_b32 v68, 16, v70
v_and_b32 v69, 0xffff0000, v70
Mvaa 200,132,116
Ta 156,23,41024
Ta 158,23,46144
Mvaa 200,128,112
W 8
v_lshlrev_b32 v70, 16, v71
K 176,176,177
Mvaa 200,136,120
Ta 128,24,20576
v_and_b32 v71, 0xffff0000, v71
Mvaa 200,140,124
Ta 130,24,25696
K 177,178,179
Mvaa 192,140,108
v_lshlrev_b32 v72, 16, v76
K 178,180,181
Mvaa 192,136,104
W 6
Ta 132,24,51296
v_and_b32 v73, 0xffff0000, v76
Mvaa 192,144,184
Ta 134,24,56416
v_lshlrev_b32 v74, 16, v77
Mvaa 192,148,188
K 179,182,183
v_and_b32 v75, 0xffff0000, v77
Mvaa 200,148,204
s_add_u32 s83, s28, 327680
Ta 136,26,96
Ta 138,26,5216
Mvaa 200,144,200
W 6
buffer_store_dwordx4 v[176:179], v1, s[8:11], s83 offen sc1
v_lshlrev_b32 v76, 16, v78
Mvaa 200,152,208
s_waitcnt vmcnt(1)
v_cmp_eq_u64 vcc, s[12:13], v[16:17]
s_and_b32 s71, vcc_lo, 255
s_bitset0_b32 s71, s59
K 184,184,185
K 185,186,187
Mvaa 200,156,212
s_lshl_b32 s92, s14, 3
s_lshr_b32 s92, s71, s92
s_mov_b32 s91, 134217728
Mvaa 192,156,196
K 186,188,189
K 187,190,191
s_add_u32 s83, s29, 368640
buffer_store_dwordx4 v[184:187], v1, s[8:11], s83 offen sc1
Mvaa 192,152,192
Ta 140,22,41056
Ta 142,22,46176
W 4
Mvaa 196,128,96
s_bitcmp1_b32 s71, 0
s_cselect_b32 s94, s78, s91
v_and_b32 v77, 0xffff0000, v78
Mvaa 196,132,100
v_lshlrev_b32 v78, 16, v79
v_and_b32 v79, 0xffff0000, v79
Mvaa 204,132,116
Ta 144,22,51296
Ta 146,22,56416
Mvaa 204,128,112
Ta 148,24,10336
Ta 150,24,15456
)"
R"(W 4
Mvaa 204,136,120
Ta 152,24,30816
Ta 154,24,35936
Mvaa 204,140,124
buffer_load_dwordx4 a[0:3], v1, s[8:11], s94 offen offset:0 sc1
Ta 156,24,41056
Mvaa 196,140,108
Ta 158,24,46176
Ta 128,23,20608
Mvaa 196,136,104
W 5
Ta 130,23,25728
Ta 132,23,51328
Mvaa 196,144,184
Ta 134,23,56448
Ta 136,25,128
Mvaa 196,148,188
s_bitcmp1_b32 s92, 0
s_cbranch_scc0 .LL_92%=
buffer_load_dwordx4 a[216:219], v1, s[8:11], s65 offen offset:0 sc1
.LL_92%=:
Ta 138,25,5248
Mvaa 204,148,204
s_bitcmp1_b32 s71, 1
s_cselect_b32 s94, s78, s91
Ta 140,21,41088
Mvaa 204,144,200
W 7
Ta 142,21,46208
Ta 144,21,51328
Mvaa 204,152,208
Ta 146,21,56448
Mvaa 204,156,212
Ta 148,23,10368
Ta 150,23,15488
Mvaa 196,156,196
buffer_load_dwordx4 a[4:7], v1, s[8:11], s94 offen offset:1024 sc1
Mvaa 196,152,192
W 8
Ta 152,23,30848
Ta 154,23,35968
Mvaa 208,128,96
Mvaa 208,132,100
Ta 156,23,41088
Ta 158,23,46208
Mvaa 216,132,116
K 192,192,193
Mvaa 216,128,112
W 8
Ta 128,24,20640
Ta 130,24,25760
Mvaa 216,136,120
K 193,194,195
Mvaa 216,140,124
s_bitcmp1_b32 s92, 1
s_cbranch_scc0 .LL_93%=
buffer_load_dwordx4 a[220:223], v1, s[8:11], s65 offen offset:1024 sc1
.LL_93%=:
K 194,196,197
Mvaa 208,140,108
s_bitcmp1_b32 s71, 2
s_cselect_b32 s94, s78, s91
Ta 132,24,51360
Mvaa 208,136,104
W 7
Ta 134,24,56480
K 195,198,199
Mvaa 208,144,184
s_add_u32 s83, s28, 335872
Ta 136,26,160
Ta 138,26,5280
Mvaa 208,148,188
buffer_store_dwordx4 v[192:195], v1, s[8:11], s83 offen sc1
K 200,200,201
Mvaa 216,148,204
K 201,202,203
K 202,204,205
Mvaa 216,144,200
W 6
Ta 140,22,41120
K 203,206,207
Mvaa 216,152,208
s_add_u32 s83, s29, 376832
Ta 142,22,46240
Ta 144,22,51360
Mvaa 216,156,212
buffer_store_dwordx4 v[200:203], v1, s[8:11], s83 offen sc1
Ta 146,22,56480
Mvaa 208,156,196
Ta 148,24,10400
Ta 150,24,15520
Mvaa 208,152,192
W 8
Ta 152,24,30880
Ta 154,24,36000
Mvaa 212,128,96
Mvaa 212,132,100
buffer_load_dwordx4 a[8:11], v1, s[8:11], s94 offen offset:2048 sc1
Ta 156,24,41120
Mvaa 220,132,116
Ta 158,24,46240
Mvaa 220,128,112
W 8
Ta 128,23,20672
Ta 130,23,25792
Mvaa 220,136,120
Mvaa 220,140,124
s_bitcmp1_b32 s92, 2
s_cbranch_scc0 .LL_94%=
buffer_load_dwordx4 a[224:227], v1, s[8:11], s65 offen offset:2048 sc1
.LL_94%=:
Ta 132,23,51392
Mvaa 212,140,108
s_bitcmp1_b32 s71, 3
s_cselect_b32 s94, s78, s91
Ta 134,23,56512
Mvaa 212,136,104
W 8
Ta 136,25,192
Ta 138,25,5312
Mvaa 212,144,184
Mvaa 212,148,188
Ta 140,21,41152
Ta 142,21,46272
Mvaa 220,148,204
buffer_load_dwordx4 a[12:15], v1, s[8:11], s94 offen offset:3072 sc1
Mvaa 220,144,200
W 8
Ta 144,21,51392
Ta 146,21,56512
Mvaa 220,152,208
Mvaa 220,156,212
Ta 148,23,10432
Ta 150,23,15552
Mvaa 212,156,196
Mvaa 212,152,192
W 8
Ta 152,23,30912
Ta 154,23,36032
Mvaa 224,128,96
Mvaa 224,132,100
s_bitcmp1_b32 s92, 3
s_cbranch_scc0 .LL_95%=
buffer_load_dwordx4 a[228:231], v1, s[8:11], s65 offen offset:3072 sc1
.LL_95%=:
Ta 156,23,41152
Mvaa 232,132,116
s_bitcmp1_b32 s71, 4
s_cselect_b32 s94, s78, s91
Ta 158,23,46272
Mvaa 232,128,112
W 8
Ta 128,24,20704
K 208,208,209
Mvaa 232,136,120
Ta 130,24,25824
K 209,210,211
Mvaa 232,140,124
Ta 132,24,51424
K 210,212,213
Mvaa 224,140,108
Ta 134,24,56544
K 211,214,215
Mvaa 224,136,104
W 8
Ta 136,26,224
Ta 138,26,5344
Mvaa 224,144,184
s_add_u32 s83, s28, 344064
buffer_load_dwordx4 a[16:19], v19, s[8:11], s94 offen offset:0 sc1
Mvaa 224,148,188
Ta 140,22,41184
Ta 142,22,46304
Mvaa 232,148,204
Mvaa 232,144,200
W 8
Ta 144,22,51424
Ta 146,22,56544
Mvaa 232,152,208
buffer_store_dwordx4 v[208:211], v1, s[8:11], s83 offen sc1
K 216,216,217
Mvaa 232,156,212
K 217,218,219
K 218,220,221
Mvaa 224,156,196
K 219,222,223
s_add_u32 s83, s29, 385024
buffer_store_dwordx4 v[216:219], v1, s[8:11], s83 offen sc1
Mvaa 224,152,192
W 6
Ta 148,24,10464
Ta 150,24,15584
Mvaa 228,128,96
Ta 152,24,30944
Ta 154,24,36064
Mvaa 228,132,100
Ta 156,24,41184
Ta 158,24,46304
Mvaa 236,132,116
s_bitcmp1_b32 s92, 4
s_cbranch_scc0 .LL_96%=
buffer_load_dwordx4 a[232:235], v19, s[8:11], s65 offen offset:0 sc1
.LL_96%=:
Mvaa 236,128,112
W 8
s_bitcmp1_b32 s71, 5
s_cselect_b32 s94, s78, s91
Mvaa 236,136,120
Ta 128,23,20736
Ta 130,23,25856
Mvaa 236,140,124
Ta 132,23,51456
Ta 134,23,56576
Mvaa 228,140,108
buffer_load_dwordx4 a[20:23], v19, s[8:11], s94 offen offset:1024 sc1
Mvaa 228,136,104
W 8
Ta 136,25,256
Ta 138,25,5376
Mvaa 228,144,184
Mvaa 228,148,188
Ta 140,21,41216
Ta 142,21,46336
Mvaa 236,148,204
Mvaa 236,144,200
W 8
Ta 144,21,51456
Ta 146,21,56576
Mvaa 236,152,208
Mvaa 236,156,212
s_bitcmp1_b32 s92, 5
s_cbranch_scc0 .LL_97%=
buffer_load_dwordx4 a[236:239], v19, s[8:11], s65 offen offset:1024 sc1
.LL_97%=:
Ta 148,23,10496
Mvaa 228,156,196
s_bitcmp1_b32 s71, 6
s_cselect_b32 s94, s78, s91
Ta 150,23,15616
Mvaa 228,152,192
W 8
Ta 152,23,30976
Ta 154,23,36096
Mvaa 240,128,96
Mvaa 240,132,100
Ta 156,23,41216
Ta 158,23,46336
Mvaa 248,132,116
K 224,224,225
Mvaa 248,128,112
W 8
Ta 128,24,20768
Ta 130,24,25888
Mvaa 248,136,120
buffer_load_dwordx4 a[24:27], v19, s[8:11], s94 offen offset:2048 sc1
K 225,226,227
Mvaa 248,140,124
Ta 132,24,51488
K 226,228,229
Mvaa 240,140,108
Ta 134,24,56608
K 227,230,231
Mvaa 240,136,104
W 8
Ta 136,26,288
Ta 138,26,5408
Mvaa 240,144,184
s_add_u32 s83, s28, 352256
Mvaa 240,148,188
buffer_store_dwordx4 v[224:227], v1, s[8:11], s83 offen sc1
K 232,232,233
Mvaa 248,148,204
K 233,234,235
K 234,236,237
Mvaa 248,144,200
W 6
Ta 140,22,41248
K 235,238,239
Mvaa 248,152,208
s_add_u32 s83, s29, 393216
Ta 142,22,46368
Ta 144,22,51488
Mvaa 248,156,212
buffer_store_dwordx4 v[232:235], v1, s[8:11], s83 offen sc1
Ta 146,22,56608
Mvaa 240,156,196
Ta 148,24,10528
Ta 150,24,15648
Mvaa 240,152,192
W 8
Ta 152,24,31008
Ta 154,24,36128
Mvaa 244,128,96
Mvaa 244,132,100
s_bitcmp1_b32 s92, 6
s_cbranch_scc0 .LL_98%=
buffer_load_dwordx4 a[240:243], v19, s[8:11], s65 offen offset:2048 sc1
.LL_98%=:
Ta 156,24,41248
Mvaa 252,132,116
s_bitcmp1_b32 s71, 7
s_cselect_b32 s94, s78, s91
Ta 158,24,46368
Mvaa 252,128,112
W 8
Mvaa 252,136,120
Mvaa 252,140,124
buffer_load_dwordx4 a[28:31], v19, s[8:11], s94 offen offset:3072 sc1
Mvaa 244,140,108
Mvaa 244,136,104
W 4
Mvaa 244,144,184
Mvaa 244,148,188
s_bitcmp1_b32 s92, 7
s_cbranch_scc0 .LL_99%=
buffer_load_dwordx4 a[244:247], v19, s[8:11], s65 offen offset:3072 sc1
.LL_99%=:
Mvaa 252,148,204
Mvaa 252,144,200
W 0
Mvaa 252,152,208
Mvaa 252,156,212
Mvaa 244,156,196
Mvaa 244,152,192
K 240,240,241
K 241,242,243
s_nop 5
K 242,244,245
K 243,246,247
s_add_u32 s83, s28, 360448
buffer_store_dwordx4 v[240:243], v1, s[8:11], s83 offen sc1
K 248,248,249
K 249,250,251
K 250,252,253
K 251,254,255
s_add_u32 s83, s29, 401408
buffer_store_dwordx4 v[248:251], v1, s[8:11], s83 offen sc1
s_branch .Ltend_83%=
.p2align 12
.Llate_82%=:
ds_read_b32 v11, v11 offset:59392
ds_read_b64 v[8:9], v10 offset:57344
Rv 176,3,0
Rv 180,3,1024
Rv 184,3,2048
Rv 188,3,3072
Rv 192,3,4096
Rv 196,3,5120
Rv 200,3,6144
Rv 204,3,7168
W 6
Mvvv 32,224,176
s_lshl_b32 s92, s58, 2
s_add_u32 s91, s92, s51
s_add_u32 s95, s91, 7
Mvvv 36,228,176
s_and_b32 s95, s95, 15
s_mul_i32 s95, s95, 40960
s_mul_i32 s71, s51, 20480
Mvvv 40,224,180
s_add_u32 s71, s71, 61440
s_add_u32 m0, s71, s48
s_lshl2_add_u32 s94, s48, s95
Mvvv 44,228,180
W 4
v_cmp_ne_u32 vcc, 0, v11
s_cmp_lg_u64 vcc, 0
s_cbranch_scc1 .Lredo%=
A 4,4,8
Mvvv 48,224,184
v_rcp_f32 v4, v4
Mvvv 52,228,184
Ta 128,25,10240
A 5,5,9
Mvvv 56,224,188
v_rcp_f32 v5, v5
Mvvv 60,228,188
W 3
Ta 130,25,15360
D 16,40,94,0
Mvvv 64,224,192
s_add_u32 s95, s91, 9
s_and_b32 s95, s95, 15
s_mul_i32 s95, s95, 40960
Mvvv 68,228,192
s_mul_i32 s71, s51, 10240
s_add_u32 s71, s71, 112640
s_lshl_b32 s93, s58, 12
Mvvv 72,224,196
s_add_u32 s93, s93, s50
U 32,32,4
U 33,33,4
Mvvv 76,228,196
Ta 132,25,20480
Ta 134,25,25600
W 4
Mvvv 80,224,200
U 34,34,4
U 35,35,4
)"
R"(Mvvv 84,228,200
U 36,36,4
U 37,37,4
Mvvv 88,224,204
U 38,38,4
U 39,39,4
K 32,32,33
K 33,34,35
K 34,36,37
K 35,38,39
buffer_store_dwordx4 v[32:35], v1, s[8:11], s93 offen offset:0 sc1
U 40,40,4
U 41,41,4
U 42,42,4
U 43,43,4
U 44,44,4
U 45,45,4
U 46,46,4
U 47,47,4
K 40,40,41
K 41,42,43
K 42,44,45
K 43,46,47
buffer_store_dwordx4 v[40:43], v1, s[8:11], s93 offen offset:1024 sc1
U 64,64,5
U 65,65,5
U 66,66,5
U 67,67,5
U 68,68,5
U 69,69,5
U 70,70,5
U 71,71,5
K 64,64,65
K 65,66,67
K 66,68,69
K 67,70,71
s_add_u32 s67, s93, 131072
buffer_store_dwordx4 v[64:67], v1, s[8:11], s67 offen offset:0 sc1
U 72,72,5
U 73,73,5
U 74,74,5
U 75,75,5
U 76,76,5
U 77,77,5
U 78,78,5
U 79,79,5
K 72,72,73
K 73,74,75
K 74,76,77
K 75,78,79
Mvvv 92,228,204
Ta 136,26,10272
Ta 138,26,15392
Ta 140,26,20512
Ta 142,26,25632
W 4
Zvav 176,128,32
Ta 144,25,10304
Ta 146,25,15424
Mvav 176,132,40
Ta 148,25,20544
Ta 150,25,25664
Zvav 184,132,72
D 17,40,94,1024
Ta 152,26,10336
Mvav 184,128,64
W 5
Ta 154,26,15456
Ta 156,26,20576
Zvav 188,136,64
Ta 158,26,25696
Ta 128,25,10368
Mvav 188,140,72
Ta 130,25,15488
Ta 132,25,20608
Zvav 180,140,40
D 18,40,94,2048
Ta 134,25,25728
Mvav 180,136,32
W 8
Ta 136,26,10400
Ta 138,26,15520
Zvav 192,144,32
Mvav 192,148,40
Ta 140,26,20640
Ta 142,26,25760
Zvav 200,148,72
D 19,40,94,3072
Mvav 200,144,64
W 8
s_add_u32 m0, m0, 4096
Ta 144,25,10432
Zvav 204,152,64
Ta 146,25,15552
Mvav 204,156,72
Ta 148,25,20672
Ta 150,25,25792
Zvav 196,156,40
buffer_load_dwordx4 v20, s[40:43], s94 offen lds
Mvav 196,152,32
W 8
s_add_u32 m0, s71, s48
s_lshl2_add_u32 s94, s48, s95
Zvav 208,128,32
Ta 152,26,10464
Ta 154,26,15584
Mvav 208,132,40
Ta 156,26,20704
Ta 158,26,25824
Zvav 216,132,72
D 16,40,94,0
v_bfe_u32 v28, v1, 4, 2
Mvav 216,128,64
W 8
Ta 128,25,10496
v_lshlrev_b32 v28, 3, v28
Zvav 220,136,64
Ta 130,25,15616
Mvav 220,140,72
Ta 132,25,20736
Ta 134,25,25856
Zvav 212,140,40
buffer_load_dwordx2 v[28:29], v28, s[8:11], s54 offen sc1
Mvav 212,136,32
W 8
Ta 136,26,10528
Ta 138,26,15648
Zvav 224,144,32
Mvav 224,148,40
Ta 140,26,20768
Ta 142,26,25888
Zvav 232,148,72
D 17,40,94,1024
Mvav 232,144,64
W 8
Ta 144,25,30720
Ta 146,25,35840
Zvav 236,152,64
Mvav 236,156,72
Ta 148,21,0
Ta 150,21,5120
Zvav 228,156,40
buffer_store_dwordx4 v[72:75], v1, s[8:11], s67 offen offset:1024 sc1
U 48,48,4
Mvav 228,152,32
W 8
U 49,49,4
U 50,50,4
Zvav 240,128,32
U 51,51,4
U 52,52,4
Mvav 240,132,40
U 53,53,4
U 54,54,4
Zvav 248,132,72
D 18,40,94,2048
U 55,55,4
Mvav 248,128,64
W 4
K 48,48,49
K 49,50,51
Zvav 252,136,64
K 50,52,53
K 51,54,55
Mvav 252,140,72
Ta 152,26,30752
Ta 154,26,35872
Zvav 244,140,40
buffer_store_dwordx4 v[48:51], v1, s[8:11], s93 offen offset:2048 sc1
U 56,56,4
U 57,57,4
U 58,58,4
U 59,59,4
U 60,60,4
U 61,61,4
U 62,62,4
U 63,63,4
K 56,56,57
K 57,58,59
K 58,60,61
K 59,62,63
buffer_store_dwordx4 v[56:59], v1, s[8:11], s93 offen offset:3072 sc1
U 80,80,5
U 81,81,5
U 82,82,5
U 83,83,5
U 84,84,5
U 85,85,5
U 86,86,5
U 87,87,5
K 80,80,81
K 81,82,83
K 82,84,85
K 83,86,87
buffer_store_dwordx4 v[80:83], v1, s[8:11], s67 offen offset:2048 sc1
U 88,88,5
U 89,89,5
U 90,90,5
U 91,91,5
U 92,92,5
U 93,93,5
U 94,94,5
U 95,95,5
K 88,88,89
K 89,90,91
K 90,92,93
K 91,94,95
Mvav 244,136,32
Ta 156,22,32
Ta 158,22,5152
W 4
Mvav 176,144,48
Ta 128,25,30784
Ta 130,25,35904
Mvav 176,148,56
Ta 132,21,64
Ta 134,21,5184
Mvav 184,148,88
D 19,40,94,3072
Ta 136,26,30816
Mvav 184,144,80
s_add_u32 m0, m0, 4096
s_nop 0
buffer_load_dwordx4 v20, s[40:43], s94 offen lds
s_waitcnt vmcnt(8)
v_cmp_eq_u64 vcc, s[12:13], v[28:29]
s_cmp_eq_u64 vcc, -1
s_cbranch_scc0 .Lspin_100%=
.Lok_101%=:
W 5
Mvav 188,152,80
s_add_u32 s81, s58, 0
s_and_b32 s81, s81, 3
s_lshl_b32 s81, s81, 12
Mvav 188,156,88
s_add_u32 s81, s81, s49
Ta 138,26,35936
Ta 140,22,96
Mvav 180,156,56
s_lshl_b32 s66, s14, 1
Ta 142,22,5216
Ta 144,25,30848
Mvav 180,152,48
W 5
buffer_store_dwordx4 v[88:91], v1, s[8:11], s67 offen offset:3072 sc1
Ta 146,25,35968
Mvav 192,128,48
s_add_u32 s66, s66, 0
Ta 148,21,128
Ta 150,21,5248
Mvav 192,132,56
s_cmp_eq_u32 s66, s59
s_cselect_b32 s66, 134217728, 0
Ta 152,26,30880
Ta 154,26,36000
Mvav 200,132,88
s_add_u32 s28, s52, s66
Ta 156,22,160
Ta 158,22,5280
Mvav 200,128,80
W 8
s_lshl_b32 s66, s14, 1
buffer_load_dwordx4 a[184:187], v1, s[8:11], s81 offen offset:0 sc1
Mvav 204,136,80
s_add_u32 s82, s81, 131072
Ta 128,25,30912
Ta 130,25,36032
Mvav 204,140,88
s_add_u32 s66, s66, 1
s_cmp_eq_u32 s66, s59
s_cselect_b32 s66, 134217728, 0
Ta 132,21,192
Mvav 196,140,56
s_add_u32 s29, s52, s66
Ta 134,21,5312
Mvav 196,136,48
W 8
buffer_load_dwordx4 a[200:203], v1, s[8:11], s82 offen offset:0 sc1
Ta 136,26,30944
Mvav 208,144,48
Ta 138,26,36064
Mvav 208,148,56
Ta 140,22,224
Ta 142,22,5344
Mvav 216,148,88
Mvav 216,144,80
W 8
buffer_load_dwordx4 a[188:191], v1, s[8:11], s81 offen offset:1024 sc1
Ta 144,25,30976
Mvav 220,152,80
Ta 146,25,36096
Mvav 220,156,88
Ta 148,21,256
Ta 150,21,5376
Mvav 212,156,56
Mvav 212,152,48
W 8
buffer_load_dwordx4 a[204:207], v1, s[8:11], s82 offen offset:1024 sc1
Ta 152,26,31008
Mvav 224,128,48
Ta 154,26,36128
Mvav 224,132,56
Ta 156,22,288
Ta 158,22,5408
Mvav 232,132,88
Mvav 232,128,80
W 8
buffer_load_dwordx4 a[192:195], v1, s[8:11], s81 offen offset:2048 sc1
Ta 128,25,10240
Mvav 236,136,80
Ta 130,25,15360
Mvav 236,140,88
Ta 132,25,20480
Ta 134,25,25600
Mvav 228,140,56
Mvav 228,136,48
W 8
buffer_load_dwordx4 a[208:211], v1, s[8:11], s82 offen offset:2048 sc1
Ta 136,26,10272
Mvav 240,144,48
Ta 138,26,15392
Mvav 240,148,56
Ta 140,26,20512
Ta 142,26,25632
Mvav 248,148,88
Mvav 248,144,80
W 8
buffer_load_dwordx4 a[196:199], v1, s[8:11], s81 offen offset:3072 sc1
Ta 144,25,10304
Mvav 252,152,80
Ta 146,25,15424
Mvav 252,156,88
Ta 148,25,20544
Ta 150,25,25664
Mvav 244,156,56
Mvav 244,152,48
s_waitcnt vmcnt(3) lgkmcnt(8)
buffer_load_dwordx4 a[212:215], v1, s[8:11], s82 offen offset:3072 sc1
Ta 152,26,10336
Zvaa 96,128,184
Ta 154,26,15456
Mvaa 96,132,188
Ta 156,26,20576
Ta 158,26,25696
Zvaa 104,132,204
Mvaa 104,128,200
W 8
Ta 128,25,10368
Ta 130,25,15488
Zvaa 108,136,200
Mvaa 108,140,204
Ta 132,25,20608
Ta 134,25,25728
Zvaa 100,140,188
s_lshl_b32 s92, s58, 2
s_add_u32 s91, s92, s51
s_add_u32 s95, s91, 11
Mvaa 100,136,184
W 8
s_and_b32 s95, s95, 15
s_mul_i32 s95, s95, 40960
Zvaa 112,144,184
s_mul_i32 s71, s51, 10240
s_add_u32 s71, s71, 40960
s_add_u32 m0, s71, s48
Mvaa 112,148,188
s_lshl2_add_u32 s94, s48, s95
Ta 136,26,10400
Ta 138,26,15520
Zvaa 120,148,204
Ta 140,26,20640
Ta 142,26,25760
Mvaa 120,144,200
W 8
Ta 144,25,10432
Ta 146,25,15552
Zvaa 124,152,200
Mvaa 124,156,204
Ta 148,25,20672
Ta 150,25,25792
Zvaa 116,156,188
Mvaa 116,152,184
W 8
Ta 152,26,10464
Ta 154,26,15584
Zvaa 128,128,184
s_lshl_b32 s80, s58, 3
s_add_u32 s80, s80, s55
v_mov_b64 v[28:29], s[12:13]
s_mov_b64 exec, 1
buffer_store_dwordx2 v[28:29], off, s[8:11], s80 sc1
s_mov_b64 exec, -1
Mvaa 128,132,188
Ta 156,26,20704
Ta 158,26,25824
Zvaa 136,132,204
Mvaa 136,128,200
W 8
Ta 128,25,10496
Ta 130,25,15616
)"
R"(Zvaa 140,136,200
s_barrier
D 16,40,94,0
Mvaa 140,140,204
s_add_u32 s95, s91, 13
s_and_b32 s95, s95, 15
s_mul_i32 s95, s95, 40960
Zvaa 132,140,188
s_mul_i32 s71, s51, 20480
s_add_u32 s71, s71, 71680
Ta 132,25,20736
Mvaa 132,136,184
W 7
Ta 134,25,25856
Ta 136,26,10528
Zvaa 144,144,184
Ta 138,26,15648
D 17,40,94,1024
Mvaa 144,148,188
Ta 140,26,20768
Ta 142,26,25888
Zvaa 152,148,204
Mvaa 152,144,200
W 8
Ta 144,25,30720
Ta 146,25,35840
Zvaa 156,152,200
D 18,40,94,2048
Mvaa 156,156,204
Ta 148,21,0
Ta 150,21,5120
Zvaa 148,156,188
Mvaa 148,152,184
W 8
Ta 152,26,30752
Ta 154,26,35872
Zvaa 160,128,184
D 19,40,94,3072
Mvaa 160,132,188
s_add_u32 m0, m0, 4096
Ta 156,22,32
Ta 158,22,5152
Zvaa 168,132,204
Mvaa 168,128,200
W 8
Ta 128,25,30784
Ta 130,25,35904
Zvaa 172,136,200
buffer_load_dwordx4 v20, s[40:43], s94 offen lds
Mvaa 172,140,204
s_add_u32 s81, s58, 1
s_and_b32 s81, s81, 3
s_lshl_b32 s81, s81, 12
Zvaa 164,140,188
s_add_u32 s81, s81, s49
Ta 132,21,64
Ta 134,21,5184
Mvaa 164,136,184
s_waitcnt vmcnt(6) lgkmcnt(8)
Ta 136,26,30816
Ta 138,26,35936
Mvaa 96,144,192
buffer_load_dwordx4 a[216:219], v1, s[8:11], s81 offen offset:0 sc1
Mvaa 96,148,196
s_add_u32 s82, s81, 131072
Ta 140,22,96
Ta 142,22,5216
Mvaa 104,148,212
Mvaa 104,144,208
W 8
Ta 144,25,30848
Ta 146,25,35968
Mvaa 108,152,208
buffer_load_dwordx4 a[232:235], v1, s[8:11], s82 offen offset:0 sc1
Mvaa 108,156,212
Ta 148,21,128
Ta 150,21,5248
Mvaa 100,156,196
Mvaa 100,152,192
W 8
Ta 152,26,30880
Ta 154,26,36000
Mvaa 112,128,192
buffer_load_dwordx4 a[220:223], v1, s[8:11], s81 offen offset:1024 sc1
Mvaa 112,132,196
Ta 156,22,160
Ta 158,22,5280
Mvaa 120,132,212
Mvaa 120,128,208
W 8
Ta 128,25,30912
Ta 130,25,36032
Mvaa 124,136,208
buffer_load_dwordx4 a[236:239], v1, s[8:11], s82 offen offset:1024 sc1
Mvaa 124,140,212
Ta 132,21,192
Ta 134,21,5312
Mvaa 116,140,196
Mvaa 116,136,192
W 8
Ta 136,26,30944
Ta 138,26,36064
Mvaa 128,144,192
buffer_load_dwordx4 a[224:227], v1, s[8:11], s81 offen offset:2048 sc1
Mvaa 128,148,196
Ta 140,22,224
Ta 142,22,5344
Mvaa 136,148,212
Mvaa 136,144,208
W 8
Ta 144,25,30976
Ta 146,25,36096
Mvaa 140,152,208
buffer_load_dwordx4 a[240:243], v1, s[8:11], s82 offen offset:2048 sc1
Mvaa 140,156,212
Ta 148,21,256
Ta 150,21,5376
Mvaa 132,156,196
Mvaa 132,152,192
W 8
Ta 152,26,31008
Ta 154,26,36128
Mvaa 144,128,192
buffer_load_dwordx4 a[228:231], v1, s[8:11], s81 offen offset:3072 sc1
Mvaa 144,132,196
Ta 156,22,288
Ta 158,22,5408
Mvaa 152,132,212
Mvaa 152,128,208
W 8
Ta 128,21,10240
Ta 130,21,15360
Mvaa 156,136,208
buffer_load_dwordx4 a[244:247], v1, s[8:11], s82 offen offset:3072 sc1
Mvaa 156,140,212
s_add_u32 m0, s71, s48
s_lshl2_add_u32 s94, s48, s95
Ta 132,21,20480
Mvaa 148,140,196
Ta 134,21,25600
Mvaa 148,136,192
W 8
Ta 136,22,10272
Ta 138,22,15392
Mvaa 160,144,192
D 16,40,94,0
Mvaa 160,148,196
s_add_u32 s95, s92, 15
s_and_b32 s95, s95, 15
s_mul_i32 s95, s95, 40960
Mvaa 168,148,212
s_mul_i32 s71, s51, 0
s_add_u32 s71, s71, 102400
Ta 140,22,20512
Mvaa 168,144,208
W 7
Ta 142,22,25632
Ta 144,21,10304
Mvaa 172,152,208
D 17,40,94,1024
Ta 146,21,15424
Mvaa 172,156,212
Ta 148,21,20544
Ta 150,21,25664
Mvaa 164,156,196
Mvaa 164,152,192
s_waitcnt vmcnt(6) lgkmcnt(8)
Ta 152,22,10336
Ta 154,22,15456
Mvaa 96,128,216
D 18,40,94,2048
Mvaa 96,132,220
Ta 156,22,20576
Ta 158,22,25696
Mvaa 104,132,236
Mvaa 104,128,232
W 8
Ta 128,21,10368
Ta 130,21,15488
Mvaa 108,136,232
D 19,40,94,3072
Mvaa 108,140,236
s_add_u32 m0, m0, 4096
Ta 132,21,20608
Ta 134,21,25728
Mvaa 100,140,220
Mvaa 100,136,216
W 8
Ta 136,22,10400
Ta 138,22,15520
Mvaa 112,144,216
buffer_load_dwordx4 v20, s[40:43], s94 offen lds
Mvaa 112,148,220
s_add_u32 m0, s71, s48
s_lshl2_add_u32 s94, s48, s95
Ta 140,22,20640
Mvaa 120,148,236
Ta 142,22,25760
Mvaa 120,144,232
W 8
Ta 144,21,10432
Ta 146,21,15552
Mvaa 124,152,232
D 16,40,94,0
Mvaa 124,156,236
Ta 148,21,20672
Ta 150,21,25792
Mvaa 116,156,220
D 17,40,94,1024
D 18,40,94,2048
D 19,40,94,3072
s_add_u32 m0, m0, 4096
s_nop 0
buffer_load_dwordx4 v20, s[40:43], s94 offen lds
Mvaa 116,152,216
W 8
s_add_u32 s81, s58, 2
s_and_b32 s81, s81, 3
Mvaa 128,128,216
s_lshl_b32 s81, s81, 12
s_add_u32 s81, s81, s49
Ta 152,22,10464
Mvaa 128,132,220
Ta 154,22,15584
Ta 156,22,20704
Mvaa 136,132,236
Ta 158,22,25824
Mvaa 136,128,232
W 8
Ta 128,21,10496
Ta 130,21,15616
Mvaa 140,136,232
s_barrier
buffer_load_dwordx4 a[0:3], v1, s[8:11], s81 offen offset:0 sc1
v_and_b32 v27, 1, v12
Mvaa 140,140,236
s_add_u32 s82, s81, 131072
Ta 132,21,20736
Ta 134,21,25856
Mvaa 132,140,220
Mvaa 132,136,216
W 8
Ta 136,22,10528
v_lshlrev_b32 v27, 1, v27
Mvaa 144,144,216
Ta 138,22,15648
buffer_load_dwordx4 a[16:19], v1, s[8:11], s82 offen offset:0 sc1
Mvaa 144,148,220
Ta 140,22,20768
v_xor_b32 v27, v27, v13
Mvaa 152,148,236
Ta 142,22,25888
Mvaa 152,144,232
W 8
Ta 144,21,30720
v_and_b32 v12, 2, v12
Mvaa 156,152,232
Ta 146,21,35840
buffer_load_dwordx4 a[4:7], v1, s[8:11], s81 offen offset:1024 sc1
Mvaa 156,156,236
Ta 148,23,0
Ta 150,23,5120
Mvaa 148,156,220
v_or_b32 v12, v12, v14
Mvaa 148,152,216
W 8
Ta 152,22,30752
Ta 154,22,35872
Mvaa 160,128,216
buffer_load_dwordx4 a[20:23], v1, s[8:11], s82 offen offset:1024 sc1
v_lshl_or_b32 v12, v27, 2, v12
Mvaa 160,132,220
Ta 156,24,32
Ta 158,24,5152
Mvaa 168,132,236
v_cmp_eq_u32_e64 s[86:87], 0, v12
Mvaa 168,128,232
W 8
Ta 128,21,30784
Ta 130,21,35904
Mvaa 172,136,232
buffer_load_dwordx4 a[8:11], v1, s[8:11], s81 offen offset:2048 sc1
Mvaa 172,140,236
Ta 132,23,64
v_cndmask_b32_e64 v4, 0, v15, s[86:87]
Mvaa 164,140,220
Ta 134,23,5184
Mvaa 164,136,216
s_waitcnt vmcnt(15) lgkmcnt(8)
Ta 136,22,30816
v_cmp_eq_u32_e64 s[86:87], 1, v12
Mvaa 96,144,224
Ta 138,22,35936
buffer_load_dwordx4 a[24:27], v1, s[8:11], s82 offen offset:2048 sc1
Mvaa 96,148,228
Ta 140,24,96
v_cndmask_b32_e64 v5, 0, v15, s[86:87]
Mvaa 104,148,244
Ta 142,24,5216
Mvaa 104,144,240
W 8
Ta 144,21,30848
Ta 146,21,35968
Mvaa 108,152,240
buffer_load_dwordx4 a[12:15], v1, s[8:11], s81 offen offset:3072 sc1
v_cmp_eq_u32_e64 s[86:87], 2, v12
Mvaa 108,156,244
Ta 148,23,128
Ta 150,23,5248
Mvaa 100,156,228
v_cndmask_b32_e64 v6, 0, v15, s[86:87]
Mvaa 100,152,224
W 8
Ta 152,22,30880
Ta 154,22,36000
Mvaa 112,128,224
buffer_load_dwordx4 a[28:31], v1, s[8:11], s82 offen offset:3072 sc1
v_cmp_eq_u32_e64 s[86:87], 3, v12
Mvaa 112,132,228
s_add_u32 s81, s58, 3
s_and_b32 s81, s81, 3
s_lshl_b32 s81, s81, 12
Mvaa 120,132,244
s_add_u32 s81, s81, s49
Ta 156,24,160
Ta 158,24,5280
Mvaa 120,128,240
W 8
Ta 128,21,30912
v_cndmask_b32_e64 v7, 0, v15, s[86:87]
Mvaa 124,136,240
Ta 130,21,36032
buffer_load_dwordx4 a[32:35], v1, s[8:11], s81 offen offset:0 sc1
Mvaa 124,140,244
s_add_u32 s82, s81, 131072
Ta 132,23,192
v_cmp_eq_u32_e64 s[86:87], 4, v12
Mvaa 116,140,228
Ta 134,23,5312
Mvaa 116,136,224
W 8
Ta 136,22,30944
v_cndmask_b32_e64 v8, 0, v15, s[86:87]
Mvaa 128,144,224
Ta 138,22,36064
buffer_load_dwordx4 a[48:51], v1, s[8:11], s82 offen offset:0 sc1
Mvaa 128,148,228
Ta 140,24,224
Ta 142,24,5344
Mvaa 136,148,244
v_cmp_eq_u32_e64 s[86:87], 5, v12
Mvaa 136,144,240
W 8
Ta 144,21,30976
Ta 146,21,36096
Mvaa 140,152,240
s_waitcnt vmcnt(10)
s_barrier
v_cndmask_b32_e64 v9, 0, v15, s[86:87]
Mvaa 140,156,244
buffer_load_dwordx4 a[36:39], v1, s[8:11], s81 offen offset:1024 sc1
Ta 148,23,256
Mvaa 132,156,228
Ta 150,23,5376
v_cmp_eq_u32_e64 s[86:87], 6, v12
Mvaa 132,152,224
W 8
Ta 152,22,31008
Ta 154,22,36128
Mvaa 144,128,224
Mvaa 144,132,228
buffer_load_dwordx4 a[52:55], v1, s[8:11], s82 offen offset:1024 sc1
v_cndmask_b32_e64 v10, 0, v15, s[86:87]
Mvaa 152,132,244
Ta 156,24,288
Ta 158,24,5408
Mvaa 152,128,240
W 8
Ta 128,23,20480
v_cmp_eq_u32_e64 s[86:87], 7, v12
Mvaa 156,136,240
Ta 130,23,25600
Mvaa 156,140,244
buffer_load_dwordx4 a[40:43], v1, s[8:11], s81 offen offset:2048 sc1
v_cndmask_b32_e64 v11, 0, v15, s[86:87]
Mvaa 148,140,228
Ta 132,23,51200
Ta 134,23,56320
Mvaa 148,136,224
W 8
Ta 136,24,20512
Ta 138,24,25632
Mvaa 160,144,224
s_mov_b32 s20, s38
Mvaa 160,148,228
buffer_load_dwordx4 a[56:59], v1, s[8:11], s82 offen offset:2048 sc1
Ta 140,24,51232
Mvaa 168,148,244
s_and_b32 s21, s39, 0xffff
Ta 142,24,56352
)"
R"(Mvaa 168,144,240
W 8
Ta 144,23,20544
Ta 146,23,25664
Mvaa 172,152,240
s_mov_b32 s22, 2621440
Mvaa 172,156,244
buffer_load_dwordx4 a[44:47], v1, s[8:11], s81 offen offset:3072 sc1
Ta 148,23,51264
Mvaa 164,156,228
Ta 150,23,56384
Mvaa 164,152,224
s_waitcnt vmcnt(11) lgkmcnt(8)
s_mov_b32 s23, 0x20000
Ta 152,24,20576
Mvaa 96,128,0
Ta 154,24,25696
Mvaa 96,132,4
s_and_b32 s17, s17, 0xffff
buffer_load_dwordx4 a[60:63], v1, s[8:11], s82 offen offset:3072 sc1
Ta 156,24,51296
Mvaa 104,132,20
Ta 158,24,56416
Mvaa 104,128,16
W 8
s_mov_b32 s18, 2621440
Ta 128,23,20608
Mvaa 108,136,16
Ta 130,23,25728
Mvaa 108,140,20
Ta 132,23,51328
Ta 134,23,56448
Mvaa 100,140,4
s_mov_b32 s19, 0x20000
Mvaa 100,136,0
W 8
Ta 136,24,20640
Ta 138,24,25760
Mvaa 112,144,0
s_lshr_b32 s66, s15, 3
v_bfe_u32 v28, v1, 4, 2
Mvaa 112,148,4
Ta 140,24,51360
v_lshlrev_b32 v28, 3, v28
Mvaa 120,148,20
s_lshl_b32 s66, s66, 4
Ta 142,24,56480
buffer_load_dwordx2 v[28:29], v28, s[8:11], s55 offen sc1
Mvaa 120,144,16
W 8
Ta 144,23,20672
Ta 146,23,25792
Mvaa 124,152,16
Mvaa 124,156,20
s_add_u32 s66, s66, s59
Ta 148,23,51392
Ta 150,23,56512
Mvaa 116,156,4
Mvaa 116,152,0
W 8
s_lshl_b32 s63, s66, 6
Ta 152,24,20704
Mvaa 128,128,0
Ta 154,24,25824
Mvaa 128,132,4
s_add_u32 s63, s63, 65536
Ta 156,24,51424
Ta 158,24,56544
Mvaa 136,132,20
Mvaa 136,128,16
W 8
s_mul_i32 s62, s66, 40960
Ta 128,23,20736
Mvaa 140,136,16
Ta 130,23,25856
Mvaa 140,140,20
Ta 132,23,51456
Ta 134,23,56576
Mvaa 132,140,4
s_add_u32 s62, s62, 56623104
Mvaa 132,136,0
W 8
Ta 136,24,20768
Ta 138,24,25888
Mvaa 144,144,0
s_lshl_b32 s66, s14, 13
Mvaa 144,148,4
Ta 140,24,51488
Ta 142,24,56608
Mvaa 152,148,20
s_add_u32 s78, s62, s66
Mvaa 152,144,16
W 8
Ta 144,25,0
Ta 146,25,5120
Mvaa 156,152,16
Mvaa 156,156,20
s_add_u32 s79, s78, 327680
Ta 148,21,40960
Ta 150,21,46080
Mvaa 148,156,4
Mvaa 148,152,0
W 8
s_and_b32 s66, s14, 1
Ta 152,26,32
Mvaa 160,128,0
Ta 154,26,5152
Mvaa 160,132,4
s_mul_i32 s66, s66, 327680
Ta 156,22,40992
Ta 158,22,46112
Mvaa 168,132,20
Mvaa 168,128,16
W 8
Ta 128,25,64
Ta 130,25,5184
Mvaa 172,136,16
s_add_u32 s65, s62, s66
Mvaa 172,140,20
Ta 132,21,41024
Ta 134,21,46144
Mvaa 164,140,4
s_add_u32 s65, s65, 32768
Mvaa 164,136,0
s_waitcnt vmcnt(9) lgkmcnt(8)
Ta 136,26,96
Ta 138,26,5216
Mvaa 96,144,8
s_mov_b32 s64, 0
Mvaa 96,148,12
Ta 140,22,41056
Ta 142,22,46176
Mvaa 104,148,28
Mvaa 104,144,24
W 8
Ta 144,25,128
v_add_u32 v19, 4096, v1
Mvaa 108,152,24
Ta 146,25,5248
Mvaa 108,156,28
Ta 148,21,41088
v_bfe_u32 v27, v1, 4, 3
Mvaa 100,156,12
Ta 150,21,46208
Mvaa 100,152,8
W 8
Ta 152,26,160
v_lshlrev_b32 v27, 3, v27
Mvaa 112,128,8
Ta 154,26,5280
Mvaa 112,132,12
Ta 156,22,41120
Ta 158,22,46240
Mvaa 120,132,28
s_waitcnt vmcnt(0)
v_cmp_eq_u64 vcc, s[12:13], v[28:29]
s_cmp_eq_u64 vcc, -1
s_cbranch_scc0 .Lspin_103%=
.Lok_104%=:
s_add_u32 s81, s58, 1
v_bfe_u32 v18, v1, 7, 1
Mvaa 120,128,24
W 8
s_and_b32 s81, s81, 3
s_lshl_b32 s81, s81, 12
Mvaa 124,136,24
s_add_u32 s81, s81, s50
buffer_load_dwordx4 a[64:67], v1, s[8:11], s81 offen offset:0 sc1
Ta 128,25,192
Mvaa 124,140,28
s_add_u32 s82, s81, 131072
s_add_u32 s66, s14, 1
Ta 130,25,5312
Mvaa 116,140,12
s_lshr_b32 s66, s66, 2
Ta 132,21,41152
Ta 134,21,46272
Mvaa 116,136,8
W 8
Ta 136,26,224
Ta 138,26,5344
Mvaa 128,144,8
buffer_load_dwordx4 a[80:83], v1, s[8:11], s82 offen offset:0 sc1
Mvaa 128,148,12
s_mul_i32 s66, s66, 0
Ta 140,22,41184
Ta 142,22,46304
Mvaa 136,148,28
Mvaa 136,144,24
W 8
s_sub_u32 s66, 512, s66
Ta 144,25,256
Mvaa 140,152,24
Ta 146,25,5376
Mvaa 140,156,28
buffer_load_dwordx4 a[68:71], v1, s[8:11], s81 offen offset:1024 sc1
v_mul_u32_u24 v18, s66, v18
Mvaa 132,156,12
Ta 148,21,41216
Ta 150,21,46336
Mvaa 132,152,8
W 8
Ta 152,26,288
Ta 154,26,5408
Mvaa 144,128,8
v_add_u32 v18, v18, v27
Mvaa 144,132,12
Ta 156,22,41248
Ta 158,22,46368
Mvaa 152,132,28
s_mov_b32 s71, 0
buffer_load_dwordx4 a[84:87], v1, s[8:11], s82 offen offset:1024 sc1
Mvaa 152,128,24
W 8
Ta 128,21,51200
Ta 130,21,56320
Mvaa 156,136,24
v_bfe_u32 v20, v1, 4, 4
Mvaa 156,140,28
Ta 132,23,10240
Ta 134,23,15360
Mvaa 148,140,12
Mvaa 148,136,8
W 8
buffer_load_dwordx4 a[72:75], v1, s[8:11], s81 offen offset:2048 sc1
v_mul_u32_u24 v20, 1280, v20
Mvaa 160,144,8
Ta 136,23,30720
Ta 138,23,35840
Mvaa 160,148,12
Ta 140,23,40960
v_lshrrev_b32 v12, 8, v1
Mvaa 168,148,28
Ta 142,23,46080
Mvaa 168,144,24
W 8
Ta 144,22,51232
v_lshl_add_u32 v20, v12, 4, v20
Mvaa 172,152,24
Ta 146,22,56352
buffer_load_dwordx4 a[88:91], v1, s[8:11], s82 offen offset:2048 sc1
Mvaa 172,156,28
Ta 148,24,10272
Ta 150,24,15392
Mvaa 164,156,12
s_bfe_u32 s66, s59, 0x10001
Mvaa 164,152,8
W 8
Ta 152,24,30752
Ta 154,24,35872
Mvaa 96,128,32
s_lshl_b32 s67, s57, 1
Mvaa 96,132,36
buffer_load_dwordx4 a[76:79], v1, s[8:11], s81 offen offset:3072 sc1
Ta 156,24,40992
Mvaa 104,132,52
s_add_u32 s66, s66, s67
Ta 158,24,46112
Mvaa 104,128,48
W 8
Ta 128,21,51264
Ta 130,21,56384
Mvaa 104,136,56
Mvaa 104,140,60
s_lshl_b32 s61, s66, 2
Ta 132,23,10304
Ta 134,23,15424
Mvaa 96,140,44
buffer_load_dwordx4 a[92:95], v1, s[8:11], s82 offen offset:3072 sc1
Mvaa 96,136,40
W 8
s_add_u32 s81, s58, 2
s_and_b32 s81, s81, 3
Mvaa 100,144,32
s_lshl_b32 s81, s81, 12
s_add_u32 s81, s81, s50
s_lshr_b32 s66, s59, 2
Mvaa 100,148,36
s_lshl_b32 s66, s66, 1
Ta 136,23,30784
Ta 138,23,35904
Mvaa 108,148,52
Ta 140,23,41024
Ta 142,23,46144
Mvaa 108,144,48
W 8
buffer_load_dwordx4 a[96:99], v1, s[8:11], s81 offen offset:0 sc1
Ta 144,22,51296
Mvaa 108,152,56
s_add_u32 s82, s81, 131072
s_add_u32 s61, s61, s66
Ta 146,22,56416
Mvaa 108,156,60
Ta 148,24,10336
Ta 150,24,15456
Mvaa 100,156,44
s_and_b32 s66, s59, 1
Mvaa 100,152,40
W 8
Ta 152,24,30816
Ta 154,24,35936
Mvaa 112,128,32
s_add_u32 s61, s61, s66
buffer_load_dwordx4 a[112:115], v1, s[8:11], s82 offen offset:0 sc1
Mvaa 112,132,36
Ta 156,24,41056
Ta 158,24,46176
Mvaa 120,132,52
s_xor_b32 s67, s61, 0
K 96,96,97
K 97,98,99
Mvaa 120,128,48
W 8
Ta 128,21,51328
Ta 130,21,56448
Mvaa 120,136,56
K 98,100,101
Mvaa 120,140,60
s_mul_i32 s72, s67, 20480
buffer_load_dwordx4 a[100:103], v1, s[8:11], s81 offen offset:1024 sc1
K 99,102,103
Mvaa 112,140,44
Ta 132,23,10368
Ta 134,23,15488
Mvaa 112,136,40
W 8
s_add_u32 s83, s28, 0
s_mul_i32 s66, s58, 320
Mvaa 116,144,32
Ta 136,23,30848
Ta 138,23,35968
Mvaa 116,148,36
s_add_u32 s72, s72, s66
buffer_store_dwordx4 v[96:99], v1, s[8:11], s83 offen sc1
Ta 140,23,41088
Mvaa 124,148,52
Ta 142,23,46208
K 104,104,105
Mvaa 124,144,48
W 8
Ta 144,22,51360
K 105,106,107
Mvaa 124,152,56
s_mul_i32 s75, s67, 20480
Ta 146,22,56480
Mvaa 124,156,60
buffer_load_dwordx4 a[116:119], v1, s[8:11], s82 offen offset:1024 sc1
K 106,108,109
Mvaa 116,156,44
s_mul_i32 s66, s58, 2560
Ta 148,24,10400
Ta 150,24,15520
Mvaa 116,152,40
W 8
Ta 152,24,30880
K 107,110,111
Mvaa 128,128,32
s_add_u32 s83, s29, 40960
s_add_u32 s75, s75, s66
Ta 154,24,36000
Mvaa 128,132,36
buffer_load_dwordx4 a[104:107], v1, s[8:11], s81 offen offset:2048 sc1
Ta 156,24,41120
Mvaa 136,132,52
Ta 158,24,46240
Mvaa 136,128,48
W 8
s_add_u32 s75, s75, 131072
Ta 128,21,51392
Mvaa 136,136,56
Ta 130,21,56512
Mvaa 136,140,60
s_xor_b32 s67, s61, 124
buffer_store_dwordx4 v[104:107], v1, s[8:11], s83 offen sc1
K 112,112,113
Mvaa 128,140,44
Ta 132,23,10432
K 113,114,115
Mvaa 128,136,40
W 7
Ta 134,23,15552
K 114,116,117
Mvaa 132,144,32
s_mul_i32 s73, s67, 20480
Ta 136,23,30912
Ta 138,23,36032
Mvaa 132,148,36
buffer_load_dwordx4 a[120:123], v1, s[8:11], s82 offen offset:2048 sc1
K 115,118,119
Mvaa 140,148,52
s_add_u32 s83, s28, 8192
Ta 140,23,41152
Ta 142,23,46272
Mvaa 140,144,48
W 8
s_mul_i32 s66, s58, 320
Ta 144,22,51424
Mvaa 140,152,56
s_add_u32 s73, s73, s66
Ta 146,22,56544
)"
R"(Mvaa 140,156,60
buffer_load_dwordx4 a[108:111], v1, s[8:11], s81 offen offset:3072 sc1
Ta 148,24,10464
Mvaa 132,156,44
s_mul_i32 s76, s67, 20480
Ta 150,24,15584
Mvaa 132,152,40
W 8
Ta 152,24,30944
Ta 154,24,36064
Mvaa 144,128,32
Mvaa 144,132,36
s_mul_i32 s66, s58, 2560
buffer_store_dwordx4 v[112:115], v1, s[8:11], s83 offen sc1
K 120,120,121
Mvaa 152,132,52
K 121,122,123
K 122,124,125
Mvaa 152,128,48
W 6
Ta 156,24,41184
K 123,126,127
Mvaa 152,136,56
s_add_u32 s83, s29, 49152
s_add_u32 s76, s76, s66
Ta 158,24,46304
Mvaa 152,140,60
s_add_u32 s76, s76, 131072
buffer_load_dwordx4 a[124:127], v1, s[8:11], s82 offen offset:3072 sc1
Ta 128,21,51456
Mvaa 144,140,44
s_add_u32 s81, s58, 3
s_and_b32 s81, s81, 3
s_lshl_b32 s81, s81, 12
Mvaa 144,136,40
W 5
s_add_u32 s81, s81, s50
Ta 130,21,56576
Mvaa 148,144,32
Ta 132,23,10496
Ta 134,23,15616
Mvaa 148,148,36
buffer_load_dwordx4 a[184:187], v1, s[8:11], s81 offen offset:0 sc1
Ta 136,23,30976
Mvaa 156,148,52
s_add_u32 s82, s81, 131072
Ta 138,23,36096
v_bfe_u32 v15, v1, 7, 1
Mvaa 156,144,48
W 6
Ta 140,23,41216
Ta 142,23,46336
Mvaa 156,152,56
Ta 144,22,51488
v_mul_u32_u24 v14, 10240, v15
Mvaa 156,156,60
buffer_store_dwordx4 v[120:123], v1, s[8:11], s83 offen sc1
K 128,128,129
Mvaa 148,156,44
K 129,130,131
K 130,132,133
Mvaa 148,152,40
W 5
Ta 146,22,56608
K 131,134,135
Mvaa 160,128,32
s_add_u32 s83, s28, 16384
Ta 148,24,10528
v_lshrrev_b32 v15, 8, v1
Mvaa 160,132,36
buffer_load_dwordx4 a[200:203], v1, s[8:11], s82 offen offset:0 sc1
Ta 150,24,15648
Mvaa 168,132,52
Ta 152,24,31008
v_lshl_add_u32 v14, v15, 7, v14
Mvaa 168,128,48
W 5
Ta 154,24,36128
v_and_b32 v15, 0x70, v1
Mvaa 168,136,56
Ta 156,24,41248
Ta 158,24,46368
Mvaa 168,140,60
buffer_load_dwordx4 a[188:191], v1, s[8:11], s81 offen offset:1024 sc1
v_add_u32 v14, v14, v15
Mvaa 160,140,44
s_bitcmp1_b32 s14, 0
s_cselect_b32 s74, s73, s72
s_cselect_b32 s77, s76, s75
Ta 128,21,10240
Ta 130,21,15360
Mvaa 160,136,40
W 6
Ta 132,21,20480
Ta 134,21,25600
Mvaa 164,144,32
s_add_u32 s74, s74, 256
Ta 136,22,10272
Ta 138,22,15392
Mvaa 164,148,36
buffer_store_dwordx4 v[128:131], v1, s[8:11], s83 offen sc1
K 136,136,137
Mvaa 172,148,52
s_add_u32 s77, s77, 2048
K 137,138,139
K 138,140,141
Mvaa 172,144,48
W 6
Ta 140,22,20512
K 139,142,143
Mvaa 172,152,56
s_add_u32 s83, s29, 57344
Ta 142,22,25632
Ta 144,21,10304
Mvaa 172,156,60
s_lshl_b32 s66, s14, 6
buffer_load_dwordx4 a[204:207], v1, s[8:11], s82 offen offset:1024 sc1
Ta 146,21,15424
Mvaa 164,156,44
Ta 148,21,20544
Ta 150,21,25664
Mvaa 164,152,40
s_waitcnt vmcnt(21) lgkmcnt(8)
s_lshl_b32 s67, s14, 9
Ta 152,22,10336
Mvaa 176,128,64
Ta 154,22,15456
Mvaa 176,132,68
s_add_u32 s72, s72, s66
buffer_load_dwordx4 a[192:195], v1, s[8:11], s81 offen offset:2048 sc1
Ta 156,22,20576
Mvaa 184,132,84
Ta 158,22,25696
Mvaa 184,128,80
W 8
Ta 128,21,10368
Ta 130,21,15488
Mvaa 188,136,80
s_add_u32 s75, s75, s67
Mvaa 188,140,84
buffer_store_dwordx4 v[136:139], v1, s[8:11], s83 offen sc1
K 144,144,145
Mvaa 180,140,68
s_add_u32 s73, s73, s66
K 145,146,147
K 146,148,149
Mvaa 180,136,64
W 6
Ta 132,21,20608
K 147,150,151
Mvaa 192,144,64
s_add_u32 s83, s28, 24576
Ta 134,21,25728
Ta 136,22,10400
Mvaa 192,148,68
s_add_u32 s76, s76, s67
buffer_load_dwordx4 a[208:211], v1, s[8:11], s82 offen offset:2048 sc1
Ta 138,22,15520
Mvaa 200,148,84
Ta 140,22,20640
Ta 142,22,25760
Mvaa 200,144,80
W 8
Ta 144,21,10432
Ta 146,21,15552
Mvaa 204,152,80
Mvaa 204,156,84
buffer_store_dwordx4 v[144:147], v1, s[8:11], s83 offen sc1
K 152,152,153
Mvaa 196,156,68
K 153,154,155
K 154,156,157
Mvaa 196,152,64
W 6
Ta 148,21,20672
K 155,158,159
Mvaa 208,128,64
s_add_u32 s83, s29, 65536
Ta 150,21,25792
Ta 152,22,10464
Mvaa 208,132,68
buffer_load_dwordx4 a[196:199], v1, s[8:11], s81 offen offset:3072 sc1
Ta 154,22,15584
Mvaa 216,132,84
Ta 156,22,20704
Ta 158,22,25824
Mvaa 216,128,80
W 8
Ta 128,21,10496
Ta 130,21,15616
Mvaa 220,136,80
Mvaa 220,140,84
buffer_load_dwordx4 a[212:215], v1, s[8:11], s82 offen offset:3072 sc1
Ta 132,21,20736
Mvaa 212,140,68
Ta 134,21,25856
Mvaa 212,136,64
W 8
Ta 136,22,10528
Ta 138,22,15648
Mvaa 224,144,64
Mvaa 224,148,68
buffer_store_dwordx4 v[152:155], v1, s[8:11], s83 offen sc1
K 160,160,161
Mvaa 232,148,84
K 161,162,163
K 162,164,165
Mvaa 232,144,80
W 6
Ta 140,22,20768
K 163,166,167
Mvaa 236,152,80
s_add_u32 s83, s28, 32768
Ta 142,22,25888
Ta 144,21,30720
Mvaa 236,156,84
buffer_load_dwordx4 v[36:39], v20, s[16:19], s72 offen
Ta 146,21,35840
Mvaa 228,156,68
Ta 148,23,0
Ta 150,23,5120
Mvaa 228,152,64
W 8
Ta 152,22,30752
Ta 154,22,35872
Mvaa 240,128,64
Mvaa 240,132,68
buffer_load_dwordx4 v[44:47], v14, s[8:11], s75 offen sc1
Ta 156,24,32
Mvaa 248,132,84
Ta 158,24,5152
Mvaa 248,128,80
W 8
Ta 128,21,30784
Ta 130,21,35904
Mvaa 252,136,80
Mvaa 252,140,84
buffer_store_dwordx4 v[160:163], v1, s[8:11], s83 offen sc1
K 168,168,169
Mvaa 244,140,68
K 169,170,171
K 170,172,173
Mvaa 244,136,64
s_waitcnt vmcnt(27) lgkmcnt(6)
Ta 132,23,64
K 171,174,175
Mvaa 176,144,72
s_add_u32 s83, s29, 73728
Ta 134,23,5184
Ta 136,22,30816
Mvaa 176,148,76
buffer_load_dwordx4 v[52:55], v20, s[16:19], s73 offen
Ta 138,22,35936
Mvaa 184,148,92
Ta 140,24,96
Ta 142,24,5216
Mvaa 184,144,88
W 8
Ta 144,21,30848
Ta 146,21,35968
Mvaa 188,152,88
Mvaa 188,156,92
buffer_store_dwordx4 v[168:171], v1, s[8:11], s83 offen sc1
Ta 148,23,128
Mvaa 180,156,76
Ta 150,23,5248
Mvaa 180,152,72
W 8
Ta 152,22,30880
Ta 154,22,36000
Mvaa 192,128,72
Mvaa 192,132,76
buffer_load_dwordx4 v[60:63], v14, s[8:11], s76 offen sc1
Ta 156,24,160
Mvaa 200,132,92
Ta 158,24,5280
Mvaa 200,128,88
W 8
Ta 128,21,30912
Ta 130,21,36032
Mvaa 204,136,88
Mvaa 204,140,92
buffer_load_dwordx4 v[68:71], v20, s[16:19], s74 offen
Ta 132,23,192
Mvaa 196,140,76
Ta 134,23,5312
Mvaa 196,136,72
W 8
Ta 136,22,30944
Ta 138,22,36064
Mvaa 208,144,72
Mvaa 208,148,76
buffer_load_dwordx4 v[76:79], v14, s[8:11], s77 offen sc1
Ta 140,24,224
Mvaa 216,148,92
Ta 142,24,5344
Mvaa 216,144,88
W 8
Ta 144,21,30976
Ta 146,21,36096
Mvaa 220,152,88
Mvaa 220,156,92
buffer_load_dwordx2 v[12:13], v27, s[8:11], 0 offen offset:3584 sc1
Ta 148,23,256
Mvaa 212,156,76
Ta 150,23,5376
Mvaa 212,152,72
W 8
Ta 152,22,31008
Ta 154,22,36128
Mvaa 224,128,72
Mvaa 224,132,76
Ta 156,24,288
Ta 158,24,5408
Mvaa 232,132,92
Mvaa 232,128,88
W 8
Ta 128,23,20480
Ta 130,23,25600
Mvaa 236,136,88
Mvaa 236,140,92
Ta 132,23,51200
Ta 134,23,56320
Mvaa 228,140,76
Mvaa 228,136,72
W 8
Ta 136,25,0
Ta 138,25,5120
Mvaa 240,144,72
Mvaa 240,148,76
Ta 140,21,40960
Ta 142,21,46080
Mvaa 248,148,92
Mvaa 248,144,88
W 8
Ta 144,21,51200
Ta 146,21,56320
Mvaa 252,152,88
Mvaa 252,156,92
Ta 148,23,10240
Ta 150,23,15360
Mvaa 244,156,76
Mvaa 244,152,72
s_waitcnt vmcnt(28) lgkmcnt(8)
Ta 152,23,30720
Ta 154,23,35840
Mvaa 176,128,96
Mvaa 176,132,100
Ta 156,23,40960
Ta 158,23,46080
Mvaa 184,132,116
Mvaa 184,128,112
s_waitcnt vmcnt(22) lgkmcnt(8)
Ta 128,24,20512
Ta 130,24,25632
Mvaa 184,136,120
Mvaa 184,140,124
Ta 132,24,51232
Ta 134,24,56352
Mvaa 176,140,108
s_barrier
Mvaa 176,136,104
s_waitcnt vmcnt(16) lgkmcnt(8)
Ta 136,26,32
Ta 138,26,5152
Mvaa 176,144,184
Mvaa 176,148,188
Ta 140,22,40992
Ta 142,22,46112
Mvaa 184,148,204
s_waitcnt vmcnt(4)
v_lshlrev_b32 v2, 2, v1
v_mov_b64 v[28:29], s[12:13]
s_mov_b64 exec, 3
buffer_store_dwordx2 v[28:29], v2, s[8:11], s53 offen offset:0 sc1
s_mov_b64 exec, -1
Mvaa 184,144,200
W 8
Ta 144,22,51232
Ta 146,22,56352
Mvaa 184,152,208
Mvaa 184,156,212
Ta 148,24,10272
Ta 150,24,15392
Mvaa 176,156,196
Mvaa 176,152,192
W 8
Ta 152,24,30752
Ta 154,24,35872
Mvaa 180,128,96
Mvaa 180,132,100
Ta 156,24,40992
Ta 158,24,46112
Mvaa 188,132,116
Mvaa 188,128,112
W 8
Ta 128,23,20544
Ta 130,23,25664
Mvaa 188,136,120
Mvaa 188,140,124
)"
R"(Ta 132,23,51264
Ta 134,23,56384
Mvaa 180,140,108
Mvaa 180,136,104
W 8
Ta 136,25,64
Ta 138,25,5184
Mvaa 180,144,184
Mvaa 180,148,188
Ta 140,21,41024
Ta 142,21,46144
Mvaa 188,148,204
Mvaa 188,144,200
W 8
Ta 144,21,51264
Ta 146,21,56384
Mvaa 188,152,208
Mvaa 188,156,212
Ta 148,23,10304
Ta 150,23,15424
Mvaa 180,156,196
s_waitcnt vmcnt(1)
v_xor_b32 v12, s12, v12
v_xor_b32 v13, s13, v13
v_lshrrev_b32 v3, 11, v12
v_or_b32 v13, v13, v3
v_cmp_eq_u32 vcc, 0, v13
s_cmp_eq_u64 vcc, -1
s_cbranch_scc0 .LL_108%=
.LL_109%=:
v_bfe_u32 v13, v12, 4, 7
s_xor_b32 s66, s61, 124
v_cmp_eq_u32 vcc, s61, v13
v_cmp_eq_u32_e64 s[86:87], s66, v13
s_or_b32 s68, vcc_lo, s86
s_and_b32 s68, s68, 255
s_cbranch_scc1 .Lup_106%=
.Lupr_107%=:
Mvaa 180,152,192
W 8
Ta 152,23,30784
Ta 154,23,35904
Mvaa 192,128,96
v_lshlrev_b32 v32, 16, v36
Mvaa 192,132,100
Ta 156,23,41024
v_and_b32 v33, 0xffff0000, v36
Mvaa 200,132,116
Ta 158,23,46144
v_lshlrev_b32 v34, 16, v37
Mvaa 200,128,112
W 8
K 176,176,177
v_and_b32 v35, 0xffff0000, v37
Mvaa 200,136,120
Ta 128,24,20576
Ta 130,24,25696
Mvaa 200,140,124
buffer_load_dwordx2 v[16:17], v18, s[8:11], s63 offen sc1
v_lshlrev_b32 v36, 16, v38
Mvaa 192,140,108
K 177,178,179
v_and_b32 v37, 0xffff0000, v38
Mvaa 192,136,104
W 6
Ta 132,24,51296
K 178,180,181
Mvaa 192,144,184
Ta 134,24,56416
v_lshlrev_b32 v38, 16, v39
Mvaa 192,148,188
K 179,182,183
v_and_b32 v39, 0xffff0000, v39
Mvaa 200,148,204
s_add_u32 s83, s28, 327680
Ta 136,26,96
Ta 138,26,5216
Mvaa 200,144,200
W 6
v_lshlrev_b32 v40, 16, v44
v_and_b32 v41, 0xffff0000, v44
Mvaa 200,152,208
buffer_store_dwordx4 v[176:179], v1, s[8:11], s83 offen sc1
K 184,184,185
Mvaa 200,156,212
K 185,186,187
K 186,188,189
Mvaa 192,156,196
K 187,190,191
s_add_u32 s83, s29, 368640
buffer_store_dwordx4 v[184:187], v1, s[8:11], s83 offen sc1
Mvaa 192,152,192
Ta 140,22,41056
Ta 142,22,46176
W 4
Mvaa 196,128,96
v_lshlrev_b32 v42, 16, v45
v_and_b32 v43, 0xffff0000, v45
Mvaa 196,132,100
Ta 144,22,51296
Ta 146,22,56416
Mvaa 204,132,116
v_lshlrev_b32 v44, 16, v46
v_and_b32 v45, 0xffff0000, v46
Mvaa 204,128,112
Ta 148,24,10336
Ta 150,24,15456
W 4
Mvaa 204,136,120
v_lshlrev_b32 v46, 16, v47
v_and_b32 v47, 0xffff0000, v47
Mvaa 204,140,124
Ta 152,24,30816
Ta 154,24,35936
Mvaa 196,140,108
v_lshlrev_b32 v48, 16, v52
v_and_b32 v49, 0xffff0000, v52
Mvaa 196,136,104
Ta 156,24,41056
Ta 158,24,46176
W 4
Mvaa 196,144,184
v_lshlrev_b32 v50, 16, v53
v_and_b32 v51, 0xffff0000, v53
Mvaa 196,148,188
Ta 128,23,20608
Ta 130,23,25728
Mvaa 204,148,204
v_lshlrev_b32 v52, 16, v54
v_and_b32 v53, 0xffff0000, v54
Mvaa 204,144,200
Ta 132,23,51328
Ta 134,23,56448
W 4
Mvaa 204,152,208
v_lshlrev_b32 v54, 16, v55
v_and_b32 v55, 0xffff0000, v55
Mvaa 204,156,212
Ta 136,25,128
Ta 138,25,5248
Mvaa 196,156,196
v_lshlrev_b32 v56, 16, v60
v_and_b32 v57, 0xffff0000, v60
Mvaa 196,152,192
Ta 140,21,41088
Ta 142,21,46208
W 4
Mvaa 208,128,96
v_lshlrev_b32 v58, 16, v61
v_and_b32 v59, 0xffff0000, v61
Mvaa 208,132,100
Ta 144,21,51328
Ta 146,21,56448
Mvaa 216,132,116
v_lshlrev_b32 v60, 16, v62
v_and_b32 v61, 0xffff0000, v62
Mvaa 216,128,112
Ta 148,23,10368
Ta 150,23,15488
W 4
Mvaa 216,136,120
v_lshlrev_b32 v62, 16, v63
v_and_b32 v63, 0xffff0000, v63
Mvaa 216,140,124
Ta 152,23,30848
Ta 154,23,35968
Mvaa 208,140,108
v_lshlrev_b32 v64, 16, v68
v_and_b32 v65, 0xffff0000, v68
Mvaa 208,136,104
Ta 156,23,41088
Ta 158,23,46208
W 4
Mvaa 208,144,184
v_lshlrev_b32 v66, 16, v69
K 192,192,193
Mvaa 208,148,188
Ta 128,24,20640
v_and_b32 v67, 0xffff0000, v69
Mvaa 216,148,204
Ta 130,24,25760
K 193,194,195
Mvaa 216,144,200
Ta 132,24,51360
Ta 134,24,56480
W 4
Mvaa 216,152,208
v_lshlrev_b32 v68, 16, v70
K 194,196,197
Mvaa 216,156,212
v_and_b32 v69, 0xffff0000, v70
v_lshlrev_b32 v70, 16, v71
Mvaa 208,156,196
K 195,198,199
s_add_u32 s83, s28, 335872
buffer_store_dwordx4 v[192:195], v1, s[8:11], s83 offen sc1
K 200,200,201
K 201,202,203
K 202,204,205
K 203,206,207
s_add_u32 s83, s29, 376832
buffer_store_dwordx4 v[200:203], v1, s[8:11], s83 offen sc1
Mvaa 208,152,192
Ta 136,26,160
Ta 138,26,5280
Ta 140,22,41120
Ta 142,22,46240
W 4
Mvaa 212,128,96
s_waitcnt vmcnt(4)
v_cmp_eq_u64 vcc, s[12:13], v[16:17]
s_and_b32 s71, vcc_lo, 255
s_bitset0_b32 s71, s59
v_and_b32 v71, 0xffff0000, v71
v_lshlrev_b32 v72, 16, v76
Mvaa 212,132,100
s_lshl_b32 s92, s14, 3
s_lshr_b32 s92, s71, s92
s_mov_b32 s91, 134217728
Mvaa 220,132,116
s_bitcmp1_b32 s71, 0
s_cselect_b32 s94, s78, s91
v_and_b32 v73, 0xffff0000, v76
Mvaa 220,128,112
Ta 144,22,51360
Ta 146,22,56480
Ta 148,24,10400
Ta 150,24,15520
W 4
Mvaa 220,136,120
v_lshlrev_b32 v74, 16, v77
v_and_b32 v75, 0xffff0000, v77
Mvaa 220,140,124
v_lshlrev_b32 v76, 16, v78
v_and_b32 v77, 0xffff0000, v78
Mvaa 212,140,108
v_lshlrev_b32 v78, 16, v79
v_and_b32 v79, 0xffff0000, v79
Mvaa 212,136,104
Ta 152,24,30880
Ta 154,24,36000
Ta 156,24,41120
Ta 158,24,46240
W 4
Mvaa 212,144,184
Ta 128,23,20672
Ta 130,23,25792
Mvaa 212,148,188
buffer_load_dwordx4 a[0:3], v1, s[8:11], s94 offen offset:0 sc1
Ta 132,23,51392
Mvaa 220,148,204
Ta 134,23,56512
Ta 136,25,192
Mvaa 220,144,200
W 5
Ta 138,25,5312
Ta 140,21,41152
Mvaa 220,152,208
Ta 142,21,46272
Ta 144,21,51392
Mvaa 220,156,212
s_bitcmp1_b32 s92, 0
s_cbranch_scc0 .LL_111%=
buffer_load_dwordx4 a[216:219], v1, s[8:11], s65 offen offset:0 sc1
.LL_111%=:
Ta 146,21,56512
Mvaa 212,156,196
s_bitcmp1_b32 s71, 1
s_cselect_b32 s94, s78, s91
Ta 148,23,10432
Mvaa 212,152,192
W 7
Ta 150,23,15552
Ta 152,23,30912
Mvaa 224,128,96
Ta 154,23,36032
Mvaa 224,132,100
buffer_load_dwordx4 a[4:7], v1, s[8:11], s94 offen offset:1024 sc1
Ta 156,23,41152
Mvaa 232,132,116
Ta 158,23,46272
K 208,208,209
Mvaa 232,128,112
W 8
Ta 128,24,20704
Ta 130,24,25824
Mvaa 232,136,120
K 209,210,211
Mvaa 232,140,124
s_bitcmp1_b32 s92, 1
s_cbranch_scc0 .LL_112%=
buffer_load_dwordx4 a[220:223], v1, s[8:11], s65 offen offset:1024 sc1
.LL_112%=:
K 210,212,213
Mvaa 224,140,108
s_bitcmp1_b32 s71, 2
s_cselect_b32 s94, s78, s91
Ta 132,24,51424
Mvaa 224,136,104
W 7
Ta 134,24,56544
K 211,214,215
Mvaa 224,144,184
s_add_u32 s83, s28, 344064
Ta 136,26,224
Ta 138,26,5344
Mvaa 224,148,188
buffer_load_dwordx4 a[8:11], v1, s[8:11], s94 offen offset:2048 sc1
Ta 140,22,41184
Mvaa 232,148,204
Ta 142,22,46304
Mvaa 232,144,200
W 8
Ta 144,22,51424
Ta 146,22,56544
Mvaa 232,152,208
Mvaa 232,156,212
buffer_store_dwordx4 v[208:211], v1, s[8:11], s83 offen sc1
K 216,216,217
Mvaa 224,156,196
K 217,218,219
K 218,220,221
K 219,222,223
s_add_u32 s83, s29, 385024
buffer_store_dwordx4 v[216:219], v1, s[8:11], s83 offen sc1
Mvaa 224,152,192
W 6
Ta 148,24,10464
Ta 150,24,15584
Mvaa 228,128,96
Ta 152,24,30944
Ta 154,24,36064
Mvaa 228,132,100
Ta 156,24,41184
Ta 158,24,46304
Mvaa 236,132,116
s_bitcmp1_b32 s92, 2
s_cbranch_scc0 .LL_113%=
buffer_load_dwordx4 a[224:227], v1, s[8:11], s65 offen offset:2048 sc1
.LL_113%=:
Mvaa 236,128,112
W 8
s_bitcmp1_b32 s71, 3
s_cselect_b32 s94, s78, s91
Mvaa 236,136,120
Ta 128,23,20736
Ta 130,23,25856
Mvaa 236,140,124
Ta 132,23,51456
Ta 134,23,56576
Mvaa 228,140,108
buffer_load_dwordx4 a[12:15], v1, s[8:11], s94 offen offset:3072 sc1
Mvaa 228,136,104
W 8
Ta 136,25,256
Ta 138,25,5376
Mvaa 228,144,184
Mvaa 228,148,188
Ta 140,21,41216
Ta 142,21,46336
Mvaa 236,148,204
s_bitcmp1_b32 s92, 3
s_cbranch_scc0 .LL_114%=
buffer_load_dwordx4 a[228:231], v1, s[8:11], s65 offen offset:3072 sc1
.LL_114%=:
Mvaa 236,144,200
W 8
s_bitcmp1_b32 s71, 4
s_cselect_b32 s94, s78, s91
Mvaa 236,152,208
Ta 144,21,51456
Ta 146,21,56576
Mvaa 236,156,212
Ta 148,23,10496
Ta 150,23,15616
Mvaa 228,156,196
buffer_load_dwordx4 a[16:19], v19, s[8:11], s94 offen offset:0 sc1
Mvaa 228,152,192
W 8
Ta 152,23,30976
Ta 154,23,36096
Mvaa 240,128,96
Mvaa 240,132,100
Ta 156,23,41216
Ta 158,23,46336
Mvaa 248,132,116
s_bitcmp1_b32 s92, 4
s_cbranch_scc0 .LL_115%=
buffer_load_dwordx4 a[232:235], v19, s[8:11], s65 offen offset:0 sc1
.LL_115%=:
K 224,224,225
Mvaa 248,128,112
W 8
s_bitcmp1_b32 s71, 5
s_cselect_b32 s94, s78, s91
Mvaa 248,136,120
Ta 128,24,20768
Ta 130,24,25888
Mvaa 248,140,124
K 225,226,227
)"
R"(K 226,228,229
Mvaa 240,140,108
buffer_load_dwordx4 a[20:23], v19, s[8:11], s94 offen offset:1024 sc1
Ta 132,24,51488
Mvaa 240,136,104
W 7
Ta 134,24,56608
K 227,230,231
Mvaa 240,144,184
s_add_u32 s83, s28, 352256
Ta 136,26,288
Ta 138,26,5408
Mvaa 240,148,188
Ta 140,22,41248
Ta 142,22,46368
Mvaa 248,148,204
s_bitcmp1_b32 s92, 5
s_cbranch_scc0 .LL_116%=
buffer_load_dwordx4 a[236:239], v19, s[8:11], s65 offen offset:1024 sc1
.LL_116%=:
Mvaa 248,144,200
W 8
s_bitcmp1_b32 s71, 6
s_cselect_b32 s94, s78, s91
Mvaa 248,152,208
Ta 144,22,51488
Ta 146,22,56608
Mvaa 248,156,212
Ta 148,24,10528
Ta 150,24,15648
Mvaa 240,156,196
buffer_store_dwordx4 v[224:227], v1, s[8:11], s83 offen sc1
K 232,232,233
K 233,234,235
K 234,236,237
K 235,238,239
s_add_u32 s83, s29, 393216
buffer_store_dwordx4 v[232:235], v1, s[8:11], s83 offen sc1
Mvaa 240,152,192
W 8
Ta 152,24,31008
Ta 154,24,36128
Mvaa 244,128,96
Mvaa 244,132,100
Ta 156,24,41248
Ta 158,24,46368
Mvaa 252,132,116
buffer_load_dwordx4 a[24:27], v19, s[8:11], s94 offen offset:2048 sc1
Mvaa 252,128,112
W 8
Mvaa 252,136,120
Mvaa 252,140,124
Mvaa 244,140,108
s_bitcmp1_b32 s92, 6
s_cbranch_scc0 .LL_117%=
buffer_load_dwordx4 a[240:243], v19, s[8:11], s65 offen offset:2048 sc1
.LL_117%=:
Mvaa 244,136,104
W 4
s_bitcmp1_b32 s71, 7
s_cselect_b32 s94, s78, s91
Mvaa 244,144,184
Mvaa 244,148,188
Mvaa 252,148,204
buffer_load_dwordx4 a[28:31], v19, s[8:11], s94 offen offset:3072 sc1
Mvaa 252,144,200
W 0
Mvaa 252,152,208
Mvaa 252,156,212
Mvaa 244,156,196
s_bitcmp1_b32 s92, 7
s_cbranch_scc0 .LL_118%=
buffer_load_dwordx4 a[244:247], v19, s[8:11], s65 offen offset:3072 sc1
.LL_118%=:
Mvaa 244,152,192
K 240,240,241
K 241,242,243
s_nop 5
K 242,244,245
K 243,246,247
s_add_u32 s83, s28, 360448
buffer_store_dwordx4 v[240:243], v1, s[8:11], s83 offen sc1
K 248,248,249
K 249,250,251
K 250,252,253
K 251,254,255
s_add_u32 s83, s29, 401408
buffer_store_dwordx4 v[248:251], v1, s[8:11], s83 offen sc1
.Ltend_83%=:
buffer_load_dwordx2 v[16:17], v18, s[8:11], s63 offen sc1
s_bitcmp1_b32 s28, 27
s_cbranch_scc0 .LL_119%=
ds_write_b128 v1, v[96:99] offset:0
ds_write_b128 v1, v[112:115] offset:1024
ds_write_b128 v1, v[128:131] offset:2048
ds_write_b128 v1, v[144:147] offset:3072
ds_write_b128 v1, v[160:163] offset:4096
ds_write_b128 v1, v[176:179] offset:5120
ds_write_b128 v1, v[192:195] offset:6144
ds_write_b128 v1, v[208:211] offset:7168
ds_write_b128 v1, v[224:227] offset:8192
ds_write_b128 v1, v[240:243] offset:9216
W 0
.LL_119%=:
s_bitcmp1_b32 s29, 27
s_cbranch_scc0 .LL_120%=
ds_write_b128 v1, v[104:107] offset:0
ds_write_b128 v1, v[120:123] offset:1024
ds_write_b128 v1, v[136:139] offset:2048
ds_write_b128 v1, v[152:155] offset:3072
ds_write_b128 v1, v[168:171] offset:4096
ds_write_b128 v1, v[184:187] offset:5120
ds_write_b128 v1, v[200:203] offset:6144
ds_write_b128 v1, v[216:219] offset:7168
ds_write_b128 v1, v[232:235] offset:8192
ds_write_b128 v1, v[248:251] offset:9216
W 0
.LL_120%=:
s_lshl_b32 s80, s14, 10
v_add_u32 v21, s80, v1
s_cmp_lg_u32 s68, 0
s_cbranch_scc1 .Lup_121%=
.Lupr_122%=:
s_lshl_b32 s66, 0x101, s59
s_waitcnt vmcnt(1)
s_cmp_eq_u32 s64, 0
s_cbranch_scc0 .LL_127%=
v_lshlrev_b32 v2, 2, v1
v_mov_b64 v[28:29], s[12:13]
s_mov_b64 exec, 3
buffer_store_dwordx2 v[28:29], v2, s[8:11], s53 offen offset:512 sc1
s_mov_b64 exec, -1
.LL_127%=:
s_barrier
s_waitcnt vmcnt(1)
v_cmp_eq_u64 vcc, s[12:13], v[16:17]
s_or_b32 s67, vcc_lo, s66
s_cmp_eq_u32 s14, 0
s_cbranch_scc1 .LL_129%=
s_cmp_eq_u32 s14, 1
s_cbranch_scc1 .LL_130%=
s_mov_b32 s84, 0
.LL_131%=:
s_or_b32 s70, s71, s66
s_andn2_b32 s69, s67, s71
s_and_b32 s69, s69, 0xffff
s_cbranch_scc0 .LL_134%=
s_and_b32 s80, s69, 255
s_cbranch_scc0 .LL_135%=
s_bitcmp1_b32 s69, 0
s_cbranch_scc0 .LL_136%=
s_cmp_eq_u32 s59, 0
s_cbranch_scc0 .LL_137%=
Ra 0,21,0
s_branch .LL_136%=
.LL_137%=:
buffer_load_dwordx4 a[0:3], v1, s[8:11], s78 offen offset:0 sc1
.LL_136%=:
s_bitcmp1_b32 s69, 1
s_cbranch_scc0 .LL_138%=
s_cmp_eq_u32 s59, 1
s_cbranch_scc0 .LL_139%=
Ra 4,21,0
s_branch .LL_138%=
.LL_139%=:
buffer_load_dwordx4 a[4:7], v1, s[8:11], s78 offen offset:1024 sc1
.LL_138%=:
s_bitcmp1_b32 s69, 2
s_cbranch_scc0 .LL_140%=
s_cmp_eq_u32 s59, 2
s_cbranch_scc0 .LL_141%=
Ra 8,21,0
s_branch .LL_140%=
.LL_141%=:
buffer_load_dwordx4 a[8:11], v1, s[8:11], s78 offen offset:2048 sc1
.LL_140%=:
s_bitcmp1_b32 s69, 3
s_cbranch_scc0 .LL_142%=
s_cmp_eq_u32 s59, 3
s_cbranch_scc0 .LL_143%=
Ra 12,21,0
s_branch .LL_142%=
.LL_143%=:
buffer_load_dwordx4 a[12:15], v1, s[8:11], s78 offen offset:3072 sc1
.LL_142%=:
s_bitcmp1_b32 s69, 4
s_cbranch_scc0 .LL_144%=
s_cmp_eq_u32 s59, 4
s_cbranch_scc0 .LL_145%=
Ra 16,21,0
s_branch .LL_144%=
.LL_145%=:
buffer_load_dwordx4 a[16:19], v19, s[8:11], s78 offen offset:0 sc1
.LL_144%=:
s_bitcmp1_b32 s69, 5
s_cbranch_scc0 .LL_146%=
s_cmp_eq_u32 s59, 5
s_cbranch_scc0 .LL_147%=
Ra 20,21,0
s_branch .LL_146%=
.LL_147%=:
buffer_load_dwordx4 a[20:23], v19, s[8:11], s78 offen offset:1024 sc1
.LL_146%=:
s_bitcmp1_b32 s69, 6
s_cbranch_scc0 .LL_148%=
s_cmp_eq_u32 s59, 6
s_cbranch_scc0 .LL_149%=
Ra 24,21,0
s_branch .LL_148%=
.LL_149%=:
buffer_load_dwordx4 a[24:27], v19, s[8:11], s78 offen offset:2048 sc1
.LL_148%=:
s_bitcmp1_b32 s69, 7
s_cbranch_scc0 .LL_150%=
s_cmp_eq_u32 s59, 7
s_cbranch_scc0 .LL_151%=
Ra 28,21,0
s_branch .LL_150%=
.LL_151%=:
buffer_load_dwordx4 a[28:31], v19, s[8:11], s78 offen offset:3072 sc1
.LL_150%=:
.LL_135%=:
s_and_b32 s80, s69, 65280
s_cbranch_scc0 .LL_152%=
s_bitcmp1_b32 s69, 8
s_cbranch_scc0 .LL_153%=
s_cmp_eq_u32 s59, 0
s_cbranch_scc0 .LL_154%=
Ra 32,21,5120
s_branch .LL_153%=
.LL_154%=:
buffer_load_dwordx4 a[32:35], v1, s[8:11], s79 offen offset:0 sc1
.LL_153%=:
s_bitcmp1_b32 s69, 9
s_cbranch_scc0 .LL_155%=
s_cmp_eq_u32 s59, 1
s_cbranch_scc0 .LL_156%=
Ra 36,21,5120
s_branch .LL_155%=
.LL_156%=:
buffer_load_dwordx4 a[36:39], v1, s[8:11], s79 offen offset:1024 sc1
.LL_155%=:
s_bitcmp1_b32 s69, 10
s_cbranch_scc0 .LL_157%=
s_cmp_eq_u32 s59, 2
s_cbranch_scc0 .LL_158%=
Ra 40,21,5120
s_branch .LL_157%=
.LL_158%=:
buffer_load_dwordx4 a[40:43], v1, s[8:11], s79 offen offset:2048 sc1
.LL_157%=:
s_bitcmp1_b32 s69, 11
s_cbranch_scc0 .LL_159%=
s_cmp_eq_u32 s59, 3
s_cbranch_scc0 .LL_160%=
Ra 44,21,5120
s_branch .LL_159%=
.LL_160%=:
buffer_load_dwordx4 a[44:47], v1, s[8:11], s79 offen offset:3072 sc1
.LL_159%=:
s_bitcmp1_b32 s69, 12
s_cbranch_scc0 .LL_161%=
s_cmp_eq_u32 s59, 4
s_cbranch_scc0 .LL_162%=
Ra 48,21,5120
s_branch .LL_161%=
.LL_162%=:
buffer_load_dwordx4 a[48:51], v19, s[8:11], s79 offen offset:0 sc1
.LL_161%=:
s_bitcmp1_b32 s69, 13
s_cbranch_scc0 .LL_163%=
s_cmp_eq_u32 s59, 5
s_cbranch_scc0 .LL_164%=
Ra 52,21,5120
s_branch .LL_163%=
.LL_164%=:
buffer_load_dwordx4 a[52:55], v19, s[8:11], s79 offen offset:1024 sc1
.LL_163%=:
s_bitcmp1_b32 s69, 14
s_cbranch_scc0 .LL_165%=
s_cmp_eq_u32 s59, 6
s_cbranch_scc0 .LL_166%=
Ra 56,21,5120
s_branch .LL_165%=
.LL_166%=:
buffer_load_dwordx4 a[56:59], v19, s[8:11], s79 offen offset:2048 sc1
.LL_165%=:
s_bitcmp1_b32 s69, 15
s_cbranch_scc0 .LL_167%=
s_cmp_eq_u32 s59, 7
s_cbranch_scc0 .LL_168%=
Ra 60,21,5120
s_branch .LL_167%=
.LL_168%=:
buffer_load_dwordx4 a[60:63], v19, s[8:11], s79 offen offset:3072 sc1
.LL_167%=:
.LL_152%=:
s_or_b32 s71, s71, s69
.LL_134%=:
s_andn2_b32 s80, 0xffff, s71
s_cbranch_scc0 .LL_133%=
s_sleep 4
buffer_load_dwordx2 v[16:17], v18, s[8:11], s63 offen sc1
s_and_b32 s80, s70, 0x100ff
s_cmp_eq_u32 s80, 0xff
s_cbranch_scc0 .LL_132%=
W 0
Zvva 80,4,0
Zvva 84,8,0
Mvva 80,4,4
Mvva 84,8,4
Mvva 80,4,8
Mvva 84,8,8
Mvva 80,4,12
Mvva 84,8,12
Mvva 80,4,16
Mvva 84,8,16
Mvva 80,4,20
Mvva 84,8,20
Mvva 80,4,24
Mvva 84,8,24
Mvva 80,4,28
Mvva 84,8,28
s_nop 6
K 160,80,81
K 161,82,83
K 162,84,85
K 163,86,87
v_lshlrev_b32 v80, 16, v160
v_and_b32 v81, 0xffff0000, v160
v_lshlrev_b32 v82, 16, v161
v_and_b32 v83, 0xffff0000, v161
v_lshlrev_b32 v84, 16, v162
v_and_b32 v85, 0xffff0000, v162
v_lshlrev_b32 v86, 16, v163
v_and_b32 v87, 0xffff0000, v163
v_pk_add_f32 v[80:81], v[32:33], v[80:81]
v_pk_add_f32 v[82:83], v[34:35], v[82:83]
v_pk_add_f32 v[84:85], v[36:37], v[84:85]
v_pk_add_f32 v[86:87], v[38:39], v[86:87]
K 160,80,81
K 161,82,83
K 162,84,85
K 163,86,87
v_lshlrev_b32 v80, 16, v160
v_and_b32 v81, 0xffff0000, v160
v_lshlrev_b32 v82, 16, v161
v_and_b32 v83, 0xffff0000, v161
v_lshlrev_b32 v84, 16, v162
v_and_b32 v85, 0xffff0000, v162
v_lshlrev_b32 v86, 16, v163
v_and_b32 v87, 0xffff0000, v163
v_pk_add_f32 v[80:81], v[40:41], v[80:81]
v_pk_add_f32 v[82:83], v[42:43], v[82:83]
v_pk_add_f32 v[84:85], v[44:45], v[84:85]
v_pk_add_f32 v[86:87], v[46:47], v[86:87]
K 80,80,81
K 81,82,83
K 82,84,85
K 83,86,87
buffer_store_dwordx4 v[80:83], v20, s[20:23], s72 offen nt
s_bitset1_b32 s71, 16
.LL_132%=:
s_add_u32 s84, s84, 1
s_waitcnt vmcnt(0)
v_cmp_eq_u64 vcc, s[12:13], v[16:17]
s_or_b32 s67, vcc_lo, s66
s_cmp_lt_u32 s84, 4194304
s_cbranch_scc1 .LL_131%=
.LL_133%=:
s_bitcmp1_b32 s71, 16
s_cbranch_scc1 .LL_169%=
s_and_b32 s80, s70, 0xff
s_cmp_eq_u32 s80, 0xff
s_cbranch_scc1 .LL_170%=
s_waitcnt vmcnt(0)
.LL_170%=:
W 0
Zvva 80,4,0
Zvva 84,8,0
Mvva 80,4,4
Mvva 84,8,4
Mvva 80,4,8
Mvva 84,8,8
Mvva 80,4,12
Mvva 84,8,12
Mvva 80,4,16
Mvva 84,8,16
Mvva 80,4,20
Mvva 84,8,20
Mvva 80,4,24
Mvva 84,8,24
Mvva 80,4,28
Mvva 84,8,28
s_nop 6
K 160,80,81
K 161,82,83
K 162,84,85
K 163,86,87
v_lshlrev_b32 v80, 16, v160
)"
R"(v_and_b32 v81, 0xffff0000, v160
v_lshlrev_b32 v82, 16, v161
v_and_b32 v83, 0xffff0000, v161
v_lshlrev_b32 v84, 16, v162
v_and_b32 v85, 0xffff0000, v162
v_lshlrev_b32 v86, 16, v163
v_and_b32 v87, 0xffff0000, v163
v_pk_add_f32 v[80:81], v[32:33], v[80:81]
v_pk_add_f32 v[82:83], v[34:35], v[82:83]
v_pk_add_f32 v[84:85], v[36:37], v[84:85]
v_pk_add_f32 v[86:87], v[38:39], v[86:87]
K 160,80,81
K 161,82,83
K 162,84,85
K 163,86,87
v_lshlrev_b32 v80, 16, v160
v_and_b32 v81, 0xffff0000, v160
v_lshlrev_b32 v82, 16, v161
v_and_b32 v83, 0xffff0000, v161
v_lshlrev_b32 v84, 16, v162
v_and_b32 v85, 0xffff0000, v162
v_lshlrev_b32 v86, 16, v163
v_and_b32 v87, 0xffff0000, v163
v_pk_add_f32 v[80:81], v[40:41], v[80:81]
v_pk_add_f32 v[82:83], v[42:43], v[82:83]
v_pk_add_f32 v[84:85], v[44:45], v[84:85]
v_pk_add_f32 v[86:87], v[46:47], v[86:87]
K 80,80,81
K 81,82,83
K 82,84,85
K 83,86,87
buffer_store_dwordx4 v[80:83], v20, s[20:23], s72 offen nt
s_waitcnt vmcnt(1)
W 0
Zvva 80,4,32
Zvva 84,8,32
Mvva 80,4,36
Mvva 84,8,36
Mvva 80,4,40
Mvva 84,8,40
Mvva 80,4,44
Mvva 84,8,44
Mvva 80,4,48
Mvva 84,8,48
Mvva 80,4,52
Mvva 84,8,52
Mvva 80,4,56
Mvva 84,8,56
Mvva 80,4,60
Mvva 84,8,60
s_nop 6
K 160,80,81
K 161,82,83
K 162,84,85
K 163,86,87
v_lshlrev_b32 v80, 16, v160
v_and_b32 v81, 0xffff0000, v160
v_lshlrev_b32 v82, 16, v161
v_and_b32 v83, 0xffff0000, v161
v_lshlrev_b32 v84, 16, v162
v_and_b32 v85, 0xffff0000, v162
v_lshlrev_b32 v86, 16, v163
v_and_b32 v87, 0xffff0000, v163
v_pk_add_f32 v[80:81], v[48:49], v[80:81]
v_pk_add_f32 v[82:83], v[50:51], v[82:83]
v_pk_add_f32 v[84:85], v[52:53], v[84:85]
v_pk_add_f32 v[86:87], v[54:55], v[86:87]
K 160,80,81
K 161,82,83
K 162,84,85
K 163,86,87
v_lshlrev_b32 v80, 16, v160
v_and_b32 v81, 0xffff0000, v160
v_lshlrev_b32 v82, 16, v161
v_and_b32 v83, 0xffff0000, v161
v_lshlrev_b32 v84, 16, v162
v_and_b32 v85, 0xffff0000, v162
v_lshlrev_b32 v86, 16, v163
v_and_b32 v87, 0xffff0000, v163
v_pk_add_f32 v[80:81], v[56:57], v[80:81]
v_pk_add_f32 v[82:83], v[58:59], v[82:83]
v_pk_add_f32 v[84:85], v[60:61], v[84:85]
v_pk_add_f32 v[86:87], v[62:63], v[86:87]
K 80,80,81
K 81,82,83
K 82,84,85
K 83,86,87
buffer_store_dwordx4 v[80:83], v20, s[20:23], s73 offen nt
s_branch .LL_128%=
.LL_169%=:
s_waitcnt vmcnt(0)
W 0
Zvva 80,4,32
Zvva 84,8,32
Mvva 80,4,36
Mvva 84,8,36
Mvva 80,4,40
Mvva 84,8,40
Mvva 80,4,44
Mvva 84,8,44
Mvva 80,4,48
Mvva 84,8,48
Mvva 80,4,52
Mvva 84,8,52
Mvva 80,4,56
Mvva 84,8,56
Mvva 80,4,60
Mvva 84,8,60
s_nop 6
K 160,80,81
K 161,82,83
K 162,84,85
K 163,86,87
v_lshlrev_b32 v80, 16, v160
v_and_b32 v81, 0xffff0000, v160
v_lshlrev_b32 v82, 16, v161
v_and_b32 v83, 0xffff0000, v161
v_lshlrev_b32 v84, 16, v162
v_and_b32 v85, 0xffff0000, v162
v_lshlrev_b32 v86, 16, v163
v_and_b32 v87, 0xffff0000, v163
v_pk_add_f32 v[80:81], v[48:49], v[80:81]
v_pk_add_f32 v[82:83], v[50:51], v[82:83]
v_pk_add_f32 v[84:85], v[52:53], v[84:85]
v_pk_add_f32 v[86:87], v[54:55], v[86:87]
K 160,80,81
K 161,82,83
K 162,84,85
K 163,86,87
v_lshlrev_b32 v80, 16, v160
v_and_b32 v81, 0xffff0000, v160
v_lshlrev_b32 v82, 16, v161
v_and_b32 v83, 0xffff0000, v161
v_lshlrev_b32 v84, 16, v162
v_and_b32 v85, 0xffff0000, v162
v_lshlrev_b32 v86, 16, v163
v_and_b32 v87, 0xffff0000, v163
v_pk_add_f32 v[80:81], v[56:57], v[80:81]
v_pk_add_f32 v[82:83], v[58:59], v[82:83]
v_pk_add_f32 v[84:85], v[60:61], v[84:85]
v_pk_add_f32 v[86:87], v[62:63], v[86:87]
K 80,80,81
K 81,82,83
K 82,84,85
K 83,86,87
buffer_store_dwordx4 v[80:83], v20, s[20:23], s73 offen nt
s_branch .LL_128%=
.LL_129%=:
s_mov_b32 s84, 0
.LL_171%=:
s_or_b32 s70, s71, s66
s_andn2_b32 s69, s67, s71
s_and_b32 s69, s69, 0xffff
s_cbranch_scc0 .LL_174%=
s_and_b32 s80, s69, 255
s_cbranch_scc0 .LL_175%=
s_bitcmp1_b32 s69, 0
s_cbranch_scc0 .LL_176%=
s_cmp_eq_u32 s59, 0
s_cbranch_scc0 .LL_177%=
Ra 0,21,0
Ra 216,1,4096
s_branch .LL_176%=
.LL_177%=:
buffer_load_dwordx4 a[0:3], v1, s[8:11], s78 offen offset:0 sc1
buffer_load_dwordx4 a[216:219], v1, s[8:11], s65 offen offset:0 sc1
.LL_176%=:
s_bitcmp1_b32 s69, 1
s_cbranch_scc0 .LL_178%=
s_cmp_eq_u32 s59, 1
s_cbranch_scc0 .LL_179%=
Ra 4,21,0
Ra 220,1,4096
s_branch .LL_178%=
.LL_179%=:
buffer_load_dwordx4 a[4:7], v1, s[8:11], s78 offen offset:1024 sc1
buffer_load_dwordx4 a[220:223], v1, s[8:11], s65 offen offset:1024 sc1
.LL_178%=:
s_bitcmp1_b32 s69, 2
s_cbranch_scc0 .LL_180%=
s_cmp_eq_u32 s59, 2
s_cbranch_scc0 .LL_181%=
Ra 8,21,0
Ra 224,1,4096
s_branch .LL_180%=
.LL_181%=:
buffer_load_dwordx4 a[8:11], v1, s[8:11], s78 offen offset:2048 sc1
buffer_load_dwordx4 a[224:227], v1, s[8:11], s65 offen offset:2048 sc1
.LL_180%=:
s_bitcmp1_b32 s69, 3
s_cbranch_scc0 .LL_182%=
s_cmp_eq_u32 s59, 3
s_cbranch_scc0 .LL_183%=
Ra 12,21,0
Ra 228,1,4096
s_branch .LL_182%=
.LL_183%=:
buffer_load_dwordx4 a[12:15], v1, s[8:11], s78 offen offset:3072 sc1
buffer_load_dwordx4 a[228:231], v1, s[8:11], s65 offen offset:3072 sc1
.LL_182%=:
s_bitcmp1_b32 s69, 4
s_cbranch_scc0 .LL_184%=
s_cmp_eq_u32 s59, 4
s_cbranch_scc0 .LL_185%=
Ra 16,21,0
Ra 232,1,4096
s_branch .LL_184%=
.LL_185%=:
buffer_load_dwordx4 a[16:19], v19, s[8:11], s78 offen offset:0 sc1
buffer_load_dwordx4 a[232:235], v19, s[8:11], s65 offen offset:0 sc1
.LL_184%=:
s_bitcmp1_b32 s69, 5
s_cbranch_scc0 .LL_186%=
s_cmp_eq_u32 s59, 5
s_cbranch_scc0 .LL_187%=
Ra 20,21,0
Ra 236,1,4096
s_branch .LL_186%=
.LL_187%=:
buffer_load_dwordx4 a[20:23], v19, s[8:11], s78 offen offset:1024 sc1
buffer_load_dwordx4 a[236:239], v19, s[8:11], s65 offen offset:1024 sc1
.LL_186%=:
s_bitcmp1_b32 s69, 6
s_cbranch_scc0 .LL_188%=
s_cmp_eq_u32 s59, 6
s_cbranch_scc0 .LL_189%=
Ra 24,21,0
Ra 240,1,4096
s_branch .LL_188%=
.LL_189%=:
buffer_load_dwordx4 a[24:27], v19, s[8:11], s78 offen offset:2048 sc1
buffer_load_dwordx4 a[240:243], v19, s[8:11], s65 offen offset:2048 sc1
.LL_188%=:
s_bitcmp1_b32 s69, 7
s_cbranch_scc0 .LL_190%=
s_cmp_eq_u32 s59, 7
s_cbranch_scc0 .LL_191%=
Ra 28,21,0
Ra 244,1,4096
s_branch .LL_190%=
.LL_191%=:
buffer_load_dwordx4 a[28:31], v19, s[8:11], s78 offen offset:3072 sc1
buffer_load_dwordx4 a[244:247], v19, s[8:11], s65 offen offset:3072 sc1
.LL_190%=:
.LL_175%=:
s_and_b32 s80, s69, 65280
s_cbranch_scc0 .LL_192%=
s_bitcmp1_b32 s69, 8
s_cbranch_scc0 .LL_193%=
s_cmp_eq_u32 s59, 0
s_cbranch_scc0 .LL_194%=
Ra 32,21,5120
s_branch .LL_193%=
.LL_194%=:
buffer_load_dwordx4 a[32:35], v1, s[8:11], s79 offen offset:0 sc1
.LL_193%=:
s_bitcmp1_b32 s69, 9
s_cbranch_scc0 .LL_195%=
s_cmp_eq_u32 s59, 1
s_cbranch_scc0 .LL_196%=
Ra 36,21,5120
s_branch .LL_195%=
.LL_196%=:
buffer_load_dwordx4 a[36:39], v1, s[8:11], s79 offen offset:1024 sc1
.LL_195%=:
s_bitcmp1_b32 s69, 10
s_cbranch_scc0 .LL_197%=
s_cmp_eq_u32 s59, 2
s_cbranch_scc0 .LL_198%=
Ra 40,21,5120
s_branch .LL_197%=
.LL_198%=:
buffer_load_dwordx4 a[40:43], v1, s[8:11], s79 offen offset:2048 sc1
.LL_197%=:
s_bitcmp1_b32 s69, 11
s_cbranch_scc0 .LL_199%=
s_cmp_eq_u32 s59, 3
s_cbranch_scc0 .LL_200%=
Ra 44,21,5120
s_branch .LL_199%=
.LL_200%=:
buffer_load_dwordx4 a[44:47], v1, s[8:11], s79 offen offset:3072 sc1
.LL_199%=:
s_bitcmp1_b32 s69, 12
s_cbranch_scc0 .LL_201%=
s_cmp_eq_u32 s59, 4
s_cbranch_scc0 .LL_202%=
Ra 48,21,5120
s_branch .LL_201%=
.LL_202%=:
buffer_load_dwordx4 a[48:51], v19, s[8:11], s79 offen offset:0 sc1
.LL_201%=:
s_bitcmp1_b32 s69, 13
s_cbranch_scc0 .LL_203%=
s_cmp_eq_u32 s59, 5
s_cbranch_scc0 .LL_204%=
Ra 52,21,5120
s_branch .LL_203%=
.LL_204%=:
buffer_load_dwordx4 a[52:55], v19, s[8:11], s79 offen offset:1024 sc1
.LL_203%=:
s_bitcmp1_b32 s69, 14
s_cbranch_scc0 .LL_205%=
s_cmp_eq_u32 s59, 6
s_cbranch_scc0 .LL_206%=
Ra 56,21,5120
s_branch .LL_205%=
.LL_206%=:
buffer_load_dwordx4 a[56:59], v19, s[8:11], s79 offen offset:2048 sc1
.LL_205%=:
s_bitcmp1_b32 s69, 15
s_cbranch_scc0 .LL_207%=
s_cmp_eq_u32 s59, 7
s_cbranch_scc0 .LL_208%=
Ra 60,21,5120
s_branch .LL_207%=
.LL_208%=:
buffer_load_dwordx4 a[60:63], v19, s[8:11], s79 offen offset:3072 sc1
.LL_207%=:
.LL_192%=:
s_or_b32 s71, s71, s69
.LL_174%=:
s_andn2_b32 s80, 0xffff, s71
s_cbranch_scc0 .LL_173%=
s_sleep 4
buffer_load_dwordx2 v[16:17], v18, s[8:11], s63 offen sc1
s_and_b32 s80, s70, 0x100ff
s_cmp_eq_u32 s80, 0xff
s_cbranch_scc0 .LL_172%=
W 0
Zvva 80,4,0
Zvva 84,8,0
Zvva 88,4,216
Zvva 92,8,216
Mvva 80,4,4
Mvva 84,8,4
Mvva 88,4,220
Mvva 92,8,220
Mvva 80,4,8
Mvva 84,8,8
Mvva 88,4,224
Mvva 92,8,224
Mvva 80,4,12
Mvva 84,8,12
Mvva 88,4,228
Mvva 92,8,228
Mvva 80,4,16
Mvva 84,8,16
Mvva 88,4,232
Mvva 92,8,232
Mvva 80,4,20
Mvva 84,8,20
Mvva 88,4,236
Mvva 92,8,236
Mvva 80,4,24
Mvva 84,8,24
Mvva 88,4,240
Mvva 92,8,240
Mvva 80,4,28
Mvva 84,8,28
Mvva 88,4,244
Mvva 92,8,244
s_nop 4
K 160,80,81
K 161,82,83
K 162,84,85
K 163,86,87
v_lshlrev_b32 v80, 16, v160
v_and_b32 v81, 0xffff0000, v160
v_lshlrev_b32 v82, 16, v161
v_and_b32 v83, 0xffff0000, v161
v_lshlrev_b32 v84, 16, v162
v_and_b32 v85, 0xffff0000, v162
v_lshlrev_b32 v86, 16, v163
v_and_b32 v87, 0xffff0000, v163
v_pk_add_f32 v[80:81], v[32:33], v[80:81]
v_pk_add_f32 v[82:83], v[34:35], v[82:83]
v_pk_add_f32 v[84:85], v[36:37], v[84:85]
v_pk_add_f32 v[86:87], v[38:39], v[86:87]
K 160,80,81
K 161,82,83
K 162,84,85
K 163,86,87
v_lshlrev_b32 v80, 16, v160
v_and_b32 v81, 0xffff0000, v160
v_lshlrev_b32 v82, 16, v161
v_and_b32 v83, 0xffff0000, v161
v_lshlrev_b32 v84, 16, v162
v_and_b32 v85, 0xffff0000, v162
v_lshlrev_b32 v86, 16, v163
v_and_b32 v87, 0xffff0000, v163
v_pk_add_f32 v[80:81], v[40:41], v[80:81]
v_pk_add_f32 v[82:83], v[42:43], v[82:83]
v_pk_add_f32 v[84:85], v[44:45], v[84:85]
v_pk_add_f32 v[86:87], v[46:47], v[86:87]
K 80,80,81
K 81,82,83
K 82,84,85
K 83,86,87
buffer_store_dwordx4 v[80:83], v20, s[20:23], s72 offen nt
)"
R"(K 160,88,89
K 161,90,91
K 162,92,93
K 163,94,95
v_lshlrev_b32 v88, 16, v160
v_and_b32 v89, 0xffff0000, v160
v_lshlrev_b32 v90, 16, v161
v_and_b32 v91, 0xffff0000, v161
v_lshlrev_b32 v92, 16, v162
v_and_b32 v93, 0xffff0000, v162
v_lshlrev_b32 v94, 16, v163
v_and_b32 v95, 0xffff0000, v163
v_pk_add_f32 v[88:89], v[64:65], v[88:89]
v_pk_add_f32 v[90:91], v[66:67], v[90:91]
v_pk_add_f32 v[92:93], v[68:69], v[92:93]
v_pk_add_f32 v[94:95], v[70:71], v[94:95]
K 160,88,89
K 161,90,91
K 162,92,93
K 163,94,95
v_lshlrev_b32 v88, 16, v160
v_and_b32 v89, 0xffff0000, v160
v_lshlrev_b32 v90, 16, v161
v_and_b32 v91, 0xffff0000, v161
v_lshlrev_b32 v92, 16, v162
v_and_b32 v93, 0xffff0000, v162
v_lshlrev_b32 v94, 16, v163
v_and_b32 v95, 0xffff0000, v163
v_pk_add_f32 v[88:89], v[72:73], v[88:89]
v_pk_add_f32 v[90:91], v[74:75], v[90:91]
v_pk_add_f32 v[92:93], v[76:77], v[92:93]
v_pk_add_f32 v[94:95], v[78:79], v[94:95]
K 88,88,89
K 89,90,91
K 90,92,93
K 91,94,95
buffer_store_dwordx4 v[88:91], v20, s[20:23], s74 offen nt
s_bitset1_b32 s71, 16
.LL_172%=:
s_add_u32 s84, s84, 1
s_waitcnt vmcnt(0)
v_cmp_eq_u64 vcc, s[12:13], v[16:17]
s_or_b32 s67, vcc_lo, s66
s_cmp_lt_u32 s84, 4194304
s_cbranch_scc1 .LL_171%=
.LL_173%=:
s_bitcmp1_b32 s71, 16
s_cbranch_scc1 .LL_209%=
s_and_b32 s80, s70, 0xff
s_cmp_eq_u32 s80, 0xff
s_cbranch_scc1 .LL_210%=
s_waitcnt vmcnt(0)
.LL_210%=:
W 0
Zvva 80,4,0
Zvva 84,8,0
Zvva 88,4,216
Zvva 92,8,216
Mvva 80,4,4
Mvva 84,8,4
Mvva 88,4,220
Mvva 92,8,220
Mvva 80,4,8
Mvva 84,8,8
Mvva 88,4,224
Mvva 92,8,224
Mvva 80,4,12
Mvva 84,8,12
Mvva 88,4,228
Mvva 92,8,228
Mvva 80,4,16
Mvva 84,8,16
Mvva 88,4,232
Mvva 92,8,232
Mvva 80,4,20
Mvva 84,8,20
Mvva 88,4,236
Mvva 92,8,236
Mvva 80,4,24
Mvva 84,8,24
Mvva 88,4,240
Mvva 92,8,240
Mvva 80,4,28
Mvva 84,8,28
Mvva 88,4,244
Mvva 92,8,244
s_nop 4
K 160,80,81
K 161,82,83
K 162,84,85
K 163,86,87
v_lshlrev_b32 v80, 16, v160
v_and_b32 v81, 0xffff0000, v160
v_lshlrev_b32 v82, 16, v161
v_and_b32 v83, 0xffff0000, v161
v_lshlrev_b32 v84, 16, v162
v_and_b32 v85, 0xffff0000, v162
v_lshlrev_b32 v86, 16, v163
v_and_b32 v87, 0xffff0000, v163
v_pk_add_f32 v[80:81], v[32:33], v[80:81]
v_pk_add_f32 v[82:83], v[34:35], v[82:83]
v_pk_add_f32 v[84:85], v[36:37], v[84:85]
v_pk_add_f32 v[86:87], v[38:39], v[86:87]
K 160,80,81
K 161,82,83
K 162,84,85
K 163,86,87
v_lshlrev_b32 v80, 16, v160
v_and_b32 v81, 0xffff0000, v160
v_lshlrev_b32 v82, 16, v161
v_and_b32 v83, 0xffff0000, v161
v_lshlrev_b32 v84, 16, v162
v_and_b32 v85, 0xffff0000, v162
v_lshlrev_b32 v86, 16, v163
v_and_b32 v87, 0xffff0000, v163
v_pk_add_f32 v[80:81], v[40:41], v[80:81]
v_pk_add_f32 v[82:83], v[42:43], v[82:83]
v_pk_add_f32 v[84:85], v[44:45], v[84:85]
v_pk_add_f32 v[86:87], v[46:47], v[86:87]
K 80,80,81
K 81,82,83
K 82,84,85
K 83,86,87
buffer_store_dwordx4 v[80:83], v20, s[20:23], s72 offen nt
K 160,88,89
K 161,90,91
K 162,92,93
K 163,94,95
v_lshlrev_b32 v88, 16, v160
v_and_b32 v89, 0xffff0000, v160
v_lshlrev_b32 v90, 16, v161
v_and_b32 v91, 0xffff0000, v161
v_lshlrev_b32 v92, 16, v162
v_and_b32 v93, 0xffff0000, v162
v_lshlrev_b32 v94, 16, v163
v_and_b32 v95, 0xffff0000, v163
v_pk_add_f32 v[88:89], v[64:65], v[88:89]
v_pk_add_f32 v[90:91], v[66:67], v[90:91]
v_pk_add_f32 v[92:93], v[68:69], v[92:93]
v_pk_add_f32 v[94:95], v[70:71], v[94:95]
K 160,88,89
K 161,90,91
K 162,92,93
K 163,94,95
v_lshlrev_b32 v88, 16, v160
v_and_b32 v89, 0xffff0000, v160
v_lshlrev_b32 v90, 16, v161
v_and_b32 v91, 0xffff0000, v161
v_lshlrev_b32 v92, 16, v162
v_and_b32 v93, 0xffff0000, v162
v_lshlrev_b32 v94, 16, v163
v_and_b32 v95, 0xffff0000, v163
v_pk_add_f32 v[88:89], v[72:73], v[88:89]
v_pk_add_f32 v[90:91], v[74:75], v[90:91]
v_pk_add_f32 v[92:93], v[76:77], v[92:93]
v_pk_add_f32 v[94:95], v[78:79], v[94:95]
K 88,88,89
K 89,90,91
K 90,92,93
K 91,94,95
buffer_store_dwordx4 v[88:91], v20, s[20:23], s74 offen nt
s_waitcnt vmcnt(2)
W 0
Zvva 80,4,32
Zvva 84,8,32
Mvva 80,4,36
Mvva 84,8,36
Mvva 80,4,40
Mvva 84,8,40
Mvva 80,4,44
Mvva 84,8,44
Mvva 80,4,48
Mvva 84,8,48
Mvva 80,4,52
Mvva 84,8,52
Mvva 80,4,56
Mvva 84,8,56
Mvva 80,4,60
Mvva 84,8,60
s_nop 6
K 160,80,81
K 161,82,83
K 162,84,85
K 163,86,87
v_lshlrev_b32 v80, 16, v160
v_and_b32 v81, 0xffff0000, v160
v_lshlrev_b32 v82, 16, v161
v_and_b32 v83, 0xffff0000, v161
v_lshlrev_b32 v84, 16, v162
v_and_b32 v85, 0xffff0000, v162
v_lshlrev_b32 v86, 16, v163
v_and_b32 v87, 0xffff0000, v163
v_pk_add_f32 v[80:81], v[48:49], v[80:81]
v_pk_add_f32 v[82:83], v[50:51], v[82:83]
v_pk_add_f32 v[84:85], v[52:53], v[84:85]
v_pk_add_f32 v[86:87], v[54:55], v[86:87]
K 160,80,81
K 161,82,83
K 162,84,85
K 163,86,87
v_lshlrev_b32 v80, 16, v160
v_and_b32 v81, 0xffff0000, v160
v_lshlrev_b32 v82, 16, v161
v_and_b32 v83, 0xffff0000, v161
v_lshlrev_b32 v84, 16, v162
v_and_b32 v85, 0xffff0000, v162
v_lshlrev_b32 v86, 16, v163
v_and_b32 v87, 0xffff0000, v163
v_pk_add_f32 v[80:81], v[56:57], v[80:81]
v_pk_add_f32 v[82:83], v[58:59], v[82:83]
v_pk_add_f32 v[84:85], v[60:61], v[84:85]
v_pk_add_f32 v[86:87], v[62:63], v[86:87]
K 80,80,81
K 81,82,83
K 82,84,85
K 83,86,87
buffer_store_dwordx4 v[80:83], v20, s[20:23], s73 offen nt
s_branch .LL_128%=
.LL_209%=:
s_waitcnt vmcnt(0)
W 0
Zvva 80,4,32
Zvva 84,8,32
Mvva 80,4,36
Mvva 84,8,36
Mvva 80,4,40
Mvva 84,8,40
Mvva 80,4,44
Mvva 84,8,44
Mvva 80,4,48
Mvva 84,8,48
Mvva 80,4,52
Mvva 84,8,52
Mvva 80,4,56
Mvva 84,8,56
Mvva 80,4,60
Mvva 84,8,60
s_nop 6
K 160,80,81
K 161,82,83
K 162,84,85
K 163,86,87
v_lshlrev_b32 v80, 16, v160
v_and_b32 v81, 0xffff0000, v160
v_lshlrev_b32 v82, 16, v161
v_and_b32 v83, 0xffff0000, v161
v_lshlrev_b32 v84, 16, v162
v_and_b32 v85, 0xffff0000, v162
v_lshlrev_b32 v86, 16, v163
v_and_b32 v87, 0xffff0000, v163
v_pk_add_f32 v[80:81], v[48:49], v[80:81]
v_pk_add_f32 v[82:83], v[50:51], v[82:83]
v_pk_add_f32 v[84:85], v[52:53], v[84:85]
v_pk_add_f32 v[86:87], v[54:55], v[86:87]
K 160,80,81
K 161,82,83
K 162,84,85
K 163,86,87
v_lshlrev_b32 v80, 16, v160
v_and_b32 v81, 0xffff0000, v160
v_lshlrev_b32 v82, 16, v161
v_and_b32 v83, 0xffff0000, v161
v_lshlrev_b32 v84, 16, v162
v_and_b32 v85, 0xffff0000, v162
v_lshlrev_b32 v86, 16, v163
v_and_b32 v87, 0xffff0000, v163
v_pk_add_f32 v[80:81], v[56:57], v[80:81]
v_pk_add_f32 v[82:83], v[58:59], v[82:83]
v_pk_add_f32 v[84:85], v[60:61], v[84:85]
v_pk_add_f32 v[86:87], v[62:63], v[86:87]
K 80,80,81
K 81,82,83
K 82,84,85
K 83,86,87
buffer_store_dwordx4 v[80:83], v20, s[20:23], s73 offen nt
s_branch .LL_128%=
.LL_130%=:
s_mov_b32 s84, 0
.LL_211%=:
s_or_b32 s70, s71, s66
s_andn2_b32 s69, s67, s71
s_and_b32 s69, s69, 0xffff
s_cbranch_scc0 .LL_214%=
s_and_b32 s80, s69, 255
s_cbranch_scc0 .LL_215%=
s_bitcmp1_b32 s69, 0
s_cbranch_scc0 .LL_216%=
s_cmp_eq_u32 s59, 0
s_cbranch_scc0 .LL_217%=
Ra 0,21,0
s_branch .LL_216%=
.LL_217%=:
buffer_load_dwordx4 a[0:3], v1, s[8:11], s78 offen offset:0 sc1
.LL_216%=:
s_bitcmp1_b32 s69, 1
s_cbranch_scc0 .LL_218%=
s_cmp_eq_u32 s59, 1
s_cbranch_scc0 .LL_219%=
Ra 4,21,0
s_branch .LL_218%=
.LL_219%=:
buffer_load_dwordx4 a[4:7], v1, s[8:11], s78 offen offset:1024 sc1
.LL_218%=:
s_bitcmp1_b32 s69, 2
s_cbranch_scc0 .LL_220%=
s_cmp_eq_u32 s59, 2
s_cbranch_scc0 .LL_221%=
Ra 8,21,0
s_branch .LL_220%=
.LL_221%=:
buffer_load_dwordx4 a[8:11], v1, s[8:11], s78 offen offset:2048 sc1
.LL_220%=:
s_bitcmp1_b32 s69, 3
s_cbranch_scc0 .LL_222%=
s_cmp_eq_u32 s59, 3
s_cbranch_scc0 .LL_223%=
Ra 12,21,0
s_branch .LL_222%=
.LL_223%=:
buffer_load_dwordx4 a[12:15], v1, s[8:11], s78 offen offset:3072 sc1
.LL_222%=:
s_bitcmp1_b32 s69, 4
s_cbranch_scc0 .LL_224%=
s_cmp_eq_u32 s59, 4
s_cbranch_scc0 .LL_225%=
Ra 16,21,0
s_branch .LL_224%=
.LL_225%=:
buffer_load_dwordx4 a[16:19], v19, s[8:11], s78 offen offset:0 sc1
.LL_224%=:
s_bitcmp1_b32 s69, 5
s_cbranch_scc0 .LL_226%=
s_cmp_eq_u32 s59, 5
s_cbranch_scc0 .LL_227%=
Ra 20,21,0
s_branch .LL_226%=
.LL_227%=:
buffer_load_dwordx4 a[20:23], v19, s[8:11], s78 offen offset:1024 sc1
.LL_226%=:
s_bitcmp1_b32 s69, 6
s_cbranch_scc0 .LL_228%=
s_cmp_eq_u32 s59, 6
s_cbranch_scc0 .LL_229%=
Ra 24,21,0
s_branch .LL_228%=
.LL_229%=:
buffer_load_dwordx4 a[24:27], v19, s[8:11], s78 offen offset:2048 sc1
.LL_228%=:
s_bitcmp1_b32 s69, 7
s_cbranch_scc0 .LL_230%=
s_cmp_eq_u32 s59, 7
s_cbranch_scc0 .LL_231%=
Ra 28,21,0
s_branch .LL_230%=
.LL_231%=:
buffer_load_dwordx4 a[28:31], v19, s[8:11], s78 offen offset:3072 sc1
.LL_230%=:
.LL_215%=:
s_and_b32 s80, s69, 65280
s_cbranch_scc0 .LL_232%=
s_bitcmp1_b32 s69, 8
s_cbranch_scc0 .LL_233%=
s_cmp_eq_u32 s59, 0
s_cbranch_scc0 .LL_234%=
Ra 32,21,5120
Ra 216,1,9216
s_branch .LL_233%=
.LL_234%=:
buffer_load_dwordx4 a[32:35], v1, s[8:11], s79 offen offset:0 sc1
buffer_load_dwordx4 a[216:219], v1, s[8:11], s65 offen offset:0 sc1
.LL_233%=:
s_bitcmp1_b32 s69, 9
s_cbranch_scc0 .LL_235%=
s_cmp_eq_u32 s59, 1
s_cbranch_scc0 .LL_236%=
Ra 36,21,5120
Ra 220,1,9216
s_branch .LL_235%=
.LL_236%=:
buffer_load_dwordx4 a[36:39], v1, s[8:11], s79 offen offset:1024 sc1
buffer_load_dwordx4 a[220:223], v1, s[8:11], s65 offen offset:1024 sc1
.LL_235%=:
s_bitcmp1_b32 s69, 10
s_cbranch_scc0 .LL_237%=
s_cmp_eq_u32 s59, 2
s_cbranch_scc0 .LL_238%=
Ra 40,21,5120
Ra 224,1,9216
s_branch .LL_237%=
.LL_238%=:
buffer_load_dwordx4 a[40:43], v1, s[8:11], s79 offen offset:2048 sc1
buffer_load_dwordx4 a[224:227], v1, s[8:11], s65 offen offset:2048 sc1
.LL_237%=:
s_bitcmp1_b32 s69, 11
s_cbranch_scc0 .LL_239%=
s_cmp_eq_u32 s59, 3
s_cbranch_scc0 .LL_240%=
Ra 44,21,5120
Ra 228,1,9216
s_branch .LL_239%=
)"
R"(.LL_240%=:
buffer_load_dwordx4 a[44:47], v1, s[8:11], s79 offen offset:3072 sc1
buffer_load_dwordx4 a[228:231], v1, s[8:11], s65 offen offset:3072 sc1
.LL_239%=:
s_bitcmp1_b32 s69, 12
s_cbranch_scc0 .LL_241%=
s_cmp_eq_u32 s59, 4
s_cbranch_scc0 .LL_242%=
Ra 48,21,5120
Ra 232,1,9216
s_branch .LL_241%=
.LL_242%=:
buffer_load_dwordx4 a[48:51], v19, s[8:11], s79 offen offset:0 sc1
buffer_load_dwordx4 a[232:235], v19, s[8:11], s65 offen offset:0 sc1
.LL_241%=:
s_bitcmp1_b32 s69, 13
s_cbranch_scc0 .LL_243%=
s_cmp_eq_u32 s59, 5
s_cbranch_scc0 .LL_244%=
Ra 52,21,5120
Ra 236,1,9216
s_branch .LL_243%=
.LL_244%=:
buffer_load_dwordx4 a[52:55], v19, s[8:11], s79 offen offset:1024 sc1
buffer_load_dwordx4 a[236:239], v19, s[8:11], s65 offen offset:1024 sc1
.LL_243%=:
s_bitcmp1_b32 s69, 14
s_cbranch_scc0 .LL_245%=
s_cmp_eq_u32 s59, 6
s_cbranch_scc0 .LL_246%=
Ra 56,21,5120
Ra 240,1,9216
s_branch .LL_245%=
.LL_246%=:
buffer_load_dwordx4 a[56:59], v19, s[8:11], s79 offen offset:2048 sc1
buffer_load_dwordx4 a[240:243], v19, s[8:11], s65 offen offset:2048 sc1
.LL_245%=:
s_bitcmp1_b32 s69, 15
s_cbranch_scc0 .LL_247%=
s_cmp_eq_u32 s59, 7
s_cbranch_scc0 .LL_248%=
Ra 60,21,5120
Ra 244,1,9216
s_branch .LL_247%=
.LL_248%=:
buffer_load_dwordx4 a[60:63], v19, s[8:11], s79 offen offset:3072 sc1
buffer_load_dwordx4 a[244:247], v19, s[8:11], s65 offen offset:3072 sc1
.LL_247%=:
.LL_232%=:
s_or_b32 s71, s71, s69
.LL_214%=:
s_andn2_b32 s80, 0xffff, s71
s_cbranch_scc0 .LL_213%=
s_sleep 4
buffer_load_dwordx2 v[16:17], v18, s[8:11], s63 offen sc1
s_and_b32 s80, s70, 0x100ff
s_cmp_eq_u32 s80, 0xff
s_cbranch_scc0 .LL_212%=
W 0
Zvva 80,4,0
Zvva 84,8,0
Mvva 80,4,4
Mvva 84,8,4
Mvva 80,4,8
Mvva 84,8,8
Mvva 80,4,12
Mvva 84,8,12
Mvva 80,4,16
Mvva 84,8,16
Mvva 80,4,20
Mvva 84,8,20
Mvva 80,4,24
Mvva 84,8,24
Mvva 80,4,28
Mvva 84,8,28
s_nop 6
K 160,80,81
K 161,82,83
K 162,84,85
K 163,86,87
v_lshlrev_b32 v80, 16, v160
v_and_b32 v81, 0xffff0000, v160
v_lshlrev_b32 v82, 16, v161
v_and_b32 v83, 0xffff0000, v161
v_lshlrev_b32 v84, 16, v162
v_and_b32 v85, 0xffff0000, v162
v_lshlrev_b32 v86, 16, v163
v_and_b32 v87, 0xffff0000, v163
v_pk_add_f32 v[80:81], v[32:33], v[80:81]
v_pk_add_f32 v[82:83], v[34:35], v[82:83]
v_pk_add_f32 v[84:85], v[36:37], v[84:85]
v_pk_add_f32 v[86:87], v[38:39], v[86:87]
K 160,80,81
K 161,82,83
K 162,84,85
K 163,86,87
v_lshlrev_b32 v80, 16, v160
v_and_b32 v81, 0xffff0000, v160
v_lshlrev_b32 v82, 16, v161
v_and_b32 v83, 0xffff0000, v161
v_lshlrev_b32 v84, 16, v162
v_and_b32 v85, 0xffff0000, v162
v_lshlrev_b32 v86, 16, v163
v_and_b32 v87, 0xffff0000, v163
v_pk_add_f32 v[80:81], v[40:41], v[80:81]
v_pk_add_f32 v[82:83], v[42:43], v[82:83]
v_pk_add_f32 v[84:85], v[44:45], v[84:85]
v_pk_add_f32 v[86:87], v[46:47], v[86:87]
K 80,80,81
K 81,82,83
K 82,84,85
K 83,86,87
buffer_store_dwordx4 v[80:83], v20, s[20:23], s72 offen nt
s_bitset1_b32 s71, 16
.LL_212%=:
s_add_u32 s84, s84, 1
s_waitcnt vmcnt(0)
v_cmp_eq_u64 vcc, s[12:13], v[16:17]
s_or_b32 s67, vcc_lo, s66
s_cmp_lt_u32 s84, 4194304
s_cbranch_scc1 .LL_211%=
.LL_213%=:
s_bitcmp1_b32 s71, 16
s_cbranch_scc1 .LL_249%=
s_and_b32 s80, s70, 0xff
s_cmp_eq_u32 s80, 0xff
s_cbranch_scc1 .LL_250%=
s_waitcnt vmcnt(0)
.LL_250%=:
W 0
Zvva 80,4,0
Zvva 84,8,0
Mvva 80,4,4
Mvva 84,8,4
Mvva 80,4,8
Mvva 84,8,8
Mvva 80,4,12
Mvva 84,8,12
Mvva 80,4,16
Mvva 84,8,16
Mvva 80,4,20
Mvva 84,8,20
Mvva 80,4,24
Mvva 84,8,24
Mvva 80,4,28
Mvva 84,8,28
s_nop 6
K 160,80,81
K 161,82,83
K 162,84,85
K 163,86,87
v_lshlrev_b32 v80, 16, v160
v_and_b32 v81, 0xffff0000, v160
v_lshlrev_b32 v82, 16, v161
v_and_b32 v83, 0xffff0000, v161
v_lshlrev_b32 v84, 16, v162
v_and_b32 v85, 0xffff0000, v162
v_lshlrev_b32 v86, 16, v163
v_and_b32 v87, 0xffff0000, v163
v_pk_add_f32 v[80:81], v[32:33], v[80:81]
v_pk_add_f32 v[82:83], v[34:35], v[82:83]
v_pk_add_f32 v[84:85], v[36:37], v[84:85]
v_pk_add_f32 v[86:87], v[38:39], v[86:87]
K 160,80,81
K 161,82,83
K 162,84,85
K 163,86,87
v_lshlrev_b32 v80, 16, v160
v_and_b32 v81, 0xffff0000, v160
v_lshlrev_b32 v82, 16, v161
v_and_b32 v83, 0xffff0000, v161
v_lshlrev_b32 v84, 16, v162
v_and_b32 v85, 0xffff0000, v162
v_lshlrev_b32 v86, 16, v163
v_and_b32 v87, 0xffff0000, v163
v_pk_add_f32 v[80:81], v[40:41], v[80:81]
v_pk_add_f32 v[82:83], v[42:43], v[82:83]
v_pk_add_f32 v[84:85], v[44:45], v[84:85]
v_pk_add_f32 v[86:87], v[46:47], v[86:87]
K 80,80,81
K 81,82,83
K 82,84,85
K 83,86,87
buffer_store_dwordx4 v[80:83], v20, s[20:23], s72 offen nt
s_waitcnt vmcnt(1)
W 0
Zvva 80,4,32
Zvva 84,8,32
Zvva 88,4,216
Zvva 92,8,216
Mvva 80,4,36
Mvva 84,8,36
Mvva 88,4,220
Mvva 92,8,220
Mvva 80,4,40
Mvva 84,8,40
Mvva 88,4,224
Mvva 92,8,224
Mvva 80,4,44
Mvva 84,8,44
Mvva 88,4,228
Mvva 92,8,228
Mvva 80,4,48
Mvva 84,8,48
Mvva 88,4,232
Mvva 92,8,232
Mvva 80,4,52
Mvva 84,8,52
Mvva 88,4,236
Mvva 92,8,236
Mvva 80,4,56
Mvva 84,8,56
Mvva 88,4,240
Mvva 92,8,240
Mvva 80,4,60
Mvva 84,8,60
Mvva 88,4,244
Mvva 92,8,244
s_nop 4
K 160,80,81
K 161,82,83
K 162,84,85
K 163,86,87
v_lshlrev_b32 v80, 16, v160
v_and_b32 v81, 0xffff0000, v160
v_lshlrev_b32 v82, 16, v161
v_and_b32 v83, 0xffff0000, v161
v_lshlrev_b32 v84, 16, v162
v_and_b32 v85, 0xffff0000, v162
v_lshlrev_b32 v86, 16, v163
v_and_b32 v87, 0xffff0000, v163
v_pk_add_f32 v[80:81], v[48:49], v[80:81]
v_pk_add_f32 v[82:83], v[50:51], v[82:83]
v_pk_add_f32 v[84:85], v[52:53], v[84:85]
v_pk_add_f32 v[86:87], v[54:55], v[86:87]
K 160,80,81
K 161,82,83
K 162,84,85
K 163,86,87
v_lshlrev_b32 v80, 16, v160
v_and_b32 v81, 0xffff0000, v160
v_lshlrev_b32 v82, 16, v161
v_and_b32 v83, 0xffff0000, v161
v_lshlrev_b32 v84, 16, v162
v_and_b32 v85, 0xffff0000, v162
v_lshlrev_b32 v86, 16, v163
v_and_b32 v87, 0xffff0000, v163
v_pk_add_f32 v[80:81], v[56:57], v[80:81]
v_pk_add_f32 v[82:83], v[58:59], v[82:83]
v_pk_add_f32 v[84:85], v[60:61], v[84:85]
v_pk_add_f32 v[86:87], v[62:63], v[86:87]
K 80,80,81
K 81,82,83
K 82,84,85
K 83,86,87
buffer_store_dwordx4 v[80:83], v20, s[20:23], s73 offen nt
K 160,88,89
K 161,90,91
K 162,92,93
K 163,94,95
v_lshlrev_b32 v88, 16, v160
v_and_b32 v89, 0xffff0000, v160
v_lshlrev_b32 v90, 16, v161
v_and_b32 v91, 0xffff0000, v161
v_lshlrev_b32 v92, 16, v162
v_and_b32 v93, 0xffff0000, v162
v_lshlrev_b32 v94, 16, v163
v_and_b32 v95, 0xffff0000, v163
v_pk_add_f32 v[88:89], v[64:65], v[88:89]
v_pk_add_f32 v[90:91], v[66:67], v[90:91]
v_pk_add_f32 v[92:93], v[68:69], v[92:93]
v_pk_add_f32 v[94:95], v[70:71], v[94:95]
K 160,88,89
K 161,90,91
K 162,92,93
K 163,94,95
v_lshlrev_b32 v88, 16, v160
v_and_b32 v89, 0xffff0000, v160
v_lshlrev_b32 v90, 16, v161
v_and_b32 v91, 0xffff0000, v161
v_lshlrev_b32 v92, 16, v162
v_and_b32 v93, 0xffff0000, v162
v_lshlrev_b32 v94, 16, v163
v_and_b32 v95, 0xffff0000, v163
v_pk_add_f32 v[88:89], v[72:73], v[88:89]
v_pk_add_f32 v[90:91], v[74:75], v[90:91]
v_pk_add_f32 v[92:93], v[76:77], v[92:93]
v_pk_add_f32 v[94:95], v[78:79], v[94:95]
K 88,88,89
K 89,90,91
K 90,92,93
K 91,94,95
buffer_store_dwordx4 v[88:91], v20, s[20:23], s74 offen nt
s_branch .LL_128%=
.LL_249%=:
s_waitcnt vmcnt(0)
W 0
Zvva 80,4,32
Zvva 84,8,32
Zvva 88,4,216
Zvva 92,8,216
Mvva 80,4,36
Mvva 84,8,36
Mvva 88,4,220
Mvva 92,8,220
Mvva 80,4,40
Mvva 84,8,40
Mvva 88,4,224
Mvva 92,8,224
Mvva 80,4,44
Mvva 84,8,44
Mvva 88,4,228
Mvva 92,8,228
Mvva 80,4,48
Mvva 84,8,48
Mvva 88,4,232
Mvva 92,8,232
Mvva 80,4,52
Mvva 84,8,52
Mvva 88,4,236
Mvva 92,8,236
Mvva 80,4,56
Mvva 84,8,56
Mvva 88,4,240
Mvva 92,8,240
Mvva 80,4,60
Mvva 84,8,60
Mvva 88,4,244
Mvva 92,8,244
s_nop 4
K 160,80,81
K 161,82,83
K 162,84,85
K 163,86,87
v_lshlrev_b32 v80, 16, v160
v_and_b32 v81, 0xffff0000, v160
v_lshlrev_b32 v82, 16, v161
v_and_b32 v83, 0xffff0000, v161
v_lshlrev_b32 v84, 16, v162
v_and_b32 v85, 0xffff0000, v162
v_lshlrev_b32 v86, 16, v163
v_and_b32 v87, 0xffff0000, v163
v_pk_add_f32 v[80:81], v[48:49], v[80:81]
v_pk_add_f32 v[82:83], v[50:51], v[82:83]
v_pk_add_f32 v[84:85], v[52:53], v[84:85]
v_pk_add_f32 v[86:87], v[54:55], v[86:87]
K 160,80,81
K 161,82,83
K 162,84,85
K 163,86,87
v_lshlrev_b32 v80, 16, v160
v_and_b32 v81, 0xffff0000, v160
v_lshlrev_b32 v82, 16, v161
v_and_b32 v83, 0xffff0000, v161
v_lshlrev_b32 v84, 16, v162
v_and_b32 v85, 0xffff0000, v162
v_lshlrev_b32 v86, 16, v163
v_and_b32 v87, 0xffff0000, v163
v_pk_add_f32 v[80:81], v[56:57], v[80:81]
v_pk_add_f32 v[82:83], v[58:59], v[82:83]
v_pk_add_f32 v[84:85], v[60:61], v[84:85]
v_pk_add_f32 v[86:87], v[62:63], v[86:87]
K 80,80,81
K 81,82,83
K 82,84,85
K 83,86,87
buffer_store_dwordx4 v[80:83], v20, s[20:23], s73 offen nt
K 160,88,89
K 161,90,91
K 162,92,93
K 163,94,95
v_lshlrev_b32 v88, 16, v160
v_and_b32 v89, 0xffff0000, v160
v_lshlrev_b32 v90, 16, v161
v_and_b32 v91, 0xffff0000, v161
v_lshlrev_b32 v92, 16, v162
v_and_b32 v93, 0xffff0000, v162
v_lshlrev_b32 v94, 16, v163
v_and_b32 v95, 0xffff0000, v163
v_pk_add_f32 v[88:89], v[64:65], v[88:89]
v_pk_add_f32 v[90:91], v[66:67], v[90:91]
v_pk_add_f32 v[92:93], v[68:69], v[92:93]
v_pk_add_f32 v[94:95], v[70:71], v[94:95]
K 160,88,89
K 161,90,91
K 162,92,93
K 163,94,95
v_lshlrev_b32 v88, 16, v160
v_and_b32 v89, 0xffff0000, v160
v_lshlrev_b32 v90, 16, v161
v_and_b32 v91, 0xffff0000, v161
v_lshlrev_b32 v92, 16, v162
v_and_b32 v93, 0xffff0000, v162
v_lshlrev_b32 v94, 16, v163
v_and_b32 v95, 0xffff0000, v163
v_pk_add_f32 v[88:89], v[72:73], v[88:89]
v_pk_add_f32 v[90:91], v[74:75], v[90:91]
v_pk_add_f32 v[92:93], v[76:77], v[92:93]
v_pk_add_f32 v[94:95], v[78:79], v[94:95]
K 88,88,89
)"
R"(K 89,90,91
K 90,92,93
K 91,94,95
buffer_store_dwordx4 v[88:91], v20, s[20:23], s74 offen nt
.LL_128%=:
s_lshl_b32 s80, s15, 2
s_add_u32 s80, s80, s14
s_cmp_eq_u32 s80, 4
s_cbranch_scc0 .LL_251%=
v_mov_b64 v[28:29], s[12:13]
s_mov_b64 exec, 1
buffer_store_dwordx2 v[28:29], off, s[8:11], 0 sc1
s_mov_b64 exec, -1
.LL_251%=:
s_branch .Lend_252%=
.Lup_121%=:
s_waitcnt vmcnt(3)
s_mov_b32 s84, 0
.LL_123%=:
v_min3_u32 v176, v101, v103, v109
v_max3_u32 v177, v101, v103, v109
v_min3_u32 v176, v176, v111, v117
v_max3_u32 v177, v177, v111, v117
v_min3_u32 v176, v176, v119, v125
v_max3_u32 v177, v177, v119, v125
v_min_u32 v176, v176, v127
v_max_u32 v177, v177, v127
v_cmp_eq_u32 vcc, s13, v176
v_cmp_eq_u32_e64 s[86:87], s13, v177
s_and_b64 vcc, vcc, s[86:87]
s_cmp_eq_u64 vcc, -1
s_cbranch_scc1 .LL_124%=
s_add_u32 s84, s84, 1
s_cmp_lt_u32 s84, 1048576
s_cbranch_scc1 .LL_125%=
.LL_124%=:
v_readlane_b32 s70, v12, s69
v_bfe_u32 v2, v1, 4, 4
s_nop 2
s_lshr_b32 s80, s70, 4
s_and_b32 s70, s70, 15
v_cmp_eq_u32 vcc, s70, v2
s_cmp_eq_u32 s80, s61
s_cselect_b64 s[86:87], vcc, 0
s_cselect_b64 s[88:89], 0, vcc
s_bitcmp1_b32 s14, 0
s_cselect_b64 vcc, s[88:89], s[86:87]
v_lshlrev_b32 v176, 16, v100
v_and_b32 v177, 0xffff0000, v100
v_lshlrev_b32 v178, 16, v102
v_and_b32 v179, 0xffff0000, v102
v_lshlrev_b32 v180, 16, v108
v_and_b32 v181, 0xffff0000, v108
v_lshlrev_b32 v182, 16, v110
v_and_b32 v183, 0xffff0000, v110
A 176,176,40
A 177,177,41
A 178,178,42
A 179,179,43
A 180,180,44
A 181,181,45
A 182,182,46
A 183,183,47
K 184,176,177
K 185,178,179
K 186,180,181
K 187,182,183
v_lshlrev_b32 v176, 16, v184
v_and_b32 v177, 0xffff0000, v184
v_lshlrev_b32 v178, 16, v185
v_and_b32 v179, 0xffff0000, v185
v_lshlrev_b32 v180, 16, v186
v_and_b32 v181, 0xffff0000, v186
v_lshlrev_b32 v182, 16, v187
v_and_b32 v183, 0xffff0000, v187
v_cndmask_b32_e64 v40, v40, v176, s[86:87]
v_cndmask_b32_e64 v41, v41, v177, s[86:87]
v_cndmask_b32_e64 v42, v42, v178, s[86:87]
v_cndmask_b32_e64 v43, v43, v179, s[86:87]
v_cndmask_b32_e64 v44, v44, v180, s[86:87]
v_cndmask_b32_e64 v45, v45, v181, s[86:87]
v_cndmask_b32_e64 v46, v46, v182, s[86:87]
v_cndmask_b32_e64 v47, v47, v183, s[86:87]
v_lshlrev_b32 v176, 16, v100
v_and_b32 v177, 0xffff0000, v100
v_lshlrev_b32 v178, 16, v102
v_and_b32 v179, 0xffff0000, v102
v_lshlrev_b32 v180, 16, v108
v_and_b32 v181, 0xffff0000, v108
v_lshlrev_b32 v182, 16, v110
v_and_b32 v183, 0xffff0000, v110
A 176,176,56
A 177,177,57
A 178,178,58
A 179,179,59
A 180,180,60
A 181,181,61
A 182,182,62
A 183,183,63
K 184,176,177
K 185,178,179
K 186,180,181
K 187,182,183
v_lshlrev_b32 v176, 16, v184
v_and_b32 v177, 0xffff0000, v184
v_lshlrev_b32 v178, 16, v185
v_and_b32 v179, 0xffff0000, v185
v_lshlrev_b32 v180, 16, v186
v_and_b32 v181, 0xffff0000, v186
v_lshlrev_b32 v182, 16, v187
v_and_b32 v183, 0xffff0000, v187
v_cndmask_b32_e64 v56, v56, v176, s[88:89]
v_cndmask_b32_e64 v57, v57, v177, s[88:89]
v_cndmask_b32_e64 v58, v58, v178, s[88:89]
v_cndmask_b32_e64 v59, v59, v179, s[88:89]
v_cndmask_b32_e64 v60, v60, v180, s[88:89]
v_cndmask_b32_e64 v61, v61, v181, s[88:89]
v_cndmask_b32_e64 v62, v62, v182, s[88:89]
v_cndmask_b32_e64 v63, v63, v183, s[88:89]
v_lshlrev_b32 v176, 16, v116
v_and_b32 v177, 0xffff0000, v116
v_lshlrev_b32 v178, 16, v118
v_and_b32 v179, 0xffff0000, v118
v_lshlrev_b32 v180, 16, v124
v_and_b32 v181, 0xffff0000, v124
v_lshlrev_b32 v182, 16, v126
v_and_b32 v183, 0xffff0000, v126
A 176,176,72
A 177,177,73
A 178,178,74
A 179,179,75
A 180,180,76
A 181,181,77
A 182,182,78
A 183,183,79
K 184,176,177
K 185,178,179
K 186,180,181
K 187,182,183
v_lshlrev_b32 v176, 16, v184
v_and_b32 v177, 0xffff0000, v184
v_lshlrev_b32 v178, 16, v185
v_and_b32 v179, 0xffff0000, v185
v_lshlrev_b32 v180, 16, v186
v_and_b32 v181, 0xffff0000, v186
v_lshlrev_b32 v182, 16, v187
v_and_b32 v183, 0xffff0000, v187
v_cndmask_b32_e64 v72, v72, v176, vcc
v_cndmask_b32_e64 v73, v73, v177, vcc
v_cndmask_b32_e64 v74, v74, v178, vcc
v_cndmask_b32_e64 v75, v75, v179, vcc
v_cndmask_b32_e64 v76, v76, v180, vcc
v_cndmask_b32_e64 v77, v77, v181, vcc
v_cndmask_b32_e64 v78, v78, v182, vcc
v_cndmask_b32_e64 v79, v79, v183, vcc
s_bitset0_b32 s68, s69
s_cmp_eq_u32 s68, 0
s_cbranch_scc1 .Lupr_122%=
s_ff1_i32_b32 s69, s68
.LL_125%=:
s_cmp_eq_u32 s64, 0
s_cbranch_scc0 .LL_126%=
s_waitcnt vmcnt(0)
v_lshlrev_b32 v2, 2, v1
v_mov_b64 v[28:29], s[12:13]
s_mov_b64 exec, 3
buffer_store_dwordx2 v[28:29], v2, s[8:11], s53 offen offset:512 sc1
s_mov_b64 exec, -1
s_mov_b32 s64, 1
.LL_126%=:
s_mul_i32 s66, s69, 20
s_mul_i32 s67, s58, 5
s_add_u32 s66, s66, s67
s_lshl_b32 s66, s66, 7
s_add_u32 s66, s66, 98304
s_lshl_b32 s67, s14, 7
s_add_u32 s67, s67, s66
buffer_load_dwordx4 v[100:103], v3, s[8:11], s67 offen offset:0 sc1
buffer_load_dwordx4 v[108:111], v3, s[8:11], s67 offen offset:16 sc1
buffer_load_dwordx4 v[116:119], v3, s[8:11], s66 offen offset:512 sc1
buffer_load_dwordx4 v[124:127], v3, s[8:11], s66 offen offset:528 sc1
s_waitcnt vmcnt(0)
s_branch .LL_123%=
.Lspin_56%=:
s_mov_b32 s84, 0
.Llp_58%=:
s_sleep 2
v_mov_b32 v28, s74
buffer_load_dwordx2 v[28:29], v28, s[8:11], 0 offen offset:3584 sc1
s_waitcnt vmcnt(0)
s_add_u32 s84, s84, 1
v_xor_b32 v28, s12, v28
v_xor_b32 v29, s13, v29
v_lshrrev_b32 v248, 11, v28
v_or_b32 v29, v29, v248
v_cmp_eq_u32 vcc, 0, v29
s_or_b64 vcc, vcc, s[72:73]
s_cmp_eq_u64 vcc, -1
s_cbranch_scc1 .Lok_57%=
s_cmp_lt_u32 s84, 1048576
s_cbranch_scc1 .Llp_58%=
s_branch .Lok_57%=
.Lspin_61%=:
s_mov_b32 s84, 0
.Llp_63%=:
s_sleep 2
v_bfe_u32 v28, v1, 4, 2
v_lshlrev_b32 v28, 3, v28
s_lshl_b32 s80, s56, 9
s_add_u32 s80, s80, 4096
buffer_load_dwordx2 v[28:29], v28, s[8:11], s80 offen sc1
s_waitcnt vmcnt(0)
s_add_u32 s84, s84, 1
v_cmp_eq_u64 vcc, s[12:13], v[28:29]
s_cmp_eq_u64 vcc, -1
s_cbranch_scc1 .Lok_62%=
s_cmp_lt_u32 s84, 1048576
s_cbranch_scc1 .Llp_63%=
s_branch .Lok_62%=
.Lspin_64%=:
s_mov_b32 s84, 0
.Llp_66%=:
s_sleep 2
v_bfe_u32 v28, v1, 4, 4
v_lshlrev_b32 v28, 3, v28
s_lshl_b32 s80, s56, 9
s_add_u32 s80, s80, 4096
buffer_load_dwordx2 v[28:29], v28, s[8:11], s80 offen sc1
s_waitcnt vmcnt(0)
s_add_u32 s84, s84, 1
v_cmp_eq_u64 vcc, s[12:13], v[28:29]
s_cmp_eq_u64 vcc, -1
s_cbranch_scc1 .Lok_65%=
s_cmp_lt_u32 s84, 1048576
s_cbranch_scc1 .Llp_66%=
s_branch .Lok_65%=
.Lspin_67%=:
s_mov_b32 s84, 0
.Llp_69%=:
s_sleep 2
v_bfe_u32 v28, v1, 4, 4
v_lshlrev_b32 v28, 3, v28
s_lshl_b32 s80, s56, 9
s_add_u32 s80, s80, 4224
buffer_load_dwordx2 v[28:29], v28, s[8:11], s80 offen sc1
s_waitcnt vmcnt(0)
s_add_u32 s84, s84, 1
v_cmp_eq_u64 vcc, s[12:13], v[28:29]
s_cmp_eq_u64 vcc, -1
s_cbranch_scc1 .Lok_68%=
s_cmp_lt_u32 s84, 1048576
s_cbranch_scc1 .Llp_69%=
s_branch .Lok_68%=
.Lspin_70%=:
s_mov_b32 s84, 0
.Llp_72%=:
s_sleep 2
v_bfe_u32 v28, v1, 4, 4
v_lshlrev_b32 v28, 3, v28
s_lshl_b32 s80, s56, 9
s_add_u32 s80, s80, 4352
buffer_load_dwordx2 v[28:29], v28, s[8:11], s80 offen sc1
s_waitcnt vmcnt(0)
s_add_u32 s84, s84, 1
v_cmp_eq_u64 vcc, s[12:13], v[28:29]
s_cmp_eq_u64 vcc, -1
s_cbranch_scc1 .Lok_71%=
s_cmp_lt_u32 s84, 1048576
s_cbranch_scc1 .Llp_72%=
s_branch .Lok_71%=
.Lspin_73%=:
s_mov_b32 s84, 0
.Llp_75%=:
s_sleep 2
v_bfe_u32 v28, v1, 4, 4
v_lshlrev_b32 v28, 3, v28
s_lshl_b32 s80, s56, 9
s_add_u32 s80, s80, 4480
buffer_load_dwordx2 v[28:29], v28, s[8:11], s80 offen sc1
s_waitcnt vmcnt(0)
s_add_u32 s84, s84, 1
v_cmp_eq_u64 vcc, s[12:13], v[28:29]
s_cmp_eq_u64 vcc, -1
s_cbranch_scc1 .Lok_74%=
s_cmp_lt_u32 s84, 1048576
s_cbranch_scc1 .Llp_75%=
s_branch .Lok_74%=
.Lspin_76%=:
s_mov_b32 s84, 0
.Llp_78%=:
s_sleep 2
buffer_load_dwordx2 v[8:9], v4, s[8:11], 0 offen offset:2560 sc1
buffer_load_dwordx2 v[10:11], v5, s[8:11], 0 offen offset:3072 sc1
buffer_load_dwordx2 v[12:13], v6, s[8:11], 0 offen offset:3584 sc1
s_waitcnt vmcnt(0)
s_add_u32 s84, s84, 1
v_cmp_eq_u64 vcc, s[12:13], v[8:9]
v_cmp_eq_u64_e64 s[86:87], s[12:13], v[10:11]
s_and_b64 vcc, vcc, s[86:87]
s_or_b64 vcc, vcc, s[20:21]
s_cmp_eq_u64 vcc, -1
s_cbranch_scc1 .Lok_77%=
s_cmp_lt_u32 s84, 1048576
s_cbranch_scc1 .Llp_78%=
s_branch .Lok_77%=
.Lmk_79%=:
s_nop 7
v_cndmask_b32_e64 v168, v241, v168, s[72:73]
v_cndmask_b32_e64 v169, v241, v169, s[74:75]
v_cndmask_b32_e64 v170, v241, v170, s[76:77]
v_cndmask_b32_e64 v171, v241, v171, s[78:79]
v_cndmask_b32_e64 v188, v241, v188, s[72:73]
v_cndmask_b32_e64 v189, v241, v189, s[74:75]
v_cndmask_b32_e64 v190, v241, v190, s[76:77]
v_cndmask_b32_e64 v191, v241, v191, s[78:79]
v_mov_b32 v184, v241
v_mov_b32 v185, v241
v_mov_b32 v186, v241
v_mov_b32 v187, v241
s_branch .Lmkr_80%=
.Lspin_84%=:
s_mov_b32 s84, 0
.Llp_86%=:
s_sleep 2
v_bfe_u32 v28, v1, 4, 2
v_lshlrev_b32 v28, 3, v28
buffer_load_dwordx2 v[28:29], v28, s[8:11], s55 offen sc1
s_waitcnt vmcnt(0)
s_add_u32 s84, s84, 1
v_cmp_eq_u64 vcc, s[12:13], v[28:29]
s_cmp_eq_u64 vcc, -1
s_cbranch_scc1 .Lok_85%=
s_cmp_lt_u32 s84, 1048576
s_cbranch_scc1 .Llp_86%=
s_branch .Lok_85%=
.LL_89%=:
s_mov_b32 s84, 0
.LL_91%=:
s_sleep 2
buffer_load_dwordx2 v[12:13], v27, s[8:11], 0 offen offset:3584 sc1
s_waitcnt vmcnt(0)
s_add_u32 s84, s84, 1
v_xor_b32 v12, s12, v12
v_xor_b32 v13, s13, v13
v_lshrrev_b32 v3, 11, v12
v_or_b32 v13, v13, v3
v_cmp_eq_u32 vcc, 0, v13
s_cmp_eq_u64 vcc, -1
s_cbranch_scc1 .LL_90%=
s_cmp_lt_u32 s84, 1048576
s_cbranch_scc1 .LL_91%=
s_branch .LL_90%=
.Lup_87%=:
v_lshrrev_b32 v3, 8, v1
v_lshlrev_b32 v3, 5, v3
s_ff1_i32_b32 s69, s68
s_mul_i32 s66, s69, 20
s_mul_i32 s67, s58, 5
s_add_u32 s66, s66, s67
s_lshl_b32 s66, s66, 7
s_add_u32 s66, s66, 98304
s_lshl_b32 s67, s14, 7
s_add_u32 s67, s67, s66
buffer_load_dwordx4 v[100:103], v3, s[8:11], s67 offen offset:0 sc1
buffer_load_dwordx4 v[108:111], v3, s[8:11], s67 offen offset:16 sc1
buffer_load_dwordx4 v[116:119], v3, s[8:11], s66 offen offset:512 sc1
buffer_load_dwordx4 v[124:127], v3, s[8:11], s66 offen offset:528 sc1
s_branch .Lupr_88%=
.Lspin_100%=:
s_mov_b32 s84, 0
.Llp_102%=:
s_sleep 2
v_bfe_u32 v28, v1, 4, 2
v_lshlrev_b32 v28, 3, v28
buffer_load_dwordx2 v[28:29], v28, s[8:11], s54 offen sc1
s_waitcnt vmcnt(0)
s_add_u32 s84, s84, 1
v_cmp_eq_u64 vcc, s[12:13], v[28:29]
s_cmp_eq_u64 vcc, -1
s_cbranch_scc1 .Lok_101%=
s_cmp_lt_u32 s84, 1048576
s_cbranch_scc1 .Llp_102%=
s_branch .Lok_101%=
.Lspin_103%=:
s_mov_b32 s84, 0
.Llp_105%=:
s_sleep 2
v_bfe_u32 v28, v1, 4, 2
v_lshlrev_b32 v28, 3, v28
buffer_load_dwordx2 v[28:29], v28, s[8:11], s55 offen sc1
s_waitcnt vmcnt(0)
s_add_u32 s84, s84, 1
v_cmp_eq_u64 vcc, s[12:13], v[28:29]
s_cmp_eq_u64 vcc, -1
s_cbranch_scc1 .Lok_104%=
s_cmp_lt_u32 s84, 1048576
s_cbranch_scc1 .Llp_105%=
s_branch .Lok_104%=
.LL_108%=:
s_mov_b32 s84, 0
)"
R"(.LL_110%=:
s_sleep 2
buffer_load_dwordx2 v[12:13], v27, s[8:11], 0 offen offset:3584 sc1
s_waitcnt vmcnt(0)
s_add_u32 s84, s84, 1
v_xor_b32 v12, s12, v12
v_xor_b32 v13, s13, v13
v_lshrrev_b32 v3, 11, v12
v_or_b32 v13, v13, v3
v_cmp_eq_u32 vcc, 0, v13
s_cmp_eq_u64 vcc, -1
s_cbranch_scc1 .LL_109%=
s_cmp_lt_u32 s84, 1048576
s_cbranch_scc1 .LL_110%=
s_branch .LL_109%=
.Lup_106%=:
v_lshrrev_b32 v3, 8, v1
v_lshlrev_b32 v3, 5, v3
s_ff1_i32_b32 s69, s68
s_mul_i32 s66, s69, 20
s_mul_i32 s67, s58, 5
s_add_u32 s66, s66, s67
s_lshl_b32 s66, s66, 7
s_add_u32 s66, s66, 98304
s_lshl_b32 s67, s14, 7
s_add_u32 s67, s67, s66
buffer_load_dwordx4 v[100:103], v3, s[8:11], s67 offen offset:0 sc1
buffer_load_dwordx4 v[108:111], v3, s[8:11], s67 offen offset:16 sc1
buffer_load_dwordx4 v[116:119], v3, s[8:11], s66 offen offset:512 sc1
buffer_load_dwordx4 v[124:127], v3, s[8:11], s66 offen offset:528 sc1
s_branch .Lupr_107%=
.Lend_252%=:
.Lend%=:
.purgem W
.purgem D
.purgem Ta
.purgem Ra
.purgem Zvaa
.purgem Mvaa
.purgem Zaaa
.purgem Maaa
.purgem A
.purgem U
.purgem K
.purgem Rv
.purgem Mvav
.purgem G
.purgem Zvav
.purgem Cvav
.purgem X
.purgem Cvaa
.purgem Dc
.purgem Maav
.purgem Mvvv
.purgem Zvva
.purgem Mvva
)"
    : : [ka] "s"(__builtin_amdgcn_kernarg_segment_ptr()), [bid] "s"(blockIdx.x), [tid] "v"(threadIdx.x)
    : "v1", "v2", "v3", "v4", "v5", "v6", "v7", "v8", "v9", "v10", "v11", "v12", "v13", "v14", "v15", "v16", "v17", "v18", "v19", "v20", "v21", "v22", "v23", "v24", "v25", "v26", "v27", "v28", "v29", "v30", "v31", "v32", "v33", "v34", "v35", "v36", "v37", "v38", "v39", "v40", "v41", "v42", "v43", "v44", "v45", "v46", "v47", "v48", "v49", "v50", "v51", "v52", "v53", "v54", "v55", "v56", "v57", "v58", "v59", "v60", "v61", "v62", "v63", "v64", "v65", "v66", "v67", "v68", "v69", "v70", "v71", "v72", "v73", "v74", "v75", "v76", "v77", "v78", "v79", "v80", "v81", "v82", "v83", "v84", "v85", "v86", "v87", "v88", "v89", "v90", "v91", "v92", "v93", "v94", "v95", "v96", "v97", "v98", "v99", "v100", "v101", "v102", "v103", "v104", "v105", "v106", "v107", "v108", "v109", "v110", "v111", "v112", "v113", "v114", "v115", "v116", "v117", "v118", "v119", "v120", "v121", "v122", "v123", "v124", "v125", "v126", "v127", "v128", "v129", "v130", "v131", "v132", "v133", "v134", "v135", "v136", "v137", "v138", "v139", "v140", "v141", "v142", "v143", "v144", "v145", "v146", "v147", "v148", "v149", "v150", "v151", "v152", "v153", "v154", "v155", "v156", "v157", "v158", "v159", "v160", "v161", "v162", "v163", "v164", "v165", "v166", "v167", "v168", "v169", "v170", "v171", "v172", "v173", "v174", "v175", "v176", "v177", "v178", "v179", "v180", "v181", "v182", "v183", "v184", "v185", "v186", "v187", "v188", "v189", "v190", "v191", "v192", "v193", "v194", "v195", "v196", "v197", "v198", "v199", "v200", "v201", "v202", "v203", "v204", "v205", "v206", "v207", "v208", "v209", "v210", "v211", "v212", "v213", "v214", "v215", "v216", "v217", "v218", "v219", "v220", "v221", "v222", "v223", "v224", "v225", "v226", "v227", "v228", "v229", "v230", "v231", "v232", "v233", "v234", "v235", "v236", "v237", "v238", "v239", "v240", "v241", "v242", "v243", "v244", "v245", "v246", "v247", "v248", "v249", "v250", "v251", "v252", "v253", "v254", "v255", "a0", "a1", "a2", "a3", "a4", "a5", "a6", "a7", "a8", "a9", "a10", "a11", "a12", "a13", "a14", "a15", "a16", "a17", "a18", "a19", "a20", "a21", "a22", "a23", "a24", "a25", "a26", "a27", "a28", "a29", "a30", "a31", "a32", "a33", "a34", "a35", "a36", "a37", "a38", "a39", "a40", "a41", "a42", "a43", "a44", "a45", "a46", "a47", "a48", "a49", "a50", "a51", "a52", "a53", "a54", "a55", "a56", "a57", "a58", "a59", "a60", "a61", "a62", "a63", "a64", "a65", "a66", "a67", "a68", "a69", "a70", "a71", "a72", "a73", "a74", "a75", "a76", "a77", "a78", "a79", "a80", "a81", "a82", "a83", "a84", "a85", "a86", "a87", "a88", "a89", "a90", "a91", "a92", "a93", "a94", "a95", "a96", "a97", "a98", "a99", "a100", "a101", "a102", "a103", "a104", "a105", "a106", "a107", "a108", "a109", "a110", "a111", "a112", "a113", "a114", "a115", "a116", "a117", "a118", "a119", "a120", "a121", "a122", "a123", "a124", "a125", "a126", "a127", "a128", "a129", "a130", "a131", "a132", "a133", "a134", "a135", "a136", "a137", "a138", "a139", "a140", "a141", "a142", "a143", "a144", "a145", "a146", "a147", "a148", "a149", "a150", "a151", "a152", "a153", "a154", "a155", "a156", "a157", "a158", "a159", "a160", "a161", "a162", "a163", "a164", "a165", "a166", "a167", "a168", "a169", "a170", "a171", "a172", "a173", "a174", "a175", "a176", "a177", "a178", "a179", "a180", "a181", "a182", "a183", "a184", "a185", "a186", "a187", "a188", "a189", "a190", "a191", "a192", "a193", "a194", "a195", "a196", "a197", "a198", "a199", "a200", "a201", "a202", "a203", "a204", "a205", "a206", "a207", "a208", "a209", "a210", "a211", "a212", "a213", "a214", "a215", "a216", "a217", "a218", "a219", "a220", "a221", "a222", "a223", "a224", "a225", "a226", "a227", "a228", "a229", "a230", "a231", "a232", "a233", "a234", "a235", "a236", "a237", "a238", "a239", "a240", "a241", "a242", "a243", "a244", "a245", "a246", "a247", "a248", "a249", "a250", "a251", "a252", "a253", "a254", "a255", "s4", "s5", "s6", "s7", "s8", "s9", "s10", "s11", "s12", "s13", "s14", "s15", "s16", "s17", "s18", "s19", "s20", "s21", "s22", "s23", "s24", "s25", "s26", "s27", "s28", "s29", "s30", "s31", "s32", "s33", "s34", "s35", "s36", "s37", "s38", "s39", "s40", "s41", "s42", "s43", "s44", "s45", "s46", "s47", "s48", "s49", "s50", "s51", "s52", "s53", "s54", "s55", "s56", "s57", "s58", "s59", "s60", "s61", "s62", "s63", "s64", "s65", "s66", "s67", "s68", "s69", "s70", "s71", "s72", "s73", "s74", "s75", "s76", "s77", "s78", "s79", "s80", "s81", "s82", "s83", "s84", "s85", "s86", "s87", "s88", "s89", "s90", "s91", "s92", "s93", "s94", "s95", "s96", "s97", "s98", "s99", "vcc", "scc", "memory"
  );
}
extern "C" void kernel_launch(void* const* d_in, const int* in_sizes, int n_in, void* d_out, int out_size, void* d_ws, size_t ws_size, hipStream_t stream) {
  (void)hipFuncSetAttribute((const void*)mega_kernel, hipFuncAttributeMaxDynamicSharedMemorySize, LDS_BYTES);
  mega_kernel<<<256, 256, LDS_BYTES, stream>>>((const bf16*)d_in[0], (const bf16*)d_in[1], (const bf16*)d_in[2], (const bf16*)d_in[3], (const bf16*)d_in[4], (const bf16*)d_in[5],
      (const bf16*)d_in[7], (const bf16*)d_in[8], (const bf16*)d_in[9], (const bf16*)d_in[10], (char*)d_ws + 0, (bf16*)d_out);
}
```

```cpp
#include <hip/hip_runtime.h>
#include <hip/hip_bf16.h>
typedef __hip_bfloat16 bf16;
constexpr int LDS_BYTES = 163840;
__global__ __launch_bounds__(256, 1) void mega_kernel(const bf16* x, const bf16* ckv, const bf16* wq, const bf16* wuk, const bf16* wuv, const bf16* wo,
    const bf16* w_r, const bf16* wg, const bf16* wu, const bf16* wd, char* ws, bf16* out) {
  asm volatile(
R"(.macro W n
s_waitcnt lgkmcnt(\n)
.endm
.macro D v,r,s,o
buffer_load_dwordx4 v\v, s[\r:\r+3], s\s offen offset:\o lds
.endm
.macro Ta d,a,o
ds_read_b64_tr_b16 a[\d:\d+1], v\a offset:\o
.endm
.macro Ra d,a,o
ds_read_b128 a[\d:\d+3], v\a offset:\o
.endm
.macro Zvaa d,a,b
v_mfma_f32_16x16x32_bf16 v[\d:\d+3], a[\a:\a+3], a[\b:\b+3], 0
.endm
.macro Mvaa d,a,b
v_mfma_f32_16x16x32_bf16 v[\d:\d+3], a[\a:\a+3], a[\b:\b+3], v[\d:\d+3]
.endm
.macro Zaaa d,a,b
v_mfma_f32_16x16x32_bf16 a[\d:\d+3], a[\a:\a+3], a[\b:\b+3], 0
.endm
.macro Maaa d,a,b
v_mfma_f32_16x16x32_bf16 a[\d:\d+3], a[\a:\a+3], a[\b:\b+3], a[\d:\d+3]
.endm
.macro A d,a,b
v_add_f32 v\d, v\a, v\b
.endm
.macro U d,a,b
v_mul_f32 v\d, v\a, v\b
.endm
.macro K d,a,b
v_cvt_pk_bf16_f32 v\d, v\a, v\b
.endm
.macro Rv d,a,o
ds_read_b128 v[\d:\d+3], v\a offset:\o
.endm
.macro Mvav d,a,b
v_mfma_f32_16x16x32_bf16 v[\d:\d+3], a[\a:\a+3], v[\b:\b+3], v[\d:\d+3]
.endm
.macro G d,a
v_accvgpr_read_b32 v\d, a\a
.endm
.macro Zvav d,a,b
v_mfma_f32_16x16x32_bf16 v[\d:\d+3], a[\a:\a+3], v[\b:\b+3], 0
.endm
.macro Cvav d,a,b,c
v_mfma_f32_16x16x32_bf16 v[\d:\d+3], a[\a:\a+3], v[\b:\b+3], v[\c:\c+3]
.endm
.macro X d
v_exp_f32 v\d, v\d
.endm
.macro Cvaa d,a,b,c
v_mfma_f32_16x16x32_bf16 v[\d:\d+3], a[\a:\a+3], a[\b:\b+3], v[\c:\c+3]
.endm
.macro Dc v,r,s,o
buffer_load_dwordx4 v\v, s[\r:\r+3], s\s offen offset:\o sc1 lds
.endm
.macro Maav d,a,b
v_mfma_f32_16x16x32_bf16 a[\d:\d+3], a[\a:\a+3], v[\b:\b+3], a[\d:\d+3]
.endm
.macro Mvvv d,a,b
v_mfma_f32_16x16x32_bf16 v[\d:\d+3], v[\a:\a+3], v[\b:\b+3], v[\d:\d+3]
.endm
.macro Zvva d,a,b
v_mfma_f32_16x16x32_bf16 v[\d:\d+3], v[\a:\a+3], a[\b:\b+3], 0
.endm
.macro Mvva d,a,b
v_mfma_f32_16x16x32_bf16 v[\d:\d+3], v[\a:\a+3], a[\b:\b+3], v[\d:\d+3]
.endm
s_load_dwordx16 s[16:31], %[ka], 0
s_load_dwordx8 s[32:39], %[ka], 64
s_getpc_b64 s[6:7]
.Lbeg%=:
s_mov_b32 s15, %[bid]
v_lshrrev_b32 v2, 6, %[tid]
v_and_b32 v100, 63, %[tid]
v_readfirstlane_b32 s14, v2
v_lshlrev_b32 v1, 4, v100
v_lshrrev_b32 v101, 4, v100
v_and_b32 v102, 15, v100
s_and_b32 s59, s15, 7
s_bfe_u32 s57, s15, 0x30003
s_lshr_b32 s58, s15, 6
s_lshl_b32 s56, s59, 2
s_add_u32 s56, s56, s58
s_lshr_b32 s80, s15, 3
s_lshl_b32 s80, s80, 2
s_add_u32 s80, s80, s14
s_lshl_b32 s80, s80, 11
v_min_u32 v146, 240, v1
v_lshl_add_u32 v146, v146, 3, s80
v_min_u32 v146, .Lend%=-.Lbeg%=-4, v146
global_load_dword v142, v146, s[6:7]
v_and_b32 v88, 0x1f0, v1
v_lshrrev_b32 v105, 1, v101
v_mul_u32_u24 v105, 1280, v105
v_add_u32 v28, v88, v105
s_lshl_b32 s85, s15, 2
s_add_u32 s85, s85, s14
s_mul_i32 s84, s85, 2560
v_and_b32 v105, 1, v101
v_lshl_or_b32 v29, v105, 31, v28
v_lshlrev_b32 v104, 1, v101
v_xor_b32 v104, v104, v102
v_lshlrev_b32 v104, 4, v104
v_lshl_add_u32 v16, v101, 13, v104
v_xor_b32 v105, 128, v16
v_add_u32 v17, 31744, v16
v_add_u32 v18, 63488, v105
v_add_u32 v19, 95232, v105
v_add_u32 v20, 131072, v16
v_add_u32 v21, 162816, v16
v_add_u32 v22, 194560, v105
v_add_u32 v23, 226304, v105
s_lshl_b32 s80, s14, 3
s_add_u32 s80, s80, s57
s_lshl_b32 s80, s80, 15
v_and_b32 v104, 7, v100
v_xor_b32 v104, v104, v101
v_lshlrev_b32 v104, 4, v104
v_lshrrev_b32 v105, 3, v100
v_lshl_add_u32 v104, v105, 10, v104
v_add_u32 v2, s80, v104
v_xor_b32 v105, 64, v2
v_add_u32 v3, 7168, v105
v_add_u32 v4, 14336, v2
v_add_u32 v5, 21504, v105
v_xor_b32 v6, 1835008, v2
v_xor_b32 v7, 1835008, v3
v_xor_b32 v8, 1835008, v4
v_xor_b32 v9, 1835008, v5
s_lshl_b32 s63, s14, 13
s_add_u32 s61, s63, 65536
s_and_b32 s80, s14, 1
s_lshr_b32 s81, s14, 1
s_lshl_b32 s62, s80, 14
s_lshl_b32 s82, s81, 13
s_add_u32 s62, s62, s82
s_lshl_b32 s81, s81, 18
s_lshl_b32 s83, s56, 8
s_add_u32 s81, s81, s83
s_mov_b32 s64, 0
s_cmp_eq_u32 s80, 0
W 0
s_cselect_b32 s44, s22, s24
s_cselect_b32 s45, s23, s25
s_cmp_eq_u32 s14, 3
s_cbranch_scc0 .LL_1%=
s_lshr_b32 s88, s15, 3
s_cmp_lt_u32 s88, 14
s_cbranch_scc0 .LL_1%=
v_lshlrev_b32 v104, 16, v1
s_mov_b64 s[86:87], s[36:37]
s_mov_b32 s89, 38797312
s_cmp_eq_u32 s88, 1
s_cselect_b32 s90, 39845888, 0
s_add_u32 s86, s86, s90
s_addc_u32 s87, s87, 0
s_cmp_eq_u32 s88, 2
s_cselect_b64 s[86:87], s[26:27], s[86:87]
s_cselect_b32 s89, 5242876, s89
s_cmp_eq_u32 s88, 3
s_cselect_b64 s[86:87], s[30:31], s[86:87]
s_cselect_b32 s89, 2211836, s89
s_cmp_eq_u32 s88, 4
s_cselect_b64 s[86:87], s[32:33], s[86:87]
s_cselect_b32 s89, 2211836, s89
s_cmp_eq_u32 s88, 5
s_cselect_b64 s[86:87], s[34:35], s[86:87]
s_cselect_b32 s89, 2211836, s89
s_cmp_eq_u32 s88, 6
s_cselect_b64 s[86:87], s[38:39], s[86:87]
s_cselect_b32 s89, 2621436, s89
s_cmp_eq_u32 s88, 7
s_cselect_b64 s[86:87], s[20:21], s[86:87]
s_cselect_b32 s89, 5242876, s89
s_cmp_eq_u32 s88, 8
s_cselect_b64 s[86:87], s[18:19], s[86:87]
s_cselect_b32 s89, 2097148, s89
s_cmp_eq_u32 s88, 9
s_cselect_b64 s[86:87], s[22:23], s[86:87]
s_cselect_b32 s89, 4194300, s89
s_cmp_eq_u32 s88, 10
s_cselect_b64 s[86:87], s[24:25], s[86:87]
s_cselect_b32 s89, 4194300, s89
s_cmp_eq_u32 s88, 11
s_cselect_b64 s[86:87], s[16:17], s[86:87]
s_cselect_b32 s89, 2621436, s89
s_cmp_eq_u32 s88, 12
s_cselect_b64 s[86:87], s[28:29], s[86:87]
s_cselect_b32 s89, 1276, s89
s_cmp_eq_u32 s88, 13
s_cselect_b64 s[86:87], s[6:7], s[86:87]
s_cselect_b32 s89, 0, s89
v_min_u32 v104, s89, v104
global_load_dword v142, v104, s[86:87] sc1
.LL_1%=:
s_mov_b32 s40, s18
s_and_b32 s41, s19, 0xffff
s_mov_b32 s42, 0x200000
s_mov_b32 s43, 0x20000
s_add_u32 s44, s44, s81
s_addc_u32 s45, s45, 0
s_and_b32 s45, s45, 0xffff
s_mov_b32 s46, 0x400000
s_mov_b32 s47, 0x20000
s_sub_u32 s46, s46, s81
s_load_dwordx2 s[12:13], s[36:37], 0
s_lshl_b32 s65, s64, 12
s_add_u32 m0, s62, 0
s_nop 0
D 16,44,65,0
D 17,44,65,1024
D 18,44,65,2048
D 19,44,65,3072
s_add_u32 m0, s62, 4096
s_nop 0
D 20,44,65,0
D 21,44,65,1024
D 22,44,65,2048
D 23,44,65,3072
s_add_u32 m0, s61, 0
s_nop 0
D 2,40,64,0
D 3,40,64,1024
D 4,40,64,2048
D 5,40,64,3072
s_add_u32 m0, s61, 4096
s_nop 0
D 6,40,64,0
D 7,40,64,1024
D 8,40,64,2048
D 9,40,64,3072
s_mov_b32 s52, s16
s_and_b32 s53, s17, 0xffff
s_mov_b32 s54, 2621440
s_mov_b32 s55, 0x20000
buffer_load_dwordx4 v[32:35], v28, s[52:55], s84 offen
buffer_load_dwordx4 v[36:39], v28, s[52:55], s84 offen offset:512
buffer_load_dwordx4 v[40:43], v29, s[52:55], s84 offen offset:1024
s_mov_b32 s52, s28
s_and_b32 s53, s29, 0xffff
s_mov_b32 s54, 1280
s_mov_b32 s55, 0x20000
buffer_load_dwordx4 v[44:47], v88, s[52:55], 0 offen offset:0
buffer_load_dwordx4 v[48:51], v88, s[52:55], 0 offen offset:512
buffer_load_dwordx4 v[52:55], v88, s[52:55], 0 offen offset:1024
s_mov_b32 s69, 0
s_cmp_eq_u32 s14, 2
s_cbranch_scc1 .LL_2%=
s_cmp_eq_u32 s14, 3
s_cbranch_scc1 .LL_3%=
s_lshl_b32 s80, s58, 3
s_add_u32 s80, s80, s57
s_lshl_b32 s80, s80, 15
s_lshl_b32 s82, s14, 7
s_add_u32 s80, s80, s82
s_add_u32 s80, s80, 256
v_lshl_add_u32 v104, v88, 6, s80
v_and_b32 v105, 512, v1
v_mul_u32_u24 v105, 3584, v105
v_xor_b32 v104, v104, v105
v_mov_b32 v140, s18
v_mov_b32 v141, s19
s_mov_b32 s68, 256
s_branch .LL_4%=
.LL_2%=:
v_lshrrev_b32 v104, 6, v1
v_lshlrev_b32 v104, 13, v104
v_and_b32 v105, 16, v1
v_lshl_add_u32 v104, v105, 3, v104
s_lshl_b32 s80, s57, 17
s_add_u32 s80, s80, s83
s_add_u32 s80, s80, 1048576
v_add_u32 v104, s80, v104
v_and_b32 v105, 32, v1
v_cmp_eq_u32 vcc, 0, v105
v_mov_b32 v140, s24
v_mov_b32 v141, s25
v_mov_b32 v105, s22
v_mov_b32 v106, s23
v_cndmask_b32 v140, v140, v105, vcc
v_cndmask_b32 v141, v141, v106, vcc
s_mov_b32 s68, 1048576
s_branch .LL_4%=
.LL_3%=:
v_lshrrev_b32 v104, 5, v1
v_lshlrev_b32 v104, 13, v104
v_and_b32 v105, 16, v1
v_lshl_add_u32 v104, v105, 3, v104
s_mul_i32 s80, s57, 655360
s_add_u32 s80, s80, s83
v_add_u32 v104, s80, v104
v_mov_b32 v140, s20
v_mov_b32 v141, s21
s_mov_b32 s68, 196608
v_mov_b32 v104, 0
s_mov_b32 s68, 0
.LL_4%=:
v_mov_b32 v105, 0
v_lshl_add_u64 v[140:141], v[104:105], 0, v[140:141]
s_add_u32 s64, s64, 128
s_and_b32 s64, s64, 1023
s_lshl_b32 s65, s64, 12
s_add_u32 m0, s62, 32768
s_nop 0
D 16,44,65,0
D 17,44,65,1024
D 18,44,65,2048
D 19,44,65,3072
s_add_u32 m0, s62, 36864
s_nop 0
D 20,44,65,0
D 21,44,65,1024
D 22,44,65,2048
D 23,44,65,3072
v_lshrrev_b32 v104, 1, v102
v_xor_b32 v104, v104, v101
v_lshlrev_b32 v104, 4, v104
v_lshl_add_u32 v104, v102, 7, v104
s_lshr_b32 s81, s14, 1
s_lshl_b32 s81, s81, 14
s_add_u32 s81, s81, 65536
v_add_u32 v26, s81, v104
v_xor_b32 v27, 64, v26
v_and_b32 v105, 1, v101
v_lshrrev_b32 v104, 2, v102
v_lshl_or_b32 v105, v105, 2, v104
v_lshl_add_u32 v104, v101, 3, v104
v_lshlrev_b32 v104, 8, v104
v_lshl_add_u32 v104, v105, 5, v104
v_and_b32 v105, 3, v102
v_lshl_add_u32 v10, v105, 3, v104
v_xor_b32 v11, 32, v10
v_xor_b32 v12, 64, v10
v_xor_b32 v13, 96, v10
v_xor_b32 v14, 128, v10
v_xor_b32 v15, 160, v10
v_xor_b32 v24, 192, v10
v_xor_b32 v25, 224, v10
.LL_8%=:
s_lshr_b32 s81, s15, 3
s_cmp_eq_u32 s14, 2
s_cbranch_scc1 .LL_5%=
s_cmp_eq_u32 s14, 3
s_cbranch_scc1 .LL_6%=
s_cmp_eq_u32 s14, 0
s_cselect_b64 s[76:77], s[30:31], s[32:33]
s_mov_b32 s72, 512
s_mov_b32 s73, 24
s_mul_i32 s74, s81, 69120
s_mul_i32 s82, s59, 8640
s_andn2_b32 s82, s82, 127
s_add_u32 s74, s74, s82
s_mov_b32 s75, 2211836
s_branch .LL_7%=
.LL_5%=:
s_mov_b64 s[76:77], s[34:35]
s_mov_b32 s72, 512
s_mov_b32 s73, 24
s_mul_i32 s74, s81, 8960
s_mul_i32 s82, s59, 286720
s_add_u32 s74, s74, s82
s_mov_b32 s75, 2211836
s_branch .LL_7%=
.LL_6%=:
s_mov_b64 s[76:77], s[26:27]
s_mov_b32 s72, 512
s_mov_b32 s73, 4
s_mul_i32 s74, s81, 20480
s_mul_i32 s82, s59, 655360
s_add_u32 s74, s74, s82
s_mov_b32 s75, 5242876
s_branch .LL_7%=
.LL_7%=:
s_mov_b32 s8, s36
s_and_b32 s9, s37, 0xffff
s_mov_b32 s10, 79691776
s_mov_b32 s11, 0x20000
s_add_u32 s48, s20, s83
s_addc_u32 s49, s21, 0
s_and_b32 s49, s49, 0xffff
s_mov_b32 s50, 5242880
s_mov_b32 s51, 0x20000
s_sub_u32 s50, s50, s83
s_lshl_b32 s80, s57, 1
s_and_b32 s81, s14, 1
s_add_u32 s80, s80, s81
s_sub_u32 s81, 31, s80
s_cmp_lt_u32 s14, 2
s_cselect_b32 s60, s80, s81
s_mul_i32 s28, s60, 81920
s_add_u32 s28, s28, 131072
s_add_u32 s4, s28, 10240
s_mov_b32 s80, 0
s_lshl_b32 s81, s80, 9
s_add_u32 s28, s28, s81
s_add_u32 s5, s80, s14
s_cmp_ge_u32 s5, 20
s_cselect_b32 s81, 20, 0
s_sub_u32 s5, s5, s81
s_and_b32 s80, s14, 1
s_mul_i32 s29, s80, 17172480
s_mul_i32 s80, s56, 536576
s_add_u32 s29, s29, s80
s_lshl_b32 s80, s57, 13
s_add_u32 s29, s29, s80
s_add_u32 s29, s29, 4194304
W 0
s_add_u32 s12, s12, 0x7f4a7c15
s_addc_u32 s13, s13, 0x9e3779b9
s_mov_b64 exec, 0
s_cmp_eq_u32 s14, 0
s_cbranch_scc1 .Lh_0_0%=
s_cmp_eq_u32 s14, 1
s_cbranch_scc1 .Lh_0_1%=
s_cmp_eq_u32 s14, 2
s_cbranch_scc1 .Lh_0_2%=
s_branch .Lh_0_3%=
.Lhr_9%=:
s_mov_b64 exec, -1
s_bitcmp1_b32 s14, 0
s_cbranch_scc1 .Lv_10%=
s_branch .LL_12%=
.p2align 12
.LL_12%=:
s_waitcnt vmcnt(14)
s_barrier
Ta 0,10,0
Ta 2,10,1024
Ra 32,26,0
Ta 4,10,8192
.Lh_0_0%=:
s_cbranch_execz .Lh_0_0_1%=
Ta 6,10,9216
Ra 128,27,0
Ra 132,27,2048
Ra 36,26,2048
Ra 40,26,4096
Ra 136,27,4096
Ra 140,27,6144
Ra 44,26,6144
Ra 48,26,8192
Ra 144,27,8192
W 6
Zvaa 160,0,32
)"
R"(s_add_u32 m0, s61, 32768
Ra 148,27,10240
Ra 52,26,10240
Ra 56,26,12288
Ra 152,27,12288
Ra 156,27,14336
Ra 60,26,14336
Mvaa 160,4,128
Ta 8,11,0
Ta 10,11,1024
Ta 12,11,8192
Ta 14,11,9216
Zvaa 192,4,132
Ta 16,12,0
Ta 18,12,1024
Mvaa 192,0,36
W 14
Ta 20,12,8192
Ta 22,12,9216
Zvaa 224,0,40
D 2,40,64,0
Mvaa 224,4,136
Zaaa 160,4,140
Maaa 160,0,44
W 12
Zaaa 192,0,48
D 3,40,64,1024
Maaa 192,4,144
Zaaa 224,4,148
Maaa 224,0,52
W 8
Zaaa 64,0,56
D 4,40,64,2048
Maaa 64,4,152
Zaaa 96,4,156
Maaa 96,0,60
W 4
Zaaa 100,8,60
D 5,40,64,3072
Maaa 100,12,156
s_add_u32 m0, s61, 36864
Zaaa 68,12,152
Ta 24,13,0
Ta 26,13,1024
Maaa 68,8,56
Ta 28,13,8192
Ta 30,13,9216
Zaaa 228,8,52
s_waitcnt vmcnt(15)
D 6,40,64,0
Maaa 228,12,148
v_lshlrev_b32 v56, 16, v32
v_and_b32 v57, 0xffff0000, v32
v_lshlrev_b32 v58, 16, v33
v_and_b32 v59, 0xffff0000, v33
v_lshlrev_b32 v60, 16, v34
Zaaa 196,12,144
v_and_b32 v61, 0xffff0000, v34
v_lshlrev_b32 v62, 16, v35
v_and_b32 v63, 0xffff0000, v35
v_lshlrev_b32 v64, 16, v36
v_and_b32 v65, 0xffff0000, v36
Maaa 196,8,48
v_lshlrev_b32 v66, 16, v37
v_and_b32 v67, 0xffff0000, v37
v_lshlrev_b32 v68, 16, v38
v_and_b32 v69, 0xffff0000, v38
v_lshlrev_b32 v70, 16, v39
Zaaa 164,8,44
D 7,40,64,1024
v_and_b32 v71, 0xffff0000, v39
v_lshlrev_b32 v72, 16, v40
v_and_b32 v73, 0xffff0000, v40
v_lshlrev_b32 v74, 16, v41
v_and_b32 v75, 0xffff0000, v41
Maaa 164,12,140
v_lshlrev_b32 v76, 16, v42
v_and_b32 v77, 0xffff0000, v42
v_lshlrev_b32 v78, 16, v43
v_and_b32 v79, 0xffff0000, v43
A 80,56,57
Zvaa 228,12,136
A 81,58,59
A 80,80,60
A 81,81,61
A 80,80,62
A 81,81,63
Mvaa 228,8,40
A 80,80,64
A 81,81,65
A 80,80,66
A 81,81,67
A 80,80,68
Zvaa 196,8,36
D 8,40,64,2048
A 81,81,69
A 80,80,70
A 81,81,71
A 80,80,72
A 81,81,73
Mvaa 196,12,132
A 80,80,74
A 81,81,75
A 80,80,76
A 81,81,77
A 80,80,78
A 81,81,79
Zvaa 164,12,128
A 80,80,81
s_nop 1
v_add_f32_dpp v80, v80, v80 quad_perm:[1,0,3,2] row_mask:0xf bank_mask:0xf
s_nop 1
v_add_f32_dpp v80, v80, v80 quad_perm:[2,3,0,1] row_mask:0xf bank_mask:0xf
s_nop 1
v_add_f32_dpp v80, v80, v80 row_half_mirror row_mask:0xf bank_mask:0xf
s_nop 1
v_add_f32_dpp v80, v80, v80 row_mirror row_mask:0xf bank_mask:0xf
Mvaa 164,8,32
W 4
v_mov_b32 v81, v80
s_nop 1
v_permlane16_swap_b32 v80, v81
A 80,80,81
v_mul_f32 v84, 0x3acccccd, v80
s_mov_b32 s86, 0xffff
Zvaa 168,16,32
s_mov_b32 s87, 0xffff
D 9,40,64,3072
v_sub_f32 v82, v56, v84
U 80,82,82
v_sub_f32 v83, v57, v84
v_fmac_f32 v80, v83, v83
Mvaa 168,20,128
v_sub_f32 v82, v58, v84
v_fmac_f32 v80, v82, v82
v_sub_f32 v83, v59, v84
v_fmac_f32 v80, v83, v83
v_sub_f32 v82, v60, v84
Zvaa 200,20,132
Ta 0,14,0
v_fmac_f32 v80, v82, v82
v_sub_f32 v83, v61, v84
v_fmac_f32 v80, v83, v83
v_sub_f32 v82, v62, v84
v_fmac_f32 v80, v82, v82
Mvaa 200,16,36
Ta 2,14,1024
v_sub_f32 v83, v63, v84
v_fmac_f32 v80, v83, v83
v_sub_f32 v82, v64, v84
v_fmac_f32 v80, v82, v82
v_sub_f32 v83, v65, v84
Zvaa 232,16,40
global_load_dword v142, v[140:141], off
v_lshl_add_u64 v[140:141], v[140:141], 0, s[68:69]
Ta 4,14,8192
Ta 6,14,9216
v_fmac_f32 v80, v83, v83
v_sub_f32 v82, v66, v84
v_fmac_f32 v80, v82, v82
Mvaa 232,20,136
v_sub_f32 v83, v67, v84
v_fmac_f32 v80, v83, v83
v_sub_f32 v82, v68, v84
v_fmac_f32 v80, v82, v82
v_sub_f32 v83, v69, v84
v_fmac_f32 v80, v83, v83
Zaaa 168,20,140
v_sub_f32 v82, v70, v84
v_fmac_f32 v80, v82, v82
v_sub_f32 v83, v71, v84
v_fmac_f32 v80, v83, v83
v_sub_f32 v82, v72, v84
U 81,82,82
Maaa 168,16,44
v_sub_f32 v83, v73, v84
v_fmac_f32 v81, v83, v83
v_sub_f32 v82, v74, v84
v_fmac_f32 v81, v82, v82
v_sub_f32 v83, v75, v84
v_fmac_f32 v81, v83, v83
Zaaa 200,16,48
v_sub_f32 v82, v76, v84
v_fmac_f32 v81, v82, v82
v_sub_f32 v83, v77, v84
v_fmac_f32 v81, v83, v83
v_sub_f32 v82, v78, v84
Maaa 200,20,144
v_fmac_f32 v81, v82, v82
v_sub_f32 v83, v79, v84
v_fmac_f32 v81, v83, v83
v_cndmask_b32_e64 v81, 0, v81, s[86:87]
A 80,80,81
Zaaa 232,20,148
Maaa 232,16,52
v_add_f32_dpp v80, v80, v80 quad_perm:[1,0,3,2] row_mask:0xf bank_mask:0xf
s_nop 1
v_add_f32_dpp v80, v80, v80 quad_perm:[2,3,0,1] row_mask:0xf bank_mask:0xf
s_nop 1
v_add_f32_dpp v80, v80, v80 row_half_mirror row_mask:0xf bank_mask:0xf
s_nop 1
v_add_f32_dpp v80, v80, v80 row_mirror row_mask:0xf bank_mask:0xf
v_mov_b32 v81, v80
s_nop 1
v_permlane16_swap_b32 v80, v81
Zaaa 72,16,56
A 80,80,81
v_mul_f32 v80, 0x3acccccd, v80
K 80,80,80
v_lshlrev_b32 v80, 16, v80
v_add_f32 v80, 0x3727c5ac, v80
K 80,80,80
Maaa 72,20,152
v_lshlrev_b32 v80, 16, v80
v_sqrt_f32 v81, v80
s_nop 0
v_rcp_f32 v82, v81
v_fma_f32 v83, -v81, v81, v80
Zaaa 104,20,156
v_mul_f32 v82, 0.5, v82
v_fma_f32 v81, v83, v82, v81
K 86,81,81
v_lshlrev_b32 v86, 16, v86
v_rcp_f32 v87, v86
Maaa 104,16,60
W 4
K 85,84,84
v_lshlrev_b32 v85, 16, v85
s_waitcnt vmcnt(17)
v_sub_f32 v56, v56, v85
v_sub_f32 v57, v57, v85
K 80,56,57
Zaaa 108,24,60
v_lshlrev_b32 v56, 16, v80
v_and_b32 v57, 0xffff0000, v80
U 80,56,87
U 81,57,87
K 32,80,81
v_lshlrev_b32 v80, 16, v32
Maaa 108,28,156
v_and_b32 v81, 0xffff0000, v32
v_lshlrev_b32 v82, 16, v44
v_and_b32 v83, 0xffff0000, v44
U 84,80,82
v_fmac_f32 v84, v81, v83
Zaaa 76,28,152
Ta 8,15,0
v_sub_f32 v58, v58, v85
v_sub_f32 v59, v59, v85
K 80,58,59
v_lshlrev_b32 v58, 16, v80
v_and_b32 v59, 0xffff0000, v80
Maaa 76,24,56
Ta 10,15,1024
U 80,58,87
U 81,59,87
K 33,80,81
v_lshlrev_b32 v80, 16, v33
v_and_b32 v81, 0xffff0000, v33
Zaaa 236,24,52
Ta 12,15,8192
Ta 14,15,9216
v_lshlrev_b32 v82, 16, v45
v_and_b32 v83, 0xffff0000, v45
v_fmac_f32 v84, v80, v82
v_fmac_f32 v84, v81, v83
Maaa 236,28,148
v_sub_f32 v60, v60, v85
v_sub_f32 v61, v61, v85
K 80,60,61
v_lshlrev_b32 v60, 16, v80
v_and_b32 v61, 0xffff0000, v80
U 80,60,87
Zaaa 204,28,144
U 81,61,87
K 34,80,81
v_lshlrev_b32 v80, 16, v34
v_and_b32 v81, 0xffff0000, v34
v_lshlrev_b32 v82, 16, v46
v_and_b32 v83, 0xffff0000, v46
Maaa 204,24,48
v_fmac_f32 v84, v80, v82
v_fmac_f32 v84, v81, v83
v_sub_f32 v62, v62, v85
v_sub_f32 v63, v63, v85
K 80,62,63
Zaaa 172,24,44
v_lshlrev_b32 v62, 16, v80
v_and_b32 v63, 0xffff0000, v80
U 80,62,87
U 81,63,87
K 35,80,81
Maaa 172,28,140
v_lshlrev_b32 v80, 16, v35
v_and_b32 v81, 0xffff0000, v35
v_lshlrev_b32 v82, 16, v47
v_and_b32 v83, 0xffff0000, v47
v_fmac_f32 v84, v80, v82
Zvaa 236,28,136
v_fmac_f32 v84, v81, v83
v_sub_f32 v64, v64, v85
v_sub_f32 v65, v65, v85
K 80,64,65
v_lshlrev_b32 v64, 16, v80
Mvaa 236,24,40
v_and_b32 v65, 0xffff0000, v80
U 80,64,87
U 81,65,87
K 36,80,81
v_lshlrev_b32 v80, 16, v36
Zvaa 204,24,36
v_and_b32 v81, 0xffff0000, v36
v_lshlrev_b32 v82, 16, v48
v_and_b32 v83, 0xffff0000, v48
v_fmac_f32 v84, v80, v82
v_fmac_f32 v84, v81, v83
Mvaa 204,28,132
v_sub_f32 v66, v66, v85
v_sub_f32 v67, v67, v85
K 80,66,67
v_lshlrev_b32 v66, 16, v80
v_and_b32 v67, 0xffff0000, v80
U 80,66,87
Zvaa 172,28,128
U 81,67,87
K 37,80,81
v_lshlrev_b32 v80, 16, v37
v_and_b32 v81, 0xffff0000, v37
v_lshlrev_b32 v82, 16, v49
Mvaa 172,24,32
W 4
v_and_b32 v83, 0xffff0000, v49
v_fmac_f32 v84, v80, v82
v_fmac_f32 v84, v81, v83
v_sub_f32 v68, v68, v85
v_sub_f32 v69, v69, v85
Zvaa 176,0,32
K 80,68,69
v_lshlrev_b32 v68, 16, v80
v_and_b32 v69, 0xffff0000, v80
U 80,68,87
U 81,69,87
Mvaa 176,4,128
K 38,80,81
v_lshlrev_b32 v80, 16, v38
v_and_b32 v81, 0xffff0000, v38
v_lshlrev_b32 v82, 16, v50
v_and_b32 v83, 0xffff0000, v50
Zvaa 208,4,132
Ta 16,24,0
v_fmac_f32 v84, v80, v82
v_fmac_f32 v84, v81, v83
v_sub_f32 v70, v70, v85
v_sub_f32 v71, v71, v85
K 80,70,71
Mvaa 208,0,36
Ta 18,24,1024
v_lshlrev_b32 v70, 16, v80
v_and_b32 v71, 0xffff0000, v80
U 80,70,87
U 81,71,87
K 39,80,81
Zvaa 240,0,40
Ta 20,24,8192
Ta 22,24,9216
v_lshlrev_b32 v80, 16, v39
v_and_b32 v81, 0xffff0000, v39
v_lshlrev_b32 v82, 16, v51
v_and_b32 v83, 0xffff0000, v51
Mvaa 240,4,136
v_fmac_f32 v84, v80, v82
v_fmac_f32 v84, v81, v83
v_sub_f32 v72, v72, v85
v_sub_f32 v73, v73, v85
K 80,72,73
v_lshlrev_b32 v72, 16, v80
Zaaa 176,4,140
v_and_b32 v73, 0xffff0000, v80
U 80,72,87
U 81,73,87
K 40,80,81
v_lshlrev_b32 v80, 16, v40
v_and_b32 v81, 0xffff0000, v40
Maaa 176,0,44
v_lshlrev_b32 v82, 16, v52
v_and_b32 v83, 0xffff0000, v52
v_fmac_f32 v84, v80, v82
v_fmac_f32 v84, v81, v83
v_sub_f32 v74, v74, v85
Zaaa 208,0,48
v_sub_f32 v75, v75, v85
K 80,74,75
v_lshlrev_b32 v74, 16, v80
v_and_b32 v75, 0xffff0000, v80
U 80,74,87
Maaa 208,4,144
U 81,75,87
K 41,80,81
v_lshlrev_b32 v80, 16, v41
v_and_b32 v81, 0xffff0000, v41
)"
R"(v_lshlrev_b32 v82, 16, v53
Zaaa 240,4,148
v_and_b32 v83, 0xffff0000, v53
v_fmac_f32 v84, v80, v82
v_fmac_f32 v84, v81, v83
v_sub_f32 v76, v76, v85
v_sub_f32 v77, v77, v85
Maaa 240,0,52
K 80,76,77
v_lshlrev_b32 v76, 16, v80
v_and_b32 v77, 0xffff0000, v80
U 80,76,87
U 81,77,87
Zaaa 80,0,56
K 42,80,81
v_lshlrev_b32 v80, 16, v42
v_and_b32 v81, 0xffff0000, v42
v_lshlrev_b32 v82, 16, v54
v_and_b32 v83, 0xffff0000, v54
Maaa 80,4,152
v_fmac_f32 v84, v80, v82
v_fmac_f32 v84, v81, v83
v_sub_f32 v78, v78, v85
v_sub_f32 v79, v79, v85
K 80,78,79
v_lshlrev_b32 v78, 16, v80
Zaaa 112,4,156
v_and_b32 v79, 0xffff0000, v80
U 80,78,87
U 81,79,87
K 43,80,81
v_lshlrev_b32 v80, 16, v43
Maaa 112,0,60
W 4
v_and_b32 v81, 0xffff0000, v43
v_lshlrev_b32 v82, 16, v55
v_and_b32 v83, 0xffff0000, v55
v_fmac_f32 v84, v80, v82
v_fmac_f32 v84, v81, v83
Zaaa 116,8,60
Ta 24,25,0
Ta 26,25,1024
v_lshrrev_b32 v82, 9, v1
s_lshl_b32 s80, s14, 1
v_add_u32 v82, s80, v82
v_mul_u32_u24 v83, 1552, v82
v_add_u32 v83, v83, v88
Maaa 116,12,156
v_add_u32 v83, 131072, v83
ds_write_b128 v83, v[32:35] offset:0
ds_write_b128 v83, v[36:39] offset:512
ds_write_b128 v83, v[40:43] offset:1024
v_add_f32_dpp v84, v84, v84 quad_perm:[1,0,3,2] row_mask:0xf bank_mask:0xf
Zaaa 84,12,152
Ta 28,25,8192
Ta 30,25,9216
Maaa 84,8,56
v_add_f32_dpp v84, v84, v84 quad_perm:[2,3,0,1] row_mask:0xf bank_mask:0xf
s_nop 1
v_add_f32_dpp v84, v84, v84 row_half_mirror row_mask:0xf bank_mask:0xf
s_nop 1
v_add_f32_dpp v84, v84, v84 row_mirror row_mask:0xf bank_mask:0xf
v_mov_b32 v81, v84
s_nop 1
v_permlane16_swap_b32 v84, v81
A 84,84,81
Zaaa 244,8,52
K 80,84,84
v_lshlrev_b32 v80, 16, v80
s_lshl_b32 s80, s15, 3
v_add_u32 v82, s80, v82
v_sub_u32 v83, 2047, v82
v_ashrrev_i32 v81, 31, v80
v_or_b32 v81, 0x80000000, v81
Maaa 244,12,148
v_xor_b32 v80, v80, v81
v_and_b32 v80, 0xffff0000, v80
v_or_b32 v80, v80, v83
v_and_b32 v82, 7, v82
v_lshlrev_b32 v82, 2, v82
v_add_u32 v82, 143488, v82
Zaaa 212,12,144
ds_write_b32 v82, v80
Maaa 212,8,48
Zaaa 180,8,44
Maaa 180,12,140
Zvaa 244,12,136
s_waitcnt vmcnt(1) lgkmcnt(0)
s_barrier
Mvaa 244,8,40
s_add_u32 s64, s64, 128
v_bfe_u32 v80, v1, 4, 3
Zvaa 212,8,36
v_mul_u32_u24 v81, 1552, v80
Mvaa 212,12,132
s_and_b32 s64, s64, 1023
v_lshrrev_b32 v82, 7, v1
Zvaa 180,12,128
v_lshl_add_u32 v81, v82, 4, v81
Mvaa 180,8,32
s_lshl_b32 s80, s14, 7
Zvaa 184,16,32
s_lshl_b32 s65, s64, 12
s_add_u32 s80, s80, 131072
Mvaa 184,20,128
v_add_u32 v81, s80, v81
Zvaa 216,20,132
ds_read_b128 v[32:35], v81
Ta 0,10,32768
Mvaa 216,16,36
s_add_u32 m0, s62, 0
Ta 2,10,33792
Rv 36,81,512
Zvaa 248,16,40
s_and_b32 s80, s14, 2
Ta 4,10,40960
Ta 6,10,41984
Mvaa 248,20,136
s_lshl_b32 s80, s80, 7
Zaaa 184,20,140
D 16,44,65,0
v_subrev_u32 v81, s80, v81
Maaa 184,16,44
Rv 40,81,1024
Zaaa 216,16,48
v_lshlrev_b32 v80, 2, v80
Maaa 216,20,144
v_add_u32 v80, 143488, v80
Zaaa 248,20,148
D 17,44,65,1024
ds_read_b32 v84, v80
Maaa 248,16,52
v_mov_b32 v85, s13
Zaaa 88,16,56
v_lshrrev_b32 v83, 1, v1
Maaa 88,20,152
s_mul_i32 s82, s15, 10240
Zaaa 120,20,156
s_add_u32 s82, s82, 131072
D 18,44,65,2048
Maaa 120,16,60
s_lshl_b32 s80, s14, 10
Zaaa 124,24,60
s_add_u32 s83, s82, s80
Ra 60,26,47104
Maaa 124,28,156
Ra 156,27,47104
Zaaa 92,28,152
D 19,44,65,3072
Ra 152,27,45056
Maaa 92,24,56
s_add_u32 m0, s62, 4096
Ta 8,11,32768
Ta 10,11,33792
Zaaa 252,24,52
Ra 56,26,45056
Ta 12,11,40960
Maaa 252,28,148
Ta 14,11,41984
Ra 52,26,43008
Zaaa 220,28,144
W 9
buffer_store_dwordx4 v[32:35], v1, s[8:11], s83 offen sc1
s_add_u32 s83, s83, 4096
Maaa 220,24,48
Ra 148,27,43008
Ra 144,27,40960
Zaaa 188,24,44
Ra 48,26,40960
Ra 44,26,38912
Maaa 188,28,140
Ra 140,27,38912
Zvaa 252,28,136
D 20,44,65,0
Ra 136,27,36864
Mvaa 252,24,40
Ra 40,26,36864
Zvaa 220,24,36
Ra 36,26,34816
Mvaa 220,28,132
Ra 132,27,34816
Zvaa 188,28,128
buffer_store_dwordx4 v[36:39], v1, s[8:11], s83 offen sc1
Ra 128,27,32768
Mvaa 188,24,32
W 13
s_and_b32 s80, s80, 1024
s_add_u32 s83, s82, s80
Maaa 96,0,60
s_add_u32 s83, s83, 8192
Ra 32,26,32768
Maaa 96,4,156
Maaa 64,4,152
D 21,44,65,1024
Ta 16,12,32768
Maaa 64,0,56
W 9
Ta 18,12,33792
Ta 20,12,40960
Maaa 224,0,52
Ta 22,12,41984
Maaa 224,4,148
Maaa 192,4,144
buffer_store_dwordx4 v[40:43], v1, s[8:11], s83 offen sc1
Maaa 192,0,48
W 8
Maaa 160,0,44
Maaa 160,4,140
Mvaa 224,4,136
D 22,44,65,2048
Mvaa 224,0,40
W 4
Mvaa 192,0,36
Mvaa 192,4,132
Mvaa 160,4,128
D 23,44,65,3072
Mvaa 160,0,32
s_add_u32 m0, s61, 0
Mvaa 164,8,32
Mvaa 164,12,128
Mvaa 196,12,132
D 2,40,64,0
Ta 24,13,32768
Mvaa 196,8,36
Ta 26,13,33792
Ta 28,13,40960
Mvaa 228,8,40
Ta 30,13,41984
Mvaa 228,12,136
.Lh_0_1%=:
s_cbranch_execz .Lh_0_1_1%=
Maaa 164,12,140
D 3,40,64,1024
Maaa 164,8,44
Maaa 196,8,48
Maaa 196,12,144
Maaa 228,12,148
D 4,40,64,2048
Maaa 228,8,52
Maaa 68,8,56
Maaa 68,12,152
Maaa 100,12,156
D 5,40,64,3072
Maaa 100,8,60
W 4
s_add_u32 m0, s61, 4096
Maaa 104,16,60
Maaa 104,20,156
Maaa 72,20,152
D 6,40,64,0
Ta 0,14,32768
Maaa 72,16,56
Ta 2,14,33792
Ta 4,14,40960
Maaa 232,16,52
Ta 6,14,41984
Maaa 232,20,148
Maaa 200,20,144
D 7,40,64,1024
Maaa 200,16,48
Maaa 168,16,44
Maaa 168,20,140
Mvaa 232,20,136
D 8,40,64,2048
Mvaa 232,16,40
Mvaa 200,16,36
Mvaa 200,20,132
Mvaa 168,20,128
D 9,40,64,3072
v_bfe_u32 v147, v1, 5, 3
Mvaa 168,16,32
W 4
v_lshlrev_b32 v147, 13, v147
v_and_b32 v148, 16, v1
Mvaa 172,24,32
v_lshl_add_u32 v147, v148, 3, v147
Mvaa 172,28,128
s_lshl_b32 s92, s57, 2
s_add_u32 s92, s92, s14
s_lshl_b32 s92, s92, 16
Mvaa 204,28,132
buffer_load_dword v142, v147, s[48:51], s92 offen
Ta 8,15,32768
Mvaa 204,24,36
Ta 10,15,33792
Ta 12,15,40960
Mvaa 236,24,40
Ta 14,15,41984
Mvaa 236,28,136
Maaa 172,28,140
Maaa 172,24,44
Maaa 204,24,48
Maaa 204,28,144
Maaa 236,28,148
Maaa 236,24,52
Maaa 76,24,56
Maaa 76,28,152
Maaa 108,28,156
Maaa 108,24,60
W 4
Maaa 112,0,60
Maaa 112,4,156
Maaa 80,4,152
Ta 16,24,32768
Ta 18,24,33792
Maaa 80,0,56
Ta 20,24,40960
Ta 22,24,41984
Maaa 240,0,52
Maaa 240,4,148
Maaa 208,4,144
Maaa 208,0,48
Maaa 176,0,44
Maaa 176,4,140
Mvaa 240,4,136
Mvaa 240,0,40
Mvaa 208,0,36
Mvaa 208,4,132
Mvaa 176,4,128
Mvaa 176,0,32
W 4
Mvaa 180,8,32
Ta 24,25,32768
Ta 26,25,33792
Mvaa 180,12,128
Mvaa 212,12,132
Ta 28,25,40960
Ta 30,25,41984
Mvaa 212,8,36
Mvaa 244,8,40
Mvaa 244,12,136
Maaa 180,12,140
Maaa 180,8,44
Maaa 212,8,48
Maaa 212,12,144
Maaa 244,12,148
s_waitcnt vmcnt(1) lgkmcnt(0)
s_barrier
s_cmp_eq_u32 s14, 0
s_cbranch_scc0 .Lpubr_14%=
s_lshl_b32 s80, s15, 6
s_add_u32 s80, s80, 40960
s_mov_b64 exec, 0xff
buffer_store_dwordx2 v[84:85], v83, s[8:11], s80 offen sc1
s_mov_b64 exec, -1
.Lpubr_14%=:
Maaa 244,8,52
s_add_u32 s64, s64, 128
Maaa 84,8,56
Maaa 84,12,152
s_and_b32 s64, s64, 1023
Maaa 116,12,156
Maaa 116,8,60
Maaa 120,16,60
s_lshl_b32 s65, s64, 12
Maaa 120,20,156
Maaa 88,20,152
s_add_u32 m0, s62, 32768
Ta 0,10,0
Ta 2,10,1024
Maaa 88,16,56
Ta 4,10,8192
Ta 6,10,9216
Maaa 248,16,52
Maaa 248,20,148
D 16,44,65,0
Maaa 216,20,144
Maaa 216,16,48
Maaa 184,16,44
Maaa 184,20,140
D 17,44,65,1024
Mvaa 248,20,136
Mvaa 248,16,40
Mvaa 216,16,36
Mvaa 216,20,132
D 18,44,65,2048
Mvaa 184,20,128
Mvaa 184,16,32
Mvaa 188,24,32
Ra 32,26,0
Mvaa 188,28,128
D 19,44,65,3072
Ra 128,27,0
Mvaa 220,28,132
s_add_u32 m0, s62, 36864
Ra 132,27,2048
Ta 8,11,0
Mvaa 220,24,36
Ta 10,11,1024
Ra 36,26,2048
Mvaa 252,24,40
Ta 12,11,8192
Ta 14,11,9216
Mvaa 252,28,136
D 20,44,65,0
Ra 40,26,4096
Maaa 188,28,140
Ra 136,27,4096
Ra 140,27,6144
Maaa 188,24,44
)"
R"(Ra 44,26,6144
Maaa 220,24,48
Ra 48,26,8192
Maaa 220,28,144
D 21,44,65,1024
Ra 144,27,8192
Maaa 252,28,148
Ra 148,27,10240
Maaa 252,24,52
Ra 52,26,10240
Maaa 92,24,56
Ra 56,26,12288
Maaa 92,28,152
D 22,44,65,2048
Ra 152,27,12288
Maaa 124,28,156
Ra 156,27,14336
Maaa 124,24,60
W 13
Ra 60,26,14336
Mvaa 160,0,32
Mvaa 160,4,128
D 23,44,65,3072
Mvaa 192,4,132
s_add_u32 m0, s61, 32768
Ta 16,12,0
Ta 18,12,1024
Mvaa 192,0,36
W 10
Ta 20,12,8192
Ta 22,12,9216
Mvaa 224,0,40
Mvaa 224,4,136
D 2,40,64,0
Maaa 160,4,140
Maaa 160,0,44
W 8
Maaa 192,0,48
Maaa 192,4,144
D 3,40,64,1024
Maaa 224,4,148
Maaa 224,0,52
W 4
Maaa 64,0,56
Maaa 64,4,152
D 4,40,64,2048
Maaa 96,4,156
Maaa 96,0,60
Maaa 100,8,60
Maaa 100,12,156
D 5,40,64,3072
Maaa 68,12,152
s_add_u32 m0, s61, 36864
Ta 24,13,0
Ta 26,13,1024
Maaa 68,8,56
Ta 28,13,8192
Ta 30,13,9216
Maaa 228,8,52
Maaa 228,12,148
D 6,40,64,0
Maaa 196,12,144
Maaa 196,8,48
Maaa 164,8,44
Maaa 164,12,140
D 7,40,64,1024
Mvaa 228,12,136
Mvaa 228,8,40
Mvaa 196,8,36
Mvaa 196,12,132
D 8,40,64,2048
Mvaa 164,12,128
Mvaa 164,8,32
W 4
Mvaa 168,16,32
Mvaa 168,20,128
D 9,40,64,3072
Mvaa 200,20,132
Ta 0,14,0
Ta 2,14,1024
Mvaa 200,16,36
Ta 4,14,8192
Ta 6,14,9216
Mvaa 232,16,40
Mvaa 232,20,136
global_load_dword v142, v[140:141], off
v_lshl_add_u64 v[140:141], v[140:141], 0, s[68:69]
Maaa 168,20,140
Maaa 168,16,44
Maaa 200,16,48
Maaa 200,20,144
Maaa 232,20,148
Maaa 232,16,52
Maaa 72,16,56
Maaa 72,20,152
Maaa 104,20,156
Maaa 104,16,60
W 4
Maaa 108,24,60
Maaa 108,28,156
Maaa 76,28,152
Ta 8,15,0
Ta 10,15,1024
Maaa 76,24,56
Ta 12,15,8192
Ta 14,15,9216
Maaa 236,24,52
Maaa 236,28,148
Maaa 204,28,144
Maaa 204,24,48
Maaa 172,24,44
Maaa 172,28,140
Mvaa 236,28,136
Mvaa 236,24,40
Mvaa 204,24,36
Mvaa 204,28,132
Mvaa 172,28,128
Mvaa 172,24,32
W 4
Mvaa 176,0,32
Mvaa 176,4,128
Mvaa 208,4,132
Ta 16,24,0
Ta 18,24,1024
Mvaa 208,0,36
Ta 20,24,8192
Ta 22,24,9216
Mvaa 240,0,40
Mvaa 240,4,136
Maaa 176,4,140
Maaa 176,0,44
Maaa 208,0,48
Maaa 208,4,144
Maaa 240,4,148
Maaa 240,0,52
Maaa 80,0,56
Maaa 80,4,152
Maaa 112,4,156
Maaa 112,0,60
W 4
Maaa 116,8,60
Ta 24,25,0
Ta 26,25,1024
Maaa 116,12,156
Maaa 84,12,152
Ta 28,25,8192
Ta 30,25,9216
Maaa 84,8,56
Maaa 244,8,52
Maaa 244,12,148
Maaa 212,12,144
Maaa 212,8,48
Maaa 180,8,44
Maaa 180,12,140
Mvaa 244,12,136
s_waitcnt vmcnt(1) lgkmcnt(0)
s_barrier
Mvaa 244,8,40
s_add_u32 s64, s64, 128
Mvaa 212,8,36
Mvaa 212,12,132
s_and_b32 s64, s64, 1023
Mvaa 180,12,128
Mvaa 180,8,32
Mvaa 184,16,32
s_lshl_b32 s65, s64, 12
Mvaa 184,20,128
Mvaa 216,20,132
s_add_u32 m0, s62, 0
Ta 0,10,32768
Ta 2,10,33792
Mvaa 216,16,36
Ta 4,10,40960
Ta 6,10,41984
Mvaa 248,16,40
Mvaa 248,20,136
D 16,44,65,0
Maaa 184,20,140
Maaa 184,16,44
Maaa 216,16,48
Maaa 216,20,144
D 17,44,65,1024
Maaa 248,20,148
Maaa 248,16,52
Maaa 88,16,56
Maaa 88,20,152
D 18,44,65,2048
Maaa 120,20,156
Maaa 120,16,60
Maaa 124,24,60
Ra 60,26,47104
Maaa 124,28,156
D 19,44,65,3072
Ra 156,27,47104
Maaa 92,28,152
s_add_u32 m0, s62, 4096
Ra 152,27,45056
Ta 8,11,32768
Maaa 92,24,56
Ta 10,11,33792
Ra 56,26,45056
Maaa 252,24,52
Ta 12,11,40960
Ta 14,11,41984
Maaa 252,28,148
D 20,44,65,0
Ra 52,26,43008
Maaa 220,28,144
Ra 148,27,43008
Ra 144,27,40960
Maaa 220,24,48
Ra 48,26,40960
Maaa 188,24,44
Ra 44,26,38912
Maaa 188,28,140
D 21,44,65,1024
Ra 140,27,38912
Mvaa 252,28,136
Ra 136,27,36864
Mvaa 252,24,40
Ra 40,26,36864
Mvaa 220,24,36
Ra 36,26,34816
Mvaa 220,28,132
D 22,44,65,2048
Ra 132,27,34816
Mvaa 188,28,128
Ra 128,27,32768
Mvaa 188,24,32
W 13
Ra 32,26,32768
Maaa 96,0,60
Maaa 96,4,156
D 23,44,65,3072
Maaa 64,4,152
s_add_u32 m0, s61, 0
Ta 16,12,32768
Ta 18,12,33792
Maaa 64,0,56
W 10
Ta 20,12,40960
Ta 22,12,41984
Maaa 224,0,52
Maaa 224,4,148
D 2,40,64,0
Maaa 192,4,144
Maaa 192,0,48
W 8
Maaa 160,0,44
Maaa 160,4,140
D 3,40,64,1024
Mvaa 224,4,136
Mvaa 224,0,40
W 4
Mvaa 192,0,36
Mvaa 192,4,132
D 4,40,64,2048
Mvaa 160,4,128
Mvaa 160,0,32
Mvaa 164,8,32
Mvaa 164,12,128
D 5,40,64,3072
Mvaa 196,12,132
s_add_u32 m0, s61, 4096
Ta 24,13,32768
Ta 26,13,33792
Mvaa 196,8,36
Ta 28,13,40960
Ta 30,13,41984
Mvaa 228,8,40
Mvaa 228,12,136
D 6,40,64,0
Maaa 164,12,140
Maaa 164,8,44
Maaa 196,8,48
Maaa 196,12,144
D 7,40,64,1024
Maaa 228,12,148
Maaa 228,8,52
Maaa 68,8,56
Maaa 68,12,152
D 8,40,64,2048
Maaa 100,12,156
Maaa 100,8,60
W 4
Maaa 104,16,60
Maaa 104,20,156
D 9,40,64,3072
Maaa 72,20,152
Ta 0,14,32768
Ta 2,14,33792
Maaa 72,16,56
Ta 4,14,40960
Ta 6,14,41984
Maaa 232,16,52
Maaa 232,20,148
global_load_dword v142, v[140:141], off
v_lshl_add_u64 v[140:141], v[140:141], 0, s[68:69]
Maaa 200,20,144
Maaa 200,16,48
Maaa 168,16,44
Maaa 168,20,140
Mvaa 232,20,136
Mvaa 232,16,40
Mvaa 200,16,36
Mvaa 200,20,132
Mvaa 168,20,128
Mvaa 168,16,32
W 4
Mvaa 172,24,32
Mvaa 172,28,128
Mvaa 204,28,132
Ta 8,15,32768
Ta 10,15,33792
Mvaa 204,24,36
Ta 12,15,40960
Ta 14,15,41984
Mvaa 236,24,40
Mvaa 236,28,136
Maaa 172,28,140
Maaa 172,24,44
Maaa 204,24,48
Maaa 204,28,144
Maaa 236,28,148
Maaa 236,24,52
Maaa 76,24,56
Maaa 76,28,152
Maaa 108,28,156
Maaa 108,24,60
W 4
Maaa 112,0,60
Maaa 112,4,156
Maaa 80,4,152
Ta 16,24,32768
Ta 18,24,33792
Maaa 80,0,56
Ta 20,24,40960
Ta 22,24,41984
Maaa 240,0,52
Maaa 240,4,148
Maaa 208,4,144
Maaa 208,0,48
Maaa 176,0,44
Maaa 176,4,140
Mvaa 240,4,136
Mvaa 240,0,40
Mvaa 208,0,36
Mvaa 208,4,132
Mvaa 176,4,128
Mvaa 176,0,32
W 4
Mvaa 180,8,32
Ta 24,25,32768
Ta 26,25,33792
Mvaa 180,12,128
Mvaa 212,12,132
Ta 28,25,40960
Ta 30,25,41984
Mvaa 212,8,36
Mvaa 244,8,40
Mvaa 244,12,136
.Lh_0_2%=:
s_cbranch_execz .Lh_0_2_1%=
Maaa 180,12,140
Maaa 180,8,44
Maaa 212,8,48
Maaa 212,12,144
Maaa 244,12,148
s_waitcnt vmcnt(1) lgkmcnt(0)
s_barrier
Maaa 244,8,52
s_add_u32 s64, s64, 128
Maaa 84,8,56
Maaa 84,12,152
s_and_b32 s64, s64, 1023
Maaa 116,12,156
Maaa 116,8,60
Maaa 120,16,60
s_lshl_b32 s65, s64, 12
Maaa 120,20,156
Maaa 88,20,152
Ta 0,10,0
Ta 2,10,1024
Maaa 88,16,56
s_add_u32 m0, s62, 32768
Ta 4,10,8192
Ta 6,10,9216
Maaa 248,16,52
Maaa 248,20,148
Maaa 216,20,144
D 16,44,65,0
Maaa 216,16,48
Maaa 184,16,44
Maaa 184,20,140
Mvaa 248,20,136
D 17,44,65,1024
Mvaa 248,16,40
Mvaa 216,16,36
Mvaa 216,20,132
Mvaa 184,20,128
)"
R"(D 18,44,65,2048
Mvaa 184,16,32
Mvaa 188,24,32
Ra 32,26,0
Mvaa 188,28,128
Ra 128,27,0
Mvaa 220,28,132
D 19,44,65,3072
Ra 132,27,2048
Mvaa 220,24,36
s_add_u32 m0, s62, 36864
Ta 8,11,0
Ta 10,11,1024
Mvaa 252,24,40
Ra 36,26,2048
Ta 12,11,8192
Mvaa 252,28,136
Ta 14,11,9216
Ra 40,26,4096
Maaa 188,28,140
D 20,44,65,0
Ra 136,27,4096
Maaa 188,24,44
Ra 140,27,6144
Ra 44,26,6144
Maaa 220,24,48
Ra 48,26,8192
Maaa 220,28,144
Ra 144,27,8192
Maaa 252,28,148
D 21,44,65,1024
Ra 148,27,10240
Maaa 252,24,52
Ra 52,26,10240
Maaa 92,24,56
Ra 56,26,12288
Maaa 92,28,152
Ra 152,27,12288
Maaa 124,28,156
D 22,44,65,2048
Ra 156,27,14336
Maaa 124,24,60
W 13
Ra 60,26,14336
Mvaa 160,0,32
Mvaa 160,4,128
Mvaa 192,4,132
D 23,44,65,3072
Ta 16,12,0
Mvaa 192,0,36
W 9
s_add_u32 m0, s61, 32768
Ta 18,12,1024
Mvaa 224,0,40
Ta 20,12,8192
Ta 22,12,9216
Mvaa 224,4,136
Maaa 160,4,140
D 2,40,64,0
Maaa 160,0,44
W 8
Maaa 192,0,48
Maaa 192,4,144
Maaa 224,4,148
D 3,40,64,1024
Maaa 224,0,52
W 4
Maaa 64,0,56
Maaa 64,4,152
Maaa 96,4,156
D 4,40,64,2048
Maaa 96,0,60
Maaa 100,8,60
Maaa 100,12,156
Maaa 68,12,152
D 5,40,64,3072
Ta 24,13,0
Maaa 68,8,56
s_add_u32 m0, s61, 36864
Ta 26,13,1024
Ta 28,13,8192
Maaa 228,8,52
Ta 30,13,9216
Maaa 228,12,148
Maaa 196,12,144
D 6,40,64,0
Maaa 196,8,48
Maaa 164,8,44
Maaa 164,12,140
Mvaa 228,12,136
D 7,40,64,1024
Mvaa 228,8,40
Mvaa 196,8,36
Mvaa 196,12,132
Mvaa 164,12,128
D 8,40,64,2048
Mvaa 164,8,32
W 4
Mvaa 168,16,32
Mvaa 168,20,128
Mvaa 200,20,132
D 9,40,64,3072
Ta 0,14,0
Mvaa 200,16,36
s_add_u32 s92, s92, 2097152
Ta 2,14,1024
Ta 4,14,8192
Mvaa 232,16,40
Ta 6,14,9216
Mvaa 232,20,136
Maaa 168,20,140
buffer_load_dword v142, v147, s[48:51], s92 offen
Maaa 168,16,44
s_add_u32 s92, s92, 2097152
Maaa 200,16,48
Maaa 200,20,144
Maaa 232,20,148
buffer_load_dword v142, v147, s[48:51], s92 offen
Maaa 232,16,52
Maaa 72,16,56
Maaa 72,20,152
Maaa 104,20,156
Maaa 104,16,60
W 4
Maaa 108,24,60
Maaa 108,28,156
Maaa 76,28,152
Ta 8,15,0
Ta 10,15,1024
Maaa 76,24,56
Ta 12,15,8192
Ta 14,15,9216
Maaa 236,24,52
Maaa 236,28,148
Maaa 204,28,144
Maaa 204,24,48
Maaa 172,24,44
Maaa 172,28,140
Mvaa 236,28,136
Mvaa 236,24,40
Mvaa 204,24,36
Mvaa 204,28,132
Mvaa 172,28,128
Mvaa 172,24,32
W 4
Mvaa 176,0,32
Mvaa 176,4,128
Mvaa 208,4,132
Ta 16,24,0
Ta 18,24,1024
Mvaa 208,0,36
Ta 20,24,8192
Ta 22,24,9216
Mvaa 240,0,40
Mvaa 240,4,136
Maaa 176,4,140
Maaa 176,0,44
Maaa 208,0,48
Maaa 208,4,144
Maaa 240,4,148
Maaa 240,0,52
Maaa 80,0,56
Maaa 80,4,152
Maaa 112,4,156
Maaa 112,0,60
W 4
Maaa 116,8,60
Ta 24,25,0
Ta 26,25,1024
Maaa 116,12,156
Maaa 84,12,152
Ta 28,25,8192
Ta 30,25,9216
Maaa 84,8,56
Maaa 244,8,52
Maaa 244,12,148
Maaa 212,12,144
Maaa 212,8,48
Maaa 180,8,44
Maaa 180,12,140
Mvaa 244,12,136
s_waitcnt vmcnt(2) lgkmcnt(0)
s_barrier
Mvaa 244,8,40
s_add_u32 s64, s64, 128
Mvaa 212,8,36
Mvaa 212,12,132
s_and_b32 s64, s64, 1023
Mvaa 180,12,128
Mvaa 180,8,32
Mvaa 184,16,32
s_lshl_b32 s65, s64, 12
Mvaa 184,20,128
Mvaa 216,20,132
Ta 0,10,32768
Ta 2,10,33792
Mvaa 216,16,36
s_add_u32 m0, s62, 0
Ta 4,10,40960
Ta 6,10,41984
Mvaa 248,16,40
Mvaa 248,20,136
Maaa 184,20,140
D 16,44,65,0
Maaa 184,16,44
Maaa 216,16,48
Maaa 216,20,144
Maaa 248,20,148
D 17,44,65,1024
Maaa 248,16,52
Maaa 88,16,56
Maaa 88,20,152
Maaa 120,20,156
D 18,44,65,2048
Maaa 120,16,60
Maaa 124,24,60
Ra 60,26,47104
Maaa 124,28,156
Ra 156,27,47104
Maaa 92,28,152
D 19,44,65,3072
Ra 152,27,45056
Maaa 92,24,56
s_add_u32 m0, s62, 4096
Ta 8,11,32768
Ta 10,11,33792
Maaa 252,24,52
Ra 56,26,45056
Ta 12,11,40960
Maaa 252,28,148
Ta 14,11,41984
Ra 52,26,43008
Maaa 220,28,144
D 20,44,65,0
v_lshrrev_b32 v28, 1, v1
Maaa 220,24,48
s_lshl_b32 s80, s60, 9
Ra 148,27,43008
Ra 144,27,40960
Maaa 188,24,44
s_add_u32 s80, s80, 40960
Ra 48,26,40960
Ra 44,26,38912
Maaa 188,28,140
Ra 140,27,38912
Mvaa 252,28,136
D 21,44,65,1024
Ra 136,27,36864
Mvaa 252,24,40
Ra 40,26,36864
Mvaa 220,24,36
Ra 36,26,34816
Mvaa 220,28,132
Ra 132,27,34816
Mvaa 188,28,128
buffer_load_dwordx2 v[28:29], v28, s[8:11], s80 offen sc1
Ra 128,27,32768
Mvaa 188,24,32
W 13
Ra 32,26,32768
Maaa 96,0,60
Maaa 96,4,156
Maaa 64,4,152
D 22,44,65,2048
Ta 16,12,32768
Maaa 64,0,56
W 9
Ta 18,12,33792
Ta 20,12,40960
Maaa 224,0,52
Ta 22,12,41984
Maaa 224,4,148
Maaa 192,4,144
D 23,44,65,3072
Maaa 192,0,48
W 8
s_add_u32 m0, s61, 0
Maaa 160,0,44
Maaa 160,4,140
Mvaa 224,4,136
D 2,40,64,0
Mvaa 224,0,40
W 4
Mvaa 192,0,36
Mvaa 192,4,132
Mvaa 160,4,128
D 3,40,64,1024
Mvaa 160,0,32
Mvaa 164,8,32
Mvaa 164,12,128
Mvaa 196,12,132
D 4,40,64,2048
Ta 24,13,32768
Mvaa 196,8,36
Ta 26,13,33792
Ta 28,13,40960
Mvaa 228,8,40
Ta 30,13,41984
Mvaa 228,12,136
Maaa 164,12,140
D 5,40,64,3072
Maaa 164,8,44
s_add_u32 m0, s61, 4096
Maaa 196,8,48
Maaa 196,12,144
Maaa 228,12,148
D 6,40,64,0
Maaa 228,8,52
Maaa 68,8,56
Maaa 68,12,152
Maaa 100,12,156
D 7,40,64,1024
Maaa 100,8,60
W 4
Maaa 104,16,60
Maaa 104,20,156
Maaa 72,20,152
D 8,40,64,2048
Ta 0,14,32768
Maaa 72,16,56
Ta 2,14,33792
Ta 4,14,40960
Maaa 232,16,52
Ta 6,14,41984
Maaa 232,20,148
Maaa 200,20,144
D 9,40,64,3072
Maaa 200,16,48
Maaa 168,16,44
Maaa 168,20,140
Mvaa 232,20,136
s_waitcnt vmcnt(10)
v_cmp_eq_u32 vcc, s13, v29
s_cmp_eq_u64 vcc, -1
s_cbranch_scc0 .Lspin_15%=
.Lok_16%=:
global_load_dword v142, v146, s[6:7]
Mvaa 232,16,40
Mvaa 200,16,36
v_bfe_u32 v144, v1, 8, 1
Mvaa 200,20,132
v_mul_u32_u24 v144, 10240, v144
Mvaa 168,20,128
v_and_b32 v145, 0xf0, v1
Mvaa 168,16,32
W 4
v_lshl_add_u32 v144, v145, 3, v144
Mvaa 172,24,32
s_mul_i32 s91, s58, 20480
Mvaa 172,28,128
s_add_u32 s91, s91, s28
Mvaa 204,28,132
buffer_load_dword v142, v144, s[8:11], s91 offen offset:0 sc1
Ta 8,15,32768
Mvaa 204,24,36
Ta 10,15,33792
Ta 12,15,40960
Mvaa 236,24,40
Ta 14,15,41984
Mvaa 236,28,136
Maaa 172,28,140
Maaa 172,24,44
Maaa 204,24,48
Maaa 204,28,144
Maaa 236,28,148
Maaa 236,24,52
Maaa 76,24,56
Maaa 76,28,152
Maaa 108,28,156
Maaa 108,24,60
W 4
Maaa 112,0,60
Maaa 112,4,156
Maaa 80,4,152
Ta 16,24,32768
Ta 18,24,33792
Maaa 80,0,56
Ta 20,24,40960
Ta 22,24,41984
Maaa 240,0,52
Maaa 240,4,148
Maaa 208,4,144
Maaa 208,0,48
Maaa 176,0,44
Maaa 176,4,140
Mvaa 240,4,136
Mvaa 240,0,40
Mvaa 208,0,36
Mvaa 208,4,132
Mvaa 176,4,128
Mvaa 176,0,32
W 4
Mvaa 180,8,32
Ta 24,25,32768
Ta 26,25,33792
Mvaa 180,12,128
Mvaa 212,12,132
Ta 28,25,40960
Ta 30,25,41984
Mvaa 212,8,36
)"
R"(Mvaa 244,8,40
Mvaa 244,12,136
Maaa 180,12,140
Maaa 180,8,44
Maaa 212,8,48
Maaa 212,12,144
Maaa 244,12,148
s_waitcnt vmcnt(2) lgkmcnt(0)
s_barrier
Maaa 244,8,52
s_add_u32 s64, s64, 128
Maaa 84,8,56
Maaa 84,12,152
s_and_b32 s64, s64, 1023
Maaa 116,12,156
Maaa 116,8,60
Maaa 120,16,60
s_lshl_b32 s65, s64, 12
Maaa 120,20,156
Maaa 88,20,152
Ta 0,10,0
Ta 2,10,1024
Maaa 88,16,56
s_add_u32 m0, s62, 32768
Ta 4,10,8192
Ta 6,10,9216
Maaa 248,16,52
Maaa 248,20,148
Maaa 216,20,144
D 16,44,65,0
Maaa 216,16,48
Maaa 184,16,44
Maaa 184,20,140
Mvaa 248,20,136
D 17,44,65,1024
Mvaa 248,16,40
Mvaa 216,16,36
Mvaa 216,20,132
Mvaa 184,20,128
D 18,44,65,2048
Mvaa 184,16,32
Mvaa 188,24,32
Ra 32,26,0
Mvaa 188,28,128
Ra 128,27,0
Mvaa 220,28,132
D 19,44,65,3072
Ra 132,27,2048
Mvaa 220,24,36
s_add_u32 m0, s62, 36864
Ta 8,11,0
Ta 10,11,1024
Mvaa 252,24,40
Ra 36,26,2048
Ta 12,11,8192
Mvaa 252,28,136
Ta 14,11,9216
Ra 40,26,4096
Maaa 188,28,140
D 20,44,65,0
Ra 136,27,4096
Maaa 188,24,44
Ra 140,27,6144
Ra 44,26,6144
Maaa 220,24,48
Ra 48,26,8192
Maaa 220,28,144
Ra 144,27,8192
Maaa 252,28,148
D 21,44,65,1024
Ra 148,27,10240
Maaa 252,24,52
Ra 52,26,10240
Maaa 92,24,56
Ra 56,26,12288
Maaa 92,28,152
Ra 152,27,12288
Maaa 124,28,156
D 22,44,65,2048
Ra 156,27,14336
Maaa 124,24,60
W 13
Ra 60,26,14336
Mvaa 160,0,32
Mvaa 160,4,128
Mvaa 192,4,132
D 23,44,65,3072
Ta 16,12,0
Mvaa 192,0,36
W 9
s_add_u32 m0, s61, 32768
Ta 18,12,1024
Mvaa 224,0,40
Ta 20,12,8192
Ta 22,12,9216
Mvaa 224,4,136
.Lh_0_3%=:
s_cbranch_execz .Lh_0_3_1%=
Maaa 160,4,140
D 2,40,64,0
Maaa 160,0,44
W 8
Maaa 192,0,48
Maaa 192,4,144
Maaa 224,4,148
D 3,40,64,1024
Maaa 224,0,52
W 4
Maaa 64,0,56
Maaa 64,4,152
Maaa 96,4,156
D 4,40,64,2048
Maaa 96,0,60
Maaa 100,8,60
Maaa 100,12,156
Maaa 68,12,152
D 5,40,64,3072
Ta 24,13,0
Maaa 68,8,56
s_add_u32 m0, s61, 36864
Ta 26,13,1024
Ta 28,13,8192
Maaa 228,8,52
Ta 30,13,9216
Maaa 228,12,148
Maaa 196,12,144
D 6,40,64,0
Maaa 196,8,48
Maaa 164,8,44
Maaa 164,12,140
Mvaa 228,12,136
D 7,40,64,1024
Mvaa 228,8,40
Mvaa 196,8,36
Mvaa 196,12,132
Mvaa 164,12,128
D 8,40,64,2048
Mvaa 164,8,32
W 4
Mvaa 168,16,32
Mvaa 168,20,128
Mvaa 200,20,132
D 9,40,64,3072
v_add_u32 v2, 131072, v10
Mvaa 200,16,36
Ta 0,14,0
v_add_u32 v3, 131072, v11
Mvaa 232,16,40
Ta 2,14,1024
v_add_u32 v4, 131072, v12
Mvaa 232,20,136
Ta 4,14,8192
Ta 6,14,9216
Maaa 168,20,140
buffer_load_dword v142, v144, s[8:11], s91 offen offset:2048 sc1
v_add_u32 v5, 131072, v13
Maaa 168,16,44
s_add_u32 s91, s91, 4096
v_add_u32 v6, 131072, v14
Maaa 200,16,48
v_add_u32 v7, 131072, v15
Maaa 200,20,144
Maaa 232,20,148
v_add_u32 v8, 131072, v24
Maaa 232,16,52
Maaa 72,16,56
v_add_u32 v9, 131072, v25
Maaa 72,20,152
s_add_u32 s65, s5, 0
s_cmp_ge_u32 s65, 20
s_cselect_b32 s90, 20, 0
s_sub_u32 s65, s65, s90
Maaa 104,20,156
s_lshl_b32 s65, s65, 18
s_add_u32 m0, s63, 131072
Maaa 104,16,60
W 4
Maaa 108,24,60
Maaa 108,28,156
Maaa 76,28,152
Ta 8,15,0
Ta 10,15,1024
Maaa 76,24,56
D 16,48,65,0
Ta 12,15,8192
Maaa 236,24,52
Ta 14,15,9216
Maaa 236,28,148
Maaa 204,28,144
Maaa 204,24,48
D 17,48,65,1024
Maaa 172,24,44
Maaa 172,28,140
Mvaa 236,28,136
Mvaa 236,24,40
Mvaa 204,24,36
D 18,48,65,2048
Mvaa 204,28,132
Mvaa 172,28,128
Mvaa 172,24,32
W 4
Mvaa 176,0,32
Mvaa 176,4,128
D 19,48,65,3072
Mvaa 208,4,132
s_add_u32 m0, s63, 135168
Ta 16,24,0
Ta 18,24,1024
Mvaa 208,0,36
Ta 20,24,8192
Ta 22,24,9216
Mvaa 240,0,40
Mvaa 240,4,136
Maaa 176,4,140
D 20,48,65,0
Maaa 176,0,44
Maaa 208,0,48
Maaa 208,4,144
Maaa 240,4,148
Maaa 240,0,52
D 21,48,65,1024
Maaa 80,0,56
Maaa 80,4,152
Maaa 112,4,156
Maaa 112,0,60
W 4
Maaa 116,8,60
D 22,48,65,2048
Ta 24,25,0
Maaa 116,12,156
Ta 26,25,1024
Maaa 84,12,152
Ta 28,25,8192
Ta 30,25,9216
Maaa 84,8,56
Maaa 244,8,52
D 23,48,65,3072
Maaa 244,12,148
Maaa 212,12,144
Maaa 212,8,48
Maaa 180,8,44
Maaa 180,12,140
Mvaa 244,12,136
s_waitcnt vmcnt(9) lgkmcnt(0)
s_barrier
Mvaa 244,8,40
s_add_u32 s65, s5, 4
Mvaa 212,8,36
Mvaa 212,12,132
s_cmp_ge_u32 s65, 20
s_cselect_b32 s90, 20, 0
Mvaa 180,12,128
Mvaa 180,8,32
s_sub_u32 s65, s65, s90
Mvaa 184,16,32
Mvaa 184,20,128
s_lshl_b32 s65, s65, 18
Mvaa 216,20,132
Ta 0,10,32768
Ta 2,10,33792
Mvaa 216,16,36
Mvaa 248,16,40
s_add_u32 m0, s63, 0
Mvaa 248,20,136
Maaa 184,20,140
D 16,48,65,0
Maaa 184,16,44
Maaa 216,16,48
Maaa 216,20,144
Maaa 248,20,148
D 17,48,65,1024
Ta 4,11,32768
Maaa 248,16,52
Ta 6,11,33792
Maaa 88,16,56
Maaa 88,20,152
Maaa 120,20,156
D 18,48,65,2048
Maaa 120,16,60
Maaa 124,24,60
Ra 60,26,47104
Maaa 124,28,156
Maaa 92,28,152
D 19,48,65,3072
Ta 8,12,32768
Maaa 92,24,56
s_add_u32 m0, s63, 4096
Ta 10,12,33792
Ra 56,26,45056
Maaa 252,24,52
Ra 52,26,43008
Maaa 252,28,148
Maaa 220,28,144
D 20,48,65,0
Maaa 220,24,48
Ra 48,26,40960
Maaa 188,24,44
Ra 44,26,38912
Maaa 188,28,140
Mvaa 252,28,136
D 21,48,65,1024
Ta 12,13,32768
Mvaa 252,24,40
Ta 14,13,33792
Ra 40,26,36864
Mvaa 220,24,36
Ra 36,26,34816
Mvaa 220,28,132
Mvaa 188,28,128
D 22,48,65,2048
Mvaa 188,24,32
Ra 32,26,32768
W 6
Maaa 96,0,60
Maaa 64,0,56
Maaa 224,0,52
D 23,48,65,3072
Ta 16,14,32768
Maaa 192,0,48
W 1
s_add_u32 s65, s5, 8
s_cmp_ge_u32 s65, 20
s_cselect_b32 s90, 20, 0
Maaa 160,0,44
s_sub_u32 s65, s65, s90
s_lshl_b32 s65, s65, 18
Ta 18,14,33792
Mvaa 224,0,40
s_add_u32 m0, s63, 65536
Mvaa 192,0,36
D 16,48,65,0
Mvaa 160,0,32
Mvaa 164,4,32
Mvaa 196,4,36
Mvaa 228,4,40
D 17,48,65,1024
Ta 20,15,32768
Maaa 164,4,44
Ta 22,15,33792
Maaa 196,4,48
Maaa 228,4,52
Maaa 68,4,56
D 18,48,65,2048
Maaa 100,4,60
Maaa 104,8,60
Maaa 72,8,56
Maaa 232,8,52
D 19,48,65,3072
Ta 24,24,32768
Maaa 200,8,48
s_add_u32 m0, s63, 69632
Ta 26,24,33792
Maaa 168,8,44
Mvaa 232,8,40
Mvaa 200,8,36
D 20,48,65,0
Mvaa 168,8,32
Mvaa 172,12,32
Mvaa 204,12,36
Mvaa 236,12,40
D 21,48,65,1024
Ta 28,25,32768
Maaa 172,12,44
Ta 30,25,33792
Maaa 204,12,48
Ra 156,27,47104
Maaa 236,12,52
Maaa 76,12,56
D 22,48,65,2048
Maaa 108,12,60
W 7
Ra 152,27,45056
Maaa 112,16,60
Maaa 80,16,56
Maaa 240,16,52
D 23,48,65,3072
Ra 148,27,43008
Maaa 208,16,48
Ta 0,10,40960
Ta 2,10,41984
Maaa 176,16,44
Mvaa 240,16,40
Ra 144,27,40960
Mvaa 208,16,36
buffer_load_dword v142, v144, s[8:11], s91 offen offset:0 sc1
Mvaa 176,16,32
W 10
Mvaa 180,20,32
Ra 140,27,38912
Mvaa 212,20,36
Mvaa 244,20,40
Ta 4,11,40960
Ta 6,11,41984
Maaa 180,20,44
Ra 136,27,36864
Maaa 212,20,48
Maaa 244,20,52
v_bfe_u32 v29, v1, 7, 1
Maaa 84,20,56
Ra 132,27,34816
)"
R"(Maaa 116,20,60
W 13
v_mul_u32_u24 v28, 10240, v29
Maaa 120,24,60
Maaa 88,24,56
Ra 128,27,32768
v_lshrrev_b32 v29, 8, v1
Maaa 248,24,52
Ta 8,12,40960
Ta 10,12,41984
Maaa 216,24,48
v_lshl_add_u32 v28, v29, 7, v28
Maaa 184,24,44
v_and_b32 v29, 0x70, v1
Mvaa 248,24,40
Mvaa 216,24,36
v_add_u32 v28, v28, v29
Mvaa 184,24,32
W 14
Mvaa 188,28,32
s_and_b32 s80, s14, 2
Mvaa 220,28,36
s_mul_i32 s80, s80, 20480
Mvaa 252,28,40
Ta 12,13,40960
Ta 14,13,41984
Maaa 188,28,44
s_sub_u32 s81, 40960, s80
Maaa 220,28,48
Maaa 252,28,52
s_add_u32 s93, s28, 20480
Maaa 92,28,56
Maaa 124,28,60
W 10
v_add_u32 v26, s80, v28
Maaa 96,0,156
Maaa 64,0,152
Maaa 224,0,148
Ta 16,14,40960
v_add_u32 v27, s81, v28
Maaa 192,0,144
W 5
Ta 18,14,41984
Maaa 160,0,140
buffer_load_dwordx4 a[32:35], v26, s[8:11], s28 offen offset:0 sc1
Mvaa 224,0,136
Mvaa 192,0,132
Mvaa 160,0,128
Mvaa 164,4,128
buffer_load_dwordx4 a[36:39], v26, s[8:11], s93 offen offset:0 sc1
Mvaa 196,4,132
Mvaa 228,4,136
Ta 20,15,40960
Ta 22,15,41984
Maaa 164,4,140
Maaa 196,4,144
buffer_load_dwordx4 a[40:43], v27, s[8:11], s28 offen offset:0 sc1
Maaa 228,4,148
Maaa 68,4,152
Maaa 100,4,156
W 6
Maaa 104,8,156
buffer_load_dwordx4 a[44:47], v27, s[8:11], s93 offen offset:0 sc1
Ta 24,24,40960
Maaa 72,8,152
s_add_u32 s28, s28, 512
s_cmp_eq_u32 s28, s4
s_cselect_b32 s93, 10240, 0
Ta 26,24,41984
Maaa 232,8,148
s_sub_u32 s28, s28, s93
Ta 28,25,40960
Ta 30,25,41984
Maaa 200,8,144
s_add_u32 s93, s28, 20480
Maaa 168,8,140
Mvaa 232,8,136
Mvaa 200,8,132
s_add_u32 s65, s5, 12
s_cmp_ge_u32 s65, 20
s_cselect_b32 s90, 20, 0
buffer_load_dwordx4 a[48:51], v26, s[8:11], s28 offen offset:0 sc1
Mvaa 168,8,128
W 8
s_sub_u32 s65, s65, s90
s_lshl_b32 s65, s65, 18
Mvaa 172,12,128
s_add_u32 m0, s63, 32768
Mvaa 204,12,132
Mvaa 236,12,136
s_waitcnt vmcnt(6) lgkmcnt(0)
s_barrier
Maaa 172,12,140
buffer_load_dwordx4 a[52:55], v26, s[8:11], s93 offen offset:0 sc1
Maaa 204,12,144
Maaa 236,12,148
Maaa 76,12,152
Maaa 108,12,156
buffer_load_dwordx4 a[56:59], v27, s[8:11], s28 offen offset:0 sc1
Maaa 112,16,156
Maaa 80,16,152
Maaa 240,16,148
Maaa 208,16,144
buffer_load_dwordx4 a[60:63], v27, s[8:11], s93 offen offset:0 sc1
Maaa 176,16,140
Mvaa 240,16,136
Mvaa 208,16,132
Mvaa 176,16,128
D 16,48,65,0
Mvaa 180,20,128
Mvaa 212,20,132
Mvaa 244,20,136
Ta 0,2,0
Ta 2,2,1024
Maaa 180,20,140
D 17,48,65,1024
Maaa 212,20,144
Maaa 244,20,148
Maaa 84,20,152
Ta 4,2,8192
Ta 6,2,9216
Maaa 116,20,156
D 18,48,65,2048
Maaa 120,24,156
Maaa 88,24,152
Maaa 248,24,148
Ta 8,3,0
Ta 10,3,1024
Maaa 216,24,144
D 19,48,65,3072
Maaa 184,24,140
s_add_u32 m0, s63, 36864
Mvaa 248,24,136
Mvaa 216,24,132
Ta 12,3,8192
Ta 14,3,9216
Mvaa 184,24,128
D 20,48,65,0
Mvaa 188,28,128
Mvaa 220,28,132
Mvaa 252,28,136
Ta 16,4,0
Ta 18,4,1024
Maaa 188,28,140
Maaa 220,28,144
D 21,48,65,1024
Maaa 252,28,148
Maaa 92,28,152
Maaa 124,28,156
s_branch .Lq_11%=
.p2align 12
.Lv_10%=:
s_waitcnt vmcnt(14)
s_barrier
Ta 0,10,16384
Ta 2,10,17408
Ra 32,26,0
Ta 4,10,24576
.Lh_0_0_1%=:
s_cbranch_execz .Lhr_9%=
Ta 6,10,25600
Ra 128,27,0
Ra 132,27,2048
Ra 36,26,2048
Ra 40,26,4096
Ra 136,27,4096
Ra 140,27,6144
Ra 44,26,6144
Ra 48,26,8192
Ra 144,27,8192
W 6
Zvaa 160,32,0
s_add_u32 m0, s61, 32768
Ra 148,27,10240
Ra 52,26,10240
Ra 56,26,12288
Ra 152,27,12288
Ra 156,27,14336
Ra 60,26,14336
Mvaa 160,128,4
Ta 8,11,16384
Ta 10,11,17408
Ta 12,11,24576
Ta 14,11,25600
Zvaa 164,132,4
Ta 16,12,16384
Ta 18,12,17408
Mvaa 164,36,0
W 14
Ta 20,12,24576
Ta 22,12,25600
Zvaa 224,40,0
D 2,40,64,0
Mvaa 224,136,4
Zvaa 228,140,4
Mvaa 228,44,0
W 12
Zaaa 192,48,0
D 3,40,64,1024
Maaa 192,144,4
Zaaa 196,148,4
Maaa 196,52,0
W 8
Zaaa 64,56,0
D 4,40,64,2048
Maaa 64,152,4
Zaaa 68,156,4
Maaa 68,60,0
W 4
Zaaa 76,60,8
D 5,40,64,3072
Maaa 76,156,12
s_add_u32 m0, s61, 36864
Zaaa 72,152,12
Ta 24,13,16384
Ta 26,13,17408
Maaa 72,56,8
Ta 28,13,24576
Ta 30,13,25600
Zaaa 204,52,8
s_waitcnt vmcnt(15)
D 6,40,64,0
Maaa 204,148,12
v_lshlrev_b32 v56, 16, v32
v_and_b32 v57, 0xffff0000, v32
v_lshlrev_b32 v58, 16, v33
v_and_b32 v59, 0xffff0000, v33
v_lshlrev_b32 v60, 16, v34
Zaaa 200,144,12
v_and_b32 v61, 0xffff0000, v34
v_lshlrev_b32 v62, 16, v35
v_and_b32 v63, 0xffff0000, v35
v_lshlrev_b32 v64, 16, v36
v_and_b32 v65, 0xffff0000, v36
Maaa 200,48,8
v_lshlrev_b32 v66, 16, v37
v_and_b32 v67, 0xffff0000, v37
v_lshlrev_b32 v68, 16, v38
v_and_b32 v69, 0xffff0000, v38
v_lshlrev_b32 v70, 16, v39
Zvaa 236,44,8
D 7,40,64,1024
v_and_b32 v71, 0xffff0000, v39
v_lshlrev_b32 v72, 16, v40
v_and_b32 v73, 0xffff0000, v40
v_lshlrev_b32 v74, 16, v41
v_and_b32 v75, 0xffff0000, v41
Mvaa 236,140,12
v_lshlrev_b32 v76, 16, v42
v_and_b32 v77, 0xffff0000, v42
v_lshlrev_b32 v78, 16, v43
v_and_b32 v79, 0xffff0000, v43
A 80,56,57
Zvaa 232,136,12
A 81,58,59
A 80,80,60
A 81,81,61
A 80,80,62
A 81,81,63
Mvaa 232,40,8
A 80,80,64
A 81,81,65
A 80,80,66
A 81,81,67
A 80,80,68
Zvaa 172,36,8
D 8,40,64,2048
A 81,81,69
A 80,80,70
A 81,81,71
A 80,80,72
A 81,81,73
Mvaa 172,132,12
A 80,80,74
A 81,81,75
A 80,80,76
A 81,81,77
A 80,80,78
A 81,81,79
Zvaa 168,128,12
A 80,80,81
s_nop 1
v_add_f32_dpp v80, v80, v80 quad_perm:[1,0,3,2] row_mask:0xf bank_mask:0xf
s_nop 1
v_add_f32_dpp v80, v80, v80 quad_perm:[2,3,0,1] row_mask:0xf bank_mask:0xf
s_nop 1
v_add_f32_dpp v80, v80, v80 row_half_mirror row_mask:0xf bank_mask:0xf
s_nop 1
v_add_f32_dpp v80, v80, v80 row_mirror row_mask:0xf bank_mask:0xf
Mvaa 168,32,8
W 4
v_mov_b32 v81, v80
s_nop 1
v_permlane16_swap_b32 v80, v81
A 80,80,81
v_mul_f32 v84, 0x3acccccd, v80
s_mov_b32 s86, 0xffff
Zvaa 176,32,16
s_mov_b32 s87, 0xffff
D 9,40,64,3072
v_sub_f32 v82, v56, v84
U 80,82,82
v_sub_f32 v83, v57, v84
v_fmac_f32 v80, v83, v83
Mvaa 176,128,20
v_sub_f32 v82, v58, v84
v_fmac_f32 v80, v82, v82
v_sub_f32 v83, v59, v84
v_fmac_f32 v80, v83, v83
v_sub_f32 v82, v60, v84
Zvaa 180,132,20
Ta 0,14,16384
v_fmac_f32 v80, v82, v82
v_sub_f32 v83, v61, v84
v_fmac_f32 v80, v83, v83
v_sub_f32 v82, v62, v84
v_fmac_f32 v80, v82, v82
Mvaa 180,36,16
Ta 2,14,17408
v_sub_f32 v83, v63, v84
v_fmac_f32 v80, v83, v83
v_sub_f32 v82, v64, v84
v_fmac_f32 v80, v82, v82
v_sub_f32 v83, v65, v84
Zvaa 240,40,16
global_load_dword v142, v[140:141], off
v_lshl_add_u64 v[140:141], v[140:141], 0, s[68:69]
Ta 4,14,24576
Ta 6,14,25600
v_fmac_f32 v80, v83, v83
v_sub_f32 v82, v66, v84
v_fmac_f32 v80, v82, v82
Mvaa 240,136,20
v_sub_f32 v83, v67, v84
v_fmac_f32 v80, v83, v83
v_sub_f32 v82, v68, v84
v_fmac_f32 v80, v82, v82
v_sub_f32 v83, v69, v84
v_fmac_f32 v80, v83, v83
Zvaa 244,140,20
v_sub_f32 v82, v70, v84
v_fmac_f32 v80, v82, v82
v_sub_f32 v83, v71, v84
v_fmac_f32 v80, v83, v83
v_sub_f32 v82, v72, v84
U 81,82,82
Mvaa 244,44,16
v_sub_f32 v83, v73, v84
v_fmac_f32 v81, v83, v83
v_sub_f32 v82, v74, v84
v_fmac_f32 v81, v82, v82
v_sub_f32 v83, v75, v84
v_fmac_f32 v81, v83, v83
Zaaa 208,48,16
v_sub_f32 v82, v76, v84
v_fmac_f32 v81, v82, v82
v_sub_f32 v83, v77, v84
v_fmac_f32 v81, v83, v83
v_sub_f32 v82, v78, v84
Maaa 208,144,20
v_fmac_f32 v81, v82, v82
v_sub_f32 v83, v79, v84
v_fmac_f32 v81, v83, v83
v_cndmask_b32_e64 v81, 0, v81, s[86:87]
A 80,80,81
Zaaa 212,148,20
Maaa 212,52,16
v_add_f32_dpp v80, v80, v80 quad_perm:[1,0,3,2] row_mask:0xf bank_mask:0xf
s_nop 1
v_add_f32_dpp v80, v80, v80 quad_perm:[2,3,0,1] row_mask:0xf bank_mask:0xf
s_nop 1
v_add_f32_dpp v80, v80, v80 row_half_mirror row_mask:0xf bank_mask:0xf
s_nop 1
v_add_f32_dpp v80, v80, v80 row_mirror row_mask:0xf bank_mask:0xf
v_mov_b32 v81, v80
s_nop 1
v_permlane16_swap_b32 v80, v81
Zaaa 80,56,16
A 80,80,81
v_mul_f32 v80, 0x3acccccd, v80
K 80,80,80
v_lshlrev_b32 v80, 16, v80
v_add_f32 v80, 0x3727c5ac, v80
K 80,80,80
Maaa 80,152,20
v_lshlrev_b32 v80, 16, v80
v_sqrt_f32 v81, v80
s_nop 0
v_rcp_f32 v82, v81
v_fma_f32 v83, -v81, v81, v80
Zaaa 84,156,20
v_mul_f32 v82, 0.5, v82
v_fma_f32 v81, v83, v82, v81
K 86,81,81
v_lshlrev_b32 v86, 16, v86
v_rcp_f32 v87, v86
Maaa 84,60,16
W 4
K 85,84,84
v_lshlrev_b32 v85, 16, v85
)"
R"(s_waitcnt vmcnt(17)
v_sub_f32 v56, v56, v85
v_sub_f32 v57, v57, v85
K 80,56,57
Zaaa 92,60,24
v_lshlrev_b32 v56, 16, v80
v_and_b32 v57, 0xffff0000, v80
U 80,56,87
U 81,57,87
K 32,80,81
v_lshlrev_b32 v80, 16, v32
Maaa 92,156,28
v_and_b32 v81, 0xffff0000, v32
v_lshlrev_b32 v82, 16, v44
v_and_b32 v83, 0xffff0000, v44
U 84,80,82
v_fmac_f32 v84, v81, v83
Zaaa 88,152,28
Ta 8,15,16384
v_sub_f32 v58, v58, v85
v_sub_f32 v59, v59, v85
K 80,58,59
v_lshlrev_b32 v58, 16, v80
v_and_b32 v59, 0xffff0000, v80
Maaa 88,56,24
Ta 10,15,17408
U 80,58,87
U 81,59,87
K 33,80,81
v_lshlrev_b32 v80, 16, v33
v_and_b32 v81, 0xffff0000, v33
Zaaa 220,52,24
Ta 12,15,24576
Ta 14,15,25600
v_lshlrev_b32 v82, 16, v45
v_and_b32 v83, 0xffff0000, v45
v_fmac_f32 v84, v80, v82
v_fmac_f32 v84, v81, v83
Maaa 220,148,28
v_sub_f32 v60, v60, v85
v_sub_f32 v61, v61, v85
K 80,60,61
v_lshlrev_b32 v60, 16, v80
v_and_b32 v61, 0xffff0000, v80
U 80,60,87
Zaaa 216,144,28
U 81,61,87
K 34,80,81
v_lshlrev_b32 v80, 16, v34
v_and_b32 v81, 0xffff0000, v34
v_lshlrev_b32 v82, 16, v46
v_and_b32 v83, 0xffff0000, v46
Maaa 216,48,24
v_fmac_f32 v84, v80, v82
v_fmac_f32 v84, v81, v83
v_sub_f32 v62, v62, v85
v_sub_f32 v63, v63, v85
K 80,62,63
Zvaa 252,44,24
v_lshlrev_b32 v62, 16, v80
v_and_b32 v63, 0xffff0000, v80
U 80,62,87
U 81,63,87
K 35,80,81
Mvaa 252,140,28
v_lshlrev_b32 v80, 16, v35
v_and_b32 v81, 0xffff0000, v35
v_lshlrev_b32 v82, 16, v47
v_and_b32 v83, 0xffff0000, v47
v_fmac_f32 v84, v80, v82
Zvaa 248,136,28
v_fmac_f32 v84, v81, v83
v_sub_f32 v64, v64, v85
v_sub_f32 v65, v65, v85
K 80,64,65
v_lshlrev_b32 v64, 16, v80
Mvaa 248,40,24
v_and_b32 v65, 0xffff0000, v80
U 80,64,87
U 81,65,87
K 36,80,81
v_lshlrev_b32 v80, 16, v36
Zvaa 188,36,24
v_and_b32 v81, 0xffff0000, v36
v_lshlrev_b32 v82, 16, v48
v_and_b32 v83, 0xffff0000, v48
v_fmac_f32 v84, v80, v82
v_fmac_f32 v84, v81, v83
Mvaa 188,132,28
v_sub_f32 v66, v66, v85
v_sub_f32 v67, v67, v85
K 80,66,67
v_lshlrev_b32 v66, 16, v80
v_and_b32 v67, 0xffff0000, v80
U 80,66,87
Zvaa 184,128,28
U 81,67,87
K 37,80,81
v_lshlrev_b32 v80, 16, v37
v_and_b32 v81, 0xffff0000, v37
v_lshlrev_b32 v82, 16, v49
Mvaa 184,32,24
W 4
v_and_b32 v83, 0xffff0000, v49
v_fmac_f32 v84, v80, v82
v_fmac_f32 v84, v81, v83
v_sub_f32 v68, v68, v85
v_sub_f32 v69, v69, v85
Zvaa 192,32,0
K 80,68,69
v_lshlrev_b32 v68, 16, v80
v_and_b32 v69, 0xffff0000, v80
U 80,68,87
U 81,69,87
Mvaa 192,128,4
K 38,80,81
v_lshlrev_b32 v80, 16, v38
v_and_b32 v81, 0xffff0000, v38
v_lshlrev_b32 v82, 16, v50
v_and_b32 v83, 0xffff0000, v50
Zvaa 196,132,4
Ta 16,24,16384
v_fmac_f32 v84, v80, v82
v_fmac_f32 v84, v81, v83
v_sub_f32 v70, v70, v85
v_sub_f32 v71, v71, v85
K 80,70,71
Mvaa 196,36,0
Ta 18,24,17408
v_lshlrev_b32 v70, 16, v80
v_and_b32 v71, 0xffff0000, v80
U 80,70,87
U 81,71,87
K 39,80,81
Zaaa 160,40,0
Ta 20,24,24576
Ta 22,24,25600
v_lshlrev_b32 v80, 16, v39
v_and_b32 v81, 0xffff0000, v39
v_lshlrev_b32 v82, 16, v51
v_and_b32 v83, 0xffff0000, v51
Maaa 160,136,4
v_fmac_f32 v84, v80, v82
v_fmac_f32 v84, v81, v83
v_sub_f32 v72, v72, v85
v_sub_f32 v73, v73, v85
K 80,72,73
v_lshlrev_b32 v72, 16, v80
Zaaa 164,140,4
v_and_b32 v73, 0xffff0000, v80
U 80,72,87
U 81,73,87
K 40,80,81
v_lshlrev_b32 v80, 16, v40
v_and_b32 v81, 0xffff0000, v40
Maaa 164,44,0
v_lshlrev_b32 v82, 16, v52
v_and_b32 v83, 0xffff0000, v52
v_fmac_f32 v84, v80, v82
v_fmac_f32 v84, v81, v83
v_sub_f32 v74, v74, v85
Zaaa 224,48,0
v_sub_f32 v75, v75, v85
K 80,74,75
v_lshlrev_b32 v74, 16, v80
v_and_b32 v75, 0xffff0000, v80
U 80,74,87
Maaa 224,144,4
U 81,75,87
K 41,80,81
v_lshlrev_b32 v80, 16, v41
v_and_b32 v81, 0xffff0000, v41
v_lshlrev_b32 v82, 16, v53
Zaaa 228,148,4
v_and_b32 v83, 0xffff0000, v53
v_fmac_f32 v84, v80, v82
v_fmac_f32 v84, v81, v83
v_sub_f32 v76, v76, v85
v_sub_f32 v77, v77, v85
Maaa 228,52,0
K 80,76,77
v_lshlrev_b32 v76, 16, v80
v_and_b32 v77, 0xffff0000, v80
U 80,76,87
U 81,77,87
Zaaa 96,56,0
K 42,80,81
v_lshlrev_b32 v80, 16, v42
v_and_b32 v81, 0xffff0000, v42
v_lshlrev_b32 v82, 16, v54
v_and_b32 v83, 0xffff0000, v54
Maaa 96,152,4
v_fmac_f32 v84, v80, v82
v_fmac_f32 v84, v81, v83
v_sub_f32 v78, v78, v85
v_sub_f32 v79, v79, v85
K 80,78,79
v_lshlrev_b32 v78, 16, v80
Zaaa 100,156,4
v_and_b32 v79, 0xffff0000, v80
U 80,78,87
U 81,79,87
K 43,80,81
v_lshlrev_b32 v80, 16, v43
Maaa 100,60,0
W 4
v_and_b32 v81, 0xffff0000, v43
v_lshlrev_b32 v82, 16, v55
v_and_b32 v83, 0xffff0000, v55
v_fmac_f32 v84, v80, v82
v_fmac_f32 v84, v81, v83
Zaaa 108,60,8
Ta 24,25,16384
Ta 26,25,17408
v_lshrrev_b32 v82, 9, v1
s_lshl_b32 s80, s14, 1
v_add_u32 v82, s80, v82
v_mul_u32_u24 v83, 1552, v82
v_add_u32 v83, v83, v88
Maaa 108,156,12
v_add_u32 v83, 131072, v83
ds_write_b128 v83, v[32:35] offset:0
ds_write_b128 v83, v[36:39] offset:512
ds_write_b128 v83, v[40:43] offset:1024
v_add_f32_dpp v84, v84, v84 quad_perm:[1,0,3,2] row_mask:0xf bank_mask:0xf
Zaaa 104,152,12
Ta 28,25,24576
Ta 30,25,25600
Maaa 104,56,8
v_add_f32_dpp v84, v84, v84 quad_perm:[2,3,0,1] row_mask:0xf bank_mask:0xf
s_nop 1
v_add_f32_dpp v84, v84, v84 row_half_mirror row_mask:0xf bank_mask:0xf
s_nop 1
v_add_f32_dpp v84, v84, v84 row_mirror row_mask:0xf bank_mask:0xf
v_mov_b32 v81, v84
s_nop 1
v_permlane16_swap_b32 v84, v81
A 84,84,81
Zaaa 236,52,8
K 80,84,84
v_lshlrev_b32 v80, 16, v80
s_lshl_b32 s80, s15, 3
v_add_u32 v82, s80, v82
v_sub_u32 v83, 2047, v82
v_ashrrev_i32 v81, 31, v80
v_or_b32 v81, 0x80000000, v81
Maaa 236,148,12
v_xor_b32 v80, v80, v81
v_and_b32 v80, 0xffff0000, v80
v_or_b32 v80, v80, v83
v_and_b32 v82, 7, v82
v_lshlrev_b32 v82, 2, v82
v_add_u32 v82, 143488, v82
Zaaa 232,144,12
ds_write_b32 v82, v80
Maaa 232,48,8
Zaaa 172,44,8
Maaa 172,140,12
Zaaa 168,136,12
s_waitcnt vmcnt(1) lgkmcnt(0)
s_barrier
Maaa 168,40,8
s_add_u32 s64, s64, 128
v_bfe_u32 v80, v1, 4, 3
Zvaa 204,36,8
v_mul_u32_u24 v81, 1552, v80
Mvaa 204,132,12
s_and_b32 s64, s64, 1023
v_lshrrev_b32 v82, 7, v1
Zvaa 200,128,12
v_lshl_add_u32 v81, v82, 4, v81
Mvaa 200,32,8
s_lshl_b32 s80, s14, 7
Zvaa 208,32,16
s_lshl_b32 s65, s64, 12
s_add_u32 s80, s80, 131072
Mvaa 208,128,20
v_add_u32 v81, s80, v81
Zvaa 212,132,20
s_add_u32 m0, s62, 0
ds_read_b128 v[32:35], v81
Ta 0,10,49152
Mvaa 212,36,16
Ta 2,10,50176
Rv 36,81,512
Zaaa 176,40,16
s_and_b32 s80, s14, 2
Ta 4,10,57344
Ta 6,10,58368
Maaa 176,136,20
s_lshl_b32 s80, s80, 7
D 16,44,65,0
Zaaa 180,140,20
v_subrev_u32 v81, s80, v81
Maaa 180,44,16
Rv 40,81,1024
Zaaa 240,48,16
v_lshlrev_b32 v80, 2, v80
Maaa 240,144,20
D 17,44,65,1024
v_add_u32 v80, 143488, v80
Zaaa 244,148,20
ds_read_b32 v84, v80
Maaa 244,52,16
v_mov_b32 v85, s13
Zaaa 112,56,16
v_lshrrev_b32 v83, 1, v1
Maaa 112,152,20
s_mul_i32 s82, s15, 10240
D 18,44,65,2048
Zaaa 116,156,20
s_add_u32 s82, s82, 131072
Maaa 116,60,16
s_lshl_b32 s80, s14, 10
Zaaa 124,60,24
s_add_u32 s83, s82, s80
Ra 60,26,47104
Maaa 124,156,28
D 19,44,65,3072
Ra 156,27,47104
Zaaa 120,152,28
s_add_u32 m0, s62, 4096
Ra 152,27,45056
Ta 8,11,49152
Maaa 120,56,24
Ta 10,11,50176
Ra 56,26,45056
Zaaa 252,52,24
Ta 12,11,57344
Ta 14,11,58368
Maaa 252,148,28
W 8
buffer_store_dwordx4 v[32:35], v1, s[8:11], s83 offen sc1
s_add_u32 s83, s83, 4096
Zaaa 248,144,28
Ra 52,26,43008
Ra 148,27,43008
Maaa 248,48,24
Ra 144,27,40960
Ra 48,26,40960
Zaaa 188,44,24
Ra 44,26,38912
Maaa 188,140,28
D 20,44,65,0
Ra 140,27,38912
Zaaa 184,136,28
Ra 136,27,36864
Maaa 184,40,24
Ra 40,26,36864
Zvaa 220,36,24
Ra 36,26,34816
Mvaa 220,132,28
buffer_store_dwordx4 v[36:39], v1, s[8:11], s83 offen sc1
Ra 132,27,34816
Zvaa 216,128,28
s_and_b32 s80, s80, 1024
s_add_u32 s83, s82, s80
s_add_u32 s83, s83, 8192
Mvaa 216,32,24
W 12
Ra 128,27,32768
Ra 32,26,32768
Maaa 68,60,0
Maaa 68,156,4
D 21,44,65,1024
Maaa 64,152,4
Ta 16,12,49152
Ta 18,12,50176
Maaa 64,56,0
W 10
Ta 20,12,57344
Ta 22,12,58368
Maaa 196,52,0
Maaa 196,148,4
D 22,44,65,2048
Maaa 192,144,4
Maaa 192,48,0
W 8
Mvaa 228,44,0
Mvaa 228,140,4
buffer_store_dwordx4 v[40:43], v1, s[8:11], s83 offen sc1
Mvaa 224,136,4
Mvaa 224,40,0
W 4
Mvaa 164,36,0
Mvaa 164,132,4
D 23,44,65,3072
Mvaa 160,128,4
s_add_u32 m0, s61, 0
Mvaa 160,32,0
Mvaa 168,32,8
Mvaa 168,128,12
D 2,40,64,0
Mvaa 172,132,12
Ta 24,13,49152
Ta 26,13,50176
Mvaa 172,36,8
Ta 28,13,57344
Ta 30,13,58368
Mvaa 232,40,8
)"
R"(.Lh_0_1_1%=:
s_cbranch_execz .Lhr_9%=
Mvaa 232,136,12
D 3,40,64,1024
Mvaa 236,140,12
Mvaa 236,44,8
Maaa 200,48,8
Maaa 200,144,12
D 4,40,64,2048
Maaa 204,148,12
Maaa 204,52,8
Maaa 72,56,8
Maaa 72,152,12
D 5,40,64,3072
Maaa 76,156,12
s_add_u32 m0, s61, 4096
Maaa 76,60,8
W 4
Maaa 84,60,16
Maaa 84,156,20
D 6,40,64,0
Maaa 80,152,20
Ta 0,14,49152
Ta 2,14,50176
Maaa 80,56,16
Ta 4,14,57344
Ta 6,14,58368
Maaa 212,52,16
Maaa 212,148,20
D 7,40,64,1024
Maaa 208,144,20
Maaa 208,48,16
Mvaa 244,44,16
Mvaa 244,140,20
D 8,40,64,2048
Mvaa 240,136,20
Mvaa 240,40,16
Mvaa 180,36,16
Mvaa 180,132,20
D 9,40,64,3072
Mvaa 176,128,20
Mvaa 176,32,16
W 4
Mvaa 184,32,24
Mvaa 184,128,28
global_load_dword v142, v[140:141], off
v_lshl_add_u64 v[140:141], v[140:141], 0, s[68:69]
v_bfe_u32 v147, v1, 5, 3
Mvaa 188,132,28
v_lshlrev_b32 v147, 13, v147
v_and_b32 v148, 16, v1
Mvaa 188,36,24
Ta 8,15,49152
v_lshl_add_u32 v147, v148, 3, v147
Mvaa 248,40,24
s_lshl_b32 s92, s57, 2
s_add_u32 s92, s92, s14
s_lshl_b32 s92, s92, 16
Mvaa 248,136,28
buffer_load_dword v142, v147, s[48:51], s92 offen
Ta 10,15,50176
Mvaa 252,140,28
Ta 12,15,57344
Ta 14,15,58368
Mvaa 252,44,24
Maaa 216,48,24
Maaa 216,144,28
Maaa 220,148,28
Maaa 220,52,24
Maaa 88,56,24
Maaa 88,152,28
Maaa 92,156,28
Maaa 92,60,24
W 4
Maaa 100,60,0
Maaa 100,156,4
Maaa 96,152,4
Ta 16,24,49152
Ta 18,24,50176
Maaa 96,56,0
Ta 20,24,57344
Ta 22,24,58368
Maaa 228,52,0
Maaa 228,148,4
Maaa 224,144,4
Maaa 224,48,0
Maaa 164,44,0
Maaa 164,140,4
Maaa 160,136,4
Maaa 160,40,0
Mvaa 196,36,0
Mvaa 196,132,4
Mvaa 192,128,4
Mvaa 192,32,0
W 4
Mvaa 200,32,8
Ta 24,25,49152
Ta 26,25,50176
Mvaa 200,128,12
Mvaa 204,132,12
Ta 28,25,57344
Ta 30,25,58368
Mvaa 204,36,8
Maaa 168,40,8
Maaa 168,136,12
Maaa 172,140,12
Maaa 172,44,8
Maaa 232,48,8
Maaa 232,144,12
Maaa 236,148,12
s_waitcnt vmcnt(2) lgkmcnt(0)
s_barrier
s_cmp_eq_u32 s14, 0
s_cbranch_scc0 .Lpubr_19%=
s_lshl_b32 s80, s15, 6
s_add_u32 s80, s80, 40960
s_mov_b64 exec, 0xff
buffer_store_dwordx2 v[84:85], v83, s[8:11], s80 offen sc1
s_mov_b64 exec, -1
.Lpubr_19%=:
Maaa 236,52,8
s_add_u32 s64, s64, 128
Maaa 104,56,8
Maaa 104,152,12
s_and_b32 s64, s64, 1023
Maaa 108,156,12
Maaa 108,60,8
Maaa 116,60,16
s_lshl_b32 s65, s64, 12
Maaa 116,156,20
Maaa 112,152,20
Ta 0,10,16384
Ta 2,10,17408
Maaa 112,56,16
s_add_u32 m0, s62, 32768
Ta 4,10,24576
Ta 6,10,25600
Maaa 244,52,16
Maaa 244,148,20
Maaa 240,144,20
D 16,44,65,0
Maaa 240,48,16
Maaa 180,44,16
Maaa 180,140,20
Maaa 176,136,20
D 17,44,65,1024
Maaa 176,40,16
Mvaa 212,36,16
Mvaa 212,132,20
Mvaa 208,128,20
D 18,44,65,2048
Mvaa 208,32,16
Mvaa 216,32,24
Ra 32,26,0
Mvaa 216,128,28
Ra 128,27,0
Mvaa 220,132,28
D 19,44,65,3072
Ra 132,27,2048
Mvaa 220,36,24
s_add_u32 m0, s62, 36864
Ta 8,11,16384
Ta 10,11,17408
Maaa 184,40,24
Ra 36,26,2048
Ta 12,11,24576
Maaa 184,136,28
Ta 14,11,25600
Ra 40,26,4096
Maaa 188,140,28
D 20,44,65,0
Ra 136,27,4096
Maaa 188,44,24
Ra 140,27,6144
Ra 44,26,6144
Maaa 248,48,24
Ra 48,26,8192
Maaa 248,144,28
Ra 144,27,8192
Maaa 252,148,28
D 21,44,65,1024
Ra 148,27,10240
Maaa 252,52,24
Ra 52,26,10240
Maaa 120,56,24
Ra 56,26,12288
Maaa 120,152,28
Ra 152,27,12288
Maaa 124,156,28
D 22,44,65,2048
Ra 156,27,14336
Maaa 124,60,24
W 13
Ra 60,26,14336
Mvaa 160,32,0
Mvaa 160,128,4
Mvaa 164,132,4
D 23,44,65,3072
Ta 16,12,16384
Mvaa 164,36,0
W 9
s_add_u32 m0, s61, 32768
Ta 18,12,17408
Mvaa 224,40,0
Ta 20,12,24576
Ta 22,12,25600
Mvaa 224,136,4
Mvaa 228,140,4
D 2,40,64,0
Mvaa 228,44,0
W 8
Maaa 192,48,0
Maaa 192,144,4
Maaa 196,148,4
D 3,40,64,1024
Maaa 196,52,0
W 4
Maaa 64,56,0
Maaa 64,152,4
Maaa 68,156,4
D 4,40,64,2048
Maaa 68,60,0
Maaa 76,60,8
Maaa 76,156,12
Maaa 72,152,12
D 5,40,64,3072
Ta 24,13,16384
Maaa 72,56,8
s_add_u32 m0, s61, 36864
Ta 26,13,17408
Ta 28,13,24576
Maaa 204,52,8
Ta 30,13,25600
Maaa 204,148,12
Maaa 200,144,12
D 6,40,64,0
Maaa 200,48,8
Mvaa 236,44,8
Mvaa 236,140,12
Mvaa 232,136,12
D 7,40,64,1024
Mvaa 232,40,8
Mvaa 172,36,8
Mvaa 172,132,12
Mvaa 168,128,12
D 8,40,64,2048
Mvaa 168,32,8
W 4
Mvaa 176,32,16
Mvaa 176,128,20
Mvaa 180,132,20
D 9,40,64,3072
Ta 0,14,16384
Mvaa 180,36,16
Ta 2,14,17408
Ta 4,14,24576
Mvaa 240,40,16
Ta 6,14,25600
Mvaa 240,136,20
Mvaa 244,140,20
Mvaa 244,44,16
Maaa 208,48,16
Maaa 208,144,20
Maaa 212,148,20
Maaa 212,52,16
Maaa 80,56,16
Maaa 80,152,20
Maaa 84,156,20
Maaa 84,60,16
W 4
Maaa 92,60,24
Maaa 92,156,28
Maaa 88,152,28
Ta 8,15,16384
Ta 10,15,17408
Maaa 88,56,24
Ta 12,15,24576
Ta 14,15,25600
Maaa 220,52,24
Maaa 220,148,28
Maaa 216,144,28
Maaa 216,48,24
Mvaa 252,44,24
Mvaa 252,140,28
Mvaa 248,136,28
Mvaa 248,40,24
Mvaa 188,36,24
Mvaa 188,132,28
Mvaa 184,128,28
Mvaa 184,32,24
W 4
Mvaa 192,32,0
Mvaa 192,128,4
Mvaa 196,132,4
Ta 16,24,16384
Ta 18,24,17408
Mvaa 196,36,0
Ta 20,24,24576
Ta 22,24,25600
Maaa 160,40,0
Maaa 160,136,4
Maaa 164,140,4
Maaa 164,44,0
Maaa 224,48,0
Maaa 224,144,4
Maaa 228,148,4
Maaa 228,52,0
Maaa 96,56,0
Maaa 96,152,4
Maaa 100,156,4
Maaa 100,60,0
W 4
Maaa 108,60,8
Ta 24,25,16384
Ta 26,25,17408
Maaa 108,156,12
Maaa 104,152,12
Ta 28,25,24576
Ta 30,25,25600
Maaa 104,56,8
Maaa 236,52,8
Maaa 236,148,12
Maaa 232,144,12
Maaa 232,48,8
Maaa 172,44,8
Maaa 172,140,12
Maaa 168,136,12
s_waitcnt vmcnt(0) lgkmcnt(0)
s_barrier
Maaa 168,40,8
s_add_u32 s64, s64, 128
Mvaa 204,36,8
Mvaa 204,132,12
s_and_b32 s64, s64, 1023
Mvaa 200,128,12
Mvaa 200,32,8
Mvaa 208,32,16
s_lshl_b32 s65, s64, 12
Mvaa 208,128,20
Mvaa 212,132,20
s_add_u32 m0, s62, 0
Ta 0,10,49152
Ta 2,10,50176
Mvaa 212,36,16
Ta 4,10,57344
Ta 6,10,58368
Maaa 176,40,16
Maaa 176,136,20
D 16,44,65,0
Maaa 180,140,20
Maaa 180,44,16
Maaa 240,48,16
Maaa 240,144,20
D 17,44,65,1024
Maaa 244,148,20
Maaa 244,52,16
Maaa 112,56,16
Maaa 112,152,20
D 18,44,65,2048
Maaa 116,156,20
Maaa 116,60,16
Maaa 124,60,24
Ra 60,26,47104
Maaa 124,156,28
D 19,44,65,3072
Ra 156,27,47104
Maaa 120,152,28
s_add_u32 m0, s62, 4096
Ra 152,27,45056
Ta 8,11,49152
Maaa 120,56,24
Ta 10,11,50176
Ra 56,26,45056
Maaa 252,52,24
Ta 12,11,57344
Ta 14,11,58368
Maaa 252,148,28
D 20,44,65,0
Ra 52,26,43008
Maaa 248,144,28
Ra 148,27,43008
Ra 144,27,40960
Maaa 248,48,24
Ra 48,26,40960
Maaa 188,44,24
Ra 44,26,38912
Maaa 188,140,28
D 21,44,65,1024
Ra 140,27,38912
Maaa 184,136,28
Ra 136,27,36864
Maaa 184,40,24
Ra 40,26,36864
Mvaa 220,36,24
Ra 36,26,34816
Mvaa 220,132,28
D 22,44,65,2048
Ra 132,27,34816
Mvaa 216,128,28
Ra 128,27,32768
)"
R"(Mvaa 216,32,24
W 13
Ra 32,26,32768
Maaa 68,60,0
Maaa 68,156,4
D 23,44,65,3072
Maaa 64,152,4
s_add_u32 m0, s61, 0
Ta 16,12,49152
Ta 18,12,50176
Maaa 64,56,0
W 10
Ta 20,12,57344
Ta 22,12,58368
Maaa 196,52,0
Maaa 196,148,4
D 2,40,64,0
Maaa 192,144,4
Maaa 192,48,0
W 8
Mvaa 228,44,0
Mvaa 228,140,4
D 3,40,64,1024
Mvaa 224,136,4
Mvaa 224,40,0
W 4
Mvaa 164,36,0
Mvaa 164,132,4
D 4,40,64,2048
Mvaa 160,128,4
Mvaa 160,32,0
Mvaa 168,32,8
Mvaa 168,128,12
D 5,40,64,3072
Mvaa 172,132,12
s_add_u32 m0, s61, 4096
Ta 24,13,49152
Ta 26,13,50176
Mvaa 172,36,8
Ta 28,13,57344
Ta 30,13,58368
Mvaa 232,40,8
Mvaa 232,136,12
D 6,40,64,0
Mvaa 236,140,12
Mvaa 236,44,8
Maaa 200,48,8
Maaa 200,144,12
D 7,40,64,1024
Maaa 204,148,12
Maaa 204,52,8
Maaa 72,56,8
Maaa 72,152,12
D 8,40,64,2048
Maaa 76,156,12
Maaa 76,60,8
W 4
Maaa 84,60,16
Maaa 84,156,20
D 9,40,64,3072
Maaa 80,152,20
Ta 0,14,49152
Ta 2,14,50176
Maaa 80,56,16
Ta 4,14,57344
Ta 6,14,58368
Maaa 212,52,16
Maaa 212,148,20
global_load_dword v142, v[140:141], off
v_lshl_add_u64 v[140:141], v[140:141], 0, s[68:69]
Maaa 208,144,20
Maaa 208,48,16
Mvaa 244,44,16
Mvaa 244,140,20
Mvaa 240,136,20
Mvaa 240,40,16
Mvaa 180,36,16
Mvaa 180,132,20
Mvaa 176,128,20
Mvaa 176,32,16
W 4
Mvaa 184,32,24
Mvaa 184,128,28
Mvaa 188,132,28
Ta 8,15,49152
Ta 10,15,50176
Mvaa 188,36,24
Ta 12,15,57344
Ta 14,15,58368
Mvaa 248,40,24
Mvaa 248,136,28
Mvaa 252,140,28
Mvaa 252,44,24
Maaa 216,48,24
Maaa 216,144,28
Maaa 220,148,28
Maaa 220,52,24
Maaa 88,56,24
Maaa 88,152,28
Maaa 92,156,28
Maaa 92,60,24
W 4
Maaa 100,60,0
Maaa 100,156,4
Maaa 96,152,4
Ta 16,24,49152
Ta 18,24,50176
Maaa 96,56,0
Ta 20,24,57344
Ta 22,24,58368
Maaa 228,52,0
Maaa 228,148,4
Maaa 224,144,4
Maaa 224,48,0
Maaa 164,44,0
Maaa 164,140,4
Maaa 160,136,4
Maaa 160,40,0
Mvaa 196,36,0
Mvaa 196,132,4
Mvaa 192,128,4
Mvaa 192,32,0
W 4
Mvaa 200,32,8
Ta 24,25,49152
Ta 26,25,50176
Mvaa 200,128,12
Mvaa 204,132,12
Ta 28,25,57344
Ta 30,25,58368
Mvaa 204,36,8
Maaa 168,40,8
Maaa 168,136,12
.Lh_0_2_1%=:
s_cbranch_execz .Lhr_9%=
Maaa 172,140,12
Maaa 172,44,8
Maaa 232,48,8
Maaa 232,144,12
Maaa 236,148,12
s_waitcnt vmcnt(1) lgkmcnt(0)
s_barrier
Maaa 236,52,8
s_add_u32 s64, s64, 128
Maaa 104,56,8
Maaa 104,152,12
s_and_b32 s64, s64, 1023
Maaa 108,156,12
Maaa 108,60,8
Maaa 116,60,16
s_lshl_b32 s65, s64, 12
Maaa 116,156,20
Maaa 112,152,20
Ta 0,10,16384
Ta 2,10,17408
Maaa 112,56,16
s_add_u32 m0, s62, 32768
Ta 4,10,24576
Ta 6,10,25600
Maaa 244,52,16
Maaa 244,148,20
Maaa 240,144,20
D 16,44,65,0
Maaa 240,48,16
Maaa 180,44,16
Maaa 180,140,20
Maaa 176,136,20
D 17,44,65,1024
Maaa 176,40,16
Mvaa 212,36,16
Mvaa 212,132,20
Mvaa 208,128,20
D 18,44,65,2048
Mvaa 208,32,16
Mvaa 216,32,24
Ra 32,26,0
Mvaa 216,128,28
Ra 128,27,0
Mvaa 220,132,28
D 19,44,65,3072
Ra 132,27,2048
Mvaa 220,36,24
s_add_u32 m0, s62, 36864
Ta 8,11,16384
Ta 10,11,17408
Maaa 184,40,24
Ra 36,26,2048
Ta 12,11,24576
Maaa 184,136,28
Ta 14,11,25600
Ra 40,26,4096
Maaa 188,140,28
D 20,44,65,0
Ra 136,27,4096
Maaa 188,44,24
Ra 140,27,6144
Ra 44,26,6144
Maaa 248,48,24
Ra 48,26,8192
Maaa 248,144,28
Ra 144,27,8192
Maaa 252,148,28
D 21,44,65,1024
Ra 148,27,10240
Maaa 252,52,24
Ra 52,26,10240
Maaa 120,56,24
Ra 56,26,12288
Maaa 120,152,28
Ra 152,27,12288
Maaa 124,156,28
D 22,44,65,2048
Ra 156,27,14336
Maaa 124,60,24
W 13
Ra 60,26,14336
Mvaa 160,32,0
Mvaa 160,128,4
Mvaa 164,132,4
D 23,44,65,3072
Ta 16,12,16384
Mvaa 164,36,0
W 9
s_add_u32 m0, s61, 32768
Ta 18,12,17408
Mvaa 224,40,0
Ta 20,12,24576
Ta 22,12,25600
Mvaa 224,136,4
Mvaa 228,140,4
D 2,40,64,0
Mvaa 228,44,0
W 8
Maaa 192,48,0
Maaa 192,144,4
Maaa 196,148,4
D 3,40,64,1024
Maaa 196,52,0
W 4
Maaa 64,56,0
Maaa 64,152,4
Maaa 68,156,4
D 4,40,64,2048
Maaa 68,60,0
Maaa 76,60,8
Maaa 76,156,12
Maaa 72,152,12
D 5,40,64,3072
Ta 24,13,16384
Maaa 72,56,8
s_add_u32 m0, s61, 36864
Ta 26,13,17408
Ta 28,13,24576
Maaa 204,52,8
Ta 30,13,25600
Maaa 204,148,12
Maaa 200,144,12
D 6,40,64,0
Maaa 200,48,8
Mvaa 236,44,8
Mvaa 236,140,12
Mvaa 232,136,12
D 7,40,64,1024
Mvaa 232,40,8
Mvaa 172,36,8
Mvaa 172,132,12
Mvaa 168,128,12
D 8,40,64,2048
Mvaa 168,32,8
W 4
Mvaa 176,32,16
Mvaa 176,128,20
Mvaa 180,132,20
D 9,40,64,3072
Ta 0,14,16384
Mvaa 180,36,16
s_add_u32 s92, s92, 2097152
Ta 2,14,17408
Ta 4,14,24576
Mvaa 240,40,16
Ta 6,14,25600
Mvaa 240,136,20
Mvaa 244,140,20
buffer_load_dword v142, v147, s[48:51], s92 offen
Mvaa 244,44,16
s_add_u32 s92, s92, 2097152
Maaa 208,48,16
Maaa 208,144,20
Maaa 212,148,20
buffer_load_dword v142, v147, s[48:51], s92 offen
Maaa 212,52,16
Maaa 80,56,16
Maaa 80,152,20
Maaa 84,156,20
Maaa 84,60,16
W 4
Maaa 92,60,24
Maaa 92,156,28
Maaa 88,152,28
Ta 8,15,16384
Ta 10,15,17408
Maaa 88,56,24
Ta 12,15,24576
Ta 14,15,25600
Maaa 220,52,24
Maaa 220,148,28
Maaa 216,144,28
Maaa 216,48,24
Mvaa 252,44,24
Mvaa 252,140,28
Mvaa 248,136,28
Mvaa 248,40,24
Mvaa 188,36,24
Mvaa 188,132,28
Mvaa 184,128,28
Mvaa 184,32,24
W 4
Mvaa 192,32,0
Mvaa 192,128,4
Mvaa 196,132,4
Ta 16,24,16384
Ta 18,24,17408
Mvaa 196,36,0
Ta 20,24,24576
Ta 22,24,25600
Maaa 160,40,0
Maaa 160,136,4
Maaa 164,140,4
Maaa 164,44,0
Maaa 224,48,0
Maaa 224,144,4
Maaa 228,148,4
Maaa 228,52,0
Maaa 96,56,0
Maaa 96,152,4
Maaa 100,156,4
Maaa 100,60,0
W 4
Maaa 108,60,8
Ta 24,25,16384
Ta 26,25,17408
Maaa 108,156,12
Maaa 104,152,12
Ta 28,25,24576
Ta 30,25,25600
Maaa 104,56,8
Maaa 236,52,8
Maaa 236,148,12
Maaa 232,144,12
Maaa 232,48,8
Maaa 172,44,8
Maaa 172,140,12
Maaa 168,136,12
s_waitcnt vmcnt(2) lgkmcnt(0)
s_barrier
Maaa 168,40,8
s_add_u32 s64, s64, 128
Mvaa 204,36,8
Mvaa 204,132,12
s_and_b32 s64, s64, 1023
Mvaa 200,128,12
Mvaa 200,32,8
Mvaa 208,32,16
s_lshl_b32 s65, s64, 12
Mvaa 208,128,20
Mvaa 212,132,20
Ta 0,10,49152
Ta 2,10,50176
Mvaa 212,36,16
s_add_u32 m0, s62, 0
Ta 4,10,57344
Ta 6,10,58368
Maaa 176,40,16
Maaa 176,136,20
Maaa 180,140,20
D 16,44,65,0
Maaa 180,44,16
Maaa 240,48,16
Maaa 240,144,20
Maaa 244,148,20
D 17,44,65,1024
Maaa 244,52,16
Maaa 112,56,16
Maaa 112,152,20
Maaa 116,156,20
D 18,44,65,2048
Maaa 116,60,16
Maaa 124,60,24
Ra 60,26,47104
Maaa 124,156,28
Ra 156,27,47104
Maaa 120,152,28
D 19,44,65,3072
Ra 152,27,45056
Maaa 120,56,24
s_add_u32 m0, s62, 4096
Ta 8,11,49152
Ta 10,11,50176
Maaa 252,52,24
Ra 56,26,45056
)"
R"(Ta 12,11,57344
Maaa 252,148,28
Ta 14,11,58368
Ra 52,26,43008
Maaa 248,144,28
D 20,44,65,0
v_lshrrev_b32 v28, 1, v1
Maaa 248,48,24
s_lshl_b32 s80, s60, 9
Ra 148,27,43008
Ra 144,27,40960
Maaa 188,44,24
s_add_u32 s80, s80, 40960
Ra 48,26,40960
Ra 44,26,38912
Maaa 188,140,28
Ra 140,27,38912
Maaa 184,136,28
D 21,44,65,1024
Ra 136,27,36864
Maaa 184,40,24
Ra 40,26,36864
Mvaa 220,36,24
Ra 36,26,34816
Mvaa 220,132,28
Ra 132,27,34816
Mvaa 216,128,28
buffer_load_dwordx2 v[28:29], v28, s[8:11], s80 offen sc1
Ra 128,27,32768
Mvaa 216,32,24
W 13
Ra 32,26,32768
Maaa 68,60,0
Maaa 68,156,4
Maaa 64,152,4
D 22,44,65,2048
Ta 16,12,49152
Maaa 64,56,0
W 9
Ta 18,12,50176
Ta 20,12,57344
Maaa 196,52,0
Ta 22,12,58368
Maaa 196,148,4
Maaa 192,144,4
D 23,44,65,3072
Maaa 192,48,0
W 8
s_add_u32 m0, s61, 0
Mvaa 228,44,0
Mvaa 228,140,4
Mvaa 224,136,4
D 2,40,64,0
Mvaa 224,40,0
W 4
Mvaa 164,36,0
Mvaa 164,132,4
Mvaa 160,128,4
D 3,40,64,1024
Mvaa 160,32,0
Mvaa 168,32,8
Mvaa 168,128,12
Mvaa 172,132,12
D 4,40,64,2048
Ta 24,13,49152
Mvaa 172,36,8
Ta 26,13,50176
Ta 28,13,57344
Mvaa 232,40,8
Ta 30,13,58368
Mvaa 232,136,12
Mvaa 236,140,12
D 5,40,64,3072
Mvaa 236,44,8
s_add_u32 m0, s61, 4096
Maaa 200,48,8
Maaa 200,144,12
Maaa 204,148,12
D 6,40,64,0
Maaa 204,52,8
Maaa 72,56,8
Maaa 72,152,12
Maaa 76,156,12
D 7,40,64,1024
Maaa 76,60,8
W 4
Maaa 84,60,16
Maaa 84,156,20
Maaa 80,152,20
D 8,40,64,2048
Ta 0,14,49152
Maaa 80,56,16
Ta 2,14,50176
Ta 4,14,57344
Maaa 212,52,16
Ta 6,14,58368
Maaa 212,148,20
Maaa 208,144,20
D 9,40,64,3072
Maaa 208,48,16
Mvaa 244,44,16
Mvaa 244,140,20
Mvaa 240,136,20
s_waitcnt vmcnt(10)
v_cmp_eq_u32 vcc, s13, v29
s_cmp_eq_u64 vcc, -1
s_cbranch_scc0 .Lspin_20%=
.Lok_21%=:
global_load_dword v142, v146, s[6:7]
Mvaa 240,40,16
Mvaa 180,36,16
v_bfe_u32 v144, v1, 8, 1
Mvaa 180,132,20
v_mul_u32_u24 v144, 10240, v144
Mvaa 176,128,20
v_and_b32 v145, 0xf0, v1
Mvaa 176,32,16
W 4
v_lshl_add_u32 v144, v145, 3, v144
Mvaa 184,32,24
s_mul_i32 s91, s58, 20480
Mvaa 184,128,28
s_add_u32 s91, s91, s28
Mvaa 188,132,28
buffer_load_dword v142, v144, s[8:11], s91 offen offset:0 sc1
Ta 8,15,49152
Mvaa 188,36,24
Ta 10,15,50176
Ta 12,15,57344
Mvaa 248,40,24
Ta 14,15,58368
Mvaa 248,136,28
Mvaa 252,140,28
Mvaa 252,44,24
Maaa 216,48,24
Maaa 216,144,28
Maaa 220,148,28
Maaa 220,52,24
Maaa 88,56,24
Maaa 88,152,28
Maaa 92,156,28
Maaa 92,60,24
W 4
Maaa 100,60,0
Maaa 100,156,4
Maaa 96,152,4
Ta 16,24,49152
Ta 18,24,50176
Maaa 96,56,0
Ta 20,24,57344
Ta 22,24,58368
Maaa 228,52,0
Maaa 228,148,4
Maaa 224,144,4
Maaa 224,48,0
Maaa 164,44,0
Maaa 164,140,4
Maaa 160,136,4
Maaa 160,40,0
Mvaa 196,36,0
Mvaa 196,132,4
Mvaa 192,128,4
Mvaa 192,32,0
W 4
Mvaa 200,32,8
Ta 24,25,49152
Ta 26,25,50176
Mvaa 200,128,12
Mvaa 204,132,12
Ta 28,25,57344
Ta 30,25,58368
Mvaa 204,36,8
Maaa 168,40,8
Maaa 168,136,12
Maaa 172,140,12
Maaa 172,44,8
Maaa 232,48,8
Maaa 232,144,12
Maaa 236,148,12
s_waitcnt vmcnt(2) lgkmcnt(0)
s_barrier
s_cmp_eq_u32 s85, 107
s_cbranch_scc1 .Lrt_23%=
.Lrtr_24%=:
Maaa 236,52,8
s_add_u32 s64, s64, 128
Maaa 104,56,8
Maaa 104,152,12
s_and_b32 s64, s64, 1023
Maaa 108,156,12
Maaa 108,60,8
Maaa 116,60,16
s_lshl_b32 s65, s64, 12
Maaa 116,156,20
Maaa 112,152,20
Ta 0,10,16384
Ta 2,10,17408
Maaa 112,56,16
s_add_u32 m0, s62, 32768
Ta 4,10,24576
Ta 6,10,25600
Maaa 244,52,16
Maaa 244,148,20
Maaa 240,144,20
D 16,44,65,0
Maaa 240,48,16
Maaa 180,44,16
Maaa 180,140,20
Maaa 176,136,20
D 17,44,65,1024
Maaa 176,40,16
Mvaa 212,36,16
Mvaa 212,132,20
Mvaa 208,128,20
D 18,44,65,2048
Mvaa 208,32,16
Mvaa 216,32,24
Ra 32,26,0
Mvaa 216,128,28
Ra 128,27,0
Mvaa 220,132,28
D 19,44,65,3072
Ra 132,27,2048
Mvaa 220,36,24
s_add_u32 m0, s62, 36864
Ta 8,11,16384
Ta 10,11,17408
Maaa 184,40,24
Ra 36,26,2048
Ta 12,11,24576
Maaa 184,136,28
Ta 14,11,25600
Ra 40,26,4096
Maaa 188,140,28
D 20,44,65,0
Ra 136,27,4096
Maaa 188,44,24
Ra 140,27,6144
Ra 44,26,6144
Maaa 248,48,24
Ra 48,26,8192
Maaa 248,144,28
Ra 144,27,8192
Maaa 252,148,28
D 21,44,65,1024
Ra 148,27,10240
Maaa 252,52,24
Ra 52,26,10240
Maaa 120,56,24
Ra 56,26,12288
Maaa 120,152,28
Ra 152,27,12288
Maaa 124,156,28
D 22,44,65,2048
Ra 156,27,14336
Maaa 124,60,24
W 13
Ra 60,26,14336
Mvaa 160,32,0
Mvaa 160,128,4
Mvaa 164,132,4
D 23,44,65,3072
Ta 16,12,16384
Mvaa 164,36,0
W 9
s_add_u32 m0, s61, 32768
Ta 18,12,17408
Mvaa 224,40,0
.Lh_0_3_1%=:
s_cbranch_execz .Lhr_9%=
Ta 20,12,24576
Ta 22,12,25600
Mvaa 224,136,4
Mvaa 228,140,4
D 2,40,64,0
Mvaa 228,44,0
W 8
Maaa 192,48,0
Maaa 192,144,4
Maaa 196,148,4
D 3,40,64,1024
Maaa 196,52,0
W 4
Maaa 64,56,0
Maaa 64,152,4
Maaa 68,156,4
D 4,40,64,2048
Maaa 68,60,0
Maaa 76,60,8
Maaa 76,156,12
Maaa 72,152,12
D 5,40,64,3072
Ta 24,13,16384
Maaa 72,56,8
s_add_u32 m0, s61, 36864
Ta 26,13,17408
Ta 28,13,24576
Maaa 204,52,8
Ta 30,13,25600
Maaa 204,148,12
Maaa 200,144,12
D 6,40,64,0
Maaa 200,48,8
Mvaa 236,44,8
Mvaa 236,140,12
Mvaa 232,136,12
D 7,40,64,1024
Mvaa 232,40,8
Mvaa 172,36,8
Mvaa 172,132,12
Mvaa 168,128,12
D 8,40,64,2048
Mvaa 168,32,8
W 4
Mvaa 176,32,16
Mvaa 176,128,20
Mvaa 180,132,20
D 9,40,64,3072
v_add_u32 v2, 131072, v10
Mvaa 180,36,16
Ta 0,14,16384
v_add_u32 v3, 131072, v11
Mvaa 240,40,16
Ta 2,14,17408
v_add_u32 v4, 131072, v12
Mvaa 240,136,20
Ta 4,14,24576
Ta 6,14,25600
Mvaa 244,140,20
buffer_load_dword v142, v144, s[8:11], s91 offen offset:2048 sc1
v_add_u32 v5, 131072, v13
Mvaa 244,44,16
s_add_u32 s91, s91, 4096
v_add_u32 v6, 131072, v14
Maaa 208,48,16
v_add_u32 v7, 131072, v15
Maaa 208,144,20
Maaa 212,148,20
v_add_u32 v8, 131072, v24
Maaa 212,52,16
Maaa 80,56,16
v_add_u32 v9, 131072, v25
Maaa 80,152,20
s_add_u32 s65, s5, 0
s_cmp_ge_u32 s65, 20
s_cselect_b32 s90, 20, 0
s_sub_u32 s65, s65, s90
Maaa 84,156,20
s_lshl_b32 s65, s65, 18
s_add_u32 m0, s63, 131072
Maaa 84,60,16
W 4
Maaa 92,60,24
Maaa 92,156,28
Maaa 88,152,28
Ta 8,15,16384
Ta 10,15,17408
Maaa 88,56,24
D 16,48,65,0
Ta 12,15,24576
Maaa 220,52,24
Ta 14,15,25600
Maaa 220,148,28
Maaa 216,144,28
Maaa 216,48,24
D 17,48,65,1024
Mvaa 252,44,24
Mvaa 252,140,28
Mvaa 248,136,28
Mvaa 248,40,24
Mvaa 188,36,24
D 18,48,65,2048
Mvaa 188,132,28
Mvaa 184,128,28
Mvaa 184,32,24
W 4
Mvaa 192,32,0
Mvaa 192,128,4
D 19,48,65,3072
Mvaa 196,132,4
s_add_u32 m0, s63, 135168
Ta 16,24,16384
Ta 18,24,17408
Mvaa 196,36,0
Ta 20,24,24576
Ta 22,24,25600
Maaa 160,40,0
Maaa 160,136,4
Maaa 164,140,4
D 20,48,65,0
Maaa 164,44,0
Maaa 224,48,0
Maaa 224,144,4
Maaa 228,148,4
Maaa 228,52,0
D 21,48,65,1024
Maaa 96,56,0
Maaa 96,152,4
Maaa 100,156,4
Maaa 100,60,0
)"
R"(W 4
Maaa 108,60,8
D 22,48,65,2048
Ta 24,25,16384
Maaa 108,156,12
Ta 26,25,17408
Maaa 104,152,12
Ta 28,25,24576
Ta 30,25,25600
Maaa 104,56,8
Maaa 236,52,8
D 23,48,65,3072
Maaa 236,148,12
Maaa 232,144,12
Maaa 232,48,8
Maaa 172,44,8
Maaa 172,140,12
Maaa 168,136,12
s_waitcnt vmcnt(9) lgkmcnt(0)
s_barrier
s_cmp_eq_u32 s85, 107
s_cbranch_scc1 .Lrt_25%=
.Lrtr_26%=:
Maaa 168,40,8
s_add_u32 s65, s5, 4
Mvaa 204,36,8
Mvaa 204,132,12
s_cmp_ge_u32 s65, 20
s_cselect_b32 s90, 20, 0
Mvaa 200,128,12
Mvaa 200,32,8
s_sub_u32 s65, s65, s90
Mvaa 208,32,16
Mvaa 208,128,20
s_lshl_b32 s65, s65, 18
Mvaa 212,132,20
Ta 0,10,49152
Ta 2,10,50176
Mvaa 212,36,16
Maaa 176,40,16
s_add_u32 m0, s63, 0
Maaa 176,136,20
Maaa 180,140,20
D 16,48,65,0
Maaa 180,44,16
Maaa 240,48,16
Maaa 240,144,20
Maaa 244,148,20
D 17,48,65,1024
Ta 4,11,49152
Maaa 244,52,16
Ta 6,11,50176
Maaa 112,56,16
Maaa 112,152,20
Maaa 116,156,20
D 18,48,65,2048
Maaa 116,60,16
Maaa 124,60,24
Ra 60,26,47104
Maaa 124,156,28
Maaa 120,152,28
D 19,48,65,3072
Ta 8,12,49152
Maaa 120,56,24
s_add_u32 m0, s63, 4096
Ta 10,12,50176
Ra 56,26,45056
Maaa 252,52,24
Ra 52,26,43008
Maaa 252,148,28
Maaa 248,144,28
D 20,48,65,0
Maaa 248,48,24
Ra 48,26,40960
Maaa 188,44,24
Ra 44,26,38912
Maaa 188,140,28
Maaa 184,136,28
D 21,48,65,1024
Ta 12,13,49152
Maaa 184,40,24
Ta 14,13,50176
Ra 40,26,36864
Mvaa 220,36,24
Ra 36,26,34816
Mvaa 220,132,28
Mvaa 216,128,28
D 22,48,65,2048
Mvaa 216,32,24
Ra 32,26,32768
W 6
Maaa 68,60,0
Maaa 64,56,0
Maaa 196,52,0
D 23,48,65,3072
Ta 16,14,49152
Maaa 192,48,0
W 1
s_add_u32 s65, s5, 8
s_cmp_ge_u32 s65, 20
s_cselect_b32 s90, 20, 0
Mvaa 228,44,0
s_sub_u32 s65, s65, s90
s_lshl_b32 s65, s65, 18
Ta 18,14,50176
Mvaa 224,40,0
s_add_u32 m0, s63, 65536
Mvaa 164,36,0
D 16,48,65,0
Mvaa 160,32,0
Mvaa 168,32,4
Mvaa 172,36,4
Mvaa 232,40,4
D 17,48,65,1024
Ta 20,15,49152
Mvaa 236,44,4
Ta 22,15,50176
Maaa 200,48,4
Maaa 204,52,4
Maaa 72,56,4
D 18,48,65,2048
Maaa 76,60,4
Maaa 84,60,8
Maaa 80,56,8
Maaa 212,52,8
D 19,48,65,3072
Ta 24,24,49152
Maaa 208,48,8
s_add_u32 m0, s63, 69632
Ta 26,24,50176
Mvaa 244,44,8
Mvaa 240,40,8
Mvaa 180,36,8
D 20,48,65,0
Mvaa 176,32,8
Mvaa 184,32,12
Mvaa 188,36,12
Mvaa 248,40,12
D 21,48,65,1024
Ta 28,25,49152
Mvaa 252,44,12
Ta 30,25,50176
Maaa 216,48,12
Ra 156,27,47104
Maaa 220,52,12
Maaa 88,56,12
D 22,48,65,2048
Maaa 92,60,12
W 7
Ra 152,27,45056
Maaa 100,60,16
Maaa 96,56,16
Maaa 228,52,16
D 23,48,65,3072
Ra 148,27,43008
Maaa 224,48,16
Ta 0,10,57344
Ta 2,10,58368
Maaa 164,44,16
Maaa 160,40,16
Ra 144,27,40960
Mvaa 196,36,16
buffer_load_dword v142, v144, s[8:11], s91 offen offset:0 sc1
Mvaa 192,32,16
W 10
Mvaa 200,32,20
Ra 140,27,38912
Mvaa 204,36,20
Maaa 168,40,20
Ta 4,11,57344
Ta 6,11,58368
Maaa 172,44,20
Ra 136,27,36864
Maaa 232,48,20
Maaa 236,52,20
v_bfe_u32 v29, v1, 7, 1
Maaa 104,56,20
Ra 132,27,34816
Maaa 108,60,20
W 13
v_mul_u32_u24 v28, 10240, v29
Maaa 116,60,24
Maaa 112,56,24
Ra 128,27,32768
v_lshrrev_b32 v29, 8, v1
Maaa 244,52,24
Ta 8,12,57344
Ta 10,12,58368
Maaa 240,48,24
v_lshl_add_u32 v28, v29, 7, v28
Maaa 180,44,24
v_and_b32 v29, 0x70, v1
Maaa 176,40,24
Mvaa 212,36,24
v_add_u32 v28, v28, v29
Mvaa 208,32,24
W 14
Mvaa 216,32,28
s_and_b32 s80, s14, 2
Mvaa 220,36,28
s_mul_i32 s80, s80, 20480
Maaa 184,40,28
Ta 12,13,57344
Ta 14,13,58368
Maaa 188,44,28
s_sub_u32 s81, 40960, s80
Maaa 248,48,28
Maaa 252,52,28
s_add_u32 s93, s28, 20480
Maaa 120,56,28
Maaa 124,60,28
W 10
v_add_u32 v26, s80, v28
Maaa 68,156,0
Maaa 64,152,0
Maaa 196,148,0
Ta 16,14,57344
v_add_u32 v27, s81, v28
Maaa 192,144,0
W 5
Ta 18,14,58368
Mvaa 228,140,0
buffer_load_dwordx4 a[32:35], v26, s[8:11], s28 offen offset:0 sc1
Mvaa 224,136,0
Mvaa 164,132,0
Mvaa 160,128,0
Mvaa 168,128,4
buffer_load_dwordx4 a[36:39], v26, s[8:11], s93 offen offset:0 sc1
Mvaa 172,132,4
Mvaa 232,136,4
Ta 20,15,57344
Ta 22,15,58368
Mvaa 236,140,4
Maaa 200,144,4
buffer_load_dwordx4 a[40:43], v27, s[8:11], s28 offen offset:0 sc1
Maaa 204,148,4
Maaa 72,152,4
Maaa 76,156,4
W 6
Maaa 84,156,8
buffer_load_dwordx4 a[44:47], v27, s[8:11], s93 offen offset:0 sc1
Ta 24,24,57344
Maaa 80,152,8
s_add_u32 s28, s28, 512
s_cmp_eq_u32 s28, s4
s_cselect_b32 s93, 10240, 0
Ta 26,24,58368
Maaa 212,148,8
s_sub_u32 s28, s28, s93
Ta 28,25,57344
Ta 30,25,58368
Maaa 208,144,8
s_add_u32 s93, s28, 20480
Mvaa 244,140,8
Mvaa 240,136,8
Mvaa 180,132,8
s_add_u32 s65, s5, 12
s_cmp_ge_u32 s65, 20
s_cselect_b32 s90, 20, 0
buffer_load_dwordx4 a[48:51], v26, s[8:11], s28 offen offset:0 sc1
Mvaa 176,128,8
W 8
s_sub_u32 s65, s65, s90
s_lshl_b32 s65, s65, 18
Mvaa 184,128,12
s_add_u32 m0, s63, 32768
Mvaa 188,132,12
Mvaa 248,136,12
s_waitcnt vmcnt(6) lgkmcnt(0)
s_barrier
Mvaa 252,140,12
buffer_load_dwordx4 a[52:55], v26, s[8:11], s93 offen offset:0 sc1
Maaa 216,144,12
Maaa 220,148,12
Maaa 88,152,12
Maaa 92,156,12
buffer_load_dwordx4 a[56:59], v27, s[8:11], s28 offen offset:0 sc1
Maaa 100,156,16
Maaa 96,152,16
Maaa 228,148,16
Maaa 224,144,16
buffer_load_dwordx4 a[60:63], v27, s[8:11], s93 offen offset:0 sc1
Maaa 164,140,16
Maaa 160,136,16
Mvaa 196,132,16
Mvaa 192,128,16
D 16,48,65,0
Mvaa 200,128,20
Mvaa 204,132,20
Maaa 168,136,20
Ta 0,2,0
Ta 2,2,1024
Maaa 172,140,20
D 17,48,65,1024
Maaa 232,144,20
Maaa 236,148,20
Maaa 104,152,20
Ta 4,2,8192
Ta 6,2,9216
Maaa 108,156,20
D 18,48,65,2048
Maaa 116,156,24
Maaa 112,152,24
Maaa 244,148,24
Ta 8,3,0
Ta 10,3,1024
Maaa 240,144,24
D 19,48,65,3072
Maaa 180,140,24
s_add_u32 m0, s63, 36864
Maaa 176,136,24
Mvaa 212,132,24
Ta 12,3,8192
Ta 14,3,9216
Mvaa 208,128,24
D 20,48,65,0
Mvaa 216,128,28
Mvaa 220,132,28
Maaa 184,136,28
Ta 16,4,0
Ta 18,4,1024
Maaa 188,140,28
Maaa 248,144,28
D 21,48,65,1024
Maaa 252,148,28
Maaa 120,152,28
Maaa 124,156,28
.Lq_11%=:
s_cmp_ge_u32 s14, 2
s_cbranch_scc1 .Lpb_31%=
s_waitcnt vmcnt(6) lgkmcnt(6)
Zvaa 32,0,32
s_add_u32 s28, s28, 512
Ta 20,4,8192
Ta 22,4,9216
Mvaa 32,4,48
s_cmp_eq_u32 s28, s4
s_cselect_b32 s93, 10240, 0
Ta 24,5,0
Ta 26,5,1024
Zvaa 64,4,52
s_sub_u32 s28, s28, s93
s_add_u32 s93, s28, 20480
buffer_load_dwordx4 a[128:131], v26, s[8:11], s28 offen offset:0 sc1
Mvaa 64,0,36
Ta 28,5,8192
Ta 30,5,9216
Zvaa 96,0,40
K 184,184,185
K 185,186,187
Mvaa 96,4,56
K 186,188,189
K 187,190,191
Zvaa 128,4,60
K 192,192,193
Mvaa 128,0,44
W 8
buffer_load_dwordx4 a[132:135], v26, s[8:11], s93 offen offset:0 sc1
K 193,194,195
Zvaa 132,8,44
Ta 0,6,0
Ta 2,6,1024
Mvaa 132,12,60
K 194,196,197
K 195,198,199
Zvaa 100,12,56
K 200,200,201
K 201,202,203
Mvaa 100,8,40
Ta 4,6,8192
Ta 6,6,9216
Zvaa 68,8,36
buffer_load_dwordx4 a[136:139], v27, s[8:11], s28 offen offset:0 sc1
K 202,204,205
Mvaa 68,12,52
K 203,206,207
K 208,208,209
Zvaa 36,12,48
K 209,210,211
K 210,212,213
Mvaa 36,8,32
W 8
Ta 8,7,0
K 211,214,215
Zvaa 40,16,32
Ta 10,7,1024
K 216,216,217
Mvaa 40,20,48
buffer_load_dwordx4 a[140:143], v27, s[8:11], s93 offen offset:0 sc1
K 217,218,219
Zvaa 72,20,52
s_add_u32 s28, s28, 512
s_cmp_eq_u32 s28, s4
s_cselect_b32 s93, 10240, 0
s_sub_u32 s28, s28, s93
Mvaa 72,16,36
s_add_u32 s93, s28, 20480
K 218,220,221
K 219,222,223
Zvaa 104,16,40
)"
R"(Ta 12,7,8192
Ta 14,7,9216
Mvaa 104,20,56
K 224,224,225
K 225,226,227
Zvaa 136,20,60
K 226,228,229
K 227,230,231
Mvaa 136,16,44
W 8
buffer_load_dwordx4 a[144:147], v26, s[8:11], s28 offen offset:0 sc1
K 232,232,233
Zvaa 140,24,44
K 233,234,235
K 234,236,237
Mvaa 140,28,60
Ta 16,8,0
K 235,238,239
Zvaa 108,28,56
Ta 18,8,1024
K 240,240,241
Mvaa 108,24,40
K 241,242,243
K 242,244,245
Zvaa 76,24,36
buffer_load_dwordx4 a[148:151], v26, s[8:11], s93 offen offset:0 sc1
K 243,246,247
Mvaa 76,28,52
Ta 20,8,8192
Ta 22,8,9216
Zvaa 44,28,48
K 248,248,249
K 249,250,251
Mvaa 44,24,32
K 250,252,253
K 251,254,255
W 8
Zvaa 48,0,32
Ta 24,9,0
Ta 26,9,1024
Mvaa 48,4,48
buffer_load_dwordx4 a[152:155], v27, s[8:11], s28 offen offset:0 sc1
K 160,160,161
Zvaa 80,4,52
Ta 28,9,8192
Ta 30,9,9216
Mvaa 80,0,36
K 161,162,163
Zvaa 112,0,40
K 162,164,165
Mvaa 112,4,56
Zvaa 144,4,60
buffer_load_dwordx4 a[156:159], v27, s[8:11], s93 offen offset:0 sc1
K 163,166,167
Mvaa 144,0,44
W 8
s_add_u32 s28, s28, 512
s_cmp_eq_u32 s28, s4
s_cselect_b32 s93, 10240, 0
Zvaa 148,8,44
s_sub_u32 s28, s28, s93
s_add_u32 s93, s28, 20480
Ta 0,2,16384
Mvaa 148,12,60
s_add_u32 s36, s29, 0
Ta 2,2,17408
Zvaa 116,12,56
Ta 4,2,24576
Ta 6,2,25600
Mvaa 116,8,40
buffer_load_dwordx4 v[188:191], v26, s[8:11], s28 offen offset:0 sc1
Zvaa 84,8,36
Mvaa 84,12,52
Zvaa 52,12,48
Mvaa 52,8,32
W 8
Ta 8,3,16384
Ta 10,3,17408
Zvaa 56,16,32
buffer_load_dwordx4 v[196:199], v26, s[8:11], s93 offen offset:0 sc1
Mvaa 56,20,48
Zvaa 88,20,52
Ta 12,3,24576
Ta 14,3,25600
Mvaa 88,16,36
Zvaa 120,16,40
buffer_store_dwordx4 v[160:163], v1, s[8:11], s36 offen offset:0 sc1
K 168,168,169
Mvaa 120,20,56
K 169,170,171
K 170,172,173
Zvaa 152,20,60
Mvaa 152,16,44
W 8
Ta 16,4,16384
K 171,174,175
Zvaa 156,24,44
Ta 18,4,17408
Mvaa 156,28,60
buffer_load_dwordx4 v[204:207], v27, s[8:11], s28 offen offset:0 sc1
Zvaa 124,28,56
Ta 20,4,24576
Ta 22,4,25600
Mvaa 124,24,40
Zvaa 92,24,36
Mvaa 92,28,52
Zvaa 60,28,48
buffer_load_dwordx4 v[212:215], v27, s[8:11], s93 offen offset:0 sc1
Mvaa 60,24,32
s_waitcnt vmcnt(5) lgkmcnt(8)
s_add_u32 s28, s28, 512
s_cmp_eq_u32 s28, s4
s_cselect_b32 s93, 10240, 0
Mvaa 32,0,128
s_sub_u32 s28, s28, s93
s_add_u32 s93, s28, 20480
Ta 24,5,16384
Mvaa 32,4,144
Ta 26,5,17408
Mvaa 64,4,148
Ta 28,5,24576
Ta 30,5,25600
Mvaa 64,0,132
buffer_store_dwordx4 v[168:171], v1, s[8:11], s36 offen offset:1024 sc1
K 176,176,177
Mvaa 96,0,136
K 177,178,179
K 178,180,181
Mvaa 96,4,152
Mvaa 128,4,156
Mvaa 128,0,140
W 8
Ta 0,6,16384
K 179,182,183
Mvaa 132,8,140
buffer_load_dwordx4 v[220:223], v26, s[8:11], s28 offen offset:0 sc1
Ta 2,6,17408
Mvaa 132,12,156
Mvaa 100,12,152
Ta 4,6,24576
Ta 6,6,25600
Mvaa 100,8,136
Mvaa 68,8,132
Mvaa 68,12,148
buffer_load_dwordx4 v[228:231], v26, s[8:11], s93 offen offset:0 sc1
Mvaa 36,12,144
Mvaa 36,8,128
W 8
Ta 8,7,16384
Ta 10,7,17408
Mvaa 40,16,128
Mvaa 40,20,144
Mvaa 72,20,148
buffer_load_dwordx4 v[236:239], v27, s[8:11], s28 offen offset:0 sc1
Ta 12,7,24576
Mvaa 72,16,132
buffer_store_dwordx4 v[176:179], v1, s[8:11], s36 offen offset:2048 sc1
Ta 14,7,25600
Mvaa 104,16,136
Mvaa 104,20,152
Mvaa 136,20,156
Mvaa 136,16,140
W 8
Ta 16,8,16384
Ta 18,8,17408
Mvaa 140,24,140
buffer_load_dwordx4 v[244:247], v27, s[8:11], s93 offen offset:0 sc1
Mvaa 140,28,156
s_add_u32 s28, s28, 512
s_cmp_eq_u32 s28, s4
s_cselect_b32 s93, 10240, 0
s_sub_u32 s28, s28, s93
Mvaa 108,28,152
s_add_u32 s93, s28, 20480
Ta 20,8,24576
Ta 22,8,25600
Mvaa 108,24,136
Mvaa 76,24,132
Mvaa 76,28,148
buffer_load_dwordx4 v[252:255], v26, s[8:11], s28 offen offset:0 sc1
Mvaa 44,28,144
Mvaa 44,24,128
W 8
Ta 24,9,16384
Ta 26,9,17408
Mvaa 48,0,128
Mvaa 48,4,144
Mvaa 80,4,148
D 22,48,65,2048
Ta 28,9,24576
Mvaa 80,0,132
buffer_store_dwordx4 v[184:187], v1, s[8:11], s36 offen offset:3072 sc1
Ta 30,9,25600
Mvaa 112,0,136
s_add_u32 s36, s29, 4096
s_lshl_b32 s80, s56, 9
Mvaa 112,4,152
Mvaa 144,4,156
s_lshl_b32 s81, s57, 4
Mvaa 144,0,140
W 8
s_add_u32 s80, s80, s81
Ta 0,10,0
Mvaa 148,8,140
buffer_load_dwordx4 a[32:35], v26, s[8:11], s93 offen offset:0 sc1
Ta 2,10,1024
Mvaa 148,12,156
s_and_b32 s81, s14, 1
Mvaa 116,12,152
s_lshl_b32 s81, s81, 3
Ta 4,10,8192
Ta 6,10,9216
Mvaa 116,8,136
Mvaa 84,8,132
s_add_u32 s80, s80, s81
Mvaa 84,12,148
s_add_u32 s71, s80, 4096
buffer_load_dwordx4 a[36:39], v27, s[8:11], s28 offen offset:0 sc1
Mvaa 52,12,144
Mvaa 52,8,128
W 8
Ta 8,11,0
Ta 10,11,1024
Mvaa 56,16,128
Mvaa 56,20,144
Mvaa 88,20,148
buffer_store_dwordx4 v[192:195], v1, s[8:11], s36 offen offset:0 sc1
Ta 12,11,8192
Mvaa 88,16,132
Ta 14,11,9216
Mvaa 120,16,136
Mvaa 120,20,152
Mvaa 152,20,156
Mvaa 152,16,140
W 8
buffer_load_dwordx4 a[40:43], v27, s[8:11], s93 offen offset:0 sc1
Ta 16,12,0
Mvaa 156,24,140
s_add_u32 s28, s28, 512
s_cmp_eq_u32 s28, s4
s_cselect_b32 s93, 10240, 0
s_sub_u32 s28, s28, s93
Mvaa 156,28,156
s_add_u32 s93, s28, 20480
Ta 18,12,1024
Mvaa 124,28,152
Ta 20,12,8192
Ta 22,12,9216
Mvaa 124,24,136
Mvaa 92,24,132
buffer_load_dwordx4 a[44:47], v26, s[8:11], s28 offen offset:0 sc1
Mvaa 92,28,148
Mvaa 60,28,144
Mvaa 60,24,128
s_waitcnt vmcnt(8) lgkmcnt(8)
Ta 24,13,0
Ta 26,13,1024
Mvav 32,0,188
Mvav 32,4,220
D 23,48,65,3072
Mvav 64,4,228
s_add_u32 s65, s5, 16
s_cmp_ge_u32 s65, 20
s_cselect_b32 s90, 20, 0
buffer_store_dwordx4 v[200:203], v1, s[8:11], s36 offen offset:1024 sc1
Mvav 64,0,196
s_sub_u32 s65, s65, s90
s_lshl_b32 s65, s65, 18
s_add_u32 m0, s63, 98304
Mvav 96,0,204
Ta 28,13,8192
Ta 30,13,9216
Mvav 96,4,236
Mvav 128,4,244
Mvav 128,0,212
W 8
buffer_load_dwordx4 a[48:51], v26, s[8:11], s93 offen offset:0 sc1
Ta 0,14,0
Mvav 132,8,212
Ta 2,14,1024
v_add_u32 v2, 65536, v10
Mvav 132,12,244
Mvav 100,12,236
Ta 4,14,8192
Ta 6,14,9216
Mvav 100,8,204
v_add_u32 v3, 65536, v11
Mvav 68,8,196
buffer_load_dwordx4 a[52:55], v27, s[8:11], s28 offen offset:0 sc1
Mvav 68,12,228
Mvav 36,12,220
Mvav 36,8,188
W 8
Ta 8,15,0
Ta 10,15,1024
Mvav 40,16,188
v_add_u32 v4, 65536, v12
Mvav 40,20,220
buffer_load_dwordx4 a[56:59], v27, s[8:11], s93 offen offset:0 sc1
Mvav 72,20,228
s_add_u32 s28, s28, 512
s_cmp_eq_u32 s28, s4
s_cselect_b32 s93, 10240, 0
buffer_store_dwordx4 v[208:211], v1, s[8:11], s36 offen offset:2048 sc1
Mvav 72,16,196
s_sub_u32 s28, s28, s93
s_add_u32 s93, s28, 20480
Ta 12,15,8192
Mvav 104,16,204
Ta 14,15,9216
v_add_u32 v5, 65536, v13
Mvav 104,20,236
Mvav 136,20,244
Mvav 136,16,212
W 8
D 16,48,65,0
Ta 16,24,0
Mvav 140,24,212
Ta 18,24,1024
v_add_u32 v6, 65536, v14
Mvav 140,28,244
Mvav 108,28,236
Ta 20,24,8192
Ta 22,24,9216
Mvav 108,24,204
v_add_u32 v7, 65536, v15
Mvav 76,24,196
buffer_load_dwordx4 a[60:63], v26, s[8:11], s28 offen offset:0 sc1
Mvav 76,28,228
Mvav 44,28,220
Mvav 44,24,188
W 8
Ta 24,25,0
Ta 26,25,1024
Mvav 48,0,188
v_add_u32 v8, 65536, v24
Mvav 48,4,220
buffer_load_dwordx4 v[184:187], v26, s[8:11], s93 offen offset:0 sc1
Mvav 80,4,228
buffer_store_dwordx4 v[216:219], v1, s[8:11], s36 offen offset:3072 sc1
Ta 28,25,8192
Mvav 80,0,196
s_add_u32 s36, s29, 458752
Ta 30,25,9216
v_add_u32 v9, 65536, v25
Mvav 112,0,204
Mvav 112,4,236
Mvav 144,4,244
Mvav 144,0,212
W 8
buffer_load_dwordx4 v[192:195], v27, s[8:11], s28 offen offset:0 sc1
Ta 0,10,16384
Mvav 148,8,212
Ta 2,10,17408
Mvav 148,12,244
Mvav 116,12,236
Ta 4,10,24576
Ta 6,10,25600
Mvav 116,8,204
Mvav 84,8,196
D 17,48,65,1024
Mvav 84,12,228
Mvav 52,12,220
Mvav 52,8,188
W 8
Ta 8,11,16384
Ta 10,11,17408
Mvav 56,16,188
Mvav 56,20,220
buffer_store_dwordx4 v[224:227], v1, s[8:11], s36 offen offset:0 sc1
Mvav 88,20,228
Ta 12,11,24576
Ta 14,11,25600
Mvav 88,16,196
Mvav 120,16,204
Mvav 120,20,236
Mvav 152,20,244
buffer_load_dwordx4 a[128:131], v27, s[8:11], s93 offen offset:0 sc1
Mvav 152,16,212
W 8
s_add_u32 s28, s28, 512
s_cmp_eq_u32 s28, s4
s_cselect_b32 s93, 10240, 0
Mvav 156,24,212
s_sub_u32 s28, s28, s93
s_add_u32 s93, s28, 20480
Ta 16,12,16384
Mvav 156,28,244
Ta 18,12,17408
Mvav 124,28,236
Ta 20,12,24576
Ta 22,12,25600
Mvav 124,24,204
buffer_load_dwordx4 a[132:135], v26, s[8:11], s28 offen offset:0 sc1
Mvav 92,24,196
Mvav 92,28,228
Mvav 60,28,220
Mvav 60,24,188
s_waitcnt vmcnt(10) lgkmcnt(8)
Ta 24,13,16384
)"
R"(Ta 26,13,17408
Mvav 32,0,252
buffer_load_dwordx4 a[136:139], v26, s[8:11], s93 offen offset:0 sc1
Mvaa 32,4,44
buffer_store_dwordx4 v[232:235], v1, s[8:11], s36 offen offset:1024 sc1
Mvaa 64,4,48
Ta 28,13,24576
Ta 30,13,25600
Mvaa 64,0,32
Mvaa 96,0,36
Mvaa 96,4,52
Mvaa 128,4,56
buffer_load_dwordx4 a[140:143], v27, s[8:11], s28 offen offset:0 sc1
Mvaa 128,0,40
W 8
Ta 0,14,16384
Ta 2,14,17408
Mvaa 132,8,40
Mvaa 132,12,56
Mvaa 100,12,52
Ta 4,14,24576
Ta 6,14,25600
Mvaa 100,8,36
D 18,48,65,2048
Mvaa 68,8,32
Mvaa 68,12,48
Mvaa 36,12,44
Mvav 36,8,252
W 8
Ta 8,15,16384
Ta 10,15,17408
Mvav 40,16,252
buffer_load_dwordx4 a[144:147], v27, s[8:11], s93 offen offset:0 sc1
Mvaa 40,20,44
s_add_u32 s28, s28, 512
s_cmp_eq_u32 s28, s4
s_cselect_b32 s93, 10240, 0
buffer_store_dwordx4 v[240:243], v1, s[8:11], s36 offen offset:2048 sc1
Mvaa 72,20,48
s_sub_u32 s28, s28, s93
s_add_u32 s93, s28, 20480
Ta 12,15,24576
Mvaa 72,16,32
Ta 14,15,25600
Mvaa 104,16,36
Mvaa 104,20,52
Mvaa 136,20,56
buffer_load_dwordx4 a[148:151], v26, s[8:11], s28 offen offset:0 sc1
Mvaa 136,16,40
W 8
Ta 16,24,16384
Ta 18,24,17408
Mvaa 140,24,40
Mvaa 140,28,56
Mvaa 108,28,52
Ta 20,24,24576
Ta 22,24,25600
Mvaa 108,24,36
buffer_load_dwordx4 a[152:155], v26, s[8:11], s93 offen offset:0 sc1
Mvaa 76,24,32
Mvaa 76,28,48
Mvaa 44,28,44
Mvav 44,24,252
W 8
Ta 24,25,16384
Ta 26,25,17408
Mvav 48,0,252
buffer_load_dwordx4 a[156:159], v27, s[8:11], s28 offen offset:0 sc1
Mvaa 48,4,44
buffer_store_dwordx4 v[248:251], v1, s[8:11], s36 offen offset:3072 sc1
G 160,160
Mvaa 80,4,48
G 161,161
G 162,162
Mvaa 80,0,32
G 163,163
G 164,164
Mvaa 112,0,36
G 165,165
G 166,166
Mvaa 112,4,52
Ta 28,25,24576
Ta 30,25,25600
Mvaa 144,4,56
D 19,48,65,3072
G 167,167
Mvaa 144,0,40
W 8
s_add_u32 m0, s63, 102400
K 160,160,161
Mvaa 148,8,40
K 161,162,163
K 162,164,165
Mvaa 148,12,56
Ta 0,2,0
K 163,166,167
Mvaa 116,12,52
s_add_u32 s36, s29, 462848
Ta 2,2,1024
Ta 4,2,8192
Mvaa 116,8,36
buffer_load_dwordx4 v[200:203], v27, s[8:11], s93 offen offset:0 sc1
Ta 6,2,9216
Mvaa 84,8,32
s_add_u32 s28, s28, 512
s_cmp_eq_u32 s28, s4
s_cselect_b32 s93, 10240, 0
s_sub_u32 s28, s28, s93
Mvaa 84,12,48
s_add_u32 s93, s28, 20480
Mvaa 52,12,44
Mvav 52,8,252
W 8
Ta 8,3,0
Ta 10,3,1024
Mvav 56,16,252
buffer_store_dwordx4 v[160:163], v1, s[8:11], s36 offen offset:0 sc1
G 168,168
Mvaa 56,20,44
G 169,169
G 170,170
Mvaa 88,20,48
G 171,171
G 172,172
Mvaa 88,16,32
G 173,173
G 174,174
Mvaa 120,16,36
Ta 12,3,8192
Ta 14,3,9216
Mvaa 120,20,52
buffer_load_dwordx4 v[208:211], v26, s[8:11], s28 offen offset:0 sc1
G 175,175
Mvaa 152,20,56
K 168,168,169
K 169,170,171
Mvaa 152,16,40
W 8
K 170,172,173
K 171,174,175
Mvaa 156,24,40
Ta 16,4,0
Ta 18,4,1024
Mvaa 156,28,56
Mvaa 124,28,52
buffer_load_dwordx4 v[216:219], v26, s[8:11], s93 offen offset:0 sc1
Ta 20,4,8192
Mvaa 124,24,36
Ta 22,4,9216
Mvaa 92,24,32
Mvaa 92,28,48
Mvaa 60,28,44
Mvav 60,24,252
s_waitcnt vmcnt(10) lgkmcnt(8)
D 20,48,65,0
Ta 24,5,0
Mvaa 32,0,60
buffer_store_dwordx4 v[168:171], v1, s[8:11], s36 offen offset:1024 sc1
G 176,176
Mvaa 32,4,132
G 177,177
G 178,178
Mvaa 64,4,136
Ta 26,5,1024
G 179,179
Mvav 64,0,184
G 180,180
G 181,181
Mvav 96,0,192
Ta 28,5,8192
G 182,182
Mvaa 96,4,140
buffer_load_dwordx4 v[224:227], v27, s[8:11], s28 offen offset:0 sc1
Ta 30,5,9216
Mvaa 128,4,144
G 183,183
K 176,176,177
Mvaa 128,0,128
W 8
K 177,178,179
K 178,180,181
Mvaa 132,8,128
Ta 0,6,0
K 179,182,183
Mvaa 132,12,144
Ta 2,6,1024
Mvaa 100,12,140
buffer_load_dwordx4 v[188:191], v27, s[8:11], s93 offen offset:0 sc1
Ta 4,6,8192
Mvav 100,8,192
s_add_u32 s28, s28, 512
s_cmp_eq_u32 s28, s4
s_cselect_b32 s93, 10240, 0
s_sub_u32 s28, s28, s93
Mvav 68,8,184
s_add_u32 s93, s28, 20480
Ta 6,6,9216
Mvaa 68,12,136
Mvaa 36,12,132
Mvaa 36,8,60
W 8
buffer_load_dwordx4 v[196:199], v26, s[8:11], s28 offen offset:0 sc1
Ta 8,7,0
Mvaa 40,16,60
buffer_store_dwordx4 v[176:179], v1, s[8:11], s36 offen offset:2048 sc1
G 160,184
Mvaa 40,20,132
G 161,185
G 162,186
Mvaa 72,20,136
Ta 10,7,1024
G 163,187
Mvav 72,16,184
G 164,188
G 165,189
Mvav 104,16,192
Ta 12,7,8192
G 166,190
Mvaa 104,20,140
buffer_load_dwordx4 v[204:207], v26, s[8:11], s93 offen offset:0 sc1
Ta 14,7,9216
Mvaa 136,20,144
G 167,191
K 160,160,161
Mvaa 136,16,128
W 8
K 161,162,163
K 162,164,165
Mvaa 140,24,128
Ta 16,8,0
K 163,166,167
Mvaa 140,28,144
Ta 18,8,1024
Mvaa 108,28,140
D 21,48,65,1024
Ta 20,8,8192
Mvav 108,24,192
Ta 22,8,9216
Mvav 76,24,184
Mvaa 76,28,136
Mvaa 44,28,132
Mvaa 44,24,60
W 8
buffer_load_dwordx4 v[212:215], v27, s[8:11], s28 offen offset:0 sc1
Ta 24,9,0
Mvaa 48,0,60
buffer_store_dwordx4 v[160:163], v1, s[8:11], s36 offen offset:3072 sc1
G 168,192
Mvaa 48,4,132
G 169,193
G 170,194
Mvaa 80,4,136
G 171,195
G 172,196
Mvav 80,0,184
Ta 26,9,1024
G 173,197
Mvav 112,0,192
G 174,198
G 175,199
Mvaa 112,4,140
buffer_load_dwordx4 v[220:223], v27, s[8:11], s93 offen offset:0 sc1
Ta 28,9,8192
Mvaa 144,4,144
s_add_u32 s28, s28, 512
s_cmp_eq_u32 s28, s4
s_cselect_b32 s93, 10240, 0
s_sub_u32 s28, s28, s93
Mvaa 144,0,128
W 7
s_add_u32 s93, s28, 20480
Ta 30,9,9216
Mvaa 148,8,128
K 168,168,169
K 169,170,171
Mvaa 148,12,144
K 170,172,173
K 171,174,175
Mvaa 116,12,140
buffer_load_dwordx4 v[228:231], v26, s[8:11], s28 offen offset:0 sc1
Ta 0,2,16384
Mvav 116,8,192
Ta 2,2,17408
v_accvgpr_write_b32 a192, v168
Mvav 84,8,184
v_accvgpr_write_b32 a193, v169
v_accvgpr_write_b32 a194, v170
Mvaa 84,12,136
Ta 4,2,24576
Ta 6,2,25600
Mvaa 52,12,132
v_accvgpr_write_b32 a195, v171
G 176,64
Mvaa 52,8,60
W 8
D 22,48,65,2048
G 177,65
Mvaa 56,16,60
D 23,48,65,3072
G 178,66
Mvaa 56,20,132
Ta 8,3,16384
Ta 10,3,17408
Mvaa 88,20,136
G 179,67
G 180,68
Mvav 88,16,184
Ta 12,3,24576
Ta 14,3,25600
Mvav 120,16,192
G 181,69
G 182,70
Mvaa 120,20,140
buffer_load_dwordx4 v[236:239], v26, s[8:11], s93 offen offset:0 sc1
G 183,71
Mvaa 152,20,144
v_lshrrev_b32 v20, 4, v1
K 176,176,177
Mvaa 152,16,128
W 8
Ta 16,4,16384
v_add_u32 v20, 0, v20
Mvaa 156,24,128
Ta 18,4,17408
K 177,178,179
Mvaa 156,28,144
v_mul_u32_u24 v22, 205, v20
K 178,180,181
Mvaa 124,28,140
buffer_load_dwordx4 v[232:235], v27, s[8:11], s28 offen offset:0 sc1
K 179,182,183
Mvav 124,24,192
Ta 20,4,24576
Ta 22,4,25600
Mvav 92,24,184
v_lshrrev_b32 v22, 12, v22
v_accvgpr_write_b32 a196, v176
Mvaa 92,28,136
v_mul_u32_u24 v23, 20, v22
v_accvgpr_write_b32 a197, v177
Mvaa 60,28,132
v_accvgpr_write_b32 a198, v178
v_sub_u32 v20, v20, v23
Mvaa 60,24,60
s_waitcnt vmcnt(12) lgkmcnt(8)
buffer_load_dwordx4 a[32:35], v27, s[8:11], s93 offen offset:0 sc1
v_accvgpr_write_b32 a199, v179
Mvaa 32,0,148
s_add_u32 s28, s28, 512
s_cmp_eq_u32 s28, s4
s_cselect_b32 s93, 10240, 0
s_sub_u32 s28, s28, s93
Mvav 32,4,208
s_add_u32 s93, s28, 20480
Ta 24,5,16384
Ta 26,5,17408
Mvav 64,4,216
v_lshrrev_b32 v23, 1, v22
G 168,200
Mvaa 64,0,152
v_and_b32 v23, 2, v23
G 169,201
Mvaa 96,0,156
buffer_load_dwordx4 a[36:39], v26, s[8:11], s28 offen offset:0 sc1
Ta 28,5,24576
Mvav 96,4,224
Ta 30,5,25600
G 170,202
Mvav 128,4,188
v_xor_b32 v20, v20, v23
G 171,203
Mvav 128,0,200
W 8
v_mul_u32_u24 v22, 80, v22
G 172,204
Mvav 132,8,200
v_add_lshl_u32 v16, v20, v22, 4
G 173,205
Mvav 132,12,188
buffer_load_dwordx4 a[40:43], v26, s[8:11], s93 offen offset:0 sc1
Ta 0,6,16384
Mvav 100,12,224
Ta 2,6,17408
G 174,206
Mvaa 100,8,156
v_lshrrev_b32 v20, 4, v1
G 175,207
Mvaa 68,8,152
Ta 4,6,24576
v_add_u32 v20, 64, v20
Mvav 68,12,216
Ta 6,6,25600
K 168,168,169
Mvav 36,12,208
buffer_load_dwordx4 a[44:47], v27, s[8:11], s28 offen offset:0 sc1
K 169,170,171
Mvaa 36,8,148
W 8
v_mul_u32_u24 v22, 205, v20
K 170,172,173
)"
R"(Mvaa 40,16,148
v_lshrrev_b32 v22, 12, v22
K 171,174,175
Mvav 40,20,208
Ta 8,7,16384
v_mul_u32_u24 v23, 20, v22
Mvav 72,20,216
Ta 10,7,17408
v_accvgpr_write_b32 a200, v168
Mvaa 72,16,152
buffer_load_dwordx4 a[48:51], v27, s[8:11], s93 offen offset:0 sc1
v_accvgpr_write_b32 a201, v169
Mvaa 104,16,156
s_add_u32 s28, s28, 512
s_cmp_eq_u32 s28, s4
s_cselect_b32 s93, 10240, 0
s_sub_u32 s28, s28, s93
Mvav 104,20,224
s_add_u32 s93, s28, 20480
v_sub_u32 v20, v20, v23
v_accvgpr_write_b32 a202, v170
Mvav 136,20,188
Ta 12,7,24576
Ta 14,7,25600
Mvav 136,16,200
W 8
v_lshrrev_b32 v23, 1, v22
v_accvgpr_write_b32 a203, v171
Mvav 140,24,200
buffer_load_dwordx4 a[52:55], v26, s[8:11], s28 offen offset:0 sc1
v_and_b32 v23, 2, v23
Mvav 140,28,188
G 176,72
G 177,73
Mvav 108,28,224
v_xor_b32 v20, v20, v23
G 178,74
Mvaa 108,24,156
Ta 16,8,16384
Ta 18,8,17408
Mvaa 76,24,152
v_mul_u32_u24 v22, 80, v22
G 179,75
Mvav 76,28,216
buffer_load_dwordx4 a[56:59], v26, s[8:11], s93 offen offset:0 sc1
G 180,76
Mvav 44,28,208
Ta 20,8,24576
v_add_lshl_u32 v17, v20, v22, 4
Mvaa 44,24,148
W 7
Ta 22,8,25600
G 181,77
Mvaa 48,0,148
v_subrev_u32 v17, 1024, v17
G 182,78
Mvav 48,4,208
v_lshrrev_b32 v20, 4, v1
G 183,79
Mvav 80,4,216
buffer_load_dwordx4 a[184:187], v27, s[8:11], s28 offen offset:0 sc1
K 176,176,177
Mvaa 80,0,152
Ta 24,9,16384
v_add_u32 v20, 128, v20
Mvaa 112,0,156
Ta 26,9,17408
K 177,178,179
Mvav 112,4,224
v_mul_u32_u24 v22, 205, v20
K 178,180,181
Mvav 144,4,188
v_lshrrev_b32 v22, 12, v22
K 179,182,183
Mvav 144,0,200
W 6
buffer_load_dwordx4 a[188:191], v27, s[8:11], s93 offen offset:0 sc1
Ta 28,9,24576
Mvav 148,8,200
s_add_u32 s28, s28, 512
s_cmp_eq_u32 s28, s4
s_cselect_b32 s93, 10240, 0
s_sub_u32 s28, s28, s93
Mvav 148,12,188
s_add_u32 s93, s28, 20480
Ta 30,9,25600
v_accvgpr_write_b32 a204, v176
Mvav 116,12,224
v_mul_u32_u24 v23, 20, v22
v_accvgpr_write_b32 a205, v177
Mvaa 116,8,156
s_waitcnt vmcnt(15)
v_mov_b64 v[28:29], s[12:13]
s_mov_b64 exec, 1
buffer_store_dwordx2 v[28:29], off, s[8:11], s71 sc1
s_mov_b64 exec, -1
v_sub_u32 v20, v20, v23
v_accvgpr_write_b32 a206, v178
Mvaa 84,8,152
s_waitcnt vmcnt(12) lgkmcnt(0)
s_barrier
Mvav 84,12,216
v_accvgpr_write_b32 a207, v179
v_lshrrev_b32 v23, 1, v22
Mvav 52,12,208
G 168,208
v_and_b32 v23, 2, v23
Mvaa 52,8,148
G 169,209
v_xor_b32 v20, v20, v23
Mvaa 56,16,148
buffer_load_dwordx4 a[60:63], v26, s[8:11], s28 offen offset:0 sc1
G 170,210
Mvav 56,20,208
Ta 0,10,32768
G 171,211
Mvav 88,20,216
Ta 2,10,33792
Ta 4,10,40960
Mvaa 88,16,152
Ta 6,10,41984
v_mul_u32_u24 v22, 80, v22
Mvaa 120,16,156
G 172,212
v_add_lshl_u32 v18, v20, v22, 4
Mvav 120,20,224
buffer_load_dwordx4 v[184:187], v26, s[8:11], s93 offen offset:0 sc1
G 173,213
Mvav 152,20,188
Ta 8,11,32768
Ta 10,11,33792
Mvav 152,16,200
G 174,214
v_subrev_u32 v18, 2048, v18
Mvav 156,24,200
G 175,215
v_lshrrev_b32 v20, 4, v1
Mvav 156,28,188
Ta 12,11,40960
Ta 14,11,41984
Mvav 124,28,224
buffer_load_dwordx4 v[192:195], v27, s[8:11], s28 offen offset:0 sc1
K 168,168,169
Mvaa 124,24,156
v_add_u32 v20, 192, v20
K 169,170,171
Mvaa 92,24,152
K 170,172,173
v_mul_u32_u24 v22, 205, v20
Mvav 92,28,216
K 171,174,175
v_lshrrev_b32 v22, 12, v22
Mvav 60,28,208
Ta 16,12,32768
Ta 18,12,33792
Mvaa 60,24,148
s_waitcnt vmcnt(12) lgkmcnt(6)
buffer_load_dwordx4 a[128:131], v27, s[8:11], s93 offen offset:0 sc1
v_accvgpr_write_b32 a208, v168
Mvav 32,0,196
s_add_u32 s28, s28, 512
v_mul_u32_u24 v23, 20, v22
v_accvgpr_write_b32 a209, v169
Mvav 32,4,228
s_cmp_eq_u32 s28, s4
s_cselect_b32 s93, 10240, 0
v_accvgpr_write_b32 a210, v170
v_sub_u32 v20, v20, v23
Mvav 64,4,236
s_sub_u32 s28, s28, s93
Ta 20,12,40960
Ta 22,12,41984
Mvav 64,0,204
s_add_u32 s93, s28, 20480
v_accvgpr_write_b32 a211, v171
v_lshrrev_b32 v23, 1, v22
Mvav 96,0,212
G 176,80
G 177,81
Mvav 96,4,232
v_and_b32 v23, 2, v23
G 178,82
Mvaa 128,4,32
Ta 24,13,32768
v_xor_b32 v20, v20, v23
Mvav 128,0,220
W 5
Ta 26,13,33792
G 179,83
Mvav 132,8,220
buffer_load_dwordx4 a[132:135], v26, s[8:11], s28 offen offset:0 sc1
v_mul_u32_u24 v22, 80, v22
Mvaa 132,12,32
Ta 28,13,40960
G 180,84
Mvav 100,12,232
Ta 30,13,41984
G 181,85
Mvav 100,8,212
v_add_lshl_u32 v19, v20, v22, 4
G 182,86
Mvav 68,8,204
v_subrev_u32 v19, 3072, v19
G 183,87
Mvav 68,12,236
v_lshrrev_b32 v20, 4, v1
K 176,176,177
Mvav 36,12,228
Ta 0,14,32768
Ta 2,14,33792
Mvav 36,8,196
W 6
K 177,178,179
v_add_u32 v20, 256, v20
Mvav 40,16,196
buffer_load_dwordx4 a[136:139], v26, s[8:11], s93 offen offset:0 sc1
K 178,180,181
Mvav 40,20,228
v_mul_u32_u24 v22, 205, v20
K 179,182,183
Mvav 72,20,236
Ta 4,14,40960
Ta 6,14,41984
Mvav 72,16,204
v_accvgpr_write_b32 a212, v176
v_lshrrev_b32 v22, 12, v22
Mvav 104,16,212
v_accvgpr_write_b32 a213, v177
v_mul_u32_u24 v23, 20, v22
Mvav 104,20,232
v_accvgpr_write_b32 a214, v178
v_sub_u32 v20, v20, v23
Mvaa 136,20,32
Ta 8,15,32768
v_accvgpr_write_b32 a215, v179
Mvav 136,16,220
W 5
Ta 10,15,33792
buffer_load_dwordx4 a[140:143], v27, s[8:11], s28 offen offset:0 sc1
Mvav 140,24,220
G 168,216
v_lshrrev_b32 v23, 1, v22
Mvaa 140,28,32
G 169,217
v_and_b32 v23, 2, v23
Mvav 108,28,232
Ta 12,15,40960
Ta 14,15,41984
Mvav 108,24,212
G 170,218
v_xor_b32 v20, v20, v23
Mvav 76,24,204
G 171,219
G 172,220
Mvav 76,28,236
v_mul_u32_u24 v22, 80, v22
G 173,221
Mvav 44,28,228
Ta 16,24,32768
Ta 18,24,33792
Mvav 44,24,196
W 6
v_add_lshl_u32 v20, v20, v22, 4
G 174,222
Mvav 48,0,196
buffer_load_dwordx4 a[144:147], v27, s[8:11], s93 offen offset:0 sc1
G 175,223
Mvav 48,4,228
s_add_u32 s28, s28, 512
Ta 20,24,40960
v_lshrrev_b32 v22, 6, v1
Mvav 80,4,236
s_cmp_eq_u32 s28, s4
s_cselect_b32 s93, 10240, 0
Ta 22,24,41984
K 168,168,169
Mvav 80,0,204
s_sub_u32 s28, s28, s93
v_mul_u32_u24 v22, 320, v22
K 169,170,171
Mvav 112,0,212
s_add_u32 s93, s28, 20480
v_bfe_u32 v23, v1, 4, 2
K 170,172,173
Mvav 112,4,232
K 171,174,175
v_lshl_add_u32 v22, v23, 3, v22
Mvaa 144,4,32
Ta 24,25,32768
Ta 26,25,33792
Mvav 144,0,220
W 6
v_accvgpr_write_b32 a216, v168
v_bfe_u32 v23, v1, 8, 1
Mvav 148,8,220
buffer_load_dwordx4 v[200:203], v26, s[8:11], s28 offen offset:0 sc1
v_accvgpr_write_b32 a217, v169
Mvaa 148,12,32
v_lshlrev_b32 v23, 5, v23
v_accvgpr_write_b32 a218, v170
Mvav 116,12,232
Ta 28,25,40960
Ta 30,25,41984
Mvav 116,8,212
v_accvgpr_write_b32 a219, v171
v_add_u32 v21, v22, v23
Mvav 84,8,204
G 176,88
v_sub_u32 v22, v22, v23
Mvav 84,12,236
s_mul_i32 s94, s59, 655360
G 177,89
G 178,90
Mvav 52,12,228
s_mul_i32 s95, s58, 320
Ta 0,10,49152
Ta 2,10,50176
Mvav 52,8,196
W 6
buffer_load_dwordx4 v[208:211], v26, s[8:11], s93 offen offset:0 sc1
G 179,91
Mvav 56,16,196
s_add_u32 s94, s94, s95
Ta 4,10,57344
G 180,92
Mvav 56,20,228
Ta 6,10,58368
G 181,93
Mvav 88,20,236
s_add_u32 s40, s26, s94
s_addc_u32 s41, s27, 0
G 182,94
Mvav 88,16,204
s_and_b32 s41, s41, 0xffff
G 183,95
K 176,176,177
Mvav 120,16,212
s_mov_b32 s42, 5242880
Ta 8,11,49152
Ta 10,11,50176
Mvav 120,20,232
s_mov_b32 s43, 0x20000
buffer_load_dwordx4 v[216:219], v27, s[8:11], s28 offen offset:0 sc1
K 177,178,179
Mvaa 152,20,32
s_sub_u32 s42, s42, s94
K 178,180,181
K 179,182,183
Mvav 152,16,220
W 6
Ta 12,11,57344
Ta 14,11,58368
Mvav 156,24,220
s_mov_b64 s[44:45], s[8:9]
v_accvgpr_write_b32 a220, v176
v_accvgpr_write_b32 a221, v177
Mvaa 156,28,32
s_mov_b32 s47, 0x20000
v_accvgpr_write_b32 a222, v178
v_accvgpr_write_b32 a223, v179
Mvav 124,28,232
s_and_b32 s48, s14, 1
Ta 16,12,49152
Ta 18,12,50176
Mvav 124,24,212
buffer_load_dwordx4 v[224:227], v27, s[8:11], s93 offen offset:0 sc1
G 168,224
Mvav 92,24,204
s_mul_i32 s48, s48, 5120
Ta 20,12,57344
G 169,225
Mvav 92,28,236
s_lshr_b32 s51, s14, 1
Ta 22,12,58368
G 170,226
Mvav 60,28,228
G 171,227
G 172,228
Mvav 60,24,196
s_waitcnt vmcnt(13) lgkmcnt(8)
s_and_b32 s94, s14, 1
G 173,229
Mvaa 32,0,36
s_add_u32 s28, s28, 512
Ta 24,13,49152
Ta 26,13,50176
Mvaa 32,4,52
s_cmp_eq_u32 s28, s4
s_cselect_b32 s93, 10240, 0
s_sub_u32 s28, s28, s93
G 174,230
Mvaa 64,4,56
s_add_u32 s93, s28, 20480
s_lshl_b32 s95, s57, 1
buffer_load_dwordx4 v[188:191], v26, s[8:11], s28 offen offset:0 sc1
Mvaa 64,0,40
s_add_u32 s94, s94, s95
Ta 28,13,57344
G 175,231
Mvaa 96,0,44
)"
R"(s_lshl_b32 s54, s94, 3
Ta 30,13,58368
K 168,168,169
Mvaa 96,4,184
K 169,170,171
K 170,172,173
Mvaa 128,4,188
s_add_u32 s54, s54, s59
K 171,174,175
Mvaa 128,0,48
W 8
Ta 0,14,49152
Ta 2,14,50176
Mvaa 132,8,48
s_lshl_b32 s54, s54, 6
buffer_load_dwordx4 v[196:199], v26, s[8:11], s93 offen offset:0 sc1
v_accvgpr_write_b32 a224, v168
Mvaa 132,12,188
s_xor_b32 s55, s54, 15872
v_accvgpr_write_b32 a225, v169
Mvaa 100,12,184
Ta 4,14,57344
v_accvgpr_write_b32 a226, v170
Mvaa 100,8,44
s_add_u32 s54, s54, 20480
Ta 6,14,58368
v_accvgpr_write_b32 a227, v171
Mvaa 68,8,40
s_and_b32 s81, s14, 1
G 176,96
Mvaa 68,12,56
s_add_u32 s55, s55, 20480
Mvaa 36,12,52
G 177,97
Mvaa 36,8,36
W 8
s_and_b32 s95, s14, 2
G 178,98
Mvaa 40,16,36
s_mul_i32 s91, s81, 17164288
Ta 8,15,49152
Ta 10,15,50176
Mvaa 40,20,52
s_lshl_b32 s94, s94, 2
buffer_load_dwordx4 v[204:207], v27, s[8:11], s28 offen offset:0 sc1
G 179,99
Mvaa 72,20,56
s_mul_i32 s82, s56, 536576
s_add_u32 s94, s94, s95
G 180,100
Mvaa 72,16,40
Ta 12,15,57344
Ta 14,15,58368
Mvaa 104,16,44
s_lshl_b32 s95, s95, 4
G 181,101
G 182,102
Mvaa 104,20,184
s_add_u32 s91, s91, s82
G 183,103
Mvaa 136,20,188
s_add_u32 s55, s55, s95
s_add_u32 s91, s91, 4194304
K 176,176,177
Mvaa 136,16,48
W 8
s_lshl_b32 s49, s94, 17
Ta 16,24,49152
Mvaa 140,24,48
Ta 18,24,50176
K 177,178,179
Mvaa 140,28,188
s_lshl_b32 s95, s59, 14
buffer_load_dwordx4 v[212:215], v27, s[8:11], s93 offen offset:0 sc1
K 178,180,181
Mvaa 108,28,184
s_lshr_b32 s82, s14, 1
Ta 20,24,57344
K 179,182,183
Mvaa 108,24,44
s_add_u32 s49, s49, s95
Ta 22,24,58368
v_accvgpr_write_b32 a228, v176
Mvaa 76,24,40
s_lshl_b32 s83, s82, 14
s_xor_b32 s50, s49, 16252928
Mvaa 76,28,56
v_accvgpr_write_b32 a229, v177
Mvaa 44,28,52
s_add_u32 s91, s91, s83
s_add_u32 s49, s49, 39845888
v_accvgpr_write_b32 a230, v178
Mvaa 44,24,36
W 8
Ta 24,25,49152
v_accvgpr_write_b32 a231, v179
Mvaa 48,0,36
s_add_u32 s50, s50, 39845888
Ta 26,25,50176
G 168,232
Mvaa 48,4,52
s_lshl_b32 s81, s81, 14
Mvaa 80,4,56
s_lshr_b32 s94, s15, 3
Ta 28,25,57344
G 169,233
Mvaa 80,0,40
s_lshl_b32 s82, s82, 16
Ta 30,25,58368
G 170,234
Mvaa 112,0,44
s_lshl_b32 s94, s94, 3
G 171,235
Mvaa 112,4,184
s_add_u32 s94, s94, s14
s_or_b32 s92, s81, s82
G 172,236
Mvaa 144,4,188
Mvaa 144,0,48
W 8
s_lshl_b32 s94, s94, 4
G 173,237
Mvaa 148,8,48
s_xor_b32 s92, s92, 0x10000
Ta 0,2,32768
Ta 2,2,33792
Mvaa 148,12,188
s_add_u32 s94, s94, s59
G 174,238
G 175,239
Mvaa 116,12,184
s_mul_i32 s52, s94, 5120
Ta 4,2,40960
Ta 6,2,41984
Mvaa 116,8,44
K 168,168,169
Mvaa 84,8,40
s_lshl_b32 s95, s59, 12
K 169,170,171
Mvaa 84,12,56
K 170,172,173
Mvaa 52,12,52
s_sub_u32 s52, s52, s95
K 171,174,175
Mvaa 52,8,36
W 8
s_add_u32 s52, s52, 56623104
Ta 8,3,32768
Mvaa 56,16,36
Ta 10,3,33792
v_accvgpr_write_b32 a232, v168
Mvaa 56,20,52
s_lshl_b32 s53, s94, 3
v_accvgpr_write_b32 a233, v169
Mvaa 88,20,56
Ta 12,3,40960
v_accvgpr_write_b32 a234, v170
Mvaa 88,16,40
s_add_u32 s53, s53, 65536
Ta 14,3,41984
v_accvgpr_write_b32 a235, v171
Mvaa 120,16,44
G 176,104
v_bfe_u32 v244, v1, 4, 1
Mvaa 120,20,184
Mvaa 152,20,188
G 177,105
v_mul_u32_u24 v244, 0xaaaa, v244
Mvaa 152,16,48
W 8
G 178,106
v_add_u32 v244, 0x4444, v244
Mvaa 156,24,48
Ta 16,4,32768
Ta 18,4,33792
Mvaa 156,28,188
G 179,107
v_lshl_or_b32 v240, v244, 16, v244
Mvaa 124,28,184
G 180,108
v_mov_b32 v241, 0xff800000
Mvaa 124,24,44
Ta 20,4,40960
Ta 22,4,41984
Mvaa 92,24,40
G 181,109
v_add_u32 v242, 0x10000, v1
Mvaa 92,28,56
G 182,110
v_bfe_u32 v244, v1, 4, 4
Mvaa 60,28,52
G 183,111
v_lshrrev_b32 v245, 8, v1
Mvaa 60,24,36
s_waitcnt vmcnt(8) lgkmcnt(8)
K 176,176,177
v_lshlrev_b32 v245, 2, v245
Mvaa 32,0,60
Ta 24,5,32768
Ta 26,5,33792
Mvaa 32,4,132
K 177,178,179
v_sub_u32 v244, v244, v245
Mvaa 64,4,136
K 178,180,181
v_cmp_le_i32_e64 s[72:73], 0, v244
Mvav 64,0,184
Ta 28,5,40960
K 179,182,183
Mvav 96,0,192
Ta 30,5,41984
v_accvgpr_write_b32 a236, v176
Mvaa 96,4,140
v_cmp_le_i32_e64 s[74:75], 1, v244
v_accvgpr_write_b32 a237, v177
Mvaa 128,4,144
v_cmp_le_i32_e64 s[76:77], 2, v244
v_accvgpr_write_b32 a238, v178
Mvaa 128,0,128
W 8
v_cmp_le_i32_e64 s[78:79], 3, v244
v_accvgpr_write_b32 a239, v179
Mvaa 132,8,128
Ta 0,6,32768
Ta 2,6,33792
Mvaa 132,12,144
v_add_u32 v243, 4096, v1
G 168,240
Mvaa 100,12,140
v_add_u32 v244, 8192, v1
G 169,241
Mvav 100,8,192
Ta 4,6,40960
v_add_u32 v245, 12288, v1
Mvav 68,8,184
s_mov_b32 s18, 0x3f803f80
Ta 6,6,41984
G 170,242
Mvaa 68,12,136
s_mov_b32 s19, 0x3f003f00
G 171,243
G 172,244
Mvaa 36,12,132
Mvaa 36,8,60
W 8
v_accvgpr_write_b32 a176, s18
G 173,245
Mvaa 40,16,60
Ta 8,7,32768
Ta 10,7,33792
Mvaa 40,20,132
v_accvgpr_write_b32 a180, s19
G 174,246
Mvaa 72,20,136
G 175,247
v_mov_b32 v252, v241
Mvav 72,16,184
Ta 12,7,40960
Ta 14,7,41984
Mvav 104,16,192
K 168,168,169
v_accvgpr_write_b32 a177, s18
Mvaa 104,20,140
K 169,170,171
v_accvgpr_write_b32 a181, s19
Mvaa 136,20,144
K 170,172,173
v_mov_b32 v253, v241
Mvaa 136,16,128
W 8
K 171,174,175
v_accvgpr_write_b32 a178, s18
Mvaa 140,24,128
Ta 16,8,32768
Ta 18,8,33792
Mvaa 140,28,144
v_accvgpr_write_b32 a240, v168
v_accvgpr_write_b32 a182, s19
Mvaa 108,28,140
v_accvgpr_write_b32 a241, v169
v_mov_b32 v254, v241
Mvav 108,24,192
Ta 20,8,40960
v_accvgpr_write_b32 a242, v170
Mvav 76,24,184
Ta 22,8,41984
v_accvgpr_write_b32 a179, s18
Mvaa 76,28,136
v_accvgpr_write_b32 a243, v171
v_accvgpr_write_b32 a183, s19
Mvaa 44,28,132
G 176,112
v_mov_b32 v255, v241
Mvaa 44,24,60
W 8
G 177,113
G 178,114
Mvaa 48,0,60
Ta 24,9,32768
v_accvgpr_write_b32 a160, 0
Mvaa 48,4,132
Ta 26,9,33792
G 179,115
Mvaa 80,4,136
v_accvgpr_write_b32 a161, 0
G 180,116
Mvav 80,0,184
Ta 28,9,40960
v_accvgpr_write_b32 a162, 0
Mvav 112,0,192
Ta 30,9,41984
G 181,117
Mvaa 112,4,140
v_accvgpr_write_b32 a163, 0
G 182,118
Mvaa 144,4,144
v_accvgpr_write_b32 a164, 0
G 183,119
Mvaa 144,0,128
W 8
v_accvgpr_write_b32 a165, 0
K 176,176,177
Mvaa 148,8,128
Ta 0,2,49152
v_accvgpr_write_b32 a166, 0
Mvaa 148,12,144
Ta 2,2,50176
K 177,178,179
Mvaa 116,12,140
v_accvgpr_write_b32 a167, 0
K 178,180,181
Mvav 116,8,192
v_accvgpr_write_b32 a168, 0
K 179,182,183
Mvav 84,8,184
Ta 4,2,57344
Ta 6,2,58368
Mvaa 84,12,136
v_accvgpr_write_b32 a169, 0
v_accvgpr_write_b32 a244, v176
Mvaa 52,12,132
v_accvgpr_write_b32 a245, v177
v_accvgpr_write_b32 a170, 0
Mvaa 52,8,60
W 8
v_accvgpr_write_b32 a246, v178
v_accvgpr_write_b32 a171, 0
Mvaa 56,16,60
Ta 8,3,49152
v_accvgpr_write_b32 a247, v179
Mvaa 56,20,132
Ta 10,3,50176
v_accvgpr_write_b32 a172, 0
Mvaa 88,20,136
G 168,248
v_accvgpr_write_b32 a173, 0
Mvav 88,16,184
G 169,249
v_accvgpr_write_b32 a174, 0
Mvav 120,16,192
Ta 12,3,57344
Ta 14,3,58368
Mvaa 120,20,140
G 170,250
v_accvgpr_write_b32 a175, 0
Mvaa 152,20,144
s_mov_b32 s90, 0x5d800000
G 171,251
G 172,252
Mvaa 152,16,128
W 8
s_mov_b64 s[88:89], 0
G 173,253
Mvaa 156,24,128
s_mov_b32 s64, 0
Ta 16,4,49152
Ta 18,4,50176
Mvaa 156,28,144
s_and_b32 s18, s14, 1
G 174,254
G 175,255
Mvaa 124,28,140
s_lshl_b32 s62, s18, 14
Ta 20,4,57344
Ta 22,4,58368
Mvav 124,24,192
K 168,168,169
Mvav 92,24,184
s_lshl_b32 s20, s57, 15
K 169,170,171
Mvaa 92,28,136
s_sub_u32 s20, 507904, s20
K 170,172,173
Mvaa 60,28,132
K 171,174,175
Mvaa 60,24,60
s_waitcnt vmcnt(0) lgkmcnt(8)
s_add_u32 s20, s20, s62
Ta 24,5,49152
Mvav 32,0,200
)"
R"(s_lshl_b32 s65, s57, 1
Ta 26,5,50176
v_accvgpr_write_b32 a248, v168
Mvav 32,4,188
s_sub_u32 s65, s65, 1
v_accvgpr_write_b32 a249, v169
Mvav 64,4,196
Ta 28,5,57344
v_accvgpr_write_b32 a250, v170
Mvav 64,0,208
s_max_i32 s65, s65, 0
Ta 30,5,58368
v_accvgpr_write_b32 a251, v171
Mvav 96,0,216
s_sub_u32 s66, s60, 1
G 176,120
Mvav 96,4,204
s_cmp_lt_u32 s14, 2
s_cselect_b32 s20, s20, 0
s_cselect_b32 s66, s66, 15
s_cselect_b32 s21, 32768, 0
Mvav 128,4,212
G 177,121
Mvav 128,0,224
W 8
s_add_u32 s61, s91, s20
G 178,122
Mvav 132,8,224
s_add_u32 s62, s62, s21
Ta 0,6,49152
Ta 2,6,50176
Mvav 132,12,212
G 179,123
G 180,124
Mvav 100,12,204
s_sub_u32 s63, 16384, s21
Ta 4,6,57344
Ta 6,6,58368
Mvav 100,8,216
s_lshl_b32 s67, s60, 7
G 181,125
Mvav 68,8,208
s_add_u32 s67, s67, s56
G 182,126
Mvav 68,12,196
G 183,127
Mvav 36,12,188
s_lshl_b32 s67, s67, 12
K 176,176,177
Mvav 36,8,200
W 8
s_add_u32 s67, s67, 39845888
Ta 8,7,49152
Mvav 40,16,200
s_lshl_b32 s69, s60, 3
Ta 10,7,50176
K 177,178,179
Mvav 40,20,188
K 178,180,181
Mvav 72,20,196
s_add_u32 s69, s69, s59
Ta 12,7,57344
K 179,182,183
Mvav 72,16,208
s_lshl_b32 s69, s69, 6
Ta 14,7,58368
v_accvgpr_write_b32 a252, v176
Mvav 104,16,216
Mvav 104,20,204
s_lshl_b32 s21, s58, 3
v_accvgpr_write_b32 a253, v177
Mvav 136,20,212
s_add_u32 s69, s69, s21
v_accvgpr_write_b32 a254, v178
Mvav 136,16,224
W 8
s_add_u32 s69, s69, 20480
v_accvgpr_write_b32 a255, v179
Mvav 140,24,224
Ta 16,8,49152
Ta 18,8,50176
Mvav 140,28,212
s_mov_b32 s70, 0
Mvav 108,28,204
s_mov_b32 s37, 0
Ta 20,8,57344
Ta 22,8,58368
Mvav 108,24,216
s_sub_u32 s18, s57, 4
Mvav 76,24,208
Mvav 76,28,196
s_lshl_b32 s18, s18, 2
Mvav 44,28,188
s_add_u32 s18, s18, s58
Mvav 44,24,200
W 8
Ta 24,9,49152
Ta 26,9,50176
Mvav 48,0,200
s_mul_i32 s85, s59, 7
Mvav 48,4,188
s_add_u32 s85, s85, s18
Mvav 80,4,196
s_cmp_lt_u32 s18, 7
s_cselect_b32 s85, s85, 99
Ta 28,9,57344
Ta 30,9,58368
Mvav 80,0,208
Mvav 112,0,216
s_cmp_lt_u32 s85, 54
s_cselect_b32 s65, 2, s65
Mvav 112,4,204
s_mov_b32 s68, 0
Mvav 144,4,212
s_mov_b64 s[24:25], -1
Mvav 144,0,224
W 8
Mvav 148,8,224
s_mov_b32 s22, 0x3e0293ee
Mvav 148,12,212
s_mov_b32 s23, 0x3e0293ee
Mvav 116,12,204
Mvav 116,8,216
Mvav 84,8,208
Mvav 84,12,196
Mvav 52,12,188
Mvav 52,8,200
W 4
Mvav 56,16,200
Mvav 56,20,188
Mvav 88,20,196
Mvav 88,16,208
Mvav 120,16,216
W 0
s_barrier
Mvav 120,20,204
Ra 128,242,0
Mvav 152,20,212
Ra 132,242,1024
Mvav 152,16,224
Mvav 156,24,224
Ra 136,242,2048
Mvav 156,28,212
Mvav 124,28,204
Ra 140,242,3072
Mvav 124,24,216
Mvav 92,24,208
Mvav 92,28,196
Mvav 60,28,188
Mvav 60,24,200
.Lago%=:
s_cmp_eq_u32 s60, 1
s_cbranch_scc1 .Lpe_48%=
s_cmp_eq_u32 s60, 0
s_cbranch_scc1 .Lpd_32%=
v_pk_mul_f32 v[32:33], v[32:33], s[22:23]
v_pk_mul_f32 v[34:35], v[34:35], s[22:23]
v_pk_mul_f32 v[36:37], v[36:37], s[22:23]
v_pk_mul_f32 v[38:39], v[38:39], s[22:23]
v_pk_mul_f32 v[64:65], v[64:65], s[22:23]
v_pk_mul_f32 v[66:67], v[66:67], s[22:23]
v_pk_mul_f32 v[68:69], v[68:69], s[22:23]
v_pk_mul_f32 v[70:71], v[70:71], s[22:23]
v_pk_mul_f32 v[96:97], v[96:97], s[22:23]
v_pk_mul_f32 v[98:99], v[98:99], s[22:23]
v_pk_mul_f32 v[100:101], v[100:101], s[22:23]
v_pk_mul_f32 v[102:103], v[102:103], s[22:23]
v_pk_mul_f32 v[128:129], v[128:129], s[22:23]
v_pk_mul_f32 v[130:131], v[130:131], s[22:23]
v_pk_mul_f32 v[132:133], v[132:133], s[22:23]
v_pk_mul_f32 v[134:135], v[134:135], s[22:23]
v_pk_mul_f32 v[40:41], v[40:41], s[22:23]
v_pk_mul_f32 v[42:43], v[42:43], s[22:23]
v_pk_mul_f32 v[44:45], v[44:45], s[22:23]
v_pk_mul_f32 v[46:47], v[46:47], s[22:23]
v_pk_mul_f32 v[72:73], v[72:73], s[22:23]
v_pk_mul_f32 v[74:75], v[74:75], s[22:23]
v_pk_mul_f32 v[76:77], v[76:77], s[22:23]
v_pk_mul_f32 v[78:79], v[78:79], s[22:23]
v_pk_mul_f32 v[104:105], v[104:105], s[22:23]
v_pk_mul_f32 v[106:107], v[106:107], s[22:23]
v_pk_mul_f32 v[108:109], v[108:109], s[22:23]
v_pk_mul_f32 v[110:111], v[110:111], s[22:23]
v_pk_mul_f32 v[136:137], v[136:137], s[22:23]
v_pk_mul_f32 v[138:139], v[138:139], s[22:23]
v_pk_mul_f32 v[140:141], v[140:141], s[22:23]
v_pk_mul_f32 v[142:143], v[142:143], s[22:23]
v_pk_mul_f32 v[48:49], v[48:49], s[22:23]
v_pk_mul_f32 v[50:51], v[50:51], s[22:23]
v_pk_mul_f32 v[52:53], v[52:53], s[22:23]
v_pk_mul_f32 v[54:55], v[54:55], s[22:23]
v_pk_mul_f32 v[80:81], v[80:81], s[22:23]
v_pk_mul_f32 v[82:83], v[82:83], s[22:23]
v_pk_mul_f32 v[84:85], v[84:85], s[22:23]
v_pk_mul_f32 v[86:87], v[86:87], s[22:23]
v_pk_mul_f32 v[112:113], v[112:113], s[22:23]
v_pk_mul_f32 v[114:115], v[114:115], s[22:23]
v_pk_mul_f32 v[116:117], v[116:117], s[22:23]
v_pk_mul_f32 v[118:119], v[118:119], s[22:23]
v_pk_mul_f32 v[144:145], v[144:145], s[22:23]
v_pk_mul_f32 v[146:147], v[146:147], s[22:23]
v_pk_mul_f32 v[148:149], v[148:149], s[22:23]
v_pk_mul_f32 v[150:151], v[150:151], s[22:23]
v_pk_mul_f32 v[56:57], v[56:57], s[22:23]
v_pk_mul_f32 v[58:59], v[58:59], s[22:23]
v_pk_mul_f32 v[60:61], v[60:61], s[22:23]
v_pk_mul_f32 v[62:63], v[62:63], s[22:23]
v_pk_mul_f32 v[88:89], v[88:89], s[22:23]
v_pk_mul_f32 v[90:91], v[90:91], s[22:23]
v_pk_mul_f32 v[92:93], v[92:93], s[22:23]
v_pk_mul_f32 v[94:95], v[94:95], s[22:23]
v_pk_mul_f32 v[120:121], v[120:121], s[22:23]
v_pk_mul_f32 v[122:123], v[122:123], s[22:23]
v_pk_mul_f32 v[124:125], v[124:125], s[22:23]
v_pk_mul_f32 v[126:127], v[126:127], s[22:23]
v_pk_mul_f32 v[152:153], v[152:153], s[22:23]
v_pk_mul_f32 v[154:155], v[154:155], s[22:23]
v_pk_mul_f32 v[156:157], v[156:157], s[22:23]
v_pk_mul_f32 v[158:159], v[158:159], s[22:23]
K 32,32,33
K 33,34,35
K 34,36,37
K 35,38,39
K 40,40,41
K 41,42,43
K 42,44,45
K 43,46,47
W 0
Zvav 196,128,32
s_add_u32 s36, s29, 65536
Ra 144,242,4096
Ra 148,242,5120
Ra 152,242,6144
Ra 156,242,7168
Mvav 196,132,40
K 64,64,65
K 65,66,67
K 66,68,69
K 67,70,71
K 72,72,73
K 73,74,75
K 74,76,77
K 75,78,79
s_nop 1
Zvav 204,132,72
buffer_store_dwordx4 a[192:195], v1, s[8:11], s36 offen offset:0 sc1
K 96,96,97
K 97,98,99
K 98,100,101
Mvav 204,128,64
K 99,102,103
K 104,104,105
K 105,106,107
K 106,108,109
K 107,110,111
Zvav 212,128,96
buffer_store_dwordx4 a[200:203], v1, s[8:11], s36 offen offset:1024 sc1
K 128,128,129
K 129,130,131
K 130,132,133
Mvav 212,132,104
K 131,134,135
K 136,136,137
K 137,138,139
K 138,140,141
K 139,142,143
s_nop 1
Zvav 220,132,136
K 48,48,49
K 49,50,51
K 50,52,53
K 51,54,55
Mvav 220,128,128
K 56,56,57
K 57,58,59
K 58,60,61
K 59,62,63
Mvav 196,136,48
buffer_store_dwordx4 a[208:211], v1, s[8:11], s36 offen offset:2048 sc1
Ra 128,242,8192
Ra 132,242,9216
K 80,80,81
Mvav 196,140,56
K 81,82,83
K 82,84,85
K 83,86,87
K 88,88,89
K 89,90,91
K 90,92,93
K 91,94,95
s_nop 1
Mvav 204,140,88
buffer_store_dwordx4 a[216:219], v1, s[8:11], s36 offen offset:3072 sc1
s_add_u32 s36, s29, 69632
K 112,112,113
K 113,114,115
K 114,116,117
Mvav 204,136,80
K 115,118,119
K 120,120,121
K 121,122,123
K 122,124,125
K 123,126,127
Mvav 212,136,112
K 144,144,145
K 145,146,147
K 146,148,149
K 147,150,151
Mvav 212,140,120
K 152,152,153
K 153,154,155
K 154,156,157
K 155,158,159
s_nop 1
Mvav 220,140,152
buffer_store_dwordx4 a[224:227], v1, s[8:11], s36 offen offset:0 sc1
v_accvgpr_write_b32 a0, v32
v_accvgpr_write_b32 a1, v33
v_accvgpr_write_b32 a2, v34
Mvav 220,136,144
W 2
Ra 136,242,10240
v_accvgpr_write_b32 a3, v35
v_accvgpr_write_b32 a4, v40
Zvav 36,144,32
Ra 140,242,11264
buffer_store_dwordx4 a[232:235], v1, s[8:11], s36 offen offset:1024 sc1
v_accvgpr_write_b32 a5, v41
Mvav 36,148,40
v_accvgpr_write_b32 a6, v42
v_accvgpr_write_b32 a7, v43
v_max3_f32 v4, v196, v197, v198
v_max3_f32 v4, v4, v199, v252
Zvav 68,148,72
v_max3_f32 v5, v204, v205, v206
v_max3_f32 v5, v5, v207, v253
v_max3_f32 v6, v212, v213, v214
v_max3_f32 v6, v6, v215, v254
Mvav 68,144,64
v_max3_f32 v7, v220, v221, v222
v_max3_f32 v7, v7, v223, v255
v_mov_b32 v8, v4
v_mov_b32 v9, v5
Zvav 100,144,96
v_mov_b32 v10, v6
v_mov_b32 v11, v7
v_permlane32_swap_b32 v4, v8
v_accvgpr_write_b32 a8, v48
Mvav 100,148,104
v_permlane32_swap_b32 v5, v9
v_permlane32_swap_b32 v6, v10
v_permlane32_swap_b32 v7, v11
v_max_f32 v4, v4, v8
Zvav 132,148,136
v_max_f32 v5, v5, v9
v_max_f32 v6, v6, v10
v_max_f32 v7, v7, v11
v_mov_b32 v8, v4
Mvav 132,144,128
v_mov_b32 v9, v5
v_mov_b32 v10, v6
v_mov_b32 v11, v7
v_accvgpr_write_b32 a9, v49
Mvav 36,152,48
v_permlane16_swap_b32 v4, v8
v_permlane16_swap_b32 v5, v9
v_permlane16_swap_b32 v6, v10
v_permlane16_swap_b32 v7, v11
Mvav 36,156,56
v_max_f32 v4, v4, v8
v_max_f32 v5, v5, v9
v_max_f32 v6, v6, v10
v_max_f32 v7, v7, v11
Mvav 68,156,88
v_sub_f32 v4, 0, v4
v_sub_f32 v5, 0, v5
v_sub_f32 v6, 0, v6
v_sub_f32 v7, 0, v7
Mvav 68,152,80
v_mov_b32 v224, v4
v_mov_b32 v225, v4
v_mov_b32 v226, v4
v_mov_b32 v227, v4
Mvav 100,152,112
v_mov_b32 v228, v5
v_mov_b32 v229, v5
v_mov_b32 v230, v5
v_mov_b32 v231, v5
Mvav 100,156,120
v_mov_b32 v232, v6
v_mov_b32 v233, v6
v_mov_b32 v234, v6
v_mov_b32 v235, v6
Mvav 132,156,152
v_mov_b32 v236, v7
v_mov_b32 v237, v7
v_mov_b32 v238, v7
v_mov_b32 v239, v7
Mvav 132,152,144
W 0
)"
R"(buffer_store_dwordx4 a[240:243], v1, s[8:11], s36 offen offset:2048 sc1
v_accvgpr_write_b32 a10, v50
v_accvgpr_write_b32 a11, v51
v_accvgpr_write_b32 a12, v56
Cvav 160,128,32,224
s_and_b32 s80, s14, 1
v_accvgpr_write_b32 a13, v57
v_accvgpr_write_b32 a14, v58
v_accvgpr_write_b32 a15, v59
v_accvgpr_write_b32 a16, v64
Mvav 160,132,40
buffer_store_dwordx4 a[248:251], v1, s[8:11], s36 offen offset:3072 sc1
s_add_u32 s36, s29, 393216
Ra 144,242,12288
Ra 148,242,13312
v_accvgpr_write_b32 a17, v65
Cvav 164,132,72,228
s_lshl_b32 s80, s80, 14
v_accvgpr_write_b32 a18, v66
v_accvgpr_write_b32 a19, v67
v_accvgpr_write_b32 a20, v72
v_accvgpr_write_b32 a21, v73
Mvav 164,128,64
s_add_u32 s80, s80, 98304
buffer_store_dwordx4 a[196:199], v1, s[8:11], s36 offen offset:0 sc1
v_accvgpr_write_b32 a22, v74
v_accvgpr_write_b32 a23, v75
v_accvgpr_write_b32 a24, v80
Cvav 168,128,96,232
v_accvgpr_write_b32 a25, v81
v_accvgpr_write_b32 a26, v82
A 196,196,224
v_accvgpr_write_b32 a27, v83
Mvav 168,132,104
buffer_store_dwordx4 a[204:207], v1, s[8:11], s36 offen offset:1024 sc1
A 197,197,224
A 198,198,224
v_accvgpr_write_b32 a28, v88
Cvav 172,132,136,236
Ra 152,242,14336
Ra 156,242,15360
A 199,199,224
v_accvgpr_write_b32 a29, v89
Mvav 172,128,128
buffer_store_dwordx4 a[212:215], v1, s[8:11], s36 offen offset:2048 sc1
X 196
v_accvgpr_write_b32 a30, v90
Mvav 160,136,48
X 197
v_accvgpr_write_b32 a31, v91
v_accvgpr_write_b32 a32, v96
Mvav 160,140,56
X 198
K 192,196,197
v_accvgpr_write_b32 a33, v97
Mvav 164,140,88
X 199
A 204,204,228
v_accvgpr_write_b32 a34, v98
Mvav 164,136,80
buffer_store_dwordx4 a[220:223], v1, s[8:11], s36 offen offset:3072 sc1
s_add_u32 s36, s29, 397312
K 193,198,199
v_accvgpr_write_b32 a35, v99
A 205,205,228
Mvav 168,136,112
A 206,206,228
v_accvgpr_write_b32 a36, v104
v_add_u32 v8, s80, v1
A 207,207,228
Mvav 168,140,120
X 204
v_accvgpr_write_b32 a37, v105
v_accvgpr_write_b32 a38, v106
Mvav 172,140,152
X 205
X 206
Mvav 172,136,144
W 0
buffer_store_dwordx4 a[228:231], v1, s[8:11], s36 offen offset:0 sc1
v_accvgpr_write_b32 a39, v107
v_accvgpr_write_b32 a40, v112
K 200,204,205
Cvav 176,144,32,224
Ra 128,242,24576
X 207
v_accvgpr_write_b32 a41, v113
Mvav 176,148,40
Ra 132,242,25600
A 212,212,232
v_accvgpr_write_b32 a42, v114
K 201,206,207
Cvav 180,148,72,228
A 213,213,232
v_accvgpr_write_b32 a43, v115
A 214,214,232
A 215,215,232
Mvav 180,144,64
buffer_store_dwordx4 a[236:239], v1, s[8:11], s36 offen offset:1024 sc1
v_accvgpr_write_b32 a44, v120
X 212
Cvav 184,144,96,232
v_accvgpr_write_b32 a45, v121
X 213
v_accvgpr_write_b32 a46, v122
Mvav 184,148,104
X 214
v_accvgpr_write_b32 a47, v123
K 208,212,213
Cvav 188,148,136,236
buffer_store_dwordx4 a[244:247], v1, s[8:11], s36 offen offset:2048 sc1
v_accvgpr_write_b32 a48, v128
X 215
Mvav 188,144,128
v_accvgpr_write_b32 a49, v129
A 220,220,236
v_accvgpr_write_b32 a50, v130
K 209,214,215
Mvav 176,152,48
A 221,221,236
v_accvgpr_write_b32 a51, v131
A 222,222,236
A 223,223,236
Mvav 176,156,56
Ra 136,242,26624
v_accvgpr_write_b32 a52, v136
X 220
Mvav 180,156,88
Ra 140,242,27648
v_accvgpr_write_b32 a53, v137
X 221
Mvav 180,152,80
buffer_store_dwordx4 a[252:255], v1, s[8:11], s36 offen offset:3072 sc1
v_accvgpr_write_b32 a54, v138
X 222
Mvav 184,152,112
v_accvgpr_write_b32 a55, v139
v_accvgpr_write_b32 a56, v144
v_accvgpr_write_b32 a57, v145
v_accvgpr_write_b32 a58, v146
v_accvgpr_write_b32 a59, v147
v_accvgpr_write_b32 a60, v152
v_accvgpr_write_b32 a61, v153
v_accvgpr_write_b32 a62, v154
v_accvgpr_write_b32 a63, v155
Mvav 184,156,120
s_barrier
K 216,220,221
X 223
A 36,36,224
Mvav 188,156,152
K 217,222,223
A 37,37,224
A 38,38,224
A 39,39,224
X 36
X 37
X 38
K 194,36,37
X 39
A 68,68,228
K 195,38,39
A 69,69,228
A 70,70,228
A 71,71,228
X 68
X 69
X 70
K 202,68,69
X 71
A 100,100,232
K 203,70,71
A 101,101,232
A 102,102,232
A 103,103,232
X 100
X 101
X 102
K 210,100,101
X 103
A 132,132,236
K 211,102,103
A 133,133,236
A 134,134,236
A 135,135,236
X 132
X 133
X 134
K 218,132,133
X 135
s_nop 0
K 219,134,135
Mvav 188,152,144
W 0
Ra 144,242,28672
Ra 148,242,29696
Ra 152,242,30720
Zvav 32,128,192
Ra 156,242,31744
Zvav 64,128,200
v_xor_b32 v242, 0x10000, v242
Zvav 96,128,208
s_and_b32 s80, s14, 1
s_xor_b32 s62, s62, 0x10000
Zvav 128,128,216
s_lshl_b32 s80, s80, 14
s_add_u32 s80, s80, 131072
v_add_u32 v23, 61440, v21
Zvav 132,132,216
v_add_u32 v12, s80, v1
Ra 64,12,8192
Ra 68,12,9216
v_add_u32 v24, 61440, v22
Zvav 100,132,208
Ra 72,12,10240
Ra 76,12,11264
Ra 80,12,12288
v_add_u32 v25, 122880, v21
Zvav 68,132,200
Ra 84,12,13312
Ra 88,12,14336
Ra 92,12,15360
v_add_u32 v26, 122880, v22
Zvav 36,132,192
Ra 96,12,0
Ra 100,12,1024
Ra 104,12,2048
Zvav 40,136,192
Ra 108,12,3072
Ra 112,12,4096
X 172
Zvav 72,136,200
Ra 116,12,5120
Ra 120,12,6144
X 173
Zvav 104,136,208
Ra 124,12,7168
X 174
K 220,172,173
Zvav 136,136,216
X 175
Zvav 140,140,216
X 168
K 221,174,175
Zvav 108,140,208
X 169
X 170
Zvav 76,140,200
s_waitcnt vmcnt(0) lgkmcnt(0)
s_barrier
ds_read_b128 v[248:251], v8
K 212,168,169
Zvav 44,140,192
s_add_u32 s80, s71, 128
v_mov_b64 v[28:29], s[12:13]
s_mov_b64 exec, 1
buffer_store_dwordx2 v[28:29], off, s[8:11], s80 sc1
s_mov_b64 exec, -1
X 171
X 164
Zvav 48,144,192
Ra 128,242,0
K 213,170,171
X 165
Zvav 80,144,200
X 166
Zvav 112,144,208
Ra 132,242,1024
K 204,164,165
X 167
Zvav 144,144,216
X 160
Zvav 148,148,216
K 205,166,167
X 161
Zvav 116,148,208
Ra 136,242,2048
X 162
Zvav 84,148,200
W 3
K 196,160,161
X 163
Zvav 52,148,192
X 176
K 197,162,163
Zvav 56,152,192
Ra 140,242,3072
X 177
Zvav 88,152,200
X 178
K 198,176,177
Zvav 120,152,208
X 179
X 180
Zvav 152,152,216
K 199,178,179
Zvav 156,156,216
X 181
X 182
Zvav 124,156,208
K 206,180,181
X 183
Zvav 92,156,200
K 207,182,183
Zvav 60,156,192
.Ll0t_46%=:
s_cmp_eq_u32 s64, s65
s_cbranch_scc1 .Ll1t_35%=
.Ll0_33%=:
W 0
Cvaa 160,128,0,224
Ra 144,242,4096
Ra 148,242,5120
Mvaa 160,132,4
s_xor_b32 s62, s62, 0x10000
Ra 152,242,6144
Cvaa 164,132,20,228
X 184
Mvaa 164,128,16
X 185
Cvaa 168,128,32,232
X 186
Mvaa 168,132,36
Ra 156,242,7168
K 214,184,185
Cvaa 172,132,52,236
X 187
Mvaa 172,128,48
X 188
Mvaa 160,136,8
Ra 128,242,16384
Mvaa 160,140,12
K 215,186,187
Mvaa 164,140,28
X 189
Mvaa 164,136,24
X 190
Mvaa 168,136,40
Ra 132,242,17408
Mvaa 168,140,44
K 222,188,189
Mvaa 172,140,60
X 191
Mvaa 172,136,56
K 223,190,191
v_smfmac_f32_16x16x64_bf16 a[160:163], a[176:179], v[192:199], v240
Ra 136,242,18432
v_smfmac_f32_16x16x64_bf16 a[164:167], a[176:179], v[200:207], v240
X 160
v_smfmac_f32_16x16x64_bf16 a[168:171], a[176:179], v[208:215], v240
X 161
v_smfmac_f32_16x16x64_bf16 a[172:175], a[176:179], v[216:223], v240
W 3
X 162
Cvaa 176,144,0,224
Ra 140,242,19456
Mvaa 176,148,4
K 192,160,161
Cvaa 180,148,20,228
X 163
Mvaa 180,144,16
X 164
Cvaa 184,144,32,232
Mvaa 184,148,36
K 193,162,163
Cvaa 188,148,52,236
X 165
Mvaa 188,144,48
X 166
Mvaa 176,152,8
Ra 144,242,20480
Ra 148,242,21504
Mvaa 176,156,12
K 200,164,165
Mvaa 180,156,28
X 167
Mvaa 180,152,24
X 168
Mvaa 184,152,40
Mvaa 184,156,44
K 201,166,167
Mvaa 188,156,60
X 169
Mvaa 188,152,56
W 2
Ra 152,242,22528
Mvav 32,128,196
X 170
Mvav 64,128,204
Ra 156,242,23552
K 208,168,169
Mvav 96,128,212
X 171
Mvav 128,128,220
Mvav 132,132,220
X 172
Mvav 100,132,212
s_add_u32 s64, s64, 1
Ra 128,242,8192
K 209,170,171
)"
R"(Mvav 68,132,204
X 173
Mvav 36,132,196
Mvav 40,136,196
X 174
Mvav 72,136,204
Ra 132,242,9216
K 216,172,173
Mvav 104,136,212
X 175
Mvav 136,136,220
Mvav 140,140,220
X 176
Mvav 108,140,212
Ra 136,242,10240
K 217,174,175
Mvav 76,140,204
X 177
Mvav 44,140,196
W 3
Mvav 48,144,196
X 178
Mvav 80,144,204
Ra 140,242,11264
Mvav 112,144,212
K 194,176,177
Mvav 144,144,220
X 179
Mvav 148,148,220
X 180
Mvav 116,148,212
Ra 144,242,12288
Mvav 84,148,204
K 195,178,179
Mvav 52,148,196
X 181
Mvav 56,152,196
X 182
Mvav 88,152,204
Ra 148,242,13312
Mvav 120,152,212
K 202,180,181
Mvav 152,152,220
X 183
Mvav 156,156,220
X 184
Mvav 124,156,212
Ra 152,242,14336
Mvav 92,156,204
K 203,182,183
Mvav 60,156,196
W 3
X 185
Cvaa 160,128,0,224
X 186
Mvaa 160,132,4
Ra 156,242,15360
Cvaa 164,132,20,228
K 210,184,185
Mvaa 164,128,16
X 187
Cvaa 168,128,32,232
X 188
Mvaa 168,132,36
Cvaa 172,132,52,236
K 211,186,187
Mvaa 172,128,48
X 189
Mvaa 160,136,8
Ra 128,242,24576
Ra 132,242,25600
Mvaa 160,140,12
X 190
Mvaa 164,140,28
K 218,188,189
Mvaa 164,136,24
X 191
Mvaa 168,136,40
Mvaa 168,140,44
Mvaa 172,140,60
X 160
Mvaa 172,136,56
W 2
Ra 136,242,26624
K 219,190,191
Cvaa 176,144,0,224
X 161
Mvaa 176,148,4
Ra 140,242,27648
Cvaa 180,148,20,228
X 162
Mvaa 180,144,16
K 196,160,161
Cvaa 184,144,32,232
X 163
Mvaa 184,148,36
X 164
Cvaa 188,148,52,236
K 197,162,163
Mvaa 188,144,48
X 165
Mvaa 176,152,8
Ra 144,242,28672
Ra 148,242,29696
Mvaa 176,156,12
X 166
Mvaa 180,156,28
K 204,164,165
Mvaa 180,152,24
X 167
Mvaa 184,152,40
Mvaa 184,156,44
X 168
Mvaa 188,156,60
Mvaa 188,152,56
W 2
Ra 152,242,30720
K 205,166,167
Mvav 32,128,192
X 169
Mvav 64,128,200
Ra 156,242,31744
v_xor_b32 v242, 0x10000, v242
Mvav 96,128,208
X 170
Mvav 128,128,216
K 212,168,169
Mvav 132,132,216
X 171
Mvav 100,132,208
X 172
Mvav 68,132,200
Mvav 36,132,192
K 213,170,171
Mvav 40,136,192
X 173
Mvav 72,136,200
X 174
Mvav 104,136,208
Mvav 136,136,216
K 220,172,173
Mvav 140,140,216
X 175
Mvav 108,140,208
X 176
Mvav 76,140,200
W 0
s_barrier
Mvav 44,140,192
Ra 128,242,0
K 221,174,175
Mvav 48,144,192
X 177
Mvav 80,144,200
X 178
Mvav 112,144,208
Ra 132,242,1024
Mvav 144,144,216
K 198,176,177
Mvav 148,148,216
X 179
Mvav 116,148,208
X 180
Mvav 84,148,200
Ra 136,242,2048
Mvav 52,148,192
K 199,178,179
Mvav 56,152,192
X 181
Mvav 88,152,200
Ra 140,242,3072
Mvav 120,152,208
X 182
Mvav 152,152,216
K 206,180,181
Mvav 156,156,216
X 183
Mvav 124,156,208
Mvav 92,156,200
K 207,182,183
Mvav 60,156,192
s_cmp_lg_u32 s64, s65
s_cbranch_scc1 .Ll0_33%=
s_cmp_lt_u32 s85, 54
s_cbranch_scc1 .Lmoe_44%=
.Ll1t_35%=:
s_cmp_eq_u32 s64, s66
s_cbranch_scc1 .Lsp1_36%=
.Ll1_34%=:
W 0
Cvaa 160,128,0,224
Ra 144,242,4096
Ra 148,242,5120
Mvaa 160,132,4
s_add_u32 m0, s62, 0
Ra 152,242,6144
Cvaa 164,132,20,228
X 184
Mvaa 164,128,16
X 185
Cvaa 168,128,32,232
X 186
Mvaa 168,132,36
Ra 156,242,7168
K 214,184,185
Cvaa 172,132,52,236
Dc 1,8,61,0
Mvaa 172,128,48
X 187
Mvaa 160,136,8
X 188
Mvaa 160,140,12
Ra 128,242,16384
K 215,186,187
Mvaa 164,140,28
Dc 1,8,61,1024
Mvaa 164,136,24
X 189
Mvaa 168,136,40
X 190
Mvaa 168,140,44
Ra 132,242,17408
K 222,188,189
Mvaa 172,140,60
Dc 1,8,61,2048
Mvaa 172,136,56
X 191
v_smfmac_f32_16x16x64_bf16 a[160:163], a[176:179], v[192:199], v240
Ra 136,242,18432
K 223,190,191
v_smfmac_f32_16x16x64_bf16 a[164:167], a[176:179], v[200:207], v240
X 160
v_smfmac_f32_16x16x64_bf16 a[168:171], a[176:179], v[208:215], v240
Dc 1,8,61,3072
v_smfmac_f32_16x16x64_bf16 a[172:175], a[176:179], v[216:223], v240
W 3
X 161
Cvaa 176,144,0,224
s_add_u32 m0, s62, 4096
X 162
Mvaa 176,148,4
Ra 140,242,19456
K 192,160,161
Cvaa 180,148,20,228
X 163
Mvaa 180,144,16
Dc 243,8,61,0
Cvaa 184,144,32,232
X 164
Mvaa 184,148,36
K 193,162,163
Cvaa 188,148,52,236
X 165
Mvaa 188,144,48
Dc 243,8,61,1024
Ra 144,242,20480
Mvaa 176,152,8
X 166
Mvaa 176,156,12
Ra 148,242,21504
K 200,164,165
Mvaa 180,156,28
X 167
Mvaa 180,152,24
Dc 243,8,61,2048
Mvaa 184,152,40
X 168
Mvaa 184,156,44
K 201,166,167
Mvaa 188,156,60
X 169
Mvaa 188,152,56
W 2
Dc 243,8,61,3072
Ra 152,242,22528
Mvav 32,128,196
s_add_u32 m0, s62, 8192
X 170
Mvav 64,128,204
Ra 156,242,23552
K 208,168,169
Mvav 96,128,212
X 171
Mvav 128,128,220
Dc 244,8,61,0
Mvav 132,132,220
X 172
Mvav 100,132,212
Ra 128,242,8192
K 209,170,171
Mvav 68,132,204
X 173
Mvav 36,132,196
Dc 244,8,61,1024
Mvav 40,136,196
X 174
Mvav 72,136,204
Ra 132,242,9216
K 216,172,173
Mvav 104,136,212
X 175
Mvav 136,136,220
Dc 244,8,61,2048
Mvav 140,140,220
X 176
Mvav 108,140,212
Ra 136,242,10240
K 217,174,175
Mvav 76,140,204
X 177
Mvav 44,140,196
W 3
Dc 244,8,61,3072
Mvav 48,144,196
X 178
Mvav 80,144,204
s_add_u32 m0, s62, 12288
Ra 140,242,11264
Mvav 112,144,212
K 194,176,177
Mvav 144,144,220
X 179
Mvav 148,148,220
X 180
Mvav 116,148,212
Dc 245,8,61,0
Ra 144,242,12288
Mvav 84,148,204
K 195,178,179
Mvav 52,148,196
X 181
Mvav 56,152,196
X 182
Mvav 88,152,204
Dc 245,8,61,1024
Ra 148,242,13312
Mvav 120,152,212
K 202,180,181
Mvav 152,152,220
X 183
Mvav 156,156,220
X 184
Mvav 124,156,212
Dc 245,8,61,2048
Ra 152,242,14336
Mvav 92,156,204
K 203,182,183
Mvav 60,156,196
W 3
X 185
Cvaa 160,128,0,224
X 186
Mvaa 160,132,4
Dc 245,8,61,3072
Ra 156,242,15360
Cvaa 164,132,20,228
s_add_u32 s61, s61, s63
K 210,184,185
Mvaa 164,128,16
X 187
Cvaa 168,128,32,232
X 188
Mvaa 168,132,36
Cvaa 172,132,52,236
s_xor_b32 s62, s62, 0x10000
K 211,186,187
Mvaa 172,128,48
X 189
Mvaa 160,136,8
Ra 128,242,24576
Ra 132,242,25600
Mvaa 160,140,12
s_add_u32 s64, s64, 1
X 190
Mvaa 164,140,28
K 218,188,189
Mvaa 164,136,24
X 191
Mvaa 168,136,40
Mvaa 168,140,44
X 160
Mvaa 172,140,60
K 219,190,191
Mvaa 172,136,56
W 2
X 161
Cvaa 176,144,0,224
Ra 136,242,26624
Ra 140,242,27648
Mvaa 176,148,4
X 162
Cvaa 180,148,20,228
K 196,160,161
Mvaa 180,144,16
X 163
Cvaa 184,144,32,232
Mvaa 184,148,36
X 164
Cvaa 188,148,52,236
K 197,162,163
)"
R"(Mvaa 188,144,48
X 165
Mvaa 176,152,8
Ra 144,242,28672
Ra 148,242,29696
Mvaa 176,156,12
X 166
Mvaa 180,156,28
K 204,164,165
Mvaa 180,152,24
X 167
Mvaa 184,152,40
Mvaa 184,156,44
X 168
Mvaa 188,156,60
Mvaa 188,152,56
W 2
Ra 152,242,30720
K 205,166,167
Mvav 32,128,192
X 169
Mvav 64,128,200
Ra 156,242,31744
v_xor_b32 v242, 0x10000, v242
Mvav 96,128,208
X 170
Mvav 128,128,216
K 212,168,169
Mvav 132,132,216
X 171
Mvav 100,132,208
X 172
Mvav 68,132,200
Mvav 36,132,192
K 213,170,171
Mvav 40,136,192
X 173
Mvav 72,136,200
X 174
Mvav 104,136,208
Mvav 136,136,216
K 220,172,173
Mvav 140,140,216
X 175
Mvav 108,140,208
X 176
Mvav 76,140,200
s_waitcnt vmcnt(0) lgkmcnt(0)
s_barrier
Mvav 44,140,192
Ra 128,242,0
K 221,174,175
Mvav 48,144,192
X 177
Mvav 80,144,200
X 178
Mvav 112,144,208
Ra 132,242,1024
Mvav 144,144,216
K 198,176,177
Mvav 148,148,216
X 179
Mvav 116,148,208
X 180
Mvav 84,148,200
Ra 136,242,2048
Mvav 52,148,192
K 199,178,179
Mvav 56,152,192
X 181
Mvav 88,152,200
Ra 140,242,3072
Mvav 120,152,208
X 182
Mvav 152,152,216
K 206,180,181
Mvav 156,156,216
X 183
Mvav 124,156,208
Mvav 92,156,200
K 207,182,183
Mvav 60,156,192
s_cmp_lg_u32 s64, s66
s_cbranch_scc1 .Ll1_34%=
s_cmp_ge_u32 s14, 2
s_cbranch_scc1 .Lf1r_47%=
.Lsp1_36%=:
W 0
Cvaa 160,128,0,224
Ra 144,242,4096
Ra 148,242,5120
Mvaa 160,132,4
s_add_u32 m0, s62, 0
Ra 152,242,6144
Cvaa 164,132,20,228
X 184
Mvaa 164,128,16
X 185
Cvaa 168,128,32,232
X 186
Mvaa 168,132,36
Ra 156,242,7168
K 214,184,185
Cvaa 172,132,52,236
Dc 1,8,61,0
Mvaa 172,128,48
X 187
Mvaa 160,136,8
X 188
Mvaa 160,140,12
Ra 128,242,16384
K 215,186,187
Mvaa 164,140,28
Dc 1,8,61,1024
Ra 132,242,17408
Mvaa 164,136,24
X 189
Mvaa 168,136,40
X 190
Mvaa 168,140,44
Dc 1,8,61,2048
K 222,188,189
Mvaa 172,140,60
X 191
Mvaa 172,136,56
Ra 136,242,18432
K 223,190,191
v_smfmac_f32_16x16x64_bf16 a[160:163], a[176:179], v[192:199], v240
Dc 1,8,61,3072
Ra 140,242,19456
v_smfmac_f32_16x16x64_bf16 a[164:167], a[176:179], v[200:207], v240
s_add_u32 m0, s62, 4096
v_cndmask_b32_e64 v160, v241, v160, s[72:73]
v_cndmask_b32_e64 v161, v241, v161, s[74:75]
v_smfmac_f32_16x16x64_bf16 a[168:171], a[176:179], v[208:215], v240
v_cndmask_b32_e64 v162, v241, v162, s[76:77]
v_cndmask_b32_e64 v163, v241, v163, s[78:79]
v_smfmac_f32_16x16x64_bf16 a[172:175], a[176:179], v[216:223], v240
W 4
X 160
Cvaa 180,144,16,228
X 161
Mvaa 180,148,20
Dc 243,8,61,0
Cvaa 184,148,36,232
X 162
Mvaa 184,144,32
K 192,160,161
Cvaa 188,144,48,236
X 163
Mvaa 188,148,52
Dc 243,8,61,1024
Ra 144,242,20480
Mvaa 180,152,24
X 164
Mvaa 180,156,28
Ra 148,242,21504
K 193,162,163
Mvaa 184,156,44
Dc 243,8,61,2048
Mvaa 184,152,40
X 165
Mvaa 188,152,56
X 166
Mvaa 188,156,60
W 2
Dc 243,8,61,3072
K 200,164,165
Mvav 32,128,196
s_add_u32 m0, s62, 8192
X 167
Mvav 64,128,204
Ra 152,242,22528
Ra 156,242,23552
Mvav 96,128,212
X 168
Mvav 128,128,220
Dc 244,8,61,0
K 201,166,167
Mvav 132,132,220
X 169
Mvav 100,132,212
X 170
Mvav 68,132,204
Ra 128,242,8192
K 208,168,169
Mvav 36,132,196
X 171
Mvav 40,136,196
Dc 244,8,61,1024
Ra 132,242,9216
Mvav 72,136,204
X 172
Mvav 104,136,212
K 209,170,171
Mvav 136,136,220
X 173
Mvav 140,140,220
Dc 244,8,61,2048
Ra 136,242,10240
Mvav 108,140,212
X 174
Mvav 76,140,204
K 216,172,173
Mvav 44,140,196
W 3
Dc 244,8,61,3072
Ra 140,242,11264
Mvav 48,144,196
s_add_u32 m0, s62, 12288
X 175
Mvav 80,144,204
v_mov_b32 v194, 0
K 217,174,175
Mvav 112,144,212
v_mov_b32 v195, 0
v_cndmask_b32_e64 v180, v241, v180, s[72:73]
Mvav 144,144,220
v_cndmask_b32_e64 v181, v241, v181, s[74:75]
v_cndmask_b32_e64 v182, v241, v182, s[76:77]
Mvav 148,148,220
Dc 245,8,61,0
v_cndmask_b32_e64 v183, v241, v183, s[78:79]
Mvav 116,148,212
X 180
Mvav 84,148,204
Ra 144,242,12288
Mvav 52,148,196
X 181
Mvav 56,152,196
Dc 245,8,61,1024
Ra 148,242,13312
Mvav 88,152,204
X 182
Mvav 120,152,212
K 202,180,181
Mvav 152,152,220
X 183
Mvav 156,156,220
Dc 245,8,61,2048
Ra 152,242,14336
Mvav 124,156,212
X 184
Mvav 92,156,204
K 203,182,183
Mvav 60,156,196
W 3
Dc 245,8,61,3072
Ra 156,242,15360
Cvaa 168,128,32,232
s_add_u32 s61, s61, s63
X 185
Mvaa 168,132,36
s_xor_b32 s62, s62, 0x10000
X 186
Cvaa 172,132,52,236
K 210,184,185
Mvaa 172,128,48
s_add_u32 s64, s64, 1
X 187
Mvaa 168,136,40
X 188
Mvaa 168,140,44
Ra 128,242,24576
K 211,186,187
Mvaa 172,140,60
Ra 132,242,25600
Mvaa 172,136,56
Ra 136,242,26624
Ra 140,242,27648
X 189
X 190
K 218,188,189
X 191
v_mov_b32 v196, 0
K 219,190,191
W 4
Cvaa 188,144,48,236
Ra 144,242,28672
Mvaa 188,148,52
v_mov_b32 v197, 0
v_mov_b32 v204, 0
Mvaa 188,152,56
Ra 148,242,29696
v_mov_b32 v205, 0
Mvaa 188,156,60
W 2
Ra 152,242,30720
v_cndmask_b32_e64 v168, v241, v168, s[72:73]
Mvav 32,128,192
v_cndmask_b32_e64 v169, v241, v169, s[74:75]
v_cndmask_b32_e64 v170, v241, v170, s[76:77]
Mvav 64,128,200
Ra 156,242,31744
v_cndmask_b32_e64 v171, v241, v171, s[78:79]
Mvav 96,128,208
v_xor_b32 v242, 0x10000, v242
Mvav 128,128,216
X 168
Mvav 132,132,216
X 169
Mvav 100,132,208
X 170
Mvav 68,132,200
v_mov_b32 v224, v250
K 212,168,169
Mvav 36,132,192
v_mov_b32 v225, v250
v_mov_b32 v226, v250
Mvav 40,136,192
X 171
Mvav 72,136,200
X 172
Mvav 104,136,208
v_mov_b32 v227, v250
K 213,170,171
Mvav 136,136,216
X 173
Mvav 140,140,216
v_mov_b32 v228, v251
v_mov_b32 v229, v251
Mvav 108,140,208
X 174
Mvav 76,140,200
K 220,172,173
v_mov_b32 v230, v251
Mvav 44,140,192
W 0
X 175
Mvav 48,144,192
v_mov_b32 v231, v251
v_mov_b32 v198, 0
Mvav 80,144,200
K 221,174,175
v_mov_b32 v232, v248
Mvav 112,144,208
v_mov_b32 v199, 0
v_mov_b32 v206, 0
Mvav 144,144,216
v_mov_b32 v233, v248
v_mov_b32 v207, 0
Mvav 148,148,216
v_mov_b32 v214, 0
v_mov_b32 v234, v248
Mvav 116,148,208
v_mov_b32 v215, 0
v_cndmask_b32_e64 v188, v241, v188, s[72:73]
Mvav 84,148,200
v_mov_b32 v235, v248
v_cndmask_b32_e64 v189, v241, v189, s[74:75]
Mvav 52,148,192
v_cndmask_b32_e64 v190, v241, v190, s[76:77]
v_mov_b32 v236, v249
Mvav 56,152,192
v_cndmask_b32_e64 v191, v241, v191, s[78:79]
v_mov_b32 v237, v249
Mvav 88,152,200
X 188
Mvav 120,152,208
X 189
Mvav 152,152,216
v_mov_b32 v238, v249
v_mov_b32 v239, v249
Mvav 156,156,216
X 190
Mvav 124,156,208
s_waitcnt vmcnt(0)
s_barrier
Mvav 92,156,200
Ra 128,242,40960
K 222,188,189
Mvav 60,156,192
Ra 132,242,41984
Ra 136,242,43008
Ra 140,242,44032
X 191
s_nop 0
K 223,190,191
s_cmp_eq_u32 s64, 15
s_cbranch_scc1 .Lfs_42%=
.Lsp2_37%=:
v_smfmac_f32_16x16x64_bf16 a[160:163], a[176:179], v[192:199], v240
s_add_u32 m0, s62, 0
Ra 144,242,45056
Ra 148,242,46080
Ra 152,242,47104
Ra 156,242,48128
v_smfmac_f32_16x16x64_bf16 a[164:167], a[176:179], v[200:207], v240
v_smfmac_f32_16x16x64_bf16 a[168:171], a[176:179], v[208:215], v240
v_smfmac_f32_16x16x64_bf16 a[172:175], a[176:179], v[216:223], v240
W 4
Dc 1,8,61,0
Cvaa 168,128,96,232
v_mov_b32 v192, 0
G 12,160
Mvaa 168,132,100
Dc 1,8,61,1024
G 4,161
A 12,12,4
v_cmp_nlt_f32_e64 s[86:87], v12, s90
)"
R"(v_mov_b32 v193, 0
Cvaa 172,132,116,236
v_rcp_f32 v12, v12
s_or_b64 s[88:89], s[88:89], s[86:87]
v_accvgpr_write_b32 a160, 0
v_mov_b32 v200, 0
v_accvgpr_write_b32 a161, 0
v_accvgpr_write_b32 a162, 0
Mvaa 172,128,112
Dc 1,8,61,2048
v_accvgpr_write_b32 a163, 0
G 13,164
v_mov_b32 v201, 0
G 5,165
A 13,13,5
Mvaa 168,136,104
Ra 128,242,16384
Ra 132,242,17408
v_cmp_nlt_f32_e64 s[86:87], v13, s90
v_rcp_f32 v13, v13
s_or_b64 s[88:89], s[88:89], s[86:87]
v_accvgpr_write_b32 a164, 0
Mvaa 168,140,108
Dc 1,8,61,3072
v_accvgpr_write_b32 a165, 0
v_accvgpr_write_b32 a166, 0
v_accvgpr_write_b32 a167, 0
U 32,32,12
U 33,33,12
Mvaa 172,140,124
U 34,34,12
U 35,35,12
U 36,36,12
U 37,37,12
U 38,38,12
U 39,39,12
Mvaa 172,136,120
s_add_u32 m0, s62, 4096
Ra 136,242,18432
Ra 140,242,19456
K 32,32,33
K 33,34,35
K 34,36,37
W 4
Cvaa 188,144,112,236
K 35,38,39
s_add_u32 s93, s67, 0
buffer_store_dwordx4 v[32:35], v1, s[8:11], s93 offen offset:0 sc1
U 40,40,12
U 41,41,12
U 42,42,12
U 43,43,12
Mvaa 188,148,116
Ra 144,242,20480
v_cndmask_b32_e64 v168, v241, v168, s[72:73]
v_cndmask_b32_e64 v169, v241, v169, s[74:75]
v_cndmask_b32_e64 v170, v241, v170, s[76:77]
v_cndmask_b32_e64 v171, v241, v171, s[78:79]
U 44,44,12
Mvaa 188,152,120
X 168
X 169
U 45,45,12
U 46,46,12
Mvaa 188,156,124
W 1
Ra 148,242,21504
Dc 243,8,61,0
U 47,47,12
K 40,40,41
K 41,42,43
K 42,44,45
Mvav 96,128,212
Ra 152,242,22528
Ra 156,242,23552
K 43,46,47
X 170
K 208,168,169
Mvav 128,128,220
buffer_store_dwordx4 v[40:43], v1, s[8:11], s93 offen offset:1024 sc1
U 48,48,12
U 49,49,12
U 50,50,12
U 51,51,12
U 52,52,12
Mvav 132,132,220
U 53,53,12
U 54,54,12
U 55,55,12
K 48,48,49
K 49,50,51
K 50,52,53
Mvav 100,132,212
Dc 243,8,61,1024
Ra 128,242,32768
Ra 132,242,33792
X 171
K 51,54,55
Mvav 104,136,212
X 172
K 209,170,171
X 173
Mvav 136,136,220
buffer_store_dwordx4 v[48:51], v1, s[8:11], s93 offen offset:2048 sc1
U 56,56,12
U 57,57,12
U 58,58,12
U 59,59,12
U 60,60,12
Mvav 140,140,220
U 61,61,12
U 62,62,12
U 63,63,12
K 56,56,57
K 57,58,59
K 58,60,61
Mvav 108,140,212
W 2
Dc 243,8,61,2048
Ra 136,242,34816
Ra 140,242,35840
K 59,62,63
X 174
Mvav 112,144,212
K 216,172,173
X 175
Mvav 144,144,220
buffer_store_dwordx4 v[56:59], v1, s[8:11], s93 offen offset:3072 sc1
Ra 144,242,36864
G 14,168
G 6,169
A 14,14,6
v_cmp_nlt_f32_e64 s[86:87], v14, s90
Mvav 148,148,220
v_rcp_f32 v14, v14
s_or_b64 s[88:89], s[88:89], s[86:87]
v_accvgpr_write_b32 a168, 0
v_accvgpr_write_b32 a169, 0
v_accvgpr_write_b32 a170, 0
v_accvgpr_write_b32 a171, 0
Mvav 116,148,212
Dc 243,8,61,3072
s_add_u32 m0, s62, 8192
G 15,172
G 7,173
A 15,15,7
v_cmp_nlt_f32_e64 s[86:87], v15, s90
v_mov_b32 v194, 0
Mvav 120,152,212
v_rcp_f32 v15, v15
s_or_b64 s[88:89], s[88:89], s[86:87]
v_accvgpr_write_b32 a172, 0
v_accvgpr_write_b32 a173, 0
v_accvgpr_write_b32 a174, 0
v_accvgpr_write_b32 a175, 0
Mvav 152,152,220
Dc 244,8,61,0
U 64,64,13
U 65,65,13
U 66,66,13
U 67,67,13
U 68,68,13
Mvav 156,156,220
U 69,69,13
U 70,70,13
U 71,71,13
K 64,64,65
K 65,66,67
K 66,68,69
Mvav 124,156,212
W 1
Dc 244,8,61,1024
Ra 148,242,37888
K 67,70,71
s_add_u32 s93, s67, 131072
K 217,174,175
v_mov_b32 v195, 0
Cvaa 160,128,64,224
Ra 152,242,38912
Ra 156,242,39936
v_mov_b32 v202, 0
v_mov_b32 v203, 0
v_mov_b32 v210, 0
v_mov_b32 v211, 0
Mvaa 160,132,68
buffer_store_dwordx4 v[64:67], v1, s[8:11], s93 offen offset:0 sc1
U 72,72,13
U 73,73,13
U 74,74,13
U 75,75,13
U 76,76,13
Cvaa 164,132,84,228
U 77,77,13
U 78,78,13
U 79,79,13
K 72,72,73
K 73,74,75
K 74,76,77
Mvaa 164,128,80
Dc 244,8,61,2048
K 75,78,79
v_cndmask_b32_e64 v188, v241, v188, s[72:73]
v_cndmask_b32_e64 v189, v241, v189, s[74:75]
v_cndmask_b32_e64 v190, v241, v190, s[76:77]
v_cndmask_b32_e64 v191, v241, v191, s[78:79]
Cvaa 168,128,96,232
X 188
X 189
Mvaa 168,132,100
buffer_store_dwordx4 v[72:75], v1, s[8:11], s93 offen offset:1024 sc1
U 80,80,13
U 81,81,13
U 82,82,13
U 83,83,13
U 84,84,13
Cvaa 172,132,116,236
U 85,85,13
U 86,86,13
U 87,87,13
K 80,80,81
K 81,82,83
K 82,84,85
Mvaa 172,128,112
Dc 244,8,61,3072
s_add_u32 m0, s62, 12288
Ra 128,242,49152
Ra 132,242,50176
K 83,86,87
X 190
Mvaa 160,136,72
K 218,188,189
X 191
Mvaa 160,140,76
buffer_store_dwordx4 v[80:83], v1, s[8:11], s93 offen offset:2048 sc1
U 88,88,13
U 89,89,13
U 90,90,13
U 91,91,13
U 92,92,13
Mvaa 164,140,92
U 93,93,13
U 94,94,13
U 95,95,13
K 88,88,89
K 89,90,91
K 90,92,93
Mvaa 164,136,88
K 91,94,95
buffer_store_dwordx4 v[88:91], v1, s[8:11], s93 offen offset:3072 sc1
U 96,96,14
U 97,97,14
U 98,98,14
U 99,99,14
Mvaa 168,136,104
U 100,100,14
U 101,101,14
U 102,102,14
U 103,103,14
K 96,96,97
K 97,98,99
Mvaa 168,140,108
Dc 245,8,61,0
K 98,100,101
K 99,102,103
s_add_u32 s93, s67, 262144
v_cndmask_b32_e64 v160, v241, v160, s[72:73]
K 219,190,191
v_cndmask_b32_e64 v161, v241, v161, s[74:75]
Mvaa 172,140,124
v_cndmask_b32_e64 v162, v241, v162, s[76:77]
v_cndmask_b32_e64 v163, v241, v163, s[78:79]
X 160
Mvaa 172,136,120
W 2
Dc 245,8,61,1024
Ra 136,242,51200
Ra 140,242,52224
X 161
Cvaa 180,144,80,228
Mvaa 180,148,84
buffer_store_dwordx4 v[96:99], v1, s[8:11], s93 offen offset:0 sc1
U 104,104,14
U 105,105,14
U 106,106,14
U 107,107,14
U 108,108,14
Cvaa 184,148,100,232
U 109,109,14
U 110,110,14
U 111,111,14
K 104,104,105
K 105,106,107
K 106,108,109
Mvaa 184,144,96
Dc 245,8,61,2048
K 107,110,111
X 162
K 196,160,161
Cvaa 188,144,112,236
Ra 144,242,53248
X 163
Mvaa 188,148,116
buffer_store_dwordx4 v[104:107], v1, s[8:11], s93 offen offset:1024 sc1
U 112,112,14
U 113,113,14
U 114,114,14
U 115,115,14
U 116,116,14
Mvaa 180,152,88
U 117,117,14
U 118,118,14
U 119,119,14
K 112,112,113
K 113,114,115
K 114,116,117
Mvaa 180,156,92
Dc 245,8,61,3072
s_add_u32 s61, s61, s63
s_xor_b32 s62, s62, 0x10000
s_add_u32 s64, s64, 1
K 115,118,119
X 164
Mvaa 184,156,108
Ra 148,242,54272
K 197,162,163
X 165
Mvaa 184,152,104
buffer_store_dwordx4 v[112:115], v1, s[8:11], s93 offen offset:2048 sc1
U 120,120,14
U 121,121,14
U 122,122,14
U 123,123,14
U 124,124,14
Mvaa 188,152,120
U 125,125,14
U 126,126,14
U 127,127,14
K 120,120,121
K 121,122,123
K 122,124,125
Mvaa 188,156,124
W 2
K 123,126,127
buffer_store_dwordx4 v[120:123], v1, s[8:11], s93 offen offset:3072 sc1
U 128,128,15
U 129,129,15
U 130,130,15
U 131,131,15
Zvav 32,128,192
U 132,132,15
U 133,133,15
U 134,134,15
U 135,135,15
K 128,128,129
K 129,130,131
Zvav 64,128,200
K 130,132,133
K 131,134,135
s_add_u32 s93, s67, 393216
buffer_store_dwordx4 v[128:131], v1, s[8:11], s93 offen offset:0 sc1
U 136,136,15
U 137,137,15
U 138,138,15
Zvav 96,128,208
U 139,139,15
U 140,140,15
U 141,141,15
U 142,142,15
U 143,143,15
K 136,136,137
Zvav 128,128,216
K 137,138,139
K 138,140,141
K 139,142,143
buffer_store_dwordx4 v[136:139], v1, s[8:11], s93 offen offset:1024 sc1
U 144,144,15
U 145,145,15
Zvav 132,132,216
U 146,146,15
U 147,147,15
U 148,148,15
U 149,149,15
U 150,150,15
U 151,151,15
Zvav 100,132,208
K 144,144,145
K 145,146,147
K 146,148,149
K 147,150,151
buffer_store_dwordx4 v[144:147], v1, s[8:11], s93 offen offset:2048 sc1
U 152,152,15
Zvav 68,132,200
U 153,153,15
U 154,154,15
U 155,155,15
U 156,156,15
U 157,157,15
U 158,158,15
Zvav 36,132,192
U 159,159,15
)"
R"(K 152,152,153
K 153,154,155
K 154,156,157
K 155,158,159
buffer_store_dwordx4 v[152:155], v1, s[8:11], s93 offen offset:3072 sc1
Zvav 40,136,192
Ra 152,242,55296
X 166
K 204,164,165
X 167
Zvav 72,136,200
Ra 156,242,56320
X 168
v_xor_b32 v242, 0x10000, v242
K 205,166,167
Zvav 104,136,208
X 169
X 170
K 212,168,169
Zvav 136,136,216
X 171
X 172
K 213,170,171
Zvav 140,140,216
X 173
Zvav 108,140,208
X 174
Zvav 76,140,200
s_waitcnt vmcnt(6) lgkmcnt(0)
s_barrier
Zvav 44,140,192
Ra 128,242,40960
K 220,172,173
X 175
Zvav 48,144,192
Zvav 80,144,200
v_mov_b32 v198, 0
Zvav 112,144,208
Ra 132,242,41984
K 221,174,175
Zvav 144,144,216
v_mov_b32 v199, 0
v_cndmask_b32_e64 v180, v241, v180, s[72:73]
Zvav 148,148,216
v_cndmask_b32_e64 v181, v241, v181, s[74:75]
Zvav 116,148,208
Ra 136,242,43008
v_cndmask_b32_e64 v182, v241, v182, s[76:77]
Zvav 84,148,200
v_cndmask_b32_e64 v183, v241, v183, s[78:79]
Zvav 52,148,192
X 180
Zvav 56,152,192
Ra 140,242,44032
X 181
Zvav 88,152,200
Zvav 120,152,208
X 182
Zvav 152,152,216
K 206,180,181
Zvav 156,156,216
X 183
Zvav 124,156,208
Zvav 92,156,200
s_waitcnt vmcnt(0)
s_and_b64 s[86:87], s[88:89], s[24:25]
s_cbranch_scc1 .LL_49%=
v_mov_b64 v[28:29], s[12:13]
s_mov_b64 exec, 1
buffer_store_dwordx2 v[28:29], off, s[8:11], s69 sc1
s_mov_b64 exec, -1
.LL_49%=:
K 207,182,183
Zvav 60,156,192
s_cmp_eq_u32 s64, 15
s_cbranch_scc1 .Lf2_41%=
.Ll2_38%=:
W 0
Cvaa 160,128,64,224
Ra 144,242,45056
Ra 148,242,46080
Mvaa 160,132,68
s_add_u32 m0, s62, 8192
Ra 152,242,47104
Cvaa 164,132,84,228
X 184
Mvaa 164,128,80
X 185
Cvaa 168,128,96,232
X 186
Mvaa 168,132,100
Ra 156,242,48128
K 214,184,185
Cvaa 172,132,116,236
Dc 244,8,61,0
Mvaa 172,128,112
X 187
Mvaa 160,136,72
X 188
Mvaa 160,140,76
Ra 128,242,57344
K 215,186,187
Mvaa 164,140,92
Dc 244,8,61,1024
Mvaa 164,136,88
X 189
Mvaa 168,136,104
X 190
Mvaa 168,140,108
Ra 132,242,58368
K 222,188,189
Mvaa 172,140,124
Dc 244,8,61,2048
Mvaa 172,136,120
X 191
v_smfmac_f32_16x16x64_bf16 a[160:163], a[176:179], v[192:199], v240
Ra 136,242,59392
K 223,190,191
v_smfmac_f32_16x16x64_bf16 a[164:167], a[176:179], v[200:207], v240
X 160
v_smfmac_f32_16x16x64_bf16 a[168:171], a[176:179], v[208:215], v240
Dc 244,8,61,3072
v_smfmac_f32_16x16x64_bf16 a[172:175], a[176:179], v[216:223], v240
W 3
X 161
Cvaa 176,144,64,224
s_add_u32 m0, s62, 12288
X 162
Mvaa 176,148,68
Ra 140,242,60416
K 192,160,161
Cvaa 180,148,84,228
X 163
Mvaa 180,144,80
Dc 245,8,61,0
Cvaa 184,144,96,232
X 164
Mvaa 184,148,100
K 193,162,163
Cvaa 188,148,116,236
X 165
Mvaa 188,144,112
Dc 245,8,61,1024
Ra 144,242,61440
Mvaa 176,152,72
X 166
Mvaa 176,156,76
Ra 148,242,62464
K 200,164,165
Mvaa 180,156,92
X 167
Mvaa 180,152,88
Dc 245,8,61,2048
Mvaa 184,152,104
X 168
Mvaa 184,156,108
K 201,166,167
Mvaa 188,156,124
X 169
Mvaa 188,152,120
W 2
Dc 245,8,61,3072
Ra 152,242,63488
Mvav 32,128,196
s_add_u32 m0, s62, 0
X 170
Mvav 64,128,204
Ra 156,242,64512
K 208,168,169
Mvav 96,128,212
X 171
Mvav 128,128,220
Dc 1,8,61,0
Mvav 132,132,220
X 172
Mvav 100,132,212
Ra 128,242,32768
K 209,170,171
Mvav 68,132,204
X 173
Mvav 36,132,196
Dc 1,8,61,1024
Mvav 40,136,196
X 174
Mvav 72,136,204
Ra 132,242,33792
K 216,172,173
Mvav 104,136,212
X 175
Mvav 136,136,220
Dc 1,8,61,2048
Mvav 140,140,220
X 176
Mvav 108,140,212
Ra 136,242,34816
K 217,174,175
Mvav 76,140,204
X 177
Mvav 44,140,196
W 3
Dc 1,8,61,3072
Mvav 48,144,196
X 178
Mvav 80,144,204
s_add_u32 m0, s62, 4096
Ra 140,242,35840
Mvav 112,144,212
K 194,176,177
Mvav 144,144,220
X 179
Mvav 148,148,220
X 180
Mvav 116,148,212
Dc 243,8,61,0
Ra 144,242,36864
Mvav 84,148,204
K 195,178,179
Mvav 52,148,196
X 181
Mvav 56,152,196
X 182
Mvav 88,152,204
Dc 243,8,61,1024
Ra 148,242,37888
Mvav 120,152,212
K 202,180,181
Mvav 152,152,220
X 183
Mvav 156,156,220
X 184
Mvav 124,156,212
Dc 243,8,61,2048
Ra 152,242,38912
Mvav 92,156,204
K 203,182,183
Mvav 60,156,196
W 3
X 185
Cvaa 160,128,64,224
X 186
Mvaa 160,132,68
Dc 243,8,61,3072
Ra 156,242,39936
Cvaa 164,132,84,228
s_add_u32 s61, s61, s63
K 210,184,185
Mvaa 164,128,80
X 187
Cvaa 168,128,96,232
X 188
Mvaa 168,132,100
Cvaa 172,132,116,236
s_xor_b32 s62, s62, 0x10000
K 211,186,187
Mvaa 172,128,112
X 189
Mvaa 160,136,72
Ra 128,242,49152
Ra 132,242,50176
Mvaa 160,140,76
s_add_u32 s64, s64, 1
X 190
Mvaa 164,140,92
K 218,188,189
Mvaa 164,136,88
X 191
Mvaa 168,136,104
Mvaa 168,140,108
X 160
Mvaa 172,140,124
K 219,190,191
Mvaa 172,136,120
W 2
X 161
Cvaa 176,144,64,224
Ra 136,242,51200
Ra 140,242,52224
Mvaa 176,148,68
X 162
Cvaa 180,148,84,228
K 196,160,161
Mvaa 180,144,80
X 163
Cvaa 184,144,96,232
Mvaa 184,148,100
X 164
Cvaa 188,148,116,236
K 197,162,163
Mvaa 188,144,112
X 165
Mvaa 176,152,72
Ra 144,242,53248
Ra 148,242,54272
Mvaa 176,156,76
X 166
Mvaa 180,156,92
K 204,164,165
Mvaa 180,152,88
X 167
Mvaa 184,152,104
Mvaa 184,156,108
X 168
Mvaa 188,156,124
Mvaa 188,152,120
W 2
Ra 152,242,55296
K 205,166,167
Mvav 32,128,192
X 169
Mvav 64,128,200
Ra 156,242,56320
v_xor_b32 v242, 0x10000, v242
Mvav 96,128,208
X 170
Mvav 128,128,216
K 212,168,169
Mvav 132,132,216
X 171
Mvav 100,132,208
X 172
Mvav 68,132,200
Mvav 36,132,192
K 213,170,171
Mvav 40,136,192
X 173
Mvav 72,136,200
X 174
Mvav 104,136,208
Mvav 136,136,216
K 220,172,173
Mvav 140,140,216
X 175
Mvav 108,140,208
X 176
Mvav 76,140,200
s_waitcnt vmcnt(0) lgkmcnt(0)
s_barrier
Mvav 44,140,192
Ra 128,242,40960
K 221,174,175
Mvav 48,144,192
X 177
Mvav 80,144,200
X 178
Mvav 112,144,208
Ra 132,242,41984
Mvav 144,144,216
K 198,176,177
Mvav 148,148,216
X 179
Mvav 116,148,208
X 180
Mvav 84,148,200
Ra 136,242,43008
Mvav 52,148,192
K 199,178,179
Mvav 56,152,192
X 181
Mvav 88,152,200
Ra 140,242,44032
Mvav 120,152,208
X 182
Mvav 152,152,216
K 206,180,181
Mvav 156,156,216
X 183
Mvav 124,156,208
Mvav 92,156,200
K 207,182,183
Mvav 60,156,192
s_cmp_lg_u32 s64, 15
s_cbranch_scc1 .Ll2_38%=
.Lf2_41%=:
W 0
Cvaa 160,128,64,224
.Lh_4_2%=:
s_cbranch_execz .Lh_4_2_1%=
v_bfe_u32 v28, v1, 4, 2
v_lshlrev_b32 v28, 3, v28
Mvaa 160,132,68
Ra 144,242,45056
Ra 148,242,46080
Cvaa 164,132,84,228
Ra 152,242,47104
buffer_load_dwordx2 v[28:29], v28, s[8:11], s54 offen sc1
Mvaa 164,128,80
s_lshl_b32 s92, s58, 2
s_add_u32 s91, s92, s51
s_add_u32 s95, s91, 3
Cvaa 168,128,96,232
s_and_b32 s95, s95, 15
s_mul_i32 s95, s95, 40960
s_mul_i32 s71, s51, 10240
Mvaa 168,132,100
s_add_u32 s71, s71, 0
s_add_u32 m0, s71, s48
s_lshl2_add_u32 s94, s48, s95
Cvaa 172,132,116,236
X 184
Mvaa 172,128,112
)"
R"(X 185
Mvaa 160,136,72
X 186
Mvaa 160,140,76
Ra 156,242,48128
K 214,184,185
Mvaa 164,140,92
X 187
Mvaa 164,136,88
D 16,40,94,0
Ra 128,242,57344
Mvaa 168,136,104
s_add_u32 s95, s91, 5
s_and_b32 s95, s95, 15
s_mul_i32 s95, s95, 40960
Mvaa 168,140,108
s_mul_i32 s71, s51, 10240
s_add_u32 s71, s71, 20480
Ra 132,242,58368
Mvaa 172,140,124
X 188
Mvaa 172,136,120
D 17,40,94,1024
K 215,186,187
v_smfmac_f32_16x16x64_bf16 a[160:163], a[176:179], v[192:199], v240
X 189
v_smfmac_f32_16x16x64_bf16 a[164:167], a[176:179], v[200:207], v240
X 190
K 222,188,189
X 191
s_nop 0
K 223,190,191
v_smfmac_f32_16x16x64_bf16 a[168:171], a[176:179], v[208:215], v240
D 18,40,94,2048
Ra 136,242,59392
v_smfmac_f32_16x16x64_bf16 a[172:175], a[176:179], v[216:223], v240
W 3
X 160
Cvaa 176,144,64,224
X 161
Mvaa 176,148,68
X 162
Cvaa 180,148,84,228
D 19,40,94,3072
K 192,160,161
Mvaa 180,144,80
s_add_u32 m0, m0, 4096
X 163
Cvaa 184,144,96,232
X 164
Mvaa 184,148,100
Ra 140,242,60416
K 193,162,163
Cvaa 188,148,116,236
buffer_load_dwordx4 v20, s[40:43], s94 offen lds
Mvaa 188,144,112
s_add_u32 m0, s71, s48
s_lshl2_add_u32 s94, s48, s95
Ra 144,242,61440
Mvaa 176,152,72
X 165
Mvaa 176,156,76
X 166
Mvaa 180,156,92
D 16,40,94,0
K 200,164,165
Mvaa 180,152,88
X 167
Mvaa 184,152,104
X 172
Mvaa 184,156,108
D 17,40,94,1024
K 201,166,167
Mvaa 188,156,124
X 173
Mvaa 188,152,120
W 1
Ra 148,242,62464
Ra 152,242,63488
Mvav 32,128,196
X 174
Mvav 64,128,204
D 18,40,94,2048
K 216,172,173
Mvav 96,128,212
X 175
Mvav 128,128,220
X 168
Mvav 132,132,220
s_mov_b32 s18, 40960
D 19,40,94,3072
K 217,174,175
Mvav 100,132,212
s_add_u32 m0, m0, 4096
X 169
Mvav 68,132,204
s_mov_b32 s19, 73728
X 170
Mvav 36,132,196
s_bitcmp1_b32 s14, 0
s_cselect_b32 s18, 49152, s18
s_cselect_b32 s19, 98304, s19
Ra 156,242,64512
.Lh_4_3%=:
s_cbranch_execz .Lh_4_3_1%=
K 208,168,169
Mvav 40,136,196
s_cmp_lt_u32 s14, 2
s_cselect_b32 s20, s18, s19
s_cselect_b32 s21, s19, s18
X 171
Mvav 72,136,204
X 184
Mvav 104,136,212
buffer_load_dwordx4 v20, s[40:43], s94 offen lds
K 209,170,171
Mvav 136,136,220
s_waitcnt vmcnt(10)
v_cmp_eq_u64 vcc, s[12:13], v[28:29]
s_cmp_eq_u64 vcc, -1
s_cselect_b32 s46, 79691776, 0
s_add_u32 s81, s58, 0
s_and_b32 s81, s81, 3
Mvav 140,140,220
s_lshl_b32 s81, s81, 12
s_add_u32 s81, s81, s49
Ra 128,242,49152
Mvav 108,140,212
X 185
Mvav 76,140,204
X 186
Mvav 44,140,196
W 1
buffer_load_dwordx4 a[184:187], v1, s[44:47], s81 offen offset:0 sc1
K 210,184,185
Mvav 48,144,196
s_add_u32 s82, s81, 131072
X 187
Mvav 80,144,204
v_add_u32 v2, s20, v1
v_add_u32 v3, s21, v1
Mvav 112,144,212
s_lshl_b32 s18, s14, 9
X 188
Mvav 144,144,220
Ra 132,242,50176
K 211,186,187
Mvav 148,148,220
buffer_load_dwordx4 a[200:203], v1, s[44:47], s82 offen offset:0 sc1
Ra 136,242,51200
Mvav 116,148,212
X 189
Mvav 84,148,204
v_lshrrev_b32 v232, 1, v1
v_add_u32 v232, s18, v232
Mvav 52,148,196
X 190
Mvav 56,152,196
K 218,188,189
v_bfe_u32 v12, v1, 6, 2
Mvav 88,152,204
buffer_load_dwordx4 a[188:191], v1, s[44:47], s81 offen offset:1024 sc1
Ra 140,242,52224
Mvav 120,152,212
X 191
Mvav 152,152,220
v_lshrrev_b32 v13, 8, v1
v_bfe_u32 v14, v1, 4, 1
Mvav 156,156,220
X 176
Mvav 124,156,212
K 219,190,191
v_lshlrev_b32 v14, 4, v14
Mvav 92,156,204
X 177
Mvav 60,156,196
buffer_load_dwordx4 a[204:207], v1, s[44:47], s82 offen offset:1024 sc1
v_mov_b32 v15, 0x3f80
Maav 168,180,208
X 178
K 194,176,177
X 179
X 180
K 195,178,179
Maav 172,180,216
X 181
X 182
K 202,180,181
X 183
s_nop 0
K 203,182,183
Maav 160,180,192
Ra 144,242,53248
v_lshlrev_b32 v15, v14, v15
Maav 164,180,200
W 1
buffer_load_dwordx4 a[192:195], v1, s[44:47], s81 offen offset:2048 sc1
v_bfe_u32 v14, v1, 5, 1
Mvav 96,128,208
Ra 148,242,54272
v_xor_b32 v27, v12, v13
v_lshl_or_b32 v27, v27, 1, v14
Mvav 128,128,216
v_cmp_eq_u32_e64 s[86:87], 0, v27
v_mov_b32 v226, 0
s_nop 0
v_cndmask_b32_e64 v224, 0, v15, s[86:87]
Mvav 132,132,216
Ra 152,242,55296
Ra 156,242,56320
buffer_load_dwordx4 a[208:211], v1, s[44:47], s82 offen offset:2048 sc1
Mvav 100,132,208
v_mov_b32 v228, 0
v_mov_b32 v230, v224
v_cmp_eq_u32_e64 s[86:87], 1, v27
Mvav 104,136,208
v_mov_b32 v227, 0
Mvav 136,136,216
buffer_load_dwordx4 a[196:199], v1, s[44:47], s81 offen offset:3072 sc1
v_cndmask_b32_e64 v225, 0, v15, s[86:87]
v_mov_b32 v229, 0
Mvav 140,140,216
v_mov_b32 v231, v225
G 4,160
G 8,161
Mvav 108,140,208
W 0
G 5,164
G 9,165
G 6,168
Mvav 112,144,208
buffer_load_dwordx4 a[212:215], v1, s[44:47], s82 offen offset:3072 sc1
s_add_u32 s81, s58, 1
s_and_b32 s81, s81, 3
s_lshl_b32 s81, s81, 12
Mvav 144,144,216
s_add_u32 s81, s81, s49
G 10,169
G 7,172
G 11,173
Mvav 148,148,216
A 4,4,8
A 5,5,9
A 6,6,10
Mvav 116,148,208
buffer_load_dwordx4 a[216:219], v1, s[44:47], s81 offen offset:0 sc1
s_add_u32 s82, s81, 131072
A 7,7,11
ds_write_b64 v232, v[6:7] offset:57344
Mvav 120,152,208
A 8,4,5
A 9,6,7
A 8,8,9
Mvav 152,152,216
v_cmp_ngt_f32_e64 s[86:87], s90, v8
s_lshl_b32 s19, s14, 2
v_mov_b32 v9, s19
s_or_b64 s[88:89], s[88:89], s[86:87]
Mvav 156,156,216
s_and_b64 s[88:89], s[88:89], s[24:25]
s_cselect_b32 s18, 1, 0
buffer_load_dwordx4 a[232:235], v1, s[44:47], s82 offen offset:0 sc1
v_mov_b32 v8, s18
ds_write_b32 v9, v8 offset:59392
Mvav 124,156,208
v_xor_b32 v10, 1024, v232
v_bfe_u32 v11, v1, 2, 4
K 96,96,97
Mvav 32,128,192
K 97,98,99
K 98,100,101
K 99,102,103
Mvav 64,128,200
ds_write_b128 v2, v[96:99] offset:0
K 128,128,129
K 129,130,131
Mvav 68,132,200
K 130,132,133
K 131,134,135
ds_write_b128 v2, v[128:131] offset:4096
Mvav 36,132,192
buffer_load_dwordx4 a[220:223], v1, s[44:47], s81 offen offset:1024 sc1
K 104,104,105
K 105,106,107
Mvav 40,136,192
K 106,108,109
K 107,110,111
ds_write_b128 v2, v[104:107] offset:1024
Mvav 72,136,200
K 136,136,137
K 137,138,139
K 138,140,141
Mvav 76,140,200
buffer_load_dwordx4 a[236:239], v1, s[44:47], s82 offen offset:1024 sc1
K 139,142,143
ds_write_b128 v2, v[136:139] offset:5120
Mvav 44,140,192
K 112,112,113
K 113,114,115
K 114,116,117
Mvav 48,144,192
K 115,118,119
ds_write_b128 v2, v[112:115] offset:2048
K 144,144,145
Mvav 80,144,200
K 145,146,147
K 146,148,149
K 147,150,151
Mvav 84,148,200
ds_write_b128 v2, v[144:147] offset:6144
buffer_load_dwordx4 a[224:227], v1, s[44:47], s81 offen offset:2048 sc1
K 120,120,121
Mvav 52,148,192
K 121,122,123
K 122,124,125
K 123,126,127
Mvav 56,152,192
ds_write_b128 v2, v[120:123] offset:3072
K 152,152,153
K 153,154,155
Mvav 88,152,200
buffer_load_dwordx4 a[240:243], v1, s[44:47], s82 offen offset:2048 sc1
K 154,156,157
K 155,158,159
Mvav 92,156,200
ds_write_b128 v2, v[152:155] offset:7168
Mvav 60,156,192
buffer_load_dwordx4 a[228:231], v1, s[44:47], s81 offen offset:3072 sc1
buffer_load_dwordx4 a[244:247], v1, s[44:47], s82 offen offset:3072 sc1
s_branch .Lend_43%=
.p2align 12
.Lfs_42%=:
v_smfmac_f32_16x16x64_bf16 a[160:163], a[176:179], v[192:199], v240
s_mov_b32 s46, 0
s_lshl_b32 s92, s58, 2
s_add_u32 s91, s92, s51
s_add_u32 s95, s91, 3
s_and_b32 s95, s95, 15
s_mul_i32 s95, s95, 40960
s_mul_i32 s71, s51, 10240
v_smfmac_f32_16x16x64_bf16 a[164:167], a[176:179], v[200:207], v240
s_add_u32 s71, s71, 0
s_add_u32 m0, s71, s48
s_lshl2_add_u32 s94, s48, s95
Ra 144,242,45056
Ra 148,242,46080
Ra 152,242,47104
Ra 156,242,48128
v_smfmac_f32_16x16x64_bf16 a[168:171], a[176:179], v[208:215], v240
D 16,40,94,0
s_add_u32 s95, s91, 5
s_and_b32 s95, s95, 15
s_mul_i32 s95, s95, 40960
s_mul_i32 s71, s51, 10240
s_add_u32 s71, s71, 20480
v_smfmac_f32_16x16x64_bf16 a[172:175], a[176:179], v[216:223], v240
W 4
Cvaa 168,128,96,232
v_mov_b32 v192, 0
G 12,160
Mvaa 168,132,100
D 17,40,94,1024
G 4,161
A 12,12,4
v_cmp_nlt_f32_e64 s[86:87], v12, s90
v_rcp_f32 v12, v12
s_or_b64 s[88:89], s[88:89], s[86:87]
Cvaa 172,132,116,236
v_accvgpr_write_b32 a160, 0
v_accvgpr_write_b32 a161, 0
v_accvgpr_write_b32 a162, 0
v_accvgpr_write_b32 a163, 0
G 13,164
G 5,165
Mvaa 172,128,112
v_mov_b32 v193, 0
A 13,13,5
v_cmp_nlt_f32_e64 s[86:87], v13, s90
v_rcp_f32 v13, v13
s_or_b64 s[88:89], s[88:89], s[86:87]
v_accvgpr_write_b32 a164, 0
Mvaa 168,136,104
v_accvgpr_write_b32 a165, 0
v_accvgpr_write_b32 a166, 0
v_accvgpr_write_b32 a167, 0
U 32,32,12
U 33,33,12
U 34,34,12
Mvaa 168,140,108
v_mov_b32 v200, 0
U 35,35,12
U 36,36,12
U 37,37,12
U 38,38,12
U 39,39,12
Mvaa 172,140,124
Ra 128,242,16384
Ra 132,242,17408
K 32,32,33
K 33,34,35
)"
R"(K 34,36,37
K 35,38,39
s_add_u32 s93, s67, 0
Mvaa 172,136,120
Ra 136,242,18432
Ra 140,242,19456
D 18,40,94,2048
v_mov_b32 v201, 0
W 4
Cvaa 188,144,112,236
Ra 144,242,20480
v_cndmask_b32_e64 v168, v241, v168, s[72:73]
v_cndmask_b32_e64 v169, v241, v169, s[74:75]
v_cndmask_b32_e64 v170, v241, v170, s[76:77]
Mvaa 188,148,116
buffer_store_dwordx4 v[32:35], v1, s[8:11], s93 offen offset:0 sc1
U 40,40,12
U 41,41,12
U 42,42,12
U 43,43,12
U 44,44,12
Mvaa 188,152,120
U 45,45,12
U 46,46,12
U 47,47,12
K 40,40,41
K 41,42,43
K 42,44,45
Mvaa 188,156,124
W 1
K 43,46,47
buffer_store_dwordx4 v[40:43], v1, s[8:11], s93 offen offset:1024 sc1
U 48,48,12
U 49,49,12
U 50,50,12
U 51,51,12
Mvav 96,128,212
U 52,52,12
U 53,53,12
U 54,54,12
U 55,55,12
K 48,48,49
K 49,50,51
Mvav 128,128,220
K 50,52,53
K 51,54,55
buffer_store_dwordx4 v[48:51], v1, s[8:11], s93 offen offset:2048 sc1
U 56,56,12
U 57,57,12
U 58,58,12
Mvav 132,132,220
Ra 148,242,21504
Ra 152,242,22528
Ra 156,242,23552
U 59,59,12
U 60,60,12
U 61,61,12
Mvav 100,132,212
D 19,40,94,3072
s_add_u32 m0, m0, 4096
U 62,62,12
U 63,63,12
K 56,56,57
K 57,58,59
K 58,60,61
Mvav 104,136,212
K 59,62,63
v_cndmask_b32_e64 v171, v241, v171, s[78:79]
X 168
X 169
Mvav 136,136,220
buffer_store_dwordx4 v[56:59], v1, s[8:11], s93 offen offset:3072 sc1
Ra 128,242,49152
G 14,168
G 6,169
A 14,14,6
v_cmp_nlt_f32_e64 s[86:87], v14, s90
Mvav 140,140,220
v_rcp_f32 v14, v14
s_or_b64 s[88:89], s[88:89], s[86:87]
v_accvgpr_write_b32 a168, 0
v_accvgpr_write_b32 a169, 0
v_accvgpr_write_b32 a170, 0
v_accvgpr_write_b32 a171, 0
Mvav 108,140,212
W 1
G 15,172
G 7,173
A 15,15,7
v_cmp_nlt_f32_e64 s[86:87], v15, s90
v_rcp_f32 v15, v15
Mvav 112,144,212
s_or_b64 s[88:89], s[88:89], s[86:87]
buffer_load_dwordx4 v20, s[40:43], s94 offen lds
v_accvgpr_write_b32 a172, 0
v_accvgpr_write_b32 a173, 0
v_accvgpr_write_b32 a174, 0
v_accvgpr_write_b32 a175, 0
U 64,64,13
Mvav 144,144,220
s_add_u32 m0, s71, s48
s_lshl2_add_u32 s94, s48, s95
U 65,65,13
U 66,66,13
U 67,67,13
U 68,68,13
U 69,69,13
Mvav 148,148,220
U 70,70,13
U 71,71,13
K 64,64,65
K 65,66,67
K 66,68,69
K 67,70,71
s_add_u32 s93, s67, 131072
Mvav 116,148,212
buffer_store_dwordx4 v[64:67], v1, s[8:11], s93 offen offset:0 sc1
U 72,72,13
U 73,73,13
U 74,74,13
U 75,75,13
U 76,76,13
Mvav 120,152,212
Ra 132,242,50176
Ra 136,242,51200
Ra 140,242,52224
U 77,77,13
U 78,78,13
U 79,79,13
Mvav 152,152,220
K 72,72,73
K 73,74,75
K 74,76,77
K 75,78,79
buffer_store_dwordx4 v[72:75], v1, s[8:11], s93 offen offset:1024 sc1
U 80,80,13
Mvav 156,156,220
X 170
K 208,168,169
X 171
X 172
K 209,170,171
X 173
X 174
K 216,172,173
X 175
v_mov_b32 v194, 0
K 217,174,175
v_mov_b32 v195, 0
v_mov_b32 v202, 0
v_mov_b32 v203, 0
v_mov_b32 v210, 0
v_mov_b32 v211, 0
Mvav 124,156,212
v_cndmask_b32_e64 v188, v241, v188, s[72:73]
v_cndmask_b32_e64 v189, v241, v189, s[74:75]
v_cndmask_b32_e64 v190, v241, v190, s[76:77]
v_cndmask_b32_e64 v191, v241, v191, s[78:79]
X 188
X 189
X 190
K 218,188,189
X 191
s_nop 0
K 219,190,191
Maav 168,180,208
D 16,40,94,0
U 81,81,13
U 82,82,13
U 83,83,13
U 84,84,13
U 85,85,13
Maav 172,180,216
W 0
U 86,86,13
U 87,87,13
K 80,80,81
K 81,82,83
K 82,84,85
K 83,86,87
Zvav 32,128,192
buffer_store_dwordx4 v[80:83], v1, s[8:11], s93 offen offset:2048 sc1
U 88,88,13
U 89,89,13
U 90,90,13
U 91,91,13
U 92,92,13
Zvav 64,128,200
U 93,93,13
U 94,94,13
U 95,95,13
K 88,88,89
K 89,90,91
K 90,92,93
K 91,94,95
buffer_store_dwordx4 v[88:91], v1, s[8:11], s93 offen offset:3072 sc1
U 96,96,14
U 97,97,14
U 98,98,14
U 99,99,14
U 100,100,14
U 101,101,14
U 102,102,14
U 103,103,14
K 96,96,97
K 97,98,99
K 98,100,101
K 99,102,103
s_add_u32 s93, s67, 262144
buffer_store_dwordx4 v[96:99], v1, s[8:11], s93 offen offset:0 sc1
s_nop 1
Zvav 96,128,208
U 104,104,14
U 105,105,14
U 106,106,14
U 107,107,14
U 108,108,14
U 109,109,14
U 110,110,14
U 111,111,14
K 104,104,105
K 105,106,107
K 106,108,109
K 107,110,111
buffer_store_dwordx4 v[104:107], v1, s[8:11], s93 offen offset:1024 sc1
U 112,112,14
U 113,113,14
U 114,114,14
U 115,115,14
U 116,116,14
U 117,117,14
U 118,118,14
U 119,119,14
K 112,112,113
K 113,114,115
K 114,116,117
K 115,118,119
buffer_store_dwordx4 v[112:115], v1, s[8:11], s93 offen offset:2048 sc1
U 120,120,14
U 121,121,14
U 122,122,14
U 123,123,14
U 124,124,14
U 125,125,14
U 126,126,14
U 127,127,14
K 120,120,121
K 121,122,123
K 122,124,125
K 123,126,127
buffer_store_dwordx4 v[120:123], v1, s[8:11], s93 offen offset:3072 sc1
U 128,128,15
U 129,129,15
U 130,130,15
U 131,131,15
U 132,132,15
U 133,133,15
U 134,134,15
U 135,135,15
K 128,128,129
K 129,130,131
K 130,132,133
K 131,134,135
s_add_u32 s93, s67, 393216
buffer_store_dwordx4 v[128:131], v1, s[8:11], s93 offen offset:0 sc1
s_nop 1
Zvav 128,128,216
Ra 144,242,53248
U 136,136,15
U 137,137,15
U 138,138,15
U 139,139,15
U 140,140,15
Zvav 132,132,216
D 17,40,94,1024
U 141,141,15
U 142,142,15
U 143,143,15
K 136,136,137
K 137,138,139
Zvav 100,132,208
Ra 148,242,54272
Ra 152,242,55296
Ra 156,242,56320
K 138,140,141
K 139,142,143
Zvav 68,132,200
D 18,40,94,2048
Zvav 36,132,192
Zvav 40,136,192
D 19,40,94,3072
s_add_u32 m0, m0, 4096
Zvav 72,136,200
Zvav 104,136,208
buffer_store_dwordx4 v[136:139], v1, s[8:11], s93 offen offset:1024 sc1
U 144,144,15
U 145,145,15
U 146,146,15
U 147,147,15
U 148,148,15
Zvav 136,136,216
U 149,149,15
U 150,150,15
U 151,151,15
K 144,144,145
K 145,146,147
K 146,148,149
Zvav 140,140,216
K 147,150,151
buffer_store_dwordx4 v[144:147], v1, s[8:11], s93 offen offset:2048 sc1
U 152,152,15
U 153,153,15
U 154,154,15
U 155,155,15
Zvav 108,140,208
U 156,156,15
U 157,157,15
U 158,158,15
U 159,159,15
K 152,152,153
K 153,154,155
Zvav 76,140,200
buffer_load_dwordx4 v20, s[40:43], s94 offen lds
K 154,156,157
K 155,158,159
Zvav 44,140,192
W 0
Zvav 48,144,192
buffer_store_dwordx4 v[152:155], v1, s[8:11], s93 offen offset:3072 sc1
s_mov_b32 s18, 40960
s_mov_b32 s19, 73728
s_bitcmp1_b32 s14, 0
s_cselect_b32 s18, 49152, s18
s_cselect_b32 s19, 98304, s19
s_cmp_lt_u32 s14, 2
s_cselect_b32 s20, s18, s19
s_cselect_b32 s21, s19, s18
v_add_u32 v2, s20, v1
v_add_u32 v3, s21, v1
Zvav 80,144,200
s_lshl_b32 s18, s14, 9
v_lshrrev_b32 v232, 1, v1
v_add_u32 v232, s18, v232
v_bfe_u32 v12, v1, 6, 2
v_lshrrev_b32 v13, 8, v1
v_bfe_u32 v14, v1, 4, 1
v_lshlrev_b32 v14, 4, v14
Zvav 112,144,208
v_mov_b32 v15, 0x3f80
v_lshlrev_b32 v15, v14, v15
v_bfe_u32 v14, v1, 5, 1
v_xor_b32 v27, v12, v13
v_lshl_or_b32 v27, v27, 1, v14
v_cmp_eq_u32_e64 s[86:87], 0, v27
Zvav 144,144,216
v_mov_b32 v226, 0
Zvav 148,148,216
v_cndmask_b32_e64 v224, 0, v15, s[86:87]
v_mov_b32 v228, 0
v_mov_b32 v230, v224
v_cmp_eq_u32_e64 s[86:87], 1, v27
v_mov_b32 v227, 0
s_nop 0
v_cndmask_b32_e64 v225, 0, v15, s[86:87]
Zvav 116,148,208
v_mov_b32 v229, 0
v_mov_b32 v231, v225
G 4,160
G 8,161
G 5,164
G 9,165
Zvav 84,148,200
G 6,168
G 10,169
G 7,172
G 11,173
A 4,4,8
A 5,5,9
Zvav 52,148,192
A 6,6,10
A 7,7,11
ds_write_b64 v232, v[6:7] offset:57344
A 8,4,5
A 9,6,7
A 8,8,9
Zvav 56,152,192
v_cmp_ngt_f32_e64 s[86:87], s90, v8
s_lshl_b32 s19, s14, 2
v_mov_b32 v9, s19
s_or_b64 s[88:89], s[88:89], s[86:87]
s_and_b64 s[88:89], s[88:89], s[24:25]
s_cselect_b32 s18, 1, 0
v_mov_b32 v8, s18
ds_write_b32 v9, v8 offset:59392
Zvav 88,152,200
v_xor_b32 v10, 1024, v232
v_bfe_u32 v11, v1, 2, 4
K 96,96,97
K 97,98,99
K 98,100,101
)"
R"(K 99,102,103
Zvav 120,152,208
ds_write_b128 v2, v[96:99] offset:0
K 128,128,129
K 129,130,131
K 130,132,133
K 131,134,135
ds_write_b128 v2, v[128:131] offset:4096
Zvav 152,152,216
K 104,104,105
K 105,106,107
K 106,108,109
K 107,110,111
ds_write_b128 v2, v[104:107] offset:1024
K 136,136,137
Zvav 156,156,216
K 137,138,139
K 138,140,141
K 139,142,143
ds_write_b128 v2, v[136:139] offset:5120
K 112,112,113
K 113,114,115
Zvav 124,156,208
K 114,116,117
K 115,118,119
ds_write_b128 v2, v[112:115] offset:2048
K 144,144,145
K 145,146,147
K 146,148,149
Zvav 92,156,200
K 147,150,151
ds_write_b128 v2, v[144:147] offset:6144
Zvav 60,156,192
K 120,120,121
K 121,122,123
K 122,124,125
K 123,126,127
ds_write_b128 v2, v[120:123] offset:3072
K 152,152,153
K 153,154,155
K 154,156,157
K 155,158,159
ds_write_b128 v2, v[152:155] offset:7168
s_waitcnt vmcnt(0)
s_and_b64 s[86:87], s[88:89], s[24:25]
s_cbranch_scc1 .LL_50%=
v_mov_b64 v[28:29], s[12:13]
s_mov_b64 exec, 1
buffer_store_dwordx2 v[28:29], off, s[8:11], s69 sc1
s_mov_b64 exec, -1
.LL_50%=:
s_mov_b32 s70, 0
s_branch .Lend_43%=
.p2align 12
.Lpd_32%=:
v_pk_mul_f32 v[32:33], v[32:33], s[22:23]
v_pk_mul_f32 v[34:35], v[34:35], s[22:23]
v_pk_mul_f32 v[36:37], v[36:37], s[22:23]
v_pk_mul_f32 v[38:39], v[38:39], s[22:23]
v_pk_mul_f32 v[64:65], v[64:65], s[22:23]
v_pk_mul_f32 v[66:67], v[66:67], s[22:23]
v_pk_mul_f32 v[68:69], v[68:69], s[22:23]
v_pk_mul_f32 v[70:71], v[70:71], s[22:23]
v_pk_mul_f32 v[96:97], v[96:97], s[22:23]
v_pk_mul_f32 v[98:99], v[98:99], s[22:23]
v_pk_mul_f32 v[100:101], v[100:101], s[22:23]
v_pk_mul_f32 v[102:103], v[102:103], s[22:23]
v_pk_mul_f32 v[128:129], v[128:129], s[22:23]
v_pk_mul_f32 v[130:131], v[130:131], s[22:23]
v_pk_mul_f32 v[132:133], v[132:133], s[22:23]
v_pk_mul_f32 v[134:135], v[134:135], s[22:23]
v_pk_mul_f32 v[40:41], v[40:41], s[22:23]
v_pk_mul_f32 v[42:43], v[42:43], s[22:23]
v_pk_mul_f32 v[44:45], v[44:45], s[22:23]
v_pk_mul_f32 v[46:47], v[46:47], s[22:23]
v_pk_mul_f32 v[72:73], v[72:73], s[22:23]
v_pk_mul_f32 v[74:75], v[74:75], s[22:23]
v_pk_mul_f32 v[76:77], v[76:77], s[22:23]
v_pk_mul_f32 v[78:79], v[78:79], s[22:23]
v_pk_mul_f32 v[104:105], v[104:105], s[22:23]
v_pk_mul_f32 v[106:107], v[106:107], s[22:23]
v_pk_mul_f32 v[108:109], v[108:109], s[22:23]
v_pk_mul_f32 v[110:111], v[110:111], s[22:23]
v_pk_mul_f32 v[136:137], v[136:137], s[22:23]
v_pk_mul_f32 v[138:139], v[138:139], s[22:23]
v_pk_mul_f32 v[140:141], v[140:141], s[22:23]
v_pk_mul_f32 v[142:143], v[142:143], s[22:23]
v_pk_mul_f32 v[48:49], v[48:49], s[22:23]
v_pk_mul_f32 v[50:51], v[50:51], s[22:23]
v_pk_mul_f32 v[52:53], v[52:53], s[22:23]
v_pk_mul_f32 v[54:55], v[54:55], s[22:23]
v_pk_mul_f32 v[80:81], v[80:81], s[22:23]
v_pk_mul_f32 v[82:83], v[82:83], s[22:23]
v_pk_mul_f32 v[84:85], v[84:85], s[22:23]
v_pk_mul_f32 v[86:87], v[86:87], s[22:23]
v_pk_mul_f32 v[112:113], v[112:113], s[22:23]
v_pk_mul_f32 v[114:115], v[114:115], s[22:23]
v_pk_mul_f32 v[116:117], v[116:117], s[22:23]
v_pk_mul_f32 v[118:119], v[118:119], s[22:23]
v_pk_mul_f32 v[144:145], v[144:145], s[22:23]
v_pk_mul_f32 v[146:147], v[146:147], s[22:23]
v_pk_mul_f32 v[148:149], v[148:149], s[22:23]
v_pk_mul_f32 v[150:151], v[150:151], s[22:23]
v_pk_mul_f32 v[56:57], v[56:57], s[22:23]
v_pk_mul_f32 v[58:59], v[58:59], s[22:23]
v_pk_mul_f32 v[60:61], v[60:61], s[22:23]
v_pk_mul_f32 v[62:63], v[62:63], s[22:23]
v_pk_mul_f32 v[88:89], v[88:89], s[22:23]
v_pk_mul_f32 v[90:91], v[90:91], s[22:23]
v_pk_mul_f32 v[92:93], v[92:93], s[22:23]
v_pk_mul_f32 v[94:95], v[94:95], s[22:23]
v_pk_mul_f32 v[120:121], v[120:121], s[22:23]
v_pk_mul_f32 v[122:123], v[122:123], s[22:23]
v_pk_mul_f32 v[124:125], v[124:125], s[22:23]
v_pk_mul_f32 v[126:127], v[126:127], s[22:23]
v_pk_mul_f32 v[152:153], v[152:153], s[22:23]
v_pk_mul_f32 v[154:155], v[154:155], s[22:23]
v_pk_mul_f32 v[156:157], v[156:157], s[22:23]
v_pk_mul_f32 v[158:159], v[158:159], s[22:23]
K 32,32,33
K 33,34,35
K 34,36,37
K 35,38,39
K 40,40,41
K 41,42,43
K 42,44,45
K 43,46,47
W 0
Zvav 196,128,32
s_add_u32 m0, s62, 0
Ra 144,242,4096
Ra 148,242,5120
Ra 152,242,6144
Ra 156,242,7168
Mvav 196,132,40
K 64,64,65
K 65,66,67
K 66,68,69
K 67,70,71
K 72,72,73
K 73,74,75
K 74,76,77
K 75,78,79
s_nop 1
Zvav 204,132,72
Dc 1,8,61,0
K 96,96,97
K 97,98,99
K 98,100,101
Mvav 204,128,64
K 99,102,103
K 104,104,105
K 105,106,107
K 106,108,109
K 107,110,111
Zvav 212,128,96
Dc 1,8,61,1024
K 128,128,129
K 129,130,131
K 130,132,133
Mvav 212,132,104
K 131,134,135
K 136,136,137
K 137,138,139
K 138,140,141
K 139,142,143
s_nop 1
Zvav 220,132,136
Dc 1,8,61,2048
K 48,48,49
K 49,50,51
K 50,52,53
Mvav 220,128,128
K 51,54,55
K 56,56,57
K 57,58,59
K 58,60,61
K 59,62,63
Mvav 196,136,48
Dc 1,8,61,3072
s_add_u32 m0, s62, 4096
Ra 128,242,8192
Ra 132,242,9216
K 80,80,81
Mvav 196,140,56
K 81,82,83
K 82,84,85
K 83,86,87
K 88,88,89
K 89,90,91
K 90,92,93
K 91,94,95
s_nop 1
Mvav 204,140,88
Dc 243,8,61,0
K 112,112,113
K 113,114,115
K 114,116,117
Mvav 204,136,80
K 115,118,119
K 120,120,121
K 121,122,123
K 122,124,125
K 123,126,127
Mvav 212,136,112
Dc 243,8,61,1024
K 144,144,145
K 145,146,147
K 146,148,149
Mvav 212,140,120
K 147,150,151
K 152,152,153
K 153,154,155
K 154,156,157
K 155,158,159
s_nop 1
Mvav 220,140,152
Dc 243,8,61,2048
v_accvgpr_write_b32 a0, v32
v_accvgpr_write_b32 a1, v33
v_accvgpr_write_b32 a2, v34
Mvav 220,136,144
W 2
Dc 243,8,61,3072
s_add_u32 m0, s62, 8192
v_accvgpr_write_b32 a3, v35
v_accvgpr_write_b32 a4, v40
Zvav 68,144,64
Dc 244,8,61,0
v_accvgpr_write_b32 a5, v41
v_accvgpr_write_b32 a6, v42
v_accvgpr_write_b32 a7, v43
Mvav 68,148,72
Dc 244,8,61,1024
v_accvgpr_write_b32 a8, v48
v_accvgpr_write_b32 a9, v49
v_accvgpr_write_b32 a10, v50
Zvav 100,148,104
Dc 244,8,61,2048
Ra 136,242,10240
Ra 140,242,11264
v_accvgpr_write_b32 a11, v51
Mvav 100,144,96
Dc 244,8,61,3072
s_add_u32 m0, s62, 12288
v_accvgpr_write_b32 a12, v56
v_accvgpr_write_b32 a13, v57
v_accvgpr_write_b32 a14, v58
Zvav 132,144,128
Dc 245,8,61,0
v_accvgpr_write_b32 a15, v59
v_accvgpr_write_b32 a16, v64
v_cndmask_b32_e64 v196, v241, v196, s[72:73]
Mvav 132,148,136
Dc 245,8,61,1024
v_cndmask_b32_e64 v197, v241, v197, s[74:75]
v_cndmask_b32_e64 v198, v241, v198, s[76:77]
v_cndmask_b32_e64 v199, v241, v199, s[78:79]
Mvav 68,152,80
Dc 245,8,61,2048
v_max3_f32 v4, v196, v197, v198
v_max3_f32 v4, v4, v199, v252
v_max3_f32 v5, v204, v205, v206
Mvav 68,156,88
v_max3_f32 v5, v5, v207, v253
v_max3_f32 v6, v212, v213, v214
v_max3_f32 v6, v6, v215, v254
v_max3_f32 v7, v220, v221, v222
Mvav 100,156,120
v_max3_f32 v7, v7, v223, v255
v_mov_b32 v8, v4
v_mov_b32 v9, v5
v_mov_b32 v10, v6
Mvav 100,152,112
v_mov_b32 v11, v7
v_permlane32_swap_b32 v4, v8
v_permlane32_swap_b32 v5, v9
v_accvgpr_write_b32 a17, v65
Mvav 132,152,144
v_permlane32_swap_b32 v6, v10
v_permlane32_swap_b32 v7, v11
v_max_f32 v4, v4, v8
v_max_f32 v5, v5, v9
v_max_f32 v6, v6, v10
v_max_f32 v7, v7, v11
v_mov_b32 v8, v4
v_mov_b32 v9, v5
v_mov_b32 v10, v6
v_mov_b32 v11, v7
v_permlane16_swap_b32 v4, v8
v_permlane16_swap_b32 v5, v9
v_permlane16_swap_b32 v6, v10
v_permlane16_swap_b32 v7, v11
v_max_f32 v4, v4, v8
v_max_f32 v5, v5, v9
v_max_f32 v6, v6, v10
v_max_f32 v7, v7, v11
v_sub_f32 v4, 0, v4
v_sub_f32 v5, 0, v5
v_sub_f32 v6, 0, v6
v_sub_f32 v7, 0, v7
v_mov_b32 v224, v4
v_mov_b32 v225, v4
v_mov_b32 v226, v4
v_mov_b32 v227, v4
v_mov_b32 v228, v5
v_mov_b32 v229, v5
v_mov_b32 v230, v5
v_mov_b32 v231, v5
v_mov_b32 v232, v6
v_mov_b32 v233, v6
v_mov_b32 v234, v6
v_mov_b32 v235, v6
v_mov_b32 v236, v7
v_mov_b32 v237, v7
v_mov_b32 v238, v7
v_mov_b32 v239, v7
Mvav 132,156,152
W 0
Dc 245,8,61,3072
v_accvgpr_write_b32 a18, v66
v_accvgpr_write_b32 a19, v67
v_accvgpr_write_b32 a20, v72
Cvav 168,128,96,232
s_add_u32 s61, s61, s63
s_add_u32 s36, s29, 65536
v_accvgpr_write_b32 a21, v73
v_accvgpr_write_b32 a22, v74
v_accvgpr_write_b32 a23, v75
Mvav 168,132,104
s_and_b32 s80, s14, 1
buffer_store_dwordx4 a[192:195], v1, s[8:11], s36 offen offset:0 sc1
v_accvgpr_write_b32 a24, v80
v_accvgpr_write_b32 a25, v81
v_accvgpr_write_b32 a26, v82
Cvav 172,132,136,236
s_lshl_b32 s80, s80, 14
Ra 144,242,12288
Ra 148,242,13312
Ra 152,242,14336
Ra 156,242,15360
Mvav 172,128,128
s_add_u32 s80, s80, 98304
v_accvgpr_write_b32 a27, v83
v_accvgpr_write_b32 a28, v88
v_accvgpr_write_b32 a29, v89
v_accvgpr_write_b32 a30, v90
Mvav 168,136,112
buffer_store_dwordx4 a[200:203], v1, s[8:11], s36 offen offset:1024 sc1
v_accvgpr_write_b32 a31, v91
v_accvgpr_write_b32 a32, v96
v_accvgpr_write_b32 a33, v97
Mvav 168,140,120
v_accvgpr_write_b32 a34, v98
v_accvgpr_write_b32 a35, v99
v_accvgpr_write_b32 a36, v104
v_accvgpr_write_b32 a37, v105
Mvav 172,140,152
Ra 128,242,24576
Ra 132,242,25600
buffer_store_dwordx4 a[208:211], v1, s[8:11], s36 offen offset:2048 sc1
v_accvgpr_write_b32 a38, v106
Mvav 172,136,144
Ra 136,242,26624
Ra 140,242,27648
v_accvgpr_write_b32 a39, v107
W 4
Cvav 188,144,128,236
v_accvgpr_write_b32 a40, v112
v_accvgpr_write_b32 a41, v113
v_accvgpr_write_b32 a42, v114
v_accvgpr_write_b32 a43, v115
v_accvgpr_write_b32 a44, v120
v_accvgpr_write_b32 a45, v121
v_accvgpr_write_b32 a46, v122
v_accvgpr_write_b32 a47, v123
v_accvgpr_write_b32 a48, v128
v_accvgpr_write_b32 a49, v129
v_accvgpr_write_b32 a50, v130
v_accvgpr_write_b32 a51, v131
v_accvgpr_write_b32 a52, v136
v_accvgpr_write_b32 a53, v137
v_accvgpr_write_b32 a54, v138
v_accvgpr_write_b32 a55, v139
v_accvgpr_write_b32 a56, v144
v_accvgpr_write_b32 a57, v145
v_accvgpr_write_b32 a58, v146
v_accvgpr_write_b32 a59, v147
v_accvgpr_write_b32 a60, v152
v_accvgpr_write_b32 a61, v153
v_accvgpr_write_b32 a62, v154
v_accvgpr_write_b32 a63, v155
Mvav 188,148,136
s_barrier
buffer_store_dwordx4 a[216:219], v1, s[8:11], s36 offen offset:3072 sc1
s_add_u32 s36, s29, 69632
v_add_u32 v8, s80, v1
A 196,196,224
Mvav 188,152,144
A 197,197,224
)"
R"(A 198,198,224
A 199,199,224
X 196
X 197
X 198
K 192,196,197
X 199
A 204,204,228
K 193,198,199
A 205,205,228
A 206,206,228
A 207,207,228
X 204
X 205
X 206
K 200,204,205
X 207
A 212,212,232
K 201,206,207
A 213,213,232
A 214,214,232
A 215,215,232
X 212
X 213
X 214
K 208,212,213
X 215
A 220,220,236
K 209,214,215
A 221,221,236
A 222,222,236
A 223,223,236
X 220
X 221
X 222
K 216,220,221
X 223
v_mov_b32 v194, 0
K 217,222,223
v_mov_b32 v195, 0
v_cndmask_b32_e64 v68, v241, v68, s[72:73]
v_cndmask_b32_e64 v69, v241, v69, s[74:75]
v_cndmask_b32_e64 v70, v241, v70, s[76:77]
v_cndmask_b32_e64 v71, v241, v71, s[78:79]
A 68,68,228
A 69,69,228
A 70,70,228
A 71,71,228
X 68
X 69
X 70
K 202,68,69
X 71
A 100,100,232
K 203,70,71
A 101,101,232
A 102,102,232
A 103,103,232
X 100
X 101
X 102
K 210,100,101
X 103
A 132,132,236
K 211,102,103
A 133,133,236
A 134,134,236
A 135,135,236
X 132
X 133
X 134
K 218,132,133
X 135
s_nop 0
K 219,134,135
Mvav 188,156,152
W 0
buffer_store_dwordx4 a[224:227], v1, s[8:11], s36 offen offset:0 sc1
Ra 144,242,28672
Ra 148,242,29696
Ra 152,242,30720
Zvav 32,128,192
buffer_store_dwordx4 a[232:235], v1, s[8:11], s36 offen offset:1024 sc1
Ra 156,242,31744
Zvav 64,128,200
buffer_store_dwordx4 a[240:243], v1, s[8:11], s36 offen offset:2048 sc1
v_xor_b32 v242, 0x10000, v242
Zvav 96,128,208
s_and_b32 s80, s14, 1
s_xor_b32 s62, s62, 0x10000
buffer_store_dwordx4 a[248:251], v1, s[8:11], s36 offen offset:3072 sc1
s_add_u32 s36, s29, 393216
Zvav 128,128,216
s_lshl_b32 s80, s80, 14
s_add_u32 s80, s80, 131072
buffer_store_dwordx4 a[196:199], v1, s[8:11], s36 offen offset:0 sc1
v_add_u32 v23, 61440, v21
Zvav 132,132,216
buffer_store_dwordx4 a[204:207], v1, s[8:11], s36 offen offset:1024 sc1
v_add_u32 v12, s80, v1
Ra 64,12,8192
v_add_u32 v24, 61440, v22
Zvav 100,132,208
buffer_store_dwordx4 a[212:215], v1, s[8:11], s36 offen offset:2048 sc1
Ra 68,12,9216
Ra 72,12,10240
v_add_u32 v25, 122880, v21
Zvav 68,132,200
buffer_store_dwordx4 a[220:223], v1, s[8:11], s36 offen offset:3072 sc1
s_add_u32 s36, s29, 397312
Ra 76,12,11264
Ra 80,12,12288
Ra 84,12,13312
Zvav 36,132,192
buffer_store_dwordx4 a[228:231], v1, s[8:11], s36 offen offset:0 sc1
Ra 88,12,14336
Ra 92,12,15360
v_add_u32 v26, 122880, v22
Zvav 40,136,192
buffer_store_dwordx4 a[236:239], v1, s[8:11], s36 offen offset:1024 sc1
Ra 96,12,0
Ra 100,12,1024
Ra 104,12,2048
Zvav 72,136,200
Ra 108,12,3072
Ra 112,12,4096
buffer_store_dwordx4 a[244:247], v1, s[8:11], s36 offen offset:2048 sc1
Ra 116,12,5120
Zvav 104,136,208
Ra 120,12,6144
Ra 124,12,7168
buffer_store_dwordx4 a[252:255], v1, s[8:11], s36 offen offset:3072 sc1
Zvav 136,136,216
Zvav 140,140,216
Zvav 108,140,208
Zvav 76,140,200
s_waitcnt vmcnt(16) lgkmcnt(0)
s_barrier
ds_read_b128 v[248:251], v8
Zvav 44,140,192
Ra 128,242,40960
Zvav 48,144,192
Zvav 80,144,200
X 172
X 173
Zvav 112,144,208
Ra 132,242,41984
X 174
K 220,172,173
Zvav 144,144,216
X 175
v_cndmask_b32_e64 v168, v241, v168, s[72:73]
K 221,174,175
Zvav 148,148,216
v_cndmask_b32_e64 v169, v241, v169, s[74:75]
v_cndmask_b32_e64 v170, v241, v170, s[76:77]
v_cndmask_b32_e64 v171, v241, v171, s[78:79]
Zvav 116,148,208
X 168
X 169
Zvav 84,148,200
W 2
Ra 136,242,43008
X 170
K 212,168,169
Zvav 52,148,192
v_mov_b32 v224, v250
v_mov_b32 v225, v250
v_mov_b32 v226, v250
v_mov_b32 v227, v250
Zvav 56,152,192
v_mov_b32 v228, v251
v_mov_b32 v229, v251
v_mov_b32 v230, v251
v_mov_b32 v231, v251
Zvav 88,152,200
v_mov_b32 v232, v248
v_mov_b32 v233, v248
v_mov_b32 v234, v248
v_mov_b32 v235, v248
Zvav 120,152,208
v_mov_b32 v236, v249
v_mov_b32 v237, v249
v_mov_b32 v238, v249
v_mov_b32 v239, v249
Zvav 152,152,216
X 171
v_mov_b32 v204, 0
K 213,170,171
Zvav 156,156,216
Ra 140,242,44032
v_mov_b32 v205, 0
v_mov_b32 v196, 0
v_mov_b32 v197, 0
Zvav 124,156,208
v_mov_b32 v198, 0
v_mov_b32 v199, 0
v_mov_b32 v206, 0
v_mov_b32 v207, 0
Zvav 92,156,200
v_mov_b32 v214, 0
v_mov_b32 v215, 0
v_cndmask_b32_e64 v188, v241, v188, s[72:73]
v_cndmask_b32_e64 v189, v241, v189, s[74:75]
Zvav 60,156,192
v_cndmask_b32_e64 v190, v241, v190, s[76:77]
v_cndmask_b32_e64 v191, v241, v191, s[78:79]
X 188
X 189
X 190
K 222,188,189
X 191
s_nop 0
K 223,190,191
s_waitcnt vmcnt(0)
s_add_u32 s80, s71, 128
v_mov_b64 v[28:29], s[12:13]
s_mov_b64 exec, 1
buffer_store_dwordx2 v[28:29], off, s[8:11], s80 sc1
s_mov_b64 exec, -1
s_nop 1
s_branch .Lsp2_37%=
.p2align 12
.Lpe_48%=:
v_pk_mul_f32 v[32:33], v[32:33], s[22:23]
v_pk_mul_f32 v[34:35], v[34:35], s[22:23]
v_pk_mul_f32 v[36:37], v[36:37], s[22:23]
v_pk_mul_f32 v[38:39], v[38:39], s[22:23]
v_pk_mul_f32 v[64:65], v[64:65], s[22:23]
v_pk_mul_f32 v[66:67], v[66:67], s[22:23]
v_pk_mul_f32 v[68:69], v[68:69], s[22:23]
v_pk_mul_f32 v[70:71], v[70:71], s[22:23]
v_pk_mul_f32 v[96:97], v[96:97], s[22:23]
v_pk_mul_f32 v[98:99], v[98:99], s[22:23]
v_pk_mul_f32 v[100:101], v[100:101], s[22:23]
v_pk_mul_f32 v[102:103], v[102:103], s[22:23]
v_pk_mul_f32 v[128:129], v[128:129], s[22:23]
v_pk_mul_f32 v[130:131], v[130:131], s[22:23]
v_pk_mul_f32 v[132:133], v[132:133], s[22:23]
v_pk_mul_f32 v[134:135], v[134:135], s[22:23]
v_pk_mul_f32 v[40:41], v[40:41], s[22:23]
v_pk_mul_f32 v[42:43], v[42:43], s[22:23]
v_pk_mul_f32 v[44:45], v[44:45], s[22:23]
v_pk_mul_f32 v[46:47], v[46:47], s[22:23]
v_pk_mul_f32 v[72:73], v[72:73], s[22:23]
v_pk_mul_f32 v[74:75], v[74:75], s[22:23]
v_pk_mul_f32 v[76:77], v[76:77], s[22:23]
v_pk_mul_f32 v[78:79], v[78:79], s[22:23]
v_pk_mul_f32 v[104:105], v[104:105], s[22:23]
v_pk_mul_f32 v[106:107], v[106:107], s[22:23]
v_pk_mul_f32 v[108:109], v[108:109], s[22:23]
v_pk_mul_f32 v[110:111], v[110:111], s[22:23]
v_pk_mul_f32 v[136:137], v[136:137], s[22:23]
v_pk_mul_f32 v[138:139], v[138:139], s[22:23]
v_pk_mul_f32 v[140:141], v[140:141], s[22:23]
v_pk_mul_f32 v[142:143], v[142:143], s[22:23]
v_pk_mul_f32 v[48:49], v[48:49], s[22:23]
v_pk_mul_f32 v[50:51], v[50:51], s[22:23]
v_pk_mul_f32 v[52:53], v[52:53], s[22:23]
v_pk_mul_f32 v[54:55], v[54:55], s[22:23]
v_pk_mul_f32 v[80:81], v[80:81], s[22:23]
v_pk_mul_f32 v[82:83], v[82:83], s[22:23]
v_pk_mul_f32 v[84:85], v[84:85], s[22:23]
v_pk_mul_f32 v[86:87], v[86:87], s[22:23]
v_pk_mul_f32 v[112:113], v[112:113], s[22:23]
v_pk_mul_f32 v[114:115], v[114:115], s[22:23]
v_pk_mul_f32 v[116:117], v[116:117], s[22:23]
v_pk_mul_f32 v[118:119], v[118:119], s[22:23]
v_pk_mul_f32 v[144:145], v[144:145], s[22:23]
v_pk_mul_f32 v[146:147], v[146:147], s[22:23]
v_pk_mul_f32 v[148:149], v[148:149], s[22:23]
v_pk_mul_f32 v[150:151], v[150:151], s[22:23]
v_pk_mul_f32 v[56:57], v[56:57], s[22:23]
v_pk_mul_f32 v[58:59], v[58:59], s[22:23]
v_pk_mul_f32 v[60:61], v[60:61], s[22:23]
v_pk_mul_f32 v[62:63], v[62:63], s[22:23]
v_pk_mul_f32 v[88:89], v[88:89], s[22:23]
v_pk_mul_f32 v[90:91], v[90:91], s[22:23]
v_pk_mul_f32 v[92:93], v[92:93], s[22:23]
v_pk_mul_f32 v[94:95], v[94:95], s[22:23]
v_pk_mul_f32 v[120:121], v[120:121], s[22:23]
v_pk_mul_f32 v[122:123], v[122:123], s[22:23]
v_pk_mul_f32 v[124:125], v[124:125], s[22:23]
v_pk_mul_f32 v[126:127], v[126:127], s[22:23]
v_pk_mul_f32 v[152:153], v[152:153], s[22:23]
v_pk_mul_f32 v[154:155], v[154:155], s[22:23]
v_pk_mul_f32 v[156:157], v[156:157], s[22:23]
v_pk_mul_f32 v[158:159], v[158:159], s[22:23]
K 32,32,33
K 33,34,35
K 34,36,37
K 35,38,39
K 40,40,41
K 41,42,43
K 42,44,45
K 43,46,47
W 0
Zvav 196,128,32
s_add_u32 m0, s62, 0
Ra 144,242,4096
Ra 148,242,5120
Ra 152,242,6144
Ra 156,242,7168
Mvav 196,132,40
K 64,64,65
K 65,66,67
K 66,68,69
K 67,70,71
K 72,72,73
K 73,74,75
K 74,76,77
K 75,78,79
s_nop 1
Zvav 204,132,72
Dc 1,8,61,0
K 96,96,97
K 97,98,99
K 98,100,101
Mvav 204,128,64
K 99,102,103
K 104,104,105
K 105,106,107
K 106,108,109
K 107,110,111
Zvav 212,128,96
K 128,128,129
K 129,130,131
K 130,132,133
K 131,134,135
Mvav 212,132,104
K 136,136,137
K 137,138,139
K 138,140,141
K 139,142,143
s_nop 1
Zvav 220,132,136
Dc 1,8,61,1024
K 48,48,49
K 49,50,51
K 50,52,53
Mvav 220,128,128
K 51,54,55
K 56,56,57
K 57,58,59
K 58,60,61
K 59,62,63
Mvav 196,136,48
Ra 128,242,8192
Ra 132,242,9216
K 80,80,81
K 81,82,83
Mvav 196,140,56
K 82,84,85
K 83,86,87
K 88,88,89
K 89,90,91
K 90,92,93
K 91,94,95
s_nop 1
Mvav 204,140,88
Dc 1,8,61,2048
K 112,112,113
K 113,114,115
K 114,116,117
Mvav 204,136,80
K 115,118,119
K 120,120,121
K 121,122,123
K 122,124,125
K 123,126,127
Mvav 212,136,112
K 144,144,145
K 145,146,147
K 146,148,149
K 147,150,151
Mvav 212,140,120
K 152,152,153
K 153,154,155
K 154,156,157
K 155,158,159
s_nop 1
Mvav 220,140,152
Dc 1,8,61,3072
s_add_u32 m0, s62, 4096
v_accvgpr_write_b32 a0, v32
v_accvgpr_write_b32 a1, v33
v_accvgpr_write_b32 a2, v34
Mvav 220,136,144
W 2
Ra 136,242,10240
v_accvgpr_write_b32 a3, v35
v_accvgpr_write_b32 a4, v40
Zvav 36,144,32
Ra 140,242,11264
v_accvgpr_write_b32 a5, v41
Mvav 36,148,40
v_accvgpr_write_b32 a6, v42
v_accvgpr_write_b32 a7, v43
v_max3_f32 v4, v196, v197, v198
v_max3_f32 v4, v4, v199, v252
)"
R"(Zvav 68,148,72
v_max3_f32 v5, v204, v205, v206
v_max3_f32 v5, v5, v207, v253
v_max3_f32 v6, v212, v213, v214
v_max3_f32 v6, v6, v215, v254
Mvav 68,144,64
v_max3_f32 v7, v220, v221, v222
v_max3_f32 v7, v7, v223, v255
v_mov_b32 v8, v4
v_mov_b32 v9, v5
Zvav 100,144,96
Dc 243,8,61,0
v_mov_b32 v10, v6
v_mov_b32 v11, v7
v_permlane32_swap_b32 v4, v8
Mvav 100,148,104
v_permlane32_swap_b32 v5, v9
v_permlane32_swap_b32 v6, v10
v_permlane32_swap_b32 v7, v11
v_max_f32 v4, v4, v8
Zvav 132,148,136
v_max_f32 v5, v5, v9
v_max_f32 v6, v6, v10
v_max_f32 v7, v7, v11
v_mov_b32 v8, v4
Mvav 132,144,128
v_mov_b32 v9, v5
v_mov_b32 v10, v6
v_mov_b32 v11, v7
v_accvgpr_write_b32 a8, v48
Mvav 36,152,48
v_permlane16_swap_b32 v4, v8
v_permlane16_swap_b32 v5, v9
v_permlane16_swap_b32 v6, v10
v_permlane16_swap_b32 v7, v11
Mvav 36,156,56
v_max_f32 v4, v4, v8
v_max_f32 v5, v5, v9
v_max_f32 v6, v6, v10
v_max_f32 v7, v7, v11
Mvav 68,156,88
v_sub_f32 v4, 0, v4
v_sub_f32 v5, 0, v5
v_sub_f32 v6, 0, v6
v_sub_f32 v7, 0, v7
Mvav 68,152,80
v_mov_b32 v224, v4
v_mov_b32 v225, v4
v_mov_b32 v226, v4
v_mov_b32 v227, v4
Mvav 100,152,112
v_mov_b32 v228, v5
v_mov_b32 v229, v5
v_mov_b32 v230, v5
v_mov_b32 v231, v5
Mvav 100,156,120
v_mov_b32 v232, v6
v_mov_b32 v233, v6
v_mov_b32 v234, v6
v_mov_b32 v235, v6
Mvav 132,156,152
v_mov_b32 v236, v7
v_mov_b32 v237, v7
v_mov_b32 v238, v7
v_mov_b32 v239, v7
Mvav 132,152,144
W 0
v_accvgpr_write_b32 a9, v49
v_accvgpr_write_b32 a10, v50
v_accvgpr_write_b32 a11, v51
v_accvgpr_write_b32 a12, v56
Cvav 160,128,32,224
s_and_b32 s80, s14, 1
Dc 243,8,61,1024
v_accvgpr_write_b32 a13, v57
v_accvgpr_write_b32 a14, v58
v_accvgpr_write_b32 a15, v59
Mvav 160,132,40
s_lshl_b32 s80, s80, 14
Ra 144,242,12288
Ra 148,242,13312
v_accvgpr_write_b32 a16, v64
v_accvgpr_write_b32 a17, v65
Cvav 164,132,72,228
Dc 243,8,61,2048
v_accvgpr_write_b32 a18, v66
v_accvgpr_write_b32 a19, v67
v_accvgpr_write_b32 a20, v72
Mvav 164,128,64
s_add_u32 s80, s80, 98304
v_accvgpr_write_b32 a21, v73
v_accvgpr_write_b32 a22, v74
v_accvgpr_write_b32 a23, v75
v_accvgpr_write_b32 a24, v80
Cvav 168,128,96,232
Dc 243,8,61,3072
s_add_u32 s61, s61, s63
v_accvgpr_write_b32 a25, v81
v_accvgpr_write_b32 a26, v82
A 196,196,224
Mvav 168,132,104
s_add_u32 s36, s29, 65536
v_accvgpr_write_b32 a27, v83
A 197,197,224
A 198,198,224
v_accvgpr_write_b32 a28, v88
Cvav 172,132,136,236
Ra 152,242,14336
buffer_store_dwordx4 a[192:195], v1, s[8:11], s36 offen offset:0 sc1
A 199,199,224
v_accvgpr_write_b32 a29, v89
Mvav 172,128,128
Ra 156,242,15360
X 196
v_accvgpr_write_b32 a30, v90
Mvav 160,136,48
X 197
v_accvgpr_write_b32 a31, v91
v_accvgpr_write_b32 a32, v96
Mvav 160,140,56
buffer_store_dwordx4 a[200:203], v1, s[8:11], s36 offen offset:1024 sc1
X 198
K 192,196,197
Mvav 164,140,88
X 199
v_accvgpr_write_b32 a33, v97
A 204,204,228
Mvav 164,136,80
v_accvgpr_write_b32 a34, v98
K 193,198,199
v_accvgpr_write_b32 a35, v99
A 205,205,228
Mvav 168,136,112
A 206,206,228
v_accvgpr_write_b32 a36, v104
v_add_u32 v8, s80, v1
A 207,207,228
Mvav 168,140,120
buffer_store_dwordx4 a[208:211], v1, s[8:11], s36 offen offset:2048 sc1
X 204
v_accvgpr_write_b32 a37, v105
Mvav 172,140,152
X 205
v_accvgpr_write_b32 a38, v106
v_accvgpr_write_b32 a39, v107
Mvav 172,136,144
W 0
X 206
v_accvgpr_write_b32 a40, v112
K 200,204,205
Cvav 176,144,32,224
Ra 128,242,24576
X 207
v_accvgpr_write_b32 a41, v113
Mvav 176,148,40
buffer_store_dwordx4 a[216:219], v1, s[8:11], s36 offen offset:3072 sc1
s_add_u32 s36, s29, 69632
Ra 132,242,25600
A 212,212,232
v_accvgpr_write_b32 a42, v114
Cvav 180,148,72,228
K 201,206,207
A 213,213,232
v_accvgpr_write_b32 a43, v115
A 214,214,232
Mvav 180,144,64
A 215,215,232
v_accvgpr_write_b32 a44, v120
X 212
Cvav 184,144,96,232
buffer_store_dwordx4 a[224:227], v1, s[8:11], s36 offen offset:0 sc1
v_accvgpr_write_b32 a45, v121
X 213
Mvav 184,148,104
v_accvgpr_write_b32 a46, v122
X 214
v_accvgpr_write_b32 a47, v123
Cvav 188,148,136,236
K 208,212,213
v_accvgpr_write_b32 a48, v128
X 215
Mvav 188,144,128
buffer_store_dwordx4 a[232:235], v1, s[8:11], s36 offen offset:1024 sc1
v_accvgpr_write_b32 a49, v129
A 220,220,236
v_accvgpr_write_b32 a50, v130
Mvav 176,152,48
K 209,214,215
A 221,221,236
v_accvgpr_write_b32 a51, v131
A 222,222,236
Mvav 176,156,56
Ra 136,242,26624
A 223,223,236
v_accvgpr_write_b32 a52, v136
v_accvgpr_write_b32 a53, v137
Mvav 180,156,88
Ra 140,242,27648
buffer_store_dwordx4 a[240:243], v1, s[8:11], s36 offen offset:2048 sc1
X 220
Mvav 180,152,80
X 221
v_accvgpr_write_b32 a54, v138
v_accvgpr_write_b32 a55, v139
Mvav 184,152,112
v_accvgpr_write_b32 a56, v144
v_accvgpr_write_b32 a57, v145
v_accvgpr_write_b32 a58, v146
v_accvgpr_write_b32 a59, v147
v_accvgpr_write_b32 a60, v152
v_accvgpr_write_b32 a61, v153
v_accvgpr_write_b32 a62, v154
v_accvgpr_write_b32 a63, v155
Mvav 184,156,120
s_barrier
buffer_store_dwordx4 a[248:251], v1, s[8:11], s36 offen offset:3072 sc1
X 222
K 216,220,221
Mvav 188,156,152
X 223
A 36,36,224
K 217,222,223
A 37,37,224
A 38,38,224
A 39,39,224
X 36
X 37
X 38
K 194,36,37
X 39
A 68,68,228
K 195,38,39
A 69,69,228
A 70,70,228
A 71,71,228
X 68
X 69
X 70
K 202,68,69
X 71
A 100,100,232
K 203,70,71
A 101,101,232
A 102,102,232
A 103,103,232
X 100
X 101
X 102
K 210,100,101
X 103
A 132,132,236
K 211,102,103
A 133,133,236
A 134,134,236
A 135,135,236
X 132
X 133
X 134
K 218,132,133
X 135
s_nop 0
K 219,134,135
Mvav 188,152,144
W 0
s_add_u32 s36, s29, 393216
buffer_store_dwordx4 a[196:199], v1, s[8:11], s36 offen offset:0 sc1
Ra 144,242,28672
Ra 148,242,29696
Zvav 32,128,192
buffer_store_dwordx4 a[204:207], v1, s[8:11], s36 offen offset:1024 sc1
Ra 152,242,30720
Ra 156,242,31744
Zvav 64,128,200
buffer_store_dwordx4 a[212:215], v1, s[8:11], s36 offen offset:2048 sc1
v_xor_b32 v242, 0x10000, v242
Zvav 96,128,208
s_and_b32 s80, s14, 1
s_xor_b32 s62, s62, 0x10000
buffer_store_dwordx4 a[220:223], v1, s[8:11], s36 offen offset:3072 sc1
s_add_u32 s36, s29, 397312
Zvav 128,128,216
s_lshl_b32 s80, s80, 14
s_add_u32 s80, s80, 131072
buffer_store_dwordx4 a[228:231], v1, s[8:11], s36 offen offset:0 sc1
v_add_u32 v23, 61440, v21
Zvav 132,132,216
v_add_u32 v12, s80, v1
Ra 64,12,8192
Ra 68,12,9216
v_add_u32 v24, 61440, v22
Zvav 100,132,208
Ra 72,12,10240
buffer_store_dwordx4 a[236:239], v1, s[8:11], s36 offen offset:1024 sc1
Ra 76,12,11264
v_add_u32 v25, 122880, v21
Zvav 68,132,200
Ra 80,12,12288
Ra 84,12,13312
Ra 88,12,14336
v_add_u32 v26, 122880, v22
Zvav 36,132,192
Ra 92,12,15360
Ra 96,12,0
Ra 100,12,1024
Ra 104,12,2048
Zvav 40,136,192
buffer_store_dwordx4 a[244:247], v1, s[8:11], s36 offen offset:2048 sc1
Ra 108,12,3072
Ra 112,12,4096
Ra 116,12,5120
Zvav 72,136,200
Ra 120,12,6144
Ra 124,12,7168
X 172
Zvav 104,136,208
X 173
X 174
Zvav 136,136,216
buffer_store_dwordx4 a[252:255], v1, s[8:11], s36 offen offset:3072 sc1
K 220,172,173
X 175
Zvav 140,140,216
X 168
K 221,174,175
Zvav 108,140,208
X 169
X 170
Zvav 76,140,200
s_waitcnt vmcnt(16) lgkmcnt(0)
s_barrier
ds_read_b128 v[248:251], v8
K 212,168,169
Zvav 44,140,192
X 171
X 164
Zvav 48,144,192
Ra 128,242,0
K 213,170,171
X 165
Zvav 80,144,200
X 166
Zvav 112,144,208
Ra 132,242,1024
K 204,164,165
X 167
Zvav 144,144,216
X 160
Zvav 148,148,216
K 205,166,167
X 161
Zvav 116,148,208
Ra 136,242,2048
X 162
Zvav 84,148,200
W 3
K 196,160,161
X 163
Zvav 52,148,192
X 176
K 197,162,163
Zvav 56,152,192
Ra 140,242,3072
X 177
Zvav 88,152,200
X 178
K 198,176,177
Zvav 120,152,208
X 179
X 180
Zvav 152,152,216
K 199,178,179
Zvav 156,156,216
X 181
X 182
Zvav 124,156,208
K 206,180,181
X 183
Zvav 92,156,200
K 207,182,183
Zvav 60,156,192
s_waitcnt vmcnt(0)
s_add_u32 s80, s71, 128
v_mov_b64 v[28:29], s[12:13]
s_mov_b64 exec, 1
buffer_store_dwordx2 v[28:29], off, s[8:11], s80 sc1
s_mov_b64 exec, -1
s_branch .Lsp1_36%=
.p2align 12
.Lmoe_44%=:
W 0
Cvaa 160,128,0,224
s_cmp_eq_u32 s14, 0
s_cselect_b32 s20, s30, s32
s_cselect_b32 s21, s31, s33
Ra 144,242,4096
Ra 148,242,5120
Mvaa 160,132,4
s_xor_b32 s62, s62, 0x10000
Ra 152,242,6144
Cvaa 164,132,20,228
)"
R"(X 184
Mvaa 164,128,16
s_and_b32 s21, s21, 0xffff
X 185
Cvaa 168,128,32,232
X 186
Mvaa 168,132,36
Ra 156,242,7168
K 214,184,185
Cvaa 172,132,52,236
s_mov_b32 s22, 2211840
X 187
Mvaa 172,128,48
X 188
Mvaa 160,136,8
Ra 128,242,16384
Mvaa 160,140,12
s_mov_b32 s23, 0x20000
K 215,186,187
Mvaa 164,140,28
X 189
Mvaa 164,136,24
X 190
Mvaa 168,136,40
Ra 132,242,17408
v_lshrrev_b32 v4, 6, v1
Mvaa 168,140,44
K 222,188,189
Mvaa 172,140,60
X 191
Mvaa 172,136,56
v_mul_u32_u24 v4, 3456, v4
K 223,190,191
v_smfmac_f32_16x16x64_bf16 a[160:163], a[176:179], v[192:199], v240
Ra 136,242,18432
v_smfmac_f32_16x16x64_bf16 a[164:167], a[176:179], v[200:207], v240
X 160
v_smfmac_f32_16x16x64_bf16 a[168:171], a[176:179], v[208:215], v240
v_lshrrev_b32 v5, 4, v1
v_smfmac_f32_16x16x64_bf16 a[172:175], a[176:179], v[216:223], v240
W 3
X 161
Cvaa 176,144,0,224
X 162
Mvaa 176,148,4
Ra 140,242,19456
v_xor_b32 v5, v5, v1
Cvaa 180,148,20,228
K 192,160,161
Mvaa 180,144,16
X 163
Cvaa 184,144,32,232
v_and_b32 v5, 32, v5
Mvaa 184,148,36
X 164
Cvaa 188,148,52,236
K 193,162,163
v_and_b32 v6, 16, v1
Mvaa 188,144,48
X 165
Mvaa 176,152,8
X 166
Mvaa 176,156,12
Ra 144,242,20480
Ra 148,242,21504
Mvaa 180,156,28
v_add3_u32 v4, v4, v5, v6
K 200,164,165
Mvaa 180,152,24
X 167
Mvaa 184,152,40
X 168
Mvaa 184,156,44
v_add_u32 v5, 54272, v4
K 201,166,167
Mvaa 188,156,60
X 169
Mvaa 188,152,56
W 2
Ra 152,242,22528
v_add_u32 v6, 108544, v4
Mvav 32,128,196
X 170
Mvav 64,128,204
Ra 156,242,23552
v_add_u32 v7, 162816, v4
Mvav 96,128,212
K 208,168,169
Mvav 128,128,220
X 171
Mvav 132,132,220
s_lshl_b32 s94, s85, 6
X 172
Mvav 100,132,212
s_add_u32 s64, s64, 1
Ra 128,242,8192
K 209,170,171
Mvav 68,132,204
X 173
Mvav 36,132,196
s_mul_i32 s95, s14, 20480
Mvav 40,136,196
X 174
Mvav 72,136,204
Ra 132,242,9216
K 216,172,173
Mvav 104,136,212
s_add_u32 s95, s95, 98304
X 175
Mvav 136,136,220
Mvav 140,140,220
s_add_u32 m0, s95, 0
X 176
Mvav 108,140,212
Ra 136,242,10240
K 217,174,175
Mvav 76,140,204
X 177
Mvav 44,140,196
W 3
D 4,20,94,0
Mvav 48,144,196
X 178
Mvav 80,144,204
Ra 140,242,11264
Mvav 112,144,212
D 5,20,94,1024
K 194,176,177
Mvav 144,144,220
X 179
Mvav 148,148,220
X 180
Mvav 116,148,212
Ra 144,242,12288
D 6,20,94,2048
Mvav 84,148,204
K 195,178,179
Mvav 52,148,196
X 181
Mvav 56,152,196
D 7,20,94,3072
Ra 148,242,13312
Mvav 88,152,204
X 182
Mvav 120,152,212
s_add_u32 s94, s94, 221184
K 202,180,181
Mvav 152,152,220
X 183
Mvav 156,156,220
X 184
Mvav 124,156,212
s_add_u32 m0, s95, 4096
Ra 152,242,14336
Mvav 92,156,204
K 203,182,183
Mvav 60,156,196
W 3
X 185
Cvaa 160,128,0,224
D 4,20,94,0
Ra 156,242,15360
Mvaa 160,132,4
X 186
Cvaa 164,132,20,228
K 210,184,185
Mvaa 164,128,16
X 187
Cvaa 168,128,32,232
D 5,20,94,1024
Mvaa 168,132,36
X 188
Cvaa 172,132,52,236
D 6,20,94,2048
K 211,186,187
Mvaa 172,128,48
X 189
Mvaa 160,136,8
Ra 128,242,24576
Ra 132,242,25600
Mvaa 160,140,12
X 190
Mvaa 164,140,28
D 7,20,94,3072
K 218,188,189
Mvaa 164,136,24
s_add_u32 s94, s94, 221184
X 191
Mvaa 168,136,40
Mvaa 168,140,44
X 160
Mvaa 172,140,60
s_add_u32 m0, s95, 8192
K 219,190,191
Mvaa 172,136,56
W 2
X 161
Cvaa 176,144,0,224
Ra 136,242,26624
Ra 140,242,27648
Mvaa 176,148,4
X 162
Cvaa 180,148,20,228
D 4,20,94,0
K 196,160,161
Mvaa 180,144,16
X 163
Cvaa 184,144,32,232
D 5,20,94,1024
Mvaa 184,148,36
X 164
Cvaa 188,148,52,236
K 197,162,163
Mvaa 188,144,48
D 6,20,94,2048
Ra 144,242,28672
Mvaa 176,152,8
X 165
Mvaa 176,156,12
Ra 148,242,29696
D 7,20,94,3072
Mvaa 180,156,28
X 166
Mvaa 180,152,24
K 204,164,165
Mvaa 184,152,40
s_add_u32 s94, s94, 221184
X 167
Mvaa 184,156,44
X 168
Mvaa 188,156,60
Mvaa 188,152,56
W 2
s_add_u32 m0, s95, 12288
K 205,166,167
Mvav 32,128,192
X 169
Mvav 64,128,200
Ra 152,242,30720
Ra 156,242,31744
Mvav 96,128,208
X 170
Mvav 128,128,216
D 4,20,94,0
v_xor_b32 v242, 0x10000, v242
Mvav 132,132,216
K 212,168,169
Mvav 100,132,208
X 171
Mvav 68,132,200
D 5,20,94,1024
Mvav 36,132,192
X 172
Mvav 40,136,192
D 6,20,94,2048
K 213,170,171
Mvav 72,136,200
X 173
Mvav 104,136,208
X 174
Mvav 136,136,216
D 7,20,94,3072
K 220,172,173
Mvav 140,140,216
X 175
Mvav 108,140,208
s_add_u32 s94, s94, 221184
X 176
Mvav 76,140,200
W 0
s_barrier
Mvav 44,140,192
Ra 128,242,0
K 221,174,175
Mvav 48,144,192
X 177
Mvav 80,144,200
v_bfe_u32 v11, v1, 4, 3
Mvav 112,144,208
X 178
Mvav 144,144,216
Ra 132,242,1024
v_lshlrev_b32 v11, 3, v11
Mvav 148,148,216
K 198,176,177
Mvav 116,148,208
X 179
Mvav 84,148,200
X 180
Mvav 52,148,192
Ra 136,242,2048
buffer_load_dwordx2 v[8:9], v11, s[8:11], 0 offen offset:3840 sc1
Mvav 56,152,192
Ra 140,242,3072
K 199,178,179
Mvav 88,152,200
X 181
Mvav 120,152,208
X 182
Mvav 152,152,216
K 206,180,181
Mvav 156,156,216
X 183
Mvav 124,156,208
Mvav 92,156,200
K 207,182,183
Mvav 60,156,192
W 0
Cvaa 160,128,0,224
s_add_u32 m0, s95, 16384
Ra 144,242,4096
Ra 148,242,5120
Mvaa 160,132,4
s_xor_b32 s62, s62, 0x10000
Ra 152,242,6144
Cvaa 164,132,20,228
X 184
Mvaa 164,128,16
X 185
Cvaa 168,128,32,232
X 186
Mvaa 168,132,36
D 4,20,94,0
K 214,184,185
Cvaa 172,132,52,236
Ra 156,242,7168
Mvaa 172,128,48
X 187
Mvaa 160,136,8
X 188
Mvaa 160,140,12
D 5,20,94,1024
Ra 128,242,16384
Mvaa 164,140,28
K 215,186,187
Mvaa 164,136,24
X 189
Mvaa 168,136,40
X 190
Mvaa 168,140,44
D 6,20,94,2048
Ra 132,242,17408
Mvaa 172,140,60
K 222,188,189
Mvaa 172,136,56
X 191
v_smfmac_f32_16x16x64_bf16 a[160:163], a[176:179], v[192:199], v240
Ra 136,242,18432
K 223,190,191
v_smfmac_f32_16x16x64_bf16 a[164:167], a[176:179], v[200:207], v240
D 7,20,94,3072
v_smfmac_f32_16x16x64_bf16 a[168:171], a[176:179], v[208:215], v240
s_add_u32 s94, s94, 221184
v_mul_u32_u24 v10, 160, v11
v_smfmac_f32_16x16x64_bf16 a[172:175], a[176:179], v[216:223], v240
W 3
X 160
Cvaa 176,144,0,224
X 161
Mvaa 176,148,4
Ra 140,242,19456
v_lshrrev_b32 v12, 7, v1
Cvaa 180,148,20,228
X 162
Mvaa 180,144,16
K 192,160,161
v_lshl_add_u32 v10, v12, 4, v10
Cvaa 184,144,32,232
s_mul_i32 s82, s14, 5120
X 163
Mvaa 184,148,36
X 164
Cvaa 188,148,52,236
K 193,162,163
Mvaa 188,144,48
s_mul_i32 s81, s14, 640
X 165
Mvaa 176,152,8
X 166
Mvaa 176,156,12
s_add_u32 s81, s81, 118784
Ra 144,242,20480
Ra 148,242,21504
Mvaa 180,156,28
K 200,164,165
Mvaa 180,152,24
X 167
Mvaa 184,152,40
s_waitcnt vmcnt(4)
v_cmp_eq_u64 vcc, s[12:13], v[8:9]
s_cmp_eq_u64 vcc, -1
s_cbranch_scc0 .Lspin_51%=
.Lok_52%=:
X 168
Mvaa 184,156,44
K 201,166,167
Mvaa 188,156,60
s_add_u32 m0, s82, 53248
X 169
Mvaa 188,152,56
)"
R"(W 2
Ra 152,242,22528
Mvav 32,128,196
X 170
Mvav 64,128,204
Ra 156,242,23552
Dc 10,8,81,0
Mvav 96,128,212
K 208,168,169
Mvav 128,128,220
X 171
Mvav 132,132,220
s_add_u32 m0, s82, 54144
X 172
Mvav 100,132,212
s_add_u32 s64, s64, 1
Ra 128,242,8192
K 209,170,171
Mvav 68,132,204
X 173
Mvav 36,132,196
Dc 10,8,81,128
Mvav 40,136,196
X 174
Mvav 72,136,204
s_add_u32 m0, s82, 55040
Ra 132,242,9216
K 216,172,173
Mvav 104,136,212
X 175
Mvav 136,136,220
Dc 10,8,81,256
Mvav 140,140,220
X 176
Mvav 108,140,212
Ra 136,242,10240
K 217,174,175
Mvav 76,140,204
s_add_u32 m0, s82, 55936
X 177
Mvav 44,140,196
W 3
Mvav 48,144,196
X 178
Mvav 80,144,204
Ra 140,242,11264
Dc 10,8,81,384
Mvav 112,144,212
K 194,176,177
Mvav 144,144,220
s_add_u32 m0, s82, 56832
X 179
Mvav 148,148,220
X 180
Mvav 116,148,212
Ra 144,242,12288
Mvav 84,148,204
Dc 10,8,81,512
K 195,178,179
Mvav 52,148,196
X 181
Mvav 56,152,196
X 182
Mvav 88,152,204
Ra 148,242,13312
Mvav 120,152,212
K 202,180,181
Mvav 152,152,220
X 183
Mvav 156,156,220
X 184
Mvav 124,156,212
Ra 152,242,14336
Mvav 92,156,204
K 203,182,183
Mvav 60,156,196
W 3
X 185
Cvaa 160,128,0,224
X 186
Mvaa 160,132,4
Ra 156,242,15360
Cvaa 164,132,20,228
K 210,184,185
Mvaa 164,128,16
X 187
Cvaa 168,128,32,232
X 188
Mvaa 168,132,36
Cvaa 172,132,52,236
K 211,186,187
Mvaa 172,128,48
X 189
Mvaa 160,136,8
Ra 128,242,24576
Ra 132,242,25600
Mvaa 160,140,12
X 190
Mvaa 164,140,28
K 218,188,189
Mvaa 164,136,24
X 191
Mvaa 168,136,40
Mvaa 168,140,44
Mvaa 172,140,60
X 160
Mvaa 172,136,56
W 2
Ra 136,242,26624
K 219,190,191
Cvaa 176,144,0,224
X 161
Mvaa 176,148,4
Ra 140,242,27648
v_lshrrev_b32 v8, 8, v1
Cvaa 180,148,20,228
X 162
Mvaa 180,144,16
K 196,160,161
Cvaa 184,144,32,232
X 163
Mvaa 184,148,36
v_bfe_u32 v9, v1, 6, 2
Cvaa 188,148,52,236
X 164
Mvaa 188,144,48
K 197,162,163
v_lshl_add_u32 v9, v8, 3, v9
Mvaa 176,152,8
X 165
Mvaa 176,156,12
Ra 144,242,28672
Ra 148,242,29696
Mvaa 180,156,28
X 166
Mvaa 180,152,24
K 204,164,165
v_lshlrev_b32 v11, 6, v9
Mvaa 184,152,40
X 167
Mvaa 184,156,44
X 168
Mvaa 188,156,60
v_xor_b32 v9, s14, v8
Mvaa 188,152,56
W 2
Ra 152,242,30720
K 205,166,167
Mvav 32,128,192
X 169
Mvav 64,128,200
Ra 156,242,31744
v_and_b32 v9, 1, v9
Mvav 96,128,208
X 170
Mvav 128,128,216
v_xor_b32 v242, 0x10000, v242
K 212,168,169
Mvav 132,132,216
X 171
Mvav 100,132,208
v_lshl_add_u32 v11, v9, 5, v11
Mvav 68,132,200
X 172
Mvav 36,132,192
K 213,170,171
Mvav 40,136,192
X 173
Mvav 72,136,200
v_bfe_u32 v9, v1, 4, 2
Mvav 104,136,208
X 174
Mvav 136,136,216
K 220,172,173
v_lshl_add_u32 v12, v9, 3, v11
Mvav 140,140,216
X 175
Mvav 108,140,208
X 176
Mvav 76,140,200
s_waitcnt vmcnt(0) lgkmcnt(0)
s_barrier
Mvav 44,140,192
v_add_u32 v11, 98304, v12
K 221,174,175
Mvav 48,144,192
Ra 128,242,0
Mvav 80,144,200
X 177
Mvav 112,144,208
X 178
Mvav 144,144,216
Ra 132,242,1024
v_and_b32 v13, 0x70, v1
Mvav 148,148,216
K 198,176,177
Mvav 116,148,208
X 179
Mvav 84,148,200
Ra 136,242,2048
v_lshl_add_u32 v13, v8, 7, v13
Mvav 52,148,192
X 180
Mvav 56,152,192
Ra 140,242,3072
K 199,178,179
Mvav 88,152,200
X 181
Mvav 120,152,208
X 182
Mvav 152,152,216
K 206,180,181
Mvav 156,156,216
X 183
Mvav 124,156,208
Mvav 92,156,200
K 207,182,183
Mvav 60,156,192
W 0
Cvaa 160,128,0,224
s_cmp_eq_u32 s14, 0
s_cselect_b32 s95, 106496, 0
Ra 144,242,4096
Ra 148,242,5120
Mvaa 160,132,4
s_xor_b32 s62, s62, 0x10000
X 184
Cvaa 164,132,20,228
Ta 184,11,0
Ta 186,11,256
Mvaa 164,128,16
s_add_u32 s95, s95, 32768
Ta 188,11,20480
Ta 190,11,20736
Cvaa 168,128,32,232
Ra 192,13,53248
Ta 196,11,2048
Mvaa 168,132,36
s_add_u32 m0, s95, 0
X 185
Cvaa 172,132,52,236
Ta 198,11,2304
Ta 200,11,22528
Mvaa 172,128,48
W 3
X 186
Zaaa 232,184,192
Ta 202,11,22784
K 214,184,185
Zaaa 236,188,192
Ra 204,13,53760
Ra 152,242,6144
Mvaa 160,136,8
X 187
Mvaa 160,140,12
Ta 208,11,4096
D 4,20,94,0
Mvaa 164,140,28
X 188
Mvaa 164,136,24
Ta 210,11,4352
D 5,20,94,1024
Mvaa 168,136,40
Ra 156,242,7168
K 215,186,187
Mvaa 168,140,44
X 189
Mvaa 172,140,60
Ta 212,11,24576
D 6,20,94,2048
Mvaa 172,136,56
X 190
v_smfmac_f32_16x16x64_bf16 a[160:163], a[176:179], v[192:199], v240
W 5
Ta 214,11,24832
D 7,20,94,3072
Maaa 232,196,204
s_add_u32 s94, s94, 221184
Ra 216,13,54272
K 222,188,189
Maaa 236,200,204
s_add_u32 m0, s95, 4096
X 191
v_smfmac_f32_16x16x64_bf16 a[164:167], a[176:179], v[200:207], v240
X 160
K 223,190,191
v_smfmac_f32_16x16x64_bf16 a[168:171], a[176:179], v[208:215], v240
Ra 128,242,16384
Ta 220,11,6144
v_smfmac_f32_16x16x64_bf16 a[172:175], a[176:179], v[216:223], v240
W 5
Ta 222,11,6400
Ra 132,242,17408
Cvaa 176,144,0,224
X 161
Mvaa 176,148,4
D 4,20,94,0
Ta 224,11,26624
Cvaa 180,148,20,228
X 162
Mvaa 180,144,16
D 5,20,94,1024
K 192,160,161
Cvaa 184,144,32,232
Ta 226,11,26880
Ra 228,13,54784
Mvaa 184,148,36
W 7
X 163
Maaa 232,208,216
D 6,20,94,2048
Ra 136,242,18432
Maaa 236,212,216
X 164
Cvaa 188,148,52,236
D 7,20,94,3072
Ta 184,11,8192
Mvaa 188,144,48
s_add_u32 s94, s94, 221184
Ra 140,242,19456
K 193,162,163
Mvaa 176,152,8
s_add_u32 m0, s95, 8192
X 165
Mvaa 176,156,12
Ta 186,11,8448
Ta 188,11,28672
Mvaa 180,156,28
X 166
Mvaa 180,152,24
Ra 144,242,20480
K 200,164,165
Mvaa 184,152,40
X 167
Mvaa 184,156,44
Ta 190,11,28928
D 4,20,94,0
Mvaa 188,156,60
W 7
X 168
Maaa 232,220,228
s_add_u32 s64, s64, 1
Ra 192,13,55296
D 5,20,94,1024
Maaa 236,224,228
Ta 196,11,10240
K 201,166,167
Mvaa 188,152,56
W 6
X 169
Mvav 32,128,196
D 6,20,94,2048
Ra 148,242,21504
Mvav 64,128,204
X 170
Mvav 96,128,212
Ta 198,11,10496
D 7,20,94,3072
Mvav 128,128,220
s_add_u32 s94, s94, 221184
Ta 200,11,30720
K 208,168,169
Mvav 132,132,220
s_add_u32 m0, s95, 12288
X 171
Mvav 100,132,212
Ra 152,242,22528
Ta 202,11,30976
Mvav 68,132,204
X 172
Mvav 36,132,196
W 6
Ra 204,13,55808
K 209,170,171
Maaa 232,184,192
X 173
Maaa 236,188,192
D 4,20,94,0
Ra 156,242,23552
Mvav 40,136,196
X 174
Mvav 72,136,204
Ta 208,11,12288
D 5,20,94,1024
Mvav 104,136,212
Ta 210,11,12544
K 216,172,173
Mvav 136,136,220
X 175
Mvav 140,140,220
D 6,20,94,2048
Ra 128,242,8192
Mvav 108,140,212
Ta 212,11,32768
Ta 214,11,33024
Mvav 76,140,204
X 176
Mvav 44,140,196
W 5
D 7,20,94,3072
)"
R"(K 217,174,175
Mvav 48,144,196
s_add_u32 s94, s94, 221184
X 177
Maaa 232,196,204
s_add_u32 m0, s95, 16384
Ra 132,242,9216
Ra 216,13,56320
Maaa 236,200,204
X 178
Mvav 80,144,204
Ta 220,11,14336
K 194,176,177
Mvav 112,144,212
X 179
Mvav 144,144,220
D 4,20,94,0
Ta 222,11,14592
Mvav 148,148,220
Ra 136,242,10240
Ta 224,11,34816
Mvav 116,148,212
X 180
Mvav 84,148,204
D 5,20,94,1024
K 195,178,179
Mvav 52,148,196
X 181
Mvav 56,152,196
D 6,20,94,2048
Ta 226,11,35072
Mvav 88,152,204
W 5
Ra 140,242,11264
Ra 228,13,56832
Maaa 232,208,216
X 182
Maaa 236,212,216
D 7,20,94,3072
K 202,180,181
Mvav 120,152,212
s_add_u32 s94, s94, 221184
X 183
Mvav 152,152,220
Ta 184,11,16384
Ra 144,242,12288
Mvav 156,156,220
X 184
Mvav 124,156,212
Ta 186,11,16640
K 203,182,183
Mvav 92,156,204
X 185
Mvav 60,156,196
W 4
Ta 188,11,36864
Ra 148,242,13312
Cvaa 160,128,0,224
X 186
Mvaa 160,132,4
Ta 190,11,37120
K 210,184,185
Cvaa 164,132,20,228
W 6
Ra 192,13,57344
Ta 196,11,18432
Maaa 232,220,228
X 187
Maaa 236,224,228
X 188
Mvaa 164,128,16
Ra 152,242,14336
K 211,186,187
Cvaa 168,128,32,232
Ta 198,11,18688
Ta 200,11,38912
Mvaa 168,132,36
X 189
Cvaa 172,132,52,236
X 190
Mvaa 172,128,48
Ra 156,242,15360
K 218,188,189
Mvaa 160,136,8
Ta 202,11,39168
Ra 204,13,57856
Mvaa 160,140,12
X 191
Mvaa 164,140,28
Ra 128,242,24576
Mvaa 164,136,24
W 8
Maaa 232,184,192
Ra 132,242,25600
Maaa 236,188,192
X 160
Mvaa 168,136,40
K 219,190,191
Mvaa 168,140,44
X 161
Mvaa 172,140,60
X 162
Mvaa 172,136,56
W 4
Ra 136,242,26624
K 196,160,161
Cvaa 176,144,0,224
X 163
Mvaa 176,148,4
X 164
Cvaa 180,148,20,228
Ra 140,242,27648
K 197,162,163
Mvaa 180,144,16
X 165
Cvaa 184,144,32,232
W 4
Maaa 232,196,204
X 166
Maaa 236,200,204
Mvaa 184,148,36
K 204,164,165
Cvaa 188,148,52,236
X 167
Mvaa 188,144,48
Ra 144,242,28672
Mvaa 176,152,8
X 168
Mvaa 176,156,12
Ra 148,242,29696
Mvaa 180,156,28
K 205,166,167
Mvaa 180,152,24
X 169
Mvaa 184,152,40
Mvaa 184,156,44
X 170
Mvaa 188,156,60
K 212,168,169
Mvaa 188,152,56
W 2
X 171
Mvav 32,128,192
Ra 152,242,30720
Ra 156,242,31744
Mvav 64,128,200
X 172
Mvav 96,128,208
v_xor_b32 v242, 0x10000, v242
Mvav 128,128,216
K 213,170,171
Mvav 132,132,216
X 173
Mvav 100,132,208
Mvav 68,132,200
X 174
Mvav 36,132,192
Mvav 40,136,192
K 220,172,173
Mvav 72,136,200
X 175
Mvav 104,136,208
Mvav 136,136,216
X 176
Mvav 140,140,216
Mvav 108,140,208
K 221,174,175
Mvav 76,140,200
s_waitcnt vmcnt(0) lgkmcnt(0)
s_barrier
Mvav 44,140,192
X 177
Mvav 48,144,192
X 178
Mvav 80,144,200
Ra 128,242,0
K 198,176,177
Mvav 112,144,208
X 179
Mvav 144,144,216
Ra 132,242,1024
Mvav 148,148,216
X 180
Mvav 116,148,208
Ra 136,242,2048
Mvav 84,148,200
K 199,178,179
Mvav 52,148,192
X 181
Mvav 56,152,192
Ra 140,242,3072
Mvav 88,152,200
X 182
Mvav 120,152,208
Mvav 152,152,216
K 206,180,181
Mvav 156,156,216
X 183
Mvav 124,156,208
Mvav 92,156,200
K 207,182,183
Mvav 60,156,192
W 0
Cvaa 160,128,0,224
s_mov_b32 s20, s34
Ra 144,242,4096
Ra 148,242,5120
Mvaa 160,132,4
s_xor_b32 s62, s62, 0x10000
X 184
Cvaa 164,132,20,228
s_and_b32 s21, s35, 0xffff
Ta 184,11,40960
Ta 186,11,41216
Mvaa 164,128,16
Ta 188,12,32768
Ta 190,12,33024
Cvaa 168,128,32,232
s_mov_b32 s22, 2211840
Ra 192,13,58368
Ta 196,11,43008
Mvaa 168,132,36
s_mov_b32 s23, 0x20000
X 185
Cvaa 172,132,52,236
Ta 198,11,43264
Ta 200,12,34816
Mvaa 172,128,48
W 3
X 186
Maaa 232,184,192
Ta 202,12,35072
K 214,184,185
Maaa 236,188,192
Ra 204,13,58880
Ra 152,242,6144
Mvaa 160,136,8
X 187
Mvaa 160,140,12
Ta 208,11,45056
v_lshrrev_b32 v4, 8, v1
Mvaa 164,140,28
X 188
Mvaa 164,136,24
Ta 210,11,45312
v_lshlrev_b32 v5, 5, v4
Mvaa 168,136,40
v_xor_b32 v5, v5, v1
K 215,186,187
Mvaa 168,140,44
Ra 156,242,7168
Ta 212,12,36864
Mvaa 172,140,60
X 189
Mvaa 172,136,56
v_and_b32 v5, 0xf0, v5
v_mul_u32_u24 v4, 1280, v4
v_smfmac_f32_16x16x64_bf16 a[160:163], a[176:179], v[192:199], v240
W 5
X 190
Maaa 232,196,204
Ta 214,12,37120
v_add_u32 v4, v4, v5
Maaa 236,200,204
Ra 216,13,59392
K 222,188,189
v_smfmac_f32_16x16x64_bf16 a[164:167], a[176:179], v[200:207], v240
X 191
X 160
K 223,190,191
v_smfmac_f32_16x16x64_bf16 a[168:171], a[176:179], v[208:215], v240
Ra 128,242,16384
v_xor_b32 v5, 128, v4
v_smfmac_f32_16x16x64_bf16 a[172:175], a[176:179], v[216:223], v240
W 4
Ta 220,11,47104
v_add_u32 v6, 8192, v4
Cvaa 176,144,0,224
Ta 222,11,47360
v_add_u32 v7, 12288, v5
Mvaa 176,148,4
Ra 132,242,17408
v_add_u32 v5, 4096, v5
Cvaa 180,148,20,228
s_mul_i32 s94, s85, 40960
X 161
Mvaa 180,144,16
s_mul_i32 s81, s14, 20480
Ta 224,12,38912
Ta 226,12,39168
Cvaa 184,144,32,232
s_add_u32 s94, s94, s81
X 162
Mvaa 184,148,36
W 6
s_lshl_b32 s95, s14, 12
K 192,160,161
Maaa 232,208,216
s_add_u32 s95, s95, 98304
X 163
Maaa 236,212,216
s_add_u32 m0, s95, 0
Ra 228,13,59904
Ra 136,242,18432
Cvaa 188,148,52,236
X 164
Mvaa 188,144,48
Ta 184,11,49152
K 193,162,163
Mvaa 176,152,8
X 165
Mvaa 176,156,12
Ra 140,242,19456
Ta 186,11,49408
Mvaa 180,156,28
D 4,20,94,0
Ta 188,12,40960
Mvaa 180,152,24
X 166
Mvaa 184,152,40
D 5,20,94,1024
K 200,164,165
Mvaa 184,156,44
X 167
Mvaa 188,156,60
W 5
D 6,20,94,2048
Ra 144,242,20480
Maaa 232,220,228
s_add_u32 s64, s64, 1
Ta 190,12,41216
Ra 192,13,60416
Maaa 236,224,228
D 7,20,94,3072
Ta 196,11,51200
Mvaa 188,152,56
W 6
X 168
Mvav 32,128,196
s_add_u32 s94, s94, 256
s_add_u32 m0, s95, 8192
K 201,166,167
Mvav 64,128,204
X 169
Mvav 96,128,212
D 4,20,94,0
Ra 148,242,21504
Mvav 128,128,220
X 170
Mvav 132,132,220
D 5,20,94,1024
Ta 198,11,51456
Mvav 100,132,212
Ta 200,12,43008
K 208,168,169
Mvav 68,132,204
D 6,20,94,2048
Ra 152,242,22528
Mvav 36,132,196
W 5
X 171
Maaa 232,184,192
D 7,20,94,3072
Ta 202,12,43264
Maaa 236,188,192
s_add_u32 s94, s94, 256
X 172
Mvav 40,136,196
s_add_u32 m0, s95, 16384
Ra 204,13,60928
K 209,170,171
Mvav 72,136,204
X 173
Mvav 104,136,212
Ra 156,242,23552
D 4,20,94,0
Mvav 136,136,220
X 174
Mvav 140,140,220
D 5,20,94,1024
Ta 208,11,53248
Mvav 108,140,212
Ta 210,11,53504
K 216,172,173
Mvav 76,140,204
D 6,20,94,2048
Ra 128,242,8192
Mvav 44,140,196
W 3
X 175
Mvav 48,144,196
D 7,20,94,3072
Ta 212,12,45056
Maaa 232,196,204
s_add_u32 s94, s94, 256
X 176
Maaa 236,200,204
s_add_u32 m0, s95, 24576
Ta 214,12,45312
K 217,174,175
)"
R"(Mvav 80,144,204
D 4,20,94,0
Ra 132,242,9216
Mvav 112,144,212
X 177
Mvav 144,144,220
Ra 216,13,61440
D 5,20,94,1024
Mvav 148,148,220
X 178
Mvav 116,148,212
D 6,20,94,2048
Ta 220,11,55296
Mvav 84,148,204
Ta 222,11,55552
K 194,176,177
Mvav 52,148,196
D 7,20,94,3072
Ra 136,242,10240
Mvav 56,152,196
s_add_u32 s94, s94, 256
X 179
Mvav 88,152,204
W 3
X 180
Maaa 232,208,216
s_add_u32 m0, s95, 32768
Ta 224,12,47104
K 195,178,179
Maaa 236,212,216
D 4,20,94,0
Ta 226,12,47360
Mvav 120,152,212
Ra 140,242,11264
Ra 228,13,61952
Mvav 152,152,220
X 181
Mvav 156,156,220
D 5,20,94,1024
Ta 184,11,57344
Mvav 124,156,212
X 182
Mvav 92,156,204
D 6,20,94,2048
K 202,180,181
Mvav 60,156,196
W 2
X 183
Cvaa 160,128,0,224
D 7,20,94,3072
Ra 144,242,12288
Mvaa 160,132,4
s_add_u32 s94, s94, 256
X 184
Cvaa 164,132,20,228
W 2
Ta 186,11,57600
K 203,182,183
Maaa 232,220,228
X 185
Maaa 236,224,228
Ta 188,12,49152
Ra 148,242,13312
Mvaa 164,128,16
X 186
Cvaa 168,128,32,232
Ta 190,12,49408
K 210,184,185
Mvaa 168,132,36
Ra 192,13,62464
Ta 196,11,59392
Cvaa 172,132,52,236
X 187
Mvaa 172,128,48
X 188
Mvaa 160,136,8
Ra 152,242,14336
K 211,186,187
Mvaa 160,140,12
Ta 198,11,59648
Ta 200,12,51200
Mvaa 164,140,28
X 189
Mvaa 164,136,24
W 4
X 190
Maaa 232,184,192
Ra 156,242,15360
K 218,188,189
Maaa 236,188,192
Ta 202,12,51456
Ra 204,13,62976
Mvaa 168,136,40
X 191
Mvaa 168,140,44
Ra 128,242,24576
Ra 132,242,25600
Mvaa 172,140,60
X 160
Mvaa 172,136,56
W 4
Ra 136,242,26624
K 219,190,191
Cvaa 176,144,0,224
X 161
Mvaa 176,148,4
X 162
Cvaa 180,148,20,228
Ra 140,242,27648
K 196,160,161
Mvaa 180,144,16
X 163
Cvaa 184,144,32,232
W 4
X 164
Maaa 232,196,204
K 197,162,163
Maaa 236,200,204
X 165
Mvaa 184,148,36
X 166
Cvaa 188,148,52,236
K 204,164,165
Mvaa 188,144,48
X 167
Mvaa 176,152,8
Ra 144,242,28672
G 252,232
Mvaa 176,156,12
G 253,233
G 254,234
Mvaa 180,156,28
X 168
Mvaa 180,152,24
G 255,235
G 8,236
Mvaa 184,152,40
Ra 148,242,29696
G 9,237
Mvaa 184,156,44
G 10,238
G 14,239
Mvaa 188,156,60
K 252,252,8
K 253,253,9
Mvaa 188,152,56
W 2
K 205,166,167
K 254,254,10
Mvav 32,128,192
K 255,255,14
v_and_b32 v8, 0xffff0000, v252
Mvav 64,128,200
v_and_b32 v9, 0xffff0000, v253
v_and_b32 v10, 0xffff0000, v254
Mvav 96,128,208
X 169
Mvav 128,128,216
v_and_b32 v14, 0xffff0000, v255
v_lshlrev_b32 v252, 16, v252
Mvav 132,132,216
v_lshlrev_b32 v253, 16, v253
v_lshlrev_b32 v254, 16, v254
Mvav 100,132,208
v_lshlrev_b32 v255, 16, v255
v_mul_f32 v15, 0xbfb8aa3b, v252
Mvav 68,132,200
v_mul_f32 v2, 0xbfb8aa3b, v253
v_mul_f32 v3, 0xbfb8aa3b, v254
Mvav 36,132,192
X 170
Mvav 40,136,192
v_mul_f32 v27, 0xbfb8aa3b, v255
K 212,168,169
Mvav 72,136,200
X 15
Mvav 104,136,208
Ra 152,242,30720
Ra 156,242,31744
Mvav 136,136,216
X 2
X 3
X 27
v_add_f32 v15, 1.0, v15
v_add_f32 v2, 1.0, v2
v_add_f32 v3, 1.0, v3
v_add_f32 v27, 1.0, v27
v_rcp_f32 v15, v15
v_rcp_f32 v2, v2
v_rcp_f32 v3, v3
v_rcp_f32 v27, v27
U 252,252,15
U 253,253,2
U 254,254,3
U 255,255,27
K 252,252,252
K 253,253,253
K 254,254,254
K 255,255,255
v_lshlrev_b32 v252, 16, v252
v_lshlrev_b32 v253, 16, v253
v_lshlrev_b32 v254, 16, v254
v_lshlrev_b32 v255, 16, v255
U 252,252,8
U 253,253,9
U 254,254,10
U 255,255,14
K 246,252,253
K 247,254,255
s_lshl_b32 s81, s14, 3
v_add_u32 v2, s81, v1
s_mov_b32 s86, 0x00ff00ff
s_mov_b32 s87, 0x00ff00ff
v_cndmask_b32_e64 v252, 0, v246, s[86:87]
v_cndmask_b32_e64 v253, 0, v247, s[86:87]
v_cndmask_b32_e64 v246, v246, 0, s[86:87]
v_cndmask_b32_e64 v247, v247, 0, s[86:87]
ds_write_b64 v2, v[252:253] offset:63488
ds_write_b64 v2, v[246:247] offset:64512
Mvav 140,140,216
X 171
Mvav 108,140,208
v_xor_b32 v242, 0x10000, v242
Mvav 76,140,200
s_waitcnt vmcnt(0) lgkmcnt(0)
s_barrier
Mvav 44,140,192
X 172
Mvav 48,144,192
Ra 128,242,0
K 213,170,171
Mvav 80,144,200
X 173
Mvav 112,144,208
X 174
Mvav 144,144,216
Ra 132,242,1024
K 220,172,173
Mvav 148,148,216
X 175
Mvav 116,148,208
X 176
Mvav 84,148,200
Ra 136,242,2048
K 221,174,175
Mvav 52,148,192
X 177
Mvav 56,152,192
X 178
Mvav 88,152,200
Ra 140,242,3072
K 198,176,177
Mvav 120,152,208
X 179
Mvav 152,152,216
X 180
Mvav 156,156,216
K 199,178,179
Mvav 124,156,208
X 181
Mvav 92,156,200
X 182
Mvav 60,156,192
K 206,180,181
X 183
s_nop 0
K 207,182,183
W 0
Cvaa 160,128,0,224
Ra 144,242,4096
Ra 148,242,5120
Mvaa 160,132,4
s_xor_b32 s62, s62, 0x10000
X 184
Cvaa 164,132,20,228
Ra 216,1,63488
Ra 220,1,64512
Mvaa 164,128,16
v_lshrrev_b32 v8, 6, v1
v_and_b32 v9, 7, v8
Cvaa 168,128,32,232
s_lshl_b32 s81, s14, 2
X 185
Mvaa 168,132,36
v_xor_b32 v9, s81, v9
v_lshlrev_b32 v9, 5, v9
Cvaa 172,132,52,236
X 186
Mvaa 172,128,48
v_lshl_add_u32 v4, v8, 8, v9
v_bfe_u32 v9, v1, 4, 2
Mvaa 160,136,8
v_lshl_add_u32 v4, v9, 3, v4
v_add_u32 v4, 98304, v4
Mvaa 160,140,12
K 214,184,185
v_xor_b32 v5, 32, v4
Mvaa 164,140,28
v_xor_b32 v6, 64, v4
v_xor_b32 v7, 96, v4
Ta 184,4,0
Ta 186,4,4096
Mvaa 164,136,24
Ta 188,5,0
Ta 190,5,4096
Mvaa 168,136,40
Ra 152,242,6144
Ta 192,6,0
Mvaa 168,140,44
W 4
X 187
Zaaa 232,184,216
Ta 194,6,4096
v_and_b32 v11, 0x70, v1
Mvaa 172,140,60
v_mul_u32_u24 v11, 160, v11
v_lshrrev_b32 v10, 8, v1
Mvaa 172,136,56
X 188
v_smfmac_f32_16x16x64_bf16 a[160:163], a[176:179], v[192:199], v240
Ra 156,242,7168
W 4
Maaa 232,188,220
K 215,186,187
v_lshl_add_u32 v11, v10, 4, v11
v_smfmac_f32_16x16x64_bf16 a[164:167], a[176:179], v[200:207], v240
X 189
X 190
K 222,188,189
X 191
X 160
K 223,190,191
v_smfmac_f32_16x16x64_bf16 a[168:171], a[176:179], v[208:215], v240
Ta 196,7,0
v_bfe_u32 v10, v1, 7, 1
v_smfmac_f32_16x16x64_bf16 a[172:175], a[176:179], v[216:223], v240
W 2
Ta 198,7,4096
v_lshl_add_u32 v10, v10, 6, v11
Zaaa 236,192,216
W 2
s_mul_i32 s94, s85, 20480
s_lshl_b32 s82, s14, 8
Cvaa 176,144,0,224
s_add_u32 s94, s94, s82
Ta 200,4,8192
Ta 202,4,12288
Mvaa 176,148,4
s_add_u32 s94, s94, 2752512
s_lshl_b32 s71, s85, 4
Ra 128,242,16384
Cvaa 180,148,20,228
W 3
s_lshl_b32 s82, s14, 3
Ta 204,5,8192
Maaa 236,196,220
s_add_u32 s71, s71, s82
s_add_u32 s71, s71, 2560
Ta 206,5,12288
Mvaa 180,144,16
X 161
Cvaa 184,144,32,232
Ra 132,242,17408
Ta 208,6,8192
Mvaa 184,148,36
W 5
X 162
Zaaa 240,200,216
Ta 210,6,12288
K 192,160,161
Cvaa 188,148,52,236
buffer_store_dwordx4 a[232:235], v10, s[8:11], s94 offen offset:0 sc1
Ta 212,7,8192
Mvaa 188,144,48
X 163
Mvaa 176,152,8
W 4
X 164
Maaa 240,204,220
Ra 136,242,18432
Ta 214,7,12288
Mvaa 176,156,12
Ta 184,4,16384
K 193,162,163
Mvaa 180,156,28
X 165
Mvaa 180,152,24
W 4
Ta 186,4,20480
Ra 140,242,19456
Zaaa 244,208,216
X 166
Mvaa 184,152,40
buffer_store_dwordx4 a[236:239], v10, s[8:11], s94 offen offset:128 sc1
K 200,164,165
Mvaa 184,156,44
X 167
Mvaa 188,156,60
W 3
Ta 188,5,16384
)"
R"(Ta 190,5,20480
Maaa 244,212,220
s_add_u32 s64, s64, 1
X 168
Mvaa 188,152,56
W 2
Ra 144,242,20480
K 201,166,167
Mvav 32,128,196
Ta 192,6,16384
Ta 194,6,20480
Mvav 64,128,204
X 169
Zaaa 248,184,216
buffer_store_dwordx4 a[240:243], v10, s[8:11], s94 offen offset:512 sc1
Ta 196,7,16384
Mvav 96,128,212
X 170
Mvav 128,128,220
Ra 148,242,21504
K 208,168,169
Mvav 132,132,220
W 5
Ta 198,7,20480
Ta 200,4,24576
Maaa 248,188,220
X 171
Mvav 100,132,212
X 172
Mvav 68,132,204
Ta 202,4,28672
K 209,170,171
Mvav 36,132,196
W 5
Ra 152,242,22528
buffer_store_dwordx4 a[244:247], v10, s[8:11], s94 offen offset:640 sc1
Zaaa 252,192,216
X 173
Mvav 40,136,196
X 174
Mvav 72,136,204
Ta 204,5,24576
Ta 206,5,28672
Mvav 104,136,212
W 5
Ra 156,242,23552
K 216,172,173
Maaa 252,196,220
X 175
Mvav 136,136,220
Ta 208,6,24576
Ta 210,6,28672
Mvav 140,140,220
X 176
Mvav 108,140,212
W 6
buffer_store_dwordx4 a[248:251], v10, s[8:11], s94 offen offset:1024 sc1
K 217,174,175
Zaaa 232,200,216
X 177
Mvav 76,140,204
Ta 212,7,24576
Ra 128,242,8192
Mvav 44,140,196
W 4
Ta 214,7,28672
Ta 184,4,32768
Mvav 48,144,196
X 178
Maaa 232,204,220
Ra 132,242,9216
K 194,176,177
Mvav 80,144,204
X 179
Mvav 112,144,212
Ta 186,4,36864
buffer_store_dwordx4 a[252:255], v10, s[8:11], s94 offen offset:1152 sc1
Mvav 144,144,220
W 6
X 180
Zaaa 236,208,216
Ta 188,5,32768
K 195,178,179
Mvav 148,148,220
X 181
Mvav 116,148,212
Ta 190,5,36864
Ra 136,242,10240
Mvav 84,148,204
W 6
X 182
Maaa 236,212,220
Ta 192,6,32768
K 202,180,181
Mvav 52,148,196
Ta 194,6,36864
Ra 140,242,11264
Mvav 56,152,196
X 183
Mvav 88,152,204
W 6
buffer_store_dwordx4 a[232:235], v10, s[8:11], s94 offen offset:1536 sc1
Ta 196,7,32768
Zaaa 240,184,216
X 184
Mvav 120,152,212
Ta 198,7,36864
K 203,182,183
Mvav 152,152,220
Ra 144,242,12288
Ra 148,242,13312
Mvav 156,156,220
W 8
X 185
Maaa 240,188,220
X 186
Mvav 124,156,212
buffer_store_dwordx4 a[236:239], v10, s[8:11], s94 offen offset:1664 sc1
K 210,184,185
Mvav 92,156,204
X 187
Mvav 60,156,196
W 5
X 188
Zaaa 244,192,216
W 4
Ra 152,242,14336
K 211,186,187
Cvaa 160,128,0,224
X 189
Mvaa 160,132,4
X 190
Cvaa 164,132,20,228
W 3
Ra 156,242,15360
buffer_store_dwordx4 a[240:243], v10, s[8:11], s94 offen offset:2048 sc1
Maaa 244,196,220
K 218,188,189
Mvaa 164,128,16
X 191
Cvaa 168,128,32,232
Mvaa 168,132,36
Cvaa 172,132,52,236
Mvaa 172,128,48
Ra 128,242,24576
Mvaa 160,136,8
Ra 132,242,25600
buffer_store_dwordx4 a[244:247], v10, s[8:11], s94 offen offset:2176 sc1
Mvaa 160,140,12
Mvaa 164,140,28
Mvaa 164,136,24
Mvaa 168,136,40
Mvaa 168,140,44
Mvaa 172,140,60
Mvaa 172,136,56
W 2
Ra 136,242,26624
Cvaa 176,144,0,224
X 160
Mvaa 176,148,4
Ra 140,242,27648
K 219,190,191
Cvaa 180,148,20,228
X 161
Mvaa 180,144,16
X 162
Cvaa 184,144,32,232
K 196,160,161
Mvaa 184,148,36
X 163
Cvaa 188,148,52,236
X 164
Mvaa 188,144,48
Ra 144,242,28672
K 197,162,163
Mvaa 176,152,8
X 165
Mvaa 176,156,12
X 166
Mvaa 180,156,28
Ra 148,242,29696
K 204,164,165
Mvaa 180,152,24
X 167
Mvaa 184,152,40
X 168
Mvaa 184,156,44
K 205,166,167
Mvaa 188,156,60
X 169
Mvaa 188,152,56
W 2
X 170
Mvav 32,128,192
Ra 152,242,30720
K 212,168,169
Mvav 64,128,200
X 171
Mvav 96,128,208
Ra 156,242,31744
v_xor_b32 v242, 0x10000, v242
Mvav 128,128,216
X 172
Mvav 132,132,216
K 213,170,171
Mvav 100,132,208
X 173
Mvav 68,132,200
X 174
Mvav 36,132,192
Mvav 40,136,192
K 220,172,173
Mvav 72,136,200
X 175
Mvav 104,136,208
Mvav 136,136,216
X 176
Mvav 140,140,216
Mvav 108,140,208
K 221,174,175
Mvav 76,140,200
W 0
s_barrier
Mvav 44,140,192
X 177
Mvav 48,144,192
X 178
Mvav 80,144,200
Ra 128,242,0
K 198,176,177
Mvav 112,144,208
X 179
Mvav 144,144,216
Ra 132,242,1024
Mvav 148,148,216
X 180
Mvav 116,148,208
Ra 136,242,2048
Mvav 84,148,200
K 199,178,179
Mvav 52,148,192
X 181
Mvav 56,152,192
Ra 140,242,3072
Mvav 88,152,200
X 182
Mvav 120,152,208
Mvav 152,152,216
K 206,180,181
Mvav 156,156,216
X 183
Mvav 124,156,208
Mvav 92,156,200
s_waitcnt vmcnt(0)
v_mov_b64 v[28:29], s[12:13]
s_mov_b64 exec, 1
buffer_store_dwordx2 v[28:29], off, s[8:11], s71 sc1
s_mov_b64 exec, -1
K 207,182,183
Mvav 60,156,192
s_mov_b32 s85, 99
s_lshl_b32 s65, s57, 1
s_sub_u32 s65, s65, 1
s_branch .Ll0t_46%=
.Lf1r_47%=:
s_lshl_b32 s92, s58, 2
s_add_u32 s91, s92, s51
s_add_u32 s95, s92, 0
s_and_b32 s95, s95, 15
s_mul_i32 s95, s95, 40960
s_mul_i32 s71, s51, 0
s_add_u32 s71, s71, 133120
s_add_u32 m0, s71, s48
s_lshl2_add_u32 s94, s48, s95
D 16,40,94,0
s_add_u32 s95, s92, 1
s_and_b32 s95, s95, 15
s_mul_i32 s95, s95, 40960
s_mul_i32 s71, s51, 0
s_add_u32 s71, s71, 143360
D 17,40,94,1024
D 18,40,94,2048
D 19,40,94,3072
s_add_u32 m0, m0, 4096
s_nop 0
buffer_load_dwordx4 v20, s[40:43], s94 offen lds
s_add_u32 m0, s71, s48
s_lshl2_add_u32 s94, s48, s95
D 16,40,94,0
s_add_u32 s95, s92, 2
s_and_b32 s95, s95, 15
s_mul_i32 s95, s95, 40960
s_mul_i32 s71, s51, 0
s_add_u32 s71, s71, 153600
D 17,40,94,1024
D 18,40,94,2048
D 19,40,94,3072
s_add_u32 m0, m0, 4096
s_nop 0
buffer_load_dwordx4 v20, s[40:43], s94 offen lds
s_add_u32 m0, s71, s48
s_lshl2_add_u32 s94, s48, s95
D 16,40,94,0
D 17,40,94,1024
D 18,40,94,2048
D 19,40,94,3072
s_add_u32 m0, m0, 4096
s_nop 0
buffer_load_dwordx4 v20, s[40:43], s94 offen lds
s_branch .Lf1_40%=
.Lspin_15%=:
s_mov_b32 s84, 0
.Llp_17%=:
s_sleep 2
v_lshrrev_b32 v28, 1, v1
s_lshl_b32 s80, s60, 9
s_add_u32 s80, s80, 40960
buffer_load_dwordx2 v[28:29], v28, s[8:11], s80 offen sc1
s_waitcnt vmcnt(0)
s_add_u32 s84, s84, 1
v_cmp_eq_u32 vcc, s13, v29
s_cmp_eq_u64 vcc, -1
s_cbranch_scc1 .Lok_16%=
s_cmp_lt_u32 s84, 1048576
s_cbranch_scc1 .Llp_17%=
s_branch .Lok_16%=
.Lspin_20%=:
s_mov_b32 s84, 0
.Llp_22%=:
s_sleep 2
v_lshrrev_b32 v28, 1, v1
s_lshl_b32 s80, s60, 9
s_add_u32 s80, s80, 40960
buffer_load_dwordx2 v[28:29], v28, s[8:11], s80 offen sc1
s_waitcnt vmcnt(0)
s_add_u32 s84, s84, 1
v_cmp_eq_u32 vcc, s13, v29
s_cmp_eq_u64 vcc, -1
s_cbranch_scc1 .Lok_21%=
s_cmp_lt_u32 s84, 1048576
s_cbranch_scc1 .Llp_22%=
s_branch .Lok_21%=
.Lrt_23%=:
s_waitcnt vmcnt(0)
s_mov_b32 s86, 40960
buffer_load_dwordx4 v[32:35], v1, s[8:11], s86 offen offset:0 sc1
buffer_load_dwordx4 v[36:39], v1, s[8:11], s86 offen offset:1024 sc1
buffer_load_dwordx4 v[40:43], v1, s[8:11], s86 offen offset:2048 sc1
buffer_load_dwordx4 v[44:47], v1, s[8:11], s86 offen offset:3072 sc1
s_mov_b32 s86, 45056
buffer_load_dwordx4 v[48:51], v1, s[8:11], s86 offen offset:0 sc1
buffer_load_dwordx4 v[52:55], v1, s[8:11], s86 offen offset:1024 sc1
buffer_load_dwordx4 v[56:59], v1, s[8:11], s86 offen offset:2048 sc1
buffer_load_dwordx4 v[60:63], v1, s[8:11], s86 offen offset:3072 sc1
s_mov_b32 s86, 49152
buffer_load_dwordx4 v[64:67], v1, s[8:11], s86 offen offset:0 sc1
buffer_load_dwordx4 v[68:71], v1, s[8:11], s86 offen offset:1024 sc1
buffer_load_dwordx4 v[72:75], v1, s[8:11], s86 offen offset:2048 sc1
buffer_load_dwordx4 v[76:79], v1, s[8:11], s86 offen offset:3072 sc1
s_mov_b32 s86, 53248
buffer_load_dwordx4 v[80:83], v1, s[8:11], s86 offen offset:0 sc1
buffer_load_dwordx4 v[84:87], v1, s[8:11], s86 offen offset:1024 sc1
buffer_load_dwordx4 v[88:91], v1, s[8:11], s86 offen offset:2048 sc1
buffer_load_dwordx4 v[92:95], v1, s[8:11], s86 offen offset:3072 sc1
s_branch .Lrtr_24%=
.Lrt_25%=:
v_lshrrev_b32 v136, 1, v1
s_mov_b32 s84, 0
.LL_27%=:
s_waitcnt vmcnt(0)
v_min3_u32 v96, v33, v35, v95
v_max3_u32 v97, v33, v35, v95
v_min3_u32 v96, v96, v37, v39
v_max3_u32 v97, v97, v37, v39
v_min3_u32 v96, v96, v41, v43
v_max3_u32 v97, v97, v41, v43
v_min3_u32 v96, v96, v45, v47
v_max3_u32 v97, v97, v45, v47
v_min3_u32 v96, v96, v49, v51
v_max3_u32 v97, v97, v49, v51
v_min3_u32 v96, v96, v53, v55
v_max3_u32 v97, v97, v53, v55
v_min3_u32 v96, v96, v57, v59
v_max3_u32 v97, v97, v57, v59
v_min3_u32 v96, v96, v61, v63
v_max3_u32 v97, v97, v61, v63
v_min3_u32 v96, v96, v65, v67
v_max3_u32 v97, v97, v65, v67
v_min3_u32 v96, v96, v69, v71
v_max3_u32 v97, v97, v69, v71
v_min3_u32 v96, v96, v73, v75
v_max3_u32 v97, v97, v73, v75
v_min3_u32 v96, v96, v77, v79
v_max3_u32 v97, v97, v77, v79
v_min3_u32 v96, v96, v81, v83
v_max3_u32 v97, v97, v81, v83
v_min3_u32 v96, v96, v85, v87
v_max3_u32 v97, v97, v85, v87
v_min3_u32 v96, v96, v89, v91
v_max3_u32 v97, v97, v89, v91
)"
R"(v_min_u32 v96, v96, v93
v_max_u32 v97, v97, v93
v_cmp_eq_u32 vcc, s13, v96
v_cmp_eq_u32_e64 s[86:87], s13, v97
s_and_b64 vcc, vcc, s[86:87]
s_cmp_eq_u64 vcc, -1
s_cbranch_scc1 .LL_28%=
s_sleep 2
s_mov_b32 s86, 40960
buffer_load_dwordx4 v[32:35], v1, s[8:11], s86 offen offset:0 sc1
buffer_load_dwordx4 v[36:39], v1, s[8:11], s86 offen offset:1024 sc1
buffer_load_dwordx4 v[40:43], v1, s[8:11], s86 offen offset:2048 sc1
buffer_load_dwordx4 v[44:47], v1, s[8:11], s86 offen offset:3072 sc1
s_mov_b32 s86, 45056
buffer_load_dwordx4 v[48:51], v1, s[8:11], s86 offen offset:0 sc1
buffer_load_dwordx4 v[52:55], v1, s[8:11], s86 offen offset:1024 sc1
buffer_load_dwordx4 v[56:59], v1, s[8:11], s86 offen offset:2048 sc1
buffer_load_dwordx4 v[60:63], v1, s[8:11], s86 offen offset:3072 sc1
s_mov_b32 s86, 49152
buffer_load_dwordx4 v[64:67], v1, s[8:11], s86 offen offset:0 sc1
buffer_load_dwordx4 v[68:71], v1, s[8:11], s86 offen offset:1024 sc1
buffer_load_dwordx4 v[72:75], v1, s[8:11], s86 offen offset:2048 sc1
buffer_load_dwordx4 v[76:79], v1, s[8:11], s86 offen offset:3072 sc1
s_mov_b32 s86, 53248
buffer_load_dwordx4 v[80:83], v1, s[8:11], s86 offen offset:0 sc1
buffer_load_dwordx4 v[84:87], v1, s[8:11], s86 offen offset:1024 sc1
buffer_load_dwordx4 v[88:91], v1, s[8:11], s86 offen offset:2048 sc1
buffer_load_dwordx4 v[92:95], v1, s[8:11], s86 offen offset:3072 sc1
s_add_u32 s84, s84, 1
s_cmp_lt_u32 s84, 1048576
s_cbranch_scc1 .LL_27%=
s_waitcnt vmcnt(0)
.LL_28%=:
v_mov_b32 v96, v32
v_mov_b32 v97, 0
v_mov_b32 v98, 0
v_mov_b32 v100, 0
v_med3_u32 v98, v97, v98, v34
v_med3_u32 v97, v96, v97, v34
v_max_u32 v96, v96, v34
v_med3_u32 v98, v97, v98, v36
v_med3_u32 v97, v96, v97, v36
v_max_u32 v96, v96, v36
v_med3_u32 v98, v97, v98, v38
v_med3_u32 v97, v96, v97, v38
v_max_u32 v96, v96, v38
v_med3_u32 v98, v97, v98, v40
v_med3_u32 v97, v96, v97, v40
v_max_u32 v96, v96, v40
v_med3_u32 v98, v97, v98, v42
v_med3_u32 v97, v96, v97, v42
v_max_u32 v96, v96, v42
v_med3_u32 v98, v97, v98, v44
v_med3_u32 v97, v96, v97, v44
v_max_u32 v96, v96, v44
v_med3_u32 v98, v97, v98, v46
v_med3_u32 v97, v96, v97, v46
v_max_u32 v96, v96, v46
v_med3_u32 v98, v97, v98, v48
v_med3_u32 v97, v96, v97, v48
v_max_u32 v96, v96, v48
v_med3_u32 v98, v97, v98, v50
v_med3_u32 v97, v96, v97, v50
v_max_u32 v96, v96, v50
v_med3_u32 v98, v97, v98, v52
v_med3_u32 v97, v96, v97, v52
v_max_u32 v96, v96, v52
v_med3_u32 v98, v97, v98, v54
v_med3_u32 v97, v96, v97, v54
v_max_u32 v96, v96, v54
v_med3_u32 v98, v97, v98, v56
v_med3_u32 v97, v96, v97, v56
v_max_u32 v96, v96, v56
v_med3_u32 v98, v97, v98, v58
v_med3_u32 v97, v96, v97, v58
v_max_u32 v96, v96, v58
v_med3_u32 v98, v97, v98, v60
v_med3_u32 v97, v96, v97, v60
v_max_u32 v96, v96, v60
v_med3_u32 v98, v97, v98, v62
v_med3_u32 v97, v96, v97, v62
v_max_u32 v96, v96, v62
v_med3_u32 v98, v97, v98, v64
v_med3_u32 v97, v96, v97, v64
v_max_u32 v96, v96, v64
v_med3_u32 v98, v97, v98, v66
v_med3_u32 v97, v96, v97, v66
v_max_u32 v96, v96, v66
v_med3_u32 v98, v97, v98, v68
v_med3_u32 v97, v96, v97, v68
v_max_u32 v96, v96, v68
v_med3_u32 v98, v97, v98, v70
v_med3_u32 v97, v96, v97, v70
v_max_u32 v96, v96, v70
v_med3_u32 v98, v97, v98, v72
v_med3_u32 v97, v96, v97, v72
v_max_u32 v96, v96, v72
v_med3_u32 v98, v97, v98, v74
v_med3_u32 v97, v96, v97, v74
v_max_u32 v96, v96, v74
v_med3_u32 v98, v97, v98, v76
v_med3_u32 v97, v96, v97, v76
v_max_u32 v96, v96, v76
v_med3_u32 v98, v97, v98, v78
v_med3_u32 v97, v96, v97, v78
v_max_u32 v96, v96, v78
v_med3_u32 v98, v97, v98, v80
v_med3_u32 v97, v96, v97, v80
v_max_u32 v96, v96, v80
v_med3_u32 v98, v97, v98, v82
v_med3_u32 v97, v96, v97, v82
v_max_u32 v96, v96, v82
v_med3_u32 v98, v97, v98, v84
v_med3_u32 v97, v96, v97, v84
v_max_u32 v96, v96, v84
v_med3_u32 v98, v97, v98, v86
v_med3_u32 v97, v96, v97, v86
v_max_u32 v96, v96, v86
v_med3_u32 v98, v97, v98, v88
v_med3_u32 v97, v96, v97, v88
v_max_u32 v96, v96, v88
v_med3_u32 v98, v97, v98, v90
v_med3_u32 v97, v96, v97, v90
v_max_u32 v96, v96, v90
v_med3_u32 v98, v97, v98, v92
v_med3_u32 v97, v96, v97, v92
v_max_u32 v96, v96, v92
v_med3_u32 v98, v97, v98, v94
v_med3_u32 v97, v96, v97, v94
v_max_u32 v96, v96, v94
v_mov_b32 v99, v96
s_nop 1
v_max_u32_dpp v99, v99, v99 row_shr:1 row_mask:0xf bank_mask:0xf
s_nop 1
v_max_u32_dpp v99, v99, v99 row_shr:2 row_mask:0xf bank_mask:0xf
s_nop 1
v_max_u32_dpp v99, v99, v99 row_shr:4 row_mask:0xf bank_mask:0xf
s_nop 1
v_max_u32_dpp v99, v99, v99 row_shr:8 row_mask:0xf bank_mask:0xf
s_nop 1
v_max_u32_dpp v99, v99, v99 row_bcast:15 row_mask:0xa bank_mask:0xf
s_nop 1
v_max_u32_dpp v99, v99, v99 row_bcast:31 row_mask:0xc bank_mask:0xf
s_nop 0
v_readlane_b32 s86, v99, 63
s_nop 1
v_cmp_eq_u32 vcc, s86, v96
s_and_b32 s87, s86, 0xffff
s_sub_u32 s87, 2047, s87
v_cndmask_b32 v96, v96, v97, vcc
v_cndmask_b32_e64 v97, v97, 0, vcc
v_writelane_b32 v100, s87, 0
v_mov_b32 v99, v96
s_nop 1
v_max_u32_dpp v99, v99, v99 row_shr:1 row_mask:0xf bank_mask:0xf
s_nop 1
v_max_u32_dpp v99, v99, v99 row_shr:2 row_mask:0xf bank_mask:0xf
s_nop 1
v_max_u32_dpp v99, v99, v99 row_shr:4 row_mask:0xf bank_mask:0xf
s_nop 1
v_max_u32_dpp v99, v99, v99 row_shr:8 row_mask:0xf bank_mask:0xf
s_nop 1
v_max_u32_dpp v99, v99, v99 row_bcast:15 row_mask:0xa bank_mask:0xf
s_nop 1
v_max_u32_dpp v99, v99, v99 row_bcast:31 row_mask:0xc bank_mask:0xf
s_nop 0
v_readlane_b32 s86, v99, 63
s_nop 1
v_cmp_eq_u32 vcc, s86, v96
s_and_b32 s87, s86, 0xffff
s_sub_u32 s87, 2047, s87
v_cndmask_b32 v96, v96, v97, vcc
v_cndmask_b32_e64 v97, v97, 0, vcc
v_writelane_b32 v100, s87, 1
v_mov_b32 v99, v96
s_nop 1
v_max_u32_dpp v99, v99, v99 row_shr:1 row_mask:0xf bank_mask:0xf
s_nop 1
v_max_u32_dpp v99, v99, v99 row_shr:2 row_mask:0xf bank_mask:0xf
s_nop 1
v_max_u32_dpp v99, v99, v99 row_shr:4 row_mask:0xf bank_mask:0xf
s_nop 1
v_max_u32_dpp v99, v99, v99 row_shr:8 row_mask:0xf bank_mask:0xf
s_nop 1
v_max_u32_dpp v99, v99, v99 row_bcast:15 row_mask:0xa bank_mask:0xf
s_nop 1
v_max_u32_dpp v99, v99, v99 row_bcast:31 row_mask:0xc bank_mask:0xf
s_nop 0
v_readlane_b32 s86, v99, 63
s_nop 1
v_cmp_eq_u32 vcc, s86, v96
s_and_b32 s87, s86, 0xffff
s_sub_u32 s87, 2047, s87
v_cndmask_b32 v96, v96, v97, vcc
v_cndmask_b32_e64 v97, v97, 0, vcc
v_writelane_b32 v100, s87, 2
v_mov_b32 v99, v96
s_nop 1
v_max_u32_dpp v99, v99, v99 row_shr:1 row_mask:0xf bank_mask:0xf
s_nop 1
v_max_u32_dpp v99, v99, v99 row_shr:2 row_mask:0xf bank_mask:0xf
s_nop 1
v_max_u32_dpp v99, v99, v99 row_shr:4 row_mask:0xf bank_mask:0xf
s_nop 1
v_max_u32_dpp v99, v99, v99 row_shr:8 row_mask:0xf bank_mask:0xf
s_nop 1
v_max_u32_dpp v99, v99, v99 row_bcast:15 row_mask:0xa bank_mask:0xf
s_nop 1
v_max_u32_dpp v99, v99, v99 row_bcast:31 row_mask:0xc bank_mask:0xf
s_nop 0
v_readlane_b32 s86, v99, 63
s_nop 1
v_cmp_eq_u32 vcc, s86, v96
s_and_b32 s87, s86, 0xffff
s_sub_u32 s87, 2047, s87
v_cndmask_b32 v96, v96, v97, vcc
v_cndmask_b32_e64 v97, v97, 0, vcc
v_writelane_b32 v100, s87, 3
v_mov_b32 v99, v96
s_nop 1
v_max_u32_dpp v99, v99, v99 row_shr:1 row_mask:0xf bank_mask:0xf
s_nop 1
v_max_u32_dpp v99, v99, v99 row_shr:2 row_mask:0xf bank_mask:0xf
s_nop 1
v_max_u32_dpp v99, v99, v99 row_shr:4 row_mask:0xf bank_mask:0xf
s_nop 1
v_max_u32_dpp v99, v99, v99 row_shr:8 row_mask:0xf bank_mask:0xf
s_nop 1
v_max_u32_dpp v99, v99, v99 row_bcast:15 row_mask:0xa bank_mask:0xf
s_nop 1
v_max_u32_dpp v99, v99, v99 row_bcast:31 row_mask:0xc bank_mask:0xf
s_nop 0
v_readlane_b32 s86, v99, 63
s_nop 1
v_cmp_eq_u32 vcc, s86, v96
s_and_b32 s87, s86, 0xffff
s_sub_u32 s87, 2047, s87
v_cndmask_b32 v96, v96, v97, vcc
v_cndmask_b32_e64 v97, v97, 0, vcc
v_writelane_b32 v100, s87, 4
v_mov_b32 v99, v96
s_nop 1
v_max_u32_dpp v99, v99, v99 row_shr:1 row_mask:0xf bank_mask:0xf
s_nop 1
v_max_u32_dpp v99, v99, v99 row_shr:2 row_mask:0xf bank_mask:0xf
s_nop 1
v_max_u32_dpp v99, v99, v99 row_shr:4 row_mask:0xf bank_mask:0xf
s_nop 1
v_max_u32_dpp v99, v99, v99 row_shr:8 row_mask:0xf bank_mask:0xf
s_nop 1
v_max_u32_dpp v99, v99, v99 row_bcast:15 row_mask:0xa bank_mask:0xf
s_nop 1
v_max_u32_dpp v99, v99, v99 row_bcast:31 row_mask:0xc bank_mask:0xf
s_nop 0
v_readlane_b32 s86, v99, 63
s_nop 1
v_cmp_eq_u32 vcc, s86, v96
s_and_b32 s87, s86, 0xffff
s_sub_u32 s87, 2047, s87
v_cndmask_b32 v96, v96, v97, vcc
v_cndmask_b32_e64 v97, v97, 0, vcc
v_writelane_b32 v100, s87, 5
v_mov_b32 v99, v96
s_nop 1
v_max_u32_dpp v99, v99, v99 row_shr:1 row_mask:0xf bank_mask:0xf
s_nop 1
v_max_u32_dpp v99, v99, v99 row_shr:2 row_mask:0xf bank_mask:0xf
s_nop 1
v_max_u32_dpp v99, v99, v99 row_shr:4 row_mask:0xf bank_mask:0xf
s_nop 1
v_max_u32_dpp v99, v99, v99 row_shr:8 row_mask:0xf bank_mask:0xf
s_nop 1
v_max_u32_dpp v99, v99, v99 row_bcast:15 row_mask:0xa bank_mask:0xf
s_nop 1
v_max_u32_dpp v99, v99, v99 row_bcast:31 row_mask:0xc bank_mask:0xf
s_nop 0
v_readlane_b32 s86, v99, 63
s_nop 1
v_cmp_eq_u32 vcc, s86, v96
s_and_b32 s87, s86, 0xffff
s_sub_u32 s87, 2047, s87
v_cndmask_b32 v96, v96, v97, vcc
v_cndmask_b32_e64 v97, v97, 0, vcc
v_writelane_b32 v100, s87, 6
v_mov_b32 v99, v96
s_nop 1
v_max_u32_dpp v99, v99, v99 row_shr:1 row_mask:0xf bank_mask:0xf
s_nop 1
v_max_u32_dpp v99, v99, v99 row_shr:2 row_mask:0xf bank_mask:0xf
s_nop 1
v_max_u32_dpp v99, v99, v99 row_shr:4 row_mask:0xf bank_mask:0xf
s_nop 1
v_max_u32_dpp v99, v99, v99 row_shr:8 row_mask:0xf bank_mask:0xf
s_nop 1
v_max_u32_dpp v99, v99, v99 row_bcast:15 row_mask:0xa bank_mask:0xf
s_nop 1
v_max_u32_dpp v99, v99, v99 row_bcast:31 row_mask:0xc bank_mask:0xf
s_nop 0
v_readlane_b32 s86, v99, 63
s_nop 1
v_cmp_eq_u32 vcc, s86, v96
s_and_b32 s87, s86, 0xffff
s_sub_u32 s87, 2047, s87
v_cndmask_b32 v96, v96, v97, vcc
v_cndmask_b32_e64 v97, v97, 0, vcc
v_writelane_b32 v100, s87, 7
v_mov_b32 v99, v98
s_nop 1
v_max_u32_dpp v99, v99, v99 row_shr:1 row_mask:0xf bank_mask:0xf
s_nop 1
v_max_u32_dpp v99, v99, v99 row_shr:2 row_mask:0xf bank_mask:0xf
s_nop 1
v_max_u32_dpp v99, v99, v99 row_shr:4 row_mask:0xf bank_mask:0xf
s_nop 1
v_max_u32_dpp v99, v99, v99 row_shr:8 row_mask:0xf bank_mask:0xf
s_nop 1
v_max_u32_dpp v99, v99, v99 row_bcast:15 row_mask:0xa bank_mask:0xf
s_nop 1
v_max_u32_dpp v99, v99, v99 row_bcast:31 row_mask:0xc bank_mask:0xf
s_nop 0
v_readlane_b32 s87, v99, 63
s_nop 0
s_cmp_gt_u32 s87, s86
s_cbranch_scc0 .LL_29%=
s_mov_b32 m0, 0
.LL_30%=:
v_max3_u32 v96, v32, v34, v36
v_max3_u32 v96, v96, v38, v40
v_max3_u32 v96, v96, v42, v44
v_max3_u32 v96, v96, v46, v48
v_max3_u32 v96, v96, v50, v52
v_max3_u32 v96, v96, v54, v56
v_max3_u32 v96, v96, v58, v60
v_max3_u32 v96, v96, v62, v64
v_max3_u32 v96, v96, v66, v68
v_max3_u32 v96, v96, v70, v72
v_max3_u32 v96, v96, v74, v76
v_max3_u32 v96, v96, v78, v80
v_max3_u32 v96, v96, v82, v84
v_max3_u32 v96, v96, v86, v88
v_max3_u32 v96, v96, v90, v92
v_max_u32 v96, v96, v94
v_mov_b32 v99, v96
s_nop 1
v_max_u32_dpp v99, v99, v99 row_shr:1 row_mask:0xf bank_mask:0xf
s_nop 1
v_max_u32_dpp v99, v99, v99 row_shr:2 row_mask:0xf bank_mask:0xf
s_nop 1
v_max_u32_dpp v99, v99, v99 row_shr:4 row_mask:0xf bank_mask:0xf
s_nop 1
v_max_u32_dpp v99, v99, v99 row_shr:8 row_mask:0xf bank_mask:0xf
s_nop 1
v_max_u32_dpp v99, v99, v99 row_bcast:15 row_mask:0xa bank_mask:0xf
s_nop 1
v_max_u32_dpp v99, v99, v99 row_bcast:31 row_mask:0xc bank_mask:0xf
s_nop 0
v_readlane_b32 s86, v99, 63
s_nop 1
v_cmp_eq_u32 vcc, s86, v32
s_nop 1
v_cndmask_b32_e64 v32, v32, 0, vcc
v_cmp_eq_u32 vcc, s86, v34
s_nop 1
v_cndmask_b32_e64 v34, v34, 0, vcc
v_cmp_eq_u32 vcc, s86, v36
s_nop 1
v_cndmask_b32_e64 v36, v36, 0, vcc
v_cmp_eq_u32 vcc, s86, v38
s_nop 1
v_cndmask_b32_e64 v38, v38, 0, vcc
v_cmp_eq_u32 vcc, s86, v40
s_nop 1
v_cndmask_b32_e64 v40, v40, 0, vcc
v_cmp_eq_u32 vcc, s86, v42
s_nop 1
v_cndmask_b32_e64 v42, v42, 0, vcc
v_cmp_eq_u32 vcc, s86, v44
s_nop 1
v_cndmask_b32_e64 v44, v44, 0, vcc
v_cmp_eq_u32 vcc, s86, v46
s_nop 1
v_cndmask_b32_e64 v46, v46, 0, vcc
v_cmp_eq_u32 vcc, s86, v48
s_nop 1
v_cndmask_b32_e64 v48, v48, 0, vcc
v_cmp_eq_u32 vcc, s86, v50
s_nop 1
v_cndmask_b32_e64 v50, v50, 0, vcc
v_cmp_eq_u32 vcc, s86, v52
s_nop 1
v_cndmask_b32_e64 v52, v52, 0, vcc
v_cmp_eq_u32 vcc, s86, v54
s_nop 1
v_cndmask_b32_e64 v54, v54, 0, vcc
v_cmp_eq_u32 vcc, s86, v56
s_nop 1
v_cndmask_b32_e64 v56, v56, 0, vcc
v_cmp_eq_u32 vcc, s86, v58
s_nop 1
v_cndmask_b32_e64 v58, v58, 0, vcc
)"
R"(v_cmp_eq_u32 vcc, s86, v60
s_nop 1
v_cndmask_b32_e64 v60, v60, 0, vcc
v_cmp_eq_u32 vcc, s86, v62
s_nop 1
v_cndmask_b32_e64 v62, v62, 0, vcc
v_cmp_eq_u32 vcc, s86, v64
s_nop 1
v_cndmask_b32_e64 v64, v64, 0, vcc
v_cmp_eq_u32 vcc, s86, v66
s_nop 1
v_cndmask_b32_e64 v66, v66, 0, vcc
v_cmp_eq_u32 vcc, s86, v68
s_nop 1
v_cndmask_b32_e64 v68, v68, 0, vcc
v_cmp_eq_u32 vcc, s86, v70
s_nop 1
v_cndmask_b32_e64 v70, v70, 0, vcc
v_cmp_eq_u32 vcc, s86, v72
s_nop 1
v_cndmask_b32_e64 v72, v72, 0, vcc
v_cmp_eq_u32 vcc, s86, v74
s_nop 1
v_cndmask_b32_e64 v74, v74, 0, vcc
v_cmp_eq_u32 vcc, s86, v76
s_nop 1
v_cndmask_b32_e64 v76, v76, 0, vcc
v_cmp_eq_u32 vcc, s86, v78
s_nop 1
v_cndmask_b32_e64 v78, v78, 0, vcc
v_cmp_eq_u32 vcc, s86, v80
s_nop 1
v_cndmask_b32_e64 v80, v80, 0, vcc
v_cmp_eq_u32 vcc, s86, v82
s_nop 1
v_cndmask_b32_e64 v82, v82, 0, vcc
v_cmp_eq_u32 vcc, s86, v84
s_nop 1
v_cndmask_b32_e64 v84, v84, 0, vcc
v_cmp_eq_u32 vcc, s86, v86
s_nop 1
v_cndmask_b32_e64 v86, v86, 0, vcc
v_cmp_eq_u32 vcc, s86, v88
s_nop 1
v_cndmask_b32_e64 v88, v88, 0, vcc
v_cmp_eq_u32 vcc, s86, v90
s_nop 1
v_cndmask_b32_e64 v90, v90, 0, vcc
v_cmp_eq_u32 vcc, s86, v92
s_nop 1
v_cndmask_b32_e64 v92, v92, 0, vcc
v_cmp_eq_u32 vcc, s86, v94
s_nop 1
v_cndmask_b32_e64 v94, v94, 0, vcc
s_and_b32 s87, s86, 0xffff
s_sub_u32 s87, 2047, s87
s_nop 0
v_writelane_b32 v100, s87, m0
s_add_u32 m0, m0, 1
s_cmp_lt_u32 m0, 8
s_cbranch_scc1 .LL_30%=
.LL_29%=:
v_xor_b32 v102, s12, v100
v_mov_b32 v103, s13
s_mov_b64 exec, 0xff
buffer_store_dwordx2 v[102:103], v136, s[8:11], 0 offen offset:3584 sc1
s_mov_b64 exec, -1
s_branch .Lrtr_26%=
.Lspin_51%=:
s_mov_b32 s84, 0
.Llp_53%=:
s_sleep 2
buffer_load_dwordx2 v[8:9], v11, s[8:11], 0 offen offset:3840 sc1
s_waitcnt vmcnt(0)
s_add_u32 s84, s84, 1
v_cmp_eq_u64 vcc, s[12:13], v[8:9]
s_cmp_eq_u64 vcc, -1
s_cbranch_scc1 .Lok_52%=
s_cmp_lt_u32 s84, 1048576
s_cbranch_scc1 .Llp_53%=
s_branch .Lok_52%=
.Lredo%=:
s_waitcnt vmcnt(0) lgkmcnt(0)
s_barrier
s_and_b32 s81, s14, 1
s_mul_i32 s91, s81, 17164288
s_mul_i32 s82, s56, 536576
s_add_u32 s91, s91, s82
s_add_u32 s91, s91, 4194304
s_lshr_b32 s82, s14, 1
s_lshl_b32 s83, s82, 14
s_add_u32 s91, s91, s83
s_lshl_b32 s81, s81, 14
s_lshl_b32 s82, s82, 16
s_or_b32 s92, s81, s82
s_xor_b32 s92, s92, 0x10000
s_add_u32 m0, s92, 0
s_add_u32 s81, s91, 0
Dc 1,8,81,0
Dc 1,8,81,1024
Dc 1,8,81,2048
Dc 1,8,81,3072
s_add_u32 m0, s92, 4096
s_add_u32 s81, s91, 4096
Dc 1,8,81,0
Dc 1,8,81,1024
Dc 1,8,81,2048
Dc 1,8,81,3072
s_add_u32 m0, s92, 8192
s_add_u32 s81, s91, 8192
Dc 1,8,81,0
Dc 1,8,81,1024
Dc 1,8,81,2048
Dc 1,8,81,3072
s_add_u32 m0, s92, 12288
s_add_u32 s81, s91, 12288
Dc 1,8,81,0
Dc 1,8,81,1024
Dc 1,8,81,2048
Dc 1,8,81,3072
v_bfe_u32 v244, v1, 4, 1
v_mul_u32_u24 v244, 0xaaaa, v244
v_add_u32 v244, 0x4444, v244
v_lshl_or_b32 v240, v244, 16, v244
v_mov_b32 v241, 0xff800000
v_add_u32 v242, 0x10000, v1
v_bfe_u32 v244, v1, 4, 4
v_lshrrev_b32 v245, 8, v1
v_lshlrev_b32 v245, 2, v245
v_sub_u32 v244, v244, v245
v_cmp_le_i32_e64 s[72:73], 0, v244
v_cmp_le_i32_e64 s[74:75], 1, v244
v_cmp_le_i32_e64 s[76:77], 2, v244
v_cmp_le_i32_e64 s[78:79], 3, v244
v_add_u32 v243, 4096, v1
v_add_u32 v244, 8192, v1
v_add_u32 v245, 12288, v1
s_mov_b32 s18, 0x3f803f80
s_mov_b32 s19, 0x3f003f00
v_accvgpr_write_b32 a176, s18
v_accvgpr_write_b32 a180, s19
v_mov_b32 v252, v241
v_accvgpr_write_b32 a177, s18
v_accvgpr_write_b32 a181, s19
v_mov_b32 v253, v241
v_accvgpr_write_b32 a178, s18
v_accvgpr_write_b32 a182, s19
v_mov_b32 v254, v241
v_accvgpr_write_b32 a179, s18
v_accvgpr_write_b32 a183, s19
v_mov_b32 v255, v241
v_accvgpr_write_b32 a160, 0
v_accvgpr_write_b32 a161, 0
v_accvgpr_write_b32 a162, 0
v_accvgpr_write_b32 a163, 0
v_accvgpr_write_b32 a164, 0
v_accvgpr_write_b32 a165, 0
v_accvgpr_write_b32 a166, 0
v_accvgpr_write_b32 a167, 0
v_accvgpr_write_b32 a168, 0
v_accvgpr_write_b32 a169, 0
v_accvgpr_write_b32 a170, 0
v_accvgpr_write_b32 a171, 0
v_accvgpr_write_b32 a172, 0
v_accvgpr_write_b32 a173, 0
v_accvgpr_write_b32 a174, 0
v_accvgpr_write_b32 a175, 0
s_mov_b32 s90, 0x5d800000
s_mov_b64 s[88:89], 0
s_mov_b32 s64, 0
s_and_b32 s18, s14, 1
s_lshl_b32 s62, s18, 14
s_lshl_b32 s20, s57, 15
s_sub_u32 s20, 507904, s20
s_add_u32 s20, s20, s62
s_lshl_b32 s65, s57, 1
s_sub_u32 s65, s65, 1
s_max_i32 s65, s65, 0
s_sub_u32 s66, s60, 1
s_cmp_lt_u32 s14, 2
s_cselect_b32 s20, s20, 0
s_cselect_b32 s66, s66, 15
s_cselect_b32 s21, 32768, 0
s_add_u32 s61, s91, s20
s_add_u32 s62, s62, s21
s_sub_u32 s63, 16384, s21
s_lshl_b32 s67, s60, 7
s_add_u32 s67, s67, s56
s_lshl_b32 s67, s67, 12
s_add_u32 s67, s67, 39845888
s_lshl_b32 s69, s60, 3
s_add_u32 s69, s69, s59
s_lshl_b32 s69, s69, 6
s_lshl_b32 s21, s58, 3
s_add_u32 s69, s69, s21
s_add_u32 s69, s69, 20480
s_mov_b32 s70, 0
s_mov_b32 s37, 0
v_bfe_u32 v28, v1, 4, 4
v_lshrrev_b32 v29, 8, v1
v_lshlrev_b32 v29, 2, v29
v_sub_u32 v28, v28, v29
s_mov_b64 s[24:25], 0
s_mov_b32 s68, 0
s_mov_b32 s85, 99
s_lshl_b32 s80, s60, 6
s_and_b32 s81, s14, 2
s_lshl_b32 s81, s81, 4
s_xor_b32 s82, s81, 0
s_add_u32 s82, s82, s80
v_add_u32 v4, s82, v28
s_xor_b32 s82, s81, 16
s_add_u32 s82, s82, s80
v_add_u32 v5, s82, v28
s_xor_b32 s82, s81, 32
s_add_u32 s82, s82, s80
v_add_u32 v6, s82, v28
s_xor_b32 s82, s81, 48
s_add_u32 s82, s82, s80
v_add_u32 v7, s82, v28
s_mul_i32 s82, s56, 536576
s_add_u32 s82, s82, 4194304
s_lshl_b32 s84, s60, 1
s_add_u32 s84, s84, 2
.LL_54%=:
buffer_load_dwordx4 a[128:131], v1, s[8:11], s82 offen offset:0 sc1
buffer_load_dwordx4 a[132:135], v1, s[8:11], s82 offen offset:1024 sc1
buffer_load_dwordx4 a[136:139], v1, s[8:11], s82 offen offset:2048 sc1
buffer_load_dwordx4 a[140:143], v1, s[8:11], s82 offen offset:3072 sc1
buffer_load_dwordx4 a[144:147], v243, s[8:11], s82 offen offset:0 sc1
buffer_load_dwordx4 a[148:151], v243, s[8:11], s82 offen offset:1024 sc1
buffer_load_dwordx4 a[152:155], v243, s[8:11], s82 offen offset:2048 sc1
buffer_load_dwordx4 a[156:159], v243, s[8:11], s82 offen offset:3072 sc1
s_waitcnt vmcnt(0)
Zvaa 160,128,0
Zvaa 164,128,16
Zvaa 168,128,32
Zvaa 172,128,48
Mvaa 160,132,4
Mvaa 164,132,20
Mvaa 168,132,36
Mvaa 172,132,52
Mvaa 160,136,8
Mvaa 164,136,24
Mvaa 168,136,40
Mvaa 172,136,56
Mvaa 160,140,12
Mvaa 164,140,28
Mvaa 168,140,44
Mvaa 172,140,60
Zvaa 176,144,0
Zvaa 180,144,16
Zvaa 184,144,32
Zvaa 188,144,48
Mvaa 176,148,4
Mvaa 180,148,20
Mvaa 184,148,36
Mvaa 188,148,52
Mvaa 176,152,8
Mvaa 180,152,24
Mvaa 184,152,40
Mvaa 188,152,56
Mvaa 176,156,12
Mvaa 180,156,28
Mvaa 184,156,44
Mvaa 188,156,60
v_cmp_le_i32 vcc, 0, v4
s_nop 1
v_cndmask_b32 v160, v241, v160, vcc
v_max_f32 v252, v252, v160
v_cmp_le_i32 vcc, 1, v4
s_nop 1
v_cndmask_b32 v161, v241, v161, vcc
v_max_f32 v252, v252, v161
v_cmp_le_i32 vcc, 2, v4
s_nop 1
v_cndmask_b32 v162, v241, v162, vcc
v_max_f32 v252, v252, v162
v_cmp_le_i32 vcc, 3, v4
s_nop 1
v_cndmask_b32 v163, v241, v163, vcc
v_max_f32 v252, v252, v163
v_cmp_le_i32 vcc, 0, v5
s_nop 1
v_cndmask_b32 v164, v241, v164, vcc
v_max_f32 v253, v253, v164
v_cmp_le_i32 vcc, 1, v5
s_nop 1
v_cndmask_b32 v165, v241, v165, vcc
v_max_f32 v253, v253, v165
v_cmp_le_i32 vcc, 2, v5
s_nop 1
v_cndmask_b32 v166, v241, v166, vcc
v_max_f32 v253, v253, v166
v_cmp_le_i32 vcc, 3, v5
s_nop 1
v_cndmask_b32 v167, v241, v167, vcc
v_max_f32 v253, v253, v167
v_cmp_le_i32 vcc, 0, v6
s_nop 1
v_cndmask_b32 v168, v241, v168, vcc
v_max_f32 v254, v254, v168
v_cmp_le_i32 vcc, 1, v6
s_nop 1
v_cndmask_b32 v169, v241, v169, vcc
v_max_f32 v254, v254, v169
v_cmp_le_i32 vcc, 2, v6
s_nop 1
v_cndmask_b32 v170, v241, v170, vcc
v_max_f32 v254, v254, v170
v_cmp_le_i32 vcc, 3, v6
s_nop 1
v_cndmask_b32 v171, v241, v171, vcc
v_max_f32 v254, v254, v171
v_cmp_le_i32 vcc, 0, v7
s_nop 1
v_cndmask_b32 v172, v241, v172, vcc
v_max_f32 v255, v255, v172
v_cmp_le_i32 vcc, 1, v7
s_nop 1
v_cndmask_b32 v173, v241, v173, vcc
v_max_f32 v255, v255, v173
v_cmp_le_i32 vcc, 2, v7
s_nop 1
v_cndmask_b32 v174, v241, v174, vcc
v_max_f32 v255, v255, v174
v_cmp_le_i32 vcc, 3, v7
s_nop 1
v_cndmask_b32 v175, v241, v175, vcc
v_max_f32 v255, v255, v175
v_cmp_le_i32 vcc, 16, v4
s_nop 1
v_cndmask_b32 v176, v241, v176, vcc
v_max_f32 v252, v252, v176
v_cmp_le_i32 vcc, 17, v4
s_nop 1
v_cndmask_b32 v177, v241, v177, vcc
v_max_f32 v252, v252, v177
v_cmp_le_i32 vcc, 18, v4
s_nop 1
v_cndmask_b32 v178, v241, v178, vcc
v_max_f32 v252, v252, v178
v_cmp_le_i32 vcc, 19, v4
s_nop 1
v_cndmask_b32 v179, v241, v179, vcc
v_max_f32 v252, v252, v179
v_cmp_le_i32 vcc, 16, v5
s_nop 1
v_cndmask_b32 v180, v241, v180, vcc
v_max_f32 v253, v253, v180
v_cmp_le_i32 vcc, 17, v5
s_nop 1
v_cndmask_b32 v181, v241, v181, vcc
v_max_f32 v253, v253, v181
v_cmp_le_i32 vcc, 18, v5
s_nop 1
v_cndmask_b32 v182, v241, v182, vcc
v_max_f32 v253, v253, v182
v_cmp_le_i32 vcc, 19, v5
s_nop 1
v_cndmask_b32 v183, v241, v183, vcc
v_max_f32 v253, v253, v183
v_cmp_le_i32 vcc, 16, v6
s_nop 1
v_cndmask_b32 v184, v241, v184, vcc
v_max_f32 v254, v254, v184
v_cmp_le_i32 vcc, 17, v6
s_nop 1
v_cndmask_b32 v185, v241, v185, vcc
v_max_f32 v254, v254, v185
v_cmp_le_i32 vcc, 18, v6
s_nop 1
v_cndmask_b32 v186, v241, v186, vcc
v_max_f32 v254, v254, v186
v_cmp_le_i32 vcc, 19, v6
s_nop 1
v_cndmask_b32 v187, v241, v187, vcc
v_max_f32 v254, v254, v187
v_cmp_le_i32 vcc, 16, v7
s_nop 1
v_cndmask_b32 v188, v241, v188, vcc
v_max_f32 v255, v255, v188
v_cmp_le_i32 vcc, 17, v7
s_nop 1
v_cndmask_b32 v189, v241, v189, vcc
v_max_f32 v255, v255, v189
v_cmp_le_i32 vcc, 18, v7
s_nop 1
v_cndmask_b32 v190, v241, v190, vcc
v_max_f32 v255, v255, v190
v_cmp_le_i32 vcc, 19, v7
s_nop 1
v_cndmask_b32 v191, v241, v191, vcc
v_max_f32 v255, v255, v191
v_subrev_u32 v4, 32, v4
v_subrev_u32 v5, 32, v5
v_subrev_u32 v6, 32, v6
v_subrev_u32 v7, 32, v7
s_add_u32 s82, s82, 8192
s_sub_u32 s84, s84, 1
)"
R"(s_cmp_lg_u32 s84, 0
s_cbranch_scc1 .LL_54%=
G 28,0
v_lshlrev_b32 v32, 16, v28
v_and_b32 v33, 0xffff0000, v28
G 28,1
v_lshlrev_b32 v34, 16, v28
v_and_b32 v35, 0xffff0000, v28
G 28,2
v_lshlrev_b32 v36, 16, v28
v_and_b32 v37, 0xffff0000, v28
G 28,3
v_lshlrev_b32 v38, 16, v28
v_and_b32 v39, 0xffff0000, v28
G 28,4
v_lshlrev_b32 v40, 16, v28
v_and_b32 v41, 0xffff0000, v28
G 28,5
v_lshlrev_b32 v42, 16, v28
v_and_b32 v43, 0xffff0000, v28
G 28,6
v_lshlrev_b32 v44, 16, v28
v_and_b32 v45, 0xffff0000, v28
G 28,7
v_lshlrev_b32 v46, 16, v28
v_and_b32 v47, 0xffff0000, v28
G 28,8
v_lshlrev_b32 v48, 16, v28
v_and_b32 v49, 0xffff0000, v28
G 28,9
v_lshlrev_b32 v50, 16, v28
v_and_b32 v51, 0xffff0000, v28
G 28,10
v_lshlrev_b32 v52, 16, v28
v_and_b32 v53, 0xffff0000, v28
G 28,11
v_lshlrev_b32 v54, 16, v28
v_and_b32 v55, 0xffff0000, v28
G 28,12
v_lshlrev_b32 v56, 16, v28
v_and_b32 v57, 0xffff0000, v28
G 28,13
v_lshlrev_b32 v58, 16, v28
v_and_b32 v59, 0xffff0000, v28
G 28,14
v_lshlrev_b32 v60, 16, v28
v_and_b32 v61, 0xffff0000, v28
G 28,15
v_lshlrev_b32 v62, 16, v28
v_and_b32 v63, 0xffff0000, v28
G 28,16
v_lshlrev_b32 v64, 16, v28
v_and_b32 v65, 0xffff0000, v28
G 28,17
v_lshlrev_b32 v66, 16, v28
v_and_b32 v67, 0xffff0000, v28
G 28,18
v_lshlrev_b32 v68, 16, v28
v_and_b32 v69, 0xffff0000, v28
G 28,19
v_lshlrev_b32 v70, 16, v28
v_and_b32 v71, 0xffff0000, v28
G 28,20
v_lshlrev_b32 v72, 16, v28
v_and_b32 v73, 0xffff0000, v28
G 28,21
v_lshlrev_b32 v74, 16, v28
v_and_b32 v75, 0xffff0000, v28
G 28,22
v_lshlrev_b32 v76, 16, v28
v_and_b32 v77, 0xffff0000, v28
G 28,23
v_lshlrev_b32 v78, 16, v28
v_and_b32 v79, 0xffff0000, v28
G 28,24
v_lshlrev_b32 v80, 16, v28
v_and_b32 v81, 0xffff0000, v28
G 28,25
v_lshlrev_b32 v82, 16, v28
v_and_b32 v83, 0xffff0000, v28
G 28,26
v_lshlrev_b32 v84, 16, v28
v_and_b32 v85, 0xffff0000, v28
G 28,27
v_lshlrev_b32 v86, 16, v28
v_and_b32 v87, 0xffff0000, v28
G 28,28
v_lshlrev_b32 v88, 16, v28
v_and_b32 v89, 0xffff0000, v28
G 28,29
v_lshlrev_b32 v90, 16, v28
v_and_b32 v91, 0xffff0000, v28
G 28,30
v_lshlrev_b32 v92, 16, v28
v_and_b32 v93, 0xffff0000, v28
G 28,31
v_lshlrev_b32 v94, 16, v28
v_and_b32 v95, 0xffff0000, v28
G 28,32
v_lshlrev_b32 v96, 16, v28
v_and_b32 v97, 0xffff0000, v28
G 28,33
v_lshlrev_b32 v98, 16, v28
v_and_b32 v99, 0xffff0000, v28
G 28,34
v_lshlrev_b32 v100, 16, v28
v_and_b32 v101, 0xffff0000, v28
G 28,35
v_lshlrev_b32 v102, 16, v28
v_and_b32 v103, 0xffff0000, v28
G 28,36
v_lshlrev_b32 v104, 16, v28
v_and_b32 v105, 0xffff0000, v28
G 28,37
v_lshlrev_b32 v106, 16, v28
v_and_b32 v107, 0xffff0000, v28
G 28,38
v_lshlrev_b32 v108, 16, v28
v_and_b32 v109, 0xffff0000, v28
G 28,39
v_lshlrev_b32 v110, 16, v28
v_and_b32 v111, 0xffff0000, v28
G 28,40
v_lshlrev_b32 v112, 16, v28
v_and_b32 v113, 0xffff0000, v28
G 28,41
v_lshlrev_b32 v114, 16, v28
v_and_b32 v115, 0xffff0000, v28
G 28,42
v_lshlrev_b32 v116, 16, v28
v_and_b32 v117, 0xffff0000, v28
G 28,43
v_lshlrev_b32 v118, 16, v28
v_and_b32 v119, 0xffff0000, v28
G 28,44
v_lshlrev_b32 v120, 16, v28
v_and_b32 v121, 0xffff0000, v28
G 28,45
v_lshlrev_b32 v122, 16, v28
v_and_b32 v123, 0xffff0000, v28
G 28,46
v_lshlrev_b32 v124, 16, v28
v_and_b32 v125, 0xffff0000, v28
G 28,47
v_lshlrev_b32 v126, 16, v28
v_and_b32 v127, 0xffff0000, v28
G 28,48
v_lshlrev_b32 v128, 16, v28
v_and_b32 v129, 0xffff0000, v28
G 28,49
v_lshlrev_b32 v130, 16, v28
v_and_b32 v131, 0xffff0000, v28
G 28,50
v_lshlrev_b32 v132, 16, v28
v_and_b32 v133, 0xffff0000, v28
G 28,51
v_lshlrev_b32 v134, 16, v28
v_and_b32 v135, 0xffff0000, v28
G 28,52
v_lshlrev_b32 v136, 16, v28
v_and_b32 v137, 0xffff0000, v28
G 28,53
v_lshlrev_b32 v138, 16, v28
v_and_b32 v139, 0xffff0000, v28
G 28,54
v_lshlrev_b32 v140, 16, v28
v_and_b32 v141, 0xffff0000, v28
G 28,55
v_lshlrev_b32 v142, 16, v28
v_and_b32 v143, 0xffff0000, v28
G 28,56
v_lshlrev_b32 v144, 16, v28
v_and_b32 v145, 0xffff0000, v28
G 28,57
v_lshlrev_b32 v146, 16, v28
v_and_b32 v147, 0xffff0000, v28
G 28,58
v_lshlrev_b32 v148, 16, v28
v_and_b32 v149, 0xffff0000, v28
G 28,59
v_lshlrev_b32 v150, 16, v28
v_and_b32 v151, 0xffff0000, v28
G 28,60
v_lshlrev_b32 v152, 16, v28
v_and_b32 v153, 0xffff0000, v28
G 28,61
v_lshlrev_b32 v154, 16, v28
v_and_b32 v155, 0xffff0000, v28
G 28,62
v_lshlrev_b32 v156, 16, v28
v_and_b32 v157, 0xffff0000, v28
G 28,63
v_lshlrev_b32 v158, 16, v28
v_and_b32 v159, 0xffff0000, v28
s_mov_b32 s22, 1.0
s_mov_b32 s23, 1.0
s_mov_b32 s29, 134217728
s_mov_b32 s71, 3584
s_barrier
Ra 128,242,0
Ra 132,242,1024
Ra 136,242,2048
Ra 140,242,3072
s_cmp_ge_u32 s14, 2
s_cbranch_scc1 .Lbgo%=
s_branch .Lago%=
.p2align 12
.Lpb_31%=:
s_waitcnt vmcnt(6) lgkmcnt(6)
Zvaa 32,0,32
s_add_u32 s28, s28, 512
Ta 20,4,8192
Ta 22,4,9216
Mvaa 32,4,48
s_cmp_eq_u32 s28, s4
s_cselect_b32 s93, 10240, 0
Ta 24,5,0
Ta 26,5,1024
Zvaa 64,4,52
s_sub_u32 s28, s28, s93
s_add_u32 s93, s28, 20480
buffer_load_dwordx4 a[128:131], v26, s[8:11], s28 offen offset:0 sc1
Mvaa 64,0,36
Ta 28,5,8192
Ta 30,5,9216
Zvaa 96,0,40
K 184,184,185
K 185,186,187
Mvaa 96,4,56
K 186,188,189
K 187,190,191
Zvaa 128,4,60
K 192,192,193
Mvaa 128,0,44
W 8
buffer_load_dwordx4 a[132:135], v26, s[8:11], s93 offen offset:0 sc1
K 193,194,195
Zvaa 132,8,44
Ta 0,6,0
Ta 2,6,1024
Mvaa 132,12,60
K 194,196,197
K 195,198,199
Zvaa 100,12,56
K 200,200,201
K 201,202,203
Mvaa 100,8,40
Ta 4,6,8192
Ta 6,6,9216
Zvaa 68,8,36
buffer_load_dwordx4 a[136:139], v27, s[8:11], s28 offen offset:0 sc1
K 202,204,205
Mvaa 68,12,52
K 203,206,207
K 208,208,209
Zvaa 36,12,48
K 209,210,211
K 210,212,213
Mvaa 36,8,32
W 8
Ta 8,7,0
K 211,214,215
Zvaa 40,16,32
Ta 10,7,1024
K 216,216,217
Mvaa 40,20,48
buffer_load_dwordx4 a[140:143], v27, s[8:11], s93 offen offset:0 sc1
K 217,218,219
Zvaa 72,20,52
s_add_u32 s28, s28, 512
s_cmp_eq_u32 s28, s4
s_cselect_b32 s93, 10240, 0
s_sub_u32 s28, s28, s93
Mvaa 72,16,36
s_add_u32 s93, s28, 20480
K 218,220,221
K 219,222,223
Zvaa 104,16,40
Ta 12,7,8192
Ta 14,7,9216
Mvaa 104,20,56
K 224,224,225
K 225,226,227
Zvaa 136,20,60
K 226,228,229
K 227,230,231
Mvaa 136,16,44
W 8
buffer_load_dwordx4 a[144:147], v26, s[8:11], s28 offen offset:0 sc1
K 232,232,233
Zvaa 140,24,44
K 233,234,235
K 234,236,237
Mvaa 140,28,60
Ta 16,8,0
K 235,238,239
Zvaa 108,28,56
Ta 18,8,1024
K 240,240,241
Mvaa 108,24,40
K 241,242,243
K 242,244,245
Zvaa 76,24,36
buffer_load_dwordx4 a[148:151], v26, s[8:11], s93 offen offset:0 sc1
K 243,246,247
Mvaa 76,28,52
Ta 20,8,8192
Ta 22,8,9216
Zvaa 44,28,48
K 248,248,249
K 249,250,251
Mvaa 44,24,32
K 250,252,253
K 251,254,255
W 8
Zvaa 48,0,32
Ta 24,9,0
Ta 26,9,1024
Mvaa 48,4,48
buffer_load_dwordx4 a[152:155], v27, s[8:11], s28 offen offset:0 sc1
K 160,160,161
Zvaa 80,4,52
K 161,162,163
K 162,164,165
Mvaa 80,0,36
Ta 28,9,8192
K 163,166,167
Zvaa 112,0,40
Ta 30,9,9216
K 168,168,169
Mvaa 112,4,56
K 169,170,171
K 170,172,173
Zvaa 144,4,60
buffer_load_dwordx4 a[156:159], v27, s[8:11], s93 offen offset:0 sc1
K 171,174,175
Mvaa 144,0,44
W 8
s_add_u32 s28, s28, 512
s_cmp_eq_u32 s28, s4
s_cselect_b32 s93, 10240, 0
Zvaa 148,8,44
s_sub_u32 s28, s28, s93
s_add_u32 s93, s28, 20480
G 164,184
Mvaa 148,12,60
G 165,185
G 166,186
Zvaa 116,12,56
Ta 0,2,16384
Ta 2,2,17408
Mvaa 116,8,40
buffer_load_dwordx4 v[188:191], v26, s[8:11], s28 offen offset:0 sc1
G 167,187
Zvaa 84,8,36
Ta 4,2,24576
G 172,188
Mvaa 84,12,52
Ta 6,2,25600
G 173,189
Zvaa 52,12,48
G 174,190
G 175,191
Mvaa 52,8,32
W 8
Ta 8,3,16384
K 164,164,165
Zvaa 56,16,32
buffer_load_dwordx4 v[196:199], v26, s[8:11], s93 offen offset:0 sc1
Ta 10,3,17408
Mvaa 56,20,48
K 165,166,167
K 166,172,173
Zvaa 88,20,52
Ta 12,3,24576
K 167,174,175
Mvaa 88,16,36
Ta 14,3,25600
v_accvgpr_write_b32 a184, v164
Zvaa 120,16,40
Mvaa 120,20,56
buffer_load_dwordx4 v[204:207], v27, s[8:11], s28 offen offset:0 sc1
v_accvgpr_write_b32 a185, v165
Zvaa 152,20,60
v_accvgpr_write_b32 a186, v166
Mvaa 152,16,44
W 8
Ta 16,4,16384
v_accvgpr_write_b32 a187, v167
Zvaa 156,24,44
Ta 18,4,17408
v_accvgpr_write_b32 a188, v240
Mvaa 156,28,60
Zvaa 124,28,56
buffer_load_dwordx4 v[212:215], v27, s[8:11], s93 offen offset:0 sc1
v_accvgpr_write_b32 a189, v241
Mvaa 124,24,40
s_add_u32 s28, s28, 512
s_cmp_eq_u32 s28, s4
s_cselect_b32 s93, 10240, 0
)"
R"(s_sub_u32 s28, s28, s93
Zvaa 92,24,36
s_add_u32 s93, s28, 20480
Ta 20,4,24576
Ta 22,4,25600
Mvaa 92,28,52
v_accvgpr_write_b32 a190, v242
v_accvgpr_write_b32 a191, v243
Zvaa 60,28,48
G 164,192
Mvaa 60,24,32
s_waitcnt vmcnt(4) lgkmcnt(8)
buffer_load_dwordx4 v[220:223], v26, s[8:11], s28 offen offset:0 sc1
G 165,193
Mvaa 32,0,128
Ta 24,5,16384
Ta 26,5,17408
Mvaa 32,4,144
G 166,194
G 167,195
Mvaa 64,4,148
Ta 28,5,24576
Ta 30,5,25600
Mvaa 64,0,132
G 172,196
Mvaa 96,0,136
buffer_load_dwordx4 v[228:231], v26, s[8:11], s93 offen offset:0 sc1
G 173,197
Mvaa 96,4,152
G 174,198
Mvaa 128,4,156
G 175,199
Mvaa 128,0,140
W 8
Ta 0,6,16384
Ta 2,6,17408
Mvaa 132,8,140
K 164,164,165
Mvaa 132,12,156
buffer_load_dwordx4 v[236:239], v27, s[8:11], s28 offen offset:0 sc1
K 165,166,167
Mvaa 100,12,152
Ta 4,6,24576
K 166,172,173
Mvaa 100,8,136
Ta 6,6,25600
K 167,174,175
Mvaa 68,8,132
v_accvgpr_write_b32 a192, v164
Mvaa 68,12,148
Mvaa 36,12,144
buffer_load_dwordx4 v[244:247], v27, s[8:11], s93 offen offset:0 sc1
v_accvgpr_write_b32 a193, v165
Mvaa 36,8,128
W 8
s_add_u32 s28, s28, 512
s_cmp_eq_u32 s28, s4
s_cselect_b32 s93, 10240, 0
Mvaa 40,16,128
s_sub_u32 s28, s28, s93
s_add_u32 s93, s28, 20480
v_accvgpr_write_b32 a194, v166
Mvaa 40,20,144
Ta 8,7,16384
Ta 10,7,17408
Mvaa 72,20,148
v_accvgpr_write_b32 a195, v167
v_accvgpr_write_b32 a196, v248
Mvaa 72,16,132
D 22,48,65,2048
Ta 12,7,24576
Mvaa 104,16,136
Ta 14,7,25600
v_accvgpr_write_b32 a197, v249
Mvaa 104,20,152
v_accvgpr_write_b32 a198, v250
v_accvgpr_write_b32 a199, v251
Mvaa 136,20,156
G 164,200
Mvaa 136,16,140
W 8
Ta 16,8,16384
Ta 18,8,17408
Mvaa 140,24,140
buffer_load_dwordx4 a[32:35], v26, s[8:11], s28 offen offset:0 sc1
G 165,201
Mvaa 140,28,156
G 166,202
Mvaa 108,28,152
Ta 20,8,24576
G 167,203
Mvaa 108,24,136
Ta 22,8,25600
G 172,204
Mvaa 76,24,132
Mvaa 76,28,148
buffer_load_dwordx4 a[36:39], v26, s[8:11], s93 offen offset:0 sc1
G 173,205
Mvaa 44,28,144
G 174,206
Mvaa 44,24,128
W 8
Ta 24,9,16384
G 175,207
Mvaa 48,0,128
Ta 26,9,17408
K 164,164,165
Mvaa 48,4,144
Mvaa 80,4,148
buffer_load_dwordx4 a[40:43], v27, s[8:11], s28 offen offset:0 sc1
K 165,166,167
Mvaa 80,0,132
Ta 28,9,24576
Ta 30,9,25600
Mvaa 112,0,136
s_lshl_b32 s80, s56, 9
K 166,172,173
K 167,174,175
Mvaa 112,4,152
v_accvgpr_write_b32 a200, v164
Mvaa 144,4,156
s_lshl_b32 s81, s57, 4
Mvaa 144,0,140
W 8
buffer_load_dwordx4 a[44:47], v27, s[8:11], s93 offen offset:0 sc1
v_accvgpr_write_b32 a201, v165
Mvaa 148,8,140
s_add_u32 s28, s28, 512
s_cmp_eq_u32 s28, s4
s_cselect_b32 s93, 10240, 0
s_sub_u32 s28, s28, s93
Mvaa 148,12,156
s_add_u32 s93, s28, 20480
s_add_u32 s80, s80, s81
Ta 0,10,0
Mvaa 116,12,152
s_and_b32 s81, s14, 1
Ta 2,10,1024
v_accvgpr_write_b32 a202, v166
Mvaa 116,8,136
s_lshl_b32 s81, s81, 3
Ta 4,10,8192
v_accvgpr_write_b32 a203, v167
Mvaa 84,8,132
s_add_u32 s80, s80, s81
buffer_load_dwordx4 a[48:51], v26, s[8:11], s28 offen offset:0 sc1
Ta 6,10,9216
Mvaa 84,12,148
s_add_u32 s71, s80, 4096
v_accvgpr_write_b32 a204, v184
v_accvgpr_write_b32 a205, v185
Mvaa 52,12,144
v_accvgpr_write_b32 a206, v186
v_accvgpr_write_b32 a207, v187
Mvaa 52,8,128
W 8
Ta 8,11,0
Ta 10,11,1024
Mvaa 56,16,128
G 164,208
Mvaa 56,20,144
D 23,48,65,3072
G 165,209
Mvaa 88,20,148
s_add_u32 s65, s5, 16
s_cmp_ge_u32 s65, 20
s_cselect_b32 s90, 20, 0
s_sub_u32 s65, s65, s90
Mvaa 88,16,132
s_lshl_b32 s65, s65, 18
s_add_u32 m0, s63, 98304
G 166,210
Mvaa 120,16,136
Ta 12,11,8192
Ta 14,11,9216
Mvaa 120,20,152
G 167,211
G 172,212
Mvaa 152,20,156
buffer_load_dwordx4 a[52:55], v26, s[8:11], s93 offen offset:0 sc1
G 173,213
Mvaa 152,16,140
W 8
Ta 16,12,0
G 174,214
Mvaa 156,24,140
s_lshr_b32 s80, s15, 3
Ta 18,12,1024
Mvaa 156,28,156
G 175,215
Mvaa 124,28,152
s_lshl_b32 s80, s80, 1
Ta 20,12,8192
K 164,164,165
Mvaa 124,24,136
s_add_u32 s80, s80, s14
buffer_load_dwordx4 a[56:59], v27, s[8:11], s28 offen offset:0 sc1
Ta 22,12,9216
Mvaa 92,24,132
s_sub_u32 s80, s80, 2
K 165,166,167
K 166,172,173
Mvaa 92,28,148
s_lshl_b32 s80, s80, 12
Mvaa 60,28,144
K 167,174,175
v_min_u32 v28, 496, v1
Mvaa 60,24,128
s_waitcnt vmcnt(9) lgkmcnt(8)
v_accvgpr_write_b32 a208, v164
v_lshl_add_u32 v28, v28, 3, s80
Mvav 32,0,188
buffer_load_dwordx4 a[60:63], v27, s[8:11], s93 offen offset:0 sc1
Ta 24,13,0
Mvav 32,4,220
s_add_u32 s28, s28, 512
s_cmp_eq_u32 s28, s4
s_cselect_b32 s93, 10240, 0
s_sub_u32 s28, s28, s93
Mvav 64,4,228
s_add_u32 s93, s28, 20480
Ta 26,13,1024
v_min_u32 v28, .Lend%=-.Lbeg%=-4, v28
Mvav 64,0,196
v_accvgpr_write_b32 a209, v165
v_accvgpr_write_b32 a210, v166
Mvav 96,0,204
Ta 28,13,8192
Ta 30,13,9216
Mvav 96,4,236
D 16,48,65,0
v_accvgpr_write_b32 a211, v167
Mvav 128,4,244
v_accvgpr_write_b32 a212, v192
v_accvgpr_write_b32 a213, v193
Mvav 128,0,212
W 8
Ta 0,14,0
v_accvgpr_write_b32 a214, v194
Mvav 132,8,212
Ta 2,14,1024
v_add_u32 v2, 65536, v10
Mvav 132,12,244
v_accvgpr_write_b32 a215, v195
G 164,216
Mvav 100,12,236
buffer_load_dwordx4 v[184:187], v26, s[8:11], s28 offen offset:0 sc1
Ta 4,14,8192
Mvav 100,8,204
Ta 6,14,9216
v_add_u32 v3, 65536, v11
Mvav 68,8,196
G 165,217
G 166,218
Mvav 68,12,228
G 167,219
Mvav 36,12,220
G 172,220
Mvav 36,8,188
W 8
D 17,48,65,1024
Ta 8,15,0
Mvav 40,16,188
Ta 10,15,1024
v_add_u32 v4, 65536, v12
Mvav 40,20,220
G 173,221
G 174,222
Mvav 72,20,228
Ta 12,15,8192
G 175,223
Mvav 72,16,196
Ta 14,15,9216
v_add_u32 v5, 65536, v13
Mvav 104,16,204
buffer_load_dwordx4 a[128:131], v26, s[8:11], s93 offen offset:0 sc1
K 164,164,165
Mvav 104,20,236
K 165,166,167
Mvav 136,20,244
K 166,172,173
Mvav 136,16,212
W 8
Ta 16,24,0
K 167,174,175
Mvav 140,24,212
Ta 18,24,1024
v_add_u32 v6, 65536, v14
Mvav 140,28,244
buffer_load_dwordx4 a[132:135], v27, s[8:11], s28 offen offset:0 sc1
v_accvgpr_write_b32 a216, v164
Mvav 108,28,236
Ta 20,24,8192
v_accvgpr_write_b32 a217, v165
Mvav 108,24,204
Ta 22,24,9216
v_add_u32 v7, 65536, v15
Mvav 76,24,196
v_accvgpr_write_b32 a218, v166
v_accvgpr_write_b32 a219, v167
Mvav 76,28,228
v_accvgpr_write_b32 a220, v200
Mvav 44,28,220
buffer_load_dwordx4 a[136:139], v27, s[8:11], s93 offen offset:0 sc1
Mvav 44,24,188
W 8
s_add_u32 s28, s28, 512
s_cmp_eq_u32 s28, s4
s_cselect_b32 s93, 10240, 0
Mvav 48,0,188
s_sub_u32 s28, s28, s93
s_add_u32 s93, s28, 20480
v_accvgpr_write_b32 a221, v201
Mvav 48,4,220
Ta 24,25,0
Ta 26,25,1024
Mvav 80,4,228
v_add_u32 v8, 65536, v24
v_accvgpr_write_b32 a222, v202
Mvav 80,0,196
buffer_load_dwordx4 a[140:143], v26, s[8:11], s28 offen offset:0 sc1
v_accvgpr_write_b32 a223, v203
Mvav 112,0,204
Ta 28,25,8192
Ta 30,25,9216
Mvav 112,4,236
v_add_u32 v9, 65536, v25
G 164,224
Mvav 144,4,244
G 165,225
G 166,226
Mvav 144,0,212
W 8
Ta 0,10,16384
G 167,227
Mvav 148,8,212
global_load_dword v29, v28, s[6:7]
Ta 2,10,17408
Mvav 148,12,244
s_sub_u32 s74, s85, 66
s_and_b32 s75, s74, 0xffffffe3
s_cselect_b64 s[72:73], -1, 0
s_and_b32 s74, s74, 28
Mvav 116,12,236
s_lshl_b32 s74, s74, 1
v_mov_b32 v28, s74
G 172,228
Mvav 116,8,204
G 173,229
G 174,230
Mvav 84,8,196
Ta 4,10,24576
Ta 6,10,25600
Mvav 84,12,228
buffer_load_dwordx4 a[144:147], v26, s[8:11], s93 offen offset:0 sc1
G 175,231
Mvav 52,12,220
K 164,164,165
K 165,166,167
Mvav 52,8,188
W 8
Ta 8,11,16384
K 166,172,173
Mvav 56,16,188
Ta 10,11,17408
K 167,174,175
Mvav 56,20,220
v_accvgpr_write_b32 a224, v164
Mvav 88,20,228
buffer_load_dwordx4 a[148:151], v27, s[8:11], s28 offen offset:0 sc1
Ta 12,11,24576
Mvav 88,16,196
Ta 14,11,25600
v_accvgpr_write_b32 a225, v165
Mvav 120,16,204
v_accvgpr_write_b32 a226, v166
Mvav 120,20,236
v_accvgpr_write_b32 a227, v167
Mvav 152,20,244
v_accvgpr_write_b32 a228, v208
Mvav 152,16,212
W 8
D 18,48,65,2048
Ta 16,12,16384
Mvav 156,24,212
Ta 18,12,17408
v_accvgpr_write_b32 a229, v209
Mvav 156,28,244
v_accvgpr_write_b32 a230, v210
Mvav 124,28,236
Ta 20,12,24576
v_accvgpr_write_b32 a231, v211
Mvav 124,24,204
Ta 22,12,25600
G 164,232
Mvav 92,24,196
buffer_load_dwordx4 a[152:155], v27, s[8:11], s93 offen offset:0 sc1
G 165,233
Mvav 92,28,228
s_add_u32 s28, s28, 512
)"
R"(s_cmp_eq_u32 s28, s4
s_cselect_b32 s93, 10240, 0
s_sub_u32 s28, s28, s93
Mvav 60,28,220
s_add_u32 s93, s28, 20480
G 166,234
G 167,235
Mvav 60,24,188
s_waitcnt vmcnt(12) lgkmcnt(8)
G 172,236
G 173,237
Mvaa 32,0,32
Ta 24,13,16384
Ta 26,13,17408
Mvaa 32,4,48
buffer_load_dwordx2 v[28:29], v28, s[8:11], 0 offen offset:3584 sc1
G 174,238
Mvaa 64,4,52
G 175,239
K 164,164,165
Mvaa 64,0,36
Ta 28,13,24576
Ta 30,13,25600
Mvaa 96,0,40
K 165,166,167
K 166,172,173
Mvaa 96,4,56
K 167,174,175
v_accvgpr_write_b32 a232, v164
Mvaa 128,4,60
buffer_load_dwordx4 a[156:159], v26, s[8:11], s28 offen offset:0 sc1
v_accvgpr_write_b32 a233, v165
Mvaa 128,0,44
W 8
v_accvgpr_write_b32 a234, v166
v_accvgpr_write_b32 a235, v167
Mvaa 132,8,44
Ta 0,14,16384
Ta 2,14,17408
Mvaa 132,12,60
G 164,160
G 165,161
Mvaa 100,12,56
G 166,162
G 167,163
Mvaa 100,8,40
buffer_load_dwordx4 v[192:195], v26, s[8:11], s93 offen offset:0 sc1
Ta 4,14,24576
Mvaa 68,8,36
Ta 6,14,25600
G 172,164
Mvaa 68,12,52
G 173,165
G 174,166
Mvaa 36,12,48
G 175,167
K 164,164,165
Mvaa 36,8,32
W 8
Ta 8,15,16384
K 165,166,167
Mvaa 40,16,32
buffer_load_dwordx4 v[200:203], v27, s[8:11], s28 offen offset:0 sc1
Ta 10,15,17408
Mvaa 40,20,48
K 166,172,173
K 167,174,175
Mvaa 72,20,52
s_cmp_lg_u32 s72, 0
s_cbranch_scc1 .LL_55%=
s_waitcnt vmcnt(3)
.LL_55%=:
v_xor_b32 v28, s12, v28
v_xor_b32 v29, s13, v29
v_lshrrev_b32 v248, 11, v28
v_or_b32 v29, v29, v248
v_cmp_eq_u32 vcc, 0, v29
s_or_b64 vcc, vcc, s[72:73]
s_cmp_eq_u64 vcc, -1
s_cbranch_scc0 .Lspin_56%=
.Lok_57%=:
v_accvgpr_write_b32 a236, v164
v_accvgpr_write_b32 a237, v165
Mvaa 72,16,36
Ta 12,15,24576
Ta 14,15,25600
Mvaa 104,16,40
v_accvgpr_write_b32 a238, v166
v_accvgpr_write_b32 a239, v167
Mvaa 104,20,56
D 19,48,65,3072
G 164,240
Mvaa 136,20,60
s_add_u32 m0, s63, 102400
G 165,241
G 166,242
Mvaa 136,16,44
W 8
v_bfe_u32 v248, v28, 3, 8
G 167,243
Mvaa 140,24,44
Ta 16,24,16384
Ta 18,24,17408
Mvaa 140,28,60
v_mul_u32_u24 v248, 10240, v248
G 172,244
Mvaa 108,28,56
buffer_load_dwordx4 v[188:191], v27, s[8:11], s93 offen offset:0 sc1
G 173,245
Mvaa 108,24,40
s_add_u32 s28, s28, 512
s_cmp_eq_u32 s28, s4
s_cselect_b32 s93, 10240, 0
s_sub_u32 s28, s28, s93
Mvaa 76,24,36
s_add_u32 s93, s28, 20480
v_and_b32 v28, 7, v28
G 174,246
Mvaa 76,28,52
v_lshl_add_u32 v248, v28, 4, v248
G 175,247
Mvaa 44,28,48
Ta 20,24,24576
Ta 22,24,25600
Mvaa 44,24,32
W 8
buffer_load_dwordx4 v[196:199], v26, s[8:11], s28 offen offset:0 sc1
K 164,164,165
Mvaa 48,0,32
v_lshl_add_u32 v248, v1, 3, v248
K 165,166,167
Mvaa 48,4,48
v_lshrrev_b32 v29, 8, v1
K 166,172,173
Mvaa 80,4,52
K 167,174,175
v_lshlrev_b32 v29, 28, v29
Mvaa 80,0,36
v_accvgpr_write_b32 a240, v164
v_or_b32 v249, v248, v29
Mvaa 112,0,40
s_and_b32 s75, s72, 134217728
buffer_load_dwordx4 v[204:207], v26, s[8:11], s93 offen offset:0 sc1
v_accvgpr_write_b32 a241, v165
Mvaa 112,4,56
s_add_u32 s76, s75, 131072
Ta 24,25,16384
Ta 26,25,17408
Mvaa 144,4,60
v_accvgpr_write_b32 a242, v166
v_accvgpr_write_b32 a243, v167
Mvaa 144,0,44
W 6
G 164,168
G 165,169
Mvaa 148,8,44
Ta 28,25,24576
Ta 30,25,25600
Mvaa 148,12,60
D 20,48,65,0
G 166,170
Mvaa 116,12,56
G 167,171
G 172,172
Mvaa 116,8,40
G 173,173
G 174,174
Mvaa 84,8,36
Ta 0,2,0
G 175,175
Mvaa 84,12,52
Ta 2,2,1024
K 164,164,165
Mvaa 52,12,48
buffer_load_dwordx4 v[212:215], v27, s[8:11], s28 offen offset:0 sc1
K 165,166,167
Mvaa 52,8,32
W 6
K 166,172,173
K 167,174,175
Mvaa 56,16,32
Ta 4,2,8192
Ta 6,2,9216
Mvaa 56,20,48
v_accvgpr_write_b32 a244, v164
v_accvgpr_write_b32 a245, v165
Mvaa 88,20,52
v_accvgpr_write_b32 a246, v166
v_accvgpr_write_b32 a247, v167
Mvaa 88,16,36
buffer_load_dwordx4 v[220:223], v27, s[8:11], s93 offen offset:0 sc1
G 164,64
Mvaa 120,16,40
s_add_u32 s28, s28, 512
s_cmp_eq_u32 s28, s4
s_cselect_b32 s93, 10240, 0
s_sub_u32 s28, s28, s93
Mvaa 120,20,56
s_add_u32 s93, s28, 20480
Ta 8,3,0
G 165,65
Mvaa 152,20,60
Ta 10,3,1024
G 166,66
Mvaa 152,16,44
W 6
G 167,67
G 172,68
Mvaa 156,24,44
buffer_load_dwordx4 v[228:231], v26, s[8:11], s28 offen offset:0 sc1
G 173,69
Mvaa 156,28,60
Ta 12,3,8192
Ta 14,3,9216
Mvaa 124,28,56
G 174,70
G 175,71
Mvaa 124,24,40
K 164,164,165
K 165,166,167
Mvaa 92,24,36
K 166,172,173
K 167,174,175
Mvaa 92,28,52
buffer_load_dwordx4 v[240:243], v248, s[8:11], s76 offen sc1
Ta 16,4,0
Mvaa 60,28,48
s_add_u32 s76, s76, 8192
Ta 18,4,1024
v_accvgpr_write_b32 a64, v164
Mvaa 60,24,32
s_waitcnt vmcnt(13) lgkmcnt(6)
v_accvgpr_write_b32 a65, v165
v_accvgpr_write_b32 a66, v166
Mvav 32,0,184
Ta 20,4,8192
v_accvgpr_write_b32 a67, v167
Mvaa 32,4,140
Ta 22,4,9216
G 164,176
Mvaa 64,4,144
buffer_load_dwordx4 v[244:247], v249, s[8:11], s76 offen sc1
v_or_b32 v249, v1, v29
Mvaa 64,0,128
s_mul_i32 s76, s74, 160
s_add_u32 s76, s76, s75
s_add_u32 s76, s76, 118784
Mvaa 96,0,132
G 165,177
G 166,178
Mvaa 96,4,148
G 167,179
G 172,180
Mvaa 128,4,152
Ta 24,5,0
G 173,181
Mvaa 128,0,136
W 5
buffer_load_dwordx4 v[236:239], v26, s[8:11], s93 offen offset:0 sc1
Ta 26,5,1024
Mvaa 132,8,136
G 174,182
G 175,183
Mvaa 132,12,152
K 164,164,165
K 165,166,167
Mvaa 100,12,148
Ta 28,5,8192
Ta 30,5,9216
Mvaa 100,8,132
K 166,172,173
K 167,174,175
Mvaa 68,8,128
D 21,48,65,1024
v_accvgpr_write_b32 a68, v164
Mvaa 68,12,144
v_accvgpr_write_b32 a69, v165
v_accvgpr_write_b32 a70, v166
Mvaa 36,12,140
Ta 0,6,0
v_accvgpr_write_b32 a71, v167
Mvav 36,8,184
W 5
Ta 2,6,1024
G 164,72
Mvav 40,16,184
G 165,73
G 166,74
Mvaa 40,20,140
buffer_load_dwordx4 v[208:211], v27, s[8:11], s28 offen offset:0 sc1
Ta 4,6,8192
Mvaa 72,20,144
Ta 6,6,9216
G 167,75
Mvaa 72,16,128
G 172,76
G 173,77
Mvaa 104,16,132
Ta 8,7,0
G 174,78
Mvaa 104,20,148
Ta 10,7,1024
G 175,79
Mvaa 136,20,152
buffer_load_dwordx4 a[32:35], v27, s[8:11], s93 offen offset:0 sc1
K 164,164,165
Mvaa 136,16,136
W 6
s_add_u32 s28, s28, 512
s_cmp_eq_u32 s28, s4
s_cselect_b32 s93, 10240, 0
Mvaa 140,24,136
s_sub_u32 s28, s28, s93
s_add_u32 s93, s28, 20480
Ta 12,7,8192
Mvaa 140,28,152
Ta 14,7,9216
K 165,166,167
Mvaa 108,28,148
K 166,172,173
K 167,174,175
Mvaa 108,24,132
buffer_load_dwordx4 a[36:39], v26, s[8:11], s28 offen offset:0 sc1
v_accvgpr_write_b32 a72, v164
Mvaa 76,24,128
Ta 16,8,0
Ta 18,8,1024
Mvaa 76,28,144
v_accvgpr_write_b32 a73, v165
v_accvgpr_write_b32 a74, v166
Mvaa 44,28,140
Ta 20,8,8192
v_accvgpr_write_b32 a75, v167
Mvav 44,24,184
W 7
Ta 22,8,9216
v_accvgpr_write_b32 a76, v216
Mvav 48,0,184
D 22,48,65,2048
v_accvgpr_write_b32 a77, v217
Mvaa 48,4,140
v_accvgpr_write_b32 a78, v218
v_accvgpr_write_b32 a79, v219
Mvaa 80,4,144
Ta 24,9,0
Ta 26,9,1024
Mvaa 80,0,128
G 164,80
G 165,81
Mvaa 112,0,132
Ta 28,9,8192
Ta 30,9,9216
Mvaa 112,4,148
buffer_load_dwordx4 a[40:43], v26, s[8:11], s93 offen offset:0 sc1
G 166,82
Mvaa 144,4,152
G 167,83
G 172,84
Mvaa 144,0,136
W 8
Ta 0,2,16384
G 173,85
Mvaa 148,8,136
Ta 2,2,17408
G 174,86
Mvaa 148,12,152
G 175,87
Mvaa 116,12,148
buffer_load_dwordx4 a[44:47], v27, s[8:11], s28 offen offset:0 sc1
Ta 4,2,24576
Mvaa 116,8,132
Ta 6,2,25600
K 164,164,165
Mvaa 84,8,128
K 165,166,167
Mvaa 84,12,144
K 166,172,173
Mvaa 52,12,140
K 167,174,175
Mvav 52,8,184
W 8
buffer_load_dwordx4 a[48:51], v27, s[8:11], s93 offen offset:0 sc1
Ta 8,3,16384
Mvav 56,16,184
s_add_u32 s28, s28, 512
s_cmp_eq_u32 s28, s4
s_cselect_b32 s93, 10240, 0
D 23,48,65,3072
Mvaa 56,20,140
s_sub_u32 s28, s28, s93
s_add_u32 s93, s28, 20480
Ta 10,3,17408
Mvaa 88,20,144
v_accvgpr_write_b32 a80, v164
v_accvgpr_write_b32 a81, v165
Mvaa 88,16,128
Ta 12,3,24576
v_accvgpr_write_b32 a82, v166
Mvaa 120,16,132
Ta 14,3,25600
)"
R"(v_accvgpr_write_b32 a83, v167
Mvaa 120,20,148
buffer_load_dwordx4 a[52:55], v26, s[8:11], s28 offen offset:0 sc1
v_accvgpr_write_b32 a84, v224
Mvaa 152,20,152
v_lshrrev_b32 v20, 4, v1
v_accvgpr_write_b32 a85, v225
Mvaa 152,16,136
W 8
v_accvgpr_write_b32 a86, v226
v_add_u32 v20, 0, v20
Mvaa 156,24,136
Ta 16,4,16384
Ta 18,4,17408
Mvaa 156,28,152
v_accvgpr_write_b32 a87, v227
v_mul_u32_u24 v22, 205, v20
Mvaa 124,28,148
buffer_load_dwordx4 a[56:59], v26, s[8:11], s93 offen offset:0 sc1
G 164,88
Mvaa 124,24,132
Ta 20,4,24576
Ta 22,4,25600
Mvaa 92,24,128
v_lshrrev_b32 v22, 12, v22
G 165,89
Mvaa 92,28,144
G 166,90
v_mul_u32_u24 v23, 20, v22
Mvaa 60,28,140
G 167,91
v_sub_u32 v20, v20, v23
Mvav 60,24,184
s_waitcnt vmcnt(15) lgkmcnt(8)
buffer_load_dwordx4 a[60:63], v27, s[8:11], s28 offen offset:0 sc1
G 172,92
Mvaa 32,0,156
Ta 24,5,16384
Ta 26,5,17408
Mvav 32,4,196
v_lshrrev_b32 v23, 1, v22
G 173,93
Mvav 64,4,204
G 174,94
v_and_b32 v23, 2, v23
Mvav 64,0,192
Ta 28,5,24576
Ta 30,5,25600
Mvav 96,0,200
buffer_load_dwordx4 v[216:219], v27, s[8:11], s93 offen offset:0 sc1
G 175,95
Mvav 96,4,212
s_add_u32 s28, s28, 512
s_cmp_eq_u32 s28, s4
s_cselect_b32 s93, 10240, 0
s_sub_u32 s28, s28, s93
Mvav 128,4,220
s_add_u32 s93, s28, 20480
v_xor_b32 v20, v20, v23
K 164,164,165
Mvav 128,0,188
W 8
v_mul_u32_u24 v22, 80, v22
K 165,166,167
Mvav 132,8,188
K 166,172,173
v_add_lshl_u32 v16, v20, v22, 4
Mvav 132,12,220
buffer_load_dwordx4 v[184:187], v26, s[8:11], s28 offen offset:0 sc1
Ta 0,6,16384
Mvav 100,12,212
Ta 2,6,17408
K 167,174,175
Mvav 100,8,200
v_lshrrev_b32 v20, 4, v1
v_accvgpr_write_b32 a88, v164
Mvav 68,8,192
v_add_u32 v20, 64, v20
v_accvgpr_write_b32 a89, v165
Mvav 68,12,204
Ta 4,6,24576
Ta 6,6,25600
Mvav 36,12,196
s_cmp_lg_u32 s72, 0
s_cbranch_scc1 .LL_59%=
s_waitcnt vmcnt(15)
.LL_59%=:
buffer_store_dwordx4 v[240:243], v1, s[8:11], s76 offen sc1
v_accvgpr_write_b32 a90, v166
v_mul_u32_u24 v22, 205, v20
Mvaa 36,8,156
W 8
v_accvgpr_write_b32 a91, v167
v_lshrrev_b32 v22, 12, v22
Mvaa 40,16,156
v_accvgpr_write_b32 a92, v232
v_mul_u32_u24 v23, 20, v22
Mvav 40,20,196
Ta 8,7,16384
Ta 10,7,17408
Mvav 72,20,204
v_accvgpr_write_b32 a93, v233
v_accvgpr_write_b32 a94, v234
Mvav 72,16,192
buffer_store_dwordx4 v[244:247], v249, s[8:11], s76 offen offset:1024 sc1
v_sub_u32 v20, v20, v23
Mvav 104,16,200
s_add_u32 s76, s74, s75
s_add_u32 s76, s76, 3840
v_accvgpr_write_b32 a95, v235
Mvav 104,20,212
Ta 12,7,24576
Ta 14,7,25600
Mvav 136,20,220
v_lshrrev_b32 v23, 1, v22
G 164,96
Mvav 136,16,188
W 8
G 165,97
v_and_b32 v23, 2, v23
Mvav 140,24,188
buffer_load_dwordx4 a[128:131], v26, s[8:11], s93 offen offset:0 sc1
G 166,98
Mvav 140,28,220
G 167,99
v_xor_b32 v20, v20, v23
Mvav 108,28,212
G 172,100
G 173,101
Mvav 108,24,200
Ta 16,8,16384
Ta 18,8,17408
Mvav 76,24,192
v_mul_u32_u24 v22, 80, v22
G 174,102
Mvav 76,28,204
buffer_load_dwordx4 a[132:135], v27, s[8:11], s28 offen offset:0 sc1
G 175,103
Mvav 44,28,196
v_add_lshl_u32 v17, v20, v22, 4
K 164,164,165
Mvaa 44,24,156
W 6
Ta 20,8,24576
Ta 22,8,25600
Mvaa 48,0,156
K 165,166,167
K 166,172,173
Mvav 48,4,196
v_subrev_u32 v17, 1024, v17
K 167,174,175
Mvav 80,4,204
buffer_load_dwordx4 a[136:139], v27, s[8:11], s93 offen offset:0 sc1
v_accvgpr_write_b32 a96, v164
Mvav 80,0,192
s_add_u32 s28, s28, 512
v_lshrrev_b32 v20, 4, v1
v_accvgpr_write_b32 a97, v165
Mvav 112,0,200
s_cmp_eq_u32 s28, s4
s_cselect_b32 s93, 10240, 0
v_accvgpr_write_b32 a98, v166
v_add_u32 v20, 128, v20
Mvav 112,4,212
s_sub_u32 s28, s28, s93
Ta 24,9,16384
v_accvgpr_write_b32 a99, v167
Mvav 144,4,220
s_add_u32 s93, s28, 20480
Ta 26,9,17408
G 164,248
Mvav 144,0,188
W 6
buffer_load_dwordx4 a[140:143], v26, s[8:11], s28 offen offset:0 sc1
v_mul_u32_u24 v22, 205, v20
Mvav 148,8,188
G 165,249
G 166,250
Mvav 148,12,220
Ta 28,9,24576
Ta 30,9,25600
Mvav 116,12,212
v_lshrrev_b32 v22, 12, v22
G 167,251
Mvav 116,8,200
G 172,252
G 173,253
Mvav 84,8,192
s_waitcnt vmcnt(11) lgkmcnt(0)
s_barrier
Mvav 84,12,204
v_mul_u32_u24 v23, 20, v22
G 174,254
Mvav 52,12,196
G 175,255
v_sub_u32 v20, v20, v23
Mvaa 52,8,156
K 164,164,165
K 165,166,167
Mvaa 56,16,156
v_lshrrev_b32 v23, 1, v22
K 166,172,173
Mvav 56,20,196
buffer_load_dwordx4 a[144:147], v26, s[8:11], s93 offen offset:0 sc1
K 167,174,175
Mvav 88,20,204
v_and_b32 v23, 2, v23
v_accvgpr_write_b32 a100, v164
Mvav 88,16,192
v_accvgpr_write_b32 a101, v165
v_xor_b32 v20, v20, v23
Mvav 120,16,200
v_accvgpr_write_b32 a102, v166
v_accvgpr_write_b32 a103, v167
Mvav 120,20,212
Ta 0,10,32768
Ta 2,10,33792
Mvav 152,20,220
Ta 4,10,40960
Ta 6,10,41984
Mvav 152,16,188
G 164,104
v_mul_u32_u24 v22, 80, v22
Mvav 156,24,188
G 165,105
v_add_lshl_u32 v18, v20, v22, 4
Mvav 156,28,220
Ta 8,11,32768
G 166,106
Mvav 124,28,212
Ta 10,11,33792
G 167,107
Mvav 124,24,200
buffer_load_dwordx4 a[148:151], v27, s[8:11], s28 offen offset:0 sc1
v_subrev_u32 v18, 2048, v18
Mvav 92,24,192
G 172,108
G 173,109
Mvav 92,28,204
Ta 12,11,40960
v_lshrrev_b32 v20, 4, v1
Mvav 60,28,196
Ta 14,11,41984
G 174,110
Mvaa 60,24,156
W 4
v_add_u32 v20, 192, v20
G 175,111
Mvav 32,0,228
K 164,164,165
v_mul_u32_u24 v22, 205, v20
Mvaa 32,4,36
K 165,166,167
v_lshrrev_b32 v22, 12, v22
Mvaa 64,4,40
Ta 16,12,32768
K 166,172,173
Mvav 64,0,236
Ta 18,12,33792
K 167,174,175
Mvav 96,0,208
buffer_load_dwordx4 a[152:155], v27, s[8:11], s93 offen offset:0 sc1
v_mul_u32_u24 v23, 20, v22
Mvaa 96,4,44
s_add_u32 s28, s28, 512
v_accvgpr_write_b32 a104, v164
v_accvgpr_write_b32 a105, v165
Mvaa 128,4,48
s_cmp_eq_u32 s28, s4
s_cselect_b32 s93, 10240, 0
Ta 20,12,40960
v_sub_u32 v20, v20, v23
Mvaa 128,0,32
W 3
Ta 22,12,41984
v_accvgpr_write_b32 a106, v166
Mvaa 132,8,32
s_sub_u32 s28, s28, s93
v_lshrrev_b32 v23, 1, v22
v_accvgpr_write_b32 a107, v167
Mvaa 132,12,48
s_add_u32 s93, s28, 20480
K 176,176,177
v_and_b32 v23, 2, v23
Mvaa 100,12,44
K 177,178,179
K 178,180,181
Mvav 100,8,208
Ta 24,13,32768
v_xor_b32 v20, v20, v23
Mvav 68,8,236
Ta 26,13,33792
K 179,182,183
Mvaa 68,12,40
v_accvgpr_write_b32 a108, v176
v_accvgpr_write_b32 a109, v177
v_accvgpr_write_b32 a110, v178
v_accvgpr_write_b32 a111, v179
Mvaa 36,12,36
buffer_load_dwordx4 v[224:227], v26, s[8:11], s28 offen offset:0 sc1
v_mul_u32_u24 v22, 80, v22
Mvav 36,8,228
W 2
Ta 28,13,40960
Ta 30,13,41984
Mvav 40,16,228
v_add_lshl_u32 v19, v20, v22, 4
v_subrev_u32 v19, 3072, v19
Mvaa 40,20,36
G 164,112
v_lshrrev_b32 v20, 4, v1
Mvaa 72,20,40
Ta 0,14,32768
G 165,113
Mvav 72,16,236
s_cmp_lg_u32 s72, 0
s_cbranch_scc1 .LL_60%=
s_waitcnt vmcnt(8)
.LL_60%=:
v_mov_b64 v[28:29], s[12:13]
s_mov_b64 exec, 1
buffer_store_dwordx2 v[28:29], off, s[8:11], s76 sc1
s_mov_b64 exec, -1
Ta 2,14,33792
G 166,114
Mvav 104,16,208
v_add_u32 v20, 256, v20
G 167,115
Mvaa 104,20,44
Ta 4,14,40960
v_mul_u32_u24 v22, 205, v20
Mvaa 136,20,48
Ta 6,14,41984
G 172,116
Mvaa 136,16,32
W 4
v_lshrrev_b32 v22, 12, v22
G 173,117
Mvaa 140,24,32
buffer_load_dwordx4 v[232:235], v26, s[8:11], s93 offen offset:0 sc1
G 174,118
Mvaa 140,28,48
v_mul_u32_u24 v23, 20, v22
G 175,119
Mvaa 108,28,44
Ta 8,15,32768
v_sub_u32 v20, v20, v23
Mvav 108,24,208
Ta 10,15,33792
K 164,164,165
Mvav 76,24,236
v_lshrrev_b32 v23, 1, v22
K 165,166,167
Mvaa 76,28,40
buffer_load_dwordx4 a[248:251], v27, s[8:11], s28 offen offset:0 sc1
K 166,172,173
Mvaa 44,28,36
Ta 12,15,40960
v_and_b32 v23, 2, v23
Mvav 44,24,228
W 3
Ta 14,15,41984
K 167,174,175
Mvav 48,0,228
v_xor_b32 v20, v20, v23
v_accvgpr_write_b32 a112, v164
Mvaa 48,4,36
v_accvgpr_write_b32 a113, v165
v_mul_u32_u24 v22, 80, v22
Mvaa 80,4,40
Ta 16,24,32768
v_accvgpr_write_b32 a114, v166
Mvav 80,0,236
Ta 18,24,33792
v_add_lshl_u32 v20, v20, v22, 4
Mvav 112,0,208
buffer_load_dwordx4 a[252:255], v27, s[8:11], s93 offen offset:0 sc1
v_accvgpr_write_b32 a115, v167
Mvaa 112,4,44
s_add_u32 s28, s28, 512
v_lshrrev_b32 v22, 6, v1
v_accvgpr_write_b32 a116, v168
Mvaa 144,4,48
v_accvgpr_write_b32 a117, v169
v_accvgpr_write_b32 a118, v170
v_accvgpr_write_b32 a119, v171
Mvaa 144,0,32
W 2
Ta 20,24,40960
Ta 22,24,41984
Mvaa 148,8,32
s_cmp_eq_u32 s28, s4
s_cselect_b32 s93, 10240, 0
v_mul_u32_u24 v22, 320, v22
v_bfe_u32 v23, v1, 4, 2
Mvaa 148,12,48
s_sub_u32 s28, s28, s93
v_lshl_add_u32 v22, v23, 3, v22
G 164,120
Mvaa 116,12,44
)"
R"(s_add_u32 s93, s28, 20480
Ta 24,25,32768
Ta 26,25,33792
Mvav 116,8,208
G 165,121
v_bfe_u32 v23, v1, 8, 1
Mvav 84,8,236
buffer_load_dwordx4 v[192:195], v26, s[8:11], s28 offen offset:0 sc1
G 166,122
Mvaa 84,12,40
v_lshlrev_b32 v23, 5, v23
G 167,123
Mvaa 52,12,36
Ta 28,25,40960
Ta 30,25,41984
Mvav 52,8,228
W 4
v_add_u32 v21, v22, v23
G 172,124
Mvav 56,16,228
G 173,125
v_sub_u32 v22, v22, v23
Mvaa 56,20,36
s_mul_i32 s94, s59, 655360
Ta 0,10,49152
G 174,126
Mvaa 88,20,40
s_mul_i32 s95, s58, 320
Ta 2,10,50176
G 175,127
Mvav 88,16,236
s_add_u32 s94, s94, s95
buffer_load_dwordx4 v[200:203], v26, s[8:11], s93 offen offset:0 sc1
K 164,164,165
Mvav 120,16,208
Ta 4,10,57344
K 165,166,167
Mvaa 120,20,44
Ta 6,10,58368
K 166,172,173
Mvaa 152,20,48
s_add_u32 s40, s26, s94
s_addc_u32 s41, s27, 0
K 167,174,175
Mvaa 152,16,32
W 4
v_accvgpr_write_b32 a120, v164
v_accvgpr_write_b32 a121, v165
Mvaa 156,24,32
s_and_b32 s41, s41, 0xffff
Ta 8,11,49152
Ta 10,11,50176
Mvaa 156,28,48
s_mov_b32 s42, 5242880
buffer_load_dwordx4 v[188:191], v27, s[8:11], s28 offen offset:0 sc1
v_accvgpr_write_b32 a122, v166
Mvaa 124,28,44
s_mov_b32 s43, 0x20000
Ta 12,11,57344
v_accvgpr_write_b32 a123, v167
Mvav 124,24,208
s_sub_u32 s42, s42, s94
Ta 14,11,58368
v_accvgpr_write_b32 a124, v160
Mvav 92,24,236
s_mov_b64 s[44:45], s[8:9]
v_accvgpr_write_b32 a125, v161
v_accvgpr_write_b32 a126, v162
Mvaa 92,28,40
s_mov_b32 s47, 0x20000
Ta 16,12,49152
v_accvgpr_write_b32 a127, v163
Mvaa 60,28,36
s_and_b32 s48, s14, 1
Ta 18,12,50176
buffer_load_dwordx4 v[196:199], v27, s[8:11], s93 offen offset:0 sc1
Mvav 60,24,228
s_waitcnt vmcnt(13) lgkmcnt(6)
v_bfe_u32 v28, v1, 4, 2
v_lshlrev_b32 v28, 3, v28
Mvaa 32,0,52
s_lshl_b32 s80, s56, 9
Ta 20,12,57344
Ta 22,12,58368
Mvav 32,4,184
s_mul_i32 s48, s48, 5120
s_add_u32 s80, s80, 4096
s_lshr_b32 s51, s14, 1
Mvaa 64,4,128
s_and_b32 s94, s14, 1
Ta 24,13,49152
Ta 26,13,50176
Mvaa 64,0,56
s_add_u32 s28, s28, 512
s_cmp_eq_u32 s28, s4
s_cselect_b32 s93, 10240, 0
buffer_load_dwordx2 v[28:29], v28, s[8:11], s80 offen sc1
Mvaa 96,0,60
s_sub_u32 s28, s28, s93
s_add_u32 s93, s28, 20480
s_lshl_b32 s95, s57, 1
Mvaa 96,4,132
s_add_u32 s94, s94, s95
Ta 28,13,57344
Ta 30,13,58368
Mvaa 128,4,136
s_lshl_b32 s54, s94, 3
s_add_u32 s54, s54, s59
Mvav 128,0,216
W 8
Ta 0,14,49152
Ta 2,14,50176
Mvav 132,8,216
s_lshl_b32 s54, s54, 6
buffer_load_dwordx4 v[204:207], v26, s[8:11], s28 offen offset:0 sc1
Mvaa 132,12,136
s_xor_b32 s55, s54, 15872
Mvaa 100,12,132
Ta 4,14,57344
Ta 6,14,58368
Mvaa 100,8,60
s_add_u32 s54, s54, 20480
Mvaa 68,8,56
s_and_b32 s81, s14, 1
Mvaa 68,12,128
s_add_u32 s55, s55, 20480
buffer_load_dwordx4 v[212:215], v26, s[8:11], s93 offen offset:0 sc1
Mvav 36,12,184
Mvaa 36,8,52
W 8
s_and_b32 s95, s14, 2
s_mul_i32 s91, s81, 17164288
Mvaa 40,16,52
s_lshl_b32 s94, s94, 2
Ta 8,15,49152
Ta 10,15,50176
Mvav 40,20,184
s_mul_i32 s82, s56, 536576
Mvaa 72,20,128
s_add_u32 s94, s94, s95
buffer_load_dwordx4 v[220:223], v27, s[8:11], s28 offen offset:0 sc1
Ta 12,15,57344
Mvaa 72,16,56
Ta 14,15,58368
Mvaa 104,16,60
s_lshl_b32 s95, s95, 4
s_add_u32 s91, s91, s82
Mvaa 104,20,132
Mvaa 136,20,136
s_add_u32 s55, s55, s95
s_add_u32 s91, s91, 4194304
Mvav 136,16,216
W 8
s_lshl_b32 s49, s94, 17
Ta 16,24,49152
Mvav 140,24,216
Ta 18,24,50176
buffer_load_dwordx4 v[176:179], v27, s[8:11], s93 offen offset:0 sc1
Mvaa 140,28,136
s_lshl_b32 s95, s59, 14
s_lshr_b32 s82, s14, 1
Mvaa 108,28,132
Ta 20,24,57344
Ta 22,24,58368
Mvaa 108,24,60
s_add_u32 s49, s49, s95
s_lshl_b32 s83, s82, 14
Mvaa 76,24,56
s_xor_b32 s50, s49, 16252928
Mvaa 76,28,128
Mvav 44,28,184
s_add_u32 s91, s91, s83
s_add_u32 s49, s49, 39845888
Mvaa 44,24,52
W 8
Ta 24,25,49152
Ta 26,25,50176
Mvaa 48,0,52
s_add_u32 s50, s50, 39845888
s_lshl_b32 s81, s81, 14
Mvav 48,4,184
Mvaa 80,4,128
s_lshr_b32 s94, s15, 3
Ta 28,25,57344
Ta 30,25,58368
Mvaa 80,0,56
s_lshl_b32 s82, s82, 16
s_lshl_b32 s94, s94, 3
Mvaa 112,0,60
Mvaa 112,4,132
s_add_u32 s94, s94, s14
s_or_b32 s92, s81, s82
Mvaa 144,4,136
Mvav 144,0,216
W 8
s_lshl_b32 s94, s94, 4
Ta 0,2,32768
Mvav 148,8,216
s_xor_b32 s92, s92, 0x10000
s_add_u32 s94, s94, s59
Ta 2,2,33792
Mvaa 148,12,136
Mvaa 116,12,132
s_mul_i32 s52, s94, 5120
Ta 4,2,40960
Ta 6,2,41984
Mvaa 116,8,60
Mvaa 84,8,56
s_lshl_b32 s95, s59, 12
Mvaa 84,12,128
Mvav 52,12,184
s_sub_u32 s52, s52, s95
Mvaa 52,8,52
W 8
s_add_u32 s52, s52, 56623104
Ta 8,3,32768
Mvaa 56,16,52
Ta 10,3,33792
Mvav 56,20,184
s_lshl_b32 s53, s94, 3
Mvaa 88,20,128
Ta 12,3,40960
Ta 14,3,41984
Mvaa 88,16,56
s_add_u32 s53, s53, 65536
Mvaa 120,16,60
s_waitcnt vmcnt(4)
v_cmp_eq_u64 vcc, s[12:13], v[28:29]
s_cmp_eq_u64 vcc, -1
s_cbranch_scc0 .Lspin_61%=
.Lok_62%=:
s_sub_u32 s82, s91, 16384
Mvaa 120,20,132
s_xor_b32 s83, s92, 0x10000
s_and_b32 s5, s14, 1
s_lshl_b32 s5, s5, 13
Mvaa 152,20,136
s_add_u32 s5, s82, s5
s_add_u32 m0, s83, 0
s_add_u32 s81, s5, 0
Mvav 152,16,216
W 8
v_bfe_u32 v244, v1, 4, 1
v_mul_u32_u24 v244, 0xaaaa, v244
Mvav 156,24,216
Dc 1,8,81,0
v_add_u32 v244, 0x4444, v244
Mvaa 156,28,136
Ta 16,4,32768
Ta 18,4,33792
Mvaa 124,28,132
v_lshl_or_b32 v240, v244, 16, v244
v_mov_b32 v241, 0xff800000
Mvaa 124,24,60
Ta 20,4,40960
Ta 22,4,41984
Mvaa 92,24,56
v_add_u32 v242, 0x10000, v1
Mvaa 92,28,128
Dc 1,8,81,1024
v_bfe_u32 v244, v1, 4, 4
Mvav 60,28,184
v_lshrrev_b32 v245, 8, v1
Mvaa 60,24,52
W 8
Ta 24,5,32768
v_lshlrev_b32 v245, 2, v245
Mvaa 32,0,140
Ta 26,5,33792
Mvav 32,4,224
v_sub_u32 v244, v244, v245
Mvav 64,4,232
Dc 1,8,81,2048
v_cmp_le_i32_e64 s[72:73], 0, v244
Mvaa 64,0,144
Ta 28,5,40960
Ta 30,5,41984
Mvaa 96,0,148
v_cmp_le_i32_e64 s[74:75], 1, v244
Mvaa 96,4,248
v_cmp_le_i32_e64 s[76:77], 2, v244
Mvaa 128,4,252
v_cmp_le_i32_e64 s[78:79], 3, v244
Mvaa 128,0,152
W 8
Dc 1,8,81,3072
Ta 0,6,32768
Mvaa 132,8,152
s_add_u32 m0, s83, 4096
s_add_u32 s81, s5, 4096
Ta 2,6,33792
Mvaa 132,12,252
v_add_u32 v243, 4096, v1
v_add_u32 v244, 8192, v1
Mvaa 100,12,248
Ta 4,6,40960
v_add_u32 v245, 12288, v1
Mvaa 100,8,148
Ta 6,6,41984
Mvaa 68,8,144
s_mov_b32 s18, 0x3f803f80
Dc 1,8,81,0
Mvav 68,12,232
s_mov_b32 s19, 0x3f003f00
Mvav 36,12,224
Mvaa 36,8,140
W 8
Ta 8,7,32768
v_accvgpr_write_b32 a176, s18
Mvaa 40,16,140
Ta 10,7,33792
v_accvgpr_write_b32 a180, s19
Mvav 40,20,224
Dc 1,8,81,1024
v_mov_b32 v252, v241
Mvav 72,20,232
Ta 12,7,40960
Ta 14,7,41984
Mvaa 72,16,144
v_accvgpr_write_b32 a177, s18
Mvaa 104,16,148
v_accvgpr_write_b32 a181, s19
Mvaa 104,20,248
v_mov_b32 v253, v241
Mvaa 136,20,252
Dc 1,8,81,2048
Mvaa 136,16,152
W 8
Ta 16,8,32768
v_accvgpr_write_b32 a178, s18
Mvaa 140,24,152
Ta 18,8,33792
v_accvgpr_write_b32 a182, s19
Mvaa 140,28,252
Mvaa 108,28,248
Ta 20,8,40960
v_mov_b32 v254, v241
Mvaa 108,24,148
Dc 1,8,81,3072
Ta 22,8,41984
Mvaa 76,24,144
s_add_u32 m0, s83, 8192
s_add_u32 s81, s82, 8192
v_accvgpr_write_b32 a179, s18
Mvav 76,28,232
v_accvgpr_write_b32 a183, s19
Mvav 44,28,224
v_mov_b32 v255, v241
Mvaa 44,24,140
W 8
Ta 24,9,32768
v_accvgpr_write_b32 a160, 0
Mvaa 48,0,140
Dc 1,8,81,0
Ta 26,9,33792
Mvav 48,4,224
v_accvgpr_write_b32 a161, 0
Mvav 80,4,232
Ta 28,9,40960
v_accvgpr_write_b32 a162, 0
Mvaa 80,0,144
Ta 30,9,41984
v_accvgpr_write_b32 a163, 0
Mvaa 112,0,148
Mvaa 112,4,248
Dc 1,8,81,1024
v_accvgpr_write_b32 a164, 0
Mvaa 144,4,252
v_accvgpr_write_b32 a165, 0
Mvaa 144,0,152
W 8
Ta 0,2,49152
v_accvgpr_write_b32 a166, 0
Mvaa 148,8,152
Ta 2,2,50176
Mvaa 148,12,252
v_accvgpr_write_b32 a167, 0
Mvaa 116,12,248
Dc 1,8,81,2048
v_accvgpr_write_b32 a168, 0
Mvaa 116,8,148
Ta 4,2,57344
Ta 6,2,58368
Mvaa 84,8,144
v_accvgpr_write_b32 a169, 0
Mvav 84,12,232
v_accvgpr_write_b32 a170, 0
Mvav 52,12,224
v_accvgpr_write_b32 a171, 0
Mvaa 52,8,140
W 8
Dc 1,8,81,3072
Ta 8,3,49152
Mvaa 56,16,140
s_add_u32 m0, s83, 12288
s_add_u32 s81, s82, 12288
Ta 10,3,50176
Mvav 56,20,224
v_accvgpr_write_b32 a172, 0
v_accvgpr_write_b32 a173, 0
)"
R"(Mvav 88,20,232
Ta 12,3,57344
v_accvgpr_write_b32 a174, 0
Mvaa 88,16,144
Ta 14,3,58368
Mvaa 120,16,148
Dc 1,8,81,0
v_accvgpr_write_b32 a175, 0
Mvaa 120,20,248
s_mov_b32 s90, 0x5d800000
Mvaa 152,20,252
s_mov_b64 s[88:89], 0
Mvaa 152,16,152
W 8
Ta 16,4,49152
Ta 18,4,50176
Mvaa 156,24,152
s_mov_b32 s64, 0
Mvaa 156,28,252
s_and_b32 s18, s14, 1
Dc 1,8,81,1024
Mvaa 124,28,248
s_lshl_b32 s62, s18, 14
Ta 20,4,57344
Ta 22,4,58368
Mvaa 124,24,148
Mvaa 92,24,144
s_lshl_b32 s20, s57, 15
Mvav 92,28,232
s_sub_u32 s20, 507904, s20
Mvav 60,28,224
Dc 1,8,81,2048
Mvaa 60,24,140
s_waitcnt vmcnt(15) lgkmcnt(8)
s_add_u32 s20, s20, s62
Ta 24,5,49152
Mvav 32,0,192
s_lshl_b32 s65, s57, 1
Ta 26,5,50176
Mvav 32,4,204
s_sub_u32 s65, s65, 1
Mvav 64,4,212
Ta 28,5,57344
Ta 30,5,58368
Mvav 64,0,200
s_max_i32 s65, s65, 0
Dc 1,8,81,3072
v_bfe_u32 v28, v1, 4, 4
Mvav 96,0,188
s_sub_u32 s66, s60, 1
v_lshlrev_b32 v28, 3, v28
Mvav 96,4,220
s_lshl_b32 s80, s56, 9
s_add_u32 s80, s80, 4096
s_cmp_lt_u32 s14, 2
s_cselect_b32 s20, s20, 0
s_cselect_b32 s66, s66, 15
s_cselect_b32 s21, 32768, 0
Mvav 128,4,176
Mvav 128,0,196
W 8
s_add_u32 s61, s91, s20
Ta 0,6,49152
Mvav 132,8,196
s_add_u32 s62, s62, s21
buffer_load_dwordx2 v[28:29], v28, s[8:11], s80 offen sc1
Ta 2,6,50176
Mvav 132,12,176
Mvav 100,12,220
s_sub_u32 s63, 16384, s21
Ta 4,6,57344
Ta 6,6,58368
Mvav 100,8,188
s_lshl_b32 s67, s60, 7
Mvav 68,8,200
s_add_u32 s67, s67, s56
Mvav 68,12,212
Mvav 36,12,204
s_lshl_b32 s67, s67, 12
Mvav 36,8,192
W 8
s_add_u32 s67, s67, 39845888
Ta 8,7,49152
Mvav 40,16,192
s_lshl_b32 s69, s60, 3
Ta 10,7,50176
Mvav 40,20,204
Mvav 72,20,212
s_add_u32 s69, s69, s59
Ta 12,7,57344
Ta 14,7,58368
Mvav 72,16,200
s_lshl_b32 s69, s69, 6
Mvav 104,16,188
Mvav 104,20,220
s_lshl_b32 s21, s58, 3
Mvav 136,20,176
s_add_u32 s69, s69, s21
Mvav 136,16,196
W 8
s_add_u32 s69, s69, 20480
Ta 16,8,49152
Mvav 140,24,196
Ta 18,8,50176
Mvav 140,28,176
s_mov_b32 s70, 0
Mvav 108,28,220
s_mov_b32 s37, 0
Ta 20,8,57344
Ta 22,8,58368
Mvav 108,24,188
s_sub_u32 s18, s57, 4
Mvav 76,24,200
Mvav 76,28,212
s_lshl_b32 s18, s18, 2
Mvav 44,28,204
s_add_u32 s18, s18, s58
Mvav 44,24,192
W 8
Ta 24,9,49152
Ta 26,9,50176
Mvav 48,0,192
s_mul_i32 s85, s59, 7
Mvav 48,4,204
s_add_u32 s85, s85, s18
Mvav 80,4,212
s_cmp_lt_u32 s18, 7
s_cselect_b32 s85, s85, 99
Ta 28,9,57344
Ta 30,9,58368
Mvav 80,0,200
Mvav 112,0,188
s_cmp_lt_u32 s85, 54
s_cselect_b32 s65, 2, s65
Mvav 112,4,220
s_mov_b32 s68, 0
Mvav 144,4,176
s_mov_b64 s[24:25], -1
Mvav 144,0,196
W 8
Mvav 148,8,196
s_mov_b32 s22, 0x3e0293ee
Mvav 148,12,176
s_mov_b32 s23, 0x3e0293ee
Mvav 116,12,220
Mvav 116,8,188
Mvav 84,8,200
Mvav 84,12,212
Mvav 52,12,204
Mvav 52,8,192
W 4
Mvav 56,16,192
Mvav 56,20,204
s_waitcnt vmcnt(0)
v_cmp_eq_u64 vcc, s[12:13], v[28:29]
s_cmp_eq_u64 vcc, -1
s_cbranch_scc0 .Lspin_64%=
.Lok_65%=:
Mvav 88,20,212
Mvav 88,16,200
Mvav 120,16,188
W 0
s_barrier
Mvav 120,20,220
Ra 128,242,0
Mvav 152,20,176
Ra 132,242,1024
Mvav 152,16,196
Mvav 156,24,196
Ra 136,242,2048
Mvav 156,28,176
Mvav 124,28,220
Ra 140,242,3072
Mvav 124,24,188
Mvav 92,24,200
Mvav 92,28,212
Mvav 60,28,204
Mvav 60,24,192
.Lbgo%=:
v_pk_mul_f32 v[32:33], v[32:33], s[22:23]
v_pk_mul_f32 v[34:35], v[34:35], s[22:23]
v_pk_mul_f32 v[36:37], v[36:37], s[22:23]
v_pk_mul_f32 v[38:39], v[38:39], s[22:23]
v_pk_mul_f32 v[64:65], v[64:65], s[22:23]
v_pk_mul_f32 v[66:67], v[66:67], s[22:23]
v_pk_mul_f32 v[68:69], v[68:69], s[22:23]
v_pk_mul_f32 v[70:71], v[70:71], s[22:23]
v_pk_mul_f32 v[96:97], v[96:97], s[22:23]
v_pk_mul_f32 v[98:99], v[98:99], s[22:23]
v_pk_mul_f32 v[100:101], v[100:101], s[22:23]
v_pk_mul_f32 v[102:103], v[102:103], s[22:23]
v_pk_mul_f32 v[128:129], v[128:129], s[22:23]
v_pk_mul_f32 v[130:131], v[130:131], s[22:23]
v_pk_mul_f32 v[132:133], v[132:133], s[22:23]
v_pk_mul_f32 v[134:135], v[134:135], s[22:23]
v_pk_mul_f32 v[40:41], v[40:41], s[22:23]
v_pk_mul_f32 v[42:43], v[42:43], s[22:23]
v_pk_mul_f32 v[44:45], v[44:45], s[22:23]
v_pk_mul_f32 v[46:47], v[46:47], s[22:23]
v_pk_mul_f32 v[72:73], v[72:73], s[22:23]
v_pk_mul_f32 v[74:75], v[74:75], s[22:23]
v_pk_mul_f32 v[76:77], v[76:77], s[22:23]
v_pk_mul_f32 v[78:79], v[78:79], s[22:23]
v_pk_mul_f32 v[104:105], v[104:105], s[22:23]
v_pk_mul_f32 v[106:107], v[106:107], s[22:23]
v_pk_mul_f32 v[108:109], v[108:109], s[22:23]
v_pk_mul_f32 v[110:111], v[110:111], s[22:23]
v_pk_mul_f32 v[136:137], v[136:137], s[22:23]
v_pk_mul_f32 v[138:139], v[138:139], s[22:23]
v_pk_mul_f32 v[140:141], v[140:141], s[22:23]
v_pk_mul_f32 v[142:143], v[142:143], s[22:23]
v_pk_mul_f32 v[48:49], v[48:49], s[22:23]
v_pk_mul_f32 v[50:51], v[50:51], s[22:23]
v_pk_mul_f32 v[52:53], v[52:53], s[22:23]
v_pk_mul_f32 v[54:55], v[54:55], s[22:23]
v_pk_mul_f32 v[80:81], v[80:81], s[22:23]
v_pk_mul_f32 v[82:83], v[82:83], s[22:23]
v_pk_mul_f32 v[84:85], v[84:85], s[22:23]
v_pk_mul_f32 v[86:87], v[86:87], s[22:23]
v_pk_mul_f32 v[112:113], v[112:113], s[22:23]
v_pk_mul_f32 v[114:115], v[114:115], s[22:23]
v_pk_mul_f32 v[116:117], v[116:117], s[22:23]
v_pk_mul_f32 v[118:119], v[118:119], s[22:23]
v_pk_mul_f32 v[144:145], v[144:145], s[22:23]
v_pk_mul_f32 v[146:147], v[146:147], s[22:23]
v_pk_mul_f32 v[148:149], v[148:149], s[22:23]
v_pk_mul_f32 v[150:151], v[150:151], s[22:23]
v_pk_mul_f32 v[56:57], v[56:57], s[22:23]
v_pk_mul_f32 v[58:59], v[58:59], s[22:23]
v_pk_mul_f32 v[60:61], v[60:61], s[22:23]
v_pk_mul_f32 v[62:63], v[62:63], s[22:23]
v_pk_mul_f32 v[88:89], v[88:89], s[22:23]
v_pk_mul_f32 v[90:91], v[90:91], s[22:23]
v_pk_mul_f32 v[92:93], v[92:93], s[22:23]
v_pk_mul_f32 v[94:95], v[94:95], s[22:23]
v_pk_mul_f32 v[120:121], v[120:121], s[22:23]
v_pk_mul_f32 v[122:123], v[122:123], s[22:23]
v_pk_mul_f32 v[124:125], v[124:125], s[22:23]
v_pk_mul_f32 v[126:127], v[126:127], s[22:23]
v_pk_mul_f32 v[152:153], v[152:153], s[22:23]
v_pk_mul_f32 v[154:155], v[154:155], s[22:23]
v_pk_mul_f32 v[156:157], v[156:157], s[22:23]
v_pk_mul_f32 v[158:159], v[158:159], s[22:23]
K 32,32,33
K 33,34,35
K 34,36,37
K 35,38,39
K 40,40,41
K 41,42,43
K 42,44,45
K 43,46,47
W 0
Zvav 196,128,32
s_add_u32 m0, s62, 0
Ra 144,242,4096
Ra 148,242,5120
Ra 152,242,6144
Ra 156,242,7168
Mvav 196,132,40
K 64,64,65
K 65,66,67
K 66,68,69
K 67,70,71
K 72,72,73
K 73,74,75
K 74,76,77
K 75,78,79
s_nop 1
Zvav 204,132,72
Dc 1,8,61,0
K 96,96,97
K 97,98,99
K 98,100,101
Mvav 204,128,64
K 99,102,103
K 104,104,105
K 105,106,107
K 106,108,109
K 107,110,111
Zvav 212,128,96
Dc 1,8,61,1024
K 128,128,129
K 129,130,131
K 130,132,133
Mvav 212,132,104
K 131,134,135
K 136,136,137
K 137,138,139
K 138,140,141
K 139,142,143
s_nop 1
Zvav 220,132,136
Dc 1,8,61,2048
K 48,48,49
K 49,50,51
K 50,52,53
Mvav 220,128,128
K 51,54,55
K 56,56,57
K 57,58,59
K 58,60,61
K 59,62,63
Mvav 196,136,48
Ra 128,242,8192
Ra 132,242,9216
K 80,80,81
K 81,82,83
Mvav 196,140,56
K 82,84,85
K 83,86,87
K 88,88,89
K 89,90,91
K 90,92,93
K 91,94,95
s_nop 1
Mvav 204,140,88
Dc 1,8,61,3072
s_add_u32 m0, s62, 4096
K 112,112,113
K 113,114,115
K 114,116,117
Mvav 204,136,80
K 115,118,119
K 120,120,121
K 121,122,123
K 122,124,125
K 123,126,127
Mvav 212,136,112
Dc 243,8,61,0
K 144,144,145
K 145,146,147
K 146,148,149
Mvav 212,140,120
K 147,150,151
K 152,152,153
K 153,154,155
K 154,156,157
K 155,158,159
s_nop 1
Mvav 220,140,152
s_add_u32 s80, s62, 131072
Dc 243,8,61,1024
v_add_u32 v12, s80, v1
Mvav 220,136,144
W 2
ds_write_b128 v12, v[32:35] offset:0
Ra 136,242,10240
Zvav 36,144,32
Ra 0,12,0
Ra 140,242,11264
Dc 243,8,61,2048
Mvav 36,148,40
v_max3_f32 v4, v196, v197, v198
v_max3_f32 v4, v4, v199, v252
v_max3_f32 v5, v204, v205, v206
v_max3_f32 v5, v5, v207, v253
Zvav 68,148,72
v_max3_f32 v6, v212, v213, v214
v_max3_f32 v6, v6, v215, v254
v_max3_f32 v7, v220, v221, v222
v_max3_f32 v7, v7, v223, v255
Mvav 68,144,64
v_mov_b32 v8, v4
v_mov_b32 v9, v5
v_mov_b32 v10, v6
v_mov_b32 v11, v7
Zvav 100,144,96
ds_write_b128 v12, v[40:43] offset:1024
v_permlane32_swap_b32 v4, v8
v_permlane32_swap_b32 v5, v9
v_permlane32_swap_b32 v6, v10
Mvav 100,148,104
v_permlane32_swap_b32 v7, v11
v_max_f32 v4, v4, v8
v_max_f32 v5, v5, v9
v_max_f32 v6, v6, v10
Zvav 132,148,136
v_max_f32 v7, v7, v11
v_mov_b32 v8, v4
v_mov_b32 v9, v5
v_mov_b32 v10, v6
Mvav 132,144,128
Ra 4,12,1024
v_mov_b32 v11, v7
v_permlane16_swap_b32 v4, v8
v_permlane16_swap_b32 v5, v9
Mvav 36,152,48
v_permlane16_swap_b32 v6, v10
v_permlane16_swap_b32 v7, v11
v_max_f32 v4, v4, v8
v_max_f32 v5, v5, v9
Mvav 36,156,56
v_max_f32 v6, v6, v10
v_max_f32 v7, v7, v11
v_sub_f32 v4, 0, v4
v_sub_f32 v5, 0, v5
Mvav 68,156,88
v_sub_f32 v6, 0, v6
v_sub_f32 v7, 0, v7
v_mov_b32 v224, v4
)"
R"(v_mov_b32 v225, v4
Mvav 68,152,80
v_mov_b32 v226, v4
v_mov_b32 v227, v4
v_mov_b32 v228, v5
v_mov_b32 v229, v5
Mvav 100,152,112
v_mov_b32 v230, v5
v_mov_b32 v231, v5
v_mov_b32 v232, v6
v_mov_b32 v233, v6
Mvav 100,156,120
v_mov_b32 v234, v6
v_mov_b32 v235, v6
v_mov_b32 v236, v7
v_mov_b32 v237, v7
Mvav 132,156,152
Dc 243,8,61,3072
s_add_u32 m0, s62, 8192
ds_write_b128 v12, v[48:51] offset:2048
v_mov_b32 v238, v7
v_mov_b32 v239, v7
Mvav 132,152,144
W 3
Ra 8,12,2048
ds_write_b128 v12, v[56:59] offset:3072
Ra 144,242,12288
Ra 12,12,3072
Cvav 160,128,32,224
s_and_b32 s80, s14, 1
Dc 244,8,61,0
Ra 148,242,13312
ds_write_b128 v12, v[64:67] offset:4096
Ra 16,12,4096
Mvav 160,132,40
s_lshl_b32 s80, s80, 14
ds_write_b128 v12, v[72:75] offset:5120
Ra 20,12,5120
ds_write_b128 v12, v[80:83] offset:6144
A 196,196,224
Cvav 164,132,72,228
s_add_u32 s80, s80, 98304
Dc 244,8,61,1024
Ra 24,12,6144
A 197,197,224
A 198,198,224
Mvav 164,128,64
Ra 152,242,14336
A 199,199,224
X 196
Cvav 168,128,96,232
Dc 244,8,61,2048
Ra 156,242,15360
X 197
Mvav 168,132,104
ds_write_b128 v12, v[88:91] offset:7168
Ra 28,12,7168
X 198
Cvav 172,132,136,236
Dc 244,8,61,3072
s_add_u32 m0, s62, 12288
K 192,196,197
X 199
Mvav 172,128,128
ds_write_b128 v12, v[96:99] offset:8192
A 204,204,228
v_add_u32 v8, s80, v1
K 193,198,199
Mvav 160,136,48
Dc 245,8,61,0
Ra 32,12,8192
A 205,205,228
A 206,206,228
Mvav 160,140,56
ds_write_b128 v8, v[4:7]
A 207,207,228
X 204
Mvav 164,140,88
Dc 245,8,61,1024
ds_write_b128 v12, v[104:107] offset:9216
X 205
Mvav 164,136,80
Ra 36,12,9216
X 206
K 200,204,205
Mvav 168,136,112
Dc 245,8,61,2048
Ra 128,242,24576
X 207
Mvav 168,140,120
ds_write_b128 v12, v[112:115] offset:10240
Ra 132,242,25600
A 212,212,232
K 201,206,207
Mvav 172,140,152
Dc 245,8,61,3072
s_add_u32 s61, s61, s63
Ra 40,12,10240
A 213,213,232
A 214,214,232
Mvav 172,136,144
W 11
ds_write_b128 v12, v[120:123] offset:11264
A 215,215,232
X 212
Cvav 176,144,32,224
X 213
X 214
Mvav 176,148,40
Ra 44,12,11264
K 208,212,213
X 215
Cvav 180,148,72,228
ds_write_b128 v12, v[128:131] offset:12288
A 220,220,236
K 209,214,215
A 221,221,236
Mvav 180,144,64
Ra 48,12,12288
Ra 136,242,26624
A 222,222,236
A 223,223,236
Cvav 184,144,96,232
ds_write_b128 v12, v[136:139] offset:13312
Ra 140,242,27648
X 220
Mvav 184,148,104
X 221
X 222
Cvav 188,148,136,236
Ra 52,12,13312
K 216,220,221
X 223
Mvav 188,144,128
ds_write_b128 v12, v[144:147] offset:14336
Ra 56,12,14336
A 36,36,224
K 217,222,223
Mvav 176,152,48
ds_write_b128 v12, v[152:155] offset:15360
A 37,37,224
A 38,38,224
A 39,39,224
Mvav 176,156,56
X 36
X 37
Mvav 180,156,88
Ra 60,12,15360
X 38
K 194,36,37
Mvav 180,152,80
Ra 144,242,28672
X 39
A 68,68,228
Mvav 184,152,112
Ra 148,242,29696
K 195,38,39
A 69,69,228
A 70,70,228
Mvav 184,156,120
W 3
s_barrier
A 71,71,228
X 68
Mvav 188,156,152
X 69
X 70
K 202,68,69
X 71
A 100,100,232
K 203,70,71
A 101,101,232
A 102,102,232
A 103,103,232
X 100
X 101
X 102
K 210,100,101
X 103
A 132,132,236
K 211,102,103
A 133,133,236
A 134,134,236
A 135,135,236
X 132
X 133
X 134
K 218,132,133
X 135
s_nop 0
K 219,134,135
Mvav 188,152,144
Ra 152,242,30720
Zvav 32,128,192
Ra 156,242,31744
Zvav 64,128,200
v_xor_b32 v242, 0x10000, v242
Zvav 96,128,208
s_xor_b32 s62, s62, 0x10000
Zvav 128,128,216
v_add_u32 v23, 61440, v21
Zvav 132,132,216
v_add_u32 v24, 61440, v22
Zvav 100,132,208
v_add_u32 v25, 122880, v21
Zvav 68,132,200
v_add_u32 v26, 122880, v22
Zvav 36,132,192
Zvav 40,136,192
X 172
Zvav 72,136,200
X 173
Zvav 104,136,208
X 174
K 220,172,173
Zvav 136,136,216
X 175
Zvav 140,140,216
X 168
K 221,174,175
Zvav 108,140,208
X 169
X 170
Zvav 76,140,200
s_waitcnt vmcnt(0) lgkmcnt(0)
s_barrier
K 212,168,169
X 171
Zvav 44,140,192
Ra 128,242,0
X 164
Zvav 48,144,192
K 213,170,171
X 165
Zvav 80,144,200
X 166
Zvav 112,144,208
Ra 132,242,1024
K 204,164,165
X 167
Zvav 144,144,216
X 160
Zvav 148,148,216
K 205,166,167
X 161
Zvav 116,148,208
Ra 136,242,2048
X 162
Zvav 84,148,200
K 196,160,161
X 163
Zvav 52,148,192
X 176
K 197,162,163
Zvav 56,152,192
Ra 140,242,3072
X 177
Zvav 88,152,200
X 178
K 198,176,177
Zvav 120,152,208
X 179
X 180
Zvav 152,152,216
K 199,178,179
Zvav 156,156,216
X 181
X 182
Zvav 124,156,208
K 206,180,181
X 183
Zvav 92,156,200
K 207,182,183
Zvav 60,156,192
s_cmp_eq_u64 s[24:25], 0
s_cbranch_scc1 .Ll1_34%=
W 0
Cvaa 160,128,0,224
Ra 144,242,4096
Ra 148,242,5120
Mvaa 160,132,4
s_add_u32 m0, s62, 0
Ra 152,242,6144
Cvaa 164,132,20,228
X 184
Mvaa 164,128,16
X 185
Cvaa 168,128,32,232
X 186
Mvaa 168,132,36
Ra 156,242,7168
K 214,184,185
Cvaa 172,132,52,236
Dc 1,8,61,0
Mvaa 172,128,48
X 187
Mvaa 160,136,8
X 188
Mvaa 160,140,12
Ra 128,242,16384
K 215,186,187
Mvaa 164,140,28
Dc 1,8,61,1024
Mvaa 164,136,24
X 189
Mvaa 168,136,40
X 190
Mvaa 168,140,44
Ra 132,242,17408
K 222,188,189
Mvaa 172,140,60
Dc 1,8,61,2048
Mvaa 172,136,56
X 191
v_smfmac_f32_16x16x64_bf16 a[160:163], a[176:179], v[192:199], v240
Ra 136,242,18432
K 223,190,191
v_smfmac_f32_16x16x64_bf16 a[164:167], a[176:179], v[200:207], v240
X 160
v_smfmac_f32_16x16x64_bf16 a[168:171], a[176:179], v[208:215], v240
Dc 1,8,61,3072
v_smfmac_f32_16x16x64_bf16 a[172:175], a[176:179], v[216:223], v240
W 3
X 161
Cvaa 176,144,0,224
s_add_u32 m0, s62, 4096
X 162
Mvaa 176,148,4
Ra 140,242,19456
K 192,160,161
Cvaa 180,148,20,228
X 163
Mvaa 180,144,16
Dc 243,8,61,0
Cvaa 184,144,32,232
X 164
Mvaa 184,148,36
K 193,162,163
Cvaa 188,148,52,236
X 165
Mvaa 188,144,48
Dc 243,8,61,1024
Ra 144,242,20480
Mvaa 176,152,8
X 166
Mvaa 176,156,12
Ra 148,242,21504
K 200,164,165
Mvaa 180,156,28
X 167
Mvaa 180,152,24
Dc 243,8,61,2048
Mvaa 184,152,40
X 168
Mvaa 184,156,44
K 201,166,167
Mvaa 188,156,60
X 169
Mvaa 188,152,56
W 2
Dc 243,8,61,3072
Ra 152,242,22528
Mvav 32,128,196
s_add_u32 m0, s62, 8192
X 170
Mvav 64,128,204
Ra 156,242,23552
K 208,168,169
Mvav 96,128,212
X 171
Mvav 128,128,220
Dc 244,8,61,0
Mvav 132,132,220
X 172
Mvav 100,132,212
Ra 128,242,8192
K 209,170,171
Mvav 68,132,204
X 173
Mvav 36,132,196
Dc 244,8,61,1024
Mvav 40,136,196
X 174
Mvav 72,136,204
Ra 132,242,9216
K 216,172,173
Mvav 104,136,212
X 175
Mvav 136,136,220
Dc 244,8,61,2048
Mvav 140,140,220
X 176
Mvav 108,140,212
Ra 136,242,10240
K 217,174,175
Mvav 76,140,204
X 177
Mvav 44,140,196
W 3
Dc 244,8,61,3072
)"
R"(Mvav 48,144,196
X 178
Mvav 80,144,204
s_add_u32 m0, s62, 12288
Ra 140,242,11264
Mvav 112,144,212
K 194,176,177
Mvav 144,144,220
X 179
Mvav 148,148,220
X 180
Mvav 116,148,212
Dc 245,8,61,0
Ra 144,242,12288
Mvav 84,148,204
K 195,178,179
Mvav 52,148,196
X 181
Mvav 56,152,196
X 182
Mvav 88,152,204
s_cmp_eq_u32 s14, 2
s_cselect_b64 s[18:19], s[30:31], s[32:33]
Dc 245,8,61,1024
Ra 148,242,13312
Mvav 120,152,212
s_mul_i32 s81, s58, 6
K 202,180,181
Mvav 152,152,220
s_add_u32 s81, s81, s57
X 183
Mvav 156,156,220
s_sub_u32 s81, s81, 2
X 184
Mvav 124,156,212
s_cmp_ge_u32 s57, 2
s_cselect_b32 s23, 2211836, 0
Dc 245,8,61,2048
Ra 152,242,14336
Mvav 92,156,204
s_mul_i32 s82, s81, 48384
K 203,182,183
Mvav 60,156,196
W 3
X 185
Cvaa 160,128,0,224
s_mul_i32 s83, s59, 448
X 186
Mvaa 160,132,4
s_andn2_b32 s83, s83, 127
Dc 245,8,61,3072
Ra 156,242,15360
Cvaa 164,132,20,228
s_add_u32 s82, s82, s83
s_add_u32 s61, s61, s63
K 210,184,185
Mvaa 164,128,16
v_min_u32 v246, 880, v1
v_lshrrev_b32 v247, 6, v246
Cvaa 168,128,32,232
X 187
Mvaa 168,132,36
X 188
Cvaa 172,132,52,236
s_xor_b32 s62, s62, 0x10000
v_mul_u32_u24 v247, 3456, v247
v_and_b32 v246, 48, v246
Mvaa 172,128,48
K 211,186,187
v_lshl_add_u32 v246, v246, 3, v247
Mvaa 160,136,8
X 189
Mvaa 160,140,12
s_add_u32 s64, s64, 1
Ra 128,242,24576
v_add_u32 v246, s82, v246
Mvaa 164,140,28
X 190
Mvaa 164,136,24
Ra 132,242,25600
v_min_u32 v246, s23, v246
Mvaa 168,136,40
global_load_dword v3, v246, s[18:19]
K 218,188,189
Mvaa 168,140,44
X 191
Mvaa 172,140,60
X 160
Mvaa 172,136,56
W 2
Ra 136,242,26624
K 219,190,191
Cvaa 176,144,0,224
X 161
Mvaa 176,148,4
Ra 140,242,27648
Cvaa 180,148,20,228
X 162
Mvaa 180,144,16
K 196,160,161
Cvaa 184,144,32,232
X 163
Mvaa 184,148,36
X 164
Cvaa 188,148,52,236
K 197,162,163
Mvaa 188,144,48
X 165
Mvaa 176,152,8
Ra 144,242,28672
Ra 148,242,29696
Mvaa 176,156,12
X 166
Mvaa 180,156,28
K 204,164,165
Mvaa 180,152,24
X 167
Mvaa 184,152,40
Mvaa 184,156,44
X 168
Mvaa 188,156,60
Mvaa 188,152,56
W 2
Ra 152,242,30720
K 205,166,167
Mvav 32,128,192
X 169
Mvav 64,128,200
Ra 156,242,31744
v_xor_b32 v242, 0x10000, v242
Mvav 96,128,208
X 170
Mvav 128,128,216
K 212,168,169
Mvav 132,132,216
X 171
Mvav 100,132,208
X 172
Mvav 68,132,200
Mvav 36,132,192
K 213,170,171
Mvav 40,136,192
X 173
Mvav 72,136,200
X 174
Mvav 104,136,208
Mvav 136,136,216
K 220,172,173
Mvav 140,140,216
X 175
Mvav 108,140,208
X 176
Mvav 76,140,200
s_waitcnt vmcnt(1) lgkmcnt(0)
s_barrier
Mvav 44,140,192
Ra 128,242,0
K 221,174,175
Mvav 48,144,192
X 177
Mvav 80,144,200
X 178
Mvav 112,144,208
Ra 132,242,1024
Mvav 144,144,216
K 198,176,177
Mvav 148,148,216
X 179
Mvav 116,148,208
X 180
Mvav 84,148,200
Ra 136,242,2048
Mvav 52,148,192
K 199,178,179
Mvav 56,152,192
X 181
Mvav 88,152,200
Ra 140,242,3072
Mvav 120,152,208
X 182
Mvav 152,152,216
K 206,180,181
Mvav 156,156,216
X 183
Mvav 124,156,208
Mvav 92,156,200
K 207,182,183
Mvav 60,156,192
W 0
Cvaa 160,128,0,224
Ra 144,242,4096
Ra 148,242,5120
Mvaa 160,132,4
s_add_u32 m0, s62, 0
Ra 152,242,6144
Cvaa 164,132,20,228
X 184
Mvaa 164,128,16
X 185
Cvaa 168,128,32,232
X 186
Mvaa 168,132,36
Ra 156,242,7168
K 214,184,185
Cvaa 172,132,52,236
Dc 1,8,61,0
Mvaa 172,128,48
X 187
Mvaa 160,136,8
X 188
Mvaa 160,140,12
Ra 128,242,16384
K 215,186,187
Mvaa 164,140,28
Dc 1,8,61,1024
Mvaa 164,136,24
X 189
Mvaa 168,136,40
X 190
Mvaa 168,140,44
Ra 132,242,17408
K 222,188,189
Mvaa 172,140,60
Dc 1,8,61,2048
Mvaa 172,136,56
X 191
v_smfmac_f32_16x16x64_bf16 a[160:163], a[176:179], v[192:199], v240
Ra 136,242,18432
K 223,190,191
v_smfmac_f32_16x16x64_bf16 a[164:167], a[176:179], v[200:207], v240
X 160
v_smfmac_f32_16x16x64_bf16 a[168:171], a[176:179], v[208:215], v240
Dc 1,8,61,3072
v_smfmac_f32_16x16x64_bf16 a[172:175], a[176:179], v[216:223], v240
W 3
X 161
Cvaa 176,144,0,224
s_add_u32 m0, s62, 4096
X 162
Mvaa 176,148,4
Ra 140,242,19456
K 192,160,161
Cvaa 180,148,20,228
X 163
Mvaa 180,144,16
Dc 243,8,61,0
Cvaa 184,144,32,232
X 164
Mvaa 184,148,36
K 193,162,163
Cvaa 188,148,52,236
X 165
Mvaa 188,144,48
Dc 243,8,61,1024
Ra 144,242,20480
Mvaa 176,152,8
X 166
Mvaa 176,156,12
Ra 148,242,21504
K 200,164,165
Mvaa 180,156,28
X 167
Mvaa 180,152,24
Dc 243,8,61,2048
Mvaa 184,152,40
X 168
Mvaa 184,156,44
K 201,166,167
Mvaa 188,156,60
X 169
Mvaa 188,152,56
W 2
Dc 243,8,61,3072
Ra 152,242,22528
Mvav 32,128,196
s_add_u32 m0, s62, 8192
X 170
Mvav 64,128,204
Ra 156,242,23552
K 208,168,169
Mvav 96,128,212
X 171
Mvav 128,128,220
Dc 244,8,61,0
Mvav 132,132,220
X 172
Mvav 100,132,212
Ra 128,242,8192
K 209,170,171
Mvav 68,132,204
X 173
Mvav 36,132,196
Dc 244,8,61,1024
Mvav 40,136,196
X 174
Mvav 72,136,204
Ra 132,242,9216
K 216,172,173
Mvav 104,136,212
X 175
Mvav 136,136,220
Dc 244,8,61,2048
Mvav 140,140,220
X 176
Mvav 108,140,212
Ra 136,242,10240
K 217,174,175
Mvav 76,140,204
X 177
Mvav 44,140,196
W 3
Dc 244,8,61,3072
Mvav 48,144,196
X 178
Mvav 80,144,204
s_add_u32 m0, s62, 12288
Ra 140,242,11264
Mvav 112,144,212
K 194,176,177
Mvav 144,144,220
X 179
Mvav 148,148,220
X 180
Mvav 116,148,212
Dc 245,8,61,0
Ra 144,242,12288
Mvav 84,148,204
K 195,178,179
Mvav 52,148,196
X 181
Mvav 56,152,196
X 182
Mvav 88,152,204
Dc 245,8,61,1024
Ra 148,242,13312
Mvav 120,152,212
v_add_u32 v246, 1161216, v246
v_min_u32 v246, s23, v246
Mvav 152,152,220
K 202,180,181
Mvav 156,156,220
X 183
Mvav 124,156,212
Dc 245,8,61,2048
Ra 152,242,14336
Mvav 92,156,204
X 184
Mvav 60,156,196
W 3
K 203,182,183
v_bfe_u32 v28, v1, 4, 4
Cvaa 160,128,0,224
X 185
Mvaa 160,132,4
Dc 245,8,61,3072
v_lshlrev_b32 v28, 3, v28
Cvaa 164,132,20,228
s_lshl_b32 s80, s56, 9
X 186
Mvaa 164,128,16
s_add_u32 s80, s80, 4224
Ra 156,242,15360
K 210,184,185
Cvaa 168,128,32,232
s_add_u32 s61, s61, s63
X 187
Mvaa 168,132,36
buffer_load_dwordx2 v[28:29], v28, s[8:11], s80 offen sc1
Cvaa 172,132,52,236
s_xor_b32 s62, s62, 0x10000
X 188
Mvaa 172,128,48
Ra 128,242,24576
K 211,186,187
Mvaa 160,136,8
X 189
Mvaa 160,140,12
s_add_u32 s64, s64, 1
global_load_dword v3, v246, s[18:19]
Ra 132,242,25600
Mvaa 164,140,28
s_mov_b64 s[18:19], s[34:35]
s_mul_i32 s81, s58, 6
s_add_u32 s81, s81, s57
Mvaa 164,136,24
s_sub_u32 s81, s81, 2
s_cmp_ge_u32 s57, 2
s_cselect_b32 s23, 2211836, 0
s_lshl_b32 s81, s81, 1
Mvaa 168,136,40
s_add_u32 s81, s81, s14
s_sub_u32 s81, s81, 2
s_mul_i32 s81, s81, 6016
Mvaa 168,140,44
s_mul_i32 s82, s59, 286720
s_add_u32 s81, s81, s82
v_min_u32 v246, 736, v1
Mvaa 172,140,60
v_lshl_add_u32 v246, v246, 3, s81
v_min_u32 v246, s23, v246
)"
R"(Mvaa 172,136,56
W 2
global_load_dword v3, v246, s[18:19]
Ra 136,242,26624
Cvaa 176,144,0,224
X 190
Mvaa 176,148,4
Ra 140,242,27648
K 218,188,189
Cvaa 180,148,20,228
X 191
Mvaa 180,144,16
X 160
Cvaa 184,144,32,232
K 219,190,191
Mvaa 184,148,36
X 161
Cvaa 188,148,52,236
X 162
Mvaa 188,144,48
Ra 144,242,28672
K 196,160,161
Mvaa 176,152,8
X 163
Mvaa 176,156,12
X 164
Mvaa 180,156,28
Ra 148,242,29696
K 197,162,163
Mvaa 180,152,24
X 165
Mvaa 184,152,40
X 166
Mvaa 184,156,44
K 204,164,165
Mvaa 188,156,60
X 167
Mvaa 188,152,56
W 2
X 168
Mvav 32,128,192
Ra 152,242,30720
K 205,166,167
Mvav 64,128,200
X 169
Mvav 96,128,208
Ra 156,242,31744
v_xor_b32 v242, 0x10000, v242
Mvav 128,128,216
X 170
Mvav 132,132,216
K 212,168,169
Mvav 100,132,208
X 171
Mvav 68,132,200
X 172
Mvav 36,132,192
K 213,170,171
Mvav 40,136,192
X 173
Mvav 72,136,200
X 174
Mvav 104,136,208
Mvav 136,136,216
s_waitcnt vmcnt(2)
v_cmp_eq_u64 vcc, s[12:13], v[28:29]
s_cmp_eq_u64 vcc, -1
s_cbranch_scc0 .Lspin_67%=
.Lok_68%=:
K 220,172,173
Mvav 140,140,216
X 175
Mvav 108,140,208
X 176
Mvav 76,140,200
W 0
s_barrier
Mvav 44,140,192
Ra 128,242,0
K 221,174,175
Mvav 48,144,192
X 177
Mvav 80,144,200
X 178
Mvav 112,144,208
Ra 132,242,1024
Mvav 144,144,216
K 198,176,177
Mvav 148,148,216
X 179
Mvav 116,148,208
X 180
Mvav 84,148,200
Ra 136,242,2048
Mvav 52,148,192
K 199,178,179
Mvav 56,152,192
X 181
Mvav 88,152,200
Ra 140,242,3072
Mvav 120,152,208
X 182
Mvav 152,152,216
K 206,180,181
Mvav 156,156,216
X 183
Mvav 124,156,208
Mvav 92,156,200
K 207,182,183
Mvav 60,156,192
W 0
Cvaa 160,128,0,224
Ra 144,242,4096
Ra 148,242,5120
Mvaa 160,132,4
s_add_u32 m0, s62, 0
Ra 152,242,6144
Cvaa 164,132,20,228
X 184
Mvaa 164,128,16
X 185
Cvaa 168,128,32,232
X 186
Mvaa 168,132,36
Ra 156,242,7168
K 214,184,185
Cvaa 172,132,52,236
Dc 1,8,61,0
Mvaa 172,128,48
X 187
Mvaa 160,136,8
X 188
Mvaa 160,140,12
Ra 128,242,16384
K 215,186,187
Mvaa 164,140,28
Dc 1,8,61,1024
Mvaa 164,136,24
X 189
Mvaa 168,136,40
X 190
Mvaa 168,140,44
Ra 132,242,17408
K 222,188,189
Mvaa 172,140,60
Dc 1,8,61,2048
Mvaa 172,136,56
X 191
v_smfmac_f32_16x16x64_bf16 a[160:163], a[176:179], v[192:199], v240
Ra 136,242,18432
K 223,190,191
v_smfmac_f32_16x16x64_bf16 a[164:167], a[176:179], v[200:207], v240
X 160
v_smfmac_f32_16x16x64_bf16 a[168:171], a[176:179], v[208:215], v240
Dc 1,8,61,3072
v_smfmac_f32_16x16x64_bf16 a[172:175], a[176:179], v[216:223], v240
W 3
X 161
Cvaa 176,144,0,224
s_add_u32 m0, s62, 4096
X 162
Mvaa 176,148,4
Ra 140,242,19456
K 192,160,161
Cvaa 180,148,20,228
X 163
Mvaa 180,144,16
Dc 243,8,61,0
Cvaa 184,144,32,232
X 164
Mvaa 184,148,36
K 193,162,163
Cvaa 188,148,52,236
X 165
Mvaa 188,144,48
Dc 243,8,61,1024
Ra 144,242,20480
Mvaa 176,152,8
X 166
Mvaa 176,156,12
Ra 148,242,21504
K 200,164,165
Mvaa 180,156,28
X 167
Mvaa 180,152,24
Dc 243,8,61,2048
Mvaa 184,152,40
X 168
Mvaa 184,156,44
K 201,166,167
Mvaa 188,156,60
X 169
Mvaa 188,152,56
W 2
Dc 243,8,61,3072
Ra 152,242,22528
Mvav 32,128,196
s_add_u32 m0, s62, 8192
X 170
Mvav 64,128,204
Ra 156,242,23552
K 208,168,169
Mvav 96,128,212
X 171
Mvav 128,128,220
Dc 244,8,61,0
Mvav 132,132,220
X 172
Mvav 100,132,212
Ra 128,242,8192
K 209,170,171
Mvav 68,132,204
X 173
Mvav 36,132,196
Dc 244,8,61,1024
Mvav 40,136,196
X 174
Mvav 72,136,204
Ra 132,242,9216
K 216,172,173
Mvav 104,136,212
X 175
Mvav 136,136,220
Dc 244,8,61,2048
Mvav 140,140,220
X 176
Mvav 108,140,212
Ra 136,242,10240
K 217,174,175
Mvav 76,140,204
X 177
Mvav 44,140,196
W 3
Dc 244,8,61,3072
Mvav 48,144,196
X 178
Mvav 80,144,204
s_add_u32 m0, s62, 12288
Ra 140,242,11264
Mvav 112,144,212
K 194,176,177
Mvav 144,144,220
X 179
Mvav 148,148,220
X 180
Mvav 116,148,212
Dc 245,8,61,0
Ra 144,242,12288
Mvav 84,148,204
K 195,178,179
Mvav 52,148,196
X 181
Mvav 56,152,196
X 182
Mvav 88,152,204
Dc 245,8,61,1024
Ra 148,242,13312
Mvav 120,152,212
K 202,180,181
Mvav 152,152,220
X 183
Mvav 156,156,220
X 184
Mvav 124,156,212
Dc 245,8,61,2048
Ra 152,242,14336
Mvav 92,156,204
K 203,182,183
Mvav 60,156,196
W 3
X 185
Cvaa 160,128,0,224
X 186
Mvaa 160,132,4
Dc 245,8,61,3072
Ra 156,242,15360
Cvaa 164,132,20,228
s_add_u32 s61, s61, s63
K 210,184,185
Mvaa 164,128,16
X 187
Cvaa 168,128,32,232
X 188
Mvaa 168,132,36
Cvaa 172,132,52,236
s_xor_b32 s62, s62, 0x10000
K 211,186,187
Mvaa 172,128,48
X 189
Mvaa 160,136,8
Ra 128,242,24576
Ra 132,242,25600
Mvaa 160,140,12
s_add_u32 s64, s64, 1
X 190
Mvaa 164,140,28
K 218,188,189
Mvaa 164,136,24
X 191
Mvaa 168,136,40
Mvaa 168,140,44
X 160
Mvaa 172,140,60
s_add_u32 s36, s29, 131072
K 219,190,191
Mvaa 172,136,56
W 2
X 161
Cvaa 176,144,0,224
Ra 136,242,26624
Ra 140,242,27648
Mvaa 176,148,4
X 162
Cvaa 180,148,20,228
K 196,160,161
Mvaa 180,144,16
buffer_store_dwordx4 a[124:127], v1, s[8:11], s36 offen offset:0 sc1
Cvaa 184,144,32,232
X 163
Mvaa 184,148,36
X 164
Cvaa 188,148,52,236
K 197,162,163
Mvaa 188,144,48
X 165
Mvaa 176,152,8
Ra 144,242,28672
buffer_store_dwordx4 a[116:119], v1, s[8:11], s36 offen offset:1024 sc1
Mvaa 176,156,12
Ra 148,242,29696
Mvaa 180,156,28
X 166
Mvaa 180,152,24
K 204,164,165
Mvaa 184,152,40
X 167
Mvaa 184,156,44
buffer_store_dwordx4 a[108:111], v1, s[8:11], s36 offen offset:2048 sc1
Mvaa 188,156,60
X 168
Mvaa 188,152,56
W 2
Ra 152,242,30720
K 205,166,167
Mvav 32,128,192
X 169
Mvav 64,128,200
Ra 156,242,31744
buffer_store_dwordx4 a[204:207], v1, s[8:11], s36 offen offset:3072 sc1
Mvav 96,128,208
X 170
Mvav 128,128,216
v_xor_b32 v242, 0x10000, v242
K 212,168,169
Mvav 132,132,216
X 171
Mvav 100,132,208
s_add_u32 s36, s29, 135168
X 172
Mvav 68,132,200
Mvav 36,132,192
K 213,170,171
Mvav 40,136,192
X 173
Mvav 72,136,200
X 174
Mvav 104,136,208
buffer_store_dwordx4 a[212:215], v1, s[8:11], s36 offen offset:0 sc1
Mvav 136,136,216
K 220,172,173
Mvav 140,140,216
X 175
Mvav 108,140,208
X 176
Mvav 76,140,200
s_waitcnt vmcnt(5) lgkmcnt(0)
s_barrier
Mvav 44,140,192
buffer_store_dwordx4 a[220:223], v1, s[8:11], s36 offen offset:1024 sc1
K 221,174,175
Mvav 48,144,192
Ra 128,242,0
Mvav 80,144,200
X 177
Mvav 112,144,208
X 178
Mvav 144,144,216
Ra 132,242,1024
K 198,176,177
Mvav 148,148,216
buffer_store_dwordx4 a[228:231], v1, s[8:11], s36 offen offset:2048 sc1
Mvav 116,148,208
X 179
Mvav 84,148,200
X 180
Mvav 52,148,192
Ra 136,242,2048
K 199,178,179
Mvav 56,152,192
buffer_store_dwordx4 a[76:79], v1, s[8:11], s36 offen offset:3072 sc1
)"
R"(Ra 140,242,3072
Mvav 88,152,200
X 181
Mvav 120,152,208
X 182
Mvav 152,152,216
K 206,180,181
Mvav 156,156,216
X 183
Mvav 124,156,208
Mvav 92,156,200
K 207,182,183
Mvav 60,156,192
W 0
Cvaa 160,128,0,224
Ra 144,242,4096
Ra 148,242,5120
Mvaa 160,132,4
s_add_u32 m0, s62, 0
Ra 152,242,6144
Cvaa 164,132,20,228
X 184
Mvaa 164,128,16
X 185
Cvaa 168,128,32,232
X 186
Mvaa 168,132,36
Ra 156,242,7168
K 214,184,185
Cvaa 172,132,52,236
Dc 1,8,61,0
Mvaa 172,128,48
X 187
Mvaa 160,136,8
X 188
Mvaa 160,140,12
Ra 128,242,16384
K 215,186,187
Mvaa 164,140,28
Dc 1,8,61,1024
Mvaa 164,136,24
X 189
Mvaa 168,136,40
X 190
Mvaa 168,140,44
Ra 132,242,17408
K 222,188,189
Mvaa 172,140,60
Dc 1,8,61,2048
Mvaa 172,136,56
X 191
v_smfmac_f32_16x16x64_bf16 a[160:163], a[176:179], v[192:199], v240
Ra 136,242,18432
K 223,190,191
v_smfmac_f32_16x16x64_bf16 a[164:167], a[176:179], v[200:207], v240
X 160
v_smfmac_f32_16x16x64_bf16 a[168:171], a[176:179], v[208:215], v240
Dc 1,8,61,3072
v_smfmac_f32_16x16x64_bf16 a[172:175], a[176:179], v[216:223], v240
W 3
X 161
Cvaa 176,144,0,224
s_add_u32 m0, s62, 4096
X 162
Mvaa 176,148,4
Ra 140,242,19456
K 192,160,161
Cvaa 180,148,20,228
X 163
Mvaa 180,144,16
Dc 243,8,61,0
Cvaa 184,144,32,232
X 164
Mvaa 184,148,36
K 193,162,163
Cvaa 188,148,52,236
X 165
Mvaa 188,144,48
Dc 243,8,61,1024
Ra 144,242,20480
Mvaa 176,152,8
X 166
Mvaa 176,156,12
Ra 148,242,21504
K 200,164,165
Mvaa 180,156,28
X 167
Mvaa 180,152,24
Dc 243,8,61,2048
Mvaa 184,152,40
X 168
Mvaa 184,156,44
K 201,166,167
Mvaa 188,156,60
X 169
Mvaa 188,152,56
W 2
Dc 243,8,61,3072
Ra 152,242,22528
Mvav 32,128,196
s_add_u32 m0, s62, 8192
X 170
Mvav 64,128,204
Ra 156,242,23552
K 208,168,169
Mvav 96,128,212
X 171
Mvav 128,128,220
Dc 244,8,61,0
Mvav 132,132,220
X 172
Mvav 100,132,212
Ra 128,242,8192
K 209,170,171
Mvav 68,132,204
X 173
Mvav 36,132,196
Dc 244,8,61,1024
Mvav 40,136,196
X 174
Mvav 72,136,204
Ra 132,242,9216
K 216,172,173
Mvav 104,136,212
X 175
Mvav 136,136,220
Dc 244,8,61,2048
Mvav 140,140,220
X 176
Mvav 108,140,212
Ra 136,242,10240
K 217,174,175
Mvav 76,140,204
X 177
Mvav 44,140,196
W 3
Dc 244,8,61,3072
Mvav 48,144,196
X 178
Mvav 80,144,204
s_add_u32 m0, s62, 12288
Ra 140,242,11264
Mvav 112,144,212
K 194,176,177
Mvav 144,144,220
X 179
Mvav 148,148,220
X 180
Mvav 116,148,212
Dc 245,8,61,0
Ra 144,242,12288
Mvav 84,148,204
K 195,178,179
Mvav 52,148,196
X 181
Mvav 56,152,196
X 182
Mvav 88,152,204
Dc 245,8,61,1024
Ra 148,242,13312
Mvav 120,152,212
K 202,180,181
Mvav 152,152,220
X 183
Mvav 156,156,220
X 184
Mvav 124,156,212
Dc 245,8,61,2048
Ra 152,242,14336
Mvav 92,156,204
K 203,182,183
Mvav 60,156,196
W 3
X 185
Cvaa 160,128,0,224
X 186
Mvaa 160,132,4
Dc 245,8,61,3072
Ra 156,242,15360
Cvaa 164,132,20,228
s_add_u32 s61, s61, s63
K 210,184,185
Mvaa 164,128,16
X 187
Cvaa 168,128,32,232
X 188
Mvaa 168,132,36
Cvaa 172,132,52,236
s_xor_b32 s62, s62, 0x10000
K 211,186,187
Mvaa 172,128,48
X 189
Mvaa 160,136,8
Ra 128,242,24576
Ra 132,242,25600
Mvaa 160,140,12
s_add_u32 s64, s64, 1
X 190
Mvaa 164,140,28
K 218,188,189
Mvaa 164,136,24
X 191
Mvaa 168,136,40
Mvaa 168,140,44
X 160
Mvaa 172,140,60
s_add_u32 s36, s29, 327680
K 219,190,191
Mvaa 172,136,56
W 2
X 161
Cvaa 176,144,0,224
Ra 136,242,26624
Ra 140,242,27648
Mvaa 176,148,4
X 162
Cvaa 180,148,20,228
K 196,160,161
Mvaa 180,144,16
buffer_store_dwordx4 a[84:87], v1, s[8:11], s36 offen offset:0 sc1
Cvaa 184,144,32,232
X 163
Mvaa 184,148,36
X 164
Cvaa 188,148,52,236
K 197,162,163
Mvaa 188,144,48
X 165
Mvaa 176,152,8
Ra 144,242,28672
buffer_store_dwordx4 a[92:95], v1, s[8:11], s36 offen offset:1024 sc1
Mvaa 176,156,12
Ra 148,242,29696
Mvaa 180,156,28
X 166
Mvaa 180,152,24
K 204,164,165
Mvaa 184,152,40
X 167
Mvaa 184,156,44
buffer_store_dwordx4 a[188:191], v1, s[8:11], s36 offen offset:2048 sc1
Mvaa 188,156,60
X 168
Mvaa 188,152,56
W 2
Ra 152,242,30720
K 205,166,167
Mvav 32,128,192
X 169
Mvav 64,128,200
Ra 156,242,31744
buffer_store_dwordx4 a[196:199], v1, s[8:11], s36 offen offset:3072 sc1
Mvav 96,128,208
X 170
Mvav 128,128,216
v_xor_b32 v242, 0x10000, v242
K 212,168,169
Mvav 132,132,216
X 171
Mvav 100,132,208
s_add_u32 s36, s29, 331776
X 172
Mvav 68,132,200
Mvav 36,132,192
K 213,170,171
Mvav 40,136,192
X 173
Mvav 72,136,200
X 174
Mvav 104,136,208
buffer_store_dwordx4 a[236:239], v1, s[8:11], s36 offen offset:0 sc1
Mvav 136,136,216
K 220,172,173
Mvav 140,140,216
X 175
Mvav 108,140,208
X 176
Mvav 76,140,200
s_waitcnt vmcnt(5) lgkmcnt(0)
s_barrier
Mvav 44,140,192
buffer_store_dwordx4 a[244:247], v1, s[8:11], s36 offen offset:1024 sc1
K 221,174,175
Mvav 48,144,192
Ra 128,242,0
Mvav 80,144,200
X 177
Mvav 112,144,208
X 178
Mvav 144,144,216
Ra 132,242,1024
K 198,176,177
Mvav 148,148,216
buffer_store_dwordx4 a[68:71], v1, s[8:11], s36 offen offset:2048 sc1
Mvav 116,148,208
X 179
Mvav 84,148,200
X 180
Mvav 52,148,192
Ra 136,242,2048
K 199,178,179
Mvav 56,152,192
buffer_store_dwordx4 a[184:187], v1, s[8:11], s36 offen offset:3072 sc1
Ra 140,242,3072
Mvav 88,152,200
X 181
Mvav 120,152,208
X 182
Mvav 152,152,216
K 206,180,181
Mvav 156,156,216
X 183
Mvav 124,156,208
Mvav 92,156,200
K 207,182,183
Mvav 60,156,192
s_mov_b64 exec, 0
s_cmp_eq_u32 s14, 2
s_cbranch_scc1 .Lh_3_2%=
s_branch .Lh_3_3%=
.Lhr_70%=:
s_mov_b64 exec, -1
W 0
Cvaa 160,128,0,224
Ra 144,242,4096
Ra 148,242,5120
Mvaa 160,132,4
s_add_u32 m0, s62, 0
Ra 152,242,6144
Cvaa 164,132,20,228
X 184
Mvaa 164,128,16
X 185
Cvaa 168,128,32,232
X 186
Mvaa 168,132,36
Ra 156,242,7168
K 214,184,185
Cvaa 172,132,52,236
Dc 1,8,61,0
Mvaa 172,128,48
X 187
Mvaa 160,136,8
X 188
Mvaa 160,140,12
Ra 128,242,16384
K 215,186,187
Mvaa 164,140,28
Dc 1,8,61,1024
Mvaa 164,136,24
X 189
Mvaa 168,136,40
X 190
Mvaa 168,140,44
Ra 132,242,17408
K 222,188,189
Mvaa 172,140,60
Dc 1,8,61,2048
Mvaa 172,136,56
X 191
v_smfmac_f32_16x16x64_bf16 a[160:163], a[176:179], v[192:199], v240
Ra 136,242,18432
K 223,190,191
v_smfmac_f32_16x16x64_bf16 a[164:167], a[176:179], v[200:207], v240
X 160
v_smfmac_f32_16x16x64_bf16 a[168:171], a[176:179], v[208:215], v240
Dc 1,8,61,3072
v_smfmac_f32_16x16x64_bf16 a[172:175], a[176:179], v[216:223], v240
W 3
X 161
Cvaa 176,144,0,224
s_add_u32 m0, s62, 4096
X 162
Mvaa 176,148,4
Ra 140,242,19456
K 192,160,161
Cvaa 180,148,20,228
X 163
Mvaa 180,144,16
Dc 243,8,61,0
Cvaa 184,144,32,232
X 164
Mvaa 184,148,36
K 193,162,163
Cvaa 188,148,52,236
X 165
Mvaa 188,144,48
Dc 243,8,61,1024
Ra 144,242,20480
Mvaa 176,152,8
X 166
Mvaa 176,156,12
Ra 148,242,21504
K 200,164,165
Mvaa 180,156,28
X 167
Mvaa 180,152,24
Dc 243,8,61,2048
Mvaa 184,152,40
X 168
)"
R"(Mvaa 184,156,44
K 201,166,167
Mvaa 188,156,60
X 169
Mvaa 188,152,56
W 2
Dc 243,8,61,3072
Ra 152,242,22528
Mvav 32,128,196
s_add_u32 m0, s62, 8192
X 170
Mvav 64,128,204
Ra 156,242,23552
K 208,168,169
Mvav 96,128,212
X 171
Mvav 128,128,220
Dc 244,8,61,0
Mvav 132,132,220
X 172
Mvav 100,132,212
Ra 128,242,8192
K 209,170,171
Mvav 68,132,204
X 173
Mvav 36,132,196
Dc 244,8,61,1024
Mvav 40,136,196
X 174
Mvav 72,136,204
Ra 132,242,9216
K 216,172,173
Mvav 104,136,212
X 175
Mvav 136,136,220
Dc 244,8,61,2048
Mvav 140,140,220
X 176
Mvav 108,140,212
Ra 136,242,10240
K 217,174,175
Mvav 76,140,204
X 177
Mvav 44,140,196
W 3
Dc 244,8,61,3072
Mvav 48,144,196
X 178
Mvav 80,144,204
s_add_u32 m0, s62, 12288
Ra 140,242,11264
Mvav 112,144,212
K 194,176,177
Mvav 144,144,220
X 179
Mvav 148,148,220
X 180
Mvav 116,148,212
Dc 245,8,61,0
Ra 144,242,12288
Mvav 84,148,204
K 195,178,179
Mvav 52,148,196
X 181
Mvav 56,152,196
X 182
Mvav 88,152,204
Dc 245,8,61,1024
Ra 148,242,13312
Mvav 120,152,212
K 202,180,181
Mvav 152,152,220
X 183
Mvav 156,156,220
s_waitcnt vmcnt(14)
s_add_u32 s80, s71, 256
v_mov_b64 v[28:29], s[12:13]
s_mov_b64 exec, 1
buffer_store_dwordx2 v[28:29], off, s[8:11], s80 sc1
s_mov_b64 exec, -1
X 184
Mvav 124,156,212
Ra 152,242,14336
Mvav 92,156,204
K 203,182,183
Mvav 60,156,196
W 3
X 185
Cvaa 160,128,0,224
Dc 245,8,61,2048
Ra 156,242,15360
Mvaa 160,132,4
X 186
Cvaa 164,132,20,228
K 210,184,185
Mvaa 164,128,16
X 187
Cvaa 168,128,32,232
Dc 245,8,61,3072
Mvaa 168,132,36
s_add_u32 s61, s61, s63
X 188
Cvaa 172,132,52,236
s_xor_b32 s62, s62, 0x10000
K 211,186,187
Mvaa 172,128,48
X 189
Mvaa 160,136,8
Ra 128,242,24576
Ra 132,242,25600
Mvaa 160,140,12
s_add_u32 s64, s64, 1
X 190
Mvaa 164,140,28
K 218,188,189
Mvaa 164,136,24
X 191
Mvaa 168,136,40
Mvaa 168,140,44
X 160
Mvaa 172,140,60
s_add_u32 s36, s29, 196608
K 219,190,191
Mvaa 172,136,56
W 2
X 161
Cvaa 176,144,0,224
Ra 136,242,26624
Ra 140,242,27648
Mvaa 176,148,4
X 162
Cvaa 180,148,20,228
K 196,160,161
Mvaa 180,144,16
buffer_store_dwordx4 a[192:195], v1, s[8:11], s36 offen offset:0 sc1
Cvaa 184,144,32,232
X 163
Mvaa 184,148,36
X 164
Cvaa 188,148,52,236
K 197,162,163
Mvaa 188,144,48
X 165
Mvaa 176,152,8
Ra 144,242,28672
buffer_store_dwordx4 a[200:203], v1, s[8:11], s36 offen offset:1024 sc1
Mvaa 176,156,12
Ra 148,242,29696
Mvaa 180,156,28
X 166
Mvaa 180,152,24
K 204,164,165
Mvaa 184,152,40
X 167
Mvaa 184,156,44
buffer_store_dwordx4 a[208:211], v1, s[8:11], s36 offen offset:2048 sc1
Mvaa 188,156,60
X 168
Mvaa 188,152,56
W 2
Ra 152,242,30720
K 205,166,167
Mvav 32,128,192
X 169
Mvav 64,128,200
Ra 156,242,31744
buffer_store_dwordx4 a[216:219], v1, s[8:11], s36 offen offset:3072 sc1
Mvav 96,128,208
X 170
Mvav 128,128,216
v_xor_b32 v242, 0x10000, v242
K 212,168,169
Mvav 132,132,216
X 171
Mvav 100,132,208
s_add_u32 s36, s29, 200704
X 172
Mvav 68,132,200
Mvav 36,132,192
K 213,170,171
Mvav 40,136,192
X 173
Mvav 72,136,200
X 174
Mvav 104,136,208
buffer_store_dwordx4 a[224:227], v1, s[8:11], s36 offen offset:0 sc1
Mvav 136,136,216
K 220,172,173
Mvav 140,140,216
X 175
Mvav 108,140,208
X 176
Mvav 76,140,200
s_waitcnt vmcnt(5) lgkmcnt(0)
s_barrier
Mvav 44,140,192
buffer_store_dwordx4 a[232:235], v1, s[8:11], s36 offen offset:1024 sc1
K 221,174,175
Mvav 48,144,192
Ra 128,242,0
Mvav 80,144,200
X 177
Mvav 112,144,208
X 178
Mvav 144,144,216
Ra 132,242,1024
K 198,176,177
Mvav 148,148,216
buffer_store_dwordx4 a[240:243], v1, s[8:11], s36 offen offset:2048 sc1
Mvav 116,148,208
X 179
Mvav 84,148,200
X 180
Mvav 52,148,192
Ra 136,242,2048
K 199,178,179
Mvav 56,152,192
buffer_store_dwordx4 a[100:103], v1, s[8:11], s36 offen offset:3072 sc1
Ra 140,242,3072
Mvav 88,152,200
X 181
Mvav 120,152,208
X 182
Mvav 152,152,216
K 206,180,181
Mvav 156,156,216
X 183
Mvav 124,156,208
Mvav 92,156,200
K 207,182,183
Mvav 60,156,192
W 0
Cvaa 160,128,0,224
Ra 144,242,4096
Ra 148,242,5120
Mvaa 160,132,4
s_add_u32 m0, s62, 0
Ra 152,242,6144
Cvaa 164,132,20,228
X 184
Mvaa 164,128,16
X 185
Cvaa 168,128,32,232
X 186
Mvaa 168,132,36
Ra 156,242,7168
K 214,184,185
Cvaa 172,132,52,236
Dc 1,8,61,0
Mvaa 172,128,48
X 187
Mvaa 160,136,8
X 188
Mvaa 160,140,12
Ra 128,242,16384
K 215,186,187
Mvaa 164,140,28
Dc 1,8,61,1024
Mvaa 164,136,24
X 189
Mvaa 168,136,40
X 190
Mvaa 168,140,44
Ra 132,242,17408
K 222,188,189
Mvaa 172,140,60
Dc 1,8,61,2048
Mvaa 172,136,56
X 191
v_smfmac_f32_16x16x64_bf16 a[160:163], a[176:179], v[192:199], v240
Ra 136,242,18432
K 223,190,191
v_smfmac_f32_16x16x64_bf16 a[164:167], a[176:179], v[200:207], v240
X 160
v_smfmac_f32_16x16x64_bf16 a[168:171], a[176:179], v[208:215], v240
Dc 1,8,61,3072
v_smfmac_f32_16x16x64_bf16 a[172:175], a[176:179], v[216:223], v240
W 3
X 161
Cvaa 176,144,0,224
s_add_u32 m0, s62, 4096
X 162
Mvaa 176,148,4
Ra 140,242,19456
K 192,160,161
Cvaa 180,148,20,228
X 163
Mvaa 180,144,16
Dc 243,8,61,0
Cvaa 184,144,32,232
X 164
Mvaa 184,148,36
K 193,162,163
Cvaa 188,148,52,236
X 165
Mvaa 188,144,48
Dc 243,8,61,1024
Ra 144,242,20480
Mvaa 176,152,8
X 166
Mvaa 176,156,12
Ra 148,242,21504
K 200,164,165
Mvaa 180,156,28
X 167
Mvaa 180,152,24
Dc 243,8,61,2048
Mvaa 184,152,40
X 168
Mvaa 184,156,44
K 201,166,167
Mvaa 188,156,60
X 169
Mvaa 188,152,56
W 2
Dc 243,8,61,3072
Ra 152,242,22528
Mvav 32,128,196
s_add_u32 m0, s62, 8192
X 170
Mvav 64,128,204
Ra 156,242,23552
K 208,168,169
Mvav 96,128,212
X 171
Mvav 128,128,220
Dc 244,8,61,0
Mvav 132,132,220
X 172
Mvav 100,132,212
Ra 128,242,8192
K 209,170,171
Mvav 68,132,204
X 173
Mvav 36,132,196
Dc 244,8,61,1024
Mvav 40,136,196
X 174
Mvav 72,136,204
Ra 132,242,9216
K 216,172,173
Mvav 104,136,212
X 175
Mvav 136,136,220
Dc 244,8,61,2048
Mvav 140,140,220
X 176
Mvav 108,140,212
Ra 136,242,10240
K 217,174,175
Mvav 76,140,204
X 177
Mvav 44,140,196
W 3
Dc 244,8,61,3072
Mvav 48,144,196
X 178
Mvav 80,144,204
s_add_u32 m0, s62, 12288
Ra 140,242,11264
Mvav 112,144,212
K 194,176,177
Mvav 144,144,220
X 179
Mvav 148,148,220
X 180
Mvav 116,148,212
Dc 245,8,61,0
Ra 144,242,12288
Mvav 84,148,204
K 195,178,179
Mvav 52,148,196
X 181
Mvav 56,152,196
X 182
Mvav 88,152,204
Dc 245,8,61,1024
Ra 148,242,13312
Mvav 120,152,212
K 202,180,181
Mvav 152,152,220
X 183
Mvav 156,156,220
X 184
Mvav 124,156,212
Dc 245,8,61,2048
Ra 152,242,14336
Mvav 92,156,204
K 203,182,183
v_bfe_u32 v28, v1, 4, 4
Mvav 60,156,196
W 3
X 185
Cvaa 160,128,0,224
Ra 156,242,15360
v_lshlrev_b32 v28, 3, v28
Mvaa 160,132,4
s_lshl_b32 s80, s56, 9
s_add_u32 s80, s80, 4352
Dc 245,8,61,3072
)"
R"(Cvaa 164,132,20,228
s_add_u32 s61, s61, s63
X 186
Mvaa 164,128,16
K 210,184,185
Cvaa 168,128,32,232
X 187
Mvaa 168,132,36
buffer_load_dwordx2 v[28:29], v28, s[8:11], s80 offen sc1
Cvaa 172,132,52,236
s_xor_b32 s62, s62, 0x10000
X 188
Mvaa 172,128,48
Ra 128,242,24576
K 211,186,187
Mvaa 160,136,8
X 189
Mvaa 160,140,12
s_add_u32 s64, s64, 1
X 190
Mvaa 164,140,28
Ra 132,242,25600
K 218,188,189
Mvaa 164,136,24
X 191
Mvaa 168,136,40
Mvaa 168,140,44
X 160
Mvaa 172,140,60
s_add_u32 s36, s29, 262144
K 219,190,191
Mvaa 172,136,56
W 2
X 161
Cvaa 176,144,0,224
Ra 136,242,26624
Ra 140,242,27648
Mvaa 176,148,4
X 162
Cvaa 180,148,20,228
K 196,160,161
Mvaa 180,144,16
buffer_store_dwordx4 a[64:67], v1, s[8:11], s36 offen offset:0 sc1
Cvaa 184,144,32,232
X 163
Mvaa 184,148,36
X 164
Cvaa 188,148,52,236
K 197,162,163
Mvaa 188,144,48
X 165
Mvaa 176,152,8
Ra 144,242,28672
buffer_store_dwordx4 a[72:75], v1, s[8:11], s36 offen offset:1024 sc1
Mvaa 176,156,12
Ra 148,242,29696
Mvaa 180,156,28
X 166
Mvaa 180,152,24
K 204,164,165
Mvaa 184,152,40
X 167
Mvaa 184,156,44
buffer_store_dwordx4 a[80:83], v1, s[8:11], s36 offen offset:2048 sc1
Mvaa 188,156,60
X 168
Mvaa 188,152,56
W 2
Ra 152,242,30720
K 205,166,167
Mvav 32,128,192
X 169
Mvav 64,128,200
Ra 156,242,31744
buffer_store_dwordx4 a[88:91], v1, s[8:11], s36 offen offset:3072 sc1
Mvav 96,128,208
X 170
Mvav 128,128,216
v_xor_b32 v242, 0x10000, v242
K 212,168,169
Mvav 132,132,216
X 171
Mvav 100,132,208
s_add_u32 s36, s29, 266240
X 172
Mvav 68,132,200
Mvav 36,132,192
K 213,170,171
Mvav 40,136,192
X 173
Mvav 72,136,200
X 174
Mvav 104,136,208
buffer_store_dwordx4 a[96:99], v1, s[8:11], s36 offen offset:0 sc1
Mvav 136,136,216
s_waitcnt vmcnt(5)
v_cmp_eq_u64 vcc, s[12:13], v[28:29]
s_cmp_eq_u64 vcc, -1
s_cbranch_scc0 .Lspin_71%=
.Lok_72%=:
K 220,172,173
Mvav 140,140,216
X 175
Mvav 108,140,208
X 176
Mvav 76,140,200
W 0
s_barrier
Mvav 44,140,192
buffer_store_dwordx4 a[104:107], v1, s[8:11], s36 offen offset:1024 sc1
K 221,174,175
Mvav 48,144,192
Ra 128,242,0
Mvav 80,144,200
X 177
Mvav 112,144,208
X 178
Mvav 144,144,216
Ra 132,242,1024
K 198,176,177
Mvav 148,148,216
buffer_store_dwordx4 a[112:115], v1, s[8:11], s36 offen offset:2048 sc1
Mvav 116,148,208
X 179
Mvav 84,148,200
X 180
Mvav 52,148,192
Ra 136,242,2048
K 199,178,179
Mvav 56,152,192
buffer_store_dwordx4 a[120:123], v1, s[8:11], s36 offen offset:3072 sc1
Ra 140,242,3072
Mvav 88,152,200
X 181
Mvav 120,152,208
X 182
Mvav 152,152,216
K 206,180,181
Mvav 156,156,216
X 183
Mvav 124,156,208
Mvav 92,156,200
K 207,182,183
Mvav 60,156,192
W 0
Cvaa 160,128,0,224
Ra 144,242,4096
Ra 148,242,5120
Mvaa 160,132,4
s_add_u32 m0, s62, 0
Ra 152,242,6144
Cvaa 164,132,20,228
X 184
Mvaa 164,128,16
X 185
Cvaa 168,128,32,232
X 186
Mvaa 168,132,36
Ra 156,242,7168
K 214,184,185
Cvaa 172,132,52,236
Dc 1,8,61,0
Mvaa 172,128,48
X 187
Mvaa 160,136,8
X 188
Mvaa 160,140,12
Ra 128,242,16384
K 215,186,187
Mvaa 164,140,28
Dc 1,8,61,1024
Mvaa 164,136,24
X 189
Mvaa 168,136,40
X 190
Mvaa 168,140,44
Ra 132,242,17408
K 222,188,189
Mvaa 172,140,60
Dc 1,8,61,2048
Mvaa 172,136,56
X 191
v_smfmac_f32_16x16x64_bf16 a[160:163], a[176:179], v[192:199], v240
Ra 136,242,18432
K 223,190,191
v_smfmac_f32_16x16x64_bf16 a[164:167], a[176:179], v[200:207], v240
X 160
v_smfmac_f32_16x16x64_bf16 a[168:171], a[176:179], v[208:215], v240
Dc 1,8,61,3072
v_smfmac_f32_16x16x64_bf16 a[172:175], a[176:179], v[216:223], v240
W 3
X 161
Cvaa 176,144,0,224
s_add_u32 m0, s62, 4096
X 162
Mvaa 176,148,4
Ra 140,242,19456
K 192,160,161
Cvaa 180,148,20,228
X 163
Mvaa 180,144,16
Dc 243,8,61,0
Cvaa 184,144,32,232
X 164
Mvaa 184,148,36
K 193,162,163
Cvaa 188,148,52,236
X 165
Mvaa 188,144,48
Dc 243,8,61,1024
Ra 144,242,20480
Mvaa 176,152,8
X 166
Mvaa 176,156,12
Ra 148,242,21504
K 200,164,165
Mvaa 180,156,28
X 167
Mvaa 180,152,24
Dc 243,8,61,2048
Mvaa 184,152,40
X 168
Mvaa 184,156,44
K 201,166,167
Mvaa 188,156,60
X 169
Mvaa 188,152,56
W 2
Dc 243,8,61,3072
Ra 152,242,22528
Mvav 32,128,196
s_add_u32 m0, s62, 8192
X 170
Mvav 64,128,204
Ra 156,242,23552
K 208,168,169
Mvav 96,128,212
X 171
Mvav 128,128,220
Dc 244,8,61,0
Mvav 132,132,220
X 172
Mvav 100,132,212
Ra 128,242,8192
K 209,170,171
Mvav 68,132,204
X 173
Mvav 36,132,196
Dc 244,8,61,1024
Mvav 40,136,196
X 174
Mvav 72,136,204
Ra 132,242,9216
K 216,172,173
Mvav 104,136,212
X 175
Mvav 136,136,220
Dc 244,8,61,2048
Mvav 140,140,220
X 176
Mvav 108,140,212
Ra 136,242,10240
K 217,174,175
Mvav 76,140,204
X 177
Mvav 44,140,196
W 3
Dc 244,8,61,3072
Mvav 48,144,196
X 178
Mvav 80,144,204
s_add_u32 m0, s62, 12288
Ra 140,242,11264
Mvav 112,144,212
K 194,176,177
Mvav 144,144,220
X 179
Mvav 148,148,220
X 180
Mvav 116,148,212
Dc 245,8,61,0
Ra 144,242,12288
Mvav 84,148,204
K 195,178,179
Mvav 52,148,196
X 181
Mvav 56,152,196
X 182
Mvav 88,152,204
Dc 245,8,61,1024
Ra 148,242,13312
Mvav 120,152,212
s_bitcmp1_b32 195, s57
s_cselect_b32 s22, -1, 0
K 202,180,181
Mvav 152,152,220
s_lshr_b32 s82, s57, 2
X 183
Mvav 156,156,220
s_waitcnt vmcnt(14)
s_add_u32 s80, s71, 384
v_mov_b64 v[28:29], s[12:13]
s_mov_b64 exec, 1
buffer_store_dwordx2 v[28:29], off, s[8:11], s80 sc1
s_mov_b64 exec, -1
X 184
Mvav 124,156,212
s_and_b32 s81, s57, 1
s_lshl1_add_u32 s81, s82, s81
Ra 152,242,14336
Mvav 92,156,204
s_lshl_b32 s82, s56, 2
K 203,182,183
Mvav 60,156,196
W 3
X 185
Cvaa 160,128,0,224
s_add_u32 s81, s81, s82
Dc 245,8,61,2048
Ra 156,242,15360
Mvaa 160,132,4
s_lshl_b32 s81, s81, 1
X 186
Cvaa 164,132,20,228
s_add_u32 s81, s81, s14
s_sub_u32 s81, s81, 2
K 210,184,185
Mvaa 164,128,16
s_mul_i32 s81, s81, 20480
X 187
Cvaa 168,128,32,232
Dc 245,8,61,3072
v_min_u32 v2, 624, v1
Mvaa 168,132,36
s_add_u32 s61, s61, s63
X 188
Cvaa 172,132,52,236
s_xor_b32 s62, s62, 0x10000
v_lshl_add_u32 v2, v2, 3, s81
K 211,186,187
Mvaa 172,128,48
Ra 128,242,24576
v_and_b32 v2, s22, v2
Mvaa 160,136,8
s_and_b32 s22, s22, 5120
X 189
Mvaa 160,140,12
s_add_u32 s64, s64, 1
global_load_dword v3, v2, s[26:27]
Ra 132,242,25600
Mvaa 164,140,28
X 190
Mvaa 164,136,24
v_add_u32 v2, s22, v2
K 218,188,189
Mvaa 168,136,40
X 191
Mvaa 168,140,44
X 160
Mvaa 172,140,60
K 219,190,191
Mvaa 172,136,56
W 2
X 161
Cvaa 176,144,0,224
Ra 136,242,26624
Ra 140,242,27648
Mvaa 176,148,4
X 162
Cvaa 180,148,20,228
K 196,160,161
Mvaa 180,144,16
X 163
Cvaa 184,144,32,232
Mvaa 184,148,36
X 164
Cvaa 188,148,52,236
K 197,162,163
Mvaa 188,144,48
X 165
Mvaa 176,152,8
Ra 144,242,28672
Ra 148,242,29696
Mvaa 176,156,12
X 166
Mvaa 180,156,28
K 204,164,165
Mvaa 180,152,24
X 167
Mvaa 184,152,40
Mvaa 184,156,44
X 168
Mvaa 188,156,60
Mvaa 188,152,56
W 2
Ra 152,242,30720
K 205,166,167
Mvav 32,128,192
X 169
Mvav 64,128,200
)"
R"(Ra 156,242,31744
v_xor_b32 v242, 0x10000, v242
Mvav 96,128,208
X 170
Mvav 128,128,216
K 212,168,169
Mvav 132,132,216
X 171
Mvav 100,132,208
X 172
Mvav 68,132,200
Mvav 36,132,192
K 213,170,171
Mvav 40,136,192
X 173
Mvav 72,136,200
X 174
Mvav 104,136,208
Mvav 136,136,216
K 220,172,173
Mvav 140,140,216
X 175
Mvav 108,140,208
X 176
Mvav 76,140,200
s_waitcnt vmcnt(1) lgkmcnt(0)
s_barrier
Mvav 44,140,192
Ra 128,242,0
K 221,174,175
Mvav 48,144,192
X 177
Mvav 80,144,200
.Lh_3_2%=:
s_cbranch_execz .Lh_3_2_1%=
X 178
Mvav 112,144,208
Ra 132,242,1024
Mvav 144,144,216
K 198,176,177
Mvav 148,148,216
X 179
Mvav 116,148,208
X 180
Mvav 84,148,200
Ra 136,242,2048
Mvav 52,148,192
K 199,178,179
Mvav 56,152,192
X 181
Mvav 88,152,200
Ra 140,242,3072
Mvav 120,152,208
X 182
Mvav 152,152,216
K 206,180,181
Mvav 156,156,216
X 183
Mvav 124,156,208
Mvav 92,156,200
K 207,182,183
Mvav 60,156,192
W 0
Cvaa 160,128,0,224
Ra 144,242,4096
Ra 148,242,5120
Mvaa 160,132,4
s_add_u32 m0, s62, 0
Ra 152,242,6144
Cvaa 164,132,20,228
X 184
Mvaa 164,128,16
X 185
Cvaa 168,128,32,232
X 186
Mvaa 168,132,36
Ra 156,242,7168
K 214,184,185
Cvaa 172,132,52,236
Dc 1,8,61,0
Mvaa 172,128,48
X 187
Mvaa 160,136,8
X 188
Mvaa 160,140,12
Ra 128,242,16384
K 215,186,187
Mvaa 164,140,28
Dc 1,8,61,1024
Mvaa 164,136,24
X 189
Mvaa 168,136,40
X 190
Mvaa 168,140,44
Ra 132,242,17408
K 222,188,189
Mvaa 172,140,60
Dc 1,8,61,2048
Mvaa 172,136,56
X 191
v_smfmac_f32_16x16x64_bf16 a[160:163], a[176:179], v[192:199], v240
Ra 136,242,18432
K 223,190,191
v_smfmac_f32_16x16x64_bf16 a[164:167], a[176:179], v[200:207], v240
X 160
v_smfmac_f32_16x16x64_bf16 a[168:171], a[176:179], v[208:215], v240
Dc 1,8,61,3072
v_smfmac_f32_16x16x64_bf16 a[172:175], a[176:179], v[216:223], v240
W 3
X 161
Cvaa 176,144,0,224
s_add_u32 m0, s62, 4096
X 162
Mvaa 176,148,4
Ra 140,242,19456
K 192,160,161
Cvaa 180,148,20,228
X 163
Mvaa 180,144,16
Dc 243,8,61,0
Cvaa 184,144,32,232
X 164
Mvaa 184,148,36
K 193,162,163
Cvaa 188,148,52,236
X 165
Mvaa 188,144,48
Dc 243,8,61,1024
Ra 144,242,20480
Mvaa 176,152,8
X 166
Mvaa 176,156,12
Ra 148,242,21504
K 200,164,165
Mvaa 180,156,28
X 167
Mvaa 180,152,24
Dc 243,8,61,2048
Mvaa 184,152,40
X 168
Mvaa 184,156,44
K 201,166,167
Mvaa 188,156,60
X 169
Mvaa 188,152,56
W 2
Dc 243,8,61,3072
Ra 152,242,22528
Mvav 32,128,196
s_add_u32 m0, s62, 8192
X 170
Mvav 64,128,204
Ra 156,242,23552
K 208,168,169
Mvav 96,128,212
X 171
Mvav 128,128,220
Dc 244,8,61,0
Mvav 132,132,220
X 172
Mvav 100,132,212
Ra 128,242,8192
K 209,170,171
Mvav 68,132,204
X 173
Mvav 36,132,196
Dc 244,8,61,1024
Mvav 40,136,196
X 174
Mvav 72,136,204
Ra 132,242,9216
K 216,172,173
Mvav 104,136,212
X 175
Mvav 136,136,220
Dc 244,8,61,2048
Mvav 140,140,220
X 176
Mvav 108,140,212
Ra 136,242,10240
K 217,174,175
Mvav 76,140,204
X 177
Mvav 44,140,196
W 3
Dc 244,8,61,3072
Mvav 48,144,196
X 178
Mvav 80,144,204
s_add_u32 m0, s62, 12288
Ra 140,242,11264
Mvav 112,144,212
K 194,176,177
Mvav 144,144,220
X 179
Mvav 148,148,220
X 180
Mvav 116,148,212
Dc 245,8,61,0
Ra 144,242,12288
Mvav 84,148,204
K 195,178,179
Mvav 52,148,196
X 181
Mvav 56,152,196
X 182
Mvav 88,152,204
Dc 245,8,61,1024
Ra 148,242,13312
Mvav 120,152,212
K 202,180,181
Mvav 152,152,220
X 183
Mvav 156,156,220
X 184
Mvav 124,156,212
Dc 245,8,61,2048
Ra 152,242,14336
Mvav 92,156,204
K 203,182,183
Mvav 60,156,196
W 3
X 185
Cvaa 160,128,0,224
X 186
Mvaa 160,132,4
Dc 245,8,61,3072
Ra 156,242,15360
Cvaa 164,132,20,228
s_add_u32 s61, s61, s63
K 210,184,185
Mvaa 164,128,16
X 187
Cvaa 168,128,32,232
X 188
Mvaa 168,132,36
Cvaa 172,132,52,236
s_xor_b32 s62, s62, 0x10000
global_load_dword v3, v2, s[26:27]
K 211,186,187
Mvaa 172,128,48
X 189
Mvaa 160,136,8
Ra 128,242,24576
Ra 132,242,25600
Mvaa 160,140,12
s_add_u32 s64, s64, 1
X 190
Mvaa 164,140,28
v_add_u32 v2, s22, v2
K 218,188,189
Mvaa 164,136,24
X 191
Mvaa 168,136,40
Mvaa 168,140,44
X 160
Mvaa 172,140,60
K 219,190,191
Mvaa 172,136,56
W 2
X 161
Cvaa 176,144,0,224
Ra 136,242,26624
Ra 140,242,27648
Mvaa 176,148,4
X 162
Cvaa 180,148,20,228
K 196,160,161
Mvaa 180,144,16
X 163
Cvaa 184,144,32,232
Mvaa 184,148,36
X 164
Cvaa 188,148,52,236
K 197,162,163
Mvaa 188,144,48
X 165
Mvaa 176,152,8
Ra 144,242,28672
Ra 148,242,29696
Mvaa 176,156,12
X 166
Mvaa 180,156,28
K 204,164,165
Mvaa 180,152,24
X 167
Mvaa 184,152,40
Mvaa 184,156,44
X 168
Mvaa 188,156,60
Mvaa 188,152,56
W 2
Ra 152,242,30720
K 205,166,167
Mvav 32,128,192
X 169
Mvav 64,128,200
Ra 156,242,31744
v_xor_b32 v242, 0x10000, v242
Mvav 96,128,208
X 170
Mvav 128,128,216
K 212,168,169
Mvav 132,132,216
X 171
Mvav 100,132,208
X 172
Mvav 68,132,200
Mvav 36,132,192
K 213,170,171
Mvav 40,136,192
X 173
Mvav 72,136,200
X 174
Mvav 104,136,208
Mvav 136,136,216
K 220,172,173
Mvav 140,140,216
X 175
Mvav 108,140,208
X 176
Mvav 76,140,200
s_waitcnt vmcnt(1) lgkmcnt(0)
s_barrier
Mvav 44,140,192
Ra 128,242,0
K 221,174,175
Mvav 48,144,192
X 177
Mvav 80,144,200
X 178
Mvav 112,144,208
Ra 132,242,1024
Mvav 144,144,216
K 198,176,177
Mvav 148,148,216
X 179
Mvav 116,148,208
X 180
Mvav 84,148,200
Ra 136,242,2048
Mvav 52,148,192
K 199,178,179
Mvav 56,152,192
X 181
Mvav 88,152,200
Ra 140,242,3072
Mvav 120,152,208
X 182
Mvav 152,152,216
K 206,180,181
Mvav 156,156,216
X 183
Mvav 124,156,208
Mvav 92,156,200
K 207,182,183
Mvav 60,156,192
W 0
Cvaa 160,128,0,224
Ra 144,242,4096
Ra 148,242,5120
Mvaa 160,132,4
s_add_u32 m0, s62, 0
Ra 152,242,6144
Cvaa 164,132,20,228
X 184
Mvaa 164,128,16
X 185
Cvaa 168,128,32,232
X 186
Mvaa 168,132,36
Ra 156,242,7168
K 214,184,185
Cvaa 172,132,52,236
Dc 1,8,61,0
Mvaa 172,128,48
X 187
Mvaa 160,136,8
X 188
Mvaa 160,140,12
Ra 128,242,16384
K 215,186,187
Mvaa 164,140,28
Dc 1,8,61,1024
Mvaa 164,136,24
X 189
Mvaa 168,136,40
X 190
Mvaa 168,140,44
Ra 132,242,17408
K 222,188,189
Mvaa 172,140,60
Dc 1,8,61,2048
Mvaa 172,136,56
X 191
v_smfmac_f32_16x16x64_bf16 a[160:163], a[176:179], v[192:199], v240
Ra 136,242,18432
K 223,190,191
v_smfmac_f32_16x16x64_bf16 a[164:167], a[176:179], v[200:207], v240
X 160
)"
R"(v_smfmac_f32_16x16x64_bf16 a[168:171], a[176:179], v[208:215], v240
Dc 1,8,61,3072
v_smfmac_f32_16x16x64_bf16 a[172:175], a[176:179], v[216:223], v240
W 3
X 161
Cvaa 176,144,0,224
s_add_u32 m0, s62, 4096
X 162
Mvaa 176,148,4
Ra 140,242,19456
K 192,160,161
Cvaa 180,148,20,228
X 163
Mvaa 180,144,16
Dc 243,8,61,0
Cvaa 184,144,32,232
X 164
Mvaa 184,148,36
K 193,162,163
Cvaa 188,148,52,236
X 165
Mvaa 188,144,48
Dc 243,8,61,1024
Ra 144,242,20480
Mvaa 176,152,8
X 166
Mvaa 176,156,12
Ra 148,242,21504
K 200,164,165
Mvaa 180,156,28
X 167
Mvaa 180,152,24
Dc 243,8,61,2048
Mvaa 184,152,40
X 168
Mvaa 184,156,44
K 201,166,167
Mvaa 188,156,60
X 169
Mvaa 188,152,56
W 2
Dc 243,8,61,3072
Ra 152,242,22528
Mvav 32,128,196
s_add_u32 m0, s62, 8192
X 170
Mvav 64,128,204
Ra 156,242,23552
K 208,168,169
Mvav 96,128,212
X 171
Mvav 128,128,220
Dc 244,8,61,0
Mvav 132,132,220
X 172
Mvav 100,132,212
Ra 128,242,8192
K 209,170,171
Mvav 68,132,204
X 173
Mvav 36,132,196
Dc 244,8,61,1024
Mvav 40,136,196
X 174
Mvav 72,136,204
Ra 132,242,9216
K 216,172,173
Mvav 104,136,212
X 175
Mvav 136,136,220
Dc 244,8,61,2048
Mvav 140,140,220
X 176
Mvav 108,140,212
Ra 136,242,10240
K 217,174,175
Mvav 76,140,204
X 177
Mvav 44,140,196
W 3
Dc 244,8,61,3072
Mvav 48,144,196
X 178
Mvav 80,144,204
s_add_u32 m0, s62, 12288
Ra 140,242,11264
Mvav 112,144,212
K 194,176,177
Mvav 144,144,220
X 179
Mvav 148,148,220
X 180
Mvav 116,148,212
Dc 245,8,61,0
Ra 144,242,12288
Mvav 84,148,204
K 195,178,179
Mvav 52,148,196
X 181
Mvav 56,152,196
X 182
Mvav 88,152,204
Dc 245,8,61,1024
Ra 148,242,13312
Mvav 120,152,212
K 202,180,181
Mvav 152,152,220
X 183
Mvav 156,156,220
X 184
Mvav 124,156,212
Dc 245,8,61,2048
Ra 152,242,14336
Mvav 92,156,204
K 203,182,183
Mvav 60,156,196
W 3
X 185
Cvaa 160,128,0,224
X 186
Mvaa 160,132,4
Dc 245,8,61,3072
Ra 156,242,15360
Cvaa 164,132,20,228
s_add_u32 s61, s61, s63
K 210,184,185
Mvaa 164,128,16
X 187
Cvaa 168,128,32,232
X 188
Mvaa 168,132,36
Cvaa 172,132,52,236
s_xor_b32 s62, s62, 0x10000
global_load_dword v3, v2, s[26:27]
K 211,186,187
Mvaa 172,128,48
Ra 128,242,24576
v_add_u32 v2, s22, v2
Mvaa 160,136,8
s_lshr_b32 s80, s15, 3
s_lshl_b32 s80, s80, 1
s_add_u32 s80, s80, s14
Mvaa 160,140,12
s_sub_u32 s80, s80, 2
s_lshl_b32 s80, s80, 12
v_min_u32 v27, 496, v1
Mvaa 164,140,28
s_add_u32 s64, s64, 1
X 189
Mvaa 164,136,24
Ra 132,242,25600
v_lshl_add_u32 v27, v27, 3, s80
Mvaa 168,136,40
X 190
Mvaa 168,140,44
v_min_u32 v27, .Lend%=-.Lbeg%=-4, v27
K 218,188,189
Mvaa 172,140,60
global_load_dword v3, v27, s[6:7]
Mvaa 172,136,56
W 2
X 191
Cvaa 176,144,0,224
X 160
Mvaa 176,148,4
Ra 136,242,26624
K 219,190,191
Cvaa 180,148,20,228
X 161
Mvaa 180,144,16
Ra 140,242,27648
Cvaa 184,144,32,232
X 162
Mvaa 184,148,36
K 196,160,161
Cvaa 188,148,52,236
X 163
Mvaa 188,144,48
X 164
Mvaa 176,152,8
Ra 144,242,28672
K 197,162,163
Mvaa 176,156,12
X 165
Mvaa 180,156,28
Ra 148,242,29696
Mvaa 180,152,24
X 166
Mvaa 184,152,40
K 204,164,165
Mvaa 184,156,44
X 167
Mvaa 188,156,60
X 168
Mvaa 188,152,56
W 2
Ra 152,242,30720
K 205,166,167
Mvav 32,128,192
X 169
Mvav 64,128,200
Ra 156,242,31744
v_xor_b32 v242, 0x10000, v242
Mvav 96,128,208
X 170
Mvav 128,128,216
K 212,168,169
Mvav 132,132,216
X 171
Mvav 100,132,208
X 172
Mvav 68,132,200
Mvav 36,132,192
K 213,170,171
Mvav 40,136,192
X 173
Mvav 72,136,200
X 174
Mvav 104,136,208
Mvav 136,136,216
K 220,172,173
Mvav 140,140,216
X 175
Mvav 108,140,208
.Lh_3_3%=:
s_cbranch_execz .Lhr_70%=
X 176
Mvav 76,140,200
s_waitcnt vmcnt(2) lgkmcnt(0)
s_barrier
Mvav 44,140,192
Ra 128,242,0
K 221,174,175
Mvav 48,144,192
X 177
Mvav 80,144,200
X 178
Mvav 112,144,208
Ra 132,242,1024
Mvav 144,144,216
K 198,176,177
Mvav 148,148,216
X 179
Mvav 116,148,208
X 180
Mvav 84,148,200
Ra 136,242,2048
Mvav 52,148,192
K 199,178,179
Mvav 56,152,192
X 181
Mvav 88,152,200
Ra 140,242,3072
Mvav 120,152,208
X 182
Mvav 152,152,216
K 206,180,181
Mvav 156,156,216
X 183
Mvav 124,156,208
Mvav 92,156,200
K 207,182,183
Mvav 60,156,192
W 0
Cvaa 160,128,0,224
Ra 144,242,4096
Ra 148,242,5120
Mvaa 160,132,4
s_add_u32 m0, s62, 0
Ra 152,242,6144
Cvaa 164,132,20,228
X 184
Mvaa 164,128,16
X 185
Cvaa 168,128,32,232
X 186
Mvaa 168,132,36
Ra 156,242,7168
K 214,184,185
Cvaa 172,132,52,236
Dc 1,8,61,0
Mvaa 172,128,48
X 187
Mvaa 160,136,8
X 188
Mvaa 160,140,12
Ra 128,242,16384
K 215,186,187
Mvaa 164,140,28
Dc 1,8,61,1024
Mvaa 164,136,24
X 189
Mvaa 168,136,40
X 190
Mvaa 168,140,44
Ra 132,242,17408
K 222,188,189
Mvaa 172,140,60
Dc 1,8,61,2048
Mvaa 172,136,56
X 191
v_smfmac_f32_16x16x64_bf16 a[160:163], a[176:179], v[192:199], v240
Ra 136,242,18432
K 223,190,191
v_smfmac_f32_16x16x64_bf16 a[164:167], a[176:179], v[200:207], v240
X 160
v_smfmac_f32_16x16x64_bf16 a[168:171], a[176:179], v[208:215], v240
Dc 1,8,61,3072
v_smfmac_f32_16x16x64_bf16 a[172:175], a[176:179], v[216:223], v240
W 3
X 161
Cvaa 176,144,0,224
s_add_u32 m0, s62, 4096
X 162
Mvaa 176,148,4
s_sub_u32 s81, s57, 1
Ra 140,242,19456
K 192,160,161
Cvaa 180,148,20,228
X 163
Mvaa 180,144,16
s_lshl_b32 s85, s81, 5
Dc 243,8,61,0
Cvaa 184,144,32,232
X 164
Mvaa 184,148,36
s_add_u32 s85, s85, s56
K 193,162,163
Cvaa 188,148,52,236
X 165
Mvaa 188,144,48
Dc 243,8,61,1024
Ra 144,242,20480
Mvaa 176,152,8
s_lshl_b32 s85, s85, 1
X 166
Mvaa 176,156,12
Ra 148,242,21504
K 200,164,165
Mvaa 180,156,28
s_add_u32 s85, s85, s14
X 167
Mvaa 180,152,24
Dc 243,8,61,2048
Mvaa 184,152,40
s_sub_u32 s85, s85, 2
X 168
Mvaa 184,156,44
K 201,166,167
Mvaa 188,156,60
X 169
Mvaa 188,152,56
W 2
s_cmp_lt_u32 s85, 160
s_cselect_b64 s[20:21], 0, -1
Dc 243,8,61,3072
Mvav 32,128,196
s_add_u32 m0, s62, 8192
Ra 152,242,22528
Ra 156,242,23552
Mvav 64,128,204
s_mul_i32 s18, s85, 205
X 170
Mvav 96,128,212
K 208,168,169
Mvav 128,128,220
s_lshr_b32 s18, s18, 12
X 171
Mvav 132,132,220
Dc 244,8,61,0
Ra 128,242,8192
Mvav 100,132,212
X 172
Mvav 68,132,204
s_and_b32 s18, s18, 7
K 209,170,171
Mvav 36,132,196
X 173
Mvav 40,136,196
Dc 244,8,61,1024
v_lshrrev_b32 v4, 1, v1
Mvav 72,136,204
X 174
Mvav 104,136,212
Ra 132,242,9216
K 216,172,173
Mvav 136,136,220
X 175
Mvav 140,140,220
Dc 244,8,61,2048
v_min_u32 v5, 344, v4
Mvav 108,140,212
s_lshl_b32 s81, s18, 3
X 176
Mvav 76,140,204
Ra 136,242,10240
K 217,174,175
Mvav 44,140,196
W 3
X 177
)"
R"(Mvav 48,144,196
Dc 244,8,61,3072
v_mov_b32 v6, s81
Mvav 80,144,204
s_add_u32 m0, s62, 12288
X 178
Mvav 112,144,212
Ra 140,242,11264
K 194,176,177
Mvav 144,144,220
X 179
Mvav 148,148,220
buffer_load_dwordx2 v[8:9], v4, s[8:11], 0 offen offset:2560 sc1
Ra 144,242,12288
Mvav 116,148,212
X 180
Mvav 84,148,204
K 195,178,179
Mvav 52,148,196
X 181
Mvav 56,152,196
buffer_load_dwordx2 v[10:11], v5, s[8:11], 0 offen offset:3072 sc1
Ra 148,242,13312
Mvav 88,152,204
X 182
Mvav 120,152,212
K 202,180,181
Mvav 152,152,220
X 183
Mvav 156,156,220
Dc 245,8,61,0
Ra 152,242,14336
Mvav 124,156,212
X 184
Mvav 92,156,204
K 203,182,183
v_bfe_u32 v28, v1, 4, 4
Mvav 60,156,196
W 3
X 185
Cvaa 160,128,0,224
buffer_load_dwordx2 v[12:13], v6, s[8:11], 0 offen offset:3584 sc1
v_lshlrev_b32 v28, 3, v28
Mvaa 160,132,4
s_lshl_b32 s80, s56, 9
X 186
Cvaa 164,132,20,228
s_add_u32 s80, s80, 4480
Ra 156,242,15360
K 210,184,185
Mvaa 164,128,16
X 187
Cvaa 168,128,32,232
Dc 245,8,61,1024
Mvaa 168,132,36
X 188
Cvaa 172,132,52,236
K 211,186,187
Mvaa 172,128,48
X 189
Mvaa 160,136,8
Dc 245,8,61,2048
Ra 128,242,24576
Mvaa 160,140,12
X 190
Mvaa 164,140,28
Ra 132,242,25600
K 218,188,189
Mvaa 164,136,24
X 191
Mvaa 168,136,40
buffer_load_dwordx2 v[28:29], v28, s[8:11], s80 offen sc1
Mvaa 168,140,44
Dc 245,8,61,3072
s_add_u32 s61, s61, s63
s_xor_b32 s62, s62, 0x10000
s_add_u32 s64, s64, 1
Mvaa 172,140,60
X 160
Mvaa 172,136,56
W 2
Ra 136,242,26624
K 219,190,191
Cvaa 176,144,0,224
X 161
Mvaa 176,148,4
global_load_dword v3, v2, s[26:27]
v_add_u32 v2, s22, v2
Cvaa 180,148,20,228
Ra 140,242,27648
Mvaa 180,144,16
X 162
Cvaa 184,144,32,232
K 196,160,161
Mvaa 184,148,36
X 163
Cvaa 188,148,52,236
X 164
Mvaa 188,144,48
Ra 144,242,28672
K 197,162,163
Mvaa 176,152,8
X 165
Mvaa 176,156,12
Ra 148,242,29696
Mvaa 180,156,28
X 166
Mvaa 180,152,24
K 204,164,165
Mvaa 184,152,40
X 167
Mvaa 184,156,44
X 168
Mvaa 188,156,60
Mvaa 188,152,56
W 2
Ra 152,242,30720
K 205,166,167
Mvav 32,128,192
X 169
Mvav 64,128,200
Ra 156,242,31744
v_xor_b32 v242, 0x10000, v242
Mvav 96,128,208
X 170
Mvav 128,128,216
K 212,168,169
Mvav 132,132,216
X 171
Mvav 100,132,208
X 172
Mvav 68,132,200
Mvav 36,132,192
K 213,170,171
Mvav 40,136,192
X 173
Mvav 72,136,200
X 174
Mvav 104,136,208
Mvav 136,136,216
s_waitcnt vmcnt(2)
v_cmp_eq_u64 vcc, s[12:13], v[28:29]
s_cmp_eq_u64 vcc, -1
s_cbranch_scc0 .Lspin_74%=
.Lok_75%=:
v_cmp_eq_u64 vcc, s[12:13], v[8:9]
v_cmp_eq_u64_e64 s[86:87], s[12:13], v[10:11]
s_and_b64 vcc, vcc, s[86:87]
s_or_b64 vcc, vcc, s[20:21]
s_cmp_eq_u64 vcc, -1
s_cbranch_scc0 .Lspin_77%=
.Lok_78%=:
K 220,172,173
Mvav 140,140,216
X 175
Mvav 108,140,208
X 176
Mvav 76,140,200
s_waitcnt vmcnt(1) lgkmcnt(0)
s_barrier
Mvav 44,140,192
Ra 128,242,0
K 221,174,175
Mvav 48,144,192
X 177
Mvav 80,144,200
X 178
Mvav 112,144,208
Ra 132,242,1024
Mvav 144,144,216
K 198,176,177
Mvav 148,148,216
X 179
Mvav 116,148,208
X 180
Mvav 84,148,200
Ra 136,242,2048
Mvav 52,148,192
K 199,178,179
Mvav 56,152,192
X 181
Mvav 88,152,200
Ra 140,242,3072
Mvav 120,152,208
X 182
Mvav 152,152,216
K 206,180,181
Mvav 156,156,216
X 183
Mvav 124,156,208
Mvav 92,156,200
K 207,182,183
Mvav 60,156,192
s_mov_b64 exec, 0
s_cmp_eq_u32 s14, 2
s_cbranch_scc1 .Lh_4_2%=
s_branch .Lh_4_3%=
.Lhr_80%=:
s_mov_b64 exec, -1
W 0
Cvaa 160,128,0,224
Ra 144,242,4096
Ra 148,242,5120
Mvaa 160,132,4
s_add_u32 m0, s62, 0
Ra 152,242,6144
Cvaa 164,132,20,228
X 184
Mvaa 164,128,16
X 185
Cvaa 168,128,32,232
X 186
Mvaa 168,132,36
Ra 156,242,7168
K 214,184,185
Cvaa 172,132,52,236
Dc 1,8,61,0
Mvaa 172,128,48
X 187
Mvaa 160,136,8
X 188
Mvaa 160,140,12
Ra 128,242,16384
K 215,186,187
Mvaa 164,140,28
Dc 1,8,61,1024
Mvaa 164,136,24
X 189
Mvaa 168,136,40
X 190
Mvaa 168,140,44
Ra 132,242,17408
K 222,188,189
Mvaa 172,140,60
Dc 1,8,61,2048
Mvaa 172,136,56
X 191
v_smfmac_f32_16x16x64_bf16 a[160:163], a[176:179], v[192:199], v240
Ra 136,242,18432
K 223,190,191
v_smfmac_f32_16x16x64_bf16 a[164:167], a[176:179], v[200:207], v240
X 160
v_smfmac_f32_16x16x64_bf16 a[168:171], a[176:179], v[208:215], v240
Dc 1,8,61,3072
v_smfmac_f32_16x16x64_bf16 a[172:175], a[176:179], v[216:223], v240
W 3
X 161
Cvaa 176,144,0,224
s_add_u32 m0, s62, 4096
X 162
Mvaa 176,148,4
Ra 140,242,19456
K 192,160,161
Cvaa 180,148,20,228
X 163
Mvaa 180,144,16
Dc 243,8,61,0
Cvaa 184,144,32,232
X 164
Mvaa 184,148,36
K 193,162,163
Cvaa 188,148,52,236
X 165
Mvaa 188,144,48
Dc 243,8,61,1024
Ra 144,242,20480
Mvaa 176,152,8
X 166
Mvaa 176,156,12
Ra 148,242,21504
K 200,164,165
Mvaa 180,156,28
X 167
Mvaa 180,152,24
Dc 243,8,61,2048
Mvaa 184,152,40
X 168
Mvaa 184,156,44
K 201,166,167
Mvaa 188,156,60
X 169
Mvaa 188,152,56
W 2
Dc 243,8,61,3072
Ra 152,242,22528
Mvav 32,128,196
s_add_u32 m0, s62, 8192
X 170
Mvav 64,128,204
Ra 156,242,23552
K 208,168,169
Mvav 96,128,212
X 171
Mvav 128,128,220
Dc 244,8,61,0
Mvav 132,132,220
X 172
Mvav 100,132,212
Ra 128,242,8192
K 209,170,171
Mvav 68,132,204
X 173
Mvav 36,132,196
Dc 244,8,61,1024
Mvav 40,136,196
X 174
Mvav 72,136,204
Ra 132,242,9216
K 216,172,173
Mvav 104,136,212
X 175
Mvav 136,136,220
Dc 244,8,61,2048
Mvav 140,140,220
X 176
Mvav 108,140,212
Ra 136,242,10240
K 217,174,175
Mvav 76,140,204
X 177
Mvav 44,140,196
W 3
Dc 244,8,61,3072
Mvav 48,144,196
X 178
Mvav 80,144,204
s_add_u32 m0, s62, 12288
Ra 140,242,11264
Mvav 112,144,212
K 194,176,177
Mvav 144,144,220
X 179
Mvav 148,148,220
X 180
Mvav 116,148,212
Dc 245,8,61,0
Ra 144,242,12288
Mvav 84,148,204
K 195,178,179
Mvav 52,148,196
X 181
Mvav 56,152,196
X 182
Mvav 88,152,204
Dc 245,8,61,1024
Ra 148,242,13312
Mvav 120,152,212
v_xor_b32 v6, s12, v12
K 202,180,181
Mvav 152,152,220
X 183
Mvav 156,156,220
Ra 152,242,14336
v_and_b32 v6, 2047, v6
Mvav 124,156,212
Dc 245,8,61,2048
v_lshlrev_b32 v6, 3, v6
Mvav 92,156,204
X 184
Mvav 60,156,196
W 3
K 203,182,183
Cvaa 160,128,0,224
s_mov_b32 s81, 40960
X 185
Mvaa 160,132,4
Dc 245,8,61,3072
Ra 156,242,15360
Cvaa 164,132,20,228
s_add_u32 s61, s61, s63
X 186
Mvaa 164,128,16
K 210,184,185
Cvaa 168,128,32,232
X 187
Mvaa 168,132,36
buffer_load_dword v7, v6, s[8:11], s81 offen sc1
v_lshrrev_b32 v5, 7, v1
Cvaa 172,132,52,236
s_xor_b32 s62, s62, 0x10000
X 188
Mvaa 172,128,48
K 211,186,187
v_mul_u32_u24 v4, 20480, v5
Mvaa 160,136,8
X 189
Mvaa 160,140,12
s_add_u32 s64, s64, 1
Ra 128,242,24576
Ra 132,242,25600
Mvaa 164,140,28
X 190
Mvaa 164,136,24
v_and_b32 v6, 0x70, v1
K 218,188,189
Mvaa 168,136,40
X 191
Mvaa 168,140,44
v_add_u32 v4, v4, v6
Mvaa 172,140,60
)"
R"(X 160
Mvaa 172,136,56
W 2
v_add_u32 v5, 2, v5
K 219,190,191
Cvaa 176,144,0,224
X 161
Mvaa 176,148,4
Ra 136,242,26624
v_lshrrev_b32 v5, 3, v5
Cvaa 180,148,20,228
Ra 140,242,27648
v_lshl_or_b32 v5, v5, 31, v4
Mvaa 180,144,16
X 162
Cvaa 184,144,32,232
K 196,160,161
Mvaa 184,148,36
s_lshl_b32 s19, s85, 7
X 163
Cvaa 188,148,52,236
X 164
Mvaa 188,144,48
s_add_u32 s19, s19, 2752512
Ra 144,242,28672
K 197,162,163
Mvaa 176,152,8
X 165
Mvaa 176,156,12
s_and_b32 s81, s20, 134217728
Ra 148,242,29696
Mvaa 180,156,28
.Lh_3_2_1%=:
s_cbranch_execz .Lhr_70%=
X 166
Mvaa 180,152,24
K 204,164,165
Mvaa 184,152,40
s_or_b32 s19, s19, s81
X 167
Mvaa 184,156,44
X 168
Mvaa 188,156,60
buffer_load_dwordx4 a[64:67], v4, s[8:11], s19 offen sc1
Mvaa 188,152,56
W 2
Ra 152,242,30720
K 205,166,167
Mvav 32,128,192
s_add_u32 s19, s19, 163840
X 169
Mvav 64,128,200
Ra 156,242,31744
v_xor_b32 v242, 0x10000, v242
Mvav 96,128,208
X 170
Mvav 128,128,216
buffer_load_dwordx4 a[68:71], v4, s[8:11], s19 offen sc1
K 212,168,169
Mvav 132,132,216
X 171
Mvav 100,132,208
s_add_u32 s19, s19, 163840
X 172
Mvav 68,132,200
Mvav 36,132,192
buffer_load_dwordx4 a[72:75], v4, s[8:11], s19 offen sc1
K 213,170,171
Mvav 40,136,192
X 173
Mvav 72,136,200
X 174
Mvav 104,136,208
s_add_u32 s19, s19, 163840
Mvav 136,136,216
K 220,172,173
Mvav 140,140,216
X 175
Mvav 108,140,208
buffer_load_dwordx4 a[76:79], v4, s[8:11], s19 offen sc1
Mvav 76,140,200
s_waitcnt vmcnt(5) lgkmcnt(0)
s_barrier
Mvav 44,140,192
s_add_u32 s19, s19, 163840
X 176
Mvav 48,144,192
Ra 128,242,0
K 221,174,175
Mvav 80,144,200
X 177
Mvav 112,144,208
buffer_load_dwordx4 a[80:83], v4, s[8:11], s19 offen sc1
Ra 132,242,1024
Mvav 144,144,216
s_add_u32 s19, s19, 163840
X 178
Mvav 148,148,216
K 198,176,177
Mvav 116,148,208
X 179
Mvav 84,148,200
buffer_load_dwordx4 a[84:87], v4, s[8:11], s19 offen sc1
Ra 136,242,2048
Mvav 52,148,192
X 180
Mvav 56,152,192
s_add_u32 s19, s19, 163840
Ra 140,242,3072
K 199,178,179
Mvav 88,152,200
X 181
Mvav 120,152,208
X 182
Mvav 152,152,216
buffer_load_dwordx4 a[88:91], v5, s[8:11], s19 offen sc1
K 206,180,181
Mvav 156,156,216
X 183
Mvav 124,156,208
Mvav 92,156,200
K 207,182,183
Mvav 60,156,192
W 0
Cvaa 160,128,0,224
Ra 144,242,4096
Ra 148,242,5120
Mvaa 160,132,4
s_add_u32 m0, s62, 0
Ra 152,242,6144
Cvaa 164,132,20,228
X 184
Mvaa 164,128,16
X 185
Cvaa 168,128,32,232
X 186
Mvaa 168,132,36
Ra 156,242,7168
K 214,184,185
Cvaa 172,132,52,236
Dc 1,8,61,0
Mvaa 172,128,48
X 187
Mvaa 160,136,8
X 188
Mvaa 160,140,12
Ra 128,242,16384
K 215,186,187
Mvaa 164,140,28
Dc 1,8,61,1024
Mvaa 164,136,24
X 189
Mvaa 168,136,40
X 190
Mvaa 168,140,44
Ra 132,242,17408
K 222,188,189
Mvaa 172,140,60
Dc 1,8,61,2048
Mvaa 172,136,56
X 191
v_smfmac_f32_16x16x64_bf16 a[160:163], a[176:179], v[192:199], v240
Ra 136,242,18432
K 223,190,191
v_smfmac_f32_16x16x64_bf16 a[164:167], a[176:179], v[200:207], v240
X 160
v_smfmac_f32_16x16x64_bf16 a[168:171], a[176:179], v[208:215], v240
Dc 1,8,61,3072
v_smfmac_f32_16x16x64_bf16 a[172:175], a[176:179], v[216:223], v240
W 3
X 161
Cvaa 176,144,0,224
s_add_u32 m0, s62, 4096
X 162
Mvaa 176,148,4
Ra 140,242,19456
K 192,160,161
Cvaa 180,148,20,228
X 163
Mvaa 180,144,16
Dc 243,8,61,0
Cvaa 184,144,32,232
X 164
Mvaa 184,148,36
K 193,162,163
Cvaa 188,148,52,236
X 165
Mvaa 188,144,48
Dc 243,8,61,1024
Ra 144,242,20480
Mvaa 176,152,8
X 166
Mvaa 176,156,12
Ra 148,242,21504
K 200,164,165
Mvaa 180,156,28
X 167
Mvaa 180,152,24
Dc 243,8,61,2048
Mvaa 184,152,40
X 168
Mvaa 184,156,44
K 201,166,167
Mvaa 188,156,60
X 169
Mvaa 188,152,56
W 2
Dc 243,8,61,3072
Ra 152,242,22528
Mvav 32,128,196
s_add_u32 m0, s62, 8192
X 170
Mvav 64,128,204
Ra 156,242,23552
K 208,168,169
Mvav 96,128,212
X 171
Mvav 128,128,220
Dc 244,8,61,0
Mvav 132,132,220
X 172
Mvav 100,132,212
Ra 128,242,8192
K 209,170,171
Mvav 68,132,204
X 173
Mvav 36,132,196
Dc 244,8,61,1024
Mvav 40,136,196
X 174
Mvav 72,136,204
Ra 132,242,9216
K 216,172,173
Mvav 104,136,212
X 175
Mvav 136,136,220
Dc 244,8,61,2048
Mvav 140,140,220
s_bfe_u32 s81, s59, 0x10001
X 176
Mvav 108,140,212
s_lshl_b32 s82, s57, 1
Ra 136,242,10240
K 217,174,175
Mvav 76,140,204
s_add_u32 s81, s81, s82
X 177
Mvav 44,140,196
W 3
s_lshl_b32 s83, s81, 2
Dc 244,8,61,3072
Mvav 48,144,196
s_lshr_b32 s81, s59, 2
X 178
Mvav 80,144,204
s_add_u32 m0, s62, 12288
s_lshl_b32 s81, s81, 1
Ra 140,242,11264
Mvav 112,144,212
s_add_u32 s83, s83, s81
K 194,176,177
Mvav 144,144,220
s_and_b32 s81, s59, 1
X 179
Mvav 148,148,220
s_add_u32 s83, s83, s81
X 180
Mvav 116,148,212
s_and_b32 s81, s14, 1
Dc 245,8,61,0
Ra 144,242,12288
Mvav 84,148,204
s_mul_i32 s81, s81, 124
K 195,178,179
Mvav 52,148,196
s_xor_b32 s83, s83, s81
X 181
Mvav 56,152,196
s_mul_i32 s83, s83, 20480
X 182
Mvav 88,152,204
s_mul_i32 s81, s58, 320
Dc 245,8,61,1024
Ra 148,242,13312
Mvav 120,152,212
s_add_u32 s82, s83, s81
K 202,180,181
Mvav 152,152,220
X 183
Mvav 156,156,220
Ra 152,242,14336
v_bfe_u32 v246, v1, 4, 4
Mvav 124,156,212
Dc 245,8,61,2048
v_mul_u32_u24 v246, 1280, v246
Mvav 92,156,204
X 184
Mvav 60,156,196
W 3
v_lshrrev_b32 v247, 8, v1
K 203,182,183
Cvaa 160,128,0,224
v_mul_u32_u24 v247, 104, v247
v_add3_u32 v246, v246, v247, s82
Mvaa 160,132,4
X 185
Cvaa 164,132,20,228
Dc 245,8,61,3072
Ra 156,242,15360
Mvaa 164,128,16
s_add_u32 s61, s61, s63
X 186
Cvaa 168,128,32,232
K 210,184,185
Mvaa 168,132,36
X 187
Cvaa 172,132,52,236
s_xor_b32 s62, s62, 0x10000
X 188
Mvaa 172,128,48
global_load_dword v3, v246, s[16:17]
K 211,186,187
Mvaa 160,136,8
s_lshl_b32 s81, s81, 3
s_add_u32 s83, s83, s81
s_add_u32 s83, s83, 131072
Mvaa 160,140,12
s_add_u32 s64, s64, 1
v_lshrrev_b32 v246, 9, v1
v_mul_u32_u24 v246, 10240, v246
Mvaa 164,140,28
Ra 128,242,24576
v_and_b32 v247, 0x1f0, v1
Mvaa 164,136,24
X 189
Mvaa 168,136,40
Ra 132,242,25600
v_min_u32 v247, 304, v247
Mvaa 168,140,44
X 190
Mvaa 172,140,60
v_lshl_add_u32 v246, v247, 3, v246
K 218,188,189
Mvaa 172,136,56
W 2
buffer_load_dword v3, v246, s[8:11], s83 offen sc1
Ra 136,242,26624
Cvaa 176,144,0,224
X 191
Mvaa 176,148,4
X 160
Cvaa 180,148,20,228
Ra 140,242,27648
K 219,190,191
Mvaa 180,144,16
X 161
Cvaa 184,144,32,232
X 162
Mvaa 184,148,36
K 196,160,161
Cvaa 188,148,52,236
X 163
Mvaa 188,144,48
X 164
Mvaa 176,152,8
Ra 144,242,28672
K 197,162,163
Mvaa 176,156,12
X 165
Mvaa 180,156,28
Ra 148,242,29696
Mvaa 180,152,24
X 166
Mvaa 184,152,40
K 204,164,165
Mvaa 184,156,44
X 167
Mvaa 188,156,60
X 168
Mvaa 188,152,56
W 2
Ra 152,242,30720
K 205,166,167
Mvav 32,128,192
X 169
Mvav 64,128,200
Ra 156,242,31744
v_xor_b32 v242, 0x10000, v242
Mvav 96,128,208
X 170
Mvav 128,128,216
K 212,168,169
Mvav 132,132,216
X 171
Mvav 100,132,208
X 172
Mvav 68,132,200
Mvav 36,132,192
K 213,170,171
Mvav 40,136,192
)"
R"(X 173
Mvav 72,136,200
X 174
Mvav 104,136,208
Mvav 136,136,216
K 220,172,173
Mvav 140,140,216
X 175
Mvav 108,140,208
X 176
Mvav 76,140,200
s_waitcnt vmcnt(2) lgkmcnt(0)
s_barrier
Mvav 44,140,192
Ra 128,242,0
K 221,174,175
Mvav 48,144,192
X 177
Mvav 80,144,200
X 178
Mvav 112,144,208
Ra 132,242,1024
Mvav 144,144,216
K 198,176,177
Mvav 148,148,216
X 179
Mvav 116,148,208
X 180
Mvav 84,148,200
Ra 136,242,2048
Mvav 52,148,192
K 199,178,179
Mvav 56,152,192
X 181
Mvav 88,152,200
Ra 140,242,3072
Mvav 120,152,208
X 182
Mvav 152,152,216
K 206,180,181
Mvav 156,156,216
X 183
Mvav 124,156,208
Mvav 92,156,200
K 207,182,183
Mvav 60,156,192
W 0
Cvaa 160,128,0,224
Ra 144,242,4096
Ra 148,242,5120
Mvaa 160,132,4
s_add_u32 m0, s62, 0
Ra 152,242,6144
G 8,64
Cvaa 164,132,20,228
X 184
Mvaa 164,128,16
X 185
Cvaa 168,128,32,232
X 186
Mvaa 168,132,36
G 9,65
K 214,184,185
Cvaa 172,132,52,236
Ra 156,242,7168
Dc 1,8,61,0
Mvaa 172,128,48
X 187
Mvaa 160,136,8
X 188
Mvaa 160,140,12
Ra 128,242,16384
G 10,66
Mvaa 164,140,28
Dc 1,8,61,1024
K 215,186,187
Mvaa 164,136,24
X 189
Mvaa 168,136,40
Ra 132,242,17408
G 11,67
Mvaa 168,140,44
X 190
Mvaa 172,140,60
Dc 1,8,61,2048
K 222,188,189
Mvaa 172,136,56
X 191
v_smfmac_f32_16x16x64_bf16 a[160:163], a[176:179], v[192:199], v240
G 12,68
K 223,190,191
v_smfmac_f32_16x16x64_bf16 a[164:167], a[176:179], v[200:207], v240
Ra 136,242,18432
v_smfmac_f32_16x16x64_bf16 a[168:171], a[176:179], v[208:215], v240
Dc 1,8,61,3072
G 13,69
v_smfmac_f32_16x16x64_bf16 a[172:175], a[176:179], v[216:223], v240
W 3
X 160
Cvaa 176,144,0,224
s_add_u32 m0, s62, 4096
X 161
Mvaa 176,148,4
X 162
Cvaa 180,148,20,228
Ra 140,242,19456
K 192,160,161
Mvaa 180,144,16
Dc 243,8,61,0
G 14,70
Cvaa 184,144,32,232
X 163
Mvaa 184,148,36
X 164
Cvaa 188,148,52,236
G 15,71
K 193,162,163
Mvaa 188,144,48
Dc 243,8,61,1024
Ra 144,242,20480
Mvaa 176,152,8
X 165
Mvaa 176,156,12
X 166
Mvaa 180,156,28
Ra 148,242,21504
A 8,8,12
Mvaa 180,152,24
Dc 243,8,61,2048
K 200,164,165
Mvaa 184,152,40
X 167
Mvaa 184,156,44
A 9,9,13
Mvaa 188,156,60
X 168
Mvaa 188,152,56
W 2
Dc 243,8,61,3072
K 201,166,167
Mvav 32,128,196
s_add_u32 m0, s62, 8192
X 169
Mvav 64,128,204
Ra 152,242,22528
A 10,10,14
Mvav 96,128,212
X 170
Mvav 128,128,220
Ra 156,242,23552
K 208,168,169
Mvav 132,132,220
Dc 244,8,61,0
A 11,11,15
Mvav 100,132,212
X 171
Mvav 68,132,204
X 172
Mvav 36,132,196
Ra 128,242,8192
K 209,170,171
Mvav 40,136,196
Dc 244,8,61,1024
G 12,72
Mvav 72,136,204
X 173
Mvav 104,136,212
X 174
Mvav 136,136,220
Ra 132,242,9216
G 13,73
Mvav 140,140,220
Dc 244,8,61,2048
K 216,172,173
Mvav 108,140,212
X 175
Mvav 76,140,204
X 176
Mvav 44,140,196
W 2
Ra 136,242,10240
G 14,74
Mvav 48,144,196
Dc 244,8,61,3072
K 217,174,175
Mvav 80,144,204
s_add_u32 m0, s62, 12288
X 177
Mvav 112,144,212
Ra 140,242,11264
G 15,75
Mvav 144,144,220
X 178
Mvav 148,148,220
K 194,176,177
A 8,8,12
Mvav 116,148,212
X 179
Mvav 84,148,204
X 180
Mvav 52,148,196
Dc 245,8,61,0
Ra 144,242,12288
Mvav 56,152,196
K 195,178,179
A 9,9,13
Mvav 88,152,204
X 181
Mvav 120,152,212
X 182
Mvav 152,152,220
Dc 245,8,61,1024
Ra 148,242,13312
Mvav 156,156,220
K 202,180,181
A 10,10,14
Mvav 124,156,212
X 183
Mvav 92,156,204
X 184
Mvav 60,156,196
W 2
Dc 245,8,61,2048
Ra 152,242,14336
Cvaa 160,128,0,224
.Lh_4_2_1%=:
s_cbranch_execz .Lh_4_2_2%=
A 11,11,15
K 203,182,183
Mvaa 160,132,4
X 185
Cvaa 164,132,20,228
X 186
Mvaa 164,128,16
Dc 245,8,61,3072
Ra 156,242,15360
Cvaa 168,128,32,232
s_add_u32 s61, s61, s63
G 12,76
K 210,184,185
Mvaa 168,132,36
X 187
Cvaa 172,132,52,236
s_xor_b32 s62, s62, 0x10000
G 13,77
Mvaa 172,128,48
X 188
Mvaa 160,136,8
K 211,186,187
G 14,78
Mvaa 160,140,12
s_add_u32 s64, s64, 1
X 189
Mvaa 164,140,28
Ra 128,242,24576
Ra 132,242,25600
Mvaa 164,136,24
X 190
Mvaa 168,136,40
K 218,188,189
G 15,79
Mvaa 168,140,44
X 191
Mvaa 172,140,60
s_lshl_b32 s92, s58, 2
X 160
Mvaa 172,136,56
W 2
K 219,190,191
A 8,8,12
Cvaa 176,144,0,224
X 161
Mvaa 176,148,4
Ra 136,242,26624
Ra 140,242,27648
Cvaa 180,148,20,228
s_add_u32 s91, s92, s51
X 162
Mvaa 180,144,16
A 9,9,13
K 196,160,161
Cvaa 184,144,32,232
X 163
Mvaa 184,148,36
s_add_u32 s95, s92, 0
X 164
Cvaa 188,148,52,236
A 10,10,14
K 197,162,163
Mvaa 188,144,48
X 165
Mvaa 176,152,8
s_and_b32 s95, s95, 15
Ra 144,242,28672
A 11,11,15
Mvaa 176,156,12
Ra 148,242,29696
Mvaa 180,156,28
X 166
Mvaa 180,152,24
s_mul_i32 s95, s95, 40960
K 204,164,165
Mvaa 184,152,40
X 167
Mvaa 184,156,44
G 12,80
Mvaa 188,156,60
s_mul_i32 s71, s51, 0
X 168
Mvaa 188,152,56
W 2
K 205,166,167
G 13,81
Mvav 32,128,192
X 169
Mvav 64,128,200
s_add_u32 s71, s71, 133120
Ra 152,242,30720
Ra 156,242,31744
Mvav 96,128,208
X 170
Mvav 128,128,216
v_xor_b32 v242, 0x10000, v242
G 14,82
Mvav 132,132,216
s_add_u32 m0, s71, s48
K 212,168,169
Mvav 100,132,208
X 171
Mvav 68,132,200
X 172
Mvav 36,132,192
s_lshl2_add_u32 s94, s48, s95
G 15,83
K 213,170,171
Mvav 40,136,192
X 173
Mvav 72,136,200
X 174
Mvav 104,136,208
D 16,40,94,0
A 8,8,12
Mvav 136,136,216
K 220,172,173
Mvav 140,140,216
X 175
Mvav 108,140,208
A 9,9,13
Mvav 76,140,200
s_waitcnt vmcnt(1) lgkmcnt(0)
s_barrier
Mvav 44,140,192
D 17,40,94,1024
Ra 128,242,0
Mvav 48,144,192
X 176
Mvav 80,144,200
K 221,174,175
A 10,10,14
Mvav 112,144,208
X 177
Mvav 144,144,216
D 18,40,94,2048
Ra 132,242,1024
Mvav 148,148,216
X 178
Mvav 116,148,208
K 198,176,177
A 11,11,15
Mvav 84,148,200
X 179
Mvav 52,148,192
D 19,40,94,3072
Ra 136,242,2048
Mvav 56,152,192
s_add_u32 m0, m0, 4096
X 180
Mvav 88,152,200
G 12,84
K 199,178,179
Mvav 120,152,208
X 181
Mvav 152,152,216
Ra 140,242,3072
buffer_load_dwordx4 v20, s[40:43], s94 offen lds
Mvav 156,156,216
X 182
Mvav 124,156,208
K 206,180,181
Mvav 92,156,200
X 183
Mvav 60,156,192
K 207,182,183
W 0
Cvaa 160,128,0,224
Ra 144,242,4096
Ra 148,242,5120
Mvaa 160,132,4
s_add_u32 m0, s62, 0
Ra 152,242,6144
)"
R"(G 13,85
Cvaa 164,132,20,228
X 184
Mvaa 164,128,16
X 185
Cvaa 168,128,32,232
X 186
Mvaa 168,132,36
G 14,86
K 214,184,185
Cvaa 172,132,52,236
Ra 156,242,7168
Dc 1,8,61,0
Mvaa 172,128,48
X 187
Mvaa 160,136,8
X 188
Mvaa 160,140,12
Ra 128,242,16384
G 15,87
Mvaa 164,140,28
Dc 1,8,61,1024
K 215,186,187
Mvaa 164,136,24
X 189
Mvaa 168,136,40
Ra 132,242,17408
A 8,8,12
Mvaa 168,140,44
X 190
Mvaa 172,140,60
Dc 1,8,61,2048
K 222,188,189
Mvaa 172,136,56
X 191
v_smfmac_f32_16x16x64_bf16 a[160:163], a[176:179], v[192:199], v240
A 9,9,13
K 223,190,191
v_smfmac_f32_16x16x64_bf16 a[164:167], a[176:179], v[200:207], v240
Ra 136,242,18432
A 10,10,14
v_smfmac_f32_16x16x64_bf16 a[168:171], a[176:179], v[208:215], v240
Dc 1,8,61,3072
v_smfmac_f32_16x16x64_bf16 a[172:175], a[176:179], v[216:223], v240
W 3
X 160
Cvaa 176,144,0,224
s_add_u32 m0, s62, 4096
X 161
Mvaa 176,148,4
X 162
Cvaa 180,148,20,228
Ra 140,242,19456
A 11,11,15
Mvaa 180,144,16
Dc 243,8,61,0
K 192,160,161
Cvaa 184,144,32,232
X 163
Mvaa 184,148,36
X 164
Cvaa 188,148,52,236
G 12,88
K 193,162,163
Mvaa 188,144,48
Dc 243,8,61,1024
G 13,89
Mvaa 176,152,8
X 165
Mvaa 176,156,12
X 166
Mvaa 180,156,28
Ra 144,242,20480
Ra 148,242,21504
Mvaa 180,152,24
Dc 243,8,61,2048
K 200,164,165
Mvaa 184,152,40
X 167
Mvaa 184,156,44
G 14,90
Mvaa 188,156,60
X 168
Mvaa 188,152,56
W 2
Dc 243,8,61,3072
K 201,166,167
Mvav 32,128,196
s_add_u32 m0, s62, 8192
Ra 152,242,22528
G 15,91
Mvav 64,128,204
X 169
Mvav 96,128,212
X 170
Mvav 128,128,220
Ra 156,242,23552
A 8,8,12
Mvav 132,132,220
Dc 244,8,61,0
K 208,168,169
Mvav 100,132,212
X 171
Mvav 68,132,204
X 172
Mvav 36,132,196
Ra 128,242,8192
A 9,9,13
Mvav 40,136,196
Dc 244,8,61,1024
K 209,170,171
Mvav 72,136,204
X 173
Mvav 104,136,212
Ra 132,242,9216
A 10,10,14
Mvav 136,136,220
X 174
Mvav 140,140,220
Dc 244,8,61,2048
K 216,172,173
Mvav 108,140,212
X 175
Mvav 76,140,204
Ra 136,242,10240
A 11,11,15
Mvav 44,140,196
W 3
X 176
Mvav 48,144,196
Dc 244,8,61,3072
K 217,174,175
Mvav 80,144,204
s_add_u32 m0, s62, 12288
Ra 140,242,11264
v_add_f32_dpp v8, v8, v8 row_ror:8 row_mask:0xf bank_mask:0xf
Mvav 112,144,212
X 177
Mvav 144,144,220
X 178
Mvav 148,148,220
K 194,176,177
v_add_f32_dpp v9, v9, v9 row_ror:8 row_mask:0xf bank_mask:0xf
Mvav 116,148,212
X 179
Mvav 84,148,204
X 180
Mvav 52,148,196
Dc 245,8,61,0
Ra 144,242,12288
Mvav 56,152,196
v_add_f32_dpp v10, v10, v10 row_ror:8 row_mask:0xf bank_mask:0xf
K 195,178,179
Mvav 88,152,204
X 181
Mvav 120,152,212
X 182
Mvav 152,152,220
Dc 245,8,61,1024
v_add_f32_dpp v11, v11, v11 row_ror:8 row_mask:0xf bank_mask:0xf
Mvav 156,156,220
Ra 148,242,13312
K 202,180,181
Mvav 124,156,212
X 183
Mvav 92,156,204
Ra 152,242,14336
v_mov_b32 v12, v8
Mvav 60,156,196
W 3
Dc 245,8,61,2048
v_mov_b32 v13, v9
Cvaa 160,128,0,224
X 184
Mvaa 160,132,4
Ra 156,242,15360
K 203,182,183
Cvaa 164,132,20,228
X 185
Mvaa 164,128,16
Dc 245,8,61,3072
v_mov_b32 v14, v10
Cvaa 168,128,32,232
s_add_u32 s61, s61, s63
X 186
Mvaa 168,132,36
K 210,184,185
v_mov_b32 v15, v11
Cvaa 172,132,52,236
s_xor_b32 s62, s62, 0x10000
X 187
Mvaa 172,128,48
X 188
Mvaa 160,136,8
Ra 128,242,24576
K 211,186,187
Mvaa 160,140,12
s_add_u32 s64, s64, 1
X 189
Mvaa 164,140,28
X 190
Mvaa 164,136,24
Ra 132,242,25600
v_permlane16_swap_b32 v8, v12
Mvaa 168,136,40
K 218,188,189
v_permlane16_swap_b32 v9, v13
Mvaa 168,140,44
X 191
Mvaa 172,140,60
s_lshl_b32 s92, s58, 2
X 160
Mvaa 172,136,56
W 2
K 219,190,191
v_permlane16_swap_b32 v10, v14
Cvaa 176,144,0,224
X 161
Mvaa 176,148,4
Ra 136,242,26624
Ra 140,242,27648
Cvaa 180,148,20,228
s_add_u32 s91, s92, s51
X 162
Mvaa 180,144,16
K 196,160,161
v_permlane16_swap_b32 v11, v15
Cvaa 184,144,32,232
X 163
Mvaa 184,148,36
s_add_u32 s95, s92, 1
X 164
Cvaa 188,148,52,236
A 8,8,12
K 197,162,163
Mvaa 188,144,48
X 165
Mvaa 176,152,8
s_and_b32 s95, s95, 15
Ra 144,242,28672
Ra 148,242,29696
Mvaa 176,156,12
X 166
Mvaa 180,156,28
A 9,9,13
K 204,164,165
Mvaa 180,152,24
s_mul_i32 s95, s95, 40960
X 167
Mvaa 184,152,40
Mvaa 184,156,44
A 10,10,14
Mvaa 188,156,60
s_mul_i32 s71, s51, 0
X 168
Mvaa 188,152,56
W 2
Ra 152,242,30720
K 205,166,167
Mvav 32,128,192
X 169
Mvav 64,128,200
s_add_u32 s71, s71, 143360
Ra 156,242,31744
A 11,11,15
Mvav 96,128,208
X 170
Mvav 128,128,216
v_xor_b32 v242, 0x10000, v242
K 212,168,169
Mvav 132,132,216
s_add_u32 m0, s71, s48
v_mov_b32 v12, v8
Mvav 100,132,208
X 171
Mvav 68,132,200
X 172
Mvav 36,132,192
s_lshl2_add_u32 s94, s48, s95
v_mov_b32 v13, v9
K 213,170,171
Mvav 40,136,192
X 173
Mvav 72,136,200
X 174
Mvav 104,136,208
D 16,40,94,0
v_mov_b32 v14, v10
Mvav 136,136,216
K 220,172,173
Mvav 140,140,216
X 175
Mvav 108,140,208
v_mov_b32 v15, v11
Mvav 76,140,200
s_waitcnt vmcnt(1) lgkmcnt(0)
s_barrier
Mvav 44,140,192
D 17,40,94,1024
Ra 128,242,0
Mvav 48,144,192
X 176
Mvav 80,144,200
K 221,174,175
v_permlane32_swap_b32 v8, v12
Mvav 112,144,208
X 177
Mvav 144,144,216
D 18,40,94,2048
Ra 132,242,1024
Mvav 148,148,216
X 178
Mvav 116,148,208
K 198,176,177
v_permlane32_swap_b32 v9, v13
Mvav 84,148,200
X 179
Mvav 52,148,192
D 19,40,94,3072
Ra 136,242,2048
Mvav 56,152,192
s_add_u32 m0, m0, 4096
X 180
Mvav 88,152,200
K 199,178,179
v_permlane32_swap_b32 v10, v14
Mvav 120,152,208
X 181
Mvav 152,152,216
Ra 140,242,3072
buffer_load_dwordx4 v20, s[40:43], s94 offen lds
Mvav 156,156,216
X 182
Mvav 124,156,208
K 206,180,181
Mvav 92,156,200
X 183
Mvav 60,156,192
K 207,182,183
W 0
Cvaa 160,128,0,224
Ra 144,242,4096
Ra 148,242,5120
Mvaa 160,132,4
s_add_u32 m0, s62, 0
Ra 152,242,6144
v_permlane32_swap_b32 v11, v15
Cvaa 164,132,20,228
X 184
Mvaa 164,128,16
X 185
Cvaa 168,128,32,232
X 186
Mvaa 168,132,36
A 8,8,12
K 214,184,185
Cvaa 172,132,52,236
Ra 156,242,7168
Dc 1,8,61,0
Mvaa 172,128,48
X 187
Mvaa 160,136,8
Ra 128,242,16384
A 9,9,13
Mvaa 160,140,12
X 188
Mvaa 164,140,28
Dc 1,8,61,1024
K 215,186,187
Mvaa 164,136,24
X 189
Mvaa 168,136,40
Ra 132,242,17408
A 10,10,14
Mvaa 168,140,44
X 190
Mvaa 172,140,60
Dc 1,8,61,2048
K 222,188,189
Mvaa 172,136,56
A 11,11,15
v_smfmac_f32_16x16x64_bf16 a[160:163], a[176:179], v[192:199], v240
X 191
v_smfmac_f32_16x16x64_bf16 a[164:167], a[176:179], v[200:207], v240
K 223,190,191
v_and_b32 v7, 0xffff0000, v7
v_smfmac_f32_16x16x64_bf16 a[168:171], a[176:179], v[208:215], v240
Dc 1,8,61,3072
Ra 136,242,18432
v_smfmac_f32_16x16x64_bf16 a[172:175], a[176:179], v[216:223], v240
W 3
X 160
Cvaa 176,144,0,224
s_add_u32 m0, s62, 4096
X 161
Mvaa 176,148,4
X 162
Cvaa 180,148,20,228
Ra 140,242,19456
v_ashrrev_i32 v6, 31, v7
)"
R"(Mvaa 180,144,16
Dc 243,8,61,0
K 192,160,161
Cvaa 184,144,32,232
X 163
Mvaa 184,148,36
v_not_b32 v6, v6
Cvaa 188,148,52,236
X 164
Mvaa 188,144,48
Dc 243,8,61,1024
K 193,162,163
Mvaa 176,152,8
X 165
Mvaa 176,156,12
Ra 144,242,20480
v_or_b32 v6, 0x80000000, v6
Mvaa 180,156,28
X 166
Mvaa 180,152,24
Ra 148,242,21504
v_xor_b32 v7, v7, v6
Mvaa 184,152,40
Dc 243,8,61,2048
K 200,164,165
Mvaa 184,156,44
X 167
Mvaa 188,156,60
X 168
Mvaa 188,152,56
W 2
v_and_b32 v7, 0xffff0000, v7
K 201,166,167
Mvav 32,128,196
.Lh_4_3_1%=:
s_cbranch_execz .Lhr_80%=
Dc 243,8,61,3072
v_mul_f32 v7, 0xbfb8aa3b, v7
Mvav 64,128,204
s_add_u32 m0, s62, 8192
X 169
Mvav 96,128,212
Ra 152,242,22528
Ra 156,242,23552
Mvav 128,128,220
X 170
Mvav 132,132,220
Dc 244,8,61,0
K 208,168,169
Mvav 100,132,212
X 171
Mvav 68,132,204
X 7
Mvav 36,132,196
X 172
Mvav 40,136,196
Ra 128,242,8192
K 209,170,171
Mvav 72,136,204
X 173
Mvav 104,136,212
Dc 244,8,61,1024
Ra 132,242,9216
Mvav 136,136,220
X 174
Mvav 140,140,220
v_add_f32 v7, 1.0, v7
K 216,172,173
Mvav 108,140,212
X 175
Mvav 76,140,204
Dc 244,8,61,2048
Ra 136,242,10240
Mvav 44,140,196
W 3
v_rcp_f32 v7, v7
Mvav 48,144,196
X 176
Mvav 80,144,204
Ra 140,242,11264
K 217,174,175
Mvav 112,144,212
Dc 244,8,61,3072
K 7,7,7
Mvav 144,144,220
s_add_u32 m0, s62, 12288
X 177
Mvav 148,148,220
X 178
Mvav 116,148,212
Ra 144,242,12288
K 194,176,177
Mvav 84,148,204
X 179
Mvav 52,148,196
X 180
Mvav 56,152,196
Dc 245,8,61,0
v_lshlrev_b32 v7, 16, v7
Mvav 88,152,204
K 195,178,179
K 12,8,9
Mvav 120,152,212
X 181
Mvav 152,152,220
X 182
Mvav 156,156,220
Dc 245,8,61,1024
Ra 148,242,13312
Mvav 124,156,212
K 202,180,181
K 13,10,11
Mvav 92,156,204
X 183
Mvav 60,156,196
W 2
X 184
Cvaa 160,128,0,224
Dc 245,8,61,2048
Ra 152,242,14336
Mvaa 160,132,4
K 203,182,183
v_lshlrev_b32 v8, 16, v12
Cvaa 164,132,20,228
X 185
Mvaa 164,128,16
X 186
Cvaa 168,128,32,232
Dc 245,8,61,3072
Ra 156,242,15360
Mvaa 168,132,36
s_add_u32 s61, s61, s63
v_and_b32 v9, 0xffff0000, v12
K 210,184,185
Cvaa 172,132,52,236
s_xor_b32 s62, s62, 0x10000
X 187
Mvaa 172,128,48
X 188
Mvaa 160,136,8
v_lshlrev_b32 v10, 16, v13
K 211,186,187
Mvaa 160,140,12
s_add_u32 s64, s64, 1
X 189
Mvaa 164,140,28
Ra 128,242,24576
v_and_b32 v11, 0xffff0000, v13
Mvaa 164,136,24
X 190
Mvaa 168,136,40
Ra 132,242,25600
K 218,188,189
Mvaa 168,140,44
X 191
Mvaa 172,140,60
s_lshl_b32 s92, s58, 2
U 8,8,7
U 9,9,7
Mvaa 172,136,56
W 2
X 160
Cvaa 176,144,0,224
Ra 136,242,26624
K 219,190,191
Mvaa 176,148,4
X 161
Cvaa 180,148,20,228
s_add_u32 s91, s92, s51
Ra 140,242,27648
U 10,10,7
Mvaa 180,144,16
X 162
Cvaa 184,144,32,232
K 196,160,161
Mvaa 184,148,36
s_add_u32 s95, s92, 2
X 163
Cvaa 188,148,52,236
U 11,11,7
Mvaa 188,144,48
X 164
Mvaa 176,152,8
s_and_b32 s95, s95, 15
Ra 144,242,28672
K 197,162,163
Mvaa 176,156,12
X 165
Mvaa 180,156,28
Ra 148,242,29696
K 12,8,9
Mvaa 180,152,24
s_mul_i32 s95, s95, 40960
X 166
Mvaa 184,152,40
K 204,164,165
K 14,10,11
Mvaa 184,156,44
X 167
Mvaa 188,156,60
s_mul_i32 s71, s51, 0
X 168
Mvaa 188,152,56
W 2
v_mov_b32 v13, s13
K 205,166,167
Mvav 32,128,192
X 169
Mvav 64,128,200
s_add_u32 s71, s71, 153600
Ra 152,242,30720
Ra 156,242,31744
Mvav 96,128,208
X 170
Mvav 128,128,216
v_mov_b32 v15, s13
v_xor_b32 v242, 0x10000, v242
Mvav 132,132,216
s_add_u32 m0, s71, s48
K 212,168,169
v_lshrrev_b32 v6, 7, v1
Mvav 100,132,208
X 171
Mvav 68,132,200
X 172
Mvav 36,132,192
s_lshl2_add_u32 s94, s48, s95
K 213,170,171
Mvav 40,136,192
X 173
Mvav 72,136,200
v_lshl_or_b32 v6, v6, 28, v1
Mvav 104,136,208
X 174
Mvav 136,136,216
s_lshl_b32 s19, s85, 7
D 16,40,94,0
K 220,172,173
Mvav 140,140,216
X 175
Mvav 108,140,208
X 176
Mvav 76,140,200
s_waitcnt vmcnt(1) lgkmcnt(0)
s_barrier
Mvav 44,140,192
s_add_u32 s19, s19, 98304
D 17,40,94,1024
K 221,174,175
Mvav 48,144,192
Ra 128,242,0
Mvav 80,144,200
s_and_b32 s21, s20, 134217728
X 177
Mvav 112,144,208
X 178
Mvav 144,144,216
D 18,40,94,2048
Ra 132,242,1024
Mvav 148,148,216
s_or_b32 s19, s19, s21
K 198,176,177
Mvav 116,148,208
X 179
Mvav 84,148,200
X 180
Mvav 52,148,192
D 19,40,94,3072
Ra 136,242,2048
Mvav 56,152,192
s_add_u32 m0, m0, 4096
Ra 140,242,3072
K 199,178,179
Mvav 88,152,200
X 181
Mvav 120,152,208
X 182
Mvav 152,152,216
buffer_store_dwordx4 v[12:15], v6, s[8:11], s19 offen sc1
K 206,180,181
Mvav 156,156,216
X 183
Mvav 124,156,208
Mvav 92,156,200
K 207,182,183
Mvav 60,156,192
buffer_load_dwordx4 v20, s[40:43], s94 offen lds
.Lf1_40%=:
W 0
Cvaa 160,128,0,224
v_bfe_u32 v28, v1, 4, 2
v_lshlrev_b32 v28, 3, v28
Mvaa 160,132,4
Ra 144,242,4096
Ra 148,242,5120
Cvaa 164,132,20,228
Ra 152,242,6144
buffer_load_dwordx2 v[28:29], v28, s[8:11], s54 offen sc1
Mvaa 164,128,16
s_lshl_b32 s92, s58, 2
s_add_u32 s91, s92, s51
s_add_u32 s95, s91, 3
Cvaa 168,128,32,232
s_and_b32 s95, s95, 15
s_mul_i32 s95, s95, 40960
s_mul_i32 s71, s51, 10240
Mvaa 168,132,36
s_add_u32 s71, s71, 0
s_add_u32 m0, s71, s48
s_lshl2_add_u32 s94, s48, s95
Cvaa 172,132,52,236
X 184
Mvaa 172,128,48
X 185
Mvaa 160,136,8
X 186
Mvaa 160,140,12
Ra 156,242,7168
K 214,184,185
Mvaa 164,140,28
X 187
Mvaa 164,136,24
D 16,40,94,0
Ra 128,242,16384
Mvaa 168,136,40
s_add_u32 s95, s91, 5
s_and_b32 s95, s95, 15
s_mul_i32 s95, s95, 40960
Mvaa 168,140,44
s_mul_i32 s71, s51, 10240
s_add_u32 s71, s71, 20480
Ra 132,242,17408
Mvaa 172,140,60
X 188
Mvaa 172,136,56
D 17,40,94,1024
K 215,186,187
v_smfmac_f32_16x16x64_bf16 a[160:163], a[176:179], v[192:199], v240
X 189
v_smfmac_f32_16x16x64_bf16 a[164:167], a[176:179], v[200:207], v240
X 190
K 222,188,189
X 191
s_nop 0
K 223,190,191
v_smfmac_f32_16x16x64_bf16 a[168:171], a[176:179], v[208:215], v240
D 18,40,94,2048
Ra 136,242,18432
v_smfmac_f32_16x16x64_bf16 a[172:175], a[176:179], v[216:223], v240
W 3
X 160
Cvaa 176,144,0,224
X 161
Mvaa 176,148,4
X 162
Cvaa 180,148,20,228
D 19,40,94,3072
K 192,160,161
Mvaa 180,144,16
s_add_u32 m0, m0, 4096
X 163
Cvaa 184,144,32,232
X 164
Mvaa 184,148,36
Ra 140,242,19456
K 193,162,163
Cvaa 188,148,52,236
buffer_load_dwordx4 v20, s[40:43], s94 offen lds
Mvaa 188,144,48
s_add_u32 m0, s71, s48
s_lshl2_add_u32 s94, s48, s95
Ra 144,242,20480
Mvaa 176,152,8
X 165
Mvaa 176,156,12
X 166
Mvaa 180,156,28
D 16,40,94,0
K 200,164,165
Mvaa 180,152,24
X 167
Mvaa 184,152,40
X 172
Mvaa 184,156,44
D 17,40,94,1024
K 201,166,167
Mvaa 188,156,60
X 173
Mvaa 188,152,56
s_cmp_eq_u32 s60, 16
s_cbranch_scc1 .Lmk_81%=
.Lmkr_82%=:
W 1
Mvav 32,128,196
Ra 148,242,21504
D 18,40,94,2048
Mvav 64,128,204
X 174
Mvav 96,128,212
Ra 152,242,22528
)"
R"(K 216,172,173
Mvav 128,128,220
X 175
Mvav 132,132,220
s_mov_b32 s18, 40960
X 168
Mvav 100,132,212
D 19,40,94,3072
K 217,174,175
Mvav 68,132,204
s_add_u32 m0, m0, 4096
X 169
Mvav 36,132,196
s_mov_b32 s19, 73728
X 170
Mvav 40,136,196
s_bitcmp1_b32 s14, 0
s_cselect_b32 s18, 49152, s18
s_cselect_b32 s19, 98304, s19
Ra 156,242,23552
K 208,168,169
Mvav 72,136,204
s_cmp_lt_u32 s14, 2
s_cselect_b32 s20, s18, s19
s_cselect_b32 s21, s19, s18
X 171
Mvav 104,136,212
X 184
Mvav 136,136,220
buffer_load_dwordx4 v20, s[40:43], s94 offen lds
K 209,170,171
Mvav 140,140,220
s_waitcnt vmcnt(10)
v_cmp_eq_u64 vcc, s[12:13], v[28:29]
s_cmp_eq_u64 vcc, -1
s_cselect_b32 s46, 79691776, 0
s_add_u32 s81, s58, 0
s_and_b32 s81, s81, 3
Mvav 108,140,212
s_lshl_b32 s81, s81, 12
s_add_u32 s81, s81, s49
Ra 128,242,24576
Mvav 76,140,204
X 185
Mvav 44,140,196
W 1
X 186
Mvav 48,144,196
buffer_load_dwordx4 a[184:187], v1, s[44:47], s81 offen offset:0 sc1
K 210,184,185
Mvav 80,144,204
s_add_u32 s82, s81, 131072
X 187
Mvav 112,144,212
v_add_u32 v2, s20, v1
v_add_u32 v3, s21, v1
Mvav 144,144,220
s_lshl_b32 s18, s14, 9
X 188
Mvav 148,148,220
Ra 132,242,25600
K 211,186,187
Mvav 116,148,212
buffer_load_dwordx4 a[200:203], v1, s[44:47], s82 offen offset:0 sc1
Ra 136,242,26624
Mvav 84,148,204
X 189
Mvav 52,148,196
v_lshrrev_b32 v232, 1, v1
v_add_u32 v232, s18, v232
Mvav 56,152,196
X 190
Mvav 88,152,204
K 218,188,189
v_bfe_u32 v12, v1, 6, 2
Mvav 120,152,212
buffer_load_dwordx4 a[188:191], v1, s[44:47], s81 offen offset:1024 sc1
Ra 140,242,27648
Mvav 152,152,220
X 191
Mvav 156,156,220
v_lshrrev_b32 v13, 8, v1
v_bfe_u32 v14, v1, 4, 1
Mvav 124,156,212
X 176
Mvav 92,156,204
K 219,190,191
v_lshlrev_b32 v14, 4, v14
Mvav 60,156,196
X 177
Maav 168,180,208
X 178
K 194,176,177
X 179
X 180
K 195,178,179
Maav 172,180,216
X 181
X 182
K 202,180,181
X 183
s_nop 0
K 203,182,183
Maav 160,180,192
buffer_load_dwordx4 a[204:207], v1, s[44:47], s82 offen offset:1024 sc1
v_mov_b32 v15, 0x3f80
Maav 164,180,200
W 0
Ra 144,242,28672
v_lshlrev_b32 v15, v14, v15
Mvav 96,128,208
Ra 148,242,29696
v_bfe_u32 v14, v1, 5, 1
v_xor_b32 v27, v12, v13
Mvav 128,128,216
buffer_load_dwordx4 a[192:195], v1, s[44:47], s81 offen offset:2048 sc1
v_lshl_or_b32 v27, v27, 1, v14
v_cmp_eq_u32_e64 s[86:87], 0, v27
Mvav 132,132,216
Ra 152,242,30720
Ra 156,242,31744
v_mov_b32 v226, 0
Mvav 100,132,208
v_cndmask_b32_e64 v224, 0, v15, s[86:87]
v_mov_b32 v228, 0
v_mov_b32 v230, v224
Mvav 104,136,208
buffer_load_dwordx4 a[208:211], v1, s[44:47], s82 offen offset:2048 sc1
v_cmp_eq_u32_e64 s[86:87], 1, v27
v_mov_b32 v227, 0
Mvav 136,136,216
v_cndmask_b32_e64 v225, 0, v15, s[86:87]
v_mov_b32 v229, 0
v_mov_b32 v231, v225
Mvav 140,140,216
G 4,160
G 8,161
G 5,164
Mvav 108,140,208
W 0
buffer_load_dwordx4 a[196:199], v1, s[44:47], s81 offen offset:3072 sc1
G 9,165
G 6,168
Mvav 112,144,208
G 10,169
G 7,172
G 11,173
Mvav 144,144,216
A 4,4,8
A 5,5,9
A 6,6,10
Mvav 148,148,216
buffer_load_dwordx4 a[212:215], v1, s[44:47], s82 offen offset:3072 sc1
s_add_u32 s81, s58, 1
s_and_b32 s81, s81, 3
s_lshl_b32 s81, s81, 12
Mvav 116,148,208
s_add_u32 s81, s81, s49
A 7,7,11
ds_write_b64 v232, v[6:7] offset:57344
A 8,4,5
Mvav 120,152,208
A 9,6,7
A 8,8,9
v_cmp_ngt_f32_e64 s[86:87], s90, v8
s_lshl_b32 s19, s14, 2
Mvav 152,152,216
buffer_load_dwordx4 a[216:219], v1, s[44:47], s81 offen offset:0 sc1
s_add_u32 s82, s81, 131072
v_mov_b32 v9, s19
s_or_b64 s[88:89], s[88:89], s[86:87]
Mvav 156,156,216
s_and_b64 s[88:89], s[88:89], s[24:25]
s_cselect_b32 s18, 1, 0
v_mov_b32 v8, s18
ds_write_b32 v9, v8 offset:59392
v_xor_b32 v10, 1024, v232
Mvav 124,156,208
v_bfe_u32 v11, v1, 2, 4
K 96,96,97
K 97,98,99
Mvav 32,128,192
buffer_load_dwordx4 a[232:235], v1, s[44:47], s82 offen offset:0 sc1
K 98,100,101
K 99,102,103
Mvav 64,128,200
ds_write_b128 v2, v[96:99] offset:0
K 128,128,129
K 129,130,131
Mvav 68,132,200
K 130,132,133
K 131,134,135
ds_write_b128 v2, v[128:131] offset:4096
Mvav 36,132,192
buffer_load_dwordx4 a[220:223], v1, s[44:47], s81 offen offset:1024 sc1
K 104,104,105
K 105,106,107
Mvav 40,136,192
K 106,108,109
K 107,110,111
ds_write_b128 v2, v[104:107] offset:1024
Mvav 72,136,200
K 136,136,137
K 137,138,139
K 138,140,141
Mvav 76,140,200
buffer_load_dwordx4 a[236:239], v1, s[44:47], s82 offen offset:1024 sc1
K 139,142,143
ds_write_b128 v2, v[136:139] offset:5120
Mvav 44,140,192
K 112,112,113
K 113,114,115
K 114,116,117
Mvav 48,144,192
K 115,118,119
ds_write_b128 v2, v[112:115] offset:2048
K 144,144,145
Mvav 80,144,200
K 145,146,147
K 146,148,149
K 147,150,151
Mvav 84,148,200
ds_write_b128 v2, v[144:147] offset:6144
buffer_load_dwordx4 a[224:227], v1, s[44:47], s81 offen offset:2048 sc1
K 120,120,121
Mvav 52,148,192
K 121,122,123
K 122,124,125
K 123,126,127
Mvav 56,152,192
ds_write_b128 v2, v[120:123] offset:3072
K 152,152,153
K 153,154,155
Mvav 88,152,200
buffer_load_dwordx4 a[240:243], v1, s[44:47], s82 offen offset:2048 sc1
K 154,156,157
K 155,158,159
Mvav 92,156,200
.Lh_4_2_2%=:
s_cbranch_execz .Lhr_80%=
ds_write_b128 v2, v[152:155] offset:7168
Mvav 60,156,192
buffer_load_dwordx4 a[228:231], v1, s[44:47], s81 offen offset:3072 sc1
buffer_load_dwordx4 a[244:247], v1, s[44:47], s82 offen offset:3072 sc1
s_branch .LL_83%=
.p2align 12
.LL_83%=:
.Lend_43%=:
s_waitcnt vmcnt(8) lgkmcnt(0)
s_barrier
s_cmp_eq_u32 s46, 0
s_cbranch_scc1 .Llate_84%=
ds_read_b32 v11, v11 offset:59392
ds_read_b64 v[8:9], v10 offset:57344
Rv 176,3,0
Rv 180,3,1024
Rv 184,3,2048
Rv 188,3,3072
Rv 192,3,4096
Rv 196,3,5120
Rv 200,3,6144
Rv 204,3,7168
W 6
Mvvv 32,224,176
s_lshl_b32 s92, s58, 2
s_add_u32 s91, s92, s51
s_add_u32 s95, s91, 7
Mvvv 36,228,176
s_and_b32 s95, s95, 15
s_mul_i32 s95, s95, 40960
s_mul_i32 s71, s51, 20480
Mvvv 40,224,180
s_add_u32 s71, s71, 61440
s_add_u32 m0, s71, s48
s_lshl2_add_u32 s94, s48, s95
Mvvv 44,228,180
W 4
v_cmp_ne_u32 vcc, 0, v11
s_cmp_lg_u64 vcc, 0
s_cbranch_scc1 .Lredo%=
A 4,4,8
Mvvv 48,224,184
v_rcp_f32 v4, v4
Mvvv 52,228,184
Ta 128,25,10240
A 5,5,9
Mvvv 56,224,188
v_rcp_f32 v5, v5
Mvvv 60,228,188
W 3
Ta 130,25,15360
D 16,40,94,0
Mvvv 64,224,192
s_add_u32 s95, s91, 9
s_and_b32 s95, s95, 15
s_mul_i32 s95, s95, 40960
Mvvv 68,228,192
s_mul_i32 s71, s51, 10240
s_add_u32 s71, s71, 112640
s_lshl_b32 s93, s58, 12
Mvvv 72,224,196
s_add_u32 s93, s93, s50
U 32,32,4
U 33,33,4
Mvvv 76,228,196
Ta 132,25,20480
Ta 134,25,25600
W 4
Mvvv 80,224,200
U 34,34,4
U 35,35,4
Mvvv 84,228,200
U 36,36,4
U 37,37,4
Mvvv 88,224,204
Ta 136,26,10272
Ta 138,26,15392
Mvvv 92,228,204
Ta 140,26,20512
Ta 142,26,25632
W 4
Zvaa 96,128,184
U 38,38,4
U 39,39,4
Mvaa 96,132,188
K 32,32,33
K 33,34,35
Zvaa 104,132,204
D 17,40,94,1024
K 34,36,37
Mvaa 104,128,200
Ta 144,25,10304
Ta 146,25,15424
Ta 148,25,20544
Ta 150,25,25664
W 4
Zvaa 108,136,200
Ta 152,26,10336
K 35,38,39
Mvaa 108,140,204
Ta 154,26,15456
Ta 156,26,20576
Zvaa 100,140,188
buffer_store_dwordx4 v[32:35], v1, s[8:11], s93 offen offset:0 sc1
U 40,40,4
Mvaa 100,136,184
Ta 158,26,25696
W 4
Zvaa 112,144,184
U 41,41,4
U 42,42,4
Mvaa 112,148,188
U 43,43,4
U 44,44,4
Zvaa 120,148,204
U 45,45,4
U 46,46,4
Mvaa 120,144,200
Ta 128,25,10368
Ta 130,25,15488
Ta 132,25,20608
Ta 134,25,25728
W 4
Zvaa 124,152,200
U 47,47,4
K 40,40,41
Mvaa 124,156,204
D 18,40,94,2048
K 41,42,43
Zvaa 116,156,188
K 42,44,45
K 43,46,47
Mvaa 116,152,184
Ta 136,26,10400
Ta 138,26,15520
Ta 140,26,20640
Ta 142,26,25760
W 4
Zvaa 128,128,184
Ta 144,25,10432
Ta 146,25,15552
Mvaa 128,132,188
buffer_store_dwordx4 v[40:43], v1, s[8:11], s93 offen offset:1024 sc1
U 48,48,4
Zvaa 136,132,204
U 49,49,4
U 50,50,4
Mvaa 136,128,200
Ta 148,25,20672
Ta 150,25,25792
W 4
Zvaa 140,136,200
U 51,51,4
U 52,52,4
Mvaa 140,140,204
U 53,53,4
U 54,54,4
Zvaa 132,140,188
U 55,55,4
K 48,48,49
)"
R"(Mvaa 132,136,184
Ta 152,26,10464
Ta 154,26,15584
Ta 156,26,20704
Ta 158,26,25824
W 4
Zvaa 144,144,184
D 19,40,94,3072
K 49,50,51
Mvaa 144,148,188
s_add_u32 m0, m0, 4096
K 50,52,53
K 51,54,55
Zvaa 152,148,204
Ta 128,25,10496
Ta 130,25,15616
Mvaa 152,144,200
Ta 132,25,20736
Ta 134,25,25856
W 4
Zvaa 156,152,200
buffer_store_dwordx4 v[48:51], v1, s[8:11], s93 offen offset:2048 sc1
U 56,56,4
Mvaa 156,156,204
U 57,57,4
U 58,58,4
Zvaa 148,156,188
U 59,59,4
U 60,60,4
Mvaa 148,152,184
Ta 136,26,10528
Ta 138,26,15648
Ta 140,26,20768
Ta 142,26,25888
W 4
Zvaa 160,128,184
U 61,61,4
U 62,62,4
Mvaa 160,132,188
U 63,63,4
K 56,56,57
Zvaa 168,132,204
buffer_load_dwordx4 v20, s[40:43], s94 offen lds
K 57,58,59
Mvaa 168,128,200
Ta 144,25,30720
Ta 146,25,35840
Ta 148,21,0
Ta 150,21,5120
W 4
Zvaa 172,136,200
s_add_u32 m0, s71, s48
s_lshl2_add_u32 s94, s48, s95
K 58,60,61
Mvaa 172,140,204
Ta 152,26,30752
K 59,62,63
Zvaa 164,140,188
buffer_store_dwordx4 v[56:59], v1, s[8:11], s93 offen offset:3072 sc1
U 64,64,5
Mvaa 164,136,184
Ta 154,26,35872
Ta 156,22,32
Ta 158,22,5152
W 4
Mvaa 96,144,192
U 65,65,5
U 66,66,5
Mvaa 96,148,196
U 67,67,5
U 68,68,5
Mvaa 104,148,212
U 69,69,5
U 70,70,5
Mvaa 104,144,208
Ta 128,25,30784
Ta 130,25,35904
Ta 132,21,64
Ta 134,21,5184
W 4
Mvaa 108,152,208
s_lshl_b32 s66, s14, 1
U 71,71,5
K 64,64,65
Mvaa 108,156,212
s_add_u32 s66, s66, 0
D 16,40,94,0
K 65,66,67
Mvaa 100,156,196
s_cmp_eq_u32 s66, s59
s_cselect_b32 s66, 134217728, 0
K 66,68,69
K 67,70,71
Mvaa 100,152,192
Ta 136,26,30816
Ta 138,26,35936
Ta 140,22,96
Ta 142,22,5216
W 4
Mvaa 112,128,192
s_add_u32 s67, s93, 131072
Ta 144,25,30848
Ta 146,25,35968
Mvaa 112,132,196
s_add_u32 s28, s52, s66
buffer_store_dwordx4 v[64:67], v1, s[8:11], s67 offen offset:0 sc1
U 72,72,5
Mvaa 120,132,212
s_lshl_b32 s66, s14, 1
U 73,73,5
U 74,74,5
Mvaa 120,128,208
Ta 148,21,128
Ta 150,21,5248
W 4
Mvaa 124,136,208
s_add_u32 s66, s66, 1
U 75,75,5
U 76,76,5
Mvaa 124,140,212
s_cmp_eq_u32 s66, s59
s_cselect_b32 s66, 134217728, 0
U 77,77,5
U 78,78,5
Mvaa 116,140,196
s_add_u32 s29, s52, s66
U 79,79,5
K 72,72,73
Mvaa 116,136,192
Ta 152,26,30880
Ta 154,26,36000
Ta 156,22,160
Ta 158,22,5280
W 4
Mvaa 128,144,192
D 17,40,94,1024
K 73,74,75
Mvaa 128,148,196
K 74,76,77
K 75,78,79
Mvaa 136,148,212
Ta 128,25,30912
Ta 130,25,36032
Mvaa 136,144,208
Ta 132,21,192
Ta 134,21,5312
W 4
Mvaa 140,152,208
D 18,40,94,2048
D 19,40,94,3072
s_add_u32 m0, m0, 4096
s_nop 0
buffer_load_dwordx4 v20, s[40:43], s94 offen lds
Mvaa 140,156,212
s_add_u32 s81, s58, 2
s_and_b32 s81, s81, 3
s_lshl_b32 s81, s81, 12
Mvaa 132,156,196
s_add_u32 s81, s81, s49
Ta 136,26,30944
Ta 138,26,36064
Mvaa 132,152,192
Ta 140,22,224
Ta 142,22,5344
W 4
Mvaa 144,128,192
buffer_store_dwordx4 v[72:75], v1, s[8:11], s67 offen offset:1024 sc1
U 80,80,5
Mvaa 144,132,196
U 81,81,5
U 82,82,5
Mvaa 152,132,212
U 83,83,5
U 84,84,5
Mvaa 152,128,208
Ta 144,25,30976
Ta 146,25,36096
Ta 148,21,256
Ta 150,21,5376
W 4
Mvaa 156,136,208
U 85,85,5
U 86,86,5
Mvaa 156,140,212
U 87,87,5
K 80,80,81
Mvaa 148,140,196
K 81,82,83
K 82,84,85
Mvaa 148,136,192
Ta 152,26,31008
Ta 154,26,36128
Ta 156,22,288
Ta 158,22,5408
W 4
Mvaa 160,144,192
buffer_load_dwordx4 a[0:3], v1, s[8:11], s81 offen offset:0 sc1
K 83,86,87
Mvaa 160,148,196
s_add_u32 s82, s81, 131072
Ta 128,21,10240
Ta 130,21,15360
Mvaa 168,148,212
Ta 132,21,20480
Ta 134,21,25600
Mvaa 168,144,208
W 4
Ta 136,22,10272
Ta 138,22,15392
Mvaa 172,152,208
buffer_store_dwordx4 v[80:83], v1, s[8:11], s67 offen offset:2048 sc1
U 88,88,5
Mvaa 172,156,212
U 89,89,5
U 90,90,5
Mvaa 164,156,196
U 91,91,5
U 92,92,5
Mvaa 164,152,192
Ta 140,22,20512
Ta 142,22,25632
s_waitcnt vmcnt(22) lgkmcnt(4)
Mvaa 96,128,216
U 93,93,5
U 94,94,5
Mvaa 96,132,220
U 95,95,5
K 88,88,89
Mvaa 104,132,236
K 89,90,91
K 90,92,93
Mvaa 104,128,232
Ta 144,21,10304
Ta 146,21,15424
Ta 148,21,20544
Ta 150,21,25664
W 4
Mvaa 108,136,232
buffer_load_dwordx4 a[16:19], v1, s[8:11], s82 offen offset:0 sc1
K 91,94,95
Mvaa 108,140,236
Ta 152,22,10336
Ta 154,22,15456
Mvaa 100,140,220
Ta 156,22,20576
Ta 158,22,25696
Mvaa 100,136,216
W 4
Ta 128,21,10368
Ta 130,21,15488
Mvaa 112,144,216
buffer_store_dwordx4 v[88:91], v1, s[8:11], s67 offen offset:3072 sc1
Ta 132,21,20608
Mvaa 112,148,220
Ta 134,21,25728
Ta 136,22,10400
Mvaa 120,148,236
Ta 138,22,15520
Ta 140,22,20640
Mvaa 120,144,232
W 7
Ta 142,22,25760
Ta 144,21,10432
Mvaa 124,152,232
buffer_load_dwordx4 a[4:7], v1, s[8:11], s81 offen offset:1024 sc1
Ta 146,21,15552
Mvaa 124,156,236
Ta 148,21,20672
Ta 150,21,25792
Mvaa 116,156,220
Mvaa 116,152,216
W 8
Ta 152,22,10464
Ta 154,22,15584
Mvaa 128,128,216
buffer_load_dwordx4 a[20:23], v1, s[8:11], s82 offen offset:1024 sc1
Mvaa 128,132,220
Ta 156,22,20704
Ta 158,22,25824
Mvaa 136,132,236
Mvaa 136,128,232
W 8
Ta 128,21,10496
Ta 130,21,15616
Mvaa 140,136,232
s_waitcnt vmcnt(7)
s_barrier
Mvaa 140,140,236
buffer_load_dwordx4 a[8:11], v1, s[8:11], s81 offen offset:2048 sc1
Ta 132,21,20736
Mvaa 132,140,220
Ta 134,21,25856
Mvaa 132,136,216
W 8
Ta 136,22,10528
Ta 138,22,15648
Mvaa 144,144,216
Mvaa 144,148,220
buffer_load_dwordx4 a[24:27], v1, s[8:11], s82 offen offset:2048 sc1
Ta 140,22,20768
Mvaa 152,148,236
Ta 142,22,25888
Mvaa 152,144,232
W 8
Ta 144,21,30720
Ta 146,21,35840
Mvaa 156,152,232
Mvaa 156,156,236
buffer_load_dwordx4 a[12:15], v1, s[8:11], s81 offen offset:3072 sc1
Ta 148,23,0
Mvaa 148,156,220
Ta 150,23,5120
Mvaa 148,152,216
W 8
Ta 152,22,30752
Ta 154,22,35872
Mvaa 160,128,216
Mvaa 160,132,220
buffer_load_dwordx4 a[28:31], v1, s[8:11], s82 offen offset:3072 sc1
Ta 156,24,32
Mvaa 168,132,236
s_lshl_b32 s92, s58, 2
s_add_u32 s91, s92, s51
s_add_u32 s95, s91, 11
Mvaa 168,128,232
W 7
s_and_b32 s95, s95, 15
s_mul_i32 s95, s95, 40960
Mvaa 172,136,232
s_mul_i32 s71, s51, 10240
s_add_u32 s71, s71, 40960
s_add_u32 m0, s71, s48
Mvaa 172,140,236
s_lshl2_add_u32 s94, s48, s95
D 16,40,94,0
Ta 158,24,5152
Mvaa 164,140,220
s_add_u32 s95, s91, 13
s_and_b32 s95, s95, 15
s_mul_i32 s95, s95, 40960
Mvaa 164,136,216
W 4
s_mul_i32 s71, s51, 20480
s_add_u32 s71, s71, 71680
Mvaa 96,144,224
s_lshl_b32 s80, s58, 3
Ta 128,21,30784
Ta 130,21,35904
Mvaa 96,148,228
s_add_u32 s80, s80, s55
D 17,40,94,1024
Ta 132,23,64
Mvaa 104,148,244
Ta 134,23,5184
Ta 136,22,30816
Mvaa 104,144,240
W 5
Ta 138,22,35936
Ta 140,24,96
Mvaa 108,152,240
s_waitcnt vmcnt(8)
v_mov_b64 v[28:29], s[12:13]
s_mov_b64 exec, 1
buffer_store_dwordx2 v[28:29], off, s[8:11], s80 sc1
s_mov_b64 exec, -1
Ta 142,24,5216
Ta 144,21,30848
Mvaa 108,156,244
Ta 146,21,35968
Ta 148,23,128
Mvaa 100,156,228
Ta 150,23,5248
Mvaa 100,152,224
W 8
Ta 152,22,30880
Ta 154,22,36000
Mvaa 112,128,224
D 18,40,94,2048
Mvaa 112,132,228
Ta 156,24,160
Ta 158,24,5280
Mvaa 120,132,244
Mvaa 120,128,240
W 8
Ta 128,21,30912
Ta 130,21,36032
Mvaa 124,136,240
D 19,40,94,3072
Mvaa 124,140,244
s_add_u32 m0, m0, 4096
Ta 132,23,192
Ta 134,23,5312
Mvaa 116,140,228
Mvaa 116,136,224
W 8
Ta 136,22,30944
Ta 138,22,36064
Mvaa 128,144,224
buffer_load_dwordx4 v20, s[40:43], s94 offen lds
Mvaa 128,148,228
)"
R"(s_add_u32 m0, s71, s48
s_lshl2_add_u32 s94, s48, s95
Ta 140,24,224
Mvaa 136,148,244
Ta 142,24,5344
Mvaa 136,144,240
W 8
Ta 144,21,30976
Ta 146,21,36096
Mvaa 140,152,240
D 16,40,94,0
Mvaa 140,156,244
s_add_u32 s95, s92, 15
s_and_b32 s95, s95, 15
s_mul_i32 s95, s95, 40960
Mvaa 132,156,228
s_mul_i32 s71, s51, 0
s_add_u32 s71, s71, 102400
Ta 148,23,256
Mvaa 132,152,224
W 7
Ta 150,23,5376
Ta 152,22,31008
Mvaa 144,128,224
D 17,40,94,1024
Ta 154,22,36128
Mvaa 144,132,228
Ta 156,24,288
Ta 158,24,5408
Mvaa 152,132,244
Mvaa 152,128,240
W 8
Ta 128,23,20480
Ta 130,23,25600
Mvaa 156,136,240
D 18,40,94,2048
Mvaa 156,140,244
Ta 132,23,51200
Ta 134,23,56320
Mvaa 148,140,228
Mvaa 148,136,224
W 8
Ta 136,24,20512
Ta 138,24,25632
Mvaa 160,144,224
D 19,40,94,3072
Mvaa 160,148,228
s_add_u32 m0, m0, 4096
Ta 140,24,51232
Ta 142,24,56352
Mvaa 168,148,244
Mvaa 168,144,240
W 8
Ta 144,23,20544
Ta 146,23,25664
Mvaa 172,152,240
buffer_load_dwordx4 v20, s[40:43], s94 offen lds
Mvaa 172,156,244
s_add_u32 m0, s71, s48
s_lshl2_add_u32 s94, s48, s95
Ta 148,23,51264
Mvaa 164,156,228
Ta 150,23,56384
Mvaa 164,152,224
s_waitcnt vmcnt(15) lgkmcnt(8)
Ta 152,24,20576
Ta 154,24,25696
Mvaa 96,128,0
D 16,40,94,0
Mvaa 96,132,4
Ta 156,24,51296
Ta 158,24,56416
Mvaa 104,132,20
Mvaa 104,128,16
W 8
Ta 128,23,20608
Ta 130,23,25728
Mvaa 108,136,16
D 17,40,94,1024
Mvaa 108,140,20
Ta 132,23,51328
Ta 134,23,56448
Mvaa 100,140,4
Mvaa 100,136,0
W 8
Ta 136,24,20640
Ta 138,24,25760
Mvaa 112,144,0
D 18,40,94,2048
Mvaa 112,148,4
Ta 140,24,51360
Ta 142,24,56480
Mvaa 120,148,20
Mvaa 120,144,16
W 8
Ta 144,23,20672
Ta 146,23,25792
Mvaa 124,152,16
D 19,40,94,3072
Mvaa 124,156,20
s_add_u32 m0, m0, 4096
Ta 148,23,51392
Ta 150,23,56512
Mvaa 116,156,4
buffer_load_dwordx4 v20, s[40:43], s94 offen lds
Mvaa 116,152,0
W 8
s_add_u32 s81, s58, 3
s_and_b32 s81, s81, 3
Mvaa 128,128,0
s_lshl_b32 s81, s81, 12
s_add_u32 s81, s81, s49
Ta 152,24,20704
Mvaa 128,132,4
Ta 154,24,25824
Ta 156,24,51424
Mvaa 136,132,20
buffer_load_dwordx4 a[32:35], v1, s[8:11], s81 offen offset:0 sc1
Ta 158,24,56544
Mvaa 136,128,16
W 8
s_add_u32 s82, s81, 131072
Ta 128,23,20736
Mvaa 140,136,16
s_waitcnt vmcnt(11)
s_barrier
Mvaa 140,140,20
Ta 130,23,25856
v_and_b32 v27, 1, v12
Mvaa 132,140,4
buffer_load_dwordx4 a[48:51], v1, s[8:11], s82 offen offset:0 sc1
Ta 132,23,51456
Mvaa 132,136,0
W 7
Ta 134,23,56576
v_lshlrev_b32 v27, 1, v27
Mvaa 144,144,0
Ta 136,24,20768
Ta 138,24,25888
Mvaa 144,148,4
Ta 140,24,51488
v_xor_b32 v27, v27, v13
Mvaa 152,148,20
buffer_load_dwordx4 a[36:39], v1, s[8:11], s81 offen offset:1024 sc1
Ta 142,24,56608
Mvaa 152,144,16
W 8
Ta 144,25,0
v_and_b32 v12, 2, v12
Mvaa 156,152,16
Ta 146,25,5120
v_or_b32 v12, v12, v14
Mvaa 156,156,20
Ta 148,21,40960
Ta 150,21,46080
Mvaa 148,156,4
buffer_load_dwordx4 a[52:55], v1, s[8:11], s82 offen offset:1024 sc1
v_lshl_or_b32 v12, v27, 2, v12
Mvaa 148,152,0
W 8
Ta 152,26,32
Ta 154,26,5152
Mvaa 160,128,0
v_cmp_eq_u32_e64 s[86:87], 0, v12
Mvaa 160,132,4
Ta 156,22,40992
Ta 158,22,46112
Mvaa 168,132,20
buffer_load_dwordx4 a[40:43], v1, s[8:11], s81 offen offset:2048 sc1
v_cndmask_b32_e64 v4, 0, v15, s[86:87]
Mvaa 168,128,16
W 8
Ta 128,25,64
Ta 130,25,5184
Mvaa 172,136,16
v_cmp_eq_u32_e64 s[86:87], 1, v12
Mvaa 172,140,20
Ta 132,21,41024
Ta 134,21,46144
Mvaa 164,140,4
buffer_load_dwordx4 a[56:59], v1, s[8:11], s82 offen offset:2048 sc1
v_cndmask_b32_e64 v5, 0, v15, s[86:87]
Mvaa 164,136,0
W 8
Ta 136,26,96
Ta 138,26,5216
Mvaa 96,144,8
v_cmp_eq_u32_e64 s[86:87], 2, v12
Mvaa 96,148,12
Ta 140,22,41056
Ta 142,22,46176
Mvaa 104,148,28
buffer_load_dwordx4 a[44:47], v1, s[8:11], s81 offen offset:3072 sc1
v_cndmask_b32_e64 v6, 0, v15, s[86:87]
Mvaa 104,144,24
W 8
Ta 144,25,128
Ta 146,25,5248
Mvaa 108,152,24
v_cmp_eq_u32_e64 s[86:87], 3, v12
Mvaa 108,156,28
Ta 148,21,41088
Ta 150,21,46208
Mvaa 100,156,12
buffer_load_dwordx4 a[60:63], v1, s[8:11], s82 offen offset:3072 sc1
v_cndmask_b32_e64 v7, 0, v15, s[86:87]
Mvaa 100,152,8
W 8
Ta 152,26,160
Ta 154,26,5280
Mvaa 112,128,8
v_cmp_eq_u32_e64 s[86:87], 4, v12
Mvaa 112,132,12
Ta 156,22,41120
Ta 158,22,46240
Mvaa 120,132,28
v_cndmask_b32_e64 v8, 0, v15, s[86:87]
Mvaa 120,128,24
W 8
Ta 128,25,192
Ta 130,25,5312
Mvaa 124,136,24
v_cmp_eq_u32_e64 s[86:87], 5, v12
v_bfe_u32 v28, v1, 4, 2
Mvaa 124,140,28
Ta 132,21,41152
v_lshlrev_b32 v28, 3, v28
Mvaa 116,140,12
Ta 134,21,46272
v_cndmask_b32_e64 v9, 0, v15, s[86:87]
Mvaa 116,136,8
W 8
buffer_load_dwordx2 v[28:29], v28, s[8:11], s55 offen sc1
Ta 136,26,224
Mvaa 128,144,8
Ta 138,26,5344
v_cmp_eq_u32_e64 s[86:87], 6, v12
Mvaa 128,148,12
Ta 140,22,41184
Ta 142,22,46304
Mvaa 136,148,28
v_cndmask_b32_e64 v10, 0, v15, s[86:87]
Mvaa 136,144,24
W 8
Ta 144,25,256
Ta 146,25,5376
Mvaa 140,152,24
s_waitcnt vmcnt(9)
s_barrier
Mvaa 140,156,28
Ta 148,21,41216
v_cmp_eq_u32_e64 s[86:87], 7, v12
Mvaa 132,156,12
Ta 150,21,46336
Mvaa 132,152,8
W 8
Ta 152,26,288
v_cndmask_b32_e64 v11, 0, v15, s[86:87]
Mvaa 144,128,8
s_mov_b32 s20, s38
Ta 154,26,5408
Mvaa 144,132,12
Ta 156,22,41248
Ta 158,22,46368
Mvaa 152,132,28
s_and_b32 s21, s39, 0xffff
Mvaa 152,128,24
W 8
Ta 128,21,51200
Ta 130,21,56320
Mvaa 156,136,24
s_mov_b32 s22, 2621440
Mvaa 156,140,28
s_mov_b32 s23, 0x20000
Ta 132,23,10240
Ta 134,23,15360
Mvaa 148,140,12
Mvaa 148,136,8
W 8
s_and_b32 s17, s17, 0xffff
Ta 136,23,30720
Mvaa 160,144,8
Ta 138,23,35840
Mvaa 160,148,12
s_mov_b32 s18, 2621440
Ta 140,23,40960
Ta 142,23,46080
Mvaa 168,148,28
Mvaa 168,144,24
W 8
s_mov_b32 s19, 0x20000
Ta 144,22,51232
Mvaa 172,152,24
Ta 146,22,56352
Mvaa 172,156,28
s_lshr_b32 s66, s15, 3
Ta 148,24,10272
Ta 150,24,15392
Mvaa 164,156,12
Mvaa 164,152,8
s_waitcnt vmcnt(5) lgkmcnt(8)
s_lshl_b32 s66, s66, 4
Ta 152,24,30752
Mvaa 96,128,32
Ta 154,24,35872
Mvaa 96,132,36
s_add_u32 s66, s66, s59
Ta 156,24,40992
Ta 158,24,46112
Mvaa 104,132,52
Mvaa 104,128,48
s_waitcnt vmcnt(1) lgkmcnt(8)
s_lshl_b32 s63, s66, 6
Ta 128,21,51264
Mvaa 104,136,56
Ta 130,21,56384
Mvaa 104,140,60
s_add_u32 s63, s63, 65536
Ta 132,23,10304
Ta 134,23,15424
Mvaa 96,140,44
Mvaa 96,136,40
W 8
s_mul_i32 s62, s66, 40960
Ta 136,23,30784
Mvaa 100,144,32
Ta 138,23,35904
Mvaa 100,148,36
s_add_u32 s62, s62, 56623104
Ta 140,23,41024
Ta 142,23,46144
Mvaa 108,148,52
Mvaa 108,144,48
W 8
s_lshl_b32 s66, s14, 13
Ta 144,22,51296
Mvaa 108,152,56
Ta 146,22,56416
Mvaa 108,156,60
s_add_u32 s78, s62, s66
Ta 148,24,10336
Ta 150,24,15456
Mvaa 100,156,44
s_waitcnt vmcnt(0)
v_cmp_eq_u64 vcc, s[12:13], v[28:29]
s_cmp_eq_u64 vcc, -1
s_cbranch_scc0 .Lspin_86%=
.Lok_87%=:
s_add_u32 s81, s58, 1
s_and_b32 s81, s81, 3
Mvaa 100,152,40
W 8
s_lshl_b32 s81, s81, 12
s_add_u32 s81, s81, s50
Mvaa 112,128,32
s_add_u32 s79, s78, 327680
buffer_load_dwordx4 a[64:67], v1, s[8:11], s81 offen offset:0 sc1
Ta 152,24,30816
Mvaa 112,132,36
s_add_u32 s82, s81, 131072
s_and_b32 s66, s14, 1
Ta 154,24,35936
Mvaa 120,132,52
Ta 156,24,41056
Ta 158,24,46176
Mvaa 120,128,48
W 8
K 96,96,97
K 97,98,99
Mvaa 120,136,56
s_mul_i32 s66, s66, 327680
Ta 128,21,51328
Ta 130,21,56448
Mvaa 120,140,60
s_add_u32 s65, s62, s66
buffer_load_dwordx4 a[80:83], v1, s[8:11], s82 offen offset:0 sc1
K 98,100,101
Mvaa 112,140,44
Ta 132,23,10368
Ta 134,23,15488
Mvaa 112,136,40
W 8
s_add_u32 s65, s65, 32768
K 99,102,103
Mvaa 116,144,32
s_add_u32 s83, s28, 0
Ta 136,23,30848
Ta 138,23,35968
Mvaa 116,148,36
s_mov_b32 s64, 0
buffer_load_dwordx4 a[68:71], v1, s[8:11], s81 offen offset:1024 sc1
Ta 140,23,41088
Mvaa 124,148,52
Ta 142,23,46208
Mvaa 124,144,48
W 8
Ta 144,22,51360
v_add_u32 v19, 4096, v1
Mvaa 124,152,56
Ta 146,22,56480
)"
R"(Mvaa 124,156,60
buffer_store_dwordx4 v[96:99], v1, s[8:11], s83 offen sc1
K 104,104,105
Mvaa 116,156,44
K 105,106,107
v_bfe_u32 v27, v1, 4, 3
Mvaa 116,152,40
W 6
Ta 148,24,10400
Ta 150,24,15520
Mvaa 128,128,32
K 106,108,109
v_lshlrev_b32 v27, 3, v27
Mvaa 128,132,36
buffer_load_dwordx4 a[84:87], v1, s[8:11], s82 offen offset:1024 sc1
Ta 152,24,30880
Mvaa 136,132,52
Ta 154,24,36000
K 107,110,111
Mvaa 136,128,48
W 6
Ta 156,24,41120
v_bfe_u32 v18, v1, 7, 1
Mvaa 136,136,56
s_add_u32 s83, s29, 40960
s_add_u32 s66, s14, 1
Ta 158,24,46240
Mvaa 136,140,60
s_lshr_b32 s66, s66, 2
buffer_load_dwordx4 a[72:75], v1, s[8:11], s81 offen offset:2048 sc1
Ta 128,21,51392
Mvaa 128,140,44
s_mul_i32 s66, s66, 0
Ta 130,21,56512
Ta 132,23,10432
Mvaa 128,136,40
W 7
Ta 134,23,15552
Ta 136,23,30912
Mvaa 132,144,32
s_sub_u32 s66, 512, s66
Ta 138,23,36032
Mvaa 132,148,36
buffer_store_dwordx4 v[104:107], v1, s[8:11], s83 offen sc1
K 112,112,113
Mvaa 140,148,52
K 113,114,115
K 114,116,117
Mvaa 140,144,48
W 6
Ta 140,23,41152
Ta 142,23,46272
Mvaa 140,152,56
v_mul_u32_u24 v18, s66, v18
K 115,118,119
Mvaa 140,156,60
s_add_u32 s83, s28, 8192
buffer_load_dwordx4 a[88:91], v1, s[8:11], s82 offen offset:2048 sc1
Ta 144,22,51424
Mvaa 132,156,44
Ta 146,22,56544
v_add_u32 v18, v18, v27
Mvaa 132,152,40
W 6
Ta 148,24,10464
Ta 150,24,15584
Mvaa 144,128,32
s_mov_b32 s71, 0
Ta 152,24,30944
Ta 154,24,36064
Mvaa 144,132,36
buffer_load_dwordx4 a[76:79], v1, s[8:11], s81 offen offset:3072 sc1
v_bfe_u32 v20, v1, 4, 4
Mvaa 152,132,52
Ta 156,24,41184
Ta 158,24,46304
Mvaa 152,128,48
W 8
Ta 128,21,51456
v_mul_u32_u24 v20, 1280, v20
Mvaa 152,136,56
Ta 130,21,56576
v_lshrrev_b32 v12, 8, v1
Mvaa 152,140,60
buffer_store_dwordx4 v[112:115], v1, s[8:11], s83 offen sc1
K 120,120,121
Mvaa 144,140,44
K 121,122,123
K 122,124,125
Mvaa 144,136,40
W 6
Ta 132,23,10496
K 123,126,127
Mvaa 148,144,32
s_add_u32 s83, s29, 49152
Ta 134,23,15616
v_lshl_add_u32 v20, v12, 4, v20
Mvaa 148,148,36
s_bfe_u32 s66, s59, 0x10001
buffer_load_dwordx4 a[92:95], v1, s[8:11], s82 offen offset:3072 sc1
Ta 136,23,30976
Mvaa 156,148,52
s_add_u32 s81, s58, 2
s_and_b32 s81, s81, 3
s_lshl_b32 s81, s81, 12
Mvaa 156,144,48
W 5
s_add_u32 s81, s81, s50
Ta 138,23,36096
Mvaa 156,152,56
s_lshl_b32 s67, s57, 1
Ta 140,23,41216
Ta 142,23,46336
Mvaa 156,156,60
s_add_u32 s66, s66, s67
buffer_load_dwordx4 a[96:99], v1, s[8:11], s81 offen offset:0 sc1
Ta 144,22,51488
Mvaa 148,156,44
s_add_u32 s82, s81, 131072
Ta 146,22,56608
Ta 148,24,10528
Mvaa 148,152,40
W 7
s_lshl_b32 s61, s66, 2
Ta 150,24,15648
Mvaa 160,128,32
s_lshr_b32 s66, s59, 2
Ta 152,24,31008
Ta 154,24,36128
Mvaa 160,132,36
buffer_store_dwordx4 v[120:123], v1, s[8:11], s83 offen sc1
K 128,128,129
Mvaa 168,132,52
s_lshl_b32 s66, s66, 1
K 129,130,131
K 130,132,133
Mvaa 168,128,48
W 6
Ta 156,24,41248
K 131,134,135
Mvaa 168,136,56
s_add_u32 s83, s28, 16384
Ta 158,24,46368
Ta 128,25,10240
Mvaa 168,140,60
s_add_u32 s61, s61, s66
buffer_load_dwordx4 a[112:115], v1, s[8:11], s82 offen offset:0 sc1
Ta 130,25,15360
Mvaa 160,140,44
s_and_b32 s66, s59, 1
Ta 132,25,20480
Ta 134,25,25600
Mvaa 160,136,40
W 8
Ta 136,26,10272
Ta 138,26,15392
Mvaa 164,144,32
s_add_u32 s61, s61, s66
Mvaa 164,148,36
buffer_load_dwordx4 a[100:103], v1, s[8:11], s81 offen offset:1024 sc1
Ta 140,26,20512
Mvaa 172,148,52
s_xor_b32 s67, s61, 0
Ta 142,26,25632
Mvaa 172,144,48
W 8
Ta 144,25,10304
Ta 146,25,15424
Mvaa 172,152,56
s_mul_i32 s72, s67, 20480
Mvaa 172,156,60
buffer_store_dwordx4 v[128:131], v1, s[8:11], s83 offen sc1
K 136,136,137
Mvaa 164,156,44
s_mul_i32 s66, s58, 320
K 137,138,139
K 138,140,141
Mvaa 164,152,40
W 6
Ta 148,25,20544
K 139,142,143
Zvav 176,128,32
s_add_u32 s83, s29, 57344
Ta 150,25,25664
Ta 152,26,10336
Mvav 176,132,40
s_add_u32 s72, s72, s66
buffer_load_dwordx4 a[116:119], v1, s[8:11], s82 offen offset:1024 sc1
Ta 154,26,15456
Zvav 184,132,72
s_mul_i32 s75, s67, 20480
Ta 156,26,20576
Ta 158,26,25696
Mvav 184,128,64
W 8
Ta 128,25,10368
Ta 130,25,15488
Zvav 188,136,64
s_mul_i32 s66, s58, 2560
Mvav 188,140,72
buffer_load_dwordx4 a[104:107], v1, s[8:11], s81 offen offset:2048 sc1
Ta 132,25,20608
Zvav 180,140,40
s_add_u32 s75, s75, s66
Ta 134,25,25728
Mvav 180,136,32
W 8
s_add_u32 s75, s75, 131072
Ta 136,26,10400
Zvav 192,144,32
Ta 138,26,15520
Mvav 192,148,40
s_xor_b32 s67, s61, 124
buffer_store_dwordx4 v[136:139], v1, s[8:11], s83 offen sc1
K 144,144,145
Zvav 200,148,72
K 145,146,147
K 146,148,149
Mvav 200,144,64
W 6
Ta 140,26,20640
K 147,150,151
Zvav 204,152,64
s_add_u32 s83, s28, 24576
s_mul_i32 s73, s67, 20480
Ta 142,26,25760
Mvav 204,156,72
s_mul_i32 s66, s58, 320
buffer_load_dwordx4 a[120:123], v1, s[8:11], s82 offen offset:2048 sc1
Ta 144,25,10432
Zvav 196,156,40
Ta 146,25,15552
Ta 148,25,20672
Mvav 196,152,32
W 7
s_add_u32 s73, s73, s66
Ta 150,25,25792
Zvav 208,128,32
Ta 152,26,10464
Ta 154,26,15584
Mvav 208,132,40
s_mul_i32 s76, s67, 20480
buffer_load_dwordx4 a[108:111], v1, s[8:11], s81 offen offset:3072 sc1
Ta 156,26,20704
Zvav 216,132,72
Ta 158,26,25824
Mvav 216,128,64
W 8
s_mul_i32 s66, s58, 2560
Ta 128,25,10496
Zvav 220,136,64
Ta 130,25,15616
Mvav 220,140,72
s_add_u32 s76, s76, s66
buffer_store_dwordx4 v[144:147], v1, s[8:11], s83 offen sc1
K 152,152,153
Zvav 212,140,40
K 153,154,155
K 154,156,157
Mvav 212,136,32
W 6
Ta 132,25,20736
K 155,158,159
Zvav 224,144,32
s_add_u32 s83, s29, 65536
s_add_u32 s76, s76, 131072
Ta 134,25,25856
Mvav 224,148,40
buffer_load_dwordx4 a[124:127], v1, s[8:11], s82 offen offset:3072 sc1
Ta 136,26,10528
Zvav 232,148,72
s_add_u32 s81, s58, 3
s_and_b32 s81, s81, 3
s_lshl_b32 s81, s81, 12
Mvav 232,144,64
W 5
s_add_u32 s81, s81, s50
Ta 138,26,15648
Zvav 236,152,64
Ta 140,26,20768
v_bfe_u32 v15, v1, 7, 1
Mvav 236,156,72
buffer_load_dwordx4 a[184:187], v1, s[8:11], s81 offen offset:0 sc1
Ta 142,26,25888
Zvav 228,156,40
s_add_u32 s82, s81, 131072
Ta 144,25,30720
v_mul_u32_u24 v14, 10240, v15
Mvav 228,152,32
W 5
Ta 146,25,35840
v_lshrrev_b32 v15, 8, v1
Zvav 240,128,32
Ta 148,21,0
Ta 150,21,5120
Mvav 240,132,40
buffer_store_dwordx4 v[152:155], v1, s[8:11], s83 offen sc1
K 160,160,161
Zvav 248,132,72
K 161,162,163
K 162,164,165
Mvav 248,128,64
W 4
K 163,166,167
v_lshl_add_u32 v14, v15, 7, v14
Zvav 252,136,64
s_add_u32 s83, s28, 32768
Ta 152,26,30752
Ta 154,26,35872
Mvav 252,140,72
buffer_load_dwordx4 a[200:203], v1, s[8:11], s82 offen offset:0 sc1
v_and_b32 v15, 0x70, v1
Zvav 244,140,40
Ta 156,22,32
Ta 158,22,5152
Mvav 244,136,32
W 4
Ta 128,25,30784
v_add_u32 v14, v14, v15
Mvav 176,144,48
s_bitcmp1_b32 s14, 0
s_cselect_b32 s74, s73, s72
s_cselect_b32 s77, s76, s75
Ta 130,25,35904
Ta 132,21,64
Mvav 176,148,56
s_add_u32 s74, s74, 256
buffer_load_dwordx4 a[188:191], v1, s[8:11], s81 offen offset:1024 sc1
Ta 134,21,5184
Mvav 184,148,88
s_add_u32 s77, s77, 2048
Ta 136,26,30816
Ta 138,26,35936
Mvav 184,144,80
W 6
Ta 140,22,96
Ta 142,22,5216
Mvav 188,152,80
s_lshl_b32 s66, s14, 6
Ta 144,25,30848
Ta 146,25,35968
Mvav 188,156,88
s_lshl_b32 s67, s14, 9
buffer_store_dwordx4 v[160:163], v1, s[8:11], s83 offen sc1
K 168,168,169
Mvav 180,156,56
K 169,170,171
K 170,172,173
Mvav 180,152,48
W 6
Ta 148,21,128
K 171,174,175
Mvav 192,128,48
s_add_u32 s83, s29, 73728
s_add_u32 s72, s72, s66
Ta 150,21,5248
Mvav 192,132,56
s_add_u32 s75, s75, s67
buffer_load_dwordx4 a[204:207], v1, s[8:11], s82 offen offset:1024 sc1
Ta 152,26,30880
Mvav 200,132,88
Ta 154,26,36000
Ta 156,22,160
Mvav 200,128,80
W 7
s_add_u32 s73, s73, s66
Ta 158,22,5280
Mvav 204,136,80
Ta 128,25,30912
Ta 130,25,36032
Mvav 204,140,88
s_add_u32 s76, s76, s67
buffer_load_dwordx4 a[192:195], v1, s[8:11], s81 offen offset:2048 sc1
Ta 132,21,192
Mvav 196,140,56
Ta 134,21,5312
Mvav 196,136,48
W 8
Ta 136,26,30944
Ta 138,26,36064
Mvav 208,144,48
Mvav 208,148,56
buffer_store_dwordx4 v[168:171], v1, s[8:11], s83 offen sc1
Ta 140,22,224
Mvav 216,148,88
Ta 142,22,5344
Mvav 216,144,80
W 8
Ta 144,25,30976
Ta 146,25,36096
Mvav 220,152,80
Mvav 220,156,88
buffer_load_dwordx4 a[208:211], v1, s[8:11], s82 offen offset:2048 sc1
Ta 148,21,256
Mvav 212,156,56
Ta 150,21,5376
Mvav 212,152,48
W 8
Ta 152,26,31008
Ta 154,26,36128
)"
R"(Mvav 224,128,48
Mvav 224,132,56
buffer_load_dwordx4 a[196:199], v1, s[8:11], s81 offen offset:3072 sc1
Ta 156,22,288
Mvav 232,132,88
Ta 158,22,5408
Mvav 232,128,80
W 8
Ta 128,21,10240
Ta 130,21,15360
Mvav 236,136,80
Mvav 236,140,88
buffer_load_dwordx4 a[212:215], v1, s[8:11], s82 offen offset:3072 sc1
Ta 132,21,20480
Mvav 228,140,56
Ta 134,21,25600
Mvav 228,136,48
W 8
Ta 136,22,10272
Ta 138,22,15392
Mvav 240,144,48
Mvav 240,148,56
buffer_load_dwordx4 v[36:39], v20, s[16:19], s72 offen
Ta 140,22,20512
Mvav 248,148,88
Ta 142,22,25632
Mvav 248,144,80
W 8
Ta 144,21,10304
Ta 146,21,15424
Mvav 252,152,80
Mvav 252,156,88
buffer_load_dwordx4 v[44:47], v14, s[8:11], s75 offen sc1
Ta 148,21,20544
Mvav 244,156,56
Ta 150,21,25664
Mvav 244,152,48
s_waitcnt vmcnt(31) lgkmcnt(8)
Ta 152,22,10336
Ta 154,22,15456
Mvaa 176,128,64
Mvaa 176,132,68
buffer_load_dwordx4 v[52:55], v20, s[16:19], s73 offen
Ta 156,22,20576
Mvaa 184,132,84
Ta 158,22,25696
Mvaa 184,128,80
W 8
Ta 128,21,10368
Ta 130,21,15488
Mvaa 188,136,80
Mvaa 188,140,84
buffer_load_dwordx4 v[60:63], v14, s[8:11], s76 offen sc1
Ta 132,21,20608
Mvaa 180,140,68
Ta 134,21,25728
Mvaa 180,136,64
W 8
Ta 136,22,10400
Ta 138,22,15520
Mvaa 192,144,64
Mvaa 192,148,68
buffer_load_dwordx4 v[68:71], v20, s[16:19], s74 offen
Ta 140,22,20640
Mvaa 200,148,84
Ta 142,22,25760
Mvaa 200,144,80
W 8
Ta 144,21,10432
Ta 146,21,15552
Mvaa 204,152,80
Mvaa 204,156,84
buffer_load_dwordx4 v[76:79], v14, s[8:11], s77 offen sc1
Ta 148,21,20672
Mvaa 196,156,68
Ta 150,21,25792
Mvaa 196,152,64
W 8
Ta 152,22,10464
Ta 154,22,15584
Mvaa 208,128,64
Mvaa 208,132,68
buffer_load_dwordx2 v[12:13], v27, s[8:11], 0 offen offset:3584 sc1
Ta 156,22,20704
Mvaa 216,132,84
Ta 158,22,25824
Mvaa 216,128,80
W 8
Ta 128,21,10496
Ta 130,21,15616
Mvaa 220,136,80
Mvaa 220,140,84
s_waitcnt vmcnt(10)
v_lshlrev_b32 v2, 2, v1
v_mov_b64 v[28:29], s[12:13]
s_mov_b64 exec, 3
buffer_store_dwordx2 v[28:29], v2, s[8:11], s53 offen offset:0 sc1
s_mov_b64 exec, -1
Ta 132,21,20736
Ta 134,21,25856
Mvaa 212,140,68
Mvaa 212,136,64
W 8
Ta 136,22,10528
Ta 138,22,15648
Mvaa 224,144,64
Mvaa 224,148,68
Ta 140,22,20768
Ta 142,22,25888
Mvaa 232,148,84
Mvaa 232,144,80
W 8
Ta 144,21,30720
Ta 146,21,35840
Mvaa 236,152,80
Mvaa 236,156,84
Ta 148,23,0
Ta 150,23,5120
Mvaa 228,156,68
Mvaa 228,152,64
W 8
Ta 152,22,30752
Ta 154,22,35872
Mvaa 240,128,64
Mvaa 240,132,68
Ta 156,24,32
Ta 158,24,5152
Mvaa 248,132,84
Mvaa 248,128,80
W 8
Ta 128,21,30784
Ta 130,21,35904
Mvaa 252,136,80
Mvaa 252,140,84
Ta 132,23,64
Ta 134,23,5184
Mvaa 244,140,68
Mvaa 244,136,64
W 8
Ta 136,22,30816
Ta 138,22,35936
Mvaa 176,144,72
Mvaa 176,148,76
Ta 140,24,96
Ta 142,24,5216
Mvaa 184,148,92
Mvaa 184,144,88
W 8
Ta 144,21,30848
Ta 146,21,35968
Mvaa 188,152,88
Mvaa 188,156,92
Ta 148,23,128
Ta 150,23,5248
Mvaa 180,156,76
Mvaa 180,152,72
W 8
Ta 152,22,30880
Ta 154,22,36000
Mvaa 192,128,72
Mvaa 192,132,76
Ta 156,24,160
Ta 158,24,5280
Mvaa 200,132,92
Mvaa 200,128,88
W 8
Ta 128,21,30912
Ta 130,21,36032
Mvaa 204,136,88
Mvaa 204,140,92
Ta 132,23,192
Ta 134,23,5312
Mvaa 196,140,76
Mvaa 196,136,72
W 8
Ta 136,22,30944
Ta 138,22,36064
Mvaa 208,144,72
Mvaa 208,148,76
Ta 140,24,224
Ta 142,24,5344
Mvaa 216,148,92
Mvaa 216,144,88
W 8
Ta 144,21,30976
Ta 146,21,36096
Mvaa 220,152,88
Mvaa 220,156,92
Ta 148,23,256
Ta 150,23,5376
Mvaa 212,156,76
Mvaa 212,152,72
W 8
Ta 152,22,31008
Ta 154,22,36128
Mvaa 224,128,72
Mvaa 224,132,76
Ta 156,24,288
Ta 158,24,5408
Mvaa 232,132,92
Mvaa 232,128,88
W 8
Ta 128,23,20480
Ta 130,23,25600
Mvaa 236,136,88
Mvaa 236,140,92
Ta 132,23,51200
Ta 134,23,56320
Mvaa 228,140,76
Mvaa 228,136,72
W 8
Ta 136,25,0
Ta 138,25,5120
Mvaa 240,144,72
Mvaa 240,148,76
Ta 140,21,40960
Ta 142,21,46080
Mvaa 248,148,92
s_waitcnt vmcnt(1)
v_xor_b32 v12, s12, v12
v_xor_b32 v13, s13, v13
v_lshrrev_b32 v3, 11, v12
v_or_b32 v13, v13, v3
v_cmp_eq_u32 vcc, 0, v13
s_cmp_eq_u64 vcc, -1
s_cbranch_scc0 .LL_91%=
.LL_92%=:
v_bfe_u32 v13, v12, 4, 7
s_xor_b32 s66, s61, 124
v_cmp_eq_u32 vcc, s61, v13
v_cmp_eq_u32_e64 s[86:87], s66, v13
s_or_b32 s68, vcc_lo, s86
s_and_b32 s68, s68, 255
s_cbranch_scc1 .Lup_89%=
.Lupr_90%=:
Mvaa 248,144,88
W 8
Ta 144,21,51200
Ta 146,21,56320
Mvaa 252,152,88
v_lshlrev_b32 v32, 16, v36
Mvaa 252,156,92
Ta 148,23,10240
v_and_b32 v33, 0xffff0000, v36
Mvaa 244,156,76
Ta 150,23,15360
v_lshlrev_b32 v34, 16, v37
Mvaa 244,152,72
W 8
Ta 152,23,30720
v_and_b32 v35, 0xffff0000, v37
Mvaa 176,128,96
Ta 154,23,35840
v_lshlrev_b32 v36, 16, v38
Mvaa 176,132,100
buffer_load_dwordx2 v[16:17], v18, s[8:11], s63 offen sc1
v_and_b32 v37, 0xffff0000, v38
Mvaa 184,132,116
Ta 156,23,40960
Ta 158,23,46080
Mvaa 184,128,112
W 8
v_lshlrev_b32 v38, 16, v39
v_and_b32 v39, 0xffff0000, v39
Mvaa 184,136,120
Ta 128,24,20512
Ta 130,24,25632
Mvaa 184,140,124
v_lshlrev_b32 v40, 16, v44
v_and_b32 v41, 0xffff0000, v44
Mvaa 176,140,108
s_barrier
Mvaa 176,136,104
W 6
Ta 132,24,51232
Ta 134,24,56352
Mvaa 176,144,184
v_lshlrev_b32 v42, 16, v45
v_and_b32 v43, 0xffff0000, v45
Mvaa 176,148,188
Ta 136,26,32
Ta 138,26,5152
Mvaa 184,148,204
v_lshlrev_b32 v44, 16, v46
v_and_b32 v45, 0xffff0000, v46
Mvaa 184,144,200
W 6
Ta 140,22,40992
Ta 142,22,46112
Mvaa 184,152,208
v_lshlrev_b32 v46, 16, v47
v_and_b32 v47, 0xffff0000, v47
Mvaa 184,156,212
Ta 144,22,51232
Ta 146,22,56352
Mvaa 176,156,196
v_lshlrev_b32 v48, 16, v52
v_and_b32 v49, 0xffff0000, v52
Mvaa 176,152,192
W 6
Ta 148,24,10272
Ta 150,24,15392
Mvaa 180,128,96
v_lshlrev_b32 v50, 16, v53
v_and_b32 v51, 0xffff0000, v53
Mvaa 180,132,100
Ta 152,24,30752
Ta 154,24,35872
Mvaa 188,132,116
v_lshlrev_b32 v52, 16, v54
v_and_b32 v53, 0xffff0000, v54
Mvaa 188,128,112
W 6
Ta 156,24,40992
Ta 158,24,46112
Mvaa 188,136,120
v_lshlrev_b32 v54, 16, v55
v_and_b32 v55, 0xffff0000, v55
Mvaa 188,140,124
Ta 128,23,20544
Ta 130,23,25664
Mvaa 180,140,108
v_lshlrev_b32 v56, 16, v60
v_and_b32 v57, 0xffff0000, v60
Mvaa 180,136,104
W 6
Ta 132,23,51264
Ta 134,23,56384
Mvaa 180,144,184
v_lshlrev_b32 v58, 16, v61
v_and_b32 v59, 0xffff0000, v61
Mvaa 180,148,188
Ta 136,25,64
Ta 138,25,5184
Mvaa 188,148,204
v_lshlrev_b32 v60, 16, v62
v_and_b32 v61, 0xffff0000, v62
Mvaa 188,144,200
W 6
Ta 140,21,41024
Ta 142,21,46144
Mvaa 188,152,208
v_lshlrev_b32 v62, 16, v63
v_and_b32 v63, 0xffff0000, v63
Mvaa 188,156,212
Ta 144,21,51264
Ta 146,21,56384
Mvaa 180,156,196
v_lshlrev_b32 v64, 16, v68
v_and_b32 v65, 0xffff0000, v68
Mvaa 180,152,192
W 6
Ta 148,23,10304
Ta 150,23,15424
Mvaa 192,128,96
v_lshlrev_b32 v66, 16, v69
v_and_b32 v67, 0xffff0000, v69
Mvaa 192,132,100
Ta 152,23,30784
Ta 154,23,35904
Mvaa 200,132,116
v_lshlrev_b32 v68, 16, v70
v_and_b32 v69, 0xffff0000, v70
Mvaa 200,128,112
W 6
Ta 156,23,41024
Ta 158,23,46144
Mvaa 200,136,120
v_lshlrev_b32 v70, 16, v71
K 176,176,177
Mvaa 200,140,124
Ta 128,24,20576
v_and_b32 v71, 0xffff0000, v71
Mvaa 192,140,108
Ta 130,24,25696
K 177,178,179
Mvaa 192,136,104
W 6
v_lshlrev_b32 v72, 16, v76
K 178,180,181
Mvaa 192,144,184
Ta 132,24,51296
v_and_b32 v73, 0xffff0000, v76
Mvaa 192,148,188
Ta 134,24,56416
v_lshlrev_b32 v74, 16, v77
Mvaa 200,148,204
K 179,182,183
v_and_b32 v75, 0xffff0000, v77
Mvaa 200,144,200
W 4
Ta 136,26,96
Ta 138,26,5216
Mvaa 200,152,208
s_add_u32 s83, s28, 327680
buffer_store_dwordx4 v[176:179], v1, s[8:11], s83 offen sc1
v_lshlrev_b32 v76, 16, v78
Mvaa 200,156,212
s_waitcnt vmcnt(1)
v_cmp_eq_u64 vcc, s[12:13], v[16:17]
)"
R"(s_and_b32 s71, vcc_lo, 255
s_bitset0_b32 s71, s59
K 184,184,185
K 185,186,187
Mvaa 192,156,196
K 186,188,189
K 187,190,191
s_add_u32 s83, s29, 368640
buffer_store_dwordx4 v[184:187], v1, s[8:11], s83 offen sc1
Mvaa 192,152,192
Ta 140,22,41056
Ta 142,22,46176
W 4
Mvaa 196,128,96
s_lshl_b32 s92, s14, 3
s_lshr_b32 s92, s71, s92
s_mov_b32 s91, 134217728
Mvaa 196,132,100
s_bitcmp1_b32 s71, 0
s_cselect_b32 s94, s78, s91
v_and_b32 v77, 0xffff0000, v78
Mvaa 204,132,116
v_lshlrev_b32 v78, 16, v79
v_and_b32 v79, 0xffff0000, v79
Mvaa 204,128,112
Ta 144,22,51296
Ta 146,22,56416
Ta 148,24,10336
Ta 150,24,15456
W 4
Mvaa 204,136,120
Ta 152,24,30816
Ta 154,24,35936
Mvaa 204,140,124
buffer_load_dwordx4 a[0:3], v1, s[8:11], s94 offen offset:0 sc1
Ta 156,24,41056
Mvaa 196,140,108
Ta 158,24,46176
Ta 128,23,20608
Mvaa 196,136,104
W 5
Ta 130,23,25728
Ta 132,23,51328
Mvaa 196,144,184
Ta 134,23,56448
Ta 136,25,128
Mvaa 196,148,188
s_bitcmp1_b32 s92, 0
s_cbranch_scc0 .LL_94%=
buffer_load_dwordx4 a[216:219], v1, s[8:11], s65 offen offset:0 sc1
.LL_94%=:
Ta 138,25,5248
Mvaa 204,148,204
s_bitcmp1_b32 s71, 1
s_cselect_b32 s94, s78, s91
Ta 140,21,41088
Mvaa 204,144,200
W 7
Ta 142,21,46208
Ta 144,21,51328
Mvaa 204,152,208
Ta 146,21,56448
Mvaa 204,156,212
Ta 148,23,10368
Ta 150,23,15488
Mvaa 196,156,196
buffer_load_dwordx4 a[4:7], v1, s[8:11], s94 offen offset:1024 sc1
Mvaa 196,152,192
W 8
Ta 152,23,30848
Ta 154,23,35968
Mvaa 208,128,96
Mvaa 208,132,100
Ta 156,23,41088
Ta 158,23,46208
Mvaa 216,132,116
K 192,192,193
Mvaa 216,128,112
W 8
Ta 128,24,20640
Ta 130,24,25760
Mvaa 216,136,120
K 193,194,195
Mvaa 216,140,124
s_bitcmp1_b32 s92, 1
s_cbranch_scc0 .LL_95%=
buffer_load_dwordx4 a[220:223], v1, s[8:11], s65 offen offset:1024 sc1
.LL_95%=:
K 194,196,197
Mvaa 208,140,108
s_bitcmp1_b32 s71, 2
s_cselect_b32 s94, s78, s91
Ta 132,24,51360
Mvaa 208,136,104
W 7
Ta 134,24,56480
K 195,198,199
Mvaa 208,144,184
s_add_u32 s83, s28, 335872
Ta 136,26,160
Ta 138,26,5280
Mvaa 208,148,188
buffer_store_dwordx4 v[192:195], v1, s[8:11], s83 offen sc1
K 200,200,201
Mvaa 216,148,204
K 201,202,203
K 202,204,205
Mvaa 216,144,200
W 6
Ta 140,22,41120
K 203,206,207
Mvaa 216,152,208
s_add_u32 s83, s29, 376832
Ta 142,22,46240
Ta 144,22,51360
Mvaa 216,156,212
buffer_store_dwordx4 v[200:203], v1, s[8:11], s83 offen sc1
Ta 146,22,56480
Mvaa 208,156,196
Ta 148,24,10400
Ta 150,24,15520
Mvaa 208,152,192
W 8
Ta 152,24,30880
Ta 154,24,36000
Mvaa 212,128,96
Mvaa 212,132,100
buffer_load_dwordx4 a[8:11], v1, s[8:11], s94 offen offset:2048 sc1
Ta 156,24,41120
Mvaa 220,132,116
Ta 158,24,46240
Mvaa 220,128,112
W 8
Ta 128,23,20672
Ta 130,23,25792
Mvaa 220,136,120
Mvaa 220,140,124
s_bitcmp1_b32 s92, 2
s_cbranch_scc0 .LL_96%=
buffer_load_dwordx4 a[224:227], v1, s[8:11], s65 offen offset:2048 sc1
.LL_96%=:
Ta 132,23,51392
Mvaa 212,140,108
s_bitcmp1_b32 s71, 3
s_cselect_b32 s94, s78, s91
Ta 134,23,56512
Mvaa 212,136,104
W 8
Ta 136,25,192
Ta 138,25,5312
Mvaa 212,144,184
Mvaa 212,148,188
Ta 140,21,41152
Ta 142,21,46272
Mvaa 220,148,204
buffer_load_dwordx4 a[12:15], v1, s[8:11], s94 offen offset:3072 sc1
Mvaa 220,144,200
W 8
Ta 144,21,51392
Ta 146,21,56512
Mvaa 220,152,208
Mvaa 220,156,212
Ta 148,23,10432
Ta 150,23,15552
Mvaa 212,156,196
Mvaa 212,152,192
W 8
Ta 152,23,30912
Ta 154,23,36032
Mvaa 224,128,96
Mvaa 224,132,100
s_bitcmp1_b32 s92, 3
s_cbranch_scc0 .LL_97%=
buffer_load_dwordx4 a[228:231], v1, s[8:11], s65 offen offset:3072 sc1
.LL_97%=:
Ta 156,23,41152
Mvaa 232,132,116
s_bitcmp1_b32 s71, 4
s_cselect_b32 s94, s78, s91
Ta 158,23,46272
Mvaa 232,128,112
W 8
Ta 128,24,20704
K 208,208,209
Mvaa 232,136,120
Ta 130,24,25824
K 209,210,211
Mvaa 232,140,124
Ta 132,24,51424
K 210,212,213
Mvaa 224,140,108
Ta 134,24,56544
K 211,214,215
Mvaa 224,136,104
W 8
Ta 136,26,224
Ta 138,26,5344
Mvaa 224,144,184
s_add_u32 s83, s28, 344064
buffer_load_dwordx4 a[16:19], v19, s[8:11], s94 offen offset:0 sc1
Mvaa 224,148,188
Ta 140,22,41184
Ta 142,22,46304
Mvaa 232,148,204
Mvaa 232,144,200
W 8
Ta 144,22,51424
Ta 146,22,56544
Mvaa 232,152,208
buffer_store_dwordx4 v[208:211], v1, s[8:11], s83 offen sc1
K 216,216,217
Mvaa 232,156,212
K 217,218,219
K 218,220,221
Mvaa 224,156,196
K 219,222,223
s_add_u32 s83, s29, 385024
buffer_store_dwordx4 v[216:219], v1, s[8:11], s83 offen sc1
Mvaa 224,152,192
W 6
Ta 148,24,10464
Ta 150,24,15584
Mvaa 228,128,96
Ta 152,24,30944
Ta 154,24,36064
Mvaa 228,132,100
Ta 156,24,41184
Ta 158,24,46304
Mvaa 236,132,116
s_bitcmp1_b32 s92, 4
s_cbranch_scc0 .LL_98%=
buffer_load_dwordx4 a[232:235], v19, s[8:11], s65 offen offset:0 sc1
.LL_98%=:
Mvaa 236,128,112
W 8
s_bitcmp1_b32 s71, 5
s_cselect_b32 s94, s78, s91
Mvaa 236,136,120
Ta 128,23,20736
Ta 130,23,25856
Mvaa 236,140,124
Ta 132,23,51456
Ta 134,23,56576
Mvaa 228,140,108
buffer_load_dwordx4 a[20:23], v19, s[8:11], s94 offen offset:1024 sc1
Mvaa 228,136,104
W 8
Ta 136,25,256
Ta 138,25,5376
Mvaa 228,144,184
Mvaa 228,148,188
Ta 140,21,41216
Ta 142,21,46336
Mvaa 236,148,204
Mvaa 236,144,200
W 8
Ta 144,21,51456
Ta 146,21,56576
Mvaa 236,152,208
Mvaa 236,156,212
s_bitcmp1_b32 s92, 5
s_cbranch_scc0 .LL_99%=
buffer_load_dwordx4 a[236:239], v19, s[8:11], s65 offen offset:1024 sc1
.LL_99%=:
Ta 148,23,10496
Mvaa 228,156,196
s_bitcmp1_b32 s71, 6
s_cselect_b32 s94, s78, s91
Ta 150,23,15616
Mvaa 228,152,192
W 8
Ta 152,23,30976
Ta 154,23,36096
Mvaa 240,128,96
Mvaa 240,132,100
Ta 156,23,41216
Ta 158,23,46336
Mvaa 248,132,116
K 224,224,225
Mvaa 248,128,112
W 8
Ta 128,24,20768
Ta 130,24,25888
Mvaa 248,136,120
buffer_load_dwordx4 a[24:27], v19, s[8:11], s94 offen offset:2048 sc1
K 225,226,227
Mvaa 248,140,124
Ta 132,24,51488
K 226,228,229
Mvaa 240,140,108
Ta 134,24,56608
K 227,230,231
Mvaa 240,136,104
W 8
Ta 136,26,288
Ta 138,26,5408
Mvaa 240,144,184
s_add_u32 s83, s28, 352256
Mvaa 240,148,188
buffer_store_dwordx4 v[224:227], v1, s[8:11], s83 offen sc1
K 232,232,233
Mvaa 248,148,204
K 233,234,235
K 234,236,237
Mvaa 248,144,200
W 6
Ta 140,22,41248
K 235,238,239
Mvaa 248,152,208
s_add_u32 s83, s29, 393216
Ta 142,22,46368
Ta 144,22,51488
Mvaa 248,156,212
buffer_store_dwordx4 v[232:235], v1, s[8:11], s83 offen sc1
Ta 146,22,56608
Mvaa 240,156,196
Ta 148,24,10528
Ta 150,24,15648
Mvaa 240,152,192
W 8
Ta 152,24,31008
Ta 154,24,36128
Mvaa 244,128,96
Mvaa 244,132,100
s_bitcmp1_b32 s92, 6
s_cbranch_scc0 .LL_100%=
buffer_load_dwordx4 a[240:243], v19, s[8:11], s65 offen offset:2048 sc1
.LL_100%=:
Ta 156,24,41248
Mvaa 252,132,116
s_bitcmp1_b32 s71, 7
s_cselect_b32 s94, s78, s91
Ta 158,24,46368
Mvaa 252,128,112
W 8
Mvaa 252,136,120
Mvaa 252,140,124
buffer_load_dwordx4 a[28:31], v19, s[8:11], s94 offen offset:3072 sc1
Mvaa 244,140,108
Mvaa 244,136,104
W 4
Mvaa 244,144,184
Mvaa 244,148,188
s_bitcmp1_b32 s92, 7
s_cbranch_scc0 .LL_101%=
buffer_load_dwordx4 a[244:247], v19, s[8:11], s65 offen offset:3072 sc1
.LL_101%=:
Mvaa 252,148,204
Mvaa 252,144,200
W 0
Mvaa 252,152,208
Mvaa 252,156,212
Mvaa 244,156,196
Mvaa 244,152,192
K 240,240,241
K 241,242,243
s_nop 5
K 242,244,245
K 243,246,247
s_add_u32 s83, s28, 360448
buffer_store_dwordx4 v[240:243], v1, s[8:11], s83 offen sc1
K 248,248,249
K 249,250,251
K 250,252,253
K 251,254,255
s_add_u32 s83, s29, 401408
buffer_store_dwordx4 v[248:251], v1, s[8:11], s83 offen sc1
s_branch .Ltend_85%=
.p2align 12
.Llate_84%=:
ds_read_b32 v11, v11 offset:59392
ds_read_b64 v[8:9], v10 offset:57344
Rv 176,3,0
Rv 180,3,1024
Rv 184,3,2048
Rv 188,3,3072
Rv 192,3,4096
Rv 196,3,5120
Rv 200,3,6144
Rv 204,3,7168
W 6
Mvvv 32,224,176
s_lshl_b32 s92, s58, 2
s_add_u32 s91, s92, s51
s_add_u32 s95, s91, 7
Mvvv 36,228,176
s_and_b32 s95, s95, 15
s_mul_i32 s95, s95, 40960
s_mul_i32 s71, s51, 20480
Mvvv 40,224,180
s_add_u32 s71, s71, 61440
s_add_u32 m0, s71, s48
s_lshl2_add_u32 s94, s48, s95
Mvvv 44,228,180
W 4
v_cmp_ne_u32 vcc, 0, v11
s_cmp_lg_u64 vcc, 0
s_cbranch_scc1 .Lredo%=
A 4,4,8
Mvvv 48,224,184
)"
R"(v_rcp_f32 v4, v4
Mvvv 52,228,184
Ta 128,25,10240
A 5,5,9
Mvvv 56,224,188
v_rcp_f32 v5, v5
Mvvv 60,228,188
W 3
Ta 130,25,15360
D 16,40,94,0
Mvvv 64,224,192
s_add_u32 s95, s91, 9
s_and_b32 s95, s95, 15
s_mul_i32 s95, s95, 40960
Mvvv 68,228,192
s_mul_i32 s71, s51, 10240
s_add_u32 s71, s71, 112640
s_lshl_b32 s93, s58, 12
Mvvv 72,224,196
s_add_u32 s93, s93, s50
U 32,32,4
U 33,33,4
Mvvv 76,228,196
Ta 132,25,20480
Ta 134,25,25600
W 4
Mvvv 80,224,200
U 34,34,4
U 35,35,4
Mvvv 84,228,200
U 36,36,4
U 37,37,4
Mvvv 88,224,204
U 38,38,4
U 39,39,4
K 32,32,33
K 33,34,35
K 34,36,37
K 35,38,39
buffer_store_dwordx4 v[32:35], v1, s[8:11], s93 offen offset:0 sc1
U 40,40,4
U 41,41,4
U 42,42,4
U 43,43,4
U 44,44,4
U 45,45,4
U 46,46,4
U 47,47,4
K 40,40,41
K 41,42,43
K 42,44,45
K 43,46,47
buffer_store_dwordx4 v[40:43], v1, s[8:11], s93 offen offset:1024 sc1
U 64,64,5
U 65,65,5
U 66,66,5
U 67,67,5
U 68,68,5
U 69,69,5
U 70,70,5
U 71,71,5
K 64,64,65
K 65,66,67
K 66,68,69
K 67,70,71
s_add_u32 s67, s93, 131072
buffer_store_dwordx4 v[64:67], v1, s[8:11], s67 offen offset:0 sc1
U 72,72,5
U 73,73,5
U 74,74,5
U 75,75,5
U 76,76,5
U 77,77,5
U 78,78,5
U 79,79,5
K 72,72,73
K 73,74,75
K 74,76,77
K 75,78,79
Mvvv 92,228,204
Ta 136,26,10272
Ta 138,26,15392
Ta 140,26,20512
Ta 142,26,25632
W 4
Zvav 176,128,32
Ta 144,25,10304
Ta 146,25,15424
Mvav 176,132,40
Ta 148,25,20544
Ta 150,25,25664
Zvav 184,132,72
D 17,40,94,1024
Ta 152,26,10336
Mvav 184,128,64
W 5
Ta 154,26,15456
Ta 156,26,20576
Zvav 188,136,64
Ta 158,26,25696
Ta 128,25,10368
Mvav 188,140,72
Ta 130,25,15488
Ta 132,25,20608
Zvav 180,140,40
D 18,40,94,2048
Ta 134,25,25728
Mvav 180,136,32
W 8
Ta 136,26,10400
Ta 138,26,15520
Zvav 192,144,32
Mvav 192,148,40
Ta 140,26,20640
Ta 142,26,25760
Zvav 200,148,72
D 19,40,94,3072
Mvav 200,144,64
W 8
s_add_u32 m0, m0, 4096
Ta 144,25,10432
Zvav 204,152,64
Ta 146,25,15552
Mvav 204,156,72
Ta 148,25,20672
Ta 150,25,25792
Zvav 196,156,40
buffer_load_dwordx4 v20, s[40:43], s94 offen lds
Mvav 196,152,32
W 8
s_add_u32 m0, s71, s48
s_lshl2_add_u32 s94, s48, s95
Zvav 208,128,32
Ta 152,26,10464
Ta 154,26,15584
Mvav 208,132,40
Ta 156,26,20704
Ta 158,26,25824
Zvav 216,132,72
D 16,40,94,0
v_bfe_u32 v28, v1, 4, 2
Mvav 216,128,64
W 8
Ta 128,25,10496
v_lshlrev_b32 v28, 3, v28
Zvav 220,136,64
Ta 130,25,15616
Mvav 220,140,72
Ta 132,25,20736
Ta 134,25,25856
Zvav 212,140,40
buffer_load_dwordx2 v[28:29], v28, s[8:11], s54 offen sc1
Mvav 212,136,32
W 8
Ta 136,26,10528
Ta 138,26,15648
Zvav 224,144,32
Mvav 224,148,40
Ta 140,26,20768
Ta 142,26,25888
Zvav 232,148,72
D 17,40,94,1024
Mvav 232,144,64
W 8
Ta 144,25,30720
Ta 146,25,35840
Zvav 236,152,64
Mvav 236,156,72
Ta 148,21,0
Ta 150,21,5120
Zvav 228,156,40
buffer_store_dwordx4 v[72:75], v1, s[8:11], s67 offen offset:1024 sc1
U 48,48,4
Mvav 228,152,32
W 8
U 49,49,4
U 50,50,4
Zvav 240,128,32
U 51,51,4
U 52,52,4
Mvav 240,132,40
U 53,53,4
U 54,54,4
Zvav 248,132,72
D 18,40,94,2048
U 55,55,4
Mvav 248,128,64
W 4
K 48,48,49
K 49,50,51
Zvav 252,136,64
K 50,52,53
K 51,54,55
Mvav 252,140,72
Ta 152,26,30752
Ta 154,26,35872
Zvav 244,140,40
buffer_store_dwordx4 v[48:51], v1, s[8:11], s93 offen offset:2048 sc1
U 56,56,4
U 57,57,4
U 58,58,4
U 59,59,4
U 60,60,4
U 61,61,4
U 62,62,4
U 63,63,4
K 56,56,57
K 57,58,59
K 58,60,61
K 59,62,63
buffer_store_dwordx4 v[56:59], v1, s[8:11], s93 offen offset:3072 sc1
U 80,80,5
U 81,81,5
U 82,82,5
U 83,83,5
U 84,84,5
U 85,85,5
U 86,86,5
U 87,87,5
K 80,80,81
K 81,82,83
K 82,84,85
K 83,86,87
buffer_store_dwordx4 v[80:83], v1, s[8:11], s67 offen offset:2048 sc1
U 88,88,5
U 89,89,5
U 90,90,5
U 91,91,5
U 92,92,5
U 93,93,5
U 94,94,5
U 95,95,5
K 88,88,89
K 89,90,91
K 90,92,93
K 91,94,95
Mvav 244,136,32
Ta 156,22,32
Ta 158,22,5152
W 4
Mvav 176,144,48
Ta 128,25,30784
Ta 130,25,35904
Mvav 176,148,56
Ta 132,21,64
Ta 134,21,5184
Mvav 184,148,88
D 19,40,94,3072
Ta 136,26,30816
Mvav 184,144,80
s_add_u32 m0, m0, 4096
s_nop 0
buffer_load_dwordx4 v20, s[40:43], s94 offen lds
s_waitcnt vmcnt(8)
v_cmp_eq_u64 vcc, s[12:13], v[28:29]
s_cmp_eq_u64 vcc, -1
s_cbranch_scc0 .Lspin_102%=
.Lok_103%=:
W 5
Mvav 188,152,80
s_add_u32 s81, s58, 0
s_and_b32 s81, s81, 3
s_lshl_b32 s81, s81, 12
Mvav 188,156,88
s_add_u32 s81, s81, s49
Ta 138,26,35936
Ta 140,22,96
Mvav 180,156,56
s_lshl_b32 s66, s14, 1
Ta 142,22,5216
Ta 144,25,30848
Mvav 180,152,48
W 5
buffer_store_dwordx4 v[88:91], v1, s[8:11], s67 offen offset:3072 sc1
Ta 146,25,35968
Mvav 192,128,48
s_add_u32 s66, s66, 0
Ta 148,21,128
Ta 150,21,5248
Mvav 192,132,56
s_cmp_eq_u32 s66, s59
s_cselect_b32 s66, 134217728, 0
Ta 152,26,30880
Ta 154,26,36000
Mvav 200,132,88
s_add_u32 s28, s52, s66
Ta 156,22,160
Ta 158,22,5280
Mvav 200,128,80
W 8
s_lshl_b32 s66, s14, 1
buffer_load_dwordx4 a[184:187], v1, s[8:11], s81 offen offset:0 sc1
Mvav 204,136,80
s_add_u32 s82, s81, 131072
Ta 128,25,30912
Ta 130,25,36032
Mvav 204,140,88
s_add_u32 s66, s66, 1
s_cmp_eq_u32 s66, s59
s_cselect_b32 s66, 134217728, 0
Ta 132,21,192
Mvav 196,140,56
s_add_u32 s29, s52, s66
Ta 134,21,5312
Mvav 196,136,48
W 8
buffer_load_dwordx4 a[200:203], v1, s[8:11], s82 offen offset:0 sc1
Ta 136,26,30944
Mvav 208,144,48
Ta 138,26,36064
Mvav 208,148,56
Ta 140,22,224
Ta 142,22,5344
Mvav 216,148,88
Mvav 216,144,80
W 8
buffer_load_dwordx4 a[188:191], v1, s[8:11], s81 offen offset:1024 sc1
Ta 144,25,30976
Mvav 220,152,80
Ta 146,25,36096
Mvav 220,156,88
Ta 148,21,256
Ta 150,21,5376
Mvav 212,156,56
Mvav 212,152,48
W 8
buffer_load_dwordx4 a[204:207], v1, s[8:11], s82 offen offset:1024 sc1
Ta 152,26,31008
Mvav 224,128,48
Ta 154,26,36128
Mvav 224,132,56
Ta 156,22,288
Ta 158,22,5408
Mvav 232,132,88
Mvav 232,128,80
W 8
buffer_load_dwordx4 a[192:195], v1, s[8:11], s81 offen offset:2048 sc1
Ta 128,25,10240
Mvav 236,136,80
Ta 130,25,15360
Mvav 236,140,88
Ta 132,25,20480
Ta 134,25,25600
Mvav 228,140,56
Mvav 228,136,48
W 8
buffer_load_dwordx4 a[208:211], v1, s[8:11], s82 offen offset:2048 sc1
Ta 136,26,10272
Mvav 240,144,48
Ta 138,26,15392
Mvav 240,148,56
Ta 140,26,20512
Ta 142,26,25632
Mvav 248,148,88
Mvav 248,144,80
W 8
buffer_load_dwordx4 a[196:199], v1, s[8:11], s81 offen offset:3072 sc1
Ta 144,25,10304
Mvav 252,152,80
Ta 146,25,15424
Mvav 252,156,88
Ta 148,25,20544
Ta 150,25,25664
Mvav 244,156,56
Mvav 244,152,48
s_waitcnt vmcnt(3) lgkmcnt(8)
buffer_load_dwordx4 a[212:215], v1, s[8:11], s82 offen offset:3072 sc1
Ta 152,26,10336
Zvaa 96,128,184
Ta 154,26,15456
Mvaa 96,132,188
Ta 156,26,20576
Ta 158,26,25696
Zvaa 104,132,204
Mvaa 104,128,200
W 8
Ta 128,25,10368
Ta 130,25,15488
Zvaa 108,136,200
Mvaa 108,140,204
Ta 132,25,20608
Ta 134,25,25728
Zvaa 100,140,188
s_lshl_b32 s92, s58, 2
s_add_u32 s91, s92, s51
s_add_u32 s95, s91, 11
Mvaa 100,136,184
W 8
s_and_b32 s95, s95, 15
s_mul_i32 s95, s95, 40960
Zvaa 112,144,184
s_mul_i32 s71, s51, 10240
s_add_u32 s71, s71, 40960
s_add_u32 m0, s71, s48
Mvaa 112,148,188
s_lshl2_add_u32 s94, s48, s95
Ta 136,26,10400
Ta 138,26,15520
Zvaa 120,148,204
Ta 140,26,20640
)"
R"(Ta 142,26,25760
Mvaa 120,144,200
W 8
Ta 144,25,10432
Ta 146,25,15552
Zvaa 124,152,200
Mvaa 124,156,204
Ta 148,25,20672
Ta 150,25,25792
Zvaa 116,156,188
Mvaa 116,152,184
W 8
Ta 152,26,10464
Ta 154,26,15584
Zvaa 128,128,184
s_lshl_b32 s80, s58, 3
s_add_u32 s80, s80, s55
v_mov_b64 v[28:29], s[12:13]
s_mov_b64 exec, 1
buffer_store_dwordx2 v[28:29], off, s[8:11], s80 sc1
s_mov_b64 exec, -1
Mvaa 128,132,188
Ta 156,26,20704
Ta 158,26,25824
Zvaa 136,132,204
Mvaa 136,128,200
W 8
Ta 128,25,10496
Ta 130,25,15616
Zvaa 140,136,200
s_barrier
Mvaa 140,140,204
D 16,40,94,0
Ta 132,25,20736
Zvaa 132,140,188
s_add_u32 s95, s91, 13
s_and_b32 s95, s95, 15
s_mul_i32 s95, s95, 40960
Mvaa 132,136,184
W 7
s_mul_i32 s71, s51, 20480
s_add_u32 s71, s71, 71680
Zvaa 144,144,184
Ta 134,25,25856
Ta 136,26,10528
Mvaa 144,148,188
Ta 138,26,15648
D 17,40,94,1024
Zvaa 152,148,204
Ta 140,26,20768
Ta 142,26,25888
Mvaa 152,144,200
W 8
Ta 144,25,30720
Ta 146,25,35840
Zvaa 156,152,200
Mvaa 156,156,204
D 18,40,94,2048
Ta 148,21,0
Zvaa 148,156,188
Ta 150,21,5120
Mvaa 148,152,184
W 8
Ta 152,26,30752
Ta 154,26,35872
Zvaa 160,128,184
Mvaa 160,132,188
D 19,40,94,3072
Ta 156,22,32
Zvaa 168,132,204
s_add_u32 m0, m0, 4096
Ta 158,22,5152
Mvaa 168,128,200
W 8
Ta 128,25,30784
Ta 130,25,35904
Zvaa 172,136,200
Mvaa 172,140,204
buffer_load_dwordx4 v20, s[40:43], s94 offen lds
Ta 132,21,64
Zvaa 164,140,188
s_add_u32 s81, s58, 1
s_and_b32 s81, s81, 3
s_lshl_b32 s81, s81, 12
Mvaa 164,136,184
s_waitcnt vmcnt(6) lgkmcnt(7)
s_add_u32 s81, s81, s49
Ta 134,21,5184
Mvaa 96,144,192
Ta 136,26,30816
Ta 138,26,35936
Mvaa 96,148,196
buffer_load_dwordx4 a[216:219], v1, s[8:11], s81 offen offset:0 sc1
Ta 140,22,96
Mvaa 104,148,212
s_add_u32 s82, s81, 131072
Ta 142,22,5216
Mvaa 104,144,208
W 8
Ta 144,25,30848
Ta 146,25,35968
Mvaa 108,152,208
Mvaa 108,156,212
buffer_load_dwordx4 a[232:235], v1, s[8:11], s82 offen offset:0 sc1
Ta 148,21,128
Mvaa 100,156,196
Ta 150,21,5248
Mvaa 100,152,192
W 8
Ta 152,26,30880
Ta 154,26,36000
Mvaa 112,128,192
Mvaa 112,132,196
buffer_load_dwordx4 a[220:223], v1, s[8:11], s81 offen offset:1024 sc1
Ta 156,22,160
Mvaa 120,132,212
Ta 158,22,5280
Mvaa 120,128,208
W 8
Ta 128,25,30912
Ta 130,25,36032
Mvaa 124,136,208
Mvaa 124,140,212
buffer_load_dwordx4 a[236:239], v1, s[8:11], s82 offen offset:1024 sc1
Ta 132,21,192
Mvaa 116,140,196
Ta 134,21,5312
Mvaa 116,136,192
W 8
Ta 136,26,30944
Ta 138,26,36064
Mvaa 128,144,192
Mvaa 128,148,196
buffer_load_dwordx4 a[224:227], v1, s[8:11], s81 offen offset:2048 sc1
Ta 140,22,224
Mvaa 136,148,212
Ta 142,22,5344
Mvaa 136,144,208
W 8
Ta 144,25,30976
Ta 146,25,36096
Mvaa 140,152,208
Mvaa 140,156,212
buffer_load_dwordx4 a[240:243], v1, s[8:11], s82 offen offset:2048 sc1
Ta 148,21,256
Mvaa 132,156,196
Ta 150,21,5376
Mvaa 132,152,192
W 8
Ta 152,26,31008
Ta 154,26,36128
Mvaa 144,128,192
Mvaa 144,132,196
buffer_load_dwordx4 a[228:231], v1, s[8:11], s81 offen offset:3072 sc1
Ta 156,22,288
Mvaa 152,132,212
Ta 158,22,5408
Mvaa 152,128,208
W 8
Ta 128,21,10240
Ta 130,21,15360
Mvaa 156,136,208
Mvaa 156,140,212
buffer_load_dwordx4 a[244:247], v1, s[8:11], s82 offen offset:3072 sc1
Ta 132,21,20480
Mvaa 148,140,196
s_add_u32 m0, s71, s48
s_lshl2_add_u32 s94, s48, s95
Ta 134,21,25600
Mvaa 148,136,192
W 8
Ta 136,22,10272
Ta 138,22,15392
Mvaa 160,144,192
Mvaa 160,148,196
D 16,40,94,0
Ta 140,22,20512
Mvaa 168,148,212
s_add_u32 s95, s92, 15
s_and_b32 s95, s95, 15
s_mul_i32 s95, s95, 40960
Mvaa 168,144,208
W 7
s_mul_i32 s71, s51, 0
s_add_u32 s71, s71, 102400
Mvaa 172,152,208
Ta 142,22,25632
Ta 144,21,10304
Mvaa 172,156,212
D 17,40,94,1024
Ta 146,21,15424
Mvaa 164,156,196
Ta 148,21,20544
Ta 150,21,25664
Mvaa 164,152,192
s_waitcnt vmcnt(6) lgkmcnt(8)
Ta 152,22,10336
Ta 154,22,15456
Mvaa 96,128,216
Mvaa 96,132,220
D 18,40,94,2048
Ta 156,22,20576
Mvaa 104,132,236
Ta 158,22,25696
Mvaa 104,128,232
W 8
Ta 128,21,10368
Ta 130,21,15488
Mvaa 108,136,232
Mvaa 108,140,236
D 19,40,94,3072
Ta 132,21,20608
Mvaa 100,140,220
s_add_u32 m0, m0, 4096
Ta 134,21,25728
Mvaa 100,136,216
W 8
Ta 136,22,10400
Ta 138,22,15520
Mvaa 112,144,216
Mvaa 112,148,220
buffer_load_dwordx4 v20, s[40:43], s94 offen lds
Ta 140,22,20640
Mvaa 120,148,236
s_add_u32 m0, s71, s48
s_lshl2_add_u32 s94, s48, s95
Ta 142,22,25760
Mvaa 120,144,232
W 8
Ta 144,21,10432
Ta 146,21,15552
Mvaa 124,152,232
Mvaa 124,156,236
D 16,40,94,0
Ta 148,21,20672
Mvaa 116,156,220
D 17,40,94,1024
D 18,40,94,2048
D 19,40,94,3072
s_add_u32 m0, m0, 4096
s_nop 0
buffer_load_dwordx4 v20, s[40:43], s94 offen lds
Mvaa 116,152,216
W 7
s_add_u32 s81, s58, 2
s_and_b32 s81, s81, 3
Mvaa 128,128,216
s_lshl_b32 s81, s81, 12
s_add_u32 s81, s81, s49
Ta 150,21,25792
Mvaa 128,132,220
Ta 152,22,10464
Ta 154,22,15584
Mvaa 136,132,236
Ta 156,22,20704
Ta 158,22,25824
Mvaa 136,128,232
W 8
Ta 128,21,10496
Ta 130,21,15616
Mvaa 140,136,232
s_barrier
Mvaa 140,140,236
buffer_load_dwordx4 a[0:3], v1, s[8:11], s81 offen offset:0 sc1
v_and_b32 v27, 1, v12
Mvaa 132,140,220
s_add_u32 s82, s81, 131072
Ta 132,21,20736
Ta 134,21,25856
Mvaa 132,136,216
W 8
Ta 136,22,10528
v_lshlrev_b32 v27, 1, v27
Mvaa 144,144,216
Ta 138,22,15648
Mvaa 144,148,220
buffer_load_dwordx4 a[16:19], v1, s[8:11], s82 offen offset:0 sc1
v_xor_b32 v27, v27, v13
Mvaa 152,148,236
Ta 140,22,20768
Ta 142,22,25888
Mvaa 152,144,232
W 8
Ta 144,21,30720
v_and_b32 v12, 2, v12
Mvaa 156,152,232
Ta 146,21,35840
Mvaa 156,156,236
buffer_load_dwordx4 a[4:7], v1, s[8:11], s81 offen offset:1024 sc1
Ta 148,23,0
Mvaa 148,156,220
Ta 150,23,5120
v_or_b32 v12, v12, v14
Mvaa 148,152,216
W 8
Ta 152,22,30752
Ta 154,22,35872
Mvaa 160,128,216
v_lshl_or_b32 v12, v27, 2, v12
Mvaa 160,132,220
buffer_load_dwordx4 a[20:23], v1, s[8:11], s82 offen offset:1024 sc1
Ta 156,24,32
Mvaa 168,132,236
Ta 158,24,5152
v_cmp_eq_u32_e64 s[86:87], 0, v12
Mvaa 168,128,232
W 8
Ta 128,21,30784
Ta 130,21,35904
Mvaa 172,136,232
Mvaa 172,140,236
buffer_load_dwordx4 a[8:11], v1, s[8:11], s81 offen offset:2048 sc1
v_cndmask_b32_e64 v4, 0, v15, s[86:87]
Mvaa 164,140,220
Ta 132,23,64
Ta 134,23,5184
Mvaa 164,136,216
s_waitcnt vmcnt(15) lgkmcnt(8)
Ta 136,22,30816
v_cmp_eq_u32_e64 s[86:87], 1, v12
Mvaa 96,144,224
Ta 138,22,35936
Mvaa 96,148,228
buffer_load_dwordx4 a[24:27], v1, s[8:11], s82 offen offset:2048 sc1
v_cndmask_b32_e64 v5, 0, v15, s[86:87]
Mvaa 104,148,244
Ta 140,24,96
Ta 142,24,5216
Mvaa 104,144,240
W 8
Ta 144,21,30848
Ta 146,21,35968
Mvaa 108,152,240
v_cmp_eq_u32_e64 s[86:87], 2, v12
Mvaa 108,156,244
buffer_load_dwordx4 a[12:15], v1, s[8:11], s81 offen offset:3072 sc1
Ta 148,23,128
Mvaa 100,156,228
Ta 150,23,5248
v_cndmask_b32_e64 v6, 0, v15, s[86:87]
Mvaa 100,152,224
W 8
Ta 152,22,30880
Ta 154,22,36000
Mvaa 112,128,224
v_cmp_eq_u32_e64 s[86:87], 3, v12
Mvaa 112,132,228
buffer_load_dwordx4 a[28:31], v1, s[8:11], s82 offen offset:3072 sc1
Ta 156,24,160
Mvaa 120,132,244
s_add_u32 s81, s58, 3
s_and_b32 s81, s81, 3
s_lshl_b32 s81, s81, 12
Mvaa 120,128,240
W 7
s_add_u32 s81, s81, s49
Ta 158,24,5280
Mvaa 124,136,240
Ta 128,21,30912
v_cndmask_b32_e64 v7, 0, v15, s[86:87]
Mvaa 124,140,244
Ta 130,21,36032
buffer_load_dwordx4 a[32:35], v1, s[8:11], s81 offen offset:0 sc1
Mvaa 116,140,228
s_add_u32 s82, s81, 131072
Ta 132,23,192
v_cmp_eq_u32_e64 s[86:87], 4, v12
Mvaa 116,136,224
W 7
Ta 134,23,5312
v_cndmask_b32_e64 v8, 0, v15, s[86:87]
Mvaa 128,144,224
Ta 136,22,30944
Ta 138,22,36064
Mvaa 128,148,228
buffer_load_dwordx4 a[48:51], v1, s[8:11], s82 offen offset:0 sc1
Ta 140,24,224
Mvaa 136,148,244
Ta 142,24,5344
v_cmp_eq_u32_e64 s[86:87], 5, v12
Mvaa 136,144,240
W 8
Ta 144,21,30976
Ta 146,21,36096
Mvaa 140,152,240
s_waitcnt vmcnt(10)
s_barrier
Mvaa 140,156,244
buffer_load_dwordx4 a[36:39], v1, s[8:11], s81 offen offset:1024 sc1
v_cndmask_b32_e64 v9, 0, v15, s[86:87]
Mvaa 132,156,228
Ta 148,23,256
Ta 150,23,5376
Mvaa 132,152,224
W 8
Ta 152,22,31008
v_cmp_eq_u32_e64 s[86:87], 6, v12
Mvaa 144,128,224
Ta 154,22,36128
Mvaa 144,132,228
)"
R"(buffer_load_dwordx4 a[52:55], v1, s[8:11], s82 offen offset:1024 sc1
v_cndmask_b32_e64 v10, 0, v15, s[86:87]
Mvaa 152,132,244
Ta 156,24,288
Ta 158,24,5408
Mvaa 152,128,240
W 8
Ta 128,23,20480
v_cmp_eq_u32_e64 s[86:87], 7, v12
Mvaa 156,136,240
Ta 130,23,25600
Mvaa 156,140,244
buffer_load_dwordx4 a[40:43], v1, s[8:11], s81 offen offset:2048 sc1
v_cndmask_b32_e64 v11, 0, v15, s[86:87]
Mvaa 148,140,228
Ta 132,23,51200
Ta 134,23,56320
Mvaa 148,136,224
W 8
Ta 136,24,20512
Ta 138,24,25632
Mvaa 160,144,224
s_mov_b32 s20, s38
Mvaa 160,148,228
buffer_load_dwordx4 a[56:59], v1, s[8:11], s82 offen offset:2048 sc1
Ta 140,24,51232
Mvaa 168,148,244
s_and_b32 s21, s39, 0xffff
Ta 142,24,56352
Mvaa 168,144,240
W 8
Ta 144,23,20544
Ta 146,23,25664
Mvaa 172,152,240
s_mov_b32 s22, 2621440
Mvaa 172,156,244
buffer_load_dwordx4 a[44:47], v1, s[8:11], s81 offen offset:3072 sc1
Ta 148,23,51264
Mvaa 164,156,228
Ta 150,23,56384
Mvaa 164,152,224
s_waitcnt vmcnt(11) lgkmcnt(8)
s_mov_b32 s23, 0x20000
Ta 152,24,20576
Mvaa 96,128,0
Ta 154,24,25696
Mvaa 96,132,4
s_and_b32 s17, s17, 0xffff
buffer_load_dwordx4 a[60:63], v1, s[8:11], s82 offen offset:3072 sc1
Ta 156,24,51296
Mvaa 104,132,20
Ta 158,24,56416
Mvaa 104,128,16
W 8
s_mov_b32 s18, 2621440
Ta 128,23,20608
Mvaa 108,136,16
Ta 130,23,25728
Mvaa 108,140,20
Ta 132,23,51328
Ta 134,23,56448
Mvaa 100,140,4
s_mov_b32 s19, 0x20000
Mvaa 100,136,0
W 8
Ta 136,24,20640
Ta 138,24,25760
Mvaa 112,144,0
s_lshr_b32 s66, s15, 3
v_bfe_u32 v28, v1, 4, 2
Mvaa 112,148,4
Ta 140,24,51360
v_lshlrev_b32 v28, 3, v28
Mvaa 120,148,20
s_lshl_b32 s66, s66, 4
Ta 142,24,56480
buffer_load_dwordx2 v[28:29], v28, s[8:11], s55 offen sc1
Mvaa 120,144,16
W 8
Ta 144,23,20672
Ta 146,23,25792
Mvaa 124,152,16
Mvaa 124,156,20
s_add_u32 s66, s66, s59
Ta 148,23,51392
Ta 150,23,56512
Mvaa 116,156,4
Mvaa 116,152,0
W 8
s_lshl_b32 s63, s66, 6
Ta 152,24,20704
Mvaa 128,128,0
Ta 154,24,25824
Mvaa 128,132,4
s_add_u32 s63, s63, 65536
Ta 156,24,51424
Ta 158,24,56544
Mvaa 136,132,20
Mvaa 136,128,16
W 8
s_mul_i32 s62, s66, 40960
Ta 128,23,20736
Mvaa 140,136,16
Ta 130,23,25856
Mvaa 140,140,20
Ta 132,23,51456
Ta 134,23,56576
Mvaa 132,140,4
s_add_u32 s62, s62, 56623104
Mvaa 132,136,0
W 8
Ta 136,24,20768
Ta 138,24,25888
Mvaa 144,144,0
s_lshl_b32 s66, s14, 13
Mvaa 144,148,4
Ta 140,24,51488
Ta 142,24,56608
Mvaa 152,148,20
s_add_u32 s78, s62, s66
Mvaa 152,144,16
W 8
Ta 144,25,0
Ta 146,25,5120
Mvaa 156,152,16
Mvaa 156,156,20
s_add_u32 s79, s78, 327680
Ta 148,21,40960
Ta 150,21,46080
Mvaa 148,156,4
Mvaa 148,152,0
W 8
s_and_b32 s66, s14, 1
Ta 152,26,32
Mvaa 160,128,0
Ta 154,26,5152
Mvaa 160,132,4
s_mul_i32 s66, s66, 327680
Ta 156,22,40992
Ta 158,22,46112
Mvaa 168,132,20
Mvaa 168,128,16
W 8
Ta 128,25,64
Ta 130,25,5184
Mvaa 172,136,16
s_add_u32 s65, s62, s66
Mvaa 172,140,20
Ta 132,21,41024
Ta 134,21,46144
Mvaa 164,140,4
s_add_u32 s65, s65, 32768
Mvaa 164,136,0
s_waitcnt vmcnt(9) lgkmcnt(8)
Ta 136,26,96
Ta 138,26,5216
Mvaa 96,144,8
s_mov_b32 s64, 0
Mvaa 96,148,12
Ta 140,22,41056
Ta 142,22,46176
Mvaa 104,148,28
Mvaa 104,144,24
W 8
Ta 144,25,128
v_add_u32 v19, 4096, v1
Mvaa 108,152,24
Ta 146,25,5248
Mvaa 108,156,28
Ta 148,21,41088
v_bfe_u32 v27, v1, 4, 3
Mvaa 100,156,12
Ta 150,21,46208
Mvaa 100,152,8
W 8
Ta 152,26,160
v_lshlrev_b32 v27, 3, v27
Mvaa 112,128,8
Ta 154,26,5280
Mvaa 112,132,12
Ta 156,22,41120
Ta 158,22,46240
Mvaa 120,132,28
s_waitcnt vmcnt(0)
v_cmp_eq_u64 vcc, s[12:13], v[28:29]
s_cmp_eq_u64 vcc, -1
s_cbranch_scc0 .Lspin_105%=
.Lok_106%=:
s_add_u32 s81, s58, 1
v_bfe_u32 v18, v1, 7, 1
Mvaa 120,128,24
W 8
s_and_b32 s81, s81, 3
s_lshl_b32 s81, s81, 12
Mvaa 124,136,24
s_add_u32 s81, s81, s50
buffer_load_dwordx4 a[64:67], v1, s[8:11], s81 offen offset:0 sc1
Ta 128,25,192
Mvaa 124,140,28
s_add_u32 s82, s81, 131072
s_add_u32 s66, s14, 1
Ta 130,25,5312
Mvaa 116,140,12
s_lshr_b32 s66, s66, 2
Ta 132,21,41152
Ta 134,21,46272
Mvaa 116,136,8
W 8
Ta 136,26,224
Ta 138,26,5344
Mvaa 128,144,8
buffer_load_dwordx4 a[80:83], v1, s[8:11], s82 offen offset:0 sc1
Mvaa 128,148,12
s_mul_i32 s66, s66, 0
Ta 140,22,41184
Ta 142,22,46304
Mvaa 136,148,28
Mvaa 136,144,24
W 8
s_sub_u32 s66, 512, s66
Ta 144,25,256
Mvaa 140,152,24
Ta 146,25,5376
Mvaa 140,156,28
buffer_load_dwordx4 a[68:71], v1, s[8:11], s81 offen offset:1024 sc1
v_mul_u32_u24 v18, s66, v18
Mvaa 132,156,12
Ta 148,21,41216
Ta 150,21,46336
Mvaa 132,152,8
W 8
Ta 152,26,288
Ta 154,26,5408
Mvaa 144,128,8
v_add_u32 v18, v18, v27
Mvaa 144,132,12
Ta 156,22,41248
Ta 158,22,46368
Mvaa 152,132,28
s_mov_b32 s71, 0
buffer_load_dwordx4 a[84:87], v1, s[8:11], s82 offen offset:1024 sc1
Mvaa 152,128,24
W 8
Ta 128,21,51200
Ta 130,21,56320
Mvaa 156,136,24
v_bfe_u32 v20, v1, 4, 4
Mvaa 156,140,28
Ta 132,23,10240
Ta 134,23,15360
Mvaa 148,140,12
Mvaa 148,136,8
W 8
buffer_load_dwordx4 a[72:75], v1, s[8:11], s81 offen offset:2048 sc1
v_mul_u32_u24 v20, 1280, v20
Mvaa 160,144,8
Ta 136,23,30720
Ta 138,23,35840
Mvaa 160,148,12
Ta 140,23,40960
v_lshrrev_b32 v12, 8, v1
Mvaa 168,148,28
Ta 142,23,46080
Mvaa 168,144,24
W 8
Ta 144,22,51232
v_lshl_add_u32 v20, v12, 4, v20
Mvaa 172,152,24
Ta 146,22,56352
buffer_load_dwordx4 a[88:91], v1, s[8:11], s82 offen offset:2048 sc1
Mvaa 172,156,28
Ta 148,24,10272
Ta 150,24,15392
Mvaa 164,156,12
s_bfe_u32 s66, s59, 0x10001
Mvaa 164,152,8
W 8
Ta 152,24,30752
Ta 154,24,35872
Mvaa 96,128,32
s_lshl_b32 s67, s57, 1
Mvaa 96,132,36
buffer_load_dwordx4 a[76:79], v1, s[8:11], s81 offen offset:3072 sc1
Ta 156,24,40992
Mvaa 104,132,52
s_add_u32 s66, s66, s67
Ta 158,24,46112
Mvaa 104,128,48
W 8
Ta 128,21,51264
Ta 130,21,56384
Mvaa 104,136,56
Mvaa 104,140,60
s_lshl_b32 s61, s66, 2
Ta 132,23,10304
Ta 134,23,15424
Mvaa 96,140,44
buffer_load_dwordx4 a[92:95], v1, s[8:11], s82 offen offset:3072 sc1
Mvaa 96,136,40
W 8
s_add_u32 s81, s58, 2
s_and_b32 s81, s81, 3
Mvaa 100,144,32
s_lshl_b32 s81, s81, 12
s_add_u32 s81, s81, s50
s_lshr_b32 s66, s59, 2
Mvaa 100,148,36
s_lshl_b32 s66, s66, 1
Ta 136,23,30784
Ta 138,23,35904
Mvaa 108,148,52
Ta 140,23,41024
Ta 142,23,46144
Mvaa 108,144,48
W 8
buffer_load_dwordx4 a[96:99], v1, s[8:11], s81 offen offset:0 sc1
Ta 144,22,51296
Mvaa 108,152,56
s_add_u32 s82, s81, 131072
s_add_u32 s61, s61, s66
Ta 146,22,56416
Mvaa 108,156,60
Ta 148,24,10336
Ta 150,24,15456
Mvaa 100,156,44
s_and_b32 s66, s59, 1
Mvaa 100,152,40
W 8
Ta 152,24,30816
Ta 154,24,35936
Mvaa 112,128,32
s_add_u32 s61, s61, s66
buffer_load_dwordx4 a[112:115], v1, s[8:11], s82 offen offset:0 sc1
Mvaa 112,132,36
Ta 156,24,41056
Ta 158,24,46176
Mvaa 120,132,52
s_xor_b32 s67, s61, 0
K 96,96,97
K 97,98,99
Mvaa 120,128,48
W 8
Ta 128,21,51328
Ta 130,21,56448
Mvaa 120,136,56
K 98,100,101
Mvaa 120,140,60
s_mul_i32 s72, s67, 20480
buffer_load_dwordx4 a[100:103], v1, s[8:11], s81 offen offset:1024 sc1
K 99,102,103
Mvaa 112,140,44
Ta 132,23,10368
Ta 134,23,15488
Mvaa 112,136,40
W 8
s_add_u32 s83, s28, 0
s_mul_i32 s66, s58, 320
Mvaa 116,144,32
Ta 136,23,30848
Ta 138,23,35968
Mvaa 116,148,36
s_add_u32 s72, s72, s66
buffer_store_dwordx4 v[96:99], v1, s[8:11], s83 offen sc1
Ta 140,23,41088
Mvaa 124,148,52
Ta 142,23,46208
K 104,104,105
Mvaa 124,144,48
W 8
Ta 144,22,51360
K 105,106,107
Mvaa 124,152,56
s_mul_i32 s75, s67, 20480
Ta 146,22,56480
Mvaa 124,156,60
buffer_load_dwordx4 a[116:119], v1, s[8:11], s82 offen offset:1024 sc1
K 106,108,109
Mvaa 116,156,44
s_mul_i32 s66, s58, 2560
Ta 148,24,10400
Ta 150,24,15520
Mvaa 116,152,40
W 8
Ta 152,24,30880
K 107,110,111
Mvaa 128,128,32
s_add_u32 s83, s29, 40960
s_add_u32 s75, s75, s66
Ta 154,24,36000
Mvaa 128,132,36
buffer_load_dwordx4 a[104:107], v1, s[8:11], s81 offen offset:2048 sc1
Ta 156,24,41120
Mvaa 136,132,52
Ta 158,24,46240
Mvaa 136,128,48
W 8
s_add_u32 s75, s75, 131072
Ta 128,21,51392
Mvaa 136,136,56
Ta 130,21,56512
)"
R"(Mvaa 136,140,60
s_xor_b32 s67, s61, 124
buffer_store_dwordx4 v[104:107], v1, s[8:11], s83 offen sc1
K 112,112,113
Mvaa 128,140,44
Ta 132,23,10432
K 113,114,115
Mvaa 128,136,40
W 7
Ta 134,23,15552
K 114,116,117
Mvaa 132,144,32
s_mul_i32 s73, s67, 20480
Ta 136,23,30912
Ta 138,23,36032
Mvaa 132,148,36
buffer_load_dwordx4 a[120:123], v1, s[8:11], s82 offen offset:2048 sc1
K 115,118,119
Mvaa 140,148,52
s_add_u32 s83, s28, 8192
Ta 140,23,41152
Ta 142,23,46272
Mvaa 140,144,48
W 8
s_mul_i32 s66, s58, 320
Ta 144,22,51424
Mvaa 140,152,56
s_add_u32 s73, s73, s66
Ta 146,22,56544
Mvaa 140,156,60
buffer_load_dwordx4 a[108:111], v1, s[8:11], s81 offen offset:3072 sc1
Ta 148,24,10464
Mvaa 132,156,44
s_mul_i32 s76, s67, 20480
Ta 150,24,15584
Mvaa 132,152,40
W 8
Ta 152,24,30944
Ta 154,24,36064
Mvaa 144,128,32
Mvaa 144,132,36
s_mul_i32 s66, s58, 2560
buffer_store_dwordx4 v[112:115], v1, s[8:11], s83 offen sc1
K 120,120,121
Mvaa 152,132,52
K 121,122,123
K 122,124,125
Mvaa 152,128,48
W 6
Ta 156,24,41184
K 123,126,127
Mvaa 152,136,56
s_add_u32 s83, s29, 49152
s_add_u32 s76, s76, s66
Ta 158,24,46304
Mvaa 152,140,60
s_add_u32 s76, s76, 131072
buffer_load_dwordx4 a[124:127], v1, s[8:11], s82 offen offset:3072 sc1
Ta 128,21,51456
Mvaa 144,140,44
s_add_u32 s81, s58, 3
s_and_b32 s81, s81, 3
s_lshl_b32 s81, s81, 12
Mvaa 144,136,40
W 5
s_add_u32 s81, s81, s50
Ta 130,21,56576
Mvaa 148,144,32
Ta 132,23,10496
Ta 134,23,15616
Mvaa 148,148,36
buffer_load_dwordx4 a[184:187], v1, s[8:11], s81 offen offset:0 sc1
Ta 136,23,30976
Mvaa 156,148,52
s_add_u32 s82, s81, 131072
Ta 138,23,36096
v_bfe_u32 v15, v1, 7, 1
Mvaa 156,144,48
W 6
Ta 140,23,41216
Ta 142,23,46336
Mvaa 156,152,56
Ta 144,22,51488
v_mul_u32_u24 v14, 10240, v15
Mvaa 156,156,60
buffer_store_dwordx4 v[120:123], v1, s[8:11], s83 offen sc1
K 128,128,129
Mvaa 148,156,44
K 129,130,131
K 130,132,133
Mvaa 148,152,40
W 5
Ta 146,22,56608
K 131,134,135
Mvaa 160,128,32
s_add_u32 s83, s28, 16384
Ta 148,24,10528
v_lshrrev_b32 v15, 8, v1
Mvaa 160,132,36
buffer_load_dwordx4 a[200:203], v1, s[8:11], s82 offen offset:0 sc1
Ta 150,24,15648
Mvaa 168,132,52
Ta 152,24,31008
v_lshl_add_u32 v14, v15, 7, v14
Mvaa 168,128,48
W 5
Ta 154,24,36128
v_and_b32 v15, 0x70, v1
Mvaa 168,136,56
Ta 156,24,41248
Ta 158,24,46368
Mvaa 168,140,60
buffer_load_dwordx4 a[188:191], v1, s[8:11], s81 offen offset:1024 sc1
v_add_u32 v14, v14, v15
Mvaa 160,140,44
s_bitcmp1_b32 s14, 0
s_cselect_b32 s74, s73, s72
s_cselect_b32 s77, s76, s75
Ta 128,21,10240
Ta 130,21,15360
Mvaa 160,136,40
W 6
Ta 132,21,20480
Ta 134,21,25600
Mvaa 164,144,32
s_add_u32 s74, s74, 256
Ta 136,22,10272
Ta 138,22,15392
Mvaa 164,148,36
buffer_store_dwordx4 v[128:131], v1, s[8:11], s83 offen sc1
K 136,136,137
Mvaa 172,148,52
s_add_u32 s77, s77, 2048
K 137,138,139
K 138,140,141
Mvaa 172,144,48
W 6
Ta 140,22,20512
K 139,142,143
Mvaa 172,152,56
s_add_u32 s83, s29, 57344
Ta 142,22,25632
Ta 144,21,10304
Mvaa 172,156,60
s_lshl_b32 s66, s14, 6
buffer_load_dwordx4 a[204:207], v1, s[8:11], s82 offen offset:1024 sc1
Ta 146,21,15424
Mvaa 164,156,44
Ta 148,21,20544
Ta 150,21,25664
Mvaa 164,152,40
s_waitcnt vmcnt(21) lgkmcnt(8)
s_lshl_b32 s67, s14, 9
Ta 152,22,10336
Mvaa 176,128,64
Ta 154,22,15456
Mvaa 176,132,68
s_add_u32 s72, s72, s66
buffer_load_dwordx4 a[192:195], v1, s[8:11], s81 offen offset:2048 sc1
Ta 156,22,20576
Mvaa 184,132,84
Ta 158,22,25696
Mvaa 184,128,80
W 8
Ta 128,21,10368
Ta 130,21,15488
Mvaa 188,136,80
s_add_u32 s75, s75, s67
Mvaa 188,140,84
buffer_store_dwordx4 v[136:139], v1, s[8:11], s83 offen sc1
K 144,144,145
Mvaa 180,140,68
s_add_u32 s73, s73, s66
K 145,146,147
K 146,148,149
Mvaa 180,136,64
W 6
Ta 132,21,20608
K 147,150,151
Mvaa 192,144,64
s_add_u32 s83, s28, 24576
Ta 134,21,25728
Ta 136,22,10400
Mvaa 192,148,68
s_add_u32 s76, s76, s67
buffer_load_dwordx4 a[208:211], v1, s[8:11], s82 offen offset:2048 sc1
Ta 138,22,15520
Mvaa 200,148,84
Ta 140,22,20640
Ta 142,22,25760
Mvaa 200,144,80
W 8
Ta 144,21,10432
Ta 146,21,15552
Mvaa 204,152,80
Mvaa 204,156,84
buffer_store_dwordx4 v[144:147], v1, s[8:11], s83 offen sc1
K 152,152,153
Mvaa 196,156,68
K 153,154,155
K 154,156,157
Mvaa 196,152,64
W 6
Ta 148,21,20672
K 155,158,159
Mvaa 208,128,64
s_add_u32 s83, s29, 65536
Ta 150,21,25792
Ta 152,22,10464
Mvaa 208,132,68
buffer_load_dwordx4 a[196:199], v1, s[8:11], s81 offen offset:3072 sc1
Ta 154,22,15584
Mvaa 216,132,84
Ta 156,22,20704
Ta 158,22,25824
Mvaa 216,128,80
W 8
Ta 128,21,10496
Ta 130,21,15616
Mvaa 220,136,80
Mvaa 220,140,84
buffer_load_dwordx4 a[212:215], v1, s[8:11], s82 offen offset:3072 sc1
Ta 132,21,20736
Mvaa 212,140,68
Ta 134,21,25856
Mvaa 212,136,64
W 8
Ta 136,22,10528
Ta 138,22,15648
Mvaa 224,144,64
Mvaa 224,148,68
buffer_store_dwordx4 v[152:155], v1, s[8:11], s83 offen sc1
K 160,160,161
Mvaa 232,148,84
K 161,162,163
K 162,164,165
Mvaa 232,144,80
W 6
Ta 140,22,20768
K 163,166,167
Mvaa 236,152,80
s_add_u32 s83, s28, 32768
Ta 142,22,25888
Ta 144,21,30720
Mvaa 236,156,84
buffer_load_dwordx4 v[36:39], v20, s[16:19], s72 offen
Ta 146,21,35840
Mvaa 228,156,68
Ta 148,23,0
Ta 150,23,5120
Mvaa 228,152,64
W 8
Ta 152,22,30752
Ta 154,22,35872
Mvaa 240,128,64
Mvaa 240,132,68
buffer_load_dwordx4 v[44:47], v14, s[8:11], s75 offen sc1
Ta 156,24,32
Mvaa 248,132,84
Ta 158,24,5152
Mvaa 248,128,80
W 8
Ta 128,21,30784
Ta 130,21,35904
Mvaa 252,136,80
Mvaa 252,140,84
buffer_store_dwordx4 v[160:163], v1, s[8:11], s83 offen sc1
K 168,168,169
Mvaa 244,140,68
K 169,170,171
K 170,172,173
Mvaa 244,136,64
s_waitcnt vmcnt(27) lgkmcnt(6)
Ta 132,23,64
K 171,174,175
Mvaa 176,144,72
s_add_u32 s83, s29, 73728
Ta 134,23,5184
Ta 136,22,30816
Mvaa 176,148,76
buffer_load_dwordx4 v[52:55], v20, s[16:19], s73 offen
Ta 138,22,35936
Mvaa 184,148,92
Ta 140,24,96
Ta 142,24,5216
Mvaa 184,144,88
W 8
Ta 144,21,30848
Ta 146,21,35968
Mvaa 188,152,88
Mvaa 188,156,92
buffer_store_dwordx4 v[168:171], v1, s[8:11], s83 offen sc1
Ta 148,23,128
Mvaa 180,156,76
Ta 150,23,5248
Mvaa 180,152,72
W 8
Ta 152,22,30880
Ta 154,22,36000
Mvaa 192,128,72
Mvaa 192,132,76
buffer_load_dwordx4 v[60:63], v14, s[8:11], s76 offen sc1
Ta 156,24,160
Mvaa 200,132,92
Ta 158,24,5280
Mvaa 200,128,88
W 8
Ta 128,21,30912
Ta 130,21,36032
Mvaa 204,136,88
Mvaa 204,140,92
buffer_load_dwordx4 v[68:71], v20, s[16:19], s74 offen
Ta 132,23,192
Mvaa 196,140,76
Ta 134,23,5312
Mvaa 196,136,72
W 8
Ta 136,22,30944
Ta 138,22,36064
Mvaa 208,144,72
Mvaa 208,148,76
buffer_load_dwordx4 v[76:79], v14, s[8:11], s77 offen sc1
Ta 140,24,224
Mvaa 216,148,92
Ta 142,24,5344
Mvaa 216,144,88
W 8
Ta 144,21,30976
Ta 146,21,36096
Mvaa 220,152,88
Mvaa 220,156,92
buffer_load_dwordx2 v[12:13], v27, s[8:11], 0 offen offset:3584 sc1
Ta 148,23,256
Mvaa 212,156,76
Ta 150,23,5376
Mvaa 212,152,72
W 8
Ta 152,22,31008
Ta 154,22,36128
Mvaa 224,128,72
Mvaa 224,132,76
Ta 156,24,288
Ta 158,24,5408
Mvaa 232,132,92
Mvaa 232,128,88
W 8
Ta 128,23,20480
Ta 130,23,25600
Mvaa 236,136,88
Mvaa 236,140,92
Ta 132,23,51200
Ta 134,23,56320
Mvaa 228,140,76
Mvaa 228,136,72
W 8
Ta 136,25,0
Ta 138,25,5120
Mvaa 240,144,72
Mvaa 240,148,76
Ta 140,21,40960
Ta 142,21,46080
Mvaa 248,148,92
Mvaa 248,144,88
W 8
Ta 144,21,51200
Ta 146,21,56320
Mvaa 252,152,88
Mvaa 252,156,92
Ta 148,23,10240
Ta 150,23,15360
Mvaa 244,156,76
Mvaa 244,152,72
s_waitcnt vmcnt(28) lgkmcnt(8)
Ta 152,23,30720
Ta 154,23,35840
Mvaa 176,128,96
Mvaa 176,132,100
Ta 156,23,40960
Ta 158,23,46080
Mvaa 184,132,116
Mvaa 184,128,112
s_waitcnt vmcnt(22) lgkmcnt(8)
Ta 128,24,20512
Ta 130,24,25632
Mvaa 184,136,120
Mvaa 184,140,124
Ta 132,24,51232
Ta 134,24,56352
Mvaa 176,140,108
s_barrier
Mvaa 176,136,104
s_waitcnt vmcnt(16) lgkmcnt(8)
Ta 136,26,32
Ta 138,26,5152
Mvaa 176,144,184
Mvaa 176,148,188
Ta 140,22,40992
Ta 142,22,46112
Mvaa 184,148,204
s_waitcnt vmcnt(4)
)"
R"(v_lshlrev_b32 v2, 2, v1
v_mov_b64 v[28:29], s[12:13]
s_mov_b64 exec, 3
buffer_store_dwordx2 v[28:29], v2, s[8:11], s53 offen offset:0 sc1
s_mov_b64 exec, -1
Mvaa 184,144,200
W 8
Ta 144,22,51232
Ta 146,22,56352
Mvaa 184,152,208
Mvaa 184,156,212
Ta 148,24,10272
Ta 150,24,15392
Mvaa 176,156,196
Mvaa 176,152,192
W 8
Ta 152,24,30752
Ta 154,24,35872
Mvaa 180,128,96
Mvaa 180,132,100
Ta 156,24,40992
Ta 158,24,46112
Mvaa 188,132,116
Mvaa 188,128,112
W 8
Ta 128,23,20544
Ta 130,23,25664
Mvaa 188,136,120
Mvaa 188,140,124
Ta 132,23,51264
Ta 134,23,56384
Mvaa 180,140,108
Mvaa 180,136,104
W 8
Ta 136,25,64
Ta 138,25,5184
Mvaa 180,144,184
Mvaa 180,148,188
Ta 140,21,41024
Ta 142,21,46144
Mvaa 188,148,204
Mvaa 188,144,200
W 8
Ta 144,21,51264
Ta 146,21,56384
Mvaa 188,152,208
Mvaa 188,156,212
Ta 148,23,10304
Ta 150,23,15424
Mvaa 180,156,196
s_waitcnt vmcnt(1)
v_xor_b32 v12, s12, v12
v_xor_b32 v13, s13, v13
v_lshrrev_b32 v3, 11, v12
v_or_b32 v13, v13, v3
v_cmp_eq_u32 vcc, 0, v13
s_cmp_eq_u64 vcc, -1
s_cbranch_scc0 .LL_110%=
.LL_111%=:
v_bfe_u32 v13, v12, 4, 7
s_xor_b32 s66, s61, 124
v_cmp_eq_u32 vcc, s61, v13
v_cmp_eq_u32_e64 s[86:87], s66, v13
s_or_b32 s68, vcc_lo, s86
s_and_b32 s68, s68, 255
s_cbranch_scc1 .Lup_108%=
.Lupr_109%=:
Mvaa 180,152,192
W 8
Ta 152,23,30784
Ta 154,23,35904
Mvaa 192,128,96
v_lshlrev_b32 v32, 16, v36
Mvaa 192,132,100
Ta 156,23,41024
v_and_b32 v33, 0xffff0000, v36
Mvaa 200,132,116
Ta 158,23,46144
v_lshlrev_b32 v34, 16, v37
Mvaa 200,128,112
W 8
K 176,176,177
v_and_b32 v35, 0xffff0000, v37
Mvaa 200,136,120
Ta 128,24,20576
Ta 130,24,25696
Mvaa 200,140,124
buffer_load_dwordx2 v[16:17], v18, s[8:11], s63 offen sc1
v_lshlrev_b32 v36, 16, v38
Mvaa 192,140,108
K 177,178,179
v_and_b32 v37, 0xffff0000, v38
Mvaa 192,136,104
W 6
Ta 132,24,51296
K 178,180,181
Mvaa 192,144,184
Ta 134,24,56416
v_lshlrev_b32 v38, 16, v39
Mvaa 192,148,188
K 179,182,183
v_and_b32 v39, 0xffff0000, v39
Mvaa 200,148,204
s_add_u32 s83, s28, 327680
Ta 136,26,96
Ta 138,26,5216
Mvaa 200,144,200
W 6
v_lshlrev_b32 v40, 16, v44
v_and_b32 v41, 0xffff0000, v44
Mvaa 200,152,208
buffer_store_dwordx4 v[176:179], v1, s[8:11], s83 offen sc1
K 184,184,185
Mvaa 200,156,212
K 185,186,187
K 186,188,189
Mvaa 192,156,196
K 187,190,191
s_add_u32 s83, s29, 368640
buffer_store_dwordx4 v[184:187], v1, s[8:11], s83 offen sc1
Mvaa 192,152,192
Ta 140,22,41056
Ta 142,22,46176
W 4
Mvaa 196,128,96
v_lshlrev_b32 v42, 16, v45
v_and_b32 v43, 0xffff0000, v45
Mvaa 196,132,100
Ta 144,22,51296
Ta 146,22,56416
Mvaa 204,132,116
v_lshlrev_b32 v44, 16, v46
v_and_b32 v45, 0xffff0000, v46
Mvaa 204,128,112
Ta 148,24,10336
Ta 150,24,15456
W 4
Mvaa 204,136,120
v_lshlrev_b32 v46, 16, v47
v_and_b32 v47, 0xffff0000, v47
Mvaa 204,140,124
Ta 152,24,30816
Ta 154,24,35936
Mvaa 196,140,108
v_lshlrev_b32 v48, 16, v52
v_and_b32 v49, 0xffff0000, v52
Mvaa 196,136,104
Ta 156,24,41056
Ta 158,24,46176
W 4
Mvaa 196,144,184
v_lshlrev_b32 v50, 16, v53
v_and_b32 v51, 0xffff0000, v53
Mvaa 196,148,188
Ta 128,23,20608
Ta 130,23,25728
Mvaa 204,148,204
v_lshlrev_b32 v52, 16, v54
v_and_b32 v53, 0xffff0000, v54
Mvaa 204,144,200
Ta 132,23,51328
Ta 134,23,56448
W 4
Mvaa 204,152,208
v_lshlrev_b32 v54, 16, v55
v_and_b32 v55, 0xffff0000, v55
Mvaa 204,156,212
Ta 136,25,128
Ta 138,25,5248
Mvaa 196,156,196
v_lshlrev_b32 v56, 16, v60
v_and_b32 v57, 0xffff0000, v60
Mvaa 196,152,192
Ta 140,21,41088
Ta 142,21,46208
W 4
Mvaa 208,128,96
v_lshlrev_b32 v58, 16, v61
v_and_b32 v59, 0xffff0000, v61
Mvaa 208,132,100
Ta 144,21,51328
Ta 146,21,56448
Mvaa 216,132,116
v_lshlrev_b32 v60, 16, v62
v_and_b32 v61, 0xffff0000, v62
Mvaa 216,128,112
Ta 148,23,10368
Ta 150,23,15488
W 4
Mvaa 216,136,120
v_lshlrev_b32 v62, 16, v63
v_and_b32 v63, 0xffff0000, v63
Mvaa 216,140,124
Ta 152,23,30848
Ta 154,23,35968
Mvaa 208,140,108
v_lshlrev_b32 v64, 16, v68
v_and_b32 v65, 0xffff0000, v68
Mvaa 208,136,104
Ta 156,23,41088
Ta 158,23,46208
W 4
Mvaa 208,144,184
v_lshlrev_b32 v66, 16, v69
K 192,192,193
Mvaa 208,148,188
Ta 128,24,20640
v_and_b32 v67, 0xffff0000, v69
Mvaa 216,148,204
Ta 130,24,25760
K 193,194,195
Mvaa 216,144,200
Ta 132,24,51360
Ta 134,24,56480
W 4
Mvaa 216,152,208
v_lshlrev_b32 v68, 16, v70
K 194,196,197
Mvaa 216,156,212
v_and_b32 v69, 0xffff0000, v70
v_lshlrev_b32 v70, 16, v71
Mvaa 208,156,196
K 195,198,199
s_add_u32 s83, s28, 335872
buffer_store_dwordx4 v[192:195], v1, s[8:11], s83 offen sc1
K 200,200,201
K 201,202,203
K 202,204,205
K 203,206,207
s_add_u32 s83, s29, 376832
buffer_store_dwordx4 v[200:203], v1, s[8:11], s83 offen sc1
Mvaa 208,152,192
Ta 136,26,160
Ta 138,26,5280
Ta 140,22,41120
Ta 142,22,46240
W 4
Mvaa 212,128,96
s_waitcnt vmcnt(4)
v_cmp_eq_u64 vcc, s[12:13], v[16:17]
s_and_b32 s71, vcc_lo, 255
s_bitset0_b32 s71, s59
v_and_b32 v71, 0xffff0000, v71
v_lshlrev_b32 v72, 16, v76
Mvaa 212,132,100
s_lshl_b32 s92, s14, 3
s_lshr_b32 s92, s71, s92
s_mov_b32 s91, 134217728
Mvaa 220,132,116
s_bitcmp1_b32 s71, 0
s_cselect_b32 s94, s78, s91
v_and_b32 v73, 0xffff0000, v76
Mvaa 220,128,112
Ta 144,22,51360
Ta 146,22,56480
Ta 148,24,10400
Ta 150,24,15520
W 4
Mvaa 220,136,120
v_lshlrev_b32 v74, 16, v77
v_and_b32 v75, 0xffff0000, v77
Mvaa 220,140,124
v_lshlrev_b32 v76, 16, v78
v_and_b32 v77, 0xffff0000, v78
Mvaa 212,140,108
v_lshlrev_b32 v78, 16, v79
v_and_b32 v79, 0xffff0000, v79
Mvaa 212,136,104
Ta 152,24,30880
Ta 154,24,36000
Ta 156,24,41120
Ta 158,24,46240
W 4
Mvaa 212,144,184
Ta 128,23,20672
Ta 130,23,25792
Mvaa 212,148,188
buffer_load_dwordx4 a[0:3], v1, s[8:11], s94 offen offset:0 sc1
Ta 132,23,51392
Mvaa 220,148,204
Ta 134,23,56512
Ta 136,25,192
Mvaa 220,144,200
W 5
Ta 138,25,5312
Ta 140,21,41152
Mvaa 220,152,208
Ta 142,21,46272
Ta 144,21,51392
Mvaa 220,156,212
s_bitcmp1_b32 s92, 0
s_cbranch_scc0 .LL_113%=
buffer_load_dwordx4 a[216:219], v1, s[8:11], s65 offen offset:0 sc1
.LL_113%=:
Ta 146,21,56512
Mvaa 212,156,196
s_bitcmp1_b32 s71, 1
s_cselect_b32 s94, s78, s91
Ta 148,23,10432
Mvaa 212,152,192
W 7
Ta 150,23,15552
Ta 152,23,30912
Mvaa 224,128,96
Ta 154,23,36032
Mvaa 224,132,100
buffer_load_dwordx4 a[4:7], v1, s[8:11], s94 offen offset:1024 sc1
Ta 156,23,41152
Mvaa 232,132,116
Ta 158,23,46272
K 208,208,209
Mvaa 232,128,112
W 8
Ta 128,24,20704
Ta 130,24,25824
Mvaa 232,136,120
K 209,210,211
Mvaa 232,140,124
s_bitcmp1_b32 s92, 1
s_cbranch_scc0 .LL_114%=
buffer_load_dwordx4 a[220:223], v1, s[8:11], s65 offen offset:1024 sc1
.LL_114%=:
K 210,212,213
Mvaa 224,140,108
s_bitcmp1_b32 s71, 2
s_cselect_b32 s94, s78, s91
Ta 132,24,51424
Mvaa 224,136,104
W 7
Ta 134,24,56544
K 211,214,215
Mvaa 224,144,184
s_add_u32 s83, s28, 344064
Ta 136,26,224
Ta 138,26,5344
Mvaa 224,148,188
buffer_load_dwordx4 a[8:11], v1, s[8:11], s94 offen offset:2048 sc1
Ta 140,22,41184
Mvaa 232,148,204
Ta 142,22,46304
Mvaa 232,144,200
W 8
Ta 144,22,51424
Ta 146,22,56544
Mvaa 232,152,208
Mvaa 232,156,212
buffer_store_dwordx4 v[208:211], v1, s[8:11], s83 offen sc1
K 216,216,217
Mvaa 224,156,196
K 217,218,219
K 218,220,221
K 219,222,223
s_add_u32 s83, s29, 385024
buffer_store_dwordx4 v[216:219], v1, s[8:11], s83 offen sc1
Mvaa 224,152,192
W 6
Ta 148,24,10464
Ta 150,24,15584
Mvaa 228,128,96
Ta 152,24,30944
Ta 154,24,36064
Mvaa 228,132,100
Ta 156,24,41184
Ta 158,24,46304
Mvaa 236,132,116
s_bitcmp1_b32 s92, 2
s_cbranch_scc0 .LL_115%=
buffer_load_dwordx4 a[224:227], v1, s[8:11], s65 offen offset:2048 sc1
.LL_115%=:
Mvaa 236,128,112
W 8
s_bitcmp1_b32 s71, 3
s_cselect_b32 s94, s78, s91
Mvaa 236,136,120
Ta 128,23,20736
Ta 130,23,25856
Mvaa 236,140,124
Ta 132,23,51456
Ta 134,23,56576
Mvaa 228,140,108
buffer_load_dwordx4 a[12:15], v1, s[8:11], s94 offen offset:3072 sc1
Mvaa 228,136,104
W 8
Ta 136,25,256
Ta 138,25,5376
Mvaa 228,144,184
Mvaa 228,148,188
Ta 140,21,41216
Ta 142,21,46336
Mvaa 236,148,204
s_bitcmp1_b32 s92, 3
s_cbranch_scc0 .LL_116%=
buffer_load_dwordx4 a[228:231], v1, s[8:11], s65 offen offset:3072 sc1
.LL_116%=:
Mvaa 236,144,200
W 8
s_bitcmp1_b32 s71, 4
s_cselect_b32 s94, s78, s91
Mvaa 236,152,208
Ta 144,21,51456
)"
R"(Ta 146,21,56576
Mvaa 236,156,212
Ta 148,23,10496
Ta 150,23,15616
Mvaa 228,156,196
buffer_load_dwordx4 a[16:19], v19, s[8:11], s94 offen offset:0 sc1
Mvaa 228,152,192
W 8
Ta 152,23,30976
Ta 154,23,36096
Mvaa 240,128,96
Mvaa 240,132,100
Ta 156,23,41216
Ta 158,23,46336
Mvaa 248,132,116
s_bitcmp1_b32 s92, 4
s_cbranch_scc0 .LL_117%=
buffer_load_dwordx4 a[232:235], v19, s[8:11], s65 offen offset:0 sc1
.LL_117%=:
K 224,224,225
Mvaa 248,128,112
W 8
s_bitcmp1_b32 s71, 5
s_cselect_b32 s94, s78, s91
Mvaa 248,136,120
Ta 128,24,20768
Ta 130,24,25888
Mvaa 248,140,124
K 225,226,227
K 226,228,229
Mvaa 240,140,108
buffer_load_dwordx4 a[20:23], v19, s[8:11], s94 offen offset:1024 sc1
Ta 132,24,51488
Mvaa 240,136,104
W 7
Ta 134,24,56608
K 227,230,231
Mvaa 240,144,184
s_add_u32 s83, s28, 352256
Ta 136,26,288
Ta 138,26,5408
Mvaa 240,148,188
Ta 140,22,41248
Ta 142,22,46368
Mvaa 248,148,204
s_bitcmp1_b32 s92, 5
s_cbranch_scc0 .LL_118%=
buffer_load_dwordx4 a[236:239], v19, s[8:11], s65 offen offset:1024 sc1
.LL_118%=:
Mvaa 248,144,200
W 8
s_bitcmp1_b32 s71, 6
s_cselect_b32 s94, s78, s91
Mvaa 248,152,208
Ta 144,22,51488
Ta 146,22,56608
Mvaa 248,156,212
Ta 148,24,10528
Ta 150,24,15648
Mvaa 240,156,196
buffer_store_dwordx4 v[224:227], v1, s[8:11], s83 offen sc1
K 232,232,233
K 233,234,235
K 234,236,237
K 235,238,239
s_add_u32 s83, s29, 393216
buffer_store_dwordx4 v[232:235], v1, s[8:11], s83 offen sc1
Mvaa 240,152,192
W 8
Ta 152,24,31008
Ta 154,24,36128
Mvaa 244,128,96
Mvaa 244,132,100
Ta 156,24,41248
Ta 158,24,46368
Mvaa 252,132,116
buffer_load_dwordx4 a[24:27], v19, s[8:11], s94 offen offset:2048 sc1
Mvaa 252,128,112
W 8
Mvaa 252,136,120
Mvaa 252,140,124
Mvaa 244,140,108
s_bitcmp1_b32 s92, 6
s_cbranch_scc0 .LL_119%=
buffer_load_dwordx4 a[240:243], v19, s[8:11], s65 offen offset:2048 sc1
.LL_119%=:
Mvaa 244,136,104
W 4
s_bitcmp1_b32 s71, 7
s_cselect_b32 s94, s78, s91
Mvaa 244,144,184
Mvaa 244,148,188
Mvaa 252,148,204
buffer_load_dwordx4 a[28:31], v19, s[8:11], s94 offen offset:3072 sc1
Mvaa 252,144,200
W 0
Mvaa 252,152,208
Mvaa 252,156,212
Mvaa 244,156,196
s_bitcmp1_b32 s92, 7
s_cbranch_scc0 .LL_120%=
buffer_load_dwordx4 a[244:247], v19, s[8:11], s65 offen offset:3072 sc1
.LL_120%=:
Mvaa 244,152,192
K 240,240,241
K 241,242,243
s_nop 5
K 242,244,245
K 243,246,247
s_add_u32 s83, s28, 360448
buffer_store_dwordx4 v[240:243], v1, s[8:11], s83 offen sc1
K 248,248,249
K 249,250,251
K 250,252,253
K 251,254,255
s_add_u32 s83, s29, 401408
buffer_store_dwordx4 v[248:251], v1, s[8:11], s83 offen sc1
.Ltend_85%=:
buffer_load_dwordx2 v[16:17], v18, s[8:11], s63 offen sc1
s_bitcmp1_b32 s28, 27
s_cbranch_scc0 .LL_121%=
ds_write_b128 v1, v[96:99] offset:0
ds_write_b128 v1, v[112:115] offset:1024
ds_write_b128 v1, v[128:131] offset:2048
ds_write_b128 v1, v[144:147] offset:3072
ds_write_b128 v1, v[160:163] offset:4096
ds_write_b128 v1, v[176:179] offset:5120
ds_write_b128 v1, v[192:195] offset:6144
ds_write_b128 v1, v[208:211] offset:7168
ds_write_b128 v1, v[224:227] offset:8192
ds_write_b128 v1, v[240:243] offset:9216
W 0
.LL_121%=:
s_bitcmp1_b32 s29, 27
s_cbranch_scc0 .LL_122%=
ds_write_b128 v1, v[104:107] offset:0
ds_write_b128 v1, v[120:123] offset:1024
ds_write_b128 v1, v[136:139] offset:2048
ds_write_b128 v1, v[152:155] offset:3072
ds_write_b128 v1, v[168:171] offset:4096
ds_write_b128 v1, v[184:187] offset:5120
ds_write_b128 v1, v[200:203] offset:6144
ds_write_b128 v1, v[216:219] offset:7168
ds_write_b128 v1, v[232:235] offset:8192
ds_write_b128 v1, v[248:251] offset:9216
W 0
.LL_122%=:
s_lshl_b32 s80, s14, 10
v_add_u32 v21, s80, v1
s_cmp_lg_u32 s68, 0
s_cbranch_scc1 .Lup_123%=
.Lupr_124%=:
s_lshl_b32 s66, 0x101, s59
s_waitcnt vmcnt(1)
s_cmp_eq_u32 s64, 0
s_cbranch_scc0 .LL_129%=
v_lshlrev_b32 v2, 2, v1
v_mov_b64 v[28:29], s[12:13]
s_mov_b64 exec, 3
buffer_store_dwordx2 v[28:29], v2, s[8:11], s53 offen offset:512 sc1
s_mov_b64 exec, -1
.LL_129%=:
s_barrier
s_waitcnt vmcnt(1)
v_cmp_eq_u64 vcc, s[12:13], v[16:17]
s_or_b32 s67, vcc_lo, s66
s_cmp_eq_u32 s14, 0
s_cbranch_scc1 .LL_131%=
s_cmp_eq_u32 s14, 1
s_cbranch_scc1 .LL_132%=
s_mov_b32 s84, 0
.LL_133%=:
s_or_b32 s70, s71, s66
s_andn2_b32 s69, s67, s71
s_and_b32 s69, s69, 0xffff
s_cbranch_scc0 .LL_136%=
s_and_b32 s80, s69, 255
s_cbranch_scc0 .LL_137%=
s_bitcmp1_b32 s69, 0
s_cbranch_scc0 .LL_138%=
s_cmp_eq_u32 s59, 0
s_cbranch_scc0 .LL_139%=
Ra 0,21,0
s_branch .LL_138%=
.LL_139%=:
buffer_load_dwordx4 a[0:3], v1, s[8:11], s78 offen offset:0 sc1
.LL_138%=:
s_bitcmp1_b32 s69, 1
s_cbranch_scc0 .LL_140%=
s_cmp_eq_u32 s59, 1
s_cbranch_scc0 .LL_141%=
Ra 4,21,0
s_branch .LL_140%=
.LL_141%=:
buffer_load_dwordx4 a[4:7], v1, s[8:11], s78 offen offset:1024 sc1
.LL_140%=:
s_bitcmp1_b32 s69, 2
s_cbranch_scc0 .LL_142%=
s_cmp_eq_u32 s59, 2
s_cbranch_scc0 .LL_143%=
Ra 8,21,0
s_branch .LL_142%=
.LL_143%=:
buffer_load_dwordx4 a[8:11], v1, s[8:11], s78 offen offset:2048 sc1
.LL_142%=:
s_bitcmp1_b32 s69, 3
s_cbranch_scc0 .LL_144%=
s_cmp_eq_u32 s59, 3
s_cbranch_scc0 .LL_145%=
Ra 12,21,0
s_branch .LL_144%=
.LL_145%=:
buffer_load_dwordx4 a[12:15], v1, s[8:11], s78 offen offset:3072 sc1
.LL_144%=:
s_bitcmp1_b32 s69, 4
s_cbranch_scc0 .LL_146%=
s_cmp_eq_u32 s59, 4
s_cbranch_scc0 .LL_147%=
Ra 16,21,0
s_branch .LL_146%=
.LL_147%=:
buffer_load_dwordx4 a[16:19], v19, s[8:11], s78 offen offset:0 sc1
.LL_146%=:
s_bitcmp1_b32 s69, 5
s_cbranch_scc0 .LL_148%=
s_cmp_eq_u32 s59, 5
s_cbranch_scc0 .LL_149%=
Ra 20,21,0
s_branch .LL_148%=
.LL_149%=:
buffer_load_dwordx4 a[20:23], v19, s[8:11], s78 offen offset:1024 sc1
.LL_148%=:
s_bitcmp1_b32 s69, 6
s_cbranch_scc0 .LL_150%=
s_cmp_eq_u32 s59, 6
s_cbranch_scc0 .LL_151%=
Ra 24,21,0
s_branch .LL_150%=
.LL_151%=:
buffer_load_dwordx4 a[24:27], v19, s[8:11], s78 offen offset:2048 sc1
.LL_150%=:
s_bitcmp1_b32 s69, 7
s_cbranch_scc0 .LL_152%=
s_cmp_eq_u32 s59, 7
s_cbranch_scc0 .LL_153%=
Ra 28,21,0
s_branch .LL_152%=
.LL_153%=:
buffer_load_dwordx4 a[28:31], v19, s[8:11], s78 offen offset:3072 sc1
.LL_152%=:
.LL_137%=:
s_and_b32 s80, s69, 65280
s_cbranch_scc0 .LL_154%=
s_bitcmp1_b32 s69, 8
s_cbranch_scc0 .LL_155%=
s_cmp_eq_u32 s59, 0
s_cbranch_scc0 .LL_156%=
Ra 32,21,5120
s_branch .LL_155%=
.LL_156%=:
buffer_load_dwordx4 a[32:35], v1, s[8:11], s79 offen offset:0 sc1
.LL_155%=:
s_bitcmp1_b32 s69, 9
s_cbranch_scc0 .LL_157%=
s_cmp_eq_u32 s59, 1
s_cbranch_scc0 .LL_158%=
Ra 36,21,5120
s_branch .LL_157%=
.LL_158%=:
buffer_load_dwordx4 a[36:39], v1, s[8:11], s79 offen offset:1024 sc1
.LL_157%=:
s_bitcmp1_b32 s69, 10
s_cbranch_scc0 .LL_159%=
s_cmp_eq_u32 s59, 2
s_cbranch_scc0 .LL_160%=
Ra 40,21,5120
s_branch .LL_159%=
.LL_160%=:
buffer_load_dwordx4 a[40:43], v1, s[8:11], s79 offen offset:2048 sc1
.LL_159%=:
s_bitcmp1_b32 s69, 11
s_cbranch_scc0 .LL_161%=
s_cmp_eq_u32 s59, 3
s_cbranch_scc0 .LL_162%=
Ra 44,21,5120
s_branch .LL_161%=
.LL_162%=:
buffer_load_dwordx4 a[44:47], v1, s[8:11], s79 offen offset:3072 sc1
.LL_161%=:
s_bitcmp1_b32 s69, 12
s_cbranch_scc0 .LL_163%=
s_cmp_eq_u32 s59, 4
s_cbranch_scc0 .LL_164%=
Ra 48,21,5120
s_branch .LL_163%=
.LL_164%=:
buffer_load_dwordx4 a[48:51], v19, s[8:11], s79 offen offset:0 sc1
.LL_163%=:
s_bitcmp1_b32 s69, 13
s_cbranch_scc0 .LL_165%=
s_cmp_eq_u32 s59, 5
s_cbranch_scc0 .LL_166%=
Ra 52,21,5120
s_branch .LL_165%=
.LL_166%=:
buffer_load_dwordx4 a[52:55], v19, s[8:11], s79 offen offset:1024 sc1
.LL_165%=:
s_bitcmp1_b32 s69, 14
s_cbranch_scc0 .LL_167%=
s_cmp_eq_u32 s59, 6
s_cbranch_scc0 .LL_168%=
Ra 56,21,5120
s_branch .LL_167%=
.LL_168%=:
buffer_load_dwordx4 a[56:59], v19, s[8:11], s79 offen offset:2048 sc1
.LL_167%=:
s_bitcmp1_b32 s69, 15
s_cbranch_scc0 .LL_169%=
s_cmp_eq_u32 s59, 7
s_cbranch_scc0 .LL_170%=
Ra 60,21,5120
s_branch .LL_169%=
.LL_170%=:
buffer_load_dwordx4 a[60:63], v19, s[8:11], s79 offen offset:3072 sc1
.LL_169%=:
.LL_154%=:
s_or_b32 s71, s71, s69
.LL_136%=:
s_andn2_b32 s80, 0xffff, s71
s_cbranch_scc0 .LL_135%=
s_sleep 4
buffer_load_dwordx2 v[16:17], v18, s[8:11], s63 offen sc1
s_and_b32 s80, s70, 0x100ff
s_cmp_eq_u32 s80, 0xff
s_cbranch_scc0 .LL_134%=
W 0
Zvva 80,4,0
Zvva 84,8,0
Mvva 80,4,4
Mvva 84,8,4
Mvva 80,4,8
Mvva 84,8,8
Mvva 80,4,12
Mvva 84,8,12
Mvva 80,4,16
Mvva 84,8,16
Mvva 80,4,20
Mvva 84,8,20
Mvva 80,4,24
Mvva 84,8,24
Mvva 80,4,28
Mvva 84,8,28
s_nop 6
K 160,80,81
K 161,82,83
K 162,84,85
K 163,86,87
v_lshlrev_b32 v80, 16, v160
v_and_b32 v81, 0xffff0000, v160
v_lshlrev_b32 v82, 16, v161
v_and_b32 v83, 0xffff0000, v161
v_lshlrev_b32 v84, 16, v162
v_and_b32 v85, 0xffff0000, v162
v_lshlrev_b32 v86, 16, v163
v_and_b32 v87, 0xffff0000, v163
v_pk_add_f32 v[80:81], v[32:33], v[80:81]
v_pk_add_f32 v[82:83], v[34:35], v[82:83]
v_pk_add_f32 v[84:85], v[36:37], v[84:85]
v_pk_add_f32 v[86:87], v[38:39], v[86:87]
K 160,80,81
K 161,82,83
K 162,84,85
K 163,86,87
v_lshlrev_b32 v80, 16, v160
v_and_b32 v81, 0xffff0000, v160
v_lshlrev_b32 v82, 16, v161
v_and_b32 v83, 0xffff0000, v161
v_lshlrev_b32 v84, 16, v162
v_and_b32 v85, 0xffff0000, v162
v_lshlrev_b32 v86, 16, v163
v_and_b32 v87, 0xffff0000, v163
v_pk_add_f32 v[80:81], v[40:41], v[80:81]
v_pk_add_f32 v[82:83], v[42:43], v[82:83]
v_pk_add_f32 v[84:85], v[44:45], v[84:85]
v_pk_add_f32 v[86:87], v[46:47], v[86:87]
K 80,80,81
K 81,82,83
K 82,84,85
K 83,86,87
buffer_store_dwordx4 v[80:83], v20, s[20:23], s72 offen nt
s_bitset1_b32 s71, 16
.LL_134%=:
s_add_u32 s84, s84, 1
s_waitcnt vmcnt(0)
v_cmp_eq_u64 vcc, s[12:13], v[16:17]
s_or_b32 s67, vcc_lo, s66
s_cmp_lt_u32 s84, 4194304
s_cbranch_scc1 .LL_133%=
.LL_135%=:
s_bitcmp1_b32 s71, 16
)"
R"(s_cbranch_scc1 .LL_171%=
s_and_b32 s80, s70, 0xff
s_cmp_eq_u32 s80, 0xff
s_cbranch_scc1 .LL_172%=
s_waitcnt vmcnt(0)
.LL_172%=:
W 0
Zvva 80,4,0
Zvva 84,8,0
Mvva 80,4,4
Mvva 84,8,4
Mvva 80,4,8
Mvva 84,8,8
Mvva 80,4,12
Mvva 84,8,12
Mvva 80,4,16
Mvva 84,8,16
Mvva 80,4,20
Mvva 84,8,20
Mvva 80,4,24
Mvva 84,8,24
Mvva 80,4,28
Mvva 84,8,28
s_nop 6
K 160,80,81
K 161,82,83
K 162,84,85
K 163,86,87
v_lshlrev_b32 v80, 16, v160
v_and_b32 v81, 0xffff0000, v160
v_lshlrev_b32 v82, 16, v161
v_and_b32 v83, 0xffff0000, v161
v_lshlrev_b32 v84, 16, v162
v_and_b32 v85, 0xffff0000, v162
v_lshlrev_b32 v86, 16, v163
v_and_b32 v87, 0xffff0000, v163
v_pk_add_f32 v[80:81], v[32:33], v[80:81]
v_pk_add_f32 v[82:83], v[34:35], v[82:83]
v_pk_add_f32 v[84:85], v[36:37], v[84:85]
v_pk_add_f32 v[86:87], v[38:39], v[86:87]
K 160,80,81
K 161,82,83
K 162,84,85
K 163,86,87
v_lshlrev_b32 v80, 16, v160
v_and_b32 v81, 0xffff0000, v160
v_lshlrev_b32 v82, 16, v161
v_and_b32 v83, 0xffff0000, v161
v_lshlrev_b32 v84, 16, v162
v_and_b32 v85, 0xffff0000, v162
v_lshlrev_b32 v86, 16, v163
v_and_b32 v87, 0xffff0000, v163
v_pk_add_f32 v[80:81], v[40:41], v[80:81]
v_pk_add_f32 v[82:83], v[42:43], v[82:83]
v_pk_add_f32 v[84:85], v[44:45], v[84:85]
v_pk_add_f32 v[86:87], v[46:47], v[86:87]
K 80,80,81
K 81,82,83
K 82,84,85
K 83,86,87
buffer_store_dwordx4 v[80:83], v20, s[20:23], s72 offen nt
s_waitcnt vmcnt(1)
W 0
Zvva 80,4,32
Zvva 84,8,32
Mvva 80,4,36
Mvva 84,8,36
Mvva 80,4,40
Mvva 84,8,40
Mvva 80,4,44
Mvva 84,8,44
Mvva 80,4,48
Mvva 84,8,48
Mvva 80,4,52
Mvva 84,8,52
Mvva 80,4,56
Mvva 84,8,56
Mvva 80,4,60
Mvva 84,8,60
s_nop 6
K 160,80,81
K 161,82,83
K 162,84,85
K 163,86,87
v_lshlrev_b32 v80, 16, v160
v_and_b32 v81, 0xffff0000, v160
v_lshlrev_b32 v82, 16, v161
v_and_b32 v83, 0xffff0000, v161
v_lshlrev_b32 v84, 16, v162
v_and_b32 v85, 0xffff0000, v162
v_lshlrev_b32 v86, 16, v163
v_and_b32 v87, 0xffff0000, v163
v_pk_add_f32 v[80:81], v[48:49], v[80:81]
v_pk_add_f32 v[82:83], v[50:51], v[82:83]
v_pk_add_f32 v[84:85], v[52:53], v[84:85]
v_pk_add_f32 v[86:87], v[54:55], v[86:87]
K 160,80,81
K 161,82,83
K 162,84,85
K 163,86,87
v_lshlrev_b32 v80, 16, v160
v_and_b32 v81, 0xffff0000, v160
v_lshlrev_b32 v82, 16, v161
v_and_b32 v83, 0xffff0000, v161
v_lshlrev_b32 v84, 16, v162
v_and_b32 v85, 0xffff0000, v162
v_lshlrev_b32 v86, 16, v163
v_and_b32 v87, 0xffff0000, v163
v_pk_add_f32 v[80:81], v[56:57], v[80:81]
v_pk_add_f32 v[82:83], v[58:59], v[82:83]
v_pk_add_f32 v[84:85], v[60:61], v[84:85]
v_pk_add_f32 v[86:87], v[62:63], v[86:87]
K 80,80,81
K 81,82,83
K 82,84,85
K 83,86,87
buffer_store_dwordx4 v[80:83], v20, s[20:23], s73 offen nt
s_branch .LL_130%=
.LL_171%=:
s_waitcnt vmcnt(0)
W 0
Zvva 80,4,32
Zvva 84,8,32
Mvva 80,4,36
Mvva 84,8,36
Mvva 80,4,40
Mvva 84,8,40
Mvva 80,4,44
Mvva 84,8,44
Mvva 80,4,48
Mvva 84,8,48
Mvva 80,4,52
Mvva 84,8,52
Mvva 80,4,56
Mvva 84,8,56
Mvva 80,4,60
Mvva 84,8,60
s_nop 6
K 160,80,81
K 161,82,83
K 162,84,85
K 163,86,87
v_lshlrev_b32 v80, 16, v160
v_and_b32 v81, 0xffff0000, v160
v_lshlrev_b32 v82, 16, v161
v_and_b32 v83, 0xffff0000, v161
v_lshlrev_b32 v84, 16, v162
v_and_b32 v85, 0xffff0000, v162
v_lshlrev_b32 v86, 16, v163
v_and_b32 v87, 0xffff0000, v163
v_pk_add_f32 v[80:81], v[48:49], v[80:81]
v_pk_add_f32 v[82:83], v[50:51], v[82:83]
v_pk_add_f32 v[84:85], v[52:53], v[84:85]
v_pk_add_f32 v[86:87], v[54:55], v[86:87]
K 160,80,81
K 161,82,83
K 162,84,85
K 163,86,87
v_lshlrev_b32 v80, 16, v160
v_and_b32 v81, 0xffff0000, v160
v_lshlrev_b32 v82, 16, v161
v_and_b32 v83, 0xffff0000, v161
v_lshlrev_b32 v84, 16, v162
v_and_b32 v85, 0xffff0000, v162
v_lshlrev_b32 v86, 16, v163
v_and_b32 v87, 0xffff0000, v163
v_pk_add_f32 v[80:81], v[56:57], v[80:81]
v_pk_add_f32 v[82:83], v[58:59], v[82:83]
v_pk_add_f32 v[84:85], v[60:61], v[84:85]
v_pk_add_f32 v[86:87], v[62:63], v[86:87]
K 80,80,81
K 81,82,83
K 82,84,85
K 83,86,87
buffer_store_dwordx4 v[80:83], v20, s[20:23], s73 offen nt
s_branch .LL_130%=
.LL_131%=:
s_mov_b32 s84, 0
.LL_173%=:
s_or_b32 s70, s71, s66
s_andn2_b32 s69, s67, s71
s_and_b32 s69, s69, 0xffff
s_cbranch_scc0 .LL_176%=
s_and_b32 s80, s69, 255
s_cbranch_scc0 .LL_177%=
s_bitcmp1_b32 s69, 0
s_cbranch_scc0 .LL_178%=
s_cmp_eq_u32 s59, 0
s_cbranch_scc0 .LL_179%=
Ra 0,21,0
Ra 216,1,4096
s_branch .LL_178%=
.LL_179%=:
buffer_load_dwordx4 a[0:3], v1, s[8:11], s78 offen offset:0 sc1
buffer_load_dwordx4 a[216:219], v1, s[8:11], s65 offen offset:0 sc1
.LL_178%=:
s_bitcmp1_b32 s69, 1
s_cbranch_scc0 .LL_180%=
s_cmp_eq_u32 s59, 1
s_cbranch_scc0 .LL_181%=
Ra 4,21,0
Ra 220,1,4096
s_branch .LL_180%=
.LL_181%=:
buffer_load_dwordx4 a[4:7], v1, s[8:11], s78 offen offset:1024 sc1
buffer_load_dwordx4 a[220:223], v1, s[8:11], s65 offen offset:1024 sc1
.LL_180%=:
s_bitcmp1_b32 s69, 2
s_cbranch_scc0 .LL_182%=
s_cmp_eq_u32 s59, 2
s_cbranch_scc0 .LL_183%=
Ra 8,21,0
Ra 224,1,4096
s_branch .LL_182%=
.LL_183%=:
buffer_load_dwordx4 a[8:11], v1, s[8:11], s78 offen offset:2048 sc1
buffer_load_dwordx4 a[224:227], v1, s[8:11], s65 offen offset:2048 sc1
.LL_182%=:
s_bitcmp1_b32 s69, 3
s_cbranch_scc0 .LL_184%=
s_cmp_eq_u32 s59, 3
s_cbranch_scc0 .LL_185%=
Ra 12,21,0
Ra 228,1,4096
s_branch .LL_184%=
.LL_185%=:
buffer_load_dwordx4 a[12:15], v1, s[8:11], s78 offen offset:3072 sc1
buffer_load_dwordx4 a[228:231], v1, s[8:11], s65 offen offset:3072 sc1
.LL_184%=:
s_bitcmp1_b32 s69, 4
s_cbranch_scc0 .LL_186%=
s_cmp_eq_u32 s59, 4
s_cbranch_scc0 .LL_187%=
Ra 16,21,0
Ra 232,1,4096
s_branch .LL_186%=
.LL_187%=:
buffer_load_dwordx4 a[16:19], v19, s[8:11], s78 offen offset:0 sc1
buffer_load_dwordx4 a[232:235], v19, s[8:11], s65 offen offset:0 sc1
.LL_186%=:
s_bitcmp1_b32 s69, 5
s_cbranch_scc0 .LL_188%=
s_cmp_eq_u32 s59, 5
s_cbranch_scc0 .LL_189%=
Ra 20,21,0
Ra 236,1,4096
s_branch .LL_188%=
.LL_189%=:
buffer_load_dwordx4 a[20:23], v19, s[8:11], s78 offen offset:1024 sc1
buffer_load_dwordx4 a[236:239], v19, s[8:11], s65 offen offset:1024 sc1
.LL_188%=:
s_bitcmp1_b32 s69, 6
s_cbranch_scc0 .LL_190%=
s_cmp_eq_u32 s59, 6
s_cbranch_scc0 .LL_191%=
Ra 24,21,0
Ra 240,1,4096
s_branch .LL_190%=
.LL_191%=:
buffer_load_dwordx4 a[24:27], v19, s[8:11], s78 offen offset:2048 sc1
buffer_load_dwordx4 a[240:243], v19, s[8:11], s65 offen offset:2048 sc1
.LL_190%=:
s_bitcmp1_b32 s69, 7
s_cbranch_scc0 .LL_192%=
s_cmp_eq_u32 s59, 7
s_cbranch_scc0 .LL_193%=
Ra 28,21,0
Ra 244,1,4096
s_branch .LL_192%=
.LL_193%=:
buffer_load_dwordx4 a[28:31], v19, s[8:11], s78 offen offset:3072 sc1
buffer_load_dwordx4 a[244:247], v19, s[8:11], s65 offen offset:3072 sc1
.LL_192%=:
.LL_177%=:
s_and_b32 s80, s69, 65280
s_cbranch_scc0 .LL_194%=
s_bitcmp1_b32 s69, 8
s_cbranch_scc0 .LL_195%=
s_cmp_eq_u32 s59, 0
s_cbranch_scc0 .LL_196%=
Ra 32,21,5120
s_branch .LL_195%=
.LL_196%=:
buffer_load_dwordx4 a[32:35], v1, s[8:11], s79 offen offset:0 sc1
.LL_195%=:
s_bitcmp1_b32 s69, 9
s_cbranch_scc0 .LL_197%=
s_cmp_eq_u32 s59, 1
s_cbranch_scc0 .LL_198%=
Ra 36,21,5120
s_branch .LL_197%=
.LL_198%=:
buffer_load_dwordx4 a[36:39], v1, s[8:11], s79 offen offset:1024 sc1
.LL_197%=:
s_bitcmp1_b32 s69, 10
s_cbranch_scc0 .LL_199%=
s_cmp_eq_u32 s59, 2
s_cbranch_scc0 .LL_200%=
Ra 40,21,5120
s_branch .LL_199%=
.LL_200%=:
buffer_load_dwordx4 a[40:43], v1, s[8:11], s79 offen offset:2048 sc1
.LL_199%=:
s_bitcmp1_b32 s69, 11
s_cbranch_scc0 .LL_201%=
s_cmp_eq_u32 s59, 3
s_cbranch_scc0 .LL_202%=
Ra 44,21,5120
s_branch .LL_201%=
.LL_202%=:
buffer_load_dwordx4 a[44:47], v1, s[8:11], s79 offen offset:3072 sc1
.LL_201%=:
s_bitcmp1_b32 s69, 12
s_cbranch_scc0 .LL_203%=
s_cmp_eq_u32 s59, 4
s_cbranch_scc0 .LL_204%=
Ra 48,21,5120
s_branch .LL_203%=
.LL_204%=:
buffer_load_dwordx4 a[48:51], v19, s[8:11], s79 offen offset:0 sc1
.LL_203%=:
s_bitcmp1_b32 s69, 13
s_cbranch_scc0 .LL_205%=
s_cmp_eq_u32 s59, 5
s_cbranch_scc0 .LL_206%=
Ra 52,21,5120
s_branch .LL_205%=
.LL_206%=:
buffer_load_dwordx4 a[52:55], v19, s[8:11], s79 offen offset:1024 sc1
.LL_205%=:
s_bitcmp1_b32 s69, 14
s_cbranch_scc0 .LL_207%=
s_cmp_eq_u32 s59, 6
s_cbranch_scc0 .LL_208%=
Ra 56,21,5120
s_branch .LL_207%=
.LL_208%=:
buffer_load_dwordx4 a[56:59], v19, s[8:11], s79 offen offset:2048 sc1
.LL_207%=:
s_bitcmp1_b32 s69, 15
s_cbranch_scc0 .LL_209%=
s_cmp_eq_u32 s59, 7
s_cbranch_scc0 .LL_210%=
Ra 60,21,5120
s_branch .LL_209%=
.LL_210%=:
buffer_load_dwordx4 a[60:63], v19, s[8:11], s79 offen offset:3072 sc1
.LL_209%=:
.LL_194%=:
s_or_b32 s71, s71, s69
.LL_176%=:
s_andn2_b32 s80, 0xffff, s71
s_cbranch_scc0 .LL_175%=
s_sleep 4
buffer_load_dwordx2 v[16:17], v18, s[8:11], s63 offen sc1
s_and_b32 s80, s70, 0x100ff
s_cmp_eq_u32 s80, 0xff
s_cbranch_scc0 .LL_174%=
W 0
Zvva 80,4,0
Zvva 84,8,0
Zvva 88,4,216
Zvva 92,8,216
Mvva 80,4,4
Mvva 84,8,4
Mvva 88,4,220
Mvva 92,8,220
Mvva 80,4,8
Mvva 84,8,8
Mvva 88,4,224
Mvva 92,8,224
Mvva 80,4,12
Mvva 84,8,12
Mvva 88,4,228
Mvva 92,8,228
Mvva 80,4,16
Mvva 84,8,16
Mvva 88,4,232
Mvva 92,8,232
Mvva 80,4,20
Mvva 84,8,20
Mvva 88,4,236
Mvva 92,8,236
Mvva 80,4,24
Mvva 84,8,24
Mvva 88,4,240
Mvva 92,8,240
Mvva 80,4,28
Mvva 84,8,28
Mvva 88,4,244
Mvva 92,8,244
s_nop 4
K 160,80,81
K 161,82,83
K 162,84,85
K 163,86,87
v_lshlrev_b32 v80, 16, v160
v_and_b32 v81, 0xffff0000, v160
v_lshlrev_b32 v82, 16, v161
v_and_b32 v83, 0xffff0000, v161
)"
R"(v_lshlrev_b32 v84, 16, v162
v_and_b32 v85, 0xffff0000, v162
v_lshlrev_b32 v86, 16, v163
v_and_b32 v87, 0xffff0000, v163
v_pk_add_f32 v[80:81], v[32:33], v[80:81]
v_pk_add_f32 v[82:83], v[34:35], v[82:83]
v_pk_add_f32 v[84:85], v[36:37], v[84:85]
v_pk_add_f32 v[86:87], v[38:39], v[86:87]
K 160,80,81
K 161,82,83
K 162,84,85
K 163,86,87
v_lshlrev_b32 v80, 16, v160
v_and_b32 v81, 0xffff0000, v160
v_lshlrev_b32 v82, 16, v161
v_and_b32 v83, 0xffff0000, v161
v_lshlrev_b32 v84, 16, v162
v_and_b32 v85, 0xffff0000, v162
v_lshlrev_b32 v86, 16, v163
v_and_b32 v87, 0xffff0000, v163
v_pk_add_f32 v[80:81], v[40:41], v[80:81]
v_pk_add_f32 v[82:83], v[42:43], v[82:83]
v_pk_add_f32 v[84:85], v[44:45], v[84:85]
v_pk_add_f32 v[86:87], v[46:47], v[86:87]
K 80,80,81
K 81,82,83
K 82,84,85
K 83,86,87
buffer_store_dwordx4 v[80:83], v20, s[20:23], s72 offen nt
K 160,88,89
K 161,90,91
K 162,92,93
K 163,94,95
v_lshlrev_b32 v88, 16, v160
v_and_b32 v89, 0xffff0000, v160
v_lshlrev_b32 v90, 16, v161
v_and_b32 v91, 0xffff0000, v161
v_lshlrev_b32 v92, 16, v162
v_and_b32 v93, 0xffff0000, v162
v_lshlrev_b32 v94, 16, v163
v_and_b32 v95, 0xffff0000, v163
v_pk_add_f32 v[88:89], v[64:65], v[88:89]
v_pk_add_f32 v[90:91], v[66:67], v[90:91]
v_pk_add_f32 v[92:93], v[68:69], v[92:93]
v_pk_add_f32 v[94:95], v[70:71], v[94:95]
K 160,88,89
K 161,90,91
K 162,92,93
K 163,94,95
v_lshlrev_b32 v88, 16, v160
v_and_b32 v89, 0xffff0000, v160
v_lshlrev_b32 v90, 16, v161
v_and_b32 v91, 0xffff0000, v161
v_lshlrev_b32 v92, 16, v162
v_and_b32 v93, 0xffff0000, v162
v_lshlrev_b32 v94, 16, v163
v_and_b32 v95, 0xffff0000, v163
v_pk_add_f32 v[88:89], v[72:73], v[88:89]
v_pk_add_f32 v[90:91], v[74:75], v[90:91]
v_pk_add_f32 v[92:93], v[76:77], v[92:93]
v_pk_add_f32 v[94:95], v[78:79], v[94:95]
K 88,88,89
K 89,90,91
K 90,92,93
K 91,94,95
buffer_store_dwordx4 v[88:91], v20, s[20:23], s74 offen nt
s_bitset1_b32 s71, 16
.LL_174%=:
s_add_u32 s84, s84, 1
s_waitcnt vmcnt(0)
v_cmp_eq_u64 vcc, s[12:13], v[16:17]
s_or_b32 s67, vcc_lo, s66
s_cmp_lt_u32 s84, 4194304
s_cbranch_scc1 .LL_173%=
.LL_175%=:
s_bitcmp1_b32 s71, 16
s_cbranch_scc1 .LL_211%=
s_and_b32 s80, s70, 0xff
s_cmp_eq_u32 s80, 0xff
s_cbranch_scc1 .LL_212%=
s_waitcnt vmcnt(0)
.LL_212%=:
W 0
Zvva 80,4,0
Zvva 84,8,0
Zvva 88,4,216
Zvva 92,8,216
Mvva 80,4,4
Mvva 84,8,4
Mvva 88,4,220
Mvva 92,8,220
Mvva 80,4,8
Mvva 84,8,8
Mvva 88,4,224
Mvva 92,8,224
Mvva 80,4,12
Mvva 84,8,12
Mvva 88,4,228
Mvva 92,8,228
Mvva 80,4,16
Mvva 84,8,16
Mvva 88,4,232
Mvva 92,8,232
Mvva 80,4,20
Mvva 84,8,20
Mvva 88,4,236
Mvva 92,8,236
Mvva 80,4,24
Mvva 84,8,24
Mvva 88,4,240
Mvva 92,8,240
Mvva 80,4,28
Mvva 84,8,28
Mvva 88,4,244
Mvva 92,8,244
s_nop 4
K 160,80,81
K 161,82,83
K 162,84,85
K 163,86,87
v_lshlrev_b32 v80, 16, v160
v_and_b32 v81, 0xffff0000, v160
v_lshlrev_b32 v82, 16, v161
v_and_b32 v83, 0xffff0000, v161
v_lshlrev_b32 v84, 16, v162
v_and_b32 v85, 0xffff0000, v162
v_lshlrev_b32 v86, 16, v163
v_and_b32 v87, 0xffff0000, v163
v_pk_add_f32 v[80:81], v[32:33], v[80:81]
v_pk_add_f32 v[82:83], v[34:35], v[82:83]
v_pk_add_f32 v[84:85], v[36:37], v[84:85]
v_pk_add_f32 v[86:87], v[38:39], v[86:87]
K 160,80,81
K 161,82,83
K 162,84,85
K 163,86,87
v_lshlrev_b32 v80, 16, v160
v_and_b32 v81, 0xffff0000, v160
v_lshlrev_b32 v82, 16, v161
v_and_b32 v83, 0xffff0000, v161
v_lshlrev_b32 v84, 16, v162
v_and_b32 v85, 0xffff0000, v162
v_lshlrev_b32 v86, 16, v163
v_and_b32 v87, 0xffff0000, v163
v_pk_add_f32 v[80:81], v[40:41], v[80:81]
v_pk_add_f32 v[82:83], v[42:43], v[82:83]
v_pk_add_f32 v[84:85], v[44:45], v[84:85]
v_pk_add_f32 v[86:87], v[46:47], v[86:87]
K 80,80,81
K 81,82,83
K 82,84,85
K 83,86,87
buffer_store_dwordx4 v[80:83], v20, s[20:23], s72 offen nt
K 160,88,89
K 161,90,91
K 162,92,93
K 163,94,95
v_lshlrev_b32 v88, 16, v160
v_and_b32 v89, 0xffff0000, v160
v_lshlrev_b32 v90, 16, v161
v_and_b32 v91, 0xffff0000, v161
v_lshlrev_b32 v92, 16, v162
v_and_b32 v93, 0xffff0000, v162
v_lshlrev_b32 v94, 16, v163
v_and_b32 v95, 0xffff0000, v163
v_pk_add_f32 v[88:89], v[64:65], v[88:89]
v_pk_add_f32 v[90:91], v[66:67], v[90:91]
v_pk_add_f32 v[92:93], v[68:69], v[92:93]
v_pk_add_f32 v[94:95], v[70:71], v[94:95]
K 160,88,89
K 161,90,91
K 162,92,93
K 163,94,95
v_lshlrev_b32 v88, 16, v160
v_and_b32 v89, 0xffff0000, v160
v_lshlrev_b32 v90, 16, v161
v_and_b32 v91, 0xffff0000, v161
v_lshlrev_b32 v92, 16, v162
v_and_b32 v93, 0xffff0000, v162
v_lshlrev_b32 v94, 16, v163
v_and_b32 v95, 0xffff0000, v163
v_pk_add_f32 v[88:89], v[72:73], v[88:89]
v_pk_add_f32 v[90:91], v[74:75], v[90:91]
v_pk_add_f32 v[92:93], v[76:77], v[92:93]
v_pk_add_f32 v[94:95], v[78:79], v[94:95]
K 88,88,89
K 89,90,91
K 90,92,93
K 91,94,95
buffer_store_dwordx4 v[88:91], v20, s[20:23], s74 offen nt
s_waitcnt vmcnt(2)
W 0
Zvva 80,4,32
Zvva 84,8,32
Mvva 80,4,36
Mvva 84,8,36
Mvva 80,4,40
Mvva 84,8,40
Mvva 80,4,44
Mvva 84,8,44
Mvva 80,4,48
Mvva 84,8,48
Mvva 80,4,52
Mvva 84,8,52
Mvva 80,4,56
Mvva 84,8,56
Mvva 80,4,60
Mvva 84,8,60
s_nop 6
K 160,80,81
K 161,82,83
K 162,84,85
K 163,86,87
v_lshlrev_b32 v80, 16, v160
v_and_b32 v81, 0xffff0000, v160
v_lshlrev_b32 v82, 16, v161
v_and_b32 v83, 0xffff0000, v161
v_lshlrev_b32 v84, 16, v162
v_and_b32 v85, 0xffff0000, v162
v_lshlrev_b32 v86, 16, v163
v_and_b32 v87, 0xffff0000, v163
v_pk_add_f32 v[80:81], v[48:49], v[80:81]
v_pk_add_f32 v[82:83], v[50:51], v[82:83]
v_pk_add_f32 v[84:85], v[52:53], v[84:85]
v_pk_add_f32 v[86:87], v[54:55], v[86:87]
K 160,80,81
K 161,82,83
K 162,84,85
K 163,86,87
v_lshlrev_b32 v80, 16, v160
v_and_b32 v81, 0xffff0000, v160
v_lshlrev_b32 v82, 16, v161
v_and_b32 v83, 0xffff0000, v161
v_lshlrev_b32 v84, 16, v162
v_and_b32 v85, 0xffff0000, v162
v_lshlrev_b32 v86, 16, v163
v_and_b32 v87, 0xffff0000, v163
v_pk_add_f32 v[80:81], v[56:57], v[80:81]
v_pk_add_f32 v[82:83], v[58:59], v[82:83]
v_pk_add_f32 v[84:85], v[60:61], v[84:85]
v_pk_add_f32 v[86:87], v[62:63], v[86:87]
K 80,80,81
K 81,82,83
K 82,84,85
K 83,86,87
buffer_store_dwordx4 v[80:83], v20, s[20:23], s73 offen nt
s_branch .LL_130%=
.LL_211%=:
s_waitcnt vmcnt(0)
W 0
Zvva 80,4,32
Zvva 84,8,32
Mvva 80,4,36
Mvva 84,8,36
Mvva 80,4,40
Mvva 84,8,40
Mvva 80,4,44
Mvva 84,8,44
Mvva 80,4,48
Mvva 84,8,48
Mvva 80,4,52
Mvva 84,8,52
Mvva 80,4,56
Mvva 84,8,56
Mvva 80,4,60
Mvva 84,8,60
s_nop 6
K 160,80,81
K 161,82,83
K 162,84,85
K 163,86,87
v_lshlrev_b32 v80, 16, v160
v_and_b32 v81, 0xffff0000, v160
v_lshlrev_b32 v82, 16, v161
v_and_b32 v83, 0xffff0000, v161
v_lshlrev_b32 v84, 16, v162
v_and_b32 v85, 0xffff0000, v162
v_lshlrev_b32 v86, 16, v163
v_and_b32 v87, 0xffff0000, v163
v_pk_add_f32 v[80:81], v[48:49], v[80:81]
v_pk_add_f32 v[82:83], v[50:51], v[82:83]
v_pk_add_f32 v[84:85], v[52:53], v[84:85]
v_pk_add_f32 v[86:87], v[54:55], v[86:87]
K 160,80,81
K 161,82,83
K 162,84,85
K 163,86,87
v_lshlrev_b32 v80, 16, v160
v_and_b32 v81, 0xffff0000, v160
v_lshlrev_b32 v82, 16, v161
v_and_b32 v83, 0xffff0000, v161
v_lshlrev_b32 v84, 16, v162
v_and_b32 v85, 0xffff0000, v162
v_lshlrev_b32 v86, 16, v163
v_and_b32 v87, 0xffff0000, v163
v_pk_add_f32 v[80:81], v[56:57], v[80:81]
v_pk_add_f32 v[82:83], v[58:59], v[82:83]
v_pk_add_f32 v[84:85], v[60:61], v[84:85]
v_pk_add_f32 v[86:87], v[62:63], v[86:87]
K 80,80,81
K 81,82,83
K 82,84,85
K 83,86,87
buffer_store_dwordx4 v[80:83], v20, s[20:23], s73 offen nt
s_branch .LL_130%=
.LL_132%=:
s_mov_b32 s84, 0
.LL_213%=:
s_or_b32 s70, s71, s66
s_andn2_b32 s69, s67, s71
s_and_b32 s69, s69, 0xffff
s_cbranch_scc0 .LL_216%=
s_and_b32 s80, s69, 255
s_cbranch_scc0 .LL_217%=
s_bitcmp1_b32 s69, 0
s_cbranch_scc0 .LL_218%=
s_cmp_eq_u32 s59, 0
s_cbranch_scc0 .LL_219%=
Ra 0,21,0
s_branch .LL_218%=
.LL_219%=:
buffer_load_dwordx4 a[0:3], v1, s[8:11], s78 offen offset:0 sc1
.LL_218%=:
s_bitcmp1_b32 s69, 1
s_cbranch_scc0 .LL_220%=
s_cmp_eq_u32 s59, 1
s_cbranch_scc0 .LL_221%=
Ra 4,21,0
s_branch .LL_220%=
.LL_221%=:
buffer_load_dwordx4 a[4:7], v1, s[8:11], s78 offen offset:1024 sc1
.LL_220%=:
s_bitcmp1_b32 s69, 2
s_cbranch_scc0 .LL_222%=
s_cmp_eq_u32 s59, 2
s_cbranch_scc0 .LL_223%=
Ra 8,21,0
s_branch .LL_222%=
.LL_223%=:
buffer_load_dwordx4 a[8:11], v1, s[8:11], s78 offen offset:2048 sc1
.LL_222%=:
s_bitcmp1_b32 s69, 3
s_cbranch_scc0 .LL_224%=
s_cmp_eq_u32 s59, 3
s_cbranch_scc0 .LL_225%=
Ra 12,21,0
s_branch .LL_224%=
.LL_225%=:
buffer_load_dwordx4 a[12:15], v1, s[8:11], s78 offen offset:3072 sc1
.LL_224%=:
s_bitcmp1_b32 s69, 4
s_cbranch_scc0 .LL_226%=
s_cmp_eq_u32 s59, 4
s_cbranch_scc0 .LL_227%=
Ra 16,21,0
s_branch .LL_226%=
.LL_227%=:
buffer_load_dwordx4 a[16:19], v19, s[8:11], s78 offen offset:0 sc1
.LL_226%=:
s_bitcmp1_b32 s69, 5
s_cbranch_scc0 .LL_228%=
s_cmp_eq_u32 s59, 5
s_cbranch_scc0 .LL_229%=
Ra 20,21,0
s_branch .LL_228%=
.LL_229%=:
buffer_load_dwordx4 a[20:23], v19, s[8:11], s78 offen offset:1024 sc1
.LL_228%=:
s_bitcmp1_b32 s69, 6
s_cbranch_scc0 .LL_230%=
s_cmp_eq_u32 s59, 6
s_cbranch_scc0 .LL_231%=
Ra 24,21,0
s_branch .LL_230%=
.LL_231%=:
buffer_load_dwordx4 a[24:27], v19, s[8:11], s78 offen offset:2048 sc1
.LL_230%=:
s_bitcmp1_b32 s69, 7
s_cbranch_scc0 .LL_232%=
s_cmp_eq_u32 s59, 7
s_cbranch_scc0 .LL_233%=
Ra 28,21,0
s_branch .LL_232%=
.LL_233%=:
buffer_load_dwordx4 a[28:31], v19, s[8:11], s78 offen offset:3072 sc1
.LL_232%=:
.LL_217%=:
s_and_b32 s80, s69, 65280
s_cbranch_scc0 .LL_234%=
s_bitcmp1_b32 s69, 8
s_cbranch_scc0 .LL_235%=
s_cmp_eq_u32 s59, 0
s_cbranch_scc0 .LL_236%=
Ra 32,21,5120
Ra 216,1,9216
s_branch .LL_235%=
.LL_236%=:
buffer_load_dwordx4 a[32:35], v1, s[8:11], s79 offen offset:0 sc1
buffer_load_dwordx4 a[216:219], v1, s[8:11], s65 offen offset:0 sc1
.LL_235%=:
)"
R"(s_bitcmp1_b32 s69, 9
s_cbranch_scc0 .LL_237%=
s_cmp_eq_u32 s59, 1
s_cbranch_scc0 .LL_238%=
Ra 36,21,5120
Ra 220,1,9216
s_branch .LL_237%=
.LL_238%=:
buffer_load_dwordx4 a[36:39], v1, s[8:11], s79 offen offset:1024 sc1
buffer_load_dwordx4 a[220:223], v1, s[8:11], s65 offen offset:1024 sc1
.LL_237%=:
s_bitcmp1_b32 s69, 10
s_cbranch_scc0 .LL_239%=
s_cmp_eq_u32 s59, 2
s_cbranch_scc0 .LL_240%=
Ra 40,21,5120
Ra 224,1,9216
s_branch .LL_239%=
.LL_240%=:
buffer_load_dwordx4 a[40:43], v1, s[8:11], s79 offen offset:2048 sc1
buffer_load_dwordx4 a[224:227], v1, s[8:11], s65 offen offset:2048 sc1
.LL_239%=:
s_bitcmp1_b32 s69, 11
s_cbranch_scc0 .LL_241%=
s_cmp_eq_u32 s59, 3
s_cbranch_scc0 .LL_242%=
Ra 44,21,5120
Ra 228,1,9216
s_branch .LL_241%=
.LL_242%=:
buffer_load_dwordx4 a[44:47], v1, s[8:11], s79 offen offset:3072 sc1
buffer_load_dwordx4 a[228:231], v1, s[8:11], s65 offen offset:3072 sc1
.LL_241%=:
s_bitcmp1_b32 s69, 12
s_cbranch_scc0 .LL_243%=
s_cmp_eq_u32 s59, 4
s_cbranch_scc0 .LL_244%=
Ra 48,21,5120
Ra 232,1,9216
s_branch .LL_243%=
.LL_244%=:
buffer_load_dwordx4 a[48:51], v19, s[8:11], s79 offen offset:0 sc1
buffer_load_dwordx4 a[232:235], v19, s[8:11], s65 offen offset:0 sc1
.LL_243%=:
s_bitcmp1_b32 s69, 13
s_cbranch_scc0 .LL_245%=
s_cmp_eq_u32 s59, 5
s_cbranch_scc0 .LL_246%=
Ra 52,21,5120
Ra 236,1,9216
s_branch .LL_245%=
.LL_246%=:
buffer_load_dwordx4 a[52:55], v19, s[8:11], s79 offen offset:1024 sc1
buffer_load_dwordx4 a[236:239], v19, s[8:11], s65 offen offset:1024 sc1
.LL_245%=:
s_bitcmp1_b32 s69, 14
s_cbranch_scc0 .LL_247%=
s_cmp_eq_u32 s59, 6
s_cbranch_scc0 .LL_248%=
Ra 56,21,5120
Ra 240,1,9216
s_branch .LL_247%=
.LL_248%=:
buffer_load_dwordx4 a[56:59], v19, s[8:11], s79 offen offset:2048 sc1
buffer_load_dwordx4 a[240:243], v19, s[8:11], s65 offen offset:2048 sc1
.LL_247%=:
s_bitcmp1_b32 s69, 15
s_cbranch_scc0 .LL_249%=
s_cmp_eq_u32 s59, 7
s_cbranch_scc0 .LL_250%=
Ra 60,21,5120
Ra 244,1,9216
s_branch .LL_249%=
.LL_250%=:
buffer_load_dwordx4 a[60:63], v19, s[8:11], s79 offen offset:3072 sc1
buffer_load_dwordx4 a[244:247], v19, s[8:11], s65 offen offset:3072 sc1
.LL_249%=:
.LL_234%=:
s_or_b32 s71, s71, s69
.LL_216%=:
s_andn2_b32 s80, 0xffff, s71
s_cbranch_scc0 .LL_215%=
s_sleep 4
buffer_load_dwordx2 v[16:17], v18, s[8:11], s63 offen sc1
s_and_b32 s80, s70, 0x100ff
s_cmp_eq_u32 s80, 0xff
s_cbranch_scc0 .LL_214%=
W 0
Zvva 80,4,0
Zvva 84,8,0
Mvva 80,4,4
Mvva 84,8,4
Mvva 80,4,8
Mvva 84,8,8
Mvva 80,4,12
Mvva 84,8,12
Mvva 80,4,16
Mvva 84,8,16
Mvva 80,4,20
Mvva 84,8,20
Mvva 80,4,24
Mvva 84,8,24
Mvva 80,4,28
Mvva 84,8,28
s_nop 6
K 160,80,81
K 161,82,83
K 162,84,85
K 163,86,87
v_lshlrev_b32 v80, 16, v160
v_and_b32 v81, 0xffff0000, v160
v_lshlrev_b32 v82, 16, v161
v_and_b32 v83, 0xffff0000, v161
v_lshlrev_b32 v84, 16, v162
v_and_b32 v85, 0xffff0000, v162
v_lshlrev_b32 v86, 16, v163
v_and_b32 v87, 0xffff0000, v163
v_pk_add_f32 v[80:81], v[32:33], v[80:81]
v_pk_add_f32 v[82:83], v[34:35], v[82:83]
v_pk_add_f32 v[84:85], v[36:37], v[84:85]
v_pk_add_f32 v[86:87], v[38:39], v[86:87]
K 160,80,81
K 161,82,83
K 162,84,85
K 163,86,87
v_lshlrev_b32 v80, 16, v160
v_and_b32 v81, 0xffff0000, v160
v_lshlrev_b32 v82, 16, v161
v_and_b32 v83, 0xffff0000, v161
v_lshlrev_b32 v84, 16, v162
v_and_b32 v85, 0xffff0000, v162
v_lshlrev_b32 v86, 16, v163
v_and_b32 v87, 0xffff0000, v163
v_pk_add_f32 v[80:81], v[40:41], v[80:81]
v_pk_add_f32 v[82:83], v[42:43], v[82:83]
v_pk_add_f32 v[84:85], v[44:45], v[84:85]
v_pk_add_f32 v[86:87], v[46:47], v[86:87]
K 80,80,81
K 81,82,83
K 82,84,85
K 83,86,87
buffer_store_dwordx4 v[80:83], v20, s[20:23], s72 offen nt
s_bitset1_b32 s71, 16
.LL_214%=:
s_add_u32 s84, s84, 1
s_waitcnt vmcnt(0)
v_cmp_eq_u64 vcc, s[12:13], v[16:17]
s_or_b32 s67, vcc_lo, s66
s_cmp_lt_u32 s84, 4194304
s_cbranch_scc1 .LL_213%=
.LL_215%=:
s_bitcmp1_b32 s71, 16
s_cbranch_scc1 .LL_251%=
s_and_b32 s80, s70, 0xff
s_cmp_eq_u32 s80, 0xff
s_cbranch_scc1 .LL_252%=
s_waitcnt vmcnt(0)
.LL_252%=:
W 0
Zvva 80,4,0
Zvva 84,8,0
Mvva 80,4,4
Mvva 84,8,4
Mvva 80,4,8
Mvva 84,8,8
Mvva 80,4,12
Mvva 84,8,12
Mvva 80,4,16
Mvva 84,8,16
Mvva 80,4,20
Mvva 84,8,20
Mvva 80,4,24
Mvva 84,8,24
Mvva 80,4,28
Mvva 84,8,28
s_nop 6
K 160,80,81
K 161,82,83
K 162,84,85
K 163,86,87
v_lshlrev_b32 v80, 16, v160
v_and_b32 v81, 0xffff0000, v160
v_lshlrev_b32 v82, 16, v161
v_and_b32 v83, 0xffff0000, v161
v_lshlrev_b32 v84, 16, v162
v_and_b32 v85, 0xffff0000, v162
v_lshlrev_b32 v86, 16, v163
v_and_b32 v87, 0xffff0000, v163
v_pk_add_f32 v[80:81], v[32:33], v[80:81]
v_pk_add_f32 v[82:83], v[34:35], v[82:83]
v_pk_add_f32 v[84:85], v[36:37], v[84:85]
v_pk_add_f32 v[86:87], v[38:39], v[86:87]
K 160,80,81
K 161,82,83
K 162,84,85
K 163,86,87
v_lshlrev_b32 v80, 16, v160
v_and_b32 v81, 0xffff0000, v160
v_lshlrev_b32 v82, 16, v161
v_and_b32 v83, 0xffff0000, v161
v_lshlrev_b32 v84, 16, v162
v_and_b32 v85, 0xffff0000, v162
v_lshlrev_b32 v86, 16, v163
v_and_b32 v87, 0xffff0000, v163
v_pk_add_f32 v[80:81], v[40:41], v[80:81]
v_pk_add_f32 v[82:83], v[42:43], v[82:83]
v_pk_add_f32 v[84:85], v[44:45], v[84:85]
v_pk_add_f32 v[86:87], v[46:47], v[86:87]
K 80,80,81
K 81,82,83
K 82,84,85
K 83,86,87
buffer_store_dwordx4 v[80:83], v20, s[20:23], s72 offen nt
s_waitcnt vmcnt(1)
W 0
Zvva 80,4,32
Zvva 84,8,32
Zvva 88,4,216
Zvva 92,8,216
Mvva 80,4,36
Mvva 84,8,36
Mvva 88,4,220
Mvva 92,8,220
Mvva 80,4,40
Mvva 84,8,40
Mvva 88,4,224
Mvva 92,8,224
Mvva 80,4,44
Mvva 84,8,44
Mvva 88,4,228
Mvva 92,8,228
Mvva 80,4,48
Mvva 84,8,48
Mvva 88,4,232
Mvva 92,8,232
Mvva 80,4,52
Mvva 84,8,52
Mvva 88,4,236
Mvva 92,8,236
Mvva 80,4,56
Mvva 84,8,56
Mvva 88,4,240
Mvva 92,8,240
Mvva 80,4,60
Mvva 84,8,60
Mvva 88,4,244
Mvva 92,8,244
s_nop 4
K 160,80,81
K 161,82,83
K 162,84,85
K 163,86,87
v_lshlrev_b32 v80, 16, v160
v_and_b32 v81, 0xffff0000, v160
v_lshlrev_b32 v82, 16, v161
v_and_b32 v83, 0xffff0000, v161
v_lshlrev_b32 v84, 16, v162
v_and_b32 v85, 0xffff0000, v162
v_lshlrev_b32 v86, 16, v163
v_and_b32 v87, 0xffff0000, v163
v_pk_add_f32 v[80:81], v[48:49], v[80:81]
v_pk_add_f32 v[82:83], v[50:51], v[82:83]
v_pk_add_f32 v[84:85], v[52:53], v[84:85]
v_pk_add_f32 v[86:87], v[54:55], v[86:87]
K 160,80,81
K 161,82,83
K 162,84,85
K 163,86,87
v_lshlrev_b32 v80, 16, v160
v_and_b32 v81, 0xffff0000, v160
v_lshlrev_b32 v82, 16, v161
v_and_b32 v83, 0xffff0000, v161
v_lshlrev_b32 v84, 16, v162
v_and_b32 v85, 0xffff0000, v162
v_lshlrev_b32 v86, 16, v163
v_and_b32 v87, 0xffff0000, v163
v_pk_add_f32 v[80:81], v[56:57], v[80:81]
v_pk_add_f32 v[82:83], v[58:59], v[82:83]
v_pk_add_f32 v[84:85], v[60:61], v[84:85]
v_pk_add_f32 v[86:87], v[62:63], v[86:87]
K 80,80,81
K 81,82,83
K 82,84,85
K 83,86,87
buffer_store_dwordx4 v[80:83], v20, s[20:23], s73 offen nt
K 160,88,89
K 161,90,91
K 162,92,93
K 163,94,95
v_lshlrev_b32 v88, 16, v160
v_and_b32 v89, 0xffff0000, v160
v_lshlrev_b32 v90, 16, v161
v_and_b32 v91, 0xffff0000, v161
v_lshlrev_b32 v92, 16, v162
v_and_b32 v93, 0xffff0000, v162
v_lshlrev_b32 v94, 16, v163
v_and_b32 v95, 0xffff0000, v163
v_pk_add_f32 v[88:89], v[64:65], v[88:89]
v_pk_add_f32 v[90:91], v[66:67], v[90:91]
v_pk_add_f32 v[92:93], v[68:69], v[92:93]
v_pk_add_f32 v[94:95], v[70:71], v[94:95]
K 160,88,89
K 161,90,91
K 162,92,93
K 163,94,95
v_lshlrev_b32 v88, 16, v160
v_and_b32 v89, 0xffff0000, v160
v_lshlrev_b32 v90, 16, v161
v_and_b32 v91, 0xffff0000, v161
v_lshlrev_b32 v92, 16, v162
v_and_b32 v93, 0xffff0000, v162
v_lshlrev_b32 v94, 16, v163
v_and_b32 v95, 0xffff0000, v163
v_pk_add_f32 v[88:89], v[72:73], v[88:89]
v_pk_add_f32 v[90:91], v[74:75], v[90:91]
v_pk_add_f32 v[92:93], v[76:77], v[92:93]
v_pk_add_f32 v[94:95], v[78:79], v[94:95]
K 88,88,89
K 89,90,91
K 90,92,93
K 91,94,95
buffer_store_dwordx4 v[88:91], v20, s[20:23], s74 offen nt
s_branch .LL_130%=
.LL_251%=:
s_waitcnt vmcnt(0)
W 0
Zvva 80,4,32
Zvva 84,8,32
Zvva 88,4,216
Zvva 92,8,216
Mvva 80,4,36
Mvva 84,8,36
Mvva 88,4,220
Mvva 92,8,220
Mvva 80,4,40
Mvva 84,8,40
Mvva 88,4,224
Mvva 92,8,224
Mvva 80,4,44
Mvva 84,8,44
Mvva 88,4,228
Mvva 92,8,228
Mvva 80,4,48
Mvva 84,8,48
Mvva 88,4,232
Mvva 92,8,232
Mvva 80,4,52
Mvva 84,8,52
Mvva 88,4,236
Mvva 92,8,236
Mvva 80,4,56
Mvva 84,8,56
Mvva 88,4,240
Mvva 92,8,240
Mvva 80,4,60
Mvva 84,8,60
Mvva 88,4,244
Mvva 92,8,244
s_nop 4
K 160,80,81
K 161,82,83
K 162,84,85
K 163,86,87
v_lshlrev_b32 v80, 16, v160
v_and_b32 v81, 0xffff0000, v160
v_lshlrev_b32 v82, 16, v161
v_and_b32 v83, 0xffff0000, v161
v_lshlrev_b32 v84, 16, v162
v_and_b32 v85, 0xffff0000, v162
v_lshlrev_b32 v86, 16, v163
v_and_b32 v87, 0xffff0000, v163
v_pk_add_f32 v[80:81], v[48:49], v[80:81]
v_pk_add_f32 v[82:83], v[50:51], v[82:83]
v_pk_add_f32 v[84:85], v[52:53], v[84:85]
v_pk_add_f32 v[86:87], v[54:55], v[86:87]
K 160,80,81
K 161,82,83
K 162,84,85
K 163,86,87
v_lshlrev_b32 v80, 16, v160
v_and_b32 v81, 0xffff0000, v160
v_lshlrev_b32 v82, 16, v161
v_and_b32 v83, 0xffff0000, v161
v_lshlrev_b32 v84, 16, v162
v_and_b32 v85, 0xffff0000, v162
v_lshlrev_b32 v86, 16, v163
v_and_b32 v87, 0xffff0000, v163
v_pk_add_f32 v[80:81], v[56:57], v[80:81]
v_pk_add_f32 v[82:83], v[58:59], v[82:83]
v_pk_add_f32 v[84:85], v[60:61], v[84:85]
v_pk_add_f32 v[86:87], v[62:63], v[86:87]
K 80,80,81
K 81,82,83
K 82,84,85
K 83,86,87
buffer_store_dwordx4 v[80:83], v20, s[20:23], s73 offen nt
K 160,88,89
K 161,90,91
K 162,92,93
K 163,94,95
)"
R"(v_lshlrev_b32 v88, 16, v160
v_and_b32 v89, 0xffff0000, v160
v_lshlrev_b32 v90, 16, v161
v_and_b32 v91, 0xffff0000, v161
v_lshlrev_b32 v92, 16, v162
v_and_b32 v93, 0xffff0000, v162
v_lshlrev_b32 v94, 16, v163
v_and_b32 v95, 0xffff0000, v163
v_pk_add_f32 v[88:89], v[64:65], v[88:89]
v_pk_add_f32 v[90:91], v[66:67], v[90:91]
v_pk_add_f32 v[92:93], v[68:69], v[92:93]
v_pk_add_f32 v[94:95], v[70:71], v[94:95]
K 160,88,89
K 161,90,91
K 162,92,93
K 163,94,95
v_lshlrev_b32 v88, 16, v160
v_and_b32 v89, 0xffff0000, v160
v_lshlrev_b32 v90, 16, v161
v_and_b32 v91, 0xffff0000, v161
v_lshlrev_b32 v92, 16, v162
v_and_b32 v93, 0xffff0000, v162
v_lshlrev_b32 v94, 16, v163
v_and_b32 v95, 0xffff0000, v163
v_pk_add_f32 v[88:89], v[72:73], v[88:89]
v_pk_add_f32 v[90:91], v[74:75], v[90:91]
v_pk_add_f32 v[92:93], v[76:77], v[92:93]
v_pk_add_f32 v[94:95], v[78:79], v[94:95]
K 88,88,89
K 89,90,91
K 90,92,93
K 91,94,95
buffer_store_dwordx4 v[88:91], v20, s[20:23], s74 offen nt
.LL_130%=:
s_lshl_b32 s80, s15, 2
s_add_u32 s80, s80, s14
s_cmp_eq_u32 s80, 4
s_cbranch_scc0 .LL_253%=
v_mov_b64 v[28:29], s[12:13]
s_mov_b64 exec, 1
buffer_store_dwordx2 v[28:29], off, s[8:11], 0 sc1
s_mov_b64 exec, -1
.LL_253%=:
s_branch .Lend_254%=
.Lup_123%=:
s_waitcnt vmcnt(3)
s_mov_b32 s84, 0
.LL_125%=:
v_min3_u32 v176, v101, v103, v109
v_max3_u32 v177, v101, v103, v109
v_min3_u32 v176, v176, v111, v117
v_max3_u32 v177, v177, v111, v117
v_min3_u32 v176, v176, v119, v125
v_max3_u32 v177, v177, v119, v125
v_min_u32 v176, v176, v127
v_max_u32 v177, v177, v127
v_cmp_eq_u32 vcc, s13, v176
v_cmp_eq_u32_e64 s[86:87], s13, v177
s_and_b64 vcc, vcc, s[86:87]
s_cmp_eq_u64 vcc, -1
s_cbranch_scc1 .LL_126%=
s_add_u32 s84, s84, 1
s_cmp_lt_u32 s84, 1048576
s_cbranch_scc1 .LL_127%=
.LL_126%=:
v_readlane_b32 s70, v12, s69
v_bfe_u32 v2, v1, 4, 4
s_nop 2
s_lshr_b32 s80, s70, 4
s_and_b32 s70, s70, 15
v_cmp_eq_u32 vcc, s70, v2
s_cmp_eq_u32 s80, s61
s_cselect_b64 s[86:87], vcc, 0
s_cselect_b64 s[88:89], 0, vcc
s_bitcmp1_b32 s14, 0
s_cselect_b64 vcc, s[88:89], s[86:87]
v_lshlrev_b32 v176, 16, v100
v_and_b32 v177, 0xffff0000, v100
v_lshlrev_b32 v178, 16, v102
v_and_b32 v179, 0xffff0000, v102
v_lshlrev_b32 v180, 16, v108
v_and_b32 v181, 0xffff0000, v108
v_lshlrev_b32 v182, 16, v110
v_and_b32 v183, 0xffff0000, v110
A 176,176,40
A 177,177,41
A 178,178,42
A 179,179,43
A 180,180,44
A 181,181,45
A 182,182,46
A 183,183,47
K 184,176,177
K 185,178,179
K 186,180,181
K 187,182,183
v_lshlrev_b32 v176, 16, v184
v_and_b32 v177, 0xffff0000, v184
v_lshlrev_b32 v178, 16, v185
v_and_b32 v179, 0xffff0000, v185
v_lshlrev_b32 v180, 16, v186
v_and_b32 v181, 0xffff0000, v186
v_lshlrev_b32 v182, 16, v187
v_and_b32 v183, 0xffff0000, v187
v_cndmask_b32_e64 v40, v40, v176, s[86:87]
v_cndmask_b32_e64 v41, v41, v177, s[86:87]
v_cndmask_b32_e64 v42, v42, v178, s[86:87]
v_cndmask_b32_e64 v43, v43, v179, s[86:87]
v_cndmask_b32_e64 v44, v44, v180, s[86:87]
v_cndmask_b32_e64 v45, v45, v181, s[86:87]
v_cndmask_b32_e64 v46, v46, v182, s[86:87]
v_cndmask_b32_e64 v47, v47, v183, s[86:87]
v_lshlrev_b32 v176, 16, v100
v_and_b32 v177, 0xffff0000, v100
v_lshlrev_b32 v178, 16, v102
v_and_b32 v179, 0xffff0000, v102
v_lshlrev_b32 v180, 16, v108
v_and_b32 v181, 0xffff0000, v108
v_lshlrev_b32 v182, 16, v110
v_and_b32 v183, 0xffff0000, v110
A 176,176,56
A 177,177,57
A 178,178,58
A 179,179,59
A 180,180,60
A 181,181,61
A 182,182,62
A 183,183,63
K 184,176,177
K 185,178,179
K 186,180,181
K 187,182,183
v_lshlrev_b32 v176, 16, v184
v_and_b32 v177, 0xffff0000, v184
v_lshlrev_b32 v178, 16, v185
v_and_b32 v179, 0xffff0000, v185
v_lshlrev_b32 v180, 16, v186
v_and_b32 v181, 0xffff0000, v186
v_lshlrev_b32 v182, 16, v187
v_and_b32 v183, 0xffff0000, v187
v_cndmask_b32_e64 v56, v56, v176, s[88:89]
v_cndmask_b32_e64 v57, v57, v177, s[88:89]
v_cndmask_b32_e64 v58, v58, v178, s[88:89]
v_cndmask_b32_e64 v59, v59, v179, s[88:89]
v_cndmask_b32_e64 v60, v60, v180, s[88:89]
v_cndmask_b32_e64 v61, v61, v181, s[88:89]
v_cndmask_b32_e64 v62, v62, v182, s[88:89]
v_cndmask_b32_e64 v63, v63, v183, s[88:89]
v_lshlrev_b32 v176, 16, v116
v_and_b32 v177, 0xffff0000, v116
v_lshlrev_b32 v178, 16, v118
v_and_b32 v179, 0xffff0000, v118
v_lshlrev_b32 v180, 16, v124
v_and_b32 v181, 0xffff0000, v124
v_lshlrev_b32 v182, 16, v126
v_and_b32 v183, 0xffff0000, v126
A 176,176,72
A 177,177,73
A 178,178,74
A 179,179,75
A 180,180,76
A 181,181,77
A 182,182,78
A 183,183,79
K 184,176,177
K 185,178,179
K 186,180,181
K 187,182,183
v_lshlrev_b32 v176, 16, v184
v_and_b32 v177, 0xffff0000, v184
v_lshlrev_b32 v178, 16, v185
v_and_b32 v179, 0xffff0000, v185
v_lshlrev_b32 v180, 16, v186
v_and_b32 v181, 0xffff0000, v186
v_lshlrev_b32 v182, 16, v187
v_and_b32 v183, 0xffff0000, v187
v_cndmask_b32_e64 v72, v72, v176, vcc
v_cndmask_b32_e64 v73, v73, v177, vcc
v_cndmask_b32_e64 v74, v74, v178, vcc
v_cndmask_b32_e64 v75, v75, v179, vcc
v_cndmask_b32_e64 v76, v76, v180, vcc
v_cndmask_b32_e64 v77, v77, v181, vcc
v_cndmask_b32_e64 v78, v78, v182, vcc
v_cndmask_b32_e64 v79, v79, v183, vcc
s_bitset0_b32 s68, s69
s_cmp_eq_u32 s68, 0
s_cbranch_scc1 .Lupr_124%=
s_ff1_i32_b32 s69, s68
.LL_127%=:
s_cmp_eq_u32 s64, 0
s_cbranch_scc0 .LL_128%=
s_waitcnt vmcnt(0)
v_lshlrev_b32 v2, 2, v1
v_mov_b64 v[28:29], s[12:13]
s_mov_b64 exec, 3
buffer_store_dwordx2 v[28:29], v2, s[8:11], s53 offen offset:512 sc1
s_mov_b64 exec, -1
s_mov_b32 s64, 1
.LL_128%=:
s_mul_i32 s66, s69, 20
s_mul_i32 s67, s58, 5
s_add_u32 s66, s66, s67
s_lshl_b32 s66, s66, 7
s_add_u32 s66, s66, 98304
s_lshl_b32 s67, s14, 7
s_add_u32 s67, s67, s66
buffer_load_dwordx4 v[100:103], v3, s[8:11], s67 offen offset:0 sc1
buffer_load_dwordx4 v[108:111], v3, s[8:11], s67 offen offset:16 sc1
buffer_load_dwordx4 v[116:119], v3, s[8:11], s66 offen offset:512 sc1
buffer_load_dwordx4 v[124:127], v3, s[8:11], s66 offen offset:528 sc1
s_waitcnt vmcnt(0)
s_branch .LL_125%=
.Lspin_56%=:
s_mov_b32 s84, 0
.Llp_58%=:
s_sleep 2
v_mov_b32 v28, s74
buffer_load_dwordx2 v[28:29], v28, s[8:11], 0 offen offset:3584 sc1
s_waitcnt vmcnt(0)
s_add_u32 s84, s84, 1
v_xor_b32 v28, s12, v28
v_xor_b32 v29, s13, v29
v_lshrrev_b32 v248, 11, v28
v_or_b32 v29, v29, v248
v_cmp_eq_u32 vcc, 0, v29
s_or_b64 vcc, vcc, s[72:73]
s_cmp_eq_u64 vcc, -1
s_cbranch_scc1 .Lok_57%=
s_cmp_lt_u32 s84, 1048576
s_cbranch_scc1 .Llp_58%=
s_branch .Lok_57%=
.Lspin_61%=:
s_mov_b32 s84, 0
.Llp_63%=:
s_sleep 2
v_bfe_u32 v28, v1, 4, 2
v_lshlrev_b32 v28, 3, v28
s_lshl_b32 s80, s56, 9
s_add_u32 s80, s80, 4096
buffer_load_dwordx2 v[28:29], v28, s[8:11], s80 offen sc1
s_waitcnt vmcnt(0)
s_add_u32 s84, s84, 1
v_cmp_eq_u64 vcc, s[12:13], v[28:29]
s_cmp_eq_u64 vcc, -1
s_cbranch_scc1 .Lok_62%=
s_cmp_lt_u32 s84, 1048576
s_cbranch_scc1 .Llp_63%=
s_branch .Lok_62%=
.Lspin_64%=:
s_mov_b32 s84, 0
.Llp_66%=:
s_sleep 2
v_bfe_u32 v28, v1, 4, 4
v_lshlrev_b32 v28, 3, v28
s_lshl_b32 s80, s56, 9
s_add_u32 s80, s80, 4096
buffer_load_dwordx2 v[28:29], v28, s[8:11], s80 offen sc1
s_waitcnt vmcnt(0)
s_add_u32 s84, s84, 1
v_cmp_eq_u64 vcc, s[12:13], v[28:29]
s_cmp_eq_u64 vcc, -1
s_cbranch_scc1 .Lok_65%=
s_cmp_lt_u32 s84, 1048576
s_cbranch_scc1 .Llp_66%=
s_branch .Lok_65%=
.Lspin_67%=:
s_mov_b32 s84, 0
.Llp_69%=:
s_sleep 2
v_bfe_u32 v28, v1, 4, 4
v_lshlrev_b32 v28, 3, v28
s_lshl_b32 s80, s56, 9
s_add_u32 s80, s80, 4224
buffer_load_dwordx2 v[28:29], v28, s[8:11], s80 offen sc1
s_waitcnt vmcnt(0)
s_add_u32 s84, s84, 1
v_cmp_eq_u64 vcc, s[12:13], v[28:29]
s_cmp_eq_u64 vcc, -1
s_cbranch_scc1 .Lok_68%=
s_cmp_lt_u32 s84, 1048576
s_cbranch_scc1 .Llp_69%=
s_branch .Lok_68%=
.Lspin_71%=:
s_mov_b32 s84, 0
.Llp_73%=:
s_sleep 2
v_bfe_u32 v28, v1, 4, 4
v_lshlrev_b32 v28, 3, v28
s_lshl_b32 s80, s56, 9
s_add_u32 s80, s80, 4352
buffer_load_dwordx2 v[28:29], v28, s[8:11], s80 offen sc1
s_waitcnt vmcnt(0)
s_add_u32 s84, s84, 1
v_cmp_eq_u64 vcc, s[12:13], v[28:29]
s_cmp_eq_u64 vcc, -1
s_cbranch_scc1 .Lok_72%=
s_cmp_lt_u32 s84, 1048576
s_cbranch_scc1 .Llp_73%=
s_branch .Lok_72%=
.Lspin_74%=:
s_mov_b32 s84, 0
.Llp_76%=:
s_sleep 2
v_bfe_u32 v28, v1, 4, 4
v_lshlrev_b32 v28, 3, v28
s_lshl_b32 s80, s56, 9
s_add_u32 s80, s80, 4480
buffer_load_dwordx2 v[28:29], v28, s[8:11], s80 offen sc1
s_waitcnt vmcnt(0)
s_add_u32 s84, s84, 1
v_cmp_eq_u64 vcc, s[12:13], v[28:29]
s_cmp_eq_u64 vcc, -1
s_cbranch_scc1 .Lok_75%=
s_cmp_lt_u32 s84, 1048576
s_cbranch_scc1 .Llp_76%=
s_branch .Lok_75%=
.Lspin_77%=:
s_mov_b32 s84, 0
.Llp_79%=:
s_sleep 2
buffer_load_dwordx2 v[8:9], v4, s[8:11], 0 offen offset:2560 sc1
buffer_load_dwordx2 v[10:11], v5, s[8:11], 0 offen offset:3072 sc1
buffer_load_dwordx2 v[12:13], v6, s[8:11], 0 offen offset:3584 sc1
s_waitcnt vmcnt(0)
s_add_u32 s84, s84, 1
v_cmp_eq_u64 vcc, s[12:13], v[8:9]
v_cmp_eq_u64_e64 s[86:87], s[12:13], v[10:11]
s_and_b64 vcc, vcc, s[86:87]
s_or_b64 vcc, vcc, s[20:21]
s_cmp_eq_u64 vcc, -1
s_cbranch_scc1 .Lok_78%=
s_cmp_lt_u32 s84, 1048576
s_cbranch_scc1 .Llp_79%=
s_branch .Lok_78%=
.Lmk_81%=:
s_nop 7
v_cndmask_b32_e64 v168, v241, v168, s[72:73]
v_cndmask_b32_e64 v169, v241, v169, s[74:75]
v_cndmask_b32_e64 v170, v241, v170, s[76:77]
v_cndmask_b32_e64 v171, v241, v171, s[78:79]
v_cndmask_b32_e64 v188, v241, v188, s[72:73]
v_cndmask_b32_e64 v189, v241, v189, s[74:75]
v_cndmask_b32_e64 v190, v241, v190, s[76:77]
v_cndmask_b32_e64 v191, v241, v191, s[78:79]
v_mov_b32 v184, v241
v_mov_b32 v185, v241
v_mov_b32 v186, v241
v_mov_b32 v187, v241
s_branch .Lmkr_82%=
.Lspin_86%=:
s_mov_b32 s84, 0
.Llp_88%=:
s_sleep 2
v_bfe_u32 v28, v1, 4, 2
v_lshlrev_b32 v28, 3, v28
buffer_load_dwordx2 v[28:29], v28, s[8:11], s55 offen sc1
s_waitcnt vmcnt(0)
s_add_u32 s84, s84, 1
v_cmp_eq_u64 vcc, s[12:13], v[28:29]
s_cmp_eq_u64 vcc, -1
s_cbranch_scc1 .Lok_87%=
s_cmp_lt_u32 s84, 1048576
s_cbranch_scc1 .Llp_88%=
s_branch .Lok_87%=
.LL_91%=:
s_mov_b32 s84, 0
.LL_93%=:
s_sleep 2
buffer_load_dwordx2 v[12:13], v27, s[8:11], 0 offen offset:3584 sc1
s_waitcnt vmcnt(0)
s_add_u32 s84, s84, 1
v_xor_b32 v12, s12, v12
v_xor_b32 v13, s13, v13
v_lshrrev_b32 v3, 11, v12
v_or_b32 v13, v13, v3
v_cmp_eq_u32 vcc, 0, v13
s_cmp_eq_u64 vcc, -1
s_cbranch_scc1 .LL_92%=
s_cmp_lt_u32 s84, 1048576
s_cbranch_scc1 .LL_93%=
s_branch .LL_92%=
.Lup_89%=:
v_lshrrev_b32 v3, 8, v1
v_lshlrev_b32 v3, 5, v3
s_ff1_i32_b32 s69, s68
s_mul_i32 s66, s69, 20
s_mul_i32 s67, s58, 5
s_add_u32 s66, s66, s67
s_lshl_b32 s66, s66, 7
s_add_u32 s66, s66, 98304
s_lshl_b32 s67, s14, 7
s_add_u32 s67, s67, s66
buffer_load_dwordx4 v[100:103], v3, s[8:11], s67 offen offset:0 sc1
buffer_load_dwordx4 v[108:111], v3, s[8:11], s67 offen offset:16 sc1
buffer_load_dwordx4 v[116:119], v3, s[8:11], s66 offen offset:512 sc1
buffer_load_dwordx4 v[124:127], v3, s[8:11], s66 offen offset:528 sc1
s_branch .Lupr_90%=
.Lspin_102%=:
s_mov_b32 s84, 0
.Llp_104%=:
)"
R"(s_sleep 2
v_bfe_u32 v28, v1, 4, 2
v_lshlrev_b32 v28, 3, v28
buffer_load_dwordx2 v[28:29], v28, s[8:11], s54 offen sc1
s_waitcnt vmcnt(0)
s_add_u32 s84, s84, 1
v_cmp_eq_u64 vcc, s[12:13], v[28:29]
s_cmp_eq_u64 vcc, -1
s_cbranch_scc1 .Lok_103%=
s_cmp_lt_u32 s84, 1048576
s_cbranch_scc1 .Llp_104%=
s_branch .Lok_103%=
.Lspin_105%=:
s_mov_b32 s84, 0
.Llp_107%=:
s_sleep 2
v_bfe_u32 v28, v1, 4, 2
v_lshlrev_b32 v28, 3, v28
buffer_load_dwordx2 v[28:29], v28, s[8:11], s55 offen sc1
s_waitcnt vmcnt(0)
s_add_u32 s84, s84, 1
v_cmp_eq_u64 vcc, s[12:13], v[28:29]
s_cmp_eq_u64 vcc, -1
s_cbranch_scc1 .Lok_106%=
s_cmp_lt_u32 s84, 1048576
s_cbranch_scc1 .Llp_107%=
s_branch .Lok_106%=
.LL_110%=:
s_mov_b32 s84, 0
.LL_112%=:
s_sleep 2
buffer_load_dwordx2 v[12:13], v27, s[8:11], 0 offen offset:3584 sc1
s_waitcnt vmcnt(0)
s_add_u32 s84, s84, 1
v_xor_b32 v12, s12, v12
v_xor_b32 v13, s13, v13
v_lshrrev_b32 v3, 11, v12
v_or_b32 v13, v13, v3
v_cmp_eq_u32 vcc, 0, v13
s_cmp_eq_u64 vcc, -1
s_cbranch_scc1 .LL_111%=
s_cmp_lt_u32 s84, 1048576
s_cbranch_scc1 .LL_112%=
s_branch .LL_111%=
.Lup_108%=:
v_lshrrev_b32 v3, 8, v1
v_lshlrev_b32 v3, 5, v3
s_ff1_i32_b32 s69, s68
s_mul_i32 s66, s69, 20
s_mul_i32 s67, s58, 5
s_add_u32 s66, s66, s67
s_lshl_b32 s66, s66, 7
s_add_u32 s66, s66, 98304
s_lshl_b32 s67, s14, 7
s_add_u32 s67, s67, s66
buffer_load_dwordx4 v[100:103], v3, s[8:11], s67 offen offset:0 sc1
buffer_load_dwordx4 v[108:111], v3, s[8:11], s67 offen offset:16 sc1
buffer_load_dwordx4 v[116:119], v3, s[8:11], s66 offen offset:512 sc1
buffer_load_dwordx4 v[124:127], v3, s[8:11], s66 offen offset:528 sc1
s_branch .Lupr_109%=
.Lend_254%=:
.Lend%=:
.purgem W
.purgem D
.purgem Ta
.purgem Ra
.purgem Zvaa
.purgem Mvaa
.purgem Zaaa
.purgem Maaa
.purgem A
.purgem U
.purgem K
.purgem Rv
.purgem Mvav
.purgem G
.purgem Zvav
.purgem Cvav
.purgem X
.purgem Cvaa
.purgem Dc
.purgem Maav
.purgem Mvvv
.purgem Zvva
.purgem Mvva
)"
    : : [ka] "s"(__builtin_amdgcn_kernarg_segment_ptr()), [bid] "s"(blockIdx.x), [tid] "v"(threadIdx.x)
    : "v1", "v2", "v3", "v4", "v5", "v6", "v7", "v8", "v9", "v10", "v11", "v12", "v13", "v14", "v15", "v16", "v17", "v18", "v19", "v20", "v21", "v22", "v23", "v24", "v25", "v26", "v27", "v28", "v29", "v30", "v31", "v32", "v33", "v34", "v35", "v36", "v37", "v38", "v39", "v40", "v41", "v42", "v43", "v44", "v45", "v46", "v47", "v48", "v49", "v50", "v51", "v52", "v53", "v54", "v55", "v56", "v57", "v58", "v59", "v60", "v61", "v62", "v63", "v64", "v65", "v66", "v67", "v68", "v69", "v70", "v71", "v72", "v73", "v74", "v75", "v76", "v77", "v78", "v79", "v80", "v81", "v82", "v83", "v84", "v85", "v86", "v87", "v88", "v89", "v90", "v91", "v92", "v93", "v94", "v95", "v96", "v97", "v98", "v99", "v100", "v101", "v102", "v103", "v104", "v105", "v106", "v107", "v108", "v109", "v110", "v111", "v112", "v113", "v114", "v115", "v116", "v117", "v118", "v119", "v120", "v121", "v122", "v123", "v124", "v125", "v126", "v127", "v128", "v129", "v130", "v131", "v132", "v133", "v134", "v135", "v136", "v137", "v138", "v139", "v140", "v141", "v142", "v143", "v144", "v145", "v146", "v147", "v148", "v149", "v150", "v151", "v152", "v153", "v154", "v155", "v156", "v157", "v158", "v159", "v160", "v161", "v162", "v163", "v164", "v165", "v166", "v167", "v168", "v169", "v170", "v171", "v172", "v173", "v174", "v175", "v176", "v177", "v178", "v179", "v180", "v181", "v182", "v183", "v184", "v185", "v186", "v187", "v188", "v189", "v190", "v191", "v192", "v193", "v194", "v195", "v196", "v197", "v198", "v199", "v200", "v201", "v202", "v203", "v204", "v205", "v206", "v207", "v208", "v209", "v210", "v211", "v212", "v213", "v214", "v215", "v216", "v217", "v218", "v219", "v220", "v221", "v222", "v223", "v224", "v225", "v226", "v227", "v228", "v229", "v230", "v231", "v232", "v233", "v234", "v235", "v236", "v237", "v238", "v239", "v240", "v241", "v242", "v243", "v244", "v245", "v246", "v247", "v248", "v249", "v250", "v251", "v252", "v253", "v254", "v255", "a0", "a1", "a2", "a3", "a4", "a5", "a6", "a7", "a8", "a9", "a10", "a11", "a12", "a13", "a14", "a15", "a16", "a17", "a18", "a19", "a20", "a21", "a22", "a23", "a24", "a25", "a26", "a27", "a28", "a29", "a30", "a31", "a32", "a33", "a34", "a35", "a36", "a37", "a38", "a39", "a40", "a41", "a42", "a43", "a44", "a45", "a46", "a47", "a48", "a49", "a50", "a51", "a52", "a53", "a54", "a55", "a56", "a57", "a58", "a59", "a60", "a61", "a62", "a63", "a64", "a65", "a66", "a67", "a68", "a69", "a70", "a71", "a72", "a73", "a74", "a75", "a76", "a77", "a78", "a79", "a80", "a81", "a82", "a83", "a84", "a85", "a86", "a87", "a88", "a89", "a90", "a91", "a92", "a93", "a94", "a95", "a96", "a97", "a98", "a99", "a100", "a101", "a102", "a103", "a104", "a105", "a106", "a107", "a108", "a109", "a110", "a111", "a112", "a113", "a114", "a115", "a116", "a117", "a118", "a119", "a120", "a121", "a122", "a123", "a124", "a125", "a126", "a127", "a128", "a129", "a130", "a131", "a132", "a133", "a134", "a135", "a136", "a137", "a138", "a139", "a140", "a141", "a142", "a143", "a144", "a145", "a146", "a147", "a148", "a149", "a150", "a151", "a152", "a153", "a154", "a155", "a156", "a157", "a158", "a159", "a160", "a161", "a162", "a163", "a164", "a165", "a166", "a167", "a168", "a169", "a170", "a171", "a172", "a173", "a174", "a175", "a176", "a177", "a178", "a179", "a180", "a181", "a182", "a183", "a184", "a185", "a186", "a187", "a188", "a189", "a190", "a191", "a192", "a193", "a194", "a195", "a196", "a197", "a198", "a199", "a200", "a201", "a202", "a203", "a204", "a205", "a206", "a207", "a208", "a209", "a210", "a211", "a212", "a213", "a214", "a215", "a216", "a217", "a218", "a219", "a220", "a221", "a222", "a223", "a224", "a225", "a226", "a227", "a228", "a229", "a230", "a231", "a232", "a233", "a234", "a235", "a236", "a237", "a238", "a239", "a240", "a241", "a242", "a243", "a244", "a245", "a246", "a247", "a248", "a249", "a250", "a251", "a252", "a253", "a254", "a255", "s4", "s5", "s6", "s7", "s8", "s9", "s10", "s11", "s12", "s13", "s14", "s15", "s16", "s17", "s18", "s19", "s20", "s21", "s22", "s23", "s24", "s25", "s26", "s27", "s28", "s29", "s30", "s31", "s32", "s33", "s34", "s35", "s36", "s37", "s38", "s39", "s40", "s41", "s42", "s43", "s44", "s45", "s46", "s47", "s48", "s49", "s50", "s51", "s52", "s53", "s54", "s55", "s56", "s57", "s58", "s59", "s60", "s61", "s62", "s63", "s64", "s65", "s66", "s67", "s68", "s69", "s70", "s71", "s72", "s73", "s74", "s75", "s76", "s77", "s78", "s79", "s80", "s81", "s82", "s83", "s84", "s85", "s86", "s87", "s88", "s89", "s90", "s91", "s92", "s93", "s94", "s95", "s96", "s97", "s98", "s99", "vcc", "scc", "memory"
  );
}
extern "C" void kernel_launch(void* const* d_in, const int* in_sizes, int n_in, void* d_out, int out_size, void* d_ws, size_t ws_size, hipStream_t stream) {
  (void)hipFuncSetAttribute((const void*)mega_kernel, hipFuncAttributeMaxDynamicSharedMemorySize, LDS_BYTES);
  mega_kernel<<<256, 256, LDS_BYTES, stream>>>((const bf16*)d_in[0], (const bf16*)d_in[1], (const bf16*)d_in[2], (const bf16*)d_in[3], (const bf16*)d_in[4], (const bf16*)d_in[5],
      (const bf16*)d_in[7], (const bf16*)d_in[8], (const bf16*)d_in[9], (const bf16*)d_in[10], (char*)d_ws + 0, (bf16*)d_out);
}
```

```cpp
#include <hip/hip_runtime.h>
#include <hip/hip_bf16.h>
typedef __hip_bfloat16 bf16;
constexpr int LDS_BYTES = 163840;
__global__ __launch_bounds__(256, 1) void mega_kernel(const bf16* x, const bf16* ckv, const bf16* wq, const bf16* wuk, const bf16* wuv, const bf16* wo,
    const bf16* w_r, const bf16* wg, const bf16* wu, const bf16* wd, char* ws, bf16* out) {
  asm volatile(
R"(.macro W n
s_waitcnt lgkmcnt(\n)
.endm
.macro D v,r,s,o
buffer_load_dwordx4 v\v, s[\r:\r+3], s\s offen offset:\o lds
.endm
.macro Ta d,a,o
ds_read_b64_tr_b16 a[\d:\d+1], v\a offset:\o
.endm
.macro Ra d,a,o
ds_read_b128 a[\d:\d+3], v\a offset:\o
.endm
.macro Zvaa d,a,b
v_mfma_f32_16x16x32_bf16 v[\d:\d+3], a[\a:\a+3], a[\b:\b+3], 0
.endm
.macro Mvaa d,a,b
v_mfma_f32_16x16x32_bf16 v[\d:\d+3], a[\a:\a+3], a[\b:\b+3], v[\d:\d+3]
.endm
.macro Zaaa d,a,b
v_mfma_f32_16x16x32_bf16 a[\d:\d+3], a[\a:\a+3], a[\b:\b+3], 0
.endm
.macro Maaa d,a,b
v_mfma_f32_16x16x32_bf16 a[\d:\d+3], a[\a:\a+3], a[\b:\b+3], a[\d:\d+3]
.endm
.macro A d,a,b
v_add_f32 v\d, v\a, v\b
.endm
.macro U d,a,b
v_mul_f32 v\d, v\a, v\b
.endm
.macro K d,a,b
v_cvt_pk_bf16_f32 v\d, v\a, v\b
.endm
.macro Rv d,a,o
ds_read_b128 v[\d:\d+3], v\a offset:\o
.endm
.macro Mvav d,a,b
v_mfma_f32_16x16x32_bf16 v[\d:\d+3], a[\a:\a+3], v[\b:\b+3], v[\d:\d+3]
.endm
.macro G d,a
v_accvgpr_read_b32 v\d, a\a
.endm
.macro Zvav d,a,b
v_mfma_f32_16x16x32_bf16 v[\d:\d+3], a[\a:\a+3], v[\b:\b+3], 0
.endm
.macro Cvav d,a,b,c
v_mfma_f32_16x16x32_bf16 v[\d:\d+3], a[\a:\a+3], v[\b:\b+3], v[\c:\c+3]
.endm
.macro X d
v_exp_f32 v\d, v\d
.endm
.macro Cvaa d,a,b,c
v_mfma_f32_16x16x32_bf16 v[\d:\d+3], a[\a:\a+3], a[\b:\b+3], v[\c:\c+3]
.endm
.macro Dc v,r,s,o
buffer_load_dwordx4 v\v, s[\r:\r+3], s\s offen offset:\o sc1 lds
.endm
.macro Maav d,a,b
v_mfma_f32_16x16x32_bf16 a[\d:\d+3], a[\a:\a+3], v[\b:\b+3], a[\d:\d+3]
.endm
.macro Mvvv d,a,b
v_mfma_f32_16x16x32_bf16 v[\d:\d+3], v[\a:\a+3], v[\b:\b+3], v[\d:\d+3]
.endm
.macro Zvva d,a,b
v_mfma_f32_16x16x32_bf16 v[\d:\d+3], v[\a:\a+3], a[\b:\b+3], 0
.endm
.macro Mvva d,a,b
v_mfma_f32_16x16x32_bf16 v[\d:\d+3], v[\a:\a+3], a[\b:\b+3], v[\d:\d+3]
.endm
s_load_dwordx16 s[16:31], %[ka], 0
s_load_dwordx8 s[32:39], %[ka], 64
s_getpc_b64 s[6:7]
.Lbeg%=:
s_mov_b32 s15, %[bid]
v_lshrrev_b32 v2, 6, %[tid]
v_and_b32 v100, 63, %[tid]
v_readfirstlane_b32 s14, v2
v_lshlrev_b32 v1, 4, v100
v_lshrrev_b32 v101, 4, v100
v_and_b32 v102, 15, v100
s_and_b32 s59, s15, 7
s_bfe_u32 s57, s15, 0x30003
s_lshr_b32 s58, s15, 6
s_lshl_b32 s56, s59, 2
s_add_u32 s56, s56, s58
s_lshr_b32 s80, s15, 3
s_lshl_b32 s80, s80, 2
s_add_u32 s80, s80, s14
s_lshl_b32 s80, s80, 11
v_min_u32 v146, 240, v1
v_lshl_add_u32 v146, v146, 3, s80
v_min_u32 v146, .Lend%=-.Lbeg%=-4, v146
global_load_dword v142, v146, s[6:7]
v_and_b32 v88, 0x1f0, v1
v_lshrrev_b32 v105, 1, v101
v_mul_u32_u24 v105, 1280, v105
v_add_u32 v28, v88, v105
s_lshl_b32 s85, s15, 2
s_add_u32 s85, s85, s14
s_mul_i32 s84, s85, 2560
v_and_b32 v105, 1, v101
v_lshl_or_b32 v29, v105, 31, v28
v_lshlrev_b32 v104, 1, v101
v_xor_b32 v104, v104, v102
v_lshlrev_b32 v104, 4, v104
v_lshl_add_u32 v16, v101, 13, v104
v_xor_b32 v105, 128, v16
v_add_u32 v17, 31744, v16
v_add_u32 v18, 63488, v105
v_add_u32 v19, 95232, v105
v_add_u32 v20, 131072, v16
v_add_u32 v21, 162816, v16
v_add_u32 v22, 194560, v105
v_add_u32 v23, 226304, v105
s_lshl_b32 s80, s14, 3
s_add_u32 s80, s80, s57
s_lshl_b32 s80, s80, 15
v_and_b32 v104, 7, v100
v_xor_b32 v104, v104, v101
v_lshlrev_b32 v104, 4, v104
v_lshrrev_b32 v105, 3, v100
v_lshl_add_u32 v104, v105, 10, v104
v_add_u32 v2, s80, v104
v_xor_b32 v105, 64, v2
v_add_u32 v3, 7168, v105
v_add_u32 v4, 14336, v2
v_add_u32 v5, 21504, v105
v_xor_b32 v6, 1835008, v2
v_xor_b32 v7, 1835008, v3
v_xor_b32 v8, 1835008, v4
v_xor_b32 v9, 1835008, v5
s_lshl_b32 s63, s14, 13
s_add_u32 s61, s63, 65536
s_and_b32 s80, s14, 1
s_lshr_b32 s81, s14, 1
s_lshl_b32 s62, s80, 14
s_lshl_b32 s82, s81, 13
s_add_u32 s62, s62, s82
s_lshl_b32 s81, s81, 18
s_lshl_b32 s83, s56, 8
s_add_u32 s81, s81, s83
s_mov_b32 s64, 0
s_cmp_eq_u32 s80, 0
W 0
s_cselect_b32 s44, s22, s24
s_cselect_b32 s45, s23, s25
s_mov_b32 s40, s18
s_and_b32 s41, s19, 0xffff
s_mov_b32 s42, 0x200000
s_mov_b32 s43, 0x20000
s_add_u32 s44, s44, s81
s_addc_u32 s45, s45, 0
s_and_b32 s45, s45, 0xffff
s_mov_b32 s46, 0x400000
s_mov_b32 s47, 0x20000
s_sub_u32 s46, s46, s81
s_load_dwordx2 s[12:13], s[36:37], 0
s_lshl_b32 s65, s64, 12
s_add_u32 m0, s62, 0
s_nop 0
D 16,44,65,0
D 17,44,65,1024
D 18,44,65,2048
D 19,44,65,3072
s_add_u32 m0, s62, 4096
s_nop 0
D 20,44,65,0
D 21,44,65,1024
D 22,44,65,2048
D 23,44,65,3072
s_add_u32 m0, s61, 0
s_nop 0
D 2,40,64,0
D 3,40,64,1024
D 4,40,64,2048
D 5,40,64,3072
s_add_u32 m0, s61, 4096
s_nop 0
D 6,40,64,0
D 7,40,64,1024
D 8,40,64,2048
D 9,40,64,3072
s_mov_b32 s52, s16
s_and_b32 s53, s17, 0xffff
s_mov_b32 s54, 2621440
s_mov_b32 s55, 0x20000
buffer_load_dwordx4 v[32:35], v28, s[52:55], s84 offen
buffer_load_dwordx4 v[36:39], v28, s[52:55], s84 offen offset:512
buffer_load_dwordx4 v[40:43], v29, s[52:55], s84 offen offset:1024
s_mov_b32 s52, s28
s_and_b32 s53, s29, 0xffff
s_mov_b32 s54, 1280
s_mov_b32 s55, 0x20000
buffer_load_dwordx4 v[44:47], v88, s[52:55], 0 offen offset:0
buffer_load_dwordx4 v[48:51], v88, s[52:55], 0 offen offset:512
buffer_load_dwordx4 v[52:55], v88, s[52:55], 0 offen offset:1024
s_mov_b32 s69, 0
s_cmp_eq_u32 s14, 2
s_cbranch_scc1 .LL_1%=
s_cmp_eq_u32 s14, 3
s_cbranch_scc1 .LL_2%=
s_lshl_b32 s80, s58, 3
s_add_u32 s80, s80, s57
s_lshl_b32 s80, s80, 15
s_lshl_b32 s82, s14, 7
s_add_u32 s80, s80, s82
s_add_u32 s80, s80, 256
v_lshl_add_u32 v104, v88, 6, s80
v_and_b32 v105, 512, v1
v_mul_u32_u24 v105, 3584, v105
v_xor_b32 v104, v104, v105
v_mov_b32 v140, s18
v_mov_b32 v141, s19
s_mov_b32 s68, 256
s_branch .LL_3%=
.LL_1%=:
v_lshrrev_b32 v104, 6, v1
v_lshlrev_b32 v104, 13, v104
v_and_b32 v105, 16, v1
v_lshl_add_u32 v104, v105, 3, v104
s_lshl_b32 s80, s57, 17
s_add_u32 s80, s80, s83
s_add_u32 s80, s80, 1048576
v_add_u32 v104, s80, v104
v_and_b32 v105, 32, v1
v_cmp_eq_u32 vcc, 0, v105
v_mov_b32 v140, s24
v_mov_b32 v141, s25
v_mov_b32 v105, s22
v_mov_b32 v106, s23
v_cndmask_b32 v140, v140, v105, vcc
v_cndmask_b32 v141, v141, v106, vcc
s_mov_b32 s68, 1048576
s_branch .LL_3%=
.LL_2%=:
v_lshrrev_b32 v104, 5, v1
v_lshlrev_b32 v104, 13, v104
v_and_b32 v105, 16, v1
v_lshl_add_u32 v104, v105, 3, v104
s_mul_i32 s80, s57, 655360
s_add_u32 s80, s80, s83
v_add_u32 v104, s80, v104
v_mov_b32 v140, s20
v_mov_b32 v141, s21
s_mov_b32 s68, 196608
v_mov_b32 v104, 0
s_mov_b32 s68, 0
.LL_3%=:
v_mov_b32 v105, 0
v_lshl_add_u64 v[140:141], v[104:105], 0, v[140:141]
s_cmp_eq_u32 s14, 3
s_cbranch_scc0 .LL_4%=
s_lshr_b32 s88, s15, 3
s_cmp_lt_u32 s88, 14
s_cbranch_scc0 .LL_4%=
v_lshlrev_b32 v104, 16, v1
s_mov_b64 s[86:87], s[36:37]
s_mov_b32 s89, 38797312
s_cmp_eq_u32 s88, 1
s_cselect_b32 s90, 39845888, 0
s_add_u32 s86, s86, s90
s_addc_u32 s87, s87, 0
s_cmp_eq_u32 s88, 2
s_cselect_b64 s[86:87], s[26:27], s[86:87]
s_cselect_b32 s89, 5242876, s89
s_cmp_eq_u32 s88, 3
s_cselect_b64 s[86:87], s[30:31], s[86:87]
s_cselect_b32 s89, 2211836, s89
s_cmp_eq_u32 s88, 4
s_cselect_b64 s[86:87], s[32:33], s[86:87]
s_cselect_b32 s89, 2211836, s89
s_cmp_eq_u32 s88, 5
s_cselect_b64 s[86:87], s[34:35], s[86:87]
s_cselect_b32 s89, 2211836, s89
s_cmp_eq_u32 s88, 6
s_cselect_b64 s[86:87], s[38:39], s[86:87]
s_cselect_b32 s89, 2621436, s89
s_cmp_eq_u32 s88, 7
s_cselect_b64 s[86:87], s[20:21], s[86:87]
s_cselect_b32 s89, 5242876, s89
s_cmp_eq_u32 s88, 8
s_cselect_b64 s[86:87], s[18:19], s[86:87]
s_cselect_b32 s89, 2097148, s89
s_cmp_eq_u32 s88, 9
s_cselect_b64 s[86:87], s[22:23], s[86:87]
s_cselect_b32 s89, 4194300, s89
s_cmp_eq_u32 s88, 10
s_cselect_b64 s[86:87], s[24:25], s[86:87]
s_cselect_b32 s89, 4194300, s89
s_cmp_eq_u32 s88, 11
s_cselect_b64 s[86:87], s[16:17], s[86:87]
s_cselect_b32 s89, 2621436, s89
s_cmp_eq_u32 s88, 12
s_cselect_b64 s[86:87], s[28:29], s[86:87]
s_cselect_b32 s89, 1276, s89
s_cmp_eq_u32 s88, 13
s_cselect_b64 s[86:87], s[6:7], s[86:87]
s_cselect_b32 s89, 0, s89
v_min_u32 v104, s89, v104
global_load_dword v142, v104, s[86:87] sc1
.LL_4%=:
v_lshrrev_b32 v104, 1, v102
v_xor_b32 v104, v104, v101
v_lshlrev_b32 v104, 4, v104
v_lshl_add_u32 v104, v102, 7, v104
s_lshr_b32 s81, s14, 1
s_lshl_b32 s81, s81, 14
s_add_u32 s81, s81, 65536
v_add_u32 v26, s81, v104
v_xor_b32 v27, 64, v26
v_and_b32 v105, 1, v101
v_lshrrev_b32 v104, 2, v102
v_lshl_or_b32 v105, v105, 2, v104
v_lshl_add_u32 v104, v101, 3, v104
v_lshlrev_b32 v104, 8, v104
v_lshl_add_u32 v104, v105, 5, v104
v_and_b32 v105, 3, v102
v_lshl_add_u32 v10, v105, 3, v104
v_xor_b32 v11, 32, v10
v_xor_b32 v12, 64, v10
v_xor_b32 v13, 96, v10
v_xor_b32 v14, 128, v10
v_xor_b32 v15, 160, v10
v_xor_b32 v24, 192, v10
v_xor_b32 v25, 224, v10
s_mov_b32 s8, s36
s_and_b32 s9, s37, 0xffff
s_mov_b32 s10, 79691776
s_mov_b32 s11, 0x20000
s_add_u32 s48, s20, s83
s_addc_u32 s49, s21, 0
s_and_b32 s49, s49, 0xffff
s_mov_b32 s50, 5242880
s_mov_b32 s51, 0x20000
s_sub_u32 s50, s50, s83
s_lshl_b32 s80, s57, 1
s_and_b32 s81, s14, 1
s_add_u32 s80, s80, s81
s_sub_u32 s81, 31, s80
s_cmp_lt_u32 s14, 2
s_cselect_b32 s60, s80, s81
s_mul_i32 s28, s60, 81920
s_add_u32 s28, s28, 131072
s_add_u32 s4, s28, 10240
s_mov_b32 s80, 0
s_lshl_b32 s81, s80, 9
s_add_u32 s28, s28, s81
s_add_u32 s5, s80, s14
s_cmp_ge_u32 s5, 20
s_cselect_b32 s81, 20, 0
s_sub_u32 s5, s5, s81
s_and_b32 s80, s14, 1
s_mul_i32 s29, s80, 17172480
s_mul_i32 s80, s56, 536576
s_add_u32 s29, s29, s80
s_lshl_b32 s80, s57, 13
s_add_u32 s29, s29, s80
s_add_u32 s29, s29, 4194304
W 0
s_add_u32 s12, s12, 0x7f4a7c15
s_addc_u32 s13, s13, 0x9e3779b9
s_mov_b64 exec, 0
s_cmp_eq_u32 s14, 0
s_cbranch_scc1 .Lh_0_0%=
s_cmp_eq_u32 s14, 1
s_cbranch_scc1 .Lh_0_1%=
s_cmp_eq_u32 s14, 2
s_cbranch_scc1 .Lh_0_2%=
s_branch .Lh_0_3%=
.Lhr_5%=:
s_mov_b64 exec, -1
s_add_u32 s64, s64, 128
s_and_b32 s64, s64, 1023
s_lshl_b32 s65, s64, 12
s_add_u32 m0, s62, 32768
s_nop 0
D 16,44,65,0
D 17,44,65,1024
D 18,44,65,2048
D 19,44,65,3072
s_add_u32 m0, s62, 36864
s_nop 0
D 20,44,65,0
D 21,44,65,1024
D 22,44,65,2048
D 23,44,65,3072
s_bitcmp1_b32 s14, 0
s_cbranch_scc1 .Lv_6%=
s_branch .LL_8%=
.p2align 12
.LL_8%=:
s_waitcnt vmcnt(14)
s_barrier
Ta 0,10,0
Ta 2,10,1024
Ra 32,26,0
Ta 4,10,8192
.Lh_0_0%=:
s_cbranch_execz .Lh_0_0_1%=
Ta 6,10,9216
Ra 128,27,0
Ra 132,27,2048
Ra 36,26,2048
Ra 40,26,4096
Ra 136,27,4096
Ra 140,27,6144
Ra 44,26,6144
Ra 48,26,8192
Ra 144,27,8192
W 6
Zvaa 160,0,32
s_add_u32 m0, s61, 32768
Ra 148,27,10240
Ra 52,26,10240
Ra 56,26,12288
Ra 152,27,12288
Ra 156,27,14336
Ra 60,26,14336
Mvaa 160,4,128
Ta 8,11,0
Ta 10,11,1024
Ta 12,11,8192
Ta 14,11,9216
Zvaa 192,4,132
Ta 16,12,0
Ta 18,12,1024
Mvaa 192,0,36
W 14
Ta 20,12,8192
Ta 22,12,9216
Zvaa 224,0,40
D 2,40,64,0
Mvaa 224,4,136
Zaaa 160,4,140
Maaa 160,0,44
W 12
Zaaa 192,0,48
D 3,40,64,1024
Maaa 192,4,144
Zaaa 224,4,148
Maaa 224,0,52
W 8
Zaaa 64,0,56
D 4,40,64,2048
Maaa 64,4,152
Zaaa 96,4,156
)"
R"(Maaa 96,0,60
W 4
Zaaa 100,8,60
D 5,40,64,3072
Maaa 100,12,156
s_add_u32 m0, s61, 36864
Zaaa 68,12,152
Ta 24,13,0
Ta 26,13,1024
Maaa 68,8,56
Ta 28,13,8192
Ta 30,13,9216
Zaaa 228,8,52
s_waitcnt vmcnt(15)
D 6,40,64,0
Maaa 228,12,148
v_lshlrev_b32 v56, 16, v32
v_and_b32 v57, 0xffff0000, v32
v_lshlrev_b32 v58, 16, v33
v_and_b32 v59, 0xffff0000, v33
v_lshlrev_b32 v60, 16, v34
Zaaa 196,12,144
v_and_b32 v61, 0xffff0000, v34
v_lshlrev_b32 v62, 16, v35
v_and_b32 v63, 0xffff0000, v35
v_lshlrev_b32 v64, 16, v36
v_and_b32 v65, 0xffff0000, v36
Maaa 196,8,48
v_lshlrev_b32 v66, 16, v37
v_and_b32 v67, 0xffff0000, v37
v_lshlrev_b32 v68, 16, v38
v_and_b32 v69, 0xffff0000, v38
v_lshlrev_b32 v70, 16, v39
Zaaa 164,8,44
D 7,40,64,1024
v_and_b32 v71, 0xffff0000, v39
v_lshlrev_b32 v72, 16, v40
v_and_b32 v73, 0xffff0000, v40
v_lshlrev_b32 v74, 16, v41
v_and_b32 v75, 0xffff0000, v41
Maaa 164,12,140
v_lshlrev_b32 v76, 16, v42
v_and_b32 v77, 0xffff0000, v42
v_lshlrev_b32 v78, 16, v43
v_and_b32 v79, 0xffff0000, v43
A 80,56,57
Zvaa 228,12,136
A 81,58,59
A 80,80,60
A 81,81,61
A 80,80,62
A 81,81,63
Mvaa 228,8,40
A 80,80,64
A 81,81,65
A 80,80,66
A 81,81,67
A 80,80,68
Zvaa 196,8,36
D 8,40,64,2048
A 81,81,69
A 80,80,70
A 81,81,71
A 80,80,72
A 81,81,73
Mvaa 196,12,132
A 80,80,74
A 81,81,75
A 80,80,76
A 81,81,77
A 80,80,78
A 81,81,79
Zvaa 164,12,128
A 80,80,81
s_nop 1
v_add_f32_dpp v80, v80, v80 quad_perm:[1,0,3,2] row_mask:0xf bank_mask:0xf
s_nop 1
v_add_f32_dpp v80, v80, v80 quad_perm:[2,3,0,1] row_mask:0xf bank_mask:0xf
s_nop 1
v_add_f32_dpp v80, v80, v80 row_half_mirror row_mask:0xf bank_mask:0xf
s_nop 1
v_add_f32_dpp v80, v80, v80 row_mirror row_mask:0xf bank_mask:0xf
Mvaa 164,8,32
W 4
v_mov_b32 v81, v80
s_nop 1
v_permlane16_swap_b32 v80, v81
A 80,80,81
v_mul_f32 v84, 0x3acccccd, v80
s_mov_b32 s86, 0xffff
Zvaa 168,16,32
s_mov_b32 s87, 0xffff
D 9,40,64,3072
v_sub_f32 v82, v56, v84
U 80,82,82
v_sub_f32 v83, v57, v84
v_fmac_f32 v80, v83, v83
Mvaa 168,20,128
v_sub_f32 v82, v58, v84
v_fmac_f32 v80, v82, v82
v_sub_f32 v83, v59, v84
v_fmac_f32 v80, v83, v83
v_sub_f32 v82, v60, v84
Zvaa 200,20,132
Ta 0,14,0
v_fmac_f32 v80, v82, v82
v_sub_f32 v83, v61, v84
v_fmac_f32 v80, v83, v83
v_sub_f32 v82, v62, v84
v_fmac_f32 v80, v82, v82
Mvaa 200,16,36
Ta 2,14,1024
v_sub_f32 v83, v63, v84
v_fmac_f32 v80, v83, v83
v_sub_f32 v82, v64, v84
v_fmac_f32 v80, v82, v82
v_sub_f32 v83, v65, v84
Zvaa 232,16,40
global_load_dword v142, v[140:141], off
v_lshl_add_u64 v[140:141], v[140:141], 0, s[68:69]
Ta 4,14,8192
Ta 6,14,9216
v_fmac_f32 v80, v83, v83
v_sub_f32 v82, v66, v84
v_fmac_f32 v80, v82, v82
Mvaa 232,20,136
v_sub_f32 v83, v67, v84
v_fmac_f32 v80, v83, v83
v_sub_f32 v82, v68, v84
v_fmac_f32 v80, v82, v82
v_sub_f32 v83, v69, v84
v_fmac_f32 v80, v83, v83
Zaaa 168,20,140
v_sub_f32 v82, v70, v84
v_fmac_f32 v80, v82, v82
v_sub_f32 v83, v71, v84
v_fmac_f32 v80, v83, v83
v_sub_f32 v82, v72, v84
U 81,82,82
Maaa 168,16,44
v_sub_f32 v83, v73, v84
v_fmac_f32 v81, v83, v83
v_sub_f32 v82, v74, v84
v_fmac_f32 v81, v82, v82
v_sub_f32 v83, v75, v84
v_fmac_f32 v81, v83, v83
Zaaa 200,16,48
v_sub_f32 v82, v76, v84
v_fmac_f32 v81, v82, v82
v_sub_f32 v83, v77, v84
v_fmac_f32 v81, v83, v83
v_sub_f32 v82, v78, v84
Maaa 200,20,144
v_fmac_f32 v81, v82, v82
v_sub_f32 v83, v79, v84
v_fmac_f32 v81, v83, v83
v_cndmask_b32_e64 v81, 0, v81, s[86:87]
A 80,80,81
Zaaa 232,20,148
Maaa 232,16,52
v_add_f32_dpp v80, v80, v80 quad_perm:[1,0,3,2] row_mask:0xf bank_mask:0xf
s_nop 1
v_add_f32_dpp v80, v80, v80 quad_perm:[2,3,0,1] row_mask:0xf bank_mask:0xf
s_nop 1
v_add_f32_dpp v80, v80, v80 row_half_mirror row_mask:0xf bank_mask:0xf
s_nop 1
v_add_f32_dpp v80, v80, v80 row_mirror row_mask:0xf bank_mask:0xf
v_mov_b32 v81, v80
s_nop 1
v_permlane16_swap_b32 v80, v81
Zaaa 72,16,56
A 80,80,81
v_mul_f32 v80, 0x3acccccd, v80
K 80,80,80
v_lshlrev_b32 v80, 16, v80
v_add_f32 v80, 0x3727c5ac, v80
K 80,80,80
Maaa 72,20,152
v_lshlrev_b32 v80, 16, v80
v_sqrt_f32 v81, v80
s_nop 0
v_rcp_f32 v82, v81
v_fma_f32 v83, -v81, v81, v80
Zaaa 104,20,156
v_mul_f32 v82, 0.5, v82
v_fma_f32 v81, v83, v82, v81
K 86,81,81
v_lshlrev_b32 v86, 16, v86
v_rcp_f32 v87, v86
Maaa 104,16,60
W 4
K 85,84,84
v_lshlrev_b32 v85, 16, v85
s_waitcnt vmcnt(17)
v_sub_f32 v56, v56, v85
v_sub_f32 v57, v57, v85
K 80,56,57
Zaaa 108,24,60
v_lshlrev_b32 v56, 16, v80
v_and_b32 v57, 0xffff0000, v80
U 80,56,87
U 81,57,87
K 32,80,81
v_lshlrev_b32 v80, 16, v32
Maaa 108,28,156
v_and_b32 v81, 0xffff0000, v32
v_lshlrev_b32 v82, 16, v44
v_and_b32 v83, 0xffff0000, v44
U 84,80,82
v_fmac_f32 v84, v81, v83
Zaaa 76,28,152
Ta 8,15,0
v_sub_f32 v58, v58, v85
v_sub_f32 v59, v59, v85
K 80,58,59
v_lshlrev_b32 v58, 16, v80
v_and_b32 v59, 0xffff0000, v80
Maaa 76,24,56
Ta 10,15,1024
U 80,58,87
U 81,59,87
K 33,80,81
v_lshlrev_b32 v80, 16, v33
v_and_b32 v81, 0xffff0000, v33
Zaaa 236,24,52
Ta 12,15,8192
Ta 14,15,9216
v_lshlrev_b32 v82, 16, v45
v_and_b32 v83, 0xffff0000, v45
v_fmac_f32 v84, v80, v82
v_fmac_f32 v84, v81, v83
Maaa 236,28,148
v_sub_f32 v60, v60, v85
v_sub_f32 v61, v61, v85
K 80,60,61
v_lshlrev_b32 v60, 16, v80
v_and_b32 v61, 0xffff0000, v80
U 80,60,87
Zaaa 204,28,144
U 81,61,87
K 34,80,81
v_lshlrev_b32 v80, 16, v34
v_and_b32 v81, 0xffff0000, v34
v_lshlrev_b32 v82, 16, v46
v_and_b32 v83, 0xffff0000, v46
Maaa 204,24,48
v_fmac_f32 v84, v80, v82
v_fmac_f32 v84, v81, v83
v_sub_f32 v62, v62, v85
v_sub_f32 v63, v63, v85
K 80,62,63
Zaaa 172,24,44
v_lshlrev_b32 v62, 16, v80
v_and_b32 v63, 0xffff0000, v80
U 80,62,87
U 81,63,87
K 35,80,81
Maaa 172,28,140
v_lshlrev_b32 v80, 16, v35
v_and_b32 v81, 0xffff0000, v35
v_lshlrev_b32 v82, 16, v47
v_and_b32 v83, 0xffff0000, v47
v_fmac_f32 v84, v80, v82
Zvaa 236,28,136
v_fmac_f32 v84, v81, v83
v_sub_f32 v64, v64, v85
v_sub_f32 v65, v65, v85
K 80,64,65
v_lshlrev_b32 v64, 16, v80
Mvaa 236,24,40
v_and_b32 v65, 0xffff0000, v80
U 80,64,87
U 81,65,87
K 36,80,81
v_lshlrev_b32 v80, 16, v36
Zvaa 204,24,36
v_and_b32 v81, 0xffff0000, v36
v_lshlrev_b32 v82, 16, v48
v_and_b32 v83, 0xffff0000, v48
v_fmac_f32 v84, v80, v82
v_fmac_f32 v84, v81, v83
Mvaa 204,28,132
v_sub_f32 v66, v66, v85
v_sub_f32 v67, v67, v85
K 80,66,67
v_lshlrev_b32 v66, 16, v80
v_and_b32 v67, 0xffff0000, v80
U 80,66,87
Zvaa 172,28,128
U 81,67,87
K 37,80,81
v_lshlrev_b32 v80, 16, v37
v_and_b32 v81, 0xffff0000, v37
v_lshlrev_b32 v82, 16, v49
Mvaa 172,24,32
W 4
v_and_b32 v83, 0xffff0000, v49
v_fmac_f32 v84, v80, v82
v_fmac_f32 v84, v81, v83
v_sub_f32 v68, v68, v85
v_sub_f32 v69, v69, v85
Zvaa 176,0,32
K 80,68,69
v_lshlrev_b32 v68, 16, v80
v_and_b32 v69, 0xffff0000, v80
U 80,68,87
U 81,69,87
Mvaa 176,4,128
K 38,80,81
v_lshlrev_b32 v80, 16, v38
v_and_b32 v81, 0xffff0000, v38
v_lshlrev_b32 v82, 16, v50
v_and_b32 v83, 0xffff0000, v50
Zvaa 208,4,132
Ta 16,24,0
v_fmac_f32 v84, v80, v82
v_fmac_f32 v84, v81, v83
v_sub_f32 v70, v70, v85
v_sub_f32 v71, v71, v85
K 80,70,71
Mvaa 208,0,36
Ta 18,24,1024
v_lshlrev_b32 v70, 16, v80
v_and_b32 v71, 0xffff0000, v80
U 80,70,87
U 81,71,87
K 39,80,81
Zvaa 240,0,40
Ta 20,24,8192
Ta 22,24,9216
v_lshlrev_b32 v80, 16, v39
v_and_b32 v81, 0xffff0000, v39
v_lshlrev_b32 v82, 16, v51
v_and_b32 v83, 0xffff0000, v51
Mvaa 240,4,136
v_fmac_f32 v84, v80, v82
v_fmac_f32 v84, v81, v83
v_sub_f32 v72, v72, v85
v_sub_f32 v73, v73, v85
K 80,72,73
v_lshlrev_b32 v72, 16, v80
Zaaa 176,4,140
v_and_b32 v73, 0xffff0000, v80
U 80,72,87
U 81,73,87
K 40,80,81
v_lshlrev_b32 v80, 16, v40
v_and_b32 v81, 0xffff0000, v40
Maaa 176,0,44
v_lshlrev_b32 v82, 16, v52
v_and_b32 v83, 0xffff0000, v52
v_fmac_f32 v84, v80, v82
v_fmac_f32 v84, v81, v83
v_sub_f32 v74, v74, v85
Zaaa 208,0,48
v_sub_f32 v75, v75, v85
K 80,74,75
v_lshlrev_b32 v74, 16, v80
v_and_b32 v75, 0xffff0000, v80
U 80,74,87
Maaa 208,4,144
U 81,75,87
K 41,80,81
v_lshlrev_b32 v80, 16, v41
v_and_b32 v81, 0xffff0000, v41
v_lshlrev_b32 v82, 16, v53
Zaaa 240,4,148
v_and_b32 v83, 0xffff0000, v53
v_fmac_f32 v84, v80, v82
v_fmac_f32 v84, v81, v83
v_sub_f32 v76, v76, v85
v_sub_f32 v77, v77, v85
Maaa 240,0,52
K 80,76,77
v_lshlrev_b32 v76, 16, v80
v_and_b32 v77, 0xffff0000, v80
U 80,76,87
U 81,77,87
Zaaa 80,0,56
K 42,80,81
v_lshlrev_b32 v80, 16, v42
v_and_b32 v81, 0xffff0000, v42
v_lshlrev_b32 v82, 16, v54
v_and_b32 v83, 0xffff0000, v54
Maaa 80,4,152
v_fmac_f32 v84, v80, v82
v_fmac_f32 v84, v81, v83
v_sub_f32 v78, v78, v85
v_sub_f32 v79, v79, v85
K 80,78,79
v_lshlrev_b32 v78, 16, v80
Zaaa 112,4,156
v_and_b32 v79, 0xffff0000, v80
U 80,78,87
U 81,79,87
K 43,80,81
v_lshlrev_b32 v80, 16, v43
Maaa 112,0,60
W 4
v_and_b32 v81, 0xffff0000, v43
)"
R"(v_lshlrev_b32 v82, 16, v55
v_and_b32 v83, 0xffff0000, v55
v_fmac_f32 v84, v80, v82
v_fmac_f32 v84, v81, v83
Zaaa 116,8,60
Ta 24,25,0
Ta 26,25,1024
v_lshrrev_b32 v82, 9, v1
s_lshl_b32 s80, s14, 1
v_add_u32 v82, s80, v82
v_mul_u32_u24 v83, 1552, v82
v_add_u32 v83, v83, v88
Maaa 116,12,156
v_add_u32 v83, 131072, v83
ds_write_b128 v83, v[32:35] offset:0
ds_write_b128 v83, v[36:39] offset:512
ds_write_b128 v83, v[40:43] offset:1024
v_add_f32_dpp v84, v84, v84 quad_perm:[1,0,3,2] row_mask:0xf bank_mask:0xf
Zaaa 84,12,152
Ta 28,25,8192
Ta 30,25,9216
Maaa 84,8,56
v_add_f32_dpp v84, v84, v84 quad_perm:[2,3,0,1] row_mask:0xf bank_mask:0xf
s_nop 1
v_add_f32_dpp v84, v84, v84 row_half_mirror row_mask:0xf bank_mask:0xf
s_nop 1
v_add_f32_dpp v84, v84, v84 row_mirror row_mask:0xf bank_mask:0xf
v_mov_b32 v81, v84
s_nop 1
v_permlane16_swap_b32 v84, v81
A 84,84,81
Zaaa 244,8,52
K 80,84,84
v_lshlrev_b32 v80, 16, v80
s_lshl_b32 s80, s15, 3
v_add_u32 v82, s80, v82
v_sub_u32 v83, 2047, v82
v_ashrrev_i32 v81, 31, v80
v_or_b32 v81, 0x80000000, v81
Maaa 244,12,148
v_xor_b32 v80, v80, v81
v_and_b32 v80, 0xffff0000, v80
v_or_b32 v80, v80, v83
v_and_b32 v82, 7, v82
v_lshlrev_b32 v82, 2, v82
v_add_u32 v82, 143488, v82
Zaaa 212,12,144
ds_write_b32 v82, v80
Maaa 212,8,48
Zaaa 180,8,44
Maaa 180,12,140
Zvaa 244,12,136
s_waitcnt vmcnt(1) lgkmcnt(0)
s_barrier
Mvaa 244,8,40
s_add_u32 s64, s64, 128
v_bfe_u32 v80, v1, 4, 3
Zvaa 212,8,36
v_mul_u32_u24 v81, 1552, v80
Mvaa 212,12,132
s_and_b32 s64, s64, 1023
v_lshrrev_b32 v82, 7, v1
Zvaa 180,12,128
v_lshl_add_u32 v81, v82, 4, v81
Mvaa 180,8,32
s_lshl_b32 s80, s14, 7
Zvaa 184,16,32
s_lshl_b32 s65, s64, 12
s_add_u32 s80, s80, 131072
Mvaa 184,20,128
v_add_u32 v81, s80, v81
Zvaa 216,20,132
ds_read_b128 v[32:35], v81
Ta 0,10,32768
Mvaa 216,16,36
s_add_u32 m0, s62, 0
Ta 2,10,33792
Rv 36,81,512
Zvaa 248,16,40
s_and_b32 s80, s14, 2
Ta 4,10,40960
Ta 6,10,41984
Mvaa 248,20,136
s_lshl_b32 s80, s80, 7
Zaaa 184,20,140
D 16,44,65,0
v_subrev_u32 v81, s80, v81
Maaa 184,16,44
Rv 40,81,1024
Zaaa 216,16,48
v_lshlrev_b32 v80, 2, v80
Maaa 216,20,144
v_add_u32 v80, 143488, v80
Zaaa 248,20,148
D 17,44,65,1024
ds_read_b32 v84, v80
Maaa 248,16,52
v_mov_b32 v85, s13
Zaaa 88,16,56
v_lshrrev_b32 v83, 1, v1
Maaa 88,20,152
s_mul_i32 s82, s15, 10240
Zaaa 120,20,156
s_add_u32 s82, s82, 131072
D 18,44,65,2048
Maaa 120,16,60
s_lshl_b32 s80, s14, 10
Zaaa 124,24,60
s_add_u32 s83, s82, s80
Ra 60,26,47104
Maaa 124,28,156
Ra 156,27,47104
Zaaa 92,28,152
D 19,44,65,3072
Ra 152,27,45056
Maaa 92,24,56
s_add_u32 m0, s62, 4096
Ta 8,11,32768
Ta 10,11,33792
Zaaa 252,24,52
Ra 56,26,45056
Ta 12,11,40960
Maaa 252,28,148
Ta 14,11,41984
Ra 52,26,43008
Zaaa 220,28,144
W 9
buffer_store_dwordx4 v[32:35], v1, s[8:11], s83 offen sc1
s_add_u32 s83, s83, 4096
Maaa 220,24,48
Ra 148,27,43008
Ra 144,27,40960
Zaaa 188,24,44
Ra 48,26,40960
Ra 44,26,38912
Maaa 188,28,140
Ra 140,27,38912
Zvaa 252,28,136
D 20,44,65,0
Ra 136,27,36864
Mvaa 252,24,40
Ra 40,26,36864
Zvaa 220,24,36
Ra 36,26,34816
Mvaa 220,28,132
Ra 132,27,34816
Zvaa 188,28,128
buffer_store_dwordx4 v[36:39], v1, s[8:11], s83 offen sc1
Ra 128,27,32768
Mvaa 188,24,32
W 13
s_and_b32 s80, s80, 1024
s_add_u32 s83, s82, s80
Maaa 96,0,60
s_add_u32 s83, s83, 8192
Ra 32,26,32768
Maaa 96,4,156
Maaa 64,4,152
D 21,44,65,1024
Ta 16,12,32768
Maaa 64,0,56
W 9
Ta 18,12,33792
Ta 20,12,40960
Maaa 224,0,52
Ta 22,12,41984
Maaa 224,4,148
Maaa 192,4,144
buffer_store_dwordx4 v[40:43], v1, s[8:11], s83 offen sc1
Maaa 192,0,48
W 8
Maaa 160,0,44
Maaa 160,4,140
Mvaa 224,4,136
D 22,44,65,2048
Mvaa 224,0,40
W 4
Mvaa 192,0,36
Mvaa 192,4,132
Mvaa 160,4,128
D 23,44,65,3072
Mvaa 160,0,32
s_add_u32 m0, s61, 0
Mvaa 164,8,32
Mvaa 164,12,128
Mvaa 196,12,132
D 2,40,64,0
Ta 24,13,32768
Mvaa 196,8,36
Ta 26,13,33792
Ta 28,13,40960
Mvaa 228,8,40
Ta 30,13,41984
Mvaa 228,12,136
.Lh_0_1%=:
s_cbranch_execz .Lh_0_1_1%=
Maaa 164,12,140
D 3,40,64,1024
Maaa 164,8,44
Maaa 196,8,48
Maaa 196,12,144
Maaa 228,12,148
D 4,40,64,2048
Maaa 228,8,52
Maaa 68,8,56
Maaa 68,12,152
Maaa 100,12,156
D 5,40,64,3072
Maaa 100,8,60
W 4
s_add_u32 m0, s61, 4096
Maaa 104,16,60
Maaa 104,20,156
Maaa 72,20,152
D 6,40,64,0
Ta 0,14,32768
Maaa 72,16,56
Ta 2,14,33792
Ta 4,14,40960
Maaa 232,16,52
Ta 6,14,41984
Maaa 232,20,148
Maaa 200,20,144
D 7,40,64,1024
Maaa 200,16,48
Maaa 168,16,44
Maaa 168,20,140
Mvaa 232,20,136
D 8,40,64,2048
Mvaa 232,16,40
Mvaa 200,16,36
Mvaa 200,20,132
Mvaa 168,20,128
D 9,40,64,3072
v_bfe_u32 v147, v1, 5, 3
Mvaa 168,16,32
W 4
v_lshlrev_b32 v147, 13, v147
v_and_b32 v148, 16, v1
Mvaa 172,24,32
v_lshl_add_u32 v147, v148, 3, v147
Mvaa 172,28,128
s_lshl_b32 s92, s57, 2
s_add_u32 s92, s92, s14
s_lshl_b32 s92, s92, 16
Mvaa 204,28,132
buffer_load_dword v142, v147, s[48:51], s92 offen
Ta 8,15,32768
Mvaa 204,24,36
Ta 10,15,33792
Ta 12,15,40960
Mvaa 236,24,40
Ta 14,15,41984
Mvaa 236,28,136
Maaa 172,28,140
Maaa 172,24,44
Maaa 204,24,48
Maaa 204,28,144
Maaa 236,28,148
Maaa 236,24,52
Maaa 76,24,56
Maaa 76,28,152
Maaa 108,28,156
Maaa 108,24,60
W 4
Maaa 112,0,60
Maaa 112,4,156
Maaa 80,4,152
Ta 16,24,32768
Ta 18,24,33792
Maaa 80,0,56
Ta 20,24,40960
Ta 22,24,41984
Maaa 240,0,52
Maaa 240,4,148
Maaa 208,4,144
Maaa 208,0,48
Maaa 176,0,44
Maaa 176,4,140
Mvaa 240,4,136
Mvaa 240,0,40
Mvaa 208,0,36
Mvaa 208,4,132
Mvaa 176,4,128
Mvaa 176,0,32
W 4
Mvaa 180,8,32
Ta 24,25,32768
Ta 26,25,33792
Mvaa 180,12,128
Mvaa 212,12,132
Ta 28,25,40960
Ta 30,25,41984
Mvaa 212,8,36
Mvaa 244,8,40
Mvaa 244,12,136
Maaa 180,12,140
Maaa 180,8,44
Maaa 212,8,48
Maaa 212,12,144
Maaa 244,12,148
s_waitcnt vmcnt(1) lgkmcnt(0)
s_barrier
s_cmp_eq_u32 s14, 0
s_cbranch_scc0 .Lpubr_10%=
s_lshl_b32 s80, s15, 6
s_add_u32 s80, s80, 40960
s_mov_b64 exec, 0xff
buffer_store_dwordx2 v[84:85], v83, s[8:11], s80 offen sc1
s_mov_b64 exec, -1
.Lpubr_10%=:
Maaa 244,8,52
s_add_u32 s64, s64, 128
Maaa 84,8,56
Maaa 84,12,152
s_and_b32 s64, s64, 1023
Maaa 116,12,156
Maaa 116,8,60
Maaa 120,16,60
s_lshl_b32 s65, s64, 12
Maaa 120,20,156
Maaa 88,20,152
s_add_u32 m0, s62, 32768
Ta 0,10,0
Ta 2,10,1024
Maaa 88,16,56
Ta 4,10,8192
Ta 6,10,9216
Maaa 248,16,52
Maaa 248,20,148
D 16,44,65,0
Maaa 216,20,144
Maaa 216,16,48
Maaa 184,16,44
Maaa 184,20,140
D 17,44,65,1024
Mvaa 248,20,136
Mvaa 248,16,40
Mvaa 216,16,36
Mvaa 216,20,132
D 18,44,65,2048
Mvaa 184,20,128
Mvaa 184,16,32
Mvaa 188,24,32
Ra 32,26,0
Mvaa 188,28,128
D 19,44,65,3072
Ra 128,27,0
Mvaa 220,28,132
s_add_u32 m0, s62, 36864
Ra 132,27,2048
Ta 8,11,0
Mvaa 220,24,36
Ta 10,11,1024
Ra 36,26,2048
Mvaa 252,24,40
Ta 12,11,8192
Ta 14,11,9216
Mvaa 252,28,136
D 20,44,65,0
Ra 40,26,4096
Maaa 188,28,140
Ra 136,27,4096
Ra 140,27,6144
Maaa 188,24,44
Ra 44,26,6144
Maaa 220,24,48
Ra 48,26,8192
Maaa 220,28,144
D 21,44,65,1024
Ra 144,27,8192
Maaa 252,28,148
Ra 148,27,10240
Maaa 252,24,52
Ra 52,26,10240
Maaa 92,24,56
Ra 56,26,12288
Maaa 92,28,152
D 22,44,65,2048
Ra 152,27,12288
Maaa 124,28,156
Ra 156,27,14336
Maaa 124,24,60
W 13
Ra 60,26,14336
Mvaa 160,0,32
Mvaa 160,4,128
D 23,44,65,3072
Mvaa 192,4,132
s_add_u32 m0, s61, 32768
Ta 16,12,0
Ta 18,12,1024
Mvaa 192,0,36
W 10
Ta 20,12,8192
Ta 22,12,9216
Mvaa 224,0,40
Mvaa 224,4,136
D 2,40,64,0
Maaa 160,4,140
)"
R"(Maaa 160,0,44
W 8
Maaa 192,0,48
Maaa 192,4,144
D 3,40,64,1024
Maaa 224,4,148
Maaa 224,0,52
W 4
Maaa 64,0,56
Maaa 64,4,152
D 4,40,64,2048
Maaa 96,4,156
Maaa 96,0,60
Maaa 100,8,60
Maaa 100,12,156
D 5,40,64,3072
Maaa 68,12,152
s_add_u32 m0, s61, 36864
Ta 24,13,0
Ta 26,13,1024
Maaa 68,8,56
Ta 28,13,8192
Ta 30,13,9216
Maaa 228,8,52
Maaa 228,12,148
D 6,40,64,0
Maaa 196,12,144
Maaa 196,8,48
Maaa 164,8,44
Maaa 164,12,140
D 7,40,64,1024
Mvaa 228,12,136
Mvaa 228,8,40
Mvaa 196,8,36
Mvaa 196,12,132
D 8,40,64,2048
Mvaa 164,12,128
Mvaa 164,8,32
W 4
Mvaa 168,16,32
Mvaa 168,20,128
D 9,40,64,3072
Mvaa 200,20,132
Ta 0,14,0
Ta 2,14,1024
Mvaa 200,16,36
Ta 4,14,8192
Ta 6,14,9216
Mvaa 232,16,40
Mvaa 232,20,136
global_load_dword v142, v[140:141], off
v_lshl_add_u64 v[140:141], v[140:141], 0, s[68:69]
Maaa 168,20,140
Maaa 168,16,44
Maaa 200,16,48
Maaa 200,20,144
Maaa 232,20,148
Maaa 232,16,52
Maaa 72,16,56
Maaa 72,20,152
Maaa 104,20,156
Maaa 104,16,60
W 4
Maaa 108,24,60
Maaa 108,28,156
Maaa 76,28,152
Ta 8,15,0
Ta 10,15,1024
Maaa 76,24,56
Ta 12,15,8192
Ta 14,15,9216
Maaa 236,24,52
Maaa 236,28,148
Maaa 204,28,144
Maaa 204,24,48
Maaa 172,24,44
Maaa 172,28,140
Mvaa 236,28,136
Mvaa 236,24,40
Mvaa 204,24,36
Mvaa 204,28,132
Mvaa 172,28,128
Mvaa 172,24,32
W 4
Mvaa 176,0,32
Mvaa 176,4,128
Mvaa 208,4,132
Ta 16,24,0
Ta 18,24,1024
Mvaa 208,0,36
Ta 20,24,8192
Ta 22,24,9216
Mvaa 240,0,40
Mvaa 240,4,136
Maaa 176,4,140
Maaa 176,0,44
Maaa 208,0,48
Maaa 208,4,144
Maaa 240,4,148
Maaa 240,0,52
Maaa 80,0,56
Maaa 80,4,152
Maaa 112,4,156
Maaa 112,0,60
W 4
Maaa 116,8,60
Ta 24,25,0
Ta 26,25,1024
Maaa 116,12,156
Maaa 84,12,152
Ta 28,25,8192
Ta 30,25,9216
Maaa 84,8,56
Maaa 244,8,52
Maaa 244,12,148
Maaa 212,12,144
Maaa 212,8,48
Maaa 180,8,44
Maaa 180,12,140
Mvaa 244,12,136
s_waitcnt vmcnt(1) lgkmcnt(0)
s_barrier
Mvaa 244,8,40
s_add_u32 s64, s64, 128
Mvaa 212,8,36
Mvaa 212,12,132
s_and_b32 s64, s64, 1023
Mvaa 180,12,128
Mvaa 180,8,32
Mvaa 184,16,32
s_lshl_b32 s65, s64, 12
Mvaa 184,20,128
Mvaa 216,20,132
s_add_u32 m0, s62, 0
Ta 0,10,32768
Ta 2,10,33792
Mvaa 216,16,36
Ta 4,10,40960
Ta 6,10,41984
Mvaa 248,16,40
Mvaa 248,20,136
D 16,44,65,0
Maaa 184,20,140
Maaa 184,16,44
Maaa 216,16,48
Maaa 216,20,144
D 17,44,65,1024
Maaa 248,20,148
Maaa 248,16,52
Maaa 88,16,56
Maaa 88,20,152
D 18,44,65,2048
Maaa 120,20,156
Maaa 120,16,60
Maaa 124,24,60
Ra 60,26,47104
Maaa 124,28,156
D 19,44,65,3072
Ra 156,27,47104
Maaa 92,28,152
s_add_u32 m0, s62, 4096
Ra 152,27,45056
Ta 8,11,32768
Maaa 92,24,56
Ta 10,11,33792
Ra 56,26,45056
Maaa 252,24,52
Ta 12,11,40960
Ta 14,11,41984
Maaa 252,28,148
D 20,44,65,0
Ra 52,26,43008
Maaa 220,28,144
Ra 148,27,43008
Ra 144,27,40960
Maaa 220,24,48
Ra 48,26,40960
Maaa 188,24,44
Ra 44,26,38912
Maaa 188,28,140
D 21,44,65,1024
Ra 140,27,38912
Mvaa 252,28,136
Ra 136,27,36864
Mvaa 252,24,40
Ra 40,26,36864
Mvaa 220,24,36
Ra 36,26,34816
Mvaa 220,28,132
D 22,44,65,2048
Ra 132,27,34816
Mvaa 188,28,128
Ra 128,27,32768
Mvaa 188,24,32
W 13
Ra 32,26,32768
Maaa 96,0,60
Maaa 96,4,156
D 23,44,65,3072
Maaa 64,4,152
s_add_u32 m0, s61, 0
Ta 16,12,32768
Ta 18,12,33792
Maaa 64,0,56
W 10
Ta 20,12,40960
Ta 22,12,41984
Maaa 224,0,52
Maaa 224,4,148
D 2,40,64,0
Maaa 192,4,144
Maaa 192,0,48
W 8
Maaa 160,0,44
Maaa 160,4,140
D 3,40,64,1024
Mvaa 224,4,136
Mvaa 224,0,40
W 4
Mvaa 192,0,36
Mvaa 192,4,132
D 4,40,64,2048
Mvaa 160,4,128
Mvaa 160,0,32
Mvaa 164,8,32
Mvaa 164,12,128
D 5,40,64,3072
Mvaa 196,12,132
s_add_u32 m0, s61, 4096
Ta 24,13,32768
Ta 26,13,33792
Mvaa 196,8,36
Ta 28,13,40960
Ta 30,13,41984
Mvaa 228,8,40
Mvaa 228,12,136
D 6,40,64,0
Maaa 164,12,140
Maaa 164,8,44
Maaa 196,8,48
Maaa 196,12,144
D 7,40,64,1024
Maaa 228,12,148
Maaa 228,8,52
Maaa 68,8,56
Maaa 68,12,152
D 8,40,64,2048
Maaa 100,12,156
Maaa 100,8,60
W 4
Maaa 104,16,60
Maaa 104,20,156
D 9,40,64,3072
Maaa 72,20,152
Ta 0,14,32768
Ta 2,14,33792
Maaa 72,16,56
Ta 4,14,40960
Ta 6,14,41984
Maaa 232,16,52
Maaa 232,20,148
global_load_dword v142, v[140:141], off
v_lshl_add_u64 v[140:141], v[140:141], 0, s[68:69]
Maaa 200,20,144
Maaa 200,16,48
Maaa 168,16,44
Maaa 168,20,140
Mvaa 232,20,136
Mvaa 232,16,40
Mvaa 200,16,36
Mvaa 200,20,132
Mvaa 168,20,128
Mvaa 168,16,32
W 4
Mvaa 172,24,32
Mvaa 172,28,128
Mvaa 204,28,132
Ta 8,15,32768
Ta 10,15,33792
Mvaa 204,24,36
Ta 12,15,40960
Ta 14,15,41984
Mvaa 236,24,40
Mvaa 236,28,136
Maaa 172,28,140
Maaa 172,24,44
Maaa 204,24,48
Maaa 204,28,144
Maaa 236,28,148
Maaa 236,24,52
Maaa 76,24,56
Maaa 76,28,152
Maaa 108,28,156
Maaa 108,24,60
W 4
Maaa 112,0,60
Maaa 112,4,156
Maaa 80,4,152
Ta 16,24,32768
Ta 18,24,33792
Maaa 80,0,56
Ta 20,24,40960
Ta 22,24,41984
Maaa 240,0,52
Maaa 240,4,148
Maaa 208,4,144
Maaa 208,0,48
Maaa 176,0,44
Maaa 176,4,140
Mvaa 240,4,136
Mvaa 240,0,40
Mvaa 208,0,36
Mvaa 208,4,132
Mvaa 176,4,128
Mvaa 176,0,32
W 4
Mvaa 180,8,32
Ta 24,25,32768
Ta 26,25,33792
Mvaa 180,12,128
Mvaa 212,12,132
Ta 28,25,40960
Ta 30,25,41984
Mvaa 212,8,36
Mvaa 244,8,40
Mvaa 244,12,136
.Lh_0_2%=:
s_cbranch_execz .Lh_0_2_1%=
Maaa 180,12,140
Maaa 180,8,44
Maaa 212,8,48
Maaa 212,12,144
Maaa 244,12,148
s_waitcnt vmcnt(1) lgkmcnt(0)
s_barrier
Maaa 244,8,52
s_add_u32 s64, s64, 128
Maaa 84,8,56
Maaa 84,12,152
s_and_b32 s64, s64, 1023
Maaa 116,12,156
Maaa 116,8,60
Maaa 120,16,60
s_lshl_b32 s65, s64, 12
Maaa 120,20,156
Maaa 88,20,152
Ta 0,10,0
Ta 2,10,1024
Maaa 88,16,56
s_add_u32 m0, s62, 32768
Ta 4,10,8192
Ta 6,10,9216
Maaa 248,16,52
Maaa 248,20,148
Maaa 216,20,144
D 16,44,65,0
Maaa 216,16,48
Maaa 184,16,44
Maaa 184,20,140
Mvaa 248,20,136
D 17,44,65,1024
Mvaa 248,16,40
Mvaa 216,16,36
Mvaa 216,20,132
Mvaa 184,20,128
D 18,44,65,2048
Mvaa 184,16,32
Mvaa 188,24,32
Ra 32,26,0
Mvaa 188,28,128
Ra 128,27,0
Mvaa 220,28,132
D 19,44,65,3072
Ra 132,27,2048
Mvaa 220,24,36
s_add_u32 m0, s62, 36864
Ta 8,11,0
Ta 10,11,1024
Mvaa 252,24,40
Ra 36,26,2048
Ta 12,11,8192
Mvaa 252,28,136
Ta 14,11,9216
Ra 40,26,4096
Maaa 188,28,140
D 20,44,65,0
Ra 136,27,4096
Maaa 188,24,44
Ra 140,27,6144
Ra 44,26,6144
Maaa 220,24,48
Ra 48,26,8192
Maaa 220,28,144
Ra 144,27,8192
Maaa 252,28,148
D 21,44,65,1024
Ra 148,27,10240
Maaa 252,24,52
Ra 52,26,10240
Maaa 92,24,56
)"
R"(Ra 56,26,12288
Maaa 92,28,152
Ra 152,27,12288
Maaa 124,28,156
D 22,44,65,2048
Ra 156,27,14336
Maaa 124,24,60
W 13
Ra 60,26,14336
Mvaa 160,0,32
Mvaa 160,4,128
Mvaa 192,4,132
D 23,44,65,3072
Ta 16,12,0
Mvaa 192,0,36
W 9
s_add_u32 m0, s61, 32768
Ta 18,12,1024
Mvaa 224,0,40
Ta 20,12,8192
Ta 22,12,9216
Mvaa 224,4,136
Maaa 160,4,140
D 2,40,64,0
Maaa 160,0,44
W 8
Maaa 192,0,48
Maaa 192,4,144
Maaa 224,4,148
D 3,40,64,1024
Maaa 224,0,52
W 4
Maaa 64,0,56
Maaa 64,4,152
Maaa 96,4,156
D 4,40,64,2048
Maaa 96,0,60
Maaa 100,8,60
Maaa 100,12,156
Maaa 68,12,152
D 5,40,64,3072
Ta 24,13,0
Maaa 68,8,56
s_add_u32 m0, s61, 36864
Ta 26,13,1024
Ta 28,13,8192
Maaa 228,8,52
Ta 30,13,9216
Maaa 228,12,148
Maaa 196,12,144
D 6,40,64,0
Maaa 196,8,48
Maaa 164,8,44
Maaa 164,12,140
Mvaa 228,12,136
D 7,40,64,1024
Mvaa 228,8,40
Mvaa 196,8,36
Mvaa 196,12,132
Mvaa 164,12,128
D 8,40,64,2048
Mvaa 164,8,32
W 4
Mvaa 168,16,32
Mvaa 168,20,128
Mvaa 200,20,132
D 9,40,64,3072
Ta 0,14,0
Mvaa 200,16,36
s_add_u32 s92, s92, 2097152
Ta 2,14,1024
Ta 4,14,8192
Mvaa 232,16,40
Ta 6,14,9216
Mvaa 232,20,136
Maaa 168,20,140
buffer_load_dword v142, v147, s[48:51], s92 offen
Maaa 168,16,44
s_add_u32 s92, s92, 2097152
Maaa 200,16,48
Maaa 200,20,144
Maaa 232,20,148
buffer_load_dword v142, v147, s[48:51], s92 offen
Maaa 232,16,52
Maaa 72,16,56
Maaa 72,20,152
Maaa 104,20,156
Maaa 104,16,60
W 4
Maaa 108,24,60
Maaa 108,28,156
Maaa 76,28,152
Ta 8,15,0
Ta 10,15,1024
Maaa 76,24,56
Ta 12,15,8192
Ta 14,15,9216
Maaa 236,24,52
Maaa 236,28,148
Maaa 204,28,144
Maaa 204,24,48
Maaa 172,24,44
Maaa 172,28,140
Mvaa 236,28,136
Mvaa 236,24,40
Mvaa 204,24,36
Mvaa 204,28,132
Mvaa 172,28,128
Mvaa 172,24,32
W 4
Mvaa 176,0,32
Mvaa 176,4,128
Mvaa 208,4,132
Ta 16,24,0
Ta 18,24,1024
Mvaa 208,0,36
Ta 20,24,8192
Ta 22,24,9216
Mvaa 240,0,40
Mvaa 240,4,136
Maaa 176,4,140
Maaa 176,0,44
Maaa 208,0,48
Maaa 208,4,144
Maaa 240,4,148
Maaa 240,0,52
Maaa 80,0,56
Maaa 80,4,152
Maaa 112,4,156
Maaa 112,0,60
W 4
Maaa 116,8,60
Ta 24,25,0
Ta 26,25,1024
Maaa 116,12,156
Maaa 84,12,152
Ta 28,25,8192
Ta 30,25,9216
Maaa 84,8,56
Maaa 244,8,52
Maaa 244,12,148
Maaa 212,12,144
Maaa 212,8,48
Maaa 180,8,44
Maaa 180,12,140
Mvaa 244,12,136
s_waitcnt vmcnt(2) lgkmcnt(0)
s_barrier
Mvaa 244,8,40
s_add_u32 s64, s64, 128
Mvaa 212,8,36
Mvaa 212,12,132
s_and_b32 s64, s64, 1023
Mvaa 180,12,128
Mvaa 180,8,32
Mvaa 184,16,32
s_lshl_b32 s65, s64, 12
Mvaa 184,20,128
Mvaa 216,20,132
Ta 0,10,32768
Ta 2,10,33792
Mvaa 216,16,36
s_add_u32 m0, s62, 0
Ta 4,10,40960
Ta 6,10,41984
Mvaa 248,16,40
Mvaa 248,20,136
Maaa 184,20,140
D 16,44,65,0
Maaa 184,16,44
Maaa 216,16,48
Maaa 216,20,144
Maaa 248,20,148
D 17,44,65,1024
Maaa 248,16,52
Maaa 88,16,56
Maaa 88,20,152
Maaa 120,20,156
D 18,44,65,2048
Maaa 120,16,60
Maaa 124,24,60
Ra 60,26,47104
Maaa 124,28,156
Ra 156,27,47104
Maaa 92,28,152
D 19,44,65,3072
Ra 152,27,45056
Maaa 92,24,56
s_add_u32 m0, s62, 4096
Ta 8,11,32768
Ta 10,11,33792
Maaa 252,24,52
Ra 56,26,45056
Ta 12,11,40960
Maaa 252,28,148
Ta 14,11,41984
Ra 52,26,43008
Maaa 220,28,144
D 20,44,65,0
v_lshrrev_b32 v28, 1, v1
Maaa 220,24,48
s_lshl_b32 s80, s60, 9
Ra 148,27,43008
Ra 144,27,40960
Maaa 188,24,44
s_add_u32 s80, s80, 40960
Ra 48,26,40960
Ra 44,26,38912
Maaa 188,28,140
Ra 140,27,38912
Mvaa 252,28,136
D 21,44,65,1024
Ra 136,27,36864
Mvaa 252,24,40
Ra 40,26,36864
Mvaa 220,24,36
Ra 36,26,34816
Mvaa 220,28,132
Ra 132,27,34816
Mvaa 188,28,128
buffer_load_dwordx2 v[28:29], v28, s[8:11], s80 offen sc1
Ra 128,27,32768
Mvaa 188,24,32
W 13
Ra 32,26,32768
Maaa 96,0,60
Maaa 96,4,156
Maaa 64,4,152
D 22,44,65,2048
Ta 16,12,32768
Maaa 64,0,56
W 9
Ta 18,12,33792
Ta 20,12,40960
Maaa 224,0,52
Ta 22,12,41984
Maaa 224,4,148
Maaa 192,4,144
D 23,44,65,3072
Maaa 192,0,48
W 8
s_add_u32 m0, s61, 0
Maaa 160,0,44
Maaa 160,4,140
Mvaa 224,4,136
D 2,40,64,0
Mvaa 224,0,40
W 4
Mvaa 192,0,36
Mvaa 192,4,132
Mvaa 160,4,128
D 3,40,64,1024
Mvaa 160,0,32
Mvaa 164,8,32
Mvaa 164,12,128
Mvaa 196,12,132
D 4,40,64,2048
Ta 24,13,32768
Mvaa 196,8,36
Ta 26,13,33792
Ta 28,13,40960
Mvaa 228,8,40
Ta 30,13,41984
Mvaa 228,12,136
Maaa 164,12,140
D 5,40,64,3072
Maaa 164,8,44
s_add_u32 m0, s61, 4096
Maaa 196,8,48
Maaa 196,12,144
Maaa 228,12,148
D 6,40,64,0
Maaa 228,8,52
Maaa 68,8,56
Maaa 68,12,152
Maaa 100,12,156
D 7,40,64,1024
Maaa 100,8,60
W 4
Maaa 104,16,60
Maaa 104,20,156
Maaa 72,20,152
D 8,40,64,2048
Ta 0,14,32768
Maaa 72,16,56
Ta 2,14,33792
Ta 4,14,40960
Maaa 232,16,52
Ta 6,14,41984
Maaa 232,20,148
Maaa 200,20,144
D 9,40,64,3072
Maaa 200,16,48
Maaa 168,16,44
Maaa 168,20,140
Mvaa 232,20,136
s_waitcnt vmcnt(10)
v_cmp_eq_u32 vcc, s13, v29
s_cmp_eq_u64 vcc, -1
s_cbranch_scc0 .Lspin_11%=
.Lok_12%=:
global_load_dword v142, v146, s[6:7]
Mvaa 232,16,40
Mvaa 200,16,36
v_bfe_u32 v144, v1, 8, 1
Mvaa 200,20,132
v_mul_u32_u24 v144, 10240, v144
Mvaa 168,20,128
v_and_b32 v145, 0xf0, v1
Mvaa 168,16,32
W 4
v_lshl_add_u32 v144, v145, 3, v144
Mvaa 172,24,32
s_mul_i32 s91, s58, 20480
Mvaa 172,28,128
s_add_u32 s91, s91, s28
Mvaa 204,28,132
buffer_load_dword v142, v144, s[8:11], s91 offen offset:0 sc1
Ta 8,15,32768
Mvaa 204,24,36
Ta 10,15,33792
Ta 12,15,40960
Mvaa 236,24,40
Ta 14,15,41984
Mvaa 236,28,136
Maaa 172,28,140
Maaa 172,24,44
Maaa 204,24,48
Maaa 204,28,144
Maaa 236,28,148
Maaa 236,24,52
Maaa 76,24,56
Maaa 76,28,152
Maaa 108,28,156
Maaa 108,24,60
W 4
Maaa 112,0,60
Maaa 112,4,156
Maaa 80,4,152
Ta 16,24,32768
Ta 18,24,33792
Maaa 80,0,56
Ta 20,24,40960
Ta 22,24,41984
Maaa 240,0,52
Maaa 240,4,148
Maaa 208,4,144
Maaa 208,0,48
Maaa 176,0,44
Maaa 176,4,140
Mvaa 240,4,136
Mvaa 240,0,40
Mvaa 208,0,36
Mvaa 208,4,132
Mvaa 176,4,128
Mvaa 176,0,32
W 4
Mvaa 180,8,32
Ta 24,25,32768
Ta 26,25,33792
Mvaa 180,12,128
Mvaa 212,12,132
Ta 28,25,40960
Ta 30,25,41984
Mvaa 212,8,36
Mvaa 244,8,40
Mvaa 244,12,136
Maaa 180,12,140
Maaa 180,8,44
Maaa 212,8,48
Maaa 212,12,144
Maaa 244,12,148
s_waitcnt vmcnt(2) lgkmcnt(0)
s_barrier
Maaa 244,8,52
s_add_u32 s64, s64, 128
Maaa 84,8,56
Maaa 84,12,152
s_and_b32 s64, s64, 1023
Maaa 116,12,156
Maaa 116,8,60
Maaa 120,16,60
s_lshl_b32 s65, s64, 12
Maaa 120,20,156
Maaa 88,20,152
Ta 0,10,0
Ta 2,10,1024
Maaa 88,16,56
s_add_u32 m0, s62, 32768
Ta 4,10,8192
Ta 6,10,9216
Maaa 248,16,52
Maaa 248,20,148
Maaa 216,20,144
D 16,44,65,0
Maaa 216,16,48
Maaa 184,16,44
Maaa 184,20,140
Mvaa 248,20,136
D 17,44,65,1024
)"
R"(Mvaa 248,16,40
Mvaa 216,16,36
Mvaa 216,20,132
Mvaa 184,20,128
D 18,44,65,2048
Mvaa 184,16,32
Mvaa 188,24,32
Ra 32,26,0
Mvaa 188,28,128
Ra 128,27,0
Mvaa 220,28,132
D 19,44,65,3072
Ra 132,27,2048
Mvaa 220,24,36
s_add_u32 m0, s62, 36864
Ta 8,11,0
Ta 10,11,1024
Mvaa 252,24,40
Ra 36,26,2048
Ta 12,11,8192
Mvaa 252,28,136
Ta 14,11,9216
Ra 40,26,4096
Maaa 188,28,140
D 20,44,65,0
Ra 136,27,4096
Maaa 188,24,44
Ra 140,27,6144
Ra 44,26,6144
Maaa 220,24,48
Ra 48,26,8192
Maaa 220,28,144
Ra 144,27,8192
Maaa 252,28,148
D 21,44,65,1024
Ra 148,27,10240
Maaa 252,24,52
Ra 52,26,10240
Maaa 92,24,56
Ra 56,26,12288
Maaa 92,28,152
Ra 152,27,12288
Maaa 124,28,156
D 22,44,65,2048
Ra 156,27,14336
Maaa 124,24,60
W 13
Ra 60,26,14336
Mvaa 160,0,32
Mvaa 160,4,128
Mvaa 192,4,132
D 23,44,65,3072
Ta 16,12,0
Mvaa 192,0,36
W 9
s_add_u32 m0, s61, 32768
Ta 18,12,1024
Mvaa 224,0,40
Ta 20,12,8192
Ta 22,12,9216
Mvaa 224,4,136
.Lh_0_3%=:
s_cbranch_execz .Lh_0_3_1%=
Maaa 160,4,140
D 2,40,64,0
Maaa 160,0,44
W 8
Maaa 192,0,48
Maaa 192,4,144
Maaa 224,4,148
D 3,40,64,1024
Maaa 224,0,52
W 4
Maaa 64,0,56
Maaa 64,4,152
Maaa 96,4,156
D 4,40,64,2048
Maaa 96,0,60
Maaa 100,8,60
Maaa 100,12,156
Maaa 68,12,152
D 5,40,64,3072
Ta 24,13,0
Maaa 68,8,56
s_add_u32 m0, s61, 36864
Ta 26,13,1024
Ta 28,13,8192
Maaa 228,8,52
Ta 30,13,9216
Maaa 228,12,148
Maaa 196,12,144
D 6,40,64,0
Maaa 196,8,48
Maaa 164,8,44
Maaa 164,12,140
Mvaa 228,12,136
D 7,40,64,1024
Mvaa 228,8,40
Mvaa 196,8,36
Mvaa 196,12,132
Mvaa 164,12,128
D 8,40,64,2048
Mvaa 164,8,32
W 4
Mvaa 168,16,32
Mvaa 168,20,128
Mvaa 200,20,132
D 9,40,64,3072
v_add_u32 v2, 131072, v10
Mvaa 200,16,36
Ta 0,14,0
v_add_u32 v3, 131072, v11
Mvaa 232,16,40
Ta 2,14,1024
v_add_u32 v4, 131072, v12
Mvaa 232,20,136
Ta 4,14,8192
Ta 6,14,9216
Maaa 168,20,140
buffer_load_dword v142, v144, s[8:11], s91 offen offset:2048 sc1
v_add_u32 v5, 131072, v13
Maaa 168,16,44
s_add_u32 s91, s91, 4096
v_add_u32 v6, 131072, v14
Maaa 200,16,48
v_add_u32 v7, 131072, v15
Maaa 200,20,144
Maaa 232,20,148
v_add_u32 v8, 131072, v24
Maaa 232,16,52
Maaa 72,16,56
v_add_u32 v9, 131072, v25
Maaa 72,20,152
s_add_u32 s65, s5, 0
s_cmp_ge_u32 s65, 20
s_cselect_b32 s90, 20, 0
s_sub_u32 s65, s65, s90
Maaa 104,20,156
s_lshl_b32 s65, s65, 18
s_add_u32 m0, s63, 131072
Maaa 104,16,60
W 4
Maaa 108,24,60
Maaa 108,28,156
Maaa 76,28,152
Ta 8,15,0
Ta 10,15,1024
Maaa 76,24,56
D 16,48,65,0
Ta 12,15,8192
Maaa 236,24,52
Ta 14,15,9216
Maaa 236,28,148
Maaa 204,28,144
Maaa 204,24,48
D 17,48,65,1024
Maaa 172,24,44
Maaa 172,28,140
Mvaa 236,28,136
Mvaa 236,24,40
Mvaa 204,24,36
D 18,48,65,2048
Mvaa 204,28,132
Mvaa 172,28,128
Mvaa 172,24,32
W 4
Mvaa 176,0,32
Mvaa 176,4,128
D 19,48,65,3072
Mvaa 208,4,132
s_add_u32 m0, s63, 135168
Ta 16,24,0
Ta 18,24,1024
Mvaa 208,0,36
Ta 20,24,8192
Ta 22,24,9216
Mvaa 240,0,40
Mvaa 240,4,136
Maaa 176,4,140
D 20,48,65,0
Maaa 176,0,44
Maaa 208,0,48
Maaa 208,4,144
Maaa 240,4,148
Maaa 240,0,52
D 21,48,65,1024
Maaa 80,0,56
Maaa 80,4,152
Maaa 112,4,156
Maaa 112,0,60
W 4
Maaa 116,8,60
D 22,48,65,2048
Ta 24,25,0
Maaa 116,12,156
Ta 26,25,1024
Maaa 84,12,152
Ta 28,25,8192
Ta 30,25,9216
Maaa 84,8,56
Maaa 244,8,52
D 23,48,65,3072
Maaa 244,12,148
Maaa 212,12,144
Maaa 212,8,48
Maaa 180,8,44
Maaa 180,12,140
Mvaa 244,12,136
s_waitcnt vmcnt(9) lgkmcnt(0)
s_barrier
Mvaa 244,8,40
s_add_u32 s65, s5, 4
Mvaa 212,8,36
Mvaa 212,12,132
s_cmp_ge_u32 s65, 20
s_cselect_b32 s90, 20, 0
Mvaa 180,12,128
Mvaa 180,8,32
s_sub_u32 s65, s65, s90
Mvaa 184,16,32
Mvaa 184,20,128
s_lshl_b32 s65, s65, 18
Mvaa 216,20,132
Ta 0,10,32768
Ta 2,10,33792
Mvaa 216,16,36
Mvaa 248,16,40
s_add_u32 m0, s63, 0
Mvaa 248,20,136
Maaa 184,20,140
D 16,48,65,0
Maaa 184,16,44
Maaa 216,16,48
Maaa 216,20,144
Maaa 248,20,148
D 17,48,65,1024
Ta 4,11,32768
Maaa 248,16,52
Ta 6,11,33792
Maaa 88,16,56
Maaa 88,20,152
Maaa 120,20,156
D 18,48,65,2048
Maaa 120,16,60
Maaa 124,24,60
Ra 60,26,47104
Maaa 124,28,156
Maaa 92,28,152
D 19,48,65,3072
Ta 8,12,32768
Maaa 92,24,56
s_add_u32 m0, s63, 4096
Ta 10,12,33792
Ra 56,26,45056
Maaa 252,24,52
Ra 52,26,43008
Maaa 252,28,148
Maaa 220,28,144
D 20,48,65,0
Maaa 220,24,48
Ra 48,26,40960
Maaa 188,24,44
Ra 44,26,38912
Maaa 188,28,140
Mvaa 252,28,136
D 21,48,65,1024
Ta 12,13,32768
Mvaa 252,24,40
Ta 14,13,33792
Ra 40,26,36864
Mvaa 220,24,36
Ra 36,26,34816
Mvaa 220,28,132
Mvaa 188,28,128
D 22,48,65,2048
Mvaa 188,24,32
Ra 32,26,32768
W 6
Maaa 96,0,60
Maaa 64,0,56
Maaa 224,0,52
D 23,48,65,3072
Ta 16,14,32768
Maaa 192,0,48
W 1
s_add_u32 s65, s5, 8
s_cmp_ge_u32 s65, 20
s_cselect_b32 s90, 20, 0
Maaa 160,0,44
s_sub_u32 s65, s65, s90
s_lshl_b32 s65, s65, 18
Ta 18,14,33792
Mvaa 224,0,40
s_add_u32 m0, s63, 65536
Mvaa 192,0,36
D 16,48,65,0
Mvaa 160,0,32
Mvaa 164,4,32
Mvaa 196,4,36
Mvaa 228,4,40
D 17,48,65,1024
Ta 20,15,32768
Maaa 164,4,44
Ta 22,15,33792
Maaa 196,4,48
Maaa 228,4,52
Maaa 68,4,56
D 18,48,65,2048
Maaa 100,4,60
Maaa 104,8,60
Maaa 72,8,56
Maaa 232,8,52
D 19,48,65,3072
Ta 24,24,32768
Maaa 200,8,48
s_add_u32 m0, s63, 69632
Ta 26,24,33792
Maaa 168,8,44
Mvaa 232,8,40
Mvaa 200,8,36
D 20,48,65,0
Mvaa 168,8,32
Mvaa 172,12,32
Mvaa 204,12,36
Mvaa 236,12,40
D 21,48,65,1024
Ta 28,25,32768
Maaa 172,12,44
Ta 30,25,33792
Maaa 204,12,48
Ra 156,27,47104
Maaa 236,12,52
Maaa 76,12,56
D 22,48,65,2048
Maaa 108,12,60
W 7
Ra 152,27,45056
Maaa 112,16,60
Maaa 80,16,56
Maaa 240,16,52
D 23,48,65,3072
Ra 148,27,43008
Maaa 208,16,48
Ta 0,10,40960
Ta 2,10,41984
Maaa 176,16,44
Mvaa 240,16,40
Ra 144,27,40960
Mvaa 208,16,36
buffer_load_dword v142, v144, s[8:11], s91 offen offset:0 sc1
Mvaa 176,16,32
W 10
Mvaa 180,20,32
Ra 140,27,38912
Mvaa 212,20,36
Mvaa 244,20,40
Ta 4,11,40960
Ta 6,11,41984
Maaa 180,20,44
Ra 136,27,36864
Maaa 212,20,48
Maaa 244,20,52
v_bfe_u32 v29, v1, 7, 1
Maaa 84,20,56
Ra 132,27,34816
Maaa 116,20,60
W 13
v_mul_u32_u24 v28, 10240, v29
Maaa 120,24,60
Maaa 88,24,56
Ra 128,27,32768
v_lshrrev_b32 v29, 8, v1
Maaa 248,24,52
Ta 8,12,40960
Ta 10,12,41984
Maaa 216,24,48
v_lshl_add_u32 v28, v29, 7, v28
Maaa 184,24,44
v_and_b32 v29, 0x70, v1
Mvaa 248,24,40
Mvaa 216,24,36
v_add_u32 v28, v28, v29
Mvaa 184,24,32
W 14
Mvaa 188,28,32
s_and_b32 s80, s14, 2
Mvaa 220,28,36
s_mul_i32 s80, s80, 20480
Mvaa 252,28,40
Ta 12,13,40960
Ta 14,13,41984
Maaa 188,28,44
s_sub_u32 s81, 40960, s80
Maaa 220,28,48
Maaa 252,28,52
s_add_u32 s93, s28, 20480
Maaa 92,28,56
Maaa 124,28,60
W 10
v_add_u32 v26, s80, v28
)"
R"(Maaa 96,0,156
Maaa 64,0,152
Maaa 224,0,148
Ta 16,14,40960
v_add_u32 v27, s81, v28
Maaa 192,0,144
W 5
Ta 18,14,41984
Maaa 160,0,140
buffer_load_dwordx4 a[32:35], v26, s[8:11], s28 offen offset:0 sc1
Mvaa 224,0,136
Mvaa 192,0,132
Mvaa 160,0,128
Mvaa 164,4,128
buffer_load_dwordx4 a[36:39], v26, s[8:11], s93 offen offset:0 sc1
Mvaa 196,4,132
Mvaa 228,4,136
Ta 20,15,40960
Ta 22,15,41984
Maaa 164,4,140
Maaa 196,4,144
buffer_load_dwordx4 a[40:43], v27, s[8:11], s28 offen offset:0 sc1
Maaa 228,4,148
Maaa 68,4,152
Maaa 100,4,156
W 6
Maaa 104,8,156
buffer_load_dwordx4 a[44:47], v27, s[8:11], s93 offen offset:0 sc1
Ta 24,24,40960
Maaa 72,8,152
s_add_u32 s28, s28, 512
s_cmp_eq_u32 s28, s4
s_cselect_b32 s93, 10240, 0
Ta 26,24,41984
Maaa 232,8,148
s_sub_u32 s28, s28, s93
Ta 28,25,40960
Ta 30,25,41984
Maaa 200,8,144
s_add_u32 s93, s28, 20480
Maaa 168,8,140
Mvaa 232,8,136
Mvaa 200,8,132
s_add_u32 s65, s5, 12
s_cmp_ge_u32 s65, 20
s_cselect_b32 s90, 20, 0
buffer_load_dwordx4 a[48:51], v26, s[8:11], s28 offen offset:0 sc1
Mvaa 168,8,128
W 8
s_sub_u32 s65, s65, s90
s_lshl_b32 s65, s65, 18
Mvaa 172,12,128
s_add_u32 m0, s63, 32768
Mvaa 204,12,132
Mvaa 236,12,136
s_waitcnt vmcnt(6) lgkmcnt(0)
s_barrier
Maaa 172,12,140
buffer_load_dwordx4 a[52:55], v26, s[8:11], s93 offen offset:0 sc1
Maaa 204,12,144
Maaa 236,12,148
Maaa 76,12,152
Maaa 108,12,156
buffer_load_dwordx4 a[56:59], v27, s[8:11], s28 offen offset:0 sc1
Maaa 112,16,156
Maaa 80,16,152
Maaa 240,16,148
Maaa 208,16,144
buffer_load_dwordx4 a[60:63], v27, s[8:11], s93 offen offset:0 sc1
Maaa 176,16,140
Mvaa 240,16,136
Mvaa 208,16,132
Mvaa 176,16,128
D 16,48,65,0
Mvaa 180,20,128
Mvaa 212,20,132
Mvaa 244,20,136
Ta 0,2,0
Ta 2,2,1024
Maaa 180,20,140
D 17,48,65,1024
Maaa 212,20,144
Maaa 244,20,148
Maaa 84,20,152
Ta 4,2,8192
Ta 6,2,9216
Maaa 116,20,156
D 18,48,65,2048
Maaa 120,24,156
Maaa 88,24,152
Maaa 248,24,148
Ta 8,3,0
Ta 10,3,1024
Maaa 216,24,144
D 19,48,65,3072
Maaa 184,24,140
s_add_u32 m0, s63, 36864
Mvaa 248,24,136
Mvaa 216,24,132
Ta 12,3,8192
Ta 14,3,9216
Mvaa 184,24,128
D 20,48,65,0
Mvaa 188,28,128
Mvaa 220,28,132
Mvaa 252,28,136
Ta 16,4,0
Ta 18,4,1024
Maaa 188,28,140
Maaa 220,28,144
D 21,48,65,1024
Maaa 252,28,148
Maaa 92,28,152
Maaa 124,28,156
s_branch .Lq_7%=
.p2align 12
.Lv_6%=:
s_waitcnt vmcnt(14)
s_barrier
Ta 0,10,16384
Ta 2,10,17408
Ra 32,26,0
Ta 4,10,24576
.Lh_0_0_1%=:
s_cbranch_execz .Lhr_5%=
Ta 6,10,25600
Ra 128,27,0
Ra 132,27,2048
Ra 36,26,2048
Ra 40,26,4096
Ra 136,27,4096
Ra 140,27,6144
Ra 44,26,6144
Ra 48,26,8192
Ra 144,27,8192
W 6
Zvaa 160,32,0
s_add_u32 m0, s61, 32768
Ra 148,27,10240
Ra 52,26,10240
Ra 56,26,12288
Ra 152,27,12288
Ra 156,27,14336
Ra 60,26,14336
Mvaa 160,128,4
Ta 8,11,16384
Ta 10,11,17408
Ta 12,11,24576
Ta 14,11,25600
Zvaa 164,132,4
Ta 16,12,16384
Ta 18,12,17408
Mvaa 164,36,0
W 14
Ta 20,12,24576
Ta 22,12,25600
Zvaa 224,40,0
D 2,40,64,0
Mvaa 224,136,4
Zvaa 228,140,4
Mvaa 228,44,0
W 12
Zaaa 192,48,0
D 3,40,64,1024
Maaa 192,144,4
Zaaa 196,148,4
Maaa 196,52,0
W 8
Zaaa 64,56,0
D 4,40,64,2048
Maaa 64,152,4
Zaaa 68,156,4
Maaa 68,60,0
W 4
Zaaa 76,60,8
D 5,40,64,3072
Maaa 76,156,12
s_add_u32 m0, s61, 36864
Zaaa 72,152,12
Ta 24,13,16384
Ta 26,13,17408
Maaa 72,56,8
Ta 28,13,24576
Ta 30,13,25600
Zaaa 204,52,8
s_waitcnt vmcnt(15)
D 6,40,64,0
Maaa 204,148,12
v_lshlrev_b32 v56, 16, v32
v_and_b32 v57, 0xffff0000, v32
v_lshlrev_b32 v58, 16, v33
v_and_b32 v59, 0xffff0000, v33
v_lshlrev_b32 v60, 16, v34
Zaaa 200,144,12
v_and_b32 v61, 0xffff0000, v34
v_lshlrev_b32 v62, 16, v35
v_and_b32 v63, 0xffff0000, v35
v_lshlrev_b32 v64, 16, v36
v_and_b32 v65, 0xffff0000, v36
Maaa 200,48,8
v_lshlrev_b32 v66, 16, v37
v_and_b32 v67, 0xffff0000, v37
v_lshlrev_b32 v68, 16, v38
v_and_b32 v69, 0xffff0000, v38
v_lshlrev_b32 v70, 16, v39
Zvaa 236,44,8
D 7,40,64,1024
v_and_b32 v71, 0xffff0000, v39
v_lshlrev_b32 v72, 16, v40
v_and_b32 v73, 0xffff0000, v40
v_lshlrev_b32 v74, 16, v41
v_and_b32 v75, 0xffff0000, v41
Mvaa 236,140,12
v_lshlrev_b32 v76, 16, v42
v_and_b32 v77, 0xffff0000, v42
v_lshlrev_b32 v78, 16, v43
v_and_b32 v79, 0xffff0000, v43
A 80,56,57
Zvaa 232,136,12
A 81,58,59
A 80,80,60
A 81,81,61
A 80,80,62
A 81,81,63
Mvaa 232,40,8
A 80,80,64
A 81,81,65
A 80,80,66
A 81,81,67
A 80,80,68
Zvaa 172,36,8
D 8,40,64,2048
A 81,81,69
A 80,80,70
A 81,81,71
A 80,80,72
A 81,81,73
Mvaa 172,132,12
A 80,80,74
A 81,81,75
A 80,80,76
A 81,81,77
A 80,80,78
A 81,81,79
Zvaa 168,128,12
A 80,80,81
s_nop 1
v_add_f32_dpp v80, v80, v80 quad_perm:[1,0,3,2] row_mask:0xf bank_mask:0xf
s_nop 1
v_add_f32_dpp v80, v80, v80 quad_perm:[2,3,0,1] row_mask:0xf bank_mask:0xf
s_nop 1
v_add_f32_dpp v80, v80, v80 row_half_mirror row_mask:0xf bank_mask:0xf
s_nop 1
v_add_f32_dpp v80, v80, v80 row_mirror row_mask:0xf bank_mask:0xf
Mvaa 168,32,8
W 4
v_mov_b32 v81, v80
s_nop 1
v_permlane16_swap_b32 v80, v81
A 80,80,81
v_mul_f32 v84, 0x3acccccd, v80
s_mov_b32 s86, 0xffff
Zvaa 176,32,16
s_mov_b32 s87, 0xffff
D 9,40,64,3072
v_sub_f32 v82, v56, v84
U 80,82,82
v_sub_f32 v83, v57, v84
v_fmac_f32 v80, v83, v83
Mvaa 176,128,20
v_sub_f32 v82, v58, v84
v_fmac_f32 v80, v82, v82
v_sub_f32 v83, v59, v84
v_fmac_f32 v80, v83, v83
v_sub_f32 v82, v60, v84
Zvaa 180,132,20
Ta 0,14,16384
v_fmac_f32 v80, v82, v82
v_sub_f32 v83, v61, v84
v_fmac_f32 v80, v83, v83
v_sub_f32 v82, v62, v84
v_fmac_f32 v80, v82, v82
Mvaa 180,36,16
Ta 2,14,17408
v_sub_f32 v83, v63, v84
v_fmac_f32 v80, v83, v83
v_sub_f32 v82, v64, v84
v_fmac_f32 v80, v82, v82
v_sub_f32 v83, v65, v84
Zvaa 240,40,16
global_load_dword v142, v[140:141], off
v_lshl_add_u64 v[140:141], v[140:141], 0, s[68:69]
Ta 4,14,24576
Ta 6,14,25600
v_fmac_f32 v80, v83, v83
v_sub_f32 v82, v66, v84
v_fmac_f32 v80, v82, v82
Mvaa 240,136,20
v_sub_f32 v83, v67, v84
v_fmac_f32 v80, v83, v83
v_sub_f32 v82, v68, v84
v_fmac_f32 v80, v82, v82
v_sub_f32 v83, v69, v84
v_fmac_f32 v80, v83, v83
Zvaa 244,140,20
v_sub_f32 v82, v70, v84
v_fmac_f32 v80, v82, v82
v_sub_f32 v83, v71, v84
v_fmac_f32 v80, v83, v83
v_sub_f32 v82, v72, v84
U 81,82,82
Mvaa 244,44,16
v_sub_f32 v83, v73, v84
v_fmac_f32 v81, v83, v83
v_sub_f32 v82, v74, v84
v_fmac_f32 v81, v82, v82
v_sub_f32 v83, v75, v84
v_fmac_f32 v81, v83, v83
Zaaa 208,48,16
v_sub_f32 v82, v76, v84
v_fmac_f32 v81, v82, v82
v_sub_f32 v83, v77, v84
v_fmac_f32 v81, v83, v83
v_sub_f32 v82, v78, v84
Maaa 208,144,20
v_fmac_f32 v81, v82, v82
v_sub_f32 v83, v79, v84
v_fmac_f32 v81, v83, v83
v_cndmask_b32_e64 v81, 0, v81, s[86:87]
A 80,80,81
Zaaa 212,148,20
Maaa 212,52,16
v_add_f32_dpp v80, v80, v80 quad_perm:[1,0,3,2] row_mask:0xf bank_mask:0xf
s_nop 1
v_add_f32_dpp v80, v80, v80 quad_perm:[2,3,0,1] row_mask:0xf bank_mask:0xf
s_nop 1
v_add_f32_dpp v80, v80, v80 row_half_mirror row_mask:0xf bank_mask:0xf
s_nop 1
v_add_f32_dpp v80, v80, v80 row_mirror row_mask:0xf bank_mask:0xf
v_mov_b32 v81, v80
s_nop 1
v_permlane16_swap_b32 v80, v81
Zaaa 80,56,16
A 80,80,81
v_mul_f32 v80, 0x3acccccd, v80
K 80,80,80
v_lshlrev_b32 v80, 16, v80
v_add_f32 v80, 0x3727c5ac, v80
K 80,80,80
Maaa 80,152,20
v_lshlrev_b32 v80, 16, v80
v_sqrt_f32 v81, v80
s_nop 0
v_rcp_f32 v82, v81
v_fma_f32 v83, -v81, v81, v80
Zaaa 84,156,20
v_mul_f32 v82, 0.5, v82
v_fma_f32 v81, v83, v82, v81
K 86,81,81
v_lshlrev_b32 v86, 16, v86
v_rcp_f32 v87, v86
Maaa 84,60,16
W 4
K 85,84,84
v_lshlrev_b32 v85, 16, v85
s_waitcnt vmcnt(17)
v_sub_f32 v56, v56, v85
v_sub_f32 v57, v57, v85
K 80,56,57
Zaaa 92,60,24
v_lshlrev_b32 v56, 16, v80
v_and_b32 v57, 0xffff0000, v80
U 80,56,87
U 81,57,87
K 32,80,81
v_lshlrev_b32 v80, 16, v32
Maaa 92,156,28
v_and_b32 v81, 0xffff0000, v32
v_lshlrev_b32 v82, 16, v44
v_and_b32 v83, 0xffff0000, v44
U 84,80,82
v_fmac_f32 v84, v81, v83
Zaaa 88,152,28
Ta 8,15,16384
v_sub_f32 v58, v58, v85
v_sub_f32 v59, v59, v85
K 80,58,59
v_lshlrev_b32 v58, 16, v80
v_and_b32 v59, 0xffff0000, v80
Maaa 88,56,24
Ta 10,15,17408
U 80,58,87
U 81,59,87
K 33,80,81
v_lshlrev_b32 v80, 16, v33
v_and_b32 v81, 0xffff0000, v33
Zaaa 220,52,24
Ta 12,15,24576
Ta 14,15,25600
v_lshlrev_b32 v82, 16, v45
)"
R"(v_and_b32 v83, 0xffff0000, v45
v_fmac_f32 v84, v80, v82
v_fmac_f32 v84, v81, v83
Maaa 220,148,28
v_sub_f32 v60, v60, v85
v_sub_f32 v61, v61, v85
K 80,60,61
v_lshlrev_b32 v60, 16, v80
v_and_b32 v61, 0xffff0000, v80
U 80,60,87
Zaaa 216,144,28
U 81,61,87
K 34,80,81
v_lshlrev_b32 v80, 16, v34
v_and_b32 v81, 0xffff0000, v34
v_lshlrev_b32 v82, 16, v46
v_and_b32 v83, 0xffff0000, v46
Maaa 216,48,24
v_fmac_f32 v84, v80, v82
v_fmac_f32 v84, v81, v83
v_sub_f32 v62, v62, v85
v_sub_f32 v63, v63, v85
K 80,62,63
Zvaa 252,44,24
v_lshlrev_b32 v62, 16, v80
v_and_b32 v63, 0xffff0000, v80
U 80,62,87
U 81,63,87
K 35,80,81
Mvaa 252,140,28
v_lshlrev_b32 v80, 16, v35
v_and_b32 v81, 0xffff0000, v35
v_lshlrev_b32 v82, 16, v47
v_and_b32 v83, 0xffff0000, v47
v_fmac_f32 v84, v80, v82
Zvaa 248,136,28
v_fmac_f32 v84, v81, v83
v_sub_f32 v64, v64, v85
v_sub_f32 v65, v65, v85
K 80,64,65
v_lshlrev_b32 v64, 16, v80
Mvaa 248,40,24
v_and_b32 v65, 0xffff0000, v80
U 80,64,87
U 81,65,87
K 36,80,81
v_lshlrev_b32 v80, 16, v36
Zvaa 188,36,24
v_and_b32 v81, 0xffff0000, v36
v_lshlrev_b32 v82, 16, v48
v_and_b32 v83, 0xffff0000, v48
v_fmac_f32 v84, v80, v82
v_fmac_f32 v84, v81, v83
Mvaa 188,132,28
v_sub_f32 v66, v66, v85
v_sub_f32 v67, v67, v85
K 80,66,67
v_lshlrev_b32 v66, 16, v80
v_and_b32 v67, 0xffff0000, v80
U 80,66,87
Zvaa 184,128,28
U 81,67,87
K 37,80,81
v_lshlrev_b32 v80, 16, v37
v_and_b32 v81, 0xffff0000, v37
v_lshlrev_b32 v82, 16, v49
Mvaa 184,32,24
W 4
v_and_b32 v83, 0xffff0000, v49
v_fmac_f32 v84, v80, v82
v_fmac_f32 v84, v81, v83
v_sub_f32 v68, v68, v85
v_sub_f32 v69, v69, v85
Zvaa 192,32,0
K 80,68,69
v_lshlrev_b32 v68, 16, v80
v_and_b32 v69, 0xffff0000, v80
U 80,68,87
U 81,69,87
Mvaa 192,128,4
K 38,80,81
v_lshlrev_b32 v80, 16, v38
v_and_b32 v81, 0xffff0000, v38
v_lshlrev_b32 v82, 16, v50
v_and_b32 v83, 0xffff0000, v50
Zvaa 196,132,4
Ta 16,24,16384
v_fmac_f32 v84, v80, v82
v_fmac_f32 v84, v81, v83
v_sub_f32 v70, v70, v85
v_sub_f32 v71, v71, v85
K 80,70,71
Mvaa 196,36,0
Ta 18,24,17408
v_lshlrev_b32 v70, 16, v80
v_and_b32 v71, 0xffff0000, v80
U 80,70,87
U 81,71,87
K 39,80,81
Zaaa 160,40,0
Ta 20,24,24576
Ta 22,24,25600
v_lshlrev_b32 v80, 16, v39
v_and_b32 v81, 0xffff0000, v39
v_lshlrev_b32 v82, 16, v51
v_and_b32 v83, 0xffff0000, v51
Maaa 160,136,4
v_fmac_f32 v84, v80, v82
v_fmac_f32 v84, v81, v83
v_sub_f32 v72, v72, v85
v_sub_f32 v73, v73, v85
K 80,72,73
v_lshlrev_b32 v72, 16, v80
Zaaa 164,140,4
v_and_b32 v73, 0xffff0000, v80
U 80,72,87
U 81,73,87
K 40,80,81
v_lshlrev_b32 v80, 16, v40
v_and_b32 v81, 0xffff0000, v40
Maaa 164,44,0
v_lshlrev_b32 v82, 16, v52
v_and_b32 v83, 0xffff0000, v52
v_fmac_f32 v84, v80, v82
v_fmac_f32 v84, v81, v83
v_sub_f32 v74, v74, v85
Zaaa 224,48,0
v_sub_f32 v75, v75, v85
K 80,74,75
v_lshlrev_b32 v74, 16, v80
v_and_b32 v75, 0xffff0000, v80
U 80,74,87
Maaa 224,144,4
U 81,75,87
K 41,80,81
v_lshlrev_b32 v80, 16, v41
v_and_b32 v81, 0xffff0000, v41
v_lshlrev_b32 v82, 16, v53
Zaaa 228,148,4
v_and_b32 v83, 0xffff0000, v53
v_fmac_f32 v84, v80, v82
v_fmac_f32 v84, v81, v83
v_sub_f32 v76, v76, v85
v_sub_f32 v77, v77, v85
Maaa 228,52,0
K 80,76,77
v_lshlrev_b32 v76, 16, v80
v_and_b32 v77, 0xffff0000, v80
U 80,76,87
U 81,77,87
Zaaa 96,56,0
K 42,80,81
v_lshlrev_b32 v80, 16, v42
v_and_b32 v81, 0xffff0000, v42
v_lshlrev_b32 v82, 16, v54
v_and_b32 v83, 0xffff0000, v54
Maaa 96,152,4
v_fmac_f32 v84, v80, v82
v_fmac_f32 v84, v81, v83
v_sub_f32 v78, v78, v85
v_sub_f32 v79, v79, v85
K 80,78,79
v_lshlrev_b32 v78, 16, v80
Zaaa 100,156,4
v_and_b32 v79, 0xffff0000, v80
U 80,78,87
U 81,79,87
K 43,80,81
v_lshlrev_b32 v80, 16, v43
Maaa 100,60,0
W 4
v_and_b32 v81, 0xffff0000, v43
v_lshlrev_b32 v82, 16, v55
v_and_b32 v83, 0xffff0000, v55
v_fmac_f32 v84, v80, v82
v_fmac_f32 v84, v81, v83
Zaaa 108,60,8
Ta 24,25,16384
Ta 26,25,17408
v_lshrrev_b32 v82, 9, v1
s_lshl_b32 s80, s14, 1
v_add_u32 v82, s80, v82
v_mul_u32_u24 v83, 1552, v82
v_add_u32 v83, v83, v88
Maaa 108,156,12
v_add_u32 v83, 131072, v83
ds_write_b128 v83, v[32:35] offset:0
ds_write_b128 v83, v[36:39] offset:512
ds_write_b128 v83, v[40:43] offset:1024
v_add_f32_dpp v84, v84, v84 quad_perm:[1,0,3,2] row_mask:0xf bank_mask:0xf
Zaaa 104,152,12
Ta 28,25,24576
Ta 30,25,25600
Maaa 104,56,8
v_add_f32_dpp v84, v84, v84 quad_perm:[2,3,0,1] row_mask:0xf bank_mask:0xf
s_nop 1
v_add_f32_dpp v84, v84, v84 row_half_mirror row_mask:0xf bank_mask:0xf
s_nop 1
v_add_f32_dpp v84, v84, v84 row_mirror row_mask:0xf bank_mask:0xf
v_mov_b32 v81, v84
s_nop 1
v_permlane16_swap_b32 v84, v81
A 84,84,81
Zaaa 236,52,8
K 80,84,84
v_lshlrev_b32 v80, 16, v80
s_lshl_b32 s80, s15, 3
v_add_u32 v82, s80, v82
v_sub_u32 v83, 2047, v82
v_ashrrev_i32 v81, 31, v80
v_or_b32 v81, 0x80000000, v81
Maaa 236,148,12
v_xor_b32 v80, v80, v81
v_and_b32 v80, 0xffff0000, v80
v_or_b32 v80, v80, v83
v_and_b32 v82, 7, v82
v_lshlrev_b32 v82, 2, v82
v_add_u32 v82, 143488, v82
Zaaa 232,144,12
ds_write_b32 v82, v80
Maaa 232,48,8
Zaaa 172,44,8
Maaa 172,140,12
Zaaa 168,136,12
s_waitcnt vmcnt(1) lgkmcnt(0)
s_barrier
Maaa 168,40,8
s_add_u32 s64, s64, 128
v_bfe_u32 v80, v1, 4, 3
Zvaa 204,36,8
v_mul_u32_u24 v81, 1552, v80
Mvaa 204,132,12
s_and_b32 s64, s64, 1023
v_lshrrev_b32 v82, 7, v1
Zvaa 200,128,12
v_lshl_add_u32 v81, v82, 4, v81
Mvaa 200,32,8
s_lshl_b32 s80, s14, 7
Zvaa 208,32,16
s_lshl_b32 s65, s64, 12
s_add_u32 s80, s80, 131072
Mvaa 208,128,20
v_add_u32 v81, s80, v81
Zvaa 212,132,20
s_add_u32 m0, s62, 0
ds_read_b128 v[32:35], v81
Ta 0,10,49152
Mvaa 212,36,16
Ta 2,10,50176
Rv 36,81,512
Zaaa 176,40,16
s_and_b32 s80, s14, 2
Ta 4,10,57344
Ta 6,10,58368
Maaa 176,136,20
s_lshl_b32 s80, s80, 7
D 16,44,65,0
Zaaa 180,140,20
v_subrev_u32 v81, s80, v81
Maaa 180,44,16
Rv 40,81,1024
Zaaa 240,48,16
v_lshlrev_b32 v80, 2, v80
Maaa 240,144,20
D 17,44,65,1024
v_add_u32 v80, 143488, v80
Zaaa 244,148,20
ds_read_b32 v84, v80
Maaa 244,52,16
v_mov_b32 v85, s13
Zaaa 112,56,16
v_lshrrev_b32 v83, 1, v1
Maaa 112,152,20
s_mul_i32 s82, s15, 10240
D 18,44,65,2048
Zaaa 116,156,20
s_add_u32 s82, s82, 131072
Maaa 116,60,16
s_lshl_b32 s80, s14, 10
Zaaa 124,60,24
s_add_u32 s83, s82, s80
Ra 60,26,47104
Maaa 124,156,28
D 19,44,65,3072
Ra 156,27,47104
Zaaa 120,152,28
s_add_u32 m0, s62, 4096
Ra 152,27,45056
Ta 8,11,49152
Maaa 120,56,24
Ta 10,11,50176
Ra 56,26,45056
Zaaa 252,52,24
Ta 12,11,57344
Ta 14,11,58368
Maaa 252,148,28
W 8
buffer_store_dwordx4 v[32:35], v1, s[8:11], s83 offen sc1
s_add_u32 s83, s83, 4096
Zaaa 248,144,28
Ra 52,26,43008
Ra 148,27,43008
Maaa 248,48,24
Ra 144,27,40960
Ra 48,26,40960
Zaaa 188,44,24
Ra 44,26,38912
Maaa 188,140,28
D 20,44,65,0
Ra 140,27,38912
Zaaa 184,136,28
Ra 136,27,36864
Maaa 184,40,24
Ra 40,26,36864
Zvaa 220,36,24
Ra 36,26,34816
Mvaa 220,132,28
buffer_store_dwordx4 v[36:39], v1, s[8:11], s83 offen sc1
Ra 132,27,34816
Zvaa 216,128,28
s_and_b32 s80, s80, 1024
s_add_u32 s83, s82, s80
s_add_u32 s83, s83, 8192
Mvaa 216,32,24
W 12
Ra 128,27,32768
Ra 32,26,32768
Maaa 68,60,0
Maaa 68,156,4
D 21,44,65,1024
Maaa 64,152,4
Ta 16,12,49152
Ta 18,12,50176
Maaa 64,56,0
W 10
Ta 20,12,57344
Ta 22,12,58368
Maaa 196,52,0
Maaa 196,148,4
D 22,44,65,2048
Maaa 192,144,4
Maaa 192,48,0
W 8
Mvaa 228,44,0
Mvaa 228,140,4
buffer_store_dwordx4 v[40:43], v1, s[8:11], s83 offen sc1
Mvaa 224,136,4
Mvaa 224,40,0
W 4
Mvaa 164,36,0
Mvaa 164,132,4
D 23,44,65,3072
Mvaa 160,128,4
s_add_u32 m0, s61, 0
Mvaa 160,32,0
Mvaa 168,32,8
Mvaa 168,128,12
D 2,40,64,0
Mvaa 172,132,12
Ta 24,13,49152
Ta 26,13,50176
Mvaa 172,36,8
Ta 28,13,57344
Ta 30,13,58368
Mvaa 232,40,8
.Lh_0_1_1%=:
s_cbranch_execz .Lhr_5%=
Mvaa 232,136,12
D 3,40,64,1024
Mvaa 236,140,12
Mvaa 236,44,8
Maaa 200,48,8
Maaa 200,144,12
D 4,40,64,2048
Maaa 204,148,12
Maaa 204,52,8
Maaa 72,56,8
Maaa 72,152,12
D 5,40,64,3072
Maaa 76,156,12
s_add_u32 m0, s61, 4096
Maaa 76,60,8
W 4
Maaa 84,60,16
Maaa 84,156,20
D 6,40,64,0
Maaa 80,152,20
Ta 0,14,49152
Ta 2,14,50176
Maaa 80,56,16
Ta 4,14,57344
Ta 6,14,58368
Maaa 212,52,16
Maaa 212,148,20
D 7,40,64,1024
Maaa 208,144,20
Maaa 208,48,16
Mvaa 244,44,16
Mvaa 244,140,20
D 8,40,64,2048
)"
R"(Mvaa 240,136,20
Mvaa 240,40,16
Mvaa 180,36,16
Mvaa 180,132,20
D 9,40,64,3072
Mvaa 176,128,20
Mvaa 176,32,16
W 4
Mvaa 184,32,24
Mvaa 184,128,28
global_load_dword v142, v[140:141], off
v_lshl_add_u64 v[140:141], v[140:141], 0, s[68:69]
v_bfe_u32 v147, v1, 5, 3
Mvaa 188,132,28
v_lshlrev_b32 v147, 13, v147
v_and_b32 v148, 16, v1
Mvaa 188,36,24
Ta 8,15,49152
v_lshl_add_u32 v147, v148, 3, v147
Mvaa 248,40,24
s_lshl_b32 s92, s57, 2
s_add_u32 s92, s92, s14
s_lshl_b32 s92, s92, 16
Mvaa 248,136,28
buffer_load_dword v142, v147, s[48:51], s92 offen
Ta 10,15,50176
Mvaa 252,140,28
Ta 12,15,57344
Ta 14,15,58368
Mvaa 252,44,24
Maaa 216,48,24
Maaa 216,144,28
Maaa 220,148,28
Maaa 220,52,24
Maaa 88,56,24
Maaa 88,152,28
Maaa 92,156,28
Maaa 92,60,24
W 4
Maaa 100,60,0
Maaa 100,156,4
Maaa 96,152,4
Ta 16,24,49152
Ta 18,24,50176
Maaa 96,56,0
Ta 20,24,57344
Ta 22,24,58368
Maaa 228,52,0
Maaa 228,148,4
Maaa 224,144,4
Maaa 224,48,0
Maaa 164,44,0
Maaa 164,140,4
Maaa 160,136,4
Maaa 160,40,0
Mvaa 196,36,0
Mvaa 196,132,4
Mvaa 192,128,4
Mvaa 192,32,0
W 4
Mvaa 200,32,8
Ta 24,25,49152
Ta 26,25,50176
Mvaa 200,128,12
Mvaa 204,132,12
Ta 28,25,57344
Ta 30,25,58368
Mvaa 204,36,8
Maaa 168,40,8
Maaa 168,136,12
Maaa 172,140,12
Maaa 172,44,8
Maaa 232,48,8
Maaa 232,144,12
Maaa 236,148,12
s_waitcnt vmcnt(2) lgkmcnt(0)
s_barrier
s_cmp_eq_u32 s14, 0
s_cbranch_scc0 .Lpubr_15%=
s_lshl_b32 s80, s15, 6
s_add_u32 s80, s80, 40960
s_mov_b64 exec, 0xff
buffer_store_dwordx2 v[84:85], v83, s[8:11], s80 offen sc1
s_mov_b64 exec, -1
.Lpubr_15%=:
Maaa 236,52,8
s_add_u32 s64, s64, 128
Maaa 104,56,8
Maaa 104,152,12
s_and_b32 s64, s64, 1023
Maaa 108,156,12
Maaa 108,60,8
Maaa 116,60,16
s_lshl_b32 s65, s64, 12
Maaa 116,156,20
Maaa 112,152,20
Ta 0,10,16384
Ta 2,10,17408
Maaa 112,56,16
s_add_u32 m0, s62, 32768
Ta 4,10,24576
Ta 6,10,25600
Maaa 244,52,16
Maaa 244,148,20
Maaa 240,144,20
D 16,44,65,0
Maaa 240,48,16
Maaa 180,44,16
Maaa 180,140,20
Maaa 176,136,20
D 17,44,65,1024
Maaa 176,40,16
Mvaa 212,36,16
Mvaa 212,132,20
Mvaa 208,128,20
D 18,44,65,2048
Mvaa 208,32,16
Mvaa 216,32,24
Ra 32,26,0
Mvaa 216,128,28
Ra 128,27,0
Mvaa 220,132,28
D 19,44,65,3072
Ra 132,27,2048
Mvaa 220,36,24
s_add_u32 m0, s62, 36864
Ta 8,11,16384
Ta 10,11,17408
Maaa 184,40,24
Ra 36,26,2048
Ta 12,11,24576
Maaa 184,136,28
Ta 14,11,25600
Ra 40,26,4096
Maaa 188,140,28
D 20,44,65,0
Ra 136,27,4096
Maaa 188,44,24
Ra 140,27,6144
Ra 44,26,6144
Maaa 248,48,24
Ra 48,26,8192
Maaa 248,144,28
Ra 144,27,8192
Maaa 252,148,28
D 21,44,65,1024
Ra 148,27,10240
Maaa 252,52,24
Ra 52,26,10240
Maaa 120,56,24
Ra 56,26,12288
Maaa 120,152,28
Ra 152,27,12288
Maaa 124,156,28
D 22,44,65,2048
Ra 156,27,14336
Maaa 124,60,24
W 13
Ra 60,26,14336
Mvaa 160,32,0
Mvaa 160,128,4
Mvaa 164,132,4
D 23,44,65,3072
Ta 16,12,16384
Mvaa 164,36,0
W 9
s_add_u32 m0, s61, 32768
Ta 18,12,17408
Mvaa 224,40,0
Ta 20,12,24576
Ta 22,12,25600
Mvaa 224,136,4
Mvaa 228,140,4
D 2,40,64,0
Mvaa 228,44,0
W 8
Maaa 192,48,0
Maaa 192,144,4
Maaa 196,148,4
D 3,40,64,1024
Maaa 196,52,0
W 4
Maaa 64,56,0
Maaa 64,152,4
Maaa 68,156,4
D 4,40,64,2048
Maaa 68,60,0
Maaa 76,60,8
Maaa 76,156,12
Maaa 72,152,12
D 5,40,64,3072
Ta 24,13,16384
Maaa 72,56,8
s_add_u32 m0, s61, 36864
Ta 26,13,17408
Ta 28,13,24576
Maaa 204,52,8
Ta 30,13,25600
Maaa 204,148,12
Maaa 200,144,12
D 6,40,64,0
Maaa 200,48,8
Mvaa 236,44,8
Mvaa 236,140,12
Mvaa 232,136,12
D 7,40,64,1024
Mvaa 232,40,8
Mvaa 172,36,8
Mvaa 172,132,12
Mvaa 168,128,12
D 8,40,64,2048
Mvaa 168,32,8
W 4
Mvaa 176,32,16
Mvaa 176,128,20
Mvaa 180,132,20
D 9,40,64,3072
Ta 0,14,16384
Mvaa 180,36,16
Ta 2,14,17408
Ta 4,14,24576
Mvaa 240,40,16
Ta 6,14,25600
Mvaa 240,136,20
Mvaa 244,140,20
Mvaa 244,44,16
Maaa 208,48,16
Maaa 208,144,20
Maaa 212,148,20
Maaa 212,52,16
Maaa 80,56,16
Maaa 80,152,20
Maaa 84,156,20
Maaa 84,60,16
W 4
Maaa 92,60,24
Maaa 92,156,28
Maaa 88,152,28
Ta 8,15,16384
Ta 10,15,17408
Maaa 88,56,24
Ta 12,15,24576
Ta 14,15,25600
Maaa 220,52,24
Maaa 220,148,28
Maaa 216,144,28
Maaa 216,48,24
Mvaa 252,44,24
Mvaa 252,140,28
Mvaa 248,136,28
Mvaa 248,40,24
Mvaa 188,36,24
Mvaa 188,132,28
Mvaa 184,128,28
Mvaa 184,32,24
W 4
Mvaa 192,32,0
Mvaa 192,128,4
Mvaa 196,132,4
Ta 16,24,16384
Ta 18,24,17408
Mvaa 196,36,0
Ta 20,24,24576
Ta 22,24,25600
Maaa 160,40,0
Maaa 160,136,4
Maaa 164,140,4
Maaa 164,44,0
Maaa 224,48,0
Maaa 224,144,4
Maaa 228,148,4
Maaa 228,52,0
Maaa 96,56,0
Maaa 96,152,4
Maaa 100,156,4
Maaa 100,60,0
W 4
Maaa 108,60,8
Ta 24,25,16384
Ta 26,25,17408
Maaa 108,156,12
Maaa 104,152,12
Ta 28,25,24576
Ta 30,25,25600
Maaa 104,56,8
Maaa 236,52,8
Maaa 236,148,12
Maaa 232,144,12
Maaa 232,48,8
Maaa 172,44,8
Maaa 172,140,12
Maaa 168,136,12
s_waitcnt vmcnt(0) lgkmcnt(0)
s_barrier
Maaa 168,40,8
s_add_u32 s64, s64, 128
Mvaa 204,36,8
Mvaa 204,132,12
s_and_b32 s64, s64, 1023
Mvaa 200,128,12
Mvaa 200,32,8
Mvaa 208,32,16
s_lshl_b32 s65, s64, 12
Mvaa 208,128,20
Mvaa 212,132,20
s_add_u32 m0, s62, 0
Ta 0,10,49152
Ta 2,10,50176
Mvaa 212,36,16
Ta 4,10,57344
Ta 6,10,58368
Maaa 176,40,16
Maaa 176,136,20
D 16,44,65,0
Maaa 180,140,20
Maaa 180,44,16
Maaa 240,48,16
Maaa 240,144,20
D 17,44,65,1024
Maaa 244,148,20
Maaa 244,52,16
Maaa 112,56,16
Maaa 112,152,20
D 18,44,65,2048
Maaa 116,156,20
Maaa 116,60,16
Maaa 124,60,24
Ra 60,26,47104
Maaa 124,156,28
D 19,44,65,3072
Ra 156,27,47104
Maaa 120,152,28
s_add_u32 m0, s62, 4096
Ra 152,27,45056
Ta 8,11,49152
Maaa 120,56,24
Ta 10,11,50176
Ra 56,26,45056
Maaa 252,52,24
Ta 12,11,57344
Ta 14,11,58368
Maaa 252,148,28
D 20,44,65,0
Ra 52,26,43008
Maaa 248,144,28
Ra 148,27,43008
Ra 144,27,40960
Maaa 248,48,24
Ra 48,26,40960
Maaa 188,44,24
Ra 44,26,38912
Maaa 188,140,28
D 21,44,65,1024
Ra 140,27,38912
Maaa 184,136,28
Ra 136,27,36864
Maaa 184,40,24
Ra 40,26,36864
Mvaa 220,36,24
Ra 36,26,34816
Mvaa 220,132,28
D 22,44,65,2048
Ra 132,27,34816
Mvaa 216,128,28
Ra 128,27,32768
Mvaa 216,32,24
W 13
Ra 32,26,32768
Maaa 68,60,0
Maaa 68,156,4
D 23,44,65,3072
Maaa 64,152,4
s_add_u32 m0, s61, 0
Ta 16,12,49152
Ta 18,12,50176
Maaa 64,56,0
W 10
Ta 20,12,57344
Ta 22,12,58368
Maaa 196,52,0
Maaa 196,148,4
D 2,40,64,0
Maaa 192,144,4
Maaa 192,48,0
W 8
Mvaa 228,44,0
Mvaa 228,140,4
D 3,40,64,1024
Mvaa 224,136,4
Mvaa 224,40,0
W 4
Mvaa 164,36,0
Mvaa 164,132,4
D 4,40,64,2048
Mvaa 160,128,4
Mvaa 160,32,0
Mvaa 168,32,8
Mvaa 168,128,12
D 5,40,64,3072
Mvaa 172,132,12
)"
R"(s_add_u32 m0, s61, 4096
Ta 24,13,49152
Ta 26,13,50176
Mvaa 172,36,8
Ta 28,13,57344
Ta 30,13,58368
Mvaa 232,40,8
Mvaa 232,136,12
D 6,40,64,0
Mvaa 236,140,12
Mvaa 236,44,8
Maaa 200,48,8
Maaa 200,144,12
D 7,40,64,1024
Maaa 204,148,12
Maaa 204,52,8
Maaa 72,56,8
Maaa 72,152,12
D 8,40,64,2048
Maaa 76,156,12
Maaa 76,60,8
W 4
Maaa 84,60,16
Maaa 84,156,20
D 9,40,64,3072
Maaa 80,152,20
Ta 0,14,49152
Ta 2,14,50176
Maaa 80,56,16
Ta 4,14,57344
Ta 6,14,58368
Maaa 212,52,16
Maaa 212,148,20
global_load_dword v142, v[140:141], off
v_lshl_add_u64 v[140:141], v[140:141], 0, s[68:69]
Maaa 208,144,20
Maaa 208,48,16
Mvaa 244,44,16
Mvaa 244,140,20
Mvaa 240,136,20
Mvaa 240,40,16
Mvaa 180,36,16
Mvaa 180,132,20
Mvaa 176,128,20
Mvaa 176,32,16
W 4
Mvaa 184,32,24
Mvaa 184,128,28
Mvaa 188,132,28
Ta 8,15,49152
Ta 10,15,50176
Mvaa 188,36,24
Ta 12,15,57344
Ta 14,15,58368
Mvaa 248,40,24
Mvaa 248,136,28
Mvaa 252,140,28
Mvaa 252,44,24
Maaa 216,48,24
Maaa 216,144,28
Maaa 220,148,28
Maaa 220,52,24
Maaa 88,56,24
Maaa 88,152,28
Maaa 92,156,28
Maaa 92,60,24
W 4
Maaa 100,60,0
Maaa 100,156,4
Maaa 96,152,4
Ta 16,24,49152
Ta 18,24,50176
Maaa 96,56,0
Ta 20,24,57344
Ta 22,24,58368
Maaa 228,52,0
Maaa 228,148,4
Maaa 224,144,4
Maaa 224,48,0
Maaa 164,44,0
Maaa 164,140,4
Maaa 160,136,4
Maaa 160,40,0
Mvaa 196,36,0
Mvaa 196,132,4
Mvaa 192,128,4
Mvaa 192,32,0
W 4
Mvaa 200,32,8
Ta 24,25,49152
Ta 26,25,50176
Mvaa 200,128,12
Mvaa 204,132,12
Ta 28,25,57344
Ta 30,25,58368
Mvaa 204,36,8
Maaa 168,40,8
Maaa 168,136,12
.Lh_0_2_1%=:
s_cbranch_execz .Lhr_5%=
Maaa 172,140,12
Maaa 172,44,8
Maaa 232,48,8
Maaa 232,144,12
Maaa 236,148,12
s_waitcnt vmcnt(1) lgkmcnt(0)
s_barrier
Maaa 236,52,8
s_add_u32 s64, s64, 128
Maaa 104,56,8
Maaa 104,152,12
s_and_b32 s64, s64, 1023
Maaa 108,156,12
Maaa 108,60,8
Maaa 116,60,16
s_lshl_b32 s65, s64, 12
Maaa 116,156,20
Maaa 112,152,20
Ta 0,10,16384
Ta 2,10,17408
Maaa 112,56,16
s_add_u32 m0, s62, 32768
Ta 4,10,24576
Ta 6,10,25600
Maaa 244,52,16
Maaa 244,148,20
Maaa 240,144,20
D 16,44,65,0
Maaa 240,48,16
Maaa 180,44,16
Maaa 180,140,20
Maaa 176,136,20
D 17,44,65,1024
Maaa 176,40,16
Mvaa 212,36,16
Mvaa 212,132,20
Mvaa 208,128,20
D 18,44,65,2048
Mvaa 208,32,16
Mvaa 216,32,24
Ra 32,26,0
Mvaa 216,128,28
Ra 128,27,0
Mvaa 220,132,28
D 19,44,65,3072
Ra 132,27,2048
Mvaa 220,36,24
s_add_u32 m0, s62, 36864
Ta 8,11,16384
Ta 10,11,17408
Maaa 184,40,24
Ra 36,26,2048
Ta 12,11,24576
Maaa 184,136,28
Ta 14,11,25600
Ra 40,26,4096
Maaa 188,140,28
D 20,44,65,0
Ra 136,27,4096
Maaa 188,44,24
Ra 140,27,6144
Ra 44,26,6144
Maaa 248,48,24
Ra 48,26,8192
Maaa 248,144,28
Ra 144,27,8192
Maaa 252,148,28
D 21,44,65,1024
Ra 148,27,10240
Maaa 252,52,24
Ra 52,26,10240
Maaa 120,56,24
Ra 56,26,12288
Maaa 120,152,28
Ra 152,27,12288
Maaa 124,156,28
D 22,44,65,2048
Ra 156,27,14336
Maaa 124,60,24
W 13
Ra 60,26,14336
Mvaa 160,32,0
Mvaa 160,128,4
Mvaa 164,132,4
D 23,44,65,3072
Ta 16,12,16384
Mvaa 164,36,0
W 9
s_add_u32 m0, s61, 32768
Ta 18,12,17408
Mvaa 224,40,0
Ta 20,12,24576
Ta 22,12,25600
Mvaa 224,136,4
Mvaa 228,140,4
D 2,40,64,0
Mvaa 228,44,0
W 8
Maaa 192,48,0
Maaa 192,144,4
Maaa 196,148,4
D 3,40,64,1024
Maaa 196,52,0
W 4
Maaa 64,56,0
Maaa 64,152,4
Maaa 68,156,4
D 4,40,64,2048
Maaa 68,60,0
Maaa 76,60,8
Maaa 76,156,12
Maaa 72,152,12
D 5,40,64,3072
Ta 24,13,16384
Maaa 72,56,8
s_add_u32 m0, s61, 36864
Ta 26,13,17408
Ta 28,13,24576
Maaa 204,52,8
Ta 30,13,25600
Maaa 204,148,12
Maaa 200,144,12
D 6,40,64,0
Maaa 200,48,8
Mvaa 236,44,8
Mvaa 236,140,12
Mvaa 232,136,12
D 7,40,64,1024
Mvaa 232,40,8
Mvaa 172,36,8
Mvaa 172,132,12
Mvaa 168,128,12
D 8,40,64,2048
Mvaa 168,32,8
W 4
Mvaa 176,32,16
Mvaa 176,128,20
Mvaa 180,132,20
D 9,40,64,3072
Ta 0,14,16384
Mvaa 180,36,16
s_add_u32 s92, s92, 2097152
Ta 2,14,17408
Ta 4,14,24576
Mvaa 240,40,16
Ta 6,14,25600
Mvaa 240,136,20
Mvaa 244,140,20
buffer_load_dword v142, v147, s[48:51], s92 offen
Mvaa 244,44,16
s_add_u32 s92, s92, 2097152
Maaa 208,48,16
Maaa 208,144,20
Maaa 212,148,20
buffer_load_dword v142, v147, s[48:51], s92 offen
Maaa 212,52,16
Maaa 80,56,16
Maaa 80,152,20
Maaa 84,156,20
Maaa 84,60,16
W 4
Maaa 92,60,24
Maaa 92,156,28
Maaa 88,152,28
Ta 8,15,16384
Ta 10,15,17408
Maaa 88,56,24
Ta 12,15,24576
Ta 14,15,25600
Maaa 220,52,24
Maaa 220,148,28
Maaa 216,144,28
Maaa 216,48,24
Mvaa 252,44,24
Mvaa 252,140,28
Mvaa 248,136,28
Mvaa 248,40,24
Mvaa 188,36,24
Mvaa 188,132,28
Mvaa 184,128,28
Mvaa 184,32,24
W 4
Mvaa 192,32,0
Mvaa 192,128,4
Mvaa 196,132,4
Ta 16,24,16384
Ta 18,24,17408
Mvaa 196,36,0
Ta 20,24,24576
Ta 22,24,25600
Maaa 160,40,0
Maaa 160,136,4
Maaa 164,140,4
Maaa 164,44,0
Maaa 224,48,0
Maaa 224,144,4
Maaa 228,148,4
Maaa 228,52,0
Maaa 96,56,0
Maaa 96,152,4
Maaa 100,156,4
Maaa 100,60,0
W 4
Maaa 108,60,8
Ta 24,25,16384
Ta 26,25,17408
Maaa 108,156,12
Maaa 104,152,12
Ta 28,25,24576
Ta 30,25,25600
Maaa 104,56,8
Maaa 236,52,8
Maaa 236,148,12
Maaa 232,144,12
Maaa 232,48,8
Maaa 172,44,8
Maaa 172,140,12
Maaa 168,136,12
s_waitcnt vmcnt(2) lgkmcnt(0)
s_barrier
Maaa 168,40,8
s_add_u32 s64, s64, 128
Mvaa 204,36,8
Mvaa 204,132,12
s_and_b32 s64, s64, 1023
Mvaa 200,128,12
Mvaa 200,32,8
Mvaa 208,32,16
s_lshl_b32 s65, s64, 12
Mvaa 208,128,20
Mvaa 212,132,20
Ta 0,10,49152
Ta 2,10,50176
Mvaa 212,36,16
s_add_u32 m0, s62, 0
Ta 4,10,57344
Ta 6,10,58368
Maaa 176,40,16
Maaa 176,136,20
Maaa 180,140,20
D 16,44,65,0
Maaa 180,44,16
Maaa 240,48,16
Maaa 240,144,20
Maaa 244,148,20
D 17,44,65,1024
Maaa 244,52,16
Maaa 112,56,16
Maaa 112,152,20
Maaa 116,156,20
D 18,44,65,2048
Maaa 116,60,16
Maaa 124,60,24
Ra 60,26,47104
Maaa 124,156,28
Ra 156,27,47104
Maaa 120,152,28
D 19,44,65,3072
Ra 152,27,45056
Maaa 120,56,24
s_add_u32 m0, s62, 4096
Ta 8,11,49152
Ta 10,11,50176
Maaa 252,52,24
Ra 56,26,45056
Ta 12,11,57344
Maaa 252,148,28
Ta 14,11,58368
Ra 52,26,43008
Maaa 248,144,28
D 20,44,65,0
v_lshrrev_b32 v28, 1, v1
Maaa 248,48,24
s_lshl_b32 s80, s60, 9
Ra 148,27,43008
Ra 144,27,40960
Maaa 188,44,24
s_add_u32 s80, s80, 40960
Ra 48,26,40960
Ra 44,26,38912
Maaa 188,140,28
Ra 140,27,38912
Maaa 184,136,28
D 21,44,65,1024
Ra 136,27,36864
Maaa 184,40,24
Ra 40,26,36864
Mvaa 220,36,24
Ra 36,26,34816
Mvaa 220,132,28
Ra 132,27,34816
Mvaa 216,128,28
buffer_load_dwordx2 v[28:29], v28, s[8:11], s80 offen sc1
Ra 128,27,32768
Mvaa 216,32,24
W 13
Ra 32,26,32768
Maaa 68,60,0
Maaa 68,156,4
Maaa 64,152,4
)"
R"(D 22,44,65,2048
Ta 16,12,49152
Maaa 64,56,0
W 9
Ta 18,12,50176
Ta 20,12,57344
Maaa 196,52,0
Ta 22,12,58368
Maaa 196,148,4
Maaa 192,144,4
D 23,44,65,3072
Maaa 192,48,0
W 8
s_add_u32 m0, s61, 0
Mvaa 228,44,0
Mvaa 228,140,4
Mvaa 224,136,4
D 2,40,64,0
Mvaa 224,40,0
W 4
Mvaa 164,36,0
Mvaa 164,132,4
Mvaa 160,128,4
D 3,40,64,1024
Mvaa 160,32,0
Mvaa 168,32,8
Mvaa 168,128,12
Mvaa 172,132,12
D 4,40,64,2048
Ta 24,13,49152
Mvaa 172,36,8
Ta 26,13,50176
Ta 28,13,57344
Mvaa 232,40,8
Ta 30,13,58368
Mvaa 232,136,12
Mvaa 236,140,12
D 5,40,64,3072
Mvaa 236,44,8
s_add_u32 m0, s61, 4096
Maaa 200,48,8
Maaa 200,144,12
Maaa 204,148,12
D 6,40,64,0
Maaa 204,52,8
Maaa 72,56,8
Maaa 72,152,12
Maaa 76,156,12
D 7,40,64,1024
Maaa 76,60,8
W 4
Maaa 84,60,16
Maaa 84,156,20
Maaa 80,152,20
D 8,40,64,2048
Ta 0,14,49152
Maaa 80,56,16
Ta 2,14,50176
Ta 4,14,57344
Maaa 212,52,16
Ta 6,14,58368
Maaa 212,148,20
Maaa 208,144,20
D 9,40,64,3072
Maaa 208,48,16
Mvaa 244,44,16
Mvaa 244,140,20
Mvaa 240,136,20
s_waitcnt vmcnt(10)
v_cmp_eq_u32 vcc, s13, v29
s_cmp_eq_u64 vcc, -1
s_cbranch_scc0 .Lspin_16%=
.Lok_17%=:
global_load_dword v142, v146, s[6:7]
Mvaa 240,40,16
Mvaa 180,36,16
v_bfe_u32 v144, v1, 8, 1
Mvaa 180,132,20
v_mul_u32_u24 v144, 10240, v144
Mvaa 176,128,20
v_and_b32 v145, 0xf0, v1
Mvaa 176,32,16
W 4
v_lshl_add_u32 v144, v145, 3, v144
Mvaa 184,32,24
s_mul_i32 s91, s58, 20480
Mvaa 184,128,28
s_add_u32 s91, s91, s28
Mvaa 188,132,28
buffer_load_dword v142, v144, s[8:11], s91 offen offset:0 sc1
Ta 8,15,49152
Mvaa 188,36,24
Ta 10,15,50176
Ta 12,15,57344
Mvaa 248,40,24
Ta 14,15,58368
Mvaa 248,136,28
Mvaa 252,140,28
Mvaa 252,44,24
Maaa 216,48,24
Maaa 216,144,28
Maaa 220,148,28
Maaa 220,52,24
Maaa 88,56,24
Maaa 88,152,28
Maaa 92,156,28
Maaa 92,60,24
W 4
Maaa 100,60,0
Maaa 100,156,4
Maaa 96,152,4
Ta 16,24,49152
Ta 18,24,50176
Maaa 96,56,0
Ta 20,24,57344
Ta 22,24,58368
Maaa 228,52,0
Maaa 228,148,4
Maaa 224,144,4
Maaa 224,48,0
Maaa 164,44,0
Maaa 164,140,4
Maaa 160,136,4
Maaa 160,40,0
Mvaa 196,36,0
Mvaa 196,132,4
Mvaa 192,128,4
Mvaa 192,32,0
W 4
Mvaa 200,32,8
Ta 24,25,49152
Ta 26,25,50176
Mvaa 200,128,12
Mvaa 204,132,12
Ta 28,25,57344
Ta 30,25,58368
Mvaa 204,36,8
Maaa 168,40,8
Maaa 168,136,12
Maaa 172,140,12
Maaa 172,44,8
Maaa 232,48,8
Maaa 232,144,12
Maaa 236,148,12
s_waitcnt vmcnt(2) lgkmcnt(0)
s_barrier
s_cmp_eq_u32 s85, 107
s_cbranch_scc1 .Lrt_19%=
.Lrtr_20%=:
Maaa 236,52,8
s_add_u32 s64, s64, 128
Maaa 104,56,8
Maaa 104,152,12
s_and_b32 s64, s64, 1023
Maaa 108,156,12
Maaa 108,60,8
Maaa 116,60,16
s_lshl_b32 s65, s64, 12
Maaa 116,156,20
Maaa 112,152,20
Ta 0,10,16384
Ta 2,10,17408
Maaa 112,56,16
s_add_u32 m0, s62, 32768
Ta 4,10,24576
Ta 6,10,25600
Maaa 244,52,16
Maaa 244,148,20
Maaa 240,144,20
D 16,44,65,0
Maaa 240,48,16
Maaa 180,44,16
Maaa 180,140,20
Maaa 176,136,20
D 17,44,65,1024
Maaa 176,40,16
Mvaa 212,36,16
Mvaa 212,132,20
Mvaa 208,128,20
D 18,44,65,2048
Mvaa 208,32,16
Mvaa 216,32,24
Ra 32,26,0
Mvaa 216,128,28
Ra 128,27,0
Mvaa 220,132,28
D 19,44,65,3072
Ra 132,27,2048
Mvaa 220,36,24
s_add_u32 m0, s62, 36864
Ta 8,11,16384
Ta 10,11,17408
Maaa 184,40,24
Ra 36,26,2048
Ta 12,11,24576
Maaa 184,136,28
Ta 14,11,25600
Ra 40,26,4096
Maaa 188,140,28
D 20,44,65,0
Ra 136,27,4096
Maaa 188,44,24
Ra 140,27,6144
Ra 44,26,6144
Maaa 248,48,24
Ra 48,26,8192
Maaa 248,144,28
Ra 144,27,8192
Maaa 252,148,28
D 21,44,65,1024
Ra 148,27,10240
Maaa 252,52,24
Ra 52,26,10240
Maaa 120,56,24
Ra 56,26,12288
Maaa 120,152,28
Ra 152,27,12288
Maaa 124,156,28
D 22,44,65,2048
Ra 156,27,14336
Maaa 124,60,24
W 13
Ra 60,26,14336
Mvaa 160,32,0
Mvaa 160,128,4
Mvaa 164,132,4
D 23,44,65,3072
Ta 16,12,16384
Mvaa 164,36,0
W 9
s_add_u32 m0, s61, 32768
Ta 18,12,17408
Mvaa 224,40,0
.Lh_0_3_1%=:
s_cbranch_execz .Lhr_5%=
Ta 20,12,24576
Ta 22,12,25600
Mvaa 224,136,4
Mvaa 228,140,4
D 2,40,64,0
Mvaa 228,44,0
W 8
Maaa 192,48,0
Maaa 192,144,4
Maaa 196,148,4
D 3,40,64,1024
Maaa 196,52,0
W 4
Maaa 64,56,0
Maaa 64,152,4
Maaa 68,156,4
D 4,40,64,2048
Maaa 68,60,0
Maaa 76,60,8
Maaa 76,156,12
Maaa 72,152,12
D 5,40,64,3072
Ta 24,13,16384
Maaa 72,56,8
s_add_u32 m0, s61, 36864
Ta 26,13,17408
Ta 28,13,24576
Maaa 204,52,8
Ta 30,13,25600
Maaa 204,148,12
Maaa 200,144,12
D 6,40,64,0
Maaa 200,48,8
Mvaa 236,44,8
Mvaa 236,140,12
Mvaa 232,136,12
D 7,40,64,1024
Mvaa 232,40,8
Mvaa 172,36,8
Mvaa 172,132,12
Mvaa 168,128,12
D 8,40,64,2048
Mvaa 168,32,8
W 4
Mvaa 176,32,16
Mvaa 176,128,20
Mvaa 180,132,20
D 9,40,64,3072
v_add_u32 v2, 131072, v10
Mvaa 180,36,16
Ta 0,14,16384
v_add_u32 v3, 131072, v11
Mvaa 240,40,16
Ta 2,14,17408
v_add_u32 v4, 131072, v12
Mvaa 240,136,20
Ta 4,14,24576
Ta 6,14,25600
Mvaa 244,140,20
buffer_load_dword v142, v144, s[8:11], s91 offen offset:2048 sc1
v_add_u32 v5, 131072, v13
Mvaa 244,44,16
s_add_u32 s91, s91, 4096
v_add_u32 v6, 131072, v14
Maaa 208,48,16
v_add_u32 v7, 131072, v15
Maaa 208,144,20
Maaa 212,148,20
v_add_u32 v8, 131072, v24
Maaa 212,52,16
Maaa 80,56,16
v_add_u32 v9, 131072, v25
Maaa 80,152,20
s_add_u32 s65, s5, 0
s_cmp_ge_u32 s65, 20
s_cselect_b32 s90, 20, 0
s_sub_u32 s65, s65, s90
Maaa 84,156,20
s_lshl_b32 s65, s65, 18
s_add_u32 m0, s63, 131072
Maaa 84,60,16
W 4
Maaa 92,60,24
Maaa 92,156,28
Maaa 88,152,28
Ta 8,15,16384
Ta 10,15,17408
Maaa 88,56,24
D 16,48,65,0
Ta 12,15,24576
Maaa 220,52,24
Ta 14,15,25600
Maaa 220,148,28
Maaa 216,144,28
Maaa 216,48,24
D 17,48,65,1024
Mvaa 252,44,24
Mvaa 252,140,28
Mvaa 248,136,28
Mvaa 248,40,24
Mvaa 188,36,24
D 18,48,65,2048
Mvaa 188,132,28
Mvaa 184,128,28
Mvaa 184,32,24
W 4
Mvaa 192,32,0
Mvaa 192,128,4
D 19,48,65,3072
Mvaa 196,132,4
s_add_u32 m0, s63, 135168
Ta 16,24,16384
Ta 18,24,17408
Mvaa 196,36,0
Ta 20,24,24576
Ta 22,24,25600
Maaa 160,40,0
Maaa 160,136,4
Maaa 164,140,4
D 20,48,65,0
Maaa 164,44,0
Maaa 224,48,0
Maaa 224,144,4
Maaa 228,148,4
Maaa 228,52,0
D 21,48,65,1024
Maaa 96,56,0
Maaa 96,152,4
Maaa 100,156,4
Maaa 100,60,0
W 4
Maaa 108,60,8
D 22,48,65,2048
Ta 24,25,16384
Maaa 108,156,12
Ta 26,25,17408
Maaa 104,152,12
Ta 28,25,24576
Ta 30,25,25600
Maaa 104,56,8
Maaa 236,52,8
D 23,48,65,3072
Maaa 236,148,12
Maaa 232,144,12
Maaa 232,48,8
Maaa 172,44,8
Maaa 172,140,12
Maaa 168,136,12
s_waitcnt vmcnt(9) lgkmcnt(0)
s_barrier
s_cmp_eq_u32 s85, 107
s_cbranch_scc1 .Lrt_21%=
.Lrtr_22%=:
Maaa 168,40,8
s_add_u32 s65, s5, 4
Mvaa 204,36,8
Mvaa 204,132,12
s_cmp_ge_u32 s65, 20
s_cselect_b32 s90, 20, 0
Mvaa 200,128,12
Mvaa 200,32,8
s_sub_u32 s65, s65, s90
Mvaa 208,32,16
Mvaa 208,128,20
s_lshl_b32 s65, s65, 18
)"
R"(Mvaa 212,132,20
Ta 0,10,49152
Ta 2,10,50176
Mvaa 212,36,16
Maaa 176,40,16
s_add_u32 m0, s63, 0
Maaa 176,136,20
Maaa 180,140,20
D 16,48,65,0
Maaa 180,44,16
Maaa 240,48,16
Maaa 240,144,20
Maaa 244,148,20
D 17,48,65,1024
Ta 4,11,49152
Maaa 244,52,16
Ta 6,11,50176
Maaa 112,56,16
Maaa 112,152,20
Maaa 116,156,20
D 18,48,65,2048
Maaa 116,60,16
Maaa 124,60,24
Ra 60,26,47104
Maaa 124,156,28
Maaa 120,152,28
D 19,48,65,3072
Ta 8,12,49152
Maaa 120,56,24
s_add_u32 m0, s63, 4096
Ta 10,12,50176
Ra 56,26,45056
Maaa 252,52,24
Ra 52,26,43008
Maaa 252,148,28
Maaa 248,144,28
D 20,48,65,0
Maaa 248,48,24
Ra 48,26,40960
Maaa 188,44,24
Ra 44,26,38912
Maaa 188,140,28
Maaa 184,136,28
D 21,48,65,1024
Ta 12,13,49152
Maaa 184,40,24
Ta 14,13,50176
Ra 40,26,36864
Mvaa 220,36,24
Ra 36,26,34816
Mvaa 220,132,28
Mvaa 216,128,28
D 22,48,65,2048
Mvaa 216,32,24
Ra 32,26,32768
W 6
Maaa 68,60,0
Maaa 64,56,0
Maaa 196,52,0
D 23,48,65,3072
Ta 16,14,49152
Maaa 192,48,0
W 1
s_add_u32 s65, s5, 8
s_cmp_ge_u32 s65, 20
s_cselect_b32 s90, 20, 0
Mvaa 228,44,0
s_sub_u32 s65, s65, s90
s_lshl_b32 s65, s65, 18
Ta 18,14,50176
Mvaa 224,40,0
s_add_u32 m0, s63, 65536
Mvaa 164,36,0
D 16,48,65,0
Mvaa 160,32,0
Mvaa 168,32,4
Mvaa 172,36,4
Mvaa 232,40,4
D 17,48,65,1024
Ta 20,15,49152
Mvaa 236,44,4
Ta 22,15,50176
Maaa 200,48,4
Maaa 204,52,4
Maaa 72,56,4
D 18,48,65,2048
Maaa 76,60,4
Maaa 84,60,8
Maaa 80,56,8
Maaa 212,52,8
D 19,48,65,3072
Ta 24,24,49152
Maaa 208,48,8
s_add_u32 m0, s63, 69632
Ta 26,24,50176
Mvaa 244,44,8
Mvaa 240,40,8
Mvaa 180,36,8
D 20,48,65,0
Mvaa 176,32,8
Mvaa 184,32,12
Mvaa 188,36,12
Mvaa 248,40,12
D 21,48,65,1024
Ta 28,25,49152
Mvaa 252,44,12
Ta 30,25,50176
Maaa 216,48,12
Ra 156,27,47104
Maaa 220,52,12
Maaa 88,56,12
D 22,48,65,2048
Maaa 92,60,12
W 7
Ra 152,27,45056
Maaa 100,60,16
Maaa 96,56,16
Maaa 228,52,16
D 23,48,65,3072
Ra 148,27,43008
Maaa 224,48,16
Ta 0,10,57344
Ta 2,10,58368
Maaa 164,44,16
Maaa 160,40,16
Ra 144,27,40960
Mvaa 196,36,16
buffer_load_dword v142, v144, s[8:11], s91 offen offset:0 sc1
Mvaa 192,32,16
W 10
Mvaa 200,32,20
Ra 140,27,38912
Mvaa 204,36,20
Maaa 168,40,20
Ta 4,11,57344
Ta 6,11,58368
Maaa 172,44,20
Ra 136,27,36864
Maaa 232,48,20
Maaa 236,52,20
v_bfe_u32 v29, v1, 7, 1
Maaa 104,56,20
Ra 132,27,34816
Maaa 108,60,20
W 13
v_mul_u32_u24 v28, 10240, v29
Maaa 116,60,24
Maaa 112,56,24
Ra 128,27,32768
v_lshrrev_b32 v29, 8, v1
Maaa 244,52,24
Ta 8,12,57344
Ta 10,12,58368
Maaa 240,48,24
v_lshl_add_u32 v28, v29, 7, v28
Maaa 180,44,24
v_and_b32 v29, 0x70, v1
Maaa 176,40,24
Mvaa 212,36,24
v_add_u32 v28, v28, v29
Mvaa 208,32,24
W 14
Mvaa 216,32,28
s_and_b32 s80, s14, 2
Mvaa 220,36,28
s_mul_i32 s80, s80, 20480
Maaa 184,40,28
Ta 12,13,57344
Ta 14,13,58368
Maaa 188,44,28
s_sub_u32 s81, 40960, s80
Maaa 248,48,28
Maaa 252,52,28
s_add_u32 s93, s28, 20480
Maaa 120,56,28
Maaa 124,60,28
W 10
v_add_u32 v26, s80, v28
Maaa 68,156,0
Maaa 64,152,0
Maaa 196,148,0
Ta 16,14,57344
v_add_u32 v27, s81, v28
Maaa 192,144,0
W 5
Ta 18,14,58368
Mvaa 228,140,0
buffer_load_dwordx4 a[32:35], v26, s[8:11], s28 offen offset:0 sc1
Mvaa 224,136,0
Mvaa 164,132,0
Mvaa 160,128,0
Mvaa 168,128,4
buffer_load_dwordx4 a[36:39], v26, s[8:11], s93 offen offset:0 sc1
Mvaa 172,132,4
Mvaa 232,136,4
Ta 20,15,57344
Ta 22,15,58368
Mvaa 236,140,4
Maaa 200,144,4
buffer_load_dwordx4 a[40:43], v27, s[8:11], s28 offen offset:0 sc1
Maaa 204,148,4
Maaa 72,152,4
Maaa 76,156,4
W 6
Maaa 84,156,8
buffer_load_dwordx4 a[44:47], v27, s[8:11], s93 offen offset:0 sc1
Ta 24,24,57344
Maaa 80,152,8
s_add_u32 s28, s28, 512
s_cmp_eq_u32 s28, s4
s_cselect_b32 s93, 10240, 0
Ta 26,24,58368
Maaa 212,148,8
s_sub_u32 s28, s28, s93
Ta 28,25,57344
Ta 30,25,58368
Maaa 208,144,8
s_add_u32 s93, s28, 20480
Mvaa 244,140,8
Mvaa 240,136,8
Mvaa 180,132,8
s_add_u32 s65, s5, 12
s_cmp_ge_u32 s65, 20
s_cselect_b32 s90, 20, 0
buffer_load_dwordx4 a[48:51], v26, s[8:11], s28 offen offset:0 sc1
Mvaa 176,128,8
W 8
s_sub_u32 s65, s65, s90
s_lshl_b32 s65, s65, 18
Mvaa 184,128,12
s_add_u32 m0, s63, 32768
Mvaa 188,132,12
Mvaa 248,136,12
s_waitcnt vmcnt(6) lgkmcnt(0)
s_barrier
Mvaa 252,140,12
buffer_load_dwordx4 a[52:55], v26, s[8:11], s93 offen offset:0 sc1
Maaa 216,144,12
Maaa 220,148,12
Maaa 88,152,12
Maaa 92,156,12
buffer_load_dwordx4 a[56:59], v27, s[8:11], s28 offen offset:0 sc1
Maaa 100,156,16
Maaa 96,152,16
Maaa 228,148,16
Maaa 224,144,16
buffer_load_dwordx4 a[60:63], v27, s[8:11], s93 offen offset:0 sc1
Maaa 164,140,16
Maaa 160,136,16
Mvaa 196,132,16
Mvaa 192,128,16
D 16,48,65,0
Mvaa 200,128,20
Mvaa 204,132,20
Maaa 168,136,20
Ta 0,2,0
Ta 2,2,1024
Maaa 172,140,20
D 17,48,65,1024
Maaa 232,144,20
Maaa 236,148,20
Maaa 104,152,20
Ta 4,2,8192
Ta 6,2,9216
Maaa 108,156,20
D 18,48,65,2048
Maaa 116,156,24
Maaa 112,152,24
Maaa 244,148,24
Ta 8,3,0
Ta 10,3,1024
Maaa 240,144,24
D 19,48,65,3072
Maaa 180,140,24
s_add_u32 m0, s63, 36864
Maaa 176,136,24
Mvaa 212,132,24
Ta 12,3,8192
Ta 14,3,9216
Mvaa 208,128,24
D 20,48,65,0
Mvaa 216,128,28
Mvaa 220,132,28
Maaa 184,136,28
Ta 16,4,0
Ta 18,4,1024
Maaa 188,140,28
Maaa 248,144,28
D 21,48,65,1024
Maaa 252,148,28
Maaa 120,152,28
Maaa 124,156,28
.Lq_7%=:
s_cmp_ge_u32 s14, 2
s_cbranch_scc1 .Lpb_27%=
s_waitcnt vmcnt(6) lgkmcnt(6)
Zvaa 32,0,32
s_add_u32 s28, s28, 512
Ta 20,4,8192
Ta 22,4,9216
Mvaa 32,4,48
s_cmp_eq_u32 s28, s4
s_cselect_b32 s93, 10240, 0
Ta 24,5,0
Ta 26,5,1024
Zvaa 64,4,52
s_sub_u32 s28, s28, s93
s_add_u32 s93, s28, 20480
buffer_load_dwordx4 a[128:131], v26, s[8:11], s28 offen offset:0 sc1
Mvaa 64,0,36
Ta 28,5,8192
Ta 30,5,9216
Zvaa 96,0,40
K 184,184,185
K 185,186,187
Mvaa 96,4,56
K 186,188,189
K 187,190,191
Zvaa 128,4,60
K 192,192,193
Mvaa 128,0,44
W 8
buffer_load_dwordx4 a[132:135], v26, s[8:11], s93 offen offset:0 sc1
K 193,194,195
Zvaa 132,8,44
Ta 0,6,0
Ta 2,6,1024
Mvaa 132,12,60
K 194,196,197
K 195,198,199
Zvaa 100,12,56
K 200,200,201
K 201,202,203
Mvaa 100,8,40
Ta 4,6,8192
Ta 6,6,9216
Zvaa 68,8,36
buffer_load_dwordx4 a[136:139], v27, s[8:11], s28 offen offset:0 sc1
K 202,204,205
Mvaa 68,12,52
K 203,206,207
K 208,208,209
Zvaa 36,12,48
K 209,210,211
K 210,212,213
Mvaa 36,8,32
W 8
Ta 8,7,0
K 211,214,215
Zvaa 40,16,32
Ta 10,7,1024
K 216,216,217
Mvaa 40,20,48
buffer_load_dwordx4 a[140:143], v27, s[8:11], s93 offen offset:0 sc1
K 217,218,219
Zvaa 72,20,52
s_add_u32 s28, s28, 512
s_cmp_eq_u32 s28, s4
s_cselect_b32 s93, 10240, 0
s_sub_u32 s28, s28, s93
Mvaa 72,16,36
s_add_u32 s93, s28, 20480
K 218,220,221
K 219,222,223
Zvaa 104,16,40
Ta 12,7,8192
Ta 14,7,9216
Mvaa 104,20,56
K 224,224,225
K 225,226,227
Zvaa 136,20,60
K 226,228,229
K 227,230,231
Mvaa 136,16,44
W 8
buffer_load_dwordx4 a[144:147], v26, s[8:11], s28 offen offset:0 sc1
K 232,232,233
Zvaa 140,24,44
K 233,234,235
K 234,236,237
Mvaa 140,28,60
Ta 16,8,0
K 235,238,239
Zvaa 108,28,56
Ta 18,8,1024
K 240,240,241
Mvaa 108,24,40
K 241,242,243
K 242,244,245
Zvaa 76,24,36
buffer_load_dwordx4 a[148:151], v26, s[8:11], s93 offen offset:0 sc1
K 243,246,247
Mvaa 76,28,52
Ta 20,8,8192
Ta 22,8,9216
Zvaa 44,28,48
K 248,248,249
K 249,250,251
Mvaa 44,24,32
K 250,252,253
)"
R"(K 251,254,255
W 8
Zvaa 48,0,32
Ta 24,9,0
Ta 26,9,1024
Mvaa 48,4,48
buffer_load_dwordx4 a[152:155], v27, s[8:11], s28 offen offset:0 sc1
K 160,160,161
Zvaa 80,4,52
Ta 28,9,8192
Ta 30,9,9216
Mvaa 80,0,36
K 161,162,163
Zvaa 112,0,40
K 162,164,165
Mvaa 112,4,56
Zvaa 144,4,60
buffer_load_dwordx4 a[156:159], v27, s[8:11], s93 offen offset:0 sc1
K 163,166,167
Mvaa 144,0,44
W 8
s_add_u32 s28, s28, 512
s_cmp_eq_u32 s28, s4
s_cselect_b32 s93, 10240, 0
Zvaa 148,8,44
s_sub_u32 s28, s28, s93
s_add_u32 s93, s28, 20480
Ta 0,2,16384
Mvaa 148,12,60
s_add_u32 s36, s29, 0
Ta 2,2,17408
Zvaa 116,12,56
Ta 4,2,24576
Ta 6,2,25600
Mvaa 116,8,40
buffer_load_dwordx4 v[188:191], v26, s[8:11], s28 offen offset:0 sc1
Zvaa 84,8,36
Mvaa 84,12,52
Zvaa 52,12,48
Mvaa 52,8,32
W 8
Ta 8,3,16384
Ta 10,3,17408
Zvaa 56,16,32
buffer_load_dwordx4 v[196:199], v26, s[8:11], s93 offen offset:0 sc1
Mvaa 56,20,48
Zvaa 88,20,52
Ta 12,3,24576
Ta 14,3,25600
Mvaa 88,16,36
Zvaa 120,16,40
buffer_store_dwordx4 v[160:163], v1, s[8:11], s36 offen offset:0 sc1
K 168,168,169
Mvaa 120,20,56
K 169,170,171
K 170,172,173
Zvaa 152,20,60
Mvaa 152,16,44
W 8
Ta 16,4,16384
K 171,174,175
Zvaa 156,24,44
Ta 18,4,17408
Mvaa 156,28,60
buffer_load_dwordx4 v[204:207], v27, s[8:11], s28 offen offset:0 sc1
Zvaa 124,28,56
Ta 20,4,24576
Ta 22,4,25600
Mvaa 124,24,40
Zvaa 92,24,36
Mvaa 92,28,52
Zvaa 60,28,48
buffer_load_dwordx4 v[212:215], v27, s[8:11], s93 offen offset:0 sc1
Mvaa 60,24,32
s_waitcnt vmcnt(5) lgkmcnt(8)
s_add_u32 s28, s28, 512
s_cmp_eq_u32 s28, s4
s_cselect_b32 s93, 10240, 0
Mvaa 32,0,128
s_sub_u32 s28, s28, s93
s_add_u32 s93, s28, 20480
Ta 24,5,16384
Mvaa 32,4,144
Ta 26,5,17408
Mvaa 64,4,148
Ta 28,5,24576
Ta 30,5,25600
Mvaa 64,0,132
buffer_store_dwordx4 v[168:171], v1, s[8:11], s36 offen offset:1024 sc1
K 176,176,177
Mvaa 96,0,136
K 177,178,179
K 178,180,181
Mvaa 96,4,152
Mvaa 128,4,156
Mvaa 128,0,140
W 8
Ta 0,6,16384
K 179,182,183
Mvaa 132,8,140
buffer_load_dwordx4 v[220:223], v26, s[8:11], s28 offen offset:0 sc1
Ta 2,6,17408
Mvaa 132,12,156
Mvaa 100,12,152
Ta 4,6,24576
Ta 6,6,25600
Mvaa 100,8,136
Mvaa 68,8,132
Mvaa 68,12,148
buffer_load_dwordx4 v[228:231], v26, s[8:11], s93 offen offset:0 sc1
Mvaa 36,12,144
Mvaa 36,8,128
W 8
Ta 8,7,16384
Ta 10,7,17408
Mvaa 40,16,128
Mvaa 40,20,144
Mvaa 72,20,148
buffer_load_dwordx4 v[236:239], v27, s[8:11], s28 offen offset:0 sc1
Ta 12,7,24576
Mvaa 72,16,132
buffer_store_dwordx4 v[176:179], v1, s[8:11], s36 offen offset:2048 sc1
Ta 14,7,25600
Mvaa 104,16,136
Mvaa 104,20,152
Mvaa 136,20,156
Mvaa 136,16,140
W 8
Ta 16,8,16384
Ta 18,8,17408
Mvaa 140,24,140
buffer_load_dwordx4 v[244:247], v27, s[8:11], s93 offen offset:0 sc1
Mvaa 140,28,156
s_add_u32 s28, s28, 512
s_cmp_eq_u32 s28, s4
s_cselect_b32 s93, 10240, 0
s_sub_u32 s28, s28, s93
Mvaa 108,28,152
s_add_u32 s93, s28, 20480
Ta 20,8,24576
Ta 22,8,25600
Mvaa 108,24,136
Mvaa 76,24,132
Mvaa 76,28,148
buffer_load_dwordx4 v[252:255], v26, s[8:11], s28 offen offset:0 sc1
Mvaa 44,28,144
Mvaa 44,24,128
W 8
Ta 24,9,16384
Ta 26,9,17408
Mvaa 48,0,128
Mvaa 48,4,144
Mvaa 80,4,148
D 22,48,65,2048
Ta 28,9,24576
Mvaa 80,0,132
buffer_store_dwordx4 v[184:187], v1, s[8:11], s36 offen offset:3072 sc1
Ta 30,9,25600
Mvaa 112,0,136
s_add_u32 s36, s29, 4096
s_lshl_b32 s80, s56, 9
Mvaa 112,4,152
Mvaa 144,4,156
s_lshl_b32 s81, s57, 4
Mvaa 144,0,140
W 8
s_add_u32 s80, s80, s81
Ta 0,10,0
Mvaa 148,8,140
buffer_load_dwordx4 a[32:35], v26, s[8:11], s93 offen offset:0 sc1
Ta 2,10,1024
Mvaa 148,12,156
s_and_b32 s81, s14, 1
Mvaa 116,12,152
s_lshl_b32 s81, s81, 3
Ta 4,10,8192
Ta 6,10,9216
Mvaa 116,8,136
Mvaa 84,8,132
s_add_u32 s80, s80, s81
Mvaa 84,12,148
s_add_u32 s71, s80, 4096
buffer_load_dwordx4 a[36:39], v27, s[8:11], s28 offen offset:0 sc1
Mvaa 52,12,144
Mvaa 52,8,128
W 8
Ta 8,11,0
Ta 10,11,1024
Mvaa 56,16,128
Mvaa 56,20,144
Mvaa 88,20,148
buffer_store_dwordx4 v[192:195], v1, s[8:11], s36 offen offset:0 sc1
Ta 12,11,8192
Mvaa 88,16,132
Ta 14,11,9216
Mvaa 120,16,136
Mvaa 120,20,152
Mvaa 152,20,156
Mvaa 152,16,140
W 8
buffer_load_dwordx4 a[40:43], v27, s[8:11], s93 offen offset:0 sc1
Ta 16,12,0
Mvaa 156,24,140
s_add_u32 s28, s28, 512
s_cmp_eq_u32 s28, s4
s_cselect_b32 s93, 10240, 0
s_sub_u32 s28, s28, s93
Mvaa 156,28,156
s_add_u32 s93, s28, 20480
Ta 18,12,1024
Mvaa 124,28,152
Ta 20,12,8192
Ta 22,12,9216
Mvaa 124,24,136
Mvaa 92,24,132
buffer_load_dwordx4 a[44:47], v26, s[8:11], s28 offen offset:0 sc1
Mvaa 92,28,148
Mvaa 60,28,144
Mvaa 60,24,128
s_waitcnt vmcnt(8) lgkmcnt(8)
Ta 24,13,0
Ta 26,13,1024
Mvav 32,0,188
Mvav 32,4,220
D 23,48,65,3072
Mvav 64,4,228
s_add_u32 s65, s5, 16
s_cmp_ge_u32 s65, 20
s_cselect_b32 s90, 20, 0
buffer_store_dwordx4 v[200:203], v1, s[8:11], s36 offen offset:1024 sc1
Mvav 64,0,196
s_sub_u32 s65, s65, s90
s_lshl_b32 s65, s65, 18
s_add_u32 m0, s63, 98304
Mvav 96,0,204
Ta 28,13,8192
Ta 30,13,9216
Mvav 96,4,236
Mvav 128,4,244
Mvav 128,0,212
W 8
buffer_load_dwordx4 a[48:51], v26, s[8:11], s93 offen offset:0 sc1
Ta 0,14,0
Mvav 132,8,212
Ta 2,14,1024
v_add_u32 v2, 65536, v10
Mvav 132,12,244
Mvav 100,12,236
Ta 4,14,8192
Ta 6,14,9216
Mvav 100,8,204
v_add_u32 v3, 65536, v11
Mvav 68,8,196
buffer_load_dwordx4 a[52:55], v27, s[8:11], s28 offen offset:0 sc1
Mvav 68,12,228
Mvav 36,12,220
Mvav 36,8,188
W 8
Ta 8,15,0
Ta 10,15,1024
Mvav 40,16,188
v_add_u32 v4, 65536, v12
Mvav 40,20,220
buffer_load_dwordx4 a[56:59], v27, s[8:11], s93 offen offset:0 sc1
Mvav 72,20,228
s_add_u32 s28, s28, 512
s_cmp_eq_u32 s28, s4
s_cselect_b32 s93, 10240, 0
buffer_store_dwordx4 v[208:211], v1, s[8:11], s36 offen offset:2048 sc1
Mvav 72,16,196
s_sub_u32 s28, s28, s93
s_add_u32 s93, s28, 20480
Ta 12,15,8192
Mvav 104,16,204
Ta 14,15,9216
v_add_u32 v5, 65536, v13
Mvav 104,20,236
Mvav 136,20,244
Mvav 136,16,212
W 8
D 16,48,65,0
Ta 16,24,0
Mvav 140,24,212
Ta 18,24,1024
v_add_u32 v6, 65536, v14
Mvav 140,28,244
Mvav 108,28,236
Ta 20,24,8192
Ta 22,24,9216
Mvav 108,24,204
v_add_u32 v7, 65536, v15
Mvav 76,24,196
buffer_load_dwordx4 a[60:63], v26, s[8:11], s28 offen offset:0 sc1
Mvav 76,28,228
Mvav 44,28,220
Mvav 44,24,188
W 8
Ta 24,25,0
Ta 26,25,1024
Mvav 48,0,188
v_add_u32 v8, 65536, v24
Mvav 48,4,220
buffer_load_dwordx4 v[184:187], v26, s[8:11], s93 offen offset:0 sc1
Mvav 80,4,228
buffer_store_dwordx4 v[216:219], v1, s[8:11], s36 offen offset:3072 sc1
Ta 28,25,8192
Mvav 80,0,196
s_add_u32 s36, s29, 458752
Ta 30,25,9216
v_add_u32 v9, 65536, v25
Mvav 112,0,204
Mvav 112,4,236
Mvav 144,4,244
Mvav 144,0,212
W 8
buffer_load_dwordx4 v[192:195], v27, s[8:11], s28 offen offset:0 sc1
Ta 0,10,16384
Mvav 148,8,212
Ta 2,10,17408
Mvav 148,12,244
Mvav 116,12,236
Ta 4,10,24576
Ta 6,10,25600
Mvav 116,8,204
Mvav 84,8,196
D 17,48,65,1024
Mvav 84,12,228
Mvav 52,12,220
Mvav 52,8,188
W 8
Ta 8,11,16384
Ta 10,11,17408
Mvav 56,16,188
Mvav 56,20,220
buffer_store_dwordx4 v[224:227], v1, s[8:11], s36 offen offset:0 sc1
Mvav 88,20,228
Ta 12,11,24576
Ta 14,11,25600
Mvav 88,16,196
Mvav 120,16,204
Mvav 120,20,236
Mvav 152,20,244
buffer_load_dwordx4 a[128:131], v27, s[8:11], s93 offen offset:0 sc1
Mvav 152,16,212
W 8
s_add_u32 s28, s28, 512
s_cmp_eq_u32 s28, s4
s_cselect_b32 s93, 10240, 0
Mvav 156,24,212
s_sub_u32 s28, s28, s93
s_add_u32 s93, s28, 20480
Ta 16,12,16384
Mvav 156,28,244
Ta 18,12,17408
Mvav 124,28,236
Ta 20,12,24576
Ta 22,12,25600
Mvav 124,24,204
buffer_load_dwordx4 a[132:135], v26, s[8:11], s28 offen offset:0 sc1
Mvav 92,24,196
Mvav 92,28,228
Mvav 60,28,220
Mvav 60,24,188
s_waitcnt vmcnt(10) lgkmcnt(8)
Ta 24,13,16384
Ta 26,13,17408
Mvav 32,0,252
buffer_load_dwordx4 a[136:139], v26, s[8:11], s93 offen offset:0 sc1
Mvaa 32,4,44
buffer_store_dwordx4 v[232:235], v1, s[8:11], s36 offen offset:1024 sc1
Mvaa 64,4,48
Ta 28,13,24576
Ta 30,13,25600
Mvaa 64,0,32
Mvaa 96,0,36
Mvaa 96,4,52
Mvaa 128,4,56
buffer_load_dwordx4 a[140:143], v27, s[8:11], s28 offen offset:0 sc1
Mvaa 128,0,40
W 8
Ta 0,14,16384
Ta 2,14,17408
Mvaa 132,8,40
Mvaa 132,12,56
Mvaa 100,12,52
Ta 4,14,24576
Ta 6,14,25600
Mvaa 100,8,36
D 18,48,65,2048
Mvaa 68,8,32
Mvaa 68,12,48
Mvaa 36,12,44
Mvav 36,8,252
W 8
Ta 8,15,16384
Ta 10,15,17408
Mvav 40,16,252
buffer_load_dwordx4 a[144:147], v27, s[8:11], s93 offen offset:0 sc1
Mvaa 40,20,44
s_add_u32 s28, s28, 512
)"
R"(s_cmp_eq_u32 s28, s4
s_cselect_b32 s93, 10240, 0
buffer_store_dwordx4 v[240:243], v1, s[8:11], s36 offen offset:2048 sc1
Mvaa 72,20,48
s_sub_u32 s28, s28, s93
s_add_u32 s93, s28, 20480
Ta 12,15,24576
Mvaa 72,16,32
Ta 14,15,25600
Mvaa 104,16,36
Mvaa 104,20,52
Mvaa 136,20,56
buffer_load_dwordx4 a[148:151], v26, s[8:11], s28 offen offset:0 sc1
Mvaa 136,16,40
W 8
Ta 16,24,16384
Ta 18,24,17408
Mvaa 140,24,40
Mvaa 140,28,56
Mvaa 108,28,52
Ta 20,24,24576
Ta 22,24,25600
Mvaa 108,24,36
buffer_load_dwordx4 a[152:155], v26, s[8:11], s93 offen offset:0 sc1
Mvaa 76,24,32
Mvaa 76,28,48
Mvaa 44,28,44
Mvav 44,24,252
W 8
Ta 24,25,16384
Ta 26,25,17408
Mvav 48,0,252
buffer_load_dwordx4 a[156:159], v27, s[8:11], s28 offen offset:0 sc1
Mvaa 48,4,44
buffer_store_dwordx4 v[248:251], v1, s[8:11], s36 offen offset:3072 sc1
G 160,160
Mvaa 80,4,48
G 161,161
G 162,162
Mvaa 80,0,32
G 163,163
G 164,164
Mvaa 112,0,36
G 165,165
G 166,166
Mvaa 112,4,52
Ta 28,25,24576
Ta 30,25,25600
Mvaa 144,4,56
D 19,48,65,3072
G 167,167
Mvaa 144,0,40
W 8
s_add_u32 m0, s63, 102400
K 160,160,161
Mvaa 148,8,40
K 161,162,163
K 162,164,165
Mvaa 148,12,56
Ta 0,2,0
K 163,166,167
Mvaa 116,12,52
s_add_u32 s36, s29, 462848
Ta 2,2,1024
Ta 4,2,8192
Mvaa 116,8,36
buffer_load_dwordx4 v[200:203], v27, s[8:11], s93 offen offset:0 sc1
Ta 6,2,9216
Mvaa 84,8,32
s_add_u32 s28, s28, 512
s_cmp_eq_u32 s28, s4
s_cselect_b32 s93, 10240, 0
s_sub_u32 s28, s28, s93
Mvaa 84,12,48
s_add_u32 s93, s28, 20480
Mvaa 52,12,44
Mvav 52,8,252
W 8
Ta 8,3,0
Ta 10,3,1024
Mvav 56,16,252
buffer_store_dwordx4 v[160:163], v1, s[8:11], s36 offen offset:0 sc1
G 168,168
Mvaa 56,20,44
G 169,169
G 170,170
Mvaa 88,20,48
G 171,171
G 172,172
Mvaa 88,16,32
G 173,173
G 174,174
Mvaa 120,16,36
Ta 12,3,8192
Ta 14,3,9216
Mvaa 120,20,52
buffer_load_dwordx4 v[208:211], v26, s[8:11], s28 offen offset:0 sc1
G 175,175
Mvaa 152,20,56
K 168,168,169
K 169,170,171
Mvaa 152,16,40
W 8
K 170,172,173
K 171,174,175
Mvaa 156,24,40
Ta 16,4,0
Ta 18,4,1024
Mvaa 156,28,56
Mvaa 124,28,52
buffer_load_dwordx4 v[216:219], v26, s[8:11], s93 offen offset:0 sc1
Ta 20,4,8192
Mvaa 124,24,36
Ta 22,4,9216
Mvaa 92,24,32
Mvaa 92,28,48
Mvaa 60,28,44
Mvav 60,24,252
s_waitcnt vmcnt(10) lgkmcnt(8)
D 20,48,65,0
Ta 24,5,0
Mvaa 32,0,60
buffer_store_dwordx4 v[168:171], v1, s[8:11], s36 offen offset:1024 sc1
G 176,176
Mvaa 32,4,132
G 177,177
G 178,178
Mvaa 64,4,136
Ta 26,5,1024
G 179,179
Mvav 64,0,184
G 180,180
G 181,181
Mvav 96,0,192
Ta 28,5,8192
G 182,182
Mvaa 96,4,140
buffer_load_dwordx4 v[224:227], v27, s[8:11], s28 offen offset:0 sc1
Ta 30,5,9216
Mvaa 128,4,144
G 183,183
K 176,176,177
Mvaa 128,0,128
W 8
K 177,178,179
K 178,180,181
Mvaa 132,8,128
Ta 0,6,0
K 179,182,183
Mvaa 132,12,144
Ta 2,6,1024
Mvaa 100,12,140
buffer_load_dwordx4 v[188:191], v27, s[8:11], s93 offen offset:0 sc1
Ta 4,6,8192
Mvav 100,8,192
s_add_u32 s28, s28, 512
s_cmp_eq_u32 s28, s4
s_cselect_b32 s93, 10240, 0
s_sub_u32 s28, s28, s93
Mvav 68,8,184
s_add_u32 s93, s28, 20480
Ta 6,6,9216
Mvaa 68,12,136
Mvaa 36,12,132
Mvaa 36,8,60
W 8
buffer_load_dwordx4 v[196:199], v26, s[8:11], s28 offen offset:0 sc1
Ta 8,7,0
Mvaa 40,16,60
buffer_store_dwordx4 v[176:179], v1, s[8:11], s36 offen offset:2048 sc1
G 160,184
Mvaa 40,20,132
G 161,185
G 162,186
Mvaa 72,20,136
Ta 10,7,1024
G 163,187
Mvav 72,16,184
G 164,188
G 165,189
Mvav 104,16,192
Ta 12,7,8192
G 166,190
Mvaa 104,20,140
buffer_load_dwordx4 v[204:207], v26, s[8:11], s93 offen offset:0 sc1
Ta 14,7,9216
Mvaa 136,20,144
G 167,191
K 160,160,161
Mvaa 136,16,128
W 8
K 161,162,163
K 162,164,165
Mvaa 140,24,128
Ta 16,8,0
K 163,166,167
Mvaa 140,28,144
Ta 18,8,1024
Mvaa 108,28,140
D 21,48,65,1024
Ta 20,8,8192
Mvav 108,24,192
Ta 22,8,9216
Mvav 76,24,184
Mvaa 76,28,136
Mvaa 44,28,132
Mvaa 44,24,60
W 8
buffer_load_dwordx4 v[212:215], v27, s[8:11], s28 offen offset:0 sc1
Ta 24,9,0
Mvaa 48,0,60
buffer_store_dwordx4 v[160:163], v1, s[8:11], s36 offen offset:3072 sc1
G 168,192
Mvaa 48,4,132
G 169,193
G 170,194
Mvaa 80,4,136
G 171,195
G 172,196
Mvav 80,0,184
Ta 26,9,1024
G 173,197
Mvav 112,0,192
G 174,198
G 175,199
Mvaa 112,4,140
buffer_load_dwordx4 v[220:223], v27, s[8:11], s93 offen offset:0 sc1
Ta 28,9,8192
Mvaa 144,4,144
s_add_u32 s28, s28, 512
s_cmp_eq_u32 s28, s4
s_cselect_b32 s93, 10240, 0
s_sub_u32 s28, s28, s93
Mvaa 144,0,128
W 7
s_add_u32 s93, s28, 20480
Ta 30,9,9216
Mvaa 148,8,128
K 168,168,169
K 169,170,171
Mvaa 148,12,144
K 170,172,173
K 171,174,175
Mvaa 116,12,140
buffer_load_dwordx4 v[228:231], v26, s[8:11], s28 offen offset:0 sc1
Ta 0,2,16384
Mvav 116,8,192
Ta 2,2,17408
v_accvgpr_write_b32 a192, v168
Mvav 84,8,184
v_accvgpr_write_b32 a193, v169
v_accvgpr_write_b32 a194, v170
Mvaa 84,12,136
Ta 4,2,24576
Ta 6,2,25600
Mvaa 52,12,132
v_accvgpr_write_b32 a195, v171
G 176,64
Mvaa 52,8,60
W 8
D 22,48,65,2048
G 177,65
Mvaa 56,16,60
D 23,48,65,3072
G 178,66
Mvaa 56,20,132
Ta 8,3,16384
Ta 10,3,17408
Mvaa 88,20,136
G 179,67
G 180,68
Mvav 88,16,184
Ta 12,3,24576
Ta 14,3,25600
Mvav 120,16,192
G 181,69
G 182,70
Mvaa 120,20,140
buffer_load_dwordx4 v[236:239], v26, s[8:11], s93 offen offset:0 sc1
G 183,71
Mvaa 152,20,144
v_lshrrev_b32 v20, 4, v1
K 176,176,177
Mvaa 152,16,128
W 8
Ta 16,4,16384
v_add_u32 v20, 0, v20
Mvaa 156,24,128
Ta 18,4,17408
K 177,178,179
Mvaa 156,28,144
v_mul_u32_u24 v22, 205, v20
K 178,180,181
Mvaa 124,28,140
buffer_load_dwordx4 v[232:235], v27, s[8:11], s28 offen offset:0 sc1
K 179,182,183
Mvav 124,24,192
Ta 20,4,24576
Ta 22,4,25600
Mvav 92,24,184
v_lshrrev_b32 v22, 12, v22
v_accvgpr_write_b32 a196, v176
Mvaa 92,28,136
v_mul_u32_u24 v23, 20, v22
v_accvgpr_write_b32 a197, v177
Mvaa 60,28,132
v_accvgpr_write_b32 a198, v178
v_sub_u32 v20, v20, v23
Mvaa 60,24,60
s_waitcnt vmcnt(12) lgkmcnt(8)
buffer_load_dwordx4 a[32:35], v27, s[8:11], s93 offen offset:0 sc1
v_accvgpr_write_b32 a199, v179
Mvaa 32,0,148
s_add_u32 s28, s28, 512
s_cmp_eq_u32 s28, s4
s_cselect_b32 s93, 10240, 0
s_sub_u32 s28, s28, s93
Mvav 32,4,208
s_add_u32 s93, s28, 20480
Ta 24,5,16384
Ta 26,5,17408
Mvav 64,4,216
v_lshrrev_b32 v23, 1, v22
G 168,200
Mvaa 64,0,152
v_and_b32 v23, 2, v23
G 169,201
Mvaa 96,0,156
buffer_load_dwordx4 a[36:39], v26, s[8:11], s28 offen offset:0 sc1
Ta 28,5,24576
Mvav 96,4,224
Ta 30,5,25600
G 170,202
Mvav 128,4,188
v_xor_b32 v20, v20, v23
G 171,203
Mvav 128,0,200
W 8
v_mul_u32_u24 v22, 80, v22
G 172,204
Mvav 132,8,200
v_add_lshl_u32 v16, v20, v22, 4
G 173,205
Mvav 132,12,188
buffer_load_dwordx4 a[40:43], v26, s[8:11], s93 offen offset:0 sc1
Ta 0,6,16384
Mvav 100,12,224
Ta 2,6,17408
G 174,206
Mvaa 100,8,156
v_lshrrev_b32 v20, 4, v1
G 175,207
Mvaa 68,8,152
Ta 4,6,24576
v_add_u32 v20, 64, v20
Mvav 68,12,216
Ta 6,6,25600
K 168,168,169
Mvav 36,12,208
buffer_load_dwordx4 a[44:47], v27, s[8:11], s28 offen offset:0 sc1
K 169,170,171
Mvaa 36,8,148
W 8
v_mul_u32_u24 v22, 205, v20
K 170,172,173
Mvaa 40,16,148
v_lshrrev_b32 v22, 12, v22
K 171,174,175
Mvav 40,20,208
Ta 8,7,16384
v_mul_u32_u24 v23, 20, v22
Mvav 72,20,216
Ta 10,7,17408
v_accvgpr_write_b32 a200, v168
Mvaa 72,16,152
buffer_load_dwordx4 a[48:51], v27, s[8:11], s93 offen offset:0 sc1
v_accvgpr_write_b32 a201, v169
Mvaa 104,16,156
s_add_u32 s28, s28, 512
s_cmp_eq_u32 s28, s4
s_cselect_b32 s93, 10240, 0
s_sub_u32 s28, s28, s93
Mvav 104,20,224
s_add_u32 s93, s28, 20480
v_sub_u32 v20, v20, v23
v_accvgpr_write_b32 a202, v170
Mvav 136,20,188
Ta 12,7,24576
Ta 14,7,25600
Mvav 136,16,200
W 8
v_lshrrev_b32 v23, 1, v22
v_accvgpr_write_b32 a203, v171
Mvav 140,24,200
buffer_load_dwordx4 a[52:55], v26, s[8:11], s28 offen offset:0 sc1
v_and_b32 v23, 2, v23
Mvav 140,28,188
G 176,72
G 177,73
Mvav 108,28,224
)"
R"(v_xor_b32 v20, v20, v23
G 178,74
Mvaa 108,24,156
Ta 16,8,16384
Ta 18,8,17408
Mvaa 76,24,152
v_mul_u32_u24 v22, 80, v22
G 179,75
Mvav 76,28,216
buffer_load_dwordx4 a[56:59], v26, s[8:11], s93 offen offset:0 sc1
G 180,76
Mvav 44,28,208
Ta 20,8,24576
v_add_lshl_u32 v17, v20, v22, 4
Mvaa 44,24,148
W 7
Ta 22,8,25600
G 181,77
Mvaa 48,0,148
v_subrev_u32 v17, 1024, v17
G 182,78
Mvav 48,4,208
v_lshrrev_b32 v20, 4, v1
G 183,79
Mvav 80,4,216
buffer_load_dwordx4 a[184:187], v27, s[8:11], s28 offen offset:0 sc1
K 176,176,177
Mvaa 80,0,152
Ta 24,9,16384
v_add_u32 v20, 128, v20
Mvaa 112,0,156
Ta 26,9,17408
K 177,178,179
Mvav 112,4,224
v_mul_u32_u24 v22, 205, v20
K 178,180,181
Mvav 144,4,188
v_lshrrev_b32 v22, 12, v22
K 179,182,183
Mvav 144,0,200
W 6
buffer_load_dwordx4 a[188:191], v27, s[8:11], s93 offen offset:0 sc1
Ta 28,9,24576
Mvav 148,8,200
s_add_u32 s28, s28, 512
s_cmp_eq_u32 s28, s4
s_cselect_b32 s93, 10240, 0
s_sub_u32 s28, s28, s93
Mvav 148,12,188
s_add_u32 s93, s28, 20480
Ta 30,9,25600
v_accvgpr_write_b32 a204, v176
Mvav 116,12,224
v_mul_u32_u24 v23, 20, v22
v_accvgpr_write_b32 a205, v177
Mvaa 116,8,156
s_waitcnt vmcnt(15)
v_mov_b64 v[28:29], s[12:13]
s_mov_b64 exec, 1
buffer_store_dwordx2 v[28:29], off, s[8:11], s71 sc1
s_mov_b64 exec, -1
v_sub_u32 v20, v20, v23
v_accvgpr_write_b32 a206, v178
Mvaa 84,8,152
s_waitcnt vmcnt(12) lgkmcnt(0)
s_barrier
Mvav 84,12,216
v_accvgpr_write_b32 a207, v179
v_lshrrev_b32 v23, 1, v22
Mvav 52,12,208
G 168,208
v_and_b32 v23, 2, v23
Mvaa 52,8,148
G 169,209
v_xor_b32 v20, v20, v23
Mvaa 56,16,148
buffer_load_dwordx4 a[60:63], v26, s[8:11], s28 offen offset:0 sc1
G 170,210
Mvav 56,20,208
Ta 0,10,32768
G 171,211
Mvav 88,20,216
Ta 2,10,33792
Ta 4,10,40960
Mvaa 88,16,152
Ta 6,10,41984
v_mul_u32_u24 v22, 80, v22
Mvaa 120,16,156
G 172,212
v_add_lshl_u32 v18, v20, v22, 4
Mvav 120,20,224
buffer_load_dwordx4 v[184:187], v26, s[8:11], s93 offen offset:0 sc1
G 173,213
Mvav 152,20,188
Ta 8,11,32768
Ta 10,11,33792
Mvav 152,16,200
G 174,214
v_subrev_u32 v18, 2048, v18
Mvav 156,24,200
G 175,215
v_lshrrev_b32 v20, 4, v1
Mvav 156,28,188
Ta 12,11,40960
Ta 14,11,41984
Mvav 124,28,224
buffer_load_dwordx4 v[192:195], v27, s[8:11], s28 offen offset:0 sc1
K 168,168,169
Mvaa 124,24,156
v_add_u32 v20, 192, v20
K 169,170,171
Mvaa 92,24,152
K 170,172,173
v_mul_u32_u24 v22, 205, v20
Mvav 92,28,216
K 171,174,175
v_lshrrev_b32 v22, 12, v22
Mvav 60,28,208
Ta 16,12,32768
Ta 18,12,33792
Mvaa 60,24,148
s_waitcnt vmcnt(12) lgkmcnt(6)
buffer_load_dwordx4 a[128:131], v27, s[8:11], s93 offen offset:0 sc1
v_accvgpr_write_b32 a208, v168
Mvav 32,0,196
s_add_u32 s28, s28, 512
v_mul_u32_u24 v23, 20, v22
v_accvgpr_write_b32 a209, v169
Mvav 32,4,228
s_cmp_eq_u32 s28, s4
s_cselect_b32 s93, 10240, 0
v_accvgpr_write_b32 a210, v170
v_sub_u32 v20, v20, v23
Mvav 64,4,236
s_sub_u32 s28, s28, s93
Ta 20,12,40960
Ta 22,12,41984
Mvav 64,0,204
s_add_u32 s93, s28, 20480
v_accvgpr_write_b32 a211, v171
v_lshrrev_b32 v23, 1, v22
Mvav 96,0,212
G 176,80
G 177,81
Mvav 96,4,232
v_and_b32 v23, 2, v23
G 178,82
Mvaa 128,4,32
Ta 24,13,32768
v_xor_b32 v20, v20, v23
Mvav 128,0,220
W 5
Ta 26,13,33792
G 179,83
Mvav 132,8,220
buffer_load_dwordx4 a[132:135], v26, s[8:11], s28 offen offset:0 sc1
v_mul_u32_u24 v22, 80, v22
Mvaa 132,12,32
Ta 28,13,40960
G 180,84
Mvav 100,12,232
Ta 30,13,41984
G 181,85
Mvav 100,8,212
v_add_lshl_u32 v19, v20, v22, 4
G 182,86
Mvav 68,8,204
v_subrev_u32 v19, 3072, v19
G 183,87
Mvav 68,12,236
v_lshrrev_b32 v20, 4, v1
K 176,176,177
Mvav 36,12,228
Ta 0,14,32768
Ta 2,14,33792
Mvav 36,8,196
W 6
K 177,178,179
v_add_u32 v20, 256, v20
Mvav 40,16,196
buffer_load_dwordx4 a[136:139], v26, s[8:11], s93 offen offset:0 sc1
K 178,180,181
Mvav 40,20,228
v_mul_u32_u24 v22, 205, v20
K 179,182,183
Mvav 72,20,236
Ta 4,14,40960
Ta 6,14,41984
Mvav 72,16,204
v_accvgpr_write_b32 a212, v176
v_lshrrev_b32 v22, 12, v22
Mvav 104,16,212
v_accvgpr_write_b32 a213, v177
v_mul_u32_u24 v23, 20, v22
Mvav 104,20,232
v_accvgpr_write_b32 a214, v178
v_sub_u32 v20, v20, v23
Mvaa 136,20,32
Ta 8,15,32768
v_accvgpr_write_b32 a215, v179
Mvav 136,16,220
W 5
Ta 10,15,33792
buffer_load_dwordx4 a[140:143], v27, s[8:11], s28 offen offset:0 sc1
Mvav 140,24,220
G 168,216
v_lshrrev_b32 v23, 1, v22
Mvaa 140,28,32
G 169,217
v_and_b32 v23, 2, v23
Mvav 108,28,232
Ta 12,15,40960
Ta 14,15,41984
Mvav 108,24,212
G 170,218
v_xor_b32 v20, v20, v23
Mvav 76,24,204
G 171,219
G 172,220
Mvav 76,28,236
v_mul_u32_u24 v22, 80, v22
G 173,221
Mvav 44,28,228
Ta 16,24,32768
Ta 18,24,33792
Mvav 44,24,196
W 6
v_add_lshl_u32 v20, v20, v22, 4
G 174,222
Mvav 48,0,196
buffer_load_dwordx4 a[144:147], v27, s[8:11], s93 offen offset:0 sc1
G 175,223
Mvav 48,4,228
s_add_u32 s28, s28, 512
Ta 20,24,40960
v_lshrrev_b32 v22, 6, v1
Mvav 80,4,236
s_cmp_eq_u32 s28, s4
s_cselect_b32 s93, 10240, 0
Ta 22,24,41984
K 168,168,169
Mvav 80,0,204
s_sub_u32 s28, s28, s93
v_mul_u32_u24 v22, 320, v22
K 169,170,171
Mvav 112,0,212
s_add_u32 s93, s28, 20480
v_bfe_u32 v23, v1, 4, 2
K 170,172,173
Mvav 112,4,232
K 171,174,175
v_lshl_add_u32 v22, v23, 3, v22
Mvaa 144,4,32
Ta 24,25,32768
Ta 26,25,33792
Mvav 144,0,220
W 6
v_accvgpr_write_b32 a216, v168
v_bfe_u32 v23, v1, 8, 1
Mvav 148,8,220
buffer_load_dwordx4 v[200:203], v26, s[8:11], s28 offen offset:0 sc1
v_accvgpr_write_b32 a217, v169
Mvaa 148,12,32
v_lshlrev_b32 v23, 5, v23
v_accvgpr_write_b32 a218, v170
Mvav 116,12,232
Ta 28,25,40960
Ta 30,25,41984
Mvav 116,8,212
v_accvgpr_write_b32 a219, v171
v_add_u32 v21, v22, v23
Mvav 84,8,204
G 176,88
v_sub_u32 v22, v22, v23
Mvav 84,12,236
s_mul_i32 s94, s59, 655360
G 177,89
G 178,90
Mvav 52,12,228
s_mul_i32 s95, s58, 320
Ta 0,10,49152
Ta 2,10,50176
Mvav 52,8,196
W 6
buffer_load_dwordx4 v[208:211], v26, s[8:11], s93 offen offset:0 sc1
G 179,91
Mvav 56,16,196
s_add_u32 s94, s94, s95
Ta 4,10,57344
G 180,92
Mvav 56,20,228
Ta 6,10,58368
G 181,93
Mvav 88,20,236
s_add_u32 s40, s26, s94
s_addc_u32 s41, s27, 0
G 182,94
Mvav 88,16,204
s_and_b32 s41, s41, 0xffff
G 183,95
K 176,176,177
Mvav 120,16,212
s_mov_b32 s42, 5242880
Ta 8,11,49152
Ta 10,11,50176
Mvav 120,20,232
s_mov_b32 s43, 0x20000
buffer_load_dwordx4 v[216:219], v27, s[8:11], s28 offen offset:0 sc1
K 177,178,179
Mvaa 152,20,32
s_sub_u32 s42, s42, s94
K 178,180,181
K 179,182,183
Mvav 152,16,220
W 6
Ta 12,11,57344
Ta 14,11,58368
Mvav 156,24,220
s_mov_b64 s[44:45], s[8:9]
v_accvgpr_write_b32 a220, v176
v_accvgpr_write_b32 a221, v177
Mvaa 156,28,32
s_mov_b32 s47, 0x20000
v_accvgpr_write_b32 a222, v178
v_accvgpr_write_b32 a223, v179
Mvav 124,28,232
s_and_b32 s48, s14, 1
Ta 16,12,49152
Ta 18,12,50176
Mvav 124,24,212
buffer_load_dwordx4 v[224:227], v27, s[8:11], s93 offen offset:0 sc1
G 168,224
Mvav 92,24,204
s_mul_i32 s48, s48, 5120
Ta 20,12,57344
G 169,225
Mvav 92,28,236
s_lshr_b32 s51, s14, 1
Ta 22,12,58368
G 170,226
Mvav 60,28,228
G 171,227
G 172,228
Mvav 60,24,196
s_waitcnt vmcnt(13) lgkmcnt(8)
s_and_b32 s94, s14, 1
G 173,229
Mvaa 32,0,36
s_add_u32 s28, s28, 512
Ta 24,13,49152
Ta 26,13,50176
Mvaa 32,4,52
s_cmp_eq_u32 s28, s4
s_cselect_b32 s93, 10240, 0
s_sub_u32 s28, s28, s93
G 174,230
Mvaa 64,4,56
s_add_u32 s93, s28, 20480
s_lshl_b32 s95, s57, 1
buffer_load_dwordx4 v[188:191], v26, s[8:11], s28 offen offset:0 sc1
Mvaa 64,0,40
s_add_u32 s94, s94, s95
Ta 28,13,57344
G 175,231
Mvaa 96,0,44
s_lshl_b32 s54, s94, 3
Ta 30,13,58368
K 168,168,169
Mvaa 96,4,184
K 169,170,171
K 170,172,173
Mvaa 128,4,188
s_add_u32 s54, s54, s59
K 171,174,175
Mvaa 128,0,48
W 8
Ta 0,14,49152
Ta 2,14,50176
Mvaa 132,8,48
s_lshl_b32 s54, s54, 6
buffer_load_dwordx4 v[196:199], v26, s[8:11], s93 offen offset:0 sc1
v_accvgpr_write_b32 a224, v168
Mvaa 132,12,188
s_xor_b32 s55, s54, 15872
v_accvgpr_write_b32 a225, v169
Mvaa 100,12,184
Ta 4,14,57344
v_accvgpr_write_b32 a226, v170
Mvaa 100,8,44
s_add_u32 s54, s54, 20480
Ta 6,14,58368
v_accvgpr_write_b32 a227, v171
Mvaa 68,8,40
s_and_b32 s81, s14, 1
G 176,96
Mvaa 68,12,56
s_add_u32 s55, s55, 20480
Mvaa 36,12,52
G 177,97
Mvaa 36,8,36
)"
R"(W 8
s_and_b32 s95, s14, 2
G 178,98
Mvaa 40,16,36
s_mul_i32 s91, s81, 17164288
Ta 8,15,49152
Ta 10,15,50176
Mvaa 40,20,52
s_lshl_b32 s94, s94, 2
buffer_load_dwordx4 v[204:207], v27, s[8:11], s28 offen offset:0 sc1
G 179,99
Mvaa 72,20,56
s_mul_i32 s82, s56, 536576
s_add_u32 s94, s94, s95
G 180,100
Mvaa 72,16,40
Ta 12,15,57344
Ta 14,15,58368
Mvaa 104,16,44
s_lshl_b32 s95, s95, 4
G 181,101
G 182,102
Mvaa 104,20,184
s_add_u32 s91, s91, s82
G 183,103
Mvaa 136,20,188
s_add_u32 s55, s55, s95
s_add_u32 s91, s91, 4194304
K 176,176,177
Mvaa 136,16,48
W 8
s_lshl_b32 s49, s94, 17
Ta 16,24,49152
Mvaa 140,24,48
Ta 18,24,50176
K 177,178,179
Mvaa 140,28,188
s_lshl_b32 s95, s59, 14
buffer_load_dwordx4 v[212:215], v27, s[8:11], s93 offen offset:0 sc1
K 178,180,181
Mvaa 108,28,184
s_lshr_b32 s82, s14, 1
Ta 20,24,57344
K 179,182,183
Mvaa 108,24,44
s_add_u32 s49, s49, s95
Ta 22,24,58368
v_accvgpr_write_b32 a228, v176
Mvaa 76,24,40
s_lshl_b32 s83, s82, 14
s_xor_b32 s50, s49, 16252928
Mvaa 76,28,56
v_accvgpr_write_b32 a229, v177
Mvaa 44,28,52
s_add_u32 s91, s91, s83
s_add_u32 s49, s49, 39845888
v_accvgpr_write_b32 a230, v178
Mvaa 44,24,36
W 8
Ta 24,25,49152
v_accvgpr_write_b32 a231, v179
Mvaa 48,0,36
s_add_u32 s50, s50, 39845888
Ta 26,25,50176
G 168,232
Mvaa 48,4,52
s_lshl_b32 s81, s81, 14
Mvaa 80,4,56
s_lshr_b32 s94, s15, 3
Ta 28,25,57344
G 169,233
Mvaa 80,0,40
s_lshl_b32 s82, s82, 16
Ta 30,25,58368
G 170,234
Mvaa 112,0,44
s_lshl_b32 s94, s94, 3
G 171,235
Mvaa 112,4,184
s_add_u32 s94, s94, s14
s_or_b32 s92, s81, s82
G 172,236
Mvaa 144,4,188
Mvaa 144,0,48
W 8
s_lshl_b32 s94, s94, 4
G 173,237
Mvaa 148,8,48
s_xor_b32 s92, s92, 0x10000
Ta 0,2,32768
Ta 2,2,33792
Mvaa 148,12,188
s_add_u32 s94, s94, s59
G 174,238
G 175,239
Mvaa 116,12,184
s_mul_i32 s52, s94, 5120
Ta 4,2,40960
Ta 6,2,41984
Mvaa 116,8,44
K 168,168,169
Mvaa 84,8,40
s_lshl_b32 s95, s59, 12
K 169,170,171
Mvaa 84,12,56
K 170,172,173
Mvaa 52,12,52
s_sub_u32 s52, s52, s95
K 171,174,175
Mvaa 52,8,36
W 8
s_add_u32 s52, s52, 56623104
Ta 8,3,32768
Mvaa 56,16,36
Ta 10,3,33792
v_accvgpr_write_b32 a232, v168
Mvaa 56,20,52
s_lshl_b32 s53, s94, 3
v_accvgpr_write_b32 a233, v169
Mvaa 88,20,56
Ta 12,3,40960
v_accvgpr_write_b32 a234, v170
Mvaa 88,16,40
s_add_u32 s53, s53, 65536
Ta 14,3,41984
v_accvgpr_write_b32 a235, v171
Mvaa 120,16,44
G 176,104
v_bfe_u32 v244, v1, 4, 1
Mvaa 120,20,184
Mvaa 152,20,188
G 177,105
v_mul_u32_u24 v244, 0xaaaa, v244
Mvaa 152,16,48
W 8
G 178,106
v_add_u32 v244, 0x4444, v244
Mvaa 156,24,48
Ta 16,4,32768
Ta 18,4,33792
Mvaa 156,28,188
G 179,107
v_lshl_or_b32 v240, v244, 16, v244
Mvaa 124,28,184
G 180,108
v_mov_b32 v241, 0xff800000
Mvaa 124,24,44
Ta 20,4,40960
Ta 22,4,41984
Mvaa 92,24,40
G 181,109
v_add_u32 v242, 0x10000, v1
Mvaa 92,28,56
G 182,110
v_bfe_u32 v244, v1, 4, 4
Mvaa 60,28,52
G 183,111
v_lshrrev_b32 v245, 8, v1
Mvaa 60,24,36
s_waitcnt vmcnt(8) lgkmcnt(8)
K 176,176,177
v_lshlrev_b32 v245, 2, v245
Mvaa 32,0,60
Ta 24,5,32768
Ta 26,5,33792
Mvaa 32,4,132
K 177,178,179
v_sub_u32 v244, v244, v245
Mvaa 64,4,136
K 178,180,181
v_cmp_le_i32_e64 s[72:73], 0, v244
Mvav 64,0,184
Ta 28,5,40960
K 179,182,183
Mvav 96,0,192
Ta 30,5,41984
v_accvgpr_write_b32 a236, v176
Mvaa 96,4,140
v_cmp_le_i32_e64 s[74:75], 1, v244
v_accvgpr_write_b32 a237, v177
Mvaa 128,4,144
v_cmp_le_i32_e64 s[76:77], 2, v244
v_accvgpr_write_b32 a238, v178
Mvaa 128,0,128
W 8
v_cmp_le_i32_e64 s[78:79], 3, v244
v_accvgpr_write_b32 a239, v179
Mvaa 132,8,128
Ta 0,6,32768
Ta 2,6,33792
Mvaa 132,12,144
v_add_u32 v243, 4096, v1
G 168,240
Mvaa 100,12,140
v_add_u32 v244, 8192, v1
G 169,241
Mvav 100,8,192
Ta 4,6,40960
v_add_u32 v245, 12288, v1
Mvav 68,8,184
s_mov_b32 s18, 0x3f803f80
Ta 6,6,41984
G 170,242
Mvaa 68,12,136
s_mov_b32 s19, 0x3f003f00
G 171,243
G 172,244
Mvaa 36,12,132
Mvaa 36,8,60
W 8
v_accvgpr_write_b32 a176, s18
G 173,245
Mvaa 40,16,60
Ta 8,7,32768
Ta 10,7,33792
Mvaa 40,20,132
v_accvgpr_write_b32 a180, s19
G 174,246
Mvaa 72,20,136
G 175,247
v_mov_b32 v252, v241
Mvav 72,16,184
Ta 12,7,40960
Ta 14,7,41984
Mvav 104,16,192
K 168,168,169
v_accvgpr_write_b32 a177, s18
Mvaa 104,20,140
K 169,170,171
v_accvgpr_write_b32 a181, s19
Mvaa 136,20,144
K 170,172,173
v_mov_b32 v253, v241
Mvaa 136,16,128
W 8
K 171,174,175
v_accvgpr_write_b32 a178, s18
Mvaa 140,24,128
Ta 16,8,32768
Ta 18,8,33792
Mvaa 140,28,144
v_accvgpr_write_b32 a240, v168
v_accvgpr_write_b32 a182, s19
Mvaa 108,28,140
v_accvgpr_write_b32 a241, v169
v_mov_b32 v254, v241
Mvav 108,24,192
Ta 20,8,40960
v_accvgpr_write_b32 a242, v170
Mvav 76,24,184
Ta 22,8,41984
v_accvgpr_write_b32 a179, s18
Mvaa 76,28,136
v_accvgpr_write_b32 a243, v171
v_accvgpr_write_b32 a183, s19
Mvaa 44,28,132
G 176,112
v_mov_b32 v255, v241
Mvaa 44,24,60
W 8
G 177,113
G 178,114
Mvaa 48,0,60
Ta 24,9,32768
v_accvgpr_write_b32 a160, 0
Mvaa 48,4,132
Ta 26,9,33792
G 179,115
Mvaa 80,4,136
v_accvgpr_write_b32 a161, 0
G 180,116
Mvav 80,0,184
Ta 28,9,40960
v_accvgpr_write_b32 a162, 0
Mvav 112,0,192
Ta 30,9,41984
G 181,117
Mvaa 112,4,140
v_accvgpr_write_b32 a163, 0
G 182,118
Mvaa 144,4,144
v_accvgpr_write_b32 a164, 0
G 183,119
Mvaa 144,0,128
W 8
v_accvgpr_write_b32 a165, 0
K 176,176,177
Mvaa 148,8,128
Ta 0,2,49152
v_accvgpr_write_b32 a166, 0
Mvaa 148,12,144
Ta 2,2,50176
K 177,178,179
Mvaa 116,12,140
v_accvgpr_write_b32 a167, 0
K 178,180,181
Mvav 116,8,192
v_accvgpr_write_b32 a168, 0
K 179,182,183
Mvav 84,8,184
Ta 4,2,57344
Ta 6,2,58368
Mvaa 84,12,136
v_accvgpr_write_b32 a169, 0
v_accvgpr_write_b32 a244, v176
Mvaa 52,12,132
v_accvgpr_write_b32 a245, v177
v_accvgpr_write_b32 a170, 0
Mvaa 52,8,60
W 8
v_accvgpr_write_b32 a246, v178
v_accvgpr_write_b32 a171, 0
Mvaa 56,16,60
Ta 8,3,49152
v_accvgpr_write_b32 a247, v179
Mvaa 56,20,132
Ta 10,3,50176
v_accvgpr_write_b32 a172, 0
Mvaa 88,20,136
G 168,248
v_accvgpr_write_b32 a173, 0
Mvav 88,16,184
G 169,249
v_accvgpr_write_b32 a174, 0
Mvav 120,16,192
Ta 12,3,57344
Ta 14,3,58368
Mvaa 120,20,140
G 170,250
v_accvgpr_write_b32 a175, 0
Mvaa 152,20,144
s_mov_b32 s90, 0x5d800000
G 171,251
G 172,252
Mvaa 152,16,128
W 8
s_mov_b64 s[88:89], 0
G 173,253
Mvaa 156,24,128
s_mov_b32 s64, 0
Ta 16,4,49152
Ta 18,4,50176
Mvaa 156,28,144
s_and_b32 s18, s14, 1
G 174,254
G 175,255
Mvaa 124,28,140
s_lshl_b32 s62, s18, 14
Ta 20,4,57344
Ta 22,4,58368
Mvav 124,24,192
K 168,168,169
Mvav 92,24,184
s_lshl_b32 s20, s57, 15
K 169,170,171
Mvaa 92,28,136
s_sub_u32 s20, 507904, s20
K 170,172,173
Mvaa 60,28,132
K 171,174,175
Mvaa 60,24,60
s_waitcnt vmcnt(0) lgkmcnt(8)
s_add_u32 s20, s20, s62
Ta 24,5,49152
Mvav 32,0,200
s_lshl_b32 s65, s57, 1
Ta 26,5,50176
v_accvgpr_write_b32 a248, v168
Mvav 32,4,188
s_sub_u32 s65, s65, 1
v_accvgpr_write_b32 a249, v169
Mvav 64,4,196
Ta 28,5,57344
v_accvgpr_write_b32 a250, v170
Mvav 64,0,208
s_max_i32 s65, s65, 0
Ta 30,5,58368
v_accvgpr_write_b32 a251, v171
Mvav 96,0,216
s_sub_u32 s66, s60, 1
G 176,120
Mvav 96,4,204
s_cmp_lt_u32 s14, 2
s_cselect_b32 s20, s20, 0
s_cselect_b32 s66, s66, 15
s_cselect_b32 s21, 32768, 0
Mvav 128,4,212
G 177,121
Mvav 128,0,224
W 8
s_add_u32 s61, s91, s20
G 178,122
Mvav 132,8,224
s_add_u32 s62, s62, s21
Ta 0,6,49152
Ta 2,6,50176
Mvav 132,12,212
G 179,123
G 180,124
Mvav 100,12,204
)"
R"(s_sub_u32 s63, 16384, s21
Ta 4,6,57344
Ta 6,6,58368
Mvav 100,8,216
s_lshl_b32 s67, s60, 7
G 181,125
Mvav 68,8,208
s_add_u32 s67, s67, s56
G 182,126
Mvav 68,12,196
G 183,127
Mvav 36,12,188
s_lshl_b32 s67, s67, 12
K 176,176,177
Mvav 36,8,200
W 8
s_add_u32 s67, s67, 39845888
Ta 8,7,49152
Mvav 40,16,200
s_lshl_b32 s69, s60, 3
Ta 10,7,50176
K 177,178,179
Mvav 40,20,188
K 178,180,181
Mvav 72,20,196
s_add_u32 s69, s69, s59
Ta 12,7,57344
K 179,182,183
Mvav 72,16,208
s_lshl_b32 s69, s69, 6
Ta 14,7,58368
v_accvgpr_write_b32 a252, v176
Mvav 104,16,216
Mvav 104,20,204
s_lshl_b32 s21, s58, 3
v_accvgpr_write_b32 a253, v177
Mvav 136,20,212
s_add_u32 s69, s69, s21
v_accvgpr_write_b32 a254, v178
Mvav 136,16,224
W 8
s_add_u32 s69, s69, 20480
v_accvgpr_write_b32 a255, v179
Mvav 140,24,224
Ta 16,8,49152
Ta 18,8,50176
Mvav 140,28,212
s_mov_b32 s70, 0
Mvav 108,28,204
s_mov_b32 s37, 0
Ta 20,8,57344
Ta 22,8,58368
Mvav 108,24,216
s_sub_u32 s18, s57, 4
Mvav 76,24,208
Mvav 76,28,196
s_lshl_b32 s18, s18, 2
Mvav 44,28,188
s_add_u32 s18, s18, s58
Mvav 44,24,200
W 8
Ta 24,9,49152
Ta 26,9,50176
Mvav 48,0,200
s_mul_i32 s85, s59, 7
Mvav 48,4,188
s_add_u32 s85, s85, s18
Mvav 80,4,196
s_cmp_lt_u32 s18, 7
s_cselect_b32 s85, s85, 99
Ta 28,9,57344
Ta 30,9,58368
Mvav 80,0,208
Mvav 112,0,216
s_cmp_lt_u32 s85, 54
s_cselect_b32 s65, 2, s65
Mvav 112,4,204
s_mov_b32 s68, 0
Mvav 144,4,212
s_mov_b64 s[24:25], -1
Mvav 144,0,224
W 8
Mvav 148,8,224
s_mov_b32 s22, 0x3e0293ee
Mvav 148,12,212
s_mov_b32 s23, 0x3e0293ee
Mvav 116,12,204
Mvav 116,8,216
Mvav 84,8,208
Mvav 84,12,196
Mvav 52,12,188
Mvav 52,8,200
W 4
Mvav 56,16,200
Mvav 56,20,188
Mvav 88,20,196
Mvav 88,16,208
Mvav 120,16,216
W 0
s_barrier
Mvav 120,20,204
Ra 128,242,0
Mvav 152,20,212
Ra 132,242,1024
Mvav 152,16,224
Mvav 156,24,224
Ra 136,242,2048
Mvav 156,28,212
Mvav 124,28,204
Ra 140,242,3072
Mvav 124,24,216
Mvav 92,24,208
Mvav 92,28,196
Mvav 60,28,188
Mvav 60,24,200
.Lago%=:
s_cmp_eq_u32 s60, 1
s_cbranch_scc1 .Lpe_44%=
s_cmp_eq_u32 s60, 0
s_cbranch_scc1 .Lpd_28%=
v_pk_mul_f32 v[32:33], v[32:33], s[22:23]
v_pk_mul_f32 v[34:35], v[34:35], s[22:23]
v_pk_mul_f32 v[36:37], v[36:37], s[22:23]
v_pk_mul_f32 v[38:39], v[38:39], s[22:23]
v_pk_mul_f32 v[64:65], v[64:65], s[22:23]
v_pk_mul_f32 v[66:67], v[66:67], s[22:23]
v_pk_mul_f32 v[68:69], v[68:69], s[22:23]
v_pk_mul_f32 v[70:71], v[70:71], s[22:23]
v_pk_mul_f32 v[96:97], v[96:97], s[22:23]
v_pk_mul_f32 v[98:99], v[98:99], s[22:23]
v_pk_mul_f32 v[100:101], v[100:101], s[22:23]
v_pk_mul_f32 v[102:103], v[102:103], s[22:23]
v_pk_mul_f32 v[128:129], v[128:129], s[22:23]
v_pk_mul_f32 v[130:131], v[130:131], s[22:23]
v_pk_mul_f32 v[132:133], v[132:133], s[22:23]
v_pk_mul_f32 v[134:135], v[134:135], s[22:23]
v_pk_mul_f32 v[40:41], v[40:41], s[22:23]
v_pk_mul_f32 v[42:43], v[42:43], s[22:23]
v_pk_mul_f32 v[44:45], v[44:45], s[22:23]
v_pk_mul_f32 v[46:47], v[46:47], s[22:23]
v_pk_mul_f32 v[72:73], v[72:73], s[22:23]
v_pk_mul_f32 v[74:75], v[74:75], s[22:23]
v_pk_mul_f32 v[76:77], v[76:77], s[22:23]
v_pk_mul_f32 v[78:79], v[78:79], s[22:23]
v_pk_mul_f32 v[104:105], v[104:105], s[22:23]
v_pk_mul_f32 v[106:107], v[106:107], s[22:23]
v_pk_mul_f32 v[108:109], v[108:109], s[22:23]
v_pk_mul_f32 v[110:111], v[110:111], s[22:23]
v_pk_mul_f32 v[136:137], v[136:137], s[22:23]
v_pk_mul_f32 v[138:139], v[138:139], s[22:23]
v_pk_mul_f32 v[140:141], v[140:141], s[22:23]
v_pk_mul_f32 v[142:143], v[142:143], s[22:23]
v_pk_mul_f32 v[48:49], v[48:49], s[22:23]
v_pk_mul_f32 v[50:51], v[50:51], s[22:23]
v_pk_mul_f32 v[52:53], v[52:53], s[22:23]
v_pk_mul_f32 v[54:55], v[54:55], s[22:23]
v_pk_mul_f32 v[80:81], v[80:81], s[22:23]
v_pk_mul_f32 v[82:83], v[82:83], s[22:23]
v_pk_mul_f32 v[84:85], v[84:85], s[22:23]
v_pk_mul_f32 v[86:87], v[86:87], s[22:23]
v_pk_mul_f32 v[112:113], v[112:113], s[22:23]
v_pk_mul_f32 v[114:115], v[114:115], s[22:23]
v_pk_mul_f32 v[116:117], v[116:117], s[22:23]
v_pk_mul_f32 v[118:119], v[118:119], s[22:23]
v_pk_mul_f32 v[144:145], v[144:145], s[22:23]
v_pk_mul_f32 v[146:147], v[146:147], s[22:23]
v_pk_mul_f32 v[148:149], v[148:149], s[22:23]
v_pk_mul_f32 v[150:151], v[150:151], s[22:23]
v_pk_mul_f32 v[56:57], v[56:57], s[22:23]
v_pk_mul_f32 v[58:59], v[58:59], s[22:23]
v_pk_mul_f32 v[60:61], v[60:61], s[22:23]
v_pk_mul_f32 v[62:63], v[62:63], s[22:23]
v_pk_mul_f32 v[88:89], v[88:89], s[22:23]
v_pk_mul_f32 v[90:91], v[90:91], s[22:23]
v_pk_mul_f32 v[92:93], v[92:93], s[22:23]
v_pk_mul_f32 v[94:95], v[94:95], s[22:23]
v_pk_mul_f32 v[120:121], v[120:121], s[22:23]
v_pk_mul_f32 v[122:123], v[122:123], s[22:23]
v_pk_mul_f32 v[124:125], v[124:125], s[22:23]
v_pk_mul_f32 v[126:127], v[126:127], s[22:23]
v_pk_mul_f32 v[152:153], v[152:153], s[22:23]
v_pk_mul_f32 v[154:155], v[154:155], s[22:23]
v_pk_mul_f32 v[156:157], v[156:157], s[22:23]
v_pk_mul_f32 v[158:159], v[158:159], s[22:23]
K 32,32,33
K 33,34,35
K 34,36,37
K 35,38,39
K 40,40,41
K 41,42,43
K 42,44,45
K 43,46,47
W 0
Zvav 196,128,32
s_add_u32 s36, s29, 65536
Ra 144,242,4096
Ra 148,242,5120
Ra 152,242,6144
Ra 156,242,7168
Mvav 196,132,40
K 64,64,65
K 65,66,67
K 66,68,69
K 67,70,71
K 72,72,73
K 73,74,75
K 74,76,77
K 75,78,79
s_nop 1
Zvav 204,132,72
buffer_store_dwordx4 a[192:195], v1, s[8:11], s36 offen offset:0 sc1
K 96,96,97
K 97,98,99
K 98,100,101
Mvav 204,128,64
K 99,102,103
K 104,104,105
K 105,106,107
K 106,108,109
K 107,110,111
Zvav 212,128,96
buffer_store_dwordx4 a[200:203], v1, s[8:11], s36 offen offset:1024 sc1
K 128,128,129
K 129,130,131
K 130,132,133
Mvav 212,132,104
K 131,134,135
K 136,136,137
K 137,138,139
K 138,140,141
K 139,142,143
s_nop 1
Zvav 220,132,136
K 48,48,49
K 49,50,51
K 50,52,53
K 51,54,55
Mvav 220,128,128
K 56,56,57
K 57,58,59
K 58,60,61
K 59,62,63
Mvav 196,136,48
buffer_store_dwordx4 a[208:211], v1, s[8:11], s36 offen offset:2048 sc1
Ra 128,242,8192
Ra 132,242,9216
K 80,80,81
Mvav 196,140,56
K 81,82,83
K 82,84,85
K 83,86,87
K 88,88,89
K 89,90,91
K 90,92,93
K 91,94,95
s_nop 1
Mvav 204,140,88
buffer_store_dwordx4 a[216:219], v1, s[8:11], s36 offen offset:3072 sc1
s_add_u32 s36, s29, 69632
K 112,112,113
K 113,114,115
K 114,116,117
Mvav 204,136,80
K 115,118,119
K 120,120,121
K 121,122,123
K 122,124,125
K 123,126,127
Mvav 212,136,112
K 144,144,145
K 145,146,147
K 146,148,149
K 147,150,151
Mvav 212,140,120
K 152,152,153
K 153,154,155
K 154,156,157
K 155,158,159
s_nop 1
Mvav 220,140,152
buffer_store_dwordx4 a[224:227], v1, s[8:11], s36 offen offset:0 sc1
v_accvgpr_write_b32 a0, v32
v_accvgpr_write_b32 a1, v33
v_accvgpr_write_b32 a2, v34
Mvav 220,136,144
W 2
Ra 136,242,10240
v_accvgpr_write_b32 a3, v35
v_accvgpr_write_b32 a4, v40
Zvav 36,144,32
Ra 140,242,11264
buffer_store_dwordx4 a[232:235], v1, s[8:11], s36 offen offset:1024 sc1
v_accvgpr_write_b32 a5, v41
Mvav 36,148,40
v_accvgpr_write_b32 a6, v42
v_accvgpr_write_b32 a7, v43
v_max3_f32 v4, v196, v197, v198
v_max3_f32 v4, v4, v199, v252
Zvav 68,148,72
v_max3_f32 v5, v204, v205, v206
v_max3_f32 v5, v5, v207, v253
v_max3_f32 v6, v212, v213, v214
v_max3_f32 v6, v6, v215, v254
Mvav 68,144,64
v_max3_f32 v7, v220, v221, v222
v_max3_f32 v7, v7, v223, v255
v_mov_b32 v8, v4
v_mov_b32 v9, v5
Zvav 100,144,96
v_mov_b32 v10, v6
v_mov_b32 v11, v7
v_permlane32_swap_b32 v4, v8
v_accvgpr_write_b32 a8, v48
Mvav 100,148,104
v_permlane32_swap_b32 v5, v9
v_permlane32_swap_b32 v6, v10
v_permlane32_swap_b32 v7, v11
v_max_f32 v4, v4, v8
Zvav 132,148,136
v_max_f32 v5, v5, v9
v_max_f32 v6, v6, v10
v_max_f32 v7, v7, v11
v_mov_b32 v8, v4
Mvav 132,144,128
v_mov_b32 v9, v5
v_mov_b32 v10, v6
v_mov_b32 v11, v7
v_accvgpr_write_b32 a9, v49
Mvav 36,152,48
v_permlane16_swap_b32 v4, v8
v_permlane16_swap_b32 v5, v9
v_permlane16_swap_b32 v6, v10
v_permlane16_swap_b32 v7, v11
Mvav 36,156,56
v_max_f32 v4, v4, v8
v_max_f32 v5, v5, v9
v_max_f32 v6, v6, v10
v_max_f32 v7, v7, v11
Mvav 68,156,88
v_sub_f32 v4, 0, v4
v_sub_f32 v5, 0, v5
v_sub_f32 v6, 0, v6
v_sub_f32 v7, 0, v7
Mvav 68,152,80
v_mov_b32 v224, v4
v_mov_b32 v225, v4
v_mov_b32 v226, v4
v_mov_b32 v227, v4
Mvav 100,152,112
v_mov_b32 v228, v5
v_mov_b32 v229, v5
v_mov_b32 v230, v5
v_mov_b32 v231, v5
Mvav 100,156,120
v_mov_b32 v232, v6
v_mov_b32 v233, v6
v_mov_b32 v234, v6
v_mov_b32 v235, v6
Mvav 132,156,152
v_mov_b32 v236, v7
v_mov_b32 v237, v7
v_mov_b32 v238, v7
v_mov_b32 v239, v7
Mvav 132,152,144
W 0
buffer_store_dwordx4 a[240:243], v1, s[8:11], s36 offen offset:2048 sc1
v_accvgpr_write_b32 a10, v50
v_accvgpr_write_b32 a11, v51
v_accvgpr_write_b32 a12, v56
Cvav 160,128,32,224
s_and_b32 s80, s14, 1
v_accvgpr_write_b32 a13, v57
v_accvgpr_write_b32 a14, v58
v_accvgpr_write_b32 a15, v59
v_accvgpr_write_b32 a16, v64
Mvav 160,132,40
buffer_store_dwordx4 a[248:251], v1, s[8:11], s36 offen offset:3072 sc1
s_add_u32 s36, s29, 393216
Ra 144,242,12288
Ra 148,242,13312
v_accvgpr_write_b32 a17, v65
Cvav 164,132,72,228
s_lshl_b32 s80, s80, 14
v_accvgpr_write_b32 a18, v66
v_accvgpr_write_b32 a19, v67
v_accvgpr_write_b32 a20, v72
v_accvgpr_write_b32 a21, v73
Mvav 164,128,64
s_add_u32 s80, s80, 98304
buffer_store_dwordx4 a[196:199], v1, s[8:11], s36 offen offset:0 sc1
v_accvgpr_write_b32 a22, v74
v_accvgpr_write_b32 a23, v75
v_accvgpr_write_b32 a24, v80
Cvav 168,128,96,232
v_accvgpr_write_b32 a25, v81
v_accvgpr_write_b32 a26, v82
A 196,196,224
v_accvgpr_write_b32 a27, v83
Mvav 168,132,104
buffer_store_dwordx4 a[204:207], v1, s[8:11], s36 offen offset:1024 sc1
)"
R"(A 197,197,224
A 198,198,224
v_accvgpr_write_b32 a28, v88
Cvav 172,132,136,236
Ra 152,242,14336
Ra 156,242,15360
A 199,199,224
v_accvgpr_write_b32 a29, v89
Mvav 172,128,128
buffer_store_dwordx4 a[212:215], v1, s[8:11], s36 offen offset:2048 sc1
X 196
v_accvgpr_write_b32 a30, v90
Mvav 160,136,48
X 197
v_accvgpr_write_b32 a31, v91
v_accvgpr_write_b32 a32, v96
Mvav 160,140,56
X 198
K 192,196,197
v_accvgpr_write_b32 a33, v97
Mvav 164,140,88
X 199
A 204,204,228
v_accvgpr_write_b32 a34, v98
Mvav 164,136,80
buffer_store_dwordx4 a[220:223], v1, s[8:11], s36 offen offset:3072 sc1
s_add_u32 s36, s29, 397312
K 193,198,199
v_accvgpr_write_b32 a35, v99
A 205,205,228
Mvav 168,136,112
A 206,206,228
v_accvgpr_write_b32 a36, v104
v_add_u32 v8, s80, v1
A 207,207,228
Mvav 168,140,120
X 204
v_accvgpr_write_b32 a37, v105
v_accvgpr_write_b32 a38, v106
Mvav 172,140,152
X 205
X 206
Mvav 172,136,144
W 0
buffer_store_dwordx4 a[228:231], v1, s[8:11], s36 offen offset:0 sc1
v_accvgpr_write_b32 a39, v107
v_accvgpr_write_b32 a40, v112
K 200,204,205
Cvav 176,144,32,224
Ra 128,242,24576
X 207
v_accvgpr_write_b32 a41, v113
Mvav 176,148,40
Ra 132,242,25600
A 212,212,232
v_accvgpr_write_b32 a42, v114
K 201,206,207
Cvav 180,148,72,228
A 213,213,232
v_accvgpr_write_b32 a43, v115
A 214,214,232
A 215,215,232
Mvav 180,144,64
buffer_store_dwordx4 a[236:239], v1, s[8:11], s36 offen offset:1024 sc1
v_accvgpr_write_b32 a44, v120
X 212
Cvav 184,144,96,232
v_accvgpr_write_b32 a45, v121
X 213
v_accvgpr_write_b32 a46, v122
Mvav 184,148,104
X 214
v_accvgpr_write_b32 a47, v123
K 208,212,213
Cvav 188,148,136,236
buffer_store_dwordx4 a[244:247], v1, s[8:11], s36 offen offset:2048 sc1
v_accvgpr_write_b32 a48, v128
X 215
Mvav 188,144,128
v_accvgpr_write_b32 a49, v129
A 220,220,236
v_accvgpr_write_b32 a50, v130
K 209,214,215
Mvav 176,152,48
A 221,221,236
v_accvgpr_write_b32 a51, v131
A 222,222,236
A 223,223,236
Mvav 176,156,56
Ra 136,242,26624
v_accvgpr_write_b32 a52, v136
X 220
Mvav 180,156,88
Ra 140,242,27648
v_accvgpr_write_b32 a53, v137
X 221
Mvav 180,152,80
buffer_store_dwordx4 a[252:255], v1, s[8:11], s36 offen offset:3072 sc1
v_accvgpr_write_b32 a54, v138
X 222
Mvav 184,152,112
v_accvgpr_write_b32 a55, v139
v_accvgpr_write_b32 a56, v144
v_accvgpr_write_b32 a57, v145
v_accvgpr_write_b32 a58, v146
v_accvgpr_write_b32 a59, v147
v_accvgpr_write_b32 a60, v152
v_accvgpr_write_b32 a61, v153
v_accvgpr_write_b32 a62, v154
v_accvgpr_write_b32 a63, v155
Mvav 184,156,120
s_barrier
K 216,220,221
X 223
A 36,36,224
Mvav 188,156,152
K 217,222,223
A 37,37,224
A 38,38,224
A 39,39,224
X 36
X 37
X 38
K 194,36,37
X 39
A 68,68,228
K 195,38,39
A 69,69,228
A 70,70,228
A 71,71,228
X 68
X 69
X 70
K 202,68,69
X 71
A 100,100,232
K 203,70,71
A 101,101,232
A 102,102,232
A 103,103,232
X 100
X 101
X 102
K 210,100,101
X 103
A 132,132,236
K 211,102,103
A 133,133,236
A 134,134,236
A 135,135,236
X 132
X 133
X 134
K 218,132,133
X 135
s_nop 0
K 219,134,135
Mvav 188,152,144
W 0
Ra 144,242,28672
Ra 148,242,29696
Ra 152,242,30720
Zvav 32,128,192
Ra 156,242,31744
Zvav 64,128,200
v_xor_b32 v242, 0x10000, v242
Zvav 96,128,208
s_and_b32 s80, s14, 1
s_xor_b32 s62, s62, 0x10000
Zvav 128,128,216
s_lshl_b32 s80, s80, 14
s_add_u32 s80, s80, 131072
v_add_u32 v23, 61440, v21
Zvav 132,132,216
v_add_u32 v12, s80, v1
Ra 64,12,8192
Ra 68,12,9216
v_add_u32 v24, 61440, v22
Zvav 100,132,208
Ra 72,12,10240
Ra 76,12,11264
Ra 80,12,12288
v_add_u32 v25, 122880, v21
Zvav 68,132,200
Ra 84,12,13312
Ra 88,12,14336
Ra 92,12,15360
v_add_u32 v26, 122880, v22
Zvav 36,132,192
Ra 96,12,0
Ra 100,12,1024
Ra 104,12,2048
Zvav 40,136,192
Ra 108,12,3072
Ra 112,12,4096
X 172
Zvav 72,136,200
Ra 116,12,5120
Ra 120,12,6144
X 173
Zvav 104,136,208
Ra 124,12,7168
X 174
K 220,172,173
Zvav 136,136,216
X 175
Zvav 140,140,216
X 168
K 221,174,175
Zvav 108,140,208
X 169
X 170
Zvav 76,140,200
s_waitcnt vmcnt(0) lgkmcnt(0)
s_barrier
ds_read_b128 v[248:251], v8
K 212,168,169
Zvav 44,140,192
s_add_u32 s80, s71, 128
v_mov_b64 v[28:29], s[12:13]
s_mov_b64 exec, 1
buffer_store_dwordx2 v[28:29], off, s[8:11], s80 sc1
s_mov_b64 exec, -1
X 171
X 164
Zvav 48,144,192
Ra 128,242,0
K 213,170,171
X 165
Zvav 80,144,200
X 166
Zvav 112,144,208
Ra 132,242,1024
K 204,164,165
X 167
Zvav 144,144,216
X 160
Zvav 148,148,216
K 205,166,167
X 161
Zvav 116,148,208
Ra 136,242,2048
X 162
Zvav 84,148,200
W 3
K 196,160,161
X 163
Zvav 52,148,192
X 176
K 197,162,163
Zvav 56,152,192
Ra 140,242,3072
X 177
Zvav 88,152,200
X 178
K 198,176,177
Zvav 120,152,208
X 179
X 180
Zvav 152,152,216
K 199,178,179
Zvav 156,156,216
X 181
X 182
Zvav 124,156,208
K 206,180,181
X 183
Zvav 92,156,200
K 207,182,183
Zvav 60,156,192
.Ll0t_42%=:
s_cmp_eq_u32 s64, s65
s_cbranch_scc1 .Ll1t_31%=
.Ll0_29%=:
W 0
Cvaa 160,128,0,224
Ra 144,242,4096
Ra 148,242,5120
Mvaa 160,132,4
s_xor_b32 s62, s62, 0x10000
Ra 152,242,6144
Cvaa 164,132,20,228
X 184
Mvaa 164,128,16
X 185
Cvaa 168,128,32,232
X 186
Mvaa 168,132,36
Ra 156,242,7168
K 214,184,185
Cvaa 172,132,52,236
X 187
Mvaa 172,128,48
X 188
Mvaa 160,136,8
Ra 128,242,16384
Mvaa 160,140,12
K 215,186,187
Mvaa 164,140,28
X 189
Mvaa 164,136,24
X 190
Mvaa 168,136,40
Ra 132,242,17408
Mvaa 168,140,44
K 222,188,189
Mvaa 172,140,60
X 191
Mvaa 172,136,56
K 223,190,191
v_smfmac_f32_16x16x64_bf16 a[160:163], a[176:179], v[192:199], v240
Ra 136,242,18432
v_smfmac_f32_16x16x64_bf16 a[164:167], a[176:179], v[200:207], v240
X 160
v_smfmac_f32_16x16x64_bf16 a[168:171], a[176:179], v[208:215], v240
X 161
v_smfmac_f32_16x16x64_bf16 a[172:175], a[176:179], v[216:223], v240
W 3
X 162
Cvaa 176,144,0,224
Ra 140,242,19456
Mvaa 176,148,4
K 192,160,161
Cvaa 180,148,20,228
X 163
Mvaa 180,144,16
X 164
Cvaa 184,144,32,232
Mvaa 184,148,36
K 193,162,163
Cvaa 188,148,52,236
X 165
Mvaa 188,144,48
X 166
Mvaa 176,152,8
Ra 144,242,20480
Ra 148,242,21504
Mvaa 176,156,12
K 200,164,165
Mvaa 180,156,28
X 167
Mvaa 180,152,24
X 168
Mvaa 184,152,40
Mvaa 184,156,44
K 201,166,167
Mvaa 188,156,60
X 169
Mvaa 188,152,56
W 2
Ra 152,242,22528
Mvav 32,128,196
X 170
Mvav 64,128,204
Ra 156,242,23552
K 208,168,169
Mvav 96,128,212
X 171
Mvav 128,128,220
Mvav 132,132,220
X 172
Mvav 100,132,212
s_add_u32 s64, s64, 1
Ra 128,242,8192
K 209,170,171
Mvav 68,132,204
X 173
Mvav 36,132,196
Mvav 40,136,196
X 174
Mvav 72,136,204
Ra 132,242,9216
K 216,172,173
Mvav 104,136,212
X 175
Mvav 136,136,220
Mvav 140,140,220
X 176
Mvav 108,140,212
Ra 136,242,10240
K 217,174,175
Mvav 76,140,204
X 177
Mvav 44,140,196
W 3
Mvav 48,144,196
X 178
Mvav 80,144,204
Ra 140,242,11264
Mvav 112,144,212
K 194,176,177
Mvav 144,144,220
X 179
Mvav 148,148,220
X 180
Mvav 116,148,212
Ra 144,242,12288
Mvav 84,148,204
K 195,178,179
Mvav 52,148,196
)"
R"(X 181
Mvav 56,152,196
X 182
Mvav 88,152,204
Ra 148,242,13312
Mvav 120,152,212
K 202,180,181
Mvav 152,152,220
X 183
Mvav 156,156,220
X 184
Mvav 124,156,212
Ra 152,242,14336
Mvav 92,156,204
K 203,182,183
Mvav 60,156,196
W 3
X 185
Cvaa 160,128,0,224
X 186
Mvaa 160,132,4
Ra 156,242,15360
Cvaa 164,132,20,228
K 210,184,185
Mvaa 164,128,16
X 187
Cvaa 168,128,32,232
X 188
Mvaa 168,132,36
Cvaa 172,132,52,236
K 211,186,187
Mvaa 172,128,48
X 189
Mvaa 160,136,8
Ra 128,242,24576
Ra 132,242,25600
Mvaa 160,140,12
X 190
Mvaa 164,140,28
K 218,188,189
Mvaa 164,136,24
X 191
Mvaa 168,136,40
Mvaa 168,140,44
Mvaa 172,140,60
X 160
Mvaa 172,136,56
W 2
Ra 136,242,26624
K 219,190,191
Cvaa 176,144,0,224
X 161
Mvaa 176,148,4
Ra 140,242,27648
Cvaa 180,148,20,228
X 162
Mvaa 180,144,16
K 196,160,161
Cvaa 184,144,32,232
X 163
Mvaa 184,148,36
X 164
Cvaa 188,148,52,236
K 197,162,163
Mvaa 188,144,48
X 165
Mvaa 176,152,8
Ra 144,242,28672
Ra 148,242,29696
Mvaa 176,156,12
X 166
Mvaa 180,156,28
K 204,164,165
Mvaa 180,152,24
X 167
Mvaa 184,152,40
Mvaa 184,156,44
X 168
Mvaa 188,156,60
Mvaa 188,152,56
W 2
Ra 152,242,30720
K 205,166,167
Mvav 32,128,192
X 169
Mvav 64,128,200
Ra 156,242,31744
v_xor_b32 v242, 0x10000, v242
Mvav 96,128,208
X 170
Mvav 128,128,216
K 212,168,169
Mvav 132,132,216
X 171
Mvav 100,132,208
X 172
Mvav 68,132,200
Mvav 36,132,192
K 213,170,171
Mvav 40,136,192
X 173
Mvav 72,136,200
X 174
Mvav 104,136,208
Mvav 136,136,216
K 220,172,173
Mvav 140,140,216
X 175
Mvav 108,140,208
X 176
Mvav 76,140,200
W 0
s_barrier
Mvav 44,140,192
Ra 128,242,0
K 221,174,175
Mvav 48,144,192
X 177
Mvav 80,144,200
X 178
Mvav 112,144,208
Ra 132,242,1024
Mvav 144,144,216
K 198,176,177
Mvav 148,148,216
X 179
Mvav 116,148,208
X 180
Mvav 84,148,200
Ra 136,242,2048
Mvav 52,148,192
K 199,178,179
Mvav 56,152,192
X 181
Mvav 88,152,200
Ra 140,242,3072
Mvav 120,152,208
X 182
Mvav 152,152,216
K 206,180,181
Mvav 156,156,216
X 183
Mvav 124,156,208
Mvav 92,156,200
K 207,182,183
Mvav 60,156,192
s_cmp_lg_u32 s64, s65
s_cbranch_scc1 .Ll0_29%=
s_cmp_lt_u32 s85, 54
s_cbranch_scc1 .Lmoe_40%=
.Ll1t_31%=:
s_cmp_eq_u32 s64, s66
s_cbranch_scc1 .Lsp1_32%=
.Ll1_30%=:
W 0
Cvaa 160,128,0,224
Ra 144,242,4096
Ra 148,242,5120
Mvaa 160,132,4
s_add_u32 m0, s62, 0
Ra 152,242,6144
Cvaa 164,132,20,228
X 184
Mvaa 164,128,16
X 185
Cvaa 168,128,32,232
X 186
Mvaa 168,132,36
Ra 156,242,7168
K 214,184,185
Cvaa 172,132,52,236
Dc 1,8,61,0
Mvaa 172,128,48
X 187
Mvaa 160,136,8
X 188
Mvaa 160,140,12
Ra 128,242,16384
K 215,186,187
Mvaa 164,140,28
Dc 1,8,61,1024
Mvaa 164,136,24
X 189
Mvaa 168,136,40
X 190
Mvaa 168,140,44
Ra 132,242,17408
K 222,188,189
Mvaa 172,140,60
Dc 1,8,61,2048
Mvaa 172,136,56
X 191
v_smfmac_f32_16x16x64_bf16 a[160:163], a[176:179], v[192:199], v240
Ra 136,242,18432
K 223,190,191
v_smfmac_f32_16x16x64_bf16 a[164:167], a[176:179], v[200:207], v240
X 160
v_smfmac_f32_16x16x64_bf16 a[168:171], a[176:179], v[208:215], v240
Dc 1,8,61,3072
v_smfmac_f32_16x16x64_bf16 a[172:175], a[176:179], v[216:223], v240
W 3
X 161
Cvaa 176,144,0,224
s_add_u32 m0, s62, 4096
X 162
Mvaa 176,148,4
Ra 140,242,19456
K 192,160,161
Cvaa 180,148,20,228
X 163
Mvaa 180,144,16
Dc 243,8,61,0
Cvaa 184,144,32,232
X 164
Mvaa 184,148,36
K 193,162,163
Cvaa 188,148,52,236
X 165
Mvaa 188,144,48
Dc 243,8,61,1024
Ra 144,242,20480
Mvaa 176,152,8
X 166
Mvaa 176,156,12
Ra 148,242,21504
K 200,164,165
Mvaa 180,156,28
X 167
Mvaa 180,152,24
Dc 243,8,61,2048
Mvaa 184,152,40
X 168
Mvaa 184,156,44
K 201,166,167
Mvaa 188,156,60
X 169
Mvaa 188,152,56
W 2
Dc 243,8,61,3072
Ra 152,242,22528
Mvav 32,128,196
s_add_u32 m0, s62, 8192
X 170
Mvav 64,128,204
Ra 156,242,23552
K 208,168,169
Mvav 96,128,212
X 171
Mvav 128,128,220
Dc 244,8,61,0
Mvav 132,132,220
X 172
Mvav 100,132,212
Ra 128,242,8192
K 209,170,171
Mvav 68,132,204
X 173
Mvav 36,132,196
Dc 244,8,61,1024
Mvav 40,136,196
X 174
Mvav 72,136,204
Ra 132,242,9216
K 216,172,173
Mvav 104,136,212
X 175
Mvav 136,136,220
Dc 244,8,61,2048
Mvav 140,140,220
X 176
Mvav 108,140,212
Ra 136,242,10240
K 217,174,175
Mvav 76,140,204
X 177
Mvav 44,140,196
W 3
Dc 244,8,61,3072
Mvav 48,144,196
X 178
Mvav 80,144,204
s_add_u32 m0, s62, 12288
Ra 140,242,11264
Mvav 112,144,212
K 194,176,177
Mvav 144,144,220
X 179
Mvav 148,148,220
X 180
Mvav 116,148,212
Dc 245,8,61,0
Ra 144,242,12288
Mvav 84,148,204
K 195,178,179
Mvav 52,148,196
X 181
Mvav 56,152,196
X 182
Mvav 88,152,204
Dc 245,8,61,1024
Ra 148,242,13312
Mvav 120,152,212
K 202,180,181
Mvav 152,152,220
X 183
Mvav 156,156,220
X 184
Mvav 124,156,212
Dc 245,8,61,2048
Ra 152,242,14336
Mvav 92,156,204
K 203,182,183
Mvav 60,156,196
W 3
X 185
Cvaa 160,128,0,224
X 186
Mvaa 160,132,4
Dc 245,8,61,3072
Ra 156,242,15360
Cvaa 164,132,20,228
s_add_u32 s61, s61, s63
K 210,184,185
Mvaa 164,128,16
X 187
Cvaa 168,128,32,232
X 188
Mvaa 168,132,36
Cvaa 172,132,52,236
s_xor_b32 s62, s62, 0x10000
K 211,186,187
Mvaa 172,128,48
X 189
Mvaa 160,136,8
Ra 128,242,24576
Ra 132,242,25600
Mvaa 160,140,12
s_add_u32 s64, s64, 1
X 190
Mvaa 164,140,28
K 218,188,189
Mvaa 164,136,24
X 191
Mvaa 168,136,40
Mvaa 168,140,44
X 160
Mvaa 172,140,60
K 219,190,191
Mvaa 172,136,56
W 2
X 161
Cvaa 176,144,0,224
Ra 136,242,26624
Ra 140,242,27648
Mvaa 176,148,4
X 162
Cvaa 180,148,20,228
K 196,160,161
Mvaa 180,144,16
X 163
Cvaa 184,144,32,232
Mvaa 184,148,36
X 164
Cvaa 188,148,52,236
K 197,162,163
Mvaa 188,144,48
X 165
Mvaa 176,152,8
Ra 144,242,28672
Ra 148,242,29696
Mvaa 176,156,12
X 166
Mvaa 180,156,28
K 204,164,165
Mvaa 180,152,24
X 167
Mvaa 184,152,40
Mvaa 184,156,44
X 168
Mvaa 188,156,60
Mvaa 188,152,56
W 2
Ra 152,242,30720
K 205,166,167
Mvav 32,128,192
X 169
Mvav 64,128,200
Ra 156,242,31744
v_xor_b32 v242, 0x10000, v242
Mvav 96,128,208
X 170
Mvav 128,128,216
K 212,168,169
Mvav 132,132,216
X 171
Mvav 100,132,208
X 172
Mvav 68,132,200
Mvav 36,132,192
K 213,170,171
)"
R"(Mvav 40,136,192
X 173
Mvav 72,136,200
X 174
Mvav 104,136,208
Mvav 136,136,216
K 220,172,173
Mvav 140,140,216
X 175
Mvav 108,140,208
X 176
Mvav 76,140,200
s_waitcnt vmcnt(0) lgkmcnt(0)
s_barrier
Mvav 44,140,192
Ra 128,242,0
K 221,174,175
Mvav 48,144,192
X 177
Mvav 80,144,200
X 178
Mvav 112,144,208
Ra 132,242,1024
Mvav 144,144,216
K 198,176,177
Mvav 148,148,216
X 179
Mvav 116,148,208
X 180
Mvav 84,148,200
Ra 136,242,2048
Mvav 52,148,192
K 199,178,179
Mvav 56,152,192
X 181
Mvav 88,152,200
Ra 140,242,3072
Mvav 120,152,208
X 182
Mvav 152,152,216
K 206,180,181
Mvav 156,156,216
X 183
Mvav 124,156,208
Mvav 92,156,200
K 207,182,183
Mvav 60,156,192
s_cmp_lg_u32 s64, s66
s_cbranch_scc1 .Ll1_30%=
s_cmp_ge_u32 s14, 2
s_cbranch_scc1 .Lf1r_43%=
.Lsp1_32%=:
W 0
Cvaa 160,128,0,224
Ra 144,242,4096
Ra 148,242,5120
Mvaa 160,132,4
s_add_u32 m0, s62, 0
Ra 152,242,6144
Cvaa 164,132,20,228
X 184
Mvaa 164,128,16
X 185
Cvaa 168,128,32,232
X 186
Mvaa 168,132,36
Ra 156,242,7168
K 214,184,185
Cvaa 172,132,52,236
Dc 1,8,61,0
Mvaa 172,128,48
X 187
Mvaa 160,136,8
X 188
Mvaa 160,140,12
Ra 128,242,16384
K 215,186,187
Mvaa 164,140,28
Dc 1,8,61,1024
Ra 132,242,17408
Mvaa 164,136,24
X 189
Mvaa 168,136,40
X 190
Mvaa 168,140,44
Dc 1,8,61,2048
K 222,188,189
Mvaa 172,140,60
X 191
Mvaa 172,136,56
Ra 136,242,18432
K 223,190,191
v_smfmac_f32_16x16x64_bf16 a[160:163], a[176:179], v[192:199], v240
Dc 1,8,61,3072
Ra 140,242,19456
v_smfmac_f32_16x16x64_bf16 a[164:167], a[176:179], v[200:207], v240
s_add_u32 m0, s62, 4096
v_cndmask_b32_e64 v160, v241, v160, s[72:73]
v_cndmask_b32_e64 v161, v241, v161, s[74:75]
v_smfmac_f32_16x16x64_bf16 a[168:171], a[176:179], v[208:215], v240
v_cndmask_b32_e64 v162, v241, v162, s[76:77]
v_cndmask_b32_e64 v163, v241, v163, s[78:79]
v_smfmac_f32_16x16x64_bf16 a[172:175], a[176:179], v[216:223], v240
W 4
X 160
Cvaa 180,144,16,228
X 161
Mvaa 180,148,20
Dc 243,8,61,0
Cvaa 184,148,36,232
X 162
Mvaa 184,144,32
K 192,160,161
Cvaa 188,144,48,236
X 163
Mvaa 188,148,52
Dc 243,8,61,1024
Ra 144,242,20480
Mvaa 180,152,24
X 164
Mvaa 180,156,28
Ra 148,242,21504
K 193,162,163
Mvaa 184,156,44
Dc 243,8,61,2048
Mvaa 184,152,40
X 165
Mvaa 188,152,56
X 166
Mvaa 188,156,60
W 2
Dc 243,8,61,3072
K 200,164,165
Mvav 32,128,196
s_add_u32 m0, s62, 8192
X 167
Mvav 64,128,204
Ra 152,242,22528
Ra 156,242,23552
Mvav 96,128,212
X 168
Mvav 128,128,220
Dc 244,8,61,0
K 201,166,167
Mvav 132,132,220
X 169
Mvav 100,132,212
X 170
Mvav 68,132,204
Ra 128,242,8192
K 208,168,169
Mvav 36,132,196
X 171
Mvav 40,136,196
Dc 244,8,61,1024
Ra 132,242,9216
Mvav 72,136,204
X 172
Mvav 104,136,212
K 209,170,171
Mvav 136,136,220
X 173
Mvav 140,140,220
Dc 244,8,61,2048
Ra 136,242,10240
Mvav 108,140,212
X 174
Mvav 76,140,204
K 216,172,173
Mvav 44,140,196
W 3
Dc 244,8,61,3072
Ra 140,242,11264
Mvav 48,144,196
s_add_u32 m0, s62, 12288
X 175
Mvav 80,144,204
v_mov_b32 v194, 0
K 217,174,175
Mvav 112,144,212
v_mov_b32 v195, 0
v_cndmask_b32_e64 v180, v241, v180, s[72:73]
Mvav 144,144,220
v_cndmask_b32_e64 v181, v241, v181, s[74:75]
v_cndmask_b32_e64 v182, v241, v182, s[76:77]
Mvav 148,148,220
Dc 245,8,61,0
v_cndmask_b32_e64 v183, v241, v183, s[78:79]
Mvav 116,148,212
X 180
Mvav 84,148,204
Ra 144,242,12288
Mvav 52,148,196
X 181
Mvav 56,152,196
Dc 245,8,61,1024
Ra 148,242,13312
Mvav 88,152,204
X 182
Mvav 120,152,212
K 202,180,181
Mvav 152,152,220
X 183
Mvav 156,156,220
Dc 245,8,61,2048
Ra 152,242,14336
Mvav 124,156,212
X 184
Mvav 92,156,204
K 203,182,183
Mvav 60,156,196
W 3
Dc 245,8,61,3072
Ra 156,242,15360
Cvaa 168,128,32,232
s_add_u32 s61, s61, s63
X 185
Mvaa 168,132,36
s_xor_b32 s62, s62, 0x10000
X 186
Cvaa 172,132,52,236
K 210,184,185
Mvaa 172,128,48
s_add_u32 s64, s64, 1
X 187
Mvaa 168,136,40
X 188
Mvaa 168,140,44
Ra 128,242,24576
K 211,186,187
Mvaa 172,140,60
Ra 132,242,25600
Mvaa 172,136,56
Ra 136,242,26624
Ra 140,242,27648
X 189
X 190
K 218,188,189
X 191
v_mov_b32 v196, 0
K 219,190,191
W 4
Cvaa 188,144,48,236
Ra 144,242,28672
Mvaa 188,148,52
v_mov_b32 v197, 0
v_mov_b32 v204, 0
Mvaa 188,152,56
Ra 148,242,29696
v_mov_b32 v205, 0
Mvaa 188,156,60
W 2
Ra 152,242,30720
v_cndmask_b32_e64 v168, v241, v168, s[72:73]
Mvav 32,128,192
v_cndmask_b32_e64 v169, v241, v169, s[74:75]
v_cndmask_b32_e64 v170, v241, v170, s[76:77]
Mvav 64,128,200
Ra 156,242,31744
v_cndmask_b32_e64 v171, v241, v171, s[78:79]
Mvav 96,128,208
v_xor_b32 v242, 0x10000, v242
Mvav 128,128,216
X 168
Mvav 132,132,216
X 169
Mvav 100,132,208
X 170
Mvav 68,132,200
v_mov_b32 v224, v250
K 212,168,169
Mvav 36,132,192
v_mov_b32 v225, v250
v_mov_b32 v226, v250
Mvav 40,136,192
X 171
Mvav 72,136,200
X 172
Mvav 104,136,208
v_mov_b32 v227, v250
K 213,170,171
Mvav 136,136,216
X 173
Mvav 140,140,216
v_mov_b32 v228, v251
v_mov_b32 v229, v251
Mvav 108,140,208
X 174
Mvav 76,140,200
K 220,172,173
v_mov_b32 v230, v251
Mvav 44,140,192
W 0
X 175
Mvav 48,144,192
v_mov_b32 v231, v251
v_mov_b32 v198, 0
Mvav 80,144,200
K 221,174,175
v_mov_b32 v232, v248
Mvav 112,144,208
v_mov_b32 v199, 0
v_mov_b32 v206, 0
Mvav 144,144,216
v_mov_b32 v233, v248
v_mov_b32 v207, 0
Mvav 148,148,216
v_mov_b32 v214, 0
v_mov_b32 v234, v248
Mvav 116,148,208
v_mov_b32 v215, 0
v_cndmask_b32_e64 v188, v241, v188, s[72:73]
Mvav 84,148,200
v_mov_b32 v235, v248
v_cndmask_b32_e64 v189, v241, v189, s[74:75]
Mvav 52,148,192
v_cndmask_b32_e64 v190, v241, v190, s[76:77]
v_mov_b32 v236, v249
Mvav 56,152,192
v_cndmask_b32_e64 v191, v241, v191, s[78:79]
v_mov_b32 v237, v249
Mvav 88,152,200
X 188
Mvav 120,152,208
X 189
Mvav 152,152,216
v_mov_b32 v238, v249
v_mov_b32 v239, v249
Mvav 156,156,216
X 190
Mvav 124,156,208
s_waitcnt vmcnt(0)
s_barrier
Mvav 92,156,200
Ra 128,242,40960
K 222,188,189
Mvav 60,156,192
Ra 132,242,41984
Ra 136,242,43008
Ra 140,242,44032
X 191
s_nop 0
K 223,190,191
s_cmp_eq_u32 s64, 15
s_cbranch_scc1 .Lfs_38%=
.Lsp2_33%=:
v_smfmac_f32_16x16x64_bf16 a[160:163], a[176:179], v[192:199], v240
s_add_u32 m0, s62, 0
Ra 144,242,45056
Ra 148,242,46080
Ra 152,242,47104
Ra 156,242,48128
v_smfmac_f32_16x16x64_bf16 a[164:167], a[176:179], v[200:207], v240
v_smfmac_f32_16x16x64_bf16 a[168:171], a[176:179], v[208:215], v240
v_smfmac_f32_16x16x64_bf16 a[172:175], a[176:179], v[216:223], v240
W 4
Dc 1,8,61,0
Cvaa 168,128,96,232
v_mov_b32 v192, 0
G 12,160
Mvaa 168,132,100
Dc 1,8,61,1024
G 4,161
A 12,12,4
v_cmp_nlt_f32_e64 s[86:87], v12, s90
v_mov_b32 v193, 0
Cvaa 172,132,116,236
v_rcp_f32 v12, v12
s_or_b64 s[88:89], s[88:89], s[86:87]
v_accvgpr_write_b32 a160, 0
v_mov_b32 v200, 0
v_accvgpr_write_b32 a161, 0
v_accvgpr_write_b32 a162, 0
Mvaa 172,128,112
Dc 1,8,61,2048
v_accvgpr_write_b32 a163, 0
G 13,164
v_mov_b32 v201, 0
G 5,165
A 13,13,5
Mvaa 168,136,104
Ra 128,242,16384
Ra 132,242,17408
v_cmp_nlt_f32_e64 s[86:87], v13, s90
v_rcp_f32 v13, v13
s_or_b64 s[88:89], s[88:89], s[86:87]
v_accvgpr_write_b32 a164, 0
Mvaa 168,140,108
Dc 1,8,61,3072
v_accvgpr_write_b32 a165, 0
v_accvgpr_write_b32 a166, 0
v_accvgpr_write_b32 a167, 0
U 32,32,12
U 33,33,12
Mvaa 172,140,124
U 34,34,12
U 35,35,12
U 36,36,12
U 37,37,12
U 38,38,12
)"
R"(U 39,39,12
Mvaa 172,136,120
s_add_u32 m0, s62, 4096
Ra 136,242,18432
Ra 140,242,19456
K 32,32,33
K 33,34,35
K 34,36,37
W 4
Cvaa 188,144,112,236
K 35,38,39
s_add_u32 s93, s67, 0
buffer_store_dwordx4 v[32:35], v1, s[8:11], s93 offen offset:0 sc1
U 40,40,12
U 41,41,12
U 42,42,12
U 43,43,12
Mvaa 188,148,116
Ra 144,242,20480
v_cndmask_b32_e64 v168, v241, v168, s[72:73]
v_cndmask_b32_e64 v169, v241, v169, s[74:75]
v_cndmask_b32_e64 v170, v241, v170, s[76:77]
v_cndmask_b32_e64 v171, v241, v171, s[78:79]
U 44,44,12
Mvaa 188,152,120
X 168
X 169
U 45,45,12
U 46,46,12
Mvaa 188,156,124
W 1
Ra 148,242,21504
Dc 243,8,61,0
U 47,47,12
K 40,40,41
K 41,42,43
K 42,44,45
Mvav 96,128,212
Ra 152,242,22528
Ra 156,242,23552
K 43,46,47
X 170
K 208,168,169
Mvav 128,128,220
buffer_store_dwordx4 v[40:43], v1, s[8:11], s93 offen offset:1024 sc1
U 48,48,12
U 49,49,12
U 50,50,12
U 51,51,12
U 52,52,12
Mvav 132,132,220
U 53,53,12
U 54,54,12
U 55,55,12
K 48,48,49
K 49,50,51
K 50,52,53
Mvav 100,132,212
Dc 243,8,61,1024
Ra 128,242,32768
Ra 132,242,33792
X 171
K 51,54,55
Mvav 104,136,212
X 172
K 209,170,171
X 173
Mvav 136,136,220
buffer_store_dwordx4 v[48:51], v1, s[8:11], s93 offen offset:2048 sc1
U 56,56,12
U 57,57,12
U 58,58,12
U 59,59,12
U 60,60,12
Mvav 140,140,220
U 61,61,12
U 62,62,12
U 63,63,12
K 56,56,57
K 57,58,59
K 58,60,61
Mvav 108,140,212
W 2
Dc 243,8,61,2048
Ra 136,242,34816
Ra 140,242,35840
K 59,62,63
X 174
Mvav 112,144,212
K 216,172,173
X 175
Mvav 144,144,220
buffer_store_dwordx4 v[56:59], v1, s[8:11], s93 offen offset:3072 sc1
Ra 144,242,36864
G 14,168
G 6,169
A 14,14,6
v_cmp_nlt_f32_e64 s[86:87], v14, s90
Mvav 148,148,220
v_rcp_f32 v14, v14
s_or_b64 s[88:89], s[88:89], s[86:87]
v_accvgpr_write_b32 a168, 0
v_accvgpr_write_b32 a169, 0
v_accvgpr_write_b32 a170, 0
v_accvgpr_write_b32 a171, 0
Mvav 116,148,212
Dc 243,8,61,3072
s_add_u32 m0, s62, 8192
G 15,172
G 7,173
A 15,15,7
v_cmp_nlt_f32_e64 s[86:87], v15, s90
v_mov_b32 v194, 0
Mvav 120,152,212
v_rcp_f32 v15, v15
s_or_b64 s[88:89], s[88:89], s[86:87]
v_accvgpr_write_b32 a172, 0
v_accvgpr_write_b32 a173, 0
v_accvgpr_write_b32 a174, 0
v_accvgpr_write_b32 a175, 0
Mvav 152,152,220
Dc 244,8,61,0
U 64,64,13
U 65,65,13
U 66,66,13
U 67,67,13
U 68,68,13
Mvav 156,156,220
U 69,69,13
U 70,70,13
U 71,71,13
K 64,64,65
K 65,66,67
K 66,68,69
Mvav 124,156,212
W 1
Dc 244,8,61,1024
Ra 148,242,37888
K 67,70,71
s_add_u32 s93, s67, 131072
K 217,174,175
v_mov_b32 v195, 0
Cvaa 160,128,64,224
Ra 152,242,38912
Ra 156,242,39936
v_mov_b32 v202, 0
v_mov_b32 v203, 0
v_mov_b32 v210, 0
v_mov_b32 v211, 0
Mvaa 160,132,68
buffer_store_dwordx4 v[64:67], v1, s[8:11], s93 offen offset:0 sc1
U 72,72,13
U 73,73,13
U 74,74,13
U 75,75,13
U 76,76,13
Cvaa 164,132,84,228
U 77,77,13
U 78,78,13
U 79,79,13
K 72,72,73
K 73,74,75
K 74,76,77
Mvaa 164,128,80
Dc 244,8,61,2048
K 75,78,79
v_cndmask_b32_e64 v188, v241, v188, s[72:73]
v_cndmask_b32_e64 v189, v241, v189, s[74:75]
v_cndmask_b32_e64 v190, v241, v190, s[76:77]
v_cndmask_b32_e64 v191, v241, v191, s[78:79]
Cvaa 168,128,96,232
X 188
X 189
Mvaa 168,132,100
buffer_store_dwordx4 v[72:75], v1, s[8:11], s93 offen offset:1024 sc1
U 80,80,13
U 81,81,13
U 82,82,13
U 83,83,13
U 84,84,13
Cvaa 172,132,116,236
U 85,85,13
U 86,86,13
U 87,87,13
K 80,80,81
K 81,82,83
K 82,84,85
Mvaa 172,128,112
Dc 244,8,61,3072
s_add_u32 m0, s62, 12288
Ra 128,242,49152
Ra 132,242,50176
K 83,86,87
X 190
Mvaa 160,136,72
K 218,188,189
X 191
Mvaa 160,140,76
buffer_store_dwordx4 v[80:83], v1, s[8:11], s93 offen offset:2048 sc1
U 88,88,13
U 89,89,13
U 90,90,13
U 91,91,13
U 92,92,13
Mvaa 164,140,92
U 93,93,13
U 94,94,13
U 95,95,13
K 88,88,89
K 89,90,91
K 90,92,93
Mvaa 164,136,88
K 91,94,95
buffer_store_dwordx4 v[88:91], v1, s[8:11], s93 offen offset:3072 sc1
U 96,96,14
U 97,97,14
U 98,98,14
U 99,99,14
Mvaa 168,136,104
U 100,100,14
U 101,101,14
U 102,102,14
U 103,103,14
K 96,96,97
K 97,98,99
Mvaa 168,140,108
Dc 245,8,61,0
K 98,100,101
K 99,102,103
s_add_u32 s93, s67, 262144
v_cndmask_b32_e64 v160, v241, v160, s[72:73]
K 219,190,191
v_cndmask_b32_e64 v161, v241, v161, s[74:75]
Mvaa 172,140,124
v_cndmask_b32_e64 v162, v241, v162, s[76:77]
v_cndmask_b32_e64 v163, v241, v163, s[78:79]
X 160
Mvaa 172,136,120
W 2
Dc 245,8,61,1024
Ra 136,242,51200
Ra 140,242,52224
X 161
Cvaa 180,144,80,228
Mvaa 180,148,84
buffer_store_dwordx4 v[96:99], v1, s[8:11], s93 offen offset:0 sc1
U 104,104,14
U 105,105,14
U 106,106,14
U 107,107,14
U 108,108,14
Cvaa 184,148,100,232
U 109,109,14
U 110,110,14
U 111,111,14
K 104,104,105
K 105,106,107
K 106,108,109
Mvaa 184,144,96
Dc 245,8,61,2048
K 107,110,111
X 162
K 196,160,161
Cvaa 188,144,112,236
Ra 144,242,53248
X 163
Mvaa 188,148,116
buffer_store_dwordx4 v[104:107], v1, s[8:11], s93 offen offset:1024 sc1
U 112,112,14
U 113,113,14
U 114,114,14
U 115,115,14
U 116,116,14
Mvaa 180,152,88
U 117,117,14
U 118,118,14
U 119,119,14
K 112,112,113
K 113,114,115
K 114,116,117
Mvaa 180,156,92
Dc 245,8,61,3072
s_add_u32 s61, s61, s63
s_xor_b32 s62, s62, 0x10000
s_add_u32 s64, s64, 1
K 115,118,119
X 164
Mvaa 184,156,108
Ra 148,242,54272
K 197,162,163
X 165
Mvaa 184,152,104
buffer_store_dwordx4 v[112:115], v1, s[8:11], s93 offen offset:2048 sc1
U 120,120,14
U 121,121,14
U 122,122,14
U 123,123,14
U 124,124,14
Mvaa 188,152,120
U 125,125,14
U 126,126,14
U 127,127,14
K 120,120,121
K 121,122,123
K 122,124,125
Mvaa 188,156,124
W 2
K 123,126,127
buffer_store_dwordx4 v[120:123], v1, s[8:11], s93 offen offset:3072 sc1
U 128,128,15
U 129,129,15
U 130,130,15
U 131,131,15
Zvav 32,128,192
U 132,132,15
U 133,133,15
U 134,134,15
U 135,135,15
K 128,128,129
K 129,130,131
Zvav 64,128,200
K 130,132,133
K 131,134,135
s_add_u32 s93, s67, 393216
buffer_store_dwordx4 v[128:131], v1, s[8:11], s93 offen offset:0 sc1
U 136,136,15
U 137,137,15
U 138,138,15
Zvav 96,128,208
U 139,139,15
U 140,140,15
U 141,141,15
U 142,142,15
U 143,143,15
K 136,136,137
Zvav 128,128,216
K 137,138,139
K 138,140,141
K 139,142,143
buffer_store_dwordx4 v[136:139], v1, s[8:11], s93 offen offset:1024 sc1
U 144,144,15
U 145,145,15
Zvav 132,132,216
U 146,146,15
U 147,147,15
U 148,148,15
U 149,149,15
U 150,150,15
U 151,151,15
Zvav 100,132,208
K 144,144,145
K 145,146,147
K 146,148,149
K 147,150,151
buffer_store_dwordx4 v[144:147], v1, s[8:11], s93 offen offset:2048 sc1
U 152,152,15
Zvav 68,132,200
U 153,153,15
U 154,154,15
U 155,155,15
U 156,156,15
U 157,157,15
U 158,158,15
Zvav 36,132,192
U 159,159,15
K 152,152,153
K 153,154,155
K 154,156,157
K 155,158,159
buffer_store_dwordx4 v[152:155], v1, s[8:11], s93 offen offset:3072 sc1
Zvav 40,136,192
Ra 152,242,55296
X 166
K 204,164,165
X 167
Zvav 72,136,200
Ra 156,242,56320
X 168
v_xor_b32 v242, 0x10000, v242
K 205,166,167
Zvav 104,136,208
X 169
X 170
K 212,168,169
Zvav 136,136,216
X 171
X 172
K 213,170,171
Zvav 140,140,216
X 173
Zvav 108,140,208
X 174
Zvav 76,140,200
s_waitcnt vmcnt(6) lgkmcnt(0)
s_barrier
Zvav 44,140,192
Ra 128,242,40960
K 220,172,173
X 175
Zvav 48,144,192
)"
R"(Zvav 80,144,200
v_mov_b32 v198, 0
Zvav 112,144,208
Ra 132,242,41984
K 221,174,175
Zvav 144,144,216
v_mov_b32 v199, 0
v_cndmask_b32_e64 v180, v241, v180, s[72:73]
Zvav 148,148,216
v_cndmask_b32_e64 v181, v241, v181, s[74:75]
Zvav 116,148,208
Ra 136,242,43008
v_cndmask_b32_e64 v182, v241, v182, s[76:77]
Zvav 84,148,200
v_cndmask_b32_e64 v183, v241, v183, s[78:79]
Zvav 52,148,192
X 180
Zvav 56,152,192
Ra 140,242,44032
X 181
Zvav 88,152,200
Zvav 120,152,208
X 182
Zvav 152,152,216
K 206,180,181
Zvav 156,156,216
X 183
Zvav 124,156,208
Zvav 92,156,200
s_waitcnt vmcnt(0)
s_and_b64 s[86:87], s[88:89], s[24:25]
s_cbranch_scc1 .LL_45%=
v_mov_b64 v[28:29], s[12:13]
s_mov_b64 exec, 1
buffer_store_dwordx2 v[28:29], off, s[8:11], s69 sc1
s_mov_b64 exec, -1
.LL_45%=:
K 207,182,183
Zvav 60,156,192
s_cmp_eq_u32 s64, 15
s_cbranch_scc1 .Lf2_37%=
.Ll2_34%=:
W 0
Cvaa 160,128,64,224
Ra 144,242,45056
Ra 148,242,46080
Mvaa 160,132,68
s_add_u32 m0, s62, 8192
Ra 152,242,47104
Cvaa 164,132,84,228
X 184
Mvaa 164,128,80
X 185
Cvaa 168,128,96,232
X 186
Mvaa 168,132,100
Ra 156,242,48128
K 214,184,185
Cvaa 172,132,116,236
Dc 244,8,61,0
Mvaa 172,128,112
X 187
Mvaa 160,136,72
X 188
Mvaa 160,140,76
Ra 128,242,57344
K 215,186,187
Mvaa 164,140,92
Dc 244,8,61,1024
Mvaa 164,136,88
X 189
Mvaa 168,136,104
X 190
Mvaa 168,140,108
Ra 132,242,58368
K 222,188,189
Mvaa 172,140,124
Dc 244,8,61,2048
Mvaa 172,136,120
X 191
v_smfmac_f32_16x16x64_bf16 a[160:163], a[176:179], v[192:199], v240
Ra 136,242,59392
K 223,190,191
v_smfmac_f32_16x16x64_bf16 a[164:167], a[176:179], v[200:207], v240
X 160
v_smfmac_f32_16x16x64_bf16 a[168:171], a[176:179], v[208:215], v240
Dc 244,8,61,3072
v_smfmac_f32_16x16x64_bf16 a[172:175], a[176:179], v[216:223], v240
W 3
X 161
Cvaa 176,144,64,224
s_add_u32 m0, s62, 12288
X 162
Mvaa 176,148,68
Ra 140,242,60416
K 192,160,161
Cvaa 180,148,84,228
X 163
Mvaa 180,144,80
Dc 245,8,61,0
Cvaa 184,144,96,232
X 164
Mvaa 184,148,100
K 193,162,163
Cvaa 188,148,116,236
X 165
Mvaa 188,144,112
Dc 245,8,61,1024
Ra 144,242,61440
Mvaa 176,152,72
X 166
Mvaa 176,156,76
Ra 148,242,62464
K 200,164,165
Mvaa 180,156,92
X 167
Mvaa 180,152,88
Dc 245,8,61,2048
Mvaa 184,152,104
X 168
Mvaa 184,156,108
K 201,166,167
Mvaa 188,156,124
X 169
Mvaa 188,152,120
W 2
Dc 245,8,61,3072
Ra 152,242,63488
Mvav 32,128,196
s_add_u32 m0, s62, 0
X 170
Mvav 64,128,204
Ra 156,242,64512
K 208,168,169
Mvav 96,128,212
X 171
Mvav 128,128,220
Dc 1,8,61,0
Mvav 132,132,220
X 172
Mvav 100,132,212
Ra 128,242,32768
K 209,170,171
Mvav 68,132,204
X 173
Mvav 36,132,196
Dc 1,8,61,1024
Mvav 40,136,196
X 174
Mvav 72,136,204
Ra 132,242,33792
K 216,172,173
Mvav 104,136,212
X 175
Mvav 136,136,220
Dc 1,8,61,2048
Mvav 140,140,220
X 176
Mvav 108,140,212
Ra 136,242,34816
K 217,174,175
Mvav 76,140,204
X 177
Mvav 44,140,196
W 3
Dc 1,8,61,3072
Mvav 48,144,196
X 178
Mvav 80,144,204
s_add_u32 m0, s62, 4096
Ra 140,242,35840
Mvav 112,144,212
K 194,176,177
Mvav 144,144,220
X 179
Mvav 148,148,220
X 180
Mvav 116,148,212
Dc 243,8,61,0
Ra 144,242,36864
Mvav 84,148,204
K 195,178,179
Mvav 52,148,196
X 181
Mvav 56,152,196
X 182
Mvav 88,152,204
Dc 243,8,61,1024
Ra 148,242,37888
Mvav 120,152,212
K 202,180,181
Mvav 152,152,220
X 183
Mvav 156,156,220
X 184
Mvav 124,156,212
Dc 243,8,61,2048
Ra 152,242,38912
Mvav 92,156,204
K 203,182,183
Mvav 60,156,196
W 3
X 185
Cvaa 160,128,64,224
X 186
Mvaa 160,132,68
Dc 243,8,61,3072
Ra 156,242,39936
Cvaa 164,132,84,228
s_add_u32 s61, s61, s63
K 210,184,185
Mvaa 164,128,80
X 187
Cvaa 168,128,96,232
X 188
Mvaa 168,132,100
Cvaa 172,132,116,236
s_xor_b32 s62, s62, 0x10000
K 211,186,187
Mvaa 172,128,112
X 189
Mvaa 160,136,72
Ra 128,242,49152
Ra 132,242,50176
Mvaa 160,140,76
s_add_u32 s64, s64, 1
X 190
Mvaa 164,140,92
K 218,188,189
Mvaa 164,136,88
X 191
Mvaa 168,136,104
Mvaa 168,140,108
X 160
Mvaa 172,140,124
K 219,190,191
Mvaa 172,136,120
W 2
X 161
Cvaa 176,144,64,224
Ra 136,242,51200
Ra 140,242,52224
Mvaa 176,148,68
X 162
Cvaa 180,148,84,228
K 196,160,161
Mvaa 180,144,80
X 163
Cvaa 184,144,96,232
Mvaa 184,148,100
X 164
Cvaa 188,148,116,236
K 197,162,163
Mvaa 188,144,112
X 165
Mvaa 176,152,72
Ra 144,242,53248
Ra 148,242,54272
Mvaa 176,156,76
X 166
Mvaa 180,156,92
K 204,164,165
Mvaa 180,152,88
X 167
Mvaa 184,152,104
Mvaa 184,156,108
X 168
Mvaa 188,156,124
Mvaa 188,152,120
W 2
Ra 152,242,55296
K 205,166,167
Mvav 32,128,192
X 169
Mvav 64,128,200
Ra 156,242,56320
v_xor_b32 v242, 0x10000, v242
Mvav 96,128,208
X 170
Mvav 128,128,216
K 212,168,169
Mvav 132,132,216
X 171
Mvav 100,132,208
X 172
Mvav 68,132,200
Mvav 36,132,192
K 213,170,171
Mvav 40,136,192
X 173
Mvav 72,136,200
X 174
Mvav 104,136,208
Mvav 136,136,216
K 220,172,173
Mvav 140,140,216
X 175
Mvav 108,140,208
X 176
Mvav 76,140,200
s_waitcnt vmcnt(0) lgkmcnt(0)
s_barrier
Mvav 44,140,192
Ra 128,242,40960
K 221,174,175
Mvav 48,144,192
X 177
Mvav 80,144,200
X 178
Mvav 112,144,208
Ra 132,242,41984
Mvav 144,144,216
K 198,176,177
Mvav 148,148,216
X 179
Mvav 116,148,208
X 180
Mvav 84,148,200
Ra 136,242,43008
Mvav 52,148,192
K 199,178,179
Mvav 56,152,192
X 181
Mvav 88,152,200
Ra 140,242,44032
Mvav 120,152,208
X 182
Mvav 152,152,216
K 206,180,181
Mvav 156,156,216
X 183
Mvav 124,156,208
Mvav 92,156,200
K 207,182,183
Mvav 60,156,192
s_cmp_lg_u32 s64, 15
s_cbranch_scc1 .Ll2_34%=
.Lf2_37%=:
W 0
Cvaa 160,128,64,224
.Lh_4_2%=:
s_cbranch_execz .Lh_4_2_1%=
v_bfe_u32 v28, v1, 4, 2
v_lshlrev_b32 v28, 3, v28
Mvaa 160,132,68
Ra 144,242,45056
Ra 148,242,46080
Cvaa 164,132,84,228
Ra 152,242,47104
buffer_load_dwordx2 v[28:29], v28, s[8:11], s54 offen sc1
Mvaa 164,128,80
s_lshl_b32 s92, s58, 2
s_add_u32 s91, s92, s51
s_add_u32 s95, s91, 3
Cvaa 168,128,96,232
s_and_b32 s95, s95, 15
s_mul_i32 s95, s95, 40960
s_mul_i32 s71, s51, 10240
Mvaa 168,132,100
s_add_u32 s71, s71, 0
s_add_u32 m0, s71, s48
s_lshl2_add_u32 s94, s48, s95
Cvaa 172,132,116,236
X 184
Mvaa 172,128,112
X 185
Mvaa 160,136,72
X 186
Mvaa 160,140,76
Ra 156,242,48128
K 214,184,185
Mvaa 164,140,92
X 187
Mvaa 164,136,88
D 16,40,94,0
Ra 128,242,57344
Mvaa 168,136,104
s_add_u32 s95, s91, 5
s_and_b32 s95, s95, 15
s_mul_i32 s95, s95, 40960
Mvaa 168,140,108
s_mul_i32 s71, s51, 10240
s_add_u32 s71, s71, 20480
Ra 132,242,58368
Mvaa 172,140,124
X 188
Mvaa 172,136,120
D 17,40,94,1024
K 215,186,187
v_smfmac_f32_16x16x64_bf16 a[160:163], a[176:179], v[192:199], v240
X 189
v_smfmac_f32_16x16x64_bf16 a[164:167], a[176:179], v[200:207], v240
X 190
K 222,188,189
X 191
s_nop 0
K 223,190,191
v_smfmac_f32_16x16x64_bf16 a[168:171], a[176:179], v[208:215], v240
D 18,40,94,2048
Ra 136,242,59392
)"
R"(v_smfmac_f32_16x16x64_bf16 a[172:175], a[176:179], v[216:223], v240
W 3
X 160
Cvaa 176,144,64,224
X 161
Mvaa 176,148,68
X 162
Cvaa 180,148,84,228
D 19,40,94,3072
K 192,160,161
Mvaa 180,144,80
s_add_u32 m0, m0, 4096
X 163
Cvaa 184,144,96,232
X 164
Mvaa 184,148,100
Ra 140,242,60416
K 193,162,163
Cvaa 188,148,116,236
buffer_load_dwordx4 v20, s[40:43], s94 offen lds
Mvaa 188,144,112
s_add_u32 m0, s71, s48
s_lshl2_add_u32 s94, s48, s95
Ra 144,242,61440
Mvaa 176,152,72
X 165
Mvaa 176,156,76
X 166
Mvaa 180,156,92
D 16,40,94,0
K 200,164,165
Mvaa 180,152,88
X 167
Mvaa 184,152,104
X 172
Mvaa 184,156,108
D 17,40,94,1024
K 201,166,167
Mvaa 188,156,124
X 173
Mvaa 188,152,120
W 1
Ra 148,242,62464
Ra 152,242,63488
Mvav 32,128,196
X 174
Mvav 64,128,204
D 18,40,94,2048
K 216,172,173
Mvav 96,128,212
X 175
Mvav 128,128,220
X 168
Mvav 132,132,220
s_mov_b32 s18, 40960
D 19,40,94,3072
K 217,174,175
Mvav 100,132,212
s_add_u32 m0, m0, 4096
X 169
Mvav 68,132,204
s_mov_b32 s19, 73728
X 170
Mvav 36,132,196
s_bitcmp1_b32 s14, 0
s_cselect_b32 s18, 49152, s18
s_cselect_b32 s19, 98304, s19
Ra 156,242,64512
.Lh_4_3%=:
s_cbranch_execz .Lh_4_3_1%=
K 208,168,169
Mvav 40,136,196
s_cmp_lt_u32 s14, 2
s_cselect_b32 s20, s18, s19
s_cselect_b32 s21, s19, s18
X 171
Mvav 72,136,204
X 184
Mvav 104,136,212
buffer_load_dwordx4 v20, s[40:43], s94 offen lds
K 209,170,171
Mvav 136,136,220
s_waitcnt vmcnt(10)
v_cmp_eq_u64 vcc, s[12:13], v[28:29]
s_cmp_eq_u64 vcc, -1
s_cselect_b32 s46, 79691776, 0
s_add_u32 s81, s58, 0
s_and_b32 s81, s81, 3
Mvav 140,140,220
s_lshl_b32 s81, s81, 12
s_add_u32 s81, s81, s49
Ra 128,242,49152
Mvav 108,140,212
X 185
Mvav 76,140,204
X 186
Mvav 44,140,196
W 1
buffer_load_dwordx4 a[184:187], v1, s[44:47], s81 offen offset:0 sc1
K 210,184,185
Mvav 48,144,196
s_add_u32 s82, s81, 131072
X 187
Mvav 80,144,204
v_add_u32 v2, s20, v1
v_add_u32 v3, s21, v1
Mvav 112,144,212
s_lshl_b32 s18, s14, 9
X 188
Mvav 144,144,220
Ra 132,242,50176
K 211,186,187
Mvav 148,148,220
buffer_load_dwordx4 a[200:203], v1, s[44:47], s82 offen offset:0 sc1
Ra 136,242,51200
Mvav 116,148,212
X 189
Mvav 84,148,204
v_lshrrev_b32 v232, 1, v1
v_add_u32 v232, s18, v232
Mvav 52,148,196
X 190
Mvav 56,152,196
K 218,188,189
v_bfe_u32 v12, v1, 6, 2
Mvav 88,152,204
buffer_load_dwordx4 a[188:191], v1, s[44:47], s81 offen offset:1024 sc1
Ra 140,242,52224
Mvav 120,152,212
X 191
Mvav 152,152,220
v_lshrrev_b32 v13, 8, v1
v_bfe_u32 v14, v1, 4, 1
Mvav 156,156,220
X 176
Mvav 124,156,212
K 219,190,191
v_lshlrev_b32 v14, 4, v14
Mvav 92,156,204
X 177
Mvav 60,156,196
buffer_load_dwordx4 a[204:207], v1, s[44:47], s82 offen offset:1024 sc1
v_mov_b32 v15, 0x3f80
Maav 168,180,208
X 178
K 194,176,177
X 179
X 180
K 195,178,179
Maav 172,180,216
X 181
X 182
K 202,180,181
X 183
s_nop 0
K 203,182,183
Maav 160,180,192
Ra 144,242,53248
v_lshlrev_b32 v15, v14, v15
Maav 164,180,200
W 1
buffer_load_dwordx4 a[192:195], v1, s[44:47], s81 offen offset:2048 sc1
v_bfe_u32 v14, v1, 5, 1
Mvav 96,128,208
Ra 148,242,54272
v_xor_b32 v27, v12, v13
v_lshl_or_b32 v27, v27, 1, v14
Mvav 128,128,216
v_cmp_eq_u32_e64 s[86:87], 0, v27
v_mov_b32 v226, 0
s_nop 0
v_cndmask_b32_e64 v224, 0, v15, s[86:87]
Mvav 132,132,216
Ra 152,242,55296
Ra 156,242,56320
buffer_load_dwordx4 a[208:211], v1, s[44:47], s82 offen offset:2048 sc1
Mvav 100,132,208
v_mov_b32 v228, 0
v_mov_b32 v230, v224
v_cmp_eq_u32_e64 s[86:87], 1, v27
Mvav 104,136,208
v_mov_b32 v227, 0
Mvav 136,136,216
buffer_load_dwordx4 a[196:199], v1, s[44:47], s81 offen offset:3072 sc1
v_cndmask_b32_e64 v225, 0, v15, s[86:87]
v_mov_b32 v229, 0
Mvav 140,140,216
v_mov_b32 v231, v225
G 4,160
G 8,161
Mvav 108,140,208
W 0
G 5,164
G 9,165
G 6,168
Mvav 112,144,208
buffer_load_dwordx4 a[212:215], v1, s[44:47], s82 offen offset:3072 sc1
s_add_u32 s81, s58, 1
s_and_b32 s81, s81, 3
s_lshl_b32 s81, s81, 12
Mvav 144,144,216
s_add_u32 s81, s81, s49
G 10,169
G 7,172
G 11,173
Mvav 148,148,216
A 4,4,8
A 5,5,9
A 6,6,10
Mvav 116,148,208
buffer_load_dwordx4 a[216:219], v1, s[44:47], s81 offen offset:0 sc1
s_add_u32 s82, s81, 131072
A 7,7,11
ds_write_b64 v232, v[6:7] offset:57344
Mvav 120,152,208
A 8,4,5
A 9,6,7
A 8,8,9
Mvav 152,152,216
v_cmp_ngt_f32_e64 s[86:87], s90, v8
s_lshl_b32 s19, s14, 2
v_mov_b32 v9, s19
s_or_b64 s[88:89], s[88:89], s[86:87]
Mvav 156,156,216
s_and_b64 s[88:89], s[88:89], s[24:25]
s_cselect_b32 s18, 1, 0
buffer_load_dwordx4 a[232:235], v1, s[44:47], s82 offen offset:0 sc1
v_mov_b32 v8, s18
ds_write_b32 v9, v8 offset:59392
Mvav 124,156,208
v_xor_b32 v10, 1024, v232
v_bfe_u32 v11, v1, 2, 4
K 96,96,97
Mvav 32,128,192
K 97,98,99
K 98,100,101
K 99,102,103
Mvav 64,128,200
ds_write_b128 v2, v[96:99] offset:0
K 128,128,129
K 129,130,131
Mvav 68,132,200
K 130,132,133
K 131,134,135
ds_write_b128 v2, v[128:131] offset:4096
Mvav 36,132,192
buffer_load_dwordx4 a[220:223], v1, s[44:47], s81 offen offset:1024 sc1
K 104,104,105
K 105,106,107
Mvav 40,136,192
K 106,108,109
K 107,110,111
ds_write_b128 v2, v[104:107] offset:1024
Mvav 72,136,200
K 136,136,137
K 137,138,139
K 138,140,141
Mvav 76,140,200
buffer_load_dwordx4 a[236:239], v1, s[44:47], s82 offen offset:1024 sc1
K 139,142,143
ds_write_b128 v2, v[136:139] offset:5120
Mvav 44,140,192
K 112,112,113
K 113,114,115
K 114,116,117
Mvav 48,144,192
K 115,118,119
ds_write_b128 v2, v[112:115] offset:2048
K 144,144,145
Mvav 80,144,200
K 145,146,147
K 146,148,149
K 147,150,151
Mvav 84,148,200
ds_write_b128 v2, v[144:147] offset:6144
buffer_load_dwordx4 a[224:227], v1, s[44:47], s81 offen offset:2048 sc1
K 120,120,121
Mvav 52,148,192
K 121,122,123
K 122,124,125
K 123,126,127
Mvav 56,152,192
ds_write_b128 v2, v[120:123] offset:3072
K 152,152,153
K 153,154,155
Mvav 88,152,200
buffer_load_dwordx4 a[240:243], v1, s[44:47], s82 offen offset:2048 sc1
K 154,156,157
K 155,158,159
Mvav 92,156,200
ds_write_b128 v2, v[152:155] offset:7168
Mvav 60,156,192
buffer_load_dwordx4 a[228:231], v1, s[44:47], s81 offen offset:3072 sc1
buffer_load_dwordx4 a[244:247], v1, s[44:47], s82 offen offset:3072 sc1
s_branch .Lend_39%=
.p2align 12
.Lfs_38%=:
v_smfmac_f32_16x16x64_bf16 a[160:163], a[176:179], v[192:199], v240
s_mov_b32 s46, 0
s_lshl_b32 s92, s58, 2
s_add_u32 s91, s92, s51
s_add_u32 s95, s91, 3
s_and_b32 s95, s95, 15
s_mul_i32 s95, s95, 40960
s_mul_i32 s71, s51, 10240
v_smfmac_f32_16x16x64_bf16 a[164:167], a[176:179], v[200:207], v240
s_add_u32 s71, s71, 0
s_add_u32 m0, s71, s48
s_lshl2_add_u32 s94, s48, s95
Ra 144,242,45056
Ra 148,242,46080
Ra 152,242,47104
Ra 156,242,48128
v_smfmac_f32_16x16x64_bf16 a[168:171], a[176:179], v[208:215], v240
D 16,40,94,0
s_add_u32 s95, s91, 5
s_and_b32 s95, s95, 15
s_mul_i32 s95, s95, 40960
s_mul_i32 s71, s51, 10240
s_add_u32 s71, s71, 20480
v_smfmac_f32_16x16x64_bf16 a[172:175], a[176:179], v[216:223], v240
W 4
Cvaa 168,128,96,232
v_mov_b32 v192, 0
G 12,160
Mvaa 168,132,100
D 17,40,94,1024
G 4,161
A 12,12,4
v_cmp_nlt_f32_e64 s[86:87], v12, s90
v_rcp_f32 v12, v12
s_or_b64 s[88:89], s[88:89], s[86:87]
Cvaa 172,132,116,236
v_accvgpr_write_b32 a160, 0
v_accvgpr_write_b32 a161, 0
v_accvgpr_write_b32 a162, 0
v_accvgpr_write_b32 a163, 0
G 13,164
G 5,165
Mvaa 172,128,112
v_mov_b32 v193, 0
A 13,13,5
v_cmp_nlt_f32_e64 s[86:87], v13, s90
v_rcp_f32 v13, v13
s_or_b64 s[88:89], s[88:89], s[86:87]
v_accvgpr_write_b32 a164, 0
Mvaa 168,136,104
v_accvgpr_write_b32 a165, 0
v_accvgpr_write_b32 a166, 0
v_accvgpr_write_b32 a167, 0
U 32,32,12
U 33,33,12
U 34,34,12
Mvaa 168,140,108
v_mov_b32 v200, 0
U 35,35,12
U 36,36,12
U 37,37,12
U 38,38,12
U 39,39,12
Mvaa 172,140,124
Ra 128,242,16384
Ra 132,242,17408
K 32,32,33
K 33,34,35
K 34,36,37
K 35,38,39
s_add_u32 s93, s67, 0
Mvaa 172,136,120
Ra 136,242,18432
Ra 140,242,19456
D 18,40,94,2048
v_mov_b32 v201, 0
W 4
Cvaa 188,144,112,236
Ra 144,242,20480
v_cndmask_b32_e64 v168, v241, v168, s[72:73]
v_cndmask_b32_e64 v169, v241, v169, s[74:75]
v_cndmask_b32_e64 v170, v241, v170, s[76:77]
Mvaa 188,148,116
buffer_store_dwordx4 v[32:35], v1, s[8:11], s93 offen offset:0 sc1
U 40,40,12
U 41,41,12
U 42,42,12
U 43,43,12
U 44,44,12
Mvaa 188,152,120
U 45,45,12
U 46,46,12
U 47,47,12
K 40,40,41
K 41,42,43
K 42,44,45
Mvaa 188,156,124
W 1
K 43,46,47
buffer_store_dwordx4 v[40:43], v1, s[8:11], s93 offen offset:1024 sc1
U 48,48,12
U 49,49,12
U 50,50,12
)"
R"(U 51,51,12
Mvav 96,128,212
U 52,52,12
U 53,53,12
U 54,54,12
U 55,55,12
K 48,48,49
K 49,50,51
Mvav 128,128,220
K 50,52,53
K 51,54,55
buffer_store_dwordx4 v[48:51], v1, s[8:11], s93 offen offset:2048 sc1
U 56,56,12
U 57,57,12
U 58,58,12
Mvav 132,132,220
Ra 148,242,21504
Ra 152,242,22528
Ra 156,242,23552
U 59,59,12
U 60,60,12
U 61,61,12
Mvav 100,132,212
D 19,40,94,3072
s_add_u32 m0, m0, 4096
U 62,62,12
U 63,63,12
K 56,56,57
K 57,58,59
K 58,60,61
Mvav 104,136,212
K 59,62,63
v_cndmask_b32_e64 v171, v241, v171, s[78:79]
X 168
X 169
Mvav 136,136,220
buffer_store_dwordx4 v[56:59], v1, s[8:11], s93 offen offset:3072 sc1
Ra 128,242,49152
G 14,168
G 6,169
A 14,14,6
v_cmp_nlt_f32_e64 s[86:87], v14, s90
Mvav 140,140,220
v_rcp_f32 v14, v14
s_or_b64 s[88:89], s[88:89], s[86:87]
v_accvgpr_write_b32 a168, 0
v_accvgpr_write_b32 a169, 0
v_accvgpr_write_b32 a170, 0
v_accvgpr_write_b32 a171, 0
Mvav 108,140,212
W 1
G 15,172
G 7,173
A 15,15,7
v_cmp_nlt_f32_e64 s[86:87], v15, s90
v_rcp_f32 v15, v15
Mvav 112,144,212
s_or_b64 s[88:89], s[88:89], s[86:87]
buffer_load_dwordx4 v20, s[40:43], s94 offen lds
v_accvgpr_write_b32 a172, 0
v_accvgpr_write_b32 a173, 0
v_accvgpr_write_b32 a174, 0
v_accvgpr_write_b32 a175, 0
U 64,64,13
Mvav 144,144,220
s_add_u32 m0, s71, s48
s_lshl2_add_u32 s94, s48, s95
U 65,65,13
U 66,66,13
U 67,67,13
U 68,68,13
U 69,69,13
Mvav 148,148,220
U 70,70,13
U 71,71,13
K 64,64,65
K 65,66,67
K 66,68,69
K 67,70,71
s_add_u32 s93, s67, 131072
Mvav 116,148,212
buffer_store_dwordx4 v[64:67], v1, s[8:11], s93 offen offset:0 sc1
U 72,72,13
U 73,73,13
U 74,74,13
U 75,75,13
U 76,76,13
Mvav 120,152,212
Ra 132,242,50176
Ra 136,242,51200
Ra 140,242,52224
U 77,77,13
U 78,78,13
U 79,79,13
Mvav 152,152,220
K 72,72,73
K 73,74,75
K 74,76,77
K 75,78,79
buffer_store_dwordx4 v[72:75], v1, s[8:11], s93 offen offset:1024 sc1
U 80,80,13
Mvav 156,156,220
X 170
K 208,168,169
X 171
X 172
K 209,170,171
X 173
X 174
K 216,172,173
X 175
v_mov_b32 v194, 0
K 217,174,175
v_mov_b32 v195, 0
v_mov_b32 v202, 0
v_mov_b32 v203, 0
v_mov_b32 v210, 0
v_mov_b32 v211, 0
Mvav 124,156,212
v_cndmask_b32_e64 v188, v241, v188, s[72:73]
v_cndmask_b32_e64 v189, v241, v189, s[74:75]
v_cndmask_b32_e64 v190, v241, v190, s[76:77]
v_cndmask_b32_e64 v191, v241, v191, s[78:79]
X 188
X 189
X 190
K 218,188,189
X 191
s_nop 0
K 219,190,191
Maav 168,180,208
D 16,40,94,0
U 81,81,13
U 82,82,13
U 83,83,13
U 84,84,13
U 85,85,13
Maav 172,180,216
W 0
U 86,86,13
U 87,87,13
K 80,80,81
K 81,82,83
K 82,84,85
K 83,86,87
Zvav 32,128,192
buffer_store_dwordx4 v[80:83], v1, s[8:11], s93 offen offset:2048 sc1
U 88,88,13
U 89,89,13
U 90,90,13
U 91,91,13
U 92,92,13
Zvav 64,128,200
U 93,93,13
U 94,94,13
U 95,95,13
K 88,88,89
K 89,90,91
K 90,92,93
K 91,94,95
buffer_store_dwordx4 v[88:91], v1, s[8:11], s93 offen offset:3072 sc1
U 96,96,14
U 97,97,14
U 98,98,14
U 99,99,14
U 100,100,14
U 101,101,14
U 102,102,14
U 103,103,14
K 96,96,97
K 97,98,99
K 98,100,101
K 99,102,103
s_add_u32 s93, s67, 262144
buffer_store_dwordx4 v[96:99], v1, s[8:11], s93 offen offset:0 sc1
s_nop 1
Zvav 96,128,208
U 104,104,14
U 105,105,14
U 106,106,14
U 107,107,14
U 108,108,14
U 109,109,14
U 110,110,14
U 111,111,14
K 104,104,105
K 105,106,107
K 106,108,109
K 107,110,111
buffer_store_dwordx4 v[104:107], v1, s[8:11], s93 offen offset:1024 sc1
U 112,112,14
U 113,113,14
U 114,114,14
U 115,115,14
U 116,116,14
U 117,117,14
U 118,118,14
U 119,119,14
K 112,112,113
K 113,114,115
K 114,116,117
K 115,118,119
buffer_store_dwordx4 v[112:115], v1, s[8:11], s93 offen offset:2048 sc1
U 120,120,14
U 121,121,14
U 122,122,14
U 123,123,14
U 124,124,14
U 125,125,14
U 126,126,14
U 127,127,14
K 120,120,121
K 121,122,123
K 122,124,125
K 123,126,127
buffer_store_dwordx4 v[120:123], v1, s[8:11], s93 offen offset:3072 sc1
U 128,128,15
U 129,129,15
U 130,130,15
U 131,131,15
U 132,132,15
U 133,133,15
U 134,134,15
U 135,135,15
K 128,128,129
K 129,130,131
K 130,132,133
K 131,134,135
s_add_u32 s93, s67, 393216
buffer_store_dwordx4 v[128:131], v1, s[8:11], s93 offen offset:0 sc1
s_nop 1
Zvav 128,128,216
Ra 144,242,53248
U 136,136,15
U 137,137,15
U 138,138,15
U 139,139,15
U 140,140,15
Zvav 132,132,216
D 17,40,94,1024
U 141,141,15
U 142,142,15
U 143,143,15
K 136,136,137
K 137,138,139
Zvav 100,132,208
Ra 148,242,54272
Ra 152,242,55296
Ra 156,242,56320
K 138,140,141
K 139,142,143
Zvav 68,132,200
D 18,40,94,2048
Zvav 36,132,192
Zvav 40,136,192
D 19,40,94,3072
s_add_u32 m0, m0, 4096
Zvav 72,136,200
Zvav 104,136,208
buffer_store_dwordx4 v[136:139], v1, s[8:11], s93 offen offset:1024 sc1
U 144,144,15
U 145,145,15
U 146,146,15
U 147,147,15
U 148,148,15
Zvav 136,136,216
U 149,149,15
U 150,150,15
U 151,151,15
K 144,144,145
K 145,146,147
K 146,148,149
Zvav 140,140,216
K 147,150,151
buffer_store_dwordx4 v[144:147], v1, s[8:11], s93 offen offset:2048 sc1
U 152,152,15
U 153,153,15
U 154,154,15
U 155,155,15
Zvav 108,140,208
U 156,156,15
U 157,157,15
U 158,158,15
U 159,159,15
K 152,152,153
K 153,154,155
Zvav 76,140,200
buffer_load_dwordx4 v20, s[40:43], s94 offen lds
K 154,156,157
K 155,158,159
Zvav 44,140,192
W 0
Zvav 48,144,192
buffer_store_dwordx4 v[152:155], v1, s[8:11], s93 offen offset:3072 sc1
s_mov_b32 s18, 40960
s_mov_b32 s19, 73728
s_bitcmp1_b32 s14, 0
s_cselect_b32 s18, 49152, s18
s_cselect_b32 s19, 98304, s19
s_cmp_lt_u32 s14, 2
s_cselect_b32 s20, s18, s19
s_cselect_b32 s21, s19, s18
v_add_u32 v2, s20, v1
v_add_u32 v3, s21, v1
Zvav 80,144,200
s_lshl_b32 s18, s14, 9
v_lshrrev_b32 v232, 1, v1
v_add_u32 v232, s18, v232
v_bfe_u32 v12, v1, 6, 2
v_lshrrev_b32 v13, 8, v1
v_bfe_u32 v14, v1, 4, 1
v_lshlrev_b32 v14, 4, v14
Zvav 112,144,208
v_mov_b32 v15, 0x3f80
v_lshlrev_b32 v15, v14, v15
v_bfe_u32 v14, v1, 5, 1
v_xor_b32 v27, v12, v13
v_lshl_or_b32 v27, v27, 1, v14
v_cmp_eq_u32_e64 s[86:87], 0, v27
Zvav 144,144,216
v_mov_b32 v226, 0
Zvav 148,148,216
v_cndmask_b32_e64 v224, 0, v15, s[86:87]
v_mov_b32 v228, 0
v_mov_b32 v230, v224
v_cmp_eq_u32_e64 s[86:87], 1, v27
v_mov_b32 v227, 0
s_nop 0
v_cndmask_b32_e64 v225, 0, v15, s[86:87]
Zvav 116,148,208
v_mov_b32 v229, 0
v_mov_b32 v231, v225
G 4,160
G 8,161
G 5,164
G 9,165
Zvav 84,148,200
G 6,168
G 10,169
G 7,172
G 11,173
A 4,4,8
A 5,5,9
Zvav 52,148,192
A 6,6,10
A 7,7,11
ds_write_b64 v232, v[6:7] offset:57344
A 8,4,5
A 9,6,7
A 8,8,9
Zvav 56,152,192
v_cmp_ngt_f32_e64 s[86:87], s90, v8
s_lshl_b32 s19, s14, 2
v_mov_b32 v9, s19
s_or_b64 s[88:89], s[88:89], s[86:87]
s_and_b64 s[88:89], s[88:89], s[24:25]
s_cselect_b32 s18, 1, 0
v_mov_b32 v8, s18
ds_write_b32 v9, v8 offset:59392
Zvav 88,152,200
v_xor_b32 v10, 1024, v232
v_bfe_u32 v11, v1, 2, 4
K 96,96,97
K 97,98,99
K 98,100,101
K 99,102,103
Zvav 120,152,208
ds_write_b128 v2, v[96:99] offset:0
K 128,128,129
K 129,130,131
K 130,132,133
K 131,134,135
ds_write_b128 v2, v[128:131] offset:4096
Zvav 152,152,216
K 104,104,105
K 105,106,107
K 106,108,109
K 107,110,111
ds_write_b128 v2, v[104:107] offset:1024
K 136,136,137
Zvav 156,156,216
K 137,138,139
K 138,140,141
K 139,142,143
ds_write_b128 v2, v[136:139] offset:5120
K 112,112,113
K 113,114,115
Zvav 124,156,208
K 114,116,117
K 115,118,119
ds_write_b128 v2, v[112:115] offset:2048
K 144,144,145
K 145,146,147
K 146,148,149
Zvav 92,156,200
K 147,150,151
ds_write_b128 v2, v[144:147] offset:6144
Zvav 60,156,192
K 120,120,121
K 121,122,123
)"
R"(K 122,124,125
K 123,126,127
ds_write_b128 v2, v[120:123] offset:3072
K 152,152,153
K 153,154,155
K 154,156,157
K 155,158,159
ds_write_b128 v2, v[152:155] offset:7168
s_waitcnt vmcnt(0)
s_and_b64 s[86:87], s[88:89], s[24:25]
s_cbranch_scc1 .LL_46%=
v_mov_b64 v[28:29], s[12:13]
s_mov_b64 exec, 1
buffer_store_dwordx2 v[28:29], off, s[8:11], s69 sc1
s_mov_b64 exec, -1
.LL_46%=:
s_mov_b32 s70, 0
s_branch .Lend_39%=
.p2align 12
.Lpd_28%=:
v_pk_mul_f32 v[32:33], v[32:33], s[22:23]
v_pk_mul_f32 v[34:35], v[34:35], s[22:23]
v_pk_mul_f32 v[36:37], v[36:37], s[22:23]
v_pk_mul_f32 v[38:39], v[38:39], s[22:23]
v_pk_mul_f32 v[64:65], v[64:65], s[22:23]
v_pk_mul_f32 v[66:67], v[66:67], s[22:23]
v_pk_mul_f32 v[68:69], v[68:69], s[22:23]
v_pk_mul_f32 v[70:71], v[70:71], s[22:23]
v_pk_mul_f32 v[96:97], v[96:97], s[22:23]
v_pk_mul_f32 v[98:99], v[98:99], s[22:23]
v_pk_mul_f32 v[100:101], v[100:101], s[22:23]
v_pk_mul_f32 v[102:103], v[102:103], s[22:23]
v_pk_mul_f32 v[128:129], v[128:129], s[22:23]
v_pk_mul_f32 v[130:131], v[130:131], s[22:23]
v_pk_mul_f32 v[132:133], v[132:133], s[22:23]
v_pk_mul_f32 v[134:135], v[134:135], s[22:23]
v_pk_mul_f32 v[40:41], v[40:41], s[22:23]
v_pk_mul_f32 v[42:43], v[42:43], s[22:23]
v_pk_mul_f32 v[44:45], v[44:45], s[22:23]
v_pk_mul_f32 v[46:47], v[46:47], s[22:23]
v_pk_mul_f32 v[72:73], v[72:73], s[22:23]
v_pk_mul_f32 v[74:75], v[74:75], s[22:23]
v_pk_mul_f32 v[76:77], v[76:77], s[22:23]
v_pk_mul_f32 v[78:79], v[78:79], s[22:23]
v_pk_mul_f32 v[104:105], v[104:105], s[22:23]
v_pk_mul_f32 v[106:107], v[106:107], s[22:23]
v_pk_mul_f32 v[108:109], v[108:109], s[22:23]
v_pk_mul_f32 v[110:111], v[110:111], s[22:23]
v_pk_mul_f32 v[136:137], v[136:137], s[22:23]
v_pk_mul_f32 v[138:139], v[138:139], s[22:23]
v_pk_mul_f32 v[140:141], v[140:141], s[22:23]
v_pk_mul_f32 v[142:143], v[142:143], s[22:23]
v_pk_mul_f32 v[48:49], v[48:49], s[22:23]
v_pk_mul_f32 v[50:51], v[50:51], s[22:23]
v_pk_mul_f32 v[52:53], v[52:53], s[22:23]
v_pk_mul_f32 v[54:55], v[54:55], s[22:23]
v_pk_mul_f32 v[80:81], v[80:81], s[22:23]
v_pk_mul_f32 v[82:83], v[82:83], s[22:23]
v_pk_mul_f32 v[84:85], v[84:85], s[22:23]
v_pk_mul_f32 v[86:87], v[86:87], s[22:23]
v_pk_mul_f32 v[112:113], v[112:113], s[22:23]
v_pk_mul_f32 v[114:115], v[114:115], s[22:23]
v_pk_mul_f32 v[116:117], v[116:117], s[22:23]
v_pk_mul_f32 v[118:119], v[118:119], s[22:23]
v_pk_mul_f32 v[144:145], v[144:145], s[22:23]
v_pk_mul_f32 v[146:147], v[146:147], s[22:23]
v_pk_mul_f32 v[148:149], v[148:149], s[22:23]
v_pk_mul_f32 v[150:151], v[150:151], s[22:23]
v_pk_mul_f32 v[56:57], v[56:57], s[22:23]
v_pk_mul_f32 v[58:59], v[58:59], s[22:23]
v_pk_mul_f32 v[60:61], v[60:61], s[22:23]
v_pk_mul_f32 v[62:63], v[62:63], s[22:23]
v_pk_mul_f32 v[88:89], v[88:89], s[22:23]
v_pk_mul_f32 v[90:91], v[90:91], s[22:23]
v_pk_mul_f32 v[92:93], v[92:93], s[22:23]
v_pk_mul_f32 v[94:95], v[94:95], s[22:23]
v_pk_mul_f32 v[120:121], v[120:121], s[22:23]
v_pk_mul_f32 v[122:123], v[122:123], s[22:23]
v_pk_mul_f32 v[124:125], v[124:125], s[22:23]
v_pk_mul_f32 v[126:127], v[126:127], s[22:23]
v_pk_mul_f32 v[152:153], v[152:153], s[22:23]
v_pk_mul_f32 v[154:155], v[154:155], s[22:23]
v_pk_mul_f32 v[156:157], v[156:157], s[22:23]
v_pk_mul_f32 v[158:159], v[158:159], s[22:23]
K 32,32,33
K 33,34,35
K 34,36,37
K 35,38,39
K 40,40,41
K 41,42,43
K 42,44,45
K 43,46,47
W 0
Zvav 196,128,32
s_add_u32 m0, s62, 0
Ra 144,242,4096
Ra 148,242,5120
Ra 152,242,6144
Ra 156,242,7168
Mvav 196,132,40
K 64,64,65
K 65,66,67
K 66,68,69
K 67,70,71
K 72,72,73
K 73,74,75
K 74,76,77
K 75,78,79
s_nop 1
Zvav 204,132,72
Dc 1,8,61,0
K 96,96,97
K 97,98,99
K 98,100,101
Mvav 204,128,64
K 99,102,103
K 104,104,105
K 105,106,107
K 106,108,109
K 107,110,111
Zvav 212,128,96
Dc 1,8,61,1024
K 128,128,129
K 129,130,131
K 130,132,133
Mvav 212,132,104
K 131,134,135
K 136,136,137
K 137,138,139
K 138,140,141
K 139,142,143
s_nop 1
Zvav 220,132,136
Dc 1,8,61,2048
K 48,48,49
K 49,50,51
K 50,52,53
Mvav 220,128,128
K 51,54,55
K 56,56,57
K 57,58,59
K 58,60,61
K 59,62,63
Mvav 196,136,48
Dc 1,8,61,3072
s_add_u32 m0, s62, 4096
Ra 128,242,8192
Ra 132,242,9216
K 80,80,81
Mvav 196,140,56
K 81,82,83
K 82,84,85
K 83,86,87
K 88,88,89
K 89,90,91
K 90,92,93
K 91,94,95
s_nop 1
Mvav 204,140,88
Dc 243,8,61,0
K 112,112,113
K 113,114,115
K 114,116,117
Mvav 204,136,80
K 115,118,119
K 120,120,121
K 121,122,123
K 122,124,125
K 123,126,127
Mvav 212,136,112
Dc 243,8,61,1024
K 144,144,145
K 145,146,147
K 146,148,149
Mvav 212,140,120
K 147,150,151
K 152,152,153
K 153,154,155
K 154,156,157
K 155,158,159
s_nop 1
Mvav 220,140,152
Dc 243,8,61,2048
v_accvgpr_write_b32 a0, v32
v_accvgpr_write_b32 a1, v33
v_accvgpr_write_b32 a2, v34
Mvav 220,136,144
W 2
Dc 243,8,61,3072
s_add_u32 m0, s62, 8192
v_accvgpr_write_b32 a3, v35
v_accvgpr_write_b32 a4, v40
Zvav 68,144,64
Dc 244,8,61,0
v_accvgpr_write_b32 a5, v41
v_accvgpr_write_b32 a6, v42
v_accvgpr_write_b32 a7, v43
Mvav 68,148,72
Dc 244,8,61,1024
v_accvgpr_write_b32 a8, v48
v_accvgpr_write_b32 a9, v49
v_accvgpr_write_b32 a10, v50
Zvav 100,148,104
Dc 244,8,61,2048
Ra 136,242,10240
Ra 140,242,11264
v_accvgpr_write_b32 a11, v51
Mvav 100,144,96
Dc 244,8,61,3072
s_add_u32 m0, s62, 12288
v_accvgpr_write_b32 a12, v56
v_accvgpr_write_b32 a13, v57
v_accvgpr_write_b32 a14, v58
Zvav 132,144,128
Dc 245,8,61,0
v_accvgpr_write_b32 a15, v59
v_accvgpr_write_b32 a16, v64
v_cndmask_b32_e64 v196, v241, v196, s[72:73]
Mvav 132,148,136
Dc 245,8,61,1024
v_cndmask_b32_e64 v197, v241, v197, s[74:75]
v_cndmask_b32_e64 v198, v241, v198, s[76:77]
v_cndmask_b32_e64 v199, v241, v199, s[78:79]
Mvav 68,152,80
Dc 245,8,61,2048
v_max3_f32 v4, v196, v197, v198
v_max3_f32 v4, v4, v199, v252
v_max3_f32 v5, v204, v205, v206
Mvav 68,156,88
v_max3_f32 v5, v5, v207, v253
v_max3_f32 v6, v212, v213, v214
v_max3_f32 v6, v6, v215, v254
v_max3_f32 v7, v220, v221, v222
Mvav 100,156,120
v_max3_f32 v7, v7, v223, v255
v_mov_b32 v8, v4
v_mov_b32 v9, v5
v_mov_b32 v10, v6
Mvav 100,152,112
v_mov_b32 v11, v7
v_permlane32_swap_b32 v4, v8
v_permlane32_swap_b32 v5, v9
v_accvgpr_write_b32 a17, v65
Mvav 132,152,144
v_permlane32_swap_b32 v6, v10
v_permlane32_swap_b32 v7, v11
v_max_f32 v4, v4, v8
v_max_f32 v5, v5, v9
v_max_f32 v6, v6, v10
v_max_f32 v7, v7, v11
v_mov_b32 v8, v4
v_mov_b32 v9, v5
v_mov_b32 v10, v6
v_mov_b32 v11, v7
v_permlane16_swap_b32 v4, v8
v_permlane16_swap_b32 v5, v9
v_permlane16_swap_b32 v6, v10
v_permlane16_swap_b32 v7, v11
v_max_f32 v4, v4, v8
v_max_f32 v5, v5, v9
v_max_f32 v6, v6, v10
v_max_f32 v7, v7, v11
v_sub_f32 v4, 0, v4
v_sub_f32 v5, 0, v5
v_sub_f32 v6, 0, v6
v_sub_f32 v7, 0, v7
v_mov_b32 v224, v4
v_mov_b32 v225, v4
v_mov_b32 v226, v4
v_mov_b32 v227, v4
v_mov_b32 v228, v5
v_mov_b32 v229, v5
v_mov_b32 v230, v5
v_mov_b32 v231, v5
v_mov_b32 v232, v6
v_mov_b32 v233, v6
v_mov_b32 v234, v6
v_mov_b32 v235, v6
v_mov_b32 v236, v7
v_mov_b32 v237, v7
v_mov_b32 v238, v7
v_mov_b32 v239, v7
Mvav 132,156,152
W 0
Dc 245,8,61,3072
v_accvgpr_write_b32 a18, v66
v_accvgpr_write_b32 a19, v67
v_accvgpr_write_b32 a20, v72
Cvav 168,128,96,232
s_add_u32 s61, s61, s63
s_add_u32 s36, s29, 65536
v_accvgpr_write_b32 a21, v73
v_accvgpr_write_b32 a22, v74
v_accvgpr_write_b32 a23, v75
Mvav 168,132,104
s_and_b32 s80, s14, 1
buffer_store_dwordx4 a[192:195], v1, s[8:11], s36 offen offset:0 sc1
v_accvgpr_write_b32 a24, v80
v_accvgpr_write_b32 a25, v81
v_accvgpr_write_b32 a26, v82
Cvav 172,132,136,236
s_lshl_b32 s80, s80, 14
Ra 144,242,12288
Ra 148,242,13312
Ra 152,242,14336
Ra 156,242,15360
Mvav 172,128,128
s_add_u32 s80, s80, 98304
v_accvgpr_write_b32 a27, v83
v_accvgpr_write_b32 a28, v88
v_accvgpr_write_b32 a29, v89
v_accvgpr_write_b32 a30, v90
Mvav 168,136,112
buffer_store_dwordx4 a[200:203], v1, s[8:11], s36 offen offset:1024 sc1
v_accvgpr_write_b32 a31, v91
v_accvgpr_write_b32 a32, v96
v_accvgpr_write_b32 a33, v97
Mvav 168,140,120
v_accvgpr_write_b32 a34, v98
v_accvgpr_write_b32 a35, v99
v_accvgpr_write_b32 a36, v104
v_accvgpr_write_b32 a37, v105
Mvav 172,140,152
Ra 128,242,24576
Ra 132,242,25600
buffer_store_dwordx4 a[208:211], v1, s[8:11], s36 offen offset:2048 sc1
v_accvgpr_write_b32 a38, v106
Mvav 172,136,144
Ra 136,242,26624
Ra 140,242,27648
v_accvgpr_write_b32 a39, v107
W 4
Cvav 188,144,128,236
v_accvgpr_write_b32 a40, v112
v_accvgpr_write_b32 a41, v113
v_accvgpr_write_b32 a42, v114
v_accvgpr_write_b32 a43, v115
v_accvgpr_write_b32 a44, v120
v_accvgpr_write_b32 a45, v121
v_accvgpr_write_b32 a46, v122
v_accvgpr_write_b32 a47, v123
v_accvgpr_write_b32 a48, v128
v_accvgpr_write_b32 a49, v129
v_accvgpr_write_b32 a50, v130
v_accvgpr_write_b32 a51, v131
v_accvgpr_write_b32 a52, v136
v_accvgpr_write_b32 a53, v137
v_accvgpr_write_b32 a54, v138
v_accvgpr_write_b32 a55, v139
v_accvgpr_write_b32 a56, v144
v_accvgpr_write_b32 a57, v145
v_accvgpr_write_b32 a58, v146
v_accvgpr_write_b32 a59, v147
v_accvgpr_write_b32 a60, v152
v_accvgpr_write_b32 a61, v153
v_accvgpr_write_b32 a62, v154
v_accvgpr_write_b32 a63, v155
Mvav 188,148,136
s_barrier
buffer_store_dwordx4 a[216:219], v1, s[8:11], s36 offen offset:3072 sc1
s_add_u32 s36, s29, 69632
v_add_u32 v8, s80, v1
A 196,196,224
Mvav 188,152,144
A 197,197,224
A 198,198,224
A 199,199,224
X 196
X 197
X 198
K 192,196,197
X 199
A 204,204,228
K 193,198,199
A 205,205,228
A 206,206,228
A 207,207,228
X 204
X 205
X 206
K 200,204,205
X 207
A 212,212,232
K 201,206,207
A 213,213,232
A 214,214,232
A 215,215,232
X 212
X 213
X 214
K 208,212,213
X 215
A 220,220,236
K 209,214,215
A 221,221,236
A 222,222,236
A 223,223,236
X 220
X 221
X 222
)"
R"(K 216,220,221
X 223
v_mov_b32 v194, 0
K 217,222,223
v_mov_b32 v195, 0
v_cndmask_b32_e64 v68, v241, v68, s[72:73]
v_cndmask_b32_e64 v69, v241, v69, s[74:75]
v_cndmask_b32_e64 v70, v241, v70, s[76:77]
v_cndmask_b32_e64 v71, v241, v71, s[78:79]
A 68,68,228
A 69,69,228
A 70,70,228
A 71,71,228
X 68
X 69
X 70
K 202,68,69
X 71
A 100,100,232
K 203,70,71
A 101,101,232
A 102,102,232
A 103,103,232
X 100
X 101
X 102
K 210,100,101
X 103
A 132,132,236
K 211,102,103
A 133,133,236
A 134,134,236
A 135,135,236
X 132
X 133
X 134
K 218,132,133
X 135
s_nop 0
K 219,134,135
Mvav 188,156,152
W 0
buffer_store_dwordx4 a[224:227], v1, s[8:11], s36 offen offset:0 sc1
Ra 144,242,28672
Ra 148,242,29696
Ra 152,242,30720
Zvav 32,128,192
buffer_store_dwordx4 a[232:235], v1, s[8:11], s36 offen offset:1024 sc1
Ra 156,242,31744
Zvav 64,128,200
buffer_store_dwordx4 a[240:243], v1, s[8:11], s36 offen offset:2048 sc1
v_xor_b32 v242, 0x10000, v242
Zvav 96,128,208
s_and_b32 s80, s14, 1
s_xor_b32 s62, s62, 0x10000
buffer_store_dwordx4 a[248:251], v1, s[8:11], s36 offen offset:3072 sc1
s_add_u32 s36, s29, 393216
Zvav 128,128,216
s_lshl_b32 s80, s80, 14
s_add_u32 s80, s80, 131072
buffer_store_dwordx4 a[196:199], v1, s[8:11], s36 offen offset:0 sc1
v_add_u32 v23, 61440, v21
Zvav 132,132,216
buffer_store_dwordx4 a[204:207], v1, s[8:11], s36 offen offset:1024 sc1
v_add_u32 v12, s80, v1
Ra 64,12,8192
v_add_u32 v24, 61440, v22
Zvav 100,132,208
buffer_store_dwordx4 a[212:215], v1, s[8:11], s36 offen offset:2048 sc1
Ra 68,12,9216
Ra 72,12,10240
v_add_u32 v25, 122880, v21
Zvav 68,132,200
buffer_store_dwordx4 a[220:223], v1, s[8:11], s36 offen offset:3072 sc1
s_add_u32 s36, s29, 397312
Ra 76,12,11264
Ra 80,12,12288
Ra 84,12,13312
Zvav 36,132,192
buffer_store_dwordx4 a[228:231], v1, s[8:11], s36 offen offset:0 sc1
Ra 88,12,14336
Ra 92,12,15360
v_add_u32 v26, 122880, v22
Zvav 40,136,192
buffer_store_dwordx4 a[236:239], v1, s[8:11], s36 offen offset:1024 sc1
Ra 96,12,0
Ra 100,12,1024
Ra 104,12,2048
Zvav 72,136,200
Ra 108,12,3072
Ra 112,12,4096
buffer_store_dwordx4 a[244:247], v1, s[8:11], s36 offen offset:2048 sc1
Ra 116,12,5120
Zvav 104,136,208
Ra 120,12,6144
Ra 124,12,7168
buffer_store_dwordx4 a[252:255], v1, s[8:11], s36 offen offset:3072 sc1
Zvav 136,136,216
Zvav 140,140,216
Zvav 108,140,208
Zvav 76,140,200
s_waitcnt vmcnt(16) lgkmcnt(0)
s_barrier
ds_read_b128 v[248:251], v8
Zvav 44,140,192
Ra 128,242,40960
Zvav 48,144,192
Zvav 80,144,200
X 172
X 173
Zvav 112,144,208
Ra 132,242,41984
X 174
K 220,172,173
Zvav 144,144,216
X 175
v_cndmask_b32_e64 v168, v241, v168, s[72:73]
K 221,174,175
Zvav 148,148,216
v_cndmask_b32_e64 v169, v241, v169, s[74:75]
v_cndmask_b32_e64 v170, v241, v170, s[76:77]
v_cndmask_b32_e64 v171, v241, v171, s[78:79]
Zvav 116,148,208
X 168
X 169
Zvav 84,148,200
W 2
Ra 136,242,43008
X 170
K 212,168,169
Zvav 52,148,192
v_mov_b32 v224, v250
v_mov_b32 v225, v250
v_mov_b32 v226, v250
v_mov_b32 v227, v250
Zvav 56,152,192
v_mov_b32 v228, v251
v_mov_b32 v229, v251
v_mov_b32 v230, v251
v_mov_b32 v231, v251
Zvav 88,152,200
v_mov_b32 v232, v248
v_mov_b32 v233, v248
v_mov_b32 v234, v248
v_mov_b32 v235, v248
Zvav 120,152,208
v_mov_b32 v236, v249
v_mov_b32 v237, v249
v_mov_b32 v238, v249
v_mov_b32 v239, v249
Zvav 152,152,216
X 171
v_mov_b32 v204, 0
K 213,170,171
Zvav 156,156,216
Ra 140,242,44032
v_mov_b32 v205, 0
v_mov_b32 v196, 0
v_mov_b32 v197, 0
Zvav 124,156,208
v_mov_b32 v198, 0
v_mov_b32 v199, 0
v_mov_b32 v206, 0
v_mov_b32 v207, 0
Zvav 92,156,200
v_mov_b32 v214, 0
v_mov_b32 v215, 0
v_cndmask_b32_e64 v188, v241, v188, s[72:73]
v_cndmask_b32_e64 v189, v241, v189, s[74:75]
Zvav 60,156,192
v_cndmask_b32_e64 v190, v241, v190, s[76:77]
v_cndmask_b32_e64 v191, v241, v191, s[78:79]
X 188
X 189
X 190
K 222,188,189
X 191
s_nop 0
K 223,190,191
s_waitcnt vmcnt(0)
s_add_u32 s80, s71, 128
v_mov_b64 v[28:29], s[12:13]
s_mov_b64 exec, 1
buffer_store_dwordx2 v[28:29], off, s[8:11], s80 sc1
s_mov_b64 exec, -1
s_nop 1
s_branch .Lsp2_33%=
.p2align 12
.Lpe_44%=:
v_pk_mul_f32 v[32:33], v[32:33], s[22:23]
v_pk_mul_f32 v[34:35], v[34:35], s[22:23]
v_pk_mul_f32 v[36:37], v[36:37], s[22:23]
v_pk_mul_f32 v[38:39], v[38:39], s[22:23]
v_pk_mul_f32 v[64:65], v[64:65], s[22:23]
v_pk_mul_f32 v[66:67], v[66:67], s[22:23]
v_pk_mul_f32 v[68:69], v[68:69], s[22:23]
v_pk_mul_f32 v[70:71], v[70:71], s[22:23]
v_pk_mul_f32 v[96:97], v[96:97], s[22:23]
v_pk_mul_f32 v[98:99], v[98:99], s[22:23]
v_pk_mul_f32 v[100:101], v[100:101], s[22:23]
v_pk_mul_f32 v[102:103], v[102:103], s[22:23]
v_pk_mul_f32 v[128:129], v[128:129], s[22:23]
v_pk_mul_f32 v[130:131], v[130:131], s[22:23]
v_pk_mul_f32 v[132:133], v[132:133], s[22:23]
v_pk_mul_f32 v[134:135], v[134:135], s[22:23]
v_pk_mul_f32 v[40:41], v[40:41], s[22:23]
v_pk_mul_f32 v[42:43], v[42:43], s[22:23]
v_pk_mul_f32 v[44:45], v[44:45], s[22:23]
v_pk_mul_f32 v[46:47], v[46:47], s[22:23]
v_pk_mul_f32 v[72:73], v[72:73], s[22:23]
v_pk_mul_f32 v[74:75], v[74:75], s[22:23]
v_pk_mul_f32 v[76:77], v[76:77], s[22:23]
v_pk_mul_f32 v[78:79], v[78:79], s[22:23]
v_pk_mul_f32 v[104:105], v[104:105], s[22:23]
v_pk_mul_f32 v[106:107], v[106:107], s[22:23]
v_pk_mul_f32 v[108:109], v[108:109], s[22:23]
v_pk_mul_f32 v[110:111], v[110:111], s[22:23]
v_pk_mul_f32 v[136:137], v[136:137], s[22:23]
v_pk_mul_f32 v[138:139], v[138:139], s[22:23]
v_pk_mul_f32 v[140:141], v[140:141], s[22:23]
v_pk_mul_f32 v[142:143], v[142:143], s[22:23]
v_pk_mul_f32 v[48:49], v[48:49], s[22:23]
v_pk_mul_f32 v[50:51], v[50:51], s[22:23]
v_pk_mul_f32 v[52:53], v[52:53], s[22:23]
v_pk_mul_f32 v[54:55], v[54:55], s[22:23]
v_pk_mul_f32 v[80:81], v[80:81], s[22:23]
v_pk_mul_f32 v[82:83], v[82:83], s[22:23]
v_pk_mul_f32 v[84:85], v[84:85], s[22:23]
v_pk_mul_f32 v[86:87], v[86:87], s[22:23]
v_pk_mul_f32 v[112:113], v[112:113], s[22:23]
v_pk_mul_f32 v[114:115], v[114:115], s[22:23]
v_pk_mul_f32 v[116:117], v[116:117], s[22:23]
v_pk_mul_f32 v[118:119], v[118:119], s[22:23]
v_pk_mul_f32 v[144:145], v[144:145], s[22:23]
v_pk_mul_f32 v[146:147], v[146:147], s[22:23]
v_pk_mul_f32 v[148:149], v[148:149], s[22:23]
v_pk_mul_f32 v[150:151], v[150:151], s[22:23]
v_pk_mul_f32 v[56:57], v[56:57], s[22:23]
v_pk_mul_f32 v[58:59], v[58:59], s[22:23]
v_pk_mul_f32 v[60:61], v[60:61], s[22:23]
v_pk_mul_f32 v[62:63], v[62:63], s[22:23]
v_pk_mul_f32 v[88:89], v[88:89], s[22:23]
v_pk_mul_f32 v[90:91], v[90:91], s[22:23]
v_pk_mul_f32 v[92:93], v[92:93], s[22:23]
v_pk_mul_f32 v[94:95], v[94:95], s[22:23]
v_pk_mul_f32 v[120:121], v[120:121], s[22:23]
v_pk_mul_f32 v[122:123], v[122:123], s[22:23]
v_pk_mul_f32 v[124:125], v[124:125], s[22:23]
v_pk_mul_f32 v[126:127], v[126:127], s[22:23]
v_pk_mul_f32 v[152:153], v[152:153], s[22:23]
v_pk_mul_f32 v[154:155], v[154:155], s[22:23]
v_pk_mul_f32 v[156:157], v[156:157], s[22:23]
v_pk_mul_f32 v[158:159], v[158:159], s[22:23]
K 32,32,33
K 33,34,35
K 34,36,37
K 35,38,39
K 40,40,41
K 41,42,43
K 42,44,45
K 43,46,47
W 0
Zvav 196,128,32
s_add_u32 m0, s62, 0
Ra 144,242,4096
Ra 148,242,5120
Ra 152,242,6144
Ra 156,242,7168
Mvav 196,132,40
K 64,64,65
K 65,66,67
K 66,68,69
K 67,70,71
K 72,72,73
K 73,74,75
K 74,76,77
K 75,78,79
s_nop 1
Zvav 204,132,72
Dc 1,8,61,0
K 96,96,97
K 97,98,99
K 98,100,101
Mvav 204,128,64
K 99,102,103
K 104,104,105
K 105,106,107
K 106,108,109
K 107,110,111
Zvav 212,128,96
K 128,128,129
K 129,130,131
K 130,132,133
K 131,134,135
Mvav 212,132,104
K 136,136,137
K 137,138,139
K 138,140,141
K 139,142,143
s_nop 1
Zvav 220,132,136
Dc 1,8,61,1024
K 48,48,49
K 49,50,51
K 50,52,53
Mvav 220,128,128
K 51,54,55
K 56,56,57
K 57,58,59
K 58,60,61
K 59,62,63
Mvav 196,136,48
Ra 128,242,8192
Ra 132,242,9216
K 80,80,81
K 81,82,83
Mvav 196,140,56
K 82,84,85
K 83,86,87
K 88,88,89
K 89,90,91
K 90,92,93
K 91,94,95
s_nop 1
Mvav 204,140,88
Dc 1,8,61,2048
K 112,112,113
K 113,114,115
K 114,116,117
Mvav 204,136,80
K 115,118,119
K 120,120,121
K 121,122,123
K 122,124,125
K 123,126,127
Mvav 212,136,112
K 144,144,145
K 145,146,147
K 146,148,149
K 147,150,151
Mvav 212,140,120
K 152,152,153
K 153,154,155
K 154,156,157
K 155,158,159
s_nop 1
Mvav 220,140,152
Dc 1,8,61,3072
s_add_u32 m0, s62, 4096
v_accvgpr_write_b32 a0, v32
v_accvgpr_write_b32 a1, v33
v_accvgpr_write_b32 a2, v34
Mvav 220,136,144
W 2
Ra 136,242,10240
v_accvgpr_write_b32 a3, v35
v_accvgpr_write_b32 a4, v40
Zvav 36,144,32
Ra 140,242,11264
v_accvgpr_write_b32 a5, v41
Mvav 36,148,40
v_accvgpr_write_b32 a6, v42
v_accvgpr_write_b32 a7, v43
v_max3_f32 v4, v196, v197, v198
v_max3_f32 v4, v4, v199, v252
Zvav 68,148,72
v_max3_f32 v5, v204, v205, v206
v_max3_f32 v5, v5, v207, v253
v_max3_f32 v6, v212, v213, v214
v_max3_f32 v6, v6, v215, v254
Mvav 68,144,64
v_max3_f32 v7, v220, v221, v222
v_max3_f32 v7, v7, v223, v255
v_mov_b32 v8, v4
v_mov_b32 v9, v5
Zvav 100,144,96
Dc 243,8,61,0
v_mov_b32 v10, v6
v_mov_b32 v11, v7
v_permlane32_swap_b32 v4, v8
Mvav 100,148,104
v_permlane32_swap_b32 v5, v9
v_permlane32_swap_b32 v6, v10
v_permlane32_swap_b32 v7, v11
v_max_f32 v4, v4, v8
Zvav 132,148,136
v_max_f32 v5, v5, v9
v_max_f32 v6, v6, v10
v_max_f32 v7, v7, v11
v_mov_b32 v8, v4
Mvav 132,144,128
v_mov_b32 v9, v5
v_mov_b32 v10, v6
v_mov_b32 v11, v7
v_accvgpr_write_b32 a8, v48
Mvav 36,152,48
v_permlane16_swap_b32 v4, v8
v_permlane16_swap_b32 v5, v9
v_permlane16_swap_b32 v6, v10
v_permlane16_swap_b32 v7, v11
)"
R"(Mvav 36,156,56
v_max_f32 v4, v4, v8
v_max_f32 v5, v5, v9
v_max_f32 v6, v6, v10
v_max_f32 v7, v7, v11
Mvav 68,156,88
v_sub_f32 v4, 0, v4
v_sub_f32 v5, 0, v5
v_sub_f32 v6, 0, v6
v_sub_f32 v7, 0, v7
Mvav 68,152,80
v_mov_b32 v224, v4
v_mov_b32 v225, v4
v_mov_b32 v226, v4
v_mov_b32 v227, v4
Mvav 100,152,112
v_mov_b32 v228, v5
v_mov_b32 v229, v5
v_mov_b32 v230, v5
v_mov_b32 v231, v5
Mvav 100,156,120
v_mov_b32 v232, v6
v_mov_b32 v233, v6
v_mov_b32 v234, v6
v_mov_b32 v235, v6
Mvav 132,156,152
v_mov_b32 v236, v7
v_mov_b32 v237, v7
v_mov_b32 v238, v7
v_mov_b32 v239, v7
Mvav 132,152,144
W 0
v_accvgpr_write_b32 a9, v49
v_accvgpr_write_b32 a10, v50
v_accvgpr_write_b32 a11, v51
v_accvgpr_write_b32 a12, v56
Cvav 160,128,32,224
s_and_b32 s80, s14, 1
Dc 243,8,61,1024
v_accvgpr_write_b32 a13, v57
v_accvgpr_write_b32 a14, v58
v_accvgpr_write_b32 a15, v59
Mvav 160,132,40
s_lshl_b32 s80, s80, 14
Ra 144,242,12288
Ra 148,242,13312
v_accvgpr_write_b32 a16, v64
v_accvgpr_write_b32 a17, v65
Cvav 164,132,72,228
Dc 243,8,61,2048
v_accvgpr_write_b32 a18, v66
v_accvgpr_write_b32 a19, v67
v_accvgpr_write_b32 a20, v72
Mvav 164,128,64
s_add_u32 s80, s80, 98304
v_accvgpr_write_b32 a21, v73
v_accvgpr_write_b32 a22, v74
v_accvgpr_write_b32 a23, v75
v_accvgpr_write_b32 a24, v80
Cvav 168,128,96,232
Dc 243,8,61,3072
s_add_u32 s61, s61, s63
v_accvgpr_write_b32 a25, v81
v_accvgpr_write_b32 a26, v82
A 196,196,224
Mvav 168,132,104
s_add_u32 s36, s29, 65536
v_accvgpr_write_b32 a27, v83
A 197,197,224
A 198,198,224
v_accvgpr_write_b32 a28, v88
Cvav 172,132,136,236
Ra 152,242,14336
buffer_store_dwordx4 a[192:195], v1, s[8:11], s36 offen offset:0 sc1
A 199,199,224
v_accvgpr_write_b32 a29, v89
Mvav 172,128,128
Ra 156,242,15360
X 196
v_accvgpr_write_b32 a30, v90
Mvav 160,136,48
X 197
v_accvgpr_write_b32 a31, v91
v_accvgpr_write_b32 a32, v96
Mvav 160,140,56
buffer_store_dwordx4 a[200:203], v1, s[8:11], s36 offen offset:1024 sc1
X 198
K 192,196,197
Mvav 164,140,88
X 199
v_accvgpr_write_b32 a33, v97
A 204,204,228
Mvav 164,136,80
v_accvgpr_write_b32 a34, v98
K 193,198,199
v_accvgpr_write_b32 a35, v99
A 205,205,228
Mvav 168,136,112
A 206,206,228
v_accvgpr_write_b32 a36, v104
v_add_u32 v8, s80, v1
A 207,207,228
Mvav 168,140,120
buffer_store_dwordx4 a[208:211], v1, s[8:11], s36 offen offset:2048 sc1
X 204
v_accvgpr_write_b32 a37, v105
Mvav 172,140,152
X 205
v_accvgpr_write_b32 a38, v106
v_accvgpr_write_b32 a39, v107
Mvav 172,136,144
W 0
X 206
v_accvgpr_write_b32 a40, v112
K 200,204,205
Cvav 176,144,32,224
Ra 128,242,24576
X 207
v_accvgpr_write_b32 a41, v113
Mvav 176,148,40
buffer_store_dwordx4 a[216:219], v1, s[8:11], s36 offen offset:3072 sc1
s_add_u32 s36, s29, 69632
Ra 132,242,25600
A 212,212,232
v_accvgpr_write_b32 a42, v114
Cvav 180,148,72,228
K 201,206,207
A 213,213,232
v_accvgpr_write_b32 a43, v115
A 214,214,232
Mvav 180,144,64
A 215,215,232
v_accvgpr_write_b32 a44, v120
X 212
Cvav 184,144,96,232
buffer_store_dwordx4 a[224:227], v1, s[8:11], s36 offen offset:0 sc1
v_accvgpr_write_b32 a45, v121
X 213
Mvav 184,148,104
v_accvgpr_write_b32 a46, v122
X 214
v_accvgpr_write_b32 a47, v123
Cvav 188,148,136,236
K 208,212,213
v_accvgpr_write_b32 a48, v128
X 215
Mvav 188,144,128
buffer_store_dwordx4 a[232:235], v1, s[8:11], s36 offen offset:1024 sc1
v_accvgpr_write_b32 a49, v129
A 220,220,236
v_accvgpr_write_b32 a50, v130
Mvav 176,152,48
K 209,214,215
A 221,221,236
v_accvgpr_write_b32 a51, v131
A 222,222,236
Mvav 176,156,56
Ra 136,242,26624
A 223,223,236
v_accvgpr_write_b32 a52, v136
v_accvgpr_write_b32 a53, v137
Mvav 180,156,88
Ra 140,242,27648
buffer_store_dwordx4 a[240:243], v1, s[8:11], s36 offen offset:2048 sc1
X 220
Mvav 180,152,80
X 221
v_accvgpr_write_b32 a54, v138
v_accvgpr_write_b32 a55, v139
Mvav 184,152,112
v_accvgpr_write_b32 a56, v144
v_accvgpr_write_b32 a57, v145
v_accvgpr_write_b32 a58, v146
v_accvgpr_write_b32 a59, v147
v_accvgpr_write_b32 a60, v152
v_accvgpr_write_b32 a61, v153
v_accvgpr_write_b32 a62, v154
v_accvgpr_write_b32 a63, v155
Mvav 184,156,120
s_barrier
buffer_store_dwordx4 a[248:251], v1, s[8:11], s36 offen offset:3072 sc1
X 222
K 216,220,221
Mvav 188,156,152
X 223
A 36,36,224
K 217,222,223
A 37,37,224
A 38,38,224
A 39,39,224
X 36
X 37
X 38
K 194,36,37
X 39
A 68,68,228
K 195,38,39
A 69,69,228
A 70,70,228
A 71,71,228
X 68
X 69
X 70
K 202,68,69
X 71
A 100,100,232
K 203,70,71
A 101,101,232
A 102,102,232
A 103,103,232
X 100
X 101
X 102
K 210,100,101
X 103
A 132,132,236
K 211,102,103
A 133,133,236
A 134,134,236
A 135,135,236
X 132
X 133
X 134
K 218,132,133
X 135
s_nop 0
K 219,134,135
Mvav 188,152,144
W 0
s_add_u32 s36, s29, 393216
buffer_store_dwordx4 a[196:199], v1, s[8:11], s36 offen offset:0 sc1
Ra 144,242,28672
Ra 148,242,29696
Zvav 32,128,192
buffer_store_dwordx4 a[204:207], v1, s[8:11], s36 offen offset:1024 sc1
Ra 152,242,30720
Ra 156,242,31744
Zvav 64,128,200
buffer_store_dwordx4 a[212:215], v1, s[8:11], s36 offen offset:2048 sc1
v_xor_b32 v242, 0x10000, v242
Zvav 96,128,208
s_and_b32 s80, s14, 1
s_xor_b32 s62, s62, 0x10000
buffer_store_dwordx4 a[220:223], v1, s[8:11], s36 offen offset:3072 sc1
s_add_u32 s36, s29, 397312
Zvav 128,128,216
s_lshl_b32 s80, s80, 14
s_add_u32 s80, s80, 131072
buffer_store_dwordx4 a[228:231], v1, s[8:11], s36 offen offset:0 sc1
v_add_u32 v23, 61440, v21
Zvav 132,132,216
v_add_u32 v12, s80, v1
Ra 64,12,8192
Ra 68,12,9216
v_add_u32 v24, 61440, v22
Zvav 100,132,208
Ra 72,12,10240
buffer_store_dwordx4 a[236:239], v1, s[8:11], s36 offen offset:1024 sc1
Ra 76,12,11264
v_add_u32 v25, 122880, v21
Zvav 68,132,200
Ra 80,12,12288
Ra 84,12,13312
Ra 88,12,14336
v_add_u32 v26, 122880, v22
Zvav 36,132,192
Ra 92,12,15360
Ra 96,12,0
Ra 100,12,1024
Ra 104,12,2048
Zvav 40,136,192
buffer_store_dwordx4 a[244:247], v1, s[8:11], s36 offen offset:2048 sc1
Ra 108,12,3072
Ra 112,12,4096
Ra 116,12,5120
Zvav 72,136,200
Ra 120,12,6144
Ra 124,12,7168
X 172
Zvav 104,136,208
X 173
X 174
Zvav 136,136,216
buffer_store_dwordx4 a[252:255], v1, s[8:11], s36 offen offset:3072 sc1
K 220,172,173
X 175
Zvav 140,140,216
X 168
K 221,174,175
Zvav 108,140,208
X 169
X 170
Zvav 76,140,200
s_waitcnt vmcnt(16) lgkmcnt(0)
s_barrier
ds_read_b128 v[248:251], v8
K 212,168,169
Zvav 44,140,192
X 171
X 164
Zvav 48,144,192
Ra 128,242,0
K 213,170,171
X 165
Zvav 80,144,200
X 166
Zvav 112,144,208
Ra 132,242,1024
K 204,164,165
X 167
Zvav 144,144,216
X 160
Zvav 148,148,216
K 205,166,167
X 161
Zvav 116,148,208
Ra 136,242,2048
X 162
Zvav 84,148,200
W 3
K 196,160,161
X 163
Zvav 52,148,192
X 176
K 197,162,163
Zvav 56,152,192
Ra 140,242,3072
X 177
Zvav 88,152,200
X 178
K 198,176,177
Zvav 120,152,208
X 179
X 180
Zvav 152,152,216
K 199,178,179
Zvav 156,156,216
X 181
X 182
Zvav 124,156,208
K 206,180,181
X 183
Zvav 92,156,200
K 207,182,183
Zvav 60,156,192
s_waitcnt vmcnt(0)
s_add_u32 s80, s71, 128
v_mov_b64 v[28:29], s[12:13]
s_mov_b64 exec, 1
buffer_store_dwordx2 v[28:29], off, s[8:11], s80 sc1
s_mov_b64 exec, -1
s_branch .Lsp1_32%=
.p2align 12
.Lmoe_40%=:
W 0
Cvaa 160,128,0,224
s_cmp_eq_u32 s14, 0
s_cselect_b32 s20, s30, s32
s_cselect_b32 s21, s31, s33
Ra 144,242,4096
Ra 148,242,5120
Mvaa 160,132,4
s_xor_b32 s62, s62, 0x10000
Ra 152,242,6144
Cvaa 164,132,20,228
X 184
Mvaa 164,128,16
s_and_b32 s21, s21, 0xffff
X 185
Cvaa 168,128,32,232
X 186
Mvaa 168,132,36
Ra 156,242,7168
K 214,184,185
Cvaa 172,132,52,236
s_mov_b32 s22, 2211840
X 187
Mvaa 172,128,48
X 188
Mvaa 160,136,8
Ra 128,242,16384
Mvaa 160,140,12
s_mov_b32 s23, 0x20000
K 215,186,187
Mvaa 164,140,28
X 189
Mvaa 164,136,24
X 190
Mvaa 168,136,40
Ra 132,242,17408
v_lshrrev_b32 v4, 6, v1
Mvaa 168,140,44
K 222,188,189
Mvaa 172,140,60
X 191
Mvaa 172,136,56
v_mul_u32_u24 v4, 3456, v4
K 223,190,191
v_smfmac_f32_16x16x64_bf16 a[160:163], a[176:179], v[192:199], v240
Ra 136,242,18432
)"
R"(v_smfmac_f32_16x16x64_bf16 a[164:167], a[176:179], v[200:207], v240
X 160
v_smfmac_f32_16x16x64_bf16 a[168:171], a[176:179], v[208:215], v240
v_lshrrev_b32 v5, 4, v1
v_smfmac_f32_16x16x64_bf16 a[172:175], a[176:179], v[216:223], v240
W 3
X 161
Cvaa 176,144,0,224
X 162
Mvaa 176,148,4
Ra 140,242,19456
v_xor_b32 v5, v5, v1
Cvaa 180,148,20,228
K 192,160,161
Mvaa 180,144,16
X 163
Cvaa 184,144,32,232
v_and_b32 v5, 32, v5
Mvaa 184,148,36
X 164
Cvaa 188,148,52,236
K 193,162,163
v_and_b32 v6, 16, v1
Mvaa 188,144,48
X 165
Mvaa 176,152,8
X 166
Mvaa 176,156,12
Ra 144,242,20480
Ra 148,242,21504
Mvaa 180,156,28
v_add3_u32 v4, v4, v5, v6
K 200,164,165
Mvaa 180,152,24
X 167
Mvaa 184,152,40
X 168
Mvaa 184,156,44
v_add_u32 v5, 54272, v4
K 201,166,167
Mvaa 188,156,60
X 169
Mvaa 188,152,56
W 2
Ra 152,242,22528
v_add_u32 v6, 108544, v4
Mvav 32,128,196
X 170
Mvav 64,128,204
Ra 156,242,23552
v_add_u32 v7, 162816, v4
Mvav 96,128,212
K 208,168,169
Mvav 128,128,220
X 171
Mvav 132,132,220
s_lshl_b32 s94, s85, 6
X 172
Mvav 100,132,212
s_add_u32 s64, s64, 1
Ra 128,242,8192
K 209,170,171
Mvav 68,132,204
X 173
Mvav 36,132,196
s_mul_i32 s95, s14, 20480
Mvav 40,136,196
X 174
Mvav 72,136,204
Ra 132,242,9216
K 216,172,173
Mvav 104,136,212
s_add_u32 s95, s95, 98304
X 175
Mvav 136,136,220
Mvav 140,140,220
s_add_u32 m0, s95, 0
X 176
Mvav 108,140,212
Ra 136,242,10240
K 217,174,175
Mvav 76,140,204
X 177
Mvav 44,140,196
W 3
D 4,20,94,0
Mvav 48,144,196
X 178
Mvav 80,144,204
Ra 140,242,11264
Mvav 112,144,212
D 5,20,94,1024
K 194,176,177
Mvav 144,144,220
X 179
Mvav 148,148,220
X 180
Mvav 116,148,212
Ra 144,242,12288
D 6,20,94,2048
Mvav 84,148,204
K 195,178,179
Mvav 52,148,196
X 181
Mvav 56,152,196
D 7,20,94,3072
Ra 148,242,13312
Mvav 88,152,204
X 182
Mvav 120,152,212
s_add_u32 s94, s94, 221184
K 202,180,181
Mvav 152,152,220
X 183
Mvav 156,156,220
X 184
Mvav 124,156,212
s_add_u32 m0, s95, 4096
Ra 152,242,14336
Mvav 92,156,204
K 203,182,183
Mvav 60,156,196
W 3
X 185
Cvaa 160,128,0,224
D 4,20,94,0
Ra 156,242,15360
Mvaa 160,132,4
X 186
Cvaa 164,132,20,228
K 210,184,185
Mvaa 164,128,16
X 187
Cvaa 168,128,32,232
D 5,20,94,1024
Mvaa 168,132,36
X 188
Cvaa 172,132,52,236
D 6,20,94,2048
K 211,186,187
Mvaa 172,128,48
X 189
Mvaa 160,136,8
Ra 128,242,24576
Ra 132,242,25600
Mvaa 160,140,12
X 190
Mvaa 164,140,28
D 7,20,94,3072
K 218,188,189
Mvaa 164,136,24
s_add_u32 s94, s94, 221184
X 191
Mvaa 168,136,40
Mvaa 168,140,44
X 160
Mvaa 172,140,60
s_add_u32 m0, s95, 8192
K 219,190,191
Mvaa 172,136,56
W 2
X 161
Cvaa 176,144,0,224
Ra 136,242,26624
Ra 140,242,27648
Mvaa 176,148,4
X 162
Cvaa 180,148,20,228
D 4,20,94,0
K 196,160,161
Mvaa 180,144,16
X 163
Cvaa 184,144,32,232
D 5,20,94,1024
Mvaa 184,148,36
X 164
Cvaa 188,148,52,236
K 197,162,163
Mvaa 188,144,48
D 6,20,94,2048
Ra 144,242,28672
Mvaa 176,152,8
X 165
Mvaa 176,156,12
Ra 148,242,29696
D 7,20,94,3072
Mvaa 180,156,28
X 166
Mvaa 180,152,24
K 204,164,165
Mvaa 184,152,40
s_add_u32 s94, s94, 221184
X 167
Mvaa 184,156,44
X 168
Mvaa 188,156,60
Mvaa 188,152,56
W 2
s_add_u32 m0, s95, 12288
K 205,166,167
Mvav 32,128,192
X 169
Mvav 64,128,200
Ra 152,242,30720
Ra 156,242,31744
Mvav 96,128,208
X 170
Mvav 128,128,216
D 4,20,94,0
v_xor_b32 v242, 0x10000, v242
Mvav 132,132,216
K 212,168,169
Mvav 100,132,208
X 171
Mvav 68,132,200
D 5,20,94,1024
Mvav 36,132,192
X 172
Mvav 40,136,192
D 6,20,94,2048
K 213,170,171
Mvav 72,136,200
X 173
Mvav 104,136,208
X 174
Mvav 136,136,216
D 7,20,94,3072
K 220,172,173
Mvav 140,140,216
X 175
Mvav 108,140,208
s_add_u32 s94, s94, 221184
X 176
Mvav 76,140,200
W 0
s_barrier
Mvav 44,140,192
Ra 128,242,0
K 221,174,175
Mvav 48,144,192
X 177
Mvav 80,144,200
v_bfe_u32 v11, v1, 4, 3
Mvav 112,144,208
X 178
Mvav 144,144,216
Ra 132,242,1024
v_lshlrev_b32 v11, 3, v11
Mvav 148,148,216
K 198,176,177
Mvav 116,148,208
X 179
Mvav 84,148,200
X 180
Mvav 52,148,192
Ra 136,242,2048
buffer_load_dwordx2 v[8:9], v11, s[8:11], 0 offen offset:3840 sc1
Mvav 56,152,192
Ra 140,242,3072
K 199,178,179
Mvav 88,152,200
X 181
Mvav 120,152,208
X 182
Mvav 152,152,216
K 206,180,181
Mvav 156,156,216
X 183
Mvav 124,156,208
Mvav 92,156,200
K 207,182,183
Mvav 60,156,192
W 0
Cvaa 160,128,0,224
s_add_u32 m0, s95, 16384
Ra 144,242,4096
Ra 148,242,5120
Mvaa 160,132,4
s_xor_b32 s62, s62, 0x10000
Ra 152,242,6144
Cvaa 164,132,20,228
X 184
Mvaa 164,128,16
X 185
Cvaa 168,128,32,232
X 186
Mvaa 168,132,36
D 4,20,94,0
K 214,184,185
Cvaa 172,132,52,236
Ra 156,242,7168
Mvaa 172,128,48
X 187
Mvaa 160,136,8
X 188
Mvaa 160,140,12
D 5,20,94,1024
Ra 128,242,16384
Mvaa 164,140,28
K 215,186,187
Mvaa 164,136,24
X 189
Mvaa 168,136,40
X 190
Mvaa 168,140,44
D 6,20,94,2048
Ra 132,242,17408
Mvaa 172,140,60
K 222,188,189
Mvaa 172,136,56
X 191
v_smfmac_f32_16x16x64_bf16 a[160:163], a[176:179], v[192:199], v240
Ra 136,242,18432
K 223,190,191
v_smfmac_f32_16x16x64_bf16 a[164:167], a[176:179], v[200:207], v240
D 7,20,94,3072
v_smfmac_f32_16x16x64_bf16 a[168:171], a[176:179], v[208:215], v240
s_add_u32 s94, s94, 221184
v_mul_u32_u24 v10, 160, v11
v_smfmac_f32_16x16x64_bf16 a[172:175], a[176:179], v[216:223], v240
W 3
X 160
Cvaa 176,144,0,224
X 161
Mvaa 176,148,4
Ra 140,242,19456
v_lshrrev_b32 v12, 7, v1
Cvaa 180,148,20,228
X 162
Mvaa 180,144,16
K 192,160,161
v_lshl_add_u32 v10, v12, 4, v10
Cvaa 184,144,32,232
s_mul_i32 s82, s14, 5120
X 163
Mvaa 184,148,36
X 164
Cvaa 188,148,52,236
K 193,162,163
Mvaa 188,144,48
s_mul_i32 s81, s14, 640
X 165
Mvaa 176,152,8
X 166
Mvaa 176,156,12
s_add_u32 s81, s81, 118784
Ra 144,242,20480
Ra 148,242,21504
Mvaa 180,156,28
K 200,164,165
Mvaa 180,152,24
X 167
Mvaa 184,152,40
s_waitcnt vmcnt(4)
v_cmp_eq_u64 vcc, s[12:13], v[8:9]
s_cmp_eq_u64 vcc, -1
s_cbranch_scc0 .Lspin_47%=
.Lok_48%=:
X 168
Mvaa 184,156,44
K 201,166,167
Mvaa 188,156,60
s_add_u32 m0, s82, 53248
X 169
Mvaa 188,152,56
W 2
Ra 152,242,22528
Mvav 32,128,196
X 170
Mvav 64,128,204
Ra 156,242,23552
Dc 10,8,81,0
Mvav 96,128,212
K 208,168,169
Mvav 128,128,220
X 171
Mvav 132,132,220
s_add_u32 m0, s82, 54144
X 172
Mvav 100,132,212
s_add_u32 s64, s64, 1
Ra 128,242,8192
K 209,170,171
Mvav 68,132,204
X 173
Mvav 36,132,196
Dc 10,8,81,128
Mvav 40,136,196
X 174
Mvav 72,136,204
s_add_u32 m0, s82, 55040
Ra 132,242,9216
K 216,172,173
Mvav 104,136,212
X 175
Mvav 136,136,220
Dc 10,8,81,256
Mvav 140,140,220
X 176
Mvav 108,140,212
)"
R"(Ra 136,242,10240
K 217,174,175
Mvav 76,140,204
s_add_u32 m0, s82, 55936
X 177
Mvav 44,140,196
W 3
Mvav 48,144,196
X 178
Mvav 80,144,204
Ra 140,242,11264
Dc 10,8,81,384
Mvav 112,144,212
K 194,176,177
Mvav 144,144,220
s_add_u32 m0, s82, 56832
X 179
Mvav 148,148,220
X 180
Mvav 116,148,212
Ra 144,242,12288
Mvav 84,148,204
Dc 10,8,81,512
K 195,178,179
Mvav 52,148,196
X 181
Mvav 56,152,196
X 182
Mvav 88,152,204
Ra 148,242,13312
Mvav 120,152,212
K 202,180,181
Mvav 152,152,220
X 183
Mvav 156,156,220
X 184
Mvav 124,156,212
Ra 152,242,14336
Mvav 92,156,204
K 203,182,183
Mvav 60,156,196
W 3
X 185
Cvaa 160,128,0,224
X 186
Mvaa 160,132,4
Ra 156,242,15360
Cvaa 164,132,20,228
K 210,184,185
Mvaa 164,128,16
X 187
Cvaa 168,128,32,232
X 188
Mvaa 168,132,36
Cvaa 172,132,52,236
K 211,186,187
Mvaa 172,128,48
X 189
Mvaa 160,136,8
Ra 128,242,24576
Ra 132,242,25600
Mvaa 160,140,12
X 190
Mvaa 164,140,28
K 218,188,189
Mvaa 164,136,24
X 191
Mvaa 168,136,40
Mvaa 168,140,44
Mvaa 172,140,60
X 160
Mvaa 172,136,56
W 2
Ra 136,242,26624
K 219,190,191
Cvaa 176,144,0,224
X 161
Mvaa 176,148,4
Ra 140,242,27648
v_lshrrev_b32 v8, 8, v1
Cvaa 180,148,20,228
X 162
Mvaa 180,144,16
K 196,160,161
Cvaa 184,144,32,232
X 163
Mvaa 184,148,36
v_bfe_u32 v9, v1, 6, 2
Cvaa 188,148,52,236
X 164
Mvaa 188,144,48
K 197,162,163
v_lshl_add_u32 v9, v8, 3, v9
Mvaa 176,152,8
X 165
Mvaa 176,156,12
Ra 144,242,28672
Ra 148,242,29696
Mvaa 180,156,28
X 166
Mvaa 180,152,24
K 204,164,165
v_lshlrev_b32 v11, 6, v9
Mvaa 184,152,40
X 167
Mvaa 184,156,44
X 168
Mvaa 188,156,60
v_xor_b32 v9, s14, v8
Mvaa 188,152,56
W 2
Ra 152,242,30720
K 205,166,167
Mvav 32,128,192
X 169
Mvav 64,128,200
Ra 156,242,31744
v_and_b32 v9, 1, v9
Mvav 96,128,208
X 170
Mvav 128,128,216
v_xor_b32 v242, 0x10000, v242
K 212,168,169
Mvav 132,132,216
X 171
Mvav 100,132,208
v_lshl_add_u32 v11, v9, 5, v11
Mvav 68,132,200
X 172
Mvav 36,132,192
K 213,170,171
Mvav 40,136,192
X 173
Mvav 72,136,200
v_bfe_u32 v9, v1, 4, 2
Mvav 104,136,208
X 174
Mvav 136,136,216
K 220,172,173
v_lshl_add_u32 v12, v9, 3, v11
Mvav 140,140,216
X 175
Mvav 108,140,208
X 176
Mvav 76,140,200
s_waitcnt vmcnt(0) lgkmcnt(0)
s_barrier
Mvav 44,140,192
v_add_u32 v11, 98304, v12
K 221,174,175
Mvav 48,144,192
Ra 128,242,0
Mvav 80,144,200
X 177
Mvav 112,144,208
X 178
Mvav 144,144,216
Ra 132,242,1024
v_and_b32 v13, 0x70, v1
Mvav 148,148,216
K 198,176,177
Mvav 116,148,208
X 179
Mvav 84,148,200
Ra 136,242,2048
v_lshl_add_u32 v13, v8, 7, v13
Mvav 52,148,192
X 180
Mvav 56,152,192
Ra 140,242,3072
K 199,178,179
Mvav 88,152,200
X 181
Mvav 120,152,208
X 182
Mvav 152,152,216
K 206,180,181
Mvav 156,156,216
X 183
Mvav 124,156,208
Mvav 92,156,200
K 207,182,183
Mvav 60,156,192
W 0
Cvaa 160,128,0,224
s_cmp_eq_u32 s14, 0
s_cselect_b32 s95, 106496, 0
Ra 144,242,4096
Ra 148,242,5120
Mvaa 160,132,4
s_xor_b32 s62, s62, 0x10000
X 184
Cvaa 164,132,20,228
Ta 184,11,0
Ta 186,11,256
Mvaa 164,128,16
s_add_u32 s95, s95, 32768
Ta 188,11,20480
Ta 190,11,20736
Cvaa 168,128,32,232
Ra 192,13,53248
Ta 196,11,2048
Mvaa 168,132,36
s_add_u32 m0, s95, 0
X 185
Cvaa 172,132,52,236
Ta 198,11,2304
Ta 200,11,22528
Mvaa 172,128,48
W 3
X 186
Zaaa 232,184,192
Ta 202,11,22784
K 214,184,185
Zaaa 236,188,192
Ra 204,13,53760
Ra 152,242,6144
Mvaa 160,136,8
X 187
Mvaa 160,140,12
Ta 208,11,4096
D 4,20,94,0
Mvaa 164,140,28
X 188
Mvaa 164,136,24
Ta 210,11,4352
D 5,20,94,1024
Mvaa 168,136,40
Ra 156,242,7168
K 215,186,187
Mvaa 168,140,44
X 189
Mvaa 172,140,60
Ta 212,11,24576
D 6,20,94,2048
Mvaa 172,136,56
X 190
v_smfmac_f32_16x16x64_bf16 a[160:163], a[176:179], v[192:199], v240
W 5
Ta 214,11,24832
D 7,20,94,3072
Maaa 232,196,204
s_add_u32 s94, s94, 221184
Ra 216,13,54272
K 222,188,189
Maaa 236,200,204
s_add_u32 m0, s95, 4096
X 191
v_smfmac_f32_16x16x64_bf16 a[164:167], a[176:179], v[200:207], v240
X 160
K 223,190,191
v_smfmac_f32_16x16x64_bf16 a[168:171], a[176:179], v[208:215], v240
Ra 128,242,16384
Ta 220,11,6144
v_smfmac_f32_16x16x64_bf16 a[172:175], a[176:179], v[216:223], v240
W 5
Ta 222,11,6400
Ra 132,242,17408
Cvaa 176,144,0,224
X 161
Mvaa 176,148,4
D 4,20,94,0
Ta 224,11,26624
Cvaa 180,148,20,228
X 162
Mvaa 180,144,16
D 5,20,94,1024
K 192,160,161
Cvaa 184,144,32,232
Ta 226,11,26880
Ra 228,13,54784
Mvaa 184,148,36
W 7
X 163
Maaa 232,208,216
D 6,20,94,2048
Ra 136,242,18432
Maaa 236,212,216
X 164
Cvaa 188,148,52,236
D 7,20,94,3072
Ta 184,11,8192
Mvaa 188,144,48
s_add_u32 s94, s94, 221184
Ra 140,242,19456
K 193,162,163
Mvaa 176,152,8
s_add_u32 m0, s95, 8192
X 165
Mvaa 176,156,12
Ta 186,11,8448
Ta 188,11,28672
Mvaa 180,156,28
X 166
Mvaa 180,152,24
Ra 144,242,20480
K 200,164,165
Mvaa 184,152,40
X 167
Mvaa 184,156,44
Ta 190,11,28928
D 4,20,94,0
Mvaa 188,156,60
W 7
X 168
Maaa 232,220,228
s_add_u32 s64, s64, 1
Ra 192,13,55296
D 5,20,94,1024
Maaa 236,224,228
Ta 196,11,10240
K 201,166,167
Mvaa 188,152,56
W 6
X 169
Mvav 32,128,196
D 6,20,94,2048
Ra 148,242,21504
Mvav 64,128,204
X 170
Mvav 96,128,212
Ta 198,11,10496
D 7,20,94,3072
Mvav 128,128,220
s_add_u32 s94, s94, 221184
Ta 200,11,30720
K 208,168,169
Mvav 132,132,220
s_add_u32 m0, s95, 12288
X 171
Mvav 100,132,212
Ra 152,242,22528
Ta 202,11,30976
Mvav 68,132,204
X 172
Mvav 36,132,196
W 6
Ra 204,13,55808
K 209,170,171
Maaa 232,184,192
X 173
Maaa 236,188,192
D 4,20,94,0
Ra 156,242,23552
Mvav 40,136,196
X 174
Mvav 72,136,204
Ta 208,11,12288
D 5,20,94,1024
Mvav 104,136,212
Ta 210,11,12544
K 216,172,173
Mvav 136,136,220
X 175
Mvav 140,140,220
D 6,20,94,2048
Ra 128,242,8192
Mvav 108,140,212
Ta 212,11,32768
Ta 214,11,33024
Mvav 76,140,204
X 176
Mvav 44,140,196
W 5
D 7,20,94,3072
K 217,174,175
Mvav 48,144,196
s_add_u32 s94, s94, 221184
X 177
Maaa 232,196,204
s_add_u32 m0, s95, 16384
Ra 132,242,9216
Ra 216,13,56320
Maaa 236,200,204
X 178
Mvav 80,144,204
Ta 220,11,14336
K 194,176,177
Mvav 112,144,212
X 179
Mvav 144,144,220
D 4,20,94,0
Ta 222,11,14592
Mvav 148,148,220
Ra 136,242,10240
Ta 224,11,34816
Mvav 116,148,212
X 180
Mvav 84,148,204
D 5,20,94,1024
K 195,178,179
Mvav 52,148,196
X 181
Mvav 56,152,196
D 6,20,94,2048
Ta 226,11,35072
Mvav 88,152,204
W 5
Ra 140,242,11264
Ra 228,13,56832
)"
R"(Maaa 232,208,216
X 182
Maaa 236,212,216
D 7,20,94,3072
K 202,180,181
Mvav 120,152,212
s_add_u32 s94, s94, 221184
X 183
Mvav 152,152,220
Ta 184,11,16384
Ra 144,242,12288
Mvav 156,156,220
X 184
Mvav 124,156,212
Ta 186,11,16640
K 203,182,183
Mvav 92,156,204
X 185
Mvav 60,156,196
W 4
Ta 188,11,36864
Ra 148,242,13312
Cvaa 160,128,0,224
X 186
Mvaa 160,132,4
Ta 190,11,37120
K 210,184,185
Cvaa 164,132,20,228
W 6
Ra 192,13,57344
Ta 196,11,18432
Maaa 232,220,228
X 187
Maaa 236,224,228
X 188
Mvaa 164,128,16
Ra 152,242,14336
K 211,186,187
Cvaa 168,128,32,232
Ta 198,11,18688
Ta 200,11,38912
Mvaa 168,132,36
X 189
Cvaa 172,132,52,236
X 190
Mvaa 172,128,48
Ra 156,242,15360
K 218,188,189
Mvaa 160,136,8
Ta 202,11,39168
Ra 204,13,57856
Mvaa 160,140,12
X 191
Mvaa 164,140,28
Ra 128,242,24576
Mvaa 164,136,24
W 8
Maaa 232,184,192
Ra 132,242,25600
Maaa 236,188,192
X 160
Mvaa 168,136,40
K 219,190,191
Mvaa 168,140,44
X 161
Mvaa 172,140,60
X 162
Mvaa 172,136,56
W 4
Ra 136,242,26624
K 196,160,161
Cvaa 176,144,0,224
X 163
Mvaa 176,148,4
X 164
Cvaa 180,148,20,228
Ra 140,242,27648
K 197,162,163
Mvaa 180,144,16
X 165
Cvaa 184,144,32,232
W 4
Maaa 232,196,204
X 166
Maaa 236,200,204
Mvaa 184,148,36
K 204,164,165
Cvaa 188,148,52,236
X 167
Mvaa 188,144,48
Ra 144,242,28672
Mvaa 176,152,8
X 168
Mvaa 176,156,12
Ra 148,242,29696
Mvaa 180,156,28
K 205,166,167
Mvaa 180,152,24
X 169
Mvaa 184,152,40
Mvaa 184,156,44
X 170
Mvaa 188,156,60
K 212,168,169
Mvaa 188,152,56
W 2
X 171
Mvav 32,128,192
Ra 152,242,30720
Ra 156,242,31744
Mvav 64,128,200
X 172
Mvav 96,128,208
v_xor_b32 v242, 0x10000, v242
Mvav 128,128,216
K 213,170,171
Mvav 132,132,216
X 173
Mvav 100,132,208
Mvav 68,132,200
X 174
Mvav 36,132,192
Mvav 40,136,192
K 220,172,173
Mvav 72,136,200
X 175
Mvav 104,136,208
Mvav 136,136,216
X 176
Mvav 140,140,216
Mvav 108,140,208
K 221,174,175
Mvav 76,140,200
s_waitcnt vmcnt(0) lgkmcnt(0)
s_barrier
Mvav 44,140,192
X 177
Mvav 48,144,192
X 178
Mvav 80,144,200
Ra 128,242,0
K 198,176,177
Mvav 112,144,208
X 179
Mvav 144,144,216
Ra 132,242,1024
Mvav 148,148,216
X 180
Mvav 116,148,208
Ra 136,242,2048
Mvav 84,148,200
K 199,178,179
Mvav 52,148,192
X 181
Mvav 56,152,192
Ra 140,242,3072
Mvav 88,152,200
X 182
Mvav 120,152,208
Mvav 152,152,216
K 206,180,181
Mvav 156,156,216
X 183
Mvav 124,156,208
Mvav 92,156,200
K 207,182,183
Mvav 60,156,192
W 0
Cvaa 160,128,0,224
s_mov_b32 s20, s34
Ra 144,242,4096
Ra 148,242,5120
Mvaa 160,132,4
s_xor_b32 s62, s62, 0x10000
X 184
Cvaa 164,132,20,228
s_and_b32 s21, s35, 0xffff
Ta 184,11,40960
Ta 186,11,41216
Mvaa 164,128,16
Ta 188,12,32768
Ta 190,12,33024
Cvaa 168,128,32,232
s_mov_b32 s22, 2211840
Ra 192,13,58368
Ta 196,11,43008
Mvaa 168,132,36
s_mov_b32 s23, 0x20000
X 185
Cvaa 172,132,52,236
Ta 198,11,43264
Ta 200,12,34816
Mvaa 172,128,48
W 3
X 186
Maaa 232,184,192
Ta 202,12,35072
K 214,184,185
Maaa 236,188,192
Ra 204,13,58880
Ra 152,242,6144
Mvaa 160,136,8
X 187
Mvaa 160,140,12
Ta 208,11,45056
v_lshrrev_b32 v4, 8, v1
Mvaa 164,140,28
X 188
Mvaa 164,136,24
Ta 210,11,45312
v_lshlrev_b32 v5, 5, v4
Mvaa 168,136,40
v_xor_b32 v5, v5, v1
K 215,186,187
Mvaa 168,140,44
Ra 156,242,7168
Ta 212,12,36864
Mvaa 172,140,60
X 189
Mvaa 172,136,56
v_and_b32 v5, 0xf0, v5
v_mul_u32_u24 v4, 1280, v4
v_smfmac_f32_16x16x64_bf16 a[160:163], a[176:179], v[192:199], v240
W 5
X 190
Maaa 232,196,204
Ta 214,12,37120
v_add_u32 v4, v4, v5
Maaa 236,200,204
Ra 216,13,59392
K 222,188,189
v_smfmac_f32_16x16x64_bf16 a[164:167], a[176:179], v[200:207], v240
X 191
X 160
K 223,190,191
v_smfmac_f32_16x16x64_bf16 a[168:171], a[176:179], v[208:215], v240
Ra 128,242,16384
v_xor_b32 v5, 128, v4
v_smfmac_f32_16x16x64_bf16 a[172:175], a[176:179], v[216:223], v240
W 4
Ta 220,11,47104
v_add_u32 v6, 8192, v4
Cvaa 176,144,0,224
Ta 222,11,47360
v_add_u32 v7, 12288, v5
Mvaa 176,148,4
Ra 132,242,17408
v_add_u32 v5, 4096, v5
Cvaa 180,148,20,228
s_mul_i32 s94, s85, 40960
X 161
Mvaa 180,144,16
s_mul_i32 s81, s14, 20480
Ta 224,12,38912
Ta 226,12,39168
Cvaa 184,144,32,232
s_add_u32 s94, s94, s81
X 162
Mvaa 184,148,36
W 6
s_lshl_b32 s95, s14, 12
K 192,160,161
Maaa 232,208,216
s_add_u32 s95, s95, 98304
X 163
Maaa 236,212,216
s_add_u32 m0, s95, 0
Ra 228,13,59904
Ra 136,242,18432
Cvaa 188,148,52,236
X 164
Mvaa 188,144,48
Ta 184,11,49152
K 193,162,163
Mvaa 176,152,8
X 165
Mvaa 176,156,12
Ra 140,242,19456
Ta 186,11,49408
Mvaa 180,156,28
D 4,20,94,0
Ta 188,12,40960
Mvaa 180,152,24
X 166
Mvaa 184,152,40
D 5,20,94,1024
K 200,164,165
Mvaa 184,156,44
X 167
Mvaa 188,156,60
W 5
D 6,20,94,2048
Ra 144,242,20480
Maaa 232,220,228
s_add_u32 s64, s64, 1
Ta 190,12,41216
Ra 192,13,60416
Maaa 236,224,228
D 7,20,94,3072
Ta 196,11,51200
Mvaa 188,152,56
W 6
X 168
Mvav 32,128,196
s_add_u32 s94, s94, 256
s_add_u32 m0, s95, 8192
K 201,166,167
Mvav 64,128,204
X 169
Mvav 96,128,212
D 4,20,94,0
Ra 148,242,21504
Mvav 128,128,220
X 170
Mvav 132,132,220
D 5,20,94,1024
Ta 198,11,51456
Mvav 100,132,212
Ta 200,12,43008
K 208,168,169
Mvav 68,132,204
D 6,20,94,2048
Ra 152,242,22528
Mvav 36,132,196
W 5
X 171
Maaa 232,184,192
D 7,20,94,3072
Ta 202,12,43264
Maaa 236,188,192
s_add_u32 s94, s94, 256
X 172
Mvav 40,136,196
s_add_u32 m0, s95, 16384
Ra 204,13,60928
K 209,170,171
Mvav 72,136,204
X 173
Mvav 104,136,212
Ra 156,242,23552
D 4,20,94,0
Mvav 136,136,220
X 174
Mvav 140,140,220
D 5,20,94,1024
Ta 208,11,53248
Mvav 108,140,212
Ta 210,11,53504
K 216,172,173
Mvav 76,140,204
D 6,20,94,2048
Ra 128,242,8192
Mvav 44,140,196
W 3
X 175
Mvav 48,144,196
D 7,20,94,3072
Ta 212,12,45056
Maaa 232,196,204
s_add_u32 s94, s94, 256
X 176
Maaa 236,200,204
s_add_u32 m0, s95, 24576
Ta 214,12,45312
K 217,174,175
Mvav 80,144,204
D 4,20,94,0
Ra 132,242,9216
Mvav 112,144,212
X 177
Mvav 144,144,220
Ra 216,13,61440
D 5,20,94,1024
Mvav 148,148,220
X 178
Mvav 116,148,212
D 6,20,94,2048
Ta 220,11,55296
Mvav 84,148,204
Ta 222,11,55552
K 194,176,177
Mvav 52,148,196
D 7,20,94,3072
Ra 136,242,10240
Mvav 56,152,196
s_add_u32 s94, s94, 256
X 179
Mvav 88,152,204
W 3
X 180
Maaa 232,208,216
s_add_u32 m0, s95, 32768
Ta 224,12,47104
K 195,178,179
Maaa 236,212,216
D 4,20,94,0
Ta 226,12,47360
Mvav 120,152,212
Ra 140,242,11264
Ra 228,13,61952
)"
R"(Mvav 152,152,220
X 181
Mvav 156,156,220
D 5,20,94,1024
Ta 184,11,57344
Mvav 124,156,212
X 182
Mvav 92,156,204
D 6,20,94,2048
K 202,180,181
Mvav 60,156,196
W 2
X 183
Cvaa 160,128,0,224
D 7,20,94,3072
Ra 144,242,12288
Mvaa 160,132,4
s_add_u32 s94, s94, 256
X 184
Cvaa 164,132,20,228
W 2
Ta 186,11,57600
K 203,182,183
Maaa 232,220,228
X 185
Maaa 236,224,228
Ta 188,12,49152
Ra 148,242,13312
Mvaa 164,128,16
X 186
Cvaa 168,128,32,232
Ta 190,12,49408
K 210,184,185
Mvaa 168,132,36
Ra 192,13,62464
Ta 196,11,59392
Cvaa 172,132,52,236
X 187
Mvaa 172,128,48
X 188
Mvaa 160,136,8
Ra 152,242,14336
K 211,186,187
Mvaa 160,140,12
Ta 198,11,59648
Ta 200,12,51200
Mvaa 164,140,28
X 189
Mvaa 164,136,24
W 4
X 190
Maaa 232,184,192
Ra 156,242,15360
K 218,188,189
Maaa 236,188,192
Ta 202,12,51456
Ra 204,13,62976
Mvaa 168,136,40
X 191
Mvaa 168,140,44
Ra 128,242,24576
Ra 132,242,25600
Mvaa 172,140,60
X 160
Mvaa 172,136,56
W 4
Ra 136,242,26624
K 219,190,191
Cvaa 176,144,0,224
X 161
Mvaa 176,148,4
X 162
Cvaa 180,148,20,228
Ra 140,242,27648
K 196,160,161
Mvaa 180,144,16
X 163
Cvaa 184,144,32,232
W 4
X 164
Maaa 232,196,204
K 197,162,163
Maaa 236,200,204
X 165
Mvaa 184,148,36
X 166
Cvaa 188,148,52,236
K 204,164,165
Mvaa 188,144,48
X 167
Mvaa 176,152,8
Ra 144,242,28672
G 252,232
Mvaa 176,156,12
G 253,233
G 254,234
Mvaa 180,156,28
X 168
Mvaa 180,152,24
G 255,235
G 8,236
Mvaa 184,152,40
Ra 148,242,29696
G 9,237
Mvaa 184,156,44
G 10,238
G 14,239
Mvaa 188,156,60
K 252,252,8
K 253,253,9
Mvaa 188,152,56
W 2
K 205,166,167
K 254,254,10
Mvav 32,128,192
K 255,255,14
v_and_b32 v8, 0xffff0000, v252
Mvav 64,128,200
v_and_b32 v9, 0xffff0000, v253
v_and_b32 v10, 0xffff0000, v254
Mvav 96,128,208
X 169
Mvav 128,128,216
v_and_b32 v14, 0xffff0000, v255
v_lshlrev_b32 v252, 16, v252
Mvav 132,132,216
v_lshlrev_b32 v253, 16, v253
v_lshlrev_b32 v254, 16, v254
Mvav 100,132,208
v_lshlrev_b32 v255, 16, v255
v_mul_f32 v15, 0xbfb8aa3b, v252
Mvav 68,132,200
v_mul_f32 v2, 0xbfb8aa3b, v253
v_mul_f32 v3, 0xbfb8aa3b, v254
Mvav 36,132,192
X 170
Mvav 40,136,192
v_mul_f32 v27, 0xbfb8aa3b, v255
K 212,168,169
Mvav 72,136,200
X 15
Mvav 104,136,208
Ra 152,242,30720
Ra 156,242,31744
Mvav 136,136,216
X 2
X 3
X 27
v_add_f32 v15, 1.0, v15
v_add_f32 v2, 1.0, v2
v_add_f32 v3, 1.0, v3
v_add_f32 v27, 1.0, v27
v_rcp_f32 v15, v15
v_rcp_f32 v2, v2
v_rcp_f32 v3, v3
v_rcp_f32 v27, v27
U 252,252,15
U 253,253,2
U 254,254,3
U 255,255,27
K 252,252,252
K 253,253,253
K 254,254,254
K 255,255,255
v_lshlrev_b32 v252, 16, v252
v_lshlrev_b32 v253, 16, v253
v_lshlrev_b32 v254, 16, v254
v_lshlrev_b32 v255, 16, v255
U 252,252,8
U 253,253,9
U 254,254,10
U 255,255,14
K 246,252,253
K 247,254,255
s_lshl_b32 s81, s14, 3
v_add_u32 v2, s81, v1
s_mov_b32 s86, 0x00ff00ff
s_mov_b32 s87, 0x00ff00ff
v_cndmask_b32_e64 v252, 0, v246, s[86:87]
v_cndmask_b32_e64 v253, 0, v247, s[86:87]
v_cndmask_b32_e64 v246, v246, 0, s[86:87]
v_cndmask_b32_e64 v247, v247, 0, s[86:87]
ds_write_b64 v2, v[252:253] offset:63488
ds_write_b64 v2, v[246:247] offset:64512
Mvav 140,140,216
X 171
Mvav 108,140,208
v_xor_b32 v242, 0x10000, v242
Mvav 76,140,200
s_waitcnt vmcnt(0) lgkmcnt(0)
s_barrier
Mvav 44,140,192
X 172
Mvav 48,144,192
Ra 128,242,0
K 213,170,171
Mvav 80,144,200
X 173
Mvav 112,144,208
X 174
Mvav 144,144,216
Ra 132,242,1024
K 220,172,173
Mvav 148,148,216
X 175
Mvav 116,148,208
X 176
Mvav 84,148,200
Ra 136,242,2048
K 221,174,175
Mvav 52,148,192
X 177
Mvav 56,152,192
X 178
Mvav 88,152,200
Ra 140,242,3072
K 198,176,177
Mvav 120,152,208
X 179
Mvav 152,152,216
X 180
Mvav 156,156,216
K 199,178,179
Mvav 124,156,208
X 181
Mvav 92,156,200
X 182
Mvav 60,156,192
K 206,180,181
X 183
s_nop 0
K 207,182,183
W 0
Cvaa 160,128,0,224
Ra 144,242,4096
Ra 148,242,5120
Mvaa 160,132,4
s_xor_b32 s62, s62, 0x10000
X 184
Cvaa 164,132,20,228
Ra 216,1,63488
Ra 220,1,64512
Mvaa 164,128,16
v_lshrrev_b32 v8, 6, v1
v_and_b32 v9, 7, v8
Cvaa 168,128,32,232
s_lshl_b32 s81, s14, 2
X 185
Mvaa 168,132,36
v_xor_b32 v9, s81, v9
v_lshlrev_b32 v9, 5, v9
Cvaa 172,132,52,236
X 186
Mvaa 172,128,48
v_lshl_add_u32 v4, v8, 8, v9
v_bfe_u32 v9, v1, 4, 2
Mvaa 160,136,8
v_lshl_add_u32 v4, v9, 3, v4
v_add_u32 v4, 98304, v4
Mvaa 160,140,12
K 214,184,185
v_xor_b32 v5, 32, v4
Mvaa 164,140,28
v_xor_b32 v6, 64, v4
v_xor_b32 v7, 96, v4
Ta 184,4,0
Ta 186,4,4096
Mvaa 164,136,24
Ta 188,5,0
Ta 190,5,4096
Mvaa 168,136,40
Ra 152,242,6144
Ta 192,6,0
Mvaa 168,140,44
W 4
X 187
Zaaa 232,184,216
Ta 194,6,4096
v_and_b32 v11, 0x70, v1
Mvaa 172,140,60
v_mul_u32_u24 v11, 160, v11
v_lshrrev_b32 v10, 8, v1
Mvaa 172,136,56
X 188
v_smfmac_f32_16x16x64_bf16 a[160:163], a[176:179], v[192:199], v240
Ra 156,242,7168
W 4
Maaa 232,188,220
K 215,186,187
v_lshl_add_u32 v11, v10, 4, v11
v_smfmac_f32_16x16x64_bf16 a[164:167], a[176:179], v[200:207], v240
X 189
X 190
K 222,188,189
X 191
X 160
K 223,190,191
v_smfmac_f32_16x16x64_bf16 a[168:171], a[176:179], v[208:215], v240
Ta 196,7,0
v_bfe_u32 v10, v1, 7, 1
v_smfmac_f32_16x16x64_bf16 a[172:175], a[176:179], v[216:223], v240
W 2
Ta 198,7,4096
v_lshl_add_u32 v10, v10, 6, v11
Zaaa 236,192,216
W 2
s_mul_i32 s94, s85, 20480
s_lshl_b32 s82, s14, 8
Cvaa 176,144,0,224
s_add_u32 s94, s94, s82
Ta 200,4,8192
Ta 202,4,12288
Mvaa 176,148,4
s_add_u32 s94, s94, 2752512
s_lshl_b32 s71, s85, 4
Ra 128,242,16384
Cvaa 180,148,20,228
W 3
s_lshl_b32 s82, s14, 3
Ta 204,5,8192
Maaa 236,196,220
s_add_u32 s71, s71, s82
s_add_u32 s71, s71, 2560
Ta 206,5,12288
Mvaa 180,144,16
X 161
Cvaa 184,144,32,232
Ra 132,242,17408
Ta 208,6,8192
Mvaa 184,148,36
W 5
X 162
Zaaa 240,200,216
Ta 210,6,12288
K 192,160,161
Cvaa 188,148,52,236
buffer_store_dwordx4 a[232:235], v10, s[8:11], s94 offen offset:0 sc1
Ta 212,7,8192
Mvaa 188,144,48
X 163
Mvaa 176,152,8
W 4
X 164
Maaa 240,204,220
Ra 136,242,18432
Ta 214,7,12288
Mvaa 176,156,12
Ta 184,4,16384
K 193,162,163
Mvaa 180,156,28
X 165
Mvaa 180,152,24
W 4
Ta 186,4,20480
Ra 140,242,19456
Zaaa 244,208,216
X 166
Mvaa 184,152,40
buffer_store_dwordx4 a[236:239], v10, s[8:11], s94 offen offset:128 sc1
K 200,164,165
Mvaa 184,156,44
X 167
Mvaa 188,156,60
W 3
Ta 188,5,16384
Ta 190,5,20480
Maaa 244,212,220
s_add_u32 s64, s64, 1
X 168
Mvaa 188,152,56
W 2
Ra 144,242,20480
K 201,166,167
Mvav 32,128,196
Ta 192,6,16384
Ta 194,6,20480
Mvav 64,128,204
X 169
Zaaa 248,184,216
buffer_store_dwordx4 a[240:243], v10, s[8:11], s94 offen offset:512 sc1
Ta 196,7,16384
Mvav 96,128,212
X 170
Mvav 128,128,220
Ra 148,242,21504
K 208,168,169
Mvav 132,132,220
W 5
Ta 198,7,20480
Ta 200,4,24576
Maaa 248,188,220
X 171
Mvav 100,132,212
X 172
Mvav 68,132,204
Ta 202,4,28672
K 209,170,171
Mvav 36,132,196
W 5
Ra 152,242,22528
)"
R"(buffer_store_dwordx4 a[244:247], v10, s[8:11], s94 offen offset:640 sc1
Zaaa 252,192,216
X 173
Mvav 40,136,196
X 174
Mvav 72,136,204
Ta 204,5,24576
Ta 206,5,28672
Mvav 104,136,212
W 5
Ra 156,242,23552
K 216,172,173
Maaa 252,196,220
X 175
Mvav 136,136,220
Ta 208,6,24576
Ta 210,6,28672
Mvav 140,140,220
X 176
Mvav 108,140,212
W 6
buffer_store_dwordx4 a[248:251], v10, s[8:11], s94 offen offset:1024 sc1
K 217,174,175
Zaaa 232,200,216
X 177
Mvav 76,140,204
Ta 212,7,24576
Ra 128,242,8192
Mvav 44,140,196
W 4
Ta 214,7,28672
Ta 184,4,32768
Mvav 48,144,196
X 178
Maaa 232,204,220
Ra 132,242,9216
K 194,176,177
Mvav 80,144,204
X 179
Mvav 112,144,212
Ta 186,4,36864
buffer_store_dwordx4 a[252:255], v10, s[8:11], s94 offen offset:1152 sc1
Mvav 144,144,220
W 6
X 180
Zaaa 236,208,216
Ta 188,5,32768
K 195,178,179
Mvav 148,148,220
X 181
Mvav 116,148,212
Ta 190,5,36864
Ra 136,242,10240
Mvav 84,148,204
W 6
X 182
Maaa 236,212,220
Ta 192,6,32768
K 202,180,181
Mvav 52,148,196
Ta 194,6,36864
Ra 140,242,11264
Mvav 56,152,196
X 183
Mvav 88,152,204
W 6
buffer_store_dwordx4 a[232:235], v10, s[8:11], s94 offen offset:1536 sc1
Ta 196,7,32768
Zaaa 240,184,216
X 184
Mvav 120,152,212
Ta 198,7,36864
K 203,182,183
Mvav 152,152,220
Ra 144,242,12288
Ra 148,242,13312
Mvav 156,156,220
W 8
X 185
Maaa 240,188,220
X 186
Mvav 124,156,212
buffer_store_dwordx4 a[236:239], v10, s[8:11], s94 offen offset:1664 sc1
K 210,184,185
Mvav 92,156,204
X 187
Mvav 60,156,196
W 5
X 188
Zaaa 244,192,216
W 4
Ra 152,242,14336
K 211,186,187
Cvaa 160,128,0,224
X 189
Mvaa 160,132,4
X 190
Cvaa 164,132,20,228
W 3
Ra 156,242,15360
buffer_store_dwordx4 a[240:243], v10, s[8:11], s94 offen offset:2048 sc1
Maaa 244,196,220
K 218,188,189
Mvaa 164,128,16
X 191
Cvaa 168,128,32,232
Mvaa 168,132,36
Cvaa 172,132,52,236
Mvaa 172,128,48
Ra 128,242,24576
Mvaa 160,136,8
Ra 132,242,25600
buffer_store_dwordx4 a[244:247], v10, s[8:11], s94 offen offset:2176 sc1
Mvaa 160,140,12
Mvaa 164,140,28
Mvaa 164,136,24
Mvaa 168,136,40
Mvaa 168,140,44
Mvaa 172,140,60
Mvaa 172,136,56
W 2
Ra 136,242,26624
Cvaa 176,144,0,224
X 160
Mvaa 176,148,4
Ra 140,242,27648
K 219,190,191
Cvaa 180,148,20,228
X 161
Mvaa 180,144,16
X 162
Cvaa 184,144,32,232
K 196,160,161
Mvaa 184,148,36
X 163
Cvaa 188,148,52,236
X 164
Mvaa 188,144,48
Ra 144,242,28672
K 197,162,163
Mvaa 176,152,8
X 165
Mvaa 176,156,12
X 166
Mvaa 180,156,28
Ra 148,242,29696
K 204,164,165
Mvaa 180,152,24
X 167
Mvaa 184,152,40
X 168
Mvaa 184,156,44
K 205,166,167
Mvaa 188,156,60
X 169
Mvaa 188,152,56
W 2
X 170
Mvav 32,128,192
Ra 152,242,30720
K 212,168,169
Mvav 64,128,200
X 171
Mvav 96,128,208
Ra 156,242,31744
v_xor_b32 v242, 0x10000, v242
Mvav 128,128,216
X 172
Mvav 132,132,216
K 213,170,171
Mvav 100,132,208
X 173
Mvav 68,132,200
X 174
Mvav 36,132,192
Mvav 40,136,192
K 220,172,173
Mvav 72,136,200
X 175
Mvav 104,136,208
Mvav 136,136,216
X 176
Mvav 140,140,216
Mvav 108,140,208
K 221,174,175
Mvav 76,140,200
W 0
s_barrier
Mvav 44,140,192
X 177
Mvav 48,144,192
X 178
Mvav 80,144,200
Ra 128,242,0
K 198,176,177
Mvav 112,144,208
X 179
Mvav 144,144,216
Ra 132,242,1024
Mvav 148,148,216
X 180
Mvav 116,148,208
Ra 136,242,2048
Mvav 84,148,200
K 199,178,179
Mvav 52,148,192
X 181
Mvav 56,152,192
Ra 140,242,3072
Mvav 88,152,200
X 182
Mvav 120,152,208
Mvav 152,152,216
K 206,180,181
Mvav 156,156,216
X 183
Mvav 124,156,208
Mvav 92,156,200
s_waitcnt vmcnt(0)
v_mov_b64 v[28:29], s[12:13]
s_mov_b64 exec, 1
buffer_store_dwordx2 v[28:29], off, s[8:11], s71 sc1
s_mov_b64 exec, -1
K 207,182,183
Mvav 60,156,192
s_mov_b32 s85, 99
s_lshl_b32 s65, s57, 1
s_sub_u32 s65, s65, 1
s_branch .Ll0t_42%=
.Lf1r_43%=:
s_lshl_b32 s92, s58, 2
s_add_u32 s91, s92, s51
s_add_u32 s95, s92, 0
s_and_b32 s95, s95, 15
s_mul_i32 s95, s95, 40960
s_mul_i32 s71, s51, 0
s_add_u32 s71, s71, 133120
s_add_u32 m0, s71, s48
s_lshl2_add_u32 s94, s48, s95
D 16,40,94,0
s_add_u32 s95, s92, 1
s_and_b32 s95, s95, 15
s_mul_i32 s95, s95, 40960
s_mul_i32 s71, s51, 0
s_add_u32 s71, s71, 143360
D 17,40,94,1024
D 18,40,94,2048
D 19,40,94,3072
s_add_u32 m0, m0, 4096
s_nop 0
buffer_load_dwordx4 v20, s[40:43], s94 offen lds
s_add_u32 m0, s71, s48
s_lshl2_add_u32 s94, s48, s95
D 16,40,94,0
s_add_u32 s95, s92, 2
s_and_b32 s95, s95, 15
s_mul_i32 s95, s95, 40960
s_mul_i32 s71, s51, 0
s_add_u32 s71, s71, 153600
D 17,40,94,1024
D 18,40,94,2048
D 19,40,94,3072
s_add_u32 m0, m0, 4096
s_nop 0
buffer_load_dwordx4 v20, s[40:43], s94 offen lds
s_add_u32 m0, s71, s48
s_lshl2_add_u32 s94, s48, s95
D 16,40,94,0
D 17,40,94,1024
D 18,40,94,2048
D 19,40,94,3072
s_add_u32 m0, m0, 4096
s_nop 0
buffer_load_dwordx4 v20, s[40:43], s94 offen lds
s_branch .Lf1_36%=
.Lspin_11%=:
s_mov_b32 s84, 0
.Llp_13%=:
s_sleep 2
v_lshrrev_b32 v28, 1, v1
s_lshl_b32 s80, s60, 9
s_add_u32 s80, s80, 40960
buffer_load_dwordx2 v[28:29], v28, s[8:11], s80 offen sc1
s_waitcnt vmcnt(0)
s_add_u32 s84, s84, 1
v_cmp_eq_u32 vcc, s13, v29
s_cmp_eq_u64 vcc, -1
s_cbranch_scc1 .Lok_12%=
s_cmp_lt_u32 s84, 1048576
s_cbranch_scc1 .Llp_13%=
s_branch .Lok_12%=
.Lspin_16%=:
s_mov_b32 s84, 0
.Llp_18%=:
s_sleep 2
v_lshrrev_b32 v28, 1, v1
s_lshl_b32 s80, s60, 9
s_add_u32 s80, s80, 40960
buffer_load_dwordx2 v[28:29], v28, s[8:11], s80 offen sc1
s_waitcnt vmcnt(0)
s_add_u32 s84, s84, 1
v_cmp_eq_u32 vcc, s13, v29
s_cmp_eq_u64 vcc, -1
s_cbranch_scc1 .Lok_17%=
s_cmp_lt_u32 s84, 1048576
s_cbranch_scc1 .Llp_18%=
s_branch .Lok_17%=
.Lrt_19%=:
s_waitcnt vmcnt(0)
s_mov_b32 s86, 40960
buffer_load_dwordx4 v[32:35], v1, s[8:11], s86 offen offset:0 sc1
buffer_load_dwordx4 v[36:39], v1, s[8:11], s86 offen offset:1024 sc1
buffer_load_dwordx4 v[40:43], v1, s[8:11], s86 offen offset:2048 sc1
buffer_load_dwordx4 v[44:47], v1, s[8:11], s86 offen offset:3072 sc1
s_mov_b32 s86, 45056
buffer_load_dwordx4 v[48:51], v1, s[8:11], s86 offen offset:0 sc1
buffer_load_dwordx4 v[52:55], v1, s[8:11], s86 offen offset:1024 sc1
buffer_load_dwordx4 v[56:59], v1, s[8:11], s86 offen offset:2048 sc1
buffer_load_dwordx4 v[60:63], v1, s[8:11], s86 offen offset:3072 sc1
s_mov_b32 s86, 49152
buffer_load_dwordx4 v[64:67], v1, s[8:11], s86 offen offset:0 sc1
buffer_load_dwordx4 v[68:71], v1, s[8:11], s86 offen offset:1024 sc1
buffer_load_dwordx4 v[72:75], v1, s[8:11], s86 offen offset:2048 sc1
buffer_load_dwordx4 v[76:79], v1, s[8:11], s86 offen offset:3072 sc1
s_mov_b32 s86, 53248
buffer_load_dwordx4 v[80:83], v1, s[8:11], s86 offen offset:0 sc1
buffer_load_dwordx4 v[84:87], v1, s[8:11], s86 offen offset:1024 sc1
buffer_load_dwordx4 v[88:91], v1, s[8:11], s86 offen offset:2048 sc1
buffer_load_dwordx4 v[92:95], v1, s[8:11], s86 offen offset:3072 sc1
s_branch .Lrtr_20%=
.Lrt_21%=:
v_lshrrev_b32 v136, 1, v1
s_mov_b32 s84, 0
.LL_23%=:
s_waitcnt vmcnt(0)
v_min3_u32 v96, v33, v35, v95
v_max3_u32 v97, v33, v35, v95
v_min3_u32 v96, v96, v37, v39
v_max3_u32 v97, v97, v37, v39
v_min3_u32 v96, v96, v41, v43
v_max3_u32 v97, v97, v41, v43
v_min3_u32 v96, v96, v45, v47
v_max3_u32 v97, v97, v45, v47
v_min3_u32 v96, v96, v49, v51
v_max3_u32 v97, v97, v49, v51
v_min3_u32 v96, v96, v53, v55
v_max3_u32 v97, v97, v53, v55
v_min3_u32 v96, v96, v57, v59
v_max3_u32 v97, v97, v57, v59
v_min3_u32 v96, v96, v61, v63
v_max3_u32 v97, v97, v61, v63
v_min3_u32 v96, v96, v65, v67
v_max3_u32 v97, v97, v65, v67
v_min3_u32 v96, v96, v69, v71
v_max3_u32 v97, v97, v69, v71
v_min3_u32 v96, v96, v73, v75
v_max3_u32 v97, v97, v73, v75
v_min3_u32 v96, v96, v77, v79
v_max3_u32 v97, v97, v77, v79
v_min3_u32 v96, v96, v81, v83
v_max3_u32 v97, v97, v81, v83
v_min3_u32 v96, v96, v85, v87
v_max3_u32 v97, v97, v85, v87
v_min3_u32 v96, v96, v89, v91
v_max3_u32 v97, v97, v89, v91
v_min_u32 v96, v96, v93
v_max_u32 v97, v97, v93
v_cmp_eq_u32 vcc, s13, v96
v_cmp_eq_u32_e64 s[86:87], s13, v97
s_and_b64 vcc, vcc, s[86:87]
s_cmp_eq_u64 vcc, -1
s_cbranch_scc1 .LL_24%=
s_sleep 2
s_mov_b32 s86, 40960
buffer_load_dwordx4 v[32:35], v1, s[8:11], s86 offen offset:0 sc1
buffer_load_dwordx4 v[36:39], v1, s[8:11], s86 offen offset:1024 sc1
buffer_load_dwordx4 v[40:43], v1, s[8:11], s86 offen offset:2048 sc1
buffer_load_dwordx4 v[44:47], v1, s[8:11], s86 offen offset:3072 sc1
s_mov_b32 s86, 45056
buffer_load_dwordx4 v[48:51], v1, s[8:11], s86 offen offset:0 sc1
buffer_load_dwordx4 v[52:55], v1, s[8:11], s86 offen offset:1024 sc1
buffer_load_dwordx4 v[56:59], v1, s[8:11], s86 offen offset:2048 sc1
buffer_load_dwordx4 v[60:63], v1, s[8:11], s86 offen offset:3072 sc1
s_mov_b32 s86, 49152
buffer_load_dwordx4 v[64:67], v1, s[8:11], s86 offen offset:0 sc1
buffer_load_dwordx4 v[68:71], v1, s[8:11], s86 offen offset:1024 sc1
buffer_load_dwordx4 v[72:75], v1, s[8:11], s86 offen offset:2048 sc1
buffer_load_dwordx4 v[76:79], v1, s[8:11], s86 offen offset:3072 sc1
s_mov_b32 s86, 53248
buffer_load_dwordx4 v[80:83], v1, s[8:11], s86 offen offset:0 sc1
buffer_load_dwordx4 v[84:87], v1, s[8:11], s86 offen offset:1024 sc1
buffer_load_dwordx4 v[88:91], v1, s[8:11], s86 offen offset:2048 sc1
buffer_load_dwordx4 v[92:95], v1, s[8:11], s86 offen offset:3072 sc1
s_add_u32 s84, s84, 1
s_cmp_lt_u32 s84, 1048576
s_cbranch_scc1 .LL_23%=
s_waitcnt vmcnt(0)
.LL_24%=:
v_mov_b32 v96, v32
v_mov_b32 v97, 0
)"
R"(v_mov_b32 v98, 0
v_mov_b32 v100, 0
v_med3_u32 v98, v97, v98, v34
v_med3_u32 v97, v96, v97, v34
v_max_u32 v96, v96, v34
v_med3_u32 v98, v97, v98, v36
v_med3_u32 v97, v96, v97, v36
v_max_u32 v96, v96, v36
v_med3_u32 v98, v97, v98, v38
v_med3_u32 v97, v96, v97, v38
v_max_u32 v96, v96, v38
v_med3_u32 v98, v97, v98, v40
v_med3_u32 v97, v96, v97, v40
v_max_u32 v96, v96, v40
v_med3_u32 v98, v97, v98, v42
v_med3_u32 v97, v96, v97, v42
v_max_u32 v96, v96, v42
v_med3_u32 v98, v97, v98, v44
v_med3_u32 v97, v96, v97, v44
v_max_u32 v96, v96, v44
v_med3_u32 v98, v97, v98, v46
v_med3_u32 v97, v96, v97, v46
v_max_u32 v96, v96, v46
v_med3_u32 v98, v97, v98, v48
v_med3_u32 v97, v96, v97, v48
v_max_u32 v96, v96, v48
v_med3_u32 v98, v97, v98, v50
v_med3_u32 v97, v96, v97, v50
v_max_u32 v96, v96, v50
v_med3_u32 v98, v97, v98, v52
v_med3_u32 v97, v96, v97, v52
v_max_u32 v96, v96, v52
v_med3_u32 v98, v97, v98, v54
v_med3_u32 v97, v96, v97, v54
v_max_u32 v96, v96, v54
v_med3_u32 v98, v97, v98, v56
v_med3_u32 v97, v96, v97, v56
v_max_u32 v96, v96, v56
v_med3_u32 v98, v97, v98, v58
v_med3_u32 v97, v96, v97, v58
v_max_u32 v96, v96, v58
v_med3_u32 v98, v97, v98, v60
v_med3_u32 v97, v96, v97, v60
v_max_u32 v96, v96, v60
v_med3_u32 v98, v97, v98, v62
v_med3_u32 v97, v96, v97, v62
v_max_u32 v96, v96, v62
v_med3_u32 v98, v97, v98, v64
v_med3_u32 v97, v96, v97, v64
v_max_u32 v96, v96, v64
v_med3_u32 v98, v97, v98, v66
v_med3_u32 v97, v96, v97, v66
v_max_u32 v96, v96, v66
v_med3_u32 v98, v97, v98, v68
v_med3_u32 v97, v96, v97, v68
v_max_u32 v96, v96, v68
v_med3_u32 v98, v97, v98, v70
v_med3_u32 v97, v96, v97, v70
v_max_u32 v96, v96, v70
v_med3_u32 v98, v97, v98, v72
v_med3_u32 v97, v96, v97, v72
v_max_u32 v96, v96, v72
v_med3_u32 v98, v97, v98, v74
v_med3_u32 v97, v96, v97, v74
v_max_u32 v96, v96, v74
v_med3_u32 v98, v97, v98, v76
v_med3_u32 v97, v96, v97, v76
v_max_u32 v96, v96, v76
v_med3_u32 v98, v97, v98, v78
v_med3_u32 v97, v96, v97, v78
v_max_u32 v96, v96, v78
v_med3_u32 v98, v97, v98, v80
v_med3_u32 v97, v96, v97, v80
v_max_u32 v96, v96, v80
v_med3_u32 v98, v97, v98, v82
v_med3_u32 v97, v96, v97, v82
v_max_u32 v96, v96, v82
v_med3_u32 v98, v97, v98, v84
v_med3_u32 v97, v96, v97, v84
v_max_u32 v96, v96, v84
v_med3_u32 v98, v97, v98, v86
v_med3_u32 v97, v96, v97, v86
v_max_u32 v96, v96, v86
v_med3_u32 v98, v97, v98, v88
v_med3_u32 v97, v96, v97, v88
v_max_u32 v96, v96, v88
v_med3_u32 v98, v97, v98, v90
v_med3_u32 v97, v96, v97, v90
v_max_u32 v96, v96, v90
v_med3_u32 v98, v97, v98, v92
v_med3_u32 v97, v96, v97, v92
v_max_u32 v96, v96, v92
v_med3_u32 v98, v97, v98, v94
v_med3_u32 v97, v96, v97, v94
v_max_u32 v96, v96, v94
v_mov_b32 v99, v96
s_nop 1
v_max_u32_dpp v99, v99, v99 row_shr:1 row_mask:0xf bank_mask:0xf
s_nop 1
v_max_u32_dpp v99, v99, v99 row_shr:2 row_mask:0xf bank_mask:0xf
s_nop 1
v_max_u32_dpp v99, v99, v99 row_shr:4 row_mask:0xf bank_mask:0xf
s_nop 1
v_max_u32_dpp v99, v99, v99 row_shr:8 row_mask:0xf bank_mask:0xf
s_nop 1
v_max_u32_dpp v99, v99, v99 row_bcast:15 row_mask:0xa bank_mask:0xf
s_nop 1
v_max_u32_dpp v99, v99, v99 row_bcast:31 row_mask:0xc bank_mask:0xf
s_nop 0
v_readlane_b32 s86, v99, 63
s_nop 1
v_cmp_eq_u32 vcc, s86, v96
s_and_b32 s87, s86, 0xffff
s_sub_u32 s87, 2047, s87
v_cndmask_b32 v96, v96, v97, vcc
v_cndmask_b32_e64 v97, v97, 0, vcc
v_writelane_b32 v100, s87, 0
v_mov_b32 v99, v96
s_nop 1
v_max_u32_dpp v99, v99, v99 row_shr:1 row_mask:0xf bank_mask:0xf
s_nop 1
v_max_u32_dpp v99, v99, v99 row_shr:2 row_mask:0xf bank_mask:0xf
s_nop 1
v_max_u32_dpp v99, v99, v99 row_shr:4 row_mask:0xf bank_mask:0xf
s_nop 1
v_max_u32_dpp v99, v99, v99 row_shr:8 row_mask:0xf bank_mask:0xf
s_nop 1
v_max_u32_dpp v99, v99, v99 row_bcast:15 row_mask:0xa bank_mask:0xf
s_nop 1
v_max_u32_dpp v99, v99, v99 row_bcast:31 row_mask:0xc bank_mask:0xf
s_nop 0
v_readlane_b32 s86, v99, 63
s_nop 1
v_cmp_eq_u32 vcc, s86, v96
s_and_b32 s87, s86, 0xffff
s_sub_u32 s87, 2047, s87
v_cndmask_b32 v96, v96, v97, vcc
v_cndmask_b32_e64 v97, v97, 0, vcc
v_writelane_b32 v100, s87, 1
v_mov_b32 v99, v96
s_nop 1
v_max_u32_dpp v99, v99, v99 row_shr:1 row_mask:0xf bank_mask:0xf
s_nop 1
v_max_u32_dpp v99, v99, v99 row_shr:2 row_mask:0xf bank_mask:0xf
s_nop 1
v_max_u32_dpp v99, v99, v99 row_shr:4 row_mask:0xf bank_mask:0xf
s_nop 1
v_max_u32_dpp v99, v99, v99 row_shr:8 row_mask:0xf bank_mask:0xf
s_nop 1
v_max_u32_dpp v99, v99, v99 row_bcast:15 row_mask:0xa bank_mask:0xf
s_nop 1
v_max_u32_dpp v99, v99, v99 row_bcast:31 row_mask:0xc bank_mask:0xf
s_nop 0
v_readlane_b32 s86, v99, 63
s_nop 1
v_cmp_eq_u32 vcc, s86, v96
s_and_b32 s87, s86, 0xffff
s_sub_u32 s87, 2047, s87
v_cndmask_b32 v96, v96, v97, vcc
v_cndmask_b32_e64 v97, v97, 0, vcc
v_writelane_b32 v100, s87, 2
v_mov_b32 v99, v96
s_nop 1
v_max_u32_dpp v99, v99, v99 row_shr:1 row_mask:0xf bank_mask:0xf
s_nop 1
v_max_u32_dpp v99, v99, v99 row_shr:2 row_mask:0xf bank_mask:0xf
s_nop 1
v_max_u32_dpp v99, v99, v99 row_shr:4 row_mask:0xf bank_mask:0xf
s_nop 1
v_max_u32_dpp v99, v99, v99 row_shr:8 row_mask:0xf bank_mask:0xf
s_nop 1
v_max_u32_dpp v99, v99, v99 row_bcast:15 row_mask:0xa bank_mask:0xf
s_nop 1
v_max_u32_dpp v99, v99, v99 row_bcast:31 row_mask:0xc bank_mask:0xf
s_nop 0
v_readlane_b32 s86, v99, 63
s_nop 1
v_cmp_eq_u32 vcc, s86, v96
s_and_b32 s87, s86, 0xffff
s_sub_u32 s87, 2047, s87
v_cndmask_b32 v96, v96, v97, vcc
v_cndmask_b32_e64 v97, v97, 0, vcc
v_writelane_b32 v100, s87, 3
v_mov_b32 v99, v96
s_nop 1
v_max_u32_dpp v99, v99, v99 row_shr:1 row_mask:0xf bank_mask:0xf
s_nop 1
v_max_u32_dpp v99, v99, v99 row_shr:2 row_mask:0xf bank_mask:0xf
s_nop 1
v_max_u32_dpp v99, v99, v99 row_shr:4 row_mask:0xf bank_mask:0xf
s_nop 1
v_max_u32_dpp v99, v99, v99 row_shr:8 row_mask:0xf bank_mask:0xf
s_nop 1
v_max_u32_dpp v99, v99, v99 row_bcast:15 row_mask:0xa bank_mask:0xf
s_nop 1
v_max_u32_dpp v99, v99, v99 row_bcast:31 row_mask:0xc bank_mask:0xf
s_nop 0
v_readlane_b32 s86, v99, 63
s_nop 1
v_cmp_eq_u32 vcc, s86, v96
s_and_b32 s87, s86, 0xffff
s_sub_u32 s87, 2047, s87
v_cndmask_b32 v96, v96, v97, vcc
v_cndmask_b32_e64 v97, v97, 0, vcc
v_writelane_b32 v100, s87, 4
v_mov_b32 v99, v96
s_nop 1
v_max_u32_dpp v99, v99, v99 row_shr:1 row_mask:0xf bank_mask:0xf
s_nop 1
v_max_u32_dpp v99, v99, v99 row_shr:2 row_mask:0xf bank_mask:0xf
s_nop 1
v_max_u32_dpp v99, v99, v99 row_shr:4 row_mask:0xf bank_mask:0xf
s_nop 1
v_max_u32_dpp v99, v99, v99 row_shr:8 row_mask:0xf bank_mask:0xf
s_nop 1
v_max_u32_dpp v99, v99, v99 row_bcast:15 row_mask:0xa bank_mask:0xf
s_nop 1
v_max_u32_dpp v99, v99, v99 row_bcast:31 row_mask:0xc bank_mask:0xf
s_nop 0
v_readlane_b32 s86, v99, 63
s_nop 1
v_cmp_eq_u32 vcc, s86, v96
s_and_b32 s87, s86, 0xffff
s_sub_u32 s87, 2047, s87
v_cndmask_b32 v96, v96, v97, vcc
v_cndmask_b32_e64 v97, v97, 0, vcc
v_writelane_b32 v100, s87, 5
v_mov_b32 v99, v96
s_nop 1
v_max_u32_dpp v99, v99, v99 row_shr:1 row_mask:0xf bank_mask:0xf
s_nop 1
v_max_u32_dpp v99, v99, v99 row_shr:2 row_mask:0xf bank_mask:0xf
s_nop 1
v_max_u32_dpp v99, v99, v99 row_shr:4 row_mask:0xf bank_mask:0xf
s_nop 1
v_max_u32_dpp v99, v99, v99 row_shr:8 row_mask:0xf bank_mask:0xf
s_nop 1
v_max_u32_dpp v99, v99, v99 row_bcast:15 row_mask:0xa bank_mask:0xf
s_nop 1
v_max_u32_dpp v99, v99, v99 row_bcast:31 row_mask:0xc bank_mask:0xf
s_nop 0
v_readlane_b32 s86, v99, 63
s_nop 1
v_cmp_eq_u32 vcc, s86, v96
s_and_b32 s87, s86, 0xffff
s_sub_u32 s87, 2047, s87
v_cndmask_b32 v96, v96, v97, vcc
v_cndmask_b32_e64 v97, v97, 0, vcc
v_writelane_b32 v100, s87, 6
v_mov_b32 v99, v96
s_nop 1
v_max_u32_dpp v99, v99, v99 row_shr:1 row_mask:0xf bank_mask:0xf
s_nop 1
v_max_u32_dpp v99, v99, v99 row_shr:2 row_mask:0xf bank_mask:0xf
s_nop 1
v_max_u32_dpp v99, v99, v99 row_shr:4 row_mask:0xf bank_mask:0xf
s_nop 1
v_max_u32_dpp v99, v99, v99 row_shr:8 row_mask:0xf bank_mask:0xf
s_nop 1
v_max_u32_dpp v99, v99, v99 row_bcast:15 row_mask:0xa bank_mask:0xf
s_nop 1
v_max_u32_dpp v99, v99, v99 row_bcast:31 row_mask:0xc bank_mask:0xf
s_nop 0
v_readlane_b32 s86, v99, 63
s_nop 1
v_cmp_eq_u32 vcc, s86, v96
s_and_b32 s87, s86, 0xffff
s_sub_u32 s87, 2047, s87
v_cndmask_b32 v96, v96, v97, vcc
v_cndmask_b32_e64 v97, v97, 0, vcc
v_writelane_b32 v100, s87, 7
v_mov_b32 v99, v98
s_nop 1
v_max_u32_dpp v99, v99, v99 row_shr:1 row_mask:0xf bank_mask:0xf
s_nop 1
v_max_u32_dpp v99, v99, v99 row_shr:2 row_mask:0xf bank_mask:0xf
s_nop 1
v_max_u32_dpp v99, v99, v99 row_shr:4 row_mask:0xf bank_mask:0xf
s_nop 1
v_max_u32_dpp v99, v99, v99 row_shr:8 row_mask:0xf bank_mask:0xf
s_nop 1
v_max_u32_dpp v99, v99, v99 row_bcast:15 row_mask:0xa bank_mask:0xf
s_nop 1
v_max_u32_dpp v99, v99, v99 row_bcast:31 row_mask:0xc bank_mask:0xf
s_nop 0
v_readlane_b32 s87, v99, 63
s_nop 0
s_cmp_gt_u32 s87, s86
s_cbranch_scc0 .LL_25%=
s_mov_b32 m0, 0
.LL_26%=:
v_max3_u32 v96, v32, v34, v36
v_max3_u32 v96, v96, v38, v40
v_max3_u32 v96, v96, v42, v44
v_max3_u32 v96, v96, v46, v48
v_max3_u32 v96, v96, v50, v52
v_max3_u32 v96, v96, v54, v56
v_max3_u32 v96, v96, v58, v60
v_max3_u32 v96, v96, v62, v64
v_max3_u32 v96, v96, v66, v68
v_max3_u32 v96, v96, v70, v72
v_max3_u32 v96, v96, v74, v76
v_max3_u32 v96, v96, v78, v80
v_max3_u32 v96, v96, v82, v84
v_max3_u32 v96, v96, v86, v88
v_max3_u32 v96, v96, v90, v92
v_max_u32 v96, v96, v94
v_mov_b32 v99, v96
s_nop 1
v_max_u32_dpp v99, v99, v99 row_shr:1 row_mask:0xf bank_mask:0xf
s_nop 1
v_max_u32_dpp v99, v99, v99 row_shr:2 row_mask:0xf bank_mask:0xf
s_nop 1
v_max_u32_dpp v99, v99, v99 row_shr:4 row_mask:0xf bank_mask:0xf
s_nop 1
v_max_u32_dpp v99, v99, v99 row_shr:8 row_mask:0xf bank_mask:0xf
s_nop 1
v_max_u32_dpp v99, v99, v99 row_bcast:15 row_mask:0xa bank_mask:0xf
s_nop 1
v_max_u32_dpp v99, v99, v99 row_bcast:31 row_mask:0xc bank_mask:0xf
s_nop 0
v_readlane_b32 s86, v99, 63
s_nop 1
v_cmp_eq_u32 vcc, s86, v32
s_nop 1
v_cndmask_b32_e64 v32, v32, 0, vcc
v_cmp_eq_u32 vcc, s86, v34
s_nop 1
v_cndmask_b32_e64 v34, v34, 0, vcc
v_cmp_eq_u32 vcc, s86, v36
s_nop 1
v_cndmask_b32_e64 v36, v36, 0, vcc
v_cmp_eq_u32 vcc, s86, v38
s_nop 1
v_cndmask_b32_e64 v38, v38, 0, vcc
v_cmp_eq_u32 vcc, s86, v40
s_nop 1
v_cndmask_b32_e64 v40, v40, 0, vcc
v_cmp_eq_u32 vcc, s86, v42
s_nop 1
v_cndmask_b32_e64 v42, v42, 0, vcc
v_cmp_eq_u32 vcc, s86, v44
s_nop 1
v_cndmask_b32_e64 v44, v44, 0, vcc
v_cmp_eq_u32 vcc, s86, v46
s_nop 1
v_cndmask_b32_e64 v46, v46, 0, vcc
v_cmp_eq_u32 vcc, s86, v48
s_nop 1
v_cndmask_b32_e64 v48, v48, 0, vcc
v_cmp_eq_u32 vcc, s86, v50
s_nop 1
v_cndmask_b32_e64 v50, v50, 0, vcc
v_cmp_eq_u32 vcc, s86, v52
s_nop 1
v_cndmask_b32_e64 v52, v52, 0, vcc
v_cmp_eq_u32 vcc, s86, v54
s_nop 1
v_cndmask_b32_e64 v54, v54, 0, vcc
v_cmp_eq_u32 vcc, s86, v56
s_nop 1
v_cndmask_b32_e64 v56, v56, 0, vcc
v_cmp_eq_u32 vcc, s86, v58
s_nop 1
v_cndmask_b32_e64 v58, v58, 0, vcc
v_cmp_eq_u32 vcc, s86, v60
s_nop 1
v_cndmask_b32_e64 v60, v60, 0, vcc
v_cmp_eq_u32 vcc, s86, v62
s_nop 1
v_cndmask_b32_e64 v62, v62, 0, vcc
v_cmp_eq_u32 vcc, s86, v64
s_nop 1
v_cndmask_b32_e64 v64, v64, 0, vcc
v_cmp_eq_u32 vcc, s86, v66
s_nop 1
v_cndmask_b32_e64 v66, v66, 0, vcc
v_cmp_eq_u32 vcc, s86, v68
s_nop 1
v_cndmask_b32_e64 v68, v68, 0, vcc
v_cmp_eq_u32 vcc, s86, v70
s_nop 1
v_cndmask_b32_e64 v70, v70, 0, vcc
v_cmp_eq_u32 vcc, s86, v72
s_nop 1
v_cndmask_b32_e64 v72, v72, 0, vcc
v_cmp_eq_u32 vcc, s86, v74
s_nop 1
v_cndmask_b32_e64 v74, v74, 0, vcc
v_cmp_eq_u32 vcc, s86, v76
s_nop 1
v_cndmask_b32_e64 v76, v76, 0, vcc
v_cmp_eq_u32 vcc, s86, v78
s_nop 1
v_cndmask_b32_e64 v78, v78, 0, vcc
v_cmp_eq_u32 vcc, s86, v80
s_nop 1
v_cndmask_b32_e64 v80, v80, 0, vcc
v_cmp_eq_u32 vcc, s86, v82
s_nop 1
)"
R"(v_cndmask_b32_e64 v82, v82, 0, vcc
v_cmp_eq_u32 vcc, s86, v84
s_nop 1
v_cndmask_b32_e64 v84, v84, 0, vcc
v_cmp_eq_u32 vcc, s86, v86
s_nop 1
v_cndmask_b32_e64 v86, v86, 0, vcc
v_cmp_eq_u32 vcc, s86, v88
s_nop 1
v_cndmask_b32_e64 v88, v88, 0, vcc
v_cmp_eq_u32 vcc, s86, v90
s_nop 1
v_cndmask_b32_e64 v90, v90, 0, vcc
v_cmp_eq_u32 vcc, s86, v92
s_nop 1
v_cndmask_b32_e64 v92, v92, 0, vcc
v_cmp_eq_u32 vcc, s86, v94
s_nop 1
v_cndmask_b32_e64 v94, v94, 0, vcc
s_and_b32 s87, s86, 0xffff
s_sub_u32 s87, 2047, s87
s_nop 0
v_writelane_b32 v100, s87, m0
s_add_u32 m0, m0, 1
s_cmp_lt_u32 m0, 8
s_cbranch_scc1 .LL_26%=
.LL_25%=:
v_xor_b32 v102, s12, v100
v_mov_b32 v103, s13
s_mov_b64 exec, 0xff
buffer_store_dwordx2 v[102:103], v136, s[8:11], 0 offen offset:3584 sc1
s_mov_b64 exec, -1
s_branch .Lrtr_22%=
.Lspin_47%=:
s_mov_b32 s84, 0
.Llp_49%=:
s_sleep 2
buffer_load_dwordx2 v[8:9], v11, s[8:11], 0 offen offset:3840 sc1
s_waitcnt vmcnt(0)
s_add_u32 s84, s84, 1
v_cmp_eq_u64 vcc, s[12:13], v[8:9]
s_cmp_eq_u64 vcc, -1
s_cbranch_scc1 .Lok_48%=
s_cmp_lt_u32 s84, 1048576
s_cbranch_scc1 .Llp_49%=
s_branch .Lok_48%=
.Lredo%=:
s_waitcnt vmcnt(0) lgkmcnt(0)
s_barrier
s_and_b32 s81, s14, 1
s_mul_i32 s91, s81, 17164288
s_mul_i32 s82, s56, 536576
s_add_u32 s91, s91, s82
s_add_u32 s91, s91, 4194304
s_lshr_b32 s82, s14, 1
s_lshl_b32 s83, s82, 14
s_add_u32 s91, s91, s83
s_lshl_b32 s81, s81, 14
s_lshl_b32 s82, s82, 16
s_or_b32 s92, s81, s82
s_xor_b32 s92, s92, 0x10000
s_add_u32 m0, s92, 0
s_add_u32 s81, s91, 0
Dc 1,8,81,0
Dc 1,8,81,1024
Dc 1,8,81,2048
Dc 1,8,81,3072
s_add_u32 m0, s92, 4096
s_add_u32 s81, s91, 4096
Dc 1,8,81,0
Dc 1,8,81,1024
Dc 1,8,81,2048
Dc 1,8,81,3072
s_add_u32 m0, s92, 8192
s_add_u32 s81, s91, 8192
Dc 1,8,81,0
Dc 1,8,81,1024
Dc 1,8,81,2048
Dc 1,8,81,3072
s_add_u32 m0, s92, 12288
s_add_u32 s81, s91, 12288
Dc 1,8,81,0
Dc 1,8,81,1024
Dc 1,8,81,2048
Dc 1,8,81,3072
v_bfe_u32 v244, v1, 4, 1
v_mul_u32_u24 v244, 0xaaaa, v244
v_add_u32 v244, 0x4444, v244
v_lshl_or_b32 v240, v244, 16, v244
v_mov_b32 v241, 0xff800000
v_add_u32 v242, 0x10000, v1
v_bfe_u32 v244, v1, 4, 4
v_lshrrev_b32 v245, 8, v1
v_lshlrev_b32 v245, 2, v245
v_sub_u32 v244, v244, v245
v_cmp_le_i32_e64 s[72:73], 0, v244
v_cmp_le_i32_e64 s[74:75], 1, v244
v_cmp_le_i32_e64 s[76:77], 2, v244
v_cmp_le_i32_e64 s[78:79], 3, v244
v_add_u32 v243, 4096, v1
v_add_u32 v244, 8192, v1
v_add_u32 v245, 12288, v1
s_mov_b32 s18, 0x3f803f80
s_mov_b32 s19, 0x3f003f00
v_accvgpr_write_b32 a176, s18
v_accvgpr_write_b32 a180, s19
v_mov_b32 v252, v241
v_accvgpr_write_b32 a177, s18
v_accvgpr_write_b32 a181, s19
v_mov_b32 v253, v241
v_accvgpr_write_b32 a178, s18
v_accvgpr_write_b32 a182, s19
v_mov_b32 v254, v241
v_accvgpr_write_b32 a179, s18
v_accvgpr_write_b32 a183, s19
v_mov_b32 v255, v241
v_accvgpr_write_b32 a160, 0
v_accvgpr_write_b32 a161, 0
v_accvgpr_write_b32 a162, 0
v_accvgpr_write_b32 a163, 0
v_accvgpr_write_b32 a164, 0
v_accvgpr_write_b32 a165, 0
v_accvgpr_write_b32 a166, 0
v_accvgpr_write_b32 a167, 0
v_accvgpr_write_b32 a168, 0
v_accvgpr_write_b32 a169, 0
v_accvgpr_write_b32 a170, 0
v_accvgpr_write_b32 a171, 0
v_accvgpr_write_b32 a172, 0
v_accvgpr_write_b32 a173, 0
v_accvgpr_write_b32 a174, 0
v_accvgpr_write_b32 a175, 0
s_mov_b32 s90, 0x5d800000
s_mov_b64 s[88:89], 0
s_mov_b32 s64, 0
s_and_b32 s18, s14, 1
s_lshl_b32 s62, s18, 14
s_lshl_b32 s20, s57, 15
s_sub_u32 s20, 507904, s20
s_add_u32 s20, s20, s62
s_lshl_b32 s65, s57, 1
s_sub_u32 s65, s65, 1
s_max_i32 s65, s65, 0
s_sub_u32 s66, s60, 1
s_cmp_lt_u32 s14, 2
s_cselect_b32 s20, s20, 0
s_cselect_b32 s66, s66, 15
s_cselect_b32 s21, 32768, 0
s_add_u32 s61, s91, s20
s_add_u32 s62, s62, s21
s_sub_u32 s63, 16384, s21
s_lshl_b32 s67, s60, 7
s_add_u32 s67, s67, s56
s_lshl_b32 s67, s67, 12
s_add_u32 s67, s67, 39845888
s_lshl_b32 s69, s60, 3
s_add_u32 s69, s69, s59
s_lshl_b32 s69, s69, 6
s_lshl_b32 s21, s58, 3
s_add_u32 s69, s69, s21
s_add_u32 s69, s69, 20480
s_mov_b32 s70, 0
s_mov_b32 s37, 0
v_bfe_u32 v28, v1, 4, 4
v_lshrrev_b32 v29, 8, v1
v_lshlrev_b32 v29, 2, v29
v_sub_u32 v28, v28, v29
s_mov_b64 s[24:25], 0
s_mov_b32 s68, 0
s_mov_b32 s85, 99
s_lshl_b32 s80, s60, 6
s_and_b32 s81, s14, 2
s_lshl_b32 s81, s81, 4
s_xor_b32 s82, s81, 0
s_add_u32 s82, s82, s80
v_add_u32 v4, s82, v28
s_xor_b32 s82, s81, 16
s_add_u32 s82, s82, s80
v_add_u32 v5, s82, v28
s_xor_b32 s82, s81, 32
s_add_u32 s82, s82, s80
v_add_u32 v6, s82, v28
s_xor_b32 s82, s81, 48
s_add_u32 s82, s82, s80
v_add_u32 v7, s82, v28
s_mul_i32 s82, s56, 536576
s_add_u32 s82, s82, 4194304
s_lshl_b32 s84, s60, 1
s_add_u32 s84, s84, 2
.LL_50%=:
buffer_load_dwordx4 a[128:131], v1, s[8:11], s82 offen offset:0 sc1
buffer_load_dwordx4 a[132:135], v1, s[8:11], s82 offen offset:1024 sc1
buffer_load_dwordx4 a[136:139], v1, s[8:11], s82 offen offset:2048 sc1
buffer_load_dwordx4 a[140:143], v1, s[8:11], s82 offen offset:3072 sc1
buffer_load_dwordx4 a[144:147], v243, s[8:11], s82 offen offset:0 sc1
buffer_load_dwordx4 a[148:151], v243, s[8:11], s82 offen offset:1024 sc1
buffer_load_dwordx4 a[152:155], v243, s[8:11], s82 offen offset:2048 sc1
buffer_load_dwordx4 a[156:159], v243, s[8:11], s82 offen offset:3072 sc1
s_waitcnt vmcnt(0)
Zvaa 160,128,0
Zvaa 164,128,16
Zvaa 168,128,32
Zvaa 172,128,48
Mvaa 160,132,4
Mvaa 164,132,20
Mvaa 168,132,36
Mvaa 172,132,52
Mvaa 160,136,8
Mvaa 164,136,24
Mvaa 168,136,40
Mvaa 172,136,56
Mvaa 160,140,12
Mvaa 164,140,28
Mvaa 168,140,44
Mvaa 172,140,60
Zvaa 176,144,0
Zvaa 180,144,16
Zvaa 184,144,32
Zvaa 188,144,48
Mvaa 176,148,4
Mvaa 180,148,20
Mvaa 184,148,36
Mvaa 188,148,52
Mvaa 176,152,8
Mvaa 180,152,24
Mvaa 184,152,40
Mvaa 188,152,56
Mvaa 176,156,12
Mvaa 180,156,28
Mvaa 184,156,44
Mvaa 188,156,60
v_cmp_le_i32 vcc, 0, v4
s_nop 1
v_cndmask_b32 v160, v241, v160, vcc
v_max_f32 v252, v252, v160
v_cmp_le_i32 vcc, 1, v4
s_nop 1
v_cndmask_b32 v161, v241, v161, vcc
v_max_f32 v252, v252, v161
v_cmp_le_i32 vcc, 2, v4
s_nop 1
v_cndmask_b32 v162, v241, v162, vcc
v_max_f32 v252, v252, v162
v_cmp_le_i32 vcc, 3, v4
s_nop 1
v_cndmask_b32 v163, v241, v163, vcc
v_max_f32 v252, v252, v163
v_cmp_le_i32 vcc, 0, v5
s_nop 1
v_cndmask_b32 v164, v241, v164, vcc
v_max_f32 v253, v253, v164
v_cmp_le_i32 vcc, 1, v5
s_nop 1
v_cndmask_b32 v165, v241, v165, vcc
v_max_f32 v253, v253, v165
v_cmp_le_i32 vcc, 2, v5
s_nop 1
v_cndmask_b32 v166, v241, v166, vcc
v_max_f32 v253, v253, v166
v_cmp_le_i32 vcc, 3, v5
s_nop 1
v_cndmask_b32 v167, v241, v167, vcc
v_max_f32 v253, v253, v167
v_cmp_le_i32 vcc, 0, v6
s_nop 1
v_cndmask_b32 v168, v241, v168, vcc
v_max_f32 v254, v254, v168
v_cmp_le_i32 vcc, 1, v6
s_nop 1
v_cndmask_b32 v169, v241, v169, vcc
v_max_f32 v254, v254, v169
v_cmp_le_i32 vcc, 2, v6
s_nop 1
v_cndmask_b32 v170, v241, v170, vcc
v_max_f32 v254, v254, v170
v_cmp_le_i32 vcc, 3, v6
s_nop 1
v_cndmask_b32 v171, v241, v171, vcc
v_max_f32 v254, v254, v171
v_cmp_le_i32 vcc, 0, v7
s_nop 1
v_cndmask_b32 v172, v241, v172, vcc
v_max_f32 v255, v255, v172
v_cmp_le_i32 vcc, 1, v7
s_nop 1
v_cndmask_b32 v173, v241, v173, vcc
v_max_f32 v255, v255, v173
v_cmp_le_i32 vcc, 2, v7
s_nop 1
v_cndmask_b32 v174, v241, v174, vcc
v_max_f32 v255, v255, v174
v_cmp_le_i32 vcc, 3, v7
s_nop 1
v_cndmask_b32 v175, v241, v175, vcc
v_max_f32 v255, v255, v175
v_cmp_le_i32 vcc, 16, v4
s_nop 1
v_cndmask_b32 v176, v241, v176, vcc
v_max_f32 v252, v252, v176
v_cmp_le_i32 vcc, 17, v4
s_nop 1
v_cndmask_b32 v177, v241, v177, vcc
v_max_f32 v252, v252, v177
v_cmp_le_i32 vcc, 18, v4
s_nop 1
v_cndmask_b32 v178, v241, v178, vcc
v_max_f32 v252, v252, v178
v_cmp_le_i32 vcc, 19, v4
s_nop 1
v_cndmask_b32 v179, v241, v179, vcc
v_max_f32 v252, v252, v179
v_cmp_le_i32 vcc, 16, v5
s_nop 1
v_cndmask_b32 v180, v241, v180, vcc
v_max_f32 v253, v253, v180
v_cmp_le_i32 vcc, 17, v5
s_nop 1
v_cndmask_b32 v181, v241, v181, vcc
v_max_f32 v253, v253, v181
v_cmp_le_i32 vcc, 18, v5
s_nop 1
v_cndmask_b32 v182, v241, v182, vcc
v_max_f32 v253, v253, v182
v_cmp_le_i32 vcc, 19, v5
s_nop 1
v_cndmask_b32 v183, v241, v183, vcc
v_max_f32 v253, v253, v183
v_cmp_le_i32 vcc, 16, v6
s_nop 1
v_cndmask_b32 v184, v241, v184, vcc
v_max_f32 v254, v254, v184
v_cmp_le_i32 vcc, 17, v6
s_nop 1
v_cndmask_b32 v185, v241, v185, vcc
v_max_f32 v254, v254, v185
v_cmp_le_i32 vcc, 18, v6
s_nop 1
v_cndmask_b32 v186, v241, v186, vcc
v_max_f32 v254, v254, v186
v_cmp_le_i32 vcc, 19, v6
s_nop 1
v_cndmask_b32 v187, v241, v187, vcc
v_max_f32 v254, v254, v187
v_cmp_le_i32 vcc, 16, v7
s_nop 1
v_cndmask_b32 v188, v241, v188, vcc
v_max_f32 v255, v255, v188
v_cmp_le_i32 vcc, 17, v7
s_nop 1
v_cndmask_b32 v189, v241, v189, vcc
v_max_f32 v255, v255, v189
v_cmp_le_i32 vcc, 18, v7
s_nop 1
v_cndmask_b32 v190, v241, v190, vcc
v_max_f32 v255, v255, v190
v_cmp_le_i32 vcc, 19, v7
s_nop 1
v_cndmask_b32 v191, v241, v191, vcc
v_max_f32 v255, v255, v191
v_subrev_u32 v4, 32, v4
v_subrev_u32 v5, 32, v5
v_subrev_u32 v6, 32, v6
v_subrev_u32 v7, 32, v7
s_add_u32 s82, s82, 8192
s_sub_u32 s84, s84, 1
s_cmp_lg_u32 s84, 0
s_cbranch_scc1 .LL_50%=
G 28,0
v_lshlrev_b32 v32, 16, v28
v_and_b32 v33, 0xffff0000, v28
G 28,1
v_lshlrev_b32 v34, 16, v28
v_and_b32 v35, 0xffff0000, v28
G 28,2
v_lshlrev_b32 v36, 16, v28
v_and_b32 v37, 0xffff0000, v28
G 28,3
v_lshlrev_b32 v38, 16, v28
v_and_b32 v39, 0xffff0000, v28
G 28,4
v_lshlrev_b32 v40, 16, v28
v_and_b32 v41, 0xffff0000, v28
G 28,5
v_lshlrev_b32 v42, 16, v28
v_and_b32 v43, 0xffff0000, v28
G 28,6
v_lshlrev_b32 v44, 16, v28
v_and_b32 v45, 0xffff0000, v28
G 28,7
v_lshlrev_b32 v46, 16, v28
v_and_b32 v47, 0xffff0000, v28
G 28,8
v_lshlrev_b32 v48, 16, v28
v_and_b32 v49, 0xffff0000, v28
G 28,9
v_lshlrev_b32 v50, 16, v28
v_and_b32 v51, 0xffff0000, v28
G 28,10
v_lshlrev_b32 v52, 16, v28
v_and_b32 v53, 0xffff0000, v28
)"
R"(G 28,11
v_lshlrev_b32 v54, 16, v28
v_and_b32 v55, 0xffff0000, v28
G 28,12
v_lshlrev_b32 v56, 16, v28
v_and_b32 v57, 0xffff0000, v28
G 28,13
v_lshlrev_b32 v58, 16, v28
v_and_b32 v59, 0xffff0000, v28
G 28,14
v_lshlrev_b32 v60, 16, v28
v_and_b32 v61, 0xffff0000, v28
G 28,15
v_lshlrev_b32 v62, 16, v28
v_and_b32 v63, 0xffff0000, v28
G 28,16
v_lshlrev_b32 v64, 16, v28
v_and_b32 v65, 0xffff0000, v28
G 28,17
v_lshlrev_b32 v66, 16, v28
v_and_b32 v67, 0xffff0000, v28
G 28,18
v_lshlrev_b32 v68, 16, v28
v_and_b32 v69, 0xffff0000, v28
G 28,19
v_lshlrev_b32 v70, 16, v28
v_and_b32 v71, 0xffff0000, v28
G 28,20
v_lshlrev_b32 v72, 16, v28
v_and_b32 v73, 0xffff0000, v28
G 28,21
v_lshlrev_b32 v74, 16, v28
v_and_b32 v75, 0xffff0000, v28
G 28,22
v_lshlrev_b32 v76, 16, v28
v_and_b32 v77, 0xffff0000, v28
G 28,23
v_lshlrev_b32 v78, 16, v28
v_and_b32 v79, 0xffff0000, v28
G 28,24
v_lshlrev_b32 v80, 16, v28
v_and_b32 v81, 0xffff0000, v28
G 28,25
v_lshlrev_b32 v82, 16, v28
v_and_b32 v83, 0xffff0000, v28
G 28,26
v_lshlrev_b32 v84, 16, v28
v_and_b32 v85, 0xffff0000, v28
G 28,27
v_lshlrev_b32 v86, 16, v28
v_and_b32 v87, 0xffff0000, v28
G 28,28
v_lshlrev_b32 v88, 16, v28
v_and_b32 v89, 0xffff0000, v28
G 28,29
v_lshlrev_b32 v90, 16, v28
v_and_b32 v91, 0xffff0000, v28
G 28,30
v_lshlrev_b32 v92, 16, v28
v_and_b32 v93, 0xffff0000, v28
G 28,31
v_lshlrev_b32 v94, 16, v28
v_and_b32 v95, 0xffff0000, v28
G 28,32
v_lshlrev_b32 v96, 16, v28
v_and_b32 v97, 0xffff0000, v28
G 28,33
v_lshlrev_b32 v98, 16, v28
v_and_b32 v99, 0xffff0000, v28
G 28,34
v_lshlrev_b32 v100, 16, v28
v_and_b32 v101, 0xffff0000, v28
G 28,35
v_lshlrev_b32 v102, 16, v28
v_and_b32 v103, 0xffff0000, v28
G 28,36
v_lshlrev_b32 v104, 16, v28
v_and_b32 v105, 0xffff0000, v28
G 28,37
v_lshlrev_b32 v106, 16, v28
v_and_b32 v107, 0xffff0000, v28
G 28,38
v_lshlrev_b32 v108, 16, v28
v_and_b32 v109, 0xffff0000, v28
G 28,39
v_lshlrev_b32 v110, 16, v28
v_and_b32 v111, 0xffff0000, v28
G 28,40
v_lshlrev_b32 v112, 16, v28
v_and_b32 v113, 0xffff0000, v28
G 28,41
v_lshlrev_b32 v114, 16, v28
v_and_b32 v115, 0xffff0000, v28
G 28,42
v_lshlrev_b32 v116, 16, v28
v_and_b32 v117, 0xffff0000, v28
G 28,43
v_lshlrev_b32 v118, 16, v28
v_and_b32 v119, 0xffff0000, v28
G 28,44
v_lshlrev_b32 v120, 16, v28
v_and_b32 v121, 0xffff0000, v28
G 28,45
v_lshlrev_b32 v122, 16, v28
v_and_b32 v123, 0xffff0000, v28
G 28,46
v_lshlrev_b32 v124, 16, v28
v_and_b32 v125, 0xffff0000, v28
G 28,47
v_lshlrev_b32 v126, 16, v28
v_and_b32 v127, 0xffff0000, v28
G 28,48
v_lshlrev_b32 v128, 16, v28
v_and_b32 v129, 0xffff0000, v28
G 28,49
v_lshlrev_b32 v130, 16, v28
v_and_b32 v131, 0xffff0000, v28
G 28,50
v_lshlrev_b32 v132, 16, v28
v_and_b32 v133, 0xffff0000, v28
G 28,51
v_lshlrev_b32 v134, 16, v28
v_and_b32 v135, 0xffff0000, v28
G 28,52
v_lshlrev_b32 v136, 16, v28
v_and_b32 v137, 0xffff0000, v28
G 28,53
v_lshlrev_b32 v138, 16, v28
v_and_b32 v139, 0xffff0000, v28
G 28,54
v_lshlrev_b32 v140, 16, v28
v_and_b32 v141, 0xffff0000, v28
G 28,55
v_lshlrev_b32 v142, 16, v28
v_and_b32 v143, 0xffff0000, v28
G 28,56
v_lshlrev_b32 v144, 16, v28
v_and_b32 v145, 0xffff0000, v28
G 28,57
v_lshlrev_b32 v146, 16, v28
v_and_b32 v147, 0xffff0000, v28
G 28,58
v_lshlrev_b32 v148, 16, v28
v_and_b32 v149, 0xffff0000, v28
G 28,59
v_lshlrev_b32 v150, 16, v28
v_and_b32 v151, 0xffff0000, v28
G 28,60
v_lshlrev_b32 v152, 16, v28
v_and_b32 v153, 0xffff0000, v28
G 28,61
v_lshlrev_b32 v154, 16, v28
v_and_b32 v155, 0xffff0000, v28
G 28,62
v_lshlrev_b32 v156, 16, v28
v_and_b32 v157, 0xffff0000, v28
G 28,63
v_lshlrev_b32 v158, 16, v28
v_and_b32 v159, 0xffff0000, v28
s_mov_b32 s22, 1.0
s_mov_b32 s23, 1.0
s_mov_b32 s29, 134217728
s_mov_b32 s71, 3584
s_barrier
Ra 128,242,0
Ra 132,242,1024
Ra 136,242,2048
Ra 140,242,3072
s_cmp_ge_u32 s14, 2
s_cbranch_scc1 .Lbgo%=
s_branch .Lago%=
.p2align 12
.Lpb_27%=:
s_waitcnt vmcnt(6) lgkmcnt(6)
Zvaa 32,0,32
s_add_u32 s28, s28, 512
Ta 20,4,8192
Ta 22,4,9216
Mvaa 32,4,48
s_cmp_eq_u32 s28, s4
s_cselect_b32 s93, 10240, 0
Ta 24,5,0
Ta 26,5,1024
Zvaa 64,4,52
s_sub_u32 s28, s28, s93
s_add_u32 s93, s28, 20480
buffer_load_dwordx4 a[128:131], v26, s[8:11], s28 offen offset:0 sc1
Mvaa 64,0,36
Ta 28,5,8192
Ta 30,5,9216
Zvaa 96,0,40
K 184,184,185
K 185,186,187
Mvaa 96,4,56
K 186,188,189
K 187,190,191
Zvaa 128,4,60
K 192,192,193
Mvaa 128,0,44
W 8
buffer_load_dwordx4 a[132:135], v26, s[8:11], s93 offen offset:0 sc1
K 193,194,195
Zvaa 132,8,44
Ta 0,6,0
Ta 2,6,1024
Mvaa 132,12,60
K 194,196,197
K 195,198,199
Zvaa 100,12,56
K 200,200,201
K 201,202,203
Mvaa 100,8,40
Ta 4,6,8192
Ta 6,6,9216
Zvaa 68,8,36
buffer_load_dwordx4 a[136:139], v27, s[8:11], s28 offen offset:0 sc1
K 202,204,205
Mvaa 68,12,52
K 203,206,207
K 208,208,209
Zvaa 36,12,48
K 209,210,211
K 210,212,213
Mvaa 36,8,32
W 8
Ta 8,7,0
K 211,214,215
Zvaa 40,16,32
Ta 10,7,1024
K 216,216,217
Mvaa 40,20,48
buffer_load_dwordx4 a[140:143], v27, s[8:11], s93 offen offset:0 sc1
K 217,218,219
Zvaa 72,20,52
s_add_u32 s28, s28, 512
s_cmp_eq_u32 s28, s4
s_cselect_b32 s93, 10240, 0
s_sub_u32 s28, s28, s93
Mvaa 72,16,36
s_add_u32 s93, s28, 20480
K 218,220,221
K 219,222,223
Zvaa 104,16,40
Ta 12,7,8192
Ta 14,7,9216
Mvaa 104,20,56
K 224,224,225
K 225,226,227
Zvaa 136,20,60
K 226,228,229
K 227,230,231
Mvaa 136,16,44
W 8
buffer_load_dwordx4 a[144:147], v26, s[8:11], s28 offen offset:0 sc1
K 232,232,233
Zvaa 140,24,44
K 233,234,235
K 234,236,237
Mvaa 140,28,60
Ta 16,8,0
K 235,238,239
Zvaa 108,28,56
Ta 18,8,1024
K 240,240,241
Mvaa 108,24,40
K 241,242,243
K 242,244,245
Zvaa 76,24,36
buffer_load_dwordx4 a[148:151], v26, s[8:11], s93 offen offset:0 sc1
K 243,246,247
Mvaa 76,28,52
Ta 20,8,8192
Ta 22,8,9216
Zvaa 44,28,48
K 248,248,249
K 249,250,251
Mvaa 44,24,32
K 250,252,253
K 251,254,255
W 8
Zvaa 48,0,32
Ta 24,9,0
Ta 26,9,1024
Mvaa 48,4,48
buffer_load_dwordx4 a[152:155], v27, s[8:11], s28 offen offset:0 sc1
K 160,160,161
Zvaa 80,4,52
K 161,162,163
K 162,164,165
Mvaa 80,0,36
Ta 28,9,8192
K 163,166,167
Zvaa 112,0,40
Ta 30,9,9216
K 168,168,169
Mvaa 112,4,56
K 169,170,171
K 170,172,173
Zvaa 144,4,60
buffer_load_dwordx4 a[156:159], v27, s[8:11], s93 offen offset:0 sc1
K 171,174,175
Mvaa 144,0,44
W 8
s_add_u32 s28, s28, 512
s_cmp_eq_u32 s28, s4
s_cselect_b32 s93, 10240, 0
Zvaa 148,8,44
s_sub_u32 s28, s28, s93
s_add_u32 s93, s28, 20480
G 164,184
Mvaa 148,12,60
G 165,185
G 166,186
Zvaa 116,12,56
Ta 0,2,16384
Ta 2,2,17408
Mvaa 116,8,40
buffer_load_dwordx4 v[188:191], v26, s[8:11], s28 offen offset:0 sc1
G 167,187
Zvaa 84,8,36
Ta 4,2,24576
G 172,188
Mvaa 84,12,52
Ta 6,2,25600
G 173,189
Zvaa 52,12,48
G 174,190
G 175,191
Mvaa 52,8,32
W 8
Ta 8,3,16384
K 164,164,165
Zvaa 56,16,32
buffer_load_dwordx4 v[196:199], v26, s[8:11], s93 offen offset:0 sc1
Ta 10,3,17408
Mvaa 56,20,48
K 165,166,167
K 166,172,173
Zvaa 88,20,52
Ta 12,3,24576
K 167,174,175
Mvaa 88,16,36
Ta 14,3,25600
v_accvgpr_write_b32 a184, v164
Zvaa 120,16,40
Mvaa 120,20,56
buffer_load_dwordx4 v[204:207], v27, s[8:11], s28 offen offset:0 sc1
v_accvgpr_write_b32 a185, v165
Zvaa 152,20,60
v_accvgpr_write_b32 a186, v166
Mvaa 152,16,44
W 8
Ta 16,4,16384
v_accvgpr_write_b32 a187, v167
Zvaa 156,24,44
Ta 18,4,17408
v_accvgpr_write_b32 a188, v240
Mvaa 156,28,60
Zvaa 124,28,56
buffer_load_dwordx4 v[212:215], v27, s[8:11], s93 offen offset:0 sc1
v_accvgpr_write_b32 a189, v241
Mvaa 124,24,40
s_add_u32 s28, s28, 512
s_cmp_eq_u32 s28, s4
s_cselect_b32 s93, 10240, 0
s_sub_u32 s28, s28, s93
Zvaa 92,24,36
s_add_u32 s93, s28, 20480
Ta 20,4,24576
Ta 22,4,25600
Mvaa 92,28,52
v_accvgpr_write_b32 a190, v242
v_accvgpr_write_b32 a191, v243
Zvaa 60,28,48
G 164,192
Mvaa 60,24,32
s_waitcnt vmcnt(4) lgkmcnt(8)
buffer_load_dwordx4 v[220:223], v26, s[8:11], s28 offen offset:0 sc1
G 165,193
Mvaa 32,0,128
Ta 24,5,16384
Ta 26,5,17408
Mvaa 32,4,144
G 166,194
G 167,195
Mvaa 64,4,148
Ta 28,5,24576
Ta 30,5,25600
Mvaa 64,0,132
G 172,196
Mvaa 96,0,136
buffer_load_dwordx4 v[228:231], v26, s[8:11], s93 offen offset:0 sc1
G 173,197
Mvaa 96,4,152
G 174,198
Mvaa 128,4,156
G 175,199
Mvaa 128,0,140
W 8
Ta 0,6,16384
)"
R"(Ta 2,6,17408
Mvaa 132,8,140
K 164,164,165
Mvaa 132,12,156
buffer_load_dwordx4 v[236:239], v27, s[8:11], s28 offen offset:0 sc1
K 165,166,167
Mvaa 100,12,152
Ta 4,6,24576
K 166,172,173
Mvaa 100,8,136
Ta 6,6,25600
K 167,174,175
Mvaa 68,8,132
v_accvgpr_write_b32 a192, v164
Mvaa 68,12,148
Mvaa 36,12,144
buffer_load_dwordx4 v[244:247], v27, s[8:11], s93 offen offset:0 sc1
v_accvgpr_write_b32 a193, v165
Mvaa 36,8,128
W 8
s_add_u32 s28, s28, 512
s_cmp_eq_u32 s28, s4
s_cselect_b32 s93, 10240, 0
Mvaa 40,16,128
s_sub_u32 s28, s28, s93
s_add_u32 s93, s28, 20480
v_accvgpr_write_b32 a194, v166
Mvaa 40,20,144
Ta 8,7,16384
Ta 10,7,17408
Mvaa 72,20,148
v_accvgpr_write_b32 a195, v167
v_accvgpr_write_b32 a196, v248
Mvaa 72,16,132
D 22,48,65,2048
Ta 12,7,24576
Mvaa 104,16,136
Ta 14,7,25600
v_accvgpr_write_b32 a197, v249
Mvaa 104,20,152
v_accvgpr_write_b32 a198, v250
v_accvgpr_write_b32 a199, v251
Mvaa 136,20,156
G 164,200
Mvaa 136,16,140
W 8
Ta 16,8,16384
Ta 18,8,17408
Mvaa 140,24,140
buffer_load_dwordx4 a[32:35], v26, s[8:11], s28 offen offset:0 sc1
G 165,201
Mvaa 140,28,156
G 166,202
Mvaa 108,28,152
Ta 20,8,24576
G 167,203
Mvaa 108,24,136
Ta 22,8,25600
G 172,204
Mvaa 76,24,132
Mvaa 76,28,148
buffer_load_dwordx4 a[36:39], v26, s[8:11], s93 offen offset:0 sc1
G 173,205
Mvaa 44,28,144
G 174,206
Mvaa 44,24,128
W 8
Ta 24,9,16384
G 175,207
Mvaa 48,0,128
Ta 26,9,17408
K 164,164,165
Mvaa 48,4,144
Mvaa 80,4,148
buffer_load_dwordx4 a[40:43], v27, s[8:11], s28 offen offset:0 sc1
K 165,166,167
Mvaa 80,0,132
Ta 28,9,24576
Ta 30,9,25600
Mvaa 112,0,136
s_lshl_b32 s80, s56, 9
K 166,172,173
K 167,174,175
Mvaa 112,4,152
v_accvgpr_write_b32 a200, v164
Mvaa 144,4,156
s_lshl_b32 s81, s57, 4
Mvaa 144,0,140
W 8
buffer_load_dwordx4 a[44:47], v27, s[8:11], s93 offen offset:0 sc1
v_accvgpr_write_b32 a201, v165
Mvaa 148,8,140
s_add_u32 s28, s28, 512
s_cmp_eq_u32 s28, s4
s_cselect_b32 s93, 10240, 0
s_sub_u32 s28, s28, s93
Mvaa 148,12,156
s_add_u32 s93, s28, 20480
s_add_u32 s80, s80, s81
Ta 0,10,0
Mvaa 116,12,152
s_and_b32 s81, s14, 1
Ta 2,10,1024
v_accvgpr_write_b32 a202, v166
Mvaa 116,8,136
s_lshl_b32 s81, s81, 3
Ta 4,10,8192
v_accvgpr_write_b32 a203, v167
Mvaa 84,8,132
s_add_u32 s80, s80, s81
buffer_load_dwordx4 a[48:51], v26, s[8:11], s28 offen offset:0 sc1
Ta 6,10,9216
Mvaa 84,12,148
s_add_u32 s71, s80, 4096
v_accvgpr_write_b32 a204, v184
v_accvgpr_write_b32 a205, v185
Mvaa 52,12,144
v_accvgpr_write_b32 a206, v186
v_accvgpr_write_b32 a207, v187
Mvaa 52,8,128
W 8
Ta 8,11,0
Ta 10,11,1024
Mvaa 56,16,128
G 164,208
Mvaa 56,20,144
D 23,48,65,3072
G 165,209
Mvaa 88,20,148
s_add_u32 s65, s5, 16
s_cmp_ge_u32 s65, 20
s_cselect_b32 s90, 20, 0
s_sub_u32 s65, s65, s90
Mvaa 88,16,132
s_lshl_b32 s65, s65, 18
s_add_u32 m0, s63, 98304
G 166,210
Mvaa 120,16,136
Ta 12,11,8192
Ta 14,11,9216
Mvaa 120,20,152
G 167,211
G 172,212
Mvaa 152,20,156
buffer_load_dwordx4 a[52:55], v26, s[8:11], s93 offen offset:0 sc1
G 173,213
Mvaa 152,16,140
W 8
Ta 16,12,0
G 174,214
Mvaa 156,24,140
s_lshr_b32 s80, s15, 3
Ta 18,12,1024
Mvaa 156,28,156
G 175,215
Mvaa 124,28,152
s_lshl_b32 s80, s80, 1
Ta 20,12,8192
K 164,164,165
Mvaa 124,24,136
s_add_u32 s80, s80, s14
buffer_load_dwordx4 a[56:59], v27, s[8:11], s28 offen offset:0 sc1
Ta 22,12,9216
Mvaa 92,24,132
s_sub_u32 s80, s80, 2
K 165,166,167
K 166,172,173
Mvaa 92,28,148
s_lshl_b32 s80, s80, 12
Mvaa 60,28,144
K 167,174,175
v_min_u32 v28, 496, v1
Mvaa 60,24,128
s_waitcnt vmcnt(9) lgkmcnt(8)
v_accvgpr_write_b32 a208, v164
v_lshl_add_u32 v28, v28, 3, s80
Mvav 32,0,188
buffer_load_dwordx4 a[60:63], v27, s[8:11], s93 offen offset:0 sc1
Ta 24,13,0
Mvav 32,4,220
s_add_u32 s28, s28, 512
s_cmp_eq_u32 s28, s4
s_cselect_b32 s93, 10240, 0
s_sub_u32 s28, s28, s93
Mvav 64,4,228
s_add_u32 s93, s28, 20480
Ta 26,13,1024
v_min_u32 v28, .Lend%=-.Lbeg%=-4, v28
Mvav 64,0,196
v_accvgpr_write_b32 a209, v165
v_accvgpr_write_b32 a210, v166
Mvav 96,0,204
Ta 28,13,8192
Ta 30,13,9216
Mvav 96,4,236
D 16,48,65,0
v_accvgpr_write_b32 a211, v167
Mvav 128,4,244
v_accvgpr_write_b32 a212, v192
v_accvgpr_write_b32 a213, v193
Mvav 128,0,212
W 8
Ta 0,14,0
v_accvgpr_write_b32 a214, v194
Mvav 132,8,212
Ta 2,14,1024
v_add_u32 v2, 65536, v10
Mvav 132,12,244
v_accvgpr_write_b32 a215, v195
G 164,216
Mvav 100,12,236
buffer_load_dwordx4 v[184:187], v26, s[8:11], s28 offen offset:0 sc1
Ta 4,14,8192
Mvav 100,8,204
Ta 6,14,9216
v_add_u32 v3, 65536, v11
Mvav 68,8,196
G 165,217
G 166,218
Mvav 68,12,228
G 167,219
Mvav 36,12,220
G 172,220
Mvav 36,8,188
W 8
D 17,48,65,1024
Ta 8,15,0
Mvav 40,16,188
Ta 10,15,1024
v_add_u32 v4, 65536, v12
Mvav 40,20,220
G 173,221
G 174,222
Mvav 72,20,228
Ta 12,15,8192
G 175,223
Mvav 72,16,196
Ta 14,15,9216
v_add_u32 v5, 65536, v13
Mvav 104,16,204
buffer_load_dwordx4 a[128:131], v26, s[8:11], s93 offen offset:0 sc1
K 164,164,165
Mvav 104,20,236
K 165,166,167
Mvav 136,20,244
K 166,172,173
Mvav 136,16,212
W 8
Ta 16,24,0
K 167,174,175
Mvav 140,24,212
Ta 18,24,1024
v_add_u32 v6, 65536, v14
Mvav 140,28,244
buffer_load_dwordx4 a[132:135], v27, s[8:11], s28 offen offset:0 sc1
v_accvgpr_write_b32 a216, v164
Mvav 108,28,236
Ta 20,24,8192
v_accvgpr_write_b32 a217, v165
Mvav 108,24,204
Ta 22,24,9216
v_add_u32 v7, 65536, v15
Mvav 76,24,196
v_accvgpr_write_b32 a218, v166
v_accvgpr_write_b32 a219, v167
Mvav 76,28,228
v_accvgpr_write_b32 a220, v200
Mvav 44,28,220
buffer_load_dwordx4 a[136:139], v27, s[8:11], s93 offen offset:0 sc1
Mvav 44,24,188
W 8
s_add_u32 s28, s28, 512
s_cmp_eq_u32 s28, s4
s_cselect_b32 s93, 10240, 0
Mvav 48,0,188
s_sub_u32 s28, s28, s93
s_add_u32 s93, s28, 20480
v_accvgpr_write_b32 a221, v201
Mvav 48,4,220
Ta 24,25,0
Ta 26,25,1024
Mvav 80,4,228
v_add_u32 v8, 65536, v24
v_accvgpr_write_b32 a222, v202
Mvav 80,0,196
buffer_load_dwordx4 a[140:143], v26, s[8:11], s28 offen offset:0 sc1
v_accvgpr_write_b32 a223, v203
Mvav 112,0,204
Ta 28,25,8192
Ta 30,25,9216
Mvav 112,4,236
v_add_u32 v9, 65536, v25
G 164,224
Mvav 144,4,244
G 165,225
G 166,226
Mvav 144,0,212
W 8
Ta 0,10,16384
G 167,227
Mvav 148,8,212
global_load_dword v29, v28, s[6:7]
Ta 2,10,17408
Mvav 148,12,244
s_sub_u32 s74, s85, 66
s_and_b32 s75, s74, 0xffffffe3
s_cselect_b64 s[72:73], -1, 0
s_and_b32 s74, s74, 28
Mvav 116,12,236
s_lshl_b32 s74, s74, 1
v_mov_b32 v28, s74
G 172,228
Mvav 116,8,204
G 173,229
G 174,230
Mvav 84,8,196
Ta 4,10,24576
Ta 6,10,25600
Mvav 84,12,228
buffer_load_dwordx4 a[144:147], v26, s[8:11], s93 offen offset:0 sc1
G 175,231
Mvav 52,12,220
K 164,164,165
K 165,166,167
Mvav 52,8,188
W 8
Ta 8,11,16384
K 166,172,173
Mvav 56,16,188
Ta 10,11,17408
K 167,174,175
Mvav 56,20,220
v_accvgpr_write_b32 a224, v164
Mvav 88,20,228
buffer_load_dwordx4 a[148:151], v27, s[8:11], s28 offen offset:0 sc1
Ta 12,11,24576
Mvav 88,16,196
Ta 14,11,25600
v_accvgpr_write_b32 a225, v165
Mvav 120,16,204
v_accvgpr_write_b32 a226, v166
Mvav 120,20,236
v_accvgpr_write_b32 a227, v167
Mvav 152,20,244
v_accvgpr_write_b32 a228, v208
Mvav 152,16,212
W 8
D 18,48,65,2048
Ta 16,12,16384
Mvav 156,24,212
Ta 18,12,17408
v_accvgpr_write_b32 a229, v209
Mvav 156,28,244
v_accvgpr_write_b32 a230, v210
Mvav 124,28,236
Ta 20,12,24576
v_accvgpr_write_b32 a231, v211
Mvav 124,24,204
Ta 22,12,25600
G 164,232
Mvav 92,24,196
buffer_load_dwordx4 a[152:155], v27, s[8:11], s93 offen offset:0 sc1
G 165,233
Mvav 92,28,228
s_add_u32 s28, s28, 512
s_cmp_eq_u32 s28, s4
s_cselect_b32 s93, 10240, 0
s_sub_u32 s28, s28, s93
Mvav 60,28,220
s_add_u32 s93, s28, 20480
G 166,234
G 167,235
Mvav 60,24,188
s_waitcnt vmcnt(12) lgkmcnt(8)
G 172,236
G 173,237
Mvaa 32,0,32
Ta 24,13,16384
Ta 26,13,17408
Mvaa 32,4,48
buffer_load_dwordx2 v[28:29], v28, s[8:11], 0 offen offset:3584 sc1
G 174,238
Mvaa 64,4,52
G 175,239
K 164,164,165
Mvaa 64,0,36
Ta 28,13,24576
Ta 30,13,25600
Mvaa 96,0,40
K 165,166,167
K 166,172,173
Mvaa 96,4,56
K 167,174,175
v_accvgpr_write_b32 a232, v164
Mvaa 128,4,60
buffer_load_dwordx4 a[156:159], v26, s[8:11], s28 offen offset:0 sc1
v_accvgpr_write_b32 a233, v165
Mvaa 128,0,44
W 8
v_accvgpr_write_b32 a234, v166
)"
R"(v_accvgpr_write_b32 a235, v167
Mvaa 132,8,44
Ta 0,14,16384
Ta 2,14,17408
Mvaa 132,12,60
G 164,160
G 165,161
Mvaa 100,12,56
G 166,162
G 167,163
Mvaa 100,8,40
buffer_load_dwordx4 v[192:195], v26, s[8:11], s93 offen offset:0 sc1
Ta 4,14,24576
Mvaa 68,8,36
Ta 6,14,25600
G 172,164
Mvaa 68,12,52
G 173,165
G 174,166
Mvaa 36,12,48
G 175,167
K 164,164,165
Mvaa 36,8,32
W 8
Ta 8,15,16384
K 165,166,167
Mvaa 40,16,32
buffer_load_dwordx4 v[200:203], v27, s[8:11], s28 offen offset:0 sc1
Ta 10,15,17408
Mvaa 40,20,48
K 166,172,173
K 167,174,175
Mvaa 72,20,52
s_cmp_lg_u32 s72, 0
s_cbranch_scc1 .LL_51%=
s_waitcnt vmcnt(3)
.LL_51%=:
v_xor_b32 v28, s12, v28
v_xor_b32 v29, s13, v29
v_lshrrev_b32 v248, 11, v28
v_or_b32 v29, v29, v248
v_cmp_eq_u32 vcc, 0, v29
s_or_b64 vcc, vcc, s[72:73]
s_cmp_eq_u64 vcc, -1
s_cbranch_scc0 .Lspin_52%=
.Lok_53%=:
v_accvgpr_write_b32 a236, v164
v_accvgpr_write_b32 a237, v165
Mvaa 72,16,36
Ta 12,15,24576
Ta 14,15,25600
Mvaa 104,16,40
v_accvgpr_write_b32 a238, v166
v_accvgpr_write_b32 a239, v167
Mvaa 104,20,56
D 19,48,65,3072
G 164,240
Mvaa 136,20,60
s_add_u32 m0, s63, 102400
G 165,241
G 166,242
Mvaa 136,16,44
W 8
v_bfe_u32 v248, v28, 3, 8
G 167,243
Mvaa 140,24,44
Ta 16,24,16384
Ta 18,24,17408
Mvaa 140,28,60
v_mul_u32_u24 v248, 10240, v248
G 172,244
Mvaa 108,28,56
buffer_load_dwordx4 v[188:191], v27, s[8:11], s93 offen offset:0 sc1
G 173,245
Mvaa 108,24,40
s_add_u32 s28, s28, 512
s_cmp_eq_u32 s28, s4
s_cselect_b32 s93, 10240, 0
s_sub_u32 s28, s28, s93
Mvaa 76,24,36
s_add_u32 s93, s28, 20480
v_and_b32 v28, 7, v28
G 174,246
Mvaa 76,28,52
v_lshl_add_u32 v248, v28, 4, v248
G 175,247
Mvaa 44,28,48
Ta 20,24,24576
Ta 22,24,25600
Mvaa 44,24,32
W 8
buffer_load_dwordx4 v[196:199], v26, s[8:11], s28 offen offset:0 sc1
K 164,164,165
Mvaa 48,0,32
v_lshl_add_u32 v248, v1, 3, v248
K 165,166,167
Mvaa 48,4,48
v_lshrrev_b32 v29, 8, v1
K 166,172,173
Mvaa 80,4,52
K 167,174,175
v_lshlrev_b32 v29, 28, v29
Mvaa 80,0,36
v_accvgpr_write_b32 a240, v164
v_or_b32 v249, v248, v29
Mvaa 112,0,40
s_and_b32 s75, s72, 134217728
buffer_load_dwordx4 v[204:207], v26, s[8:11], s93 offen offset:0 sc1
v_accvgpr_write_b32 a241, v165
Mvaa 112,4,56
s_add_u32 s76, s75, 131072
Ta 24,25,16384
Ta 26,25,17408
Mvaa 144,4,60
v_accvgpr_write_b32 a242, v166
v_accvgpr_write_b32 a243, v167
Mvaa 144,0,44
W 6
G 164,168
G 165,169
Mvaa 148,8,44
Ta 28,25,24576
Ta 30,25,25600
Mvaa 148,12,60
D 20,48,65,0
G 166,170
Mvaa 116,12,56
G 167,171
G 172,172
Mvaa 116,8,40
G 173,173
G 174,174
Mvaa 84,8,36
Ta 0,2,0
G 175,175
Mvaa 84,12,52
Ta 2,2,1024
K 164,164,165
Mvaa 52,12,48
buffer_load_dwordx4 v[212:215], v27, s[8:11], s28 offen offset:0 sc1
K 165,166,167
Mvaa 52,8,32
W 6
K 166,172,173
K 167,174,175
Mvaa 56,16,32
Ta 4,2,8192
Ta 6,2,9216
Mvaa 56,20,48
v_accvgpr_write_b32 a244, v164
v_accvgpr_write_b32 a245, v165
Mvaa 88,20,52
v_accvgpr_write_b32 a246, v166
v_accvgpr_write_b32 a247, v167
Mvaa 88,16,36
buffer_load_dwordx4 v[220:223], v27, s[8:11], s93 offen offset:0 sc1
G 164,64
Mvaa 120,16,40
s_add_u32 s28, s28, 512
s_cmp_eq_u32 s28, s4
s_cselect_b32 s93, 10240, 0
s_sub_u32 s28, s28, s93
Mvaa 120,20,56
s_add_u32 s93, s28, 20480
Ta 8,3,0
G 165,65
Mvaa 152,20,60
Ta 10,3,1024
G 166,66
Mvaa 152,16,44
W 6
G 167,67
G 172,68
Mvaa 156,24,44
buffer_load_dwordx4 v[228:231], v26, s[8:11], s28 offen offset:0 sc1
G 173,69
Mvaa 156,28,60
Ta 12,3,8192
Ta 14,3,9216
Mvaa 124,28,56
G 174,70
G 175,71
Mvaa 124,24,40
K 164,164,165
K 165,166,167
Mvaa 92,24,36
K 166,172,173
K 167,174,175
Mvaa 92,28,52
buffer_load_dwordx4 v[240:243], v248, s[8:11], s76 offen sc1
Ta 16,4,0
Mvaa 60,28,48
s_add_u32 s76, s76, 8192
Ta 18,4,1024
v_accvgpr_write_b32 a64, v164
Mvaa 60,24,32
s_waitcnt vmcnt(13) lgkmcnt(6)
v_accvgpr_write_b32 a65, v165
v_accvgpr_write_b32 a66, v166
Mvav 32,0,184
Ta 20,4,8192
v_accvgpr_write_b32 a67, v167
Mvaa 32,4,140
Ta 22,4,9216
G 164,176
Mvaa 64,4,144
buffer_load_dwordx4 v[244:247], v249, s[8:11], s76 offen sc1
v_or_b32 v249, v1, v29
Mvaa 64,0,128
s_mul_i32 s76, s74, 160
s_add_u32 s76, s76, s75
s_add_u32 s76, s76, 118784
Mvaa 96,0,132
G 165,177
G 166,178
Mvaa 96,4,148
G 167,179
G 172,180
Mvaa 128,4,152
Ta 24,5,0
G 173,181
Mvaa 128,0,136
W 5
buffer_load_dwordx4 v[236:239], v26, s[8:11], s93 offen offset:0 sc1
Ta 26,5,1024
Mvaa 132,8,136
G 174,182
G 175,183
Mvaa 132,12,152
K 164,164,165
K 165,166,167
Mvaa 100,12,148
Ta 28,5,8192
Ta 30,5,9216
Mvaa 100,8,132
K 166,172,173
K 167,174,175
Mvaa 68,8,128
D 21,48,65,1024
v_accvgpr_write_b32 a68, v164
Mvaa 68,12,144
v_accvgpr_write_b32 a69, v165
v_accvgpr_write_b32 a70, v166
Mvaa 36,12,140
Ta 0,6,0
v_accvgpr_write_b32 a71, v167
Mvav 36,8,184
W 5
Ta 2,6,1024
G 164,72
Mvav 40,16,184
G 165,73
G 166,74
Mvaa 40,20,140
buffer_load_dwordx4 v[208:211], v27, s[8:11], s28 offen offset:0 sc1
Ta 4,6,8192
Mvaa 72,20,144
Ta 6,6,9216
G 167,75
Mvaa 72,16,128
G 172,76
G 173,77
Mvaa 104,16,132
Ta 8,7,0
G 174,78
Mvaa 104,20,148
Ta 10,7,1024
G 175,79
Mvaa 136,20,152
buffer_load_dwordx4 a[32:35], v27, s[8:11], s93 offen offset:0 sc1
K 164,164,165
Mvaa 136,16,136
W 6
s_add_u32 s28, s28, 512
s_cmp_eq_u32 s28, s4
s_cselect_b32 s93, 10240, 0
Mvaa 140,24,136
s_sub_u32 s28, s28, s93
s_add_u32 s93, s28, 20480
Ta 12,7,8192
Mvaa 140,28,152
Ta 14,7,9216
K 165,166,167
Mvaa 108,28,148
K 166,172,173
K 167,174,175
Mvaa 108,24,132
buffer_load_dwordx4 a[36:39], v26, s[8:11], s28 offen offset:0 sc1
v_accvgpr_write_b32 a72, v164
Mvaa 76,24,128
Ta 16,8,0
Ta 18,8,1024
Mvaa 76,28,144
v_accvgpr_write_b32 a73, v165
v_accvgpr_write_b32 a74, v166
Mvaa 44,28,140
Ta 20,8,8192
v_accvgpr_write_b32 a75, v167
Mvav 44,24,184
W 7
Ta 22,8,9216
v_accvgpr_write_b32 a76, v216
Mvav 48,0,184
D 22,48,65,2048
v_accvgpr_write_b32 a77, v217
Mvaa 48,4,140
v_accvgpr_write_b32 a78, v218
v_accvgpr_write_b32 a79, v219
Mvaa 80,4,144
Ta 24,9,0
Ta 26,9,1024
Mvaa 80,0,128
G 164,80
G 165,81
Mvaa 112,0,132
Ta 28,9,8192
Ta 30,9,9216
Mvaa 112,4,148
buffer_load_dwordx4 a[40:43], v26, s[8:11], s93 offen offset:0 sc1
G 166,82
Mvaa 144,4,152
G 167,83
G 172,84
Mvaa 144,0,136
W 8
Ta 0,2,16384
G 173,85
Mvaa 148,8,136
Ta 2,2,17408
G 174,86
Mvaa 148,12,152
G 175,87
Mvaa 116,12,148
buffer_load_dwordx4 a[44:47], v27, s[8:11], s28 offen offset:0 sc1
Ta 4,2,24576
Mvaa 116,8,132
Ta 6,2,25600
K 164,164,165
Mvaa 84,8,128
K 165,166,167
Mvaa 84,12,144
K 166,172,173
Mvaa 52,12,140
K 167,174,175
Mvav 52,8,184
W 8
buffer_load_dwordx4 a[48:51], v27, s[8:11], s93 offen offset:0 sc1
Ta 8,3,16384
Mvav 56,16,184
s_add_u32 s28, s28, 512
s_cmp_eq_u32 s28, s4
s_cselect_b32 s93, 10240, 0
D 23,48,65,3072
Mvaa 56,20,140
s_sub_u32 s28, s28, s93
s_add_u32 s93, s28, 20480
Ta 10,3,17408
Mvaa 88,20,144
v_accvgpr_write_b32 a80, v164
v_accvgpr_write_b32 a81, v165
Mvaa 88,16,128
Ta 12,3,24576
v_accvgpr_write_b32 a82, v166
Mvaa 120,16,132
Ta 14,3,25600
v_accvgpr_write_b32 a83, v167
Mvaa 120,20,148
buffer_load_dwordx4 a[52:55], v26, s[8:11], s28 offen offset:0 sc1
v_accvgpr_write_b32 a84, v224
Mvaa 152,20,152
v_lshrrev_b32 v20, 4, v1
v_accvgpr_write_b32 a85, v225
Mvaa 152,16,136
W 8
v_accvgpr_write_b32 a86, v226
v_add_u32 v20, 0, v20
Mvaa 156,24,136
Ta 16,4,16384
Ta 18,4,17408
Mvaa 156,28,152
v_accvgpr_write_b32 a87, v227
v_mul_u32_u24 v22, 205, v20
Mvaa 124,28,148
buffer_load_dwordx4 a[56:59], v26, s[8:11], s93 offen offset:0 sc1
G 164,88
Mvaa 124,24,132
Ta 20,4,24576
Ta 22,4,25600
Mvaa 92,24,128
v_lshrrev_b32 v22, 12, v22
G 165,89
Mvaa 92,28,144
G 166,90
v_mul_u32_u24 v23, 20, v22
Mvaa 60,28,140
G 167,91
v_sub_u32 v20, v20, v23
Mvav 60,24,184
s_waitcnt vmcnt(15) lgkmcnt(8)
buffer_load_dwordx4 a[60:63], v27, s[8:11], s28 offen offset:0 sc1
)"
R"(G 172,92
Mvaa 32,0,156
Ta 24,5,16384
Ta 26,5,17408
Mvav 32,4,196
v_lshrrev_b32 v23, 1, v22
G 173,93
Mvav 64,4,204
G 174,94
v_and_b32 v23, 2, v23
Mvav 64,0,192
Ta 28,5,24576
Ta 30,5,25600
Mvav 96,0,200
buffer_load_dwordx4 v[216:219], v27, s[8:11], s93 offen offset:0 sc1
G 175,95
Mvav 96,4,212
s_add_u32 s28, s28, 512
s_cmp_eq_u32 s28, s4
s_cselect_b32 s93, 10240, 0
s_sub_u32 s28, s28, s93
Mvav 128,4,220
s_add_u32 s93, s28, 20480
v_xor_b32 v20, v20, v23
K 164,164,165
Mvav 128,0,188
W 8
v_mul_u32_u24 v22, 80, v22
K 165,166,167
Mvav 132,8,188
K 166,172,173
v_add_lshl_u32 v16, v20, v22, 4
Mvav 132,12,220
buffer_load_dwordx4 v[184:187], v26, s[8:11], s28 offen offset:0 sc1
Ta 0,6,16384
Mvav 100,12,212
Ta 2,6,17408
K 167,174,175
Mvav 100,8,200
v_lshrrev_b32 v20, 4, v1
v_accvgpr_write_b32 a88, v164
Mvav 68,8,192
v_add_u32 v20, 64, v20
v_accvgpr_write_b32 a89, v165
Mvav 68,12,204
Ta 4,6,24576
Ta 6,6,25600
Mvav 36,12,196
s_cmp_lg_u32 s72, 0
s_cbranch_scc1 .LL_55%=
s_waitcnt vmcnt(15)
.LL_55%=:
buffer_store_dwordx4 v[240:243], v1, s[8:11], s76 offen sc1
v_accvgpr_write_b32 a90, v166
v_mul_u32_u24 v22, 205, v20
Mvaa 36,8,156
W 8
v_accvgpr_write_b32 a91, v167
v_lshrrev_b32 v22, 12, v22
Mvaa 40,16,156
v_accvgpr_write_b32 a92, v232
v_mul_u32_u24 v23, 20, v22
Mvav 40,20,196
Ta 8,7,16384
Ta 10,7,17408
Mvav 72,20,204
v_accvgpr_write_b32 a93, v233
v_accvgpr_write_b32 a94, v234
Mvav 72,16,192
buffer_store_dwordx4 v[244:247], v249, s[8:11], s76 offen offset:1024 sc1
v_sub_u32 v20, v20, v23
Mvav 104,16,200
s_add_u32 s76, s74, s75
s_add_u32 s76, s76, 3840
v_accvgpr_write_b32 a95, v235
Mvav 104,20,212
Ta 12,7,24576
Ta 14,7,25600
Mvav 136,20,220
v_lshrrev_b32 v23, 1, v22
G 164,96
Mvav 136,16,188
W 8
G 165,97
v_and_b32 v23, 2, v23
Mvav 140,24,188
buffer_load_dwordx4 a[128:131], v26, s[8:11], s93 offen offset:0 sc1
G 166,98
Mvav 140,28,220
G 167,99
v_xor_b32 v20, v20, v23
Mvav 108,28,212
G 172,100
G 173,101
Mvav 108,24,200
Ta 16,8,16384
Ta 18,8,17408
Mvav 76,24,192
v_mul_u32_u24 v22, 80, v22
G 174,102
Mvav 76,28,204
buffer_load_dwordx4 a[132:135], v27, s[8:11], s28 offen offset:0 sc1
G 175,103
Mvav 44,28,196
v_add_lshl_u32 v17, v20, v22, 4
K 164,164,165
Mvaa 44,24,156
W 6
Ta 20,8,24576
Ta 22,8,25600
Mvaa 48,0,156
K 165,166,167
K 166,172,173
Mvav 48,4,196
v_subrev_u32 v17, 1024, v17
K 167,174,175
Mvav 80,4,204
buffer_load_dwordx4 a[136:139], v27, s[8:11], s93 offen offset:0 sc1
v_accvgpr_write_b32 a96, v164
Mvav 80,0,192
s_add_u32 s28, s28, 512
v_lshrrev_b32 v20, 4, v1
v_accvgpr_write_b32 a97, v165
Mvav 112,0,200
s_cmp_eq_u32 s28, s4
s_cselect_b32 s93, 10240, 0
v_accvgpr_write_b32 a98, v166
v_add_u32 v20, 128, v20
Mvav 112,4,212
s_sub_u32 s28, s28, s93
Ta 24,9,16384
v_accvgpr_write_b32 a99, v167
Mvav 144,4,220
s_add_u32 s93, s28, 20480
Ta 26,9,17408
G 164,248
Mvav 144,0,188
W 6
buffer_load_dwordx4 a[140:143], v26, s[8:11], s28 offen offset:0 sc1
v_mul_u32_u24 v22, 205, v20
Mvav 148,8,188
G 165,249
G 166,250
Mvav 148,12,220
Ta 28,9,24576
Ta 30,9,25600
Mvav 116,12,212
v_lshrrev_b32 v22, 12, v22
G 167,251
Mvav 116,8,200
G 172,252
G 173,253
Mvav 84,8,192
s_waitcnt vmcnt(11) lgkmcnt(0)
s_barrier
Mvav 84,12,204
v_mul_u32_u24 v23, 20, v22
G 174,254
Mvav 52,12,196
G 175,255
v_sub_u32 v20, v20, v23
Mvaa 52,8,156
K 164,164,165
K 165,166,167
Mvaa 56,16,156
v_lshrrev_b32 v23, 1, v22
K 166,172,173
Mvav 56,20,196
buffer_load_dwordx4 a[144:147], v26, s[8:11], s93 offen offset:0 sc1
K 167,174,175
Mvav 88,20,204
v_and_b32 v23, 2, v23
v_accvgpr_write_b32 a100, v164
Mvav 88,16,192
v_accvgpr_write_b32 a101, v165
v_xor_b32 v20, v20, v23
Mvav 120,16,200
v_accvgpr_write_b32 a102, v166
v_accvgpr_write_b32 a103, v167
Mvav 120,20,212
Ta 0,10,32768
Ta 2,10,33792
Mvav 152,20,220
Ta 4,10,40960
Ta 6,10,41984
Mvav 152,16,188
G 164,104
v_mul_u32_u24 v22, 80, v22
Mvav 156,24,188
G 165,105
v_add_lshl_u32 v18, v20, v22, 4
Mvav 156,28,220
Ta 8,11,32768
G 166,106
Mvav 124,28,212
Ta 10,11,33792
G 167,107
Mvav 124,24,200
buffer_load_dwordx4 a[148:151], v27, s[8:11], s28 offen offset:0 sc1
v_subrev_u32 v18, 2048, v18
Mvav 92,24,192
G 172,108
G 173,109
Mvav 92,28,204
Ta 12,11,40960
v_lshrrev_b32 v20, 4, v1
Mvav 60,28,196
Ta 14,11,41984
G 174,110
Mvaa 60,24,156
W 4
v_add_u32 v20, 192, v20
G 175,111
Mvav 32,0,228
K 164,164,165
v_mul_u32_u24 v22, 205, v20
Mvaa 32,4,36
K 165,166,167
v_lshrrev_b32 v22, 12, v22
Mvaa 64,4,40
Ta 16,12,32768
K 166,172,173
Mvav 64,0,236
Ta 18,12,33792
K 167,174,175
Mvav 96,0,208
buffer_load_dwordx4 a[152:155], v27, s[8:11], s93 offen offset:0 sc1
v_mul_u32_u24 v23, 20, v22
Mvaa 96,4,44
s_add_u32 s28, s28, 512
v_accvgpr_write_b32 a104, v164
v_accvgpr_write_b32 a105, v165
Mvaa 128,4,48
s_cmp_eq_u32 s28, s4
s_cselect_b32 s93, 10240, 0
Ta 20,12,40960
v_sub_u32 v20, v20, v23
Mvaa 128,0,32
W 3
Ta 22,12,41984
v_accvgpr_write_b32 a106, v166
Mvaa 132,8,32
s_sub_u32 s28, s28, s93
v_lshrrev_b32 v23, 1, v22
v_accvgpr_write_b32 a107, v167
Mvaa 132,12,48
s_add_u32 s93, s28, 20480
K 176,176,177
v_and_b32 v23, 2, v23
Mvaa 100,12,44
K 177,178,179
K 178,180,181
Mvav 100,8,208
Ta 24,13,32768
v_xor_b32 v20, v20, v23
Mvav 68,8,236
Ta 26,13,33792
K 179,182,183
Mvaa 68,12,40
v_accvgpr_write_b32 a108, v176
v_accvgpr_write_b32 a109, v177
v_accvgpr_write_b32 a110, v178
v_accvgpr_write_b32 a111, v179
Mvaa 36,12,36
buffer_load_dwordx4 v[224:227], v26, s[8:11], s28 offen offset:0 sc1
v_mul_u32_u24 v22, 80, v22
Mvav 36,8,228
W 2
Ta 28,13,40960
Ta 30,13,41984
Mvav 40,16,228
v_add_lshl_u32 v19, v20, v22, 4
v_subrev_u32 v19, 3072, v19
Mvaa 40,20,36
G 164,112
v_lshrrev_b32 v20, 4, v1
Mvaa 72,20,40
Ta 0,14,32768
G 165,113
Mvav 72,16,236
s_cmp_lg_u32 s72, 0
s_cbranch_scc1 .LL_56%=
s_waitcnt vmcnt(8)
.LL_56%=:
v_mov_b64 v[28:29], s[12:13]
s_mov_b64 exec, 1
buffer_store_dwordx2 v[28:29], off, s[8:11], s76 sc1
s_mov_b64 exec, -1
Ta 2,14,33792
G 166,114
Mvav 104,16,208
v_add_u32 v20, 256, v20
G 167,115
Mvaa 104,20,44
Ta 4,14,40960
v_mul_u32_u24 v22, 205, v20
Mvaa 136,20,48
Ta 6,14,41984
G 172,116
Mvaa 136,16,32
W 4
v_lshrrev_b32 v22, 12, v22
G 173,117
Mvaa 140,24,32
buffer_load_dwordx4 v[232:235], v26, s[8:11], s93 offen offset:0 sc1
G 174,118
Mvaa 140,28,48
v_mul_u32_u24 v23, 20, v22
G 175,119
Mvaa 108,28,44
Ta 8,15,32768
v_sub_u32 v20, v20, v23
Mvav 108,24,208
Ta 10,15,33792
K 164,164,165
Mvav 76,24,236
v_lshrrev_b32 v23, 1, v22
K 165,166,167
Mvaa 76,28,40
buffer_load_dwordx4 a[248:251], v27, s[8:11], s28 offen offset:0 sc1
K 166,172,173
Mvaa 44,28,36
Ta 12,15,40960
v_and_b32 v23, 2, v23
Mvav 44,24,228
W 3
Ta 14,15,41984
K 167,174,175
Mvav 48,0,228
v_xor_b32 v20, v20, v23
v_accvgpr_write_b32 a112, v164
Mvaa 48,4,36
v_accvgpr_write_b32 a113, v165
v_mul_u32_u24 v22, 80, v22
Mvaa 80,4,40
Ta 16,24,32768
v_accvgpr_write_b32 a114, v166
Mvav 80,0,236
Ta 18,24,33792
v_add_lshl_u32 v20, v20, v22, 4
Mvav 112,0,208
buffer_load_dwordx4 a[252:255], v27, s[8:11], s93 offen offset:0 sc1
v_accvgpr_write_b32 a115, v167
Mvaa 112,4,44
s_add_u32 s28, s28, 512
v_lshrrev_b32 v22, 6, v1
v_accvgpr_write_b32 a116, v168
Mvaa 144,4,48
v_accvgpr_write_b32 a117, v169
v_accvgpr_write_b32 a118, v170
v_accvgpr_write_b32 a119, v171
Mvaa 144,0,32
W 2
Ta 20,24,40960
Ta 22,24,41984
Mvaa 148,8,32
s_cmp_eq_u32 s28, s4
s_cselect_b32 s93, 10240, 0
v_mul_u32_u24 v22, 320, v22
v_bfe_u32 v23, v1, 4, 2
Mvaa 148,12,48
s_sub_u32 s28, s28, s93
v_lshl_add_u32 v22, v23, 3, v22
G 164,120
Mvaa 116,12,44
s_add_u32 s93, s28, 20480
Ta 24,25,32768
Ta 26,25,33792
Mvav 116,8,208
G 165,121
v_bfe_u32 v23, v1, 8, 1
Mvav 84,8,236
buffer_load_dwordx4 v[192:195], v26, s[8:11], s28 offen offset:0 sc1
G 166,122
Mvaa 84,12,40
v_lshlrev_b32 v23, 5, v23
G 167,123
Mvaa 52,12,36
Ta 28,25,40960
Ta 30,25,41984
Mvav 52,8,228
W 4
v_add_u32 v21, v22, v23
G 172,124
Mvav 56,16,228
G 173,125
v_sub_u32 v22, v22, v23
Mvaa 56,20,36
s_mul_i32 s94, s59, 655360
Ta 0,10,49152
G 174,126
Mvaa 88,20,40
s_mul_i32 s95, s58, 320
Ta 2,10,50176
G 175,127
Mvav 88,16,236
s_add_u32 s94, s94, s95
buffer_load_dwordx4 v[200:203], v26, s[8:11], s93 offen offset:0 sc1
K 164,164,165
Mvav 120,16,208
)"
R"(Ta 4,10,57344
K 165,166,167
Mvaa 120,20,44
Ta 6,10,58368
K 166,172,173
Mvaa 152,20,48
s_add_u32 s40, s26, s94
s_addc_u32 s41, s27, 0
K 167,174,175
Mvaa 152,16,32
W 4
v_accvgpr_write_b32 a120, v164
v_accvgpr_write_b32 a121, v165
Mvaa 156,24,32
s_and_b32 s41, s41, 0xffff
Ta 8,11,49152
Ta 10,11,50176
Mvaa 156,28,48
s_mov_b32 s42, 5242880
buffer_load_dwordx4 v[188:191], v27, s[8:11], s28 offen offset:0 sc1
v_accvgpr_write_b32 a122, v166
Mvaa 124,28,44
s_mov_b32 s43, 0x20000
Ta 12,11,57344
v_accvgpr_write_b32 a123, v167
Mvav 124,24,208
s_sub_u32 s42, s42, s94
Ta 14,11,58368
v_accvgpr_write_b32 a124, v160
Mvav 92,24,236
s_mov_b64 s[44:45], s[8:9]
v_accvgpr_write_b32 a125, v161
v_accvgpr_write_b32 a126, v162
Mvaa 92,28,40
s_mov_b32 s47, 0x20000
Ta 16,12,49152
v_accvgpr_write_b32 a127, v163
Mvaa 60,28,36
s_and_b32 s48, s14, 1
Ta 18,12,50176
buffer_load_dwordx4 v[196:199], v27, s[8:11], s93 offen offset:0 sc1
Mvav 60,24,228
s_waitcnt vmcnt(13) lgkmcnt(6)
v_bfe_u32 v28, v1, 4, 2
v_lshlrev_b32 v28, 3, v28
Mvaa 32,0,52
s_lshl_b32 s80, s56, 9
Ta 20,12,57344
Ta 22,12,58368
Mvav 32,4,184
s_mul_i32 s48, s48, 5120
s_add_u32 s80, s80, 4096
s_lshr_b32 s51, s14, 1
Mvaa 64,4,128
s_and_b32 s94, s14, 1
Ta 24,13,49152
Ta 26,13,50176
Mvaa 64,0,56
s_add_u32 s28, s28, 512
s_cmp_eq_u32 s28, s4
s_cselect_b32 s93, 10240, 0
buffer_load_dwordx2 v[28:29], v28, s[8:11], s80 offen sc1
Mvaa 96,0,60
s_sub_u32 s28, s28, s93
s_add_u32 s93, s28, 20480
s_lshl_b32 s95, s57, 1
Mvaa 96,4,132
s_add_u32 s94, s94, s95
Ta 28,13,57344
Ta 30,13,58368
Mvaa 128,4,136
s_lshl_b32 s54, s94, 3
s_add_u32 s54, s54, s59
Mvav 128,0,216
W 8
Ta 0,14,49152
Ta 2,14,50176
Mvav 132,8,216
s_lshl_b32 s54, s54, 6
buffer_load_dwordx4 v[204:207], v26, s[8:11], s28 offen offset:0 sc1
Mvaa 132,12,136
s_xor_b32 s55, s54, 15872
Mvaa 100,12,132
Ta 4,14,57344
Ta 6,14,58368
Mvaa 100,8,60
s_add_u32 s54, s54, 20480
Mvaa 68,8,56
s_and_b32 s81, s14, 1
Mvaa 68,12,128
s_add_u32 s55, s55, 20480
buffer_load_dwordx4 v[212:215], v26, s[8:11], s93 offen offset:0 sc1
Mvav 36,12,184
Mvaa 36,8,52
W 8
s_and_b32 s95, s14, 2
s_mul_i32 s91, s81, 17164288
Mvaa 40,16,52
s_lshl_b32 s94, s94, 2
Ta 8,15,49152
Ta 10,15,50176
Mvav 40,20,184
s_mul_i32 s82, s56, 536576
Mvaa 72,20,128
s_add_u32 s94, s94, s95
buffer_load_dwordx4 v[220:223], v27, s[8:11], s28 offen offset:0 sc1
Ta 12,15,57344
Mvaa 72,16,56
Ta 14,15,58368
Mvaa 104,16,60
s_lshl_b32 s95, s95, 4
s_add_u32 s91, s91, s82
Mvaa 104,20,132
Mvaa 136,20,136
s_add_u32 s55, s55, s95
s_add_u32 s91, s91, 4194304
Mvav 136,16,216
W 8
s_lshl_b32 s49, s94, 17
Ta 16,24,49152
Mvav 140,24,216
Ta 18,24,50176
buffer_load_dwordx4 v[176:179], v27, s[8:11], s93 offen offset:0 sc1
Mvaa 140,28,136
s_lshl_b32 s95, s59, 14
s_lshr_b32 s82, s14, 1
Mvaa 108,28,132
Ta 20,24,57344
Ta 22,24,58368
Mvaa 108,24,60
s_add_u32 s49, s49, s95
s_lshl_b32 s83, s82, 14
Mvaa 76,24,56
s_xor_b32 s50, s49, 16252928
Mvaa 76,28,128
Mvav 44,28,184
s_add_u32 s91, s91, s83
s_add_u32 s49, s49, 39845888
Mvaa 44,24,52
W 8
Ta 24,25,49152
Ta 26,25,50176
Mvaa 48,0,52
s_add_u32 s50, s50, 39845888
s_lshl_b32 s81, s81, 14
Mvav 48,4,184
Mvaa 80,4,128
s_lshr_b32 s94, s15, 3
Ta 28,25,57344
Ta 30,25,58368
Mvaa 80,0,56
s_lshl_b32 s82, s82, 16
s_lshl_b32 s94, s94, 3
Mvaa 112,0,60
Mvaa 112,4,132
s_add_u32 s94, s94, s14
s_or_b32 s92, s81, s82
Mvaa 144,4,136
Mvav 144,0,216
W 8
s_lshl_b32 s94, s94, 4
Ta 0,2,32768
Mvav 148,8,216
s_xor_b32 s92, s92, 0x10000
s_add_u32 s94, s94, s59
Ta 2,2,33792
Mvaa 148,12,136
Mvaa 116,12,132
s_mul_i32 s52, s94, 5120
Ta 4,2,40960
Ta 6,2,41984
Mvaa 116,8,60
Mvaa 84,8,56
s_lshl_b32 s95, s59, 12
Mvaa 84,12,128
Mvav 52,12,184
s_sub_u32 s52, s52, s95
Mvaa 52,8,52
W 8
s_add_u32 s52, s52, 56623104
Ta 8,3,32768
Mvaa 56,16,52
Ta 10,3,33792
Mvav 56,20,184
s_lshl_b32 s53, s94, 3
Mvaa 88,20,128
Ta 12,3,40960
Ta 14,3,41984
Mvaa 88,16,56
s_add_u32 s53, s53, 65536
Mvaa 120,16,60
s_waitcnt vmcnt(4)
v_cmp_eq_u64 vcc, s[12:13], v[28:29]
s_cmp_eq_u64 vcc, -1
s_cbranch_scc0 .Lspin_57%=
.Lok_58%=:
s_sub_u32 s82, s91, 16384
Mvaa 120,20,132
s_xor_b32 s83, s92, 0x10000
s_and_b32 s5, s14, 1
s_lshl_b32 s5, s5, 13
Mvaa 152,20,136
s_add_u32 s5, s82, s5
s_add_u32 m0, s83, 0
s_add_u32 s81, s5, 0
Mvav 152,16,216
W 8
v_bfe_u32 v244, v1, 4, 1
v_mul_u32_u24 v244, 0xaaaa, v244
Mvav 156,24,216
Dc 1,8,81,0
v_add_u32 v244, 0x4444, v244
Mvaa 156,28,136
Ta 16,4,32768
Ta 18,4,33792
Mvaa 124,28,132
v_lshl_or_b32 v240, v244, 16, v244
v_mov_b32 v241, 0xff800000
Mvaa 124,24,60
Ta 20,4,40960
Ta 22,4,41984
Mvaa 92,24,56
v_add_u32 v242, 0x10000, v1
Mvaa 92,28,128
Dc 1,8,81,1024
v_bfe_u32 v244, v1, 4, 4
Mvav 60,28,184
v_lshrrev_b32 v245, 8, v1
Mvaa 60,24,52
W 8
Ta 24,5,32768
v_lshlrev_b32 v245, 2, v245
Mvaa 32,0,140
Ta 26,5,33792
Mvav 32,4,224
v_sub_u32 v244, v244, v245
Mvav 64,4,232
Dc 1,8,81,2048
v_cmp_le_i32_e64 s[72:73], 0, v244
Mvaa 64,0,144
Ta 28,5,40960
Ta 30,5,41984
Mvaa 96,0,148
v_cmp_le_i32_e64 s[74:75], 1, v244
Mvaa 96,4,248
v_cmp_le_i32_e64 s[76:77], 2, v244
Mvaa 128,4,252
v_cmp_le_i32_e64 s[78:79], 3, v244
Mvaa 128,0,152
W 8
Dc 1,8,81,3072
Ta 0,6,32768
Mvaa 132,8,152
s_add_u32 m0, s83, 4096
s_add_u32 s81, s5, 4096
Ta 2,6,33792
Mvaa 132,12,252
v_add_u32 v243, 4096, v1
v_add_u32 v244, 8192, v1
Mvaa 100,12,248
Ta 4,6,40960
v_add_u32 v245, 12288, v1
Mvaa 100,8,148
Ta 6,6,41984
Mvaa 68,8,144
s_mov_b32 s18, 0x3f803f80
Dc 1,8,81,0
Mvav 68,12,232
s_mov_b32 s19, 0x3f003f00
Mvav 36,12,224
Mvaa 36,8,140
W 8
Ta 8,7,32768
v_accvgpr_write_b32 a176, s18
Mvaa 40,16,140
Ta 10,7,33792
v_accvgpr_write_b32 a180, s19
Mvav 40,20,224
Dc 1,8,81,1024
v_mov_b32 v252, v241
Mvav 72,20,232
Ta 12,7,40960
Ta 14,7,41984
Mvaa 72,16,144
v_accvgpr_write_b32 a177, s18
Mvaa 104,16,148
v_accvgpr_write_b32 a181, s19
Mvaa 104,20,248
v_mov_b32 v253, v241
Mvaa 136,20,252
Dc 1,8,81,2048
Mvaa 136,16,152
W 8
Ta 16,8,32768
v_accvgpr_write_b32 a178, s18
Mvaa 140,24,152
Ta 18,8,33792
v_accvgpr_write_b32 a182, s19
Mvaa 140,28,252
Mvaa 108,28,248
Ta 20,8,40960
v_mov_b32 v254, v241
Mvaa 108,24,148
Dc 1,8,81,3072
Ta 22,8,41984
Mvaa 76,24,144
s_add_u32 m0, s83, 8192
s_add_u32 s81, s82, 8192
v_accvgpr_write_b32 a179, s18
Mvav 76,28,232
v_accvgpr_write_b32 a183, s19
Mvav 44,28,224
v_mov_b32 v255, v241
Mvaa 44,24,140
W 8
Ta 24,9,32768
v_accvgpr_write_b32 a160, 0
Mvaa 48,0,140
Dc 1,8,81,0
Ta 26,9,33792
Mvav 48,4,224
v_accvgpr_write_b32 a161, 0
Mvav 80,4,232
Ta 28,9,40960
v_accvgpr_write_b32 a162, 0
Mvaa 80,0,144
Ta 30,9,41984
v_accvgpr_write_b32 a163, 0
Mvaa 112,0,148
Mvaa 112,4,248
Dc 1,8,81,1024
v_accvgpr_write_b32 a164, 0
Mvaa 144,4,252
v_accvgpr_write_b32 a165, 0
Mvaa 144,0,152
W 8
Ta 0,2,49152
v_accvgpr_write_b32 a166, 0
Mvaa 148,8,152
Ta 2,2,50176
Mvaa 148,12,252
v_accvgpr_write_b32 a167, 0
Mvaa 116,12,248
Dc 1,8,81,2048
v_accvgpr_write_b32 a168, 0
Mvaa 116,8,148
Ta 4,2,57344
Ta 6,2,58368
Mvaa 84,8,144
v_accvgpr_write_b32 a169, 0
Mvav 84,12,232
v_accvgpr_write_b32 a170, 0
Mvav 52,12,224
v_accvgpr_write_b32 a171, 0
Mvaa 52,8,140
W 8
Dc 1,8,81,3072
Ta 8,3,49152
Mvaa 56,16,140
s_add_u32 m0, s83, 12288
s_add_u32 s81, s82, 12288
Ta 10,3,50176
Mvav 56,20,224
v_accvgpr_write_b32 a172, 0
v_accvgpr_write_b32 a173, 0
Mvav 88,20,232
Ta 12,3,57344
v_accvgpr_write_b32 a174, 0
Mvaa 88,16,144
Ta 14,3,58368
Mvaa 120,16,148
Dc 1,8,81,0
v_accvgpr_write_b32 a175, 0
Mvaa 120,20,248
s_mov_b32 s90, 0x5d800000
Mvaa 152,20,252
s_mov_b64 s[88:89], 0
Mvaa 152,16,152
W 8
Ta 16,4,49152
Ta 18,4,50176
Mvaa 156,24,152
s_mov_b32 s64, 0
Mvaa 156,28,252
s_and_b32 s18, s14, 1
Dc 1,8,81,1024
Mvaa 124,28,248
s_lshl_b32 s62, s18, 14
Ta 20,4,57344
Ta 22,4,58368
Mvaa 124,24,148
Mvaa 92,24,144
s_lshl_b32 s20, s57, 15
Mvav 92,28,232
s_sub_u32 s20, 507904, s20
Mvav 60,28,224
Dc 1,8,81,2048
Mvaa 60,24,140
s_waitcnt vmcnt(15) lgkmcnt(8)
s_add_u32 s20, s20, s62
)"
R"(Ta 24,5,49152
Mvav 32,0,192
s_lshl_b32 s65, s57, 1
Ta 26,5,50176
Mvav 32,4,204
s_sub_u32 s65, s65, 1
Mvav 64,4,212
Ta 28,5,57344
Ta 30,5,58368
Mvav 64,0,200
s_max_i32 s65, s65, 0
Dc 1,8,81,3072
v_bfe_u32 v28, v1, 4, 4
Mvav 96,0,188
s_sub_u32 s66, s60, 1
v_lshlrev_b32 v28, 3, v28
Mvav 96,4,220
s_lshl_b32 s80, s56, 9
s_add_u32 s80, s80, 4096
s_cmp_lt_u32 s14, 2
s_cselect_b32 s20, s20, 0
s_cselect_b32 s66, s66, 15
s_cselect_b32 s21, 32768, 0
Mvav 128,4,176
Mvav 128,0,196
W 8
s_add_u32 s61, s91, s20
Ta 0,6,49152
Mvav 132,8,196
s_add_u32 s62, s62, s21
buffer_load_dwordx2 v[28:29], v28, s[8:11], s80 offen sc1
Ta 2,6,50176
Mvav 132,12,176
Mvav 100,12,220
s_sub_u32 s63, 16384, s21
Ta 4,6,57344
Ta 6,6,58368
Mvav 100,8,188
s_lshl_b32 s67, s60, 7
Mvav 68,8,200
s_add_u32 s67, s67, s56
Mvav 68,12,212
Mvav 36,12,204
s_lshl_b32 s67, s67, 12
Mvav 36,8,192
W 8
s_add_u32 s67, s67, 39845888
Ta 8,7,49152
Mvav 40,16,192
s_lshl_b32 s69, s60, 3
Ta 10,7,50176
Mvav 40,20,204
Mvav 72,20,212
s_add_u32 s69, s69, s59
Ta 12,7,57344
Ta 14,7,58368
Mvav 72,16,200
s_lshl_b32 s69, s69, 6
Mvav 104,16,188
Mvav 104,20,220
s_lshl_b32 s21, s58, 3
Mvav 136,20,176
s_add_u32 s69, s69, s21
Mvav 136,16,196
W 8
s_add_u32 s69, s69, 20480
Ta 16,8,49152
Mvav 140,24,196
Ta 18,8,50176
Mvav 140,28,176
s_mov_b32 s70, 0
Mvav 108,28,220
s_mov_b32 s37, 0
Ta 20,8,57344
Ta 22,8,58368
Mvav 108,24,188
s_sub_u32 s18, s57, 4
Mvav 76,24,200
Mvav 76,28,212
s_lshl_b32 s18, s18, 2
Mvav 44,28,204
s_add_u32 s18, s18, s58
Mvav 44,24,192
W 8
Ta 24,9,49152
Ta 26,9,50176
Mvav 48,0,192
s_mul_i32 s85, s59, 7
Mvav 48,4,204
s_add_u32 s85, s85, s18
Mvav 80,4,212
s_cmp_lt_u32 s18, 7
s_cselect_b32 s85, s85, 99
Ta 28,9,57344
Ta 30,9,58368
Mvav 80,0,200
Mvav 112,0,188
s_cmp_lt_u32 s85, 54
s_cselect_b32 s65, 2, s65
Mvav 112,4,220
s_mov_b32 s68, 0
Mvav 144,4,176
s_mov_b64 s[24:25], -1
Mvav 144,0,196
W 8
Mvav 148,8,196
s_mov_b32 s22, 0x3e0293ee
Mvav 148,12,176
s_mov_b32 s23, 0x3e0293ee
Mvav 116,12,220
Mvav 116,8,188
Mvav 84,8,200
Mvav 84,12,212
Mvav 52,12,204
Mvav 52,8,192
W 4
Mvav 56,16,192
Mvav 56,20,204
s_waitcnt vmcnt(0)
v_cmp_eq_u64 vcc, s[12:13], v[28:29]
s_cmp_eq_u64 vcc, -1
s_cbranch_scc0 .Lspin_60%=
.Lok_61%=:
Mvav 88,20,212
Mvav 88,16,200
Mvav 120,16,188
W 0
s_barrier
Mvav 120,20,220
Ra 128,242,0
Mvav 152,20,176
Ra 132,242,1024
Mvav 152,16,196
Mvav 156,24,196
Ra 136,242,2048
Mvav 156,28,176
Mvav 124,28,220
Ra 140,242,3072
Mvav 124,24,188
Mvav 92,24,200
Mvav 92,28,212
Mvav 60,28,204
Mvav 60,24,192
.Lbgo%=:
v_pk_mul_f32 v[32:33], v[32:33], s[22:23]
v_pk_mul_f32 v[34:35], v[34:35], s[22:23]
v_pk_mul_f32 v[36:37], v[36:37], s[22:23]
v_pk_mul_f32 v[38:39], v[38:39], s[22:23]
v_pk_mul_f32 v[64:65], v[64:65], s[22:23]
v_pk_mul_f32 v[66:67], v[66:67], s[22:23]
v_pk_mul_f32 v[68:69], v[68:69], s[22:23]
v_pk_mul_f32 v[70:71], v[70:71], s[22:23]
v_pk_mul_f32 v[96:97], v[96:97], s[22:23]
v_pk_mul_f32 v[98:99], v[98:99], s[22:23]
v_pk_mul_f32 v[100:101], v[100:101], s[22:23]
v_pk_mul_f32 v[102:103], v[102:103], s[22:23]
v_pk_mul_f32 v[128:129], v[128:129], s[22:23]
v_pk_mul_f32 v[130:131], v[130:131], s[22:23]
v_pk_mul_f32 v[132:133], v[132:133], s[22:23]
v_pk_mul_f32 v[134:135], v[134:135], s[22:23]
v_pk_mul_f32 v[40:41], v[40:41], s[22:23]
v_pk_mul_f32 v[42:43], v[42:43], s[22:23]
v_pk_mul_f32 v[44:45], v[44:45], s[22:23]
v_pk_mul_f32 v[46:47], v[46:47], s[22:23]
v_pk_mul_f32 v[72:73], v[72:73], s[22:23]
v_pk_mul_f32 v[74:75], v[74:75], s[22:23]
v_pk_mul_f32 v[76:77], v[76:77], s[22:23]
v_pk_mul_f32 v[78:79], v[78:79], s[22:23]
v_pk_mul_f32 v[104:105], v[104:105], s[22:23]
v_pk_mul_f32 v[106:107], v[106:107], s[22:23]
v_pk_mul_f32 v[108:109], v[108:109], s[22:23]
v_pk_mul_f32 v[110:111], v[110:111], s[22:23]
v_pk_mul_f32 v[136:137], v[136:137], s[22:23]
v_pk_mul_f32 v[138:139], v[138:139], s[22:23]
v_pk_mul_f32 v[140:141], v[140:141], s[22:23]
v_pk_mul_f32 v[142:143], v[142:143], s[22:23]
v_pk_mul_f32 v[48:49], v[48:49], s[22:23]
v_pk_mul_f32 v[50:51], v[50:51], s[22:23]
v_pk_mul_f32 v[52:53], v[52:53], s[22:23]
v_pk_mul_f32 v[54:55], v[54:55], s[22:23]
v_pk_mul_f32 v[80:81], v[80:81], s[22:23]
v_pk_mul_f32 v[82:83], v[82:83], s[22:23]
v_pk_mul_f32 v[84:85], v[84:85], s[22:23]
v_pk_mul_f32 v[86:87], v[86:87], s[22:23]
v_pk_mul_f32 v[112:113], v[112:113], s[22:23]
v_pk_mul_f32 v[114:115], v[114:115], s[22:23]
v_pk_mul_f32 v[116:117], v[116:117], s[22:23]
v_pk_mul_f32 v[118:119], v[118:119], s[22:23]
v_pk_mul_f32 v[144:145], v[144:145], s[22:23]
v_pk_mul_f32 v[146:147], v[146:147], s[22:23]
v_pk_mul_f32 v[148:149], v[148:149], s[22:23]
v_pk_mul_f32 v[150:151], v[150:151], s[22:23]
v_pk_mul_f32 v[56:57], v[56:57], s[22:23]
v_pk_mul_f32 v[58:59], v[58:59], s[22:23]
v_pk_mul_f32 v[60:61], v[60:61], s[22:23]
v_pk_mul_f32 v[62:63], v[62:63], s[22:23]
v_pk_mul_f32 v[88:89], v[88:89], s[22:23]
v_pk_mul_f32 v[90:91], v[90:91], s[22:23]
v_pk_mul_f32 v[92:93], v[92:93], s[22:23]
v_pk_mul_f32 v[94:95], v[94:95], s[22:23]
v_pk_mul_f32 v[120:121], v[120:121], s[22:23]
v_pk_mul_f32 v[122:123], v[122:123], s[22:23]
v_pk_mul_f32 v[124:125], v[124:125], s[22:23]
v_pk_mul_f32 v[126:127], v[126:127], s[22:23]
v_pk_mul_f32 v[152:153], v[152:153], s[22:23]
v_pk_mul_f32 v[154:155], v[154:155], s[22:23]
v_pk_mul_f32 v[156:157], v[156:157], s[22:23]
v_pk_mul_f32 v[158:159], v[158:159], s[22:23]
K 32,32,33
K 33,34,35
K 34,36,37
K 35,38,39
K 40,40,41
K 41,42,43
K 42,44,45
K 43,46,47
W 0
Zvav 196,128,32
s_add_u32 m0, s62, 0
Ra 144,242,4096
Ra 148,242,5120
Ra 152,242,6144
Ra 156,242,7168
Mvav 196,132,40
K 64,64,65
K 65,66,67
K 66,68,69
K 67,70,71
K 72,72,73
K 73,74,75
K 74,76,77
K 75,78,79
s_nop 1
Zvav 204,132,72
Dc 1,8,61,0
K 96,96,97
K 97,98,99
K 98,100,101
Mvav 204,128,64
K 99,102,103
K 104,104,105
K 105,106,107
K 106,108,109
K 107,110,111
Zvav 212,128,96
Dc 1,8,61,1024
K 128,128,129
K 129,130,131
K 130,132,133
Mvav 212,132,104
K 131,134,135
K 136,136,137
K 137,138,139
K 138,140,141
K 139,142,143
s_nop 1
Zvav 220,132,136
Dc 1,8,61,2048
K 48,48,49
K 49,50,51
K 50,52,53
Mvav 220,128,128
K 51,54,55
K 56,56,57
K 57,58,59
K 58,60,61
K 59,62,63
Mvav 196,136,48
Ra 128,242,8192
Ra 132,242,9216
K 80,80,81
K 81,82,83
Mvav 196,140,56
K 82,84,85
K 83,86,87
K 88,88,89
K 89,90,91
K 90,92,93
K 91,94,95
s_nop 1
Mvav 204,140,88
Dc 1,8,61,3072
s_add_u32 m0, s62, 4096
K 112,112,113
K 113,114,115
K 114,116,117
Mvav 204,136,80
K 115,118,119
K 120,120,121
K 121,122,123
K 122,124,125
K 123,126,127
Mvav 212,136,112
Dc 243,8,61,0
K 144,144,145
K 145,146,147
K 146,148,149
Mvav 212,140,120
K 147,150,151
K 152,152,153
K 153,154,155
K 154,156,157
K 155,158,159
s_nop 1
Mvav 220,140,152
s_add_u32 s80, s62, 131072
Dc 243,8,61,1024
v_add_u32 v12, s80, v1
Mvav 220,136,144
W 2
ds_write_b128 v12, v[32:35] offset:0
Ra 136,242,10240
Zvav 36,144,32
Ra 0,12,0
Ra 140,242,11264
Dc 243,8,61,2048
Mvav 36,148,40
v_max3_f32 v4, v196, v197, v198
v_max3_f32 v4, v4, v199, v252
v_max3_f32 v5, v204, v205, v206
v_max3_f32 v5, v5, v207, v253
Zvav 68,148,72
v_max3_f32 v6, v212, v213, v214
v_max3_f32 v6, v6, v215, v254
v_max3_f32 v7, v220, v221, v222
v_max3_f32 v7, v7, v223, v255
Mvav 68,144,64
v_mov_b32 v8, v4
v_mov_b32 v9, v5
v_mov_b32 v10, v6
v_mov_b32 v11, v7
Zvav 100,144,96
ds_write_b128 v12, v[40:43] offset:1024
v_permlane32_swap_b32 v4, v8
v_permlane32_swap_b32 v5, v9
v_permlane32_swap_b32 v6, v10
Mvav 100,148,104
v_permlane32_swap_b32 v7, v11
v_max_f32 v4, v4, v8
v_max_f32 v5, v5, v9
v_max_f32 v6, v6, v10
Zvav 132,148,136
v_max_f32 v7, v7, v11
v_mov_b32 v8, v4
v_mov_b32 v9, v5
v_mov_b32 v10, v6
Mvav 132,144,128
Ra 4,12,1024
v_mov_b32 v11, v7
v_permlane16_swap_b32 v4, v8
v_permlane16_swap_b32 v5, v9
Mvav 36,152,48
v_permlane16_swap_b32 v6, v10
v_permlane16_swap_b32 v7, v11
v_max_f32 v4, v4, v8
v_max_f32 v5, v5, v9
Mvav 36,156,56
v_max_f32 v6, v6, v10
v_max_f32 v7, v7, v11
v_sub_f32 v4, 0, v4
v_sub_f32 v5, 0, v5
Mvav 68,156,88
v_sub_f32 v6, 0, v6
v_sub_f32 v7, 0, v7
v_mov_b32 v224, v4
v_mov_b32 v225, v4
Mvav 68,152,80
v_mov_b32 v226, v4
v_mov_b32 v227, v4
v_mov_b32 v228, v5
v_mov_b32 v229, v5
Mvav 100,152,112
v_mov_b32 v230, v5
v_mov_b32 v231, v5
v_mov_b32 v232, v6
v_mov_b32 v233, v6
Mvav 100,156,120
v_mov_b32 v234, v6
v_mov_b32 v235, v6
v_mov_b32 v236, v7
v_mov_b32 v237, v7
Mvav 132,156,152
Dc 243,8,61,3072
s_add_u32 m0, s62, 8192
ds_write_b128 v12, v[48:51] offset:2048
v_mov_b32 v238, v7
v_mov_b32 v239, v7
Mvav 132,152,144
W 3
Ra 8,12,2048
ds_write_b128 v12, v[56:59] offset:3072
Ra 144,242,12288
Ra 12,12,3072
Cvav 160,128,32,224
s_and_b32 s80, s14, 1
Dc 244,8,61,0
Ra 148,242,13312
ds_write_b128 v12, v[64:67] offset:4096
Ra 16,12,4096
Mvav 160,132,40
)"
R"(s_lshl_b32 s80, s80, 14
ds_write_b128 v12, v[72:75] offset:5120
Ra 20,12,5120
ds_write_b128 v12, v[80:83] offset:6144
A 196,196,224
Cvav 164,132,72,228
s_add_u32 s80, s80, 98304
Dc 244,8,61,1024
Ra 24,12,6144
A 197,197,224
A 198,198,224
Mvav 164,128,64
Ra 152,242,14336
A 199,199,224
X 196
Cvav 168,128,96,232
Dc 244,8,61,2048
Ra 156,242,15360
X 197
Mvav 168,132,104
ds_write_b128 v12, v[88:91] offset:7168
Ra 28,12,7168
X 198
Cvav 172,132,136,236
Dc 244,8,61,3072
s_add_u32 m0, s62, 12288
K 192,196,197
X 199
Mvav 172,128,128
ds_write_b128 v12, v[96:99] offset:8192
A 204,204,228
v_add_u32 v8, s80, v1
K 193,198,199
Mvav 160,136,48
Dc 245,8,61,0
Ra 32,12,8192
A 205,205,228
A 206,206,228
Mvav 160,140,56
ds_write_b128 v8, v[4:7]
A 207,207,228
X 204
Mvav 164,140,88
Dc 245,8,61,1024
ds_write_b128 v12, v[104:107] offset:9216
X 205
Mvav 164,136,80
Ra 36,12,9216
X 206
K 200,204,205
Mvav 168,136,112
Dc 245,8,61,2048
Ra 128,242,24576
X 207
Mvav 168,140,120
ds_write_b128 v12, v[112:115] offset:10240
Ra 132,242,25600
A 212,212,232
K 201,206,207
Mvav 172,140,152
Dc 245,8,61,3072
s_add_u32 s61, s61, s63
Ra 40,12,10240
A 213,213,232
A 214,214,232
Mvav 172,136,144
W 11
ds_write_b128 v12, v[120:123] offset:11264
A 215,215,232
X 212
Cvav 176,144,32,224
X 213
X 214
Mvav 176,148,40
Ra 44,12,11264
K 208,212,213
X 215
Cvav 180,148,72,228
ds_write_b128 v12, v[128:131] offset:12288
A 220,220,236
K 209,214,215
A 221,221,236
Mvav 180,144,64
Ra 48,12,12288
Ra 136,242,26624
A 222,222,236
A 223,223,236
Cvav 184,144,96,232
ds_write_b128 v12, v[136:139] offset:13312
Ra 140,242,27648
X 220
Mvav 184,148,104
X 221
X 222
Cvav 188,148,136,236
Ra 52,12,13312
K 216,220,221
X 223
Mvav 188,144,128
ds_write_b128 v12, v[144:147] offset:14336
Ra 56,12,14336
A 36,36,224
K 217,222,223
Mvav 176,152,48
ds_write_b128 v12, v[152:155] offset:15360
A 37,37,224
A 38,38,224
A 39,39,224
Mvav 176,156,56
X 36
X 37
Mvav 180,156,88
Ra 60,12,15360
X 38
K 194,36,37
Mvav 180,152,80
Ra 144,242,28672
X 39
A 68,68,228
Mvav 184,152,112
Ra 148,242,29696
K 195,38,39
A 69,69,228
A 70,70,228
Mvav 184,156,120
W 3
s_barrier
A 71,71,228
X 68
Mvav 188,156,152
X 69
X 70
K 202,68,69
X 71
A 100,100,232
K 203,70,71
A 101,101,232
A 102,102,232
A 103,103,232
X 100
X 101
X 102
K 210,100,101
X 103
A 132,132,236
K 211,102,103
A 133,133,236
A 134,134,236
A 135,135,236
X 132
X 133
X 134
K 218,132,133
X 135
s_nop 0
K 219,134,135
Mvav 188,152,144
Ra 152,242,30720
Zvav 32,128,192
Ra 156,242,31744
Zvav 64,128,200
v_xor_b32 v242, 0x10000, v242
Zvav 96,128,208
s_xor_b32 s62, s62, 0x10000
Zvav 128,128,216
v_add_u32 v23, 61440, v21
Zvav 132,132,216
v_add_u32 v24, 61440, v22
Zvav 100,132,208
v_add_u32 v25, 122880, v21
Zvav 68,132,200
v_add_u32 v26, 122880, v22
Zvav 36,132,192
Zvav 40,136,192
X 172
Zvav 72,136,200
X 173
Zvav 104,136,208
X 174
K 220,172,173
Zvav 136,136,216
X 175
Zvav 140,140,216
X 168
K 221,174,175
Zvav 108,140,208
X 169
X 170
Zvav 76,140,200
s_waitcnt vmcnt(0) lgkmcnt(0)
s_barrier
K 212,168,169
X 171
Zvav 44,140,192
Ra 128,242,0
X 164
Zvav 48,144,192
K 213,170,171
X 165
Zvav 80,144,200
X 166
Zvav 112,144,208
Ra 132,242,1024
K 204,164,165
X 167
Zvav 144,144,216
X 160
Zvav 148,148,216
K 205,166,167
X 161
Zvav 116,148,208
Ra 136,242,2048
X 162
Zvav 84,148,200
K 196,160,161
X 163
Zvav 52,148,192
X 176
K 197,162,163
Zvav 56,152,192
Ra 140,242,3072
X 177
Zvav 88,152,200
X 178
K 198,176,177
Zvav 120,152,208
X 179
X 180
Zvav 152,152,216
K 199,178,179
Zvav 156,156,216
X 181
X 182
Zvav 124,156,208
K 206,180,181
X 183
Zvav 92,156,200
K 207,182,183
Zvav 60,156,192
s_cmp_eq_u64 s[24:25], 0
s_cbranch_scc1 .Ll1_30%=
W 0
Cvaa 160,128,0,224
Ra 144,242,4096
Ra 148,242,5120
Mvaa 160,132,4
s_add_u32 m0, s62, 0
Ra 152,242,6144
Cvaa 164,132,20,228
X 184
Mvaa 164,128,16
X 185
Cvaa 168,128,32,232
X 186
Mvaa 168,132,36
Ra 156,242,7168
K 214,184,185
Cvaa 172,132,52,236
Dc 1,8,61,0
Mvaa 172,128,48
X 187
Mvaa 160,136,8
X 188
Mvaa 160,140,12
Ra 128,242,16384
K 215,186,187
Mvaa 164,140,28
Dc 1,8,61,1024
Mvaa 164,136,24
X 189
Mvaa 168,136,40
X 190
Mvaa 168,140,44
Ra 132,242,17408
K 222,188,189
Mvaa 172,140,60
Dc 1,8,61,2048
Mvaa 172,136,56
X 191
v_smfmac_f32_16x16x64_bf16 a[160:163], a[176:179], v[192:199], v240
Ra 136,242,18432
K 223,190,191
v_smfmac_f32_16x16x64_bf16 a[164:167], a[176:179], v[200:207], v240
X 160
v_smfmac_f32_16x16x64_bf16 a[168:171], a[176:179], v[208:215], v240
Dc 1,8,61,3072
v_smfmac_f32_16x16x64_bf16 a[172:175], a[176:179], v[216:223], v240
W 3
X 161
Cvaa 176,144,0,224
s_add_u32 m0, s62, 4096
X 162
Mvaa 176,148,4
Ra 140,242,19456
K 192,160,161
Cvaa 180,148,20,228
X 163
Mvaa 180,144,16
Dc 243,8,61,0
Cvaa 184,144,32,232
X 164
Mvaa 184,148,36
K 193,162,163
Cvaa 188,148,52,236
X 165
Mvaa 188,144,48
Dc 243,8,61,1024
Ra 144,242,20480
Mvaa 176,152,8
X 166
Mvaa 176,156,12
Ra 148,242,21504
K 200,164,165
Mvaa 180,156,28
X 167
Mvaa 180,152,24
Dc 243,8,61,2048
Mvaa 184,152,40
X 168
Mvaa 184,156,44
K 201,166,167
Mvaa 188,156,60
X 169
Mvaa 188,152,56
W 2
Dc 243,8,61,3072
Ra 152,242,22528
Mvav 32,128,196
s_add_u32 m0, s62, 8192
X 170
Mvav 64,128,204
Ra 156,242,23552
K 208,168,169
Mvav 96,128,212
X 171
Mvav 128,128,220
Dc 244,8,61,0
Mvav 132,132,220
X 172
Mvav 100,132,212
Ra 128,242,8192
K 209,170,171
Mvav 68,132,204
X 173
Mvav 36,132,196
Dc 244,8,61,1024
Mvav 40,136,196
X 174
Mvav 72,136,204
Ra 132,242,9216
K 216,172,173
Mvav 104,136,212
X 175
Mvav 136,136,220
Dc 244,8,61,2048
Mvav 140,140,220
X 176
Mvav 108,140,212
Ra 136,242,10240
K 217,174,175
Mvav 76,140,204
X 177
Mvav 44,140,196
W 3
Dc 244,8,61,3072
Mvav 48,144,196
X 178
Mvav 80,144,204
s_add_u32 m0, s62, 12288
Ra 140,242,11264
Mvav 112,144,212
K 194,176,177
Mvav 144,144,220
X 179
Mvav 148,148,220
X 180
Mvav 116,148,212
Dc 245,8,61,0
Ra 144,242,12288
Mvav 84,148,204
K 195,178,179
Mvav 52,148,196
X 181
Mvav 56,152,196
X 182
Mvav 88,152,204
s_cmp_eq_u32 s14, 2
s_cselect_b64 s[18:19], s[30:31], s[32:33]
Dc 245,8,61,1024
Ra 148,242,13312
Mvav 120,152,212
s_mul_i32 s81, s58, 6
K 202,180,181
Mvav 152,152,220
s_add_u32 s81, s81, s57
X 183
Mvav 156,156,220
s_sub_u32 s81, s81, 2
X 184
Mvav 124,156,212
)"
R"(s_cmp_ge_u32 s57, 2
s_cselect_b32 s23, 2211836, 0
Dc 245,8,61,2048
Ra 152,242,14336
Mvav 92,156,204
s_mul_i32 s82, s81, 48384
K 203,182,183
Mvav 60,156,196
W 3
X 185
Cvaa 160,128,0,224
s_mul_i32 s83, s59, 448
X 186
Mvaa 160,132,4
s_andn2_b32 s83, s83, 127
Dc 245,8,61,3072
Ra 156,242,15360
Cvaa 164,132,20,228
s_add_u32 s82, s82, s83
s_add_u32 s61, s61, s63
K 210,184,185
Mvaa 164,128,16
v_min_u32 v246, 880, v1
v_lshrrev_b32 v247, 6, v246
Cvaa 168,128,32,232
X 187
Mvaa 168,132,36
X 188
Cvaa 172,132,52,236
s_xor_b32 s62, s62, 0x10000
v_mul_u32_u24 v247, 3456, v247
v_and_b32 v246, 48, v246
Mvaa 172,128,48
K 211,186,187
v_lshl_add_u32 v246, v246, 3, v247
Mvaa 160,136,8
X 189
Mvaa 160,140,12
s_add_u32 s64, s64, 1
Ra 128,242,24576
v_add_u32 v246, s82, v246
Mvaa 164,140,28
X 190
Mvaa 164,136,24
Ra 132,242,25600
v_min_u32 v246, s23, v246
Mvaa 168,136,40
global_load_dword v3, v246, s[18:19]
K 218,188,189
Mvaa 168,140,44
X 191
Mvaa 172,140,60
X 160
Mvaa 172,136,56
W 2
Ra 136,242,26624
K 219,190,191
Cvaa 176,144,0,224
X 161
Mvaa 176,148,4
Ra 140,242,27648
Cvaa 180,148,20,228
X 162
Mvaa 180,144,16
K 196,160,161
Cvaa 184,144,32,232
X 163
Mvaa 184,148,36
X 164
Cvaa 188,148,52,236
K 197,162,163
Mvaa 188,144,48
X 165
Mvaa 176,152,8
Ra 144,242,28672
Ra 148,242,29696
Mvaa 176,156,12
X 166
Mvaa 180,156,28
K 204,164,165
Mvaa 180,152,24
X 167
Mvaa 184,152,40
Mvaa 184,156,44
X 168
Mvaa 188,156,60
Mvaa 188,152,56
W 2
Ra 152,242,30720
K 205,166,167
Mvav 32,128,192
X 169
Mvav 64,128,200
Ra 156,242,31744
v_xor_b32 v242, 0x10000, v242
Mvav 96,128,208
X 170
Mvav 128,128,216
K 212,168,169
Mvav 132,132,216
X 171
Mvav 100,132,208
X 172
Mvav 68,132,200
Mvav 36,132,192
K 213,170,171
Mvav 40,136,192
X 173
Mvav 72,136,200
X 174
Mvav 104,136,208
Mvav 136,136,216
K 220,172,173
Mvav 140,140,216
X 175
Mvav 108,140,208
X 176
Mvav 76,140,200
s_waitcnt vmcnt(1) lgkmcnt(0)
s_barrier
Mvav 44,140,192
Ra 128,242,0
K 221,174,175
Mvav 48,144,192
X 177
Mvav 80,144,200
X 178
Mvav 112,144,208
Ra 132,242,1024
Mvav 144,144,216
K 198,176,177
Mvav 148,148,216
X 179
Mvav 116,148,208
X 180
Mvav 84,148,200
Ra 136,242,2048
Mvav 52,148,192
K 199,178,179
Mvav 56,152,192
X 181
Mvav 88,152,200
Ra 140,242,3072
Mvav 120,152,208
X 182
Mvav 152,152,216
K 206,180,181
Mvav 156,156,216
X 183
Mvav 124,156,208
Mvav 92,156,200
K 207,182,183
Mvav 60,156,192
W 0
Cvaa 160,128,0,224
Ra 144,242,4096
Ra 148,242,5120
Mvaa 160,132,4
s_add_u32 m0, s62, 0
Ra 152,242,6144
Cvaa 164,132,20,228
X 184
Mvaa 164,128,16
X 185
Cvaa 168,128,32,232
X 186
Mvaa 168,132,36
Ra 156,242,7168
K 214,184,185
Cvaa 172,132,52,236
Dc 1,8,61,0
Mvaa 172,128,48
X 187
Mvaa 160,136,8
X 188
Mvaa 160,140,12
Ra 128,242,16384
K 215,186,187
Mvaa 164,140,28
Dc 1,8,61,1024
Mvaa 164,136,24
X 189
Mvaa 168,136,40
X 190
Mvaa 168,140,44
Ra 132,242,17408
K 222,188,189
Mvaa 172,140,60
Dc 1,8,61,2048
Mvaa 172,136,56
X 191
v_smfmac_f32_16x16x64_bf16 a[160:163], a[176:179], v[192:199], v240
Ra 136,242,18432
K 223,190,191
v_smfmac_f32_16x16x64_bf16 a[164:167], a[176:179], v[200:207], v240
X 160
v_smfmac_f32_16x16x64_bf16 a[168:171], a[176:179], v[208:215], v240
Dc 1,8,61,3072
v_smfmac_f32_16x16x64_bf16 a[172:175], a[176:179], v[216:223], v240
W 3
X 161
Cvaa 176,144,0,224
s_add_u32 m0, s62, 4096
X 162
Mvaa 176,148,4
Ra 140,242,19456
K 192,160,161
Cvaa 180,148,20,228
X 163
Mvaa 180,144,16
Dc 243,8,61,0
Cvaa 184,144,32,232
X 164
Mvaa 184,148,36
K 193,162,163
Cvaa 188,148,52,236
X 165
Mvaa 188,144,48
Dc 243,8,61,1024
Ra 144,242,20480
Mvaa 176,152,8
X 166
Mvaa 176,156,12
Ra 148,242,21504
K 200,164,165
Mvaa 180,156,28
X 167
Mvaa 180,152,24
Dc 243,8,61,2048
Mvaa 184,152,40
X 168
Mvaa 184,156,44
K 201,166,167
Mvaa 188,156,60
X 169
Mvaa 188,152,56
W 2
Dc 243,8,61,3072
Ra 152,242,22528
Mvav 32,128,196
s_add_u32 m0, s62, 8192
X 170
Mvav 64,128,204
Ra 156,242,23552
K 208,168,169
Mvav 96,128,212
X 171
Mvav 128,128,220
Dc 244,8,61,0
Mvav 132,132,220
X 172
Mvav 100,132,212
Ra 128,242,8192
K 209,170,171
Mvav 68,132,204
X 173
Mvav 36,132,196
Dc 244,8,61,1024
Mvav 40,136,196
X 174
Mvav 72,136,204
Ra 132,242,9216
K 216,172,173
Mvav 104,136,212
X 175
Mvav 136,136,220
Dc 244,8,61,2048
Mvav 140,140,220
X 176
Mvav 108,140,212
Ra 136,242,10240
K 217,174,175
Mvav 76,140,204
X 177
Mvav 44,140,196
W 3
Dc 244,8,61,3072
Mvav 48,144,196
X 178
Mvav 80,144,204
s_add_u32 m0, s62, 12288
Ra 140,242,11264
Mvav 112,144,212
K 194,176,177
Mvav 144,144,220
X 179
Mvav 148,148,220
X 180
Mvav 116,148,212
Dc 245,8,61,0
Ra 144,242,12288
Mvav 84,148,204
K 195,178,179
Mvav 52,148,196
X 181
Mvav 56,152,196
X 182
Mvav 88,152,204
Dc 245,8,61,1024
Ra 148,242,13312
Mvav 120,152,212
v_add_u32 v246, 1161216, v246
v_min_u32 v246, s23, v246
Mvav 152,152,220
K 202,180,181
Mvav 156,156,220
X 183
Mvav 124,156,212
Dc 245,8,61,2048
Ra 152,242,14336
Mvav 92,156,204
X 184
Mvav 60,156,196
W 3
K 203,182,183
v_bfe_u32 v28, v1, 4, 4
Cvaa 160,128,0,224
X 185
Mvaa 160,132,4
Dc 245,8,61,3072
v_lshlrev_b32 v28, 3, v28
Cvaa 164,132,20,228
s_lshl_b32 s80, s56, 9
X 186
Mvaa 164,128,16
s_add_u32 s80, s80, 4224
Ra 156,242,15360
K 210,184,185
Cvaa 168,128,32,232
s_add_u32 s61, s61, s63
X 187
Mvaa 168,132,36
buffer_load_dwordx2 v[28:29], v28, s[8:11], s80 offen sc1
Cvaa 172,132,52,236
s_xor_b32 s62, s62, 0x10000
X 188
Mvaa 172,128,48
Ra 128,242,24576
K 211,186,187
Mvaa 160,136,8
X 189
Mvaa 160,140,12
s_add_u32 s64, s64, 1
global_load_dword v3, v246, s[18:19]
Ra 132,242,25600
Mvaa 164,140,28
s_mov_b64 s[18:19], s[34:35]
s_mul_i32 s81, s58, 6
s_add_u32 s81, s81, s57
Mvaa 164,136,24
s_sub_u32 s81, s81, 2
s_cmp_ge_u32 s57, 2
s_cselect_b32 s23, 2211836, 0
s_lshl_b32 s81, s81, 1
Mvaa 168,136,40
s_add_u32 s81, s81, s14
s_sub_u32 s81, s81, 2
s_mul_i32 s81, s81, 6016
Mvaa 168,140,44
s_mul_i32 s82, s59, 286720
s_add_u32 s81, s81, s82
v_min_u32 v246, 736, v1
Mvaa 172,140,60
v_lshl_add_u32 v246, v246, 3, s81
v_min_u32 v246, s23, v246
Mvaa 172,136,56
W 2
global_load_dword v3, v246, s[18:19]
Ra 136,242,26624
Cvaa 176,144,0,224
X 190
Mvaa 176,148,4
Ra 140,242,27648
K 218,188,189
Cvaa 180,148,20,228
X 191
Mvaa 180,144,16
X 160
Cvaa 184,144,32,232
K 219,190,191
Mvaa 184,148,36
X 161
Cvaa 188,148,52,236
X 162
Mvaa 188,144,48
Ra 144,242,28672
K 196,160,161
Mvaa 176,152,8
X 163
Mvaa 176,156,12
X 164
Mvaa 180,156,28
Ra 148,242,29696
K 197,162,163
Mvaa 180,152,24
X 165
Mvaa 184,152,40
X 166
Mvaa 184,156,44
K 204,164,165
)"
R"(Mvaa 188,156,60
X 167
Mvaa 188,152,56
W 2
X 168
Mvav 32,128,192
Ra 152,242,30720
K 205,166,167
Mvav 64,128,200
X 169
Mvav 96,128,208
Ra 156,242,31744
v_xor_b32 v242, 0x10000, v242
Mvav 128,128,216
X 170
Mvav 132,132,216
K 212,168,169
Mvav 100,132,208
X 171
Mvav 68,132,200
X 172
Mvav 36,132,192
K 213,170,171
Mvav 40,136,192
X 173
Mvav 72,136,200
X 174
Mvav 104,136,208
Mvav 136,136,216
s_waitcnt vmcnt(2)
v_cmp_eq_u64 vcc, s[12:13], v[28:29]
s_cmp_eq_u64 vcc, -1
s_cbranch_scc0 .Lspin_63%=
.Lok_64%=:
K 220,172,173
Mvav 140,140,216
X 175
Mvav 108,140,208
X 176
Mvav 76,140,200
W 0
s_barrier
Mvav 44,140,192
Ra 128,242,0
K 221,174,175
Mvav 48,144,192
X 177
Mvav 80,144,200
X 178
Mvav 112,144,208
Ra 132,242,1024
Mvav 144,144,216
K 198,176,177
Mvav 148,148,216
X 179
Mvav 116,148,208
X 180
Mvav 84,148,200
Ra 136,242,2048
Mvav 52,148,192
K 199,178,179
Mvav 56,152,192
X 181
Mvav 88,152,200
Ra 140,242,3072
Mvav 120,152,208
X 182
Mvav 152,152,216
K 206,180,181
Mvav 156,156,216
X 183
Mvav 124,156,208
Mvav 92,156,200
K 207,182,183
Mvav 60,156,192
W 0
Cvaa 160,128,0,224
Ra 144,242,4096
Ra 148,242,5120
Mvaa 160,132,4
s_add_u32 m0, s62, 0
Ra 152,242,6144
Cvaa 164,132,20,228
X 184
Mvaa 164,128,16
X 185
Cvaa 168,128,32,232
X 186
Mvaa 168,132,36
Ra 156,242,7168
K 214,184,185
Cvaa 172,132,52,236
Dc 1,8,61,0
Mvaa 172,128,48
X 187
Mvaa 160,136,8
X 188
Mvaa 160,140,12
Ra 128,242,16384
K 215,186,187
Mvaa 164,140,28
Dc 1,8,61,1024
Mvaa 164,136,24
X 189
Mvaa 168,136,40
X 190
Mvaa 168,140,44
Ra 132,242,17408
K 222,188,189
Mvaa 172,140,60
Dc 1,8,61,2048
Mvaa 172,136,56
X 191
v_smfmac_f32_16x16x64_bf16 a[160:163], a[176:179], v[192:199], v240
Ra 136,242,18432
K 223,190,191
v_smfmac_f32_16x16x64_bf16 a[164:167], a[176:179], v[200:207], v240
X 160
v_smfmac_f32_16x16x64_bf16 a[168:171], a[176:179], v[208:215], v240
Dc 1,8,61,3072
v_smfmac_f32_16x16x64_bf16 a[172:175], a[176:179], v[216:223], v240
W 3
X 161
Cvaa 176,144,0,224
s_add_u32 m0, s62, 4096
X 162
Mvaa 176,148,4
Ra 140,242,19456
K 192,160,161
Cvaa 180,148,20,228
X 163
Mvaa 180,144,16
Dc 243,8,61,0
Cvaa 184,144,32,232
X 164
Mvaa 184,148,36
K 193,162,163
Cvaa 188,148,52,236
X 165
Mvaa 188,144,48
Dc 243,8,61,1024
Ra 144,242,20480
Mvaa 176,152,8
X 166
Mvaa 176,156,12
Ra 148,242,21504
K 200,164,165
Mvaa 180,156,28
X 167
Mvaa 180,152,24
Dc 243,8,61,2048
Mvaa 184,152,40
X 168
Mvaa 184,156,44
K 201,166,167
Mvaa 188,156,60
X 169
Mvaa 188,152,56
W 2
Dc 243,8,61,3072
Ra 152,242,22528
Mvav 32,128,196
s_add_u32 m0, s62, 8192
X 170
Mvav 64,128,204
Ra 156,242,23552
K 208,168,169
Mvav 96,128,212
X 171
Mvav 128,128,220
Dc 244,8,61,0
Mvav 132,132,220
X 172
Mvav 100,132,212
Ra 128,242,8192
K 209,170,171
Mvav 68,132,204
X 173
Mvav 36,132,196
Dc 244,8,61,1024
Mvav 40,136,196
X 174
Mvav 72,136,204
Ra 132,242,9216
K 216,172,173
Mvav 104,136,212
X 175
Mvav 136,136,220
Dc 244,8,61,2048
Mvav 140,140,220
X 176
Mvav 108,140,212
Ra 136,242,10240
K 217,174,175
Mvav 76,140,204
X 177
Mvav 44,140,196
W 3
Dc 244,8,61,3072
Mvav 48,144,196
X 178
Mvav 80,144,204
s_add_u32 m0, s62, 12288
Ra 140,242,11264
Mvav 112,144,212
K 194,176,177
Mvav 144,144,220
X 179
Mvav 148,148,220
X 180
Mvav 116,148,212
Dc 245,8,61,0
Ra 144,242,12288
Mvav 84,148,204
K 195,178,179
Mvav 52,148,196
X 181
Mvav 56,152,196
X 182
Mvav 88,152,204
Dc 245,8,61,1024
Ra 148,242,13312
Mvav 120,152,212
K 202,180,181
Mvav 152,152,220
X 183
Mvav 156,156,220
X 184
Mvav 124,156,212
Dc 245,8,61,2048
Ra 152,242,14336
Mvav 92,156,204
K 203,182,183
Mvav 60,156,196
W 3
X 185
Cvaa 160,128,0,224
X 186
Mvaa 160,132,4
Dc 245,8,61,3072
Ra 156,242,15360
Cvaa 164,132,20,228
s_add_u32 s61, s61, s63
K 210,184,185
Mvaa 164,128,16
X 187
Cvaa 168,128,32,232
X 188
Mvaa 168,132,36
Cvaa 172,132,52,236
s_xor_b32 s62, s62, 0x10000
K 211,186,187
Mvaa 172,128,48
X 189
Mvaa 160,136,8
Ra 128,242,24576
Ra 132,242,25600
Mvaa 160,140,12
s_add_u32 s64, s64, 1
X 190
Mvaa 164,140,28
K 218,188,189
Mvaa 164,136,24
X 191
Mvaa 168,136,40
Mvaa 168,140,44
X 160
Mvaa 172,140,60
s_add_u32 s36, s29, 131072
K 219,190,191
Mvaa 172,136,56
W 2
X 161
Cvaa 176,144,0,224
Ra 136,242,26624
Ra 140,242,27648
Mvaa 176,148,4
X 162
Cvaa 180,148,20,228
K 196,160,161
Mvaa 180,144,16
buffer_store_dwordx4 a[124:127], v1, s[8:11], s36 offen offset:0 sc1
Cvaa 184,144,32,232
X 163
Mvaa 184,148,36
X 164
Cvaa 188,148,52,236
K 197,162,163
Mvaa 188,144,48
X 165
Mvaa 176,152,8
Ra 144,242,28672
buffer_store_dwordx4 a[116:119], v1, s[8:11], s36 offen offset:1024 sc1
Mvaa 176,156,12
Ra 148,242,29696
Mvaa 180,156,28
X 166
Mvaa 180,152,24
K 204,164,165
Mvaa 184,152,40
X 167
Mvaa 184,156,44
buffer_store_dwordx4 a[108:111], v1, s[8:11], s36 offen offset:2048 sc1
Mvaa 188,156,60
X 168
Mvaa 188,152,56
W 2
Ra 152,242,30720
K 205,166,167
Mvav 32,128,192
X 169
Mvav 64,128,200
Ra 156,242,31744
buffer_store_dwordx4 a[204:207], v1, s[8:11], s36 offen offset:3072 sc1
Mvav 96,128,208
X 170
Mvav 128,128,216
v_xor_b32 v242, 0x10000, v242
K 212,168,169
Mvav 132,132,216
X 171
Mvav 100,132,208
s_add_u32 s36, s29, 135168
X 172
Mvav 68,132,200
Mvav 36,132,192
K 213,170,171
Mvav 40,136,192
X 173
Mvav 72,136,200
X 174
Mvav 104,136,208
buffer_store_dwordx4 a[212:215], v1, s[8:11], s36 offen offset:0 sc1
Mvav 136,136,216
K 220,172,173
Mvav 140,140,216
X 175
Mvav 108,140,208
X 176
Mvav 76,140,200
s_waitcnt vmcnt(5) lgkmcnt(0)
s_barrier
Mvav 44,140,192
buffer_store_dwordx4 a[220:223], v1, s[8:11], s36 offen offset:1024 sc1
K 221,174,175
Mvav 48,144,192
Ra 128,242,0
Mvav 80,144,200
X 177
Mvav 112,144,208
X 178
Mvav 144,144,216
Ra 132,242,1024
K 198,176,177
Mvav 148,148,216
buffer_store_dwordx4 a[228:231], v1, s[8:11], s36 offen offset:2048 sc1
Mvav 116,148,208
X 179
Mvav 84,148,200
X 180
Mvav 52,148,192
Ra 136,242,2048
K 199,178,179
Mvav 56,152,192
buffer_store_dwordx4 a[76:79], v1, s[8:11], s36 offen offset:3072 sc1
Ra 140,242,3072
Mvav 88,152,200
X 181
Mvav 120,152,208
X 182
Mvav 152,152,216
K 206,180,181
Mvav 156,156,216
X 183
Mvav 124,156,208
Mvav 92,156,200
K 207,182,183
Mvav 60,156,192
W 0
Cvaa 160,128,0,224
Ra 144,242,4096
Ra 148,242,5120
Mvaa 160,132,4
s_add_u32 m0, s62, 0
Ra 152,242,6144
Cvaa 164,132,20,228
X 184
Mvaa 164,128,16
X 185
Cvaa 168,128,32,232
X 186
Mvaa 168,132,36
Ra 156,242,7168
K 214,184,185
Cvaa 172,132,52,236
Dc 1,8,61,0
Mvaa 172,128,48
X 187
Mvaa 160,136,8
X 188
)"
R"(Mvaa 160,140,12
Ra 128,242,16384
K 215,186,187
Mvaa 164,140,28
Dc 1,8,61,1024
Mvaa 164,136,24
X 189
Mvaa 168,136,40
X 190
Mvaa 168,140,44
Ra 132,242,17408
K 222,188,189
Mvaa 172,140,60
Dc 1,8,61,2048
Mvaa 172,136,56
X 191
v_smfmac_f32_16x16x64_bf16 a[160:163], a[176:179], v[192:199], v240
Ra 136,242,18432
K 223,190,191
v_smfmac_f32_16x16x64_bf16 a[164:167], a[176:179], v[200:207], v240
X 160
v_smfmac_f32_16x16x64_bf16 a[168:171], a[176:179], v[208:215], v240
Dc 1,8,61,3072
v_smfmac_f32_16x16x64_bf16 a[172:175], a[176:179], v[216:223], v240
W 3
X 161
Cvaa 176,144,0,224
s_add_u32 m0, s62, 4096
X 162
Mvaa 176,148,4
Ra 140,242,19456
K 192,160,161
Cvaa 180,148,20,228
X 163
Mvaa 180,144,16
Dc 243,8,61,0
Cvaa 184,144,32,232
X 164
Mvaa 184,148,36
K 193,162,163
Cvaa 188,148,52,236
X 165
Mvaa 188,144,48
Dc 243,8,61,1024
Ra 144,242,20480
Mvaa 176,152,8
X 166
Mvaa 176,156,12
Ra 148,242,21504
K 200,164,165
Mvaa 180,156,28
X 167
Mvaa 180,152,24
Dc 243,8,61,2048
Mvaa 184,152,40
X 168
Mvaa 184,156,44
K 201,166,167
Mvaa 188,156,60
X 169
Mvaa 188,152,56
W 2
Dc 243,8,61,3072
Ra 152,242,22528
Mvav 32,128,196
s_add_u32 m0, s62, 8192
X 170
Mvav 64,128,204
Ra 156,242,23552
K 208,168,169
Mvav 96,128,212
X 171
Mvav 128,128,220
Dc 244,8,61,0
Mvav 132,132,220
X 172
Mvav 100,132,212
Ra 128,242,8192
K 209,170,171
Mvav 68,132,204
X 173
Mvav 36,132,196
Dc 244,8,61,1024
Mvav 40,136,196
X 174
Mvav 72,136,204
Ra 132,242,9216
K 216,172,173
Mvav 104,136,212
X 175
Mvav 136,136,220
Dc 244,8,61,2048
Mvav 140,140,220
X 176
Mvav 108,140,212
Ra 136,242,10240
K 217,174,175
Mvav 76,140,204
X 177
Mvav 44,140,196
W 3
Dc 244,8,61,3072
Mvav 48,144,196
X 178
Mvav 80,144,204
s_add_u32 m0, s62, 12288
Ra 140,242,11264
Mvav 112,144,212
K 194,176,177
Mvav 144,144,220
X 179
Mvav 148,148,220
X 180
Mvav 116,148,212
Dc 245,8,61,0
Ra 144,242,12288
Mvav 84,148,204
K 195,178,179
Mvav 52,148,196
X 181
Mvav 56,152,196
X 182
Mvav 88,152,204
Dc 245,8,61,1024
Ra 148,242,13312
Mvav 120,152,212
K 202,180,181
Mvav 152,152,220
X 183
Mvav 156,156,220
X 184
Mvav 124,156,212
Dc 245,8,61,2048
Ra 152,242,14336
Mvav 92,156,204
K 203,182,183
Mvav 60,156,196
W 3
X 185
Cvaa 160,128,0,224
X 186
Mvaa 160,132,4
Dc 245,8,61,3072
Ra 156,242,15360
Cvaa 164,132,20,228
s_add_u32 s61, s61, s63
K 210,184,185
Mvaa 164,128,16
X 187
Cvaa 168,128,32,232
X 188
Mvaa 168,132,36
Cvaa 172,132,52,236
s_xor_b32 s62, s62, 0x10000
K 211,186,187
Mvaa 172,128,48
X 189
Mvaa 160,136,8
Ra 128,242,24576
Ra 132,242,25600
Mvaa 160,140,12
s_add_u32 s64, s64, 1
X 190
Mvaa 164,140,28
K 218,188,189
Mvaa 164,136,24
X 191
Mvaa 168,136,40
Mvaa 168,140,44
X 160
Mvaa 172,140,60
s_add_u32 s36, s29, 327680
K 219,190,191
Mvaa 172,136,56
W 2
X 161
Cvaa 176,144,0,224
Ra 136,242,26624
Ra 140,242,27648
Mvaa 176,148,4
X 162
Cvaa 180,148,20,228
K 196,160,161
Mvaa 180,144,16
buffer_store_dwordx4 a[84:87], v1, s[8:11], s36 offen offset:0 sc1
Cvaa 184,144,32,232
X 163
Mvaa 184,148,36
X 164
Cvaa 188,148,52,236
K 197,162,163
Mvaa 188,144,48
X 165
Mvaa 176,152,8
Ra 144,242,28672
buffer_store_dwordx4 a[92:95], v1, s[8:11], s36 offen offset:1024 sc1
Mvaa 176,156,12
Ra 148,242,29696
Mvaa 180,156,28
X 166
Mvaa 180,152,24
K 204,164,165
Mvaa 184,152,40
X 167
Mvaa 184,156,44
buffer_store_dwordx4 a[188:191], v1, s[8:11], s36 offen offset:2048 sc1
Mvaa 188,156,60
X 168
Mvaa 188,152,56
W 2
Ra 152,242,30720
K 205,166,167
Mvav 32,128,192
X 169
Mvav 64,128,200
Ra 156,242,31744
buffer_store_dwordx4 a[196:199], v1, s[8:11], s36 offen offset:3072 sc1
Mvav 96,128,208
X 170
Mvav 128,128,216
v_xor_b32 v242, 0x10000, v242
K 212,168,169
Mvav 132,132,216
X 171
Mvav 100,132,208
s_add_u32 s36, s29, 331776
X 172
Mvav 68,132,200
Mvav 36,132,192
K 213,170,171
Mvav 40,136,192
X 173
Mvav 72,136,200
X 174
Mvav 104,136,208
buffer_store_dwordx4 a[236:239], v1, s[8:11], s36 offen offset:0 sc1
Mvav 136,136,216
K 220,172,173
Mvav 140,140,216
X 175
Mvav 108,140,208
X 176
Mvav 76,140,200
s_waitcnt vmcnt(5) lgkmcnt(0)
s_barrier
Mvav 44,140,192
buffer_store_dwordx4 a[244:247], v1, s[8:11], s36 offen offset:1024 sc1
K 221,174,175
Mvav 48,144,192
Ra 128,242,0
Mvav 80,144,200
X 177
Mvav 112,144,208
X 178
Mvav 144,144,216
Ra 132,242,1024
K 198,176,177
Mvav 148,148,216
buffer_store_dwordx4 a[68:71], v1, s[8:11], s36 offen offset:2048 sc1
Mvav 116,148,208
X 179
Mvav 84,148,200
X 180
Mvav 52,148,192
Ra 136,242,2048
K 199,178,179
Mvav 56,152,192
buffer_store_dwordx4 a[184:187], v1, s[8:11], s36 offen offset:3072 sc1
Ra 140,242,3072
Mvav 88,152,200
X 181
Mvav 120,152,208
X 182
Mvav 152,152,216
K 206,180,181
Mvav 156,156,216
X 183
Mvav 124,156,208
Mvav 92,156,200
K 207,182,183
Mvav 60,156,192
s_mov_b64 exec, 0
s_cmp_eq_u32 s14, 2
s_cbranch_scc1 .Lh_3_2%=
s_branch .Lh_3_3%=
.Lhr_66%=:
s_mov_b64 exec, -1
W 0
Cvaa 160,128,0,224
Ra 144,242,4096
Ra 148,242,5120
Mvaa 160,132,4
s_add_u32 m0, s62, 0
Ra 152,242,6144
Cvaa 164,132,20,228
X 184
Mvaa 164,128,16
X 185
Cvaa 168,128,32,232
X 186
Mvaa 168,132,36
Ra 156,242,7168
K 214,184,185
Cvaa 172,132,52,236
Dc 1,8,61,0
Mvaa 172,128,48
X 187
Mvaa 160,136,8
X 188
Mvaa 160,140,12
Ra 128,242,16384
K 215,186,187
Mvaa 164,140,28
Dc 1,8,61,1024
Mvaa 164,136,24
X 189
Mvaa 168,136,40
X 190
Mvaa 168,140,44
Ra 132,242,17408
K 222,188,189
Mvaa 172,140,60
Dc 1,8,61,2048
Mvaa 172,136,56
X 191
v_smfmac_f32_16x16x64_bf16 a[160:163], a[176:179], v[192:199], v240
Ra 136,242,18432
K 223,190,191
v_smfmac_f32_16x16x64_bf16 a[164:167], a[176:179], v[200:207], v240
X 160
v_smfmac_f32_16x16x64_bf16 a[168:171], a[176:179], v[208:215], v240
Dc 1,8,61,3072
v_smfmac_f32_16x16x64_bf16 a[172:175], a[176:179], v[216:223], v240
W 3
X 161
Cvaa 176,144,0,224
s_add_u32 m0, s62, 4096
X 162
Mvaa 176,148,4
Ra 140,242,19456
K 192,160,161
Cvaa 180,148,20,228
X 163
Mvaa 180,144,16
Dc 243,8,61,0
Cvaa 184,144,32,232
X 164
Mvaa 184,148,36
K 193,162,163
Cvaa 188,148,52,236
X 165
Mvaa 188,144,48
Dc 243,8,61,1024
Ra 144,242,20480
Mvaa 176,152,8
X 166
Mvaa 176,156,12
Ra 148,242,21504
K 200,164,165
Mvaa 180,156,28
X 167
Mvaa 180,152,24
Dc 243,8,61,2048
Mvaa 184,152,40
X 168
Mvaa 184,156,44
K 201,166,167
Mvaa 188,156,60
X 169
Mvaa 188,152,56
W 2
Dc 243,8,61,3072
Ra 152,242,22528
Mvav 32,128,196
s_add_u32 m0, s62, 8192
X 170
Mvav 64,128,204
Ra 156,242,23552
K 208,168,169
Mvav 96,128,212
X 171
Mvav 128,128,220
Dc 244,8,61,0
Mvav 132,132,220
X 172
Mvav 100,132,212
Ra 128,242,8192
K 209,170,171
Mvav 68,132,204
X 173
Mvav 36,132,196
Dc 244,8,61,1024
Mvav 40,136,196
X 174
Mvav 72,136,204
Ra 132,242,9216
K 216,172,173
Mvav 104,136,212
X 175
Mvav 136,136,220
)"
R"(Dc 244,8,61,2048
Mvav 140,140,220
X 176
Mvav 108,140,212
Ra 136,242,10240
K 217,174,175
Mvav 76,140,204
X 177
Mvav 44,140,196
W 3
Dc 244,8,61,3072
Mvav 48,144,196
X 178
Mvav 80,144,204
s_add_u32 m0, s62, 12288
Ra 140,242,11264
Mvav 112,144,212
K 194,176,177
Mvav 144,144,220
X 179
Mvav 148,148,220
X 180
Mvav 116,148,212
Dc 245,8,61,0
Ra 144,242,12288
Mvav 84,148,204
K 195,178,179
Mvav 52,148,196
X 181
Mvav 56,152,196
X 182
Mvav 88,152,204
Dc 245,8,61,1024
Ra 148,242,13312
Mvav 120,152,212
K 202,180,181
Mvav 152,152,220
X 183
Mvav 156,156,220
s_waitcnt vmcnt(14)
s_add_u32 s80, s71, 256
v_mov_b64 v[28:29], s[12:13]
s_mov_b64 exec, 1
buffer_store_dwordx2 v[28:29], off, s[8:11], s80 sc1
s_mov_b64 exec, -1
X 184
Mvav 124,156,212
Ra 152,242,14336
Mvav 92,156,204
K 203,182,183
Mvav 60,156,196
W 3
X 185
Cvaa 160,128,0,224
Dc 245,8,61,2048
Ra 156,242,15360
Mvaa 160,132,4
X 186
Cvaa 164,132,20,228
K 210,184,185
Mvaa 164,128,16
X 187
Cvaa 168,128,32,232
Dc 245,8,61,3072
Mvaa 168,132,36
s_add_u32 s61, s61, s63
X 188
Cvaa 172,132,52,236
s_xor_b32 s62, s62, 0x10000
K 211,186,187
Mvaa 172,128,48
X 189
Mvaa 160,136,8
Ra 128,242,24576
Ra 132,242,25600
Mvaa 160,140,12
s_add_u32 s64, s64, 1
X 190
Mvaa 164,140,28
K 218,188,189
Mvaa 164,136,24
X 191
Mvaa 168,136,40
Mvaa 168,140,44
X 160
Mvaa 172,140,60
s_add_u32 s36, s29, 196608
K 219,190,191
Mvaa 172,136,56
W 2
X 161
Cvaa 176,144,0,224
Ra 136,242,26624
Ra 140,242,27648
Mvaa 176,148,4
X 162
Cvaa 180,148,20,228
K 196,160,161
Mvaa 180,144,16
buffer_store_dwordx4 a[192:195], v1, s[8:11], s36 offen offset:0 sc1
Cvaa 184,144,32,232
X 163
Mvaa 184,148,36
X 164
Cvaa 188,148,52,236
K 197,162,163
Mvaa 188,144,48
X 165
Mvaa 176,152,8
Ra 144,242,28672
buffer_store_dwordx4 a[200:203], v1, s[8:11], s36 offen offset:1024 sc1
Mvaa 176,156,12
Ra 148,242,29696
Mvaa 180,156,28
X 166
Mvaa 180,152,24
K 204,164,165
Mvaa 184,152,40
X 167
Mvaa 184,156,44
buffer_store_dwordx4 a[208:211], v1, s[8:11], s36 offen offset:2048 sc1
Mvaa 188,156,60
X 168
Mvaa 188,152,56
W 2
Ra 152,242,30720
K 205,166,167
Mvav 32,128,192
X 169
Mvav 64,128,200
Ra 156,242,31744
buffer_store_dwordx4 a[216:219], v1, s[8:11], s36 offen offset:3072 sc1
Mvav 96,128,208
X 170
Mvav 128,128,216
v_xor_b32 v242, 0x10000, v242
K 212,168,169
Mvav 132,132,216
X 171
Mvav 100,132,208
s_add_u32 s36, s29, 200704
X 172
Mvav 68,132,200
Mvav 36,132,192
K 213,170,171
Mvav 40,136,192
X 173
Mvav 72,136,200
X 174
Mvav 104,136,208
buffer_store_dwordx4 a[224:227], v1, s[8:11], s36 offen offset:0 sc1
Mvav 136,136,216
K 220,172,173
Mvav 140,140,216
X 175
Mvav 108,140,208
X 176
Mvav 76,140,200
s_waitcnt vmcnt(5) lgkmcnt(0)
s_barrier
Mvav 44,140,192
buffer_store_dwordx4 a[232:235], v1, s[8:11], s36 offen offset:1024 sc1
K 221,174,175
Mvav 48,144,192
Ra 128,242,0
Mvav 80,144,200
X 177
Mvav 112,144,208
X 178
Mvav 144,144,216
Ra 132,242,1024
K 198,176,177
Mvav 148,148,216
buffer_store_dwordx4 a[240:243], v1, s[8:11], s36 offen offset:2048 sc1
Mvav 116,148,208
X 179
Mvav 84,148,200
X 180
Mvav 52,148,192
Ra 136,242,2048
K 199,178,179
Mvav 56,152,192
buffer_store_dwordx4 a[100:103], v1, s[8:11], s36 offen offset:3072 sc1
Ra 140,242,3072
Mvav 88,152,200
X 181
Mvav 120,152,208
X 182
Mvav 152,152,216
K 206,180,181
Mvav 156,156,216
X 183
Mvav 124,156,208
Mvav 92,156,200
K 207,182,183
Mvav 60,156,192
W 0
Cvaa 160,128,0,224
Ra 144,242,4096
Ra 148,242,5120
Mvaa 160,132,4
s_add_u32 m0, s62, 0
Ra 152,242,6144
Cvaa 164,132,20,228
X 184
Mvaa 164,128,16
X 185
Cvaa 168,128,32,232
X 186
Mvaa 168,132,36
Ra 156,242,7168
K 214,184,185
Cvaa 172,132,52,236
Dc 1,8,61,0
Mvaa 172,128,48
X 187
Mvaa 160,136,8
X 188
Mvaa 160,140,12
Ra 128,242,16384
K 215,186,187
Mvaa 164,140,28
Dc 1,8,61,1024
Mvaa 164,136,24
X 189
Mvaa 168,136,40
X 190
Mvaa 168,140,44
Ra 132,242,17408
K 222,188,189
Mvaa 172,140,60
Dc 1,8,61,2048
Mvaa 172,136,56
X 191
v_smfmac_f32_16x16x64_bf16 a[160:163], a[176:179], v[192:199], v240
Ra 136,242,18432
K 223,190,191
v_smfmac_f32_16x16x64_bf16 a[164:167], a[176:179], v[200:207], v240
X 160
v_smfmac_f32_16x16x64_bf16 a[168:171], a[176:179], v[208:215], v240
Dc 1,8,61,3072
v_smfmac_f32_16x16x64_bf16 a[172:175], a[176:179], v[216:223], v240
W 3
X 161
Cvaa 176,144,0,224
s_add_u32 m0, s62, 4096
X 162
Mvaa 176,148,4
Ra 140,242,19456
K 192,160,161
Cvaa 180,148,20,228
X 163
Mvaa 180,144,16
Dc 243,8,61,0
Cvaa 184,144,32,232
X 164
Mvaa 184,148,36
K 193,162,163
Cvaa 188,148,52,236
X 165
Mvaa 188,144,48
Dc 243,8,61,1024
Ra 144,242,20480
Mvaa 176,152,8
X 166
Mvaa 176,156,12
Ra 148,242,21504
K 200,164,165
Mvaa 180,156,28
X 167
Mvaa 180,152,24
Dc 243,8,61,2048
Mvaa 184,152,40
X 168
Mvaa 184,156,44
K 201,166,167
Mvaa 188,156,60
X 169
Mvaa 188,152,56
W 2
Dc 243,8,61,3072
Ra 152,242,22528
Mvav 32,128,196
s_add_u32 m0, s62, 8192
X 170
Mvav 64,128,204
Ra 156,242,23552
K 208,168,169
Mvav 96,128,212
X 171
Mvav 128,128,220
Dc 244,8,61,0
Mvav 132,132,220
X 172
Mvav 100,132,212
Ra 128,242,8192
K 209,170,171
Mvav 68,132,204
X 173
Mvav 36,132,196
Dc 244,8,61,1024
Mvav 40,136,196
X 174
Mvav 72,136,204
Ra 132,242,9216
K 216,172,173
Mvav 104,136,212
X 175
Mvav 136,136,220
Dc 244,8,61,2048
Mvav 140,140,220
X 176
Mvav 108,140,212
Ra 136,242,10240
K 217,174,175
Mvav 76,140,204
X 177
Mvav 44,140,196
W 3
Dc 244,8,61,3072
Mvav 48,144,196
X 178
Mvav 80,144,204
s_add_u32 m0, s62, 12288
Ra 140,242,11264
Mvav 112,144,212
K 194,176,177
Mvav 144,144,220
X 179
Mvav 148,148,220
X 180
Mvav 116,148,212
Dc 245,8,61,0
Ra 144,242,12288
Mvav 84,148,204
K 195,178,179
Mvav 52,148,196
X 181
Mvav 56,152,196
X 182
Mvav 88,152,204
Dc 245,8,61,1024
Ra 148,242,13312
Mvav 120,152,212
K 202,180,181
Mvav 152,152,220
X 183
Mvav 156,156,220
X 184
Mvav 124,156,212
Dc 245,8,61,2048
Ra 152,242,14336
Mvav 92,156,204
K 203,182,183
v_bfe_u32 v28, v1, 4, 4
Mvav 60,156,196
W 3
X 185
Cvaa 160,128,0,224
Ra 156,242,15360
v_lshlrev_b32 v28, 3, v28
Mvaa 160,132,4
s_lshl_b32 s80, s56, 9
s_add_u32 s80, s80, 4352
Dc 245,8,61,3072
Cvaa 164,132,20,228
s_add_u32 s61, s61, s63
X 186
Mvaa 164,128,16
K 210,184,185
Cvaa 168,128,32,232
X 187
Mvaa 168,132,36
buffer_load_dwordx2 v[28:29], v28, s[8:11], s80 offen sc1
Cvaa 172,132,52,236
s_xor_b32 s62, s62, 0x10000
X 188
Mvaa 172,128,48
Ra 128,242,24576
K 211,186,187
Mvaa 160,136,8
X 189
Mvaa 160,140,12
s_add_u32 s64, s64, 1
X 190
Mvaa 164,140,28
Ra 132,242,25600
K 218,188,189
Mvaa 164,136,24
X 191
Mvaa 168,136,40
Mvaa 168,140,44
X 160
Mvaa 172,140,60
s_add_u32 s36, s29, 262144
K 219,190,191
Mvaa 172,136,56
W 2
X 161
Cvaa 176,144,0,224
)"
R"(Ra 136,242,26624
Ra 140,242,27648
Mvaa 176,148,4
X 162
Cvaa 180,148,20,228
K 196,160,161
Mvaa 180,144,16
buffer_store_dwordx4 a[64:67], v1, s[8:11], s36 offen offset:0 sc1
Cvaa 184,144,32,232
X 163
Mvaa 184,148,36
X 164
Cvaa 188,148,52,236
K 197,162,163
Mvaa 188,144,48
X 165
Mvaa 176,152,8
Ra 144,242,28672
buffer_store_dwordx4 a[72:75], v1, s[8:11], s36 offen offset:1024 sc1
Mvaa 176,156,12
Ra 148,242,29696
Mvaa 180,156,28
X 166
Mvaa 180,152,24
K 204,164,165
Mvaa 184,152,40
X 167
Mvaa 184,156,44
buffer_store_dwordx4 a[80:83], v1, s[8:11], s36 offen offset:2048 sc1
Mvaa 188,156,60
X 168
Mvaa 188,152,56
W 2
Ra 152,242,30720
K 205,166,167
Mvav 32,128,192
X 169
Mvav 64,128,200
Ra 156,242,31744
buffer_store_dwordx4 a[88:91], v1, s[8:11], s36 offen offset:3072 sc1
Mvav 96,128,208
X 170
Mvav 128,128,216
v_xor_b32 v242, 0x10000, v242
K 212,168,169
Mvav 132,132,216
X 171
Mvav 100,132,208
s_add_u32 s36, s29, 266240
X 172
Mvav 68,132,200
Mvav 36,132,192
K 213,170,171
Mvav 40,136,192
X 173
Mvav 72,136,200
X 174
Mvav 104,136,208
buffer_store_dwordx4 a[96:99], v1, s[8:11], s36 offen offset:0 sc1
Mvav 136,136,216
s_waitcnt vmcnt(5)
v_cmp_eq_u64 vcc, s[12:13], v[28:29]
s_cmp_eq_u64 vcc, -1
s_cbranch_scc0 .Lspin_67%=
.Lok_68%=:
K 220,172,173
Mvav 140,140,216
X 175
Mvav 108,140,208
X 176
Mvav 76,140,200
W 0
s_barrier
Mvav 44,140,192
buffer_store_dwordx4 a[104:107], v1, s[8:11], s36 offen offset:1024 sc1
K 221,174,175
Mvav 48,144,192
Ra 128,242,0
Mvav 80,144,200
X 177
Mvav 112,144,208
X 178
Mvav 144,144,216
Ra 132,242,1024
K 198,176,177
Mvav 148,148,216
buffer_store_dwordx4 a[112:115], v1, s[8:11], s36 offen offset:2048 sc1
Mvav 116,148,208
X 179
Mvav 84,148,200
X 180
Mvav 52,148,192
Ra 136,242,2048
K 199,178,179
Mvav 56,152,192
buffer_store_dwordx4 a[120:123], v1, s[8:11], s36 offen offset:3072 sc1
Ra 140,242,3072
Mvav 88,152,200
X 181
Mvav 120,152,208
X 182
Mvav 152,152,216
K 206,180,181
Mvav 156,156,216
X 183
Mvav 124,156,208
Mvav 92,156,200
K 207,182,183
Mvav 60,156,192
W 0
Cvaa 160,128,0,224
Ra 144,242,4096
Ra 148,242,5120
Mvaa 160,132,4
s_add_u32 m0, s62, 0
Ra 152,242,6144
Cvaa 164,132,20,228
X 184
Mvaa 164,128,16
X 185
Cvaa 168,128,32,232
X 186
Mvaa 168,132,36
Ra 156,242,7168
K 214,184,185
Cvaa 172,132,52,236
Dc 1,8,61,0
Mvaa 172,128,48
X 187
Mvaa 160,136,8
X 188
Mvaa 160,140,12
Ra 128,242,16384
K 215,186,187
Mvaa 164,140,28
Dc 1,8,61,1024
Mvaa 164,136,24
X 189
Mvaa 168,136,40
X 190
Mvaa 168,140,44
Ra 132,242,17408
K 222,188,189
Mvaa 172,140,60
Dc 1,8,61,2048
Mvaa 172,136,56
X 191
v_smfmac_f32_16x16x64_bf16 a[160:163], a[176:179], v[192:199], v240
Ra 136,242,18432
K 223,190,191
v_smfmac_f32_16x16x64_bf16 a[164:167], a[176:179], v[200:207], v240
X 160
v_smfmac_f32_16x16x64_bf16 a[168:171], a[176:179], v[208:215], v240
Dc 1,8,61,3072
v_smfmac_f32_16x16x64_bf16 a[172:175], a[176:179], v[216:223], v240
W 3
X 161
Cvaa 176,144,0,224
s_add_u32 m0, s62, 4096
X 162
Mvaa 176,148,4
Ra 140,242,19456
K 192,160,161
Cvaa 180,148,20,228
X 163
Mvaa 180,144,16
Dc 243,8,61,0
Cvaa 184,144,32,232
X 164
Mvaa 184,148,36
K 193,162,163
Cvaa 188,148,52,236
X 165
Mvaa 188,144,48
Dc 243,8,61,1024
Ra 144,242,20480
Mvaa 176,152,8
X 166
Mvaa 176,156,12
Ra 148,242,21504
K 200,164,165
Mvaa 180,156,28
X 167
Mvaa 180,152,24
Dc 243,8,61,2048
Mvaa 184,152,40
X 168
Mvaa 184,156,44
K 201,166,167
Mvaa 188,156,60
X 169
Mvaa 188,152,56
W 2
Dc 243,8,61,3072
Ra 152,242,22528
Mvav 32,128,196
s_add_u32 m0, s62, 8192
X 170
Mvav 64,128,204
Ra 156,242,23552
K 208,168,169
Mvav 96,128,212
X 171
Mvav 128,128,220
Dc 244,8,61,0
Mvav 132,132,220
X 172
Mvav 100,132,212
Ra 128,242,8192
K 209,170,171
Mvav 68,132,204
X 173
Mvav 36,132,196
Dc 244,8,61,1024
Mvav 40,136,196
X 174
Mvav 72,136,204
Ra 132,242,9216
K 216,172,173
Mvav 104,136,212
X 175
Mvav 136,136,220
Dc 244,8,61,2048
Mvav 140,140,220
X 176
Mvav 108,140,212
Ra 136,242,10240
K 217,174,175
Mvav 76,140,204
X 177
Mvav 44,140,196
W 3
Dc 244,8,61,3072
Mvav 48,144,196
X 178
Mvav 80,144,204
s_add_u32 m0, s62, 12288
Ra 140,242,11264
Mvav 112,144,212
K 194,176,177
Mvav 144,144,220
X 179
Mvav 148,148,220
X 180
Mvav 116,148,212
Dc 245,8,61,0
Ra 144,242,12288
Mvav 84,148,204
K 195,178,179
Mvav 52,148,196
X 181
Mvav 56,152,196
X 182
Mvav 88,152,204
Dc 245,8,61,1024
Ra 148,242,13312
Mvav 120,152,212
s_bitcmp1_b32 195, s57
s_cselect_b32 s22, -1, 0
K 202,180,181
Mvav 152,152,220
s_lshr_b32 s82, s57, 2
X 183
Mvav 156,156,220
s_waitcnt vmcnt(14)
s_add_u32 s80, s71, 384
v_mov_b64 v[28:29], s[12:13]
s_mov_b64 exec, 1
buffer_store_dwordx2 v[28:29], off, s[8:11], s80 sc1
s_mov_b64 exec, -1
X 184
Mvav 124,156,212
s_and_b32 s81, s57, 1
s_lshl1_add_u32 s81, s82, s81
Ra 152,242,14336
Mvav 92,156,204
s_lshl_b32 s82, s56, 2
K 203,182,183
Mvav 60,156,196
W 3
X 185
Cvaa 160,128,0,224
s_add_u32 s81, s81, s82
Dc 245,8,61,2048
Ra 156,242,15360
Mvaa 160,132,4
s_lshl_b32 s81, s81, 1
X 186
Cvaa 164,132,20,228
s_add_u32 s81, s81, s14
s_sub_u32 s81, s81, 2
K 210,184,185
Mvaa 164,128,16
s_mul_i32 s81, s81, 20480
X 187
Cvaa 168,128,32,232
Dc 245,8,61,3072
v_min_u32 v2, 624, v1
Mvaa 168,132,36
s_add_u32 s61, s61, s63
X 188
Cvaa 172,132,52,236
s_xor_b32 s62, s62, 0x10000
v_lshl_add_u32 v2, v2, 3, s81
K 211,186,187
Mvaa 172,128,48
Ra 128,242,24576
v_and_b32 v2, s22, v2
Mvaa 160,136,8
s_and_b32 s22, s22, 5120
X 189
Mvaa 160,140,12
s_add_u32 s64, s64, 1
global_load_dword v3, v2, s[26:27]
Ra 132,242,25600
Mvaa 164,140,28
X 190
Mvaa 164,136,24
v_add_u32 v2, s22, v2
K 218,188,189
Mvaa 168,136,40
X 191
Mvaa 168,140,44
X 160
Mvaa 172,140,60
K 219,190,191
Mvaa 172,136,56
W 2
X 161
Cvaa 176,144,0,224
Ra 136,242,26624
Ra 140,242,27648
Mvaa 176,148,4
X 162
Cvaa 180,148,20,228
K 196,160,161
Mvaa 180,144,16
X 163
Cvaa 184,144,32,232
Mvaa 184,148,36
X 164
Cvaa 188,148,52,236
K 197,162,163
Mvaa 188,144,48
X 165
Mvaa 176,152,8
Ra 144,242,28672
Ra 148,242,29696
Mvaa 176,156,12
X 166
Mvaa 180,156,28
K 204,164,165
Mvaa 180,152,24
X 167
Mvaa 184,152,40
Mvaa 184,156,44
X 168
Mvaa 188,156,60
Mvaa 188,152,56
W 2
Ra 152,242,30720
K 205,166,167
Mvav 32,128,192
X 169
Mvav 64,128,200
Ra 156,242,31744
v_xor_b32 v242, 0x10000, v242
Mvav 96,128,208
X 170
Mvav 128,128,216
K 212,168,169
Mvav 132,132,216
X 171
Mvav 100,132,208
X 172
Mvav 68,132,200
Mvav 36,132,192
K 213,170,171
Mvav 40,136,192
X 173
Mvav 72,136,200
X 174
Mvav 104,136,208
Mvav 136,136,216
K 220,172,173
Mvav 140,140,216
X 175
Mvav 108,140,208
X 176
Mvav 76,140,200
s_waitcnt vmcnt(1) lgkmcnt(0)
s_barrier
Mvav 44,140,192
Ra 128,242,0
K 221,174,175
Mvav 48,144,192
X 177
Mvav 80,144,200
.Lh_3_2%=:
s_cbranch_execz .Lh_3_2_1%=
)"
R"(X 178
Mvav 112,144,208
Ra 132,242,1024
Mvav 144,144,216
K 198,176,177
Mvav 148,148,216
X 179
Mvav 116,148,208
X 180
Mvav 84,148,200
Ra 136,242,2048
Mvav 52,148,192
K 199,178,179
Mvav 56,152,192
X 181
Mvav 88,152,200
Ra 140,242,3072
Mvav 120,152,208
X 182
Mvav 152,152,216
K 206,180,181
Mvav 156,156,216
X 183
Mvav 124,156,208
Mvav 92,156,200
K 207,182,183
Mvav 60,156,192
W 0
Cvaa 160,128,0,224
Ra 144,242,4096
Ra 148,242,5120
Mvaa 160,132,4
s_add_u32 m0, s62, 0
Ra 152,242,6144
Cvaa 164,132,20,228
X 184
Mvaa 164,128,16
X 185
Cvaa 168,128,32,232
X 186
Mvaa 168,132,36
Ra 156,242,7168
K 214,184,185
Cvaa 172,132,52,236
Dc 1,8,61,0
Mvaa 172,128,48
X 187
Mvaa 160,136,8
X 188
Mvaa 160,140,12
Ra 128,242,16384
K 215,186,187
Mvaa 164,140,28
Dc 1,8,61,1024
Mvaa 164,136,24
X 189
Mvaa 168,136,40
X 190
Mvaa 168,140,44
Ra 132,242,17408
K 222,188,189
Mvaa 172,140,60
Dc 1,8,61,2048
Mvaa 172,136,56
X 191
v_smfmac_f32_16x16x64_bf16 a[160:163], a[176:179], v[192:199], v240
Ra 136,242,18432
K 223,190,191
v_smfmac_f32_16x16x64_bf16 a[164:167], a[176:179], v[200:207], v240
X 160
v_smfmac_f32_16x16x64_bf16 a[168:171], a[176:179], v[208:215], v240
Dc 1,8,61,3072
v_smfmac_f32_16x16x64_bf16 a[172:175], a[176:179], v[216:223], v240
W 3
X 161
Cvaa 176,144,0,224
s_add_u32 m0, s62, 4096
X 162
Mvaa 176,148,4
Ra 140,242,19456
K 192,160,161
Cvaa 180,148,20,228
X 163
Mvaa 180,144,16
Dc 243,8,61,0
Cvaa 184,144,32,232
X 164
Mvaa 184,148,36
K 193,162,163
Cvaa 188,148,52,236
X 165
Mvaa 188,144,48
Dc 243,8,61,1024
Ra 144,242,20480
Mvaa 176,152,8
X 166
Mvaa 176,156,12
Ra 148,242,21504
K 200,164,165
Mvaa 180,156,28
X 167
Mvaa 180,152,24
Dc 243,8,61,2048
Mvaa 184,152,40
X 168
Mvaa 184,156,44
K 201,166,167
Mvaa 188,156,60
X 169
Mvaa 188,152,56
W 2
Dc 243,8,61,3072
Ra 152,242,22528
Mvav 32,128,196
s_add_u32 m0, s62, 8192
X 170
Mvav 64,128,204
Ra 156,242,23552
K 208,168,169
Mvav 96,128,212
X 171
Mvav 128,128,220
Dc 244,8,61,0
Mvav 132,132,220
X 172
Mvav 100,132,212
Ra 128,242,8192
K 209,170,171
Mvav 68,132,204
X 173
Mvav 36,132,196
Dc 244,8,61,1024
Mvav 40,136,196
X 174
Mvav 72,136,204
Ra 132,242,9216
K 216,172,173
Mvav 104,136,212
X 175
Mvav 136,136,220
Dc 244,8,61,2048
Mvav 140,140,220
X 176
Mvav 108,140,212
Ra 136,242,10240
K 217,174,175
Mvav 76,140,204
X 177
Mvav 44,140,196
W 3
Dc 244,8,61,3072
Mvav 48,144,196
X 178
Mvav 80,144,204
s_add_u32 m0, s62, 12288
Ra 140,242,11264
Mvav 112,144,212
K 194,176,177
Mvav 144,144,220
X 179
Mvav 148,148,220
X 180
Mvav 116,148,212
Dc 245,8,61,0
Ra 144,242,12288
Mvav 84,148,204
K 195,178,179
Mvav 52,148,196
X 181
Mvav 56,152,196
X 182
Mvav 88,152,204
Dc 245,8,61,1024
Ra 148,242,13312
Mvav 120,152,212
K 202,180,181
Mvav 152,152,220
X 183
Mvav 156,156,220
X 184
Mvav 124,156,212
Dc 245,8,61,2048
Ra 152,242,14336
Mvav 92,156,204
K 203,182,183
Mvav 60,156,196
W 3
X 185
Cvaa 160,128,0,224
X 186
Mvaa 160,132,4
Dc 245,8,61,3072
Ra 156,242,15360
Cvaa 164,132,20,228
s_add_u32 s61, s61, s63
K 210,184,185
Mvaa 164,128,16
X 187
Cvaa 168,128,32,232
X 188
Mvaa 168,132,36
Cvaa 172,132,52,236
s_xor_b32 s62, s62, 0x10000
global_load_dword v3, v2, s[26:27]
K 211,186,187
Mvaa 172,128,48
X 189
Mvaa 160,136,8
Ra 128,242,24576
Ra 132,242,25600
Mvaa 160,140,12
s_add_u32 s64, s64, 1
X 190
Mvaa 164,140,28
v_add_u32 v2, s22, v2
K 218,188,189
Mvaa 164,136,24
X 191
Mvaa 168,136,40
Mvaa 168,140,44
X 160
Mvaa 172,140,60
K 219,190,191
Mvaa 172,136,56
W 2
X 161
Cvaa 176,144,0,224
Ra 136,242,26624
Ra 140,242,27648
Mvaa 176,148,4
X 162
Cvaa 180,148,20,228
K 196,160,161
Mvaa 180,144,16
X 163
Cvaa 184,144,32,232
Mvaa 184,148,36
X 164
Cvaa 188,148,52,236
K 197,162,163
Mvaa 188,144,48
X 165
Mvaa 176,152,8
Ra 144,242,28672
Ra 148,242,29696
Mvaa 176,156,12
X 166
Mvaa 180,156,28
K 204,164,165
Mvaa 180,152,24
X 167
Mvaa 184,152,40
Mvaa 184,156,44
X 168
Mvaa 188,156,60
Mvaa 188,152,56
W 2
Ra 152,242,30720
K 205,166,167
Mvav 32,128,192
X 169
Mvav 64,128,200
Ra 156,242,31744
v_xor_b32 v242, 0x10000, v242
Mvav 96,128,208
X 170
Mvav 128,128,216
K 212,168,169
Mvav 132,132,216
X 171
Mvav 100,132,208
X 172
Mvav 68,132,200
Mvav 36,132,192
K 213,170,171
Mvav 40,136,192
X 173
Mvav 72,136,200
X 174
Mvav 104,136,208
Mvav 136,136,216
K 220,172,173
Mvav 140,140,216
X 175
Mvav 108,140,208
X 176
Mvav 76,140,200
s_waitcnt vmcnt(1) lgkmcnt(0)
s_barrier
Mvav 44,140,192
Ra 128,242,0
K 221,174,175
Mvav 48,144,192
X 177
Mvav 80,144,200
X 178
Mvav 112,144,208
Ra 132,242,1024
Mvav 144,144,216
K 198,176,177
Mvav 148,148,216
X 179
Mvav 116,148,208
X 180
Mvav 84,148,200
Ra 136,242,2048
Mvav 52,148,192
K 199,178,179
Mvav 56,152,192
X 181
Mvav 88,152,200
Ra 140,242,3072
Mvav 120,152,208
X 182
Mvav 152,152,216
K 206,180,181
Mvav 156,156,216
X 183
Mvav 124,156,208
Mvav 92,156,200
K 207,182,183
Mvav 60,156,192
W 0
Cvaa 160,128,0,224
Ra 144,242,4096
Ra 148,242,5120
Mvaa 160,132,4
s_add_u32 m0, s62, 0
Ra 152,242,6144
Cvaa 164,132,20,228
X 184
Mvaa 164,128,16
X 185
Cvaa 168,128,32,232
X 186
Mvaa 168,132,36
Ra 156,242,7168
K 214,184,185
Cvaa 172,132,52,236
Dc 1,8,61,0
Mvaa 172,128,48
X 187
Mvaa 160,136,8
X 188
Mvaa 160,140,12
Ra 128,242,16384
K 215,186,187
Mvaa 164,140,28
Dc 1,8,61,1024
Mvaa 164,136,24
X 189
Mvaa 168,136,40
X 190
Mvaa 168,140,44
Ra 132,242,17408
K 222,188,189
Mvaa 172,140,60
Dc 1,8,61,2048
Mvaa 172,136,56
X 191
v_smfmac_f32_16x16x64_bf16 a[160:163], a[176:179], v[192:199], v240
Ra 136,242,18432
K 223,190,191
v_smfmac_f32_16x16x64_bf16 a[164:167], a[176:179], v[200:207], v240
X 160
v_smfmac_f32_16x16x64_bf16 a[168:171], a[176:179], v[208:215], v240
Dc 1,8,61,3072
v_smfmac_f32_16x16x64_bf16 a[172:175], a[176:179], v[216:223], v240
W 3
X 161
Cvaa 176,144,0,224
s_add_u32 m0, s62, 4096
X 162
Mvaa 176,148,4
Ra 140,242,19456
K 192,160,161
Cvaa 180,148,20,228
X 163
Mvaa 180,144,16
Dc 243,8,61,0
Cvaa 184,144,32,232
X 164
Mvaa 184,148,36
K 193,162,163
Cvaa 188,148,52,236
X 165
Mvaa 188,144,48
Dc 243,8,61,1024
Ra 144,242,20480
Mvaa 176,152,8
X 166
Mvaa 176,156,12
Ra 148,242,21504
K 200,164,165
Mvaa 180,156,28
X 167
Mvaa 180,152,24
Dc 243,8,61,2048
Mvaa 184,152,40
X 168
)"
R"(Mvaa 184,156,44
K 201,166,167
Mvaa 188,156,60
X 169
Mvaa 188,152,56
W 2
Dc 243,8,61,3072
Ra 152,242,22528
Mvav 32,128,196
s_add_u32 m0, s62, 8192
X 170
Mvav 64,128,204
Ra 156,242,23552
K 208,168,169
Mvav 96,128,212
X 171
Mvav 128,128,220
Dc 244,8,61,0
Mvav 132,132,220
X 172
Mvav 100,132,212
Ra 128,242,8192
K 209,170,171
Mvav 68,132,204
X 173
Mvav 36,132,196
Dc 244,8,61,1024
Mvav 40,136,196
X 174
Mvav 72,136,204
Ra 132,242,9216
K 216,172,173
Mvav 104,136,212
X 175
Mvav 136,136,220
Dc 244,8,61,2048
Mvav 140,140,220
X 176
Mvav 108,140,212
Ra 136,242,10240
K 217,174,175
Mvav 76,140,204
X 177
Mvav 44,140,196
W 3
Dc 244,8,61,3072
Mvav 48,144,196
X 178
Mvav 80,144,204
s_add_u32 m0, s62, 12288
Ra 140,242,11264
Mvav 112,144,212
K 194,176,177
Mvav 144,144,220
X 179
Mvav 148,148,220
X 180
Mvav 116,148,212
Dc 245,8,61,0
Ra 144,242,12288
Mvav 84,148,204
K 195,178,179
Mvav 52,148,196
X 181
Mvav 56,152,196
X 182
Mvav 88,152,204
Dc 245,8,61,1024
Ra 148,242,13312
Mvav 120,152,212
K 202,180,181
Mvav 152,152,220
X 183
Mvav 156,156,220
X 184
Mvav 124,156,212
Dc 245,8,61,2048
Ra 152,242,14336
Mvav 92,156,204
K 203,182,183
Mvav 60,156,196
W 3
X 185
Cvaa 160,128,0,224
X 186
Mvaa 160,132,4
Dc 245,8,61,3072
Ra 156,242,15360
Cvaa 164,132,20,228
s_add_u32 s61, s61, s63
K 210,184,185
Mvaa 164,128,16
X 187
Cvaa 168,128,32,232
X 188
Mvaa 168,132,36
Cvaa 172,132,52,236
s_xor_b32 s62, s62, 0x10000
global_load_dword v3, v2, s[26:27]
K 211,186,187
Mvaa 172,128,48
Ra 128,242,24576
v_add_u32 v2, s22, v2
Mvaa 160,136,8
s_lshr_b32 s80, s15, 3
s_lshl_b32 s80, s80, 1
s_add_u32 s80, s80, s14
Mvaa 160,140,12
s_sub_u32 s80, s80, 2
s_lshl_b32 s80, s80, 12
v_min_u32 v27, 496, v1
Mvaa 164,140,28
s_add_u32 s64, s64, 1
X 189
Mvaa 164,136,24
Ra 132,242,25600
v_lshl_add_u32 v27, v27, 3, s80
Mvaa 168,136,40
X 190
Mvaa 168,140,44
v_min_u32 v27, .Lend%=-.Lbeg%=-4, v27
K 218,188,189
Mvaa 172,140,60
global_load_dword v3, v27, s[6:7]
Mvaa 172,136,56
W 2
X 191
Cvaa 176,144,0,224
X 160
Mvaa 176,148,4
Ra 136,242,26624
K 219,190,191
Cvaa 180,148,20,228
X 161
Mvaa 180,144,16
Ra 140,242,27648
Cvaa 184,144,32,232
X 162
Mvaa 184,148,36
K 196,160,161
Cvaa 188,148,52,236
X 163
Mvaa 188,144,48
X 164
Mvaa 176,152,8
Ra 144,242,28672
K 197,162,163
Mvaa 176,156,12
X 165
Mvaa 180,156,28
Ra 148,242,29696
Mvaa 180,152,24
X 166
Mvaa 184,152,40
K 204,164,165
Mvaa 184,156,44
X 167
Mvaa 188,156,60
X 168
Mvaa 188,152,56
W 2
Ra 152,242,30720
K 205,166,167
Mvav 32,128,192
X 169
Mvav 64,128,200
Ra 156,242,31744
v_xor_b32 v242, 0x10000, v242
Mvav 96,128,208
X 170
Mvav 128,128,216
K 212,168,169
Mvav 132,132,216
X 171
Mvav 100,132,208
X 172
Mvav 68,132,200
Mvav 36,132,192
K 213,170,171
Mvav 40,136,192
X 173
Mvav 72,136,200
X 174
Mvav 104,136,208
Mvav 136,136,216
K 220,172,173
Mvav 140,140,216
X 175
Mvav 108,140,208
.Lh_3_3%=:
s_cbranch_execz .Lhr_66%=
X 176
Mvav 76,140,200
s_waitcnt vmcnt(2) lgkmcnt(0)
s_barrier
Mvav 44,140,192
Ra 128,242,0
K 221,174,175
Mvav 48,144,192
X 177
Mvav 80,144,200
X 178
Mvav 112,144,208
Ra 132,242,1024
Mvav 144,144,216
K 198,176,177
Mvav 148,148,216
X 179
Mvav 116,148,208
X 180
Mvav 84,148,200
Ra 136,242,2048
Mvav 52,148,192
K 199,178,179
Mvav 56,152,192
X 181
Mvav 88,152,200
Ra 140,242,3072
Mvav 120,152,208
X 182
Mvav 152,152,216
K 206,180,181
Mvav 156,156,216
X 183
Mvav 124,156,208
Mvav 92,156,200
K 207,182,183
Mvav 60,156,192
W 0
Cvaa 160,128,0,224
Ra 144,242,4096
Ra 148,242,5120
Mvaa 160,132,4
s_add_u32 m0, s62, 0
Ra 152,242,6144
Cvaa 164,132,20,228
X 184
Mvaa 164,128,16
X 185
Cvaa 168,128,32,232
X 186
Mvaa 168,132,36
Ra 156,242,7168
K 214,184,185
Cvaa 172,132,52,236
Dc 1,8,61,0
Mvaa 172,128,48
X 187
Mvaa 160,136,8
X 188
Mvaa 160,140,12
Ra 128,242,16384
K 215,186,187
Mvaa 164,140,28
Dc 1,8,61,1024
Mvaa 164,136,24
X 189
Mvaa 168,136,40
X 190
Mvaa 168,140,44
Ra 132,242,17408
K 222,188,189
Mvaa 172,140,60
Dc 1,8,61,2048
Mvaa 172,136,56
X 191
v_smfmac_f32_16x16x64_bf16 a[160:163], a[176:179], v[192:199], v240
Ra 136,242,18432
K 223,190,191
v_smfmac_f32_16x16x64_bf16 a[164:167], a[176:179], v[200:207], v240
X 160
v_smfmac_f32_16x16x64_bf16 a[168:171], a[176:179], v[208:215], v240
Dc 1,8,61,3072
v_smfmac_f32_16x16x64_bf16 a[172:175], a[176:179], v[216:223], v240
W 3
X 161
Cvaa 176,144,0,224
s_add_u32 m0, s62, 4096
X 162
Mvaa 176,148,4
s_sub_u32 s81, s57, 1
Ra 140,242,19456
K 192,160,161
Cvaa 180,148,20,228
X 163
Mvaa 180,144,16
s_lshl_b32 s85, s81, 5
Dc 243,8,61,0
Cvaa 184,144,32,232
X 164
Mvaa 184,148,36
s_add_u32 s85, s85, s56
K 193,162,163
Cvaa 188,148,52,236
X 165
Mvaa 188,144,48
Dc 243,8,61,1024
Ra 144,242,20480
Mvaa 176,152,8
s_lshl_b32 s85, s85, 1
X 166
Mvaa 176,156,12
Ra 148,242,21504
K 200,164,165
Mvaa 180,156,28
s_add_u32 s85, s85, s14
X 167
Mvaa 180,152,24
Dc 243,8,61,2048
Mvaa 184,152,40
s_sub_u32 s85, s85, 2
X 168
Mvaa 184,156,44
K 201,166,167
Mvaa 188,156,60
X 169
Mvaa 188,152,56
W 2
s_cmp_lt_u32 s85, 160
s_cselect_b64 s[20:21], 0, -1
Dc 243,8,61,3072
Mvav 32,128,196
s_add_u32 m0, s62, 8192
Ra 152,242,22528
Ra 156,242,23552
Mvav 64,128,204
s_mul_i32 s18, s85, 205
X 170
Mvav 96,128,212
K 208,168,169
Mvav 128,128,220
s_lshr_b32 s18, s18, 12
X 171
Mvav 132,132,220
Dc 244,8,61,0
Ra 128,242,8192
Mvav 100,132,212
X 172
Mvav 68,132,204
s_and_b32 s18, s18, 7
K 209,170,171
Mvav 36,132,196
X 173
Mvav 40,136,196
Dc 244,8,61,1024
v_lshrrev_b32 v4, 1, v1
Mvav 72,136,204
X 174
Mvav 104,136,212
Ra 132,242,9216
K 216,172,173
Mvav 136,136,220
X 175
Mvav 140,140,220
Dc 244,8,61,2048
v_min_u32 v5, 344, v4
Mvav 108,140,212
s_lshl_b32 s81, s18, 3
X 176
Mvav 76,140,204
Ra 136,242,10240
K 217,174,175
Mvav 44,140,196
W 3
X 177
Mvav 48,144,196
Dc 244,8,61,3072
v_mov_b32 v6, s81
Mvav 80,144,204
s_add_u32 m0, s62, 12288
X 178
Mvav 112,144,212
Ra 140,242,11264
K 194,176,177
Mvav 144,144,220
X 179
Mvav 148,148,220
buffer_load_dwordx2 v[8:9], v4, s[8:11], 0 offen offset:2560 sc1
Ra 144,242,12288
Mvav 116,148,212
X 180
Mvav 84,148,204
K 195,178,179
Mvav 52,148,196
X 181
Mvav 56,152,196
buffer_load_dwordx2 v[10:11], v5, s[8:11], 0 offen offset:3072 sc1
Ra 148,242,13312
Mvav 88,152,204
X 182
Mvav 120,152,212
K 202,180,181
Mvav 152,152,220
X 183
Mvav 156,156,220
Dc 245,8,61,0
Ra 152,242,14336
Mvav 124,156,212
X 184
Mvav 92,156,204
)"
R"(K 203,182,183
v_bfe_u32 v28, v1, 4, 4
Mvav 60,156,196
W 3
X 185
Cvaa 160,128,0,224
buffer_load_dwordx2 v[12:13], v6, s[8:11], 0 offen offset:3584 sc1
v_lshlrev_b32 v28, 3, v28
Mvaa 160,132,4
s_lshl_b32 s80, s56, 9
X 186
Cvaa 164,132,20,228
s_add_u32 s80, s80, 4480
Ra 156,242,15360
K 210,184,185
Mvaa 164,128,16
X 187
Cvaa 168,128,32,232
Dc 245,8,61,1024
Mvaa 168,132,36
X 188
Cvaa 172,132,52,236
K 211,186,187
Mvaa 172,128,48
X 189
Mvaa 160,136,8
Dc 245,8,61,2048
Ra 128,242,24576
Mvaa 160,140,12
X 190
Mvaa 164,140,28
Ra 132,242,25600
K 218,188,189
Mvaa 164,136,24
X 191
Mvaa 168,136,40
buffer_load_dwordx2 v[28:29], v28, s[8:11], s80 offen sc1
Mvaa 168,140,44
Dc 245,8,61,3072
s_add_u32 s61, s61, s63
s_xor_b32 s62, s62, 0x10000
s_add_u32 s64, s64, 1
Mvaa 172,140,60
X 160
Mvaa 172,136,56
W 2
Ra 136,242,26624
K 219,190,191
Cvaa 176,144,0,224
X 161
Mvaa 176,148,4
global_load_dword v3, v2, s[26:27]
v_add_u32 v2, s22, v2
Cvaa 180,148,20,228
Ra 140,242,27648
Mvaa 180,144,16
X 162
Cvaa 184,144,32,232
K 196,160,161
Mvaa 184,148,36
X 163
Cvaa 188,148,52,236
X 164
Mvaa 188,144,48
Ra 144,242,28672
K 197,162,163
Mvaa 176,152,8
X 165
Mvaa 176,156,12
Ra 148,242,29696
Mvaa 180,156,28
X 166
Mvaa 180,152,24
K 204,164,165
Mvaa 184,152,40
X 167
Mvaa 184,156,44
X 168
Mvaa 188,156,60
Mvaa 188,152,56
W 2
Ra 152,242,30720
K 205,166,167
Mvav 32,128,192
X 169
Mvav 64,128,200
Ra 156,242,31744
v_xor_b32 v242, 0x10000, v242
Mvav 96,128,208
X 170
Mvav 128,128,216
K 212,168,169
Mvav 132,132,216
X 171
Mvav 100,132,208
X 172
Mvav 68,132,200
Mvav 36,132,192
K 213,170,171
Mvav 40,136,192
X 173
Mvav 72,136,200
X 174
Mvav 104,136,208
Mvav 136,136,216
s_waitcnt vmcnt(2)
v_cmp_eq_u64 vcc, s[12:13], v[28:29]
s_cmp_eq_u64 vcc, -1
s_cbranch_scc0 .Lspin_70%=
.Lok_71%=:
v_cmp_eq_u64 vcc, s[12:13], v[8:9]
v_cmp_eq_u64_e64 s[86:87], s[12:13], v[10:11]
s_and_b64 vcc, vcc, s[86:87]
s_or_b64 vcc, vcc, s[20:21]
s_cmp_eq_u64 vcc, -1
s_cbranch_scc0 .Lspin_73%=
.Lok_74%=:
K 220,172,173
Mvav 140,140,216
X 175
Mvav 108,140,208
X 176
Mvav 76,140,200
s_waitcnt vmcnt(1) lgkmcnt(0)
s_barrier
Mvav 44,140,192
Ra 128,242,0
K 221,174,175
Mvav 48,144,192
X 177
Mvav 80,144,200
X 178
Mvav 112,144,208
Ra 132,242,1024
Mvav 144,144,216
K 198,176,177
Mvav 148,148,216
X 179
Mvav 116,148,208
X 180
Mvav 84,148,200
Ra 136,242,2048
Mvav 52,148,192
K 199,178,179
Mvav 56,152,192
X 181
Mvav 88,152,200
Ra 140,242,3072
Mvav 120,152,208
X 182
Mvav 152,152,216
K 206,180,181
Mvav 156,156,216
X 183
Mvav 124,156,208
Mvav 92,156,200
K 207,182,183
Mvav 60,156,192
s_mov_b64 exec, 0
s_cmp_eq_u32 s14, 2
s_cbranch_scc1 .Lh_4_2%=
s_branch .Lh_4_3%=
.Lhr_76%=:
s_mov_b64 exec, -1
W 0
Cvaa 160,128,0,224
Ra 144,242,4096
Ra 148,242,5120
Mvaa 160,132,4
s_add_u32 m0, s62, 0
Ra 152,242,6144
Cvaa 164,132,20,228
X 184
Mvaa 164,128,16
X 185
Cvaa 168,128,32,232
X 186
Mvaa 168,132,36
Ra 156,242,7168
K 214,184,185
Cvaa 172,132,52,236
Dc 1,8,61,0
Mvaa 172,128,48
X 187
Mvaa 160,136,8
X 188
Mvaa 160,140,12
Ra 128,242,16384
K 215,186,187
Mvaa 164,140,28
Dc 1,8,61,1024
Mvaa 164,136,24
X 189
Mvaa 168,136,40
X 190
Mvaa 168,140,44
Ra 132,242,17408
K 222,188,189
Mvaa 172,140,60
Dc 1,8,61,2048
Mvaa 172,136,56
X 191
v_smfmac_f32_16x16x64_bf16 a[160:163], a[176:179], v[192:199], v240
Ra 136,242,18432
K 223,190,191
v_smfmac_f32_16x16x64_bf16 a[164:167], a[176:179], v[200:207], v240
X 160
v_smfmac_f32_16x16x64_bf16 a[168:171], a[176:179], v[208:215], v240
Dc 1,8,61,3072
v_smfmac_f32_16x16x64_bf16 a[172:175], a[176:179], v[216:223], v240
W 3
X 161
Cvaa 176,144,0,224
s_add_u32 m0, s62, 4096
X 162
Mvaa 176,148,4
Ra 140,242,19456
K 192,160,161
Cvaa 180,148,20,228
X 163
Mvaa 180,144,16
Dc 243,8,61,0
Cvaa 184,144,32,232
X 164
Mvaa 184,148,36
K 193,162,163
Cvaa 188,148,52,236
X 165
Mvaa 188,144,48
Dc 243,8,61,1024
Ra 144,242,20480
Mvaa 176,152,8
X 166
Mvaa 176,156,12
Ra 148,242,21504
K 200,164,165
Mvaa 180,156,28
X 167
Mvaa 180,152,24
Dc 243,8,61,2048
Mvaa 184,152,40
X 168
Mvaa 184,156,44
K 201,166,167
Mvaa 188,156,60
X 169
Mvaa 188,152,56
W 2
Dc 243,8,61,3072
Ra 152,242,22528
Mvav 32,128,196
s_add_u32 m0, s62, 8192
X 170
Mvav 64,128,204
Ra 156,242,23552
K 208,168,169
Mvav 96,128,212
X 171
Mvav 128,128,220
Dc 244,8,61,0
Mvav 132,132,220
X 172
Mvav 100,132,212
Ra 128,242,8192
K 209,170,171
Mvav 68,132,204
X 173
Mvav 36,132,196
Dc 244,8,61,1024
Mvav 40,136,196
X 174
Mvav 72,136,204
Ra 132,242,9216
K 216,172,173
Mvav 104,136,212
X 175
Mvav 136,136,220
Dc 244,8,61,2048
Mvav 140,140,220
X 176
Mvav 108,140,212
Ra 136,242,10240
K 217,174,175
Mvav 76,140,204
X 177
Mvav 44,140,196
W 3
Dc 244,8,61,3072
Mvav 48,144,196
X 178
Mvav 80,144,204
s_add_u32 m0, s62, 12288
Ra 140,242,11264
Mvav 112,144,212
K 194,176,177
Mvav 144,144,220
X 179
Mvav 148,148,220
X 180
Mvav 116,148,212
Dc 245,8,61,0
Ra 144,242,12288
Mvav 84,148,204
K 195,178,179
Mvav 52,148,196
X 181
Mvav 56,152,196
X 182
Mvav 88,152,204
Dc 245,8,61,1024
Ra 148,242,13312
Mvav 120,152,212
v_xor_b32 v6, s12, v12
K 202,180,181
Mvav 152,152,220
X 183
Mvav 156,156,220
Ra 152,242,14336
v_and_b32 v6, 2047, v6
Mvav 124,156,212
Dc 245,8,61,2048
v_lshlrev_b32 v6, 3, v6
Mvav 92,156,204
X 184
Mvav 60,156,196
W 3
K 203,182,183
Cvaa 160,128,0,224
s_mov_b32 s81, 40960
X 185
Mvaa 160,132,4
Dc 245,8,61,3072
Ra 156,242,15360
Cvaa 164,132,20,228
s_add_u32 s61, s61, s63
X 186
Mvaa 164,128,16
K 210,184,185
Cvaa 168,128,32,232
X 187
Mvaa 168,132,36
buffer_load_dword v7, v6, s[8:11], s81 offen sc1
v_lshrrev_b32 v5, 7, v1
Cvaa 172,132,52,236
s_xor_b32 s62, s62, 0x10000
X 188
Mvaa 172,128,48
K 211,186,187
v_mul_u32_u24 v4, 20480, v5
Mvaa 160,136,8
X 189
Mvaa 160,140,12
s_add_u32 s64, s64, 1
Ra 128,242,24576
Ra 132,242,25600
Mvaa 164,140,28
X 190
Mvaa 164,136,24
v_and_b32 v6, 0x70, v1
K 218,188,189
Mvaa 168,136,40
X 191
Mvaa 168,140,44
v_add_u32 v4, v4, v6
Mvaa 172,140,60
X 160
Mvaa 172,136,56
W 2
v_add_u32 v5, 2, v5
K 219,190,191
Cvaa 176,144,0,224
X 161
Mvaa 176,148,4
Ra 136,242,26624
v_lshrrev_b32 v5, 3, v5
Cvaa 180,148,20,228
Ra 140,242,27648
v_lshl_or_b32 v5, v5, 31, v4
Mvaa 180,144,16
X 162
Cvaa 184,144,32,232
K 196,160,161
Mvaa 184,148,36
s_lshl_b32 s19, s85, 7
X 163
Cvaa 188,148,52,236
X 164
Mvaa 188,144,48
s_add_u32 s19, s19, 2752512
Ra 144,242,28672
K 197,162,163
Mvaa 176,152,8
X 165
Mvaa 176,156,12
s_and_b32 s81, s20, 134217728
Ra 148,242,29696
Mvaa 180,156,28
.Lh_3_2_1%=:
s_cbranch_execz .Lhr_66%=
X 166
)"
R"(Mvaa 180,152,24
K 204,164,165
Mvaa 184,152,40
s_or_b32 s19, s19, s81
X 167
Mvaa 184,156,44
X 168
Mvaa 188,156,60
buffer_load_dwordx4 a[64:67], v4, s[8:11], s19 offen sc1
Mvaa 188,152,56
W 2
Ra 152,242,30720
K 205,166,167
Mvav 32,128,192
s_add_u32 s19, s19, 163840
X 169
Mvav 64,128,200
Ra 156,242,31744
v_xor_b32 v242, 0x10000, v242
Mvav 96,128,208
X 170
Mvav 128,128,216
buffer_load_dwordx4 a[68:71], v4, s[8:11], s19 offen sc1
K 212,168,169
Mvav 132,132,216
X 171
Mvav 100,132,208
s_add_u32 s19, s19, 163840
X 172
Mvav 68,132,200
Mvav 36,132,192
buffer_load_dwordx4 a[72:75], v4, s[8:11], s19 offen sc1
K 213,170,171
Mvav 40,136,192
X 173
Mvav 72,136,200
X 174
Mvav 104,136,208
s_add_u32 s19, s19, 163840
Mvav 136,136,216
K 220,172,173
Mvav 140,140,216
X 175
Mvav 108,140,208
buffer_load_dwordx4 a[76:79], v4, s[8:11], s19 offen sc1
Mvav 76,140,200
s_waitcnt vmcnt(5) lgkmcnt(0)
s_barrier
Mvav 44,140,192
s_add_u32 s19, s19, 163840
X 176
Mvav 48,144,192
Ra 128,242,0
K 221,174,175
Mvav 80,144,200
X 177
Mvav 112,144,208
buffer_load_dwordx4 a[80:83], v4, s[8:11], s19 offen sc1
Ra 132,242,1024
Mvav 144,144,216
s_add_u32 s19, s19, 163840
X 178
Mvav 148,148,216
K 198,176,177
Mvav 116,148,208
X 179
Mvav 84,148,200
buffer_load_dwordx4 a[84:87], v4, s[8:11], s19 offen sc1
Ra 136,242,2048
Mvav 52,148,192
X 180
Mvav 56,152,192
s_add_u32 s19, s19, 163840
Ra 140,242,3072
K 199,178,179
Mvav 88,152,200
X 181
Mvav 120,152,208
X 182
Mvav 152,152,216
buffer_load_dwordx4 a[88:91], v5, s[8:11], s19 offen sc1
K 206,180,181
Mvav 156,156,216
X 183
Mvav 124,156,208
Mvav 92,156,200
K 207,182,183
Mvav 60,156,192
W 0
Cvaa 160,128,0,224
Ra 144,242,4096
Ra 148,242,5120
Mvaa 160,132,4
s_add_u32 m0, s62, 0
Ra 152,242,6144
Cvaa 164,132,20,228
X 184
Mvaa 164,128,16
X 185
Cvaa 168,128,32,232
X 186
Mvaa 168,132,36
Ra 156,242,7168
K 214,184,185
Cvaa 172,132,52,236
Dc 1,8,61,0
Mvaa 172,128,48
X 187
Mvaa 160,136,8
X 188
Mvaa 160,140,12
Ra 128,242,16384
K 215,186,187
Mvaa 164,140,28
Dc 1,8,61,1024
Mvaa 164,136,24
X 189
Mvaa 168,136,40
X 190
Mvaa 168,140,44
Ra 132,242,17408
K 222,188,189
Mvaa 172,140,60
Dc 1,8,61,2048
Mvaa 172,136,56
X 191
v_smfmac_f32_16x16x64_bf16 a[160:163], a[176:179], v[192:199], v240
Ra 136,242,18432
K 223,190,191
v_smfmac_f32_16x16x64_bf16 a[164:167], a[176:179], v[200:207], v240
X 160
v_smfmac_f32_16x16x64_bf16 a[168:171], a[176:179], v[208:215], v240
Dc 1,8,61,3072
v_smfmac_f32_16x16x64_bf16 a[172:175], a[176:179], v[216:223], v240
W 3
X 161
Cvaa 176,144,0,224
s_add_u32 m0, s62, 4096
X 162
Mvaa 176,148,4
Ra 140,242,19456
K 192,160,161
Cvaa 180,148,20,228
X 163
Mvaa 180,144,16
Dc 243,8,61,0
Cvaa 184,144,32,232
X 164
Mvaa 184,148,36
K 193,162,163
Cvaa 188,148,52,236
X 165
Mvaa 188,144,48
Dc 243,8,61,1024
Ra 144,242,20480
Mvaa 176,152,8
X 166
Mvaa 176,156,12
Ra 148,242,21504
K 200,164,165
Mvaa 180,156,28
X 167
Mvaa 180,152,24
Dc 243,8,61,2048
Mvaa 184,152,40
X 168
Mvaa 184,156,44
K 201,166,167
Mvaa 188,156,60
X 169
Mvaa 188,152,56
W 2
Dc 243,8,61,3072
Ra 152,242,22528
Mvav 32,128,196
s_add_u32 m0, s62, 8192
X 170
Mvav 64,128,204
Ra 156,242,23552
K 208,168,169
Mvav 96,128,212
X 171
Mvav 128,128,220
Dc 244,8,61,0
Mvav 132,132,220
X 172
Mvav 100,132,212
Ra 128,242,8192
K 209,170,171
Mvav 68,132,204
X 173
Mvav 36,132,196
Dc 244,8,61,1024
Mvav 40,136,196
X 174
Mvav 72,136,204
Ra 132,242,9216
K 216,172,173
Mvav 104,136,212
X 175
Mvav 136,136,220
Dc 244,8,61,2048
Mvav 140,140,220
s_bfe_u32 s81, s59, 0x10001
X 176
Mvav 108,140,212
s_lshl_b32 s82, s57, 1
Ra 136,242,10240
K 217,174,175
Mvav 76,140,204
s_add_u32 s81, s81, s82
X 177
Mvav 44,140,196
W 3
s_lshl_b32 s83, s81, 2
Dc 244,8,61,3072
Mvav 48,144,196
s_lshr_b32 s81, s59, 2
X 178
Mvav 80,144,204
s_add_u32 m0, s62, 12288
s_lshl_b32 s81, s81, 1
Ra 140,242,11264
Mvav 112,144,212
s_add_u32 s83, s83, s81
K 194,176,177
Mvav 144,144,220
s_and_b32 s81, s59, 1
X 179
Mvav 148,148,220
s_add_u32 s83, s83, s81
X 180
Mvav 116,148,212
s_and_b32 s81, s14, 1
Dc 245,8,61,0
Ra 144,242,12288
Mvav 84,148,204
s_mul_i32 s81, s81, 124
K 195,178,179
Mvav 52,148,196
s_xor_b32 s83, s83, s81
X 181
Mvav 56,152,196
s_mul_i32 s83, s83, 20480
X 182
Mvav 88,152,204
s_mul_i32 s81, s58, 320
Dc 245,8,61,1024
Ra 148,242,13312
Mvav 120,152,212
s_add_u32 s82, s83, s81
K 202,180,181
Mvav 152,152,220
X 183
Mvav 156,156,220
Ra 152,242,14336
v_bfe_u32 v246, v1, 4, 4
Mvav 124,156,212
Dc 245,8,61,2048
v_mul_u32_u24 v246, 1280, v246
Mvav 92,156,204
X 184
Mvav 60,156,196
W 3
v_lshrrev_b32 v247, 8, v1
K 203,182,183
Cvaa 160,128,0,224
v_mul_u32_u24 v247, 104, v247
v_add3_u32 v246, v246, v247, s82
Mvaa 160,132,4
X 185
Cvaa 164,132,20,228
Dc 245,8,61,3072
Ra 156,242,15360
Mvaa 164,128,16
s_add_u32 s61, s61, s63
X 186
Cvaa 168,128,32,232
K 210,184,185
Mvaa 168,132,36
X 187
Cvaa 172,132,52,236
s_xor_b32 s62, s62, 0x10000
X 188
Mvaa 172,128,48
global_load_dword v3, v246, s[16:17]
K 211,186,187
Mvaa 160,136,8
s_lshl_b32 s81, s81, 3
s_add_u32 s83, s83, s81
s_add_u32 s83, s83, 131072
Mvaa 160,140,12
s_add_u32 s64, s64, 1
v_lshrrev_b32 v246, 9, v1
v_mul_u32_u24 v246, 10240, v246
Mvaa 164,140,28
Ra 128,242,24576
v_and_b32 v247, 0x1f0, v1
Mvaa 164,136,24
X 189
Mvaa 168,136,40
Ra 132,242,25600
v_min_u32 v247, 304, v247
Mvaa 168,140,44
X 190
Mvaa 172,140,60
v_lshl_add_u32 v246, v247, 3, v246
K 218,188,189
Mvaa 172,136,56
W 2
buffer_load_dword v3, v246, s[8:11], s83 offen sc1
Ra 136,242,26624
Cvaa 176,144,0,224
X 191
Mvaa 176,148,4
X 160
Cvaa 180,148,20,228
Ra 140,242,27648
K 219,190,191
Mvaa 180,144,16
X 161
Cvaa 184,144,32,232
X 162
Mvaa 184,148,36
K 196,160,161
Cvaa 188,148,52,236
X 163
Mvaa 188,144,48
X 164
Mvaa 176,152,8
Ra 144,242,28672
K 197,162,163
Mvaa 176,156,12
X 165
Mvaa 180,156,28
Ra 148,242,29696
Mvaa 180,152,24
X 166
Mvaa 184,152,40
K 204,164,165
Mvaa 184,156,44
X 167
Mvaa 188,156,60
X 168
Mvaa 188,152,56
W 2
Ra 152,242,30720
K 205,166,167
Mvav 32,128,192
X 169
Mvav 64,128,200
Ra 156,242,31744
v_xor_b32 v242, 0x10000, v242
Mvav 96,128,208
X 170
Mvav 128,128,216
K 212,168,169
Mvav 132,132,216
X 171
Mvav 100,132,208
X 172
Mvav 68,132,200
Mvav 36,132,192
K 213,170,171
Mvav 40,136,192
X 173
Mvav 72,136,200
X 174
Mvav 104,136,208
Mvav 136,136,216
K 220,172,173
Mvav 140,140,216
X 175
Mvav 108,140,208
X 176
Mvav 76,140,200
s_waitcnt vmcnt(2) lgkmcnt(0)
s_barrier
Mvav 44,140,192
Ra 128,242,0
K 221,174,175
Mvav 48,144,192
X 177
Mvav 80,144,200
X 178
Mvav 112,144,208
Ra 132,242,1024
Mvav 144,144,216
K 198,176,177
Mvav 148,148,216
X 179
Mvav 116,148,208
X 180
Mvav 84,148,200
Ra 136,242,2048
Mvav 52,148,192
K 199,178,179
Mvav 56,152,192
X 181
Mvav 88,152,200
)"
R"(Ra 140,242,3072
Mvav 120,152,208
X 182
Mvav 152,152,216
K 206,180,181
Mvav 156,156,216
X 183
Mvav 124,156,208
Mvav 92,156,200
K 207,182,183
Mvav 60,156,192
W 0
Cvaa 160,128,0,224
Ra 144,242,4096
Ra 148,242,5120
Mvaa 160,132,4
s_add_u32 m0, s62, 0
Ra 152,242,6144
G 8,64
Cvaa 164,132,20,228
X 184
Mvaa 164,128,16
X 185
Cvaa 168,128,32,232
X 186
Mvaa 168,132,36
G 9,65
K 214,184,185
Cvaa 172,132,52,236
Ra 156,242,7168
Dc 1,8,61,0
Mvaa 172,128,48
X 187
Mvaa 160,136,8
X 188
Mvaa 160,140,12
Ra 128,242,16384
G 10,66
Mvaa 164,140,28
Dc 1,8,61,1024
K 215,186,187
Mvaa 164,136,24
X 189
Mvaa 168,136,40
Ra 132,242,17408
G 11,67
Mvaa 168,140,44
X 190
Mvaa 172,140,60
Dc 1,8,61,2048
K 222,188,189
Mvaa 172,136,56
X 191
v_smfmac_f32_16x16x64_bf16 a[160:163], a[176:179], v[192:199], v240
G 12,68
K 223,190,191
v_smfmac_f32_16x16x64_bf16 a[164:167], a[176:179], v[200:207], v240
Ra 136,242,18432
v_smfmac_f32_16x16x64_bf16 a[168:171], a[176:179], v[208:215], v240
Dc 1,8,61,3072
G 13,69
v_smfmac_f32_16x16x64_bf16 a[172:175], a[176:179], v[216:223], v240
W 3
X 160
Cvaa 176,144,0,224
s_add_u32 m0, s62, 4096
X 161
Mvaa 176,148,4
X 162
Cvaa 180,148,20,228
Ra 140,242,19456
K 192,160,161
Mvaa 180,144,16
Dc 243,8,61,0
G 14,70
Cvaa 184,144,32,232
X 163
Mvaa 184,148,36
X 164
Cvaa 188,148,52,236
G 15,71
K 193,162,163
Mvaa 188,144,48
Dc 243,8,61,1024
Ra 144,242,20480
Mvaa 176,152,8
X 165
Mvaa 176,156,12
X 166
Mvaa 180,156,28
Ra 148,242,21504
A 8,8,12
Mvaa 180,152,24
Dc 243,8,61,2048
K 200,164,165
Mvaa 184,152,40
X 167
Mvaa 184,156,44
A 9,9,13
Mvaa 188,156,60
X 168
Mvaa 188,152,56
W 2
Dc 243,8,61,3072
K 201,166,167
Mvav 32,128,196
s_add_u32 m0, s62, 8192
X 169
Mvav 64,128,204
Ra 152,242,22528
A 10,10,14
Mvav 96,128,212
X 170
Mvav 128,128,220
Ra 156,242,23552
K 208,168,169
Mvav 132,132,220
Dc 244,8,61,0
A 11,11,15
Mvav 100,132,212
X 171
Mvav 68,132,204
X 172
Mvav 36,132,196
Ra 128,242,8192
K 209,170,171
Mvav 40,136,196
Dc 244,8,61,1024
G 12,72
Mvav 72,136,204
X 173
Mvav 104,136,212
X 174
Mvav 136,136,220
Ra 132,242,9216
G 13,73
Mvav 140,140,220
Dc 244,8,61,2048
K 216,172,173
Mvav 108,140,212
X 175
Mvav 76,140,204
X 176
Mvav 44,140,196
W 2
Ra 136,242,10240
G 14,74
Mvav 48,144,196
Dc 244,8,61,3072
K 217,174,175
Mvav 80,144,204
s_add_u32 m0, s62, 12288
X 177
Mvav 112,144,212
Ra 140,242,11264
G 15,75
Mvav 144,144,220
X 178
Mvav 148,148,220
K 194,176,177
A 8,8,12
Mvav 116,148,212
X 179
Mvav 84,148,204
X 180
Mvav 52,148,196
Dc 245,8,61,0
Ra 144,242,12288
Mvav 56,152,196
K 195,178,179
A 9,9,13
Mvav 88,152,204
X 181
Mvav 120,152,212
X 182
Mvav 152,152,220
Dc 245,8,61,1024
Ra 148,242,13312
Mvav 156,156,220
K 202,180,181
A 10,10,14
Mvav 124,156,212
X 183
Mvav 92,156,204
X 184
Mvav 60,156,196
W 2
Dc 245,8,61,2048
Ra 152,242,14336
Cvaa 160,128,0,224
.Lh_4_2_1%=:
s_cbranch_execz .Lh_4_2_2%=
A 11,11,15
K 203,182,183
Mvaa 160,132,4
X 185
Cvaa 164,132,20,228
X 186
Mvaa 164,128,16
Dc 245,8,61,3072
Ra 156,242,15360
Cvaa 168,128,32,232
s_add_u32 s61, s61, s63
G 12,76
K 210,184,185
Mvaa 168,132,36
X 187
Cvaa 172,132,52,236
s_xor_b32 s62, s62, 0x10000
G 13,77
Mvaa 172,128,48
X 188
Mvaa 160,136,8
K 211,186,187
G 14,78
Mvaa 160,140,12
s_add_u32 s64, s64, 1
X 189
Mvaa 164,140,28
Ra 128,242,24576
Ra 132,242,25600
Mvaa 164,136,24
X 190
Mvaa 168,136,40
K 218,188,189
G 15,79
Mvaa 168,140,44
X 191
Mvaa 172,140,60
s_lshl_b32 s92, s58, 2
X 160
Mvaa 172,136,56
W 2
K 219,190,191
A 8,8,12
Cvaa 176,144,0,224
X 161
Mvaa 176,148,4
Ra 136,242,26624
Ra 140,242,27648
Cvaa 180,148,20,228
s_add_u32 s91, s92, s51
X 162
Mvaa 180,144,16
A 9,9,13
K 196,160,161
Cvaa 184,144,32,232
X 163
Mvaa 184,148,36
s_add_u32 s95, s92, 0
X 164
Cvaa 188,148,52,236
A 10,10,14
K 197,162,163
Mvaa 188,144,48
X 165
Mvaa 176,152,8
s_and_b32 s95, s95, 15
Ra 144,242,28672
A 11,11,15
Mvaa 176,156,12
Ra 148,242,29696
Mvaa 180,156,28
X 166
Mvaa 180,152,24
s_mul_i32 s95, s95, 40960
K 204,164,165
Mvaa 184,152,40
X 167
Mvaa 184,156,44
G 12,80
Mvaa 188,156,60
s_mul_i32 s71, s51, 0
X 168
Mvaa 188,152,56
W 2
K 205,166,167
G 13,81
Mvav 32,128,192
X 169
Mvav 64,128,200
s_add_u32 s71, s71, 133120
Ra 152,242,30720
Ra 156,242,31744
Mvav 96,128,208
X 170
Mvav 128,128,216
v_xor_b32 v242, 0x10000, v242
G 14,82
Mvav 132,132,216
s_add_u32 m0, s71, s48
K 212,168,169
Mvav 100,132,208
X 171
Mvav 68,132,200
X 172
Mvav 36,132,192
s_lshl2_add_u32 s94, s48, s95
G 15,83
K 213,170,171
Mvav 40,136,192
X 173
Mvav 72,136,200
X 174
Mvav 104,136,208
D 16,40,94,0
A 8,8,12
Mvav 136,136,216
K 220,172,173
Mvav 140,140,216
X 175
Mvav 108,140,208
A 9,9,13
Mvav 76,140,200
s_waitcnt vmcnt(1) lgkmcnt(0)
s_barrier
Mvav 44,140,192
D 17,40,94,1024
Ra 128,242,0
Mvav 48,144,192
X 176
Mvav 80,144,200
K 221,174,175
A 10,10,14
Mvav 112,144,208
X 177
Mvav 144,144,216
D 18,40,94,2048
Ra 132,242,1024
Mvav 148,148,216
X 178
Mvav 116,148,208
K 198,176,177
A 11,11,15
Mvav 84,148,200
X 179
Mvav 52,148,192
D 19,40,94,3072
Ra 136,242,2048
Mvav 56,152,192
s_add_u32 m0, m0, 4096
X 180
Mvav 88,152,200
G 12,84
K 199,178,179
Mvav 120,152,208
X 181
Mvav 152,152,216
Ra 140,242,3072
buffer_load_dwordx4 v20, s[40:43], s94 offen lds
Mvav 156,156,216
X 182
Mvav 124,156,208
K 206,180,181
Mvav 92,156,200
X 183
Mvav 60,156,192
K 207,182,183
W 0
Cvaa 160,128,0,224
Ra 144,242,4096
Ra 148,242,5120
Mvaa 160,132,4
s_add_u32 m0, s62, 0
Ra 152,242,6144
G 13,85
Cvaa 164,132,20,228
X 184
Mvaa 164,128,16
X 185
Cvaa 168,128,32,232
X 186
Mvaa 168,132,36
G 14,86
K 214,184,185
Cvaa 172,132,52,236
Ra 156,242,7168
Dc 1,8,61,0
Mvaa 172,128,48
X 187
Mvaa 160,136,8
X 188
Mvaa 160,140,12
Ra 128,242,16384
G 15,87
Mvaa 164,140,28
Dc 1,8,61,1024
K 215,186,187
Mvaa 164,136,24
X 189
Mvaa 168,136,40
Ra 132,242,17408
A 8,8,12
Mvaa 168,140,44
X 190
Mvaa 172,140,60
Dc 1,8,61,2048
K 222,188,189
Mvaa 172,136,56
X 191
)"
R"(v_smfmac_f32_16x16x64_bf16 a[160:163], a[176:179], v[192:199], v240
A 9,9,13
K 223,190,191
v_smfmac_f32_16x16x64_bf16 a[164:167], a[176:179], v[200:207], v240
Ra 136,242,18432
A 10,10,14
v_smfmac_f32_16x16x64_bf16 a[168:171], a[176:179], v[208:215], v240
Dc 1,8,61,3072
v_smfmac_f32_16x16x64_bf16 a[172:175], a[176:179], v[216:223], v240
W 3
X 160
Cvaa 176,144,0,224
s_add_u32 m0, s62, 4096
X 161
Mvaa 176,148,4
X 162
Cvaa 180,148,20,228
Ra 140,242,19456
A 11,11,15
Mvaa 180,144,16
Dc 243,8,61,0
K 192,160,161
Cvaa 184,144,32,232
X 163
Mvaa 184,148,36
X 164
Cvaa 188,148,52,236
G 12,88
K 193,162,163
Mvaa 188,144,48
Dc 243,8,61,1024
G 13,89
Mvaa 176,152,8
X 165
Mvaa 176,156,12
X 166
Mvaa 180,156,28
Ra 144,242,20480
Ra 148,242,21504
Mvaa 180,152,24
Dc 243,8,61,2048
K 200,164,165
Mvaa 184,152,40
X 167
Mvaa 184,156,44
G 14,90
Mvaa 188,156,60
X 168
Mvaa 188,152,56
W 2
Dc 243,8,61,3072
K 201,166,167
Mvav 32,128,196
s_add_u32 m0, s62, 8192
Ra 152,242,22528
G 15,91
Mvav 64,128,204
X 169
Mvav 96,128,212
X 170
Mvav 128,128,220
Ra 156,242,23552
A 8,8,12
Mvav 132,132,220
Dc 244,8,61,0
K 208,168,169
Mvav 100,132,212
X 171
Mvav 68,132,204
X 172
Mvav 36,132,196
Ra 128,242,8192
A 9,9,13
Mvav 40,136,196
Dc 244,8,61,1024
K 209,170,171
Mvav 72,136,204
X 173
Mvav 104,136,212
Ra 132,242,9216
A 10,10,14
Mvav 136,136,220
X 174
Mvav 140,140,220
Dc 244,8,61,2048
K 216,172,173
Mvav 108,140,212
X 175
Mvav 76,140,204
Ra 136,242,10240
A 11,11,15
Mvav 44,140,196
W 3
X 176
Mvav 48,144,196
Dc 244,8,61,3072
K 217,174,175
Mvav 80,144,204
s_add_u32 m0, s62, 12288
Ra 140,242,11264
v_add_f32_dpp v8, v8, v8 row_ror:8 row_mask:0xf bank_mask:0xf
Mvav 112,144,212
X 177
Mvav 144,144,220
X 178
Mvav 148,148,220
K 194,176,177
v_add_f32_dpp v9, v9, v9 row_ror:8 row_mask:0xf bank_mask:0xf
Mvav 116,148,212
X 179
Mvav 84,148,204
X 180
Mvav 52,148,196
Dc 245,8,61,0
Ra 144,242,12288
Mvav 56,152,196
v_add_f32_dpp v10, v10, v10 row_ror:8 row_mask:0xf bank_mask:0xf
K 195,178,179
Mvav 88,152,204
X 181
Mvav 120,152,212
X 182
Mvav 152,152,220
Dc 245,8,61,1024
v_add_f32_dpp v11, v11, v11 row_ror:8 row_mask:0xf bank_mask:0xf
Mvav 156,156,220
Ra 148,242,13312
K 202,180,181
Mvav 124,156,212
X 183
Mvav 92,156,204
Ra 152,242,14336
v_mov_b32 v12, v8
Mvav 60,156,196
W 3
Dc 245,8,61,2048
v_mov_b32 v13, v9
Cvaa 160,128,0,224
X 184
Mvaa 160,132,4
Ra 156,242,15360
K 203,182,183
Cvaa 164,132,20,228
X 185
Mvaa 164,128,16
Dc 245,8,61,3072
v_mov_b32 v14, v10
Cvaa 168,128,32,232
s_add_u32 s61, s61, s63
X 186
Mvaa 168,132,36
K 210,184,185
v_mov_b32 v15, v11
Cvaa 172,132,52,236
s_xor_b32 s62, s62, 0x10000
X 187
Mvaa 172,128,48
X 188
Mvaa 160,136,8
Ra 128,242,24576
K 211,186,187
Mvaa 160,140,12
s_add_u32 s64, s64, 1
X 189
Mvaa 164,140,28
X 190
Mvaa 164,136,24
Ra 132,242,25600
v_permlane16_swap_b32 v8, v12
Mvaa 168,136,40
K 218,188,189
v_permlane16_swap_b32 v9, v13
Mvaa 168,140,44
X 191
Mvaa 172,140,60
s_lshl_b32 s92, s58, 2
X 160
Mvaa 172,136,56
W 2
K 219,190,191
v_permlane16_swap_b32 v10, v14
Cvaa 176,144,0,224
X 161
Mvaa 176,148,4
Ra 136,242,26624
Ra 140,242,27648
Cvaa 180,148,20,228
s_add_u32 s91, s92, s51
X 162
Mvaa 180,144,16
K 196,160,161
v_permlane16_swap_b32 v11, v15
Cvaa 184,144,32,232
X 163
Mvaa 184,148,36
s_add_u32 s95, s92, 1
X 164
Cvaa 188,148,52,236
A 8,8,12
K 197,162,163
Mvaa 188,144,48
X 165
Mvaa 176,152,8
s_and_b32 s95, s95, 15
Ra 144,242,28672
Ra 148,242,29696
Mvaa 176,156,12
X 166
Mvaa 180,156,28
A 9,9,13
K 204,164,165
Mvaa 180,152,24
s_mul_i32 s95, s95, 40960
X 167
Mvaa 184,152,40
Mvaa 184,156,44
A 10,10,14
Mvaa 188,156,60
s_mul_i32 s71, s51, 0
X 168
Mvaa 188,152,56
W 2
Ra 152,242,30720
K 205,166,167
Mvav 32,128,192
X 169
Mvav 64,128,200
s_add_u32 s71, s71, 143360
Ra 156,242,31744
A 11,11,15
Mvav 96,128,208
X 170
Mvav 128,128,216
v_xor_b32 v242, 0x10000, v242
K 212,168,169
Mvav 132,132,216
s_add_u32 m0, s71, s48
v_mov_b32 v12, v8
Mvav 100,132,208
X 171
Mvav 68,132,200
X 172
Mvav 36,132,192
s_lshl2_add_u32 s94, s48, s95
v_mov_b32 v13, v9
K 213,170,171
Mvav 40,136,192
X 173
Mvav 72,136,200
X 174
Mvav 104,136,208
D 16,40,94,0
v_mov_b32 v14, v10
Mvav 136,136,216
K 220,172,173
Mvav 140,140,216
X 175
Mvav 108,140,208
v_mov_b32 v15, v11
Mvav 76,140,200
s_waitcnt vmcnt(1) lgkmcnt(0)
s_barrier
Mvav 44,140,192
D 17,40,94,1024
Ra 128,242,0
Mvav 48,144,192
X 176
Mvav 80,144,200
K 221,174,175
v_permlane32_swap_b32 v8, v12
Mvav 112,144,208
X 177
Mvav 144,144,216
D 18,40,94,2048
Ra 132,242,1024
Mvav 148,148,216
X 178
Mvav 116,148,208
K 198,176,177
v_permlane32_swap_b32 v9, v13
Mvav 84,148,200
X 179
Mvav 52,148,192
D 19,40,94,3072
Ra 136,242,2048
Mvav 56,152,192
s_add_u32 m0, m0, 4096
X 180
Mvav 88,152,200
K 199,178,179
v_permlane32_swap_b32 v10, v14
Mvav 120,152,208
X 181
Mvav 152,152,216
Ra 140,242,3072
buffer_load_dwordx4 v20, s[40:43], s94 offen lds
Mvav 156,156,216
X 182
Mvav 124,156,208
K 206,180,181
Mvav 92,156,200
X 183
Mvav 60,156,192
K 207,182,183
W 0
Cvaa 160,128,0,224
Ra 144,242,4096
Ra 148,242,5120
Mvaa 160,132,4
s_add_u32 m0, s62, 0
Ra 152,242,6144
v_permlane32_swap_b32 v11, v15
Cvaa 164,132,20,228
X 184
Mvaa 164,128,16
X 185
Cvaa 168,128,32,232
X 186
Mvaa 168,132,36
A 8,8,12
K 214,184,185
Cvaa 172,132,52,236
Ra 156,242,7168
Dc 1,8,61,0
Mvaa 172,128,48
X 187
Mvaa 160,136,8
Ra 128,242,16384
A 9,9,13
Mvaa 160,140,12
X 188
Mvaa 164,140,28
Dc 1,8,61,1024
K 215,186,187
Mvaa 164,136,24
X 189
Mvaa 168,136,40
Ra 132,242,17408
A 10,10,14
Mvaa 168,140,44
X 190
Mvaa 172,140,60
Dc 1,8,61,2048
K 222,188,189
Mvaa 172,136,56
A 11,11,15
v_smfmac_f32_16x16x64_bf16 a[160:163], a[176:179], v[192:199], v240
X 191
v_smfmac_f32_16x16x64_bf16 a[164:167], a[176:179], v[200:207], v240
K 223,190,191
v_and_b32 v7, 0xffff0000, v7
v_smfmac_f32_16x16x64_bf16 a[168:171], a[176:179], v[208:215], v240
Dc 1,8,61,3072
Ra 136,242,18432
v_smfmac_f32_16x16x64_bf16 a[172:175], a[176:179], v[216:223], v240
W 3
X 160
Cvaa 176,144,0,224
s_add_u32 m0, s62, 4096
X 161
Mvaa 176,148,4
X 162
Cvaa 180,148,20,228
Ra 140,242,19456
v_ashrrev_i32 v6, 31, v7
Mvaa 180,144,16
Dc 243,8,61,0
K 192,160,161
Cvaa 184,144,32,232
X 163
Mvaa 184,148,36
v_not_b32 v6, v6
Cvaa 188,148,52,236
X 164
Mvaa 188,144,48
Dc 243,8,61,1024
K 193,162,163
Mvaa 176,152,8
X 165
Mvaa 176,156,12
Ra 144,242,20480
v_or_b32 v6, 0x80000000, v6
Mvaa 180,156,28
X 166
Mvaa 180,152,24
Ra 148,242,21504
v_xor_b32 v7, v7, v6
Mvaa 184,152,40
Dc 243,8,61,2048
K 200,164,165
Mvaa 184,156,44
X 167
Mvaa 188,156,60
X 168
Mvaa 188,152,56
W 2
v_and_b32 v7, 0xffff0000, v7
K 201,166,167
Mvav 32,128,196
.Lh_4_3_1%=:
)"
R"(s_cbranch_execz .Lhr_76%=
Dc 243,8,61,3072
v_mul_f32 v7, 0xbfb8aa3b, v7
Mvav 64,128,204
s_add_u32 m0, s62, 8192
X 169
Mvav 96,128,212
Ra 152,242,22528
Ra 156,242,23552
Mvav 128,128,220
X 170
Mvav 132,132,220
Dc 244,8,61,0
K 208,168,169
Mvav 100,132,212
X 171
Mvav 68,132,204
X 7
Mvav 36,132,196
X 172
Mvav 40,136,196
Ra 128,242,8192
K 209,170,171
Mvav 72,136,204
X 173
Mvav 104,136,212
Dc 244,8,61,1024
Ra 132,242,9216
Mvav 136,136,220
X 174
Mvav 140,140,220
v_add_f32 v7, 1.0, v7
K 216,172,173
Mvav 108,140,212
X 175
Mvav 76,140,204
Dc 244,8,61,2048
Ra 136,242,10240
Mvav 44,140,196
W 3
v_rcp_f32 v7, v7
Mvav 48,144,196
X 176
Mvav 80,144,204
Ra 140,242,11264
K 217,174,175
Mvav 112,144,212
Dc 244,8,61,3072
K 7,7,7
Mvav 144,144,220
s_add_u32 m0, s62, 12288
X 177
Mvav 148,148,220
X 178
Mvav 116,148,212
Ra 144,242,12288
K 194,176,177
Mvav 84,148,204
X 179
Mvav 52,148,196
X 180
Mvav 56,152,196
Dc 245,8,61,0
v_lshlrev_b32 v7, 16, v7
Mvav 88,152,204
K 195,178,179
K 12,8,9
Mvav 120,152,212
X 181
Mvav 152,152,220
X 182
Mvav 156,156,220
Dc 245,8,61,1024
Ra 148,242,13312
Mvav 124,156,212
K 202,180,181
K 13,10,11
Mvav 92,156,204
X 183
Mvav 60,156,196
W 2
X 184
Cvaa 160,128,0,224
Dc 245,8,61,2048
Ra 152,242,14336
Mvaa 160,132,4
K 203,182,183
v_lshlrev_b32 v8, 16, v12
Cvaa 164,132,20,228
X 185
Mvaa 164,128,16
X 186
Cvaa 168,128,32,232
Dc 245,8,61,3072
Ra 156,242,15360
Mvaa 168,132,36
s_add_u32 s61, s61, s63
v_and_b32 v9, 0xffff0000, v12
K 210,184,185
Cvaa 172,132,52,236
s_xor_b32 s62, s62, 0x10000
X 187
Mvaa 172,128,48
X 188
Mvaa 160,136,8
v_lshlrev_b32 v10, 16, v13
K 211,186,187
Mvaa 160,140,12
s_add_u32 s64, s64, 1
X 189
Mvaa 164,140,28
Ra 128,242,24576
v_and_b32 v11, 0xffff0000, v13
Mvaa 164,136,24
X 190
Mvaa 168,136,40
Ra 132,242,25600
K 218,188,189
Mvaa 168,140,44
X 191
Mvaa 172,140,60
s_lshl_b32 s92, s58, 2
U 8,8,7
U 9,9,7
Mvaa 172,136,56
W 2
X 160
Cvaa 176,144,0,224
Ra 136,242,26624
K 219,190,191
Mvaa 176,148,4
X 161
Cvaa 180,148,20,228
s_add_u32 s91, s92, s51
Ra 140,242,27648
U 10,10,7
Mvaa 180,144,16
X 162
Cvaa 184,144,32,232
K 196,160,161
Mvaa 184,148,36
s_add_u32 s95, s92, 2
X 163
Cvaa 188,148,52,236
U 11,11,7
Mvaa 188,144,48
X 164
Mvaa 176,152,8
s_and_b32 s95, s95, 15
Ra 144,242,28672
K 197,162,163
Mvaa 176,156,12
X 165
Mvaa 180,156,28
Ra 148,242,29696
K 12,8,9
Mvaa 180,152,24
s_mul_i32 s95, s95, 40960
X 166
Mvaa 184,152,40
K 204,164,165
K 14,10,11
Mvaa 184,156,44
X 167
Mvaa 188,156,60
s_mul_i32 s71, s51, 0
X 168
Mvaa 188,152,56
W 2
v_mov_b32 v13, s13
K 205,166,167
Mvav 32,128,192
X 169
Mvav 64,128,200
s_add_u32 s71, s71, 153600
Ra 152,242,30720
Ra 156,242,31744
Mvav 96,128,208
X 170
Mvav 128,128,216
v_mov_b32 v15, s13
v_xor_b32 v242, 0x10000, v242
Mvav 132,132,216
s_add_u32 m0, s71, s48
K 212,168,169
v_lshrrev_b32 v6, 7, v1
Mvav 100,132,208
X 171
Mvav 68,132,200
X 172
Mvav 36,132,192
s_lshl2_add_u32 s94, s48, s95
K 213,170,171
Mvav 40,136,192
X 173
Mvav 72,136,200
v_lshl_or_b32 v6, v6, 28, v1
Mvav 104,136,208
X 174
Mvav 136,136,216
s_lshl_b32 s19, s85, 7
D 16,40,94,0
K 220,172,173
Mvav 140,140,216
X 175
Mvav 108,140,208
X 176
Mvav 76,140,200
s_waitcnt vmcnt(1) lgkmcnt(0)
s_barrier
Mvav 44,140,192
s_add_u32 s19, s19, 98304
D 17,40,94,1024
K 221,174,175
Mvav 48,144,192
Ra 128,242,0
Mvav 80,144,200
s_and_b32 s21, s20, 134217728
X 177
Mvav 112,144,208
X 178
Mvav 144,144,216
D 18,40,94,2048
Ra 132,242,1024
Mvav 148,148,216
s_or_b32 s19, s19, s21
K 198,176,177
Mvav 116,148,208
X 179
Mvav 84,148,200
X 180
Mvav 52,148,192
D 19,40,94,3072
Ra 136,242,2048
Mvav 56,152,192
s_add_u32 m0, m0, 4096
Ra 140,242,3072
K 199,178,179
Mvav 88,152,200
X 181
Mvav 120,152,208
X 182
Mvav 152,152,216
buffer_store_dwordx4 v[12:15], v6, s[8:11], s19 offen sc1
K 206,180,181
Mvav 156,156,216
X 183
Mvav 124,156,208
Mvav 92,156,200
K 207,182,183
Mvav 60,156,192
buffer_load_dwordx4 v20, s[40:43], s94 offen lds
.Lf1_36%=:
W 0
Cvaa 160,128,0,224
v_bfe_u32 v28, v1, 4, 2
v_lshlrev_b32 v28, 3, v28
Mvaa 160,132,4
Ra 144,242,4096
Ra 148,242,5120
Cvaa 164,132,20,228
Ra 152,242,6144
buffer_load_dwordx2 v[28:29], v28, s[8:11], s54 offen sc1
Mvaa 164,128,16
s_lshl_b32 s92, s58, 2
s_add_u32 s91, s92, s51
s_add_u32 s95, s91, 3
Cvaa 168,128,32,232
s_and_b32 s95, s95, 15
s_mul_i32 s95, s95, 40960
s_mul_i32 s71, s51, 10240
Mvaa 168,132,36
s_add_u32 s71, s71, 0
s_add_u32 m0, s71, s48
s_lshl2_add_u32 s94, s48, s95
Cvaa 172,132,52,236
X 184
Mvaa 172,128,48
X 185
Mvaa 160,136,8
X 186
Mvaa 160,140,12
Ra 156,242,7168
K 214,184,185
Mvaa 164,140,28
X 187
Mvaa 164,136,24
D 16,40,94,0
Ra 128,242,16384
Mvaa 168,136,40
s_add_u32 s95, s91, 5
s_and_b32 s95, s95, 15
s_mul_i32 s95, s95, 40960
Mvaa 168,140,44
s_mul_i32 s71, s51, 10240
s_add_u32 s71, s71, 20480
Ra 132,242,17408
Mvaa 172,140,60
X 188
Mvaa 172,136,56
D 17,40,94,1024
K 215,186,187
v_smfmac_f32_16x16x64_bf16 a[160:163], a[176:179], v[192:199], v240
X 189
v_smfmac_f32_16x16x64_bf16 a[164:167], a[176:179], v[200:207], v240
X 190
K 222,188,189
X 191
s_nop 0
K 223,190,191
v_smfmac_f32_16x16x64_bf16 a[168:171], a[176:179], v[208:215], v240
D 18,40,94,2048
Ra 136,242,18432
v_smfmac_f32_16x16x64_bf16 a[172:175], a[176:179], v[216:223], v240
W 3
X 160
Cvaa 176,144,0,224
X 161
Mvaa 176,148,4
X 162
Cvaa 180,148,20,228
D 19,40,94,3072
K 192,160,161
Mvaa 180,144,16
s_add_u32 m0, m0, 4096
X 163
Cvaa 184,144,32,232
X 164
Mvaa 184,148,36
Ra 140,242,19456
K 193,162,163
Cvaa 188,148,52,236
buffer_load_dwordx4 v20, s[40:43], s94 offen lds
Mvaa 188,144,48
s_add_u32 m0, s71, s48
s_lshl2_add_u32 s94, s48, s95
Ra 144,242,20480
Mvaa 176,152,8
X 165
Mvaa 176,156,12
X 166
Mvaa 180,156,28
D 16,40,94,0
K 200,164,165
Mvaa 180,152,24
X 167
Mvaa 184,152,40
X 172
Mvaa 184,156,44
D 17,40,94,1024
K 201,166,167
Mvaa 188,156,60
X 173
Mvaa 188,152,56
s_cmp_eq_u32 s60, 16
s_cbranch_scc1 .Lmk_77%=
.Lmkr_78%=:
W 1
Mvav 32,128,196
Ra 148,242,21504
D 18,40,94,2048
Mvav 64,128,204
X 174
Mvav 96,128,212
Ra 152,242,22528
K 216,172,173
Mvav 128,128,220
X 175
Mvav 132,132,220
s_mov_b32 s18, 40960
X 168
Mvav 100,132,212
D 19,40,94,3072
K 217,174,175
Mvav 68,132,204
s_add_u32 m0, m0, 4096
X 169
Mvav 36,132,196
s_mov_b32 s19, 73728
X 170
Mvav 40,136,196
s_bitcmp1_b32 s14, 0
s_cselect_b32 s18, 49152, s18
s_cselect_b32 s19, 98304, s19
Ra 156,242,23552
K 208,168,169
Mvav 72,136,204
s_cmp_lt_u32 s14, 2
s_cselect_b32 s20, s18, s19
s_cselect_b32 s21, s19, s18
X 171
Mvav 104,136,212
X 184
Mvav 136,136,220
buffer_load_dwordx4 v20, s[40:43], s94 offen lds
K 209,170,171
Mvav 140,140,220
s_waitcnt vmcnt(10)
v_cmp_eq_u64 vcc, s[12:13], v[28:29]
s_cmp_eq_u64 vcc, -1
)"
R"(s_cselect_b32 s46, 79691776, 0
s_add_u32 s81, s58, 0
s_and_b32 s81, s81, 3
Mvav 108,140,212
s_lshl_b32 s81, s81, 12
s_add_u32 s81, s81, s49
Ra 128,242,24576
Mvav 76,140,204
X 185
Mvav 44,140,196
W 1
X 186
Mvav 48,144,196
buffer_load_dwordx4 a[184:187], v1, s[44:47], s81 offen offset:0 sc1
K 210,184,185
Mvav 80,144,204
s_add_u32 s82, s81, 131072
X 187
Mvav 112,144,212
v_add_u32 v2, s20, v1
v_add_u32 v3, s21, v1
Mvav 144,144,220
s_lshl_b32 s18, s14, 9
X 188
Mvav 148,148,220
Ra 132,242,25600
K 211,186,187
Mvav 116,148,212
buffer_load_dwordx4 a[200:203], v1, s[44:47], s82 offen offset:0 sc1
Ra 136,242,26624
Mvav 84,148,204
X 189
Mvav 52,148,196
v_lshrrev_b32 v232, 1, v1
v_add_u32 v232, s18, v232
Mvav 56,152,196
X 190
Mvav 88,152,204
K 218,188,189
v_bfe_u32 v12, v1, 6, 2
Mvav 120,152,212
buffer_load_dwordx4 a[188:191], v1, s[44:47], s81 offen offset:1024 sc1
Ra 140,242,27648
Mvav 152,152,220
X 191
Mvav 156,156,220
v_lshrrev_b32 v13, 8, v1
v_bfe_u32 v14, v1, 4, 1
Mvav 124,156,212
X 176
Mvav 92,156,204
K 219,190,191
v_lshlrev_b32 v14, 4, v14
Mvav 60,156,196
X 177
Maav 168,180,208
X 178
K 194,176,177
X 179
X 180
K 195,178,179
Maav 172,180,216
X 181
X 182
K 202,180,181
X 183
s_nop 0
K 203,182,183
Maav 160,180,192
buffer_load_dwordx4 a[204:207], v1, s[44:47], s82 offen offset:1024 sc1
v_mov_b32 v15, 0x3f80
Maav 164,180,200
W 0
Ra 144,242,28672
v_lshlrev_b32 v15, v14, v15
Mvav 96,128,208
Ra 148,242,29696
v_bfe_u32 v14, v1, 5, 1
v_xor_b32 v27, v12, v13
Mvav 128,128,216
buffer_load_dwordx4 a[192:195], v1, s[44:47], s81 offen offset:2048 sc1
v_lshl_or_b32 v27, v27, 1, v14
v_cmp_eq_u32_e64 s[86:87], 0, v27
Mvav 132,132,216
Ra 152,242,30720
Ra 156,242,31744
v_mov_b32 v226, 0
Mvav 100,132,208
v_cndmask_b32_e64 v224, 0, v15, s[86:87]
v_mov_b32 v228, 0
v_mov_b32 v230, v224
Mvav 104,136,208
buffer_load_dwordx4 a[208:211], v1, s[44:47], s82 offen offset:2048 sc1
v_cmp_eq_u32_e64 s[86:87], 1, v27
v_mov_b32 v227, 0
Mvav 136,136,216
v_cndmask_b32_e64 v225, 0, v15, s[86:87]
v_mov_b32 v229, 0
v_mov_b32 v231, v225
Mvav 140,140,216
G 4,160
G 8,161
G 5,164
Mvav 108,140,208
W 0
buffer_load_dwordx4 a[196:199], v1, s[44:47], s81 offen offset:3072 sc1
G 9,165
G 6,168
Mvav 112,144,208
G 10,169
G 7,172
G 11,173
Mvav 144,144,216
A 4,4,8
A 5,5,9
A 6,6,10
Mvav 148,148,216
buffer_load_dwordx4 a[212:215], v1, s[44:47], s82 offen offset:3072 sc1
s_add_u32 s81, s58, 1
s_and_b32 s81, s81, 3
s_lshl_b32 s81, s81, 12
Mvav 116,148,208
s_add_u32 s81, s81, s49
A 7,7,11
ds_write_b64 v232, v[6:7] offset:57344
A 8,4,5
Mvav 120,152,208
A 9,6,7
A 8,8,9
v_cmp_ngt_f32_e64 s[86:87], s90, v8
s_lshl_b32 s19, s14, 2
Mvav 152,152,216
buffer_load_dwordx4 a[216:219], v1, s[44:47], s81 offen offset:0 sc1
s_add_u32 s82, s81, 131072
v_mov_b32 v9, s19
s_or_b64 s[88:89], s[88:89], s[86:87]
Mvav 156,156,216
s_and_b64 s[88:89], s[88:89], s[24:25]
s_cselect_b32 s18, 1, 0
v_mov_b32 v8, s18
ds_write_b32 v9, v8 offset:59392
v_xor_b32 v10, 1024, v232
Mvav 124,156,208
v_bfe_u32 v11, v1, 2, 4
K 96,96,97
K 97,98,99
Mvav 32,128,192
buffer_load_dwordx4 a[232:235], v1, s[44:47], s82 offen offset:0 sc1
K 98,100,101
K 99,102,103
Mvav 64,128,200
ds_write_b128 v2, v[96:99] offset:0
K 128,128,129
K 129,130,131
Mvav 68,132,200
K 130,132,133
K 131,134,135
ds_write_b128 v2, v[128:131] offset:4096
Mvav 36,132,192
buffer_load_dwordx4 a[220:223], v1, s[44:47], s81 offen offset:1024 sc1
K 104,104,105
K 105,106,107
Mvav 40,136,192
K 106,108,109
K 107,110,111
ds_write_b128 v2, v[104:107] offset:1024
Mvav 72,136,200
K 136,136,137
K 137,138,139
K 138,140,141
Mvav 76,140,200
buffer_load_dwordx4 a[236:239], v1, s[44:47], s82 offen offset:1024 sc1
K 139,142,143
ds_write_b128 v2, v[136:139] offset:5120
Mvav 44,140,192
K 112,112,113
K 113,114,115
K 114,116,117
Mvav 48,144,192
K 115,118,119
ds_write_b128 v2, v[112:115] offset:2048
K 144,144,145
Mvav 80,144,200
K 145,146,147
K 146,148,149
K 147,150,151
Mvav 84,148,200
ds_write_b128 v2, v[144:147] offset:6144
buffer_load_dwordx4 a[224:227], v1, s[44:47], s81 offen offset:2048 sc1
K 120,120,121
Mvav 52,148,192
K 121,122,123
K 122,124,125
K 123,126,127
Mvav 56,152,192
ds_write_b128 v2, v[120:123] offset:3072
K 152,152,153
K 153,154,155
Mvav 88,152,200
buffer_load_dwordx4 a[240:243], v1, s[44:47], s82 offen offset:2048 sc1
K 154,156,157
K 155,158,159
Mvav 92,156,200
.Lh_4_2_2%=:
s_cbranch_execz .Lhr_76%=
ds_write_b128 v2, v[152:155] offset:7168
Mvav 60,156,192
buffer_load_dwordx4 a[228:231], v1, s[44:47], s81 offen offset:3072 sc1
buffer_load_dwordx4 a[244:247], v1, s[44:47], s82 offen offset:3072 sc1
s_branch .LL_79%=
.p2align 12
.LL_79%=:
.Lend_39%=:
s_waitcnt vmcnt(8) lgkmcnt(0)
s_barrier
s_cmp_eq_u32 s46, 0
s_cbranch_scc1 .Llate_80%=
ds_read_b32 v11, v11 offset:59392
ds_read_b64 v[8:9], v10 offset:57344
Rv 176,3,0
Rv 180,3,1024
Rv 184,3,2048
Rv 188,3,3072
Rv 192,3,4096
Rv 196,3,5120
Rv 200,3,6144
Rv 204,3,7168
W 6
Mvvv 32,224,176
s_lshl_b32 s92, s58, 2
s_add_u32 s91, s92, s51
s_add_u32 s95, s91, 7
Mvvv 36,228,176
s_and_b32 s95, s95, 15
s_mul_i32 s95, s95, 40960
s_mul_i32 s71, s51, 20480
Mvvv 40,224,180
s_add_u32 s71, s71, 61440
s_add_u32 m0, s71, s48
s_lshl2_add_u32 s94, s48, s95
Mvvv 44,228,180
W 4
v_cmp_ne_u32 vcc, 0, v11
s_cmp_lg_u64 vcc, 0
s_cbranch_scc1 .Lredo%=
A 4,4,8
Mvvv 48,224,184
v_rcp_f32 v4, v4
Mvvv 52,228,184
Ta 128,25,10240
A 5,5,9
Mvvv 56,224,188
v_rcp_f32 v5, v5
Mvvv 60,228,188
W 3
Ta 130,25,15360
D 16,40,94,0
Mvvv 64,224,192
s_add_u32 s95, s91, 9
s_and_b32 s95, s95, 15
s_mul_i32 s95, s95, 40960
Mvvv 68,228,192
s_mul_i32 s71, s51, 10240
s_add_u32 s71, s71, 112640
s_lshl_b32 s93, s58, 12
Mvvv 72,224,196
s_add_u32 s93, s93, s50
U 32,32,4
U 33,33,4
Mvvv 76,228,196
Ta 132,25,20480
Ta 134,25,25600
W 4
Mvvv 80,224,200
U 34,34,4
U 35,35,4
Mvvv 84,228,200
U 36,36,4
U 37,37,4
Mvvv 88,224,204
Ta 136,26,10272
Ta 138,26,15392
Mvvv 92,228,204
Ta 140,26,20512
Ta 142,26,25632
W 4
Zvaa 96,128,184
U 38,38,4
U 39,39,4
Mvaa 96,132,188
K 32,32,33
K 33,34,35
Zvaa 104,132,204
D 17,40,94,1024
K 34,36,37
Mvaa 104,128,200
Ta 144,25,10304
Ta 146,25,15424
Ta 148,25,20544
Ta 150,25,25664
W 4
Zvaa 108,136,200
Ta 152,26,10336
K 35,38,39
Mvaa 108,140,204
Ta 154,26,15456
Ta 156,26,20576
Zvaa 100,140,188
buffer_store_dwordx4 v[32:35], v1, s[8:11], s93 offen offset:0 sc1
U 40,40,4
Mvaa 100,136,184
Ta 158,26,25696
W 4
Zvaa 112,144,184
U 41,41,4
U 42,42,4
Mvaa 112,148,188
U 43,43,4
U 44,44,4
Zvaa 120,148,204
U 45,45,4
U 46,46,4
Mvaa 120,144,200
Ta 128,25,10368
Ta 130,25,15488
Ta 132,25,20608
Ta 134,25,25728
W 4
Zvaa 124,152,200
U 47,47,4
K 40,40,41
Mvaa 124,156,204
D 18,40,94,2048
K 41,42,43
Zvaa 116,156,188
K 42,44,45
K 43,46,47
Mvaa 116,152,184
Ta 136,26,10400
Ta 138,26,15520
Ta 140,26,20640
Ta 142,26,25760
W 4
Zvaa 128,128,184
Ta 144,25,10432
Ta 146,25,15552
Mvaa 128,132,188
buffer_store_dwordx4 v[40:43], v1, s[8:11], s93 offen offset:1024 sc1
U 48,48,4
Zvaa 136,132,204
U 49,49,4
U 50,50,4
Mvaa 136,128,200
Ta 148,25,20672
Ta 150,25,25792
W 4
Zvaa 140,136,200
U 51,51,4
U 52,52,4
Mvaa 140,140,204
U 53,53,4
U 54,54,4
Zvaa 132,140,188
U 55,55,4
K 48,48,49
Mvaa 132,136,184
Ta 152,26,10464
Ta 154,26,15584
Ta 156,26,20704
Ta 158,26,25824
W 4
Zvaa 144,144,184
D 19,40,94,3072
K 49,50,51
Mvaa 144,148,188
s_add_u32 m0, m0, 4096
K 50,52,53
K 51,54,55
Zvaa 152,148,204
Ta 128,25,10496
Ta 130,25,15616
Mvaa 152,144,200
Ta 132,25,20736
Ta 134,25,25856
W 4
Zvaa 156,152,200
buffer_store_dwordx4 v[48:51], v1, s[8:11], s93 offen offset:2048 sc1
U 56,56,4
Mvaa 156,156,204
U 57,57,4
U 58,58,4
Zvaa 148,156,188
U 59,59,4
U 60,60,4
Mvaa 148,152,184
Ta 136,26,10528
Ta 138,26,15648
Ta 140,26,20768
Ta 142,26,25888
W 4
)"
R"(Zvaa 160,128,184
U 61,61,4
U 62,62,4
Mvaa 160,132,188
U 63,63,4
K 56,56,57
Zvaa 168,132,204
buffer_load_dwordx4 v20, s[40:43], s94 offen lds
K 57,58,59
Mvaa 168,128,200
Ta 144,25,30720
Ta 146,25,35840
Ta 148,21,0
Ta 150,21,5120
W 4
Zvaa 172,136,200
s_add_u32 m0, s71, s48
s_lshl2_add_u32 s94, s48, s95
K 58,60,61
Mvaa 172,140,204
Ta 152,26,30752
K 59,62,63
Zvaa 164,140,188
buffer_store_dwordx4 v[56:59], v1, s[8:11], s93 offen offset:3072 sc1
U 64,64,5
Mvaa 164,136,184
Ta 154,26,35872
Ta 156,22,32
Ta 158,22,5152
W 4
Mvaa 96,144,192
U 65,65,5
U 66,66,5
Mvaa 96,148,196
U 67,67,5
U 68,68,5
Mvaa 104,148,212
U 69,69,5
U 70,70,5
Mvaa 104,144,208
Ta 128,25,30784
Ta 130,25,35904
Ta 132,21,64
Ta 134,21,5184
W 4
Mvaa 108,152,208
s_lshl_b32 s66, s14, 1
U 71,71,5
K 64,64,65
Mvaa 108,156,212
s_add_u32 s66, s66, 0
D 16,40,94,0
K 65,66,67
Mvaa 100,156,196
s_cmp_eq_u32 s66, s59
s_cselect_b32 s66, 134217728, 0
K 66,68,69
K 67,70,71
Mvaa 100,152,192
Ta 136,26,30816
Ta 138,26,35936
Ta 140,22,96
Ta 142,22,5216
W 4
Mvaa 112,128,192
s_add_u32 s67, s93, 131072
Ta 144,25,30848
Ta 146,25,35968
Mvaa 112,132,196
s_add_u32 s28, s52, s66
buffer_store_dwordx4 v[64:67], v1, s[8:11], s67 offen offset:0 sc1
U 72,72,5
Mvaa 120,132,212
s_lshl_b32 s66, s14, 1
U 73,73,5
U 74,74,5
Mvaa 120,128,208
Ta 148,21,128
Ta 150,21,5248
W 4
Mvaa 124,136,208
s_add_u32 s66, s66, 1
U 75,75,5
U 76,76,5
Mvaa 124,140,212
s_cmp_eq_u32 s66, s59
s_cselect_b32 s66, 134217728, 0
U 77,77,5
U 78,78,5
Mvaa 116,140,196
s_add_u32 s29, s52, s66
U 79,79,5
K 72,72,73
Mvaa 116,136,192
Ta 152,26,30880
Ta 154,26,36000
Ta 156,22,160
Ta 158,22,5280
W 4
Mvaa 128,144,192
D 17,40,94,1024
K 73,74,75
Mvaa 128,148,196
K 74,76,77
K 75,78,79
Mvaa 136,148,212
Ta 128,25,30912
Ta 130,25,36032
Mvaa 136,144,208
Ta 132,21,192
Ta 134,21,5312
W 4
Mvaa 140,152,208
D 18,40,94,2048
D 19,40,94,3072
s_add_u32 m0, m0, 4096
s_nop 0
buffer_load_dwordx4 v20, s[40:43], s94 offen lds
Mvaa 140,156,212
s_add_u32 s81, s58, 2
s_and_b32 s81, s81, 3
s_lshl_b32 s81, s81, 12
Mvaa 132,156,196
s_add_u32 s81, s81, s49
Ta 136,26,30944
Ta 138,26,36064
Mvaa 132,152,192
Ta 140,22,224
Ta 142,22,5344
W 4
Mvaa 144,128,192
buffer_store_dwordx4 v[72:75], v1, s[8:11], s67 offen offset:1024 sc1
U 80,80,5
Mvaa 144,132,196
U 81,81,5
U 82,82,5
Mvaa 152,132,212
U 83,83,5
U 84,84,5
Mvaa 152,128,208
Ta 144,25,30976
Ta 146,25,36096
Ta 148,21,256
Ta 150,21,5376
W 4
Mvaa 156,136,208
U 85,85,5
U 86,86,5
Mvaa 156,140,212
U 87,87,5
K 80,80,81
Mvaa 148,140,196
K 81,82,83
K 82,84,85
Mvaa 148,136,192
Ta 152,26,31008
Ta 154,26,36128
Ta 156,22,288
Ta 158,22,5408
W 4
Mvaa 160,144,192
buffer_load_dwordx4 a[0:3], v1, s[8:11], s81 offen offset:0 sc1
K 83,86,87
Mvaa 160,148,196
s_add_u32 s82, s81, 131072
Ta 128,21,10240
Ta 130,21,15360
Mvaa 168,148,212
Ta 132,21,20480
Ta 134,21,25600
Mvaa 168,144,208
W 4
Ta 136,22,10272
Ta 138,22,15392
Mvaa 172,152,208
buffer_store_dwordx4 v[80:83], v1, s[8:11], s67 offen offset:2048 sc1
U 88,88,5
Mvaa 172,156,212
U 89,89,5
U 90,90,5
Mvaa 164,156,196
U 91,91,5
U 92,92,5
Mvaa 164,152,192
Ta 140,22,20512
Ta 142,22,25632
s_waitcnt vmcnt(22) lgkmcnt(4)
Mvaa 96,128,216
U 93,93,5
U 94,94,5
Mvaa 96,132,220
U 95,95,5
K 88,88,89
Mvaa 104,132,236
K 89,90,91
K 90,92,93
Mvaa 104,128,232
Ta 144,21,10304
Ta 146,21,15424
Ta 148,21,20544
Ta 150,21,25664
W 4
Mvaa 108,136,232
buffer_load_dwordx4 a[16:19], v1, s[8:11], s82 offen offset:0 sc1
K 91,94,95
Mvaa 108,140,236
Ta 152,22,10336
Ta 154,22,15456
Mvaa 100,140,220
Ta 156,22,20576
Ta 158,22,25696
Mvaa 100,136,216
W 4
Ta 128,21,10368
Ta 130,21,15488
Mvaa 112,144,216
buffer_store_dwordx4 v[88:91], v1, s[8:11], s67 offen offset:3072 sc1
Ta 132,21,20608
Mvaa 112,148,220
Ta 134,21,25728
Ta 136,22,10400
Mvaa 120,148,236
Ta 138,22,15520
Ta 140,22,20640
Mvaa 120,144,232
W 7
Ta 142,22,25760
Ta 144,21,10432
Mvaa 124,152,232
buffer_load_dwordx4 a[4:7], v1, s[8:11], s81 offen offset:1024 sc1
Ta 146,21,15552
Mvaa 124,156,236
Ta 148,21,20672
Ta 150,21,25792
Mvaa 116,156,220
Mvaa 116,152,216
W 8
Ta 152,22,10464
Ta 154,22,15584
Mvaa 128,128,216
buffer_load_dwordx4 a[20:23], v1, s[8:11], s82 offen offset:1024 sc1
Mvaa 128,132,220
Ta 156,22,20704
Ta 158,22,25824
Mvaa 136,132,236
Mvaa 136,128,232
W 8
Ta 128,21,10496
Ta 130,21,15616
Mvaa 140,136,232
s_waitcnt vmcnt(7)
s_barrier
Mvaa 140,140,236
buffer_load_dwordx4 a[8:11], v1, s[8:11], s81 offen offset:2048 sc1
Ta 132,21,20736
Mvaa 132,140,220
Ta 134,21,25856
Mvaa 132,136,216
W 8
Ta 136,22,10528
Ta 138,22,15648
Mvaa 144,144,216
Mvaa 144,148,220
buffer_load_dwordx4 a[24:27], v1, s[8:11], s82 offen offset:2048 sc1
Ta 140,22,20768
Mvaa 152,148,236
Ta 142,22,25888
Mvaa 152,144,232
W 8
Ta 144,21,30720
Ta 146,21,35840
Mvaa 156,152,232
Mvaa 156,156,236
buffer_load_dwordx4 a[12:15], v1, s[8:11], s81 offen offset:3072 sc1
Ta 148,23,0
Mvaa 148,156,220
Ta 150,23,5120
Mvaa 148,152,216
W 8
Ta 152,22,30752
Ta 154,22,35872
Mvaa 160,128,216
Mvaa 160,132,220
buffer_load_dwordx4 a[28:31], v1, s[8:11], s82 offen offset:3072 sc1
Ta 156,24,32
Mvaa 168,132,236
s_lshl_b32 s92, s58, 2
s_add_u32 s91, s92, s51
s_add_u32 s95, s91, 11
Mvaa 168,128,232
W 7
s_and_b32 s95, s95, 15
s_mul_i32 s95, s95, 40960
Mvaa 172,136,232
s_mul_i32 s71, s51, 10240
s_add_u32 s71, s71, 40960
s_add_u32 m0, s71, s48
Mvaa 172,140,236
s_lshl2_add_u32 s94, s48, s95
D 16,40,94,0
Ta 158,24,5152
Mvaa 164,140,220
s_add_u32 s95, s91, 13
s_and_b32 s95, s95, 15
s_mul_i32 s95, s95, 40960
Mvaa 164,136,216
W 4
s_mul_i32 s71, s51, 20480
s_add_u32 s71, s71, 71680
Mvaa 96,144,224
s_lshl_b32 s80, s58, 3
Ta 128,21,30784
Ta 130,21,35904
Mvaa 96,148,228
s_add_u32 s80, s80, s55
D 17,40,94,1024
Ta 132,23,64
Mvaa 104,148,244
Ta 134,23,5184
Ta 136,22,30816
Mvaa 104,144,240
W 5
Ta 138,22,35936
Ta 140,24,96
Mvaa 108,152,240
s_waitcnt vmcnt(8)
v_mov_b64 v[28:29], s[12:13]
s_mov_b64 exec, 1
buffer_store_dwordx2 v[28:29], off, s[8:11], s80 sc1
s_mov_b64 exec, -1
Ta 142,24,5216
Ta 144,21,30848
Mvaa 108,156,244
Ta 146,21,35968
Ta 148,23,128
Mvaa 100,156,228
Ta 150,23,5248
Mvaa 100,152,224
W 8
Ta 152,22,30880
Ta 154,22,36000
Mvaa 112,128,224
D 18,40,94,2048
Mvaa 112,132,228
Ta 156,24,160
Ta 158,24,5280
Mvaa 120,132,244
Mvaa 120,128,240
W 8
Ta 128,21,30912
Ta 130,21,36032
Mvaa 124,136,240
D 19,40,94,3072
Mvaa 124,140,244
s_add_u32 m0, m0, 4096
Ta 132,23,192
Ta 134,23,5312
Mvaa 116,140,228
Mvaa 116,136,224
W 8
Ta 136,22,30944
Ta 138,22,36064
Mvaa 128,144,224
buffer_load_dwordx4 v20, s[40:43], s94 offen lds
Mvaa 128,148,228
s_add_u32 m0, s71, s48
s_lshl2_add_u32 s94, s48, s95
Ta 140,24,224
Mvaa 136,148,244
Ta 142,24,5344
Mvaa 136,144,240
W 8
Ta 144,21,30976
Ta 146,21,36096
Mvaa 140,152,240
D 16,40,94,0
Mvaa 140,156,244
s_add_u32 s95, s92, 15
s_and_b32 s95, s95, 15
s_mul_i32 s95, s95, 40960
Mvaa 132,156,228
s_mul_i32 s71, s51, 0
s_add_u32 s71, s71, 102400
Ta 148,23,256
Mvaa 132,152,224
W 7
Ta 150,23,5376
Ta 152,22,31008
Mvaa 144,128,224
D 17,40,94,1024
Ta 154,22,36128
Mvaa 144,132,228
Ta 156,24,288
Ta 158,24,5408
Mvaa 152,132,244
Mvaa 152,128,240
W 8
Ta 128,23,20480
Ta 130,23,25600
Mvaa 156,136,240
)"
R"(D 18,40,94,2048
Mvaa 156,140,244
Ta 132,23,51200
Ta 134,23,56320
Mvaa 148,140,228
Mvaa 148,136,224
W 8
Ta 136,24,20512
Ta 138,24,25632
Mvaa 160,144,224
D 19,40,94,3072
Mvaa 160,148,228
s_add_u32 m0, m0, 4096
Ta 140,24,51232
Ta 142,24,56352
Mvaa 168,148,244
Mvaa 168,144,240
W 8
Ta 144,23,20544
Ta 146,23,25664
Mvaa 172,152,240
buffer_load_dwordx4 v20, s[40:43], s94 offen lds
Mvaa 172,156,244
s_add_u32 m0, s71, s48
s_lshl2_add_u32 s94, s48, s95
Ta 148,23,51264
Mvaa 164,156,228
Ta 150,23,56384
Mvaa 164,152,224
s_waitcnt vmcnt(15) lgkmcnt(8)
Ta 152,24,20576
Ta 154,24,25696
Mvaa 96,128,0
D 16,40,94,0
Mvaa 96,132,4
Ta 156,24,51296
Ta 158,24,56416
Mvaa 104,132,20
Mvaa 104,128,16
W 8
Ta 128,23,20608
Ta 130,23,25728
Mvaa 108,136,16
D 17,40,94,1024
Mvaa 108,140,20
Ta 132,23,51328
Ta 134,23,56448
Mvaa 100,140,4
Mvaa 100,136,0
W 8
Ta 136,24,20640
Ta 138,24,25760
Mvaa 112,144,0
D 18,40,94,2048
Mvaa 112,148,4
Ta 140,24,51360
Ta 142,24,56480
Mvaa 120,148,20
Mvaa 120,144,16
W 8
Ta 144,23,20672
Ta 146,23,25792
Mvaa 124,152,16
D 19,40,94,3072
Mvaa 124,156,20
s_add_u32 m0, m0, 4096
Ta 148,23,51392
Ta 150,23,56512
Mvaa 116,156,4
buffer_load_dwordx4 v20, s[40:43], s94 offen lds
Mvaa 116,152,0
W 8
s_add_u32 s81, s58, 3
s_and_b32 s81, s81, 3
Mvaa 128,128,0
s_lshl_b32 s81, s81, 12
s_add_u32 s81, s81, s49
Ta 152,24,20704
Mvaa 128,132,4
Ta 154,24,25824
Ta 156,24,51424
Mvaa 136,132,20
buffer_load_dwordx4 a[32:35], v1, s[8:11], s81 offen offset:0 sc1
Ta 158,24,56544
Mvaa 136,128,16
W 8
s_add_u32 s82, s81, 131072
Ta 128,23,20736
Mvaa 140,136,16
s_waitcnt vmcnt(11)
s_barrier
Mvaa 140,140,20
Ta 130,23,25856
v_and_b32 v27, 1, v12
Mvaa 132,140,4
buffer_load_dwordx4 a[48:51], v1, s[8:11], s82 offen offset:0 sc1
Ta 132,23,51456
Mvaa 132,136,0
W 7
Ta 134,23,56576
v_lshlrev_b32 v27, 1, v27
Mvaa 144,144,0
Ta 136,24,20768
Ta 138,24,25888
Mvaa 144,148,4
Ta 140,24,51488
v_xor_b32 v27, v27, v13
Mvaa 152,148,20
buffer_load_dwordx4 a[36:39], v1, s[8:11], s81 offen offset:1024 sc1
Ta 142,24,56608
Mvaa 152,144,16
W 8
Ta 144,25,0
v_and_b32 v12, 2, v12
Mvaa 156,152,16
Ta 146,25,5120
v_or_b32 v12, v12, v14
Mvaa 156,156,20
Ta 148,21,40960
Ta 150,21,46080
Mvaa 148,156,4
buffer_load_dwordx4 a[52:55], v1, s[8:11], s82 offen offset:1024 sc1
v_lshl_or_b32 v12, v27, 2, v12
Mvaa 148,152,0
W 8
Ta 152,26,32
Ta 154,26,5152
Mvaa 160,128,0
v_cmp_eq_u32_e64 s[86:87], 0, v12
Mvaa 160,132,4
Ta 156,22,40992
Ta 158,22,46112
Mvaa 168,132,20
buffer_load_dwordx4 a[40:43], v1, s[8:11], s81 offen offset:2048 sc1
v_cndmask_b32_e64 v4, 0, v15, s[86:87]
Mvaa 168,128,16
W 8
Ta 128,25,64
Ta 130,25,5184
Mvaa 172,136,16
v_cmp_eq_u32_e64 s[86:87], 1, v12
Mvaa 172,140,20
Ta 132,21,41024
Ta 134,21,46144
Mvaa 164,140,4
buffer_load_dwordx4 a[56:59], v1, s[8:11], s82 offen offset:2048 sc1
v_cndmask_b32_e64 v5, 0, v15, s[86:87]
Mvaa 164,136,0
W 8
Ta 136,26,96
Ta 138,26,5216
Mvaa 96,144,8
v_cmp_eq_u32_e64 s[86:87], 2, v12
Mvaa 96,148,12
Ta 140,22,41056
Ta 142,22,46176
Mvaa 104,148,28
buffer_load_dwordx4 a[44:47], v1, s[8:11], s81 offen offset:3072 sc1
v_cndmask_b32_e64 v6, 0, v15, s[86:87]
Mvaa 104,144,24
W 8
Ta 144,25,128
Ta 146,25,5248
Mvaa 108,152,24
v_cmp_eq_u32_e64 s[86:87], 3, v12
Mvaa 108,156,28
Ta 148,21,41088
Ta 150,21,46208
Mvaa 100,156,12
buffer_load_dwordx4 a[60:63], v1, s[8:11], s82 offen offset:3072 sc1
v_cndmask_b32_e64 v7, 0, v15, s[86:87]
Mvaa 100,152,8
W 8
Ta 152,26,160
Ta 154,26,5280
Mvaa 112,128,8
v_cmp_eq_u32_e64 s[86:87], 4, v12
Mvaa 112,132,12
Ta 156,22,41120
Ta 158,22,46240
Mvaa 120,132,28
v_cndmask_b32_e64 v8, 0, v15, s[86:87]
Mvaa 120,128,24
W 8
Ta 128,25,192
Ta 130,25,5312
Mvaa 124,136,24
v_cmp_eq_u32_e64 s[86:87], 5, v12
v_bfe_u32 v28, v1, 4, 2
Mvaa 124,140,28
Ta 132,21,41152
v_lshlrev_b32 v28, 3, v28
Mvaa 116,140,12
Ta 134,21,46272
v_cndmask_b32_e64 v9, 0, v15, s[86:87]
Mvaa 116,136,8
W 8
buffer_load_dwordx2 v[28:29], v28, s[8:11], s55 offen sc1
Ta 136,26,224
Mvaa 128,144,8
Ta 138,26,5344
v_cmp_eq_u32_e64 s[86:87], 6, v12
Mvaa 128,148,12
Ta 140,22,41184
Ta 142,22,46304
Mvaa 136,148,28
v_cndmask_b32_e64 v10, 0, v15, s[86:87]
Mvaa 136,144,24
W 8
Ta 144,25,256
Ta 146,25,5376
Mvaa 140,152,24
s_waitcnt vmcnt(9)
s_barrier
Mvaa 140,156,28
Ta 148,21,41216
v_cmp_eq_u32_e64 s[86:87], 7, v12
Mvaa 132,156,12
Ta 150,21,46336
Mvaa 132,152,8
W 8
Ta 152,26,288
v_cndmask_b32_e64 v11, 0, v15, s[86:87]
Mvaa 144,128,8
s_mov_b32 s20, s38
Ta 154,26,5408
Mvaa 144,132,12
Ta 156,22,41248
Ta 158,22,46368
Mvaa 152,132,28
s_and_b32 s21, s39, 0xffff
Mvaa 152,128,24
W 8
Ta 128,21,51200
Ta 130,21,56320
Mvaa 156,136,24
s_mov_b32 s22, 2621440
Mvaa 156,140,28
s_mov_b32 s23, 0x20000
Ta 132,23,10240
Ta 134,23,15360
Mvaa 148,140,12
Mvaa 148,136,8
W 8
s_and_b32 s17, s17, 0xffff
Ta 136,23,30720
Mvaa 160,144,8
Ta 138,23,35840
Mvaa 160,148,12
s_mov_b32 s18, 2621440
Ta 140,23,40960
Ta 142,23,46080
Mvaa 168,148,28
Mvaa 168,144,24
W 8
s_mov_b32 s19, 0x20000
Ta 144,22,51232
Mvaa 172,152,24
Ta 146,22,56352
Mvaa 172,156,28
s_lshr_b32 s66, s15, 3
Ta 148,24,10272
Ta 150,24,15392
Mvaa 164,156,12
Mvaa 164,152,8
s_waitcnt vmcnt(5) lgkmcnt(8)
s_lshl_b32 s66, s66, 4
Ta 152,24,30752
Mvaa 96,128,32
Ta 154,24,35872
Mvaa 96,132,36
s_add_u32 s66, s66, s59
Ta 156,24,40992
Ta 158,24,46112
Mvaa 104,132,52
Mvaa 104,128,48
s_waitcnt vmcnt(1) lgkmcnt(8)
s_lshl_b32 s63, s66, 6
Ta 128,21,51264
Mvaa 104,136,56
Ta 130,21,56384
Mvaa 104,140,60
s_add_u32 s63, s63, 65536
Ta 132,23,10304
Ta 134,23,15424
Mvaa 96,140,44
Mvaa 96,136,40
W 8
s_mul_i32 s62, s66, 40960
Ta 136,23,30784
Mvaa 100,144,32
Ta 138,23,35904
Mvaa 100,148,36
s_add_u32 s62, s62, 56623104
Ta 140,23,41024
Ta 142,23,46144
Mvaa 108,148,52
Mvaa 108,144,48
W 8
s_lshl_b32 s66, s14, 13
Ta 144,22,51296
Mvaa 108,152,56
Ta 146,22,56416
Mvaa 108,156,60
s_add_u32 s78, s62, s66
Ta 148,24,10336
Ta 150,24,15456
Mvaa 100,156,44
s_waitcnt vmcnt(0)
v_cmp_eq_u64 vcc, s[12:13], v[28:29]
s_cmp_eq_u64 vcc, -1
s_cbranch_scc0 .Lspin_82%=
.Lok_83%=:
s_add_u32 s81, s58, 1
s_and_b32 s81, s81, 3
Mvaa 100,152,40
W 8
s_lshl_b32 s81, s81, 12
s_add_u32 s81, s81, s50
Mvaa 112,128,32
s_add_u32 s79, s78, 327680
buffer_load_dwordx4 a[64:67], v1, s[8:11], s81 offen offset:0 sc1
Ta 152,24,30816
Mvaa 112,132,36
s_add_u32 s82, s81, 131072
s_and_b32 s66, s14, 1
Ta 154,24,35936
Mvaa 120,132,52
Ta 156,24,41056
Ta 158,24,46176
Mvaa 120,128,48
W 8
K 96,96,97
K 97,98,99
Mvaa 120,136,56
s_mul_i32 s66, s66, 327680
Ta 128,21,51328
Ta 130,21,56448
Mvaa 120,140,60
s_add_u32 s65, s62, s66
buffer_load_dwordx4 a[80:83], v1, s[8:11], s82 offen offset:0 sc1
K 98,100,101
Mvaa 112,140,44
Ta 132,23,10368
Ta 134,23,15488
Mvaa 112,136,40
W 8
s_add_u32 s65, s65, 32768
K 99,102,103
Mvaa 116,144,32
s_add_u32 s83, s28, 0
Ta 136,23,30848
Ta 138,23,35968
Mvaa 116,148,36
s_mov_b32 s64, 0
buffer_load_dwordx4 a[68:71], v1, s[8:11], s81 offen offset:1024 sc1
Ta 140,23,41088
Mvaa 124,148,52
Ta 142,23,46208
Mvaa 124,144,48
W 8
Ta 144,22,51360
v_add_u32 v19, 4096, v1
Mvaa 124,152,56
Ta 146,22,56480
Mvaa 124,156,60
buffer_store_dwordx4 v[96:99], v1, s[8:11], s83 offen sc1
K 104,104,105
Mvaa 116,156,44
K 105,106,107
v_bfe_u32 v27, v1, 4, 3
Mvaa 116,152,40
W 6
Ta 148,24,10400
Ta 150,24,15520
Mvaa 128,128,32
K 106,108,109
v_lshlrev_b32 v27, 3, v27
Mvaa 128,132,36
buffer_load_dwordx4 a[84:87], v1, s[8:11], s82 offen offset:1024 sc1
Ta 152,24,30880
Mvaa 136,132,52
Ta 154,24,36000
K 107,110,111
Mvaa 136,128,48
W 6
Ta 156,24,41120
v_bfe_u32 v18, v1, 7, 1
Mvaa 136,136,56
s_add_u32 s83, s29, 40960
s_add_u32 s66, s14, 1
Ta 158,24,46240
Mvaa 136,140,60
s_lshr_b32 s66, s66, 2
buffer_load_dwordx4 a[72:75], v1, s[8:11], s81 offen offset:2048 sc1
Ta 128,21,51392
Mvaa 128,140,44
s_mul_i32 s66, s66, 0
Ta 130,21,56512
Ta 132,23,10432
)"
R"(Mvaa 128,136,40
W 7
Ta 134,23,15552
Ta 136,23,30912
Mvaa 132,144,32
s_sub_u32 s66, 512, s66
Ta 138,23,36032
Mvaa 132,148,36
buffer_store_dwordx4 v[104:107], v1, s[8:11], s83 offen sc1
K 112,112,113
Mvaa 140,148,52
K 113,114,115
K 114,116,117
Mvaa 140,144,48
W 6
Ta 140,23,41152
Ta 142,23,46272
Mvaa 140,152,56
v_mul_u32_u24 v18, s66, v18
K 115,118,119
Mvaa 140,156,60
s_add_u32 s83, s28, 8192
buffer_load_dwordx4 a[88:91], v1, s[8:11], s82 offen offset:2048 sc1
Ta 144,22,51424
Mvaa 132,156,44
Ta 146,22,56544
v_add_u32 v18, v18, v27
Mvaa 132,152,40
W 6
Ta 148,24,10464
Ta 150,24,15584
Mvaa 144,128,32
s_mov_b32 s71, 0
Ta 152,24,30944
Ta 154,24,36064
Mvaa 144,132,36
buffer_load_dwordx4 a[76:79], v1, s[8:11], s81 offen offset:3072 sc1
v_bfe_u32 v20, v1, 4, 4
Mvaa 152,132,52
Ta 156,24,41184
Ta 158,24,46304
Mvaa 152,128,48
W 8
Ta 128,21,51456
v_mul_u32_u24 v20, 1280, v20
Mvaa 152,136,56
Ta 130,21,56576
v_lshrrev_b32 v12, 8, v1
Mvaa 152,140,60
buffer_store_dwordx4 v[112:115], v1, s[8:11], s83 offen sc1
K 120,120,121
Mvaa 144,140,44
K 121,122,123
K 122,124,125
Mvaa 144,136,40
W 6
Ta 132,23,10496
K 123,126,127
Mvaa 148,144,32
s_add_u32 s83, s29, 49152
Ta 134,23,15616
v_lshl_add_u32 v20, v12, 4, v20
Mvaa 148,148,36
s_bfe_u32 s66, s59, 0x10001
buffer_load_dwordx4 a[92:95], v1, s[8:11], s82 offen offset:3072 sc1
Ta 136,23,30976
Mvaa 156,148,52
s_add_u32 s81, s58, 2
s_and_b32 s81, s81, 3
s_lshl_b32 s81, s81, 12
Mvaa 156,144,48
W 5
s_add_u32 s81, s81, s50
Ta 138,23,36096
Mvaa 156,152,56
s_lshl_b32 s67, s57, 1
Ta 140,23,41216
Ta 142,23,46336
Mvaa 156,156,60
s_add_u32 s66, s66, s67
buffer_load_dwordx4 a[96:99], v1, s[8:11], s81 offen offset:0 sc1
Ta 144,22,51488
Mvaa 148,156,44
s_add_u32 s82, s81, 131072
Ta 146,22,56608
Ta 148,24,10528
Mvaa 148,152,40
W 7
s_lshl_b32 s61, s66, 2
Ta 150,24,15648
Mvaa 160,128,32
s_lshr_b32 s66, s59, 2
Ta 152,24,31008
Ta 154,24,36128
Mvaa 160,132,36
buffer_store_dwordx4 v[120:123], v1, s[8:11], s83 offen sc1
K 128,128,129
Mvaa 168,132,52
s_lshl_b32 s66, s66, 1
K 129,130,131
K 130,132,133
Mvaa 168,128,48
W 6
Ta 156,24,41248
K 131,134,135
Mvaa 168,136,56
s_add_u32 s83, s28, 16384
Ta 158,24,46368
Ta 128,25,10240
Mvaa 168,140,60
s_add_u32 s61, s61, s66
buffer_load_dwordx4 a[112:115], v1, s[8:11], s82 offen offset:0 sc1
Ta 130,25,15360
Mvaa 160,140,44
s_and_b32 s66, s59, 1
Ta 132,25,20480
Ta 134,25,25600
Mvaa 160,136,40
W 8
Ta 136,26,10272
Ta 138,26,15392
Mvaa 164,144,32
s_add_u32 s61, s61, s66
Mvaa 164,148,36
buffer_load_dwordx4 a[100:103], v1, s[8:11], s81 offen offset:1024 sc1
Ta 140,26,20512
Mvaa 172,148,52
s_xor_b32 s67, s61, 0
Ta 142,26,25632
Mvaa 172,144,48
W 8
Ta 144,25,10304
Ta 146,25,15424
Mvaa 172,152,56
s_mul_i32 s72, s67, 20480
Mvaa 172,156,60
buffer_store_dwordx4 v[128:131], v1, s[8:11], s83 offen sc1
K 136,136,137
Mvaa 164,156,44
s_mul_i32 s66, s58, 320
K 137,138,139
K 138,140,141
Mvaa 164,152,40
W 6
Ta 148,25,20544
K 139,142,143
Zvav 176,128,32
s_add_u32 s83, s29, 57344
Ta 150,25,25664
Ta 152,26,10336
Mvav 176,132,40
s_add_u32 s72, s72, s66
buffer_load_dwordx4 a[116:119], v1, s[8:11], s82 offen offset:1024 sc1
Ta 154,26,15456
Zvav 184,132,72
s_mul_i32 s75, s67, 20480
Ta 156,26,20576
Ta 158,26,25696
Mvav 184,128,64
W 8
Ta 128,25,10368
Ta 130,25,15488
Zvav 188,136,64
s_mul_i32 s66, s58, 2560
Mvav 188,140,72
buffer_load_dwordx4 a[104:107], v1, s[8:11], s81 offen offset:2048 sc1
Ta 132,25,20608
Zvav 180,140,40
s_add_u32 s75, s75, s66
Ta 134,25,25728
Mvav 180,136,32
W 8
s_add_u32 s75, s75, 131072
Ta 136,26,10400
Zvav 192,144,32
Ta 138,26,15520
Mvav 192,148,40
s_xor_b32 s67, s61, 124
buffer_store_dwordx4 v[136:139], v1, s[8:11], s83 offen sc1
K 144,144,145
Zvav 200,148,72
K 145,146,147
K 146,148,149
Mvav 200,144,64
W 6
Ta 140,26,20640
K 147,150,151
Zvav 204,152,64
s_add_u32 s83, s28, 24576
s_mul_i32 s73, s67, 20480
Ta 142,26,25760
Mvav 204,156,72
s_mul_i32 s66, s58, 320
buffer_load_dwordx4 a[120:123], v1, s[8:11], s82 offen offset:2048 sc1
Ta 144,25,10432
Zvav 196,156,40
Ta 146,25,15552
Ta 148,25,20672
Mvav 196,152,32
W 7
s_add_u32 s73, s73, s66
Ta 150,25,25792
Zvav 208,128,32
Ta 152,26,10464
Ta 154,26,15584
Mvav 208,132,40
s_mul_i32 s76, s67, 20480
buffer_load_dwordx4 a[108:111], v1, s[8:11], s81 offen offset:3072 sc1
Ta 156,26,20704
Zvav 216,132,72
Ta 158,26,25824
Mvav 216,128,64
W 8
s_mul_i32 s66, s58, 2560
Ta 128,25,10496
Zvav 220,136,64
Ta 130,25,15616
Mvav 220,140,72
s_add_u32 s76, s76, s66
buffer_store_dwordx4 v[144:147], v1, s[8:11], s83 offen sc1
K 152,152,153
Zvav 212,140,40
K 153,154,155
K 154,156,157
Mvav 212,136,32
W 6
Ta 132,25,20736
K 155,158,159
Zvav 224,144,32
s_add_u32 s83, s29, 65536
s_add_u32 s76, s76, 131072
Ta 134,25,25856
Mvav 224,148,40
buffer_load_dwordx4 a[124:127], v1, s[8:11], s82 offen offset:3072 sc1
Ta 136,26,10528
Zvav 232,148,72
s_add_u32 s81, s58, 3
s_and_b32 s81, s81, 3
s_lshl_b32 s81, s81, 12
Mvav 232,144,64
W 5
s_add_u32 s81, s81, s50
Ta 138,26,15648
Zvav 236,152,64
Ta 140,26,20768
v_bfe_u32 v15, v1, 7, 1
Mvav 236,156,72
buffer_load_dwordx4 a[184:187], v1, s[8:11], s81 offen offset:0 sc1
Ta 142,26,25888
Zvav 228,156,40
s_add_u32 s82, s81, 131072
Ta 144,25,30720
v_mul_u32_u24 v14, 10240, v15
Mvav 228,152,32
W 5
Ta 146,25,35840
v_lshrrev_b32 v15, 8, v1
Zvav 240,128,32
Ta 148,21,0
Ta 150,21,5120
Mvav 240,132,40
buffer_store_dwordx4 v[152:155], v1, s[8:11], s83 offen sc1
K 160,160,161
Zvav 248,132,72
K 161,162,163
K 162,164,165
Mvav 248,128,64
W 4
K 163,166,167
v_lshl_add_u32 v14, v15, 7, v14
Zvav 252,136,64
s_add_u32 s83, s28, 32768
Ta 152,26,30752
Ta 154,26,35872
Mvav 252,140,72
buffer_load_dwordx4 a[200:203], v1, s[8:11], s82 offen offset:0 sc1
v_and_b32 v15, 0x70, v1
Zvav 244,140,40
Ta 156,22,32
Ta 158,22,5152
Mvav 244,136,32
W 4
Ta 128,25,30784
v_add_u32 v14, v14, v15
Mvav 176,144,48
s_bitcmp1_b32 s14, 0
s_cselect_b32 s74, s73, s72
s_cselect_b32 s77, s76, s75
Ta 130,25,35904
Ta 132,21,64
Mvav 176,148,56
s_add_u32 s74, s74, 256
buffer_load_dwordx4 a[188:191], v1, s[8:11], s81 offen offset:1024 sc1
Ta 134,21,5184
Mvav 184,148,88
s_add_u32 s77, s77, 2048
Ta 136,26,30816
Ta 138,26,35936
Mvav 184,144,80
W 6
Ta 140,22,96
Ta 142,22,5216
Mvav 188,152,80
s_lshl_b32 s66, s14, 6
Ta 144,25,30848
Ta 146,25,35968
Mvav 188,156,88
s_lshl_b32 s67, s14, 9
buffer_store_dwordx4 v[160:163], v1, s[8:11], s83 offen sc1
K 168,168,169
Mvav 180,156,56
K 169,170,171
K 170,172,173
Mvav 180,152,48
W 6
Ta 148,21,128
K 171,174,175
Mvav 192,128,48
s_add_u32 s83, s29, 73728
s_add_u32 s72, s72, s66
Ta 150,21,5248
Mvav 192,132,56
s_add_u32 s75, s75, s67
buffer_load_dwordx4 a[204:207], v1, s[8:11], s82 offen offset:1024 sc1
Ta 152,26,30880
Mvav 200,132,88
Ta 154,26,36000
Ta 156,22,160
Mvav 200,128,80
W 7
s_add_u32 s73, s73, s66
Ta 158,22,5280
Mvav 204,136,80
Ta 128,25,30912
Ta 130,25,36032
Mvav 204,140,88
s_add_u32 s76, s76, s67
buffer_load_dwordx4 a[192:195], v1, s[8:11], s81 offen offset:2048 sc1
Ta 132,21,192
Mvav 196,140,56
Ta 134,21,5312
Mvav 196,136,48
W 8
Ta 136,26,30944
Ta 138,26,36064
Mvav 208,144,48
Mvav 208,148,56
buffer_store_dwordx4 v[168:171], v1, s[8:11], s83 offen sc1
Ta 140,22,224
Mvav 216,148,88
Ta 142,22,5344
Mvav 216,144,80
W 8
Ta 144,25,30976
Ta 146,25,36096
Mvav 220,152,80
Mvav 220,156,88
buffer_load_dwordx4 a[208:211], v1, s[8:11], s82 offen offset:2048 sc1
Ta 148,21,256
Mvav 212,156,56
Ta 150,21,5376
Mvav 212,152,48
W 8
Ta 152,26,31008
Ta 154,26,36128
Mvav 224,128,48
Mvav 224,132,56
buffer_load_dwordx4 a[196:199], v1, s[8:11], s81 offen offset:3072 sc1
Ta 156,22,288
Mvav 232,132,88
Ta 158,22,5408
Mvav 232,128,80
W 8
Ta 128,21,10240
Ta 130,21,15360
Mvav 236,136,80
Mvav 236,140,88
buffer_load_dwordx4 a[212:215], v1, s[8:11], s82 offen offset:3072 sc1
Ta 132,21,20480
Mvav 228,140,56
Ta 134,21,25600
Mvav 228,136,48
W 8
Ta 136,22,10272
Ta 138,22,15392
Mvav 240,144,48
Mvav 240,148,56
buffer_load_dwordx4 v[36:39], v20, s[16:19], s72 offen
Ta 140,22,20512
Mvav 248,148,88
Ta 142,22,25632
Mvav 248,144,80
W 8
Ta 144,21,10304
Ta 146,21,15424
Mvav 252,152,80
Mvav 252,156,88
buffer_load_dwordx4 v[44:47], v14, s[8:11], s75 offen sc1
Ta 148,21,20544
Mvav 244,156,56
)"
R"(Ta 150,21,25664
Mvav 244,152,48
s_waitcnt vmcnt(31) lgkmcnt(8)
Ta 152,22,10336
Ta 154,22,15456
Mvaa 176,128,64
Mvaa 176,132,68
buffer_load_dwordx4 v[52:55], v20, s[16:19], s73 offen
Ta 156,22,20576
Mvaa 184,132,84
Ta 158,22,25696
Mvaa 184,128,80
W 8
Ta 128,21,10368
Ta 130,21,15488
Mvaa 188,136,80
Mvaa 188,140,84
buffer_load_dwordx4 v[60:63], v14, s[8:11], s76 offen sc1
Ta 132,21,20608
Mvaa 180,140,68
Ta 134,21,25728
Mvaa 180,136,64
W 8
Ta 136,22,10400
Ta 138,22,15520
Mvaa 192,144,64
Mvaa 192,148,68
buffer_load_dwordx4 v[68:71], v20, s[16:19], s74 offen
Ta 140,22,20640
Mvaa 200,148,84
Ta 142,22,25760
Mvaa 200,144,80
W 8
Ta 144,21,10432
Ta 146,21,15552
Mvaa 204,152,80
Mvaa 204,156,84
buffer_load_dwordx4 v[76:79], v14, s[8:11], s77 offen sc1
Ta 148,21,20672
Mvaa 196,156,68
Ta 150,21,25792
Mvaa 196,152,64
W 8
Ta 152,22,10464
Ta 154,22,15584
Mvaa 208,128,64
Mvaa 208,132,68
buffer_load_dwordx2 v[12:13], v27, s[8:11], 0 offen offset:3584 sc1
Ta 156,22,20704
Mvaa 216,132,84
Ta 158,22,25824
Mvaa 216,128,80
W 8
Ta 128,21,10496
Ta 130,21,15616
Mvaa 220,136,80
Mvaa 220,140,84
s_waitcnt vmcnt(10)
v_lshlrev_b32 v2, 2, v1
v_mov_b64 v[28:29], s[12:13]
s_mov_b64 exec, 3
buffer_store_dwordx2 v[28:29], v2, s[8:11], s53 offen offset:0 sc1
s_mov_b64 exec, -1
Ta 132,21,20736
Ta 134,21,25856
Mvaa 212,140,68
Mvaa 212,136,64
W 8
Ta 136,22,10528
Ta 138,22,15648
Mvaa 224,144,64
Mvaa 224,148,68
Ta 140,22,20768
Ta 142,22,25888
Mvaa 232,148,84
Mvaa 232,144,80
W 8
Ta 144,21,30720
Ta 146,21,35840
Mvaa 236,152,80
Mvaa 236,156,84
Ta 148,23,0
Ta 150,23,5120
Mvaa 228,156,68
Mvaa 228,152,64
W 8
Ta 152,22,30752
Ta 154,22,35872
Mvaa 240,128,64
Mvaa 240,132,68
Ta 156,24,32
Ta 158,24,5152
Mvaa 248,132,84
Mvaa 248,128,80
W 8
Ta 128,21,30784
Ta 130,21,35904
Mvaa 252,136,80
Mvaa 252,140,84
Ta 132,23,64
Ta 134,23,5184
Mvaa 244,140,68
Mvaa 244,136,64
W 8
Ta 136,22,30816
Ta 138,22,35936
Mvaa 176,144,72
Mvaa 176,148,76
Ta 140,24,96
Ta 142,24,5216
Mvaa 184,148,92
Mvaa 184,144,88
W 8
Ta 144,21,30848
Ta 146,21,35968
Mvaa 188,152,88
Mvaa 188,156,92
Ta 148,23,128
Ta 150,23,5248
Mvaa 180,156,76
Mvaa 180,152,72
W 8
Ta 152,22,30880
Ta 154,22,36000
Mvaa 192,128,72
Mvaa 192,132,76
Ta 156,24,160
Ta 158,24,5280
Mvaa 200,132,92
Mvaa 200,128,88
W 8
Ta 128,21,30912
Ta 130,21,36032
Mvaa 204,136,88
Mvaa 204,140,92
Ta 132,23,192
Ta 134,23,5312
Mvaa 196,140,76
Mvaa 196,136,72
W 8
Ta 136,22,30944
Ta 138,22,36064
Mvaa 208,144,72
Mvaa 208,148,76
Ta 140,24,224
Ta 142,24,5344
Mvaa 216,148,92
Mvaa 216,144,88
W 8
Ta 144,21,30976
Ta 146,21,36096
Mvaa 220,152,88
Mvaa 220,156,92
Ta 148,23,256
Ta 150,23,5376
Mvaa 212,156,76
Mvaa 212,152,72
W 8
Ta 152,22,31008
Ta 154,22,36128
Mvaa 224,128,72
Mvaa 224,132,76
Ta 156,24,288
Ta 158,24,5408
Mvaa 232,132,92
Mvaa 232,128,88
W 8
Ta 128,23,20480
Ta 130,23,25600
Mvaa 236,136,88
Mvaa 236,140,92
Ta 132,23,51200
Ta 134,23,56320
Mvaa 228,140,76
Mvaa 228,136,72
W 8
Ta 136,25,0
Ta 138,25,5120
Mvaa 240,144,72
Mvaa 240,148,76
Ta 140,21,40960
Ta 142,21,46080
Mvaa 248,148,92
s_waitcnt vmcnt(1)
v_xor_b32 v12, s12, v12
v_xor_b32 v13, s13, v13
v_lshrrev_b32 v3, 11, v12
v_or_b32 v13, v13, v3
v_cmp_eq_u32 vcc, 0, v13
s_cmp_eq_u64 vcc, -1
s_cbranch_scc0 .LL_87%=
.LL_88%=:
v_bfe_u32 v13, v12, 4, 7
s_xor_b32 s66, s61, 124
v_cmp_eq_u32 vcc, s61, v13
v_cmp_eq_u32_e64 s[86:87], s66, v13
s_or_b32 s68, vcc_lo, s86
s_and_b32 s68, s68, 255
s_cbranch_scc1 .Lup_85%=
.Lupr_86%=:
Mvaa 248,144,88
W 8
Ta 144,21,51200
Ta 146,21,56320
Mvaa 252,152,88
v_lshlrev_b32 v32, 16, v36
Mvaa 252,156,92
Ta 148,23,10240
v_and_b32 v33, 0xffff0000, v36
Mvaa 244,156,76
Ta 150,23,15360
v_lshlrev_b32 v34, 16, v37
Mvaa 244,152,72
W 8
Ta 152,23,30720
v_and_b32 v35, 0xffff0000, v37
Mvaa 176,128,96
Ta 154,23,35840
v_lshlrev_b32 v36, 16, v38
Mvaa 176,132,100
buffer_load_dwordx2 v[16:17], v18, s[8:11], s63 offen sc1
v_and_b32 v37, 0xffff0000, v38
Mvaa 184,132,116
Ta 156,23,40960
Ta 158,23,46080
Mvaa 184,128,112
W 8
v_lshlrev_b32 v38, 16, v39
v_and_b32 v39, 0xffff0000, v39
Mvaa 184,136,120
Ta 128,24,20512
Ta 130,24,25632
Mvaa 184,140,124
v_lshlrev_b32 v40, 16, v44
v_and_b32 v41, 0xffff0000, v44
Mvaa 176,140,108
s_barrier
Mvaa 176,136,104
W 6
Ta 132,24,51232
Ta 134,24,56352
Mvaa 176,144,184
v_lshlrev_b32 v42, 16, v45
v_and_b32 v43, 0xffff0000, v45
Mvaa 176,148,188
Ta 136,26,32
Ta 138,26,5152
Mvaa 184,148,204
v_lshlrev_b32 v44, 16, v46
v_and_b32 v45, 0xffff0000, v46
Mvaa 184,144,200
W 6
Ta 140,22,40992
Ta 142,22,46112
Mvaa 184,152,208
v_lshlrev_b32 v46, 16, v47
v_and_b32 v47, 0xffff0000, v47
Mvaa 184,156,212
Ta 144,22,51232
Ta 146,22,56352
Mvaa 176,156,196
v_lshlrev_b32 v48, 16, v52
v_and_b32 v49, 0xffff0000, v52
Mvaa 176,152,192
W 6
Ta 148,24,10272
Ta 150,24,15392
Mvaa 180,128,96
v_lshlrev_b32 v50, 16, v53
v_and_b32 v51, 0xffff0000, v53
Mvaa 180,132,100
Ta 152,24,30752
Ta 154,24,35872
Mvaa 188,132,116
v_lshlrev_b32 v52, 16, v54
v_and_b32 v53, 0xffff0000, v54
Mvaa 188,128,112
W 6
Ta 156,24,40992
Ta 158,24,46112
Mvaa 188,136,120
v_lshlrev_b32 v54, 16, v55
v_and_b32 v55, 0xffff0000, v55
Mvaa 188,140,124
Ta 128,23,20544
Ta 130,23,25664
Mvaa 180,140,108
v_lshlrev_b32 v56, 16, v60
v_and_b32 v57, 0xffff0000, v60
Mvaa 180,136,104
W 6
Ta 132,23,51264
Ta 134,23,56384
Mvaa 180,144,184
v_lshlrev_b32 v58, 16, v61
v_and_b32 v59, 0xffff0000, v61
Mvaa 180,148,188
Ta 136,25,64
Ta 138,25,5184
Mvaa 188,148,204
v_lshlrev_b32 v60, 16, v62
v_and_b32 v61, 0xffff0000, v62
Mvaa 188,144,200
W 6
Ta 140,21,41024
Ta 142,21,46144
Mvaa 188,152,208
v_lshlrev_b32 v62, 16, v63
v_and_b32 v63, 0xffff0000, v63
Mvaa 188,156,212
Ta 144,21,51264
Ta 146,21,56384
Mvaa 180,156,196
v_lshlrev_b32 v64, 16, v68
v_and_b32 v65, 0xffff0000, v68
Mvaa 180,152,192
W 6
Ta 148,23,10304
Ta 150,23,15424
Mvaa 192,128,96
v_lshlrev_b32 v66, 16, v69
v_and_b32 v67, 0xffff0000, v69
Mvaa 192,132,100
Ta 152,23,30784
Ta 154,23,35904
Mvaa 200,132,116
v_lshlrev_b32 v68, 16, v70
v_and_b32 v69, 0xffff0000, v70
Mvaa 200,128,112
W 6
Ta 156,23,41024
Ta 158,23,46144
Mvaa 200,136,120
v_lshlrev_b32 v70, 16, v71
K 176,176,177
Mvaa 200,140,124
Ta 128,24,20576
v_and_b32 v71, 0xffff0000, v71
Mvaa 192,140,108
Ta 130,24,25696
K 177,178,179
Mvaa 192,136,104
W 6
v_lshlrev_b32 v72, 16, v76
K 178,180,181
Mvaa 192,144,184
Ta 132,24,51296
v_and_b32 v73, 0xffff0000, v76
Mvaa 192,148,188
Ta 134,24,56416
v_lshlrev_b32 v74, 16, v77
Mvaa 200,148,204
K 179,182,183
v_and_b32 v75, 0xffff0000, v77
Mvaa 200,144,200
W 4
Ta 136,26,96
Ta 138,26,5216
Mvaa 200,152,208
s_add_u32 s83, s28, 327680
buffer_store_dwordx4 v[176:179], v1, s[8:11], s83 offen sc1
v_lshlrev_b32 v76, 16, v78
Mvaa 200,156,212
s_waitcnt vmcnt(1)
v_cmp_eq_u64 vcc, s[12:13], v[16:17]
s_and_b32 s71, vcc_lo, 255
s_bitset0_b32 s71, s59
K 184,184,185
K 185,186,187
Mvaa 192,156,196
K 186,188,189
K 187,190,191
s_add_u32 s83, s29, 368640
buffer_store_dwordx4 v[184:187], v1, s[8:11], s83 offen sc1
Mvaa 192,152,192
Ta 140,22,41056
Ta 142,22,46176
W 4
Mvaa 196,128,96
s_lshl_b32 s92, s14, 3
s_lshr_b32 s92, s71, s92
s_mov_b32 s91, 134217728
Mvaa 196,132,100
s_bitcmp1_b32 s71, 0
s_cselect_b32 s94, s78, s91
v_and_b32 v77, 0xffff0000, v78
Mvaa 204,132,116
v_lshlrev_b32 v78, 16, v79
v_and_b32 v79, 0xffff0000, v79
Mvaa 204,128,112
Ta 144,22,51296
Ta 146,22,56416
Ta 148,24,10336
Ta 150,24,15456
W 4
Mvaa 204,136,120
Ta 152,24,30816
Ta 154,24,35936
Mvaa 204,140,124
buffer_load_dwordx4 a[0:3], v1, s[8:11], s94 offen offset:0 sc1
)"
R"(Ta 156,24,41056
Mvaa 196,140,108
Ta 158,24,46176
Ta 128,23,20608
Mvaa 196,136,104
W 5
Ta 130,23,25728
Ta 132,23,51328
Mvaa 196,144,184
Ta 134,23,56448
Ta 136,25,128
Mvaa 196,148,188
s_bitcmp1_b32 s92, 0
s_cbranch_scc0 .LL_90%=
buffer_load_dwordx4 a[216:219], v1, s[8:11], s65 offen offset:0 sc1
.LL_90%=:
Ta 138,25,5248
Mvaa 204,148,204
s_bitcmp1_b32 s71, 1
s_cselect_b32 s94, s78, s91
Ta 140,21,41088
Mvaa 204,144,200
W 7
Ta 142,21,46208
Ta 144,21,51328
Mvaa 204,152,208
Ta 146,21,56448
Mvaa 204,156,212
Ta 148,23,10368
Ta 150,23,15488
Mvaa 196,156,196
buffer_load_dwordx4 a[4:7], v1, s[8:11], s94 offen offset:1024 sc1
Mvaa 196,152,192
W 8
Ta 152,23,30848
Ta 154,23,35968
Mvaa 208,128,96
Mvaa 208,132,100
Ta 156,23,41088
Ta 158,23,46208
Mvaa 216,132,116
K 192,192,193
Mvaa 216,128,112
W 8
Ta 128,24,20640
Ta 130,24,25760
Mvaa 216,136,120
K 193,194,195
Mvaa 216,140,124
s_bitcmp1_b32 s92, 1
s_cbranch_scc0 .LL_91%=
buffer_load_dwordx4 a[220:223], v1, s[8:11], s65 offen offset:1024 sc1
.LL_91%=:
K 194,196,197
Mvaa 208,140,108
s_bitcmp1_b32 s71, 2
s_cselect_b32 s94, s78, s91
Ta 132,24,51360
Mvaa 208,136,104
W 7
Ta 134,24,56480
K 195,198,199
Mvaa 208,144,184
s_add_u32 s83, s28, 335872
Ta 136,26,160
Ta 138,26,5280
Mvaa 208,148,188
buffer_store_dwordx4 v[192:195], v1, s[8:11], s83 offen sc1
K 200,200,201
Mvaa 216,148,204
K 201,202,203
K 202,204,205
Mvaa 216,144,200
W 6
Ta 140,22,41120
K 203,206,207
Mvaa 216,152,208
s_add_u32 s83, s29, 376832
Ta 142,22,46240
Ta 144,22,51360
Mvaa 216,156,212
buffer_store_dwordx4 v[200:203], v1, s[8:11], s83 offen sc1
Ta 146,22,56480
Mvaa 208,156,196
Ta 148,24,10400
Ta 150,24,15520
Mvaa 208,152,192
W 8
Ta 152,24,30880
Ta 154,24,36000
Mvaa 212,128,96
Mvaa 212,132,100
buffer_load_dwordx4 a[8:11], v1, s[8:11], s94 offen offset:2048 sc1
Ta 156,24,41120
Mvaa 220,132,116
Ta 158,24,46240
Mvaa 220,128,112
W 8
Ta 128,23,20672
Ta 130,23,25792
Mvaa 220,136,120
Mvaa 220,140,124
s_bitcmp1_b32 s92, 2
s_cbranch_scc0 .LL_92%=
buffer_load_dwordx4 a[224:227], v1, s[8:11], s65 offen offset:2048 sc1
.LL_92%=:
Ta 132,23,51392
Mvaa 212,140,108
s_bitcmp1_b32 s71, 3
s_cselect_b32 s94, s78, s91
Ta 134,23,56512
Mvaa 212,136,104
W 8
Ta 136,25,192
Ta 138,25,5312
Mvaa 212,144,184
Mvaa 212,148,188
Ta 140,21,41152
Ta 142,21,46272
Mvaa 220,148,204
buffer_load_dwordx4 a[12:15], v1, s[8:11], s94 offen offset:3072 sc1
Mvaa 220,144,200
W 8
Ta 144,21,51392
Ta 146,21,56512
Mvaa 220,152,208
Mvaa 220,156,212
Ta 148,23,10432
Ta 150,23,15552
Mvaa 212,156,196
Mvaa 212,152,192
W 8
Ta 152,23,30912
Ta 154,23,36032
Mvaa 224,128,96
Mvaa 224,132,100
s_bitcmp1_b32 s92, 3
s_cbranch_scc0 .LL_93%=
buffer_load_dwordx4 a[228:231], v1, s[8:11], s65 offen offset:3072 sc1
.LL_93%=:
Ta 156,23,41152
Mvaa 232,132,116
s_bitcmp1_b32 s71, 4
s_cselect_b32 s94, s78, s91
Ta 158,23,46272
Mvaa 232,128,112
W 8
Ta 128,24,20704
K 208,208,209
Mvaa 232,136,120
Ta 130,24,25824
K 209,210,211
Mvaa 232,140,124
Ta 132,24,51424
K 210,212,213
Mvaa 224,140,108
Ta 134,24,56544
K 211,214,215
Mvaa 224,136,104
W 8
Ta 136,26,224
Ta 138,26,5344
Mvaa 224,144,184
s_add_u32 s83, s28, 344064
buffer_load_dwordx4 a[16:19], v19, s[8:11], s94 offen offset:0 sc1
Mvaa 224,148,188
Ta 140,22,41184
Ta 142,22,46304
Mvaa 232,148,204
Mvaa 232,144,200
W 8
Ta 144,22,51424
Ta 146,22,56544
Mvaa 232,152,208
buffer_store_dwordx4 v[208:211], v1, s[8:11], s83 offen sc1
K 216,216,217
Mvaa 232,156,212
K 217,218,219
K 218,220,221
Mvaa 224,156,196
K 219,222,223
s_add_u32 s83, s29, 385024
buffer_store_dwordx4 v[216:219], v1, s[8:11], s83 offen sc1
Mvaa 224,152,192
W 6
Ta 148,24,10464
Ta 150,24,15584
Mvaa 228,128,96
Ta 152,24,30944
Ta 154,24,36064
Mvaa 228,132,100
Ta 156,24,41184
Ta 158,24,46304
Mvaa 236,132,116
s_bitcmp1_b32 s92, 4
s_cbranch_scc0 .LL_94%=
buffer_load_dwordx4 a[232:235], v19, s[8:11], s65 offen offset:0 sc1
.LL_94%=:
Mvaa 236,128,112
W 8
s_bitcmp1_b32 s71, 5
s_cselect_b32 s94, s78, s91
Mvaa 236,136,120
Ta 128,23,20736
Ta 130,23,25856
Mvaa 236,140,124
Ta 132,23,51456
Ta 134,23,56576
Mvaa 228,140,108
buffer_load_dwordx4 a[20:23], v19, s[8:11], s94 offen offset:1024 sc1
Mvaa 228,136,104
W 8
Ta 136,25,256
Ta 138,25,5376
Mvaa 228,144,184
Mvaa 228,148,188
Ta 140,21,41216
Ta 142,21,46336
Mvaa 236,148,204
Mvaa 236,144,200
W 8
Ta 144,21,51456
Ta 146,21,56576
Mvaa 236,152,208
Mvaa 236,156,212
s_bitcmp1_b32 s92, 5
s_cbranch_scc0 .LL_95%=
buffer_load_dwordx4 a[236:239], v19, s[8:11], s65 offen offset:1024 sc1
.LL_95%=:
Ta 148,23,10496
Mvaa 228,156,196
s_bitcmp1_b32 s71, 6
s_cselect_b32 s94, s78, s91
Ta 150,23,15616
Mvaa 228,152,192
W 8
Ta 152,23,30976
Ta 154,23,36096
Mvaa 240,128,96
Mvaa 240,132,100
Ta 156,23,41216
Ta 158,23,46336
Mvaa 248,132,116
K 224,224,225
Mvaa 248,128,112
W 8
Ta 128,24,20768
Ta 130,24,25888
Mvaa 248,136,120
buffer_load_dwordx4 a[24:27], v19, s[8:11], s94 offen offset:2048 sc1
K 225,226,227
Mvaa 248,140,124
Ta 132,24,51488
K 226,228,229
Mvaa 240,140,108
Ta 134,24,56608
K 227,230,231
Mvaa 240,136,104
W 8
Ta 136,26,288
Ta 138,26,5408
Mvaa 240,144,184
s_add_u32 s83, s28, 352256
Mvaa 240,148,188
buffer_store_dwordx4 v[224:227], v1, s[8:11], s83 offen sc1
K 232,232,233
Mvaa 248,148,204
K 233,234,235
K 234,236,237
Mvaa 248,144,200
W 6
Ta 140,22,41248
K 235,238,239
Mvaa 248,152,208
s_add_u32 s83, s29, 393216
Ta 142,22,46368
Ta 144,22,51488
Mvaa 248,156,212
buffer_store_dwordx4 v[232:235], v1, s[8:11], s83 offen sc1
Ta 146,22,56608
Mvaa 240,156,196
Ta 148,24,10528
Ta 150,24,15648
Mvaa 240,152,192
W 8
Ta 152,24,31008
Ta 154,24,36128
Mvaa 244,128,96
Mvaa 244,132,100
s_bitcmp1_b32 s92, 6
s_cbranch_scc0 .LL_96%=
buffer_load_dwordx4 a[240:243], v19, s[8:11], s65 offen offset:2048 sc1
.LL_96%=:
Ta 156,24,41248
Mvaa 252,132,116
s_bitcmp1_b32 s71, 7
s_cselect_b32 s94, s78, s91
Ta 158,24,46368
Mvaa 252,128,112
W 8
Mvaa 252,136,120
Mvaa 252,140,124
buffer_load_dwordx4 a[28:31], v19, s[8:11], s94 offen offset:3072 sc1
Mvaa 244,140,108
Mvaa 244,136,104
W 4
Mvaa 244,144,184
Mvaa 244,148,188
s_bitcmp1_b32 s92, 7
s_cbranch_scc0 .LL_97%=
buffer_load_dwordx4 a[244:247], v19, s[8:11], s65 offen offset:3072 sc1
.LL_97%=:
Mvaa 252,148,204
Mvaa 252,144,200
W 0
Mvaa 252,152,208
Mvaa 252,156,212
Mvaa 244,156,196
Mvaa 244,152,192
K 240,240,241
K 241,242,243
s_nop 5
K 242,244,245
K 243,246,247
s_add_u32 s83, s28, 360448
buffer_store_dwordx4 v[240:243], v1, s[8:11], s83 offen sc1
K 248,248,249
K 249,250,251
K 250,252,253
K 251,254,255
s_add_u32 s83, s29, 401408
buffer_store_dwordx4 v[248:251], v1, s[8:11], s83 offen sc1
s_branch .Ltend_81%=
.p2align 12
.Llate_80%=:
ds_read_b32 v11, v11 offset:59392
ds_read_b64 v[8:9], v10 offset:57344
Rv 176,3,0
Rv 180,3,1024
Rv 184,3,2048
Rv 188,3,3072
Rv 192,3,4096
Rv 196,3,5120
Rv 200,3,6144
Rv 204,3,7168
W 6
Mvvv 32,224,176
s_lshl_b32 s92, s58, 2
s_add_u32 s91, s92, s51
s_add_u32 s95, s91, 7
Mvvv 36,228,176
s_and_b32 s95, s95, 15
s_mul_i32 s95, s95, 40960
s_mul_i32 s71, s51, 20480
Mvvv 40,224,180
s_add_u32 s71, s71, 61440
s_add_u32 m0, s71, s48
s_lshl2_add_u32 s94, s48, s95
Mvvv 44,228,180
W 4
v_cmp_ne_u32 vcc, 0, v11
s_cmp_lg_u64 vcc, 0
s_cbranch_scc1 .Lredo%=
A 4,4,8
Mvvv 48,224,184
v_rcp_f32 v4, v4
Mvvv 52,228,184
Ta 128,25,10240
A 5,5,9
Mvvv 56,224,188
v_rcp_f32 v5, v5
Mvvv 60,228,188
W 3
Ta 130,25,15360
D 16,40,94,0
Mvvv 64,224,192
s_add_u32 s95, s91, 9
s_and_b32 s95, s95, 15
s_mul_i32 s95, s95, 40960
Mvvv 68,228,192
s_mul_i32 s71, s51, 10240
s_add_u32 s71, s71, 112640
s_lshl_b32 s93, s58, 12
Mvvv 72,224,196
s_add_u32 s93, s93, s50
U 32,32,4
U 33,33,4
Mvvv 76,228,196
Ta 132,25,20480
Ta 134,25,25600
W 4
Mvvv 80,224,200
U 34,34,4
U 35,35,4
Mvvv 84,228,200
U 36,36,4
U 37,37,4
Mvvv 88,224,204
U 38,38,4
U 39,39,4
)"
R"(K 32,32,33
K 33,34,35
K 34,36,37
K 35,38,39
buffer_store_dwordx4 v[32:35], v1, s[8:11], s93 offen offset:0 sc1
U 40,40,4
U 41,41,4
U 42,42,4
U 43,43,4
U 44,44,4
U 45,45,4
U 46,46,4
U 47,47,4
K 40,40,41
K 41,42,43
K 42,44,45
K 43,46,47
buffer_store_dwordx4 v[40:43], v1, s[8:11], s93 offen offset:1024 sc1
U 64,64,5
U 65,65,5
U 66,66,5
U 67,67,5
U 68,68,5
U 69,69,5
U 70,70,5
U 71,71,5
K 64,64,65
K 65,66,67
K 66,68,69
K 67,70,71
s_add_u32 s67, s93, 131072
buffer_store_dwordx4 v[64:67], v1, s[8:11], s67 offen offset:0 sc1
U 72,72,5
U 73,73,5
U 74,74,5
U 75,75,5
U 76,76,5
U 77,77,5
U 78,78,5
U 79,79,5
K 72,72,73
K 73,74,75
K 74,76,77
K 75,78,79
Mvvv 92,228,204
Ta 136,26,10272
Ta 138,26,15392
Ta 140,26,20512
Ta 142,26,25632
W 4
Zvav 176,128,32
Ta 144,25,10304
Ta 146,25,15424
Mvav 176,132,40
Ta 148,25,20544
Ta 150,25,25664
Zvav 184,132,72
D 17,40,94,1024
Ta 152,26,10336
Mvav 184,128,64
W 5
Ta 154,26,15456
Ta 156,26,20576
Zvav 188,136,64
Ta 158,26,25696
Ta 128,25,10368
Mvav 188,140,72
Ta 130,25,15488
Ta 132,25,20608
Zvav 180,140,40
D 18,40,94,2048
Ta 134,25,25728
Mvav 180,136,32
W 8
Ta 136,26,10400
Ta 138,26,15520
Zvav 192,144,32
Mvav 192,148,40
Ta 140,26,20640
Ta 142,26,25760
Zvav 200,148,72
D 19,40,94,3072
Mvav 200,144,64
W 8
s_add_u32 m0, m0, 4096
Ta 144,25,10432
Zvav 204,152,64
Ta 146,25,15552
Mvav 204,156,72
Ta 148,25,20672
Ta 150,25,25792
Zvav 196,156,40
buffer_load_dwordx4 v20, s[40:43], s94 offen lds
Mvav 196,152,32
W 8
s_add_u32 m0, s71, s48
s_lshl2_add_u32 s94, s48, s95
Zvav 208,128,32
Ta 152,26,10464
Ta 154,26,15584
Mvav 208,132,40
Ta 156,26,20704
Ta 158,26,25824
Zvav 216,132,72
D 16,40,94,0
v_bfe_u32 v28, v1, 4, 2
Mvav 216,128,64
W 8
Ta 128,25,10496
v_lshlrev_b32 v28, 3, v28
Zvav 220,136,64
Ta 130,25,15616
Mvav 220,140,72
Ta 132,25,20736
Ta 134,25,25856
Zvav 212,140,40
buffer_load_dwordx2 v[28:29], v28, s[8:11], s54 offen sc1
Mvav 212,136,32
W 8
Ta 136,26,10528
Ta 138,26,15648
Zvav 224,144,32
Mvav 224,148,40
Ta 140,26,20768
Ta 142,26,25888
Zvav 232,148,72
D 17,40,94,1024
Mvav 232,144,64
W 8
Ta 144,25,30720
Ta 146,25,35840
Zvav 236,152,64
Mvav 236,156,72
Ta 148,21,0
Ta 150,21,5120
Zvav 228,156,40
buffer_store_dwordx4 v[72:75], v1, s[8:11], s67 offen offset:1024 sc1
U 48,48,4
Mvav 228,152,32
W 8
U 49,49,4
U 50,50,4
Zvav 240,128,32
U 51,51,4
U 52,52,4
Mvav 240,132,40
U 53,53,4
U 54,54,4
Zvav 248,132,72
D 18,40,94,2048
U 55,55,4
Mvav 248,128,64
W 4
K 48,48,49
K 49,50,51
Zvav 252,136,64
K 50,52,53
K 51,54,55
Mvav 252,140,72
Ta 152,26,30752
Ta 154,26,35872
Zvav 244,140,40
buffer_store_dwordx4 v[48:51], v1, s[8:11], s93 offen offset:2048 sc1
U 56,56,4
U 57,57,4
U 58,58,4
U 59,59,4
U 60,60,4
U 61,61,4
U 62,62,4
U 63,63,4
K 56,56,57
K 57,58,59
K 58,60,61
K 59,62,63
buffer_store_dwordx4 v[56:59], v1, s[8:11], s93 offen offset:3072 sc1
U 80,80,5
U 81,81,5
U 82,82,5
U 83,83,5
U 84,84,5
U 85,85,5
U 86,86,5
U 87,87,5
K 80,80,81
K 81,82,83
K 82,84,85
K 83,86,87
buffer_store_dwordx4 v[80:83], v1, s[8:11], s67 offen offset:2048 sc1
U 88,88,5
U 89,89,5
U 90,90,5
U 91,91,5
U 92,92,5
U 93,93,5
U 94,94,5
U 95,95,5
K 88,88,89
K 89,90,91
K 90,92,93
K 91,94,95
Mvav 244,136,32
Ta 156,22,32
Ta 158,22,5152
W 4
Mvav 176,144,48
Ta 128,25,30784
Ta 130,25,35904
Mvav 176,148,56
Ta 132,21,64
Ta 134,21,5184
Mvav 184,148,88
D 19,40,94,3072
Ta 136,26,30816
Mvav 184,144,80
s_add_u32 m0, m0, 4096
s_nop 0
buffer_load_dwordx4 v20, s[40:43], s94 offen lds
s_waitcnt vmcnt(8)
v_cmp_eq_u64 vcc, s[12:13], v[28:29]
s_cmp_eq_u64 vcc, -1
s_cbranch_scc0 .Lspin_98%=
.Lok_99%=:
W 5
Mvav 188,152,80
s_add_u32 s81, s58, 0
s_and_b32 s81, s81, 3
s_lshl_b32 s81, s81, 12
Mvav 188,156,88
s_add_u32 s81, s81, s49
Ta 138,26,35936
Ta 140,22,96
Mvav 180,156,56
s_lshl_b32 s66, s14, 1
Ta 142,22,5216
Ta 144,25,30848
Mvav 180,152,48
W 5
buffer_store_dwordx4 v[88:91], v1, s[8:11], s67 offen offset:3072 sc1
Ta 146,25,35968
Mvav 192,128,48
s_add_u32 s66, s66, 0
Ta 148,21,128
Ta 150,21,5248
Mvav 192,132,56
s_cmp_eq_u32 s66, s59
s_cselect_b32 s66, 134217728, 0
Ta 152,26,30880
Ta 154,26,36000
Mvav 200,132,88
s_add_u32 s28, s52, s66
Ta 156,22,160
Ta 158,22,5280
Mvav 200,128,80
W 8
s_lshl_b32 s66, s14, 1
buffer_load_dwordx4 a[184:187], v1, s[8:11], s81 offen offset:0 sc1
Mvav 204,136,80
s_add_u32 s82, s81, 131072
Ta 128,25,30912
Ta 130,25,36032
Mvav 204,140,88
s_add_u32 s66, s66, 1
s_cmp_eq_u32 s66, s59
s_cselect_b32 s66, 134217728, 0
Ta 132,21,192
Mvav 196,140,56
s_add_u32 s29, s52, s66
Ta 134,21,5312
Mvav 196,136,48
W 8
buffer_load_dwordx4 a[200:203], v1, s[8:11], s82 offen offset:0 sc1
Ta 136,26,30944
Mvav 208,144,48
Ta 138,26,36064
Mvav 208,148,56
Ta 140,22,224
Ta 142,22,5344
Mvav 216,148,88
Mvav 216,144,80
W 8
buffer_load_dwordx4 a[188:191], v1, s[8:11], s81 offen offset:1024 sc1
Ta 144,25,30976
Mvav 220,152,80
Ta 146,25,36096
Mvav 220,156,88
Ta 148,21,256
Ta 150,21,5376
Mvav 212,156,56
Mvav 212,152,48
W 8
buffer_load_dwordx4 a[204:207], v1, s[8:11], s82 offen offset:1024 sc1
Ta 152,26,31008
Mvav 224,128,48
Ta 154,26,36128
Mvav 224,132,56
Ta 156,22,288
Ta 158,22,5408
Mvav 232,132,88
Mvav 232,128,80
W 8
buffer_load_dwordx4 a[192:195], v1, s[8:11], s81 offen offset:2048 sc1
Ta 128,25,10240
Mvav 236,136,80
Ta 130,25,15360
Mvav 236,140,88
Ta 132,25,20480
Ta 134,25,25600
Mvav 228,140,56
Mvav 228,136,48
W 8
buffer_load_dwordx4 a[208:211], v1, s[8:11], s82 offen offset:2048 sc1
Ta 136,26,10272
Mvav 240,144,48
Ta 138,26,15392
Mvav 240,148,56
Ta 140,26,20512
Ta 142,26,25632
Mvav 248,148,88
Mvav 248,144,80
W 8
buffer_load_dwordx4 a[196:199], v1, s[8:11], s81 offen offset:3072 sc1
Ta 144,25,10304
Mvav 252,152,80
Ta 146,25,15424
Mvav 252,156,88
Ta 148,25,20544
Ta 150,25,25664
Mvav 244,156,56
Mvav 244,152,48
s_waitcnt vmcnt(3) lgkmcnt(8)
buffer_load_dwordx4 a[212:215], v1, s[8:11], s82 offen offset:3072 sc1
Ta 152,26,10336
Zvaa 96,128,184
Ta 154,26,15456
Mvaa 96,132,188
Ta 156,26,20576
Ta 158,26,25696
Zvaa 104,132,204
Mvaa 104,128,200
W 8
Ta 128,25,10368
Ta 130,25,15488
Zvaa 108,136,200
Mvaa 108,140,204
Ta 132,25,20608
Ta 134,25,25728
Zvaa 100,140,188
s_lshl_b32 s92, s58, 2
s_add_u32 s91, s92, s51
s_add_u32 s95, s91, 11
Mvaa 100,136,184
W 8
s_and_b32 s95, s95, 15
s_mul_i32 s95, s95, 40960
Zvaa 112,144,184
s_mul_i32 s71, s51, 10240
s_add_u32 s71, s71, 40960
s_add_u32 m0, s71, s48
Mvaa 112,148,188
s_lshl2_add_u32 s94, s48, s95
Ta 136,26,10400
Ta 138,26,15520
Zvaa 120,148,204
Ta 140,26,20640
Ta 142,26,25760
Mvaa 120,144,200
W 8
Ta 144,25,10432
Ta 146,25,15552
Zvaa 124,152,200
Mvaa 124,156,204
Ta 148,25,20672
Ta 150,25,25792
Zvaa 116,156,188
Mvaa 116,152,184
W 8
Ta 152,26,10464
Ta 154,26,15584
Zvaa 128,128,184
s_lshl_b32 s80, s58, 3
s_add_u32 s80, s80, s55
v_mov_b64 v[28:29], s[12:13]
s_mov_b64 exec, 1
buffer_store_dwordx2 v[28:29], off, s[8:11], s80 sc1
s_mov_b64 exec, -1
Mvaa 128,132,188
Ta 156,26,20704
Ta 158,26,25824
Zvaa 136,132,204
Mvaa 136,128,200
W 8
Ta 128,25,10496
Ta 130,25,15616
Zvaa 140,136,200
s_barrier
Mvaa 140,140,204
D 16,40,94,0
Ta 132,25,20736
Zvaa 132,140,188
)"
R"(s_add_u32 s95, s91, 13
s_and_b32 s95, s95, 15
s_mul_i32 s95, s95, 40960
Mvaa 132,136,184
W 7
s_mul_i32 s71, s51, 20480
s_add_u32 s71, s71, 71680
Zvaa 144,144,184
Ta 134,25,25856
Ta 136,26,10528
Mvaa 144,148,188
Ta 138,26,15648
D 17,40,94,1024
Zvaa 152,148,204
Ta 140,26,20768
Ta 142,26,25888
Mvaa 152,144,200
W 8
Ta 144,25,30720
Ta 146,25,35840
Zvaa 156,152,200
Mvaa 156,156,204
D 18,40,94,2048
Ta 148,21,0
Zvaa 148,156,188
Ta 150,21,5120
Mvaa 148,152,184
W 8
Ta 152,26,30752
Ta 154,26,35872
Zvaa 160,128,184
Mvaa 160,132,188
D 19,40,94,3072
Ta 156,22,32
Zvaa 168,132,204
s_add_u32 m0, m0, 4096
Ta 158,22,5152
Mvaa 168,128,200
W 8
Ta 128,25,30784
Ta 130,25,35904
Zvaa 172,136,200
Mvaa 172,140,204
buffer_load_dwordx4 v20, s[40:43], s94 offen lds
Ta 132,21,64
Zvaa 164,140,188
s_add_u32 s81, s58, 1
s_and_b32 s81, s81, 3
s_lshl_b32 s81, s81, 12
Mvaa 164,136,184
s_waitcnt vmcnt(6) lgkmcnt(7)
s_add_u32 s81, s81, s49
Ta 134,21,5184
Mvaa 96,144,192
Ta 136,26,30816
Ta 138,26,35936
Mvaa 96,148,196
buffer_load_dwordx4 a[216:219], v1, s[8:11], s81 offen offset:0 sc1
Ta 140,22,96
Mvaa 104,148,212
s_add_u32 s82, s81, 131072
Ta 142,22,5216
Mvaa 104,144,208
W 8
Ta 144,25,30848
Ta 146,25,35968
Mvaa 108,152,208
Mvaa 108,156,212
buffer_load_dwordx4 a[232:235], v1, s[8:11], s82 offen offset:0 sc1
Ta 148,21,128
Mvaa 100,156,196
Ta 150,21,5248
Mvaa 100,152,192
W 8
Ta 152,26,30880
Ta 154,26,36000
Mvaa 112,128,192
Mvaa 112,132,196
buffer_load_dwordx4 a[220:223], v1, s[8:11], s81 offen offset:1024 sc1
Ta 156,22,160
Mvaa 120,132,212
Ta 158,22,5280
Mvaa 120,128,208
W 8
Ta 128,25,30912
Ta 130,25,36032
Mvaa 124,136,208
Mvaa 124,140,212
buffer_load_dwordx4 a[236:239], v1, s[8:11], s82 offen offset:1024 sc1
Ta 132,21,192
Mvaa 116,140,196
Ta 134,21,5312
Mvaa 116,136,192
W 8
Ta 136,26,30944
Ta 138,26,36064
Mvaa 128,144,192
Mvaa 128,148,196
buffer_load_dwordx4 a[224:227], v1, s[8:11], s81 offen offset:2048 sc1
Ta 140,22,224
Mvaa 136,148,212
Ta 142,22,5344
Mvaa 136,144,208
W 8
Ta 144,25,30976
Ta 146,25,36096
Mvaa 140,152,208
Mvaa 140,156,212
buffer_load_dwordx4 a[240:243], v1, s[8:11], s82 offen offset:2048 sc1
Ta 148,21,256
Mvaa 132,156,196
Ta 150,21,5376
Mvaa 132,152,192
W 8
Ta 152,26,31008
Ta 154,26,36128
Mvaa 144,128,192
Mvaa 144,132,196
buffer_load_dwordx4 a[228:231], v1, s[8:11], s81 offen offset:3072 sc1
Ta 156,22,288
Mvaa 152,132,212
Ta 158,22,5408
Mvaa 152,128,208
W 8
Ta 128,21,10240
Ta 130,21,15360
Mvaa 156,136,208
Mvaa 156,140,212
buffer_load_dwordx4 a[244:247], v1, s[8:11], s82 offen offset:3072 sc1
Ta 132,21,20480
Mvaa 148,140,196
s_add_u32 m0, s71, s48
s_lshl2_add_u32 s94, s48, s95
Ta 134,21,25600
Mvaa 148,136,192
W 8
Ta 136,22,10272
Ta 138,22,15392
Mvaa 160,144,192
Mvaa 160,148,196
D 16,40,94,0
Ta 140,22,20512
Mvaa 168,148,212
s_add_u32 s95, s92, 15
s_and_b32 s95, s95, 15
s_mul_i32 s95, s95, 40960
Mvaa 168,144,208
W 7
s_mul_i32 s71, s51, 0
s_add_u32 s71, s71, 102400
Mvaa 172,152,208
Ta 142,22,25632
Ta 144,21,10304
Mvaa 172,156,212
D 17,40,94,1024
Ta 146,21,15424
Mvaa 164,156,196
Ta 148,21,20544
Ta 150,21,25664
Mvaa 164,152,192
s_waitcnt vmcnt(6) lgkmcnt(8)
Ta 152,22,10336
Ta 154,22,15456
Mvaa 96,128,216
Mvaa 96,132,220
D 18,40,94,2048
Ta 156,22,20576
Mvaa 104,132,236
Ta 158,22,25696
Mvaa 104,128,232
W 8
Ta 128,21,10368
Ta 130,21,15488
Mvaa 108,136,232
Mvaa 108,140,236
D 19,40,94,3072
Ta 132,21,20608
Mvaa 100,140,220
s_add_u32 m0, m0, 4096
Ta 134,21,25728
Mvaa 100,136,216
W 8
Ta 136,22,10400
Ta 138,22,15520
Mvaa 112,144,216
Mvaa 112,148,220
buffer_load_dwordx4 v20, s[40:43], s94 offen lds
Ta 140,22,20640
Mvaa 120,148,236
s_add_u32 m0, s71, s48
s_lshl2_add_u32 s94, s48, s95
Ta 142,22,25760
Mvaa 120,144,232
W 8
Ta 144,21,10432
Ta 146,21,15552
Mvaa 124,152,232
Mvaa 124,156,236
D 16,40,94,0
Ta 148,21,20672
Mvaa 116,156,220
D 17,40,94,1024
D 18,40,94,2048
D 19,40,94,3072
s_add_u32 m0, m0, 4096
s_nop 0
buffer_load_dwordx4 v20, s[40:43], s94 offen lds
Mvaa 116,152,216
W 7
s_add_u32 s81, s58, 2
s_and_b32 s81, s81, 3
Mvaa 128,128,216
s_lshl_b32 s81, s81, 12
s_add_u32 s81, s81, s49
Ta 150,21,25792
Mvaa 128,132,220
Ta 152,22,10464
Ta 154,22,15584
Mvaa 136,132,236
Ta 156,22,20704
Ta 158,22,25824
Mvaa 136,128,232
W 8
Ta 128,21,10496
Ta 130,21,15616
Mvaa 140,136,232
s_barrier
Mvaa 140,140,236
buffer_load_dwordx4 a[0:3], v1, s[8:11], s81 offen offset:0 sc1
v_and_b32 v27, 1, v12
Mvaa 132,140,220
s_add_u32 s82, s81, 131072
Ta 132,21,20736
Ta 134,21,25856
Mvaa 132,136,216
W 8
Ta 136,22,10528
v_lshlrev_b32 v27, 1, v27
Mvaa 144,144,216
Ta 138,22,15648
Mvaa 144,148,220
buffer_load_dwordx4 a[16:19], v1, s[8:11], s82 offen offset:0 sc1
v_xor_b32 v27, v27, v13
Mvaa 152,148,236
Ta 140,22,20768
Ta 142,22,25888
Mvaa 152,144,232
W 8
Ta 144,21,30720
v_and_b32 v12, 2, v12
Mvaa 156,152,232
Ta 146,21,35840
Mvaa 156,156,236
buffer_load_dwordx4 a[4:7], v1, s[8:11], s81 offen offset:1024 sc1
Ta 148,23,0
Mvaa 148,156,220
Ta 150,23,5120
v_or_b32 v12, v12, v14
Mvaa 148,152,216
W 8
Ta 152,22,30752
Ta 154,22,35872
Mvaa 160,128,216
v_lshl_or_b32 v12, v27, 2, v12
Mvaa 160,132,220
buffer_load_dwordx4 a[20:23], v1, s[8:11], s82 offen offset:1024 sc1
Ta 156,24,32
Mvaa 168,132,236
Ta 158,24,5152
v_cmp_eq_u32_e64 s[86:87], 0, v12
Mvaa 168,128,232
W 8
Ta 128,21,30784
Ta 130,21,35904
Mvaa 172,136,232
Mvaa 172,140,236
buffer_load_dwordx4 a[8:11], v1, s[8:11], s81 offen offset:2048 sc1
v_cndmask_b32_e64 v4, 0, v15, s[86:87]
Mvaa 164,140,220
Ta 132,23,64
Ta 134,23,5184
Mvaa 164,136,216
s_waitcnt vmcnt(15) lgkmcnt(8)
Ta 136,22,30816
v_cmp_eq_u32_e64 s[86:87], 1, v12
Mvaa 96,144,224
Ta 138,22,35936
Mvaa 96,148,228
buffer_load_dwordx4 a[24:27], v1, s[8:11], s82 offen offset:2048 sc1
v_cndmask_b32_e64 v5, 0, v15, s[86:87]
Mvaa 104,148,244
Ta 140,24,96
Ta 142,24,5216
Mvaa 104,144,240
W 8
Ta 144,21,30848
Ta 146,21,35968
Mvaa 108,152,240
v_cmp_eq_u32_e64 s[86:87], 2, v12
Mvaa 108,156,244
buffer_load_dwordx4 a[12:15], v1, s[8:11], s81 offen offset:3072 sc1
Ta 148,23,128
Mvaa 100,156,228
Ta 150,23,5248
v_cndmask_b32_e64 v6, 0, v15, s[86:87]
Mvaa 100,152,224
W 8
Ta 152,22,30880
Ta 154,22,36000
Mvaa 112,128,224
v_cmp_eq_u32_e64 s[86:87], 3, v12
Mvaa 112,132,228
buffer_load_dwordx4 a[28:31], v1, s[8:11], s82 offen offset:3072 sc1
Ta 156,24,160
Mvaa 120,132,244
s_add_u32 s81, s58, 3
s_and_b32 s81, s81, 3
s_lshl_b32 s81, s81, 12
Mvaa 120,128,240
W 7
s_add_u32 s81, s81, s49
Ta 158,24,5280
Mvaa 124,136,240
Ta 128,21,30912
v_cndmask_b32_e64 v7, 0, v15, s[86:87]
Mvaa 124,140,244
Ta 130,21,36032
buffer_load_dwordx4 a[32:35], v1, s[8:11], s81 offen offset:0 sc1
Mvaa 116,140,228
s_add_u32 s82, s81, 131072
Ta 132,23,192
v_cmp_eq_u32_e64 s[86:87], 4, v12
Mvaa 116,136,224
W 7
Ta 134,23,5312
v_cndmask_b32_e64 v8, 0, v15, s[86:87]
Mvaa 128,144,224
Ta 136,22,30944
Ta 138,22,36064
Mvaa 128,148,228
buffer_load_dwordx4 a[48:51], v1, s[8:11], s82 offen offset:0 sc1
Ta 140,24,224
Mvaa 136,148,244
Ta 142,24,5344
v_cmp_eq_u32_e64 s[86:87], 5, v12
Mvaa 136,144,240
W 8
Ta 144,21,30976
Ta 146,21,36096
Mvaa 140,152,240
s_waitcnt vmcnt(10)
s_barrier
Mvaa 140,156,244
buffer_load_dwordx4 a[36:39], v1, s[8:11], s81 offen offset:1024 sc1
v_cndmask_b32_e64 v9, 0, v15, s[86:87]
Mvaa 132,156,228
Ta 148,23,256
Ta 150,23,5376
Mvaa 132,152,224
W 8
Ta 152,22,31008
v_cmp_eq_u32_e64 s[86:87], 6, v12
Mvaa 144,128,224
Ta 154,22,36128
Mvaa 144,132,228
buffer_load_dwordx4 a[52:55], v1, s[8:11], s82 offen offset:1024 sc1
v_cndmask_b32_e64 v10, 0, v15, s[86:87]
Mvaa 152,132,244
Ta 156,24,288
Ta 158,24,5408
Mvaa 152,128,240
W 8
Ta 128,23,20480
v_cmp_eq_u32_e64 s[86:87], 7, v12
Mvaa 156,136,240
Ta 130,23,25600
Mvaa 156,140,244
buffer_load_dwordx4 a[40:43], v1, s[8:11], s81 offen offset:2048 sc1
v_cndmask_b32_e64 v11, 0, v15, s[86:87]
Mvaa 148,140,228
Ta 132,23,51200
Ta 134,23,56320
Mvaa 148,136,224
W 8
Ta 136,24,20512
Ta 138,24,25632
Mvaa 160,144,224
s_mov_b32 s20, s38
Mvaa 160,148,228
buffer_load_dwordx4 a[56:59], v1, s[8:11], s82 offen offset:2048 sc1
Ta 140,24,51232
Mvaa 168,148,244
s_and_b32 s21, s39, 0xffff
Ta 142,24,56352
Mvaa 168,144,240
W 8
Ta 144,23,20544
Ta 146,23,25664
Mvaa 172,152,240
s_mov_b32 s22, 2621440
)"
R"(Mvaa 172,156,244
buffer_load_dwordx4 a[44:47], v1, s[8:11], s81 offen offset:3072 sc1
Ta 148,23,51264
Mvaa 164,156,228
Ta 150,23,56384
Mvaa 164,152,224
s_waitcnt vmcnt(11) lgkmcnt(8)
s_mov_b32 s23, 0x20000
Ta 152,24,20576
Mvaa 96,128,0
Ta 154,24,25696
Mvaa 96,132,4
s_and_b32 s17, s17, 0xffff
buffer_load_dwordx4 a[60:63], v1, s[8:11], s82 offen offset:3072 sc1
Ta 156,24,51296
Mvaa 104,132,20
Ta 158,24,56416
Mvaa 104,128,16
W 8
s_mov_b32 s18, 2621440
Ta 128,23,20608
Mvaa 108,136,16
Ta 130,23,25728
Mvaa 108,140,20
Ta 132,23,51328
Ta 134,23,56448
Mvaa 100,140,4
s_mov_b32 s19, 0x20000
Mvaa 100,136,0
W 8
Ta 136,24,20640
Ta 138,24,25760
Mvaa 112,144,0
s_lshr_b32 s66, s15, 3
v_bfe_u32 v28, v1, 4, 2
Mvaa 112,148,4
Ta 140,24,51360
v_lshlrev_b32 v28, 3, v28
Mvaa 120,148,20
s_lshl_b32 s66, s66, 4
Ta 142,24,56480
buffer_load_dwordx2 v[28:29], v28, s[8:11], s55 offen sc1
Mvaa 120,144,16
W 8
Ta 144,23,20672
Ta 146,23,25792
Mvaa 124,152,16
Mvaa 124,156,20
s_add_u32 s66, s66, s59
Ta 148,23,51392
Ta 150,23,56512
Mvaa 116,156,4
Mvaa 116,152,0
W 8
s_lshl_b32 s63, s66, 6
Ta 152,24,20704
Mvaa 128,128,0
Ta 154,24,25824
Mvaa 128,132,4
s_add_u32 s63, s63, 65536
Ta 156,24,51424
Ta 158,24,56544
Mvaa 136,132,20
Mvaa 136,128,16
W 8
s_mul_i32 s62, s66, 40960
Ta 128,23,20736
Mvaa 140,136,16
Ta 130,23,25856
Mvaa 140,140,20
Ta 132,23,51456
Ta 134,23,56576
Mvaa 132,140,4
s_add_u32 s62, s62, 56623104
Mvaa 132,136,0
W 8
Ta 136,24,20768
Ta 138,24,25888
Mvaa 144,144,0
s_lshl_b32 s66, s14, 13
Mvaa 144,148,4
Ta 140,24,51488
Ta 142,24,56608
Mvaa 152,148,20
s_add_u32 s78, s62, s66
Mvaa 152,144,16
W 8
Ta 144,25,0
Ta 146,25,5120
Mvaa 156,152,16
Mvaa 156,156,20
s_add_u32 s79, s78, 327680
Ta 148,21,40960
Ta 150,21,46080
Mvaa 148,156,4
Mvaa 148,152,0
W 8
s_and_b32 s66, s14, 1
Ta 152,26,32
Mvaa 160,128,0
Ta 154,26,5152
Mvaa 160,132,4
s_mul_i32 s66, s66, 327680
Ta 156,22,40992
Ta 158,22,46112
Mvaa 168,132,20
Mvaa 168,128,16
W 8
Ta 128,25,64
Ta 130,25,5184
Mvaa 172,136,16
s_add_u32 s65, s62, s66
Mvaa 172,140,20
Ta 132,21,41024
Ta 134,21,46144
Mvaa 164,140,4
s_add_u32 s65, s65, 32768
Mvaa 164,136,0
s_waitcnt vmcnt(9) lgkmcnt(8)
Ta 136,26,96
Ta 138,26,5216
Mvaa 96,144,8
s_mov_b32 s64, 0
Mvaa 96,148,12
Ta 140,22,41056
Ta 142,22,46176
Mvaa 104,148,28
Mvaa 104,144,24
W 8
Ta 144,25,128
v_add_u32 v19, 4096, v1
Mvaa 108,152,24
Ta 146,25,5248
Mvaa 108,156,28
Ta 148,21,41088
v_bfe_u32 v27, v1, 4, 3
Mvaa 100,156,12
Ta 150,21,46208
Mvaa 100,152,8
W 8
Ta 152,26,160
v_lshlrev_b32 v27, 3, v27
Mvaa 112,128,8
Ta 154,26,5280
Mvaa 112,132,12
Ta 156,22,41120
Ta 158,22,46240
Mvaa 120,132,28
s_waitcnt vmcnt(0)
v_cmp_eq_u64 vcc, s[12:13], v[28:29]
s_cmp_eq_u64 vcc, -1
s_cbranch_scc0 .Lspin_101%=
.Lok_102%=:
s_add_u32 s81, s58, 1
v_bfe_u32 v18, v1, 7, 1
Mvaa 120,128,24
W 8
s_and_b32 s81, s81, 3
s_lshl_b32 s81, s81, 12
Mvaa 124,136,24
s_add_u32 s81, s81, s50
buffer_load_dwordx4 a[64:67], v1, s[8:11], s81 offen offset:0 sc1
Ta 128,25,192
Mvaa 124,140,28
s_add_u32 s82, s81, 131072
s_add_u32 s66, s14, 1
Ta 130,25,5312
Mvaa 116,140,12
s_lshr_b32 s66, s66, 2
Ta 132,21,41152
Ta 134,21,46272
Mvaa 116,136,8
W 8
Ta 136,26,224
Ta 138,26,5344
Mvaa 128,144,8
buffer_load_dwordx4 a[80:83], v1, s[8:11], s82 offen offset:0 sc1
Mvaa 128,148,12
s_mul_i32 s66, s66, 0
Ta 140,22,41184
Ta 142,22,46304
Mvaa 136,148,28
Mvaa 136,144,24
W 8
s_sub_u32 s66, 512, s66
Ta 144,25,256
Mvaa 140,152,24
Ta 146,25,5376
Mvaa 140,156,28
buffer_load_dwordx4 a[68:71], v1, s[8:11], s81 offen offset:1024 sc1
v_mul_u32_u24 v18, s66, v18
Mvaa 132,156,12
Ta 148,21,41216
Ta 150,21,46336
Mvaa 132,152,8
W 8
Ta 152,26,288
Ta 154,26,5408
Mvaa 144,128,8
v_add_u32 v18, v18, v27
Mvaa 144,132,12
Ta 156,22,41248
Ta 158,22,46368
Mvaa 152,132,28
s_mov_b32 s71, 0
buffer_load_dwordx4 a[84:87], v1, s[8:11], s82 offen offset:1024 sc1
Mvaa 152,128,24
W 8
Ta 128,21,51200
Ta 130,21,56320
Mvaa 156,136,24
v_bfe_u32 v20, v1, 4, 4
Mvaa 156,140,28
Ta 132,23,10240
Ta 134,23,15360
Mvaa 148,140,12
Mvaa 148,136,8
W 8
buffer_load_dwordx4 a[72:75], v1, s[8:11], s81 offen offset:2048 sc1
v_mul_u32_u24 v20, 1280, v20
Mvaa 160,144,8
Ta 136,23,30720
Ta 138,23,35840
Mvaa 160,148,12
Ta 140,23,40960
v_lshrrev_b32 v12, 8, v1
Mvaa 168,148,28
Ta 142,23,46080
Mvaa 168,144,24
W 8
Ta 144,22,51232
v_lshl_add_u32 v20, v12, 4, v20
Mvaa 172,152,24
Ta 146,22,56352
buffer_load_dwordx4 a[88:91], v1, s[8:11], s82 offen offset:2048 sc1
Mvaa 172,156,28
Ta 148,24,10272
Ta 150,24,15392
Mvaa 164,156,12
s_bfe_u32 s66, s59, 0x10001
Mvaa 164,152,8
W 8
Ta 152,24,30752
Ta 154,24,35872
Mvaa 96,128,32
s_lshl_b32 s67, s57, 1
Mvaa 96,132,36
buffer_load_dwordx4 a[76:79], v1, s[8:11], s81 offen offset:3072 sc1
Ta 156,24,40992
Mvaa 104,132,52
s_add_u32 s66, s66, s67
Ta 158,24,46112
Mvaa 104,128,48
W 8
Ta 128,21,51264
Ta 130,21,56384
Mvaa 104,136,56
Mvaa 104,140,60
s_lshl_b32 s61, s66, 2
Ta 132,23,10304
Ta 134,23,15424
Mvaa 96,140,44
buffer_load_dwordx4 a[92:95], v1, s[8:11], s82 offen offset:3072 sc1
Mvaa 96,136,40
W 8
s_add_u32 s81, s58, 2
s_and_b32 s81, s81, 3
Mvaa 100,144,32
s_lshl_b32 s81, s81, 12
s_add_u32 s81, s81, s50
s_lshr_b32 s66, s59, 2
Mvaa 100,148,36
s_lshl_b32 s66, s66, 1
Ta 136,23,30784
Ta 138,23,35904
Mvaa 108,148,52
Ta 140,23,41024
Ta 142,23,46144
Mvaa 108,144,48
W 8
buffer_load_dwordx4 a[96:99], v1, s[8:11], s81 offen offset:0 sc1
Ta 144,22,51296
Mvaa 108,152,56
s_add_u32 s82, s81, 131072
s_add_u32 s61, s61, s66
Ta 146,22,56416
Mvaa 108,156,60
Ta 148,24,10336
Ta 150,24,15456
Mvaa 100,156,44
s_and_b32 s66, s59, 1
Mvaa 100,152,40
W 8
Ta 152,24,30816
Ta 154,24,35936
Mvaa 112,128,32
s_add_u32 s61, s61, s66
buffer_load_dwordx4 a[112:115], v1, s[8:11], s82 offen offset:0 sc1
Mvaa 112,132,36
Ta 156,24,41056
Ta 158,24,46176
Mvaa 120,132,52
s_xor_b32 s67, s61, 0
K 96,96,97
K 97,98,99
Mvaa 120,128,48
W 8
Ta 128,21,51328
Ta 130,21,56448
Mvaa 120,136,56
K 98,100,101
Mvaa 120,140,60
s_mul_i32 s72, s67, 20480
buffer_load_dwordx4 a[100:103], v1, s[8:11], s81 offen offset:1024 sc1
K 99,102,103
Mvaa 112,140,44
Ta 132,23,10368
Ta 134,23,15488
Mvaa 112,136,40
W 8
s_add_u32 s83, s28, 0
s_mul_i32 s66, s58, 320
Mvaa 116,144,32
Ta 136,23,30848
Ta 138,23,35968
Mvaa 116,148,36
s_add_u32 s72, s72, s66
buffer_store_dwordx4 v[96:99], v1, s[8:11], s83 offen sc1
Ta 140,23,41088
Mvaa 124,148,52
Ta 142,23,46208
K 104,104,105
Mvaa 124,144,48
W 8
Ta 144,22,51360
K 105,106,107
Mvaa 124,152,56
s_mul_i32 s75, s67, 20480
Ta 146,22,56480
Mvaa 124,156,60
buffer_load_dwordx4 a[116:119], v1, s[8:11], s82 offen offset:1024 sc1
K 106,108,109
Mvaa 116,156,44
s_mul_i32 s66, s58, 2560
Ta 148,24,10400
Ta 150,24,15520
Mvaa 116,152,40
W 8
Ta 152,24,30880
K 107,110,111
Mvaa 128,128,32
s_add_u32 s83, s29, 40960
s_add_u32 s75, s75, s66
Ta 154,24,36000
Mvaa 128,132,36
buffer_load_dwordx4 a[104:107], v1, s[8:11], s81 offen offset:2048 sc1
Ta 156,24,41120
Mvaa 136,132,52
Ta 158,24,46240
Mvaa 136,128,48
W 8
s_add_u32 s75, s75, 131072
Ta 128,21,51392
Mvaa 136,136,56
Ta 130,21,56512
Mvaa 136,140,60
s_xor_b32 s67, s61, 124
buffer_store_dwordx4 v[104:107], v1, s[8:11], s83 offen sc1
K 112,112,113
Mvaa 128,140,44
Ta 132,23,10432
K 113,114,115
Mvaa 128,136,40
W 7
Ta 134,23,15552
K 114,116,117
Mvaa 132,144,32
s_mul_i32 s73, s67, 20480
Ta 136,23,30912
Ta 138,23,36032
Mvaa 132,148,36
buffer_load_dwordx4 a[120:123], v1, s[8:11], s82 offen offset:2048 sc1
K 115,118,119
Mvaa 140,148,52
s_add_u32 s83, s28, 8192
Ta 140,23,41152
Ta 142,23,46272
Mvaa 140,144,48
W 8
s_mul_i32 s66, s58, 320
Ta 144,22,51424
Mvaa 140,152,56
s_add_u32 s73, s73, s66
Ta 146,22,56544
Mvaa 140,156,60
buffer_load_dwordx4 a[108:111], v1, s[8:11], s81 offen offset:3072 sc1
Ta 148,24,10464
Mvaa 132,156,44
s_mul_i32 s76, s67, 20480
Ta 150,24,15584
)"
R"(Mvaa 132,152,40
W 8
Ta 152,24,30944
Ta 154,24,36064
Mvaa 144,128,32
Mvaa 144,132,36
s_mul_i32 s66, s58, 2560
buffer_store_dwordx4 v[112:115], v1, s[8:11], s83 offen sc1
K 120,120,121
Mvaa 152,132,52
K 121,122,123
K 122,124,125
Mvaa 152,128,48
W 6
Ta 156,24,41184
K 123,126,127
Mvaa 152,136,56
s_add_u32 s83, s29, 49152
s_add_u32 s76, s76, s66
Ta 158,24,46304
Mvaa 152,140,60
s_add_u32 s76, s76, 131072
buffer_load_dwordx4 a[124:127], v1, s[8:11], s82 offen offset:3072 sc1
Ta 128,21,51456
Mvaa 144,140,44
s_add_u32 s81, s58, 3
s_and_b32 s81, s81, 3
s_lshl_b32 s81, s81, 12
Mvaa 144,136,40
W 5
s_add_u32 s81, s81, s50
Ta 130,21,56576
Mvaa 148,144,32
Ta 132,23,10496
Ta 134,23,15616
Mvaa 148,148,36
buffer_load_dwordx4 a[184:187], v1, s[8:11], s81 offen offset:0 sc1
Ta 136,23,30976
Mvaa 156,148,52
s_add_u32 s82, s81, 131072
Ta 138,23,36096
v_bfe_u32 v15, v1, 7, 1
Mvaa 156,144,48
W 6
Ta 140,23,41216
Ta 142,23,46336
Mvaa 156,152,56
Ta 144,22,51488
v_mul_u32_u24 v14, 10240, v15
Mvaa 156,156,60
buffer_store_dwordx4 v[120:123], v1, s[8:11], s83 offen sc1
K 128,128,129
Mvaa 148,156,44
K 129,130,131
K 130,132,133
Mvaa 148,152,40
W 5
Ta 146,22,56608
K 131,134,135
Mvaa 160,128,32
s_add_u32 s83, s28, 16384
Ta 148,24,10528
v_lshrrev_b32 v15, 8, v1
Mvaa 160,132,36
buffer_load_dwordx4 a[200:203], v1, s[8:11], s82 offen offset:0 sc1
Ta 150,24,15648
Mvaa 168,132,52
Ta 152,24,31008
v_lshl_add_u32 v14, v15, 7, v14
Mvaa 168,128,48
W 5
Ta 154,24,36128
v_and_b32 v15, 0x70, v1
Mvaa 168,136,56
Ta 156,24,41248
Ta 158,24,46368
Mvaa 168,140,60
buffer_load_dwordx4 a[188:191], v1, s[8:11], s81 offen offset:1024 sc1
v_add_u32 v14, v14, v15
Mvaa 160,140,44
s_bitcmp1_b32 s14, 0
s_cselect_b32 s74, s73, s72
s_cselect_b32 s77, s76, s75
Ta 128,21,10240
Ta 130,21,15360
Mvaa 160,136,40
W 6
Ta 132,21,20480
Ta 134,21,25600
Mvaa 164,144,32
s_add_u32 s74, s74, 256
Ta 136,22,10272
Ta 138,22,15392
Mvaa 164,148,36
buffer_store_dwordx4 v[128:131], v1, s[8:11], s83 offen sc1
K 136,136,137
Mvaa 172,148,52
s_add_u32 s77, s77, 2048
K 137,138,139
K 138,140,141
Mvaa 172,144,48
W 6
Ta 140,22,20512
K 139,142,143
Mvaa 172,152,56
s_add_u32 s83, s29, 57344
Ta 142,22,25632
Ta 144,21,10304
Mvaa 172,156,60
s_lshl_b32 s66, s14, 6
buffer_load_dwordx4 a[204:207], v1, s[8:11], s82 offen offset:1024 sc1
Ta 146,21,15424
Mvaa 164,156,44
Ta 148,21,20544
Ta 150,21,25664
Mvaa 164,152,40
s_waitcnt vmcnt(21) lgkmcnt(8)
s_lshl_b32 s67, s14, 9
Ta 152,22,10336
Mvaa 176,128,64
Ta 154,22,15456
Mvaa 176,132,68
s_add_u32 s72, s72, s66
buffer_load_dwordx4 a[192:195], v1, s[8:11], s81 offen offset:2048 sc1
Ta 156,22,20576
Mvaa 184,132,84
Ta 158,22,25696
Mvaa 184,128,80
W 8
Ta 128,21,10368
Ta 130,21,15488
Mvaa 188,136,80
s_add_u32 s75, s75, s67
Mvaa 188,140,84
buffer_store_dwordx4 v[136:139], v1, s[8:11], s83 offen sc1
K 144,144,145
Mvaa 180,140,68
s_add_u32 s73, s73, s66
K 145,146,147
K 146,148,149
Mvaa 180,136,64
W 6
Ta 132,21,20608
K 147,150,151
Mvaa 192,144,64
s_add_u32 s83, s28, 24576
Ta 134,21,25728
Ta 136,22,10400
Mvaa 192,148,68
s_add_u32 s76, s76, s67
buffer_load_dwordx4 a[208:211], v1, s[8:11], s82 offen offset:2048 sc1
Ta 138,22,15520
Mvaa 200,148,84
Ta 140,22,20640
Ta 142,22,25760
Mvaa 200,144,80
W 8
Ta 144,21,10432
Ta 146,21,15552
Mvaa 204,152,80
Mvaa 204,156,84
buffer_store_dwordx4 v[144:147], v1, s[8:11], s83 offen sc1
K 152,152,153
Mvaa 196,156,68
K 153,154,155
K 154,156,157
Mvaa 196,152,64
W 6
Ta 148,21,20672
K 155,158,159
Mvaa 208,128,64
s_add_u32 s83, s29, 65536
Ta 150,21,25792
Ta 152,22,10464
Mvaa 208,132,68
buffer_load_dwordx4 a[196:199], v1, s[8:11], s81 offen offset:3072 sc1
Ta 154,22,15584
Mvaa 216,132,84
Ta 156,22,20704
Ta 158,22,25824
Mvaa 216,128,80
W 8
Ta 128,21,10496
Ta 130,21,15616
Mvaa 220,136,80
Mvaa 220,140,84
buffer_load_dwordx4 a[212:215], v1, s[8:11], s82 offen offset:3072 sc1
Ta 132,21,20736
Mvaa 212,140,68
Ta 134,21,25856
Mvaa 212,136,64
W 8
Ta 136,22,10528
Ta 138,22,15648
Mvaa 224,144,64
Mvaa 224,148,68
buffer_store_dwordx4 v[152:155], v1, s[8:11], s83 offen sc1
K 160,160,161
Mvaa 232,148,84
K 161,162,163
K 162,164,165
Mvaa 232,144,80
W 6
Ta 140,22,20768
K 163,166,167
Mvaa 236,152,80
s_add_u32 s83, s28, 32768
Ta 142,22,25888
Ta 144,21,30720
Mvaa 236,156,84
buffer_load_dwordx4 v[36:39], v20, s[16:19], s72 offen
Ta 146,21,35840
Mvaa 228,156,68
Ta 148,23,0
Ta 150,23,5120
Mvaa 228,152,64
W 8
Ta 152,22,30752
Ta 154,22,35872
Mvaa 240,128,64
Mvaa 240,132,68
buffer_load_dwordx4 v[44:47], v14, s[8:11], s75 offen sc1
Ta 156,24,32
Mvaa 248,132,84
Ta 158,24,5152
Mvaa 248,128,80
W 8
Ta 128,21,30784
Ta 130,21,35904
Mvaa 252,136,80
Mvaa 252,140,84
buffer_store_dwordx4 v[160:163], v1, s[8:11], s83 offen sc1
K 168,168,169
Mvaa 244,140,68
K 169,170,171
K 170,172,173
Mvaa 244,136,64
s_waitcnt vmcnt(27) lgkmcnt(6)
Ta 132,23,64
K 171,174,175
Mvaa 176,144,72
s_add_u32 s83, s29, 73728
Ta 134,23,5184
Ta 136,22,30816
Mvaa 176,148,76
buffer_load_dwordx4 v[52:55], v20, s[16:19], s73 offen
Ta 138,22,35936
Mvaa 184,148,92
Ta 140,24,96
Ta 142,24,5216
Mvaa 184,144,88
W 8
Ta 144,21,30848
Ta 146,21,35968
Mvaa 188,152,88
Mvaa 188,156,92
buffer_store_dwordx4 v[168:171], v1, s[8:11], s83 offen sc1
Ta 148,23,128
Mvaa 180,156,76
Ta 150,23,5248
Mvaa 180,152,72
W 8
Ta 152,22,30880
Ta 154,22,36000
Mvaa 192,128,72
Mvaa 192,132,76
buffer_load_dwordx4 v[60:63], v14, s[8:11], s76 offen sc1
Ta 156,24,160
Mvaa 200,132,92
Ta 158,24,5280
Mvaa 200,128,88
W 8
Ta 128,21,30912
Ta 130,21,36032
Mvaa 204,136,88
Mvaa 204,140,92
buffer_load_dwordx4 v[68:71], v20, s[16:19], s74 offen
Ta 132,23,192
Mvaa 196,140,76
Ta 134,23,5312
Mvaa 196,136,72
W 8
Ta 136,22,30944
Ta 138,22,36064
Mvaa 208,144,72
Mvaa 208,148,76
buffer_load_dwordx4 v[76:79], v14, s[8:11], s77 offen sc1
Ta 140,24,224
Mvaa 216,148,92
Ta 142,24,5344
Mvaa 216,144,88
W 8
Ta 144,21,30976
Ta 146,21,36096
Mvaa 220,152,88
Mvaa 220,156,92
buffer_load_dwordx2 v[12:13], v27, s[8:11], 0 offen offset:3584 sc1
Ta 148,23,256
Mvaa 212,156,76
Ta 150,23,5376
Mvaa 212,152,72
W 8
Ta 152,22,31008
Ta 154,22,36128
Mvaa 224,128,72
Mvaa 224,132,76
Ta 156,24,288
Ta 158,24,5408
Mvaa 232,132,92
Mvaa 232,128,88
W 8
Ta 128,23,20480
Ta 130,23,25600
Mvaa 236,136,88
Mvaa 236,140,92
Ta 132,23,51200
Ta 134,23,56320
Mvaa 228,140,76
Mvaa 228,136,72
W 8
Ta 136,25,0
Ta 138,25,5120
Mvaa 240,144,72
Mvaa 240,148,76
Ta 140,21,40960
Ta 142,21,46080
Mvaa 248,148,92
Mvaa 248,144,88
W 8
Ta 144,21,51200
Ta 146,21,56320
Mvaa 252,152,88
Mvaa 252,156,92
Ta 148,23,10240
Ta 150,23,15360
Mvaa 244,156,76
Mvaa 244,152,72
s_waitcnt vmcnt(28) lgkmcnt(8)
Ta 152,23,30720
Ta 154,23,35840
Mvaa 176,128,96
Mvaa 176,132,100
Ta 156,23,40960
Ta 158,23,46080
Mvaa 184,132,116
Mvaa 184,128,112
s_waitcnt vmcnt(22) lgkmcnt(8)
Ta 128,24,20512
Ta 130,24,25632
Mvaa 184,136,120
Mvaa 184,140,124
Ta 132,24,51232
Ta 134,24,56352
Mvaa 176,140,108
s_barrier
Mvaa 176,136,104
s_waitcnt vmcnt(16) lgkmcnt(8)
Ta 136,26,32
Ta 138,26,5152
Mvaa 176,144,184
Mvaa 176,148,188
Ta 140,22,40992
Ta 142,22,46112
Mvaa 184,148,204
s_waitcnt vmcnt(4)
v_lshlrev_b32 v2, 2, v1
v_mov_b64 v[28:29], s[12:13]
s_mov_b64 exec, 3
buffer_store_dwordx2 v[28:29], v2, s[8:11], s53 offen offset:0 sc1
s_mov_b64 exec, -1
Mvaa 184,144,200
W 8
Ta 144,22,51232
Ta 146,22,56352
Mvaa 184,152,208
Mvaa 184,156,212
Ta 148,24,10272
Ta 150,24,15392
Mvaa 176,156,196
Mvaa 176,152,192
W 8
Ta 152,24,30752
Ta 154,24,35872
Mvaa 180,128,96
Mvaa 180,132,100
Ta 156,24,40992
Ta 158,24,46112
Mvaa 188,132,116
Mvaa 188,128,112
W 8
Ta 128,23,20544
Ta 130,23,25664
Mvaa 188,136,120
Mvaa 188,140,124
Ta 132,23,51264
Ta 134,23,56384
Mvaa 180,140,108
Mvaa 180,136,104
W 8
Ta 136,25,64
)"
R"(Ta 138,25,5184
Mvaa 180,144,184
Mvaa 180,148,188
Ta 140,21,41024
Ta 142,21,46144
Mvaa 188,148,204
Mvaa 188,144,200
W 8
Ta 144,21,51264
Ta 146,21,56384
Mvaa 188,152,208
Mvaa 188,156,212
Ta 148,23,10304
Ta 150,23,15424
Mvaa 180,156,196
s_waitcnt vmcnt(1)
v_xor_b32 v12, s12, v12
v_xor_b32 v13, s13, v13
v_lshrrev_b32 v3, 11, v12
v_or_b32 v13, v13, v3
v_cmp_eq_u32 vcc, 0, v13
s_cmp_eq_u64 vcc, -1
s_cbranch_scc0 .LL_106%=
.LL_107%=:
v_bfe_u32 v13, v12, 4, 7
s_xor_b32 s66, s61, 124
v_cmp_eq_u32 vcc, s61, v13
v_cmp_eq_u32_e64 s[86:87], s66, v13
s_or_b32 s68, vcc_lo, s86
s_and_b32 s68, s68, 255
s_cbranch_scc1 .Lup_104%=
.Lupr_105%=:
Mvaa 180,152,192
W 8
Ta 152,23,30784
Ta 154,23,35904
Mvaa 192,128,96
v_lshlrev_b32 v32, 16, v36
Mvaa 192,132,100
Ta 156,23,41024
v_and_b32 v33, 0xffff0000, v36
Mvaa 200,132,116
Ta 158,23,46144
v_lshlrev_b32 v34, 16, v37
Mvaa 200,128,112
W 8
K 176,176,177
v_and_b32 v35, 0xffff0000, v37
Mvaa 200,136,120
Ta 128,24,20576
Ta 130,24,25696
Mvaa 200,140,124
buffer_load_dwordx2 v[16:17], v18, s[8:11], s63 offen sc1
v_lshlrev_b32 v36, 16, v38
Mvaa 192,140,108
K 177,178,179
v_and_b32 v37, 0xffff0000, v38
Mvaa 192,136,104
W 6
Ta 132,24,51296
K 178,180,181
Mvaa 192,144,184
Ta 134,24,56416
v_lshlrev_b32 v38, 16, v39
Mvaa 192,148,188
K 179,182,183
v_and_b32 v39, 0xffff0000, v39
Mvaa 200,148,204
s_add_u32 s83, s28, 327680
Ta 136,26,96
Ta 138,26,5216
Mvaa 200,144,200
W 6
v_lshlrev_b32 v40, 16, v44
v_and_b32 v41, 0xffff0000, v44
Mvaa 200,152,208
buffer_store_dwordx4 v[176:179], v1, s[8:11], s83 offen sc1
K 184,184,185
Mvaa 200,156,212
K 185,186,187
K 186,188,189
Mvaa 192,156,196
K 187,190,191
s_add_u32 s83, s29, 368640
buffer_store_dwordx4 v[184:187], v1, s[8:11], s83 offen sc1
Mvaa 192,152,192
Ta 140,22,41056
Ta 142,22,46176
W 4
Mvaa 196,128,96
v_lshlrev_b32 v42, 16, v45
v_and_b32 v43, 0xffff0000, v45
Mvaa 196,132,100
Ta 144,22,51296
Ta 146,22,56416
Mvaa 204,132,116
v_lshlrev_b32 v44, 16, v46
v_and_b32 v45, 0xffff0000, v46
Mvaa 204,128,112
Ta 148,24,10336
Ta 150,24,15456
W 4
Mvaa 204,136,120
v_lshlrev_b32 v46, 16, v47
v_and_b32 v47, 0xffff0000, v47
Mvaa 204,140,124
Ta 152,24,30816
Ta 154,24,35936
Mvaa 196,140,108
v_lshlrev_b32 v48, 16, v52
v_and_b32 v49, 0xffff0000, v52
Mvaa 196,136,104
Ta 156,24,41056
Ta 158,24,46176
W 4
Mvaa 196,144,184
v_lshlrev_b32 v50, 16, v53
v_and_b32 v51, 0xffff0000, v53
Mvaa 196,148,188
Ta 128,23,20608
Ta 130,23,25728
Mvaa 204,148,204
v_lshlrev_b32 v52, 16, v54
v_and_b32 v53, 0xffff0000, v54
Mvaa 204,144,200
Ta 132,23,51328
Ta 134,23,56448
W 4
Mvaa 204,152,208
v_lshlrev_b32 v54, 16, v55
v_and_b32 v55, 0xffff0000, v55
Mvaa 204,156,212
Ta 136,25,128
Ta 138,25,5248
Mvaa 196,156,196
v_lshlrev_b32 v56, 16, v60
v_and_b32 v57, 0xffff0000, v60
Mvaa 196,152,192
Ta 140,21,41088
Ta 142,21,46208
W 4
Mvaa 208,128,96
v_lshlrev_b32 v58, 16, v61
v_and_b32 v59, 0xffff0000, v61
Mvaa 208,132,100
Ta 144,21,51328
Ta 146,21,56448
Mvaa 216,132,116
v_lshlrev_b32 v60, 16, v62
v_and_b32 v61, 0xffff0000, v62
Mvaa 216,128,112
Ta 148,23,10368
Ta 150,23,15488
W 4
Mvaa 216,136,120
v_lshlrev_b32 v62, 16, v63
v_and_b32 v63, 0xffff0000, v63
Mvaa 216,140,124
Ta 152,23,30848
Ta 154,23,35968
Mvaa 208,140,108
v_lshlrev_b32 v64, 16, v68
v_and_b32 v65, 0xffff0000, v68
Mvaa 208,136,104
Ta 156,23,41088
Ta 158,23,46208
W 4
Mvaa 208,144,184
v_lshlrev_b32 v66, 16, v69
K 192,192,193
Mvaa 208,148,188
Ta 128,24,20640
v_and_b32 v67, 0xffff0000, v69
Mvaa 216,148,204
Ta 130,24,25760
K 193,194,195
Mvaa 216,144,200
Ta 132,24,51360
Ta 134,24,56480
W 4
Mvaa 216,152,208
v_lshlrev_b32 v68, 16, v70
K 194,196,197
Mvaa 216,156,212
v_and_b32 v69, 0xffff0000, v70
v_lshlrev_b32 v70, 16, v71
Mvaa 208,156,196
K 195,198,199
s_add_u32 s83, s28, 335872
buffer_store_dwordx4 v[192:195], v1, s[8:11], s83 offen sc1
K 200,200,201
K 201,202,203
K 202,204,205
K 203,206,207
s_add_u32 s83, s29, 376832
buffer_store_dwordx4 v[200:203], v1, s[8:11], s83 offen sc1
Mvaa 208,152,192
Ta 136,26,160
Ta 138,26,5280
Ta 140,22,41120
Ta 142,22,46240
W 4
Mvaa 212,128,96
s_waitcnt vmcnt(4)
v_cmp_eq_u64 vcc, s[12:13], v[16:17]
s_and_b32 s71, vcc_lo, 255
s_bitset0_b32 s71, s59
v_and_b32 v71, 0xffff0000, v71
v_lshlrev_b32 v72, 16, v76
Mvaa 212,132,100
s_lshl_b32 s92, s14, 3
s_lshr_b32 s92, s71, s92
s_mov_b32 s91, 134217728
Mvaa 220,132,116
s_bitcmp1_b32 s71, 0
s_cselect_b32 s94, s78, s91
v_and_b32 v73, 0xffff0000, v76
Mvaa 220,128,112
Ta 144,22,51360
Ta 146,22,56480
Ta 148,24,10400
Ta 150,24,15520
W 4
Mvaa 220,136,120
v_lshlrev_b32 v74, 16, v77
v_and_b32 v75, 0xffff0000, v77
Mvaa 220,140,124
v_lshlrev_b32 v76, 16, v78
v_and_b32 v77, 0xffff0000, v78
Mvaa 212,140,108
v_lshlrev_b32 v78, 16, v79
v_and_b32 v79, 0xffff0000, v79
Mvaa 212,136,104
Ta 152,24,30880
Ta 154,24,36000
Ta 156,24,41120
Ta 158,24,46240
W 4
Mvaa 212,144,184
Ta 128,23,20672
Ta 130,23,25792
Mvaa 212,148,188
buffer_load_dwordx4 a[0:3], v1, s[8:11], s94 offen offset:0 sc1
Ta 132,23,51392
Mvaa 220,148,204
Ta 134,23,56512
Ta 136,25,192
Mvaa 220,144,200
W 5
Ta 138,25,5312
Ta 140,21,41152
Mvaa 220,152,208
Ta 142,21,46272
Ta 144,21,51392
Mvaa 220,156,212
s_bitcmp1_b32 s92, 0
s_cbranch_scc0 .LL_109%=
buffer_load_dwordx4 a[216:219], v1, s[8:11], s65 offen offset:0 sc1
.LL_109%=:
Ta 146,21,56512
Mvaa 212,156,196
s_bitcmp1_b32 s71, 1
s_cselect_b32 s94, s78, s91
Ta 148,23,10432
Mvaa 212,152,192
W 7
Ta 150,23,15552
Ta 152,23,30912
Mvaa 224,128,96
Ta 154,23,36032
Mvaa 224,132,100
buffer_load_dwordx4 a[4:7], v1, s[8:11], s94 offen offset:1024 sc1
Ta 156,23,41152
Mvaa 232,132,116
Ta 158,23,46272
K 208,208,209
Mvaa 232,128,112
W 8
Ta 128,24,20704
Ta 130,24,25824
Mvaa 232,136,120
K 209,210,211
Mvaa 232,140,124
s_bitcmp1_b32 s92, 1
s_cbranch_scc0 .LL_110%=
buffer_load_dwordx4 a[220:223], v1, s[8:11], s65 offen offset:1024 sc1
.LL_110%=:
K 210,212,213
Mvaa 224,140,108
s_bitcmp1_b32 s71, 2
s_cselect_b32 s94, s78, s91
Ta 132,24,51424
Mvaa 224,136,104
W 7
Ta 134,24,56544
K 211,214,215
Mvaa 224,144,184
s_add_u32 s83, s28, 344064
Ta 136,26,224
Ta 138,26,5344
Mvaa 224,148,188
buffer_load_dwordx4 a[8:11], v1, s[8:11], s94 offen offset:2048 sc1
Ta 140,22,41184
Mvaa 232,148,204
Ta 142,22,46304
Mvaa 232,144,200
W 8
Ta 144,22,51424
Ta 146,22,56544
Mvaa 232,152,208
Mvaa 232,156,212
buffer_store_dwordx4 v[208:211], v1, s[8:11], s83 offen sc1
K 216,216,217
Mvaa 224,156,196
K 217,218,219
K 218,220,221
K 219,222,223
s_add_u32 s83, s29, 385024
buffer_store_dwordx4 v[216:219], v1, s[8:11], s83 offen sc1
Mvaa 224,152,192
W 6
Ta 148,24,10464
Ta 150,24,15584
Mvaa 228,128,96
Ta 152,24,30944
Ta 154,24,36064
Mvaa 228,132,100
Ta 156,24,41184
Ta 158,24,46304
Mvaa 236,132,116
s_bitcmp1_b32 s92, 2
s_cbranch_scc0 .LL_111%=
buffer_load_dwordx4 a[224:227], v1, s[8:11], s65 offen offset:2048 sc1
.LL_111%=:
Mvaa 236,128,112
W 8
s_bitcmp1_b32 s71, 3
s_cselect_b32 s94, s78, s91
Mvaa 236,136,120
Ta 128,23,20736
Ta 130,23,25856
Mvaa 236,140,124
Ta 132,23,51456
Ta 134,23,56576
Mvaa 228,140,108
buffer_load_dwordx4 a[12:15], v1, s[8:11], s94 offen offset:3072 sc1
Mvaa 228,136,104
W 8
Ta 136,25,256
Ta 138,25,5376
Mvaa 228,144,184
Mvaa 228,148,188
Ta 140,21,41216
Ta 142,21,46336
Mvaa 236,148,204
s_bitcmp1_b32 s92, 3
s_cbranch_scc0 .LL_112%=
buffer_load_dwordx4 a[228:231], v1, s[8:11], s65 offen offset:3072 sc1
.LL_112%=:
Mvaa 236,144,200
W 8
s_bitcmp1_b32 s71, 4
s_cselect_b32 s94, s78, s91
Mvaa 236,152,208
Ta 144,21,51456
Ta 146,21,56576
Mvaa 236,156,212
Ta 148,23,10496
Ta 150,23,15616
Mvaa 228,156,196
buffer_load_dwordx4 a[16:19], v19, s[8:11], s94 offen offset:0 sc1
Mvaa 228,152,192
W 8
Ta 152,23,30976
Ta 154,23,36096
Mvaa 240,128,96
Mvaa 240,132,100
Ta 156,23,41216
Ta 158,23,46336
Mvaa 248,132,116
s_bitcmp1_b32 s92, 4
s_cbranch_scc0 .LL_113%=
buffer_load_dwordx4 a[232:235], v19, s[8:11], s65 offen offset:0 sc1
.LL_113%=:
K 224,224,225
Mvaa 248,128,112
W 8
s_bitcmp1_b32 s71, 5
s_cselect_b32 s94, s78, s91
Mvaa 248,136,120
Ta 128,24,20768
Ta 130,24,25888
Mvaa 248,140,124
K 225,226,227
K 226,228,229
Mvaa 240,140,108
buffer_load_dwordx4 a[20:23], v19, s[8:11], s94 offen offset:1024 sc1
Ta 132,24,51488
Mvaa 240,136,104
W 7
)"
R"(Ta 134,24,56608
K 227,230,231
Mvaa 240,144,184
s_add_u32 s83, s28, 352256
Ta 136,26,288
Ta 138,26,5408
Mvaa 240,148,188
Ta 140,22,41248
Ta 142,22,46368
Mvaa 248,148,204
s_bitcmp1_b32 s92, 5
s_cbranch_scc0 .LL_114%=
buffer_load_dwordx4 a[236:239], v19, s[8:11], s65 offen offset:1024 sc1
.LL_114%=:
Mvaa 248,144,200
W 8
s_bitcmp1_b32 s71, 6
s_cselect_b32 s94, s78, s91
Mvaa 248,152,208
Ta 144,22,51488
Ta 146,22,56608
Mvaa 248,156,212
Ta 148,24,10528
Ta 150,24,15648
Mvaa 240,156,196
buffer_store_dwordx4 v[224:227], v1, s[8:11], s83 offen sc1
K 232,232,233
K 233,234,235
K 234,236,237
K 235,238,239
s_add_u32 s83, s29, 393216
buffer_store_dwordx4 v[232:235], v1, s[8:11], s83 offen sc1
Mvaa 240,152,192
W 8
Ta 152,24,31008
Ta 154,24,36128
Mvaa 244,128,96
Mvaa 244,132,100
Ta 156,24,41248
Ta 158,24,46368
Mvaa 252,132,116
buffer_load_dwordx4 a[24:27], v19, s[8:11], s94 offen offset:2048 sc1
Mvaa 252,128,112
W 8
Mvaa 252,136,120
Mvaa 252,140,124
Mvaa 244,140,108
s_bitcmp1_b32 s92, 6
s_cbranch_scc0 .LL_115%=
buffer_load_dwordx4 a[240:243], v19, s[8:11], s65 offen offset:2048 sc1
.LL_115%=:
Mvaa 244,136,104
W 4
s_bitcmp1_b32 s71, 7
s_cselect_b32 s94, s78, s91
Mvaa 244,144,184
Mvaa 244,148,188
Mvaa 252,148,204
buffer_load_dwordx4 a[28:31], v19, s[8:11], s94 offen offset:3072 sc1
Mvaa 252,144,200
W 0
Mvaa 252,152,208
Mvaa 252,156,212
Mvaa 244,156,196
s_bitcmp1_b32 s92, 7
s_cbranch_scc0 .LL_116%=
buffer_load_dwordx4 a[244:247], v19, s[8:11], s65 offen offset:3072 sc1
.LL_116%=:
Mvaa 244,152,192
K 240,240,241
K 241,242,243
s_nop 5
K 242,244,245
K 243,246,247
s_add_u32 s83, s28, 360448
buffer_store_dwordx4 v[240:243], v1, s[8:11], s83 offen sc1
K 248,248,249
K 249,250,251
K 250,252,253
K 251,254,255
s_add_u32 s83, s29, 401408
buffer_store_dwordx4 v[248:251], v1, s[8:11], s83 offen sc1
.Ltend_81%=:
buffer_load_dwordx2 v[16:17], v18, s[8:11], s63 offen sc1
s_bitcmp1_b32 s28, 27
s_cbranch_scc0 .LL_117%=
ds_write_b128 v1, v[96:99] offset:0
ds_write_b128 v1, v[112:115] offset:1024
ds_write_b128 v1, v[128:131] offset:2048
ds_write_b128 v1, v[144:147] offset:3072
ds_write_b128 v1, v[160:163] offset:4096
ds_write_b128 v1, v[176:179] offset:5120
ds_write_b128 v1, v[192:195] offset:6144
ds_write_b128 v1, v[208:211] offset:7168
ds_write_b128 v1, v[224:227] offset:8192
ds_write_b128 v1, v[240:243] offset:9216
W 0
.LL_117%=:
s_bitcmp1_b32 s29, 27
s_cbranch_scc0 .LL_118%=
ds_write_b128 v1, v[104:107] offset:0
ds_write_b128 v1, v[120:123] offset:1024
ds_write_b128 v1, v[136:139] offset:2048
ds_write_b128 v1, v[152:155] offset:3072
ds_write_b128 v1, v[168:171] offset:4096
ds_write_b128 v1, v[184:187] offset:5120
ds_write_b128 v1, v[200:203] offset:6144
ds_write_b128 v1, v[216:219] offset:7168
ds_write_b128 v1, v[232:235] offset:8192
ds_write_b128 v1, v[248:251] offset:9216
W 0
.LL_118%=:
s_lshl_b32 s80, s14, 10
v_add_u32 v21, s80, v1
s_cmp_lg_u32 s68, 0
s_cbranch_scc1 .Lup_119%=
.Lupr_120%=:
s_lshl_b32 s66, 0x101, s59
s_waitcnt vmcnt(1)
s_cmp_eq_u32 s64, 0
s_cbranch_scc0 .LL_125%=
v_lshlrev_b32 v2, 2, v1
v_mov_b64 v[28:29], s[12:13]
s_mov_b64 exec, 3
buffer_store_dwordx2 v[28:29], v2, s[8:11], s53 offen offset:512 sc1
s_mov_b64 exec, -1
.LL_125%=:
s_barrier
s_waitcnt vmcnt(1)
v_cmp_eq_u64 vcc, s[12:13], v[16:17]
s_or_b32 s67, vcc_lo, s66
s_cmp_eq_u32 s14, 0
s_cbranch_scc1 .LL_127%=
s_cmp_eq_u32 s14, 1
s_cbranch_scc1 .LL_128%=
s_mov_b32 s84, 0
.LL_129%=:
s_or_b32 s70, s71, s66
s_andn2_b32 s69, s67, s71
s_and_b32 s69, s69, 0xffff
s_cbranch_scc0 .LL_132%=
s_and_b32 s80, s69, 255
s_cbranch_scc0 .LL_133%=
s_bitcmp1_b32 s69, 0
s_cbranch_scc0 .LL_134%=
s_cmp_eq_u32 s59, 0
s_cbranch_scc0 .LL_135%=
Ra 0,21,0
s_branch .LL_134%=
.LL_135%=:
buffer_load_dwordx4 a[0:3], v1, s[8:11], s78 offen offset:0 sc1
.LL_134%=:
s_bitcmp1_b32 s69, 1
s_cbranch_scc0 .LL_136%=
s_cmp_eq_u32 s59, 1
s_cbranch_scc0 .LL_137%=
Ra 4,21,0
s_branch .LL_136%=
.LL_137%=:
buffer_load_dwordx4 a[4:7], v1, s[8:11], s78 offen offset:1024 sc1
.LL_136%=:
s_bitcmp1_b32 s69, 2
s_cbranch_scc0 .LL_138%=
s_cmp_eq_u32 s59, 2
s_cbranch_scc0 .LL_139%=
Ra 8,21,0
s_branch .LL_138%=
.LL_139%=:
buffer_load_dwordx4 a[8:11], v1, s[8:11], s78 offen offset:2048 sc1
.LL_138%=:
s_bitcmp1_b32 s69, 3
s_cbranch_scc0 .LL_140%=
s_cmp_eq_u32 s59, 3
s_cbranch_scc0 .LL_141%=
Ra 12,21,0
s_branch .LL_140%=
.LL_141%=:
buffer_load_dwordx4 a[12:15], v1, s[8:11], s78 offen offset:3072 sc1
.LL_140%=:
s_bitcmp1_b32 s69, 4
s_cbranch_scc0 .LL_142%=
s_cmp_eq_u32 s59, 4
s_cbranch_scc0 .LL_143%=
Ra 16,21,0
s_branch .LL_142%=
.LL_143%=:
buffer_load_dwordx4 a[16:19], v19, s[8:11], s78 offen offset:0 sc1
.LL_142%=:
s_bitcmp1_b32 s69, 5
s_cbranch_scc0 .LL_144%=
s_cmp_eq_u32 s59, 5
s_cbranch_scc0 .LL_145%=
Ra 20,21,0
s_branch .LL_144%=
.LL_145%=:
buffer_load_dwordx4 a[20:23], v19, s[8:11], s78 offen offset:1024 sc1
.LL_144%=:
s_bitcmp1_b32 s69, 6
s_cbranch_scc0 .LL_146%=
s_cmp_eq_u32 s59, 6
s_cbranch_scc0 .LL_147%=
Ra 24,21,0
s_branch .LL_146%=
.LL_147%=:
buffer_load_dwordx4 a[24:27], v19, s[8:11], s78 offen offset:2048 sc1
.LL_146%=:
s_bitcmp1_b32 s69, 7
s_cbranch_scc0 .LL_148%=
s_cmp_eq_u32 s59, 7
s_cbranch_scc0 .LL_149%=
Ra 28,21,0
s_branch .LL_148%=
.LL_149%=:
buffer_load_dwordx4 a[28:31], v19, s[8:11], s78 offen offset:3072 sc1
.LL_148%=:
.LL_133%=:
s_and_b32 s80, s69, 65280
s_cbranch_scc0 .LL_150%=
s_bitcmp1_b32 s69, 8
s_cbranch_scc0 .LL_151%=
s_cmp_eq_u32 s59, 0
s_cbranch_scc0 .LL_152%=
Ra 32,21,5120
s_branch .LL_151%=
.LL_152%=:
buffer_load_dwordx4 a[32:35], v1, s[8:11], s79 offen offset:0 sc1
.LL_151%=:
s_bitcmp1_b32 s69, 9
s_cbranch_scc0 .LL_153%=
s_cmp_eq_u32 s59, 1
s_cbranch_scc0 .LL_154%=
Ra 36,21,5120
s_branch .LL_153%=
.LL_154%=:
buffer_load_dwordx4 a[36:39], v1, s[8:11], s79 offen offset:1024 sc1
.LL_153%=:
s_bitcmp1_b32 s69, 10
s_cbranch_scc0 .LL_155%=
s_cmp_eq_u32 s59, 2
s_cbranch_scc0 .LL_156%=
Ra 40,21,5120
s_branch .LL_155%=
.LL_156%=:
buffer_load_dwordx4 a[40:43], v1, s[8:11], s79 offen offset:2048 sc1
.LL_155%=:
s_bitcmp1_b32 s69, 11
s_cbranch_scc0 .LL_157%=
s_cmp_eq_u32 s59, 3
s_cbranch_scc0 .LL_158%=
Ra 44,21,5120
s_branch .LL_157%=
.LL_158%=:
buffer_load_dwordx4 a[44:47], v1, s[8:11], s79 offen offset:3072 sc1
.LL_157%=:
s_bitcmp1_b32 s69, 12
s_cbranch_scc0 .LL_159%=
s_cmp_eq_u32 s59, 4
s_cbranch_scc0 .LL_160%=
Ra 48,21,5120
s_branch .LL_159%=
.LL_160%=:
buffer_load_dwordx4 a[48:51], v19, s[8:11], s79 offen offset:0 sc1
.LL_159%=:
s_bitcmp1_b32 s69, 13
s_cbranch_scc0 .LL_161%=
s_cmp_eq_u32 s59, 5
s_cbranch_scc0 .LL_162%=
Ra 52,21,5120
s_branch .LL_161%=
.LL_162%=:
buffer_load_dwordx4 a[52:55], v19, s[8:11], s79 offen offset:1024 sc1
.LL_161%=:
s_bitcmp1_b32 s69, 14
s_cbranch_scc0 .LL_163%=
s_cmp_eq_u32 s59, 6
s_cbranch_scc0 .LL_164%=
Ra 56,21,5120
s_branch .LL_163%=
.LL_164%=:
buffer_load_dwordx4 a[56:59], v19, s[8:11], s79 offen offset:2048 sc1
.LL_163%=:
s_bitcmp1_b32 s69, 15
s_cbranch_scc0 .LL_165%=
s_cmp_eq_u32 s59, 7
s_cbranch_scc0 .LL_166%=
Ra 60,21,5120
s_branch .LL_165%=
.LL_166%=:
buffer_load_dwordx4 a[60:63], v19, s[8:11], s79 offen offset:3072 sc1
.LL_165%=:
.LL_150%=:
s_or_b32 s71, s71, s69
.LL_132%=:
s_andn2_b32 s80, 0xffff, s71
s_cbranch_scc0 .LL_131%=
s_sleep 4
buffer_load_dwordx2 v[16:17], v18, s[8:11], s63 offen sc1
s_and_b32 s80, s70, 0x100ff
s_cmp_eq_u32 s80, 0xff
s_cbranch_scc0 .LL_130%=
W 0
Zvva 80,4,0
Zvva 84,8,0
Mvva 80,4,4
Mvva 84,8,4
Mvva 80,4,8
Mvva 84,8,8
Mvva 80,4,12
Mvva 84,8,12
Mvva 80,4,16
Mvva 84,8,16
Mvva 80,4,20
Mvva 84,8,20
Mvva 80,4,24
Mvva 84,8,24
Mvva 80,4,28
Mvva 84,8,28
s_nop 6
K 160,80,81
K 161,82,83
K 162,84,85
K 163,86,87
v_lshlrev_b32 v80, 16, v160
v_and_b32 v81, 0xffff0000, v160
v_lshlrev_b32 v82, 16, v161
v_and_b32 v83, 0xffff0000, v161
v_lshlrev_b32 v84, 16, v162
v_and_b32 v85, 0xffff0000, v162
v_lshlrev_b32 v86, 16, v163
v_and_b32 v87, 0xffff0000, v163
v_pk_add_f32 v[80:81], v[32:33], v[80:81]
v_pk_add_f32 v[82:83], v[34:35], v[82:83]
v_pk_add_f32 v[84:85], v[36:37], v[84:85]
v_pk_add_f32 v[86:87], v[38:39], v[86:87]
K 160,80,81
K 161,82,83
K 162,84,85
K 163,86,87
v_lshlrev_b32 v80, 16, v160
v_and_b32 v81, 0xffff0000, v160
v_lshlrev_b32 v82, 16, v161
v_and_b32 v83, 0xffff0000, v161
v_lshlrev_b32 v84, 16, v162
v_and_b32 v85, 0xffff0000, v162
v_lshlrev_b32 v86, 16, v163
v_and_b32 v87, 0xffff0000, v163
v_pk_add_f32 v[80:81], v[40:41], v[80:81]
v_pk_add_f32 v[82:83], v[42:43], v[82:83]
v_pk_add_f32 v[84:85], v[44:45], v[84:85]
v_pk_add_f32 v[86:87], v[46:47], v[86:87]
K 80,80,81
K 81,82,83
K 82,84,85
K 83,86,87
buffer_store_dwordx4 v[80:83], v20, s[20:23], s72 offen nt
s_bitset1_b32 s71, 16
.LL_130%=:
s_add_u32 s84, s84, 1
s_waitcnt vmcnt(0)
v_cmp_eq_u64 vcc, s[12:13], v[16:17]
s_or_b32 s67, vcc_lo, s66
s_cmp_lt_u32 s84, 4194304
s_cbranch_scc1 .LL_129%=
.LL_131%=:
s_bitcmp1_b32 s71, 16
s_cbranch_scc1 .LL_167%=
s_and_b32 s80, s70, 0xff
s_cmp_eq_u32 s80, 0xff
s_cbranch_scc1 .LL_168%=
s_waitcnt vmcnt(0)
.LL_168%=:
W 0
Zvva 80,4,0
Zvva 84,8,0
Mvva 80,4,4
Mvva 84,8,4
Mvva 80,4,8
Mvva 84,8,8
Mvva 80,4,12
Mvva 84,8,12
Mvva 80,4,16
Mvva 84,8,16
Mvva 80,4,20
Mvva 84,8,20
Mvva 80,4,24
Mvva 84,8,24
Mvva 80,4,28
Mvva 84,8,28
s_nop 6
K 160,80,81
K 161,82,83
K 162,84,85
K 163,86,87
v_lshlrev_b32 v80, 16, v160
v_and_b32 v81, 0xffff0000, v160
v_lshlrev_b32 v82, 16, v161
v_and_b32 v83, 0xffff0000, v161
v_lshlrev_b32 v84, 16, v162
v_and_b32 v85, 0xffff0000, v162
v_lshlrev_b32 v86, 16, v163
)"
R"(v_and_b32 v87, 0xffff0000, v163
v_pk_add_f32 v[80:81], v[32:33], v[80:81]
v_pk_add_f32 v[82:83], v[34:35], v[82:83]
v_pk_add_f32 v[84:85], v[36:37], v[84:85]
v_pk_add_f32 v[86:87], v[38:39], v[86:87]
K 160,80,81
K 161,82,83
K 162,84,85
K 163,86,87
v_lshlrev_b32 v80, 16, v160
v_and_b32 v81, 0xffff0000, v160
v_lshlrev_b32 v82, 16, v161
v_and_b32 v83, 0xffff0000, v161
v_lshlrev_b32 v84, 16, v162
v_and_b32 v85, 0xffff0000, v162
v_lshlrev_b32 v86, 16, v163
v_and_b32 v87, 0xffff0000, v163
v_pk_add_f32 v[80:81], v[40:41], v[80:81]
v_pk_add_f32 v[82:83], v[42:43], v[82:83]
v_pk_add_f32 v[84:85], v[44:45], v[84:85]
v_pk_add_f32 v[86:87], v[46:47], v[86:87]
K 80,80,81
K 81,82,83
K 82,84,85
K 83,86,87
buffer_store_dwordx4 v[80:83], v20, s[20:23], s72 offen nt
s_waitcnt vmcnt(1)
W 0
Zvva 80,4,32
Zvva 84,8,32
Mvva 80,4,36
Mvva 84,8,36
Mvva 80,4,40
Mvva 84,8,40
Mvva 80,4,44
Mvva 84,8,44
Mvva 80,4,48
Mvva 84,8,48
Mvva 80,4,52
Mvva 84,8,52
Mvva 80,4,56
Mvva 84,8,56
Mvva 80,4,60
Mvva 84,8,60
s_nop 6
K 160,80,81
K 161,82,83
K 162,84,85
K 163,86,87
v_lshlrev_b32 v80, 16, v160
v_and_b32 v81, 0xffff0000, v160
v_lshlrev_b32 v82, 16, v161
v_and_b32 v83, 0xffff0000, v161
v_lshlrev_b32 v84, 16, v162
v_and_b32 v85, 0xffff0000, v162
v_lshlrev_b32 v86, 16, v163
v_and_b32 v87, 0xffff0000, v163
v_pk_add_f32 v[80:81], v[48:49], v[80:81]
v_pk_add_f32 v[82:83], v[50:51], v[82:83]
v_pk_add_f32 v[84:85], v[52:53], v[84:85]
v_pk_add_f32 v[86:87], v[54:55], v[86:87]
K 160,80,81
K 161,82,83
K 162,84,85
K 163,86,87
v_lshlrev_b32 v80, 16, v160
v_and_b32 v81, 0xffff0000, v160
v_lshlrev_b32 v82, 16, v161
v_and_b32 v83, 0xffff0000, v161
v_lshlrev_b32 v84, 16, v162
v_and_b32 v85, 0xffff0000, v162
v_lshlrev_b32 v86, 16, v163
v_and_b32 v87, 0xffff0000, v163
v_pk_add_f32 v[80:81], v[56:57], v[80:81]
v_pk_add_f32 v[82:83], v[58:59], v[82:83]
v_pk_add_f32 v[84:85], v[60:61], v[84:85]
v_pk_add_f32 v[86:87], v[62:63], v[86:87]
K 80,80,81
K 81,82,83
K 82,84,85
K 83,86,87
buffer_store_dwordx4 v[80:83], v20, s[20:23], s73 offen nt
s_branch .LL_126%=
.LL_167%=:
s_waitcnt vmcnt(0)
W 0
Zvva 80,4,32
Zvva 84,8,32
Mvva 80,4,36
Mvva 84,8,36
Mvva 80,4,40
Mvva 84,8,40
Mvva 80,4,44
Mvva 84,8,44
Mvva 80,4,48
Mvva 84,8,48
Mvva 80,4,52
Mvva 84,8,52
Mvva 80,4,56
Mvva 84,8,56
Mvva 80,4,60
Mvva 84,8,60
s_nop 6
K 160,80,81
K 161,82,83
K 162,84,85
K 163,86,87
v_lshlrev_b32 v80, 16, v160
v_and_b32 v81, 0xffff0000, v160
v_lshlrev_b32 v82, 16, v161
v_and_b32 v83, 0xffff0000, v161
v_lshlrev_b32 v84, 16, v162
v_and_b32 v85, 0xffff0000, v162
v_lshlrev_b32 v86, 16, v163
v_and_b32 v87, 0xffff0000, v163
v_pk_add_f32 v[80:81], v[48:49], v[80:81]
v_pk_add_f32 v[82:83], v[50:51], v[82:83]
v_pk_add_f32 v[84:85], v[52:53], v[84:85]
v_pk_add_f32 v[86:87], v[54:55], v[86:87]
K 160,80,81
K 161,82,83
K 162,84,85
K 163,86,87
v_lshlrev_b32 v80, 16, v160
v_and_b32 v81, 0xffff0000, v160
v_lshlrev_b32 v82, 16, v161
v_and_b32 v83, 0xffff0000, v161
v_lshlrev_b32 v84, 16, v162
v_and_b32 v85, 0xffff0000, v162
v_lshlrev_b32 v86, 16, v163
v_and_b32 v87, 0xffff0000, v163
v_pk_add_f32 v[80:81], v[56:57], v[80:81]
v_pk_add_f32 v[82:83], v[58:59], v[82:83]
v_pk_add_f32 v[84:85], v[60:61], v[84:85]
v_pk_add_f32 v[86:87], v[62:63], v[86:87]
K 80,80,81
K 81,82,83
K 82,84,85
K 83,86,87
buffer_store_dwordx4 v[80:83], v20, s[20:23], s73 offen nt
s_branch .LL_126%=
.LL_127%=:
s_mov_b32 s84, 0
.LL_169%=:
s_or_b32 s70, s71, s66
s_andn2_b32 s69, s67, s71
s_and_b32 s69, s69, 0xffff
s_cbranch_scc0 .LL_172%=
s_and_b32 s80, s69, 255
s_cbranch_scc0 .LL_173%=
s_bitcmp1_b32 s69, 0
s_cbranch_scc0 .LL_174%=
s_cmp_eq_u32 s59, 0
s_cbranch_scc0 .LL_175%=
Ra 0,21,0
Ra 216,1,4096
s_branch .LL_174%=
.LL_175%=:
buffer_load_dwordx4 a[0:3], v1, s[8:11], s78 offen offset:0 sc1
buffer_load_dwordx4 a[216:219], v1, s[8:11], s65 offen offset:0 sc1
.LL_174%=:
s_bitcmp1_b32 s69, 1
s_cbranch_scc0 .LL_176%=
s_cmp_eq_u32 s59, 1
s_cbranch_scc0 .LL_177%=
Ra 4,21,0
Ra 220,1,4096
s_branch .LL_176%=
.LL_177%=:
buffer_load_dwordx4 a[4:7], v1, s[8:11], s78 offen offset:1024 sc1
buffer_load_dwordx4 a[220:223], v1, s[8:11], s65 offen offset:1024 sc1
.LL_176%=:
s_bitcmp1_b32 s69, 2
s_cbranch_scc0 .LL_178%=
s_cmp_eq_u32 s59, 2
s_cbranch_scc0 .LL_179%=
Ra 8,21,0
Ra 224,1,4096
s_branch .LL_178%=
.LL_179%=:
buffer_load_dwordx4 a[8:11], v1, s[8:11], s78 offen offset:2048 sc1
buffer_load_dwordx4 a[224:227], v1, s[8:11], s65 offen offset:2048 sc1
.LL_178%=:
s_bitcmp1_b32 s69, 3
s_cbranch_scc0 .LL_180%=
s_cmp_eq_u32 s59, 3
s_cbranch_scc0 .LL_181%=
Ra 12,21,0
Ra 228,1,4096
s_branch .LL_180%=
.LL_181%=:
buffer_load_dwordx4 a[12:15], v1, s[8:11], s78 offen offset:3072 sc1
buffer_load_dwordx4 a[228:231], v1, s[8:11], s65 offen offset:3072 sc1
.LL_180%=:
s_bitcmp1_b32 s69, 4
s_cbranch_scc0 .LL_182%=
s_cmp_eq_u32 s59, 4
s_cbranch_scc0 .LL_183%=
Ra 16,21,0
Ra 232,1,4096
s_branch .LL_182%=
.LL_183%=:
buffer_load_dwordx4 a[16:19], v19, s[8:11], s78 offen offset:0 sc1
buffer_load_dwordx4 a[232:235], v19, s[8:11], s65 offen offset:0 sc1
.LL_182%=:
s_bitcmp1_b32 s69, 5
s_cbranch_scc0 .LL_184%=
s_cmp_eq_u32 s59, 5
s_cbranch_scc0 .LL_185%=
Ra 20,21,0
Ra 236,1,4096
s_branch .LL_184%=
.LL_185%=:
buffer_load_dwordx4 a[20:23], v19, s[8:11], s78 offen offset:1024 sc1
buffer_load_dwordx4 a[236:239], v19, s[8:11], s65 offen offset:1024 sc1
.LL_184%=:
s_bitcmp1_b32 s69, 6
s_cbranch_scc0 .LL_186%=
s_cmp_eq_u32 s59, 6
s_cbranch_scc0 .LL_187%=
Ra 24,21,0
Ra 240,1,4096
s_branch .LL_186%=
.LL_187%=:
buffer_load_dwordx4 a[24:27], v19, s[8:11], s78 offen offset:2048 sc1
buffer_load_dwordx4 a[240:243], v19, s[8:11], s65 offen offset:2048 sc1
.LL_186%=:
s_bitcmp1_b32 s69, 7
s_cbranch_scc0 .LL_188%=
s_cmp_eq_u32 s59, 7
s_cbranch_scc0 .LL_189%=
Ra 28,21,0
Ra 244,1,4096
s_branch .LL_188%=
.LL_189%=:
buffer_load_dwordx4 a[28:31], v19, s[8:11], s78 offen offset:3072 sc1
buffer_load_dwordx4 a[244:247], v19, s[8:11], s65 offen offset:3072 sc1
.LL_188%=:
.LL_173%=:
s_and_b32 s80, s69, 65280
s_cbranch_scc0 .LL_190%=
s_bitcmp1_b32 s69, 8
s_cbranch_scc0 .LL_191%=
s_cmp_eq_u32 s59, 0
s_cbranch_scc0 .LL_192%=
Ra 32,21,5120
s_branch .LL_191%=
.LL_192%=:
buffer_load_dwordx4 a[32:35], v1, s[8:11], s79 offen offset:0 sc1
.LL_191%=:
s_bitcmp1_b32 s69, 9
s_cbranch_scc0 .LL_193%=
s_cmp_eq_u32 s59, 1
s_cbranch_scc0 .LL_194%=
Ra 36,21,5120
s_branch .LL_193%=
.LL_194%=:
buffer_load_dwordx4 a[36:39], v1, s[8:11], s79 offen offset:1024 sc1
.LL_193%=:
s_bitcmp1_b32 s69, 10
s_cbranch_scc0 .LL_195%=
s_cmp_eq_u32 s59, 2
s_cbranch_scc0 .LL_196%=
Ra 40,21,5120
s_branch .LL_195%=
.LL_196%=:
buffer_load_dwordx4 a[40:43], v1, s[8:11], s79 offen offset:2048 sc1
.LL_195%=:
s_bitcmp1_b32 s69, 11
s_cbranch_scc0 .LL_197%=
s_cmp_eq_u32 s59, 3
s_cbranch_scc0 .LL_198%=
Ra 44,21,5120
s_branch .LL_197%=
.LL_198%=:
buffer_load_dwordx4 a[44:47], v1, s[8:11], s79 offen offset:3072 sc1
.LL_197%=:
s_bitcmp1_b32 s69, 12
s_cbranch_scc0 .LL_199%=
s_cmp_eq_u32 s59, 4
s_cbranch_scc0 .LL_200%=
Ra 48,21,5120
s_branch .LL_199%=
.LL_200%=:
buffer_load_dwordx4 a[48:51], v19, s[8:11], s79 offen offset:0 sc1
.LL_199%=:
s_bitcmp1_b32 s69, 13
s_cbranch_scc0 .LL_201%=
s_cmp_eq_u32 s59, 5
s_cbranch_scc0 .LL_202%=
Ra 52,21,5120
s_branch .LL_201%=
.LL_202%=:
buffer_load_dwordx4 a[52:55], v19, s[8:11], s79 offen offset:1024 sc1
.LL_201%=:
s_bitcmp1_b32 s69, 14
s_cbranch_scc0 .LL_203%=
s_cmp_eq_u32 s59, 6
s_cbranch_scc0 .LL_204%=
Ra 56,21,5120
s_branch .LL_203%=
.LL_204%=:
buffer_load_dwordx4 a[56:59], v19, s[8:11], s79 offen offset:2048 sc1
.LL_203%=:
s_bitcmp1_b32 s69, 15
s_cbranch_scc0 .LL_205%=
s_cmp_eq_u32 s59, 7
s_cbranch_scc0 .LL_206%=
Ra 60,21,5120
s_branch .LL_205%=
.LL_206%=:
buffer_load_dwordx4 a[60:63], v19, s[8:11], s79 offen offset:3072 sc1
.LL_205%=:
.LL_190%=:
s_or_b32 s71, s71, s69
.LL_172%=:
s_andn2_b32 s80, 0xffff, s71
s_cbranch_scc0 .LL_171%=
s_sleep 4
buffer_load_dwordx2 v[16:17], v18, s[8:11], s63 offen sc1
s_and_b32 s80, s70, 0x100ff
s_cmp_eq_u32 s80, 0xff
s_cbranch_scc0 .LL_170%=
W 0
Zvva 80,4,0
Zvva 84,8,0
Zvva 88,4,216
Zvva 92,8,216
Mvva 80,4,4
Mvva 84,8,4
Mvva 88,4,220
Mvva 92,8,220
Mvva 80,4,8
Mvva 84,8,8
Mvva 88,4,224
Mvva 92,8,224
Mvva 80,4,12
Mvva 84,8,12
Mvva 88,4,228
Mvva 92,8,228
Mvva 80,4,16
Mvva 84,8,16
Mvva 88,4,232
Mvva 92,8,232
Mvva 80,4,20
Mvva 84,8,20
Mvva 88,4,236
Mvva 92,8,236
Mvva 80,4,24
Mvva 84,8,24
Mvva 88,4,240
Mvva 92,8,240
Mvva 80,4,28
Mvva 84,8,28
Mvva 88,4,244
Mvva 92,8,244
s_nop 4
K 160,80,81
K 161,82,83
K 162,84,85
K 163,86,87
v_lshlrev_b32 v80, 16, v160
v_and_b32 v81, 0xffff0000, v160
v_lshlrev_b32 v82, 16, v161
v_and_b32 v83, 0xffff0000, v161
v_lshlrev_b32 v84, 16, v162
v_and_b32 v85, 0xffff0000, v162
v_lshlrev_b32 v86, 16, v163
v_and_b32 v87, 0xffff0000, v163
v_pk_add_f32 v[80:81], v[32:33], v[80:81]
v_pk_add_f32 v[82:83], v[34:35], v[82:83]
v_pk_add_f32 v[84:85], v[36:37], v[84:85]
v_pk_add_f32 v[86:87], v[38:39], v[86:87]
K 160,80,81
K 161,82,83
K 162,84,85
K 163,86,87
v_lshlrev_b32 v80, 16, v160
v_and_b32 v81, 0xffff0000, v160
v_lshlrev_b32 v82, 16, v161
v_and_b32 v83, 0xffff0000, v161
v_lshlrev_b32 v84, 16, v162
v_and_b32 v85, 0xffff0000, v162
v_lshlrev_b32 v86, 16, v163
v_and_b32 v87, 0xffff0000, v163
v_pk_add_f32 v[80:81], v[40:41], v[80:81]
v_pk_add_f32 v[82:83], v[42:43], v[82:83]
v_pk_add_f32 v[84:85], v[44:45], v[84:85]
v_pk_add_f32 v[86:87], v[46:47], v[86:87]
K 80,80,81
K 81,82,83
K 82,84,85
K 83,86,87
buffer_store_dwordx4 v[80:83], v20, s[20:23], s72 offen nt
K 160,88,89
K 161,90,91
K 162,92,93
K 163,94,95
v_lshlrev_b32 v88, 16, v160
v_and_b32 v89, 0xffff0000, v160
)"
R"(v_lshlrev_b32 v90, 16, v161
v_and_b32 v91, 0xffff0000, v161
v_lshlrev_b32 v92, 16, v162
v_and_b32 v93, 0xffff0000, v162
v_lshlrev_b32 v94, 16, v163
v_and_b32 v95, 0xffff0000, v163
v_pk_add_f32 v[88:89], v[64:65], v[88:89]
v_pk_add_f32 v[90:91], v[66:67], v[90:91]
v_pk_add_f32 v[92:93], v[68:69], v[92:93]
v_pk_add_f32 v[94:95], v[70:71], v[94:95]
K 160,88,89
K 161,90,91
K 162,92,93
K 163,94,95
v_lshlrev_b32 v88, 16, v160
v_and_b32 v89, 0xffff0000, v160
v_lshlrev_b32 v90, 16, v161
v_and_b32 v91, 0xffff0000, v161
v_lshlrev_b32 v92, 16, v162
v_and_b32 v93, 0xffff0000, v162
v_lshlrev_b32 v94, 16, v163
v_and_b32 v95, 0xffff0000, v163
v_pk_add_f32 v[88:89], v[72:73], v[88:89]
v_pk_add_f32 v[90:91], v[74:75], v[90:91]
v_pk_add_f32 v[92:93], v[76:77], v[92:93]
v_pk_add_f32 v[94:95], v[78:79], v[94:95]
K 88,88,89
K 89,90,91
K 90,92,93
K 91,94,95
buffer_store_dwordx4 v[88:91], v20, s[20:23], s74 offen nt
s_bitset1_b32 s71, 16
.LL_170%=:
s_add_u32 s84, s84, 1
s_waitcnt vmcnt(0)
v_cmp_eq_u64 vcc, s[12:13], v[16:17]
s_or_b32 s67, vcc_lo, s66
s_cmp_lt_u32 s84, 4194304
s_cbranch_scc1 .LL_169%=
.LL_171%=:
s_bitcmp1_b32 s71, 16
s_cbranch_scc1 .LL_207%=
s_and_b32 s80, s70, 0xff
s_cmp_eq_u32 s80, 0xff
s_cbranch_scc1 .LL_208%=
s_waitcnt vmcnt(0)
.LL_208%=:
W 0
Zvva 80,4,0
Zvva 84,8,0
Zvva 88,4,216
Zvva 92,8,216
Mvva 80,4,4
Mvva 84,8,4
Mvva 88,4,220
Mvva 92,8,220
Mvva 80,4,8
Mvva 84,8,8
Mvva 88,4,224
Mvva 92,8,224
Mvva 80,4,12
Mvva 84,8,12
Mvva 88,4,228
Mvva 92,8,228
Mvva 80,4,16
Mvva 84,8,16
Mvva 88,4,232
Mvva 92,8,232
Mvva 80,4,20
Mvva 84,8,20
Mvva 88,4,236
Mvva 92,8,236
Mvva 80,4,24
Mvva 84,8,24
Mvva 88,4,240
Mvva 92,8,240
Mvva 80,4,28
Mvva 84,8,28
Mvva 88,4,244
Mvva 92,8,244
s_nop 4
K 160,80,81
K 161,82,83
K 162,84,85
K 163,86,87
v_lshlrev_b32 v80, 16, v160
v_and_b32 v81, 0xffff0000, v160
v_lshlrev_b32 v82, 16, v161
v_and_b32 v83, 0xffff0000, v161
v_lshlrev_b32 v84, 16, v162
v_and_b32 v85, 0xffff0000, v162
v_lshlrev_b32 v86, 16, v163
v_and_b32 v87, 0xffff0000, v163
v_pk_add_f32 v[80:81], v[32:33], v[80:81]
v_pk_add_f32 v[82:83], v[34:35], v[82:83]
v_pk_add_f32 v[84:85], v[36:37], v[84:85]
v_pk_add_f32 v[86:87], v[38:39], v[86:87]
K 160,80,81
K 161,82,83
K 162,84,85
K 163,86,87
v_lshlrev_b32 v80, 16, v160
v_and_b32 v81, 0xffff0000, v160
v_lshlrev_b32 v82, 16, v161
v_and_b32 v83, 0xffff0000, v161
v_lshlrev_b32 v84, 16, v162
v_and_b32 v85, 0xffff0000, v162
v_lshlrev_b32 v86, 16, v163
v_and_b32 v87, 0xffff0000, v163
v_pk_add_f32 v[80:81], v[40:41], v[80:81]
v_pk_add_f32 v[82:83], v[42:43], v[82:83]
v_pk_add_f32 v[84:85], v[44:45], v[84:85]
v_pk_add_f32 v[86:87], v[46:47], v[86:87]
K 80,80,81
K 81,82,83
K 82,84,85
K 83,86,87
buffer_store_dwordx4 v[80:83], v20, s[20:23], s72 offen nt
K 160,88,89
K 161,90,91
K 162,92,93
K 163,94,95
v_lshlrev_b32 v88, 16, v160
v_and_b32 v89, 0xffff0000, v160
v_lshlrev_b32 v90, 16, v161
v_and_b32 v91, 0xffff0000, v161
v_lshlrev_b32 v92, 16, v162
v_and_b32 v93, 0xffff0000, v162
v_lshlrev_b32 v94, 16, v163
v_and_b32 v95, 0xffff0000, v163
v_pk_add_f32 v[88:89], v[64:65], v[88:89]
v_pk_add_f32 v[90:91], v[66:67], v[90:91]
v_pk_add_f32 v[92:93], v[68:69], v[92:93]
v_pk_add_f32 v[94:95], v[70:71], v[94:95]
K 160,88,89
K 161,90,91
K 162,92,93
K 163,94,95
v_lshlrev_b32 v88, 16, v160
v_and_b32 v89, 0xffff0000, v160
v_lshlrev_b32 v90, 16, v161
v_and_b32 v91, 0xffff0000, v161
v_lshlrev_b32 v92, 16, v162
v_and_b32 v93, 0xffff0000, v162
v_lshlrev_b32 v94, 16, v163
v_and_b32 v95, 0xffff0000, v163
v_pk_add_f32 v[88:89], v[72:73], v[88:89]
v_pk_add_f32 v[90:91], v[74:75], v[90:91]
v_pk_add_f32 v[92:93], v[76:77], v[92:93]
v_pk_add_f32 v[94:95], v[78:79], v[94:95]
K 88,88,89
K 89,90,91
K 90,92,93
K 91,94,95
buffer_store_dwordx4 v[88:91], v20, s[20:23], s74 offen nt
s_waitcnt vmcnt(2)
W 0
Zvva 80,4,32
Zvva 84,8,32
Mvva 80,4,36
Mvva 84,8,36
Mvva 80,4,40
Mvva 84,8,40
Mvva 80,4,44
Mvva 84,8,44
Mvva 80,4,48
Mvva 84,8,48
Mvva 80,4,52
Mvva 84,8,52
Mvva 80,4,56
Mvva 84,8,56
Mvva 80,4,60
Mvva 84,8,60
s_nop 6
K 160,80,81
K 161,82,83
K 162,84,85
K 163,86,87
v_lshlrev_b32 v80, 16, v160
v_and_b32 v81, 0xffff0000, v160
v_lshlrev_b32 v82, 16, v161
v_and_b32 v83, 0xffff0000, v161
v_lshlrev_b32 v84, 16, v162
v_and_b32 v85, 0xffff0000, v162
v_lshlrev_b32 v86, 16, v163
v_and_b32 v87, 0xffff0000, v163
v_pk_add_f32 v[80:81], v[48:49], v[80:81]
v_pk_add_f32 v[82:83], v[50:51], v[82:83]
v_pk_add_f32 v[84:85], v[52:53], v[84:85]
v_pk_add_f32 v[86:87], v[54:55], v[86:87]
K 160,80,81
K 161,82,83
K 162,84,85
K 163,86,87
v_lshlrev_b32 v80, 16, v160
v_and_b32 v81, 0xffff0000, v160
v_lshlrev_b32 v82, 16, v161
v_and_b32 v83, 0xffff0000, v161
v_lshlrev_b32 v84, 16, v162
v_and_b32 v85, 0xffff0000, v162
v_lshlrev_b32 v86, 16, v163
v_and_b32 v87, 0xffff0000, v163
v_pk_add_f32 v[80:81], v[56:57], v[80:81]
v_pk_add_f32 v[82:83], v[58:59], v[82:83]
v_pk_add_f32 v[84:85], v[60:61], v[84:85]
v_pk_add_f32 v[86:87], v[62:63], v[86:87]
K 80,80,81
K 81,82,83
K 82,84,85
K 83,86,87
buffer_store_dwordx4 v[80:83], v20, s[20:23], s73 offen nt
s_branch .LL_126%=
.LL_207%=:
s_waitcnt vmcnt(0)
W 0
Zvva 80,4,32
Zvva 84,8,32
Mvva 80,4,36
Mvva 84,8,36
Mvva 80,4,40
Mvva 84,8,40
Mvva 80,4,44
Mvva 84,8,44
Mvva 80,4,48
Mvva 84,8,48
Mvva 80,4,52
Mvva 84,8,52
Mvva 80,4,56
Mvva 84,8,56
Mvva 80,4,60
Mvva 84,8,60
s_nop 6
K 160,80,81
K 161,82,83
K 162,84,85
K 163,86,87
v_lshlrev_b32 v80, 16, v160
v_and_b32 v81, 0xffff0000, v160
v_lshlrev_b32 v82, 16, v161
v_and_b32 v83, 0xffff0000, v161
v_lshlrev_b32 v84, 16, v162
v_and_b32 v85, 0xffff0000, v162
v_lshlrev_b32 v86, 16, v163
v_and_b32 v87, 0xffff0000, v163
v_pk_add_f32 v[80:81], v[48:49], v[80:81]
v_pk_add_f32 v[82:83], v[50:51], v[82:83]
v_pk_add_f32 v[84:85], v[52:53], v[84:85]
v_pk_add_f32 v[86:87], v[54:55], v[86:87]
K 160,80,81
K 161,82,83
K 162,84,85
K 163,86,87
v_lshlrev_b32 v80, 16, v160
v_and_b32 v81, 0xffff0000, v160
v_lshlrev_b32 v82, 16, v161
v_and_b32 v83, 0xffff0000, v161
v_lshlrev_b32 v84, 16, v162
v_and_b32 v85, 0xffff0000, v162
v_lshlrev_b32 v86, 16, v163
v_and_b32 v87, 0xffff0000, v163
v_pk_add_f32 v[80:81], v[56:57], v[80:81]
v_pk_add_f32 v[82:83], v[58:59], v[82:83]
v_pk_add_f32 v[84:85], v[60:61], v[84:85]
v_pk_add_f32 v[86:87], v[62:63], v[86:87]
K 80,80,81
K 81,82,83
K 82,84,85
K 83,86,87
buffer_store_dwordx4 v[80:83], v20, s[20:23], s73 offen nt
s_branch .LL_126%=
.LL_128%=:
s_mov_b32 s84, 0
.LL_209%=:
s_or_b32 s70, s71, s66
s_andn2_b32 s69, s67, s71
s_and_b32 s69, s69, 0xffff
s_cbranch_scc0 .LL_212%=
s_and_b32 s80, s69, 255
s_cbranch_scc0 .LL_213%=
s_bitcmp1_b32 s69, 0
s_cbranch_scc0 .LL_214%=
s_cmp_eq_u32 s59, 0
s_cbranch_scc0 .LL_215%=
Ra 0,21,0
s_branch .LL_214%=
.LL_215%=:
buffer_load_dwordx4 a[0:3], v1, s[8:11], s78 offen offset:0 sc1
.LL_214%=:
s_bitcmp1_b32 s69, 1
s_cbranch_scc0 .LL_216%=
s_cmp_eq_u32 s59, 1
s_cbranch_scc0 .LL_217%=
Ra 4,21,0
s_branch .LL_216%=
.LL_217%=:
buffer_load_dwordx4 a[4:7], v1, s[8:11], s78 offen offset:1024 sc1
.LL_216%=:
s_bitcmp1_b32 s69, 2
s_cbranch_scc0 .LL_218%=
s_cmp_eq_u32 s59, 2
s_cbranch_scc0 .LL_219%=
Ra 8,21,0
s_branch .LL_218%=
.LL_219%=:
buffer_load_dwordx4 a[8:11], v1, s[8:11], s78 offen offset:2048 sc1
.LL_218%=:
s_bitcmp1_b32 s69, 3
s_cbranch_scc0 .LL_220%=
s_cmp_eq_u32 s59, 3
s_cbranch_scc0 .LL_221%=
Ra 12,21,0
s_branch .LL_220%=
.LL_221%=:
buffer_load_dwordx4 a[12:15], v1, s[8:11], s78 offen offset:3072 sc1
.LL_220%=:
s_bitcmp1_b32 s69, 4
s_cbranch_scc0 .LL_222%=
s_cmp_eq_u32 s59, 4
s_cbranch_scc0 .LL_223%=
Ra 16,21,0
s_branch .LL_222%=
.LL_223%=:
buffer_load_dwordx4 a[16:19], v19, s[8:11], s78 offen offset:0 sc1
.LL_222%=:
s_bitcmp1_b32 s69, 5
s_cbranch_scc0 .LL_224%=
s_cmp_eq_u32 s59, 5
s_cbranch_scc0 .LL_225%=
Ra 20,21,0
s_branch .LL_224%=
.LL_225%=:
buffer_load_dwordx4 a[20:23], v19, s[8:11], s78 offen offset:1024 sc1
.LL_224%=:
s_bitcmp1_b32 s69, 6
s_cbranch_scc0 .LL_226%=
s_cmp_eq_u32 s59, 6
s_cbranch_scc0 .LL_227%=
Ra 24,21,0
s_branch .LL_226%=
.LL_227%=:
buffer_load_dwordx4 a[24:27], v19, s[8:11], s78 offen offset:2048 sc1
.LL_226%=:
s_bitcmp1_b32 s69, 7
s_cbranch_scc0 .LL_228%=
s_cmp_eq_u32 s59, 7
s_cbranch_scc0 .LL_229%=
Ra 28,21,0
s_branch .LL_228%=
.LL_229%=:
buffer_load_dwordx4 a[28:31], v19, s[8:11], s78 offen offset:3072 sc1
.LL_228%=:
.LL_213%=:
s_and_b32 s80, s69, 65280
s_cbranch_scc0 .LL_230%=
s_bitcmp1_b32 s69, 8
s_cbranch_scc0 .LL_231%=
s_cmp_eq_u32 s59, 0
s_cbranch_scc0 .LL_232%=
Ra 32,21,5120
Ra 216,1,9216
s_branch .LL_231%=
.LL_232%=:
buffer_load_dwordx4 a[32:35], v1, s[8:11], s79 offen offset:0 sc1
buffer_load_dwordx4 a[216:219], v1, s[8:11], s65 offen offset:0 sc1
.LL_231%=:
s_bitcmp1_b32 s69, 9
s_cbranch_scc0 .LL_233%=
s_cmp_eq_u32 s59, 1
s_cbranch_scc0 .LL_234%=
Ra 36,21,5120
Ra 220,1,9216
s_branch .LL_233%=
.LL_234%=:
buffer_load_dwordx4 a[36:39], v1, s[8:11], s79 offen offset:1024 sc1
buffer_load_dwordx4 a[220:223], v1, s[8:11], s65 offen offset:1024 sc1
.LL_233%=:
s_bitcmp1_b32 s69, 10
s_cbranch_scc0 .LL_235%=
s_cmp_eq_u32 s59, 2
s_cbranch_scc0 .LL_236%=
Ra 40,21,5120
Ra 224,1,9216
s_branch .LL_235%=
.LL_236%=:
buffer_load_dwordx4 a[40:43], v1, s[8:11], s79 offen offset:2048 sc1
buffer_load_dwordx4 a[224:227], v1, s[8:11], s65 offen offset:2048 sc1
.LL_235%=:
s_bitcmp1_b32 s69, 11
s_cbranch_scc0 .LL_237%=
s_cmp_eq_u32 s59, 3
s_cbranch_scc0 .LL_238%=
Ra 44,21,5120
Ra 228,1,9216
s_branch .LL_237%=
.LL_238%=:
buffer_load_dwordx4 a[44:47], v1, s[8:11], s79 offen offset:3072 sc1
buffer_load_dwordx4 a[228:231], v1, s[8:11], s65 offen offset:3072 sc1
.LL_237%=:
s_bitcmp1_b32 s69, 12
s_cbranch_scc0 .LL_239%=
)"
R"(s_cmp_eq_u32 s59, 4
s_cbranch_scc0 .LL_240%=
Ra 48,21,5120
Ra 232,1,9216
s_branch .LL_239%=
.LL_240%=:
buffer_load_dwordx4 a[48:51], v19, s[8:11], s79 offen offset:0 sc1
buffer_load_dwordx4 a[232:235], v19, s[8:11], s65 offen offset:0 sc1
.LL_239%=:
s_bitcmp1_b32 s69, 13
s_cbranch_scc0 .LL_241%=
s_cmp_eq_u32 s59, 5
s_cbranch_scc0 .LL_242%=
Ra 52,21,5120
Ra 236,1,9216
s_branch .LL_241%=
.LL_242%=:
buffer_load_dwordx4 a[52:55], v19, s[8:11], s79 offen offset:1024 sc1
buffer_load_dwordx4 a[236:239], v19, s[8:11], s65 offen offset:1024 sc1
.LL_241%=:
s_bitcmp1_b32 s69, 14
s_cbranch_scc0 .LL_243%=
s_cmp_eq_u32 s59, 6
s_cbranch_scc0 .LL_244%=
Ra 56,21,5120
Ra 240,1,9216
s_branch .LL_243%=
.LL_244%=:
buffer_load_dwordx4 a[56:59], v19, s[8:11], s79 offen offset:2048 sc1
buffer_load_dwordx4 a[240:243], v19, s[8:11], s65 offen offset:2048 sc1
.LL_243%=:
s_bitcmp1_b32 s69, 15
s_cbranch_scc0 .LL_245%=
s_cmp_eq_u32 s59, 7
s_cbranch_scc0 .LL_246%=
Ra 60,21,5120
Ra 244,1,9216
s_branch .LL_245%=
.LL_246%=:
buffer_load_dwordx4 a[60:63], v19, s[8:11], s79 offen offset:3072 sc1
buffer_load_dwordx4 a[244:247], v19, s[8:11], s65 offen offset:3072 sc1
.LL_245%=:
.LL_230%=:
s_or_b32 s71, s71, s69
.LL_212%=:
s_andn2_b32 s80, 0xffff, s71
s_cbranch_scc0 .LL_211%=
s_sleep 4
buffer_load_dwordx2 v[16:17], v18, s[8:11], s63 offen sc1
s_and_b32 s80, s70, 0x100ff
s_cmp_eq_u32 s80, 0xff
s_cbranch_scc0 .LL_210%=
W 0
Zvva 80,4,0
Zvva 84,8,0
Mvva 80,4,4
Mvva 84,8,4
Mvva 80,4,8
Mvva 84,8,8
Mvva 80,4,12
Mvva 84,8,12
Mvva 80,4,16
Mvva 84,8,16
Mvva 80,4,20
Mvva 84,8,20
Mvva 80,4,24
Mvva 84,8,24
Mvva 80,4,28
Mvva 84,8,28
s_nop 6
K 160,80,81
K 161,82,83
K 162,84,85
K 163,86,87
v_lshlrev_b32 v80, 16, v160
v_and_b32 v81, 0xffff0000, v160
v_lshlrev_b32 v82, 16, v161
v_and_b32 v83, 0xffff0000, v161
v_lshlrev_b32 v84, 16, v162
v_and_b32 v85, 0xffff0000, v162
v_lshlrev_b32 v86, 16, v163
v_and_b32 v87, 0xffff0000, v163
v_pk_add_f32 v[80:81], v[32:33], v[80:81]
v_pk_add_f32 v[82:83], v[34:35], v[82:83]
v_pk_add_f32 v[84:85], v[36:37], v[84:85]
v_pk_add_f32 v[86:87], v[38:39], v[86:87]
K 160,80,81
K 161,82,83
K 162,84,85
K 163,86,87
v_lshlrev_b32 v80, 16, v160
v_and_b32 v81, 0xffff0000, v160
v_lshlrev_b32 v82, 16, v161
v_and_b32 v83, 0xffff0000, v161
v_lshlrev_b32 v84, 16, v162
v_and_b32 v85, 0xffff0000, v162
v_lshlrev_b32 v86, 16, v163
v_and_b32 v87, 0xffff0000, v163
v_pk_add_f32 v[80:81], v[40:41], v[80:81]
v_pk_add_f32 v[82:83], v[42:43], v[82:83]
v_pk_add_f32 v[84:85], v[44:45], v[84:85]
v_pk_add_f32 v[86:87], v[46:47], v[86:87]
K 80,80,81
K 81,82,83
K 82,84,85
K 83,86,87
buffer_store_dwordx4 v[80:83], v20, s[20:23], s72 offen nt
s_bitset1_b32 s71, 16
.LL_210%=:
s_add_u32 s84, s84, 1
s_waitcnt vmcnt(0)
v_cmp_eq_u64 vcc, s[12:13], v[16:17]
s_or_b32 s67, vcc_lo, s66
s_cmp_lt_u32 s84, 4194304
s_cbranch_scc1 .LL_209%=
.LL_211%=:
s_bitcmp1_b32 s71, 16
s_cbranch_scc1 .LL_247%=
s_and_b32 s80, s70, 0xff
s_cmp_eq_u32 s80, 0xff
s_cbranch_scc1 .LL_248%=
s_waitcnt vmcnt(0)
.LL_248%=:
W 0
Zvva 80,4,0
Zvva 84,8,0
Mvva 80,4,4
Mvva 84,8,4
Mvva 80,4,8
Mvva 84,8,8
Mvva 80,4,12
Mvva 84,8,12
Mvva 80,4,16
Mvva 84,8,16
Mvva 80,4,20
Mvva 84,8,20
Mvva 80,4,24
Mvva 84,8,24
Mvva 80,4,28
Mvva 84,8,28
s_nop 6
K 160,80,81
K 161,82,83
K 162,84,85
K 163,86,87
v_lshlrev_b32 v80, 16, v160
v_and_b32 v81, 0xffff0000, v160
v_lshlrev_b32 v82, 16, v161
v_and_b32 v83, 0xffff0000, v161
v_lshlrev_b32 v84, 16, v162
v_and_b32 v85, 0xffff0000, v162
v_lshlrev_b32 v86, 16, v163
v_and_b32 v87, 0xffff0000, v163
v_pk_add_f32 v[80:81], v[32:33], v[80:81]
v_pk_add_f32 v[82:83], v[34:35], v[82:83]
v_pk_add_f32 v[84:85], v[36:37], v[84:85]
v_pk_add_f32 v[86:87], v[38:39], v[86:87]
K 160,80,81
K 161,82,83
K 162,84,85
K 163,86,87
v_lshlrev_b32 v80, 16, v160
v_and_b32 v81, 0xffff0000, v160
v_lshlrev_b32 v82, 16, v161
v_and_b32 v83, 0xffff0000, v161
v_lshlrev_b32 v84, 16, v162
v_and_b32 v85, 0xffff0000, v162
v_lshlrev_b32 v86, 16, v163
v_and_b32 v87, 0xffff0000, v163
v_pk_add_f32 v[80:81], v[40:41], v[80:81]
v_pk_add_f32 v[82:83], v[42:43], v[82:83]
v_pk_add_f32 v[84:85], v[44:45], v[84:85]
v_pk_add_f32 v[86:87], v[46:47], v[86:87]
K 80,80,81
K 81,82,83
K 82,84,85
K 83,86,87
buffer_store_dwordx4 v[80:83], v20, s[20:23], s72 offen nt
s_waitcnt vmcnt(1)
W 0
Zvva 80,4,32
Zvva 84,8,32
Zvva 88,4,216
Zvva 92,8,216
Mvva 80,4,36
Mvva 84,8,36
Mvva 88,4,220
Mvva 92,8,220
Mvva 80,4,40
Mvva 84,8,40
Mvva 88,4,224
Mvva 92,8,224
Mvva 80,4,44
Mvva 84,8,44
Mvva 88,4,228
Mvva 92,8,228
Mvva 80,4,48
Mvva 84,8,48
Mvva 88,4,232
Mvva 92,8,232
Mvva 80,4,52
Mvva 84,8,52
Mvva 88,4,236
Mvva 92,8,236
Mvva 80,4,56
Mvva 84,8,56
Mvva 88,4,240
Mvva 92,8,240
Mvva 80,4,60
Mvva 84,8,60
Mvva 88,4,244
Mvva 92,8,244
s_nop 4
K 160,80,81
K 161,82,83
K 162,84,85
K 163,86,87
v_lshlrev_b32 v80, 16, v160
v_and_b32 v81, 0xffff0000, v160
v_lshlrev_b32 v82, 16, v161
v_and_b32 v83, 0xffff0000, v161
v_lshlrev_b32 v84, 16, v162
v_and_b32 v85, 0xffff0000, v162
v_lshlrev_b32 v86, 16, v163
v_and_b32 v87, 0xffff0000, v163
v_pk_add_f32 v[80:81], v[48:49], v[80:81]
v_pk_add_f32 v[82:83], v[50:51], v[82:83]
v_pk_add_f32 v[84:85], v[52:53], v[84:85]
v_pk_add_f32 v[86:87], v[54:55], v[86:87]
K 160,80,81
K 161,82,83
K 162,84,85
K 163,86,87
v_lshlrev_b32 v80, 16, v160
v_and_b32 v81, 0xffff0000, v160
v_lshlrev_b32 v82, 16, v161
v_and_b32 v83, 0xffff0000, v161
v_lshlrev_b32 v84, 16, v162
v_and_b32 v85, 0xffff0000, v162
v_lshlrev_b32 v86, 16, v163
v_and_b32 v87, 0xffff0000, v163
v_pk_add_f32 v[80:81], v[56:57], v[80:81]
v_pk_add_f32 v[82:83], v[58:59], v[82:83]
v_pk_add_f32 v[84:85], v[60:61], v[84:85]
v_pk_add_f32 v[86:87], v[62:63], v[86:87]
K 80,80,81
K 81,82,83
K 82,84,85
K 83,86,87
buffer_store_dwordx4 v[80:83], v20, s[20:23], s73 offen nt
K 160,88,89
K 161,90,91
K 162,92,93
K 163,94,95
v_lshlrev_b32 v88, 16, v160
v_and_b32 v89, 0xffff0000, v160
v_lshlrev_b32 v90, 16, v161
v_and_b32 v91, 0xffff0000, v161
v_lshlrev_b32 v92, 16, v162
v_and_b32 v93, 0xffff0000, v162
v_lshlrev_b32 v94, 16, v163
v_and_b32 v95, 0xffff0000, v163
v_pk_add_f32 v[88:89], v[64:65], v[88:89]
v_pk_add_f32 v[90:91], v[66:67], v[90:91]
v_pk_add_f32 v[92:93], v[68:69], v[92:93]
v_pk_add_f32 v[94:95], v[70:71], v[94:95]
K 160,88,89
K 161,90,91
K 162,92,93
K 163,94,95
v_lshlrev_b32 v88, 16, v160
v_and_b32 v89, 0xffff0000, v160
v_lshlrev_b32 v90, 16, v161
v_and_b32 v91, 0xffff0000, v161
v_lshlrev_b32 v92, 16, v162
v_and_b32 v93, 0xffff0000, v162
v_lshlrev_b32 v94, 16, v163
v_and_b32 v95, 0xffff0000, v163
v_pk_add_f32 v[88:89], v[72:73], v[88:89]
v_pk_add_f32 v[90:91], v[74:75], v[90:91]
v_pk_add_f32 v[92:93], v[76:77], v[92:93]
v_pk_add_f32 v[94:95], v[78:79], v[94:95]
K 88,88,89
K 89,90,91
K 90,92,93
K 91,94,95
buffer_store_dwordx4 v[88:91], v20, s[20:23], s74 offen nt
s_branch .LL_126%=
.LL_247%=:
s_waitcnt vmcnt(0)
W 0
Zvva 80,4,32
Zvva 84,8,32
Zvva 88,4,216
Zvva 92,8,216
Mvva 80,4,36
Mvva 84,8,36
Mvva 88,4,220
Mvva 92,8,220
Mvva 80,4,40
Mvva 84,8,40
Mvva 88,4,224
Mvva 92,8,224
Mvva 80,4,44
Mvva 84,8,44
Mvva 88,4,228
Mvva 92,8,228
Mvva 80,4,48
Mvva 84,8,48
Mvva 88,4,232
Mvva 92,8,232
Mvva 80,4,52
Mvva 84,8,52
Mvva 88,4,236
Mvva 92,8,236
Mvva 80,4,56
Mvva 84,8,56
Mvva 88,4,240
Mvva 92,8,240
Mvva 80,4,60
Mvva 84,8,60
Mvva 88,4,244
Mvva 92,8,244
s_nop 4
K 160,80,81
K 161,82,83
K 162,84,85
K 163,86,87
v_lshlrev_b32 v80, 16, v160
v_and_b32 v81, 0xffff0000, v160
v_lshlrev_b32 v82, 16, v161
v_and_b32 v83, 0xffff0000, v161
v_lshlrev_b32 v84, 16, v162
v_and_b32 v85, 0xffff0000, v162
v_lshlrev_b32 v86, 16, v163
v_and_b32 v87, 0xffff0000, v163
v_pk_add_f32 v[80:81], v[48:49], v[80:81]
v_pk_add_f32 v[82:83], v[50:51], v[82:83]
v_pk_add_f32 v[84:85], v[52:53], v[84:85]
v_pk_add_f32 v[86:87], v[54:55], v[86:87]
K 160,80,81
K 161,82,83
K 162,84,85
K 163,86,87
v_lshlrev_b32 v80, 16, v160
v_and_b32 v81, 0xffff0000, v160
v_lshlrev_b32 v82, 16, v161
v_and_b32 v83, 0xffff0000, v161
v_lshlrev_b32 v84, 16, v162
v_and_b32 v85, 0xffff0000, v162
v_lshlrev_b32 v86, 16, v163
v_and_b32 v87, 0xffff0000, v163
v_pk_add_f32 v[80:81], v[56:57], v[80:81]
v_pk_add_f32 v[82:83], v[58:59], v[82:83]
v_pk_add_f32 v[84:85], v[60:61], v[84:85]
v_pk_add_f32 v[86:87], v[62:63], v[86:87]
K 80,80,81
K 81,82,83
K 82,84,85
K 83,86,87
buffer_store_dwordx4 v[80:83], v20, s[20:23], s73 offen nt
K 160,88,89
K 161,90,91
K 162,92,93
K 163,94,95
v_lshlrev_b32 v88, 16, v160
v_and_b32 v89, 0xffff0000, v160
v_lshlrev_b32 v90, 16, v161
v_and_b32 v91, 0xffff0000, v161
v_lshlrev_b32 v92, 16, v162
v_and_b32 v93, 0xffff0000, v162
v_lshlrev_b32 v94, 16, v163
v_and_b32 v95, 0xffff0000, v163
v_pk_add_f32 v[88:89], v[64:65], v[88:89]
v_pk_add_f32 v[90:91], v[66:67], v[90:91]
v_pk_add_f32 v[92:93], v[68:69], v[92:93]
v_pk_add_f32 v[94:95], v[70:71], v[94:95]
K 160,88,89
K 161,90,91
K 162,92,93
K 163,94,95
v_lshlrev_b32 v88, 16, v160
v_and_b32 v89, 0xffff0000, v160
v_lshlrev_b32 v90, 16, v161
v_and_b32 v91, 0xffff0000, v161
v_lshlrev_b32 v92, 16, v162
v_and_b32 v93, 0xffff0000, v162
v_lshlrev_b32 v94, 16, v163
v_and_b32 v95, 0xffff0000, v163
v_pk_add_f32 v[88:89], v[72:73], v[88:89]
v_pk_add_f32 v[90:91], v[74:75], v[90:91]
v_pk_add_f32 v[92:93], v[76:77], v[92:93]
v_pk_add_f32 v[94:95], v[78:79], v[94:95]
K 88,88,89
K 89,90,91
K 90,92,93
K 91,94,95
buffer_store_dwordx4 v[88:91], v20, s[20:23], s74 offen nt
.LL_126%=:
s_lshl_b32 s80, s15, 2
)"
R"(s_add_u32 s80, s80, s14
s_cmp_eq_u32 s80, 4
s_cbranch_scc0 .LL_249%=
v_mov_b64 v[28:29], s[12:13]
s_mov_b64 exec, 1
buffer_store_dwordx2 v[28:29], off, s[8:11], 0 sc1
s_mov_b64 exec, -1
.LL_249%=:
s_branch .Lend_250%=
.Lup_119%=:
s_waitcnt vmcnt(3)
s_mov_b32 s84, 0
.LL_121%=:
v_min3_u32 v176, v101, v103, v109
v_max3_u32 v177, v101, v103, v109
v_min3_u32 v176, v176, v111, v117
v_max3_u32 v177, v177, v111, v117
v_min3_u32 v176, v176, v119, v125
v_max3_u32 v177, v177, v119, v125
v_min_u32 v176, v176, v127
v_max_u32 v177, v177, v127
v_cmp_eq_u32 vcc, s13, v176
v_cmp_eq_u32_e64 s[86:87], s13, v177
s_and_b64 vcc, vcc, s[86:87]
s_cmp_eq_u64 vcc, -1
s_cbranch_scc1 .LL_122%=
s_add_u32 s84, s84, 1
s_cmp_lt_u32 s84, 1048576
s_cbranch_scc1 .LL_123%=
.LL_122%=:
v_readlane_b32 s70, v12, s69
v_bfe_u32 v2, v1, 4, 4
s_nop 2
s_lshr_b32 s80, s70, 4
s_and_b32 s70, s70, 15
v_cmp_eq_u32 vcc, s70, v2
s_cmp_eq_u32 s80, s61
s_cselect_b64 s[86:87], vcc, 0
s_cselect_b64 s[88:89], 0, vcc
s_bitcmp1_b32 s14, 0
s_cselect_b64 vcc, s[88:89], s[86:87]
v_lshlrev_b32 v176, 16, v100
v_and_b32 v177, 0xffff0000, v100
v_lshlrev_b32 v178, 16, v102
v_and_b32 v179, 0xffff0000, v102
v_lshlrev_b32 v180, 16, v108
v_and_b32 v181, 0xffff0000, v108
v_lshlrev_b32 v182, 16, v110
v_and_b32 v183, 0xffff0000, v110
A 176,176,40
A 177,177,41
A 178,178,42
A 179,179,43
A 180,180,44
A 181,181,45
A 182,182,46
A 183,183,47
K 184,176,177
K 185,178,179
K 186,180,181
K 187,182,183
v_lshlrev_b32 v176, 16, v184
v_and_b32 v177, 0xffff0000, v184
v_lshlrev_b32 v178, 16, v185
v_and_b32 v179, 0xffff0000, v185
v_lshlrev_b32 v180, 16, v186
v_and_b32 v181, 0xffff0000, v186
v_lshlrev_b32 v182, 16, v187
v_and_b32 v183, 0xffff0000, v187
v_cndmask_b32_e64 v40, v40, v176, s[86:87]
v_cndmask_b32_e64 v41, v41, v177, s[86:87]
v_cndmask_b32_e64 v42, v42, v178, s[86:87]
v_cndmask_b32_e64 v43, v43, v179, s[86:87]
v_cndmask_b32_e64 v44, v44, v180, s[86:87]
v_cndmask_b32_e64 v45, v45, v181, s[86:87]
v_cndmask_b32_e64 v46, v46, v182, s[86:87]
v_cndmask_b32_e64 v47, v47, v183, s[86:87]
v_lshlrev_b32 v176, 16, v100
v_and_b32 v177, 0xffff0000, v100
v_lshlrev_b32 v178, 16, v102
v_and_b32 v179, 0xffff0000, v102
v_lshlrev_b32 v180, 16, v108
v_and_b32 v181, 0xffff0000, v108
v_lshlrev_b32 v182, 16, v110
v_and_b32 v183, 0xffff0000, v110
A 176,176,56
A 177,177,57
A 178,178,58
A 179,179,59
A 180,180,60
A 181,181,61
A 182,182,62
A 183,183,63
K 184,176,177
K 185,178,179
K 186,180,181
K 187,182,183
v_lshlrev_b32 v176, 16, v184
v_and_b32 v177, 0xffff0000, v184
v_lshlrev_b32 v178, 16, v185
v_and_b32 v179, 0xffff0000, v185
v_lshlrev_b32 v180, 16, v186
v_and_b32 v181, 0xffff0000, v186
v_lshlrev_b32 v182, 16, v187
v_and_b32 v183, 0xffff0000, v187
v_cndmask_b32_e64 v56, v56, v176, s[88:89]
v_cndmask_b32_e64 v57, v57, v177, s[88:89]
v_cndmask_b32_e64 v58, v58, v178, s[88:89]
v_cndmask_b32_e64 v59, v59, v179, s[88:89]
v_cndmask_b32_e64 v60, v60, v180, s[88:89]
v_cndmask_b32_e64 v61, v61, v181, s[88:89]
v_cndmask_b32_e64 v62, v62, v182, s[88:89]
v_cndmask_b32_e64 v63, v63, v183, s[88:89]
v_lshlrev_b32 v176, 16, v116
v_and_b32 v177, 0xffff0000, v116
v_lshlrev_b32 v178, 16, v118
v_and_b32 v179, 0xffff0000, v118
v_lshlrev_b32 v180, 16, v124
v_and_b32 v181, 0xffff0000, v124
v_lshlrev_b32 v182, 16, v126
v_and_b32 v183, 0xffff0000, v126
A 176,176,72
A 177,177,73
A 178,178,74
A 179,179,75
A 180,180,76
A 181,181,77
A 182,182,78
A 183,183,79
K 184,176,177
K 185,178,179
K 186,180,181
K 187,182,183
v_lshlrev_b32 v176, 16, v184
v_and_b32 v177, 0xffff0000, v184
v_lshlrev_b32 v178, 16, v185
v_and_b32 v179, 0xffff0000, v185
v_lshlrev_b32 v180, 16, v186
v_and_b32 v181, 0xffff0000, v186
v_lshlrev_b32 v182, 16, v187
v_and_b32 v183, 0xffff0000, v187
v_cndmask_b32_e64 v72, v72, v176, vcc
v_cndmask_b32_e64 v73, v73, v177, vcc
v_cndmask_b32_e64 v74, v74, v178, vcc
v_cndmask_b32_e64 v75, v75, v179, vcc
v_cndmask_b32_e64 v76, v76, v180, vcc
v_cndmask_b32_e64 v77, v77, v181, vcc
v_cndmask_b32_e64 v78, v78, v182, vcc
v_cndmask_b32_e64 v79, v79, v183, vcc
s_bitset0_b32 s68, s69
s_cmp_eq_u32 s68, 0
s_cbranch_scc1 .Lupr_120%=
s_ff1_i32_b32 s69, s68
.LL_123%=:
s_cmp_eq_u32 s64, 0
s_cbranch_scc0 .LL_124%=
s_waitcnt vmcnt(0)
v_lshlrev_b32 v2, 2, v1
v_mov_b64 v[28:29], s[12:13]
s_mov_b64 exec, 3
buffer_store_dwordx2 v[28:29], v2, s[8:11], s53 offen offset:512 sc1
s_mov_b64 exec, -1
s_mov_b32 s64, 1
.LL_124%=:
s_mul_i32 s66, s69, 20
s_mul_i32 s67, s58, 5
s_add_u32 s66, s66, s67
s_lshl_b32 s66, s66, 7
s_add_u32 s66, s66, 98304
s_lshl_b32 s67, s14, 7
s_add_u32 s67, s67, s66
buffer_load_dwordx4 v[100:103], v3, s[8:11], s67 offen offset:0 sc1
buffer_load_dwordx4 v[108:111], v3, s[8:11], s67 offen offset:16 sc1
buffer_load_dwordx4 v[116:119], v3, s[8:11], s66 offen offset:512 sc1
buffer_load_dwordx4 v[124:127], v3, s[8:11], s66 offen offset:528 sc1
s_waitcnt vmcnt(0)
s_branch .LL_121%=
.Lspin_52%=:
s_mov_b32 s84, 0
.Llp_54%=:
s_sleep 2
v_mov_b32 v28, s74
buffer_load_dwordx2 v[28:29], v28, s[8:11], 0 offen offset:3584 sc1
s_waitcnt vmcnt(0)
s_add_u32 s84, s84, 1
v_xor_b32 v28, s12, v28
v_xor_b32 v29, s13, v29
v_lshrrev_b32 v248, 11, v28
v_or_b32 v29, v29, v248
v_cmp_eq_u32 vcc, 0, v29
s_or_b64 vcc, vcc, s[72:73]
s_cmp_eq_u64 vcc, -1
s_cbranch_scc1 .Lok_53%=
s_cmp_lt_u32 s84, 1048576
s_cbranch_scc1 .Llp_54%=
s_branch .Lok_53%=
.Lspin_57%=:
s_mov_b32 s84, 0
.Llp_59%=:
s_sleep 2
v_bfe_u32 v28, v1, 4, 2
v_lshlrev_b32 v28, 3, v28
s_lshl_b32 s80, s56, 9
s_add_u32 s80, s80, 4096
buffer_load_dwordx2 v[28:29], v28, s[8:11], s80 offen sc1
s_waitcnt vmcnt(0)
s_add_u32 s84, s84, 1
v_cmp_eq_u64 vcc, s[12:13], v[28:29]
s_cmp_eq_u64 vcc, -1
s_cbranch_scc1 .Lok_58%=
s_cmp_lt_u32 s84, 1048576
s_cbranch_scc1 .Llp_59%=
s_branch .Lok_58%=
.Lspin_60%=:
s_mov_b32 s84, 0
.Llp_62%=:
s_sleep 2
v_bfe_u32 v28, v1, 4, 4
v_lshlrev_b32 v28, 3, v28
s_lshl_b32 s80, s56, 9
s_add_u32 s80, s80, 4096
buffer_load_dwordx2 v[28:29], v28, s[8:11], s80 offen sc1
s_waitcnt vmcnt(0)
s_add_u32 s84, s84, 1
v_cmp_eq_u64 vcc, s[12:13], v[28:29]
s_cmp_eq_u64 vcc, -1
s_cbranch_scc1 .Lok_61%=
s_cmp_lt_u32 s84, 1048576
s_cbranch_scc1 .Llp_62%=
s_branch .Lok_61%=
.Lspin_63%=:
s_mov_b32 s84, 0
.Llp_65%=:
s_sleep 2
v_bfe_u32 v28, v1, 4, 4
v_lshlrev_b32 v28, 3, v28
s_lshl_b32 s80, s56, 9
s_add_u32 s80, s80, 4224
buffer_load_dwordx2 v[28:29], v28, s[8:11], s80 offen sc1
s_waitcnt vmcnt(0)
s_add_u32 s84, s84, 1
v_cmp_eq_u64 vcc, s[12:13], v[28:29]
s_cmp_eq_u64 vcc, -1
s_cbranch_scc1 .Lok_64%=
s_cmp_lt_u32 s84, 1048576
s_cbranch_scc1 .Llp_65%=
s_branch .Lok_64%=
.Lspin_67%=:
s_mov_b32 s84, 0
.Llp_69%=:
s_sleep 2
v_bfe_u32 v28, v1, 4, 4
v_lshlrev_b32 v28, 3, v28
s_lshl_b32 s80, s56, 9
s_add_u32 s80, s80, 4352
buffer_load_dwordx2 v[28:29], v28, s[8:11], s80 offen sc1
s_waitcnt vmcnt(0)
s_add_u32 s84, s84, 1
v_cmp_eq_u64 vcc, s[12:13], v[28:29]
s_cmp_eq_u64 vcc, -1
s_cbranch_scc1 .Lok_68%=
s_cmp_lt_u32 s84, 1048576
s_cbranch_scc1 .Llp_69%=
s_branch .Lok_68%=
.Lspin_70%=:
s_mov_b32 s84, 0
.Llp_72%=:
s_sleep 2
v_bfe_u32 v28, v1, 4, 4
v_lshlrev_b32 v28, 3, v28
s_lshl_b32 s80, s56, 9
s_add_u32 s80, s80, 4480
buffer_load_dwordx2 v[28:29], v28, s[8:11], s80 offen sc1
s_waitcnt vmcnt(0)
s_add_u32 s84, s84, 1
v_cmp_eq_u64 vcc, s[12:13], v[28:29]
s_cmp_eq_u64 vcc, -1
s_cbranch_scc1 .Lok_71%=
s_cmp_lt_u32 s84, 1048576
s_cbranch_scc1 .Llp_72%=
s_branch .Lok_71%=
.Lspin_73%=:
s_mov_b32 s84, 0
.Llp_75%=:
s_sleep 2
buffer_load_dwordx2 v[8:9], v4, s[8:11], 0 offen offset:2560 sc1
buffer_load_dwordx2 v[10:11], v5, s[8:11], 0 offen offset:3072 sc1
buffer_load_dwordx2 v[12:13], v6, s[8:11], 0 offen offset:3584 sc1
s_waitcnt vmcnt(0)
s_add_u32 s84, s84, 1
v_cmp_eq_u64 vcc, s[12:13], v[8:9]
v_cmp_eq_u64_e64 s[86:87], s[12:13], v[10:11]
s_and_b64 vcc, vcc, s[86:87]
s_or_b64 vcc, vcc, s[20:21]
s_cmp_eq_u64 vcc, -1
s_cbranch_scc1 .Lok_74%=
s_cmp_lt_u32 s84, 1048576
s_cbranch_scc1 .Llp_75%=
s_branch .Lok_74%=
.Lmk_77%=:
s_nop 7
v_cndmask_b32_e64 v168, v241, v168, s[72:73]
v_cndmask_b32_e64 v169, v241, v169, s[74:75]
v_cndmask_b32_e64 v170, v241, v170, s[76:77]
v_cndmask_b32_e64 v171, v241, v171, s[78:79]
v_cndmask_b32_e64 v188, v241, v188, s[72:73]
v_cndmask_b32_e64 v189, v241, v189, s[74:75]
v_cndmask_b32_e64 v190, v241, v190, s[76:77]
v_cndmask_b32_e64 v191, v241, v191, s[78:79]
v_mov_b32 v184, v241
v_mov_b32 v185, v241
v_mov_b32 v186, v241
v_mov_b32 v187, v241
s_branch .Lmkr_78%=
.Lspin_82%=:
s_mov_b32 s84, 0
.Llp_84%=:
s_sleep 2
v_bfe_u32 v28, v1, 4, 2
v_lshlrev_b32 v28, 3, v28
buffer_load_dwordx2 v[28:29], v28, s[8:11], s55 offen sc1
s_waitcnt vmcnt(0)
s_add_u32 s84, s84, 1
v_cmp_eq_u64 vcc, s[12:13], v[28:29]
s_cmp_eq_u64 vcc, -1
s_cbranch_scc1 .Lok_83%=
s_cmp_lt_u32 s84, 1048576
s_cbranch_scc1 .Llp_84%=
s_branch .Lok_83%=
.LL_87%=:
s_mov_b32 s84, 0
.LL_89%=:
s_sleep 2
buffer_load_dwordx2 v[12:13], v27, s[8:11], 0 offen offset:3584 sc1
s_waitcnt vmcnt(0)
s_add_u32 s84, s84, 1
v_xor_b32 v12, s12, v12
v_xor_b32 v13, s13, v13
v_lshrrev_b32 v3, 11, v12
v_or_b32 v13, v13, v3
v_cmp_eq_u32 vcc, 0, v13
s_cmp_eq_u64 vcc, -1
s_cbranch_scc1 .LL_88%=
s_cmp_lt_u32 s84, 1048576
s_cbranch_scc1 .LL_89%=
s_branch .LL_88%=
.Lup_85%=:
v_lshrrev_b32 v3, 8, v1
v_lshlrev_b32 v3, 5, v3
s_ff1_i32_b32 s69, s68
s_mul_i32 s66, s69, 20
s_mul_i32 s67, s58, 5
s_add_u32 s66, s66, s67
s_lshl_b32 s66, s66, 7
s_add_u32 s66, s66, 98304
s_lshl_b32 s67, s14, 7
s_add_u32 s67, s67, s66
buffer_load_dwordx4 v[100:103], v3, s[8:11], s67 offen offset:0 sc1
buffer_load_dwordx4 v[108:111], v3, s[8:11], s67 offen offset:16 sc1
buffer_load_dwordx4 v[116:119], v3, s[8:11], s66 offen offset:512 sc1
buffer_load_dwordx4 v[124:127], v3, s[8:11], s66 offen offset:528 sc1
s_branch .Lupr_86%=
.Lspin_98%=:
s_mov_b32 s84, 0
.Llp_100%=:
s_sleep 2
v_bfe_u32 v28, v1, 4, 2
v_lshlrev_b32 v28, 3, v28
buffer_load_dwordx2 v[28:29], v28, s[8:11], s54 offen sc1
s_waitcnt vmcnt(0)
s_add_u32 s84, s84, 1
v_cmp_eq_u64 vcc, s[12:13], v[28:29]
s_cmp_eq_u64 vcc, -1
s_cbranch_scc1 .Lok_99%=
s_cmp_lt_u32 s84, 1048576
s_cbranch_scc1 .Llp_100%=
s_branch .Lok_99%=
.Lspin_101%=:
s_mov_b32 s84, 0
.Llp_103%=:
s_sleep 2
v_bfe_u32 v28, v1, 4, 2
v_lshlrev_b32 v28, 3, v28
buffer_load_dwordx2 v[28:29], v28, s[8:11], s55 offen sc1
s_waitcnt vmcnt(0)
s_add_u32 s84, s84, 1
v_cmp_eq_u64 vcc, s[12:13], v[28:29]
s_cmp_eq_u64 vcc, -1
s_cbranch_scc1 .Lok_102%=
s_cmp_lt_u32 s84, 1048576
s_cbranch_scc1 .Llp_103%=
s_branch .Lok_102%=
.LL_106%=:
s_mov_b32 s84, 0
.LL_108%=:
s_sleep 2
buffer_load_dwordx2 v[12:13], v27, s[8:11], 0 offen offset:3584 sc1
s_waitcnt vmcnt(0)
s_add_u32 s84, s84, 1
v_xor_b32 v12, s12, v12
)"
R"(v_xor_b32 v13, s13, v13
v_lshrrev_b32 v3, 11, v12
v_or_b32 v13, v13, v3
v_cmp_eq_u32 vcc, 0, v13
s_cmp_eq_u64 vcc, -1
s_cbranch_scc1 .LL_107%=
s_cmp_lt_u32 s84, 1048576
s_cbranch_scc1 .LL_108%=
s_branch .LL_107%=
.Lup_104%=:
v_lshrrev_b32 v3, 8, v1
v_lshlrev_b32 v3, 5, v3
s_ff1_i32_b32 s69, s68
s_mul_i32 s66, s69, 20
s_mul_i32 s67, s58, 5
s_add_u32 s66, s66, s67
s_lshl_b32 s66, s66, 7
s_add_u32 s66, s66, 98304
s_lshl_b32 s67, s14, 7
s_add_u32 s67, s67, s66
buffer_load_dwordx4 v[100:103], v3, s[8:11], s67 offen offset:0 sc1
buffer_load_dwordx4 v[108:111], v3, s[8:11], s67 offen offset:16 sc1
buffer_load_dwordx4 v[116:119], v3, s[8:11], s66 offen offset:512 sc1
buffer_load_dwordx4 v[124:127], v3, s[8:11], s66 offen offset:528 sc1
s_branch .Lupr_105%=
.Lend_250%=:
.Lend%=:
.purgem W
.purgem D
.purgem Ta
.purgem Ra
.purgem Zvaa
.purgem Mvaa
.purgem Zaaa
.purgem Maaa
.purgem A
.purgem U
.purgem K
.purgem Rv
.purgem Mvav
.purgem G
.purgem Zvav
.purgem Cvav
.purgem X
.purgem Cvaa
.purgem Dc
.purgem Maav
.purgem Mvvv
.purgem Zvva
.purgem Mvva
)"
    : : [ka] "s"(__builtin_amdgcn_kernarg_segment_ptr()), [bid] "s"(blockIdx.x), [tid] "v"(threadIdx.x)
    : "v1", "v2", "v3", "v4", "v5", "v6", "v7", "v8", "v9", "v10", "v11", "v12", "v13", "v14", "v15", "v16", "v17", "v18", "v19", "v20", "v21", "v22", "v23", "v24", "v25", "v26", "v27", "v28", "v29", "v30", "v31", "v32", "v33", "v34", "v35", "v36", "v37", "v38", "v39", "v40", "v41", "v42", "v43", "v44", "v45", "v46", "v47", "v48", "v49", "v50", "v51", "v52", "v53", "v54", "v55", "v56", "v57", "v58", "v59", "v60", "v61", "v62", "v63", "v64", "v65", "v66", "v67", "v68", "v69", "v70", "v71", "v72", "v73", "v74", "v75", "v76", "v77", "v78", "v79", "v80", "v81", "v82", "v83", "v84", "v85", "v86", "v87", "v88", "v89", "v90", "v91", "v92", "v93", "v94", "v95", "v96", "v97", "v98", "v99", "v100", "v101", "v102", "v103", "v104", "v105", "v106", "v107", "v108", "v109", "v110", "v111", "v112", "v113", "v114", "v115", "v116", "v117", "v118", "v119", "v120", "v121", "v122", "v123", "v124", "v125", "v126", "v127", "v128", "v129", "v130", "v131", "v132", "v133", "v134", "v135", "v136", "v137", "v138", "v139", "v140", "v141", "v142", "v143", "v144", "v145", "v146", "v147", "v148", "v149", "v150", "v151", "v152", "v153", "v154", "v155", "v156", "v157", "v158", "v159", "v160", "v161", "v162", "v163", "v164", "v165", "v166", "v167", "v168", "v169", "v170", "v171", "v172", "v173", "v174", "v175", "v176", "v177", "v178", "v179", "v180", "v181", "v182", "v183", "v184", "v185", "v186", "v187", "v188", "v189", "v190", "v191", "v192", "v193", "v194", "v195", "v196", "v197", "v198", "v199", "v200", "v201", "v202", "v203", "v204", "v205", "v206", "v207", "v208", "v209", "v210", "v211", "v212", "v213", "v214", "v215", "v216", "v217", "v218", "v219", "v220", "v221", "v222", "v223", "v224", "v225", "v226", "v227", "v228", "v229", "v230", "v231", "v232", "v233", "v234", "v235", "v236", "v237", "v238", "v239", "v240", "v241", "v242", "v243", "v244", "v245", "v246", "v247", "v248", "v249", "v250", "v251", "v252", "v253", "v254", "v255", "a0", "a1", "a2", "a3", "a4", "a5", "a6", "a7", "a8", "a9", "a10", "a11", "a12", "a13", "a14", "a15", "a16", "a17", "a18", "a19", "a20", "a21", "a22", "a23", "a24", "a25", "a26", "a27", "a28", "a29", "a30", "a31", "a32", "a33", "a34", "a35", "a36", "a37", "a38", "a39", "a40", "a41", "a42", "a43", "a44", "a45", "a46", "a47", "a48", "a49", "a50", "a51", "a52", "a53", "a54", "a55", "a56", "a57", "a58", "a59", "a60", "a61", "a62", "a63", "a64", "a65", "a66", "a67", "a68", "a69", "a70", "a71", "a72", "a73", "a74", "a75", "a76", "a77", "a78", "a79", "a80", "a81", "a82", "a83", "a84", "a85", "a86", "a87", "a88", "a89", "a90", "a91", "a92", "a93", "a94", "a95", "a96", "a97", "a98", "a99", "a100", "a101", "a102", "a103", "a104", "a105", "a106", "a107", "a108", "a109", "a110", "a111", "a112", "a113", "a114", "a115", "a116", "a117", "a118", "a119", "a120", "a121", "a122", "a123", "a124", "a125", "a126", "a127", "a128", "a129", "a130", "a131", "a132", "a133", "a134", "a135", "a136", "a137", "a138", "a139", "a140", "a141", "a142", "a143", "a144", "a145", "a146", "a147", "a148", "a149", "a150", "a151", "a152", "a153", "a154", "a155", "a156", "a157", "a158", "a159", "a160", "a161", "a162", "a163", "a164", "a165", "a166", "a167", "a168", "a169", "a170", "a171", "a172", "a173", "a174", "a175", "a176", "a177", "a178", "a179", "a180", "a181", "a182", "a183", "a184", "a185", "a186", "a187", "a188", "a189", "a190", "a191", "a192", "a193", "a194", "a195", "a196", "a197", "a198", "a199", "a200", "a201", "a202", "a203", "a204", "a205", "a206", "a207", "a208", "a209", "a210", "a211", "a212", "a213", "a214", "a215", "a216", "a217", "a218", "a219", "a220", "a221", "a222", "a223", "a224", "a225", "a226", "a227", "a228", "a229", "a230", "a231", "a232", "a233", "a234", "a235", "a236", "a237", "a238", "a239", "a240", "a241", "a242", "a243", "a244", "a245", "a246", "a247", "a248", "a249", "a250", "a251", "a252", "a253", "a254", "a255", "s4", "s5", "s6", "s7", "s8", "s9", "s10", "s11", "s12", "s13", "s14", "s15", "s16", "s17", "s18", "s19", "s20", "s21", "s22", "s23", "s24", "s25", "s26", "s27", "s28", "s29", "s30", "s31", "s32", "s33", "s34", "s35", "s36", "s37", "s38", "s39", "s40", "s41", "s42", "s43", "s44", "s45", "s46", "s47", "s48", "s49", "s50", "s51", "s52", "s53", "s54", "s55", "s56", "s57", "s58", "s59", "s60", "s61", "s62", "s63", "s64", "s65", "s66", "s67", "s68", "s69", "s70", "s71", "s72", "s73", "s74", "s75", "s76", "s77", "s78", "s79", "s80", "s81", "s82", "s83", "s84", "s85", "s86", "s87", "s88", "s89", "s90", "s91", "s92", "s93", "s94", "s95", "s96", "s97", "s98", "s99", "vcc", "scc", "memory"
  );
}
extern "C" void kernel_launch(void* const* d_in, const int* in_sizes, int n_in, void* d_out, int out_size, void* d_ws, size_t ws_size, hipStream_t stream) {
  (void)hipFuncSetAttribute((const void*)mega_kernel, hipFuncAttributeMaxDynamicSharedMemorySize, LDS_BYTES);
  mega_kernel<<<256, 256, LDS_BYTES, stream>>>((const bf16*)d_in[0], (const bf16*)d_in[1], (const bf16*)d_in[2], (const bf16*)d_in[3], (const bf16*)d_in[4], (const bf16*)d_in[5],
      (const bf16*)d_in[7], (const bf16*)d_in[8], (const bf16*)d_in[9], (const bf16*)d_in[10], (char*)d_ws + 0, (bf16*)d_out);
}
```

```cpp
#include <hip/hip_runtime.h>
#include <hip/hip_bf16.h>
typedef __hip_bfloat16 bf16;
constexpr int LDS_BYTES = 163840;
__global__ __launch_bounds__(256, 1) void mega_kernel(const bf16* x, const bf16* ckv, const bf16* wq, const bf16* wuk, const bf16* wuv, const bf16* wo,
    const bf16* w_r, const bf16* wg, const bf16* wu, const bf16* wd, char* ws, bf16* out) {
  asm volatile(
R"(.macro W n
s_waitcnt lgkmcnt(\n)
.endm
.macro D v,r,s,o
buffer_load_dwordx4 v\v, s[\r:\r+3], s\s offen offset:\o lds
.endm
.macro Ta d,a,o
ds_read_b64_tr_b16 a[\d:\d+1], v\a offset:\o
.endm
.macro Ra d,a,o
ds_read_b128 a[\d:\d+3], v\a offset:\o
.endm
.macro Zvaa d,a,b
v_mfma_f32_16x16x32_bf16 v[\d:\d+3], a[\a:\a+3], a[\b:\b+3], 0
.endm
.macro Mvaa d,a,b
v_mfma_f32_16x16x32_bf16 v[\d:\d+3], a[\a:\a+3], a[\b:\b+3], v[\d:\d+3]
.endm
.macro Zaaa d,a,b
v_mfma_f32_16x16x32_bf16 a[\d:\d+3], a[\a:\a+3], a[\b:\b+3], 0
.endm
.macro Maaa d,a,b
v_mfma_f32_16x16x32_bf16 a[\d:\d+3], a[\a:\a+3], a[\b:\b+3], a[\d:\d+3]
.endm
.macro A d,a,b
v_add_f32 v\d, v\a, v\b
.endm
.macro U d,a,b
v_mul_f32 v\d, v\a, v\b
.endm
.macro K d,a,b
v_cvt_pk_bf16_f32 v\d, v\a, v\b
.endm
.macro Rv d,a,o
ds_read_b128 v[\d:\d+3], v\a offset:\o
.endm
.macro Mvav d,a,b
v_mfma_f32_16x16x32_bf16 v[\d:\d+3], a[\a:\a+3], v[\b:\b+3], v[\d:\d+3]
.endm
.macro G d,a
v_accvgpr_read_b32 v\d, a\a
.endm
.macro Zvav d,a,b
v_mfma_f32_16x16x32_bf16 v[\d:\d+3], a[\a:\a+3], v[\b:\b+3], 0
.endm
.macro Cvav d,a,b,c
v_mfma_f32_16x16x32_bf16 v[\d:\d+3], a[\a:\a+3], v[\b:\b+3], v[\c:\c+3]
.endm
.macro X d
v_exp_f32 v\d, v\d
.endm
.macro Cvaa d,a,b,c
v_mfma_f32_16x16x32_bf16 v[\d:\d+3], a[\a:\a+3], a[\b:\b+3], v[\c:\c+3]
.endm
.macro Dc v,r,s,o
buffer_load_dwordx4 v\v, s[\r:\r+3], s\s offen offset:\o sc1 lds
.endm
.macro Maav d,a,b
v_mfma_f32_16x16x32_bf16 a[\d:\d+3], a[\a:\a+3], v[\b:\b+3], a[\d:\d+3]
.endm
.macro Mvvv d,a,b
v_mfma_f32_16x16x32_bf16 v[\d:\d+3], v[\a:\a+3], v[\b:\b+3], v[\d:\d+3]
.endm
.macro Zvva d,a,b
v_mfma_f32_16x16x32_bf16 v[\d:\d+3], v[\a:\a+3], a[\b:\b+3], 0
.endm
.macro Mvva d,a,b
v_mfma_f32_16x16x32_bf16 v[\d:\d+3], v[\a:\a+3], a[\b:\b+3], v[\d:\d+3]
.endm
s_load_dwordx16 s[16:31], %[ka], 0
s_load_dwordx8 s[32:39], %[ka], 64
s_getpc_b64 s[6:7]
.Lbeg%=:
s_mov_b32 s15, %[bid]
v_lshrrev_b32 v2, 6, %[tid]
v_and_b32 v100, 63, %[tid]
v_readfirstlane_b32 s14, v2
v_lshlrev_b32 v1, 4, v100
v_lshrrev_b32 v101, 4, v100
v_and_b32 v102, 15, v100
s_and_b32 s59, s15, 7
s_bfe_u32 s57, s15, 0x30003
s_lshr_b32 s58, s15, 6
s_lshl_b32 s56, s59, 2
s_add_u32 s56, s56, s58
s_lshr_b32 s80, s15, 3
s_lshl_b32 s80, s80, 2
s_add_u32 s80, s80, s14
s_lshl_b32 s80, s80, 11
v_min_u32 v146, 240, v1
v_lshl_add_u32 v146, v146, 3, s80
v_min_u32 v146, .Lend%=-.Lbeg%=-4, v146
global_load_dword v142, v146, s[6:7]
v_and_b32 v88, 0x1f0, v1
v_lshrrev_b32 v105, 1, v101
v_mul_u32_u24 v105, 1280, v105
v_add_u32 v28, v88, v105
s_lshl_b32 s85, s15, 2
s_add_u32 s85, s85, s14
s_mul_i32 s84, s85, 2560
v_and_b32 v105, 1, v101
v_lshl_or_b32 v29, v105, 31, v28
v_lshlrev_b32 v104, 1, v101
v_xor_b32 v104, v104, v102
v_lshlrev_b32 v104, 4, v104
v_lshl_add_u32 v16, v101, 13, v104
v_xor_b32 v105, 128, v16
v_add_u32 v17, 31744, v16
v_add_u32 v18, 63488, v105
v_add_u32 v19, 95232, v105
v_add_u32 v20, 131072, v16
v_add_u32 v21, 162816, v16
v_add_u32 v22, 194560, v105
v_add_u32 v23, 226304, v105
s_lshl_b32 s80, s14, 3
s_add_u32 s80, s80, s57
s_lshl_b32 s80, s80, 15
v_and_b32 v104, 7, v100
v_xor_b32 v104, v104, v101
v_lshlrev_b32 v104, 4, v104
v_lshrrev_b32 v105, 3, v100
v_lshl_add_u32 v104, v105, 10, v104
v_add_u32 v2, s80, v104
v_xor_b32 v105, 64, v2
v_add_u32 v3, 7168, v105
v_add_u32 v4, 14336, v2
v_add_u32 v5, 21504, v105
v_xor_b32 v6, 1835008, v2
v_xor_b32 v7, 1835008, v3
v_xor_b32 v8, 1835008, v4
v_xor_b32 v9, 1835008, v5
s_lshl_b32 s63, s14, 13
s_add_u32 s61, s63, 65536
s_and_b32 s80, s14, 1
s_lshr_b32 s81, s14, 1
s_lshl_b32 s62, s80, 14
s_lshl_b32 s82, s81, 13
s_add_u32 s62, s62, s82
s_lshl_b32 s81, s81, 18
s_lshl_b32 s83, s56, 8
s_add_u32 s81, s81, s83
s_mov_b32 s64, 0
s_cmp_eq_u32 s80, 0
W 0
s_cselect_b32 s44, s22, s24
s_cselect_b32 s45, s23, s25
s_mov_b32 s40, s18
s_and_b32 s41, s19, 0xffff
s_mov_b32 s42, 0x200000
s_mov_b32 s43, 0x20000
s_add_u32 s44, s44, s81
s_addc_u32 s45, s45, 0
s_and_b32 s45, s45, 0xffff
s_mov_b32 s46, 0x400000
s_mov_b32 s47, 0x20000
s_sub_u32 s46, s46, s81
s_load_dwordx2 s[12:13], s[36:37], 0
s_lshl_b32 s65, s64, 12
s_add_u32 m0, s62, 0
s_nop 0
D 16,44,65,0
D 17,44,65,1024
D 18,44,65,2048
D 19,44,65,3072
s_add_u32 m0, s62, 4096
s_nop 0
D 20,44,65,0
D 21,44,65,1024
D 22,44,65,2048
D 23,44,65,3072
s_add_u32 m0, s61, 0
s_nop 0
D 2,40,64,0
D 3,40,64,1024
D 4,40,64,2048
D 5,40,64,3072
s_add_u32 m0, s61, 4096
s_nop 0
D 6,40,64,0
D 7,40,64,1024
D 8,40,64,2048
D 9,40,64,3072
s_mov_b32 s52, s16
s_and_b32 s53, s17, 0xffff
s_mov_b32 s54, 2621440
s_mov_b32 s55, 0x20000
buffer_load_dwordx4 v[32:35], v28, s[52:55], s84 offen
buffer_load_dwordx4 v[36:39], v28, s[52:55], s84 offen offset:512
buffer_load_dwordx4 v[40:43], v29, s[52:55], s84 offen offset:1024
s_mov_b32 s52, s28
s_and_b32 s53, s29, 0xffff
s_mov_b32 s54, 1280
s_mov_b32 s55, 0x20000
buffer_load_dwordx4 v[44:47], v88, s[52:55], 0 offen offset:0
buffer_load_dwordx4 v[48:51], v88, s[52:55], 0 offen offset:512
buffer_load_dwordx4 v[52:55], v88, s[52:55], 0 offen offset:1024
s_mov_b32 s69, 0
s_cmp_eq_u32 s14, 2
s_cbranch_scc1 .LL_1%=
s_cmp_eq_u32 s14, 3
s_cbranch_scc1 .LL_2%=
s_lshl_b32 s80, s58, 3
s_add_u32 s80, s80, s57
s_lshl_b32 s80, s80, 15
s_lshl_b32 s82, s14, 7
s_add_u32 s80, s80, s82
s_add_u32 s80, s80, 256
v_lshl_add_u32 v104, v88, 6, s80
v_and_b32 v105, 512, v1
v_mul_u32_u24 v105, 3584, v105
v_xor_b32 v104, v104, v105
v_mov_b32 v140, s18
v_mov_b32 v141, s19
s_mov_b32 s68, 256
s_branch .LL_3%=
.LL_1%=:
v_lshrrev_b32 v104, 6, v1
v_lshlrev_b32 v104, 13, v104
v_and_b32 v105, 16, v1
v_lshl_add_u32 v104, v105, 3, v104
s_lshl_b32 s80, s57, 17
s_add_u32 s80, s80, s83
s_add_u32 s80, s80, 1048576
v_add_u32 v104, s80, v104
v_and_b32 v105, 32, v1
v_cmp_eq_u32 vcc, 0, v105
v_mov_b32 v140, s24
v_mov_b32 v141, s25
v_mov_b32 v105, s22
v_mov_b32 v106, s23
v_cndmask_b32 v140, v140, v105, vcc
v_cndmask_b32 v141, v141, v106, vcc
s_mov_b32 s68, 1048576
s_branch .LL_3%=
.LL_2%=:
v_lshrrev_b32 v104, 5, v1
v_lshlrev_b32 v104, 13, v104
v_and_b32 v105, 16, v1
v_lshl_add_u32 v104, v105, 3, v104
s_mul_i32 s80, s57, 655360
s_add_u32 s80, s80, s83
v_add_u32 v104, s80, v104
v_mov_b32 v140, s20
v_mov_b32 v141, s21
s_mov_b32 s68, 196608
v_mov_b32 v104, 0
s_mov_b32 s68, 0
.LL_3%=:
v_mov_b32 v105, 0
v_lshl_add_u64 v[140:141], v[104:105], 0, v[140:141]
s_cmp_eq_u32 s14, 3
s_cbranch_scc0 .LL_4%=
s_lshr_b32 s88, s15, 3
s_cmp_lt_u32 s88, 14
s_cbranch_scc0 .LL_4%=
v_lshlrev_b32 v104, 16, v1
s_mov_b64 s[86:87], s[36:37]
s_mov_b32 s89, 38797312
s_cmp_eq_u32 s88, 1
s_cselect_b32 s90, 39845888, 0
s_add_u32 s86, s86, s90
s_addc_u32 s87, s87, 0
s_cmp_eq_u32 s88, 2
s_cselect_b64 s[86:87], s[26:27], s[86:87]
s_cselect_b32 s89, 5242876, s89
s_cmp_eq_u32 s88, 3
s_cselect_b64 s[86:87], s[30:31], s[86:87]
s_cselect_b32 s89, 2211836, s89
s_cmp_eq_u32 s88, 4
s_cselect_b64 s[86:87], s[32:33], s[86:87]
s_cselect_b32 s89, 2211836, s89
s_cmp_eq_u32 s88, 5
s_cselect_b64 s[86:87], s[34:35], s[86:87]
s_cselect_b32 s89, 2211836, s89
s_cmp_eq_u32 s88, 6
s_cselect_b64 s[86:87], s[38:39], s[86:87]
s_cselect_b32 s89, 2621436, s89
s_cmp_eq_u32 s88, 7
s_cselect_b64 s[86:87], s[20:21], s[86:87]
s_cselect_b32 s89, 5242876, s89
s_cmp_eq_u32 s88, 8
s_cselect_b64 s[86:87], s[18:19], s[86:87]
s_cselect_b32 s89, 2097148, s89
s_cmp_eq_u32 s88, 9
s_cselect_b64 s[86:87], s[22:23], s[86:87]
s_cselect_b32 s89, 4194300, s89
s_cmp_eq_u32 s88, 10
s_cselect_b64 s[86:87], s[24:25], s[86:87]
s_cselect_b32 s89, 4194300, s89
s_cmp_eq_u32 s88, 11
s_cselect_b64 s[86:87], s[16:17], s[86:87]
s_cselect_b32 s89, 2621436, s89
s_cmp_eq_u32 s88, 12
s_cselect_b64 s[86:87], s[28:29], s[86:87]
s_cselect_b32 s89, 1276, s89
s_cmp_eq_u32 s88, 13
s_cselect_b64 s[86:87], s[6:7], s[86:87]
s_cselect_b32 s89, 0, s89
v_min_u32 v104, s89, v104
global_load_dword v142, v104, s[86:87] sc1
.LL_4%=:
v_lshrrev_b32 v104, 1, v102
v_xor_b32 v104, v104, v101
v_lshlrev_b32 v104, 4, v104
v_lshl_add_u32 v104, v102, 7, v104
s_lshr_b32 s81, s14, 1
s_lshl_b32 s81, s81, 14
s_add_u32 s81, s81, 65536
v_add_u32 v26, s81, v104
v_xor_b32 v27, 64, v26
v_and_b32 v105, 1, v101
v_lshrrev_b32 v104, 2, v102
v_lshl_or_b32 v105, v105, 2, v104
v_lshl_add_u32 v104, v101, 3, v104
v_lshlrev_b32 v104, 8, v104
v_lshl_add_u32 v104, v105, 5, v104
v_and_b32 v105, 3, v102
v_lshl_add_u32 v10, v105, 3, v104
v_xor_b32 v11, 32, v10
v_xor_b32 v12, 64, v10
v_xor_b32 v13, 96, v10
v_xor_b32 v14, 128, v10
v_xor_b32 v15, 160, v10
v_xor_b32 v24, 192, v10
v_xor_b32 v25, 224, v10
s_mov_b32 s8, s36
s_and_b32 s9, s37, 0xffff
s_mov_b32 s10, 79691776
s_mov_b32 s11, 0x20000
s_add_u32 s48, s20, s83
s_addc_u32 s49, s21, 0
s_and_b32 s49, s49, 0xffff
s_mov_b32 s50, 5242880
s_mov_b32 s51, 0x20000
s_sub_u32 s50, s50, s83
s_lshl_b32 s80, s57, 1
s_and_b32 s81, s14, 1
s_add_u32 s80, s80, s81
s_sub_u32 s81, 31, s80
s_cmp_lt_u32 s14, 2
s_cselect_b32 s60, s80, s81
s_mul_i32 s28, s60, 81920
s_add_u32 s28, s28, 131072
s_add_u32 s4, s28, 10240
s_mov_b32 s80, 0
s_lshl_b32 s81, s80, 9
s_add_u32 s28, s28, s81
s_add_u32 s5, s80, s14
s_cmp_ge_u32 s5, 20
s_cselect_b32 s81, 20, 0
s_sub_u32 s5, s5, s81
s_and_b32 s80, s14, 1
s_mul_i32 s29, s80, 17172480
s_mul_i32 s80, s56, 536576
s_add_u32 s29, s29, s80
s_lshl_b32 s80, s57, 13
s_add_u32 s29, s29, s80
s_add_u32 s29, s29, 4194304
W 0
s_add_u32 s12, s12, 0x7f4a7c15
s_addc_u32 s13, s13, 0x9e3779b9
s_mov_b64 exec, 0
s_cmp_eq_u32 s14, 0
s_cbranch_scc1 .Lh_0_0%=
s_cmp_eq_u32 s14, 1
s_cbranch_scc1 .Lh_0_1%=
s_cmp_eq_u32 s14, 2
s_cbranch_scc1 .Lh_0_2%=
s_branch .Lh_0_3%=
.Lhr_5%=:
s_mov_b64 exec, -1
s_add_u32 s64, s64, 128
s_and_b32 s64, s64, 1023
s_lshl_b32 s65, s64, 12
s_add_u32 m0, s62, 32768
s_nop 0
D 16,44,65,0
D 17,44,65,1024
D 18,44,65,2048
D 19,44,65,3072
s_add_u32 m0, s62, 36864
s_nop 0
D 20,44,65,0
D 21,44,65,1024
D 22,44,65,2048
D 23,44,65,3072
s_bitcmp1_b32 s14, 0
s_cbranch_scc1 .Lv_6%=
s_branch .LL_8%=
.p2align 12
.LL_8%=:
s_waitcnt vmcnt(14)
s_barrier
Ta 0,10,0
Ta 2,10,1024
Ra 32,26,0
Ta 4,10,8192
.Lh_0_0%=:
s_cbranch_execz .Lh_0_0_1%=
Ta 6,10,9216
Ra 128,27,0
Ra 132,27,2048
Ra 36,26,2048
Ra 40,26,4096
Ra 136,27,4096
Ra 140,27,6144
Ra 44,26,6144
Ra 48,26,8192
Ra 144,27,8192
W 6
Zvaa 160,0,32
s_add_u32 m0, s61, 32768
Ra 148,27,10240
Ra 52,26,10240
Ra 56,26,12288
Ra 152,27,12288
Ra 156,27,14336
Ra 60,26,14336
Mvaa 160,4,128
Ta 8,11,0
Ta 10,11,1024
Ta 12,11,8192
Ta 14,11,9216
Zvaa 192,4,132
Ta 16,12,0
Ta 18,12,1024
Mvaa 192,0,36
W 14
Ta 20,12,8192
Ta 22,12,9216
Zvaa 224,0,40
D 2,40,64,0
Mvaa 224,4,136
Zaaa 160,4,140
Maaa 160,0,44
W 12
Zaaa 192,0,48
D 3,40,64,1024
Maaa 192,4,144
Zaaa 224,4,148
Maaa 224,0,52
W 8
Zaaa 64,0,56
D 4,40,64,2048
Maaa 64,4,152
Zaaa 96,4,156
)"
R"(Maaa 96,0,60
W 4
Zaaa 100,8,60
D 5,40,64,3072
Maaa 100,12,156
s_add_u32 m0, s61, 36864
Zaaa 68,12,152
Ta 24,13,0
Ta 26,13,1024
Maaa 68,8,56
Ta 28,13,8192
Ta 30,13,9216
Zaaa 228,8,52
s_waitcnt vmcnt(15)
D 6,40,64,0
Maaa 228,12,148
v_lshlrev_b32 v56, 16, v32
v_and_b32 v57, 0xffff0000, v32
v_lshlrev_b32 v58, 16, v33
v_and_b32 v59, 0xffff0000, v33
v_lshlrev_b32 v60, 16, v34
Zaaa 196,12,144
v_and_b32 v61, 0xffff0000, v34
v_lshlrev_b32 v62, 16, v35
v_and_b32 v63, 0xffff0000, v35
v_lshlrev_b32 v64, 16, v36
v_and_b32 v65, 0xffff0000, v36
Maaa 196,8,48
v_lshlrev_b32 v66, 16, v37
v_and_b32 v67, 0xffff0000, v37
v_lshlrev_b32 v68, 16, v38
v_and_b32 v69, 0xffff0000, v38
v_lshlrev_b32 v70, 16, v39
Zaaa 164,8,44
D 7,40,64,1024
v_and_b32 v71, 0xffff0000, v39
v_lshlrev_b32 v72, 16, v40
v_and_b32 v73, 0xffff0000, v40
v_lshlrev_b32 v74, 16, v41
v_and_b32 v75, 0xffff0000, v41
Maaa 164,12,140
v_lshlrev_b32 v76, 16, v42
v_and_b32 v77, 0xffff0000, v42
v_lshlrev_b32 v78, 16, v43
v_and_b32 v79, 0xffff0000, v43
A 80,56,57
Zvaa 228,12,136
A 81,58,59
A 80,80,60
A 81,81,61
A 80,80,62
A 81,81,63
Mvaa 228,8,40
A 80,80,64
A 81,81,65
A 80,80,66
A 81,81,67
A 80,80,68
Zvaa 196,8,36
D 8,40,64,2048
A 81,81,69
A 80,80,70
A 81,81,71
A 80,80,72
A 81,81,73
Mvaa 196,12,132
A 80,80,74
A 81,81,75
A 80,80,76
A 81,81,77
A 80,80,78
A 81,81,79
Zvaa 164,12,128
A 80,80,81
s_nop 1
v_add_f32_dpp v80, v80, v80 quad_perm:[1,0,3,2] row_mask:0xf bank_mask:0xf
s_nop 1
v_add_f32_dpp v80, v80, v80 quad_perm:[2,3,0,1] row_mask:0xf bank_mask:0xf
s_nop 1
v_add_f32_dpp v80, v80, v80 row_half_mirror row_mask:0xf bank_mask:0xf
s_nop 1
v_add_f32_dpp v80, v80, v80 row_mirror row_mask:0xf bank_mask:0xf
Mvaa 164,8,32
W 4
v_mov_b32 v81, v80
s_nop 1
v_permlane16_swap_b32 v80, v81
A 80,80,81
v_mul_f32 v84, 0x3acccccd, v80
s_mov_b32 s86, 0xffff
Zvaa 168,16,32
s_mov_b32 s87, 0xffff
D 9,40,64,3072
v_sub_f32 v82, v56, v84
U 80,82,82
v_sub_f32 v83, v57, v84
v_fmac_f32 v80, v83, v83
Mvaa 168,20,128
v_sub_f32 v82, v58, v84
v_fmac_f32 v80, v82, v82
v_sub_f32 v83, v59, v84
v_fmac_f32 v80, v83, v83
v_sub_f32 v82, v60, v84
Zvaa 200,20,132
Ta 0,14,0
v_fmac_f32 v80, v82, v82
v_sub_f32 v83, v61, v84
v_fmac_f32 v80, v83, v83
v_sub_f32 v82, v62, v84
v_fmac_f32 v80, v82, v82
Mvaa 200,16,36
Ta 2,14,1024
v_sub_f32 v83, v63, v84
v_fmac_f32 v80, v83, v83
v_sub_f32 v82, v64, v84
v_fmac_f32 v80, v82, v82
v_sub_f32 v83, v65, v84
Zvaa 232,16,40
global_load_dword v142, v[140:141], off
v_lshl_add_u64 v[140:141], v[140:141], 0, s[68:69]
Ta 4,14,8192
Ta 6,14,9216
v_fmac_f32 v80, v83, v83
v_sub_f32 v82, v66, v84
v_fmac_f32 v80, v82, v82
Mvaa 232,20,136
v_sub_f32 v83, v67, v84
v_fmac_f32 v80, v83, v83
v_sub_f32 v82, v68, v84
v_fmac_f32 v80, v82, v82
v_sub_f32 v83, v69, v84
v_fmac_f32 v80, v83, v83
Zaaa 168,20,140
v_sub_f32 v82, v70, v84
v_fmac_f32 v80, v82, v82
v_sub_f32 v83, v71, v84
v_fmac_f32 v80, v83, v83
v_sub_f32 v82, v72, v84
U 81,82,82
Maaa 168,16,44
v_sub_f32 v83, v73, v84
v_fmac_f32 v81, v83, v83
v_sub_f32 v82, v74, v84
v_fmac_f32 v81, v82, v82
v_sub_f32 v83, v75, v84
v_fmac_f32 v81, v83, v83
Zaaa 200,16,48
v_sub_f32 v82, v76, v84
v_fmac_f32 v81, v82, v82
v_sub_f32 v83, v77, v84
v_fmac_f32 v81, v83, v83
v_sub_f32 v82, v78, v84
Maaa 200,20,144
v_fmac_f32 v81, v82, v82
v_sub_f32 v83, v79, v84
v_fmac_f32 v81, v83, v83
v_cndmask_b32_e64 v81, 0, v81, s[86:87]
A 80,80,81
Zaaa 232,20,148
Maaa 232,16,52
v_add_f32_dpp v80, v80, v80 quad_perm:[1,0,3,2] row_mask:0xf bank_mask:0xf
s_nop 1
v_add_f32_dpp v80, v80, v80 quad_perm:[2,3,0,1] row_mask:0xf bank_mask:0xf
s_nop 1
v_add_f32_dpp v80, v80, v80 row_half_mirror row_mask:0xf bank_mask:0xf
s_nop 1
v_add_f32_dpp v80, v80, v80 row_mirror row_mask:0xf bank_mask:0xf
v_mov_b32 v81, v80
s_nop 1
v_permlane16_swap_b32 v80, v81
Zaaa 72,16,56
A 80,80,81
v_mul_f32 v80, 0x3acccccd, v80
K 80,80,80
v_lshlrev_b32 v80, 16, v80
v_add_f32 v80, 0x3727c5ac, v80
K 80,80,80
Maaa 72,20,152
v_lshlrev_b32 v80, 16, v80
v_sqrt_f32 v81, v80
s_nop 0
v_rcp_f32 v82, v81
v_fma_f32 v83, -v81, v81, v80
Zaaa 104,20,156
v_mul_f32 v82, 0.5, v82
v_fma_f32 v81, v83, v82, v81
K 86,81,81
v_lshlrev_b32 v86, 16, v86
v_rcp_f32 v87, v86
Maaa 104,16,60
W 4
K 85,84,84
v_lshlrev_b32 v85, 16, v85
s_waitcnt vmcnt(17)
v_sub_f32 v56, v56, v85
v_sub_f32 v57, v57, v85
K 80,56,57
Zaaa 108,24,60
v_lshlrev_b32 v56, 16, v80
v_and_b32 v57, 0xffff0000, v80
U 80,56,87
U 81,57,87
K 32,80,81
v_lshlrev_b32 v80, 16, v32
Maaa 108,28,156
v_and_b32 v81, 0xffff0000, v32
v_lshlrev_b32 v82, 16, v44
v_and_b32 v83, 0xffff0000, v44
U 84,80,82
v_fmac_f32 v84, v81, v83
Zaaa 76,28,152
Ta 8,15,0
v_sub_f32 v58, v58, v85
v_sub_f32 v59, v59, v85
K 80,58,59
v_lshlrev_b32 v58, 16, v80
v_and_b32 v59, 0xffff0000, v80
Maaa 76,24,56
Ta 10,15,1024
U 80,58,87
U 81,59,87
K 33,80,81
v_lshlrev_b32 v80, 16, v33
v_and_b32 v81, 0xffff0000, v33
Zaaa 236,24,52
Ta 12,15,8192
Ta 14,15,9216
v_lshlrev_b32 v82, 16, v45
v_and_b32 v83, 0xffff0000, v45
v_fmac_f32 v84, v80, v82
v_fmac_f32 v84, v81, v83
Maaa 236,28,148
v_sub_f32 v60, v60, v85
v_sub_f32 v61, v61, v85
K 80,60,61
v_lshlrev_b32 v60, 16, v80
v_and_b32 v61, 0xffff0000, v80
U 80,60,87
Zaaa 204,28,144
U 81,61,87
K 34,80,81
v_lshlrev_b32 v80, 16, v34
v_and_b32 v81, 0xffff0000, v34
v_lshlrev_b32 v82, 16, v46
v_and_b32 v83, 0xffff0000, v46
Maaa 204,24,48
v_fmac_f32 v84, v80, v82
v_fmac_f32 v84, v81, v83
v_sub_f32 v62, v62, v85
v_sub_f32 v63, v63, v85
K 80,62,63
Zaaa 172,24,44
v_lshlrev_b32 v62, 16, v80
v_and_b32 v63, 0xffff0000, v80
U 80,62,87
U 81,63,87
K 35,80,81
Maaa 172,28,140
v_lshlrev_b32 v80, 16, v35
v_and_b32 v81, 0xffff0000, v35
v_lshlrev_b32 v82, 16, v47
v_and_b32 v83, 0xffff0000, v47
v_fmac_f32 v84, v80, v82
Zvaa 236,28,136
v_fmac_f32 v84, v81, v83
v_sub_f32 v64, v64, v85
v_sub_f32 v65, v65, v85
K 80,64,65
v_lshlrev_b32 v64, 16, v80
Mvaa 236,24,40
v_and_b32 v65, 0xffff0000, v80
U 80,64,87
U 81,65,87
K 36,80,81
v_lshlrev_b32 v80, 16, v36
Zvaa 204,24,36
v_and_b32 v81, 0xffff0000, v36
v_lshlrev_b32 v82, 16, v48
v_and_b32 v83, 0xffff0000, v48
v_fmac_f32 v84, v80, v82
v_fmac_f32 v84, v81, v83
Mvaa 204,28,132
v_sub_f32 v66, v66, v85
v_sub_f32 v67, v67, v85
K 80,66,67
v_lshlrev_b32 v66, 16, v80
v_and_b32 v67, 0xffff0000, v80
U 80,66,87
Zvaa 172,28,128
U 81,67,87
K 37,80,81
v_lshlrev_b32 v80, 16, v37
v_and_b32 v81, 0xffff0000, v37
v_lshlrev_b32 v82, 16, v49
Mvaa 172,24,32
W 4
v_and_b32 v83, 0xffff0000, v49
v_fmac_f32 v84, v80, v82
v_fmac_f32 v84, v81, v83
v_sub_f32 v68, v68, v85
v_sub_f32 v69, v69, v85
Zvaa 176,0,32
K 80,68,69
v_lshlrev_b32 v68, 16, v80
v_and_b32 v69, 0xffff0000, v80
U 80,68,87
U 81,69,87
Mvaa 176,4,128
K 38,80,81
v_lshlrev_b32 v80, 16, v38
v_and_b32 v81, 0xffff0000, v38
v_lshlrev_b32 v82, 16, v50
v_and_b32 v83, 0xffff0000, v50
Zvaa 208,4,132
Ta 16,24,0
v_fmac_f32 v84, v80, v82
v_fmac_f32 v84, v81, v83
v_sub_f32 v70, v70, v85
v_sub_f32 v71, v71, v85
K 80,70,71
Mvaa 208,0,36
Ta 18,24,1024
v_lshlrev_b32 v70, 16, v80
v_and_b32 v71, 0xffff0000, v80
U 80,70,87
U 81,71,87
K 39,80,81
Zvaa 240,0,40
Ta 20,24,8192
Ta 22,24,9216
v_lshlrev_b32 v80, 16, v39
v_and_b32 v81, 0xffff0000, v39
v_lshlrev_b32 v82, 16, v51
v_and_b32 v83, 0xffff0000, v51
Mvaa 240,4,136
v_fmac_f32 v84, v80, v82
v_fmac_f32 v84, v81, v83
v_sub_f32 v72, v72, v85
v_sub_f32 v73, v73, v85
K 80,72,73
v_lshlrev_b32 v72, 16, v80
Zaaa 176,4,140
v_and_b32 v73, 0xffff0000, v80
U 80,72,87
U 81,73,87
K 40,80,81
v_lshlrev_b32 v80, 16, v40
v_and_b32 v81, 0xffff0000, v40
Maaa 176,0,44
v_lshlrev_b32 v82, 16, v52
v_and_b32 v83, 0xffff0000, v52
v_fmac_f32 v84, v80, v82
v_fmac_f32 v84, v81, v83
v_sub_f32 v74, v74, v85
Zaaa 208,0,48
v_sub_f32 v75, v75, v85
K 80,74,75
v_lshlrev_b32 v74, 16, v80
v_and_b32 v75, 0xffff0000, v80
U 80,74,87
Maaa 208,4,144
U 81,75,87
K 41,80,81
v_lshlrev_b32 v80, 16, v41
v_and_b32 v81, 0xffff0000, v41
v_lshlrev_b32 v82, 16, v53
Zaaa 240,4,148
v_and_b32 v83, 0xffff0000, v53
v_fmac_f32 v84, v80, v82
v_fmac_f32 v84, v81, v83
v_sub_f32 v76, v76, v85
v_sub_f32 v77, v77, v85
Maaa 240,0,52
K 80,76,77
v_lshlrev_b32 v76, 16, v80
v_and_b32 v77, 0xffff0000, v80
U 80,76,87
U 81,77,87
Zaaa 80,0,56
K 42,80,81
v_lshlrev_b32 v80, 16, v42
v_and_b32 v81, 0xffff0000, v42
v_lshlrev_b32 v82, 16, v54
v_and_b32 v83, 0xffff0000, v54
Maaa 80,4,152
v_fmac_f32 v84, v80, v82
v_fmac_f32 v84, v81, v83
v_sub_f32 v78, v78, v85
v_sub_f32 v79, v79, v85
K 80,78,79
v_lshlrev_b32 v78, 16, v80
Zaaa 112,4,156
v_and_b32 v79, 0xffff0000, v80
U 80,78,87
U 81,79,87
K 43,80,81
v_lshlrev_b32 v80, 16, v43
Maaa 112,0,60
W 4
v_and_b32 v81, 0xffff0000, v43
)"
R"(v_lshlrev_b32 v82, 16, v55
v_and_b32 v83, 0xffff0000, v55
v_fmac_f32 v84, v80, v82
v_fmac_f32 v84, v81, v83
Zaaa 116,8,60
Ta 24,25,0
Ta 26,25,1024
v_lshrrev_b32 v82, 9, v1
s_lshl_b32 s80, s14, 1
v_add_u32 v82, s80, v82
v_mul_u32_u24 v83, 1552, v82
v_add_u32 v83, v83, v88
Maaa 116,12,156
v_add_u32 v83, 131072, v83
ds_write_b128 v83, v[32:35] offset:0
ds_write_b128 v83, v[36:39] offset:512
ds_write_b128 v83, v[40:43] offset:1024
v_add_f32_dpp v84, v84, v84 quad_perm:[1,0,3,2] row_mask:0xf bank_mask:0xf
Zaaa 84,12,152
Ta 28,25,8192
Ta 30,25,9216
Maaa 84,8,56
v_add_f32_dpp v84, v84, v84 quad_perm:[2,3,0,1] row_mask:0xf bank_mask:0xf
s_nop 1
v_add_f32_dpp v84, v84, v84 row_half_mirror row_mask:0xf bank_mask:0xf
s_nop 1
v_add_f32_dpp v84, v84, v84 row_mirror row_mask:0xf bank_mask:0xf
v_mov_b32 v81, v84
s_nop 1
v_permlane16_swap_b32 v84, v81
A 84,84,81
Zaaa 244,8,52
K 80,84,84
v_lshlrev_b32 v80, 16, v80
s_lshl_b32 s80, s15, 3
v_add_u32 v82, s80, v82
v_sub_u32 v83, 2047, v82
v_ashrrev_i32 v81, 31, v80
v_or_b32 v81, 0x80000000, v81
Maaa 244,12,148
v_xor_b32 v80, v80, v81
v_and_b32 v80, 0xffff0000, v80
v_or_b32 v80, v80, v83
v_and_b32 v82, 7, v82
v_lshlrev_b32 v82, 2, v82
v_add_u32 v82, 143488, v82
Zaaa 212,12,144
ds_write_b32 v82, v80
Maaa 212,8,48
Zaaa 180,8,44
Maaa 180,12,140
Zvaa 244,12,136
s_waitcnt vmcnt(1) lgkmcnt(0)
s_barrier
Mvaa 244,8,40
s_add_u32 s64, s64, 128
v_bfe_u32 v80, v1, 4, 3
Zvaa 212,8,36
v_mul_u32_u24 v81, 1552, v80
Mvaa 212,12,132
s_and_b32 s64, s64, 1023
v_lshrrev_b32 v82, 7, v1
Zvaa 180,12,128
v_lshl_add_u32 v81, v82, 4, v81
Mvaa 180,8,32
s_lshl_b32 s80, s14, 7
Zvaa 184,16,32
s_lshl_b32 s65, s64, 12
s_add_u32 s80, s80, 131072
Mvaa 184,20,128
v_add_u32 v81, s80, v81
Zvaa 216,20,132
ds_read_b128 v[32:35], v81
Ta 0,10,32768
Mvaa 216,16,36
s_add_u32 m0, s62, 0
Ta 2,10,33792
Rv 36,81,512
Zvaa 248,16,40
s_and_b32 s80, s14, 2
Ta 4,10,40960
Ta 6,10,41984
Mvaa 248,20,136
s_lshl_b32 s80, s80, 7
Zaaa 184,20,140
D 16,44,65,0
v_subrev_u32 v81, s80, v81
Maaa 184,16,44
Rv 40,81,1024
Zaaa 216,16,48
v_lshlrev_b32 v80, 2, v80
Maaa 216,20,144
v_add_u32 v80, 143488, v80
Zaaa 248,20,148
D 17,44,65,1024
ds_read_b32 v84, v80
Maaa 248,16,52
v_mov_b32 v85, s13
Zaaa 88,16,56
v_lshrrev_b32 v83, 1, v1
Maaa 88,20,152
s_mul_i32 s82, s15, 10240
Zaaa 120,20,156
s_add_u32 s82, s82, 131072
D 18,44,65,2048
Maaa 120,16,60
s_lshl_b32 s80, s14, 10
Zaaa 124,24,60
s_add_u32 s83, s82, s80
Ra 60,26,47104
Maaa 124,28,156
Ra 156,27,47104
Zaaa 92,28,152
D 19,44,65,3072
Ra 152,27,45056
Maaa 92,24,56
s_add_u32 m0, s62, 4096
Ta 8,11,32768
Ta 10,11,33792
Zaaa 252,24,52
Ra 56,26,45056
Ta 12,11,40960
Maaa 252,28,148
Ta 14,11,41984
Ra 52,26,43008
Zaaa 220,28,144
W 9
buffer_store_dwordx4 v[32:35], v1, s[8:11], s83 offen sc1
s_add_u32 s83, s83, 4096
Maaa 220,24,48
Ra 148,27,43008
Ra 144,27,40960
Zaaa 188,24,44
Ra 48,26,40960
Ra 44,26,38912
Maaa 188,28,140
Ra 140,27,38912
Zvaa 252,28,136
D 20,44,65,0
Ra 136,27,36864
Mvaa 252,24,40
Ra 40,26,36864
Zvaa 220,24,36
Ra 36,26,34816
Mvaa 220,28,132
Ra 132,27,34816
Zvaa 188,28,128
buffer_store_dwordx4 v[36:39], v1, s[8:11], s83 offen sc1
Ra 128,27,32768
Mvaa 188,24,32
W 13
s_and_b32 s80, s80, 1024
s_add_u32 s83, s82, s80
Maaa 96,0,60
s_add_u32 s83, s83, 8192
Ra 32,26,32768
Maaa 96,4,156
Maaa 64,4,152
D 21,44,65,1024
Ta 16,12,32768
Maaa 64,0,56
W 9
Ta 18,12,33792
Ta 20,12,40960
Maaa 224,0,52
Ta 22,12,41984
Maaa 224,4,148
Maaa 192,4,144
buffer_store_dwordx4 v[40:43], v1, s[8:11], s83 offen sc1
Maaa 192,0,48
W 8
Maaa 160,0,44
Maaa 160,4,140
Mvaa 224,4,136
D 22,44,65,2048
Mvaa 224,0,40
W 4
Mvaa 192,0,36
Mvaa 192,4,132
Mvaa 160,4,128
D 23,44,65,3072
Mvaa 160,0,32
s_add_u32 m0, s61, 0
Mvaa 164,8,32
Mvaa 164,12,128
Mvaa 196,12,132
D 2,40,64,0
Ta 24,13,32768
Mvaa 196,8,36
Ta 26,13,33792
Ta 28,13,40960
Mvaa 228,8,40
Ta 30,13,41984
Mvaa 228,12,136
.Lh_0_1%=:
s_cbranch_execz .Lh_0_1_1%=
Maaa 164,12,140
D 3,40,64,1024
Maaa 164,8,44
Maaa 196,8,48
Maaa 196,12,144
Maaa 228,12,148
D 4,40,64,2048
Maaa 228,8,52
Maaa 68,8,56
Maaa 68,12,152
Maaa 100,12,156
D 5,40,64,3072
Maaa 100,8,60
W 4
s_add_u32 m0, s61, 4096
Maaa 104,16,60
Maaa 104,20,156
Maaa 72,20,152
D 6,40,64,0
Ta 0,14,32768
Maaa 72,16,56
Ta 2,14,33792
Ta 4,14,40960
Maaa 232,16,52
Ta 6,14,41984
Maaa 232,20,148
Maaa 200,20,144
D 7,40,64,1024
Maaa 200,16,48
Maaa 168,16,44
Maaa 168,20,140
Mvaa 232,20,136
D 8,40,64,2048
Mvaa 232,16,40
Mvaa 200,16,36
Mvaa 200,20,132
Mvaa 168,20,128
D 9,40,64,3072
v_bfe_u32 v147, v1, 5, 3
Mvaa 168,16,32
W 4
v_lshlrev_b32 v147, 13, v147
v_and_b32 v148, 16, v1
Mvaa 172,24,32
v_lshl_add_u32 v147, v148, 3, v147
Mvaa 172,28,128
s_lshl_b32 s92, s57, 2
s_add_u32 s92, s92, s14
s_lshl_b32 s92, s92, 16
Mvaa 204,28,132
buffer_load_dword v142, v147, s[48:51], s92 offen
Ta 8,15,32768
Mvaa 204,24,36
Ta 10,15,33792
Ta 12,15,40960
Mvaa 236,24,40
Ta 14,15,41984
Mvaa 236,28,136
Maaa 172,28,140
Maaa 172,24,44
Maaa 204,24,48
Maaa 204,28,144
Maaa 236,28,148
Maaa 236,24,52
Maaa 76,24,56
Maaa 76,28,152
Maaa 108,28,156
Maaa 108,24,60
W 4
Maaa 112,0,60
Maaa 112,4,156
Maaa 80,4,152
Ta 16,24,32768
Ta 18,24,33792
Maaa 80,0,56
Ta 20,24,40960
Ta 22,24,41984
Maaa 240,0,52
Maaa 240,4,148
Maaa 208,4,144
Maaa 208,0,48
Maaa 176,0,44
Maaa 176,4,140
Mvaa 240,4,136
Mvaa 240,0,40
Mvaa 208,0,36
Mvaa 208,4,132
Mvaa 176,4,128
Mvaa 176,0,32
W 4
Mvaa 180,8,32
Ta 24,25,32768
Ta 26,25,33792
Mvaa 180,12,128
Mvaa 212,12,132
Ta 28,25,40960
Ta 30,25,41984
Mvaa 212,8,36
Mvaa 244,8,40
Mvaa 244,12,136
Maaa 180,12,140
Maaa 180,8,44
Maaa 212,8,48
Maaa 212,12,144
Maaa 244,12,148
s_waitcnt vmcnt(1) lgkmcnt(0)
s_barrier
s_cmp_eq_u32 s14, 0
s_cbranch_scc0 .Lpubr_10%=
s_lshl_b32 s80, s15, 6
s_add_u32 s80, s80, 40960
s_mov_b64 exec, 0xff
buffer_store_dwordx2 v[84:85], v83, s[8:11], s80 offen sc1
s_mov_b64 exec, -1
.Lpubr_10%=:
Maaa 244,8,52
s_add_u32 s64, s64, 128
Maaa 84,8,56
Maaa 84,12,152
s_and_b32 s64, s64, 1023
Maaa 116,12,156
Maaa 116,8,60
Maaa 120,16,60
s_lshl_b32 s65, s64, 12
Maaa 120,20,156
Maaa 88,20,152
s_add_u32 m0, s62, 32768
Ta 0,10,0
Ta 2,10,1024
Maaa 88,16,56
Ta 4,10,8192
Ta 6,10,9216
Maaa 248,16,52
Maaa 248,20,148
D 16,44,65,0
Maaa 216,20,144
Maaa 216,16,48
Maaa 184,16,44
Maaa 184,20,140
D 17,44,65,1024
Mvaa 248,20,136
Mvaa 248,16,40
Mvaa 216,16,36
Mvaa 216,20,132
D 18,44,65,2048
Mvaa 184,20,128
Mvaa 184,16,32
Mvaa 188,24,32
Ra 32,26,0
Mvaa 188,28,128
D 19,44,65,3072
Ra 128,27,0
Mvaa 220,28,132
s_add_u32 m0, s62, 36864
Ra 132,27,2048
Ta 8,11,0
Mvaa 220,24,36
Ta 10,11,1024
Ra 36,26,2048
Mvaa 252,24,40
Ta 12,11,8192
Ta 14,11,9216
Mvaa 252,28,136
D 20,44,65,0
Ra 40,26,4096
Maaa 188,28,140
Ra 136,27,4096
Ra 140,27,6144
Maaa 188,24,44
Ra 44,26,6144
Maaa 220,24,48
Ra 48,26,8192
Maaa 220,28,144
D 21,44,65,1024
Ra 144,27,8192
Maaa 252,28,148
Ra 148,27,10240
Maaa 252,24,52
Ra 52,26,10240
Maaa 92,24,56
Ra 56,26,12288
Maaa 92,28,152
D 22,44,65,2048
Ra 152,27,12288
Maaa 124,28,156
Ra 156,27,14336
Maaa 124,24,60
W 13
Ra 60,26,14336
Mvaa 160,0,32
Mvaa 160,4,128
D 23,44,65,3072
Mvaa 192,4,132
s_add_u32 m0, s61, 32768
Ta 16,12,0
Ta 18,12,1024
Mvaa 192,0,36
W 10
Ta 20,12,8192
Ta 22,12,9216
Mvaa 224,0,40
Mvaa 224,4,136
D 2,40,64,0
Maaa 160,4,140
)"
R"(Maaa 160,0,44
W 8
Maaa 192,0,48
Maaa 192,4,144
D 3,40,64,1024
Maaa 224,4,148
Maaa 224,0,52
W 4
Maaa 64,0,56
Maaa 64,4,152
D 4,40,64,2048
Maaa 96,4,156
Maaa 96,0,60
Maaa 100,8,60
Maaa 100,12,156
D 5,40,64,3072
Maaa 68,12,152
s_add_u32 m0, s61, 36864
Ta 24,13,0
Ta 26,13,1024
Maaa 68,8,56
Ta 28,13,8192
Ta 30,13,9216
Maaa 228,8,52
Maaa 228,12,148
D 6,40,64,0
Maaa 196,12,144
Maaa 196,8,48
Maaa 164,8,44
Maaa 164,12,140
D 7,40,64,1024
Mvaa 228,12,136
Mvaa 228,8,40
Mvaa 196,8,36
Mvaa 196,12,132
D 8,40,64,2048
Mvaa 164,12,128
Mvaa 164,8,32
W 4
Mvaa 168,16,32
Mvaa 168,20,128
D 9,40,64,3072
Mvaa 200,20,132
Ta 0,14,0
Ta 2,14,1024
Mvaa 200,16,36
Ta 4,14,8192
Ta 6,14,9216
Mvaa 232,16,40
Mvaa 232,20,136
global_load_dword v142, v[140:141], off
v_lshl_add_u64 v[140:141], v[140:141], 0, s[68:69]
Maaa 168,20,140
Maaa 168,16,44
Maaa 200,16,48
Maaa 200,20,144
Maaa 232,20,148
Maaa 232,16,52
Maaa 72,16,56
Maaa 72,20,152
Maaa 104,20,156
Maaa 104,16,60
W 4
Maaa 108,24,60
Maaa 108,28,156
Maaa 76,28,152
Ta 8,15,0
Ta 10,15,1024
Maaa 76,24,56
Ta 12,15,8192
Ta 14,15,9216
Maaa 236,24,52
Maaa 236,28,148
Maaa 204,28,144
Maaa 204,24,48
Maaa 172,24,44
Maaa 172,28,140
Mvaa 236,28,136
Mvaa 236,24,40
Mvaa 204,24,36
Mvaa 204,28,132
Mvaa 172,28,128
Mvaa 172,24,32
W 4
Mvaa 176,0,32
Mvaa 176,4,128
Mvaa 208,4,132
Ta 16,24,0
Ta 18,24,1024
Mvaa 208,0,36
Ta 20,24,8192
Ta 22,24,9216
Mvaa 240,0,40
Mvaa 240,4,136
Maaa 176,4,140
Maaa 176,0,44
Maaa 208,0,48
Maaa 208,4,144
Maaa 240,4,148
Maaa 240,0,52
Maaa 80,0,56
Maaa 80,4,152
Maaa 112,4,156
Maaa 112,0,60
W 4
Maaa 116,8,60
Ta 24,25,0
Ta 26,25,1024
Maaa 116,12,156
Maaa 84,12,152
Ta 28,25,8192
Ta 30,25,9216
Maaa 84,8,56
Maaa 244,8,52
Maaa 244,12,148
Maaa 212,12,144
Maaa 212,8,48
Maaa 180,8,44
Maaa 180,12,140
Mvaa 244,12,136
s_waitcnt vmcnt(1) lgkmcnt(0)
s_barrier
Mvaa 244,8,40
s_add_u32 s64, s64, 128
Mvaa 212,8,36
Mvaa 212,12,132
s_and_b32 s64, s64, 1023
Mvaa 180,12,128
Mvaa 180,8,32
Mvaa 184,16,32
s_lshl_b32 s65, s64, 12
Mvaa 184,20,128
Mvaa 216,20,132
s_add_u32 m0, s62, 0
Ta 0,10,32768
Ta 2,10,33792
Mvaa 216,16,36
Ta 4,10,40960
Ta 6,10,41984
Mvaa 248,16,40
Mvaa 248,20,136
D 16,44,65,0
Maaa 184,20,140
Maaa 184,16,44
Maaa 216,16,48
Maaa 216,20,144
D 17,44,65,1024
Maaa 248,20,148
Maaa 248,16,52
Maaa 88,16,56
Maaa 88,20,152
D 18,44,65,2048
Maaa 120,20,156
Maaa 120,16,60
Maaa 124,24,60
Ra 60,26,47104
Maaa 124,28,156
D 19,44,65,3072
Ra 156,27,47104
Maaa 92,28,152
s_add_u32 m0, s62, 4096
Ra 152,27,45056
Ta 8,11,32768
Maaa 92,24,56
Ta 10,11,33792
Ra 56,26,45056
Maaa 252,24,52
Ta 12,11,40960
Ta 14,11,41984
Maaa 252,28,148
D 20,44,65,0
Ra 52,26,43008
Maaa 220,28,144
Ra 148,27,43008
Ra 144,27,40960
Maaa 220,24,48
Ra 48,26,40960
Maaa 188,24,44
Ra 44,26,38912
Maaa 188,28,140
D 21,44,65,1024
Ra 140,27,38912
Mvaa 252,28,136
Ra 136,27,36864
Mvaa 252,24,40
Ra 40,26,36864
Mvaa 220,24,36
Ra 36,26,34816
Mvaa 220,28,132
D 22,44,65,2048
Ra 132,27,34816
Mvaa 188,28,128
Ra 128,27,32768
Mvaa 188,24,32
W 13
Ra 32,26,32768
Maaa 96,0,60
Maaa 96,4,156
D 23,44,65,3072
Maaa 64,4,152
s_add_u32 m0, s61, 0
Ta 16,12,32768
Ta 18,12,33792
Maaa 64,0,56
W 10
Ta 20,12,40960
Ta 22,12,41984
Maaa 224,0,52
Maaa 224,4,148
D 2,40,64,0
Maaa 192,4,144
Maaa 192,0,48
W 8
Maaa 160,0,44
Maaa 160,4,140
D 3,40,64,1024
Mvaa 224,4,136
Mvaa 224,0,40
W 4
Mvaa 192,0,36
Mvaa 192,4,132
D 4,40,64,2048
Mvaa 160,4,128
Mvaa 160,0,32
Mvaa 164,8,32
Mvaa 164,12,128
D 5,40,64,3072
Mvaa 196,12,132
s_add_u32 m0, s61, 4096
Ta 24,13,32768
Ta 26,13,33792
Mvaa 196,8,36
Ta 28,13,40960
Ta 30,13,41984
Mvaa 228,8,40
Mvaa 228,12,136
D 6,40,64,0
Maaa 164,12,140
Maaa 164,8,44
Maaa 196,8,48
Maaa 196,12,144
D 7,40,64,1024
Maaa 228,12,148
Maaa 228,8,52
Maaa 68,8,56
Maaa 68,12,152
D 8,40,64,2048
Maaa 100,12,156
Maaa 100,8,60
W 4
Maaa 104,16,60
Maaa 104,20,156
D 9,40,64,3072
Maaa 72,20,152
Ta 0,14,32768
Ta 2,14,33792
Maaa 72,16,56
Ta 4,14,40960
Ta 6,14,41984
Maaa 232,16,52
Maaa 232,20,148
global_load_dword v142, v[140:141], off
v_lshl_add_u64 v[140:141], v[140:141], 0, s[68:69]
Maaa 200,20,144
Maaa 200,16,48
Maaa 168,16,44
Maaa 168,20,140
Mvaa 232,20,136
Mvaa 232,16,40
Mvaa 200,16,36
Mvaa 200,20,132
Mvaa 168,20,128
Mvaa 168,16,32
W 4
Mvaa 172,24,32
Mvaa 172,28,128
Mvaa 204,28,132
Ta 8,15,32768
Ta 10,15,33792
Mvaa 204,24,36
Ta 12,15,40960
Ta 14,15,41984
Mvaa 236,24,40
Mvaa 236,28,136
Maaa 172,28,140
Maaa 172,24,44
Maaa 204,24,48
Maaa 204,28,144
Maaa 236,28,148
Maaa 236,24,52
Maaa 76,24,56
Maaa 76,28,152
Maaa 108,28,156
Maaa 108,24,60
W 4
Maaa 112,0,60
Maaa 112,4,156
Maaa 80,4,152
Ta 16,24,32768
Ta 18,24,33792
Maaa 80,0,56
Ta 20,24,40960
Ta 22,24,41984
Maaa 240,0,52
Maaa 240,4,148
Maaa 208,4,144
Maaa 208,0,48
Maaa 176,0,44
Maaa 176,4,140
Mvaa 240,4,136
Mvaa 240,0,40
Mvaa 208,0,36
Mvaa 208,4,132
Mvaa 176,4,128
Mvaa 176,0,32
W 4
Mvaa 180,8,32
Ta 24,25,32768
Ta 26,25,33792
Mvaa 180,12,128
Mvaa 212,12,132
Ta 28,25,40960
Ta 30,25,41984
Mvaa 212,8,36
Mvaa 244,8,40
Mvaa 244,12,136
.Lh_0_2%=:
s_cbranch_execz .Lh_0_2_1%=
Maaa 180,12,140
Maaa 180,8,44
Maaa 212,8,48
Maaa 212,12,144
Maaa 244,12,148
s_waitcnt vmcnt(1) lgkmcnt(0)
s_barrier
Maaa 244,8,52
s_add_u32 s64, s64, 128
Maaa 84,8,56
Maaa 84,12,152
s_and_b32 s64, s64, 1023
Maaa 116,12,156
Maaa 116,8,60
Maaa 120,16,60
s_lshl_b32 s65, s64, 12
Maaa 120,20,156
Maaa 88,20,152
Ta 0,10,0
Ta 2,10,1024
Maaa 88,16,56
s_add_u32 m0, s62, 32768
Ta 4,10,8192
Ta 6,10,9216
Maaa 248,16,52
Maaa 248,20,148
Maaa 216,20,144
D 16,44,65,0
Maaa 216,16,48
Maaa 184,16,44
Maaa 184,20,140
Mvaa 248,20,136
D 17,44,65,1024
Mvaa 248,16,40
Mvaa 216,16,36
Mvaa 216,20,132
Mvaa 184,20,128
D 18,44,65,2048
Mvaa 184,16,32
Mvaa 188,24,32
Ra 32,26,0
Mvaa 188,28,128
Ra 128,27,0
Mvaa 220,28,132
D 19,44,65,3072
Ra 132,27,2048
Mvaa 220,24,36
s_add_u32 m0, s62, 36864
Ta 8,11,0
Ta 10,11,1024
Mvaa 252,24,40
Ra 36,26,2048
Ta 12,11,8192
Mvaa 252,28,136
Ta 14,11,9216
Ra 40,26,4096
Maaa 188,28,140
D 20,44,65,0
Ra 136,27,4096
Maaa 188,24,44
Ra 140,27,6144
Ra 44,26,6144
Maaa 220,24,48
Ra 48,26,8192
Maaa 220,28,144
Ra 144,27,8192
Maaa 252,28,148
D 21,44,65,1024
Ra 148,27,10240
Maaa 252,24,52
Ra 52,26,10240
Maaa 92,24,56
)"
R"(Ra 56,26,12288
Maaa 92,28,152
Ra 152,27,12288
Maaa 124,28,156
D 22,44,65,2048
Ra 156,27,14336
Maaa 124,24,60
W 13
Ra 60,26,14336
Mvaa 160,0,32
Mvaa 160,4,128
Mvaa 192,4,132
D 23,44,65,3072
Ta 16,12,0
Mvaa 192,0,36
W 9
s_add_u32 m0, s61, 32768
Ta 18,12,1024
Mvaa 224,0,40
Ta 20,12,8192
Ta 22,12,9216
Mvaa 224,4,136
Maaa 160,4,140
D 2,40,64,0
Maaa 160,0,44
W 8
Maaa 192,0,48
Maaa 192,4,144
Maaa 224,4,148
D 3,40,64,1024
Maaa 224,0,52
W 4
Maaa 64,0,56
Maaa 64,4,152
Maaa 96,4,156
D 4,40,64,2048
Maaa 96,0,60
Maaa 100,8,60
Maaa 100,12,156
Maaa 68,12,152
D 5,40,64,3072
Ta 24,13,0
Maaa 68,8,56
s_add_u32 m0, s61, 36864
Ta 26,13,1024
Ta 28,13,8192
Maaa 228,8,52
Ta 30,13,9216
Maaa 228,12,148
Maaa 196,12,144
D 6,40,64,0
Maaa 196,8,48
Maaa 164,8,44
Maaa 164,12,140
Mvaa 228,12,136
D 7,40,64,1024
Mvaa 228,8,40
Mvaa 196,8,36
Mvaa 196,12,132
Mvaa 164,12,128
D 8,40,64,2048
Mvaa 164,8,32
W 4
Mvaa 168,16,32
Mvaa 168,20,128
Mvaa 200,20,132
D 9,40,64,3072
Ta 0,14,0
Mvaa 200,16,36
s_add_u32 s92, s92, 2097152
Ta 2,14,1024
Ta 4,14,8192
Mvaa 232,16,40
Ta 6,14,9216
Mvaa 232,20,136
Maaa 168,20,140
buffer_load_dword v142, v147, s[48:51], s92 offen
Maaa 168,16,44
s_add_u32 s92, s92, 2097152
Maaa 200,16,48
Maaa 200,20,144
Maaa 232,20,148
buffer_load_dword v142, v147, s[48:51], s92 offen
Maaa 232,16,52
Maaa 72,16,56
Maaa 72,20,152
Maaa 104,20,156
Maaa 104,16,60
W 4
Maaa 108,24,60
Maaa 108,28,156
Maaa 76,28,152
Ta 8,15,0
Ta 10,15,1024
Maaa 76,24,56
Ta 12,15,8192
Ta 14,15,9216
Maaa 236,24,52
Maaa 236,28,148
Maaa 204,28,144
Maaa 204,24,48
Maaa 172,24,44
Maaa 172,28,140
Mvaa 236,28,136
Mvaa 236,24,40
Mvaa 204,24,36
Mvaa 204,28,132
Mvaa 172,28,128
Mvaa 172,24,32
W 4
Mvaa 176,0,32
Mvaa 176,4,128
Mvaa 208,4,132
Ta 16,24,0
Ta 18,24,1024
Mvaa 208,0,36
Ta 20,24,8192
Ta 22,24,9216
Mvaa 240,0,40
Mvaa 240,4,136
Maaa 176,4,140
Maaa 176,0,44
Maaa 208,0,48
Maaa 208,4,144
Maaa 240,4,148
Maaa 240,0,52
Maaa 80,0,56
Maaa 80,4,152
Maaa 112,4,156
Maaa 112,0,60
W 4
Maaa 116,8,60
Ta 24,25,0
Ta 26,25,1024
Maaa 116,12,156
Maaa 84,12,152
Ta 28,25,8192
Ta 30,25,9216
Maaa 84,8,56
Maaa 244,8,52
Maaa 244,12,148
Maaa 212,12,144
Maaa 212,8,48
Maaa 180,8,44
Maaa 180,12,140
Mvaa 244,12,136
s_waitcnt vmcnt(2) lgkmcnt(0)
s_barrier
Mvaa 244,8,40
s_add_u32 s64, s64, 128
Mvaa 212,8,36
Mvaa 212,12,132
s_and_b32 s64, s64, 1023
Mvaa 180,12,128
Mvaa 180,8,32
Mvaa 184,16,32
s_lshl_b32 s65, s64, 12
Mvaa 184,20,128
Mvaa 216,20,132
Ta 0,10,32768
Ta 2,10,33792
Mvaa 216,16,36
s_add_u32 m0, s62, 0
Ta 4,10,40960
Ta 6,10,41984
Mvaa 248,16,40
Mvaa 248,20,136
Maaa 184,20,140
D 16,44,65,0
Maaa 184,16,44
Maaa 216,16,48
Maaa 216,20,144
Maaa 248,20,148
D 17,44,65,1024
Maaa 248,16,52
Maaa 88,16,56
Maaa 88,20,152
Maaa 120,20,156
D 18,44,65,2048
Maaa 120,16,60
Maaa 124,24,60
Ra 60,26,47104
Maaa 124,28,156
Ra 156,27,47104
Maaa 92,28,152
D 19,44,65,3072
Ra 152,27,45056
Maaa 92,24,56
s_add_u32 m0, s62, 4096
Ta 8,11,32768
Ta 10,11,33792
Maaa 252,24,52
Ra 56,26,45056
Ta 12,11,40960
Maaa 252,28,148
Ta 14,11,41984
Ra 52,26,43008
Maaa 220,28,144
D 20,44,65,0
v_lshrrev_b32 v28, 1, v1
Maaa 220,24,48
s_lshl_b32 s80, s60, 9
Ra 148,27,43008
Ra 144,27,40960
Maaa 188,24,44
s_add_u32 s80, s80, 40960
Ra 48,26,40960
Ra 44,26,38912
Maaa 188,28,140
Ra 140,27,38912
Mvaa 252,28,136
D 21,44,65,1024
Ra 136,27,36864
Mvaa 252,24,40
Ra 40,26,36864
Mvaa 220,24,36
Ra 36,26,34816
Mvaa 220,28,132
Ra 132,27,34816
Mvaa 188,28,128
buffer_load_dwordx2 v[28:29], v28, s[8:11], s80 offen sc1
Ra 128,27,32768
Mvaa 188,24,32
W 13
Ra 32,26,32768
Maaa 96,0,60
Maaa 96,4,156
Maaa 64,4,152
D 22,44,65,2048
Ta 16,12,32768
Maaa 64,0,56
W 9
Ta 18,12,33792
Ta 20,12,40960
Maaa 224,0,52
Ta 22,12,41984
Maaa 224,4,148
Maaa 192,4,144
D 23,44,65,3072
Maaa 192,0,48
W 8
s_add_u32 m0, s61, 0
Maaa 160,0,44
Maaa 160,4,140
Mvaa 224,4,136
D 2,40,64,0
Mvaa 224,0,40
W 4
Mvaa 192,0,36
Mvaa 192,4,132
Mvaa 160,4,128
D 3,40,64,1024
Mvaa 160,0,32
Mvaa 164,8,32
Mvaa 164,12,128
Mvaa 196,12,132
D 4,40,64,2048
Ta 24,13,32768
Mvaa 196,8,36
Ta 26,13,33792
Ta 28,13,40960
Mvaa 228,8,40
Ta 30,13,41984
Mvaa 228,12,136
Maaa 164,12,140
D 5,40,64,3072
Maaa 164,8,44
s_add_u32 m0, s61, 4096
Maaa 196,8,48
Maaa 196,12,144
Maaa 228,12,148
D 6,40,64,0
Maaa 228,8,52
Maaa 68,8,56
Maaa 68,12,152
Maaa 100,12,156
D 7,40,64,1024
Maaa 100,8,60
W 4
Maaa 104,16,60
Maaa 104,20,156
Maaa 72,20,152
D 8,40,64,2048
Ta 0,14,32768
Maaa 72,16,56
Ta 2,14,33792
Ta 4,14,40960
Maaa 232,16,52
Ta 6,14,41984
Maaa 232,20,148
Maaa 200,20,144
D 9,40,64,3072
Maaa 200,16,48
Maaa 168,16,44
Maaa 168,20,140
Mvaa 232,20,136
s_waitcnt vmcnt(10)
v_cmp_eq_u32 vcc, s13, v29
s_cmp_eq_u64 vcc, -1
s_cbranch_scc0 .Lspin_11%=
.Lok_12%=:
global_load_dword v142, v146, s[6:7]
Mvaa 232,16,40
Mvaa 200,16,36
v_bfe_u32 v144, v1, 8, 1
Mvaa 200,20,132
v_mul_u32_u24 v144, 10240, v144
Mvaa 168,20,128
v_and_b32 v145, 0xf0, v1
Mvaa 168,16,32
W 4
v_lshl_add_u32 v144, v145, 3, v144
Mvaa 172,24,32
s_mul_i32 s91, s58, 20480
Mvaa 172,28,128
s_add_u32 s91, s91, s28
Mvaa 204,28,132
buffer_load_dword v142, v144, s[8:11], s91 offen offset:0 sc1
Ta 8,15,32768
Mvaa 204,24,36
Ta 10,15,33792
Ta 12,15,40960
Mvaa 236,24,40
Ta 14,15,41984
Mvaa 236,28,136
Maaa 172,28,140
Maaa 172,24,44
Maaa 204,24,48
Maaa 204,28,144
Maaa 236,28,148
Maaa 236,24,52
Maaa 76,24,56
Maaa 76,28,152
Maaa 108,28,156
Maaa 108,24,60
W 4
Maaa 112,0,60
Maaa 112,4,156
Maaa 80,4,152
Ta 16,24,32768
Ta 18,24,33792
Maaa 80,0,56
Ta 20,24,40960
Ta 22,24,41984
Maaa 240,0,52
Maaa 240,4,148
Maaa 208,4,144
Maaa 208,0,48
Maaa 176,0,44
Maaa 176,4,140
Mvaa 240,4,136
Mvaa 240,0,40
Mvaa 208,0,36
Mvaa 208,4,132
Mvaa 176,4,128
Mvaa 176,0,32
W 4
Mvaa 180,8,32
Ta 24,25,32768
Ta 26,25,33792
Mvaa 180,12,128
Mvaa 212,12,132
Ta 28,25,40960
Ta 30,25,41984
Mvaa 212,8,36
Mvaa 244,8,40
Mvaa 244,12,136
Maaa 180,12,140
Maaa 180,8,44
Maaa 212,8,48
Maaa 212,12,144
Maaa 244,12,148
s_waitcnt vmcnt(2) lgkmcnt(0)
s_barrier
Maaa 244,8,52
s_add_u32 s64, s64, 128
Maaa 84,8,56
Maaa 84,12,152
s_and_b32 s64, s64, 1023
Maaa 116,12,156
Maaa 116,8,60
Maaa 120,16,60
s_lshl_b32 s65, s64, 12
Maaa 120,20,156
Maaa 88,20,152
Ta 0,10,0
Ta 2,10,1024
Maaa 88,16,56
s_add_u32 m0, s62, 32768
Ta 4,10,8192
Ta 6,10,9216
Maaa 248,16,52
Maaa 248,20,148
Maaa 216,20,144
D 16,44,65,0
Maaa 216,16,48
Maaa 184,16,44
Maaa 184,20,140
Mvaa 248,20,136
D 17,44,65,1024
)"
R"(Mvaa 248,16,40
Mvaa 216,16,36
Mvaa 216,20,132
Mvaa 184,20,128
D 18,44,65,2048
Mvaa 184,16,32
Mvaa 188,24,32
Ra 32,26,0
Mvaa 188,28,128
Ra 128,27,0
Mvaa 220,28,132
D 19,44,65,3072
Ra 132,27,2048
Mvaa 220,24,36
s_add_u32 m0, s62, 36864
Ta 8,11,0
Ta 10,11,1024
Mvaa 252,24,40
Ra 36,26,2048
Ta 12,11,8192
Mvaa 252,28,136
Ta 14,11,9216
Ra 40,26,4096
Maaa 188,28,140
D 20,44,65,0
Ra 136,27,4096
Maaa 188,24,44
Ra 140,27,6144
Ra 44,26,6144
Maaa 220,24,48
Ra 48,26,8192
Maaa 220,28,144
Ra 144,27,8192
Maaa 252,28,148
D 21,44,65,1024
Ra 148,27,10240
Maaa 252,24,52
Ra 52,26,10240
Maaa 92,24,56
Ra 56,26,12288
Maaa 92,28,152
Ra 152,27,12288
Maaa 124,28,156
D 22,44,65,2048
Ra 156,27,14336
Maaa 124,24,60
W 13
Ra 60,26,14336
Mvaa 160,0,32
Mvaa 160,4,128
Mvaa 192,4,132
D 23,44,65,3072
Ta 16,12,0
Mvaa 192,0,36
W 9
s_add_u32 m0, s61, 32768
Ta 18,12,1024
Mvaa 224,0,40
Ta 20,12,8192
Ta 22,12,9216
Mvaa 224,4,136
.Lh_0_3%=:
s_cbranch_execz .Lh_0_3_1%=
Maaa 160,4,140
D 2,40,64,0
Maaa 160,0,44
W 8
Maaa 192,0,48
Maaa 192,4,144
Maaa 224,4,148
D 3,40,64,1024
Maaa 224,0,52
W 4
Maaa 64,0,56
Maaa 64,4,152
Maaa 96,4,156
D 4,40,64,2048
Maaa 96,0,60
Maaa 100,8,60
Maaa 100,12,156
Maaa 68,12,152
D 5,40,64,3072
Ta 24,13,0
Maaa 68,8,56
s_add_u32 m0, s61, 36864
Ta 26,13,1024
Ta 28,13,8192
Maaa 228,8,52
Ta 30,13,9216
Maaa 228,12,148
Maaa 196,12,144
D 6,40,64,0
Maaa 196,8,48
Maaa 164,8,44
Maaa 164,12,140
Mvaa 228,12,136
D 7,40,64,1024
Mvaa 228,8,40
Mvaa 196,8,36
Mvaa 196,12,132
Mvaa 164,12,128
D 8,40,64,2048
Mvaa 164,8,32
W 4
Mvaa 168,16,32
Mvaa 168,20,128
Mvaa 200,20,132
D 9,40,64,3072
v_add_u32 v2, 131072, v10
Mvaa 200,16,36
Ta 0,14,0
v_add_u32 v3, 131072, v11
Mvaa 232,16,40
Ta 2,14,1024
v_add_u32 v4, 131072, v12
Mvaa 232,20,136
Ta 4,14,8192
Ta 6,14,9216
Maaa 168,20,140
buffer_load_dword v142, v144, s[8:11], s91 offen offset:2048 sc1
v_add_u32 v5, 131072, v13
Maaa 168,16,44
s_add_u32 s91, s91, 4096
v_add_u32 v6, 131072, v14
Maaa 200,16,48
v_add_u32 v7, 131072, v15
Maaa 200,20,144
Maaa 232,20,148
v_add_u32 v8, 131072, v24
Maaa 232,16,52
Maaa 72,16,56
v_add_u32 v9, 131072, v25
Maaa 72,20,152
s_add_u32 s65, s5, 0
s_cmp_ge_u32 s65, 20
s_cselect_b32 s90, 20, 0
s_sub_u32 s65, s65, s90
Maaa 104,20,156
s_lshl_b32 s65, s65, 18
s_add_u32 m0, s63, 131072
Maaa 104,16,60
W 4
Maaa 108,24,60
Maaa 108,28,156
Maaa 76,28,152
Ta 8,15,0
Ta 10,15,1024
Maaa 76,24,56
D 16,48,65,0
Ta 12,15,8192
Maaa 236,24,52
Ta 14,15,9216
Maaa 236,28,148
Maaa 204,28,144
Maaa 204,24,48
D 17,48,65,1024
Maaa 172,24,44
Maaa 172,28,140
Mvaa 236,28,136
Mvaa 236,24,40
Mvaa 204,24,36
D 18,48,65,2048
Mvaa 204,28,132
Mvaa 172,28,128
Mvaa 172,24,32
W 4
Mvaa 176,0,32
Mvaa 176,4,128
D 19,48,65,3072
Mvaa 208,4,132
s_add_u32 m0, s63, 135168
Ta 16,24,0
Ta 18,24,1024
Mvaa 208,0,36
Ta 20,24,8192
Ta 22,24,9216
Mvaa 240,0,40
Mvaa 240,4,136
Maaa 176,4,140
D 20,48,65,0
Maaa 176,0,44
Maaa 208,0,48
Maaa 208,4,144
Maaa 240,4,148
Maaa 240,0,52
D 21,48,65,1024
Maaa 80,0,56
Maaa 80,4,152
Maaa 112,4,156
Maaa 112,0,60
W 4
Maaa 116,8,60
D 22,48,65,2048
Ta 24,25,0
Maaa 116,12,156
Ta 26,25,1024
Maaa 84,12,152
Ta 28,25,8192
Ta 30,25,9216
Maaa 84,8,56
Maaa 244,8,52
D 23,48,65,3072
Maaa 244,12,148
Maaa 212,12,144
Maaa 212,8,48
Maaa 180,8,44
Maaa 180,12,140
Mvaa 244,12,136
s_waitcnt vmcnt(9) lgkmcnt(0)
s_barrier
Mvaa 244,8,40
s_add_u32 s65, s5, 4
Mvaa 212,8,36
Mvaa 212,12,132
s_cmp_ge_u32 s65, 20
s_cselect_b32 s90, 20, 0
Mvaa 180,12,128
Mvaa 180,8,32
s_sub_u32 s65, s65, s90
Mvaa 184,16,32
Mvaa 184,20,128
s_lshl_b32 s65, s65, 18
Mvaa 216,20,132
Ta 0,10,32768
Ta 2,10,33792
Mvaa 216,16,36
Mvaa 248,16,40
s_add_u32 m0, s63, 0
Mvaa 248,20,136
Maaa 184,20,140
D 16,48,65,0
Maaa 184,16,44
Maaa 216,16,48
Maaa 216,20,144
Maaa 248,20,148
D 17,48,65,1024
Ta 4,11,32768
Maaa 248,16,52
Ta 6,11,33792
Maaa 88,16,56
Maaa 88,20,152
Maaa 120,20,156
D 18,48,65,2048
Maaa 120,16,60
Maaa 124,24,60
Ra 60,26,47104
Maaa 124,28,156
Maaa 92,28,152
D 19,48,65,3072
Ta 8,12,32768
Maaa 92,24,56
s_add_u32 m0, s63, 4096
Ta 10,12,33792
Ra 56,26,45056
Maaa 252,24,52
Ra 52,26,43008
Maaa 252,28,148
Maaa 220,28,144
D 20,48,65,0
Maaa 220,24,48
Ra 48,26,40960
Maaa 188,24,44
Ra 44,26,38912
Maaa 188,28,140
Mvaa 252,28,136
D 21,48,65,1024
Ta 12,13,32768
Mvaa 252,24,40
Ta 14,13,33792
Ra 40,26,36864
Mvaa 220,24,36
Ra 36,26,34816
Mvaa 220,28,132
Mvaa 188,28,128
D 22,48,65,2048
Mvaa 188,24,32
Ra 32,26,32768
W 6
Maaa 96,0,60
Maaa 64,0,56
Maaa 224,0,52
D 23,48,65,3072
Ta 16,14,32768
Maaa 192,0,48
W 1
s_add_u32 s65, s5, 8
s_cmp_ge_u32 s65, 20
s_cselect_b32 s90, 20, 0
Maaa 160,0,44
s_sub_u32 s65, s65, s90
s_lshl_b32 s65, s65, 18
Ta 18,14,33792
Mvaa 224,0,40
s_add_u32 m0, s63, 65536
Mvaa 192,0,36
D 16,48,65,0
Mvaa 160,0,32
Mvaa 164,4,32
Mvaa 196,4,36
Mvaa 228,4,40
D 17,48,65,1024
Ta 20,15,32768
Maaa 164,4,44
Ta 22,15,33792
Maaa 196,4,48
Maaa 228,4,52
Maaa 68,4,56
D 18,48,65,2048
Maaa 100,4,60
Maaa 104,8,60
Maaa 72,8,56
Maaa 232,8,52
D 19,48,65,3072
Ta 24,24,32768
Maaa 200,8,48
s_add_u32 m0, s63, 69632
Ta 26,24,33792
Maaa 168,8,44
Mvaa 232,8,40
Mvaa 200,8,36
D 20,48,65,0
Mvaa 168,8,32
Mvaa 172,12,32
Mvaa 204,12,36
Mvaa 236,12,40
D 21,48,65,1024
Ta 28,25,32768
Maaa 172,12,44
Ta 30,25,33792
Maaa 204,12,48
Ra 156,27,47104
Maaa 236,12,52
Maaa 76,12,56
D 22,48,65,2048
Maaa 108,12,60
W 7
Ra 152,27,45056
Maaa 112,16,60
Maaa 80,16,56
Maaa 240,16,52
D 23,48,65,3072
Ra 148,27,43008
Maaa 208,16,48
Ta 0,10,40960
Ta 2,10,41984
Maaa 176,16,44
Mvaa 240,16,40
Ra 144,27,40960
Mvaa 208,16,36
buffer_load_dword v142, v144, s[8:11], s91 offen offset:0 sc1
Mvaa 176,16,32
W 10
Mvaa 180,20,32
Ra 140,27,38912
Mvaa 212,20,36
Mvaa 244,20,40
Ta 4,11,40960
Ta 6,11,41984
Maaa 180,20,44
Ra 136,27,36864
Maaa 212,20,48
Maaa 244,20,52
v_bfe_u32 v29, v1, 7, 1
Maaa 84,20,56
Ra 132,27,34816
Maaa 116,20,60
W 13
v_mul_u32_u24 v28, 10240, v29
Maaa 120,24,60
Maaa 88,24,56
Ra 128,27,32768
v_lshrrev_b32 v29, 8, v1
Maaa 248,24,52
Ta 8,12,40960
Ta 10,12,41984
Maaa 216,24,48
v_lshl_add_u32 v28, v29, 7, v28
Maaa 184,24,44
v_and_b32 v29, 0x70, v1
Mvaa 248,24,40
Mvaa 216,24,36
v_add_u32 v28, v28, v29
Mvaa 184,24,32
W 14
Mvaa 188,28,32
s_and_b32 s80, s14, 2
Mvaa 220,28,36
s_mul_i32 s80, s80, 20480
Mvaa 252,28,40
Ta 12,13,40960
Ta 14,13,41984
Maaa 188,28,44
s_sub_u32 s81, 40960, s80
Maaa 220,28,48
Maaa 252,28,52
s_add_u32 s93, s28, 20480
Maaa 92,28,56
Maaa 124,28,60
W 10
v_add_u32 v26, s80, v28
)"
R"(Maaa 96,0,156
Maaa 64,0,152
Maaa 224,0,148
Ta 16,14,40960
v_add_u32 v27, s81, v28
Maaa 192,0,144
W 5
Ta 18,14,41984
Maaa 160,0,140
buffer_load_dwordx4 a[32:35], v26, s[8:11], s28 offen offset:0 sc1
Mvaa 224,0,136
Mvaa 192,0,132
Mvaa 160,0,128
Mvaa 164,4,128
buffer_load_dwordx4 a[36:39], v26, s[8:11], s93 offen offset:0 sc1
Mvaa 196,4,132
Mvaa 228,4,136
Ta 20,15,40960
Ta 22,15,41984
Maaa 164,4,140
Maaa 196,4,144
buffer_load_dwordx4 a[40:43], v27, s[8:11], s28 offen offset:0 sc1
Maaa 228,4,148
Maaa 68,4,152
Maaa 100,4,156
W 6
Maaa 104,8,156
buffer_load_dwordx4 a[44:47], v27, s[8:11], s93 offen offset:0 sc1
Ta 24,24,40960
Maaa 72,8,152
s_add_u32 s28, s28, 512
s_cmp_eq_u32 s28, s4
s_cselect_b32 s93, 10240, 0
Ta 26,24,41984
Maaa 232,8,148
s_sub_u32 s28, s28, s93
Ta 28,25,40960
Ta 30,25,41984
Maaa 200,8,144
s_add_u32 s93, s28, 20480
Maaa 168,8,140
Mvaa 232,8,136
Mvaa 200,8,132
s_add_u32 s65, s5, 12
s_cmp_ge_u32 s65, 20
s_cselect_b32 s90, 20, 0
buffer_load_dwordx4 a[48:51], v26, s[8:11], s28 offen offset:0 sc1
Mvaa 168,8,128
W 8
s_sub_u32 s65, s65, s90
s_lshl_b32 s65, s65, 18
Mvaa 172,12,128
s_add_u32 m0, s63, 32768
Mvaa 204,12,132
Mvaa 236,12,136
s_waitcnt vmcnt(6) lgkmcnt(0)
s_barrier
Maaa 172,12,140
buffer_load_dwordx4 a[52:55], v26, s[8:11], s93 offen offset:0 sc1
Maaa 204,12,144
Maaa 236,12,148
Maaa 76,12,152
Maaa 108,12,156
buffer_load_dwordx4 a[56:59], v27, s[8:11], s28 offen offset:0 sc1
Maaa 112,16,156
Maaa 80,16,152
Maaa 240,16,148
Maaa 208,16,144
buffer_load_dwordx4 a[60:63], v27, s[8:11], s93 offen offset:0 sc1
Maaa 176,16,140
Mvaa 240,16,136
Mvaa 208,16,132
Mvaa 176,16,128
D 16,48,65,0
Mvaa 180,20,128
Mvaa 212,20,132
Mvaa 244,20,136
Ta 0,2,0
Ta 2,2,1024
Maaa 180,20,140
D 17,48,65,1024
Maaa 212,20,144
Maaa 244,20,148
Maaa 84,20,152
Ta 4,2,8192
Ta 6,2,9216
Maaa 116,20,156
D 18,48,65,2048
Maaa 120,24,156
Maaa 88,24,152
Maaa 248,24,148
Ta 8,3,0
Ta 10,3,1024
Maaa 216,24,144
D 19,48,65,3072
Maaa 184,24,140
s_add_u32 m0, s63, 36864
Mvaa 248,24,136
Mvaa 216,24,132
Ta 12,3,8192
Ta 14,3,9216
Mvaa 184,24,128
D 20,48,65,0
Mvaa 188,28,128
Mvaa 220,28,132
Mvaa 252,28,136
Ta 16,4,0
Ta 18,4,1024
Maaa 188,28,140
Maaa 220,28,144
D 21,48,65,1024
Maaa 252,28,148
Maaa 92,28,152
Maaa 124,28,156
s_branch .Lq_7%=
.p2align 12
.Lv_6%=:
s_waitcnt vmcnt(14)
s_barrier
Ta 0,10,16384
Ta 2,10,17408
Ra 32,26,0
Ta 4,10,24576
.Lh_0_0_1%=:
s_cbranch_execz .Lhr_5%=
Ta 6,10,25600
Ra 128,27,0
Ra 132,27,2048
Ra 36,26,2048
Ra 40,26,4096
Ra 136,27,4096
Ra 140,27,6144
Ra 44,26,6144
Ra 48,26,8192
Ra 144,27,8192
W 6
Zvaa 160,32,0
s_add_u32 m0, s61, 32768
Ra 148,27,10240
Ra 52,26,10240
Ra 56,26,12288
Ra 152,27,12288
Ra 156,27,14336
Ra 60,26,14336
Mvaa 160,128,4
Ta 8,11,16384
Ta 10,11,17408
Ta 12,11,24576
Ta 14,11,25600
Zvaa 164,132,4
Ta 16,12,16384
Ta 18,12,17408
Mvaa 164,36,0
W 14
Ta 20,12,24576
Ta 22,12,25600
Zvaa 224,40,0
D 2,40,64,0
Mvaa 224,136,4
Zvaa 228,140,4
Mvaa 228,44,0
W 12
Zaaa 192,48,0
D 3,40,64,1024
Maaa 192,144,4
Zaaa 196,148,4
Maaa 196,52,0
W 8
Zaaa 64,56,0
D 4,40,64,2048
Maaa 64,152,4
Zaaa 68,156,4
Maaa 68,60,0
W 4
Zaaa 76,60,8
D 5,40,64,3072
Maaa 76,156,12
s_add_u32 m0, s61, 36864
Zaaa 72,152,12
Ta 24,13,16384
Ta 26,13,17408
Maaa 72,56,8
Ta 28,13,24576
Ta 30,13,25600
Zaaa 204,52,8
s_waitcnt vmcnt(15)
D 6,40,64,0
Maaa 204,148,12
v_lshlrev_b32 v56, 16, v32
v_and_b32 v57, 0xffff0000, v32
v_lshlrev_b32 v58, 16, v33
v_and_b32 v59, 0xffff0000, v33
v_lshlrev_b32 v60, 16, v34
Zaaa 200,144,12
v_and_b32 v61, 0xffff0000, v34
v_lshlrev_b32 v62, 16, v35
v_and_b32 v63, 0xffff0000, v35
v_lshlrev_b32 v64, 16, v36
v_and_b32 v65, 0xffff0000, v36
Maaa 200,48,8
v_lshlrev_b32 v66, 16, v37
v_and_b32 v67, 0xffff0000, v37
v_lshlrev_b32 v68, 16, v38
v_and_b32 v69, 0xffff0000, v38
v_lshlrev_b32 v70, 16, v39
Zvaa 236,44,8
D 7,40,64,1024
v_and_b32 v71, 0xffff0000, v39
v_lshlrev_b32 v72, 16, v40
v_and_b32 v73, 0xffff0000, v40
v_lshlrev_b32 v74, 16, v41
v_and_b32 v75, 0xffff0000, v41
Mvaa 236,140,12
v_lshlrev_b32 v76, 16, v42
v_and_b32 v77, 0xffff0000, v42
v_lshlrev_b32 v78, 16, v43
v_and_b32 v79, 0xffff0000, v43
A 80,56,57
Zvaa 232,136,12
A 81,58,59
A 80,80,60
A 81,81,61
A 80,80,62
A 81,81,63
Mvaa 232,40,8
A 80,80,64
A 81,81,65
A 80,80,66
A 81,81,67
A 80,80,68
Zvaa 172,36,8
D 8,40,64,2048
A 81,81,69
A 80,80,70
A 81,81,71
A 80,80,72
A 81,81,73
Mvaa 172,132,12
A 80,80,74
A 81,81,75
A 80,80,76
A 81,81,77
A 80,80,78
A 81,81,79
Zvaa 168,128,12
A 80,80,81
s_nop 1
v_add_f32_dpp v80, v80, v80 quad_perm:[1,0,3,2] row_mask:0xf bank_mask:0xf
s_nop 1
v_add_f32_dpp v80, v80, v80 quad_perm:[2,3,0,1] row_mask:0xf bank_mask:0xf
s_nop 1
v_add_f32_dpp v80, v80, v80 row_half_mirror row_mask:0xf bank_mask:0xf
s_nop 1
v_add_f32_dpp v80, v80, v80 row_mirror row_mask:0xf bank_mask:0xf
Mvaa 168,32,8
W 4
v_mov_b32 v81, v80
s_nop 1
v_permlane16_swap_b32 v80, v81
A 80,80,81
v_mul_f32 v84, 0x3acccccd, v80
s_mov_b32 s86, 0xffff
Zvaa 176,32,16
s_mov_b32 s87, 0xffff
D 9,40,64,3072
v_sub_f32 v82, v56, v84
U 80,82,82
v_sub_f32 v83, v57, v84
v_fmac_f32 v80, v83, v83
Mvaa 176,128,20
v_sub_f32 v82, v58, v84
v_fmac_f32 v80, v82, v82
v_sub_f32 v83, v59, v84
v_fmac_f32 v80, v83, v83
v_sub_f32 v82, v60, v84
Zvaa 180,132,20
Ta 0,14,16384
v_fmac_f32 v80, v82, v82
v_sub_f32 v83, v61, v84
v_fmac_f32 v80, v83, v83
v_sub_f32 v82, v62, v84
v_fmac_f32 v80, v82, v82
Mvaa 180,36,16
Ta 2,14,17408
v_sub_f32 v83, v63, v84
v_fmac_f32 v80, v83, v83
v_sub_f32 v82, v64, v84
v_fmac_f32 v80, v82, v82
v_sub_f32 v83, v65, v84
Zvaa 240,40,16
global_load_dword v142, v[140:141], off
v_lshl_add_u64 v[140:141], v[140:141], 0, s[68:69]
Ta 4,14,24576
Ta 6,14,25600
v_fmac_f32 v80, v83, v83
v_sub_f32 v82, v66, v84
v_fmac_f32 v80, v82, v82
Mvaa 240,136,20
v_sub_f32 v83, v67, v84
v_fmac_f32 v80, v83, v83
v_sub_f32 v82, v68, v84
v_fmac_f32 v80, v82, v82
v_sub_f32 v83, v69, v84
v_fmac_f32 v80, v83, v83
Zvaa 244,140,20
v_sub_f32 v82, v70, v84
v_fmac_f32 v80, v82, v82
v_sub_f32 v83, v71, v84
v_fmac_f32 v80, v83, v83
v_sub_f32 v82, v72, v84
U 81,82,82
Mvaa 244,44,16
v_sub_f32 v83, v73, v84
v_fmac_f32 v81, v83, v83
v_sub_f32 v82, v74, v84
v_fmac_f32 v81, v82, v82
v_sub_f32 v83, v75, v84
v_fmac_f32 v81, v83, v83
Zaaa 208,48,16
v_sub_f32 v82, v76, v84
v_fmac_f32 v81, v82, v82
v_sub_f32 v83, v77, v84
v_fmac_f32 v81, v83, v83
v_sub_f32 v82, v78, v84
Maaa 208,144,20
v_fmac_f32 v81, v82, v82
v_sub_f32 v83, v79, v84
v_fmac_f32 v81, v83, v83
v_cndmask_b32_e64 v81, 0, v81, s[86:87]
A 80,80,81
Zaaa 212,148,20
Maaa 212,52,16
v_add_f32_dpp v80, v80, v80 quad_perm:[1,0,3,2] row_mask:0xf bank_mask:0xf
s_nop 1
v_add_f32_dpp v80, v80, v80 quad_perm:[2,3,0,1] row_mask:0xf bank_mask:0xf
s_nop 1
v_add_f32_dpp v80, v80, v80 row_half_mirror row_mask:0xf bank_mask:0xf
s_nop 1
v_add_f32_dpp v80, v80, v80 row_mirror row_mask:0xf bank_mask:0xf
v_mov_b32 v81, v80
s_nop 1
v_permlane16_swap_b32 v80, v81
Zaaa 80,56,16
A 80,80,81
v_mul_f32 v80, 0x3acccccd, v80
K 80,80,80
v_lshlrev_b32 v80, 16, v80
v_add_f32 v80, 0x3727c5ac, v80
K 80,80,80
Maaa 80,152,20
v_lshlrev_b32 v80, 16, v80
v_sqrt_f32 v81, v80
s_nop 0
v_rcp_f32 v82, v81
v_fma_f32 v83, -v81, v81, v80
Zaaa 84,156,20
v_mul_f32 v82, 0.5, v82
v_fma_f32 v81, v83, v82, v81
K 86,81,81
v_lshlrev_b32 v86, 16, v86
v_rcp_f32 v87, v86
Maaa 84,60,16
W 4
K 85,84,84
v_lshlrev_b32 v85, 16, v85
s_waitcnt vmcnt(17)
v_sub_f32 v56, v56, v85
v_sub_f32 v57, v57, v85
K 80,56,57
Zaaa 92,60,24
v_lshlrev_b32 v56, 16, v80
v_and_b32 v57, 0xffff0000, v80
U 80,56,87
U 81,57,87
K 32,80,81
v_lshlrev_b32 v80, 16, v32
Maaa 92,156,28
v_and_b32 v81, 0xffff0000, v32
v_lshlrev_b32 v82, 16, v44
v_and_b32 v83, 0xffff0000, v44
U 84,80,82
v_fmac_f32 v84, v81, v83
Zaaa 88,152,28
Ta 8,15,16384
v_sub_f32 v58, v58, v85
v_sub_f32 v59, v59, v85
K 80,58,59
v_lshlrev_b32 v58, 16, v80
v_and_b32 v59, 0xffff0000, v80
Maaa 88,56,24
Ta 10,15,17408
U 80,58,87
U 81,59,87
K 33,80,81
v_lshlrev_b32 v80, 16, v33
v_and_b32 v81, 0xffff0000, v33
Zaaa 220,52,24
Ta 12,15,24576
Ta 14,15,25600
v_lshlrev_b32 v82, 16, v45
)"
R"(v_and_b32 v83, 0xffff0000, v45
v_fmac_f32 v84, v80, v82
v_fmac_f32 v84, v81, v83
Maaa 220,148,28
v_sub_f32 v60, v60, v85
v_sub_f32 v61, v61, v85
K 80,60,61
v_lshlrev_b32 v60, 16, v80
v_and_b32 v61, 0xffff0000, v80
U 80,60,87
Zaaa 216,144,28
U 81,61,87
K 34,80,81
v_lshlrev_b32 v80, 16, v34
v_and_b32 v81, 0xffff0000, v34
v_lshlrev_b32 v82, 16, v46
v_and_b32 v83, 0xffff0000, v46
Maaa 216,48,24
v_fmac_f32 v84, v80, v82
v_fmac_f32 v84, v81, v83
v_sub_f32 v62, v62, v85
v_sub_f32 v63, v63, v85
K 80,62,63
Zvaa 252,44,24
v_lshlrev_b32 v62, 16, v80
v_and_b32 v63, 0xffff0000, v80
U 80,62,87
U 81,63,87
K 35,80,81
Mvaa 252,140,28
v_lshlrev_b32 v80, 16, v35
v_and_b32 v81, 0xffff0000, v35
v_lshlrev_b32 v82, 16, v47
v_and_b32 v83, 0xffff0000, v47
v_fmac_f32 v84, v80, v82
Zvaa 248,136,28
v_fmac_f32 v84, v81, v83
v_sub_f32 v64, v64, v85
v_sub_f32 v65, v65, v85
K 80,64,65
v_lshlrev_b32 v64, 16, v80
Mvaa 248,40,24
v_and_b32 v65, 0xffff0000, v80
U 80,64,87
U 81,65,87
K 36,80,81
v_lshlrev_b32 v80, 16, v36
Zvaa 188,36,24
v_and_b32 v81, 0xffff0000, v36
v_lshlrev_b32 v82, 16, v48
v_and_b32 v83, 0xffff0000, v48
v_fmac_f32 v84, v80, v82
v_fmac_f32 v84, v81, v83
Mvaa 188,132,28
v_sub_f32 v66, v66, v85
v_sub_f32 v67, v67, v85
K 80,66,67
v_lshlrev_b32 v66, 16, v80
v_and_b32 v67, 0xffff0000, v80
U 80,66,87
Zvaa 184,128,28
U 81,67,87
K 37,80,81
v_lshlrev_b32 v80, 16, v37
v_and_b32 v81, 0xffff0000, v37
v_lshlrev_b32 v82, 16, v49
Mvaa 184,32,24
W 4
v_and_b32 v83, 0xffff0000, v49
v_fmac_f32 v84, v80, v82
v_fmac_f32 v84, v81, v83
v_sub_f32 v68, v68, v85
v_sub_f32 v69, v69, v85
Zvaa 192,32,0
K 80,68,69
v_lshlrev_b32 v68, 16, v80
v_and_b32 v69, 0xffff0000, v80
U 80,68,87
U 81,69,87
Mvaa 192,128,4
K 38,80,81
v_lshlrev_b32 v80, 16, v38
v_and_b32 v81, 0xffff0000, v38
v_lshlrev_b32 v82, 16, v50
v_and_b32 v83, 0xffff0000, v50
Zvaa 196,132,4
Ta 16,24,16384
v_fmac_f32 v84, v80, v82
v_fmac_f32 v84, v81, v83
v_sub_f32 v70, v70, v85
v_sub_f32 v71, v71, v85
K 80,70,71
Mvaa 196,36,0
Ta 18,24,17408
v_lshlrev_b32 v70, 16, v80
v_and_b32 v71, 0xffff0000, v80
U 80,70,87
U 81,71,87
K 39,80,81
Zaaa 160,40,0
Ta 20,24,24576
Ta 22,24,25600
v_lshlrev_b32 v80, 16, v39
v_and_b32 v81, 0xffff0000, v39
v_lshlrev_b32 v82, 16, v51
v_and_b32 v83, 0xffff0000, v51
Maaa 160,136,4
v_fmac_f32 v84, v80, v82
v_fmac_f32 v84, v81, v83
v_sub_f32 v72, v72, v85
v_sub_f32 v73, v73, v85
K 80,72,73
v_lshlrev_b32 v72, 16, v80
Zaaa 164,140,4
v_and_b32 v73, 0xffff0000, v80
U 80,72,87
U 81,73,87
K 40,80,81
v_lshlrev_b32 v80, 16, v40
v_and_b32 v81, 0xffff0000, v40
Maaa 164,44,0
v_lshlrev_b32 v82, 16, v52
v_and_b32 v83, 0xffff0000, v52
v_fmac_f32 v84, v80, v82
v_fmac_f32 v84, v81, v83
v_sub_f32 v74, v74, v85
Zaaa 224,48,0
v_sub_f32 v75, v75, v85
K 80,74,75
v_lshlrev_b32 v74, 16, v80
v_and_b32 v75, 0xffff0000, v80
U 80,74,87
Maaa 224,144,4
U 81,75,87
K 41,80,81
v_lshlrev_b32 v80, 16, v41
v_and_b32 v81, 0xffff0000, v41
v_lshlrev_b32 v82, 16, v53
Zaaa 228,148,4
v_and_b32 v83, 0xffff0000, v53
v_fmac_f32 v84, v80, v82
v_fmac_f32 v84, v81, v83
v_sub_f32 v76, v76, v85
v_sub_f32 v77, v77, v85
Maaa 228,52,0
K 80,76,77
v_lshlrev_b32 v76, 16, v80
v_and_b32 v77, 0xffff0000, v80
U 80,76,87
U 81,77,87
Zaaa 96,56,0
K 42,80,81
v_lshlrev_b32 v80, 16, v42
v_and_b32 v81, 0xffff0000, v42
v_lshlrev_b32 v82, 16, v54
v_and_b32 v83, 0xffff0000, v54
Maaa 96,152,4
v_fmac_f32 v84, v80, v82
v_fmac_f32 v84, v81, v83
v_sub_f32 v78, v78, v85
v_sub_f32 v79, v79, v85
K 80,78,79
v_lshlrev_b32 v78, 16, v80
Zaaa 100,156,4
v_and_b32 v79, 0xffff0000, v80
U 80,78,87
U 81,79,87
K 43,80,81
v_lshlrev_b32 v80, 16, v43
Maaa 100,60,0
W 4
v_and_b32 v81, 0xffff0000, v43
v_lshlrev_b32 v82, 16, v55
v_and_b32 v83, 0xffff0000, v55
v_fmac_f32 v84, v80, v82
v_fmac_f32 v84, v81, v83
Zaaa 108,60,8
Ta 24,25,16384
Ta 26,25,17408
v_lshrrev_b32 v82, 9, v1
s_lshl_b32 s80, s14, 1
v_add_u32 v82, s80, v82
v_mul_u32_u24 v83, 1552, v82
v_add_u32 v83, v83, v88
Maaa 108,156,12
v_add_u32 v83, 131072, v83
ds_write_b128 v83, v[32:35] offset:0
ds_write_b128 v83, v[36:39] offset:512
ds_write_b128 v83, v[40:43] offset:1024
v_add_f32_dpp v84, v84, v84 quad_perm:[1,0,3,2] row_mask:0xf bank_mask:0xf
Zaaa 104,152,12
Ta 28,25,24576
Ta 30,25,25600
Maaa 104,56,8
v_add_f32_dpp v84, v84, v84 quad_perm:[2,3,0,1] row_mask:0xf bank_mask:0xf
s_nop 1
v_add_f32_dpp v84, v84, v84 row_half_mirror row_mask:0xf bank_mask:0xf
s_nop 1
v_add_f32_dpp v84, v84, v84 row_mirror row_mask:0xf bank_mask:0xf
v_mov_b32 v81, v84
s_nop 1
v_permlane16_swap_b32 v84, v81
A 84,84,81
Zaaa 236,52,8
K 80,84,84
v_lshlrev_b32 v80, 16, v80
s_lshl_b32 s80, s15, 3
v_add_u32 v82, s80, v82
v_sub_u32 v83, 2047, v82
v_ashrrev_i32 v81, 31, v80
v_or_b32 v81, 0x80000000, v81
Maaa 236,148,12
v_xor_b32 v80, v80, v81
v_and_b32 v80, 0xffff0000, v80
v_or_b32 v80, v80, v83
v_and_b32 v82, 7, v82
v_lshlrev_b32 v82, 2, v82
v_add_u32 v82, 143488, v82
Zaaa 232,144,12
ds_write_b32 v82, v80
Maaa 232,48,8
Zaaa 172,44,8
Maaa 172,140,12
Zaaa 168,136,12
s_waitcnt vmcnt(1) lgkmcnt(0)
s_barrier
Maaa 168,40,8
s_add_u32 s64, s64, 128
v_bfe_u32 v80, v1, 4, 3
Zvaa 204,36,8
v_mul_u32_u24 v81, 1552, v80
Mvaa 204,132,12
s_and_b32 s64, s64, 1023
v_lshrrev_b32 v82, 7, v1
Zvaa 200,128,12
v_lshl_add_u32 v81, v82, 4, v81
Mvaa 200,32,8
s_lshl_b32 s80, s14, 7
Zvaa 208,32,16
s_lshl_b32 s65, s64, 12
s_add_u32 s80, s80, 131072
Mvaa 208,128,20
v_add_u32 v81, s80, v81
Zvaa 212,132,20
s_add_u32 m0, s62, 0
ds_read_b128 v[32:35], v81
Ta 0,10,49152
Mvaa 212,36,16
Ta 2,10,50176
Rv 36,81,512
Zaaa 176,40,16
s_and_b32 s80, s14, 2
Ta 4,10,57344
Ta 6,10,58368
Maaa 176,136,20
s_lshl_b32 s80, s80, 7
D 16,44,65,0
Zaaa 180,140,20
v_subrev_u32 v81, s80, v81
Maaa 180,44,16
Rv 40,81,1024
Zaaa 240,48,16
v_lshlrev_b32 v80, 2, v80
Maaa 240,144,20
D 17,44,65,1024
v_add_u32 v80, 143488, v80
Zaaa 244,148,20
ds_read_b32 v84, v80
Maaa 244,52,16
v_mov_b32 v85, s13
Zaaa 112,56,16
v_lshrrev_b32 v83, 1, v1
Maaa 112,152,20
s_mul_i32 s82, s15, 10240
D 18,44,65,2048
Zaaa 116,156,20
s_add_u32 s82, s82, 131072
Maaa 116,60,16
s_lshl_b32 s80, s14, 10
Zaaa 124,60,24
s_add_u32 s83, s82, s80
Ra 60,26,47104
Maaa 124,156,28
D 19,44,65,3072
Ra 156,27,47104
Zaaa 120,152,28
s_add_u32 m0, s62, 4096
Ra 152,27,45056
Ta 8,11,49152
Maaa 120,56,24
Ta 10,11,50176
Ra 56,26,45056
Zaaa 252,52,24
Ta 12,11,57344
Ta 14,11,58368
Maaa 252,148,28
W 8
buffer_store_dwordx4 v[32:35], v1, s[8:11], s83 offen sc1
s_add_u32 s83, s83, 4096
Zaaa 248,144,28
Ra 52,26,43008
Ra 148,27,43008
Maaa 248,48,24
Ra 144,27,40960
Ra 48,26,40960
Zaaa 188,44,24
Ra 44,26,38912
Maaa 188,140,28
D 20,44,65,0
Ra 140,27,38912
Zaaa 184,136,28
Ra 136,27,36864
Maaa 184,40,24
Ra 40,26,36864
Zvaa 220,36,24
Ra 36,26,34816
Mvaa 220,132,28
buffer_store_dwordx4 v[36:39], v1, s[8:11], s83 offen sc1
Ra 132,27,34816
Zvaa 216,128,28
s_and_b32 s80, s80, 1024
s_add_u32 s83, s82, s80
s_add_u32 s83, s83, 8192
Mvaa 216,32,24
W 12
Ra 128,27,32768
Ra 32,26,32768
Maaa 68,60,0
Maaa 68,156,4
D 21,44,65,1024
Maaa 64,152,4
Ta 16,12,49152
Ta 18,12,50176
Maaa 64,56,0
W 10
Ta 20,12,57344
Ta 22,12,58368
Maaa 196,52,0
Maaa 196,148,4
D 22,44,65,2048
Maaa 192,144,4
Maaa 192,48,0
W 8
Mvaa 228,44,0
Mvaa 228,140,4
buffer_store_dwordx4 v[40:43], v1, s[8:11], s83 offen sc1
Mvaa 224,136,4
Mvaa 224,40,0
W 4
Mvaa 164,36,0
Mvaa 164,132,4
D 23,44,65,3072
Mvaa 160,128,4
s_add_u32 m0, s61, 0
Mvaa 160,32,0
Mvaa 168,32,8
Mvaa 168,128,12
D 2,40,64,0
Mvaa 172,132,12
Ta 24,13,49152
Ta 26,13,50176
Mvaa 172,36,8
Ta 28,13,57344
Ta 30,13,58368
Mvaa 232,40,8
.Lh_0_1_1%=:
s_cbranch_execz .Lhr_5%=
Mvaa 232,136,12
D 3,40,64,1024
Mvaa 236,140,12
Mvaa 236,44,8
Maaa 200,48,8
Maaa 200,144,12
D 4,40,64,2048
Maaa 204,148,12
Maaa 204,52,8
Maaa 72,56,8
Maaa 72,152,12
D 5,40,64,3072
Maaa 76,156,12
s_add_u32 m0, s61, 4096
Maaa 76,60,8
W 4
Maaa 84,60,16
Maaa 84,156,20
D 6,40,64,0
Maaa 80,152,20
Ta 0,14,49152
Ta 2,14,50176
Maaa 80,56,16
Ta 4,14,57344
Ta 6,14,58368
Maaa 212,52,16
Maaa 212,148,20
D 7,40,64,1024
Maaa 208,144,20
Maaa 208,48,16
Mvaa 244,44,16
Mvaa 244,140,20
D 8,40,64,2048
)"
R"(Mvaa 240,136,20
Mvaa 240,40,16
Mvaa 180,36,16
Mvaa 180,132,20
D 9,40,64,3072
Mvaa 176,128,20
Mvaa 176,32,16
W 4
Mvaa 184,32,24
Mvaa 184,128,28
global_load_dword v142, v[140:141], off
v_lshl_add_u64 v[140:141], v[140:141], 0, s[68:69]
v_bfe_u32 v147, v1, 5, 3
Mvaa 188,132,28
v_lshlrev_b32 v147, 13, v147
v_and_b32 v148, 16, v1
Mvaa 188,36,24
Ta 8,15,49152
v_lshl_add_u32 v147, v148, 3, v147
Mvaa 248,40,24
s_lshl_b32 s92, s57, 2
s_add_u32 s92, s92, s14
s_lshl_b32 s92, s92, 16
Mvaa 248,136,28
buffer_load_dword v142, v147, s[48:51], s92 offen
Ta 10,15,50176
Mvaa 252,140,28
Ta 12,15,57344
Ta 14,15,58368
Mvaa 252,44,24
Maaa 216,48,24
Maaa 216,144,28
Maaa 220,148,28
Maaa 220,52,24
Maaa 88,56,24
Maaa 88,152,28
Maaa 92,156,28
Maaa 92,60,24
W 4
Maaa 100,60,0
Maaa 100,156,4
Maaa 96,152,4
Ta 16,24,49152
Ta 18,24,50176
Maaa 96,56,0
Ta 20,24,57344
Ta 22,24,58368
Maaa 228,52,0
Maaa 228,148,4
Maaa 224,144,4
Maaa 224,48,0
Maaa 164,44,0
Maaa 164,140,4
Maaa 160,136,4
Maaa 160,40,0
Mvaa 196,36,0
Mvaa 196,132,4
Mvaa 192,128,4
Mvaa 192,32,0
W 4
Mvaa 200,32,8
Ta 24,25,49152
Ta 26,25,50176
Mvaa 200,128,12
Mvaa 204,132,12
Ta 28,25,57344
Ta 30,25,58368
Mvaa 204,36,8
Maaa 168,40,8
Maaa 168,136,12
Maaa 172,140,12
Maaa 172,44,8
Maaa 232,48,8
Maaa 232,144,12
Maaa 236,148,12
s_waitcnt vmcnt(2) lgkmcnt(0)
s_barrier
s_cmp_eq_u32 s14, 0
s_cbranch_scc0 .Lpubr_15%=
s_lshl_b32 s80, s15, 6
s_add_u32 s80, s80, 40960
s_mov_b64 exec, 0xff
buffer_store_dwordx2 v[84:85], v83, s[8:11], s80 offen sc1
s_mov_b64 exec, -1
.Lpubr_15%=:
Maaa 236,52,8
s_add_u32 s64, s64, 128
Maaa 104,56,8
Maaa 104,152,12
s_and_b32 s64, s64, 1023
Maaa 108,156,12
Maaa 108,60,8
Maaa 116,60,16
s_lshl_b32 s65, s64, 12
Maaa 116,156,20
Maaa 112,152,20
Ta 0,10,16384
Ta 2,10,17408
Maaa 112,56,16
s_add_u32 m0, s62, 32768
Ta 4,10,24576
Ta 6,10,25600
Maaa 244,52,16
Maaa 244,148,20
Maaa 240,144,20
D 16,44,65,0
Maaa 240,48,16
Maaa 180,44,16
Maaa 180,140,20
Maaa 176,136,20
D 17,44,65,1024
Maaa 176,40,16
Mvaa 212,36,16
Mvaa 212,132,20
Mvaa 208,128,20
D 18,44,65,2048
Mvaa 208,32,16
Mvaa 216,32,24
Ra 32,26,0
Mvaa 216,128,28
Ra 128,27,0
Mvaa 220,132,28
D 19,44,65,3072
Ra 132,27,2048
Mvaa 220,36,24
s_add_u32 m0, s62, 36864
Ta 8,11,16384
Ta 10,11,17408
Maaa 184,40,24
Ra 36,26,2048
Ta 12,11,24576
Maaa 184,136,28
Ta 14,11,25600
Ra 40,26,4096
Maaa 188,140,28
D 20,44,65,0
Ra 136,27,4096
Maaa 188,44,24
Ra 140,27,6144
Ra 44,26,6144
Maaa 248,48,24
Ra 48,26,8192
Maaa 248,144,28
Ra 144,27,8192
Maaa 252,148,28
D 21,44,65,1024
Ra 148,27,10240
Maaa 252,52,24
Ra 52,26,10240
Maaa 120,56,24
Ra 56,26,12288
Maaa 120,152,28
Ra 152,27,12288
Maaa 124,156,28
D 22,44,65,2048
Ra 156,27,14336
Maaa 124,60,24
W 13
Ra 60,26,14336
Mvaa 160,32,0
Mvaa 160,128,4
Mvaa 164,132,4
D 23,44,65,3072
Ta 16,12,16384
Mvaa 164,36,0
W 9
s_add_u32 m0, s61, 32768
Ta 18,12,17408
Mvaa 224,40,0
Ta 20,12,24576
Ta 22,12,25600
Mvaa 224,136,4
Mvaa 228,140,4
D 2,40,64,0
Mvaa 228,44,0
W 8
Maaa 192,48,0
Maaa 192,144,4
Maaa 196,148,4
D 3,40,64,1024
Maaa 196,52,0
W 4
Maaa 64,56,0
Maaa 64,152,4
Maaa 68,156,4
D 4,40,64,2048
Maaa 68,60,0
Maaa 76,60,8
Maaa 76,156,12
Maaa 72,152,12
D 5,40,64,3072
Ta 24,13,16384
Maaa 72,56,8
s_add_u32 m0, s61, 36864
Ta 26,13,17408
Ta 28,13,24576
Maaa 204,52,8
Ta 30,13,25600
Maaa 204,148,12
Maaa 200,144,12
D 6,40,64,0
Maaa 200,48,8
Mvaa 236,44,8
Mvaa 236,140,12
Mvaa 232,136,12
D 7,40,64,1024
Mvaa 232,40,8
Mvaa 172,36,8
Mvaa 172,132,12
Mvaa 168,128,12
D 8,40,64,2048
Mvaa 168,32,8
W 4
Mvaa 176,32,16
Mvaa 176,128,20
Mvaa 180,132,20
D 9,40,64,3072
Ta 0,14,16384
Mvaa 180,36,16
Ta 2,14,17408
Ta 4,14,24576
Mvaa 240,40,16
Ta 6,14,25600
Mvaa 240,136,20
Mvaa 244,140,20
Mvaa 244,44,16
Maaa 208,48,16
Maaa 208,144,20
Maaa 212,148,20
Maaa 212,52,16
Maaa 80,56,16
Maaa 80,152,20
Maaa 84,156,20
Maaa 84,60,16
W 4
Maaa 92,60,24
Maaa 92,156,28
Maaa 88,152,28
Ta 8,15,16384
Ta 10,15,17408
Maaa 88,56,24
Ta 12,15,24576
Ta 14,15,25600
Maaa 220,52,24
Maaa 220,148,28
Maaa 216,144,28
Maaa 216,48,24
Mvaa 252,44,24
Mvaa 252,140,28
Mvaa 248,136,28
Mvaa 248,40,24
Mvaa 188,36,24
Mvaa 188,132,28
Mvaa 184,128,28
Mvaa 184,32,24
W 4
Mvaa 192,32,0
Mvaa 192,128,4
Mvaa 196,132,4
Ta 16,24,16384
Ta 18,24,17408
Mvaa 196,36,0
Ta 20,24,24576
Ta 22,24,25600
Maaa 160,40,0
Maaa 160,136,4
Maaa 164,140,4
Maaa 164,44,0
Maaa 224,48,0
Maaa 224,144,4
Maaa 228,148,4
Maaa 228,52,0
Maaa 96,56,0
Maaa 96,152,4
Maaa 100,156,4
Maaa 100,60,0
W 4
Maaa 108,60,8
Ta 24,25,16384
Ta 26,25,17408
Maaa 108,156,12
Maaa 104,152,12
Ta 28,25,24576
Ta 30,25,25600
Maaa 104,56,8
Maaa 236,52,8
Maaa 236,148,12
Maaa 232,144,12
Maaa 232,48,8
Maaa 172,44,8
Maaa 172,140,12
Maaa 168,136,12
s_waitcnt vmcnt(0) lgkmcnt(0)
s_barrier
Maaa 168,40,8
s_add_u32 s64, s64, 128
Mvaa 204,36,8
Mvaa 204,132,12
s_and_b32 s64, s64, 1023
Mvaa 200,128,12
Mvaa 200,32,8
Mvaa 208,32,16
s_lshl_b32 s65, s64, 12
Mvaa 208,128,20
Mvaa 212,132,20
s_add_u32 m0, s62, 0
Ta 0,10,49152
Ta 2,10,50176
Mvaa 212,36,16
Ta 4,10,57344
Ta 6,10,58368
Maaa 176,40,16
Maaa 176,136,20
D 16,44,65,0
Maaa 180,140,20
Maaa 180,44,16
Maaa 240,48,16
Maaa 240,144,20
D 17,44,65,1024
Maaa 244,148,20
Maaa 244,52,16
Maaa 112,56,16
Maaa 112,152,20
D 18,44,65,2048
Maaa 116,156,20
Maaa 116,60,16
Maaa 124,60,24
Ra 60,26,47104
Maaa 124,156,28
D 19,44,65,3072
Ra 156,27,47104
Maaa 120,152,28
s_add_u32 m0, s62, 4096
Ra 152,27,45056
Ta 8,11,49152
Maaa 120,56,24
Ta 10,11,50176
Ra 56,26,45056
Maaa 252,52,24
Ta 12,11,57344
Ta 14,11,58368
Maaa 252,148,28
D 20,44,65,0
Ra 52,26,43008
Maaa 248,144,28
Ra 148,27,43008
Ra 144,27,40960
Maaa 248,48,24
Ra 48,26,40960
Maaa 188,44,24
Ra 44,26,38912
Maaa 188,140,28
D 21,44,65,1024
Ra 140,27,38912
Maaa 184,136,28
Ra 136,27,36864
Maaa 184,40,24
Ra 40,26,36864
Mvaa 220,36,24
Ra 36,26,34816
Mvaa 220,132,28
D 22,44,65,2048
Ra 132,27,34816
Mvaa 216,128,28
Ra 128,27,32768
Mvaa 216,32,24
W 13
Ra 32,26,32768
Maaa 68,60,0
Maaa 68,156,4
D 23,44,65,3072
Maaa 64,152,4
s_add_u32 m0, s61, 0
Ta 16,12,49152
Ta 18,12,50176
Maaa 64,56,0
W 10
Ta 20,12,57344
Ta 22,12,58368
Maaa 196,52,0
Maaa 196,148,4
D 2,40,64,0
Maaa 192,144,4
Maaa 192,48,0
W 8
Mvaa 228,44,0
Mvaa 228,140,4
D 3,40,64,1024
Mvaa 224,136,4
Mvaa 224,40,0
W 4
Mvaa 164,36,0
Mvaa 164,132,4
D 4,40,64,2048
Mvaa 160,128,4
Mvaa 160,32,0
Mvaa 168,32,8
Mvaa 168,128,12
D 5,40,64,3072
Mvaa 172,132,12
)"
R"(s_add_u32 m0, s61, 4096
Ta 24,13,49152
Ta 26,13,50176
Mvaa 172,36,8
Ta 28,13,57344
Ta 30,13,58368
Mvaa 232,40,8
Mvaa 232,136,12
D 6,40,64,0
Mvaa 236,140,12
Mvaa 236,44,8
Maaa 200,48,8
Maaa 200,144,12
D 7,40,64,1024
Maaa 204,148,12
Maaa 204,52,8
Maaa 72,56,8
Maaa 72,152,12
D 8,40,64,2048
Maaa 76,156,12
Maaa 76,60,8
W 4
Maaa 84,60,16
Maaa 84,156,20
D 9,40,64,3072
Maaa 80,152,20
Ta 0,14,49152
Ta 2,14,50176
Maaa 80,56,16
Ta 4,14,57344
Ta 6,14,58368
Maaa 212,52,16
Maaa 212,148,20
global_load_dword v142, v[140:141], off
v_lshl_add_u64 v[140:141], v[140:141], 0, s[68:69]
Maaa 208,144,20
Maaa 208,48,16
Mvaa 244,44,16
Mvaa 244,140,20
Mvaa 240,136,20
Mvaa 240,40,16
Mvaa 180,36,16
Mvaa 180,132,20
Mvaa 176,128,20
Mvaa 176,32,16
W 4
Mvaa 184,32,24
Mvaa 184,128,28
Mvaa 188,132,28
Ta 8,15,49152
Ta 10,15,50176
Mvaa 188,36,24
Ta 12,15,57344
Ta 14,15,58368
Mvaa 248,40,24
Mvaa 248,136,28
Mvaa 252,140,28
Mvaa 252,44,24
Maaa 216,48,24
Maaa 216,144,28
Maaa 220,148,28
Maaa 220,52,24
Maaa 88,56,24
Maaa 88,152,28
Maaa 92,156,28
Maaa 92,60,24
W 4
Maaa 100,60,0
Maaa 100,156,4
Maaa 96,152,4
Ta 16,24,49152
Ta 18,24,50176
Maaa 96,56,0
Ta 20,24,57344
Ta 22,24,58368
Maaa 228,52,0
Maaa 228,148,4
Maaa 224,144,4
Maaa 224,48,0
Maaa 164,44,0
Maaa 164,140,4
Maaa 160,136,4
Maaa 160,40,0
Mvaa 196,36,0
Mvaa 196,132,4
Mvaa 192,128,4
Mvaa 192,32,0
W 4
Mvaa 200,32,8
Ta 24,25,49152
Ta 26,25,50176
Mvaa 200,128,12
Mvaa 204,132,12
Ta 28,25,57344
Ta 30,25,58368
Mvaa 204,36,8
Maaa 168,40,8
Maaa 168,136,12
.Lh_0_2_1%=:
s_cbranch_execz .Lhr_5%=
Maaa 172,140,12
Maaa 172,44,8
Maaa 232,48,8
Maaa 232,144,12
Maaa 236,148,12
s_waitcnt vmcnt(1) lgkmcnt(0)
s_barrier
Maaa 236,52,8
s_add_u32 s64, s64, 128
Maaa 104,56,8
Maaa 104,152,12
s_and_b32 s64, s64, 1023
Maaa 108,156,12
Maaa 108,60,8
Maaa 116,60,16
s_lshl_b32 s65, s64, 12
Maaa 116,156,20
Maaa 112,152,20
Ta 0,10,16384
Ta 2,10,17408
Maaa 112,56,16
s_add_u32 m0, s62, 32768
Ta 4,10,24576
Ta 6,10,25600
Maaa 244,52,16
Maaa 244,148,20
Maaa 240,144,20
D 16,44,65,0
Maaa 240,48,16
Maaa 180,44,16
Maaa 180,140,20
Maaa 176,136,20
D 17,44,65,1024
Maaa 176,40,16
Mvaa 212,36,16
Mvaa 212,132,20
Mvaa 208,128,20
D 18,44,65,2048
Mvaa 208,32,16
Mvaa 216,32,24
Ra 32,26,0
Mvaa 216,128,28
Ra 128,27,0
Mvaa 220,132,28
D 19,44,65,3072
Ra 132,27,2048
Mvaa 220,36,24
s_add_u32 m0, s62, 36864
Ta 8,11,16384
Ta 10,11,17408
Maaa 184,40,24
Ra 36,26,2048
Ta 12,11,24576
Maaa 184,136,28
Ta 14,11,25600
Ra 40,26,4096
Maaa 188,140,28
D 20,44,65,0
Ra 136,27,4096
Maaa 188,44,24
Ra 140,27,6144
Ra 44,26,6144
Maaa 248,48,24
Ra 48,26,8192
Maaa 248,144,28
Ra 144,27,8192
Maaa 252,148,28
D 21,44,65,1024
Ra 148,27,10240
Maaa 252,52,24
Ra 52,26,10240
Maaa 120,56,24
Ra 56,26,12288
Maaa 120,152,28
Ra 152,27,12288
Maaa 124,156,28
D 22,44,65,2048
Ra 156,27,14336
Maaa 124,60,24
W 13
Ra 60,26,14336
Mvaa 160,32,0
Mvaa 160,128,4
Mvaa 164,132,4
D 23,44,65,3072
Ta 16,12,16384
Mvaa 164,36,0
W 9
s_add_u32 m0, s61, 32768
Ta 18,12,17408
Mvaa 224,40,0
Ta 20,12,24576
Ta 22,12,25600
Mvaa 224,136,4
Mvaa 228,140,4
D 2,40,64,0
Mvaa 228,44,0
W 8
Maaa 192,48,0
Maaa 192,144,4
Maaa 196,148,4
D 3,40,64,1024
Maaa 196,52,0
W 4
Maaa 64,56,0
Maaa 64,152,4
Maaa 68,156,4
D 4,40,64,2048
Maaa 68,60,0
Maaa 76,60,8
Maaa 76,156,12
Maaa 72,152,12
D 5,40,64,3072
Ta 24,13,16384
Maaa 72,56,8
s_add_u32 m0, s61, 36864
Ta 26,13,17408
Ta 28,13,24576
Maaa 204,52,8
Ta 30,13,25600
Maaa 204,148,12
Maaa 200,144,12
D 6,40,64,0
Maaa 200,48,8
Mvaa 236,44,8
Mvaa 236,140,12
Mvaa 232,136,12
D 7,40,64,1024
Mvaa 232,40,8
Mvaa 172,36,8
Mvaa 172,132,12
Mvaa 168,128,12
D 8,40,64,2048
Mvaa 168,32,8
W 4
Mvaa 176,32,16
Mvaa 176,128,20
Mvaa 180,132,20
D 9,40,64,3072
Ta 0,14,16384
Mvaa 180,36,16
s_add_u32 s92, s92, 2097152
Ta 2,14,17408
Ta 4,14,24576
Mvaa 240,40,16
Ta 6,14,25600
Mvaa 240,136,20
Mvaa 244,140,20
buffer_load_dword v142, v147, s[48:51], s92 offen
Mvaa 244,44,16
s_add_u32 s92, s92, 2097152
Maaa 208,48,16
Maaa 208,144,20
Maaa 212,148,20
buffer_load_dword v142, v147, s[48:51], s92 offen
Maaa 212,52,16
Maaa 80,56,16
Maaa 80,152,20
Maaa 84,156,20
Maaa 84,60,16
W 4
Maaa 92,60,24
Maaa 92,156,28
Maaa 88,152,28
Ta 8,15,16384
Ta 10,15,17408
Maaa 88,56,24
Ta 12,15,24576
Ta 14,15,25600
Maaa 220,52,24
Maaa 220,148,28
Maaa 216,144,28
Maaa 216,48,24
Mvaa 252,44,24
Mvaa 252,140,28
Mvaa 248,136,28
Mvaa 248,40,24
Mvaa 188,36,24
Mvaa 188,132,28
Mvaa 184,128,28
Mvaa 184,32,24
W 4
Mvaa 192,32,0
Mvaa 192,128,4
Mvaa 196,132,4
Ta 16,24,16384
Ta 18,24,17408
Mvaa 196,36,0
Ta 20,24,24576
Ta 22,24,25600
Maaa 160,40,0
Maaa 160,136,4
Maaa 164,140,4
Maaa 164,44,0
Maaa 224,48,0
Maaa 224,144,4
Maaa 228,148,4
Maaa 228,52,0
Maaa 96,56,0
Maaa 96,152,4
Maaa 100,156,4
Maaa 100,60,0
W 4
Maaa 108,60,8
Ta 24,25,16384
Ta 26,25,17408
Maaa 108,156,12
Maaa 104,152,12
Ta 28,25,24576
Ta 30,25,25600
Maaa 104,56,8
Maaa 236,52,8
Maaa 236,148,12
Maaa 232,144,12
Maaa 232,48,8
Maaa 172,44,8
Maaa 172,140,12
Maaa 168,136,12
s_waitcnt vmcnt(2) lgkmcnt(0)
s_barrier
Maaa 168,40,8
s_add_u32 s64, s64, 128
Mvaa 204,36,8
Mvaa 204,132,12
s_and_b32 s64, s64, 1023
Mvaa 200,128,12
Mvaa 200,32,8
Mvaa 208,32,16
s_lshl_b32 s65, s64, 12
Mvaa 208,128,20
Mvaa 212,132,20
Ta 0,10,49152
Ta 2,10,50176
Mvaa 212,36,16
s_add_u32 m0, s62, 0
Ta 4,10,57344
Ta 6,10,58368
Maaa 176,40,16
Maaa 176,136,20
Maaa 180,140,20
D 16,44,65,0
Maaa 180,44,16
Maaa 240,48,16
Maaa 240,144,20
Maaa 244,148,20
D 17,44,65,1024
Maaa 244,52,16
Maaa 112,56,16
Maaa 112,152,20
Maaa 116,156,20
D 18,44,65,2048
Maaa 116,60,16
Maaa 124,60,24
Ra 60,26,47104
Maaa 124,156,28
Ra 156,27,47104
Maaa 120,152,28
D 19,44,65,3072
Ra 152,27,45056
Maaa 120,56,24
s_add_u32 m0, s62, 4096
Ta 8,11,49152
Ta 10,11,50176
Maaa 252,52,24
Ra 56,26,45056
Ta 12,11,57344
Maaa 252,148,28
Ta 14,11,58368
Ra 52,26,43008
Maaa 248,144,28
D 20,44,65,0
v_lshrrev_b32 v28, 1, v1
Maaa 248,48,24
s_lshl_b32 s80, s60, 9
Ra 148,27,43008
Ra 144,27,40960
Maaa 188,44,24
s_add_u32 s80, s80, 40960
Ra 48,26,40960
Ra 44,26,38912
Maaa 188,140,28
Ra 140,27,38912
Maaa 184,136,28
D 21,44,65,1024
Ra 136,27,36864
Maaa 184,40,24
Ra 40,26,36864
Mvaa 220,36,24
Ra 36,26,34816
Mvaa 220,132,28
Ra 132,27,34816
Mvaa 216,128,28
buffer_load_dwordx2 v[28:29], v28, s[8:11], s80 offen sc1
Ra 128,27,32768
Mvaa 216,32,24
W 13
Ra 32,26,32768
Maaa 68,60,0
Maaa 68,156,4
Maaa 64,152,4
)"
R"(D 22,44,65,2048
Ta 16,12,49152
Maaa 64,56,0
W 9
Ta 18,12,50176
Ta 20,12,57344
Maaa 196,52,0
Ta 22,12,58368
Maaa 196,148,4
Maaa 192,144,4
D 23,44,65,3072
Maaa 192,48,0
W 8
s_add_u32 m0, s61, 0
Mvaa 228,44,0
Mvaa 228,140,4
Mvaa 224,136,4
D 2,40,64,0
Mvaa 224,40,0
W 4
Mvaa 164,36,0
Mvaa 164,132,4
Mvaa 160,128,4
D 3,40,64,1024
Mvaa 160,32,0
Mvaa 168,32,8
Mvaa 168,128,12
Mvaa 172,132,12
D 4,40,64,2048
Ta 24,13,49152
Mvaa 172,36,8
Ta 26,13,50176
Ta 28,13,57344
Mvaa 232,40,8
Ta 30,13,58368
Mvaa 232,136,12
Mvaa 236,140,12
D 5,40,64,3072
Mvaa 236,44,8
s_add_u32 m0, s61, 4096
Maaa 200,48,8
Maaa 200,144,12
Maaa 204,148,12
D 6,40,64,0
Maaa 204,52,8
Maaa 72,56,8
Maaa 72,152,12
Maaa 76,156,12
D 7,40,64,1024
Maaa 76,60,8
W 4
Maaa 84,60,16
Maaa 84,156,20
Maaa 80,152,20
D 8,40,64,2048
Ta 0,14,49152
Maaa 80,56,16
Ta 2,14,50176
Ta 4,14,57344
Maaa 212,52,16
Ta 6,14,58368
Maaa 212,148,20
Maaa 208,144,20
D 9,40,64,3072
Maaa 208,48,16
Mvaa 244,44,16
Mvaa 244,140,20
Mvaa 240,136,20
s_waitcnt vmcnt(10)
v_cmp_eq_u32 vcc, s13, v29
s_cmp_eq_u64 vcc, -1
s_cbranch_scc0 .Lspin_16%=
.Lok_17%=:
global_load_dword v142, v146, s[6:7]
Mvaa 240,40,16
Mvaa 180,36,16
v_bfe_u32 v144, v1, 8, 1
Mvaa 180,132,20
v_mul_u32_u24 v144, 10240, v144
Mvaa 176,128,20
v_and_b32 v145, 0xf0, v1
Mvaa 176,32,16
W 4
v_lshl_add_u32 v144, v145, 3, v144
Mvaa 184,32,24
s_mul_i32 s91, s58, 20480
Mvaa 184,128,28
s_add_u32 s91, s91, s28
Mvaa 188,132,28
buffer_load_dword v142, v144, s[8:11], s91 offen offset:0 sc1
Ta 8,15,49152
Mvaa 188,36,24
Ta 10,15,50176
Ta 12,15,57344
Mvaa 248,40,24
Ta 14,15,58368
Mvaa 248,136,28
Mvaa 252,140,28
Mvaa 252,44,24
Maaa 216,48,24
Maaa 216,144,28
Maaa 220,148,28
Maaa 220,52,24
Maaa 88,56,24
Maaa 88,152,28
Maaa 92,156,28
Maaa 92,60,24
W 4
Maaa 100,60,0
Maaa 100,156,4
Maaa 96,152,4
Ta 16,24,49152
Ta 18,24,50176
Maaa 96,56,0
Ta 20,24,57344
Ta 22,24,58368
Maaa 228,52,0
Maaa 228,148,4
Maaa 224,144,4
Maaa 224,48,0
Maaa 164,44,0
Maaa 164,140,4
Maaa 160,136,4
Maaa 160,40,0
Mvaa 196,36,0
Mvaa 196,132,4
Mvaa 192,128,4
Mvaa 192,32,0
W 4
Mvaa 200,32,8
Ta 24,25,49152
Ta 26,25,50176
Mvaa 200,128,12
Mvaa 204,132,12
Ta 28,25,57344
Ta 30,25,58368
Mvaa 204,36,8
Maaa 168,40,8
Maaa 168,136,12
Maaa 172,140,12
Maaa 172,44,8
Maaa 232,48,8
Maaa 232,144,12
Maaa 236,148,12
s_waitcnt vmcnt(2) lgkmcnt(0)
s_barrier
s_cmp_eq_u32 s85, 107
s_cbranch_scc1 .Lrt_19%=
.Lrtr_20%=:
Maaa 236,52,8
s_add_u32 s64, s64, 128
Maaa 104,56,8
Maaa 104,152,12
s_and_b32 s64, s64, 1023
Maaa 108,156,12
Maaa 108,60,8
Maaa 116,60,16
s_lshl_b32 s65, s64, 12
Maaa 116,156,20
Maaa 112,152,20
Ta 0,10,16384
Ta 2,10,17408
Maaa 112,56,16
s_add_u32 m0, s62, 32768
Ta 4,10,24576
Ta 6,10,25600
Maaa 244,52,16
Maaa 244,148,20
Maaa 240,144,20
D 16,44,65,0
Maaa 240,48,16
Maaa 180,44,16
Maaa 180,140,20
Maaa 176,136,20
D 17,44,65,1024
Maaa 176,40,16
Mvaa 212,36,16
Mvaa 212,132,20
Mvaa 208,128,20
D 18,44,65,2048
Mvaa 208,32,16
Mvaa 216,32,24
Ra 32,26,0
Mvaa 216,128,28
Ra 128,27,0
Mvaa 220,132,28
D 19,44,65,3072
Ra 132,27,2048
Mvaa 220,36,24
s_add_u32 m0, s62, 36864
Ta 8,11,16384
Ta 10,11,17408
Maaa 184,40,24
Ra 36,26,2048
Ta 12,11,24576
Maaa 184,136,28
Ta 14,11,25600
Ra 40,26,4096
Maaa 188,140,28
D 20,44,65,0
Ra 136,27,4096
Maaa 188,44,24
Ra 140,27,6144
Ra 44,26,6144
Maaa 248,48,24
Ra 48,26,8192
Maaa 248,144,28
Ra 144,27,8192
Maaa 252,148,28
D 21,44,65,1024
Ra 148,27,10240
Maaa 252,52,24
Ra 52,26,10240
Maaa 120,56,24
Ra 56,26,12288
Maaa 120,152,28
Ra 152,27,12288
Maaa 124,156,28
D 22,44,65,2048
Ra 156,27,14336
Maaa 124,60,24
W 13
Ra 60,26,14336
Mvaa 160,32,0
Mvaa 160,128,4
Mvaa 164,132,4
D 23,44,65,3072
Ta 16,12,16384
Mvaa 164,36,0
W 9
s_add_u32 m0, s61, 32768
Ta 18,12,17408
Mvaa 224,40,0
.Lh_0_3_1%=:
s_cbranch_execz .Lhr_5%=
Ta 20,12,24576
Ta 22,12,25600
Mvaa 224,136,4
Mvaa 228,140,4
D 2,40,64,0
Mvaa 228,44,0
W 8
Maaa 192,48,0
Maaa 192,144,4
Maaa 196,148,4
D 3,40,64,1024
Maaa 196,52,0
W 4
Maaa 64,56,0
Maaa 64,152,4
Maaa 68,156,4
D 4,40,64,2048
Maaa 68,60,0
Maaa 76,60,8
Maaa 76,156,12
Maaa 72,152,12
D 5,40,64,3072
Ta 24,13,16384
Maaa 72,56,8
s_add_u32 m0, s61, 36864
Ta 26,13,17408
Ta 28,13,24576
Maaa 204,52,8
Ta 30,13,25600
Maaa 204,148,12
Maaa 200,144,12
D 6,40,64,0
Maaa 200,48,8
Mvaa 236,44,8
Mvaa 236,140,12
Mvaa 232,136,12
D 7,40,64,1024
Mvaa 232,40,8
Mvaa 172,36,8
Mvaa 172,132,12
Mvaa 168,128,12
D 8,40,64,2048
Mvaa 168,32,8
W 4
Mvaa 176,32,16
Mvaa 176,128,20
Mvaa 180,132,20
D 9,40,64,3072
v_add_u32 v2, 131072, v10
Mvaa 180,36,16
Ta 0,14,16384
v_add_u32 v3, 131072, v11
Mvaa 240,40,16
Ta 2,14,17408
v_add_u32 v4, 131072, v12
Mvaa 240,136,20
Ta 4,14,24576
Ta 6,14,25600
Mvaa 244,140,20
buffer_load_dword v142, v144, s[8:11], s91 offen offset:2048 sc1
v_add_u32 v5, 131072, v13
Mvaa 244,44,16
s_add_u32 s91, s91, 4096
v_add_u32 v6, 131072, v14
Maaa 208,48,16
v_add_u32 v7, 131072, v15
Maaa 208,144,20
Maaa 212,148,20
v_add_u32 v8, 131072, v24
Maaa 212,52,16
Maaa 80,56,16
v_add_u32 v9, 131072, v25
Maaa 80,152,20
s_add_u32 s65, s5, 0
s_cmp_ge_u32 s65, 20
s_cselect_b32 s90, 20, 0
s_sub_u32 s65, s65, s90
Maaa 84,156,20
s_lshl_b32 s65, s65, 18
s_add_u32 m0, s63, 131072
Maaa 84,60,16
W 4
Maaa 92,60,24
Maaa 92,156,28
Maaa 88,152,28
Ta 8,15,16384
Ta 10,15,17408
Maaa 88,56,24
D 16,48,65,0
Ta 12,15,24576
Maaa 220,52,24
Ta 14,15,25600
Maaa 220,148,28
Maaa 216,144,28
Maaa 216,48,24
D 17,48,65,1024
Mvaa 252,44,24
Mvaa 252,140,28
Mvaa 248,136,28
Mvaa 248,40,24
Mvaa 188,36,24
D 18,48,65,2048
Mvaa 188,132,28
Mvaa 184,128,28
Mvaa 184,32,24
W 4
Mvaa 192,32,0
Mvaa 192,128,4
D 19,48,65,3072
Mvaa 196,132,4
s_add_u32 m0, s63, 135168
Ta 16,24,16384
Ta 18,24,17408
Mvaa 196,36,0
Ta 20,24,24576
Ta 22,24,25600
Maaa 160,40,0
Maaa 160,136,4
Maaa 164,140,4
D 20,48,65,0
Maaa 164,44,0
Maaa 224,48,0
Maaa 224,144,4
Maaa 228,148,4
Maaa 228,52,0
D 21,48,65,1024
Maaa 96,56,0
Maaa 96,152,4
Maaa 100,156,4
Maaa 100,60,0
W 4
Maaa 108,60,8
D 22,48,65,2048
Ta 24,25,16384
Maaa 108,156,12
Ta 26,25,17408
Maaa 104,152,12
Ta 28,25,24576
Ta 30,25,25600
Maaa 104,56,8
Maaa 236,52,8
D 23,48,65,3072
Maaa 236,148,12
Maaa 232,144,12
Maaa 232,48,8
Maaa 172,44,8
Maaa 172,140,12
Maaa 168,136,12
s_waitcnt vmcnt(9) lgkmcnt(0)
s_barrier
s_cmp_eq_u32 s85, 107
s_cbranch_scc1 .Lrt_21%=
.Lrtr_22%=:
Maaa 168,40,8
s_add_u32 s65, s5, 4
Mvaa 204,36,8
Mvaa 204,132,12
s_cmp_ge_u32 s65, 20
s_cselect_b32 s90, 20, 0
Mvaa 200,128,12
Mvaa 200,32,8
s_sub_u32 s65, s65, s90
Mvaa 208,32,16
Mvaa 208,128,20
s_lshl_b32 s65, s65, 18
)"
R"(Mvaa 212,132,20
Ta 0,10,49152
Ta 2,10,50176
Mvaa 212,36,16
Maaa 176,40,16
s_add_u32 m0, s63, 0
Maaa 176,136,20
Maaa 180,140,20
D 16,48,65,0
Maaa 180,44,16
Maaa 240,48,16
Maaa 240,144,20
Maaa 244,148,20
D 17,48,65,1024
Ta 4,11,49152
Maaa 244,52,16
Ta 6,11,50176
Maaa 112,56,16
Maaa 112,152,20
Maaa 116,156,20
D 18,48,65,2048
Maaa 116,60,16
Maaa 124,60,24
Ra 60,26,47104
Maaa 124,156,28
Maaa 120,152,28
D 19,48,65,3072
Ta 8,12,49152
Maaa 120,56,24
s_add_u32 m0, s63, 4096
Ta 10,12,50176
Ra 56,26,45056
Maaa 252,52,24
Ra 52,26,43008
Maaa 252,148,28
Maaa 248,144,28
D 20,48,65,0
Maaa 248,48,24
Ra 48,26,40960
Maaa 188,44,24
Ra 44,26,38912
Maaa 188,140,28
Maaa 184,136,28
D 21,48,65,1024
Ta 12,13,49152
Maaa 184,40,24
Ta 14,13,50176
Ra 40,26,36864
Mvaa 220,36,24
Ra 36,26,34816
Mvaa 220,132,28
Mvaa 216,128,28
D 22,48,65,2048
Mvaa 216,32,24
Ra 32,26,32768
W 6
Maaa 68,60,0
Maaa 64,56,0
Maaa 196,52,0
D 23,48,65,3072
Ta 16,14,49152
Maaa 192,48,0
W 1
s_add_u32 s65, s5, 8
s_cmp_ge_u32 s65, 20
s_cselect_b32 s90, 20, 0
Mvaa 228,44,0
s_sub_u32 s65, s65, s90
s_lshl_b32 s65, s65, 18
Ta 18,14,50176
Mvaa 224,40,0
s_add_u32 m0, s63, 65536
Mvaa 164,36,0
D 16,48,65,0
Mvaa 160,32,0
Mvaa 168,32,4
Mvaa 172,36,4
Mvaa 232,40,4
D 17,48,65,1024
Ta 20,15,49152
Mvaa 236,44,4
Ta 22,15,50176
Maaa 200,48,4
Maaa 204,52,4
Maaa 72,56,4
D 18,48,65,2048
Maaa 76,60,4
Maaa 84,60,8
Maaa 80,56,8
Maaa 212,52,8
D 19,48,65,3072
Ta 24,24,49152
Maaa 208,48,8
s_add_u32 m0, s63, 69632
Ta 26,24,50176
Mvaa 244,44,8
Mvaa 240,40,8
Mvaa 180,36,8
D 20,48,65,0
Mvaa 176,32,8
Mvaa 184,32,12
Mvaa 188,36,12
Mvaa 248,40,12
D 21,48,65,1024
Ta 28,25,49152
Mvaa 252,44,12
Ta 30,25,50176
Maaa 216,48,12
Ra 156,27,47104
Maaa 220,52,12
Maaa 88,56,12
D 22,48,65,2048
Maaa 92,60,12
W 7
Ra 152,27,45056
Maaa 100,60,16
Maaa 96,56,16
Maaa 228,52,16
D 23,48,65,3072
Ra 148,27,43008
Maaa 224,48,16
Ta 0,10,57344
Ta 2,10,58368
Maaa 164,44,16
Maaa 160,40,16
Ra 144,27,40960
Mvaa 196,36,16
buffer_load_dword v142, v144, s[8:11], s91 offen offset:0 sc1
Mvaa 192,32,16
W 10
Mvaa 200,32,20
Ra 140,27,38912
Mvaa 204,36,20
Maaa 168,40,20
Ta 4,11,57344
Ta 6,11,58368
Maaa 172,44,20
Ra 136,27,36864
Maaa 232,48,20
Maaa 236,52,20
v_bfe_u32 v29, v1, 7, 1
Maaa 104,56,20
Ra 132,27,34816
Maaa 108,60,20
W 13
v_mul_u32_u24 v28, 10240, v29
Maaa 116,60,24
Maaa 112,56,24
Ra 128,27,32768
v_lshrrev_b32 v29, 8, v1
Maaa 244,52,24
Ta 8,12,57344
Ta 10,12,58368
Maaa 240,48,24
v_lshl_add_u32 v28, v29, 7, v28
Maaa 180,44,24
v_and_b32 v29, 0x70, v1
Maaa 176,40,24
Mvaa 212,36,24
v_add_u32 v28, v28, v29
Mvaa 208,32,24
W 14
Mvaa 216,32,28
s_and_b32 s80, s14, 2
Mvaa 220,36,28
s_mul_i32 s80, s80, 20480
Maaa 184,40,28
Ta 12,13,57344
Ta 14,13,58368
Maaa 188,44,28
s_sub_u32 s81, 40960, s80
Maaa 248,48,28
Maaa 252,52,28
s_add_u32 s93, s28, 20480
Maaa 120,56,28
Maaa 124,60,28
W 10
v_add_u32 v26, s80, v28
Maaa 68,156,0
Maaa 64,152,0
Maaa 196,148,0
Ta 16,14,57344
v_add_u32 v27, s81, v28
Maaa 192,144,0
W 5
Ta 18,14,58368
Mvaa 228,140,0
buffer_load_dwordx4 a[32:35], v26, s[8:11], s28 offen offset:0 sc1
Mvaa 224,136,0
Mvaa 164,132,0
Mvaa 160,128,0
Mvaa 168,128,4
buffer_load_dwordx4 a[36:39], v26, s[8:11], s93 offen offset:0 sc1
Mvaa 172,132,4
Mvaa 232,136,4
Ta 20,15,57344
Ta 22,15,58368
Mvaa 236,140,4
Maaa 200,144,4
buffer_load_dwordx4 a[40:43], v27, s[8:11], s28 offen offset:0 sc1
Maaa 204,148,4
Maaa 72,152,4
Maaa 76,156,4
W 6
Maaa 84,156,8
buffer_load_dwordx4 a[44:47], v27, s[8:11], s93 offen offset:0 sc1
Ta 24,24,57344
Maaa 80,152,8
s_add_u32 s28, s28, 512
s_cmp_eq_u32 s28, s4
s_cselect_b32 s93, 10240, 0
Ta 26,24,58368
Maaa 212,148,8
s_sub_u32 s28, s28, s93
Ta 28,25,57344
Ta 30,25,58368
Maaa 208,144,8
s_add_u32 s93, s28, 20480
Mvaa 244,140,8
Mvaa 240,136,8
Mvaa 180,132,8
s_add_u32 s65, s5, 12
s_cmp_ge_u32 s65, 20
s_cselect_b32 s90, 20, 0
buffer_load_dwordx4 a[48:51], v26, s[8:11], s28 offen offset:0 sc1
Mvaa 176,128,8
W 8
s_sub_u32 s65, s65, s90
s_lshl_b32 s65, s65, 18
Mvaa 184,128,12
s_add_u32 m0, s63, 32768
Mvaa 188,132,12
Mvaa 248,136,12
s_waitcnt vmcnt(6) lgkmcnt(0)
s_barrier
Mvaa 252,140,12
buffer_load_dwordx4 a[52:55], v26, s[8:11], s93 offen offset:0 sc1
Maaa 216,144,12
Maaa 220,148,12
Maaa 88,152,12
Maaa 92,156,12
buffer_load_dwordx4 a[56:59], v27, s[8:11], s28 offen offset:0 sc1
Maaa 100,156,16
Maaa 96,152,16
Maaa 228,148,16
Maaa 224,144,16
buffer_load_dwordx4 a[60:63], v27, s[8:11], s93 offen offset:0 sc1
Maaa 164,140,16
Maaa 160,136,16
Mvaa 196,132,16
Mvaa 192,128,16
D 16,48,65,0
Mvaa 200,128,20
Mvaa 204,132,20
Maaa 168,136,20
Ta 0,2,0
Ta 2,2,1024
Maaa 172,140,20
D 17,48,65,1024
Maaa 232,144,20
Maaa 236,148,20
Maaa 104,152,20
Ta 4,2,8192
Ta 6,2,9216
Maaa 108,156,20
D 18,48,65,2048
Maaa 116,156,24
Maaa 112,152,24
Maaa 244,148,24
Ta 8,3,0
Ta 10,3,1024
Maaa 240,144,24
D 19,48,65,3072
Maaa 180,140,24
s_add_u32 m0, s63, 36864
Maaa 176,136,24
Mvaa 212,132,24
Ta 12,3,8192
Ta 14,3,9216
Mvaa 208,128,24
D 20,48,65,0
Mvaa 216,128,28
Mvaa 220,132,28
Maaa 184,136,28
Ta 16,4,0
Ta 18,4,1024
Maaa 188,140,28
Maaa 248,144,28
D 21,48,65,1024
Maaa 252,148,28
Maaa 120,152,28
Maaa 124,156,28
.Lq_7%=:
s_cmp_ge_u32 s14, 2
s_cbranch_scc1 .Lpb_27%=
s_waitcnt vmcnt(6) lgkmcnt(6)
Zvaa 32,0,32
s_add_u32 s28, s28, 512
Ta 20,4,8192
Ta 22,4,9216
Mvaa 32,4,48
s_cmp_eq_u32 s28, s4
s_cselect_b32 s93, 10240, 0
Ta 24,5,0
Ta 26,5,1024
Zvaa 64,4,52
s_sub_u32 s28, s28, s93
s_add_u32 s93, s28, 20480
buffer_load_dwordx4 a[128:131], v26, s[8:11], s28 offen offset:0 sc1
Mvaa 64,0,36
Ta 28,5,8192
Ta 30,5,9216
Zvaa 96,0,40
K 184,184,185
K 185,186,187
Mvaa 96,4,56
K 186,188,189
K 187,190,191
Zvaa 128,4,60
K 192,192,193
Mvaa 128,0,44
W 8
buffer_load_dwordx4 a[132:135], v26, s[8:11], s93 offen offset:0 sc1
K 193,194,195
Zvaa 132,8,44
Ta 0,6,0
Ta 2,6,1024
Mvaa 132,12,60
K 194,196,197
K 195,198,199
Zvaa 100,12,56
K 200,200,201
K 201,202,203
Mvaa 100,8,40
Ta 4,6,8192
Ta 6,6,9216
Zvaa 68,8,36
buffer_load_dwordx4 a[136:139], v27, s[8:11], s28 offen offset:0 sc1
K 202,204,205
Mvaa 68,12,52
K 203,206,207
K 208,208,209
Zvaa 36,12,48
K 209,210,211
K 210,212,213
Mvaa 36,8,32
W 8
Ta 8,7,0
K 211,214,215
Zvaa 40,16,32
Ta 10,7,1024
K 216,216,217
Mvaa 40,20,48
buffer_load_dwordx4 a[140:143], v27, s[8:11], s93 offen offset:0 sc1
K 217,218,219
Zvaa 72,20,52
s_add_u32 s28, s28, 512
s_cmp_eq_u32 s28, s4
s_cselect_b32 s93, 10240, 0
s_sub_u32 s28, s28, s93
Mvaa 72,16,36
s_add_u32 s93, s28, 20480
K 218,220,221
K 219,222,223
Zvaa 104,16,40
Ta 12,7,8192
Ta 14,7,9216
Mvaa 104,20,56
K 224,224,225
K 225,226,227
Zvaa 136,20,60
K 226,228,229
K 227,230,231
Mvaa 136,16,44
W 8
buffer_load_dwordx4 a[144:147], v26, s[8:11], s28 offen offset:0 sc1
K 232,232,233
Zvaa 140,24,44
K 233,234,235
K 234,236,237
Mvaa 140,28,60
Ta 16,8,0
K 235,238,239
Zvaa 108,28,56
Ta 18,8,1024
K 240,240,241
Mvaa 108,24,40
K 241,242,243
K 242,244,245
Zvaa 76,24,36
buffer_load_dwordx4 a[148:151], v26, s[8:11], s93 offen offset:0 sc1
K 243,246,247
Mvaa 76,28,52
Ta 20,8,8192
Ta 22,8,9216
Zvaa 44,28,48
K 248,248,249
K 249,250,251
Mvaa 44,24,32
K 250,252,253
)"
R"(K 251,254,255
W 8
Zvaa 48,0,32
Ta 24,9,0
Ta 26,9,1024
Mvaa 48,4,48
buffer_load_dwordx4 a[152:155], v27, s[8:11], s28 offen offset:0 sc1
K 160,160,161
Zvaa 80,4,52
Ta 28,9,8192
Ta 30,9,9216
Mvaa 80,0,36
K 161,162,163
Zvaa 112,0,40
K 162,164,165
Mvaa 112,4,56
Zvaa 144,4,60
buffer_load_dwordx4 a[156:159], v27, s[8:11], s93 offen offset:0 sc1
K 163,166,167
Mvaa 144,0,44
W 8
s_add_u32 s28, s28, 512
s_cmp_eq_u32 s28, s4
s_cselect_b32 s93, 10240, 0
Zvaa 148,8,44
s_sub_u32 s28, s28, s93
s_add_u32 s93, s28, 20480
Ta 0,2,16384
Mvaa 148,12,60
s_add_u32 s36, s29, 0
Ta 2,2,17408
Zvaa 116,12,56
Ta 4,2,24576
Ta 6,2,25600
Mvaa 116,8,40
buffer_load_dwordx4 v[188:191], v26, s[8:11], s28 offen offset:0 sc1
Zvaa 84,8,36
Mvaa 84,12,52
Zvaa 52,12,48
Mvaa 52,8,32
W 8
Ta 8,3,16384
Ta 10,3,17408
Zvaa 56,16,32
buffer_load_dwordx4 v[196:199], v26, s[8:11], s93 offen offset:0 sc1
Mvaa 56,20,48
Zvaa 88,20,52
Ta 12,3,24576
Ta 14,3,25600
Mvaa 88,16,36
Zvaa 120,16,40
buffer_store_dwordx4 v[160:163], v1, s[8:11], s36 offen offset:0 sc1
K 168,168,169
Mvaa 120,20,56
K 169,170,171
K 170,172,173
Zvaa 152,20,60
Mvaa 152,16,44
W 8
Ta 16,4,16384
K 171,174,175
Zvaa 156,24,44
Ta 18,4,17408
Mvaa 156,28,60
buffer_load_dwordx4 v[204:207], v27, s[8:11], s28 offen offset:0 sc1
Zvaa 124,28,56
Ta 20,4,24576
Ta 22,4,25600
Mvaa 124,24,40
Zvaa 92,24,36
Mvaa 92,28,52
Zvaa 60,28,48
buffer_load_dwordx4 v[212:215], v27, s[8:11], s93 offen offset:0 sc1
Mvaa 60,24,32
s_waitcnt vmcnt(5) lgkmcnt(8)
s_add_u32 s28, s28, 512
s_cmp_eq_u32 s28, s4
s_cselect_b32 s93, 10240, 0
Mvaa 32,0,128
s_sub_u32 s28, s28, s93
s_add_u32 s93, s28, 20480
Ta 24,5,16384
Mvaa 32,4,144
Ta 26,5,17408
Mvaa 64,4,148
Ta 28,5,24576
Ta 30,5,25600
Mvaa 64,0,132
buffer_store_dwordx4 v[168:171], v1, s[8:11], s36 offen offset:1024 sc1
K 176,176,177
Mvaa 96,0,136
K 177,178,179
K 178,180,181
Mvaa 96,4,152
Mvaa 128,4,156
Mvaa 128,0,140
W 8
Ta 0,6,16384
K 179,182,183
Mvaa 132,8,140
buffer_load_dwordx4 v[220:223], v26, s[8:11], s28 offen offset:0 sc1
Ta 2,6,17408
Mvaa 132,12,156
Mvaa 100,12,152
Ta 4,6,24576
Ta 6,6,25600
Mvaa 100,8,136
Mvaa 68,8,132
Mvaa 68,12,148
buffer_load_dwordx4 v[228:231], v26, s[8:11], s93 offen offset:0 sc1
Mvaa 36,12,144
Mvaa 36,8,128
W 8
Ta 8,7,16384
Ta 10,7,17408
Mvaa 40,16,128
Mvaa 40,20,144
Mvaa 72,20,148
buffer_load_dwordx4 v[236:239], v27, s[8:11], s28 offen offset:0 sc1
Ta 12,7,24576
Mvaa 72,16,132
buffer_store_dwordx4 v[176:179], v1, s[8:11], s36 offen offset:2048 sc1
Ta 14,7,25600
Mvaa 104,16,136
Mvaa 104,20,152
Mvaa 136,20,156
Mvaa 136,16,140
W 8
Ta 16,8,16384
Ta 18,8,17408
Mvaa 140,24,140
buffer_load_dwordx4 v[244:247], v27, s[8:11], s93 offen offset:0 sc1
Mvaa 140,28,156
s_add_u32 s28, s28, 512
s_cmp_eq_u32 s28, s4
s_cselect_b32 s93, 10240, 0
s_sub_u32 s28, s28, s93
Mvaa 108,28,152
s_add_u32 s93, s28, 20480
Ta 20,8,24576
Ta 22,8,25600
Mvaa 108,24,136
Mvaa 76,24,132
Mvaa 76,28,148
buffer_load_dwordx4 v[252:255], v26, s[8:11], s28 offen offset:0 sc1
Mvaa 44,28,144
Mvaa 44,24,128
W 8
Ta 24,9,16384
Ta 26,9,17408
Mvaa 48,0,128
Mvaa 48,4,144
Mvaa 80,4,148
D 22,48,65,2048
Ta 28,9,24576
Mvaa 80,0,132
buffer_store_dwordx4 v[184:187], v1, s[8:11], s36 offen offset:3072 sc1
Ta 30,9,25600
Mvaa 112,0,136
s_add_u32 s36, s29, 4096
s_lshl_b32 s80, s56, 9
Mvaa 112,4,152
Mvaa 144,4,156
s_lshl_b32 s81, s57, 4
Mvaa 144,0,140
W 8
s_add_u32 s80, s80, s81
Ta 0,10,0
Mvaa 148,8,140
buffer_load_dwordx4 a[32:35], v26, s[8:11], s93 offen offset:0 sc1
Ta 2,10,1024
Mvaa 148,12,156
s_and_b32 s81, s14, 1
Mvaa 116,12,152
s_lshl_b32 s81, s81, 3
Ta 4,10,8192
Ta 6,10,9216
Mvaa 116,8,136
Mvaa 84,8,132
s_add_u32 s80, s80, s81
Mvaa 84,12,148
s_add_u32 s71, s80, 4096
buffer_load_dwordx4 a[36:39], v27, s[8:11], s28 offen offset:0 sc1
Mvaa 52,12,144
Mvaa 52,8,128
W 8
Ta 8,11,0
Ta 10,11,1024
Mvaa 56,16,128
Mvaa 56,20,144
Mvaa 88,20,148
buffer_store_dwordx4 v[192:195], v1, s[8:11], s36 offen offset:0 sc1
Ta 12,11,8192
Mvaa 88,16,132
Ta 14,11,9216
Mvaa 120,16,136
Mvaa 120,20,152
Mvaa 152,20,156
Mvaa 152,16,140
W 8
buffer_load_dwordx4 a[40:43], v27, s[8:11], s93 offen offset:0 sc1
Ta 16,12,0
Mvaa 156,24,140
s_add_u32 s28, s28, 512
s_cmp_eq_u32 s28, s4
s_cselect_b32 s93, 10240, 0
s_sub_u32 s28, s28, s93
Mvaa 156,28,156
s_add_u32 s93, s28, 20480
Ta 18,12,1024
Mvaa 124,28,152
Ta 20,12,8192
Ta 22,12,9216
Mvaa 124,24,136
Mvaa 92,24,132
buffer_load_dwordx4 a[44:47], v26, s[8:11], s28 offen offset:0 sc1
Mvaa 92,28,148
Mvaa 60,28,144
Mvaa 60,24,128
s_waitcnt vmcnt(8) lgkmcnt(8)
Ta 24,13,0
Ta 26,13,1024
Mvav 32,0,188
Mvav 32,4,220
D 23,48,65,3072
Mvav 64,4,228
s_add_u32 s65, s5, 16
s_cmp_ge_u32 s65, 20
s_cselect_b32 s90, 20, 0
buffer_store_dwordx4 v[200:203], v1, s[8:11], s36 offen offset:1024 sc1
Mvav 64,0,196
s_sub_u32 s65, s65, s90
s_lshl_b32 s65, s65, 18
s_add_u32 m0, s63, 98304
Mvav 96,0,204
Ta 28,13,8192
Ta 30,13,9216
Mvav 96,4,236
Mvav 128,4,244
Mvav 128,0,212
W 8
buffer_load_dwordx4 a[48:51], v26, s[8:11], s93 offen offset:0 sc1
Ta 0,14,0
Mvav 132,8,212
Ta 2,14,1024
v_add_u32 v2, 65536, v10
Mvav 132,12,244
Mvav 100,12,236
Ta 4,14,8192
Ta 6,14,9216
Mvav 100,8,204
v_add_u32 v3, 65536, v11
Mvav 68,8,196
buffer_load_dwordx4 a[52:55], v27, s[8:11], s28 offen offset:0 sc1
Mvav 68,12,228
Mvav 36,12,220
Mvav 36,8,188
W 8
Ta 8,15,0
Ta 10,15,1024
Mvav 40,16,188
v_add_u32 v4, 65536, v12
Mvav 40,20,220
buffer_load_dwordx4 a[56:59], v27, s[8:11], s93 offen offset:0 sc1
Mvav 72,20,228
s_add_u32 s28, s28, 512
s_cmp_eq_u32 s28, s4
s_cselect_b32 s93, 10240, 0
buffer_store_dwordx4 v[208:211], v1, s[8:11], s36 offen offset:2048 sc1
Mvav 72,16,196
s_sub_u32 s28, s28, s93
s_add_u32 s93, s28, 20480
Ta 12,15,8192
Mvav 104,16,204
Ta 14,15,9216
v_add_u32 v5, 65536, v13
Mvav 104,20,236
Mvav 136,20,244
Mvav 136,16,212
W 8
D 16,48,65,0
Ta 16,24,0
Mvav 140,24,212
Ta 18,24,1024
v_add_u32 v6, 65536, v14
Mvav 140,28,244
Mvav 108,28,236
Ta 20,24,8192
Ta 22,24,9216
Mvav 108,24,204
v_add_u32 v7, 65536, v15
Mvav 76,24,196
buffer_load_dwordx4 a[60:63], v26, s[8:11], s28 offen offset:0 sc1
Mvav 76,28,228
Mvav 44,28,220
Mvav 44,24,188
W 8
Ta 24,25,0
Ta 26,25,1024
Mvav 48,0,188
v_add_u32 v8, 65536, v24
Mvav 48,4,220
buffer_load_dwordx4 v[184:187], v26, s[8:11], s93 offen offset:0 sc1
Mvav 80,4,228
buffer_store_dwordx4 v[216:219], v1, s[8:11], s36 offen offset:3072 sc1
Ta 28,25,8192
Mvav 80,0,196
s_add_u32 s36, s29, 458752
Ta 30,25,9216
v_add_u32 v9, 65536, v25
Mvav 112,0,204
Mvav 112,4,236
Mvav 144,4,244
Mvav 144,0,212
W 8
buffer_load_dwordx4 v[192:195], v27, s[8:11], s28 offen offset:0 sc1
Ta 0,10,16384
Mvav 148,8,212
Ta 2,10,17408
Mvav 148,12,244
Mvav 116,12,236
Ta 4,10,24576
Ta 6,10,25600
Mvav 116,8,204
Mvav 84,8,196
D 17,48,65,1024
Mvav 84,12,228
Mvav 52,12,220
Mvav 52,8,188
W 8
Ta 8,11,16384
Ta 10,11,17408
Mvav 56,16,188
Mvav 56,20,220
buffer_store_dwordx4 v[224:227], v1, s[8:11], s36 offen offset:0 sc1
Mvav 88,20,228
Ta 12,11,24576
Ta 14,11,25600
Mvav 88,16,196
Mvav 120,16,204
Mvav 120,20,236
Mvav 152,20,244
buffer_load_dwordx4 a[128:131], v27, s[8:11], s93 offen offset:0 sc1
Mvav 152,16,212
W 8
s_add_u32 s28, s28, 512
s_cmp_eq_u32 s28, s4
s_cselect_b32 s93, 10240, 0
Mvav 156,24,212
s_sub_u32 s28, s28, s93
s_add_u32 s93, s28, 20480
Ta 16,12,16384
Mvav 156,28,244
Ta 18,12,17408
Mvav 124,28,236
Ta 20,12,24576
Ta 22,12,25600
Mvav 124,24,204
buffer_load_dwordx4 a[132:135], v26, s[8:11], s28 offen offset:0 sc1
Mvav 92,24,196
Mvav 92,28,228
Mvav 60,28,220
Mvav 60,24,188
s_waitcnt vmcnt(10) lgkmcnt(8)
Ta 24,13,16384
Ta 26,13,17408
Mvav 32,0,252
buffer_load_dwordx4 a[136:139], v26, s[8:11], s93 offen offset:0 sc1
Mvaa 32,4,44
buffer_store_dwordx4 v[232:235], v1, s[8:11], s36 offen offset:1024 sc1
Mvaa 64,4,48
Ta 28,13,24576
Ta 30,13,25600
Mvaa 64,0,32
Mvaa 96,0,36
Mvaa 96,4,52
Mvaa 128,4,56
buffer_load_dwordx4 a[140:143], v27, s[8:11], s28 offen offset:0 sc1
Mvaa 128,0,40
W 8
Ta 0,14,16384
Ta 2,14,17408
Mvaa 132,8,40
Mvaa 132,12,56
Mvaa 100,12,52
Ta 4,14,24576
Ta 6,14,25600
Mvaa 100,8,36
D 18,48,65,2048
Mvaa 68,8,32
Mvaa 68,12,48
Mvaa 36,12,44
Mvav 36,8,252
W 8
Ta 8,15,16384
Ta 10,15,17408
Mvav 40,16,252
buffer_load_dwordx4 a[144:147], v27, s[8:11], s93 offen offset:0 sc1
Mvaa 40,20,44
s_add_u32 s28, s28, 512
)"
R"(s_cmp_eq_u32 s28, s4
s_cselect_b32 s93, 10240, 0
buffer_store_dwordx4 v[240:243], v1, s[8:11], s36 offen offset:2048 sc1
Mvaa 72,20,48
s_sub_u32 s28, s28, s93
s_add_u32 s93, s28, 20480
Ta 12,15,24576
Mvaa 72,16,32
Ta 14,15,25600
Mvaa 104,16,36
Mvaa 104,20,52
Mvaa 136,20,56
buffer_load_dwordx4 a[148:151], v26, s[8:11], s28 offen offset:0 sc1
Mvaa 136,16,40
W 8
Ta 16,24,16384
Ta 18,24,17408
Mvaa 140,24,40
Mvaa 140,28,56
Mvaa 108,28,52
Ta 20,24,24576
Ta 22,24,25600
Mvaa 108,24,36
buffer_load_dwordx4 a[152:155], v26, s[8:11], s93 offen offset:0 sc1
Mvaa 76,24,32
Mvaa 76,28,48
Mvaa 44,28,44
Mvav 44,24,252
W 8
Ta 24,25,16384
Ta 26,25,17408
Mvav 48,0,252
buffer_load_dwordx4 a[156:159], v27, s[8:11], s28 offen offset:0 sc1
Mvaa 48,4,44
buffer_store_dwordx4 v[248:251], v1, s[8:11], s36 offen offset:3072 sc1
G 160,160
Mvaa 80,4,48
G 161,161
G 162,162
Mvaa 80,0,32
G 163,163
G 164,164
Mvaa 112,0,36
G 165,165
G 166,166
Mvaa 112,4,52
Ta 28,25,24576
Ta 30,25,25600
Mvaa 144,4,56
D 19,48,65,3072
G 167,167
Mvaa 144,0,40
W 8
s_add_u32 m0, s63, 102400
K 160,160,161
Mvaa 148,8,40
K 161,162,163
K 162,164,165
Mvaa 148,12,56
Ta 0,2,0
K 163,166,167
Mvaa 116,12,52
s_add_u32 s36, s29, 462848
Ta 2,2,1024
Ta 4,2,8192
Mvaa 116,8,36
buffer_load_dwordx4 v[200:203], v27, s[8:11], s93 offen offset:0 sc1
Ta 6,2,9216
Mvaa 84,8,32
s_add_u32 s28, s28, 512
s_cmp_eq_u32 s28, s4
s_cselect_b32 s93, 10240, 0
s_sub_u32 s28, s28, s93
Mvaa 84,12,48
s_add_u32 s93, s28, 20480
Mvaa 52,12,44
Mvav 52,8,252
W 8
Ta 8,3,0
Ta 10,3,1024
Mvav 56,16,252
buffer_store_dwordx4 v[160:163], v1, s[8:11], s36 offen offset:0 sc1
G 168,168
Mvaa 56,20,44
G 169,169
G 170,170
Mvaa 88,20,48
G 171,171
G 172,172
Mvaa 88,16,32
G 173,173
G 174,174
Mvaa 120,16,36
Ta 12,3,8192
Ta 14,3,9216
Mvaa 120,20,52
buffer_load_dwordx4 v[208:211], v26, s[8:11], s28 offen offset:0 sc1
G 175,175
Mvaa 152,20,56
K 168,168,169
K 169,170,171
Mvaa 152,16,40
W 8
K 170,172,173
K 171,174,175
Mvaa 156,24,40
Ta 16,4,0
Ta 18,4,1024
Mvaa 156,28,56
Mvaa 124,28,52
buffer_load_dwordx4 v[216:219], v26, s[8:11], s93 offen offset:0 sc1
Ta 20,4,8192
Mvaa 124,24,36
Ta 22,4,9216
Mvaa 92,24,32
Mvaa 92,28,48
Mvaa 60,28,44
Mvav 60,24,252
s_waitcnt vmcnt(10) lgkmcnt(8)
D 20,48,65,0
Ta 24,5,0
Mvaa 32,0,60
buffer_store_dwordx4 v[168:171], v1, s[8:11], s36 offen offset:1024 sc1
G 176,176
Mvaa 32,4,132
G 177,177
G 178,178
Mvaa 64,4,136
Ta 26,5,1024
G 179,179
Mvav 64,0,184
G 180,180
G 181,181
Mvav 96,0,192
Ta 28,5,8192
G 182,182
Mvaa 96,4,140
buffer_load_dwordx4 v[224:227], v27, s[8:11], s28 offen offset:0 sc1
Ta 30,5,9216
Mvaa 128,4,144
G 183,183
K 176,176,177
Mvaa 128,0,128
W 8
K 177,178,179
K 178,180,181
Mvaa 132,8,128
Ta 0,6,0
K 179,182,183
Mvaa 132,12,144
Ta 2,6,1024
Mvaa 100,12,140
buffer_load_dwordx4 v[188:191], v27, s[8:11], s93 offen offset:0 sc1
Ta 4,6,8192
Mvav 100,8,192
s_add_u32 s28, s28, 512
s_cmp_eq_u32 s28, s4
s_cselect_b32 s93, 10240, 0
s_sub_u32 s28, s28, s93
Mvav 68,8,184
s_add_u32 s93, s28, 20480
Ta 6,6,9216
Mvaa 68,12,136
Mvaa 36,12,132
Mvaa 36,8,60
W 8
buffer_load_dwordx4 v[196:199], v26, s[8:11], s28 offen offset:0 sc1
Ta 8,7,0
Mvaa 40,16,60
buffer_store_dwordx4 v[176:179], v1, s[8:11], s36 offen offset:2048 sc1
G 160,184
Mvaa 40,20,132
G 161,185
G 162,186
Mvaa 72,20,136
Ta 10,7,1024
G 163,187
Mvav 72,16,184
G 164,188
G 165,189
Mvav 104,16,192
Ta 12,7,8192
G 166,190
Mvaa 104,20,140
buffer_load_dwordx4 v[204:207], v26, s[8:11], s93 offen offset:0 sc1
Ta 14,7,9216
Mvaa 136,20,144
G 167,191
K 160,160,161
Mvaa 136,16,128
W 8
K 161,162,163
K 162,164,165
Mvaa 140,24,128
Ta 16,8,0
K 163,166,167
Mvaa 140,28,144
Ta 18,8,1024
Mvaa 108,28,140
D 21,48,65,1024
Ta 20,8,8192
Mvav 108,24,192
Ta 22,8,9216
Mvav 76,24,184
Mvaa 76,28,136
Mvaa 44,28,132
Mvaa 44,24,60
W 8
buffer_load_dwordx4 v[212:215], v27, s[8:11], s28 offen offset:0 sc1
Ta 24,9,0
Mvaa 48,0,60
buffer_store_dwordx4 v[160:163], v1, s[8:11], s36 offen offset:3072 sc1
G 168,192
Mvaa 48,4,132
G 169,193
G 170,194
Mvaa 80,4,136
G 171,195
G 172,196
Mvav 80,0,184
Ta 26,9,1024
G 173,197
Mvav 112,0,192
G 174,198
G 175,199
Mvaa 112,4,140
buffer_load_dwordx4 v[220:223], v27, s[8:11], s93 offen offset:0 sc1
Ta 28,9,8192
Mvaa 144,4,144
s_add_u32 s28, s28, 512
s_cmp_eq_u32 s28, s4
s_cselect_b32 s93, 10240, 0
s_sub_u32 s28, s28, s93
Mvaa 144,0,128
W 7
s_add_u32 s93, s28, 20480
Ta 30,9,9216
Mvaa 148,8,128
K 168,168,169
K 169,170,171
Mvaa 148,12,144
K 170,172,173
K 171,174,175
Mvaa 116,12,140
buffer_load_dwordx4 v[228:231], v26, s[8:11], s28 offen offset:0 sc1
Ta 0,2,16384
Mvav 116,8,192
Ta 2,2,17408
v_accvgpr_write_b32 a192, v168
Mvav 84,8,184
v_accvgpr_write_b32 a193, v169
v_accvgpr_write_b32 a194, v170
Mvaa 84,12,136
Ta 4,2,24576
Ta 6,2,25600
Mvaa 52,12,132
v_accvgpr_write_b32 a195, v171
G 176,64
Mvaa 52,8,60
W 8
D 22,48,65,2048
G 177,65
Mvaa 56,16,60
D 23,48,65,3072
G 178,66
Mvaa 56,20,132
Ta 8,3,16384
Ta 10,3,17408
Mvaa 88,20,136
G 179,67
G 180,68
Mvav 88,16,184
Ta 12,3,24576
Ta 14,3,25600
Mvav 120,16,192
G 181,69
G 182,70
Mvaa 120,20,140
buffer_load_dwordx4 v[236:239], v26, s[8:11], s93 offen offset:0 sc1
G 183,71
Mvaa 152,20,144
v_lshrrev_b32 v20, 4, v1
K 176,176,177
Mvaa 152,16,128
W 8
Ta 16,4,16384
v_add_u32 v20, 0, v20
Mvaa 156,24,128
Ta 18,4,17408
K 177,178,179
Mvaa 156,28,144
v_mul_u32_u24 v22, 205, v20
K 178,180,181
Mvaa 124,28,140
buffer_load_dwordx4 v[232:235], v27, s[8:11], s28 offen offset:0 sc1
K 179,182,183
Mvav 124,24,192
Ta 20,4,24576
Ta 22,4,25600
Mvav 92,24,184
v_lshrrev_b32 v22, 12, v22
v_accvgpr_write_b32 a196, v176
Mvaa 92,28,136
v_mul_u32_u24 v23, 20, v22
v_accvgpr_write_b32 a197, v177
Mvaa 60,28,132
v_accvgpr_write_b32 a198, v178
v_sub_u32 v20, v20, v23
Mvaa 60,24,60
s_waitcnt vmcnt(12) lgkmcnt(8)
buffer_load_dwordx4 a[32:35], v27, s[8:11], s93 offen offset:0 sc1
v_accvgpr_write_b32 a199, v179
Mvaa 32,0,148
s_add_u32 s28, s28, 512
s_cmp_eq_u32 s28, s4
s_cselect_b32 s93, 10240, 0
s_sub_u32 s28, s28, s93
Mvav 32,4,208
s_add_u32 s93, s28, 20480
Ta 24,5,16384
Ta 26,5,17408
Mvav 64,4,216
v_lshrrev_b32 v23, 1, v22
G 168,200
Mvaa 64,0,152
v_and_b32 v23, 2, v23
G 169,201
Mvaa 96,0,156
buffer_load_dwordx4 a[36:39], v26, s[8:11], s28 offen offset:0 sc1
Ta 28,5,24576
Mvav 96,4,224
Ta 30,5,25600
G 170,202
Mvav 128,4,188
v_xor_b32 v20, v20, v23
G 171,203
Mvav 128,0,200
W 8
v_mul_u32_u24 v22, 80, v22
G 172,204
Mvav 132,8,200
v_add_lshl_u32 v16, v20, v22, 4
G 173,205
Mvav 132,12,188
buffer_load_dwordx4 a[40:43], v26, s[8:11], s93 offen offset:0 sc1
Ta 0,6,16384
Mvav 100,12,224
Ta 2,6,17408
G 174,206
Mvaa 100,8,156
v_lshrrev_b32 v20, 4, v1
G 175,207
Mvaa 68,8,152
Ta 4,6,24576
v_add_u32 v20, 64, v20
Mvav 68,12,216
Ta 6,6,25600
K 168,168,169
Mvav 36,12,208
buffer_load_dwordx4 a[44:47], v27, s[8:11], s28 offen offset:0 sc1
K 169,170,171
Mvaa 36,8,148
W 8
v_mul_u32_u24 v22, 205, v20
K 170,172,173
Mvaa 40,16,148
v_lshrrev_b32 v22, 12, v22
K 171,174,175
Mvav 40,20,208
Ta 8,7,16384
v_mul_u32_u24 v23, 20, v22
Mvav 72,20,216
Ta 10,7,17408
v_accvgpr_write_b32 a200, v168
Mvaa 72,16,152
buffer_load_dwordx4 a[48:51], v27, s[8:11], s93 offen offset:0 sc1
v_accvgpr_write_b32 a201, v169
Mvaa 104,16,156
s_add_u32 s28, s28, 512
s_cmp_eq_u32 s28, s4
s_cselect_b32 s93, 10240, 0
s_sub_u32 s28, s28, s93
Mvav 104,20,224
s_add_u32 s93, s28, 20480
v_sub_u32 v20, v20, v23
v_accvgpr_write_b32 a202, v170
Mvav 136,20,188
Ta 12,7,24576
Ta 14,7,25600
Mvav 136,16,200
W 8
v_lshrrev_b32 v23, 1, v22
v_accvgpr_write_b32 a203, v171
Mvav 140,24,200
buffer_load_dwordx4 a[52:55], v26, s[8:11], s28 offen offset:0 sc1
v_and_b32 v23, 2, v23
Mvav 140,28,188
G 176,72
G 177,73
Mvav 108,28,224
)"
R"(v_xor_b32 v20, v20, v23
G 178,74
Mvaa 108,24,156
Ta 16,8,16384
Ta 18,8,17408
Mvaa 76,24,152
v_mul_u32_u24 v22, 80, v22
G 179,75
Mvav 76,28,216
buffer_load_dwordx4 a[56:59], v26, s[8:11], s93 offen offset:0 sc1
G 180,76
Mvav 44,28,208
Ta 20,8,24576
v_add_lshl_u32 v17, v20, v22, 4
Mvaa 44,24,148
W 7
Ta 22,8,25600
G 181,77
Mvaa 48,0,148
v_subrev_u32 v17, 1024, v17
G 182,78
Mvav 48,4,208
v_lshrrev_b32 v20, 4, v1
G 183,79
Mvav 80,4,216
buffer_load_dwordx4 a[184:187], v27, s[8:11], s28 offen offset:0 sc1
K 176,176,177
Mvaa 80,0,152
Ta 24,9,16384
v_add_u32 v20, 128, v20
Mvaa 112,0,156
Ta 26,9,17408
K 177,178,179
Mvav 112,4,224
v_mul_u32_u24 v22, 205, v20
K 178,180,181
Mvav 144,4,188
v_lshrrev_b32 v22, 12, v22
K 179,182,183
Mvav 144,0,200
W 6
buffer_load_dwordx4 a[188:191], v27, s[8:11], s93 offen offset:0 sc1
Ta 28,9,24576
Mvav 148,8,200
s_add_u32 s28, s28, 512
s_cmp_eq_u32 s28, s4
s_cselect_b32 s93, 10240, 0
s_sub_u32 s28, s28, s93
Mvav 148,12,188
s_add_u32 s93, s28, 20480
Ta 30,9,25600
v_accvgpr_write_b32 a204, v176
Mvav 116,12,224
v_mul_u32_u24 v23, 20, v22
v_accvgpr_write_b32 a205, v177
Mvaa 116,8,156
s_waitcnt vmcnt(15)
v_mov_b64 v[28:29], s[12:13]
s_mov_b64 exec, 1
buffer_store_dwordx2 v[28:29], off, s[8:11], s71 sc1
s_mov_b64 exec, -1
v_sub_u32 v20, v20, v23
v_accvgpr_write_b32 a206, v178
Mvaa 84,8,152
s_waitcnt vmcnt(12) lgkmcnt(0)
s_barrier
Mvav 84,12,216
v_accvgpr_write_b32 a207, v179
v_lshrrev_b32 v23, 1, v22
Mvav 52,12,208
G 168,208
v_and_b32 v23, 2, v23
Mvaa 52,8,148
G 169,209
v_xor_b32 v20, v20, v23
Mvaa 56,16,148
buffer_load_dwordx4 a[60:63], v26, s[8:11], s28 offen offset:0 sc1
G 170,210
Mvav 56,20,208
Ta 0,10,32768
G 171,211
Mvav 88,20,216
Ta 2,10,33792
Ta 4,10,40960
Mvaa 88,16,152
Ta 6,10,41984
v_mul_u32_u24 v22, 80, v22
Mvaa 120,16,156
G 172,212
v_add_lshl_u32 v18, v20, v22, 4
Mvav 120,20,224
buffer_load_dwordx4 v[184:187], v26, s[8:11], s93 offen offset:0 sc1
G 173,213
Mvav 152,20,188
Ta 8,11,32768
Ta 10,11,33792
Mvav 152,16,200
G 174,214
v_subrev_u32 v18, 2048, v18
Mvav 156,24,200
G 175,215
v_lshrrev_b32 v20, 4, v1
Mvav 156,28,188
Ta 12,11,40960
Ta 14,11,41984
Mvav 124,28,224
buffer_load_dwordx4 v[192:195], v27, s[8:11], s28 offen offset:0 sc1
K 168,168,169
Mvaa 124,24,156
v_add_u32 v20, 192, v20
K 169,170,171
Mvaa 92,24,152
K 170,172,173
v_mul_u32_u24 v22, 205, v20
Mvav 92,28,216
K 171,174,175
v_lshrrev_b32 v22, 12, v22
Mvav 60,28,208
Ta 16,12,32768
Ta 18,12,33792
Mvaa 60,24,148
s_waitcnt vmcnt(12) lgkmcnt(6)
buffer_load_dwordx4 a[128:131], v27, s[8:11], s93 offen offset:0 sc1
v_accvgpr_write_b32 a208, v168
Mvav 32,0,196
s_add_u32 s28, s28, 512
v_mul_u32_u24 v23, 20, v22
v_accvgpr_write_b32 a209, v169
Mvav 32,4,228
s_cmp_eq_u32 s28, s4
s_cselect_b32 s93, 10240, 0
v_accvgpr_write_b32 a210, v170
v_sub_u32 v20, v20, v23
Mvav 64,4,236
s_sub_u32 s28, s28, s93
Ta 20,12,40960
Ta 22,12,41984
Mvav 64,0,204
s_add_u32 s93, s28, 20480
v_accvgpr_write_b32 a211, v171
v_lshrrev_b32 v23, 1, v22
Mvav 96,0,212
G 176,80
G 177,81
Mvav 96,4,232
v_and_b32 v23, 2, v23
G 178,82
Mvaa 128,4,32
Ta 24,13,32768
v_xor_b32 v20, v20, v23
Mvav 128,0,220
W 5
Ta 26,13,33792
G 179,83
Mvav 132,8,220
buffer_load_dwordx4 a[132:135], v26, s[8:11], s28 offen offset:0 sc1
v_mul_u32_u24 v22, 80, v22
Mvaa 132,12,32
Ta 28,13,40960
G 180,84
Mvav 100,12,232
Ta 30,13,41984
G 181,85
Mvav 100,8,212
v_add_lshl_u32 v19, v20, v22, 4
G 182,86
Mvav 68,8,204
v_subrev_u32 v19, 3072, v19
G 183,87
Mvav 68,12,236
v_lshrrev_b32 v20, 4, v1
K 176,176,177
Mvav 36,12,228
Ta 0,14,32768
Ta 2,14,33792
Mvav 36,8,196
W 6
K 177,178,179
v_add_u32 v20, 256, v20
Mvav 40,16,196
buffer_load_dwordx4 a[136:139], v26, s[8:11], s93 offen offset:0 sc1
K 178,180,181
Mvav 40,20,228
v_mul_u32_u24 v22, 205, v20
K 179,182,183
Mvav 72,20,236
Ta 4,14,40960
Ta 6,14,41984
Mvav 72,16,204
v_accvgpr_write_b32 a212, v176
v_lshrrev_b32 v22, 12, v22
Mvav 104,16,212
v_accvgpr_write_b32 a213, v177
v_mul_u32_u24 v23, 20, v22
Mvav 104,20,232
v_accvgpr_write_b32 a214, v178
v_sub_u32 v20, v20, v23
Mvaa 136,20,32
Ta 8,15,32768
v_accvgpr_write_b32 a215, v179
Mvav 136,16,220
W 5
Ta 10,15,33792
buffer_load_dwordx4 a[140:143], v27, s[8:11], s28 offen offset:0 sc1
Mvav 140,24,220
G 168,216
v_lshrrev_b32 v23, 1, v22
Mvaa 140,28,32
G 169,217
v_and_b32 v23, 2, v23
Mvav 108,28,232
Ta 12,15,40960
Ta 14,15,41984
Mvav 108,24,212
G 170,218
v_xor_b32 v20, v20, v23
Mvav 76,24,204
G 171,219
G 172,220
Mvav 76,28,236
v_mul_u32_u24 v22, 80, v22
G 173,221
Mvav 44,28,228
Ta 16,24,32768
Ta 18,24,33792
Mvav 44,24,196
W 6
v_add_lshl_u32 v20, v20, v22, 4
G 174,222
Mvav 48,0,196
buffer_load_dwordx4 a[144:147], v27, s[8:11], s93 offen offset:0 sc1
G 175,223
Mvav 48,4,228
s_add_u32 s28, s28, 512
Ta 20,24,40960
v_lshrrev_b32 v22, 6, v1
Mvav 80,4,236
s_cmp_eq_u32 s28, s4
s_cselect_b32 s93, 10240, 0
Ta 22,24,41984
K 168,168,169
Mvav 80,0,204
s_sub_u32 s28, s28, s93
v_mul_u32_u24 v22, 320, v22
K 169,170,171
Mvav 112,0,212
s_add_u32 s93, s28, 20480
v_bfe_u32 v23, v1, 4, 2
K 170,172,173
Mvav 112,4,232
K 171,174,175
v_lshl_add_u32 v22, v23, 3, v22
Mvaa 144,4,32
Ta 24,25,32768
Ta 26,25,33792
Mvav 144,0,220
W 6
v_accvgpr_write_b32 a216, v168
v_bfe_u32 v23, v1, 8, 1
Mvav 148,8,220
buffer_load_dwordx4 v[200:203], v26, s[8:11], s28 offen offset:0 sc1
v_accvgpr_write_b32 a217, v169
Mvaa 148,12,32
v_lshlrev_b32 v23, 5, v23
v_accvgpr_write_b32 a218, v170
Mvav 116,12,232
Ta 28,25,40960
Ta 30,25,41984
Mvav 116,8,212
v_accvgpr_write_b32 a219, v171
v_add_u32 v21, v22, v23
Mvav 84,8,204
G 176,88
v_sub_u32 v22, v22, v23
Mvav 84,12,236
s_mul_i32 s94, s59, 655360
G 177,89
G 178,90
Mvav 52,12,228
s_mul_i32 s95, s58, 320
Ta 0,10,49152
Ta 2,10,50176
Mvav 52,8,196
W 6
buffer_load_dwordx4 v[208:211], v26, s[8:11], s93 offen offset:0 sc1
G 179,91
Mvav 56,16,196
s_add_u32 s94, s94, s95
Ta 4,10,57344
G 180,92
Mvav 56,20,228
Ta 6,10,58368
G 181,93
Mvav 88,20,236
s_add_u32 s40, s26, s94
s_addc_u32 s41, s27, 0
G 182,94
Mvav 88,16,204
s_and_b32 s41, s41, 0xffff
G 183,95
K 176,176,177
Mvav 120,16,212
s_mov_b32 s42, 5242880
Ta 8,11,49152
Ta 10,11,50176
Mvav 120,20,232
s_mov_b32 s43, 0x20000
buffer_load_dwordx4 v[216:219], v27, s[8:11], s28 offen offset:0 sc1
K 177,178,179
Mvaa 152,20,32
s_sub_u32 s42, s42, s94
K 178,180,181
K 179,182,183
Mvav 152,16,220
W 6
Ta 12,11,57344
Ta 14,11,58368
Mvav 156,24,220
s_mov_b64 s[44:45], s[8:9]
v_accvgpr_write_b32 a220, v176
v_accvgpr_write_b32 a221, v177
Mvaa 156,28,32
s_mov_b32 s47, 0x20000
v_accvgpr_write_b32 a222, v178
v_accvgpr_write_b32 a223, v179
Mvav 124,28,232
s_and_b32 s48, s14, 1
Ta 16,12,49152
Ta 18,12,50176
Mvav 124,24,212
buffer_load_dwordx4 v[224:227], v27, s[8:11], s93 offen offset:0 sc1
G 168,224
Mvav 92,24,204
s_mul_i32 s48, s48, 5120
Ta 20,12,57344
G 169,225
Mvav 92,28,236
s_lshr_b32 s51, s14, 1
Ta 22,12,58368
G 170,226
Mvav 60,28,228
G 171,227
G 172,228
Mvav 60,24,196
s_waitcnt vmcnt(13) lgkmcnt(8)
s_and_b32 s94, s14, 1
G 173,229
Mvaa 32,0,36
s_add_u32 s28, s28, 512
Ta 24,13,49152
Ta 26,13,50176
Mvaa 32,4,52
s_cmp_eq_u32 s28, s4
s_cselect_b32 s93, 10240, 0
s_sub_u32 s28, s28, s93
G 174,230
Mvaa 64,4,56
s_add_u32 s93, s28, 20480
s_lshl_b32 s95, s57, 1
buffer_load_dwordx4 v[188:191], v26, s[8:11], s28 offen offset:0 sc1
Mvaa 64,0,40
s_add_u32 s94, s94, s95
Ta 28,13,57344
G 175,231
Mvaa 96,0,44
s_lshl_b32 s54, s94, 3
Ta 30,13,58368
K 168,168,169
Mvaa 96,4,184
K 169,170,171
K 170,172,173
Mvaa 128,4,188
s_add_u32 s54, s54, s59
K 171,174,175
Mvaa 128,0,48
W 8
Ta 0,14,49152
Ta 2,14,50176
Mvaa 132,8,48
s_lshl_b32 s54, s54, 6
buffer_load_dwordx4 v[196:199], v26, s[8:11], s93 offen offset:0 sc1
v_accvgpr_write_b32 a224, v168
Mvaa 132,12,188
s_xor_b32 s55, s54, 15872
v_accvgpr_write_b32 a225, v169
Mvaa 100,12,184
Ta 4,14,57344
v_accvgpr_write_b32 a226, v170
Mvaa 100,8,44
s_add_u32 s54, s54, 20480
Ta 6,14,58368
v_accvgpr_write_b32 a227, v171
Mvaa 68,8,40
s_and_b32 s81, s14, 1
G 176,96
Mvaa 68,12,56
s_add_u32 s55, s55, 20480
Mvaa 36,12,52
G 177,97
Mvaa 36,8,36
)"
R"(W 8
s_and_b32 s95, s14, 2
G 178,98
Mvaa 40,16,36
s_mul_i32 s91, s81, 17164288
Ta 8,15,49152
Ta 10,15,50176
Mvaa 40,20,52
s_lshl_b32 s94, s94, 2
buffer_load_dwordx4 v[204:207], v27, s[8:11], s28 offen offset:0 sc1
G 179,99
Mvaa 72,20,56
s_mul_i32 s82, s56, 536576
s_add_u32 s94, s94, s95
G 180,100
Mvaa 72,16,40
Ta 12,15,57344
Ta 14,15,58368
Mvaa 104,16,44
s_lshl_b32 s95, s95, 4
G 181,101
G 182,102
Mvaa 104,20,184
s_add_u32 s91, s91, s82
G 183,103
Mvaa 136,20,188
s_add_u32 s55, s55, s95
s_add_u32 s91, s91, 4194304
K 176,176,177
Mvaa 136,16,48
W 8
s_lshl_b32 s49, s94, 17
Ta 16,24,49152
Mvaa 140,24,48
Ta 18,24,50176
K 177,178,179
Mvaa 140,28,188
s_lshl_b32 s95, s59, 14
buffer_load_dwordx4 v[212:215], v27, s[8:11], s93 offen offset:0 sc1
K 178,180,181
Mvaa 108,28,184
s_lshr_b32 s82, s14, 1
Ta 20,24,57344
K 179,182,183
Mvaa 108,24,44
s_add_u32 s49, s49, s95
Ta 22,24,58368
v_accvgpr_write_b32 a228, v176
Mvaa 76,24,40
s_lshl_b32 s83, s82, 14
s_xor_b32 s50, s49, 16252928
Mvaa 76,28,56
v_accvgpr_write_b32 a229, v177
Mvaa 44,28,52
s_add_u32 s91, s91, s83
s_add_u32 s49, s49, 39845888
v_accvgpr_write_b32 a230, v178
Mvaa 44,24,36
W 8
Ta 24,25,49152
v_accvgpr_write_b32 a231, v179
Mvaa 48,0,36
s_add_u32 s50, s50, 39845888
Ta 26,25,50176
G 168,232
Mvaa 48,4,52
s_lshl_b32 s81, s81, 14
Mvaa 80,4,56
s_lshr_b32 s94, s15, 3
Ta 28,25,57344
G 169,233
Mvaa 80,0,40
s_lshl_b32 s82, s82, 16
Ta 30,25,58368
G 170,234
Mvaa 112,0,44
s_lshl_b32 s94, s94, 3
G 171,235
Mvaa 112,4,184
s_add_u32 s94, s94, s14
s_or_b32 s92, s81, s82
G 172,236
Mvaa 144,4,188
Mvaa 144,0,48
W 8
s_lshl_b32 s94, s94, 4
G 173,237
Mvaa 148,8,48
s_xor_b32 s92, s92, 0x10000
Ta 0,2,32768
Ta 2,2,33792
Mvaa 148,12,188
s_add_u32 s94, s94, s59
G 174,238
G 175,239
Mvaa 116,12,184
s_mul_i32 s52, s94, 5120
Ta 4,2,40960
Ta 6,2,41984
Mvaa 116,8,44
K 168,168,169
Mvaa 84,8,40
s_lshl_b32 s95, s59, 12
K 169,170,171
Mvaa 84,12,56
K 170,172,173
Mvaa 52,12,52
s_sub_u32 s52, s52, s95
K 171,174,175
Mvaa 52,8,36
W 8
s_add_u32 s52, s52, 56623104
Ta 8,3,32768
Mvaa 56,16,36
Ta 10,3,33792
v_accvgpr_write_b32 a232, v168
Mvaa 56,20,52
s_lshl_b32 s53, s94, 3
v_accvgpr_write_b32 a233, v169
Mvaa 88,20,56
Ta 12,3,40960
v_accvgpr_write_b32 a234, v170
Mvaa 88,16,40
s_add_u32 s53, s53, 65536
Ta 14,3,41984
v_accvgpr_write_b32 a235, v171
Mvaa 120,16,44
G 176,104
v_bfe_u32 v244, v1, 4, 1
Mvaa 120,20,184
Mvaa 152,20,188
G 177,105
v_mul_u32_u24 v244, 0xaaaa, v244
Mvaa 152,16,48
W 8
G 178,106
v_add_u32 v244, 0x4444, v244
Mvaa 156,24,48
Ta 16,4,32768
Ta 18,4,33792
Mvaa 156,28,188
G 179,107
v_lshl_or_b32 v240, v244, 16, v244
Mvaa 124,28,184
G 180,108
v_mov_b32 v241, 0xff800000
Mvaa 124,24,44
Ta 20,4,40960
Ta 22,4,41984
Mvaa 92,24,40
G 181,109
v_add_u32 v242, 0x10000, v1
Mvaa 92,28,56
G 182,110
v_bfe_u32 v244, v1, 4, 4
Mvaa 60,28,52
G 183,111
v_lshrrev_b32 v245, 8, v1
Mvaa 60,24,36
s_waitcnt vmcnt(8) lgkmcnt(8)
K 176,176,177
v_lshlrev_b32 v245, 2, v245
Mvaa 32,0,60
Ta 24,5,32768
Ta 26,5,33792
Mvaa 32,4,132
K 177,178,179
v_sub_u32 v244, v244, v245
Mvaa 64,4,136
K 178,180,181
v_cmp_le_i32_e64 s[72:73], 0, v244
Mvav 64,0,184
Ta 28,5,40960
K 179,182,183
Mvav 96,0,192
Ta 30,5,41984
v_accvgpr_write_b32 a236, v176
Mvaa 96,4,140
v_cmp_le_i32_e64 s[74:75], 1, v244
v_accvgpr_write_b32 a237, v177
Mvaa 128,4,144
v_cmp_le_i32_e64 s[76:77], 2, v244
v_accvgpr_write_b32 a238, v178
Mvaa 128,0,128
W 8
v_cmp_le_i32_e64 s[78:79], 3, v244
v_accvgpr_write_b32 a239, v179
Mvaa 132,8,128
Ta 0,6,32768
Ta 2,6,33792
Mvaa 132,12,144
v_add_u32 v243, 4096, v1
G 168,240
Mvaa 100,12,140
v_add_u32 v244, 8192, v1
G 169,241
Mvav 100,8,192
Ta 4,6,40960
v_add_u32 v245, 12288, v1
Mvav 68,8,184
s_mov_b32 s18, 0x3f803f80
Ta 6,6,41984
G 170,242
Mvaa 68,12,136
s_mov_b32 s19, 0x3f003f00
G 171,243
G 172,244
Mvaa 36,12,132
Mvaa 36,8,60
W 8
v_accvgpr_write_b32 a176, s18
G 173,245
Mvaa 40,16,60
Ta 8,7,32768
Ta 10,7,33792
Mvaa 40,20,132
v_accvgpr_write_b32 a180, s19
G 174,246
Mvaa 72,20,136
G 175,247
v_mov_b32 v252, v241
Mvav 72,16,184
Ta 12,7,40960
Ta 14,7,41984
Mvav 104,16,192
K 168,168,169
v_accvgpr_write_b32 a177, s18
Mvaa 104,20,140
K 169,170,171
v_accvgpr_write_b32 a181, s19
Mvaa 136,20,144
K 170,172,173
v_mov_b32 v253, v241
Mvaa 136,16,128
W 8
K 171,174,175
v_accvgpr_write_b32 a178, s18
Mvaa 140,24,128
Ta 16,8,32768
Ta 18,8,33792
Mvaa 140,28,144
v_accvgpr_write_b32 a240, v168
v_accvgpr_write_b32 a182, s19
Mvaa 108,28,140
v_accvgpr_write_b32 a241, v169
v_mov_b32 v254, v241
Mvav 108,24,192
Ta 20,8,40960
v_accvgpr_write_b32 a242, v170
Mvav 76,24,184
Ta 22,8,41984
v_accvgpr_write_b32 a179, s18
Mvaa 76,28,136
v_accvgpr_write_b32 a243, v171
v_accvgpr_write_b32 a183, s19
Mvaa 44,28,132
G 176,112
v_mov_b32 v255, v241
Mvaa 44,24,60
W 8
G 177,113
G 178,114
Mvaa 48,0,60
Ta 24,9,32768
v_accvgpr_write_b32 a160, 0
Mvaa 48,4,132
Ta 26,9,33792
G 179,115
Mvaa 80,4,136
v_accvgpr_write_b32 a161, 0
G 180,116
Mvav 80,0,184
Ta 28,9,40960
v_accvgpr_write_b32 a162, 0
Mvav 112,0,192
Ta 30,9,41984
G 181,117
Mvaa 112,4,140
v_accvgpr_write_b32 a163, 0
G 182,118
Mvaa 144,4,144
v_accvgpr_write_b32 a164, 0
G 183,119
Mvaa 144,0,128
W 8
v_accvgpr_write_b32 a165, 0
K 176,176,177
Mvaa 148,8,128
Ta 0,2,49152
v_accvgpr_write_b32 a166, 0
Mvaa 148,12,144
Ta 2,2,50176
K 177,178,179
Mvaa 116,12,140
v_accvgpr_write_b32 a167, 0
K 178,180,181
Mvav 116,8,192
v_accvgpr_write_b32 a168, 0
K 179,182,183
Mvav 84,8,184
Ta 4,2,57344
Ta 6,2,58368
Mvaa 84,12,136
v_accvgpr_write_b32 a169, 0
v_accvgpr_write_b32 a244, v176
Mvaa 52,12,132
v_accvgpr_write_b32 a245, v177
v_accvgpr_write_b32 a170, 0
Mvaa 52,8,60
W 8
v_accvgpr_write_b32 a246, v178
v_accvgpr_write_b32 a171, 0
Mvaa 56,16,60
Ta 8,3,49152
v_accvgpr_write_b32 a247, v179
Mvaa 56,20,132
Ta 10,3,50176
v_accvgpr_write_b32 a172, 0
Mvaa 88,20,136
G 168,248
v_accvgpr_write_b32 a173, 0
Mvav 88,16,184
G 169,249
v_accvgpr_write_b32 a174, 0
Mvav 120,16,192
Ta 12,3,57344
Ta 14,3,58368
Mvaa 120,20,140
G 170,250
v_accvgpr_write_b32 a175, 0
Mvaa 152,20,144
s_mov_b32 s90, 0x5d800000
G 171,251
G 172,252
Mvaa 152,16,128
W 8
s_mov_b64 s[88:89], 0
G 173,253
Mvaa 156,24,128
s_mov_b32 s64, 0
Ta 16,4,49152
Ta 18,4,50176
Mvaa 156,28,144
s_and_b32 s18, s14, 1
G 174,254
G 175,255
Mvaa 124,28,140
s_lshl_b32 s62, s18, 14
Ta 20,4,57344
Ta 22,4,58368
Mvav 124,24,192
K 168,168,169
Mvav 92,24,184
s_lshl_b32 s20, s57, 15
K 169,170,171
Mvaa 92,28,136
s_sub_u32 s20, 507904, s20
K 170,172,173
Mvaa 60,28,132
K 171,174,175
Mvaa 60,24,60
s_waitcnt vmcnt(0) lgkmcnt(8)
s_add_u32 s20, s20, s62
Ta 24,5,49152
Mvav 32,0,200
s_lshl_b32 s65, s57, 1
Ta 26,5,50176
v_accvgpr_write_b32 a248, v168
Mvav 32,4,188
s_sub_u32 s65, s65, 1
v_accvgpr_write_b32 a249, v169
Mvav 64,4,196
Ta 28,5,57344
v_accvgpr_write_b32 a250, v170
Mvav 64,0,208
s_max_i32 s65, s65, 0
Ta 30,5,58368
v_accvgpr_write_b32 a251, v171
Mvav 96,0,216
s_sub_u32 s66, s60, 1
G 176,120
Mvav 96,4,204
s_cmp_lt_u32 s14, 2
s_cselect_b32 s20, s20, 0
s_cselect_b32 s66, s66, 15
s_cselect_b32 s21, 32768, 0
Mvav 128,4,212
G 177,121
Mvav 128,0,224
W 8
s_add_u32 s61, s91, s20
G 178,122
Mvav 132,8,224
s_add_u32 s62, s62, s21
Ta 0,6,49152
Ta 2,6,50176
Mvav 132,12,212
G 179,123
G 180,124
Mvav 100,12,204
)"
R"(s_sub_u32 s63, 16384, s21
Ta 4,6,57344
Ta 6,6,58368
Mvav 100,8,216
s_lshl_b32 s67, s60, 7
G 181,125
Mvav 68,8,208
s_add_u32 s67, s67, s56
G 182,126
Mvav 68,12,196
G 183,127
Mvav 36,12,188
s_lshl_b32 s67, s67, 12
K 176,176,177
Mvav 36,8,200
W 8
s_add_u32 s67, s67, 39845888
Ta 8,7,49152
Mvav 40,16,200
s_lshl_b32 s69, s60, 3
Ta 10,7,50176
K 177,178,179
Mvav 40,20,188
K 178,180,181
Mvav 72,20,196
s_add_u32 s69, s69, s59
Ta 12,7,57344
K 179,182,183
Mvav 72,16,208
s_lshl_b32 s69, s69, 6
Ta 14,7,58368
v_accvgpr_write_b32 a252, v176
Mvav 104,16,216
Mvav 104,20,204
s_lshl_b32 s21, s58, 3
v_accvgpr_write_b32 a253, v177
Mvav 136,20,212
s_add_u32 s69, s69, s21
v_accvgpr_write_b32 a254, v178
Mvav 136,16,224
W 8
s_add_u32 s69, s69, 20480
v_accvgpr_write_b32 a255, v179
Mvav 140,24,224
Ta 16,8,49152
Ta 18,8,50176
Mvav 140,28,212
s_mov_b32 s70, 0
Mvav 108,28,204
s_mov_b32 s37, 0
Ta 20,8,57344
Ta 22,8,58368
Mvav 108,24,216
s_sub_u32 s18, s57, 4
Mvav 76,24,208
Mvav 76,28,196
s_lshl_b32 s18, s18, 2
Mvav 44,28,188
s_add_u32 s18, s18, s58
Mvav 44,24,200
W 8
Ta 24,9,49152
Ta 26,9,50176
Mvav 48,0,200
s_mul_i32 s85, s59, 7
Mvav 48,4,188
s_add_u32 s85, s85, s18
Mvav 80,4,196
s_cmp_lt_u32 s18, 7
s_cselect_b32 s85, s85, 99
Ta 28,9,57344
Ta 30,9,58368
Mvav 80,0,208
Mvav 112,0,216
s_cmp_lt_u32 s85, 54
s_cselect_b32 s65, 2, s65
Mvav 112,4,204
s_mov_b32 s68, 0
Mvav 144,4,212
s_mov_b64 s[24:25], -1
Mvav 144,0,224
W 8
Mvav 148,8,224
s_mov_b32 s22, 0x3e0293ee
Mvav 148,12,212
s_mov_b32 s23, 0x3e0293ee
Mvav 116,12,204
Mvav 116,8,216
Mvav 84,8,208
Mvav 84,12,196
Mvav 52,12,188
Mvav 52,8,200
W 4
Mvav 56,16,200
Mvav 56,20,188
Mvav 88,20,196
Mvav 88,16,208
Mvav 120,16,216
W 0
s_barrier
Mvav 120,20,204
Ra 128,242,0
Mvav 152,20,212
Ra 132,242,1024
Mvav 152,16,224
Mvav 156,24,224
Ra 136,242,2048
Mvav 156,28,212
Mvav 124,28,204
Ra 140,242,3072
Mvav 124,24,216
Mvav 92,24,208
Mvav 92,28,196
Mvav 60,28,188
Mvav 60,24,200
.Lago%=:
s_cmp_eq_u32 s60, 1
s_cbranch_scc1 .Lpe_44%=
s_cmp_eq_u32 s60, 0
s_cbranch_scc1 .Lpd_28%=
v_pk_mul_f32 v[32:33], v[32:33], s[22:23]
v_pk_mul_f32 v[34:35], v[34:35], s[22:23]
v_pk_mul_f32 v[36:37], v[36:37], s[22:23]
v_pk_mul_f32 v[38:39], v[38:39], s[22:23]
v_pk_mul_f32 v[64:65], v[64:65], s[22:23]
v_pk_mul_f32 v[66:67], v[66:67], s[22:23]
v_pk_mul_f32 v[68:69], v[68:69], s[22:23]
v_pk_mul_f32 v[70:71], v[70:71], s[22:23]
v_pk_mul_f32 v[96:97], v[96:97], s[22:23]
v_pk_mul_f32 v[98:99], v[98:99], s[22:23]
v_pk_mul_f32 v[100:101], v[100:101], s[22:23]
v_pk_mul_f32 v[102:103], v[102:103], s[22:23]
v_pk_mul_f32 v[128:129], v[128:129], s[22:23]
v_pk_mul_f32 v[130:131], v[130:131], s[22:23]
v_pk_mul_f32 v[132:133], v[132:133], s[22:23]
v_pk_mul_f32 v[134:135], v[134:135], s[22:23]
v_pk_mul_f32 v[40:41], v[40:41], s[22:23]
v_pk_mul_f32 v[42:43], v[42:43], s[22:23]
v_pk_mul_f32 v[44:45], v[44:45], s[22:23]
v_pk_mul_f32 v[46:47], v[46:47], s[22:23]
v_pk_mul_f32 v[72:73], v[72:73], s[22:23]
v_pk_mul_f32 v[74:75], v[74:75], s[22:23]
v_pk_mul_f32 v[76:77], v[76:77], s[22:23]
v_pk_mul_f32 v[78:79], v[78:79], s[22:23]
v_pk_mul_f32 v[104:105], v[104:105], s[22:23]
v_pk_mul_f32 v[106:107], v[106:107], s[22:23]
v_pk_mul_f32 v[108:109], v[108:109], s[22:23]
v_pk_mul_f32 v[110:111], v[110:111], s[22:23]
v_pk_mul_f32 v[136:137], v[136:137], s[22:23]
v_pk_mul_f32 v[138:139], v[138:139], s[22:23]
v_pk_mul_f32 v[140:141], v[140:141], s[22:23]
v_pk_mul_f32 v[142:143], v[142:143], s[22:23]
v_pk_mul_f32 v[48:49], v[48:49], s[22:23]
v_pk_mul_f32 v[50:51], v[50:51], s[22:23]
v_pk_mul_f32 v[52:53], v[52:53], s[22:23]
v_pk_mul_f32 v[54:55], v[54:55], s[22:23]
v_pk_mul_f32 v[80:81], v[80:81], s[22:23]
v_pk_mul_f32 v[82:83], v[82:83], s[22:23]
v_pk_mul_f32 v[84:85], v[84:85], s[22:23]
v_pk_mul_f32 v[86:87], v[86:87], s[22:23]
v_pk_mul_f32 v[112:113], v[112:113], s[22:23]
v_pk_mul_f32 v[114:115], v[114:115], s[22:23]
v_pk_mul_f32 v[116:117], v[116:117], s[22:23]
v_pk_mul_f32 v[118:119], v[118:119], s[22:23]
v_pk_mul_f32 v[144:145], v[144:145], s[22:23]
v_pk_mul_f32 v[146:147], v[146:147], s[22:23]
v_pk_mul_f32 v[148:149], v[148:149], s[22:23]
v_pk_mul_f32 v[150:151], v[150:151], s[22:23]
v_pk_mul_f32 v[56:57], v[56:57], s[22:23]
v_pk_mul_f32 v[58:59], v[58:59], s[22:23]
v_pk_mul_f32 v[60:61], v[60:61], s[22:23]
v_pk_mul_f32 v[62:63], v[62:63], s[22:23]
v_pk_mul_f32 v[88:89], v[88:89], s[22:23]
v_pk_mul_f32 v[90:91], v[90:91], s[22:23]
v_pk_mul_f32 v[92:93], v[92:93], s[22:23]
v_pk_mul_f32 v[94:95], v[94:95], s[22:23]
v_pk_mul_f32 v[120:121], v[120:121], s[22:23]
v_pk_mul_f32 v[122:123], v[122:123], s[22:23]
v_pk_mul_f32 v[124:125], v[124:125], s[22:23]
v_pk_mul_f32 v[126:127], v[126:127], s[22:23]
v_pk_mul_f32 v[152:153], v[152:153], s[22:23]
v_pk_mul_f32 v[154:155], v[154:155], s[22:23]
v_pk_mul_f32 v[156:157], v[156:157], s[22:23]
v_pk_mul_f32 v[158:159], v[158:159], s[22:23]
K 32,32,33
K 33,34,35
K 34,36,37
K 35,38,39
K 40,40,41
K 41,42,43
K 42,44,45
K 43,46,47
W 0
Zvav 196,128,32
s_add_u32 s36, s29, 65536
Ra 144,242,4096
Ra 148,242,5120
Ra 152,242,6144
Ra 156,242,7168
Mvav 196,132,40
K 64,64,65
K 65,66,67
K 66,68,69
K 67,70,71
K 72,72,73
K 73,74,75
K 74,76,77
K 75,78,79
s_nop 1
Zvav 204,132,72
buffer_store_dwordx4 a[192:195], v1, s[8:11], s36 offen offset:0 sc1
K 96,96,97
K 97,98,99
K 98,100,101
Mvav 204,128,64
K 99,102,103
K 104,104,105
K 105,106,107
K 106,108,109
K 107,110,111
Zvav 212,128,96
buffer_store_dwordx4 a[200:203], v1, s[8:11], s36 offen offset:1024 sc1
K 128,128,129
K 129,130,131
K 130,132,133
Mvav 212,132,104
K 131,134,135
K 136,136,137
K 137,138,139
K 138,140,141
K 139,142,143
s_nop 1
Zvav 220,132,136
K 48,48,49
K 49,50,51
K 50,52,53
K 51,54,55
Mvav 220,128,128
K 56,56,57
K 57,58,59
K 58,60,61
K 59,62,63
Mvav 196,136,48
buffer_store_dwordx4 a[208:211], v1, s[8:11], s36 offen offset:2048 sc1
Ra 128,242,8192
Ra 132,242,9216
K 80,80,81
Mvav 196,140,56
K 81,82,83
K 82,84,85
K 83,86,87
K 88,88,89
K 89,90,91
K 90,92,93
K 91,94,95
s_nop 1
Mvav 204,140,88
buffer_store_dwordx4 a[216:219], v1, s[8:11], s36 offen offset:3072 sc1
s_add_u32 s36, s29, 69632
K 112,112,113
K 113,114,115
K 114,116,117
Mvav 204,136,80
K 115,118,119
K 120,120,121
K 121,122,123
K 122,124,125
K 123,126,127
Mvav 212,136,112
K 144,144,145
K 145,146,147
K 146,148,149
K 147,150,151
Mvav 212,140,120
K 152,152,153
K 153,154,155
K 154,156,157
K 155,158,159
s_nop 1
Mvav 220,140,152
buffer_store_dwordx4 a[224:227], v1, s[8:11], s36 offen offset:0 sc1
v_accvgpr_write_b32 a0, v32
v_accvgpr_write_b32 a1, v33
v_accvgpr_write_b32 a2, v34
Mvav 220,136,144
W 2
Ra 136,242,10240
v_accvgpr_write_b32 a3, v35
v_accvgpr_write_b32 a4, v40
Zvav 36,144,32
Ra 140,242,11264
buffer_store_dwordx4 a[232:235], v1, s[8:11], s36 offen offset:1024 sc1
v_accvgpr_write_b32 a5, v41
Mvav 36,148,40
v_accvgpr_write_b32 a6, v42
v_accvgpr_write_b32 a7, v43
v_max3_f32 v4, v196, v197, v198
v_max3_f32 v4, v4, v199, v252
Zvav 68,148,72
v_max3_f32 v5, v204, v205, v206
v_max3_f32 v5, v5, v207, v253
v_max3_f32 v6, v212, v213, v214
v_max3_f32 v6, v6, v215, v254
Mvav 68,144,64
v_max3_f32 v7, v220, v221, v222
v_max3_f32 v7, v7, v223, v255
v_mov_b32 v8, v4
v_mov_b32 v9, v5
Zvav 100,144,96
v_mov_b32 v10, v6
v_mov_b32 v11, v7
v_permlane32_swap_b32 v4, v8
v_accvgpr_write_b32 a8, v48
Mvav 100,148,104
v_permlane32_swap_b32 v5, v9
v_permlane32_swap_b32 v6, v10
v_permlane32_swap_b32 v7, v11
v_max_f32 v4, v4, v8
Zvav 132,148,136
v_max_f32 v5, v5, v9
v_max_f32 v6, v6, v10
v_max_f32 v7, v7, v11
v_mov_b32 v8, v4
Mvav 132,144,128
v_mov_b32 v9, v5
v_mov_b32 v10, v6
v_mov_b32 v11, v7
v_accvgpr_write_b32 a9, v49
Mvav 36,152,48
v_permlane16_swap_b32 v4, v8
v_permlane16_swap_b32 v5, v9
v_permlane16_swap_b32 v6, v10
v_permlane16_swap_b32 v7, v11
Mvav 36,156,56
v_max_f32 v4, v4, v8
v_max_f32 v5, v5, v9
v_max_f32 v6, v6, v10
v_max_f32 v7, v7, v11
Mvav 68,156,88
v_sub_f32 v4, 0, v4
v_sub_f32 v5, 0, v5
v_sub_f32 v6, 0, v6
v_sub_f32 v7, 0, v7
Mvav 68,152,80
v_mov_b32 v224, v4
v_mov_b32 v225, v4
v_mov_b32 v226, v4
v_mov_b32 v227, v4
Mvav 100,152,112
v_mov_b32 v228, v5
v_mov_b32 v229, v5
v_mov_b32 v230, v5
v_mov_b32 v231, v5
Mvav 100,156,120
v_mov_b32 v232, v6
v_mov_b32 v233, v6
v_mov_b32 v234, v6
v_mov_b32 v235, v6
Mvav 132,156,152
v_mov_b32 v236, v7
v_mov_b32 v237, v7
v_mov_b32 v238, v7
v_mov_b32 v239, v7
Mvav 132,152,144
W 0
buffer_store_dwordx4 a[240:243], v1, s[8:11], s36 offen offset:2048 sc1
v_accvgpr_write_b32 a10, v50
v_accvgpr_write_b32 a11, v51
v_accvgpr_write_b32 a12, v56
Cvav 160,128,32,224
s_and_b32 s80, s14, 1
v_accvgpr_write_b32 a13, v57
v_accvgpr_write_b32 a14, v58
v_accvgpr_write_b32 a15, v59
v_accvgpr_write_b32 a16, v64
Mvav 160,132,40
buffer_store_dwordx4 a[248:251], v1, s[8:11], s36 offen offset:3072 sc1
s_add_u32 s36, s29, 393216
Ra 144,242,12288
Ra 148,242,13312
v_accvgpr_write_b32 a17, v65
Cvav 164,132,72,228
s_lshl_b32 s80, s80, 14
v_accvgpr_write_b32 a18, v66
v_accvgpr_write_b32 a19, v67
v_accvgpr_write_b32 a20, v72
v_accvgpr_write_b32 a21, v73
Mvav 164,128,64
s_add_u32 s80, s80, 98304
buffer_store_dwordx4 a[196:199], v1, s[8:11], s36 offen offset:0 sc1
v_accvgpr_write_b32 a22, v74
v_accvgpr_write_b32 a23, v75
v_accvgpr_write_b32 a24, v80
Cvav 168,128,96,232
v_accvgpr_write_b32 a25, v81
v_accvgpr_write_b32 a26, v82
A 196,196,224
v_accvgpr_write_b32 a27, v83
Mvav 168,132,104
buffer_store_dwordx4 a[204:207], v1, s[8:11], s36 offen offset:1024 sc1
)"
R"(A 197,197,224
A 198,198,224
v_accvgpr_write_b32 a28, v88
Cvav 172,132,136,236
Ra 152,242,14336
Ra 156,242,15360
A 199,199,224
v_accvgpr_write_b32 a29, v89
Mvav 172,128,128
buffer_store_dwordx4 a[212:215], v1, s[8:11], s36 offen offset:2048 sc1
X 196
v_accvgpr_write_b32 a30, v90
Mvav 160,136,48
X 197
v_accvgpr_write_b32 a31, v91
v_accvgpr_write_b32 a32, v96
Mvav 160,140,56
X 198
K 192,196,197
v_accvgpr_write_b32 a33, v97
Mvav 164,140,88
X 199
A 204,204,228
v_accvgpr_write_b32 a34, v98
Mvav 164,136,80
buffer_store_dwordx4 a[220:223], v1, s[8:11], s36 offen offset:3072 sc1
s_add_u32 s36, s29, 397312
K 193,198,199
v_accvgpr_write_b32 a35, v99
A 205,205,228
Mvav 168,136,112
A 206,206,228
v_accvgpr_write_b32 a36, v104
v_add_u32 v8, s80, v1
A 207,207,228
Mvav 168,140,120
X 204
v_accvgpr_write_b32 a37, v105
v_accvgpr_write_b32 a38, v106
Mvav 172,140,152
X 205
X 206
Mvav 172,136,144
W 0
buffer_store_dwordx4 a[228:231], v1, s[8:11], s36 offen offset:0 sc1
v_accvgpr_write_b32 a39, v107
v_accvgpr_write_b32 a40, v112
K 200,204,205
Cvav 176,144,32,224
Ra 128,242,24576
X 207
v_accvgpr_write_b32 a41, v113
Mvav 176,148,40
Ra 132,242,25600
A 212,212,232
v_accvgpr_write_b32 a42, v114
K 201,206,207
Cvav 180,148,72,228
A 213,213,232
v_accvgpr_write_b32 a43, v115
A 214,214,232
A 215,215,232
Mvav 180,144,64
buffer_store_dwordx4 a[236:239], v1, s[8:11], s36 offen offset:1024 sc1
v_accvgpr_write_b32 a44, v120
X 212
Cvav 184,144,96,232
v_accvgpr_write_b32 a45, v121
X 213
v_accvgpr_write_b32 a46, v122
Mvav 184,148,104
X 214
v_accvgpr_write_b32 a47, v123
K 208,212,213
Cvav 188,148,136,236
buffer_store_dwordx4 a[244:247], v1, s[8:11], s36 offen offset:2048 sc1
v_accvgpr_write_b32 a48, v128
X 215
Mvav 188,144,128
v_accvgpr_write_b32 a49, v129
A 220,220,236
v_accvgpr_write_b32 a50, v130
K 209,214,215
Mvav 176,152,48
A 221,221,236
v_accvgpr_write_b32 a51, v131
A 222,222,236
A 223,223,236
Mvav 176,156,56
Ra 136,242,26624
v_accvgpr_write_b32 a52, v136
X 220
Mvav 180,156,88
Ra 140,242,27648
v_accvgpr_write_b32 a53, v137
X 221
Mvav 180,152,80
buffer_store_dwordx4 a[252:255], v1, s[8:11], s36 offen offset:3072 sc1
v_accvgpr_write_b32 a54, v138
X 222
Mvav 184,152,112
v_accvgpr_write_b32 a55, v139
v_accvgpr_write_b32 a56, v144
v_accvgpr_write_b32 a57, v145
v_accvgpr_write_b32 a58, v146
v_accvgpr_write_b32 a59, v147
v_accvgpr_write_b32 a60, v152
v_accvgpr_write_b32 a61, v153
v_accvgpr_write_b32 a62, v154
v_accvgpr_write_b32 a63, v155
Mvav 184,156,120
s_barrier
K 216,220,221
X 223
A 36,36,224
Mvav 188,156,152
K 217,222,223
A 37,37,224
A 38,38,224
A 39,39,224
X 36
X 37
X 38
K 194,36,37
X 39
A 68,68,228
K 195,38,39
A 69,69,228
A 70,70,228
A 71,71,228
X 68
X 69
X 70
K 202,68,69
X 71
A 100,100,232
K 203,70,71
A 101,101,232
A 102,102,232
A 103,103,232
X 100
X 101
X 102
K 210,100,101
X 103
A 132,132,236
K 211,102,103
A 133,133,236
A 134,134,236
A 135,135,236
X 132
X 133
X 134
K 218,132,133
X 135
s_nop 0
K 219,134,135
Mvav 188,152,144
W 0
Ra 144,242,28672
Ra 148,242,29696
Ra 152,242,30720
Zvav 32,128,192
Ra 156,242,31744
Zvav 64,128,200
v_xor_b32 v242, 0x10000, v242
Zvav 96,128,208
s_and_b32 s80, s14, 1
s_xor_b32 s62, s62, 0x10000
Zvav 128,128,216
s_lshl_b32 s80, s80, 14
s_add_u32 s80, s80, 131072
v_add_u32 v23, 61440, v21
Zvav 132,132,216
v_add_u32 v12, s80, v1
Ra 64,12,8192
Ra 68,12,9216
v_add_u32 v24, 61440, v22
Zvav 100,132,208
Ra 72,12,10240
Ra 76,12,11264
Ra 80,12,12288
v_add_u32 v25, 122880, v21
Zvav 68,132,200
Ra 84,12,13312
Ra 88,12,14336
Ra 92,12,15360
v_add_u32 v26, 122880, v22
Zvav 36,132,192
Ra 96,12,0
Ra 100,12,1024
Ra 104,12,2048
Zvav 40,136,192
Ra 108,12,3072
Ra 112,12,4096
X 172
Zvav 72,136,200
Ra 116,12,5120
Ra 120,12,6144
X 173
Zvav 104,136,208
Ra 124,12,7168
X 174
K 220,172,173
Zvav 136,136,216
X 175
Zvav 140,140,216
X 168
K 221,174,175
Zvav 108,140,208
X 169
X 170
Zvav 76,140,200
s_waitcnt vmcnt(0) lgkmcnt(0)
s_barrier
ds_read_b128 v[248:251], v8
K 212,168,169
Zvav 44,140,192
s_add_u32 s80, s71, 128
v_mov_b64 v[28:29], s[12:13]
s_mov_b64 exec, 1
buffer_store_dwordx2 v[28:29], off, s[8:11], s80 sc1
s_mov_b64 exec, -1
X 171
X 164
Zvav 48,144,192
Ra 128,242,0
K 213,170,171
X 165
Zvav 80,144,200
X 166
Zvav 112,144,208
Ra 132,242,1024
K 204,164,165
X 167
Zvav 144,144,216
X 160
Zvav 148,148,216
K 205,166,167
X 161
Zvav 116,148,208
Ra 136,242,2048
X 162
Zvav 84,148,200
W 3
K 196,160,161
X 163
Zvav 52,148,192
X 176
K 197,162,163
Zvav 56,152,192
Ra 140,242,3072
X 177
Zvav 88,152,200
X 178
K 198,176,177
Zvav 120,152,208
X 179
X 180
Zvav 152,152,216
K 199,178,179
Zvav 156,156,216
X 181
X 182
Zvav 124,156,208
K 206,180,181
X 183
Zvav 92,156,200
K 207,182,183
Zvav 60,156,192
.Ll0t_42%=:
s_cmp_eq_u32 s64, s65
s_cbranch_scc1 .Ll1t_31%=
.Ll0_29%=:
W 0
Cvaa 160,128,0,224
Ra 144,242,4096
Ra 148,242,5120
Mvaa 160,132,4
s_xor_b32 s62, s62, 0x10000
Ra 152,242,6144
Cvaa 164,132,20,228
X 184
Mvaa 164,128,16
X 185
Cvaa 168,128,32,232
X 186
Mvaa 168,132,36
Ra 156,242,7168
K 214,184,185
Cvaa 172,132,52,236
X 187
Mvaa 172,128,48
X 188
Mvaa 160,136,8
Ra 128,242,16384
Mvaa 160,140,12
K 215,186,187
Mvaa 164,140,28
X 189
Mvaa 164,136,24
X 190
Mvaa 168,136,40
Ra 132,242,17408
Mvaa 168,140,44
K 222,188,189
Mvaa 172,140,60
X 191
Mvaa 172,136,56
K 223,190,191
v_smfmac_f32_16x16x64_bf16 a[160:163], a[176:179], v[192:199], v240
Ra 136,242,18432
v_smfmac_f32_16x16x64_bf16 a[164:167], a[176:179], v[200:207], v240
X 160
v_smfmac_f32_16x16x64_bf16 a[168:171], a[176:179], v[208:215], v240
X 161
v_smfmac_f32_16x16x64_bf16 a[172:175], a[176:179], v[216:223], v240
W 3
X 162
Cvaa 176,144,0,224
Ra 140,242,19456
Mvaa 176,148,4
K 192,160,161
Cvaa 180,148,20,228
X 163
Mvaa 180,144,16
X 164
Cvaa 184,144,32,232
Mvaa 184,148,36
K 193,162,163
Cvaa 188,148,52,236
X 165
Mvaa 188,144,48
X 166
Mvaa 176,152,8
Ra 144,242,20480
Ra 148,242,21504
Mvaa 176,156,12
K 200,164,165
Mvaa 180,156,28
X 167
Mvaa 180,152,24
X 168
Mvaa 184,152,40
Mvaa 184,156,44
K 201,166,167
Mvaa 188,156,60
X 169
Mvaa 188,152,56
W 2
Ra 152,242,22528
Mvav 32,128,196
X 170
Mvav 64,128,204
Ra 156,242,23552
K 208,168,169
Mvav 96,128,212
X 171
Mvav 128,128,220
Mvav 132,132,220
X 172
Mvav 100,132,212
s_add_u32 s64, s64, 1
Ra 128,242,8192
K 209,170,171
Mvav 68,132,204
X 173
Mvav 36,132,196
Mvav 40,136,196
X 174
Mvav 72,136,204
Ra 132,242,9216
K 216,172,173
Mvav 104,136,212
X 175
Mvav 136,136,220
Mvav 140,140,220
X 176
Mvav 108,140,212
Ra 136,242,10240
K 217,174,175
Mvav 76,140,204
X 177
Mvav 44,140,196
W 3
Mvav 48,144,196
X 178
Mvav 80,144,204
Ra 140,242,11264
Mvav 112,144,212
K 194,176,177
Mvav 144,144,220
X 179
Mvav 148,148,220
X 180
Mvav 116,148,212
Ra 144,242,12288
Mvav 84,148,204
K 195,178,179
Mvav 52,148,196
)"
R"(X 181
Mvav 56,152,196
X 182
Mvav 88,152,204
Ra 148,242,13312
Mvav 120,152,212
K 202,180,181
Mvav 152,152,220
X 183
Mvav 156,156,220
X 184
Mvav 124,156,212
Ra 152,242,14336
Mvav 92,156,204
K 203,182,183
Mvav 60,156,196
W 3
X 185
Cvaa 160,128,0,224
X 186
Mvaa 160,132,4
Ra 156,242,15360
Cvaa 164,132,20,228
K 210,184,185
Mvaa 164,128,16
X 187
Cvaa 168,128,32,232
X 188
Mvaa 168,132,36
Cvaa 172,132,52,236
K 211,186,187
Mvaa 172,128,48
X 189
Mvaa 160,136,8
Ra 128,242,24576
Ra 132,242,25600
Mvaa 160,140,12
X 190
Mvaa 164,140,28
K 218,188,189
Mvaa 164,136,24
X 191
Mvaa 168,136,40
Mvaa 168,140,44
Mvaa 172,140,60
X 160
Mvaa 172,136,56
W 2
Ra 136,242,26624
K 219,190,191
Cvaa 176,144,0,224
X 161
Mvaa 176,148,4
Ra 140,242,27648
Cvaa 180,148,20,228
X 162
Mvaa 180,144,16
K 196,160,161
Cvaa 184,144,32,232
X 163
Mvaa 184,148,36
X 164
Cvaa 188,148,52,236
K 197,162,163
Mvaa 188,144,48
X 165
Mvaa 176,152,8
Ra 144,242,28672
Ra 148,242,29696
Mvaa 176,156,12
X 166
Mvaa 180,156,28
K 204,164,165
Mvaa 180,152,24
X 167
Mvaa 184,152,40
Mvaa 184,156,44
X 168
Mvaa 188,156,60
Mvaa 188,152,56
W 2
Ra 152,242,30720
K 205,166,167
Mvav 32,128,192
X 169
Mvav 64,128,200
Ra 156,242,31744
v_xor_b32 v242, 0x10000, v242
Mvav 96,128,208
X 170
Mvav 128,128,216
K 212,168,169
Mvav 132,132,216
X 171
Mvav 100,132,208
X 172
Mvav 68,132,200
Mvav 36,132,192
K 213,170,171
Mvav 40,136,192
X 173
Mvav 72,136,200
X 174
Mvav 104,136,208
Mvav 136,136,216
K 220,172,173
Mvav 140,140,216
X 175
Mvav 108,140,208
X 176
Mvav 76,140,200
W 0
s_barrier
Mvav 44,140,192
Ra 128,242,0
K 221,174,175
Mvav 48,144,192
X 177
Mvav 80,144,200
X 178
Mvav 112,144,208
Ra 132,242,1024
Mvav 144,144,216
K 198,176,177
Mvav 148,148,216
X 179
Mvav 116,148,208
X 180
Mvav 84,148,200
Ra 136,242,2048
Mvav 52,148,192
K 199,178,179
Mvav 56,152,192
X 181
Mvav 88,152,200
Ra 140,242,3072
Mvav 120,152,208
X 182
Mvav 152,152,216
K 206,180,181
Mvav 156,156,216
X 183
Mvav 124,156,208
Mvav 92,156,200
K 207,182,183
Mvav 60,156,192
s_cmp_lg_u32 s64, s65
s_cbranch_scc1 .Ll0_29%=
s_cmp_lt_u32 s85, 54
s_cbranch_scc1 .Lmoe_40%=
.Ll1t_31%=:
s_cmp_eq_u32 s64, s66
s_cbranch_scc1 .Lsp1_32%=
.Ll1_30%=:
W 0
Cvaa 160,128,0,224
Ra 144,242,4096
Ra 148,242,5120
Mvaa 160,132,4
s_add_u32 m0, s62, 0
Ra 152,242,6144
Cvaa 164,132,20,228
X 184
Mvaa 164,128,16
X 185
Cvaa 168,128,32,232
X 186
Mvaa 168,132,36
Ra 156,242,7168
K 214,184,185
Cvaa 172,132,52,236
Dc 1,8,61,0
Mvaa 172,128,48
X 187
Mvaa 160,136,8
X 188
Mvaa 160,140,12
Ra 128,242,16384
K 215,186,187
Mvaa 164,140,28
Dc 1,8,61,1024
Mvaa 164,136,24
X 189
Mvaa 168,136,40
X 190
Mvaa 168,140,44
Ra 132,242,17408
K 222,188,189
Mvaa 172,140,60
Dc 1,8,61,2048
Mvaa 172,136,56
X 191
v_smfmac_f32_16x16x64_bf16 a[160:163], a[176:179], v[192:199], v240
Ra 136,242,18432
K 223,190,191
v_smfmac_f32_16x16x64_bf16 a[164:167], a[176:179], v[200:207], v240
X 160
v_smfmac_f32_16x16x64_bf16 a[168:171], a[176:179], v[208:215], v240
Dc 1,8,61,3072
v_smfmac_f32_16x16x64_bf16 a[172:175], a[176:179], v[216:223], v240
W 3
X 161
Cvaa 176,144,0,224
s_add_u32 m0, s62, 4096
X 162
Mvaa 176,148,4
Ra 140,242,19456
K 192,160,161
Cvaa 180,148,20,228
X 163
Mvaa 180,144,16
Dc 243,8,61,0
Cvaa 184,144,32,232
X 164
Mvaa 184,148,36
K 193,162,163
Cvaa 188,148,52,236
X 165
Mvaa 188,144,48
Dc 243,8,61,1024
Ra 144,242,20480
Mvaa 176,152,8
X 166
Mvaa 176,156,12
Ra 148,242,21504
K 200,164,165
Mvaa 180,156,28
X 167
Mvaa 180,152,24
Dc 243,8,61,2048
Mvaa 184,152,40
X 168
Mvaa 184,156,44
K 201,166,167
Mvaa 188,156,60
X 169
Mvaa 188,152,56
W 2
Dc 243,8,61,3072
Ra 152,242,22528
Mvav 32,128,196
s_add_u32 m0, s62, 8192
X 170
Mvav 64,128,204
Ra 156,242,23552
K 208,168,169
Mvav 96,128,212
X 171
Mvav 128,128,220
Dc 244,8,61,0
Mvav 132,132,220
X 172
Mvav 100,132,212
Ra 128,242,8192
K 209,170,171
Mvav 68,132,204
X 173
Mvav 36,132,196
Dc 244,8,61,1024
Mvav 40,136,196
X 174
Mvav 72,136,204
Ra 132,242,9216
K 216,172,173
Mvav 104,136,212
X 175
Mvav 136,136,220
Dc 244,8,61,2048
Mvav 140,140,220
X 176
Mvav 108,140,212
Ra 136,242,10240
K 217,174,175
Mvav 76,140,204
X 177
Mvav 44,140,196
W 3
Dc 244,8,61,3072
Mvav 48,144,196
X 178
Mvav 80,144,204
s_add_u32 m0, s62, 12288
Ra 140,242,11264
Mvav 112,144,212
K 194,176,177
Mvav 144,144,220
X 179
Mvav 148,148,220
X 180
Mvav 116,148,212
Dc 245,8,61,0
Ra 144,242,12288
Mvav 84,148,204
K 195,178,179
Mvav 52,148,196
X 181
Mvav 56,152,196
X 182
Mvav 88,152,204
Dc 245,8,61,1024
Ra 148,242,13312
Mvav 120,152,212
K 202,180,181
Mvav 152,152,220
X 183
Mvav 156,156,220
X 184
Mvav 124,156,212
Dc 245,8,61,2048
Ra 152,242,14336
Mvav 92,156,204
K 203,182,183
Mvav 60,156,196
W 3
X 185
Cvaa 160,128,0,224
X 186
Mvaa 160,132,4
Dc 245,8,61,3072
Ra 156,242,15360
Cvaa 164,132,20,228
s_add_u32 s61, s61, s63
K 210,184,185
Mvaa 164,128,16
X 187
Cvaa 168,128,32,232
X 188
Mvaa 168,132,36
Cvaa 172,132,52,236
s_xor_b32 s62, s62, 0x10000
K 211,186,187
Mvaa 172,128,48
X 189
Mvaa 160,136,8
Ra 128,242,24576
Ra 132,242,25600
Mvaa 160,140,12
s_add_u32 s64, s64, 1
X 190
Mvaa 164,140,28
K 218,188,189
Mvaa 164,136,24
X 191
Mvaa 168,136,40
Mvaa 168,140,44
X 160
Mvaa 172,140,60
K 219,190,191
Mvaa 172,136,56
W 2
X 161
Cvaa 176,144,0,224
Ra 136,242,26624
Ra 140,242,27648
Mvaa 176,148,4
X 162
Cvaa 180,148,20,228
K 196,160,161
Mvaa 180,144,16
X 163
Cvaa 184,144,32,232
Mvaa 184,148,36
X 164
Cvaa 188,148,52,236
K 197,162,163
Mvaa 188,144,48
X 165
Mvaa 176,152,8
Ra 144,242,28672
Ra 148,242,29696
Mvaa 176,156,12
X 166
Mvaa 180,156,28
K 204,164,165
Mvaa 180,152,24
X 167
Mvaa 184,152,40
Mvaa 184,156,44
X 168
Mvaa 188,156,60
Mvaa 188,152,56
W 2
Ra 152,242,30720
K 205,166,167
Mvav 32,128,192
X 169
Mvav 64,128,200
Ra 156,242,31744
v_xor_b32 v242, 0x10000, v242
Mvav 96,128,208
X 170
Mvav 128,128,216
K 212,168,169
Mvav 132,132,216
X 171
Mvav 100,132,208
X 172
Mvav 68,132,200
Mvav 36,132,192
K 213,170,171
)"
R"(Mvav 40,136,192
X 173
Mvav 72,136,200
X 174
Mvav 104,136,208
Mvav 136,136,216
K 220,172,173
Mvav 140,140,216
X 175
Mvav 108,140,208
X 176
Mvav 76,140,200
s_waitcnt vmcnt(0) lgkmcnt(0)
s_barrier
Mvav 44,140,192
Ra 128,242,0
K 221,174,175
Mvav 48,144,192
X 177
Mvav 80,144,200
X 178
Mvav 112,144,208
Ra 132,242,1024
Mvav 144,144,216
K 198,176,177
Mvav 148,148,216
X 179
Mvav 116,148,208
X 180
Mvav 84,148,200
Ra 136,242,2048
Mvav 52,148,192
K 199,178,179
Mvav 56,152,192
X 181
Mvav 88,152,200
Ra 140,242,3072
Mvav 120,152,208
X 182
Mvav 152,152,216
K 206,180,181
Mvav 156,156,216
X 183
Mvav 124,156,208
Mvav 92,156,200
K 207,182,183
Mvav 60,156,192
s_cmp_lg_u32 s64, s66
s_cbranch_scc1 .Ll1_30%=
s_cmp_ge_u32 s14, 2
s_cbranch_scc1 .Lf1r_43%=
.Lsp1_32%=:
W 0
Cvaa 160,128,0,224
Ra 144,242,4096
Ra 148,242,5120
Mvaa 160,132,4
s_add_u32 m0, s62, 0
Ra 152,242,6144
Cvaa 164,132,20,228
X 184
Mvaa 164,128,16
X 185
Cvaa 168,128,32,232
X 186
Mvaa 168,132,36
Ra 156,242,7168
K 214,184,185
Cvaa 172,132,52,236
Dc 1,8,61,0
Mvaa 172,128,48
X 187
Mvaa 160,136,8
X 188
Mvaa 160,140,12
Ra 128,242,16384
K 215,186,187
Mvaa 164,140,28
Dc 1,8,61,1024
Ra 132,242,17408
Mvaa 164,136,24
X 189
Mvaa 168,136,40
X 190
Mvaa 168,140,44
Dc 1,8,61,2048
K 222,188,189
Mvaa 172,140,60
X 191
Mvaa 172,136,56
Ra 136,242,18432
K 223,190,191
v_smfmac_f32_16x16x64_bf16 a[160:163], a[176:179], v[192:199], v240
Dc 1,8,61,3072
Ra 140,242,19456
v_smfmac_f32_16x16x64_bf16 a[164:167], a[176:179], v[200:207], v240
s_add_u32 m0, s62, 4096
v_cndmask_b32_e64 v160, v241, v160, s[72:73]
v_cndmask_b32_e64 v161, v241, v161, s[74:75]
v_smfmac_f32_16x16x64_bf16 a[168:171], a[176:179], v[208:215], v240
v_cndmask_b32_e64 v162, v241, v162, s[76:77]
v_cndmask_b32_e64 v163, v241, v163, s[78:79]
v_smfmac_f32_16x16x64_bf16 a[172:175], a[176:179], v[216:223], v240
W 4
X 160
Cvaa 180,144,16,228
X 161
Mvaa 180,148,20
Dc 243,8,61,0
Cvaa 184,148,36,232
X 162
Mvaa 184,144,32
K 192,160,161
Cvaa 188,144,48,236
X 163
Mvaa 188,148,52
Dc 243,8,61,1024
Ra 144,242,20480
Mvaa 180,152,24
X 164
Mvaa 180,156,28
Ra 148,242,21504
K 193,162,163
Mvaa 184,156,44
Dc 243,8,61,2048
Mvaa 184,152,40
X 165
Mvaa 188,152,56
X 166
Mvaa 188,156,60
W 2
Dc 243,8,61,3072
K 200,164,165
Mvav 32,128,196
s_add_u32 m0, s62, 8192
X 167
Mvav 64,128,204
Ra 152,242,22528
Ra 156,242,23552
Mvav 96,128,212
X 168
Mvav 128,128,220
Dc 244,8,61,0
K 201,166,167
Mvav 132,132,220
X 169
Mvav 100,132,212
X 170
Mvav 68,132,204
Ra 128,242,8192
K 208,168,169
Mvav 36,132,196
X 171
Mvav 40,136,196
Dc 244,8,61,1024
Ra 132,242,9216
Mvav 72,136,204
X 172
Mvav 104,136,212
K 209,170,171
Mvav 136,136,220
X 173
Mvav 140,140,220
Dc 244,8,61,2048
Ra 136,242,10240
Mvav 108,140,212
X 174
Mvav 76,140,204
K 216,172,173
Mvav 44,140,196
W 3
Dc 244,8,61,3072
Ra 140,242,11264
Mvav 48,144,196
s_add_u32 m0, s62, 12288
X 175
Mvav 80,144,204
v_mov_b32 v194, 0
K 217,174,175
Mvav 112,144,212
v_mov_b32 v195, 0
v_cndmask_b32_e64 v180, v241, v180, s[72:73]
Mvav 144,144,220
v_cndmask_b32_e64 v181, v241, v181, s[74:75]
v_cndmask_b32_e64 v182, v241, v182, s[76:77]
Mvav 148,148,220
Dc 245,8,61,0
v_cndmask_b32_e64 v183, v241, v183, s[78:79]
Mvav 116,148,212
X 180
Mvav 84,148,204
Ra 144,242,12288
Mvav 52,148,196
X 181
Mvav 56,152,196
Dc 245,8,61,1024
Ra 148,242,13312
Mvav 88,152,204
X 182
Mvav 120,152,212
K 202,180,181
Mvav 152,152,220
X 183
Mvav 156,156,220
Dc 245,8,61,2048
Ra 152,242,14336
Mvav 124,156,212
X 184
Mvav 92,156,204
K 203,182,183
Mvav 60,156,196
W 3
Dc 245,8,61,3072
Ra 156,242,15360
Cvaa 168,128,32,232
s_add_u32 s61, s61, s63
X 185
Mvaa 168,132,36
s_xor_b32 s62, s62, 0x10000
X 186
Cvaa 172,132,52,236
K 210,184,185
Mvaa 172,128,48
s_add_u32 s64, s64, 1
X 187
Mvaa 168,136,40
X 188
Mvaa 168,140,44
Ra 128,242,24576
K 211,186,187
Mvaa 172,140,60
Ra 132,242,25600
Mvaa 172,136,56
Ra 136,242,26624
Ra 140,242,27648
X 189
X 190
K 218,188,189
X 191
v_mov_b32 v196, 0
K 219,190,191
W 4
Cvaa 188,144,48,236
Ra 144,242,28672
Mvaa 188,148,52
v_mov_b32 v197, 0
v_mov_b32 v204, 0
Mvaa 188,152,56
Ra 148,242,29696
v_mov_b32 v205, 0
Mvaa 188,156,60
W 2
Ra 152,242,30720
v_cndmask_b32_e64 v168, v241, v168, s[72:73]
Mvav 32,128,192
v_cndmask_b32_e64 v169, v241, v169, s[74:75]
v_cndmask_b32_e64 v170, v241, v170, s[76:77]
Mvav 64,128,200
Ra 156,242,31744
v_cndmask_b32_e64 v171, v241, v171, s[78:79]
Mvav 96,128,208
v_xor_b32 v242, 0x10000, v242
Mvav 128,128,216
X 168
Mvav 132,132,216
X 169
Mvav 100,132,208
X 170
Mvav 68,132,200
v_mov_b32 v224, v250
K 212,168,169
Mvav 36,132,192
v_mov_b32 v225, v250
v_mov_b32 v226, v250
Mvav 40,136,192
X 171
Mvav 72,136,200
X 172
Mvav 104,136,208
v_mov_b32 v227, v250
K 213,170,171
Mvav 136,136,216
X 173
Mvav 140,140,216
v_mov_b32 v228, v251
v_mov_b32 v229, v251
Mvav 108,140,208
X 174
Mvav 76,140,200
K 220,172,173
v_mov_b32 v230, v251
Mvav 44,140,192
W 0
X 175
Mvav 48,144,192
v_mov_b32 v231, v251
v_mov_b32 v198, 0
Mvav 80,144,200
K 221,174,175
v_mov_b32 v232, v248
Mvav 112,144,208
v_mov_b32 v199, 0
v_mov_b32 v206, 0
Mvav 144,144,216
v_mov_b32 v233, v248
v_mov_b32 v207, 0
Mvav 148,148,216
v_mov_b32 v214, 0
v_mov_b32 v234, v248
Mvav 116,148,208
v_mov_b32 v215, 0
v_cndmask_b32_e64 v188, v241, v188, s[72:73]
Mvav 84,148,200
v_mov_b32 v235, v248
v_cndmask_b32_e64 v189, v241, v189, s[74:75]
Mvav 52,148,192
v_cndmask_b32_e64 v190, v241, v190, s[76:77]
v_mov_b32 v236, v249
Mvav 56,152,192
v_cndmask_b32_e64 v191, v241, v191, s[78:79]
v_mov_b32 v237, v249
Mvav 88,152,200
X 188
Mvav 120,152,208
X 189
Mvav 152,152,216
v_mov_b32 v238, v249
v_mov_b32 v239, v249
Mvav 156,156,216
X 190
Mvav 124,156,208
s_waitcnt vmcnt(0)
s_barrier
Mvav 92,156,200
Ra 128,242,40960
K 222,188,189
Mvav 60,156,192
Ra 132,242,41984
Ra 136,242,43008
Ra 140,242,44032
X 191
s_nop 0
K 223,190,191
s_cmp_eq_u32 s64, 15
s_cbranch_scc1 .Lfs_38%=
.Lsp2_33%=:
v_smfmac_f32_16x16x64_bf16 a[160:163], a[176:179], v[192:199], v240
s_add_u32 m0, s62, 0
Ra 144,242,45056
Ra 148,242,46080
Ra 152,242,47104
Ra 156,242,48128
v_smfmac_f32_16x16x64_bf16 a[164:167], a[176:179], v[200:207], v240
v_smfmac_f32_16x16x64_bf16 a[168:171], a[176:179], v[208:215], v240
v_smfmac_f32_16x16x64_bf16 a[172:175], a[176:179], v[216:223], v240
W 4
Dc 1,8,61,0
Cvaa 168,128,96,232
v_mov_b32 v192, 0
G 12,160
Mvaa 168,132,100
Dc 1,8,61,1024
G 4,161
A 12,12,4
v_cmp_nlt_f32_e64 s[86:87], v12, s90
v_mov_b32 v193, 0
Cvaa 172,132,116,236
v_rcp_f32 v12, v12
s_or_b64 s[88:89], s[88:89], s[86:87]
v_accvgpr_write_b32 a160, 0
v_mov_b32 v200, 0
v_accvgpr_write_b32 a161, 0
v_accvgpr_write_b32 a162, 0
Mvaa 172,128,112
Dc 1,8,61,2048
v_accvgpr_write_b32 a163, 0
G 13,164
v_mov_b32 v201, 0
G 5,165
A 13,13,5
Mvaa 168,136,104
Ra 128,242,16384
Ra 132,242,17408
v_cmp_nlt_f32_e64 s[86:87], v13, s90
v_rcp_f32 v13, v13
s_or_b64 s[88:89], s[88:89], s[86:87]
v_accvgpr_write_b32 a164, 0
Mvaa 168,140,108
Dc 1,8,61,3072
v_accvgpr_write_b32 a165, 0
v_accvgpr_write_b32 a166, 0
v_accvgpr_write_b32 a167, 0
U 32,32,12
U 33,33,12
Mvaa 172,140,124
U 34,34,12
U 35,35,12
U 36,36,12
U 37,37,12
U 38,38,12
)"
R"(U 39,39,12
Mvaa 172,136,120
s_add_u32 m0, s62, 4096
Ra 136,242,18432
Ra 140,242,19456
K 32,32,33
K 33,34,35
K 34,36,37
W 4
Cvaa 188,144,112,236
K 35,38,39
s_add_u32 s93, s67, 0
buffer_store_dwordx4 v[32:35], v1, s[8:11], s93 offen offset:0 sc1
U 40,40,12
U 41,41,12
U 42,42,12
U 43,43,12
Mvaa 188,148,116
Ra 144,242,20480
v_cndmask_b32_e64 v168, v241, v168, s[72:73]
v_cndmask_b32_e64 v169, v241, v169, s[74:75]
v_cndmask_b32_e64 v170, v241, v170, s[76:77]
v_cndmask_b32_e64 v171, v241, v171, s[78:79]
U 44,44,12
Mvaa 188,152,120
X 168
X 169
U 45,45,12
U 46,46,12
Mvaa 188,156,124
W 1
Ra 148,242,21504
Dc 243,8,61,0
U 47,47,12
K 40,40,41
K 41,42,43
K 42,44,45
Mvav 96,128,212
Ra 152,242,22528
Ra 156,242,23552
K 43,46,47
X 170
K 208,168,169
Mvav 128,128,220
buffer_store_dwordx4 v[40:43], v1, s[8:11], s93 offen offset:1024 sc1
U 48,48,12
U 49,49,12
U 50,50,12
U 51,51,12
U 52,52,12
Mvav 132,132,220
U 53,53,12
U 54,54,12
U 55,55,12
K 48,48,49
K 49,50,51
K 50,52,53
Mvav 100,132,212
Dc 243,8,61,1024
Ra 128,242,32768
Ra 132,242,33792
X 171
K 51,54,55
Mvav 104,136,212
X 172
K 209,170,171
X 173
Mvav 136,136,220
buffer_store_dwordx4 v[48:51], v1, s[8:11], s93 offen offset:2048 sc1
U 56,56,12
U 57,57,12
U 58,58,12
U 59,59,12
U 60,60,12
Mvav 140,140,220
U 61,61,12
U 62,62,12
U 63,63,12
K 56,56,57
K 57,58,59
K 58,60,61
Mvav 108,140,212
W 2
Dc 243,8,61,2048
Ra 136,242,34816
Ra 140,242,35840
K 59,62,63
X 174
Mvav 112,144,212
K 216,172,173
X 175
Mvav 144,144,220
buffer_store_dwordx4 v[56:59], v1, s[8:11], s93 offen offset:3072 sc1
Ra 144,242,36864
G 14,168
G 6,169
A 14,14,6
v_cmp_nlt_f32_e64 s[86:87], v14, s90
Mvav 148,148,220
v_rcp_f32 v14, v14
s_or_b64 s[88:89], s[88:89], s[86:87]
v_accvgpr_write_b32 a168, 0
v_accvgpr_write_b32 a169, 0
v_accvgpr_write_b32 a170, 0
v_accvgpr_write_b32 a171, 0
Mvav 116,148,212
Dc 243,8,61,3072
s_add_u32 m0, s62, 8192
G 15,172
G 7,173
A 15,15,7
v_cmp_nlt_f32_e64 s[86:87], v15, s90
v_mov_b32 v194, 0
Mvav 120,152,212
v_rcp_f32 v15, v15
s_or_b64 s[88:89], s[88:89], s[86:87]
v_accvgpr_write_b32 a172, 0
v_accvgpr_write_b32 a173, 0
v_accvgpr_write_b32 a174, 0
v_accvgpr_write_b32 a175, 0
Mvav 152,152,220
Dc 244,8,61,0
U 64,64,13
U 65,65,13
U 66,66,13
U 67,67,13
U 68,68,13
Mvav 156,156,220
U 69,69,13
U 70,70,13
U 71,71,13
K 64,64,65
K 65,66,67
K 66,68,69
Mvav 124,156,212
W 1
Dc 244,8,61,1024
Ra 148,242,37888
K 67,70,71
s_add_u32 s93, s67, 131072
K 217,174,175
v_mov_b32 v195, 0
Cvaa 160,128,64,224
Ra 152,242,38912
Ra 156,242,39936
v_mov_b32 v202, 0
v_mov_b32 v203, 0
v_mov_b32 v210, 0
v_mov_b32 v211, 0
Mvaa 160,132,68
buffer_store_dwordx4 v[64:67], v1, s[8:11], s93 offen offset:0 sc1
U 72,72,13
U 73,73,13
U 74,74,13
U 75,75,13
U 76,76,13
Cvaa 164,132,84,228
U 77,77,13
U 78,78,13
U 79,79,13
K 72,72,73
K 73,74,75
K 74,76,77
Mvaa 164,128,80
Dc 244,8,61,2048
K 75,78,79
v_cndmask_b32_e64 v188, v241, v188, s[72:73]
v_cndmask_b32_e64 v189, v241, v189, s[74:75]
v_cndmask_b32_e64 v190, v241, v190, s[76:77]
v_cndmask_b32_e64 v191, v241, v191, s[78:79]
Cvaa 168,128,96,232
X 188
X 189
Mvaa 168,132,100
buffer_store_dwordx4 v[72:75], v1, s[8:11], s93 offen offset:1024 sc1
U 80,80,13
U 81,81,13
U 82,82,13
U 83,83,13
U 84,84,13
Cvaa 172,132,116,236
U 85,85,13
U 86,86,13
U 87,87,13
K 80,80,81
K 81,82,83
K 82,84,85
Mvaa 172,128,112
Dc 244,8,61,3072
s_add_u32 m0, s62, 12288
Ra 128,242,49152
Ra 132,242,50176
K 83,86,87
X 190
Mvaa 160,136,72
K 218,188,189
X 191
Mvaa 160,140,76
buffer_store_dwordx4 v[80:83], v1, s[8:11], s93 offen offset:2048 sc1
U 88,88,13
U 89,89,13
U 90,90,13
U 91,91,13
U 92,92,13
Mvaa 164,140,92
U 93,93,13
U 94,94,13
U 95,95,13
K 88,88,89
K 89,90,91
K 90,92,93
Mvaa 164,136,88
K 91,94,95
buffer_store_dwordx4 v[88:91], v1, s[8:11], s93 offen offset:3072 sc1
U 96,96,14
U 97,97,14
U 98,98,14
U 99,99,14
Mvaa 168,136,104
U 100,100,14
U 101,101,14
U 102,102,14
U 103,103,14
K 96,96,97
K 97,98,99
Mvaa 168,140,108
Dc 245,8,61,0
K 98,100,101
K 99,102,103
s_add_u32 s93, s67, 262144
v_cndmask_b32_e64 v160, v241, v160, s[72:73]
K 219,190,191
v_cndmask_b32_e64 v161, v241, v161, s[74:75]
Mvaa 172,140,124
v_cndmask_b32_e64 v162, v241, v162, s[76:77]
v_cndmask_b32_e64 v163, v241, v163, s[78:79]
X 160
Mvaa 172,136,120
W 2
Dc 245,8,61,1024
Ra 136,242,51200
Ra 140,242,52224
X 161
Cvaa 180,144,80,228
Mvaa 180,148,84
buffer_store_dwordx4 v[96:99], v1, s[8:11], s93 offen offset:0 sc1
U 104,104,14
U 105,105,14
U 106,106,14
U 107,107,14
U 108,108,14
Cvaa 184,148,100,232
U 109,109,14
U 110,110,14
U 111,111,14
K 104,104,105
K 105,106,107
K 106,108,109
Mvaa 184,144,96
Dc 245,8,61,2048
K 107,110,111
X 162
K 196,160,161
Cvaa 188,144,112,236
Ra 144,242,53248
X 163
Mvaa 188,148,116
buffer_store_dwordx4 v[104:107], v1, s[8:11], s93 offen offset:1024 sc1
U 112,112,14
U 113,113,14
U 114,114,14
U 115,115,14
U 116,116,14
Mvaa 180,152,88
U 117,117,14
U 118,118,14
U 119,119,14
K 112,112,113
K 113,114,115
K 114,116,117
Mvaa 180,156,92
Dc 245,8,61,3072
s_add_u32 s61, s61, s63
s_xor_b32 s62, s62, 0x10000
s_add_u32 s64, s64, 1
K 115,118,119
X 164
Mvaa 184,156,108
Ra 148,242,54272
K 197,162,163
X 165
Mvaa 184,152,104
buffer_store_dwordx4 v[112:115], v1, s[8:11], s93 offen offset:2048 sc1
U 120,120,14
U 121,121,14
U 122,122,14
U 123,123,14
U 124,124,14
Mvaa 188,152,120
U 125,125,14
U 126,126,14
U 127,127,14
K 120,120,121
K 121,122,123
K 122,124,125
Mvaa 188,156,124
W 2
K 123,126,127
buffer_store_dwordx4 v[120:123], v1, s[8:11], s93 offen offset:3072 sc1
U 128,128,15
U 129,129,15
U 130,130,15
U 131,131,15
Zvav 32,128,192
U 132,132,15
U 133,133,15
U 134,134,15
U 135,135,15
K 128,128,129
K 129,130,131
Zvav 64,128,200
K 130,132,133
K 131,134,135
s_add_u32 s93, s67, 393216
buffer_store_dwordx4 v[128:131], v1, s[8:11], s93 offen offset:0 sc1
U 136,136,15
U 137,137,15
U 138,138,15
Zvav 96,128,208
U 139,139,15
U 140,140,15
U 141,141,15
U 142,142,15
U 143,143,15
K 136,136,137
Zvav 128,128,216
K 137,138,139
K 138,140,141
K 139,142,143
buffer_store_dwordx4 v[136:139], v1, s[8:11], s93 offen offset:1024 sc1
U 144,144,15
U 145,145,15
Zvav 132,132,216
U 146,146,15
U 147,147,15
U 148,148,15
U 149,149,15
U 150,150,15
U 151,151,15
Zvav 100,132,208
K 144,144,145
K 145,146,147
K 146,148,149
K 147,150,151
buffer_store_dwordx4 v[144:147], v1, s[8:11], s93 offen offset:2048 sc1
U 152,152,15
Zvav 68,132,200
U 153,153,15
U 154,154,15
U 155,155,15
U 156,156,15
U 157,157,15
U 158,158,15
Zvav 36,132,192
U 159,159,15
K 152,152,153
K 153,154,155
K 154,156,157
K 155,158,159
buffer_store_dwordx4 v[152:155], v1, s[8:11], s93 offen offset:3072 sc1
Zvav 40,136,192
Ra 152,242,55296
X 166
K 204,164,165
X 167
Zvav 72,136,200
Ra 156,242,56320
X 168
v_xor_b32 v242, 0x10000, v242
K 205,166,167
Zvav 104,136,208
X 169
X 170
K 212,168,169
Zvav 136,136,216
X 171
X 172
K 213,170,171
Zvav 140,140,216
X 173
Zvav 108,140,208
X 174
Zvav 76,140,200
s_waitcnt vmcnt(6) lgkmcnt(0)
s_barrier
Zvav 44,140,192
Ra 128,242,40960
K 220,172,173
X 175
Zvav 48,144,192
)"
R"(Zvav 80,144,200
v_mov_b32 v198, 0
Zvav 112,144,208
Ra 132,242,41984
K 221,174,175
Zvav 144,144,216
v_mov_b32 v199, 0
v_cndmask_b32_e64 v180, v241, v180, s[72:73]
Zvav 148,148,216
v_cndmask_b32_e64 v181, v241, v181, s[74:75]
Zvav 116,148,208
Ra 136,242,43008
v_cndmask_b32_e64 v182, v241, v182, s[76:77]
Zvav 84,148,200
v_cndmask_b32_e64 v183, v241, v183, s[78:79]
Zvav 52,148,192
X 180
Zvav 56,152,192
Ra 140,242,44032
X 181
Zvav 88,152,200
Zvav 120,152,208
X 182
Zvav 152,152,216
K 206,180,181
Zvav 156,156,216
X 183
Zvav 124,156,208
Zvav 92,156,200
s_waitcnt vmcnt(0)
s_and_b64 s[86:87], s[88:89], s[24:25]
s_cbranch_scc1 .LL_45%=
v_mov_b64 v[28:29], s[12:13]
s_mov_b64 exec, 1
buffer_store_dwordx2 v[28:29], off, s[8:11], s69 sc1
s_mov_b64 exec, -1
.LL_45%=:
K 207,182,183
Zvav 60,156,192
s_cmp_eq_u32 s64, 15
s_cbranch_scc1 .Lf2_37%=
.Ll2_34%=:
W 0
Cvaa 160,128,64,224
Ra 144,242,45056
Ra 148,242,46080
Mvaa 160,132,68
s_add_u32 m0, s62, 8192
Ra 152,242,47104
Cvaa 164,132,84,228
X 184
Mvaa 164,128,80
X 185
Cvaa 168,128,96,232
X 186
Mvaa 168,132,100
Ra 156,242,48128
K 214,184,185
Cvaa 172,132,116,236
Dc 244,8,61,0
Mvaa 172,128,112
X 187
Mvaa 160,136,72
X 188
Mvaa 160,140,76
Ra 128,242,57344
K 215,186,187
Mvaa 164,140,92
Dc 244,8,61,1024
Mvaa 164,136,88
X 189
Mvaa 168,136,104
X 190
Mvaa 168,140,108
Ra 132,242,58368
K 222,188,189
Mvaa 172,140,124
Dc 244,8,61,2048
Mvaa 172,136,120
X 191
v_smfmac_f32_16x16x64_bf16 a[160:163], a[176:179], v[192:199], v240
Ra 136,242,59392
K 223,190,191
v_smfmac_f32_16x16x64_bf16 a[164:167], a[176:179], v[200:207], v240
X 160
v_smfmac_f32_16x16x64_bf16 a[168:171], a[176:179], v[208:215], v240
Dc 244,8,61,3072
v_smfmac_f32_16x16x64_bf16 a[172:175], a[176:179], v[216:223], v240
W 3
X 161
Cvaa 176,144,64,224
s_add_u32 m0, s62, 12288
X 162
Mvaa 176,148,68
Ra 140,242,60416
K 192,160,161
Cvaa 180,148,84,228
X 163
Mvaa 180,144,80
Dc 245,8,61,0
Cvaa 184,144,96,232
X 164
Mvaa 184,148,100
K 193,162,163
Cvaa 188,148,116,236
X 165
Mvaa 188,144,112
Dc 245,8,61,1024
Ra 144,242,61440
Mvaa 176,152,72
X 166
Mvaa 176,156,76
Ra 148,242,62464
K 200,164,165
Mvaa 180,156,92
X 167
Mvaa 180,152,88
Dc 245,8,61,2048
Mvaa 184,152,104
X 168
Mvaa 184,156,108
K 201,166,167
Mvaa 188,156,124
X 169
Mvaa 188,152,120
W 2
Dc 245,8,61,3072
Ra 152,242,63488
Mvav 32,128,196
s_add_u32 m0, s62, 0
X 170
Mvav 64,128,204
Ra 156,242,64512
K 208,168,169
Mvav 96,128,212
X 171
Mvav 128,128,220
Dc 1,8,61,0
Mvav 132,132,220
X 172
Mvav 100,132,212
Ra 128,242,32768
K 209,170,171
Mvav 68,132,204
X 173
Mvav 36,132,196
Dc 1,8,61,1024
Mvav 40,136,196
X 174
Mvav 72,136,204
Ra 132,242,33792
K 216,172,173
Mvav 104,136,212
X 175
Mvav 136,136,220
Dc 1,8,61,2048
Mvav 140,140,220
X 176
Mvav 108,140,212
Ra 136,242,34816
K 217,174,175
Mvav 76,140,204
X 177
Mvav 44,140,196
W 3
Dc 1,8,61,3072
Mvav 48,144,196
X 178
Mvav 80,144,204
s_add_u32 m0, s62, 4096
Ra 140,242,35840
Mvav 112,144,212
K 194,176,177
Mvav 144,144,220
X 179
Mvav 148,148,220
X 180
Mvav 116,148,212
Dc 243,8,61,0
Ra 144,242,36864
Mvav 84,148,204
K 195,178,179
Mvav 52,148,196
X 181
Mvav 56,152,196
X 182
Mvav 88,152,204
Dc 243,8,61,1024
Ra 148,242,37888
Mvav 120,152,212
K 202,180,181
Mvav 152,152,220
X 183
Mvav 156,156,220
X 184
Mvav 124,156,212
Dc 243,8,61,2048
Ra 152,242,38912
Mvav 92,156,204
K 203,182,183
Mvav 60,156,196
W 3
X 185
Cvaa 160,128,64,224
X 186
Mvaa 160,132,68
Dc 243,8,61,3072
Ra 156,242,39936
Cvaa 164,132,84,228
s_add_u32 s61, s61, s63
K 210,184,185
Mvaa 164,128,80
X 187
Cvaa 168,128,96,232
X 188
Mvaa 168,132,100
Cvaa 172,132,116,236
s_xor_b32 s62, s62, 0x10000
K 211,186,187
Mvaa 172,128,112
X 189
Mvaa 160,136,72
Ra 128,242,49152
Ra 132,242,50176
Mvaa 160,140,76
s_add_u32 s64, s64, 1
X 190
Mvaa 164,140,92
K 218,188,189
Mvaa 164,136,88
X 191
Mvaa 168,136,104
Mvaa 168,140,108
X 160
Mvaa 172,140,124
K 219,190,191
Mvaa 172,136,120
W 2
X 161
Cvaa 176,144,64,224
Ra 136,242,51200
Ra 140,242,52224
Mvaa 176,148,68
X 162
Cvaa 180,148,84,228
K 196,160,161
Mvaa 180,144,80
X 163
Cvaa 184,144,96,232
Mvaa 184,148,100
X 164
Cvaa 188,148,116,236
K 197,162,163
Mvaa 188,144,112
X 165
Mvaa 176,152,72
Ra 144,242,53248
Ra 148,242,54272
Mvaa 176,156,76
X 166
Mvaa 180,156,92
K 204,164,165
Mvaa 180,152,88
X 167
Mvaa 184,152,104
Mvaa 184,156,108
X 168
Mvaa 188,156,124
Mvaa 188,152,120
W 2
Ra 152,242,55296
K 205,166,167
Mvav 32,128,192
X 169
Mvav 64,128,200
Ra 156,242,56320
v_xor_b32 v242, 0x10000, v242
Mvav 96,128,208
X 170
Mvav 128,128,216
K 212,168,169
Mvav 132,132,216
X 171
Mvav 100,132,208
X 172
Mvav 68,132,200
Mvav 36,132,192
K 213,170,171
Mvav 40,136,192
X 173
Mvav 72,136,200
X 174
Mvav 104,136,208
Mvav 136,136,216
K 220,172,173
Mvav 140,140,216
X 175
Mvav 108,140,208
X 176
Mvav 76,140,200
s_waitcnt vmcnt(0) lgkmcnt(0)
s_barrier
Mvav 44,140,192
Ra 128,242,40960
K 221,174,175
Mvav 48,144,192
X 177
Mvav 80,144,200
X 178
Mvav 112,144,208
Ra 132,242,41984
Mvav 144,144,216
K 198,176,177
Mvav 148,148,216
X 179
Mvav 116,148,208
X 180
Mvav 84,148,200
Ra 136,242,43008
Mvav 52,148,192
K 199,178,179
Mvav 56,152,192
X 181
Mvav 88,152,200
Ra 140,242,44032
Mvav 120,152,208
X 182
Mvav 152,152,216
K 206,180,181
Mvav 156,156,216
X 183
Mvav 124,156,208
Mvav 92,156,200
K 207,182,183
Mvav 60,156,192
s_cmp_lg_u32 s64, 15
s_cbranch_scc1 .Ll2_34%=
.Lf2_37%=:
W 0
Cvaa 160,128,64,224
.Lh_4_2%=:
s_cbranch_execz .Lh_4_2_1%=
v_bfe_u32 v28, v1, 4, 2
v_lshlrev_b32 v28, 3, v28
Mvaa 160,132,68
Ra 144,242,45056
Ra 148,242,46080
Cvaa 164,132,84,228
Ra 152,242,47104
buffer_load_dwordx2 v[28:29], v28, s[8:11], s54 offen sc1
Mvaa 164,128,80
s_lshl_b32 s92, s58, 2
s_add_u32 s91, s92, s51
s_add_u32 s95, s91, 3
Cvaa 168,128,96,232
s_and_b32 s95, s95, 15
s_mul_i32 s95, s95, 40960
s_mul_i32 s71, s51, 10240
Mvaa 168,132,100
s_add_u32 s71, s71, 0
s_add_u32 m0, s71, s48
s_lshl2_add_u32 s94, s48, s95
Cvaa 172,132,116,236
X 184
Mvaa 172,128,112
X 185
Mvaa 160,136,72
X 186
Mvaa 160,140,76
Ra 156,242,48128
K 214,184,185
Mvaa 164,140,92
X 187
Mvaa 164,136,88
D 16,40,94,0
Ra 128,242,57344
Mvaa 168,136,104
s_add_u32 s95, s91, 5
s_and_b32 s95, s95, 15
s_mul_i32 s95, s95, 40960
Mvaa 168,140,108
s_mul_i32 s71, s51, 10240
s_add_u32 s71, s71, 20480
Ra 132,242,58368
Mvaa 172,140,124
X 188
Mvaa 172,136,120
D 17,40,94,1024
K 215,186,187
v_smfmac_f32_16x16x64_bf16 a[160:163], a[176:179], v[192:199], v240
X 189
v_smfmac_f32_16x16x64_bf16 a[164:167], a[176:179], v[200:207], v240
X 190
K 222,188,189
X 191
s_nop 0
K 223,190,191
v_smfmac_f32_16x16x64_bf16 a[168:171], a[176:179], v[208:215], v240
D 18,40,94,2048
Ra 136,242,59392
)"
R"(v_smfmac_f32_16x16x64_bf16 a[172:175], a[176:179], v[216:223], v240
W 3
X 160
Cvaa 176,144,64,224
X 161
Mvaa 176,148,68
X 162
Cvaa 180,148,84,228
D 19,40,94,3072
K 192,160,161
Mvaa 180,144,80
s_add_u32 m0, m0, 4096
X 163
Cvaa 184,144,96,232
X 164
Mvaa 184,148,100
Ra 140,242,60416
K 193,162,163
Cvaa 188,148,116,236
buffer_load_dwordx4 v20, s[40:43], s94 offen lds
Mvaa 188,144,112
s_add_u32 m0, s71, s48
s_lshl2_add_u32 s94, s48, s95
Ra 144,242,61440
Mvaa 176,152,72
X 165
Mvaa 176,156,76
X 166
Mvaa 180,156,92
D 16,40,94,0
K 200,164,165
Mvaa 180,152,88
X 167
Mvaa 184,152,104
X 172
Mvaa 184,156,108
D 17,40,94,1024
K 201,166,167
Mvaa 188,156,124
X 173
Mvaa 188,152,120
W 1
Ra 148,242,62464
Ra 152,242,63488
Mvav 32,128,196
X 174
Mvav 64,128,204
D 18,40,94,2048
K 216,172,173
Mvav 96,128,212
X 175
Mvav 128,128,220
X 168
Mvav 132,132,220
s_mov_b32 s18, 40960
D 19,40,94,3072
K 217,174,175
Mvav 100,132,212
s_add_u32 m0, m0, 4096
X 169
Mvav 68,132,204
s_mov_b32 s19, 73728
X 170
Mvav 36,132,196
s_bitcmp1_b32 s14, 0
s_cselect_b32 s18, 49152, s18
s_cselect_b32 s19, 98304, s19
Ra 156,242,64512
.Lh_4_3%=:
s_cbranch_execz .Lh_4_3_1%=
K 208,168,169
Mvav 40,136,196
s_cmp_lt_u32 s14, 2
s_cselect_b32 s20, s18, s19
s_cselect_b32 s21, s19, s18
X 171
Mvav 72,136,204
X 184
Mvav 104,136,212
buffer_load_dwordx4 v20, s[40:43], s94 offen lds
K 209,170,171
Mvav 136,136,220
s_waitcnt vmcnt(10)
v_cmp_eq_u64 vcc, s[12:13], v[28:29]
s_cmp_eq_u64 vcc, -1
s_cselect_b32 s46, 79691776, 0
s_add_u32 s81, s58, 0
s_and_b32 s81, s81, 3
Mvav 140,140,220
s_lshl_b32 s81, s81, 12
s_add_u32 s81, s81, s49
Ra 128,242,49152
Mvav 108,140,212
X 185
Mvav 76,140,204
X 186
Mvav 44,140,196
W 1
buffer_load_dwordx4 a[184:187], v1, s[44:47], s81 offen offset:0 sc1
K 210,184,185
Mvav 48,144,196
s_add_u32 s82, s81, 131072
X 187
Mvav 80,144,204
v_add_u32 v2, s20, v1
v_add_u32 v3, s21, v1
Mvav 112,144,212
s_lshl_b32 s18, s14, 9
X 188
Mvav 144,144,220
Ra 132,242,50176
K 211,186,187
Mvav 148,148,220
buffer_load_dwordx4 a[200:203], v1, s[44:47], s82 offen offset:0 sc1
Ra 136,242,51200
Mvav 116,148,212
X 189
Mvav 84,148,204
v_lshrrev_b32 v232, 1, v1
v_add_u32 v232, s18, v232
Mvav 52,148,196
X 190
Mvav 56,152,196
K 218,188,189
v_bfe_u32 v12, v1, 6, 2
Mvav 88,152,204
buffer_load_dwordx4 a[188:191], v1, s[44:47], s81 offen offset:1024 sc1
Ra 140,242,52224
Mvav 120,152,212
X 191
Mvav 152,152,220
v_lshrrev_b32 v13, 8, v1
v_bfe_u32 v14, v1, 4, 1
Mvav 156,156,220
X 176
Mvav 124,156,212
K 219,190,191
v_lshlrev_b32 v14, 4, v14
Mvav 92,156,204
X 177
Mvav 60,156,196
buffer_load_dwordx4 a[204:207], v1, s[44:47], s82 offen offset:1024 sc1
v_mov_b32 v15, 0x3f80
Maav 168,180,208
X 178
K 194,176,177
X 179
X 180
K 195,178,179
Maav 172,180,216
X 181
X 182
K 202,180,181
X 183
s_nop 0
K 203,182,183
Maav 160,180,192
Ra 144,242,53248
v_lshlrev_b32 v15, v14, v15
Maav 164,180,200
W 1
buffer_load_dwordx4 a[192:195], v1, s[44:47], s81 offen offset:2048 sc1
v_bfe_u32 v14, v1, 5, 1
Mvav 96,128,208
Ra 148,242,54272
v_xor_b32 v27, v12, v13
v_lshl_or_b32 v27, v27, 1, v14
Mvav 128,128,216
v_cmp_eq_u32_e64 s[86:87], 0, v27
v_mov_b32 v226, 0
s_nop 0
v_cndmask_b32_e64 v224, 0, v15, s[86:87]
Mvav 132,132,216
Ra 152,242,55296
Ra 156,242,56320
buffer_load_dwordx4 a[208:211], v1, s[44:47], s82 offen offset:2048 sc1
Mvav 100,132,208
v_mov_b32 v228, 0
v_mov_b32 v230, v224
v_cmp_eq_u32_e64 s[86:87], 1, v27
Mvav 104,136,208
v_mov_b32 v227, 0
Mvav 136,136,216
buffer_load_dwordx4 a[196:199], v1, s[44:47], s81 offen offset:3072 sc1
v_cndmask_b32_e64 v225, 0, v15, s[86:87]
v_mov_b32 v229, 0
Mvav 140,140,216
v_mov_b32 v231, v225
G 4,160
G 8,161
Mvav 108,140,208
W 0
G 5,164
G 9,165
G 6,168
Mvav 112,144,208
buffer_load_dwordx4 a[212:215], v1, s[44:47], s82 offen offset:3072 sc1
s_add_u32 s81, s58, 1
s_and_b32 s81, s81, 3
s_lshl_b32 s81, s81, 12
Mvav 144,144,216
s_add_u32 s81, s81, s49
G 10,169
G 7,172
G 11,173
Mvav 148,148,216
A 4,4,8
A 5,5,9
A 6,6,10
Mvav 116,148,208
buffer_load_dwordx4 a[216:219], v1, s[44:47], s81 offen offset:0 sc1
s_add_u32 s82, s81, 131072
A 7,7,11
ds_write_b64 v232, v[6:7] offset:57344
Mvav 120,152,208
A 8,4,5
A 9,6,7
A 8,8,9
Mvav 152,152,216
v_cmp_ngt_f32_e64 s[86:87], s90, v8
s_lshl_b32 s19, s14, 2
v_mov_b32 v9, s19
s_or_b64 s[88:89], s[88:89], s[86:87]
Mvav 156,156,216
s_and_b64 s[88:89], s[88:89], s[24:25]
s_cselect_b32 s18, 1, 0
buffer_load_dwordx4 a[232:235], v1, s[44:47], s82 offen offset:0 sc1
v_mov_b32 v8, s18
ds_write_b32 v9, v8 offset:59392
Mvav 124,156,208
v_xor_b32 v10, 1024, v232
v_bfe_u32 v11, v1, 2, 4
K 96,96,97
Mvav 32,128,192
K 97,98,99
K 98,100,101
K 99,102,103
Mvav 64,128,200
ds_write_b128 v2, v[96:99] offset:0
K 128,128,129
K 129,130,131
Mvav 68,132,200
K 130,132,133
K 131,134,135
ds_write_b128 v2, v[128:131] offset:4096
Mvav 36,132,192
buffer_load_dwordx4 a[220:223], v1, s[44:47], s81 offen offset:1024 sc1
K 104,104,105
K 105,106,107
Mvav 40,136,192
K 106,108,109
K 107,110,111
ds_write_b128 v2, v[104:107] offset:1024
Mvav 72,136,200
K 136,136,137
K 137,138,139
K 138,140,141
Mvav 76,140,200
buffer_load_dwordx4 a[236:239], v1, s[44:47], s82 offen offset:1024 sc1
K 139,142,143
ds_write_b128 v2, v[136:139] offset:5120
Mvav 44,140,192
K 112,112,113
K 113,114,115
K 114,116,117
Mvav 48,144,192
K 115,118,119
ds_write_b128 v2, v[112:115] offset:2048
K 144,144,145
Mvav 80,144,200
K 145,146,147
K 146,148,149
K 147,150,151
Mvav 84,148,200
ds_write_b128 v2, v[144:147] offset:6144
buffer_load_dwordx4 a[224:227], v1, s[44:47], s81 offen offset:2048 sc1
K 120,120,121
Mvav 52,148,192
K 121,122,123
K 122,124,125
K 123,126,127
Mvav 56,152,192
ds_write_b128 v2, v[120:123] offset:3072
K 152,152,153
K 153,154,155
Mvav 88,152,200
buffer_load_dwordx4 a[240:243], v1, s[44:47], s82 offen offset:2048 sc1
K 154,156,157
K 155,158,159
Mvav 92,156,200
ds_write_b128 v2, v[152:155] offset:7168
Mvav 60,156,192
buffer_load_dwordx4 a[228:231], v1, s[44:47], s81 offen offset:3072 sc1
buffer_load_dwordx4 a[244:247], v1, s[44:47], s82 offen offset:3072 sc1
s_branch .Lend_39%=
.p2align 12
.Lfs_38%=:
v_smfmac_f32_16x16x64_bf16 a[160:163], a[176:179], v[192:199], v240
s_mov_b32 s46, 0
s_lshl_b32 s92, s58, 2
s_add_u32 s91, s92, s51
s_add_u32 s95, s91, 3
s_and_b32 s95, s95, 15
s_mul_i32 s95, s95, 40960
s_mul_i32 s71, s51, 10240
v_smfmac_f32_16x16x64_bf16 a[164:167], a[176:179], v[200:207], v240
s_add_u32 s71, s71, 0
s_add_u32 m0, s71, s48
s_lshl2_add_u32 s94, s48, s95
Ra 144,242,45056
Ra 148,242,46080
Ra 152,242,47104
Ra 156,242,48128
v_smfmac_f32_16x16x64_bf16 a[168:171], a[176:179], v[208:215], v240
D 16,40,94,0
s_add_u32 s95, s91, 5
s_and_b32 s95, s95, 15
s_mul_i32 s95, s95, 40960
s_mul_i32 s71, s51, 10240
s_add_u32 s71, s71, 20480
v_smfmac_f32_16x16x64_bf16 a[172:175], a[176:179], v[216:223], v240
W 4
Cvaa 168,128,96,232
v_mov_b32 v192, 0
G 12,160
Mvaa 168,132,100
D 17,40,94,1024
G 4,161
A 12,12,4
v_cmp_nlt_f32_e64 s[86:87], v12, s90
v_rcp_f32 v12, v12
s_or_b64 s[88:89], s[88:89], s[86:87]
Cvaa 172,132,116,236
v_accvgpr_write_b32 a160, 0
v_accvgpr_write_b32 a161, 0
v_accvgpr_write_b32 a162, 0
v_accvgpr_write_b32 a163, 0
G 13,164
G 5,165
Mvaa 172,128,112
v_mov_b32 v193, 0
A 13,13,5
v_cmp_nlt_f32_e64 s[86:87], v13, s90
v_rcp_f32 v13, v13
s_or_b64 s[88:89], s[88:89], s[86:87]
v_accvgpr_write_b32 a164, 0
Mvaa 168,136,104
v_accvgpr_write_b32 a165, 0
v_accvgpr_write_b32 a166, 0
v_accvgpr_write_b32 a167, 0
U 32,32,12
U 33,33,12
U 34,34,12
Mvaa 168,140,108
v_mov_b32 v200, 0
U 35,35,12
U 36,36,12
U 37,37,12
U 38,38,12
U 39,39,12
Mvaa 172,140,124
Ra 128,242,16384
Ra 132,242,17408
K 32,32,33
K 33,34,35
K 34,36,37
K 35,38,39
s_add_u32 s93, s67, 0
Mvaa 172,136,120
Ra 136,242,18432
Ra 140,242,19456
D 18,40,94,2048
v_mov_b32 v201, 0
W 4
Cvaa 188,144,112,236
Ra 144,242,20480
v_cndmask_b32_e64 v168, v241, v168, s[72:73]
v_cndmask_b32_e64 v169, v241, v169, s[74:75]
v_cndmask_b32_e64 v170, v241, v170, s[76:77]
Mvaa 188,148,116
buffer_store_dwordx4 v[32:35], v1, s[8:11], s93 offen offset:0 sc1
U 40,40,12
U 41,41,12
U 42,42,12
U 43,43,12
U 44,44,12
Mvaa 188,152,120
U 45,45,12
U 46,46,12
U 47,47,12
K 40,40,41
K 41,42,43
K 42,44,45
Mvaa 188,156,124
W 1
K 43,46,47
buffer_store_dwordx4 v[40:43], v1, s[8:11], s93 offen offset:1024 sc1
U 48,48,12
U 49,49,12
U 50,50,12
)"
R"(U 51,51,12
Mvav 96,128,212
U 52,52,12
U 53,53,12
U 54,54,12
U 55,55,12
K 48,48,49
K 49,50,51
Mvav 128,128,220
K 50,52,53
K 51,54,55
buffer_store_dwordx4 v[48:51], v1, s[8:11], s93 offen offset:2048 sc1
U 56,56,12
U 57,57,12
U 58,58,12
Mvav 132,132,220
Ra 148,242,21504
Ra 152,242,22528
Ra 156,242,23552
U 59,59,12
U 60,60,12
U 61,61,12
Mvav 100,132,212
D 19,40,94,3072
s_add_u32 m0, m0, 4096
U 62,62,12
U 63,63,12
K 56,56,57
K 57,58,59
K 58,60,61
Mvav 104,136,212
K 59,62,63
v_cndmask_b32_e64 v171, v241, v171, s[78:79]
X 168
X 169
Mvav 136,136,220
buffer_store_dwordx4 v[56:59], v1, s[8:11], s93 offen offset:3072 sc1
Ra 128,242,49152
G 14,168
G 6,169
A 14,14,6
v_cmp_nlt_f32_e64 s[86:87], v14, s90
Mvav 140,140,220
v_rcp_f32 v14, v14
s_or_b64 s[88:89], s[88:89], s[86:87]
v_accvgpr_write_b32 a168, 0
v_accvgpr_write_b32 a169, 0
v_accvgpr_write_b32 a170, 0
v_accvgpr_write_b32 a171, 0
Mvav 108,140,212
W 1
G 15,172
G 7,173
A 15,15,7
v_cmp_nlt_f32_e64 s[86:87], v15, s90
v_rcp_f32 v15, v15
Mvav 112,144,212
s_or_b64 s[88:89], s[88:89], s[86:87]
buffer_load_dwordx4 v20, s[40:43], s94 offen lds
v_accvgpr_write_b32 a172, 0
v_accvgpr_write_b32 a173, 0
v_accvgpr_write_b32 a174, 0
v_accvgpr_write_b32 a175, 0
U 64,64,13
Mvav 144,144,220
s_add_u32 m0, s71, s48
s_lshl2_add_u32 s94, s48, s95
U 65,65,13
U 66,66,13
U 67,67,13
U 68,68,13
U 69,69,13
Mvav 148,148,220
U 70,70,13
U 71,71,13
K 64,64,65
K 65,66,67
K 66,68,69
K 67,70,71
s_add_u32 s93, s67, 131072
Mvav 116,148,212
buffer_store_dwordx4 v[64:67], v1, s[8:11], s93 offen offset:0 sc1
U 72,72,13
U 73,73,13
U 74,74,13
U 75,75,13
U 76,76,13
Mvav 120,152,212
Ra 132,242,50176
Ra 136,242,51200
Ra 140,242,52224
U 77,77,13
U 78,78,13
U 79,79,13
Mvav 152,152,220
K 72,72,73
K 73,74,75
K 74,76,77
K 75,78,79
buffer_store_dwordx4 v[72:75], v1, s[8:11], s93 offen offset:1024 sc1
U 80,80,13
Mvav 156,156,220
X 170
K 208,168,169
X 171
X 172
K 209,170,171
X 173
X 174
K 216,172,173
X 175
v_mov_b32 v194, 0
K 217,174,175
v_mov_b32 v195, 0
v_mov_b32 v202, 0
v_mov_b32 v203, 0
v_mov_b32 v210, 0
v_mov_b32 v211, 0
Mvav 124,156,212
v_cndmask_b32_e64 v188, v241, v188, s[72:73]
v_cndmask_b32_e64 v189, v241, v189, s[74:75]
v_cndmask_b32_e64 v190, v241, v190, s[76:77]
v_cndmask_b32_e64 v191, v241, v191, s[78:79]
X 188
X 189
X 190
K 218,188,189
X 191
s_nop 0
K 219,190,191
Maav 168,180,208
D 16,40,94,0
U 81,81,13
U 82,82,13
U 83,83,13
U 84,84,13
U 85,85,13
Maav 172,180,216
W 0
U 86,86,13
U 87,87,13
K 80,80,81
K 81,82,83
K 82,84,85
K 83,86,87
Zvav 32,128,192
buffer_store_dwordx4 v[80:83], v1, s[8:11], s93 offen offset:2048 sc1
U 88,88,13
U 89,89,13
U 90,90,13
U 91,91,13
U 92,92,13
Zvav 64,128,200
U 93,93,13
U 94,94,13
U 95,95,13
K 88,88,89
K 89,90,91
K 90,92,93
K 91,94,95
buffer_store_dwordx4 v[88:91], v1, s[8:11], s93 offen offset:3072 sc1
U 96,96,14
U 97,97,14
U 98,98,14
U 99,99,14
U 100,100,14
U 101,101,14
U 102,102,14
U 103,103,14
K 96,96,97
K 97,98,99
K 98,100,101
K 99,102,103
s_add_u32 s93, s67, 262144
buffer_store_dwordx4 v[96:99], v1, s[8:11], s93 offen offset:0 sc1
s_nop 1
Zvav 96,128,208
U 104,104,14
U 105,105,14
U 106,106,14
U 107,107,14
U 108,108,14
U 109,109,14
U 110,110,14
U 111,111,14
K 104,104,105
K 105,106,107
K 106,108,109
K 107,110,111
buffer_store_dwordx4 v[104:107], v1, s[8:11], s93 offen offset:1024 sc1
U 112,112,14
U 113,113,14
U 114,114,14
U 115,115,14
U 116,116,14
U 117,117,14
U 118,118,14
U 119,119,14
K 112,112,113
K 113,114,115
K 114,116,117
K 115,118,119
buffer_store_dwordx4 v[112:115], v1, s[8:11], s93 offen offset:2048 sc1
U 120,120,14
U 121,121,14
U 122,122,14
U 123,123,14
U 124,124,14
U 125,125,14
U 126,126,14
U 127,127,14
K 120,120,121
K 121,122,123
K 122,124,125
K 123,126,127
buffer_store_dwordx4 v[120:123], v1, s[8:11], s93 offen offset:3072 sc1
U 128,128,15
U 129,129,15
U 130,130,15
U 131,131,15
U 132,132,15
U 133,133,15
U 134,134,15
U 135,135,15
K 128,128,129
K 129,130,131
K 130,132,133
K 131,134,135
s_add_u32 s93, s67, 393216
buffer_store_dwordx4 v[128:131], v1, s[8:11], s93 offen offset:0 sc1
s_nop 1
Zvav 128,128,216
Ra 144,242,53248
U 136,136,15
U 137,137,15
U 138,138,15
U 139,139,15
U 140,140,15
Zvav 132,132,216
D 17,40,94,1024
U 141,141,15
U 142,142,15
U 143,143,15
K 136,136,137
K 137,138,139
Zvav 100,132,208
Ra 148,242,54272
Ra 152,242,55296
Ra 156,242,56320
K 138,140,141
K 139,142,143
Zvav 68,132,200
D 18,40,94,2048
Zvav 36,132,192
Zvav 40,136,192
D 19,40,94,3072
s_add_u32 m0, m0, 4096
Zvav 72,136,200
Zvav 104,136,208
buffer_store_dwordx4 v[136:139], v1, s[8:11], s93 offen offset:1024 sc1
U 144,144,15
U 145,145,15
U 146,146,15
U 147,147,15
U 148,148,15
Zvav 136,136,216
U 149,149,15
U 150,150,15
U 151,151,15
K 144,144,145
K 145,146,147
K 146,148,149
Zvav 140,140,216
K 147,150,151
buffer_store_dwordx4 v[144:147], v1, s[8:11], s93 offen offset:2048 sc1
U 152,152,15
U 153,153,15
U 154,154,15
U 155,155,15
Zvav 108,140,208
U 156,156,15
U 157,157,15
U 158,158,15
U 159,159,15
K 152,152,153
K 153,154,155
Zvav 76,140,200
buffer_load_dwordx4 v20, s[40:43], s94 offen lds
K 154,156,157
K 155,158,159
Zvav 44,140,192
W 0
Zvav 48,144,192
buffer_store_dwordx4 v[152:155], v1, s[8:11], s93 offen offset:3072 sc1
s_mov_b32 s18, 40960
s_mov_b32 s19, 73728
s_bitcmp1_b32 s14, 0
s_cselect_b32 s18, 49152, s18
s_cselect_b32 s19, 98304, s19
s_cmp_lt_u32 s14, 2
s_cselect_b32 s20, s18, s19
s_cselect_b32 s21, s19, s18
v_add_u32 v2, s20, v1
v_add_u32 v3, s21, v1
Zvav 80,144,200
s_lshl_b32 s18, s14, 9
v_lshrrev_b32 v232, 1, v1
v_add_u32 v232, s18, v232
v_bfe_u32 v12, v1, 6, 2
v_lshrrev_b32 v13, 8, v1
v_bfe_u32 v14, v1, 4, 1
v_lshlrev_b32 v14, 4, v14
Zvav 112,144,208
v_mov_b32 v15, 0x3f80
v_lshlrev_b32 v15, v14, v15
v_bfe_u32 v14, v1, 5, 1
v_xor_b32 v27, v12, v13
v_lshl_or_b32 v27, v27, 1, v14
v_cmp_eq_u32_e64 s[86:87], 0, v27
Zvav 144,144,216
v_mov_b32 v226, 0
Zvav 148,148,216
v_cndmask_b32_e64 v224, 0, v15, s[86:87]
v_mov_b32 v228, 0
v_mov_b32 v230, v224
v_cmp_eq_u32_e64 s[86:87], 1, v27
v_mov_b32 v227, 0
s_nop 0
v_cndmask_b32_e64 v225, 0, v15, s[86:87]
Zvav 116,148,208
v_mov_b32 v229, 0
v_mov_b32 v231, v225
G 4,160
G 8,161
G 5,164
G 9,165
Zvav 84,148,200
G 6,168
G 10,169
G 7,172
G 11,173
A 4,4,8
A 5,5,9
Zvav 52,148,192
A 6,6,10
A 7,7,11
ds_write_b64 v232, v[6:7] offset:57344
A 8,4,5
A 9,6,7
A 8,8,9
Zvav 56,152,192
v_cmp_ngt_f32_e64 s[86:87], s90, v8
s_lshl_b32 s19, s14, 2
v_mov_b32 v9, s19
s_or_b64 s[88:89], s[88:89], s[86:87]
s_and_b64 s[88:89], s[88:89], s[24:25]
s_cselect_b32 s18, 1, 0
v_mov_b32 v8, s18
ds_write_b32 v9, v8 offset:59392
Zvav 88,152,200
v_xor_b32 v10, 1024, v232
v_bfe_u32 v11, v1, 2, 4
K 96,96,97
K 97,98,99
K 98,100,101
K 99,102,103
Zvav 120,152,208
ds_write_b128 v2, v[96:99] offset:0
K 128,128,129
K 129,130,131
K 130,132,133
K 131,134,135
ds_write_b128 v2, v[128:131] offset:4096
Zvav 152,152,216
K 104,104,105
K 105,106,107
K 106,108,109
K 107,110,111
ds_write_b128 v2, v[104:107] offset:1024
K 136,136,137
Zvav 156,156,216
K 137,138,139
K 138,140,141
K 139,142,143
ds_write_b128 v2, v[136:139] offset:5120
K 112,112,113
K 113,114,115
Zvav 124,156,208
K 114,116,117
K 115,118,119
ds_write_b128 v2, v[112:115] offset:2048
K 144,144,145
K 145,146,147
K 146,148,149
Zvav 92,156,200
K 147,150,151
ds_write_b128 v2, v[144:147] offset:6144
Zvav 60,156,192
K 120,120,121
K 121,122,123
)"
R"(K 122,124,125
K 123,126,127
ds_write_b128 v2, v[120:123] offset:3072
K 152,152,153
K 153,154,155
K 154,156,157
K 155,158,159
ds_write_b128 v2, v[152:155] offset:7168
s_waitcnt vmcnt(0)
s_and_b64 s[86:87], s[88:89], s[24:25]
s_cbranch_scc1 .LL_46%=
v_mov_b64 v[28:29], s[12:13]
s_mov_b64 exec, 1
buffer_store_dwordx2 v[28:29], off, s[8:11], s69 sc1
s_mov_b64 exec, -1
.LL_46%=:
s_mov_b32 s70, 0
s_branch .Lend_39%=
.p2align 12
.Lpd_28%=:
v_pk_mul_f32 v[32:33], v[32:33], s[22:23]
v_pk_mul_f32 v[34:35], v[34:35], s[22:23]
v_pk_mul_f32 v[36:37], v[36:37], s[22:23]
v_pk_mul_f32 v[38:39], v[38:39], s[22:23]
v_pk_mul_f32 v[64:65], v[64:65], s[22:23]
v_pk_mul_f32 v[66:67], v[66:67], s[22:23]
v_pk_mul_f32 v[68:69], v[68:69], s[22:23]
v_pk_mul_f32 v[70:71], v[70:71], s[22:23]
v_pk_mul_f32 v[96:97], v[96:97], s[22:23]
v_pk_mul_f32 v[98:99], v[98:99], s[22:23]
v_pk_mul_f32 v[100:101], v[100:101], s[22:23]
v_pk_mul_f32 v[102:103], v[102:103], s[22:23]
v_pk_mul_f32 v[128:129], v[128:129], s[22:23]
v_pk_mul_f32 v[130:131], v[130:131], s[22:23]
v_pk_mul_f32 v[132:133], v[132:133], s[22:23]
v_pk_mul_f32 v[134:135], v[134:135], s[22:23]
v_pk_mul_f32 v[40:41], v[40:41], s[22:23]
v_pk_mul_f32 v[42:43], v[42:43], s[22:23]
v_pk_mul_f32 v[44:45], v[44:45], s[22:23]
v_pk_mul_f32 v[46:47], v[46:47], s[22:23]
v_pk_mul_f32 v[72:73], v[72:73], s[22:23]
v_pk_mul_f32 v[74:75], v[74:75], s[22:23]
v_pk_mul_f32 v[76:77], v[76:77], s[22:23]
v_pk_mul_f32 v[78:79], v[78:79], s[22:23]
v_pk_mul_f32 v[104:105], v[104:105], s[22:23]
v_pk_mul_f32 v[106:107], v[106:107], s[22:23]
v_pk_mul_f32 v[108:109], v[108:109], s[22:23]
v_pk_mul_f32 v[110:111], v[110:111], s[22:23]
v_pk_mul_f32 v[136:137], v[136:137], s[22:23]
v_pk_mul_f32 v[138:139], v[138:139], s[22:23]
v_pk_mul_f32 v[140:141], v[140:141], s[22:23]
v_pk_mul_f32 v[142:143], v[142:143], s[22:23]
v_pk_mul_f32 v[48:49], v[48:49], s[22:23]
v_pk_mul_f32 v[50:51], v[50:51], s[22:23]
v_pk_mul_f32 v[52:53], v[52:53], s[22:23]
v_pk_mul_f32 v[54:55], v[54:55], s[22:23]
v_pk_mul_f32 v[80:81], v[80:81], s[22:23]
v_pk_mul_f32 v[82:83], v[82:83], s[22:23]
v_pk_mul_f32 v[84:85], v[84:85], s[22:23]
v_pk_mul_f32 v[86:87], v[86:87], s[22:23]
v_pk_mul_f32 v[112:113], v[112:113], s[22:23]
v_pk_mul_f32 v[114:115], v[114:115], s[22:23]
v_pk_mul_f32 v[116:117], v[116:117], s[22:23]
v_pk_mul_f32 v[118:119], v[118:119], s[22:23]
v_pk_mul_f32 v[144:145], v[144:145], s[22:23]
v_pk_mul_f32 v[146:147], v[146:147], s[22:23]
v_pk_mul_f32 v[148:149], v[148:149], s[22:23]
v_pk_mul_f32 v[150:151], v[150:151], s[22:23]
v_pk_mul_f32 v[56:57], v[56:57], s[22:23]
v_pk_mul_f32 v[58:59], v[58:59], s[22:23]
v_pk_mul_f32 v[60:61], v[60:61], s[22:23]
v_pk_mul_f32 v[62:63], v[62:63], s[22:23]
v_pk_mul_f32 v[88:89], v[88:89], s[22:23]
v_pk_mul_f32 v[90:91], v[90:91], s[22:23]
v_pk_mul_f32 v[92:93], v[92:93], s[22:23]
v_pk_mul_f32 v[94:95], v[94:95], s[22:23]
v_pk_mul_f32 v[120:121], v[120:121], s[22:23]
v_pk_mul_f32 v[122:123], v[122:123], s[22:23]
v_pk_mul_f32 v[124:125], v[124:125], s[22:23]
v_pk_mul_f32 v[126:127], v[126:127], s[22:23]
v_pk_mul_f32 v[152:153], v[152:153], s[22:23]
v_pk_mul_f32 v[154:155], v[154:155], s[22:23]
v_pk_mul_f32 v[156:157], v[156:157], s[22:23]
v_pk_mul_f32 v[158:159], v[158:159], s[22:23]
K 32,32,33
K 33,34,35
K 34,36,37
K 35,38,39
K 40,40,41
K 41,42,43
K 42,44,45
K 43,46,47
W 0
Zvav 196,128,32
s_add_u32 m0, s62, 0
Ra 144,242,4096
Ra 148,242,5120
Ra 152,242,6144
Ra 156,242,7168
Mvav 196,132,40
K 64,64,65
K 65,66,67
K 66,68,69
K 67,70,71
K 72,72,73
K 73,74,75
K 74,76,77
K 75,78,79
s_nop 1
Zvav 204,132,72
Dc 1,8,61,0
K 96,96,97
K 97,98,99
K 98,100,101
Mvav 204,128,64
K 99,102,103
K 104,104,105
K 105,106,107
K 106,108,109
K 107,110,111
Zvav 212,128,96
Dc 1,8,61,1024
K 128,128,129
K 129,130,131
K 130,132,133
Mvav 212,132,104
K 131,134,135
K 136,136,137
K 137,138,139
K 138,140,141
K 139,142,143
s_nop 1
Zvav 220,132,136
Dc 1,8,61,2048
K 48,48,49
K 49,50,51
K 50,52,53
Mvav 220,128,128
K 51,54,55
K 56,56,57
K 57,58,59
K 58,60,61
K 59,62,63
Mvav 196,136,48
Dc 1,8,61,3072
s_add_u32 m0, s62, 4096
Ra 128,242,8192
Ra 132,242,9216
K 80,80,81
Mvav 196,140,56
K 81,82,83
K 82,84,85
K 83,86,87
K 88,88,89
K 89,90,91
K 90,92,93
K 91,94,95
s_nop 1
Mvav 204,140,88
Dc 243,8,61,0
K 112,112,113
K 113,114,115
K 114,116,117
Mvav 204,136,80
K 115,118,119
K 120,120,121
K 121,122,123
K 122,124,125
K 123,126,127
Mvav 212,136,112
Dc 243,8,61,1024
K 144,144,145
K 145,146,147
K 146,148,149
Mvav 212,140,120
K 147,150,151
K 152,152,153
K 153,154,155
K 154,156,157
K 155,158,159
s_nop 1
Mvav 220,140,152
Dc 243,8,61,2048
v_accvgpr_write_b32 a0, v32
v_accvgpr_write_b32 a1, v33
v_accvgpr_write_b32 a2, v34
Mvav 220,136,144
W 2
Dc 243,8,61,3072
s_add_u32 m0, s62, 8192
v_accvgpr_write_b32 a3, v35
v_accvgpr_write_b32 a4, v40
Zvav 68,144,64
Dc 244,8,61,0
v_accvgpr_write_b32 a5, v41
v_accvgpr_write_b32 a6, v42
v_accvgpr_write_b32 a7, v43
Mvav 68,148,72
Dc 244,8,61,1024
v_accvgpr_write_b32 a8, v48
v_accvgpr_write_b32 a9, v49
v_accvgpr_write_b32 a10, v50
Zvav 100,148,104
Dc 244,8,61,2048
Ra 136,242,10240
Ra 140,242,11264
v_accvgpr_write_b32 a11, v51
Mvav 100,144,96
Dc 244,8,61,3072
s_add_u32 m0, s62, 12288
v_accvgpr_write_b32 a12, v56
v_accvgpr_write_b32 a13, v57
v_accvgpr_write_b32 a14, v58
Zvav 132,144,128
Dc 245,8,61,0
v_accvgpr_write_b32 a15, v59
v_accvgpr_write_b32 a16, v64
v_cndmask_b32_e64 v196, v241, v196, s[72:73]
Mvav 132,148,136
Dc 245,8,61,1024
v_cndmask_b32_e64 v197, v241, v197, s[74:75]
v_cndmask_b32_e64 v198, v241, v198, s[76:77]
v_cndmask_b32_e64 v199, v241, v199, s[78:79]
Mvav 68,152,80
Dc 245,8,61,2048
v_max3_f32 v4, v196, v197, v198
v_max3_f32 v4, v4, v199, v252
v_max3_f32 v5, v204, v205, v206
Mvav 68,156,88
v_max3_f32 v5, v5, v207, v253
v_max3_f32 v6, v212, v213, v214
v_max3_f32 v6, v6, v215, v254
v_max3_f32 v7, v220, v221, v222
Mvav 100,156,120
v_max3_f32 v7, v7, v223, v255
v_mov_b32 v8, v4
v_mov_b32 v9, v5
v_mov_b32 v10, v6
Mvav 100,152,112
v_mov_b32 v11, v7
v_permlane32_swap_b32 v4, v8
v_permlane32_swap_b32 v5, v9
v_accvgpr_write_b32 a17, v65
Mvav 132,152,144
v_permlane32_swap_b32 v6, v10
v_permlane32_swap_b32 v7, v11
v_max_f32 v4, v4, v8
v_max_f32 v5, v5, v9
v_max_f32 v6, v6, v10
v_max_f32 v7, v7, v11
v_mov_b32 v8, v4
v_mov_b32 v9, v5
v_mov_b32 v10, v6
v_mov_b32 v11, v7
v_permlane16_swap_b32 v4, v8
v_permlane16_swap_b32 v5, v9
v_permlane16_swap_b32 v6, v10
v_permlane16_swap_b32 v7, v11
v_max_f32 v4, v4, v8
v_max_f32 v5, v5, v9
v_max_f32 v6, v6, v10
v_max_f32 v7, v7, v11
v_sub_f32 v4, 0, v4
v_sub_f32 v5, 0, v5
v_sub_f32 v6, 0, v6
v_sub_f32 v7, 0, v7
v_mov_b32 v224, v4
v_mov_b32 v225, v4
v_mov_b32 v226, v4
v_mov_b32 v227, v4
v_mov_b32 v228, v5
v_mov_b32 v229, v5
v_mov_b32 v230, v5
v_mov_b32 v231, v5
v_mov_b32 v232, v6
v_mov_b32 v233, v6
v_mov_b32 v234, v6
v_mov_b32 v235, v6
v_mov_b32 v236, v7
v_mov_b32 v237, v7
v_mov_b32 v238, v7
v_mov_b32 v239, v7
Mvav 132,156,152
W 0
Dc 245,8,61,3072
v_accvgpr_write_b32 a18, v66
v_accvgpr_write_b32 a19, v67
v_accvgpr_write_b32 a20, v72
Cvav 168,128,96,232
s_add_u32 s61, s61, s63
s_add_u32 s36, s29, 65536
v_accvgpr_write_b32 a21, v73
v_accvgpr_write_b32 a22, v74
v_accvgpr_write_b32 a23, v75
Mvav 168,132,104
s_and_b32 s80, s14, 1
buffer_store_dwordx4 a[192:195], v1, s[8:11], s36 offen offset:0 sc1
v_accvgpr_write_b32 a24, v80
v_accvgpr_write_b32 a25, v81
v_accvgpr_write_b32 a26, v82
Cvav 172,132,136,236
s_lshl_b32 s80, s80, 14
Ra 144,242,12288
Ra 148,242,13312
Ra 152,242,14336
Ra 156,242,15360
Mvav 172,128,128
s_add_u32 s80, s80, 98304
v_accvgpr_write_b32 a27, v83
v_accvgpr_write_b32 a28, v88
v_accvgpr_write_b32 a29, v89
v_accvgpr_write_b32 a30, v90
Mvav 168,136,112
buffer_store_dwordx4 a[200:203], v1, s[8:11], s36 offen offset:1024 sc1
v_accvgpr_write_b32 a31, v91
v_accvgpr_write_b32 a32, v96
v_accvgpr_write_b32 a33, v97
Mvav 168,140,120
v_accvgpr_write_b32 a34, v98
v_accvgpr_write_b32 a35, v99
v_accvgpr_write_b32 a36, v104
v_accvgpr_write_b32 a37, v105
Mvav 172,140,152
Ra 128,242,24576
Ra 132,242,25600
buffer_store_dwordx4 a[208:211], v1, s[8:11], s36 offen offset:2048 sc1
v_accvgpr_write_b32 a38, v106
Mvav 172,136,144
Ra 136,242,26624
Ra 140,242,27648
v_accvgpr_write_b32 a39, v107
W 4
Cvav 188,144,128,236
v_accvgpr_write_b32 a40, v112
v_accvgpr_write_b32 a41, v113
v_accvgpr_write_b32 a42, v114
v_accvgpr_write_b32 a43, v115
v_accvgpr_write_b32 a44, v120
v_accvgpr_write_b32 a45, v121
v_accvgpr_write_b32 a46, v122
v_accvgpr_write_b32 a47, v123
v_accvgpr_write_b32 a48, v128
v_accvgpr_write_b32 a49, v129
v_accvgpr_write_b32 a50, v130
v_accvgpr_write_b32 a51, v131
v_accvgpr_write_b32 a52, v136
v_accvgpr_write_b32 a53, v137
v_accvgpr_write_b32 a54, v138
v_accvgpr_write_b32 a55, v139
v_accvgpr_write_b32 a56, v144
v_accvgpr_write_b32 a57, v145
v_accvgpr_write_b32 a58, v146
v_accvgpr_write_b32 a59, v147
v_accvgpr_write_b32 a60, v152
v_accvgpr_write_b32 a61, v153
v_accvgpr_write_b32 a62, v154
v_accvgpr_write_b32 a63, v155
Mvav 188,148,136
s_barrier
buffer_store_dwordx4 a[216:219], v1, s[8:11], s36 offen offset:3072 sc1
s_add_u32 s36, s29, 69632
v_add_u32 v8, s80, v1
A 196,196,224
Mvav 188,152,144
A 197,197,224
A 198,198,224
A 199,199,224
X 196
X 197
X 198
K 192,196,197
X 199
A 204,204,228
K 193,198,199
A 205,205,228
A 206,206,228
A 207,207,228
X 204
X 205
X 206
K 200,204,205
X 207
A 212,212,232
K 201,206,207
A 213,213,232
A 214,214,232
A 215,215,232
X 212
X 213
X 214
K 208,212,213
X 215
A 220,220,236
K 209,214,215
A 221,221,236
A 222,222,236
A 223,223,236
X 220
X 221
X 222
)"
R"(K 216,220,221
X 223
v_mov_b32 v194, 0
K 217,222,223
v_mov_b32 v195, 0
v_cndmask_b32_e64 v68, v241, v68, s[72:73]
v_cndmask_b32_e64 v69, v241, v69, s[74:75]
v_cndmask_b32_e64 v70, v241, v70, s[76:77]
v_cndmask_b32_e64 v71, v241, v71, s[78:79]
A 68,68,228
A 69,69,228
A 70,70,228
A 71,71,228
X 68
X 69
X 70
K 202,68,69
X 71
A 100,100,232
K 203,70,71
A 101,101,232
A 102,102,232
A 103,103,232
X 100
X 101
X 102
K 210,100,101
X 103
A 132,132,236
K 211,102,103
A 133,133,236
A 134,134,236
A 135,135,236
X 132
X 133
X 134
K 218,132,133
X 135
s_nop 0
K 219,134,135
Mvav 188,156,152
W 0
buffer_store_dwordx4 a[224:227], v1, s[8:11], s36 offen offset:0 sc1
Ra 144,242,28672
Ra 148,242,29696
Ra 152,242,30720
Zvav 32,128,192
buffer_store_dwordx4 a[232:235], v1, s[8:11], s36 offen offset:1024 sc1
Ra 156,242,31744
Zvav 64,128,200
buffer_store_dwordx4 a[240:243], v1, s[8:11], s36 offen offset:2048 sc1
v_xor_b32 v242, 0x10000, v242
Zvav 96,128,208
s_and_b32 s80, s14, 1
s_xor_b32 s62, s62, 0x10000
buffer_store_dwordx4 a[248:251], v1, s[8:11], s36 offen offset:3072 sc1
s_add_u32 s36, s29, 393216
Zvav 128,128,216
s_lshl_b32 s80, s80, 14
s_add_u32 s80, s80, 131072
buffer_store_dwordx4 a[196:199], v1, s[8:11], s36 offen offset:0 sc1
v_add_u32 v23, 61440, v21
Zvav 132,132,216
buffer_store_dwordx4 a[204:207], v1, s[8:11], s36 offen offset:1024 sc1
v_add_u32 v12, s80, v1
Ra 64,12,8192
v_add_u32 v24, 61440, v22
Zvav 100,132,208
buffer_store_dwordx4 a[212:215], v1, s[8:11], s36 offen offset:2048 sc1
Ra 68,12,9216
Ra 72,12,10240
v_add_u32 v25, 122880, v21
Zvav 68,132,200
buffer_store_dwordx4 a[220:223], v1, s[8:11], s36 offen offset:3072 sc1
s_add_u32 s36, s29, 397312
Ra 76,12,11264
Ra 80,12,12288
Ra 84,12,13312
Zvav 36,132,192
buffer_store_dwordx4 a[228:231], v1, s[8:11], s36 offen offset:0 sc1
Ra 88,12,14336
Ra 92,12,15360
v_add_u32 v26, 122880, v22
Zvav 40,136,192
buffer_store_dwordx4 a[236:239], v1, s[8:11], s36 offen offset:1024 sc1
Ra 96,12,0
Ra 100,12,1024
Ra 104,12,2048
Zvav 72,136,200
Ra 108,12,3072
Ra 112,12,4096
buffer_store_dwordx4 a[244:247], v1, s[8:11], s36 offen offset:2048 sc1
Ra 116,12,5120
Zvav 104,136,208
Ra 120,12,6144
Ra 124,12,7168
buffer_store_dwordx4 a[252:255], v1, s[8:11], s36 offen offset:3072 sc1
Zvav 136,136,216
Zvav 140,140,216
Zvav 108,140,208
Zvav 76,140,200
s_waitcnt vmcnt(16) lgkmcnt(0)
s_barrier
ds_read_b128 v[248:251], v8
Zvav 44,140,192
Ra 128,242,40960
Zvav 48,144,192
Zvav 80,144,200
X 172
X 173
Zvav 112,144,208
Ra 132,242,41984
X 174
K 220,172,173
Zvav 144,144,216
X 175
v_cndmask_b32_e64 v168, v241, v168, s[72:73]
K 221,174,175
Zvav 148,148,216
v_cndmask_b32_e64 v169, v241, v169, s[74:75]
v_cndmask_b32_e64 v170, v241, v170, s[76:77]
v_cndmask_b32_e64 v171, v241, v171, s[78:79]
Zvav 116,148,208
X 168
X 169
Zvav 84,148,200
W 2
Ra 136,242,43008
X 170
K 212,168,169
Zvav 52,148,192
v_mov_b32 v224, v250
v_mov_b32 v225, v250
v_mov_b32 v226, v250
v_mov_b32 v227, v250
Zvav 56,152,192
v_mov_b32 v228, v251
v_mov_b32 v229, v251
v_mov_b32 v230, v251
v_mov_b32 v231, v251
Zvav 88,152,200
v_mov_b32 v232, v248
v_mov_b32 v233, v248
v_mov_b32 v234, v248
v_mov_b32 v235, v248
Zvav 120,152,208
v_mov_b32 v236, v249
v_mov_b32 v237, v249
v_mov_b32 v238, v249
v_mov_b32 v239, v249
Zvav 152,152,216
X 171
v_mov_b32 v204, 0
K 213,170,171
Zvav 156,156,216
Ra 140,242,44032
v_mov_b32 v205, 0
v_mov_b32 v196, 0
v_mov_b32 v197, 0
Zvav 124,156,208
v_mov_b32 v198, 0
v_mov_b32 v199, 0
v_mov_b32 v206, 0
v_mov_b32 v207, 0
Zvav 92,156,200
v_mov_b32 v214, 0
v_mov_b32 v215, 0
v_cndmask_b32_e64 v188, v241, v188, s[72:73]
v_cndmask_b32_e64 v189, v241, v189, s[74:75]
Zvav 60,156,192
v_cndmask_b32_e64 v190, v241, v190, s[76:77]
v_cndmask_b32_e64 v191, v241, v191, s[78:79]
X 188
X 189
X 190
K 222,188,189
X 191
s_nop 0
K 223,190,191
s_waitcnt vmcnt(0)
s_add_u32 s80, s71, 128
v_mov_b64 v[28:29], s[12:13]
s_mov_b64 exec, 1
buffer_store_dwordx2 v[28:29], off, s[8:11], s80 sc1
s_mov_b64 exec, -1
s_nop 1
s_branch .Lsp2_33%=
.p2align 12
.Lpe_44%=:
v_pk_mul_f32 v[32:33], v[32:33], s[22:23]
v_pk_mul_f32 v[34:35], v[34:35], s[22:23]
v_pk_mul_f32 v[36:37], v[36:37], s[22:23]
v_pk_mul_f32 v[38:39], v[38:39], s[22:23]
v_pk_mul_f32 v[64:65], v[64:65], s[22:23]
v_pk_mul_f32 v[66:67], v[66:67], s[22:23]
v_pk_mul_f32 v[68:69], v[68:69], s[22:23]
v_pk_mul_f32 v[70:71], v[70:71], s[22:23]
v_pk_mul_f32 v[96:97], v[96:97], s[22:23]
v_pk_mul_f32 v[98:99], v[98:99], s[22:23]
v_pk_mul_f32 v[100:101], v[100:101], s[22:23]
v_pk_mul_f32 v[102:103], v[102:103], s[22:23]
v_pk_mul_f32 v[128:129], v[128:129], s[22:23]
v_pk_mul_f32 v[130:131], v[130:131], s[22:23]
v_pk_mul_f32 v[132:133], v[132:133], s[22:23]
v_pk_mul_f32 v[134:135], v[134:135], s[22:23]
v_pk_mul_f32 v[40:41], v[40:41], s[22:23]
v_pk_mul_f32 v[42:43], v[42:43], s[22:23]
v_pk_mul_f32 v[44:45], v[44:45], s[22:23]
v_pk_mul_f32 v[46:47], v[46:47], s[22:23]
v_pk_mul_f32 v[72:73], v[72:73], s[22:23]
v_pk_mul_f32 v[74:75], v[74:75], s[22:23]
v_pk_mul_f32 v[76:77], v[76:77], s[22:23]
v_pk_mul_f32 v[78:79], v[78:79], s[22:23]
v_pk_mul_f32 v[104:105], v[104:105], s[22:23]
v_pk_mul_f32 v[106:107], v[106:107], s[22:23]
v_pk_mul_f32 v[108:109], v[108:109], s[22:23]
v_pk_mul_f32 v[110:111], v[110:111], s[22:23]
v_pk_mul_f32 v[136:137], v[136:137], s[22:23]
v_pk_mul_f32 v[138:139], v[138:139], s[22:23]
v_pk_mul_f32 v[140:141], v[140:141], s[22:23]
v_pk_mul_f32 v[142:143], v[142:143], s[22:23]
v_pk_mul_f32 v[48:49], v[48:49], s[22:23]
v_pk_mul_f32 v[50:51], v[50:51], s[22:23]
v_pk_mul_f32 v[52:53], v[52:53], s[22:23]
v_pk_mul_f32 v[54:55], v[54:55], s[22:23]
v_pk_mul_f32 v[80:81], v[80:81], s[22:23]
v_pk_mul_f32 v[82:83], v[82:83], s[22:23]
v_pk_mul_f32 v[84:85], v[84:85], s[22:23]
v_pk_mul_f32 v[86:87], v[86:87], s[22:23]
v_pk_mul_f32 v[112:113], v[112:113], s[22:23]
v_pk_mul_f32 v[114:115], v[114:115], s[22:23]
v_pk_mul_f32 v[116:117], v[116:117], s[22:23]
v_pk_mul_f32 v[118:119], v[118:119], s[22:23]
v_pk_mul_f32 v[144:145], v[144:145], s[22:23]
v_pk_mul_f32 v[146:147], v[146:147], s[22:23]
v_pk_mul_f32 v[148:149], v[148:149], s[22:23]
v_pk_mul_f32 v[150:151], v[150:151], s[22:23]
v_pk_mul_f32 v[56:57], v[56:57], s[22:23]
v_pk_mul_f32 v[58:59], v[58:59], s[22:23]
v_pk_mul_f32 v[60:61], v[60:61], s[22:23]
v_pk_mul_f32 v[62:63], v[62:63], s[22:23]
v_pk_mul_f32 v[88:89], v[88:89], s[22:23]
v_pk_mul_f32 v[90:91], v[90:91], s[22:23]
v_pk_mul_f32 v[92:93], v[92:93], s[22:23]
v_pk_mul_f32 v[94:95], v[94:95], s[22:23]
v_pk_mul_f32 v[120:121], v[120:121], s[22:23]
v_pk_mul_f32 v[122:123], v[122:123], s[22:23]
v_pk_mul_f32 v[124:125], v[124:125], s[22:23]
v_pk_mul_f32 v[126:127], v[126:127], s[22:23]
v_pk_mul_f32 v[152:153], v[152:153], s[22:23]
v_pk_mul_f32 v[154:155], v[154:155], s[22:23]
v_pk_mul_f32 v[156:157], v[156:157], s[22:23]
v_pk_mul_f32 v[158:159], v[158:159], s[22:23]
K 32,32,33
K 33,34,35
K 34,36,37
K 35,38,39
K 40,40,41
K 41,42,43
K 42,44,45
K 43,46,47
W 0
Zvav 196,128,32
s_add_u32 m0, s62, 0
Ra 144,242,4096
Ra 148,242,5120
Ra 152,242,6144
Ra 156,242,7168
Mvav 196,132,40
K 64,64,65
K 65,66,67
K 66,68,69
K 67,70,71
K 72,72,73
K 73,74,75
K 74,76,77
K 75,78,79
s_nop 1
Zvav 204,132,72
Dc 1,8,61,0
K 96,96,97
K 97,98,99
K 98,100,101
Mvav 204,128,64
K 99,102,103
K 104,104,105
K 105,106,107
K 106,108,109
K 107,110,111
Zvav 212,128,96
K 128,128,129
K 129,130,131
K 130,132,133
K 131,134,135
Mvav 212,132,104
K 136,136,137
K 137,138,139
K 138,140,141
K 139,142,143
s_nop 1
Zvav 220,132,136
Dc 1,8,61,1024
K 48,48,49
K 49,50,51
K 50,52,53
Mvav 220,128,128
K 51,54,55
K 56,56,57
K 57,58,59
K 58,60,61
K 59,62,63
Mvav 196,136,48
Ra 128,242,8192
Ra 132,242,9216
K 80,80,81
K 81,82,83
Mvav 196,140,56
K 82,84,85
K 83,86,87
K 88,88,89
K 89,90,91
K 90,92,93
K 91,94,95
s_nop 1
Mvav 204,140,88
Dc 1,8,61,2048
K 112,112,113
K 113,114,115
K 114,116,117
Mvav 204,136,80
K 115,118,119
K 120,120,121
K 121,122,123
K 122,124,125
K 123,126,127
Mvav 212,136,112
K 144,144,145
K 145,146,147
K 146,148,149
K 147,150,151
Mvav 212,140,120
K 152,152,153
K 153,154,155
K 154,156,157
K 155,158,159
s_nop 1
Mvav 220,140,152
Dc 1,8,61,3072
s_add_u32 m0, s62, 4096
v_accvgpr_write_b32 a0, v32
v_accvgpr_write_b32 a1, v33
v_accvgpr_write_b32 a2, v34
Mvav 220,136,144
W 2
Ra 136,242,10240
v_accvgpr_write_b32 a3, v35
v_accvgpr_write_b32 a4, v40
Zvav 36,144,32
Ra 140,242,11264
v_accvgpr_write_b32 a5, v41
Mvav 36,148,40
v_accvgpr_write_b32 a6, v42
v_accvgpr_write_b32 a7, v43
v_max3_f32 v4, v196, v197, v198
v_max3_f32 v4, v4, v199, v252
Zvav 68,148,72
v_max3_f32 v5, v204, v205, v206
v_max3_f32 v5, v5, v207, v253
v_max3_f32 v6, v212, v213, v214
v_max3_f32 v6, v6, v215, v254
Mvav 68,144,64
v_max3_f32 v7, v220, v221, v222
v_max3_f32 v7, v7, v223, v255
v_mov_b32 v8, v4
v_mov_b32 v9, v5
Zvav 100,144,96
Dc 243,8,61,0
v_mov_b32 v10, v6
v_mov_b32 v11, v7
v_permlane32_swap_b32 v4, v8
Mvav 100,148,104
v_permlane32_swap_b32 v5, v9
v_permlane32_swap_b32 v6, v10
v_permlane32_swap_b32 v7, v11
v_max_f32 v4, v4, v8
Zvav 132,148,136
v_max_f32 v5, v5, v9
v_max_f32 v6, v6, v10
v_max_f32 v7, v7, v11
v_mov_b32 v8, v4
Mvav 132,144,128
v_mov_b32 v9, v5
v_mov_b32 v10, v6
v_mov_b32 v11, v7
v_accvgpr_write_b32 a8, v48
Mvav 36,152,48
v_permlane16_swap_b32 v4, v8
v_permlane16_swap_b32 v5, v9
v_permlane16_swap_b32 v6, v10
v_permlane16_swap_b32 v7, v11
)"
R"(Mvav 36,156,56
v_max_f32 v4, v4, v8
v_max_f32 v5, v5, v9
v_max_f32 v6, v6, v10
v_max_f32 v7, v7, v11
Mvav 68,156,88
v_sub_f32 v4, 0, v4
v_sub_f32 v5, 0, v5
v_sub_f32 v6, 0, v6
v_sub_f32 v7, 0, v7
Mvav 68,152,80
v_mov_b32 v224, v4
v_mov_b32 v225, v4
v_mov_b32 v226, v4
v_mov_b32 v227, v4
Mvav 100,152,112
v_mov_b32 v228, v5
v_mov_b32 v229, v5
v_mov_b32 v230, v5
v_mov_b32 v231, v5
Mvav 100,156,120
v_mov_b32 v232, v6
v_mov_b32 v233, v6
v_mov_b32 v234, v6
v_mov_b32 v235, v6
Mvav 132,156,152
v_mov_b32 v236, v7
v_mov_b32 v237, v7
v_mov_b32 v238, v7
v_mov_b32 v239, v7
Mvav 132,152,144
W 0
v_accvgpr_write_b32 a9, v49
v_accvgpr_write_b32 a10, v50
v_accvgpr_write_b32 a11, v51
v_accvgpr_write_b32 a12, v56
Cvav 160,128,32,224
s_and_b32 s80, s14, 1
Dc 243,8,61,1024
v_accvgpr_write_b32 a13, v57
v_accvgpr_write_b32 a14, v58
v_accvgpr_write_b32 a15, v59
Mvav 160,132,40
s_lshl_b32 s80, s80, 14
Ra 144,242,12288
Ra 148,242,13312
v_accvgpr_write_b32 a16, v64
v_accvgpr_write_b32 a17, v65
Cvav 164,132,72,228
Dc 243,8,61,2048
v_accvgpr_write_b32 a18, v66
v_accvgpr_write_b32 a19, v67
v_accvgpr_write_b32 a20, v72
Mvav 164,128,64
s_add_u32 s80, s80, 98304
v_accvgpr_write_b32 a21, v73
v_accvgpr_write_b32 a22, v74
v_accvgpr_write_b32 a23, v75
v_accvgpr_write_b32 a24, v80
Cvav 168,128,96,232
Dc 243,8,61,3072
s_add_u32 s61, s61, s63
v_accvgpr_write_b32 a25, v81
v_accvgpr_write_b32 a26, v82
A 196,196,224
Mvav 168,132,104
s_add_u32 s36, s29, 65536
v_accvgpr_write_b32 a27, v83
A 197,197,224
A 198,198,224
v_accvgpr_write_b32 a28, v88
Cvav 172,132,136,236
Ra 152,242,14336
buffer_store_dwordx4 a[192:195], v1, s[8:11], s36 offen offset:0 sc1
A 199,199,224
v_accvgpr_write_b32 a29, v89
Mvav 172,128,128
Ra 156,242,15360
X 196
v_accvgpr_write_b32 a30, v90
Mvav 160,136,48
X 197
v_accvgpr_write_b32 a31, v91
v_accvgpr_write_b32 a32, v96
Mvav 160,140,56
buffer_store_dwordx4 a[200:203], v1, s[8:11], s36 offen offset:1024 sc1
X 198
K 192,196,197
Mvav 164,140,88
X 199
v_accvgpr_write_b32 a33, v97
A 204,204,228
Mvav 164,136,80
v_accvgpr_write_b32 a34, v98
K 193,198,199
v_accvgpr_write_b32 a35, v99
A 205,205,228
Mvav 168,136,112
A 206,206,228
v_accvgpr_write_b32 a36, v104
v_add_u32 v8, s80, v1
A 207,207,228
Mvav 168,140,120
buffer_store_dwordx4 a[208:211], v1, s[8:11], s36 offen offset:2048 sc1
X 204
v_accvgpr_write_b32 a37, v105
Mvav 172,140,152
X 205
v_accvgpr_write_b32 a38, v106
v_accvgpr_write_b32 a39, v107
Mvav 172,136,144
W 0
X 206
v_accvgpr_write_b32 a40, v112
K 200,204,205
Cvav 176,144,32,224
Ra 128,242,24576
X 207
v_accvgpr_write_b32 a41, v113
Mvav 176,148,40
buffer_store_dwordx4 a[216:219], v1, s[8:11], s36 offen offset:3072 sc1
s_add_u32 s36, s29, 69632
Ra 132,242,25600
A 212,212,232
v_accvgpr_write_b32 a42, v114
Cvav 180,148,72,228
K 201,206,207
A 213,213,232
v_accvgpr_write_b32 a43, v115
A 214,214,232
Mvav 180,144,64
A 215,215,232
v_accvgpr_write_b32 a44, v120
X 212
Cvav 184,144,96,232
buffer_store_dwordx4 a[224:227], v1, s[8:11], s36 offen offset:0 sc1
v_accvgpr_write_b32 a45, v121
X 213
Mvav 184,148,104
v_accvgpr_write_b32 a46, v122
X 214
v_accvgpr_write_b32 a47, v123
Cvav 188,148,136,236
K 208,212,213
v_accvgpr_write_b32 a48, v128
X 215
Mvav 188,144,128
buffer_store_dwordx4 a[232:235], v1, s[8:11], s36 offen offset:1024 sc1
v_accvgpr_write_b32 a49, v129
A 220,220,236
v_accvgpr_write_b32 a50, v130
Mvav 176,152,48
K 209,214,215
A 221,221,236
v_accvgpr_write_b32 a51, v131
A 222,222,236
Mvav 176,156,56
Ra 136,242,26624
A 223,223,236
v_accvgpr_write_b32 a52, v136
v_accvgpr_write_b32 a53, v137
Mvav 180,156,88
Ra 140,242,27648
buffer_store_dwordx4 a[240:243], v1, s[8:11], s36 offen offset:2048 sc1
X 220
Mvav 180,152,80
X 221
v_accvgpr_write_b32 a54, v138
v_accvgpr_write_b32 a55, v139
Mvav 184,152,112
v_accvgpr_write_b32 a56, v144
v_accvgpr_write_b32 a57, v145
v_accvgpr_write_b32 a58, v146
v_accvgpr_write_b32 a59, v147
v_accvgpr_write_b32 a60, v152
v_accvgpr_write_b32 a61, v153
v_accvgpr_write_b32 a62, v154
v_accvgpr_write_b32 a63, v155
Mvav 184,156,120
s_barrier
buffer_store_dwordx4 a[248:251], v1, s[8:11], s36 offen offset:3072 sc1
X 222
K 216,220,221
Mvav 188,156,152
X 223
A 36,36,224
K 217,222,223
A 37,37,224
A 38,38,224
A 39,39,224
X 36
X 37
X 38
K 194,36,37
X 39
A 68,68,228
K 195,38,39
A 69,69,228
A 70,70,228
A 71,71,228
X 68
X 69
X 70
K 202,68,69
X 71
A 100,100,232
K 203,70,71
A 101,101,232
A 102,102,232
A 103,103,232
X 100
X 101
X 102
K 210,100,101
X 103
A 132,132,236
K 211,102,103
A 133,133,236
A 134,134,236
A 135,135,236
X 132
X 133
X 134
K 218,132,133
X 135
s_nop 0
K 219,134,135
Mvav 188,152,144
W 0
s_add_u32 s36, s29, 393216
buffer_store_dwordx4 a[196:199], v1, s[8:11], s36 offen offset:0 sc1
Ra 144,242,28672
Ra 148,242,29696
Zvav 32,128,192
buffer_store_dwordx4 a[204:207], v1, s[8:11], s36 offen offset:1024 sc1
Ra 152,242,30720
Ra 156,242,31744
Zvav 64,128,200
buffer_store_dwordx4 a[212:215], v1, s[8:11], s36 offen offset:2048 sc1
v_xor_b32 v242, 0x10000, v242
Zvav 96,128,208
s_and_b32 s80, s14, 1
s_xor_b32 s62, s62, 0x10000
buffer_store_dwordx4 a[220:223], v1, s[8:11], s36 offen offset:3072 sc1
s_add_u32 s36, s29, 397312
Zvav 128,128,216
s_lshl_b32 s80, s80, 14
s_add_u32 s80, s80, 131072
buffer_store_dwordx4 a[228:231], v1, s[8:11], s36 offen offset:0 sc1
v_add_u32 v23, 61440, v21
Zvav 132,132,216
v_add_u32 v12, s80, v1
Ra 64,12,8192
Ra 68,12,9216
v_add_u32 v24, 61440, v22
Zvav 100,132,208
Ra 72,12,10240
buffer_store_dwordx4 a[236:239], v1, s[8:11], s36 offen offset:1024 sc1
Ra 76,12,11264
v_add_u32 v25, 122880, v21
Zvav 68,132,200
Ra 80,12,12288
Ra 84,12,13312
Ra 88,12,14336
v_add_u32 v26, 122880, v22
Zvav 36,132,192
Ra 92,12,15360
Ra 96,12,0
Ra 100,12,1024
Ra 104,12,2048
Zvav 40,136,192
buffer_store_dwordx4 a[244:247], v1, s[8:11], s36 offen offset:2048 sc1
Ra 108,12,3072
Ra 112,12,4096
Ra 116,12,5120
Zvav 72,136,200
Ra 120,12,6144
Ra 124,12,7168
X 172
Zvav 104,136,208
X 173
X 174
Zvav 136,136,216
buffer_store_dwordx4 a[252:255], v1, s[8:11], s36 offen offset:3072 sc1
K 220,172,173
X 175
Zvav 140,140,216
X 168
K 221,174,175
Zvav 108,140,208
X 169
X 170
Zvav 76,140,200
s_waitcnt vmcnt(16) lgkmcnt(0)
s_barrier
ds_read_b128 v[248:251], v8
K 212,168,169
Zvav 44,140,192
X 171
X 164
Zvav 48,144,192
Ra 128,242,0
K 213,170,171
X 165
Zvav 80,144,200
X 166
Zvav 112,144,208
Ra 132,242,1024
K 204,164,165
X 167
Zvav 144,144,216
X 160
Zvav 148,148,216
K 205,166,167
X 161
Zvav 116,148,208
Ra 136,242,2048
X 162
Zvav 84,148,200
W 3
K 196,160,161
X 163
Zvav 52,148,192
X 176
K 197,162,163
Zvav 56,152,192
Ra 140,242,3072
X 177
Zvav 88,152,200
X 178
K 198,176,177
Zvav 120,152,208
X 179
X 180
Zvav 152,152,216
K 199,178,179
Zvav 156,156,216
X 181
X 182
Zvav 124,156,208
K 206,180,181
X 183
Zvav 92,156,200
K 207,182,183
Zvav 60,156,192
s_waitcnt vmcnt(0)
s_add_u32 s80, s71, 128
v_mov_b64 v[28:29], s[12:13]
s_mov_b64 exec, 1
buffer_store_dwordx2 v[28:29], off, s[8:11], s80 sc1
s_mov_b64 exec, -1
s_branch .Lsp1_32%=
.p2align 12
.Lmoe_40%=:
W 0
Cvaa 160,128,0,224
s_cmp_eq_u32 s14, 0
s_cselect_b32 s20, s30, s32
s_cselect_b32 s21, s31, s33
Ra 144,242,4096
Ra 148,242,5120
Mvaa 160,132,4
s_xor_b32 s62, s62, 0x10000
Ra 152,242,6144
Cvaa 164,132,20,228
X 184
Mvaa 164,128,16
s_and_b32 s21, s21, 0xffff
X 185
Cvaa 168,128,32,232
X 186
Mvaa 168,132,36
Ra 156,242,7168
K 214,184,185
Cvaa 172,132,52,236
s_mov_b32 s22, 2211840
X 187
Mvaa 172,128,48
X 188
Mvaa 160,136,8
Ra 128,242,16384
Mvaa 160,140,12
s_mov_b32 s23, 0x20000
K 215,186,187
Mvaa 164,140,28
X 189
Mvaa 164,136,24
X 190
Mvaa 168,136,40
Ra 132,242,17408
v_lshrrev_b32 v4, 6, v1
Mvaa 168,140,44
K 222,188,189
Mvaa 172,140,60
X 191
Mvaa 172,136,56
v_mul_u32_u24 v4, 3456, v4
K 223,190,191
v_smfmac_f32_16x16x64_bf16 a[160:163], a[176:179], v[192:199], v240
Ra 136,242,18432
)"
R"(v_smfmac_f32_16x16x64_bf16 a[164:167], a[176:179], v[200:207], v240
X 160
v_smfmac_f32_16x16x64_bf16 a[168:171], a[176:179], v[208:215], v240
v_lshrrev_b32 v5, 4, v1
v_smfmac_f32_16x16x64_bf16 a[172:175], a[176:179], v[216:223], v240
W 3
X 161
Cvaa 176,144,0,224
X 162
Mvaa 176,148,4
Ra 140,242,19456
v_xor_b32 v5, v5, v1
Cvaa 180,148,20,228
K 192,160,161
Mvaa 180,144,16
X 163
Cvaa 184,144,32,232
v_and_b32 v5, 32, v5
Mvaa 184,148,36
X 164
Cvaa 188,148,52,236
K 193,162,163
v_and_b32 v6, 16, v1
Mvaa 188,144,48
X 165
Mvaa 176,152,8
X 166
Mvaa 176,156,12
Ra 144,242,20480
Ra 148,242,21504
Mvaa 180,156,28
v_add3_u32 v4, v4, v5, v6
K 200,164,165
Mvaa 180,152,24
X 167
Mvaa 184,152,40
X 168
Mvaa 184,156,44
v_add_u32 v5, 54272, v4
K 201,166,167
Mvaa 188,156,60
X 169
Mvaa 188,152,56
W 2
Ra 152,242,22528
v_add_u32 v6, 108544, v4
Mvav 32,128,196
X 170
Mvav 64,128,204
Ra 156,242,23552
v_add_u32 v7, 162816, v4
Mvav 96,128,212
K 208,168,169
Mvav 128,128,220
X 171
Mvav 132,132,220
s_lshl_b32 s94, s85, 6
X 172
Mvav 100,132,212
s_add_u32 s64, s64, 1
Ra 128,242,8192
K 209,170,171
Mvav 68,132,204
X 173
Mvav 36,132,196
s_mul_i32 s95, s14, 20480
Mvav 40,136,196
X 174
Mvav 72,136,204
Ra 132,242,9216
K 216,172,173
Mvav 104,136,212
s_add_u32 s95, s95, 98304
X 175
Mvav 136,136,220
Mvav 140,140,220
s_add_u32 m0, s95, 0
X 176
Mvav 108,140,212
Ra 136,242,10240
K 217,174,175
Mvav 76,140,204
X 177
Mvav 44,140,196
W 3
D 4,20,94,0
Mvav 48,144,196
X 178
Mvav 80,144,204
Ra 140,242,11264
Mvav 112,144,212
D 5,20,94,1024
K 194,176,177
Mvav 144,144,220
X 179
Mvav 148,148,220
X 180
Mvav 116,148,212
Ra 144,242,12288
D 6,20,94,2048
Mvav 84,148,204
K 195,178,179
Mvav 52,148,196
X 181
Mvav 56,152,196
D 7,20,94,3072
Ra 148,242,13312
Mvav 88,152,204
X 182
Mvav 120,152,212
s_add_u32 s94, s94, 221184
K 202,180,181
Mvav 152,152,220
X 183
Mvav 156,156,220
X 184
Mvav 124,156,212
s_add_u32 m0, s95, 4096
Ra 152,242,14336
Mvav 92,156,204
K 203,182,183
Mvav 60,156,196
W 3
X 185
Cvaa 160,128,0,224
D 4,20,94,0
Ra 156,242,15360
Mvaa 160,132,4
X 186
Cvaa 164,132,20,228
K 210,184,185
Mvaa 164,128,16
X 187
Cvaa 168,128,32,232
D 5,20,94,1024
Mvaa 168,132,36
X 188
Cvaa 172,132,52,236
D 6,20,94,2048
K 211,186,187
Mvaa 172,128,48
X 189
Mvaa 160,136,8
Ra 128,242,24576
Ra 132,242,25600
Mvaa 160,140,12
X 190
Mvaa 164,140,28
D 7,20,94,3072
K 218,188,189
Mvaa 164,136,24
s_add_u32 s94, s94, 221184
X 191
Mvaa 168,136,40
Mvaa 168,140,44
X 160
Mvaa 172,140,60
s_add_u32 m0, s95, 8192
K 219,190,191
Mvaa 172,136,56
W 2
X 161
Cvaa 176,144,0,224
Ra 136,242,26624
Ra 140,242,27648
Mvaa 176,148,4
X 162
Cvaa 180,148,20,228
D 4,20,94,0
K 196,160,161
Mvaa 180,144,16
X 163
Cvaa 184,144,32,232
D 5,20,94,1024
Mvaa 184,148,36
X 164
Cvaa 188,148,52,236
K 197,162,163
Mvaa 188,144,48
D 6,20,94,2048
Ra 144,242,28672
Mvaa 176,152,8
X 165
Mvaa 176,156,12
Ra 148,242,29696
D 7,20,94,3072
Mvaa 180,156,28
X 166
Mvaa 180,152,24
K 204,164,165
Mvaa 184,152,40
s_add_u32 s94, s94, 221184
X 167
Mvaa 184,156,44
X 168
Mvaa 188,156,60
Mvaa 188,152,56
W 2
s_add_u32 m0, s95, 12288
K 205,166,167
Mvav 32,128,192
X 169
Mvav 64,128,200
Ra 152,242,30720
Ra 156,242,31744
Mvav 96,128,208
X 170
Mvav 128,128,216
D 4,20,94,0
v_xor_b32 v242, 0x10000, v242
Mvav 132,132,216
K 212,168,169
Mvav 100,132,208
X 171
Mvav 68,132,200
D 5,20,94,1024
Mvav 36,132,192
X 172
Mvav 40,136,192
D 6,20,94,2048
K 213,170,171
Mvav 72,136,200
X 173
Mvav 104,136,208
X 174
Mvav 136,136,216
D 7,20,94,3072
K 220,172,173
Mvav 140,140,216
X 175
Mvav 108,140,208
s_add_u32 s94, s94, 221184
X 176
Mvav 76,140,200
W 0
s_barrier
Mvav 44,140,192
Ra 128,242,0
K 221,174,175
Mvav 48,144,192
X 177
Mvav 80,144,200
v_bfe_u32 v11, v1, 4, 3
Mvav 112,144,208
X 178
Mvav 144,144,216
Ra 132,242,1024
v_lshlrev_b32 v11, 3, v11
Mvav 148,148,216
K 198,176,177
Mvav 116,148,208
X 179
Mvav 84,148,200
X 180
Mvav 52,148,192
Ra 136,242,2048
buffer_load_dwordx2 v[8:9], v11, s[8:11], 0 offen offset:3840 sc1
Mvav 56,152,192
Ra 140,242,3072
K 199,178,179
Mvav 88,152,200
X 181
Mvav 120,152,208
X 182
Mvav 152,152,216
K 206,180,181
Mvav 156,156,216
X 183
Mvav 124,156,208
Mvav 92,156,200
K 207,182,183
Mvav 60,156,192
W 0
Cvaa 160,128,0,224
s_add_u32 m0, s95, 16384
Ra 144,242,4096
Ra 148,242,5120
Mvaa 160,132,4
s_xor_b32 s62, s62, 0x10000
Ra 152,242,6144
Cvaa 164,132,20,228
X 184
Mvaa 164,128,16
X 185
Cvaa 168,128,32,232
X 186
Mvaa 168,132,36
D 4,20,94,0
K 214,184,185
Cvaa 172,132,52,236
Ra 156,242,7168
Mvaa 172,128,48
X 187
Mvaa 160,136,8
X 188
Mvaa 160,140,12
D 5,20,94,1024
Ra 128,242,16384
Mvaa 164,140,28
K 215,186,187
Mvaa 164,136,24
X 189
Mvaa 168,136,40
X 190
Mvaa 168,140,44
D 6,20,94,2048
Ra 132,242,17408
Mvaa 172,140,60
K 222,188,189
Mvaa 172,136,56
X 191
v_smfmac_f32_16x16x64_bf16 a[160:163], a[176:179], v[192:199], v240
Ra 136,242,18432
K 223,190,191
v_smfmac_f32_16x16x64_bf16 a[164:167], a[176:179], v[200:207], v240
D 7,20,94,3072
v_smfmac_f32_16x16x64_bf16 a[168:171], a[176:179], v[208:215], v240
s_add_u32 s94, s94, 221184
v_mul_u32_u24 v10, 160, v11
v_smfmac_f32_16x16x64_bf16 a[172:175], a[176:179], v[216:223], v240
W 3
X 160
Cvaa 176,144,0,224
X 161
Mvaa 176,148,4
Ra 140,242,19456
v_lshrrev_b32 v12, 7, v1
Cvaa 180,148,20,228
X 162
Mvaa 180,144,16
K 192,160,161
v_lshl_add_u32 v10, v12, 4, v10
Cvaa 184,144,32,232
s_mul_i32 s82, s14, 5120
X 163
Mvaa 184,148,36
X 164
Cvaa 188,148,52,236
K 193,162,163
Mvaa 188,144,48
s_mul_i32 s81, s14, 640
X 165
Mvaa 176,152,8
X 166
Mvaa 176,156,12
s_add_u32 s81, s81, 118784
Ra 144,242,20480
Ra 148,242,21504
Mvaa 180,156,28
K 200,164,165
Mvaa 180,152,24
X 167
Mvaa 184,152,40
s_waitcnt vmcnt(4)
v_cmp_eq_u64 vcc, s[12:13], v[8:9]
s_cmp_eq_u64 vcc, -1
s_cbranch_scc0 .Lspin_47%=
.Lok_48%=:
X 168
Mvaa 184,156,44
K 201,166,167
Mvaa 188,156,60
s_add_u32 m0, s82, 53248
X 169
Mvaa 188,152,56
W 2
Ra 152,242,22528
Mvav 32,128,196
X 170
Mvav 64,128,204
Ra 156,242,23552
Dc 10,8,81,0
Mvav 96,128,212
K 208,168,169
Mvav 128,128,220
X 171
Mvav 132,132,220
s_add_u32 m0, s82, 54144
X 172
Mvav 100,132,212
s_add_u32 s64, s64, 1
Ra 128,242,8192
K 209,170,171
Mvav 68,132,204
X 173
Mvav 36,132,196
Dc 10,8,81,128
Mvav 40,136,196
X 174
Mvav 72,136,204
s_add_u32 m0, s82, 55040
Ra 132,242,9216
K 216,172,173
Mvav 104,136,212
X 175
Mvav 136,136,220
Dc 10,8,81,256
Mvav 140,140,220
X 176
Mvav 108,140,212
)"
R"(Ra 136,242,10240
K 217,174,175
Mvav 76,140,204
s_add_u32 m0, s82, 55936
X 177
Mvav 44,140,196
W 3
Mvav 48,144,196
X 178
Mvav 80,144,204
Ra 140,242,11264
Dc 10,8,81,384
Mvav 112,144,212
K 194,176,177
Mvav 144,144,220
s_add_u32 m0, s82, 56832
X 179
Mvav 148,148,220
X 180
Mvav 116,148,212
Ra 144,242,12288
Mvav 84,148,204
Dc 10,8,81,512
K 195,178,179
Mvav 52,148,196
X 181
Mvav 56,152,196
X 182
Mvav 88,152,204
Ra 148,242,13312
Mvav 120,152,212
K 202,180,181
Mvav 152,152,220
X 183
Mvav 156,156,220
X 184
Mvav 124,156,212
Ra 152,242,14336
Mvav 92,156,204
K 203,182,183
Mvav 60,156,196
W 3
X 185
Cvaa 160,128,0,224
X 186
Mvaa 160,132,4
Ra 156,242,15360
Cvaa 164,132,20,228
K 210,184,185
Mvaa 164,128,16
X 187
Cvaa 168,128,32,232
X 188
Mvaa 168,132,36
Cvaa 172,132,52,236
K 211,186,187
Mvaa 172,128,48
X 189
Mvaa 160,136,8
Ra 128,242,24576
Ra 132,242,25600
Mvaa 160,140,12
X 190
Mvaa 164,140,28
K 218,188,189
Mvaa 164,136,24
X 191
Mvaa 168,136,40
Mvaa 168,140,44
Mvaa 172,140,60
X 160
Mvaa 172,136,56
W 2
Ra 136,242,26624
K 219,190,191
Cvaa 176,144,0,224
X 161
Mvaa 176,148,4
Ra 140,242,27648
v_lshrrev_b32 v8, 8, v1
Cvaa 180,148,20,228
X 162
Mvaa 180,144,16
K 196,160,161
Cvaa 184,144,32,232
X 163
Mvaa 184,148,36
v_bfe_u32 v9, v1, 6, 2
Cvaa 188,148,52,236
X 164
Mvaa 188,144,48
K 197,162,163
v_lshl_add_u32 v9, v8, 3, v9
Mvaa 176,152,8
X 165
Mvaa 176,156,12
Ra 144,242,28672
Ra 148,242,29696
Mvaa 180,156,28
X 166
Mvaa 180,152,24
K 204,164,165
v_lshlrev_b32 v11, 6, v9
Mvaa 184,152,40
X 167
Mvaa 184,156,44
X 168
Mvaa 188,156,60
v_xor_b32 v9, s14, v8
Mvaa 188,152,56
W 2
Ra 152,242,30720
K 205,166,167
Mvav 32,128,192
X 169
Mvav 64,128,200
Ra 156,242,31744
v_and_b32 v9, 1, v9
Mvav 96,128,208
X 170
Mvav 128,128,216
v_xor_b32 v242, 0x10000, v242
K 212,168,169
Mvav 132,132,216
X 171
Mvav 100,132,208
v_lshl_add_u32 v11, v9, 5, v11
Mvav 68,132,200
X 172
Mvav 36,132,192
K 213,170,171
Mvav 40,136,192
X 173
Mvav 72,136,200
v_bfe_u32 v9, v1, 4, 2
Mvav 104,136,208
X 174
Mvav 136,136,216
K 220,172,173
v_lshl_add_u32 v12, v9, 3, v11
Mvav 140,140,216
X 175
Mvav 108,140,208
X 176
Mvav 76,140,200
s_waitcnt vmcnt(0) lgkmcnt(0)
s_barrier
Mvav 44,140,192
v_add_u32 v11, 98304, v12
K 221,174,175
Mvav 48,144,192
Ra 128,242,0
Mvav 80,144,200
X 177
Mvav 112,144,208
X 178
Mvav 144,144,216
Ra 132,242,1024
v_and_b32 v13, 0x70, v1
Mvav 148,148,216
K 198,176,177
Mvav 116,148,208
X 179
Mvav 84,148,200
Ra 136,242,2048
v_lshl_add_u32 v13, v8, 7, v13
Mvav 52,148,192
X 180
Mvav 56,152,192
Ra 140,242,3072
K 199,178,179
Mvav 88,152,200
X 181
Mvav 120,152,208
X 182
Mvav 152,152,216
K 206,180,181
Mvav 156,156,216
X 183
Mvav 124,156,208
Mvav 92,156,200
K 207,182,183
Mvav 60,156,192
W 0
Cvaa 160,128,0,224
s_cmp_eq_u32 s14, 0
s_cselect_b32 s95, 106496, 0
Ra 144,242,4096
Ra 148,242,5120
Mvaa 160,132,4
s_xor_b32 s62, s62, 0x10000
X 184
Cvaa 164,132,20,228
Ta 184,11,0
Ta 186,11,256
Mvaa 164,128,16
s_add_u32 s95, s95, 32768
Ta 188,11,20480
Ta 190,11,20736
Cvaa 168,128,32,232
Ra 192,13,53248
Ta 196,11,2048
Mvaa 168,132,36
s_add_u32 m0, s95, 0
X 185
Cvaa 172,132,52,236
Ta 198,11,2304
Ta 200,11,22528
Mvaa 172,128,48
W 3
X 186
Zaaa 232,184,192
Ta 202,11,22784
K 214,184,185
Zaaa 236,188,192
Ra 204,13,53760
Ra 152,242,6144
Mvaa 160,136,8
X 187
Mvaa 160,140,12
Ta 208,11,4096
D 4,20,94,0
Mvaa 164,140,28
X 188
Mvaa 164,136,24
Ta 210,11,4352
D 5,20,94,1024
Mvaa 168,136,40
Ra 156,242,7168
K 215,186,187
Mvaa 168,140,44
X 189
Mvaa 172,140,60
Ta 212,11,24576
D 6,20,94,2048
Mvaa 172,136,56
X 190
v_smfmac_f32_16x16x64_bf16 a[160:163], a[176:179], v[192:199], v240
W 5
Ta 214,11,24832
D 7,20,94,3072
Maaa 232,196,204
s_add_u32 s94, s94, 221184
Ra 216,13,54272
K 222,188,189
Maaa 236,200,204
s_add_u32 m0, s95, 4096
X 191
v_smfmac_f32_16x16x64_bf16 a[164:167], a[176:179], v[200:207], v240
X 160
K 223,190,191
v_smfmac_f32_16x16x64_bf16 a[168:171], a[176:179], v[208:215], v240
Ra 128,242,16384
Ta 220,11,6144
v_smfmac_f32_16x16x64_bf16 a[172:175], a[176:179], v[216:223], v240
W 5
Ta 222,11,6400
Ra 132,242,17408
Cvaa 176,144,0,224
X 161
Mvaa 176,148,4
D 4,20,94,0
Ta 224,11,26624
Cvaa 180,148,20,228
X 162
Mvaa 180,144,16
D 5,20,94,1024
K 192,160,161
Cvaa 184,144,32,232
Ta 226,11,26880
Ra 228,13,54784
Mvaa 184,148,36
W 7
X 163
Maaa 232,208,216
D 6,20,94,2048
Ra 136,242,18432
Maaa 236,212,216
X 164
Cvaa 188,148,52,236
D 7,20,94,3072
Ta 184,11,8192
Mvaa 188,144,48
s_add_u32 s94, s94, 221184
Ra 140,242,19456
K 193,162,163
Mvaa 176,152,8
s_add_u32 m0, s95, 8192
X 165
Mvaa 176,156,12
Ta 186,11,8448
Ta 188,11,28672
Mvaa 180,156,28
X 166
Mvaa 180,152,24
Ra 144,242,20480
K 200,164,165
Mvaa 184,152,40
X 167
Mvaa 184,156,44
Ta 190,11,28928
D 4,20,94,0
Mvaa 188,156,60
W 7
X 168
Maaa 232,220,228
s_add_u32 s64, s64, 1
Ra 192,13,55296
D 5,20,94,1024
Maaa 236,224,228
Ta 196,11,10240
K 201,166,167
Mvaa 188,152,56
W 6
X 169
Mvav 32,128,196
D 6,20,94,2048
Ra 148,242,21504
Mvav 64,128,204
X 170
Mvav 96,128,212
Ta 198,11,10496
D 7,20,94,3072
Mvav 128,128,220
s_add_u32 s94, s94, 221184
Ta 200,11,30720
K 208,168,169
Mvav 132,132,220
s_add_u32 m0, s95, 12288
X 171
Mvav 100,132,212
Ra 152,242,22528
Ta 202,11,30976
Mvav 68,132,204
X 172
Mvav 36,132,196
W 6
Ra 204,13,55808
K 209,170,171
Maaa 232,184,192
X 173
Maaa 236,188,192
D 4,20,94,0
Ra 156,242,23552
Mvav 40,136,196
X 174
Mvav 72,136,204
Ta 208,11,12288
D 5,20,94,1024
Mvav 104,136,212
Ta 210,11,12544
K 216,172,173
Mvav 136,136,220
X 175
Mvav 140,140,220
D 6,20,94,2048
Ra 128,242,8192
Mvav 108,140,212
Ta 212,11,32768
Ta 214,11,33024
Mvav 76,140,204
X 176
Mvav 44,140,196
W 5
D 7,20,94,3072
K 217,174,175
Mvav 48,144,196
s_add_u32 s94, s94, 221184
X 177
Maaa 232,196,204
s_add_u32 m0, s95, 16384
Ra 132,242,9216
Ra 216,13,56320
Maaa 236,200,204
X 178
Mvav 80,144,204
Ta 220,11,14336
K 194,176,177
Mvav 112,144,212
X 179
Mvav 144,144,220
D 4,20,94,0
Ta 222,11,14592
Mvav 148,148,220
Ra 136,242,10240
Ta 224,11,34816
Mvav 116,148,212
X 180
Mvav 84,148,204
D 5,20,94,1024
K 195,178,179
Mvav 52,148,196
X 181
Mvav 56,152,196
D 6,20,94,2048
Ta 226,11,35072
Mvav 88,152,204
W 5
Ra 140,242,11264
Ra 228,13,56832
)"
R"(Maaa 232,208,216
X 182
Maaa 236,212,216
D 7,20,94,3072
K 202,180,181
Mvav 120,152,212
s_add_u32 s94, s94, 221184
X 183
Mvav 152,152,220
Ta 184,11,16384
Ra 144,242,12288
Mvav 156,156,220
X 184
Mvav 124,156,212
Ta 186,11,16640
K 203,182,183
Mvav 92,156,204
X 185
Mvav 60,156,196
W 4
Ta 188,11,36864
Ra 148,242,13312
Cvaa 160,128,0,224
X 186
Mvaa 160,132,4
Ta 190,11,37120
K 210,184,185
Cvaa 164,132,20,228
W 6
Ra 192,13,57344
Ta 196,11,18432
Maaa 232,220,228
X 187
Maaa 236,224,228
X 188
Mvaa 164,128,16
Ra 152,242,14336
K 211,186,187
Cvaa 168,128,32,232
Ta 198,11,18688
Ta 200,11,38912
Mvaa 168,132,36
X 189
Cvaa 172,132,52,236
X 190
Mvaa 172,128,48
Ra 156,242,15360
K 218,188,189
Mvaa 160,136,8
Ta 202,11,39168
Ra 204,13,57856
Mvaa 160,140,12
X 191
Mvaa 164,140,28
Ra 128,242,24576
Mvaa 164,136,24
W 8
Maaa 232,184,192
Ra 132,242,25600
Maaa 236,188,192
X 160
Mvaa 168,136,40
K 219,190,191
Mvaa 168,140,44
X 161
Mvaa 172,140,60
X 162
Mvaa 172,136,56
W 4
Ra 136,242,26624
K 196,160,161
Cvaa 176,144,0,224
X 163
Mvaa 176,148,4
X 164
Cvaa 180,148,20,228
Ra 140,242,27648
K 197,162,163
Mvaa 180,144,16
X 165
Cvaa 184,144,32,232
W 4
Maaa 232,196,204
X 166
Maaa 236,200,204
Mvaa 184,148,36
K 204,164,165
Cvaa 188,148,52,236
X 167
Mvaa 188,144,48
Ra 144,242,28672
Mvaa 176,152,8
X 168
Mvaa 176,156,12
Ra 148,242,29696
Mvaa 180,156,28
K 205,166,167
Mvaa 180,152,24
X 169
Mvaa 184,152,40
Mvaa 184,156,44
X 170
Mvaa 188,156,60
K 212,168,169
Mvaa 188,152,56
W 2
X 171
Mvav 32,128,192
Ra 152,242,30720
Ra 156,242,31744
Mvav 64,128,200
X 172
Mvav 96,128,208
v_xor_b32 v242, 0x10000, v242
Mvav 128,128,216
K 213,170,171
Mvav 132,132,216
X 173
Mvav 100,132,208
Mvav 68,132,200
X 174
Mvav 36,132,192
Mvav 40,136,192
K 220,172,173
Mvav 72,136,200
X 175
Mvav 104,136,208
Mvav 136,136,216
X 176
Mvav 140,140,216
Mvav 108,140,208
K 221,174,175
Mvav 76,140,200
s_waitcnt vmcnt(0) lgkmcnt(0)
s_barrier
Mvav 44,140,192
X 177
Mvav 48,144,192
X 178
Mvav 80,144,200
Ra 128,242,0
K 198,176,177
Mvav 112,144,208
X 179
Mvav 144,144,216
Ra 132,242,1024
Mvav 148,148,216
X 180
Mvav 116,148,208
Ra 136,242,2048
Mvav 84,148,200
K 199,178,179
Mvav 52,148,192
X 181
Mvav 56,152,192
Ra 140,242,3072
Mvav 88,152,200
X 182
Mvav 120,152,208
Mvav 152,152,216
K 206,180,181
Mvav 156,156,216
X 183
Mvav 124,156,208
Mvav 92,156,200
K 207,182,183
Mvav 60,156,192
W 0
Cvaa 160,128,0,224
s_mov_b32 s20, s34
Ra 144,242,4096
Ra 148,242,5120
Mvaa 160,132,4
s_xor_b32 s62, s62, 0x10000
X 184
Cvaa 164,132,20,228
s_and_b32 s21, s35, 0xffff
Ta 184,11,40960
Ta 186,11,41216
Mvaa 164,128,16
Ta 188,12,32768
Ta 190,12,33024
Cvaa 168,128,32,232
s_mov_b32 s22, 2211840
Ra 192,13,58368
Ta 196,11,43008
Mvaa 168,132,36
s_mov_b32 s23, 0x20000
X 185
Cvaa 172,132,52,236
Ta 198,11,43264
Ta 200,12,34816
Mvaa 172,128,48
W 3
X 186
Maaa 232,184,192
Ta 202,12,35072
K 214,184,185
Maaa 236,188,192
Ra 204,13,58880
Ra 152,242,6144
Mvaa 160,136,8
X 187
Mvaa 160,140,12
Ta 208,11,45056
v_lshrrev_b32 v4, 8, v1
Mvaa 164,140,28
X 188
Mvaa 164,136,24
Ta 210,11,45312
v_lshlrev_b32 v5, 5, v4
Mvaa 168,136,40
v_xor_b32 v5, v5, v1
K 215,186,187
Mvaa 168,140,44
Ra 156,242,7168
Ta 212,12,36864
Mvaa 172,140,60
X 189
Mvaa 172,136,56
v_and_b32 v5, 0xf0, v5
v_mul_u32_u24 v4, 1280, v4
v_smfmac_f32_16x16x64_bf16 a[160:163], a[176:179], v[192:199], v240
W 5
X 190
Maaa 232,196,204
Ta 214,12,37120
v_add_u32 v4, v4, v5
Maaa 236,200,204
Ra 216,13,59392
K 222,188,189
v_smfmac_f32_16x16x64_bf16 a[164:167], a[176:179], v[200:207], v240
X 191
X 160
K 223,190,191
v_smfmac_f32_16x16x64_bf16 a[168:171], a[176:179], v[208:215], v240
Ra 128,242,16384
v_xor_b32 v5, 128, v4
v_smfmac_f32_16x16x64_bf16 a[172:175], a[176:179], v[216:223], v240
W 4
Ta 220,11,47104
v_add_u32 v6, 8192, v4
Cvaa 176,144,0,224
Ta 222,11,47360
v_add_u32 v7, 12288, v5
Mvaa 176,148,4
Ra 132,242,17408
v_add_u32 v5, 4096, v5
Cvaa 180,148,20,228
s_mul_i32 s94, s85, 40960
X 161
Mvaa 180,144,16
s_mul_i32 s81, s14, 20480
Ta 224,12,38912
Ta 226,12,39168
Cvaa 184,144,32,232
s_add_u32 s94, s94, s81
X 162
Mvaa 184,148,36
W 6
s_lshl_b32 s95, s14, 12
K 192,160,161
Maaa 232,208,216
s_add_u32 s95, s95, 98304
X 163
Maaa 236,212,216
s_add_u32 m0, s95, 0
Ra 228,13,59904
Ra 136,242,18432
Cvaa 188,148,52,236
X 164
Mvaa 188,144,48
Ta 184,11,49152
K 193,162,163
Mvaa 176,152,8
X 165
Mvaa 176,156,12
Ra 140,242,19456
Ta 186,11,49408
Mvaa 180,156,28
D 4,20,94,0
Ta 188,12,40960
Mvaa 180,152,24
X 166
Mvaa 184,152,40
D 5,20,94,1024
K 200,164,165
Mvaa 184,156,44
X 167
Mvaa 188,156,60
W 5
D 6,20,94,2048
Ra 144,242,20480
Maaa 232,220,228
s_add_u32 s64, s64, 1
Ta 190,12,41216
Ra 192,13,60416
Maaa 236,224,228
D 7,20,94,3072
Ta 196,11,51200
Mvaa 188,152,56
W 6
X 168
Mvav 32,128,196
s_add_u32 s94, s94, 256
s_add_u32 m0, s95, 8192
K 201,166,167
Mvav 64,128,204
X 169
Mvav 96,128,212
D 4,20,94,0
Ra 148,242,21504
Mvav 128,128,220
X 170
Mvav 132,132,220
D 5,20,94,1024
Ta 198,11,51456
Mvav 100,132,212
Ta 200,12,43008
K 208,168,169
Mvav 68,132,204
D 6,20,94,2048
Ra 152,242,22528
Mvav 36,132,196
W 5
X 171
Maaa 232,184,192
D 7,20,94,3072
Ta 202,12,43264
Maaa 236,188,192
s_add_u32 s94, s94, 256
X 172
Mvav 40,136,196
s_add_u32 m0, s95, 16384
Ra 204,13,60928
K 209,170,171
Mvav 72,136,204
X 173
Mvav 104,136,212
Ra 156,242,23552
D 4,20,94,0
Mvav 136,136,220
X 174
Mvav 140,140,220
D 5,20,94,1024
Ta 208,11,53248
Mvav 108,140,212
Ta 210,11,53504
K 216,172,173
Mvav 76,140,204
D 6,20,94,2048
Ra 128,242,8192
Mvav 44,140,196
W 3
X 175
Mvav 48,144,196
D 7,20,94,3072
Ta 212,12,45056
Maaa 232,196,204
s_add_u32 s94, s94, 256
X 176
Maaa 236,200,204
s_add_u32 m0, s95, 24576
Ta 214,12,45312
K 217,174,175
Mvav 80,144,204
D 4,20,94,0
Ra 132,242,9216
Mvav 112,144,212
X 177
Mvav 144,144,220
Ra 216,13,61440
D 5,20,94,1024
Mvav 148,148,220
X 178
Mvav 116,148,212
D 6,20,94,2048
Ta 220,11,55296
Mvav 84,148,204
Ta 222,11,55552
K 194,176,177
Mvav 52,148,196
D 7,20,94,3072
Ra 136,242,10240
Mvav 56,152,196
s_add_u32 s94, s94, 256
X 179
Mvav 88,152,204
W 3
X 180
Maaa 232,208,216
s_add_u32 m0, s95, 32768
Ta 224,12,47104
K 195,178,179
Maaa 236,212,216
D 4,20,94,0
Ta 226,12,47360
Mvav 120,152,212
Ra 140,242,11264
Ra 228,13,61952
)"
R"(Mvav 152,152,220
X 181
Mvav 156,156,220
D 5,20,94,1024
Ta 184,11,57344
Mvav 124,156,212
X 182
Mvav 92,156,204
D 6,20,94,2048
K 202,180,181
Mvav 60,156,196
W 2
X 183
Cvaa 160,128,0,224
D 7,20,94,3072
Ra 144,242,12288
Mvaa 160,132,4
s_add_u32 s94, s94, 256
X 184
Cvaa 164,132,20,228
W 2
Ta 186,11,57600
K 203,182,183
Maaa 232,220,228
X 185
Maaa 236,224,228
Ta 188,12,49152
Ra 148,242,13312
Mvaa 164,128,16
X 186
Cvaa 168,128,32,232
Ta 190,12,49408
K 210,184,185
Mvaa 168,132,36
Ra 192,13,62464
Ta 196,11,59392
Cvaa 172,132,52,236
X 187
Mvaa 172,128,48
X 188
Mvaa 160,136,8
Ra 152,242,14336
K 211,186,187
Mvaa 160,140,12
Ta 198,11,59648
Ta 200,12,51200
Mvaa 164,140,28
X 189
Mvaa 164,136,24
W 4
X 190
Maaa 232,184,192
Ra 156,242,15360
K 218,188,189
Maaa 236,188,192
Ta 202,12,51456
Ra 204,13,62976
Mvaa 168,136,40
X 191
Mvaa 168,140,44
Ra 128,242,24576
Ra 132,242,25600
Mvaa 172,140,60
X 160
Mvaa 172,136,56
W 4
Ra 136,242,26624
K 219,190,191
Cvaa 176,144,0,224
X 161
Mvaa 176,148,4
X 162
Cvaa 180,148,20,228
Ra 140,242,27648
K 196,160,161
Mvaa 180,144,16
X 163
Cvaa 184,144,32,232
W 4
X 164
Maaa 232,196,204
K 197,162,163
Maaa 236,200,204
X 165
Mvaa 184,148,36
X 166
Cvaa 188,148,52,236
K 204,164,165
Mvaa 188,144,48
X 167
Mvaa 176,152,8
Ra 144,242,28672
G 252,232
Mvaa 176,156,12
G 253,233
G 254,234
Mvaa 180,156,28
X 168
Mvaa 180,152,24
G 255,235
G 8,236
Mvaa 184,152,40
Ra 148,242,29696
G 9,237
Mvaa 184,156,44
G 10,238
G 14,239
Mvaa 188,156,60
K 252,252,8
K 253,253,9
Mvaa 188,152,56
W 2
K 205,166,167
K 254,254,10
Mvav 32,128,192
K 255,255,14
v_and_b32 v8, 0xffff0000, v252
Mvav 64,128,200
v_and_b32 v9, 0xffff0000, v253
v_and_b32 v10, 0xffff0000, v254
Mvav 96,128,208
X 169
Mvav 128,128,216
v_and_b32 v14, 0xffff0000, v255
v_lshlrev_b32 v252, 16, v252
Mvav 132,132,216
v_lshlrev_b32 v253, 16, v253
v_lshlrev_b32 v254, 16, v254
Mvav 100,132,208
v_lshlrev_b32 v255, 16, v255
v_mul_f32 v15, 0xbfb8aa3b, v252
Mvav 68,132,200
v_mul_f32 v2, 0xbfb8aa3b, v253
v_mul_f32 v3, 0xbfb8aa3b, v254
Mvav 36,132,192
X 170
Mvav 40,136,192
v_mul_f32 v27, 0xbfb8aa3b, v255
K 212,168,169
Mvav 72,136,200
X 15
Mvav 104,136,208
Ra 152,242,30720
Ra 156,242,31744
Mvav 136,136,216
X 2
X 3
X 27
v_add_f32 v15, 1.0, v15
v_add_f32 v2, 1.0, v2
v_add_f32 v3, 1.0, v3
v_add_f32 v27, 1.0, v27
v_rcp_f32 v15, v15
v_rcp_f32 v2, v2
v_rcp_f32 v3, v3
v_rcp_f32 v27, v27
U 252,252,15
U 253,253,2
U 254,254,3
U 255,255,27
K 252,252,252
K 253,253,253
K 254,254,254
K 255,255,255
v_lshlrev_b32 v252, 16, v252
v_lshlrev_b32 v253, 16, v253
v_lshlrev_b32 v254, 16, v254
v_lshlrev_b32 v255, 16, v255
U 252,252,8
U 253,253,9
U 254,254,10
U 255,255,14
K 246,252,253
K 247,254,255
s_lshl_b32 s81, s14, 3
v_add_u32 v2, s81, v1
s_mov_b32 s86, 0x00ff00ff
s_mov_b32 s87, 0x00ff00ff
v_cndmask_b32_e64 v252, 0, v246, s[86:87]
v_cndmask_b32_e64 v253, 0, v247, s[86:87]
v_cndmask_b32_e64 v246, v246, 0, s[86:87]
v_cndmask_b32_e64 v247, v247, 0, s[86:87]
ds_write_b64 v2, v[252:253] offset:63488
ds_write_b64 v2, v[246:247] offset:64512
Mvav 140,140,216
X 171
Mvav 108,140,208
v_xor_b32 v242, 0x10000, v242
Mvav 76,140,200
s_waitcnt vmcnt(0) lgkmcnt(0)
s_barrier
Mvav 44,140,192
X 172
Mvav 48,144,192
Ra 128,242,0
K 213,170,171
Mvav 80,144,200
X 173
Mvav 112,144,208
X 174
Mvav 144,144,216
Ra 132,242,1024
K 220,172,173
Mvav 148,148,216
X 175
Mvav 116,148,208
X 176
Mvav 84,148,200
Ra 136,242,2048
K 221,174,175
Mvav 52,148,192
X 177
Mvav 56,152,192
X 178
Mvav 88,152,200
Ra 140,242,3072
K 198,176,177
Mvav 120,152,208
X 179
Mvav 152,152,216
X 180
Mvav 156,156,216
K 199,178,179
Mvav 124,156,208
X 181
Mvav 92,156,200
X 182
Mvav 60,156,192
K 206,180,181
X 183
s_nop 0
K 207,182,183
W 0
Cvaa 160,128,0,224
Ra 144,242,4096
Ra 148,242,5120
Mvaa 160,132,4
s_xor_b32 s62, s62, 0x10000
X 184
Cvaa 164,132,20,228
Ra 216,1,63488
Ra 220,1,64512
Mvaa 164,128,16
v_lshrrev_b32 v8, 6, v1
v_and_b32 v9, 7, v8
Cvaa 168,128,32,232
s_lshl_b32 s81, s14, 2
X 185
Mvaa 168,132,36
v_xor_b32 v9, s81, v9
v_lshlrev_b32 v9, 5, v9
Cvaa 172,132,52,236
X 186
Mvaa 172,128,48
v_lshl_add_u32 v4, v8, 8, v9
v_bfe_u32 v9, v1, 4, 2
Mvaa 160,136,8
v_lshl_add_u32 v4, v9, 3, v4
v_add_u32 v4, 98304, v4
Mvaa 160,140,12
K 214,184,185
v_xor_b32 v5, 32, v4
Mvaa 164,140,28
v_xor_b32 v6, 64, v4
v_xor_b32 v7, 96, v4
Ta 184,4,0
Ta 186,4,4096
Mvaa 164,136,24
Ta 188,5,0
Ta 190,5,4096
Mvaa 168,136,40
Ra 152,242,6144
Ta 192,6,0
Mvaa 168,140,44
W 4
X 187
Zaaa 232,184,216
Ta 194,6,4096
v_and_b32 v11, 0x70, v1
Mvaa 172,140,60
v_mul_u32_u24 v11, 160, v11
v_lshrrev_b32 v10, 8, v1
Mvaa 172,136,56
X 188
v_smfmac_f32_16x16x64_bf16 a[160:163], a[176:179], v[192:199], v240
Ra 156,242,7168
W 4
Maaa 232,188,220
K 215,186,187
v_lshl_add_u32 v11, v10, 4, v11
v_smfmac_f32_16x16x64_bf16 a[164:167], a[176:179], v[200:207], v240
X 189
X 190
K 222,188,189
X 191
X 160
K 223,190,191
v_smfmac_f32_16x16x64_bf16 a[168:171], a[176:179], v[208:215], v240
Ta 196,7,0
v_bfe_u32 v10, v1, 7, 1
v_smfmac_f32_16x16x64_bf16 a[172:175], a[176:179], v[216:223], v240
W 2
Ta 198,7,4096
v_lshl_add_u32 v10, v10, 6, v11
Zaaa 236,192,216
W 2
s_mul_i32 s94, s85, 20480
s_lshl_b32 s82, s14, 8
Cvaa 176,144,0,224
s_add_u32 s94, s94, s82
Ta 200,4,8192
Ta 202,4,12288
Mvaa 176,148,4
s_add_u32 s94, s94, 2752512
s_lshl_b32 s71, s85, 4
Ra 128,242,16384
Cvaa 180,148,20,228
W 3
s_lshl_b32 s82, s14, 3
Ta 204,5,8192
Maaa 236,196,220
s_add_u32 s71, s71, s82
s_add_u32 s71, s71, 2560
Ta 206,5,12288
Mvaa 180,144,16
X 161
Cvaa 184,144,32,232
Ra 132,242,17408
Ta 208,6,8192
Mvaa 184,148,36
W 5
X 162
Zaaa 240,200,216
Ta 210,6,12288
K 192,160,161
Cvaa 188,148,52,236
buffer_store_dwordx4 a[232:235], v10, s[8:11], s94 offen offset:0 sc1
Ta 212,7,8192
Mvaa 188,144,48
X 163
Mvaa 176,152,8
W 4
X 164
Maaa 240,204,220
Ra 136,242,18432
Ta 214,7,12288
Mvaa 176,156,12
Ta 184,4,16384
K 193,162,163
Mvaa 180,156,28
X 165
Mvaa 180,152,24
W 4
Ta 186,4,20480
Ra 140,242,19456
Zaaa 244,208,216
X 166
Mvaa 184,152,40
buffer_store_dwordx4 a[236:239], v10, s[8:11], s94 offen offset:128 sc1
K 200,164,165
Mvaa 184,156,44
X 167
Mvaa 188,156,60
W 3
Ta 188,5,16384
Ta 190,5,20480
Maaa 244,212,220
s_add_u32 s64, s64, 1
X 168
Mvaa 188,152,56
W 2
Ra 144,242,20480
K 201,166,167
Mvav 32,128,196
Ta 192,6,16384
Ta 194,6,20480
Mvav 64,128,204
X 169
Zaaa 248,184,216
buffer_store_dwordx4 a[240:243], v10, s[8:11], s94 offen offset:512 sc1
Ta 196,7,16384
Mvav 96,128,212
X 170
Mvav 128,128,220
Ra 148,242,21504
K 208,168,169
Mvav 132,132,220
W 5
Ta 198,7,20480
Ta 200,4,24576
Maaa 248,188,220
X 171
Mvav 100,132,212
X 172
Mvav 68,132,204
Ta 202,4,28672
K 209,170,171
Mvav 36,132,196
W 5
Ra 152,242,22528
)"
R"(buffer_store_dwordx4 a[244:247], v10, s[8:11], s94 offen offset:640 sc1
Zaaa 252,192,216
X 173
Mvav 40,136,196
X 174
Mvav 72,136,204
Ta 204,5,24576
Ta 206,5,28672
Mvav 104,136,212
W 5
Ra 156,242,23552
K 216,172,173
Maaa 252,196,220
X 175
Mvav 136,136,220
Ta 208,6,24576
Ta 210,6,28672
Mvav 140,140,220
X 176
Mvav 108,140,212
W 6
buffer_store_dwordx4 a[248:251], v10, s[8:11], s94 offen offset:1024 sc1
K 217,174,175
Zaaa 232,200,216
X 177
Mvav 76,140,204
Ta 212,7,24576
Ra 128,242,8192
Mvav 44,140,196
W 4
Ta 214,7,28672
Ta 184,4,32768
Mvav 48,144,196
X 178
Maaa 232,204,220
Ra 132,242,9216
K 194,176,177
Mvav 80,144,204
X 179
Mvav 112,144,212
Ta 186,4,36864
buffer_store_dwordx4 a[252:255], v10, s[8:11], s94 offen offset:1152 sc1
Mvav 144,144,220
W 6
X 180
Zaaa 236,208,216
Ta 188,5,32768
K 195,178,179
Mvav 148,148,220
X 181
Mvav 116,148,212
Ta 190,5,36864
Ra 136,242,10240
Mvav 84,148,204
W 6
X 182
Maaa 236,212,220
Ta 192,6,32768
K 202,180,181
Mvav 52,148,196
Ta 194,6,36864
Ra 140,242,11264
Mvav 56,152,196
X 183
Mvav 88,152,204
W 6
buffer_store_dwordx4 a[232:235], v10, s[8:11], s94 offen offset:1536 sc1
Ta 196,7,32768
Zaaa 240,184,216
X 184
Mvav 120,152,212
Ta 198,7,36864
K 203,182,183
Mvav 152,152,220
Ra 144,242,12288
Ra 148,242,13312
Mvav 156,156,220
W 8
X 185
Maaa 240,188,220
X 186
Mvav 124,156,212
buffer_store_dwordx4 a[236:239], v10, s[8:11], s94 offen offset:1664 sc1
K 210,184,185
Mvav 92,156,204
X 187
Mvav 60,156,196
W 5
X 188
Zaaa 244,192,216
W 4
Ra 152,242,14336
K 211,186,187
Cvaa 160,128,0,224
X 189
Mvaa 160,132,4
X 190
Cvaa 164,132,20,228
W 3
Ra 156,242,15360
buffer_store_dwordx4 a[240:243], v10, s[8:11], s94 offen offset:2048 sc1
Maaa 244,196,220
K 218,188,189
Mvaa 164,128,16
X 191
Cvaa 168,128,32,232
Mvaa 168,132,36
Cvaa 172,132,52,236
Mvaa 172,128,48
Ra 128,242,24576
Mvaa 160,136,8
Ra 132,242,25600
buffer_store_dwordx4 a[244:247], v10, s[8:11], s94 offen offset:2176 sc1
Mvaa 160,140,12
Mvaa 164,140,28
Mvaa 164,136,24
Mvaa 168,136,40
Mvaa 168,140,44
Mvaa 172,140,60
Mvaa 172,136,56
W 2
Ra 136,242,26624
Cvaa 176,144,0,224
X 160
Mvaa 176,148,4
Ra 140,242,27648
K 219,190,191
Cvaa 180,148,20,228
X 161
Mvaa 180,144,16
X 162
Cvaa 184,144,32,232
K 196,160,161
Mvaa 184,148,36
X 163
Cvaa 188,148,52,236
X 164
Mvaa 188,144,48
Ra 144,242,28672
K 197,162,163
Mvaa 176,152,8
X 165
Mvaa 176,156,12
X 166
Mvaa 180,156,28
Ra 148,242,29696
K 204,164,165
Mvaa 180,152,24
X 167
Mvaa 184,152,40
X 168
Mvaa 184,156,44
K 205,166,167
Mvaa 188,156,60
X 169
Mvaa 188,152,56
W 2
X 170
Mvav 32,128,192
Ra 152,242,30720
K 212,168,169
Mvav 64,128,200
X 171
Mvav 96,128,208
Ra 156,242,31744
v_xor_b32 v242, 0x10000, v242
Mvav 128,128,216
X 172
Mvav 132,132,216
K 213,170,171
Mvav 100,132,208
X 173
Mvav 68,132,200
X 174
Mvav 36,132,192
Mvav 40,136,192
K 220,172,173
Mvav 72,136,200
X 175
Mvav 104,136,208
Mvav 136,136,216
X 176
Mvav 140,140,216
Mvav 108,140,208
K 221,174,175
Mvav 76,140,200
W 0
s_barrier
Mvav 44,140,192
X 177
Mvav 48,144,192
X 178
Mvav 80,144,200
Ra 128,242,0
K 198,176,177
Mvav 112,144,208
X 179
Mvav 144,144,216
Ra 132,242,1024
Mvav 148,148,216
X 180
Mvav 116,148,208
Ra 136,242,2048
Mvav 84,148,200
K 199,178,179
Mvav 52,148,192
X 181
Mvav 56,152,192
Ra 140,242,3072
Mvav 88,152,200
X 182
Mvav 120,152,208
Mvav 152,152,216
K 206,180,181
Mvav 156,156,216
X 183
Mvav 124,156,208
Mvav 92,156,200
s_waitcnt vmcnt(0)
v_mov_b64 v[28:29], s[12:13]
s_mov_b64 exec, 1
buffer_store_dwordx2 v[28:29], off, s[8:11], s71 sc1
s_mov_b64 exec, -1
K 207,182,183
Mvav 60,156,192
s_mov_b32 s85, 99
s_lshl_b32 s65, s57, 1
s_sub_u32 s65, s65, 1
s_branch .Ll0t_42%=
.Lf1r_43%=:
s_lshl_b32 s92, s58, 2
s_add_u32 s91, s92, s51
s_add_u32 s95, s92, 0
s_and_b32 s95, s95, 15
s_mul_i32 s95, s95, 40960
s_mul_i32 s71, s51, 0
s_add_u32 s71, s71, 133120
s_add_u32 m0, s71, s48
s_lshl2_add_u32 s94, s48, s95
D 16,40,94,0
s_add_u32 s95, s92, 1
s_and_b32 s95, s95, 15
s_mul_i32 s95, s95, 40960
s_mul_i32 s71, s51, 0
s_add_u32 s71, s71, 143360
D 17,40,94,1024
D 18,40,94,2048
D 19,40,94,3072
s_add_u32 m0, m0, 4096
s_nop 0
buffer_load_dwordx4 v20, s[40:43], s94 offen lds
s_add_u32 m0, s71, s48
s_lshl2_add_u32 s94, s48, s95
D 16,40,94,0
s_add_u32 s95, s92, 2
s_and_b32 s95, s95, 15
s_mul_i32 s95, s95, 40960
s_mul_i32 s71, s51, 0
s_add_u32 s71, s71, 153600
D 17,40,94,1024
D 18,40,94,2048
D 19,40,94,3072
s_add_u32 m0, m0, 4096
s_nop 0
buffer_load_dwordx4 v20, s[40:43], s94 offen lds
s_add_u32 m0, s71, s48
s_lshl2_add_u32 s94, s48, s95
D 16,40,94,0
D 17,40,94,1024
D 18,40,94,2048
D 19,40,94,3072
s_add_u32 m0, m0, 4096
s_nop 0
buffer_load_dwordx4 v20, s[40:43], s94 offen lds
s_branch .Lf1_36%=
.Lspin_11%=:
s_mov_b32 s84, 0
.Llp_13%=:
s_sleep 2
v_lshrrev_b32 v28, 1, v1
s_lshl_b32 s80, s60, 9
s_add_u32 s80, s80, 40960
buffer_load_dwordx2 v[28:29], v28, s[8:11], s80 offen sc1
s_waitcnt vmcnt(0)
s_add_u32 s84, s84, 1
v_cmp_eq_u32 vcc, s13, v29
s_cmp_eq_u64 vcc, -1
s_cbranch_scc1 .Lok_12%=
s_cmp_lt_u32 s84, 1048576
s_cbranch_scc1 .Llp_13%=
s_branch .Lok_12%=
.Lspin_16%=:
s_mov_b32 s84, 0
.Llp_18%=:
s_sleep 2
v_lshrrev_b32 v28, 1, v1
s_lshl_b32 s80, s60, 9
s_add_u32 s80, s80, 40960
buffer_load_dwordx2 v[28:29], v28, s[8:11], s80 offen sc1
s_waitcnt vmcnt(0)
s_add_u32 s84, s84, 1
v_cmp_eq_u32 vcc, s13, v29
s_cmp_eq_u64 vcc, -1
s_cbranch_scc1 .Lok_17%=
s_cmp_lt_u32 s84, 1048576
s_cbranch_scc1 .Llp_18%=
s_branch .Lok_17%=
.Lrt_19%=:
s_waitcnt vmcnt(0)
s_mov_b32 s86, 40960
buffer_load_dwordx4 v[32:35], v1, s[8:11], s86 offen offset:0 sc1
buffer_load_dwordx4 v[36:39], v1, s[8:11], s86 offen offset:1024 sc1
buffer_load_dwordx4 v[40:43], v1, s[8:11], s86 offen offset:2048 sc1
buffer_load_dwordx4 v[44:47], v1, s[8:11], s86 offen offset:3072 sc1
s_mov_b32 s86, 45056
buffer_load_dwordx4 v[48:51], v1, s[8:11], s86 offen offset:0 sc1
buffer_load_dwordx4 v[52:55], v1, s[8:11], s86 offen offset:1024 sc1
buffer_load_dwordx4 v[56:59], v1, s[8:11], s86 offen offset:2048 sc1
buffer_load_dwordx4 v[60:63], v1, s[8:11], s86 offen offset:3072 sc1
s_mov_b32 s86, 49152
buffer_load_dwordx4 v[64:67], v1, s[8:11], s86 offen offset:0 sc1
buffer_load_dwordx4 v[68:71], v1, s[8:11], s86 offen offset:1024 sc1
buffer_load_dwordx4 v[72:75], v1, s[8:11], s86 offen offset:2048 sc1
buffer_load_dwordx4 v[76:79], v1, s[8:11], s86 offen offset:3072 sc1
s_mov_b32 s86, 53248
buffer_load_dwordx4 v[80:83], v1, s[8:11], s86 offen offset:0 sc1
buffer_load_dwordx4 v[84:87], v1, s[8:11], s86 offen offset:1024 sc1
buffer_load_dwordx4 v[88:91], v1, s[8:11], s86 offen offset:2048 sc1
buffer_load_dwordx4 v[92:95], v1, s[8:11], s86 offen offset:3072 sc1
s_branch .Lrtr_20%=
.Lrt_21%=:
v_lshrrev_b32 v136, 1, v1
s_mov_b32 s84, 0
.LL_23%=:
s_waitcnt vmcnt(0)
v_min3_u32 v96, v33, v35, v95
v_max3_u32 v97, v33, v35, v95
v_min3_u32 v96, v96, v37, v39
v_max3_u32 v97, v97, v37, v39
v_min3_u32 v96, v96, v41, v43
v_max3_u32 v97, v97, v41, v43
v_min3_u32 v96, v96, v45, v47
v_max3_u32 v97, v97, v45, v47
v_min3_u32 v96, v96, v49, v51
v_max3_u32 v97, v97, v49, v51
v_min3_u32 v96, v96, v53, v55
v_max3_u32 v97, v97, v53, v55
v_min3_u32 v96, v96, v57, v59
v_max3_u32 v97, v97, v57, v59
v_min3_u32 v96, v96, v61, v63
v_max3_u32 v97, v97, v61, v63
v_min3_u32 v96, v96, v65, v67
v_max3_u32 v97, v97, v65, v67
v_min3_u32 v96, v96, v69, v71
v_max3_u32 v97, v97, v69, v71
v_min3_u32 v96, v96, v73, v75
v_max3_u32 v97, v97, v73, v75
v_min3_u32 v96, v96, v77, v79
v_max3_u32 v97, v97, v77, v79
v_min3_u32 v96, v96, v81, v83
v_max3_u32 v97, v97, v81, v83
v_min3_u32 v96, v96, v85, v87
v_max3_u32 v97, v97, v85, v87
v_min3_u32 v96, v96, v89, v91
v_max3_u32 v97, v97, v89, v91
v_min_u32 v96, v96, v93
v_max_u32 v97, v97, v93
v_cmp_eq_u32 vcc, s13, v96
v_cmp_eq_u32_e64 s[86:87], s13, v97
s_and_b64 vcc, vcc, s[86:87]
s_cmp_eq_u64 vcc, -1
s_cbranch_scc1 .LL_24%=
s_sleep 2
s_mov_b32 s86, 40960
buffer_load_dwordx4 v[32:35], v1, s[8:11], s86 offen offset:0 sc1
buffer_load_dwordx4 v[36:39], v1, s[8:11], s86 offen offset:1024 sc1
buffer_load_dwordx4 v[40:43], v1, s[8:11], s86 offen offset:2048 sc1
buffer_load_dwordx4 v[44:47], v1, s[8:11], s86 offen offset:3072 sc1
s_mov_b32 s86, 45056
buffer_load_dwordx4 v[48:51], v1, s[8:11], s86 offen offset:0 sc1
buffer_load_dwordx4 v[52:55], v1, s[8:11], s86 offen offset:1024 sc1
buffer_load_dwordx4 v[56:59], v1, s[8:11], s86 offen offset:2048 sc1
buffer_load_dwordx4 v[60:63], v1, s[8:11], s86 offen offset:3072 sc1
s_mov_b32 s86, 49152
buffer_load_dwordx4 v[64:67], v1, s[8:11], s86 offen offset:0 sc1
buffer_load_dwordx4 v[68:71], v1, s[8:11], s86 offen offset:1024 sc1
buffer_load_dwordx4 v[72:75], v1, s[8:11], s86 offen offset:2048 sc1
buffer_load_dwordx4 v[76:79], v1, s[8:11], s86 offen offset:3072 sc1
s_mov_b32 s86, 53248
buffer_load_dwordx4 v[80:83], v1, s[8:11], s86 offen offset:0 sc1
buffer_load_dwordx4 v[84:87], v1, s[8:11], s86 offen offset:1024 sc1
buffer_load_dwordx4 v[88:91], v1, s[8:11], s86 offen offset:2048 sc1
buffer_load_dwordx4 v[92:95], v1, s[8:11], s86 offen offset:3072 sc1
s_add_u32 s84, s84, 1
s_cmp_lt_u32 s84, 1048576
s_cbranch_scc1 .LL_23%=
s_waitcnt vmcnt(0)
.LL_24%=:
v_mov_b32 v96, v32
v_mov_b32 v97, 0
)"
R"(v_mov_b32 v98, 0
v_mov_b32 v100, 0
v_med3_u32 v98, v97, v98, v34
v_med3_u32 v97, v96, v97, v34
v_max_u32 v96, v96, v34
v_med3_u32 v98, v97, v98, v36
v_med3_u32 v97, v96, v97, v36
v_max_u32 v96, v96, v36
v_med3_u32 v98, v97, v98, v38
v_med3_u32 v97, v96, v97, v38
v_max_u32 v96, v96, v38
v_med3_u32 v98, v97, v98, v40
v_med3_u32 v97, v96, v97, v40
v_max_u32 v96, v96, v40
v_med3_u32 v98, v97, v98, v42
v_med3_u32 v97, v96, v97, v42
v_max_u32 v96, v96, v42
v_med3_u32 v98, v97, v98, v44
v_med3_u32 v97, v96, v97, v44
v_max_u32 v96, v96, v44
v_med3_u32 v98, v97, v98, v46
v_med3_u32 v97, v96, v97, v46
v_max_u32 v96, v96, v46
v_med3_u32 v98, v97, v98, v48
v_med3_u32 v97, v96, v97, v48
v_max_u32 v96, v96, v48
v_med3_u32 v98, v97, v98, v50
v_med3_u32 v97, v96, v97, v50
v_max_u32 v96, v96, v50
v_med3_u32 v98, v97, v98, v52
v_med3_u32 v97, v96, v97, v52
v_max_u32 v96, v96, v52
v_med3_u32 v98, v97, v98, v54
v_med3_u32 v97, v96, v97, v54
v_max_u32 v96, v96, v54
v_med3_u32 v98, v97, v98, v56
v_med3_u32 v97, v96, v97, v56
v_max_u32 v96, v96, v56
v_med3_u32 v98, v97, v98, v58
v_med3_u32 v97, v96, v97, v58
v_max_u32 v96, v96, v58
v_med3_u32 v98, v97, v98, v60
v_med3_u32 v97, v96, v97, v60
v_max_u32 v96, v96, v60
v_med3_u32 v98, v97, v98, v62
v_med3_u32 v97, v96, v97, v62
v_max_u32 v96, v96, v62
v_med3_u32 v98, v97, v98, v64
v_med3_u32 v97, v96, v97, v64
v_max_u32 v96, v96, v64
v_med3_u32 v98, v97, v98, v66
v_med3_u32 v97, v96, v97, v66
v_max_u32 v96, v96, v66
v_med3_u32 v98, v97, v98, v68
v_med3_u32 v97, v96, v97, v68
v_max_u32 v96, v96, v68
v_med3_u32 v98, v97, v98, v70
v_med3_u32 v97, v96, v97, v70
v_max_u32 v96, v96, v70
v_med3_u32 v98, v97, v98, v72
v_med3_u32 v97, v96, v97, v72
v_max_u32 v96, v96, v72
v_med3_u32 v98, v97, v98, v74
v_med3_u32 v97, v96, v97, v74
v_max_u32 v96, v96, v74
v_med3_u32 v98, v97, v98, v76
v_med3_u32 v97, v96, v97, v76
v_max_u32 v96, v96, v76
v_med3_u32 v98, v97, v98, v78
v_med3_u32 v97, v96, v97, v78
v_max_u32 v96, v96, v78
v_med3_u32 v98, v97, v98, v80
v_med3_u32 v97, v96, v97, v80
v_max_u32 v96, v96, v80
v_med3_u32 v98, v97, v98, v82
v_med3_u32 v97, v96, v97, v82
v_max_u32 v96, v96, v82
v_med3_u32 v98, v97, v98, v84
v_med3_u32 v97, v96, v97, v84
v_max_u32 v96, v96, v84
v_med3_u32 v98, v97, v98, v86
v_med3_u32 v97, v96, v97, v86
v_max_u32 v96, v96, v86
v_med3_u32 v98, v97, v98, v88
v_med3_u32 v97, v96, v97, v88
v_max_u32 v96, v96, v88
v_med3_u32 v98, v97, v98, v90
v_med3_u32 v97, v96, v97, v90
v_max_u32 v96, v96, v90
v_med3_u32 v98, v97, v98, v92
v_med3_u32 v97, v96, v97, v92
v_max_u32 v96, v96, v92
v_med3_u32 v98, v97, v98, v94
v_med3_u32 v97, v96, v97, v94
v_max_u32 v96, v96, v94
v_mov_b32 v99, v96
s_nop 1
v_max_u32_dpp v99, v99, v99 row_shr:1 row_mask:0xf bank_mask:0xf
s_nop 1
v_max_u32_dpp v99, v99, v99 row_shr:2 row_mask:0xf bank_mask:0xf
s_nop 1
v_max_u32_dpp v99, v99, v99 row_shr:4 row_mask:0xf bank_mask:0xf
s_nop 1
v_max_u32_dpp v99, v99, v99 row_shr:8 row_mask:0xf bank_mask:0xf
s_nop 1
v_max_u32_dpp v99, v99, v99 row_bcast:15 row_mask:0xa bank_mask:0xf
s_nop 1
v_max_u32_dpp v99, v99, v99 row_bcast:31 row_mask:0xc bank_mask:0xf
s_nop 0
v_readlane_b32 s86, v99, 63
s_nop 1
v_cmp_eq_u32 vcc, s86, v96
s_and_b32 s87, s86, 0xffff
s_sub_u32 s87, 2047, s87
v_cndmask_b32 v96, v96, v97, vcc
v_cndmask_b32_e64 v97, v97, 0, vcc
v_writelane_b32 v100, s87, 0
v_mov_b32 v99, v96
s_nop 1
v_max_u32_dpp v99, v99, v99 row_shr:1 row_mask:0xf bank_mask:0xf
s_nop 1
v_max_u32_dpp v99, v99, v99 row_shr:2 row_mask:0xf bank_mask:0xf
s_nop 1
v_max_u32_dpp v99, v99, v99 row_shr:4 row_mask:0xf bank_mask:0xf
s_nop 1
v_max_u32_dpp v99, v99, v99 row_shr:8 row_mask:0xf bank_mask:0xf
s_nop 1
v_max_u32_dpp v99, v99, v99 row_bcast:15 row_mask:0xa bank_mask:0xf
s_nop 1
v_max_u32_dpp v99, v99, v99 row_bcast:31 row_mask:0xc bank_mask:0xf
s_nop 0
v_readlane_b32 s86, v99, 63
s_nop 1
v_cmp_eq_u32 vcc, s86, v96
s_and_b32 s87, s86, 0xffff
s_sub_u32 s87, 2047, s87
v_cndmask_b32 v96, v96, v97, vcc
v_cndmask_b32_e64 v97, v97, 0, vcc
v_writelane_b32 v100, s87, 1
v_mov_b32 v99, v96
s_nop 1
v_max_u32_dpp v99, v99, v99 row_shr:1 row_mask:0xf bank_mask:0xf
s_nop 1
v_max_u32_dpp v99, v99, v99 row_shr:2 row_mask:0xf bank_mask:0xf
s_nop 1
v_max_u32_dpp v99, v99, v99 row_shr:4 row_mask:0xf bank_mask:0xf
s_nop 1
v_max_u32_dpp v99, v99, v99 row_shr:8 row_mask:0xf bank_mask:0xf
s_nop 1
v_max_u32_dpp v99, v99, v99 row_bcast:15 row_mask:0xa bank_mask:0xf
s_nop 1
v_max_u32_dpp v99, v99, v99 row_bcast:31 row_mask:0xc bank_mask:0xf
s_nop 0
v_readlane_b32 s86, v99, 63
s_nop 1
v_cmp_eq_u32 vcc, s86, v96
s_and_b32 s87, s86, 0xffff
s_sub_u32 s87, 2047, s87
v_cndmask_b32 v96, v96, v97, vcc
v_cndmask_b32_e64 v97, v97, 0, vcc
v_writelane_b32 v100, s87, 2
v_mov_b32 v99, v96
s_nop 1
v_max_u32_dpp v99, v99, v99 row_shr:1 row_mask:0xf bank_mask:0xf
s_nop 1
v_max_u32_dpp v99, v99, v99 row_shr:2 row_mask:0xf bank_mask:0xf
s_nop 1
v_max_u32_dpp v99, v99, v99 row_shr:4 row_mask:0xf bank_mask:0xf
s_nop 1
v_max_u32_dpp v99, v99, v99 row_shr:8 row_mask:0xf bank_mask:0xf
s_nop 1
v_max_u32_dpp v99, v99, v99 row_bcast:15 row_mask:0xa bank_mask:0xf
s_nop 1
v_max_u32_dpp v99, v99, v99 row_bcast:31 row_mask:0xc bank_mask:0xf
s_nop 0
v_readlane_b32 s86, v99, 63
s_nop 1
v_cmp_eq_u32 vcc, s86, v96
s_and_b32 s87, s86, 0xffff
s_sub_u32 s87, 2047, s87
v_cndmask_b32 v96, v96, v97, vcc
v_cndmask_b32_e64 v97, v97, 0, vcc
v_writelane_b32 v100, s87, 3
v_mov_b32 v99, v96
s_nop 1
v_max_u32_dpp v99, v99, v99 row_shr:1 row_mask:0xf bank_mask:0xf
s_nop 1
v_max_u32_dpp v99, v99, v99 row_shr:2 row_mask:0xf bank_mask:0xf
s_nop 1
v_max_u32_dpp v99, v99, v99 row_shr:4 row_mask:0xf bank_mask:0xf
s_nop 1
v_max_u32_dpp v99, v99, v99 row_shr:8 row_mask:0xf bank_mask:0xf
s_nop 1
v_max_u32_dpp v99, v99, v99 row_bcast:15 row_mask:0xa bank_mask:0xf
s_nop 1
v_max_u32_dpp v99, v99, v99 row_bcast:31 row_mask:0xc bank_mask:0xf
s_nop 0
v_readlane_b32 s86, v99, 63
s_nop 1
v_cmp_eq_u32 vcc, s86, v96
s_and_b32 s87, s86, 0xffff
s_sub_u32 s87, 2047, s87
v_cndmask_b32 v96, v96, v97, vcc
v_cndmask_b32_e64 v97, v97, 0, vcc
v_writelane_b32 v100, s87, 4
v_mov_b32 v99, v96
s_nop 1
v_max_u32_dpp v99, v99, v99 row_shr:1 row_mask:0xf bank_mask:0xf
s_nop 1
v_max_u32_dpp v99, v99, v99 row_shr:2 row_mask:0xf bank_mask:0xf
s_nop 1
v_max_u32_dpp v99, v99, v99 row_shr:4 row_mask:0xf bank_mask:0xf
s_nop 1
v_max_u32_dpp v99, v99, v99 row_shr:8 row_mask:0xf bank_mask:0xf
s_nop 1
v_max_u32_dpp v99, v99, v99 row_bcast:15 row_mask:0xa bank_mask:0xf
s_nop 1
v_max_u32_dpp v99, v99, v99 row_bcast:31 row_mask:0xc bank_mask:0xf
s_nop 0
v_readlane_b32 s86, v99, 63
s_nop 1
v_cmp_eq_u32 vcc, s86, v96
s_and_b32 s87, s86, 0xffff
s_sub_u32 s87, 2047, s87
v_cndmask_b32 v96, v96, v97, vcc
v_cndmask_b32_e64 v97, v97, 0, vcc
v_writelane_b32 v100, s87, 5
v_mov_b32 v99, v96
s_nop 1
v_max_u32_dpp v99, v99, v99 row_shr:1 row_mask:0xf bank_mask:0xf
s_nop 1
v_max_u32_dpp v99, v99, v99 row_shr:2 row_mask:0xf bank_mask:0xf
s_nop 1
v_max_u32_dpp v99, v99, v99 row_shr:4 row_mask:0xf bank_mask:0xf
s_nop 1
v_max_u32_dpp v99, v99, v99 row_shr:8 row_mask:0xf bank_mask:0xf
s_nop 1
v_max_u32_dpp v99, v99, v99 row_bcast:15 row_mask:0xa bank_mask:0xf
s_nop 1
v_max_u32_dpp v99, v99, v99 row_bcast:31 row_mask:0xc bank_mask:0xf
s_nop 0
v_readlane_b32 s86, v99, 63
s_nop 1
v_cmp_eq_u32 vcc, s86, v96
s_and_b32 s87, s86, 0xffff
s_sub_u32 s87, 2047, s87
v_cndmask_b32 v96, v96, v97, vcc
v_cndmask_b32_e64 v97, v97, 0, vcc
v_writelane_b32 v100, s87, 6
v_mov_b32 v99, v96
s_nop 1
v_max_u32_dpp v99, v99, v99 row_shr:1 row_mask:0xf bank_mask:0xf
s_nop 1
v_max_u32_dpp v99, v99, v99 row_shr:2 row_mask:0xf bank_mask:0xf
s_nop 1
v_max_u32_dpp v99, v99, v99 row_shr:4 row_mask:0xf bank_mask:0xf
s_nop 1
v_max_u32_dpp v99, v99, v99 row_shr:8 row_mask:0xf bank_mask:0xf
s_nop 1
v_max_u32_dpp v99, v99, v99 row_bcast:15 row_mask:0xa bank_mask:0xf
s_nop 1
v_max_u32_dpp v99, v99, v99 row_bcast:31 row_mask:0xc bank_mask:0xf
s_nop 0
v_readlane_b32 s86, v99, 63
s_nop 1
v_cmp_eq_u32 vcc, s86, v96
s_and_b32 s87, s86, 0xffff
s_sub_u32 s87, 2047, s87
v_cndmask_b32 v96, v96, v97, vcc
v_cndmask_b32_e64 v97, v97, 0, vcc
v_writelane_b32 v100, s87, 7
v_mov_b32 v99, v98
s_nop 1
v_max_u32_dpp v99, v99, v99 row_shr:1 row_mask:0xf bank_mask:0xf
s_nop 1
v_max_u32_dpp v99, v99, v99 row_shr:2 row_mask:0xf bank_mask:0xf
s_nop 1
v_max_u32_dpp v99, v99, v99 row_shr:4 row_mask:0xf bank_mask:0xf
s_nop 1
v_max_u32_dpp v99, v99, v99 row_shr:8 row_mask:0xf bank_mask:0xf
s_nop 1
v_max_u32_dpp v99, v99, v99 row_bcast:15 row_mask:0xa bank_mask:0xf
s_nop 1
v_max_u32_dpp v99, v99, v99 row_bcast:31 row_mask:0xc bank_mask:0xf
s_nop 0
v_readlane_b32 s87, v99, 63
s_nop 0
s_cmp_gt_u32 s87, s86
s_cbranch_scc0 .LL_25%=
s_mov_b32 m0, 0
.LL_26%=:
v_max3_u32 v96, v32, v34, v36
v_max3_u32 v96, v96, v38, v40
v_max3_u32 v96, v96, v42, v44
v_max3_u32 v96, v96, v46, v48
v_max3_u32 v96, v96, v50, v52
v_max3_u32 v96, v96, v54, v56
v_max3_u32 v96, v96, v58, v60
v_max3_u32 v96, v96, v62, v64
v_max3_u32 v96, v96, v66, v68
v_max3_u32 v96, v96, v70, v72
v_max3_u32 v96, v96, v74, v76
v_max3_u32 v96, v96, v78, v80
v_max3_u32 v96, v96, v82, v84
v_max3_u32 v96, v96, v86, v88
v_max3_u32 v96, v96, v90, v92
v_max_u32 v96, v96, v94
v_mov_b32 v99, v96
s_nop 1
v_max_u32_dpp v99, v99, v99 row_shr:1 row_mask:0xf bank_mask:0xf
s_nop 1
v_max_u32_dpp v99, v99, v99 row_shr:2 row_mask:0xf bank_mask:0xf
s_nop 1
v_max_u32_dpp v99, v99, v99 row_shr:4 row_mask:0xf bank_mask:0xf
s_nop 1
v_max_u32_dpp v99, v99, v99 row_shr:8 row_mask:0xf bank_mask:0xf
s_nop 1
v_max_u32_dpp v99, v99, v99 row_bcast:15 row_mask:0xa bank_mask:0xf
s_nop 1
v_max_u32_dpp v99, v99, v99 row_bcast:31 row_mask:0xc bank_mask:0xf
s_nop 0
v_readlane_b32 s86, v99, 63
s_nop 1
v_cmp_eq_u32 vcc, s86, v32
s_nop 1
v_cndmask_b32_e64 v32, v32, 0, vcc
v_cmp_eq_u32 vcc, s86, v34
s_nop 1
v_cndmask_b32_e64 v34, v34, 0, vcc
v_cmp_eq_u32 vcc, s86, v36
s_nop 1
v_cndmask_b32_e64 v36, v36, 0, vcc
v_cmp_eq_u32 vcc, s86, v38
s_nop 1
v_cndmask_b32_e64 v38, v38, 0, vcc
v_cmp_eq_u32 vcc, s86, v40
s_nop 1
v_cndmask_b32_e64 v40, v40, 0, vcc
v_cmp_eq_u32 vcc, s86, v42
s_nop 1
v_cndmask_b32_e64 v42, v42, 0, vcc
v_cmp_eq_u32 vcc, s86, v44
s_nop 1
v_cndmask_b32_e64 v44, v44, 0, vcc
v_cmp_eq_u32 vcc, s86, v46
s_nop 1
v_cndmask_b32_e64 v46, v46, 0, vcc
v_cmp_eq_u32 vcc, s86, v48
s_nop 1
v_cndmask_b32_e64 v48, v48, 0, vcc
v_cmp_eq_u32 vcc, s86, v50
s_nop 1
v_cndmask_b32_e64 v50, v50, 0, vcc
v_cmp_eq_u32 vcc, s86, v52
s_nop 1
v_cndmask_b32_e64 v52, v52, 0, vcc
v_cmp_eq_u32 vcc, s86, v54
s_nop 1
v_cndmask_b32_e64 v54, v54, 0, vcc
v_cmp_eq_u32 vcc, s86, v56
s_nop 1
v_cndmask_b32_e64 v56, v56, 0, vcc
v_cmp_eq_u32 vcc, s86, v58
s_nop 1
v_cndmask_b32_e64 v58, v58, 0, vcc
v_cmp_eq_u32 vcc, s86, v60
s_nop 1
v_cndmask_b32_e64 v60, v60, 0, vcc
v_cmp_eq_u32 vcc, s86, v62
s_nop 1
v_cndmask_b32_e64 v62, v62, 0, vcc
v_cmp_eq_u32 vcc, s86, v64
s_nop 1
v_cndmask_b32_e64 v64, v64, 0, vcc
v_cmp_eq_u32 vcc, s86, v66
s_nop 1
v_cndmask_b32_e64 v66, v66, 0, vcc
v_cmp_eq_u32 vcc, s86, v68
s_nop 1
v_cndmask_b32_e64 v68, v68, 0, vcc
v_cmp_eq_u32 vcc, s86, v70
s_nop 1
v_cndmask_b32_e64 v70, v70, 0, vcc
v_cmp_eq_u32 vcc, s86, v72
s_nop 1
v_cndmask_b32_e64 v72, v72, 0, vcc
v_cmp_eq_u32 vcc, s86, v74
s_nop 1
v_cndmask_b32_e64 v74, v74, 0, vcc
v_cmp_eq_u32 vcc, s86, v76
s_nop 1
v_cndmask_b32_e64 v76, v76, 0, vcc
v_cmp_eq_u32 vcc, s86, v78
s_nop 1
v_cndmask_b32_e64 v78, v78, 0, vcc
v_cmp_eq_u32 vcc, s86, v80
s_nop 1
v_cndmask_b32_e64 v80, v80, 0, vcc
v_cmp_eq_u32 vcc, s86, v82
s_nop 1
)"
R"(v_cndmask_b32_e64 v82, v82, 0, vcc
v_cmp_eq_u32 vcc, s86, v84
s_nop 1
v_cndmask_b32_e64 v84, v84, 0, vcc
v_cmp_eq_u32 vcc, s86, v86
s_nop 1
v_cndmask_b32_e64 v86, v86, 0, vcc
v_cmp_eq_u32 vcc, s86, v88
s_nop 1
v_cndmask_b32_e64 v88, v88, 0, vcc
v_cmp_eq_u32 vcc, s86, v90
s_nop 1
v_cndmask_b32_e64 v90, v90, 0, vcc
v_cmp_eq_u32 vcc, s86, v92
s_nop 1
v_cndmask_b32_e64 v92, v92, 0, vcc
v_cmp_eq_u32 vcc, s86, v94
s_nop 1
v_cndmask_b32_e64 v94, v94, 0, vcc
s_and_b32 s87, s86, 0xffff
s_sub_u32 s87, 2047, s87
s_nop 0
v_writelane_b32 v100, s87, m0
s_add_u32 m0, m0, 1
s_cmp_lt_u32 m0, 8
s_cbranch_scc1 .LL_26%=
.LL_25%=:
v_xor_b32 v102, s12, v100
v_mov_b32 v103, s13
s_mov_b64 exec, 0xff
buffer_store_dwordx2 v[102:103], v136, s[8:11], 0 offen offset:3584 sc1
s_mov_b64 exec, -1
s_branch .Lrtr_22%=
.Lspin_47%=:
s_mov_b32 s84, 0
.Llp_49%=:
s_sleep 2
buffer_load_dwordx2 v[8:9], v11, s[8:11], 0 offen offset:3840 sc1
s_waitcnt vmcnt(0)
s_add_u32 s84, s84, 1
v_cmp_eq_u64 vcc, s[12:13], v[8:9]
s_cmp_eq_u64 vcc, -1
s_cbranch_scc1 .Lok_48%=
s_cmp_lt_u32 s84, 1048576
s_cbranch_scc1 .Llp_49%=
s_branch .Lok_48%=
.Lredo%=:
s_waitcnt vmcnt(0) lgkmcnt(0)
s_barrier
s_and_b32 s81, s14, 1
s_mul_i32 s91, s81, 17164288
s_mul_i32 s82, s56, 536576
s_add_u32 s91, s91, s82
s_add_u32 s91, s91, 4194304
s_lshr_b32 s82, s14, 1
s_lshl_b32 s83, s82, 14
s_add_u32 s91, s91, s83
s_lshl_b32 s81, s81, 14
s_lshl_b32 s82, s82, 16
s_or_b32 s92, s81, s82
s_xor_b32 s92, s92, 0x10000
s_add_u32 m0, s92, 0
s_add_u32 s81, s91, 0
Dc 1,8,81,0
Dc 1,8,81,1024
Dc 1,8,81,2048
Dc 1,8,81,3072
s_add_u32 m0, s92, 4096
s_add_u32 s81, s91, 4096
Dc 1,8,81,0
Dc 1,8,81,1024
Dc 1,8,81,2048
Dc 1,8,81,3072
s_add_u32 m0, s92, 8192
s_add_u32 s81, s91, 8192
Dc 1,8,81,0
Dc 1,8,81,1024
Dc 1,8,81,2048
Dc 1,8,81,3072
s_add_u32 m0, s92, 12288
s_add_u32 s81, s91, 12288
Dc 1,8,81,0
Dc 1,8,81,1024
Dc 1,8,81,2048
Dc 1,8,81,3072
v_bfe_u32 v244, v1, 4, 1
v_mul_u32_u24 v244, 0xaaaa, v244
v_add_u32 v244, 0x4444, v244
v_lshl_or_b32 v240, v244, 16, v244
v_mov_b32 v241, 0xff800000
v_add_u32 v242, 0x10000, v1
v_bfe_u32 v244, v1, 4, 4
v_lshrrev_b32 v245, 8, v1
v_lshlrev_b32 v245, 2, v245
v_sub_u32 v244, v244, v245
v_cmp_le_i32_e64 s[72:73], 0, v244
v_cmp_le_i32_e64 s[74:75], 1, v244
v_cmp_le_i32_e64 s[76:77], 2, v244
v_cmp_le_i32_e64 s[78:79], 3, v244
v_add_u32 v243, 4096, v1
v_add_u32 v244, 8192, v1
v_add_u32 v245, 12288, v1
s_mov_b32 s18, 0x3f803f80
s_mov_b32 s19, 0x3f003f00
v_accvgpr_write_b32 a176, s18
v_accvgpr_write_b32 a180, s19
v_mov_b32 v252, v241
v_accvgpr_write_b32 a177, s18
v_accvgpr_write_b32 a181, s19
v_mov_b32 v253, v241
v_accvgpr_write_b32 a178, s18
v_accvgpr_write_b32 a182, s19
v_mov_b32 v254, v241
v_accvgpr_write_b32 a179, s18
v_accvgpr_write_b32 a183, s19
v_mov_b32 v255, v241
v_accvgpr_write_b32 a160, 0
v_accvgpr_write_b32 a161, 0
v_accvgpr_write_b32 a162, 0
v_accvgpr_write_b32 a163, 0
v_accvgpr_write_b32 a164, 0
v_accvgpr_write_b32 a165, 0
v_accvgpr_write_b32 a166, 0
v_accvgpr_write_b32 a167, 0
v_accvgpr_write_b32 a168, 0
v_accvgpr_write_b32 a169, 0
v_accvgpr_write_b32 a170, 0
v_accvgpr_write_b32 a171, 0
v_accvgpr_write_b32 a172, 0
v_accvgpr_write_b32 a173, 0
v_accvgpr_write_b32 a174, 0
v_accvgpr_write_b32 a175, 0
s_mov_b32 s90, 0x5d800000
s_mov_b64 s[88:89], 0
s_mov_b32 s64, 0
s_and_b32 s18, s14, 1
s_lshl_b32 s62, s18, 14
s_lshl_b32 s20, s57, 15
s_sub_u32 s20, 507904, s20
s_add_u32 s20, s20, s62
s_lshl_b32 s65, s57, 1
s_sub_u32 s65, s65, 1
s_max_i32 s65, s65, 0
s_sub_u32 s66, s60, 1
s_cmp_lt_u32 s14, 2
s_cselect_b32 s20, s20, 0
s_cselect_b32 s66, s66, 15
s_cselect_b32 s21, 32768, 0
s_add_u32 s61, s91, s20
s_add_u32 s62, s62, s21
s_sub_u32 s63, 16384, s21
s_lshl_b32 s67, s60, 7
s_add_u32 s67, s67, s56
s_lshl_b32 s67, s67, 12
s_add_u32 s67, s67, 39845888
s_lshl_b32 s69, s60, 3
s_add_u32 s69, s69, s59
s_lshl_b32 s69, s69, 6
s_lshl_b32 s21, s58, 3
s_add_u32 s69, s69, s21
s_add_u32 s69, s69, 20480
s_mov_b32 s70, 0
s_mov_b32 s37, 0
v_bfe_u32 v28, v1, 4, 4
v_lshrrev_b32 v29, 8, v1
v_lshlrev_b32 v29, 2, v29
v_sub_u32 v28, v28, v29
s_mov_b64 s[24:25], 0
s_mov_b32 s68, 0
s_mov_b32 s85, 99
s_lshl_b32 s80, s60, 6
s_and_b32 s81, s14, 2
s_lshl_b32 s81, s81, 4
s_xor_b32 s82, s81, 0
s_add_u32 s82, s82, s80
v_add_u32 v4, s82, v28
s_xor_b32 s82, s81, 16
s_add_u32 s82, s82, s80
v_add_u32 v5, s82, v28
s_xor_b32 s82, s81, 32
s_add_u32 s82, s82, s80
v_add_u32 v6, s82, v28
s_xor_b32 s82, s81, 48
s_add_u32 s82, s82, s80
v_add_u32 v7, s82, v28
s_mul_i32 s82, s56, 536576
s_add_u32 s82, s82, 4194304
s_lshl_b32 s84, s60, 1
s_add_u32 s84, s84, 2
.LL_50%=:
buffer_load_dwordx4 a[128:131], v1, s[8:11], s82 offen offset:0 sc1
buffer_load_dwordx4 a[132:135], v1, s[8:11], s82 offen offset:1024 sc1
buffer_load_dwordx4 a[136:139], v1, s[8:11], s82 offen offset:2048 sc1
buffer_load_dwordx4 a[140:143], v1, s[8:11], s82 offen offset:3072 sc1
buffer_load_dwordx4 a[144:147], v243, s[8:11], s82 offen offset:0 sc1
buffer_load_dwordx4 a[148:151], v243, s[8:11], s82 offen offset:1024 sc1
buffer_load_dwordx4 a[152:155], v243, s[8:11], s82 offen offset:2048 sc1
buffer_load_dwordx4 a[156:159], v243, s[8:11], s82 offen offset:3072 sc1
s_waitcnt vmcnt(0)
Zvaa 160,128,0
Zvaa 164,128,16
Zvaa 168,128,32
Zvaa 172,128,48
Mvaa 160,132,4
Mvaa 164,132,20
Mvaa 168,132,36
Mvaa 172,132,52
Mvaa 160,136,8
Mvaa 164,136,24
Mvaa 168,136,40
Mvaa 172,136,56
Mvaa 160,140,12
Mvaa 164,140,28
Mvaa 168,140,44
Mvaa 172,140,60
Zvaa 176,144,0
Zvaa 180,144,16
Zvaa 184,144,32
Zvaa 188,144,48
Mvaa 176,148,4
Mvaa 180,148,20
Mvaa 184,148,36
Mvaa 188,148,52
Mvaa 176,152,8
Mvaa 180,152,24
Mvaa 184,152,40
Mvaa 188,152,56
Mvaa 176,156,12
Mvaa 180,156,28
Mvaa 184,156,44
Mvaa 188,156,60
v_cmp_le_i32 vcc, 0, v4
s_nop 1
v_cndmask_b32 v160, v241, v160, vcc
v_max_f32 v252, v252, v160
v_cmp_le_i32 vcc, 1, v4
s_nop 1
v_cndmask_b32 v161, v241, v161, vcc
v_max_f32 v252, v252, v161
v_cmp_le_i32 vcc, 2, v4
s_nop 1
v_cndmask_b32 v162, v241, v162, vcc
v_max_f32 v252, v252, v162
v_cmp_le_i32 vcc, 3, v4
s_nop 1
v_cndmask_b32 v163, v241, v163, vcc
v_max_f32 v252, v252, v163
v_cmp_le_i32 vcc, 0, v5
s_nop 1
v_cndmask_b32 v164, v241, v164, vcc
v_max_f32 v253, v253, v164
v_cmp_le_i32 vcc, 1, v5
s_nop 1
v_cndmask_b32 v165, v241, v165, vcc
v_max_f32 v253, v253, v165
v_cmp_le_i32 vcc, 2, v5
s_nop 1
v_cndmask_b32 v166, v241, v166, vcc
v_max_f32 v253, v253, v166
v_cmp_le_i32 vcc, 3, v5
s_nop 1
v_cndmask_b32 v167, v241, v167, vcc
v_max_f32 v253, v253, v167
v_cmp_le_i32 vcc, 0, v6
s_nop 1
v_cndmask_b32 v168, v241, v168, vcc
v_max_f32 v254, v254, v168
v_cmp_le_i32 vcc, 1, v6
s_nop 1
v_cndmask_b32 v169, v241, v169, vcc
v_max_f32 v254, v254, v169
v_cmp_le_i32 vcc, 2, v6
s_nop 1
v_cndmask_b32 v170, v241, v170, vcc
v_max_f32 v254, v254, v170
v_cmp_le_i32 vcc, 3, v6
s_nop 1
v_cndmask_b32 v171, v241, v171, vcc
v_max_f32 v254, v254, v171
v_cmp_le_i32 vcc, 0, v7
s_nop 1
v_cndmask_b32 v172, v241, v172, vcc
v_max_f32 v255, v255, v172
v_cmp_le_i32 vcc, 1, v7
s_nop 1
v_cndmask_b32 v173, v241, v173, vcc
v_max_f32 v255, v255, v173
v_cmp_le_i32 vcc, 2, v7
s_nop 1
v_cndmask_b32 v174, v241, v174, vcc
v_max_f32 v255, v255, v174
v_cmp_le_i32 vcc, 3, v7
s_nop 1
v_cndmask_b32 v175, v241, v175, vcc
v_max_f32 v255, v255, v175
v_cmp_le_i32 vcc, 16, v4
s_nop 1
v_cndmask_b32 v176, v241, v176, vcc
v_max_f32 v252, v252, v176
v_cmp_le_i32 vcc, 17, v4
s_nop 1
v_cndmask_b32 v177, v241, v177, vcc
v_max_f32 v252, v252, v177
v_cmp_le_i32 vcc, 18, v4
s_nop 1
v_cndmask_b32 v178, v241, v178, vcc
v_max_f32 v252, v252, v178
v_cmp_le_i32 vcc, 19, v4
s_nop 1
v_cndmask_b32 v179, v241, v179, vcc
v_max_f32 v252, v252, v179
v_cmp_le_i32 vcc, 16, v5
s_nop 1
v_cndmask_b32 v180, v241, v180, vcc
v_max_f32 v253, v253, v180
v_cmp_le_i32 vcc, 17, v5
s_nop 1
v_cndmask_b32 v181, v241, v181, vcc
v_max_f32 v253, v253, v181
v_cmp_le_i32 vcc, 18, v5
s_nop 1
v_cndmask_b32 v182, v241, v182, vcc
v_max_f32 v253, v253, v182
v_cmp_le_i32 vcc, 19, v5
s_nop 1
v_cndmask_b32 v183, v241, v183, vcc
v_max_f32 v253, v253, v183
v_cmp_le_i32 vcc, 16, v6
s_nop 1
v_cndmask_b32 v184, v241, v184, vcc
v_max_f32 v254, v254, v184
v_cmp_le_i32 vcc, 17, v6
s_nop 1
v_cndmask_b32 v185, v241, v185, vcc
v_max_f32 v254, v254, v185
v_cmp_le_i32 vcc, 18, v6
s_nop 1
v_cndmask_b32 v186, v241, v186, vcc
v_max_f32 v254, v254, v186
v_cmp_le_i32 vcc, 19, v6
s_nop 1
v_cndmask_b32 v187, v241, v187, vcc
v_max_f32 v254, v254, v187
v_cmp_le_i32 vcc, 16, v7
s_nop 1
v_cndmask_b32 v188, v241, v188, vcc
v_max_f32 v255, v255, v188
v_cmp_le_i32 vcc, 17, v7
s_nop 1
v_cndmask_b32 v189, v241, v189, vcc
v_max_f32 v255, v255, v189
v_cmp_le_i32 vcc, 18, v7
s_nop 1
v_cndmask_b32 v190, v241, v190, vcc
v_max_f32 v255, v255, v190
v_cmp_le_i32 vcc, 19, v7
s_nop 1
v_cndmask_b32 v191, v241, v191, vcc
v_max_f32 v255, v255, v191
v_subrev_u32 v4, 32, v4
v_subrev_u32 v5, 32, v5
v_subrev_u32 v6, 32, v6
v_subrev_u32 v7, 32, v7
s_add_u32 s82, s82, 8192
s_sub_u32 s84, s84, 1
s_cmp_lg_u32 s84, 0
s_cbranch_scc1 .LL_50%=
G 28,0
v_lshlrev_b32 v32, 16, v28
v_and_b32 v33, 0xffff0000, v28
G 28,1
v_lshlrev_b32 v34, 16, v28
v_and_b32 v35, 0xffff0000, v28
G 28,2
v_lshlrev_b32 v36, 16, v28
v_and_b32 v37, 0xffff0000, v28
G 28,3
v_lshlrev_b32 v38, 16, v28
v_and_b32 v39, 0xffff0000, v28
G 28,4
v_lshlrev_b32 v40, 16, v28
v_and_b32 v41, 0xffff0000, v28
G 28,5
v_lshlrev_b32 v42, 16, v28
v_and_b32 v43, 0xffff0000, v28
G 28,6
v_lshlrev_b32 v44, 16, v28
v_and_b32 v45, 0xffff0000, v28
G 28,7
v_lshlrev_b32 v46, 16, v28
v_and_b32 v47, 0xffff0000, v28
G 28,8
v_lshlrev_b32 v48, 16, v28
v_and_b32 v49, 0xffff0000, v28
G 28,9
v_lshlrev_b32 v50, 16, v28
v_and_b32 v51, 0xffff0000, v28
G 28,10
v_lshlrev_b32 v52, 16, v28
v_and_b32 v53, 0xffff0000, v28
)"
R"(G 28,11
v_lshlrev_b32 v54, 16, v28
v_and_b32 v55, 0xffff0000, v28
G 28,12
v_lshlrev_b32 v56, 16, v28
v_and_b32 v57, 0xffff0000, v28
G 28,13
v_lshlrev_b32 v58, 16, v28
v_and_b32 v59, 0xffff0000, v28
G 28,14
v_lshlrev_b32 v60, 16, v28
v_and_b32 v61, 0xffff0000, v28
G 28,15
v_lshlrev_b32 v62, 16, v28
v_and_b32 v63, 0xffff0000, v28
G 28,16
v_lshlrev_b32 v64, 16, v28
v_and_b32 v65, 0xffff0000, v28
G 28,17
v_lshlrev_b32 v66, 16, v28
v_and_b32 v67, 0xffff0000, v28
G 28,18
v_lshlrev_b32 v68, 16, v28
v_and_b32 v69, 0xffff0000, v28
G 28,19
v_lshlrev_b32 v70, 16, v28
v_and_b32 v71, 0xffff0000, v28
G 28,20
v_lshlrev_b32 v72, 16, v28
v_and_b32 v73, 0xffff0000, v28
G 28,21
v_lshlrev_b32 v74, 16, v28
v_and_b32 v75, 0xffff0000, v28
G 28,22
v_lshlrev_b32 v76, 16, v28
v_and_b32 v77, 0xffff0000, v28
G 28,23
v_lshlrev_b32 v78, 16, v28
v_and_b32 v79, 0xffff0000, v28
G 28,24
v_lshlrev_b32 v80, 16, v28
v_and_b32 v81, 0xffff0000, v28
G 28,25
v_lshlrev_b32 v82, 16, v28
v_and_b32 v83, 0xffff0000, v28
G 28,26
v_lshlrev_b32 v84, 16, v28
v_and_b32 v85, 0xffff0000, v28
G 28,27
v_lshlrev_b32 v86, 16, v28
v_and_b32 v87, 0xffff0000, v28
G 28,28
v_lshlrev_b32 v88, 16, v28
v_and_b32 v89, 0xffff0000, v28
G 28,29
v_lshlrev_b32 v90, 16, v28
v_and_b32 v91, 0xffff0000, v28
G 28,30
v_lshlrev_b32 v92, 16, v28
v_and_b32 v93, 0xffff0000, v28
G 28,31
v_lshlrev_b32 v94, 16, v28
v_and_b32 v95, 0xffff0000, v28
G 28,32
v_lshlrev_b32 v96, 16, v28
v_and_b32 v97, 0xffff0000, v28
G 28,33
v_lshlrev_b32 v98, 16, v28
v_and_b32 v99, 0xffff0000, v28
G 28,34
v_lshlrev_b32 v100, 16, v28
v_and_b32 v101, 0xffff0000, v28
G 28,35
v_lshlrev_b32 v102, 16, v28
v_and_b32 v103, 0xffff0000, v28
G 28,36
v_lshlrev_b32 v104, 16, v28
v_and_b32 v105, 0xffff0000, v28
G 28,37
v_lshlrev_b32 v106, 16, v28
v_and_b32 v107, 0xffff0000, v28
G 28,38
v_lshlrev_b32 v108, 16, v28
v_and_b32 v109, 0xffff0000, v28
G 28,39
v_lshlrev_b32 v110, 16, v28
v_and_b32 v111, 0xffff0000, v28
G 28,40
v_lshlrev_b32 v112, 16, v28
v_and_b32 v113, 0xffff0000, v28
G 28,41
v_lshlrev_b32 v114, 16, v28
v_and_b32 v115, 0xffff0000, v28
G 28,42
v_lshlrev_b32 v116, 16, v28
v_and_b32 v117, 0xffff0000, v28
G 28,43
v_lshlrev_b32 v118, 16, v28
v_and_b32 v119, 0xffff0000, v28
G 28,44
v_lshlrev_b32 v120, 16, v28
v_and_b32 v121, 0xffff0000, v28
G 28,45
v_lshlrev_b32 v122, 16, v28
v_and_b32 v123, 0xffff0000, v28
G 28,46
v_lshlrev_b32 v124, 16, v28
v_and_b32 v125, 0xffff0000, v28
G 28,47
v_lshlrev_b32 v126, 16, v28
v_and_b32 v127, 0xffff0000, v28
G 28,48
v_lshlrev_b32 v128, 16, v28
v_and_b32 v129, 0xffff0000, v28
G 28,49
v_lshlrev_b32 v130, 16, v28
v_and_b32 v131, 0xffff0000, v28
G 28,50
v_lshlrev_b32 v132, 16, v28
v_and_b32 v133, 0xffff0000, v28
G 28,51
v_lshlrev_b32 v134, 16, v28
v_and_b32 v135, 0xffff0000, v28
G 28,52
v_lshlrev_b32 v136, 16, v28
v_and_b32 v137, 0xffff0000, v28
G 28,53
v_lshlrev_b32 v138, 16, v28
v_and_b32 v139, 0xffff0000, v28
G 28,54
v_lshlrev_b32 v140, 16, v28
v_and_b32 v141, 0xffff0000, v28
G 28,55
v_lshlrev_b32 v142, 16, v28
v_and_b32 v143, 0xffff0000, v28
G 28,56
v_lshlrev_b32 v144, 16, v28
v_and_b32 v145, 0xffff0000, v28
G 28,57
v_lshlrev_b32 v146, 16, v28
v_and_b32 v147, 0xffff0000, v28
G 28,58
v_lshlrev_b32 v148, 16, v28
v_and_b32 v149, 0xffff0000, v28
G 28,59
v_lshlrev_b32 v150, 16, v28
v_and_b32 v151, 0xffff0000, v28
G 28,60
v_lshlrev_b32 v152, 16, v28
v_and_b32 v153, 0xffff0000, v28
G 28,61
v_lshlrev_b32 v154, 16, v28
v_and_b32 v155, 0xffff0000, v28
G 28,62
v_lshlrev_b32 v156, 16, v28
v_and_b32 v157, 0xffff0000, v28
G 28,63
v_lshlrev_b32 v158, 16, v28
v_and_b32 v159, 0xffff0000, v28
s_mov_b32 s22, 1.0
s_mov_b32 s23, 1.0
s_mov_b32 s29, 134217728
s_mov_b32 s71, 3584
s_barrier
Ra 128,242,0
Ra 132,242,1024
Ra 136,242,2048
Ra 140,242,3072
s_cmp_ge_u32 s14, 2
s_cbranch_scc1 .Lbgo%=
s_branch .Lago%=
.p2align 12
.Lpb_27%=:
s_waitcnt vmcnt(6) lgkmcnt(6)
Zvaa 32,0,32
s_add_u32 s28, s28, 512
Ta 20,4,8192
Ta 22,4,9216
Mvaa 32,4,48
s_cmp_eq_u32 s28, s4
s_cselect_b32 s93, 10240, 0
Ta 24,5,0
Ta 26,5,1024
Zvaa 64,4,52
s_sub_u32 s28, s28, s93
s_add_u32 s93, s28, 20480
buffer_load_dwordx4 a[128:131], v26, s[8:11], s28 offen offset:0 sc1
Mvaa 64,0,36
Ta 28,5,8192
Ta 30,5,9216
Zvaa 96,0,40
K 184,184,185
K 185,186,187
Mvaa 96,4,56
K 186,188,189
K 187,190,191
Zvaa 128,4,60
K 192,192,193
Mvaa 128,0,44
W 8
buffer_load_dwordx4 a[132:135], v26, s[8:11], s93 offen offset:0 sc1
K 193,194,195
Zvaa 132,8,44
Ta 0,6,0
Ta 2,6,1024
Mvaa 132,12,60
K 194,196,197
K 195,198,199
Zvaa 100,12,56
K 200,200,201
K 201,202,203
Mvaa 100,8,40
Ta 4,6,8192
Ta 6,6,9216
Zvaa 68,8,36
buffer_load_dwordx4 a[136:139], v27, s[8:11], s28 offen offset:0 sc1
K 202,204,205
Mvaa 68,12,52
K 203,206,207
K 208,208,209
Zvaa 36,12,48
K 209,210,211
K 210,212,213
Mvaa 36,8,32
W 8
Ta 8,7,0
K 211,214,215
Zvaa 40,16,32
Ta 10,7,1024
K 216,216,217
Mvaa 40,20,48
buffer_load_dwordx4 a[140:143], v27, s[8:11], s93 offen offset:0 sc1
K 217,218,219
Zvaa 72,20,52
s_add_u32 s28, s28, 512
s_cmp_eq_u32 s28, s4
s_cselect_b32 s93, 10240, 0
s_sub_u32 s28, s28, s93
Mvaa 72,16,36
s_add_u32 s93, s28, 20480
K 218,220,221
K 219,222,223
Zvaa 104,16,40
Ta 12,7,8192
Ta 14,7,9216
Mvaa 104,20,56
K 224,224,225
K 225,226,227
Zvaa 136,20,60
K 226,228,229
K 227,230,231
Mvaa 136,16,44
W 8
buffer_load_dwordx4 a[144:147], v26, s[8:11], s28 offen offset:0 sc1
K 232,232,233
Zvaa 140,24,44
K 233,234,235
K 234,236,237
Mvaa 140,28,60
Ta 16,8,0
K 235,238,239
Zvaa 108,28,56
Ta 18,8,1024
K 240,240,241
Mvaa 108,24,40
K 241,242,243
K 242,244,245
Zvaa 76,24,36
buffer_load_dwordx4 a[148:151], v26, s[8:11], s93 offen offset:0 sc1
K 243,246,247
Mvaa 76,28,52
Ta 20,8,8192
Ta 22,8,9216
Zvaa 44,28,48
K 248,248,249
K 249,250,251
Mvaa 44,24,32
K 250,252,253
K 251,254,255
W 8
Zvaa 48,0,32
Ta 24,9,0
Ta 26,9,1024
Mvaa 48,4,48
buffer_load_dwordx4 a[152:155], v27, s[8:11], s28 offen offset:0 sc1
K 160,160,161
Zvaa 80,4,52
K 161,162,163
K 162,164,165
Mvaa 80,0,36
Ta 28,9,8192
K 163,166,167
Zvaa 112,0,40
Ta 30,9,9216
K 168,168,169
Mvaa 112,4,56
K 169,170,171
K 170,172,173
Zvaa 144,4,60
buffer_load_dwordx4 a[156:159], v27, s[8:11], s93 offen offset:0 sc1
K 171,174,175
Mvaa 144,0,44
W 8
s_add_u32 s28, s28, 512
s_cmp_eq_u32 s28, s4
s_cselect_b32 s93, 10240, 0
Zvaa 148,8,44
s_sub_u32 s28, s28, s93
s_add_u32 s93, s28, 20480
G 164,184
Mvaa 148,12,60
G 165,185
G 166,186
Zvaa 116,12,56
Ta 0,2,16384
Ta 2,2,17408
Mvaa 116,8,40
buffer_load_dwordx4 v[188:191], v26, s[8:11], s28 offen offset:0 sc1
G 167,187
Zvaa 84,8,36
Ta 4,2,24576
G 172,188
Mvaa 84,12,52
Ta 6,2,25600
G 173,189
Zvaa 52,12,48
G 174,190
G 175,191
Mvaa 52,8,32
W 8
Ta 8,3,16384
K 164,164,165
Zvaa 56,16,32
buffer_load_dwordx4 v[196:199], v26, s[8:11], s93 offen offset:0 sc1
Ta 10,3,17408
Mvaa 56,20,48
K 165,166,167
K 166,172,173
Zvaa 88,20,52
Ta 12,3,24576
K 167,174,175
Mvaa 88,16,36
Ta 14,3,25600
v_accvgpr_write_b32 a184, v164
Zvaa 120,16,40
Mvaa 120,20,56
buffer_load_dwordx4 v[204:207], v27, s[8:11], s28 offen offset:0 sc1
v_accvgpr_write_b32 a185, v165
Zvaa 152,20,60
v_accvgpr_write_b32 a186, v166
Mvaa 152,16,44
W 8
Ta 16,4,16384
v_accvgpr_write_b32 a187, v167
Zvaa 156,24,44
Ta 18,4,17408
v_accvgpr_write_b32 a188, v240
Mvaa 156,28,60
Zvaa 124,28,56
buffer_load_dwordx4 v[212:215], v27, s[8:11], s93 offen offset:0 sc1
v_accvgpr_write_b32 a189, v241
Mvaa 124,24,40
s_add_u32 s28, s28, 512
s_cmp_eq_u32 s28, s4
s_cselect_b32 s93, 10240, 0
s_sub_u32 s28, s28, s93
Zvaa 92,24,36
s_add_u32 s93, s28, 20480
Ta 20,4,24576
Ta 22,4,25600
Mvaa 92,28,52
v_accvgpr_write_b32 a190, v242
v_accvgpr_write_b32 a191, v243
Zvaa 60,28,48
G 164,192
Mvaa 60,24,32
s_waitcnt vmcnt(4) lgkmcnt(8)
buffer_load_dwordx4 v[220:223], v26, s[8:11], s28 offen offset:0 sc1
G 165,193
Mvaa 32,0,128
Ta 24,5,16384
Ta 26,5,17408
Mvaa 32,4,144
G 166,194
G 167,195
Mvaa 64,4,148
Ta 28,5,24576
Ta 30,5,25600
Mvaa 64,0,132
G 172,196
Mvaa 96,0,136
buffer_load_dwordx4 v[228:231], v26, s[8:11], s93 offen offset:0 sc1
G 173,197
Mvaa 96,4,152
G 174,198
Mvaa 128,4,156
G 175,199
Mvaa 128,0,140
W 8
Ta 0,6,16384
)"
R"(Ta 2,6,17408
Mvaa 132,8,140
K 164,164,165
Mvaa 132,12,156
buffer_load_dwordx4 v[236:239], v27, s[8:11], s28 offen offset:0 sc1
K 165,166,167
Mvaa 100,12,152
Ta 4,6,24576
K 166,172,173
Mvaa 100,8,136
Ta 6,6,25600
K 167,174,175
Mvaa 68,8,132
v_accvgpr_write_b32 a192, v164
Mvaa 68,12,148
Mvaa 36,12,144
buffer_load_dwordx4 v[244:247], v27, s[8:11], s93 offen offset:0 sc1
v_accvgpr_write_b32 a193, v165
Mvaa 36,8,128
W 8
s_add_u32 s28, s28, 512
s_cmp_eq_u32 s28, s4
s_cselect_b32 s93, 10240, 0
Mvaa 40,16,128
s_sub_u32 s28, s28, s93
s_add_u32 s93, s28, 20480
v_accvgpr_write_b32 a194, v166
Mvaa 40,20,144
Ta 8,7,16384
Ta 10,7,17408
Mvaa 72,20,148
v_accvgpr_write_b32 a195, v167
v_accvgpr_write_b32 a196, v248
Mvaa 72,16,132
D 22,48,65,2048
Ta 12,7,24576
Mvaa 104,16,136
Ta 14,7,25600
v_accvgpr_write_b32 a197, v249
Mvaa 104,20,152
v_accvgpr_write_b32 a198, v250
v_accvgpr_write_b32 a199, v251
Mvaa 136,20,156
G 164,200
Mvaa 136,16,140
W 8
Ta 16,8,16384
Ta 18,8,17408
Mvaa 140,24,140
buffer_load_dwordx4 a[32:35], v26, s[8:11], s28 offen offset:0 sc1
G 165,201
Mvaa 140,28,156
G 166,202
Mvaa 108,28,152
Ta 20,8,24576
G 167,203
Mvaa 108,24,136
Ta 22,8,25600
G 172,204
Mvaa 76,24,132
Mvaa 76,28,148
buffer_load_dwordx4 a[36:39], v26, s[8:11], s93 offen offset:0 sc1
G 173,205
Mvaa 44,28,144
G 174,206
Mvaa 44,24,128
W 8
Ta 24,9,16384
G 175,207
Mvaa 48,0,128
Ta 26,9,17408
K 164,164,165
Mvaa 48,4,144
Mvaa 80,4,148
buffer_load_dwordx4 a[40:43], v27, s[8:11], s28 offen offset:0 sc1
K 165,166,167
Mvaa 80,0,132
Ta 28,9,24576
Ta 30,9,25600
Mvaa 112,0,136
s_lshl_b32 s80, s56, 9
K 166,172,173
K 167,174,175
Mvaa 112,4,152
v_accvgpr_write_b32 a200, v164
Mvaa 144,4,156
s_lshl_b32 s81, s57, 4
Mvaa 144,0,140
W 8
buffer_load_dwordx4 a[44:47], v27, s[8:11], s93 offen offset:0 sc1
v_accvgpr_write_b32 a201, v165
Mvaa 148,8,140
s_add_u32 s28, s28, 512
s_cmp_eq_u32 s28, s4
s_cselect_b32 s93, 10240, 0
s_sub_u32 s28, s28, s93
Mvaa 148,12,156
s_add_u32 s93, s28, 20480
s_add_u32 s80, s80, s81
Ta 0,10,0
Mvaa 116,12,152
s_and_b32 s81, s14, 1
Ta 2,10,1024
v_accvgpr_write_b32 a202, v166
Mvaa 116,8,136
s_lshl_b32 s81, s81, 3
Ta 4,10,8192
v_accvgpr_write_b32 a203, v167
Mvaa 84,8,132
s_add_u32 s80, s80, s81
buffer_load_dwordx4 a[48:51], v26, s[8:11], s28 offen offset:0 sc1
Ta 6,10,9216
Mvaa 84,12,148
s_add_u32 s71, s80, 4096
v_accvgpr_write_b32 a204, v184
v_accvgpr_write_b32 a205, v185
Mvaa 52,12,144
v_accvgpr_write_b32 a206, v186
v_accvgpr_write_b32 a207, v187
Mvaa 52,8,128
W 8
Ta 8,11,0
Ta 10,11,1024
Mvaa 56,16,128
G 164,208
Mvaa 56,20,144
D 23,48,65,3072
G 165,209
Mvaa 88,20,148
s_add_u32 s65, s5, 16
s_cmp_ge_u32 s65, 20
s_cselect_b32 s90, 20, 0
s_sub_u32 s65, s65, s90
Mvaa 88,16,132
s_lshl_b32 s65, s65, 18
s_add_u32 m0, s63, 98304
G 166,210
Mvaa 120,16,136
Ta 12,11,8192
Ta 14,11,9216
Mvaa 120,20,152
G 167,211
G 172,212
Mvaa 152,20,156
buffer_load_dwordx4 a[52:55], v26, s[8:11], s93 offen offset:0 sc1
G 173,213
Mvaa 152,16,140
W 8
Ta 16,12,0
G 174,214
Mvaa 156,24,140
s_lshr_b32 s80, s15, 3
Ta 18,12,1024
Mvaa 156,28,156
G 175,215
Mvaa 124,28,152
s_lshl_b32 s80, s80, 1
Ta 20,12,8192
K 164,164,165
Mvaa 124,24,136
s_add_u32 s80, s80, s14
buffer_load_dwordx4 a[56:59], v27, s[8:11], s28 offen offset:0 sc1
Ta 22,12,9216
Mvaa 92,24,132
s_sub_u32 s80, s80, 2
K 165,166,167
K 166,172,173
Mvaa 92,28,148
s_lshl_b32 s80, s80, 12
Mvaa 60,28,144
K 167,174,175
v_min_u32 v28, 496, v1
Mvaa 60,24,128
s_waitcnt vmcnt(9) lgkmcnt(8)
v_accvgpr_write_b32 a208, v164
v_lshl_add_u32 v28, v28, 3, s80
Mvav 32,0,188
buffer_load_dwordx4 a[60:63], v27, s[8:11], s93 offen offset:0 sc1
Ta 24,13,0
Mvav 32,4,220
s_add_u32 s28, s28, 512
s_cmp_eq_u32 s28, s4
s_cselect_b32 s93, 10240, 0
s_sub_u32 s28, s28, s93
Mvav 64,4,228
s_add_u32 s93, s28, 20480
Ta 26,13,1024
v_min_u32 v28, .Lend%=-.Lbeg%=-4, v28
Mvav 64,0,196
v_accvgpr_write_b32 a209, v165
v_accvgpr_write_b32 a210, v166
Mvav 96,0,204
Ta 28,13,8192
Ta 30,13,9216
Mvav 96,4,236
D 16,48,65,0
v_accvgpr_write_b32 a211, v167
Mvav 128,4,244
v_accvgpr_write_b32 a212, v192
v_accvgpr_write_b32 a213, v193
Mvav 128,0,212
W 8
Ta 0,14,0
v_accvgpr_write_b32 a214, v194
Mvav 132,8,212
Ta 2,14,1024
v_add_u32 v2, 65536, v10
Mvav 132,12,244
v_accvgpr_write_b32 a215, v195
G 164,216
Mvav 100,12,236
buffer_load_dwordx4 v[184:187], v26, s[8:11], s28 offen offset:0 sc1
Ta 4,14,8192
Mvav 100,8,204
Ta 6,14,9216
v_add_u32 v3, 65536, v11
Mvav 68,8,196
G 165,217
G 166,218
Mvav 68,12,228
G 167,219
Mvav 36,12,220
G 172,220
Mvav 36,8,188
W 8
D 17,48,65,1024
Ta 8,15,0
Mvav 40,16,188
Ta 10,15,1024
v_add_u32 v4, 65536, v12
Mvav 40,20,220
G 173,221
G 174,222
Mvav 72,20,228
Ta 12,15,8192
G 175,223
Mvav 72,16,196
Ta 14,15,9216
v_add_u32 v5, 65536, v13
Mvav 104,16,204
buffer_load_dwordx4 a[128:131], v26, s[8:11], s93 offen offset:0 sc1
K 164,164,165
Mvav 104,20,236
K 165,166,167
Mvav 136,20,244
K 166,172,173
Mvav 136,16,212
W 8
Ta 16,24,0
K 167,174,175
Mvav 140,24,212
Ta 18,24,1024
v_add_u32 v6, 65536, v14
Mvav 140,28,244
buffer_load_dwordx4 a[132:135], v27, s[8:11], s28 offen offset:0 sc1
v_accvgpr_write_b32 a216, v164
Mvav 108,28,236
Ta 20,24,8192
v_accvgpr_write_b32 a217, v165
Mvav 108,24,204
Ta 22,24,9216
v_add_u32 v7, 65536, v15
Mvav 76,24,196
v_accvgpr_write_b32 a218, v166
v_accvgpr_write_b32 a219, v167
Mvav 76,28,228
v_accvgpr_write_b32 a220, v200
Mvav 44,28,220
buffer_load_dwordx4 a[136:139], v27, s[8:11], s93 offen offset:0 sc1
Mvav 44,24,188
W 8
s_add_u32 s28, s28, 512
s_cmp_eq_u32 s28, s4
s_cselect_b32 s93, 10240, 0
Mvav 48,0,188
s_sub_u32 s28, s28, s93
s_add_u32 s93, s28, 20480
v_accvgpr_write_b32 a221, v201
Mvav 48,4,220
Ta 24,25,0
Ta 26,25,1024
Mvav 80,4,228
v_add_u32 v8, 65536, v24
v_accvgpr_write_b32 a222, v202
Mvav 80,0,196
buffer_load_dwordx4 a[140:143], v26, s[8:11], s28 offen offset:0 sc1
v_accvgpr_write_b32 a223, v203
Mvav 112,0,204
Ta 28,25,8192
Ta 30,25,9216
Mvav 112,4,236
v_add_u32 v9, 65536, v25
G 164,224
Mvav 144,4,244
G 165,225
G 166,226
Mvav 144,0,212
W 8
Ta 0,10,16384
G 167,227
Mvav 148,8,212
global_load_dword v29, v28, s[6:7]
Ta 2,10,17408
Mvav 148,12,244
s_sub_u32 s74, s85, 66
s_and_b32 s75, s74, 0xffffffe3
s_cselect_b64 s[72:73], -1, 0
s_and_b32 s74, s74, 28
Mvav 116,12,236
s_lshl_b32 s74, s74, 1
v_mov_b32 v28, s74
G 172,228
Mvav 116,8,204
G 173,229
G 174,230
Mvav 84,8,196
Ta 4,10,24576
Ta 6,10,25600
Mvav 84,12,228
buffer_load_dwordx4 a[144:147], v26, s[8:11], s93 offen offset:0 sc1
G 175,231
Mvav 52,12,220
K 164,164,165
K 165,166,167
Mvav 52,8,188
W 8
Ta 8,11,16384
K 166,172,173
Mvav 56,16,188
Ta 10,11,17408
K 167,174,175
Mvav 56,20,220
v_accvgpr_write_b32 a224, v164
Mvav 88,20,228
buffer_load_dwordx4 a[148:151], v27, s[8:11], s28 offen offset:0 sc1
Ta 12,11,24576
Mvav 88,16,196
Ta 14,11,25600
v_accvgpr_write_b32 a225, v165
Mvav 120,16,204
v_accvgpr_write_b32 a226, v166
Mvav 120,20,236
v_accvgpr_write_b32 a227, v167
Mvav 152,20,244
v_accvgpr_write_b32 a228, v208
Mvav 152,16,212
W 8
D 18,48,65,2048
Ta 16,12,16384
Mvav 156,24,212
Ta 18,12,17408
v_accvgpr_write_b32 a229, v209
Mvav 156,28,244
v_accvgpr_write_b32 a230, v210
Mvav 124,28,236
Ta 20,12,24576
v_accvgpr_write_b32 a231, v211
Mvav 124,24,204
Ta 22,12,25600
G 164,232
Mvav 92,24,196
buffer_load_dwordx4 a[152:155], v27, s[8:11], s93 offen offset:0 sc1
G 165,233
Mvav 92,28,228
s_add_u32 s28, s28, 512
s_cmp_eq_u32 s28, s4
s_cselect_b32 s93, 10240, 0
s_sub_u32 s28, s28, s93
Mvav 60,28,220
s_add_u32 s93, s28, 20480
G 166,234
G 167,235
Mvav 60,24,188
s_waitcnt vmcnt(12) lgkmcnt(8)
G 172,236
G 173,237
Mvaa 32,0,32
Ta 24,13,16384
Ta 26,13,17408
Mvaa 32,4,48
buffer_load_dwordx2 v[28:29], v28, s[8:11], 0 offen offset:3584 sc1
G 174,238
Mvaa 64,4,52
G 175,239
K 164,164,165
Mvaa 64,0,36
Ta 28,13,24576
Ta 30,13,25600
Mvaa 96,0,40
K 165,166,167
K 166,172,173
Mvaa 96,4,56
K 167,174,175
v_accvgpr_write_b32 a232, v164
Mvaa 128,4,60
buffer_load_dwordx4 a[156:159], v26, s[8:11], s28 offen offset:0 sc1
v_accvgpr_write_b32 a233, v165
Mvaa 128,0,44
W 8
v_accvgpr_write_b32 a234, v166
)"
R"(v_accvgpr_write_b32 a235, v167
Mvaa 132,8,44
Ta 0,14,16384
Ta 2,14,17408
Mvaa 132,12,60
G 164,160
G 165,161
Mvaa 100,12,56
G 166,162
G 167,163
Mvaa 100,8,40
buffer_load_dwordx4 v[192:195], v26, s[8:11], s93 offen offset:0 sc1
Ta 4,14,24576
Mvaa 68,8,36
Ta 6,14,25600
G 172,164
Mvaa 68,12,52
G 173,165
G 174,166
Mvaa 36,12,48
G 175,167
K 164,164,165
Mvaa 36,8,32
W 8
Ta 8,15,16384
K 165,166,167
Mvaa 40,16,32
buffer_load_dwordx4 v[200:203], v27, s[8:11], s28 offen offset:0 sc1
Ta 10,15,17408
Mvaa 40,20,48
K 166,172,173
K 167,174,175
Mvaa 72,20,52
s_cmp_lg_u32 s72, 0
s_cbranch_scc1 .LL_51%=
s_waitcnt vmcnt(3)
.LL_51%=:
v_xor_b32 v28, s12, v28
v_xor_b32 v29, s13, v29
v_lshrrev_b32 v248, 11, v28
v_or_b32 v29, v29, v248
v_cmp_eq_u32 vcc, 0, v29
s_or_b64 vcc, vcc, s[72:73]
s_cmp_eq_u64 vcc, -1
s_cbranch_scc0 .Lspin_52%=
.Lok_53%=:
v_accvgpr_write_b32 a236, v164
v_accvgpr_write_b32 a237, v165
Mvaa 72,16,36
Ta 12,15,24576
Ta 14,15,25600
Mvaa 104,16,40
v_accvgpr_write_b32 a238, v166
v_accvgpr_write_b32 a239, v167
Mvaa 104,20,56
D 19,48,65,3072
G 164,240
Mvaa 136,20,60
s_add_u32 m0, s63, 102400
G 165,241
G 166,242
Mvaa 136,16,44
W 8
v_bfe_u32 v248, v28, 3, 8
G 167,243
Mvaa 140,24,44
Ta 16,24,16384
Ta 18,24,17408
Mvaa 140,28,60
v_mul_u32_u24 v248, 10240, v248
G 172,244
Mvaa 108,28,56
buffer_load_dwordx4 v[188:191], v27, s[8:11], s93 offen offset:0 sc1
G 173,245
Mvaa 108,24,40
s_add_u32 s28, s28, 512
s_cmp_eq_u32 s28, s4
s_cselect_b32 s93, 10240, 0
s_sub_u32 s28, s28, s93
Mvaa 76,24,36
s_add_u32 s93, s28, 20480
v_and_b32 v28, 7, v28
G 174,246
Mvaa 76,28,52
v_lshl_add_u32 v248, v28, 4, v248
G 175,247
Mvaa 44,28,48
Ta 20,24,24576
Ta 22,24,25600
Mvaa 44,24,32
W 8
buffer_load_dwordx4 v[196:199], v26, s[8:11], s28 offen offset:0 sc1
K 164,164,165
Mvaa 48,0,32
v_lshl_add_u32 v248, v1, 3, v248
K 165,166,167
Mvaa 48,4,48
v_lshrrev_b32 v29, 8, v1
K 166,172,173
Mvaa 80,4,52
K 167,174,175
v_lshlrev_b32 v29, 28, v29
Mvaa 80,0,36
v_accvgpr_write_b32 a240, v164
v_or_b32 v249, v248, v29
Mvaa 112,0,40
s_and_b32 s75, s72, 134217728
buffer_load_dwordx4 v[204:207], v26, s[8:11], s93 offen offset:0 sc1
v_accvgpr_write_b32 a241, v165
Mvaa 112,4,56
s_add_u32 s76, s75, 131072
Ta 24,25,16384
Ta 26,25,17408
Mvaa 144,4,60
v_accvgpr_write_b32 a242, v166
v_accvgpr_write_b32 a243, v167
Mvaa 144,0,44
W 6
G 164,168
G 165,169
Mvaa 148,8,44
Ta 28,25,24576
Ta 30,25,25600
Mvaa 148,12,60
D 20,48,65,0
G 166,170
Mvaa 116,12,56
G 167,171
G 172,172
Mvaa 116,8,40
G 173,173
G 174,174
Mvaa 84,8,36
Ta 0,2,0
G 175,175
Mvaa 84,12,52
Ta 2,2,1024
K 164,164,165
Mvaa 52,12,48
buffer_load_dwordx4 v[212:215], v27, s[8:11], s28 offen offset:0 sc1
K 165,166,167
Mvaa 52,8,32
W 6
K 166,172,173
K 167,174,175
Mvaa 56,16,32
Ta 4,2,8192
Ta 6,2,9216
Mvaa 56,20,48
v_accvgpr_write_b32 a244, v164
v_accvgpr_write_b32 a245, v165
Mvaa 88,20,52
v_accvgpr_write_b32 a246, v166
v_accvgpr_write_b32 a247, v167
Mvaa 88,16,36
buffer_load_dwordx4 v[220:223], v27, s[8:11], s93 offen offset:0 sc1
G 164,64
Mvaa 120,16,40
s_add_u32 s28, s28, 512
s_cmp_eq_u32 s28, s4
s_cselect_b32 s93, 10240, 0
s_sub_u32 s28, s28, s93
Mvaa 120,20,56
s_add_u32 s93, s28, 20480
Ta 8,3,0
G 165,65
Mvaa 152,20,60
Ta 10,3,1024
G 166,66
Mvaa 152,16,44
W 6
G 167,67
G 172,68
Mvaa 156,24,44
buffer_load_dwordx4 v[228:231], v26, s[8:11], s28 offen offset:0 sc1
G 173,69
Mvaa 156,28,60
Ta 12,3,8192
Ta 14,3,9216
Mvaa 124,28,56
G 174,70
G 175,71
Mvaa 124,24,40
K 164,164,165
K 165,166,167
Mvaa 92,24,36
K 166,172,173
K 167,174,175
Mvaa 92,28,52
buffer_load_dwordx4 v[240:243], v248, s[8:11], s76 offen sc1
Ta 16,4,0
Mvaa 60,28,48
s_add_u32 s76, s76, 8192
Ta 18,4,1024
v_accvgpr_write_b32 a64, v164
Mvaa 60,24,32
s_waitcnt vmcnt(13) lgkmcnt(6)
v_accvgpr_write_b32 a65, v165
v_accvgpr_write_b32 a66, v166
Mvav 32,0,184
Ta 20,4,8192
v_accvgpr_write_b32 a67, v167
Mvaa 32,4,140
Ta 22,4,9216
G 164,176
Mvaa 64,4,144
buffer_load_dwordx4 v[244:247], v249, s[8:11], s76 offen sc1
v_or_b32 v249, v1, v29
Mvaa 64,0,128
s_mul_i32 s76, s74, 160
s_add_u32 s76, s76, s75
s_add_u32 s76, s76, 118784
Mvaa 96,0,132
G 165,177
G 166,178
Mvaa 96,4,148
G 167,179
G 172,180
Mvaa 128,4,152
Ta 24,5,0
G 173,181
Mvaa 128,0,136
W 5
buffer_load_dwordx4 v[236:239], v26, s[8:11], s93 offen offset:0 sc1
Ta 26,5,1024
Mvaa 132,8,136
G 174,182
G 175,183
Mvaa 132,12,152
K 164,164,165
K 165,166,167
Mvaa 100,12,148
Ta 28,5,8192
Ta 30,5,9216
Mvaa 100,8,132
K 166,172,173
K 167,174,175
Mvaa 68,8,128
D 21,48,65,1024
v_accvgpr_write_b32 a68, v164
Mvaa 68,12,144
v_accvgpr_write_b32 a69, v165
v_accvgpr_write_b32 a70, v166
Mvaa 36,12,140
Ta 0,6,0
v_accvgpr_write_b32 a71, v167
Mvav 36,8,184
W 5
Ta 2,6,1024
G 164,72
Mvav 40,16,184
G 165,73
G 166,74
Mvaa 40,20,140
buffer_load_dwordx4 v[208:211], v27, s[8:11], s28 offen offset:0 sc1
Ta 4,6,8192
Mvaa 72,20,144
Ta 6,6,9216
G 167,75
Mvaa 72,16,128
G 172,76
G 173,77
Mvaa 104,16,132
Ta 8,7,0
G 174,78
Mvaa 104,20,148
Ta 10,7,1024
G 175,79
Mvaa 136,20,152
buffer_load_dwordx4 a[32:35], v27, s[8:11], s93 offen offset:0 sc1
K 164,164,165
Mvaa 136,16,136
W 6
s_add_u32 s28, s28, 512
s_cmp_eq_u32 s28, s4
s_cselect_b32 s93, 10240, 0
Mvaa 140,24,136
s_sub_u32 s28, s28, s93
s_add_u32 s93, s28, 20480
Ta 12,7,8192
Mvaa 140,28,152
Ta 14,7,9216
K 165,166,167
Mvaa 108,28,148
K 166,172,173
K 167,174,175
Mvaa 108,24,132
buffer_load_dwordx4 a[36:39], v26, s[8:11], s28 offen offset:0 sc1
v_accvgpr_write_b32 a72, v164
Mvaa 76,24,128
Ta 16,8,0
Ta 18,8,1024
Mvaa 76,28,144
v_accvgpr_write_b32 a73, v165
v_accvgpr_write_b32 a74, v166
Mvaa 44,28,140
Ta 20,8,8192
v_accvgpr_write_b32 a75, v167
Mvav 44,24,184
W 7
Ta 22,8,9216
v_accvgpr_write_b32 a76, v216
Mvav 48,0,184
D 22,48,65,2048
v_accvgpr_write_b32 a77, v217
Mvaa 48,4,140
v_accvgpr_write_b32 a78, v218
v_accvgpr_write_b32 a79, v219
Mvaa 80,4,144
Ta 24,9,0
Ta 26,9,1024
Mvaa 80,0,128
G 164,80
G 165,81
Mvaa 112,0,132
Ta 28,9,8192
Ta 30,9,9216
Mvaa 112,4,148
buffer_load_dwordx4 a[40:43], v26, s[8:11], s93 offen offset:0 sc1
G 166,82
Mvaa 144,4,152
G 167,83
G 172,84
Mvaa 144,0,136
W 8
Ta 0,2,16384
G 173,85
Mvaa 148,8,136
Ta 2,2,17408
G 174,86
Mvaa 148,12,152
G 175,87
Mvaa 116,12,148
buffer_load_dwordx4 a[44:47], v27, s[8:11], s28 offen offset:0 sc1
Ta 4,2,24576
Mvaa 116,8,132
Ta 6,2,25600
K 164,164,165
Mvaa 84,8,128
K 165,166,167
Mvaa 84,12,144
K 166,172,173
Mvaa 52,12,140
K 167,174,175
Mvav 52,8,184
W 8
buffer_load_dwordx4 a[48:51], v27, s[8:11], s93 offen offset:0 sc1
Ta 8,3,16384
Mvav 56,16,184
s_add_u32 s28, s28, 512
s_cmp_eq_u32 s28, s4
s_cselect_b32 s93, 10240, 0
D 23,48,65,3072
Mvaa 56,20,140
s_sub_u32 s28, s28, s93
s_add_u32 s93, s28, 20480
Ta 10,3,17408
Mvaa 88,20,144
v_accvgpr_write_b32 a80, v164
v_accvgpr_write_b32 a81, v165
Mvaa 88,16,128
Ta 12,3,24576
v_accvgpr_write_b32 a82, v166
Mvaa 120,16,132
Ta 14,3,25600
v_accvgpr_write_b32 a83, v167
Mvaa 120,20,148
buffer_load_dwordx4 a[52:55], v26, s[8:11], s28 offen offset:0 sc1
v_accvgpr_write_b32 a84, v224
Mvaa 152,20,152
v_lshrrev_b32 v20, 4, v1
v_accvgpr_write_b32 a85, v225
Mvaa 152,16,136
W 8
v_accvgpr_write_b32 a86, v226
v_add_u32 v20, 0, v20
Mvaa 156,24,136
Ta 16,4,16384
Ta 18,4,17408
Mvaa 156,28,152
v_accvgpr_write_b32 a87, v227
v_mul_u32_u24 v22, 205, v20
Mvaa 124,28,148
buffer_load_dwordx4 a[56:59], v26, s[8:11], s93 offen offset:0 sc1
G 164,88
Mvaa 124,24,132
Ta 20,4,24576
Ta 22,4,25600
Mvaa 92,24,128
v_lshrrev_b32 v22, 12, v22
G 165,89
Mvaa 92,28,144
G 166,90
v_mul_u32_u24 v23, 20, v22
Mvaa 60,28,140
G 167,91
v_sub_u32 v20, v20, v23
Mvav 60,24,184
s_waitcnt vmcnt(15) lgkmcnt(8)
buffer_load_dwordx4 a[60:63], v27, s[8:11], s28 offen offset:0 sc1
)"
R"(G 172,92
Mvaa 32,0,156
Ta 24,5,16384
Ta 26,5,17408
Mvav 32,4,196
v_lshrrev_b32 v23, 1, v22
G 173,93
Mvav 64,4,204
G 174,94
v_and_b32 v23, 2, v23
Mvav 64,0,192
Ta 28,5,24576
Ta 30,5,25600
Mvav 96,0,200
buffer_load_dwordx4 v[216:219], v27, s[8:11], s93 offen offset:0 sc1
G 175,95
Mvav 96,4,212
s_add_u32 s28, s28, 512
s_cmp_eq_u32 s28, s4
s_cselect_b32 s93, 10240, 0
s_sub_u32 s28, s28, s93
Mvav 128,4,220
s_add_u32 s93, s28, 20480
v_xor_b32 v20, v20, v23
K 164,164,165
Mvav 128,0,188
W 8
v_mul_u32_u24 v22, 80, v22
K 165,166,167
Mvav 132,8,188
K 166,172,173
v_add_lshl_u32 v16, v20, v22, 4
Mvav 132,12,220
buffer_load_dwordx4 v[184:187], v26, s[8:11], s28 offen offset:0 sc1
Ta 0,6,16384
Mvav 100,12,212
Ta 2,6,17408
K 167,174,175
Mvav 100,8,200
v_lshrrev_b32 v20, 4, v1
v_accvgpr_write_b32 a88, v164
Mvav 68,8,192
v_add_u32 v20, 64, v20
v_accvgpr_write_b32 a89, v165
Mvav 68,12,204
Ta 4,6,24576
Ta 6,6,25600
Mvav 36,12,196
s_cmp_lg_u32 s72, 0
s_cbranch_scc1 .LL_55%=
s_waitcnt vmcnt(15)
.LL_55%=:
buffer_store_dwordx4 v[240:243], v1, s[8:11], s76 offen sc1
v_accvgpr_write_b32 a90, v166
v_mul_u32_u24 v22, 205, v20
Mvaa 36,8,156
W 8
v_accvgpr_write_b32 a91, v167
v_lshrrev_b32 v22, 12, v22
Mvaa 40,16,156
v_accvgpr_write_b32 a92, v232
v_mul_u32_u24 v23, 20, v22
Mvav 40,20,196
Ta 8,7,16384
Ta 10,7,17408
Mvav 72,20,204
v_accvgpr_write_b32 a93, v233
v_accvgpr_write_b32 a94, v234
Mvav 72,16,192
buffer_store_dwordx4 v[244:247], v249, s[8:11], s76 offen offset:1024 sc1
v_sub_u32 v20, v20, v23
Mvav 104,16,200
s_add_u32 s76, s74, s75
s_add_u32 s76, s76, 3840
v_accvgpr_write_b32 a95, v235
Mvav 104,20,212
Ta 12,7,24576
Ta 14,7,25600
Mvav 136,20,220
v_lshrrev_b32 v23, 1, v22
G 164,96
Mvav 136,16,188
W 8
G 165,97
v_and_b32 v23, 2, v23
Mvav 140,24,188
buffer_load_dwordx4 a[128:131], v26, s[8:11], s93 offen offset:0 sc1
G 166,98
Mvav 140,28,220
G 167,99
v_xor_b32 v20, v20, v23
Mvav 108,28,212
G 172,100
G 173,101
Mvav 108,24,200
Ta 16,8,16384
Ta 18,8,17408
Mvav 76,24,192
v_mul_u32_u24 v22, 80, v22
G 174,102
Mvav 76,28,204
buffer_load_dwordx4 a[132:135], v27, s[8:11], s28 offen offset:0 sc1
G 175,103
Mvav 44,28,196
v_add_lshl_u32 v17, v20, v22, 4
K 164,164,165
Mvaa 44,24,156
W 6
Ta 20,8,24576
Ta 22,8,25600
Mvaa 48,0,156
K 165,166,167
K 166,172,173
Mvav 48,4,196
v_subrev_u32 v17, 1024, v17
K 167,174,175
Mvav 80,4,204
buffer_load_dwordx4 a[136:139], v27, s[8:11], s93 offen offset:0 sc1
v_accvgpr_write_b32 a96, v164
Mvav 80,0,192
s_add_u32 s28, s28, 512
v_lshrrev_b32 v20, 4, v1
v_accvgpr_write_b32 a97, v165
Mvav 112,0,200
s_cmp_eq_u32 s28, s4
s_cselect_b32 s93, 10240, 0
v_accvgpr_write_b32 a98, v166
v_add_u32 v20, 128, v20
Mvav 112,4,212
s_sub_u32 s28, s28, s93
Ta 24,9,16384
v_accvgpr_write_b32 a99, v167
Mvav 144,4,220
s_add_u32 s93, s28, 20480
Ta 26,9,17408
G 164,248
Mvav 144,0,188
W 6
buffer_load_dwordx4 a[140:143], v26, s[8:11], s28 offen offset:0 sc1
v_mul_u32_u24 v22, 205, v20
Mvav 148,8,188
G 165,249
G 166,250
Mvav 148,12,220
Ta 28,9,24576
Ta 30,9,25600
Mvav 116,12,212
v_lshrrev_b32 v22, 12, v22
G 167,251
Mvav 116,8,200
G 172,252
G 173,253
Mvav 84,8,192
s_waitcnt vmcnt(11) lgkmcnt(0)
s_barrier
Mvav 84,12,204
v_mul_u32_u24 v23, 20, v22
G 174,254
Mvav 52,12,196
G 175,255
v_sub_u32 v20, v20, v23
Mvaa 52,8,156
K 164,164,165
K 165,166,167
Mvaa 56,16,156
v_lshrrev_b32 v23, 1, v22
K 166,172,173
Mvav 56,20,196
buffer_load_dwordx4 a[144:147], v26, s[8:11], s93 offen offset:0 sc1
K 167,174,175
Mvav 88,20,204
v_and_b32 v23, 2, v23
v_accvgpr_write_b32 a100, v164
Mvav 88,16,192
v_accvgpr_write_b32 a101, v165
v_xor_b32 v20, v20, v23
Mvav 120,16,200
v_accvgpr_write_b32 a102, v166
v_accvgpr_write_b32 a103, v167
Mvav 120,20,212
Ta 0,10,32768
Ta 2,10,33792
Mvav 152,20,220
Ta 4,10,40960
Ta 6,10,41984
Mvav 152,16,188
G 164,104
v_mul_u32_u24 v22, 80, v22
Mvav 156,24,188
G 165,105
v_add_lshl_u32 v18, v20, v22, 4
Mvav 156,28,220
Ta 8,11,32768
G 166,106
Mvav 124,28,212
Ta 10,11,33792
G 167,107
Mvav 124,24,200
buffer_load_dwordx4 a[148:151], v27, s[8:11], s28 offen offset:0 sc1
v_subrev_u32 v18, 2048, v18
Mvav 92,24,192
G 172,108
G 173,109
Mvav 92,28,204
Ta 12,11,40960
v_lshrrev_b32 v20, 4, v1
Mvav 60,28,196
Ta 14,11,41984
G 174,110
Mvaa 60,24,156
W 4
v_add_u32 v20, 192, v20
G 175,111
Mvav 32,0,228
K 164,164,165
v_mul_u32_u24 v22, 205, v20
Mvaa 32,4,36
K 165,166,167
v_lshrrev_b32 v22, 12, v22
Mvaa 64,4,40
Ta 16,12,32768
K 166,172,173
Mvav 64,0,236
Ta 18,12,33792
K 167,174,175
Mvav 96,0,208
buffer_load_dwordx4 a[152:155], v27, s[8:11], s93 offen offset:0 sc1
v_mul_u32_u24 v23, 20, v22
Mvaa 96,4,44
s_add_u32 s28, s28, 512
v_accvgpr_write_b32 a104, v164
v_accvgpr_write_b32 a105, v165
Mvaa 128,4,48
s_cmp_eq_u32 s28, s4
s_cselect_b32 s93, 10240, 0
Ta 20,12,40960
v_sub_u32 v20, v20, v23
Mvaa 128,0,32
W 3
Ta 22,12,41984
v_accvgpr_write_b32 a106, v166
Mvaa 132,8,32
s_sub_u32 s28, s28, s93
v_lshrrev_b32 v23, 1, v22
v_accvgpr_write_b32 a107, v167
Mvaa 132,12,48
s_add_u32 s93, s28, 20480
K 176,176,177
v_and_b32 v23, 2, v23
Mvaa 100,12,44
K 177,178,179
K 178,180,181
Mvav 100,8,208
Ta 24,13,32768
v_xor_b32 v20, v20, v23
Mvav 68,8,236
Ta 26,13,33792
K 179,182,183
Mvaa 68,12,40
v_accvgpr_write_b32 a108, v176
v_accvgpr_write_b32 a109, v177
v_accvgpr_write_b32 a110, v178
v_accvgpr_write_b32 a111, v179
Mvaa 36,12,36
buffer_load_dwordx4 v[224:227], v26, s[8:11], s28 offen offset:0 sc1
v_mul_u32_u24 v22, 80, v22
Mvav 36,8,228
W 2
Ta 28,13,40960
Ta 30,13,41984
Mvav 40,16,228
v_add_lshl_u32 v19, v20, v22, 4
v_subrev_u32 v19, 3072, v19
Mvaa 40,20,36
G 164,112
v_lshrrev_b32 v20, 4, v1
Mvaa 72,20,40
Ta 0,14,32768
G 165,113
Mvav 72,16,236
s_cmp_lg_u32 s72, 0
s_cbranch_scc1 .LL_56%=
s_waitcnt vmcnt(8)
.LL_56%=:
v_mov_b64 v[28:29], s[12:13]
s_mov_b64 exec, 1
buffer_store_dwordx2 v[28:29], off, s[8:11], s76 sc1
s_mov_b64 exec, -1
Ta 2,14,33792
G 166,114
Mvav 104,16,208
v_add_u32 v20, 256, v20
G 167,115
Mvaa 104,20,44
Ta 4,14,40960
v_mul_u32_u24 v22, 205, v20
Mvaa 136,20,48
Ta 6,14,41984
G 172,116
Mvaa 136,16,32
W 4
v_lshrrev_b32 v22, 12, v22
G 173,117
Mvaa 140,24,32
buffer_load_dwordx4 v[232:235], v26, s[8:11], s93 offen offset:0 sc1
G 174,118
Mvaa 140,28,48
v_mul_u32_u24 v23, 20, v22
G 175,119
Mvaa 108,28,44
Ta 8,15,32768
v_sub_u32 v20, v20, v23
Mvav 108,24,208
Ta 10,15,33792
K 164,164,165
Mvav 76,24,236
v_lshrrev_b32 v23, 1, v22
K 165,166,167
Mvaa 76,28,40
buffer_load_dwordx4 a[248:251], v27, s[8:11], s28 offen offset:0 sc1
K 166,172,173
Mvaa 44,28,36
Ta 12,15,40960
v_and_b32 v23, 2, v23
Mvav 44,24,228
W 3
Ta 14,15,41984
K 167,174,175
Mvav 48,0,228
v_xor_b32 v20, v20, v23
v_accvgpr_write_b32 a112, v164
Mvaa 48,4,36
v_accvgpr_write_b32 a113, v165
v_mul_u32_u24 v22, 80, v22
Mvaa 80,4,40
Ta 16,24,32768
v_accvgpr_write_b32 a114, v166
Mvav 80,0,236
Ta 18,24,33792
v_add_lshl_u32 v20, v20, v22, 4
Mvav 112,0,208
buffer_load_dwordx4 a[252:255], v27, s[8:11], s93 offen offset:0 sc1
v_accvgpr_write_b32 a115, v167
Mvaa 112,4,44
s_add_u32 s28, s28, 512
v_lshrrev_b32 v22, 6, v1
v_accvgpr_write_b32 a116, v168
Mvaa 144,4,48
v_accvgpr_write_b32 a117, v169
v_accvgpr_write_b32 a118, v170
v_accvgpr_write_b32 a119, v171
Mvaa 144,0,32
W 2
Ta 20,24,40960
Ta 22,24,41984
Mvaa 148,8,32
s_cmp_eq_u32 s28, s4
s_cselect_b32 s93, 10240, 0
v_mul_u32_u24 v22, 320, v22
v_bfe_u32 v23, v1, 4, 2
Mvaa 148,12,48
s_sub_u32 s28, s28, s93
v_lshl_add_u32 v22, v23, 3, v22
G 164,120
Mvaa 116,12,44
s_add_u32 s93, s28, 20480
Ta 24,25,32768
Ta 26,25,33792
Mvav 116,8,208
G 165,121
v_bfe_u32 v23, v1, 8, 1
Mvav 84,8,236
buffer_load_dwordx4 v[192:195], v26, s[8:11], s28 offen offset:0 sc1
G 166,122
Mvaa 84,12,40
v_lshlrev_b32 v23, 5, v23
G 167,123
Mvaa 52,12,36
Ta 28,25,40960
Ta 30,25,41984
Mvav 52,8,228
W 4
v_add_u32 v21, v22, v23
G 172,124
Mvav 56,16,228
G 173,125
v_sub_u32 v22, v22, v23
Mvaa 56,20,36
s_mul_i32 s94, s59, 655360
Ta 0,10,49152
G 174,126
Mvaa 88,20,40
s_mul_i32 s95, s58, 320
Ta 2,10,50176
G 175,127
Mvav 88,16,236
s_add_u32 s94, s94, s95
buffer_load_dwordx4 v[200:203], v26, s[8:11], s93 offen offset:0 sc1
K 164,164,165
Mvav 120,16,208
)"
R"(Ta 4,10,57344
K 165,166,167
Mvaa 120,20,44
Ta 6,10,58368
K 166,172,173
Mvaa 152,20,48
s_add_u32 s40, s26, s94
s_addc_u32 s41, s27, 0
K 167,174,175
Mvaa 152,16,32
W 4
v_accvgpr_write_b32 a120, v164
v_accvgpr_write_b32 a121, v165
Mvaa 156,24,32
s_and_b32 s41, s41, 0xffff
Ta 8,11,49152
Ta 10,11,50176
Mvaa 156,28,48
s_mov_b32 s42, 5242880
buffer_load_dwordx4 v[188:191], v27, s[8:11], s28 offen offset:0 sc1
v_accvgpr_write_b32 a122, v166
Mvaa 124,28,44
s_mov_b32 s43, 0x20000
Ta 12,11,57344
v_accvgpr_write_b32 a123, v167
Mvav 124,24,208
s_sub_u32 s42, s42, s94
Ta 14,11,58368
v_accvgpr_write_b32 a124, v160
Mvav 92,24,236
s_mov_b64 s[44:45], s[8:9]
v_accvgpr_write_b32 a125, v161
v_accvgpr_write_b32 a126, v162
Mvaa 92,28,40
s_mov_b32 s47, 0x20000
Ta 16,12,49152
v_accvgpr_write_b32 a127, v163
Mvaa 60,28,36
s_and_b32 s48, s14, 1
Ta 18,12,50176
buffer_load_dwordx4 v[196:199], v27, s[8:11], s93 offen offset:0 sc1
Mvav 60,24,228
s_waitcnt vmcnt(13) lgkmcnt(6)
v_bfe_u32 v28, v1, 4, 2
v_lshlrev_b32 v28, 3, v28
Mvaa 32,0,52
s_lshl_b32 s80, s56, 9
Ta 20,12,57344
Ta 22,12,58368
Mvav 32,4,184
s_mul_i32 s48, s48, 5120
s_add_u32 s80, s80, 4096
s_lshr_b32 s51, s14, 1
Mvaa 64,4,128
s_and_b32 s94, s14, 1
Ta 24,13,49152
Ta 26,13,50176
Mvaa 64,0,56
s_add_u32 s28, s28, 512
s_cmp_eq_u32 s28, s4
s_cselect_b32 s93, 10240, 0
buffer_load_dwordx2 v[28:29], v28, s[8:11], s80 offen sc1
Mvaa 96,0,60
s_sub_u32 s28, s28, s93
s_add_u32 s93, s28, 20480
s_lshl_b32 s95, s57, 1
Mvaa 96,4,132
s_add_u32 s94, s94, s95
Ta 28,13,57344
Ta 30,13,58368
Mvaa 128,4,136
s_lshl_b32 s54, s94, 3
s_add_u32 s54, s54, s59
Mvav 128,0,216
W 8
Ta 0,14,49152
Ta 2,14,50176
Mvav 132,8,216
s_lshl_b32 s54, s54, 6
buffer_load_dwordx4 v[204:207], v26, s[8:11], s28 offen offset:0 sc1
Mvaa 132,12,136
s_xor_b32 s55, s54, 15872
Mvaa 100,12,132
Ta 4,14,57344
Ta 6,14,58368
Mvaa 100,8,60
s_add_u32 s54, s54, 20480
Mvaa 68,8,56
s_and_b32 s81, s14, 1
Mvaa 68,12,128
s_add_u32 s55, s55, 20480
buffer_load_dwordx4 v[212:215], v26, s[8:11], s93 offen offset:0 sc1
Mvav 36,12,184
Mvaa 36,8,52
W 8
s_and_b32 s95, s14, 2
s_mul_i32 s91, s81, 17164288
Mvaa 40,16,52
s_lshl_b32 s94, s94, 2
Ta 8,15,49152
Ta 10,15,50176
Mvav 40,20,184
s_mul_i32 s82, s56, 536576
Mvaa 72,20,128
s_add_u32 s94, s94, s95
buffer_load_dwordx4 v[220:223], v27, s[8:11], s28 offen offset:0 sc1
Ta 12,15,57344
Mvaa 72,16,56
Ta 14,15,58368
Mvaa 104,16,60
s_lshl_b32 s95, s95, 4
s_add_u32 s91, s91, s82
Mvaa 104,20,132
Mvaa 136,20,136
s_add_u32 s55, s55, s95
s_add_u32 s91, s91, 4194304
Mvav 136,16,216
W 8
s_lshl_b32 s49, s94, 17
Ta 16,24,49152
Mvav 140,24,216
Ta 18,24,50176
buffer_load_dwordx4 v[176:179], v27, s[8:11], s93 offen offset:0 sc1
Mvaa 140,28,136
s_lshl_b32 s95, s59, 14
s_lshr_b32 s82, s14, 1
Mvaa 108,28,132
Ta 20,24,57344
Ta 22,24,58368
Mvaa 108,24,60
s_add_u32 s49, s49, s95
s_lshl_b32 s83, s82, 14
Mvaa 76,24,56
s_xor_b32 s50, s49, 16252928
Mvaa 76,28,128
Mvav 44,28,184
s_add_u32 s91, s91, s83
s_add_u32 s49, s49, 39845888
Mvaa 44,24,52
W 8
Ta 24,25,49152
Ta 26,25,50176
Mvaa 48,0,52
s_add_u32 s50, s50, 39845888
s_lshl_b32 s81, s81, 14
Mvav 48,4,184
Mvaa 80,4,128
s_lshr_b32 s94, s15, 3
Ta 28,25,57344
Ta 30,25,58368
Mvaa 80,0,56
s_lshl_b32 s82, s82, 16
s_lshl_b32 s94, s94, 3
Mvaa 112,0,60
Mvaa 112,4,132
s_add_u32 s94, s94, s14
s_or_b32 s92, s81, s82
Mvaa 144,4,136
Mvav 144,0,216
W 8
s_lshl_b32 s94, s94, 4
Ta 0,2,32768
Mvav 148,8,216
s_xor_b32 s92, s92, 0x10000
s_add_u32 s94, s94, s59
Ta 2,2,33792
Mvaa 148,12,136
Mvaa 116,12,132
s_mul_i32 s52, s94, 5120
Ta 4,2,40960
Ta 6,2,41984
Mvaa 116,8,60
Mvaa 84,8,56
s_lshl_b32 s95, s59, 12
Mvaa 84,12,128
Mvav 52,12,184
s_sub_u32 s52, s52, s95
Mvaa 52,8,52
W 8
s_add_u32 s52, s52, 56623104
Ta 8,3,32768
Mvaa 56,16,52
Ta 10,3,33792
Mvav 56,20,184
s_lshl_b32 s53, s94, 3
Mvaa 88,20,128
Ta 12,3,40960
Ta 14,3,41984
Mvaa 88,16,56
s_add_u32 s53, s53, 65536
Mvaa 120,16,60
s_waitcnt vmcnt(4)
v_cmp_eq_u64 vcc, s[12:13], v[28:29]
s_cmp_eq_u64 vcc, -1
s_cbranch_scc0 .Lspin_57%=
.Lok_58%=:
s_sub_u32 s82, s91, 16384
Mvaa 120,20,132
s_xor_b32 s83, s92, 0x10000
s_and_b32 s5, s14, 1
s_lshl_b32 s5, s5, 13
Mvaa 152,20,136
s_add_u32 s5, s82, s5
s_add_u32 m0, s83, 0
s_add_u32 s81, s5, 0
Mvav 152,16,216
W 8
v_bfe_u32 v244, v1, 4, 1
v_mul_u32_u24 v244, 0xaaaa, v244
Mvav 156,24,216
Dc 1,8,81,0
v_add_u32 v244, 0x4444, v244
Mvaa 156,28,136
Ta 16,4,32768
Ta 18,4,33792
Mvaa 124,28,132
v_lshl_or_b32 v240, v244, 16, v244
v_mov_b32 v241, 0xff800000
Mvaa 124,24,60
Ta 20,4,40960
Ta 22,4,41984
Mvaa 92,24,56
v_add_u32 v242, 0x10000, v1
Mvaa 92,28,128
Dc 1,8,81,1024
v_bfe_u32 v244, v1, 4, 4
Mvav 60,28,184
v_lshrrev_b32 v245, 8, v1
Mvaa 60,24,52
W 8
Ta 24,5,32768
v_lshlrev_b32 v245, 2, v245
Mvaa 32,0,140
Ta 26,5,33792
Mvav 32,4,224
v_sub_u32 v244, v244, v245
Mvav 64,4,232
Dc 1,8,81,2048
v_cmp_le_i32_e64 s[72:73], 0, v244
Mvaa 64,0,144
Ta 28,5,40960
Ta 30,5,41984
Mvaa 96,0,148
v_cmp_le_i32_e64 s[74:75], 1, v244
Mvaa 96,4,248
v_cmp_le_i32_e64 s[76:77], 2, v244
Mvaa 128,4,252
v_cmp_le_i32_e64 s[78:79], 3, v244
Mvaa 128,0,152
W 8
Dc 1,8,81,3072
Ta 0,6,32768
Mvaa 132,8,152
s_add_u32 m0, s83, 4096
s_add_u32 s81, s5, 4096
Ta 2,6,33792
Mvaa 132,12,252
v_add_u32 v243, 4096, v1
v_add_u32 v244, 8192, v1
Mvaa 100,12,248
Ta 4,6,40960
v_add_u32 v245, 12288, v1
Mvaa 100,8,148
Ta 6,6,41984
Mvaa 68,8,144
s_mov_b32 s18, 0x3f803f80
Dc 1,8,81,0
Mvav 68,12,232
s_mov_b32 s19, 0x3f003f00
Mvav 36,12,224
Mvaa 36,8,140
W 8
Ta 8,7,32768
v_accvgpr_write_b32 a176, s18
Mvaa 40,16,140
Ta 10,7,33792
v_accvgpr_write_b32 a180, s19
Mvav 40,20,224
Dc 1,8,81,1024
v_mov_b32 v252, v241
Mvav 72,20,232
Ta 12,7,40960
Ta 14,7,41984
Mvaa 72,16,144
v_accvgpr_write_b32 a177, s18
Mvaa 104,16,148
v_accvgpr_write_b32 a181, s19
Mvaa 104,20,248
v_mov_b32 v253, v241
Mvaa 136,20,252
Dc 1,8,81,2048
Mvaa 136,16,152
W 8
Ta 16,8,32768
v_accvgpr_write_b32 a178, s18
Mvaa 140,24,152
Ta 18,8,33792
v_accvgpr_write_b32 a182, s19
Mvaa 140,28,252
Mvaa 108,28,248
Ta 20,8,40960
v_mov_b32 v254, v241
Mvaa 108,24,148
Dc 1,8,81,3072
Ta 22,8,41984
Mvaa 76,24,144
s_add_u32 m0, s83, 8192
s_add_u32 s81, s82, 8192
v_accvgpr_write_b32 a179, s18
Mvav 76,28,232
v_accvgpr_write_b32 a183, s19
Mvav 44,28,224
v_mov_b32 v255, v241
Mvaa 44,24,140
W 8
Ta 24,9,32768
v_accvgpr_write_b32 a160, 0
Mvaa 48,0,140
Dc 1,8,81,0
Ta 26,9,33792
Mvav 48,4,224
v_accvgpr_write_b32 a161, 0
Mvav 80,4,232
Ta 28,9,40960
v_accvgpr_write_b32 a162, 0
Mvaa 80,0,144
Ta 30,9,41984
v_accvgpr_write_b32 a163, 0
Mvaa 112,0,148
Mvaa 112,4,248
Dc 1,8,81,1024
v_accvgpr_write_b32 a164, 0
Mvaa 144,4,252
v_accvgpr_write_b32 a165, 0
Mvaa 144,0,152
W 8
Ta 0,2,49152
v_accvgpr_write_b32 a166, 0
Mvaa 148,8,152
Ta 2,2,50176
Mvaa 148,12,252
v_accvgpr_write_b32 a167, 0
Mvaa 116,12,248
Dc 1,8,81,2048
v_accvgpr_write_b32 a168, 0
Mvaa 116,8,148
Ta 4,2,57344
Ta 6,2,58368
Mvaa 84,8,144
v_accvgpr_write_b32 a169, 0
Mvav 84,12,232
v_accvgpr_write_b32 a170, 0
Mvav 52,12,224
v_accvgpr_write_b32 a171, 0
Mvaa 52,8,140
W 8
Dc 1,8,81,3072
Ta 8,3,49152
Mvaa 56,16,140
s_add_u32 m0, s83, 12288
s_add_u32 s81, s82, 12288
Ta 10,3,50176
Mvav 56,20,224
v_accvgpr_write_b32 a172, 0
v_accvgpr_write_b32 a173, 0
Mvav 88,20,232
Ta 12,3,57344
v_accvgpr_write_b32 a174, 0
Mvaa 88,16,144
Ta 14,3,58368
Mvaa 120,16,148
Dc 1,8,81,0
v_accvgpr_write_b32 a175, 0
Mvaa 120,20,248
s_mov_b32 s90, 0x5d800000
Mvaa 152,20,252
s_mov_b64 s[88:89], 0
Mvaa 152,16,152
W 8
Ta 16,4,49152
Ta 18,4,50176
Mvaa 156,24,152
s_mov_b32 s64, 0
Mvaa 156,28,252
s_and_b32 s18, s14, 1
Dc 1,8,81,1024
Mvaa 124,28,248
s_lshl_b32 s62, s18, 14
Ta 20,4,57344
Ta 22,4,58368
Mvaa 124,24,148
Mvaa 92,24,144
s_lshl_b32 s20, s57, 15
Mvav 92,28,232
s_sub_u32 s20, 507904, s20
Mvav 60,28,224
Dc 1,8,81,2048
Mvaa 60,24,140
s_waitcnt vmcnt(15) lgkmcnt(8)
s_add_u32 s20, s20, s62
)"
R"(Ta 24,5,49152
Mvav 32,0,192
s_lshl_b32 s65, s57, 1
Ta 26,5,50176
Mvav 32,4,204
s_sub_u32 s65, s65, 1
Mvav 64,4,212
Ta 28,5,57344
Ta 30,5,58368
Mvav 64,0,200
s_max_i32 s65, s65, 0
Dc 1,8,81,3072
v_bfe_u32 v28, v1, 4, 4
Mvav 96,0,188
s_sub_u32 s66, s60, 1
v_lshlrev_b32 v28, 3, v28
Mvav 96,4,220
s_lshl_b32 s80, s56, 9
s_add_u32 s80, s80, 4096
s_cmp_lt_u32 s14, 2
s_cselect_b32 s20, s20, 0
s_cselect_b32 s66, s66, 15
s_cselect_b32 s21, 32768, 0
Mvav 128,4,176
Mvav 128,0,196
W 8
s_add_u32 s61, s91, s20
Ta 0,6,49152
Mvav 132,8,196
s_add_u32 s62, s62, s21
buffer_load_dwordx2 v[28:29], v28, s[8:11], s80 offen sc1
Ta 2,6,50176
Mvav 132,12,176
Mvav 100,12,220
s_sub_u32 s63, 16384, s21
Ta 4,6,57344
Ta 6,6,58368
Mvav 100,8,188
s_lshl_b32 s67, s60, 7
Mvav 68,8,200
s_add_u32 s67, s67, s56
Mvav 68,12,212
Mvav 36,12,204
s_lshl_b32 s67, s67, 12
Mvav 36,8,192
W 8
s_add_u32 s67, s67, 39845888
Ta 8,7,49152
Mvav 40,16,192
s_lshl_b32 s69, s60, 3
Ta 10,7,50176
Mvav 40,20,204
Mvav 72,20,212
s_add_u32 s69, s69, s59
Ta 12,7,57344
Ta 14,7,58368
Mvav 72,16,200
s_lshl_b32 s69, s69, 6
Mvav 104,16,188
Mvav 104,20,220
s_lshl_b32 s21, s58, 3
Mvav 136,20,176
s_add_u32 s69, s69, s21
Mvav 136,16,196
W 8
s_add_u32 s69, s69, 20480
Ta 16,8,49152
Mvav 140,24,196
Ta 18,8,50176
Mvav 140,28,176
s_mov_b32 s70, 0
Mvav 108,28,220
s_mov_b32 s37, 0
Ta 20,8,57344
Ta 22,8,58368
Mvav 108,24,188
s_sub_u32 s18, s57, 4
Mvav 76,24,200
Mvav 76,28,212
s_lshl_b32 s18, s18, 2
Mvav 44,28,204
s_add_u32 s18, s18, s58
Mvav 44,24,192
W 8
Ta 24,9,49152
Ta 26,9,50176
Mvav 48,0,192
s_mul_i32 s85, s59, 7
Mvav 48,4,204
s_add_u32 s85, s85, s18
Mvav 80,4,212
s_cmp_lt_u32 s18, 7
s_cselect_b32 s85, s85, 99
Ta 28,9,57344
Ta 30,9,58368
Mvav 80,0,200
Mvav 112,0,188
s_cmp_lt_u32 s85, 54
s_cselect_b32 s65, 2, s65
Mvav 112,4,220
s_mov_b32 s68, 0
Mvav 144,4,176
s_mov_b64 s[24:25], -1
Mvav 144,0,196
W 8
Mvav 148,8,196
s_mov_b32 s22, 0x3e0293ee
Mvav 148,12,176
s_mov_b32 s23, 0x3e0293ee
Mvav 116,12,220
Mvav 116,8,188
Mvav 84,8,200
Mvav 84,12,212
Mvav 52,12,204
Mvav 52,8,192
W 4
Mvav 56,16,192
Mvav 56,20,204
s_waitcnt vmcnt(0)
v_cmp_eq_u64 vcc, s[12:13], v[28:29]
s_cmp_eq_u64 vcc, -1
s_cbranch_scc0 .Lspin_60%=
.Lok_61%=:
Mvav 88,20,212
Mvav 88,16,200
Mvav 120,16,188
W 0
s_barrier
Mvav 120,20,220
Ra 128,242,0
Mvav 152,20,176
Ra 132,242,1024
Mvav 152,16,196
Mvav 156,24,196
Ra 136,242,2048
Mvav 156,28,176
Mvav 124,28,220
Ra 140,242,3072
Mvav 124,24,188
Mvav 92,24,200
Mvav 92,28,212
Mvav 60,28,204
Mvav 60,24,192
.Lbgo%=:
v_pk_mul_f32 v[32:33], v[32:33], s[22:23]
v_pk_mul_f32 v[34:35], v[34:35], s[22:23]
v_pk_mul_f32 v[36:37], v[36:37], s[22:23]
v_pk_mul_f32 v[38:39], v[38:39], s[22:23]
v_pk_mul_f32 v[64:65], v[64:65], s[22:23]
v_pk_mul_f32 v[66:67], v[66:67], s[22:23]
v_pk_mul_f32 v[68:69], v[68:69], s[22:23]
v_pk_mul_f32 v[70:71], v[70:71], s[22:23]
v_pk_mul_f32 v[96:97], v[96:97], s[22:23]
v_pk_mul_f32 v[98:99], v[98:99], s[22:23]
v_pk_mul_f32 v[100:101], v[100:101], s[22:23]
v_pk_mul_f32 v[102:103], v[102:103], s[22:23]
v_pk_mul_f32 v[128:129], v[128:129], s[22:23]
v_pk_mul_f32 v[130:131], v[130:131], s[22:23]
v_pk_mul_f32 v[132:133], v[132:133], s[22:23]
v_pk_mul_f32 v[134:135], v[134:135], s[22:23]
v_pk_mul_f32 v[40:41], v[40:41], s[22:23]
v_pk_mul_f32 v[42:43], v[42:43], s[22:23]
v_pk_mul_f32 v[44:45], v[44:45], s[22:23]
v_pk_mul_f32 v[46:47], v[46:47], s[22:23]
v_pk_mul_f32 v[72:73], v[72:73], s[22:23]
v_pk_mul_f32 v[74:75], v[74:75], s[22:23]
v_pk_mul_f32 v[76:77], v[76:77], s[22:23]
v_pk_mul_f32 v[78:79], v[78:79], s[22:23]
v_pk_mul_f32 v[104:105], v[104:105], s[22:23]
v_pk_mul_f32 v[106:107], v[106:107], s[22:23]
v_pk_mul_f32 v[108:109], v[108:109], s[22:23]
v_pk_mul_f32 v[110:111], v[110:111], s[22:23]
v_pk_mul_f32 v[136:137], v[136:137], s[22:23]
v_pk_mul_f32 v[138:139], v[138:139], s[22:23]
v_pk_mul_f32 v[140:141], v[140:141], s[22:23]
v_pk_mul_f32 v[142:143], v[142:143], s[22:23]
v_pk_mul_f32 v[48:49], v[48:49], s[22:23]
v_pk_mul_f32 v[50:51], v[50:51], s[22:23]
v_pk_mul_f32 v[52:53], v[52:53], s[22:23]
v_pk_mul_f32 v[54:55], v[54:55], s[22:23]
v_pk_mul_f32 v[80:81], v[80:81], s[22:23]
v_pk_mul_f32 v[82:83], v[82:83], s[22:23]
v_pk_mul_f32 v[84:85], v[84:85], s[22:23]
v_pk_mul_f32 v[86:87], v[86:87], s[22:23]
v_pk_mul_f32 v[112:113], v[112:113], s[22:23]
v_pk_mul_f32 v[114:115], v[114:115], s[22:23]
v_pk_mul_f32 v[116:117], v[116:117], s[22:23]
v_pk_mul_f32 v[118:119], v[118:119], s[22:23]
v_pk_mul_f32 v[144:145], v[144:145], s[22:23]
v_pk_mul_f32 v[146:147], v[146:147], s[22:23]
v_pk_mul_f32 v[148:149], v[148:149], s[22:23]
v_pk_mul_f32 v[150:151], v[150:151], s[22:23]
v_pk_mul_f32 v[56:57], v[56:57], s[22:23]
v_pk_mul_f32 v[58:59], v[58:59], s[22:23]
v_pk_mul_f32 v[60:61], v[60:61], s[22:23]
v_pk_mul_f32 v[62:63], v[62:63], s[22:23]
v_pk_mul_f32 v[88:89], v[88:89], s[22:23]
v_pk_mul_f32 v[90:91], v[90:91], s[22:23]
v_pk_mul_f32 v[92:93], v[92:93], s[22:23]
v_pk_mul_f32 v[94:95], v[94:95], s[22:23]
v_pk_mul_f32 v[120:121], v[120:121], s[22:23]
v_pk_mul_f32 v[122:123], v[122:123], s[22:23]
v_pk_mul_f32 v[124:125], v[124:125], s[22:23]
v_pk_mul_f32 v[126:127], v[126:127], s[22:23]
v_pk_mul_f32 v[152:153], v[152:153], s[22:23]
v_pk_mul_f32 v[154:155], v[154:155], s[22:23]
v_pk_mul_f32 v[156:157], v[156:157], s[22:23]
v_pk_mul_f32 v[158:159], v[158:159], s[22:23]
K 32,32,33
K 33,34,35
K 34,36,37
K 35,38,39
K 40,40,41
K 41,42,43
K 42,44,45
K 43,46,47
W 0
Zvav 196,128,32
s_add_u32 m0, s62, 0
Ra 144,242,4096
Ra 148,242,5120
Ra 152,242,6144
Ra 156,242,7168
Mvav 196,132,40
K 64,64,65
K 65,66,67
K 66,68,69
K 67,70,71
K 72,72,73
K 73,74,75
K 74,76,77
K 75,78,79
s_nop 1
Zvav 204,132,72
Dc 1,8,61,0
K 96,96,97
K 97,98,99
K 98,100,101
Mvav 204,128,64
K 99,102,103
K 104,104,105
K 105,106,107
K 106,108,109
K 107,110,111
Zvav 212,128,96
Dc 1,8,61,1024
K 128,128,129
K 129,130,131
K 130,132,133
Mvav 212,132,104
K 131,134,135
K 136,136,137
K 137,138,139
K 138,140,141
K 139,142,143
s_nop 1
Zvav 220,132,136
Dc 1,8,61,2048
K 48,48,49
K 49,50,51
K 50,52,53
Mvav 220,128,128
K 51,54,55
K 56,56,57
K 57,58,59
K 58,60,61
K 59,62,63
Mvav 196,136,48
Ra 128,242,8192
Ra 132,242,9216
K 80,80,81
K 81,82,83
Mvav 196,140,56
K 82,84,85
K 83,86,87
K 88,88,89
K 89,90,91
K 90,92,93
K 91,94,95
s_nop 1
Mvav 204,140,88
Dc 1,8,61,3072
s_add_u32 m0, s62, 4096
K 112,112,113
K 113,114,115
K 114,116,117
Mvav 204,136,80
K 115,118,119
K 120,120,121
K 121,122,123
K 122,124,125
K 123,126,127
Mvav 212,136,112
Dc 243,8,61,0
K 144,144,145
K 145,146,147
K 146,148,149
Mvav 212,140,120
K 147,150,151
K 152,152,153
K 153,154,155
K 154,156,157
K 155,158,159
s_nop 1
Mvav 220,140,152
s_add_u32 s80, s62, 131072
Dc 243,8,61,1024
v_add_u32 v12, s80, v1
Mvav 220,136,144
W 2
ds_write_b128 v12, v[32:35] offset:0
Ra 136,242,10240
Zvav 36,144,32
Ra 0,12,0
Ra 140,242,11264
Dc 243,8,61,2048
Mvav 36,148,40
v_max3_f32 v4, v196, v197, v198
v_max3_f32 v4, v4, v199, v252
v_max3_f32 v5, v204, v205, v206
v_max3_f32 v5, v5, v207, v253
Zvav 68,148,72
v_max3_f32 v6, v212, v213, v214
v_max3_f32 v6, v6, v215, v254
v_max3_f32 v7, v220, v221, v222
v_max3_f32 v7, v7, v223, v255
Mvav 68,144,64
v_mov_b32 v8, v4
v_mov_b32 v9, v5
v_mov_b32 v10, v6
v_mov_b32 v11, v7
Zvav 100,144,96
ds_write_b128 v12, v[40:43] offset:1024
v_permlane32_swap_b32 v4, v8
v_permlane32_swap_b32 v5, v9
v_permlane32_swap_b32 v6, v10
Mvav 100,148,104
v_permlane32_swap_b32 v7, v11
v_max_f32 v4, v4, v8
v_max_f32 v5, v5, v9
v_max_f32 v6, v6, v10
Zvav 132,148,136
v_max_f32 v7, v7, v11
v_mov_b32 v8, v4
v_mov_b32 v9, v5
v_mov_b32 v10, v6
Mvav 132,144,128
Ra 4,12,1024
v_mov_b32 v11, v7
v_permlane16_swap_b32 v4, v8
v_permlane16_swap_b32 v5, v9
Mvav 36,152,48
v_permlane16_swap_b32 v6, v10
v_permlane16_swap_b32 v7, v11
v_max_f32 v4, v4, v8
v_max_f32 v5, v5, v9
Mvav 36,156,56
v_max_f32 v6, v6, v10
v_max_f32 v7, v7, v11
v_sub_f32 v4, 0, v4
v_sub_f32 v5, 0, v5
Mvav 68,156,88
v_sub_f32 v6, 0, v6
v_sub_f32 v7, 0, v7
v_mov_b32 v224, v4
v_mov_b32 v225, v4
Mvav 68,152,80
v_mov_b32 v226, v4
v_mov_b32 v227, v4
v_mov_b32 v228, v5
v_mov_b32 v229, v5
Mvav 100,152,112
v_mov_b32 v230, v5
v_mov_b32 v231, v5
v_mov_b32 v232, v6
v_mov_b32 v233, v6
Mvav 100,156,120
v_mov_b32 v234, v6
v_mov_b32 v235, v6
v_mov_b32 v236, v7
v_mov_b32 v237, v7
Mvav 132,156,152
Dc 243,8,61,3072
s_add_u32 m0, s62, 8192
ds_write_b128 v12, v[48:51] offset:2048
v_mov_b32 v238, v7
v_mov_b32 v239, v7
Mvav 132,152,144
W 3
Ra 8,12,2048
ds_write_b128 v12, v[56:59] offset:3072
Ra 144,242,12288
Ra 12,12,3072
Cvav 160,128,32,224
s_and_b32 s80, s14, 1
Dc 244,8,61,0
Ra 148,242,13312
ds_write_b128 v12, v[64:67] offset:4096
Ra 16,12,4096
Mvav 160,132,40
)"
R"(s_lshl_b32 s80, s80, 14
ds_write_b128 v12, v[72:75] offset:5120
Ra 20,12,5120
ds_write_b128 v12, v[80:83] offset:6144
A 196,196,224
Cvav 164,132,72,228
s_add_u32 s80, s80, 98304
Dc 244,8,61,1024
Ra 24,12,6144
A 197,197,224
A 198,198,224
Mvav 164,128,64
Ra 152,242,14336
A 199,199,224
X 196
Cvav 168,128,96,232
Dc 244,8,61,2048
Ra 156,242,15360
X 197
Mvav 168,132,104
ds_write_b128 v12, v[88:91] offset:7168
Ra 28,12,7168
X 198
Cvav 172,132,136,236
Dc 244,8,61,3072
s_add_u32 m0, s62, 12288
K 192,196,197
X 199
Mvav 172,128,128
ds_write_b128 v12, v[96:99] offset:8192
A 204,204,228
v_add_u32 v8, s80, v1
K 193,198,199
Mvav 160,136,48
Dc 245,8,61,0
Ra 32,12,8192
A 205,205,228
A 206,206,228
Mvav 160,140,56
ds_write_b128 v8, v[4:7]
A 207,207,228
X 204
Mvav 164,140,88
Dc 245,8,61,1024
ds_write_b128 v12, v[104:107] offset:9216
X 205
Mvav 164,136,80
Ra 36,12,9216
X 206
K 200,204,205
Mvav 168,136,112
Dc 245,8,61,2048
Ra 128,242,24576
X 207
Mvav 168,140,120
ds_write_b128 v12, v[112:115] offset:10240
Ra 132,242,25600
A 212,212,232
K 201,206,207
Mvav 172,140,152
Dc 245,8,61,3072
s_add_u32 s61, s61, s63
Ra 40,12,10240
A 213,213,232
A 214,214,232
Mvav 172,136,144
W 11
ds_write_b128 v12, v[120:123] offset:11264
A 215,215,232
X 212
Cvav 176,144,32,224
X 213
X 214
Mvav 176,148,40
Ra 44,12,11264
K 208,212,213
X 215
Cvav 180,148,72,228
ds_write_b128 v12, v[128:131] offset:12288
A 220,220,236
K 209,214,215
A 221,221,236
Mvav 180,144,64
Ra 48,12,12288
Ra 136,242,26624
A 222,222,236
A 223,223,236
Cvav 184,144,96,232
ds_write_b128 v12, v[136:139] offset:13312
Ra 140,242,27648
X 220
Mvav 184,148,104
X 221
X 222
Cvav 188,148,136,236
Ra 52,12,13312
K 216,220,221
X 223
Mvav 188,144,128
ds_write_b128 v12, v[144:147] offset:14336
Ra 56,12,14336
A 36,36,224
K 217,222,223
Mvav 176,152,48
ds_write_b128 v12, v[152:155] offset:15360
A 37,37,224
A 38,38,224
A 39,39,224
Mvav 176,156,56
X 36
X 37
Mvav 180,156,88
Ra 60,12,15360
X 38
K 194,36,37
Mvav 180,152,80
Ra 144,242,28672
X 39
A 68,68,228
Mvav 184,152,112
Ra 148,242,29696
K 195,38,39
A 69,69,228
A 70,70,228
Mvav 184,156,120
W 3
s_barrier
A 71,71,228
X 68
Mvav 188,156,152
X 69
X 70
K 202,68,69
X 71
A 100,100,232
K 203,70,71
A 101,101,232
A 102,102,232
A 103,103,232
X 100
X 101
X 102
K 210,100,101
X 103
A 132,132,236
K 211,102,103
A 133,133,236
A 134,134,236
A 135,135,236
X 132
X 133
X 134
K 218,132,133
X 135
s_nop 0
K 219,134,135
Mvav 188,152,144
Ra 152,242,30720
Zvav 32,128,192
Ra 156,242,31744
Zvav 64,128,200
v_xor_b32 v242, 0x10000, v242
Zvav 96,128,208
s_xor_b32 s62, s62, 0x10000
Zvav 128,128,216
v_add_u32 v23, 61440, v21
Zvav 132,132,216
v_add_u32 v24, 61440, v22
Zvav 100,132,208
v_add_u32 v25, 122880, v21
Zvav 68,132,200
v_add_u32 v26, 122880, v22
Zvav 36,132,192
Zvav 40,136,192
X 172
Zvav 72,136,200
X 173
Zvav 104,136,208
X 174
K 220,172,173
Zvav 136,136,216
X 175
Zvav 140,140,216
X 168
K 221,174,175
Zvav 108,140,208
X 169
X 170
Zvav 76,140,200
s_waitcnt vmcnt(0) lgkmcnt(0)
s_barrier
K 212,168,169
X 171
Zvav 44,140,192
Ra 128,242,0
X 164
Zvav 48,144,192
K 213,170,171
X 165
Zvav 80,144,200
X 166
Zvav 112,144,208
Ra 132,242,1024
K 204,164,165
X 167
Zvav 144,144,216
X 160
Zvav 148,148,216
K 205,166,167
X 161
Zvav 116,148,208
Ra 136,242,2048
X 162
Zvav 84,148,200
K 196,160,161
X 163
Zvav 52,148,192
X 176
K 197,162,163
Zvav 56,152,192
Ra 140,242,3072
X 177
Zvav 88,152,200
X 178
K 198,176,177
Zvav 120,152,208
X 179
X 180
Zvav 152,152,216
K 199,178,179
Zvav 156,156,216
X 181
X 182
Zvav 124,156,208
K 206,180,181
X 183
Zvav 92,156,200
K 207,182,183
Zvav 60,156,192
s_cmp_eq_u64 s[24:25], 0
s_cbranch_scc1 .Ll1_30%=
W 0
Cvaa 160,128,0,224
Ra 144,242,4096
Ra 148,242,5120
Mvaa 160,132,4
s_add_u32 m0, s62, 0
Ra 152,242,6144
Cvaa 164,132,20,228
X 184
Mvaa 164,128,16
X 185
Cvaa 168,128,32,232
X 186
Mvaa 168,132,36
Ra 156,242,7168
K 214,184,185
Cvaa 172,132,52,236
Dc 1,8,61,0
Mvaa 172,128,48
X 187
Mvaa 160,136,8
X 188
Mvaa 160,140,12
Ra 128,242,16384
K 215,186,187
Mvaa 164,140,28
Dc 1,8,61,1024
Mvaa 164,136,24
X 189
Mvaa 168,136,40
X 190
Mvaa 168,140,44
Ra 132,242,17408
K 222,188,189
Mvaa 172,140,60
Dc 1,8,61,2048
Mvaa 172,136,56
X 191
v_smfmac_f32_16x16x64_bf16 a[160:163], a[176:179], v[192:199], v240
Ra 136,242,18432
K 223,190,191
v_smfmac_f32_16x16x64_bf16 a[164:167], a[176:179], v[200:207], v240
X 160
v_smfmac_f32_16x16x64_bf16 a[168:171], a[176:179], v[208:215], v240
Dc 1,8,61,3072
v_smfmac_f32_16x16x64_bf16 a[172:175], a[176:179], v[216:223], v240
W 3
X 161
Cvaa 176,144,0,224
s_add_u32 m0, s62, 4096
X 162
Mvaa 176,148,4
Ra 140,242,19456
K 192,160,161
Cvaa 180,148,20,228
X 163
Mvaa 180,144,16
Dc 243,8,61,0
Cvaa 184,144,32,232
X 164
Mvaa 184,148,36
K 193,162,163
Cvaa 188,148,52,236
X 165
Mvaa 188,144,48
Dc 243,8,61,1024
Ra 144,242,20480
Mvaa 176,152,8
X 166
Mvaa 176,156,12
Ra 148,242,21504
K 200,164,165
Mvaa 180,156,28
X 167
Mvaa 180,152,24
Dc 243,8,61,2048
Mvaa 184,152,40
X 168
Mvaa 184,156,44
K 201,166,167
Mvaa 188,156,60
X 169
Mvaa 188,152,56
W 2
Dc 243,8,61,3072
Ra 152,242,22528
Mvav 32,128,196
s_add_u32 m0, s62, 8192
X 170
Mvav 64,128,204
Ra 156,242,23552
K 208,168,169
Mvav 96,128,212
X 171
Mvav 128,128,220
Dc 244,8,61,0
Mvav 132,132,220
X 172
Mvav 100,132,212
Ra 128,242,8192
K 209,170,171
Mvav 68,132,204
X 173
Mvav 36,132,196
Dc 244,8,61,1024
Mvav 40,136,196
X 174
Mvav 72,136,204
Ra 132,242,9216
K 216,172,173
Mvav 104,136,212
X 175
Mvav 136,136,220
Dc 244,8,61,2048
Mvav 140,140,220
X 176
Mvav 108,140,212
Ra 136,242,10240
K 217,174,175
Mvav 76,140,204
X 177
Mvav 44,140,196
W 3
Dc 244,8,61,3072
Mvav 48,144,196
X 178
Mvav 80,144,204
s_add_u32 m0, s62, 12288
Ra 140,242,11264
Mvav 112,144,212
K 194,176,177
Mvav 144,144,220
X 179
Mvav 148,148,220
X 180
Mvav 116,148,212
Dc 245,8,61,0
Ra 144,242,12288
Mvav 84,148,204
K 195,178,179
Mvav 52,148,196
X 181
Mvav 56,152,196
X 182
Mvav 88,152,204
s_cmp_eq_u32 s14, 2
s_cselect_b64 s[18:19], s[30:31], s[32:33]
Dc 245,8,61,1024
Ra 148,242,13312
Mvav 120,152,212
s_mul_i32 s81, s58, 6
K 202,180,181
Mvav 152,152,220
s_add_u32 s81, s81, s57
X 183
Mvav 156,156,220
s_sub_u32 s81, s81, 2
X 184
Mvav 124,156,212
)"
R"(s_cmp_ge_u32 s57, 2
s_cselect_b32 s23, 2211836, 0
Dc 245,8,61,2048
Ra 152,242,14336
Mvav 92,156,204
s_mul_i32 s82, s81, 48384
K 203,182,183
Mvav 60,156,196
W 3
X 185
Cvaa 160,128,0,224
s_mul_i32 s83, s59, 448
X 186
Mvaa 160,132,4
s_andn2_b32 s83, s83, 127
Dc 245,8,61,3072
Ra 156,242,15360
Cvaa 164,132,20,228
s_add_u32 s82, s82, s83
s_add_u32 s61, s61, s63
K 210,184,185
Mvaa 164,128,16
v_min_u32 v246, 880, v1
v_lshrrev_b32 v247, 6, v246
Cvaa 168,128,32,232
X 187
Mvaa 168,132,36
X 188
Cvaa 172,132,52,236
s_xor_b32 s62, s62, 0x10000
v_mul_u32_u24 v247, 3456, v247
v_and_b32 v246, 48, v246
Mvaa 172,128,48
K 211,186,187
v_lshl_add_u32 v246, v246, 3, v247
Mvaa 160,136,8
X 189
Mvaa 160,140,12
s_add_u32 s64, s64, 1
Ra 128,242,24576
v_add_u32 v246, s82, v246
Mvaa 164,140,28
X 190
Mvaa 164,136,24
Ra 132,242,25600
v_min_u32 v246, s23, v246
Mvaa 168,136,40
global_load_dword v3, v246, s[18:19]
K 218,188,189
Mvaa 168,140,44
X 191
Mvaa 172,140,60
X 160
Mvaa 172,136,56
W 2
Ra 136,242,26624
K 219,190,191
Cvaa 176,144,0,224
X 161
Mvaa 176,148,4
Ra 140,242,27648
Cvaa 180,148,20,228
X 162
Mvaa 180,144,16
K 196,160,161
Cvaa 184,144,32,232
X 163
Mvaa 184,148,36
X 164
Cvaa 188,148,52,236
K 197,162,163
Mvaa 188,144,48
X 165
Mvaa 176,152,8
Ra 144,242,28672
Ra 148,242,29696
Mvaa 176,156,12
X 166
Mvaa 180,156,28
K 204,164,165
Mvaa 180,152,24
X 167
Mvaa 184,152,40
Mvaa 184,156,44
X 168
Mvaa 188,156,60
Mvaa 188,152,56
W 2
Ra 152,242,30720
K 205,166,167
Mvav 32,128,192
X 169
Mvav 64,128,200
Ra 156,242,31744
v_xor_b32 v242, 0x10000, v242
Mvav 96,128,208
X 170
Mvav 128,128,216
K 212,168,169
Mvav 132,132,216
X 171
Mvav 100,132,208
X 172
Mvav 68,132,200
Mvav 36,132,192
K 213,170,171
Mvav 40,136,192
X 173
Mvav 72,136,200
X 174
Mvav 104,136,208
Mvav 136,136,216
K 220,172,173
Mvav 140,140,216
X 175
Mvav 108,140,208
X 176
Mvav 76,140,200
s_waitcnt vmcnt(1) lgkmcnt(0)
s_barrier
Mvav 44,140,192
Ra 128,242,0
K 221,174,175
Mvav 48,144,192
X 177
Mvav 80,144,200
X 178
Mvav 112,144,208
Ra 132,242,1024
Mvav 144,144,216
K 198,176,177
Mvav 148,148,216
X 179
Mvav 116,148,208
X 180
Mvav 84,148,200
Ra 136,242,2048
Mvav 52,148,192
K 199,178,179
Mvav 56,152,192
X 181
Mvav 88,152,200
Ra 140,242,3072
Mvav 120,152,208
X 182
Mvav 152,152,216
K 206,180,181
Mvav 156,156,216
X 183
Mvav 124,156,208
Mvav 92,156,200
K 207,182,183
Mvav 60,156,192
W 0
Cvaa 160,128,0,224
Ra 144,242,4096
Ra 148,242,5120
Mvaa 160,132,4
s_add_u32 m0, s62, 0
Ra 152,242,6144
Cvaa 164,132,20,228
X 184
Mvaa 164,128,16
X 185
Cvaa 168,128,32,232
X 186
Mvaa 168,132,36
Ra 156,242,7168
K 214,184,185
Cvaa 172,132,52,236
Dc 1,8,61,0
Mvaa 172,128,48
X 187
Mvaa 160,136,8
X 188
Mvaa 160,140,12
Ra 128,242,16384
K 215,186,187
Mvaa 164,140,28
Dc 1,8,61,1024
Mvaa 164,136,24
X 189
Mvaa 168,136,40
X 190
Mvaa 168,140,44
Ra 132,242,17408
K 222,188,189
Mvaa 172,140,60
Dc 1,8,61,2048
Mvaa 172,136,56
X 191
v_smfmac_f32_16x16x64_bf16 a[160:163], a[176:179], v[192:199], v240
Ra 136,242,18432
K 223,190,191
v_smfmac_f32_16x16x64_bf16 a[164:167], a[176:179], v[200:207], v240
X 160
v_smfmac_f32_16x16x64_bf16 a[168:171], a[176:179], v[208:215], v240
Dc 1,8,61,3072
v_smfmac_f32_16x16x64_bf16 a[172:175], a[176:179], v[216:223], v240
W 3
X 161
Cvaa 176,144,0,224
s_add_u32 m0, s62, 4096
X 162
Mvaa 176,148,4
Ra 140,242,19456
K 192,160,161
Cvaa 180,148,20,228
X 163
Mvaa 180,144,16
Dc 243,8,61,0
Cvaa 184,144,32,232
X 164
Mvaa 184,148,36
K 193,162,163
Cvaa 188,148,52,236
X 165
Mvaa 188,144,48
Dc 243,8,61,1024
Ra 144,242,20480
Mvaa 176,152,8
X 166
Mvaa 176,156,12
Ra 148,242,21504
K 200,164,165
Mvaa 180,156,28
X 167
Mvaa 180,152,24
Dc 243,8,61,2048
Mvaa 184,152,40
X 168
Mvaa 184,156,44
K 201,166,167
Mvaa 188,156,60
X 169
Mvaa 188,152,56
W 2
Dc 243,8,61,3072
Ra 152,242,22528
Mvav 32,128,196
s_add_u32 m0, s62, 8192
X 170
Mvav 64,128,204
Ra 156,242,23552
K 208,168,169
Mvav 96,128,212
X 171
Mvav 128,128,220
Dc 244,8,61,0
Mvav 132,132,220
X 172
Mvav 100,132,212
Ra 128,242,8192
K 209,170,171
Mvav 68,132,204
X 173
Mvav 36,132,196
Dc 244,8,61,1024
Mvav 40,136,196
X 174
Mvav 72,136,204
Ra 132,242,9216
K 216,172,173
Mvav 104,136,212
X 175
Mvav 136,136,220
Dc 244,8,61,2048
Mvav 140,140,220
X 176
Mvav 108,140,212
Ra 136,242,10240
K 217,174,175
Mvav 76,140,204
X 177
Mvav 44,140,196
W 3
Dc 244,8,61,3072
Mvav 48,144,196
X 178
Mvav 80,144,204
s_add_u32 m0, s62, 12288
Ra 140,242,11264
Mvav 112,144,212
K 194,176,177
Mvav 144,144,220
X 179
Mvav 148,148,220
X 180
Mvav 116,148,212
Dc 245,8,61,0
Ra 144,242,12288
Mvav 84,148,204
K 195,178,179
Mvav 52,148,196
X 181
Mvav 56,152,196
X 182
Mvav 88,152,204
Dc 245,8,61,1024
Ra 148,242,13312
Mvav 120,152,212
v_add_u32 v246, 1161216, v246
v_min_u32 v246, s23, v246
Mvav 152,152,220
K 202,180,181
Mvav 156,156,220
X 183
Mvav 124,156,212
Dc 245,8,61,2048
Ra 152,242,14336
Mvav 92,156,204
X 184
Mvav 60,156,196
W 3
K 203,182,183
v_bfe_u32 v28, v1, 4, 4
Cvaa 160,128,0,224
X 185
Mvaa 160,132,4
Dc 245,8,61,3072
v_lshlrev_b32 v28, 3, v28
Cvaa 164,132,20,228
s_lshl_b32 s80, s56, 9
X 186
Mvaa 164,128,16
s_add_u32 s80, s80, 4224
Ra 156,242,15360
K 210,184,185
Cvaa 168,128,32,232
s_add_u32 s61, s61, s63
X 187
Mvaa 168,132,36
buffer_load_dwordx2 v[28:29], v28, s[8:11], s80 offen sc1
Cvaa 172,132,52,236
s_xor_b32 s62, s62, 0x10000
X 188
Mvaa 172,128,48
Ra 128,242,24576
K 211,186,187
Mvaa 160,136,8
X 189
Mvaa 160,140,12
s_add_u32 s64, s64, 1
global_load_dword v3, v246, s[18:19]
Ra 132,242,25600
Mvaa 164,140,28
s_mov_b64 s[18:19], s[34:35]
s_mul_i32 s81, s58, 6
s_add_u32 s81, s81, s57
Mvaa 164,136,24
s_sub_u32 s81, s81, 2
s_cmp_ge_u32 s57, 2
s_cselect_b32 s23, 2211836, 0
s_lshl_b32 s81, s81, 1
Mvaa 168,136,40
s_add_u32 s81, s81, s14
s_sub_u32 s81, s81, 2
s_mul_i32 s81, s81, 6016
Mvaa 168,140,44
s_mul_i32 s82, s59, 286720
s_add_u32 s81, s81, s82
v_min_u32 v246, 736, v1
Mvaa 172,140,60
v_lshl_add_u32 v246, v246, 3, s81
v_min_u32 v246, s23, v246
Mvaa 172,136,56
W 2
global_load_dword v3, v246, s[18:19]
Ra 136,242,26624
Cvaa 176,144,0,224
X 190
Mvaa 176,148,4
Ra 140,242,27648
K 218,188,189
Cvaa 180,148,20,228
X 191
Mvaa 180,144,16
X 160
Cvaa 184,144,32,232
K 219,190,191
Mvaa 184,148,36
X 161
Cvaa 188,148,52,236
X 162
Mvaa 188,144,48
Ra 144,242,28672
K 196,160,161
Mvaa 176,152,8
X 163
Mvaa 176,156,12
X 164
Mvaa 180,156,28
Ra 148,242,29696
K 197,162,163
Mvaa 180,152,24
X 165
Mvaa 184,152,40
X 166
Mvaa 184,156,44
K 204,164,165
)"
R"(Mvaa 188,156,60
X 167
Mvaa 188,152,56
W 2
X 168
Mvav 32,128,192
Ra 152,242,30720
K 205,166,167
Mvav 64,128,200
X 169
Mvav 96,128,208
Ra 156,242,31744
v_xor_b32 v242, 0x10000, v242
Mvav 128,128,216
X 170
Mvav 132,132,216
K 212,168,169
Mvav 100,132,208
X 171
Mvav 68,132,200
X 172
Mvav 36,132,192
K 213,170,171
Mvav 40,136,192
X 173
Mvav 72,136,200
X 174
Mvav 104,136,208
Mvav 136,136,216
s_waitcnt vmcnt(2)
v_cmp_eq_u64 vcc, s[12:13], v[28:29]
s_cmp_eq_u64 vcc, -1
s_cbranch_scc0 .Lspin_63%=
.Lok_64%=:
K 220,172,173
Mvav 140,140,216
X 175
Mvav 108,140,208
X 176
Mvav 76,140,200
W 0
s_barrier
Mvav 44,140,192
Ra 128,242,0
K 221,174,175
Mvav 48,144,192
X 177
Mvav 80,144,200
X 178
Mvav 112,144,208
Ra 132,242,1024
Mvav 144,144,216
K 198,176,177
Mvav 148,148,216
X 179
Mvav 116,148,208
X 180
Mvav 84,148,200
Ra 136,242,2048
Mvav 52,148,192
K 199,178,179
Mvav 56,152,192
X 181
Mvav 88,152,200
Ra 140,242,3072
Mvav 120,152,208
X 182
Mvav 152,152,216
K 206,180,181
Mvav 156,156,216
X 183
Mvav 124,156,208
Mvav 92,156,200
K 207,182,183
Mvav 60,156,192
W 0
Cvaa 160,128,0,224
Ra 144,242,4096
Ra 148,242,5120
Mvaa 160,132,4
s_add_u32 m0, s62, 0
Ra 152,242,6144
Cvaa 164,132,20,228
X 184
Mvaa 164,128,16
X 185
Cvaa 168,128,32,232
X 186
Mvaa 168,132,36
Ra 156,242,7168
K 214,184,185
Cvaa 172,132,52,236
Dc 1,8,61,0
Mvaa 172,128,48
X 187
Mvaa 160,136,8
X 188
Mvaa 160,140,12
Ra 128,242,16384
K 215,186,187
Mvaa 164,140,28
Dc 1,8,61,1024
Mvaa 164,136,24
X 189
Mvaa 168,136,40
X 190
Mvaa 168,140,44
Ra 132,242,17408
K 222,188,189
Mvaa 172,140,60
Dc 1,8,61,2048
Mvaa 172,136,56
X 191
v_smfmac_f32_16x16x64_bf16 a[160:163], a[176:179], v[192:199], v240
Ra 136,242,18432
K 223,190,191
v_smfmac_f32_16x16x64_bf16 a[164:167], a[176:179], v[200:207], v240
X 160
v_smfmac_f32_16x16x64_bf16 a[168:171], a[176:179], v[208:215], v240
Dc 1,8,61,3072
v_smfmac_f32_16x16x64_bf16 a[172:175], a[176:179], v[216:223], v240
W 3
X 161
Cvaa 176,144,0,224
s_add_u32 m0, s62, 4096
X 162
Mvaa 176,148,4
Ra 140,242,19456
K 192,160,161
Cvaa 180,148,20,228
X 163
Mvaa 180,144,16
Dc 243,8,61,0
Cvaa 184,144,32,232
X 164
Mvaa 184,148,36
K 193,162,163
Cvaa 188,148,52,236
X 165
Mvaa 188,144,48
Dc 243,8,61,1024
Ra 144,242,20480
Mvaa 176,152,8
X 166
Mvaa 176,156,12
Ra 148,242,21504
K 200,164,165
Mvaa 180,156,28
X 167
Mvaa 180,152,24
Dc 243,8,61,2048
Mvaa 184,152,40
X 168
Mvaa 184,156,44
K 201,166,167
Mvaa 188,156,60
X 169
Mvaa 188,152,56
W 2
Dc 243,8,61,3072
Ra 152,242,22528
Mvav 32,128,196
s_add_u32 m0, s62, 8192
X 170
Mvav 64,128,204
Ra 156,242,23552
K 208,168,169
Mvav 96,128,212
X 171
Mvav 128,128,220
Dc 244,8,61,0
Mvav 132,132,220
X 172
Mvav 100,132,212
Ra 128,242,8192
K 209,170,171
Mvav 68,132,204
X 173
Mvav 36,132,196
Dc 244,8,61,1024
Mvav 40,136,196
X 174
Mvav 72,136,204
Ra 132,242,9216
K 216,172,173
Mvav 104,136,212
X 175
Mvav 136,136,220
Dc 244,8,61,2048
Mvav 140,140,220
X 176
Mvav 108,140,212
Ra 136,242,10240
K 217,174,175
Mvav 76,140,204
X 177
Mvav 44,140,196
W 3
Dc 244,8,61,3072
Mvav 48,144,196
X 178
Mvav 80,144,204
s_add_u32 m0, s62, 12288
Ra 140,242,11264
Mvav 112,144,212
K 194,176,177
Mvav 144,144,220
X 179
Mvav 148,148,220
X 180
Mvav 116,148,212
Dc 245,8,61,0
Ra 144,242,12288
Mvav 84,148,204
K 195,178,179
Mvav 52,148,196
X 181
Mvav 56,152,196
X 182
Mvav 88,152,204
Dc 245,8,61,1024
Ra 148,242,13312
Mvav 120,152,212
K 202,180,181
Mvav 152,152,220
X 183
Mvav 156,156,220
X 184
Mvav 124,156,212
Dc 245,8,61,2048
Ra 152,242,14336
Mvav 92,156,204
K 203,182,183
Mvav 60,156,196
W 3
X 185
Cvaa 160,128,0,224
X 186
Mvaa 160,132,4
Dc 245,8,61,3072
Ra 156,242,15360
Cvaa 164,132,20,228
s_add_u32 s61, s61, s63
K 210,184,185
Mvaa 164,128,16
X 187
Cvaa 168,128,32,232
X 188
Mvaa 168,132,36
Cvaa 172,132,52,236
s_xor_b32 s62, s62, 0x10000
K 211,186,187
Mvaa 172,128,48
X 189
Mvaa 160,136,8
Ra 128,242,24576
Ra 132,242,25600
Mvaa 160,140,12
s_add_u32 s64, s64, 1
X 190
Mvaa 164,140,28
K 218,188,189
Mvaa 164,136,24
X 191
Mvaa 168,136,40
Mvaa 168,140,44
X 160
Mvaa 172,140,60
s_add_u32 s36, s29, 131072
K 219,190,191
Mvaa 172,136,56
W 2
X 161
Cvaa 176,144,0,224
Ra 136,242,26624
Ra 140,242,27648
Mvaa 176,148,4
X 162
Cvaa 180,148,20,228
K 196,160,161
Mvaa 180,144,16
buffer_store_dwordx4 a[124:127], v1, s[8:11], s36 offen offset:0 sc1
Cvaa 184,144,32,232
X 163
Mvaa 184,148,36
X 164
Cvaa 188,148,52,236
K 197,162,163
Mvaa 188,144,48
X 165
Mvaa 176,152,8
Ra 144,242,28672
buffer_store_dwordx4 a[116:119], v1, s[8:11], s36 offen offset:1024 sc1
Mvaa 176,156,12
Ra 148,242,29696
Mvaa 180,156,28
X 166
Mvaa 180,152,24
K 204,164,165
Mvaa 184,152,40
X 167
Mvaa 184,156,44
buffer_store_dwordx4 a[108:111], v1, s[8:11], s36 offen offset:2048 sc1
Mvaa 188,156,60
X 168
Mvaa 188,152,56
W 2
Ra 152,242,30720
K 205,166,167
Mvav 32,128,192
X 169
Mvav 64,128,200
Ra 156,242,31744
buffer_store_dwordx4 a[204:207], v1, s[8:11], s36 offen offset:3072 sc1
Mvav 96,128,208
X 170
Mvav 128,128,216
v_xor_b32 v242, 0x10000, v242
K 212,168,169
Mvav 132,132,216
X 171
Mvav 100,132,208
s_add_u32 s36, s29, 135168
X 172
Mvav 68,132,200
Mvav 36,132,192
K 213,170,171
Mvav 40,136,192
X 173
Mvav 72,136,200
X 174
Mvav 104,136,208
buffer_store_dwordx4 a[212:215], v1, s[8:11], s36 offen offset:0 sc1
Mvav 136,136,216
K 220,172,173
Mvav 140,140,216
X 175
Mvav 108,140,208
X 176
Mvav 76,140,200
s_waitcnt vmcnt(5) lgkmcnt(0)
s_barrier
Mvav 44,140,192
buffer_store_dwordx4 a[220:223], v1, s[8:11], s36 offen offset:1024 sc1
K 221,174,175
Mvav 48,144,192
Ra 128,242,0
Mvav 80,144,200
X 177
Mvav 112,144,208
X 178
Mvav 144,144,216
Ra 132,242,1024
K 198,176,177
Mvav 148,148,216
buffer_store_dwordx4 a[228:231], v1, s[8:11], s36 offen offset:2048 sc1
Mvav 116,148,208
X 179
Mvav 84,148,200
X 180
Mvav 52,148,192
Ra 136,242,2048
K 199,178,179
Mvav 56,152,192
buffer_store_dwordx4 a[76:79], v1, s[8:11], s36 offen offset:3072 sc1
Ra 140,242,3072
Mvav 88,152,200
X 181
Mvav 120,152,208
X 182
Mvav 152,152,216
K 206,180,181
Mvav 156,156,216
X 183
Mvav 124,156,208
Mvav 92,156,200
K 207,182,183
Mvav 60,156,192
W 0
Cvaa 160,128,0,224
Ra 144,242,4096
Ra 148,242,5120
Mvaa 160,132,4
s_add_u32 m0, s62, 0
Ra 152,242,6144
Cvaa 164,132,20,228
X 184
Mvaa 164,128,16
X 185
Cvaa 168,128,32,232
X 186
Mvaa 168,132,36
Ra 156,242,7168
K 214,184,185
Cvaa 172,132,52,236
Dc 1,8,61,0
Mvaa 172,128,48
X 187
Mvaa 160,136,8
X 188
)"
R"(Mvaa 160,140,12
Ra 128,242,16384
K 215,186,187
Mvaa 164,140,28
Dc 1,8,61,1024
Mvaa 164,136,24
X 189
Mvaa 168,136,40
X 190
Mvaa 168,140,44
Ra 132,242,17408
K 222,188,189
Mvaa 172,140,60
Dc 1,8,61,2048
Mvaa 172,136,56
X 191
v_smfmac_f32_16x16x64_bf16 a[160:163], a[176:179], v[192:199], v240
Ra 136,242,18432
K 223,190,191
v_smfmac_f32_16x16x64_bf16 a[164:167], a[176:179], v[200:207], v240
X 160
v_smfmac_f32_16x16x64_bf16 a[168:171], a[176:179], v[208:215], v240
Dc 1,8,61,3072
v_smfmac_f32_16x16x64_bf16 a[172:175], a[176:179], v[216:223], v240
W 3
X 161
Cvaa 176,144,0,224
s_add_u32 m0, s62, 4096
X 162
Mvaa 176,148,4
Ra 140,242,19456
K 192,160,161
Cvaa 180,148,20,228
X 163
Mvaa 180,144,16
Dc 243,8,61,0
Cvaa 184,144,32,232
X 164
Mvaa 184,148,36
K 193,162,163
Cvaa 188,148,52,236
X 165
Mvaa 188,144,48
Dc 243,8,61,1024
Ra 144,242,20480
Mvaa 176,152,8
X 166
Mvaa 176,156,12
Ra 148,242,21504
K 200,164,165
Mvaa 180,156,28
X 167
Mvaa 180,152,24
Dc 243,8,61,2048
Mvaa 184,152,40
X 168
Mvaa 184,156,44
K 201,166,167
Mvaa 188,156,60
X 169
Mvaa 188,152,56
W 2
Dc 243,8,61,3072
Ra 152,242,22528
Mvav 32,128,196
s_add_u32 m0, s62, 8192
X 170
Mvav 64,128,204
Ra 156,242,23552
K 208,168,169
Mvav 96,128,212
X 171
Mvav 128,128,220
Dc 244,8,61,0
Mvav 132,132,220
X 172
Mvav 100,132,212
Ra 128,242,8192
K 209,170,171
Mvav 68,132,204
X 173
Mvav 36,132,196
Dc 244,8,61,1024
Mvav 40,136,196
X 174
Mvav 72,136,204
Ra 132,242,9216
K 216,172,173
Mvav 104,136,212
X 175
Mvav 136,136,220
Dc 244,8,61,2048
Mvav 140,140,220
X 176
Mvav 108,140,212
Ra 136,242,10240
K 217,174,175
Mvav 76,140,204
X 177
Mvav 44,140,196
W 3
Dc 244,8,61,3072
Mvav 48,144,196
X 178
Mvav 80,144,204
s_add_u32 m0, s62, 12288
Ra 140,242,11264
Mvav 112,144,212
K 194,176,177
Mvav 144,144,220
X 179
Mvav 148,148,220
X 180
Mvav 116,148,212
Dc 245,8,61,0
Ra 144,242,12288
Mvav 84,148,204
K 195,178,179
Mvav 52,148,196
X 181
Mvav 56,152,196
X 182
Mvav 88,152,204
Dc 245,8,61,1024
Ra 148,242,13312
Mvav 120,152,212
K 202,180,181
Mvav 152,152,220
X 183
Mvav 156,156,220
X 184
Mvav 124,156,212
Dc 245,8,61,2048
Ra 152,242,14336
Mvav 92,156,204
K 203,182,183
Mvav 60,156,196
W 3
X 185
Cvaa 160,128,0,224
X 186
Mvaa 160,132,4
Dc 245,8,61,3072
Ra 156,242,15360
Cvaa 164,132,20,228
s_add_u32 s61, s61, s63
K 210,184,185
Mvaa 164,128,16
X 187
Cvaa 168,128,32,232
X 188
Mvaa 168,132,36
Cvaa 172,132,52,236
s_xor_b32 s62, s62, 0x10000
K 211,186,187
Mvaa 172,128,48
X 189
Mvaa 160,136,8
Ra 128,242,24576
Ra 132,242,25600
Mvaa 160,140,12
s_add_u32 s64, s64, 1
X 190
Mvaa 164,140,28
K 218,188,189
Mvaa 164,136,24
X 191
Mvaa 168,136,40
Mvaa 168,140,44
X 160
Mvaa 172,140,60
s_add_u32 s36, s29, 327680
K 219,190,191
Mvaa 172,136,56
W 2
X 161
Cvaa 176,144,0,224
Ra 136,242,26624
Ra 140,242,27648
Mvaa 176,148,4
X 162
Cvaa 180,148,20,228
K 196,160,161
Mvaa 180,144,16
buffer_store_dwordx4 a[84:87], v1, s[8:11], s36 offen offset:0 sc1
Cvaa 184,144,32,232
X 163
Mvaa 184,148,36
X 164
Cvaa 188,148,52,236
K 197,162,163
Mvaa 188,144,48
X 165
Mvaa 176,152,8
Ra 144,242,28672
buffer_store_dwordx4 a[92:95], v1, s[8:11], s36 offen offset:1024 sc1
Mvaa 176,156,12
Ra 148,242,29696
Mvaa 180,156,28
X 166
Mvaa 180,152,24
K 204,164,165
Mvaa 184,152,40
X 167
Mvaa 184,156,44
buffer_store_dwordx4 a[188:191], v1, s[8:11], s36 offen offset:2048 sc1
Mvaa 188,156,60
X 168
Mvaa 188,152,56
W 2
Ra 152,242,30720
K 205,166,167
Mvav 32,128,192
X 169
Mvav 64,128,200
Ra 156,242,31744
buffer_store_dwordx4 a[196:199], v1, s[8:11], s36 offen offset:3072 sc1
Mvav 96,128,208
X 170
Mvav 128,128,216
v_xor_b32 v242, 0x10000, v242
K 212,168,169
Mvav 132,132,216
X 171
Mvav 100,132,208
s_add_u32 s36, s29, 331776
X 172
Mvav 68,132,200
Mvav 36,132,192
K 213,170,171
Mvav 40,136,192
X 173
Mvav 72,136,200
X 174
Mvav 104,136,208
buffer_store_dwordx4 a[236:239], v1, s[8:11], s36 offen offset:0 sc1
Mvav 136,136,216
K 220,172,173
Mvav 140,140,216
X 175
Mvav 108,140,208
X 176
Mvav 76,140,200
s_waitcnt vmcnt(5) lgkmcnt(0)
s_barrier
Mvav 44,140,192
buffer_store_dwordx4 a[244:247], v1, s[8:11], s36 offen offset:1024 sc1
K 221,174,175
Mvav 48,144,192
Ra 128,242,0
Mvav 80,144,200
X 177
Mvav 112,144,208
X 178
Mvav 144,144,216
Ra 132,242,1024
K 198,176,177
Mvav 148,148,216
buffer_store_dwordx4 a[68:71], v1, s[8:11], s36 offen offset:2048 sc1
Mvav 116,148,208
X 179
Mvav 84,148,200
X 180
Mvav 52,148,192
Ra 136,242,2048
K 199,178,179
Mvav 56,152,192
buffer_store_dwordx4 a[184:187], v1, s[8:11], s36 offen offset:3072 sc1
Ra 140,242,3072
Mvav 88,152,200
X 181
Mvav 120,152,208
X 182
Mvav 152,152,216
K 206,180,181
Mvav 156,156,216
X 183
Mvav 124,156,208
Mvav 92,156,200
K 207,182,183
Mvav 60,156,192
s_mov_b64 exec, 0
s_cmp_eq_u32 s14, 2
s_cbranch_scc1 .Lh_3_2%=
s_branch .Lh_3_3%=
.Lhr_66%=:
s_mov_b64 exec, -1
W 0
Cvaa 160,128,0,224
Ra 144,242,4096
Ra 148,242,5120
Mvaa 160,132,4
s_add_u32 m0, s62, 0
Ra 152,242,6144
Cvaa 164,132,20,228
X 184
Mvaa 164,128,16
X 185
Cvaa 168,128,32,232
X 186
Mvaa 168,132,36
Ra 156,242,7168
K 214,184,185
Cvaa 172,132,52,236
Dc 1,8,61,0
Mvaa 172,128,48
X 187
Mvaa 160,136,8
X 188
Mvaa 160,140,12
Ra 128,242,16384
K 215,186,187
Mvaa 164,140,28
Dc 1,8,61,1024
Mvaa 164,136,24
X 189
Mvaa 168,136,40
X 190
Mvaa 168,140,44
Ra 132,242,17408
K 222,188,189
Mvaa 172,140,60
Dc 1,8,61,2048
Mvaa 172,136,56
X 191
v_smfmac_f32_16x16x64_bf16 a[160:163], a[176:179], v[192:199], v240
Ra 136,242,18432
K 223,190,191
v_smfmac_f32_16x16x64_bf16 a[164:167], a[176:179], v[200:207], v240
X 160
v_smfmac_f32_16x16x64_bf16 a[168:171], a[176:179], v[208:215], v240
Dc 1,8,61,3072
v_smfmac_f32_16x16x64_bf16 a[172:175], a[176:179], v[216:223], v240
W 3
X 161
Cvaa 176,144,0,224
s_add_u32 m0, s62, 4096
X 162
Mvaa 176,148,4
Ra 140,242,19456
K 192,160,161
Cvaa 180,148,20,228
X 163
Mvaa 180,144,16
Dc 243,8,61,0
Cvaa 184,144,32,232
X 164
Mvaa 184,148,36
K 193,162,163
Cvaa 188,148,52,236
X 165
Mvaa 188,144,48
Dc 243,8,61,1024
Ra 144,242,20480
Mvaa 176,152,8
X 166
Mvaa 176,156,12
Ra 148,242,21504
K 200,164,165
Mvaa 180,156,28
X 167
Mvaa 180,152,24
Dc 243,8,61,2048
Mvaa 184,152,40
X 168
Mvaa 184,156,44
K 201,166,167
Mvaa 188,156,60
X 169
Mvaa 188,152,56
W 2
Dc 243,8,61,3072
Ra 152,242,22528
Mvav 32,128,196
s_add_u32 m0, s62, 8192
X 170
Mvav 64,128,204
Ra 156,242,23552
K 208,168,169
Mvav 96,128,212
X 171
Mvav 128,128,220
Dc 244,8,61,0
Mvav 132,132,220
X 172
Mvav 100,132,212
Ra 128,242,8192
K 209,170,171
Mvav 68,132,204
X 173
Mvav 36,132,196
Dc 244,8,61,1024
Mvav 40,136,196
X 174
Mvav 72,136,204
Ra 132,242,9216
K 216,172,173
Mvav 104,136,212
X 175
Mvav 136,136,220
)"
R"(Dc 244,8,61,2048
Mvav 140,140,220
X 176
Mvav 108,140,212
Ra 136,242,10240
K 217,174,175
Mvav 76,140,204
X 177
Mvav 44,140,196
W 3
Dc 244,8,61,3072
Mvav 48,144,196
X 178
Mvav 80,144,204
s_add_u32 m0, s62, 12288
Ra 140,242,11264
Mvav 112,144,212
K 194,176,177
Mvav 144,144,220
X 179
Mvav 148,148,220
X 180
Mvav 116,148,212
Dc 245,8,61,0
Ra 144,242,12288
Mvav 84,148,204
K 195,178,179
Mvav 52,148,196
X 181
Mvav 56,152,196
X 182
Mvav 88,152,204
Dc 245,8,61,1024
Ra 148,242,13312
Mvav 120,152,212
K 202,180,181
Mvav 152,152,220
X 183
Mvav 156,156,220
s_waitcnt vmcnt(14)
s_add_u32 s80, s71, 256
v_mov_b64 v[28:29], s[12:13]
s_mov_b64 exec, 1
buffer_store_dwordx2 v[28:29], off, s[8:11], s80 sc1
s_mov_b64 exec, -1
X 184
Mvav 124,156,212
Ra 152,242,14336
Mvav 92,156,204
K 203,182,183
Mvav 60,156,196
W 3
X 185
Cvaa 160,128,0,224
Dc 245,8,61,2048
Ra 156,242,15360
Mvaa 160,132,4
X 186
Cvaa 164,132,20,228
K 210,184,185
Mvaa 164,128,16
X 187
Cvaa 168,128,32,232
Dc 245,8,61,3072
Mvaa 168,132,36
s_add_u32 s61, s61, s63
X 188
Cvaa 172,132,52,236
s_xor_b32 s62, s62, 0x10000
K 211,186,187
Mvaa 172,128,48
X 189
Mvaa 160,136,8
Ra 128,242,24576
Ra 132,242,25600
Mvaa 160,140,12
s_add_u32 s64, s64, 1
X 190
Mvaa 164,140,28
K 218,188,189
Mvaa 164,136,24
X 191
Mvaa 168,136,40
Mvaa 168,140,44
X 160
Mvaa 172,140,60
s_add_u32 s36, s29, 196608
K 219,190,191
Mvaa 172,136,56
W 2
X 161
Cvaa 176,144,0,224
Ra 136,242,26624
Ra 140,242,27648
Mvaa 176,148,4
X 162
Cvaa 180,148,20,228
K 196,160,161
Mvaa 180,144,16
buffer_store_dwordx4 a[192:195], v1, s[8:11], s36 offen offset:0 sc1
Cvaa 184,144,32,232
X 163
Mvaa 184,148,36
X 164
Cvaa 188,148,52,236
K 197,162,163
Mvaa 188,144,48
X 165
Mvaa 176,152,8
Ra 144,242,28672
buffer_store_dwordx4 a[200:203], v1, s[8:11], s36 offen offset:1024 sc1
Mvaa 176,156,12
Ra 148,242,29696
Mvaa 180,156,28
X 166
Mvaa 180,152,24
K 204,164,165
Mvaa 184,152,40
X 167
Mvaa 184,156,44
buffer_store_dwordx4 a[208:211], v1, s[8:11], s36 offen offset:2048 sc1
Mvaa 188,156,60
X 168
Mvaa 188,152,56
W 2
Ra 152,242,30720
K 205,166,167
Mvav 32,128,192
X 169
Mvav 64,128,200
Ra 156,242,31744
buffer_store_dwordx4 a[216:219], v1, s[8:11], s36 offen offset:3072 sc1
Mvav 96,128,208
X 170
Mvav 128,128,216
v_xor_b32 v242, 0x10000, v242
K 212,168,169
Mvav 132,132,216
X 171
Mvav 100,132,208
s_add_u32 s36, s29, 200704
X 172
Mvav 68,132,200
Mvav 36,132,192
K 213,170,171
Mvav 40,136,192
X 173
Mvav 72,136,200
X 174
Mvav 104,136,208
buffer_store_dwordx4 a[224:227], v1, s[8:11], s36 offen offset:0 sc1
Mvav 136,136,216
K 220,172,173
Mvav 140,140,216
X 175
Mvav 108,140,208
X 176
Mvav 76,140,200
s_waitcnt vmcnt(5) lgkmcnt(0)
s_barrier
Mvav 44,140,192
buffer_store_dwordx4 a[232:235], v1, s[8:11], s36 offen offset:1024 sc1
K 221,174,175
Mvav 48,144,192
Ra 128,242,0
Mvav 80,144,200
X 177
Mvav 112,144,208
X 178
Mvav 144,144,216
Ra 132,242,1024
K 198,176,177
Mvav 148,148,216
buffer_store_dwordx4 a[240:243], v1, s[8:11], s36 offen offset:2048 sc1
Mvav 116,148,208
X 179
Mvav 84,148,200
X 180
Mvav 52,148,192
Ra 136,242,2048
K 199,178,179
Mvav 56,152,192
buffer_store_dwordx4 a[100:103], v1, s[8:11], s36 offen offset:3072 sc1
Ra 140,242,3072
Mvav 88,152,200
X 181
Mvav 120,152,208
X 182
Mvav 152,152,216
K 206,180,181
Mvav 156,156,216
X 183
Mvav 124,156,208
Mvav 92,156,200
K 207,182,183
Mvav 60,156,192
W 0
Cvaa 160,128,0,224
Ra 144,242,4096
Ra 148,242,5120
Mvaa 160,132,4
s_add_u32 m0, s62, 0
Ra 152,242,6144
Cvaa 164,132,20,228
X 184
Mvaa 164,128,16
X 185
Cvaa 168,128,32,232
X 186
Mvaa 168,132,36
Ra 156,242,7168
K 214,184,185
Cvaa 172,132,52,236
Dc 1,8,61,0
Mvaa 172,128,48
X 187
Mvaa 160,136,8
X 188
Mvaa 160,140,12
Ra 128,242,16384
K 215,186,187
Mvaa 164,140,28
Dc 1,8,61,1024
Mvaa 164,136,24
X 189
Mvaa 168,136,40
X 190
Mvaa 168,140,44
Ra 132,242,17408
K 222,188,189
Mvaa 172,140,60
Dc 1,8,61,2048
Mvaa 172,136,56
X 191
v_smfmac_f32_16x16x64_bf16 a[160:163], a[176:179], v[192:199], v240
Ra 136,242,18432
K 223,190,191
v_smfmac_f32_16x16x64_bf16 a[164:167], a[176:179], v[200:207], v240
X 160
v_smfmac_f32_16x16x64_bf16 a[168:171], a[176:179], v[208:215], v240
Dc 1,8,61,3072
v_smfmac_f32_16x16x64_bf16 a[172:175], a[176:179], v[216:223], v240
W 3
X 161
Cvaa 176,144,0,224
s_add_u32 m0, s62, 4096
X 162
Mvaa 176,148,4
Ra 140,242,19456
K 192,160,161
Cvaa 180,148,20,228
X 163
Mvaa 180,144,16
Dc 243,8,61,0
Cvaa 184,144,32,232
X 164
Mvaa 184,148,36
K 193,162,163
Cvaa 188,148,52,236
X 165
Mvaa 188,144,48
Dc 243,8,61,1024
Ra 144,242,20480
Mvaa 176,152,8
X 166
Mvaa 176,156,12
Ra 148,242,21504
K 200,164,165
Mvaa 180,156,28
X 167
Mvaa 180,152,24
Dc 243,8,61,2048
Mvaa 184,152,40
X 168
Mvaa 184,156,44
K 201,166,167
Mvaa 188,156,60
X 169
Mvaa 188,152,56
W 2
Dc 243,8,61,3072
Ra 152,242,22528
Mvav 32,128,196
s_add_u32 m0, s62, 8192
X 170
Mvav 64,128,204
Ra 156,242,23552
K 208,168,169
Mvav 96,128,212
X 171
Mvav 128,128,220
Dc 244,8,61,0
Mvav 132,132,220
X 172
Mvav 100,132,212
Ra 128,242,8192
K 209,170,171
Mvav 68,132,204
X 173
Mvav 36,132,196
Dc 244,8,61,1024
Mvav 40,136,196
X 174
Mvav 72,136,204
Ra 132,242,9216
K 216,172,173
Mvav 104,136,212
X 175
Mvav 136,136,220
Dc 244,8,61,2048
Mvav 140,140,220
X 176
Mvav 108,140,212
Ra 136,242,10240
K 217,174,175
Mvav 76,140,204
X 177
Mvav 44,140,196
W 3
Dc 244,8,61,3072
Mvav 48,144,196
X 178
Mvav 80,144,204
s_add_u32 m0, s62, 12288
Ra 140,242,11264
Mvav 112,144,212
K 194,176,177
Mvav 144,144,220
X 179
Mvav 148,148,220
X 180
Mvav 116,148,212
Dc 245,8,61,0
Ra 144,242,12288
Mvav 84,148,204
K 195,178,179
Mvav 52,148,196
X 181
Mvav 56,152,196
X 182
Mvav 88,152,204
Dc 245,8,61,1024
Ra 148,242,13312
Mvav 120,152,212
K 202,180,181
Mvav 152,152,220
X 183
Mvav 156,156,220
X 184
Mvav 124,156,212
Dc 245,8,61,2048
Ra 152,242,14336
Mvav 92,156,204
K 203,182,183
v_bfe_u32 v28, v1, 4, 4
Mvav 60,156,196
W 3
X 185
Cvaa 160,128,0,224
Ra 156,242,15360
v_lshlrev_b32 v28, 3, v28
Mvaa 160,132,4
s_lshl_b32 s80, s56, 9
s_add_u32 s80, s80, 4352
Dc 245,8,61,3072
Cvaa 164,132,20,228
s_add_u32 s61, s61, s63
X 186
Mvaa 164,128,16
K 210,184,185
Cvaa 168,128,32,232
X 187
Mvaa 168,132,36
buffer_load_dwordx2 v[28:29], v28, s[8:11], s80 offen sc1
Cvaa 172,132,52,236
s_xor_b32 s62, s62, 0x10000
X 188
Mvaa 172,128,48
Ra 128,242,24576
K 211,186,187
Mvaa 160,136,8
X 189
Mvaa 160,140,12
s_add_u32 s64, s64, 1
X 190
Mvaa 164,140,28
Ra 132,242,25600
K 218,188,189
Mvaa 164,136,24
X 191
Mvaa 168,136,40
Mvaa 168,140,44
X 160
Mvaa 172,140,60
s_add_u32 s36, s29, 262144
K 219,190,191
Mvaa 172,136,56
W 2
X 161
Cvaa 176,144,0,224
)"
R"(Ra 136,242,26624
Ra 140,242,27648
Mvaa 176,148,4
X 162
Cvaa 180,148,20,228
K 196,160,161
Mvaa 180,144,16
buffer_store_dwordx4 a[64:67], v1, s[8:11], s36 offen offset:0 sc1
Cvaa 184,144,32,232
X 163
Mvaa 184,148,36
X 164
Cvaa 188,148,52,236
K 197,162,163
Mvaa 188,144,48
X 165
Mvaa 176,152,8
Ra 144,242,28672
buffer_store_dwordx4 a[72:75], v1, s[8:11], s36 offen offset:1024 sc1
Mvaa 176,156,12
Ra 148,242,29696
Mvaa 180,156,28
X 166
Mvaa 180,152,24
K 204,164,165
Mvaa 184,152,40
X 167
Mvaa 184,156,44
buffer_store_dwordx4 a[80:83], v1, s[8:11], s36 offen offset:2048 sc1
Mvaa 188,156,60
X 168
Mvaa 188,152,56
W 2
Ra 152,242,30720
K 205,166,167
Mvav 32,128,192
X 169
Mvav 64,128,200
Ra 156,242,31744
buffer_store_dwordx4 a[88:91], v1, s[8:11], s36 offen offset:3072 sc1
Mvav 96,128,208
X 170
Mvav 128,128,216
v_xor_b32 v242, 0x10000, v242
K 212,168,169
Mvav 132,132,216
X 171
Mvav 100,132,208
s_add_u32 s36, s29, 266240
X 172
Mvav 68,132,200
Mvav 36,132,192
K 213,170,171
Mvav 40,136,192
X 173
Mvav 72,136,200
X 174
Mvav 104,136,208
buffer_store_dwordx4 a[96:99], v1, s[8:11], s36 offen offset:0 sc1
Mvav 136,136,216
s_waitcnt vmcnt(5)
v_cmp_eq_u64 vcc, s[12:13], v[28:29]
s_cmp_eq_u64 vcc, -1
s_cbranch_scc0 .Lspin_67%=
.Lok_68%=:
K 220,172,173
Mvav 140,140,216
X 175
Mvav 108,140,208
X 176
Mvav 76,140,200
W 0
s_barrier
Mvav 44,140,192
buffer_store_dwordx4 a[104:107], v1, s[8:11], s36 offen offset:1024 sc1
K 221,174,175
Mvav 48,144,192
Ra 128,242,0
Mvav 80,144,200
X 177
Mvav 112,144,208
X 178
Mvav 144,144,216
Ra 132,242,1024
K 198,176,177
Mvav 148,148,216
buffer_store_dwordx4 a[112:115], v1, s[8:11], s36 offen offset:2048 sc1
Mvav 116,148,208
X 179
Mvav 84,148,200
X 180
Mvav 52,148,192
Ra 136,242,2048
K 199,178,179
Mvav 56,152,192
buffer_store_dwordx4 a[120:123], v1, s[8:11], s36 offen offset:3072 sc1
Ra 140,242,3072
Mvav 88,152,200
X 181
Mvav 120,152,208
X 182
Mvav 152,152,216
K 206,180,181
Mvav 156,156,216
X 183
Mvav 124,156,208
Mvav 92,156,200
K 207,182,183
Mvav 60,156,192
W 0
Cvaa 160,128,0,224
Ra 144,242,4096
Ra 148,242,5120
Mvaa 160,132,4
s_add_u32 m0, s62, 0
Ra 152,242,6144
Cvaa 164,132,20,228
X 184
Mvaa 164,128,16
X 185
Cvaa 168,128,32,232
X 186
Mvaa 168,132,36
Ra 156,242,7168
K 214,184,185
Cvaa 172,132,52,236
Dc 1,8,61,0
Mvaa 172,128,48
X 187
Mvaa 160,136,8
X 188
Mvaa 160,140,12
Ra 128,242,16384
K 215,186,187
Mvaa 164,140,28
Dc 1,8,61,1024
Mvaa 164,136,24
X 189
Mvaa 168,136,40
X 190
Mvaa 168,140,44
Ra 132,242,17408
K 222,188,189
Mvaa 172,140,60
Dc 1,8,61,2048
Mvaa 172,136,56
X 191
v_smfmac_f32_16x16x64_bf16 a[160:163], a[176:179], v[192:199], v240
Ra 136,242,18432
K 223,190,191
v_smfmac_f32_16x16x64_bf16 a[164:167], a[176:179], v[200:207], v240
X 160
v_smfmac_f32_16x16x64_bf16 a[168:171], a[176:179], v[208:215], v240
Dc 1,8,61,3072
v_smfmac_f32_16x16x64_bf16 a[172:175], a[176:179], v[216:223], v240
W 3
X 161
Cvaa 176,144,0,224
s_add_u32 m0, s62, 4096
X 162
Mvaa 176,148,4
Ra 140,242,19456
K 192,160,161
Cvaa 180,148,20,228
X 163
Mvaa 180,144,16
Dc 243,8,61,0
Cvaa 184,144,32,232
X 164
Mvaa 184,148,36
K 193,162,163
Cvaa 188,148,52,236
X 165
Mvaa 188,144,48
Dc 243,8,61,1024
Ra 144,242,20480
Mvaa 176,152,8
X 166
Mvaa 176,156,12
Ra 148,242,21504
K 200,164,165
Mvaa 180,156,28
X 167
Mvaa 180,152,24
Dc 243,8,61,2048
Mvaa 184,152,40
X 168
Mvaa 184,156,44
K 201,166,167
Mvaa 188,156,60
X 169
Mvaa 188,152,56
W 2
Dc 243,8,61,3072
Ra 152,242,22528
Mvav 32,128,196
s_add_u32 m0, s62, 8192
X 170
Mvav 64,128,204
Ra 156,242,23552
K 208,168,169
Mvav 96,128,212
X 171
Mvav 128,128,220
Dc 244,8,61,0
Mvav 132,132,220
X 172
Mvav 100,132,212
Ra 128,242,8192
K 209,170,171
Mvav 68,132,204
X 173
Mvav 36,132,196
Dc 244,8,61,1024
Mvav 40,136,196
X 174
Mvav 72,136,204
Ra 132,242,9216
K 216,172,173
Mvav 104,136,212
X 175
Mvav 136,136,220
Dc 244,8,61,2048
Mvav 140,140,220
X 176
Mvav 108,140,212
Ra 136,242,10240
K 217,174,175
Mvav 76,140,204
X 177
Mvav 44,140,196
W 3
Dc 244,8,61,3072
Mvav 48,144,196
X 178
Mvav 80,144,204
s_add_u32 m0, s62, 12288
Ra 140,242,11264
Mvav 112,144,212
K 194,176,177
Mvav 144,144,220
X 179
Mvav 148,148,220
X 180
Mvav 116,148,212
Dc 245,8,61,0
Ra 144,242,12288
Mvav 84,148,204
K 195,178,179
Mvav 52,148,196
X 181
Mvav 56,152,196
X 182
Mvav 88,152,204
Dc 245,8,61,1024
Ra 148,242,13312
Mvav 120,152,212
s_bitcmp1_b32 195, s57
s_cselect_b32 s22, -1, 0
K 202,180,181
Mvav 152,152,220
s_lshr_b32 s82, s57, 2
X 183
Mvav 156,156,220
s_waitcnt vmcnt(14)
s_add_u32 s80, s71, 384
v_mov_b64 v[28:29], s[12:13]
s_mov_b64 exec, 1
buffer_store_dwordx2 v[28:29], off, s[8:11], s80 sc1
s_mov_b64 exec, -1
X 184
Mvav 124,156,212
s_and_b32 s81, s57, 1
s_lshl1_add_u32 s81, s82, s81
Ra 152,242,14336
Mvav 92,156,204
s_lshl_b32 s82, s56, 2
K 203,182,183
Mvav 60,156,196
W 3
X 185
Cvaa 160,128,0,224
s_add_u32 s81, s81, s82
Dc 245,8,61,2048
Ra 156,242,15360
Mvaa 160,132,4
s_lshl_b32 s81, s81, 1
X 186
Cvaa 164,132,20,228
s_add_u32 s81, s81, s14
s_sub_u32 s81, s81, 2
K 210,184,185
Mvaa 164,128,16
s_mul_i32 s81, s81, 20480
X 187
Cvaa 168,128,32,232
Dc 245,8,61,3072
v_min_u32 v2, 624, v1
Mvaa 168,132,36
s_add_u32 s61, s61, s63
X 188
Cvaa 172,132,52,236
s_xor_b32 s62, s62, 0x10000
v_lshl_add_u32 v2, v2, 3, s81
K 211,186,187
Mvaa 172,128,48
Ra 128,242,24576
v_and_b32 v2, s22, v2
Mvaa 160,136,8
s_and_b32 s22, s22, 5120
X 189
Mvaa 160,140,12
s_add_u32 s64, s64, 1
global_load_dword v3, v2, s[26:27]
Ra 132,242,25600
Mvaa 164,140,28
X 190
Mvaa 164,136,24
v_add_u32 v2, s22, v2
K 218,188,189
Mvaa 168,136,40
X 191
Mvaa 168,140,44
X 160
Mvaa 172,140,60
K 219,190,191
Mvaa 172,136,56
W 2
X 161
Cvaa 176,144,0,224
Ra 136,242,26624
Ra 140,242,27648
Mvaa 176,148,4
X 162
Cvaa 180,148,20,228
K 196,160,161
Mvaa 180,144,16
X 163
Cvaa 184,144,32,232
Mvaa 184,148,36
X 164
Cvaa 188,148,52,236
K 197,162,163
Mvaa 188,144,48
X 165
Mvaa 176,152,8
Ra 144,242,28672
Ra 148,242,29696
Mvaa 176,156,12
X 166
Mvaa 180,156,28
K 204,164,165
Mvaa 180,152,24
X 167
Mvaa 184,152,40
Mvaa 184,156,44
X 168
Mvaa 188,156,60
Mvaa 188,152,56
W 2
Ra 152,242,30720
K 205,166,167
Mvav 32,128,192
X 169
Mvav 64,128,200
Ra 156,242,31744
v_xor_b32 v242, 0x10000, v242
Mvav 96,128,208
X 170
Mvav 128,128,216
K 212,168,169
Mvav 132,132,216
X 171
Mvav 100,132,208
X 172
Mvav 68,132,200
Mvav 36,132,192
K 213,170,171
Mvav 40,136,192
X 173
Mvav 72,136,200
X 174
Mvav 104,136,208
Mvav 136,136,216
K 220,172,173
Mvav 140,140,216
X 175
Mvav 108,140,208
X 176
Mvav 76,140,200
s_waitcnt vmcnt(1) lgkmcnt(0)
s_barrier
Mvav 44,140,192
Ra 128,242,0
K 221,174,175
Mvav 48,144,192
X 177
Mvav 80,144,200
.Lh_3_2%=:
s_cbranch_execz .Lh_3_2_1%=
)"
R"(X 178
Mvav 112,144,208
Ra 132,242,1024
Mvav 144,144,216
K 198,176,177
Mvav 148,148,216
X 179
Mvav 116,148,208
X 180
Mvav 84,148,200
Ra 136,242,2048
Mvav 52,148,192
K 199,178,179
Mvav 56,152,192
X 181
Mvav 88,152,200
Ra 140,242,3072
Mvav 120,152,208
X 182
Mvav 152,152,216
K 206,180,181
Mvav 156,156,216
X 183
Mvav 124,156,208
Mvav 92,156,200
K 207,182,183
Mvav 60,156,192
W 0
Cvaa 160,128,0,224
Ra 144,242,4096
Ra 148,242,5120
Mvaa 160,132,4
s_add_u32 m0, s62, 0
Ra 152,242,6144
Cvaa 164,132,20,228
X 184
Mvaa 164,128,16
X 185
Cvaa 168,128,32,232
X 186
Mvaa 168,132,36
Ra 156,242,7168
K 214,184,185
Cvaa 172,132,52,236
Dc 1,8,61,0
Mvaa 172,128,48
X 187
Mvaa 160,136,8
X 188
Mvaa 160,140,12
Ra 128,242,16384
K 215,186,187
Mvaa 164,140,28
Dc 1,8,61,1024
Mvaa 164,136,24
X 189
Mvaa 168,136,40
X 190
Mvaa 168,140,44
Ra 132,242,17408
K 222,188,189
Mvaa 172,140,60
Dc 1,8,61,2048
Mvaa 172,136,56
X 191
v_smfmac_f32_16x16x64_bf16 a[160:163], a[176:179], v[192:199], v240
Ra 136,242,18432
K 223,190,191
v_smfmac_f32_16x16x64_bf16 a[164:167], a[176:179], v[200:207], v240
X 160
v_smfmac_f32_16x16x64_bf16 a[168:171], a[176:179], v[208:215], v240
Dc 1,8,61,3072
v_smfmac_f32_16x16x64_bf16 a[172:175], a[176:179], v[216:223], v240
W 3
X 161
Cvaa 176,144,0,224
s_add_u32 m0, s62, 4096
X 162
Mvaa 176,148,4
Ra 140,242,19456
K 192,160,161
Cvaa 180,148,20,228
X 163
Mvaa 180,144,16
Dc 243,8,61,0
Cvaa 184,144,32,232
X 164
Mvaa 184,148,36
K 193,162,163
Cvaa 188,148,52,236
X 165
Mvaa 188,144,48
Dc 243,8,61,1024
Ra 144,242,20480
Mvaa 176,152,8
X 166
Mvaa 176,156,12
Ra 148,242,21504
K 200,164,165
Mvaa 180,156,28
X 167
Mvaa 180,152,24
Dc 243,8,61,2048
Mvaa 184,152,40
X 168
Mvaa 184,156,44
K 201,166,167
Mvaa 188,156,60
X 169
Mvaa 188,152,56
W 2
Dc 243,8,61,3072
Ra 152,242,22528
Mvav 32,128,196
s_add_u32 m0, s62, 8192
X 170
Mvav 64,128,204
Ra 156,242,23552
K 208,168,169
Mvav 96,128,212
X 171
Mvav 128,128,220
Dc 244,8,61,0
Mvav 132,132,220
X 172
Mvav 100,132,212
Ra 128,242,8192
K 209,170,171
Mvav 68,132,204
X 173
Mvav 36,132,196
Dc 244,8,61,1024
Mvav 40,136,196
X 174
Mvav 72,136,204
Ra 132,242,9216
K 216,172,173
Mvav 104,136,212
X 175
Mvav 136,136,220
Dc 244,8,61,2048
Mvav 140,140,220
X 176
Mvav 108,140,212
Ra 136,242,10240
K 217,174,175
Mvav 76,140,204
X 177
Mvav 44,140,196
W 3
Dc 244,8,61,3072
Mvav 48,144,196
X 178
Mvav 80,144,204
s_add_u32 m0, s62, 12288
Ra 140,242,11264
Mvav 112,144,212
K 194,176,177
Mvav 144,144,220
X 179
Mvav 148,148,220
X 180
Mvav 116,148,212
Dc 245,8,61,0
Ra 144,242,12288
Mvav 84,148,204
K 195,178,179
Mvav 52,148,196
X 181
Mvav 56,152,196
X 182
Mvav 88,152,204
Dc 245,8,61,1024
Ra 148,242,13312
Mvav 120,152,212
K 202,180,181
Mvav 152,152,220
X 183
Mvav 156,156,220
X 184
Mvav 124,156,212
Dc 245,8,61,2048
Ra 152,242,14336
Mvav 92,156,204
K 203,182,183
Mvav 60,156,196
W 3
X 185
Cvaa 160,128,0,224
X 186
Mvaa 160,132,4
Dc 245,8,61,3072
Ra 156,242,15360
Cvaa 164,132,20,228
s_add_u32 s61, s61, s63
K 210,184,185
Mvaa 164,128,16
X 187
Cvaa 168,128,32,232
X 188
Mvaa 168,132,36
Cvaa 172,132,52,236
s_xor_b32 s62, s62, 0x10000
global_load_dword v3, v2, s[26:27]
K 211,186,187
Mvaa 172,128,48
X 189
Mvaa 160,136,8
Ra 128,242,24576
Ra 132,242,25600
Mvaa 160,140,12
s_add_u32 s64, s64, 1
X 190
Mvaa 164,140,28
v_add_u32 v2, s22, v2
K 218,188,189
Mvaa 164,136,24
X 191
Mvaa 168,136,40
Mvaa 168,140,44
X 160
Mvaa 172,140,60
K 219,190,191
Mvaa 172,136,56
W 2
X 161
Cvaa 176,144,0,224
Ra 136,242,26624
Ra 140,242,27648
Mvaa 176,148,4
X 162
Cvaa 180,148,20,228
K 196,160,161
Mvaa 180,144,16
X 163
Cvaa 184,144,32,232
Mvaa 184,148,36
X 164
Cvaa 188,148,52,236
K 197,162,163
Mvaa 188,144,48
X 165
Mvaa 176,152,8
Ra 144,242,28672
Ra 148,242,29696
Mvaa 176,156,12
X 166
Mvaa 180,156,28
K 204,164,165
Mvaa 180,152,24
X 167
Mvaa 184,152,40
Mvaa 184,156,44
X 168
Mvaa 188,156,60
Mvaa 188,152,56
W 2
Ra 152,242,30720
K 205,166,167
Mvav 32,128,192
X 169
Mvav 64,128,200
Ra 156,242,31744
v_xor_b32 v242, 0x10000, v242
Mvav 96,128,208
X 170
Mvav 128,128,216
K 212,168,169
Mvav 132,132,216
X 171
Mvav 100,132,208
X 172
Mvav 68,132,200
Mvav 36,132,192
K 213,170,171
Mvav 40,136,192
X 173
Mvav 72,136,200
X 174
Mvav 104,136,208
Mvav 136,136,216
K 220,172,173
Mvav 140,140,216
X 175
Mvav 108,140,208
X 176
Mvav 76,140,200
s_waitcnt vmcnt(1) lgkmcnt(0)
s_barrier
Mvav 44,140,192
Ra 128,242,0
K 221,174,175
Mvav 48,144,192
X 177
Mvav 80,144,200
X 178
Mvav 112,144,208
Ra 132,242,1024
Mvav 144,144,216
K 198,176,177
Mvav 148,148,216
X 179
Mvav 116,148,208
X 180
Mvav 84,148,200
Ra 136,242,2048
Mvav 52,148,192
K 199,178,179
Mvav 56,152,192
X 181
Mvav 88,152,200
Ra 140,242,3072
Mvav 120,152,208
X 182
Mvav 152,152,216
K 206,180,181
Mvav 156,156,216
X 183
Mvav 124,156,208
Mvav 92,156,200
K 207,182,183
Mvav 60,156,192
W 0
Cvaa 160,128,0,224
Ra 144,242,4096
Ra 148,242,5120
Mvaa 160,132,4
s_add_u32 m0, s62, 0
Ra 152,242,6144
Cvaa 164,132,20,228
X 184
Mvaa 164,128,16
X 185
Cvaa 168,128,32,232
X 186
Mvaa 168,132,36
Ra 156,242,7168
K 214,184,185
Cvaa 172,132,52,236
Dc 1,8,61,0
Mvaa 172,128,48
X 187
Mvaa 160,136,8
X 188
Mvaa 160,140,12
Ra 128,242,16384
K 215,186,187
Mvaa 164,140,28
Dc 1,8,61,1024
Mvaa 164,136,24
X 189
Mvaa 168,136,40
X 190
Mvaa 168,140,44
Ra 132,242,17408
K 222,188,189
Mvaa 172,140,60
Dc 1,8,61,2048
Mvaa 172,136,56
X 191
v_smfmac_f32_16x16x64_bf16 a[160:163], a[176:179], v[192:199], v240
Ra 136,242,18432
K 223,190,191
v_smfmac_f32_16x16x64_bf16 a[164:167], a[176:179], v[200:207], v240
X 160
v_smfmac_f32_16x16x64_bf16 a[168:171], a[176:179], v[208:215], v240
Dc 1,8,61,3072
v_smfmac_f32_16x16x64_bf16 a[172:175], a[176:179], v[216:223], v240
W 3
X 161
Cvaa 176,144,0,224
s_add_u32 m0, s62, 4096
X 162
Mvaa 176,148,4
Ra 140,242,19456
K 192,160,161
Cvaa 180,148,20,228
X 163
Mvaa 180,144,16
Dc 243,8,61,0
Cvaa 184,144,32,232
X 164
Mvaa 184,148,36
K 193,162,163
Cvaa 188,148,52,236
X 165
Mvaa 188,144,48
Dc 243,8,61,1024
Ra 144,242,20480
Mvaa 176,152,8
X 166
Mvaa 176,156,12
Ra 148,242,21504
K 200,164,165
Mvaa 180,156,28
X 167
Mvaa 180,152,24
Dc 243,8,61,2048
Mvaa 184,152,40
X 168
)"
R"(Mvaa 184,156,44
K 201,166,167
Mvaa 188,156,60
X 169
Mvaa 188,152,56
W 2
Dc 243,8,61,3072
Ra 152,242,22528
Mvav 32,128,196
s_add_u32 m0, s62, 8192
X 170
Mvav 64,128,204
Ra 156,242,23552
K 208,168,169
Mvav 96,128,212
X 171
Mvav 128,128,220
Dc 244,8,61,0
Mvav 132,132,220
X 172
Mvav 100,132,212
Ra 128,242,8192
K 209,170,171
Mvav 68,132,204
X 173
Mvav 36,132,196
Dc 244,8,61,1024
Mvav 40,136,196
X 174
Mvav 72,136,204
Ra 132,242,9216
K 216,172,173
Mvav 104,136,212
X 175
Mvav 136,136,220
Dc 244,8,61,2048
Mvav 140,140,220
X 176
Mvav 108,140,212
Ra 136,242,10240
K 217,174,175
Mvav 76,140,204
X 177
Mvav 44,140,196
W 3
Dc 244,8,61,3072
Mvav 48,144,196
X 178
Mvav 80,144,204
s_add_u32 m0, s62, 12288
Ra 140,242,11264
Mvav 112,144,212
K 194,176,177
Mvav 144,144,220
X 179
Mvav 148,148,220
X 180
Mvav 116,148,212
Dc 245,8,61,0
Ra 144,242,12288
Mvav 84,148,204
K 195,178,179
Mvav 52,148,196
X 181
Mvav 56,152,196
X 182
Mvav 88,152,204
Dc 245,8,61,1024
Ra 148,242,13312
Mvav 120,152,212
K 202,180,181
Mvav 152,152,220
X 183
Mvav 156,156,220
X 184
Mvav 124,156,212
Dc 245,8,61,2048
Ra 152,242,14336
Mvav 92,156,204
K 203,182,183
Mvav 60,156,196
W 3
X 185
Cvaa 160,128,0,224
X 186
Mvaa 160,132,4
Dc 245,8,61,3072
Ra 156,242,15360
Cvaa 164,132,20,228
s_add_u32 s61, s61, s63
K 210,184,185
Mvaa 164,128,16
X 187
Cvaa 168,128,32,232
X 188
Mvaa 168,132,36
Cvaa 172,132,52,236
s_xor_b32 s62, s62, 0x10000
global_load_dword v3, v2, s[26:27]
K 211,186,187
Mvaa 172,128,48
Ra 128,242,24576
v_add_u32 v2, s22, v2
Mvaa 160,136,8
s_lshr_b32 s80, s15, 3
s_lshl_b32 s80, s80, 1
s_add_u32 s80, s80, s14
Mvaa 160,140,12
s_sub_u32 s80, s80, 2
s_lshl_b32 s80, s80, 12
v_min_u32 v27, 496, v1
Mvaa 164,140,28
s_add_u32 s64, s64, 1
X 189
Mvaa 164,136,24
Ra 132,242,25600
v_lshl_add_u32 v27, v27, 3, s80
Mvaa 168,136,40
X 190
Mvaa 168,140,44
v_min_u32 v27, .Lend%=-.Lbeg%=-4, v27
K 218,188,189
Mvaa 172,140,60
global_load_dword v3, v27, s[6:7]
Mvaa 172,136,56
W 2
X 191
Cvaa 176,144,0,224
X 160
Mvaa 176,148,4
Ra 136,242,26624
K 219,190,191
Cvaa 180,148,20,228
X 161
Mvaa 180,144,16
Ra 140,242,27648
Cvaa 184,144,32,232
X 162
Mvaa 184,148,36
K 196,160,161
Cvaa 188,148,52,236
X 163
Mvaa 188,144,48
X 164
Mvaa 176,152,8
Ra 144,242,28672
K 197,162,163
Mvaa 176,156,12
X 165
Mvaa 180,156,28
Ra 148,242,29696
Mvaa 180,152,24
X 166
Mvaa 184,152,40
K 204,164,165
Mvaa 184,156,44
X 167
Mvaa 188,156,60
X 168
Mvaa 188,152,56
W 2
Ra 152,242,30720
K 205,166,167
Mvav 32,128,192
X 169
Mvav 64,128,200
Ra 156,242,31744
v_xor_b32 v242, 0x10000, v242
Mvav 96,128,208
X 170
Mvav 128,128,216
K 212,168,169
Mvav 132,132,216
X 171
Mvav 100,132,208
X 172
Mvav 68,132,200
Mvav 36,132,192
K 213,170,171
Mvav 40,136,192
X 173
Mvav 72,136,200
X 174
Mvav 104,136,208
Mvav 136,136,216
K 220,172,173
Mvav 140,140,216
X 175
Mvav 108,140,208
.Lh_3_3%=:
s_cbranch_execz .Lhr_66%=
X 176
Mvav 76,140,200
s_waitcnt vmcnt(2) lgkmcnt(0)
s_barrier
Mvav 44,140,192
Ra 128,242,0
K 221,174,175
Mvav 48,144,192
X 177
Mvav 80,144,200
X 178
Mvav 112,144,208
Ra 132,242,1024
Mvav 144,144,216
K 198,176,177
Mvav 148,148,216
X 179
Mvav 116,148,208
X 180
Mvav 84,148,200
Ra 136,242,2048
Mvav 52,148,192
K 199,178,179
Mvav 56,152,192
X 181
Mvav 88,152,200
Ra 140,242,3072
Mvav 120,152,208
X 182
Mvav 152,152,216
K 206,180,181
Mvav 156,156,216
X 183
Mvav 124,156,208
Mvav 92,156,200
K 207,182,183
Mvav 60,156,192
W 0
Cvaa 160,128,0,224
Ra 144,242,4096
Ra 148,242,5120
Mvaa 160,132,4
s_add_u32 m0, s62, 0
Ra 152,242,6144
Cvaa 164,132,20,228
X 184
Mvaa 164,128,16
X 185
Cvaa 168,128,32,232
X 186
Mvaa 168,132,36
Ra 156,242,7168
K 214,184,185
Cvaa 172,132,52,236
Dc 1,8,61,0
Mvaa 172,128,48
X 187
Mvaa 160,136,8
X 188
Mvaa 160,140,12
Ra 128,242,16384
K 215,186,187
Mvaa 164,140,28
Dc 1,8,61,1024
Mvaa 164,136,24
X 189
Mvaa 168,136,40
X 190
Mvaa 168,140,44
Ra 132,242,17408
K 222,188,189
Mvaa 172,140,60
Dc 1,8,61,2048
Mvaa 172,136,56
X 191
v_smfmac_f32_16x16x64_bf16 a[160:163], a[176:179], v[192:199], v240
Ra 136,242,18432
K 223,190,191
v_smfmac_f32_16x16x64_bf16 a[164:167], a[176:179], v[200:207], v240
X 160
v_smfmac_f32_16x16x64_bf16 a[168:171], a[176:179], v[208:215], v240
Dc 1,8,61,3072
v_smfmac_f32_16x16x64_bf16 a[172:175], a[176:179], v[216:223], v240
W 3
X 161
Cvaa 176,144,0,224
s_add_u32 m0, s62, 4096
X 162
Mvaa 176,148,4
s_sub_u32 s81, s57, 1
Ra 140,242,19456
K 192,160,161
Cvaa 180,148,20,228
X 163
Mvaa 180,144,16
s_lshl_b32 s85, s81, 5
Dc 243,8,61,0
Cvaa 184,144,32,232
X 164
Mvaa 184,148,36
s_add_u32 s85, s85, s56
K 193,162,163
Cvaa 188,148,52,236
X 165
Mvaa 188,144,48
Dc 243,8,61,1024
Ra 144,242,20480
Mvaa 176,152,8
s_lshl_b32 s85, s85, 1
X 166
Mvaa 176,156,12
Ra 148,242,21504
K 200,164,165
Mvaa 180,156,28
s_add_u32 s85, s85, s14
X 167
Mvaa 180,152,24
Dc 243,8,61,2048
Mvaa 184,152,40
s_sub_u32 s85, s85, 2
X 168
Mvaa 184,156,44
K 201,166,167
Mvaa 188,156,60
X 169
Mvaa 188,152,56
W 2
s_cmp_lt_u32 s85, 160
s_cselect_b64 s[20:21], 0, -1
Dc 243,8,61,3072
Mvav 32,128,196
s_add_u32 m0, s62, 8192
Ra 152,242,22528
Ra 156,242,23552
Mvav 64,128,204
s_mul_i32 s18, s85, 205
X 170
Mvav 96,128,212
K 208,168,169
Mvav 128,128,220
s_lshr_b32 s18, s18, 12
X 171
Mvav 132,132,220
Dc 244,8,61,0
Ra 128,242,8192
Mvav 100,132,212
X 172
Mvav 68,132,204
s_and_b32 s18, s18, 7
K 209,170,171
Mvav 36,132,196
X 173
Mvav 40,136,196
Dc 244,8,61,1024
v_lshrrev_b32 v4, 1, v1
Mvav 72,136,204
X 174
Mvav 104,136,212
Ra 132,242,9216
K 216,172,173
Mvav 136,136,220
X 175
Mvav 140,140,220
Dc 244,8,61,2048
v_min_u32 v5, 344, v4
Mvav 108,140,212
s_lshl_b32 s81, s18, 3
X 176
Mvav 76,140,204
Ra 136,242,10240
K 217,174,175
Mvav 44,140,196
W 3
X 177
Mvav 48,144,196
Dc 244,8,61,3072
v_mov_b32 v6, s81
Mvav 80,144,204
s_add_u32 m0, s62, 12288
X 178
Mvav 112,144,212
Ra 140,242,11264
K 194,176,177
Mvav 144,144,220
X 179
Mvav 148,148,220
buffer_load_dwordx2 v[8:9], v4, s[8:11], 0 offen offset:2560 sc1
Ra 144,242,12288
Mvav 116,148,212
X 180
Mvav 84,148,204
K 195,178,179
Mvav 52,148,196
X 181
Mvav 56,152,196
buffer_load_dwordx2 v[10:11], v5, s[8:11], 0 offen offset:3072 sc1
Ra 148,242,13312
Mvav 88,152,204
X 182
Mvav 120,152,212
K 202,180,181
Mvav 152,152,220
X 183
Mvav 156,156,220
Dc 245,8,61,0
Ra 152,242,14336
Mvav 124,156,212
X 184
Mvav 92,156,204
)"
R"(K 203,182,183
v_bfe_u32 v28, v1, 4, 4
Mvav 60,156,196
W 3
X 185
Cvaa 160,128,0,224
buffer_load_dwordx2 v[12:13], v6, s[8:11], 0 offen offset:3584 sc1
v_lshlrev_b32 v28, 3, v28
Mvaa 160,132,4
s_lshl_b32 s80, s56, 9
X 186
Cvaa 164,132,20,228
s_add_u32 s80, s80, 4480
Ra 156,242,15360
K 210,184,185
Mvaa 164,128,16
X 187
Cvaa 168,128,32,232
Dc 245,8,61,1024
Mvaa 168,132,36
X 188
Cvaa 172,132,52,236
K 211,186,187
Mvaa 172,128,48
X 189
Mvaa 160,136,8
Dc 245,8,61,2048
Ra 128,242,24576
Mvaa 160,140,12
X 190
Mvaa 164,140,28
Ra 132,242,25600
K 218,188,189
Mvaa 164,136,24
X 191
Mvaa 168,136,40
buffer_load_dwordx2 v[28:29], v28, s[8:11], s80 offen sc1
Mvaa 168,140,44
Dc 245,8,61,3072
s_add_u32 s61, s61, s63
s_xor_b32 s62, s62, 0x10000
s_add_u32 s64, s64, 1
Mvaa 172,140,60
X 160
Mvaa 172,136,56
W 2
Ra 136,242,26624
K 219,190,191
Cvaa 176,144,0,224
X 161
Mvaa 176,148,4
global_load_dword v3, v2, s[26:27]
v_add_u32 v2, s22, v2
Cvaa 180,148,20,228
Ra 140,242,27648
Mvaa 180,144,16
X 162
Cvaa 184,144,32,232
K 196,160,161
Mvaa 184,148,36
X 163
Cvaa 188,148,52,236
X 164
Mvaa 188,144,48
Ra 144,242,28672
K 197,162,163
Mvaa 176,152,8
X 165
Mvaa 176,156,12
Ra 148,242,29696
Mvaa 180,156,28
X 166
Mvaa 180,152,24
K 204,164,165
Mvaa 184,152,40
X 167
Mvaa 184,156,44
X 168
Mvaa 188,156,60
Mvaa 188,152,56
W 2
Ra 152,242,30720
K 205,166,167
Mvav 32,128,192
X 169
Mvav 64,128,200
Ra 156,242,31744
v_xor_b32 v242, 0x10000, v242
Mvav 96,128,208
X 170
Mvav 128,128,216
K 212,168,169
Mvav 132,132,216
X 171
Mvav 100,132,208
X 172
Mvav 68,132,200
Mvav 36,132,192
K 213,170,171
Mvav 40,136,192
X 173
Mvav 72,136,200
X 174
Mvav 104,136,208
Mvav 136,136,216
s_waitcnt vmcnt(2)
v_cmp_eq_u64 vcc, s[12:13], v[28:29]
s_cmp_eq_u64 vcc, -1
s_cbranch_scc0 .Lspin_70%=
.Lok_71%=:
v_cmp_eq_u64 vcc, s[12:13], v[8:9]
v_cmp_eq_u64_e64 s[86:87], s[12:13], v[10:11]
s_and_b64 vcc, vcc, s[86:87]
s_or_b64 vcc, vcc, s[20:21]
s_cmp_eq_u64 vcc, -1
s_cbranch_scc0 .Lspin_73%=
.Lok_74%=:
K 220,172,173
Mvav 140,140,216
X 175
Mvav 108,140,208
X 176
Mvav 76,140,200
s_waitcnt vmcnt(1) lgkmcnt(0)
s_barrier
Mvav 44,140,192
Ra 128,242,0
K 221,174,175
Mvav 48,144,192
X 177
Mvav 80,144,200
X 178
Mvav 112,144,208
Ra 132,242,1024
Mvav 144,144,216
K 198,176,177
Mvav 148,148,216
X 179
Mvav 116,148,208
X 180
Mvav 84,148,200
Ra 136,242,2048
Mvav 52,148,192
K 199,178,179
Mvav 56,152,192
X 181
Mvav 88,152,200
Ra 140,242,3072
Mvav 120,152,208
X 182
Mvav 152,152,216
K 206,180,181
Mvav 156,156,216
X 183
Mvav 124,156,208
Mvav 92,156,200
K 207,182,183
Mvav 60,156,192
s_mov_b64 exec, 0
s_cmp_eq_u32 s14, 2
s_cbranch_scc1 .Lh_4_2%=
s_branch .Lh_4_3%=
.Lhr_76%=:
s_mov_b64 exec, -1
W 0
Cvaa 160,128,0,224
Ra 144,242,4096
Ra 148,242,5120
Mvaa 160,132,4
s_add_u32 m0, s62, 0
Ra 152,242,6144
Cvaa 164,132,20,228
X 184
Mvaa 164,128,16
X 185
Cvaa 168,128,32,232
X 186
Mvaa 168,132,36
Ra 156,242,7168
K 214,184,185
Cvaa 172,132,52,236
Dc 1,8,61,0
Mvaa 172,128,48
X 187
Mvaa 160,136,8
X 188
Mvaa 160,140,12
Ra 128,242,16384
K 215,186,187
Mvaa 164,140,28
Dc 1,8,61,1024
Mvaa 164,136,24
X 189
Mvaa 168,136,40
X 190
Mvaa 168,140,44
Ra 132,242,17408
K 222,188,189
Mvaa 172,140,60
Dc 1,8,61,2048
Mvaa 172,136,56
X 191
v_smfmac_f32_16x16x64_bf16 a[160:163], a[176:179], v[192:199], v240
Ra 136,242,18432
K 223,190,191
v_smfmac_f32_16x16x64_bf16 a[164:167], a[176:179], v[200:207], v240
X 160
v_smfmac_f32_16x16x64_bf16 a[168:171], a[176:179], v[208:215], v240
Dc 1,8,61,3072
v_smfmac_f32_16x16x64_bf16 a[172:175], a[176:179], v[216:223], v240
W 3
X 161
Cvaa 176,144,0,224
s_add_u32 m0, s62, 4096
X 162
Mvaa 176,148,4
Ra 140,242,19456
K 192,160,161
Cvaa 180,148,20,228
X 163
Mvaa 180,144,16
Dc 243,8,61,0
Cvaa 184,144,32,232
X 164
Mvaa 184,148,36
K 193,162,163
Cvaa 188,148,52,236
X 165
Mvaa 188,144,48
Dc 243,8,61,1024
Ra 144,242,20480
Mvaa 176,152,8
X 166
Mvaa 176,156,12
Ra 148,242,21504
K 200,164,165
Mvaa 180,156,28
X 167
Mvaa 180,152,24
Dc 243,8,61,2048
Mvaa 184,152,40
X 168
Mvaa 184,156,44
K 201,166,167
Mvaa 188,156,60
X 169
Mvaa 188,152,56
W 2
Dc 243,8,61,3072
Ra 152,242,22528
Mvav 32,128,196
s_add_u32 m0, s62, 8192
X 170
Mvav 64,128,204
Ra 156,242,23552
K 208,168,169
Mvav 96,128,212
X 171
Mvav 128,128,220
Dc 244,8,61,0
Mvav 132,132,220
X 172
Mvav 100,132,212
Ra 128,242,8192
K 209,170,171
Mvav 68,132,204
X 173
Mvav 36,132,196
Dc 244,8,61,1024
Mvav 40,136,196
X 174
Mvav 72,136,204
Ra 132,242,9216
K 216,172,173
Mvav 104,136,212
X 175
Mvav 136,136,220
Dc 244,8,61,2048
Mvav 140,140,220
X 176
Mvav 108,140,212
Ra 136,242,10240
K 217,174,175
Mvav 76,140,204
X 177
Mvav 44,140,196
W 3
Dc 244,8,61,3072
Mvav 48,144,196
X 178
Mvav 80,144,204
s_add_u32 m0, s62, 12288
Ra 140,242,11264
Mvav 112,144,212
K 194,176,177
Mvav 144,144,220
X 179
Mvav 148,148,220
X 180
Mvav 116,148,212
Dc 245,8,61,0
Ra 144,242,12288
Mvav 84,148,204
K 195,178,179
Mvav 52,148,196
X 181
Mvav 56,152,196
X 182
Mvav 88,152,204
Dc 245,8,61,1024
Ra 148,242,13312
Mvav 120,152,212
v_xor_b32 v6, s12, v12
K 202,180,181
Mvav 152,152,220
X 183
Mvav 156,156,220
Ra 152,242,14336
v_and_b32 v6, 2047, v6
Mvav 124,156,212
Dc 245,8,61,2048
v_lshlrev_b32 v6, 3, v6
Mvav 92,156,204
X 184
Mvav 60,156,196
W 3
K 203,182,183
Cvaa 160,128,0,224
s_mov_b32 s81, 40960
X 185
Mvaa 160,132,4
Dc 245,8,61,3072
Ra 156,242,15360
Cvaa 164,132,20,228
s_add_u32 s61, s61, s63
X 186
Mvaa 164,128,16
K 210,184,185
Cvaa 168,128,32,232
X 187
Mvaa 168,132,36
buffer_load_dword v7, v6, s[8:11], s81 offen sc1
v_lshrrev_b32 v5, 7, v1
Cvaa 172,132,52,236
s_xor_b32 s62, s62, 0x10000
X 188
Mvaa 172,128,48
K 211,186,187
v_mul_u32_u24 v4, 20480, v5
Mvaa 160,136,8
X 189
Mvaa 160,140,12
s_add_u32 s64, s64, 1
Ra 128,242,24576
Ra 132,242,25600
Mvaa 164,140,28
X 190
Mvaa 164,136,24
v_and_b32 v6, 0x70, v1
K 218,188,189
Mvaa 168,136,40
X 191
Mvaa 168,140,44
v_add_u32 v4, v4, v6
Mvaa 172,140,60
X 160
Mvaa 172,136,56
W 2
v_add_u32 v5, 2, v5
K 219,190,191
Cvaa 176,144,0,224
X 161
Mvaa 176,148,4
Ra 136,242,26624
v_lshrrev_b32 v5, 3, v5
Cvaa 180,148,20,228
Ra 140,242,27648
v_lshl_or_b32 v5, v5, 31, v4
Mvaa 180,144,16
X 162
Cvaa 184,144,32,232
K 196,160,161
Mvaa 184,148,36
s_lshl_b32 s19, s85, 7
X 163
Cvaa 188,148,52,236
X 164
Mvaa 188,144,48
s_add_u32 s19, s19, 2752512
Ra 144,242,28672
K 197,162,163
Mvaa 176,152,8
X 165
Mvaa 176,156,12
s_and_b32 s81, s20, 134217728
Ra 148,242,29696
Mvaa 180,156,28
.Lh_3_2_1%=:
s_cbranch_execz .Lhr_66%=
X 166
)"
R"(Mvaa 180,152,24
K 204,164,165
Mvaa 184,152,40
s_or_b32 s19, s19, s81
X 167
Mvaa 184,156,44
X 168
Mvaa 188,156,60
buffer_load_dwordx4 a[64:67], v4, s[8:11], s19 offen sc1
Mvaa 188,152,56
W 2
Ra 152,242,30720
K 205,166,167
Mvav 32,128,192
s_add_u32 s19, s19, 163840
X 169
Mvav 64,128,200
Ra 156,242,31744
v_xor_b32 v242, 0x10000, v242
Mvav 96,128,208
X 170
Mvav 128,128,216
buffer_load_dwordx4 a[68:71], v4, s[8:11], s19 offen sc1
K 212,168,169
Mvav 132,132,216
X 171
Mvav 100,132,208
s_add_u32 s19, s19, 163840
X 172
Mvav 68,132,200
Mvav 36,132,192
buffer_load_dwordx4 a[72:75], v4, s[8:11], s19 offen sc1
K 213,170,171
Mvav 40,136,192
X 173
Mvav 72,136,200
X 174
Mvav 104,136,208
s_add_u32 s19, s19, 163840
Mvav 136,136,216
K 220,172,173
Mvav 140,140,216
X 175
Mvav 108,140,208
buffer_load_dwordx4 a[76:79], v4, s[8:11], s19 offen sc1
Mvav 76,140,200
s_waitcnt vmcnt(5) lgkmcnt(0)
s_barrier
Mvav 44,140,192
s_add_u32 s19, s19, 163840
X 176
Mvav 48,144,192
Ra 128,242,0
K 221,174,175
Mvav 80,144,200
X 177
Mvav 112,144,208
buffer_load_dwordx4 a[80:83], v4, s[8:11], s19 offen sc1
Ra 132,242,1024
Mvav 144,144,216
s_add_u32 s19, s19, 163840
X 178
Mvav 148,148,216
K 198,176,177
Mvav 116,148,208
X 179
Mvav 84,148,200
buffer_load_dwordx4 a[84:87], v4, s[8:11], s19 offen sc1
Ra 136,242,2048
Mvav 52,148,192
X 180
Mvav 56,152,192
s_add_u32 s19, s19, 163840
Ra 140,242,3072
K 199,178,179
Mvav 88,152,200
X 181
Mvav 120,152,208
X 182
Mvav 152,152,216
buffer_load_dwordx4 a[88:91], v5, s[8:11], s19 offen sc1
K 206,180,181
Mvav 156,156,216
X 183
Mvav 124,156,208
Mvav 92,156,200
K 207,182,183
Mvav 60,156,192
W 0
Cvaa 160,128,0,224
Ra 144,242,4096
Ra 148,242,5120
Mvaa 160,132,4
s_add_u32 m0, s62, 0
Ra 152,242,6144
Cvaa 164,132,20,228
X 184
Mvaa 164,128,16
X 185
Cvaa 168,128,32,232
X 186
Mvaa 168,132,36
Ra 156,242,7168
K 214,184,185
Cvaa 172,132,52,236
Dc 1,8,61,0
Mvaa 172,128,48
X 187
Mvaa 160,136,8
X 188
Mvaa 160,140,12
Ra 128,242,16384
K 215,186,187
Mvaa 164,140,28
Dc 1,8,61,1024
Mvaa 164,136,24
X 189
Mvaa 168,136,40
X 190
Mvaa 168,140,44
Ra 132,242,17408
K 222,188,189
Mvaa 172,140,60
Dc 1,8,61,2048
Mvaa 172,136,56
X 191
v_smfmac_f32_16x16x64_bf16 a[160:163], a[176:179], v[192:199], v240
Ra 136,242,18432
K 223,190,191
v_smfmac_f32_16x16x64_bf16 a[164:167], a[176:179], v[200:207], v240
X 160
v_smfmac_f32_16x16x64_bf16 a[168:171], a[176:179], v[208:215], v240
Dc 1,8,61,3072
v_smfmac_f32_16x16x64_bf16 a[172:175], a[176:179], v[216:223], v240
W 3
X 161
Cvaa 176,144,0,224
s_add_u32 m0, s62, 4096
X 162
Mvaa 176,148,4
Ra 140,242,19456
K 192,160,161
Cvaa 180,148,20,228
X 163
Mvaa 180,144,16
Dc 243,8,61,0
Cvaa 184,144,32,232
X 164
Mvaa 184,148,36
K 193,162,163
Cvaa 188,148,52,236
X 165
Mvaa 188,144,48
Dc 243,8,61,1024
Ra 144,242,20480
Mvaa 176,152,8
X 166
Mvaa 176,156,12
Ra 148,242,21504
K 200,164,165
Mvaa 180,156,28
X 167
Mvaa 180,152,24
Dc 243,8,61,2048
Mvaa 184,152,40
X 168
Mvaa 184,156,44
K 201,166,167
Mvaa 188,156,60
X 169
Mvaa 188,152,56
W 2
Dc 243,8,61,3072
Ra 152,242,22528
Mvav 32,128,196
s_add_u32 m0, s62, 8192
X 170
Mvav 64,128,204
Ra 156,242,23552
K 208,168,169
Mvav 96,128,212
X 171
Mvav 128,128,220
Dc 244,8,61,0
Mvav 132,132,220
X 172
Mvav 100,132,212
Ra 128,242,8192
K 209,170,171
Mvav 68,132,204
X 173
Mvav 36,132,196
Dc 244,8,61,1024
Mvav 40,136,196
X 174
Mvav 72,136,204
Ra 132,242,9216
K 216,172,173
Mvav 104,136,212
X 175
Mvav 136,136,220
Dc 244,8,61,2048
Mvav 140,140,220
s_bfe_u32 s81, s59, 0x10001
X 176
Mvav 108,140,212
s_lshl_b32 s82, s57, 1
Ra 136,242,10240
K 217,174,175
Mvav 76,140,204
s_add_u32 s81, s81, s82
X 177
Mvav 44,140,196
W 3
s_lshl_b32 s83, s81, 2
Dc 244,8,61,3072
Mvav 48,144,196
s_lshr_b32 s81, s59, 2
X 178
Mvav 80,144,204
s_add_u32 m0, s62, 12288
s_lshl_b32 s81, s81, 1
Ra 140,242,11264
Mvav 112,144,212
s_add_u32 s83, s83, s81
K 194,176,177
Mvav 144,144,220
s_and_b32 s81, s59, 1
X 179
Mvav 148,148,220
s_add_u32 s83, s83, s81
X 180
Mvav 116,148,212
s_and_b32 s81, s14, 1
Dc 245,8,61,0
Ra 144,242,12288
Mvav 84,148,204
s_mul_i32 s81, s81, 124
K 195,178,179
Mvav 52,148,196
s_xor_b32 s83, s83, s81
X 181
Mvav 56,152,196
s_mul_i32 s83, s83, 20480
X 182
Mvav 88,152,204
s_mul_i32 s81, s58, 320
Dc 245,8,61,1024
Ra 148,242,13312
Mvav 120,152,212
s_add_u32 s82, s83, s81
K 202,180,181
Mvav 152,152,220
X 183
Mvav 156,156,220
Ra 152,242,14336
v_bfe_u32 v246, v1, 4, 4
Mvav 124,156,212
Dc 245,8,61,2048
v_mul_u32_u24 v246, 1280, v246
Mvav 92,156,204
X 184
Mvav 60,156,196
W 3
v_lshrrev_b32 v247, 8, v1
K 203,182,183
Cvaa 160,128,0,224
v_mul_u32_u24 v247, 104, v247
v_add3_u32 v246, v246, v247, s82
Mvaa 160,132,4
X 185
Cvaa 164,132,20,228
Dc 245,8,61,3072
Ra 156,242,15360
Mvaa 164,128,16
s_add_u32 s61, s61, s63
X 186
Cvaa 168,128,32,232
K 210,184,185
Mvaa 168,132,36
X 187
Cvaa 172,132,52,236
s_xor_b32 s62, s62, 0x10000
X 188
Mvaa 172,128,48
global_load_dword v3, v246, s[16:17]
K 211,186,187
Mvaa 160,136,8
s_lshl_b32 s81, s81, 3
s_add_u32 s83, s83, s81
s_add_u32 s83, s83, 131072
Mvaa 160,140,12
s_add_u32 s64, s64, 1
v_lshrrev_b32 v246, 9, v1
v_mul_u32_u24 v246, 10240, v246
Mvaa 164,140,28
Ra 128,242,24576
v_and_b32 v247, 0x1f0, v1
Mvaa 164,136,24
X 189
Mvaa 168,136,40
Ra 132,242,25600
v_min_u32 v247, 304, v247
Mvaa 168,140,44
X 190
Mvaa 172,140,60
v_lshl_add_u32 v246, v247, 3, v246
K 218,188,189
Mvaa 172,136,56
W 2
buffer_load_dword v3, v246, s[8:11], s83 offen sc1
Ra 136,242,26624
Cvaa 176,144,0,224
X 191
Mvaa 176,148,4
X 160
Cvaa 180,148,20,228
Ra 140,242,27648
K 219,190,191
Mvaa 180,144,16
X 161
Cvaa 184,144,32,232
X 162
Mvaa 184,148,36
K 196,160,161
Cvaa 188,148,52,236
X 163
Mvaa 188,144,48
X 164
Mvaa 176,152,8
Ra 144,242,28672
K 197,162,163
Mvaa 176,156,12
X 165
Mvaa 180,156,28
Ra 148,242,29696
Mvaa 180,152,24
X 166
Mvaa 184,152,40
K 204,164,165
Mvaa 184,156,44
X 167
Mvaa 188,156,60
X 168
Mvaa 188,152,56
W 2
Ra 152,242,30720
K 205,166,167
Mvav 32,128,192
X 169
Mvav 64,128,200
Ra 156,242,31744
v_xor_b32 v242, 0x10000, v242
Mvav 96,128,208
X 170
Mvav 128,128,216
K 212,168,169
Mvav 132,132,216
X 171
Mvav 100,132,208
X 172
Mvav 68,132,200
Mvav 36,132,192
K 213,170,171
Mvav 40,136,192
X 173
Mvav 72,136,200
X 174
Mvav 104,136,208
Mvav 136,136,216
K 220,172,173
Mvav 140,140,216
X 175
Mvav 108,140,208
X 176
Mvav 76,140,200
s_waitcnt vmcnt(2) lgkmcnt(0)
s_barrier
Mvav 44,140,192
Ra 128,242,0
K 221,174,175
Mvav 48,144,192
X 177
Mvav 80,144,200
X 178
Mvav 112,144,208
Ra 132,242,1024
Mvav 144,144,216
K 198,176,177
Mvav 148,148,216
X 179
Mvav 116,148,208
X 180
Mvav 84,148,200
Ra 136,242,2048
Mvav 52,148,192
K 199,178,179
Mvav 56,152,192
X 181
Mvav 88,152,200
)"
R"(Ra 140,242,3072
Mvav 120,152,208
X 182
Mvav 152,152,216
K 206,180,181
Mvav 156,156,216
X 183
Mvav 124,156,208
Mvav 92,156,200
K 207,182,183
Mvav 60,156,192
W 0
Cvaa 160,128,0,224
Ra 144,242,4096
Ra 148,242,5120
Mvaa 160,132,4
s_add_u32 m0, s62, 0
Ra 152,242,6144
G 8,64
Cvaa 164,132,20,228
X 184
Mvaa 164,128,16
X 185
Cvaa 168,128,32,232
X 186
Mvaa 168,132,36
G 9,65
K 214,184,185
Cvaa 172,132,52,236
Ra 156,242,7168
Dc 1,8,61,0
Mvaa 172,128,48
X 187
Mvaa 160,136,8
X 188
Mvaa 160,140,12
Ra 128,242,16384
G 10,66
Mvaa 164,140,28
Dc 1,8,61,1024
K 215,186,187
Mvaa 164,136,24
X 189
Mvaa 168,136,40
Ra 132,242,17408
G 11,67
Mvaa 168,140,44
X 190
Mvaa 172,140,60
Dc 1,8,61,2048
K 222,188,189
Mvaa 172,136,56
X 191
v_smfmac_f32_16x16x64_bf16 a[160:163], a[176:179], v[192:199], v240
G 12,68
K 223,190,191
v_smfmac_f32_16x16x64_bf16 a[164:167], a[176:179], v[200:207], v240
Ra 136,242,18432
v_smfmac_f32_16x16x64_bf16 a[168:171], a[176:179], v[208:215], v240
Dc 1,8,61,3072
G 13,69
v_smfmac_f32_16x16x64_bf16 a[172:175], a[176:179], v[216:223], v240
W 3
X 160
Cvaa 176,144,0,224
s_add_u32 m0, s62, 4096
X 161
Mvaa 176,148,4
X 162
Cvaa 180,148,20,228
Ra 140,242,19456
K 192,160,161
Mvaa 180,144,16
Dc 243,8,61,0
G 14,70
Cvaa 184,144,32,232
X 163
Mvaa 184,148,36
X 164
Cvaa 188,148,52,236
G 15,71
K 193,162,163
Mvaa 188,144,48
Dc 243,8,61,1024
Ra 144,242,20480
Mvaa 176,152,8
X 165
Mvaa 176,156,12
X 166
Mvaa 180,156,28
Ra 148,242,21504
A 8,8,12
Mvaa 180,152,24
Dc 243,8,61,2048
K 200,164,165
Mvaa 184,152,40
X 167
Mvaa 184,156,44
A 9,9,13
Mvaa 188,156,60
X 168
Mvaa 188,152,56
W 2
Dc 243,8,61,3072
K 201,166,167
Mvav 32,128,196
s_add_u32 m0, s62, 8192
X 169
Mvav 64,128,204
Ra 152,242,22528
A 10,10,14
Mvav 96,128,212
X 170
Mvav 128,128,220
Ra 156,242,23552
K 208,168,169
Mvav 132,132,220
Dc 244,8,61,0
A 11,11,15
Mvav 100,132,212
X 171
Mvav 68,132,204
X 172
Mvav 36,132,196
Ra 128,242,8192
K 209,170,171
Mvav 40,136,196
Dc 244,8,61,1024
G 12,72
Mvav 72,136,204
X 173
Mvav 104,136,212
X 174
Mvav 136,136,220
Ra 132,242,9216
G 13,73
Mvav 140,140,220
Dc 244,8,61,2048
K 216,172,173
Mvav 108,140,212
X 175
Mvav 76,140,204
X 176
Mvav 44,140,196
W 2
Ra 136,242,10240
G 14,74
Mvav 48,144,196
Dc 244,8,61,3072
K 217,174,175
Mvav 80,144,204
s_add_u32 m0, s62, 12288
X 177
Mvav 112,144,212
Ra 140,242,11264
G 15,75
Mvav 144,144,220
X 178
Mvav 148,148,220
K 194,176,177
A 8,8,12
Mvav 116,148,212
X 179
Mvav 84,148,204
X 180
Mvav 52,148,196
Dc 245,8,61,0
Ra 144,242,12288
Mvav 56,152,196
K 195,178,179
A 9,9,13
Mvav 88,152,204
X 181
Mvav 120,152,212
X 182
Mvav 152,152,220
Dc 245,8,61,1024
Ra 148,242,13312
Mvav 156,156,220
K 202,180,181
A 10,10,14
Mvav 124,156,212
X 183
Mvav 92,156,204
X 184
Mvav 60,156,196
W 2
Dc 245,8,61,2048
Ra 152,242,14336
Cvaa 160,128,0,224
.Lh_4_2_1%=:
s_cbranch_execz .Lh_4_2_2%=
A 11,11,15
K 203,182,183
Mvaa 160,132,4
X 185
Cvaa 164,132,20,228
X 186
Mvaa 164,128,16
Dc 245,8,61,3072
Ra 156,242,15360
Cvaa 168,128,32,232
s_add_u32 s61, s61, s63
G 12,76
K 210,184,185
Mvaa 168,132,36
X 187
Cvaa 172,132,52,236
s_xor_b32 s62, s62, 0x10000
G 13,77
Mvaa 172,128,48
X 188
Mvaa 160,136,8
K 211,186,187
G 14,78
Mvaa 160,140,12
s_add_u32 s64, s64, 1
X 189
Mvaa 164,140,28
Ra 128,242,24576
Ra 132,242,25600
Mvaa 164,136,24
X 190
Mvaa 168,136,40
K 218,188,189
G 15,79
Mvaa 168,140,44
X 191
Mvaa 172,140,60
s_lshl_b32 s92, s58, 2
X 160
Mvaa 172,136,56
W 2
K 219,190,191
A 8,8,12
Cvaa 176,144,0,224
X 161
Mvaa 176,148,4
Ra 136,242,26624
Ra 140,242,27648
Cvaa 180,148,20,228
s_add_u32 s91, s92, s51
X 162
Mvaa 180,144,16
A 9,9,13
K 196,160,161
Cvaa 184,144,32,232
X 163
Mvaa 184,148,36
s_add_u32 s95, s92, 0
X 164
Cvaa 188,148,52,236
A 10,10,14
K 197,162,163
Mvaa 188,144,48
X 165
Mvaa 176,152,8
s_and_b32 s95, s95, 15
Ra 144,242,28672
A 11,11,15
Mvaa 176,156,12
Ra 148,242,29696
Mvaa 180,156,28
X 166
Mvaa 180,152,24
s_mul_i32 s95, s95, 40960
K 204,164,165
Mvaa 184,152,40
X 167
Mvaa 184,156,44
G 12,80
Mvaa 188,156,60
s_mul_i32 s71, s51, 0
X 168
Mvaa 188,152,56
W 2
K 205,166,167
G 13,81
Mvav 32,128,192
X 169
Mvav 64,128,200
s_add_u32 s71, s71, 133120
Ra 152,242,30720
Ra 156,242,31744
Mvav 96,128,208
X 170
Mvav 128,128,216
v_xor_b32 v242, 0x10000, v242
G 14,82
Mvav 132,132,216
s_add_u32 m0, s71, s48
K 212,168,169
Mvav 100,132,208
X 171
Mvav 68,132,200
X 172
Mvav 36,132,192
s_lshl2_add_u32 s94, s48, s95
G 15,83
K 213,170,171
Mvav 40,136,192
X 173
Mvav 72,136,200
X 174
Mvav 104,136,208
D 16,40,94,0
A 8,8,12
Mvav 136,136,216
K 220,172,173
Mvav 140,140,216
X 175
Mvav 108,140,208
A 9,9,13
Mvav 76,140,200
s_waitcnt vmcnt(1) lgkmcnt(0)
s_barrier
Mvav 44,140,192
D 17,40,94,1024
Ra 128,242,0
Mvav 48,144,192
X 176
Mvav 80,144,200
K 221,174,175
A 10,10,14
Mvav 112,144,208
X 177
Mvav 144,144,216
D 18,40,94,2048
Ra 132,242,1024
Mvav 148,148,216
X 178
Mvav 116,148,208
K 198,176,177
A 11,11,15
Mvav 84,148,200
X 179
Mvav 52,148,192
D 19,40,94,3072
Ra 136,242,2048
Mvav 56,152,192
s_add_u32 m0, m0, 4096
X 180
Mvav 88,152,200
G 12,84
K 199,178,179
Mvav 120,152,208
X 181
Mvav 152,152,216
Ra 140,242,3072
buffer_load_dwordx4 v20, s[40:43], s94 offen lds
Mvav 156,156,216
X 182
Mvav 124,156,208
K 206,180,181
Mvav 92,156,200
X 183
Mvav 60,156,192
K 207,182,183
W 0
Cvaa 160,128,0,224
Ra 144,242,4096
Ra 148,242,5120
Mvaa 160,132,4
s_add_u32 m0, s62, 0
Ra 152,242,6144
G 13,85
Cvaa 164,132,20,228
X 184
Mvaa 164,128,16
X 185
Cvaa 168,128,32,232
X 186
Mvaa 168,132,36
G 14,86
K 214,184,185
Cvaa 172,132,52,236
Ra 156,242,7168
Dc 1,8,61,0
Mvaa 172,128,48
X 187
Mvaa 160,136,8
X 188
Mvaa 160,140,12
Ra 128,242,16384
G 15,87
Mvaa 164,140,28
Dc 1,8,61,1024
K 215,186,187
Mvaa 164,136,24
X 189
Mvaa 168,136,40
Ra 132,242,17408
A 8,8,12
Mvaa 168,140,44
X 190
Mvaa 172,140,60
Dc 1,8,61,2048
K 222,188,189
Mvaa 172,136,56
X 191
)"
R"(v_smfmac_f32_16x16x64_bf16 a[160:163], a[176:179], v[192:199], v240
A 9,9,13
K 223,190,191
v_smfmac_f32_16x16x64_bf16 a[164:167], a[176:179], v[200:207], v240
Ra 136,242,18432
A 10,10,14
v_smfmac_f32_16x16x64_bf16 a[168:171], a[176:179], v[208:215], v240
Dc 1,8,61,3072
v_smfmac_f32_16x16x64_bf16 a[172:175], a[176:179], v[216:223], v240
W 3
X 160
Cvaa 176,144,0,224
s_add_u32 m0, s62, 4096
X 161
Mvaa 176,148,4
X 162
Cvaa 180,148,20,228
Ra 140,242,19456
A 11,11,15
Mvaa 180,144,16
Dc 243,8,61,0
K 192,160,161
Cvaa 184,144,32,232
X 163
Mvaa 184,148,36
X 164
Cvaa 188,148,52,236
G 12,88
K 193,162,163
Mvaa 188,144,48
Dc 243,8,61,1024
G 13,89
Mvaa 176,152,8
X 165
Mvaa 176,156,12
X 166
Mvaa 180,156,28
Ra 144,242,20480
Ra 148,242,21504
Mvaa 180,152,24
Dc 243,8,61,2048
K 200,164,165
Mvaa 184,152,40
X 167
Mvaa 184,156,44
G 14,90
Mvaa 188,156,60
X 168
Mvaa 188,152,56
W 2
Dc 243,8,61,3072
K 201,166,167
Mvav 32,128,196
s_add_u32 m0, s62, 8192
Ra 152,242,22528
G 15,91
Mvav 64,128,204
X 169
Mvav 96,128,212
X 170
Mvav 128,128,220
Ra 156,242,23552
A 8,8,12
Mvav 132,132,220
Dc 244,8,61,0
K 208,168,169
Mvav 100,132,212
X 171
Mvav 68,132,204
X 172
Mvav 36,132,196
Ra 128,242,8192
A 9,9,13
Mvav 40,136,196
Dc 244,8,61,1024
K 209,170,171
Mvav 72,136,204
X 173
Mvav 104,136,212
Ra 132,242,9216
A 10,10,14
Mvav 136,136,220
X 174
Mvav 140,140,220
Dc 244,8,61,2048
K 216,172,173
Mvav 108,140,212
X 175
Mvav 76,140,204
Ra 136,242,10240
A 11,11,15
Mvav 44,140,196
W 3
X 176
Mvav 48,144,196
Dc 244,8,61,3072
K 217,174,175
Mvav 80,144,204
s_add_u32 m0, s62, 12288
Ra 140,242,11264
v_add_f32_dpp v8, v8, v8 row_ror:8 row_mask:0xf bank_mask:0xf
Mvav 112,144,212
X 177
Mvav 144,144,220
X 178
Mvav 148,148,220
K 194,176,177
v_add_f32_dpp v9, v9, v9 row_ror:8 row_mask:0xf bank_mask:0xf
Mvav 116,148,212
X 179
Mvav 84,148,204
X 180
Mvav 52,148,196
Dc 245,8,61,0
Ra 144,242,12288
Mvav 56,152,196
v_add_f32_dpp v10, v10, v10 row_ror:8 row_mask:0xf bank_mask:0xf
K 195,178,179
Mvav 88,152,204
X 181
Mvav 120,152,212
X 182
Mvav 152,152,220
Dc 245,8,61,1024
v_add_f32_dpp v11, v11, v11 row_ror:8 row_mask:0xf bank_mask:0xf
Mvav 156,156,220
Ra 148,242,13312
K 202,180,181
Mvav 124,156,212
X 183
Mvav 92,156,204
Ra 152,242,14336
v_mov_b32 v12, v8
Mvav 60,156,196
W 3
Dc 245,8,61,2048
v_mov_b32 v13, v9
Cvaa 160,128,0,224
X 184
Mvaa 160,132,4
Ra 156,242,15360
K 203,182,183
Cvaa 164,132,20,228
X 185
Mvaa 164,128,16
Dc 245,8,61,3072
v_mov_b32 v14, v10
Cvaa 168,128,32,232
s_add_u32 s61, s61, s63
X 186
Mvaa 168,132,36
K 210,184,185
v_mov_b32 v15, v11
Cvaa 172,132,52,236
s_xor_b32 s62, s62, 0x10000
X 187
Mvaa 172,128,48
X 188
Mvaa 160,136,8
Ra 128,242,24576
K 211,186,187
Mvaa 160,140,12
s_add_u32 s64, s64, 1
X 189
Mvaa 164,140,28
X 190
Mvaa 164,136,24
Ra 132,242,25600
v_permlane16_swap_b32 v8, v12
Mvaa 168,136,40
K 218,188,189
v_permlane16_swap_b32 v9, v13
Mvaa 168,140,44
X 191
Mvaa 172,140,60
s_lshl_b32 s92, s58, 2
X 160
Mvaa 172,136,56
W 2
K 219,190,191
v_permlane16_swap_b32 v10, v14
Cvaa 176,144,0,224
X 161
Mvaa 176,148,4
Ra 136,242,26624
Ra 140,242,27648
Cvaa 180,148,20,228
s_add_u32 s91, s92, s51
X 162
Mvaa 180,144,16
K 196,160,161
v_permlane16_swap_b32 v11, v15
Cvaa 184,144,32,232
X 163
Mvaa 184,148,36
s_add_u32 s95, s92, 1
X 164
Cvaa 188,148,52,236
A 8,8,12
K 197,162,163
Mvaa 188,144,48
X 165
Mvaa 176,152,8
s_and_b32 s95, s95, 15
Ra 144,242,28672
Ra 148,242,29696
Mvaa 176,156,12
X 166
Mvaa 180,156,28
A 9,9,13
K 204,164,165
Mvaa 180,152,24
s_mul_i32 s95, s95, 40960
X 167
Mvaa 184,152,40
Mvaa 184,156,44
A 10,10,14
Mvaa 188,156,60
s_mul_i32 s71, s51, 0
X 168
Mvaa 188,152,56
W 2
Ra 152,242,30720
K 205,166,167
Mvav 32,128,192
X 169
Mvav 64,128,200
s_add_u32 s71, s71, 143360
Ra 156,242,31744
A 11,11,15
Mvav 96,128,208
X 170
Mvav 128,128,216
v_xor_b32 v242, 0x10000, v242
K 212,168,169
Mvav 132,132,216
s_add_u32 m0, s71, s48
v_mov_b32 v12, v8
Mvav 100,132,208
X 171
Mvav 68,132,200
X 172
Mvav 36,132,192
s_lshl2_add_u32 s94, s48, s95
v_mov_b32 v13, v9
K 213,170,171
Mvav 40,136,192
X 173
Mvav 72,136,200
X 174
Mvav 104,136,208
D 16,40,94,0
v_mov_b32 v14, v10
Mvav 136,136,216
K 220,172,173
Mvav 140,140,216
X 175
Mvav 108,140,208
v_mov_b32 v15, v11
Mvav 76,140,200
s_waitcnt vmcnt(1) lgkmcnt(0)
s_barrier
Mvav 44,140,192
D 17,40,94,1024
Ra 128,242,0
Mvav 48,144,192
X 176
Mvav 80,144,200
K 221,174,175
v_permlane32_swap_b32 v8, v12
Mvav 112,144,208
X 177
Mvav 144,144,216
D 18,40,94,2048
Ra 132,242,1024
Mvav 148,148,216
X 178
Mvav 116,148,208
K 198,176,177
v_permlane32_swap_b32 v9, v13
Mvav 84,148,200
X 179
Mvav 52,148,192
D 19,40,94,3072
Ra 136,242,2048
Mvav 56,152,192
s_add_u32 m0, m0, 4096
X 180
Mvav 88,152,200
K 199,178,179
v_permlane32_swap_b32 v10, v14
Mvav 120,152,208
X 181
Mvav 152,152,216
Ra 140,242,3072
buffer_load_dwordx4 v20, s[40:43], s94 offen lds
Mvav 156,156,216
X 182
Mvav 124,156,208
K 206,180,181
Mvav 92,156,200
X 183
Mvav 60,156,192
K 207,182,183
W 0
Cvaa 160,128,0,224
Ra 144,242,4096
Ra 148,242,5120
Mvaa 160,132,4
s_add_u32 m0, s62, 0
Ra 152,242,6144
v_permlane32_swap_b32 v11, v15
Cvaa 164,132,20,228
X 184
Mvaa 164,128,16
X 185
Cvaa 168,128,32,232
X 186
Mvaa 168,132,36
A 8,8,12
K 214,184,185
Cvaa 172,132,52,236
Ra 156,242,7168
Dc 1,8,61,0
Mvaa 172,128,48
X 187
Mvaa 160,136,8
Ra 128,242,16384
A 9,9,13
Mvaa 160,140,12
X 188
Mvaa 164,140,28
Dc 1,8,61,1024
K 215,186,187
Mvaa 164,136,24
X 189
Mvaa 168,136,40
Ra 132,242,17408
A 10,10,14
Mvaa 168,140,44
X 190
Mvaa 172,140,60
Dc 1,8,61,2048
K 222,188,189
Mvaa 172,136,56
A 11,11,15
v_smfmac_f32_16x16x64_bf16 a[160:163], a[176:179], v[192:199], v240
X 191
v_smfmac_f32_16x16x64_bf16 a[164:167], a[176:179], v[200:207], v240
K 223,190,191
v_and_b32 v7, 0xffff0000, v7
v_smfmac_f32_16x16x64_bf16 a[168:171], a[176:179], v[208:215], v240
Dc 1,8,61,3072
Ra 136,242,18432
v_smfmac_f32_16x16x64_bf16 a[172:175], a[176:179], v[216:223], v240
W 3
X 160
Cvaa 176,144,0,224
s_add_u32 m0, s62, 4096
X 161
Mvaa 176,148,4
X 162
Cvaa 180,148,20,228
Ra 140,242,19456
v_ashrrev_i32 v6, 31, v7
Mvaa 180,144,16
Dc 243,8,61,0
K 192,160,161
Cvaa 184,144,32,232
X 163
Mvaa 184,148,36
v_not_b32 v6, v6
Cvaa 188,148,52,236
X 164
Mvaa 188,144,48
Dc 243,8,61,1024
K 193,162,163
Mvaa 176,152,8
X 165
Mvaa 176,156,12
Ra 144,242,20480
v_or_b32 v6, 0x80000000, v6
Mvaa 180,156,28
X 166
Mvaa 180,152,24
Ra 148,242,21504
v_xor_b32 v7, v7, v6
Mvaa 184,152,40
Dc 243,8,61,2048
K 200,164,165
Mvaa 184,156,44
X 167
Mvaa 188,156,60
X 168
Mvaa 188,152,56
W 2
v_and_b32 v7, 0xffff0000, v7
K 201,166,167
Mvav 32,128,196
.Lh_4_3_1%=:
)"
R"(s_cbranch_execz .Lhr_76%=
Dc 243,8,61,3072
v_mul_f32 v7, 0xbfb8aa3b, v7
Mvav 64,128,204
s_add_u32 m0, s62, 8192
X 169
Mvav 96,128,212
Ra 152,242,22528
Ra 156,242,23552
Mvav 128,128,220
X 170
Mvav 132,132,220
Dc 244,8,61,0
K 208,168,169
Mvav 100,132,212
X 171
Mvav 68,132,204
X 7
Mvav 36,132,196
X 172
Mvav 40,136,196
Ra 128,242,8192
K 209,170,171
Mvav 72,136,204
X 173
Mvav 104,136,212
Dc 244,8,61,1024
Ra 132,242,9216
Mvav 136,136,220
X 174
Mvav 140,140,220
v_add_f32 v7, 1.0, v7
K 216,172,173
Mvav 108,140,212
X 175
Mvav 76,140,204
Dc 244,8,61,2048
Ra 136,242,10240
Mvav 44,140,196
W 3
v_rcp_f32 v7, v7
Mvav 48,144,196
X 176
Mvav 80,144,204
Ra 140,242,11264
K 217,174,175
Mvav 112,144,212
Dc 244,8,61,3072
K 7,7,7
Mvav 144,144,220
s_add_u32 m0, s62, 12288
X 177
Mvav 148,148,220
X 178
Mvav 116,148,212
Ra 144,242,12288
K 194,176,177
Mvav 84,148,204
X 179
Mvav 52,148,196
X 180
Mvav 56,152,196
Dc 245,8,61,0
v_lshlrev_b32 v7, 16, v7
Mvav 88,152,204
K 195,178,179
K 12,8,9
Mvav 120,152,212
X 181
Mvav 152,152,220
X 182
Mvav 156,156,220
Dc 245,8,61,1024
Ra 148,242,13312
Mvav 124,156,212
K 202,180,181
K 13,10,11
Mvav 92,156,204
X 183
Mvav 60,156,196
W 2
X 184
Cvaa 160,128,0,224
Dc 245,8,61,2048
Ra 152,242,14336
Mvaa 160,132,4
K 203,182,183
v_lshlrev_b32 v8, 16, v12
Cvaa 164,132,20,228
X 185
Mvaa 164,128,16
X 186
Cvaa 168,128,32,232
Dc 245,8,61,3072
Ra 156,242,15360
Mvaa 168,132,36
s_add_u32 s61, s61, s63
v_and_b32 v9, 0xffff0000, v12
K 210,184,185
Cvaa 172,132,52,236
s_xor_b32 s62, s62, 0x10000
X 187
Mvaa 172,128,48
X 188
Mvaa 160,136,8
v_lshlrev_b32 v10, 16, v13
K 211,186,187
Mvaa 160,140,12
s_add_u32 s64, s64, 1
X 189
Mvaa 164,140,28
Ra 128,242,24576
v_and_b32 v11, 0xffff0000, v13
Mvaa 164,136,24
X 190
Mvaa 168,136,40
Ra 132,242,25600
K 218,188,189
Mvaa 168,140,44
X 191
Mvaa 172,140,60
s_lshl_b32 s92, s58, 2
U 8,8,7
U 9,9,7
Mvaa 172,136,56
W 2
X 160
Cvaa 176,144,0,224
Ra 136,242,26624
K 219,190,191
Mvaa 176,148,4
X 161
Cvaa 180,148,20,228
s_add_u32 s91, s92, s51
Ra 140,242,27648
U 10,10,7
Mvaa 180,144,16
X 162
Cvaa 184,144,32,232
K 196,160,161
Mvaa 184,148,36
s_add_u32 s95, s92, 2
X 163
Cvaa 188,148,52,236
U 11,11,7
Mvaa 188,144,48
X 164
Mvaa 176,152,8
s_and_b32 s95, s95, 15
Ra 144,242,28672
K 197,162,163
Mvaa 176,156,12
X 165
Mvaa 180,156,28
Ra 148,242,29696
K 12,8,9
Mvaa 180,152,24
s_mul_i32 s95, s95, 40960
X 166
Mvaa 184,152,40
K 204,164,165
K 14,10,11
Mvaa 184,156,44
X 167
Mvaa 188,156,60
s_mul_i32 s71, s51, 0
X 168
Mvaa 188,152,56
W 2
v_mov_b32 v13, s13
K 205,166,167
Mvav 32,128,192
X 169
Mvav 64,128,200
s_add_u32 s71, s71, 153600
Ra 152,242,30720
Ra 156,242,31744
Mvav 96,128,208
X 170
Mvav 128,128,216
v_mov_b32 v15, s13
v_xor_b32 v242, 0x10000, v242
Mvav 132,132,216
s_add_u32 m0, s71, s48
K 212,168,169
v_lshrrev_b32 v6, 7, v1
Mvav 100,132,208
X 171
Mvav 68,132,200
X 172
Mvav 36,132,192
s_lshl2_add_u32 s94, s48, s95
K 213,170,171
Mvav 40,136,192
X 173
Mvav 72,136,200
v_lshl_or_b32 v6, v6, 28, v1
Mvav 104,136,208
X 174
Mvav 136,136,216
s_lshl_b32 s19, s85, 7
D 16,40,94,0
K 220,172,173
Mvav 140,140,216
X 175
Mvav 108,140,208
X 176
Mvav 76,140,200
s_waitcnt vmcnt(1) lgkmcnt(0)
s_barrier
Mvav 44,140,192
s_add_u32 s19, s19, 98304
D 17,40,94,1024
K 221,174,175
Mvav 48,144,192
Ra 128,242,0
Mvav 80,144,200
s_and_b32 s21, s20, 134217728
X 177
Mvav 112,144,208
X 178
Mvav 144,144,216
D 18,40,94,2048
Ra 132,242,1024
Mvav 148,148,216
s_or_b32 s19, s19, s21
K 198,176,177
Mvav 116,148,208
X 179
Mvav 84,148,200
X 180
Mvav 52,148,192
D 19,40,94,3072
Ra 136,242,2048
Mvav 56,152,192
s_add_u32 m0, m0, 4096
Ra 140,242,3072
K 199,178,179
Mvav 88,152,200
X 181
Mvav 120,152,208
X 182
Mvav 152,152,216
buffer_store_dwordx4 v[12:15], v6, s[8:11], s19 offen sc1
K 206,180,181
Mvav 156,156,216
X 183
Mvav 124,156,208
Mvav 92,156,200
K 207,182,183
Mvav 60,156,192
buffer_load_dwordx4 v20, s[40:43], s94 offen lds
.Lf1_36%=:
W 0
Cvaa 160,128,0,224
v_bfe_u32 v28, v1, 4, 2
v_lshlrev_b32 v28, 3, v28
Mvaa 160,132,4
Ra 144,242,4096
Ra 148,242,5120
Cvaa 164,132,20,228
Ra 152,242,6144
buffer_load_dwordx2 v[28:29], v28, s[8:11], s54 offen sc1
Mvaa 164,128,16
s_lshl_b32 s92, s58, 2
s_add_u32 s91, s92, s51
s_add_u32 s95, s91, 3
Cvaa 168,128,32,232
s_and_b32 s95, s95, 15
s_mul_i32 s95, s95, 40960
s_mul_i32 s71, s51, 10240
Mvaa 168,132,36
s_add_u32 s71, s71, 0
s_add_u32 m0, s71, s48
s_lshl2_add_u32 s94, s48, s95
Cvaa 172,132,52,236
X 184
Mvaa 172,128,48
X 185
Mvaa 160,136,8
X 186
Mvaa 160,140,12
Ra 156,242,7168
K 214,184,185
Mvaa 164,140,28
X 187
Mvaa 164,136,24
D 16,40,94,0
Ra 128,242,16384
Mvaa 168,136,40
s_add_u32 s95, s91, 5
s_and_b32 s95, s95, 15
s_mul_i32 s95, s95, 40960
Mvaa 168,140,44
s_mul_i32 s71, s51, 10240
s_add_u32 s71, s71, 20480
Ra 132,242,17408
Mvaa 172,140,60
X 188
Mvaa 172,136,56
D 17,40,94,1024
K 215,186,187
v_smfmac_f32_16x16x64_bf16 a[160:163], a[176:179], v[192:199], v240
X 189
v_smfmac_f32_16x16x64_bf16 a[164:167], a[176:179], v[200:207], v240
X 190
K 222,188,189
X 191
s_nop 0
K 223,190,191
v_smfmac_f32_16x16x64_bf16 a[168:171], a[176:179], v[208:215], v240
D 18,40,94,2048
Ra 136,242,18432
v_smfmac_f32_16x16x64_bf16 a[172:175], a[176:179], v[216:223], v240
W 3
X 160
Cvaa 176,144,0,224
X 161
Mvaa 176,148,4
X 162
Cvaa 180,148,20,228
D 19,40,94,3072
K 192,160,161
Mvaa 180,144,16
s_add_u32 m0, m0, 4096
X 163
Cvaa 184,144,32,232
X 164
Mvaa 184,148,36
Ra 140,242,19456
K 193,162,163
Cvaa 188,148,52,236
buffer_load_dwordx4 v20, s[40:43], s94 offen lds
Mvaa 188,144,48
s_add_u32 m0, s71, s48
s_lshl2_add_u32 s94, s48, s95
Ra 144,242,20480
Mvaa 176,152,8
X 165
Mvaa 176,156,12
X 166
Mvaa 180,156,28
D 16,40,94,0
K 200,164,165
Mvaa 180,152,24
X 167
Mvaa 184,152,40
X 172
Mvaa 184,156,44
D 17,40,94,1024
K 201,166,167
Mvaa 188,156,60
X 173
Mvaa 188,152,56
s_cmp_eq_u32 s60, 16
s_cbranch_scc1 .Lmk_77%=
.Lmkr_78%=:
W 1
Mvav 32,128,196
Ra 148,242,21504
D 18,40,94,2048
Mvav 64,128,204
X 174
Mvav 96,128,212
Ra 152,242,22528
K 216,172,173
Mvav 128,128,220
X 175
Mvav 132,132,220
s_mov_b32 s18, 40960
X 168
Mvav 100,132,212
D 19,40,94,3072
K 217,174,175
Mvav 68,132,204
s_add_u32 m0, m0, 4096
X 169
Mvav 36,132,196
s_mov_b32 s19, 73728
X 170
Mvav 40,136,196
s_bitcmp1_b32 s14, 0
s_cselect_b32 s18, 49152, s18
s_cselect_b32 s19, 98304, s19
Ra 156,242,23552
K 208,168,169
Mvav 72,136,204
s_cmp_lt_u32 s14, 2
s_cselect_b32 s20, s18, s19
s_cselect_b32 s21, s19, s18
X 171
Mvav 104,136,212
X 184
Mvav 136,136,220
buffer_load_dwordx4 v20, s[40:43], s94 offen lds
K 209,170,171
Mvav 140,140,220
s_waitcnt vmcnt(10)
v_cmp_eq_u64 vcc, s[12:13], v[28:29]
s_cmp_eq_u64 vcc, -1
)"
R"(s_cselect_b32 s46, 79691776, 0
s_add_u32 s81, s58, 0
s_and_b32 s81, s81, 3
Mvav 108,140,212
s_lshl_b32 s81, s81, 12
s_add_u32 s81, s81, s49
Ra 128,242,24576
Mvav 76,140,204
X 185
Mvav 44,140,196
W 1
X 186
Mvav 48,144,196
buffer_load_dwordx4 a[184:187], v1, s[44:47], s81 offen offset:0 sc1
K 210,184,185
Mvav 80,144,204
s_add_u32 s82, s81, 131072
X 187
Mvav 112,144,212
v_add_u32 v2, s20, v1
v_add_u32 v3, s21, v1
Mvav 144,144,220
s_lshl_b32 s18, s14, 9
X 188
Mvav 148,148,220
Ra 132,242,25600
K 211,186,187
Mvav 116,148,212
buffer_load_dwordx4 a[200:203], v1, s[44:47], s82 offen offset:0 sc1
Ra 136,242,26624
Mvav 84,148,204
X 189
Mvav 52,148,196
v_lshrrev_b32 v232, 1, v1
v_add_u32 v232, s18, v232
Mvav 56,152,196
X 190
Mvav 88,152,204
K 218,188,189
v_bfe_u32 v12, v1, 6, 2
Mvav 120,152,212
buffer_load_dwordx4 a[188:191], v1, s[44:47], s81 offen offset:1024 sc1
Ra 140,242,27648
Mvav 152,152,220
X 191
Mvav 156,156,220
v_lshrrev_b32 v13, 8, v1
v_bfe_u32 v14, v1, 4, 1
Mvav 124,156,212
X 176
Mvav 92,156,204
K 219,190,191
v_lshlrev_b32 v14, 4, v14
Mvav 60,156,196
X 177
Maav 168,180,208
X 178
K 194,176,177
X 179
X 180
K 195,178,179
Maav 172,180,216
X 181
X 182
K 202,180,181
X 183
s_nop 0
K 203,182,183
Maav 160,180,192
buffer_load_dwordx4 a[204:207], v1, s[44:47], s82 offen offset:1024 sc1
v_mov_b32 v15, 0x3f80
Maav 164,180,200
W 0
Ra 144,242,28672
v_lshlrev_b32 v15, v14, v15
Mvav 96,128,208
Ra 148,242,29696
v_bfe_u32 v14, v1, 5, 1
v_xor_b32 v27, v12, v13
Mvav 128,128,216
buffer_load_dwordx4 a[192:195], v1, s[44:47], s81 offen offset:2048 sc1
v_lshl_or_b32 v27, v27, 1, v14
v_cmp_eq_u32_e64 s[86:87], 0, v27
Mvav 132,132,216
Ra 152,242,30720
Ra 156,242,31744
v_mov_b32 v226, 0
Mvav 100,132,208
v_cndmask_b32_e64 v224, 0, v15, s[86:87]
v_mov_b32 v228, 0
v_mov_b32 v230, v224
Mvav 104,136,208
buffer_load_dwordx4 a[208:211], v1, s[44:47], s82 offen offset:2048 sc1
v_cmp_eq_u32_e64 s[86:87], 1, v27
v_mov_b32 v227, 0
Mvav 136,136,216
v_cndmask_b32_e64 v225, 0, v15, s[86:87]
v_mov_b32 v229, 0
v_mov_b32 v231, v225
Mvav 140,140,216
G 4,160
G 8,161
G 5,164
Mvav 108,140,208
W 0
buffer_load_dwordx4 a[196:199], v1, s[44:47], s81 offen offset:3072 sc1
G 9,165
G 6,168
Mvav 112,144,208
G 10,169
G 7,172
G 11,173
Mvav 144,144,216
A 4,4,8
A 5,5,9
A 6,6,10
Mvav 148,148,216
buffer_load_dwordx4 a[212:215], v1, s[44:47], s82 offen offset:3072 sc1
s_add_u32 s81, s58, 1
s_and_b32 s81, s81, 3
s_lshl_b32 s81, s81, 12
Mvav 116,148,208
s_add_u32 s81, s81, s49
A 7,7,11
ds_write_b64 v232, v[6:7] offset:57344
A 8,4,5
Mvav 120,152,208
A 9,6,7
A 8,8,9
v_cmp_ngt_f32_e64 s[86:87], s90, v8
s_lshl_b32 s19, s14, 2
Mvav 152,152,216
buffer_load_dwordx4 a[216:219], v1, s[44:47], s81 offen offset:0 sc1
s_add_u32 s82, s81, 131072
v_mov_b32 v9, s19
s_or_b64 s[88:89], s[88:89], s[86:87]
Mvav 156,156,216
s_and_b64 s[88:89], s[88:89], s[24:25]
s_cselect_b32 s18, 1, 0
v_mov_b32 v8, s18
ds_write_b32 v9, v8 offset:59392
v_xor_b32 v10, 1024, v232
Mvav 124,156,208
v_bfe_u32 v11, v1, 2, 4
K 96,96,97
K 97,98,99
Mvav 32,128,192
buffer_load_dwordx4 a[232:235], v1, s[44:47], s82 offen offset:0 sc1
K 98,100,101
K 99,102,103
Mvav 64,128,200
ds_write_b128 v2, v[96:99] offset:0
K 128,128,129
K 129,130,131
Mvav 68,132,200
K 130,132,133
K 131,134,135
ds_write_b128 v2, v[128:131] offset:4096
Mvav 36,132,192
buffer_load_dwordx4 a[220:223], v1, s[44:47], s81 offen offset:1024 sc1
K 104,104,105
K 105,106,107
Mvav 40,136,192
K 106,108,109
K 107,110,111
ds_write_b128 v2, v[104:107] offset:1024
Mvav 72,136,200
K 136,136,137
K 137,138,139
K 138,140,141
Mvav 76,140,200
buffer_load_dwordx4 a[236:239], v1, s[44:47], s82 offen offset:1024 sc1
K 139,142,143
ds_write_b128 v2, v[136:139] offset:5120
Mvav 44,140,192
K 112,112,113
K 113,114,115
K 114,116,117
Mvav 48,144,192
K 115,118,119
ds_write_b128 v2, v[112:115] offset:2048
K 144,144,145
Mvav 80,144,200
K 145,146,147
K 146,148,149
K 147,150,151
Mvav 84,148,200
ds_write_b128 v2, v[144:147] offset:6144
buffer_load_dwordx4 a[224:227], v1, s[44:47], s81 offen offset:2048 sc1
K 120,120,121
Mvav 52,148,192
K 121,122,123
K 122,124,125
K 123,126,127
Mvav 56,152,192
ds_write_b128 v2, v[120:123] offset:3072
K 152,152,153
K 153,154,155
Mvav 88,152,200
buffer_load_dwordx4 a[240:243], v1, s[44:47], s82 offen offset:2048 sc1
K 154,156,157
K 155,158,159
Mvav 92,156,200
.Lh_4_2_2%=:
s_cbranch_execz .Lhr_76%=
ds_write_b128 v2, v[152:155] offset:7168
Mvav 60,156,192
buffer_load_dwordx4 a[228:231], v1, s[44:47], s81 offen offset:3072 sc1
buffer_load_dwordx4 a[244:247], v1, s[44:47], s82 offen offset:3072 sc1
s_branch .LL_79%=
.p2align 12
.LL_79%=:
.Lend_39%=:
s_waitcnt vmcnt(8) lgkmcnt(0)
s_barrier
s_cmp_eq_u32 s46, 0
s_cbranch_scc1 .Llate_80%=
ds_read_b32 v11, v11 offset:59392
ds_read_b64 v[8:9], v10 offset:57344
Rv 176,3,0
Rv 180,3,1024
Rv 184,3,2048
Rv 188,3,3072
Rv 192,3,4096
Rv 196,3,5120
Rv 200,3,6144
Rv 204,3,7168
W 6
Mvvv 32,224,176
s_lshl_b32 s92, s58, 2
s_add_u32 s91, s92, s51
s_add_u32 s95, s91, 7
Mvvv 36,228,176
s_and_b32 s95, s95, 15
s_mul_i32 s95, s95, 40960
s_mul_i32 s71, s51, 20480
Mvvv 40,224,180
s_add_u32 s71, s71, 61440
s_add_u32 m0, s71, s48
s_lshl2_add_u32 s94, s48, s95
Mvvv 44,228,180
W 4
v_cmp_ne_u32 vcc, 0, v11
s_cmp_lg_u64 vcc, 0
s_cbranch_scc1 .Lredo%=
A 4,4,8
Mvvv 48,224,184
v_rcp_f32 v4, v4
Mvvv 52,228,184
Ta 128,25,10240
A 5,5,9
Mvvv 56,224,188
v_rcp_f32 v5, v5
Mvvv 60,228,188
W 3
Ta 130,25,15360
D 16,40,94,0
Mvvv 64,224,192
s_add_u32 s95, s91, 9
s_and_b32 s95, s95, 15
s_mul_i32 s95, s95, 40960
Mvvv 68,228,192
s_mul_i32 s71, s51, 10240
s_add_u32 s71, s71, 112640
s_lshl_b32 s93, s58, 12
Mvvv 72,224,196
s_add_u32 s93, s93, s50
U 32,32,4
U 33,33,4
Mvvv 76,228,196
Ta 132,25,20480
Ta 134,25,25600
W 4
Mvvv 80,224,200
U 34,34,4
U 35,35,4
Mvvv 84,228,200
U 36,36,4
U 37,37,4
Mvvv 88,224,204
Ta 136,26,10272
Ta 138,26,15392
Mvvv 92,228,204
Ta 140,26,20512
Ta 142,26,25632
W 4
Zvaa 96,128,184
U 38,38,4
U 39,39,4
Mvaa 96,132,188
K 32,32,33
K 33,34,35
Zvaa 104,132,204
D 17,40,94,1024
K 34,36,37
Mvaa 104,128,200
Ta 144,25,10304
Ta 146,25,15424
Ta 148,25,20544
Ta 150,25,25664
W 4
Zvaa 108,136,200
Ta 152,26,10336
K 35,38,39
Mvaa 108,140,204
Ta 154,26,15456
Ta 156,26,20576
Zvaa 100,140,188
buffer_store_dwordx4 v[32:35], v1, s[8:11], s93 offen offset:0 sc1
U 40,40,4
Mvaa 100,136,184
Ta 158,26,25696
W 4
Zvaa 112,144,184
U 41,41,4
U 42,42,4
Mvaa 112,148,188
U 43,43,4
U 44,44,4
Zvaa 120,148,204
U 45,45,4
U 46,46,4
Mvaa 120,144,200
Ta 128,25,10368
Ta 130,25,15488
Ta 132,25,20608
Ta 134,25,25728
W 4
Zvaa 124,152,200
U 47,47,4
K 40,40,41
Mvaa 124,156,204
D 18,40,94,2048
K 41,42,43
Zvaa 116,156,188
K 42,44,45
K 43,46,47
Mvaa 116,152,184
Ta 136,26,10400
Ta 138,26,15520
Ta 140,26,20640
Ta 142,26,25760
W 4
Zvaa 128,128,184
Ta 144,25,10432
Ta 146,25,15552
Mvaa 128,132,188
buffer_store_dwordx4 v[40:43], v1, s[8:11], s93 offen offset:1024 sc1
U 48,48,4
Zvaa 136,132,204
U 49,49,4
U 50,50,4
Mvaa 136,128,200
Ta 148,25,20672
Ta 150,25,25792
W 4
Zvaa 140,136,200
U 51,51,4
U 52,52,4
Mvaa 140,140,204
U 53,53,4
U 54,54,4
Zvaa 132,140,188
U 55,55,4
K 48,48,49
Mvaa 132,136,184
Ta 152,26,10464
Ta 154,26,15584
Ta 156,26,20704
Ta 158,26,25824
W 4
Zvaa 144,144,184
D 19,40,94,3072
K 49,50,51
Mvaa 144,148,188
s_add_u32 m0, m0, 4096
K 50,52,53
K 51,54,55
Zvaa 152,148,204
Ta 128,25,10496
Ta 130,25,15616
Mvaa 152,144,200
Ta 132,25,20736
Ta 134,25,25856
W 4
Zvaa 156,152,200
buffer_store_dwordx4 v[48:51], v1, s[8:11], s93 offen offset:2048 sc1
U 56,56,4
Mvaa 156,156,204
U 57,57,4
U 58,58,4
Zvaa 148,156,188
U 59,59,4
U 60,60,4
Mvaa 148,152,184
Ta 136,26,10528
Ta 138,26,15648
Ta 140,26,20768
Ta 142,26,25888
W 4
)"
R"(Zvaa 160,128,184
U 61,61,4
U 62,62,4
Mvaa 160,132,188
U 63,63,4
K 56,56,57
Zvaa 168,132,204
buffer_load_dwordx4 v20, s[40:43], s94 offen lds
K 57,58,59
Mvaa 168,128,200
Ta 144,25,30720
Ta 146,25,35840
Ta 148,21,0
Ta 150,21,5120
W 4
Zvaa 172,136,200
s_add_u32 m0, s71, s48
s_lshl2_add_u32 s94, s48, s95
K 58,60,61
Mvaa 172,140,204
Ta 152,26,30752
K 59,62,63
Zvaa 164,140,188
buffer_store_dwordx4 v[56:59], v1, s[8:11], s93 offen offset:3072 sc1
U 64,64,5
Mvaa 164,136,184
Ta 154,26,35872
Ta 156,22,32
Ta 158,22,5152
W 4
Mvaa 96,144,192
U 65,65,5
U 66,66,5
Mvaa 96,148,196
U 67,67,5
U 68,68,5
Mvaa 104,148,212
U 69,69,5
U 70,70,5
Mvaa 104,144,208
Ta 128,25,30784
Ta 130,25,35904
Ta 132,21,64
Ta 134,21,5184
W 4
Mvaa 108,152,208
s_lshl_b32 s66, s14, 1
U 71,71,5
K 64,64,65
Mvaa 108,156,212
s_add_u32 s66, s66, 0
D 16,40,94,0
K 65,66,67
Mvaa 100,156,196
s_cmp_eq_u32 s66, s59
s_cselect_b32 s66, 134217728, 0
K 66,68,69
K 67,70,71
Mvaa 100,152,192
Ta 136,26,30816
Ta 138,26,35936
Ta 140,22,96
Ta 142,22,5216
W 4
Mvaa 112,128,192
s_add_u32 s67, s93, 131072
Ta 144,25,30848
Ta 146,25,35968
Mvaa 112,132,196
s_add_u32 s28, s52, s66
buffer_store_dwordx4 v[64:67], v1, s[8:11], s67 offen offset:0 sc1
U 72,72,5
Mvaa 120,132,212
s_lshl_b32 s66, s14, 1
U 73,73,5
U 74,74,5
Mvaa 120,128,208
Ta 148,21,128
Ta 150,21,5248
W 4
Mvaa 124,136,208
s_add_u32 s66, s66, 1
U 75,75,5
U 76,76,5
Mvaa 124,140,212
s_cmp_eq_u32 s66, s59
s_cselect_b32 s66, 134217728, 0
U 77,77,5
U 78,78,5
Mvaa 116,140,196
s_add_u32 s29, s52, s66
U 79,79,5
K 72,72,73
Mvaa 116,136,192
Ta 152,26,30880
Ta 154,26,36000
Ta 156,22,160
Ta 158,22,5280
W 4
Mvaa 128,144,192
D 17,40,94,1024
K 73,74,75
Mvaa 128,148,196
K 74,76,77
K 75,78,79
Mvaa 136,148,212
Ta 128,25,30912
Ta 130,25,36032
Mvaa 136,144,208
Ta 132,21,192
Ta 134,21,5312
W 4
Mvaa 140,152,208
D 18,40,94,2048
D 19,40,94,3072
s_add_u32 m0, m0, 4096
s_nop 0
buffer_load_dwordx4 v20, s[40:43], s94 offen lds
Mvaa 140,156,212
s_add_u32 s81, s58, 2
s_and_b32 s81, s81, 3
s_lshl_b32 s81, s81, 12
Mvaa 132,156,196
s_add_u32 s81, s81, s49
Ta 136,26,30944
Ta 138,26,36064
Mvaa 132,152,192
Ta 140,22,224
Ta 142,22,5344
W 4
Mvaa 144,128,192
buffer_store_dwordx4 v[72:75], v1, s[8:11], s67 offen offset:1024 sc1
U 80,80,5
Mvaa 144,132,196
U 81,81,5
U 82,82,5
Mvaa 152,132,212
U 83,83,5
U 84,84,5
Mvaa 152,128,208
Ta 144,25,30976
Ta 146,25,36096
Ta 148,21,256
Ta 150,21,5376
W 4
Mvaa 156,136,208
U 85,85,5
U 86,86,5
Mvaa 156,140,212
U 87,87,5
K 80,80,81
Mvaa 148,140,196
K 81,82,83
K 82,84,85
Mvaa 148,136,192
Ta 152,26,31008
Ta 154,26,36128
Ta 156,22,288
Ta 158,22,5408
W 4
Mvaa 160,144,192
buffer_load_dwordx4 a[0:3], v1, s[8:11], s81 offen offset:0 sc1
K 83,86,87
Mvaa 160,148,196
s_add_u32 s82, s81, 131072
Ta 128,21,10240
Ta 130,21,15360
Mvaa 168,148,212
Ta 132,21,20480
Ta 134,21,25600
Mvaa 168,144,208
W 4
Ta 136,22,10272
Ta 138,22,15392
Mvaa 172,152,208
buffer_store_dwordx4 v[80:83], v1, s[8:11], s67 offen offset:2048 sc1
U 88,88,5
Mvaa 172,156,212
U 89,89,5
U 90,90,5
Mvaa 164,156,196
U 91,91,5
U 92,92,5
Mvaa 164,152,192
Ta 140,22,20512
Ta 142,22,25632
s_waitcnt vmcnt(22) lgkmcnt(4)
Mvaa 96,128,216
U 93,93,5
U 94,94,5
Mvaa 96,132,220
U 95,95,5
K 88,88,89
Mvaa 104,132,236
K 89,90,91
K 90,92,93
Mvaa 104,128,232
Ta 144,21,10304
Ta 146,21,15424
Ta 148,21,20544
Ta 150,21,25664
W 4
Mvaa 108,136,232
buffer_load_dwordx4 a[16:19], v1, s[8:11], s82 offen offset:0 sc1
K 91,94,95
Mvaa 108,140,236
Ta 152,22,10336
Ta 154,22,15456
Mvaa 100,140,220
Ta 156,22,20576
Ta 158,22,25696
Mvaa 100,136,216
W 4
Ta 128,21,10368
Ta 130,21,15488
Mvaa 112,144,216
buffer_store_dwordx4 v[88:91], v1, s[8:11], s67 offen offset:3072 sc1
Ta 132,21,20608
Mvaa 112,148,220
Ta 134,21,25728
Ta 136,22,10400
Mvaa 120,148,236
Ta 138,22,15520
Ta 140,22,20640
Mvaa 120,144,232
W 7
Ta 142,22,25760
Ta 144,21,10432
Mvaa 124,152,232
buffer_load_dwordx4 a[4:7], v1, s[8:11], s81 offen offset:1024 sc1
Ta 146,21,15552
Mvaa 124,156,236
Ta 148,21,20672
Ta 150,21,25792
Mvaa 116,156,220
Mvaa 116,152,216
W 8
Ta 152,22,10464
Ta 154,22,15584
Mvaa 128,128,216
buffer_load_dwordx4 a[20:23], v1, s[8:11], s82 offen offset:1024 sc1
Mvaa 128,132,220
Ta 156,22,20704
Ta 158,22,25824
Mvaa 136,132,236
Mvaa 136,128,232
W 8
Ta 128,21,10496
Ta 130,21,15616
Mvaa 140,136,232
s_waitcnt vmcnt(7)
s_barrier
Mvaa 140,140,236
buffer_load_dwordx4 a[8:11], v1, s[8:11], s81 offen offset:2048 sc1
Ta 132,21,20736
Mvaa 132,140,220
Ta 134,21,25856
Mvaa 132,136,216
W 8
Ta 136,22,10528
Ta 138,22,15648
Mvaa 144,144,216
Mvaa 144,148,220
buffer_load_dwordx4 a[24:27], v1, s[8:11], s82 offen offset:2048 sc1
Ta 140,22,20768
Mvaa 152,148,236
Ta 142,22,25888
Mvaa 152,144,232
W 8
Ta 144,21,30720
Ta 146,21,35840
Mvaa 156,152,232
Mvaa 156,156,236
buffer_load_dwordx4 a[12:15], v1, s[8:11], s81 offen offset:3072 sc1
Ta 148,23,0
Mvaa 148,156,220
Ta 150,23,5120
Mvaa 148,152,216
W 8
Ta 152,22,30752
Ta 154,22,35872
Mvaa 160,128,216
Mvaa 160,132,220
buffer_load_dwordx4 a[28:31], v1, s[8:11], s82 offen offset:3072 sc1
Ta 156,24,32
Mvaa 168,132,236
s_lshl_b32 s92, s58, 2
s_add_u32 s91, s92, s51
s_add_u32 s95, s91, 11
Mvaa 168,128,232
W 7
s_and_b32 s95, s95, 15
s_mul_i32 s95, s95, 40960
Mvaa 172,136,232
s_mul_i32 s71, s51, 10240
s_add_u32 s71, s71, 40960
s_add_u32 m0, s71, s48
Mvaa 172,140,236
s_lshl2_add_u32 s94, s48, s95
D 16,40,94,0
Ta 158,24,5152
Mvaa 164,140,220
s_add_u32 s95, s91, 13
s_and_b32 s95, s95, 15
s_mul_i32 s95, s95, 40960
Mvaa 164,136,216
W 4
s_mul_i32 s71, s51, 20480
s_add_u32 s71, s71, 71680
Mvaa 96,144,224
s_lshl_b32 s80, s58, 3
Ta 128,21,30784
Ta 130,21,35904
Mvaa 96,148,228
s_add_u32 s80, s80, s55
D 17,40,94,1024
Ta 132,23,64
Mvaa 104,148,244
Ta 134,23,5184
Ta 136,22,30816
Mvaa 104,144,240
W 5
Ta 138,22,35936
v_and_b32 v27, 1, v12
Mvaa 108,152,240
s_waitcnt vmcnt(8)
v_mov_b64 v[28:29], s[12:13]
s_mov_b64 exec, 1
buffer_store_dwordx2 v[28:29], off, s[8:11], s80 sc1
s_mov_b64 exec, -1
Ta 140,24,96
v_lshlrev_b32 v27, 1, v27
Mvaa 108,156,244
Ta 142,24,5216
v_xor_b32 v27, v27, v13
Mvaa 100,156,228
Ta 144,21,30848
v_and_b32 v12, 2, v12
Mvaa 100,152,224
W 5
Ta 146,21,35968
v_or_b32 v12, v12, v14
Mvaa 112,128,224
D 18,40,94,2048
v_lshl_or_b32 v12, v27, 2, v12
Mvaa 112,132,228
Ta 148,23,128
v_cmp_eq_u32_e64 s[86:87], 0, v12
Mvaa 120,132,244
Ta 150,23,5248
Ta 152,22,30880
Mvaa 120,128,240
W 5
v_cndmask_b32_e64 v4, 0, v15, s[86:87]
v_cmp_eq_u32_e64 s[86:87], 1, v12
Mvaa 124,136,240
D 19,40,94,3072
Ta 154,22,36000
Mvaa 124,140,244
s_add_u32 m0, m0, 4096
v_cndmask_b32_e64 v5, 0, v15, s[86:87]
v_cmp_eq_u32_e64 s[86:87], 2, v12
Mvaa 116,140,228
Ta 156,24,160
Ta 158,24,5280
Mvaa 116,136,224
W 4
v_cndmask_b32_e64 v6, 0, v15, s[86:87]
v_cmp_eq_u32_e64 s[86:87], 3, v12
Mvaa 128,144,224
buffer_load_dwordx4 v20, s[40:43], s94 offen lds
Ta 128,21,30912
Mvaa 128,148,228
s_add_u32 m0, s71, s48
s_lshl2_add_u32 s94, s48, s95
v_cndmask_b32_e64 v7, 0, v15, s[86:87]
Mvaa 136,148,244
Ta 130,21,36032
v_cmp_eq_u32_e64 s[86:87], 4, v12
Mvaa 136,144,240
Ta 132,23,192
Ta 134,23,5312
W 4
Mvaa 140,152,240
D 16,40,94,0
v_cndmask_b32_e64 v8, 0, v15, s[86:87]
Mvaa 140,156,244
s_add_u32 s95, s92, 15
s_and_b32 s95, s95, 15
s_mul_i32 s95, s95, 40960
Mvaa 132,156,228
s_mul_i32 s71, s51, 0
s_add_u32 s71, s71, 102400
v_cmp_eq_u32_e64 s[86:87], 5, v12
Mvaa 132,152,224
Ta 136,22,30944
Ta 138,22,36064
Ta 140,24,224
Ta 142,24,5344
W 4
)"
R"(Mvaa 144,128,224
D 17,40,94,1024
v_cndmask_b32_e64 v9, 0, v15, s[86:87]
Mvaa 144,132,228
Ta 144,21,30976
v_cmp_eq_u32_e64 s[86:87], 6, v12
Mvaa 152,132,244
Ta 146,21,36096
Ta 148,23,256
Mvaa 152,128,240
Ta 150,23,5376
W 4
Mvaa 156,136,240
D 18,40,94,2048
v_cndmask_b32_e64 v10, 0, v15, s[86:87]
Mvaa 156,140,244
Ta 152,22,31008
v_cmp_eq_u32_e64 s[86:87], 7, v12
Mvaa 148,140,228
Ta 154,22,36128
Ta 156,24,288
Mvaa 148,136,224
Ta 158,24,5408
W 4
Mvaa 160,144,224
D 19,40,94,3072
v_cndmask_b32_e64 v11, 0, v15, s[86:87]
Mvaa 160,148,228
s_add_u32 m0, m0, 4096
s_mov_b32 s20, s38
s_and_b32 s21, s39, 0xffff
Mvaa 168,148,244
s_mov_b32 s22, 2621440
s_mov_b32 s23, 0x20000
s_and_b32 s17, s17, 0xffff
Mvaa 168,144,240
Ta 128,23,20480
Ta 130,23,25600
Ta 132,23,51200
Ta 134,23,56320
W 4
Mvaa 172,152,240
s_mov_b32 s18, 2621440
s_mov_b32 s19, 0x20000
buffer_load_dwordx4 v20, s[40:43], s94 offen lds
Mvaa 172,156,244
s_add_u32 m0, s71, s48
s_lshl2_add_u32 s94, s48, s95
s_lshr_b32 s66, s15, 3
Mvaa 164,156,228
s_lshl_b32 s66, s66, 4
s_add_u32 s66, s66, s59
s_lshl_b32 s63, s66, 6
Mvaa 164,152,224
Ta 136,24,20512
Ta 138,24,25632
Ta 140,24,51232
Ta 142,24,56352
s_waitcnt vmcnt(15) lgkmcnt(4)
Mvaa 96,128,0
s_add_u32 s63, s63, 65536
s_mul_i32 s62, s66, 40960
D 16,40,94,0
Mvaa 96,132,4
s_add_u32 s62, s62, 56623104
s_lshl_b32 s66, s14, 13
s_add_u32 s78, s62, s66
Mvaa 104,132,20
s_add_u32 s79, s78, 327680
s_and_b32 s66, s14, 1
s_mul_i32 s66, s66, 327680
Mvaa 104,128,16
Ta 144,23,20544
Ta 146,23,25664
Ta 148,23,51264
Ta 150,23,56384
W 4
Mvaa 108,136,16
s_add_u32 s65, s62, s66
s_add_u32 s65, s65, 32768
D 17,40,94,1024
Mvaa 108,140,20
s_mov_b32 s64, 0
s_bfe_u32 s66, s59, 0x10001
s_lshl_b32 s67, s57, 1
Mvaa 100,140,4
s_add_u32 s66, s66, s67
s_lshl_b32 s61, s66, 2
s_lshr_b32 s66, s59, 2
Mvaa 100,136,0
Ta 152,24,20576
Ta 154,24,25696
Ta 156,24,51296
Ta 158,24,56416
W 4
Mvaa 112,144,0
s_lshl_b32 s66, s66, 1
s_add_u32 s61, s61, s66
D 18,40,94,2048
Mvaa 112,148,4
s_and_b32 s66, s59, 1
s_add_u32 s61, s61, s66
s_xor_b32 s67, s61, 0
Mvaa 120,148,20
s_mul_i32 s72, s67, 20480
s_mul_i32 s66, s58, 320
Ta 128,23,20608
Mvaa 120,144,16
Ta 130,23,25728
Ta 132,23,51328
Ta 134,23,56448
W 4
Mvaa 124,152,16
s_add_u32 s72, s72, s66
s_mul_i32 s75, s67, 20480
D 19,40,94,3072
Mvaa 124,156,20
s_add_u32 m0, m0, 4096
s_mul_i32 s66, s58, 2560
s_add_u32 s75, s75, s66
Mvaa 116,156,4
s_add_u32 s75, s75, 131072
s_xor_b32 s67, s61, 124
buffer_load_dwordx4 v20, s[40:43], s94 offen lds
Mvaa 116,152,0
Ta 136,24,20640
Ta 138,24,25760
Ta 140,24,51360
Ta 142,24,56480
W 4
Mvaa 128,128,0
s_add_u32 s81, s58, 3
s_and_b32 s81, s81, 3
s_lshl_b32 s81, s81, 12
Mvaa 128,132,4
s_add_u32 s81, s81, s49
s_mul_i32 s73, s67, 20480
s_mul_i32 s66, s58, 320
Mvaa 136,132,20
s_add_u32 s73, s73, s66
s_mul_i32 s76, s67, 20480
buffer_load_dwordx4 a[32:35], v1, s[8:11], s81 offen offset:0 sc1
Mvaa 136,128,16
Ta 144,23,20672
Ta 146,23,25792
Ta 148,23,51392
Ta 150,23,56512
W 4
Mvaa 140,136,16
s_waitcnt vmcnt(11)
s_barrier
Mvaa 140,140,20
s_add_u32 s82, s81, 131072
s_mul_i32 s66, s58, 2560
s_add_u32 s76, s76, s66
Mvaa 132,140,4
s_add_u32 s76, s76, 131072
s_bitcmp1_b32 s14, 0
s_cselect_b32 s74, s73, s72
s_cselect_b32 s77, s76, s75
buffer_load_dwordx4 a[48:51], v1, s[8:11], s82 offen offset:0 sc1
Mvaa 132,136,0
Ta 152,24,20704
Ta 154,24,25824
Ta 156,24,51424
Ta 158,24,56544
W 4
Mvaa 144,144,0
s_add_u32 s74, s74, 256
s_add_u32 s77, s77, 2048
s_lshl_b32 s66, s14, 6
Mvaa 144,148,4
s_lshl_b32 s67, s14, 9
s_add_u32 s72, s72, s66
s_add_u32 s75, s75, s67
Mvaa 152,148,20
s_add_u32 s73, s73, s66
buffer_load_dwordx4 a[36:39], v1, s[8:11], s81 offen offset:1024 sc1
Ta 128,23,20736
Mvaa 152,144,16
Ta 130,23,25856
Ta 132,23,51456
Ta 134,23,56576
W 4
Mvaa 156,152,16
s_add_u32 s76, s76, s67
v_add_u32 v19, 4096, v1
v_bfe_u32 v27, v1, 4, 3
Mvaa 156,156,20
Ta 136,24,20768
Ta 138,24,25888
Mvaa 148,156,4
buffer_load_dwordx4 a[52:55], v1, s[8:11], s82 offen offset:1024 sc1
v_lshlrev_b32 v27, 3, v27
Mvaa 148,152,0
Ta 140,24,51488
Ta 142,24,56608
W 4
Mvaa 160,128,0
Ta 144,25,0
Ta 146,25,5120
Mvaa 160,132,4
Ta 148,21,40960
v_bfe_u32 v18, v1, 7, 1
Mvaa 168,132,20
buffer_load_dwordx4 a[40:43], v1, s[8:11], s81 offen offset:2048 sc1
Ta 150,21,46080
Mvaa 168,128,16
W 4
Ta 152,26,32
v_lshl_add_u32 v18, v18, 9, v27
Mvaa 172,136,16
s_mov_b32 s71, 0
Ta 154,26,5152
Ta 156,22,40992
Mvaa 172,140,20
Ta 158,22,46112
v_bfe_u32 v20, v1, 4, 4
Mvaa 164,140,4
buffer_load_dwordx4 a[56:59], v1, s[8:11], s82 offen offset:2048 sc1
Ta 128,25,64
Mvaa 164,136,0
W 5
Ta 130,25,5184
v_mul_u32_u24 v20, 1280, v20
Mvaa 96,144,8
Ta 132,21,41024
Ta 134,21,46144
Mvaa 96,148,12
Ta 136,26,96
Ta 138,26,5216
Mvaa 104,148,28
buffer_load_dwordx4 a[44:47], v1, s[8:11], s81 offen offset:3072 sc1
v_lshrrev_b32 v12, 8, v1
Mvaa 104,144,24
W 6
Ta 140,22,41056
Ta 142,22,46176
Mvaa 108,152,24
Ta 144,25,128
v_lshl_add_u32 v20, v12, 4, v20
Mvaa 108,156,28
Ta 146,25,5248
v_bfe_u32 v15, v1, 7, 1
Mvaa 100,156,12
buffer_load_dwordx4 a[60:63], v1, s[8:11], s82 offen offset:3072 sc1
Ta 148,21,41088
Mvaa 100,152,8
W 7
Ta 150,21,46208
v_mul_u32_u24 v14, 10240, v15
Mvaa 112,128,8
Ta 152,26,160
Ta 154,26,5280
Mvaa 112,132,12
Ta 156,22,41120
v_lshrrev_b32 v15, 8, v1
Mvaa 120,132,28
Ta 158,22,46240
Mvaa 120,128,24
W 8
Ta 128,25,192
v_lshl_add_u32 v14, v15, 7, v14
Mvaa 124,136,24
Ta 130,25,5312
v_bfe_u32 v28, v1, 4, 2
Mvaa 124,140,28
Ta 132,21,41152
v_lshlrev_b32 v28, 3, v28
Mvaa 116,140,12
Ta 134,21,46272
v_and_b32 v15, 0x70, v1
Mvaa 116,136,8
W 8
buffer_load_dwordx2 v[28:29], v28, s[8:11], s55 offen sc1
Ta 136,26,224
Mvaa 128,144,8
Ta 138,26,5344
v_add_u32 v14, v14, v15
Mvaa 128,148,12
Ta 140,22,41184
Ta 142,22,46304
Mvaa 136,148,28
Mvaa 136,144,24
W 8
buffer_load_dwordx4 v[36:39], v20, s[16:19], s72 offen
Ta 144,25,256
Mvaa 140,152,24
s_waitcnt vmcnt(10)
s_barrier
Mvaa 140,156,28
Ta 146,25,5376
Ta 148,21,41216
Mvaa 132,156,12
Ta 150,21,46336
Mvaa 132,152,8
W 8
Ta 152,26,288
Ta 154,26,5408
Mvaa 144,128,8
Mvaa 144,132,12
buffer_load_dwordx4 v[44:47], v14, s[8:11], s75 offen sc1
Ta 156,22,41248
Mvaa 152,132,28
Ta 158,22,46368
Mvaa 152,128,24
W 8
Ta 128,21,51200
Ta 130,21,56320
Mvaa 156,136,24
Mvaa 156,140,28
Ta 132,23,10240
Ta 134,23,15360
Mvaa 148,140,12
Mvaa 148,136,8
W 8
Ta 136,23,30720
Ta 138,23,35840
Mvaa 160,144,8
buffer_load_dwordx4 v[52:55], v20, s[16:19], s73 offen
Mvaa 160,148,12
Ta 140,23,40960
Ta 142,23,46080
Mvaa 168,148,28
Mvaa 168,144,24
W 8
Ta 144,22,51232
Ta 146,22,56352
Mvaa 172,152,24
Mvaa 172,156,28
Ta 148,24,10272
Ta 150,24,15392
Mvaa 164,156,12
buffer_load_dwordx4 v[60:63], v14, s[8:11], s76 offen sc1
Mvaa 164,152,8
s_waitcnt vmcnt(9) lgkmcnt(8)
Ta 152,24,30752
Ta 154,24,35872
Mvaa 96,128,32
Mvaa 96,132,36
Ta 156,24,40992
Ta 158,24,46112
Mvaa 104,132,52
Mvaa 104,128,48
s_waitcnt vmcnt(5) lgkmcnt(8)
Ta 128,21,51264
Ta 130,21,56384
Mvaa 104,136,56
Mvaa 104,140,60
buffer_load_dwordx4 v[68:71], v20, s[16:19], s74 offen
Ta 132,23,10304
Mvaa 96,140,44
Ta 134,23,15424
Mvaa 96,136,40
W 8
Ta 136,23,30784
Ta 138,23,35904
Mvaa 100,144,32
Mvaa 100,148,36
Ta 140,23,41024
Ta 142,23,46144
Mvaa 108,148,52
Mvaa 108,144,48
W 8
Ta 144,22,51296
Ta 146,22,56416
Mvaa 108,152,56
buffer_load_dwordx4 v[76:79], v14, s[8:11], s77 offen sc1
Mvaa 108,156,60
Ta 148,24,10336
Ta 150,24,15456
Mvaa 100,156,44
s_waitcnt vmcnt(6)
v_cmp_eq_u64 vcc, s[12:13], v[28:29]
s_cmp_eq_u64 vcc, -1
s_cbranch_scc0 .Lspin_82%=
.Lok_83%=:
s_add_u32 s81, s58, 1
s_and_b32 s81, s81, 3
Mvaa 100,152,40
W 8
s_lshl_b32 s81, s81, 12
s_add_u32 s81, s81, s50
Mvaa 112,128,32
buffer_load_dwordx4 a[64:67], v1, s[8:11], s81 offen offset:0 sc1
Ta 152,24,30816
Mvaa 112,132,36
s_add_u32 s82, s81, 131072
Ta 154,24,35936
Ta 156,24,41056
Mvaa 120,132,52
Ta 158,24,46176
K 96,96,97
Mvaa 120,128,48
W 8
Ta 128,21,51328
K 97,98,99
Mvaa 120,136,56
buffer_load_dwordx4 a[80:83], v1, s[8:11], s82 offen offset:0 sc1
Ta 130,21,56448
)"
R"(Mvaa 120,140,60
Ta 132,23,10368
K 98,100,101
Mvaa 112,140,44
Ta 134,23,15488
Mvaa 112,136,40
W 8
Ta 136,23,30848
K 99,102,103
Mvaa 116,144,32
buffer_load_dwordx2 v[12:13], v27, s[8:11], 0 offen offset:3584 sc1
Ta 138,23,35968
Mvaa 116,148,36
s_add_u32 s83, s28, 0
Ta 140,23,41088
Ta 142,23,46208
Mvaa 124,148,52
Mvaa 124,144,48
W 8
Ta 144,22,51360
Ta 146,22,56480
Mvaa 124,152,56
buffer_load_dwordx4 a[68:71], v1, s[8:11], s81 offen offset:1024 sc1
Mvaa 124,156,60
Ta 148,24,10400
Ta 150,24,15520
Mvaa 116,156,44
Mvaa 116,152,40
W 8
Ta 152,24,30880
Ta 154,24,36000
Mvaa 128,128,32
buffer_load_dwordx4 a[84:87], v1, s[8:11], s82 offen offset:1024 sc1
Mvaa 128,132,36
Ta 156,24,41120
Ta 158,24,46240
Mvaa 136,132,52
Mvaa 136,128,48
W 8
Ta 128,21,51392
Ta 130,21,56512
Mvaa 136,136,56
buffer_store_dwordx4 v[96:99], v1, s[8:11], s83 offen sc1
K 104,104,105
Mvaa 136,140,60
K 105,106,107
K 106,108,109
Mvaa 128,140,44
Ta 132,23,10432
K 107,110,111
Mvaa 128,136,40
W 7
s_add_u32 s83, s29, 40960
Ta 134,23,15552
Mvaa 132,144,32
Ta 136,23,30912
Ta 138,23,36032
Mvaa 132,148,36
Ta 140,23,41152
Ta 142,23,46272
Mvaa 140,148,52
Mvaa 140,144,48
W 8
Ta 144,22,51424
Ta 146,22,56544
Mvaa 140,152,56
Mvaa 140,156,60
Ta 148,24,10464
Ta 150,24,15584
Mvaa 132,156,44
Mvaa 132,152,40
W 8
buffer_load_dwordx4 a[72:75], v1, s[8:11], s81 offen offset:2048 sc1
Ta 152,24,30944
Mvaa 144,128,32
Ta 154,24,36064
Mvaa 144,132,36
Ta 156,24,41184
Ta 158,24,46304
Mvaa 152,132,52
Mvaa 152,128,48
W 8
buffer_load_dwordx4 a[88:91], v1, s[8:11], s82 offen offset:2048 sc1
Ta 128,21,51456
Mvaa 152,136,56
Ta 130,21,56576
Mvaa 152,140,60
Ta 132,23,10496
Ta 134,23,15616
Mvaa 144,140,44
Mvaa 144,136,40
W 8
buffer_load_dwordx4 a[76:79], v1, s[8:11], s81 offen offset:3072 sc1
Ta 136,23,30976
Mvaa 148,144,32
Ta 138,23,36096
Mvaa 148,148,36
Ta 140,23,41216
Ta 142,23,46336
Mvaa 156,148,52
Mvaa 156,144,48
W 8
buffer_store_dwordx4 v[104:107], v1, s[8:11], s83 offen sc1
K 112,112,113
Mvaa 156,152,56
K 113,114,115
K 114,116,117
Mvaa 156,156,60
Ta 144,22,51488
K 115,118,119
Mvaa 148,156,44
s_add_u32 s83, s28, 8192
Ta 146,22,56608
Ta 148,24,10528
Mvaa 148,152,40
W 7
Ta 150,24,15648
Ta 152,24,31008
Mvaa 160,128,32
Ta 154,24,36128
Mvaa 160,132,36
Ta 156,24,41248
Ta 158,24,46368
Mvaa 168,132,52
Mvaa 168,128,48
W 8
Ta 128,25,10240
Ta 130,25,15360
Mvaa 168,136,56
Mvaa 168,140,60
Ta 132,25,20480
Ta 134,25,25600
Mvaa 160,140,44
buffer_load_dwordx4 a[92:95], v1, s[8:11], s82 offen offset:3072 sc1
Mvaa 160,136,40
W 8
Ta 136,26,10272
Ta 138,26,15392
Mvaa 164,144,32
Mvaa 164,148,36
Ta 140,26,20512
Ta 142,26,25632
Mvaa 172,148,52
buffer_store_dwordx4 v[112:115], v1, s[8:11], s83 offen sc1
K 120,120,121
Mvaa 172,144,48
W 8
K 121,122,123
K 122,124,125
Mvaa 172,152,56
Ta 144,25,10304
K 123,126,127
Mvaa 172,156,60
s_add_u32 s83, s29, 49152
Ta 146,25,15424
Ta 148,25,20544
Mvaa 164,156,44
Ta 150,25,25664
Mvaa 164,152,40
W 8
Ta 152,26,10336
Ta 154,26,15456
Zvav 176,128,32
Mvav 176,132,40
Ta 156,26,20576
Ta 158,26,25696
Zvav 184,132,72
Mvav 184,128,64
W 8
Ta 128,25,10368
Ta 130,25,15488
Zvav 188,136,64
Mvav 188,140,72
buffer_store_dwordx4 v[120:123], v1, s[8:11], s83 offen sc1
K 128,128,129
Zvav 180,140,40
K 129,130,131
K 130,132,133
Mvav 180,136,32
W 6
Ta 132,25,20608
K 131,134,135
Zvav 192,144,32
s_add_u32 s83, s28, 16384
Ta 134,25,25728
Ta 136,26,10400
Mvav 192,148,40
Ta 138,26,15520
Ta 140,26,20640
Zvav 200,148,72
Ta 142,26,25760
Mvav 200,144,64
W 8
Ta 144,25,10432
Ta 146,25,15552
Zvav 204,152,64
Mvav 204,156,72
Ta 148,25,20672
Ta 150,25,25792
Zvav 196,156,40
Mvav 196,152,32
W 8
Ta 152,26,10464
Ta 154,26,15584
Zvav 208,128,32
s_add_u32 s81, s58, 2
buffer_store_dwordx4 v[128:131], v1, s[8:11], s83 offen sc1
K 136,136,137
Mvav 208,132,40
s_and_b32 s81, s81, 3
K 137,138,139
K 138,140,141
Zvav 216,132,72
s_lshl_b32 s81, s81, 12
s_add_u32 s81, s81, s50
K 139,142,143
Mvav 216,128,64
W 6
s_add_u32 s83, s29, 57344
Ta 156,26,20704
Zvav 220,136,64
Ta 158,26,25824
Ta 128,25,10496
Mvav 220,140,72
Ta 130,25,15616
Ta 132,25,20736
Zvav 212,140,40
Ta 134,25,25856
Mvav 212,136,32
W 8
Ta 136,26,10528
Ta 138,26,15648
Zvav 224,144,32
Mvav 224,148,40
Ta 140,26,20768
Ta 142,26,25888
Zvav 232,148,72
Mvav 232,144,64
W 8
buffer_store_dwordx4 v[136:139], v1, s[8:11], s83 offen sc1
K 144,144,145
Zvav 236,152,64
K 145,146,147
K 146,148,149
Mvav 236,156,72
Ta 144,25,30720
K 147,150,151
Zvav 228,156,40
s_add_u32 s83, s28, 24576
Ta 146,25,35840
Ta 148,21,0
Mvav 228,152,32
W 7
Ta 150,21,5120
Ta 152,26,30752
Zvav 240,128,32
Ta 154,26,35872
Mvav 240,132,40
Ta 156,22,32
Ta 158,22,5152
Zvav 248,132,72
Mvav 248,128,64
W 8
Ta 128,25,30784
Ta 130,25,35904
Zvav 252,136,64
Mvav 252,140,72
Ta 132,21,64
Ta 134,21,5184
Zvav 244,140,40
buffer_store_dwordx4 v[144:147], v1, s[8:11], s83 offen sc1
K 152,152,153
Mvav 244,136,32
W 8
K 153,154,155
K 154,156,157
Mvav 176,144,48
Ta 136,26,30816
K 155,158,159
Mvav 176,148,56
s_add_u32 s83, s29, 65536
Ta 138,26,35936
Ta 140,22,96
Mvav 184,148,88
Ta 142,22,5216
Mvav 184,144,80
W 8
Ta 144,25,30848
Ta 146,25,35968
Mvav 188,152,80
Mvav 188,156,88
Ta 148,21,128
Ta 150,21,5248
Mvav 180,156,56
Mvav 180,152,48
W 8
Ta 152,26,30880
Ta 154,26,36000
Mvav 192,128,48
Mvav 192,132,56
buffer_load_dwordx4 a[96:99], v1, s[8:11], s81 offen offset:0 sc1
Ta 156,22,160
Mvav 200,132,88
s_add_u32 s82, s81, 131072
Ta 158,22,5280
Mvav 200,128,80
W 8
Ta 128,25,30912
Ta 130,25,36032
Mvav 204,136,80
Mvav 204,140,88
buffer_store_dwordx4 v[152:155], v1, s[8:11], s83 offen sc1
K 160,160,161
Mvav 196,140,56
K 161,162,163
K 162,164,165
Mvav 196,136,48
W 6
Ta 132,21,192
K 163,166,167
Mvav 208,144,48
s_add_u32 s83, s28, 32768
Ta 134,21,5312
Ta 136,26,30944
Mvav 208,148,56
Ta 138,26,36064
Ta 140,22,224
Mvav 216,148,88
Ta 142,22,5344
Mvav 216,144,80
W 8
Ta 144,25,30976
Ta 146,25,36096
Mvav 220,152,80
Mvav 220,156,88
Ta 148,21,256
Ta 150,21,5376
Mvav 212,156,56
Mvav 212,152,48
W 8
Ta 152,26,31008
Ta 154,26,36128
Mvav 224,128,48
buffer_load_dwordx4 a[112:115], v1, s[8:11], s82 offen offset:0 sc1
Mvav 224,132,56
Ta 156,22,288
Ta 158,22,5408
Mvav 232,132,88
Mvav 232,128,80
W 8
Ta 128,21,10240
Ta 130,21,15360
Mvav 236,136,80
buffer_load_dwordx4 a[100:103], v1, s[8:11], s81 offen offset:1024 sc1
Mvav 236,140,88
Ta 132,21,20480
Ta 134,21,25600
Mvav 228,140,56
Mvav 228,136,48
W 8
Ta 136,22,10272
Ta 138,22,15392
Mvav 240,144,48
buffer_load_dwordx4 a[116:119], v1, s[8:11], s82 offen offset:1024 sc1
Mvav 240,148,56
Ta 140,22,20512
Ta 142,22,25632
Mvav 248,148,88
Mvav 248,144,80
W 8
Ta 144,21,10304
Ta 146,21,15424
Mvav 252,152,80
buffer_store_dwordx4 v[160:163], v1, s[8:11], s83 offen sc1
K 168,168,169
Mvav 252,156,88
K 169,170,171
K 170,172,173
Mvav 244,156,56
Ta 148,21,20544
K 171,174,175
Mvav 244,152,48
s_waitcnt vmcnt(17) lgkmcnt(7)
s_add_u32 s83, s29, 73728
Ta 150,21,25664
Mvaa 176,128,64
Ta 152,22,10336
Ta 154,22,15456
Mvaa 176,132,68
Ta 156,22,20576
Ta 158,22,25696
Mvaa 184,132,84
Mvaa 184,128,80
W 8
Ta 128,21,10368
Ta 130,21,15488
Mvaa 188,136,80
Mvaa 188,140,84
Ta 132,21,20608
Ta 134,21,25728
)"
R"(Mvaa 180,140,68
Mvaa 180,136,64
W 8
buffer_load_dwordx4 a[104:107], v1, s[8:11], s81 offen offset:2048 sc1
Ta 136,22,10400
Mvaa 192,144,64
Ta 138,22,15520
Mvaa 192,148,68
Ta 140,22,20640
Ta 142,22,25760
Mvaa 200,148,84
Mvaa 200,144,80
W 8
buffer_load_dwordx4 a[120:123], v1, s[8:11], s82 offen offset:2048 sc1
Ta 144,21,10432
Mvaa 204,152,80
Ta 146,21,15552
Mvaa 204,156,84
Ta 148,21,20672
Ta 150,21,25792
Mvaa 196,156,68
Mvaa 196,152,64
W 8
buffer_load_dwordx4 a[108:111], v1, s[8:11], s81 offen offset:3072 sc1
Ta 152,22,10464
Mvaa 208,128,64
Ta 154,22,15584
Mvaa 208,132,68
Ta 156,22,20704
Ta 158,22,25824
Mvaa 216,132,84
Mvaa 216,128,80
W 8
buffer_store_dwordx4 v[168:171], v1, s[8:11], s83 offen sc1
Ta 128,21,10496
Mvaa 220,136,80
Ta 130,21,15616
Mvaa 220,140,84
Ta 132,21,20736
Ta 134,21,25856
Mvaa 212,140,68
Mvaa 212,136,64
W 8
Ta 136,22,10528
Ta 138,22,15648
Mvaa 224,144,64
Mvaa 224,148,68
Ta 140,22,20768
Ta 142,22,25888
Mvaa 232,148,84
Mvaa 232,144,80
W 8
Ta 144,21,30720
Ta 146,21,35840
Mvaa 236,152,80
Mvaa 236,156,84
Ta 148,23,0
Ta 150,23,5120
Mvaa 228,156,68
buffer_load_dwordx4 a[124:127], v1, s[8:11], s82 offen offset:3072 sc1
Mvaa 228,152,64
W 8
s_add_u32 s81, s58, 3
s_and_b32 s81, s81, 3
Mvaa 240,128,64
s_lshl_b32 s81, s81, 12
s_add_u32 s81, s81, s50
Ta 152,22,30752
Mvaa 240,132,68
Ta 154,22,35872
Ta 156,24,32
Mvaa 248,132,84
buffer_load_dwordx4 a[184:187], v1, s[8:11], s81 offen offset:0 sc1
Ta 158,24,5152
Mvaa 248,128,80
W 8
s_add_u32 s82, s81, 131072
Ta 128,21,30784
Mvaa 252,136,80
Ta 130,21,35904
Mvaa 252,140,84
Ta 132,23,64
Ta 134,23,5184
Mvaa 244,140,68
buffer_load_dwordx4 a[200:203], v1, s[8:11], s82 offen offset:0 sc1
Mvaa 244,136,64
s_waitcnt vmcnt(18) lgkmcnt(8)
Ta 136,22,30816
Ta 138,22,35936
Mvaa 176,144,72
Mvaa 176,148,76
Ta 140,24,96
Ta 142,24,5216
Mvaa 184,148,92
buffer_load_dwordx4 a[188:191], v1, s[8:11], s81 offen offset:1024 sc1
Mvaa 184,144,88
W 8
Ta 144,21,30848
Ta 146,21,35968
Mvaa 188,152,88
Mvaa 188,156,92
Ta 148,23,128
Ta 150,23,5248
Mvaa 180,156,76
buffer_load_dwordx4 a[204:207], v1, s[8:11], s82 offen offset:1024 sc1
Mvaa 180,152,72
W 8
Ta 152,22,30880
Ta 154,22,36000
Mvaa 192,128,72
Mvaa 192,132,76
Ta 156,24,160
Ta 158,24,5280
Mvaa 200,132,92
buffer_load_dwordx4 a[192:195], v1, s[8:11], s81 offen offset:2048 sc1
Mvaa 200,128,88
W 8
Ta 128,21,30912
Ta 130,21,36032
Mvaa 204,136,88
Mvaa 204,140,92
Ta 132,23,192
Ta 134,23,5312
Mvaa 196,140,76
buffer_load_dwordx4 a[208:211], v1, s[8:11], s82 offen offset:2048 sc1
Mvaa 196,136,72
W 8
Ta 136,22,30944
Ta 138,22,36064
Mvaa 208,144,72
Mvaa 208,148,76
Ta 140,24,224
Ta 142,24,5344
Mvaa 216,148,92
buffer_load_dwordx4 a[196:199], v1, s[8:11], s81 offen offset:3072 sc1
Mvaa 216,144,88
W 8
Ta 144,21,30976
Ta 146,21,36096
Mvaa 220,152,88
Mvaa 220,156,92
Ta 148,23,256
Ta 150,23,5376
Mvaa 212,156,76
buffer_load_dwordx4 a[212:215], v1, s[8:11], s82 offen offset:3072 sc1
Mvaa 212,152,72
W 8
Ta 152,22,31008
Ta 154,22,36128
Mvaa 224,128,72
Mvaa 224,132,76
Ta 156,24,288
Ta 158,24,5408
Mvaa 232,132,92
Mvaa 232,128,88
W 8
Ta 128,23,20480
Ta 130,23,25600
Mvaa 236,136,88
Mvaa 236,140,92
Ta 132,23,51200
Ta 134,23,56320
Mvaa 228,140,76
Mvaa 228,136,72
W 8
Ta 136,25,0
Ta 138,25,5120
Mvaa 240,144,72
Mvaa 240,148,76
Ta 140,21,40960
Ta 142,21,46080
Mvaa 248,148,92
Mvaa 248,144,88
W 8
Ta 144,21,51200
Ta 146,21,56320
Mvaa 252,152,88
Mvaa 252,156,92
Ta 148,23,10240
Ta 150,23,15360
Mvaa 244,156,76
Mvaa 244,152,72
s_waitcnt vmcnt(14) lgkmcnt(8)
Ta 152,23,30720
Ta 154,23,35840
Mvaa 176,128,96
s_waitcnt vmcnt(9)
v_lshlrev_b32 v2, 2, v1
v_mov_b64 v[28:29], s[12:13]
s_mov_b64 exec, 3
buffer_store_dwordx2 v[28:29], v2, s[8:11], s53 offen offset:0 sc1
s_mov_b64 exec, -1
Mvaa 176,132,100
Ta 156,23,40960
Ta 158,23,46080
Mvaa 184,132,116
Mvaa 184,128,112
s_waitcnt vmcnt(9) lgkmcnt(8)
Ta 128,24,20512
Ta 130,24,25632
Mvaa 184,136,120
Mvaa 184,140,124
v_xor_b32 v12, s12, v12
v_xor_b32 v13, s13, v13
v_lshrrev_b32 v3, 11, v12
v_or_b32 v13, v13, v3
v_cmp_eq_u32 vcc, 0, v13
s_cmp_eq_u64 vcc, -1
s_cbranch_scc0 .LL_87%=
.LL_88%=:
v_bfe_u32 v13, v12, 4, 7
s_xor_b32 s66, s61, 124
v_cmp_eq_u32 vcc, s61, v13
v_cmp_eq_u32_e64 s[86:87], s66, v13
s_or_b32 s68, vcc_lo, s86
s_and_b32 s68, s68, 255
s_cbranch_scc1 .Lup_85%=
.Lupr_86%=:
Ta 132,24,51232
Ta 134,24,56352
Mvaa 176,140,108
Mvaa 176,136,104
s_waitcnt vmcnt(5) lgkmcnt(8)
Ta 136,26,32
v_lshlrev_b32 v32, 16, v36
Mvaa 176,144,184
Ta 138,26,5152
v_and_b32 v33, 0xffff0000, v36
Mvaa 176,148,188
Ta 140,22,40992
v_lshlrev_b32 v34, 16, v37
Mvaa 184,148,204
Ta 142,22,46112
v_and_b32 v35, 0xffff0000, v37
Mvaa 184,144,200
s_waitcnt vmcnt(1) lgkmcnt(8)
Ta 144,22,51232
v_lshlrev_b32 v36, 16, v38
Mvaa 184,152,208
Ta 146,22,56352
v_and_b32 v37, 0xffff0000, v38
Mvaa 184,156,212
Ta 148,24,10272
v_lshlrev_b32 v38, 16, v39
Mvaa 176,156,196
Ta 150,24,15392
v_and_b32 v39, 0xffff0000, v39
Mvaa 176,152,192
W 8
Ta 152,24,30752
v_lshlrev_b32 v40, 16, v44
Mvaa 180,128,96
Ta 154,24,35872
v_and_b32 v41, 0xffff0000, v44
Mvaa 180,132,100
Ta 156,24,40992
v_lshlrev_b32 v42, 16, v45
Mvaa 188,132,116
Ta 158,24,46112
v_and_b32 v43, 0xffff0000, v45
Mvaa 188,128,112
W 8
Ta 128,23,20544
v_lshlrev_b32 v44, 16, v46
Mvaa 188,136,120
Ta 130,23,25664
v_and_b32 v45, 0xffff0000, v46
Mvaa 188,140,124
buffer_load_dwordx2 v[16:17], v18, s[8:11], s63 offen sc1
v_lshlrev_b32 v46, 16, v47
Mvaa 180,140,108
Ta 132,23,51264
Ta 134,23,56384
Mvaa 180,136,104
W 8
v_and_b32 v47, 0xffff0000, v47
v_lshlrev_b32 v48, 16, v52
Mvaa 180,144,184
Ta 136,25,64
Ta 138,25,5184
Mvaa 180,148,188
v_and_b32 v49, 0xffff0000, v52
v_lshlrev_b32 v50, 16, v53
Mvaa 188,148,204
Ta 140,21,41024
Ta 142,21,46144
Mvaa 188,144,200
W 8
v_and_b32 v51, 0xffff0000, v53
v_lshlrev_b32 v52, 16, v54
Mvaa 188,152,208
Ta 144,21,51264
Ta 146,21,56384
Mvaa 188,156,212
v_and_b32 v53, 0xffff0000, v54
v_lshlrev_b32 v54, 16, v55
Mvaa 180,156,196
Ta 148,23,10304
Ta 150,23,15424
Mvaa 180,152,192
W 8
v_and_b32 v55, 0xffff0000, v55
v_lshlrev_b32 v56, 16, v60
Mvaa 192,128,96
Ta 152,23,30784
Ta 154,23,35904
Mvaa 192,132,100
v_and_b32 v57, 0xffff0000, v60
v_lshlrev_b32 v58, 16, v61
Mvaa 200,132,116
Ta 156,23,41024
Ta 158,23,46144
Mvaa 200,128,112
W 8
v_and_b32 v59, 0xffff0000, v61
K 176,176,177
Mvaa 200,136,120
Ta 128,24,20576
v_lshlrev_b32 v60, 16, v62
Mvaa 200,140,124
Ta 130,24,25696
K 177,178,179
Mvaa 192,140,108
v_and_b32 v61, 0xffff0000, v62
v_lshlrev_b32 v62, 16, v63
Mvaa 192,136,104
W 6
Ta 132,24,51296
K 178,180,181
Mvaa 192,144,184
Ta 134,24,56416
v_and_b32 v63, 0xffff0000, v63
Mvaa 192,148,188
K 179,182,183
v_lshlrev_b32 v64, 16, v68
Mvaa 200,148,204
s_add_u32 s83, s28, 327680
Ta 136,26,96
Ta 138,26,5216
Mvaa 200,144,200
W 6
buffer_store_dwordx4 v[176:179], v1, s[8:11], s83 offen sc1
v_and_b32 v65, 0xffff0000, v68
Mvaa 200,152,208
K 184,184,185
K 185,186,187
Mvaa 200,156,212
K 186,188,189
K 187,190,191
Mvaa 192,156,196
s_add_u32 s83, s29, 368640
buffer_store_dwordx4 v[184:187], v1, s[8:11], s83 offen sc1
v_lshlrev_b32 v66, 16, v69
Mvaa 192,152,192
Ta 140,22,41056
Ta 142,22,46176
W 4
Mvaa 196,128,96
v_and_b32 v67, 0xffff0000, v69
v_lshlrev_b32 v68, 16, v70
Mvaa 196,132,100
Ta 144,22,51296
Ta 146,22,56416
Mvaa 204,132,116
v_and_b32 v69, 0xffff0000, v70
v_lshlrev_b32 v70, 16, v71
Mvaa 204,128,112
Ta 148,24,10336
Ta 150,24,15456
W 4
Mvaa 204,136,120
v_and_b32 v71, 0xffff0000, v71
v_lshlrev_b32 v72, 16, v76
Mvaa 204,140,124
Ta 152,24,30816
Ta 154,24,35936
Mvaa 196,140,108
v_and_b32 v73, 0xffff0000, v76
v_lshlrev_b32 v74, 16, v77
Mvaa 196,136,104
Ta 156,24,41056
Ta 158,24,46176
W 4
Mvaa 196,144,184
v_and_b32 v75, 0xffff0000, v77
v_lshlrev_b32 v76, 16, v78
Mvaa 196,148,188
Ta 128,23,20608
Ta 130,23,25728
Mvaa 204,148,204
v_and_b32 v77, 0xffff0000, v78
v_lshlrev_b32 v78, 16, v79
Mvaa 204,144,200
Ta 132,23,51328
Ta 134,23,56448
W 4
Mvaa 204,152,208
Ta 136,25,128
v_and_b32 v79, 0xffff0000, v79
Mvaa 204,156,212
)"
R"(Ta 138,25,5248
Ta 140,21,41088
Mvaa 196,156,196
Ta 142,21,46208
Ta 144,21,51328
Mvaa 196,152,192
W 5
Ta 146,21,56448
Ta 148,23,10368
Mvaa 208,128,96
s_waitcnt vmcnt(2)
v_cmp_eq_u64 vcc, s[12:13], v[16:17]
s_and_b32 s71, vcc_lo, 255
s_bitset0_b32 s71, s59
Ta 150,23,15488
Ta 152,23,30848
Mvaa 208,132,100
s_lshl_b32 s92, s14, 3
s_lshr_b32 s92, s71, s92
s_mov_b32 s91, 134217728
Mvaa 216,132,116
s_bitcmp1_b32 s71, 0
s_cselect_b32 s94, s78, s91
Ta 154,23,35968
Mvaa 216,128,112
W 6
Ta 156,23,41088
Ta 158,23,46208
Mvaa 216,136,120
Ta 128,24,20640
K 192,192,193
Mvaa 216,140,124
Ta 130,24,25760
K 193,194,195
Mvaa 208,140,108
buffer_load_dwordx4 a[0:3], v1, s[8:11], s94 offen offset:0 sc1
K 194,196,197
Mvaa 208,136,104
W 6
Ta 132,24,51360
Ta 134,24,56480
Mvaa 208,144,184
Ta 136,26,160
K 195,198,199
Mvaa 208,148,188
s_add_u32 s83, s28, 335872
Ta 138,26,5280
Ta 140,22,41120
Mvaa 216,148,204
buffer_store_dwordx4 v[192:195], v1, s[8:11], s83 offen sc1
K 200,200,201
Mvaa 216,144,200
W 7
K 201,202,203
K 202,204,205
Mvaa 216,152,208
Ta 142,22,46240
K 203,206,207
Mvaa 216,156,212
s_add_u32 s83, s29, 376832
Ta 144,22,51360
Ta 146,22,56480
Mvaa 208,156,196
buffer_store_dwordx4 v[200:203], v1, s[8:11], s83 offen sc1
Ta 148,24,10400
Mvaa 208,152,192
W 7
Ta 150,24,15520
Ta 152,24,30880
Mvaa 212,128,96
Ta 154,24,36000
Mvaa 212,132,100
Ta 156,24,41120
Ta 158,24,46240
Mvaa 220,132,116
s_bitcmp1_b32 s92, 0
s_cbranch_scc0 .LL_90%=
buffer_load_dwordx4 a[216:219], v1, s[8:11], s65 offen offset:0 sc1
.LL_90%=:
Mvaa 220,128,112
W 8
s_bitcmp1_b32 s71, 1
s_cselect_b32 s94, s78, s91
Mvaa 220,136,120
Ta 128,23,20672
Ta 130,23,25792
Mvaa 220,140,124
Ta 132,23,51392
Ta 134,23,56512
Mvaa 212,140,108
buffer_load_dwordx4 a[4:7], v1, s[8:11], s94 offen offset:1024 sc1
Mvaa 212,136,104
W 8
Ta 136,25,192
Ta 138,25,5312
Mvaa 212,144,184
Mvaa 212,148,188
Ta 140,21,41152
Ta 142,21,46272
Mvaa 220,148,204
s_bitcmp1_b32 s92, 1
s_cbranch_scc0 .LL_91%=
buffer_load_dwordx4 a[220:223], v1, s[8:11], s65 offen offset:1024 sc1
.LL_91%=:
Mvaa 220,144,200
W 8
s_bitcmp1_b32 s71, 2
s_cselect_b32 s94, s78, s91
Mvaa 220,152,208
Ta 144,21,51392
Ta 146,21,56512
Mvaa 220,156,212
Ta 148,23,10432
Ta 150,23,15552
Mvaa 212,156,196
buffer_load_dwordx4 a[8:11], v1, s[8:11], s94 offen offset:2048 sc1
Mvaa 212,152,192
W 8
Ta 152,23,30912
Ta 154,23,36032
Mvaa 224,128,96
Mvaa 224,132,100
Ta 156,23,41152
Ta 158,23,46272
Mvaa 232,132,116
s_bitcmp1_b32 s92, 2
s_cbranch_scc0 .LL_92%=
buffer_load_dwordx4 a[224:227], v1, s[8:11], s65 offen offset:2048 sc1
.LL_92%=:
K 208,208,209
Mvaa 232,128,112
W 8
s_bitcmp1_b32 s71, 3
s_cselect_b32 s94, s78, s91
Mvaa 232,136,120
Ta 128,24,20704
Ta 130,24,25824
Mvaa 232,140,124
K 209,210,211
K 210,212,213
Mvaa 224,140,108
Ta 132,24,51424
Ta 134,24,56544
Mvaa 224,136,104
W 8
buffer_load_dwordx4 a[12:15], v1, s[8:11], s94 offen offset:3072 sc1
K 211,214,215
Mvaa 224,144,184
s_add_u32 s83, s28, 344064
Ta 136,26,224
Ta 138,26,5344
Mvaa 224,148,188
Ta 140,22,41184
Ta 142,22,46304
Mvaa 232,148,204
Mvaa 232,144,200
W 8
buffer_store_dwordx4 v[208:211], v1, s[8:11], s83 offen sc1
K 216,216,217
Mvaa 232,152,208
K 217,218,219
K 218,220,221
Mvaa 232,156,212
Ta 144,22,51424
K 219,222,223
Mvaa 224,156,196
s_add_u32 s83, s29, 385024
buffer_store_dwordx4 v[216:219], v1, s[8:11], s83 offen sc1
Ta 146,22,56544
Mvaa 224,152,192
W 6
Ta 148,24,10464
Ta 150,24,15584
Mvaa 228,128,96
Ta 152,24,30944
Ta 154,24,36064
Mvaa 228,132,100
Ta 156,24,41184
Ta 158,24,46304
Mvaa 236,132,116
s_bitcmp1_b32 s92, 3
s_cbranch_scc0 .LL_93%=
buffer_load_dwordx4 a[228:231], v1, s[8:11], s65 offen offset:3072 sc1
.LL_93%=:
Mvaa 236,128,112
W 8
s_bitcmp1_b32 s71, 4
s_cselect_b32 s94, s78, s91
Mvaa 236,136,120
Ta 128,23,20736
Ta 130,23,25856
Mvaa 236,140,124
Ta 132,23,51456
Ta 134,23,56576
Mvaa 228,140,108
buffer_load_dwordx4 a[16:19], v19, s[8:11], s94 offen offset:0 sc1
Mvaa 228,136,104
W 8
Ta 136,25,256
Ta 138,25,5376
Mvaa 228,144,184
Mvaa 228,148,188
Ta 140,21,41216
Ta 142,21,46336
Mvaa 236,148,204
s_bitcmp1_b32 s92, 4
s_cbranch_scc0 .LL_94%=
buffer_load_dwordx4 a[232:235], v19, s[8:11], s65 offen offset:0 sc1
.LL_94%=:
Mvaa 236,144,200
W 8
s_bitcmp1_b32 s71, 5
s_cselect_b32 s94, s78, s91
Mvaa 236,152,208
Ta 144,21,51456
Ta 146,21,56576
Mvaa 236,156,212
Ta 148,23,10496
Ta 150,23,15616
Mvaa 228,156,196
buffer_load_dwordx4 a[20:23], v19, s[8:11], s94 offen offset:1024 sc1
Mvaa 228,152,192
W 8
Ta 152,23,30976
Ta 154,23,36096
Mvaa 240,128,96
Mvaa 240,132,100
Ta 156,23,41216
Ta 158,23,46336
Mvaa 248,132,116
s_bitcmp1_b32 s92, 5
s_cbranch_scc0 .LL_95%=
buffer_load_dwordx4 a[236:239], v19, s[8:11], s65 offen offset:1024 sc1
.LL_95%=:
K 224,224,225
Mvaa 248,128,112
W 8
s_bitcmp1_b32 s71, 6
s_cselect_b32 s94, s78, s91
Mvaa 248,136,120
Ta 128,24,20768
Ta 130,24,25888
Mvaa 248,140,124
K 225,226,227
K 226,228,229
Mvaa 240,140,108
Ta 132,24,51488
Ta 134,24,56608
Mvaa 240,136,104
W 8
Ta 136,26,288
K 227,230,231
Mvaa 240,144,184
s_add_u32 s83, s28, 352256
Ta 138,26,5408
buffer_load_dwordx4 a[24:27], v19, s[8:11], s94 offen offset:2048 sc1
Mvaa 240,148,188
Ta 140,22,41248
Ta 142,22,46368
Mvaa 248,148,204
Mvaa 248,144,200
W 8
Ta 144,22,51488
Ta 146,22,56608
Mvaa 248,152,208
buffer_store_dwordx4 v[224:227], v1, s[8:11], s83 offen sc1
K 232,232,233
Mvaa 248,156,212
K 233,234,235
K 234,236,237
Mvaa 240,156,196
K 235,238,239
s_add_u32 s83, s29, 393216
buffer_store_dwordx4 v[232:235], v1, s[8:11], s83 offen sc1
Mvaa 240,152,192
W 6
Ta 148,24,10528
Ta 150,24,15648
Mvaa 244,128,96
Ta 152,24,31008
Ta 154,24,36128
Mvaa 244,132,100
Ta 156,24,41248
Ta 158,24,46368
Mvaa 252,132,116
s_bitcmp1_b32 s92, 6
s_cbranch_scc0 .LL_96%=
buffer_load_dwordx4 a[240:243], v19, s[8:11], s65 offen offset:2048 sc1
.LL_96%=:
Mvaa 252,128,112
W 8
s_bitcmp1_b32 s71, 7
s_cselect_b32 s94, s78, s91
Mvaa 252,136,120
Mvaa 252,140,124
Mvaa 244,140,108
buffer_load_dwordx4 a[28:31], v19, s[8:11], s94 offen offset:3072 sc1
Mvaa 244,136,104
W 4
Mvaa 244,144,184
Mvaa 244,148,188
Mvaa 252,148,204
s_bitcmp1_b32 s92, 7
s_cbranch_scc0 .LL_97%=
buffer_load_dwordx4 a[244:247], v19, s[8:11], s65 offen offset:3072 sc1
.LL_97%=:
Mvaa 252,144,200
W 0
Mvaa 252,152,208
Mvaa 252,156,212
Mvaa 244,156,196
Mvaa 244,152,192
K 240,240,241
K 241,242,243
s_nop 5
K 242,244,245
K 243,246,247
s_add_u32 s83, s28, 360448
buffer_store_dwordx4 v[240:243], v1, s[8:11], s83 offen sc1
K 248,248,249
K 249,250,251
K 250,252,253
K 251,254,255
s_add_u32 s83, s29, 401408
buffer_store_dwordx4 v[248:251], v1, s[8:11], s83 offen sc1
s_branch .Ltend_81%=
.p2align 12
.Llate_80%=:
ds_read_b32 v11, v11 offset:59392
ds_read_b64 v[8:9], v10 offset:57344
Rv 176,3,0
Rv 180,3,1024
Rv 184,3,2048
Rv 188,3,3072
Rv 192,3,4096
Rv 196,3,5120
Rv 200,3,6144
Rv 204,3,7168
W 6
Mvvv 32,224,176
s_lshl_b32 s92, s58, 2
s_add_u32 s91, s92, s51
s_add_u32 s95, s91, 7
Mvvv 36,228,176
s_and_b32 s95, s95, 15
s_mul_i32 s95, s95, 40960
s_mul_i32 s71, s51, 20480
Mvvv 40,224,180
s_add_u32 s71, s71, 61440
s_add_u32 m0, s71, s48
s_lshl2_add_u32 s94, s48, s95
Mvvv 44,228,180
W 4
v_cmp_ne_u32 vcc, 0, v11
s_cmp_lg_u64 vcc, 0
s_cbranch_scc1 .Lredo%=
A 4,4,8
Mvvv 48,224,184
v_rcp_f32 v4, v4
Mvvv 52,228,184
Ta 128,25,10240
A 5,5,9
Mvvv 56,224,188
v_rcp_f32 v5, v5
Mvvv 60,228,188
W 3
Ta 130,25,15360
D 16,40,94,0
Mvvv 64,224,192
s_add_u32 s95, s91, 9
s_and_b32 s95, s95, 15
s_mul_i32 s95, s95, 40960
Mvvv 68,228,192
s_mul_i32 s71, s51, 10240
s_add_u32 s71, s71, 112640
s_lshl_b32 s93, s58, 12
Mvvv 72,224,196
s_add_u32 s93, s93, s50
U 32,32,4
U 33,33,4
Mvvv 76,228,196
Ta 132,25,20480
Ta 134,25,25600
W 4
Mvvv 80,224,200
U 34,34,4
U 35,35,4
Mvvv 84,228,200
U 36,36,4
U 37,37,4
Mvvv 88,224,204
U 38,38,4
U 39,39,4
K 32,32,33
K 33,34,35
K 34,36,37
K 35,38,39
buffer_store_dwordx4 v[32:35], v1, s[8:11], s93 offen offset:0 sc1
U 40,40,4
U 41,41,4
)"
R"(U 42,42,4
U 43,43,4
U 44,44,4
U 45,45,4
U 46,46,4
U 47,47,4
K 40,40,41
K 41,42,43
K 42,44,45
K 43,46,47
buffer_store_dwordx4 v[40:43], v1, s[8:11], s93 offen offset:1024 sc1
U 64,64,5
U 65,65,5
U 66,66,5
U 67,67,5
U 68,68,5
U 69,69,5
U 70,70,5
U 71,71,5
K 64,64,65
K 65,66,67
K 66,68,69
K 67,70,71
s_add_u32 s67, s93, 131072
buffer_store_dwordx4 v[64:67], v1, s[8:11], s67 offen offset:0 sc1
U 72,72,5
U 73,73,5
U 74,74,5
U 75,75,5
U 76,76,5
U 77,77,5
U 78,78,5
U 79,79,5
K 72,72,73
K 73,74,75
K 74,76,77
K 75,78,79
Mvvv 92,228,204
Ta 136,26,10272
Ta 138,26,15392
Ta 140,26,20512
Ta 142,26,25632
W 4
Zvav 176,128,32
Ta 144,25,10304
Ta 146,25,15424
Mvav 176,132,40
Ta 148,25,20544
Ta 150,25,25664
Zvav 184,132,72
D 17,40,94,1024
Ta 152,26,10336
Mvav 184,128,64
W 5
Ta 154,26,15456
Ta 156,26,20576
Zvav 188,136,64
Ta 158,26,25696
Ta 128,25,10368
Mvav 188,140,72
Ta 130,25,15488
Ta 132,25,20608
Zvav 180,140,40
D 18,40,94,2048
Ta 134,25,25728
Mvav 180,136,32
W 8
Ta 136,26,10400
Ta 138,26,15520
Zvav 192,144,32
Mvav 192,148,40
Ta 140,26,20640
Ta 142,26,25760
Zvav 200,148,72
D 19,40,94,3072
Mvav 200,144,64
W 8
s_add_u32 m0, m0, 4096
Ta 144,25,10432
Zvav 204,152,64
Ta 146,25,15552
Mvav 204,156,72
Ta 148,25,20672
Ta 150,25,25792
Zvav 196,156,40
buffer_load_dwordx4 v20, s[40:43], s94 offen lds
Mvav 196,152,32
W 8
s_add_u32 m0, s71, s48
s_lshl2_add_u32 s94, s48, s95
Zvav 208,128,32
Ta 152,26,10464
Ta 154,26,15584
Mvav 208,132,40
Ta 156,26,20704
Ta 158,26,25824
Zvav 216,132,72
D 16,40,94,0
v_bfe_u32 v28, v1, 4, 2
Mvav 216,128,64
W 8
Ta 128,25,10496
v_lshlrev_b32 v28, 3, v28
Zvav 220,136,64
Ta 130,25,15616
Mvav 220,140,72
Ta 132,25,20736
Ta 134,25,25856
Zvav 212,140,40
buffer_load_dwordx2 v[28:29], v28, s[8:11], s54 offen sc1
Mvav 212,136,32
W 8
Ta 136,26,10528
Ta 138,26,15648
Zvav 224,144,32
Mvav 224,148,40
Ta 140,26,20768
Ta 142,26,25888
Zvav 232,148,72
D 17,40,94,1024
Mvav 232,144,64
W 8
Ta 144,25,30720
Ta 146,25,35840
Zvav 236,152,64
Mvav 236,156,72
Ta 148,21,0
Ta 150,21,5120
Zvav 228,156,40
buffer_store_dwordx4 v[72:75], v1, s[8:11], s67 offen offset:1024 sc1
U 48,48,4
Mvav 228,152,32
W 8
U 49,49,4
U 50,50,4
Zvav 240,128,32
U 51,51,4
U 52,52,4
Mvav 240,132,40
U 53,53,4
U 54,54,4
Zvav 248,132,72
D 18,40,94,2048
U 55,55,4
Mvav 248,128,64
W 4
K 48,48,49
K 49,50,51
Zvav 252,136,64
K 50,52,53
K 51,54,55
Mvav 252,140,72
Ta 152,26,30752
Ta 154,26,35872
Zvav 244,140,40
buffer_store_dwordx4 v[48:51], v1, s[8:11], s93 offen offset:2048 sc1
U 56,56,4
U 57,57,4
U 58,58,4
U 59,59,4
U 60,60,4
U 61,61,4
U 62,62,4
U 63,63,4
K 56,56,57
K 57,58,59
K 58,60,61
K 59,62,63
buffer_store_dwordx4 v[56:59], v1, s[8:11], s93 offen offset:3072 sc1
U 80,80,5
U 81,81,5
U 82,82,5
U 83,83,5
U 84,84,5
U 85,85,5
U 86,86,5
U 87,87,5
K 80,80,81
K 81,82,83
K 82,84,85
K 83,86,87
buffer_store_dwordx4 v[80:83], v1, s[8:11], s67 offen offset:2048 sc1
U 88,88,5
U 89,89,5
U 90,90,5
U 91,91,5
U 92,92,5
U 93,93,5
U 94,94,5
U 95,95,5
K 88,88,89
K 89,90,91
K 90,92,93
K 91,94,95
Mvav 244,136,32
Ta 156,22,32
Ta 158,22,5152
W 4
Mvav 176,144,48
Ta 128,25,30784
Ta 130,25,35904
Mvav 176,148,56
Ta 132,21,64
Ta 134,21,5184
Mvav 184,148,88
D 19,40,94,3072
Ta 136,26,30816
Mvav 184,144,80
s_add_u32 m0, m0, 4096
s_nop 0
buffer_load_dwordx4 v20, s[40:43], s94 offen lds
s_waitcnt vmcnt(8)
v_cmp_eq_u64 vcc, s[12:13], v[28:29]
s_cmp_eq_u64 vcc, -1
s_cbranch_scc0 .Lspin_98%=
.Lok_99%=:
W 5
Mvav 188,152,80
s_add_u32 s81, s58, 0
s_and_b32 s81, s81, 3
s_lshl_b32 s81, s81, 12
Mvav 188,156,88
s_add_u32 s81, s81, s49
Ta 138,26,35936
Ta 140,22,96
Mvav 180,156,56
s_lshl_b32 s66, s14, 1
Ta 142,22,5216
Ta 144,25,30848
Mvav 180,152,48
W 5
buffer_store_dwordx4 v[88:91], v1, s[8:11], s67 offen offset:3072 sc1
Ta 146,25,35968
Mvav 192,128,48
s_add_u32 s66, s66, 0
Ta 148,21,128
Ta 150,21,5248
Mvav 192,132,56
s_cmp_eq_u32 s66, s59
s_cselect_b32 s66, 134217728, 0
Ta 152,26,30880
Ta 154,26,36000
Mvav 200,132,88
s_add_u32 s28, s52, s66
Ta 156,22,160
Ta 158,22,5280
Mvav 200,128,80
W 8
s_lshl_b32 s66, s14, 1
buffer_load_dwordx4 a[184:187], v1, s[8:11], s81 offen offset:0 sc1
Mvav 204,136,80
s_add_u32 s82, s81, 131072
Ta 128,25,30912
Ta 130,25,36032
Mvav 204,140,88
s_add_u32 s66, s66, 1
s_cmp_eq_u32 s66, s59
s_cselect_b32 s66, 134217728, 0
Ta 132,21,192
Mvav 196,140,56
s_add_u32 s29, s52, s66
Ta 134,21,5312
Mvav 196,136,48
W 8
buffer_load_dwordx4 a[200:203], v1, s[8:11], s82 offen offset:0 sc1
Ta 136,26,30944
Mvav 208,144,48
Ta 138,26,36064
Mvav 208,148,56
Ta 140,22,224
Ta 142,22,5344
Mvav 216,148,88
Mvav 216,144,80
W 8
buffer_load_dwordx4 a[188:191], v1, s[8:11], s81 offen offset:1024 sc1
Ta 144,25,30976
Mvav 220,152,80
Ta 146,25,36096
Mvav 220,156,88
Ta 148,21,256
Ta 150,21,5376
Mvav 212,156,56
Mvav 212,152,48
W 8
buffer_load_dwordx4 a[204:207], v1, s[8:11], s82 offen offset:1024 sc1
Ta 152,26,31008
Mvav 224,128,48
Ta 154,26,36128
Mvav 224,132,56
Ta 156,22,288
Ta 158,22,5408
Mvav 232,132,88
Mvav 232,128,80
W 8
buffer_load_dwordx4 a[192:195], v1, s[8:11], s81 offen offset:2048 sc1
Ta 128,25,10240
Mvav 236,136,80
Ta 130,25,15360
Mvav 236,140,88
Ta 132,25,20480
Ta 134,25,25600
Mvav 228,140,56
Mvav 228,136,48
W 8
buffer_load_dwordx4 a[208:211], v1, s[8:11], s82 offen offset:2048 sc1
Ta 136,26,10272
Mvav 240,144,48
Ta 138,26,15392
Mvav 240,148,56
Ta 140,26,20512
Ta 142,26,25632
Mvav 248,148,88
Mvav 248,144,80
W 8
buffer_load_dwordx4 a[196:199], v1, s[8:11], s81 offen offset:3072 sc1
Ta 144,25,10304
Mvav 252,152,80
Ta 146,25,15424
Mvav 252,156,88
Ta 148,25,20544
Ta 150,25,25664
Mvav 244,156,56
Mvav 244,152,48
s_waitcnt vmcnt(3) lgkmcnt(8)
buffer_load_dwordx4 a[212:215], v1, s[8:11], s82 offen offset:3072 sc1
Ta 152,26,10336
Zvaa 96,128,184
Ta 154,26,15456
Mvaa 96,132,188
Ta 156,26,20576
Ta 158,26,25696
Zvaa 104,132,204
Mvaa 104,128,200
W 8
Ta 128,25,10368
Ta 130,25,15488
Zvaa 108,136,200
Mvaa 108,140,204
Ta 132,25,20608
Ta 134,25,25728
Zvaa 100,140,188
s_lshl_b32 s92, s58, 2
s_add_u32 s91, s92, s51
s_add_u32 s95, s91, 11
Mvaa 100,136,184
W 8
s_and_b32 s95, s95, 15
s_mul_i32 s95, s95, 40960
Zvaa 112,144,184
s_mul_i32 s71, s51, 10240
s_add_u32 s71, s71, 40960
s_add_u32 m0, s71, s48
Mvaa 112,148,188
s_lshl2_add_u32 s94, s48, s95
Ta 136,26,10400
Ta 138,26,15520
Zvaa 120,148,204
Ta 140,26,20640
Ta 142,26,25760
Mvaa 120,144,200
W 8
Ta 144,25,10432
Ta 146,25,15552
Zvaa 124,152,200
Mvaa 124,156,204
Ta 148,25,20672
Ta 150,25,25792
Zvaa 116,156,188
Mvaa 116,152,184
W 8
Ta 152,26,10464
Ta 154,26,15584
Zvaa 128,128,184
s_lshl_b32 s80, s58, 3
s_add_u32 s80, s80, s55
v_mov_b64 v[28:29], s[12:13]
s_mov_b64 exec, 1
buffer_store_dwordx2 v[28:29], off, s[8:11], s80 sc1
s_mov_b64 exec, -1
Mvaa 128,132,188
Ta 156,26,20704
v_and_b32 v27, 1, v12
Zvaa 136,132,204
Ta 158,26,25824
v_lshlrev_b32 v27, 1, v27
Mvaa 136,128,200
W 8
Ta 128,25,10496
v_xor_b32 v27, v27, v13
Zvaa 140,136,200
s_barrier
Mvaa 140,140,204
Ta 130,25,15616
v_and_b32 v12, 2, v12
Zvaa 132,140,188
D 16,40,94,0
v_or_b32 v12, v12, v14
Mvaa 132,136,184
W 6
s_add_u32 s95, s91, 13
)"
R"(s_and_b32 s95, s95, 15
Zvaa 144,144,184
s_mul_i32 s95, s95, 40960
s_mul_i32 s71, s51, 20480
s_add_u32 s71, s71, 71680
Mvaa 144,148,188
Ta 132,25,20736
Ta 134,25,25856
Zvaa 152,148,204
v_lshl_or_b32 v12, v27, 2, v12
v_cmp_eq_u32_e64 s[86:87], 0, v12
Mvaa 152,144,200
W 4
Ta 136,26,10528
Ta 138,26,15648
Zvaa 156,152,200
D 17,40,94,1024
v_cndmask_b32_e64 v4, 0, v15, s[86:87]
Mvaa 156,156,204
Ta 140,26,20768
v_cmp_eq_u32_e64 s[86:87], 1, v12
Zvaa 148,156,188
Ta 142,26,25888
Ta 144,25,30720
Mvaa 148,152,184
W 5
v_cndmask_b32_e64 v5, 0, v15, s[86:87]
v_cmp_eq_u32_e64 s[86:87], 2, v12
Zvaa 160,128,184
Ta 146,25,35840
D 18,40,94,2048
Mvaa 160,132,188
v_cndmask_b32_e64 v6, 0, v15, s[86:87]
v_cmp_eq_u32_e64 s[86:87], 3, v12
Zvaa 168,132,204
Ta 148,21,0
Ta 150,21,5120
Mvaa 168,128,200
W 4
Ta 152,26,30752
v_cndmask_b32_e64 v7, 0, v15, s[86:87]
Zvaa 172,136,200
Ta 154,26,35872
v_cmp_eq_u32_e64 s[86:87], 4, v12
Mvaa 172,140,204
D 19,40,94,3072
Ta 156,22,32
Zvaa 164,140,188
s_add_u32 m0, m0, 4096
Ta 158,22,5152
v_cndmask_b32_e64 v8, 0, v15, s[86:87]
Mvaa 164,136,184
s_waitcnt vmcnt(5) lgkmcnt(4)
Ta 128,25,30784
v_cmp_eq_u32_e64 s[86:87], 5, v12
Mvaa 96,144,192
Ta 130,25,35904
Ta 132,21,64
Mvaa 96,148,196
v_cndmask_b32_e64 v9, 0, v15, s[86:87]
v_cmp_eq_u32_e64 s[86:87], 6, v12
Mvaa 104,148,212
buffer_load_dwordx4 v20, s[40:43], s94 offen lds
Ta 134,21,5184
Mvaa 104,144,208
W 4
s_add_u32 s81, s58, 1
s_and_b32 s81, s81, 3
Mvaa 108,152,208
s_lshl_b32 s81, s81, 12
s_add_u32 s81, s81, s49
v_cndmask_b32_e64 v10, 0, v15, s[86:87]
Mvaa 108,156,212
Ta 136,26,30816
v_cmp_eq_u32_e64 s[86:87], 7, v12
Mvaa 100,156,196
Ta 138,26,35936
buffer_load_dwordx4 a[216:219], v1, s[8:11], s81 offen offset:0 sc1
Mvaa 100,152,192
Ta 140,22,96
Ta 142,22,5216
W 4
Mvaa 112,128,192
s_add_u32 s82, s81, 131072
Ta 144,25,30848
v_cndmask_b32_e64 v11, 0, v15, s[86:87]
Mvaa 112,132,196
s_mov_b32 s20, s38
s_and_b32 s21, s39, 0xffff
s_mov_b32 s22, 2621440
Mvaa 120,132,212
s_mov_b32 s23, 0x20000
buffer_load_dwordx4 a[232:235], v1, s[8:11], s82 offen offset:0 sc1
Ta 146,25,35968
Mvaa 120,128,208
Ta 148,21,128
Ta 150,21,5248
W 4
Mvaa 124,136,208
s_and_b32 s17, s17, 0xffff
s_mov_b32 s18, 2621440
s_mov_b32 s19, 0x20000
Mvaa 124,140,212
s_lshr_b32 s66, s15, 3
Ta 152,26,30880
Ta 154,26,36000
Mvaa 116,140,196
s_lshl_b32 s66, s66, 4
buffer_load_dwordx4 a[220:223], v1, s[8:11], s81 offen offset:1024 sc1
Ta 156,22,160
Mvaa 116,136,192
s_add_u32 s66, s66, s59
Ta 158,22,5280
W 4
Mvaa 128,144,192
s_lshl_b32 s63, s66, 6
Ta 128,25,30912
Ta 130,25,36032
Mvaa 128,148,196
s_add_u32 s63, s63, 65536
s_mul_i32 s62, s66, 40960
Ta 132,21,192
Mvaa 136,148,212
s_add_u32 s62, s62, 56623104
buffer_load_dwordx4 a[236:239], v1, s[8:11], s82 offen offset:1024 sc1
Ta 134,21,5312
Mvaa 136,144,208
W 4
s_lshl_b32 s66, s14, 13
Ta 136,26,30944
Mvaa 140,152,208
s_add_u32 s78, s62, s66
s_add_u32 s79, s78, 327680
Ta 138,26,36064
Mvaa 140,156,212
s_and_b32 s66, s14, 1
Ta 140,22,224
Ta 142,22,5344
Mvaa 132,156,196
s_mul_i32 s66, s66, 327680
buffer_load_dwordx4 a[224:227], v1, s[8:11], s81 offen offset:2048 sc1
Ta 144,25,30976
Mvaa 132,152,192
W 5
s_add_u32 s65, s62, s66
Ta 146,25,36096
Mvaa 144,128,192
s_add_u32 s65, s65, 32768
Ta 148,21,256
Ta 150,21,5376
Mvaa 144,132,196
s_mov_b32 s64, 0
Ta 152,26,31008
Ta 154,26,36128
Mvaa 152,132,212
s_bfe_u32 s66, s59, 0x10001
s_lshl_b32 s67, s57, 1
buffer_load_dwordx4 a[240:243], v1, s[8:11], s82 offen offset:2048 sc1
Mvaa 152,128,208
W 6
Ta 156,22,288
Ta 158,22,5408
Mvaa 156,136,208
s_add_u32 s66, s66, s67
s_lshl_b32 s61, s66, 2
Ta 128,21,10240
Mvaa 156,140,212
s_lshr_b32 s66, s59, 2
s_lshl_b32 s66, s66, 1
Ta 130,21,15360
Mvaa 148,140,196
s_add_u32 s61, s61, s66
buffer_load_dwordx4 a[228:231], v1, s[8:11], s81 offen offset:3072 sc1
Ta 132,21,20480
Mvaa 148,136,192
W 7
s_and_b32 s66, s59, 1
Ta 134,21,25600
Mvaa 160,144,192
s_add_u32 s61, s61, s66
Ta 136,22,10272
Ta 138,22,15392
Mvaa 160,148,196
Ta 140,22,20512
Ta 142,22,25632
Mvaa 168,148,212
s_xor_b32 s67, s61, 0
buffer_load_dwordx4 a[244:247], v1, s[8:11], s82 offen offset:3072 sc1
Mvaa 168,144,208
W 8
s_add_u32 m0, s71, s48
s_lshl2_add_u32 s94, s48, s95
Mvaa 172,152,208
s_mul_i32 s72, s67, 20480
Ta 144,21,10304
Ta 146,21,15424
Mvaa 172,156,212
s_mul_i32 s66, s58, 320
s_add_u32 s72, s72, s66
Ta 148,21,20544
Mvaa 164,156,196
s_mul_i32 s75, s67, 20480
D 16,40,94,0
Ta 150,21,25664
Mvaa 164,152,192
s_waitcnt vmcnt(5) lgkmcnt(8)
s_add_u32 s95, s92, 15
s_and_b32 s95, s95, 15
Mvaa 96,128,216
s_mul_i32 s95, s95, 40960
s_mul_i32 s71, s51, 0
s_add_u32 s71, s71, 102400
Mvaa 96,132,220
s_mul_i32 s66, s58, 2560
Ta 152,22,10336
Ta 154,22,15456
Mvaa 104,132,236
s_add_u32 s75, s75, s66
s_add_u32 s75, s75, 131072
D 17,40,94,1024
Mvaa 104,128,232
W 6
Ta 156,22,20576
Ta 158,22,25696
Mvaa 108,136,232
s_xor_b32 s67, s61, 124
s_mul_i32 s73, s67, 20480
Ta 128,21,10368
Mvaa 108,140,236
s_mul_i32 s66, s58, 320
s_add_u32 s73, s73, s66
Ta 130,21,15488
Mvaa 100,140,220
s_mul_i32 s76, s67, 20480
D 18,40,94,2048
Ta 132,21,20608
Mvaa 100,136,216
W 7
Ta 134,21,25728
Ta 136,22,10400
Mvaa 112,144,216
s_mul_i32 s66, s58, 2560
Ta 138,22,15520
Mvaa 112,148,220
s_add_u32 s76, s76, s66
Ta 140,22,20640
Ta 142,22,25760
Mvaa 120,148,236
s_add_u32 s76, s76, 131072
D 19,40,94,3072
Mvaa 120,144,232
W 8
s_add_u32 m0, m0, 4096
s_bitcmp1_b32 s14, 0
s_cselect_b32 s74, s73, s72
s_cselect_b32 s77, s76, s75
Mvaa 124,152,232
s_add_u32 s74, s74, 256
Ta 144,21,10432
Ta 146,21,15552
Mvaa 124,156,236
s_add_u32 s77, s77, 2048
Ta 148,21,20672
Ta 150,21,25792
Mvaa 116,156,220
buffer_load_dwordx4 v20, s[40:43], s94 offen lds
s_add_u32 m0, s71, s48
s_lshl2_add_u32 s94, s48, s95
D 16,40,94,0
D 17,40,94,1024
D 18,40,94,2048
D 19,40,94,3072
s_add_u32 m0, m0, 4096
s_nop 0
buffer_load_dwordx4 v20, s[40:43], s94 offen lds
Mvaa 116,152,216
W 8
s_add_u32 s81, s58, 2
s_and_b32 s81, s81, 3
Mvaa 128,128,216
s_lshl_b32 s81, s81, 12
s_add_u32 s81, s81, s49
s_lshl_b32 s66, s14, 6
Mvaa 128,132,220
s_lshl_b32 s67, s14, 9
Ta 152,22,10464
Ta 154,22,15584
Mvaa 136,132,236
s_add_u32 s72, s72, s66
s_add_u32 s75, s75, s67
Ta 156,22,20704
Mvaa 136,128,232
W 7
s_add_u32 s73, s73, s66
Ta 158,22,25824
Mvaa 140,136,232
s_barrier
Mvaa 140,140,236
s_add_u32 s76, s76, s67
Ta 128,21,10496
Ta 130,21,15616
Mvaa 132,140,220
buffer_load_dwordx4 a[0:3], v1, s[8:11], s81 offen offset:0 sc1
v_add_u32 v19, 4096, v1
Mvaa 132,136,216
W 6
s_add_u32 s82, s81, 131072
Ta 132,21,20736
Mvaa 144,144,216
Ta 134,21,25856
Ta 136,22,10528
Mvaa 144,148,220
Ta 138,22,15648
Ta 140,22,20768
Mvaa 152,148,236
buffer_load_dwordx4 a[16:19], v1, s[8:11], s82 offen offset:0 sc1
Ta 142,22,25888
Mvaa 152,144,232
W 8
Ta 144,21,30720
Ta 146,21,35840
Mvaa 156,152,232
Mvaa 156,156,236
Ta 148,23,0
Ta 150,23,5120
Mvaa 148,156,220
buffer_load_dwordx4 a[4:7], v1, s[8:11], s81 offen offset:1024 sc1
Mvaa 148,152,216
W 8
Ta 152,22,30752
Ta 154,22,35872
Mvaa 160,128,216
Mvaa 160,132,220
Ta 156,24,32
Ta 158,24,5152
Mvaa 168,132,236
buffer_load_dwordx4 a[20:23], v1, s[8:11], s82 offen offset:1024 sc1
Mvaa 168,128,232
W 8
Ta 128,21,30784
v_bfe_u32 v27, v1, 4, 3
Mvaa 172,136,232
Ta 130,21,35904
Mvaa 172,140,236
Ta 132,23,64
Ta 134,23,5184
Mvaa 164,140,220
buffer_load_dwordx4 a[8:11], v1, s[8:11], s81 offen offset:2048 sc1
Mvaa 164,136,216
s_waitcnt vmcnt(15) lgkmcnt(8)
Ta 136,22,30816
Ta 138,22,35936
Mvaa 96,144,224
Mvaa 96,148,228
Ta 140,24,96
Ta 142,24,5216
Mvaa 104,148,244
buffer_load_dwordx4 a[24:27], v1, s[8:11], s82 offen offset:2048 sc1
Mvaa 104,144,240
W 8
Ta 144,21,30848
Ta 146,21,35968
Mvaa 108,152,240
Mvaa 108,156,244
Ta 148,23,128
Ta 150,23,5248
Mvaa 100,156,228
buffer_load_dwordx4 a[12:15], v1, s[8:11], s81 offen offset:3072 sc1
Mvaa 100,152,224
W 8
Ta 152,22,30880
Ta 154,22,36000
Mvaa 112,128,224
Mvaa 112,132,228
Ta 156,24,160
v_lshlrev_b32 v27, 3, v27
Mvaa 120,132,244
buffer_load_dwordx4 a[28:31], v1, s[8:11], s82 offen offset:3072 sc1
Ta 158,24,5280
Mvaa 120,128,240
W 8
s_add_u32 s81, s58, 3
s_and_b32 s81, s81, 3
Mvaa 124,136,240
s_lshl_b32 s81, s81, 12
s_add_u32 s81, s81, s49
Ta 128,21,30912
Mvaa 124,140,244
Ta 130,21,36032
Ta 132,23,192
Mvaa 116,140,228
buffer_load_dwordx4 a[32:35], v1, s[8:11], s81 offen offset:0 sc1
Ta 134,23,5312
Mvaa 116,136,224
W 8
s_add_u32 s82, s81, 131072
Ta 136,22,30944
Mvaa 128,144,224
Ta 138,22,36064
Mvaa 128,148,228
)"
R"(Ta 140,24,224
Ta 142,24,5344
Mvaa 136,148,244
buffer_load_dwordx4 a[48:51], v1, s[8:11], s82 offen offset:0 sc1
Mvaa 136,144,240
W 8
Ta 144,21,30976
Ta 146,21,36096
Mvaa 140,152,240
s_waitcnt vmcnt(10)
s_barrier
Mvaa 140,156,244
Ta 148,23,256
Ta 150,23,5376
Mvaa 132,156,228
buffer_load_dwordx4 a[36:39], v1, s[8:11], s81 offen offset:1024 sc1
Mvaa 132,152,224
W 8
Ta 152,22,31008
Ta 154,22,36128
Mvaa 144,128,224
v_bfe_u32 v18, v1, 7, 1
Mvaa 144,132,228
Ta 156,24,288
Ta 158,24,5408
Mvaa 152,132,244
buffer_load_dwordx4 a[52:55], v1, s[8:11], s82 offen offset:1024 sc1
Mvaa 152,128,240
W 8
Ta 128,23,20480
Ta 130,23,25600
Mvaa 156,136,240
Mvaa 156,140,244
Ta 132,23,51200
Ta 134,23,56320
Mvaa 148,140,228
buffer_load_dwordx4 a[40:43], v1, s[8:11], s81 offen offset:2048 sc1
Mvaa 148,136,224
W 8
Ta 136,24,20512
Ta 138,24,25632
Mvaa 160,144,224
Mvaa 160,148,228
Ta 140,24,51232
Ta 142,24,56352
Mvaa 168,148,244
buffer_load_dwordx4 a[56:59], v1, s[8:11], s82 offen offset:2048 sc1
Mvaa 168,144,240
W 8
Ta 144,23,20544
Ta 146,23,25664
Mvaa 172,152,240
Mvaa 172,156,244
Ta 148,23,51264
Ta 150,23,56384
Mvaa 164,156,228
buffer_load_dwordx4 a[44:47], v1, s[8:11], s81 offen offset:3072 sc1
v_lshl_add_u32 v18, v18, 9, v27
Mvaa 164,152,224
s_waitcnt vmcnt(11) lgkmcnt(8)
Ta 152,24,20576
Ta 154,24,25696
Mvaa 96,128,0
Mvaa 96,132,4
Ta 156,24,51296
Ta 158,24,56416
Mvaa 104,132,20
buffer_load_dwordx4 a[60:63], v1, s[8:11], s82 offen offset:3072 sc1
Mvaa 104,128,16
W 8
Ta 128,23,20608
Ta 130,23,25728
Mvaa 108,136,16
Mvaa 108,140,20
Ta 132,23,51328
Ta 134,23,56448
Mvaa 100,140,4
Mvaa 100,136,0
W 8
Ta 136,24,20640
Ta 138,24,25760
Mvaa 112,144,0
v_bfe_u32 v28, v1, 4, 2
Mvaa 112,148,4
Ta 140,24,51360
v_lshlrev_b32 v28, 3, v28
Mvaa 120,148,20
Ta 142,24,56480
buffer_load_dwordx2 v[28:29], v28, s[8:11], s55 offen sc1
Mvaa 120,144,16
W 8
Ta 144,23,20672
Ta 146,23,25792
Mvaa 124,152,16
s_mov_b32 s71, 0
Mvaa 124,156,20
Ta 148,23,51392
Ta 150,23,56512
Mvaa 116,156,4
Mvaa 116,152,0
W 8
Ta 152,24,20704
Ta 154,24,25824
Mvaa 128,128,0
Mvaa 128,132,4
Ta 156,24,51424
Ta 158,24,56544
Mvaa 136,132,20
Mvaa 136,128,16
W 8
Ta 128,23,20736
Ta 130,23,25856
Mvaa 140,136,16
Mvaa 140,140,20
Ta 132,23,51456
Ta 134,23,56576
Mvaa 132,140,4
Mvaa 132,136,0
W 8
Ta 136,24,20768
Ta 138,24,25888
Mvaa 144,144,0
Mvaa 144,148,4
Ta 140,24,51488
Ta 142,24,56608
Mvaa 152,148,20
Mvaa 152,144,16
W 8
Ta 144,25,0
v_bfe_u32 v20, v1, 4, 4
Mvaa 156,152,16
Ta 146,25,5120
Mvaa 156,156,20
Ta 148,21,40960
Ta 150,21,46080
Mvaa 148,156,4
Mvaa 148,152,0
W 8
Ta 152,26,32
Ta 154,26,5152
Mvaa 160,128,0
Mvaa 160,132,4
Ta 156,22,40992
Ta 158,22,46112
Mvaa 168,132,20
Mvaa 168,128,16
W 8
Ta 128,25,64
Ta 130,25,5184
Mvaa 172,136,16
Mvaa 172,140,20
Ta 132,21,41024
Ta 134,21,46144
Mvaa 164,140,4
Mvaa 164,136,0
s_waitcnt vmcnt(9) lgkmcnt(8)
Ta 136,26,96
Ta 138,26,5216
Mvaa 96,144,8
Mvaa 96,148,12
Ta 140,22,41056
v_mul_u32_u24 v20, 1280, v20
Mvaa 104,148,28
Ta 142,22,46176
Mvaa 104,144,24
W 8
Ta 144,25,128
Ta 146,25,5248
Mvaa 108,152,24
Mvaa 108,156,28
Ta 148,21,41088
Ta 150,21,46208
Mvaa 100,156,12
Mvaa 100,152,8
W 8
Ta 152,26,160
Ta 154,26,5280
Mvaa 112,128,8
Mvaa 112,132,12
Ta 156,22,41120
Ta 158,22,46240
Mvaa 120,132,28
s_waitcnt vmcnt(0)
v_cmp_eq_u64 vcc, s[12:13], v[28:29]
s_cmp_eq_u64 vcc, -1
s_cbranch_scc0 .Lspin_101%=
.Lok_102%=:
s_add_u32 s81, s58, 1
s_and_b32 s81, s81, 3
Mvaa 120,128,24
W 8
s_lshl_b32 s81, s81, 12
s_add_u32 s81, s81, s50
Mvaa 124,136,24
buffer_load_dwordx4 a[64:67], v1, s[8:11], s81 offen offset:0 sc1
Ta 128,25,192
Mvaa 124,140,28
s_add_u32 s82, s81, 131072
Ta 130,25,5312
Ta 132,21,41152
Mvaa 116,140,12
Ta 134,21,46272
Mvaa 116,136,8
W 8
Ta 136,26,224
Ta 138,26,5344
Mvaa 128,144,8
buffer_load_dwordx4 a[80:83], v1, s[8:11], s82 offen offset:0 sc1
v_lshrrev_b32 v12, 8, v1
Mvaa 128,148,12
Ta 140,22,41184
Ta 142,22,46304
Mvaa 136,148,28
Mvaa 136,144,24
W 8
Ta 144,25,256
Ta 146,25,5376
Mvaa 140,152,24
Mvaa 140,156,28
buffer_load_dwordx4 a[68:71], v1, s[8:11], s81 offen offset:1024 sc1
Ta 148,21,41216
Mvaa 132,156,12
Ta 150,21,46336
Mvaa 132,152,8
W 8
Ta 152,26,288
Ta 154,26,5408
Mvaa 144,128,8
Mvaa 144,132,12
Ta 156,22,41248
Ta 158,22,46368
Mvaa 152,132,28
buffer_load_dwordx4 a[84:87], v1, s[8:11], s82 offen offset:1024 sc1
Mvaa 152,128,24
W 8
Ta 128,21,51200
Ta 130,21,56320
Mvaa 156,136,24
Mvaa 156,140,28
Ta 132,23,10240
Ta 134,23,15360
Mvaa 148,140,12
v_lshl_add_u32 v20, v12, 4, v20
Mvaa 148,136,8
W 8
buffer_load_dwordx4 a[72:75], v1, s[8:11], s81 offen offset:2048 sc1
Ta 136,23,30720
Mvaa 160,144,8
Ta 138,23,35840
Mvaa 160,148,12
Ta 140,23,40960
Ta 142,23,46080
Mvaa 168,148,28
Mvaa 168,144,24
W 8
Ta 144,22,51232
Ta 146,22,56352
Mvaa 172,152,24
buffer_load_dwordx4 a[88:91], v1, s[8:11], s82 offen offset:2048 sc1
Mvaa 172,156,28
Ta 148,24,10272
Ta 150,24,15392
Mvaa 164,156,12
Mvaa 164,152,8
W 8
Ta 152,24,30752
Ta 154,24,35872
Mvaa 96,128,32
Mvaa 96,132,36
buffer_load_dwordx4 a[76:79], v1, s[8:11], s81 offen offset:3072 sc1
Ta 156,24,40992
Mvaa 104,132,52
Ta 158,24,46112
Mvaa 104,128,48
W 8
Ta 128,21,51264
Ta 130,21,56384
Mvaa 104,136,56
v_bfe_u32 v15, v1, 7, 1
Mvaa 104,140,60
Ta 132,23,10304
Ta 134,23,15424
Mvaa 96,140,44
buffer_load_dwordx4 a[92:95], v1, s[8:11], s82 offen offset:3072 sc1
Mvaa 96,136,40
W 8
s_add_u32 s81, s58, 2
s_and_b32 s81, s81, 3
Mvaa 100,144,32
s_lshl_b32 s81, s81, 12
s_add_u32 s81, s81, s50
Ta 136,23,30784
Mvaa 100,148,36
Ta 138,23,35904
Ta 140,23,41024
Mvaa 108,148,52
Ta 142,23,46144
buffer_load_dwordx4 a[96:99], v1, s[8:11], s81 offen offset:0 sc1
Mvaa 108,144,48
W 8
s_add_u32 s82, s81, 131072
Ta 144,22,51296
Mvaa 108,152,56
Ta 146,22,56416
Mvaa 108,156,60
Ta 148,24,10336
Ta 150,24,15456
Mvaa 100,156,44
Mvaa 100,152,40
W 8
Ta 152,24,30816
Ta 154,24,35936
Mvaa 112,128,32
buffer_load_dwordx4 a[112:115], v1, s[8:11], s82 offen offset:0 sc1
Mvaa 112,132,36
Ta 156,24,41056
Ta 158,24,46176
Mvaa 120,132,52
K 96,96,97
K 97,98,99
Mvaa 120,128,48
W 8
Ta 128,21,51328
v_mul_u32_u24 v14, 10240, v15
Mvaa 120,136,56
Ta 130,21,56448
K 98,100,101
Mvaa 120,140,60
buffer_load_dwordx4 a[100:103], v1, s[8:11], s81 offen offset:1024 sc1
K 99,102,103
Mvaa 112,140,44
Ta 132,23,10368
Ta 134,23,15488
Mvaa 112,136,40
W 8
s_add_u32 s83, s28, 0
Ta 136,23,30848
Mvaa 116,144,32
Ta 138,23,35968
Mvaa 116,148,36
buffer_store_dwordx4 v[96:99], v1, s[8:11], s83 offen sc1
Ta 140,23,41088
Mvaa 124,148,52
Ta 142,23,46208
K 104,104,105
Mvaa 124,144,48
W 8
Ta 144,22,51360
K 105,106,107
Mvaa 124,152,56
Ta 146,22,56480
Mvaa 124,156,60
buffer_load_dwordx4 a[116:119], v1, s[8:11], s82 offen offset:1024 sc1
K 106,108,109
Mvaa 116,156,44
Ta 148,24,10400
Ta 150,24,15520
Mvaa 116,152,40
W 8
Ta 152,24,30880
K 107,110,111
Mvaa 128,128,32
s_add_u32 s83, s29, 40960
Ta 154,24,36000
Mvaa 128,132,36
buffer_load_dwordx4 a[104:107], v1, s[8:11], s81 offen offset:2048 sc1
v_lshrrev_b32 v15, 8, v1
Mvaa 136,132,52
Ta 156,24,41120
Ta 158,24,46240
Mvaa 136,128,48
W 8
Ta 128,21,51392
Ta 130,21,56512
Mvaa 136,136,56
Mvaa 136,140,60
buffer_store_dwordx4 v[104:107], v1, s[8:11], s83 offen sc1
K 112,112,113
Mvaa 128,140,44
Ta 132,23,10432
K 113,114,115
Mvaa 128,136,40
W 7
Ta 134,23,15552
K 114,116,117
Mvaa 132,144,32
Ta 136,23,30912
Ta 138,23,36032
Mvaa 132,148,36
buffer_load_dwordx4 a[120:123], v1, s[8:11], s82 offen offset:2048 sc1
K 115,118,119
Mvaa 140,148,52
s_add_u32 s83, s28, 8192
Ta 140,23,41152
Ta 142,23,46272
Mvaa 140,144,48
W 8
Ta 144,22,51424
Ta 146,22,56544
Mvaa 140,152,56
)"
R"(Mvaa 140,156,60
buffer_load_dwordx4 a[108:111], v1, s[8:11], s81 offen offset:3072 sc1
Ta 148,24,10464
Mvaa 132,156,44
Ta 150,24,15584
Mvaa 132,152,40
W 8
Ta 152,24,30944
v_lshl_add_u32 v14, v15, 7, v14
Mvaa 144,128,32
Ta 154,24,36064
Mvaa 144,132,36
buffer_store_dwordx4 v[112:115], v1, s[8:11], s83 offen sc1
K 120,120,121
Mvaa 152,132,52
K 121,122,123
K 122,124,125
Mvaa 152,128,48
W 6
Ta 156,24,41184
K 123,126,127
Mvaa 152,136,56
s_add_u32 s83, s29, 49152
Ta 158,24,46304
Ta 128,21,51456
Mvaa 152,140,60
buffer_load_dwordx4 a[124:127], v1, s[8:11], s82 offen offset:3072 sc1
Ta 130,21,56576
Mvaa 144,140,44
s_add_u32 s81, s58, 3
s_and_b32 s81, s81, 3
s_lshl_b32 s81, s81, 12
Mvaa 144,136,40
W 6
s_add_u32 s81, s81, s50
Ta 132,23,10496
Mvaa 148,144,32
Ta 134,23,15616
Ta 136,23,30976
Mvaa 148,148,36
buffer_load_dwordx4 a[184:187], v1, s[8:11], s81 offen offset:0 sc1
Ta 138,23,36096
Mvaa 156,148,52
s_add_u32 s82, s81, 131072
Ta 140,23,41216
Ta 142,23,46336
Mvaa 156,144,48
W 8
Ta 144,22,51488
Ta 146,22,56608
Mvaa 156,152,56
Mvaa 156,156,60
buffer_store_dwordx4 v[120:123], v1, s[8:11], s83 offen sc1
K 128,128,129
Mvaa 148,156,44
K 129,130,131
K 130,132,133
Mvaa 148,152,40
W 6
Ta 148,24,10528
K 131,134,135
Mvaa 160,128,32
s_add_u32 s83, s28, 16384
Ta 150,24,15648
v_and_b32 v15, 0x70, v1
Mvaa 160,132,36
buffer_load_dwordx4 a[200:203], v1, s[8:11], s82 offen offset:0 sc1
Ta 152,24,31008
Mvaa 168,132,52
Ta 154,24,36128
Ta 156,24,41248
Mvaa 168,128,48
W 7
Ta 158,24,46368
Ta 128,21,10240
Mvaa 168,136,56
Ta 130,21,15360
Mvaa 168,140,60
buffer_load_dwordx4 a[188:191], v1, s[8:11], s81 offen offset:1024 sc1
Ta 132,21,20480
Mvaa 160,140,44
Ta 134,21,25600
Mvaa 160,136,40
W 8
Ta 136,22,10272
Ta 138,22,15392
Mvaa 164,144,32
Mvaa 164,148,36
buffer_store_dwordx4 v[128:131], v1, s[8:11], s83 offen sc1
K 136,136,137
Mvaa 172,148,52
K 137,138,139
K 138,140,141
Mvaa 172,144,48
W 6
Ta 140,22,20512
K 139,142,143
Mvaa 172,152,56
s_add_u32 s83, s29, 57344
Ta 142,22,25632
Ta 144,21,10304
Mvaa 172,156,60
buffer_load_dwordx4 a[204:207], v1, s[8:11], s82 offen offset:1024 sc1
Ta 146,21,15424
Mvaa 164,156,44
Ta 148,21,20544
v_add_u32 v14, v14, v15
Mvaa 164,152,40
s_waitcnt vmcnt(21) lgkmcnt(7)
Ta 150,21,25664
Ta 152,22,10336
Mvaa 176,128,64
Ta 154,22,15456
Mvaa 176,132,68
buffer_load_dwordx4 a[192:195], v1, s[8:11], s81 offen offset:2048 sc1
Ta 156,22,20576
Mvaa 184,132,84
Ta 158,22,25696
Mvaa 184,128,80
W 8
Ta 128,21,10368
Ta 130,21,15488
Mvaa 188,136,80
Mvaa 188,140,84
buffer_store_dwordx4 v[136:139], v1, s[8:11], s83 offen sc1
K 144,144,145
Mvaa 180,140,68
K 145,146,147
K 146,148,149
Mvaa 180,136,64
W 6
Ta 132,21,20608
K 147,150,151
Mvaa 192,144,64
s_add_u32 s83, s28, 24576
Ta 134,21,25728
Ta 136,22,10400
Mvaa 192,148,68
buffer_load_dwordx4 a[208:211], v1, s[8:11], s82 offen offset:2048 sc1
Ta 138,22,15520
Mvaa 200,148,84
Ta 140,22,20640
Ta 142,22,25760
Mvaa 200,144,80
W 8
Ta 144,21,10432
Ta 146,21,15552
Mvaa 204,152,80
Mvaa 204,156,84
buffer_store_dwordx4 v[144:147], v1, s[8:11], s83 offen sc1
K 152,152,153
Mvaa 196,156,68
K 153,154,155
K 154,156,157
Mvaa 196,152,64
W 6
Ta 148,21,20672
K 155,158,159
Mvaa 208,128,64
s_add_u32 s83, s29, 65536
Ta 150,21,25792
Ta 152,22,10464
Mvaa 208,132,68
buffer_load_dwordx4 a[196:199], v1, s[8:11], s81 offen offset:3072 sc1
Ta 154,22,15584
Mvaa 216,132,84
Ta 156,22,20704
Ta 158,22,25824
Mvaa 216,128,80
W 8
Ta 128,21,10496
Ta 130,21,15616
Mvaa 220,136,80
Mvaa 220,140,84
buffer_load_dwordx4 a[212:215], v1, s[8:11], s82 offen offset:3072 sc1
Ta 132,21,20736
Mvaa 212,140,68
Ta 134,21,25856
Mvaa 212,136,64
W 8
Ta 136,22,10528
Ta 138,22,15648
Mvaa 224,144,64
Mvaa 224,148,68
buffer_store_dwordx4 v[152:155], v1, s[8:11], s83 offen sc1
K 160,160,161
Mvaa 232,148,84
K 161,162,163
K 162,164,165
Mvaa 232,144,80
W 6
Ta 140,22,20768
K 163,166,167
Mvaa 236,152,80
s_add_u32 s83, s28, 32768
Ta 142,22,25888
Ta 144,21,30720
Mvaa 236,156,84
buffer_load_dwordx4 v[36:39], v20, s[16:19], s72 offen
Ta 146,21,35840
Mvaa 228,156,68
Ta 148,23,0
Ta 150,23,5120
Mvaa 228,152,64
W 8
Ta 152,22,30752
Ta 154,22,35872
Mvaa 240,128,64
Mvaa 240,132,68
buffer_load_dwordx4 v[44:47], v14, s[8:11], s75 offen sc1
Ta 156,24,32
Mvaa 248,132,84
Ta 158,24,5152
Mvaa 248,128,80
W 8
Ta 128,21,30784
Ta 130,21,35904
Mvaa 252,136,80
Mvaa 252,140,84
buffer_store_dwordx4 v[160:163], v1, s[8:11], s83 offen sc1
K 168,168,169
Mvaa 244,140,68
K 169,170,171
K 170,172,173
Mvaa 244,136,64
s_waitcnt vmcnt(27) lgkmcnt(6)
Ta 132,23,64
K 171,174,175
Mvaa 176,144,72
s_add_u32 s83, s29, 73728
Ta 134,23,5184
Ta 136,22,30816
Mvaa 176,148,76
buffer_load_dwordx4 v[52:55], v20, s[16:19], s73 offen
Ta 138,22,35936
Mvaa 184,148,92
Ta 140,24,96
Ta 142,24,5216
Mvaa 184,144,88
W 8
Ta 144,21,30848
Ta 146,21,35968
Mvaa 188,152,88
Mvaa 188,156,92
buffer_store_dwordx4 v[168:171], v1, s[8:11], s83 offen sc1
Ta 148,23,128
Mvaa 180,156,76
Ta 150,23,5248
Mvaa 180,152,72
W 8
Ta 152,22,30880
Ta 154,22,36000
Mvaa 192,128,72
Mvaa 192,132,76
buffer_load_dwordx4 v[60:63], v14, s[8:11], s76 offen sc1
Ta 156,24,160
Mvaa 200,132,92
Ta 158,24,5280
Mvaa 200,128,88
W 8
Ta 128,21,30912
Ta 130,21,36032
Mvaa 204,136,88
Mvaa 204,140,92
buffer_load_dwordx4 v[68:71], v20, s[16:19], s74 offen
Ta 132,23,192
Mvaa 196,140,76
Ta 134,23,5312
Mvaa 196,136,72
W 8
Ta 136,22,30944
Ta 138,22,36064
Mvaa 208,144,72
Mvaa 208,148,76
buffer_load_dwordx4 v[76:79], v14, s[8:11], s77 offen sc1
Ta 140,24,224
Mvaa 216,148,92
Ta 142,24,5344
Mvaa 216,144,88
W 8
Ta 144,21,30976
Ta 146,21,36096
Mvaa 220,152,88
Mvaa 220,156,92
buffer_load_dwordx2 v[12:13], v27, s[8:11], 0 offen offset:3584 sc1
Ta 148,23,256
Mvaa 212,156,76
Ta 150,23,5376
Mvaa 212,152,72
W 8
Ta 152,22,31008
Ta 154,22,36128
Mvaa 224,128,72
Mvaa 224,132,76
Ta 156,24,288
Ta 158,24,5408
Mvaa 232,132,92
Mvaa 232,128,88
W 8
Ta 128,23,20480
Ta 130,23,25600
Mvaa 236,136,88
Mvaa 236,140,92
Ta 132,23,51200
Ta 134,23,56320
Mvaa 228,140,76
Mvaa 228,136,72
W 8
Ta 136,25,0
Ta 138,25,5120
Mvaa 240,144,72
Mvaa 240,148,76
Ta 140,21,40960
Ta 142,21,46080
Mvaa 248,148,92
Mvaa 248,144,88
W 8
Ta 144,21,51200
Ta 146,21,56320
Mvaa 252,152,88
Mvaa 252,156,92
Ta 148,23,10240
Ta 150,23,15360
Mvaa 244,156,76
Mvaa 244,152,72
s_waitcnt vmcnt(28) lgkmcnt(8)
Ta 152,23,30720
Ta 154,23,35840
Mvaa 176,128,96
Mvaa 176,132,100
Ta 156,23,40960
Ta 158,23,46080
Mvaa 184,132,116
Mvaa 184,128,112
s_waitcnt vmcnt(22) lgkmcnt(8)
Ta 128,24,20512
Ta 130,24,25632
Mvaa 184,136,120
Mvaa 184,140,124
Ta 132,24,51232
Ta 134,24,56352
Mvaa 176,140,108
Mvaa 176,136,104
s_waitcnt vmcnt(16) lgkmcnt(8)
Ta 136,26,32
Ta 138,26,5152
Mvaa 176,144,184
Mvaa 176,148,188
Ta 140,22,40992
Ta 142,22,46112
Mvaa 184,148,204
s_waitcnt vmcnt(4)
v_lshlrev_b32 v2, 2, v1
v_mov_b64 v[28:29], s[12:13]
s_mov_b64 exec, 3
buffer_store_dwordx2 v[28:29], v2, s[8:11], s53 offen offset:0 sc1
s_mov_b64 exec, -1
Mvaa 184,144,200
W 8
Ta 144,22,51232
Ta 146,22,56352
Mvaa 184,152,208
Mvaa 184,156,212
Ta 148,24,10272
Ta 150,24,15392
Mvaa 176,156,196
Mvaa 176,152,192
W 8
Ta 152,24,30752
Ta 154,24,35872
Mvaa 180,128,96
Mvaa 180,132,100
Ta 156,24,40992
Ta 158,24,46112
Mvaa 188,132,116
Mvaa 188,128,112
W 8
Ta 128,23,20544
Ta 130,23,25664
Mvaa 188,136,120
Mvaa 188,140,124
Ta 132,23,51264
Ta 134,23,56384
Mvaa 180,140,108
Mvaa 180,136,104
W 8
Ta 136,25,64
Ta 138,25,5184
Mvaa 180,144,184
Mvaa 180,148,188
Ta 140,21,41024
Ta 142,21,46144
Mvaa 188,148,204
Mvaa 188,144,200
W 8
Ta 144,21,51264
Ta 146,21,56384
Mvaa 188,152,208
Mvaa 188,156,212
Ta 148,23,10304
)"
R"(Ta 150,23,15424
Mvaa 180,156,196
s_waitcnt vmcnt(1)
v_xor_b32 v12, s12, v12
v_xor_b32 v13, s13, v13
v_lshrrev_b32 v3, 11, v12
v_or_b32 v13, v13, v3
v_cmp_eq_u32 vcc, 0, v13
s_cmp_eq_u64 vcc, -1
s_cbranch_scc0 .LL_106%=
.LL_107%=:
v_bfe_u32 v13, v12, 4, 7
s_xor_b32 s66, s61, 124
v_cmp_eq_u32 vcc, s61, v13
v_cmp_eq_u32_e64 s[86:87], s66, v13
s_or_b32 s68, vcc_lo, s86
s_and_b32 s68, s68, 255
s_cbranch_scc1 .Lup_104%=
.Lupr_105%=:
Mvaa 180,152,192
W 8
Ta 152,23,30784
Ta 154,23,35904
Mvaa 192,128,96
v_lshlrev_b32 v32, 16, v36
Mvaa 192,132,100
Ta 156,23,41024
v_and_b32 v33, 0xffff0000, v36
Mvaa 200,132,116
Ta 158,23,46144
v_lshlrev_b32 v34, 16, v37
Mvaa 200,128,112
W 8
K 176,176,177
v_and_b32 v35, 0xffff0000, v37
Mvaa 200,136,120
Ta 128,24,20576
Ta 130,24,25696
Mvaa 200,140,124
buffer_load_dwordx2 v[16:17], v18, s[8:11], s63 offen sc1
v_lshlrev_b32 v36, 16, v38
Mvaa 192,140,108
K 177,178,179
v_and_b32 v37, 0xffff0000, v38
Mvaa 192,136,104
W 6
Ta 132,24,51296
K 178,180,181
Mvaa 192,144,184
Ta 134,24,56416
v_lshlrev_b32 v38, 16, v39
Mvaa 192,148,188
K 179,182,183
v_and_b32 v39, 0xffff0000, v39
Mvaa 200,148,204
s_add_u32 s83, s28, 327680
Ta 136,26,96
Ta 138,26,5216
Mvaa 200,144,200
W 6
v_lshlrev_b32 v40, 16, v44
v_and_b32 v41, 0xffff0000, v44
Mvaa 200,152,208
buffer_store_dwordx4 v[176:179], v1, s[8:11], s83 offen sc1
K 184,184,185
Mvaa 200,156,212
K 185,186,187
K 186,188,189
Mvaa 192,156,196
K 187,190,191
s_add_u32 s83, s29, 368640
buffer_store_dwordx4 v[184:187], v1, s[8:11], s83 offen sc1
Mvaa 192,152,192
Ta 140,22,41056
Ta 142,22,46176
W 4
Mvaa 196,128,96
v_lshlrev_b32 v42, 16, v45
v_and_b32 v43, 0xffff0000, v45
Mvaa 196,132,100
Ta 144,22,51296
Ta 146,22,56416
Mvaa 204,132,116
v_lshlrev_b32 v44, 16, v46
v_and_b32 v45, 0xffff0000, v46
Mvaa 204,128,112
Ta 148,24,10336
Ta 150,24,15456
W 4
Mvaa 204,136,120
v_lshlrev_b32 v46, 16, v47
v_and_b32 v47, 0xffff0000, v47
Mvaa 204,140,124
Ta 152,24,30816
Ta 154,24,35936
Mvaa 196,140,108
v_lshlrev_b32 v48, 16, v52
v_and_b32 v49, 0xffff0000, v52
Mvaa 196,136,104
Ta 156,24,41056
Ta 158,24,46176
W 4
Mvaa 196,144,184
v_lshlrev_b32 v50, 16, v53
v_and_b32 v51, 0xffff0000, v53
Mvaa 196,148,188
Ta 128,23,20608
Ta 130,23,25728
Mvaa 204,148,204
v_lshlrev_b32 v52, 16, v54
v_and_b32 v53, 0xffff0000, v54
Mvaa 204,144,200
Ta 132,23,51328
Ta 134,23,56448
W 4
Mvaa 204,152,208
v_lshlrev_b32 v54, 16, v55
v_and_b32 v55, 0xffff0000, v55
Mvaa 204,156,212
Ta 136,25,128
Ta 138,25,5248
Mvaa 196,156,196
v_lshlrev_b32 v56, 16, v60
v_and_b32 v57, 0xffff0000, v60
Mvaa 196,152,192
Ta 140,21,41088
Ta 142,21,46208
W 4
Mvaa 208,128,96
v_lshlrev_b32 v58, 16, v61
v_and_b32 v59, 0xffff0000, v61
Mvaa 208,132,100
Ta 144,21,51328
Ta 146,21,56448
Mvaa 216,132,116
v_lshlrev_b32 v60, 16, v62
v_and_b32 v61, 0xffff0000, v62
Mvaa 216,128,112
Ta 148,23,10368
Ta 150,23,15488
W 4
Mvaa 216,136,120
v_lshlrev_b32 v62, 16, v63
v_and_b32 v63, 0xffff0000, v63
Mvaa 216,140,124
Ta 152,23,30848
Ta 154,23,35968
Mvaa 208,140,108
v_lshlrev_b32 v64, 16, v68
v_and_b32 v65, 0xffff0000, v68
Mvaa 208,136,104
Ta 156,23,41088
Ta 158,23,46208
W 4
Mvaa 208,144,184
v_lshlrev_b32 v66, 16, v69
K 192,192,193
Mvaa 208,148,188
Ta 128,24,20640
v_and_b32 v67, 0xffff0000, v69
Mvaa 216,148,204
Ta 130,24,25760
K 193,194,195
Mvaa 216,144,200
Ta 132,24,51360
Ta 134,24,56480
W 4
Mvaa 216,152,208
v_lshlrev_b32 v68, 16, v70
K 194,196,197
Mvaa 216,156,212
v_and_b32 v69, 0xffff0000, v70
v_lshlrev_b32 v70, 16, v71
Mvaa 208,156,196
K 195,198,199
s_add_u32 s83, s28, 335872
buffer_store_dwordx4 v[192:195], v1, s[8:11], s83 offen sc1
K 200,200,201
K 201,202,203
K 202,204,205
K 203,206,207
s_add_u32 s83, s29, 376832
buffer_store_dwordx4 v[200:203], v1, s[8:11], s83 offen sc1
Mvaa 208,152,192
Ta 136,26,160
Ta 138,26,5280
Ta 140,22,41120
Ta 142,22,46240
W 4
Mvaa 212,128,96
s_waitcnt vmcnt(4)
v_cmp_eq_u64 vcc, s[12:13], v[16:17]
s_and_b32 s71, vcc_lo, 255
s_bitset0_b32 s71, s59
v_and_b32 v71, 0xffff0000, v71
v_lshlrev_b32 v72, 16, v76
Mvaa 212,132,100
s_lshl_b32 s92, s14, 3
s_lshr_b32 s92, s71, s92
s_mov_b32 s91, 134217728
Mvaa 220,132,116
s_bitcmp1_b32 s71, 0
s_cselect_b32 s94, s78, s91
v_and_b32 v73, 0xffff0000, v76
Mvaa 220,128,112
Ta 144,22,51360
Ta 146,22,56480
Ta 148,24,10400
Ta 150,24,15520
W 4
Mvaa 220,136,120
v_lshlrev_b32 v74, 16, v77
v_and_b32 v75, 0xffff0000, v77
Mvaa 220,140,124
v_lshlrev_b32 v76, 16, v78
v_and_b32 v77, 0xffff0000, v78
Mvaa 212,140,108
v_lshlrev_b32 v78, 16, v79
v_and_b32 v79, 0xffff0000, v79
Mvaa 212,136,104
Ta 152,24,30880
Ta 154,24,36000
Ta 156,24,41120
Ta 158,24,46240
W 4
Mvaa 212,144,184
Ta 128,23,20672
Ta 130,23,25792
Mvaa 212,148,188
buffer_load_dwordx4 a[0:3], v1, s[8:11], s94 offen offset:0 sc1
Ta 132,23,51392
Mvaa 220,148,204
Ta 134,23,56512
Ta 136,25,192
Mvaa 220,144,200
W 5
Ta 138,25,5312
Ta 140,21,41152
Mvaa 220,152,208
Ta 142,21,46272
Ta 144,21,51392
Mvaa 220,156,212
s_bitcmp1_b32 s92, 0
s_cbranch_scc0 .LL_109%=
buffer_load_dwordx4 a[216:219], v1, s[8:11], s65 offen offset:0 sc1
.LL_109%=:
Ta 146,21,56512
Mvaa 212,156,196
s_bitcmp1_b32 s71, 1
s_cselect_b32 s94, s78, s91
Ta 148,23,10432
Mvaa 212,152,192
W 7
Ta 150,23,15552
Ta 152,23,30912
Mvaa 224,128,96
Ta 154,23,36032
Mvaa 224,132,100
buffer_load_dwordx4 a[4:7], v1, s[8:11], s94 offen offset:1024 sc1
Ta 156,23,41152
Mvaa 232,132,116
Ta 158,23,46272
K 208,208,209
Mvaa 232,128,112
W 8
Ta 128,24,20704
Ta 130,24,25824
Mvaa 232,136,120
K 209,210,211
Mvaa 232,140,124
s_bitcmp1_b32 s92, 1
s_cbranch_scc0 .LL_110%=
buffer_load_dwordx4 a[220:223], v1, s[8:11], s65 offen offset:1024 sc1
.LL_110%=:
K 210,212,213
Mvaa 224,140,108
s_bitcmp1_b32 s71, 2
s_cselect_b32 s94, s78, s91
Ta 132,24,51424
Mvaa 224,136,104
W 7
Ta 134,24,56544
K 211,214,215
Mvaa 224,144,184
s_add_u32 s83, s28, 344064
Ta 136,26,224
Ta 138,26,5344
Mvaa 224,148,188
buffer_load_dwordx4 a[8:11], v1, s[8:11], s94 offen offset:2048 sc1
Ta 140,22,41184
Mvaa 232,148,204
Ta 142,22,46304
Mvaa 232,144,200
W 8
Ta 144,22,51424
Ta 146,22,56544
Mvaa 232,152,208
Mvaa 232,156,212
buffer_store_dwordx4 v[208:211], v1, s[8:11], s83 offen sc1
K 216,216,217
Mvaa 224,156,196
K 217,218,219
K 218,220,221
K 219,222,223
s_add_u32 s83, s29, 385024
buffer_store_dwordx4 v[216:219], v1, s[8:11], s83 offen sc1
Mvaa 224,152,192
W 6
Ta 148,24,10464
Ta 150,24,15584
Mvaa 228,128,96
Ta 152,24,30944
Ta 154,24,36064
Mvaa 228,132,100
Ta 156,24,41184
Ta 158,24,46304
Mvaa 236,132,116
s_bitcmp1_b32 s92, 2
s_cbranch_scc0 .LL_111%=
buffer_load_dwordx4 a[224:227], v1, s[8:11], s65 offen offset:2048 sc1
.LL_111%=:
Mvaa 236,128,112
W 8
s_bitcmp1_b32 s71, 3
s_cselect_b32 s94, s78, s91
Mvaa 236,136,120
Ta 128,23,20736
Ta 130,23,25856
Mvaa 236,140,124
Ta 132,23,51456
Ta 134,23,56576
Mvaa 228,140,108
buffer_load_dwordx4 a[12:15], v1, s[8:11], s94 offen offset:3072 sc1
Mvaa 228,136,104
W 8
Ta 136,25,256
Ta 138,25,5376
Mvaa 228,144,184
Mvaa 228,148,188
Ta 140,21,41216
Ta 142,21,46336
Mvaa 236,148,204
s_bitcmp1_b32 s92, 3
s_cbranch_scc0 .LL_112%=
buffer_load_dwordx4 a[228:231], v1, s[8:11], s65 offen offset:3072 sc1
.LL_112%=:
Mvaa 236,144,200
W 8
s_bitcmp1_b32 s71, 4
s_cselect_b32 s94, s78, s91
Mvaa 236,152,208
Ta 144,21,51456
Ta 146,21,56576
Mvaa 236,156,212
Ta 148,23,10496
Ta 150,23,15616
Mvaa 228,156,196
buffer_load_dwordx4 a[16:19], v19, s[8:11], s94 offen offset:0 sc1
Mvaa 228,152,192
W 8
Ta 152,23,30976
Ta 154,23,36096
Mvaa 240,128,96
Mvaa 240,132,100
Ta 156,23,41216
Ta 158,23,46336
Mvaa 248,132,116
s_bitcmp1_b32 s92, 4
s_cbranch_scc0 .LL_113%=
buffer_load_dwordx4 a[232:235], v19, s[8:11], s65 offen offset:0 sc1
.LL_113%=:
K 224,224,225
Mvaa 248,128,112
W 8
s_bitcmp1_b32 s71, 5
s_cselect_b32 s94, s78, s91
Mvaa 248,136,120
Ta 128,24,20768
Ta 130,24,25888
Mvaa 248,140,124
K 225,226,227
K 226,228,229
Mvaa 240,140,108
buffer_load_dwordx4 a[20:23], v19, s[8:11], s94 offen offset:1024 sc1
Ta 132,24,51488
Mvaa 240,136,104
W 7
Ta 134,24,56608
K 227,230,231
Mvaa 240,144,184
s_add_u32 s83, s28, 352256
Ta 136,26,288
Ta 138,26,5408
Mvaa 240,148,188
Ta 140,22,41248
Ta 142,22,46368
Mvaa 248,148,204
s_bitcmp1_b32 s92, 5
s_cbranch_scc0 .LL_114%=
buffer_load_dwordx4 a[236:239], v19, s[8:11], s65 offen offset:1024 sc1
)"
R"(.LL_114%=:
Mvaa 248,144,200
W 8
s_bitcmp1_b32 s71, 6
s_cselect_b32 s94, s78, s91
Mvaa 248,152,208
Ta 144,22,51488
Ta 146,22,56608
Mvaa 248,156,212
Ta 148,24,10528
Ta 150,24,15648
Mvaa 240,156,196
buffer_store_dwordx4 v[224:227], v1, s[8:11], s83 offen sc1
K 232,232,233
K 233,234,235
K 234,236,237
K 235,238,239
s_add_u32 s83, s29, 393216
buffer_store_dwordx4 v[232:235], v1, s[8:11], s83 offen sc1
Mvaa 240,152,192
W 8
Ta 152,24,31008
Ta 154,24,36128
Mvaa 244,128,96
Mvaa 244,132,100
Ta 156,24,41248
Ta 158,24,46368
Mvaa 252,132,116
buffer_load_dwordx4 a[24:27], v19, s[8:11], s94 offen offset:2048 sc1
Mvaa 252,128,112
W 8
Mvaa 252,136,120
Mvaa 252,140,124
Mvaa 244,140,108
s_bitcmp1_b32 s92, 6
s_cbranch_scc0 .LL_115%=
buffer_load_dwordx4 a[240:243], v19, s[8:11], s65 offen offset:2048 sc1
.LL_115%=:
Mvaa 244,136,104
W 4
s_bitcmp1_b32 s71, 7
s_cselect_b32 s94, s78, s91
Mvaa 244,144,184
Mvaa 244,148,188
Mvaa 252,148,204
buffer_load_dwordx4 a[28:31], v19, s[8:11], s94 offen offset:3072 sc1
Mvaa 252,144,200
W 0
Mvaa 252,152,208
Mvaa 252,156,212
Mvaa 244,156,196
s_bitcmp1_b32 s92, 7
s_cbranch_scc0 .LL_116%=
buffer_load_dwordx4 a[244:247], v19, s[8:11], s65 offen offset:3072 sc1
.LL_116%=:
Mvaa 244,152,192
K 240,240,241
K 241,242,243
s_nop 5
K 242,244,245
K 243,246,247
s_add_u32 s83, s28, 360448
buffer_store_dwordx4 v[240:243], v1, s[8:11], s83 offen sc1
K 248,248,249
K 249,250,251
K 250,252,253
K 251,254,255
s_add_u32 s83, s29, 401408
buffer_store_dwordx4 v[248:251], v1, s[8:11], s83 offen sc1
.Ltend_81%=:
buffer_load_dwordx2 v[16:17], v18, s[8:11], s63 offen sc1
s_barrier
s_bitcmp1_b32 s28, 27
s_cbranch_scc0 .LL_117%=
ds_write_b128 v1, v[96:99] offset:0
ds_write_b128 v1, v[112:115] offset:1024
ds_write_b128 v1, v[128:131] offset:2048
ds_write_b128 v1, v[144:147] offset:3072
ds_write_b128 v1, v[160:163] offset:4096
ds_write_b128 v1, v[176:179] offset:5120
ds_write_b128 v1, v[192:195] offset:6144
ds_write_b128 v1, v[208:211] offset:7168
ds_write_b128 v1, v[224:227] offset:8192
ds_write_b128 v1, v[240:243] offset:9216
W 0
.LL_117%=:
s_bitcmp1_b32 s29, 27
s_cbranch_scc0 .LL_118%=
ds_write_b128 v1, v[104:107] offset:0
ds_write_b128 v1, v[120:123] offset:1024
ds_write_b128 v1, v[136:139] offset:2048
ds_write_b128 v1, v[152:155] offset:3072
ds_write_b128 v1, v[168:171] offset:4096
ds_write_b128 v1, v[184:187] offset:5120
ds_write_b128 v1, v[200:203] offset:6144
ds_write_b128 v1, v[216:219] offset:7168
ds_write_b128 v1, v[232:235] offset:8192
ds_write_b128 v1, v[248:251] offset:9216
W 0
.LL_118%=:
s_lshl_b32 s80, s14, 10
v_add_u32 v21, s80, v1
s_cmp_lg_u32 s68, 0
s_cbranch_scc1 .Lup_119%=
.Lupr_120%=:
s_lshl_b32 s66, 0x101, s59
s_waitcnt vmcnt(1)
s_cmp_eq_u32 s64, 0
s_cbranch_scc0 .LL_125%=
v_lshlrev_b32 v2, 2, v1
v_mov_b64 v[28:29], s[12:13]
s_mov_b64 exec, 3
buffer_store_dwordx2 v[28:29], v2, s[8:11], s53 offen offset:512 sc1
s_mov_b64 exec, -1
.LL_125%=:
s_barrier
s_waitcnt vmcnt(1)
v_cmp_eq_u64 vcc, s[12:13], v[16:17]
s_or_b32 s67, vcc_lo, s66
s_cmp_eq_u32 s14, 0
s_cbranch_scc1 .LL_127%=
s_cmp_eq_u32 s14, 1
s_cbranch_scc1 .LL_128%=
s_mov_b32 s84, 0
.LL_129%=:
s_or_b32 s70, s71, s66
s_andn2_b32 s69, s67, s71
s_and_b32 s69, s69, 0xffff
s_cbranch_scc0 .LL_132%=
s_and_b32 s80, s69, 255
s_cbranch_scc0 .LL_133%=
s_bitcmp1_b32 s69, 0
s_cbranch_scc0 .LL_134%=
s_cmp_eq_u32 s59, 0
s_cbranch_scc0 .LL_135%=
Ra 0,21,0
s_branch .LL_134%=
.LL_135%=:
buffer_load_dwordx4 a[0:3], v1, s[8:11], s78 offen offset:0 sc1
.LL_134%=:
s_bitcmp1_b32 s69, 1
s_cbranch_scc0 .LL_136%=
s_cmp_eq_u32 s59, 1
s_cbranch_scc0 .LL_137%=
Ra 4,21,0
s_branch .LL_136%=
.LL_137%=:
buffer_load_dwordx4 a[4:7], v1, s[8:11], s78 offen offset:1024 sc1
.LL_136%=:
s_bitcmp1_b32 s69, 2
s_cbranch_scc0 .LL_138%=
s_cmp_eq_u32 s59, 2
s_cbranch_scc0 .LL_139%=
Ra 8,21,0
s_branch .LL_138%=
.LL_139%=:
buffer_load_dwordx4 a[8:11], v1, s[8:11], s78 offen offset:2048 sc1
.LL_138%=:
s_bitcmp1_b32 s69, 3
s_cbranch_scc0 .LL_140%=
s_cmp_eq_u32 s59, 3
s_cbranch_scc0 .LL_141%=
Ra 12,21,0
s_branch .LL_140%=
.LL_141%=:
buffer_load_dwordx4 a[12:15], v1, s[8:11], s78 offen offset:3072 sc1
.LL_140%=:
s_bitcmp1_b32 s69, 4
s_cbranch_scc0 .LL_142%=
s_cmp_eq_u32 s59, 4
s_cbranch_scc0 .LL_143%=
Ra 16,21,0
s_branch .LL_142%=
.LL_143%=:
buffer_load_dwordx4 a[16:19], v19, s[8:11], s78 offen offset:0 sc1
.LL_142%=:
s_bitcmp1_b32 s69, 5
s_cbranch_scc0 .LL_144%=
s_cmp_eq_u32 s59, 5
s_cbranch_scc0 .LL_145%=
Ra 20,21,0
s_branch .LL_144%=
.LL_145%=:
buffer_load_dwordx4 a[20:23], v19, s[8:11], s78 offen offset:1024 sc1
.LL_144%=:
s_bitcmp1_b32 s69, 6
s_cbranch_scc0 .LL_146%=
s_cmp_eq_u32 s59, 6
s_cbranch_scc0 .LL_147%=
Ra 24,21,0
s_branch .LL_146%=
.LL_147%=:
buffer_load_dwordx4 a[24:27], v19, s[8:11], s78 offen offset:2048 sc1
.LL_146%=:
s_bitcmp1_b32 s69, 7
s_cbranch_scc0 .LL_148%=
s_cmp_eq_u32 s59, 7
s_cbranch_scc0 .LL_149%=
Ra 28,21,0
s_branch .LL_148%=
.LL_149%=:
buffer_load_dwordx4 a[28:31], v19, s[8:11], s78 offen offset:3072 sc1
.LL_148%=:
.LL_133%=:
s_and_b32 s80, s69, 65280
s_cbranch_scc0 .LL_150%=
s_bitcmp1_b32 s69, 8
s_cbranch_scc0 .LL_151%=
s_cmp_eq_u32 s59, 0
s_cbranch_scc0 .LL_152%=
Ra 32,21,5120
s_branch .LL_151%=
.LL_152%=:
buffer_load_dwordx4 a[32:35], v1, s[8:11], s79 offen offset:0 sc1
.LL_151%=:
s_bitcmp1_b32 s69, 9
s_cbranch_scc0 .LL_153%=
s_cmp_eq_u32 s59, 1
s_cbranch_scc0 .LL_154%=
Ra 36,21,5120
s_branch .LL_153%=
.LL_154%=:
buffer_load_dwordx4 a[36:39], v1, s[8:11], s79 offen offset:1024 sc1
.LL_153%=:
s_bitcmp1_b32 s69, 10
s_cbranch_scc0 .LL_155%=
s_cmp_eq_u32 s59, 2
s_cbranch_scc0 .LL_156%=
Ra 40,21,5120
s_branch .LL_155%=
.LL_156%=:
buffer_load_dwordx4 a[40:43], v1, s[8:11], s79 offen offset:2048 sc1
.LL_155%=:
s_bitcmp1_b32 s69, 11
s_cbranch_scc0 .LL_157%=
s_cmp_eq_u32 s59, 3
s_cbranch_scc0 .LL_158%=
Ra 44,21,5120
s_branch .LL_157%=
.LL_158%=:
buffer_load_dwordx4 a[44:47], v1, s[8:11], s79 offen offset:3072 sc1
.LL_157%=:
s_bitcmp1_b32 s69, 12
s_cbranch_scc0 .LL_159%=
s_cmp_eq_u32 s59, 4
s_cbranch_scc0 .LL_160%=
Ra 48,21,5120
s_branch .LL_159%=
.LL_160%=:
buffer_load_dwordx4 a[48:51], v19, s[8:11], s79 offen offset:0 sc1
.LL_159%=:
s_bitcmp1_b32 s69, 13
s_cbranch_scc0 .LL_161%=
s_cmp_eq_u32 s59, 5
s_cbranch_scc0 .LL_162%=
Ra 52,21,5120
s_branch .LL_161%=
.LL_162%=:
buffer_load_dwordx4 a[52:55], v19, s[8:11], s79 offen offset:1024 sc1
.LL_161%=:
s_bitcmp1_b32 s69, 14
s_cbranch_scc0 .LL_163%=
s_cmp_eq_u32 s59, 6
s_cbranch_scc0 .LL_164%=
Ra 56,21,5120
s_branch .LL_163%=
.LL_164%=:
buffer_load_dwordx4 a[56:59], v19, s[8:11], s79 offen offset:2048 sc1
.LL_163%=:
s_bitcmp1_b32 s69, 15
s_cbranch_scc0 .LL_165%=
s_cmp_eq_u32 s59, 7
s_cbranch_scc0 .LL_166%=
Ra 60,21,5120
s_branch .LL_165%=
.LL_166%=:
buffer_load_dwordx4 a[60:63], v19, s[8:11], s79 offen offset:3072 sc1
.LL_165%=:
.LL_150%=:
s_or_b32 s71, s71, s69
.LL_132%=:
s_andn2_b32 s80, 0xffff, s71
s_cbranch_scc0 .LL_131%=
s_sleep 4
buffer_load_dwordx2 v[16:17], v18, s[8:11], s63 offen sc1
s_and_b32 s80, s70, 0x100ff
s_cmp_eq_u32 s80, 0xff
s_cbranch_scc0 .LL_130%=
W 0
Zvva 80,4,0
Zvva 84,8,0
Mvva 80,4,4
Mvva 84,8,4
Mvva 80,4,8
Mvva 84,8,8
Mvva 80,4,12
Mvva 84,8,12
Mvva 80,4,16
Mvva 84,8,16
Mvva 80,4,20
Mvva 84,8,20
Mvva 80,4,24
Mvva 84,8,24
Mvva 80,4,28
Mvva 84,8,28
s_nop 6
K 160,80,81
K 161,82,83
K 162,84,85
K 163,86,87
v_lshlrev_b32 v80, 16, v160
v_and_b32 v81, 0xffff0000, v160
v_lshlrev_b32 v82, 16, v161
v_and_b32 v83, 0xffff0000, v161
v_lshlrev_b32 v84, 16, v162
v_and_b32 v85, 0xffff0000, v162
v_lshlrev_b32 v86, 16, v163
v_and_b32 v87, 0xffff0000, v163
v_pk_add_f32 v[80:81], v[32:33], v[80:81]
v_pk_add_f32 v[82:83], v[34:35], v[82:83]
v_pk_add_f32 v[84:85], v[36:37], v[84:85]
v_pk_add_f32 v[86:87], v[38:39], v[86:87]
K 160,80,81
K 161,82,83
K 162,84,85
K 163,86,87
v_lshlrev_b32 v80, 16, v160
v_and_b32 v81, 0xffff0000, v160
v_lshlrev_b32 v82, 16, v161
v_and_b32 v83, 0xffff0000, v161
v_lshlrev_b32 v84, 16, v162
v_and_b32 v85, 0xffff0000, v162
v_lshlrev_b32 v86, 16, v163
v_and_b32 v87, 0xffff0000, v163
v_pk_add_f32 v[80:81], v[40:41], v[80:81]
v_pk_add_f32 v[82:83], v[42:43], v[82:83]
v_pk_add_f32 v[84:85], v[44:45], v[84:85]
v_pk_add_f32 v[86:87], v[46:47], v[86:87]
K 80,80,81
K 81,82,83
K 82,84,85
K 83,86,87
buffer_store_dwordx4 v[80:83], v20, s[20:23], s72 offen nt
s_bitset1_b32 s71, 16
.LL_130%=:
s_add_u32 s84, s84, 1
s_waitcnt vmcnt(0)
v_cmp_eq_u64 vcc, s[12:13], v[16:17]
s_or_b32 s67, vcc_lo, s66
s_cmp_lt_u32 s84, 4194304
s_cbranch_scc1 .LL_129%=
.LL_131%=:
s_bitcmp1_b32 s71, 16
s_cbranch_scc1 .LL_167%=
s_and_b32 s80, s70, 0xff
s_cmp_eq_u32 s80, 0xff
s_cbranch_scc1 .LL_168%=
s_waitcnt vmcnt(0)
.LL_168%=:
W 0
Zvva 80,4,0
Zvva 84,8,0
Mvva 80,4,4
Mvva 84,8,4
Mvva 80,4,8
Mvva 84,8,8
Mvva 80,4,12
Mvva 84,8,12
Mvva 80,4,16
Mvva 84,8,16
Mvva 80,4,20
Mvva 84,8,20
Mvva 80,4,24
Mvva 84,8,24
Mvva 80,4,28
Mvva 84,8,28
s_nop 6
K 160,80,81
K 161,82,83
K 162,84,85
K 163,86,87
v_lshlrev_b32 v80, 16, v160
v_and_b32 v81, 0xffff0000, v160
v_lshlrev_b32 v82, 16, v161
v_and_b32 v83, 0xffff0000, v161
v_lshlrev_b32 v84, 16, v162
v_and_b32 v85, 0xffff0000, v162
v_lshlrev_b32 v86, 16, v163
v_and_b32 v87, 0xffff0000, v163
v_pk_add_f32 v[80:81], v[32:33], v[80:81]
v_pk_add_f32 v[82:83], v[34:35], v[82:83]
v_pk_add_f32 v[84:85], v[36:37], v[84:85]
v_pk_add_f32 v[86:87], v[38:39], v[86:87]
K 160,80,81
K 161,82,83
K 162,84,85
K 163,86,87
v_lshlrev_b32 v80, 16, v160
v_and_b32 v81, 0xffff0000, v160
v_lshlrev_b32 v82, 16, v161
)"
R"(v_and_b32 v83, 0xffff0000, v161
v_lshlrev_b32 v84, 16, v162
v_and_b32 v85, 0xffff0000, v162
v_lshlrev_b32 v86, 16, v163
v_and_b32 v87, 0xffff0000, v163
v_pk_add_f32 v[80:81], v[40:41], v[80:81]
v_pk_add_f32 v[82:83], v[42:43], v[82:83]
v_pk_add_f32 v[84:85], v[44:45], v[84:85]
v_pk_add_f32 v[86:87], v[46:47], v[86:87]
K 80,80,81
K 81,82,83
K 82,84,85
K 83,86,87
buffer_store_dwordx4 v[80:83], v20, s[20:23], s72 offen nt
s_waitcnt vmcnt(1)
W 0
Zvva 80,4,32
Zvva 84,8,32
Mvva 80,4,36
Mvva 84,8,36
Mvva 80,4,40
Mvva 84,8,40
Mvva 80,4,44
Mvva 84,8,44
Mvva 80,4,48
Mvva 84,8,48
Mvva 80,4,52
Mvva 84,8,52
Mvva 80,4,56
Mvva 84,8,56
Mvva 80,4,60
Mvva 84,8,60
s_nop 6
K 160,80,81
K 161,82,83
K 162,84,85
K 163,86,87
v_lshlrev_b32 v80, 16, v160
v_and_b32 v81, 0xffff0000, v160
v_lshlrev_b32 v82, 16, v161
v_and_b32 v83, 0xffff0000, v161
v_lshlrev_b32 v84, 16, v162
v_and_b32 v85, 0xffff0000, v162
v_lshlrev_b32 v86, 16, v163
v_and_b32 v87, 0xffff0000, v163
v_pk_add_f32 v[80:81], v[48:49], v[80:81]
v_pk_add_f32 v[82:83], v[50:51], v[82:83]
v_pk_add_f32 v[84:85], v[52:53], v[84:85]
v_pk_add_f32 v[86:87], v[54:55], v[86:87]
K 160,80,81
K 161,82,83
K 162,84,85
K 163,86,87
v_lshlrev_b32 v80, 16, v160
v_and_b32 v81, 0xffff0000, v160
v_lshlrev_b32 v82, 16, v161
v_and_b32 v83, 0xffff0000, v161
v_lshlrev_b32 v84, 16, v162
v_and_b32 v85, 0xffff0000, v162
v_lshlrev_b32 v86, 16, v163
v_and_b32 v87, 0xffff0000, v163
v_pk_add_f32 v[80:81], v[56:57], v[80:81]
v_pk_add_f32 v[82:83], v[58:59], v[82:83]
v_pk_add_f32 v[84:85], v[60:61], v[84:85]
v_pk_add_f32 v[86:87], v[62:63], v[86:87]
K 80,80,81
K 81,82,83
K 82,84,85
K 83,86,87
buffer_store_dwordx4 v[80:83], v20, s[20:23], s73 offen nt
s_branch .LL_126%=
.LL_167%=:
s_waitcnt vmcnt(0)
W 0
Zvva 80,4,32
Zvva 84,8,32
Mvva 80,4,36
Mvva 84,8,36
Mvva 80,4,40
Mvva 84,8,40
Mvva 80,4,44
Mvva 84,8,44
Mvva 80,4,48
Mvva 84,8,48
Mvva 80,4,52
Mvva 84,8,52
Mvva 80,4,56
Mvva 84,8,56
Mvva 80,4,60
Mvva 84,8,60
s_nop 6
K 160,80,81
K 161,82,83
K 162,84,85
K 163,86,87
v_lshlrev_b32 v80, 16, v160
v_and_b32 v81, 0xffff0000, v160
v_lshlrev_b32 v82, 16, v161
v_and_b32 v83, 0xffff0000, v161
v_lshlrev_b32 v84, 16, v162
v_and_b32 v85, 0xffff0000, v162
v_lshlrev_b32 v86, 16, v163
v_and_b32 v87, 0xffff0000, v163
v_pk_add_f32 v[80:81], v[48:49], v[80:81]
v_pk_add_f32 v[82:83], v[50:51], v[82:83]
v_pk_add_f32 v[84:85], v[52:53], v[84:85]
v_pk_add_f32 v[86:87], v[54:55], v[86:87]
K 160,80,81
K 161,82,83
K 162,84,85
K 163,86,87
v_lshlrev_b32 v80, 16, v160
v_and_b32 v81, 0xffff0000, v160
v_lshlrev_b32 v82, 16, v161
v_and_b32 v83, 0xffff0000, v161
v_lshlrev_b32 v84, 16, v162
v_and_b32 v85, 0xffff0000, v162
v_lshlrev_b32 v86, 16, v163
v_and_b32 v87, 0xffff0000, v163
v_pk_add_f32 v[80:81], v[56:57], v[80:81]
v_pk_add_f32 v[82:83], v[58:59], v[82:83]
v_pk_add_f32 v[84:85], v[60:61], v[84:85]
v_pk_add_f32 v[86:87], v[62:63], v[86:87]
K 80,80,81
K 81,82,83
K 82,84,85
K 83,86,87
buffer_store_dwordx4 v[80:83], v20, s[20:23], s73 offen nt
s_branch .LL_126%=
.LL_127%=:
s_mov_b32 s84, 0
.LL_169%=:
s_or_b32 s70, s71, s66
s_andn2_b32 s69, s67, s71
s_and_b32 s69, s69, 0xffff
s_cbranch_scc0 .LL_172%=
s_and_b32 s80, s69, 255
s_cbranch_scc0 .LL_173%=
s_bitcmp1_b32 s69, 0
s_cbranch_scc0 .LL_174%=
s_cmp_eq_u32 s59, 0
s_cbranch_scc0 .LL_175%=
Ra 0,21,0
Ra 216,1,4096
s_branch .LL_174%=
.LL_175%=:
buffer_load_dwordx4 a[0:3], v1, s[8:11], s78 offen offset:0 sc1
buffer_load_dwordx4 a[216:219], v1, s[8:11], s65 offen offset:0 sc1
.LL_174%=:
s_bitcmp1_b32 s69, 1
s_cbranch_scc0 .LL_176%=
s_cmp_eq_u32 s59, 1
s_cbranch_scc0 .LL_177%=
Ra 4,21,0
Ra 220,1,4096
s_branch .LL_176%=
.LL_177%=:
buffer_load_dwordx4 a[4:7], v1, s[8:11], s78 offen offset:1024 sc1
buffer_load_dwordx4 a[220:223], v1, s[8:11], s65 offen offset:1024 sc1
.LL_176%=:
s_bitcmp1_b32 s69, 2
s_cbranch_scc0 .LL_178%=
s_cmp_eq_u32 s59, 2
s_cbranch_scc0 .LL_179%=
Ra 8,21,0
Ra 224,1,4096
s_branch .LL_178%=
.LL_179%=:
buffer_load_dwordx4 a[8:11], v1, s[8:11], s78 offen offset:2048 sc1
buffer_load_dwordx4 a[224:227], v1, s[8:11], s65 offen offset:2048 sc1
.LL_178%=:
s_bitcmp1_b32 s69, 3
s_cbranch_scc0 .LL_180%=
s_cmp_eq_u32 s59, 3
s_cbranch_scc0 .LL_181%=
Ra 12,21,0
Ra 228,1,4096
s_branch .LL_180%=
.LL_181%=:
buffer_load_dwordx4 a[12:15], v1, s[8:11], s78 offen offset:3072 sc1
buffer_load_dwordx4 a[228:231], v1, s[8:11], s65 offen offset:3072 sc1
.LL_180%=:
s_bitcmp1_b32 s69, 4
s_cbranch_scc0 .LL_182%=
s_cmp_eq_u32 s59, 4
s_cbranch_scc0 .LL_183%=
Ra 16,21,0
Ra 232,1,4096
s_branch .LL_182%=
.LL_183%=:
buffer_load_dwordx4 a[16:19], v19, s[8:11], s78 offen offset:0 sc1
buffer_load_dwordx4 a[232:235], v19, s[8:11], s65 offen offset:0 sc1
.LL_182%=:
s_bitcmp1_b32 s69, 5
s_cbranch_scc0 .LL_184%=
s_cmp_eq_u32 s59, 5
s_cbranch_scc0 .LL_185%=
Ra 20,21,0
Ra 236,1,4096
s_branch .LL_184%=
.LL_185%=:
buffer_load_dwordx4 a[20:23], v19, s[8:11], s78 offen offset:1024 sc1
buffer_load_dwordx4 a[236:239], v19, s[8:11], s65 offen offset:1024 sc1
.LL_184%=:
s_bitcmp1_b32 s69, 6
s_cbranch_scc0 .LL_186%=
s_cmp_eq_u32 s59, 6
s_cbranch_scc0 .LL_187%=
Ra 24,21,0
Ra 240,1,4096
s_branch .LL_186%=
.LL_187%=:
buffer_load_dwordx4 a[24:27], v19, s[8:11], s78 offen offset:2048 sc1
buffer_load_dwordx4 a[240:243], v19, s[8:11], s65 offen offset:2048 sc1
.LL_186%=:
s_bitcmp1_b32 s69, 7
s_cbranch_scc0 .LL_188%=
s_cmp_eq_u32 s59, 7
s_cbranch_scc0 .LL_189%=
Ra 28,21,0
Ra 244,1,4096
s_branch .LL_188%=
.LL_189%=:
buffer_load_dwordx4 a[28:31], v19, s[8:11], s78 offen offset:3072 sc1
buffer_load_dwordx4 a[244:247], v19, s[8:11], s65 offen offset:3072 sc1
.LL_188%=:
.LL_173%=:
s_and_b32 s80, s69, 65280
s_cbranch_scc0 .LL_190%=
s_bitcmp1_b32 s69, 8
s_cbranch_scc0 .LL_191%=
s_cmp_eq_u32 s59, 0
s_cbranch_scc0 .LL_192%=
Ra 32,21,5120
s_branch .LL_191%=
.LL_192%=:
buffer_load_dwordx4 a[32:35], v1, s[8:11], s79 offen offset:0 sc1
.LL_191%=:
s_bitcmp1_b32 s69, 9
s_cbranch_scc0 .LL_193%=
s_cmp_eq_u32 s59, 1
s_cbranch_scc0 .LL_194%=
Ra 36,21,5120
s_branch .LL_193%=
.LL_194%=:
buffer_load_dwordx4 a[36:39], v1, s[8:11], s79 offen offset:1024 sc1
.LL_193%=:
s_bitcmp1_b32 s69, 10
s_cbranch_scc0 .LL_195%=
s_cmp_eq_u32 s59, 2
s_cbranch_scc0 .LL_196%=
Ra 40,21,5120
s_branch .LL_195%=
.LL_196%=:
buffer_load_dwordx4 a[40:43], v1, s[8:11], s79 offen offset:2048 sc1
.LL_195%=:
s_bitcmp1_b32 s69, 11
s_cbranch_scc0 .LL_197%=
s_cmp_eq_u32 s59, 3
s_cbranch_scc0 .LL_198%=
Ra 44,21,5120
s_branch .LL_197%=
.LL_198%=:
buffer_load_dwordx4 a[44:47], v1, s[8:11], s79 offen offset:3072 sc1
.LL_197%=:
s_bitcmp1_b32 s69, 12
s_cbranch_scc0 .LL_199%=
s_cmp_eq_u32 s59, 4
s_cbranch_scc0 .LL_200%=
Ra 48,21,5120
s_branch .LL_199%=
.LL_200%=:
buffer_load_dwordx4 a[48:51], v19, s[8:11], s79 offen offset:0 sc1
.LL_199%=:
s_bitcmp1_b32 s69, 13
s_cbranch_scc0 .LL_201%=
s_cmp_eq_u32 s59, 5
s_cbranch_scc0 .LL_202%=
Ra 52,21,5120
s_branch .LL_201%=
.LL_202%=:
buffer_load_dwordx4 a[52:55], v19, s[8:11], s79 offen offset:1024 sc1
.LL_201%=:
s_bitcmp1_b32 s69, 14
s_cbranch_scc0 .LL_203%=
s_cmp_eq_u32 s59, 6
s_cbranch_scc0 .LL_204%=
Ra 56,21,5120
s_branch .LL_203%=
.LL_204%=:
buffer_load_dwordx4 a[56:59], v19, s[8:11], s79 offen offset:2048 sc1
.LL_203%=:
s_bitcmp1_b32 s69, 15
s_cbranch_scc0 .LL_205%=
s_cmp_eq_u32 s59, 7
s_cbranch_scc0 .LL_206%=
Ra 60,21,5120
s_branch .LL_205%=
.LL_206%=:
buffer_load_dwordx4 a[60:63], v19, s[8:11], s79 offen offset:3072 sc1
.LL_205%=:
.LL_190%=:
s_or_b32 s71, s71, s69
.LL_172%=:
s_andn2_b32 s80, 0xffff, s71
s_cbranch_scc0 .LL_171%=
s_sleep 4
buffer_load_dwordx2 v[16:17], v18, s[8:11], s63 offen sc1
s_and_b32 s80, s70, 0x100ff
s_cmp_eq_u32 s80, 0xff
s_cbranch_scc0 .LL_170%=
W 0
Zvva 80,4,0
Zvva 84,8,0
Zvva 88,4,216
Zvva 92,8,216
Mvva 80,4,4
Mvva 84,8,4
Mvva 88,4,220
Mvva 92,8,220
Mvva 80,4,8
Mvva 84,8,8
Mvva 88,4,224
Mvva 92,8,224
Mvva 80,4,12
Mvva 84,8,12
Mvva 88,4,228
Mvva 92,8,228
Mvva 80,4,16
Mvva 84,8,16
Mvva 88,4,232
Mvva 92,8,232
Mvva 80,4,20
Mvva 84,8,20
Mvva 88,4,236
Mvva 92,8,236
Mvva 80,4,24
Mvva 84,8,24
Mvva 88,4,240
Mvva 92,8,240
Mvva 80,4,28
Mvva 84,8,28
Mvva 88,4,244
Mvva 92,8,244
s_nop 4
K 160,80,81
K 161,82,83
K 162,84,85
K 163,86,87
v_lshlrev_b32 v80, 16, v160
v_and_b32 v81, 0xffff0000, v160
v_lshlrev_b32 v82, 16, v161
v_and_b32 v83, 0xffff0000, v161
v_lshlrev_b32 v84, 16, v162
v_and_b32 v85, 0xffff0000, v162
v_lshlrev_b32 v86, 16, v163
v_and_b32 v87, 0xffff0000, v163
v_pk_add_f32 v[80:81], v[32:33], v[80:81]
v_pk_add_f32 v[82:83], v[34:35], v[82:83]
v_pk_add_f32 v[84:85], v[36:37], v[84:85]
v_pk_add_f32 v[86:87], v[38:39], v[86:87]
K 160,80,81
K 161,82,83
K 162,84,85
K 163,86,87
v_lshlrev_b32 v80, 16, v160
v_and_b32 v81, 0xffff0000, v160
v_lshlrev_b32 v82, 16, v161
v_and_b32 v83, 0xffff0000, v161
v_lshlrev_b32 v84, 16, v162
v_and_b32 v85, 0xffff0000, v162
v_lshlrev_b32 v86, 16, v163
v_and_b32 v87, 0xffff0000, v163
v_pk_add_f32 v[80:81], v[40:41], v[80:81]
v_pk_add_f32 v[82:83], v[42:43], v[82:83]
v_pk_add_f32 v[84:85], v[44:45], v[84:85]
v_pk_add_f32 v[86:87], v[46:47], v[86:87]
K 80,80,81
K 81,82,83
K 82,84,85
K 83,86,87
buffer_store_dwordx4 v[80:83], v20, s[20:23], s72 offen nt
K 160,88,89
K 161,90,91
K 162,92,93
K 163,94,95
v_lshlrev_b32 v88, 16, v160
v_and_b32 v89, 0xffff0000, v160
v_lshlrev_b32 v90, 16, v161
v_and_b32 v91, 0xffff0000, v161
v_lshlrev_b32 v92, 16, v162
v_and_b32 v93, 0xffff0000, v162
v_lshlrev_b32 v94, 16, v163
v_and_b32 v95, 0xffff0000, v163
v_pk_add_f32 v[88:89], v[64:65], v[88:89]
v_pk_add_f32 v[90:91], v[66:67], v[90:91]
v_pk_add_f32 v[92:93], v[68:69], v[92:93]
v_pk_add_f32 v[94:95], v[70:71], v[94:95]
K 160,88,89
K 161,90,91
)"
R"(K 162,92,93
K 163,94,95
v_lshlrev_b32 v88, 16, v160
v_and_b32 v89, 0xffff0000, v160
v_lshlrev_b32 v90, 16, v161
v_and_b32 v91, 0xffff0000, v161
v_lshlrev_b32 v92, 16, v162
v_and_b32 v93, 0xffff0000, v162
v_lshlrev_b32 v94, 16, v163
v_and_b32 v95, 0xffff0000, v163
v_pk_add_f32 v[88:89], v[72:73], v[88:89]
v_pk_add_f32 v[90:91], v[74:75], v[90:91]
v_pk_add_f32 v[92:93], v[76:77], v[92:93]
v_pk_add_f32 v[94:95], v[78:79], v[94:95]
K 88,88,89
K 89,90,91
K 90,92,93
K 91,94,95
buffer_store_dwordx4 v[88:91], v20, s[20:23], s74 offen nt
s_bitset1_b32 s71, 16
.LL_170%=:
s_add_u32 s84, s84, 1
s_waitcnt vmcnt(0)
v_cmp_eq_u64 vcc, s[12:13], v[16:17]
s_or_b32 s67, vcc_lo, s66
s_cmp_lt_u32 s84, 4194304
s_cbranch_scc1 .LL_169%=
.LL_171%=:
s_bitcmp1_b32 s71, 16
s_cbranch_scc1 .LL_207%=
s_and_b32 s80, s70, 0xff
s_cmp_eq_u32 s80, 0xff
s_cbranch_scc1 .LL_208%=
s_waitcnt vmcnt(0)
.LL_208%=:
W 0
Zvva 80,4,0
Zvva 84,8,0
Zvva 88,4,216
Zvva 92,8,216
Mvva 80,4,4
Mvva 84,8,4
Mvva 88,4,220
Mvva 92,8,220
Mvva 80,4,8
Mvva 84,8,8
Mvva 88,4,224
Mvva 92,8,224
Mvva 80,4,12
Mvva 84,8,12
Mvva 88,4,228
Mvva 92,8,228
Mvva 80,4,16
Mvva 84,8,16
Mvva 88,4,232
Mvva 92,8,232
Mvva 80,4,20
Mvva 84,8,20
Mvva 88,4,236
Mvva 92,8,236
Mvva 80,4,24
Mvva 84,8,24
Mvva 88,4,240
Mvva 92,8,240
Mvva 80,4,28
Mvva 84,8,28
Mvva 88,4,244
Mvva 92,8,244
s_nop 4
K 160,80,81
K 161,82,83
K 162,84,85
K 163,86,87
v_lshlrev_b32 v80, 16, v160
v_and_b32 v81, 0xffff0000, v160
v_lshlrev_b32 v82, 16, v161
v_and_b32 v83, 0xffff0000, v161
v_lshlrev_b32 v84, 16, v162
v_and_b32 v85, 0xffff0000, v162
v_lshlrev_b32 v86, 16, v163
v_and_b32 v87, 0xffff0000, v163
v_pk_add_f32 v[80:81], v[32:33], v[80:81]
v_pk_add_f32 v[82:83], v[34:35], v[82:83]
v_pk_add_f32 v[84:85], v[36:37], v[84:85]
v_pk_add_f32 v[86:87], v[38:39], v[86:87]
K 160,80,81
K 161,82,83
K 162,84,85
K 163,86,87
v_lshlrev_b32 v80, 16, v160
v_and_b32 v81, 0xffff0000, v160
v_lshlrev_b32 v82, 16, v161
v_and_b32 v83, 0xffff0000, v161
v_lshlrev_b32 v84, 16, v162
v_and_b32 v85, 0xffff0000, v162
v_lshlrev_b32 v86, 16, v163
v_and_b32 v87, 0xffff0000, v163
v_pk_add_f32 v[80:81], v[40:41], v[80:81]
v_pk_add_f32 v[82:83], v[42:43], v[82:83]
v_pk_add_f32 v[84:85], v[44:45], v[84:85]
v_pk_add_f32 v[86:87], v[46:47], v[86:87]
K 80,80,81
K 81,82,83
K 82,84,85
K 83,86,87
buffer_store_dwordx4 v[80:83], v20, s[20:23], s72 offen nt
K 160,88,89
K 161,90,91
K 162,92,93
K 163,94,95
v_lshlrev_b32 v88, 16, v160
v_and_b32 v89, 0xffff0000, v160
v_lshlrev_b32 v90, 16, v161
v_and_b32 v91, 0xffff0000, v161
v_lshlrev_b32 v92, 16, v162
v_and_b32 v93, 0xffff0000, v162
v_lshlrev_b32 v94, 16, v163
v_and_b32 v95, 0xffff0000, v163
v_pk_add_f32 v[88:89], v[64:65], v[88:89]
v_pk_add_f32 v[90:91], v[66:67], v[90:91]
v_pk_add_f32 v[92:93], v[68:69], v[92:93]
v_pk_add_f32 v[94:95], v[70:71], v[94:95]
K 160,88,89
K 161,90,91
K 162,92,93
K 163,94,95
v_lshlrev_b32 v88, 16, v160
v_and_b32 v89, 0xffff0000, v160
v_lshlrev_b32 v90, 16, v161
v_and_b32 v91, 0xffff0000, v161
v_lshlrev_b32 v92, 16, v162
v_and_b32 v93, 0xffff0000, v162
v_lshlrev_b32 v94, 16, v163
v_and_b32 v95, 0xffff0000, v163
v_pk_add_f32 v[88:89], v[72:73], v[88:89]
v_pk_add_f32 v[90:91], v[74:75], v[90:91]
v_pk_add_f32 v[92:93], v[76:77], v[92:93]
v_pk_add_f32 v[94:95], v[78:79], v[94:95]
K 88,88,89
K 89,90,91
K 90,92,93
K 91,94,95
buffer_store_dwordx4 v[88:91], v20, s[20:23], s74 offen nt
s_waitcnt vmcnt(2)
W 0
Zvva 80,4,32
Zvva 84,8,32
Mvva 80,4,36
Mvva 84,8,36
Mvva 80,4,40
Mvva 84,8,40
Mvva 80,4,44
Mvva 84,8,44
Mvva 80,4,48
Mvva 84,8,48
Mvva 80,4,52
Mvva 84,8,52
Mvva 80,4,56
Mvva 84,8,56
Mvva 80,4,60
Mvva 84,8,60
s_nop 6
K 160,80,81
K 161,82,83
K 162,84,85
K 163,86,87
v_lshlrev_b32 v80, 16, v160
v_and_b32 v81, 0xffff0000, v160
v_lshlrev_b32 v82, 16, v161
v_and_b32 v83, 0xffff0000, v161
v_lshlrev_b32 v84, 16, v162
v_and_b32 v85, 0xffff0000, v162
v_lshlrev_b32 v86, 16, v163
v_and_b32 v87, 0xffff0000, v163
v_pk_add_f32 v[80:81], v[48:49], v[80:81]
v_pk_add_f32 v[82:83], v[50:51], v[82:83]
v_pk_add_f32 v[84:85], v[52:53], v[84:85]
v_pk_add_f32 v[86:87], v[54:55], v[86:87]
K 160,80,81
K 161,82,83
K 162,84,85
K 163,86,87
v_lshlrev_b32 v80, 16, v160
v_and_b32 v81, 0xffff0000, v160
v_lshlrev_b32 v82, 16, v161
v_and_b32 v83, 0xffff0000, v161
v_lshlrev_b32 v84, 16, v162
v_and_b32 v85, 0xffff0000, v162
v_lshlrev_b32 v86, 16, v163
v_and_b32 v87, 0xffff0000, v163
v_pk_add_f32 v[80:81], v[56:57], v[80:81]
v_pk_add_f32 v[82:83], v[58:59], v[82:83]
v_pk_add_f32 v[84:85], v[60:61], v[84:85]
v_pk_add_f32 v[86:87], v[62:63], v[86:87]
K 80,80,81
K 81,82,83
K 82,84,85
K 83,86,87
buffer_store_dwordx4 v[80:83], v20, s[20:23], s73 offen nt
s_branch .LL_126%=
.LL_207%=:
s_waitcnt vmcnt(0)
W 0
Zvva 80,4,32
Zvva 84,8,32
Mvva 80,4,36
Mvva 84,8,36
Mvva 80,4,40
Mvva 84,8,40
Mvva 80,4,44
Mvva 84,8,44
Mvva 80,4,48
Mvva 84,8,48
Mvva 80,4,52
Mvva 84,8,52
Mvva 80,4,56
Mvva 84,8,56
Mvva 80,4,60
Mvva 84,8,60
s_nop 6
K 160,80,81
K 161,82,83
K 162,84,85
K 163,86,87
v_lshlrev_b32 v80, 16, v160
v_and_b32 v81, 0xffff0000, v160
v_lshlrev_b32 v82, 16, v161
v_and_b32 v83, 0xffff0000, v161
v_lshlrev_b32 v84, 16, v162
v_and_b32 v85, 0xffff0000, v162
v_lshlrev_b32 v86, 16, v163
v_and_b32 v87, 0xffff0000, v163
v_pk_add_f32 v[80:81], v[48:49], v[80:81]
v_pk_add_f32 v[82:83], v[50:51], v[82:83]
v_pk_add_f32 v[84:85], v[52:53], v[84:85]
v_pk_add_f32 v[86:87], v[54:55], v[86:87]
K 160,80,81
K 161,82,83
K 162,84,85
K 163,86,87
v_lshlrev_b32 v80, 16, v160
v_and_b32 v81, 0xffff0000, v160
v_lshlrev_b32 v82, 16, v161
v_and_b32 v83, 0xffff0000, v161
v_lshlrev_b32 v84, 16, v162
v_and_b32 v85, 0xffff0000, v162
v_lshlrev_b32 v86, 16, v163
v_and_b32 v87, 0xffff0000, v163
v_pk_add_f32 v[80:81], v[56:57], v[80:81]
v_pk_add_f32 v[82:83], v[58:59], v[82:83]
v_pk_add_f32 v[84:85], v[60:61], v[84:85]
v_pk_add_f32 v[86:87], v[62:63], v[86:87]
K 80,80,81
K 81,82,83
K 82,84,85
K 83,86,87
buffer_store_dwordx4 v[80:83], v20, s[20:23], s73 offen nt
s_branch .LL_126%=
.LL_128%=:
s_mov_b32 s84, 0
.LL_209%=:
s_or_b32 s70, s71, s66
s_andn2_b32 s69, s67, s71
s_and_b32 s69, s69, 0xffff
s_cbranch_scc0 .LL_212%=
s_and_b32 s80, s69, 255
s_cbranch_scc0 .LL_213%=
s_bitcmp1_b32 s69, 0
s_cbranch_scc0 .LL_214%=
s_cmp_eq_u32 s59, 0
s_cbranch_scc0 .LL_215%=
Ra 0,21,0
s_branch .LL_214%=
.LL_215%=:
buffer_load_dwordx4 a[0:3], v1, s[8:11], s78 offen offset:0 sc1
.LL_214%=:
s_bitcmp1_b32 s69, 1
s_cbranch_scc0 .LL_216%=
s_cmp_eq_u32 s59, 1
s_cbranch_scc0 .LL_217%=
Ra 4,21,0
s_branch .LL_216%=
.LL_217%=:
buffer_load_dwordx4 a[4:7], v1, s[8:11], s78 offen offset:1024 sc1
.LL_216%=:
s_bitcmp1_b32 s69, 2
s_cbranch_scc0 .LL_218%=
s_cmp_eq_u32 s59, 2
s_cbranch_scc0 .LL_219%=
Ra 8,21,0
s_branch .LL_218%=
.LL_219%=:
buffer_load_dwordx4 a[8:11], v1, s[8:11], s78 offen offset:2048 sc1
.LL_218%=:
s_bitcmp1_b32 s69, 3
s_cbranch_scc0 .LL_220%=
s_cmp_eq_u32 s59, 3
s_cbranch_scc0 .LL_221%=
Ra 12,21,0
s_branch .LL_220%=
.LL_221%=:
buffer_load_dwordx4 a[12:15], v1, s[8:11], s78 offen offset:3072 sc1
.LL_220%=:
s_bitcmp1_b32 s69, 4
s_cbranch_scc0 .LL_222%=
s_cmp_eq_u32 s59, 4
s_cbranch_scc0 .LL_223%=
Ra 16,21,0
s_branch .LL_222%=
.LL_223%=:
buffer_load_dwordx4 a[16:19], v19, s[8:11], s78 offen offset:0 sc1
.LL_222%=:
s_bitcmp1_b32 s69, 5
s_cbranch_scc0 .LL_224%=
s_cmp_eq_u32 s59, 5
s_cbranch_scc0 .LL_225%=
Ra 20,21,0
s_branch .LL_224%=
.LL_225%=:
buffer_load_dwordx4 a[20:23], v19, s[8:11], s78 offen offset:1024 sc1
.LL_224%=:
s_bitcmp1_b32 s69, 6
s_cbranch_scc0 .LL_226%=
s_cmp_eq_u32 s59, 6
s_cbranch_scc0 .LL_227%=
Ra 24,21,0
s_branch .LL_226%=
.LL_227%=:
buffer_load_dwordx4 a[24:27], v19, s[8:11], s78 offen offset:2048 sc1
.LL_226%=:
s_bitcmp1_b32 s69, 7
s_cbranch_scc0 .LL_228%=
s_cmp_eq_u32 s59, 7
s_cbranch_scc0 .LL_229%=
Ra 28,21,0
s_branch .LL_228%=
.LL_229%=:
buffer_load_dwordx4 a[28:31], v19, s[8:11], s78 offen offset:3072 sc1
.LL_228%=:
.LL_213%=:
s_and_b32 s80, s69, 65280
s_cbranch_scc0 .LL_230%=
s_bitcmp1_b32 s69, 8
s_cbranch_scc0 .LL_231%=
s_cmp_eq_u32 s59, 0
s_cbranch_scc0 .LL_232%=
Ra 32,21,5120
Ra 216,1,9216
s_branch .LL_231%=
.LL_232%=:
buffer_load_dwordx4 a[32:35], v1, s[8:11], s79 offen offset:0 sc1
buffer_load_dwordx4 a[216:219], v1, s[8:11], s65 offen offset:0 sc1
.LL_231%=:
s_bitcmp1_b32 s69, 9
s_cbranch_scc0 .LL_233%=
s_cmp_eq_u32 s59, 1
s_cbranch_scc0 .LL_234%=
Ra 36,21,5120
Ra 220,1,9216
s_branch .LL_233%=
.LL_234%=:
buffer_load_dwordx4 a[36:39], v1, s[8:11], s79 offen offset:1024 sc1
buffer_load_dwordx4 a[220:223], v1, s[8:11], s65 offen offset:1024 sc1
.LL_233%=:
s_bitcmp1_b32 s69, 10
s_cbranch_scc0 .LL_235%=
s_cmp_eq_u32 s59, 2
s_cbranch_scc0 .LL_236%=
Ra 40,21,5120
Ra 224,1,9216
s_branch .LL_235%=
.LL_236%=:
buffer_load_dwordx4 a[40:43], v1, s[8:11], s79 offen offset:2048 sc1
buffer_load_dwordx4 a[224:227], v1, s[8:11], s65 offen offset:2048 sc1
.LL_235%=:
s_bitcmp1_b32 s69, 11
s_cbranch_scc0 .LL_237%=
s_cmp_eq_u32 s59, 3
s_cbranch_scc0 .LL_238%=
Ra 44,21,5120
Ra 228,1,9216
s_branch .LL_237%=
.LL_238%=:
buffer_load_dwordx4 a[44:47], v1, s[8:11], s79 offen offset:3072 sc1
buffer_load_dwordx4 a[228:231], v1, s[8:11], s65 offen offset:3072 sc1
.LL_237%=:
s_bitcmp1_b32 s69, 12
s_cbranch_scc0 .LL_239%=
s_cmp_eq_u32 s59, 4
s_cbranch_scc0 .LL_240%=
Ra 48,21,5120
Ra 232,1,9216
s_branch .LL_239%=
.LL_240%=:
buffer_load_dwordx4 a[48:51], v19, s[8:11], s79 offen offset:0 sc1
buffer_load_dwordx4 a[232:235], v19, s[8:11], s65 offen offset:0 sc1
.LL_239%=:
s_bitcmp1_b32 s69, 13
s_cbranch_scc0 .LL_241%=
s_cmp_eq_u32 s59, 5
)"
R"(s_cbranch_scc0 .LL_242%=
Ra 52,21,5120
Ra 236,1,9216
s_branch .LL_241%=
.LL_242%=:
buffer_load_dwordx4 a[52:55], v19, s[8:11], s79 offen offset:1024 sc1
buffer_load_dwordx4 a[236:239], v19, s[8:11], s65 offen offset:1024 sc1
.LL_241%=:
s_bitcmp1_b32 s69, 14
s_cbranch_scc0 .LL_243%=
s_cmp_eq_u32 s59, 6
s_cbranch_scc0 .LL_244%=
Ra 56,21,5120
Ra 240,1,9216
s_branch .LL_243%=
.LL_244%=:
buffer_load_dwordx4 a[56:59], v19, s[8:11], s79 offen offset:2048 sc1
buffer_load_dwordx4 a[240:243], v19, s[8:11], s65 offen offset:2048 sc1
.LL_243%=:
s_bitcmp1_b32 s69, 15
s_cbranch_scc0 .LL_245%=
s_cmp_eq_u32 s59, 7
s_cbranch_scc0 .LL_246%=
Ra 60,21,5120
Ra 244,1,9216
s_branch .LL_245%=
.LL_246%=:
buffer_load_dwordx4 a[60:63], v19, s[8:11], s79 offen offset:3072 sc1
buffer_load_dwordx4 a[244:247], v19, s[8:11], s65 offen offset:3072 sc1
.LL_245%=:
.LL_230%=:
s_or_b32 s71, s71, s69
.LL_212%=:
s_andn2_b32 s80, 0xffff, s71
s_cbranch_scc0 .LL_211%=
s_sleep 4
buffer_load_dwordx2 v[16:17], v18, s[8:11], s63 offen sc1
s_and_b32 s80, s70, 0x100ff
s_cmp_eq_u32 s80, 0xff
s_cbranch_scc0 .LL_210%=
W 0
Zvva 80,4,0
Zvva 84,8,0
Mvva 80,4,4
Mvva 84,8,4
Mvva 80,4,8
Mvva 84,8,8
Mvva 80,4,12
Mvva 84,8,12
Mvva 80,4,16
Mvva 84,8,16
Mvva 80,4,20
Mvva 84,8,20
Mvva 80,4,24
Mvva 84,8,24
Mvva 80,4,28
Mvva 84,8,28
s_nop 6
K 160,80,81
K 161,82,83
K 162,84,85
K 163,86,87
v_lshlrev_b32 v80, 16, v160
v_and_b32 v81, 0xffff0000, v160
v_lshlrev_b32 v82, 16, v161
v_and_b32 v83, 0xffff0000, v161
v_lshlrev_b32 v84, 16, v162
v_and_b32 v85, 0xffff0000, v162
v_lshlrev_b32 v86, 16, v163
v_and_b32 v87, 0xffff0000, v163
v_pk_add_f32 v[80:81], v[32:33], v[80:81]
v_pk_add_f32 v[82:83], v[34:35], v[82:83]
v_pk_add_f32 v[84:85], v[36:37], v[84:85]
v_pk_add_f32 v[86:87], v[38:39], v[86:87]
K 160,80,81
K 161,82,83
K 162,84,85
K 163,86,87
v_lshlrev_b32 v80, 16, v160
v_and_b32 v81, 0xffff0000, v160
v_lshlrev_b32 v82, 16, v161
v_and_b32 v83, 0xffff0000, v161
v_lshlrev_b32 v84, 16, v162
v_and_b32 v85, 0xffff0000, v162
v_lshlrev_b32 v86, 16, v163
v_and_b32 v87, 0xffff0000, v163
v_pk_add_f32 v[80:81], v[40:41], v[80:81]
v_pk_add_f32 v[82:83], v[42:43], v[82:83]
v_pk_add_f32 v[84:85], v[44:45], v[84:85]
v_pk_add_f32 v[86:87], v[46:47], v[86:87]
K 80,80,81
K 81,82,83
K 82,84,85
K 83,86,87
buffer_store_dwordx4 v[80:83], v20, s[20:23], s72 offen nt
s_bitset1_b32 s71, 16
.LL_210%=:
s_add_u32 s84, s84, 1
s_waitcnt vmcnt(0)
v_cmp_eq_u64 vcc, s[12:13], v[16:17]
s_or_b32 s67, vcc_lo, s66
s_cmp_lt_u32 s84, 4194304
s_cbranch_scc1 .LL_209%=
.LL_211%=:
s_bitcmp1_b32 s71, 16
s_cbranch_scc1 .LL_247%=
s_and_b32 s80, s70, 0xff
s_cmp_eq_u32 s80, 0xff
s_cbranch_scc1 .LL_248%=
s_waitcnt vmcnt(0)
.LL_248%=:
W 0
Zvva 80,4,0
Zvva 84,8,0
Mvva 80,4,4
Mvva 84,8,4
Mvva 80,4,8
Mvva 84,8,8
Mvva 80,4,12
Mvva 84,8,12
Mvva 80,4,16
Mvva 84,8,16
Mvva 80,4,20
Mvva 84,8,20
Mvva 80,4,24
Mvva 84,8,24
Mvva 80,4,28
Mvva 84,8,28
s_nop 6
K 160,80,81
K 161,82,83
K 162,84,85
K 163,86,87
v_lshlrev_b32 v80, 16, v160
v_and_b32 v81, 0xffff0000, v160
v_lshlrev_b32 v82, 16, v161
v_and_b32 v83, 0xffff0000, v161
v_lshlrev_b32 v84, 16, v162
v_and_b32 v85, 0xffff0000, v162
v_lshlrev_b32 v86, 16, v163
v_and_b32 v87, 0xffff0000, v163
v_pk_add_f32 v[80:81], v[32:33], v[80:81]
v_pk_add_f32 v[82:83], v[34:35], v[82:83]
v_pk_add_f32 v[84:85], v[36:37], v[84:85]
v_pk_add_f32 v[86:87], v[38:39], v[86:87]
K 160,80,81
K 161,82,83
K 162,84,85
K 163,86,87
v_lshlrev_b32 v80, 16, v160
v_and_b32 v81, 0xffff0000, v160
v_lshlrev_b32 v82, 16, v161
v_and_b32 v83, 0xffff0000, v161
v_lshlrev_b32 v84, 16, v162
v_and_b32 v85, 0xffff0000, v162
v_lshlrev_b32 v86, 16, v163
v_and_b32 v87, 0xffff0000, v163
v_pk_add_f32 v[80:81], v[40:41], v[80:81]
v_pk_add_f32 v[82:83], v[42:43], v[82:83]
v_pk_add_f32 v[84:85], v[44:45], v[84:85]
v_pk_add_f32 v[86:87], v[46:47], v[86:87]
K 80,80,81
K 81,82,83
K 82,84,85
K 83,86,87
buffer_store_dwordx4 v[80:83], v20, s[20:23], s72 offen nt
s_waitcnt vmcnt(1)
W 0
Zvva 80,4,32
Zvva 84,8,32
Zvva 88,4,216
Zvva 92,8,216
Mvva 80,4,36
Mvva 84,8,36
Mvva 88,4,220
Mvva 92,8,220
Mvva 80,4,40
Mvva 84,8,40
Mvva 88,4,224
Mvva 92,8,224
Mvva 80,4,44
Mvva 84,8,44
Mvva 88,4,228
Mvva 92,8,228
Mvva 80,4,48
Mvva 84,8,48
Mvva 88,4,232
Mvva 92,8,232
Mvva 80,4,52
Mvva 84,8,52
Mvva 88,4,236
Mvva 92,8,236
Mvva 80,4,56
Mvva 84,8,56
Mvva 88,4,240
Mvva 92,8,240
Mvva 80,4,60
Mvva 84,8,60
Mvva 88,4,244
Mvva 92,8,244
s_nop 4
K 160,80,81
K 161,82,83
K 162,84,85
K 163,86,87
v_lshlrev_b32 v80, 16, v160
v_and_b32 v81, 0xffff0000, v160
v_lshlrev_b32 v82, 16, v161
v_and_b32 v83, 0xffff0000, v161
v_lshlrev_b32 v84, 16, v162
v_and_b32 v85, 0xffff0000, v162
v_lshlrev_b32 v86, 16, v163
v_and_b32 v87, 0xffff0000, v163
v_pk_add_f32 v[80:81], v[48:49], v[80:81]
v_pk_add_f32 v[82:83], v[50:51], v[82:83]
v_pk_add_f32 v[84:85], v[52:53], v[84:85]
v_pk_add_f32 v[86:87], v[54:55], v[86:87]
K 160,80,81
K 161,82,83
K 162,84,85
K 163,86,87
v_lshlrev_b32 v80, 16, v160
v_and_b32 v81, 0xffff0000, v160
v_lshlrev_b32 v82, 16, v161
v_and_b32 v83, 0xffff0000, v161
v_lshlrev_b32 v84, 16, v162
v_and_b32 v85, 0xffff0000, v162
v_lshlrev_b32 v86, 16, v163
v_and_b32 v87, 0xffff0000, v163
v_pk_add_f32 v[80:81], v[56:57], v[80:81]
v_pk_add_f32 v[82:83], v[58:59], v[82:83]
v_pk_add_f32 v[84:85], v[60:61], v[84:85]
v_pk_add_f32 v[86:87], v[62:63], v[86:87]
K 80,80,81
K 81,82,83
K 82,84,85
K 83,86,87
buffer_store_dwordx4 v[80:83], v20, s[20:23], s73 offen nt
K 160,88,89
K 161,90,91
K 162,92,93
K 163,94,95
v_lshlrev_b32 v88, 16, v160
v_and_b32 v89, 0xffff0000, v160
v_lshlrev_b32 v90, 16, v161
v_and_b32 v91, 0xffff0000, v161
v_lshlrev_b32 v92, 16, v162
v_and_b32 v93, 0xffff0000, v162
v_lshlrev_b32 v94, 16, v163
v_and_b32 v95, 0xffff0000, v163
v_pk_add_f32 v[88:89], v[64:65], v[88:89]
v_pk_add_f32 v[90:91], v[66:67], v[90:91]
v_pk_add_f32 v[92:93], v[68:69], v[92:93]
v_pk_add_f32 v[94:95], v[70:71], v[94:95]
K 160,88,89
K 161,90,91
K 162,92,93
K 163,94,95
v_lshlrev_b32 v88, 16, v160
v_and_b32 v89, 0xffff0000, v160
v_lshlrev_b32 v90, 16, v161
v_and_b32 v91, 0xffff0000, v161
v_lshlrev_b32 v92, 16, v162
v_and_b32 v93, 0xffff0000, v162
v_lshlrev_b32 v94, 16, v163
v_and_b32 v95, 0xffff0000, v163
v_pk_add_f32 v[88:89], v[72:73], v[88:89]
v_pk_add_f32 v[90:91], v[74:75], v[90:91]
v_pk_add_f32 v[92:93], v[76:77], v[92:93]
v_pk_add_f32 v[94:95], v[78:79], v[94:95]
K 88,88,89
K 89,90,91
K 90,92,93
K 91,94,95
buffer_store_dwordx4 v[88:91], v20, s[20:23], s74 offen nt
s_branch .LL_126%=
.LL_247%=:
s_waitcnt vmcnt(0)
W 0
Zvva 80,4,32
Zvva 84,8,32
Zvva 88,4,216
Zvva 92,8,216
Mvva 80,4,36
Mvva 84,8,36
Mvva 88,4,220
Mvva 92,8,220
Mvva 80,4,40
Mvva 84,8,40
Mvva 88,4,224
Mvva 92,8,224
Mvva 80,4,44
Mvva 84,8,44
Mvva 88,4,228
Mvva 92,8,228
Mvva 80,4,48
Mvva 84,8,48
Mvva 88,4,232
Mvva 92,8,232
Mvva 80,4,52
Mvva 84,8,52
Mvva 88,4,236
Mvva 92,8,236
Mvva 80,4,56
Mvva 84,8,56
Mvva 88,4,240
Mvva 92,8,240
Mvva 80,4,60
Mvva 84,8,60
Mvva 88,4,244
Mvva 92,8,244
s_nop 4
K 160,80,81
K 161,82,83
K 162,84,85
K 163,86,87
v_lshlrev_b32 v80, 16, v160
v_and_b32 v81, 0xffff0000, v160
v_lshlrev_b32 v82, 16, v161
v_and_b32 v83, 0xffff0000, v161
v_lshlrev_b32 v84, 16, v162
v_and_b32 v85, 0xffff0000, v162
v_lshlrev_b32 v86, 16, v163
v_and_b32 v87, 0xffff0000, v163
v_pk_add_f32 v[80:81], v[48:49], v[80:81]
v_pk_add_f32 v[82:83], v[50:51], v[82:83]
v_pk_add_f32 v[84:85], v[52:53], v[84:85]
v_pk_add_f32 v[86:87], v[54:55], v[86:87]
K 160,80,81
K 161,82,83
K 162,84,85
K 163,86,87
v_lshlrev_b32 v80, 16, v160
v_and_b32 v81, 0xffff0000, v160
v_lshlrev_b32 v82, 16, v161
v_and_b32 v83, 0xffff0000, v161
v_lshlrev_b32 v84, 16, v162
v_and_b32 v85, 0xffff0000, v162
v_lshlrev_b32 v86, 16, v163
v_and_b32 v87, 0xffff0000, v163
v_pk_add_f32 v[80:81], v[56:57], v[80:81]
v_pk_add_f32 v[82:83], v[58:59], v[82:83]
v_pk_add_f32 v[84:85], v[60:61], v[84:85]
v_pk_add_f32 v[86:87], v[62:63], v[86:87]
K 80,80,81
K 81,82,83
K 82,84,85
K 83,86,87
buffer_store_dwordx4 v[80:83], v20, s[20:23], s73 offen nt
K 160,88,89
K 161,90,91
K 162,92,93
K 163,94,95
v_lshlrev_b32 v88, 16, v160
v_and_b32 v89, 0xffff0000, v160
v_lshlrev_b32 v90, 16, v161
v_and_b32 v91, 0xffff0000, v161
v_lshlrev_b32 v92, 16, v162
v_and_b32 v93, 0xffff0000, v162
v_lshlrev_b32 v94, 16, v163
v_and_b32 v95, 0xffff0000, v163
v_pk_add_f32 v[88:89], v[64:65], v[88:89]
v_pk_add_f32 v[90:91], v[66:67], v[90:91]
v_pk_add_f32 v[92:93], v[68:69], v[92:93]
v_pk_add_f32 v[94:95], v[70:71], v[94:95]
K 160,88,89
K 161,90,91
K 162,92,93
K 163,94,95
v_lshlrev_b32 v88, 16, v160
v_and_b32 v89, 0xffff0000, v160
v_lshlrev_b32 v90, 16, v161
v_and_b32 v91, 0xffff0000, v161
v_lshlrev_b32 v92, 16, v162
v_and_b32 v93, 0xffff0000, v162
v_lshlrev_b32 v94, 16, v163
v_and_b32 v95, 0xffff0000, v163
v_pk_add_f32 v[88:89], v[72:73], v[88:89]
v_pk_add_f32 v[90:91], v[74:75], v[90:91]
v_pk_add_f32 v[92:93], v[76:77], v[92:93]
v_pk_add_f32 v[94:95], v[78:79], v[94:95]
K 88,88,89
K 89,90,91
K 90,92,93
K 91,94,95
buffer_store_dwordx4 v[88:91], v20, s[20:23], s74 offen nt
.LL_126%=:
s_lshl_b32 s80, s15, 2
s_add_u32 s80, s80, s14
s_cmp_eq_u32 s80, 4
s_cbranch_scc0 .LL_249%=
v_mov_b64 v[28:29], s[12:13]
s_mov_b64 exec, 1
buffer_store_dwordx2 v[28:29], off, s[8:11], 0 sc1
s_mov_b64 exec, -1
.LL_249%=:
s_branch .Lend_250%=
.Lup_119%=:
s_waitcnt vmcnt(3)
s_mov_b32 s84, 0
)"
R"(.LL_121%=:
v_min3_u32 v176, v101, v103, v109
v_max3_u32 v177, v101, v103, v109
v_min3_u32 v176, v176, v111, v117
v_max3_u32 v177, v177, v111, v117
v_min3_u32 v176, v176, v119, v125
v_max3_u32 v177, v177, v119, v125
v_min_u32 v176, v176, v127
v_max_u32 v177, v177, v127
v_cmp_eq_u32 vcc, s13, v176
v_cmp_eq_u32_e64 s[86:87], s13, v177
s_and_b64 vcc, vcc, s[86:87]
s_cmp_eq_u64 vcc, -1
s_cbranch_scc1 .LL_122%=
s_add_u32 s84, s84, 1
s_cmp_lt_u32 s84, 1048576
s_cbranch_scc1 .LL_123%=
.LL_122%=:
v_readlane_b32 s70, v12, s69
v_bfe_u32 v2, v1, 4, 4
s_nop 2
s_lshr_b32 s80, s70, 4
s_and_b32 s70, s70, 15
v_cmp_eq_u32 vcc, s70, v2
s_cmp_eq_u32 s80, s61
s_cselect_b64 s[86:87], vcc, 0
s_cselect_b64 s[88:89], 0, vcc
s_bitcmp1_b32 s14, 0
s_cselect_b64 vcc, s[88:89], s[86:87]
v_lshlrev_b32 v176, 16, v100
v_and_b32 v177, 0xffff0000, v100
v_lshlrev_b32 v178, 16, v102
v_and_b32 v179, 0xffff0000, v102
v_lshlrev_b32 v180, 16, v108
v_and_b32 v181, 0xffff0000, v108
v_lshlrev_b32 v182, 16, v110
v_and_b32 v183, 0xffff0000, v110
A 176,176,40
A 177,177,41
A 178,178,42
A 179,179,43
A 180,180,44
A 181,181,45
A 182,182,46
A 183,183,47
K 184,176,177
K 185,178,179
K 186,180,181
K 187,182,183
v_lshlrev_b32 v176, 16, v184
v_and_b32 v177, 0xffff0000, v184
v_lshlrev_b32 v178, 16, v185
v_and_b32 v179, 0xffff0000, v185
v_lshlrev_b32 v180, 16, v186
v_and_b32 v181, 0xffff0000, v186
v_lshlrev_b32 v182, 16, v187
v_and_b32 v183, 0xffff0000, v187
v_cndmask_b32_e64 v40, v40, v176, s[86:87]
v_cndmask_b32_e64 v41, v41, v177, s[86:87]
v_cndmask_b32_e64 v42, v42, v178, s[86:87]
v_cndmask_b32_e64 v43, v43, v179, s[86:87]
v_cndmask_b32_e64 v44, v44, v180, s[86:87]
v_cndmask_b32_e64 v45, v45, v181, s[86:87]
v_cndmask_b32_e64 v46, v46, v182, s[86:87]
v_cndmask_b32_e64 v47, v47, v183, s[86:87]
v_lshlrev_b32 v176, 16, v100
v_and_b32 v177, 0xffff0000, v100
v_lshlrev_b32 v178, 16, v102
v_and_b32 v179, 0xffff0000, v102
v_lshlrev_b32 v180, 16, v108
v_and_b32 v181, 0xffff0000, v108
v_lshlrev_b32 v182, 16, v110
v_and_b32 v183, 0xffff0000, v110
A 176,176,56
A 177,177,57
A 178,178,58
A 179,179,59
A 180,180,60
A 181,181,61
A 182,182,62
A 183,183,63
K 184,176,177
K 185,178,179
K 186,180,181
K 187,182,183
v_lshlrev_b32 v176, 16, v184
v_and_b32 v177, 0xffff0000, v184
v_lshlrev_b32 v178, 16, v185
v_and_b32 v179, 0xffff0000, v185
v_lshlrev_b32 v180, 16, v186
v_and_b32 v181, 0xffff0000, v186
v_lshlrev_b32 v182, 16, v187
v_and_b32 v183, 0xffff0000, v187
v_cndmask_b32_e64 v56, v56, v176, s[88:89]
v_cndmask_b32_e64 v57, v57, v177, s[88:89]
v_cndmask_b32_e64 v58, v58, v178, s[88:89]
v_cndmask_b32_e64 v59, v59, v179, s[88:89]
v_cndmask_b32_e64 v60, v60, v180, s[88:89]
v_cndmask_b32_e64 v61, v61, v181, s[88:89]
v_cndmask_b32_e64 v62, v62, v182, s[88:89]
v_cndmask_b32_e64 v63, v63, v183, s[88:89]
v_lshlrev_b32 v176, 16, v116
v_and_b32 v177, 0xffff0000, v116
v_lshlrev_b32 v178, 16, v118
v_and_b32 v179, 0xffff0000, v118
v_lshlrev_b32 v180, 16, v124
v_and_b32 v181, 0xffff0000, v124
v_lshlrev_b32 v182, 16, v126
v_and_b32 v183, 0xffff0000, v126
A 176,176,72
A 177,177,73
A 178,178,74
A 179,179,75
A 180,180,76
A 181,181,77
A 182,182,78
A 183,183,79
K 184,176,177
K 185,178,179
K 186,180,181
K 187,182,183
v_lshlrev_b32 v176, 16, v184
v_and_b32 v177, 0xffff0000, v184
v_lshlrev_b32 v178, 16, v185
v_and_b32 v179, 0xffff0000, v185
v_lshlrev_b32 v180, 16, v186
v_and_b32 v181, 0xffff0000, v186
v_lshlrev_b32 v182, 16, v187
v_and_b32 v183, 0xffff0000, v187
v_cndmask_b32_e64 v72, v72, v176, vcc
v_cndmask_b32_e64 v73, v73, v177, vcc
v_cndmask_b32_e64 v74, v74, v178, vcc
v_cndmask_b32_e64 v75, v75, v179, vcc
v_cndmask_b32_e64 v76, v76, v180, vcc
v_cndmask_b32_e64 v77, v77, v181, vcc
v_cndmask_b32_e64 v78, v78, v182, vcc
v_cndmask_b32_e64 v79, v79, v183, vcc
s_bitset0_b32 s68, s69
s_cmp_eq_u32 s68, 0
s_cbranch_scc1 .Lupr_120%=
s_ff1_i32_b32 s69, s68
.LL_123%=:
s_cmp_eq_u32 s64, 0
s_cbranch_scc0 .LL_124%=
s_waitcnt vmcnt(0)
v_lshlrev_b32 v2, 2, v1
v_mov_b64 v[28:29], s[12:13]
s_mov_b64 exec, 3
buffer_store_dwordx2 v[28:29], v2, s[8:11], s53 offen offset:512 sc1
s_mov_b64 exec, -1
s_mov_b32 s64, 1
.LL_124%=:
s_mul_i32 s66, s69, 20
s_mul_i32 s67, s58, 5
s_add_u32 s66, s66, s67
s_lshl_b32 s66, s66, 7
s_add_u32 s66, s66, 98304
s_lshl_b32 s67, s14, 7
s_add_u32 s67, s67, s66
buffer_load_dwordx4 v[100:103], v3, s[8:11], s67 offen offset:0 sc1
buffer_load_dwordx4 v[108:111], v3, s[8:11], s67 offen offset:16 sc1
buffer_load_dwordx4 v[116:119], v3, s[8:11], s66 offen offset:512 sc1
buffer_load_dwordx4 v[124:127], v3, s[8:11], s66 offen offset:528 sc1
s_waitcnt vmcnt(0)
s_branch .LL_121%=
.Lspin_52%=:
s_mov_b32 s84, 0
.Llp_54%=:
s_sleep 2
v_mov_b32 v28, s74
buffer_load_dwordx2 v[28:29], v28, s[8:11], 0 offen offset:3584 sc1
s_waitcnt vmcnt(0)
s_add_u32 s84, s84, 1
v_xor_b32 v28, s12, v28
v_xor_b32 v29, s13, v29
v_lshrrev_b32 v248, 11, v28
v_or_b32 v29, v29, v248
v_cmp_eq_u32 vcc, 0, v29
s_or_b64 vcc, vcc, s[72:73]
s_cmp_eq_u64 vcc, -1
s_cbranch_scc1 .Lok_53%=
s_cmp_lt_u32 s84, 1048576
s_cbranch_scc1 .Llp_54%=
s_branch .Lok_53%=
.Lspin_57%=:
s_mov_b32 s84, 0
.Llp_59%=:
s_sleep 2
v_bfe_u32 v28, v1, 4, 2
v_lshlrev_b32 v28, 3, v28
s_lshl_b32 s80, s56, 9
s_add_u32 s80, s80, 4096
buffer_load_dwordx2 v[28:29], v28, s[8:11], s80 offen sc1
s_waitcnt vmcnt(0)
s_add_u32 s84, s84, 1
v_cmp_eq_u64 vcc, s[12:13], v[28:29]
s_cmp_eq_u64 vcc, -1
s_cbranch_scc1 .Lok_58%=
s_cmp_lt_u32 s84, 1048576
s_cbranch_scc1 .Llp_59%=
s_branch .Lok_58%=
.Lspin_60%=:
s_mov_b32 s84, 0
.Llp_62%=:
s_sleep 2
v_bfe_u32 v28, v1, 4, 4
v_lshlrev_b32 v28, 3, v28
s_lshl_b32 s80, s56, 9
s_add_u32 s80, s80, 4096
buffer_load_dwordx2 v[28:29], v28, s[8:11], s80 offen sc1
s_waitcnt vmcnt(0)
s_add_u32 s84, s84, 1
v_cmp_eq_u64 vcc, s[12:13], v[28:29]
s_cmp_eq_u64 vcc, -1
s_cbranch_scc1 .Lok_61%=
s_cmp_lt_u32 s84, 1048576
s_cbranch_scc1 .Llp_62%=
s_branch .Lok_61%=
.Lspin_63%=:
s_mov_b32 s84, 0
.Llp_65%=:
s_sleep 2
v_bfe_u32 v28, v1, 4, 4
v_lshlrev_b32 v28, 3, v28
s_lshl_b32 s80, s56, 9
s_add_u32 s80, s80, 4224
buffer_load_dwordx2 v[28:29], v28, s[8:11], s80 offen sc1
s_waitcnt vmcnt(0)
s_add_u32 s84, s84, 1
v_cmp_eq_u64 vcc, s[12:13], v[28:29]
s_cmp_eq_u64 vcc, -1
s_cbranch_scc1 .Lok_64%=
s_cmp_lt_u32 s84, 1048576
s_cbranch_scc1 .Llp_65%=
s_branch .Lok_64%=
.Lspin_67%=:
s_mov_b32 s84, 0
.Llp_69%=:
s_sleep 2
v_bfe_u32 v28, v1, 4, 4
v_lshlrev_b32 v28, 3, v28
s_lshl_b32 s80, s56, 9
s_add_u32 s80, s80, 4352
buffer_load_dwordx2 v[28:29], v28, s[8:11], s80 offen sc1
s_waitcnt vmcnt(0)
s_add_u32 s84, s84, 1
v_cmp_eq_u64 vcc, s[12:13], v[28:29]
s_cmp_eq_u64 vcc, -1
s_cbranch_scc1 .Lok_68%=
s_cmp_lt_u32 s84, 1048576
s_cbranch_scc1 .Llp_69%=
s_branch .Lok_68%=
.Lspin_70%=:
s_mov_b32 s84, 0
.Llp_72%=:
s_sleep 2
v_bfe_u32 v28, v1, 4, 4
v_lshlrev_b32 v28, 3, v28
s_lshl_b32 s80, s56, 9
s_add_u32 s80, s80, 4480
buffer_load_dwordx2 v[28:29], v28, s[8:11], s80 offen sc1
s_waitcnt vmcnt(0)
s_add_u32 s84, s84, 1
v_cmp_eq_u64 vcc, s[12:13], v[28:29]
s_cmp_eq_u64 vcc, -1
s_cbranch_scc1 .Lok_71%=
s_cmp_lt_u32 s84, 1048576
s_cbranch_scc1 .Llp_72%=
s_branch .Lok_71%=
.Lspin_73%=:
s_mov_b32 s84, 0
.Llp_75%=:
s_sleep 2
buffer_load_dwordx2 v[8:9], v4, s[8:11], 0 offen offset:2560 sc1
buffer_load_dwordx2 v[10:11], v5, s[8:11], 0 offen offset:3072 sc1
buffer_load_dwordx2 v[12:13], v6, s[8:11], 0 offen offset:3584 sc1
s_waitcnt vmcnt(0)
s_add_u32 s84, s84, 1
v_cmp_eq_u64 vcc, s[12:13], v[8:9]
v_cmp_eq_u64_e64 s[86:87], s[12:13], v[10:11]
s_and_b64 vcc, vcc, s[86:87]
s_or_b64 vcc, vcc, s[20:21]
s_cmp_eq_u64 vcc, -1
s_cbranch_scc1 .Lok_74%=
s_cmp_lt_u32 s84, 1048576
s_cbranch_scc1 .Llp_75%=
s_branch .Lok_74%=
.Lmk_77%=:
s_nop 7
v_cndmask_b32_e64 v168, v241, v168, s[72:73]
v_cndmask_b32_e64 v169, v241, v169, s[74:75]
v_cndmask_b32_e64 v170, v241, v170, s[76:77]
v_cndmask_b32_e64 v171, v241, v171, s[78:79]
v_cndmask_b32_e64 v188, v241, v188, s[72:73]
v_cndmask_b32_e64 v189, v241, v189, s[74:75]
v_cndmask_b32_e64 v190, v241, v190, s[76:77]
v_cndmask_b32_e64 v191, v241, v191, s[78:79]
v_mov_b32 v184, v241
v_mov_b32 v185, v241
v_mov_b32 v186, v241
v_mov_b32 v187, v241
s_branch .Lmkr_78%=
.Lspin_82%=:
s_mov_b32 s84, 0
.Llp_84%=:
s_sleep 2
v_bfe_u32 v28, v1, 4, 2
v_lshlrev_b32 v28, 3, v28
buffer_load_dwordx2 v[28:29], v28, s[8:11], s55 offen sc1
s_waitcnt vmcnt(0)
s_add_u32 s84, s84, 1
v_cmp_eq_u64 vcc, s[12:13], v[28:29]
s_cmp_eq_u64 vcc, -1
s_cbranch_scc1 .Lok_83%=
s_cmp_lt_u32 s84, 1048576
s_cbranch_scc1 .Llp_84%=
s_branch .Lok_83%=
.LL_87%=:
s_mov_b32 s84, 0
.LL_89%=:
s_sleep 2
buffer_load_dwordx2 v[12:13], v27, s[8:11], 0 offen offset:3584 sc1
s_waitcnt vmcnt(0)
s_add_u32 s84, s84, 1
v_xor_b32 v12, s12, v12
v_xor_b32 v13, s13, v13
v_lshrrev_b32 v3, 11, v12
v_or_b32 v13, v13, v3
v_cmp_eq_u32 vcc, 0, v13
s_cmp_eq_u64 vcc, -1
s_cbranch_scc1 .LL_88%=
s_cmp_lt_u32 s84, 1048576
s_cbranch_scc1 .LL_89%=
s_branch .LL_88%=
.Lup_85%=:
v_lshrrev_b32 v3, 8, v1
v_lshlrev_b32 v3, 5, v3
s_ff1_i32_b32 s69, s68
s_mul_i32 s66, s69, 20
s_mul_i32 s67, s58, 5
s_add_u32 s66, s66, s67
s_lshl_b32 s66, s66, 7
s_add_u32 s66, s66, 98304
s_lshl_b32 s67, s14, 7
s_add_u32 s67, s67, s66
buffer_load_dwordx4 v[100:103], v3, s[8:11], s67 offen offset:0 sc1
buffer_load_dwordx4 v[108:111], v3, s[8:11], s67 offen offset:16 sc1
buffer_load_dwordx4 v[116:119], v3, s[8:11], s66 offen offset:512 sc1
buffer_load_dwordx4 v[124:127], v3, s[8:11], s66 offen offset:528 sc1
s_branch .Lupr_86%=
.Lspin_98%=:
s_mov_b32 s84, 0
.Llp_100%=:
s_sleep 2
v_bfe_u32 v28, v1, 4, 2
v_lshlrev_b32 v28, 3, v28
buffer_load_dwordx2 v[28:29], v28, s[8:11], s54 offen sc1
s_waitcnt vmcnt(0)
s_add_u32 s84, s84, 1
v_cmp_eq_u64 vcc, s[12:13], v[28:29]
s_cmp_eq_u64 vcc, -1
s_cbranch_scc1 .Lok_99%=
s_cmp_lt_u32 s84, 1048576
s_cbranch_scc1 .Llp_100%=
s_branch .Lok_99%=
.Lspin_101%=:
s_mov_b32 s84, 0
.Llp_103%=:
s_sleep 2
v_bfe_u32 v28, v1, 4, 2
v_lshlrev_b32 v28, 3, v28
buffer_load_dwordx2 v[28:29], v28, s[8:11], s55 offen sc1
s_waitcnt vmcnt(0)
s_add_u32 s84, s84, 1
v_cmp_eq_u64 vcc, s[12:13], v[28:29]
s_cmp_eq_u64 vcc, -1
s_cbranch_scc1 .Lok_102%=
s_cmp_lt_u32 s84, 1048576
s_cbranch_scc1 .Llp_103%=
s_branch .Lok_102%=
.LL_106%=:
s_mov_b32 s84, 0
.LL_108%=:
s_sleep 2
buffer_load_dwordx2 v[12:13], v27, s[8:11], 0 offen offset:3584 sc1
s_waitcnt vmcnt(0)
s_add_u32 s84, s84, 1
v_xor_b32 v12, s12, v12
v_xor_b32 v13, s13, v13
v_lshrrev_b32 v3, 11, v12
v_or_b32 v13, v13, v3
v_cmp_eq_u32 vcc, 0, v13
s_cmp_eq_u64 vcc, -1
s_cbranch_scc1 .LL_107%=
s_cmp_lt_u32 s84, 1048576
s_cbranch_scc1 .LL_108%=
s_branch .LL_107%=
.Lup_104%=:
v_lshrrev_b32 v3, 8, v1
v_lshlrev_b32 v3, 5, v3
)"
R"(s_ff1_i32_b32 s69, s68
s_mul_i32 s66, s69, 20
s_mul_i32 s67, s58, 5
s_add_u32 s66, s66, s67
s_lshl_b32 s66, s66, 7
s_add_u32 s66, s66, 98304
s_lshl_b32 s67, s14, 7
s_add_u32 s67, s67, s66
buffer_load_dwordx4 v[100:103], v3, s[8:11], s67 offen offset:0 sc1
buffer_load_dwordx4 v[108:111], v3, s[8:11], s67 offen offset:16 sc1
buffer_load_dwordx4 v[116:119], v3, s[8:11], s66 offen offset:512 sc1
buffer_load_dwordx4 v[124:127], v3, s[8:11], s66 offen offset:528 sc1
s_branch .Lupr_105%=
.Lend_250%=:
.Lend%=:
.purgem W
.purgem D
.purgem Ta
.purgem Ra
.purgem Zvaa
.purgem Mvaa
.purgem Zaaa
.purgem Maaa
.purgem A
.purgem U
.purgem K
.purgem Rv
.purgem Mvav
.purgem G
.purgem Zvav
.purgem Cvav
.purgem X
.purgem Cvaa
.purgem Dc
.purgem Maav
.purgem Mvvv
.purgem Zvva
.purgem Mvva
)"
    : : [ka] "s"(__builtin_amdgcn_kernarg_segment_ptr()), [bid] "s"(blockIdx.x), [tid] "v"(threadIdx.x)
    : "v1", "v2", "v3", "v4", "v5", "v6", "v7", "v8", "v9", "v10", "v11", "v12", "v13", "v14", "v15", "v16", "v17", "v18", "v19", "v20", "v21", "v22", "v23", "v24", "v25", "v26", "v27", "v28", "v29", "v30", "v31", "v32", "v33", "v34", "v35", "v36", "v37", "v38", "v39", "v40", "v41", "v42", "v43", "v44", "v45", "v46", "v47", "v48", "v49", "v50", "v51", "v52", "v53", "v54", "v55", "v56", "v57", "v58", "v59", "v60", "v61", "v62", "v63", "v64", "v65", "v66", "v67", "v68", "v69", "v70", "v71", "v72", "v73", "v74", "v75", "v76", "v77", "v78", "v79", "v80", "v81", "v82", "v83", "v84", "v85", "v86", "v87", "v88", "v89", "v90", "v91", "v92", "v93", "v94", "v95", "v96", "v97", "v98", "v99", "v100", "v101", "v102", "v103", "v104", "v105", "v106", "v107", "v108", "v109", "v110", "v111", "v112", "v113", "v114", "v115", "v116", "v117", "v118", "v119", "v120", "v121", "v122", "v123", "v124", "v125", "v126", "v127", "v128", "v129", "v130", "v131", "v132", "v133", "v134", "v135", "v136", "v137", "v138", "v139", "v140", "v141", "v142", "v143", "v144", "v145", "v146", "v147", "v148", "v149", "v150", "v151", "v152", "v153", "v154", "v155", "v156", "v157", "v158", "v159", "v160", "v161", "v162", "v163", "v164", "v165", "v166", "v167", "v168", "v169", "v170", "v171", "v172", "v173", "v174", "v175", "v176", "v177", "v178", "v179", "v180", "v181", "v182", "v183", "v184", "v185", "v186", "v187", "v188", "v189", "v190", "v191", "v192", "v193", "v194", "v195", "v196", "v197", "v198", "v199", "v200", "v201", "v202", "v203", "v204", "v205", "v206", "v207", "v208", "v209", "v210", "v211", "v212", "v213", "v214", "v215", "v216", "v217", "v218", "v219", "v220", "v221", "v222", "v223", "v224", "v225", "v226", "v227", "v228", "v229", "v230", "v231", "v232", "v233", "v234", "v235", "v236", "v237", "v238", "v239", "v240", "v241", "v242", "v243", "v244", "v245", "v246", "v247", "v248", "v249", "v250", "v251", "v252", "v253", "v254", "v255", "a0", "a1", "a2", "a3", "a4", "a5", "a6", "a7", "a8", "a9", "a10", "a11", "a12", "a13", "a14", "a15", "a16", "a17", "a18", "a19", "a20", "a21", "a22", "a23", "a24", "a25", "a26", "a27", "a28", "a29", "a30", "a31", "a32", "a33", "a34", "a35", "a36", "a37", "a38", "a39", "a40", "a41", "a42", "a43", "a44", "a45", "a46", "a47", "a48", "a49", "a50", "a51", "a52", "a53", "a54", "a55", "a56", "a57", "a58", "a59", "a60", "a61", "a62", "a63", "a64", "a65", "a66", "a67", "a68", "a69", "a70", "a71", "a72", "a73", "a74", "a75", "a76", "a77", "a78", "a79", "a80", "a81", "a82", "a83", "a84", "a85", "a86", "a87", "a88", "a89", "a90", "a91", "a92", "a93", "a94", "a95", "a96", "a97", "a98", "a99", "a100", "a101", "a102", "a103", "a104", "a105", "a106", "a107", "a108", "a109", "a110", "a111", "a112", "a113", "a114", "a115", "a116", "a117", "a118", "a119", "a120", "a121", "a122", "a123", "a124", "a125", "a126", "a127", "a128", "a129", "a130", "a131", "a132", "a133", "a134", "a135", "a136", "a137", "a138", "a139", "a140", "a141", "a142", "a143", "a144", "a145", "a146", "a147", "a148", "a149", "a150", "a151", "a152", "a153", "a154", "a155", "a156", "a157", "a158", "a159", "a160", "a161", "a162", "a163", "a164", "a165", "a166", "a167", "a168", "a169", "a170", "a171", "a172", "a173", "a174", "a175", "a176", "a177", "a178", "a179", "a180", "a181", "a182", "a183", "a184", "a185", "a186", "a187", "a188", "a189", "a190", "a191", "a192", "a193", "a194", "a195", "a196", "a197", "a198", "a199", "a200", "a201", "a202", "a203", "a204", "a205", "a206", "a207", "a208", "a209", "a210", "a211", "a212", "a213", "a214", "a215", "a216", "a217", "a218", "a219", "a220", "a221", "a222", "a223", "a224", "a225", "a226", "a227", "a228", "a229", "a230", "a231", "a232", "a233", "a234", "a235", "a236", "a237", "a238", "a239", "a240", "a241", "a242", "a243", "a244", "a245", "a246", "a247", "a248", "a249", "a250", "a251", "a252", "a253", "a254", "a255", "s4", "s5", "s6", "s7", "s8", "s9", "s10", "s11", "s12", "s13", "s14", "s15", "s16", "s17", "s18", "s19", "s20", "s21", "s22", "s23", "s24", "s25", "s26", "s27", "s28", "s29", "s30", "s31", "s32", "s33", "s34", "s35", "s36", "s37", "s38", "s39", "s40", "s41", "s42", "s43", "s44", "s45", "s46", "s47", "s48", "s49", "s50", "s51", "s52", "s53", "s54", "s55", "s56", "s57", "s58", "s59", "s60", "s61", "s62", "s63", "s64", "s65", "s66", "s67", "s68", "s69", "s70", "s71", "s72", "s73", "s74", "s75", "s76", "s77", "s78", "s79", "s80", "s81", "s82", "s83", "s84", "s85", "s86", "s87", "s88", "s89", "s90", "s91", "s92", "s93", "s94", "s95", "s96", "s97", "s98", "s99", "vcc", "scc", "memory"
  );
}
extern "C" void kernel_launch(void* const* d_in, const int* in_sizes, int n_in, void* d_out, int out_size, void* d_ws, size_t ws_size, hipStream_t stream) {
  (void)hipFuncSetAttribute((const void*)mega_kernel, hipFuncAttributeMaxDynamicSharedMemorySize, LDS_BYTES);
  mega_kernel<<<256, 256, LDS_BYTES, stream>>>((const bf16*)d_in[0], (const bf16*)d_in[1], (const bf16*)d_in[2], (const bf16*)d_in[3], (const bf16*)d_in[4], (const bf16*)d_in[5],
      (const bf16*)d_in[7], (const bf16*)d_in[8], (const bf16*)d_in[9], (const bf16*)d_in[10], (char*)d_ws + 0, (bf16*)d_out);
}
```

```cpp
#include <hip/hip_runtime.h>
#include <hip/hip_bf16.h>
typedef __hip_bfloat16 bf16;
constexpr int LDS_BYTES = 163840;
__global__ __launch_bounds__(256, 1) void mega_kernel(const bf16* x, const bf16* ckv, const bf16* wq, const bf16* wuk, const bf16* wuv, const bf16* wo,
    const bf16* w_r, const bf16* wg, const bf16* wu, const bf16* wd, char* ws, bf16* out) {
  asm volatile(
R"(.macro W n
s_waitcnt lgkmcnt(\n)
.endm
.macro D v,r,s,o
buffer_load_dwordx4 v\v, s[\r:\r+3], s\s offen offset:\o lds
.endm
.macro Ta d,a,o
ds_read_b64_tr_b16 a[\d:\d+1], v\a offset:\o
.endm
.macro Ra d,a,o
ds_read_b128 a[\d:\d+3], v\a offset:\o
.endm
.macro Zvaa d,a,b
v_mfma_f32_16x16x32_bf16 v[\d:\d+3], a[\a:\a+3], a[\b:\b+3], 0
.endm
.macro Mvaa d,a,b
v_mfma_f32_16x16x32_bf16 v[\d:\d+3], a[\a:\a+3], a[\b:\b+3], v[\d:\d+3]
.endm
.macro Zaaa d,a,b
v_mfma_f32_16x16x32_bf16 a[\d:\d+3], a[\a:\a+3], a[\b:\b+3], 0
.endm
.macro Maaa d,a,b
v_mfma_f32_16x16x32_bf16 a[\d:\d+3], a[\a:\a+3], a[\b:\b+3], a[\d:\d+3]
.endm
.macro A d,a,b
v_add_f32 v\d, v\a, v\b
.endm
.macro U d,a,b
v_mul_f32 v\d, v\a, v\b
.endm
.macro K d,a,b
v_cvt_pk_bf16_f32 v\d, v\a, v\b
.endm
.macro Rv d,a,o
ds_read_b128 v[\d:\d+3], v\a offset:\o
.endm
.macro Mvav d,a,b
v_mfma_f32_16x16x32_bf16 v[\d:\d+3], a[\a:\a+3], v[\b:\b+3], v[\d:\d+3]
.endm
.macro G d,a
v_accvgpr_read_b32 v\d, a\a
.endm
.macro Zvav d,a,b
v_mfma_f32_16x16x32_bf16 v[\d:\d+3], a[\a:\a+3], v[\b:\b+3], 0
.endm
.macro Cvav d,a,b,c
v_mfma_f32_16x16x32_bf16 v[\d:\d+3], a[\a:\a+3], v[\b:\b+3], v[\c:\c+3]
.endm
.macro X d
v_exp_f32 v\d, v\d
.endm
.macro Cvaa d,a,b,c
v_mfma_f32_16x16x32_bf16 v[\d:\d+3], a[\a:\a+3], a[\b:\b+3], v[\c:\c+3]
.endm
.macro Dc v,r,s,o
buffer_load_dwordx4 v\v, s[\r:\r+3], s\s offen offset:\o sc1 lds
.endm
.macro Maav d,a,b
v_mfma_f32_16x16x32_bf16 a[\d:\d+3], a[\a:\a+3], v[\b:\b+3], a[\d:\d+3]
.endm
.macro Mvvv d,a,b
v_mfma_f32_16x16x32_bf16 v[\d:\d+3], v[\a:\a+3], v[\b:\b+3], v[\d:\d+3]
.endm
.macro Zvva d,a,b
v_mfma_f32_16x16x32_bf16 v[\d:\d+3], v[\a:\a+3], a[\b:\b+3], 0
.endm
.macro Mvva d,a,b
v_mfma_f32_16x16x32_bf16 v[\d:\d+3], v[\a:\a+3], a[\b:\b+3], v[\d:\d+3]
.endm
s_load_dwordx16 s[16:31], %[ka], 0
s_load_dwordx8 s[32:39], %[ka], 64
s_getpc_b64 s[6:7]
.Lbeg%=:
s_mov_b32 s15, %[bid]
v_lshrrev_b32 v2, 6, %[tid]
v_and_b32 v100, 63, %[tid]
v_readfirstlane_b32 s14, v2
v_lshlrev_b32 v1, 4, v100
v_lshrrev_b32 v101, 4, v100
v_and_b32 v102, 15, v100
s_and_b32 s59, s15, 7
s_bfe_u32 s57, s15, 0x30003
s_lshr_b32 s58, s15, 6
s_lshl_b32 s56, s59, 2
s_add_u32 s56, s56, s58
s_lshr_b32 s80, s15, 3
s_lshl_b32 s80, s80, 2
s_add_u32 s80, s80, s14
s_lshl_b32 s80, s80, 11
v_min_u32 v146, 240, v1
v_lshl_add_u32 v146, v146, 3, s80
v_min_u32 v146, .Lend%=-.Lbeg%=-4, v146
global_load_dword v142, v146, s[6:7]
v_and_b32 v88, 0x1f0, v1
v_lshrrev_b32 v105, 1, v101
v_mul_u32_u24 v105, 1280, v105
v_add_u32 v28, v88, v105
s_lshl_b32 s85, s15, 2
s_add_u32 s85, s85, s14
s_mul_i32 s84, s85, 2560
v_and_b32 v105, 1, v101
v_lshl_or_b32 v29, v105, 31, v28
v_lshlrev_b32 v104, 1, v101
v_xor_b32 v104, v104, v102
v_lshlrev_b32 v104, 4, v104
v_lshl_add_u32 v16, v101, 13, v104
v_xor_b32 v105, 128, v16
v_add_u32 v17, 31744, v16
v_add_u32 v18, 63488, v105
v_add_u32 v19, 95232, v105
v_add_u32 v20, 131072, v16
v_add_u32 v21, 162816, v16
v_add_u32 v22, 194560, v105
v_add_u32 v23, 226304, v105
s_lshl_b32 s80, s14, 3
s_add_u32 s80, s80, s57
s_lshl_b32 s80, s80, 15
v_and_b32 v104, 7, v100
v_xor_b32 v104, v104, v101
v_lshlrev_b32 v104, 4, v104
v_lshrrev_b32 v105, 3, v100
v_lshl_add_u32 v104, v105, 10, v104
v_add_u32 v2, s80, v104
v_xor_b32 v105, 64, v2
v_add_u32 v3, 7168, v105
v_add_u32 v4, 14336, v2
v_add_u32 v5, 21504, v105
v_xor_b32 v6, 1835008, v2
v_xor_b32 v7, 1835008, v3
v_xor_b32 v8, 1835008, v4
v_xor_b32 v9, 1835008, v5
s_lshl_b32 s63, s14, 13
s_add_u32 s61, s63, 65536
s_and_b32 s80, s14, 1
s_lshr_b32 s81, s14, 1
s_lshl_b32 s62, s80, 14
s_lshl_b32 s82, s81, 13
s_add_u32 s62, s62, s82
s_lshl_b32 s81, s81, 18
s_lshl_b32 s83, s56, 8
s_add_u32 s81, s81, s83
s_mov_b32 s64, 0
s_cmp_eq_u32 s80, 0
W 0
s_cselect_b32 s44, s22, s24
s_cselect_b32 s45, s23, s25
s_mov_b32 s40, s18
s_and_b32 s41, s19, 0xffff
s_mov_b32 s42, 0x200000
s_mov_b32 s43, 0x20000
s_add_u32 s44, s44, s81
s_addc_u32 s45, s45, 0
s_and_b32 s45, s45, 0xffff
s_mov_b32 s46, 0x400000
s_mov_b32 s47, 0x20000
s_sub_u32 s46, s46, s81
s_load_dwordx2 s[12:13], s[36:37], 0
s_lshl_b32 s65, s64, 12
s_add_u32 m0, s62, 0
s_nop 0
D 16,44,65,0
D 17,44,65,1024
D 18,44,65,2048
D 19,44,65,3072
s_add_u32 m0, s62, 4096
s_nop 0
D 20,44,65,0
D 21,44,65,1024
D 22,44,65,2048
D 23,44,65,3072
s_add_u32 m0, s61, 0
s_nop 0
D 2,40,64,0
D 3,40,64,1024
D 4,40,64,2048
D 5,40,64,3072
s_add_u32 m0, s61, 4096
s_nop 0
D 6,40,64,0
D 7,40,64,1024
D 8,40,64,2048
D 9,40,64,3072
s_mov_b32 s52, s16
s_and_b32 s53, s17, 0xffff
s_mov_b32 s54, 2621440
s_mov_b32 s55, 0x20000
buffer_load_dwordx4 v[32:35], v28, s[52:55], s84 offen
buffer_load_dwordx4 v[36:39], v28, s[52:55], s84 offen offset:512
buffer_load_dwordx4 v[40:43], v29, s[52:55], s84 offen offset:1024
s_mov_b32 s52, s28
s_and_b32 s53, s29, 0xffff
s_mov_b32 s54, 1280
s_mov_b32 s55, 0x20000
buffer_load_dwordx4 v[44:47], v88, s[52:55], 0 offen offset:0
buffer_load_dwordx4 v[48:51], v88, s[52:55], 0 offen offset:512
buffer_load_dwordx4 v[52:55], v88, s[52:55], 0 offen offset:1024
s_mov_b32 s69, 0
s_cmp_eq_u32 s14, 2
s_cbranch_scc1 .LL_1%=
s_cmp_eq_u32 s14, 3
s_cbranch_scc1 .LL_2%=
s_lshl_b32 s80, s58, 3
s_add_u32 s80, s80, s57
s_lshl_b32 s80, s80, 15
s_lshl_b32 s82, s14, 7
s_add_u32 s80, s80, s82
s_add_u32 s80, s80, 256
v_lshl_add_u32 v104, v88, 6, s80
v_and_b32 v105, 512, v1
v_mul_u32_u24 v105, 3584, v105
v_xor_b32 v104, v104, v105
v_mov_b32 v140, s18
v_mov_b32 v141, s19
s_mov_b32 s68, 256
s_branch .LL_3%=
.LL_1%=:
v_lshrrev_b32 v104, 6, v1
v_lshlrev_b32 v104, 13, v104
v_and_b32 v105, 16, v1
v_lshl_add_u32 v104, v105, 3, v104
s_lshl_b32 s80, s57, 17
s_add_u32 s80, s80, s83
s_add_u32 s80, s80, 1048576
v_add_u32 v104, s80, v104
v_and_b32 v105, 32, v1
v_cmp_eq_u32 vcc, 0, v105
v_mov_b32 v140, s24
v_mov_b32 v141, s25
v_mov_b32 v105, s22
v_mov_b32 v106, s23
v_cndmask_b32 v140, v140, v105, vcc
v_cndmask_b32 v141, v141, v106, vcc
s_mov_b32 s68, 1048576
s_branch .LL_3%=
.LL_2%=:
v_lshrrev_b32 v104, 5, v1
v_lshlrev_b32 v104, 13, v104
v_and_b32 v105, 16, v1
v_lshl_add_u32 v104, v105, 3, v104
s_mul_i32 s80, s57, 655360
s_add_u32 s80, s80, s83
v_add_u32 v104, s80, v104
v_mov_b32 v140, s20
v_mov_b32 v141, s21
s_mov_b32 s68, 196608
v_mov_b32 v104, 0
s_mov_b32 s68, 0
.LL_3%=:
v_mov_b32 v105, 0
v_lshl_add_u64 v[140:141], v[104:105], 0, v[140:141]
s_cmp_eq_u32 s14, 3
s_cbranch_scc0 .LL_4%=
s_lshr_b32 s88, s15, 3
s_cmp_lt_u32 s88, 14
s_cbranch_scc0 .LL_4%=
v_lshlrev_b32 v104, 16, v1
s_mov_b64 s[86:87], s[36:37]
s_mov_b32 s89, 38797312
s_cmp_eq_u32 s88, 1
s_cselect_b32 s90, 39845888, 0
s_add_u32 s86, s86, s90
s_addc_u32 s87, s87, 0
s_cmp_eq_u32 s88, 2
s_cselect_b64 s[86:87], s[26:27], s[86:87]
s_cselect_b32 s89, 5242876, s89
s_cmp_eq_u32 s88, 3
s_cselect_b64 s[86:87], s[30:31], s[86:87]
s_cselect_b32 s89, 2211836, s89
s_cmp_eq_u32 s88, 4
s_cselect_b64 s[86:87], s[32:33], s[86:87]
s_cselect_b32 s89, 2211836, s89
s_cmp_eq_u32 s88, 5
s_cselect_b64 s[86:87], s[34:35], s[86:87]
s_cselect_b32 s89, 2211836, s89
s_cmp_eq_u32 s88, 6
s_cselect_b64 s[86:87], s[38:39], s[86:87]
s_cselect_b32 s89, 2621436, s89
s_cmp_eq_u32 s88, 7
s_cselect_b64 s[86:87], s[20:21], s[86:87]
s_cselect_b32 s89, 5242876, s89
s_cmp_eq_u32 s88, 8
s_cselect_b64 s[86:87], s[18:19], s[86:87]
s_cselect_b32 s89, 2097148, s89
s_cmp_eq_u32 s88, 9
s_cselect_b64 s[86:87], s[22:23], s[86:87]
s_cselect_b32 s89, 4194300, s89
s_cmp_eq_u32 s88, 10
s_cselect_b64 s[86:87], s[24:25], s[86:87]
s_cselect_b32 s89, 4194300, s89
s_cmp_eq_u32 s88, 11
s_cselect_b64 s[86:87], s[16:17], s[86:87]
s_cselect_b32 s89, 2621436, s89
s_cmp_eq_u32 s88, 12
s_cselect_b64 s[86:87], s[28:29], s[86:87]
s_cselect_b32 s89, 1276, s89
s_cmp_eq_u32 s88, 13
s_cselect_b64 s[86:87], s[6:7], s[86:87]
s_cselect_b32 s89, 0, s89
v_min_u32 v104, s89, v104
global_load_dword v142, v104, s[86:87] sc1
.LL_4%=:
v_lshrrev_b32 v104, 1, v102
v_xor_b32 v104, v104, v101
v_lshlrev_b32 v104, 4, v104
v_lshl_add_u32 v104, v102, 7, v104
s_lshr_b32 s81, s14, 1
s_lshl_b32 s81, s81, 14
s_add_u32 s81, s81, 65536
v_add_u32 v26, s81, v104
v_xor_b32 v27, 64, v26
v_and_b32 v105, 1, v101
v_lshrrev_b32 v104, 2, v102
v_lshl_or_b32 v105, v105, 2, v104
v_lshl_add_u32 v104, v101, 3, v104
v_lshlrev_b32 v104, 8, v104
v_lshl_add_u32 v104, v105, 5, v104
v_and_b32 v105, 3, v102
v_lshl_add_u32 v10, v105, 3, v104
v_xor_b32 v11, 32, v10
v_xor_b32 v12, 64, v10
v_xor_b32 v13, 96, v10
v_xor_b32 v14, 128, v10
v_xor_b32 v15, 160, v10
v_xor_b32 v24, 192, v10
v_xor_b32 v25, 224, v10
s_mov_b32 s8, s36
s_and_b32 s9, s37, 0xffff
s_mov_b32 s10, 79691776
s_mov_b32 s11, 0x20000
s_add_u32 s48, s20, s83
s_addc_u32 s49, s21, 0
s_and_b32 s49, s49, 0xffff
s_mov_b32 s50, 5242880
s_mov_b32 s51, 0x20000
s_sub_u32 s50, s50, s83
s_lshl_b32 s80, s57, 1
s_and_b32 s81, s14, 1
s_add_u32 s80, s80, s81
s_sub_u32 s81, 31, s80
s_cmp_lt_u32 s14, 2
s_cselect_b32 s60, s80, s81
s_mul_i32 s28, s60, 81920
s_add_u32 s28, s28, 131072
s_add_u32 s4, s28, 10240
s_mov_b32 s80, 0
s_lshl_b32 s81, s80, 9
s_add_u32 s28, s28, s81
s_add_u32 s5, s80, s14
s_cmp_ge_u32 s5, 20
s_cselect_b32 s81, 20, 0
s_sub_u32 s5, s5, s81
s_and_b32 s80, s14, 1
s_mul_i32 s29, s80, 17172480
s_mul_i32 s80, s56, 536576
s_add_u32 s29, s29, s80
s_lshl_b32 s80, s57, 13
s_add_u32 s29, s29, s80
s_add_u32 s29, s29, 4194304
W 0
s_add_u32 s12, s12, 0x7f4a7c15
s_addc_u32 s13, s13, 0x9e3779b9
s_mov_b64 exec, 0
s_cmp_eq_u32 s14, 0
s_cbranch_scc1 .Lh_0_0%=
s_cmp_eq_u32 s14, 1
s_cbranch_scc1 .Lh_0_1%=
s_cmp_eq_u32 s14, 2
s_cbranch_scc1 .Lh_0_2%=
s_branch .Lh_0_3%=
.Lhr_5%=:
s_mov_b64 exec, -1
s_add_u32 s64, s64, 128
s_and_b32 s64, s64, 1023
s_lshl_b32 s65, s64, 12
s_add_u32 m0, s62, 32768
s_nop 0
D 16,44,65,0
D 17,44,65,1024
D 18,44,65,2048
D 19,44,65,3072
s_add_u32 m0, s62, 36864
s_nop 0
D 20,44,65,0
D 21,44,65,1024
D 22,44,65,2048
D 23,44,65,3072
s_bitcmp1_b32 s14, 0
s_cbranch_scc1 .Lv_6%=
s_branch .LL_8%=
.p2align 12
.LL_8%=:
s_waitcnt vmcnt(14)
s_barrier
Ta 0,10,0
Ta 2,10,1024
Ra 32,26,0
Ta 4,10,8192
.Lh_0_0%=:
s_cbranch_execz .Lh_0_0_1%=
Ta 6,10,9216
Ra 128,27,0
Ra 132,27,2048
Ra 36,26,2048
Ra 40,26,4096
Ra 136,27,4096
Ra 140,27,6144
Ra 44,26,6144
Ra 48,26,8192
Ra 144,27,8192
W 6
Zvaa 160,0,32
s_add_u32 m0, s61, 32768
Ra 148,27,10240
Ra 52,26,10240
Ra 56,26,12288
Ra 152,27,12288
Ra 156,27,14336
Ra 60,26,14336
Mvaa 160,4,128
Ta 8,11,0
Ta 10,11,1024
Ta 12,11,8192
Ta 14,11,9216
Zvaa 192,4,132
Ta 16,12,0
Ta 18,12,1024
Mvaa 192,0,36
W 14
Ta 20,12,8192
Ta 22,12,9216
Zvaa 224,0,40
D 2,40,64,0
Mvaa 224,4,136
Zaaa 160,4,140
Maaa 160,0,44
W 12
Zaaa 192,0,48
D 3,40,64,1024
Maaa 192,4,144
Zaaa 224,4,148
Maaa 224,0,52
W 8
Zaaa 64,0,56
D 4,40,64,2048
Maaa 64,4,152
Zaaa 96,4,156
)"
R"(Maaa 96,0,60
W 4
Zaaa 100,8,60
D 5,40,64,3072
Maaa 100,12,156
s_add_u32 m0, s61, 36864
Zaaa 68,12,152
Ta 24,13,0
Ta 26,13,1024
Maaa 68,8,56
Ta 28,13,8192
Ta 30,13,9216
Zaaa 228,8,52
s_waitcnt vmcnt(15)
D 6,40,64,0
Maaa 228,12,148
v_lshlrev_b32 v56, 16, v32
v_and_b32 v57, 0xffff0000, v32
v_lshlrev_b32 v58, 16, v33
v_and_b32 v59, 0xffff0000, v33
v_lshlrev_b32 v60, 16, v34
Zaaa 196,12,144
v_and_b32 v61, 0xffff0000, v34
v_lshlrev_b32 v62, 16, v35
v_and_b32 v63, 0xffff0000, v35
v_lshlrev_b32 v64, 16, v36
v_and_b32 v65, 0xffff0000, v36
Maaa 196,8,48
v_lshlrev_b32 v66, 16, v37
v_and_b32 v67, 0xffff0000, v37
v_lshlrev_b32 v68, 16, v38
v_and_b32 v69, 0xffff0000, v38
v_lshlrev_b32 v70, 16, v39
Zaaa 164,8,44
D 7,40,64,1024
v_and_b32 v71, 0xffff0000, v39
v_lshlrev_b32 v72, 16, v40
v_and_b32 v73, 0xffff0000, v40
v_lshlrev_b32 v74, 16, v41
v_and_b32 v75, 0xffff0000, v41
Maaa 164,12,140
v_lshlrev_b32 v76, 16, v42
v_and_b32 v77, 0xffff0000, v42
v_lshlrev_b32 v78, 16, v43
v_and_b32 v79, 0xffff0000, v43
A 80,56,57
Zvaa 228,12,136
A 81,58,59
A 80,80,60
A 81,81,61
A 80,80,62
A 81,81,63
Mvaa 228,8,40
A 80,80,64
A 81,81,65
A 80,80,66
A 81,81,67
A 80,80,68
Zvaa 196,8,36
D 8,40,64,2048
A 81,81,69
A 80,80,70
A 81,81,71
A 80,80,72
A 81,81,73
Mvaa 196,12,132
A 80,80,74
A 81,81,75
A 80,80,76
A 81,81,77
A 80,80,78
A 81,81,79
Zvaa 164,12,128
A 80,80,81
s_nop 1
v_add_f32_dpp v80, v80, v80 quad_perm:[1,0,3,2] row_mask:0xf bank_mask:0xf
s_nop 1
v_add_f32_dpp v80, v80, v80 quad_perm:[2,3,0,1] row_mask:0xf bank_mask:0xf
s_nop 1
v_add_f32_dpp v80, v80, v80 row_half_mirror row_mask:0xf bank_mask:0xf
s_nop 1
v_add_f32_dpp v80, v80, v80 row_mirror row_mask:0xf bank_mask:0xf
Mvaa 164,8,32
W 4
v_mov_b32 v81, v80
s_nop 1
v_permlane16_swap_b32 v80, v81
A 80,80,81
v_mul_f32 v84, 0x3acccccd, v80
s_mov_b32 s86, 0xffff
Zvaa 168,16,32
s_mov_b32 s87, 0xffff
D 9,40,64,3072
v_sub_f32 v82, v56, v84
U 80,82,82
v_sub_f32 v83, v57, v84
v_fmac_f32 v80, v83, v83
Mvaa 168,20,128
v_sub_f32 v82, v58, v84
v_fmac_f32 v80, v82, v82
v_sub_f32 v83, v59, v84
v_fmac_f32 v80, v83, v83
v_sub_f32 v82, v60, v84
Zvaa 200,20,132
Ta 0,14,0
v_fmac_f32 v80, v82, v82
v_sub_f32 v83, v61, v84
v_fmac_f32 v80, v83, v83
v_sub_f32 v82, v62, v84
v_fmac_f32 v80, v82, v82
Mvaa 200,16,36
Ta 2,14,1024
v_sub_f32 v83, v63, v84
v_fmac_f32 v80, v83, v83
v_sub_f32 v82, v64, v84
v_fmac_f32 v80, v82, v82
v_sub_f32 v83, v65, v84
Zvaa 232,16,40
global_load_dword v142, v[140:141], off
v_lshl_add_u64 v[140:141], v[140:141], 0, s[68:69]
Ta 4,14,8192
Ta 6,14,9216
v_fmac_f32 v80, v83, v83
v_sub_f32 v82, v66, v84
v_fmac_f32 v80, v82, v82
Mvaa 232,20,136
v_sub_f32 v83, v67, v84
v_fmac_f32 v80, v83, v83
v_sub_f32 v82, v68, v84
v_fmac_f32 v80, v82, v82
v_sub_f32 v83, v69, v84
v_fmac_f32 v80, v83, v83
Zaaa 168,20,140
v_sub_f32 v82, v70, v84
v_fmac_f32 v80, v82, v82
v_sub_f32 v83, v71, v84
v_fmac_f32 v80, v83, v83
v_sub_f32 v82, v72, v84
U 81,82,82
Maaa 168,16,44
v_sub_f32 v83, v73, v84
v_fmac_f32 v81, v83, v83
v_sub_f32 v82, v74, v84
v_fmac_f32 v81, v82, v82
v_sub_f32 v83, v75, v84
v_fmac_f32 v81, v83, v83
Zaaa 200,16,48
v_sub_f32 v82, v76, v84
v_fmac_f32 v81, v82, v82
v_sub_f32 v83, v77, v84
v_fmac_f32 v81, v83, v83
v_sub_f32 v82, v78, v84
Maaa 200,20,144
v_fmac_f32 v81, v82, v82
v_sub_f32 v83, v79, v84
v_fmac_f32 v81, v83, v83
v_cndmask_b32_e64 v81, 0, v81, s[86:87]
A 80,80,81
Zaaa 232,20,148
Maaa 232,16,52
v_add_f32_dpp v80, v80, v80 quad_perm:[1,0,3,2] row_mask:0xf bank_mask:0xf
s_nop 1
v_add_f32_dpp v80, v80, v80 quad_perm:[2,3,0,1] row_mask:0xf bank_mask:0xf
s_nop 1
v_add_f32_dpp v80, v80, v80 row_half_mirror row_mask:0xf bank_mask:0xf
s_nop 1
v_add_f32_dpp v80, v80, v80 row_mirror row_mask:0xf bank_mask:0xf
v_mov_b32 v81, v80
s_nop 1
v_permlane16_swap_b32 v80, v81
Zaaa 72,16,56
A 80,80,81
v_mul_f32 v80, 0x3acccccd, v80
K 80,80,80
v_lshlrev_b32 v80, 16, v80
v_add_f32 v80, 0x3727c5ac, v80
K 80,80,80
Maaa 72,20,152
v_lshlrev_b32 v80, 16, v80
v_sqrt_f32 v81, v80
s_nop 0
v_rcp_f32 v82, v81
v_fma_f32 v83, -v81, v81, v80
Zaaa 104,20,156
v_mul_f32 v82, 0.5, v82
v_fma_f32 v81, v83, v82, v81
K 86,81,81
v_lshlrev_b32 v86, 16, v86
v_rcp_f32 v87, v86
Maaa 104,16,60
W 4
K 85,84,84
v_lshlrev_b32 v85, 16, v85
s_waitcnt vmcnt(17)
v_sub_f32 v56, v56, v85
v_sub_f32 v57, v57, v85
K 80,56,57
Zaaa 108,24,60
v_lshlrev_b32 v56, 16, v80
v_and_b32 v57, 0xffff0000, v80
U 80,56,87
U 81,57,87
K 32,80,81
v_lshlrev_b32 v80, 16, v32
Maaa 108,28,156
v_and_b32 v81, 0xffff0000, v32
v_lshlrev_b32 v82, 16, v44
v_and_b32 v83, 0xffff0000, v44
U 84,80,82
v_fmac_f32 v84, v81, v83
Zaaa 76,28,152
Ta 8,15,0
v_sub_f32 v58, v58, v85
v_sub_f32 v59, v59, v85
K 80,58,59
v_lshlrev_b32 v58, 16, v80
v_and_b32 v59, 0xffff0000, v80
Maaa 76,24,56
Ta 10,15,1024
U 80,58,87
U 81,59,87
K 33,80,81
v_lshlrev_b32 v80, 16, v33
v_and_b32 v81, 0xffff0000, v33
Zaaa 236,24,52
Ta 12,15,8192
Ta 14,15,9216
v_lshlrev_b32 v82, 16, v45
v_and_b32 v83, 0xffff0000, v45
v_fmac_f32 v84, v80, v82
v_fmac_f32 v84, v81, v83
Maaa 236,28,148
v_sub_f32 v60, v60, v85
v_sub_f32 v61, v61, v85
K 80,60,61
v_lshlrev_b32 v60, 16, v80
v_and_b32 v61, 0xffff0000, v80
U 80,60,87
Zaaa 204,28,144
U 81,61,87
K 34,80,81
v_lshlrev_b32 v80, 16, v34
v_and_b32 v81, 0xffff0000, v34
v_lshlrev_b32 v82, 16, v46
v_and_b32 v83, 0xffff0000, v46
Maaa 204,24,48
v_fmac_f32 v84, v80, v82
v_fmac_f32 v84, v81, v83
v_sub_f32 v62, v62, v85
v_sub_f32 v63, v63, v85
K 80,62,63
Zaaa 172,24,44
v_lshlrev_b32 v62, 16, v80
v_and_b32 v63, 0xffff0000, v80
U 80,62,87
U 81,63,87
K 35,80,81
Maaa 172,28,140
v_lshlrev_b32 v80, 16, v35
v_and_b32 v81, 0xffff0000, v35
v_lshlrev_b32 v82, 16, v47
v_and_b32 v83, 0xffff0000, v47
v_fmac_f32 v84, v80, v82
Zvaa 236,28,136
v_fmac_f32 v84, v81, v83
v_sub_f32 v64, v64, v85
v_sub_f32 v65, v65, v85
K 80,64,65
v_lshlrev_b32 v64, 16, v80
Mvaa 236,24,40
v_and_b32 v65, 0xffff0000, v80
U 80,64,87
U 81,65,87
K 36,80,81
v_lshlrev_b32 v80, 16, v36
Zvaa 204,24,36
v_and_b32 v81, 0xffff0000, v36
v_lshlrev_b32 v82, 16, v48
v_and_b32 v83, 0xffff0000, v48
v_fmac_f32 v84, v80, v82
v_fmac_f32 v84, v81, v83
Mvaa 204,28,132
v_sub_f32 v66, v66, v85
v_sub_f32 v67, v67, v85
K 80,66,67
v_lshlrev_b32 v66, 16, v80
v_and_b32 v67, 0xffff0000, v80
U 80,66,87
Zvaa 172,28,128
U 81,67,87
K 37,80,81
v_lshlrev_b32 v80, 16, v37
v_and_b32 v81, 0xffff0000, v37
v_lshlrev_b32 v82, 16, v49
Mvaa 172,24,32
W 4
v_and_b32 v83, 0xffff0000, v49
v_fmac_f32 v84, v80, v82
v_fmac_f32 v84, v81, v83
v_sub_f32 v68, v68, v85
v_sub_f32 v69, v69, v85
Zvaa 176,0,32
K 80,68,69
v_lshlrev_b32 v68, 16, v80
v_and_b32 v69, 0xffff0000, v80
U 80,68,87
U 81,69,87
Mvaa 176,4,128
K 38,80,81
v_lshlrev_b32 v80, 16, v38
v_and_b32 v81, 0xffff0000, v38
v_lshlrev_b32 v82, 16, v50
v_and_b32 v83, 0xffff0000, v50
Zvaa 208,4,132
Ta 16,24,0
v_fmac_f32 v84, v80, v82
v_fmac_f32 v84, v81, v83
v_sub_f32 v70, v70, v85
v_sub_f32 v71, v71, v85
K 80,70,71
Mvaa 208,0,36
Ta 18,24,1024
v_lshlrev_b32 v70, 16, v80
v_and_b32 v71, 0xffff0000, v80
U 80,70,87
U 81,71,87
K 39,80,81
Zvaa 240,0,40
Ta 20,24,8192
Ta 22,24,9216
v_lshlrev_b32 v80, 16, v39
v_and_b32 v81, 0xffff0000, v39
v_lshlrev_b32 v82, 16, v51
v_and_b32 v83, 0xffff0000, v51
Mvaa 240,4,136
v_fmac_f32 v84, v80, v82
v_fmac_f32 v84, v81, v83
v_sub_f32 v72, v72, v85
v_sub_f32 v73, v73, v85
K 80,72,73
v_lshlrev_b32 v72, 16, v80
Zaaa 176,4,140
v_and_b32 v73, 0xffff0000, v80
U 80,72,87
U 81,73,87
K 40,80,81
v_lshlrev_b32 v80, 16, v40
v_and_b32 v81, 0xffff0000, v40
Maaa 176,0,44
v_lshlrev_b32 v82, 16, v52
v_and_b32 v83, 0xffff0000, v52
v_fmac_f32 v84, v80, v82
v_fmac_f32 v84, v81, v83
v_sub_f32 v74, v74, v85
Zaaa 208,0,48
v_sub_f32 v75, v75, v85
K 80,74,75
v_lshlrev_b32 v74, 16, v80
v_and_b32 v75, 0xffff0000, v80
U 80,74,87
Maaa 208,4,144
U 81,75,87
K 41,80,81
v_lshlrev_b32 v80, 16, v41
v_and_b32 v81, 0xffff0000, v41
v_lshlrev_b32 v82, 16, v53
Zaaa 240,4,148
v_and_b32 v83, 0xffff0000, v53
v_fmac_f32 v84, v80, v82
v_fmac_f32 v84, v81, v83
v_sub_f32 v76, v76, v85
v_sub_f32 v77, v77, v85
Maaa 240,0,52
K 80,76,77
v_lshlrev_b32 v76, 16, v80
v_and_b32 v77, 0xffff0000, v80
U 80,76,87
U 81,77,87
Zaaa 80,0,56
K 42,80,81
v_lshlrev_b32 v80, 16, v42
v_and_b32 v81, 0xffff0000, v42
v_lshlrev_b32 v82, 16, v54
v_and_b32 v83, 0xffff0000, v54
Maaa 80,4,152
v_fmac_f32 v84, v80, v82
v_fmac_f32 v84, v81, v83
v_sub_f32 v78, v78, v85
v_sub_f32 v79, v79, v85
K 80,78,79
v_lshlrev_b32 v78, 16, v80
Zaaa 112,4,156
v_and_b32 v79, 0xffff0000, v80
U 80,78,87
U 81,79,87
K 43,80,81
v_lshlrev_b32 v80, 16, v43
Maaa 112,0,60
W 4
v_and_b32 v81, 0xffff0000, v43
)"
R"(v_lshlrev_b32 v82, 16, v55
v_and_b32 v83, 0xffff0000, v55
v_fmac_f32 v84, v80, v82
v_fmac_f32 v84, v81, v83
Zaaa 116,8,60
Ta 24,25,0
Ta 26,25,1024
v_lshrrev_b32 v82, 9, v1
s_lshl_b32 s80, s14, 1
v_add_u32 v82, s80, v82
v_mul_u32_u24 v83, 1552, v82
v_add_u32 v83, v83, v88
Maaa 116,12,156
v_add_u32 v83, 131072, v83
ds_write_b128 v83, v[32:35] offset:0
ds_write_b128 v83, v[36:39] offset:512
ds_write_b128 v83, v[40:43] offset:1024
v_add_f32_dpp v84, v84, v84 quad_perm:[1,0,3,2] row_mask:0xf bank_mask:0xf
Zaaa 84,12,152
Ta 28,25,8192
Ta 30,25,9216
Maaa 84,8,56
v_add_f32_dpp v84, v84, v84 quad_perm:[2,3,0,1] row_mask:0xf bank_mask:0xf
s_nop 1
v_add_f32_dpp v84, v84, v84 row_half_mirror row_mask:0xf bank_mask:0xf
s_nop 1
v_add_f32_dpp v84, v84, v84 row_mirror row_mask:0xf bank_mask:0xf
v_mov_b32 v81, v84
s_nop 1
v_permlane16_swap_b32 v84, v81
A 84,84,81
Zaaa 244,8,52
K 80,84,84
v_lshlrev_b32 v80, 16, v80
s_lshl_b32 s80, s15, 3
v_add_u32 v82, s80, v82
v_sub_u32 v83, 2047, v82
v_ashrrev_i32 v81, 31, v80
v_or_b32 v81, 0x80000000, v81
Maaa 244,12,148
v_xor_b32 v80, v80, v81
v_and_b32 v80, 0xffff0000, v80
v_or_b32 v80, v80, v83
v_and_b32 v82, 7, v82
v_lshlrev_b32 v82, 2, v82
v_add_u32 v82, 143488, v82
Zaaa 212,12,144
ds_write_b32 v82, v80
Maaa 212,8,48
Zaaa 180,8,44
Maaa 180,12,140
Zvaa 244,12,136
s_waitcnt vmcnt(1) lgkmcnt(0)
s_barrier
Mvaa 244,8,40
s_add_u32 s64, s64, 128
v_bfe_u32 v80, v1, 4, 3
Zvaa 212,8,36
v_mul_u32_u24 v81, 1552, v80
Mvaa 212,12,132
s_and_b32 s64, s64, 1023
v_lshrrev_b32 v82, 7, v1
Zvaa 180,12,128
v_lshl_add_u32 v81, v82, 4, v81
Mvaa 180,8,32
s_lshl_b32 s80, s14, 7
Zvaa 184,16,32
s_lshl_b32 s65, s64, 12
s_add_u32 s80, s80, 131072
Mvaa 184,20,128
v_add_u32 v81, s80, v81
Zvaa 216,20,132
ds_read_b128 v[32:35], v81
Ta 0,10,32768
Mvaa 216,16,36
s_add_u32 m0, s62, 0
Ta 2,10,33792
Rv 36,81,512
Zvaa 248,16,40
s_and_b32 s80, s14, 2
Ta 4,10,40960
Ta 6,10,41984
Mvaa 248,20,136
s_lshl_b32 s80, s80, 7
Zaaa 184,20,140
D 16,44,65,0
v_subrev_u32 v81, s80, v81
Maaa 184,16,44
Rv 40,81,1024
Zaaa 216,16,48
v_lshlrev_b32 v80, 2, v80
Maaa 216,20,144
v_add_u32 v80, 143488, v80
Zaaa 248,20,148
D 17,44,65,1024
ds_read_b32 v84, v80
Maaa 248,16,52
v_mov_b32 v85, s13
Zaaa 88,16,56
v_lshrrev_b32 v83, 1, v1
Maaa 88,20,152
s_mul_i32 s82, s15, 10240
Zaaa 120,20,156
s_add_u32 s82, s82, 131072
D 18,44,65,2048
Maaa 120,16,60
s_lshl_b32 s80, s14, 10
Zaaa 124,24,60
s_add_u32 s83, s82, s80
Ra 60,26,47104
Maaa 124,28,156
Ra 156,27,47104
Zaaa 92,28,152
D 19,44,65,3072
Ra 152,27,45056
Maaa 92,24,56
s_add_u32 m0, s62, 4096
Ta 8,11,32768
Ta 10,11,33792
Zaaa 252,24,52
Ra 56,26,45056
Ta 12,11,40960
Maaa 252,28,148
Ta 14,11,41984
Ra 52,26,43008
Zaaa 220,28,144
W 9
buffer_store_dwordx4 v[32:35], v1, s[8:11], s83 offen sc1
s_add_u32 s83, s83, 4096
Maaa 220,24,48
Ra 148,27,43008
Ra 144,27,40960
Zaaa 188,24,44
Ra 48,26,40960
Ra 44,26,38912
Maaa 188,28,140
Ra 140,27,38912
Zvaa 252,28,136
D 20,44,65,0
Ra 136,27,36864
Mvaa 252,24,40
Ra 40,26,36864
Zvaa 220,24,36
Ra 36,26,34816
Mvaa 220,28,132
Ra 132,27,34816
Zvaa 188,28,128
buffer_store_dwordx4 v[36:39], v1, s[8:11], s83 offen sc1
Ra 128,27,32768
Mvaa 188,24,32
W 13
s_and_b32 s80, s80, 1024
s_add_u32 s83, s82, s80
Maaa 96,0,60
s_add_u32 s83, s83, 8192
Ra 32,26,32768
Maaa 96,4,156
Maaa 64,4,152
D 21,44,65,1024
Ta 16,12,32768
Maaa 64,0,56
W 9
Ta 18,12,33792
Ta 20,12,40960
Maaa 224,0,52
Ta 22,12,41984
Maaa 224,4,148
Maaa 192,4,144
buffer_store_dwordx4 v[40:43], v1, s[8:11], s83 offen sc1
Maaa 192,0,48
W 8
Maaa 160,0,44
Maaa 160,4,140
Mvaa 224,4,136
D 22,44,65,2048
Mvaa 224,0,40
W 4
Mvaa 192,0,36
Mvaa 192,4,132
Mvaa 160,4,128
D 23,44,65,3072
Mvaa 160,0,32
s_add_u32 m0, s61, 0
Mvaa 164,8,32
Mvaa 164,12,128
Mvaa 196,12,132
D 2,40,64,0
Ta 24,13,32768
Mvaa 196,8,36
Ta 26,13,33792
Ta 28,13,40960
Mvaa 228,8,40
Ta 30,13,41984
Mvaa 228,12,136
.Lh_0_1%=:
s_cbranch_execz .Lh_0_1_1%=
Maaa 164,12,140
D 3,40,64,1024
Maaa 164,8,44
Maaa 196,8,48
Maaa 196,12,144
Maaa 228,12,148
D 4,40,64,2048
Maaa 228,8,52
Maaa 68,8,56
Maaa 68,12,152
Maaa 100,12,156
D 5,40,64,3072
Maaa 100,8,60
W 4
s_add_u32 m0, s61, 4096
Maaa 104,16,60
Maaa 104,20,156
Maaa 72,20,152
D 6,40,64,0
Ta 0,14,32768
Maaa 72,16,56
Ta 2,14,33792
Ta 4,14,40960
Maaa 232,16,52
Ta 6,14,41984
Maaa 232,20,148
Maaa 200,20,144
D 7,40,64,1024
Maaa 200,16,48
Maaa 168,16,44
Maaa 168,20,140
Mvaa 232,20,136
D 8,40,64,2048
Mvaa 232,16,40
Mvaa 200,16,36
Mvaa 200,20,132
Mvaa 168,20,128
D 9,40,64,3072
v_bfe_u32 v147, v1, 5, 3
Mvaa 168,16,32
W 4
v_lshlrev_b32 v147, 13, v147
v_and_b32 v148, 16, v1
Mvaa 172,24,32
v_lshl_add_u32 v147, v148, 3, v147
Mvaa 172,28,128
s_lshl_b32 s92, s57, 2
s_add_u32 s92, s92, s14
s_lshl_b32 s92, s92, 16
Mvaa 204,28,132
buffer_load_dword v142, v147, s[48:51], s92 offen
Ta 8,15,32768
Mvaa 204,24,36
Ta 10,15,33792
Ta 12,15,40960
Mvaa 236,24,40
Ta 14,15,41984
Mvaa 236,28,136
Maaa 172,28,140
Maaa 172,24,44
Maaa 204,24,48
Maaa 204,28,144
Maaa 236,28,148
Maaa 236,24,52
Maaa 76,24,56
Maaa 76,28,152
Maaa 108,28,156
Maaa 108,24,60
W 4
Maaa 112,0,60
Maaa 112,4,156
Maaa 80,4,152
Ta 16,24,32768
Ta 18,24,33792
Maaa 80,0,56
Ta 20,24,40960
Ta 22,24,41984
Maaa 240,0,52
Maaa 240,4,148
Maaa 208,4,144
Maaa 208,0,48
Maaa 176,0,44
Maaa 176,4,140
Mvaa 240,4,136
Mvaa 240,0,40
Mvaa 208,0,36
Mvaa 208,4,132
Mvaa 176,4,128
Mvaa 176,0,32
W 4
Mvaa 180,8,32
Ta 24,25,32768
Ta 26,25,33792
Mvaa 180,12,128
Mvaa 212,12,132
Ta 28,25,40960
Ta 30,25,41984
Mvaa 212,8,36
Mvaa 244,8,40
Mvaa 244,12,136
Maaa 180,12,140
Maaa 180,8,44
Maaa 212,8,48
Maaa 212,12,144
Maaa 244,12,148
s_waitcnt vmcnt(1) lgkmcnt(0)
s_barrier
s_cmp_eq_u32 s14, 0
s_cbranch_scc0 .Lpubr_10%=
s_lshl_b32 s80, s15, 6
s_add_u32 s80, s80, 40960
s_mov_b64 exec, 0xff
buffer_store_dwordx2 v[84:85], v83, s[8:11], s80 offen sc1
s_mov_b64 exec, -1
.Lpubr_10%=:
Maaa 244,8,52
s_add_u32 s64, s64, 128
Maaa 84,8,56
Maaa 84,12,152
s_and_b32 s64, s64, 1023
Maaa 116,12,156
Maaa 116,8,60
Maaa 120,16,60
s_lshl_b32 s65, s64, 12
Maaa 120,20,156
Maaa 88,20,152
s_add_u32 m0, s62, 32768
Ta 0,10,0
Ta 2,10,1024
Maaa 88,16,56
Ta 4,10,8192
Ta 6,10,9216
Maaa 248,16,52
Maaa 248,20,148
D 16,44,65,0
Maaa 216,20,144
Maaa 216,16,48
Maaa 184,16,44
Maaa 184,20,140
D 17,44,65,1024
Mvaa 248,20,136
Mvaa 248,16,40
Mvaa 216,16,36
Mvaa 216,20,132
D 18,44,65,2048
Mvaa 184,20,128
Mvaa 184,16,32
Mvaa 188,24,32
Ra 32,26,0
Mvaa 188,28,128
D 19,44,65,3072
Ra 128,27,0
Mvaa 220,28,132
s_add_u32 m0, s62, 36864
Ra 132,27,2048
Ta 8,11,0
Mvaa 220,24,36
Ta 10,11,1024
Ra 36,26,2048
Mvaa 252,24,40
Ta 12,11,8192
Ta 14,11,9216
Mvaa 252,28,136
D 20,44,65,0
Ra 40,26,4096
Maaa 188,28,140
Ra 136,27,4096
Ra 140,27,6144
Maaa 188,24,44
Ra 44,26,6144
Maaa 220,24,48
Ra 48,26,8192
Maaa 220,28,144
D 21,44,65,1024
Ra 144,27,8192
Maaa 252,28,148
Ra 148,27,10240
Maaa 252,24,52
Ra 52,26,10240
Maaa 92,24,56
Ra 56,26,12288
Maaa 92,28,152
D 22,44,65,2048
Ra 152,27,12288
Maaa 124,28,156
Ra 156,27,14336
Maaa 124,24,60
W 13
Ra 60,26,14336
Mvaa 160,0,32
Mvaa 160,4,128
D 23,44,65,3072
Mvaa 192,4,132
s_add_u32 m0, s61, 32768
Ta 16,12,0
Ta 18,12,1024
Mvaa 192,0,36
W 10
Ta 20,12,8192
Ta 22,12,9216
Mvaa 224,0,40
Mvaa 224,4,136
D 2,40,64,0
Maaa 160,4,140
)"
R"(Maaa 160,0,44
W 8
Maaa 192,0,48
Maaa 192,4,144
D 3,40,64,1024
Maaa 224,4,148
Maaa 224,0,52
W 4
Maaa 64,0,56
Maaa 64,4,152
D 4,40,64,2048
Maaa 96,4,156
Maaa 96,0,60
Maaa 100,8,60
Maaa 100,12,156
D 5,40,64,3072
Maaa 68,12,152
s_add_u32 m0, s61, 36864
Ta 24,13,0
Ta 26,13,1024
Maaa 68,8,56
Ta 28,13,8192
Ta 30,13,9216
Maaa 228,8,52
Maaa 228,12,148
D 6,40,64,0
Maaa 196,12,144
Maaa 196,8,48
Maaa 164,8,44
Maaa 164,12,140
D 7,40,64,1024
Mvaa 228,12,136
Mvaa 228,8,40
Mvaa 196,8,36
Mvaa 196,12,132
D 8,40,64,2048
Mvaa 164,12,128
Mvaa 164,8,32
W 4
Mvaa 168,16,32
Mvaa 168,20,128
D 9,40,64,3072
Mvaa 200,20,132
Ta 0,14,0
Ta 2,14,1024
Mvaa 200,16,36
Ta 4,14,8192
Ta 6,14,9216
Mvaa 232,16,40
Mvaa 232,20,136
global_load_dword v142, v[140:141], off
v_lshl_add_u64 v[140:141], v[140:141], 0, s[68:69]
Maaa 168,20,140
Maaa 168,16,44
Maaa 200,16,48
Maaa 200,20,144
Maaa 232,20,148
Maaa 232,16,52
Maaa 72,16,56
Maaa 72,20,152
Maaa 104,20,156
Maaa 104,16,60
W 4
Maaa 108,24,60
Maaa 108,28,156
Maaa 76,28,152
Ta 8,15,0
Ta 10,15,1024
Maaa 76,24,56
Ta 12,15,8192
Ta 14,15,9216
Maaa 236,24,52
Maaa 236,28,148
Maaa 204,28,144
Maaa 204,24,48
Maaa 172,24,44
Maaa 172,28,140
Mvaa 236,28,136
Mvaa 236,24,40
Mvaa 204,24,36
Mvaa 204,28,132
Mvaa 172,28,128
Mvaa 172,24,32
W 4
Mvaa 176,0,32
Mvaa 176,4,128
Mvaa 208,4,132
Ta 16,24,0
Ta 18,24,1024
Mvaa 208,0,36
Ta 20,24,8192
Ta 22,24,9216
Mvaa 240,0,40
Mvaa 240,4,136
Maaa 176,4,140
Maaa 176,0,44
Maaa 208,0,48
Maaa 208,4,144
Maaa 240,4,148
Maaa 240,0,52
Maaa 80,0,56
Maaa 80,4,152
Maaa 112,4,156
Maaa 112,0,60
W 4
Maaa 116,8,60
Ta 24,25,0
Ta 26,25,1024
Maaa 116,12,156
Maaa 84,12,152
Ta 28,25,8192
Ta 30,25,9216
Maaa 84,8,56
Maaa 244,8,52
Maaa 244,12,148
Maaa 212,12,144
Maaa 212,8,48
Maaa 180,8,44
Maaa 180,12,140
Mvaa 244,12,136
s_waitcnt vmcnt(1) lgkmcnt(0)
s_barrier
Mvaa 244,8,40
s_add_u32 s64, s64, 128
Mvaa 212,8,36
Mvaa 212,12,132
s_and_b32 s64, s64, 1023
Mvaa 180,12,128
Mvaa 180,8,32
Mvaa 184,16,32
s_lshl_b32 s65, s64, 12
Mvaa 184,20,128
Mvaa 216,20,132
s_add_u32 m0, s62, 0
Ta 0,10,32768
Ta 2,10,33792
Mvaa 216,16,36
Ta 4,10,40960
Ta 6,10,41984
Mvaa 248,16,40
Mvaa 248,20,136
D 16,44,65,0
Maaa 184,20,140
Maaa 184,16,44
Maaa 216,16,48
Maaa 216,20,144
D 17,44,65,1024
Maaa 248,20,148
Maaa 248,16,52
Maaa 88,16,56
Maaa 88,20,152
D 18,44,65,2048
Maaa 120,20,156
Maaa 120,16,60
Maaa 124,24,60
Ra 60,26,47104
Maaa 124,28,156
D 19,44,65,3072
Ra 156,27,47104
Maaa 92,28,152
s_add_u32 m0, s62, 4096
Ra 152,27,45056
Ta 8,11,32768
Maaa 92,24,56
Ta 10,11,33792
Ra 56,26,45056
Maaa 252,24,52
Ta 12,11,40960
Ta 14,11,41984
Maaa 252,28,148
D 20,44,65,0
Ra 52,26,43008
Maaa 220,28,144
Ra 148,27,43008
Ra 144,27,40960
Maaa 220,24,48
Ra 48,26,40960
Maaa 188,24,44
Ra 44,26,38912
Maaa 188,28,140
D 21,44,65,1024
Ra 140,27,38912
Mvaa 252,28,136
Ra 136,27,36864
Mvaa 252,24,40
Ra 40,26,36864
Mvaa 220,24,36
Ra 36,26,34816
Mvaa 220,28,132
D 22,44,65,2048
Ra 132,27,34816
Mvaa 188,28,128
Ra 128,27,32768
Mvaa 188,24,32
W 13
Ra 32,26,32768
Maaa 96,0,60
Maaa 96,4,156
D 23,44,65,3072
Maaa 64,4,152
s_add_u32 m0, s61, 0
Ta 16,12,32768
Ta 18,12,33792
Maaa 64,0,56
W 10
Ta 20,12,40960
Ta 22,12,41984
Maaa 224,0,52
Maaa 224,4,148
D 2,40,64,0
Maaa 192,4,144
Maaa 192,0,48
W 8
Maaa 160,0,44
Maaa 160,4,140
D 3,40,64,1024
Mvaa 224,4,136
Mvaa 224,0,40
W 4
Mvaa 192,0,36
Mvaa 192,4,132
D 4,40,64,2048
Mvaa 160,4,128
Mvaa 160,0,32
Mvaa 164,8,32
Mvaa 164,12,128
D 5,40,64,3072
Mvaa 196,12,132
s_add_u32 m0, s61, 4096
Ta 24,13,32768
Ta 26,13,33792
Mvaa 196,8,36
Ta 28,13,40960
Ta 30,13,41984
Mvaa 228,8,40
Mvaa 228,12,136
D 6,40,64,0
Maaa 164,12,140
Maaa 164,8,44
Maaa 196,8,48
Maaa 196,12,144
D 7,40,64,1024
Maaa 228,12,148
Maaa 228,8,52
Maaa 68,8,56
Maaa 68,12,152
D 8,40,64,2048
Maaa 100,12,156
Maaa 100,8,60
W 4
Maaa 104,16,60
Maaa 104,20,156
D 9,40,64,3072
Maaa 72,20,152
Ta 0,14,32768
Ta 2,14,33792
Maaa 72,16,56
Ta 4,14,40960
Ta 6,14,41984
Maaa 232,16,52
Maaa 232,20,148
global_load_dword v142, v[140:141], off
v_lshl_add_u64 v[140:141], v[140:141], 0, s[68:69]
Maaa 200,20,144
Maaa 200,16,48
Maaa 168,16,44
Maaa 168,20,140
Mvaa 232,20,136
Mvaa 232,16,40
Mvaa 200,16,36
Mvaa 200,20,132
Mvaa 168,20,128
Mvaa 168,16,32
W 4
Mvaa 172,24,32
Mvaa 172,28,128
Mvaa 204,28,132
Ta 8,15,32768
Ta 10,15,33792
Mvaa 204,24,36
Ta 12,15,40960
Ta 14,15,41984
Mvaa 236,24,40
Mvaa 236,28,136
Maaa 172,28,140
Maaa 172,24,44
Maaa 204,24,48
Maaa 204,28,144
Maaa 236,28,148
Maaa 236,24,52
Maaa 76,24,56
Maaa 76,28,152
Maaa 108,28,156
Maaa 108,24,60
W 4
Maaa 112,0,60
Maaa 112,4,156
Maaa 80,4,152
Ta 16,24,32768
Ta 18,24,33792
Maaa 80,0,56
Ta 20,24,40960
Ta 22,24,41984
Maaa 240,0,52
Maaa 240,4,148
Maaa 208,4,144
Maaa 208,0,48
Maaa 176,0,44
Maaa 176,4,140
Mvaa 240,4,136
Mvaa 240,0,40
Mvaa 208,0,36
Mvaa 208,4,132
Mvaa 176,4,128
Mvaa 176,0,32
W 4
Mvaa 180,8,32
Ta 24,25,32768
Ta 26,25,33792
Mvaa 180,12,128
Mvaa 212,12,132
Ta 28,25,40960
Ta 30,25,41984
Mvaa 212,8,36
Mvaa 244,8,40
Mvaa 244,12,136
.Lh_0_2%=:
s_cbranch_execz .Lh_0_2_1%=
Maaa 180,12,140
Maaa 180,8,44
Maaa 212,8,48
Maaa 212,12,144
Maaa 244,12,148
s_waitcnt vmcnt(1) lgkmcnt(0)
s_barrier
Maaa 244,8,52
s_add_u32 s64, s64, 128
Maaa 84,8,56
Maaa 84,12,152
s_and_b32 s64, s64, 1023
Maaa 116,12,156
Maaa 116,8,60
Maaa 120,16,60
s_lshl_b32 s65, s64, 12
Maaa 120,20,156
Maaa 88,20,152
Ta 0,10,0
Ta 2,10,1024
Maaa 88,16,56
s_add_u32 m0, s62, 32768
Ta 4,10,8192
Ta 6,10,9216
Maaa 248,16,52
Maaa 248,20,148
Maaa 216,20,144
D 16,44,65,0
Maaa 216,16,48
Maaa 184,16,44
Maaa 184,20,140
Mvaa 248,20,136
D 17,44,65,1024
Mvaa 248,16,40
Mvaa 216,16,36
Mvaa 216,20,132
Mvaa 184,20,128
D 18,44,65,2048
Mvaa 184,16,32
Mvaa 188,24,32
Ra 32,26,0
Mvaa 188,28,128
Ra 128,27,0
Mvaa 220,28,132
D 19,44,65,3072
Ra 132,27,2048
Mvaa 220,24,36
s_add_u32 m0, s62, 36864
Ta 8,11,0
Ta 10,11,1024
Mvaa 252,24,40
Ra 36,26,2048
Ta 12,11,8192
Mvaa 252,28,136
Ta 14,11,9216
Ra 40,26,4096
Maaa 188,28,140
D 20,44,65,0
Ra 136,27,4096
Maaa 188,24,44
Ra 140,27,6144
Ra 44,26,6144
Maaa 220,24,48
Ra 48,26,8192
Maaa 220,28,144
Ra 144,27,8192
Maaa 252,28,148
D 21,44,65,1024
Ra 148,27,10240
Maaa 252,24,52
Ra 52,26,10240
Maaa 92,24,56
)"
R"(Ra 56,26,12288
Maaa 92,28,152
Ra 152,27,12288
Maaa 124,28,156
D 22,44,65,2048
Ra 156,27,14336
Maaa 124,24,60
W 13
Ra 60,26,14336
Mvaa 160,0,32
Mvaa 160,4,128
Mvaa 192,4,132
D 23,44,65,3072
Ta 16,12,0
Mvaa 192,0,36
W 9
s_add_u32 m0, s61, 32768
Ta 18,12,1024
Mvaa 224,0,40
Ta 20,12,8192
Ta 22,12,9216
Mvaa 224,4,136
Maaa 160,4,140
D 2,40,64,0
Maaa 160,0,44
W 8
Maaa 192,0,48
Maaa 192,4,144
Maaa 224,4,148
D 3,40,64,1024
Maaa 224,0,52
W 4
Maaa 64,0,56
Maaa 64,4,152
Maaa 96,4,156
D 4,40,64,2048
Maaa 96,0,60
Maaa 100,8,60
Maaa 100,12,156
Maaa 68,12,152
D 5,40,64,3072
Ta 24,13,0
Maaa 68,8,56
s_add_u32 m0, s61, 36864
Ta 26,13,1024
Ta 28,13,8192
Maaa 228,8,52
Ta 30,13,9216
Maaa 228,12,148
Maaa 196,12,144
D 6,40,64,0
Maaa 196,8,48
Maaa 164,8,44
Maaa 164,12,140
Mvaa 228,12,136
D 7,40,64,1024
Mvaa 228,8,40
Mvaa 196,8,36
Mvaa 196,12,132
Mvaa 164,12,128
D 8,40,64,2048
Mvaa 164,8,32
W 4
Mvaa 168,16,32
Mvaa 168,20,128
Mvaa 200,20,132
D 9,40,64,3072
Ta 0,14,0
Mvaa 200,16,36
s_add_u32 s92, s92, 2097152
Ta 2,14,1024
Ta 4,14,8192
Mvaa 232,16,40
Ta 6,14,9216
Mvaa 232,20,136
Maaa 168,20,140
buffer_load_dword v142, v147, s[48:51], s92 offen
Maaa 168,16,44
s_add_u32 s92, s92, 2097152
Maaa 200,16,48
Maaa 200,20,144
Maaa 232,20,148
buffer_load_dword v142, v147, s[48:51], s92 offen
Maaa 232,16,52
Maaa 72,16,56
Maaa 72,20,152
Maaa 104,20,156
Maaa 104,16,60
W 4
Maaa 108,24,60
Maaa 108,28,156
Maaa 76,28,152
Ta 8,15,0
Ta 10,15,1024
Maaa 76,24,56
Ta 12,15,8192
Ta 14,15,9216
Maaa 236,24,52
Maaa 236,28,148
Maaa 204,28,144
Maaa 204,24,48
Maaa 172,24,44
Maaa 172,28,140
Mvaa 236,28,136
Mvaa 236,24,40
Mvaa 204,24,36
Mvaa 204,28,132
Mvaa 172,28,128
Mvaa 172,24,32
W 4
Mvaa 176,0,32
Mvaa 176,4,128
Mvaa 208,4,132
Ta 16,24,0
Ta 18,24,1024
Mvaa 208,0,36
Ta 20,24,8192
Ta 22,24,9216
Mvaa 240,0,40
Mvaa 240,4,136
Maaa 176,4,140
Maaa 176,0,44
Maaa 208,0,48
Maaa 208,4,144
Maaa 240,4,148
Maaa 240,0,52
Maaa 80,0,56
Maaa 80,4,152
Maaa 112,4,156
Maaa 112,0,60
W 4
Maaa 116,8,60
Ta 24,25,0
Ta 26,25,1024
Maaa 116,12,156
Maaa 84,12,152
Ta 28,25,8192
Ta 30,25,9216
Maaa 84,8,56
Maaa 244,8,52
Maaa 244,12,148
Maaa 212,12,144
Maaa 212,8,48
Maaa 180,8,44
Maaa 180,12,140
Mvaa 244,12,136
s_waitcnt vmcnt(2) lgkmcnt(0)
s_barrier
Mvaa 244,8,40
s_add_u32 s64, s64, 128
Mvaa 212,8,36
Mvaa 212,12,132
s_and_b32 s64, s64, 1023
Mvaa 180,12,128
Mvaa 180,8,32
Mvaa 184,16,32
s_lshl_b32 s65, s64, 12
Mvaa 184,20,128
Mvaa 216,20,132
Ta 0,10,32768
Ta 2,10,33792
Mvaa 216,16,36
s_add_u32 m0, s62, 0
Ta 4,10,40960
Ta 6,10,41984
Mvaa 248,16,40
Mvaa 248,20,136
Maaa 184,20,140
D 16,44,65,0
Maaa 184,16,44
Maaa 216,16,48
Maaa 216,20,144
Maaa 248,20,148
D 17,44,65,1024
Maaa 248,16,52
Maaa 88,16,56
Maaa 88,20,152
Maaa 120,20,156
D 18,44,65,2048
Maaa 120,16,60
Maaa 124,24,60
Ra 60,26,47104
Maaa 124,28,156
Ra 156,27,47104
Maaa 92,28,152
D 19,44,65,3072
Ra 152,27,45056
Maaa 92,24,56
s_add_u32 m0, s62, 4096
Ta 8,11,32768
Ta 10,11,33792
Maaa 252,24,52
Ra 56,26,45056
Ta 12,11,40960
Maaa 252,28,148
Ta 14,11,41984
Ra 52,26,43008
Maaa 220,28,144
D 20,44,65,0
v_lshrrev_b32 v28, 1, v1
Maaa 220,24,48
s_lshl_b32 s80, s60, 9
Ra 148,27,43008
Ra 144,27,40960
Maaa 188,24,44
s_add_u32 s80, s80, 40960
Ra 48,26,40960
Ra 44,26,38912
Maaa 188,28,140
Ra 140,27,38912
Mvaa 252,28,136
D 21,44,65,1024
Ra 136,27,36864
Mvaa 252,24,40
Ra 40,26,36864
Mvaa 220,24,36
Ra 36,26,34816
Mvaa 220,28,132
Ra 132,27,34816
Mvaa 188,28,128
buffer_load_dwordx2 v[28:29], v28, s[8:11], s80 offen sc1
Ra 128,27,32768
Mvaa 188,24,32
W 13
Ra 32,26,32768
Maaa 96,0,60
Maaa 96,4,156
Maaa 64,4,152
D 22,44,65,2048
Ta 16,12,32768
Maaa 64,0,56
W 9
Ta 18,12,33792
Ta 20,12,40960
Maaa 224,0,52
Ta 22,12,41984
Maaa 224,4,148
Maaa 192,4,144
D 23,44,65,3072
Maaa 192,0,48
W 8
s_add_u32 m0, s61, 0
Maaa 160,0,44
Maaa 160,4,140
Mvaa 224,4,136
D 2,40,64,0
Mvaa 224,0,40
W 4
Mvaa 192,0,36
Mvaa 192,4,132
Mvaa 160,4,128
D 3,40,64,1024
Mvaa 160,0,32
Mvaa 164,8,32
Mvaa 164,12,128
Mvaa 196,12,132
D 4,40,64,2048
Ta 24,13,32768
Mvaa 196,8,36
Ta 26,13,33792
Ta 28,13,40960
Mvaa 228,8,40
Ta 30,13,41984
Mvaa 228,12,136
Maaa 164,12,140
D 5,40,64,3072
Maaa 164,8,44
s_add_u32 m0, s61, 4096
Maaa 196,8,48
Maaa 196,12,144
Maaa 228,12,148
D 6,40,64,0
Maaa 228,8,52
Maaa 68,8,56
Maaa 68,12,152
Maaa 100,12,156
D 7,40,64,1024
Maaa 100,8,60
W 4
Maaa 104,16,60
Maaa 104,20,156
Maaa 72,20,152
D 8,40,64,2048
Ta 0,14,32768
Maaa 72,16,56
Ta 2,14,33792
Ta 4,14,40960
Maaa 232,16,52
Ta 6,14,41984
Maaa 232,20,148
Maaa 200,20,144
D 9,40,64,3072
Maaa 200,16,48
Maaa 168,16,44
Maaa 168,20,140
Mvaa 232,20,136
s_waitcnt vmcnt(10)
v_cmp_eq_u32 vcc, s13, v29
s_cmp_eq_u64 vcc, -1
s_cbranch_scc0 .Lspin_11%=
.Lok_12%=:
global_load_dword v142, v146, s[6:7]
Mvaa 232,16,40
Mvaa 200,16,36
v_bfe_u32 v144, v1, 8, 1
Mvaa 200,20,132
v_mul_u32_u24 v144, 10240, v144
Mvaa 168,20,128
v_and_b32 v145, 0xf0, v1
Mvaa 168,16,32
W 4
v_lshl_add_u32 v144, v145, 3, v144
Mvaa 172,24,32
s_mul_i32 s91, s58, 20480
Mvaa 172,28,128
s_add_u32 s91, s91, s28
Mvaa 204,28,132
buffer_load_dword v142, v144, s[8:11], s91 offen offset:0 sc1
Ta 8,15,32768
Mvaa 204,24,36
Ta 10,15,33792
Ta 12,15,40960
Mvaa 236,24,40
Ta 14,15,41984
Mvaa 236,28,136
Maaa 172,28,140
Maaa 172,24,44
Maaa 204,24,48
Maaa 204,28,144
Maaa 236,28,148
Maaa 236,24,52
Maaa 76,24,56
Maaa 76,28,152
Maaa 108,28,156
Maaa 108,24,60
W 4
Maaa 112,0,60
Maaa 112,4,156
Maaa 80,4,152
Ta 16,24,32768
Ta 18,24,33792
Maaa 80,0,56
Ta 20,24,40960
Ta 22,24,41984
Maaa 240,0,52
Maaa 240,4,148
Maaa 208,4,144
Maaa 208,0,48
Maaa 176,0,44
Maaa 176,4,140
Mvaa 240,4,136
Mvaa 240,0,40
Mvaa 208,0,36
Mvaa 208,4,132
Mvaa 176,4,128
Mvaa 176,0,32
W 4
Mvaa 180,8,32
Ta 24,25,32768
Ta 26,25,33792
Mvaa 180,12,128
Mvaa 212,12,132
Ta 28,25,40960
Ta 30,25,41984
Mvaa 212,8,36
Mvaa 244,8,40
Mvaa 244,12,136
Maaa 180,12,140
Maaa 180,8,44
Maaa 212,8,48
Maaa 212,12,144
Maaa 244,12,148
s_waitcnt vmcnt(2) lgkmcnt(0)
s_barrier
Maaa 244,8,52
s_add_u32 s64, s64, 128
Maaa 84,8,56
Maaa 84,12,152
s_and_b32 s64, s64, 1023
Maaa 116,12,156
Maaa 116,8,60
Maaa 120,16,60
s_lshl_b32 s65, s64, 12
Maaa 120,20,156
Maaa 88,20,152
Ta 0,10,0
Ta 2,10,1024
Maaa 88,16,56
s_add_u32 m0, s62, 32768
Ta 4,10,8192
Ta 6,10,9216
Maaa 248,16,52
Maaa 248,20,148
Maaa 216,20,144
D 16,44,65,0
Maaa 216,16,48
Maaa 184,16,44
Maaa 184,20,140
Mvaa 248,20,136
D 17,44,65,1024
)"
R"(Mvaa 248,16,40
Mvaa 216,16,36
Mvaa 216,20,132
Mvaa 184,20,128
D 18,44,65,2048
Mvaa 184,16,32
Mvaa 188,24,32
Ra 32,26,0
Mvaa 188,28,128
Ra 128,27,0
Mvaa 220,28,132
D 19,44,65,3072
Ra 132,27,2048
Mvaa 220,24,36
s_add_u32 m0, s62, 36864
Ta 8,11,0
Ta 10,11,1024
Mvaa 252,24,40
Ra 36,26,2048
Ta 12,11,8192
Mvaa 252,28,136
Ta 14,11,9216
Ra 40,26,4096
Maaa 188,28,140
D 20,44,65,0
Ra 136,27,4096
Maaa 188,24,44
Ra 140,27,6144
Ra 44,26,6144
Maaa 220,24,48
Ra 48,26,8192
Maaa 220,28,144
Ra 144,27,8192
Maaa 252,28,148
D 21,44,65,1024
Ra 148,27,10240
Maaa 252,24,52
Ra 52,26,10240
Maaa 92,24,56
Ra 56,26,12288
Maaa 92,28,152
Ra 152,27,12288
Maaa 124,28,156
D 22,44,65,2048
Ra 156,27,14336
Maaa 124,24,60
W 13
Ra 60,26,14336
Mvaa 160,0,32
Mvaa 160,4,128
Mvaa 192,4,132
D 23,44,65,3072
Ta 16,12,0
Mvaa 192,0,36
W 9
s_add_u32 m0, s61, 32768
Ta 18,12,1024
Mvaa 224,0,40
Ta 20,12,8192
Ta 22,12,9216
Mvaa 224,4,136
.Lh_0_3%=:
s_cbranch_execz .Lh_0_3_1%=
Maaa 160,4,140
D 2,40,64,0
Maaa 160,0,44
W 8
Maaa 192,0,48
Maaa 192,4,144
Maaa 224,4,148
D 3,40,64,1024
Maaa 224,0,52
W 4
Maaa 64,0,56
Maaa 64,4,152
Maaa 96,4,156
D 4,40,64,2048
Maaa 96,0,60
Maaa 100,8,60
Maaa 100,12,156
Maaa 68,12,152
D 5,40,64,3072
Ta 24,13,0
Maaa 68,8,56
s_add_u32 m0, s61, 36864
Ta 26,13,1024
Ta 28,13,8192
Maaa 228,8,52
Ta 30,13,9216
Maaa 228,12,148
Maaa 196,12,144
D 6,40,64,0
Maaa 196,8,48
Maaa 164,8,44
Maaa 164,12,140
Mvaa 228,12,136
D 7,40,64,1024
Mvaa 228,8,40
Mvaa 196,8,36
Mvaa 196,12,132
Mvaa 164,12,128
D 8,40,64,2048
Mvaa 164,8,32
W 4
Mvaa 168,16,32
Mvaa 168,20,128
Mvaa 200,20,132
D 9,40,64,3072
v_add_u32 v2, 131072, v10
Mvaa 200,16,36
Ta 0,14,0
v_add_u32 v3, 131072, v11
Mvaa 232,16,40
Ta 2,14,1024
v_add_u32 v4, 131072, v12
Mvaa 232,20,136
Ta 4,14,8192
Ta 6,14,9216
Maaa 168,20,140
buffer_load_dword v142, v144, s[8:11], s91 offen offset:2048 sc1
v_add_u32 v5, 131072, v13
Maaa 168,16,44
s_add_u32 s91, s91, 4096
v_add_u32 v6, 131072, v14
Maaa 200,16,48
v_add_u32 v7, 131072, v15
Maaa 200,20,144
Maaa 232,20,148
v_add_u32 v8, 131072, v24
Maaa 232,16,52
Maaa 72,16,56
v_add_u32 v9, 131072, v25
Maaa 72,20,152
s_add_u32 s65, s5, 0
s_cmp_ge_u32 s65, 20
s_cselect_b32 s90, 20, 0
s_sub_u32 s65, s65, s90
Maaa 104,20,156
s_lshl_b32 s65, s65, 18
s_add_u32 m0, s63, 131072
Maaa 104,16,60
W 4
Maaa 108,24,60
Maaa 108,28,156
Maaa 76,28,152
Ta 8,15,0
Ta 10,15,1024
Maaa 76,24,56
D 16,48,65,0
Ta 12,15,8192
Maaa 236,24,52
Ta 14,15,9216
Maaa 236,28,148
Maaa 204,28,144
Maaa 204,24,48
D 17,48,65,1024
Maaa 172,24,44
Maaa 172,28,140
Mvaa 236,28,136
Mvaa 236,24,40
Mvaa 204,24,36
D 18,48,65,2048
Mvaa 204,28,132
Mvaa 172,28,128
Mvaa 172,24,32
W 4
Mvaa 176,0,32
Mvaa 176,4,128
D 19,48,65,3072
Mvaa 208,4,132
s_add_u32 m0, s63, 135168
Ta 16,24,0
Ta 18,24,1024
Mvaa 208,0,36
Ta 20,24,8192
Ta 22,24,9216
Mvaa 240,0,40
Mvaa 240,4,136
Maaa 176,4,140
D 20,48,65,0
Maaa 176,0,44
Maaa 208,0,48
Maaa 208,4,144
Maaa 240,4,148
Maaa 240,0,52
D 21,48,65,1024
Maaa 80,0,56
Maaa 80,4,152
Maaa 112,4,156
Maaa 112,0,60
W 4
Maaa 116,8,60
D 22,48,65,2048
Ta 24,25,0
Maaa 116,12,156
Ta 26,25,1024
Maaa 84,12,152
Ta 28,25,8192
Ta 30,25,9216
Maaa 84,8,56
Maaa 244,8,52
D 23,48,65,3072
Maaa 244,12,148
Maaa 212,12,144
Maaa 212,8,48
Maaa 180,8,44
Maaa 180,12,140
Mvaa 244,12,136
s_waitcnt vmcnt(9) lgkmcnt(0)
s_barrier
Mvaa 244,8,40
s_add_u32 s65, s5, 4
Mvaa 212,8,36
Mvaa 212,12,132
s_cmp_ge_u32 s65, 20
s_cselect_b32 s90, 20, 0
Mvaa 180,12,128
Mvaa 180,8,32
s_sub_u32 s65, s65, s90
Mvaa 184,16,32
Mvaa 184,20,128
s_lshl_b32 s65, s65, 18
Mvaa 216,20,132
Ta 0,10,32768
Ta 2,10,33792
Mvaa 216,16,36
Mvaa 248,16,40
s_add_u32 m0, s63, 0
Mvaa 248,20,136
Maaa 184,20,140
D 16,48,65,0
Maaa 184,16,44
Maaa 216,16,48
Maaa 216,20,144
Maaa 248,20,148
D 17,48,65,1024
Ta 4,11,32768
Maaa 248,16,52
Ta 6,11,33792
Maaa 88,16,56
Maaa 88,20,152
Maaa 120,20,156
D 18,48,65,2048
Maaa 120,16,60
Maaa 124,24,60
Ra 60,26,47104
Maaa 124,28,156
Maaa 92,28,152
D 19,48,65,3072
Ta 8,12,32768
Maaa 92,24,56
s_add_u32 m0, s63, 4096
Ta 10,12,33792
Ra 56,26,45056
Maaa 252,24,52
Ra 52,26,43008
Maaa 252,28,148
Maaa 220,28,144
D 20,48,65,0
Maaa 220,24,48
Ra 48,26,40960
Maaa 188,24,44
Ra 44,26,38912
Maaa 188,28,140
Mvaa 252,28,136
D 21,48,65,1024
Ta 12,13,32768
Mvaa 252,24,40
Ta 14,13,33792
Ra 40,26,36864
Mvaa 220,24,36
Ra 36,26,34816
Mvaa 220,28,132
Mvaa 188,28,128
D 22,48,65,2048
Mvaa 188,24,32
Ra 32,26,32768
W 6
Maaa 96,0,60
Maaa 64,0,56
Maaa 224,0,52
D 23,48,65,3072
Ta 16,14,32768
Maaa 192,0,48
W 1
s_add_u32 s65, s5, 8
s_cmp_ge_u32 s65, 20
s_cselect_b32 s90, 20, 0
Maaa 160,0,44
s_sub_u32 s65, s65, s90
s_lshl_b32 s65, s65, 18
Ta 18,14,33792
Mvaa 224,0,40
s_add_u32 m0, s63, 65536
Mvaa 192,0,36
D 16,48,65,0
Mvaa 160,0,32
Mvaa 164,4,32
Mvaa 196,4,36
Mvaa 228,4,40
D 17,48,65,1024
Ta 20,15,32768
Maaa 164,4,44
Ta 22,15,33792
Maaa 196,4,48
Maaa 228,4,52
Maaa 68,4,56
D 18,48,65,2048
Maaa 100,4,60
Maaa 104,8,60
Maaa 72,8,56
Maaa 232,8,52
D 19,48,65,3072
Ta 24,24,32768
Maaa 200,8,48
s_add_u32 m0, s63, 69632
Ta 26,24,33792
Maaa 168,8,44
Mvaa 232,8,40
Mvaa 200,8,36
D 20,48,65,0
Mvaa 168,8,32
Mvaa 172,12,32
Mvaa 204,12,36
Mvaa 236,12,40
D 21,48,65,1024
Ta 28,25,32768
Maaa 172,12,44
Ta 30,25,33792
Maaa 204,12,48
Ra 156,27,47104
Maaa 236,12,52
Maaa 76,12,56
D 22,48,65,2048
Maaa 108,12,60
W 7
Ra 152,27,45056
Maaa 112,16,60
Maaa 80,16,56
Maaa 240,16,52
D 23,48,65,3072
Ra 148,27,43008
Maaa 208,16,48
Ta 0,10,40960
Ta 2,10,41984
Maaa 176,16,44
Mvaa 240,16,40
Ra 144,27,40960
Mvaa 208,16,36
buffer_load_dword v142, v144, s[8:11], s91 offen offset:0 sc1
Mvaa 176,16,32
W 10
Mvaa 180,20,32
Ra 140,27,38912
Mvaa 212,20,36
Mvaa 244,20,40
Ta 4,11,40960
Ta 6,11,41984
Maaa 180,20,44
Ra 136,27,36864
Maaa 212,20,48
Maaa 244,20,52
v_bfe_u32 v29, v1, 7, 1
Maaa 84,20,56
Ra 132,27,34816
Maaa 116,20,60
W 13
v_mul_u32_u24 v28, 10240, v29
Maaa 120,24,60
Maaa 88,24,56
Ra 128,27,32768
v_lshrrev_b32 v29, 8, v1
Maaa 248,24,52
Ta 8,12,40960
Ta 10,12,41984
Maaa 216,24,48
v_lshl_add_u32 v28, v29, 7, v28
Maaa 184,24,44
v_and_b32 v29, 0x70, v1
Mvaa 248,24,40
Mvaa 216,24,36
v_add_u32 v28, v28, v29
Mvaa 184,24,32
W 14
Mvaa 188,28,32
s_and_b32 s80, s14, 2
Mvaa 220,28,36
s_mul_i32 s80, s80, 20480
Mvaa 252,28,40
Ta 12,13,40960
Ta 14,13,41984
Maaa 188,28,44
s_sub_u32 s81, 40960, s80
Maaa 220,28,48
Maaa 252,28,52
s_add_u32 s93, s28, 20480
Maaa 92,28,56
Maaa 124,28,60
W 10
v_add_u32 v26, s80, v28
)"
R"(Maaa 96,0,156
Maaa 64,0,152
Maaa 224,0,148
Ta 16,14,40960
v_add_u32 v27, s81, v28
Maaa 192,0,144
W 5
Ta 18,14,41984
Maaa 160,0,140
buffer_load_dwordx4 a[32:35], v26, s[8:11], s28 offen offset:0 sc1
Mvaa 224,0,136
Mvaa 192,0,132
Mvaa 160,0,128
Mvaa 164,4,128
buffer_load_dwordx4 a[36:39], v26, s[8:11], s93 offen offset:0 sc1
Mvaa 196,4,132
Mvaa 228,4,136
Ta 20,15,40960
Ta 22,15,41984
Maaa 164,4,140
Maaa 196,4,144
buffer_load_dwordx4 a[40:43], v27, s[8:11], s28 offen offset:0 sc1
Maaa 228,4,148
Maaa 68,4,152
Maaa 100,4,156
W 6
Maaa 104,8,156
buffer_load_dwordx4 a[44:47], v27, s[8:11], s93 offen offset:0 sc1
Ta 24,24,40960
Maaa 72,8,152
s_add_u32 s28, s28, 512
s_cmp_eq_u32 s28, s4
s_cselect_b32 s93, 10240, 0
Ta 26,24,41984
Maaa 232,8,148
s_sub_u32 s28, s28, s93
Ta 28,25,40960
Ta 30,25,41984
Maaa 200,8,144
s_add_u32 s93, s28, 20480
Maaa 168,8,140
Mvaa 232,8,136
Mvaa 200,8,132
s_add_u32 s65, s5, 12
s_cmp_ge_u32 s65, 20
s_cselect_b32 s90, 20, 0
buffer_load_dwordx4 a[48:51], v26, s[8:11], s28 offen offset:0 sc1
Mvaa 168,8,128
W 8
s_sub_u32 s65, s65, s90
s_lshl_b32 s65, s65, 18
Mvaa 172,12,128
s_add_u32 m0, s63, 32768
Mvaa 204,12,132
Mvaa 236,12,136
s_waitcnt vmcnt(6) lgkmcnt(0)
s_barrier
Maaa 172,12,140
buffer_load_dwordx4 a[52:55], v26, s[8:11], s93 offen offset:0 sc1
Maaa 204,12,144
Maaa 236,12,148
Maaa 76,12,152
Maaa 108,12,156
buffer_load_dwordx4 a[56:59], v27, s[8:11], s28 offen offset:0 sc1
Maaa 112,16,156
Maaa 80,16,152
Maaa 240,16,148
Maaa 208,16,144
buffer_load_dwordx4 a[60:63], v27, s[8:11], s93 offen offset:0 sc1
Maaa 176,16,140
Mvaa 240,16,136
Mvaa 208,16,132
Mvaa 176,16,128
D 16,48,65,0
Mvaa 180,20,128
Mvaa 212,20,132
Mvaa 244,20,136
Ta 0,2,0
Ta 2,2,1024
Maaa 180,20,140
D 17,48,65,1024
Maaa 212,20,144
Maaa 244,20,148
Maaa 84,20,152
Ta 4,2,8192
Ta 6,2,9216
Maaa 116,20,156
D 18,48,65,2048
Maaa 120,24,156
Maaa 88,24,152
Maaa 248,24,148
Ta 8,3,0
Ta 10,3,1024
Maaa 216,24,144
D 19,48,65,3072
Maaa 184,24,140
s_add_u32 m0, s63, 36864
Mvaa 248,24,136
Mvaa 216,24,132
Ta 12,3,8192
Ta 14,3,9216
Mvaa 184,24,128
D 20,48,65,0
Mvaa 188,28,128
Mvaa 220,28,132
Mvaa 252,28,136
Ta 16,4,0
Ta 18,4,1024
Maaa 188,28,140
Maaa 220,28,144
D 21,48,65,1024
Maaa 252,28,148
Maaa 92,28,152
Maaa 124,28,156
s_branch .Lq_7%=
.p2align 12
.Lv_6%=:
s_waitcnt vmcnt(14)
s_barrier
Ta 0,10,16384
Ta 2,10,17408
Ra 32,26,0
Ta 4,10,24576
.Lh_0_0_1%=:
s_cbranch_execz .Lhr_5%=
Ta 6,10,25600
Ra 128,27,0
Ra 132,27,2048
Ra 36,26,2048
Ra 40,26,4096
Ra 136,27,4096
Ra 140,27,6144
Ra 44,26,6144
Ra 48,26,8192
Ra 144,27,8192
W 6
Zvaa 160,32,0
s_add_u32 m0, s61, 32768
Ra 148,27,10240
Ra 52,26,10240
Ra 56,26,12288
Ra 152,27,12288
Ra 156,27,14336
Ra 60,26,14336
Mvaa 160,128,4
Ta 8,11,16384
Ta 10,11,17408
Ta 12,11,24576
Ta 14,11,25600
Zvaa 164,132,4
Ta 16,12,16384
Ta 18,12,17408
Mvaa 164,36,0
W 14
Ta 20,12,24576
Ta 22,12,25600
Zvaa 224,40,0
D 2,40,64,0
Mvaa 224,136,4
Zvaa 228,140,4
Mvaa 228,44,0
W 12
Zaaa 192,48,0
D 3,40,64,1024
Maaa 192,144,4
Zaaa 196,148,4
Maaa 196,52,0
W 8
Zaaa 64,56,0
D 4,40,64,2048
Maaa 64,152,4
Zaaa 68,156,4
Maaa 68,60,0
W 4
Zaaa 76,60,8
D 5,40,64,3072
Maaa 76,156,12
s_add_u32 m0, s61, 36864
Zaaa 72,152,12
Ta 24,13,16384
Ta 26,13,17408
Maaa 72,56,8
Ta 28,13,24576
Ta 30,13,25600
Zaaa 204,52,8
s_waitcnt vmcnt(15)
D 6,40,64,0
Maaa 204,148,12
v_lshlrev_b32 v56, 16, v32
v_and_b32 v57, 0xffff0000, v32
v_lshlrev_b32 v58, 16, v33
v_and_b32 v59, 0xffff0000, v33
v_lshlrev_b32 v60, 16, v34
Zaaa 200,144,12
v_and_b32 v61, 0xffff0000, v34
v_lshlrev_b32 v62, 16, v35
v_and_b32 v63, 0xffff0000, v35
v_lshlrev_b32 v64, 16, v36
v_and_b32 v65, 0xffff0000, v36
Maaa 200,48,8
v_lshlrev_b32 v66, 16, v37
v_and_b32 v67, 0xffff0000, v37
v_lshlrev_b32 v68, 16, v38
v_and_b32 v69, 0xffff0000, v38
v_lshlrev_b32 v70, 16, v39
Zvaa 236,44,8
D 7,40,64,1024
v_and_b32 v71, 0xffff0000, v39
v_lshlrev_b32 v72, 16, v40
v_and_b32 v73, 0xffff0000, v40
v_lshlrev_b32 v74, 16, v41
v_and_b32 v75, 0xffff0000, v41
Mvaa 236,140,12
v_lshlrev_b32 v76, 16, v42
v_and_b32 v77, 0xffff0000, v42
v_lshlrev_b32 v78, 16, v43
v_and_b32 v79, 0xffff0000, v43
A 80,56,57
Zvaa 232,136,12
A 81,58,59
A 80,80,60
A 81,81,61
A 80,80,62
A 81,81,63
Mvaa 232,40,8
A 80,80,64
A 81,81,65
A 80,80,66
A 81,81,67
A 80,80,68
Zvaa 172,36,8
D 8,40,64,2048
A 81,81,69
A 80,80,70
A 81,81,71
A 80,80,72
A 81,81,73
Mvaa 172,132,12
A 80,80,74
A 81,81,75
A 80,80,76
A 81,81,77
A 80,80,78
A 81,81,79
Zvaa 168,128,12
A 80,80,81
s_nop 1
v_add_f32_dpp v80, v80, v80 quad_perm:[1,0,3,2] row_mask:0xf bank_mask:0xf
s_nop 1
v_add_f32_dpp v80, v80, v80 quad_perm:[2,3,0,1] row_mask:0xf bank_mask:0xf
s_nop 1
v_add_f32_dpp v80, v80, v80 row_half_mirror row_mask:0xf bank_mask:0xf
s_nop 1
v_add_f32_dpp v80, v80, v80 row_mirror row_mask:0xf bank_mask:0xf
Mvaa 168,32,8
W 4
v_mov_b32 v81, v80
s_nop 1
v_permlane16_swap_b32 v80, v81
A 80,80,81
v_mul_f32 v84, 0x3acccccd, v80
s_mov_b32 s86, 0xffff
Zvaa 176,32,16
s_mov_b32 s87, 0xffff
D 9,40,64,3072
v_sub_f32 v82, v56, v84
U 80,82,82
v_sub_f32 v83, v57, v84
v_fmac_f32 v80, v83, v83
Mvaa 176,128,20
v_sub_f32 v82, v58, v84
v_fmac_f32 v80, v82, v82
v_sub_f32 v83, v59, v84
v_fmac_f32 v80, v83, v83
v_sub_f32 v82, v60, v84
Zvaa 180,132,20
Ta 0,14,16384
v_fmac_f32 v80, v82, v82
v_sub_f32 v83, v61, v84
v_fmac_f32 v80, v83, v83
v_sub_f32 v82, v62, v84
v_fmac_f32 v80, v82, v82
Mvaa 180,36,16
Ta 2,14,17408
v_sub_f32 v83, v63, v84
v_fmac_f32 v80, v83, v83
v_sub_f32 v82, v64, v84
v_fmac_f32 v80, v82, v82
v_sub_f32 v83, v65, v84
Zvaa 240,40,16
global_load_dword v142, v[140:141], off
v_lshl_add_u64 v[140:141], v[140:141], 0, s[68:69]
Ta 4,14,24576
Ta 6,14,25600
v_fmac_f32 v80, v83, v83
v_sub_f32 v82, v66, v84
v_fmac_f32 v80, v82, v82
Mvaa 240,136,20
v_sub_f32 v83, v67, v84
v_fmac_f32 v80, v83, v83
v_sub_f32 v82, v68, v84
v_fmac_f32 v80, v82, v82
v_sub_f32 v83, v69, v84
v_fmac_f32 v80, v83, v83
Zvaa 244,140,20
v_sub_f32 v82, v70, v84
v_fmac_f32 v80, v82, v82
v_sub_f32 v83, v71, v84
v_fmac_f32 v80, v83, v83
v_sub_f32 v82, v72, v84
U 81,82,82
Mvaa 244,44,16
v_sub_f32 v83, v73, v84
v_fmac_f32 v81, v83, v83
v_sub_f32 v82, v74, v84
v_fmac_f32 v81, v82, v82
v_sub_f32 v83, v75, v84
v_fmac_f32 v81, v83, v83
Zaaa 208,48,16
v_sub_f32 v82, v76, v84
v_fmac_f32 v81, v82, v82
v_sub_f32 v83, v77, v84
v_fmac_f32 v81, v83, v83
v_sub_f32 v82, v78, v84
Maaa 208,144,20
v_fmac_f32 v81, v82, v82
v_sub_f32 v83, v79, v84
v_fmac_f32 v81, v83, v83
v_cndmask_b32_e64 v81, 0, v81, s[86:87]
A 80,80,81
Zaaa 212,148,20
Maaa 212,52,16
v_add_f32_dpp v80, v80, v80 quad_perm:[1,0,3,2] row_mask:0xf bank_mask:0xf
s_nop 1
v_add_f32_dpp v80, v80, v80 quad_perm:[2,3,0,1] row_mask:0xf bank_mask:0xf
s_nop 1
v_add_f32_dpp v80, v80, v80 row_half_mirror row_mask:0xf bank_mask:0xf
s_nop 1
v_add_f32_dpp v80, v80, v80 row_mirror row_mask:0xf bank_mask:0xf
v_mov_b32 v81, v80
s_nop 1
v_permlane16_swap_b32 v80, v81
Zaaa 80,56,16
A 80,80,81
v_mul_f32 v80, 0x3acccccd, v80
K 80,80,80
v_lshlrev_b32 v80, 16, v80
v_add_f32 v80, 0x3727c5ac, v80
K 80,80,80
Maaa 80,152,20
v_lshlrev_b32 v80, 16, v80
v_sqrt_f32 v81, v80
s_nop 0
v_rcp_f32 v82, v81
v_fma_f32 v83, -v81, v81, v80
Zaaa 84,156,20
v_mul_f32 v82, 0.5, v82
v_fma_f32 v81, v83, v82, v81
K 86,81,81
v_lshlrev_b32 v86, 16, v86
v_rcp_f32 v87, v86
Maaa 84,60,16
W 4
K 85,84,84
v_lshlrev_b32 v85, 16, v85
s_waitcnt vmcnt(17)
v_sub_f32 v56, v56, v85
v_sub_f32 v57, v57, v85
K 80,56,57
Zaaa 92,60,24
v_lshlrev_b32 v56, 16, v80
v_and_b32 v57, 0xffff0000, v80
U 80,56,87
U 81,57,87
K 32,80,81
v_lshlrev_b32 v80, 16, v32
Maaa 92,156,28
v_and_b32 v81, 0xffff0000, v32
v_lshlrev_b32 v82, 16, v44
v_and_b32 v83, 0xffff0000, v44
U 84,80,82
v_fmac_f32 v84, v81, v83
Zaaa 88,152,28
Ta 8,15,16384
v_sub_f32 v58, v58, v85
v_sub_f32 v59, v59, v85
K 80,58,59
v_lshlrev_b32 v58, 16, v80
v_and_b32 v59, 0xffff0000, v80
Maaa 88,56,24
Ta 10,15,17408
U 80,58,87
U 81,59,87
K 33,80,81
v_lshlrev_b32 v80, 16, v33
v_and_b32 v81, 0xffff0000, v33
Zaaa 220,52,24
Ta 12,15,24576
Ta 14,15,25600
v_lshlrev_b32 v82, 16, v45
)"
R"(v_and_b32 v83, 0xffff0000, v45
v_fmac_f32 v84, v80, v82
v_fmac_f32 v84, v81, v83
Maaa 220,148,28
v_sub_f32 v60, v60, v85
v_sub_f32 v61, v61, v85
K 80,60,61
v_lshlrev_b32 v60, 16, v80
v_and_b32 v61, 0xffff0000, v80
U 80,60,87
Zaaa 216,144,28
U 81,61,87
K 34,80,81
v_lshlrev_b32 v80, 16, v34
v_and_b32 v81, 0xffff0000, v34
v_lshlrev_b32 v82, 16, v46
v_and_b32 v83, 0xffff0000, v46
Maaa 216,48,24
v_fmac_f32 v84, v80, v82
v_fmac_f32 v84, v81, v83
v_sub_f32 v62, v62, v85
v_sub_f32 v63, v63, v85
K 80,62,63
Zvaa 252,44,24
v_lshlrev_b32 v62, 16, v80
v_and_b32 v63, 0xffff0000, v80
U 80,62,87
U 81,63,87
K 35,80,81
Mvaa 252,140,28
v_lshlrev_b32 v80, 16, v35
v_and_b32 v81, 0xffff0000, v35
v_lshlrev_b32 v82, 16, v47
v_and_b32 v83, 0xffff0000, v47
v_fmac_f32 v84, v80, v82
Zvaa 248,136,28
v_fmac_f32 v84, v81, v83
v_sub_f32 v64, v64, v85
v_sub_f32 v65, v65, v85
K 80,64,65
v_lshlrev_b32 v64, 16, v80
Mvaa 248,40,24
v_and_b32 v65, 0xffff0000, v80
U 80,64,87
U 81,65,87
K 36,80,81
v_lshlrev_b32 v80, 16, v36
Zvaa 188,36,24
v_and_b32 v81, 0xffff0000, v36
v_lshlrev_b32 v82, 16, v48
v_and_b32 v83, 0xffff0000, v48
v_fmac_f32 v84, v80, v82
v_fmac_f32 v84, v81, v83
Mvaa 188,132,28
v_sub_f32 v66, v66, v85
v_sub_f32 v67, v67, v85
K 80,66,67
v_lshlrev_b32 v66, 16, v80
v_and_b32 v67, 0xffff0000, v80
U 80,66,87
Zvaa 184,128,28
U 81,67,87
K 37,80,81
v_lshlrev_b32 v80, 16, v37
v_and_b32 v81, 0xffff0000, v37
v_lshlrev_b32 v82, 16, v49
Mvaa 184,32,24
W 4
v_and_b32 v83, 0xffff0000, v49
v_fmac_f32 v84, v80, v82
v_fmac_f32 v84, v81, v83
v_sub_f32 v68, v68, v85
v_sub_f32 v69, v69, v85
Zvaa 192,32,0
K 80,68,69
v_lshlrev_b32 v68, 16, v80
v_and_b32 v69, 0xffff0000, v80
U 80,68,87
U 81,69,87
Mvaa 192,128,4
K 38,80,81
v_lshlrev_b32 v80, 16, v38
v_and_b32 v81, 0xffff0000, v38
v_lshlrev_b32 v82, 16, v50
v_and_b32 v83, 0xffff0000, v50
Zvaa 196,132,4
Ta 16,24,16384
v_fmac_f32 v84, v80, v82
v_fmac_f32 v84, v81, v83
v_sub_f32 v70, v70, v85
v_sub_f32 v71, v71, v85
K 80,70,71
Mvaa 196,36,0
Ta 18,24,17408
v_lshlrev_b32 v70, 16, v80
v_and_b32 v71, 0xffff0000, v80
U 80,70,87
U 81,71,87
K 39,80,81
Zaaa 160,40,0
Ta 20,24,24576
Ta 22,24,25600
v_lshlrev_b32 v80, 16, v39
v_and_b32 v81, 0xffff0000, v39
v_lshlrev_b32 v82, 16, v51
v_and_b32 v83, 0xffff0000, v51
Maaa 160,136,4
v_fmac_f32 v84, v80, v82
v_fmac_f32 v84, v81, v83
v_sub_f32 v72, v72, v85
v_sub_f32 v73, v73, v85
K 80,72,73
v_lshlrev_b32 v72, 16, v80
Zaaa 164,140,4
v_and_b32 v73, 0xffff0000, v80
U 80,72,87
U 81,73,87
K 40,80,81
v_lshlrev_b32 v80, 16, v40
v_and_b32 v81, 0xffff0000, v40
Maaa 164,44,0
v_lshlrev_b32 v82, 16, v52
v_and_b32 v83, 0xffff0000, v52
v_fmac_f32 v84, v80, v82
v_fmac_f32 v84, v81, v83
v_sub_f32 v74, v74, v85
Zaaa 224,48,0
v_sub_f32 v75, v75, v85
K 80,74,75
v_lshlrev_b32 v74, 16, v80
v_and_b32 v75, 0xffff0000, v80
U 80,74,87
Maaa 224,144,4
U 81,75,87
K 41,80,81
v_lshlrev_b32 v80, 16, v41
v_and_b32 v81, 0xffff0000, v41
v_lshlrev_b32 v82, 16, v53
Zaaa 228,148,4
v_and_b32 v83, 0xffff0000, v53
v_fmac_f32 v84, v80, v82
v_fmac_f32 v84, v81, v83
v_sub_f32 v76, v76, v85
v_sub_f32 v77, v77, v85
Maaa 228,52,0
K 80,76,77
v_lshlrev_b32 v76, 16, v80
v_and_b32 v77, 0xffff0000, v80
U 80,76,87
U 81,77,87
Zaaa 96,56,0
K 42,80,81
v_lshlrev_b32 v80, 16, v42
v_and_b32 v81, 0xffff0000, v42
v_lshlrev_b32 v82, 16, v54
v_and_b32 v83, 0xffff0000, v54
Maaa 96,152,4
v_fmac_f32 v84, v80, v82
v_fmac_f32 v84, v81, v83
v_sub_f32 v78, v78, v85
v_sub_f32 v79, v79, v85
K 80,78,79
v_lshlrev_b32 v78, 16, v80
Zaaa 100,156,4
v_and_b32 v79, 0xffff0000, v80
U 80,78,87
U 81,79,87
K 43,80,81
v_lshlrev_b32 v80, 16, v43
Maaa 100,60,0
W 4
v_and_b32 v81, 0xffff0000, v43
v_lshlrev_b32 v82, 16, v55
v_and_b32 v83, 0xffff0000, v55
v_fmac_f32 v84, v80, v82
v_fmac_f32 v84, v81, v83
Zaaa 108,60,8
Ta 24,25,16384
Ta 26,25,17408
v_lshrrev_b32 v82, 9, v1
s_lshl_b32 s80, s14, 1
v_add_u32 v82, s80, v82
v_mul_u32_u24 v83, 1552, v82
v_add_u32 v83, v83, v88
Maaa 108,156,12
v_add_u32 v83, 131072, v83
ds_write_b128 v83, v[32:35] offset:0
ds_write_b128 v83, v[36:39] offset:512
ds_write_b128 v83, v[40:43] offset:1024
v_add_f32_dpp v84, v84, v84 quad_perm:[1,0,3,2] row_mask:0xf bank_mask:0xf
Zaaa 104,152,12
Ta 28,25,24576
Ta 30,25,25600
Maaa 104,56,8
v_add_f32_dpp v84, v84, v84 quad_perm:[2,3,0,1] row_mask:0xf bank_mask:0xf
s_nop 1
v_add_f32_dpp v84, v84, v84 row_half_mirror row_mask:0xf bank_mask:0xf
s_nop 1
v_add_f32_dpp v84, v84, v84 row_mirror row_mask:0xf bank_mask:0xf
v_mov_b32 v81, v84
s_nop 1
v_permlane16_swap_b32 v84, v81
A 84,84,81
Zaaa 236,52,8
K 80,84,84
v_lshlrev_b32 v80, 16, v80
s_lshl_b32 s80, s15, 3
v_add_u32 v82, s80, v82
v_sub_u32 v83, 2047, v82
v_ashrrev_i32 v81, 31, v80
v_or_b32 v81, 0x80000000, v81
Maaa 236,148,12
v_xor_b32 v80, v80, v81
v_and_b32 v80, 0xffff0000, v80
v_or_b32 v80, v80, v83
v_and_b32 v82, 7, v82
v_lshlrev_b32 v82, 2, v82
v_add_u32 v82, 143488, v82
Zaaa 232,144,12
ds_write_b32 v82, v80
Maaa 232,48,8
Zaaa 172,44,8
Maaa 172,140,12
Zaaa 168,136,12
s_waitcnt vmcnt(1) lgkmcnt(0)
s_barrier
Maaa 168,40,8
s_add_u32 s64, s64, 128
v_bfe_u32 v80, v1, 4, 3
Zvaa 204,36,8
v_mul_u32_u24 v81, 1552, v80
Mvaa 204,132,12
s_and_b32 s64, s64, 1023
v_lshrrev_b32 v82, 7, v1
Zvaa 200,128,12
v_lshl_add_u32 v81, v82, 4, v81
Mvaa 200,32,8
s_lshl_b32 s80, s14, 7
Zvaa 208,32,16
s_lshl_b32 s65, s64, 12
s_add_u32 s80, s80, 131072
Mvaa 208,128,20
v_add_u32 v81, s80, v81
Zvaa 212,132,20
s_add_u32 m0, s62, 0
ds_read_b128 v[32:35], v81
Ta 0,10,49152
Mvaa 212,36,16
Ta 2,10,50176
Rv 36,81,512
Zaaa 176,40,16
s_and_b32 s80, s14, 2
Ta 4,10,57344
Ta 6,10,58368
Maaa 176,136,20
s_lshl_b32 s80, s80, 7
D 16,44,65,0
Zaaa 180,140,20
v_subrev_u32 v81, s80, v81
Maaa 180,44,16
Rv 40,81,1024
Zaaa 240,48,16
v_lshlrev_b32 v80, 2, v80
Maaa 240,144,20
D 17,44,65,1024
v_add_u32 v80, 143488, v80
Zaaa 244,148,20
ds_read_b32 v84, v80
Maaa 244,52,16
v_mov_b32 v85, s13
Zaaa 112,56,16
v_lshrrev_b32 v83, 1, v1
Maaa 112,152,20
s_mul_i32 s82, s15, 10240
D 18,44,65,2048
Zaaa 116,156,20
s_add_u32 s82, s82, 131072
Maaa 116,60,16
s_lshl_b32 s80, s14, 10
Zaaa 124,60,24
s_add_u32 s83, s82, s80
Ra 60,26,47104
Maaa 124,156,28
D 19,44,65,3072
Ra 156,27,47104
Zaaa 120,152,28
s_add_u32 m0, s62, 4096
Ra 152,27,45056
Ta 8,11,49152
Maaa 120,56,24
Ta 10,11,50176
Ra 56,26,45056
Zaaa 252,52,24
Ta 12,11,57344
Ta 14,11,58368
Maaa 252,148,28
W 8
buffer_store_dwordx4 v[32:35], v1, s[8:11], s83 offen sc1
s_add_u32 s83, s83, 4096
Zaaa 248,144,28
Ra 52,26,43008
Ra 148,27,43008
Maaa 248,48,24
Ra 144,27,40960
Ra 48,26,40960
Zaaa 188,44,24
Ra 44,26,38912
Maaa 188,140,28
D 20,44,65,0
Ra 140,27,38912
Zaaa 184,136,28
Ra 136,27,36864
Maaa 184,40,24
Ra 40,26,36864
Zvaa 220,36,24
Ra 36,26,34816
Mvaa 220,132,28
buffer_store_dwordx4 v[36:39], v1, s[8:11], s83 offen sc1
Ra 132,27,34816
Zvaa 216,128,28
s_and_b32 s80, s80, 1024
s_add_u32 s83, s82, s80
s_add_u32 s83, s83, 8192
Mvaa 216,32,24
W 12
Ra 128,27,32768
Ra 32,26,32768
Maaa 68,60,0
Maaa 68,156,4
D 21,44,65,1024
Maaa 64,152,4
Ta 16,12,49152
Ta 18,12,50176
Maaa 64,56,0
W 10
Ta 20,12,57344
Ta 22,12,58368
Maaa 196,52,0
Maaa 196,148,4
D 22,44,65,2048
Maaa 192,144,4
Maaa 192,48,0
W 8
Mvaa 228,44,0
Mvaa 228,140,4
buffer_store_dwordx4 v[40:43], v1, s[8:11], s83 offen sc1
Mvaa 224,136,4
Mvaa 224,40,0
W 4
Mvaa 164,36,0
Mvaa 164,132,4
D 23,44,65,3072
Mvaa 160,128,4
s_add_u32 m0, s61, 0
Mvaa 160,32,0
Mvaa 168,32,8
Mvaa 168,128,12
D 2,40,64,0
Mvaa 172,132,12
Ta 24,13,49152
Ta 26,13,50176
Mvaa 172,36,8
Ta 28,13,57344
Ta 30,13,58368
Mvaa 232,40,8
.Lh_0_1_1%=:
s_cbranch_execz .Lhr_5%=
Mvaa 232,136,12
D 3,40,64,1024
Mvaa 236,140,12
Mvaa 236,44,8
Maaa 200,48,8
Maaa 200,144,12
D 4,40,64,2048
Maaa 204,148,12
Maaa 204,52,8
Maaa 72,56,8
Maaa 72,152,12
D 5,40,64,3072
Maaa 76,156,12
s_add_u32 m0, s61, 4096
Maaa 76,60,8
W 4
Maaa 84,60,16
Maaa 84,156,20
D 6,40,64,0
Maaa 80,152,20
Ta 0,14,49152
Ta 2,14,50176
Maaa 80,56,16
Ta 4,14,57344
Ta 6,14,58368
Maaa 212,52,16
Maaa 212,148,20
D 7,40,64,1024
Maaa 208,144,20
Maaa 208,48,16
Mvaa 244,44,16
Mvaa 244,140,20
D 8,40,64,2048
)"
R"(Mvaa 240,136,20
Mvaa 240,40,16
Mvaa 180,36,16
Mvaa 180,132,20
D 9,40,64,3072
Mvaa 176,128,20
Mvaa 176,32,16
W 4
Mvaa 184,32,24
Mvaa 184,128,28
global_load_dword v142, v[140:141], off
v_lshl_add_u64 v[140:141], v[140:141], 0, s[68:69]
v_bfe_u32 v147, v1, 5, 3
Mvaa 188,132,28
v_lshlrev_b32 v147, 13, v147
v_and_b32 v148, 16, v1
Mvaa 188,36,24
Ta 8,15,49152
v_lshl_add_u32 v147, v148, 3, v147
Mvaa 248,40,24
s_lshl_b32 s92, s57, 2
s_add_u32 s92, s92, s14
s_lshl_b32 s92, s92, 16
Mvaa 248,136,28
buffer_load_dword v142, v147, s[48:51], s92 offen
Ta 10,15,50176
Mvaa 252,140,28
Ta 12,15,57344
Ta 14,15,58368
Mvaa 252,44,24
Maaa 216,48,24
Maaa 216,144,28
Maaa 220,148,28
Maaa 220,52,24
Maaa 88,56,24
Maaa 88,152,28
Maaa 92,156,28
Maaa 92,60,24
W 4
Maaa 100,60,0
Maaa 100,156,4
Maaa 96,152,4
Ta 16,24,49152
Ta 18,24,50176
Maaa 96,56,0
Ta 20,24,57344
Ta 22,24,58368
Maaa 228,52,0
Maaa 228,148,4
Maaa 224,144,4
Maaa 224,48,0
Maaa 164,44,0
Maaa 164,140,4
Maaa 160,136,4
Maaa 160,40,0
Mvaa 196,36,0
Mvaa 196,132,4
Mvaa 192,128,4
Mvaa 192,32,0
W 4
Mvaa 200,32,8
Ta 24,25,49152
Ta 26,25,50176
Mvaa 200,128,12
Mvaa 204,132,12
Ta 28,25,57344
Ta 30,25,58368
Mvaa 204,36,8
Maaa 168,40,8
Maaa 168,136,12
Maaa 172,140,12
Maaa 172,44,8
Maaa 232,48,8
Maaa 232,144,12
Maaa 236,148,12
s_waitcnt vmcnt(2) lgkmcnt(0)
s_barrier
s_cmp_eq_u32 s14, 0
s_cbranch_scc0 .Lpubr_15%=
s_lshl_b32 s80, s15, 6
s_add_u32 s80, s80, 40960
s_mov_b64 exec, 0xff
buffer_store_dwordx2 v[84:85], v83, s[8:11], s80 offen sc1
s_mov_b64 exec, -1
.Lpubr_15%=:
Maaa 236,52,8
s_add_u32 s64, s64, 128
Maaa 104,56,8
Maaa 104,152,12
s_and_b32 s64, s64, 1023
Maaa 108,156,12
Maaa 108,60,8
Maaa 116,60,16
s_lshl_b32 s65, s64, 12
Maaa 116,156,20
Maaa 112,152,20
Ta 0,10,16384
Ta 2,10,17408
Maaa 112,56,16
s_add_u32 m0, s62, 32768
Ta 4,10,24576
Ta 6,10,25600
Maaa 244,52,16
Maaa 244,148,20
Maaa 240,144,20
D 16,44,65,0
Maaa 240,48,16
Maaa 180,44,16
Maaa 180,140,20
Maaa 176,136,20
D 17,44,65,1024
Maaa 176,40,16
Mvaa 212,36,16
Mvaa 212,132,20
Mvaa 208,128,20
D 18,44,65,2048
Mvaa 208,32,16
Mvaa 216,32,24
Ra 32,26,0
Mvaa 216,128,28
Ra 128,27,0
Mvaa 220,132,28
D 19,44,65,3072
Ra 132,27,2048
Mvaa 220,36,24
s_add_u32 m0, s62, 36864
Ta 8,11,16384
Ta 10,11,17408
Maaa 184,40,24
Ra 36,26,2048
Ta 12,11,24576
Maaa 184,136,28
Ta 14,11,25600
Ra 40,26,4096
Maaa 188,140,28
D 20,44,65,0
Ra 136,27,4096
Maaa 188,44,24
Ra 140,27,6144
Ra 44,26,6144
Maaa 248,48,24
Ra 48,26,8192
Maaa 248,144,28
Ra 144,27,8192
Maaa 252,148,28
D 21,44,65,1024
Ra 148,27,10240
Maaa 252,52,24
Ra 52,26,10240
Maaa 120,56,24
Ra 56,26,12288
Maaa 120,152,28
Ra 152,27,12288
Maaa 124,156,28
D 22,44,65,2048
Ra 156,27,14336
Maaa 124,60,24
W 13
Ra 60,26,14336
Mvaa 160,32,0
Mvaa 160,128,4
Mvaa 164,132,4
D 23,44,65,3072
Ta 16,12,16384
Mvaa 164,36,0
W 9
s_add_u32 m0, s61, 32768
Ta 18,12,17408
Mvaa 224,40,0
Ta 20,12,24576
Ta 22,12,25600
Mvaa 224,136,4
Mvaa 228,140,4
D 2,40,64,0
Mvaa 228,44,0
W 8
Maaa 192,48,0
Maaa 192,144,4
Maaa 196,148,4
D 3,40,64,1024
Maaa 196,52,0
W 4
Maaa 64,56,0
Maaa 64,152,4
Maaa 68,156,4
D 4,40,64,2048
Maaa 68,60,0
Maaa 76,60,8
Maaa 76,156,12
Maaa 72,152,12
D 5,40,64,3072
Ta 24,13,16384
Maaa 72,56,8
s_add_u32 m0, s61, 36864
Ta 26,13,17408
Ta 28,13,24576
Maaa 204,52,8
Ta 30,13,25600
Maaa 204,148,12
Maaa 200,144,12
D 6,40,64,0
Maaa 200,48,8
Mvaa 236,44,8
Mvaa 236,140,12
Mvaa 232,136,12
D 7,40,64,1024
Mvaa 232,40,8
Mvaa 172,36,8
Mvaa 172,132,12
Mvaa 168,128,12
D 8,40,64,2048
Mvaa 168,32,8
W 4
Mvaa 176,32,16
Mvaa 176,128,20
Mvaa 180,132,20
D 9,40,64,3072
Ta 0,14,16384
Mvaa 180,36,16
Ta 2,14,17408
Ta 4,14,24576
Mvaa 240,40,16
Ta 6,14,25600
Mvaa 240,136,20
Mvaa 244,140,20
Mvaa 244,44,16
Maaa 208,48,16
Maaa 208,144,20
Maaa 212,148,20
Maaa 212,52,16
Maaa 80,56,16
Maaa 80,152,20
Maaa 84,156,20
Maaa 84,60,16
W 4
Maaa 92,60,24
Maaa 92,156,28
Maaa 88,152,28
Ta 8,15,16384
Ta 10,15,17408
Maaa 88,56,24
Ta 12,15,24576
Ta 14,15,25600
Maaa 220,52,24
Maaa 220,148,28
Maaa 216,144,28
Maaa 216,48,24
Mvaa 252,44,24
Mvaa 252,140,28
Mvaa 248,136,28
Mvaa 248,40,24
Mvaa 188,36,24
Mvaa 188,132,28
Mvaa 184,128,28
Mvaa 184,32,24
W 4
Mvaa 192,32,0
Mvaa 192,128,4
Mvaa 196,132,4
Ta 16,24,16384
Ta 18,24,17408
Mvaa 196,36,0
Ta 20,24,24576
Ta 22,24,25600
Maaa 160,40,0
Maaa 160,136,4
Maaa 164,140,4
Maaa 164,44,0
Maaa 224,48,0
Maaa 224,144,4
Maaa 228,148,4
Maaa 228,52,0
Maaa 96,56,0
Maaa 96,152,4
Maaa 100,156,4
Maaa 100,60,0
W 4
Maaa 108,60,8
Ta 24,25,16384
Ta 26,25,17408
Maaa 108,156,12
Maaa 104,152,12
Ta 28,25,24576
Ta 30,25,25600
Maaa 104,56,8
Maaa 236,52,8
Maaa 236,148,12
Maaa 232,144,12
Maaa 232,48,8
Maaa 172,44,8
Maaa 172,140,12
Maaa 168,136,12
s_waitcnt vmcnt(0) lgkmcnt(0)
s_barrier
Maaa 168,40,8
s_add_u32 s64, s64, 128
Mvaa 204,36,8
Mvaa 204,132,12
s_and_b32 s64, s64, 1023
Mvaa 200,128,12
Mvaa 200,32,8
Mvaa 208,32,16
s_lshl_b32 s65, s64, 12
Mvaa 208,128,20
Mvaa 212,132,20
s_add_u32 m0, s62, 0
Ta 0,10,49152
Ta 2,10,50176
Mvaa 212,36,16
Ta 4,10,57344
Ta 6,10,58368
Maaa 176,40,16
Maaa 176,136,20
D 16,44,65,0
Maaa 180,140,20
Maaa 180,44,16
Maaa 240,48,16
Maaa 240,144,20
D 17,44,65,1024
Maaa 244,148,20
Maaa 244,52,16
Maaa 112,56,16
Maaa 112,152,20
D 18,44,65,2048
Maaa 116,156,20
Maaa 116,60,16
Maaa 124,60,24
Ra 60,26,47104
Maaa 124,156,28
D 19,44,65,3072
Ra 156,27,47104
Maaa 120,152,28
s_add_u32 m0, s62, 4096
Ra 152,27,45056
Ta 8,11,49152
Maaa 120,56,24
Ta 10,11,50176
Ra 56,26,45056
Maaa 252,52,24
Ta 12,11,57344
Ta 14,11,58368
Maaa 252,148,28
D 20,44,65,0
Ra 52,26,43008
Maaa 248,144,28
Ra 148,27,43008
Ra 144,27,40960
Maaa 248,48,24
Ra 48,26,40960
Maaa 188,44,24
Ra 44,26,38912
Maaa 188,140,28
D 21,44,65,1024
Ra 140,27,38912
Maaa 184,136,28
Ra 136,27,36864
Maaa 184,40,24
Ra 40,26,36864
Mvaa 220,36,24
Ra 36,26,34816
Mvaa 220,132,28
D 22,44,65,2048
Ra 132,27,34816
Mvaa 216,128,28
Ra 128,27,32768
Mvaa 216,32,24
W 13
Ra 32,26,32768
Maaa 68,60,0
Maaa 68,156,4
D 23,44,65,3072
Maaa 64,152,4
s_add_u32 m0, s61, 0
Ta 16,12,49152
Ta 18,12,50176
Maaa 64,56,0
W 10
Ta 20,12,57344
Ta 22,12,58368
Maaa 196,52,0
Maaa 196,148,4
D 2,40,64,0
Maaa 192,144,4
Maaa 192,48,0
W 8
Mvaa 228,44,0
Mvaa 228,140,4
D 3,40,64,1024
Mvaa 224,136,4
Mvaa 224,40,0
W 4
Mvaa 164,36,0
Mvaa 164,132,4
D 4,40,64,2048
Mvaa 160,128,4
Mvaa 160,32,0
Mvaa 168,32,8
Mvaa 168,128,12
D 5,40,64,3072
Mvaa 172,132,12
)"
R"(s_add_u32 m0, s61, 4096
Ta 24,13,49152
Ta 26,13,50176
Mvaa 172,36,8
Ta 28,13,57344
Ta 30,13,58368
Mvaa 232,40,8
Mvaa 232,136,12
D 6,40,64,0
Mvaa 236,140,12
Mvaa 236,44,8
Maaa 200,48,8
Maaa 200,144,12
D 7,40,64,1024
Maaa 204,148,12
Maaa 204,52,8
Maaa 72,56,8
Maaa 72,152,12
D 8,40,64,2048
Maaa 76,156,12
Maaa 76,60,8
W 4
Maaa 84,60,16
Maaa 84,156,20
D 9,40,64,3072
Maaa 80,152,20
Ta 0,14,49152
Ta 2,14,50176
Maaa 80,56,16
Ta 4,14,57344
Ta 6,14,58368
Maaa 212,52,16
Maaa 212,148,20
global_load_dword v142, v[140:141], off
v_lshl_add_u64 v[140:141], v[140:141], 0, s[68:69]
Maaa 208,144,20
Maaa 208,48,16
Mvaa 244,44,16
Mvaa 244,140,20
Mvaa 240,136,20
Mvaa 240,40,16
Mvaa 180,36,16
Mvaa 180,132,20
Mvaa 176,128,20
Mvaa 176,32,16
W 4
Mvaa 184,32,24
Mvaa 184,128,28
Mvaa 188,132,28
Ta 8,15,49152
Ta 10,15,50176
Mvaa 188,36,24
Ta 12,15,57344
Ta 14,15,58368
Mvaa 248,40,24
Mvaa 248,136,28
Mvaa 252,140,28
Mvaa 252,44,24
Maaa 216,48,24
Maaa 216,144,28
Maaa 220,148,28
Maaa 220,52,24
Maaa 88,56,24
Maaa 88,152,28
Maaa 92,156,28
Maaa 92,60,24
W 4
Maaa 100,60,0
Maaa 100,156,4
Maaa 96,152,4
Ta 16,24,49152
Ta 18,24,50176
Maaa 96,56,0
Ta 20,24,57344
Ta 22,24,58368
Maaa 228,52,0
Maaa 228,148,4
Maaa 224,144,4
Maaa 224,48,0
Maaa 164,44,0
Maaa 164,140,4
Maaa 160,136,4
Maaa 160,40,0
Mvaa 196,36,0
Mvaa 196,132,4
Mvaa 192,128,4
Mvaa 192,32,0
W 4
Mvaa 200,32,8
Ta 24,25,49152
Ta 26,25,50176
Mvaa 200,128,12
Mvaa 204,132,12
Ta 28,25,57344
Ta 30,25,58368
Mvaa 204,36,8
Maaa 168,40,8
Maaa 168,136,12
.Lh_0_2_1%=:
s_cbranch_execz .Lhr_5%=
Maaa 172,140,12
Maaa 172,44,8
Maaa 232,48,8
Maaa 232,144,12
Maaa 236,148,12
s_waitcnt vmcnt(1) lgkmcnt(0)
s_barrier
Maaa 236,52,8
s_add_u32 s64, s64, 128
Maaa 104,56,8
Maaa 104,152,12
s_and_b32 s64, s64, 1023
Maaa 108,156,12
Maaa 108,60,8
Maaa 116,60,16
s_lshl_b32 s65, s64, 12
Maaa 116,156,20
Maaa 112,152,20
Ta 0,10,16384
Ta 2,10,17408
Maaa 112,56,16
s_add_u32 m0, s62, 32768
Ta 4,10,24576
Ta 6,10,25600
Maaa 244,52,16
Maaa 244,148,20
Maaa 240,144,20
D 16,44,65,0
Maaa 240,48,16
Maaa 180,44,16
Maaa 180,140,20
Maaa 176,136,20
D 17,44,65,1024
Maaa 176,40,16
Mvaa 212,36,16
Mvaa 212,132,20
Mvaa 208,128,20
D 18,44,65,2048
Mvaa 208,32,16
Mvaa 216,32,24
Ra 32,26,0
Mvaa 216,128,28
Ra 128,27,0
Mvaa 220,132,28
D 19,44,65,3072
Ra 132,27,2048
Mvaa 220,36,24
s_add_u32 m0, s62, 36864
Ta 8,11,16384
Ta 10,11,17408
Maaa 184,40,24
Ra 36,26,2048
Ta 12,11,24576
Maaa 184,136,28
Ta 14,11,25600
Ra 40,26,4096
Maaa 188,140,28
D 20,44,65,0
Ra 136,27,4096
Maaa 188,44,24
Ra 140,27,6144
Ra 44,26,6144
Maaa 248,48,24
Ra 48,26,8192
Maaa 248,144,28
Ra 144,27,8192
Maaa 252,148,28
D 21,44,65,1024
Ra 148,27,10240
Maaa 252,52,24
Ra 52,26,10240
Maaa 120,56,24
Ra 56,26,12288
Maaa 120,152,28
Ra 152,27,12288
Maaa 124,156,28
D 22,44,65,2048
Ra 156,27,14336
Maaa 124,60,24
W 13
Ra 60,26,14336
Mvaa 160,32,0
Mvaa 160,128,4
Mvaa 164,132,4
D 23,44,65,3072
Ta 16,12,16384
Mvaa 164,36,0
W 9
s_add_u32 m0, s61, 32768
Ta 18,12,17408
Mvaa 224,40,0
Ta 20,12,24576
Ta 22,12,25600
Mvaa 224,136,4
Mvaa 228,140,4
D 2,40,64,0
Mvaa 228,44,0
W 8
Maaa 192,48,0
Maaa 192,144,4
Maaa 196,148,4
D 3,40,64,1024
Maaa 196,52,0
W 4
Maaa 64,56,0
Maaa 64,152,4
Maaa 68,156,4
D 4,40,64,2048
Maaa 68,60,0
Maaa 76,60,8
Maaa 76,156,12
Maaa 72,152,12
D 5,40,64,3072
Ta 24,13,16384
Maaa 72,56,8
s_add_u32 m0, s61, 36864
Ta 26,13,17408
Ta 28,13,24576
Maaa 204,52,8
Ta 30,13,25600
Maaa 204,148,12
Maaa 200,144,12
D 6,40,64,0
Maaa 200,48,8
Mvaa 236,44,8
Mvaa 236,140,12
Mvaa 232,136,12
D 7,40,64,1024
Mvaa 232,40,8
Mvaa 172,36,8
Mvaa 172,132,12
Mvaa 168,128,12
D 8,40,64,2048
Mvaa 168,32,8
W 4
Mvaa 176,32,16
Mvaa 176,128,20
Mvaa 180,132,20
D 9,40,64,3072
Ta 0,14,16384
Mvaa 180,36,16
s_add_u32 s92, s92, 2097152
Ta 2,14,17408
Ta 4,14,24576
Mvaa 240,40,16
Ta 6,14,25600
Mvaa 240,136,20
Mvaa 244,140,20
buffer_load_dword v142, v147, s[48:51], s92 offen
Mvaa 244,44,16
s_add_u32 s92, s92, 2097152
Maaa 208,48,16
Maaa 208,144,20
Maaa 212,148,20
buffer_load_dword v142, v147, s[48:51], s92 offen
Maaa 212,52,16
Maaa 80,56,16
Maaa 80,152,20
Maaa 84,156,20
Maaa 84,60,16
W 4
Maaa 92,60,24
Maaa 92,156,28
Maaa 88,152,28
Ta 8,15,16384
Ta 10,15,17408
Maaa 88,56,24
Ta 12,15,24576
Ta 14,15,25600
Maaa 220,52,24
Maaa 220,148,28
Maaa 216,144,28
Maaa 216,48,24
Mvaa 252,44,24
Mvaa 252,140,28
Mvaa 248,136,28
Mvaa 248,40,24
Mvaa 188,36,24
Mvaa 188,132,28
Mvaa 184,128,28
Mvaa 184,32,24
W 4
Mvaa 192,32,0
Mvaa 192,128,4
Mvaa 196,132,4
Ta 16,24,16384
Ta 18,24,17408
Mvaa 196,36,0
Ta 20,24,24576
Ta 22,24,25600
Maaa 160,40,0
Maaa 160,136,4
Maaa 164,140,4
Maaa 164,44,0
Maaa 224,48,0
Maaa 224,144,4
Maaa 228,148,4
Maaa 228,52,0
Maaa 96,56,0
Maaa 96,152,4
Maaa 100,156,4
Maaa 100,60,0
W 4
Maaa 108,60,8
Ta 24,25,16384
Ta 26,25,17408
Maaa 108,156,12
Maaa 104,152,12
Ta 28,25,24576
Ta 30,25,25600
Maaa 104,56,8
Maaa 236,52,8
Maaa 236,148,12
Maaa 232,144,12
Maaa 232,48,8
Maaa 172,44,8
Maaa 172,140,12
Maaa 168,136,12
s_waitcnt vmcnt(2) lgkmcnt(0)
s_barrier
Maaa 168,40,8
s_add_u32 s64, s64, 128
Mvaa 204,36,8
Mvaa 204,132,12
s_and_b32 s64, s64, 1023
Mvaa 200,128,12
Mvaa 200,32,8
Mvaa 208,32,16
s_lshl_b32 s65, s64, 12
Mvaa 208,128,20
Mvaa 212,132,20
Ta 0,10,49152
Ta 2,10,50176
Mvaa 212,36,16
s_add_u32 m0, s62, 0
Ta 4,10,57344
Ta 6,10,58368
Maaa 176,40,16
Maaa 176,136,20
Maaa 180,140,20
D 16,44,65,0
Maaa 180,44,16
Maaa 240,48,16
Maaa 240,144,20
Maaa 244,148,20
D 17,44,65,1024
Maaa 244,52,16
Maaa 112,56,16
Maaa 112,152,20
Maaa 116,156,20
D 18,44,65,2048
Maaa 116,60,16
Maaa 124,60,24
Ra 60,26,47104
Maaa 124,156,28
Ra 156,27,47104
Maaa 120,152,28
D 19,44,65,3072
Ra 152,27,45056
Maaa 120,56,24
s_add_u32 m0, s62, 4096
Ta 8,11,49152
Ta 10,11,50176
Maaa 252,52,24
Ra 56,26,45056
Ta 12,11,57344
Maaa 252,148,28
Ta 14,11,58368
Ra 52,26,43008
Maaa 248,144,28
D 20,44,65,0
v_lshrrev_b32 v28, 1, v1
Maaa 248,48,24
s_lshl_b32 s80, s60, 9
Ra 148,27,43008
Ra 144,27,40960
Maaa 188,44,24
s_add_u32 s80, s80, 40960
Ra 48,26,40960
Ra 44,26,38912
Maaa 188,140,28
Ra 140,27,38912
Maaa 184,136,28
D 21,44,65,1024
Ra 136,27,36864
Maaa 184,40,24
Ra 40,26,36864
Mvaa 220,36,24
Ra 36,26,34816
Mvaa 220,132,28
Ra 132,27,34816
Mvaa 216,128,28
buffer_load_dwordx2 v[28:29], v28, s[8:11], s80 offen sc1
Ra 128,27,32768
Mvaa 216,32,24
W 13
Ra 32,26,32768
Maaa 68,60,0
Maaa 68,156,4
Maaa 64,152,4
)"
R"(D 22,44,65,2048
Ta 16,12,49152
Maaa 64,56,0
W 9
Ta 18,12,50176
Ta 20,12,57344
Maaa 196,52,0
Ta 22,12,58368
Maaa 196,148,4
Maaa 192,144,4
D 23,44,65,3072
Maaa 192,48,0
W 8
s_add_u32 m0, s61, 0
Mvaa 228,44,0
Mvaa 228,140,4
Mvaa 224,136,4
D 2,40,64,0
Mvaa 224,40,0
W 4
Mvaa 164,36,0
Mvaa 164,132,4
Mvaa 160,128,4
D 3,40,64,1024
Mvaa 160,32,0
Mvaa 168,32,8
Mvaa 168,128,12
Mvaa 172,132,12
D 4,40,64,2048
Ta 24,13,49152
Mvaa 172,36,8
Ta 26,13,50176
Ta 28,13,57344
Mvaa 232,40,8
Ta 30,13,58368
Mvaa 232,136,12
Mvaa 236,140,12
D 5,40,64,3072
Mvaa 236,44,8
s_add_u32 m0, s61, 4096
Maaa 200,48,8
Maaa 200,144,12
Maaa 204,148,12
D 6,40,64,0
Maaa 204,52,8
Maaa 72,56,8
Maaa 72,152,12
Maaa 76,156,12
D 7,40,64,1024
Maaa 76,60,8
W 4
Maaa 84,60,16
Maaa 84,156,20
Maaa 80,152,20
D 8,40,64,2048
Ta 0,14,49152
Maaa 80,56,16
Ta 2,14,50176
Ta 4,14,57344
Maaa 212,52,16
Ta 6,14,58368
Maaa 212,148,20
Maaa 208,144,20
D 9,40,64,3072
Maaa 208,48,16
Mvaa 244,44,16
Mvaa 244,140,20
Mvaa 240,136,20
s_waitcnt vmcnt(10)
v_cmp_eq_u32 vcc, s13, v29
s_cmp_eq_u64 vcc, -1
s_cbranch_scc0 .Lspin_16%=
.Lok_17%=:
global_load_dword v142, v146, s[6:7]
Mvaa 240,40,16
Mvaa 180,36,16
v_bfe_u32 v144, v1, 8, 1
Mvaa 180,132,20
v_mul_u32_u24 v144, 10240, v144
Mvaa 176,128,20
v_and_b32 v145, 0xf0, v1
Mvaa 176,32,16
W 4
v_lshl_add_u32 v144, v145, 3, v144
Mvaa 184,32,24
s_mul_i32 s91, s58, 20480
Mvaa 184,128,28
s_add_u32 s91, s91, s28
Mvaa 188,132,28
buffer_load_dword v142, v144, s[8:11], s91 offen offset:0 sc1
Ta 8,15,49152
Mvaa 188,36,24
Ta 10,15,50176
Ta 12,15,57344
Mvaa 248,40,24
Ta 14,15,58368
Mvaa 248,136,28
Mvaa 252,140,28
Mvaa 252,44,24
Maaa 216,48,24
Maaa 216,144,28
Maaa 220,148,28
Maaa 220,52,24
Maaa 88,56,24
Maaa 88,152,28
Maaa 92,156,28
Maaa 92,60,24
W 4
Maaa 100,60,0
Maaa 100,156,4
Maaa 96,152,4
Ta 16,24,49152
Ta 18,24,50176
Maaa 96,56,0
Ta 20,24,57344
Ta 22,24,58368
Maaa 228,52,0
Maaa 228,148,4
Maaa 224,144,4
Maaa 224,48,0
Maaa 164,44,0
Maaa 164,140,4
Maaa 160,136,4
Maaa 160,40,0
Mvaa 196,36,0
Mvaa 196,132,4
Mvaa 192,128,4
Mvaa 192,32,0
W 4
Mvaa 200,32,8
Ta 24,25,49152
Ta 26,25,50176
Mvaa 200,128,12
Mvaa 204,132,12
Ta 28,25,57344
Ta 30,25,58368
Mvaa 204,36,8
Maaa 168,40,8
Maaa 168,136,12
Maaa 172,140,12
Maaa 172,44,8
Maaa 232,48,8
Maaa 232,144,12
Maaa 236,148,12
s_waitcnt vmcnt(2) lgkmcnt(0)
s_barrier
s_cmp_eq_u32 s85, 107
s_cbranch_scc1 .Lrt_19%=
.Lrtr_20%=:
Maaa 236,52,8
s_add_u32 s64, s64, 128
Maaa 104,56,8
Maaa 104,152,12
s_and_b32 s64, s64, 1023
Maaa 108,156,12
Maaa 108,60,8
Maaa 116,60,16
s_lshl_b32 s65, s64, 12
Maaa 116,156,20
Maaa 112,152,20
Ta 0,10,16384
Ta 2,10,17408
Maaa 112,56,16
s_add_u32 m0, s62, 32768
Ta 4,10,24576
Ta 6,10,25600
Maaa 244,52,16
Maaa 244,148,20
Maaa 240,144,20
D 16,44,65,0
Maaa 240,48,16
Maaa 180,44,16
Maaa 180,140,20
Maaa 176,136,20
D 17,44,65,1024
Maaa 176,40,16
Mvaa 212,36,16
Mvaa 212,132,20
Mvaa 208,128,20
D 18,44,65,2048
Mvaa 208,32,16
Mvaa 216,32,24
Ra 32,26,0
Mvaa 216,128,28
Ra 128,27,0
Mvaa 220,132,28
D 19,44,65,3072
Ra 132,27,2048
Mvaa 220,36,24
s_add_u32 m0, s62, 36864
Ta 8,11,16384
Ta 10,11,17408
Maaa 184,40,24
Ra 36,26,2048
Ta 12,11,24576
Maaa 184,136,28
Ta 14,11,25600
Ra 40,26,4096
Maaa 188,140,28
D 20,44,65,0
Ra 136,27,4096
Maaa 188,44,24
Ra 140,27,6144
Ra 44,26,6144
Maaa 248,48,24
Ra 48,26,8192
Maaa 248,144,28
Ra 144,27,8192
Maaa 252,148,28
D 21,44,65,1024
Ra 148,27,10240
Maaa 252,52,24
Ra 52,26,10240
Maaa 120,56,24
Ra 56,26,12288
Maaa 120,152,28
Ra 152,27,12288
Maaa 124,156,28
D 22,44,65,2048
Ra 156,27,14336
Maaa 124,60,24
W 13
Ra 60,26,14336
Mvaa 160,32,0
Mvaa 160,128,4
Mvaa 164,132,4
D 23,44,65,3072
Ta 16,12,16384
Mvaa 164,36,0
W 9
s_add_u32 m0, s61, 32768
Ta 18,12,17408
Mvaa 224,40,0
.Lh_0_3_1%=:
s_cbranch_execz .Lhr_5%=
Ta 20,12,24576
Ta 22,12,25600
Mvaa 224,136,4
Mvaa 228,140,4
D 2,40,64,0
Mvaa 228,44,0
W 8
Maaa 192,48,0
Maaa 192,144,4
Maaa 196,148,4
D 3,40,64,1024
Maaa 196,52,0
W 4
Maaa 64,56,0
Maaa 64,152,4
Maaa 68,156,4
D 4,40,64,2048
Maaa 68,60,0
Maaa 76,60,8
Maaa 76,156,12
Maaa 72,152,12
D 5,40,64,3072
Ta 24,13,16384
Maaa 72,56,8
s_add_u32 m0, s61, 36864
Ta 26,13,17408
Ta 28,13,24576
Maaa 204,52,8
Ta 30,13,25600
Maaa 204,148,12
Maaa 200,144,12
D 6,40,64,0
Maaa 200,48,8
Mvaa 236,44,8
Mvaa 236,140,12
Mvaa 232,136,12
D 7,40,64,1024
Mvaa 232,40,8
Mvaa 172,36,8
Mvaa 172,132,12
Mvaa 168,128,12
D 8,40,64,2048
Mvaa 168,32,8
W 4
Mvaa 176,32,16
Mvaa 176,128,20
Mvaa 180,132,20
D 9,40,64,3072
v_add_u32 v2, 131072, v10
Mvaa 180,36,16
Ta 0,14,16384
v_add_u32 v3, 131072, v11
Mvaa 240,40,16
Ta 2,14,17408
v_add_u32 v4, 131072, v12
Mvaa 240,136,20
Ta 4,14,24576
Ta 6,14,25600
Mvaa 244,140,20
buffer_load_dword v142, v144, s[8:11], s91 offen offset:2048 sc1
v_add_u32 v5, 131072, v13
Mvaa 244,44,16
s_add_u32 s91, s91, 4096
v_add_u32 v6, 131072, v14
Maaa 208,48,16
v_add_u32 v7, 131072, v15
Maaa 208,144,20
Maaa 212,148,20
v_add_u32 v8, 131072, v24
Maaa 212,52,16
Maaa 80,56,16
v_add_u32 v9, 131072, v25
Maaa 80,152,20
s_add_u32 s65, s5, 0
s_cmp_ge_u32 s65, 20
s_cselect_b32 s90, 20, 0
s_sub_u32 s65, s65, s90
Maaa 84,156,20
s_lshl_b32 s65, s65, 18
s_add_u32 m0, s63, 131072
Maaa 84,60,16
W 4
Maaa 92,60,24
Maaa 92,156,28
Maaa 88,152,28
Ta 8,15,16384
Ta 10,15,17408
Maaa 88,56,24
D 16,48,65,0
Ta 12,15,24576
Maaa 220,52,24
Ta 14,15,25600
Maaa 220,148,28
Maaa 216,144,28
Maaa 216,48,24
D 17,48,65,1024
Mvaa 252,44,24
Mvaa 252,140,28
Mvaa 248,136,28
Mvaa 248,40,24
Mvaa 188,36,24
D 18,48,65,2048
Mvaa 188,132,28
Mvaa 184,128,28
Mvaa 184,32,24
W 4
Mvaa 192,32,0
Mvaa 192,128,4
D 19,48,65,3072
Mvaa 196,132,4
s_add_u32 m0, s63, 135168
Ta 16,24,16384
Ta 18,24,17408
Mvaa 196,36,0
Ta 20,24,24576
Ta 22,24,25600
Maaa 160,40,0
Maaa 160,136,4
Maaa 164,140,4
D 20,48,65,0
Maaa 164,44,0
Maaa 224,48,0
Maaa 224,144,4
Maaa 228,148,4
Maaa 228,52,0
D 21,48,65,1024
Maaa 96,56,0
Maaa 96,152,4
Maaa 100,156,4
Maaa 100,60,0
W 4
Maaa 108,60,8
D 22,48,65,2048
Ta 24,25,16384
Maaa 108,156,12
Ta 26,25,17408
Maaa 104,152,12
Ta 28,25,24576
Ta 30,25,25600
Maaa 104,56,8
Maaa 236,52,8
D 23,48,65,3072
Maaa 236,148,12
Maaa 232,144,12
Maaa 232,48,8
Maaa 172,44,8
Maaa 172,140,12
Maaa 168,136,12
s_waitcnt vmcnt(9) lgkmcnt(0)
s_barrier
s_cmp_eq_u32 s85, 107
s_cbranch_scc1 .Lrt_21%=
.Lrtr_22%=:
Maaa 168,40,8
s_add_u32 s65, s5, 4
Mvaa 204,36,8
Mvaa 204,132,12
s_cmp_ge_u32 s65, 20
s_cselect_b32 s90, 20, 0
Mvaa 200,128,12
Mvaa 200,32,8
s_sub_u32 s65, s65, s90
Mvaa 208,32,16
Mvaa 208,128,20
s_lshl_b32 s65, s65, 18
)"
R"(Mvaa 212,132,20
Ta 0,10,49152
Ta 2,10,50176
Mvaa 212,36,16
Maaa 176,40,16
s_add_u32 m0, s63, 0
Maaa 176,136,20
Maaa 180,140,20
D 16,48,65,0
Maaa 180,44,16
Maaa 240,48,16
Maaa 240,144,20
Maaa 244,148,20
D 17,48,65,1024
Ta 4,11,49152
Maaa 244,52,16
Ta 6,11,50176
Maaa 112,56,16
Maaa 112,152,20
Maaa 116,156,20
D 18,48,65,2048
Maaa 116,60,16
Maaa 124,60,24
Ra 60,26,47104
Maaa 124,156,28
Maaa 120,152,28
D 19,48,65,3072
Ta 8,12,49152
Maaa 120,56,24
s_add_u32 m0, s63, 4096
Ta 10,12,50176
Ra 56,26,45056
Maaa 252,52,24
Ra 52,26,43008
Maaa 252,148,28
Maaa 248,144,28
D 20,48,65,0
Maaa 248,48,24
Ra 48,26,40960
Maaa 188,44,24
Ra 44,26,38912
Maaa 188,140,28
Maaa 184,136,28
D 21,48,65,1024
Ta 12,13,49152
Maaa 184,40,24
Ta 14,13,50176
Ra 40,26,36864
Mvaa 220,36,24
Ra 36,26,34816
Mvaa 220,132,28
Mvaa 216,128,28
D 22,48,65,2048
Mvaa 216,32,24
Ra 32,26,32768
W 6
Maaa 68,60,0
Maaa 64,56,0
Maaa 196,52,0
D 23,48,65,3072
Ta 16,14,49152
Maaa 192,48,0
W 1
s_add_u32 s65, s5, 8
s_cmp_ge_u32 s65, 20
s_cselect_b32 s90, 20, 0
Mvaa 228,44,0
s_sub_u32 s65, s65, s90
s_lshl_b32 s65, s65, 18
Ta 18,14,50176
Mvaa 224,40,0
s_add_u32 m0, s63, 65536
Mvaa 164,36,0
D 16,48,65,0
Mvaa 160,32,0
Mvaa 168,32,4
Mvaa 172,36,4
Mvaa 232,40,4
D 17,48,65,1024
Ta 20,15,49152
Mvaa 236,44,4
Ta 22,15,50176
Maaa 200,48,4
Maaa 204,52,4
Maaa 72,56,4
D 18,48,65,2048
Maaa 76,60,4
Maaa 84,60,8
Maaa 80,56,8
Maaa 212,52,8
D 19,48,65,3072
Ta 24,24,49152
Maaa 208,48,8
s_add_u32 m0, s63, 69632
Ta 26,24,50176
Mvaa 244,44,8
Mvaa 240,40,8
Mvaa 180,36,8
D 20,48,65,0
Mvaa 176,32,8
Mvaa 184,32,12
Mvaa 188,36,12
Mvaa 248,40,12
D 21,48,65,1024
Ta 28,25,49152
Mvaa 252,44,12
Ta 30,25,50176
Maaa 216,48,12
Ra 156,27,47104
Maaa 220,52,12
Maaa 88,56,12
D 22,48,65,2048
Maaa 92,60,12
W 7
Ra 152,27,45056
Maaa 100,60,16
Maaa 96,56,16
Maaa 228,52,16
D 23,48,65,3072
Ra 148,27,43008
Maaa 224,48,16
Ta 0,10,57344
Ta 2,10,58368
Maaa 164,44,16
Maaa 160,40,16
Ra 144,27,40960
Mvaa 196,36,16
buffer_load_dword v142, v144, s[8:11], s91 offen offset:0 sc1
Mvaa 192,32,16
W 10
Mvaa 200,32,20
Ra 140,27,38912
Mvaa 204,36,20
Maaa 168,40,20
Ta 4,11,57344
Ta 6,11,58368
Maaa 172,44,20
Ra 136,27,36864
Maaa 232,48,20
Maaa 236,52,20
v_bfe_u32 v29, v1, 7, 1
Maaa 104,56,20
Ra 132,27,34816
Maaa 108,60,20
W 13
v_mul_u32_u24 v28, 10240, v29
Maaa 116,60,24
Maaa 112,56,24
Ra 128,27,32768
v_lshrrev_b32 v29, 8, v1
Maaa 244,52,24
Ta 8,12,57344
Ta 10,12,58368
Maaa 240,48,24
v_lshl_add_u32 v28, v29, 7, v28
Maaa 180,44,24
v_and_b32 v29, 0x70, v1
Maaa 176,40,24
Mvaa 212,36,24
v_add_u32 v28, v28, v29
Mvaa 208,32,24
W 14
Mvaa 216,32,28
s_and_b32 s80, s14, 2
Mvaa 220,36,28
s_mul_i32 s80, s80, 20480
Maaa 184,40,28
Ta 12,13,57344
Ta 14,13,58368
Maaa 188,44,28
s_sub_u32 s81, 40960, s80
Maaa 248,48,28
Maaa 252,52,28
s_add_u32 s93, s28, 20480
Maaa 120,56,28
Maaa 124,60,28
W 10
v_add_u32 v26, s80, v28
Maaa 68,156,0
Maaa 64,152,0
Maaa 196,148,0
Ta 16,14,57344
v_add_u32 v27, s81, v28
Maaa 192,144,0
W 5
Ta 18,14,58368
Mvaa 228,140,0
buffer_load_dwordx4 a[32:35], v26, s[8:11], s28 offen offset:0 sc1
Mvaa 224,136,0
Mvaa 164,132,0
Mvaa 160,128,0
Mvaa 168,128,4
buffer_load_dwordx4 a[36:39], v26, s[8:11], s93 offen offset:0 sc1
Mvaa 172,132,4
Mvaa 232,136,4
Ta 20,15,57344
Ta 22,15,58368
Mvaa 236,140,4
Maaa 200,144,4
buffer_load_dwordx4 a[40:43], v27, s[8:11], s28 offen offset:0 sc1
Maaa 204,148,4
Maaa 72,152,4
Maaa 76,156,4
W 6
Maaa 84,156,8
buffer_load_dwordx4 a[44:47], v27, s[8:11], s93 offen offset:0 sc1
Ta 24,24,57344
Maaa 80,152,8
s_add_u32 s28, s28, 512
s_cmp_eq_u32 s28, s4
s_cselect_b32 s93, 10240, 0
Ta 26,24,58368
Maaa 212,148,8
s_sub_u32 s28, s28, s93
Ta 28,25,57344
Ta 30,25,58368
Maaa 208,144,8
s_add_u32 s93, s28, 20480
Mvaa 244,140,8
Mvaa 240,136,8
Mvaa 180,132,8
s_add_u32 s65, s5, 12
s_cmp_ge_u32 s65, 20
s_cselect_b32 s90, 20, 0
buffer_load_dwordx4 a[48:51], v26, s[8:11], s28 offen offset:0 sc1
Mvaa 176,128,8
W 8
s_sub_u32 s65, s65, s90
s_lshl_b32 s65, s65, 18
Mvaa 184,128,12
s_add_u32 m0, s63, 32768
Mvaa 188,132,12
Mvaa 248,136,12
s_waitcnt vmcnt(6) lgkmcnt(0)
s_barrier
Mvaa 252,140,12
buffer_load_dwordx4 a[52:55], v26, s[8:11], s93 offen offset:0 sc1
Maaa 216,144,12
Maaa 220,148,12
Maaa 88,152,12
Maaa 92,156,12
buffer_load_dwordx4 a[56:59], v27, s[8:11], s28 offen offset:0 sc1
Maaa 100,156,16
Maaa 96,152,16
Maaa 228,148,16
Maaa 224,144,16
buffer_load_dwordx4 a[60:63], v27, s[8:11], s93 offen offset:0 sc1
Maaa 164,140,16
Maaa 160,136,16
Mvaa 196,132,16
Mvaa 192,128,16
D 16,48,65,0
Mvaa 200,128,20
Mvaa 204,132,20
Maaa 168,136,20
Ta 0,2,0
Ta 2,2,1024
Maaa 172,140,20
D 17,48,65,1024
Maaa 232,144,20
Maaa 236,148,20
Maaa 104,152,20
Ta 4,2,8192
Ta 6,2,9216
Maaa 108,156,20
D 18,48,65,2048
Maaa 116,156,24
Maaa 112,152,24
Maaa 244,148,24
Ta 8,3,0
Ta 10,3,1024
Maaa 240,144,24
D 19,48,65,3072
Maaa 180,140,24
s_add_u32 m0, s63, 36864
Maaa 176,136,24
Mvaa 212,132,24
Ta 12,3,8192
Ta 14,3,9216
Mvaa 208,128,24
D 20,48,65,0
Mvaa 216,128,28
Mvaa 220,132,28
Maaa 184,136,28
Ta 16,4,0
Ta 18,4,1024
Maaa 188,140,28
Maaa 248,144,28
D 21,48,65,1024
Maaa 252,148,28
Maaa 120,152,28
Maaa 124,156,28
.Lq_7%=:
s_cmp_ge_u32 s14, 2
s_cbranch_scc1 .Lpb_27%=
s_waitcnt vmcnt(6) lgkmcnt(6)
Zvaa 32,0,32
s_add_u32 s28, s28, 512
Ta 20,4,8192
Ta 22,4,9216
Mvaa 32,4,48
s_cmp_eq_u32 s28, s4
s_cselect_b32 s93, 10240, 0
Ta 24,5,0
Ta 26,5,1024
Zvaa 64,4,52
s_sub_u32 s28, s28, s93
s_add_u32 s93, s28, 20480
buffer_load_dwordx4 a[128:131], v26, s[8:11], s28 offen offset:0 sc1
Mvaa 64,0,36
Ta 28,5,8192
Ta 30,5,9216
Zvaa 96,0,40
K 184,184,185
K 185,186,187
Mvaa 96,4,56
K 186,188,189
K 187,190,191
Zvaa 128,4,60
K 192,192,193
Mvaa 128,0,44
W 8
buffer_load_dwordx4 a[132:135], v26, s[8:11], s93 offen offset:0 sc1
K 193,194,195
Zvaa 132,8,44
Ta 0,6,0
Ta 2,6,1024
Mvaa 132,12,60
K 194,196,197
K 195,198,199
Zvaa 100,12,56
K 200,200,201
K 201,202,203
Mvaa 100,8,40
Ta 4,6,8192
Ta 6,6,9216
Zvaa 68,8,36
buffer_load_dwordx4 a[136:139], v27, s[8:11], s28 offen offset:0 sc1
K 202,204,205
Mvaa 68,12,52
K 203,206,207
K 208,208,209
Zvaa 36,12,48
K 209,210,211
K 210,212,213
Mvaa 36,8,32
W 8
Ta 8,7,0
K 211,214,215
Zvaa 40,16,32
Ta 10,7,1024
K 216,216,217
Mvaa 40,20,48
buffer_load_dwordx4 a[140:143], v27, s[8:11], s93 offen offset:0 sc1
K 217,218,219
Zvaa 72,20,52
s_add_u32 s28, s28, 512
s_cmp_eq_u32 s28, s4
s_cselect_b32 s93, 10240, 0
s_sub_u32 s28, s28, s93
Mvaa 72,16,36
s_add_u32 s93, s28, 20480
K 218,220,221
K 219,222,223
Zvaa 104,16,40
Ta 12,7,8192
Ta 14,7,9216
Mvaa 104,20,56
K 224,224,225
K 225,226,227
Zvaa 136,20,60
K 226,228,229
K 227,230,231
Mvaa 136,16,44
W 8
buffer_load_dwordx4 a[144:147], v26, s[8:11], s28 offen offset:0 sc1
K 232,232,233
Zvaa 140,24,44
K 233,234,235
K 234,236,237
Mvaa 140,28,60
Ta 16,8,0
K 235,238,239
Zvaa 108,28,56
Ta 18,8,1024
K 240,240,241
Mvaa 108,24,40
K 241,242,243
K 242,244,245
Zvaa 76,24,36
buffer_load_dwordx4 a[148:151], v26, s[8:11], s93 offen offset:0 sc1
K 243,246,247
Mvaa 76,28,52
Ta 20,8,8192
Ta 22,8,9216
Zvaa 44,28,48
K 248,248,249
K 249,250,251
Mvaa 44,24,32
K 250,252,253
)"
R"(K 251,254,255
W 8
Zvaa 48,0,32
Ta 24,9,0
Ta 26,9,1024
Mvaa 48,4,48
buffer_load_dwordx4 a[152:155], v27, s[8:11], s28 offen offset:0 sc1
K 160,160,161
Zvaa 80,4,52
Ta 28,9,8192
Ta 30,9,9216
Mvaa 80,0,36
K 161,162,163
Zvaa 112,0,40
K 162,164,165
Mvaa 112,4,56
Zvaa 144,4,60
buffer_load_dwordx4 a[156:159], v27, s[8:11], s93 offen offset:0 sc1
K 163,166,167
Mvaa 144,0,44
W 8
s_add_u32 s28, s28, 512
s_cmp_eq_u32 s28, s4
s_cselect_b32 s93, 10240, 0
Zvaa 148,8,44
s_sub_u32 s28, s28, s93
s_add_u32 s93, s28, 20480
Ta 0,2,16384
Mvaa 148,12,60
s_add_u32 s36, s29, 0
Ta 2,2,17408
Zvaa 116,12,56
Ta 4,2,24576
Ta 6,2,25600
Mvaa 116,8,40
buffer_load_dwordx4 v[188:191], v26, s[8:11], s28 offen offset:0 sc1
Zvaa 84,8,36
Mvaa 84,12,52
Zvaa 52,12,48
Mvaa 52,8,32
W 8
Ta 8,3,16384
Ta 10,3,17408
Zvaa 56,16,32
buffer_load_dwordx4 v[196:199], v26, s[8:11], s93 offen offset:0 sc1
Mvaa 56,20,48
Zvaa 88,20,52
Ta 12,3,24576
Ta 14,3,25600
Mvaa 88,16,36
Zvaa 120,16,40
buffer_store_dwordx4 v[160:163], v1, s[8:11], s36 offen offset:0 sc1
K 168,168,169
Mvaa 120,20,56
K 169,170,171
K 170,172,173
Zvaa 152,20,60
Mvaa 152,16,44
W 8
Ta 16,4,16384
K 171,174,175
Zvaa 156,24,44
Ta 18,4,17408
Mvaa 156,28,60
buffer_load_dwordx4 v[204:207], v27, s[8:11], s28 offen offset:0 sc1
Zvaa 124,28,56
Ta 20,4,24576
Ta 22,4,25600
Mvaa 124,24,40
Zvaa 92,24,36
Mvaa 92,28,52
Zvaa 60,28,48
buffer_load_dwordx4 v[212:215], v27, s[8:11], s93 offen offset:0 sc1
Mvaa 60,24,32
s_waitcnt vmcnt(5) lgkmcnt(8)
s_add_u32 s28, s28, 512
s_cmp_eq_u32 s28, s4
s_cselect_b32 s93, 10240, 0
Mvaa 32,0,128
s_sub_u32 s28, s28, s93
s_add_u32 s93, s28, 20480
Ta 24,5,16384
Mvaa 32,4,144
Ta 26,5,17408
Mvaa 64,4,148
Ta 28,5,24576
Ta 30,5,25600
Mvaa 64,0,132
buffer_store_dwordx4 v[168:171], v1, s[8:11], s36 offen offset:1024 sc1
K 176,176,177
Mvaa 96,0,136
K 177,178,179
K 178,180,181
Mvaa 96,4,152
Mvaa 128,4,156
Mvaa 128,0,140
W 8
Ta 0,6,16384
K 179,182,183
Mvaa 132,8,140
buffer_load_dwordx4 v[220:223], v26, s[8:11], s28 offen offset:0 sc1
Ta 2,6,17408
Mvaa 132,12,156
Mvaa 100,12,152
Ta 4,6,24576
Ta 6,6,25600
Mvaa 100,8,136
Mvaa 68,8,132
Mvaa 68,12,148
buffer_load_dwordx4 v[228:231], v26, s[8:11], s93 offen offset:0 sc1
Mvaa 36,12,144
Mvaa 36,8,128
W 8
Ta 8,7,16384
Ta 10,7,17408
Mvaa 40,16,128
Mvaa 40,20,144
Mvaa 72,20,148
buffer_load_dwordx4 v[236:239], v27, s[8:11], s28 offen offset:0 sc1
Ta 12,7,24576
Mvaa 72,16,132
buffer_store_dwordx4 v[176:179], v1, s[8:11], s36 offen offset:2048 sc1
Ta 14,7,25600
Mvaa 104,16,136
Mvaa 104,20,152
Mvaa 136,20,156
Mvaa 136,16,140
W 8
Ta 16,8,16384
Ta 18,8,17408
Mvaa 140,24,140
buffer_load_dwordx4 v[244:247], v27, s[8:11], s93 offen offset:0 sc1
Mvaa 140,28,156
s_add_u32 s28, s28, 512
s_cmp_eq_u32 s28, s4
s_cselect_b32 s93, 10240, 0
s_sub_u32 s28, s28, s93
Mvaa 108,28,152
s_add_u32 s93, s28, 20480
Ta 20,8,24576
Ta 22,8,25600
Mvaa 108,24,136
Mvaa 76,24,132
Mvaa 76,28,148
buffer_load_dwordx4 v[252:255], v26, s[8:11], s28 offen offset:0 sc1
Mvaa 44,28,144
Mvaa 44,24,128
W 8
Ta 24,9,16384
Ta 26,9,17408
Mvaa 48,0,128
Mvaa 48,4,144
Mvaa 80,4,148
D 22,48,65,2048
Ta 28,9,24576
Mvaa 80,0,132
buffer_store_dwordx4 v[184:187], v1, s[8:11], s36 offen offset:3072 sc1
Ta 30,9,25600
Mvaa 112,0,136
s_add_u32 s36, s29, 4096
s_lshl_b32 s80, s56, 9
Mvaa 112,4,152
Mvaa 144,4,156
s_lshl_b32 s81, s57, 4
Mvaa 144,0,140
W 8
s_add_u32 s80, s80, s81
Ta 0,10,0
Mvaa 148,8,140
buffer_load_dwordx4 a[32:35], v26, s[8:11], s93 offen offset:0 sc1
Ta 2,10,1024
Mvaa 148,12,156
s_and_b32 s81, s14, 1
Mvaa 116,12,152
s_lshl_b32 s81, s81, 3
Ta 4,10,8192
Ta 6,10,9216
Mvaa 116,8,136
Mvaa 84,8,132
s_add_u32 s80, s80, s81
Mvaa 84,12,148
s_add_u32 s71, s80, 4096
buffer_load_dwordx4 a[36:39], v27, s[8:11], s28 offen offset:0 sc1
Mvaa 52,12,144
Mvaa 52,8,128
W 8
Ta 8,11,0
Ta 10,11,1024
Mvaa 56,16,128
Mvaa 56,20,144
Mvaa 88,20,148
buffer_store_dwordx4 v[192:195], v1, s[8:11], s36 offen offset:0 sc1
Ta 12,11,8192
Mvaa 88,16,132
Ta 14,11,9216
Mvaa 120,16,136
Mvaa 120,20,152
Mvaa 152,20,156
Mvaa 152,16,140
W 8
buffer_load_dwordx4 a[40:43], v27, s[8:11], s93 offen offset:0 sc1
Ta 16,12,0
Mvaa 156,24,140
s_add_u32 s28, s28, 512
s_cmp_eq_u32 s28, s4
s_cselect_b32 s93, 10240, 0
s_sub_u32 s28, s28, s93
Mvaa 156,28,156
s_add_u32 s93, s28, 20480
Ta 18,12,1024
Mvaa 124,28,152
Ta 20,12,8192
Ta 22,12,9216
Mvaa 124,24,136
Mvaa 92,24,132
buffer_load_dwordx4 a[44:47], v26, s[8:11], s28 offen offset:0 sc1
Mvaa 92,28,148
Mvaa 60,28,144
Mvaa 60,24,128
s_waitcnt vmcnt(8) lgkmcnt(8)
Ta 24,13,0
Ta 26,13,1024
Mvav 32,0,188
Mvav 32,4,220
D 23,48,65,3072
Mvav 64,4,228
s_add_u32 s65, s5, 16
s_cmp_ge_u32 s65, 20
s_cselect_b32 s90, 20, 0
buffer_store_dwordx4 v[200:203], v1, s[8:11], s36 offen offset:1024 sc1
Mvav 64,0,196
s_sub_u32 s65, s65, s90
s_lshl_b32 s65, s65, 18
s_add_u32 m0, s63, 98304
Mvav 96,0,204
Ta 28,13,8192
Ta 30,13,9216
Mvav 96,4,236
Mvav 128,4,244
Mvav 128,0,212
W 8
buffer_load_dwordx4 a[48:51], v26, s[8:11], s93 offen offset:0 sc1
Ta 0,14,0
Mvav 132,8,212
Ta 2,14,1024
v_add_u32 v2, 65536, v10
Mvav 132,12,244
Mvav 100,12,236
Ta 4,14,8192
Ta 6,14,9216
Mvav 100,8,204
v_add_u32 v3, 65536, v11
Mvav 68,8,196
buffer_load_dwordx4 a[52:55], v27, s[8:11], s28 offen offset:0 sc1
Mvav 68,12,228
Mvav 36,12,220
Mvav 36,8,188
W 8
Ta 8,15,0
Ta 10,15,1024
Mvav 40,16,188
v_add_u32 v4, 65536, v12
Mvav 40,20,220
buffer_load_dwordx4 a[56:59], v27, s[8:11], s93 offen offset:0 sc1
Mvav 72,20,228
s_add_u32 s28, s28, 512
s_cmp_eq_u32 s28, s4
s_cselect_b32 s93, 10240, 0
buffer_store_dwordx4 v[208:211], v1, s[8:11], s36 offen offset:2048 sc1
Mvav 72,16,196
s_sub_u32 s28, s28, s93
s_add_u32 s93, s28, 20480
Ta 12,15,8192
Mvav 104,16,204
Ta 14,15,9216
v_add_u32 v5, 65536, v13
Mvav 104,20,236
Mvav 136,20,244
Mvav 136,16,212
W 8
D 16,48,65,0
Ta 16,24,0
Mvav 140,24,212
Ta 18,24,1024
v_add_u32 v6, 65536, v14
Mvav 140,28,244
Mvav 108,28,236
Ta 20,24,8192
Ta 22,24,9216
Mvav 108,24,204
v_add_u32 v7, 65536, v15
Mvav 76,24,196
buffer_load_dwordx4 a[60:63], v26, s[8:11], s28 offen offset:0 sc1
Mvav 76,28,228
Mvav 44,28,220
Mvav 44,24,188
W 8
Ta 24,25,0
Ta 26,25,1024
Mvav 48,0,188
v_add_u32 v8, 65536, v24
Mvav 48,4,220
buffer_load_dwordx4 v[184:187], v26, s[8:11], s93 offen offset:0 sc1
Mvav 80,4,228
buffer_store_dwordx4 v[216:219], v1, s[8:11], s36 offen offset:3072 sc1
Ta 28,25,8192
Mvav 80,0,196
s_add_u32 s36, s29, 458752
Ta 30,25,9216
v_add_u32 v9, 65536, v25
Mvav 112,0,204
Mvav 112,4,236
Mvav 144,4,244
Mvav 144,0,212
W 8
buffer_load_dwordx4 v[192:195], v27, s[8:11], s28 offen offset:0 sc1
Ta 0,10,16384
Mvav 148,8,212
Ta 2,10,17408
Mvav 148,12,244
Mvav 116,12,236
Ta 4,10,24576
Ta 6,10,25600
Mvav 116,8,204
Mvav 84,8,196
D 17,48,65,1024
Mvav 84,12,228
Mvav 52,12,220
Mvav 52,8,188
W 8
Ta 8,11,16384
Ta 10,11,17408
Mvav 56,16,188
Mvav 56,20,220
buffer_store_dwordx4 v[224:227], v1, s[8:11], s36 offen offset:0 sc1
Mvav 88,20,228
Ta 12,11,24576
Ta 14,11,25600
Mvav 88,16,196
Mvav 120,16,204
Mvav 120,20,236
Mvav 152,20,244
buffer_load_dwordx4 a[128:131], v27, s[8:11], s93 offen offset:0 sc1
Mvav 152,16,212
W 8
s_add_u32 s28, s28, 512
s_cmp_eq_u32 s28, s4
s_cselect_b32 s93, 10240, 0
Mvav 156,24,212
s_sub_u32 s28, s28, s93
s_add_u32 s93, s28, 20480
Ta 16,12,16384
Mvav 156,28,244
Ta 18,12,17408
Mvav 124,28,236
Ta 20,12,24576
Ta 22,12,25600
Mvav 124,24,204
buffer_load_dwordx4 a[132:135], v26, s[8:11], s28 offen offset:0 sc1
Mvav 92,24,196
Mvav 92,28,228
Mvav 60,28,220
Mvav 60,24,188
s_waitcnt vmcnt(10) lgkmcnt(8)
Ta 24,13,16384
Ta 26,13,17408
Mvav 32,0,252
buffer_load_dwordx4 a[136:139], v26, s[8:11], s93 offen offset:0 sc1
Mvaa 32,4,44
buffer_store_dwordx4 v[232:235], v1, s[8:11], s36 offen offset:1024 sc1
Mvaa 64,4,48
Ta 28,13,24576
Ta 30,13,25600
Mvaa 64,0,32
Mvaa 96,0,36
Mvaa 96,4,52
Mvaa 128,4,56
buffer_load_dwordx4 a[140:143], v27, s[8:11], s28 offen offset:0 sc1
Mvaa 128,0,40
W 8
Ta 0,14,16384
Ta 2,14,17408
Mvaa 132,8,40
Mvaa 132,12,56
Mvaa 100,12,52
Ta 4,14,24576
Ta 6,14,25600
Mvaa 100,8,36
D 18,48,65,2048
Mvaa 68,8,32
Mvaa 68,12,48
Mvaa 36,12,44
Mvav 36,8,252
W 8
Ta 8,15,16384
Ta 10,15,17408
Mvav 40,16,252
buffer_load_dwordx4 a[144:147], v27, s[8:11], s93 offen offset:0 sc1
Mvaa 40,20,44
s_add_u32 s28, s28, 512
)"
R"(s_cmp_eq_u32 s28, s4
s_cselect_b32 s93, 10240, 0
buffer_store_dwordx4 v[240:243], v1, s[8:11], s36 offen offset:2048 sc1
Mvaa 72,20,48
s_sub_u32 s28, s28, s93
s_add_u32 s93, s28, 20480
Ta 12,15,24576
Mvaa 72,16,32
Ta 14,15,25600
Mvaa 104,16,36
Mvaa 104,20,52
Mvaa 136,20,56
buffer_load_dwordx4 a[148:151], v26, s[8:11], s28 offen offset:0 sc1
Mvaa 136,16,40
W 8
Ta 16,24,16384
Ta 18,24,17408
Mvaa 140,24,40
Mvaa 140,28,56
Mvaa 108,28,52
Ta 20,24,24576
Ta 22,24,25600
Mvaa 108,24,36
buffer_load_dwordx4 a[152:155], v26, s[8:11], s93 offen offset:0 sc1
Mvaa 76,24,32
Mvaa 76,28,48
Mvaa 44,28,44
Mvav 44,24,252
W 8
Ta 24,25,16384
Ta 26,25,17408
Mvav 48,0,252
buffer_load_dwordx4 a[156:159], v27, s[8:11], s28 offen offset:0 sc1
Mvaa 48,4,44
buffer_store_dwordx4 v[248:251], v1, s[8:11], s36 offen offset:3072 sc1
G 160,160
Mvaa 80,4,48
G 161,161
G 162,162
Mvaa 80,0,32
G 163,163
G 164,164
Mvaa 112,0,36
G 165,165
G 166,166
Mvaa 112,4,52
Ta 28,25,24576
Ta 30,25,25600
Mvaa 144,4,56
D 19,48,65,3072
G 167,167
Mvaa 144,0,40
W 8
s_add_u32 m0, s63, 102400
K 160,160,161
Mvaa 148,8,40
K 161,162,163
K 162,164,165
Mvaa 148,12,56
Ta 0,2,0
K 163,166,167
Mvaa 116,12,52
s_add_u32 s36, s29, 462848
Ta 2,2,1024
Ta 4,2,8192
Mvaa 116,8,36
buffer_load_dwordx4 v[200:203], v27, s[8:11], s93 offen offset:0 sc1
Ta 6,2,9216
Mvaa 84,8,32
s_add_u32 s28, s28, 512
s_cmp_eq_u32 s28, s4
s_cselect_b32 s93, 10240, 0
s_sub_u32 s28, s28, s93
Mvaa 84,12,48
s_add_u32 s93, s28, 20480
Mvaa 52,12,44
Mvav 52,8,252
W 8
Ta 8,3,0
Ta 10,3,1024
Mvav 56,16,252
buffer_store_dwordx4 v[160:163], v1, s[8:11], s36 offen offset:0 sc1
G 168,168
Mvaa 56,20,44
G 169,169
G 170,170
Mvaa 88,20,48
G 171,171
G 172,172
Mvaa 88,16,32
G 173,173
G 174,174
Mvaa 120,16,36
Ta 12,3,8192
Ta 14,3,9216
Mvaa 120,20,52
buffer_load_dwordx4 v[208:211], v26, s[8:11], s28 offen offset:0 sc1
G 175,175
Mvaa 152,20,56
K 168,168,169
K 169,170,171
Mvaa 152,16,40
W 8
K 170,172,173
K 171,174,175
Mvaa 156,24,40
Ta 16,4,0
Ta 18,4,1024
Mvaa 156,28,56
Mvaa 124,28,52
buffer_load_dwordx4 v[216:219], v26, s[8:11], s93 offen offset:0 sc1
Ta 20,4,8192
Mvaa 124,24,36
Ta 22,4,9216
Mvaa 92,24,32
Mvaa 92,28,48
Mvaa 60,28,44
Mvav 60,24,252
s_waitcnt vmcnt(10) lgkmcnt(8)
D 20,48,65,0
Ta 24,5,0
Mvaa 32,0,60
buffer_store_dwordx4 v[168:171], v1, s[8:11], s36 offen offset:1024 sc1
G 176,176
Mvaa 32,4,132
G 177,177
G 178,178
Mvaa 64,4,136
Ta 26,5,1024
G 179,179
Mvav 64,0,184
G 180,180
G 181,181
Mvav 96,0,192
Ta 28,5,8192
G 182,182
Mvaa 96,4,140
buffer_load_dwordx4 v[224:227], v27, s[8:11], s28 offen offset:0 sc1
Ta 30,5,9216
Mvaa 128,4,144
G 183,183
K 176,176,177
Mvaa 128,0,128
W 8
K 177,178,179
K 178,180,181
Mvaa 132,8,128
Ta 0,6,0
K 179,182,183
Mvaa 132,12,144
Ta 2,6,1024
Mvaa 100,12,140
buffer_load_dwordx4 v[188:191], v27, s[8:11], s93 offen offset:0 sc1
Ta 4,6,8192
Mvav 100,8,192
s_add_u32 s28, s28, 512
s_cmp_eq_u32 s28, s4
s_cselect_b32 s93, 10240, 0
s_sub_u32 s28, s28, s93
Mvav 68,8,184
s_add_u32 s93, s28, 20480
Ta 6,6,9216
Mvaa 68,12,136
Mvaa 36,12,132
Mvaa 36,8,60
W 8
buffer_load_dwordx4 v[196:199], v26, s[8:11], s28 offen offset:0 sc1
Ta 8,7,0
Mvaa 40,16,60
buffer_store_dwordx4 v[176:179], v1, s[8:11], s36 offen offset:2048 sc1
G 160,184
Mvaa 40,20,132
G 161,185
G 162,186
Mvaa 72,20,136
Ta 10,7,1024
G 163,187
Mvav 72,16,184
G 164,188
G 165,189
Mvav 104,16,192
Ta 12,7,8192
G 166,190
Mvaa 104,20,140
buffer_load_dwordx4 v[204:207], v26, s[8:11], s93 offen offset:0 sc1
Ta 14,7,9216
Mvaa 136,20,144
G 167,191
K 160,160,161
Mvaa 136,16,128
W 8
K 161,162,163
K 162,164,165
Mvaa 140,24,128
Ta 16,8,0
K 163,166,167
Mvaa 140,28,144
Ta 18,8,1024
Mvaa 108,28,140
D 21,48,65,1024
Ta 20,8,8192
Mvav 108,24,192
Ta 22,8,9216
Mvav 76,24,184
Mvaa 76,28,136
Mvaa 44,28,132
Mvaa 44,24,60
W 8
buffer_load_dwordx4 v[212:215], v27, s[8:11], s28 offen offset:0 sc1
Ta 24,9,0
Mvaa 48,0,60
buffer_store_dwordx4 v[160:163], v1, s[8:11], s36 offen offset:3072 sc1
G 168,192
Mvaa 48,4,132
G 169,193
G 170,194
Mvaa 80,4,136
G 171,195
G 172,196
Mvav 80,0,184
Ta 26,9,1024
G 173,197
Mvav 112,0,192
G 174,198
G 175,199
Mvaa 112,4,140
buffer_load_dwordx4 v[220:223], v27, s[8:11], s93 offen offset:0 sc1
Ta 28,9,8192
Mvaa 144,4,144
s_add_u32 s28, s28, 512
s_cmp_eq_u32 s28, s4
s_cselect_b32 s93, 10240, 0
s_sub_u32 s28, s28, s93
Mvaa 144,0,128
W 7
s_add_u32 s93, s28, 20480
Ta 30,9,9216
Mvaa 148,8,128
K 168,168,169
K 169,170,171
Mvaa 148,12,144
K 170,172,173
K 171,174,175
Mvaa 116,12,140
buffer_load_dwordx4 v[228:231], v26, s[8:11], s28 offen offset:0 sc1
Ta 0,2,16384
Mvav 116,8,192
Ta 2,2,17408
v_accvgpr_write_b32 a192, v168
Mvav 84,8,184
v_accvgpr_write_b32 a193, v169
v_accvgpr_write_b32 a194, v170
Mvaa 84,12,136
Ta 4,2,24576
Ta 6,2,25600
Mvaa 52,12,132
v_accvgpr_write_b32 a195, v171
G 176,64
Mvaa 52,8,60
W 8
D 22,48,65,2048
G 177,65
Mvaa 56,16,60
D 23,48,65,3072
G 178,66
Mvaa 56,20,132
Ta 8,3,16384
Ta 10,3,17408
Mvaa 88,20,136
G 179,67
G 180,68
Mvav 88,16,184
Ta 12,3,24576
Ta 14,3,25600
Mvav 120,16,192
G 181,69
G 182,70
Mvaa 120,20,140
buffer_load_dwordx4 v[236:239], v26, s[8:11], s93 offen offset:0 sc1
G 183,71
Mvaa 152,20,144
v_lshrrev_b32 v20, 4, v1
K 176,176,177
Mvaa 152,16,128
W 8
Ta 16,4,16384
v_add_u32 v20, 0, v20
Mvaa 156,24,128
Ta 18,4,17408
K 177,178,179
Mvaa 156,28,144
v_mul_u32_u24 v22, 205, v20
K 178,180,181
Mvaa 124,28,140
buffer_load_dwordx4 v[232:235], v27, s[8:11], s28 offen offset:0 sc1
K 179,182,183
Mvav 124,24,192
Ta 20,4,24576
Ta 22,4,25600
Mvav 92,24,184
v_lshrrev_b32 v22, 12, v22
v_accvgpr_write_b32 a196, v176
Mvaa 92,28,136
v_mul_u32_u24 v23, 20, v22
v_accvgpr_write_b32 a197, v177
Mvaa 60,28,132
v_accvgpr_write_b32 a198, v178
v_sub_u32 v20, v20, v23
Mvaa 60,24,60
s_waitcnt vmcnt(12) lgkmcnt(8)
buffer_load_dwordx4 a[32:35], v27, s[8:11], s93 offen offset:0 sc1
v_accvgpr_write_b32 a199, v179
Mvaa 32,0,148
s_add_u32 s28, s28, 512
s_cmp_eq_u32 s28, s4
s_cselect_b32 s93, 10240, 0
s_sub_u32 s28, s28, s93
Mvav 32,4,208
s_add_u32 s93, s28, 20480
Ta 24,5,16384
Ta 26,5,17408
Mvav 64,4,216
v_lshrrev_b32 v23, 1, v22
G 168,200
Mvaa 64,0,152
v_and_b32 v23, 2, v23
G 169,201
Mvaa 96,0,156
buffer_load_dwordx4 a[36:39], v26, s[8:11], s28 offen offset:0 sc1
Ta 28,5,24576
Mvav 96,4,224
Ta 30,5,25600
G 170,202
Mvav 128,4,188
v_xor_b32 v20, v20, v23
G 171,203
Mvav 128,0,200
W 8
v_mul_u32_u24 v22, 80, v22
G 172,204
Mvav 132,8,200
v_add_lshl_u32 v16, v20, v22, 4
G 173,205
Mvav 132,12,188
buffer_load_dwordx4 a[40:43], v26, s[8:11], s93 offen offset:0 sc1
Ta 0,6,16384
Mvav 100,12,224
Ta 2,6,17408
G 174,206
Mvaa 100,8,156
v_lshrrev_b32 v20, 4, v1
G 175,207
Mvaa 68,8,152
Ta 4,6,24576
v_add_u32 v20, 64, v20
Mvav 68,12,216
Ta 6,6,25600
K 168,168,169
Mvav 36,12,208
buffer_load_dwordx4 a[44:47], v27, s[8:11], s28 offen offset:0 sc1
K 169,170,171
Mvaa 36,8,148
W 8
v_mul_u32_u24 v22, 205, v20
K 170,172,173
Mvaa 40,16,148
v_lshrrev_b32 v22, 12, v22
K 171,174,175
Mvav 40,20,208
Ta 8,7,16384
v_mul_u32_u24 v23, 20, v22
Mvav 72,20,216
Ta 10,7,17408
v_accvgpr_write_b32 a200, v168
Mvaa 72,16,152
buffer_load_dwordx4 a[48:51], v27, s[8:11], s93 offen offset:0 sc1
v_accvgpr_write_b32 a201, v169
Mvaa 104,16,156
s_add_u32 s28, s28, 512
s_cmp_eq_u32 s28, s4
s_cselect_b32 s93, 10240, 0
s_sub_u32 s28, s28, s93
Mvav 104,20,224
s_add_u32 s93, s28, 20480
v_sub_u32 v20, v20, v23
v_accvgpr_write_b32 a202, v170
Mvav 136,20,188
Ta 12,7,24576
Ta 14,7,25600
Mvav 136,16,200
W 8
v_lshrrev_b32 v23, 1, v22
v_accvgpr_write_b32 a203, v171
Mvav 140,24,200
buffer_load_dwordx4 a[52:55], v26, s[8:11], s28 offen offset:0 sc1
v_and_b32 v23, 2, v23
Mvav 140,28,188
G 176,72
G 177,73
Mvav 108,28,224
)"
R"(v_xor_b32 v20, v20, v23
G 178,74
Mvaa 108,24,156
Ta 16,8,16384
Ta 18,8,17408
Mvaa 76,24,152
v_mul_u32_u24 v22, 80, v22
G 179,75
Mvav 76,28,216
buffer_load_dwordx4 a[56:59], v26, s[8:11], s93 offen offset:0 sc1
G 180,76
Mvav 44,28,208
Ta 20,8,24576
v_add_lshl_u32 v17, v20, v22, 4
Mvaa 44,24,148
W 7
Ta 22,8,25600
G 181,77
Mvaa 48,0,148
v_subrev_u32 v17, 1024, v17
G 182,78
Mvav 48,4,208
v_lshrrev_b32 v20, 4, v1
G 183,79
Mvav 80,4,216
buffer_load_dwordx4 a[184:187], v27, s[8:11], s28 offen offset:0 sc1
K 176,176,177
Mvaa 80,0,152
Ta 24,9,16384
v_add_u32 v20, 128, v20
Mvaa 112,0,156
Ta 26,9,17408
K 177,178,179
Mvav 112,4,224
v_mul_u32_u24 v22, 205, v20
K 178,180,181
Mvav 144,4,188
v_lshrrev_b32 v22, 12, v22
K 179,182,183
Mvav 144,0,200
W 6
buffer_load_dwordx4 a[188:191], v27, s[8:11], s93 offen offset:0 sc1
Ta 28,9,24576
Mvav 148,8,200
s_add_u32 s28, s28, 512
s_cmp_eq_u32 s28, s4
s_cselect_b32 s93, 10240, 0
s_sub_u32 s28, s28, s93
Mvav 148,12,188
s_add_u32 s93, s28, 20480
Ta 30,9,25600
v_accvgpr_write_b32 a204, v176
Mvav 116,12,224
v_mul_u32_u24 v23, 20, v22
v_accvgpr_write_b32 a205, v177
Mvaa 116,8,156
s_waitcnt vmcnt(15)
v_mov_b64 v[28:29], s[12:13]
s_mov_b64 exec, 1
buffer_store_dwordx2 v[28:29], off, s[8:11], s71 sc1
s_mov_b64 exec, -1
v_sub_u32 v20, v20, v23
v_accvgpr_write_b32 a206, v178
Mvaa 84,8,152
s_waitcnt vmcnt(12) lgkmcnt(0)
s_barrier
Mvav 84,12,216
v_accvgpr_write_b32 a207, v179
v_lshrrev_b32 v23, 1, v22
Mvav 52,12,208
G 168,208
v_and_b32 v23, 2, v23
Mvaa 52,8,148
G 169,209
v_xor_b32 v20, v20, v23
Mvaa 56,16,148
buffer_load_dwordx4 a[60:63], v26, s[8:11], s28 offen offset:0 sc1
G 170,210
Mvav 56,20,208
Ta 0,10,32768
G 171,211
Mvav 88,20,216
Ta 2,10,33792
Ta 4,10,40960
Mvaa 88,16,152
Ta 6,10,41984
v_mul_u32_u24 v22, 80, v22
Mvaa 120,16,156
G 172,212
v_add_lshl_u32 v18, v20, v22, 4
Mvav 120,20,224
buffer_load_dwordx4 v[184:187], v26, s[8:11], s93 offen offset:0 sc1
G 173,213
Mvav 152,20,188
Ta 8,11,32768
Ta 10,11,33792
Mvav 152,16,200
G 174,214
v_subrev_u32 v18, 2048, v18
Mvav 156,24,200
G 175,215
v_lshrrev_b32 v20, 4, v1
Mvav 156,28,188
Ta 12,11,40960
Ta 14,11,41984
Mvav 124,28,224
buffer_load_dwordx4 v[192:195], v27, s[8:11], s28 offen offset:0 sc1
K 168,168,169
Mvaa 124,24,156
v_add_u32 v20, 192, v20
K 169,170,171
Mvaa 92,24,152
K 170,172,173
v_mul_u32_u24 v22, 205, v20
Mvav 92,28,216
K 171,174,175
v_lshrrev_b32 v22, 12, v22
Mvav 60,28,208
Ta 16,12,32768
Ta 18,12,33792
Mvaa 60,24,148
s_waitcnt vmcnt(12) lgkmcnt(6)
buffer_load_dwordx4 a[128:131], v27, s[8:11], s93 offen offset:0 sc1
v_accvgpr_write_b32 a208, v168
Mvav 32,0,196
s_add_u32 s28, s28, 512
v_mul_u32_u24 v23, 20, v22
v_accvgpr_write_b32 a209, v169
Mvav 32,4,228
s_cmp_eq_u32 s28, s4
s_cselect_b32 s93, 10240, 0
v_accvgpr_write_b32 a210, v170
v_sub_u32 v20, v20, v23
Mvav 64,4,236
s_sub_u32 s28, s28, s93
Ta 20,12,40960
Ta 22,12,41984
Mvav 64,0,204
s_add_u32 s93, s28, 20480
v_accvgpr_write_b32 a211, v171
v_lshrrev_b32 v23, 1, v22
Mvav 96,0,212
G 176,80
G 177,81
Mvav 96,4,232
v_and_b32 v23, 2, v23
G 178,82
Mvaa 128,4,32
Ta 24,13,32768
v_xor_b32 v20, v20, v23
Mvav 128,0,220
W 5
Ta 26,13,33792
G 179,83
Mvav 132,8,220
buffer_load_dwordx4 a[132:135], v26, s[8:11], s28 offen offset:0 sc1
v_mul_u32_u24 v22, 80, v22
Mvaa 132,12,32
Ta 28,13,40960
G 180,84
Mvav 100,12,232
Ta 30,13,41984
G 181,85
Mvav 100,8,212
v_add_lshl_u32 v19, v20, v22, 4
G 182,86
Mvav 68,8,204
v_subrev_u32 v19, 3072, v19
G 183,87
Mvav 68,12,236
v_lshrrev_b32 v20, 4, v1
K 176,176,177
Mvav 36,12,228
Ta 0,14,32768
Ta 2,14,33792
Mvav 36,8,196
W 6
K 177,178,179
v_add_u32 v20, 256, v20
Mvav 40,16,196
buffer_load_dwordx4 a[136:139], v26, s[8:11], s93 offen offset:0 sc1
K 178,180,181
Mvav 40,20,228
v_mul_u32_u24 v22, 205, v20
K 179,182,183
Mvav 72,20,236
Ta 4,14,40960
Ta 6,14,41984
Mvav 72,16,204
v_accvgpr_write_b32 a212, v176
v_lshrrev_b32 v22, 12, v22
Mvav 104,16,212
v_accvgpr_write_b32 a213, v177
v_mul_u32_u24 v23, 20, v22
Mvav 104,20,232
v_accvgpr_write_b32 a214, v178
v_sub_u32 v20, v20, v23
Mvaa 136,20,32
Ta 8,15,32768
v_accvgpr_write_b32 a215, v179
Mvav 136,16,220
W 5
Ta 10,15,33792
buffer_load_dwordx4 a[140:143], v27, s[8:11], s28 offen offset:0 sc1
Mvav 140,24,220
G 168,216
v_lshrrev_b32 v23, 1, v22
Mvaa 140,28,32
G 169,217
v_and_b32 v23, 2, v23
Mvav 108,28,232
Ta 12,15,40960
Ta 14,15,41984
Mvav 108,24,212
G 170,218
v_xor_b32 v20, v20, v23
Mvav 76,24,204
G 171,219
G 172,220
Mvav 76,28,236
v_mul_u32_u24 v22, 80, v22
G 173,221
Mvav 44,28,228
Ta 16,24,32768
Ta 18,24,33792
Mvav 44,24,196
W 6
v_add_lshl_u32 v20, v20, v22, 4
G 174,222
Mvav 48,0,196
buffer_load_dwordx4 a[144:147], v27, s[8:11], s93 offen offset:0 sc1
G 175,223
Mvav 48,4,228
s_add_u32 s28, s28, 512
Ta 20,24,40960
v_lshrrev_b32 v22, 6, v1
Mvav 80,4,236
s_cmp_eq_u32 s28, s4
s_cselect_b32 s93, 10240, 0
Ta 22,24,41984
K 168,168,169
Mvav 80,0,204
s_sub_u32 s28, s28, s93
v_mul_u32_u24 v22, 320, v22
K 169,170,171
Mvav 112,0,212
s_add_u32 s93, s28, 20480
v_bfe_u32 v23, v1, 4, 2
K 170,172,173
Mvav 112,4,232
K 171,174,175
v_lshl_add_u32 v22, v23, 3, v22
Mvaa 144,4,32
Ta 24,25,32768
Ta 26,25,33792
Mvav 144,0,220
W 6
v_accvgpr_write_b32 a216, v168
v_bfe_u32 v23, v1, 8, 1
Mvav 148,8,220
buffer_load_dwordx4 v[200:203], v26, s[8:11], s28 offen offset:0 sc1
v_accvgpr_write_b32 a217, v169
Mvaa 148,12,32
v_lshlrev_b32 v23, 5, v23
v_accvgpr_write_b32 a218, v170
Mvav 116,12,232
Ta 28,25,40960
Ta 30,25,41984
Mvav 116,8,212
v_accvgpr_write_b32 a219, v171
v_add_u32 v21, v22, v23
Mvav 84,8,204
G 176,88
v_sub_u32 v22, v22, v23
Mvav 84,12,236
s_mul_i32 s94, s59, 655360
G 177,89
G 178,90
Mvav 52,12,228
s_mul_i32 s95, s58, 320
Ta 0,10,49152
Ta 2,10,50176
Mvav 52,8,196
W 6
buffer_load_dwordx4 v[208:211], v26, s[8:11], s93 offen offset:0 sc1
G 179,91
Mvav 56,16,196
s_add_u32 s94, s94, s95
Ta 4,10,57344
G 180,92
Mvav 56,20,228
Ta 6,10,58368
G 181,93
Mvav 88,20,236
s_add_u32 s40, s26, s94
s_addc_u32 s41, s27, 0
G 182,94
Mvav 88,16,204
s_and_b32 s41, s41, 0xffff
G 183,95
K 176,176,177
Mvav 120,16,212
s_mov_b32 s42, 5242880
Ta 8,11,49152
Ta 10,11,50176
Mvav 120,20,232
s_mov_b32 s43, 0x20000
buffer_load_dwordx4 v[216:219], v27, s[8:11], s28 offen offset:0 sc1
K 177,178,179
Mvaa 152,20,32
s_sub_u32 s42, s42, s94
K 178,180,181
K 179,182,183
Mvav 152,16,220
W 6
Ta 12,11,57344
Ta 14,11,58368
Mvav 156,24,220
s_mov_b64 s[44:45], s[8:9]
v_accvgpr_write_b32 a220, v176
v_accvgpr_write_b32 a221, v177
Mvaa 156,28,32
s_mov_b32 s47, 0x20000
v_accvgpr_write_b32 a222, v178
v_accvgpr_write_b32 a223, v179
Mvav 124,28,232
s_and_b32 s48, s14, 1
Ta 16,12,49152
Ta 18,12,50176
Mvav 124,24,212
buffer_load_dwordx4 v[224:227], v27, s[8:11], s93 offen offset:0 sc1
G 168,224
Mvav 92,24,204
s_mul_i32 s48, s48, 5120
Ta 20,12,57344
G 169,225
Mvav 92,28,236
s_lshr_b32 s51, s14, 1
Ta 22,12,58368
G 170,226
Mvav 60,28,228
G 171,227
G 172,228
Mvav 60,24,196
s_waitcnt vmcnt(13) lgkmcnt(8)
s_and_b32 s94, s14, 1
G 173,229
Mvaa 32,0,36
s_add_u32 s28, s28, 512
Ta 24,13,49152
Ta 26,13,50176
Mvaa 32,4,52
s_cmp_eq_u32 s28, s4
s_cselect_b32 s93, 10240, 0
s_sub_u32 s28, s28, s93
G 174,230
Mvaa 64,4,56
s_add_u32 s93, s28, 20480
s_lshl_b32 s95, s57, 1
buffer_load_dwordx4 v[188:191], v26, s[8:11], s28 offen offset:0 sc1
Mvaa 64,0,40
s_add_u32 s94, s94, s95
Ta 28,13,57344
G 175,231
Mvaa 96,0,44
s_lshl_b32 s54, s94, 3
Ta 30,13,58368
K 168,168,169
Mvaa 96,4,184
K 169,170,171
K 170,172,173
Mvaa 128,4,188
s_add_u32 s54, s54, s59
K 171,174,175
Mvaa 128,0,48
W 8
Ta 0,14,49152
Ta 2,14,50176
Mvaa 132,8,48
s_lshl_b32 s54, s54, 6
buffer_load_dwordx4 v[196:199], v26, s[8:11], s93 offen offset:0 sc1
v_accvgpr_write_b32 a224, v168
Mvaa 132,12,188
s_xor_b32 s55, s54, 15872
v_accvgpr_write_b32 a225, v169
Mvaa 100,12,184
Ta 4,14,57344
v_accvgpr_write_b32 a226, v170
Mvaa 100,8,44
s_add_u32 s54, s54, 20480
Ta 6,14,58368
v_accvgpr_write_b32 a227, v171
Mvaa 68,8,40
s_and_b32 s81, s14, 1
G 176,96
Mvaa 68,12,56
s_add_u32 s55, s55, 20480
Mvaa 36,12,52
G 177,97
Mvaa 36,8,36
)"
R"(W 8
s_and_b32 s95, s14, 2
G 178,98
Mvaa 40,16,36
s_mul_i32 s91, s81, 17164288
Ta 8,15,49152
Ta 10,15,50176
Mvaa 40,20,52
s_lshl_b32 s94, s94, 2
buffer_load_dwordx4 v[204:207], v27, s[8:11], s28 offen offset:0 sc1
G 179,99
Mvaa 72,20,56
s_mul_i32 s82, s56, 536576
s_add_u32 s94, s94, s95
G 180,100
Mvaa 72,16,40
Ta 12,15,57344
Ta 14,15,58368
Mvaa 104,16,44
s_lshl_b32 s95, s95, 4
G 181,101
G 182,102
Mvaa 104,20,184
s_add_u32 s91, s91, s82
G 183,103
Mvaa 136,20,188
s_add_u32 s55, s55, s95
s_add_u32 s91, s91, 4194304
K 176,176,177
Mvaa 136,16,48
W 8
s_lshl_b32 s49, s94, 17
Ta 16,24,49152
Mvaa 140,24,48
Ta 18,24,50176
K 177,178,179
Mvaa 140,28,188
s_lshl_b32 s95, s59, 14
buffer_load_dwordx4 v[212:215], v27, s[8:11], s93 offen offset:0 sc1
K 178,180,181
Mvaa 108,28,184
s_lshr_b32 s82, s14, 1
Ta 20,24,57344
K 179,182,183
Mvaa 108,24,44
s_add_u32 s49, s49, s95
Ta 22,24,58368
v_accvgpr_write_b32 a228, v176
Mvaa 76,24,40
s_lshl_b32 s83, s82, 14
s_xor_b32 s50, s49, 16252928
Mvaa 76,28,56
v_accvgpr_write_b32 a229, v177
Mvaa 44,28,52
s_add_u32 s91, s91, s83
s_add_u32 s49, s49, 39845888
v_accvgpr_write_b32 a230, v178
Mvaa 44,24,36
W 8
Ta 24,25,49152
v_accvgpr_write_b32 a231, v179
Mvaa 48,0,36
s_add_u32 s50, s50, 39845888
Ta 26,25,50176
G 168,232
Mvaa 48,4,52
s_lshl_b32 s81, s81, 14
Mvaa 80,4,56
s_lshr_b32 s94, s15, 3
Ta 28,25,57344
G 169,233
Mvaa 80,0,40
s_lshl_b32 s82, s82, 16
Ta 30,25,58368
G 170,234
Mvaa 112,0,44
s_lshl_b32 s94, s94, 3
G 171,235
Mvaa 112,4,184
s_add_u32 s94, s94, s14
s_or_b32 s92, s81, s82
G 172,236
Mvaa 144,4,188
Mvaa 144,0,48
W 8
s_lshl_b32 s94, s94, 4
G 173,237
Mvaa 148,8,48
s_xor_b32 s92, s92, 0x10000
Ta 0,2,32768
Ta 2,2,33792
Mvaa 148,12,188
s_add_u32 s94, s94, s59
G 174,238
G 175,239
Mvaa 116,12,184
s_mul_i32 s52, s94, 5120
Ta 4,2,40960
Ta 6,2,41984
Mvaa 116,8,44
K 168,168,169
Mvaa 84,8,40
s_lshl_b32 s95, s59, 12
K 169,170,171
Mvaa 84,12,56
K 170,172,173
Mvaa 52,12,52
s_sub_u32 s52, s52, s95
K 171,174,175
Mvaa 52,8,36
W 8
s_add_u32 s52, s52, 56623104
Ta 8,3,32768
Mvaa 56,16,36
Ta 10,3,33792
v_accvgpr_write_b32 a232, v168
Mvaa 56,20,52
s_lshl_b32 s53, s94, 3
v_accvgpr_write_b32 a233, v169
Mvaa 88,20,56
Ta 12,3,40960
v_accvgpr_write_b32 a234, v170
Mvaa 88,16,40
s_add_u32 s53, s53, 65536
Ta 14,3,41984
v_accvgpr_write_b32 a235, v171
Mvaa 120,16,44
G 176,104
v_bfe_u32 v244, v1, 4, 1
Mvaa 120,20,184
Mvaa 152,20,188
G 177,105
v_mul_u32_u24 v244, 0xaaaa, v244
Mvaa 152,16,48
W 8
G 178,106
v_add_u32 v244, 0x4444, v244
Mvaa 156,24,48
Ta 16,4,32768
Ta 18,4,33792
Mvaa 156,28,188
G 179,107
v_lshl_or_b32 v240, v244, 16, v244
Mvaa 124,28,184
G 180,108
v_mov_b32 v241, 0xff800000
Mvaa 124,24,44
Ta 20,4,40960
Ta 22,4,41984
Mvaa 92,24,40
G 181,109
v_add_u32 v242, 0x10000, v1
Mvaa 92,28,56
G 182,110
v_bfe_u32 v244, v1, 4, 4
Mvaa 60,28,52
G 183,111
v_lshrrev_b32 v245, 8, v1
Mvaa 60,24,36
s_waitcnt vmcnt(8) lgkmcnt(8)
K 176,176,177
v_lshlrev_b32 v245, 2, v245
Mvaa 32,0,60
Ta 24,5,32768
Ta 26,5,33792
Mvaa 32,4,132
K 177,178,179
v_sub_u32 v244, v244, v245
Mvaa 64,4,136
K 178,180,181
v_cmp_le_i32_e64 s[72:73], 0, v244
Mvav 64,0,184
Ta 28,5,40960
K 179,182,183
Mvav 96,0,192
Ta 30,5,41984
v_cmp_le_i32_e64 s[74:75], 1, v244
Mvaa 96,4,140
v_accvgpr_write_b32 a236, v176
v_accvgpr_write_b32 a237, v177
Mvaa 128,4,144
v_cmp_le_i32_e64 s[76:77], 2, v244
v_accvgpr_write_b32 a238, v178
Mvaa 128,0,128
W 8
v_cmp_le_i32_e64 s[78:79], 3, v244
v_accvgpr_write_b32 a239, v179
Mvaa 132,8,128
Ta 0,6,32768
v_add_u32 v243, 4096, v1
Mvaa 132,12,144
Ta 2,6,33792
G 168,240
Mvaa 100,12,140
v_add_u32 v244, 8192, v1
G 169,241
Mvav 100,8,192
Ta 4,6,40960
v_add_u32 v245, 12288, v1
Mvav 68,8,184
s_mov_b32 s18, 0x3f803f80
Ta 6,6,41984
G 170,242
Mvaa 68,12,136
s_mov_b32 s19, 0x3f003f00
G 171,243
G 172,244
Mvaa 36,12,132
v_accvgpr_write_b32 a176, s18
Mvaa 36,8,60
W 8
Ta 8,7,32768
G 173,245
Mvaa 40,16,60
Ta 10,7,33792
v_accvgpr_write_b32 a180, s19
Mvaa 40,20,132
G 174,246
v_mov_b32 v252, v241
Mvaa 72,20,136
G 175,247
v_accvgpr_write_b32 a177, s18
Mvav 72,16,184
Ta 12,7,40960
Ta 14,7,41984
Mvav 104,16,192
K 168,168,169
v_accvgpr_write_b32 a181, s19
Mvaa 104,20,140
K 169,170,171
K 170,172,173
Mvaa 136,20,144
v_mov_b32 v253, v241
K 171,174,175
Mvaa 136,16,128
W 8
Ta 16,8,32768
v_accvgpr_write_b32 a178, s18
Mvaa 140,24,128
Ta 18,8,33792
v_accvgpr_write_b32 a240, v168
Mvaa 140,28,144
v_accvgpr_write_b32 a182, s19
v_accvgpr_write_b32 a241, v169
Mvaa 108,28,140
v_mov_b32 v254, v241
v_accvgpr_write_b32 a242, v170
Mvav 108,24,192
Ta 20,8,40960
v_accvgpr_write_b32 a179, s18
Mvav 76,24,184
Ta 22,8,41984
v_accvgpr_write_b32 a243, v171
Mvaa 76,28,136
v_accvgpr_write_b32 a183, s19
G 176,112
Mvaa 44,28,132
v_mov_b32 v255, v241
G 177,113
Mvaa 44,24,60
W 8
v_accvgpr_write_b32 a160, 0
G 178,114
Mvaa 48,0,60
Ta 24,9,32768
Ta 26,9,33792
Mvaa 48,4,132
v_accvgpr_write_b32 a161, 0
G 179,115
Mvaa 80,4,136
v_accvgpr_write_b32 a162, 0
G 180,116
Mvav 80,0,184
Ta 28,9,40960
Ta 30,9,41984
Mvav 112,0,192
v_accvgpr_write_b32 a163, 0
G 181,117
Mvaa 112,4,140
v_accvgpr_write_b32 a164, 0
G 182,118
Mvaa 144,4,144
G 183,119
v_accvgpr_write_b32 a165, 0
Mvaa 144,0,128
W 8
K 176,176,177
v_accvgpr_write_b32 a166, 0
Mvaa 148,8,128
Ta 0,2,49152
Ta 2,2,50176
Mvaa 148,12,144
K 177,178,179
v_accvgpr_write_b32 a167, 0
Mvaa 116,12,140
K 178,180,181
v_accvgpr_write_b32 a168, 0
Mvav 116,8,192
Ta 4,2,57344
K 179,182,183
Mvav 84,8,184
Ta 6,2,58368
v_accvgpr_write_b32 a169, 0
Mvaa 84,12,136
v_accvgpr_write_b32 a244, v176
v_accvgpr_write_b32 a170, 0
Mvaa 52,12,132
v_accvgpr_write_b32 a245, v177
v_accvgpr_write_b32 a171, 0
Mvaa 52,8,60
W 8
v_accvgpr_write_b32 a246, v178
v_accvgpr_write_b32 a172, 0
Mvaa 56,16,60
Ta 8,3,49152
v_accvgpr_write_b32 a247, v179
Mvaa 56,20,132
Ta 10,3,50176
v_accvgpr_write_b32 a173, 0
Mvaa 88,20,136
G 168,248
v_accvgpr_write_b32 a174, 0
Mvav 88,16,184
Ta 12,3,57344
G 169,249
Mvav 120,16,192
Ta 14,3,58368
v_accvgpr_write_b32 a175, 0
Mvaa 120,20,140
s_mov_b32 s90, 0x5d800000
G 170,250
G 171,251
Mvaa 152,20,144
s_mov_b64 s[88:89], 0
G 172,252
Mvaa 152,16,128
W 8
s_mov_b32 s64, 0
G 173,253
Mvaa 156,24,128
s_and_b32 s18, s14, 1
Ta 16,4,49152
Ta 18,4,50176
Mvaa 156,28,144
G 174,254
G 175,255
Mvaa 124,28,140
s_lshl_b32 s62, s18, 14
Ta 20,4,57344
Ta 22,4,58368
Mvav 124,24,192
s_lshl_b32 s20, s57, 15
K 168,168,169
Mvav 92,24,184
s_sub_u32 s20, 507904, s20
K 169,170,171
Mvaa 92,28,136
K 170,172,173
Mvaa 60,28,132
s_add_u32 s20, s20, s62
K 171,174,175
Mvaa 60,24,60
s_waitcnt vmcnt(0) lgkmcnt(8)
s_lshl_b32 s65, s57, 1
Ta 24,5,49152
Mvav 32,0,200
s_sub_u32 s65, s65, 1
Ta 26,5,50176
v_accvgpr_write_b32 a248, v168
Mvav 32,4,188
v_accvgpr_write_b32 a249, v169
Mvav 64,4,196
s_max_i32 s65, s65, 0
Ta 28,5,57344
v_accvgpr_write_b32 a250, v170
Mvav 64,0,208
s_sub_u32 s66, s60, 1
Ta 30,5,58368
v_accvgpr_write_b32 a251, v171
Mvav 96,0,216
G 176,120
Mvav 96,4,204
s_cmp_lt_u32 s14, 2
s_cselect_b32 s20, s20, 0
s_cselect_b32 s66, s66, 15
s_cselect_b32 s21, 32768, 0
Mvav 128,4,212
s_add_u32 s61, s91, s20
G 177,121
Mvav 128,0,224
W 8
s_add_u32 s62, s62, s21
G 178,122
Mvav 132,8,224
Ta 0,6,49152
Ta 2,6,50176
Mvav 132,12,212
s_sub_u32 s63, 16384, s21
G 179,123
G 180,124
)"
R"(Mvav 100,12,204
s_lshl_b32 s67, s60, 7
Ta 4,6,57344
Ta 6,6,58368
Mvav 100,8,216
s_add_u32 s67, s67, s56
G 181,125
Mvav 68,8,208
G 182,126
Mvav 68,12,196
s_lshl_b32 s67, s67, 12
G 183,127
Mvav 36,12,188
s_add_u32 s67, s67, 39845888
K 176,176,177
Mvav 36,8,200
W 8
s_lshl_b32 s69, s60, 3
Ta 8,7,49152
Mvav 40,16,200
Ta 10,7,50176
K 177,178,179
Mvav 40,20,188
s_add_u32 s69, s69, s59
K 178,180,181
Mvav 72,20,196
s_lshl_b32 s69, s69, 6
Ta 12,7,57344
K 179,182,183
Mvav 72,16,208
s_lshl_b32 s21, s58, 3
Ta 14,7,58368
v_accvgpr_write_b32 a252, v176
Mvav 104,16,216
Mvav 104,20,204
s_add_u32 s69, s69, s21
v_accvgpr_write_b32 a253, v177
Mvav 136,20,212
s_add_u32 s69, s69, 20480
v_accvgpr_write_b32 a254, v178
Mvav 136,16,224
W 8
Ta 16,8,49152
v_accvgpr_write_b32 a255, v179
Mvav 140,24,224
s_mov_b32 s70, 0
Ta 18,8,50176
Mvav 140,28,212
s_mov_b32 s37, 0
Mvav 108,28,204
s_sub_u32 s18, s57, 4
Ta 20,8,57344
Ta 22,8,58368
Mvav 108,24,216
Mvav 76,24,208
s_lshl_b32 s18, s18, 2
Mvav 76,28,196
s_add_u32 s18, s18, s58
Mvav 44,28,188
s_mov_b32 s85, s59
Mvav 44,24,200
W 8
Ta 24,9,49152
Ta 26,9,50176
Mvav 48,0,200
s_mul_i32 s85, s85, 7
Mvav 48,4,188
s_add_u32 s85, s85, s18
Mvav 80,4,196
s_cmp_lt_u32 s18, 7
s_cselect_b32 s85, s85, 99
Ta 28,9,57344
Ta 30,9,58368
Mvav 80,0,208
Mvav 112,0,216
s_cmp_lt_u32 s85, 54
s_cselect_b32 s65, 2, s65
Mvav 112,4,204
s_mov_b32 s68, 0
Mvav 144,4,212
s_mov_b64 s[24:25], -1
Mvav 144,0,224
W 8
Mvav 148,8,224
s_mov_b32 s22, 0x3e0293ee
Mvav 148,12,212
s_mov_b32 s23, 0x3e0293ee
Mvav 116,12,204
Mvav 116,8,216
Mvav 84,8,208
Mvav 84,12,196
Mvav 52,12,188
Mvav 52,8,200
W 4
Mvav 56,16,200
Mvav 56,20,188
Mvav 88,20,196
Mvav 88,16,208
Mvav 120,16,216
W 0
s_barrier
Mvav 120,20,204
Ra 128,242,0
Mvav 152,20,212
Ra 132,242,1024
Mvav 152,16,224
Mvav 156,24,224
Ra 136,242,2048
Mvav 156,28,212
Mvav 124,28,204
Ra 140,242,3072
Mvav 124,24,216
Mvav 92,24,208
Mvav 92,28,196
Mvav 60,28,188
Mvav 60,24,200
.Lago%=:
s_cmp_eq_u32 s60, 1
s_cbranch_scc1 .Lpe_44%=
s_cmp_eq_u32 s60, 0
s_cbranch_scc1 .Lpd_28%=
v_pk_mul_f32 v[32:33], v[32:33], s[22:23]
v_pk_mul_f32 v[34:35], v[34:35], s[22:23]
v_pk_mul_f32 v[36:37], v[36:37], s[22:23]
v_pk_mul_f32 v[38:39], v[38:39], s[22:23]
v_pk_mul_f32 v[64:65], v[64:65], s[22:23]
v_pk_mul_f32 v[66:67], v[66:67], s[22:23]
v_pk_mul_f32 v[68:69], v[68:69], s[22:23]
v_pk_mul_f32 v[70:71], v[70:71], s[22:23]
v_pk_mul_f32 v[96:97], v[96:97], s[22:23]
v_pk_mul_f32 v[98:99], v[98:99], s[22:23]
v_pk_mul_f32 v[100:101], v[100:101], s[22:23]
v_pk_mul_f32 v[102:103], v[102:103], s[22:23]
v_pk_mul_f32 v[128:129], v[128:129], s[22:23]
v_pk_mul_f32 v[130:131], v[130:131], s[22:23]
v_pk_mul_f32 v[132:133], v[132:133], s[22:23]
v_pk_mul_f32 v[134:135], v[134:135], s[22:23]
v_pk_mul_f32 v[40:41], v[40:41], s[22:23]
v_pk_mul_f32 v[42:43], v[42:43], s[22:23]
v_pk_mul_f32 v[44:45], v[44:45], s[22:23]
v_pk_mul_f32 v[46:47], v[46:47], s[22:23]
v_pk_mul_f32 v[72:73], v[72:73], s[22:23]
v_pk_mul_f32 v[74:75], v[74:75], s[22:23]
v_pk_mul_f32 v[76:77], v[76:77], s[22:23]
v_pk_mul_f32 v[78:79], v[78:79], s[22:23]
v_pk_mul_f32 v[104:105], v[104:105], s[22:23]
v_pk_mul_f32 v[106:107], v[106:107], s[22:23]
v_pk_mul_f32 v[108:109], v[108:109], s[22:23]
v_pk_mul_f32 v[110:111], v[110:111], s[22:23]
v_pk_mul_f32 v[136:137], v[136:137], s[22:23]
v_pk_mul_f32 v[138:139], v[138:139], s[22:23]
v_pk_mul_f32 v[140:141], v[140:141], s[22:23]
v_pk_mul_f32 v[142:143], v[142:143], s[22:23]
v_pk_mul_f32 v[48:49], v[48:49], s[22:23]
v_pk_mul_f32 v[50:51], v[50:51], s[22:23]
v_pk_mul_f32 v[52:53], v[52:53], s[22:23]
v_pk_mul_f32 v[54:55], v[54:55], s[22:23]
v_pk_mul_f32 v[80:81], v[80:81], s[22:23]
v_pk_mul_f32 v[82:83], v[82:83], s[22:23]
v_pk_mul_f32 v[84:85], v[84:85], s[22:23]
v_pk_mul_f32 v[86:87], v[86:87], s[22:23]
v_pk_mul_f32 v[112:113], v[112:113], s[22:23]
v_pk_mul_f32 v[114:115], v[114:115], s[22:23]
v_pk_mul_f32 v[116:117], v[116:117], s[22:23]
v_pk_mul_f32 v[118:119], v[118:119], s[22:23]
v_pk_mul_f32 v[144:145], v[144:145], s[22:23]
v_pk_mul_f32 v[146:147], v[146:147], s[22:23]
v_pk_mul_f32 v[148:149], v[148:149], s[22:23]
v_pk_mul_f32 v[150:151], v[150:151], s[22:23]
v_pk_mul_f32 v[56:57], v[56:57], s[22:23]
v_pk_mul_f32 v[58:59], v[58:59], s[22:23]
v_pk_mul_f32 v[60:61], v[60:61], s[22:23]
v_pk_mul_f32 v[62:63], v[62:63], s[22:23]
v_pk_mul_f32 v[88:89], v[88:89], s[22:23]
v_pk_mul_f32 v[90:91], v[90:91], s[22:23]
v_pk_mul_f32 v[92:93], v[92:93], s[22:23]
v_pk_mul_f32 v[94:95], v[94:95], s[22:23]
v_pk_mul_f32 v[120:121], v[120:121], s[22:23]
v_pk_mul_f32 v[122:123], v[122:123], s[22:23]
v_pk_mul_f32 v[124:125], v[124:125], s[22:23]
v_pk_mul_f32 v[126:127], v[126:127], s[22:23]
v_pk_mul_f32 v[152:153], v[152:153], s[22:23]
v_pk_mul_f32 v[154:155], v[154:155], s[22:23]
v_pk_mul_f32 v[156:157], v[156:157], s[22:23]
v_pk_mul_f32 v[158:159], v[158:159], s[22:23]
K 32,32,33
K 33,34,35
K 34,36,37
K 35,38,39
K 40,40,41
K 41,42,43
K 42,44,45
K 43,46,47
W 0
Zvav 196,128,32
s_add_u32 s36, s29, 65536
Ra 144,242,4096
Ra 148,242,5120
Ra 152,242,6144
Ra 156,242,7168
Mvav 196,132,40
K 64,64,65
K 65,66,67
K 66,68,69
K 67,70,71
K 72,72,73
K 73,74,75
K 74,76,77
K 75,78,79
s_nop 1
Zvav 204,132,72
buffer_store_dwordx4 a[192:195], v1, s[8:11], s36 offen offset:0 sc1
K 96,96,97
K 97,98,99
K 98,100,101
Mvav 204,128,64
K 99,102,103
K 104,104,105
K 105,106,107
K 106,108,109
K 107,110,111
Zvav 212,128,96
buffer_store_dwordx4 a[200:203], v1, s[8:11], s36 offen offset:1024 sc1
K 128,128,129
K 129,130,131
K 130,132,133
Mvav 212,132,104
K 131,134,135
K 136,136,137
K 137,138,139
K 138,140,141
K 139,142,143
s_nop 1
Zvav 220,132,136
K 48,48,49
K 49,50,51
K 50,52,53
K 51,54,55
Mvav 220,128,128
K 56,56,57
K 57,58,59
K 58,60,61
K 59,62,63
Mvav 196,136,48
buffer_store_dwordx4 a[208:211], v1, s[8:11], s36 offen offset:2048 sc1
Ra 128,242,8192
Ra 132,242,9216
K 80,80,81
Mvav 196,140,56
K 81,82,83
K 82,84,85
K 83,86,87
K 88,88,89
K 89,90,91
K 90,92,93
K 91,94,95
s_nop 1
Mvav 204,140,88
buffer_store_dwordx4 a[216:219], v1, s[8:11], s36 offen offset:3072 sc1
s_add_u32 s36, s29, 69632
K 112,112,113
K 113,114,115
K 114,116,117
Mvav 204,136,80
K 115,118,119
K 120,120,121
K 121,122,123
K 122,124,125
K 123,126,127
Mvav 212,136,112
K 144,144,145
K 145,146,147
K 146,148,149
K 147,150,151
Mvav 212,140,120
K 152,152,153
K 153,154,155
K 154,156,157
K 155,158,159
s_nop 1
Mvav 220,140,152
buffer_store_dwordx4 a[224:227], v1, s[8:11], s36 offen offset:0 sc1
v_accvgpr_write_b32 a0, v32
v_accvgpr_write_b32 a1, v33
v_accvgpr_write_b32 a2, v34
Mvav 220,136,144
W 2
Ra 136,242,10240
v_accvgpr_write_b32 a3, v35
v_accvgpr_write_b32 a4, v40
Zvav 36,144,32
Ra 140,242,11264
buffer_store_dwordx4 a[232:235], v1, s[8:11], s36 offen offset:1024 sc1
v_accvgpr_write_b32 a5, v41
Mvav 36,148,40
v_accvgpr_write_b32 a6, v42
v_accvgpr_write_b32 a7, v43
v_max3_f32 v4, v196, v197, v198
v_max3_f32 v4, v4, v199, v252
Zvav 68,148,72
v_max3_f32 v5, v204, v205, v206
v_max3_f32 v5, v5, v207, v253
v_max3_f32 v6, v212, v213, v214
v_max3_f32 v6, v6, v215, v254
Mvav 68,144,64
v_max3_f32 v7, v220, v221, v222
v_max3_f32 v7, v7, v223, v255
v_mov_b32 v8, v4
v_mov_b32 v9, v5
Zvav 100,144,96
v_mov_b32 v10, v6
v_mov_b32 v11, v7
v_permlane32_swap_b32 v4, v8
v_accvgpr_write_b32 a8, v48
Mvav 100,148,104
v_permlane32_swap_b32 v5, v9
v_permlane32_swap_b32 v6, v10
v_permlane32_swap_b32 v7, v11
v_max_f32 v4, v4, v8
Zvav 132,148,136
v_max_f32 v5, v5, v9
v_max_f32 v6, v6, v10
v_max_f32 v7, v7, v11
v_mov_b32 v8, v4
Mvav 132,144,128
v_mov_b32 v9, v5
v_mov_b32 v10, v6
v_mov_b32 v11, v7
v_accvgpr_write_b32 a9, v49
Mvav 36,152,48
v_permlane16_swap_b32 v4, v8
v_permlane16_swap_b32 v5, v9
v_permlane16_swap_b32 v6, v10
v_permlane16_swap_b32 v7, v11
Mvav 36,156,56
v_max_f32 v4, v4, v8
v_max_f32 v5, v5, v9
v_max_f32 v6, v6, v10
v_max_f32 v7, v7, v11
Mvav 68,156,88
v_sub_f32 v4, 0, v4
v_sub_f32 v5, 0, v5
v_sub_f32 v6, 0, v6
v_sub_f32 v7, 0, v7
Mvav 68,152,80
v_mov_b32 v224, v4
v_mov_b32 v225, v4
v_mov_b32 v226, v4
v_mov_b32 v227, v4
Mvav 100,152,112
v_mov_b32 v228, v5
v_mov_b32 v229, v5
v_mov_b32 v230, v5
v_mov_b32 v231, v5
Mvav 100,156,120
v_mov_b32 v232, v6
v_mov_b32 v233, v6
v_mov_b32 v234, v6
v_mov_b32 v235, v6
Mvav 132,156,152
v_mov_b32 v236, v7
v_mov_b32 v237, v7
v_mov_b32 v238, v7
v_mov_b32 v239, v7
Mvav 132,152,144
W 0
buffer_store_dwordx4 a[240:243], v1, s[8:11], s36 offen offset:2048 sc1
v_accvgpr_write_b32 a10, v50
v_accvgpr_write_b32 a11, v51
v_accvgpr_write_b32 a12, v56
Cvav 160,128,32,224
s_and_b32 s80, s14, 1
v_accvgpr_write_b32 a13, v57
v_accvgpr_write_b32 a14, v58
v_accvgpr_write_b32 a15, v59
v_accvgpr_write_b32 a16, v64
Mvav 160,132,40
buffer_store_dwordx4 a[248:251], v1, s[8:11], s36 offen offset:3072 sc1
s_add_u32 s36, s29, 393216
Ra 144,242,12288
Ra 148,242,13312
v_accvgpr_write_b32 a17, v65
Cvav 164,132,72,228
s_lshl_b32 s80, s80, 14
v_accvgpr_write_b32 a18, v66
v_accvgpr_write_b32 a19, v67
v_accvgpr_write_b32 a20, v72
v_accvgpr_write_b32 a21, v73
Mvav 164,128,64
s_add_u32 s80, s80, 98304
buffer_store_dwordx4 a[196:199], v1, s[8:11], s36 offen offset:0 sc1
v_accvgpr_write_b32 a22, v74
v_accvgpr_write_b32 a23, v75
v_accvgpr_write_b32 a24, v80
Cvav 168,128,96,232
v_accvgpr_write_b32 a25, v81
v_accvgpr_write_b32 a26, v82
A 196,196,224
v_accvgpr_write_b32 a27, v83
Mvav 168,132,104
)"
R"(buffer_store_dwordx4 a[204:207], v1, s[8:11], s36 offen offset:1024 sc1
A 197,197,224
A 198,198,224
v_accvgpr_write_b32 a28, v88
Cvav 172,132,136,236
Ra 152,242,14336
Ra 156,242,15360
A 199,199,224
v_accvgpr_write_b32 a29, v89
Mvav 172,128,128
buffer_store_dwordx4 a[212:215], v1, s[8:11], s36 offen offset:2048 sc1
X 196
v_accvgpr_write_b32 a30, v90
Mvav 160,136,48
X 197
v_accvgpr_write_b32 a31, v91
v_accvgpr_write_b32 a32, v96
Mvav 160,140,56
X 198
K 192,196,197
v_accvgpr_write_b32 a33, v97
Mvav 164,140,88
X 199
A 204,204,228
v_accvgpr_write_b32 a34, v98
Mvav 164,136,80
buffer_store_dwordx4 a[220:223], v1, s[8:11], s36 offen offset:3072 sc1
s_add_u32 s36, s29, 397312
K 193,198,199
v_accvgpr_write_b32 a35, v99
A 205,205,228
Mvav 168,136,112
A 206,206,228
v_accvgpr_write_b32 a36, v104
v_add_u32 v8, s80, v1
A 207,207,228
Mvav 168,140,120
X 204
v_accvgpr_write_b32 a37, v105
v_accvgpr_write_b32 a38, v106
Mvav 172,140,152
X 205
X 206
Mvav 172,136,144
W 0
buffer_store_dwordx4 a[228:231], v1, s[8:11], s36 offen offset:0 sc1
v_accvgpr_write_b32 a39, v107
v_accvgpr_write_b32 a40, v112
K 200,204,205
Cvav 176,144,32,224
Ra 128,242,24576
X 207
v_accvgpr_write_b32 a41, v113
Mvav 176,148,40
Ra 132,242,25600
A 212,212,232
v_accvgpr_write_b32 a42, v114
K 201,206,207
Cvav 180,148,72,228
A 213,213,232
v_accvgpr_write_b32 a43, v115
A 214,214,232
A 215,215,232
Mvav 180,144,64
buffer_store_dwordx4 a[236:239], v1, s[8:11], s36 offen offset:1024 sc1
v_accvgpr_write_b32 a44, v120
X 212
Cvav 184,144,96,232
v_accvgpr_write_b32 a45, v121
X 213
v_accvgpr_write_b32 a46, v122
Mvav 184,148,104
X 214
v_accvgpr_write_b32 a47, v123
K 208,212,213
Cvav 188,148,136,236
buffer_store_dwordx4 a[244:247], v1, s[8:11], s36 offen offset:2048 sc1
v_accvgpr_write_b32 a48, v128
X 215
Mvav 188,144,128
v_accvgpr_write_b32 a49, v129
A 220,220,236
v_accvgpr_write_b32 a50, v130
K 209,214,215
Mvav 176,152,48
A 221,221,236
v_accvgpr_write_b32 a51, v131
A 222,222,236
A 223,223,236
Mvav 176,156,56
Ra 136,242,26624
v_accvgpr_write_b32 a52, v136
X 220
Mvav 180,156,88
Ra 140,242,27648
v_accvgpr_write_b32 a53, v137
X 221
Mvav 180,152,80
buffer_store_dwordx4 a[252:255], v1, s[8:11], s36 offen offset:3072 sc1
v_accvgpr_write_b32 a54, v138
X 222
Mvav 184,152,112
v_accvgpr_write_b32 a55, v139
v_accvgpr_write_b32 a56, v144
v_accvgpr_write_b32 a57, v145
v_accvgpr_write_b32 a58, v146
v_accvgpr_write_b32 a59, v147
v_accvgpr_write_b32 a60, v152
v_accvgpr_write_b32 a61, v153
v_accvgpr_write_b32 a62, v154
v_accvgpr_write_b32 a63, v155
Mvav 184,156,120
s_barrier
K 216,220,221
X 223
A 36,36,224
Mvav 188,156,152
K 217,222,223
A 37,37,224
A 38,38,224
A 39,39,224
X 36
X 37
X 38
K 194,36,37
X 39
A 68,68,228
K 195,38,39
A 69,69,228
A 70,70,228
A 71,71,228
X 68
X 69
X 70
K 202,68,69
X 71
A 100,100,232
K 203,70,71
A 101,101,232
A 102,102,232
A 103,103,232
X 100
X 101
X 102
K 210,100,101
X 103
A 132,132,236
K 211,102,103
A 133,133,236
A 134,134,236
A 135,135,236
X 132
X 133
X 134
K 218,132,133
X 135
s_nop 0
K 219,134,135
Mvav 188,152,144
W 0
Ra 144,242,28672
Ra 148,242,29696
Ra 152,242,30720
Zvav 32,128,192
Ra 156,242,31744
Zvav 64,128,200
v_xor_b32 v242, 0x10000, v242
Zvav 96,128,208
s_and_b32 s80, s14, 1
s_xor_b32 s62, s62, 0x10000
Zvav 128,128,216
s_lshl_b32 s80, s80, 14
s_add_u32 s80, s80, 131072
v_add_u32 v23, 61440, v21
Zvav 132,132,216
v_add_u32 v12, s80, v1
Ra 64,12,8192
Ra 68,12,9216
v_add_u32 v24, 61440, v22
Zvav 100,132,208
Ra 72,12,10240
Ra 76,12,11264
Ra 80,12,12288
v_add_u32 v25, 122880, v21
Zvav 68,132,200
Ra 84,12,13312
Ra 88,12,14336
Ra 92,12,15360
v_add_u32 v26, 122880, v22
Zvav 36,132,192
Ra 96,12,0
Ra 100,12,1024
Ra 104,12,2048
Zvav 40,136,192
Ra 108,12,3072
Ra 112,12,4096
X 172
Zvav 72,136,200
Ra 116,12,5120
Ra 120,12,6144
X 173
Zvav 104,136,208
Ra 124,12,7168
X 174
K 220,172,173
Zvav 136,136,216
X 175
Zvav 140,140,216
X 168
K 221,174,175
Zvav 108,140,208
X 169
X 170
Zvav 76,140,200
s_waitcnt vmcnt(0) lgkmcnt(0)
s_barrier
ds_read_b128 v[248:251], v8
K 212,168,169
Zvav 44,140,192
s_add_u32 s80, s71, 128
v_mov_b64 v[28:29], s[12:13]
s_mov_b64 exec, 1
buffer_store_dwordx2 v[28:29], off, s[8:11], s80 sc1
s_mov_b64 exec, -1
X 171
X 164
Zvav 48,144,192
Ra 128,242,0
K 213,170,171
X 165
Zvav 80,144,200
X 166
Zvav 112,144,208
Ra 132,242,1024
K 204,164,165
X 167
Zvav 144,144,216
X 160
Zvav 148,148,216
K 205,166,167
X 161
Zvav 116,148,208
Ra 136,242,2048
X 162
Zvav 84,148,200
W 3
K 196,160,161
X 163
Zvav 52,148,192
X 176
K 197,162,163
Zvav 56,152,192
Ra 140,242,3072
X 177
Zvav 88,152,200
X 178
K 198,176,177
Zvav 120,152,208
X 179
X 180
Zvav 152,152,216
K 199,178,179
Zvav 156,156,216
X 181
X 182
Zvav 124,156,208
K 206,180,181
X 183
Zvav 92,156,200
K 207,182,183
Zvav 60,156,192
.Ll0t_42%=:
s_cmp_eq_u32 s64, s65
s_cbranch_scc1 .Ll1t_31%=
.Ll0_29%=:
W 0
Cvaa 160,128,0,224
Ra 144,242,4096
Ra 148,242,5120
Mvaa 160,132,4
s_xor_b32 s62, s62, 0x10000
Ra 152,242,6144
Cvaa 164,132,20,228
X 184
Mvaa 164,128,16
X 185
Cvaa 168,128,32,232
X 186
Mvaa 168,132,36
Ra 156,242,7168
K 214,184,185
Cvaa 172,132,52,236
X 187
Mvaa 172,128,48
X 188
Mvaa 160,136,8
Ra 128,242,16384
Mvaa 160,140,12
K 215,186,187
Mvaa 164,140,28
X 189
Mvaa 164,136,24
X 190
Mvaa 168,136,40
Ra 132,242,17408
Mvaa 168,140,44
K 222,188,189
Mvaa 172,140,60
X 191
Mvaa 172,136,56
K 223,190,191
v_smfmac_f32_16x16x64_bf16 a[160:163], a[176:179], v[192:199], v240
Ra 136,242,18432
v_smfmac_f32_16x16x64_bf16 a[164:167], a[176:179], v[200:207], v240
X 160
v_smfmac_f32_16x16x64_bf16 a[168:171], a[176:179], v[208:215], v240
X 161
v_smfmac_f32_16x16x64_bf16 a[172:175], a[176:179], v[216:223], v240
W 3
X 162
Cvaa 176,144,0,224
Ra 140,242,19456
Mvaa 176,148,4
K 192,160,161
Cvaa 180,148,20,228
X 163
Mvaa 180,144,16
X 164
Cvaa 184,144,32,232
Mvaa 184,148,36
K 193,162,163
Cvaa 188,148,52,236
X 165
Mvaa 188,144,48
X 166
Mvaa 176,152,8
Ra 144,242,20480
Ra 148,242,21504
Mvaa 176,156,12
K 200,164,165
Mvaa 180,156,28
X 167
Mvaa 180,152,24
X 168
Mvaa 184,152,40
Mvaa 184,156,44
K 201,166,167
Mvaa 188,156,60
X 169
Mvaa 188,152,56
W 2
Ra 152,242,22528
Mvav 32,128,196
X 170
Mvav 64,128,204
Ra 156,242,23552
K 208,168,169
Mvav 96,128,212
X 171
Mvav 128,128,220
Mvav 132,132,220
X 172
Mvav 100,132,212
s_add_u32 s64, s64, 1
Ra 128,242,8192
K 209,170,171
Mvav 68,132,204
X 173
Mvav 36,132,196
Mvav 40,136,196
X 174
Mvav 72,136,204
Ra 132,242,9216
K 216,172,173
Mvav 104,136,212
X 175
Mvav 136,136,220
Mvav 140,140,220
X 176
Mvav 108,140,212
Ra 136,242,10240
K 217,174,175
Mvav 76,140,204
X 177
Mvav 44,140,196
W 3
Mvav 48,144,196
X 178
Mvav 80,144,204
Ra 140,242,11264
Mvav 112,144,212
K 194,176,177
Mvav 144,144,220
X 179
Mvav 148,148,220
X 180
Mvav 116,148,212
Ra 144,242,12288
Mvav 84,148,204
K 195,178,179
)"
R"(Mvav 52,148,196
X 181
Mvav 56,152,196
X 182
Mvav 88,152,204
Ra 148,242,13312
Mvav 120,152,212
K 202,180,181
Mvav 152,152,220
X 183
Mvav 156,156,220
X 184
Mvav 124,156,212
Ra 152,242,14336
Mvav 92,156,204
K 203,182,183
Mvav 60,156,196
W 3
X 185
Cvaa 160,128,0,224
X 186
Mvaa 160,132,4
Ra 156,242,15360
Cvaa 164,132,20,228
K 210,184,185
Mvaa 164,128,16
X 187
Cvaa 168,128,32,232
X 188
Mvaa 168,132,36
Cvaa 172,132,52,236
K 211,186,187
Mvaa 172,128,48
X 189
Mvaa 160,136,8
Ra 128,242,24576
Ra 132,242,25600
Mvaa 160,140,12
X 190
Mvaa 164,140,28
K 218,188,189
Mvaa 164,136,24
X 191
Mvaa 168,136,40
Mvaa 168,140,44
Mvaa 172,140,60
X 160
Mvaa 172,136,56
W 2
Ra 136,242,26624
K 219,190,191
Cvaa 176,144,0,224
X 161
Mvaa 176,148,4
Ra 140,242,27648
Cvaa 180,148,20,228
X 162
Mvaa 180,144,16
K 196,160,161
Cvaa 184,144,32,232
X 163
Mvaa 184,148,36
X 164
Cvaa 188,148,52,236
K 197,162,163
Mvaa 188,144,48
X 165
Mvaa 176,152,8
Ra 144,242,28672
Ra 148,242,29696
Mvaa 176,156,12
X 166
Mvaa 180,156,28
K 204,164,165
Mvaa 180,152,24
X 167
Mvaa 184,152,40
Mvaa 184,156,44
X 168
Mvaa 188,156,60
Mvaa 188,152,56
W 2
Ra 152,242,30720
K 205,166,167
Mvav 32,128,192
X 169
Mvav 64,128,200
Ra 156,242,31744
v_xor_b32 v242, 0x10000, v242
Mvav 96,128,208
X 170
Mvav 128,128,216
K 212,168,169
Mvav 132,132,216
X 171
Mvav 100,132,208
X 172
Mvav 68,132,200
Mvav 36,132,192
K 213,170,171
Mvav 40,136,192
X 173
Mvav 72,136,200
X 174
Mvav 104,136,208
Mvav 136,136,216
K 220,172,173
Mvav 140,140,216
X 175
Mvav 108,140,208
X 176
Mvav 76,140,200
W 0
s_barrier
Mvav 44,140,192
Ra 128,242,0
K 221,174,175
Mvav 48,144,192
X 177
Mvav 80,144,200
X 178
Mvav 112,144,208
Ra 132,242,1024
Mvav 144,144,216
K 198,176,177
Mvav 148,148,216
X 179
Mvav 116,148,208
X 180
Mvav 84,148,200
Ra 136,242,2048
Mvav 52,148,192
K 199,178,179
Mvav 56,152,192
X 181
Mvav 88,152,200
Ra 140,242,3072
Mvav 120,152,208
X 182
Mvav 152,152,216
K 206,180,181
Mvav 156,156,216
X 183
Mvav 124,156,208
Mvav 92,156,200
K 207,182,183
Mvav 60,156,192
s_cmp_lg_u32 s64, s65
s_cbranch_scc1 .Ll0_29%=
s_cmp_lt_u32 s85, 54
s_cbranch_scc1 .Lmoe_40%=
.Ll1t_31%=:
s_cmp_eq_u32 s64, s66
s_cbranch_scc1 .Lsp1_32%=
.Ll1_30%=:
W 0
Cvaa 160,128,0,224
Ra 144,242,4096
Ra 148,242,5120
Mvaa 160,132,4
s_add_u32 m0, s62, 0
Ra 152,242,6144
Cvaa 164,132,20,228
X 184
Mvaa 164,128,16
X 185
Cvaa 168,128,32,232
X 186
Mvaa 168,132,36
Ra 156,242,7168
K 214,184,185
Cvaa 172,132,52,236
Dc 1,8,61,0
Mvaa 172,128,48
X 187
Mvaa 160,136,8
X 188
Mvaa 160,140,12
Ra 128,242,16384
K 215,186,187
Mvaa 164,140,28
Dc 1,8,61,1024
Mvaa 164,136,24
X 189
Mvaa 168,136,40
X 190
Mvaa 168,140,44
Ra 132,242,17408
K 222,188,189
Mvaa 172,140,60
Dc 1,8,61,2048
Mvaa 172,136,56
X 191
v_smfmac_f32_16x16x64_bf16 a[160:163], a[176:179], v[192:199], v240
Ra 136,242,18432
K 223,190,191
v_smfmac_f32_16x16x64_bf16 a[164:167], a[176:179], v[200:207], v240
X 160
v_smfmac_f32_16x16x64_bf16 a[168:171], a[176:179], v[208:215], v240
Dc 1,8,61,3072
v_smfmac_f32_16x16x64_bf16 a[172:175], a[176:179], v[216:223], v240
W 3
X 161
Cvaa 176,144,0,224
s_add_u32 m0, s62, 4096
X 162
Mvaa 176,148,4
Ra 140,242,19456
K 192,160,161
Cvaa 180,148,20,228
X 163
Mvaa 180,144,16
Dc 243,8,61,0
Cvaa 184,144,32,232
X 164
Mvaa 184,148,36
K 193,162,163
Cvaa 188,148,52,236
X 165
Mvaa 188,144,48
Dc 243,8,61,1024
Ra 144,242,20480
Mvaa 176,152,8
X 166
Mvaa 176,156,12
Ra 148,242,21504
K 200,164,165
Mvaa 180,156,28
X 167
Mvaa 180,152,24
Dc 243,8,61,2048
Mvaa 184,152,40
X 168
Mvaa 184,156,44
K 201,166,167
Mvaa 188,156,60
X 169
Mvaa 188,152,56
W 2
Dc 243,8,61,3072
Ra 152,242,22528
Mvav 32,128,196
s_add_u32 m0, s62, 8192
X 170
Mvav 64,128,204
Ra 156,242,23552
K 208,168,169
Mvav 96,128,212
X 171
Mvav 128,128,220
Dc 244,8,61,0
Mvav 132,132,220
X 172
Mvav 100,132,212
Ra 128,242,8192
K 209,170,171
Mvav 68,132,204
X 173
Mvav 36,132,196
Dc 244,8,61,1024
Mvav 40,136,196
X 174
Mvav 72,136,204
Ra 132,242,9216
K 216,172,173
Mvav 104,136,212
X 175
Mvav 136,136,220
Dc 244,8,61,2048
Mvav 140,140,220
X 176
Mvav 108,140,212
Ra 136,242,10240
K 217,174,175
Mvav 76,140,204
X 177
Mvav 44,140,196
W 3
Dc 244,8,61,3072
Mvav 48,144,196
X 178
Mvav 80,144,204
s_add_u32 m0, s62, 12288
Ra 140,242,11264
Mvav 112,144,212
K 194,176,177
Mvav 144,144,220
X 179
Mvav 148,148,220
X 180
Mvav 116,148,212
Dc 245,8,61,0
Ra 144,242,12288
Mvav 84,148,204
K 195,178,179
Mvav 52,148,196
X 181
Mvav 56,152,196
X 182
Mvav 88,152,204
Dc 245,8,61,1024
Ra 148,242,13312
Mvav 120,152,212
K 202,180,181
Mvav 152,152,220
X 183
Mvav 156,156,220
X 184
Mvav 124,156,212
Dc 245,8,61,2048
Ra 152,242,14336
Mvav 92,156,204
K 203,182,183
Mvav 60,156,196
W 3
X 185
Cvaa 160,128,0,224
X 186
Mvaa 160,132,4
Dc 245,8,61,3072
Ra 156,242,15360
Cvaa 164,132,20,228
s_add_u32 s61, s61, s63
K 210,184,185
Mvaa 164,128,16
X 187
Cvaa 168,128,32,232
X 188
Mvaa 168,132,36
Cvaa 172,132,52,236
s_xor_b32 s62, s62, 0x10000
K 211,186,187
Mvaa 172,128,48
X 189
Mvaa 160,136,8
Ra 128,242,24576
Ra 132,242,25600
Mvaa 160,140,12
s_add_u32 s64, s64, 1
X 190
Mvaa 164,140,28
K 218,188,189
Mvaa 164,136,24
X 191
Mvaa 168,136,40
Mvaa 168,140,44
X 160
Mvaa 172,140,60
K 219,190,191
Mvaa 172,136,56
W 2
X 161
Cvaa 176,144,0,224
Ra 136,242,26624
Ra 140,242,27648
Mvaa 176,148,4
X 162
Cvaa 180,148,20,228
K 196,160,161
Mvaa 180,144,16
X 163
Cvaa 184,144,32,232
Mvaa 184,148,36
X 164
Cvaa 188,148,52,236
K 197,162,163
Mvaa 188,144,48
X 165
Mvaa 176,152,8
Ra 144,242,28672
Ra 148,242,29696
Mvaa 176,156,12
X 166
Mvaa 180,156,28
K 204,164,165
Mvaa 180,152,24
X 167
Mvaa 184,152,40
Mvaa 184,156,44
X 168
Mvaa 188,156,60
Mvaa 188,152,56
W 2
Ra 152,242,30720
K 205,166,167
Mvav 32,128,192
X 169
Mvav 64,128,200
Ra 156,242,31744
v_xor_b32 v242, 0x10000, v242
Mvav 96,128,208
X 170
Mvav 128,128,216
K 212,168,169
Mvav 132,132,216
X 171
Mvav 100,132,208
X 172
Mvav 68,132,200
Mvav 36,132,192
)"
R"(K 213,170,171
Mvav 40,136,192
X 173
Mvav 72,136,200
X 174
Mvav 104,136,208
Mvav 136,136,216
K 220,172,173
Mvav 140,140,216
X 175
Mvav 108,140,208
X 176
Mvav 76,140,200
s_waitcnt vmcnt(0) lgkmcnt(0)
s_barrier
Mvav 44,140,192
Ra 128,242,0
K 221,174,175
Mvav 48,144,192
X 177
Mvav 80,144,200
X 178
Mvav 112,144,208
Ra 132,242,1024
Mvav 144,144,216
K 198,176,177
Mvav 148,148,216
X 179
Mvav 116,148,208
X 180
Mvav 84,148,200
Ra 136,242,2048
Mvav 52,148,192
K 199,178,179
Mvav 56,152,192
X 181
Mvav 88,152,200
Ra 140,242,3072
Mvav 120,152,208
X 182
Mvav 152,152,216
K 206,180,181
Mvav 156,156,216
X 183
Mvav 124,156,208
Mvav 92,156,200
K 207,182,183
Mvav 60,156,192
s_cmp_lg_u32 s64, s66
s_cbranch_scc1 .Ll1_30%=
s_cmp_ge_u32 s14, 2
s_cbranch_scc1 .Lf1r_43%=
.Lsp1_32%=:
W 0
Cvaa 160,128,0,224
Ra 144,242,4096
Ra 148,242,5120
Mvaa 160,132,4
s_add_u32 m0, s62, 0
Ra 152,242,6144
Cvaa 164,132,20,228
X 184
Mvaa 164,128,16
X 185
Cvaa 168,128,32,232
X 186
Mvaa 168,132,36
Ra 156,242,7168
K 214,184,185
Cvaa 172,132,52,236
Dc 1,8,61,0
Mvaa 172,128,48
X 187
Mvaa 160,136,8
X 188
Mvaa 160,140,12
Ra 128,242,16384
K 215,186,187
Mvaa 164,140,28
Dc 1,8,61,1024
Ra 132,242,17408
Mvaa 164,136,24
X 189
Mvaa 168,136,40
X 190
Mvaa 168,140,44
Dc 1,8,61,2048
K 222,188,189
Mvaa 172,140,60
X 191
Mvaa 172,136,56
Ra 136,242,18432
K 223,190,191
v_smfmac_f32_16x16x64_bf16 a[160:163], a[176:179], v[192:199], v240
Dc 1,8,61,3072
Ra 140,242,19456
v_smfmac_f32_16x16x64_bf16 a[164:167], a[176:179], v[200:207], v240
s_add_u32 m0, s62, 4096
v_cndmask_b32_e64 v160, v241, v160, s[72:73]
v_cndmask_b32_e64 v161, v241, v161, s[74:75]
v_smfmac_f32_16x16x64_bf16 a[168:171], a[176:179], v[208:215], v240
v_cndmask_b32_e64 v162, v241, v162, s[76:77]
v_cndmask_b32_e64 v163, v241, v163, s[78:79]
v_smfmac_f32_16x16x64_bf16 a[172:175], a[176:179], v[216:223], v240
W 4
X 160
Cvaa 180,144,16,228
X 161
Mvaa 180,148,20
Dc 243,8,61,0
Cvaa 184,148,36,232
X 162
Mvaa 184,144,32
K 192,160,161
Cvaa 188,144,48,236
X 163
Mvaa 188,148,52
Dc 243,8,61,1024
Ra 144,242,20480
Mvaa 180,152,24
X 164
Mvaa 180,156,28
Ra 148,242,21504
K 193,162,163
Mvaa 184,156,44
Dc 243,8,61,2048
Mvaa 184,152,40
X 165
Mvaa 188,152,56
X 166
Mvaa 188,156,60
W 2
Dc 243,8,61,3072
K 200,164,165
Mvav 32,128,196
s_add_u32 m0, s62, 8192
X 167
Mvav 64,128,204
Ra 152,242,22528
Ra 156,242,23552
Mvav 96,128,212
X 168
Mvav 128,128,220
Dc 244,8,61,0
K 201,166,167
Mvav 132,132,220
X 169
Mvav 100,132,212
X 170
Mvav 68,132,204
Ra 128,242,8192
K 208,168,169
Mvav 36,132,196
X 171
Mvav 40,136,196
Dc 244,8,61,1024
Ra 132,242,9216
Mvav 72,136,204
X 172
Mvav 104,136,212
K 209,170,171
Mvav 136,136,220
X 173
Mvav 140,140,220
Dc 244,8,61,2048
Ra 136,242,10240
Mvav 108,140,212
X 174
Mvav 76,140,204
K 216,172,173
Mvav 44,140,196
W 3
Dc 244,8,61,3072
Ra 140,242,11264
Mvav 48,144,196
s_add_u32 m0, s62, 12288
X 175
Mvav 80,144,204
v_mov_b32 v194, 0
K 217,174,175
Mvav 112,144,212
v_mov_b32 v195, 0
v_cndmask_b32_e64 v180, v241, v180, s[72:73]
Mvav 144,144,220
v_cndmask_b32_e64 v181, v241, v181, s[74:75]
v_cndmask_b32_e64 v182, v241, v182, s[76:77]
Mvav 148,148,220
Dc 245,8,61,0
v_cndmask_b32_e64 v183, v241, v183, s[78:79]
Mvav 116,148,212
X 180
Mvav 84,148,204
Ra 144,242,12288
Mvav 52,148,196
X 181
Mvav 56,152,196
Dc 245,8,61,1024
Ra 148,242,13312
Mvav 88,152,204
X 182
Mvav 120,152,212
K 202,180,181
Mvav 152,152,220
X 183
Mvav 156,156,220
Dc 245,8,61,2048
Ra 152,242,14336
Mvav 124,156,212
X 184
Mvav 92,156,204
K 203,182,183
Mvav 60,156,196
W 3
Dc 245,8,61,3072
Ra 156,242,15360
Cvaa 168,128,32,232
s_add_u32 s61, s61, s63
X 185
Mvaa 168,132,36
s_xor_b32 s62, s62, 0x10000
X 186
Cvaa 172,132,52,236
K 210,184,185
Mvaa 172,128,48
s_add_u32 s64, s64, 1
X 187
Mvaa 168,136,40
X 188
Mvaa 168,140,44
Ra 128,242,24576
K 211,186,187
Mvaa 172,140,60
Ra 132,242,25600
Mvaa 172,136,56
Ra 136,242,26624
Ra 140,242,27648
X 189
X 190
K 218,188,189
X 191
v_mov_b32 v196, 0
K 219,190,191
W 4
Cvaa 188,144,48,236
Ra 144,242,28672
Mvaa 188,148,52
v_mov_b32 v197, 0
v_mov_b32 v204, 0
Mvaa 188,152,56
Ra 148,242,29696
v_mov_b32 v205, 0
Mvaa 188,156,60
W 2
Ra 152,242,30720
v_cndmask_b32_e64 v168, v241, v168, s[72:73]
Mvav 32,128,192
v_cndmask_b32_e64 v169, v241, v169, s[74:75]
v_cndmask_b32_e64 v170, v241, v170, s[76:77]
Mvav 64,128,200
Ra 156,242,31744
v_cndmask_b32_e64 v171, v241, v171, s[78:79]
Mvav 96,128,208
v_xor_b32 v242, 0x10000, v242
Mvav 128,128,216
X 168
Mvav 132,132,216
X 169
Mvav 100,132,208
X 170
Mvav 68,132,200
v_mov_b32 v224, v250
K 212,168,169
Mvav 36,132,192
v_mov_b32 v225, v250
v_mov_b32 v226, v250
Mvav 40,136,192
X 171
Mvav 72,136,200
X 172
Mvav 104,136,208
v_mov_b32 v227, v250
K 213,170,171
Mvav 136,136,216
X 173
Mvav 140,140,216
v_mov_b32 v228, v251
v_mov_b32 v229, v251
Mvav 108,140,208
X 174
Mvav 76,140,200
K 220,172,173
v_mov_b32 v230, v251
Mvav 44,140,192
W 0
X 175
Mvav 48,144,192
v_mov_b32 v231, v251
v_mov_b32 v198, 0
Mvav 80,144,200
K 221,174,175
v_mov_b32 v232, v248
Mvav 112,144,208
v_mov_b32 v199, 0
v_mov_b32 v206, 0
Mvav 144,144,216
v_mov_b32 v233, v248
v_mov_b32 v207, 0
Mvav 148,148,216
v_mov_b32 v214, 0
v_mov_b32 v234, v248
Mvav 116,148,208
v_mov_b32 v215, 0
v_cndmask_b32_e64 v188, v241, v188, s[72:73]
Mvav 84,148,200
v_mov_b32 v235, v248
v_cndmask_b32_e64 v189, v241, v189, s[74:75]
Mvav 52,148,192
v_cndmask_b32_e64 v190, v241, v190, s[76:77]
v_mov_b32 v236, v249
Mvav 56,152,192
v_cndmask_b32_e64 v191, v241, v191, s[78:79]
v_mov_b32 v237, v249
Mvav 88,152,200
X 188
Mvav 120,152,208
X 189
Mvav 152,152,216
v_mov_b32 v238, v249
v_mov_b32 v239, v249
Mvav 156,156,216
X 190
Mvav 124,156,208
s_waitcnt vmcnt(0)
s_barrier
Mvav 92,156,200
Ra 128,242,40960
K 222,188,189
Mvav 60,156,192
Ra 132,242,41984
Ra 136,242,43008
Ra 140,242,44032
X 191
s_nop 0
K 223,190,191
s_cmp_eq_u32 s64, 15
s_cbranch_scc1 .Lfs_38%=
.Lsp2_33%=:
v_smfmac_f32_16x16x64_bf16 a[160:163], a[176:179], v[192:199], v240
s_add_u32 m0, s62, 0
Ra 144,242,45056
Ra 148,242,46080
Ra 152,242,47104
Ra 156,242,48128
v_smfmac_f32_16x16x64_bf16 a[164:167], a[176:179], v[200:207], v240
v_smfmac_f32_16x16x64_bf16 a[168:171], a[176:179], v[208:215], v240
v_smfmac_f32_16x16x64_bf16 a[172:175], a[176:179], v[216:223], v240
W 4
Dc 1,8,61,0
Cvaa 168,128,96,232
v_mov_b32 v192, 0
G 12,160
Mvaa 168,132,100
Dc 1,8,61,1024
G 4,161
A 12,12,4
v_cmp_nlt_f32_e64 s[86:87], v12, s90
v_mov_b32 v193, 0
Cvaa 172,132,116,236
v_rcp_f32 v12, v12
s_or_b64 s[88:89], s[88:89], s[86:87]
v_accvgpr_write_b32 a160, 0
v_mov_b32 v200, 0
v_accvgpr_write_b32 a161, 0
v_accvgpr_write_b32 a162, 0
Mvaa 172,128,112
Dc 1,8,61,2048
v_accvgpr_write_b32 a163, 0
G 13,164
v_mov_b32 v201, 0
G 5,165
A 13,13,5
Mvaa 168,136,104
Ra 128,242,16384
Ra 132,242,17408
v_cmp_nlt_f32_e64 s[86:87], v13, s90
v_rcp_f32 v13, v13
s_or_b64 s[88:89], s[88:89], s[86:87]
v_accvgpr_write_b32 a164, 0
Mvaa 168,140,108
Dc 1,8,61,3072
v_accvgpr_write_b32 a165, 0
v_accvgpr_write_b32 a166, 0
v_accvgpr_write_b32 a167, 0
U 32,32,12
U 33,33,12
Mvaa 172,140,124
U 34,34,12
U 35,35,12
U 36,36,12
U 37,37,12
)"
R"(U 38,38,12
U 39,39,12
Mvaa 172,136,120
s_add_u32 m0, s62, 4096
Ra 136,242,18432
Ra 140,242,19456
K 32,32,33
K 33,34,35
K 34,36,37
W 4
Cvaa 188,144,112,236
K 35,38,39
s_add_u32 s93, s67, 0
buffer_store_dwordx4 v[32:35], v1, s[8:11], s93 offen offset:0 sc1
U 40,40,12
U 41,41,12
U 42,42,12
U 43,43,12
Mvaa 188,148,116
Ra 144,242,20480
v_cndmask_b32_e64 v168, v241, v168, s[72:73]
v_cndmask_b32_e64 v169, v241, v169, s[74:75]
v_cndmask_b32_e64 v170, v241, v170, s[76:77]
v_cndmask_b32_e64 v171, v241, v171, s[78:79]
U 44,44,12
Mvaa 188,152,120
X 168
X 169
U 45,45,12
U 46,46,12
Mvaa 188,156,124
W 1
Ra 148,242,21504
Dc 243,8,61,0
U 47,47,12
K 40,40,41
K 41,42,43
K 42,44,45
Mvav 96,128,212
Ra 152,242,22528
Ra 156,242,23552
K 43,46,47
X 170
K 208,168,169
Mvav 128,128,220
buffer_store_dwordx4 v[40:43], v1, s[8:11], s93 offen offset:1024 sc1
U 48,48,12
U 49,49,12
U 50,50,12
U 51,51,12
U 52,52,12
Mvav 132,132,220
U 53,53,12
U 54,54,12
U 55,55,12
K 48,48,49
K 49,50,51
K 50,52,53
Mvav 100,132,212
Dc 243,8,61,1024
Ra 128,242,32768
Ra 132,242,33792
X 171
K 51,54,55
Mvav 104,136,212
X 172
K 209,170,171
X 173
Mvav 136,136,220
buffer_store_dwordx4 v[48:51], v1, s[8:11], s93 offen offset:2048 sc1
U 56,56,12
U 57,57,12
U 58,58,12
U 59,59,12
U 60,60,12
Mvav 140,140,220
U 61,61,12
U 62,62,12
U 63,63,12
K 56,56,57
K 57,58,59
K 58,60,61
Mvav 108,140,212
W 2
Dc 243,8,61,2048
Ra 136,242,34816
Ra 140,242,35840
K 59,62,63
X 174
Mvav 112,144,212
K 216,172,173
X 175
Mvav 144,144,220
buffer_store_dwordx4 v[56:59], v1, s[8:11], s93 offen offset:3072 sc1
Ra 144,242,36864
G 14,168
G 6,169
A 14,14,6
v_cmp_nlt_f32_e64 s[86:87], v14, s90
Mvav 148,148,220
v_rcp_f32 v14, v14
s_or_b64 s[88:89], s[88:89], s[86:87]
v_accvgpr_write_b32 a168, 0
v_accvgpr_write_b32 a169, 0
v_accvgpr_write_b32 a170, 0
v_accvgpr_write_b32 a171, 0
Mvav 116,148,212
Dc 243,8,61,3072
s_add_u32 m0, s62, 8192
G 15,172
G 7,173
A 15,15,7
v_cmp_nlt_f32_e64 s[86:87], v15, s90
v_mov_b32 v194, 0
Mvav 120,152,212
v_rcp_f32 v15, v15
s_or_b64 s[88:89], s[88:89], s[86:87]
v_accvgpr_write_b32 a172, 0
v_accvgpr_write_b32 a173, 0
v_accvgpr_write_b32 a174, 0
v_accvgpr_write_b32 a175, 0
Mvav 152,152,220
Dc 244,8,61,0
U 64,64,13
U 65,65,13
U 66,66,13
U 67,67,13
U 68,68,13
Mvav 156,156,220
U 69,69,13
U 70,70,13
U 71,71,13
K 64,64,65
K 65,66,67
K 66,68,69
Mvav 124,156,212
W 1
Dc 244,8,61,1024
Ra 148,242,37888
K 67,70,71
s_add_u32 s93, s67, 131072
K 217,174,175
v_mov_b32 v195, 0
Cvaa 160,128,64,224
Ra 152,242,38912
Ra 156,242,39936
v_mov_b32 v202, 0
v_mov_b32 v203, 0
v_mov_b32 v210, 0
v_mov_b32 v211, 0
Mvaa 160,132,68
buffer_store_dwordx4 v[64:67], v1, s[8:11], s93 offen offset:0 sc1
U 72,72,13
U 73,73,13
U 74,74,13
U 75,75,13
U 76,76,13
Cvaa 164,132,84,228
U 77,77,13
U 78,78,13
U 79,79,13
K 72,72,73
K 73,74,75
K 74,76,77
Mvaa 164,128,80
Dc 244,8,61,2048
K 75,78,79
v_cndmask_b32_e64 v188, v241, v188, s[72:73]
v_cndmask_b32_e64 v189, v241, v189, s[74:75]
v_cndmask_b32_e64 v190, v241, v190, s[76:77]
v_cndmask_b32_e64 v191, v241, v191, s[78:79]
Cvaa 168,128,96,232
X 188
X 189
Mvaa 168,132,100
buffer_store_dwordx4 v[72:75], v1, s[8:11], s93 offen offset:1024 sc1
U 80,80,13
U 81,81,13
U 82,82,13
U 83,83,13
U 84,84,13
Cvaa 172,132,116,236
U 85,85,13
U 86,86,13
U 87,87,13
K 80,80,81
K 81,82,83
K 82,84,85
Mvaa 172,128,112
Dc 244,8,61,3072
s_add_u32 m0, s62, 12288
Ra 128,242,49152
Ra 132,242,50176
K 83,86,87
X 190
Mvaa 160,136,72
K 218,188,189
X 191
Mvaa 160,140,76
buffer_store_dwordx4 v[80:83], v1, s[8:11], s93 offen offset:2048 sc1
U 88,88,13
U 89,89,13
U 90,90,13
U 91,91,13
U 92,92,13
Mvaa 164,140,92
U 93,93,13
U 94,94,13
U 95,95,13
K 88,88,89
K 89,90,91
K 90,92,93
Mvaa 164,136,88
K 91,94,95
buffer_store_dwordx4 v[88:91], v1, s[8:11], s93 offen offset:3072 sc1
U 96,96,14
U 97,97,14
U 98,98,14
U 99,99,14
Mvaa 168,136,104
U 100,100,14
U 101,101,14
U 102,102,14
U 103,103,14
K 96,96,97
K 97,98,99
Mvaa 168,140,108
Dc 245,8,61,0
K 98,100,101
K 99,102,103
s_add_u32 s93, s67, 262144
v_cndmask_b32_e64 v160, v241, v160, s[72:73]
K 219,190,191
v_cndmask_b32_e64 v161, v241, v161, s[74:75]
Mvaa 172,140,124
v_cndmask_b32_e64 v162, v241, v162, s[76:77]
v_cndmask_b32_e64 v163, v241, v163, s[78:79]
X 160
Mvaa 172,136,120
W 2
Dc 245,8,61,1024
Ra 136,242,51200
Ra 140,242,52224
X 161
Cvaa 180,144,80,228
Mvaa 180,148,84
buffer_store_dwordx4 v[96:99], v1, s[8:11], s93 offen offset:0 sc1
U 104,104,14
U 105,105,14
U 106,106,14
U 107,107,14
U 108,108,14
Cvaa 184,148,100,232
U 109,109,14
U 110,110,14
U 111,111,14
K 104,104,105
K 105,106,107
K 106,108,109
Mvaa 184,144,96
Dc 245,8,61,2048
K 107,110,111
X 162
K 196,160,161
Cvaa 188,144,112,236
Ra 144,242,53248
X 163
Mvaa 188,148,116
buffer_store_dwordx4 v[104:107], v1, s[8:11], s93 offen offset:1024 sc1
U 112,112,14
U 113,113,14
U 114,114,14
U 115,115,14
U 116,116,14
Mvaa 180,152,88
U 117,117,14
U 118,118,14
U 119,119,14
K 112,112,113
K 113,114,115
K 114,116,117
Mvaa 180,156,92
Dc 245,8,61,3072
s_add_u32 s61, s61, s63
s_xor_b32 s62, s62, 0x10000
s_add_u32 s64, s64, 1
K 115,118,119
X 164
Mvaa 184,156,108
Ra 148,242,54272
K 197,162,163
X 165
Mvaa 184,152,104
buffer_store_dwordx4 v[112:115], v1, s[8:11], s93 offen offset:2048 sc1
U 120,120,14
U 121,121,14
U 122,122,14
U 123,123,14
U 124,124,14
Mvaa 188,152,120
U 125,125,14
U 126,126,14
U 127,127,14
K 120,120,121
K 121,122,123
K 122,124,125
Mvaa 188,156,124
W 2
K 123,126,127
buffer_store_dwordx4 v[120:123], v1, s[8:11], s93 offen offset:3072 sc1
U 128,128,15
U 129,129,15
U 130,130,15
U 131,131,15
Zvav 32,128,192
U 132,132,15
U 133,133,15
U 134,134,15
U 135,135,15
K 128,128,129
K 129,130,131
Zvav 64,128,200
K 130,132,133
K 131,134,135
s_add_u32 s93, s67, 393216
buffer_store_dwordx4 v[128:131], v1, s[8:11], s93 offen offset:0 sc1
U 136,136,15
U 137,137,15
U 138,138,15
Zvav 96,128,208
U 139,139,15
U 140,140,15
U 141,141,15
U 142,142,15
U 143,143,15
K 136,136,137
Zvav 128,128,216
K 137,138,139
K 138,140,141
K 139,142,143
buffer_store_dwordx4 v[136:139], v1, s[8:11], s93 offen offset:1024 sc1
U 144,144,15
U 145,145,15
Zvav 132,132,216
U 146,146,15
U 147,147,15
U 148,148,15
U 149,149,15
U 150,150,15
U 151,151,15
Zvav 100,132,208
K 144,144,145
K 145,146,147
K 146,148,149
K 147,150,151
buffer_store_dwordx4 v[144:147], v1, s[8:11], s93 offen offset:2048 sc1
U 152,152,15
Zvav 68,132,200
U 153,153,15
U 154,154,15
U 155,155,15
U 156,156,15
U 157,157,15
U 158,158,15
Zvav 36,132,192
U 159,159,15
K 152,152,153
K 153,154,155
K 154,156,157
K 155,158,159
buffer_store_dwordx4 v[152:155], v1, s[8:11], s93 offen offset:3072 sc1
Zvav 40,136,192
Ra 152,242,55296
X 166
K 204,164,165
X 167
Zvav 72,136,200
Ra 156,242,56320
X 168
v_xor_b32 v242, 0x10000, v242
K 205,166,167
Zvav 104,136,208
X 169
X 170
K 212,168,169
Zvav 136,136,216
X 171
X 172
K 213,170,171
Zvav 140,140,216
X 173
Zvav 108,140,208
X 174
Zvav 76,140,200
s_waitcnt vmcnt(6) lgkmcnt(0)
s_barrier
Zvav 44,140,192
Ra 128,242,40960
K 220,172,173
X 175
)"
R"(Zvav 48,144,192
Zvav 80,144,200
v_mov_b32 v198, 0
Zvav 112,144,208
Ra 132,242,41984
K 221,174,175
Zvav 144,144,216
v_mov_b32 v199, 0
v_cndmask_b32_e64 v180, v241, v180, s[72:73]
Zvav 148,148,216
v_cndmask_b32_e64 v181, v241, v181, s[74:75]
Zvav 116,148,208
Ra 136,242,43008
v_cndmask_b32_e64 v182, v241, v182, s[76:77]
Zvav 84,148,200
v_cndmask_b32_e64 v183, v241, v183, s[78:79]
Zvav 52,148,192
X 180
Zvav 56,152,192
Ra 140,242,44032
X 181
Zvav 88,152,200
Zvav 120,152,208
X 182
Zvav 152,152,216
K 206,180,181
Zvav 156,156,216
X 183
Zvav 124,156,208
Zvav 92,156,200
s_waitcnt vmcnt(0)
s_and_b64 s[86:87], s[88:89], s[24:25]
s_cbranch_scc1 .LL_45%=
v_mov_b64 v[28:29], s[12:13]
s_mov_b64 exec, 1
buffer_store_dwordx2 v[28:29], off, s[8:11], s69 sc1
s_mov_b64 exec, -1
.LL_45%=:
K 207,182,183
Zvav 60,156,192
s_cmp_eq_u32 s64, 15
s_cbranch_scc1 .Lf2_37%=
.Ll2_34%=:
W 0
Cvaa 160,128,64,224
Ra 144,242,45056
Ra 148,242,46080
Mvaa 160,132,68
s_add_u32 m0, s62, 8192
Ra 152,242,47104
Cvaa 164,132,84,228
X 184
Mvaa 164,128,80
X 185
Cvaa 168,128,96,232
X 186
Mvaa 168,132,100
Ra 156,242,48128
K 214,184,185
Cvaa 172,132,116,236
Dc 244,8,61,0
Mvaa 172,128,112
X 187
Mvaa 160,136,72
X 188
Mvaa 160,140,76
Ra 128,242,57344
K 215,186,187
Mvaa 164,140,92
Dc 244,8,61,1024
Mvaa 164,136,88
X 189
Mvaa 168,136,104
X 190
Mvaa 168,140,108
Ra 132,242,58368
K 222,188,189
Mvaa 172,140,124
Dc 244,8,61,2048
Mvaa 172,136,120
X 191
v_smfmac_f32_16x16x64_bf16 a[160:163], a[176:179], v[192:199], v240
Ra 136,242,59392
K 223,190,191
v_smfmac_f32_16x16x64_bf16 a[164:167], a[176:179], v[200:207], v240
X 160
v_smfmac_f32_16x16x64_bf16 a[168:171], a[176:179], v[208:215], v240
Dc 244,8,61,3072
v_smfmac_f32_16x16x64_bf16 a[172:175], a[176:179], v[216:223], v240
W 3
X 161
Cvaa 176,144,64,224
s_add_u32 m0, s62, 12288
X 162
Mvaa 176,148,68
Ra 140,242,60416
K 192,160,161
Cvaa 180,148,84,228
X 163
Mvaa 180,144,80
Dc 245,8,61,0
Cvaa 184,144,96,232
X 164
Mvaa 184,148,100
K 193,162,163
Cvaa 188,148,116,236
X 165
Mvaa 188,144,112
Dc 245,8,61,1024
Ra 144,242,61440
Mvaa 176,152,72
X 166
Mvaa 176,156,76
Ra 148,242,62464
K 200,164,165
Mvaa 180,156,92
X 167
Mvaa 180,152,88
Dc 245,8,61,2048
Mvaa 184,152,104
X 168
Mvaa 184,156,108
K 201,166,167
Mvaa 188,156,124
X 169
Mvaa 188,152,120
W 2
Dc 245,8,61,3072
Ra 152,242,63488
Mvav 32,128,196
s_add_u32 m0, s62, 0
X 170
Mvav 64,128,204
Ra 156,242,64512
K 208,168,169
Mvav 96,128,212
X 171
Mvav 128,128,220
Dc 1,8,61,0
Mvav 132,132,220
X 172
Mvav 100,132,212
Ra 128,242,32768
K 209,170,171
Mvav 68,132,204
X 173
Mvav 36,132,196
Dc 1,8,61,1024
Mvav 40,136,196
X 174
Mvav 72,136,204
Ra 132,242,33792
K 216,172,173
Mvav 104,136,212
X 175
Mvav 136,136,220
Dc 1,8,61,2048
Mvav 140,140,220
X 176
Mvav 108,140,212
Ra 136,242,34816
K 217,174,175
Mvav 76,140,204
X 177
Mvav 44,140,196
W 3
Dc 1,8,61,3072
Mvav 48,144,196
X 178
Mvav 80,144,204
s_add_u32 m0, s62, 4096
Ra 140,242,35840
Mvav 112,144,212
K 194,176,177
Mvav 144,144,220
X 179
Mvav 148,148,220
X 180
Mvav 116,148,212
Dc 243,8,61,0
Ra 144,242,36864
Mvav 84,148,204
K 195,178,179
Mvav 52,148,196
X 181
Mvav 56,152,196
X 182
Mvav 88,152,204
Dc 243,8,61,1024
Ra 148,242,37888
Mvav 120,152,212
K 202,180,181
Mvav 152,152,220
X 183
Mvav 156,156,220
X 184
Mvav 124,156,212
Dc 243,8,61,2048
Ra 152,242,38912
Mvav 92,156,204
K 203,182,183
Mvav 60,156,196
W 3
X 185
Cvaa 160,128,64,224
X 186
Mvaa 160,132,68
Dc 243,8,61,3072
Ra 156,242,39936
Cvaa 164,132,84,228
s_add_u32 s61, s61, s63
K 210,184,185
Mvaa 164,128,80
X 187
Cvaa 168,128,96,232
X 188
Mvaa 168,132,100
Cvaa 172,132,116,236
s_xor_b32 s62, s62, 0x10000
K 211,186,187
Mvaa 172,128,112
X 189
Mvaa 160,136,72
Ra 128,242,49152
Ra 132,242,50176
Mvaa 160,140,76
s_add_u32 s64, s64, 1
X 190
Mvaa 164,140,92
K 218,188,189
Mvaa 164,136,88
X 191
Mvaa 168,136,104
Mvaa 168,140,108
X 160
Mvaa 172,140,124
K 219,190,191
Mvaa 172,136,120
W 2
X 161
Cvaa 176,144,64,224
Ra 136,242,51200
Ra 140,242,52224
Mvaa 176,148,68
X 162
Cvaa 180,148,84,228
K 196,160,161
Mvaa 180,144,80
X 163
Cvaa 184,144,96,232
Mvaa 184,148,100
X 164
Cvaa 188,148,116,236
K 197,162,163
Mvaa 188,144,112
X 165
Mvaa 176,152,72
Ra 144,242,53248
Ra 148,242,54272
Mvaa 176,156,76
X 166
Mvaa 180,156,92
K 204,164,165
Mvaa 180,152,88
X 167
Mvaa 184,152,104
Mvaa 184,156,108
X 168
Mvaa 188,156,124
Mvaa 188,152,120
W 2
Ra 152,242,55296
K 205,166,167
Mvav 32,128,192
X 169
Mvav 64,128,200
Ra 156,242,56320
v_xor_b32 v242, 0x10000, v242
Mvav 96,128,208
X 170
Mvav 128,128,216
K 212,168,169
Mvav 132,132,216
X 171
Mvav 100,132,208
X 172
Mvav 68,132,200
Mvav 36,132,192
K 213,170,171
Mvav 40,136,192
X 173
Mvav 72,136,200
X 174
Mvav 104,136,208
Mvav 136,136,216
K 220,172,173
Mvav 140,140,216
X 175
Mvav 108,140,208
X 176
Mvav 76,140,200
s_waitcnt vmcnt(0) lgkmcnt(0)
s_barrier
Mvav 44,140,192
Ra 128,242,40960
K 221,174,175
Mvav 48,144,192
X 177
Mvav 80,144,200
X 178
Mvav 112,144,208
Ra 132,242,41984
Mvav 144,144,216
K 198,176,177
Mvav 148,148,216
X 179
Mvav 116,148,208
X 180
Mvav 84,148,200
Ra 136,242,43008
Mvav 52,148,192
K 199,178,179
Mvav 56,152,192
X 181
Mvav 88,152,200
Ra 140,242,44032
Mvav 120,152,208
X 182
Mvav 152,152,216
K 206,180,181
Mvav 156,156,216
X 183
Mvav 124,156,208
Mvav 92,156,200
K 207,182,183
Mvav 60,156,192
s_cmp_lg_u32 s64, 15
s_cbranch_scc1 .Ll2_34%=
.Lf2_37%=:
W 0
Cvaa 160,128,64,224
.Lh_4_2%=:
s_cbranch_execz .Lh_4_2_1%=
v_bfe_u32 v28, v1, 4, 2
v_lshlrev_b32 v28, 3, v28
Mvaa 160,132,68
Ra 144,242,45056
Ra 148,242,46080
Cvaa 164,132,84,228
Ra 152,242,47104
buffer_load_dwordx2 v[28:29], v28, s[8:11], s54 offen sc1
Mvaa 164,128,80
s_lshl_b32 s92, s58, 2
s_add_u32 s91, s92, s51
s_add_u32 s95, s91, 3
Cvaa 168,128,96,232
s_and_b32 s95, s95, 15
s_mul_i32 s95, s95, 40960
s_mul_i32 s71, s51, 10240
Mvaa 168,132,100
s_add_u32 s71, s71, 0
s_add_u32 m0, s71, s48
s_lshl2_add_u32 s94, s48, s95
Cvaa 172,132,116,236
X 184
Mvaa 172,128,112
X 185
Mvaa 160,136,72
X 186
Mvaa 160,140,76
Ra 156,242,48128
K 214,184,185
Mvaa 164,140,92
X 187
Mvaa 164,136,88
D 16,40,94,0
Ra 128,242,57344
Mvaa 168,136,104
s_add_u32 s95, s91, 5
s_and_b32 s95, s95, 15
s_mul_i32 s95, s95, 40960
Mvaa 168,140,108
s_mul_i32 s71, s51, 10240
s_add_u32 s71, s71, 20480
Ra 132,242,58368
Mvaa 172,140,124
X 188
Mvaa 172,136,120
D 17,40,94,1024
K 215,186,187
v_smfmac_f32_16x16x64_bf16 a[160:163], a[176:179], v[192:199], v240
X 189
v_smfmac_f32_16x16x64_bf16 a[164:167], a[176:179], v[200:207], v240
X 190
K 222,188,189
X 191
s_nop 0
K 223,190,191
v_smfmac_f32_16x16x64_bf16 a[168:171], a[176:179], v[208:215], v240
D 18,40,94,2048
)"
R"(Ra 136,242,59392
v_smfmac_f32_16x16x64_bf16 a[172:175], a[176:179], v[216:223], v240
W 3
X 160
Cvaa 176,144,64,224
X 161
Mvaa 176,148,68
X 162
Cvaa 180,148,84,228
D 19,40,94,3072
K 192,160,161
Mvaa 180,144,80
s_add_u32 m0, m0, 4096
X 163
Cvaa 184,144,96,232
X 164
Mvaa 184,148,100
Ra 140,242,60416
K 193,162,163
Cvaa 188,148,116,236
buffer_load_dwordx4 v20, s[40:43], s94 offen lds
Mvaa 188,144,112
s_add_u32 m0, s71, s48
s_lshl2_add_u32 s94, s48, s95
Ra 144,242,61440
Mvaa 176,152,72
X 165
Mvaa 176,156,76
X 166
Mvaa 180,156,92
D 16,40,94,0
K 200,164,165
Mvaa 180,152,88
X 167
Mvaa 184,152,104
X 172
Mvaa 184,156,108
D 17,40,94,1024
K 201,166,167
Mvaa 188,156,124
X 173
Mvaa 188,152,120
W 1
Ra 148,242,62464
Ra 152,242,63488
Mvav 32,128,196
X 174
Mvav 64,128,204
D 18,40,94,2048
K 216,172,173
Mvav 96,128,212
X 175
Mvav 128,128,220
X 168
Mvav 132,132,220
s_mov_b32 s18, 40960
D 19,40,94,3072
K 217,174,175
Mvav 100,132,212
s_add_u32 m0, m0, 4096
X 169
Mvav 68,132,204
s_mov_b32 s19, 73728
X 170
Mvav 36,132,196
s_bitcmp1_b32 s14, 0
s_cselect_b32 s18, 49152, s18
s_cselect_b32 s19, 98304, s19
Ra 156,242,64512
.Lh_4_3%=:
s_cbranch_execz .Lh_4_3_1%=
K 208,168,169
Mvav 40,136,196
s_cmp_lt_u32 s14, 2
s_cselect_b32 s20, s18, s19
s_cselect_b32 s21, s19, s18
X 171
Mvav 72,136,204
X 184
Mvav 104,136,212
buffer_load_dwordx4 v20, s[40:43], s94 offen lds
K 209,170,171
Mvav 136,136,220
s_waitcnt vmcnt(10)
v_cmp_eq_u64 vcc, s[12:13], v[28:29]
s_cmp_eq_u64 vcc, -1
s_cselect_b32 s46, 79691776, 0
s_add_u32 s81, s58, 0
s_and_b32 s81, s81, 3
Mvav 140,140,220
s_lshl_b32 s81, s81, 12
s_add_u32 s81, s81, s49
Ra 128,242,49152
Mvav 108,140,212
X 185
Mvav 76,140,204
X 186
Mvav 44,140,196
W 1
buffer_load_dwordx4 a[184:187], v1, s[44:47], s81 offen offset:0 sc1
K 210,184,185
Mvav 48,144,196
s_add_u32 s82, s81, 131072
X 187
Mvav 80,144,204
v_add_u32 v2, s20, v1
v_add_u32 v3, s21, v1
Mvav 112,144,212
s_lshl_b32 s18, s14, 9
X 188
Mvav 144,144,220
Ra 132,242,50176
K 211,186,187
Mvav 148,148,220
buffer_load_dwordx4 a[200:203], v1, s[44:47], s82 offen offset:0 sc1
Ra 136,242,51200
Mvav 116,148,212
X 189
Mvav 84,148,204
v_lshrrev_b32 v232, 1, v1
v_add_u32 v232, s18, v232
Mvav 52,148,196
X 190
Mvav 56,152,196
K 218,188,189
v_bfe_u32 v12, v1, 6, 2
Mvav 88,152,204
buffer_load_dwordx4 a[188:191], v1, s[44:47], s81 offen offset:1024 sc1
Ra 140,242,52224
Mvav 120,152,212
X 191
Mvav 152,152,220
v_lshrrev_b32 v13, 8, v1
v_bfe_u32 v14, v1, 4, 1
Mvav 156,156,220
X 176
Mvav 124,156,212
K 219,190,191
v_lshlrev_b32 v14, 4, v14
Mvav 92,156,204
X 177
Mvav 60,156,196
buffer_load_dwordx4 a[204:207], v1, s[44:47], s82 offen offset:1024 sc1
v_mov_b32 v15, 0x3f80
Maav 168,180,208
X 178
K 194,176,177
X 179
X 180
K 195,178,179
Maav 172,180,216
X 181
X 182
K 202,180,181
X 183
s_nop 0
K 203,182,183
Maav 160,180,192
Ra 144,242,53248
v_lshlrev_b32 v15, v14, v15
Maav 164,180,200
W 1
buffer_load_dwordx4 a[192:195], v1, s[44:47], s81 offen offset:2048 sc1
v_bfe_u32 v14, v1, 5, 1
Mvav 96,128,208
Ra 148,242,54272
v_xor_b32 v27, v12, v13
v_lshl_or_b32 v27, v27, 1, v14
Mvav 128,128,216
v_cmp_eq_u32_e64 s[86:87], 0, v27
v_mov_b32 v226, 0
s_nop 0
v_cndmask_b32_e64 v224, 0, v15, s[86:87]
Mvav 132,132,216
Ra 152,242,55296
Ra 156,242,56320
buffer_load_dwordx4 a[208:211], v1, s[44:47], s82 offen offset:2048 sc1
Mvav 100,132,208
v_mov_b32 v228, 0
v_mov_b32 v230, v224
v_cmp_eq_u32_e64 s[86:87], 1, v27
Mvav 104,136,208
v_mov_b32 v227, 0
Mvav 136,136,216
buffer_load_dwordx4 a[196:199], v1, s[44:47], s81 offen offset:3072 sc1
v_cndmask_b32_e64 v225, 0, v15, s[86:87]
v_mov_b32 v229, 0
Mvav 140,140,216
v_mov_b32 v231, v225
G 4,160
G 8,161
Mvav 108,140,208
W 0
G 5,164
G 9,165
G 6,168
Mvav 112,144,208
buffer_load_dwordx4 a[212:215], v1, s[44:47], s82 offen offset:3072 sc1
s_add_u32 s81, s58, 1
s_and_b32 s81, s81, 3
s_lshl_b32 s81, s81, 12
Mvav 144,144,216
s_add_u32 s81, s81, s49
G 10,169
G 7,172
G 11,173
Mvav 148,148,216
A 4,4,8
A 5,5,9
A 6,6,10
Mvav 116,148,208
buffer_load_dwordx4 a[216:219], v1, s[44:47], s81 offen offset:0 sc1
s_add_u32 s82, s81, 131072
A 7,7,11
ds_write_b64 v232, v[6:7] offset:57344
Mvav 120,152,208
A 8,4,5
A 9,6,7
A 8,8,9
Mvav 152,152,216
v_cmp_ngt_f32_e64 s[86:87], s90, v8
s_lshl_b32 s19, s14, 2
v_mov_b32 v9, s19
s_or_b64 s[88:89], s[88:89], s[86:87]
Mvav 156,156,216
s_and_b64 s[88:89], s[88:89], s[24:25]
s_cselect_b32 s18, 1, 0
buffer_load_dwordx4 a[232:235], v1, s[44:47], s82 offen offset:0 sc1
v_mov_b32 v8, s18
ds_write_b32 v9, v8 offset:59392
Mvav 124,156,208
v_xor_b32 v10, 1024, v232
v_bfe_u32 v11, v1, 2, 4
K 96,96,97
Mvav 32,128,192
K 97,98,99
K 98,100,101
K 99,102,103
Mvav 64,128,200
ds_write_b128 v2, v[96:99] offset:0
K 128,128,129
K 129,130,131
Mvav 68,132,200
K 130,132,133
K 131,134,135
ds_write_b128 v2, v[128:131] offset:4096
Mvav 36,132,192
buffer_load_dwordx4 a[220:223], v1, s[44:47], s81 offen offset:1024 sc1
K 104,104,105
K 105,106,107
Mvav 40,136,192
K 106,108,109
K 107,110,111
ds_write_b128 v2, v[104:107] offset:1024
Mvav 72,136,200
K 136,136,137
K 137,138,139
K 138,140,141
Mvav 76,140,200
buffer_load_dwordx4 a[236:239], v1, s[44:47], s82 offen offset:1024 sc1
K 139,142,143
ds_write_b128 v2, v[136:139] offset:5120
Mvav 44,140,192
K 112,112,113
K 113,114,115
K 114,116,117
Mvav 48,144,192
K 115,118,119
ds_write_b128 v2, v[112:115] offset:2048
K 144,144,145
Mvav 80,144,200
K 145,146,147
K 146,148,149
K 147,150,151
Mvav 84,148,200
ds_write_b128 v2, v[144:147] offset:6144
buffer_load_dwordx4 a[224:227], v1, s[44:47], s81 offen offset:2048 sc1
K 120,120,121
Mvav 52,148,192
K 121,122,123
K 122,124,125
K 123,126,127
Mvav 56,152,192
ds_write_b128 v2, v[120:123] offset:3072
K 152,152,153
K 153,154,155
Mvav 88,152,200
buffer_load_dwordx4 a[240:243], v1, s[44:47], s82 offen offset:2048 sc1
K 154,156,157
K 155,158,159
Mvav 92,156,200
ds_write_b128 v2, v[152:155] offset:7168
Mvav 60,156,192
buffer_load_dwordx4 a[228:231], v1, s[44:47], s81 offen offset:3072 sc1
buffer_load_dwordx4 a[244:247], v1, s[44:47], s82 offen offset:3072 sc1
s_branch .Lend_39%=
.p2align 12
.Lfs_38%=:
v_smfmac_f32_16x16x64_bf16 a[160:163], a[176:179], v[192:199], v240
s_mov_b32 s46, 0
s_lshl_b32 s92, s58, 2
s_add_u32 s91, s92, s51
s_add_u32 s95, s91, 3
s_and_b32 s95, s95, 15
s_mul_i32 s95, s95, 40960
s_mul_i32 s71, s51, 10240
v_smfmac_f32_16x16x64_bf16 a[164:167], a[176:179], v[200:207], v240
s_add_u32 s71, s71, 0
s_add_u32 m0, s71, s48
s_lshl2_add_u32 s94, s48, s95
Ra 144,242,45056
Ra 148,242,46080
Ra 152,242,47104
Ra 156,242,48128
v_smfmac_f32_16x16x64_bf16 a[168:171], a[176:179], v[208:215], v240
D 16,40,94,0
s_add_u32 s95, s91, 5
s_and_b32 s95, s95, 15
s_mul_i32 s95, s95, 40960
s_mul_i32 s71, s51, 10240
s_add_u32 s71, s71, 20480
v_smfmac_f32_16x16x64_bf16 a[172:175], a[176:179], v[216:223], v240
W 4
Cvaa 168,128,96,232
v_mov_b32 v192, 0
G 12,160
Mvaa 168,132,100
D 17,40,94,1024
G 4,161
A 12,12,4
v_cmp_nlt_f32_e64 s[86:87], v12, s90
v_rcp_f32 v12, v12
s_or_b64 s[88:89], s[88:89], s[86:87]
Cvaa 172,132,116,236
v_accvgpr_write_b32 a160, 0
v_accvgpr_write_b32 a161, 0
v_accvgpr_write_b32 a162, 0
v_accvgpr_write_b32 a163, 0
G 13,164
G 5,165
Mvaa 172,128,112
v_mov_b32 v193, 0
A 13,13,5
v_cmp_nlt_f32_e64 s[86:87], v13, s90
v_rcp_f32 v13, v13
s_or_b64 s[88:89], s[88:89], s[86:87]
v_accvgpr_write_b32 a164, 0
Mvaa 168,136,104
v_accvgpr_write_b32 a165, 0
v_accvgpr_write_b32 a166, 0
v_accvgpr_write_b32 a167, 0
U 32,32,12
U 33,33,12
U 34,34,12
Mvaa 168,140,108
v_mov_b32 v200, 0
U 35,35,12
U 36,36,12
U 37,37,12
U 38,38,12
U 39,39,12
Mvaa 172,140,124
Ra 128,242,16384
Ra 132,242,17408
K 32,32,33
K 33,34,35
K 34,36,37
K 35,38,39
s_add_u32 s93, s67, 0
Mvaa 172,136,120
Ra 136,242,18432
Ra 140,242,19456
D 18,40,94,2048
v_mov_b32 v201, 0
W 4
Cvaa 188,144,112,236
Ra 144,242,20480
v_cndmask_b32_e64 v168, v241, v168, s[72:73]
v_cndmask_b32_e64 v169, v241, v169, s[74:75]
v_cndmask_b32_e64 v170, v241, v170, s[76:77]
Mvaa 188,148,116
buffer_store_dwordx4 v[32:35], v1, s[8:11], s93 offen offset:0 sc1
U 40,40,12
U 41,41,12
U 42,42,12
U 43,43,12
U 44,44,12
Mvaa 188,152,120
U 45,45,12
U 46,46,12
U 47,47,12
K 40,40,41
K 41,42,43
K 42,44,45
Mvaa 188,156,124
W 1
K 43,46,47
buffer_store_dwordx4 v[40:43], v1, s[8:11], s93 offen offset:1024 sc1
U 48,48,12
U 49,49,12
)"
R"(U 50,50,12
U 51,51,12
Mvav 96,128,212
U 52,52,12
U 53,53,12
U 54,54,12
U 55,55,12
K 48,48,49
K 49,50,51
Mvav 128,128,220
K 50,52,53
K 51,54,55
buffer_store_dwordx4 v[48:51], v1, s[8:11], s93 offen offset:2048 sc1
U 56,56,12
U 57,57,12
U 58,58,12
Mvav 132,132,220
Ra 148,242,21504
Ra 152,242,22528
Ra 156,242,23552
U 59,59,12
U 60,60,12
U 61,61,12
Mvav 100,132,212
D 19,40,94,3072
s_add_u32 m0, m0, 4096
U 62,62,12
U 63,63,12
K 56,56,57
K 57,58,59
K 58,60,61
Mvav 104,136,212
K 59,62,63
v_cndmask_b32_e64 v171, v241, v171, s[78:79]
X 168
X 169
Mvav 136,136,220
buffer_store_dwordx4 v[56:59], v1, s[8:11], s93 offen offset:3072 sc1
Ra 128,242,49152
G 14,168
G 6,169
A 14,14,6
v_cmp_nlt_f32_e64 s[86:87], v14, s90
Mvav 140,140,220
v_rcp_f32 v14, v14
s_or_b64 s[88:89], s[88:89], s[86:87]
v_accvgpr_write_b32 a168, 0
v_accvgpr_write_b32 a169, 0
v_accvgpr_write_b32 a170, 0
v_accvgpr_write_b32 a171, 0
Mvav 108,140,212
W 1
G 15,172
G 7,173
A 15,15,7
v_cmp_nlt_f32_e64 s[86:87], v15, s90
v_rcp_f32 v15, v15
Mvav 112,144,212
s_or_b64 s[88:89], s[88:89], s[86:87]
buffer_load_dwordx4 v20, s[40:43], s94 offen lds
v_accvgpr_write_b32 a172, 0
v_accvgpr_write_b32 a173, 0
v_accvgpr_write_b32 a174, 0
v_accvgpr_write_b32 a175, 0
U 64,64,13
Mvav 144,144,220
s_add_u32 m0, s71, s48
s_lshl2_add_u32 s94, s48, s95
U 65,65,13
U 66,66,13
U 67,67,13
U 68,68,13
U 69,69,13
Mvav 148,148,220
U 70,70,13
U 71,71,13
K 64,64,65
K 65,66,67
K 66,68,69
K 67,70,71
s_add_u32 s93, s67, 131072
Mvav 116,148,212
buffer_store_dwordx4 v[64:67], v1, s[8:11], s93 offen offset:0 sc1
U 72,72,13
U 73,73,13
U 74,74,13
U 75,75,13
U 76,76,13
Mvav 120,152,212
Ra 132,242,50176
Ra 136,242,51200
Ra 140,242,52224
U 77,77,13
U 78,78,13
U 79,79,13
Mvav 152,152,220
K 72,72,73
K 73,74,75
K 74,76,77
K 75,78,79
buffer_store_dwordx4 v[72:75], v1, s[8:11], s93 offen offset:1024 sc1
U 80,80,13
Mvav 156,156,220
X 170
K 208,168,169
X 171
X 172
K 209,170,171
X 173
X 174
K 216,172,173
X 175
v_mov_b32 v194, 0
K 217,174,175
v_mov_b32 v195, 0
v_mov_b32 v202, 0
v_mov_b32 v203, 0
v_mov_b32 v210, 0
v_mov_b32 v211, 0
Mvav 124,156,212
v_cndmask_b32_e64 v188, v241, v188, s[72:73]
v_cndmask_b32_e64 v189, v241, v189, s[74:75]
v_cndmask_b32_e64 v190, v241, v190, s[76:77]
v_cndmask_b32_e64 v191, v241, v191, s[78:79]
X 188
X 189
X 190
K 218,188,189
X 191
s_nop 0
K 219,190,191
Maav 168,180,208
D 16,40,94,0
U 81,81,13
U 82,82,13
U 83,83,13
U 84,84,13
U 85,85,13
Maav 172,180,216
W 0
U 86,86,13
U 87,87,13
K 80,80,81
K 81,82,83
K 82,84,85
K 83,86,87
Zvav 32,128,192
buffer_store_dwordx4 v[80:83], v1, s[8:11], s93 offen offset:2048 sc1
U 88,88,13
U 89,89,13
U 90,90,13
U 91,91,13
U 92,92,13
Zvav 64,128,200
U 93,93,13
U 94,94,13
U 95,95,13
K 88,88,89
K 89,90,91
K 90,92,93
K 91,94,95
buffer_store_dwordx4 v[88:91], v1, s[8:11], s93 offen offset:3072 sc1
U 96,96,14
U 97,97,14
U 98,98,14
U 99,99,14
U 100,100,14
U 101,101,14
U 102,102,14
U 103,103,14
K 96,96,97
K 97,98,99
K 98,100,101
K 99,102,103
s_add_u32 s93, s67, 262144
buffer_store_dwordx4 v[96:99], v1, s[8:11], s93 offen offset:0 sc1
s_nop 1
Zvav 96,128,208
U 104,104,14
U 105,105,14
U 106,106,14
U 107,107,14
U 108,108,14
U 109,109,14
U 110,110,14
U 111,111,14
K 104,104,105
K 105,106,107
K 106,108,109
K 107,110,111
buffer_store_dwordx4 v[104:107], v1, s[8:11], s93 offen offset:1024 sc1
U 112,112,14
U 113,113,14
U 114,114,14
U 115,115,14
U 116,116,14
U 117,117,14
U 118,118,14
U 119,119,14
K 112,112,113
K 113,114,115
K 114,116,117
K 115,118,119
buffer_store_dwordx4 v[112:115], v1, s[8:11], s93 offen offset:2048 sc1
U 120,120,14
U 121,121,14
U 122,122,14
U 123,123,14
U 124,124,14
U 125,125,14
U 126,126,14
U 127,127,14
K 120,120,121
K 121,122,123
K 122,124,125
K 123,126,127
buffer_store_dwordx4 v[120:123], v1, s[8:11], s93 offen offset:3072 sc1
U 128,128,15
U 129,129,15
U 130,130,15
U 131,131,15
U 132,132,15
U 133,133,15
U 134,134,15
U 135,135,15
K 128,128,129
K 129,130,131
K 130,132,133
K 131,134,135
s_add_u32 s93, s67, 393216
buffer_store_dwordx4 v[128:131], v1, s[8:11], s93 offen offset:0 sc1
s_nop 1
Zvav 128,128,216
Ra 144,242,53248
U 136,136,15
U 137,137,15
U 138,138,15
U 139,139,15
U 140,140,15
Zvav 132,132,216
D 17,40,94,1024
U 141,141,15
U 142,142,15
U 143,143,15
K 136,136,137
K 137,138,139
Zvav 100,132,208
Ra 148,242,54272
Ra 152,242,55296
Ra 156,242,56320
K 138,140,141
K 139,142,143
Zvav 68,132,200
D 18,40,94,2048
Zvav 36,132,192
Zvav 40,136,192
D 19,40,94,3072
s_add_u32 m0, m0, 4096
Zvav 72,136,200
Zvav 104,136,208
buffer_store_dwordx4 v[136:139], v1, s[8:11], s93 offen offset:1024 sc1
U 144,144,15
U 145,145,15
U 146,146,15
U 147,147,15
U 148,148,15
Zvav 136,136,216
U 149,149,15
U 150,150,15
U 151,151,15
K 144,144,145
K 145,146,147
K 146,148,149
Zvav 140,140,216
K 147,150,151
buffer_store_dwordx4 v[144:147], v1, s[8:11], s93 offen offset:2048 sc1
U 152,152,15
U 153,153,15
U 154,154,15
U 155,155,15
Zvav 108,140,208
U 156,156,15
U 157,157,15
U 158,158,15
U 159,159,15
K 152,152,153
K 153,154,155
Zvav 76,140,200
buffer_load_dwordx4 v20, s[40:43], s94 offen lds
K 154,156,157
K 155,158,159
Zvav 44,140,192
W 0
Zvav 48,144,192
buffer_store_dwordx4 v[152:155], v1, s[8:11], s93 offen offset:3072 sc1
s_mov_b32 s18, 40960
s_mov_b32 s19, 73728
s_bitcmp1_b32 s14, 0
s_cselect_b32 s18, 49152, s18
s_cselect_b32 s19, 98304, s19
s_cmp_lt_u32 s14, 2
s_cselect_b32 s20, s18, s19
s_cselect_b32 s21, s19, s18
v_add_u32 v2, s20, v1
v_add_u32 v3, s21, v1
Zvav 80,144,200
s_lshl_b32 s18, s14, 9
v_lshrrev_b32 v232, 1, v1
v_add_u32 v232, s18, v232
v_bfe_u32 v12, v1, 6, 2
v_lshrrev_b32 v13, 8, v1
v_bfe_u32 v14, v1, 4, 1
v_lshlrev_b32 v14, 4, v14
Zvav 112,144,208
v_mov_b32 v15, 0x3f80
v_lshlrev_b32 v15, v14, v15
v_bfe_u32 v14, v1, 5, 1
v_xor_b32 v27, v12, v13
v_lshl_or_b32 v27, v27, 1, v14
v_cmp_eq_u32_e64 s[86:87], 0, v27
Zvav 144,144,216
v_mov_b32 v226, 0
Zvav 148,148,216
v_cndmask_b32_e64 v224, 0, v15, s[86:87]
v_mov_b32 v228, 0
v_mov_b32 v230, v224
v_cmp_eq_u32_e64 s[86:87], 1, v27
v_mov_b32 v227, 0
s_nop 0
v_cndmask_b32_e64 v225, 0, v15, s[86:87]
Zvav 116,148,208
v_mov_b32 v229, 0
v_mov_b32 v231, v225
G 4,160
G 8,161
G 5,164
G 9,165
Zvav 84,148,200
G 6,168
G 10,169
G 7,172
G 11,173
A 4,4,8
A 5,5,9
Zvav 52,148,192
A 6,6,10
A 7,7,11
ds_write_b64 v232, v[6:7] offset:57344
A 8,4,5
A 9,6,7
A 8,8,9
Zvav 56,152,192
v_cmp_ngt_f32_e64 s[86:87], s90, v8
s_lshl_b32 s19, s14, 2
v_mov_b32 v9, s19
s_or_b64 s[88:89], s[88:89], s[86:87]
s_and_b64 s[88:89], s[88:89], s[24:25]
s_cselect_b32 s18, 1, 0
v_mov_b32 v8, s18
ds_write_b32 v9, v8 offset:59392
Zvav 88,152,200
v_xor_b32 v10, 1024, v232
v_bfe_u32 v11, v1, 2, 4
K 96,96,97
K 97,98,99
K 98,100,101
K 99,102,103
Zvav 120,152,208
ds_write_b128 v2, v[96:99] offset:0
K 128,128,129
K 129,130,131
K 130,132,133
K 131,134,135
ds_write_b128 v2, v[128:131] offset:4096
Zvav 152,152,216
K 104,104,105
K 105,106,107
K 106,108,109
K 107,110,111
ds_write_b128 v2, v[104:107] offset:1024
K 136,136,137
Zvav 156,156,216
K 137,138,139
K 138,140,141
K 139,142,143
ds_write_b128 v2, v[136:139] offset:5120
K 112,112,113
K 113,114,115
Zvav 124,156,208
K 114,116,117
K 115,118,119
ds_write_b128 v2, v[112:115] offset:2048
K 144,144,145
K 145,146,147
K 146,148,149
Zvav 92,156,200
K 147,150,151
ds_write_b128 v2, v[144:147] offset:6144
Zvav 60,156,192
K 120,120,121
)"
R"(K 121,122,123
K 122,124,125
K 123,126,127
ds_write_b128 v2, v[120:123] offset:3072
K 152,152,153
K 153,154,155
K 154,156,157
K 155,158,159
ds_write_b128 v2, v[152:155] offset:7168
s_waitcnt vmcnt(0)
s_and_b64 s[86:87], s[88:89], s[24:25]
s_cbranch_scc1 .LL_46%=
v_mov_b64 v[28:29], s[12:13]
s_mov_b64 exec, 1
buffer_store_dwordx2 v[28:29], off, s[8:11], s69 sc1
s_mov_b64 exec, -1
.LL_46%=:
s_mov_b32 s70, 0
s_branch .Lend_39%=
.p2align 12
.Lpd_28%=:
v_pk_mul_f32 v[32:33], v[32:33], s[22:23]
v_pk_mul_f32 v[34:35], v[34:35], s[22:23]
v_pk_mul_f32 v[36:37], v[36:37], s[22:23]
v_pk_mul_f32 v[38:39], v[38:39], s[22:23]
v_pk_mul_f32 v[64:65], v[64:65], s[22:23]
v_pk_mul_f32 v[66:67], v[66:67], s[22:23]
v_pk_mul_f32 v[68:69], v[68:69], s[22:23]
v_pk_mul_f32 v[70:71], v[70:71], s[22:23]
v_pk_mul_f32 v[96:97], v[96:97], s[22:23]
v_pk_mul_f32 v[98:99], v[98:99], s[22:23]
v_pk_mul_f32 v[100:101], v[100:101], s[22:23]
v_pk_mul_f32 v[102:103], v[102:103], s[22:23]
v_pk_mul_f32 v[128:129], v[128:129], s[22:23]
v_pk_mul_f32 v[130:131], v[130:131], s[22:23]
v_pk_mul_f32 v[132:133], v[132:133], s[22:23]
v_pk_mul_f32 v[134:135], v[134:135], s[22:23]
v_pk_mul_f32 v[40:41], v[40:41], s[22:23]
v_pk_mul_f32 v[42:43], v[42:43], s[22:23]
v_pk_mul_f32 v[44:45], v[44:45], s[22:23]
v_pk_mul_f32 v[46:47], v[46:47], s[22:23]
v_pk_mul_f32 v[72:73], v[72:73], s[22:23]
v_pk_mul_f32 v[74:75], v[74:75], s[22:23]
v_pk_mul_f32 v[76:77], v[76:77], s[22:23]
v_pk_mul_f32 v[78:79], v[78:79], s[22:23]
v_pk_mul_f32 v[104:105], v[104:105], s[22:23]
v_pk_mul_f32 v[106:107], v[106:107], s[22:23]
v_pk_mul_f32 v[108:109], v[108:109], s[22:23]
v_pk_mul_f32 v[110:111], v[110:111], s[22:23]
v_pk_mul_f32 v[136:137], v[136:137], s[22:23]
v_pk_mul_f32 v[138:139], v[138:139], s[22:23]
v_pk_mul_f32 v[140:141], v[140:141], s[22:23]
v_pk_mul_f32 v[142:143], v[142:143], s[22:23]
v_pk_mul_f32 v[48:49], v[48:49], s[22:23]
v_pk_mul_f32 v[50:51], v[50:51], s[22:23]
v_pk_mul_f32 v[52:53], v[52:53], s[22:23]
v_pk_mul_f32 v[54:55], v[54:55], s[22:23]
v_pk_mul_f32 v[80:81], v[80:81], s[22:23]
v_pk_mul_f32 v[82:83], v[82:83], s[22:23]
v_pk_mul_f32 v[84:85], v[84:85], s[22:23]
v_pk_mul_f32 v[86:87], v[86:87], s[22:23]
v_pk_mul_f32 v[112:113], v[112:113], s[22:23]
v_pk_mul_f32 v[114:115], v[114:115], s[22:23]
v_pk_mul_f32 v[116:117], v[116:117], s[22:23]
v_pk_mul_f32 v[118:119], v[118:119], s[22:23]
v_pk_mul_f32 v[144:145], v[144:145], s[22:23]
v_pk_mul_f32 v[146:147], v[146:147], s[22:23]
v_pk_mul_f32 v[148:149], v[148:149], s[22:23]
v_pk_mul_f32 v[150:151], v[150:151], s[22:23]
v_pk_mul_f32 v[56:57], v[56:57], s[22:23]
v_pk_mul_f32 v[58:59], v[58:59], s[22:23]
v_pk_mul_f32 v[60:61], v[60:61], s[22:23]
v_pk_mul_f32 v[62:63], v[62:63], s[22:23]
v_pk_mul_f32 v[88:89], v[88:89], s[22:23]
v_pk_mul_f32 v[90:91], v[90:91], s[22:23]
v_pk_mul_f32 v[92:93], v[92:93], s[22:23]
v_pk_mul_f32 v[94:95], v[94:95], s[22:23]
v_pk_mul_f32 v[120:121], v[120:121], s[22:23]
v_pk_mul_f32 v[122:123], v[122:123], s[22:23]
v_pk_mul_f32 v[124:125], v[124:125], s[22:23]
v_pk_mul_f32 v[126:127], v[126:127], s[22:23]
v_pk_mul_f32 v[152:153], v[152:153], s[22:23]
v_pk_mul_f32 v[154:155], v[154:155], s[22:23]
v_pk_mul_f32 v[156:157], v[156:157], s[22:23]
v_pk_mul_f32 v[158:159], v[158:159], s[22:23]
K 32,32,33
K 33,34,35
K 34,36,37
K 35,38,39
K 40,40,41
K 41,42,43
K 42,44,45
K 43,46,47
W 0
Zvav 196,128,32
s_add_u32 m0, s62, 0
Ra 144,242,4096
Ra 148,242,5120
Ra 152,242,6144
Ra 156,242,7168
Mvav 196,132,40
K 64,64,65
K 65,66,67
K 66,68,69
K 67,70,71
K 72,72,73
K 73,74,75
K 74,76,77
K 75,78,79
s_nop 1
Zvav 204,132,72
Dc 1,8,61,0
K 96,96,97
K 97,98,99
K 98,100,101
Mvav 204,128,64
K 99,102,103
K 104,104,105
K 105,106,107
K 106,108,109
K 107,110,111
Zvav 212,128,96
Dc 1,8,61,1024
K 128,128,129
K 129,130,131
K 130,132,133
Mvav 212,132,104
K 131,134,135
K 136,136,137
K 137,138,139
K 138,140,141
K 139,142,143
s_nop 1
Zvav 220,132,136
Dc 1,8,61,2048
K 48,48,49
K 49,50,51
K 50,52,53
Mvav 220,128,128
K 51,54,55
K 56,56,57
K 57,58,59
K 58,60,61
K 59,62,63
Mvav 196,136,48
Dc 1,8,61,3072
s_add_u32 m0, s62, 4096
Ra 128,242,8192
Ra 132,242,9216
K 80,80,81
Mvav 196,140,56
K 81,82,83
K 82,84,85
K 83,86,87
K 88,88,89
K 89,90,91
K 90,92,93
K 91,94,95
s_nop 1
Mvav 204,140,88
Dc 243,8,61,0
K 112,112,113
K 113,114,115
K 114,116,117
Mvav 204,136,80
K 115,118,119
K 120,120,121
K 121,122,123
K 122,124,125
K 123,126,127
Mvav 212,136,112
Dc 243,8,61,1024
K 144,144,145
K 145,146,147
K 146,148,149
Mvav 212,140,120
K 147,150,151
K 152,152,153
K 153,154,155
K 154,156,157
K 155,158,159
s_nop 1
Mvav 220,140,152
Dc 243,8,61,2048
v_accvgpr_write_b32 a0, v32
v_accvgpr_write_b32 a1, v33
v_accvgpr_write_b32 a2, v34
Mvav 220,136,144
W 2
Dc 243,8,61,3072
s_add_u32 m0, s62, 8192
v_accvgpr_write_b32 a3, v35
v_accvgpr_write_b32 a4, v40
Zvav 68,144,64
Dc 244,8,61,0
v_accvgpr_write_b32 a5, v41
v_accvgpr_write_b32 a6, v42
v_accvgpr_write_b32 a7, v43
Mvav 68,148,72
Dc 244,8,61,1024
v_accvgpr_write_b32 a8, v48
v_accvgpr_write_b32 a9, v49
v_accvgpr_write_b32 a10, v50
Zvav 100,148,104
Dc 244,8,61,2048
Ra 136,242,10240
Ra 140,242,11264
v_accvgpr_write_b32 a11, v51
Mvav 100,144,96
Dc 244,8,61,3072
s_add_u32 m0, s62, 12288
v_accvgpr_write_b32 a12, v56
v_accvgpr_write_b32 a13, v57
v_accvgpr_write_b32 a14, v58
Zvav 132,144,128
Dc 245,8,61,0
v_accvgpr_write_b32 a15, v59
v_accvgpr_write_b32 a16, v64
v_cndmask_b32_e64 v196, v241, v196, s[72:73]
Mvav 132,148,136
Dc 245,8,61,1024
v_cndmask_b32_e64 v197, v241, v197, s[74:75]
v_cndmask_b32_e64 v198, v241, v198, s[76:77]
v_cndmask_b32_e64 v199, v241, v199, s[78:79]
Mvav 68,152,80
Dc 245,8,61,2048
v_max3_f32 v4, v196, v197, v198
v_max3_f32 v4, v4, v199, v252
v_max3_f32 v5, v204, v205, v206
Mvav 68,156,88
v_max3_f32 v5, v5, v207, v253
v_max3_f32 v6, v212, v213, v214
v_max3_f32 v6, v6, v215, v254
v_max3_f32 v7, v220, v221, v222
Mvav 100,156,120
v_max3_f32 v7, v7, v223, v255
v_mov_b32 v8, v4
v_mov_b32 v9, v5
v_mov_b32 v10, v6
Mvav 100,152,112
v_mov_b32 v11, v7
v_permlane32_swap_b32 v4, v8
v_permlane32_swap_b32 v5, v9
v_accvgpr_write_b32 a17, v65
Mvav 132,152,144
v_permlane32_swap_b32 v6, v10
v_permlane32_swap_b32 v7, v11
v_max_f32 v4, v4, v8
v_max_f32 v5, v5, v9
v_max_f32 v6, v6, v10
v_max_f32 v7, v7, v11
v_mov_b32 v8, v4
v_mov_b32 v9, v5
v_mov_b32 v10, v6
v_mov_b32 v11, v7
v_permlane16_swap_b32 v4, v8
v_permlane16_swap_b32 v5, v9
v_permlane16_swap_b32 v6, v10
v_permlane16_swap_b32 v7, v11
v_max_f32 v4, v4, v8
v_max_f32 v5, v5, v9
v_max_f32 v6, v6, v10
v_max_f32 v7, v7, v11
v_sub_f32 v4, 0, v4
v_sub_f32 v5, 0, v5
v_sub_f32 v6, 0, v6
v_sub_f32 v7, 0, v7
v_mov_b32 v224, v4
v_mov_b32 v225, v4
v_mov_b32 v226, v4
v_mov_b32 v227, v4
v_mov_b32 v228, v5
v_mov_b32 v229, v5
v_mov_b32 v230, v5
v_mov_b32 v231, v5
v_mov_b32 v232, v6
v_mov_b32 v233, v6
v_mov_b32 v234, v6
v_mov_b32 v235, v6
v_mov_b32 v236, v7
v_mov_b32 v237, v7
v_mov_b32 v238, v7
v_mov_b32 v239, v7
Mvav 132,156,152
W 0
Dc 245,8,61,3072
v_accvgpr_write_b32 a18, v66
v_accvgpr_write_b32 a19, v67
v_accvgpr_write_b32 a20, v72
Cvav 168,128,96,232
s_add_u32 s61, s61, s63
s_add_u32 s36, s29, 65536
v_accvgpr_write_b32 a21, v73
v_accvgpr_write_b32 a22, v74
v_accvgpr_write_b32 a23, v75
Mvav 168,132,104
s_and_b32 s80, s14, 1
buffer_store_dwordx4 a[192:195], v1, s[8:11], s36 offen offset:0 sc1
v_accvgpr_write_b32 a24, v80
v_accvgpr_write_b32 a25, v81
v_accvgpr_write_b32 a26, v82
Cvav 172,132,136,236
s_lshl_b32 s80, s80, 14
Ra 144,242,12288
Ra 148,242,13312
Ra 152,242,14336
Ra 156,242,15360
Mvav 172,128,128
s_add_u32 s80, s80, 98304
v_accvgpr_write_b32 a27, v83
v_accvgpr_write_b32 a28, v88
v_accvgpr_write_b32 a29, v89
v_accvgpr_write_b32 a30, v90
Mvav 168,136,112
buffer_store_dwordx4 a[200:203], v1, s[8:11], s36 offen offset:1024 sc1
v_accvgpr_write_b32 a31, v91
v_accvgpr_write_b32 a32, v96
v_accvgpr_write_b32 a33, v97
Mvav 168,140,120
v_accvgpr_write_b32 a34, v98
v_accvgpr_write_b32 a35, v99
v_accvgpr_write_b32 a36, v104
v_accvgpr_write_b32 a37, v105
Mvav 172,140,152
Ra 128,242,24576
Ra 132,242,25600
buffer_store_dwordx4 a[208:211], v1, s[8:11], s36 offen offset:2048 sc1
v_accvgpr_write_b32 a38, v106
Mvav 172,136,144
Ra 136,242,26624
Ra 140,242,27648
v_accvgpr_write_b32 a39, v107
W 4
Cvav 188,144,128,236
v_accvgpr_write_b32 a40, v112
v_accvgpr_write_b32 a41, v113
v_accvgpr_write_b32 a42, v114
v_accvgpr_write_b32 a43, v115
v_accvgpr_write_b32 a44, v120
v_accvgpr_write_b32 a45, v121
v_accvgpr_write_b32 a46, v122
v_accvgpr_write_b32 a47, v123
v_accvgpr_write_b32 a48, v128
v_accvgpr_write_b32 a49, v129
v_accvgpr_write_b32 a50, v130
v_accvgpr_write_b32 a51, v131
v_accvgpr_write_b32 a52, v136
v_accvgpr_write_b32 a53, v137
v_accvgpr_write_b32 a54, v138
v_accvgpr_write_b32 a55, v139
v_accvgpr_write_b32 a56, v144
v_accvgpr_write_b32 a57, v145
v_accvgpr_write_b32 a58, v146
v_accvgpr_write_b32 a59, v147
v_accvgpr_write_b32 a60, v152
v_accvgpr_write_b32 a61, v153
v_accvgpr_write_b32 a62, v154
v_accvgpr_write_b32 a63, v155
Mvav 188,148,136
s_barrier
buffer_store_dwordx4 a[216:219], v1, s[8:11], s36 offen offset:3072 sc1
s_add_u32 s36, s29, 69632
v_add_u32 v8, s80, v1
A 196,196,224
Mvav 188,152,144
A 197,197,224
A 198,198,224
A 199,199,224
X 196
X 197
X 198
K 192,196,197
X 199
A 204,204,228
K 193,198,199
A 205,205,228
A 206,206,228
A 207,207,228
X 204
X 205
X 206
K 200,204,205
X 207
A 212,212,232
K 201,206,207
A 213,213,232
A 214,214,232
A 215,215,232
X 212
X 213
X 214
K 208,212,213
X 215
A 220,220,236
K 209,214,215
A 221,221,236
A 222,222,236
A 223,223,236
X 220
X 221
)"
R"(X 222
K 216,220,221
X 223
v_mov_b32 v194, 0
K 217,222,223
v_mov_b32 v195, 0
v_cndmask_b32_e64 v68, v241, v68, s[72:73]
v_cndmask_b32_e64 v69, v241, v69, s[74:75]
v_cndmask_b32_e64 v70, v241, v70, s[76:77]
v_cndmask_b32_e64 v71, v241, v71, s[78:79]
A 68,68,228
A 69,69,228
A 70,70,228
A 71,71,228
X 68
X 69
X 70
K 202,68,69
X 71
A 100,100,232
K 203,70,71
A 101,101,232
A 102,102,232
A 103,103,232
X 100
X 101
X 102
K 210,100,101
X 103
A 132,132,236
K 211,102,103
A 133,133,236
A 134,134,236
A 135,135,236
X 132
X 133
X 134
K 218,132,133
X 135
s_nop 0
K 219,134,135
Mvav 188,156,152
W 0
buffer_store_dwordx4 a[224:227], v1, s[8:11], s36 offen offset:0 sc1
Ra 144,242,28672
Ra 148,242,29696
Ra 152,242,30720
Zvav 32,128,192
buffer_store_dwordx4 a[232:235], v1, s[8:11], s36 offen offset:1024 sc1
Ra 156,242,31744
Zvav 64,128,200
buffer_store_dwordx4 a[240:243], v1, s[8:11], s36 offen offset:2048 sc1
v_xor_b32 v242, 0x10000, v242
Zvav 96,128,208
s_and_b32 s80, s14, 1
s_xor_b32 s62, s62, 0x10000
buffer_store_dwordx4 a[248:251], v1, s[8:11], s36 offen offset:3072 sc1
s_add_u32 s36, s29, 393216
Zvav 128,128,216
s_lshl_b32 s80, s80, 14
s_add_u32 s80, s80, 131072
buffer_store_dwordx4 a[196:199], v1, s[8:11], s36 offen offset:0 sc1
v_add_u32 v23, 61440, v21
Zvav 132,132,216
buffer_store_dwordx4 a[204:207], v1, s[8:11], s36 offen offset:1024 sc1
v_add_u32 v12, s80, v1
Ra 64,12,8192
v_add_u32 v24, 61440, v22
Zvav 100,132,208
buffer_store_dwordx4 a[212:215], v1, s[8:11], s36 offen offset:2048 sc1
Ra 68,12,9216
Ra 72,12,10240
v_add_u32 v25, 122880, v21
Zvav 68,132,200
buffer_store_dwordx4 a[220:223], v1, s[8:11], s36 offen offset:3072 sc1
s_add_u32 s36, s29, 397312
Ra 76,12,11264
Ra 80,12,12288
Ra 84,12,13312
Zvav 36,132,192
buffer_store_dwordx4 a[228:231], v1, s[8:11], s36 offen offset:0 sc1
Ra 88,12,14336
Ra 92,12,15360
v_add_u32 v26, 122880, v22
Zvav 40,136,192
buffer_store_dwordx4 a[236:239], v1, s[8:11], s36 offen offset:1024 sc1
Ra 96,12,0
Ra 100,12,1024
Ra 104,12,2048
Zvav 72,136,200
Ra 108,12,3072
Ra 112,12,4096
buffer_store_dwordx4 a[244:247], v1, s[8:11], s36 offen offset:2048 sc1
Ra 116,12,5120
Zvav 104,136,208
Ra 120,12,6144
Ra 124,12,7168
buffer_store_dwordx4 a[252:255], v1, s[8:11], s36 offen offset:3072 sc1
Zvav 136,136,216
Zvav 140,140,216
Zvav 108,140,208
Zvav 76,140,200
s_waitcnt vmcnt(16) lgkmcnt(0)
s_barrier
ds_read_b128 v[248:251], v8
Zvav 44,140,192
Ra 128,242,40960
Zvav 48,144,192
Zvav 80,144,200
X 172
X 173
Zvav 112,144,208
Ra 132,242,41984
X 174
K 220,172,173
Zvav 144,144,216
X 175
v_cndmask_b32_e64 v168, v241, v168, s[72:73]
K 221,174,175
Zvav 148,148,216
v_cndmask_b32_e64 v169, v241, v169, s[74:75]
v_cndmask_b32_e64 v170, v241, v170, s[76:77]
v_cndmask_b32_e64 v171, v241, v171, s[78:79]
Zvav 116,148,208
X 168
X 169
Zvav 84,148,200
W 2
Ra 136,242,43008
X 170
K 212,168,169
Zvav 52,148,192
v_mov_b32 v224, v250
v_mov_b32 v225, v250
v_mov_b32 v226, v250
v_mov_b32 v227, v250
Zvav 56,152,192
v_mov_b32 v228, v251
v_mov_b32 v229, v251
v_mov_b32 v230, v251
v_mov_b32 v231, v251
Zvav 88,152,200
v_mov_b32 v232, v248
v_mov_b32 v233, v248
v_mov_b32 v234, v248
v_mov_b32 v235, v248
Zvav 120,152,208
v_mov_b32 v236, v249
v_mov_b32 v237, v249
v_mov_b32 v238, v249
v_mov_b32 v239, v249
Zvav 152,152,216
X 171
v_mov_b32 v204, 0
K 213,170,171
Zvav 156,156,216
Ra 140,242,44032
v_mov_b32 v205, 0
v_mov_b32 v196, 0
v_mov_b32 v197, 0
Zvav 124,156,208
v_mov_b32 v198, 0
v_mov_b32 v199, 0
v_mov_b32 v206, 0
v_mov_b32 v207, 0
Zvav 92,156,200
v_mov_b32 v214, 0
v_mov_b32 v215, 0
v_cndmask_b32_e64 v188, v241, v188, s[72:73]
v_cndmask_b32_e64 v189, v241, v189, s[74:75]
Zvav 60,156,192
v_cndmask_b32_e64 v190, v241, v190, s[76:77]
v_cndmask_b32_e64 v191, v241, v191, s[78:79]
X 188
X 189
X 190
K 222,188,189
X 191
s_nop 0
K 223,190,191
s_waitcnt vmcnt(0)
s_add_u32 s80, s71, 128
v_mov_b64 v[28:29], s[12:13]
s_mov_b64 exec, 1
buffer_store_dwordx2 v[28:29], off, s[8:11], s80 sc1
s_mov_b64 exec, -1
s_nop 1
s_branch .Lsp2_33%=
.p2align 12
.Lpe_44%=:
v_pk_mul_f32 v[32:33], v[32:33], s[22:23]
v_pk_mul_f32 v[34:35], v[34:35], s[22:23]
v_pk_mul_f32 v[36:37], v[36:37], s[22:23]
v_pk_mul_f32 v[38:39], v[38:39], s[22:23]
v_pk_mul_f32 v[64:65], v[64:65], s[22:23]
v_pk_mul_f32 v[66:67], v[66:67], s[22:23]
v_pk_mul_f32 v[68:69], v[68:69], s[22:23]
v_pk_mul_f32 v[70:71], v[70:71], s[22:23]
v_pk_mul_f32 v[96:97], v[96:97], s[22:23]
v_pk_mul_f32 v[98:99], v[98:99], s[22:23]
v_pk_mul_f32 v[100:101], v[100:101], s[22:23]
v_pk_mul_f32 v[102:103], v[102:103], s[22:23]
v_pk_mul_f32 v[128:129], v[128:129], s[22:23]
v_pk_mul_f32 v[130:131], v[130:131], s[22:23]
v_pk_mul_f32 v[132:133], v[132:133], s[22:23]
v_pk_mul_f32 v[134:135], v[134:135], s[22:23]
v_pk_mul_f32 v[40:41], v[40:41], s[22:23]
v_pk_mul_f32 v[42:43], v[42:43], s[22:23]
v_pk_mul_f32 v[44:45], v[44:45], s[22:23]
v_pk_mul_f32 v[46:47], v[46:47], s[22:23]
v_pk_mul_f32 v[72:73], v[72:73], s[22:23]
v_pk_mul_f32 v[74:75], v[74:75], s[22:23]
v_pk_mul_f32 v[76:77], v[76:77], s[22:23]
v_pk_mul_f32 v[78:79], v[78:79], s[22:23]
v_pk_mul_f32 v[104:105], v[104:105], s[22:23]
v_pk_mul_f32 v[106:107], v[106:107], s[22:23]
v_pk_mul_f32 v[108:109], v[108:109], s[22:23]
v_pk_mul_f32 v[110:111], v[110:111], s[22:23]
v_pk_mul_f32 v[136:137], v[136:137], s[22:23]
v_pk_mul_f32 v[138:139], v[138:139], s[22:23]
v_pk_mul_f32 v[140:141], v[140:141], s[22:23]
v_pk_mul_f32 v[142:143], v[142:143], s[22:23]
v_pk_mul_f32 v[48:49], v[48:49], s[22:23]
v_pk_mul_f32 v[50:51], v[50:51], s[22:23]
v_pk_mul_f32 v[52:53], v[52:53], s[22:23]
v_pk_mul_f32 v[54:55], v[54:55], s[22:23]
v_pk_mul_f32 v[80:81], v[80:81], s[22:23]
v_pk_mul_f32 v[82:83], v[82:83], s[22:23]
v_pk_mul_f32 v[84:85], v[84:85], s[22:23]
v_pk_mul_f32 v[86:87], v[86:87], s[22:23]
v_pk_mul_f32 v[112:113], v[112:113], s[22:23]
v_pk_mul_f32 v[114:115], v[114:115], s[22:23]
v_pk_mul_f32 v[116:117], v[116:117], s[22:23]
v_pk_mul_f32 v[118:119], v[118:119], s[22:23]
v_pk_mul_f32 v[144:145], v[144:145], s[22:23]
v_pk_mul_f32 v[146:147], v[146:147], s[22:23]
v_pk_mul_f32 v[148:149], v[148:149], s[22:23]
v_pk_mul_f32 v[150:151], v[150:151], s[22:23]
v_pk_mul_f32 v[56:57], v[56:57], s[22:23]
v_pk_mul_f32 v[58:59], v[58:59], s[22:23]
v_pk_mul_f32 v[60:61], v[60:61], s[22:23]
v_pk_mul_f32 v[62:63], v[62:63], s[22:23]
v_pk_mul_f32 v[88:89], v[88:89], s[22:23]
v_pk_mul_f32 v[90:91], v[90:91], s[22:23]
v_pk_mul_f32 v[92:93], v[92:93], s[22:23]
v_pk_mul_f32 v[94:95], v[94:95], s[22:23]
v_pk_mul_f32 v[120:121], v[120:121], s[22:23]
v_pk_mul_f32 v[122:123], v[122:123], s[22:23]
v_pk_mul_f32 v[124:125], v[124:125], s[22:23]
v_pk_mul_f32 v[126:127], v[126:127], s[22:23]
v_pk_mul_f32 v[152:153], v[152:153], s[22:23]
v_pk_mul_f32 v[154:155], v[154:155], s[22:23]
v_pk_mul_f32 v[156:157], v[156:157], s[22:23]
v_pk_mul_f32 v[158:159], v[158:159], s[22:23]
K 32,32,33
K 33,34,35
K 34,36,37
K 35,38,39
K 40,40,41
K 41,42,43
K 42,44,45
K 43,46,47
W 0
Zvav 196,128,32
s_add_u32 m0, s62, 0
Ra 144,242,4096
Ra 148,242,5120
Ra 152,242,6144
Ra 156,242,7168
Mvav 196,132,40
K 64,64,65
K 65,66,67
K 66,68,69
K 67,70,71
K 72,72,73
K 73,74,75
K 74,76,77
K 75,78,79
s_nop 1
Zvav 204,132,72
Dc 1,8,61,0
K 96,96,97
K 97,98,99
K 98,100,101
Mvav 204,128,64
K 99,102,103
K 104,104,105
K 105,106,107
K 106,108,109
K 107,110,111
Zvav 212,128,96
K 128,128,129
K 129,130,131
K 130,132,133
K 131,134,135
Mvav 212,132,104
K 136,136,137
K 137,138,139
K 138,140,141
K 139,142,143
s_nop 1
Zvav 220,132,136
Dc 1,8,61,1024
K 48,48,49
K 49,50,51
K 50,52,53
Mvav 220,128,128
K 51,54,55
K 56,56,57
K 57,58,59
K 58,60,61
K 59,62,63
Mvav 196,136,48
Ra 128,242,8192
Ra 132,242,9216
K 80,80,81
K 81,82,83
Mvav 196,140,56
K 82,84,85
K 83,86,87
K 88,88,89
K 89,90,91
K 90,92,93
K 91,94,95
s_nop 1
Mvav 204,140,88
Dc 1,8,61,2048
K 112,112,113
K 113,114,115
K 114,116,117
Mvav 204,136,80
K 115,118,119
K 120,120,121
K 121,122,123
K 122,124,125
K 123,126,127
Mvav 212,136,112
K 144,144,145
K 145,146,147
K 146,148,149
K 147,150,151
Mvav 212,140,120
K 152,152,153
K 153,154,155
K 154,156,157
K 155,158,159
s_nop 1
Mvav 220,140,152
Dc 1,8,61,3072
s_add_u32 m0, s62, 4096
v_accvgpr_write_b32 a0, v32
v_accvgpr_write_b32 a1, v33
v_accvgpr_write_b32 a2, v34
Mvav 220,136,144
W 2
Ra 136,242,10240
v_accvgpr_write_b32 a3, v35
v_accvgpr_write_b32 a4, v40
Zvav 36,144,32
Ra 140,242,11264
v_accvgpr_write_b32 a5, v41
Mvav 36,148,40
v_accvgpr_write_b32 a6, v42
v_accvgpr_write_b32 a7, v43
v_max3_f32 v4, v196, v197, v198
v_max3_f32 v4, v4, v199, v252
Zvav 68,148,72
v_max3_f32 v5, v204, v205, v206
v_max3_f32 v5, v5, v207, v253
v_max3_f32 v6, v212, v213, v214
v_max3_f32 v6, v6, v215, v254
Mvav 68,144,64
v_max3_f32 v7, v220, v221, v222
v_max3_f32 v7, v7, v223, v255
v_mov_b32 v8, v4
v_mov_b32 v9, v5
Zvav 100,144,96
Dc 243,8,61,0
v_mov_b32 v10, v6
v_mov_b32 v11, v7
v_permlane32_swap_b32 v4, v8
Mvav 100,148,104
v_permlane32_swap_b32 v5, v9
v_permlane32_swap_b32 v6, v10
v_permlane32_swap_b32 v7, v11
v_max_f32 v4, v4, v8
Zvav 132,148,136
v_max_f32 v5, v5, v9
v_max_f32 v6, v6, v10
v_max_f32 v7, v7, v11
v_mov_b32 v8, v4
Mvav 132,144,128
v_mov_b32 v9, v5
v_mov_b32 v10, v6
v_mov_b32 v11, v7
v_accvgpr_write_b32 a8, v48
Mvav 36,152,48
v_permlane16_swap_b32 v4, v8
v_permlane16_swap_b32 v5, v9
v_permlane16_swap_b32 v6, v10
)"
R"(v_permlane16_swap_b32 v7, v11
Mvav 36,156,56
v_max_f32 v4, v4, v8
v_max_f32 v5, v5, v9
v_max_f32 v6, v6, v10
v_max_f32 v7, v7, v11
Mvav 68,156,88
v_sub_f32 v4, 0, v4
v_sub_f32 v5, 0, v5
v_sub_f32 v6, 0, v6
v_sub_f32 v7, 0, v7
Mvav 68,152,80
v_mov_b32 v224, v4
v_mov_b32 v225, v4
v_mov_b32 v226, v4
v_mov_b32 v227, v4
Mvav 100,152,112
v_mov_b32 v228, v5
v_mov_b32 v229, v5
v_mov_b32 v230, v5
v_mov_b32 v231, v5
Mvav 100,156,120
v_mov_b32 v232, v6
v_mov_b32 v233, v6
v_mov_b32 v234, v6
v_mov_b32 v235, v6
Mvav 132,156,152
v_mov_b32 v236, v7
v_mov_b32 v237, v7
v_mov_b32 v238, v7
v_mov_b32 v239, v7
Mvav 132,152,144
W 0
v_accvgpr_write_b32 a9, v49
v_accvgpr_write_b32 a10, v50
v_accvgpr_write_b32 a11, v51
v_accvgpr_write_b32 a12, v56
Cvav 160,128,32,224
s_and_b32 s80, s14, 1
Dc 243,8,61,1024
v_accvgpr_write_b32 a13, v57
v_accvgpr_write_b32 a14, v58
v_accvgpr_write_b32 a15, v59
Mvav 160,132,40
s_lshl_b32 s80, s80, 14
Ra 144,242,12288
Ra 148,242,13312
v_accvgpr_write_b32 a16, v64
v_accvgpr_write_b32 a17, v65
Cvav 164,132,72,228
Dc 243,8,61,2048
v_accvgpr_write_b32 a18, v66
v_accvgpr_write_b32 a19, v67
v_accvgpr_write_b32 a20, v72
Mvav 164,128,64
s_add_u32 s80, s80, 98304
v_accvgpr_write_b32 a21, v73
v_accvgpr_write_b32 a22, v74
v_accvgpr_write_b32 a23, v75
v_accvgpr_write_b32 a24, v80
Cvav 168,128,96,232
Dc 243,8,61,3072
s_add_u32 s61, s61, s63
v_accvgpr_write_b32 a25, v81
v_accvgpr_write_b32 a26, v82
A 196,196,224
Mvav 168,132,104
s_add_u32 s36, s29, 65536
v_accvgpr_write_b32 a27, v83
A 197,197,224
A 198,198,224
v_accvgpr_write_b32 a28, v88
Cvav 172,132,136,236
Ra 152,242,14336
buffer_store_dwordx4 a[192:195], v1, s[8:11], s36 offen offset:0 sc1
A 199,199,224
v_accvgpr_write_b32 a29, v89
Mvav 172,128,128
Ra 156,242,15360
X 196
v_accvgpr_write_b32 a30, v90
Mvav 160,136,48
X 197
v_accvgpr_write_b32 a31, v91
v_accvgpr_write_b32 a32, v96
Mvav 160,140,56
buffer_store_dwordx4 a[200:203], v1, s[8:11], s36 offen offset:1024 sc1
X 198
K 192,196,197
Mvav 164,140,88
X 199
v_accvgpr_write_b32 a33, v97
A 204,204,228
Mvav 164,136,80
v_accvgpr_write_b32 a34, v98
K 193,198,199
v_accvgpr_write_b32 a35, v99
A 205,205,228
Mvav 168,136,112
A 206,206,228
v_accvgpr_write_b32 a36, v104
v_add_u32 v8, s80, v1
A 207,207,228
Mvav 168,140,120
buffer_store_dwordx4 a[208:211], v1, s[8:11], s36 offen offset:2048 sc1
X 204
v_accvgpr_write_b32 a37, v105
Mvav 172,140,152
X 205
v_accvgpr_write_b32 a38, v106
v_accvgpr_write_b32 a39, v107
Mvav 172,136,144
W 0
X 206
v_accvgpr_write_b32 a40, v112
K 200,204,205
Cvav 176,144,32,224
Ra 128,242,24576
X 207
v_accvgpr_write_b32 a41, v113
Mvav 176,148,40
buffer_store_dwordx4 a[216:219], v1, s[8:11], s36 offen offset:3072 sc1
s_add_u32 s36, s29, 69632
Ra 132,242,25600
A 212,212,232
v_accvgpr_write_b32 a42, v114
Cvav 180,148,72,228
K 201,206,207
A 213,213,232
v_accvgpr_write_b32 a43, v115
A 214,214,232
Mvav 180,144,64
A 215,215,232
v_accvgpr_write_b32 a44, v120
X 212
Cvav 184,144,96,232
buffer_store_dwordx4 a[224:227], v1, s[8:11], s36 offen offset:0 sc1
v_accvgpr_write_b32 a45, v121
X 213
Mvav 184,148,104
v_accvgpr_write_b32 a46, v122
X 214
v_accvgpr_write_b32 a47, v123
Cvav 188,148,136,236
K 208,212,213
v_accvgpr_write_b32 a48, v128
X 215
Mvav 188,144,128
buffer_store_dwordx4 a[232:235], v1, s[8:11], s36 offen offset:1024 sc1
v_accvgpr_write_b32 a49, v129
A 220,220,236
v_accvgpr_write_b32 a50, v130
Mvav 176,152,48
K 209,214,215
A 221,221,236
v_accvgpr_write_b32 a51, v131
A 222,222,236
Mvav 176,156,56
Ra 136,242,26624
A 223,223,236
v_accvgpr_write_b32 a52, v136
v_accvgpr_write_b32 a53, v137
Mvav 180,156,88
Ra 140,242,27648
buffer_store_dwordx4 a[240:243], v1, s[8:11], s36 offen offset:2048 sc1
X 220
Mvav 180,152,80
X 221
v_accvgpr_write_b32 a54, v138
v_accvgpr_write_b32 a55, v139
Mvav 184,152,112
v_accvgpr_write_b32 a56, v144
v_accvgpr_write_b32 a57, v145
v_accvgpr_write_b32 a58, v146
v_accvgpr_write_b32 a59, v147
v_accvgpr_write_b32 a60, v152
v_accvgpr_write_b32 a61, v153
v_accvgpr_write_b32 a62, v154
v_accvgpr_write_b32 a63, v155
Mvav 184,156,120
s_barrier
buffer_store_dwordx4 a[248:251], v1, s[8:11], s36 offen offset:3072 sc1
X 222
K 216,220,221
Mvav 188,156,152
X 223
A 36,36,224
K 217,222,223
A 37,37,224
A 38,38,224
A 39,39,224
X 36
X 37
X 38
K 194,36,37
X 39
A 68,68,228
K 195,38,39
A 69,69,228
A 70,70,228
A 71,71,228
X 68
X 69
X 70
K 202,68,69
X 71
A 100,100,232
K 203,70,71
A 101,101,232
A 102,102,232
A 103,103,232
X 100
X 101
X 102
K 210,100,101
X 103
A 132,132,236
K 211,102,103
A 133,133,236
A 134,134,236
A 135,135,236
X 132
X 133
X 134
K 218,132,133
X 135
s_nop 0
K 219,134,135
Mvav 188,152,144
W 0
s_add_u32 s36, s29, 393216
buffer_store_dwordx4 a[196:199], v1, s[8:11], s36 offen offset:0 sc1
Ra 144,242,28672
Ra 148,242,29696
Zvav 32,128,192
buffer_store_dwordx4 a[204:207], v1, s[8:11], s36 offen offset:1024 sc1
Ra 152,242,30720
Ra 156,242,31744
Zvav 64,128,200
buffer_store_dwordx4 a[212:215], v1, s[8:11], s36 offen offset:2048 sc1
v_xor_b32 v242, 0x10000, v242
Zvav 96,128,208
s_and_b32 s80, s14, 1
s_xor_b32 s62, s62, 0x10000
buffer_store_dwordx4 a[220:223], v1, s[8:11], s36 offen offset:3072 sc1
s_add_u32 s36, s29, 397312
Zvav 128,128,216
s_lshl_b32 s80, s80, 14
s_add_u32 s80, s80, 131072
buffer_store_dwordx4 a[228:231], v1, s[8:11], s36 offen offset:0 sc1
v_add_u32 v23, 61440, v21
Zvav 132,132,216
v_add_u32 v12, s80, v1
Ra 64,12,8192
Ra 68,12,9216
v_add_u32 v24, 61440, v22
Zvav 100,132,208
Ra 72,12,10240
buffer_store_dwordx4 a[236:239], v1, s[8:11], s36 offen offset:1024 sc1
Ra 76,12,11264
v_add_u32 v25, 122880, v21
Zvav 68,132,200
Ra 80,12,12288
Ra 84,12,13312
Ra 88,12,14336
v_add_u32 v26, 122880, v22
Zvav 36,132,192
Ra 92,12,15360
Ra 96,12,0
Ra 100,12,1024
Ra 104,12,2048
Zvav 40,136,192
buffer_store_dwordx4 a[244:247], v1, s[8:11], s36 offen offset:2048 sc1
Ra 108,12,3072
Ra 112,12,4096
Ra 116,12,5120
Zvav 72,136,200
Ra 120,12,6144
Ra 124,12,7168
X 172
Zvav 104,136,208
X 173
X 174
Zvav 136,136,216
buffer_store_dwordx4 a[252:255], v1, s[8:11], s36 offen offset:3072 sc1
K 220,172,173
X 175
Zvav 140,140,216
X 168
K 221,174,175
Zvav 108,140,208
X 169
X 170
Zvav 76,140,200
s_waitcnt vmcnt(16) lgkmcnt(0)
s_barrier
ds_read_b128 v[248:251], v8
K 212,168,169
Zvav 44,140,192
X 171
X 164
Zvav 48,144,192
Ra 128,242,0
K 213,170,171
X 165
Zvav 80,144,200
X 166
Zvav 112,144,208
Ra 132,242,1024
K 204,164,165
X 167
Zvav 144,144,216
X 160
Zvav 148,148,216
K 205,166,167
X 161
Zvav 116,148,208
Ra 136,242,2048
X 162
Zvav 84,148,200
W 3
K 196,160,161
X 163
Zvav 52,148,192
X 176
K 197,162,163
Zvav 56,152,192
Ra 140,242,3072
X 177
Zvav 88,152,200
X 178
K 198,176,177
Zvav 120,152,208
X 179
X 180
Zvav 152,152,216
K 199,178,179
Zvav 156,156,216
X 181
X 182
Zvav 124,156,208
K 206,180,181
X 183
Zvav 92,156,200
K 207,182,183
Zvav 60,156,192
s_waitcnt vmcnt(0)
s_add_u32 s80, s71, 128
v_mov_b64 v[28:29], s[12:13]
s_mov_b64 exec, 1
buffer_store_dwordx2 v[28:29], off, s[8:11], s80 sc1
s_mov_b64 exec, -1
s_branch .Lsp1_32%=
.p2align 12
.Lmoe_40%=:
W 0
Cvaa 160,128,0,224
s_cmp_eq_u32 s14, 0
s_cselect_b32 s20, s30, s32
s_cselect_b32 s21, s31, s33
Ra 144,242,4096
Ra 148,242,5120
Mvaa 160,132,4
s_xor_b32 s62, s62, 0x10000
Ra 152,242,6144
Cvaa 164,132,20,228
X 184
Mvaa 164,128,16
s_and_b32 s21, s21, 0xffff
X 185
Cvaa 168,128,32,232
X 186
Mvaa 168,132,36
Ra 156,242,7168
K 214,184,185
Cvaa 172,132,52,236
s_mov_b32 s22, 2211840
X 187
Mvaa 172,128,48
X 188
Mvaa 160,136,8
Ra 128,242,16384
Mvaa 160,140,12
s_mov_b32 s23, 0x20000
K 215,186,187
Mvaa 164,140,28
X 189
Mvaa 164,136,24
X 190
Mvaa 168,136,40
Ra 132,242,17408
v_lshrrev_b32 v4, 6, v1
Mvaa 168,140,44
K 222,188,189
Mvaa 172,140,60
X 191
Mvaa 172,136,56
v_mul_u32_u24 v4, 3456, v4
K 223,190,191
v_smfmac_f32_16x16x64_bf16 a[160:163], a[176:179], v[192:199], v240
)"
R"(Ra 136,242,18432
v_smfmac_f32_16x16x64_bf16 a[164:167], a[176:179], v[200:207], v240
X 160
v_smfmac_f32_16x16x64_bf16 a[168:171], a[176:179], v[208:215], v240
v_lshrrev_b32 v5, 4, v1
v_smfmac_f32_16x16x64_bf16 a[172:175], a[176:179], v[216:223], v240
W 3
X 161
Cvaa 176,144,0,224
X 162
Mvaa 176,148,4
Ra 140,242,19456
v_xor_b32 v5, v5, v1
Cvaa 180,148,20,228
K 192,160,161
Mvaa 180,144,16
X 163
Cvaa 184,144,32,232
v_and_b32 v5, 32, v5
Mvaa 184,148,36
X 164
Cvaa 188,148,52,236
K 193,162,163
v_and_b32 v6, 16, v1
Mvaa 188,144,48
X 165
Mvaa 176,152,8
X 166
Mvaa 176,156,12
Ra 144,242,20480
Ra 148,242,21504
Mvaa 180,156,28
v_add3_u32 v4, v4, v5, v6
K 200,164,165
Mvaa 180,152,24
X 167
Mvaa 184,152,40
X 168
Mvaa 184,156,44
v_add_u32 v5, 54272, v4
K 201,166,167
Mvaa 188,156,60
X 169
Mvaa 188,152,56
W 2
Ra 152,242,22528
v_add_u32 v6, 108544, v4
Mvav 32,128,196
X 170
Mvav 64,128,204
Ra 156,242,23552
v_add_u32 v7, 162816, v4
Mvav 96,128,212
K 208,168,169
Mvav 128,128,220
X 171
Mvav 132,132,220
s_lshl_b32 s94, s85, 6
X 172
Mvav 100,132,212
s_add_u32 s64, s64, 1
Ra 128,242,8192
K 209,170,171
Mvav 68,132,204
X 173
Mvav 36,132,196
s_mul_i32 s95, s14, 20480
Mvav 40,136,196
X 174
Mvav 72,136,204
Ra 132,242,9216
K 216,172,173
Mvav 104,136,212
s_add_u32 s95, s95, 98304
X 175
Mvav 136,136,220
Mvav 140,140,220
s_add_u32 m0, s95, 0
X 176
Mvav 108,140,212
Ra 136,242,10240
K 217,174,175
Mvav 76,140,204
X 177
Mvav 44,140,196
W 3
D 4,20,94,0
Mvav 48,144,196
X 178
Mvav 80,144,204
Ra 140,242,11264
Mvav 112,144,212
D 5,20,94,1024
K 194,176,177
Mvav 144,144,220
X 179
Mvav 148,148,220
X 180
Mvav 116,148,212
Ra 144,242,12288
D 6,20,94,2048
Mvav 84,148,204
K 195,178,179
Mvav 52,148,196
X 181
Mvav 56,152,196
D 7,20,94,3072
Ra 148,242,13312
Mvav 88,152,204
X 182
Mvav 120,152,212
s_add_u32 s94, s94, 221184
K 202,180,181
Mvav 152,152,220
X 183
Mvav 156,156,220
X 184
Mvav 124,156,212
s_add_u32 m0, s95, 4096
Ra 152,242,14336
Mvav 92,156,204
K 203,182,183
Mvav 60,156,196
W 3
X 185
Cvaa 160,128,0,224
D 4,20,94,0
Ra 156,242,15360
Mvaa 160,132,4
X 186
Cvaa 164,132,20,228
K 210,184,185
Mvaa 164,128,16
X 187
Cvaa 168,128,32,232
D 5,20,94,1024
Mvaa 168,132,36
X 188
Cvaa 172,132,52,236
D 6,20,94,2048
K 211,186,187
Mvaa 172,128,48
X 189
Mvaa 160,136,8
Ra 128,242,24576
Ra 132,242,25600
Mvaa 160,140,12
X 190
Mvaa 164,140,28
D 7,20,94,3072
K 218,188,189
Mvaa 164,136,24
s_add_u32 s94, s94, 221184
X 191
Mvaa 168,136,40
Mvaa 168,140,44
X 160
Mvaa 172,140,60
s_add_u32 m0, s95, 8192
K 219,190,191
Mvaa 172,136,56
W 2
X 161
Cvaa 176,144,0,224
Ra 136,242,26624
Ra 140,242,27648
Mvaa 176,148,4
X 162
Cvaa 180,148,20,228
D 4,20,94,0
K 196,160,161
Mvaa 180,144,16
X 163
Cvaa 184,144,32,232
D 5,20,94,1024
Mvaa 184,148,36
X 164
Cvaa 188,148,52,236
K 197,162,163
Mvaa 188,144,48
D 6,20,94,2048
Ra 144,242,28672
Mvaa 176,152,8
X 165
Mvaa 176,156,12
Ra 148,242,29696
D 7,20,94,3072
Mvaa 180,156,28
X 166
Mvaa 180,152,24
K 204,164,165
Mvaa 184,152,40
s_add_u32 s94, s94, 221184
X 167
Mvaa 184,156,44
X 168
Mvaa 188,156,60
Mvaa 188,152,56
W 2
s_add_u32 m0, s95, 12288
K 205,166,167
Mvav 32,128,192
X 169
Mvav 64,128,200
Ra 152,242,30720
Ra 156,242,31744
Mvav 96,128,208
X 170
Mvav 128,128,216
D 4,20,94,0
v_xor_b32 v242, 0x10000, v242
Mvav 132,132,216
K 212,168,169
Mvav 100,132,208
X 171
Mvav 68,132,200
D 5,20,94,1024
Mvav 36,132,192
X 172
Mvav 40,136,192
D 6,20,94,2048
K 213,170,171
Mvav 72,136,200
X 173
Mvav 104,136,208
X 174
Mvav 136,136,216
D 7,20,94,3072
K 220,172,173
Mvav 140,140,216
X 175
Mvav 108,140,208
s_add_u32 s94, s94, 221184
X 176
Mvav 76,140,200
W 0
s_barrier
Mvav 44,140,192
Ra 128,242,0
K 221,174,175
Mvav 48,144,192
X 177
Mvav 80,144,200
v_bfe_u32 v11, v1, 4, 3
Mvav 112,144,208
X 178
Mvav 144,144,216
Ra 132,242,1024
v_lshlrev_b32 v11, 3, v11
Mvav 148,148,216
K 198,176,177
Mvav 116,148,208
X 179
Mvav 84,148,200
X 180
Mvav 52,148,192
Ra 136,242,2048
buffer_load_dwordx2 v[8:9], v11, s[8:11], 0 offen offset:3840 sc1
Mvav 56,152,192
Ra 140,242,3072
K 199,178,179
Mvav 88,152,200
X 181
Mvav 120,152,208
X 182
Mvav 152,152,216
K 206,180,181
Mvav 156,156,216
X 183
Mvav 124,156,208
Mvav 92,156,200
K 207,182,183
Mvav 60,156,192
W 0
Cvaa 160,128,0,224
s_add_u32 m0, s95, 16384
Ra 144,242,4096
Ra 148,242,5120
Mvaa 160,132,4
s_xor_b32 s62, s62, 0x10000
Ra 152,242,6144
Cvaa 164,132,20,228
X 184
Mvaa 164,128,16
X 185
Cvaa 168,128,32,232
X 186
Mvaa 168,132,36
D 4,20,94,0
K 214,184,185
Cvaa 172,132,52,236
Ra 156,242,7168
Mvaa 172,128,48
X 187
Mvaa 160,136,8
X 188
Mvaa 160,140,12
D 5,20,94,1024
Ra 128,242,16384
Mvaa 164,140,28
K 215,186,187
Mvaa 164,136,24
X 189
Mvaa 168,136,40
X 190
Mvaa 168,140,44
D 6,20,94,2048
Ra 132,242,17408
Mvaa 172,140,60
K 222,188,189
Mvaa 172,136,56
X 191
v_smfmac_f32_16x16x64_bf16 a[160:163], a[176:179], v[192:199], v240
Ra 136,242,18432
K 223,190,191
v_smfmac_f32_16x16x64_bf16 a[164:167], a[176:179], v[200:207], v240
D 7,20,94,3072
v_smfmac_f32_16x16x64_bf16 a[168:171], a[176:179], v[208:215], v240
s_add_u32 s94, s94, 221184
v_mul_u32_u24 v10, 160, v11
v_smfmac_f32_16x16x64_bf16 a[172:175], a[176:179], v[216:223], v240
W 3
X 160
Cvaa 176,144,0,224
X 161
Mvaa 176,148,4
Ra 140,242,19456
v_lshrrev_b32 v12, 7, v1
Cvaa 180,148,20,228
X 162
Mvaa 180,144,16
K 192,160,161
v_lshl_add_u32 v10, v12, 4, v10
Cvaa 184,144,32,232
s_mul_i32 s82, s14, 5120
X 163
Mvaa 184,148,36
X 164
Cvaa 188,148,52,236
K 193,162,163
Mvaa 188,144,48
s_mul_i32 s81, s14, 640
X 165
Mvaa 176,152,8
X 166
Mvaa 176,156,12
s_add_u32 s81, s81, 118784
Ra 144,242,20480
Ra 148,242,21504
Mvaa 180,156,28
K 200,164,165
Mvaa 180,152,24
X 167
Mvaa 184,152,40
s_waitcnt vmcnt(4)
v_cmp_eq_u64 vcc, s[12:13], v[8:9]
s_cmp_eq_u64 vcc, -1
s_cbranch_scc0 .Lspin_47%=
.Lok_48%=:
X 168
Mvaa 184,156,44
K 201,166,167
Mvaa 188,156,60
s_add_u32 m0, s82, 53248
X 169
Mvaa 188,152,56
W 2
Ra 152,242,22528
Mvav 32,128,196
X 170
Mvav 64,128,204
Ra 156,242,23552
Dc 10,8,81,0
Mvav 96,128,212
K 208,168,169
Mvav 128,128,220
X 171
Mvav 132,132,220
s_add_u32 m0, s82, 54144
X 172
Mvav 100,132,212
s_add_u32 s64, s64, 1
Ra 128,242,8192
K 209,170,171
Mvav 68,132,204
X 173
Mvav 36,132,196
Dc 10,8,81,128
Mvav 40,136,196
X 174
Mvav 72,136,204
s_add_u32 m0, s82, 55040
Ra 132,242,9216
K 216,172,173
Mvav 104,136,212
X 175
Mvav 136,136,220
Dc 10,8,81,256
Mvav 140,140,220
X 176
)"
R"(Mvav 108,140,212
Ra 136,242,10240
K 217,174,175
Mvav 76,140,204
s_add_u32 m0, s82, 55936
X 177
Mvav 44,140,196
W 3
Mvav 48,144,196
X 178
Mvav 80,144,204
Ra 140,242,11264
Dc 10,8,81,384
Mvav 112,144,212
K 194,176,177
Mvav 144,144,220
s_add_u32 m0, s82, 56832
X 179
Mvav 148,148,220
X 180
Mvav 116,148,212
Ra 144,242,12288
Mvav 84,148,204
Dc 10,8,81,512
K 195,178,179
Mvav 52,148,196
X 181
Mvav 56,152,196
X 182
Mvav 88,152,204
Ra 148,242,13312
Mvav 120,152,212
K 202,180,181
Mvav 152,152,220
X 183
Mvav 156,156,220
X 184
Mvav 124,156,212
Ra 152,242,14336
Mvav 92,156,204
K 203,182,183
Mvav 60,156,196
W 3
X 185
Cvaa 160,128,0,224
X 186
Mvaa 160,132,4
Ra 156,242,15360
Cvaa 164,132,20,228
K 210,184,185
Mvaa 164,128,16
X 187
Cvaa 168,128,32,232
X 188
Mvaa 168,132,36
Cvaa 172,132,52,236
K 211,186,187
Mvaa 172,128,48
X 189
Mvaa 160,136,8
Ra 128,242,24576
Ra 132,242,25600
Mvaa 160,140,12
X 190
Mvaa 164,140,28
K 218,188,189
Mvaa 164,136,24
X 191
Mvaa 168,136,40
Mvaa 168,140,44
Mvaa 172,140,60
X 160
Mvaa 172,136,56
W 2
Ra 136,242,26624
K 219,190,191
Cvaa 176,144,0,224
X 161
Mvaa 176,148,4
Ra 140,242,27648
v_lshrrev_b32 v8, 8, v1
Cvaa 180,148,20,228
X 162
Mvaa 180,144,16
K 196,160,161
Cvaa 184,144,32,232
X 163
Mvaa 184,148,36
v_bfe_u32 v9, v1, 6, 2
Cvaa 188,148,52,236
X 164
Mvaa 188,144,48
K 197,162,163
v_lshl_add_u32 v9, v8, 3, v9
Mvaa 176,152,8
X 165
Mvaa 176,156,12
Ra 144,242,28672
Ra 148,242,29696
Mvaa 180,156,28
X 166
Mvaa 180,152,24
K 204,164,165
v_lshlrev_b32 v11, 6, v9
Mvaa 184,152,40
X 167
Mvaa 184,156,44
X 168
Mvaa 188,156,60
v_xor_b32 v9, s14, v8
Mvaa 188,152,56
W 2
Ra 152,242,30720
K 205,166,167
Mvav 32,128,192
X 169
Mvav 64,128,200
Ra 156,242,31744
v_and_b32 v9, 1, v9
Mvav 96,128,208
X 170
Mvav 128,128,216
v_xor_b32 v242, 0x10000, v242
K 212,168,169
Mvav 132,132,216
X 171
Mvav 100,132,208
v_lshl_add_u32 v11, v9, 5, v11
Mvav 68,132,200
X 172
Mvav 36,132,192
K 213,170,171
Mvav 40,136,192
X 173
Mvav 72,136,200
v_bfe_u32 v9, v1, 4, 2
Mvav 104,136,208
X 174
Mvav 136,136,216
K 220,172,173
v_lshl_add_u32 v12, v9, 3, v11
Mvav 140,140,216
X 175
Mvav 108,140,208
X 176
Mvav 76,140,200
s_waitcnt vmcnt(0) lgkmcnt(0)
s_barrier
Mvav 44,140,192
v_add_u32 v11, 98304, v12
K 221,174,175
Mvav 48,144,192
Ra 128,242,0
Mvav 80,144,200
X 177
Mvav 112,144,208
X 178
Mvav 144,144,216
Ra 132,242,1024
v_and_b32 v13, 0x70, v1
Mvav 148,148,216
K 198,176,177
Mvav 116,148,208
X 179
Mvav 84,148,200
Ra 136,242,2048
v_lshl_add_u32 v13, v8, 7, v13
Mvav 52,148,192
X 180
Mvav 56,152,192
Ra 140,242,3072
K 199,178,179
Mvav 88,152,200
X 181
Mvav 120,152,208
X 182
Mvav 152,152,216
K 206,180,181
Mvav 156,156,216
X 183
Mvav 124,156,208
Mvav 92,156,200
K 207,182,183
Mvav 60,156,192
W 0
Cvaa 160,128,0,224
s_cmp_eq_u32 s14, 0
s_cselect_b32 s95, 106496, 0
Ra 144,242,4096
Ra 148,242,5120
Mvaa 160,132,4
s_xor_b32 s62, s62, 0x10000
X 184
Cvaa 164,132,20,228
Ta 184,11,0
Ta 186,11,256
Mvaa 164,128,16
s_add_u32 s95, s95, 32768
Ta 188,11,20480
Ta 190,11,20736
Cvaa 168,128,32,232
Ra 192,13,53248
Ta 196,11,2048
Mvaa 168,132,36
s_add_u32 m0, s95, 0
X 185
Cvaa 172,132,52,236
Ta 198,11,2304
Ta 200,11,22528
Mvaa 172,128,48
W 3
X 186
Zaaa 232,184,192
Ta 202,11,22784
K 214,184,185
Zaaa 236,188,192
Ra 204,13,53760
Ra 152,242,6144
Mvaa 160,136,8
X 187
Mvaa 160,140,12
Ta 208,11,4096
D 4,20,94,0
Mvaa 164,140,28
X 188
Mvaa 164,136,24
Ta 210,11,4352
D 5,20,94,1024
Mvaa 168,136,40
Ra 156,242,7168
K 215,186,187
Mvaa 168,140,44
X 189
Mvaa 172,140,60
Ta 212,11,24576
D 6,20,94,2048
Mvaa 172,136,56
X 190
v_smfmac_f32_16x16x64_bf16 a[160:163], a[176:179], v[192:199], v240
W 5
Ta 214,11,24832
D 7,20,94,3072
Maaa 232,196,204
s_add_u32 s94, s94, 221184
Ra 216,13,54272
K 222,188,189
Maaa 236,200,204
s_add_u32 m0, s95, 4096
X 191
v_smfmac_f32_16x16x64_bf16 a[164:167], a[176:179], v[200:207], v240
X 160
K 223,190,191
v_smfmac_f32_16x16x64_bf16 a[168:171], a[176:179], v[208:215], v240
Ra 128,242,16384
Ta 220,11,6144
v_smfmac_f32_16x16x64_bf16 a[172:175], a[176:179], v[216:223], v240
W 5
Ta 222,11,6400
Ra 132,242,17408
Cvaa 176,144,0,224
X 161
Mvaa 176,148,4
D 4,20,94,0
Ta 224,11,26624
Cvaa 180,148,20,228
X 162
Mvaa 180,144,16
D 5,20,94,1024
K 192,160,161
Cvaa 184,144,32,232
Ta 226,11,26880
Ra 228,13,54784
Mvaa 184,148,36
W 7
X 163
Maaa 232,208,216
D 6,20,94,2048
Ra 136,242,18432
Maaa 236,212,216
X 164
Cvaa 188,148,52,236
D 7,20,94,3072
Ta 184,11,8192
Mvaa 188,144,48
s_add_u32 s94, s94, 221184
Ra 140,242,19456
K 193,162,163
Mvaa 176,152,8
s_add_u32 m0, s95, 8192
X 165
Mvaa 176,156,12
Ta 186,11,8448
Ta 188,11,28672
Mvaa 180,156,28
X 166
Mvaa 180,152,24
Ra 144,242,20480
K 200,164,165
Mvaa 184,152,40
X 167
Mvaa 184,156,44
Ta 190,11,28928
D 4,20,94,0
Mvaa 188,156,60
W 7
X 168
Maaa 232,220,228
s_add_u32 s64, s64, 1
Ra 192,13,55296
D 5,20,94,1024
Maaa 236,224,228
Ta 196,11,10240
K 201,166,167
Mvaa 188,152,56
W 6
X 169
Mvav 32,128,196
D 6,20,94,2048
Ra 148,242,21504
Mvav 64,128,204
X 170
Mvav 96,128,212
Ta 198,11,10496
D 7,20,94,3072
Mvav 128,128,220
s_add_u32 s94, s94, 221184
Ta 200,11,30720
K 208,168,169
Mvav 132,132,220
s_add_u32 m0, s95, 12288
X 171
Mvav 100,132,212
Ra 152,242,22528
Ta 202,11,30976
Mvav 68,132,204
X 172
Mvav 36,132,196
W 6
Ra 204,13,55808
K 209,170,171
Maaa 232,184,192
X 173
Maaa 236,188,192
D 4,20,94,0
Ra 156,242,23552
Mvav 40,136,196
X 174
Mvav 72,136,204
Ta 208,11,12288
D 5,20,94,1024
Mvav 104,136,212
Ta 210,11,12544
K 216,172,173
Mvav 136,136,220
X 175
Mvav 140,140,220
D 6,20,94,2048
Ra 128,242,8192
Mvav 108,140,212
Ta 212,11,32768
Ta 214,11,33024
Mvav 76,140,204
X 176
Mvav 44,140,196
W 5
D 7,20,94,3072
K 217,174,175
Mvav 48,144,196
s_add_u32 s94, s94, 221184
X 177
Maaa 232,196,204
s_add_u32 m0, s95, 16384
Ra 132,242,9216
Ra 216,13,56320
Maaa 236,200,204
X 178
Mvav 80,144,204
Ta 220,11,14336
K 194,176,177
Mvav 112,144,212
X 179
Mvav 144,144,220
D 4,20,94,0
Ta 222,11,14592
Mvav 148,148,220
Ra 136,242,10240
Ta 224,11,34816
Mvav 116,148,212
X 180
Mvav 84,148,204
D 5,20,94,1024
K 195,178,179
Mvav 52,148,196
X 181
Mvav 56,152,196
D 6,20,94,2048
Ta 226,11,35072
Mvav 88,152,204
W 5
Ra 140,242,11264
)"
R"(Ra 228,13,56832
Maaa 232,208,216
X 182
Maaa 236,212,216
D 7,20,94,3072
K 202,180,181
Mvav 120,152,212
s_add_u32 s94, s94, 221184
X 183
Mvav 152,152,220
Ta 184,11,16384
Ra 144,242,12288
Mvav 156,156,220
X 184
Mvav 124,156,212
Ta 186,11,16640
K 203,182,183
Mvav 92,156,204
X 185
Mvav 60,156,196
W 4
Ta 188,11,36864
Ra 148,242,13312
Cvaa 160,128,0,224
X 186
Mvaa 160,132,4
Ta 190,11,37120
K 210,184,185
Cvaa 164,132,20,228
W 6
Ra 192,13,57344
Ta 196,11,18432
Maaa 232,220,228
X 187
Maaa 236,224,228
X 188
Mvaa 164,128,16
Ra 152,242,14336
K 211,186,187
Cvaa 168,128,32,232
Ta 198,11,18688
Ta 200,11,38912
Mvaa 168,132,36
X 189
Cvaa 172,132,52,236
X 190
Mvaa 172,128,48
Ra 156,242,15360
K 218,188,189
Mvaa 160,136,8
Ta 202,11,39168
Ra 204,13,57856
Mvaa 160,140,12
X 191
Mvaa 164,140,28
Ra 128,242,24576
Mvaa 164,136,24
W 8
Maaa 232,184,192
Ra 132,242,25600
Maaa 236,188,192
X 160
Mvaa 168,136,40
K 219,190,191
Mvaa 168,140,44
X 161
Mvaa 172,140,60
X 162
Mvaa 172,136,56
W 4
Ra 136,242,26624
K 196,160,161
Cvaa 176,144,0,224
X 163
Mvaa 176,148,4
X 164
Cvaa 180,148,20,228
Ra 140,242,27648
K 197,162,163
Mvaa 180,144,16
X 165
Cvaa 184,144,32,232
W 4
Maaa 232,196,204
X 166
Maaa 236,200,204
Mvaa 184,148,36
K 204,164,165
Cvaa 188,148,52,236
X 167
Mvaa 188,144,48
Ra 144,242,28672
Mvaa 176,152,8
X 168
Mvaa 176,156,12
Ra 148,242,29696
Mvaa 180,156,28
K 205,166,167
Mvaa 180,152,24
X 169
Mvaa 184,152,40
Mvaa 184,156,44
X 170
Mvaa 188,156,60
K 212,168,169
Mvaa 188,152,56
W 2
X 171
Mvav 32,128,192
Ra 152,242,30720
Ra 156,242,31744
Mvav 64,128,200
X 172
Mvav 96,128,208
v_xor_b32 v242, 0x10000, v242
Mvav 128,128,216
K 213,170,171
Mvav 132,132,216
X 173
Mvav 100,132,208
Mvav 68,132,200
X 174
Mvav 36,132,192
Mvav 40,136,192
K 220,172,173
Mvav 72,136,200
X 175
Mvav 104,136,208
Mvav 136,136,216
X 176
Mvav 140,140,216
Mvav 108,140,208
K 221,174,175
Mvav 76,140,200
s_waitcnt vmcnt(0) lgkmcnt(0)
s_barrier
Mvav 44,140,192
X 177
Mvav 48,144,192
X 178
Mvav 80,144,200
Ra 128,242,0
K 198,176,177
Mvav 112,144,208
X 179
Mvav 144,144,216
Ra 132,242,1024
Mvav 148,148,216
X 180
Mvav 116,148,208
Ra 136,242,2048
Mvav 84,148,200
K 199,178,179
Mvav 52,148,192
X 181
Mvav 56,152,192
Ra 140,242,3072
Mvav 88,152,200
X 182
Mvav 120,152,208
Mvav 152,152,216
K 206,180,181
Mvav 156,156,216
X 183
Mvav 124,156,208
Mvav 92,156,200
K 207,182,183
Mvav 60,156,192
W 0
Cvaa 160,128,0,224
s_mov_b32 s20, s34
Ra 144,242,4096
Ra 148,242,5120
Mvaa 160,132,4
s_xor_b32 s62, s62, 0x10000
X 184
Cvaa 164,132,20,228
s_and_b32 s21, s35, 0xffff
Ta 184,11,40960
Ta 186,11,41216
Mvaa 164,128,16
Ta 188,12,32768
Ta 190,12,33024
Cvaa 168,128,32,232
s_mov_b32 s22, 2211840
Ra 192,13,58368
Ta 196,11,43008
Mvaa 168,132,36
s_mov_b32 s23, 0x20000
X 185
Cvaa 172,132,52,236
Ta 198,11,43264
Ta 200,12,34816
Mvaa 172,128,48
W 3
X 186
Maaa 232,184,192
Ta 202,12,35072
K 214,184,185
Maaa 236,188,192
Ra 204,13,58880
Ra 152,242,6144
Mvaa 160,136,8
X 187
Mvaa 160,140,12
Ta 208,11,45056
v_lshrrev_b32 v4, 8, v1
Mvaa 164,140,28
X 188
Mvaa 164,136,24
Ta 210,11,45312
v_lshlrev_b32 v5, 5, v4
Mvaa 168,136,40
v_xor_b32 v5, v5, v1
K 215,186,187
Mvaa 168,140,44
Ra 156,242,7168
Ta 212,12,36864
Mvaa 172,140,60
X 189
Mvaa 172,136,56
v_and_b32 v5, 0xf0, v5
v_mul_u32_u24 v4, 1280, v4
v_smfmac_f32_16x16x64_bf16 a[160:163], a[176:179], v[192:199], v240
W 5
X 190
Maaa 232,196,204
Ta 214,12,37120
v_add_u32 v4, v4, v5
Maaa 236,200,204
Ra 216,13,59392
K 222,188,189
v_smfmac_f32_16x16x64_bf16 a[164:167], a[176:179], v[200:207], v240
X 191
X 160
K 223,190,191
v_smfmac_f32_16x16x64_bf16 a[168:171], a[176:179], v[208:215], v240
Ra 128,242,16384
v_xor_b32 v5, 128, v4
v_smfmac_f32_16x16x64_bf16 a[172:175], a[176:179], v[216:223], v240
W 4
Ta 220,11,47104
v_add_u32 v6, 8192, v4
Cvaa 176,144,0,224
Ta 222,11,47360
v_add_u32 v7, 12288, v5
Mvaa 176,148,4
Ra 132,242,17408
v_add_u32 v5, 4096, v5
Cvaa 180,148,20,228
s_mul_i32 s94, s85, 40960
X 161
Mvaa 180,144,16
s_mul_i32 s81, s14, 20480
Ta 224,12,38912
Ta 226,12,39168
Cvaa 184,144,32,232
s_add_u32 s94, s94, s81
X 162
Mvaa 184,148,36
W 6
s_lshl_b32 s95, s14, 12
K 192,160,161
Maaa 232,208,216
s_add_u32 s95, s95, 98304
X 163
Maaa 236,212,216
s_add_u32 m0, s95, 0
Ra 228,13,59904
Ra 136,242,18432
Cvaa 188,148,52,236
X 164
Mvaa 188,144,48
Ta 184,11,49152
K 193,162,163
Mvaa 176,152,8
X 165
Mvaa 176,156,12
Ra 140,242,19456
Ta 186,11,49408
Mvaa 180,156,28
D 4,20,94,0
Ta 188,12,40960
Mvaa 180,152,24
X 166
Mvaa 184,152,40
D 5,20,94,1024
K 200,164,165
Mvaa 184,156,44
X 167
Mvaa 188,156,60
W 5
D 6,20,94,2048
Ra 144,242,20480
Maaa 232,220,228
s_add_u32 s64, s64, 1
Ta 190,12,41216
Ra 192,13,60416
Maaa 236,224,228
D 7,20,94,3072
Ta 196,11,51200
Mvaa 188,152,56
W 6
X 168
Mvav 32,128,196
s_add_u32 s94, s94, 256
s_add_u32 m0, s95, 8192
K 201,166,167
Mvav 64,128,204
X 169
Mvav 96,128,212
D 4,20,94,0
Ra 148,242,21504
Mvav 128,128,220
X 170
Mvav 132,132,220
D 5,20,94,1024
Ta 198,11,51456
Mvav 100,132,212
Ta 200,12,43008
K 208,168,169
Mvav 68,132,204
D 6,20,94,2048
Ra 152,242,22528
Mvav 36,132,196
W 5
X 171
Maaa 232,184,192
D 7,20,94,3072
Ta 202,12,43264
Maaa 236,188,192
s_add_u32 s94, s94, 256
X 172
Mvav 40,136,196
s_add_u32 m0, s95, 16384
Ra 204,13,60928
K 209,170,171
Mvav 72,136,204
X 173
Mvav 104,136,212
Ra 156,242,23552
D 4,20,94,0
Mvav 136,136,220
X 174
Mvav 140,140,220
D 5,20,94,1024
Ta 208,11,53248
Mvav 108,140,212
Ta 210,11,53504
K 216,172,173
Mvav 76,140,204
D 6,20,94,2048
Ra 128,242,8192
Mvav 44,140,196
W 3
X 175
Mvav 48,144,196
D 7,20,94,3072
Ta 212,12,45056
Maaa 232,196,204
s_add_u32 s94, s94, 256
X 176
Maaa 236,200,204
s_add_u32 m0, s95, 24576
Ta 214,12,45312
K 217,174,175
Mvav 80,144,204
D 4,20,94,0
Ra 132,242,9216
Mvav 112,144,212
X 177
Mvav 144,144,220
Ra 216,13,61440
D 5,20,94,1024
Mvav 148,148,220
X 178
Mvav 116,148,212
D 6,20,94,2048
Ta 220,11,55296
Mvav 84,148,204
Ta 222,11,55552
K 194,176,177
Mvav 52,148,196
D 7,20,94,3072
Ra 136,242,10240
Mvav 56,152,196
s_add_u32 s94, s94, 256
X 179
Mvav 88,152,204
W 3
X 180
Maaa 232,208,216
s_add_u32 m0, s95, 32768
Ta 224,12,47104
K 195,178,179
Maaa 236,212,216
D 4,20,94,0
Ta 226,12,47360
Mvav 120,152,212
Ra 140,242,11264
)"
R"(Ra 228,13,61952
Mvav 152,152,220
X 181
Mvav 156,156,220
D 5,20,94,1024
Ta 184,11,57344
Mvav 124,156,212
X 182
Mvav 92,156,204
D 6,20,94,2048
K 202,180,181
Mvav 60,156,196
W 2
X 183
Cvaa 160,128,0,224
D 7,20,94,3072
Ra 144,242,12288
Mvaa 160,132,4
s_add_u32 s94, s94, 256
X 184
Cvaa 164,132,20,228
W 2
Ta 186,11,57600
K 203,182,183
Maaa 232,220,228
X 185
Maaa 236,224,228
Ta 188,12,49152
Ra 148,242,13312
Mvaa 164,128,16
X 186
Cvaa 168,128,32,232
Ta 190,12,49408
K 210,184,185
Mvaa 168,132,36
Ra 192,13,62464
Ta 196,11,59392
Cvaa 172,132,52,236
X 187
Mvaa 172,128,48
X 188
Mvaa 160,136,8
Ra 152,242,14336
K 211,186,187
Mvaa 160,140,12
Ta 198,11,59648
Ta 200,12,51200
Mvaa 164,140,28
X 189
Mvaa 164,136,24
W 4
X 190
Maaa 232,184,192
Ra 156,242,15360
K 218,188,189
Maaa 236,188,192
Ta 202,12,51456
Ra 204,13,62976
Mvaa 168,136,40
X 191
Mvaa 168,140,44
Ra 128,242,24576
Ra 132,242,25600
Mvaa 172,140,60
X 160
Mvaa 172,136,56
W 4
Ra 136,242,26624
K 219,190,191
Cvaa 176,144,0,224
X 161
Mvaa 176,148,4
X 162
Cvaa 180,148,20,228
Ra 140,242,27648
K 196,160,161
Mvaa 180,144,16
X 163
Cvaa 184,144,32,232
W 4
X 164
Maaa 232,196,204
K 197,162,163
Maaa 236,200,204
X 165
Mvaa 184,148,36
X 166
Cvaa 188,148,52,236
K 204,164,165
Mvaa 188,144,48
X 167
Mvaa 176,152,8
Ra 144,242,28672
G 252,232
Mvaa 176,156,12
G 253,233
G 254,234
Mvaa 180,156,28
X 168
Mvaa 180,152,24
G 255,235
G 8,236
Mvaa 184,152,40
Ra 148,242,29696
G 9,237
Mvaa 184,156,44
G 10,238
G 14,239
Mvaa 188,156,60
K 252,252,8
K 253,253,9
Mvaa 188,152,56
W 2
K 205,166,167
K 254,254,10
Mvav 32,128,192
K 255,255,14
v_and_b32 v8, 0xffff0000, v252
Mvav 64,128,200
v_and_b32 v9, 0xffff0000, v253
v_and_b32 v10, 0xffff0000, v254
Mvav 96,128,208
X 169
Mvav 128,128,216
v_and_b32 v14, 0xffff0000, v255
v_lshlrev_b32 v252, 16, v252
Mvav 132,132,216
v_lshlrev_b32 v253, 16, v253
v_lshlrev_b32 v254, 16, v254
Mvav 100,132,208
v_lshlrev_b32 v255, 16, v255
v_mul_f32 v15, 0xbfb8aa3b, v252
Mvav 68,132,200
v_mul_f32 v2, 0xbfb8aa3b, v253
v_mul_f32 v3, 0xbfb8aa3b, v254
Mvav 36,132,192
X 170
Mvav 40,136,192
v_mul_f32 v27, 0xbfb8aa3b, v255
K 212,168,169
Mvav 72,136,200
X 15
Mvav 104,136,208
Ra 152,242,30720
Ra 156,242,31744
Mvav 136,136,216
X 2
X 3
X 27
v_add_f32 v15, 1.0, v15
v_add_f32 v2, 1.0, v2
v_add_f32 v3, 1.0, v3
v_add_f32 v27, 1.0, v27
v_rcp_f32 v15, v15
v_rcp_f32 v2, v2
v_rcp_f32 v3, v3
v_rcp_f32 v27, v27
U 252,252,15
U 253,253,2
U 254,254,3
U 255,255,27
K 252,252,252
K 253,253,253
K 254,254,254
K 255,255,255
v_lshlrev_b32 v252, 16, v252
v_lshlrev_b32 v253, 16, v253
v_lshlrev_b32 v254, 16, v254
v_lshlrev_b32 v255, 16, v255
U 252,252,8
U 253,253,9
U 254,254,10
U 255,255,14
K 246,252,253
K 247,254,255
s_lshl_b32 s81, s14, 3
v_add_u32 v2, s81, v1
s_mov_b32 s86, 0x00ff00ff
s_mov_b32 s87, 0x00ff00ff
v_cndmask_b32_e64 v252, 0, v246, s[86:87]
v_cndmask_b32_e64 v253, 0, v247, s[86:87]
v_cndmask_b32_e64 v246, v246, 0, s[86:87]
v_cndmask_b32_e64 v247, v247, 0, s[86:87]
ds_write_b64 v2, v[252:253] offset:63488
ds_write_b64 v2, v[246:247] offset:64512
Mvav 140,140,216
X 171
Mvav 108,140,208
v_xor_b32 v242, 0x10000, v242
Mvav 76,140,200
s_waitcnt vmcnt(0) lgkmcnt(0)
s_barrier
Mvav 44,140,192
X 172
Mvav 48,144,192
Ra 128,242,0
K 213,170,171
Mvav 80,144,200
X 173
Mvav 112,144,208
X 174
Mvav 144,144,216
Ra 132,242,1024
K 220,172,173
Mvav 148,148,216
X 175
Mvav 116,148,208
X 176
Mvav 84,148,200
Ra 136,242,2048
K 221,174,175
Mvav 52,148,192
X 177
Mvav 56,152,192
X 178
Mvav 88,152,200
Ra 140,242,3072
K 198,176,177
Mvav 120,152,208
X 179
Mvav 152,152,216
X 180
Mvav 156,156,216
K 199,178,179
Mvav 124,156,208
X 181
Mvav 92,156,200
X 182
Mvav 60,156,192
K 206,180,181
X 183
s_nop 0
K 207,182,183
W 0
Cvaa 160,128,0,224
Ra 144,242,4096
Ra 148,242,5120
Mvaa 160,132,4
s_xor_b32 s62, s62, 0x10000
X 184
Cvaa 164,132,20,228
Ra 216,1,63488
Ra 220,1,64512
Mvaa 164,128,16
v_lshrrev_b32 v8, 6, v1
v_and_b32 v9, 7, v8
Cvaa 168,128,32,232
s_lshl_b32 s81, s14, 2
X 185
Mvaa 168,132,36
v_xor_b32 v9, s81, v9
v_lshlrev_b32 v9, 5, v9
Cvaa 172,132,52,236
X 186
Mvaa 172,128,48
v_lshl_add_u32 v4, v8, 8, v9
v_bfe_u32 v9, v1, 4, 2
Mvaa 160,136,8
v_lshl_add_u32 v4, v9, 3, v4
v_add_u32 v4, 98304, v4
Mvaa 160,140,12
K 214,184,185
v_xor_b32 v5, 32, v4
Mvaa 164,140,28
v_xor_b32 v6, 64, v4
v_xor_b32 v7, 96, v4
Ta 184,4,0
Ta 186,4,4096
Mvaa 164,136,24
Ta 188,5,0
Ta 190,5,4096
Mvaa 168,136,40
Ra 152,242,6144
Ta 192,6,0
Mvaa 168,140,44
W 4
X 187
Zaaa 232,184,216
Ta 194,6,4096
v_and_b32 v11, 0x70, v1
Mvaa 172,140,60
v_mul_u32_u24 v11, 160, v11
v_lshrrev_b32 v10, 8, v1
Mvaa 172,136,56
X 188
v_smfmac_f32_16x16x64_bf16 a[160:163], a[176:179], v[192:199], v240
Ra 156,242,7168
W 4
Maaa 232,188,220
K 215,186,187
v_lshl_add_u32 v11, v10, 4, v11
v_smfmac_f32_16x16x64_bf16 a[164:167], a[176:179], v[200:207], v240
X 189
X 190
K 222,188,189
X 191
X 160
K 223,190,191
v_smfmac_f32_16x16x64_bf16 a[168:171], a[176:179], v[208:215], v240
Ta 196,7,0
v_bfe_u32 v10, v1, 7, 1
v_smfmac_f32_16x16x64_bf16 a[172:175], a[176:179], v[216:223], v240
W 2
Ta 198,7,4096
v_lshl_add_u32 v10, v10, 6, v11
Zaaa 236,192,216
W 2
s_mul_i32 s94, s85, 20480
s_lshl_b32 s82, s14, 8
Cvaa 176,144,0,224
s_add_u32 s94, s94, s82
Ta 200,4,8192
Ta 202,4,12288
Mvaa 176,148,4
s_add_u32 s94, s94, 2752512
s_lshl_b32 s71, s85, 4
Ra 128,242,16384
Cvaa 180,148,20,228
W 3
s_lshl_b32 s82, s14, 3
Ta 204,5,8192
Maaa 236,196,220
s_add_u32 s71, s71, s82
s_add_u32 s71, s71, 2560
Ta 206,5,12288
Mvaa 180,144,16
X 161
Cvaa 184,144,32,232
Ra 132,242,17408
Ta 208,6,8192
Mvaa 184,148,36
W 5
X 162
Zaaa 240,200,216
Ta 210,6,12288
K 192,160,161
Cvaa 188,148,52,236
buffer_store_dwordx4 a[232:235], v10, s[8:11], s94 offen offset:0 sc1
Ta 212,7,8192
Mvaa 188,144,48
X 163
Mvaa 176,152,8
W 4
X 164
Maaa 240,204,220
Ra 136,242,18432
Ta 214,7,12288
Mvaa 176,156,12
Ta 184,4,16384
K 193,162,163
Mvaa 180,156,28
X 165
Mvaa 180,152,24
W 4
Ta 186,4,20480
Ra 140,242,19456
Zaaa 244,208,216
X 166
Mvaa 184,152,40
buffer_store_dwordx4 a[236:239], v10, s[8:11], s94 offen offset:128 sc1
K 200,164,165
Mvaa 184,156,44
X 167
Mvaa 188,156,60
W 3
Ta 188,5,16384
Ta 190,5,20480
Maaa 244,212,220
s_add_u32 s64, s64, 1
X 168
Mvaa 188,152,56
W 2
Ra 144,242,20480
K 201,166,167
Mvav 32,128,196
Ta 192,6,16384
Ta 194,6,20480
Mvav 64,128,204
X 169
Zaaa 248,184,216
buffer_store_dwordx4 a[240:243], v10, s[8:11], s94 offen offset:512 sc1
Ta 196,7,16384
Mvav 96,128,212
X 170
Mvav 128,128,220
Ra 148,242,21504
K 208,168,169
Mvav 132,132,220
W 5
Ta 198,7,20480
Ta 200,4,24576
Maaa 248,188,220
X 171
Mvav 100,132,212
X 172
Mvav 68,132,204
Ta 202,4,28672
K 209,170,171
Mvav 36,132,196
W 5
)"
R"(Ra 152,242,22528
buffer_store_dwordx4 a[244:247], v10, s[8:11], s94 offen offset:640 sc1
Zaaa 252,192,216
X 173
Mvav 40,136,196
X 174
Mvav 72,136,204
Ta 204,5,24576
Ta 206,5,28672
Mvav 104,136,212
W 5
Ra 156,242,23552
K 216,172,173
Maaa 252,196,220
X 175
Mvav 136,136,220
Ta 208,6,24576
Ta 210,6,28672
Mvav 140,140,220
X 176
Mvav 108,140,212
W 6
buffer_store_dwordx4 a[248:251], v10, s[8:11], s94 offen offset:1024 sc1
K 217,174,175
Zaaa 232,200,216
X 177
Mvav 76,140,204
Ta 212,7,24576
Ra 128,242,8192
Mvav 44,140,196
W 4
Ta 214,7,28672
Ta 184,4,32768
Mvav 48,144,196
X 178
Maaa 232,204,220
Ra 132,242,9216
K 194,176,177
Mvav 80,144,204
X 179
Mvav 112,144,212
Ta 186,4,36864
buffer_store_dwordx4 a[252:255], v10, s[8:11], s94 offen offset:1152 sc1
Mvav 144,144,220
W 6
X 180
Zaaa 236,208,216
Ta 188,5,32768
K 195,178,179
Mvav 148,148,220
X 181
Mvav 116,148,212
Ta 190,5,36864
Ra 136,242,10240
Mvav 84,148,204
W 6
X 182
Maaa 236,212,220
Ta 192,6,32768
K 202,180,181
Mvav 52,148,196
Ta 194,6,36864
Ra 140,242,11264
Mvav 56,152,196
X 183
Mvav 88,152,204
W 6
buffer_store_dwordx4 a[232:235], v10, s[8:11], s94 offen offset:1536 sc1
Ta 196,7,32768
Zaaa 240,184,216
X 184
Mvav 120,152,212
Ta 198,7,36864
K 203,182,183
Mvav 152,152,220
Ra 144,242,12288
Ra 148,242,13312
Mvav 156,156,220
W 8
X 185
Maaa 240,188,220
X 186
Mvav 124,156,212
buffer_store_dwordx4 a[236:239], v10, s[8:11], s94 offen offset:1664 sc1
K 210,184,185
Mvav 92,156,204
X 187
Mvav 60,156,196
W 5
X 188
Zaaa 244,192,216
W 4
Ra 152,242,14336
K 211,186,187
Cvaa 160,128,0,224
X 189
Mvaa 160,132,4
X 190
Cvaa 164,132,20,228
W 3
Ra 156,242,15360
buffer_store_dwordx4 a[240:243], v10, s[8:11], s94 offen offset:2048 sc1
Maaa 244,196,220
K 218,188,189
Mvaa 164,128,16
X 191
Cvaa 168,128,32,232
Mvaa 168,132,36
Cvaa 172,132,52,236
Mvaa 172,128,48
Ra 128,242,24576
Mvaa 160,136,8
Ra 132,242,25600
buffer_store_dwordx4 a[244:247], v10, s[8:11], s94 offen offset:2176 sc1
Mvaa 160,140,12
Mvaa 164,140,28
Mvaa 164,136,24
Mvaa 168,136,40
Mvaa 168,140,44
Mvaa 172,140,60
Mvaa 172,136,56
W 2
Ra 136,242,26624
Cvaa 176,144,0,224
X 160
Mvaa 176,148,4
Ra 140,242,27648
K 219,190,191
Cvaa 180,148,20,228
X 161
Mvaa 180,144,16
X 162
Cvaa 184,144,32,232
K 196,160,161
Mvaa 184,148,36
X 163
Cvaa 188,148,52,236
X 164
Mvaa 188,144,48
Ra 144,242,28672
K 197,162,163
Mvaa 176,152,8
X 165
Mvaa 176,156,12
X 166
Mvaa 180,156,28
Ra 148,242,29696
K 204,164,165
Mvaa 180,152,24
X 167
Mvaa 184,152,40
X 168
Mvaa 184,156,44
K 205,166,167
Mvaa 188,156,60
X 169
Mvaa 188,152,56
W 2
X 170
Mvav 32,128,192
Ra 152,242,30720
K 212,168,169
Mvav 64,128,200
X 171
Mvav 96,128,208
Ra 156,242,31744
v_xor_b32 v242, 0x10000, v242
Mvav 128,128,216
X 172
Mvav 132,132,216
K 213,170,171
Mvav 100,132,208
X 173
Mvav 68,132,200
X 174
Mvav 36,132,192
Mvav 40,136,192
K 220,172,173
Mvav 72,136,200
X 175
Mvav 104,136,208
Mvav 136,136,216
X 176
Mvav 140,140,216
Mvav 108,140,208
K 221,174,175
Mvav 76,140,200
W 0
s_barrier
Mvav 44,140,192
X 177
Mvav 48,144,192
X 178
Mvav 80,144,200
Ra 128,242,0
K 198,176,177
Mvav 112,144,208
X 179
Mvav 144,144,216
Ra 132,242,1024
Mvav 148,148,216
X 180
Mvav 116,148,208
Ra 136,242,2048
Mvav 84,148,200
K 199,178,179
Mvav 52,148,192
X 181
Mvav 56,152,192
Ra 140,242,3072
Mvav 88,152,200
X 182
Mvav 120,152,208
Mvav 152,152,216
K 206,180,181
Mvav 156,156,216
X 183
Mvav 124,156,208
Mvav 92,156,200
s_waitcnt vmcnt(0)
v_mov_b64 v[28:29], s[12:13]
s_mov_b64 exec, 1
buffer_store_dwordx2 v[28:29], off, s[8:11], s71 sc1
s_mov_b64 exec, -1
K 207,182,183
Mvav 60,156,192
s_mov_b32 s85, 99
s_lshl_b32 s65, s57, 1
s_sub_u32 s65, s65, 1
s_branch .Ll0t_42%=
.Lf1r_43%=:
s_lshl_b32 s92, s58, 2
s_add_u32 s91, s92, s51
s_add_u32 s95, s92, 0
s_and_b32 s95, s95, 15
s_mul_i32 s95, s95, 40960
s_mul_i32 s71, s51, 0
s_add_u32 s71, s71, 133120
s_add_u32 m0, s71, s48
s_lshl2_add_u32 s94, s48, s95
D 16,40,94,0
s_add_u32 s95, s92, 1
s_and_b32 s95, s95, 15
s_mul_i32 s95, s95, 40960
s_mul_i32 s71, s51, 0
s_add_u32 s71, s71, 143360
D 17,40,94,1024
D 18,40,94,2048
D 19,40,94,3072
s_add_u32 m0, m0, 4096
s_nop 0
buffer_load_dwordx4 v20, s[40:43], s94 offen lds
s_add_u32 m0, s71, s48
s_lshl2_add_u32 s94, s48, s95
D 16,40,94,0
s_add_u32 s95, s92, 2
s_and_b32 s95, s95, 15
s_mul_i32 s95, s95, 40960
s_mul_i32 s71, s51, 0
s_add_u32 s71, s71, 153600
D 17,40,94,1024
D 18,40,94,2048
D 19,40,94,3072
s_add_u32 m0, m0, 4096
s_nop 0
buffer_load_dwordx4 v20, s[40:43], s94 offen lds
s_add_u32 m0, s71, s48
s_lshl2_add_u32 s94, s48, s95
D 16,40,94,0
D 17,40,94,1024
D 18,40,94,2048
D 19,40,94,3072
s_add_u32 m0, m0, 4096
s_nop 0
buffer_load_dwordx4 v20, s[40:43], s94 offen lds
s_branch .Lf1_36%=
.Lspin_11%=:
s_mov_b32 s84, 0
.Llp_13%=:
s_sleep 2
v_lshrrev_b32 v28, 1, v1
s_lshl_b32 s80, s60, 9
s_add_u32 s80, s80, 40960
buffer_load_dwordx2 v[28:29], v28, s[8:11], s80 offen sc1
s_waitcnt vmcnt(0)
s_add_u32 s84, s84, 1
v_cmp_eq_u32 vcc, s13, v29
s_cmp_eq_u64 vcc, -1
s_cbranch_scc1 .Lok_12%=
s_cmp_lt_u32 s84, 1048576
s_cbranch_scc1 .Llp_13%=
s_branch .Lok_12%=
.Lspin_16%=:
s_mov_b32 s84, 0
.Llp_18%=:
s_sleep 2
v_lshrrev_b32 v28, 1, v1
s_lshl_b32 s80, s60, 9
s_add_u32 s80, s80, 40960
buffer_load_dwordx2 v[28:29], v28, s[8:11], s80 offen sc1
s_waitcnt vmcnt(0)
s_add_u32 s84, s84, 1
v_cmp_eq_u32 vcc, s13, v29
s_cmp_eq_u64 vcc, -1
s_cbranch_scc1 .Lok_17%=
s_cmp_lt_u32 s84, 1048576
s_cbranch_scc1 .Llp_18%=
s_branch .Lok_17%=
.Lrt_19%=:
s_waitcnt vmcnt(0)
s_mov_b32 s86, 40960
buffer_load_dwordx4 v[32:35], v1, s[8:11], s86 offen offset:0 sc1
buffer_load_dwordx4 v[36:39], v1, s[8:11], s86 offen offset:1024 sc1
buffer_load_dwordx4 v[40:43], v1, s[8:11], s86 offen offset:2048 sc1
buffer_load_dwordx4 v[44:47], v1, s[8:11], s86 offen offset:3072 sc1
s_mov_b32 s86, 45056
buffer_load_dwordx4 v[48:51], v1, s[8:11], s86 offen offset:0 sc1
buffer_load_dwordx4 v[52:55], v1, s[8:11], s86 offen offset:1024 sc1
buffer_load_dwordx4 v[56:59], v1, s[8:11], s86 offen offset:2048 sc1
buffer_load_dwordx4 v[60:63], v1, s[8:11], s86 offen offset:3072 sc1
s_mov_b32 s86, 49152
buffer_load_dwordx4 v[64:67], v1, s[8:11], s86 offen offset:0 sc1
buffer_load_dwordx4 v[68:71], v1, s[8:11], s86 offen offset:1024 sc1
buffer_load_dwordx4 v[72:75], v1, s[8:11], s86 offen offset:2048 sc1
buffer_load_dwordx4 v[76:79], v1, s[8:11], s86 offen offset:3072 sc1
s_mov_b32 s86, 53248
buffer_load_dwordx4 v[80:83], v1, s[8:11], s86 offen offset:0 sc1
buffer_load_dwordx4 v[84:87], v1, s[8:11], s86 offen offset:1024 sc1
buffer_load_dwordx4 v[88:91], v1, s[8:11], s86 offen offset:2048 sc1
buffer_load_dwordx4 v[92:95], v1, s[8:11], s86 offen offset:3072 sc1
s_branch .Lrtr_20%=
.Lrt_21%=:
v_lshrrev_b32 v136, 1, v1
s_mov_b32 s84, 0
.LL_23%=:
s_waitcnt vmcnt(0)
v_min3_u32 v96, v33, v35, v95
v_max3_u32 v97, v33, v35, v95
v_min3_u32 v96, v96, v37, v39
v_max3_u32 v97, v97, v37, v39
v_min3_u32 v96, v96, v41, v43
v_max3_u32 v97, v97, v41, v43
v_min3_u32 v96, v96, v45, v47
v_max3_u32 v97, v97, v45, v47
v_min3_u32 v96, v96, v49, v51
v_max3_u32 v97, v97, v49, v51
v_min3_u32 v96, v96, v53, v55
v_max3_u32 v97, v97, v53, v55
v_min3_u32 v96, v96, v57, v59
v_max3_u32 v97, v97, v57, v59
v_min3_u32 v96, v96, v61, v63
v_max3_u32 v97, v97, v61, v63
v_min3_u32 v96, v96, v65, v67
v_max3_u32 v97, v97, v65, v67
v_min3_u32 v96, v96, v69, v71
v_max3_u32 v97, v97, v69, v71
v_min3_u32 v96, v96, v73, v75
v_max3_u32 v97, v97, v73, v75
v_min3_u32 v96, v96, v77, v79
v_max3_u32 v97, v97, v77, v79
v_min3_u32 v96, v96, v81, v83
v_max3_u32 v97, v97, v81, v83
v_min3_u32 v96, v96, v85, v87
v_max3_u32 v97, v97, v85, v87
v_min3_u32 v96, v96, v89, v91
v_max3_u32 v97, v97, v89, v91
v_min_u32 v96, v96, v93
v_max_u32 v97, v97, v93
v_cmp_eq_u32 vcc, s13, v96
v_cmp_eq_u32_e64 s[86:87], s13, v97
s_and_b64 vcc, vcc, s[86:87]
s_cmp_eq_u64 vcc, -1
s_cbranch_scc1 .LL_24%=
s_sleep 2
s_mov_b32 s86, 40960
buffer_load_dwordx4 v[32:35], v1, s[8:11], s86 offen offset:0 sc1
buffer_load_dwordx4 v[36:39], v1, s[8:11], s86 offen offset:1024 sc1
buffer_load_dwordx4 v[40:43], v1, s[8:11], s86 offen offset:2048 sc1
buffer_load_dwordx4 v[44:47], v1, s[8:11], s86 offen offset:3072 sc1
s_mov_b32 s86, 45056
buffer_load_dwordx4 v[48:51], v1, s[8:11], s86 offen offset:0 sc1
buffer_load_dwordx4 v[52:55], v1, s[8:11], s86 offen offset:1024 sc1
buffer_load_dwordx4 v[56:59], v1, s[8:11], s86 offen offset:2048 sc1
buffer_load_dwordx4 v[60:63], v1, s[8:11], s86 offen offset:3072 sc1
s_mov_b32 s86, 49152
buffer_load_dwordx4 v[64:67], v1, s[8:11], s86 offen offset:0 sc1
buffer_load_dwordx4 v[68:71], v1, s[8:11], s86 offen offset:1024 sc1
buffer_load_dwordx4 v[72:75], v1, s[8:11], s86 offen offset:2048 sc1
buffer_load_dwordx4 v[76:79], v1, s[8:11], s86 offen offset:3072 sc1
s_mov_b32 s86, 53248
buffer_load_dwordx4 v[80:83], v1, s[8:11], s86 offen offset:0 sc1
buffer_load_dwordx4 v[84:87], v1, s[8:11], s86 offen offset:1024 sc1
buffer_load_dwordx4 v[88:91], v1, s[8:11], s86 offen offset:2048 sc1
buffer_load_dwordx4 v[92:95], v1, s[8:11], s86 offen offset:3072 sc1
s_add_u32 s84, s84, 1
s_cmp_lt_u32 s84, 1048576
s_cbranch_scc1 .LL_23%=
s_waitcnt vmcnt(0)
.LL_24%=:
v_mov_b32 v96, v32
)"
R"(v_mov_b32 v97, 0
v_mov_b32 v98, 0
v_mov_b32 v100, 0
v_med3_u32 v98, v97, v98, v34
v_med3_u32 v97, v96, v97, v34
v_max_u32 v96, v96, v34
v_med3_u32 v98, v97, v98, v36
v_med3_u32 v97, v96, v97, v36
v_max_u32 v96, v96, v36
v_med3_u32 v98, v97, v98, v38
v_med3_u32 v97, v96, v97, v38
v_max_u32 v96, v96, v38
v_med3_u32 v98, v97, v98, v40
v_med3_u32 v97, v96, v97, v40
v_max_u32 v96, v96, v40
v_med3_u32 v98, v97, v98, v42
v_med3_u32 v97, v96, v97, v42
v_max_u32 v96, v96, v42
v_med3_u32 v98, v97, v98, v44
v_med3_u32 v97, v96, v97, v44
v_max_u32 v96, v96, v44
v_med3_u32 v98, v97, v98, v46
v_med3_u32 v97, v96, v97, v46
v_max_u32 v96, v96, v46
v_med3_u32 v98, v97, v98, v48
v_med3_u32 v97, v96, v97, v48
v_max_u32 v96, v96, v48
v_med3_u32 v98, v97, v98, v50
v_med3_u32 v97, v96, v97, v50
v_max_u32 v96, v96, v50
v_med3_u32 v98, v97, v98, v52
v_med3_u32 v97, v96, v97, v52
v_max_u32 v96, v96, v52
v_med3_u32 v98, v97, v98, v54
v_med3_u32 v97, v96, v97, v54
v_max_u32 v96, v96, v54
v_med3_u32 v98, v97, v98, v56
v_med3_u32 v97, v96, v97, v56
v_max_u32 v96, v96, v56
v_med3_u32 v98, v97, v98, v58
v_med3_u32 v97, v96, v97, v58
v_max_u32 v96, v96, v58
v_med3_u32 v98, v97, v98, v60
v_med3_u32 v97, v96, v97, v60
v_max_u32 v96, v96, v60
v_med3_u32 v98, v97, v98, v62
v_med3_u32 v97, v96, v97, v62
v_max_u32 v96, v96, v62
v_med3_u32 v98, v97, v98, v64
v_med3_u32 v97, v96, v97, v64
v_max_u32 v96, v96, v64
v_med3_u32 v98, v97, v98, v66
v_med3_u32 v97, v96, v97, v66
v_max_u32 v96, v96, v66
v_med3_u32 v98, v97, v98, v68
v_med3_u32 v97, v96, v97, v68
v_max_u32 v96, v96, v68
v_med3_u32 v98, v97, v98, v70
v_med3_u32 v97, v96, v97, v70
v_max_u32 v96, v96, v70
v_med3_u32 v98, v97, v98, v72
v_med3_u32 v97, v96, v97, v72
v_max_u32 v96, v96, v72
v_med3_u32 v98, v97, v98, v74
v_med3_u32 v97, v96, v97, v74
v_max_u32 v96, v96, v74
v_med3_u32 v98, v97, v98, v76
v_med3_u32 v97, v96, v97, v76
v_max_u32 v96, v96, v76
v_med3_u32 v98, v97, v98, v78
v_med3_u32 v97, v96, v97, v78
v_max_u32 v96, v96, v78
v_med3_u32 v98, v97, v98, v80
v_med3_u32 v97, v96, v97, v80
v_max_u32 v96, v96, v80
v_med3_u32 v98, v97, v98, v82
v_med3_u32 v97, v96, v97, v82
v_max_u32 v96, v96, v82
v_med3_u32 v98, v97, v98, v84
v_med3_u32 v97, v96, v97, v84
v_max_u32 v96, v96, v84
v_med3_u32 v98, v97, v98, v86
v_med3_u32 v97, v96, v97, v86
v_max_u32 v96, v96, v86
v_med3_u32 v98, v97, v98, v88
v_med3_u32 v97, v96, v97, v88
v_max_u32 v96, v96, v88
v_med3_u32 v98, v97, v98, v90
v_med3_u32 v97, v96, v97, v90
v_max_u32 v96, v96, v90
v_med3_u32 v98, v97, v98, v92
v_med3_u32 v97, v96, v97, v92
v_max_u32 v96, v96, v92
v_med3_u32 v98, v97, v98, v94
v_med3_u32 v97, v96, v97, v94
v_max_u32 v96, v96, v94
v_mov_b32 v99, v96
s_nop 1
v_max_u32_dpp v99, v99, v99 row_shr:1 row_mask:0xf bank_mask:0xf
s_nop 1
v_max_u32_dpp v99, v99, v99 row_shr:2 row_mask:0xf bank_mask:0xf
s_nop 1
v_max_u32_dpp v99, v99, v99 row_shr:4 row_mask:0xf bank_mask:0xf
s_nop 1
v_max_u32_dpp v99, v99, v99 row_shr:8 row_mask:0xf bank_mask:0xf
s_nop 1
v_max_u32_dpp v99, v99, v99 row_bcast:15 row_mask:0xa bank_mask:0xf
s_nop 1
v_max_u32_dpp v99, v99, v99 row_bcast:31 row_mask:0xc bank_mask:0xf
s_nop 0
v_readlane_b32 s86, v99, 63
s_nop 1
v_cmp_eq_u32 vcc, s86, v96
s_and_b32 s87, s86, 0xffff
s_sub_u32 s87, 2047, s87
v_cndmask_b32 v96, v96, v97, vcc
v_cndmask_b32_e64 v97, v97, 0, vcc
v_writelane_b32 v100, s87, 0
v_mov_b32 v99, v96
s_nop 1
v_max_u32_dpp v99, v99, v99 row_shr:1 row_mask:0xf bank_mask:0xf
s_nop 1
v_max_u32_dpp v99, v99, v99 row_shr:2 row_mask:0xf bank_mask:0xf
s_nop 1
v_max_u32_dpp v99, v99, v99 row_shr:4 row_mask:0xf bank_mask:0xf
s_nop 1
v_max_u32_dpp v99, v99, v99 row_shr:8 row_mask:0xf bank_mask:0xf
s_nop 1
v_max_u32_dpp v99, v99, v99 row_bcast:15 row_mask:0xa bank_mask:0xf
s_nop 1
v_max_u32_dpp v99, v99, v99 row_bcast:31 row_mask:0xc bank_mask:0xf
s_nop 0
v_readlane_b32 s86, v99, 63
s_nop 1
v_cmp_eq_u32 vcc, s86, v96
s_and_b32 s87, s86, 0xffff
s_sub_u32 s87, 2047, s87
v_cndmask_b32 v96, v96, v97, vcc
v_cndmask_b32_e64 v97, v97, 0, vcc
v_writelane_b32 v100, s87, 1
v_mov_b32 v99, v96
s_nop 1
v_max_u32_dpp v99, v99, v99 row_shr:1 row_mask:0xf bank_mask:0xf
s_nop 1
v_max_u32_dpp v99, v99, v99 row_shr:2 row_mask:0xf bank_mask:0xf
s_nop 1
v_max_u32_dpp v99, v99, v99 row_shr:4 row_mask:0xf bank_mask:0xf
s_nop 1
v_max_u32_dpp v99, v99, v99 row_shr:8 row_mask:0xf bank_mask:0xf
s_nop 1
v_max_u32_dpp v99, v99, v99 row_bcast:15 row_mask:0xa bank_mask:0xf
s_nop 1
v_max_u32_dpp v99, v99, v99 row_bcast:31 row_mask:0xc bank_mask:0xf
s_nop 0
v_readlane_b32 s86, v99, 63
s_nop 1
v_cmp_eq_u32 vcc, s86, v96
s_and_b32 s87, s86, 0xffff
s_sub_u32 s87, 2047, s87
v_cndmask_b32 v96, v96, v97, vcc
v_cndmask_b32_e64 v97, v97, 0, vcc
v_writelane_b32 v100, s87, 2
v_mov_b32 v99, v96
s_nop 1
v_max_u32_dpp v99, v99, v99 row_shr:1 row_mask:0xf bank_mask:0xf
s_nop 1
v_max_u32_dpp v99, v99, v99 row_shr:2 row_mask:0xf bank_mask:0xf
s_nop 1
v_max_u32_dpp v99, v99, v99 row_shr:4 row_mask:0xf bank_mask:0xf
s_nop 1
v_max_u32_dpp v99, v99, v99 row_shr:8 row_mask:0xf bank_mask:0xf
s_nop 1
v_max_u32_dpp v99, v99, v99 row_bcast:15 row_mask:0xa bank_mask:0xf
s_nop 1
v_max_u32_dpp v99, v99, v99 row_bcast:31 row_mask:0xc bank_mask:0xf
s_nop 0
v_readlane_b32 s86, v99, 63
s_nop 1
v_cmp_eq_u32 vcc, s86, v96
s_and_b32 s87, s86, 0xffff
s_sub_u32 s87, 2047, s87
v_cndmask_b32 v96, v96, v97, vcc
v_cndmask_b32_e64 v97, v97, 0, vcc
v_writelane_b32 v100, s87, 3
v_mov_b32 v99, v96
s_nop 1
v_max_u32_dpp v99, v99, v99 row_shr:1 row_mask:0xf bank_mask:0xf
s_nop 1
v_max_u32_dpp v99, v99, v99 row_shr:2 row_mask:0xf bank_mask:0xf
s_nop 1
v_max_u32_dpp v99, v99, v99 row_shr:4 row_mask:0xf bank_mask:0xf
s_nop 1
v_max_u32_dpp v99, v99, v99 row_shr:8 row_mask:0xf bank_mask:0xf
s_nop 1
v_max_u32_dpp v99, v99, v99 row_bcast:15 row_mask:0xa bank_mask:0xf
s_nop 1
v_max_u32_dpp v99, v99, v99 row_bcast:31 row_mask:0xc bank_mask:0xf
s_nop 0
v_readlane_b32 s86, v99, 63
s_nop 1
v_cmp_eq_u32 vcc, s86, v96
s_and_b32 s87, s86, 0xffff
s_sub_u32 s87, 2047, s87
v_cndmask_b32 v96, v96, v97, vcc
v_cndmask_b32_e64 v97, v97, 0, vcc
v_writelane_b32 v100, s87, 4
v_mov_b32 v99, v96
s_nop 1
v_max_u32_dpp v99, v99, v99 row_shr:1 row_mask:0xf bank_mask:0xf
s_nop 1
v_max_u32_dpp v99, v99, v99 row_shr:2 row_mask:0xf bank_mask:0xf
s_nop 1
v_max_u32_dpp v99, v99, v99 row_shr:4 row_mask:0xf bank_mask:0xf
s_nop 1
v_max_u32_dpp v99, v99, v99 row_shr:8 row_mask:0xf bank_mask:0xf
s_nop 1
v_max_u32_dpp v99, v99, v99 row_bcast:15 row_mask:0xa bank_mask:0xf
s_nop 1
v_max_u32_dpp v99, v99, v99 row_bcast:31 row_mask:0xc bank_mask:0xf
s_nop 0
v_readlane_b32 s86, v99, 63
s_nop 1
v_cmp_eq_u32 vcc, s86, v96
s_and_b32 s87, s86, 0xffff
s_sub_u32 s87, 2047, s87
v_cndmask_b32 v96, v96, v97, vcc
v_cndmask_b32_e64 v97, v97, 0, vcc
v_writelane_b32 v100, s87, 5
v_mov_b32 v99, v96
s_nop 1
v_max_u32_dpp v99, v99, v99 row_shr:1 row_mask:0xf bank_mask:0xf
s_nop 1
v_max_u32_dpp v99, v99, v99 row_shr:2 row_mask:0xf bank_mask:0xf
s_nop 1
v_max_u32_dpp v99, v99, v99 row_shr:4 row_mask:0xf bank_mask:0xf
s_nop 1
v_max_u32_dpp v99, v99, v99 row_shr:8 row_mask:0xf bank_mask:0xf
s_nop 1
v_max_u32_dpp v99, v99, v99 row_bcast:15 row_mask:0xa bank_mask:0xf
s_nop 1
v_max_u32_dpp v99, v99, v99 row_bcast:31 row_mask:0xc bank_mask:0xf
s_nop 0
v_readlane_b32 s86, v99, 63
s_nop 1
v_cmp_eq_u32 vcc, s86, v96
s_and_b32 s87, s86, 0xffff
s_sub_u32 s87, 2047, s87
v_cndmask_b32 v96, v96, v97, vcc
v_cndmask_b32_e64 v97, v97, 0, vcc
v_writelane_b32 v100, s87, 6
v_mov_b32 v99, v96
s_nop 1
v_max_u32_dpp v99, v99, v99 row_shr:1 row_mask:0xf bank_mask:0xf
s_nop 1
v_max_u32_dpp v99, v99, v99 row_shr:2 row_mask:0xf bank_mask:0xf
s_nop 1
v_max_u32_dpp v99, v99, v99 row_shr:4 row_mask:0xf bank_mask:0xf
s_nop 1
v_max_u32_dpp v99, v99, v99 row_shr:8 row_mask:0xf bank_mask:0xf
s_nop 1
v_max_u32_dpp v99, v99, v99 row_bcast:15 row_mask:0xa bank_mask:0xf
s_nop 1
v_max_u32_dpp v99, v99, v99 row_bcast:31 row_mask:0xc bank_mask:0xf
s_nop 0
v_readlane_b32 s86, v99, 63
s_nop 1
v_cmp_eq_u32 vcc, s86, v96
s_and_b32 s87, s86, 0xffff
s_sub_u32 s87, 2047, s87
v_cndmask_b32 v96, v96, v97, vcc
v_cndmask_b32_e64 v97, v97, 0, vcc
v_writelane_b32 v100, s87, 7
v_mov_b32 v99, v98
s_nop 1
v_max_u32_dpp v99, v99, v99 row_shr:1 row_mask:0xf bank_mask:0xf
s_nop 1
v_max_u32_dpp v99, v99, v99 row_shr:2 row_mask:0xf bank_mask:0xf
s_nop 1
v_max_u32_dpp v99, v99, v99 row_shr:4 row_mask:0xf bank_mask:0xf
s_nop 1
v_max_u32_dpp v99, v99, v99 row_shr:8 row_mask:0xf bank_mask:0xf
s_nop 1
v_max_u32_dpp v99, v99, v99 row_bcast:15 row_mask:0xa bank_mask:0xf
s_nop 1
v_max_u32_dpp v99, v99, v99 row_bcast:31 row_mask:0xc bank_mask:0xf
s_nop 0
v_readlane_b32 s87, v99, 63
s_nop 0
s_cmp_gt_u32 s87, s86
s_cbranch_scc0 .LL_25%=
s_mov_b32 m0, 0
.LL_26%=:
v_max3_u32 v96, v32, v34, v36
v_max3_u32 v96, v96, v38, v40
v_max3_u32 v96, v96, v42, v44
v_max3_u32 v96, v96, v46, v48
v_max3_u32 v96, v96, v50, v52
v_max3_u32 v96, v96, v54, v56
v_max3_u32 v96, v96, v58, v60
v_max3_u32 v96, v96, v62, v64
v_max3_u32 v96, v96, v66, v68
v_max3_u32 v96, v96, v70, v72
v_max3_u32 v96, v96, v74, v76
v_max3_u32 v96, v96, v78, v80
v_max3_u32 v96, v96, v82, v84
v_max3_u32 v96, v96, v86, v88
v_max3_u32 v96, v96, v90, v92
v_max_u32 v96, v96, v94
v_mov_b32 v99, v96
s_nop 1
v_max_u32_dpp v99, v99, v99 row_shr:1 row_mask:0xf bank_mask:0xf
s_nop 1
v_max_u32_dpp v99, v99, v99 row_shr:2 row_mask:0xf bank_mask:0xf
s_nop 1
v_max_u32_dpp v99, v99, v99 row_shr:4 row_mask:0xf bank_mask:0xf
s_nop 1
v_max_u32_dpp v99, v99, v99 row_shr:8 row_mask:0xf bank_mask:0xf
s_nop 1
v_max_u32_dpp v99, v99, v99 row_bcast:15 row_mask:0xa bank_mask:0xf
s_nop 1
v_max_u32_dpp v99, v99, v99 row_bcast:31 row_mask:0xc bank_mask:0xf
s_nop 0
v_readlane_b32 s86, v99, 63
s_nop 1
v_cmp_eq_u32 vcc, s86, v32
s_nop 1
v_cndmask_b32_e64 v32, v32, 0, vcc
v_cmp_eq_u32 vcc, s86, v34
s_nop 1
v_cndmask_b32_e64 v34, v34, 0, vcc
v_cmp_eq_u32 vcc, s86, v36
s_nop 1
v_cndmask_b32_e64 v36, v36, 0, vcc
v_cmp_eq_u32 vcc, s86, v38
s_nop 1
v_cndmask_b32_e64 v38, v38, 0, vcc
v_cmp_eq_u32 vcc, s86, v40
s_nop 1
v_cndmask_b32_e64 v40, v40, 0, vcc
v_cmp_eq_u32 vcc, s86, v42
s_nop 1
v_cndmask_b32_e64 v42, v42, 0, vcc
v_cmp_eq_u32 vcc, s86, v44
s_nop 1
v_cndmask_b32_e64 v44, v44, 0, vcc
v_cmp_eq_u32 vcc, s86, v46
s_nop 1
v_cndmask_b32_e64 v46, v46, 0, vcc
v_cmp_eq_u32 vcc, s86, v48
s_nop 1
v_cndmask_b32_e64 v48, v48, 0, vcc
v_cmp_eq_u32 vcc, s86, v50
s_nop 1
v_cndmask_b32_e64 v50, v50, 0, vcc
v_cmp_eq_u32 vcc, s86, v52
s_nop 1
v_cndmask_b32_e64 v52, v52, 0, vcc
v_cmp_eq_u32 vcc, s86, v54
s_nop 1
v_cndmask_b32_e64 v54, v54, 0, vcc
v_cmp_eq_u32 vcc, s86, v56
s_nop 1
v_cndmask_b32_e64 v56, v56, 0, vcc
v_cmp_eq_u32 vcc, s86, v58
s_nop 1
v_cndmask_b32_e64 v58, v58, 0, vcc
v_cmp_eq_u32 vcc, s86, v60
s_nop 1
v_cndmask_b32_e64 v60, v60, 0, vcc
v_cmp_eq_u32 vcc, s86, v62
s_nop 1
v_cndmask_b32_e64 v62, v62, 0, vcc
v_cmp_eq_u32 vcc, s86, v64
s_nop 1
v_cndmask_b32_e64 v64, v64, 0, vcc
v_cmp_eq_u32 vcc, s86, v66
s_nop 1
v_cndmask_b32_e64 v66, v66, 0, vcc
v_cmp_eq_u32 vcc, s86, v68
s_nop 1
v_cndmask_b32_e64 v68, v68, 0, vcc
v_cmp_eq_u32 vcc, s86, v70
s_nop 1
v_cndmask_b32_e64 v70, v70, 0, vcc
v_cmp_eq_u32 vcc, s86, v72
s_nop 1
v_cndmask_b32_e64 v72, v72, 0, vcc
v_cmp_eq_u32 vcc, s86, v74
s_nop 1
v_cndmask_b32_e64 v74, v74, 0, vcc
v_cmp_eq_u32 vcc, s86, v76
s_nop 1
v_cndmask_b32_e64 v76, v76, 0, vcc
v_cmp_eq_u32 vcc, s86, v78
s_nop 1
v_cndmask_b32_e64 v78, v78, 0, vcc
v_cmp_eq_u32 vcc, s86, v80
s_nop 1
v_cndmask_b32_e64 v80, v80, 0, vcc
v_cmp_eq_u32 vcc, s86, v82
)"
R"(s_nop 1
v_cndmask_b32_e64 v82, v82, 0, vcc
v_cmp_eq_u32 vcc, s86, v84
s_nop 1
v_cndmask_b32_e64 v84, v84, 0, vcc
v_cmp_eq_u32 vcc, s86, v86
s_nop 1
v_cndmask_b32_e64 v86, v86, 0, vcc
v_cmp_eq_u32 vcc, s86, v88
s_nop 1
v_cndmask_b32_e64 v88, v88, 0, vcc
v_cmp_eq_u32 vcc, s86, v90
s_nop 1
v_cndmask_b32_e64 v90, v90, 0, vcc
v_cmp_eq_u32 vcc, s86, v92
s_nop 1
v_cndmask_b32_e64 v92, v92, 0, vcc
v_cmp_eq_u32 vcc, s86, v94
s_nop 1
v_cndmask_b32_e64 v94, v94, 0, vcc
s_and_b32 s87, s86, 0xffff
s_sub_u32 s87, 2047, s87
s_nop 0
v_writelane_b32 v100, s87, m0
s_add_u32 m0, m0, 1
s_cmp_lt_u32 m0, 8
s_cbranch_scc1 .LL_26%=
.LL_25%=:
v_xor_b32 v102, s12, v100
v_mov_b32 v103, s13
s_mov_b64 exec, 0xff
buffer_store_dwordx2 v[102:103], v136, s[8:11], 0 offen offset:3584 sc1
s_mov_b64 exec, -1
s_branch .Lrtr_22%=
.Lspin_47%=:
s_mov_b32 s84, 0
.Llp_49%=:
s_sleep 2
buffer_load_dwordx2 v[8:9], v11, s[8:11], 0 offen offset:3840 sc1
s_waitcnt vmcnt(0)
s_add_u32 s84, s84, 1
v_cmp_eq_u64 vcc, s[12:13], v[8:9]
s_cmp_eq_u64 vcc, -1
s_cbranch_scc1 .Lok_48%=
s_cmp_lt_u32 s84, 1048576
s_cbranch_scc1 .Llp_49%=
s_branch .Lok_48%=
.Lredo%=:
s_waitcnt vmcnt(0) lgkmcnt(0)
s_barrier
s_and_b32 s81, s14, 1
s_mul_i32 s91, s81, 17164288
s_mul_i32 s82, s56, 536576
s_add_u32 s91, s91, s82
s_add_u32 s91, s91, 4194304
s_lshr_b32 s82, s14, 1
s_lshl_b32 s83, s82, 14
s_add_u32 s91, s91, s83
s_lshl_b32 s81, s81, 14
s_lshl_b32 s82, s82, 16
s_or_b32 s92, s81, s82
s_xor_b32 s92, s92, 0x10000
s_add_u32 m0, s92, 0
s_add_u32 s81, s91, 0
Dc 1,8,81,0
Dc 1,8,81,1024
Dc 1,8,81,2048
Dc 1,8,81,3072
s_add_u32 m0, s92, 4096
s_add_u32 s81, s91, 4096
Dc 1,8,81,0
Dc 1,8,81,1024
Dc 1,8,81,2048
Dc 1,8,81,3072
s_add_u32 m0, s92, 8192
s_add_u32 s81, s91, 8192
Dc 1,8,81,0
Dc 1,8,81,1024
Dc 1,8,81,2048
Dc 1,8,81,3072
s_add_u32 m0, s92, 12288
s_add_u32 s81, s91, 12288
Dc 1,8,81,0
Dc 1,8,81,1024
Dc 1,8,81,2048
Dc 1,8,81,3072
v_bfe_u32 v244, v1, 4, 1
v_mul_u32_u24 v244, 0xaaaa, v244
v_add_u32 v244, 0x4444, v244
v_lshl_or_b32 v240, v244, 16, v244
v_mov_b32 v241, 0xff800000
v_add_u32 v242, 0x10000, v1
v_bfe_u32 v244, v1, 4, 4
v_lshrrev_b32 v245, 8, v1
v_lshlrev_b32 v245, 2, v245
v_sub_u32 v244, v244, v245
v_cmp_le_i32_e64 s[72:73], 0, v244
v_cmp_le_i32_e64 s[74:75], 1, v244
v_cmp_le_i32_e64 s[76:77], 2, v244
v_cmp_le_i32_e64 s[78:79], 3, v244
v_add_u32 v243, 4096, v1
v_add_u32 v244, 8192, v1
v_add_u32 v245, 12288, v1
s_mov_b32 s18, 0x3f803f80
s_mov_b32 s19, 0x3f003f00
v_accvgpr_write_b32 a176, s18
v_accvgpr_write_b32 a180, s19
v_mov_b32 v252, v241
v_accvgpr_write_b32 a177, s18
v_accvgpr_write_b32 a181, s19
v_mov_b32 v253, v241
v_accvgpr_write_b32 a178, s18
v_accvgpr_write_b32 a182, s19
v_mov_b32 v254, v241
v_accvgpr_write_b32 a179, s18
v_accvgpr_write_b32 a183, s19
v_mov_b32 v255, v241
v_accvgpr_write_b32 a160, 0
v_accvgpr_write_b32 a161, 0
v_accvgpr_write_b32 a162, 0
v_accvgpr_write_b32 a163, 0
v_accvgpr_write_b32 a164, 0
v_accvgpr_write_b32 a165, 0
v_accvgpr_write_b32 a166, 0
v_accvgpr_write_b32 a167, 0
v_accvgpr_write_b32 a168, 0
v_accvgpr_write_b32 a169, 0
v_accvgpr_write_b32 a170, 0
v_accvgpr_write_b32 a171, 0
v_accvgpr_write_b32 a172, 0
v_accvgpr_write_b32 a173, 0
v_accvgpr_write_b32 a174, 0
v_accvgpr_write_b32 a175, 0
s_mov_b32 s90, 0x5d800000
s_mov_b64 s[88:89], 0
s_mov_b32 s64, 0
s_and_b32 s18, s14, 1
s_lshl_b32 s62, s18, 14
s_lshl_b32 s20, s57, 15
s_sub_u32 s20, 507904, s20
s_add_u32 s20, s20, s62
s_lshl_b32 s65, s57, 1
s_sub_u32 s65, s65, 1
s_max_i32 s65, s65, 0
s_sub_u32 s66, s60, 1
s_cmp_lt_u32 s14, 2
s_cselect_b32 s20, s20, 0
s_cselect_b32 s66, s66, 15
s_cselect_b32 s21, 32768, 0
s_add_u32 s61, s91, s20
s_add_u32 s62, s62, s21
s_sub_u32 s63, 16384, s21
s_lshl_b32 s67, s60, 7
s_add_u32 s67, s67, s56
s_lshl_b32 s67, s67, 12
s_add_u32 s67, s67, 39845888
s_lshl_b32 s69, s60, 3
s_add_u32 s69, s69, s59
s_lshl_b32 s69, s69, 6
s_lshl_b32 s21, s58, 3
s_add_u32 s69, s69, s21
s_add_u32 s69, s69, 20480
s_mov_b32 s70, 0
s_mov_b32 s37, 0
v_bfe_u32 v28, v1, 4, 4
v_lshrrev_b32 v29, 8, v1
v_lshlrev_b32 v29, 2, v29
v_sub_u32 v28, v28, v29
s_mov_b64 s[24:25], 0
s_mov_b32 s68, 0
s_mov_b32 s85, 99
s_lshl_b32 s80, s60, 6
s_and_b32 s81, s14, 2
s_lshl_b32 s81, s81, 4
s_xor_b32 s82, s81, 0
s_add_u32 s82, s82, s80
v_add_u32 v4, s82, v28
s_xor_b32 s82, s81, 16
s_add_u32 s82, s82, s80
v_add_u32 v5, s82, v28
s_xor_b32 s82, s81, 32
s_add_u32 s82, s82, s80
v_add_u32 v6, s82, v28
s_xor_b32 s82, s81, 48
s_add_u32 s82, s82, s80
v_add_u32 v7, s82, v28
s_mul_i32 s82, s56, 536576
s_add_u32 s82, s82, 4194304
s_lshl_b32 s84, s60, 1
s_add_u32 s84, s84, 2
.LL_50%=:
buffer_load_dwordx4 a[128:131], v1, s[8:11], s82 offen offset:0 sc1
buffer_load_dwordx4 a[132:135], v1, s[8:11], s82 offen offset:1024 sc1
buffer_load_dwordx4 a[136:139], v1, s[8:11], s82 offen offset:2048 sc1
buffer_load_dwordx4 a[140:143], v1, s[8:11], s82 offen offset:3072 sc1
buffer_load_dwordx4 a[144:147], v243, s[8:11], s82 offen offset:0 sc1
buffer_load_dwordx4 a[148:151], v243, s[8:11], s82 offen offset:1024 sc1
buffer_load_dwordx4 a[152:155], v243, s[8:11], s82 offen offset:2048 sc1
buffer_load_dwordx4 a[156:159], v243, s[8:11], s82 offen offset:3072 sc1
s_waitcnt vmcnt(0)
Zvaa 160,128,0
Zvaa 164,128,16
Zvaa 168,128,32
Zvaa 172,128,48
Mvaa 160,132,4
Mvaa 164,132,20
Mvaa 168,132,36
Mvaa 172,132,52
Mvaa 160,136,8
Mvaa 164,136,24
Mvaa 168,136,40
Mvaa 172,136,56
Mvaa 160,140,12
Mvaa 164,140,28
Mvaa 168,140,44
Mvaa 172,140,60
Zvaa 176,144,0
Zvaa 180,144,16
Zvaa 184,144,32
Zvaa 188,144,48
Mvaa 176,148,4
Mvaa 180,148,20
Mvaa 184,148,36
Mvaa 188,148,52
Mvaa 176,152,8
Mvaa 180,152,24
Mvaa 184,152,40
Mvaa 188,152,56
Mvaa 176,156,12
Mvaa 180,156,28
Mvaa 184,156,44
Mvaa 188,156,60
v_cmp_le_i32 vcc, 0, v4
s_nop 1
v_cndmask_b32 v160, v241, v160, vcc
v_max_f32 v252, v252, v160
v_cmp_le_i32 vcc, 1, v4
s_nop 1
v_cndmask_b32 v161, v241, v161, vcc
v_max_f32 v252, v252, v161
v_cmp_le_i32 vcc, 2, v4
s_nop 1
v_cndmask_b32 v162, v241, v162, vcc
v_max_f32 v252, v252, v162
v_cmp_le_i32 vcc, 3, v4
s_nop 1
v_cndmask_b32 v163, v241, v163, vcc
v_max_f32 v252, v252, v163
v_cmp_le_i32 vcc, 0, v5
s_nop 1
v_cndmask_b32 v164, v241, v164, vcc
v_max_f32 v253, v253, v164
v_cmp_le_i32 vcc, 1, v5
s_nop 1
v_cndmask_b32 v165, v241, v165, vcc
v_max_f32 v253, v253, v165
v_cmp_le_i32 vcc, 2, v5
s_nop 1
v_cndmask_b32 v166, v241, v166, vcc
v_max_f32 v253, v253, v166
v_cmp_le_i32 vcc, 3, v5
s_nop 1
v_cndmask_b32 v167, v241, v167, vcc
v_max_f32 v253, v253, v167
v_cmp_le_i32 vcc, 0, v6
s_nop 1
v_cndmask_b32 v168, v241, v168, vcc
v_max_f32 v254, v254, v168
v_cmp_le_i32 vcc, 1, v6
s_nop 1
v_cndmask_b32 v169, v241, v169, vcc
v_max_f32 v254, v254, v169
v_cmp_le_i32 vcc, 2, v6
s_nop 1
v_cndmask_b32 v170, v241, v170, vcc
v_max_f32 v254, v254, v170
v_cmp_le_i32 vcc, 3, v6
s_nop 1
v_cndmask_b32 v171, v241, v171, vcc
v_max_f32 v254, v254, v171
v_cmp_le_i32 vcc, 0, v7
s_nop 1
v_cndmask_b32 v172, v241, v172, vcc
v_max_f32 v255, v255, v172
v_cmp_le_i32 vcc, 1, v7
s_nop 1
v_cndmask_b32 v173, v241, v173, vcc
v_max_f32 v255, v255, v173
v_cmp_le_i32 vcc, 2, v7
s_nop 1
v_cndmask_b32 v174, v241, v174, vcc
v_max_f32 v255, v255, v174
v_cmp_le_i32 vcc, 3, v7
s_nop 1
v_cndmask_b32 v175, v241, v175, vcc
v_max_f32 v255, v255, v175
v_cmp_le_i32 vcc, 16, v4
s_nop 1
v_cndmask_b32 v176, v241, v176, vcc
v_max_f32 v252, v252, v176
v_cmp_le_i32 vcc, 17, v4
s_nop 1
v_cndmask_b32 v177, v241, v177, vcc
v_max_f32 v252, v252, v177
v_cmp_le_i32 vcc, 18, v4
s_nop 1
v_cndmask_b32 v178, v241, v178, vcc
v_max_f32 v252, v252, v178
v_cmp_le_i32 vcc, 19, v4
s_nop 1
v_cndmask_b32 v179, v241, v179, vcc
v_max_f32 v252, v252, v179
v_cmp_le_i32 vcc, 16, v5
s_nop 1
v_cndmask_b32 v180, v241, v180, vcc
v_max_f32 v253, v253, v180
v_cmp_le_i32 vcc, 17, v5
s_nop 1
v_cndmask_b32 v181, v241, v181, vcc
v_max_f32 v253, v253, v181
v_cmp_le_i32 vcc, 18, v5
s_nop 1
v_cndmask_b32 v182, v241, v182, vcc
v_max_f32 v253, v253, v182
v_cmp_le_i32 vcc, 19, v5
s_nop 1
v_cndmask_b32 v183, v241, v183, vcc
v_max_f32 v253, v253, v183
v_cmp_le_i32 vcc, 16, v6
s_nop 1
v_cndmask_b32 v184, v241, v184, vcc
v_max_f32 v254, v254, v184
v_cmp_le_i32 vcc, 17, v6
s_nop 1
v_cndmask_b32 v185, v241, v185, vcc
v_max_f32 v254, v254, v185
v_cmp_le_i32 vcc, 18, v6
s_nop 1
v_cndmask_b32 v186, v241, v186, vcc
v_max_f32 v254, v254, v186
v_cmp_le_i32 vcc, 19, v6
s_nop 1
v_cndmask_b32 v187, v241, v187, vcc
v_max_f32 v254, v254, v187
v_cmp_le_i32 vcc, 16, v7
s_nop 1
v_cndmask_b32 v188, v241, v188, vcc
v_max_f32 v255, v255, v188
v_cmp_le_i32 vcc, 17, v7
s_nop 1
v_cndmask_b32 v189, v241, v189, vcc
v_max_f32 v255, v255, v189
v_cmp_le_i32 vcc, 18, v7
s_nop 1
v_cndmask_b32 v190, v241, v190, vcc
v_max_f32 v255, v255, v190
v_cmp_le_i32 vcc, 19, v7
s_nop 1
v_cndmask_b32 v191, v241, v191, vcc
v_max_f32 v255, v255, v191
v_subrev_u32 v4, 32, v4
v_subrev_u32 v5, 32, v5
v_subrev_u32 v6, 32, v6
v_subrev_u32 v7, 32, v7
s_add_u32 s82, s82, 8192
s_sub_u32 s84, s84, 1
s_cmp_lg_u32 s84, 0
s_cbranch_scc1 .LL_50%=
G 28,0
v_lshlrev_b32 v32, 16, v28
v_and_b32 v33, 0xffff0000, v28
G 28,1
v_lshlrev_b32 v34, 16, v28
v_and_b32 v35, 0xffff0000, v28
G 28,2
v_lshlrev_b32 v36, 16, v28
v_and_b32 v37, 0xffff0000, v28
G 28,3
v_lshlrev_b32 v38, 16, v28
v_and_b32 v39, 0xffff0000, v28
G 28,4
v_lshlrev_b32 v40, 16, v28
v_and_b32 v41, 0xffff0000, v28
G 28,5
v_lshlrev_b32 v42, 16, v28
v_and_b32 v43, 0xffff0000, v28
G 28,6
v_lshlrev_b32 v44, 16, v28
v_and_b32 v45, 0xffff0000, v28
G 28,7
v_lshlrev_b32 v46, 16, v28
v_and_b32 v47, 0xffff0000, v28
G 28,8
v_lshlrev_b32 v48, 16, v28
v_and_b32 v49, 0xffff0000, v28
G 28,9
v_lshlrev_b32 v50, 16, v28
v_and_b32 v51, 0xffff0000, v28
G 28,10
v_lshlrev_b32 v52, 16, v28
)"
R"(v_and_b32 v53, 0xffff0000, v28
G 28,11
v_lshlrev_b32 v54, 16, v28
v_and_b32 v55, 0xffff0000, v28
G 28,12
v_lshlrev_b32 v56, 16, v28
v_and_b32 v57, 0xffff0000, v28
G 28,13
v_lshlrev_b32 v58, 16, v28
v_and_b32 v59, 0xffff0000, v28
G 28,14
v_lshlrev_b32 v60, 16, v28
v_and_b32 v61, 0xffff0000, v28
G 28,15
v_lshlrev_b32 v62, 16, v28
v_and_b32 v63, 0xffff0000, v28
G 28,16
v_lshlrev_b32 v64, 16, v28
v_and_b32 v65, 0xffff0000, v28
G 28,17
v_lshlrev_b32 v66, 16, v28
v_and_b32 v67, 0xffff0000, v28
G 28,18
v_lshlrev_b32 v68, 16, v28
v_and_b32 v69, 0xffff0000, v28
G 28,19
v_lshlrev_b32 v70, 16, v28
v_and_b32 v71, 0xffff0000, v28
G 28,20
v_lshlrev_b32 v72, 16, v28
v_and_b32 v73, 0xffff0000, v28
G 28,21
v_lshlrev_b32 v74, 16, v28
v_and_b32 v75, 0xffff0000, v28
G 28,22
v_lshlrev_b32 v76, 16, v28
v_and_b32 v77, 0xffff0000, v28
G 28,23
v_lshlrev_b32 v78, 16, v28
v_and_b32 v79, 0xffff0000, v28
G 28,24
v_lshlrev_b32 v80, 16, v28
v_and_b32 v81, 0xffff0000, v28
G 28,25
v_lshlrev_b32 v82, 16, v28
v_and_b32 v83, 0xffff0000, v28
G 28,26
v_lshlrev_b32 v84, 16, v28
v_and_b32 v85, 0xffff0000, v28
G 28,27
v_lshlrev_b32 v86, 16, v28
v_and_b32 v87, 0xffff0000, v28
G 28,28
v_lshlrev_b32 v88, 16, v28
v_and_b32 v89, 0xffff0000, v28
G 28,29
v_lshlrev_b32 v90, 16, v28
v_and_b32 v91, 0xffff0000, v28
G 28,30
v_lshlrev_b32 v92, 16, v28
v_and_b32 v93, 0xffff0000, v28
G 28,31
v_lshlrev_b32 v94, 16, v28
v_and_b32 v95, 0xffff0000, v28
G 28,32
v_lshlrev_b32 v96, 16, v28
v_and_b32 v97, 0xffff0000, v28
G 28,33
v_lshlrev_b32 v98, 16, v28
v_and_b32 v99, 0xffff0000, v28
G 28,34
v_lshlrev_b32 v100, 16, v28
v_and_b32 v101, 0xffff0000, v28
G 28,35
v_lshlrev_b32 v102, 16, v28
v_and_b32 v103, 0xffff0000, v28
G 28,36
v_lshlrev_b32 v104, 16, v28
v_and_b32 v105, 0xffff0000, v28
G 28,37
v_lshlrev_b32 v106, 16, v28
v_and_b32 v107, 0xffff0000, v28
G 28,38
v_lshlrev_b32 v108, 16, v28
v_and_b32 v109, 0xffff0000, v28
G 28,39
v_lshlrev_b32 v110, 16, v28
v_and_b32 v111, 0xffff0000, v28
G 28,40
v_lshlrev_b32 v112, 16, v28
v_and_b32 v113, 0xffff0000, v28
G 28,41
v_lshlrev_b32 v114, 16, v28
v_and_b32 v115, 0xffff0000, v28
G 28,42
v_lshlrev_b32 v116, 16, v28
v_and_b32 v117, 0xffff0000, v28
G 28,43
v_lshlrev_b32 v118, 16, v28
v_and_b32 v119, 0xffff0000, v28
G 28,44
v_lshlrev_b32 v120, 16, v28
v_and_b32 v121, 0xffff0000, v28
G 28,45
v_lshlrev_b32 v122, 16, v28
v_and_b32 v123, 0xffff0000, v28
G 28,46
v_lshlrev_b32 v124, 16, v28
v_and_b32 v125, 0xffff0000, v28
G 28,47
v_lshlrev_b32 v126, 16, v28
v_and_b32 v127, 0xffff0000, v28
G 28,48
v_lshlrev_b32 v128, 16, v28
v_and_b32 v129, 0xffff0000, v28
G 28,49
v_lshlrev_b32 v130, 16, v28
v_and_b32 v131, 0xffff0000, v28
G 28,50
v_lshlrev_b32 v132, 16, v28
v_and_b32 v133, 0xffff0000, v28
G 28,51
v_lshlrev_b32 v134, 16, v28
v_and_b32 v135, 0xffff0000, v28
G 28,52
v_lshlrev_b32 v136, 16, v28
v_and_b32 v137, 0xffff0000, v28
G 28,53
v_lshlrev_b32 v138, 16, v28
v_and_b32 v139, 0xffff0000, v28
G 28,54
v_lshlrev_b32 v140, 16, v28
v_and_b32 v141, 0xffff0000, v28
G 28,55
v_lshlrev_b32 v142, 16, v28
v_and_b32 v143, 0xffff0000, v28
G 28,56
v_lshlrev_b32 v144, 16, v28
v_and_b32 v145, 0xffff0000, v28
G 28,57
v_lshlrev_b32 v146, 16, v28
v_and_b32 v147, 0xffff0000, v28
G 28,58
v_lshlrev_b32 v148, 16, v28
v_and_b32 v149, 0xffff0000, v28
G 28,59
v_lshlrev_b32 v150, 16, v28
v_and_b32 v151, 0xffff0000, v28
G 28,60
v_lshlrev_b32 v152, 16, v28
v_and_b32 v153, 0xffff0000, v28
G 28,61
v_lshlrev_b32 v154, 16, v28
v_and_b32 v155, 0xffff0000, v28
G 28,62
v_lshlrev_b32 v156, 16, v28
v_and_b32 v157, 0xffff0000, v28
G 28,63
v_lshlrev_b32 v158, 16, v28
v_and_b32 v159, 0xffff0000, v28
s_mov_b32 s22, 1.0
s_mov_b32 s23, 1.0
s_mov_b32 s29, 134217728
s_mov_b32 s71, 3584
s_barrier
Ra 128,242,0
Ra 132,242,1024
Ra 136,242,2048
Ra 140,242,3072
s_cmp_ge_u32 s14, 2
s_cbranch_scc1 .Lbgo%=
s_branch .Lago%=
.p2align 12
.Lpb_27%=:
s_waitcnt vmcnt(6) lgkmcnt(6)
Zvaa 32,0,32
s_add_u32 s28, s28, 512
Ta 20,4,8192
Ta 22,4,9216
Mvaa 32,4,48
s_cmp_eq_u32 s28, s4
s_cselect_b32 s93, 10240, 0
Ta 24,5,0
Ta 26,5,1024
Zvaa 64,4,52
s_sub_u32 s28, s28, s93
s_add_u32 s93, s28, 20480
buffer_load_dwordx4 a[128:131], v26, s[8:11], s28 offen offset:0 sc1
Mvaa 64,0,36
Ta 28,5,8192
Ta 30,5,9216
Zvaa 96,0,40
K 184,184,185
K 185,186,187
Mvaa 96,4,56
K 186,188,189
K 187,190,191
Zvaa 128,4,60
K 192,192,193
Mvaa 128,0,44
W 8
buffer_load_dwordx4 a[132:135], v26, s[8:11], s93 offen offset:0 sc1
K 193,194,195
Zvaa 132,8,44
Ta 0,6,0
Ta 2,6,1024
Mvaa 132,12,60
K 194,196,197
K 195,198,199
Zvaa 100,12,56
K 200,200,201
K 201,202,203
Mvaa 100,8,40
Ta 4,6,8192
Ta 6,6,9216
Zvaa 68,8,36
buffer_load_dwordx4 a[136:139], v27, s[8:11], s28 offen offset:0 sc1
K 202,204,205
Mvaa 68,12,52
K 203,206,207
K 208,208,209
Zvaa 36,12,48
K 209,210,211
K 210,212,213
Mvaa 36,8,32
W 8
Ta 8,7,0
K 211,214,215
Zvaa 40,16,32
Ta 10,7,1024
K 216,216,217
Mvaa 40,20,48
buffer_load_dwordx4 a[140:143], v27, s[8:11], s93 offen offset:0 sc1
K 217,218,219
Zvaa 72,20,52
s_add_u32 s28, s28, 512
s_cmp_eq_u32 s28, s4
s_cselect_b32 s93, 10240, 0
s_sub_u32 s28, s28, s93
Mvaa 72,16,36
s_add_u32 s93, s28, 20480
K 218,220,221
K 219,222,223
Zvaa 104,16,40
Ta 12,7,8192
Ta 14,7,9216
Mvaa 104,20,56
K 224,224,225
K 225,226,227
Zvaa 136,20,60
K 226,228,229
K 227,230,231
Mvaa 136,16,44
W 8
buffer_load_dwordx4 a[144:147], v26, s[8:11], s28 offen offset:0 sc1
K 232,232,233
Zvaa 140,24,44
K 233,234,235
K 234,236,237
Mvaa 140,28,60
Ta 16,8,0
K 235,238,239
Zvaa 108,28,56
Ta 18,8,1024
K 240,240,241
Mvaa 108,24,40
K 241,242,243
K 242,244,245
Zvaa 76,24,36
buffer_load_dwordx4 a[148:151], v26, s[8:11], s93 offen offset:0 sc1
K 243,246,247
Mvaa 76,28,52
Ta 20,8,8192
Ta 22,8,9216
Zvaa 44,28,48
K 248,248,249
K 249,250,251
Mvaa 44,24,32
K 250,252,253
K 251,254,255
W 8
Zvaa 48,0,32
Ta 24,9,0
Ta 26,9,1024
Mvaa 48,4,48
buffer_load_dwordx4 a[152:155], v27, s[8:11], s28 offen offset:0 sc1
K 160,160,161
Zvaa 80,4,52
K 161,162,163
K 162,164,165
Mvaa 80,0,36
Ta 28,9,8192
K 163,166,167
Zvaa 112,0,40
Ta 30,9,9216
K 168,168,169
Mvaa 112,4,56
K 169,170,171
K 170,172,173
Zvaa 144,4,60
buffer_load_dwordx4 a[156:159], v27, s[8:11], s93 offen offset:0 sc1
K 171,174,175
Mvaa 144,0,44
W 8
s_add_u32 s28, s28, 512
s_cmp_eq_u32 s28, s4
s_cselect_b32 s93, 10240, 0
Zvaa 148,8,44
s_sub_u32 s28, s28, s93
s_add_u32 s93, s28, 20480
G 164,184
Mvaa 148,12,60
G 165,185
G 166,186
Zvaa 116,12,56
Ta 0,2,16384
Ta 2,2,17408
Mvaa 116,8,40
buffer_load_dwordx4 v[188:191], v26, s[8:11], s28 offen offset:0 sc1
G 167,187
Zvaa 84,8,36
Ta 4,2,24576
G 172,188
Mvaa 84,12,52
Ta 6,2,25600
G 173,189
Zvaa 52,12,48
G 174,190
G 175,191
Mvaa 52,8,32
W 8
Ta 8,3,16384
K 164,164,165
Zvaa 56,16,32
buffer_load_dwordx4 v[196:199], v26, s[8:11], s93 offen offset:0 sc1
Ta 10,3,17408
Mvaa 56,20,48
K 165,166,167
K 166,172,173
Zvaa 88,20,52
Ta 12,3,24576
K 167,174,175
Mvaa 88,16,36
Ta 14,3,25600
v_accvgpr_write_b32 a184, v164
Zvaa 120,16,40
Mvaa 120,20,56
buffer_load_dwordx4 v[204:207], v27, s[8:11], s28 offen offset:0 sc1
v_accvgpr_write_b32 a185, v165
Zvaa 152,20,60
v_accvgpr_write_b32 a186, v166
Mvaa 152,16,44
W 8
Ta 16,4,16384
v_accvgpr_write_b32 a187, v167
Zvaa 156,24,44
Ta 18,4,17408
v_accvgpr_write_b32 a188, v240
Mvaa 156,28,60
Zvaa 124,28,56
buffer_load_dwordx4 v[212:215], v27, s[8:11], s93 offen offset:0 sc1
v_accvgpr_write_b32 a189, v241
Mvaa 124,24,40
s_add_u32 s28, s28, 512
s_cmp_eq_u32 s28, s4
s_cselect_b32 s93, 10240, 0
s_sub_u32 s28, s28, s93
Zvaa 92,24,36
s_add_u32 s93, s28, 20480
Ta 20,4,24576
Ta 22,4,25600
Mvaa 92,28,52
v_accvgpr_write_b32 a190, v242
v_accvgpr_write_b32 a191, v243
Zvaa 60,28,48
G 164,192
Mvaa 60,24,32
s_waitcnt vmcnt(4) lgkmcnt(8)
buffer_load_dwordx4 v[220:223], v26, s[8:11], s28 offen offset:0 sc1
G 165,193
Mvaa 32,0,128
Ta 24,5,16384
Ta 26,5,17408
Mvaa 32,4,144
G 166,194
G 167,195
Mvaa 64,4,148
Ta 28,5,24576
Ta 30,5,25600
Mvaa 64,0,132
G 172,196
Mvaa 96,0,136
buffer_load_dwordx4 v[228:231], v26, s[8:11], s93 offen offset:0 sc1
G 173,197
Mvaa 96,4,152
G 174,198
Mvaa 128,4,156
G 175,199
Mvaa 128,0,140
W 8
)"
R"(Ta 0,6,16384
Ta 2,6,17408
Mvaa 132,8,140
K 164,164,165
Mvaa 132,12,156
buffer_load_dwordx4 v[236:239], v27, s[8:11], s28 offen offset:0 sc1
K 165,166,167
Mvaa 100,12,152
Ta 4,6,24576
K 166,172,173
Mvaa 100,8,136
Ta 6,6,25600
K 167,174,175
Mvaa 68,8,132
v_accvgpr_write_b32 a192, v164
Mvaa 68,12,148
Mvaa 36,12,144
buffer_load_dwordx4 v[244:247], v27, s[8:11], s93 offen offset:0 sc1
v_accvgpr_write_b32 a193, v165
Mvaa 36,8,128
W 8
s_add_u32 s28, s28, 512
s_cmp_eq_u32 s28, s4
s_cselect_b32 s93, 10240, 0
Mvaa 40,16,128
s_sub_u32 s28, s28, s93
s_add_u32 s93, s28, 20480
v_accvgpr_write_b32 a194, v166
Mvaa 40,20,144
Ta 8,7,16384
Ta 10,7,17408
Mvaa 72,20,148
v_accvgpr_write_b32 a195, v167
v_accvgpr_write_b32 a196, v248
Mvaa 72,16,132
D 22,48,65,2048
Ta 12,7,24576
Mvaa 104,16,136
Ta 14,7,25600
v_accvgpr_write_b32 a197, v249
Mvaa 104,20,152
v_accvgpr_write_b32 a198, v250
v_accvgpr_write_b32 a199, v251
Mvaa 136,20,156
G 164,200
Mvaa 136,16,140
W 8
Ta 16,8,16384
Ta 18,8,17408
Mvaa 140,24,140
buffer_load_dwordx4 a[32:35], v26, s[8:11], s28 offen offset:0 sc1
G 165,201
Mvaa 140,28,156
G 166,202
Mvaa 108,28,152
Ta 20,8,24576
G 167,203
Mvaa 108,24,136
Ta 22,8,25600
G 172,204
Mvaa 76,24,132
Mvaa 76,28,148
buffer_load_dwordx4 a[36:39], v26, s[8:11], s93 offen offset:0 sc1
G 173,205
Mvaa 44,28,144
G 174,206
Mvaa 44,24,128
W 8
Ta 24,9,16384
G 175,207
Mvaa 48,0,128
Ta 26,9,17408
K 164,164,165
Mvaa 48,4,144
Mvaa 80,4,148
buffer_load_dwordx4 a[40:43], v27, s[8:11], s28 offen offset:0 sc1
K 165,166,167
Mvaa 80,0,132
Ta 28,9,24576
Ta 30,9,25600
Mvaa 112,0,136
s_lshl_b32 s80, s56, 9
K 166,172,173
K 167,174,175
Mvaa 112,4,152
v_accvgpr_write_b32 a200, v164
Mvaa 144,4,156
s_lshl_b32 s81, s57, 4
Mvaa 144,0,140
W 8
buffer_load_dwordx4 a[44:47], v27, s[8:11], s93 offen offset:0 sc1
v_accvgpr_write_b32 a201, v165
Mvaa 148,8,140
s_add_u32 s28, s28, 512
s_cmp_eq_u32 s28, s4
s_cselect_b32 s93, 10240, 0
s_sub_u32 s28, s28, s93
Mvaa 148,12,156
s_add_u32 s93, s28, 20480
s_add_u32 s80, s80, s81
Ta 0,10,0
Mvaa 116,12,152
s_and_b32 s81, s14, 1
Ta 2,10,1024
v_accvgpr_write_b32 a202, v166
Mvaa 116,8,136
s_lshl_b32 s81, s81, 3
Ta 4,10,8192
v_accvgpr_write_b32 a203, v167
Mvaa 84,8,132
s_add_u32 s80, s80, s81
buffer_load_dwordx4 a[48:51], v26, s[8:11], s28 offen offset:0 sc1
Ta 6,10,9216
Mvaa 84,12,148
s_add_u32 s71, s80, 4096
v_accvgpr_write_b32 a204, v184
v_accvgpr_write_b32 a205, v185
Mvaa 52,12,144
v_accvgpr_write_b32 a206, v186
v_accvgpr_write_b32 a207, v187
Mvaa 52,8,128
W 8
Ta 8,11,0
Ta 10,11,1024
Mvaa 56,16,128
G 164,208
Mvaa 56,20,144
D 23,48,65,3072
G 165,209
Mvaa 88,20,148
s_add_u32 s65, s5, 16
s_cmp_ge_u32 s65, 20
s_cselect_b32 s90, 20, 0
s_sub_u32 s65, s65, s90
Mvaa 88,16,132
s_lshl_b32 s65, s65, 18
s_add_u32 m0, s63, 98304
G 166,210
Mvaa 120,16,136
Ta 12,11,8192
Ta 14,11,9216
Mvaa 120,20,152
G 167,211
G 172,212
Mvaa 152,20,156
buffer_load_dwordx4 a[52:55], v26, s[8:11], s93 offen offset:0 sc1
G 173,213
Mvaa 152,16,140
W 8
Ta 16,12,0
G 174,214
Mvaa 156,24,140
s_lshr_b32 s80, s15, 3
Ta 18,12,1024
Mvaa 156,28,156
G 175,215
Mvaa 124,28,152
s_lshl_b32 s80, s80, 1
Ta 20,12,8192
K 164,164,165
Mvaa 124,24,136
s_add_u32 s80, s80, s14
buffer_load_dwordx4 a[56:59], v27, s[8:11], s28 offen offset:0 sc1
Ta 22,12,9216
Mvaa 92,24,132
s_sub_u32 s80, s80, 2
K 165,166,167
K 166,172,173
Mvaa 92,28,148
s_lshl_b32 s80, s80, 12
Mvaa 60,28,144
K 167,174,175
v_min_u32 v28, 496, v1
Mvaa 60,24,128
s_waitcnt vmcnt(9) lgkmcnt(8)
v_accvgpr_write_b32 a208, v164
v_lshl_add_u32 v28, v28, 3, s80
Mvav 32,0,188
buffer_load_dwordx4 a[60:63], v27, s[8:11], s93 offen offset:0 sc1
Ta 24,13,0
Mvav 32,4,220
s_add_u32 s28, s28, 512
s_cmp_eq_u32 s28, s4
s_cselect_b32 s93, 10240, 0
s_sub_u32 s28, s28, s93
Mvav 64,4,228
s_add_u32 s93, s28, 20480
Ta 26,13,1024
v_min_u32 v28, .Lend%=-.Lbeg%=-4, v28
Mvav 64,0,196
v_accvgpr_write_b32 a209, v165
v_accvgpr_write_b32 a210, v166
Mvav 96,0,204
Ta 28,13,8192
Ta 30,13,9216
Mvav 96,4,236
D 16,48,65,0
v_accvgpr_write_b32 a211, v167
Mvav 128,4,244
v_accvgpr_write_b32 a212, v192
v_accvgpr_write_b32 a213, v193
Mvav 128,0,212
W 8
Ta 0,14,0
v_accvgpr_write_b32 a214, v194
Mvav 132,8,212
Ta 2,14,1024
v_add_u32 v2, 65536, v10
Mvav 132,12,244
v_accvgpr_write_b32 a215, v195
G 164,216
Mvav 100,12,236
buffer_load_dwordx4 v[184:187], v26, s[8:11], s28 offen offset:0 sc1
Ta 4,14,8192
Mvav 100,8,204
Ta 6,14,9216
v_add_u32 v3, 65536, v11
Mvav 68,8,196
G 165,217
G 166,218
Mvav 68,12,228
G 167,219
Mvav 36,12,220
G 172,220
Mvav 36,8,188
W 8
D 17,48,65,1024
Ta 8,15,0
Mvav 40,16,188
Ta 10,15,1024
v_add_u32 v4, 65536, v12
Mvav 40,20,220
G 173,221
G 174,222
Mvav 72,20,228
Ta 12,15,8192
G 175,223
Mvav 72,16,196
Ta 14,15,9216
v_add_u32 v5, 65536, v13
Mvav 104,16,204
buffer_load_dwordx4 a[128:131], v26, s[8:11], s93 offen offset:0 sc1
K 164,164,165
Mvav 104,20,236
K 165,166,167
Mvav 136,20,244
K 166,172,173
Mvav 136,16,212
W 8
Ta 16,24,0
K 167,174,175
Mvav 140,24,212
Ta 18,24,1024
v_add_u32 v6, 65536, v14
Mvav 140,28,244
buffer_load_dwordx4 a[132:135], v27, s[8:11], s28 offen offset:0 sc1
v_accvgpr_write_b32 a216, v164
Mvav 108,28,236
Ta 20,24,8192
v_accvgpr_write_b32 a217, v165
Mvav 108,24,204
Ta 22,24,9216
v_add_u32 v7, 65536, v15
Mvav 76,24,196
v_accvgpr_write_b32 a218, v166
v_accvgpr_write_b32 a219, v167
Mvav 76,28,228
v_accvgpr_write_b32 a220, v200
Mvav 44,28,220
buffer_load_dwordx4 a[136:139], v27, s[8:11], s93 offen offset:0 sc1
Mvav 44,24,188
W 8
s_add_u32 s28, s28, 512
s_cmp_eq_u32 s28, s4
s_cselect_b32 s93, 10240, 0
Mvav 48,0,188
s_sub_u32 s28, s28, s93
s_add_u32 s93, s28, 20480
v_accvgpr_write_b32 a221, v201
Mvav 48,4,220
Ta 24,25,0
Ta 26,25,1024
Mvav 80,4,228
v_add_u32 v8, 65536, v24
v_accvgpr_write_b32 a222, v202
Mvav 80,0,196
buffer_load_dwordx4 a[140:143], v26, s[8:11], s28 offen offset:0 sc1
v_accvgpr_write_b32 a223, v203
Mvav 112,0,204
Ta 28,25,8192
Ta 30,25,9216
Mvav 112,4,236
v_add_u32 v9, 65536, v25
G 164,224
Mvav 144,4,244
G 165,225
G 166,226
Mvav 144,0,212
W 8
Ta 0,10,16384
G 167,227
Mvav 148,8,212
global_load_dword v29, v28, s[6:7]
Ta 2,10,17408
Mvav 148,12,244
s_sub_u32 s74, s85, 66
s_and_b32 s75, s74, 0xffffffe3
s_cselect_b64 s[72:73], -1, 0
s_and_b32 s74, s74, 28
Mvav 116,12,236
s_lshl_b32 s74, s74, 1
v_mov_b32 v28, s74
G 172,228
Mvav 116,8,204
G 173,229
G 174,230
Mvav 84,8,196
Ta 4,10,24576
Ta 6,10,25600
Mvav 84,12,228
buffer_load_dwordx4 a[144:147], v26, s[8:11], s93 offen offset:0 sc1
G 175,231
Mvav 52,12,220
K 164,164,165
K 165,166,167
Mvav 52,8,188
W 8
Ta 8,11,16384
K 166,172,173
Mvav 56,16,188
Ta 10,11,17408
K 167,174,175
Mvav 56,20,220
v_accvgpr_write_b32 a224, v164
Mvav 88,20,228
buffer_load_dwordx4 a[148:151], v27, s[8:11], s28 offen offset:0 sc1
Ta 12,11,24576
Mvav 88,16,196
Ta 14,11,25600
v_accvgpr_write_b32 a225, v165
Mvav 120,16,204
v_accvgpr_write_b32 a226, v166
Mvav 120,20,236
v_accvgpr_write_b32 a227, v167
Mvav 152,20,244
v_accvgpr_write_b32 a228, v208
Mvav 152,16,212
W 8
D 18,48,65,2048
Ta 16,12,16384
Mvav 156,24,212
Ta 18,12,17408
v_accvgpr_write_b32 a229, v209
Mvav 156,28,244
v_accvgpr_write_b32 a230, v210
Mvav 124,28,236
Ta 20,12,24576
v_accvgpr_write_b32 a231, v211
Mvav 124,24,204
Ta 22,12,25600
G 164,232
Mvav 92,24,196
buffer_load_dwordx4 a[152:155], v27, s[8:11], s93 offen offset:0 sc1
G 165,233
Mvav 92,28,228
s_add_u32 s28, s28, 512
s_cmp_eq_u32 s28, s4
s_cselect_b32 s93, 10240, 0
s_sub_u32 s28, s28, s93
Mvav 60,28,220
s_add_u32 s93, s28, 20480
G 166,234
G 167,235
Mvav 60,24,188
s_waitcnt vmcnt(12) lgkmcnt(8)
G 172,236
G 173,237
Mvaa 32,0,32
Ta 24,13,16384
Ta 26,13,17408
Mvaa 32,4,48
buffer_load_dwordx2 v[28:29], v28, s[8:11], 0 offen offset:3584 sc1
G 174,238
Mvaa 64,4,52
G 175,239
K 164,164,165
Mvaa 64,0,36
Ta 28,13,24576
Ta 30,13,25600
Mvaa 96,0,40
K 165,166,167
K 166,172,173
Mvaa 96,4,56
K 167,174,175
v_accvgpr_write_b32 a232, v164
Mvaa 128,4,60
buffer_load_dwordx4 a[156:159], v26, s[8:11], s28 offen offset:0 sc1
v_accvgpr_write_b32 a233, v165
Mvaa 128,0,44
W 8
)"
R"(v_accvgpr_write_b32 a234, v166
v_accvgpr_write_b32 a235, v167
Mvaa 132,8,44
Ta 0,14,16384
Ta 2,14,17408
Mvaa 132,12,60
G 164,160
G 165,161
Mvaa 100,12,56
G 166,162
G 167,163
Mvaa 100,8,40
buffer_load_dwordx4 v[192:195], v26, s[8:11], s93 offen offset:0 sc1
Ta 4,14,24576
Mvaa 68,8,36
Ta 6,14,25600
G 172,164
Mvaa 68,12,52
G 173,165
G 174,166
Mvaa 36,12,48
G 175,167
K 164,164,165
Mvaa 36,8,32
W 8
Ta 8,15,16384
K 165,166,167
Mvaa 40,16,32
buffer_load_dwordx4 v[200:203], v27, s[8:11], s28 offen offset:0 sc1
Ta 10,15,17408
Mvaa 40,20,48
K 166,172,173
K 167,174,175
Mvaa 72,20,52
s_cmp_lg_u32 s72, 0
s_cbranch_scc1 .LL_51%=
s_waitcnt vmcnt(3)
.LL_51%=:
v_xor_b32 v28, s12, v28
v_xor_b32 v29, s13, v29
v_lshrrev_b32 v248, 11, v28
v_or_b32 v29, v29, v248
v_cmp_eq_u32 vcc, 0, v29
s_or_b64 vcc, vcc, s[72:73]
s_cmp_eq_u64 vcc, -1
s_cbranch_scc0 .Lspin_52%=
.Lok_53%=:
v_accvgpr_write_b32 a236, v164
v_accvgpr_write_b32 a237, v165
Mvaa 72,16,36
Ta 12,15,24576
Ta 14,15,25600
Mvaa 104,16,40
v_accvgpr_write_b32 a238, v166
v_accvgpr_write_b32 a239, v167
Mvaa 104,20,56
D 19,48,65,3072
G 164,240
Mvaa 136,20,60
s_add_u32 m0, s63, 102400
G 165,241
G 166,242
Mvaa 136,16,44
W 8
v_bfe_u32 v248, v28, 3, 8
G 167,243
Mvaa 140,24,44
Ta 16,24,16384
Ta 18,24,17408
Mvaa 140,28,60
v_mul_u32_u24 v248, 10240, v248
G 172,244
Mvaa 108,28,56
buffer_load_dwordx4 v[188:191], v27, s[8:11], s93 offen offset:0 sc1
G 173,245
Mvaa 108,24,40
s_add_u32 s28, s28, 512
s_cmp_eq_u32 s28, s4
s_cselect_b32 s93, 10240, 0
s_sub_u32 s28, s28, s93
Mvaa 76,24,36
s_add_u32 s93, s28, 20480
v_and_b32 v28, 7, v28
G 174,246
Mvaa 76,28,52
v_lshl_add_u32 v248, v28, 4, v248
G 175,247
Mvaa 44,28,48
Ta 20,24,24576
Ta 22,24,25600
Mvaa 44,24,32
W 8
buffer_load_dwordx4 v[196:199], v26, s[8:11], s28 offen offset:0 sc1
K 164,164,165
Mvaa 48,0,32
v_lshl_add_u32 v248, v1, 3, v248
K 165,166,167
Mvaa 48,4,48
v_lshrrev_b32 v29, 8, v1
K 166,172,173
Mvaa 80,4,52
K 167,174,175
v_lshlrev_b32 v29, 28, v29
Mvaa 80,0,36
v_accvgpr_write_b32 a240, v164
v_or_b32 v249, v248, v29
Mvaa 112,0,40
s_and_b32 s75, s72, 134217728
buffer_load_dwordx4 v[204:207], v26, s[8:11], s93 offen offset:0 sc1
v_accvgpr_write_b32 a241, v165
Mvaa 112,4,56
s_add_u32 s76, s75, 131072
Ta 24,25,16384
Ta 26,25,17408
Mvaa 144,4,60
v_accvgpr_write_b32 a242, v166
v_accvgpr_write_b32 a243, v167
Mvaa 144,0,44
W 6
G 164,168
G 165,169
Mvaa 148,8,44
Ta 28,25,24576
Ta 30,25,25600
Mvaa 148,12,60
D 20,48,65,0
G 166,170
Mvaa 116,12,56
G 167,171
G 172,172
Mvaa 116,8,40
G 173,173
G 174,174
Mvaa 84,8,36
Ta 0,2,0
G 175,175
Mvaa 84,12,52
Ta 2,2,1024
K 164,164,165
Mvaa 52,12,48
buffer_load_dwordx4 v[212:215], v27, s[8:11], s28 offen offset:0 sc1
K 165,166,167
Mvaa 52,8,32
W 6
K 166,172,173
K 167,174,175
Mvaa 56,16,32
Ta 4,2,8192
Ta 6,2,9216
Mvaa 56,20,48
v_accvgpr_write_b32 a244, v164
v_accvgpr_write_b32 a245, v165
Mvaa 88,20,52
v_accvgpr_write_b32 a246, v166
v_accvgpr_write_b32 a247, v167
Mvaa 88,16,36
buffer_load_dwordx4 v[220:223], v27, s[8:11], s93 offen offset:0 sc1
G 164,64
Mvaa 120,16,40
s_add_u32 s28, s28, 512
s_cmp_eq_u32 s28, s4
s_cselect_b32 s93, 10240, 0
s_sub_u32 s28, s28, s93
Mvaa 120,20,56
s_add_u32 s93, s28, 20480
Ta 8,3,0
G 165,65
Mvaa 152,20,60
Ta 10,3,1024
G 166,66
Mvaa 152,16,44
W 6
G 167,67
G 172,68
Mvaa 156,24,44
buffer_load_dwordx4 v[228:231], v26, s[8:11], s28 offen offset:0 sc1
G 173,69
Mvaa 156,28,60
Ta 12,3,8192
Ta 14,3,9216
Mvaa 124,28,56
G 174,70
G 175,71
Mvaa 124,24,40
K 164,164,165
K 165,166,167
Mvaa 92,24,36
K 166,172,173
K 167,174,175
Mvaa 92,28,52
buffer_load_dwordx4 v[240:243], v248, s[8:11], s76 offen sc1
Ta 16,4,0
Mvaa 60,28,48
s_add_u32 s76, s76, 8192
Ta 18,4,1024
v_accvgpr_write_b32 a64, v164
Mvaa 60,24,32
s_waitcnt vmcnt(13) lgkmcnt(6)
v_accvgpr_write_b32 a65, v165
v_accvgpr_write_b32 a66, v166
Mvav 32,0,184
Ta 20,4,8192
v_accvgpr_write_b32 a67, v167
Mvaa 32,4,140
Ta 22,4,9216
G 164,176
Mvaa 64,4,144
buffer_load_dwordx4 v[244:247], v249, s[8:11], s76 offen sc1
v_or_b32 v249, v1, v29
Mvaa 64,0,128
s_mul_i32 s76, s74, 160
s_add_u32 s76, s76, s75
s_add_u32 s76, s76, 118784
Mvaa 96,0,132
G 165,177
G 166,178
Mvaa 96,4,148
G 167,179
G 172,180
Mvaa 128,4,152
Ta 24,5,0
G 173,181
Mvaa 128,0,136
W 5
buffer_load_dwordx4 v[236:239], v26, s[8:11], s93 offen offset:0 sc1
Ta 26,5,1024
Mvaa 132,8,136
G 174,182
G 175,183
Mvaa 132,12,152
K 164,164,165
K 165,166,167
Mvaa 100,12,148
Ta 28,5,8192
Ta 30,5,9216
Mvaa 100,8,132
K 166,172,173
K 167,174,175
Mvaa 68,8,128
D 21,48,65,1024
v_accvgpr_write_b32 a68, v164
Mvaa 68,12,144
v_accvgpr_write_b32 a69, v165
v_accvgpr_write_b32 a70, v166
Mvaa 36,12,140
Ta 0,6,0
v_accvgpr_write_b32 a71, v167
Mvav 36,8,184
W 5
Ta 2,6,1024
G 164,72
Mvav 40,16,184
G 165,73
G 166,74
Mvaa 40,20,140
buffer_load_dwordx4 v[208:211], v27, s[8:11], s28 offen offset:0 sc1
Ta 4,6,8192
Mvaa 72,20,144
Ta 6,6,9216
G 167,75
Mvaa 72,16,128
G 172,76
G 173,77
Mvaa 104,16,132
Ta 8,7,0
G 174,78
Mvaa 104,20,148
Ta 10,7,1024
G 175,79
Mvaa 136,20,152
buffer_load_dwordx4 a[32:35], v27, s[8:11], s93 offen offset:0 sc1
K 164,164,165
Mvaa 136,16,136
W 6
s_add_u32 s28, s28, 512
s_cmp_eq_u32 s28, s4
s_cselect_b32 s93, 10240, 0
Mvaa 140,24,136
s_sub_u32 s28, s28, s93
s_add_u32 s93, s28, 20480
Ta 12,7,8192
Mvaa 140,28,152
Ta 14,7,9216
K 165,166,167
Mvaa 108,28,148
K 166,172,173
K 167,174,175
Mvaa 108,24,132
buffer_load_dwordx4 a[36:39], v26, s[8:11], s28 offen offset:0 sc1
v_accvgpr_write_b32 a72, v164
Mvaa 76,24,128
Ta 16,8,0
Ta 18,8,1024
Mvaa 76,28,144
v_accvgpr_write_b32 a73, v165
v_accvgpr_write_b32 a74, v166
Mvaa 44,28,140
Ta 20,8,8192
v_accvgpr_write_b32 a75, v167
Mvav 44,24,184
W 7
Ta 22,8,9216
v_accvgpr_write_b32 a76, v216
Mvav 48,0,184
D 22,48,65,2048
v_accvgpr_write_b32 a77, v217
Mvaa 48,4,140
v_accvgpr_write_b32 a78, v218
v_accvgpr_write_b32 a79, v219
Mvaa 80,4,144
Ta 24,9,0
Ta 26,9,1024
Mvaa 80,0,128
G 164,80
G 165,81
Mvaa 112,0,132
Ta 28,9,8192
Ta 30,9,9216
Mvaa 112,4,148
buffer_load_dwordx4 a[40:43], v26, s[8:11], s93 offen offset:0 sc1
G 166,82
Mvaa 144,4,152
G 167,83
G 172,84
Mvaa 144,0,136
W 8
Ta 0,2,16384
G 173,85
Mvaa 148,8,136
Ta 2,2,17408
G 174,86
Mvaa 148,12,152
G 175,87
Mvaa 116,12,148
buffer_load_dwordx4 a[44:47], v27, s[8:11], s28 offen offset:0 sc1
Ta 4,2,24576
Mvaa 116,8,132
Ta 6,2,25600
K 164,164,165
Mvaa 84,8,128
K 165,166,167
Mvaa 84,12,144
K 166,172,173
Mvaa 52,12,140
K 167,174,175
Mvav 52,8,184
W 8
buffer_load_dwordx4 a[48:51], v27, s[8:11], s93 offen offset:0 sc1
Ta 8,3,16384
Mvav 56,16,184
s_add_u32 s28, s28, 512
s_cmp_eq_u32 s28, s4
s_cselect_b32 s93, 10240, 0
D 23,48,65,3072
Mvaa 56,20,140
s_sub_u32 s28, s28, s93
s_add_u32 s93, s28, 20480
Ta 10,3,17408
Mvaa 88,20,144
v_accvgpr_write_b32 a80, v164
v_accvgpr_write_b32 a81, v165
Mvaa 88,16,128
Ta 12,3,24576
v_accvgpr_write_b32 a82, v166
Mvaa 120,16,132
Ta 14,3,25600
v_accvgpr_write_b32 a83, v167
Mvaa 120,20,148
buffer_load_dwordx4 a[52:55], v26, s[8:11], s28 offen offset:0 sc1
v_accvgpr_write_b32 a84, v224
Mvaa 152,20,152
v_lshrrev_b32 v20, 4, v1
v_accvgpr_write_b32 a85, v225
Mvaa 152,16,136
W 8
v_accvgpr_write_b32 a86, v226
v_add_u32 v20, 0, v20
Mvaa 156,24,136
Ta 16,4,16384
Ta 18,4,17408
Mvaa 156,28,152
v_accvgpr_write_b32 a87, v227
v_mul_u32_u24 v22, 205, v20
Mvaa 124,28,148
buffer_load_dwordx4 a[56:59], v26, s[8:11], s93 offen offset:0 sc1
G 164,88
Mvaa 124,24,132
Ta 20,4,24576
Ta 22,4,25600
Mvaa 92,24,128
v_lshrrev_b32 v22, 12, v22
G 165,89
Mvaa 92,28,144
G 166,90
v_mul_u32_u24 v23, 20, v22
Mvaa 60,28,140
G 167,91
v_sub_u32 v20, v20, v23
Mvav 60,24,184
s_waitcnt vmcnt(15) lgkmcnt(8)
)"
R"(buffer_load_dwordx4 a[60:63], v27, s[8:11], s28 offen offset:0 sc1
G 172,92
Mvaa 32,0,156
Ta 24,5,16384
Ta 26,5,17408
Mvav 32,4,196
v_lshrrev_b32 v23, 1, v22
G 173,93
Mvav 64,4,204
G 174,94
v_and_b32 v23, 2, v23
Mvav 64,0,192
Ta 28,5,24576
Ta 30,5,25600
Mvav 96,0,200
buffer_load_dwordx4 v[216:219], v27, s[8:11], s93 offen offset:0 sc1
G 175,95
Mvav 96,4,212
s_add_u32 s28, s28, 512
s_cmp_eq_u32 s28, s4
s_cselect_b32 s93, 10240, 0
s_sub_u32 s28, s28, s93
Mvav 128,4,220
s_add_u32 s93, s28, 20480
v_xor_b32 v20, v20, v23
K 164,164,165
Mvav 128,0,188
W 8
v_mul_u32_u24 v22, 80, v22
K 165,166,167
Mvav 132,8,188
K 166,172,173
v_add_lshl_u32 v16, v20, v22, 4
Mvav 132,12,220
buffer_load_dwordx4 v[184:187], v26, s[8:11], s28 offen offset:0 sc1
Ta 0,6,16384
Mvav 100,12,212
Ta 2,6,17408
K 167,174,175
Mvav 100,8,200
v_lshrrev_b32 v20, 4, v1
v_accvgpr_write_b32 a88, v164
Mvav 68,8,192
v_add_u32 v20, 64, v20
v_accvgpr_write_b32 a89, v165
Mvav 68,12,204
Ta 4,6,24576
Ta 6,6,25600
Mvav 36,12,196
s_cmp_lg_u32 s72, 0
s_cbranch_scc1 .LL_55%=
s_waitcnt vmcnt(15)
.LL_55%=:
buffer_store_dwordx4 v[240:243], v1, s[8:11], s76 offen sc1
v_accvgpr_write_b32 a90, v166
v_mul_u32_u24 v22, 205, v20
Mvaa 36,8,156
W 8
v_accvgpr_write_b32 a91, v167
v_lshrrev_b32 v22, 12, v22
Mvaa 40,16,156
v_accvgpr_write_b32 a92, v232
v_mul_u32_u24 v23, 20, v22
Mvav 40,20,196
Ta 8,7,16384
Ta 10,7,17408
Mvav 72,20,204
v_accvgpr_write_b32 a93, v233
v_accvgpr_write_b32 a94, v234
Mvav 72,16,192
buffer_store_dwordx4 v[244:247], v249, s[8:11], s76 offen offset:1024 sc1
v_sub_u32 v20, v20, v23
Mvav 104,16,200
s_add_u32 s76, s74, s75
s_add_u32 s76, s76, 3840
v_accvgpr_write_b32 a95, v235
Mvav 104,20,212
Ta 12,7,24576
Ta 14,7,25600
Mvav 136,20,220
v_lshrrev_b32 v23, 1, v22
G 164,96
Mvav 136,16,188
W 8
G 165,97
v_and_b32 v23, 2, v23
Mvav 140,24,188
buffer_load_dwordx4 a[128:131], v26, s[8:11], s93 offen offset:0 sc1
G 166,98
Mvav 140,28,220
G 167,99
v_xor_b32 v20, v20, v23
Mvav 108,28,212
G 172,100
G 173,101
Mvav 108,24,200
Ta 16,8,16384
Ta 18,8,17408
Mvav 76,24,192
v_mul_u32_u24 v22, 80, v22
G 174,102
Mvav 76,28,204
buffer_load_dwordx4 a[132:135], v27, s[8:11], s28 offen offset:0 sc1
G 175,103
Mvav 44,28,196
v_add_lshl_u32 v17, v20, v22, 4
K 164,164,165
Mvaa 44,24,156
W 6
Ta 20,8,24576
Ta 22,8,25600
Mvaa 48,0,156
K 165,166,167
K 166,172,173
Mvav 48,4,196
v_subrev_u32 v17, 1024, v17
K 167,174,175
Mvav 80,4,204
buffer_load_dwordx4 a[136:139], v27, s[8:11], s93 offen offset:0 sc1
v_accvgpr_write_b32 a96, v164
Mvav 80,0,192
s_add_u32 s28, s28, 512
v_lshrrev_b32 v20, 4, v1
v_accvgpr_write_b32 a97, v165
Mvav 112,0,200
s_cmp_eq_u32 s28, s4
s_cselect_b32 s93, 10240, 0
v_accvgpr_write_b32 a98, v166
v_add_u32 v20, 128, v20
Mvav 112,4,212
s_sub_u32 s28, s28, s93
Ta 24,9,16384
v_accvgpr_write_b32 a99, v167
Mvav 144,4,220
s_add_u32 s93, s28, 20480
Ta 26,9,17408
G 164,248
Mvav 144,0,188
W 6
buffer_load_dwordx4 a[140:143], v26, s[8:11], s28 offen offset:0 sc1
v_mul_u32_u24 v22, 205, v20
Mvav 148,8,188
G 165,249
G 166,250
Mvav 148,12,220
Ta 28,9,24576
Ta 30,9,25600
Mvav 116,12,212
v_lshrrev_b32 v22, 12, v22
G 167,251
Mvav 116,8,200
G 172,252
G 173,253
Mvav 84,8,192
s_waitcnt vmcnt(11) lgkmcnt(0)
s_barrier
Mvav 84,12,204
v_mul_u32_u24 v23, 20, v22
G 174,254
Mvav 52,12,196
G 175,255
v_sub_u32 v20, v20, v23
Mvaa 52,8,156
K 164,164,165
K 165,166,167
Mvaa 56,16,156
v_lshrrev_b32 v23, 1, v22
K 166,172,173
Mvav 56,20,196
buffer_load_dwordx4 a[144:147], v26, s[8:11], s93 offen offset:0 sc1
K 167,174,175
Mvav 88,20,204
v_and_b32 v23, 2, v23
v_accvgpr_write_b32 a100, v164
Mvav 88,16,192
v_accvgpr_write_b32 a101, v165
v_xor_b32 v20, v20, v23
Mvav 120,16,200
v_accvgpr_write_b32 a102, v166
v_accvgpr_write_b32 a103, v167
Mvav 120,20,212
Ta 0,10,32768
Ta 2,10,33792
Mvav 152,20,220
Ta 4,10,40960
Ta 6,10,41984
Mvav 152,16,188
G 164,104
v_mul_u32_u24 v22, 80, v22
Mvav 156,24,188
G 165,105
v_add_lshl_u32 v18, v20, v22, 4
Mvav 156,28,220
Ta 8,11,32768
G 166,106
Mvav 124,28,212
Ta 10,11,33792
G 167,107
Mvav 124,24,200
buffer_load_dwordx4 a[148:151], v27, s[8:11], s28 offen offset:0 sc1
v_subrev_u32 v18, 2048, v18
Mvav 92,24,192
G 172,108
G 173,109
Mvav 92,28,204
Ta 12,11,40960
v_lshrrev_b32 v20, 4, v1
Mvav 60,28,196
Ta 14,11,41984
G 174,110
Mvaa 60,24,156
W 4
v_add_u32 v20, 192, v20
G 175,111
Mvav 32,0,228
K 164,164,165
v_mul_u32_u24 v22, 205, v20
Mvaa 32,4,36
K 165,166,167
v_lshrrev_b32 v22, 12, v22
Mvaa 64,4,40
Ta 16,12,32768
K 166,172,173
Mvav 64,0,236
Ta 18,12,33792
K 167,174,175
Mvav 96,0,208
buffer_load_dwordx4 a[152:155], v27, s[8:11], s93 offen offset:0 sc1
v_mul_u32_u24 v23, 20, v22
Mvaa 96,4,44
s_add_u32 s28, s28, 512
v_accvgpr_write_b32 a104, v164
v_accvgpr_write_b32 a105, v165
Mvaa 128,4,48
s_cmp_eq_u32 s28, s4
s_cselect_b32 s93, 10240, 0
Ta 20,12,40960
v_sub_u32 v20, v20, v23
Mvaa 128,0,32
W 3
Ta 22,12,41984
v_accvgpr_write_b32 a106, v166
Mvaa 132,8,32
s_sub_u32 s28, s28, s93
v_lshrrev_b32 v23, 1, v22
v_accvgpr_write_b32 a107, v167
Mvaa 132,12,48
s_add_u32 s93, s28, 20480
K 176,176,177
v_and_b32 v23, 2, v23
Mvaa 100,12,44
K 177,178,179
K 178,180,181
Mvav 100,8,208
Ta 24,13,32768
v_xor_b32 v20, v20, v23
Mvav 68,8,236
Ta 26,13,33792
K 179,182,183
Mvaa 68,12,40
v_accvgpr_write_b32 a108, v176
v_accvgpr_write_b32 a109, v177
v_accvgpr_write_b32 a110, v178
v_accvgpr_write_b32 a111, v179
Mvaa 36,12,36
buffer_load_dwordx4 v[224:227], v26, s[8:11], s28 offen offset:0 sc1
v_mul_u32_u24 v22, 80, v22
Mvav 36,8,228
W 2
Ta 28,13,40960
Ta 30,13,41984
Mvav 40,16,228
v_add_lshl_u32 v19, v20, v22, 4
v_subrev_u32 v19, 3072, v19
Mvaa 40,20,36
G 164,112
v_lshrrev_b32 v20, 4, v1
Mvaa 72,20,40
Ta 0,14,32768
G 165,113
Mvav 72,16,236
s_cmp_lg_u32 s72, 0
s_cbranch_scc1 .LL_56%=
s_waitcnt vmcnt(8)
.LL_56%=:
v_mov_b64 v[28:29], s[12:13]
s_mov_b64 exec, 1
buffer_store_dwordx2 v[28:29], off, s[8:11], s76 sc1
s_mov_b64 exec, -1
Ta 2,14,33792
G 166,114
Mvav 104,16,208
v_add_u32 v20, 256, v20
G 167,115
Mvaa 104,20,44
Ta 4,14,40960
v_mul_u32_u24 v22, 205, v20
Mvaa 136,20,48
Ta 6,14,41984
G 172,116
Mvaa 136,16,32
W 4
v_lshrrev_b32 v22, 12, v22
G 173,117
Mvaa 140,24,32
buffer_load_dwordx4 v[232:235], v26, s[8:11], s93 offen offset:0 sc1
G 174,118
Mvaa 140,28,48
v_mul_u32_u24 v23, 20, v22
G 175,119
Mvaa 108,28,44
Ta 8,15,32768
v_sub_u32 v20, v20, v23
Mvav 108,24,208
Ta 10,15,33792
K 164,164,165
Mvav 76,24,236
v_lshrrev_b32 v23, 1, v22
K 165,166,167
Mvaa 76,28,40
buffer_load_dwordx4 a[248:251], v27, s[8:11], s28 offen offset:0 sc1
K 166,172,173
Mvaa 44,28,36
Ta 12,15,40960
v_and_b32 v23, 2, v23
Mvav 44,24,228
W 3
Ta 14,15,41984
K 167,174,175
Mvav 48,0,228
v_xor_b32 v20, v20, v23
v_accvgpr_write_b32 a112, v164
Mvaa 48,4,36
v_accvgpr_write_b32 a113, v165
v_mul_u32_u24 v22, 80, v22
Mvaa 80,4,40
Ta 16,24,32768
v_accvgpr_write_b32 a114, v166
Mvav 80,0,236
Ta 18,24,33792
v_add_lshl_u32 v20, v20, v22, 4
Mvav 112,0,208
buffer_load_dwordx4 a[252:255], v27, s[8:11], s93 offen offset:0 sc1
v_accvgpr_write_b32 a115, v167
Mvaa 112,4,44
s_add_u32 s28, s28, 512
v_lshrrev_b32 v22, 6, v1
v_accvgpr_write_b32 a116, v168
Mvaa 144,4,48
v_accvgpr_write_b32 a117, v169
v_accvgpr_write_b32 a118, v170
v_accvgpr_write_b32 a119, v171
Mvaa 144,0,32
W 2
Ta 20,24,40960
Ta 22,24,41984
Mvaa 148,8,32
s_cmp_eq_u32 s28, s4
s_cselect_b32 s93, 10240, 0
v_mul_u32_u24 v22, 320, v22
v_bfe_u32 v23, v1, 4, 2
Mvaa 148,12,48
s_sub_u32 s28, s28, s93
v_lshl_add_u32 v22, v23, 3, v22
G 164,120
Mvaa 116,12,44
s_add_u32 s93, s28, 20480
Ta 24,25,32768
Ta 26,25,33792
Mvav 116,8,208
G 165,121
v_bfe_u32 v23, v1, 8, 1
Mvav 84,8,236
buffer_load_dwordx4 v[192:195], v26, s[8:11], s28 offen offset:0 sc1
G 166,122
Mvaa 84,12,40
v_lshlrev_b32 v23, 5, v23
G 167,123
Mvaa 52,12,36
Ta 28,25,40960
Ta 30,25,41984
Mvav 52,8,228
W 4
v_add_u32 v21, v22, v23
G 172,124
Mvav 56,16,228
G 173,125
v_sub_u32 v22, v22, v23
Mvaa 56,20,36
s_mul_i32 s94, s59, 655360
Ta 0,10,49152
G 174,126
Mvaa 88,20,40
s_mul_i32 s95, s58, 320
Ta 2,10,50176
G 175,127
Mvav 88,16,236
s_add_u32 s94, s94, s95
buffer_load_dwordx4 v[200:203], v26, s[8:11], s93 offen offset:0 sc1
K 164,164,165
)"
R"(Mvav 120,16,208
Ta 4,10,57344
K 165,166,167
Mvaa 120,20,44
Ta 6,10,58368
K 166,172,173
Mvaa 152,20,48
s_add_u32 s40, s26, s94
s_addc_u32 s41, s27, 0
K 167,174,175
Mvaa 152,16,32
W 4
v_accvgpr_write_b32 a120, v164
v_accvgpr_write_b32 a121, v165
Mvaa 156,24,32
s_and_b32 s41, s41, 0xffff
Ta 8,11,49152
Ta 10,11,50176
Mvaa 156,28,48
s_mov_b32 s42, 5242880
buffer_load_dwordx4 v[188:191], v27, s[8:11], s28 offen offset:0 sc1
v_accvgpr_write_b32 a122, v166
Mvaa 124,28,44
s_mov_b32 s43, 0x20000
Ta 12,11,57344
v_accvgpr_write_b32 a123, v167
Mvav 124,24,208
s_sub_u32 s42, s42, s94
Ta 14,11,58368
v_accvgpr_write_b32 a124, v160
Mvav 92,24,236
s_mov_b64 s[44:45], s[8:9]
v_accvgpr_write_b32 a125, v161
v_accvgpr_write_b32 a126, v162
Mvaa 92,28,40
s_mov_b32 s47, 0x20000
Ta 16,12,49152
v_accvgpr_write_b32 a127, v163
Mvaa 60,28,36
s_and_b32 s48, s14, 1
Ta 18,12,50176
buffer_load_dwordx4 v[196:199], v27, s[8:11], s93 offen offset:0 sc1
Mvav 60,24,228
s_waitcnt vmcnt(13) lgkmcnt(6)
v_bfe_u32 v28, v1, 4, 2
v_lshlrev_b32 v28, 3, v28
Mvaa 32,0,52
s_lshl_b32 s80, s56, 9
Ta 20,12,57344
Ta 22,12,58368
Mvav 32,4,184
s_mul_i32 s48, s48, 5120
s_add_u32 s80, s80, 4096
s_lshr_b32 s51, s14, 1
Mvaa 64,4,128
s_and_b32 s94, s14, 1
Ta 24,13,49152
Ta 26,13,50176
Mvaa 64,0,56
s_add_u32 s28, s28, 512
s_cmp_eq_u32 s28, s4
s_cselect_b32 s93, 10240, 0
buffer_load_dwordx2 v[28:29], v28, s[8:11], s80 offen sc1
Mvaa 96,0,60
s_sub_u32 s28, s28, s93
s_add_u32 s93, s28, 20480
s_lshl_b32 s95, s57, 1
Mvaa 96,4,132
s_add_u32 s94, s94, s95
Ta 28,13,57344
Ta 30,13,58368
Mvaa 128,4,136
s_lshl_b32 s54, s94, 3
s_add_u32 s54, s54, s59
Mvav 128,0,216
W 8
Ta 0,14,49152
Ta 2,14,50176
Mvav 132,8,216
s_lshl_b32 s54, s54, 6
buffer_load_dwordx4 v[204:207], v26, s[8:11], s28 offen offset:0 sc1
Mvaa 132,12,136
s_xor_b32 s55, s54, 15872
Mvaa 100,12,132
Ta 4,14,57344
Ta 6,14,58368
Mvaa 100,8,60
s_add_u32 s54, s54, 20480
Mvaa 68,8,56
s_and_b32 s81, s14, 1
Mvaa 68,12,128
s_add_u32 s55, s55, 20480
buffer_load_dwordx4 v[212:215], v26, s[8:11], s93 offen offset:0 sc1
Mvav 36,12,184
Mvaa 36,8,52
W 8
s_and_b32 s95, s14, 2
s_mul_i32 s91, s81, 17164288
Mvaa 40,16,52
s_lshl_b32 s94, s94, 2
Ta 8,15,49152
Ta 10,15,50176
Mvav 40,20,184
s_mul_i32 s82, s56, 536576
Mvaa 72,20,128
s_add_u32 s94, s94, s95
buffer_load_dwordx4 v[220:223], v27, s[8:11], s28 offen offset:0 sc1
Ta 12,15,57344
Mvaa 72,16,56
Ta 14,15,58368
Mvaa 104,16,60
s_lshl_b32 s95, s95, 4
s_add_u32 s91, s91, s82
Mvaa 104,20,132
Mvaa 136,20,136
s_add_u32 s55, s55, s95
s_add_u32 s91, s91, 4194304
Mvav 136,16,216
W 8
s_lshl_b32 s49, s94, 17
Ta 16,24,49152
Mvav 140,24,216
Ta 18,24,50176
buffer_load_dwordx4 v[176:179], v27, s[8:11], s93 offen offset:0 sc1
Mvaa 140,28,136
s_lshl_b32 s95, s59, 14
s_lshr_b32 s82, s14, 1
Mvaa 108,28,132
Ta 20,24,57344
Ta 22,24,58368
Mvaa 108,24,60
s_add_u32 s49, s49, s95
s_lshl_b32 s83, s82, 14
Mvaa 76,24,56
s_xor_b32 s50, s49, 16252928
Mvaa 76,28,128
Mvav 44,28,184
s_add_u32 s91, s91, s83
s_add_u32 s49, s49, 39845888
Mvaa 44,24,52
W 8
Ta 24,25,49152
Ta 26,25,50176
Mvaa 48,0,52
s_add_u32 s50, s50, 39845888
s_lshl_b32 s81, s81, 14
Mvav 48,4,184
Mvaa 80,4,128
s_lshr_b32 s94, s15, 3
Ta 28,25,57344
Ta 30,25,58368
Mvaa 80,0,56
s_lshl_b32 s82, s82, 16
s_lshl_b32 s94, s94, 3
Mvaa 112,0,60
Mvaa 112,4,132
s_add_u32 s94, s94, s14
s_or_b32 s92, s81, s82
Mvaa 144,4,136
Mvav 144,0,216
W 8
s_lshl_b32 s94, s94, 4
Ta 0,2,32768
Mvav 148,8,216
s_xor_b32 s92, s92, 0x10000
s_add_u32 s94, s94, s59
Ta 2,2,33792
Mvaa 148,12,136
Mvaa 116,12,132
s_mul_i32 s52, s94, 5120
Ta 4,2,40960
Ta 6,2,41984
Mvaa 116,8,60
Mvaa 84,8,56
s_lshl_b32 s95, s59, 12
Mvaa 84,12,128
Mvav 52,12,184
s_sub_u32 s52, s52, s95
Mvaa 52,8,52
W 8
s_add_u32 s52, s52, 56623104
Ta 8,3,32768
Mvaa 56,16,52
Ta 10,3,33792
Mvav 56,20,184
s_lshl_b32 s53, s94, 3
Mvaa 88,20,128
Ta 12,3,40960
Ta 14,3,41984
Mvaa 88,16,56
s_add_u32 s53, s53, 65536
Mvaa 120,16,60
s_waitcnt vmcnt(4)
v_cmp_eq_u64 vcc, s[12:13], v[28:29]
s_cmp_eq_u64 vcc, -1
s_cbranch_scc0 .Lspin_57%=
.Lok_58%=:
s_sub_u32 s82, s91, 16384
Mvaa 120,20,132
s_xor_b32 s83, s92, 0x10000
s_and_b32 s5, s14, 1
s_lshl_b32 s5, s5, 13
Mvaa 152,20,136
s_add_u32 s5, s82, s5
s_add_u32 m0, s83, 0
s_add_u32 s81, s5, 0
Mvav 152,16,216
W 8
v_bfe_u32 v244, v1, 4, 1
v_mul_u32_u24 v244, 0xaaaa, v244
Mvav 156,24,216
Dc 1,8,81,0
v_add_u32 v244, 0x4444, v244
Mvaa 156,28,136
Ta 16,4,32768
Ta 18,4,33792
Mvaa 124,28,132
v_lshl_or_b32 v240, v244, 16, v244
v_mov_b32 v241, 0xff800000
Mvaa 124,24,60
Ta 20,4,40960
Ta 22,4,41984
Mvaa 92,24,56
v_add_u32 v242, 0x10000, v1
Mvaa 92,28,128
Dc 1,8,81,1024
v_bfe_u32 v244, v1, 4, 4
Mvav 60,28,184
v_lshrrev_b32 v245, 8, v1
Mvaa 60,24,52
W 8
Ta 24,5,32768
v_lshlrev_b32 v245, 2, v245
Mvaa 32,0,140
Ta 26,5,33792
Mvav 32,4,224
v_sub_u32 v244, v244, v245
Mvav 64,4,232
Dc 1,8,81,2048
v_cmp_le_i32_e64 s[72:73], 0, v244
Mvaa 64,0,144
Ta 28,5,40960
Ta 30,5,41984
Mvaa 96,0,148
v_cmp_le_i32_e64 s[74:75], 1, v244
Mvaa 96,4,248
v_cmp_le_i32_e64 s[76:77], 2, v244
Mvaa 128,4,252
v_cmp_le_i32_e64 s[78:79], 3, v244
Mvaa 128,0,152
W 8
Dc 1,8,81,3072
v_add_u32 v243, 4096, v1
Mvaa 132,8,152
s_add_u32 m0, s83, 4096
s_add_u32 s81, s5, 4096
Ta 0,6,32768
Mvaa 132,12,252
Ta 2,6,33792
v_add_u32 v244, 8192, v1
Mvaa 100,12,248
Ta 4,6,40960
v_add_u32 v245, 12288, v1
Mvaa 100,8,148
Ta 6,6,41984
Mvaa 68,8,144
s_mov_b32 s18, 0x3f803f80
Dc 1,8,81,0
Mvav 68,12,232
s_mov_b32 s19, 0x3f003f00
Mvav 36,12,224
v_accvgpr_write_b32 a176, s18
Mvaa 36,8,140
W 8
Ta 8,7,32768
Ta 10,7,33792
Mvaa 40,16,140
v_accvgpr_write_b32 a180, s19
Mvav 40,20,224
Dc 1,8,81,1024
v_mov_b32 v252, v241
Mvav 72,20,232
Ta 12,7,40960
v_accvgpr_write_b32 a177, s18
Mvaa 72,16,144
Ta 14,7,41984
Mvaa 104,16,148
v_accvgpr_write_b32 a181, s19
Mvaa 104,20,248
v_mov_b32 v253, v241
Mvaa 136,20,252
Dc 1,8,81,2048
v_accvgpr_write_b32 a178, s18
Mvaa 136,16,152
W 8
Ta 16,8,32768
Ta 18,8,33792
Mvaa 140,24,152
v_accvgpr_write_b32 a182, s19
Mvaa 140,28,252
v_mov_b32 v254, v241
Mvaa 108,28,248
Ta 20,8,40960
v_accvgpr_write_b32 a179, s18
Mvaa 108,24,148
Dc 1,8,81,3072
Ta 22,8,41984
Mvaa 76,24,144
s_add_u32 m0, s83, 8192
s_add_u32 s81, s82, 8192
v_accvgpr_write_b32 a183, s19
Mvav 76,28,232
v_mov_b32 v255, v241
Mvav 44,28,224
Mvaa 44,24,140
W 8
Ta 24,9,32768
v_accvgpr_write_b32 a160, 0
Mvaa 48,0,140
Dc 1,8,81,0
Ta 26,9,33792
Mvav 48,4,224
v_accvgpr_write_b32 a161, 0
v_accvgpr_write_b32 a162, 0
Mvav 80,4,232
Ta 28,9,40960
Ta 30,9,41984
Mvaa 80,0,144
v_accvgpr_write_b32 a163, 0
Mvaa 112,0,148
v_accvgpr_write_b32 a164, 0
Mvaa 112,4,248
Dc 1,8,81,1024
v_accvgpr_write_b32 a165, 0
Mvaa 144,4,252
Mvaa 144,0,152
W 8
Ta 0,2,49152
v_accvgpr_write_b32 a166, 0
Mvaa 148,8,152
Ta 2,2,50176
v_accvgpr_write_b32 a167, 0
Mvaa 148,12,252
v_accvgpr_write_b32 a168, 0
Mvaa 116,12,248
Dc 1,8,81,2048
Ta 4,2,57344
Mvaa 116,8,148
Ta 6,2,58368
v_accvgpr_write_b32 a169, 0
Mvaa 84,8,144
v_accvgpr_write_b32 a170, 0
Mvav 84,12,232
v_accvgpr_write_b32 a171, 0
Mvav 52,12,224
Mvaa 52,8,140
W 8
Dc 1,8,81,3072
v_accvgpr_write_b32 a172, 0
Mvaa 56,16,140
s_add_u32 m0, s83, 12288
s_add_u32 s81, s82, 12288
Ta 8,3,49152
Mvav 56,20,224
Ta 10,3,50176
v_accvgpr_write_b32 a173, 0
Mvav 88,20,232
Ta 12,3,57344
v_accvgpr_write_b32 a174, 0
Mvaa 88,16,144
Ta 14,3,58368
v_accvgpr_write_b32 a175, 0
Mvaa 120,16,148
s_mov_b32 s90, 0x5d800000
Dc 1,8,81,0
Mvaa 120,20,248
Mvaa 152,20,252
s_mov_b64 s[88:89], 0
Mvaa 152,16,152
W 8
s_mov_b32 s64, 0
Ta 16,4,49152
Mvaa 156,24,152
s_and_b32 s18, s14, 1
Ta 18,4,50176
Mvaa 156,28,252
Dc 1,8,81,1024
Mvaa 124,28,248
s_lshl_b32 s62, s18, 14
Ta 20,4,57344
Ta 22,4,58368
Mvaa 124,24,148
s_lshl_b32 s20, s57, 15
Mvaa 92,24,144
s_sub_u32 s20, 507904, s20
Mvav 92,28,232
Mvav 60,28,224
s_add_u32 s20, s20, s62
Dc 1,8,81,2048
Mvaa 60,24,140
)"
R"(s_waitcnt vmcnt(15) lgkmcnt(8)
s_lshl_b32 s65, s57, 1
Ta 24,5,49152
Mvav 32,0,192
s_sub_u32 s65, s65, 1
Ta 26,5,50176
Mvav 32,4,204
Mvav 64,4,212
s_max_i32 s65, s65, 0
Ta 28,5,57344
Ta 30,5,58368
Mvav 64,0,200
s_sub_u32 s66, s60, 1
Dc 1,8,81,3072
v_bfe_u32 v28, v1, 4, 4
Mvav 96,0,188
v_lshlrev_b32 v28, 3, v28
Mvav 96,4,220
s_lshl_b32 s80, s56, 9
s_cmp_lt_u32 s14, 2
s_cselect_b32 s20, s20, 0
s_cselect_b32 s66, s66, 15
s_cselect_b32 s21, 32768, 0
s_add_u32 s80, s80, 4096
Mvav 128,4,176
s_add_u32 s61, s91, s20
Mvav 128,0,196
W 8
s_add_u32 s62, s62, s21
Ta 0,6,49152
Mvav 132,8,196
buffer_load_dwordx2 v[28:29], v28, s[8:11], s80 offen sc1
Ta 2,6,50176
Mvav 132,12,176
s_sub_u32 s63, 16384, s21
Mvav 100,12,220
s_lshl_b32 s67, s60, 7
Ta 4,6,57344
Ta 6,6,58368
Mvav 100,8,188
s_add_u32 s67, s67, s56
Mvav 68,8,200
Mvav 68,12,212
s_lshl_b32 s67, s67, 12
Mvav 36,12,204
s_add_u32 s67, s67, 39845888
Mvav 36,8,192
W 8
s_lshl_b32 s69, s60, 3
Ta 8,7,49152
Mvav 40,16,192
Ta 10,7,50176
Mvav 40,20,204
s_add_u32 s69, s69, s59
Mvav 72,20,212
s_lshl_b32 s69, s69, 6
Ta 12,7,57344
Ta 14,7,58368
Mvav 72,16,200
s_lshl_b32 s21, s58, 3
Mvav 104,16,188
Mvav 104,20,220
s_add_u32 s69, s69, s21
Mvav 136,20,176
s_add_u32 s69, s69, 20480
Mvav 136,16,196
W 8
Ta 16,8,49152
Ta 18,8,50176
Mvav 140,24,196
s_mov_b32 s70, 0
Mvav 140,28,176
s_mov_b32 s37, 0
Mvav 108,28,220
s_sub_u32 s18, s57, 4
Ta 20,8,57344
Ta 22,8,58368
Mvav 108,24,188
Mvav 76,24,200
s_lshl_b32 s18, s18, 2
Mvav 76,28,212
s_add_u32 s18, s18, s58
Mvav 44,28,204
s_mov_b32 s85, s59
Mvav 44,24,192
W 8
Ta 24,9,49152
Ta 26,9,50176
Mvav 48,0,192
s_mul_i32 s85, s85, 7
Mvav 48,4,204
s_add_u32 s85, s85, s18
Mvav 80,4,212
s_cmp_lt_u32 s18, 7
s_cselect_b32 s85, s85, 99
Ta 28,9,57344
Ta 30,9,58368
Mvav 80,0,200
Mvav 112,0,188
s_cmp_lt_u32 s85, 54
s_cselect_b32 s65, 2, s65
Mvav 112,4,220
s_mov_b32 s68, 0
Mvav 144,4,176
s_mov_b64 s[24:25], -1
Mvav 144,0,196
W 8
Mvav 148,8,196
s_mov_b32 s22, 0x3e0293ee
Mvav 148,12,176
s_mov_b32 s23, 0x3e0293ee
Mvav 116,12,220
Mvav 116,8,188
Mvav 84,8,200
Mvav 84,12,212
Mvav 52,12,204
Mvav 52,8,192
W 4
Mvav 56,16,192
Mvav 56,20,204
s_waitcnt vmcnt(0)
v_cmp_eq_u64 vcc, s[12:13], v[28:29]
s_cmp_eq_u64 vcc, -1
s_cbranch_scc0 .Lspin_60%=
.Lok_61%=:
Mvav 88,20,212
Mvav 88,16,200
Mvav 120,16,188
W 0
s_barrier
Mvav 120,20,220
Ra 128,242,0
Mvav 152,20,176
Ra 132,242,1024
Mvav 152,16,196
Mvav 156,24,196
Ra 136,242,2048
Mvav 156,28,176
Mvav 124,28,220
Ra 140,242,3072
Mvav 124,24,188
Mvav 92,24,200
Mvav 92,28,212
Mvav 60,28,204
Mvav 60,24,192
.Lbgo%=:
v_pk_mul_f32 v[32:33], v[32:33], s[22:23]
v_pk_mul_f32 v[34:35], v[34:35], s[22:23]
v_pk_mul_f32 v[36:37], v[36:37], s[22:23]
v_pk_mul_f32 v[38:39], v[38:39], s[22:23]
v_pk_mul_f32 v[64:65], v[64:65], s[22:23]
v_pk_mul_f32 v[66:67], v[66:67], s[22:23]
v_pk_mul_f32 v[68:69], v[68:69], s[22:23]
v_pk_mul_f32 v[70:71], v[70:71], s[22:23]
v_pk_mul_f32 v[96:97], v[96:97], s[22:23]
v_pk_mul_f32 v[98:99], v[98:99], s[22:23]
v_pk_mul_f32 v[100:101], v[100:101], s[22:23]
v_pk_mul_f32 v[102:103], v[102:103], s[22:23]
v_pk_mul_f32 v[128:129], v[128:129], s[22:23]
v_pk_mul_f32 v[130:131], v[130:131], s[22:23]
v_pk_mul_f32 v[132:133], v[132:133], s[22:23]
v_pk_mul_f32 v[134:135], v[134:135], s[22:23]
v_pk_mul_f32 v[40:41], v[40:41], s[22:23]
v_pk_mul_f32 v[42:43], v[42:43], s[22:23]
v_pk_mul_f32 v[44:45], v[44:45], s[22:23]
v_pk_mul_f32 v[46:47], v[46:47], s[22:23]
v_pk_mul_f32 v[72:73], v[72:73], s[22:23]
v_pk_mul_f32 v[74:75], v[74:75], s[22:23]
v_pk_mul_f32 v[76:77], v[76:77], s[22:23]
v_pk_mul_f32 v[78:79], v[78:79], s[22:23]
v_pk_mul_f32 v[104:105], v[104:105], s[22:23]
v_pk_mul_f32 v[106:107], v[106:107], s[22:23]
v_pk_mul_f32 v[108:109], v[108:109], s[22:23]
v_pk_mul_f32 v[110:111], v[110:111], s[22:23]
v_pk_mul_f32 v[136:137], v[136:137], s[22:23]
v_pk_mul_f32 v[138:139], v[138:139], s[22:23]
v_pk_mul_f32 v[140:141], v[140:141], s[22:23]
v_pk_mul_f32 v[142:143], v[142:143], s[22:23]
v_pk_mul_f32 v[48:49], v[48:49], s[22:23]
v_pk_mul_f32 v[50:51], v[50:51], s[22:23]
v_pk_mul_f32 v[52:53], v[52:53], s[22:23]
v_pk_mul_f32 v[54:55], v[54:55], s[22:23]
v_pk_mul_f32 v[80:81], v[80:81], s[22:23]
v_pk_mul_f32 v[82:83], v[82:83], s[22:23]
v_pk_mul_f32 v[84:85], v[84:85], s[22:23]
v_pk_mul_f32 v[86:87], v[86:87], s[22:23]
v_pk_mul_f32 v[112:113], v[112:113], s[22:23]
v_pk_mul_f32 v[114:115], v[114:115], s[22:23]
v_pk_mul_f32 v[116:117], v[116:117], s[22:23]
v_pk_mul_f32 v[118:119], v[118:119], s[22:23]
v_pk_mul_f32 v[144:145], v[144:145], s[22:23]
v_pk_mul_f32 v[146:147], v[146:147], s[22:23]
v_pk_mul_f32 v[148:149], v[148:149], s[22:23]
v_pk_mul_f32 v[150:151], v[150:151], s[22:23]
v_pk_mul_f32 v[56:57], v[56:57], s[22:23]
v_pk_mul_f32 v[58:59], v[58:59], s[22:23]
v_pk_mul_f32 v[60:61], v[60:61], s[22:23]
v_pk_mul_f32 v[62:63], v[62:63], s[22:23]
v_pk_mul_f32 v[88:89], v[88:89], s[22:23]
v_pk_mul_f32 v[90:91], v[90:91], s[22:23]
v_pk_mul_f32 v[92:93], v[92:93], s[22:23]
v_pk_mul_f32 v[94:95], v[94:95], s[22:23]
v_pk_mul_f32 v[120:121], v[120:121], s[22:23]
v_pk_mul_f32 v[122:123], v[122:123], s[22:23]
v_pk_mul_f32 v[124:125], v[124:125], s[22:23]
v_pk_mul_f32 v[126:127], v[126:127], s[22:23]
v_pk_mul_f32 v[152:153], v[152:153], s[22:23]
v_pk_mul_f32 v[154:155], v[154:155], s[22:23]
v_pk_mul_f32 v[156:157], v[156:157], s[22:23]
v_pk_mul_f32 v[158:159], v[158:159], s[22:23]
K 32,32,33
K 33,34,35
K 34,36,37
K 35,38,39
K 40,40,41
K 41,42,43
K 42,44,45
K 43,46,47
W 0
Zvav 196,128,32
s_add_u32 m0, s62, 0
Ra 144,242,4096
Ra 148,242,5120
Ra 152,242,6144
Ra 156,242,7168
Mvav 196,132,40
K 64,64,65
K 65,66,67
K 66,68,69
K 67,70,71
K 72,72,73
K 73,74,75
K 74,76,77
K 75,78,79
s_nop 1
Zvav 204,132,72
Dc 1,8,61,0
K 96,96,97
K 97,98,99
K 98,100,101
Mvav 204,128,64
K 99,102,103
K 104,104,105
K 105,106,107
K 106,108,109
K 107,110,111
Zvav 212,128,96
Dc 1,8,61,1024
K 128,128,129
K 129,130,131
K 130,132,133
Mvav 212,132,104
K 131,134,135
K 136,136,137
K 137,138,139
K 138,140,141
K 139,142,143
s_nop 1
Zvav 220,132,136
Dc 1,8,61,2048
K 48,48,49
K 49,50,51
K 50,52,53
Mvav 220,128,128
K 51,54,55
K 56,56,57
K 57,58,59
K 58,60,61
K 59,62,63
Mvav 196,136,48
Ra 128,242,8192
Ra 132,242,9216
K 80,80,81
K 81,82,83
Mvav 196,140,56
K 82,84,85
K 83,86,87
K 88,88,89
K 89,90,91
K 90,92,93
K 91,94,95
s_nop 1
Mvav 204,140,88
Dc 1,8,61,3072
s_add_u32 m0, s62, 4096
K 112,112,113
K 113,114,115
K 114,116,117
Mvav 204,136,80
K 115,118,119
K 120,120,121
K 121,122,123
K 122,124,125
K 123,126,127
Mvav 212,136,112
Dc 243,8,61,0
K 144,144,145
K 145,146,147
K 146,148,149
Mvav 212,140,120
K 147,150,151
K 152,152,153
K 153,154,155
K 154,156,157
K 155,158,159
s_nop 1
Mvav 220,140,152
s_add_u32 s80, s62, 131072
Dc 243,8,61,1024
v_add_u32 v12, s80, v1
Mvav 220,136,144
W 2
ds_write_b128 v12, v[32:35] offset:0
Ra 136,242,10240
Zvav 36,144,32
Ra 0,12,0
Ra 140,242,11264
Dc 243,8,61,2048
Mvav 36,148,40
v_max3_f32 v4, v196, v197, v198
v_max3_f32 v4, v4, v199, v252
v_max3_f32 v5, v204, v205, v206
v_max3_f32 v5, v5, v207, v253
Zvav 68,148,72
v_max3_f32 v6, v212, v213, v214
v_max3_f32 v6, v6, v215, v254
v_max3_f32 v7, v220, v221, v222
v_max3_f32 v7, v7, v223, v255
Mvav 68,144,64
v_mov_b32 v8, v4
v_mov_b32 v9, v5
v_mov_b32 v10, v6
v_mov_b32 v11, v7
Zvav 100,144,96
ds_write_b128 v12, v[40:43] offset:1024
v_permlane32_swap_b32 v4, v8
v_permlane32_swap_b32 v5, v9
v_permlane32_swap_b32 v6, v10
Mvav 100,148,104
v_permlane32_swap_b32 v7, v11
v_max_f32 v4, v4, v8
v_max_f32 v5, v5, v9
v_max_f32 v6, v6, v10
Zvav 132,148,136
v_max_f32 v7, v7, v11
v_mov_b32 v8, v4
v_mov_b32 v9, v5
v_mov_b32 v10, v6
Mvav 132,144,128
Ra 4,12,1024
v_mov_b32 v11, v7
v_permlane16_swap_b32 v4, v8
v_permlane16_swap_b32 v5, v9
Mvav 36,152,48
v_permlane16_swap_b32 v6, v10
v_permlane16_swap_b32 v7, v11
v_max_f32 v4, v4, v8
v_max_f32 v5, v5, v9
Mvav 36,156,56
v_max_f32 v6, v6, v10
v_max_f32 v7, v7, v11
v_sub_f32 v4, 0, v4
v_sub_f32 v5, 0, v5
Mvav 68,156,88
v_sub_f32 v6, 0, v6
v_sub_f32 v7, 0, v7
v_mov_b32 v224, v4
v_mov_b32 v225, v4
Mvav 68,152,80
v_mov_b32 v226, v4
v_mov_b32 v227, v4
v_mov_b32 v228, v5
v_mov_b32 v229, v5
Mvav 100,152,112
v_mov_b32 v230, v5
v_mov_b32 v231, v5
v_mov_b32 v232, v6
v_mov_b32 v233, v6
Mvav 100,156,120
v_mov_b32 v234, v6
v_mov_b32 v235, v6
v_mov_b32 v236, v7
v_mov_b32 v237, v7
Mvav 132,156,152
Dc 243,8,61,3072
s_add_u32 m0, s62, 8192
ds_write_b128 v12, v[48:51] offset:2048
v_mov_b32 v238, v7
v_mov_b32 v239, v7
Mvav 132,152,144
W 3
Ra 8,12,2048
ds_write_b128 v12, v[56:59] offset:3072
Ra 144,242,12288
Ra 12,12,3072
Cvav 160,128,32,224
s_and_b32 s80, s14, 1
Dc 244,8,61,0
Ra 148,242,13312
ds_write_b128 v12, v[64:67] offset:4096
)"
R"(Ra 16,12,4096
Mvav 160,132,40
s_lshl_b32 s80, s80, 14
ds_write_b128 v12, v[72:75] offset:5120
Ra 20,12,5120
ds_write_b128 v12, v[80:83] offset:6144
A 196,196,224
Cvav 164,132,72,228
s_add_u32 s80, s80, 98304
Dc 244,8,61,1024
Ra 24,12,6144
A 197,197,224
A 198,198,224
Mvav 164,128,64
Ra 152,242,14336
A 199,199,224
X 196
Cvav 168,128,96,232
Dc 244,8,61,2048
Ra 156,242,15360
X 197
Mvav 168,132,104
ds_write_b128 v12, v[88:91] offset:7168
Ra 28,12,7168
X 198
Cvav 172,132,136,236
Dc 244,8,61,3072
s_add_u32 m0, s62, 12288
K 192,196,197
X 199
Mvav 172,128,128
ds_write_b128 v12, v[96:99] offset:8192
A 204,204,228
v_add_u32 v8, s80, v1
K 193,198,199
Mvav 160,136,48
Dc 245,8,61,0
Ra 32,12,8192
A 205,205,228
A 206,206,228
Mvav 160,140,56
ds_write_b128 v8, v[4:7]
A 207,207,228
X 204
Mvav 164,140,88
Dc 245,8,61,1024
ds_write_b128 v12, v[104:107] offset:9216
X 205
Mvav 164,136,80
Ra 36,12,9216
X 206
K 200,204,205
Mvav 168,136,112
Dc 245,8,61,2048
Ra 128,242,24576
X 207
Mvav 168,140,120
ds_write_b128 v12, v[112:115] offset:10240
Ra 132,242,25600
A 212,212,232
K 201,206,207
Mvav 172,140,152
Dc 245,8,61,3072
s_add_u32 s61, s61, s63
Ra 40,12,10240
A 213,213,232
A 214,214,232
Mvav 172,136,144
W 11
ds_write_b128 v12, v[120:123] offset:11264
A 215,215,232
X 212
Cvav 176,144,32,224
X 213
X 214
Mvav 176,148,40
Ra 44,12,11264
K 208,212,213
X 215
Cvav 180,148,72,228
ds_write_b128 v12, v[128:131] offset:12288
A 220,220,236
K 209,214,215
A 221,221,236
Mvav 180,144,64
Ra 48,12,12288
Ra 136,242,26624
A 222,222,236
A 223,223,236
Cvav 184,144,96,232
ds_write_b128 v12, v[136:139] offset:13312
Ra 140,242,27648
X 220
Mvav 184,148,104
X 221
X 222
Cvav 188,148,136,236
Ra 52,12,13312
K 216,220,221
X 223
Mvav 188,144,128
ds_write_b128 v12, v[144:147] offset:14336
Ra 56,12,14336
A 36,36,224
K 217,222,223
Mvav 176,152,48
ds_write_b128 v12, v[152:155] offset:15360
A 37,37,224
A 38,38,224
A 39,39,224
Mvav 176,156,56
X 36
X 37
Mvav 180,156,88
Ra 60,12,15360
X 38
K 194,36,37
Mvav 180,152,80
Ra 144,242,28672
X 39
A 68,68,228
Mvav 184,152,112
Ra 148,242,29696
K 195,38,39
A 69,69,228
A 70,70,228
Mvav 184,156,120
W 3
s_barrier
A 71,71,228
X 68
Mvav 188,156,152
X 69
X 70
K 202,68,69
X 71
A 100,100,232
K 203,70,71
A 101,101,232
A 102,102,232
A 103,103,232
X 100
X 101
X 102
K 210,100,101
X 103
A 132,132,236
K 211,102,103
A 133,133,236
A 134,134,236
A 135,135,236
X 132
X 133
X 134
K 218,132,133
X 135
s_nop 0
K 219,134,135
Mvav 188,152,144
Ra 152,242,30720
Zvav 32,128,192
Ra 156,242,31744
Zvav 64,128,200
v_xor_b32 v242, 0x10000, v242
Zvav 96,128,208
s_xor_b32 s62, s62, 0x10000
Zvav 128,128,216
v_add_u32 v23, 61440, v21
Zvav 132,132,216
v_add_u32 v24, 61440, v22
Zvav 100,132,208
v_add_u32 v25, 122880, v21
Zvav 68,132,200
v_add_u32 v26, 122880, v22
Zvav 36,132,192
Zvav 40,136,192
X 172
Zvav 72,136,200
X 173
Zvav 104,136,208
X 174
K 220,172,173
Zvav 136,136,216
X 175
Zvav 140,140,216
X 168
K 221,174,175
Zvav 108,140,208
X 169
X 170
Zvav 76,140,200
s_waitcnt vmcnt(0) lgkmcnt(0)
s_barrier
K 212,168,169
X 171
Zvav 44,140,192
Ra 128,242,0
X 164
Zvav 48,144,192
K 213,170,171
X 165
Zvav 80,144,200
X 166
Zvav 112,144,208
Ra 132,242,1024
K 204,164,165
X 167
Zvav 144,144,216
X 160
Zvav 148,148,216
K 205,166,167
X 161
Zvav 116,148,208
Ra 136,242,2048
X 162
Zvav 84,148,200
K 196,160,161
X 163
Zvav 52,148,192
X 176
K 197,162,163
Zvav 56,152,192
Ra 140,242,3072
X 177
Zvav 88,152,200
X 178
K 198,176,177
Zvav 120,152,208
X 179
X 180
Zvav 152,152,216
K 199,178,179
Zvav 156,156,216
X 181
X 182
Zvav 124,156,208
K 206,180,181
X 183
Zvav 92,156,200
K 207,182,183
Zvav 60,156,192
s_cmp_eq_u64 s[24:25], 0
s_cbranch_scc1 .Ll1_30%=
W 0
Cvaa 160,128,0,224
Ra 144,242,4096
Ra 148,242,5120
Mvaa 160,132,4
s_add_u32 m0, s62, 0
Ra 152,242,6144
Cvaa 164,132,20,228
X 184
Mvaa 164,128,16
X 185
Cvaa 168,128,32,232
X 186
Mvaa 168,132,36
Ra 156,242,7168
K 214,184,185
Cvaa 172,132,52,236
Dc 1,8,61,0
Mvaa 172,128,48
X 187
Mvaa 160,136,8
X 188
Mvaa 160,140,12
Ra 128,242,16384
K 215,186,187
Mvaa 164,140,28
Dc 1,8,61,1024
Mvaa 164,136,24
X 189
Mvaa 168,136,40
X 190
Mvaa 168,140,44
Ra 132,242,17408
K 222,188,189
Mvaa 172,140,60
Dc 1,8,61,2048
Mvaa 172,136,56
X 191
v_smfmac_f32_16x16x64_bf16 a[160:163], a[176:179], v[192:199], v240
Ra 136,242,18432
K 223,190,191
v_smfmac_f32_16x16x64_bf16 a[164:167], a[176:179], v[200:207], v240
X 160
v_smfmac_f32_16x16x64_bf16 a[168:171], a[176:179], v[208:215], v240
Dc 1,8,61,3072
v_smfmac_f32_16x16x64_bf16 a[172:175], a[176:179], v[216:223], v240
W 3
X 161
Cvaa 176,144,0,224
s_add_u32 m0, s62, 4096
X 162
Mvaa 176,148,4
Ra 140,242,19456
K 192,160,161
Cvaa 180,148,20,228
X 163
Mvaa 180,144,16
Dc 243,8,61,0
Cvaa 184,144,32,232
X 164
Mvaa 184,148,36
K 193,162,163
Cvaa 188,148,52,236
X 165
Mvaa 188,144,48
Dc 243,8,61,1024
Ra 144,242,20480
Mvaa 176,152,8
X 166
Mvaa 176,156,12
Ra 148,242,21504
K 200,164,165
Mvaa 180,156,28
X 167
Mvaa 180,152,24
Dc 243,8,61,2048
Mvaa 184,152,40
X 168
Mvaa 184,156,44
K 201,166,167
Mvaa 188,156,60
X 169
Mvaa 188,152,56
W 2
Dc 243,8,61,3072
Ra 152,242,22528
Mvav 32,128,196
s_add_u32 m0, s62, 8192
X 170
Mvav 64,128,204
Ra 156,242,23552
K 208,168,169
Mvav 96,128,212
X 171
Mvav 128,128,220
Dc 244,8,61,0
Mvav 132,132,220
X 172
Mvav 100,132,212
Ra 128,242,8192
K 209,170,171
Mvav 68,132,204
X 173
Mvav 36,132,196
Dc 244,8,61,1024
Mvav 40,136,196
X 174
Mvav 72,136,204
Ra 132,242,9216
K 216,172,173
Mvav 104,136,212
X 175
Mvav 136,136,220
Dc 244,8,61,2048
Mvav 140,140,220
X 176
Mvav 108,140,212
Ra 136,242,10240
K 217,174,175
Mvav 76,140,204
X 177
Mvav 44,140,196
W 3
Dc 244,8,61,3072
Mvav 48,144,196
X 178
Mvav 80,144,204
s_add_u32 m0, s62, 12288
Ra 140,242,11264
Mvav 112,144,212
K 194,176,177
Mvav 144,144,220
X 179
Mvav 148,148,220
X 180
Mvav 116,148,212
Dc 245,8,61,0
Ra 144,242,12288
Mvav 84,148,204
K 195,178,179
Mvav 52,148,196
X 181
Mvav 56,152,196
s_cmp_eq_u32 s14, 2
s_cselect_b64 s[18:19], s[30:31], s[32:33]
X 182
Mvav 88,152,204
s_mul_i32 s81, s58, 6
Dc 245,8,61,1024
Ra 148,242,13312
Mvav 120,152,212
s_add_u32 s81, s81, s57
K 202,180,181
Mvav 152,152,220
s_sub_u32 s81, s81, 2
X 183
Mvav 156,156,220
)"
R"(s_cmp_ge_u32 s57, 2
s_cselect_b32 s23, 2211836, 0
X 184
Mvav 124,156,212
s_mul_i32 s82, s81, 48384
Dc 245,8,61,2048
Ra 152,242,14336
Mvav 92,156,204
s_mov_b32 s83, s59
K 203,182,183
Mvav 60,156,196
W 3
X 185
Cvaa 160,128,0,224
s_mul_i32 s83, s83, 448
X 186
Mvaa 160,132,4
s_andn2_b32 s83, s83, 127
Dc 245,8,61,3072
Ra 156,242,15360
Cvaa 164,132,20,228
s_add_u32 s82, s82, s83
s_add_u32 s61, s61, s63
K 210,184,185
Mvaa 164,128,16
v_min_u32 v246, 880, v1
v_lshrrev_b32 v247, 6, v246
Cvaa 168,128,32,232
X 187
Mvaa 168,132,36
X 188
Cvaa 172,132,52,236
s_xor_b32 s62, s62, 0x10000
v_mul_u32_u24 v247, 3456, v247
v_and_b32 v246, 48, v246
Mvaa 172,128,48
K 211,186,187
v_lshl_add_u32 v246, v246, 3, v247
Mvaa 160,136,8
X 189
Mvaa 160,140,12
s_add_u32 s64, s64, 1
Ra 128,242,24576
v_add_u32 v246, s82, v246
Mvaa 164,140,28
X 190
Mvaa 164,136,24
Ra 132,242,25600
v_min_u32 v246, s23, v246
Mvaa 168,136,40
global_load_dword v3, v246, s[18:19]
K 218,188,189
Mvaa 168,140,44
X 191
Mvaa 172,140,60
X 160
Mvaa 172,136,56
W 2
Ra 136,242,26624
K 219,190,191
Cvaa 176,144,0,224
X 161
Mvaa 176,148,4
Ra 140,242,27648
Cvaa 180,148,20,228
X 162
Mvaa 180,144,16
K 196,160,161
Cvaa 184,144,32,232
X 163
Mvaa 184,148,36
X 164
Cvaa 188,148,52,236
K 197,162,163
Mvaa 188,144,48
X 165
Mvaa 176,152,8
Ra 144,242,28672
Ra 148,242,29696
Mvaa 176,156,12
X 166
Mvaa 180,156,28
K 204,164,165
Mvaa 180,152,24
X 167
Mvaa 184,152,40
Mvaa 184,156,44
X 168
Mvaa 188,156,60
Mvaa 188,152,56
W 2
Ra 152,242,30720
K 205,166,167
Mvav 32,128,192
X 169
Mvav 64,128,200
Ra 156,242,31744
v_xor_b32 v242, 0x10000, v242
Mvav 96,128,208
X 170
Mvav 128,128,216
K 212,168,169
Mvav 132,132,216
X 171
Mvav 100,132,208
X 172
Mvav 68,132,200
Mvav 36,132,192
K 213,170,171
Mvav 40,136,192
X 173
Mvav 72,136,200
X 174
Mvav 104,136,208
Mvav 136,136,216
K 220,172,173
Mvav 140,140,216
X 175
Mvav 108,140,208
X 176
Mvav 76,140,200
s_waitcnt vmcnt(1) lgkmcnt(0)
s_barrier
Mvav 44,140,192
Ra 128,242,0
K 221,174,175
Mvav 48,144,192
X 177
Mvav 80,144,200
X 178
Mvav 112,144,208
Ra 132,242,1024
Mvav 144,144,216
K 198,176,177
Mvav 148,148,216
X 179
Mvav 116,148,208
X 180
Mvav 84,148,200
Ra 136,242,2048
Mvav 52,148,192
K 199,178,179
Mvav 56,152,192
X 181
Mvav 88,152,200
Ra 140,242,3072
Mvav 120,152,208
X 182
Mvav 152,152,216
K 206,180,181
Mvav 156,156,216
X 183
Mvav 124,156,208
Mvav 92,156,200
K 207,182,183
Mvav 60,156,192
W 0
Cvaa 160,128,0,224
Ra 144,242,4096
Ra 148,242,5120
Mvaa 160,132,4
s_add_u32 m0, s62, 0
Ra 152,242,6144
Cvaa 164,132,20,228
X 184
Mvaa 164,128,16
X 185
Cvaa 168,128,32,232
X 186
Mvaa 168,132,36
Ra 156,242,7168
K 214,184,185
Cvaa 172,132,52,236
Dc 1,8,61,0
Mvaa 172,128,48
X 187
Mvaa 160,136,8
X 188
Mvaa 160,140,12
Ra 128,242,16384
K 215,186,187
Mvaa 164,140,28
Dc 1,8,61,1024
Mvaa 164,136,24
X 189
Mvaa 168,136,40
X 190
Mvaa 168,140,44
Ra 132,242,17408
K 222,188,189
Mvaa 172,140,60
Dc 1,8,61,2048
Mvaa 172,136,56
X 191
v_smfmac_f32_16x16x64_bf16 a[160:163], a[176:179], v[192:199], v240
Ra 136,242,18432
K 223,190,191
v_smfmac_f32_16x16x64_bf16 a[164:167], a[176:179], v[200:207], v240
X 160
v_smfmac_f32_16x16x64_bf16 a[168:171], a[176:179], v[208:215], v240
Dc 1,8,61,3072
v_smfmac_f32_16x16x64_bf16 a[172:175], a[176:179], v[216:223], v240
W 3
X 161
Cvaa 176,144,0,224
s_add_u32 m0, s62, 4096
X 162
Mvaa 176,148,4
Ra 140,242,19456
K 192,160,161
Cvaa 180,148,20,228
X 163
Mvaa 180,144,16
Dc 243,8,61,0
Cvaa 184,144,32,232
X 164
Mvaa 184,148,36
K 193,162,163
Cvaa 188,148,52,236
X 165
Mvaa 188,144,48
Dc 243,8,61,1024
Ra 144,242,20480
Mvaa 176,152,8
X 166
Mvaa 176,156,12
Ra 148,242,21504
K 200,164,165
Mvaa 180,156,28
X 167
Mvaa 180,152,24
Dc 243,8,61,2048
Mvaa 184,152,40
X 168
Mvaa 184,156,44
K 201,166,167
Mvaa 188,156,60
X 169
Mvaa 188,152,56
W 2
Dc 243,8,61,3072
Ra 152,242,22528
Mvav 32,128,196
s_add_u32 m0, s62, 8192
X 170
Mvav 64,128,204
Ra 156,242,23552
K 208,168,169
Mvav 96,128,212
X 171
Mvav 128,128,220
Dc 244,8,61,0
Mvav 132,132,220
X 172
Mvav 100,132,212
Ra 128,242,8192
K 209,170,171
Mvav 68,132,204
X 173
Mvav 36,132,196
Dc 244,8,61,1024
Mvav 40,136,196
X 174
Mvav 72,136,204
Ra 132,242,9216
K 216,172,173
Mvav 104,136,212
X 175
Mvav 136,136,220
Dc 244,8,61,2048
Mvav 140,140,220
X 176
Mvav 108,140,212
Ra 136,242,10240
K 217,174,175
Mvav 76,140,204
X 177
Mvav 44,140,196
W 3
Dc 244,8,61,3072
Mvav 48,144,196
X 178
Mvav 80,144,204
s_add_u32 m0, s62, 12288
Ra 140,242,11264
Mvav 112,144,212
K 194,176,177
Mvav 144,144,220
X 179
Mvav 148,148,220
X 180
Mvav 116,148,212
Dc 245,8,61,0
Ra 144,242,12288
Mvav 84,148,204
K 195,178,179
Mvav 52,148,196
X 181
Mvav 56,152,196
Ra 148,242,13312
v_add_u32 v246, 1161216, v246
Mvav 88,152,204
Dc 245,8,61,1024
v_min_u32 v246, s23, v246
Mvav 120,152,212
X 182
Mvav 152,152,220
K 202,180,181
Mvav 156,156,220
X 183
Mvav 124,156,212
Dc 245,8,61,2048
Ra 152,242,14336
Mvav 92,156,204
X 184
Mvav 60,156,196
W 3
K 203,182,183
v_bfe_u32 v28, v1, 4, 4
Cvaa 160,128,0,224
X 185
Mvaa 160,132,4
Dc 245,8,61,3072
v_lshlrev_b32 v28, 3, v28
Cvaa 164,132,20,228
s_lshl_b32 s80, s56, 9
X 186
Mvaa 164,128,16
s_add_u32 s80, s80, 4224
Ra 156,242,15360
K 210,184,185
Cvaa 168,128,32,232
s_add_u32 s61, s61, s63
X 187
Mvaa 168,132,36
buffer_load_dwordx2 v[28:29], v28, s[8:11], s80 offen sc1
Cvaa 172,132,52,236
s_xor_b32 s62, s62, 0x10000
X 188
Mvaa 172,128,48
Ra 128,242,24576
K 211,186,187
Mvaa 160,136,8
X 189
Mvaa 160,140,12
s_add_u32 s64, s64, 1
global_load_dword v3, v246, s[18:19]
Ra 132,242,25600
Mvaa 164,140,28
s_mov_b64 s[18:19], s[34:35]
s_mul_i32 s81, s58, 6
s_add_u32 s81, s81, s57
Mvaa 164,136,24
s_sub_u32 s81, s81, 2
s_cmp_ge_u32 s57, 2
s_cselect_b32 s23, 2211836, 0
s_lshl_b32 s81, s81, 1
Mvaa 168,136,40
s_add_u32 s81, s81, s14
s_sub_u32 s81, s81, 2
s_mul_i32 s81, s81, 6016
Mvaa 168,140,44
s_mov_b32 s82, s59
s_mul_i32 s82, s82, 286720
s_add_u32 s81, s81, s82
Mvaa 172,140,60
v_min_u32 v246, 736, v1
v_lshl_add_u32 v246, v246, 3, s81
Mvaa 172,136,56
W 2
X 190
Cvaa 176,144,0,224
v_min_u32 v246, s23, v246
K 218,188,189
Mvaa 176,148,4
global_load_dword v3, v246, s[18:19]
Ra 136,242,26624
Cvaa 180,148,20,228
X 191
Mvaa 180,144,16
X 160
Cvaa 184,144,32,232
Ra 140,242,27648
K 219,190,191
Mvaa 184,148,36
X 161
Cvaa 188,148,52,236
X 162
Mvaa 188,144,48
Ra 144,242,28672
K 196,160,161
Mvaa 176,152,8
X 163
Mvaa 176,156,12
X 164
Mvaa 180,156,28
Ra 148,242,29696
K 197,162,163
Mvaa 180,152,24
X 165
)"
R"(Mvaa 184,152,40
X 166
Mvaa 184,156,44
K 204,164,165
Mvaa 188,156,60
X 167
Mvaa 188,152,56
W 2
X 168
Mvav 32,128,192
Ra 152,242,30720
K 205,166,167
Mvav 64,128,200
X 169
Mvav 96,128,208
Ra 156,242,31744
v_xor_b32 v242, 0x10000, v242
Mvav 128,128,216
X 170
Mvav 132,132,216
K 212,168,169
Mvav 100,132,208
X 171
Mvav 68,132,200
X 172
Mvav 36,132,192
K 213,170,171
Mvav 40,136,192
X 173
Mvav 72,136,200
X 174
Mvav 104,136,208
Mvav 136,136,216
s_waitcnt vmcnt(2)
v_cmp_eq_u64 vcc, s[12:13], v[28:29]
s_cmp_eq_u64 vcc, -1
s_cbranch_scc0 .Lspin_63%=
.Lok_64%=:
K 220,172,173
Mvav 140,140,216
X 175
Mvav 108,140,208
X 176
Mvav 76,140,200
W 0
s_barrier
Mvav 44,140,192
Ra 128,242,0
K 221,174,175
Mvav 48,144,192
X 177
Mvav 80,144,200
X 178
Mvav 112,144,208
Ra 132,242,1024
Mvav 144,144,216
K 198,176,177
Mvav 148,148,216
X 179
Mvav 116,148,208
X 180
Mvav 84,148,200
Ra 136,242,2048
Mvav 52,148,192
K 199,178,179
Mvav 56,152,192
X 181
Mvav 88,152,200
Ra 140,242,3072
Mvav 120,152,208
X 182
Mvav 152,152,216
K 206,180,181
Mvav 156,156,216
X 183
Mvav 124,156,208
Mvav 92,156,200
K 207,182,183
Mvav 60,156,192
W 0
Cvaa 160,128,0,224
Ra 144,242,4096
Ra 148,242,5120
Mvaa 160,132,4
s_add_u32 m0, s62, 0
Ra 152,242,6144
Cvaa 164,132,20,228
X 184
Mvaa 164,128,16
X 185
Cvaa 168,128,32,232
X 186
Mvaa 168,132,36
Ra 156,242,7168
K 214,184,185
Cvaa 172,132,52,236
Dc 1,8,61,0
Mvaa 172,128,48
X 187
Mvaa 160,136,8
X 188
Mvaa 160,140,12
Ra 128,242,16384
K 215,186,187
Mvaa 164,140,28
Dc 1,8,61,1024
Mvaa 164,136,24
X 189
Mvaa 168,136,40
X 190
Mvaa 168,140,44
Ra 132,242,17408
K 222,188,189
Mvaa 172,140,60
Dc 1,8,61,2048
Mvaa 172,136,56
X 191
v_smfmac_f32_16x16x64_bf16 a[160:163], a[176:179], v[192:199], v240
Ra 136,242,18432
K 223,190,191
v_smfmac_f32_16x16x64_bf16 a[164:167], a[176:179], v[200:207], v240
X 160
v_smfmac_f32_16x16x64_bf16 a[168:171], a[176:179], v[208:215], v240
Dc 1,8,61,3072
v_smfmac_f32_16x16x64_bf16 a[172:175], a[176:179], v[216:223], v240
W 3
X 161
Cvaa 176,144,0,224
s_add_u32 m0, s62, 4096
X 162
Mvaa 176,148,4
Ra 140,242,19456
K 192,160,161
Cvaa 180,148,20,228
X 163
Mvaa 180,144,16
Dc 243,8,61,0
Cvaa 184,144,32,232
X 164
Mvaa 184,148,36
K 193,162,163
Cvaa 188,148,52,236
X 165
Mvaa 188,144,48
Dc 243,8,61,1024
Ra 144,242,20480
Mvaa 176,152,8
X 166
Mvaa 176,156,12
Ra 148,242,21504
K 200,164,165
Mvaa 180,156,28
X 167
Mvaa 180,152,24
Dc 243,8,61,2048
Mvaa 184,152,40
X 168
Mvaa 184,156,44
K 201,166,167
Mvaa 188,156,60
X 169
Mvaa 188,152,56
W 2
Dc 243,8,61,3072
Ra 152,242,22528
Mvav 32,128,196
s_add_u32 m0, s62, 8192
X 170
Mvav 64,128,204
Ra 156,242,23552
K 208,168,169
Mvav 96,128,212
X 171
Mvav 128,128,220
Dc 244,8,61,0
Mvav 132,132,220
X 172
Mvav 100,132,212
Ra 128,242,8192
K 209,170,171
Mvav 68,132,204
X 173
Mvav 36,132,196
Dc 244,8,61,1024
Mvav 40,136,196
X 174
Mvav 72,136,204
Ra 132,242,9216
K 216,172,173
Mvav 104,136,212
X 175
Mvav 136,136,220
Dc 244,8,61,2048
Mvav 140,140,220
X 176
Mvav 108,140,212
Ra 136,242,10240
K 217,174,175
Mvav 76,140,204
X 177
Mvav 44,140,196
W 3
Dc 244,8,61,3072
Mvav 48,144,196
X 178
Mvav 80,144,204
s_add_u32 m0, s62, 12288
Ra 140,242,11264
Mvav 112,144,212
K 194,176,177
Mvav 144,144,220
X 179
Mvav 148,148,220
X 180
Mvav 116,148,212
Dc 245,8,61,0
Ra 144,242,12288
Mvav 84,148,204
K 195,178,179
Mvav 52,148,196
X 181
Mvav 56,152,196
X 182
Mvav 88,152,204
Dc 245,8,61,1024
Ra 148,242,13312
Mvav 120,152,212
K 202,180,181
Mvav 152,152,220
X 183
Mvav 156,156,220
X 184
Mvav 124,156,212
Dc 245,8,61,2048
Ra 152,242,14336
Mvav 92,156,204
K 203,182,183
Mvav 60,156,196
W 3
X 185
Cvaa 160,128,0,224
X 186
Mvaa 160,132,4
Dc 245,8,61,3072
Ra 156,242,15360
Cvaa 164,132,20,228
s_add_u32 s61, s61, s63
K 210,184,185
Mvaa 164,128,16
X 187
Cvaa 168,128,32,232
X 188
Mvaa 168,132,36
Cvaa 172,132,52,236
s_xor_b32 s62, s62, 0x10000
K 211,186,187
Mvaa 172,128,48
X 189
Mvaa 160,136,8
Ra 128,242,24576
Ra 132,242,25600
Mvaa 160,140,12
s_add_u32 s64, s64, 1
X 190
Mvaa 164,140,28
K 218,188,189
Mvaa 164,136,24
X 191
Mvaa 168,136,40
Mvaa 168,140,44
X 160
Mvaa 172,140,60
s_add_u32 s36, s29, 131072
K 219,190,191
Mvaa 172,136,56
W 2
X 161
Cvaa 176,144,0,224
Ra 136,242,26624
Ra 140,242,27648
Mvaa 176,148,4
X 162
Cvaa 180,148,20,228
K 196,160,161
Mvaa 180,144,16
buffer_store_dwordx4 a[124:127], v1, s[8:11], s36 offen offset:0 sc1
Cvaa 184,144,32,232
X 163
Mvaa 184,148,36
X 164
Cvaa 188,148,52,236
K 197,162,163
Mvaa 188,144,48
X 165
Mvaa 176,152,8
Ra 144,242,28672
buffer_store_dwordx4 a[116:119], v1, s[8:11], s36 offen offset:1024 sc1
Mvaa 176,156,12
Ra 148,242,29696
Mvaa 180,156,28
X 166
Mvaa 180,152,24
K 204,164,165
Mvaa 184,152,40
X 167
Mvaa 184,156,44
buffer_store_dwordx4 a[108:111], v1, s[8:11], s36 offen offset:2048 sc1
Mvaa 188,156,60
X 168
Mvaa 188,152,56
W 2
Ra 152,242,30720
K 205,166,167
Mvav 32,128,192
X 169
Mvav 64,128,200
Ra 156,242,31744
buffer_store_dwordx4 a[204:207], v1, s[8:11], s36 offen offset:3072 sc1
Mvav 96,128,208
X 170
Mvav 128,128,216
v_xor_b32 v242, 0x10000, v242
K 212,168,169
Mvav 132,132,216
X 171
Mvav 100,132,208
s_add_u32 s36, s29, 135168
X 172
Mvav 68,132,200
Mvav 36,132,192
K 213,170,171
Mvav 40,136,192
X 173
Mvav 72,136,200
X 174
Mvav 104,136,208
buffer_store_dwordx4 a[212:215], v1, s[8:11], s36 offen offset:0 sc1
Mvav 136,136,216
K 220,172,173
Mvav 140,140,216
X 175
Mvav 108,140,208
X 176
Mvav 76,140,200
s_waitcnt vmcnt(5) lgkmcnt(0)
s_barrier
Mvav 44,140,192
buffer_store_dwordx4 a[220:223], v1, s[8:11], s36 offen offset:1024 sc1
K 221,174,175
Mvav 48,144,192
Ra 128,242,0
Mvav 80,144,200
X 177
Mvav 112,144,208
X 178
Mvav 144,144,216
Ra 132,242,1024
K 198,176,177
Mvav 148,148,216
buffer_store_dwordx4 a[228:231], v1, s[8:11], s36 offen offset:2048 sc1
Mvav 116,148,208
X 179
Mvav 84,148,200
X 180
Mvav 52,148,192
Ra 136,242,2048
K 199,178,179
Mvav 56,152,192
buffer_store_dwordx4 a[76:79], v1, s[8:11], s36 offen offset:3072 sc1
Ra 140,242,3072
Mvav 88,152,200
X 181
Mvav 120,152,208
X 182
Mvav 152,152,216
K 206,180,181
Mvav 156,156,216
X 183
Mvav 124,156,208
Mvav 92,156,200
K 207,182,183
Mvav 60,156,192
W 0
Cvaa 160,128,0,224
Ra 144,242,4096
Ra 148,242,5120
Mvaa 160,132,4
s_add_u32 m0, s62, 0
Ra 152,242,6144
Cvaa 164,132,20,228
X 184
Mvaa 164,128,16
X 185
Cvaa 168,128,32,232
X 186
Mvaa 168,132,36
Ra 156,242,7168
K 214,184,185
Cvaa 172,132,52,236
Dc 1,8,61,0
)"
R"(Mvaa 172,128,48
X 187
Mvaa 160,136,8
X 188
Mvaa 160,140,12
Ra 128,242,16384
K 215,186,187
Mvaa 164,140,28
Dc 1,8,61,1024
Mvaa 164,136,24
X 189
Mvaa 168,136,40
X 190
Mvaa 168,140,44
Ra 132,242,17408
K 222,188,189
Mvaa 172,140,60
Dc 1,8,61,2048
Mvaa 172,136,56
X 191
v_smfmac_f32_16x16x64_bf16 a[160:163], a[176:179], v[192:199], v240
Ra 136,242,18432
K 223,190,191
v_smfmac_f32_16x16x64_bf16 a[164:167], a[176:179], v[200:207], v240
X 160
v_smfmac_f32_16x16x64_bf16 a[168:171], a[176:179], v[208:215], v240
Dc 1,8,61,3072
v_smfmac_f32_16x16x64_bf16 a[172:175], a[176:179], v[216:223], v240
W 3
X 161
Cvaa 176,144,0,224
s_add_u32 m0, s62, 4096
X 162
Mvaa 176,148,4
Ra 140,242,19456
K 192,160,161
Cvaa 180,148,20,228
X 163
Mvaa 180,144,16
Dc 243,8,61,0
Cvaa 184,144,32,232
X 164
Mvaa 184,148,36
K 193,162,163
Cvaa 188,148,52,236
X 165
Mvaa 188,144,48
Dc 243,8,61,1024
Ra 144,242,20480
Mvaa 176,152,8
X 166
Mvaa 176,156,12
Ra 148,242,21504
K 200,164,165
Mvaa 180,156,28
X 167
Mvaa 180,152,24
Dc 243,8,61,2048
Mvaa 184,152,40
X 168
Mvaa 184,156,44
K 201,166,167
Mvaa 188,156,60
X 169
Mvaa 188,152,56
W 2
Dc 243,8,61,3072
Ra 152,242,22528
Mvav 32,128,196
s_add_u32 m0, s62, 8192
X 170
Mvav 64,128,204
Ra 156,242,23552
K 208,168,169
Mvav 96,128,212
X 171
Mvav 128,128,220
Dc 244,8,61,0
Mvav 132,132,220
X 172
Mvav 100,132,212
Ra 128,242,8192
K 209,170,171
Mvav 68,132,204
X 173
Mvav 36,132,196
Dc 244,8,61,1024
Mvav 40,136,196
X 174
Mvav 72,136,204
Ra 132,242,9216
K 216,172,173
Mvav 104,136,212
X 175
Mvav 136,136,220
Dc 244,8,61,2048
Mvav 140,140,220
X 176
Mvav 108,140,212
Ra 136,242,10240
K 217,174,175
Mvav 76,140,204
X 177
Mvav 44,140,196
W 3
Dc 244,8,61,3072
Mvav 48,144,196
X 178
Mvav 80,144,204
s_add_u32 m0, s62, 12288
Ra 140,242,11264
Mvav 112,144,212
K 194,176,177
Mvav 144,144,220
X 179
Mvav 148,148,220
X 180
Mvav 116,148,212
Dc 245,8,61,0
Ra 144,242,12288
Mvav 84,148,204
K 195,178,179
Mvav 52,148,196
X 181
Mvav 56,152,196
X 182
Mvav 88,152,204
Dc 245,8,61,1024
Ra 148,242,13312
Mvav 120,152,212
K 202,180,181
Mvav 152,152,220
X 183
Mvav 156,156,220
X 184
Mvav 124,156,212
Dc 245,8,61,2048
Ra 152,242,14336
Mvav 92,156,204
K 203,182,183
Mvav 60,156,196
W 3
X 185
Cvaa 160,128,0,224
X 186
Mvaa 160,132,4
Dc 245,8,61,3072
Ra 156,242,15360
Cvaa 164,132,20,228
s_add_u32 s61, s61, s63
K 210,184,185
Mvaa 164,128,16
X 187
Cvaa 168,128,32,232
X 188
Mvaa 168,132,36
Cvaa 172,132,52,236
s_xor_b32 s62, s62, 0x10000
K 211,186,187
Mvaa 172,128,48
X 189
Mvaa 160,136,8
Ra 128,242,24576
Ra 132,242,25600
Mvaa 160,140,12
s_add_u32 s64, s64, 1
X 190
Mvaa 164,140,28
K 218,188,189
Mvaa 164,136,24
X 191
Mvaa 168,136,40
Mvaa 168,140,44
X 160
Mvaa 172,140,60
s_add_u32 s36, s29, 327680
K 219,190,191
Mvaa 172,136,56
W 2
X 161
Cvaa 176,144,0,224
Ra 136,242,26624
Ra 140,242,27648
Mvaa 176,148,4
X 162
Cvaa 180,148,20,228
K 196,160,161
Mvaa 180,144,16
buffer_store_dwordx4 a[84:87], v1, s[8:11], s36 offen offset:0 sc1
Cvaa 184,144,32,232
X 163
Mvaa 184,148,36
X 164
Cvaa 188,148,52,236
K 197,162,163
Mvaa 188,144,48
X 165
Mvaa 176,152,8
Ra 144,242,28672
buffer_store_dwordx4 a[92:95], v1, s[8:11], s36 offen offset:1024 sc1
Mvaa 176,156,12
Ra 148,242,29696
Mvaa 180,156,28
X 166
Mvaa 180,152,24
K 204,164,165
Mvaa 184,152,40
X 167
Mvaa 184,156,44
buffer_store_dwordx4 a[188:191], v1, s[8:11], s36 offen offset:2048 sc1
Mvaa 188,156,60
X 168
Mvaa 188,152,56
W 2
Ra 152,242,30720
K 205,166,167
Mvav 32,128,192
X 169
Mvav 64,128,200
Ra 156,242,31744
buffer_store_dwordx4 a[196:199], v1, s[8:11], s36 offen offset:3072 sc1
Mvav 96,128,208
X 170
Mvav 128,128,216
v_xor_b32 v242, 0x10000, v242
K 212,168,169
Mvav 132,132,216
X 171
Mvav 100,132,208
s_add_u32 s36, s29, 331776
X 172
Mvav 68,132,200
Mvav 36,132,192
K 213,170,171
Mvav 40,136,192
X 173
Mvav 72,136,200
X 174
Mvav 104,136,208
buffer_store_dwordx4 a[236:239], v1, s[8:11], s36 offen offset:0 sc1
Mvav 136,136,216
K 220,172,173
Mvav 140,140,216
X 175
Mvav 108,140,208
X 176
Mvav 76,140,200
s_waitcnt vmcnt(5) lgkmcnt(0)
s_barrier
Mvav 44,140,192
buffer_store_dwordx4 a[244:247], v1, s[8:11], s36 offen offset:1024 sc1
K 221,174,175
Mvav 48,144,192
Ra 128,242,0
Mvav 80,144,200
X 177
Mvav 112,144,208
X 178
Mvav 144,144,216
Ra 132,242,1024
K 198,176,177
Mvav 148,148,216
buffer_store_dwordx4 a[68:71], v1, s[8:11], s36 offen offset:2048 sc1
Mvav 116,148,208
X 179
Mvav 84,148,200
X 180
Mvav 52,148,192
Ra 136,242,2048
K 199,178,179
Mvav 56,152,192
buffer_store_dwordx4 a[184:187], v1, s[8:11], s36 offen offset:3072 sc1
Ra 140,242,3072
Mvav 88,152,200
X 181
Mvav 120,152,208
X 182
Mvav 152,152,216
K 206,180,181
Mvav 156,156,216
X 183
Mvav 124,156,208
Mvav 92,156,200
K 207,182,183
Mvav 60,156,192
s_mov_b64 exec, 0
s_cmp_eq_u32 s14, 2
s_cbranch_scc1 .Lh_3_2%=
s_branch .Lh_3_3%=
.Lhr_66%=:
s_mov_b64 exec, -1
W 0
Cvaa 160,128,0,224
Ra 144,242,4096
Ra 148,242,5120
Mvaa 160,132,4
s_add_u32 m0, s62, 0
Ra 152,242,6144
Cvaa 164,132,20,228
X 184
Mvaa 164,128,16
X 185
Cvaa 168,128,32,232
X 186
Mvaa 168,132,36
Ra 156,242,7168
K 214,184,185
Cvaa 172,132,52,236
Dc 1,8,61,0
Mvaa 172,128,48
X 187
Mvaa 160,136,8
X 188
Mvaa 160,140,12
Ra 128,242,16384
K 215,186,187
Mvaa 164,140,28
Dc 1,8,61,1024
Mvaa 164,136,24
X 189
Mvaa 168,136,40
X 190
Mvaa 168,140,44
Ra 132,242,17408
K 222,188,189
Mvaa 172,140,60
Dc 1,8,61,2048
Mvaa 172,136,56
X 191
v_smfmac_f32_16x16x64_bf16 a[160:163], a[176:179], v[192:199], v240
Ra 136,242,18432
K 223,190,191
v_smfmac_f32_16x16x64_bf16 a[164:167], a[176:179], v[200:207], v240
X 160
v_smfmac_f32_16x16x64_bf16 a[168:171], a[176:179], v[208:215], v240
Dc 1,8,61,3072
v_smfmac_f32_16x16x64_bf16 a[172:175], a[176:179], v[216:223], v240
W 3
X 161
Cvaa 176,144,0,224
s_add_u32 m0, s62, 4096
X 162
Mvaa 176,148,4
Ra 140,242,19456
K 192,160,161
Cvaa 180,148,20,228
X 163
Mvaa 180,144,16
Dc 243,8,61,0
Cvaa 184,144,32,232
X 164
Mvaa 184,148,36
K 193,162,163
Cvaa 188,148,52,236
X 165
Mvaa 188,144,48
Dc 243,8,61,1024
Ra 144,242,20480
Mvaa 176,152,8
X 166
Mvaa 176,156,12
Ra 148,242,21504
K 200,164,165
Mvaa 180,156,28
X 167
Mvaa 180,152,24
Dc 243,8,61,2048
Mvaa 184,152,40
X 168
Mvaa 184,156,44
K 201,166,167
Mvaa 188,156,60
X 169
Mvaa 188,152,56
W 2
Dc 243,8,61,3072
Ra 152,242,22528
Mvav 32,128,196
s_add_u32 m0, s62, 8192
X 170
Mvav 64,128,204
Ra 156,242,23552
K 208,168,169
Mvav 96,128,212
X 171
Mvav 128,128,220
Dc 244,8,61,0
Mvav 132,132,220
X 172
Mvav 100,132,212
Ra 128,242,8192
K 209,170,171
Mvav 68,132,204
X 173
Mvav 36,132,196
Dc 244,8,61,1024
Mvav 40,136,196
X 174
Mvav 72,136,204
Ra 132,242,9216
)"
R"(K 216,172,173
Mvav 104,136,212
X 175
Mvav 136,136,220
Dc 244,8,61,2048
Mvav 140,140,220
X 176
Mvav 108,140,212
Ra 136,242,10240
K 217,174,175
Mvav 76,140,204
X 177
Mvav 44,140,196
W 3
Dc 244,8,61,3072
Mvav 48,144,196
X 178
Mvav 80,144,204
s_add_u32 m0, s62, 12288
Ra 140,242,11264
Mvav 112,144,212
K 194,176,177
Mvav 144,144,220
X 179
Mvav 148,148,220
X 180
Mvav 116,148,212
Dc 245,8,61,0
Ra 144,242,12288
Mvav 84,148,204
K 195,178,179
Mvav 52,148,196
X 181
Mvav 56,152,196
X 182
Mvav 88,152,204
Dc 245,8,61,1024
Ra 148,242,13312
Mvav 120,152,212
K 202,180,181
Mvav 152,152,220
X 183
Mvav 156,156,220
s_waitcnt vmcnt(14)
s_add_u32 s80, s71, 256
v_mov_b64 v[28:29], s[12:13]
s_mov_b64 exec, 1
buffer_store_dwordx2 v[28:29], off, s[8:11], s80 sc1
s_mov_b64 exec, -1
X 184
Mvav 124,156,212
Ra 152,242,14336
Mvav 92,156,204
K 203,182,183
Mvav 60,156,196
W 3
X 185
Cvaa 160,128,0,224
Dc 245,8,61,2048
Ra 156,242,15360
Mvaa 160,132,4
X 186
Cvaa 164,132,20,228
K 210,184,185
Mvaa 164,128,16
X 187
Cvaa 168,128,32,232
Dc 245,8,61,3072
Mvaa 168,132,36
s_add_u32 s61, s61, s63
X 188
Cvaa 172,132,52,236
s_xor_b32 s62, s62, 0x10000
K 211,186,187
Mvaa 172,128,48
X 189
Mvaa 160,136,8
Ra 128,242,24576
Ra 132,242,25600
Mvaa 160,140,12
s_add_u32 s64, s64, 1
X 190
Mvaa 164,140,28
K 218,188,189
Mvaa 164,136,24
X 191
Mvaa 168,136,40
Mvaa 168,140,44
X 160
Mvaa 172,140,60
s_add_u32 s36, s29, 196608
K 219,190,191
Mvaa 172,136,56
W 2
X 161
Cvaa 176,144,0,224
Ra 136,242,26624
Ra 140,242,27648
Mvaa 176,148,4
X 162
Cvaa 180,148,20,228
K 196,160,161
Mvaa 180,144,16
buffer_store_dwordx4 a[192:195], v1, s[8:11], s36 offen offset:0 sc1
Cvaa 184,144,32,232
X 163
Mvaa 184,148,36
X 164
Cvaa 188,148,52,236
K 197,162,163
Mvaa 188,144,48
X 165
Mvaa 176,152,8
Ra 144,242,28672
buffer_store_dwordx4 a[200:203], v1, s[8:11], s36 offen offset:1024 sc1
Mvaa 176,156,12
Ra 148,242,29696
Mvaa 180,156,28
X 166
Mvaa 180,152,24
K 204,164,165
Mvaa 184,152,40
X 167
Mvaa 184,156,44
buffer_store_dwordx4 a[208:211], v1, s[8:11], s36 offen offset:2048 sc1
Mvaa 188,156,60
X 168
Mvaa 188,152,56
W 2
Ra 152,242,30720
K 205,166,167
Mvav 32,128,192
X 169
Mvav 64,128,200
Ra 156,242,31744
buffer_store_dwordx4 a[216:219], v1, s[8:11], s36 offen offset:3072 sc1
Mvav 96,128,208
X 170
Mvav 128,128,216
v_xor_b32 v242, 0x10000, v242
K 212,168,169
Mvav 132,132,216
X 171
Mvav 100,132,208
s_add_u32 s36, s29, 200704
X 172
Mvav 68,132,200
Mvav 36,132,192
K 213,170,171
Mvav 40,136,192
X 173
Mvav 72,136,200
X 174
Mvav 104,136,208
buffer_store_dwordx4 a[224:227], v1, s[8:11], s36 offen offset:0 sc1
Mvav 136,136,216
K 220,172,173
Mvav 140,140,216
X 175
Mvav 108,140,208
X 176
Mvav 76,140,200
s_waitcnt vmcnt(5) lgkmcnt(0)
s_barrier
Mvav 44,140,192
buffer_store_dwordx4 a[232:235], v1, s[8:11], s36 offen offset:1024 sc1
K 221,174,175
Mvav 48,144,192
Ra 128,242,0
Mvav 80,144,200
X 177
Mvav 112,144,208
X 178
Mvav 144,144,216
Ra 132,242,1024
K 198,176,177
Mvav 148,148,216
buffer_store_dwordx4 a[240:243], v1, s[8:11], s36 offen offset:2048 sc1
Mvav 116,148,208
X 179
Mvav 84,148,200
X 180
Mvav 52,148,192
Ra 136,242,2048
K 199,178,179
Mvav 56,152,192
buffer_store_dwordx4 a[100:103], v1, s[8:11], s36 offen offset:3072 sc1
Ra 140,242,3072
Mvav 88,152,200
X 181
Mvav 120,152,208
X 182
Mvav 152,152,216
K 206,180,181
Mvav 156,156,216
X 183
Mvav 124,156,208
Mvav 92,156,200
K 207,182,183
Mvav 60,156,192
W 0
Cvaa 160,128,0,224
Ra 144,242,4096
Ra 148,242,5120
Mvaa 160,132,4
s_add_u32 m0, s62, 0
Ra 152,242,6144
Cvaa 164,132,20,228
X 184
Mvaa 164,128,16
X 185
Cvaa 168,128,32,232
X 186
Mvaa 168,132,36
Ra 156,242,7168
K 214,184,185
Cvaa 172,132,52,236
Dc 1,8,61,0
Mvaa 172,128,48
X 187
Mvaa 160,136,8
X 188
Mvaa 160,140,12
Ra 128,242,16384
K 215,186,187
Mvaa 164,140,28
Dc 1,8,61,1024
Mvaa 164,136,24
X 189
Mvaa 168,136,40
X 190
Mvaa 168,140,44
Ra 132,242,17408
K 222,188,189
Mvaa 172,140,60
Dc 1,8,61,2048
Mvaa 172,136,56
X 191
v_smfmac_f32_16x16x64_bf16 a[160:163], a[176:179], v[192:199], v240
Ra 136,242,18432
K 223,190,191
v_smfmac_f32_16x16x64_bf16 a[164:167], a[176:179], v[200:207], v240
X 160
v_smfmac_f32_16x16x64_bf16 a[168:171], a[176:179], v[208:215], v240
Dc 1,8,61,3072
v_smfmac_f32_16x16x64_bf16 a[172:175], a[176:179], v[216:223], v240
W 3
X 161
Cvaa 176,144,0,224
s_add_u32 m0, s62, 4096
X 162
Mvaa 176,148,4
Ra 140,242,19456
K 192,160,161
Cvaa 180,148,20,228
X 163
Mvaa 180,144,16
Dc 243,8,61,0
Cvaa 184,144,32,232
X 164
Mvaa 184,148,36
K 193,162,163
Cvaa 188,148,52,236
X 165
Mvaa 188,144,48
Dc 243,8,61,1024
Ra 144,242,20480
Mvaa 176,152,8
X 166
Mvaa 176,156,12
Ra 148,242,21504
K 200,164,165
Mvaa 180,156,28
X 167
Mvaa 180,152,24
Dc 243,8,61,2048
Mvaa 184,152,40
X 168
Mvaa 184,156,44
K 201,166,167
Mvaa 188,156,60
X 169
Mvaa 188,152,56
W 2
Dc 243,8,61,3072
Ra 152,242,22528
Mvav 32,128,196
s_add_u32 m0, s62, 8192
X 170
Mvav 64,128,204
Ra 156,242,23552
K 208,168,169
Mvav 96,128,212
X 171
Mvav 128,128,220
Dc 244,8,61,0
Mvav 132,132,220
X 172
Mvav 100,132,212
Ra 128,242,8192
K 209,170,171
Mvav 68,132,204
X 173
Mvav 36,132,196
Dc 244,8,61,1024
Mvav 40,136,196
X 174
Mvav 72,136,204
Ra 132,242,9216
K 216,172,173
Mvav 104,136,212
X 175
Mvav 136,136,220
Dc 244,8,61,2048
Mvav 140,140,220
X 176
Mvav 108,140,212
Ra 136,242,10240
K 217,174,175
Mvav 76,140,204
X 177
Mvav 44,140,196
W 3
Dc 244,8,61,3072
Mvav 48,144,196
X 178
Mvav 80,144,204
s_add_u32 m0, s62, 12288
Ra 140,242,11264
Mvav 112,144,212
K 194,176,177
Mvav 144,144,220
X 179
Mvav 148,148,220
X 180
Mvav 116,148,212
Dc 245,8,61,0
Ra 144,242,12288
Mvav 84,148,204
K 195,178,179
Mvav 52,148,196
X 181
Mvav 56,152,196
X 182
Mvav 88,152,204
Dc 245,8,61,1024
Ra 148,242,13312
Mvav 120,152,212
K 202,180,181
Mvav 152,152,220
X 183
Mvav 156,156,220
X 184
Mvav 124,156,212
Dc 245,8,61,2048
Ra 152,242,14336
Mvav 92,156,204
K 203,182,183
v_bfe_u32 v28, v1, 4, 4
Mvav 60,156,196
W 3
X 185
Cvaa 160,128,0,224
Ra 156,242,15360
v_lshlrev_b32 v28, 3, v28
Mvaa 160,132,4
s_lshl_b32 s80, s56, 9
s_add_u32 s80, s80, 4352
Dc 245,8,61,3072
Cvaa 164,132,20,228
s_add_u32 s61, s61, s63
X 186
Mvaa 164,128,16
K 210,184,185
Cvaa 168,128,32,232
X 187
Mvaa 168,132,36
buffer_load_dwordx2 v[28:29], v28, s[8:11], s80 offen sc1
Cvaa 172,132,52,236
s_xor_b32 s62, s62, 0x10000
X 188
Mvaa 172,128,48
Ra 128,242,24576
K 211,186,187
Mvaa 160,136,8
X 189
Mvaa 160,140,12
s_add_u32 s64, s64, 1
X 190
Mvaa 164,140,28
Ra 132,242,25600
K 218,188,189
Mvaa 164,136,24
X 191
Mvaa 168,136,40
Mvaa 168,140,44
X 160
Mvaa 172,140,60
s_add_u32 s36, s29, 262144
K 219,190,191
)"
R"(Mvaa 172,136,56
W 2
X 161
Cvaa 176,144,0,224
Ra 136,242,26624
Ra 140,242,27648
Mvaa 176,148,4
X 162
Cvaa 180,148,20,228
K 196,160,161
Mvaa 180,144,16
buffer_store_dwordx4 a[64:67], v1, s[8:11], s36 offen offset:0 sc1
Cvaa 184,144,32,232
X 163
Mvaa 184,148,36
X 164
Cvaa 188,148,52,236
K 197,162,163
Mvaa 188,144,48
X 165
Mvaa 176,152,8
Ra 144,242,28672
buffer_store_dwordx4 a[72:75], v1, s[8:11], s36 offen offset:1024 sc1
Mvaa 176,156,12
Ra 148,242,29696
Mvaa 180,156,28
X 166
Mvaa 180,152,24
K 204,164,165
Mvaa 184,152,40
X 167
Mvaa 184,156,44
buffer_store_dwordx4 a[80:83], v1, s[8:11], s36 offen offset:2048 sc1
Mvaa 188,156,60
X 168
Mvaa 188,152,56
W 2
Ra 152,242,30720
K 205,166,167
Mvav 32,128,192
X 169
Mvav 64,128,200
Ra 156,242,31744
buffer_store_dwordx4 a[88:91], v1, s[8:11], s36 offen offset:3072 sc1
Mvav 96,128,208
X 170
Mvav 128,128,216
v_xor_b32 v242, 0x10000, v242
K 212,168,169
Mvav 132,132,216
X 171
Mvav 100,132,208
s_add_u32 s36, s29, 266240
X 172
Mvav 68,132,200
Mvav 36,132,192
K 213,170,171
Mvav 40,136,192
X 173
Mvav 72,136,200
X 174
Mvav 104,136,208
buffer_store_dwordx4 a[96:99], v1, s[8:11], s36 offen offset:0 sc1
Mvav 136,136,216
s_waitcnt vmcnt(5)
v_cmp_eq_u64 vcc, s[12:13], v[28:29]
s_cmp_eq_u64 vcc, -1
s_cbranch_scc0 .Lspin_67%=
.Lok_68%=:
K 220,172,173
Mvav 140,140,216
X 175
Mvav 108,140,208
X 176
Mvav 76,140,200
W 0
s_barrier
Mvav 44,140,192
buffer_store_dwordx4 a[104:107], v1, s[8:11], s36 offen offset:1024 sc1
K 221,174,175
Mvav 48,144,192
Ra 128,242,0
Mvav 80,144,200
X 177
Mvav 112,144,208
X 178
Mvav 144,144,216
Ra 132,242,1024
K 198,176,177
Mvav 148,148,216
buffer_store_dwordx4 a[112:115], v1, s[8:11], s36 offen offset:2048 sc1
Mvav 116,148,208
X 179
Mvav 84,148,200
X 180
Mvav 52,148,192
Ra 136,242,2048
K 199,178,179
Mvav 56,152,192
buffer_store_dwordx4 a[120:123], v1, s[8:11], s36 offen offset:3072 sc1
Ra 140,242,3072
Mvav 88,152,200
X 181
Mvav 120,152,208
X 182
Mvav 152,152,216
K 206,180,181
Mvav 156,156,216
X 183
Mvav 124,156,208
Mvav 92,156,200
K 207,182,183
Mvav 60,156,192
W 0
Cvaa 160,128,0,224
Ra 144,242,4096
Ra 148,242,5120
Mvaa 160,132,4
s_add_u32 m0, s62, 0
Ra 152,242,6144
Cvaa 164,132,20,228
X 184
Mvaa 164,128,16
X 185
Cvaa 168,128,32,232
X 186
Mvaa 168,132,36
Ra 156,242,7168
K 214,184,185
Cvaa 172,132,52,236
Dc 1,8,61,0
Mvaa 172,128,48
X 187
Mvaa 160,136,8
X 188
Mvaa 160,140,12
Ra 128,242,16384
K 215,186,187
Mvaa 164,140,28
Dc 1,8,61,1024
Mvaa 164,136,24
X 189
Mvaa 168,136,40
X 190
Mvaa 168,140,44
Ra 132,242,17408
K 222,188,189
Mvaa 172,140,60
Dc 1,8,61,2048
Mvaa 172,136,56
X 191
v_smfmac_f32_16x16x64_bf16 a[160:163], a[176:179], v[192:199], v240
Ra 136,242,18432
K 223,190,191
v_smfmac_f32_16x16x64_bf16 a[164:167], a[176:179], v[200:207], v240
X 160
v_smfmac_f32_16x16x64_bf16 a[168:171], a[176:179], v[208:215], v240
Dc 1,8,61,3072
v_smfmac_f32_16x16x64_bf16 a[172:175], a[176:179], v[216:223], v240
W 3
X 161
Cvaa 176,144,0,224
s_add_u32 m0, s62, 4096
X 162
Mvaa 176,148,4
Ra 140,242,19456
K 192,160,161
Cvaa 180,148,20,228
X 163
Mvaa 180,144,16
Dc 243,8,61,0
Cvaa 184,144,32,232
X 164
Mvaa 184,148,36
K 193,162,163
Cvaa 188,148,52,236
X 165
Mvaa 188,144,48
Dc 243,8,61,1024
Ra 144,242,20480
Mvaa 176,152,8
X 166
Mvaa 176,156,12
Ra 148,242,21504
K 200,164,165
Mvaa 180,156,28
X 167
Mvaa 180,152,24
Dc 243,8,61,2048
Mvaa 184,152,40
X 168
Mvaa 184,156,44
K 201,166,167
Mvaa 188,156,60
X 169
Mvaa 188,152,56
W 2
Dc 243,8,61,3072
Ra 152,242,22528
Mvav 32,128,196
s_add_u32 m0, s62, 8192
X 170
Mvav 64,128,204
Ra 156,242,23552
K 208,168,169
Mvav 96,128,212
X 171
Mvav 128,128,220
Dc 244,8,61,0
Mvav 132,132,220
X 172
Mvav 100,132,212
Ra 128,242,8192
K 209,170,171
Mvav 68,132,204
X 173
Mvav 36,132,196
Dc 244,8,61,1024
Mvav 40,136,196
X 174
Mvav 72,136,204
Ra 132,242,9216
K 216,172,173
Mvav 104,136,212
X 175
Mvav 136,136,220
Dc 244,8,61,2048
Mvav 140,140,220
X 176
Mvav 108,140,212
Ra 136,242,10240
K 217,174,175
Mvav 76,140,204
X 177
Mvav 44,140,196
W 3
Dc 244,8,61,3072
Mvav 48,144,196
X 178
Mvav 80,144,204
s_add_u32 m0, s62, 12288
Ra 140,242,11264
Mvav 112,144,212
K 194,176,177
Mvav 144,144,220
X 179
Mvav 148,148,220
X 180
Mvav 116,148,212
Dc 245,8,61,0
Ra 144,242,12288
Mvav 84,148,204
K 195,178,179
Mvav 52,148,196
X 181
Mvav 56,152,196
X 182
Mvav 88,152,204
Dc 245,8,61,1024
Ra 148,242,13312
Mvav 120,152,212
s_bitcmp1_b32 195, s57
s_cselect_b32 s22, -1, 0
K 202,180,181
Mvav 152,152,220
s_lshr_b32 s82, s57, 2
X 183
Mvav 156,156,220
s_waitcnt vmcnt(14)
s_add_u32 s80, s71, 384
v_mov_b64 v[28:29], s[12:13]
s_mov_b64 exec, 1
buffer_store_dwordx2 v[28:29], off, s[8:11], s80 sc1
s_mov_b64 exec, -1
X 184
Mvav 124,156,212
s_and_b32 s81, s57, 1
s_lshl1_add_u32 s81, s82, s81
Ra 152,242,14336
Mvav 92,156,204
s_lshl_b32 s82, s56, 2
K 203,182,183
Mvav 60,156,196
W 3
X 185
Cvaa 160,128,0,224
s_add_u32 s81, s81, s82
Dc 245,8,61,2048
Ra 156,242,15360
Mvaa 160,132,4
s_lshl_b32 s81, s81, 1
X 186
Cvaa 164,132,20,228
s_add_u32 s81, s81, s14
s_sub_u32 s81, s81, 2
K 210,184,185
Mvaa 164,128,16
s_mul_i32 s81, s81, 20480
X 187
Cvaa 168,128,32,232
Dc 245,8,61,3072
v_min_u32 v2, 624, v1
Mvaa 168,132,36
s_add_u32 s61, s61, s63
X 188
Cvaa 172,132,52,236
s_xor_b32 s62, s62, 0x10000
v_lshl_add_u32 v2, v2, 3, s81
K 211,186,187
Mvaa 172,128,48
Ra 128,242,24576
v_and_b32 v2, s22, v2
Mvaa 160,136,8
s_and_b32 s22, s22, 5120
X 189
Mvaa 160,140,12
s_add_u32 s64, s64, 1
global_load_dword v3, v2, s[26:27]
Ra 132,242,25600
Mvaa 164,140,28
X 190
Mvaa 164,136,24
v_add_u32 v2, s22, v2
K 218,188,189
Mvaa 168,136,40
X 191
Mvaa 168,140,44
X 160
Mvaa 172,140,60
K 219,190,191
Mvaa 172,136,56
W 2
X 161
Cvaa 176,144,0,224
Ra 136,242,26624
Ra 140,242,27648
Mvaa 176,148,4
X 162
Cvaa 180,148,20,228
K 196,160,161
Mvaa 180,144,16
X 163
Cvaa 184,144,32,232
Mvaa 184,148,36
X 164
Cvaa 188,148,52,236
K 197,162,163
Mvaa 188,144,48
X 165
Mvaa 176,152,8
Ra 144,242,28672
Ra 148,242,29696
Mvaa 176,156,12
X 166
Mvaa 180,156,28
K 204,164,165
Mvaa 180,152,24
X 167
Mvaa 184,152,40
Mvaa 184,156,44
X 168
Mvaa 188,156,60
Mvaa 188,152,56
W 2
Ra 152,242,30720
K 205,166,167
Mvav 32,128,192
X 169
Mvav 64,128,200
Ra 156,242,31744
v_xor_b32 v242, 0x10000, v242
Mvav 96,128,208
X 170
Mvav 128,128,216
K 212,168,169
Mvav 132,132,216
X 171
Mvav 100,132,208
X 172
Mvav 68,132,200
Mvav 36,132,192
K 213,170,171
Mvav 40,136,192
X 173
Mvav 72,136,200
X 174
Mvav 104,136,208
Mvav 136,136,216
K 220,172,173
Mvav 140,140,216
X 175
Mvav 108,140,208
X 176
Mvav 76,140,200
s_waitcnt vmcnt(1) lgkmcnt(0)
s_barrier
Mvav 44,140,192
Ra 128,242,0
K 221,174,175
Mvav 48,144,192
)"
R"(.Lh_3_2%=:
s_cbranch_execz .Lh_3_2_1%=
X 177
Mvav 80,144,200
X 178
Mvav 112,144,208
Ra 132,242,1024
Mvav 144,144,216
K 198,176,177
Mvav 148,148,216
X 179
Mvav 116,148,208
X 180
Mvav 84,148,200
Ra 136,242,2048
Mvav 52,148,192
K 199,178,179
Mvav 56,152,192
X 181
Mvav 88,152,200
Ra 140,242,3072
Mvav 120,152,208
X 182
Mvav 152,152,216
K 206,180,181
Mvav 156,156,216
X 183
Mvav 124,156,208
Mvav 92,156,200
K 207,182,183
Mvav 60,156,192
W 0
Cvaa 160,128,0,224
Ra 144,242,4096
Ra 148,242,5120
Mvaa 160,132,4
s_add_u32 m0, s62, 0
Ra 152,242,6144
Cvaa 164,132,20,228
X 184
Mvaa 164,128,16
X 185
Cvaa 168,128,32,232
X 186
Mvaa 168,132,36
Ra 156,242,7168
K 214,184,185
Cvaa 172,132,52,236
Dc 1,8,61,0
Mvaa 172,128,48
X 187
Mvaa 160,136,8
X 188
Mvaa 160,140,12
Ra 128,242,16384
K 215,186,187
Mvaa 164,140,28
Dc 1,8,61,1024
Mvaa 164,136,24
X 189
Mvaa 168,136,40
X 190
Mvaa 168,140,44
Ra 132,242,17408
K 222,188,189
Mvaa 172,140,60
Dc 1,8,61,2048
Mvaa 172,136,56
X 191
v_smfmac_f32_16x16x64_bf16 a[160:163], a[176:179], v[192:199], v240
Ra 136,242,18432
K 223,190,191
v_smfmac_f32_16x16x64_bf16 a[164:167], a[176:179], v[200:207], v240
X 160
v_smfmac_f32_16x16x64_bf16 a[168:171], a[176:179], v[208:215], v240
Dc 1,8,61,3072
v_smfmac_f32_16x16x64_bf16 a[172:175], a[176:179], v[216:223], v240
W 3
X 161
Cvaa 176,144,0,224
s_add_u32 m0, s62, 4096
X 162
Mvaa 176,148,4
Ra 140,242,19456
K 192,160,161
Cvaa 180,148,20,228
X 163
Mvaa 180,144,16
Dc 243,8,61,0
Cvaa 184,144,32,232
X 164
Mvaa 184,148,36
K 193,162,163
Cvaa 188,148,52,236
X 165
Mvaa 188,144,48
Dc 243,8,61,1024
Ra 144,242,20480
Mvaa 176,152,8
X 166
Mvaa 176,156,12
Ra 148,242,21504
K 200,164,165
Mvaa 180,156,28
X 167
Mvaa 180,152,24
Dc 243,8,61,2048
Mvaa 184,152,40
X 168
Mvaa 184,156,44
K 201,166,167
Mvaa 188,156,60
X 169
Mvaa 188,152,56
W 2
Dc 243,8,61,3072
Ra 152,242,22528
Mvav 32,128,196
s_add_u32 m0, s62, 8192
X 170
Mvav 64,128,204
Ra 156,242,23552
K 208,168,169
Mvav 96,128,212
X 171
Mvav 128,128,220
Dc 244,8,61,0
Mvav 132,132,220
X 172
Mvav 100,132,212
Ra 128,242,8192
K 209,170,171
Mvav 68,132,204
X 173
Mvav 36,132,196
Dc 244,8,61,1024
Mvav 40,136,196
X 174
Mvav 72,136,204
Ra 132,242,9216
K 216,172,173
Mvav 104,136,212
X 175
Mvav 136,136,220
Dc 244,8,61,2048
Mvav 140,140,220
X 176
Mvav 108,140,212
Ra 136,242,10240
K 217,174,175
Mvav 76,140,204
X 177
Mvav 44,140,196
W 3
Dc 244,8,61,3072
Mvav 48,144,196
X 178
Mvav 80,144,204
s_add_u32 m0, s62, 12288
Ra 140,242,11264
Mvav 112,144,212
K 194,176,177
Mvav 144,144,220
X 179
Mvav 148,148,220
X 180
Mvav 116,148,212
Dc 245,8,61,0
Ra 144,242,12288
Mvav 84,148,204
K 195,178,179
Mvav 52,148,196
X 181
Mvav 56,152,196
X 182
Mvav 88,152,204
Dc 245,8,61,1024
Ra 148,242,13312
Mvav 120,152,212
K 202,180,181
Mvav 152,152,220
X 183
Mvav 156,156,220
X 184
Mvav 124,156,212
Dc 245,8,61,2048
Ra 152,242,14336
Mvav 92,156,204
K 203,182,183
Mvav 60,156,196
W 3
X 185
Cvaa 160,128,0,224
X 186
Mvaa 160,132,4
Dc 245,8,61,3072
Ra 156,242,15360
Cvaa 164,132,20,228
s_add_u32 s61, s61, s63
K 210,184,185
Mvaa 164,128,16
X 187
Cvaa 168,128,32,232
X 188
Mvaa 168,132,36
Cvaa 172,132,52,236
s_xor_b32 s62, s62, 0x10000
global_load_dword v3, v2, s[26:27]
K 211,186,187
Mvaa 172,128,48
X 189
Mvaa 160,136,8
Ra 128,242,24576
Ra 132,242,25600
Mvaa 160,140,12
s_add_u32 s64, s64, 1
X 190
Mvaa 164,140,28
v_add_u32 v2, s22, v2
K 218,188,189
Mvaa 164,136,24
X 191
Mvaa 168,136,40
Mvaa 168,140,44
X 160
Mvaa 172,140,60
K 219,190,191
Mvaa 172,136,56
W 2
X 161
Cvaa 176,144,0,224
Ra 136,242,26624
Ra 140,242,27648
Mvaa 176,148,4
X 162
Cvaa 180,148,20,228
K 196,160,161
Mvaa 180,144,16
X 163
Cvaa 184,144,32,232
Mvaa 184,148,36
X 164
Cvaa 188,148,52,236
K 197,162,163
Mvaa 188,144,48
X 165
Mvaa 176,152,8
Ra 144,242,28672
Ra 148,242,29696
Mvaa 176,156,12
X 166
Mvaa 180,156,28
K 204,164,165
Mvaa 180,152,24
X 167
Mvaa 184,152,40
Mvaa 184,156,44
X 168
Mvaa 188,156,60
Mvaa 188,152,56
W 2
Ra 152,242,30720
K 205,166,167
Mvav 32,128,192
X 169
Mvav 64,128,200
Ra 156,242,31744
v_xor_b32 v242, 0x10000, v242
Mvav 96,128,208
X 170
Mvav 128,128,216
K 212,168,169
Mvav 132,132,216
X 171
Mvav 100,132,208
X 172
Mvav 68,132,200
Mvav 36,132,192
K 213,170,171
Mvav 40,136,192
X 173
Mvav 72,136,200
X 174
Mvav 104,136,208
Mvav 136,136,216
K 220,172,173
Mvav 140,140,216
X 175
Mvav 108,140,208
X 176
Mvav 76,140,200
s_waitcnt vmcnt(1) lgkmcnt(0)
s_barrier
Mvav 44,140,192
Ra 128,242,0
K 221,174,175
Mvav 48,144,192
X 177
Mvav 80,144,200
X 178
Mvav 112,144,208
Ra 132,242,1024
Mvav 144,144,216
K 198,176,177
Mvav 148,148,216
X 179
Mvav 116,148,208
X 180
Mvav 84,148,200
Ra 136,242,2048
Mvav 52,148,192
K 199,178,179
Mvav 56,152,192
X 181
Mvav 88,152,200
Ra 140,242,3072
Mvav 120,152,208
X 182
Mvav 152,152,216
K 206,180,181
Mvav 156,156,216
X 183
Mvav 124,156,208
Mvav 92,156,200
K 207,182,183
Mvav 60,156,192
W 0
Cvaa 160,128,0,224
Ra 144,242,4096
Ra 148,242,5120
Mvaa 160,132,4
s_add_u32 m0, s62, 0
Ra 152,242,6144
Cvaa 164,132,20,228
X 184
Mvaa 164,128,16
X 185
Cvaa 168,128,32,232
X 186
Mvaa 168,132,36
Ra 156,242,7168
K 214,184,185
Cvaa 172,132,52,236
Dc 1,8,61,0
Mvaa 172,128,48
X 187
Mvaa 160,136,8
X 188
Mvaa 160,140,12
Ra 128,242,16384
K 215,186,187
Mvaa 164,140,28
Dc 1,8,61,1024
Mvaa 164,136,24
X 189
Mvaa 168,136,40
X 190
Mvaa 168,140,44
Ra 132,242,17408
K 222,188,189
Mvaa 172,140,60
Dc 1,8,61,2048
Mvaa 172,136,56
X 191
v_smfmac_f32_16x16x64_bf16 a[160:163], a[176:179], v[192:199], v240
Ra 136,242,18432
K 223,190,191
v_smfmac_f32_16x16x64_bf16 a[164:167], a[176:179], v[200:207], v240
X 160
v_smfmac_f32_16x16x64_bf16 a[168:171], a[176:179], v[208:215], v240
Dc 1,8,61,3072
v_smfmac_f32_16x16x64_bf16 a[172:175], a[176:179], v[216:223], v240
W 3
X 161
Cvaa 176,144,0,224
s_add_u32 m0, s62, 4096
X 162
Mvaa 176,148,4
Ra 140,242,19456
K 192,160,161
Cvaa 180,148,20,228
X 163
Mvaa 180,144,16
Dc 243,8,61,0
Cvaa 184,144,32,232
X 164
Mvaa 184,148,36
K 193,162,163
Cvaa 188,148,52,236
X 165
Mvaa 188,144,48
Dc 243,8,61,1024
Ra 144,242,20480
Mvaa 176,152,8
X 166
Mvaa 176,156,12
Ra 148,242,21504
K 200,164,165
Mvaa 180,156,28
X 167
)"
R"(Mvaa 180,152,24
Dc 243,8,61,2048
Mvaa 184,152,40
X 168
Mvaa 184,156,44
K 201,166,167
Mvaa 188,156,60
X 169
Mvaa 188,152,56
W 2
Dc 243,8,61,3072
Ra 152,242,22528
Mvav 32,128,196
s_add_u32 m0, s62, 8192
X 170
Mvav 64,128,204
Ra 156,242,23552
K 208,168,169
Mvav 96,128,212
X 171
Mvav 128,128,220
Dc 244,8,61,0
Mvav 132,132,220
X 172
Mvav 100,132,212
Ra 128,242,8192
K 209,170,171
Mvav 68,132,204
X 173
Mvav 36,132,196
Dc 244,8,61,1024
Mvav 40,136,196
X 174
Mvav 72,136,204
Ra 132,242,9216
K 216,172,173
Mvav 104,136,212
X 175
Mvav 136,136,220
Dc 244,8,61,2048
Mvav 140,140,220
X 176
Mvav 108,140,212
Ra 136,242,10240
K 217,174,175
Mvav 76,140,204
X 177
Mvav 44,140,196
W 3
Dc 244,8,61,3072
Mvav 48,144,196
X 178
Mvav 80,144,204
s_add_u32 m0, s62, 12288
Ra 140,242,11264
Mvav 112,144,212
K 194,176,177
Mvav 144,144,220
X 179
Mvav 148,148,220
X 180
Mvav 116,148,212
Dc 245,8,61,0
Ra 144,242,12288
Mvav 84,148,204
K 195,178,179
Mvav 52,148,196
X 181
Mvav 56,152,196
X 182
Mvav 88,152,204
Dc 245,8,61,1024
Ra 148,242,13312
Mvav 120,152,212
K 202,180,181
Mvav 152,152,220
X 183
Mvav 156,156,220
X 184
Mvav 124,156,212
Dc 245,8,61,2048
Ra 152,242,14336
Mvav 92,156,204
K 203,182,183
Mvav 60,156,196
W 3
X 185
Cvaa 160,128,0,224
X 186
Mvaa 160,132,4
Dc 245,8,61,3072
Ra 156,242,15360
Cvaa 164,132,20,228
s_add_u32 s61, s61, s63
K 210,184,185
Mvaa 164,128,16
X 187
Cvaa 168,128,32,232
X 188
Mvaa 168,132,36
Cvaa 172,132,52,236
s_xor_b32 s62, s62, 0x10000
global_load_dword v3, v2, s[26:27]
K 211,186,187
Mvaa 172,128,48
Ra 128,242,24576
v_add_u32 v2, s22, v2
Mvaa 160,136,8
s_lshr_b32 s80, s15, 3
s_lshl_b32 s80, s80, 1
s_add_u32 s80, s80, s14
Mvaa 160,140,12
s_sub_u32 s80, s80, 2
s_lshl_b32 s80, s80, 12
v_min_u32 v27, 496, v1
Mvaa 164,140,28
s_add_u32 s64, s64, 1
X 189
Mvaa 164,136,24
Ra 132,242,25600
v_lshl_add_u32 v27, v27, 3, s80
Mvaa 168,136,40
X 190
Mvaa 168,140,44
v_min_u32 v27, .Lend%=-.Lbeg%=-4, v27
K 218,188,189
Mvaa 172,140,60
global_load_dword v3, v27, s[6:7]
Mvaa 172,136,56
W 2
X 191
Cvaa 176,144,0,224
X 160
Mvaa 176,148,4
Ra 136,242,26624
K 219,190,191
Cvaa 180,148,20,228
X 161
Mvaa 180,144,16
Ra 140,242,27648
Cvaa 184,144,32,232
X 162
Mvaa 184,148,36
K 196,160,161
Cvaa 188,148,52,236
X 163
Mvaa 188,144,48
X 164
Mvaa 176,152,8
Ra 144,242,28672
K 197,162,163
Mvaa 176,156,12
X 165
Mvaa 180,156,28
Ra 148,242,29696
Mvaa 180,152,24
X 166
Mvaa 184,152,40
K 204,164,165
Mvaa 184,156,44
X 167
Mvaa 188,156,60
X 168
Mvaa 188,152,56
W 2
Ra 152,242,30720
K 205,166,167
Mvav 32,128,192
X 169
Mvav 64,128,200
Ra 156,242,31744
v_xor_b32 v242, 0x10000, v242
Mvav 96,128,208
X 170
Mvav 128,128,216
K 212,168,169
Mvav 132,132,216
X 171
Mvav 100,132,208
X 172
Mvav 68,132,200
Mvav 36,132,192
K 213,170,171
Mvav 40,136,192
X 173
Mvav 72,136,200
X 174
Mvav 104,136,208
Mvav 136,136,216
K 220,172,173
Mvav 140,140,216
.Lh_3_3%=:
s_cbranch_execz .Lhr_66%=
X 175
Mvav 108,140,208
X 176
Mvav 76,140,200
s_waitcnt vmcnt(2) lgkmcnt(0)
s_barrier
Mvav 44,140,192
Ra 128,242,0
K 221,174,175
Mvav 48,144,192
X 177
Mvav 80,144,200
X 178
Mvav 112,144,208
Ra 132,242,1024
Mvav 144,144,216
K 198,176,177
Mvav 148,148,216
X 179
Mvav 116,148,208
X 180
Mvav 84,148,200
Ra 136,242,2048
Mvav 52,148,192
K 199,178,179
Mvav 56,152,192
X 181
Mvav 88,152,200
Ra 140,242,3072
Mvav 120,152,208
X 182
Mvav 152,152,216
K 206,180,181
Mvav 156,156,216
X 183
Mvav 124,156,208
Mvav 92,156,200
K 207,182,183
Mvav 60,156,192
W 0
Cvaa 160,128,0,224
Ra 144,242,4096
Ra 148,242,5120
Mvaa 160,132,4
s_add_u32 m0, s62, 0
Ra 152,242,6144
Cvaa 164,132,20,228
X 184
Mvaa 164,128,16
X 185
Cvaa 168,128,32,232
X 186
Mvaa 168,132,36
Ra 156,242,7168
K 214,184,185
Cvaa 172,132,52,236
Dc 1,8,61,0
Mvaa 172,128,48
X 187
Mvaa 160,136,8
X 188
Mvaa 160,140,12
Ra 128,242,16384
K 215,186,187
Mvaa 164,140,28
Dc 1,8,61,1024
Mvaa 164,136,24
X 189
Mvaa 168,136,40
X 190
Mvaa 168,140,44
Ra 132,242,17408
K 222,188,189
Mvaa 172,140,60
Dc 1,8,61,2048
Mvaa 172,136,56
X 191
v_smfmac_f32_16x16x64_bf16 a[160:163], a[176:179], v[192:199], v240
Ra 136,242,18432
K 223,190,191
v_smfmac_f32_16x16x64_bf16 a[164:167], a[176:179], v[200:207], v240
X 160
v_smfmac_f32_16x16x64_bf16 a[168:171], a[176:179], v[208:215], v240
Dc 1,8,61,3072
v_smfmac_f32_16x16x64_bf16 a[172:175], a[176:179], v[216:223], v240
W 3
X 161
Cvaa 176,144,0,224
s_add_u32 m0, s62, 4096
X 162
Mvaa 176,148,4
s_sub_u32 s81, s57, 1
Ra 140,242,19456
K 192,160,161
Cvaa 180,148,20,228
X 163
Mvaa 180,144,16
s_lshl_b32 s85, s81, 5
Dc 243,8,61,0
Cvaa 184,144,32,232
X 164
Mvaa 184,148,36
s_add_u32 s85, s85, s56
K 193,162,163
Cvaa 188,148,52,236
X 165
Mvaa 188,144,48
Dc 243,8,61,1024
Ra 144,242,20480
Mvaa 176,152,8
s_lshl_b32 s85, s85, 1
X 166
Mvaa 176,156,12
Ra 148,242,21504
K 200,164,165
Mvaa 180,156,28
s_add_u32 s85, s85, s14
X 167
Mvaa 180,152,24
Dc 243,8,61,2048
Mvaa 184,152,40
s_sub_u32 s85, s85, 2
X 168
Mvaa 184,156,44
K 201,166,167
Mvaa 188,156,60
X 169
Mvaa 188,152,56
W 2
s_cmp_lt_u32 s85, 160
s_cselect_b64 s[20:21], 0, -1
Dc 243,8,61,3072
Mvav 32,128,196
s_add_u32 m0, s62, 8192
Ra 152,242,22528
Ra 156,242,23552
Mvav 64,128,204
s_mul_i32 s18, s85, 205
X 170
Mvav 96,128,212
K 208,168,169
Mvav 128,128,220
s_lshr_b32 s18, s18, 12
X 171
Mvav 132,132,220
Dc 244,8,61,0
Ra 128,242,8192
Mvav 100,132,212
X 172
Mvav 68,132,204
s_and_b32 s18, s18, 7
K 209,170,171
Mvav 36,132,196
X 173
Mvav 40,136,196
Dc 244,8,61,1024
v_lshrrev_b32 v4, 1, v1
Mvav 72,136,204
X 174
Mvav 104,136,212
Ra 132,242,9216
K 216,172,173
Mvav 136,136,220
X 175
Mvav 140,140,220
Dc 244,8,61,2048
v_min_u32 v5, 344, v4
Mvav 108,140,212
s_lshl_b32 s81, s18, 3
X 176
Mvav 76,140,204
Ra 136,242,10240
K 217,174,175
Mvav 44,140,196
W 3
X 177
Mvav 48,144,196
Dc 244,8,61,3072
v_mov_b32 v6, s81
Mvav 80,144,204
s_add_u32 m0, s62, 12288
X 178
Mvav 112,144,212
Ra 140,242,11264
K 194,176,177
Mvav 144,144,220
X 179
Mvav 148,148,220
buffer_load_dwordx2 v[8:9], v4, s[8:11], 0 offen offset:2560 sc1
Ra 144,242,12288
Mvav 116,148,212
X 180
Mvav 84,148,204
K 195,178,179
Mvav 52,148,196
X 181
Mvav 56,152,196
buffer_load_dwordx2 v[10:11], v5, s[8:11], 0 offen offset:3072 sc1
Ra 148,242,13312
Mvav 88,152,204
X 182
Mvav 120,152,212
K 202,180,181
Mvav 152,152,220
X 183
Mvav 156,156,220
Dc 245,8,61,0
)"
R"(Ra 152,242,14336
Mvav 124,156,212
X 184
Mvav 92,156,204
K 203,182,183
v_bfe_u32 v28, v1, 4, 4
Mvav 60,156,196
W 3
X 185
Cvaa 160,128,0,224
buffer_load_dwordx2 v[12:13], v6, s[8:11], 0 offen offset:3584 sc1
v_lshlrev_b32 v28, 3, v28
Mvaa 160,132,4
s_lshl_b32 s80, s56, 9
X 186
Cvaa 164,132,20,228
s_add_u32 s80, s80, 4480
Ra 156,242,15360
K 210,184,185
Mvaa 164,128,16
X 187
Cvaa 168,128,32,232
Dc 245,8,61,1024
Mvaa 168,132,36
X 188
Cvaa 172,132,52,236
K 211,186,187
Mvaa 172,128,48
X 189
Mvaa 160,136,8
Dc 245,8,61,2048
Ra 128,242,24576
Mvaa 160,140,12
X 190
Mvaa 164,140,28
Ra 132,242,25600
K 218,188,189
Mvaa 164,136,24
X 191
Mvaa 168,136,40
buffer_load_dwordx2 v[28:29], v28, s[8:11], s80 offen sc1
Mvaa 168,140,44
Dc 245,8,61,3072
s_add_u32 s61, s61, s63
s_xor_b32 s62, s62, 0x10000
s_add_u32 s64, s64, 1
Mvaa 172,140,60
X 160
Mvaa 172,136,56
W 2
Ra 136,242,26624
K 219,190,191
Cvaa 176,144,0,224
X 161
Mvaa 176,148,4
global_load_dword v3, v2, s[26:27]
v_add_u32 v2, s22, v2
Cvaa 180,148,20,228
Ra 140,242,27648
Mvaa 180,144,16
X 162
Cvaa 184,144,32,232
K 196,160,161
Mvaa 184,148,36
X 163
Cvaa 188,148,52,236
X 164
Mvaa 188,144,48
Ra 144,242,28672
K 197,162,163
Mvaa 176,152,8
X 165
Mvaa 176,156,12
Ra 148,242,29696
Mvaa 180,156,28
X 166
Mvaa 180,152,24
K 204,164,165
Mvaa 184,152,40
X 167
Mvaa 184,156,44
X 168
Mvaa 188,156,60
Mvaa 188,152,56
W 2
Ra 152,242,30720
K 205,166,167
Mvav 32,128,192
X 169
Mvav 64,128,200
Ra 156,242,31744
v_xor_b32 v242, 0x10000, v242
Mvav 96,128,208
X 170
Mvav 128,128,216
K 212,168,169
Mvav 132,132,216
X 171
Mvav 100,132,208
X 172
Mvav 68,132,200
Mvav 36,132,192
K 213,170,171
Mvav 40,136,192
X 173
Mvav 72,136,200
X 174
Mvav 104,136,208
Mvav 136,136,216
s_waitcnt vmcnt(2)
v_cmp_eq_u64 vcc, s[12:13], v[28:29]
s_cmp_eq_u64 vcc, -1
s_cbranch_scc0 .Lspin_70%=
.Lok_71%=:
v_cmp_eq_u64 vcc, s[12:13], v[8:9]
v_cmp_eq_u64_e64 s[86:87], s[12:13], v[10:11]
s_and_b64 vcc, vcc, s[86:87]
s_or_b64 vcc, vcc, s[20:21]
s_cmp_eq_u64 vcc, -1
s_cbranch_scc0 .Lspin_73%=
.Lok_74%=:
K 220,172,173
Mvav 140,140,216
X 175
Mvav 108,140,208
X 176
Mvav 76,140,200
s_waitcnt vmcnt(1) lgkmcnt(0)
s_barrier
Mvav 44,140,192
Ra 128,242,0
K 221,174,175
Mvav 48,144,192
X 177
Mvav 80,144,200
X 178
Mvav 112,144,208
Ra 132,242,1024
Mvav 144,144,216
K 198,176,177
Mvav 148,148,216
X 179
Mvav 116,148,208
X 180
Mvav 84,148,200
Ra 136,242,2048
Mvav 52,148,192
K 199,178,179
Mvav 56,152,192
X 181
Mvav 88,152,200
Ra 140,242,3072
Mvav 120,152,208
X 182
Mvav 152,152,216
K 206,180,181
Mvav 156,156,216
X 183
Mvav 124,156,208
Mvav 92,156,200
K 207,182,183
Mvav 60,156,192
s_mov_b64 exec, 0
s_cmp_eq_u32 s14, 2
s_cbranch_scc1 .Lh_4_2%=
s_branch .Lh_4_3%=
.Lhr_76%=:
s_mov_b64 exec, -1
W 0
Cvaa 160,128,0,224
Ra 144,242,4096
Ra 148,242,5120
Mvaa 160,132,4
s_add_u32 m0, s62, 0
Ra 152,242,6144
Cvaa 164,132,20,228
X 184
Mvaa 164,128,16
X 185
Cvaa 168,128,32,232
X 186
Mvaa 168,132,36
Ra 156,242,7168
K 214,184,185
Cvaa 172,132,52,236
Dc 1,8,61,0
Mvaa 172,128,48
X 187
Mvaa 160,136,8
X 188
Mvaa 160,140,12
Ra 128,242,16384
K 215,186,187
Mvaa 164,140,28
Dc 1,8,61,1024
Mvaa 164,136,24
X 189
Mvaa 168,136,40
X 190
Mvaa 168,140,44
Ra 132,242,17408
K 222,188,189
Mvaa 172,140,60
Dc 1,8,61,2048
Mvaa 172,136,56
X 191
v_smfmac_f32_16x16x64_bf16 a[160:163], a[176:179], v[192:199], v240
Ra 136,242,18432
K 223,190,191
v_smfmac_f32_16x16x64_bf16 a[164:167], a[176:179], v[200:207], v240
X 160
v_smfmac_f32_16x16x64_bf16 a[168:171], a[176:179], v[208:215], v240
Dc 1,8,61,3072
v_smfmac_f32_16x16x64_bf16 a[172:175], a[176:179], v[216:223], v240
W 3
X 161
Cvaa 176,144,0,224
s_add_u32 m0, s62, 4096
X 162
Mvaa 176,148,4
Ra 140,242,19456
K 192,160,161
Cvaa 180,148,20,228
X 163
Mvaa 180,144,16
Dc 243,8,61,0
Cvaa 184,144,32,232
X 164
Mvaa 184,148,36
K 193,162,163
Cvaa 188,148,52,236
X 165
Mvaa 188,144,48
Dc 243,8,61,1024
Ra 144,242,20480
Mvaa 176,152,8
X 166
Mvaa 176,156,12
Ra 148,242,21504
K 200,164,165
Mvaa 180,156,28
X 167
Mvaa 180,152,24
Dc 243,8,61,2048
Mvaa 184,152,40
X 168
Mvaa 184,156,44
K 201,166,167
Mvaa 188,156,60
X 169
Mvaa 188,152,56
W 2
Dc 243,8,61,3072
Ra 152,242,22528
Mvav 32,128,196
s_add_u32 m0, s62, 8192
X 170
Mvav 64,128,204
Ra 156,242,23552
K 208,168,169
Mvav 96,128,212
X 171
Mvav 128,128,220
Dc 244,8,61,0
Mvav 132,132,220
X 172
Mvav 100,132,212
Ra 128,242,8192
K 209,170,171
Mvav 68,132,204
X 173
Mvav 36,132,196
Dc 244,8,61,1024
Mvav 40,136,196
X 174
Mvav 72,136,204
Ra 132,242,9216
K 216,172,173
Mvav 104,136,212
X 175
Mvav 136,136,220
Dc 244,8,61,2048
Mvav 140,140,220
X 176
Mvav 108,140,212
Ra 136,242,10240
K 217,174,175
Mvav 76,140,204
X 177
Mvav 44,140,196
W 3
Dc 244,8,61,3072
Mvav 48,144,196
X 178
Mvav 80,144,204
s_add_u32 m0, s62, 12288
Ra 140,242,11264
Mvav 112,144,212
K 194,176,177
Mvav 144,144,220
X 179
Mvav 148,148,220
X 180
Mvav 116,148,212
Dc 245,8,61,0
Ra 144,242,12288
Mvav 84,148,204
K 195,178,179
Mvav 52,148,196
X 181
Mvav 56,152,196
X 182
Mvav 88,152,204
Dc 245,8,61,1024
Ra 148,242,13312
Mvav 120,152,212
v_xor_b32 v6, s12, v12
K 202,180,181
Mvav 152,152,220
X 183
Mvav 156,156,220
Ra 152,242,14336
v_and_b32 v6, 2047, v6
Mvav 124,156,212
Dc 245,8,61,2048
v_lshlrev_b32 v6, 3, v6
Mvav 92,156,204
X 184
Mvav 60,156,196
W 3
K 203,182,183
Cvaa 160,128,0,224
s_mov_b32 s81, 40960
X 185
Mvaa 160,132,4
Dc 245,8,61,3072
Ra 156,242,15360
Cvaa 164,132,20,228
s_add_u32 s61, s61, s63
X 186
Mvaa 164,128,16
K 210,184,185
Cvaa 168,128,32,232
X 187
Mvaa 168,132,36
buffer_load_dword v7, v6, s[8:11], s81 offen sc1
v_lshrrev_b32 v5, 7, v1
Cvaa 172,132,52,236
s_xor_b32 s62, s62, 0x10000
X 188
Mvaa 172,128,48
K 211,186,187
v_mul_u32_u24 v4, 20480, v5
Mvaa 160,136,8
X 189
Mvaa 160,140,12
s_add_u32 s64, s64, 1
Ra 128,242,24576
Ra 132,242,25600
Mvaa 164,140,28
X 190
Mvaa 164,136,24
v_and_b32 v6, 0x70, v1
K 218,188,189
Mvaa 168,136,40
X 191
Mvaa 168,140,44
v_add_u32 v4, v4, v6
Mvaa 172,140,60
X 160
Mvaa 172,136,56
W 2
v_add_u32 v5, 2, v5
K 219,190,191
Cvaa 176,144,0,224
X 161
Mvaa 176,148,4
Ra 136,242,26624
v_lshrrev_b32 v5, 3, v5
Cvaa 180,148,20,228
Ra 140,242,27648
v_lshl_or_b32 v5, v5, 31, v4
Mvaa 180,144,16
X 162
Cvaa 184,144,32,232
K 196,160,161
Mvaa 184,148,36
s_lshl_b32 s19, s85, 7
X 163
Cvaa 188,148,52,236
X 164
Mvaa 188,144,48
s_add_u32 s19, s19, 2752512
Ra 144,242,28672
K 197,162,163
Mvaa 176,152,8
.Lh_3_2_1%=:
s_cbranch_execz .Lhr_66%=
X 165
Mvaa 176,156,12
)"
R"(s_and_b32 s81, s20, 134217728
Ra 148,242,29696
Mvaa 180,156,28
X 166
Mvaa 180,152,24
K 204,164,165
Mvaa 184,152,40
s_or_b32 s19, s19, s81
X 167
Mvaa 184,156,44
X 168
Mvaa 188,156,60
buffer_load_dwordx4 a[64:67], v4, s[8:11], s19 offen sc1
Mvaa 188,152,56
W 2
Ra 152,242,30720
K 205,166,167
Mvav 32,128,192
s_add_u32 s19, s19, 163840
X 169
Mvav 64,128,200
Ra 156,242,31744
v_xor_b32 v242, 0x10000, v242
Mvav 96,128,208
X 170
Mvav 128,128,216
buffer_load_dwordx4 a[68:71], v4, s[8:11], s19 offen sc1
K 212,168,169
Mvav 132,132,216
X 171
Mvav 100,132,208
s_add_u32 s19, s19, 163840
X 172
Mvav 68,132,200
Mvav 36,132,192
buffer_load_dwordx4 a[72:75], v4, s[8:11], s19 offen sc1
K 213,170,171
Mvav 40,136,192
X 173
Mvav 72,136,200
X 174
Mvav 104,136,208
s_add_u32 s19, s19, 163840
Mvav 136,136,216
K 220,172,173
Mvav 140,140,216
X 175
Mvav 108,140,208
buffer_load_dwordx4 a[76:79], v4, s[8:11], s19 offen sc1
Mvav 76,140,200
s_waitcnt vmcnt(5) lgkmcnt(0)
s_barrier
Mvav 44,140,192
s_add_u32 s19, s19, 163840
X 176
Mvav 48,144,192
Ra 128,242,0
K 221,174,175
Mvav 80,144,200
X 177
Mvav 112,144,208
buffer_load_dwordx4 a[80:83], v4, s[8:11], s19 offen sc1
Ra 132,242,1024
Mvav 144,144,216
s_add_u32 s19, s19, 163840
X 178
Mvav 148,148,216
K 198,176,177
Mvav 116,148,208
X 179
Mvav 84,148,200
buffer_load_dwordx4 a[84:87], v4, s[8:11], s19 offen sc1
Ra 136,242,2048
Mvav 52,148,192
X 180
Mvav 56,152,192
s_add_u32 s19, s19, 163840
Ra 140,242,3072
K 199,178,179
Mvav 88,152,200
X 181
Mvav 120,152,208
X 182
Mvav 152,152,216
buffer_load_dwordx4 a[88:91], v5, s[8:11], s19 offen sc1
K 206,180,181
Mvav 156,156,216
X 183
Mvav 124,156,208
Mvav 92,156,200
K 207,182,183
Mvav 60,156,192
W 0
Cvaa 160,128,0,224
Ra 144,242,4096
Ra 148,242,5120
Mvaa 160,132,4
s_add_u32 m0, s62, 0
Ra 152,242,6144
Cvaa 164,132,20,228
X 184
Mvaa 164,128,16
X 185
Cvaa 168,128,32,232
X 186
Mvaa 168,132,36
Ra 156,242,7168
K 214,184,185
Cvaa 172,132,52,236
Dc 1,8,61,0
Mvaa 172,128,48
X 187
Mvaa 160,136,8
X 188
Mvaa 160,140,12
Ra 128,242,16384
K 215,186,187
Mvaa 164,140,28
Dc 1,8,61,1024
Mvaa 164,136,24
X 189
Mvaa 168,136,40
X 190
Mvaa 168,140,44
Ra 132,242,17408
K 222,188,189
Mvaa 172,140,60
Dc 1,8,61,2048
Mvaa 172,136,56
X 191
v_smfmac_f32_16x16x64_bf16 a[160:163], a[176:179], v[192:199], v240
Ra 136,242,18432
K 223,190,191
v_smfmac_f32_16x16x64_bf16 a[164:167], a[176:179], v[200:207], v240
X 160
v_smfmac_f32_16x16x64_bf16 a[168:171], a[176:179], v[208:215], v240
Dc 1,8,61,3072
v_smfmac_f32_16x16x64_bf16 a[172:175], a[176:179], v[216:223], v240
W 3
X 161
Cvaa 176,144,0,224
s_add_u32 m0, s62, 4096
X 162
Mvaa 176,148,4
Ra 140,242,19456
K 192,160,161
Cvaa 180,148,20,228
X 163
Mvaa 180,144,16
Dc 243,8,61,0
Cvaa 184,144,32,232
X 164
Mvaa 184,148,36
K 193,162,163
Cvaa 188,148,52,236
X 165
Mvaa 188,144,48
Dc 243,8,61,1024
Ra 144,242,20480
Mvaa 176,152,8
X 166
Mvaa 176,156,12
Ra 148,242,21504
K 200,164,165
Mvaa 180,156,28
X 167
Mvaa 180,152,24
Dc 243,8,61,2048
Mvaa 184,152,40
X 168
Mvaa 184,156,44
K 201,166,167
Mvaa 188,156,60
X 169
Mvaa 188,152,56
W 2
Dc 243,8,61,3072
Ra 152,242,22528
Mvav 32,128,196
s_add_u32 m0, s62, 8192
X 170
Mvav 64,128,204
Ra 156,242,23552
K 208,168,169
Mvav 96,128,212
X 171
Mvav 128,128,220
Dc 244,8,61,0
Mvav 132,132,220
X 172
Mvav 100,132,212
Ra 128,242,8192
K 209,170,171
Mvav 68,132,204
X 173
Mvav 36,132,196
Dc 244,8,61,1024
Mvav 40,136,196
X 174
Mvav 72,136,204
Ra 132,242,9216
K 216,172,173
Mvav 104,136,212
X 175
Mvav 136,136,220
Dc 244,8,61,2048
Mvav 140,140,220
s_bfe_u32 s81, s59, 0x10001
X 176
Mvav 108,140,212
s_lshl_b32 s82, s57, 1
Ra 136,242,10240
K 217,174,175
Mvav 76,140,204
s_add_u32 s81, s81, s82
X 177
Mvav 44,140,196
W 3
s_lshl_b32 s83, s81, 2
Dc 244,8,61,3072
Mvav 48,144,196
s_lshr_b32 s81, s59, 2
X 178
Mvav 80,144,204
s_add_u32 m0, s62, 12288
s_lshl_b32 s81, s81, 1
Ra 140,242,11264
Mvav 112,144,212
s_add_u32 s83, s83, s81
K 194,176,177
Mvav 144,144,220
s_and_b32 s81, s59, 1
X 179
Mvav 148,148,220
s_add_u32 s83, s83, s81
X 180
Mvav 116,148,212
s_and_b32 s81, s14, 1
Dc 245,8,61,0
Ra 144,242,12288
Mvav 84,148,204
s_mul_i32 s81, s81, 124
K 195,178,179
Mvav 52,148,196
s_xor_b32 s83, s83, s81
X 181
Mvav 56,152,196
s_mul_i32 s83, s83, 20480
X 182
Mvav 88,152,204
s_mul_i32 s81, s58, 320
Dc 245,8,61,1024
Ra 148,242,13312
Mvav 120,152,212
s_add_u32 s82, s83, s81
K 202,180,181
Mvav 152,152,220
X 183
Mvav 156,156,220
Ra 152,242,14336
v_bfe_u32 v246, v1, 4, 4
Mvav 124,156,212
Dc 245,8,61,2048
v_mul_u32_u24 v246, 1280, v246
Mvav 92,156,204
X 184
Mvav 60,156,196
W 3
v_lshrrev_b32 v247, 8, v1
K 203,182,183
Cvaa 160,128,0,224
v_mul_u32_u24 v247, 104, v247
v_add3_u32 v246, v246, v247, s82
Mvaa 160,132,4
X 185
Cvaa 164,132,20,228
Dc 245,8,61,3072
Ra 156,242,15360
Mvaa 164,128,16
s_add_u32 s61, s61, s63
X 186
Cvaa 168,128,32,232
K 210,184,185
Mvaa 168,132,36
X 187
Cvaa 172,132,52,236
s_xor_b32 s62, s62, 0x10000
X 188
Mvaa 172,128,48
global_load_dword v3, v246, s[16:17]
K 211,186,187
Mvaa 160,136,8
s_lshl_b32 s81, s81, 3
s_add_u32 s83, s83, s81
s_add_u32 s83, s83, 131072
Mvaa 160,140,12
s_add_u32 s64, s64, 1
v_lshrrev_b32 v246, 9, v1
v_mul_u32_u24 v246, 10240, v246
Mvaa 164,140,28
Ra 128,242,24576
v_and_b32 v247, 0x1f0, v1
Mvaa 164,136,24
X 189
Mvaa 168,136,40
Ra 132,242,25600
v_min_u32 v247, 304, v247
Mvaa 168,140,44
X 190
Mvaa 172,140,60
v_lshl_add_u32 v246, v247, 3, v246
K 218,188,189
Mvaa 172,136,56
W 2
buffer_load_dword v3, v246, s[8:11], s83 offen sc1
Ra 136,242,26624
Cvaa 176,144,0,224
X 191
Mvaa 176,148,4
X 160
Cvaa 180,148,20,228
Ra 140,242,27648
K 219,190,191
Mvaa 180,144,16
X 161
Cvaa 184,144,32,232
X 162
Mvaa 184,148,36
K 196,160,161
Cvaa 188,148,52,236
X 163
Mvaa 188,144,48
X 164
Mvaa 176,152,8
Ra 144,242,28672
K 197,162,163
Mvaa 176,156,12
X 165
Mvaa 180,156,28
Ra 148,242,29696
Mvaa 180,152,24
X 166
Mvaa 184,152,40
K 204,164,165
Mvaa 184,156,44
X 167
Mvaa 188,156,60
X 168
Mvaa 188,152,56
W 2
Ra 152,242,30720
K 205,166,167
Mvav 32,128,192
X 169
Mvav 64,128,200
Ra 156,242,31744
v_xor_b32 v242, 0x10000, v242
Mvav 96,128,208
X 170
Mvav 128,128,216
K 212,168,169
Mvav 132,132,216
X 171
Mvav 100,132,208
X 172
Mvav 68,132,200
Mvav 36,132,192
K 213,170,171
Mvav 40,136,192
X 173
Mvav 72,136,200
X 174
Mvav 104,136,208
Mvav 136,136,216
K 220,172,173
Mvav 140,140,216
X 175
Mvav 108,140,208
X 176
Mvav 76,140,200
s_waitcnt vmcnt(2) lgkmcnt(0)
s_barrier
Mvav 44,140,192
Ra 128,242,0
K 221,174,175
Mvav 48,144,192
X 177
Mvav 80,144,200
X 178
Mvav 112,144,208
Ra 132,242,1024
Mvav 144,144,216
K 198,176,177
Mvav 148,148,216
X 179
Mvav 116,148,208
X 180
Mvav 84,148,200
Ra 136,242,2048
Mvav 52,148,192
)"
R"(K 199,178,179
Mvav 56,152,192
X 181
Mvav 88,152,200
Ra 140,242,3072
Mvav 120,152,208
X 182
Mvav 152,152,216
K 206,180,181
Mvav 156,156,216
X 183
Mvav 124,156,208
Mvav 92,156,200
K 207,182,183
Mvav 60,156,192
W 0
Cvaa 160,128,0,224
Ra 144,242,4096
Ra 148,242,5120
Mvaa 160,132,4
s_add_u32 m0, s62, 0
Ra 152,242,6144
G 8,64
Cvaa 164,132,20,228
X 184
Mvaa 164,128,16
X 185
Cvaa 168,128,32,232
X 186
Mvaa 168,132,36
G 9,65
K 214,184,185
Cvaa 172,132,52,236
Ra 156,242,7168
Dc 1,8,61,0
Mvaa 172,128,48
X 187
Mvaa 160,136,8
X 188
Mvaa 160,140,12
Ra 128,242,16384
G 10,66
Mvaa 164,140,28
Dc 1,8,61,1024
K 215,186,187
Mvaa 164,136,24
X 189
Mvaa 168,136,40
Ra 132,242,17408
G 11,67
Mvaa 168,140,44
X 190
Mvaa 172,140,60
Dc 1,8,61,2048
K 222,188,189
Mvaa 172,136,56
X 191
v_smfmac_f32_16x16x64_bf16 a[160:163], a[176:179], v[192:199], v240
G 12,68
K 223,190,191
v_smfmac_f32_16x16x64_bf16 a[164:167], a[176:179], v[200:207], v240
Ra 136,242,18432
v_smfmac_f32_16x16x64_bf16 a[168:171], a[176:179], v[208:215], v240
Dc 1,8,61,3072
G 13,69
v_smfmac_f32_16x16x64_bf16 a[172:175], a[176:179], v[216:223], v240
W 3
X 160
Cvaa 176,144,0,224
s_add_u32 m0, s62, 4096
X 161
Mvaa 176,148,4
X 162
Cvaa 180,148,20,228
Ra 140,242,19456
K 192,160,161
Mvaa 180,144,16
Dc 243,8,61,0
G 14,70
Cvaa 184,144,32,232
X 163
Mvaa 184,148,36
X 164
Cvaa 188,148,52,236
G 15,71
K 193,162,163
Mvaa 188,144,48
Dc 243,8,61,1024
Ra 144,242,20480
Mvaa 176,152,8
X 165
Mvaa 176,156,12
X 166
Mvaa 180,156,28
Ra 148,242,21504
A 8,8,12
Mvaa 180,152,24
Dc 243,8,61,2048
K 200,164,165
Mvaa 184,152,40
X 167
Mvaa 184,156,44
A 9,9,13
Mvaa 188,156,60
X 168
Mvaa 188,152,56
W 2
Dc 243,8,61,3072
K 201,166,167
Mvav 32,128,196
s_add_u32 m0, s62, 8192
X 169
Mvav 64,128,204
Ra 152,242,22528
A 10,10,14
Mvav 96,128,212
X 170
Mvav 128,128,220
Ra 156,242,23552
K 208,168,169
Mvav 132,132,220
Dc 244,8,61,0
A 11,11,15
Mvav 100,132,212
X 171
Mvav 68,132,204
X 172
Mvav 36,132,196
Ra 128,242,8192
K 209,170,171
Mvav 40,136,196
Dc 244,8,61,1024
G 12,72
Mvav 72,136,204
X 173
Mvav 104,136,212
X 174
Mvav 136,136,220
Ra 132,242,9216
G 13,73
Mvav 140,140,220
Dc 244,8,61,2048
K 216,172,173
Mvav 108,140,212
X 175
Mvav 76,140,204
X 176
Mvav 44,140,196
W 2
Ra 136,242,10240
G 14,74
Mvav 48,144,196
Dc 244,8,61,3072
K 217,174,175
Mvav 80,144,204
s_add_u32 m0, s62, 12288
X 177
Mvav 112,144,212
Ra 140,242,11264
G 15,75
Mvav 144,144,220
X 178
Mvav 148,148,220
K 194,176,177
A 8,8,12
Mvav 116,148,212
X 179
Mvav 84,148,204
X 180
Mvav 52,148,196
Dc 245,8,61,0
Ra 144,242,12288
Mvav 56,152,196
K 195,178,179
A 9,9,13
Mvav 88,152,204
X 181
Mvav 120,152,212
X 182
Mvav 152,152,220
Dc 245,8,61,1024
Ra 148,242,13312
Mvav 156,156,220
K 202,180,181
A 10,10,14
Mvav 124,156,212
X 183
Mvav 92,156,204
X 184
Mvav 60,156,196
W 2
Dc 245,8,61,2048
Ra 152,242,14336
Cvaa 160,128,0,224
.Lh_4_2_1%=:
s_cbranch_execz .Lh_4_2_2%=
A 11,11,15
K 203,182,183
Mvaa 160,132,4
X 185
Cvaa 164,132,20,228
X 186
Mvaa 164,128,16
Dc 245,8,61,3072
Ra 156,242,15360
Cvaa 168,128,32,232
s_add_u32 s61, s61, s63
G 12,76
K 210,184,185
Mvaa 168,132,36
X 187
Cvaa 172,132,52,236
s_xor_b32 s62, s62, 0x10000
G 13,77
Mvaa 172,128,48
X 188
Mvaa 160,136,8
K 211,186,187
G 14,78
Mvaa 160,140,12
s_add_u32 s64, s64, 1
X 189
Mvaa 164,140,28
Ra 128,242,24576
Ra 132,242,25600
Mvaa 164,136,24
X 190
Mvaa 168,136,40
K 218,188,189
G 15,79
Mvaa 168,140,44
X 191
Mvaa 172,140,60
s_lshl_b32 s92, s58, 2
X 160
Mvaa 172,136,56
W 2
K 219,190,191
A 8,8,12
Cvaa 176,144,0,224
X 161
Mvaa 176,148,4
Ra 136,242,26624
Ra 140,242,27648
Cvaa 180,148,20,228
s_add_u32 s91, s92, s51
X 162
Mvaa 180,144,16
A 9,9,13
K 196,160,161
Cvaa 184,144,32,232
X 163
Mvaa 184,148,36
s_add_u32 s95, s92, 0
X 164
Cvaa 188,148,52,236
A 10,10,14
K 197,162,163
Mvaa 188,144,48
X 165
Mvaa 176,152,8
s_and_b32 s95, s95, 15
Ra 144,242,28672
A 11,11,15
Mvaa 176,156,12
Ra 148,242,29696
Mvaa 180,156,28
X 166
Mvaa 180,152,24
s_mul_i32 s95, s95, 40960
K 204,164,165
Mvaa 184,152,40
X 167
Mvaa 184,156,44
G 12,80
Mvaa 188,156,60
s_mul_i32 s71, s51, 0
X 168
Mvaa 188,152,56
W 2
K 205,166,167
G 13,81
Mvav 32,128,192
X 169
Mvav 64,128,200
s_add_u32 s71, s71, 133120
Ra 152,242,30720
Ra 156,242,31744
Mvav 96,128,208
X 170
Mvav 128,128,216
v_xor_b32 v242, 0x10000, v242
G 14,82
Mvav 132,132,216
s_add_u32 m0, s71, s48
K 212,168,169
Mvav 100,132,208
X 171
Mvav 68,132,200
X 172
Mvav 36,132,192
s_lshl2_add_u32 s94, s48, s95
G 15,83
K 213,170,171
Mvav 40,136,192
X 173
Mvav 72,136,200
X 174
Mvav 104,136,208
D 16,40,94,0
A 8,8,12
Mvav 136,136,216
K 220,172,173
Mvav 140,140,216
X 175
Mvav 108,140,208
A 9,9,13
Mvav 76,140,200
s_waitcnt vmcnt(1) lgkmcnt(0)
s_barrier
Mvav 44,140,192
D 17,40,94,1024
Ra 128,242,0
Mvav 48,144,192
X 176
Mvav 80,144,200
K 221,174,175
A 10,10,14
Mvav 112,144,208
X 177
Mvav 144,144,216
D 18,40,94,2048
Ra 132,242,1024
Mvav 148,148,216
X 178
Mvav 116,148,208
K 198,176,177
A 11,11,15
Mvav 84,148,200
X 179
Mvav 52,148,192
D 19,40,94,3072
Ra 136,242,2048
Mvav 56,152,192
s_add_u32 m0, m0, 4096
X 180
Mvav 88,152,200
G 12,84
K 199,178,179
Mvav 120,152,208
X 181
Mvav 152,152,216
Ra 140,242,3072
buffer_load_dwordx4 v20, s[40:43], s94 offen lds
Mvav 156,156,216
X 182
Mvav 124,156,208
K 206,180,181
Mvav 92,156,200
X 183
Mvav 60,156,192
K 207,182,183
W 0
Cvaa 160,128,0,224
Ra 144,242,4096
Ra 148,242,5120
Mvaa 160,132,4
s_add_u32 m0, s62, 0
Ra 152,242,6144
G 13,85
Cvaa 164,132,20,228
X 184
Mvaa 164,128,16
X 185
Cvaa 168,128,32,232
X 186
Mvaa 168,132,36
G 14,86
K 214,184,185
Cvaa 172,132,52,236
Ra 156,242,7168
Dc 1,8,61,0
Mvaa 172,128,48
X 187
Mvaa 160,136,8
X 188
Mvaa 160,140,12
Ra 128,242,16384
G 15,87
Mvaa 164,140,28
Dc 1,8,61,1024
K 215,186,187
Mvaa 164,136,24
X 189
Mvaa 168,136,40
Ra 132,242,17408
A 8,8,12
Mvaa 168,140,44
X 190
Mvaa 172,140,60
)"
R"(Dc 1,8,61,2048
K 222,188,189
Mvaa 172,136,56
X 191
v_smfmac_f32_16x16x64_bf16 a[160:163], a[176:179], v[192:199], v240
A 9,9,13
K 223,190,191
v_smfmac_f32_16x16x64_bf16 a[164:167], a[176:179], v[200:207], v240
Ra 136,242,18432
A 10,10,14
v_smfmac_f32_16x16x64_bf16 a[168:171], a[176:179], v[208:215], v240
Dc 1,8,61,3072
v_smfmac_f32_16x16x64_bf16 a[172:175], a[176:179], v[216:223], v240
W 3
X 160
Cvaa 176,144,0,224
s_add_u32 m0, s62, 4096
X 161
Mvaa 176,148,4
X 162
Cvaa 180,148,20,228
Ra 140,242,19456
A 11,11,15
Mvaa 180,144,16
Dc 243,8,61,0
K 192,160,161
Cvaa 184,144,32,232
X 163
Mvaa 184,148,36
X 164
Cvaa 188,148,52,236
G 12,88
K 193,162,163
Mvaa 188,144,48
Dc 243,8,61,1024
G 13,89
Mvaa 176,152,8
X 165
Mvaa 176,156,12
X 166
Mvaa 180,156,28
Ra 144,242,20480
Ra 148,242,21504
Mvaa 180,152,24
Dc 243,8,61,2048
K 200,164,165
Mvaa 184,152,40
X 167
Mvaa 184,156,44
G 14,90
Mvaa 188,156,60
X 168
Mvaa 188,152,56
W 2
Dc 243,8,61,3072
K 201,166,167
Mvav 32,128,196
s_add_u32 m0, s62, 8192
Ra 152,242,22528
G 15,91
Mvav 64,128,204
X 169
Mvav 96,128,212
X 170
Mvav 128,128,220
Ra 156,242,23552
A 8,8,12
Mvav 132,132,220
Dc 244,8,61,0
K 208,168,169
Mvav 100,132,212
X 171
Mvav 68,132,204
X 172
Mvav 36,132,196
Ra 128,242,8192
A 9,9,13
Mvav 40,136,196
Dc 244,8,61,1024
K 209,170,171
Mvav 72,136,204
X 173
Mvav 104,136,212
Ra 132,242,9216
A 10,10,14
Mvav 136,136,220
X 174
Mvav 140,140,220
Dc 244,8,61,2048
K 216,172,173
Mvav 108,140,212
X 175
Mvav 76,140,204
Ra 136,242,10240
A 11,11,15
Mvav 44,140,196
W 3
X 176
Mvav 48,144,196
Dc 244,8,61,3072
K 217,174,175
Mvav 80,144,204
s_add_u32 m0, s62, 12288
Ra 140,242,11264
v_add_f32_dpp v8, v8, v8 row_ror:8 row_mask:0xf bank_mask:0xf
Mvav 112,144,212
X 177
Mvav 144,144,220
X 178
Mvav 148,148,220
K 194,176,177
v_add_f32_dpp v9, v9, v9 row_ror:8 row_mask:0xf bank_mask:0xf
Mvav 116,148,212
X 179
Mvav 84,148,204
X 180
Mvav 52,148,196
Dc 245,8,61,0
Ra 144,242,12288
Mvav 56,152,196
v_add_f32_dpp v10, v10, v10 row_ror:8 row_mask:0xf bank_mask:0xf
K 195,178,179
Mvav 88,152,204
X 181
Mvav 120,152,212
X 182
Mvav 152,152,220
Dc 245,8,61,1024
v_add_f32_dpp v11, v11, v11 row_ror:8 row_mask:0xf bank_mask:0xf
Mvav 156,156,220
Ra 148,242,13312
K 202,180,181
Mvav 124,156,212
X 183
Mvav 92,156,204
Ra 152,242,14336
v_mov_b32 v12, v8
Mvav 60,156,196
W 3
Dc 245,8,61,2048
v_mov_b32 v13, v9
Cvaa 160,128,0,224
X 184
Mvaa 160,132,4
Ra 156,242,15360
K 203,182,183
Cvaa 164,132,20,228
X 185
Mvaa 164,128,16
Dc 245,8,61,3072
v_mov_b32 v14, v10
Cvaa 168,128,32,232
s_add_u32 s61, s61, s63
X 186
Mvaa 168,132,36
K 210,184,185
v_mov_b32 v15, v11
Cvaa 172,132,52,236
s_xor_b32 s62, s62, 0x10000
X 187
Mvaa 172,128,48
X 188
Mvaa 160,136,8
Ra 128,242,24576
K 211,186,187
Mvaa 160,140,12
s_add_u32 s64, s64, 1
X 189
Mvaa 164,140,28
X 190
Mvaa 164,136,24
Ra 132,242,25600
v_permlane16_swap_b32 v8, v12
Mvaa 168,136,40
K 218,188,189
v_permlane16_swap_b32 v9, v13
Mvaa 168,140,44
X 191
Mvaa 172,140,60
s_lshl_b32 s92, s58, 2
X 160
Mvaa 172,136,56
W 2
K 219,190,191
v_permlane16_swap_b32 v10, v14
Cvaa 176,144,0,224
X 161
Mvaa 176,148,4
Ra 136,242,26624
Ra 140,242,27648
Cvaa 180,148,20,228
s_add_u32 s91, s92, s51
X 162
Mvaa 180,144,16
K 196,160,161
v_permlane16_swap_b32 v11, v15
Cvaa 184,144,32,232
X 163
Mvaa 184,148,36
s_add_u32 s95, s92, 1
X 164
Cvaa 188,148,52,236
A 8,8,12
K 197,162,163
Mvaa 188,144,48
X 165
Mvaa 176,152,8
s_and_b32 s95, s95, 15
Ra 144,242,28672
Ra 148,242,29696
Mvaa 176,156,12
X 166
Mvaa 180,156,28
A 9,9,13
K 204,164,165
Mvaa 180,152,24
s_mul_i32 s95, s95, 40960
X 167
Mvaa 184,152,40
Mvaa 184,156,44
A 10,10,14
Mvaa 188,156,60
s_mul_i32 s71, s51, 0
X 168
Mvaa 188,152,56
W 2
Ra 152,242,30720
K 205,166,167
Mvav 32,128,192
X 169
Mvav 64,128,200
s_add_u32 s71, s71, 143360
Ra 156,242,31744
A 11,11,15
Mvav 96,128,208
X 170
Mvav 128,128,216
v_xor_b32 v242, 0x10000, v242
K 212,168,169
Mvav 132,132,216
s_add_u32 m0, s71, s48
v_mov_b32 v12, v8
Mvav 100,132,208
X 171
Mvav 68,132,200
X 172
Mvav 36,132,192
s_lshl2_add_u32 s94, s48, s95
v_mov_b32 v13, v9
K 213,170,171
Mvav 40,136,192
X 173
Mvav 72,136,200
X 174
Mvav 104,136,208
D 16,40,94,0
v_mov_b32 v14, v10
Mvav 136,136,216
K 220,172,173
Mvav 140,140,216
X 175
Mvav 108,140,208
v_mov_b32 v15, v11
Mvav 76,140,200
s_waitcnt vmcnt(1) lgkmcnt(0)
s_barrier
Mvav 44,140,192
D 17,40,94,1024
Ra 128,242,0
Mvav 48,144,192
X 176
Mvav 80,144,200
K 221,174,175
v_permlane32_swap_b32 v8, v12
Mvav 112,144,208
X 177
Mvav 144,144,216
D 18,40,94,2048
Ra 132,242,1024
Mvav 148,148,216
X 178
Mvav 116,148,208
K 198,176,177
v_permlane32_swap_b32 v9, v13
Mvav 84,148,200
X 179
Mvav 52,148,192
D 19,40,94,3072
Ra 136,242,2048
Mvav 56,152,192
s_add_u32 m0, m0, 4096
X 180
Mvav 88,152,200
K 199,178,179
v_permlane32_swap_b32 v10, v14
Mvav 120,152,208
X 181
Mvav 152,152,216
Ra 140,242,3072
buffer_load_dwordx4 v20, s[40:43], s94 offen lds
Mvav 156,156,216
X 182
Mvav 124,156,208
K 206,180,181
Mvav 92,156,200
X 183
Mvav 60,156,192
K 207,182,183
W 0
Cvaa 160,128,0,224
Ra 144,242,4096
Ra 148,242,5120
Mvaa 160,132,4
s_add_u32 m0, s62, 0
Ra 152,242,6144
v_permlane32_swap_b32 v11, v15
Cvaa 164,132,20,228
X 184
Mvaa 164,128,16
X 185
Cvaa 168,128,32,232
X 186
Mvaa 168,132,36
A 8,8,12
K 214,184,185
Cvaa 172,132,52,236
Ra 156,242,7168
Dc 1,8,61,0
Mvaa 172,128,48
X 187
Mvaa 160,136,8
Ra 128,242,16384
A 9,9,13
Mvaa 160,140,12
X 188
Mvaa 164,140,28
Dc 1,8,61,1024
K 215,186,187
Mvaa 164,136,24
X 189
Mvaa 168,136,40
Ra 132,242,17408
A 10,10,14
Mvaa 168,140,44
X 190
Mvaa 172,140,60
Dc 1,8,61,2048
K 222,188,189
Mvaa 172,136,56
A 11,11,15
v_smfmac_f32_16x16x64_bf16 a[160:163], a[176:179], v[192:199], v240
X 191
v_smfmac_f32_16x16x64_bf16 a[164:167], a[176:179], v[200:207], v240
K 223,190,191
v_and_b32 v7, 0xffff0000, v7
v_smfmac_f32_16x16x64_bf16 a[168:171], a[176:179], v[208:215], v240
Dc 1,8,61,3072
Ra 136,242,18432
v_smfmac_f32_16x16x64_bf16 a[172:175], a[176:179], v[216:223], v240
W 3
X 160
Cvaa 176,144,0,224
s_add_u32 m0, s62, 4096
X 161
Mvaa 176,148,4
X 162
Cvaa 180,148,20,228
Ra 140,242,19456
v_ashrrev_i32 v6, 31, v7
Mvaa 180,144,16
Dc 243,8,61,0
K 192,160,161
Cvaa 184,144,32,232
X 163
Mvaa 184,148,36
v_not_b32 v6, v6
Cvaa 188,148,52,236
X 164
Mvaa 188,144,48
Dc 243,8,61,1024
K 193,162,163
Mvaa 176,152,8
X 165
Mvaa 176,156,12
Ra 144,242,20480
v_or_b32 v6, 0x80000000, v6
Mvaa 180,156,28
X 166
Mvaa 180,152,24
Ra 148,242,21504
v_xor_b32 v7, v7, v6
Mvaa 184,152,40
Dc 243,8,61,2048
K 200,164,165
Mvaa 184,156,44
X 167
Mvaa 188,156,60
X 168
Mvaa 188,152,56
W 2
)"
R"(v_and_b32 v7, 0xffff0000, v7
K 201,166,167
Mvav 32,128,196
.Lh_4_3_1%=:
s_cbranch_execz .Lhr_76%=
Dc 243,8,61,3072
v_mul_f32 v7, 0xbfb8aa3b, v7
Mvav 64,128,204
s_add_u32 m0, s62, 8192
X 169
Mvav 96,128,212
Ra 152,242,22528
Ra 156,242,23552
Mvav 128,128,220
X 170
Mvav 132,132,220
Dc 244,8,61,0
K 208,168,169
Mvav 100,132,212
X 171
Mvav 68,132,204
X 7
Mvav 36,132,196
X 172
Mvav 40,136,196
Ra 128,242,8192
K 209,170,171
Mvav 72,136,204
X 173
Mvav 104,136,212
Dc 244,8,61,1024
Ra 132,242,9216
Mvav 136,136,220
X 174
Mvav 140,140,220
v_add_f32 v7, 1.0, v7
K 216,172,173
Mvav 108,140,212
X 175
Mvav 76,140,204
Dc 244,8,61,2048
Ra 136,242,10240
Mvav 44,140,196
W 3
v_rcp_f32 v7, v7
Mvav 48,144,196
X 176
Mvav 80,144,204
Ra 140,242,11264
K 217,174,175
Mvav 112,144,212
Dc 244,8,61,3072
K 7,7,7
Mvav 144,144,220
s_add_u32 m0, s62, 12288
X 177
Mvav 148,148,220
X 178
Mvav 116,148,212
Ra 144,242,12288
K 194,176,177
Mvav 84,148,204
X 179
Mvav 52,148,196
X 180
Mvav 56,152,196
Dc 245,8,61,0
v_lshlrev_b32 v7, 16, v7
Mvav 88,152,204
K 195,178,179
K 12,8,9
Mvav 120,152,212
X 181
Mvav 152,152,220
X 182
Mvav 156,156,220
Dc 245,8,61,1024
Ra 148,242,13312
Mvav 124,156,212
K 202,180,181
K 13,10,11
Mvav 92,156,204
X 183
Mvav 60,156,196
W 2
X 184
Cvaa 160,128,0,224
Dc 245,8,61,2048
Ra 152,242,14336
Mvaa 160,132,4
K 203,182,183
v_lshlrev_b32 v8, 16, v12
Cvaa 164,132,20,228
X 185
Mvaa 164,128,16
X 186
Cvaa 168,128,32,232
Dc 245,8,61,3072
Ra 156,242,15360
Mvaa 168,132,36
s_add_u32 s61, s61, s63
v_and_b32 v9, 0xffff0000, v12
K 210,184,185
Cvaa 172,132,52,236
s_xor_b32 s62, s62, 0x10000
X 187
Mvaa 172,128,48
X 188
Mvaa 160,136,8
v_lshlrev_b32 v10, 16, v13
K 211,186,187
Mvaa 160,140,12
s_add_u32 s64, s64, 1
X 189
Mvaa 164,140,28
Ra 128,242,24576
v_and_b32 v11, 0xffff0000, v13
Mvaa 164,136,24
X 190
Mvaa 168,136,40
Ra 132,242,25600
K 218,188,189
Mvaa 168,140,44
X 191
Mvaa 172,140,60
s_lshl_b32 s92, s58, 2
U 8,8,7
U 9,9,7
Mvaa 172,136,56
W 2
X 160
Cvaa 176,144,0,224
Ra 136,242,26624
K 219,190,191
Mvaa 176,148,4
X 161
Cvaa 180,148,20,228
s_add_u32 s91, s92, s51
Ra 140,242,27648
U 10,10,7
Mvaa 180,144,16
X 162
Cvaa 184,144,32,232
K 196,160,161
Mvaa 184,148,36
s_add_u32 s95, s92, 2
X 163
Cvaa 188,148,52,236
U 11,11,7
Mvaa 188,144,48
X 164
Mvaa 176,152,8
s_and_b32 s95, s95, 15
Ra 144,242,28672
K 197,162,163
Mvaa 176,156,12
X 165
Mvaa 180,156,28
Ra 148,242,29696
K 12,8,9
Mvaa 180,152,24
s_mul_i32 s95, s95, 40960
X 166
Mvaa 184,152,40
K 204,164,165
K 14,10,11
Mvaa 184,156,44
X 167
Mvaa 188,156,60
s_mul_i32 s71, s51, 0
X 168
Mvaa 188,152,56
W 2
v_mov_b32 v13, s13
K 205,166,167
Mvav 32,128,192
X 169
Mvav 64,128,200
s_add_u32 s71, s71, 153600
Ra 152,242,30720
Ra 156,242,31744
Mvav 96,128,208
X 170
Mvav 128,128,216
v_mov_b32 v15, s13
v_xor_b32 v242, 0x10000, v242
Mvav 132,132,216
s_add_u32 m0, s71, s48
K 212,168,169
v_lshrrev_b32 v6, 7, v1
Mvav 100,132,208
X 171
Mvav 68,132,200
X 172
Mvav 36,132,192
s_lshl2_add_u32 s94, s48, s95
K 213,170,171
Mvav 40,136,192
X 173
Mvav 72,136,200
v_lshl_or_b32 v6, v6, 28, v1
Mvav 104,136,208
X 174
Mvav 136,136,216
s_lshl_b32 s19, s85, 7
D 16,40,94,0
K 220,172,173
Mvav 140,140,216
X 175
Mvav 108,140,208
X 176
Mvav 76,140,200
s_waitcnt vmcnt(1) lgkmcnt(0)
s_barrier
Mvav 44,140,192
s_add_u32 s19, s19, 98304
D 17,40,94,1024
K 221,174,175
Mvav 48,144,192
Ra 128,242,0
Mvav 80,144,200
s_and_b32 s21, s20, 134217728
X 177
Mvav 112,144,208
X 178
Mvav 144,144,216
D 18,40,94,2048
Ra 132,242,1024
Mvav 148,148,216
s_or_b32 s19, s19, s21
K 198,176,177
Mvav 116,148,208
X 179
Mvav 84,148,200
X 180
Mvav 52,148,192
D 19,40,94,3072
Ra 136,242,2048
Mvav 56,152,192
s_add_u32 m0, m0, 4096
Ra 140,242,3072
K 199,178,179
Mvav 88,152,200
X 181
Mvav 120,152,208
X 182
Mvav 152,152,216
buffer_store_dwordx4 v[12:15], v6, s[8:11], s19 offen sc1
K 206,180,181
Mvav 156,156,216
X 183
Mvav 124,156,208
Mvav 92,156,200
K 207,182,183
Mvav 60,156,192
buffer_load_dwordx4 v20, s[40:43], s94 offen lds
.Lf1_36%=:
W 0
Cvaa 160,128,0,224
v_bfe_u32 v28, v1, 4, 2
v_lshlrev_b32 v28, 3, v28
Mvaa 160,132,4
Ra 144,242,4096
Ra 148,242,5120
Cvaa 164,132,20,228
Ra 152,242,6144
buffer_load_dwordx2 v[28:29], v28, s[8:11], s54 offen sc1
Mvaa 164,128,16
s_lshl_b32 s92, s58, 2
s_add_u32 s91, s92, s51
s_add_u32 s95, s91, 3
Cvaa 168,128,32,232
s_and_b32 s95, s95, 15
s_mul_i32 s95, s95, 40960
s_mul_i32 s71, s51, 10240
Mvaa 168,132,36
s_add_u32 s71, s71, 0
s_add_u32 m0, s71, s48
s_lshl2_add_u32 s94, s48, s95
Cvaa 172,132,52,236
X 184
Mvaa 172,128,48
X 185
Mvaa 160,136,8
X 186
Mvaa 160,140,12
Ra 156,242,7168
K 214,184,185
Mvaa 164,140,28
X 187
Mvaa 164,136,24
D 16,40,94,0
Ra 128,242,16384
Mvaa 168,136,40
s_add_u32 s95, s91, 5
s_and_b32 s95, s95, 15
s_mul_i32 s95, s95, 40960
Mvaa 168,140,44
s_mul_i32 s71, s51, 10240
s_add_u32 s71, s71, 20480
Ra 132,242,17408
Mvaa 172,140,60
X 188
Mvaa 172,136,56
D 17,40,94,1024
K 215,186,187
v_smfmac_f32_16x16x64_bf16 a[160:163], a[176:179], v[192:199], v240
X 189
v_smfmac_f32_16x16x64_bf16 a[164:167], a[176:179], v[200:207], v240
X 190
K 222,188,189
X 191
s_nop 0
K 223,190,191
v_smfmac_f32_16x16x64_bf16 a[168:171], a[176:179], v[208:215], v240
D 18,40,94,2048
Ra 136,242,18432
v_smfmac_f32_16x16x64_bf16 a[172:175], a[176:179], v[216:223], v240
W 3
X 160
Cvaa 176,144,0,224
X 161
Mvaa 176,148,4
X 162
Cvaa 180,148,20,228
D 19,40,94,3072
K 192,160,161
Mvaa 180,144,16
s_add_u32 m0, m0, 4096
X 163
Cvaa 184,144,32,232
X 164
Mvaa 184,148,36
Ra 140,242,19456
K 193,162,163
Cvaa 188,148,52,236
buffer_load_dwordx4 v20, s[40:43], s94 offen lds
Mvaa 188,144,48
s_add_u32 m0, s71, s48
s_lshl2_add_u32 s94, s48, s95
Ra 144,242,20480
Mvaa 176,152,8
X 165
Mvaa 176,156,12
X 166
Mvaa 180,156,28
D 16,40,94,0
K 200,164,165
Mvaa 180,152,24
X 167
Mvaa 184,152,40
X 172
Mvaa 184,156,44
D 17,40,94,1024
K 201,166,167
Mvaa 188,156,60
X 173
Mvaa 188,152,56
s_cmp_eq_u32 s60, 16
s_cbranch_scc1 .Lmk_77%=
.Lmkr_78%=:
W 1
Mvav 32,128,196
Ra 148,242,21504
D 18,40,94,2048
Mvav 64,128,204
X 174
Mvav 96,128,212
Ra 152,242,22528
K 216,172,173
Mvav 128,128,220
X 175
Mvav 132,132,220
s_mov_b32 s18, 40960
X 168
Mvav 100,132,212
D 19,40,94,3072
K 217,174,175
Mvav 68,132,204
s_add_u32 m0, m0, 4096
X 169
Mvav 36,132,196
s_mov_b32 s19, 73728
X 170
Mvav 40,136,196
s_bitcmp1_b32 s14, 0
s_cselect_b32 s18, 49152, s18
s_cselect_b32 s19, 98304, s19
Ra 156,242,23552
K 208,168,169
Mvav 72,136,204
s_cmp_lt_u32 s14, 2
s_cselect_b32 s20, s18, s19
s_cselect_b32 s21, s19, s18
X 171
Mvav 104,136,212
X 184
Mvav 136,136,220
buffer_load_dwordx4 v20, s[40:43], s94 offen lds
K 209,170,171
)"
R"(Mvav 140,140,220
s_waitcnt vmcnt(10)
v_cmp_eq_u64 vcc, s[12:13], v[28:29]
s_cmp_eq_u64 vcc, -1
s_cselect_b32 s46, 79691776, 0
s_add_u32 s81, s58, 0
s_and_b32 s81, s81, 3
Mvav 108,140,212
s_lshl_b32 s81, s81, 12
s_add_u32 s81, s81, s49
Ra 128,242,24576
Mvav 76,140,204
X 185
Mvav 44,140,196
W 1
X 186
Mvav 48,144,196
buffer_load_dwordx4 a[184:187], v1, s[44:47], s81 offen offset:0 sc1
K 210,184,185
Mvav 80,144,204
s_add_u32 s82, s81, 131072
X 187
Mvav 112,144,212
v_add_u32 v2, s20, v1
v_add_u32 v3, s21, v1
Mvav 144,144,220
s_lshl_b32 s18, s14, 9
X 188
Mvav 148,148,220
Ra 132,242,25600
K 211,186,187
Mvav 116,148,212
buffer_load_dwordx4 a[200:203], v1, s[44:47], s82 offen offset:0 sc1
Ra 136,242,26624
Mvav 84,148,204
X 189
Mvav 52,148,196
v_lshrrev_b32 v232, 1, v1
v_add_u32 v232, s18, v232
Mvav 56,152,196
X 190
Mvav 88,152,204
K 218,188,189
v_bfe_u32 v12, v1, 6, 2
Mvav 120,152,212
buffer_load_dwordx4 a[188:191], v1, s[44:47], s81 offen offset:1024 sc1
Ra 140,242,27648
Mvav 152,152,220
X 191
Mvav 156,156,220
v_lshrrev_b32 v13, 8, v1
v_bfe_u32 v14, v1, 4, 1
Mvav 124,156,212
X 176
Mvav 92,156,204
K 219,190,191
v_lshlrev_b32 v14, 4, v14
Mvav 60,156,196
X 177
Maav 168,180,208
X 178
K 194,176,177
X 179
X 180
K 195,178,179
Maav 172,180,216
X 181
X 182
K 202,180,181
X 183
s_nop 0
K 203,182,183
Maav 160,180,192
buffer_load_dwordx4 a[204:207], v1, s[44:47], s82 offen offset:1024 sc1
v_mov_b32 v15, 0x3f80
Maav 164,180,200
W 0
Ra 144,242,28672
v_lshlrev_b32 v15, v14, v15
Mvav 96,128,208
Ra 148,242,29696
v_bfe_u32 v14, v1, 5, 1
v_xor_b32 v27, v12, v13
Mvav 128,128,216
buffer_load_dwordx4 a[192:195], v1, s[44:47], s81 offen offset:2048 sc1
v_lshl_or_b32 v27, v27, 1, v14
v_cmp_eq_u32_e64 s[86:87], 0, v27
Mvav 132,132,216
Ra 152,242,30720
Ra 156,242,31744
v_mov_b32 v226, 0
Mvav 100,132,208
v_cndmask_b32_e64 v224, 0, v15, s[86:87]
v_mov_b32 v228, 0
v_mov_b32 v230, v224
Mvav 104,136,208
buffer_load_dwordx4 a[208:211], v1, s[44:47], s82 offen offset:2048 sc1
v_cmp_eq_u32_e64 s[86:87], 1, v27
v_mov_b32 v227, 0
Mvav 136,136,216
v_cndmask_b32_e64 v225, 0, v15, s[86:87]
v_mov_b32 v229, 0
v_mov_b32 v231, v225
Mvav 140,140,216
G 4,160
G 8,161
G 5,164
Mvav 108,140,208
W 0
buffer_load_dwordx4 a[196:199], v1, s[44:47], s81 offen offset:3072 sc1
G 9,165
G 6,168
Mvav 112,144,208
G 10,169
G 7,172
G 11,173
Mvav 144,144,216
A 4,4,8
A 5,5,9
A 6,6,10
Mvav 148,148,216
buffer_load_dwordx4 a[212:215], v1, s[44:47], s82 offen offset:3072 sc1
s_add_u32 s81, s58, 1
s_and_b32 s81, s81, 3
s_lshl_b32 s81, s81, 12
Mvav 116,148,208
s_add_u32 s81, s81, s49
A 7,7,11
ds_write_b64 v232, v[6:7] offset:57344
A 8,4,5
Mvav 120,152,208
A 9,6,7
A 8,8,9
v_cmp_ngt_f32_e64 s[86:87], s90, v8
s_lshl_b32 s19, s14, 2
Mvav 152,152,216
buffer_load_dwordx4 a[216:219], v1, s[44:47], s81 offen offset:0 sc1
s_add_u32 s82, s81, 131072
v_mov_b32 v9, s19
s_or_b64 s[88:89], s[88:89], s[86:87]
Mvav 156,156,216
s_and_b64 s[88:89], s[88:89], s[24:25]
s_cselect_b32 s18, 1, 0
v_mov_b32 v8, s18
ds_write_b32 v9, v8 offset:59392
v_xor_b32 v10, 1024, v232
Mvav 124,156,208
v_bfe_u32 v11, v1, 2, 4
K 96,96,97
K 97,98,99
Mvav 32,128,192
buffer_load_dwordx4 a[232:235], v1, s[44:47], s82 offen offset:0 sc1
K 98,100,101
K 99,102,103
Mvav 64,128,200
ds_write_b128 v2, v[96:99] offset:0
K 128,128,129
K 129,130,131
Mvav 68,132,200
K 130,132,133
K 131,134,135
ds_write_b128 v2, v[128:131] offset:4096
Mvav 36,132,192
buffer_load_dwordx4 a[220:223], v1, s[44:47], s81 offen offset:1024 sc1
K 104,104,105
K 105,106,107
Mvav 40,136,192
K 106,108,109
K 107,110,111
ds_write_b128 v2, v[104:107] offset:1024
Mvav 72,136,200
K 136,136,137
K 137,138,139
K 138,140,141
Mvav 76,140,200
buffer_load_dwordx4 a[236:239], v1, s[44:47], s82 offen offset:1024 sc1
K 139,142,143
ds_write_b128 v2, v[136:139] offset:5120
Mvav 44,140,192
K 112,112,113
K 113,114,115
K 114,116,117
Mvav 48,144,192
K 115,118,119
ds_write_b128 v2, v[112:115] offset:2048
K 144,144,145
Mvav 80,144,200
K 145,146,147
K 146,148,149
K 147,150,151
Mvav 84,148,200
ds_write_b128 v2, v[144:147] offset:6144
buffer_load_dwordx4 a[224:227], v1, s[44:47], s81 offen offset:2048 sc1
K 120,120,121
Mvav 52,148,192
K 121,122,123
K 122,124,125
K 123,126,127
Mvav 56,152,192
ds_write_b128 v2, v[120:123] offset:3072
K 152,152,153
K 153,154,155
Mvav 88,152,200
buffer_load_dwordx4 a[240:243], v1, s[44:47], s82 offen offset:2048 sc1
K 154,156,157
K 155,158,159
Mvav 92,156,200
.Lh_4_2_2%=:
s_cbranch_execz .Lhr_76%=
ds_write_b128 v2, v[152:155] offset:7168
Mvav 60,156,192
buffer_load_dwordx4 a[228:231], v1, s[44:47], s81 offen offset:3072 sc1
buffer_load_dwordx4 a[244:247], v1, s[44:47], s82 offen offset:3072 sc1
s_branch .LL_79%=
.p2align 12
.LL_79%=:
.Lend_39%=:
s_waitcnt vmcnt(8) lgkmcnt(0)
s_barrier
s_cmp_eq_u32 s46, 0
s_cbranch_scc1 .Llate_80%=
ds_read_b32 v11, v11 offset:59392
ds_read_b64 v[8:9], v10 offset:57344
Rv 176,3,0
Rv 180,3,1024
Rv 184,3,2048
Rv 188,3,3072
Rv 192,3,4096
Rv 196,3,5120
Rv 200,3,6144
Rv 204,3,7168
W 6
Mvvv 32,224,176
s_lshl_b32 s92, s58, 2
s_add_u32 s91, s92, s51
s_add_u32 s95, s91, 7
Mvvv 36,228,176
s_and_b32 s95, s95, 15
s_mul_i32 s95, s95, 40960
s_mul_i32 s71, s51, 20480
Mvvv 40,224,180
s_add_u32 s71, s71, 61440
s_add_u32 m0, s71, s48
s_lshl2_add_u32 s94, s48, s95
Mvvv 44,228,180
W 4
v_cmp_ne_u32 vcc, 0, v11
s_cmp_lg_u64 vcc, 0
s_cbranch_scc1 .Lredo%=
A 4,4,8
Mvvv 48,224,184
v_rcp_f32 v4, v4
Mvvv 52,228,184
Ta 128,25,10240
A 5,5,9
Mvvv 56,224,188
v_rcp_f32 v5, v5
Mvvv 60,228,188
W 3
Ta 130,25,15360
D 16,40,94,0
Mvvv 64,224,192
s_add_u32 s95, s91, 9
s_and_b32 s95, s95, 15
s_mul_i32 s95, s95, 40960
Mvvv 68,228,192
s_mul_i32 s71, s51, 10240
s_add_u32 s71, s71, 112640
s_lshl_b32 s93, s58, 12
Mvvv 72,224,196
s_add_u32 s93, s93, s50
U 32,32,4
U 33,33,4
Mvvv 76,228,196
Ta 132,25,20480
Ta 134,25,25600
W 4
Mvvv 80,224,200
U 34,34,4
U 35,35,4
Mvvv 84,228,200
U 36,36,4
U 37,37,4
Mvvv 88,224,204
Ta 136,26,10272
Ta 138,26,15392
Mvvv 92,228,204
Ta 140,26,20512
Ta 142,26,25632
W 4
Zvaa 96,128,184
U 38,38,4
U 39,39,4
Mvaa 96,132,188
K 32,32,33
K 33,34,35
Zvaa 104,132,204
D 17,40,94,1024
K 34,36,37
Mvaa 104,128,200
Ta 144,25,10304
Ta 146,25,15424
Ta 148,25,20544
Ta 150,25,25664
W 4
Zvaa 108,136,200
Ta 152,26,10336
K 35,38,39
Mvaa 108,140,204
Ta 154,26,15456
Ta 156,26,20576
Zvaa 100,140,188
buffer_store_dwordx4 v[32:35], v1, s[8:11], s93 offen offset:0 sc1
U 40,40,4
Mvaa 100,136,184
Ta 158,26,25696
W 4
Zvaa 112,144,184
U 41,41,4
U 42,42,4
Mvaa 112,148,188
U 43,43,4
U 44,44,4
Zvaa 120,148,204
U 45,45,4
U 46,46,4
Mvaa 120,144,200
Ta 128,25,10368
Ta 130,25,15488
Ta 132,25,20608
Ta 134,25,25728
W 4
Zvaa 124,152,200
U 47,47,4
K 40,40,41
Mvaa 124,156,204
D 18,40,94,2048
K 41,42,43
Zvaa 116,156,188
K 42,44,45
K 43,46,47
Mvaa 116,152,184
Ta 136,26,10400
Ta 138,26,15520
Ta 140,26,20640
Ta 142,26,25760
W 4
Zvaa 128,128,184
Ta 144,25,10432
Ta 146,25,15552
Mvaa 128,132,188
buffer_store_dwordx4 v[40:43], v1, s[8:11], s93 offen offset:1024 sc1
U 48,48,4
Zvaa 136,132,204
U 49,49,4
U 50,50,4
Mvaa 136,128,200
Ta 148,25,20672
Ta 150,25,25792
W 4
Zvaa 140,136,200
U 51,51,4
U 52,52,4
Mvaa 140,140,204
U 53,53,4
U 54,54,4
Zvaa 132,140,188
U 55,55,4
K 48,48,49
Mvaa 132,136,184
Ta 152,26,10464
Ta 154,26,15584
Ta 156,26,20704
Ta 158,26,25824
W 4
Zvaa 144,144,184
D 19,40,94,3072
K 49,50,51
Mvaa 144,148,188
s_add_u32 m0, m0, 4096
K 50,52,53
K 51,54,55
Zvaa 152,148,204
Ta 128,25,10496
Ta 130,25,15616
Mvaa 152,144,200
Ta 132,25,20736
Ta 134,25,25856
W 4
Zvaa 156,152,200
buffer_store_dwordx4 v[48:51], v1, s[8:11], s93 offen offset:2048 sc1
U 56,56,4
Mvaa 156,156,204
U 57,57,4
U 58,58,4
Zvaa 148,156,188
U 59,59,4
U 60,60,4
Mvaa 148,152,184
Ta 136,26,10528
)"
R"(Ta 138,26,15648
Ta 140,26,20768
Ta 142,26,25888
W 4
Zvaa 160,128,184
U 61,61,4
U 62,62,4
Mvaa 160,132,188
U 63,63,4
K 56,56,57
Zvaa 168,132,204
buffer_load_dwordx4 v20, s[40:43], s94 offen lds
K 57,58,59
Mvaa 168,128,200
Ta 144,25,30720
Ta 146,25,35840
Ta 148,21,0
Ta 150,21,5120
W 4
Zvaa 172,136,200
s_add_u32 m0, s71, s48
s_lshl2_add_u32 s94, s48, s95
K 58,60,61
Mvaa 172,140,204
Ta 152,26,30752
K 59,62,63
Zvaa 164,140,188
buffer_store_dwordx4 v[56:59], v1, s[8:11], s93 offen offset:3072 sc1
U 64,64,5
Mvaa 164,136,184
Ta 154,26,35872
Ta 156,22,32
Ta 158,22,5152
W 4
Mvaa 96,144,192
U 65,65,5
U 66,66,5
Mvaa 96,148,196
U 67,67,5
U 68,68,5
Mvaa 104,148,212
U 69,69,5
U 70,70,5
Mvaa 104,144,208
Ta 128,25,30784
Ta 130,25,35904
Ta 132,21,64
Ta 134,21,5184
W 4
Mvaa 108,152,208
s_lshl_b32 s66, s14, 1
U 71,71,5
K 64,64,65
Mvaa 108,156,212
s_add_u32 s66, s66, 0
D 16,40,94,0
K 65,66,67
Mvaa 100,156,196
s_cmp_eq_u32 s66, s59
s_cselect_b32 s66, 134217728, 0
K 66,68,69
K 67,70,71
Mvaa 100,152,192
Ta 136,26,30816
Ta 138,26,35936
Ta 140,22,96
Ta 142,22,5216
W 4
Mvaa 112,128,192
s_add_u32 s67, s93, 131072
Ta 144,25,30848
Ta 146,25,35968
Mvaa 112,132,196
s_add_u32 s28, s52, s66
buffer_store_dwordx4 v[64:67], v1, s[8:11], s67 offen offset:0 sc1
U 72,72,5
Mvaa 120,132,212
s_lshl_b32 s66, s14, 1
U 73,73,5
U 74,74,5
Mvaa 120,128,208
Ta 148,21,128
Ta 150,21,5248
W 4
Mvaa 124,136,208
s_add_u32 s66, s66, 1
U 75,75,5
U 76,76,5
Mvaa 124,140,212
s_cmp_eq_u32 s66, s59
s_cselect_b32 s66, 134217728, 0
U 77,77,5
U 78,78,5
Mvaa 116,140,196
s_add_u32 s29, s52, s66
U 79,79,5
K 72,72,73
Mvaa 116,136,192
Ta 152,26,30880
Ta 154,26,36000
Ta 156,22,160
Ta 158,22,5280
W 4
Mvaa 128,144,192
D 17,40,94,1024
K 73,74,75
Mvaa 128,148,196
K 74,76,77
K 75,78,79
Mvaa 136,148,212
Ta 128,25,30912
Ta 130,25,36032
Mvaa 136,144,208
Ta 132,21,192
Ta 134,21,5312
W 4
Mvaa 140,152,208
D 18,40,94,2048
D 19,40,94,3072
s_add_u32 m0, m0, 4096
s_nop 0
buffer_load_dwordx4 v20, s[40:43], s94 offen lds
Mvaa 140,156,212
s_add_u32 s81, s58, 2
s_and_b32 s81, s81, 3
s_lshl_b32 s81, s81, 12
Mvaa 132,156,196
s_add_u32 s81, s81, s49
Ta 136,26,30944
Ta 138,26,36064
Mvaa 132,152,192
Ta 140,22,224
Ta 142,22,5344
W 4
Mvaa 144,128,192
buffer_store_dwordx4 v[72:75], v1, s[8:11], s67 offen offset:1024 sc1
U 80,80,5
Mvaa 144,132,196
U 81,81,5
U 82,82,5
Mvaa 152,132,212
U 83,83,5
U 84,84,5
Mvaa 152,128,208
Ta 144,25,30976
Ta 146,25,36096
Ta 148,21,256
Ta 150,21,5376
W 4
Mvaa 156,136,208
U 85,85,5
U 86,86,5
Mvaa 156,140,212
U 87,87,5
K 80,80,81
Mvaa 148,140,196
K 81,82,83
K 82,84,85
Mvaa 148,136,192
Ta 152,26,31008
Ta 154,26,36128
Ta 156,22,288
Ta 158,22,5408
W 4
Mvaa 160,144,192
buffer_load_dwordx4 a[0:3], v1, s[8:11], s81 offen offset:0 sc1
K 83,86,87
Mvaa 160,148,196
s_add_u32 s82, s81, 131072
Ta 128,21,10240
Ta 130,21,15360
Mvaa 168,148,212
Ta 132,21,20480
Ta 134,21,25600
Mvaa 168,144,208
W 4
Ta 136,22,10272
Ta 138,22,15392
Mvaa 172,152,208
buffer_store_dwordx4 v[80:83], v1, s[8:11], s67 offen offset:2048 sc1
U 88,88,5
Mvaa 172,156,212
U 89,89,5
U 90,90,5
Mvaa 164,156,196
U 91,91,5
U 92,92,5
Mvaa 164,152,192
Ta 140,22,20512
Ta 142,22,25632
s_waitcnt vmcnt(22) lgkmcnt(4)
Mvaa 96,128,216
U 93,93,5
U 94,94,5
Mvaa 96,132,220
U 95,95,5
K 88,88,89
Mvaa 104,132,236
K 89,90,91
K 90,92,93
Mvaa 104,128,232
Ta 144,21,10304
Ta 146,21,15424
Ta 148,21,20544
Ta 150,21,25664
W 4
Mvaa 108,136,232
buffer_load_dwordx4 a[16:19], v1, s[8:11], s82 offen offset:0 sc1
K 91,94,95
Mvaa 108,140,236
Ta 152,22,10336
Ta 154,22,15456
Mvaa 100,140,220
Ta 156,22,20576
Ta 158,22,25696
Mvaa 100,136,216
W 4
Ta 128,21,10368
Ta 130,21,15488
Mvaa 112,144,216
buffer_store_dwordx4 v[88:91], v1, s[8:11], s67 offen offset:3072 sc1
Ta 132,21,20608
Mvaa 112,148,220
Ta 134,21,25728
Ta 136,22,10400
Mvaa 120,148,236
Ta 138,22,15520
Ta 140,22,20640
Mvaa 120,144,232
W 7
Ta 142,22,25760
Ta 144,21,10432
Mvaa 124,152,232
buffer_load_dwordx4 a[4:7], v1, s[8:11], s81 offen offset:1024 sc1
Ta 146,21,15552
Mvaa 124,156,236
Ta 148,21,20672
Ta 150,21,25792
Mvaa 116,156,220
Mvaa 116,152,216
W 8
Ta 152,22,10464
Ta 154,22,15584
Mvaa 128,128,216
buffer_load_dwordx4 a[20:23], v1, s[8:11], s82 offen offset:1024 sc1
Mvaa 128,132,220
Ta 156,22,20704
Ta 158,22,25824
Mvaa 136,132,236
Mvaa 136,128,232
W 8
Ta 128,21,10496
Ta 130,21,15616
Mvaa 140,136,232
s_waitcnt vmcnt(7)
s_barrier
Mvaa 140,140,236
buffer_load_dwordx4 a[8:11], v1, s[8:11], s81 offen offset:2048 sc1
Ta 132,21,20736
Mvaa 132,140,220
Ta 134,21,25856
Mvaa 132,136,216
W 8
Ta 136,22,10528
Ta 138,22,15648
Mvaa 144,144,216
Mvaa 144,148,220
buffer_load_dwordx4 a[24:27], v1, s[8:11], s82 offen offset:2048 sc1
Ta 140,22,20768
Mvaa 152,148,236
Ta 142,22,25888
Mvaa 152,144,232
W 8
Ta 144,21,30720
Ta 146,21,35840
Mvaa 156,152,232
Mvaa 156,156,236
buffer_load_dwordx4 a[12:15], v1, s[8:11], s81 offen offset:3072 sc1
Ta 148,23,0
Mvaa 148,156,220
Ta 150,23,5120
Mvaa 148,152,216
W 8
Ta 152,22,30752
Ta 154,22,35872
Mvaa 160,128,216
Mvaa 160,132,220
buffer_load_dwordx4 a[28:31], v1, s[8:11], s82 offen offset:3072 sc1
Ta 156,24,32
Mvaa 168,132,236
s_lshl_b32 s92, s58, 2
s_add_u32 s91, s92, s51
s_add_u32 s95, s91, 11
Mvaa 168,128,232
W 7
s_and_b32 s95, s95, 15
s_mul_i32 s95, s95, 40960
Mvaa 172,136,232
s_mul_i32 s71, s51, 10240
s_add_u32 s71, s71, 40960
s_add_u32 m0, s71, s48
Mvaa 172,140,236
s_lshl2_add_u32 s94, s48, s95
D 16,40,94,0
Ta 158,24,5152
Mvaa 164,140,220
s_add_u32 s95, s91, 13
s_and_b32 s95, s95, 15
s_mul_i32 s95, s95, 40960
Mvaa 164,136,216
W 4
s_mul_i32 s71, s51, 20480
s_add_u32 s71, s71, 71680
Mvaa 96,144,224
s_lshl_b32 s80, s58, 3
Ta 128,21,30784
Ta 130,21,35904
Mvaa 96,148,228
s_add_u32 s80, s80, s55
D 17,40,94,1024
Ta 132,23,64
Mvaa 104,148,244
Ta 134,23,5184
Ta 136,22,30816
Mvaa 104,144,240
W 5
Ta 138,22,35936
v_and_b32 v27, 1, v12
Mvaa 108,152,240
s_waitcnt vmcnt(8)
v_mov_b64 v[28:29], s[12:13]
s_mov_b64 exec, 1
buffer_store_dwordx2 v[28:29], off, s[8:11], s80 sc1
s_mov_b64 exec, -1
Ta 140,24,96
v_lshlrev_b32 v27, 1, v27
Mvaa 108,156,244
Ta 142,24,5216
v_xor_b32 v27, v27, v13
Mvaa 100,156,228
Ta 144,21,30848
v_and_b32 v12, 2, v12
Mvaa 100,152,224
W 5
Ta 146,21,35968
v_or_b32 v12, v12, v14
Mvaa 112,128,224
D 18,40,94,2048
v_lshl_or_b32 v12, v27, 2, v12
Mvaa 112,132,228
Ta 148,23,128
v_cmp_eq_u32_e64 s[86:87], 0, v12
Mvaa 120,132,244
Ta 150,23,5248
Ta 152,22,30880
Mvaa 120,128,240
W 5
v_cndmask_b32_e64 v4, 0, v15, s[86:87]
v_cmp_eq_u32_e64 s[86:87], 1, v12
Mvaa 124,136,240
D 19,40,94,3072
Ta 154,22,36000
Mvaa 124,140,244
s_add_u32 m0, m0, 4096
v_cndmask_b32_e64 v5, 0, v15, s[86:87]
v_cmp_eq_u32_e64 s[86:87], 2, v12
Mvaa 116,140,228
Ta 156,24,160
Ta 158,24,5280
Mvaa 116,136,224
W 4
v_cndmask_b32_e64 v6, 0, v15, s[86:87]
v_cmp_eq_u32_e64 s[86:87], 3, v12
Mvaa 128,144,224
buffer_load_dwordx4 v20, s[40:43], s94 offen lds
Ta 128,21,30912
Mvaa 128,148,228
s_add_u32 m0, s71, s48
s_lshl2_add_u32 s94, s48, s95
v_cndmask_b32_e64 v7, 0, v15, s[86:87]
Mvaa 136,148,244
Ta 130,21,36032
v_cmp_eq_u32_e64 s[86:87], 4, v12
Mvaa 136,144,240
Ta 132,23,192
Ta 134,23,5312
W 4
Mvaa 140,152,240
D 16,40,94,0
v_cndmask_b32_e64 v8, 0, v15, s[86:87]
Mvaa 140,156,244
s_add_u32 s95, s92, 15
s_and_b32 s95, s95, 15
s_mul_i32 s95, s95, 40960
Mvaa 132,156,228
s_mul_i32 s71, s51, 0
s_add_u32 s71, s71, 102400
v_cmp_eq_u32_e64 s[86:87], 5, v12
Mvaa 132,152,224
Ta 136,22,30944
)"
R"(Ta 138,22,36064
Ta 140,24,224
Ta 142,24,5344
W 4
Mvaa 144,128,224
D 17,40,94,1024
v_cndmask_b32_e64 v9, 0, v15, s[86:87]
Mvaa 144,132,228
Ta 144,21,30976
v_cmp_eq_u32_e64 s[86:87], 6, v12
Mvaa 152,132,244
Ta 146,21,36096
Ta 148,23,256
Mvaa 152,128,240
Ta 150,23,5376
W 4
Mvaa 156,136,240
D 18,40,94,2048
v_cndmask_b32_e64 v10, 0, v15, s[86:87]
Mvaa 156,140,244
Ta 152,22,31008
v_cmp_eq_u32_e64 s[86:87], 7, v12
Mvaa 148,140,228
Ta 154,22,36128
Ta 156,24,288
Mvaa 148,136,224
Ta 158,24,5408
W 4
Mvaa 160,144,224
D 19,40,94,3072
v_cndmask_b32_e64 v11, 0, v15, s[86:87]
Mvaa 160,148,228
s_add_u32 m0, m0, 4096
s_mov_b32 s20, s38
s_and_b32 s21, s39, 0xffff
Mvaa 168,148,244
s_mov_b32 s22, 2621440
s_mov_b32 s23, 0x20000
s_and_b32 s17, s17, 0xffff
Mvaa 168,144,240
Ta 128,23,20480
Ta 130,23,25600
Ta 132,23,51200
Ta 134,23,56320
W 4
Mvaa 172,152,240
s_mov_b32 s18, 2621440
s_mov_b32 s19, 0x20000
buffer_load_dwordx4 v20, s[40:43], s94 offen lds
Mvaa 172,156,244
s_add_u32 m0, s71, s48
s_lshl2_add_u32 s94, s48, s95
s_lshr_b32 s66, s15, 3
Mvaa 164,156,228
s_lshl_b32 s66, s66, 4
s_add_u32 s66, s66, s59
s_lshl_b32 s63, s66, 6
Mvaa 164,152,224
Ta 136,24,20512
Ta 138,24,25632
Ta 140,24,51232
Ta 142,24,56352
s_waitcnt vmcnt(15) lgkmcnt(4)
Mvaa 96,128,0
s_add_u32 s63, s63, 65536
s_mul_i32 s62, s66, 40960
D 16,40,94,0
Mvaa 96,132,4
s_add_u32 s62, s62, 56623104
s_lshl_b32 s66, s14, 13
s_add_u32 s78, s62, s66
Mvaa 104,132,20
s_add_u32 s79, s78, 327680
s_and_b32 s66, s14, 1
s_mul_i32 s66, s66, 327680
Mvaa 104,128,16
Ta 144,23,20544
Ta 146,23,25664
Ta 148,23,51264
Ta 150,23,56384
W 4
Mvaa 108,136,16
s_add_u32 s65, s62, s66
s_add_u32 s65, s65, 32768
D 17,40,94,1024
Mvaa 108,140,20
s_mov_b32 s64, 0
s_bfe_u32 s66, s59, 0x10001
s_lshl_b32 s67, s57, 1
Mvaa 100,140,4
s_add_u32 s66, s66, s67
s_lshl_b32 s61, s66, 2
s_lshr_b32 s66, s59, 2
Mvaa 100,136,0
Ta 152,24,20576
Ta 154,24,25696
Ta 156,24,51296
Ta 158,24,56416
W 4
Mvaa 112,144,0
s_lshl_b32 s66, s66, 1
s_add_u32 s61, s61, s66
D 18,40,94,2048
Mvaa 112,148,4
s_and_b32 s66, s59, 1
s_add_u32 s61, s61, s66
s_xor_b32 s67, s61, 0
Mvaa 120,148,20
s_mul_i32 s72, s67, 20480
s_mul_i32 s66, s58, 320
Ta 128,23,20608
Mvaa 120,144,16
Ta 130,23,25728
Ta 132,23,51328
Ta 134,23,56448
W 4
Mvaa 124,152,16
s_add_u32 s72, s72, s66
s_mul_i32 s75, s67, 20480
D 19,40,94,3072
Mvaa 124,156,20
s_add_u32 m0, m0, 4096
s_mul_i32 s66, s58, 2560
s_add_u32 s75, s75, s66
Mvaa 116,156,4
s_add_u32 s75, s75, 131072
s_xor_b32 s67, s61, 124
buffer_load_dwordx4 v20, s[40:43], s94 offen lds
Mvaa 116,152,0
Ta 136,24,20640
Ta 138,24,25760
Ta 140,24,51360
Ta 142,24,56480
W 4
Mvaa 128,128,0
s_add_u32 s81, s58, 3
s_and_b32 s81, s81, 3
s_lshl_b32 s81, s81, 12
Mvaa 128,132,4
s_add_u32 s81, s81, s49
s_mul_i32 s73, s67, 20480
s_mul_i32 s66, s58, 320
Mvaa 136,132,20
s_add_u32 s73, s73, s66
s_mul_i32 s76, s67, 20480
buffer_load_dwordx4 a[32:35], v1, s[8:11], s81 offen offset:0 sc1
Mvaa 136,128,16
Ta 144,23,20672
Ta 146,23,25792
Ta 148,23,51392
Ta 150,23,56512
W 4
Mvaa 140,136,16
s_waitcnt vmcnt(11)
s_barrier
Mvaa 140,140,20
s_add_u32 s82, s81, 131072
s_mul_i32 s66, s58, 2560
s_add_u32 s76, s76, s66
Mvaa 132,140,4
s_add_u32 s76, s76, 131072
s_bitcmp1_b32 s14, 0
s_cselect_b32 s74, s73, s72
s_cselect_b32 s77, s76, s75
buffer_load_dwordx4 a[48:51], v1, s[8:11], s82 offen offset:0 sc1
Mvaa 132,136,0
Ta 152,24,20704
Ta 154,24,25824
Ta 156,24,51424
Ta 158,24,56544
W 4
Mvaa 144,144,0
s_add_u32 s74, s74, 256
s_add_u32 s77, s77, 2048
s_lshl_b32 s66, s14, 6
Mvaa 144,148,4
s_lshl_b32 s67, s14, 9
s_add_u32 s72, s72, s66
s_add_u32 s75, s75, s67
Mvaa 152,148,20
s_add_u32 s73, s73, s66
buffer_load_dwordx4 a[36:39], v1, s[8:11], s81 offen offset:1024 sc1
Ta 128,23,20736
Mvaa 152,144,16
Ta 130,23,25856
Ta 132,23,51456
Ta 134,23,56576
W 4
Mvaa 156,152,16
s_add_u32 s76, s76, s67
v_add_u32 v19, 4096, v1
v_bfe_u32 v27, v1, 4, 3
Mvaa 156,156,20
Ta 136,24,20768
Ta 138,24,25888
Mvaa 148,156,4
buffer_load_dwordx4 a[52:55], v1, s[8:11], s82 offen offset:1024 sc1
v_lshlrev_b32 v27, 3, v27
Mvaa 148,152,0
Ta 140,24,51488
Ta 142,24,56608
W 4
Mvaa 160,128,0
Ta 144,25,0
Ta 146,25,5120
Mvaa 160,132,4
Ta 148,21,40960
v_bfe_u32 v18, v1, 7, 1
Mvaa 168,132,20
buffer_load_dwordx4 a[40:43], v1, s[8:11], s81 offen offset:2048 sc1
Ta 150,21,46080
Mvaa 168,128,16
W 4
Ta 152,26,32
v_lshl_add_u32 v18, v18, 9, v27
Mvaa 172,136,16
s_mov_b32 s71, 0
Ta 154,26,5152
Ta 156,22,40992
Mvaa 172,140,20
Ta 158,22,46112
v_bfe_u32 v20, v1, 4, 4
Mvaa 164,140,4
buffer_load_dwordx4 a[56:59], v1, s[8:11], s82 offen offset:2048 sc1
Ta 128,25,64
Mvaa 164,136,0
W 5
Ta 130,25,5184
v_mul_u32_u24 v20, 1280, v20
Mvaa 96,144,8
Ta 132,21,41024
Ta 134,21,46144
Mvaa 96,148,12
Ta 136,26,96
Ta 138,26,5216
Mvaa 104,148,28
buffer_load_dwordx4 a[44:47], v1, s[8:11], s81 offen offset:3072 sc1
v_lshrrev_b32 v12, 8, v1
Mvaa 104,144,24
W 6
Ta 140,22,41056
Ta 142,22,46176
Mvaa 108,152,24
Ta 144,25,128
v_lshl_add_u32 v20, v12, 4, v20
Mvaa 108,156,28
Ta 146,25,5248
v_bfe_u32 v15, v1, 7, 1
Mvaa 100,156,12
buffer_load_dwordx4 a[60:63], v1, s[8:11], s82 offen offset:3072 sc1
Ta 148,21,41088
Mvaa 100,152,8
W 7
Ta 150,21,46208
v_mul_u32_u24 v14, 10240, v15
Mvaa 112,128,8
Ta 152,26,160
Ta 154,26,5280
Mvaa 112,132,12
Ta 156,22,41120
v_lshrrev_b32 v15, 8, v1
Mvaa 120,132,28
Ta 158,22,46240
Mvaa 120,128,24
W 8
Ta 128,25,192
v_lshl_add_u32 v14, v15, 7, v14
Mvaa 124,136,24
Ta 130,25,5312
v_bfe_u32 v28, v1, 4, 2
Mvaa 124,140,28
Ta 132,21,41152
v_lshlrev_b32 v28, 3, v28
Mvaa 116,140,12
Ta 134,21,46272
v_and_b32 v15, 0x70, v1
Mvaa 116,136,8
W 8
buffer_load_dwordx2 v[28:29], v28, s[8:11], s55 offen sc1
Ta 136,26,224
Mvaa 128,144,8
Ta 138,26,5344
v_add_u32 v14, v14, v15
Mvaa 128,148,12
Ta 140,22,41184
Ta 142,22,46304
Mvaa 136,148,28
Mvaa 136,144,24
W 8
buffer_load_dwordx4 v[36:39], v20, s[16:19], s72 offen
Ta 144,25,256
Mvaa 140,152,24
s_waitcnt vmcnt(10)
s_barrier
Mvaa 140,156,28
Ta 146,25,5376
Ta 148,21,41216
Mvaa 132,156,12
Ta 150,21,46336
Mvaa 132,152,8
W 8
Ta 152,26,288
Ta 154,26,5408
Mvaa 144,128,8
Mvaa 144,132,12
buffer_load_dwordx4 v[44:47], v14, s[8:11], s75 offen sc1
Ta 156,22,41248
Mvaa 152,132,28
Ta 158,22,46368
Mvaa 152,128,24
W 8
Ta 128,21,51200
Ta 130,21,56320
Mvaa 156,136,24
Mvaa 156,140,28
Ta 132,23,10240
Ta 134,23,15360
Mvaa 148,140,12
Mvaa 148,136,8
W 8
Ta 136,23,30720
Ta 138,23,35840
Mvaa 160,144,8
buffer_load_dwordx4 v[52:55], v20, s[16:19], s73 offen
Mvaa 160,148,12
Ta 140,23,40960
Ta 142,23,46080
Mvaa 168,148,28
Mvaa 168,144,24
W 8
Ta 144,22,51232
Ta 146,22,56352
Mvaa 172,152,24
Mvaa 172,156,28
Ta 148,24,10272
Ta 150,24,15392
Mvaa 164,156,12
buffer_load_dwordx4 v[60:63], v14, s[8:11], s76 offen sc1
Mvaa 164,152,8
s_waitcnt vmcnt(9) lgkmcnt(8)
Ta 152,24,30752
Ta 154,24,35872
Mvaa 96,128,32
Mvaa 96,132,36
Ta 156,24,40992
Ta 158,24,46112
Mvaa 104,132,52
Mvaa 104,128,48
s_waitcnt vmcnt(5) lgkmcnt(8)
Ta 128,21,51264
Ta 130,21,56384
Mvaa 104,136,56
Mvaa 104,140,60
buffer_load_dwordx4 v[68:71], v20, s[16:19], s74 offen
Ta 132,23,10304
Mvaa 96,140,44
Ta 134,23,15424
Mvaa 96,136,40
W 8
Ta 136,23,30784
Ta 138,23,35904
Mvaa 100,144,32
Mvaa 100,148,36
Ta 140,23,41024
Ta 142,23,46144
Mvaa 108,148,52
Mvaa 108,144,48
W 8
Ta 144,22,51296
Ta 146,22,56416
Mvaa 108,152,56
buffer_load_dwordx4 v[76:79], v14, s[8:11], s77 offen sc1
Mvaa 108,156,60
Ta 148,24,10336
Ta 150,24,15456
Mvaa 100,156,44
s_waitcnt vmcnt(6)
v_cmp_eq_u64 vcc, s[12:13], v[28:29]
s_cmp_eq_u64 vcc, -1
s_cbranch_scc0 .Lspin_82%=
.Lok_83%=:
s_add_u32 s81, s58, 1
s_and_b32 s81, s81, 3
Mvaa 100,152,40
W 8
s_lshl_b32 s81, s81, 12
s_add_u32 s81, s81, s50
Mvaa 112,128,32
buffer_load_dwordx4 a[64:67], v1, s[8:11], s81 offen offset:0 sc1
Ta 152,24,30816
Mvaa 112,132,36
s_add_u32 s82, s81, 131072
Ta 154,24,35936
Ta 156,24,41056
Mvaa 120,132,52
Ta 158,24,46176
K 96,96,97
Mvaa 120,128,48
W 8
Ta 128,21,51328
)"
R"(K 97,98,99
Mvaa 120,136,56
buffer_load_dwordx4 a[80:83], v1, s[8:11], s82 offen offset:0 sc1
Ta 130,21,56448
Mvaa 120,140,60
Ta 132,23,10368
K 98,100,101
Mvaa 112,140,44
Ta 134,23,15488
Mvaa 112,136,40
W 8
Ta 136,23,30848
K 99,102,103
Mvaa 116,144,32
buffer_load_dwordx2 v[12:13], v27, s[8:11], 0 offen offset:3584 sc1
Ta 138,23,35968
Mvaa 116,148,36
s_add_u32 s83, s28, 0
Ta 140,23,41088
Ta 142,23,46208
Mvaa 124,148,52
Mvaa 124,144,48
W 8
Ta 144,22,51360
Ta 146,22,56480
Mvaa 124,152,56
buffer_load_dwordx4 a[68:71], v1, s[8:11], s81 offen offset:1024 sc1
Mvaa 124,156,60
Ta 148,24,10400
Ta 150,24,15520
Mvaa 116,156,44
Mvaa 116,152,40
W 8
Ta 152,24,30880
Ta 154,24,36000
Mvaa 128,128,32
buffer_load_dwordx4 a[84:87], v1, s[8:11], s82 offen offset:1024 sc1
Mvaa 128,132,36
Ta 156,24,41120
Ta 158,24,46240
Mvaa 136,132,52
Mvaa 136,128,48
W 8
Ta 128,21,51392
Ta 130,21,56512
Mvaa 136,136,56
buffer_store_dwordx4 v[96:99], v1, s[8:11], s83 offen sc1
K 104,104,105
Mvaa 136,140,60
K 105,106,107
K 106,108,109
Mvaa 128,140,44
Ta 132,23,10432
K 107,110,111
Mvaa 128,136,40
W 7
s_add_u32 s83, s29, 40960
Ta 134,23,15552
Mvaa 132,144,32
Ta 136,23,30912
Ta 138,23,36032
Mvaa 132,148,36
Ta 140,23,41152
Ta 142,23,46272
Mvaa 140,148,52
Mvaa 140,144,48
W 8
Ta 144,22,51424
Ta 146,22,56544
Mvaa 140,152,56
Mvaa 140,156,60
Ta 148,24,10464
Ta 150,24,15584
Mvaa 132,156,44
Mvaa 132,152,40
W 8
buffer_load_dwordx4 a[72:75], v1, s[8:11], s81 offen offset:2048 sc1
Ta 152,24,30944
Mvaa 144,128,32
Ta 154,24,36064
Mvaa 144,132,36
Ta 156,24,41184
Ta 158,24,46304
Mvaa 152,132,52
Mvaa 152,128,48
W 8
buffer_load_dwordx4 a[88:91], v1, s[8:11], s82 offen offset:2048 sc1
Ta 128,21,51456
Mvaa 152,136,56
Ta 130,21,56576
Mvaa 152,140,60
Ta 132,23,10496
Ta 134,23,15616
Mvaa 144,140,44
Mvaa 144,136,40
W 8
buffer_load_dwordx4 a[76:79], v1, s[8:11], s81 offen offset:3072 sc1
Ta 136,23,30976
Mvaa 148,144,32
Ta 138,23,36096
Mvaa 148,148,36
Ta 140,23,41216
Ta 142,23,46336
Mvaa 156,148,52
Mvaa 156,144,48
W 8
buffer_store_dwordx4 v[104:107], v1, s[8:11], s83 offen sc1
K 112,112,113
Mvaa 156,152,56
K 113,114,115
K 114,116,117
Mvaa 156,156,60
Ta 144,22,51488
K 115,118,119
Mvaa 148,156,44
s_add_u32 s83, s28, 8192
Ta 146,22,56608
Ta 148,24,10528
Mvaa 148,152,40
W 7
Ta 150,24,15648
Ta 152,24,31008
Mvaa 160,128,32
Ta 154,24,36128
Mvaa 160,132,36
Ta 156,24,41248
Ta 158,24,46368
Mvaa 168,132,52
Mvaa 168,128,48
W 8
Ta 128,25,10240
Ta 130,25,15360
Mvaa 168,136,56
Mvaa 168,140,60
Ta 132,25,20480
Ta 134,25,25600
Mvaa 160,140,44
buffer_load_dwordx4 a[92:95], v1, s[8:11], s82 offen offset:3072 sc1
Mvaa 160,136,40
W 8
Ta 136,26,10272
Ta 138,26,15392
Mvaa 164,144,32
Mvaa 164,148,36
Ta 140,26,20512
Ta 142,26,25632
Mvaa 172,148,52
buffer_store_dwordx4 v[112:115], v1, s[8:11], s83 offen sc1
K 120,120,121
Mvaa 172,144,48
W 8
K 121,122,123
K 122,124,125
Mvaa 172,152,56
Ta 144,25,10304
K 123,126,127
Mvaa 172,156,60
s_add_u32 s83, s29, 49152
Ta 146,25,15424
Ta 148,25,20544
Mvaa 164,156,44
Ta 150,25,25664
Mvaa 164,152,40
W 8
Ta 152,26,10336
Ta 154,26,15456
Zvav 176,128,32
Mvav 176,132,40
Ta 156,26,20576
Ta 158,26,25696
Zvav 184,132,72
Mvav 184,128,64
W 8
Ta 128,25,10368
Ta 130,25,15488
Zvav 188,136,64
Mvav 188,140,72
buffer_store_dwordx4 v[120:123], v1, s[8:11], s83 offen sc1
K 128,128,129
Zvav 180,140,40
K 129,130,131
K 130,132,133
Mvav 180,136,32
W 6
Ta 132,25,20608
K 131,134,135
Zvav 192,144,32
s_add_u32 s83, s28, 16384
Ta 134,25,25728
Ta 136,26,10400
Mvav 192,148,40
Ta 138,26,15520
Ta 140,26,20640
Zvav 200,148,72
Ta 142,26,25760
Mvav 200,144,64
W 8
Ta 144,25,10432
Ta 146,25,15552
Zvav 204,152,64
Mvav 204,156,72
Ta 148,25,20672
Ta 150,25,25792
Zvav 196,156,40
Mvav 196,152,32
W 8
Ta 152,26,10464
Ta 154,26,15584
Zvav 208,128,32
s_add_u32 s81, s58, 2
buffer_store_dwordx4 v[128:131], v1, s[8:11], s83 offen sc1
K 136,136,137
Mvav 208,132,40
s_and_b32 s81, s81, 3
K 137,138,139
K 138,140,141
Zvav 216,132,72
s_lshl_b32 s81, s81, 12
s_add_u32 s81, s81, s50
K 139,142,143
Mvav 216,128,64
W 6
s_add_u32 s83, s29, 57344
Ta 156,26,20704
Zvav 220,136,64
Ta 158,26,25824
Ta 128,25,10496
Mvav 220,140,72
Ta 130,25,15616
Ta 132,25,20736
Zvav 212,140,40
Ta 134,25,25856
Mvav 212,136,32
W 8
Ta 136,26,10528
Ta 138,26,15648
Zvav 224,144,32
Mvav 224,148,40
Ta 140,26,20768
Ta 142,26,25888
Zvav 232,148,72
Mvav 232,144,64
W 8
buffer_store_dwordx4 v[136:139], v1, s[8:11], s83 offen sc1
K 144,144,145
Zvav 236,152,64
K 145,146,147
K 146,148,149
Mvav 236,156,72
Ta 144,25,30720
K 147,150,151
Zvav 228,156,40
s_add_u32 s83, s28, 24576
Ta 146,25,35840
Ta 148,21,0
Mvav 228,152,32
W 7
Ta 150,21,5120
Ta 152,26,30752
Zvav 240,128,32
Ta 154,26,35872
Mvav 240,132,40
Ta 156,22,32
Ta 158,22,5152
Zvav 248,132,72
Mvav 248,128,64
W 8
Ta 128,25,30784
Ta 130,25,35904
Zvav 252,136,64
Mvav 252,140,72
Ta 132,21,64
Ta 134,21,5184
Zvav 244,140,40
buffer_store_dwordx4 v[144:147], v1, s[8:11], s83 offen sc1
K 152,152,153
Mvav 244,136,32
W 8
K 153,154,155
K 154,156,157
Mvav 176,144,48
Ta 136,26,30816
K 155,158,159
Mvav 176,148,56
s_add_u32 s83, s29, 65536
Ta 138,26,35936
Ta 140,22,96
Mvav 184,148,88
Ta 142,22,5216
Mvav 184,144,80
W 8
Ta 144,25,30848
Ta 146,25,35968
Mvav 188,152,80
Mvav 188,156,88
Ta 148,21,128
Ta 150,21,5248
Mvav 180,156,56
Mvav 180,152,48
W 8
Ta 152,26,30880
Ta 154,26,36000
Mvav 192,128,48
Mvav 192,132,56
buffer_load_dwordx4 a[96:99], v1, s[8:11], s81 offen offset:0 sc1
Ta 156,22,160
Mvav 200,132,88
s_add_u32 s82, s81, 131072
Ta 158,22,5280
Mvav 200,128,80
W 8
Ta 128,25,30912
Ta 130,25,36032
Mvav 204,136,80
Mvav 204,140,88
buffer_store_dwordx4 v[152:155], v1, s[8:11], s83 offen sc1
K 160,160,161
Mvav 196,140,56
K 161,162,163
K 162,164,165
Mvav 196,136,48
W 6
Ta 132,21,192
K 163,166,167
Mvav 208,144,48
s_add_u32 s83, s28, 32768
Ta 134,21,5312
Ta 136,26,30944
Mvav 208,148,56
Ta 138,26,36064
Ta 140,22,224
Mvav 216,148,88
Ta 142,22,5344
Mvav 216,144,80
W 8
Ta 144,25,30976
Ta 146,25,36096
Mvav 220,152,80
Mvav 220,156,88
Ta 148,21,256
Ta 150,21,5376
Mvav 212,156,56
Mvav 212,152,48
W 8
Ta 152,26,31008
Ta 154,26,36128
Mvav 224,128,48
buffer_load_dwordx4 a[112:115], v1, s[8:11], s82 offen offset:0 sc1
Mvav 224,132,56
Ta 156,22,288
Ta 158,22,5408
Mvav 232,132,88
Mvav 232,128,80
W 8
Ta 128,21,10240
Ta 130,21,15360
Mvav 236,136,80
buffer_load_dwordx4 a[100:103], v1, s[8:11], s81 offen offset:1024 sc1
Mvav 236,140,88
Ta 132,21,20480
Ta 134,21,25600
Mvav 228,140,56
Mvav 228,136,48
W 8
Ta 136,22,10272
Ta 138,22,15392
Mvav 240,144,48
buffer_load_dwordx4 a[116:119], v1, s[8:11], s82 offen offset:1024 sc1
Mvav 240,148,56
Ta 140,22,20512
Ta 142,22,25632
Mvav 248,148,88
Mvav 248,144,80
W 8
Ta 144,21,10304
Ta 146,21,15424
Mvav 252,152,80
buffer_store_dwordx4 v[160:163], v1, s[8:11], s83 offen sc1
K 168,168,169
Mvav 252,156,88
K 169,170,171
K 170,172,173
Mvav 244,156,56
Ta 148,21,20544
K 171,174,175
Mvav 244,152,48
s_waitcnt vmcnt(17) lgkmcnt(7)
s_add_u32 s83, s29, 73728
Ta 150,21,25664
Mvaa 176,128,64
Ta 152,22,10336
Ta 154,22,15456
Mvaa 176,132,68
Ta 156,22,20576
Ta 158,22,25696
Mvaa 184,132,84
Mvaa 184,128,80
W 8
Ta 128,21,10368
Ta 130,21,15488
)"
R"(Mvaa 188,136,80
Mvaa 188,140,84
Ta 132,21,20608
Ta 134,21,25728
Mvaa 180,140,68
Mvaa 180,136,64
W 8
buffer_load_dwordx4 a[104:107], v1, s[8:11], s81 offen offset:2048 sc1
Ta 136,22,10400
Mvaa 192,144,64
Ta 138,22,15520
Mvaa 192,148,68
Ta 140,22,20640
Ta 142,22,25760
Mvaa 200,148,84
Mvaa 200,144,80
W 8
buffer_load_dwordx4 a[120:123], v1, s[8:11], s82 offen offset:2048 sc1
Ta 144,21,10432
Mvaa 204,152,80
Ta 146,21,15552
Mvaa 204,156,84
Ta 148,21,20672
Ta 150,21,25792
Mvaa 196,156,68
Mvaa 196,152,64
W 8
buffer_load_dwordx4 a[108:111], v1, s[8:11], s81 offen offset:3072 sc1
Ta 152,22,10464
Mvaa 208,128,64
Ta 154,22,15584
Mvaa 208,132,68
Ta 156,22,20704
Ta 158,22,25824
Mvaa 216,132,84
Mvaa 216,128,80
W 8
buffer_store_dwordx4 v[168:171], v1, s[8:11], s83 offen sc1
Ta 128,21,10496
Mvaa 220,136,80
Ta 130,21,15616
Mvaa 220,140,84
Ta 132,21,20736
Ta 134,21,25856
Mvaa 212,140,68
Mvaa 212,136,64
W 8
Ta 136,22,10528
Ta 138,22,15648
Mvaa 224,144,64
Mvaa 224,148,68
Ta 140,22,20768
Ta 142,22,25888
Mvaa 232,148,84
Mvaa 232,144,80
W 8
Ta 144,21,30720
Ta 146,21,35840
Mvaa 236,152,80
Mvaa 236,156,84
Ta 148,23,0
Ta 150,23,5120
Mvaa 228,156,68
buffer_load_dwordx4 a[124:127], v1, s[8:11], s82 offen offset:3072 sc1
Mvaa 228,152,64
W 8
s_add_u32 s81, s58, 3
s_and_b32 s81, s81, 3
Mvaa 240,128,64
s_lshl_b32 s81, s81, 12
s_add_u32 s81, s81, s50
Ta 152,22,30752
Mvaa 240,132,68
Ta 154,22,35872
Ta 156,24,32
Mvaa 248,132,84
buffer_load_dwordx4 a[184:187], v1, s[8:11], s81 offen offset:0 sc1
Ta 158,24,5152
Mvaa 248,128,80
W 8
s_add_u32 s82, s81, 131072
Ta 128,21,30784
Mvaa 252,136,80
Ta 130,21,35904
Mvaa 252,140,84
Ta 132,23,64
Ta 134,23,5184
Mvaa 244,140,68
buffer_load_dwordx4 a[200:203], v1, s[8:11], s82 offen offset:0 sc1
Mvaa 244,136,64
s_waitcnt vmcnt(18) lgkmcnt(8)
Ta 136,22,30816
Ta 138,22,35936
Mvaa 176,144,72
Mvaa 176,148,76
Ta 140,24,96
Ta 142,24,5216
Mvaa 184,148,92
buffer_load_dwordx4 a[188:191], v1, s[8:11], s81 offen offset:1024 sc1
Mvaa 184,144,88
W 8
Ta 144,21,30848
Ta 146,21,35968
Mvaa 188,152,88
Mvaa 188,156,92
Ta 148,23,128
Ta 150,23,5248
Mvaa 180,156,76
buffer_load_dwordx4 a[204:207], v1, s[8:11], s82 offen offset:1024 sc1
Mvaa 180,152,72
W 8
Ta 152,22,30880
Ta 154,22,36000
Mvaa 192,128,72
Mvaa 192,132,76
Ta 156,24,160
Ta 158,24,5280
Mvaa 200,132,92
buffer_load_dwordx4 a[192:195], v1, s[8:11], s81 offen offset:2048 sc1
Mvaa 200,128,88
W 8
Ta 128,21,30912
Ta 130,21,36032
Mvaa 204,136,88
Mvaa 204,140,92
Ta 132,23,192
Ta 134,23,5312
Mvaa 196,140,76
buffer_load_dwordx4 a[208:211], v1, s[8:11], s82 offen offset:2048 sc1
Mvaa 196,136,72
W 8
Ta 136,22,30944
Ta 138,22,36064
Mvaa 208,144,72
Mvaa 208,148,76
Ta 140,24,224
Ta 142,24,5344
Mvaa 216,148,92
buffer_load_dwordx4 a[196:199], v1, s[8:11], s81 offen offset:3072 sc1
Mvaa 216,144,88
W 8
Ta 144,21,30976
Ta 146,21,36096
Mvaa 220,152,88
Mvaa 220,156,92
Ta 148,23,256
Ta 150,23,5376
Mvaa 212,156,76
buffer_load_dwordx4 a[212:215], v1, s[8:11], s82 offen offset:3072 sc1
Mvaa 212,152,72
W 8
Ta 152,22,31008
Ta 154,22,36128
Mvaa 224,128,72
Mvaa 224,132,76
Ta 156,24,288
Ta 158,24,5408
Mvaa 232,132,92
Mvaa 232,128,88
W 8
Ta 128,23,20480
Ta 130,23,25600
Mvaa 236,136,88
Mvaa 236,140,92
Ta 132,23,51200
Ta 134,23,56320
Mvaa 228,140,76
Mvaa 228,136,72
W 8
Ta 136,25,0
Ta 138,25,5120
Mvaa 240,144,72
Mvaa 240,148,76
Ta 140,21,40960
Ta 142,21,46080
Mvaa 248,148,92
Mvaa 248,144,88
W 8
Ta 144,21,51200
Ta 146,21,56320
Mvaa 252,152,88
Mvaa 252,156,92
Ta 148,23,10240
Ta 150,23,15360
Mvaa 244,156,76
Mvaa 244,152,72
s_waitcnt vmcnt(14) lgkmcnt(8)
Ta 152,23,30720
Ta 154,23,35840
Mvaa 176,128,96
s_waitcnt vmcnt(9)
v_lshlrev_b32 v2, 2, v1
v_mov_b64 v[28:29], s[12:13]
s_mov_b64 exec, 3
buffer_store_dwordx2 v[28:29], v2, s[8:11], s53 offen offset:0 sc1
s_mov_b64 exec, -1
Mvaa 176,132,100
Ta 156,23,40960
Ta 158,23,46080
Mvaa 184,132,116
Mvaa 184,128,112
s_waitcnt vmcnt(9) lgkmcnt(8)
Ta 128,24,20512
Ta 130,24,25632
Mvaa 184,136,120
Mvaa 184,140,124
v_xor_b32 v12, s12, v12
v_xor_b32 v13, s13, v13
v_lshrrev_b32 v3, 11, v12
v_or_b32 v13, v13, v3
v_cmp_eq_u32 vcc, 0, v13
s_cmp_eq_u64 vcc, -1
s_cbranch_scc0 .LL_87%=
.LL_88%=:
v_bfe_u32 v13, v12, 4, 7
s_xor_b32 s66, s61, 124
v_cmp_eq_u32 vcc, s61, v13
v_cmp_eq_u32_e64 s[86:87], s66, v13
s_or_b32 s68, vcc_lo, s86
s_and_b32 s68, s68, 255
s_cbranch_scc1 .Lup_85%=
.Lupr_86%=:
Ta 132,24,51232
Ta 134,24,56352
Mvaa 176,140,108
Mvaa 176,136,104
s_waitcnt vmcnt(5) lgkmcnt(8)
Ta 136,26,32
v_lshlrev_b32 v32, 16, v36
Mvaa 176,144,184
Ta 138,26,5152
v_and_b32 v33, 0xffff0000, v36
Mvaa 176,148,188
Ta 140,22,40992
v_lshlrev_b32 v34, 16, v37
Mvaa 184,148,204
Ta 142,22,46112
v_and_b32 v35, 0xffff0000, v37
Mvaa 184,144,200
s_waitcnt vmcnt(1) lgkmcnt(8)
Ta 144,22,51232
v_lshlrev_b32 v36, 16, v38
Mvaa 184,152,208
Ta 146,22,56352
v_and_b32 v37, 0xffff0000, v38
Mvaa 184,156,212
Ta 148,24,10272
v_lshlrev_b32 v38, 16, v39
Mvaa 176,156,196
Ta 150,24,15392
v_and_b32 v39, 0xffff0000, v39
Mvaa 176,152,192
W 8
Ta 152,24,30752
v_lshlrev_b32 v40, 16, v44
Mvaa 180,128,96
Ta 154,24,35872
v_and_b32 v41, 0xffff0000, v44
Mvaa 180,132,100
Ta 156,24,40992
v_lshlrev_b32 v42, 16, v45
Mvaa 188,132,116
Ta 158,24,46112
v_and_b32 v43, 0xffff0000, v45
Mvaa 188,128,112
W 8
Ta 128,23,20544
v_lshlrev_b32 v44, 16, v46
Mvaa 188,136,120
Ta 130,23,25664
v_and_b32 v45, 0xffff0000, v46
Mvaa 188,140,124
buffer_load_dwordx2 v[16:17], v18, s[8:11], s63 offen sc1
v_lshlrev_b32 v46, 16, v47
Mvaa 180,140,108
Ta 132,23,51264
Ta 134,23,56384
Mvaa 180,136,104
W 8
v_and_b32 v47, 0xffff0000, v47
v_lshlrev_b32 v48, 16, v52
Mvaa 180,144,184
Ta 136,25,64
Ta 138,25,5184
Mvaa 180,148,188
v_and_b32 v49, 0xffff0000, v52
v_lshlrev_b32 v50, 16, v53
Mvaa 188,148,204
Ta 140,21,41024
Ta 142,21,46144
Mvaa 188,144,200
W 8
v_and_b32 v51, 0xffff0000, v53
v_lshlrev_b32 v52, 16, v54
Mvaa 188,152,208
Ta 144,21,51264
Ta 146,21,56384
Mvaa 188,156,212
v_and_b32 v53, 0xffff0000, v54
v_lshlrev_b32 v54, 16, v55
Mvaa 180,156,196
Ta 148,23,10304
Ta 150,23,15424
Mvaa 180,152,192
W 8
v_and_b32 v55, 0xffff0000, v55
v_lshlrev_b32 v56, 16, v60
Mvaa 192,128,96
Ta 152,23,30784
Ta 154,23,35904
Mvaa 192,132,100
v_and_b32 v57, 0xffff0000, v60
v_lshlrev_b32 v58, 16, v61
Mvaa 200,132,116
Ta 156,23,41024
Ta 158,23,46144
Mvaa 200,128,112
W 8
v_and_b32 v59, 0xffff0000, v61
K 176,176,177
Mvaa 200,136,120
Ta 128,24,20576
v_lshlrev_b32 v60, 16, v62
Mvaa 200,140,124
Ta 130,24,25696
K 177,178,179
Mvaa 192,140,108
v_and_b32 v61, 0xffff0000, v62
v_lshlrev_b32 v62, 16, v63
Mvaa 192,136,104
W 6
Ta 132,24,51296
K 178,180,181
Mvaa 192,144,184
Ta 134,24,56416
v_and_b32 v63, 0xffff0000, v63
Mvaa 192,148,188
K 179,182,183
v_lshlrev_b32 v64, 16, v68
Mvaa 200,148,204
s_add_u32 s83, s28, 327680
Ta 136,26,96
Ta 138,26,5216
Mvaa 200,144,200
W 6
buffer_store_dwordx4 v[176:179], v1, s[8:11], s83 offen sc1
v_and_b32 v65, 0xffff0000, v68
Mvaa 200,152,208
K 184,184,185
K 185,186,187
Mvaa 200,156,212
K 186,188,189
K 187,190,191
Mvaa 192,156,196
s_add_u32 s83, s29, 368640
buffer_store_dwordx4 v[184:187], v1, s[8:11], s83 offen sc1
v_lshlrev_b32 v66, 16, v69
Mvaa 192,152,192
Ta 140,22,41056
Ta 142,22,46176
W 4
Mvaa 196,128,96
v_and_b32 v67, 0xffff0000, v69
v_lshlrev_b32 v68, 16, v70
Mvaa 196,132,100
Ta 144,22,51296
Ta 146,22,56416
Mvaa 204,132,116
v_and_b32 v69, 0xffff0000, v70
v_lshlrev_b32 v70, 16, v71
Mvaa 204,128,112
Ta 148,24,10336
Ta 150,24,15456
W 4
Mvaa 204,136,120
v_and_b32 v71, 0xffff0000, v71
v_lshlrev_b32 v72, 16, v76
Mvaa 204,140,124
Ta 152,24,30816
Ta 154,24,35936
Mvaa 196,140,108
v_and_b32 v73, 0xffff0000, v76
v_lshlrev_b32 v74, 16, v77
Mvaa 196,136,104
Ta 156,24,41056
Ta 158,24,46176
W 4
Mvaa 196,144,184
v_and_b32 v75, 0xffff0000, v77
v_lshlrev_b32 v76, 16, v78
Mvaa 196,148,188
Ta 128,23,20608
Ta 130,23,25728
Mvaa 204,148,204
v_and_b32 v77, 0xffff0000, v78
v_lshlrev_b32 v78, 16, v79
Mvaa 204,144,200
Ta 132,23,51328
Ta 134,23,56448
W 4
)"
R"(Mvaa 204,152,208
Ta 136,25,128
v_and_b32 v79, 0xffff0000, v79
Mvaa 204,156,212
Ta 138,25,5248
Ta 140,21,41088
Mvaa 196,156,196
Ta 142,21,46208
Ta 144,21,51328
Mvaa 196,152,192
W 5
Ta 146,21,56448
Ta 148,23,10368
Mvaa 208,128,96
s_waitcnt vmcnt(2)
v_cmp_eq_u64 vcc, s[12:13], v[16:17]
s_and_b32 s71, vcc_lo, 255
s_bitset0_b32 s71, s59
Ta 150,23,15488
Ta 152,23,30848
Mvaa 208,132,100
s_lshl_b32 s92, s14, 3
s_lshr_b32 s92, s71, s92
s_mov_b32 s91, 134217728
Mvaa 216,132,116
s_bitcmp1_b32 s71, 0
s_cselect_b32 s94, s78, s91
Ta 154,23,35968
Mvaa 216,128,112
W 6
Ta 156,23,41088
Ta 158,23,46208
Mvaa 216,136,120
Ta 128,24,20640
K 192,192,193
Mvaa 216,140,124
Ta 130,24,25760
K 193,194,195
Mvaa 208,140,108
buffer_load_dwordx4 a[0:3], v1, s[8:11], s94 offen offset:0 sc1
K 194,196,197
Mvaa 208,136,104
W 6
Ta 132,24,51360
Ta 134,24,56480
Mvaa 208,144,184
Ta 136,26,160
K 195,198,199
Mvaa 208,148,188
s_add_u32 s83, s28, 335872
Ta 138,26,5280
Ta 140,22,41120
Mvaa 216,148,204
buffer_store_dwordx4 v[192:195], v1, s[8:11], s83 offen sc1
K 200,200,201
Mvaa 216,144,200
W 7
K 201,202,203
K 202,204,205
Mvaa 216,152,208
Ta 142,22,46240
K 203,206,207
Mvaa 216,156,212
s_add_u32 s83, s29, 376832
Ta 144,22,51360
Ta 146,22,56480
Mvaa 208,156,196
buffer_store_dwordx4 v[200:203], v1, s[8:11], s83 offen sc1
Ta 148,24,10400
Mvaa 208,152,192
W 7
Ta 150,24,15520
Ta 152,24,30880
Mvaa 212,128,96
Ta 154,24,36000
Mvaa 212,132,100
Ta 156,24,41120
Ta 158,24,46240
Mvaa 220,132,116
s_bitcmp1_b32 s92, 0
s_cbranch_scc0 .LL_90%=
buffer_load_dwordx4 a[216:219], v1, s[8:11], s65 offen offset:0 sc1
.LL_90%=:
Mvaa 220,128,112
W 8
s_bitcmp1_b32 s71, 1
s_cselect_b32 s94, s78, s91
Mvaa 220,136,120
Ta 128,23,20672
Ta 130,23,25792
Mvaa 220,140,124
Ta 132,23,51392
Ta 134,23,56512
Mvaa 212,140,108
buffer_load_dwordx4 a[4:7], v1, s[8:11], s94 offen offset:1024 sc1
Mvaa 212,136,104
W 8
Ta 136,25,192
Ta 138,25,5312
Mvaa 212,144,184
Mvaa 212,148,188
Ta 140,21,41152
Ta 142,21,46272
Mvaa 220,148,204
s_bitcmp1_b32 s92, 1
s_cbranch_scc0 .LL_91%=
buffer_load_dwordx4 a[220:223], v1, s[8:11], s65 offen offset:1024 sc1
.LL_91%=:
Mvaa 220,144,200
W 8
s_bitcmp1_b32 s71, 2
s_cselect_b32 s94, s78, s91
Mvaa 220,152,208
Ta 144,21,51392
Ta 146,21,56512
Mvaa 220,156,212
Ta 148,23,10432
Ta 150,23,15552
Mvaa 212,156,196
buffer_load_dwordx4 a[8:11], v1, s[8:11], s94 offen offset:2048 sc1
Mvaa 212,152,192
W 8
Ta 152,23,30912
Ta 154,23,36032
Mvaa 224,128,96
Mvaa 224,132,100
Ta 156,23,41152
Ta 158,23,46272
Mvaa 232,132,116
s_bitcmp1_b32 s92, 2
s_cbranch_scc0 .LL_92%=
buffer_load_dwordx4 a[224:227], v1, s[8:11], s65 offen offset:2048 sc1
.LL_92%=:
K 208,208,209
Mvaa 232,128,112
W 8
s_bitcmp1_b32 s71, 3
s_cselect_b32 s94, s78, s91
Mvaa 232,136,120
Ta 128,24,20704
Ta 130,24,25824
Mvaa 232,140,124
K 209,210,211
K 210,212,213
Mvaa 224,140,108
Ta 132,24,51424
Ta 134,24,56544
Mvaa 224,136,104
W 8
buffer_load_dwordx4 a[12:15], v1, s[8:11], s94 offen offset:3072 sc1
K 211,214,215
Mvaa 224,144,184
s_add_u32 s83, s28, 344064
Ta 136,26,224
Ta 138,26,5344
Mvaa 224,148,188
Ta 140,22,41184
Ta 142,22,46304
Mvaa 232,148,204
Mvaa 232,144,200
W 8
buffer_store_dwordx4 v[208:211], v1, s[8:11], s83 offen sc1
K 216,216,217
Mvaa 232,152,208
K 217,218,219
K 218,220,221
Mvaa 232,156,212
Ta 144,22,51424
K 219,222,223
Mvaa 224,156,196
s_add_u32 s83, s29, 385024
buffer_store_dwordx4 v[216:219], v1, s[8:11], s83 offen sc1
Ta 146,22,56544
Mvaa 224,152,192
W 6
Ta 148,24,10464
Ta 150,24,15584
Mvaa 228,128,96
Ta 152,24,30944
Ta 154,24,36064
Mvaa 228,132,100
Ta 156,24,41184
Ta 158,24,46304
Mvaa 236,132,116
s_bitcmp1_b32 s92, 3
s_cbranch_scc0 .LL_93%=
buffer_load_dwordx4 a[228:231], v1, s[8:11], s65 offen offset:3072 sc1
.LL_93%=:
Mvaa 236,128,112
W 8
s_bitcmp1_b32 s71, 4
s_cselect_b32 s94, s78, s91
Mvaa 236,136,120
Ta 128,23,20736
Ta 130,23,25856
Mvaa 236,140,124
Ta 132,23,51456
Ta 134,23,56576
Mvaa 228,140,108
buffer_load_dwordx4 a[16:19], v19, s[8:11], s94 offen offset:0 sc1
Mvaa 228,136,104
W 8
Ta 136,25,256
Ta 138,25,5376
Mvaa 228,144,184
Mvaa 228,148,188
Ta 140,21,41216
Ta 142,21,46336
Mvaa 236,148,204
s_bitcmp1_b32 s92, 4
s_cbranch_scc0 .LL_94%=
buffer_load_dwordx4 a[232:235], v19, s[8:11], s65 offen offset:0 sc1
.LL_94%=:
Mvaa 236,144,200
W 8
s_bitcmp1_b32 s71, 5
s_cselect_b32 s94, s78, s91
Mvaa 236,152,208
Ta 144,21,51456
Ta 146,21,56576
Mvaa 236,156,212
Ta 148,23,10496
Ta 150,23,15616
Mvaa 228,156,196
buffer_load_dwordx4 a[20:23], v19, s[8:11], s94 offen offset:1024 sc1
Mvaa 228,152,192
W 8
Ta 152,23,30976
Ta 154,23,36096
Mvaa 240,128,96
Mvaa 240,132,100
Ta 156,23,41216
Ta 158,23,46336
Mvaa 248,132,116
s_bitcmp1_b32 s92, 5
s_cbranch_scc0 .LL_95%=
buffer_load_dwordx4 a[236:239], v19, s[8:11], s65 offen offset:1024 sc1
.LL_95%=:
K 224,224,225
Mvaa 248,128,112
W 8
s_bitcmp1_b32 s71, 6
s_cselect_b32 s94, s78, s91
Mvaa 248,136,120
Ta 128,24,20768
Ta 130,24,25888
Mvaa 248,140,124
K 225,226,227
K 226,228,229
Mvaa 240,140,108
Ta 132,24,51488
Ta 134,24,56608
Mvaa 240,136,104
W 8
Ta 136,26,288
K 227,230,231
Mvaa 240,144,184
s_add_u32 s83, s28, 352256
Ta 138,26,5408
buffer_load_dwordx4 a[24:27], v19, s[8:11], s94 offen offset:2048 sc1
Mvaa 240,148,188
Ta 140,22,41248
Ta 142,22,46368
Mvaa 248,148,204
Mvaa 248,144,200
W 8
Ta 144,22,51488
Ta 146,22,56608
Mvaa 248,152,208
buffer_store_dwordx4 v[224:227], v1, s[8:11], s83 offen sc1
K 232,232,233
Mvaa 248,156,212
K 233,234,235
K 234,236,237
Mvaa 240,156,196
K 235,238,239
s_add_u32 s83, s29, 393216
buffer_store_dwordx4 v[232:235], v1, s[8:11], s83 offen sc1
Mvaa 240,152,192
W 6
Ta 148,24,10528
Ta 150,24,15648
Mvaa 244,128,96
Ta 152,24,31008
Ta 154,24,36128
Mvaa 244,132,100
Ta 156,24,41248
Ta 158,24,46368
Mvaa 252,132,116
s_bitcmp1_b32 s92, 6
s_cbranch_scc0 .LL_96%=
buffer_load_dwordx4 a[240:243], v19, s[8:11], s65 offen offset:2048 sc1
.LL_96%=:
Mvaa 252,128,112
W 8
s_bitcmp1_b32 s71, 7
s_cselect_b32 s94, s78, s91
Mvaa 252,136,120
Mvaa 252,140,124
Mvaa 244,140,108
buffer_load_dwordx4 a[28:31], v19, s[8:11], s94 offen offset:3072 sc1
Mvaa 244,136,104
W 4
Mvaa 244,144,184
Mvaa 244,148,188
Mvaa 252,148,204
s_bitcmp1_b32 s92, 7
s_cbranch_scc0 .LL_97%=
buffer_load_dwordx4 a[244:247], v19, s[8:11], s65 offen offset:3072 sc1
.LL_97%=:
Mvaa 252,144,200
W 0
Mvaa 252,152,208
Mvaa 252,156,212
Mvaa 244,156,196
Mvaa 244,152,192
K 240,240,241
K 241,242,243
s_nop 5
K 242,244,245
K 243,246,247
s_add_u32 s83, s28, 360448
buffer_store_dwordx4 v[240:243], v1, s[8:11], s83 offen sc1
K 248,248,249
K 249,250,251
K 250,252,253
K 251,254,255
s_add_u32 s83, s29, 401408
buffer_store_dwordx4 v[248:251], v1, s[8:11], s83 offen sc1
s_branch .Ltend_81%=
.p2align 12
.Llate_80%=:
ds_read_b32 v11, v11 offset:59392
ds_read_b64 v[8:9], v10 offset:57344
Rv 176,3,0
Rv 180,3,1024
Rv 184,3,2048
Rv 188,3,3072
Rv 192,3,4096
Rv 196,3,5120
Rv 200,3,6144
Rv 204,3,7168
W 6
Mvvv 32,224,176
s_lshl_b32 s92, s58, 2
s_add_u32 s91, s92, s51
s_add_u32 s95, s91, 7
Mvvv 36,228,176
s_and_b32 s95, s95, 15
s_mul_i32 s95, s95, 40960
s_mul_i32 s71, s51, 20480
Mvvv 40,224,180
s_add_u32 s71, s71, 61440
s_add_u32 m0, s71, s48
s_lshl2_add_u32 s94, s48, s95
Mvvv 44,228,180
W 4
v_cmp_ne_u32 vcc, 0, v11
s_cmp_lg_u64 vcc, 0
s_cbranch_scc1 .Lredo%=
A 4,4,8
Mvvv 48,224,184
v_rcp_f32 v4, v4
Mvvv 52,228,184
Ta 128,25,10240
A 5,5,9
Mvvv 56,224,188
v_rcp_f32 v5, v5
Mvvv 60,228,188
W 3
Ta 130,25,15360
D 16,40,94,0
Mvvv 64,224,192
s_add_u32 s95, s91, 9
s_and_b32 s95, s95, 15
s_mul_i32 s95, s95, 40960
Mvvv 68,228,192
s_mul_i32 s71, s51, 10240
s_add_u32 s71, s71, 112640
s_lshl_b32 s93, s58, 12
Mvvv 72,224,196
s_add_u32 s93, s93, s50
U 32,32,4
U 33,33,4
Mvvv 76,228,196
Ta 132,25,20480
Ta 134,25,25600
W 4
Mvvv 80,224,200
U 34,34,4
U 35,35,4
Mvvv 84,228,200
U 36,36,4
U 37,37,4
Mvvv 88,224,204
U 38,38,4
U 39,39,4
K 32,32,33
K 33,34,35
K 34,36,37
)"
R"(K 35,38,39
buffer_store_dwordx4 v[32:35], v1, s[8:11], s93 offen offset:0 sc1
U 40,40,4
U 41,41,4
U 42,42,4
U 43,43,4
U 44,44,4
U 45,45,4
U 46,46,4
U 47,47,4
K 40,40,41
K 41,42,43
K 42,44,45
K 43,46,47
buffer_store_dwordx4 v[40:43], v1, s[8:11], s93 offen offset:1024 sc1
U 64,64,5
U 65,65,5
U 66,66,5
U 67,67,5
U 68,68,5
U 69,69,5
U 70,70,5
U 71,71,5
K 64,64,65
K 65,66,67
K 66,68,69
K 67,70,71
s_add_u32 s67, s93, 131072
buffer_store_dwordx4 v[64:67], v1, s[8:11], s67 offen offset:0 sc1
U 72,72,5
U 73,73,5
U 74,74,5
U 75,75,5
U 76,76,5
U 77,77,5
U 78,78,5
U 79,79,5
K 72,72,73
K 73,74,75
K 74,76,77
K 75,78,79
Mvvv 92,228,204
Ta 136,26,10272
Ta 138,26,15392
Ta 140,26,20512
Ta 142,26,25632
W 4
Zvav 176,128,32
Ta 144,25,10304
Ta 146,25,15424
Mvav 176,132,40
Ta 148,25,20544
Ta 150,25,25664
Zvav 184,132,72
D 17,40,94,1024
Ta 152,26,10336
Mvav 184,128,64
W 5
Ta 154,26,15456
Ta 156,26,20576
Zvav 188,136,64
Ta 158,26,25696
Ta 128,25,10368
Mvav 188,140,72
Ta 130,25,15488
Ta 132,25,20608
Zvav 180,140,40
D 18,40,94,2048
Ta 134,25,25728
Mvav 180,136,32
W 8
Ta 136,26,10400
Ta 138,26,15520
Zvav 192,144,32
Mvav 192,148,40
Ta 140,26,20640
Ta 142,26,25760
Zvav 200,148,72
D 19,40,94,3072
Mvav 200,144,64
W 8
s_add_u32 m0, m0, 4096
Ta 144,25,10432
Zvav 204,152,64
Ta 146,25,15552
Mvav 204,156,72
Ta 148,25,20672
Ta 150,25,25792
Zvav 196,156,40
buffer_load_dwordx4 v20, s[40:43], s94 offen lds
Mvav 196,152,32
W 8
s_add_u32 m0, s71, s48
s_lshl2_add_u32 s94, s48, s95
Zvav 208,128,32
Ta 152,26,10464
Ta 154,26,15584
Mvav 208,132,40
Ta 156,26,20704
Ta 158,26,25824
Zvav 216,132,72
D 16,40,94,0
v_bfe_u32 v28, v1, 4, 2
Mvav 216,128,64
W 8
Ta 128,25,10496
v_lshlrev_b32 v28, 3, v28
Zvav 220,136,64
Ta 130,25,15616
Mvav 220,140,72
Ta 132,25,20736
Ta 134,25,25856
Zvav 212,140,40
buffer_load_dwordx2 v[28:29], v28, s[8:11], s54 offen sc1
Mvav 212,136,32
W 8
Ta 136,26,10528
Ta 138,26,15648
Zvav 224,144,32
Mvav 224,148,40
Ta 140,26,20768
Ta 142,26,25888
Zvav 232,148,72
D 17,40,94,1024
Mvav 232,144,64
W 8
Ta 144,25,30720
Ta 146,25,35840
Zvav 236,152,64
Mvav 236,156,72
Ta 148,21,0
Ta 150,21,5120
Zvav 228,156,40
buffer_store_dwordx4 v[72:75], v1, s[8:11], s67 offen offset:1024 sc1
U 48,48,4
Mvav 228,152,32
W 8
U 49,49,4
U 50,50,4
Zvav 240,128,32
U 51,51,4
U 52,52,4
Mvav 240,132,40
U 53,53,4
U 54,54,4
Zvav 248,132,72
D 18,40,94,2048
U 55,55,4
Mvav 248,128,64
W 4
K 48,48,49
K 49,50,51
Zvav 252,136,64
K 50,52,53
K 51,54,55
Mvav 252,140,72
Ta 152,26,30752
Ta 154,26,35872
Zvav 244,140,40
buffer_store_dwordx4 v[48:51], v1, s[8:11], s93 offen offset:2048 sc1
U 56,56,4
U 57,57,4
U 58,58,4
U 59,59,4
U 60,60,4
U 61,61,4
U 62,62,4
U 63,63,4
K 56,56,57
K 57,58,59
K 58,60,61
K 59,62,63
buffer_store_dwordx4 v[56:59], v1, s[8:11], s93 offen offset:3072 sc1
U 80,80,5
U 81,81,5
U 82,82,5
U 83,83,5
U 84,84,5
U 85,85,5
U 86,86,5
U 87,87,5
K 80,80,81
K 81,82,83
K 82,84,85
K 83,86,87
buffer_store_dwordx4 v[80:83], v1, s[8:11], s67 offen offset:2048 sc1
U 88,88,5
U 89,89,5
U 90,90,5
U 91,91,5
U 92,92,5
U 93,93,5
U 94,94,5
U 95,95,5
K 88,88,89
K 89,90,91
K 90,92,93
K 91,94,95
Mvav 244,136,32
Ta 156,22,32
Ta 158,22,5152
W 4
Mvav 176,144,48
Ta 128,25,30784
Ta 130,25,35904
Mvav 176,148,56
Ta 132,21,64
Ta 134,21,5184
Mvav 184,148,88
D 19,40,94,3072
Ta 136,26,30816
Mvav 184,144,80
s_add_u32 m0, m0, 4096
s_nop 0
buffer_load_dwordx4 v20, s[40:43], s94 offen lds
s_waitcnt vmcnt(8)
v_cmp_eq_u64 vcc, s[12:13], v[28:29]
s_cmp_eq_u64 vcc, -1
s_cbranch_scc0 .Lspin_98%=
.Lok_99%=:
W 5
Mvav 188,152,80
s_add_u32 s81, s58, 0
s_and_b32 s81, s81, 3
s_lshl_b32 s81, s81, 12
Mvav 188,156,88
s_add_u32 s81, s81, s49
Ta 138,26,35936
Ta 140,22,96
Mvav 180,156,56
s_lshl_b32 s66, s14, 1
Ta 142,22,5216
Ta 144,25,30848
Mvav 180,152,48
W 5
buffer_store_dwordx4 v[88:91], v1, s[8:11], s67 offen offset:3072 sc1
Ta 146,25,35968
Mvav 192,128,48
s_add_u32 s66, s66, 0
Ta 148,21,128
Ta 150,21,5248
Mvav 192,132,56
s_cmp_eq_u32 s66, s59
s_cselect_b32 s66, 134217728, 0
Ta 152,26,30880
Ta 154,26,36000
Mvav 200,132,88
s_add_u32 s28, s52, s66
Ta 156,22,160
Ta 158,22,5280
Mvav 200,128,80
W 8
s_lshl_b32 s66, s14, 1
buffer_load_dwordx4 a[184:187], v1, s[8:11], s81 offen offset:0 sc1
Mvav 204,136,80
s_add_u32 s82, s81, 131072
Ta 128,25,30912
Ta 130,25,36032
Mvav 204,140,88
s_add_u32 s66, s66, 1
s_cmp_eq_u32 s66, s59
s_cselect_b32 s66, 134217728, 0
Ta 132,21,192
Mvav 196,140,56
s_add_u32 s29, s52, s66
Ta 134,21,5312
Mvav 196,136,48
W 8
buffer_load_dwordx4 a[200:203], v1, s[8:11], s82 offen offset:0 sc1
Ta 136,26,30944
Mvav 208,144,48
Ta 138,26,36064
Mvav 208,148,56
Ta 140,22,224
Ta 142,22,5344
Mvav 216,148,88
Mvav 216,144,80
W 8
buffer_load_dwordx4 a[188:191], v1, s[8:11], s81 offen offset:1024 sc1
Ta 144,25,30976
Mvav 220,152,80
Ta 146,25,36096
Mvav 220,156,88
Ta 148,21,256
Ta 150,21,5376
Mvav 212,156,56
Mvav 212,152,48
W 8
buffer_load_dwordx4 a[204:207], v1, s[8:11], s82 offen offset:1024 sc1
Ta 152,26,31008
Mvav 224,128,48
Ta 154,26,36128
Mvav 224,132,56
Ta 156,22,288
Ta 158,22,5408
Mvav 232,132,88
Mvav 232,128,80
W 8
buffer_load_dwordx4 a[192:195], v1, s[8:11], s81 offen offset:2048 sc1
Ta 128,25,10240
Mvav 236,136,80
Ta 130,25,15360
Mvav 236,140,88
Ta 132,25,20480
Ta 134,25,25600
Mvav 228,140,56
Mvav 228,136,48
W 8
buffer_load_dwordx4 a[208:211], v1, s[8:11], s82 offen offset:2048 sc1
Ta 136,26,10272
Mvav 240,144,48
Ta 138,26,15392
Mvav 240,148,56
Ta 140,26,20512
Ta 142,26,25632
Mvav 248,148,88
Mvav 248,144,80
W 8
buffer_load_dwordx4 a[196:199], v1, s[8:11], s81 offen offset:3072 sc1
Ta 144,25,10304
Mvav 252,152,80
Ta 146,25,15424
Mvav 252,156,88
Ta 148,25,20544
Ta 150,25,25664
Mvav 244,156,56
Mvav 244,152,48
s_waitcnt vmcnt(3) lgkmcnt(8)
buffer_load_dwordx4 a[212:215], v1, s[8:11], s82 offen offset:3072 sc1
Ta 152,26,10336
Zvaa 96,128,184
Ta 154,26,15456
Mvaa 96,132,188
Ta 156,26,20576
Ta 158,26,25696
Zvaa 104,132,204
Mvaa 104,128,200
W 8
Ta 128,25,10368
Ta 130,25,15488
Zvaa 108,136,200
Mvaa 108,140,204
Ta 132,25,20608
Ta 134,25,25728
Zvaa 100,140,188
s_lshl_b32 s92, s58, 2
s_add_u32 s91, s92, s51
s_add_u32 s95, s91, 11
Mvaa 100,136,184
W 8
s_and_b32 s95, s95, 15
s_mul_i32 s95, s95, 40960
Zvaa 112,144,184
s_mul_i32 s71, s51, 10240
s_add_u32 s71, s71, 40960
s_add_u32 m0, s71, s48
Mvaa 112,148,188
s_lshl2_add_u32 s94, s48, s95
Ta 136,26,10400
Ta 138,26,15520
Zvaa 120,148,204
Ta 140,26,20640
Ta 142,26,25760
Mvaa 120,144,200
W 8
Ta 144,25,10432
Ta 146,25,15552
Zvaa 124,152,200
Mvaa 124,156,204
Ta 148,25,20672
Ta 150,25,25792
Zvaa 116,156,188
Mvaa 116,152,184
W 8
Ta 152,26,10464
Ta 154,26,15584
Zvaa 128,128,184
s_lshl_b32 s80, s58, 3
s_add_u32 s80, s80, s55
v_mov_b64 v[28:29], s[12:13]
s_mov_b64 exec, 1
buffer_store_dwordx2 v[28:29], off, s[8:11], s80 sc1
s_mov_b64 exec, -1
Mvaa 128,132,188
Ta 156,26,20704
v_and_b32 v27, 1, v12
Zvaa 136,132,204
Ta 158,26,25824
v_lshlrev_b32 v27, 1, v27
Mvaa 136,128,200
W 8
Ta 128,25,10496
v_xor_b32 v27, v27, v13
Zvaa 140,136,200
s_barrier
Mvaa 140,140,204
Ta 130,25,15616
v_and_b32 v12, 2, v12
Zvaa 132,140,188
D 16,40,94,0
)"
R"(v_or_b32 v12, v12, v14
Mvaa 132,136,184
W 6
s_add_u32 s95, s91, 13
s_and_b32 s95, s95, 15
Zvaa 144,144,184
s_mul_i32 s95, s95, 40960
s_mul_i32 s71, s51, 20480
s_add_u32 s71, s71, 71680
Mvaa 144,148,188
Ta 132,25,20736
Ta 134,25,25856
Zvaa 152,148,204
v_lshl_or_b32 v12, v27, 2, v12
v_cmp_eq_u32_e64 s[86:87], 0, v12
Mvaa 152,144,200
W 4
Ta 136,26,10528
Ta 138,26,15648
Zvaa 156,152,200
D 17,40,94,1024
v_cndmask_b32_e64 v4, 0, v15, s[86:87]
Mvaa 156,156,204
Ta 140,26,20768
v_cmp_eq_u32_e64 s[86:87], 1, v12
Zvaa 148,156,188
Ta 142,26,25888
Ta 144,25,30720
Mvaa 148,152,184
W 5
v_cndmask_b32_e64 v5, 0, v15, s[86:87]
v_cmp_eq_u32_e64 s[86:87], 2, v12
Zvaa 160,128,184
Ta 146,25,35840
D 18,40,94,2048
Mvaa 160,132,188
v_cndmask_b32_e64 v6, 0, v15, s[86:87]
v_cmp_eq_u32_e64 s[86:87], 3, v12
Zvaa 168,132,204
Ta 148,21,0
Ta 150,21,5120
Mvaa 168,128,200
W 4
Ta 152,26,30752
v_cndmask_b32_e64 v7, 0, v15, s[86:87]
Zvaa 172,136,200
Ta 154,26,35872
v_cmp_eq_u32_e64 s[86:87], 4, v12
Mvaa 172,140,204
D 19,40,94,3072
Ta 156,22,32
Zvaa 164,140,188
s_add_u32 m0, m0, 4096
Ta 158,22,5152
v_cndmask_b32_e64 v8, 0, v15, s[86:87]
Mvaa 164,136,184
s_waitcnt vmcnt(5) lgkmcnt(4)
Ta 128,25,30784
v_cmp_eq_u32_e64 s[86:87], 5, v12
Mvaa 96,144,192
Ta 130,25,35904
Ta 132,21,64
Mvaa 96,148,196
v_cndmask_b32_e64 v9, 0, v15, s[86:87]
v_cmp_eq_u32_e64 s[86:87], 6, v12
Mvaa 104,148,212
buffer_load_dwordx4 v20, s[40:43], s94 offen lds
Ta 134,21,5184
Mvaa 104,144,208
W 4
s_add_u32 s81, s58, 1
s_and_b32 s81, s81, 3
Mvaa 108,152,208
s_lshl_b32 s81, s81, 12
s_add_u32 s81, s81, s49
v_cndmask_b32_e64 v10, 0, v15, s[86:87]
Mvaa 108,156,212
Ta 136,26,30816
v_cmp_eq_u32_e64 s[86:87], 7, v12
Mvaa 100,156,196
Ta 138,26,35936
buffer_load_dwordx4 a[216:219], v1, s[8:11], s81 offen offset:0 sc1
Mvaa 100,152,192
Ta 140,22,96
Ta 142,22,5216
W 4
Mvaa 112,128,192
s_add_u32 s82, s81, 131072
Ta 144,25,30848
v_cndmask_b32_e64 v11, 0, v15, s[86:87]
Mvaa 112,132,196
s_mov_b32 s20, s38
s_and_b32 s21, s39, 0xffff
s_mov_b32 s22, 2621440
Mvaa 120,132,212
s_mov_b32 s23, 0x20000
buffer_load_dwordx4 a[232:235], v1, s[8:11], s82 offen offset:0 sc1
Ta 146,25,35968
Mvaa 120,128,208
Ta 148,21,128
Ta 150,21,5248
W 4
Mvaa 124,136,208
s_and_b32 s17, s17, 0xffff
s_mov_b32 s18, 2621440
s_mov_b32 s19, 0x20000
Mvaa 124,140,212
s_lshr_b32 s66, s15, 3
Ta 152,26,30880
Ta 154,26,36000
Mvaa 116,140,196
s_lshl_b32 s66, s66, 4
buffer_load_dwordx4 a[220:223], v1, s[8:11], s81 offen offset:1024 sc1
Ta 156,22,160
Mvaa 116,136,192
s_add_u32 s66, s66, s59
Ta 158,22,5280
W 4
Mvaa 128,144,192
s_lshl_b32 s63, s66, 6
Ta 128,25,30912
Ta 130,25,36032
Mvaa 128,148,196
s_add_u32 s63, s63, 65536
s_mul_i32 s62, s66, 40960
Ta 132,21,192
Mvaa 136,148,212
s_add_u32 s62, s62, 56623104
buffer_load_dwordx4 a[236:239], v1, s[8:11], s82 offen offset:1024 sc1
Ta 134,21,5312
Mvaa 136,144,208
W 4
s_lshl_b32 s66, s14, 13
Ta 136,26,30944
Mvaa 140,152,208
s_add_u32 s78, s62, s66
s_add_u32 s79, s78, 327680
Ta 138,26,36064
Mvaa 140,156,212
s_and_b32 s66, s14, 1
Ta 140,22,224
Ta 142,22,5344
Mvaa 132,156,196
s_mul_i32 s66, s66, 327680
buffer_load_dwordx4 a[224:227], v1, s[8:11], s81 offen offset:2048 sc1
Ta 144,25,30976
Mvaa 132,152,192
W 5
s_add_u32 s65, s62, s66
Ta 146,25,36096
Mvaa 144,128,192
s_add_u32 s65, s65, 32768
Ta 148,21,256
Ta 150,21,5376
Mvaa 144,132,196
s_mov_b32 s64, 0
Ta 152,26,31008
Ta 154,26,36128
Mvaa 152,132,212
s_bfe_u32 s66, s59, 0x10001
s_lshl_b32 s67, s57, 1
buffer_load_dwordx4 a[240:243], v1, s[8:11], s82 offen offset:2048 sc1
Mvaa 152,128,208
W 6
Ta 156,22,288
Ta 158,22,5408
Mvaa 156,136,208
s_add_u32 s66, s66, s67
s_lshl_b32 s61, s66, 2
Ta 128,21,10240
Mvaa 156,140,212
s_lshr_b32 s66, s59, 2
s_lshl_b32 s66, s66, 1
Ta 130,21,15360
Mvaa 148,140,196
s_add_u32 s61, s61, s66
buffer_load_dwordx4 a[228:231], v1, s[8:11], s81 offen offset:3072 sc1
Ta 132,21,20480
Mvaa 148,136,192
W 7
s_and_b32 s66, s59, 1
Ta 134,21,25600
Mvaa 160,144,192
s_add_u32 s61, s61, s66
Ta 136,22,10272
Ta 138,22,15392
Mvaa 160,148,196
Ta 140,22,20512
Ta 142,22,25632
Mvaa 168,148,212
s_xor_b32 s67, s61, 0
buffer_load_dwordx4 a[244:247], v1, s[8:11], s82 offen offset:3072 sc1
Mvaa 168,144,208
W 8
s_add_u32 m0, s71, s48
s_lshl2_add_u32 s94, s48, s95
Mvaa 172,152,208
s_mul_i32 s72, s67, 20480
Ta 144,21,10304
Ta 146,21,15424
Mvaa 172,156,212
s_mul_i32 s66, s58, 320
s_add_u32 s72, s72, s66
Ta 148,21,20544
Mvaa 164,156,196
s_mul_i32 s75, s67, 20480
D 16,40,94,0
Ta 150,21,25664
Mvaa 164,152,192
s_waitcnt vmcnt(5) lgkmcnt(8)
s_add_u32 s95, s92, 15
s_and_b32 s95, s95, 15
Mvaa 96,128,216
s_mul_i32 s95, s95, 40960
s_mul_i32 s71, s51, 0
s_add_u32 s71, s71, 102400
Mvaa 96,132,220
s_mul_i32 s66, s58, 2560
Ta 152,22,10336
Ta 154,22,15456
Mvaa 104,132,236
s_add_u32 s75, s75, s66
s_add_u32 s75, s75, 131072
D 17,40,94,1024
Mvaa 104,128,232
W 6
Ta 156,22,20576
Ta 158,22,25696
Mvaa 108,136,232
s_xor_b32 s67, s61, 124
s_mul_i32 s73, s67, 20480
Ta 128,21,10368
Mvaa 108,140,236
s_mul_i32 s66, s58, 320
s_add_u32 s73, s73, s66
Ta 130,21,15488
Mvaa 100,140,220
s_mul_i32 s76, s67, 20480
D 18,40,94,2048
Ta 132,21,20608
Mvaa 100,136,216
W 7
Ta 134,21,25728
Ta 136,22,10400
Mvaa 112,144,216
s_mul_i32 s66, s58, 2560
Ta 138,22,15520
Mvaa 112,148,220
s_add_u32 s76, s76, s66
Ta 140,22,20640
Ta 142,22,25760
Mvaa 120,148,236
s_add_u32 s76, s76, 131072
D 19,40,94,3072
Mvaa 120,144,232
W 8
s_add_u32 m0, m0, 4096
s_bitcmp1_b32 s14, 0
s_cselect_b32 s74, s73, s72
s_cselect_b32 s77, s76, s75
Mvaa 124,152,232
s_add_u32 s74, s74, 256
Ta 144,21,10432
Ta 146,21,15552
Mvaa 124,156,236
s_add_u32 s77, s77, 2048
Ta 148,21,20672
Ta 150,21,25792
Mvaa 116,156,220
buffer_load_dwordx4 v20, s[40:43], s94 offen lds
s_add_u32 m0, s71, s48
s_lshl2_add_u32 s94, s48, s95
D 16,40,94,0
D 17,40,94,1024
D 18,40,94,2048
D 19,40,94,3072
s_add_u32 m0, m0, 4096
s_nop 0
buffer_load_dwordx4 v20, s[40:43], s94 offen lds
Mvaa 116,152,216
W 8
s_add_u32 s81, s58, 2
s_and_b32 s81, s81, 3
Mvaa 128,128,216
s_lshl_b32 s81, s81, 12
s_add_u32 s81, s81, s49
s_lshl_b32 s66, s14, 6
Mvaa 128,132,220
s_lshl_b32 s67, s14, 9
Ta 152,22,10464
Ta 154,22,15584
Mvaa 136,132,236
s_add_u32 s72, s72, s66
s_add_u32 s75, s75, s67
Ta 156,22,20704
Mvaa 136,128,232
W 7
s_add_u32 s73, s73, s66
Ta 158,22,25824
Mvaa 140,136,232
s_barrier
Mvaa 140,140,236
s_add_u32 s76, s76, s67
Ta 128,21,10496
Ta 130,21,15616
Mvaa 132,140,220
buffer_load_dwordx4 a[0:3], v1, s[8:11], s81 offen offset:0 sc1
v_add_u32 v19, 4096, v1
Mvaa 132,136,216
W 6
s_add_u32 s82, s81, 131072
Ta 132,21,20736
Mvaa 144,144,216
Ta 134,21,25856
Ta 136,22,10528
Mvaa 144,148,220
Ta 138,22,15648
Ta 140,22,20768
Mvaa 152,148,236
buffer_load_dwordx4 a[16:19], v1, s[8:11], s82 offen offset:0 sc1
Ta 142,22,25888
Mvaa 152,144,232
W 8
Ta 144,21,30720
Ta 146,21,35840
Mvaa 156,152,232
Mvaa 156,156,236
Ta 148,23,0
Ta 150,23,5120
Mvaa 148,156,220
buffer_load_dwordx4 a[4:7], v1, s[8:11], s81 offen offset:1024 sc1
Mvaa 148,152,216
W 8
Ta 152,22,30752
Ta 154,22,35872
Mvaa 160,128,216
Mvaa 160,132,220
Ta 156,24,32
Ta 158,24,5152
Mvaa 168,132,236
buffer_load_dwordx4 a[20:23], v1, s[8:11], s82 offen offset:1024 sc1
Mvaa 168,128,232
W 8
Ta 128,21,30784
v_bfe_u32 v27, v1, 4, 3
Mvaa 172,136,232
Ta 130,21,35904
Mvaa 172,140,236
Ta 132,23,64
Ta 134,23,5184
Mvaa 164,140,220
buffer_load_dwordx4 a[8:11], v1, s[8:11], s81 offen offset:2048 sc1
Mvaa 164,136,216
s_waitcnt vmcnt(15) lgkmcnt(8)
Ta 136,22,30816
Ta 138,22,35936
Mvaa 96,144,224
Mvaa 96,148,228
Ta 140,24,96
Ta 142,24,5216
Mvaa 104,148,244
buffer_load_dwordx4 a[24:27], v1, s[8:11], s82 offen offset:2048 sc1
Mvaa 104,144,240
W 8
Ta 144,21,30848
Ta 146,21,35968
Mvaa 108,152,240
Mvaa 108,156,244
Ta 148,23,128
Ta 150,23,5248
Mvaa 100,156,228
buffer_load_dwordx4 a[12:15], v1, s[8:11], s81 offen offset:3072 sc1
Mvaa 100,152,224
W 8
Ta 152,22,30880
Ta 154,22,36000
Mvaa 112,128,224
Mvaa 112,132,228
Ta 156,24,160
v_lshlrev_b32 v27, 3, v27
Mvaa 120,132,244
buffer_load_dwordx4 a[28:31], v1, s[8:11], s82 offen offset:3072 sc1
Ta 158,24,5280
Mvaa 120,128,240
W 8
s_add_u32 s81, s58, 3
s_and_b32 s81, s81, 3
Mvaa 124,136,240
s_lshl_b32 s81, s81, 12
s_add_u32 s81, s81, s49
Ta 128,21,30912
Mvaa 124,140,244
Ta 130,21,36032
Ta 132,23,192
Mvaa 116,140,228
buffer_load_dwordx4 a[32:35], v1, s[8:11], s81 offen offset:0 sc1
Ta 134,23,5312
Mvaa 116,136,224
W 8
s_add_u32 s82, s81, 131072
)"
R"(Ta 136,22,30944
Mvaa 128,144,224
Ta 138,22,36064
Mvaa 128,148,228
Ta 140,24,224
Ta 142,24,5344
Mvaa 136,148,244
buffer_load_dwordx4 a[48:51], v1, s[8:11], s82 offen offset:0 sc1
Mvaa 136,144,240
W 8
Ta 144,21,30976
Ta 146,21,36096
Mvaa 140,152,240
s_waitcnt vmcnt(10)
s_barrier
Mvaa 140,156,244
Ta 148,23,256
Ta 150,23,5376
Mvaa 132,156,228
buffer_load_dwordx4 a[36:39], v1, s[8:11], s81 offen offset:1024 sc1
Mvaa 132,152,224
W 8
Ta 152,22,31008
Ta 154,22,36128
Mvaa 144,128,224
v_bfe_u32 v18, v1, 7, 1
Mvaa 144,132,228
Ta 156,24,288
Ta 158,24,5408
Mvaa 152,132,244
buffer_load_dwordx4 a[52:55], v1, s[8:11], s82 offen offset:1024 sc1
Mvaa 152,128,240
W 8
Ta 128,23,20480
Ta 130,23,25600
Mvaa 156,136,240
Mvaa 156,140,244
Ta 132,23,51200
Ta 134,23,56320
Mvaa 148,140,228
buffer_load_dwordx4 a[40:43], v1, s[8:11], s81 offen offset:2048 sc1
Mvaa 148,136,224
W 8
Ta 136,24,20512
Ta 138,24,25632
Mvaa 160,144,224
Mvaa 160,148,228
Ta 140,24,51232
Ta 142,24,56352
Mvaa 168,148,244
buffer_load_dwordx4 a[56:59], v1, s[8:11], s82 offen offset:2048 sc1
Mvaa 168,144,240
W 8
Ta 144,23,20544
Ta 146,23,25664
Mvaa 172,152,240
Mvaa 172,156,244
Ta 148,23,51264
Ta 150,23,56384
Mvaa 164,156,228
buffer_load_dwordx4 a[44:47], v1, s[8:11], s81 offen offset:3072 sc1
v_lshl_add_u32 v18, v18, 9, v27
Mvaa 164,152,224
s_waitcnt vmcnt(11) lgkmcnt(8)
Ta 152,24,20576
Ta 154,24,25696
Mvaa 96,128,0
Mvaa 96,132,4
Ta 156,24,51296
Ta 158,24,56416
Mvaa 104,132,20
buffer_load_dwordx4 a[60:63], v1, s[8:11], s82 offen offset:3072 sc1
Mvaa 104,128,16
W 8
Ta 128,23,20608
Ta 130,23,25728
Mvaa 108,136,16
Mvaa 108,140,20
Ta 132,23,51328
Ta 134,23,56448
Mvaa 100,140,4
Mvaa 100,136,0
W 8
Ta 136,24,20640
Ta 138,24,25760
Mvaa 112,144,0
v_bfe_u32 v28, v1, 4, 2
Mvaa 112,148,4
Ta 140,24,51360
v_lshlrev_b32 v28, 3, v28
Mvaa 120,148,20
Ta 142,24,56480
buffer_load_dwordx2 v[28:29], v28, s[8:11], s55 offen sc1
Mvaa 120,144,16
W 8
Ta 144,23,20672
Ta 146,23,25792
Mvaa 124,152,16
s_mov_b32 s71, 0
Mvaa 124,156,20
Ta 148,23,51392
Ta 150,23,56512
Mvaa 116,156,4
Mvaa 116,152,0
W 8
Ta 152,24,20704
Ta 154,24,25824
Mvaa 128,128,0
Mvaa 128,132,4
Ta 156,24,51424
Ta 158,24,56544
Mvaa 136,132,20
Mvaa 136,128,16
W 8
Ta 128,23,20736
Ta 130,23,25856
Mvaa 140,136,16
Mvaa 140,140,20
Ta 132,23,51456
Ta 134,23,56576
Mvaa 132,140,4
Mvaa 132,136,0
W 8
Ta 136,24,20768
Ta 138,24,25888
Mvaa 144,144,0
Mvaa 144,148,4
Ta 140,24,51488
Ta 142,24,56608
Mvaa 152,148,20
Mvaa 152,144,16
W 8
Ta 144,25,0
v_bfe_u32 v20, v1, 4, 4
Mvaa 156,152,16
Ta 146,25,5120
Mvaa 156,156,20
Ta 148,21,40960
Ta 150,21,46080
Mvaa 148,156,4
Mvaa 148,152,0
W 8
Ta 152,26,32
Ta 154,26,5152
Mvaa 160,128,0
Mvaa 160,132,4
Ta 156,22,40992
Ta 158,22,46112
Mvaa 168,132,20
Mvaa 168,128,16
W 8
Ta 128,25,64
Ta 130,25,5184
Mvaa 172,136,16
Mvaa 172,140,20
Ta 132,21,41024
Ta 134,21,46144
Mvaa 164,140,4
Mvaa 164,136,0
s_waitcnt vmcnt(9) lgkmcnt(8)
Ta 136,26,96
Ta 138,26,5216
Mvaa 96,144,8
Mvaa 96,148,12
Ta 140,22,41056
v_mul_u32_u24 v20, 1280, v20
Mvaa 104,148,28
Ta 142,22,46176
Mvaa 104,144,24
W 8
Ta 144,25,128
Ta 146,25,5248
Mvaa 108,152,24
Mvaa 108,156,28
Ta 148,21,41088
Ta 150,21,46208
Mvaa 100,156,12
Mvaa 100,152,8
W 8
Ta 152,26,160
Ta 154,26,5280
Mvaa 112,128,8
Mvaa 112,132,12
Ta 156,22,41120
Ta 158,22,46240
Mvaa 120,132,28
s_waitcnt vmcnt(0)
v_cmp_eq_u64 vcc, s[12:13], v[28:29]
s_cmp_eq_u64 vcc, -1
s_cbranch_scc0 .Lspin_101%=
.Lok_102%=:
s_add_u32 s81, s58, 1
s_and_b32 s81, s81, 3
Mvaa 120,128,24
W 8
s_lshl_b32 s81, s81, 12
s_add_u32 s81, s81, s50
Mvaa 124,136,24
buffer_load_dwordx4 a[64:67], v1, s[8:11], s81 offen offset:0 sc1
Ta 128,25,192
Mvaa 124,140,28
s_add_u32 s82, s81, 131072
Ta 130,25,5312
Ta 132,21,41152
Mvaa 116,140,12
Ta 134,21,46272
Mvaa 116,136,8
W 8
Ta 136,26,224
Ta 138,26,5344
Mvaa 128,144,8
buffer_load_dwordx4 a[80:83], v1, s[8:11], s82 offen offset:0 sc1
v_lshrrev_b32 v12, 8, v1
Mvaa 128,148,12
Ta 140,22,41184
Ta 142,22,46304
Mvaa 136,148,28
Mvaa 136,144,24
W 8
Ta 144,25,256
Ta 146,25,5376
Mvaa 140,152,24
Mvaa 140,156,28
buffer_load_dwordx4 a[68:71], v1, s[8:11], s81 offen offset:1024 sc1
Ta 148,21,41216
Mvaa 132,156,12
Ta 150,21,46336
Mvaa 132,152,8
W 8
Ta 152,26,288
Ta 154,26,5408
Mvaa 144,128,8
Mvaa 144,132,12
Ta 156,22,41248
Ta 158,22,46368
Mvaa 152,132,28
buffer_load_dwordx4 a[84:87], v1, s[8:11], s82 offen offset:1024 sc1
Mvaa 152,128,24
W 8
Ta 128,21,51200
Ta 130,21,56320
Mvaa 156,136,24
Mvaa 156,140,28
Ta 132,23,10240
Ta 134,23,15360
Mvaa 148,140,12
v_lshl_add_u32 v20, v12, 4, v20
Mvaa 148,136,8
W 8
buffer_load_dwordx4 a[72:75], v1, s[8:11], s81 offen offset:2048 sc1
Ta 136,23,30720
Mvaa 160,144,8
Ta 138,23,35840
Mvaa 160,148,12
Ta 140,23,40960
Ta 142,23,46080
Mvaa 168,148,28
Mvaa 168,144,24
W 8
Ta 144,22,51232
Ta 146,22,56352
Mvaa 172,152,24
buffer_load_dwordx4 a[88:91], v1, s[8:11], s82 offen offset:2048 sc1
Mvaa 172,156,28
Ta 148,24,10272
Ta 150,24,15392
Mvaa 164,156,12
Mvaa 164,152,8
W 8
Ta 152,24,30752
Ta 154,24,35872
Mvaa 96,128,32
Mvaa 96,132,36
buffer_load_dwordx4 a[76:79], v1, s[8:11], s81 offen offset:3072 sc1
Ta 156,24,40992
Mvaa 104,132,52
Ta 158,24,46112
Mvaa 104,128,48
W 8
Ta 128,21,51264
Ta 130,21,56384
Mvaa 104,136,56
v_bfe_u32 v15, v1, 7, 1
Mvaa 104,140,60
Ta 132,23,10304
Ta 134,23,15424
Mvaa 96,140,44
buffer_load_dwordx4 a[92:95], v1, s[8:11], s82 offen offset:3072 sc1
Mvaa 96,136,40
W 8
s_add_u32 s81, s58, 2
s_and_b32 s81, s81, 3
Mvaa 100,144,32
s_lshl_b32 s81, s81, 12
s_add_u32 s81, s81, s50
Ta 136,23,30784
Mvaa 100,148,36
Ta 138,23,35904
Ta 140,23,41024
Mvaa 108,148,52
Ta 142,23,46144
buffer_load_dwordx4 a[96:99], v1, s[8:11], s81 offen offset:0 sc1
Mvaa 108,144,48
W 8
s_add_u32 s82, s81, 131072
Ta 144,22,51296
Mvaa 108,152,56
Ta 146,22,56416
Mvaa 108,156,60
Ta 148,24,10336
Ta 150,24,15456
Mvaa 100,156,44
Mvaa 100,152,40
W 8
Ta 152,24,30816
Ta 154,24,35936
Mvaa 112,128,32
buffer_load_dwordx4 a[112:115], v1, s[8:11], s82 offen offset:0 sc1
Mvaa 112,132,36
Ta 156,24,41056
Ta 158,24,46176
Mvaa 120,132,52
K 96,96,97
K 97,98,99
Mvaa 120,128,48
W 8
Ta 128,21,51328
v_mul_u32_u24 v14, 10240, v15
Mvaa 120,136,56
Ta 130,21,56448
K 98,100,101
Mvaa 120,140,60
buffer_load_dwordx4 a[100:103], v1, s[8:11], s81 offen offset:1024 sc1
K 99,102,103
Mvaa 112,140,44
Ta 132,23,10368
Ta 134,23,15488
Mvaa 112,136,40
W 8
s_add_u32 s83, s28, 0
Ta 136,23,30848
Mvaa 116,144,32
Ta 138,23,35968
Mvaa 116,148,36
buffer_store_dwordx4 v[96:99], v1, s[8:11], s83 offen sc1
Ta 140,23,41088
Mvaa 124,148,52
Ta 142,23,46208
K 104,104,105
Mvaa 124,144,48
W 8
Ta 144,22,51360
K 105,106,107
Mvaa 124,152,56
Ta 146,22,56480
Mvaa 124,156,60
buffer_load_dwordx4 a[116:119], v1, s[8:11], s82 offen offset:1024 sc1
K 106,108,109
Mvaa 116,156,44
Ta 148,24,10400
Ta 150,24,15520
Mvaa 116,152,40
W 8
Ta 152,24,30880
K 107,110,111
Mvaa 128,128,32
s_add_u32 s83, s29, 40960
Ta 154,24,36000
Mvaa 128,132,36
buffer_load_dwordx4 a[104:107], v1, s[8:11], s81 offen offset:2048 sc1
v_lshrrev_b32 v15, 8, v1
Mvaa 136,132,52
Ta 156,24,41120
Ta 158,24,46240
Mvaa 136,128,48
W 8
Ta 128,21,51392
Ta 130,21,56512
Mvaa 136,136,56
Mvaa 136,140,60
buffer_store_dwordx4 v[104:107], v1, s[8:11], s83 offen sc1
K 112,112,113
Mvaa 128,140,44
Ta 132,23,10432
K 113,114,115
Mvaa 128,136,40
W 7
Ta 134,23,15552
K 114,116,117
Mvaa 132,144,32
Ta 136,23,30912
Ta 138,23,36032
Mvaa 132,148,36
buffer_load_dwordx4 a[120:123], v1, s[8:11], s82 offen offset:2048 sc1
K 115,118,119
Mvaa 140,148,52
s_add_u32 s83, s28, 8192
Ta 140,23,41152
Ta 142,23,46272
Mvaa 140,144,48
)"
R"(W 8
Ta 144,22,51424
Ta 146,22,56544
Mvaa 140,152,56
Mvaa 140,156,60
buffer_load_dwordx4 a[108:111], v1, s[8:11], s81 offen offset:3072 sc1
Ta 148,24,10464
Mvaa 132,156,44
Ta 150,24,15584
Mvaa 132,152,40
W 8
Ta 152,24,30944
v_lshl_add_u32 v14, v15, 7, v14
Mvaa 144,128,32
Ta 154,24,36064
Mvaa 144,132,36
buffer_store_dwordx4 v[112:115], v1, s[8:11], s83 offen sc1
K 120,120,121
Mvaa 152,132,52
K 121,122,123
K 122,124,125
Mvaa 152,128,48
W 6
Ta 156,24,41184
K 123,126,127
Mvaa 152,136,56
s_add_u32 s83, s29, 49152
Ta 158,24,46304
Ta 128,21,51456
Mvaa 152,140,60
buffer_load_dwordx4 a[124:127], v1, s[8:11], s82 offen offset:3072 sc1
Ta 130,21,56576
Mvaa 144,140,44
s_add_u32 s81, s58, 3
s_and_b32 s81, s81, 3
s_lshl_b32 s81, s81, 12
Mvaa 144,136,40
W 6
s_add_u32 s81, s81, s50
Ta 132,23,10496
Mvaa 148,144,32
Ta 134,23,15616
Ta 136,23,30976
Mvaa 148,148,36
buffer_load_dwordx4 a[184:187], v1, s[8:11], s81 offen offset:0 sc1
Ta 138,23,36096
Mvaa 156,148,52
s_add_u32 s82, s81, 131072
Ta 140,23,41216
Ta 142,23,46336
Mvaa 156,144,48
W 8
Ta 144,22,51488
Ta 146,22,56608
Mvaa 156,152,56
Mvaa 156,156,60
buffer_store_dwordx4 v[120:123], v1, s[8:11], s83 offen sc1
K 128,128,129
Mvaa 148,156,44
K 129,130,131
K 130,132,133
Mvaa 148,152,40
W 6
Ta 148,24,10528
K 131,134,135
Mvaa 160,128,32
s_add_u32 s83, s28, 16384
Ta 150,24,15648
v_and_b32 v15, 0x70, v1
Mvaa 160,132,36
buffer_load_dwordx4 a[200:203], v1, s[8:11], s82 offen offset:0 sc1
Ta 152,24,31008
Mvaa 168,132,52
Ta 154,24,36128
Ta 156,24,41248
Mvaa 168,128,48
W 7
Ta 158,24,46368
Ta 128,21,10240
Mvaa 168,136,56
Ta 130,21,15360
Mvaa 168,140,60
buffer_load_dwordx4 a[188:191], v1, s[8:11], s81 offen offset:1024 sc1
Ta 132,21,20480
Mvaa 160,140,44
Ta 134,21,25600
Mvaa 160,136,40
W 8
Ta 136,22,10272
Ta 138,22,15392
Mvaa 164,144,32
Mvaa 164,148,36
buffer_store_dwordx4 v[128:131], v1, s[8:11], s83 offen sc1
K 136,136,137
Mvaa 172,148,52
K 137,138,139
K 138,140,141
Mvaa 172,144,48
W 6
Ta 140,22,20512
K 139,142,143
Mvaa 172,152,56
s_add_u32 s83, s29, 57344
Ta 142,22,25632
Ta 144,21,10304
Mvaa 172,156,60
buffer_load_dwordx4 a[204:207], v1, s[8:11], s82 offen offset:1024 sc1
Ta 146,21,15424
Mvaa 164,156,44
Ta 148,21,20544
v_add_u32 v14, v14, v15
Mvaa 164,152,40
s_waitcnt vmcnt(21) lgkmcnt(7)
Ta 150,21,25664
Ta 152,22,10336
Mvaa 176,128,64
Ta 154,22,15456
Mvaa 176,132,68
buffer_load_dwordx4 a[192:195], v1, s[8:11], s81 offen offset:2048 sc1
Ta 156,22,20576
Mvaa 184,132,84
Ta 158,22,25696
Mvaa 184,128,80
W 8
Ta 128,21,10368
Ta 130,21,15488
Mvaa 188,136,80
Mvaa 188,140,84
buffer_store_dwordx4 v[136:139], v1, s[8:11], s83 offen sc1
K 144,144,145
Mvaa 180,140,68
K 145,146,147
K 146,148,149
Mvaa 180,136,64
W 6
Ta 132,21,20608
K 147,150,151
Mvaa 192,144,64
s_add_u32 s83, s28, 24576
Ta 134,21,25728
Ta 136,22,10400
Mvaa 192,148,68
buffer_load_dwordx4 a[208:211], v1, s[8:11], s82 offen offset:2048 sc1
Ta 138,22,15520
Mvaa 200,148,84
Ta 140,22,20640
Ta 142,22,25760
Mvaa 200,144,80
W 8
Ta 144,21,10432
Ta 146,21,15552
Mvaa 204,152,80
Mvaa 204,156,84
buffer_store_dwordx4 v[144:147], v1, s[8:11], s83 offen sc1
K 152,152,153
Mvaa 196,156,68
K 153,154,155
K 154,156,157
Mvaa 196,152,64
W 6
Ta 148,21,20672
K 155,158,159
Mvaa 208,128,64
s_add_u32 s83, s29, 65536
Ta 150,21,25792
Ta 152,22,10464
Mvaa 208,132,68
buffer_load_dwordx4 a[196:199], v1, s[8:11], s81 offen offset:3072 sc1
Ta 154,22,15584
Mvaa 216,132,84
Ta 156,22,20704
Ta 158,22,25824
Mvaa 216,128,80
W 8
Ta 128,21,10496
Ta 130,21,15616
Mvaa 220,136,80
Mvaa 220,140,84
buffer_load_dwordx4 a[212:215], v1, s[8:11], s82 offen offset:3072 sc1
Ta 132,21,20736
Mvaa 212,140,68
Ta 134,21,25856
Mvaa 212,136,64
W 8
Ta 136,22,10528
Ta 138,22,15648
Mvaa 224,144,64
Mvaa 224,148,68
buffer_store_dwordx4 v[152:155], v1, s[8:11], s83 offen sc1
K 160,160,161
Mvaa 232,148,84
K 161,162,163
K 162,164,165
Mvaa 232,144,80
W 6
Ta 140,22,20768
K 163,166,167
Mvaa 236,152,80
s_add_u32 s83, s28, 32768
Ta 142,22,25888
Ta 144,21,30720
Mvaa 236,156,84
buffer_load_dwordx4 v[36:39], v20, s[16:19], s72 offen
Ta 146,21,35840
Mvaa 228,156,68
Ta 148,23,0
Ta 150,23,5120
Mvaa 228,152,64
W 8
Ta 152,22,30752
Ta 154,22,35872
Mvaa 240,128,64
Mvaa 240,132,68
buffer_load_dwordx4 v[44:47], v14, s[8:11], s75 offen sc1
Ta 156,24,32
Mvaa 248,132,84
Ta 158,24,5152
Mvaa 248,128,80
W 8
Ta 128,21,30784
Ta 130,21,35904
Mvaa 252,136,80
Mvaa 252,140,84
buffer_store_dwordx4 v[160:163], v1, s[8:11], s83 offen sc1
K 168,168,169
Mvaa 244,140,68
K 169,170,171
K 170,172,173
Mvaa 244,136,64
s_waitcnt vmcnt(27) lgkmcnt(6)
Ta 132,23,64
K 171,174,175
Mvaa 176,144,72
s_add_u32 s83, s29, 73728
Ta 134,23,5184
Ta 136,22,30816
Mvaa 176,148,76
buffer_load_dwordx4 v[52:55], v20, s[16:19], s73 offen
Ta 138,22,35936
Mvaa 184,148,92
Ta 140,24,96
Ta 142,24,5216
Mvaa 184,144,88
W 8
Ta 144,21,30848
Ta 146,21,35968
Mvaa 188,152,88
Mvaa 188,156,92
buffer_store_dwordx4 v[168:171], v1, s[8:11], s83 offen sc1
Ta 148,23,128
Mvaa 180,156,76
Ta 150,23,5248
Mvaa 180,152,72
W 8
Ta 152,22,30880
Ta 154,22,36000
Mvaa 192,128,72
Mvaa 192,132,76
buffer_load_dwordx4 v[60:63], v14, s[8:11], s76 offen sc1
Ta 156,24,160
Mvaa 200,132,92
Ta 158,24,5280
Mvaa 200,128,88
W 8
Ta 128,21,30912
Ta 130,21,36032
Mvaa 204,136,88
Mvaa 204,140,92
buffer_load_dwordx4 v[68:71], v20, s[16:19], s74 offen
Ta 132,23,192
Mvaa 196,140,76
Ta 134,23,5312
Mvaa 196,136,72
W 8
Ta 136,22,30944
Ta 138,22,36064
Mvaa 208,144,72
Mvaa 208,148,76
buffer_load_dwordx4 v[76:79], v14, s[8:11], s77 offen sc1
Ta 140,24,224
Mvaa 216,148,92
Ta 142,24,5344
Mvaa 216,144,88
W 8
Ta 144,21,30976
Ta 146,21,36096
Mvaa 220,152,88
Mvaa 220,156,92
buffer_load_dwordx2 v[12:13], v27, s[8:11], 0 offen offset:3584 sc1
Ta 148,23,256
Mvaa 212,156,76
Ta 150,23,5376
Mvaa 212,152,72
W 8
Ta 152,22,31008
Ta 154,22,36128
Mvaa 224,128,72
Mvaa 224,132,76
Ta 156,24,288
Ta 158,24,5408
Mvaa 232,132,92
Mvaa 232,128,88
W 8
Ta 128,23,20480
Ta 130,23,25600
Mvaa 236,136,88
Mvaa 236,140,92
Ta 132,23,51200
Ta 134,23,56320
Mvaa 228,140,76
Mvaa 228,136,72
W 8
Ta 136,25,0
Ta 138,25,5120
Mvaa 240,144,72
Mvaa 240,148,76
Ta 140,21,40960
Ta 142,21,46080
Mvaa 248,148,92
Mvaa 248,144,88
W 8
Ta 144,21,51200
Ta 146,21,56320
Mvaa 252,152,88
Mvaa 252,156,92
Ta 148,23,10240
Ta 150,23,15360
Mvaa 244,156,76
Mvaa 244,152,72
s_waitcnt vmcnt(28) lgkmcnt(8)
Ta 152,23,30720
Ta 154,23,35840
Mvaa 176,128,96
Mvaa 176,132,100
Ta 156,23,40960
Ta 158,23,46080
Mvaa 184,132,116
Mvaa 184,128,112
s_waitcnt vmcnt(22) lgkmcnt(8)
Ta 128,24,20512
Ta 130,24,25632
Mvaa 184,136,120
Mvaa 184,140,124
Ta 132,24,51232
Ta 134,24,56352
Mvaa 176,140,108
Mvaa 176,136,104
s_waitcnt vmcnt(16) lgkmcnt(8)
Ta 136,26,32
Ta 138,26,5152
Mvaa 176,144,184
Mvaa 176,148,188
Ta 140,22,40992
Ta 142,22,46112
Mvaa 184,148,204
s_waitcnt vmcnt(4)
v_lshlrev_b32 v2, 2, v1
v_mov_b64 v[28:29], s[12:13]
s_mov_b64 exec, 3
buffer_store_dwordx2 v[28:29], v2, s[8:11], s53 offen offset:0 sc1
s_mov_b64 exec, -1
Mvaa 184,144,200
W 8
Ta 144,22,51232
Ta 146,22,56352
Mvaa 184,152,208
Mvaa 184,156,212
Ta 148,24,10272
Ta 150,24,15392
Mvaa 176,156,196
Mvaa 176,152,192
W 8
Ta 152,24,30752
Ta 154,24,35872
Mvaa 180,128,96
Mvaa 180,132,100
Ta 156,24,40992
Ta 158,24,46112
Mvaa 188,132,116
Mvaa 188,128,112
W 8
Ta 128,23,20544
Ta 130,23,25664
Mvaa 188,136,120
Mvaa 188,140,124
Ta 132,23,51264
Ta 134,23,56384
Mvaa 180,140,108
Mvaa 180,136,104
W 8
Ta 136,25,64
Ta 138,25,5184
Mvaa 180,144,184
Mvaa 180,148,188
Ta 140,21,41024
Ta 142,21,46144
Mvaa 188,148,204
Mvaa 188,144,200
W 8
Ta 144,21,51264
)"
R"(Ta 146,21,56384
Mvaa 188,152,208
Mvaa 188,156,212
Ta 148,23,10304
Ta 150,23,15424
Mvaa 180,156,196
s_waitcnt vmcnt(1)
v_xor_b32 v12, s12, v12
v_xor_b32 v13, s13, v13
v_lshrrev_b32 v3, 11, v12
v_or_b32 v13, v13, v3
v_cmp_eq_u32 vcc, 0, v13
s_cmp_eq_u64 vcc, -1
s_cbranch_scc0 .LL_106%=
.LL_107%=:
v_bfe_u32 v13, v12, 4, 7
s_xor_b32 s66, s61, 124
v_cmp_eq_u32 vcc, s61, v13
v_cmp_eq_u32_e64 s[86:87], s66, v13
s_or_b32 s68, vcc_lo, s86
s_and_b32 s68, s68, 255
s_cbranch_scc1 .Lup_104%=
.Lupr_105%=:
Mvaa 180,152,192
W 8
Ta 152,23,30784
Ta 154,23,35904
Mvaa 192,128,96
v_lshlrev_b32 v32, 16, v36
Mvaa 192,132,100
Ta 156,23,41024
v_and_b32 v33, 0xffff0000, v36
Mvaa 200,132,116
Ta 158,23,46144
v_lshlrev_b32 v34, 16, v37
Mvaa 200,128,112
W 8
K 176,176,177
v_and_b32 v35, 0xffff0000, v37
Mvaa 200,136,120
Ta 128,24,20576
Ta 130,24,25696
Mvaa 200,140,124
buffer_load_dwordx2 v[16:17], v18, s[8:11], s63 offen sc1
v_lshlrev_b32 v36, 16, v38
Mvaa 192,140,108
K 177,178,179
v_and_b32 v37, 0xffff0000, v38
Mvaa 192,136,104
W 6
Ta 132,24,51296
K 178,180,181
Mvaa 192,144,184
Ta 134,24,56416
v_lshlrev_b32 v38, 16, v39
Mvaa 192,148,188
K 179,182,183
v_and_b32 v39, 0xffff0000, v39
Mvaa 200,148,204
s_add_u32 s83, s28, 327680
Ta 136,26,96
Ta 138,26,5216
Mvaa 200,144,200
W 6
v_lshlrev_b32 v40, 16, v44
v_and_b32 v41, 0xffff0000, v44
Mvaa 200,152,208
buffer_store_dwordx4 v[176:179], v1, s[8:11], s83 offen sc1
K 184,184,185
Mvaa 200,156,212
K 185,186,187
K 186,188,189
Mvaa 192,156,196
K 187,190,191
s_add_u32 s83, s29, 368640
buffer_store_dwordx4 v[184:187], v1, s[8:11], s83 offen sc1
Mvaa 192,152,192
Ta 140,22,41056
Ta 142,22,46176
W 4
Mvaa 196,128,96
v_lshlrev_b32 v42, 16, v45
v_and_b32 v43, 0xffff0000, v45
Mvaa 196,132,100
Ta 144,22,51296
Ta 146,22,56416
Mvaa 204,132,116
v_lshlrev_b32 v44, 16, v46
v_and_b32 v45, 0xffff0000, v46
Mvaa 204,128,112
Ta 148,24,10336
Ta 150,24,15456
W 4
Mvaa 204,136,120
v_lshlrev_b32 v46, 16, v47
v_and_b32 v47, 0xffff0000, v47
Mvaa 204,140,124
Ta 152,24,30816
Ta 154,24,35936
Mvaa 196,140,108
v_lshlrev_b32 v48, 16, v52
v_and_b32 v49, 0xffff0000, v52
Mvaa 196,136,104
Ta 156,24,41056
Ta 158,24,46176
W 4
Mvaa 196,144,184
v_lshlrev_b32 v50, 16, v53
v_and_b32 v51, 0xffff0000, v53
Mvaa 196,148,188
Ta 128,23,20608
Ta 130,23,25728
Mvaa 204,148,204
v_lshlrev_b32 v52, 16, v54
v_and_b32 v53, 0xffff0000, v54
Mvaa 204,144,200
Ta 132,23,51328
Ta 134,23,56448
W 4
Mvaa 204,152,208
v_lshlrev_b32 v54, 16, v55
v_and_b32 v55, 0xffff0000, v55
Mvaa 204,156,212
Ta 136,25,128
Ta 138,25,5248
Mvaa 196,156,196
v_lshlrev_b32 v56, 16, v60
v_and_b32 v57, 0xffff0000, v60
Mvaa 196,152,192
Ta 140,21,41088
Ta 142,21,46208
W 4
Mvaa 208,128,96
v_lshlrev_b32 v58, 16, v61
v_and_b32 v59, 0xffff0000, v61
Mvaa 208,132,100
Ta 144,21,51328
Ta 146,21,56448
Mvaa 216,132,116
v_lshlrev_b32 v60, 16, v62
v_and_b32 v61, 0xffff0000, v62
Mvaa 216,128,112
Ta 148,23,10368
Ta 150,23,15488
W 4
Mvaa 216,136,120
v_lshlrev_b32 v62, 16, v63
v_and_b32 v63, 0xffff0000, v63
Mvaa 216,140,124
Ta 152,23,30848
Ta 154,23,35968
Mvaa 208,140,108
v_lshlrev_b32 v64, 16, v68
v_and_b32 v65, 0xffff0000, v68
Mvaa 208,136,104
Ta 156,23,41088
Ta 158,23,46208
W 4
Mvaa 208,144,184
v_lshlrev_b32 v66, 16, v69
K 192,192,193
Mvaa 208,148,188
Ta 128,24,20640
v_and_b32 v67, 0xffff0000, v69
Mvaa 216,148,204
Ta 130,24,25760
K 193,194,195
Mvaa 216,144,200
Ta 132,24,51360
Ta 134,24,56480
W 4
Mvaa 216,152,208
v_lshlrev_b32 v68, 16, v70
K 194,196,197
Mvaa 216,156,212
v_and_b32 v69, 0xffff0000, v70
v_lshlrev_b32 v70, 16, v71
Mvaa 208,156,196
K 195,198,199
s_add_u32 s83, s28, 335872
buffer_store_dwordx4 v[192:195], v1, s[8:11], s83 offen sc1
K 200,200,201
K 201,202,203
K 202,204,205
K 203,206,207
s_add_u32 s83, s29, 376832
buffer_store_dwordx4 v[200:203], v1, s[8:11], s83 offen sc1
Mvaa 208,152,192
Ta 136,26,160
Ta 138,26,5280
Ta 140,22,41120
Ta 142,22,46240
W 4
Mvaa 212,128,96
s_waitcnt vmcnt(4)
v_cmp_eq_u64 vcc, s[12:13], v[16:17]
s_and_b32 s71, vcc_lo, 255
s_bitset0_b32 s71, s59
v_and_b32 v71, 0xffff0000, v71
v_lshlrev_b32 v72, 16, v76
Mvaa 212,132,100
s_lshl_b32 s92, s14, 3
s_lshr_b32 s92, s71, s92
s_mov_b32 s91, 134217728
Mvaa 220,132,116
s_bitcmp1_b32 s71, 0
s_cselect_b32 s94, s78, s91
v_and_b32 v73, 0xffff0000, v76
Mvaa 220,128,112
Ta 144,22,51360
Ta 146,22,56480
Ta 148,24,10400
Ta 150,24,15520
W 4
Mvaa 220,136,120
v_lshlrev_b32 v74, 16, v77
v_and_b32 v75, 0xffff0000, v77
Mvaa 220,140,124
v_lshlrev_b32 v76, 16, v78
v_and_b32 v77, 0xffff0000, v78
Mvaa 212,140,108
v_lshlrev_b32 v78, 16, v79
v_and_b32 v79, 0xffff0000, v79
Mvaa 212,136,104
Ta 152,24,30880
Ta 154,24,36000
Ta 156,24,41120
Ta 158,24,46240
W 4
Mvaa 212,144,184
Ta 128,23,20672
Ta 130,23,25792
Mvaa 212,148,188
buffer_load_dwordx4 a[0:3], v1, s[8:11], s94 offen offset:0 sc1
Ta 132,23,51392
Mvaa 220,148,204
Ta 134,23,56512
Ta 136,25,192
Mvaa 220,144,200
W 5
Ta 138,25,5312
Ta 140,21,41152
Mvaa 220,152,208
Ta 142,21,46272
Ta 144,21,51392
Mvaa 220,156,212
s_bitcmp1_b32 s92, 0
s_cbranch_scc0 .LL_109%=
buffer_load_dwordx4 a[216:219], v1, s[8:11], s65 offen offset:0 sc1
.LL_109%=:
Ta 146,21,56512
Mvaa 212,156,196
s_bitcmp1_b32 s71, 1
s_cselect_b32 s94, s78, s91
Ta 148,23,10432
Mvaa 212,152,192
W 7
Ta 150,23,15552
Ta 152,23,30912
Mvaa 224,128,96
Ta 154,23,36032
Mvaa 224,132,100
buffer_load_dwordx4 a[4:7], v1, s[8:11], s94 offen offset:1024 sc1
Ta 156,23,41152
Mvaa 232,132,116
Ta 158,23,46272
K 208,208,209
Mvaa 232,128,112
W 8
Ta 128,24,20704
Ta 130,24,25824
Mvaa 232,136,120
K 209,210,211
Mvaa 232,140,124
s_bitcmp1_b32 s92, 1
s_cbranch_scc0 .LL_110%=
buffer_load_dwordx4 a[220:223], v1, s[8:11], s65 offen offset:1024 sc1
.LL_110%=:
K 210,212,213
Mvaa 224,140,108
s_bitcmp1_b32 s71, 2
s_cselect_b32 s94, s78, s91
Ta 132,24,51424
Mvaa 224,136,104
W 7
Ta 134,24,56544
K 211,214,215
Mvaa 224,144,184
s_add_u32 s83, s28, 344064
Ta 136,26,224
Ta 138,26,5344
Mvaa 224,148,188
buffer_load_dwordx4 a[8:11], v1, s[8:11], s94 offen offset:2048 sc1
Ta 140,22,41184
Mvaa 232,148,204
Ta 142,22,46304
Mvaa 232,144,200
W 8
Ta 144,22,51424
Ta 146,22,56544
Mvaa 232,152,208
Mvaa 232,156,212
buffer_store_dwordx4 v[208:211], v1, s[8:11], s83 offen sc1
K 216,216,217
Mvaa 224,156,196
K 217,218,219
K 218,220,221
K 219,222,223
s_add_u32 s83, s29, 385024
buffer_store_dwordx4 v[216:219], v1, s[8:11], s83 offen sc1
Mvaa 224,152,192
W 6
Ta 148,24,10464
Ta 150,24,15584
Mvaa 228,128,96
Ta 152,24,30944
Ta 154,24,36064
Mvaa 228,132,100
Ta 156,24,41184
Ta 158,24,46304
Mvaa 236,132,116
s_bitcmp1_b32 s92, 2
s_cbranch_scc0 .LL_111%=
buffer_load_dwordx4 a[224:227], v1, s[8:11], s65 offen offset:2048 sc1
.LL_111%=:
Mvaa 236,128,112
W 8
s_bitcmp1_b32 s71, 3
s_cselect_b32 s94, s78, s91
Mvaa 236,136,120
Ta 128,23,20736
Ta 130,23,25856
Mvaa 236,140,124
Ta 132,23,51456
Ta 134,23,56576
Mvaa 228,140,108
buffer_load_dwordx4 a[12:15], v1, s[8:11], s94 offen offset:3072 sc1
Mvaa 228,136,104
W 8
Ta 136,25,256
Ta 138,25,5376
Mvaa 228,144,184
Mvaa 228,148,188
Ta 140,21,41216
Ta 142,21,46336
Mvaa 236,148,204
s_bitcmp1_b32 s92, 3
s_cbranch_scc0 .LL_112%=
buffer_load_dwordx4 a[228:231], v1, s[8:11], s65 offen offset:3072 sc1
.LL_112%=:
Mvaa 236,144,200
W 8
s_bitcmp1_b32 s71, 4
s_cselect_b32 s94, s78, s91
Mvaa 236,152,208
Ta 144,21,51456
Ta 146,21,56576
Mvaa 236,156,212
Ta 148,23,10496
Ta 150,23,15616
Mvaa 228,156,196
buffer_load_dwordx4 a[16:19], v19, s[8:11], s94 offen offset:0 sc1
Mvaa 228,152,192
W 8
Ta 152,23,30976
Ta 154,23,36096
Mvaa 240,128,96
Mvaa 240,132,100
Ta 156,23,41216
Ta 158,23,46336
Mvaa 248,132,116
s_bitcmp1_b32 s92, 4
s_cbranch_scc0 .LL_113%=
buffer_load_dwordx4 a[232:235], v19, s[8:11], s65 offen offset:0 sc1
.LL_113%=:
K 224,224,225
Mvaa 248,128,112
W 8
s_bitcmp1_b32 s71, 5
s_cselect_b32 s94, s78, s91
Mvaa 248,136,120
Ta 128,24,20768
Ta 130,24,25888
Mvaa 248,140,124
K 225,226,227
K 226,228,229
Mvaa 240,140,108
buffer_load_dwordx4 a[20:23], v19, s[8:11], s94 offen offset:1024 sc1
Ta 132,24,51488
Mvaa 240,136,104
W 7
Ta 134,24,56608
K 227,230,231
Mvaa 240,144,184
s_add_u32 s83, s28, 352256
Ta 136,26,288
Ta 138,26,5408
Mvaa 240,148,188
Ta 140,22,41248
Ta 142,22,46368
)"
R"(Mvaa 248,148,204
s_bitcmp1_b32 s92, 5
s_cbranch_scc0 .LL_114%=
buffer_load_dwordx4 a[236:239], v19, s[8:11], s65 offen offset:1024 sc1
.LL_114%=:
Mvaa 248,144,200
W 8
s_bitcmp1_b32 s71, 6
s_cselect_b32 s94, s78, s91
Mvaa 248,152,208
Ta 144,22,51488
Ta 146,22,56608
Mvaa 248,156,212
Ta 148,24,10528
Ta 150,24,15648
Mvaa 240,156,196
buffer_store_dwordx4 v[224:227], v1, s[8:11], s83 offen sc1
K 232,232,233
K 233,234,235
K 234,236,237
K 235,238,239
s_add_u32 s83, s29, 393216
buffer_store_dwordx4 v[232:235], v1, s[8:11], s83 offen sc1
Mvaa 240,152,192
W 8
Ta 152,24,31008
Ta 154,24,36128
Mvaa 244,128,96
Mvaa 244,132,100
Ta 156,24,41248
Ta 158,24,46368
Mvaa 252,132,116
buffer_load_dwordx4 a[24:27], v19, s[8:11], s94 offen offset:2048 sc1
Mvaa 252,128,112
W 8
Mvaa 252,136,120
Mvaa 252,140,124
Mvaa 244,140,108
s_bitcmp1_b32 s92, 6
s_cbranch_scc0 .LL_115%=
buffer_load_dwordx4 a[240:243], v19, s[8:11], s65 offen offset:2048 sc1
.LL_115%=:
Mvaa 244,136,104
W 4
s_bitcmp1_b32 s71, 7
s_cselect_b32 s94, s78, s91
Mvaa 244,144,184
Mvaa 244,148,188
Mvaa 252,148,204
buffer_load_dwordx4 a[28:31], v19, s[8:11], s94 offen offset:3072 sc1
Mvaa 252,144,200
W 0
Mvaa 252,152,208
Mvaa 252,156,212
Mvaa 244,156,196
s_bitcmp1_b32 s92, 7
s_cbranch_scc0 .LL_116%=
buffer_load_dwordx4 a[244:247], v19, s[8:11], s65 offen offset:3072 sc1
.LL_116%=:
Mvaa 244,152,192
K 240,240,241
K 241,242,243
s_nop 5
K 242,244,245
K 243,246,247
s_add_u32 s83, s28, 360448
buffer_store_dwordx4 v[240:243], v1, s[8:11], s83 offen sc1
K 248,248,249
K 249,250,251
K 250,252,253
K 251,254,255
s_add_u32 s83, s29, 401408
buffer_store_dwordx4 v[248:251], v1, s[8:11], s83 offen sc1
.Ltend_81%=:
buffer_load_dwordx2 v[16:17], v18, s[8:11], s63 offen sc1
s_barrier
s_bitcmp1_b32 s28, 27
s_cbranch_scc0 .LL_117%=
ds_write_b128 v1, v[96:99] offset:0
ds_write_b128 v1, v[112:115] offset:1024
ds_write_b128 v1, v[128:131] offset:2048
ds_write_b128 v1, v[144:147] offset:3072
ds_write_b128 v1, v[160:163] offset:4096
ds_write_b128 v1, v[176:179] offset:5120
ds_write_b128 v1, v[192:195] offset:6144
ds_write_b128 v1, v[208:211] offset:7168
ds_write_b128 v1, v[224:227] offset:8192
ds_write_b128 v1, v[240:243] offset:9216
W 0
.LL_117%=:
s_bitcmp1_b32 s29, 27
s_cbranch_scc0 .LL_118%=
ds_write_b128 v1, v[104:107] offset:0
ds_write_b128 v1, v[120:123] offset:1024
ds_write_b128 v1, v[136:139] offset:2048
ds_write_b128 v1, v[152:155] offset:3072
ds_write_b128 v1, v[168:171] offset:4096
ds_write_b128 v1, v[184:187] offset:5120
ds_write_b128 v1, v[200:203] offset:6144
ds_write_b128 v1, v[216:219] offset:7168
ds_write_b128 v1, v[232:235] offset:8192
ds_write_b128 v1, v[248:251] offset:9216
W 0
.LL_118%=:
s_lshl_b32 s80, s14, 10
v_add_u32 v21, s80, v1
s_cmp_lg_u32 s68, 0
s_cbranch_scc1 .Lup_119%=
.Lupr_120%=:
s_lshl_b32 s66, 0x101, s59
s_waitcnt vmcnt(1)
s_cmp_eq_u32 s64, 0
s_cbranch_scc0 .LL_125%=
v_lshlrev_b32 v2, 2, v1
v_mov_b64 v[28:29], s[12:13]
s_mov_b64 exec, 3
buffer_store_dwordx2 v[28:29], v2, s[8:11], s53 offen offset:512 sc1
s_mov_b64 exec, -1
.LL_125%=:
s_barrier
s_waitcnt vmcnt(1)
v_cmp_eq_u64 vcc, s[12:13], v[16:17]
s_or_b32 s67, vcc_lo, s66
s_cmp_eq_u32 s14, 0
s_cbranch_scc1 .LL_127%=
s_cmp_eq_u32 s14, 1
s_cbranch_scc1 .LL_128%=
s_mov_b32 s84, 0
.LL_129%=:
s_or_b32 s70, s71, s66
s_andn2_b32 s69, s67, s71
s_and_b32 s69, s69, 0xffff
s_cbranch_scc0 .LL_132%=
s_and_b32 s80, s69, 255
s_cbranch_scc0 .LL_133%=
s_bitcmp1_b32 s69, 0
s_cbranch_scc0 .LL_134%=
s_cmp_eq_u32 s59, 0
s_cbranch_scc0 .LL_135%=
Ra 0,21,0
s_branch .LL_134%=
.LL_135%=:
buffer_load_dwordx4 a[0:3], v1, s[8:11], s78 offen offset:0 sc1
.LL_134%=:
s_bitcmp1_b32 s69, 1
s_cbranch_scc0 .LL_136%=
s_cmp_eq_u32 s59, 1
s_cbranch_scc0 .LL_137%=
Ra 4,21,0
s_branch .LL_136%=
.LL_137%=:
buffer_load_dwordx4 a[4:7], v1, s[8:11], s78 offen offset:1024 sc1
.LL_136%=:
s_bitcmp1_b32 s69, 2
s_cbranch_scc0 .LL_138%=
s_cmp_eq_u32 s59, 2
s_cbranch_scc0 .LL_139%=
Ra 8,21,0
s_branch .LL_138%=
.LL_139%=:
buffer_load_dwordx4 a[8:11], v1, s[8:11], s78 offen offset:2048 sc1
.LL_138%=:
s_bitcmp1_b32 s69, 3
s_cbranch_scc0 .LL_140%=
s_cmp_eq_u32 s59, 3
s_cbranch_scc0 .LL_141%=
Ra 12,21,0
s_branch .LL_140%=
.LL_141%=:
buffer_load_dwordx4 a[12:15], v1, s[8:11], s78 offen offset:3072 sc1
.LL_140%=:
s_bitcmp1_b32 s69, 4
s_cbranch_scc0 .LL_142%=
s_cmp_eq_u32 s59, 4
s_cbranch_scc0 .LL_143%=
Ra 16,21,0
s_branch .LL_142%=
.LL_143%=:
buffer_load_dwordx4 a[16:19], v19, s[8:11], s78 offen offset:0 sc1
.LL_142%=:
s_bitcmp1_b32 s69, 5
s_cbranch_scc0 .LL_144%=
s_cmp_eq_u32 s59, 5
s_cbranch_scc0 .LL_145%=
Ra 20,21,0
s_branch .LL_144%=
.LL_145%=:
buffer_load_dwordx4 a[20:23], v19, s[8:11], s78 offen offset:1024 sc1
.LL_144%=:
s_bitcmp1_b32 s69, 6
s_cbranch_scc0 .LL_146%=
s_cmp_eq_u32 s59, 6
s_cbranch_scc0 .LL_147%=
Ra 24,21,0
s_branch .LL_146%=
.LL_147%=:
buffer_load_dwordx4 a[24:27], v19, s[8:11], s78 offen offset:2048 sc1
.LL_146%=:
s_bitcmp1_b32 s69, 7
s_cbranch_scc0 .LL_148%=
s_cmp_eq_u32 s59, 7
s_cbranch_scc0 .LL_149%=
Ra 28,21,0
s_branch .LL_148%=
.LL_149%=:
buffer_load_dwordx4 a[28:31], v19, s[8:11], s78 offen offset:3072 sc1
.LL_148%=:
.LL_133%=:
s_and_b32 s80, s69, 65280
s_cbranch_scc0 .LL_150%=
s_bitcmp1_b32 s69, 8
s_cbranch_scc0 .LL_151%=
s_cmp_eq_u32 s59, 0
s_cbranch_scc0 .LL_152%=
Ra 32,21,5120
s_branch .LL_151%=
.LL_152%=:
buffer_load_dwordx4 a[32:35], v1, s[8:11], s79 offen offset:0 sc1
.LL_151%=:
s_bitcmp1_b32 s69, 9
s_cbranch_scc0 .LL_153%=
s_cmp_eq_u32 s59, 1
s_cbranch_scc0 .LL_154%=
Ra 36,21,5120
s_branch .LL_153%=
.LL_154%=:
buffer_load_dwordx4 a[36:39], v1, s[8:11], s79 offen offset:1024 sc1
.LL_153%=:
s_bitcmp1_b32 s69, 10
s_cbranch_scc0 .LL_155%=
s_cmp_eq_u32 s59, 2
s_cbranch_scc0 .LL_156%=
Ra 40,21,5120
s_branch .LL_155%=
.LL_156%=:
buffer_load_dwordx4 a[40:43], v1, s[8:11], s79 offen offset:2048 sc1
.LL_155%=:
s_bitcmp1_b32 s69, 11
s_cbranch_scc0 .LL_157%=
s_cmp_eq_u32 s59, 3
s_cbranch_scc0 .LL_158%=
Ra 44,21,5120
s_branch .LL_157%=
.LL_158%=:
buffer_load_dwordx4 a[44:47], v1, s[8:11], s79 offen offset:3072 sc1
.LL_157%=:
s_bitcmp1_b32 s69, 12
s_cbranch_scc0 .LL_159%=
s_cmp_eq_u32 s59, 4
s_cbranch_scc0 .LL_160%=
Ra 48,21,5120
s_branch .LL_159%=
.LL_160%=:
buffer_load_dwordx4 a[48:51], v19, s[8:11], s79 offen offset:0 sc1
.LL_159%=:
s_bitcmp1_b32 s69, 13
s_cbranch_scc0 .LL_161%=
s_cmp_eq_u32 s59, 5
s_cbranch_scc0 .LL_162%=
Ra 52,21,5120
s_branch .LL_161%=
.LL_162%=:
buffer_load_dwordx4 a[52:55], v19, s[8:11], s79 offen offset:1024 sc1
.LL_161%=:
s_bitcmp1_b32 s69, 14
s_cbranch_scc0 .LL_163%=
s_cmp_eq_u32 s59, 6
s_cbranch_scc0 .LL_164%=
Ra 56,21,5120
s_branch .LL_163%=
.LL_164%=:
buffer_load_dwordx4 a[56:59], v19, s[8:11], s79 offen offset:2048 sc1
.LL_163%=:
s_bitcmp1_b32 s69, 15
s_cbranch_scc0 .LL_165%=
s_cmp_eq_u32 s59, 7
s_cbranch_scc0 .LL_166%=
Ra 60,21,5120
s_branch .LL_165%=
.LL_166%=:
buffer_load_dwordx4 a[60:63], v19, s[8:11], s79 offen offset:3072 sc1
.LL_165%=:
.LL_150%=:
s_or_b32 s71, s71, s69
.LL_132%=:
s_andn2_b32 s80, 0xffff, s71
s_cbranch_scc0 .LL_131%=
s_sleep 4
buffer_load_dwordx2 v[16:17], v18, s[8:11], s63 offen sc1
s_and_b32 s80, s70, 0x100ff
s_cmp_eq_u32 s80, 0xff
s_cbranch_scc0 .LL_130%=
W 0
Zvva 80,4,0
Zvva 84,8,0
Mvva 80,4,4
Mvva 84,8,4
Mvva 80,4,8
Mvva 84,8,8
Mvva 80,4,12
Mvva 84,8,12
Mvva 80,4,16
Mvva 84,8,16
Mvva 80,4,20
Mvva 84,8,20
Mvva 80,4,24
Mvva 84,8,24
Mvva 80,4,28
Mvva 84,8,28
s_nop 6
K 160,80,81
K 161,82,83
K 162,84,85
K 163,86,87
v_lshlrev_b32 v80, 16, v160
v_and_b32 v81, 0xffff0000, v160
v_lshlrev_b32 v82, 16, v161
v_and_b32 v83, 0xffff0000, v161
v_lshlrev_b32 v84, 16, v162
v_and_b32 v85, 0xffff0000, v162
v_lshlrev_b32 v86, 16, v163
v_and_b32 v87, 0xffff0000, v163
v_pk_add_f32 v[80:81], v[32:33], v[80:81]
v_pk_add_f32 v[82:83], v[34:35], v[82:83]
v_pk_add_f32 v[84:85], v[36:37], v[84:85]
v_pk_add_f32 v[86:87], v[38:39], v[86:87]
K 160,80,81
K 161,82,83
K 162,84,85
K 163,86,87
v_lshlrev_b32 v80, 16, v160
v_and_b32 v81, 0xffff0000, v160
v_lshlrev_b32 v82, 16, v161
v_and_b32 v83, 0xffff0000, v161
v_lshlrev_b32 v84, 16, v162
v_and_b32 v85, 0xffff0000, v162
v_lshlrev_b32 v86, 16, v163
v_and_b32 v87, 0xffff0000, v163
v_pk_add_f32 v[80:81], v[40:41], v[80:81]
v_pk_add_f32 v[82:83], v[42:43], v[82:83]
v_pk_add_f32 v[84:85], v[44:45], v[84:85]
v_pk_add_f32 v[86:87], v[46:47], v[86:87]
K 80,80,81
K 81,82,83
K 82,84,85
K 83,86,87
buffer_store_dwordx4 v[80:83], v20, s[20:23], s72 offen nt
s_bitset1_b32 s71, 16
.LL_130%=:
s_add_u32 s84, s84, 1
s_waitcnt vmcnt(0)
v_cmp_eq_u64 vcc, s[12:13], v[16:17]
s_or_b32 s67, vcc_lo, s66
s_cmp_lt_u32 s84, 4194304
s_cbranch_scc1 .LL_129%=
.LL_131%=:
s_bitcmp1_b32 s71, 16
s_cbranch_scc1 .LL_167%=
s_and_b32 s80, s70, 0xff
s_cmp_eq_u32 s80, 0xff
s_cbranch_scc1 .LL_168%=
s_waitcnt vmcnt(0)
.LL_168%=:
W 0
Zvva 80,4,0
Zvva 84,8,0
Mvva 80,4,4
Mvva 84,8,4
Mvva 80,4,8
Mvva 84,8,8
Mvva 80,4,12
Mvva 84,8,12
Mvva 80,4,16
Mvva 84,8,16
Mvva 80,4,20
Mvva 84,8,20
Mvva 80,4,24
Mvva 84,8,24
Mvva 80,4,28
Mvva 84,8,28
s_nop 6
K 160,80,81
K 161,82,83
K 162,84,85
K 163,86,87
v_lshlrev_b32 v80, 16, v160
v_and_b32 v81, 0xffff0000, v160
v_lshlrev_b32 v82, 16, v161
v_and_b32 v83, 0xffff0000, v161
v_lshlrev_b32 v84, 16, v162
v_and_b32 v85, 0xffff0000, v162
v_lshlrev_b32 v86, 16, v163
v_and_b32 v87, 0xffff0000, v163
v_pk_add_f32 v[80:81], v[32:33], v[80:81]
v_pk_add_f32 v[82:83], v[34:35], v[82:83]
v_pk_add_f32 v[84:85], v[36:37], v[84:85]
v_pk_add_f32 v[86:87], v[38:39], v[86:87]
K 160,80,81
K 161,82,83
K 162,84,85
)"
R"(K 163,86,87
v_lshlrev_b32 v80, 16, v160
v_and_b32 v81, 0xffff0000, v160
v_lshlrev_b32 v82, 16, v161
v_and_b32 v83, 0xffff0000, v161
v_lshlrev_b32 v84, 16, v162
v_and_b32 v85, 0xffff0000, v162
v_lshlrev_b32 v86, 16, v163
v_and_b32 v87, 0xffff0000, v163
v_pk_add_f32 v[80:81], v[40:41], v[80:81]
v_pk_add_f32 v[82:83], v[42:43], v[82:83]
v_pk_add_f32 v[84:85], v[44:45], v[84:85]
v_pk_add_f32 v[86:87], v[46:47], v[86:87]
K 80,80,81
K 81,82,83
K 82,84,85
K 83,86,87
buffer_store_dwordx4 v[80:83], v20, s[20:23], s72 offen nt
s_waitcnt vmcnt(1)
W 0
Zvva 80,4,32
Zvva 84,8,32
Mvva 80,4,36
Mvva 84,8,36
Mvva 80,4,40
Mvva 84,8,40
Mvva 80,4,44
Mvva 84,8,44
Mvva 80,4,48
Mvva 84,8,48
Mvva 80,4,52
Mvva 84,8,52
Mvva 80,4,56
Mvva 84,8,56
Mvva 80,4,60
Mvva 84,8,60
s_nop 6
K 160,80,81
K 161,82,83
K 162,84,85
K 163,86,87
v_lshlrev_b32 v80, 16, v160
v_and_b32 v81, 0xffff0000, v160
v_lshlrev_b32 v82, 16, v161
v_and_b32 v83, 0xffff0000, v161
v_lshlrev_b32 v84, 16, v162
v_and_b32 v85, 0xffff0000, v162
v_lshlrev_b32 v86, 16, v163
v_and_b32 v87, 0xffff0000, v163
v_pk_add_f32 v[80:81], v[48:49], v[80:81]
v_pk_add_f32 v[82:83], v[50:51], v[82:83]
v_pk_add_f32 v[84:85], v[52:53], v[84:85]
v_pk_add_f32 v[86:87], v[54:55], v[86:87]
K 160,80,81
K 161,82,83
K 162,84,85
K 163,86,87
v_lshlrev_b32 v80, 16, v160
v_and_b32 v81, 0xffff0000, v160
v_lshlrev_b32 v82, 16, v161
v_and_b32 v83, 0xffff0000, v161
v_lshlrev_b32 v84, 16, v162
v_and_b32 v85, 0xffff0000, v162
v_lshlrev_b32 v86, 16, v163
v_and_b32 v87, 0xffff0000, v163
v_pk_add_f32 v[80:81], v[56:57], v[80:81]
v_pk_add_f32 v[82:83], v[58:59], v[82:83]
v_pk_add_f32 v[84:85], v[60:61], v[84:85]
v_pk_add_f32 v[86:87], v[62:63], v[86:87]
K 80,80,81
K 81,82,83
K 82,84,85
K 83,86,87
buffer_store_dwordx4 v[80:83], v20, s[20:23], s73 offen nt
s_branch .LL_126%=
.LL_167%=:
s_waitcnt vmcnt(0)
W 0
Zvva 80,4,32
Zvva 84,8,32
Mvva 80,4,36
Mvva 84,8,36
Mvva 80,4,40
Mvva 84,8,40
Mvva 80,4,44
Mvva 84,8,44
Mvva 80,4,48
Mvva 84,8,48
Mvva 80,4,52
Mvva 84,8,52
Mvva 80,4,56
Mvva 84,8,56
Mvva 80,4,60
Mvva 84,8,60
s_nop 6
K 160,80,81
K 161,82,83
K 162,84,85
K 163,86,87
v_lshlrev_b32 v80, 16, v160
v_and_b32 v81, 0xffff0000, v160
v_lshlrev_b32 v82, 16, v161
v_and_b32 v83, 0xffff0000, v161
v_lshlrev_b32 v84, 16, v162
v_and_b32 v85, 0xffff0000, v162
v_lshlrev_b32 v86, 16, v163
v_and_b32 v87, 0xffff0000, v163
v_pk_add_f32 v[80:81], v[48:49], v[80:81]
v_pk_add_f32 v[82:83], v[50:51], v[82:83]
v_pk_add_f32 v[84:85], v[52:53], v[84:85]
v_pk_add_f32 v[86:87], v[54:55], v[86:87]
K 160,80,81
K 161,82,83
K 162,84,85
K 163,86,87
v_lshlrev_b32 v80, 16, v160
v_and_b32 v81, 0xffff0000, v160
v_lshlrev_b32 v82, 16, v161
v_and_b32 v83, 0xffff0000, v161
v_lshlrev_b32 v84, 16, v162
v_and_b32 v85, 0xffff0000, v162
v_lshlrev_b32 v86, 16, v163
v_and_b32 v87, 0xffff0000, v163
v_pk_add_f32 v[80:81], v[56:57], v[80:81]
v_pk_add_f32 v[82:83], v[58:59], v[82:83]
v_pk_add_f32 v[84:85], v[60:61], v[84:85]
v_pk_add_f32 v[86:87], v[62:63], v[86:87]
K 80,80,81
K 81,82,83
K 82,84,85
K 83,86,87
buffer_store_dwordx4 v[80:83], v20, s[20:23], s73 offen nt
s_branch .LL_126%=
.LL_127%=:
s_mov_b32 s84, 0
.LL_169%=:
s_or_b32 s70, s71, s66
s_andn2_b32 s69, s67, s71
s_and_b32 s69, s69, 0xffff
s_cbranch_scc0 .LL_172%=
s_and_b32 s80, s69, 255
s_cbranch_scc0 .LL_173%=
s_bitcmp1_b32 s69, 0
s_cbranch_scc0 .LL_174%=
s_cmp_eq_u32 s59, 0
s_cbranch_scc0 .LL_175%=
Ra 0,21,0
Ra 216,1,4096
s_branch .LL_174%=
.LL_175%=:
buffer_load_dwordx4 a[0:3], v1, s[8:11], s78 offen offset:0 sc1
buffer_load_dwordx4 a[216:219], v1, s[8:11], s65 offen offset:0 sc1
.LL_174%=:
s_bitcmp1_b32 s69, 1
s_cbranch_scc0 .LL_176%=
s_cmp_eq_u32 s59, 1
s_cbranch_scc0 .LL_177%=
Ra 4,21,0
Ra 220,1,4096
s_branch .LL_176%=
.LL_177%=:
buffer_load_dwordx4 a[4:7], v1, s[8:11], s78 offen offset:1024 sc1
buffer_load_dwordx4 a[220:223], v1, s[8:11], s65 offen offset:1024 sc1
.LL_176%=:
s_bitcmp1_b32 s69, 2
s_cbranch_scc0 .LL_178%=
s_cmp_eq_u32 s59, 2
s_cbranch_scc0 .LL_179%=
Ra 8,21,0
Ra 224,1,4096
s_branch .LL_178%=
.LL_179%=:
buffer_load_dwordx4 a[8:11], v1, s[8:11], s78 offen offset:2048 sc1
buffer_load_dwordx4 a[224:227], v1, s[8:11], s65 offen offset:2048 sc1
.LL_178%=:
s_bitcmp1_b32 s69, 3
s_cbranch_scc0 .LL_180%=
s_cmp_eq_u32 s59, 3
s_cbranch_scc0 .LL_181%=
Ra 12,21,0
Ra 228,1,4096
s_branch .LL_180%=
.LL_181%=:
buffer_load_dwordx4 a[12:15], v1, s[8:11], s78 offen offset:3072 sc1
buffer_load_dwordx4 a[228:231], v1, s[8:11], s65 offen offset:3072 sc1
.LL_180%=:
s_bitcmp1_b32 s69, 4
s_cbranch_scc0 .LL_182%=
s_cmp_eq_u32 s59, 4
s_cbranch_scc0 .LL_183%=
Ra 16,21,0
Ra 232,1,4096
s_branch .LL_182%=
.LL_183%=:
buffer_load_dwordx4 a[16:19], v19, s[8:11], s78 offen offset:0 sc1
buffer_load_dwordx4 a[232:235], v19, s[8:11], s65 offen offset:0 sc1
.LL_182%=:
s_bitcmp1_b32 s69, 5
s_cbranch_scc0 .LL_184%=
s_cmp_eq_u32 s59, 5
s_cbranch_scc0 .LL_185%=
Ra 20,21,0
Ra 236,1,4096
s_branch .LL_184%=
.LL_185%=:
buffer_load_dwordx4 a[20:23], v19, s[8:11], s78 offen offset:1024 sc1
buffer_load_dwordx4 a[236:239], v19, s[8:11], s65 offen offset:1024 sc1
.LL_184%=:
s_bitcmp1_b32 s69, 6
s_cbranch_scc0 .LL_186%=
s_cmp_eq_u32 s59, 6
s_cbranch_scc0 .LL_187%=
Ra 24,21,0
Ra 240,1,4096
s_branch .LL_186%=
.LL_187%=:
buffer_load_dwordx4 a[24:27], v19, s[8:11], s78 offen offset:2048 sc1
buffer_load_dwordx4 a[240:243], v19, s[8:11], s65 offen offset:2048 sc1
.LL_186%=:
s_bitcmp1_b32 s69, 7
s_cbranch_scc0 .LL_188%=
s_cmp_eq_u32 s59, 7
s_cbranch_scc0 .LL_189%=
Ra 28,21,0
Ra 244,1,4096
s_branch .LL_188%=
.LL_189%=:
buffer_load_dwordx4 a[28:31], v19, s[8:11], s78 offen offset:3072 sc1
buffer_load_dwordx4 a[244:247], v19, s[8:11], s65 offen offset:3072 sc1
.LL_188%=:
.LL_173%=:
s_and_b32 s80, s69, 65280
s_cbranch_scc0 .LL_190%=
s_bitcmp1_b32 s69, 8
s_cbranch_scc0 .LL_191%=
s_cmp_eq_u32 s59, 0
s_cbranch_scc0 .LL_192%=
Ra 32,21,5120
s_branch .LL_191%=
.LL_192%=:
buffer_load_dwordx4 a[32:35], v1, s[8:11], s79 offen offset:0 sc1
.LL_191%=:
s_bitcmp1_b32 s69, 9
s_cbranch_scc0 .LL_193%=
s_cmp_eq_u32 s59, 1
s_cbranch_scc0 .LL_194%=
Ra 36,21,5120
s_branch .LL_193%=
.LL_194%=:
buffer_load_dwordx4 a[36:39], v1, s[8:11], s79 offen offset:1024 sc1
.LL_193%=:
s_bitcmp1_b32 s69, 10
s_cbranch_scc0 .LL_195%=
s_cmp_eq_u32 s59, 2
s_cbranch_scc0 .LL_196%=
Ra 40,21,5120
s_branch .LL_195%=
.LL_196%=:
buffer_load_dwordx4 a[40:43], v1, s[8:11], s79 offen offset:2048 sc1
.LL_195%=:
s_bitcmp1_b32 s69, 11
s_cbranch_scc0 .LL_197%=
s_cmp_eq_u32 s59, 3
s_cbranch_scc0 .LL_198%=
Ra 44,21,5120
s_branch .LL_197%=
.LL_198%=:
buffer_load_dwordx4 a[44:47], v1, s[8:11], s79 offen offset:3072 sc1
.LL_197%=:
s_bitcmp1_b32 s69, 12
s_cbranch_scc0 .LL_199%=
s_cmp_eq_u32 s59, 4
s_cbranch_scc0 .LL_200%=
Ra 48,21,5120
s_branch .LL_199%=
.LL_200%=:
buffer_load_dwordx4 a[48:51], v19, s[8:11], s79 offen offset:0 sc1
.LL_199%=:
s_bitcmp1_b32 s69, 13
s_cbranch_scc0 .LL_201%=
s_cmp_eq_u32 s59, 5
s_cbranch_scc0 .LL_202%=
Ra 52,21,5120
s_branch .LL_201%=
.LL_202%=:
buffer_load_dwordx4 a[52:55], v19, s[8:11], s79 offen offset:1024 sc1
.LL_201%=:
s_bitcmp1_b32 s69, 14
s_cbranch_scc0 .LL_203%=
s_cmp_eq_u32 s59, 6
s_cbranch_scc0 .LL_204%=
Ra 56,21,5120
s_branch .LL_203%=
.LL_204%=:
buffer_load_dwordx4 a[56:59], v19, s[8:11], s79 offen offset:2048 sc1
.LL_203%=:
s_bitcmp1_b32 s69, 15
s_cbranch_scc0 .LL_205%=
s_cmp_eq_u32 s59, 7
s_cbranch_scc0 .LL_206%=
Ra 60,21,5120
s_branch .LL_205%=
.LL_206%=:
buffer_load_dwordx4 a[60:63], v19, s[8:11], s79 offen offset:3072 sc1
.LL_205%=:
.LL_190%=:
s_or_b32 s71, s71, s69
.LL_172%=:
s_andn2_b32 s80, 0xffff, s71
s_cbranch_scc0 .LL_171%=
s_sleep 4
buffer_load_dwordx2 v[16:17], v18, s[8:11], s63 offen sc1
s_and_b32 s80, s70, 0x100ff
s_cmp_eq_u32 s80, 0xff
s_cbranch_scc0 .LL_170%=
W 0
Zvva 80,4,0
Zvva 84,8,0
Zvva 88,4,216
Zvva 92,8,216
Mvva 80,4,4
Mvva 84,8,4
Mvva 88,4,220
Mvva 92,8,220
Mvva 80,4,8
Mvva 84,8,8
Mvva 88,4,224
Mvva 92,8,224
Mvva 80,4,12
Mvva 84,8,12
Mvva 88,4,228
Mvva 92,8,228
Mvva 80,4,16
Mvva 84,8,16
Mvva 88,4,232
Mvva 92,8,232
Mvva 80,4,20
Mvva 84,8,20
Mvva 88,4,236
Mvva 92,8,236
Mvva 80,4,24
Mvva 84,8,24
Mvva 88,4,240
Mvva 92,8,240
Mvva 80,4,28
Mvva 84,8,28
Mvva 88,4,244
Mvva 92,8,244
s_nop 4
K 160,80,81
K 161,82,83
K 162,84,85
K 163,86,87
v_lshlrev_b32 v80, 16, v160
v_and_b32 v81, 0xffff0000, v160
v_lshlrev_b32 v82, 16, v161
v_and_b32 v83, 0xffff0000, v161
v_lshlrev_b32 v84, 16, v162
v_and_b32 v85, 0xffff0000, v162
v_lshlrev_b32 v86, 16, v163
v_and_b32 v87, 0xffff0000, v163
v_pk_add_f32 v[80:81], v[32:33], v[80:81]
v_pk_add_f32 v[82:83], v[34:35], v[82:83]
v_pk_add_f32 v[84:85], v[36:37], v[84:85]
v_pk_add_f32 v[86:87], v[38:39], v[86:87]
K 160,80,81
K 161,82,83
K 162,84,85
K 163,86,87
v_lshlrev_b32 v80, 16, v160
v_and_b32 v81, 0xffff0000, v160
v_lshlrev_b32 v82, 16, v161
v_and_b32 v83, 0xffff0000, v161
v_lshlrev_b32 v84, 16, v162
v_and_b32 v85, 0xffff0000, v162
v_lshlrev_b32 v86, 16, v163
v_and_b32 v87, 0xffff0000, v163
v_pk_add_f32 v[80:81], v[40:41], v[80:81]
v_pk_add_f32 v[82:83], v[42:43], v[82:83]
v_pk_add_f32 v[84:85], v[44:45], v[84:85]
v_pk_add_f32 v[86:87], v[46:47], v[86:87]
K 80,80,81
K 81,82,83
K 82,84,85
K 83,86,87
buffer_store_dwordx4 v[80:83], v20, s[20:23], s72 offen nt
K 160,88,89
K 161,90,91
K 162,92,93
K 163,94,95
v_lshlrev_b32 v88, 16, v160
v_and_b32 v89, 0xffff0000, v160
v_lshlrev_b32 v90, 16, v161
v_and_b32 v91, 0xffff0000, v161
v_lshlrev_b32 v92, 16, v162
v_and_b32 v93, 0xffff0000, v162
v_lshlrev_b32 v94, 16, v163
v_and_b32 v95, 0xffff0000, v163
v_pk_add_f32 v[88:89], v[64:65], v[88:89]
v_pk_add_f32 v[90:91], v[66:67], v[90:91]
)"
R"(v_pk_add_f32 v[92:93], v[68:69], v[92:93]
v_pk_add_f32 v[94:95], v[70:71], v[94:95]
K 160,88,89
K 161,90,91
K 162,92,93
K 163,94,95
v_lshlrev_b32 v88, 16, v160
v_and_b32 v89, 0xffff0000, v160
v_lshlrev_b32 v90, 16, v161
v_and_b32 v91, 0xffff0000, v161
v_lshlrev_b32 v92, 16, v162
v_and_b32 v93, 0xffff0000, v162
v_lshlrev_b32 v94, 16, v163
v_and_b32 v95, 0xffff0000, v163
v_pk_add_f32 v[88:89], v[72:73], v[88:89]
v_pk_add_f32 v[90:91], v[74:75], v[90:91]
v_pk_add_f32 v[92:93], v[76:77], v[92:93]
v_pk_add_f32 v[94:95], v[78:79], v[94:95]
K 88,88,89
K 89,90,91
K 90,92,93
K 91,94,95
buffer_store_dwordx4 v[88:91], v20, s[20:23], s74 offen nt
s_bitset1_b32 s71, 16
.LL_170%=:
s_add_u32 s84, s84, 1
s_waitcnt vmcnt(0)
v_cmp_eq_u64 vcc, s[12:13], v[16:17]
s_or_b32 s67, vcc_lo, s66
s_cmp_lt_u32 s84, 4194304
s_cbranch_scc1 .LL_169%=
.LL_171%=:
s_bitcmp1_b32 s71, 16
s_cbranch_scc1 .LL_207%=
s_and_b32 s80, s70, 0xff
s_cmp_eq_u32 s80, 0xff
s_cbranch_scc1 .LL_208%=
s_waitcnt vmcnt(0)
.LL_208%=:
W 0
Zvva 80,4,0
Zvva 84,8,0
Zvva 88,4,216
Zvva 92,8,216
Mvva 80,4,4
Mvva 84,8,4
Mvva 88,4,220
Mvva 92,8,220
Mvva 80,4,8
Mvva 84,8,8
Mvva 88,4,224
Mvva 92,8,224
Mvva 80,4,12
Mvva 84,8,12
Mvva 88,4,228
Mvva 92,8,228
Mvva 80,4,16
Mvva 84,8,16
Mvva 88,4,232
Mvva 92,8,232
Mvva 80,4,20
Mvva 84,8,20
Mvva 88,4,236
Mvva 92,8,236
Mvva 80,4,24
Mvva 84,8,24
Mvva 88,4,240
Mvva 92,8,240
Mvva 80,4,28
Mvva 84,8,28
Mvva 88,4,244
Mvva 92,8,244
s_nop 4
K 160,80,81
K 161,82,83
K 162,84,85
K 163,86,87
v_lshlrev_b32 v80, 16, v160
v_and_b32 v81, 0xffff0000, v160
v_lshlrev_b32 v82, 16, v161
v_and_b32 v83, 0xffff0000, v161
v_lshlrev_b32 v84, 16, v162
v_and_b32 v85, 0xffff0000, v162
v_lshlrev_b32 v86, 16, v163
v_and_b32 v87, 0xffff0000, v163
v_pk_add_f32 v[80:81], v[32:33], v[80:81]
v_pk_add_f32 v[82:83], v[34:35], v[82:83]
v_pk_add_f32 v[84:85], v[36:37], v[84:85]
v_pk_add_f32 v[86:87], v[38:39], v[86:87]
K 160,80,81
K 161,82,83
K 162,84,85
K 163,86,87
v_lshlrev_b32 v80, 16, v160
v_and_b32 v81, 0xffff0000, v160
v_lshlrev_b32 v82, 16, v161
v_and_b32 v83, 0xffff0000, v161
v_lshlrev_b32 v84, 16, v162
v_and_b32 v85, 0xffff0000, v162
v_lshlrev_b32 v86, 16, v163
v_and_b32 v87, 0xffff0000, v163
v_pk_add_f32 v[80:81], v[40:41], v[80:81]
v_pk_add_f32 v[82:83], v[42:43], v[82:83]
v_pk_add_f32 v[84:85], v[44:45], v[84:85]
v_pk_add_f32 v[86:87], v[46:47], v[86:87]
K 80,80,81
K 81,82,83
K 82,84,85
K 83,86,87
buffer_store_dwordx4 v[80:83], v20, s[20:23], s72 offen nt
K 160,88,89
K 161,90,91
K 162,92,93
K 163,94,95
v_lshlrev_b32 v88, 16, v160
v_and_b32 v89, 0xffff0000, v160
v_lshlrev_b32 v90, 16, v161
v_and_b32 v91, 0xffff0000, v161
v_lshlrev_b32 v92, 16, v162
v_and_b32 v93, 0xffff0000, v162
v_lshlrev_b32 v94, 16, v163
v_and_b32 v95, 0xffff0000, v163
v_pk_add_f32 v[88:89], v[64:65], v[88:89]
v_pk_add_f32 v[90:91], v[66:67], v[90:91]
v_pk_add_f32 v[92:93], v[68:69], v[92:93]
v_pk_add_f32 v[94:95], v[70:71], v[94:95]
K 160,88,89
K 161,90,91
K 162,92,93
K 163,94,95
v_lshlrev_b32 v88, 16, v160
v_and_b32 v89, 0xffff0000, v160
v_lshlrev_b32 v90, 16, v161
v_and_b32 v91, 0xffff0000, v161
v_lshlrev_b32 v92, 16, v162
v_and_b32 v93, 0xffff0000, v162
v_lshlrev_b32 v94, 16, v163
v_and_b32 v95, 0xffff0000, v163
v_pk_add_f32 v[88:89], v[72:73], v[88:89]
v_pk_add_f32 v[90:91], v[74:75], v[90:91]
v_pk_add_f32 v[92:93], v[76:77], v[92:93]
v_pk_add_f32 v[94:95], v[78:79], v[94:95]
K 88,88,89
K 89,90,91
K 90,92,93
K 91,94,95
buffer_store_dwordx4 v[88:91], v20, s[20:23], s74 offen nt
s_waitcnt vmcnt(2)
W 0
Zvva 80,4,32
Zvva 84,8,32
Mvva 80,4,36
Mvva 84,8,36
Mvva 80,4,40
Mvva 84,8,40
Mvva 80,4,44
Mvva 84,8,44
Mvva 80,4,48
Mvva 84,8,48
Mvva 80,4,52
Mvva 84,8,52
Mvva 80,4,56
Mvva 84,8,56
Mvva 80,4,60
Mvva 84,8,60
s_nop 6
K 160,80,81
K 161,82,83
K 162,84,85
K 163,86,87
v_lshlrev_b32 v80, 16, v160
v_and_b32 v81, 0xffff0000, v160
v_lshlrev_b32 v82, 16, v161
v_and_b32 v83, 0xffff0000, v161
v_lshlrev_b32 v84, 16, v162
v_and_b32 v85, 0xffff0000, v162
v_lshlrev_b32 v86, 16, v163
v_and_b32 v87, 0xffff0000, v163
v_pk_add_f32 v[80:81], v[48:49], v[80:81]
v_pk_add_f32 v[82:83], v[50:51], v[82:83]
v_pk_add_f32 v[84:85], v[52:53], v[84:85]
v_pk_add_f32 v[86:87], v[54:55], v[86:87]
K 160,80,81
K 161,82,83
K 162,84,85
K 163,86,87
v_lshlrev_b32 v80, 16, v160
v_and_b32 v81, 0xffff0000, v160
v_lshlrev_b32 v82, 16, v161
v_and_b32 v83, 0xffff0000, v161
v_lshlrev_b32 v84, 16, v162
v_and_b32 v85, 0xffff0000, v162
v_lshlrev_b32 v86, 16, v163
v_and_b32 v87, 0xffff0000, v163
v_pk_add_f32 v[80:81], v[56:57], v[80:81]
v_pk_add_f32 v[82:83], v[58:59], v[82:83]
v_pk_add_f32 v[84:85], v[60:61], v[84:85]
v_pk_add_f32 v[86:87], v[62:63], v[86:87]
K 80,80,81
K 81,82,83
K 82,84,85
K 83,86,87
buffer_store_dwordx4 v[80:83], v20, s[20:23], s73 offen nt
s_branch .LL_126%=
.LL_207%=:
s_waitcnt vmcnt(0)
W 0
Zvva 80,4,32
Zvva 84,8,32
Mvva 80,4,36
Mvva 84,8,36
Mvva 80,4,40
Mvva 84,8,40
Mvva 80,4,44
Mvva 84,8,44
Mvva 80,4,48
Mvva 84,8,48
Mvva 80,4,52
Mvva 84,8,52
Mvva 80,4,56
Mvva 84,8,56
Mvva 80,4,60
Mvva 84,8,60
s_nop 6
K 160,80,81
K 161,82,83
K 162,84,85
K 163,86,87
v_lshlrev_b32 v80, 16, v160
v_and_b32 v81, 0xffff0000, v160
v_lshlrev_b32 v82, 16, v161
v_and_b32 v83, 0xffff0000, v161
v_lshlrev_b32 v84, 16, v162
v_and_b32 v85, 0xffff0000, v162
v_lshlrev_b32 v86, 16, v163
v_and_b32 v87, 0xffff0000, v163
v_pk_add_f32 v[80:81], v[48:49], v[80:81]
v_pk_add_f32 v[82:83], v[50:51], v[82:83]
v_pk_add_f32 v[84:85], v[52:53], v[84:85]
v_pk_add_f32 v[86:87], v[54:55], v[86:87]
K 160,80,81
K 161,82,83
K 162,84,85
K 163,86,87
v_lshlrev_b32 v80, 16, v160
v_and_b32 v81, 0xffff0000, v160
v_lshlrev_b32 v82, 16, v161
v_and_b32 v83, 0xffff0000, v161
v_lshlrev_b32 v84, 16, v162
v_and_b32 v85, 0xffff0000, v162
v_lshlrev_b32 v86, 16, v163
v_and_b32 v87, 0xffff0000, v163
v_pk_add_f32 v[80:81], v[56:57], v[80:81]
v_pk_add_f32 v[82:83], v[58:59], v[82:83]
v_pk_add_f32 v[84:85], v[60:61], v[84:85]
v_pk_add_f32 v[86:87], v[62:63], v[86:87]
K 80,80,81
K 81,82,83
K 82,84,85
K 83,86,87
buffer_store_dwordx4 v[80:83], v20, s[20:23], s73 offen nt
s_branch .LL_126%=
.LL_128%=:
s_mov_b32 s84, 0
.LL_209%=:
s_or_b32 s70, s71, s66
s_andn2_b32 s69, s67, s71
s_and_b32 s69, s69, 0xffff
s_cbranch_scc0 .LL_212%=
s_and_b32 s80, s69, 255
s_cbranch_scc0 .LL_213%=
s_bitcmp1_b32 s69, 0
s_cbranch_scc0 .LL_214%=
s_cmp_eq_u32 s59, 0
s_cbranch_scc0 .LL_215%=
Ra 0,21,0
s_branch .LL_214%=
.LL_215%=:
buffer_load_dwordx4 a[0:3], v1, s[8:11], s78 offen offset:0 sc1
.LL_214%=:
s_bitcmp1_b32 s69, 1
s_cbranch_scc0 .LL_216%=
s_cmp_eq_u32 s59, 1
s_cbranch_scc0 .LL_217%=
Ra 4,21,0
s_branch .LL_216%=
.LL_217%=:
buffer_load_dwordx4 a[4:7], v1, s[8:11], s78 offen offset:1024 sc1
.LL_216%=:
s_bitcmp1_b32 s69, 2
s_cbranch_scc0 .LL_218%=
s_cmp_eq_u32 s59, 2
s_cbranch_scc0 .LL_219%=
Ra 8,21,0
s_branch .LL_218%=
.LL_219%=:
buffer_load_dwordx4 a[8:11], v1, s[8:11], s78 offen offset:2048 sc1
.LL_218%=:
s_bitcmp1_b32 s69, 3
s_cbranch_scc0 .LL_220%=
s_cmp_eq_u32 s59, 3
s_cbranch_scc0 .LL_221%=
Ra 12,21,0
s_branch .LL_220%=
.LL_221%=:
buffer_load_dwordx4 a[12:15], v1, s[8:11], s78 offen offset:3072 sc1
.LL_220%=:
s_bitcmp1_b32 s69, 4
s_cbranch_scc0 .LL_222%=
s_cmp_eq_u32 s59, 4
s_cbranch_scc0 .LL_223%=
Ra 16,21,0
s_branch .LL_222%=
.LL_223%=:
buffer_load_dwordx4 a[16:19], v19, s[8:11], s78 offen offset:0 sc1
.LL_222%=:
s_bitcmp1_b32 s69, 5
s_cbranch_scc0 .LL_224%=
s_cmp_eq_u32 s59, 5
s_cbranch_scc0 .LL_225%=
Ra 20,21,0
s_branch .LL_224%=
.LL_225%=:
buffer_load_dwordx4 a[20:23], v19, s[8:11], s78 offen offset:1024 sc1
.LL_224%=:
s_bitcmp1_b32 s69, 6
s_cbranch_scc0 .LL_226%=
s_cmp_eq_u32 s59, 6
s_cbranch_scc0 .LL_227%=
Ra 24,21,0
s_branch .LL_226%=
.LL_227%=:
buffer_load_dwordx4 a[24:27], v19, s[8:11], s78 offen offset:2048 sc1
.LL_226%=:
s_bitcmp1_b32 s69, 7
s_cbranch_scc0 .LL_228%=
s_cmp_eq_u32 s59, 7
s_cbranch_scc0 .LL_229%=
Ra 28,21,0
s_branch .LL_228%=
.LL_229%=:
buffer_load_dwordx4 a[28:31], v19, s[8:11], s78 offen offset:3072 sc1
.LL_228%=:
.LL_213%=:
s_and_b32 s80, s69, 65280
s_cbranch_scc0 .LL_230%=
s_bitcmp1_b32 s69, 8
s_cbranch_scc0 .LL_231%=
s_cmp_eq_u32 s59, 0
s_cbranch_scc0 .LL_232%=
Ra 32,21,5120
Ra 216,1,9216
s_branch .LL_231%=
.LL_232%=:
buffer_load_dwordx4 a[32:35], v1, s[8:11], s79 offen offset:0 sc1
buffer_load_dwordx4 a[216:219], v1, s[8:11], s65 offen offset:0 sc1
.LL_231%=:
s_bitcmp1_b32 s69, 9
s_cbranch_scc0 .LL_233%=
s_cmp_eq_u32 s59, 1
s_cbranch_scc0 .LL_234%=
Ra 36,21,5120
Ra 220,1,9216
s_branch .LL_233%=
.LL_234%=:
buffer_load_dwordx4 a[36:39], v1, s[8:11], s79 offen offset:1024 sc1
buffer_load_dwordx4 a[220:223], v1, s[8:11], s65 offen offset:1024 sc1
.LL_233%=:
s_bitcmp1_b32 s69, 10
s_cbranch_scc0 .LL_235%=
s_cmp_eq_u32 s59, 2
s_cbranch_scc0 .LL_236%=
Ra 40,21,5120
Ra 224,1,9216
s_branch .LL_235%=
.LL_236%=:
buffer_load_dwordx4 a[40:43], v1, s[8:11], s79 offen offset:2048 sc1
buffer_load_dwordx4 a[224:227], v1, s[8:11], s65 offen offset:2048 sc1
.LL_235%=:
s_bitcmp1_b32 s69, 11
s_cbranch_scc0 .LL_237%=
s_cmp_eq_u32 s59, 3
s_cbranch_scc0 .LL_238%=
Ra 44,21,5120
Ra 228,1,9216
s_branch .LL_237%=
.LL_238%=:
buffer_load_dwordx4 a[44:47], v1, s[8:11], s79 offen offset:3072 sc1
buffer_load_dwordx4 a[228:231], v1, s[8:11], s65 offen offset:3072 sc1
.LL_237%=:
s_bitcmp1_b32 s69, 12
s_cbranch_scc0 .LL_239%=
s_cmp_eq_u32 s59, 4
s_cbranch_scc0 .LL_240%=
Ra 48,21,5120
Ra 232,1,9216
s_branch .LL_239%=
.LL_240%=:
buffer_load_dwordx4 a[48:51], v19, s[8:11], s79 offen offset:0 sc1
buffer_load_dwordx4 a[232:235], v19, s[8:11], s65 offen offset:0 sc1
)"
R"(.LL_239%=:
s_bitcmp1_b32 s69, 13
s_cbranch_scc0 .LL_241%=
s_cmp_eq_u32 s59, 5
s_cbranch_scc0 .LL_242%=
Ra 52,21,5120
Ra 236,1,9216
s_branch .LL_241%=
.LL_242%=:
buffer_load_dwordx4 a[52:55], v19, s[8:11], s79 offen offset:1024 sc1
buffer_load_dwordx4 a[236:239], v19, s[8:11], s65 offen offset:1024 sc1
.LL_241%=:
s_bitcmp1_b32 s69, 14
s_cbranch_scc0 .LL_243%=
s_cmp_eq_u32 s59, 6
s_cbranch_scc0 .LL_244%=
Ra 56,21,5120
Ra 240,1,9216
s_branch .LL_243%=
.LL_244%=:
buffer_load_dwordx4 a[56:59], v19, s[8:11], s79 offen offset:2048 sc1
buffer_load_dwordx4 a[240:243], v19, s[8:11], s65 offen offset:2048 sc1
.LL_243%=:
s_bitcmp1_b32 s69, 15
s_cbranch_scc0 .LL_245%=
s_cmp_eq_u32 s59, 7
s_cbranch_scc0 .LL_246%=
Ra 60,21,5120
Ra 244,1,9216
s_branch .LL_245%=
.LL_246%=:
buffer_load_dwordx4 a[60:63], v19, s[8:11], s79 offen offset:3072 sc1
buffer_load_dwordx4 a[244:247], v19, s[8:11], s65 offen offset:3072 sc1
.LL_245%=:
.LL_230%=:
s_or_b32 s71, s71, s69
.LL_212%=:
s_andn2_b32 s80, 0xffff, s71
s_cbranch_scc0 .LL_211%=
s_sleep 4
buffer_load_dwordx2 v[16:17], v18, s[8:11], s63 offen sc1
s_and_b32 s80, s70, 0x100ff
s_cmp_eq_u32 s80, 0xff
s_cbranch_scc0 .LL_210%=
W 0
Zvva 80,4,0
Zvva 84,8,0
Mvva 80,4,4
Mvva 84,8,4
Mvva 80,4,8
Mvva 84,8,8
Mvva 80,4,12
Mvva 84,8,12
Mvva 80,4,16
Mvva 84,8,16
Mvva 80,4,20
Mvva 84,8,20
Mvva 80,4,24
Mvva 84,8,24
Mvva 80,4,28
Mvva 84,8,28
s_nop 6
K 160,80,81
K 161,82,83
K 162,84,85
K 163,86,87
v_lshlrev_b32 v80, 16, v160
v_and_b32 v81, 0xffff0000, v160
v_lshlrev_b32 v82, 16, v161
v_and_b32 v83, 0xffff0000, v161
v_lshlrev_b32 v84, 16, v162
v_and_b32 v85, 0xffff0000, v162
v_lshlrev_b32 v86, 16, v163
v_and_b32 v87, 0xffff0000, v163
v_pk_add_f32 v[80:81], v[32:33], v[80:81]
v_pk_add_f32 v[82:83], v[34:35], v[82:83]
v_pk_add_f32 v[84:85], v[36:37], v[84:85]
v_pk_add_f32 v[86:87], v[38:39], v[86:87]
K 160,80,81
K 161,82,83
K 162,84,85
K 163,86,87
v_lshlrev_b32 v80, 16, v160
v_and_b32 v81, 0xffff0000, v160
v_lshlrev_b32 v82, 16, v161
v_and_b32 v83, 0xffff0000, v161
v_lshlrev_b32 v84, 16, v162
v_and_b32 v85, 0xffff0000, v162
v_lshlrev_b32 v86, 16, v163
v_and_b32 v87, 0xffff0000, v163
v_pk_add_f32 v[80:81], v[40:41], v[80:81]
v_pk_add_f32 v[82:83], v[42:43], v[82:83]
v_pk_add_f32 v[84:85], v[44:45], v[84:85]
v_pk_add_f32 v[86:87], v[46:47], v[86:87]
K 80,80,81
K 81,82,83
K 82,84,85
K 83,86,87
buffer_store_dwordx4 v[80:83], v20, s[20:23], s72 offen nt
s_bitset1_b32 s71, 16
.LL_210%=:
s_add_u32 s84, s84, 1
s_waitcnt vmcnt(0)
v_cmp_eq_u64 vcc, s[12:13], v[16:17]
s_or_b32 s67, vcc_lo, s66
s_cmp_lt_u32 s84, 4194304
s_cbranch_scc1 .LL_209%=
.LL_211%=:
s_bitcmp1_b32 s71, 16
s_cbranch_scc1 .LL_247%=
s_and_b32 s80, s70, 0xff
s_cmp_eq_u32 s80, 0xff
s_cbranch_scc1 .LL_248%=
s_waitcnt vmcnt(0)
.LL_248%=:
W 0
Zvva 80,4,0
Zvva 84,8,0
Mvva 80,4,4
Mvva 84,8,4
Mvva 80,4,8
Mvva 84,8,8
Mvva 80,4,12
Mvva 84,8,12
Mvva 80,4,16
Mvva 84,8,16
Mvva 80,4,20
Mvva 84,8,20
Mvva 80,4,24
Mvva 84,8,24
Mvva 80,4,28
Mvva 84,8,28
s_nop 6
K 160,80,81
K 161,82,83
K 162,84,85
K 163,86,87
v_lshlrev_b32 v80, 16, v160
v_and_b32 v81, 0xffff0000, v160
v_lshlrev_b32 v82, 16, v161
v_and_b32 v83, 0xffff0000, v161
v_lshlrev_b32 v84, 16, v162
v_and_b32 v85, 0xffff0000, v162
v_lshlrev_b32 v86, 16, v163
v_and_b32 v87, 0xffff0000, v163
v_pk_add_f32 v[80:81], v[32:33], v[80:81]
v_pk_add_f32 v[82:83], v[34:35], v[82:83]
v_pk_add_f32 v[84:85], v[36:37], v[84:85]
v_pk_add_f32 v[86:87], v[38:39], v[86:87]
K 160,80,81
K 161,82,83
K 162,84,85
K 163,86,87
v_lshlrev_b32 v80, 16, v160
v_and_b32 v81, 0xffff0000, v160
v_lshlrev_b32 v82, 16, v161
v_and_b32 v83, 0xffff0000, v161
v_lshlrev_b32 v84, 16, v162
v_and_b32 v85, 0xffff0000, v162
v_lshlrev_b32 v86, 16, v163
v_and_b32 v87, 0xffff0000, v163
v_pk_add_f32 v[80:81], v[40:41], v[80:81]
v_pk_add_f32 v[82:83], v[42:43], v[82:83]
v_pk_add_f32 v[84:85], v[44:45], v[84:85]
v_pk_add_f32 v[86:87], v[46:47], v[86:87]
K 80,80,81
K 81,82,83
K 82,84,85
K 83,86,87
buffer_store_dwordx4 v[80:83], v20, s[20:23], s72 offen nt
s_waitcnt vmcnt(1)
W 0
Zvva 80,4,32
Zvva 84,8,32
Zvva 88,4,216
Zvva 92,8,216
Mvva 80,4,36
Mvva 84,8,36
Mvva 88,4,220
Mvva 92,8,220
Mvva 80,4,40
Mvva 84,8,40
Mvva 88,4,224
Mvva 92,8,224
Mvva 80,4,44
Mvva 84,8,44
Mvva 88,4,228
Mvva 92,8,228
Mvva 80,4,48
Mvva 84,8,48
Mvva 88,4,232
Mvva 92,8,232
Mvva 80,4,52
Mvva 84,8,52
Mvva 88,4,236
Mvva 92,8,236
Mvva 80,4,56
Mvva 84,8,56
Mvva 88,4,240
Mvva 92,8,240
Mvva 80,4,60
Mvva 84,8,60
Mvva 88,4,244
Mvva 92,8,244
s_nop 4
K 160,80,81
K 161,82,83
K 162,84,85
K 163,86,87
v_lshlrev_b32 v80, 16, v160
v_and_b32 v81, 0xffff0000, v160
v_lshlrev_b32 v82, 16, v161
v_and_b32 v83, 0xffff0000, v161
v_lshlrev_b32 v84, 16, v162
v_and_b32 v85, 0xffff0000, v162
v_lshlrev_b32 v86, 16, v163
v_and_b32 v87, 0xffff0000, v163
v_pk_add_f32 v[80:81], v[48:49], v[80:81]
v_pk_add_f32 v[82:83], v[50:51], v[82:83]
v_pk_add_f32 v[84:85], v[52:53], v[84:85]
v_pk_add_f32 v[86:87], v[54:55], v[86:87]
K 160,80,81
K 161,82,83
K 162,84,85
K 163,86,87
v_lshlrev_b32 v80, 16, v160
v_and_b32 v81, 0xffff0000, v160
v_lshlrev_b32 v82, 16, v161
v_and_b32 v83, 0xffff0000, v161
v_lshlrev_b32 v84, 16, v162
v_and_b32 v85, 0xffff0000, v162
v_lshlrev_b32 v86, 16, v163
v_and_b32 v87, 0xffff0000, v163
v_pk_add_f32 v[80:81], v[56:57], v[80:81]
v_pk_add_f32 v[82:83], v[58:59], v[82:83]
v_pk_add_f32 v[84:85], v[60:61], v[84:85]
v_pk_add_f32 v[86:87], v[62:63], v[86:87]
K 80,80,81
K 81,82,83
K 82,84,85
K 83,86,87
buffer_store_dwordx4 v[80:83], v20, s[20:23], s73 offen nt
K 160,88,89
K 161,90,91
K 162,92,93
K 163,94,95
v_lshlrev_b32 v88, 16, v160
v_and_b32 v89, 0xffff0000, v160
v_lshlrev_b32 v90, 16, v161
v_and_b32 v91, 0xffff0000, v161
v_lshlrev_b32 v92, 16, v162
v_and_b32 v93, 0xffff0000, v162
v_lshlrev_b32 v94, 16, v163
v_and_b32 v95, 0xffff0000, v163
v_pk_add_f32 v[88:89], v[64:65], v[88:89]
v_pk_add_f32 v[90:91], v[66:67], v[90:91]
v_pk_add_f32 v[92:93], v[68:69], v[92:93]
v_pk_add_f32 v[94:95], v[70:71], v[94:95]
K 160,88,89
K 161,90,91
K 162,92,93
K 163,94,95
v_lshlrev_b32 v88, 16, v160
v_and_b32 v89, 0xffff0000, v160
v_lshlrev_b32 v90, 16, v161
v_and_b32 v91, 0xffff0000, v161
v_lshlrev_b32 v92, 16, v162
v_and_b32 v93, 0xffff0000, v162
v_lshlrev_b32 v94, 16, v163
v_and_b32 v95, 0xffff0000, v163
v_pk_add_f32 v[88:89], v[72:73], v[88:89]
v_pk_add_f32 v[90:91], v[74:75], v[90:91]
v_pk_add_f32 v[92:93], v[76:77], v[92:93]
v_pk_add_f32 v[94:95], v[78:79], v[94:95]
K 88,88,89
K 89,90,91
K 90,92,93
K 91,94,95
buffer_store_dwordx4 v[88:91], v20, s[20:23], s74 offen nt
s_branch .LL_126%=
.LL_247%=:
s_waitcnt vmcnt(0)
W 0
Zvva 80,4,32
Zvva 84,8,32
Zvva 88,4,216
Zvva 92,8,216
Mvva 80,4,36
Mvva 84,8,36
Mvva 88,4,220
Mvva 92,8,220
Mvva 80,4,40
Mvva 84,8,40
Mvva 88,4,224
Mvva 92,8,224
Mvva 80,4,44
Mvva 84,8,44
Mvva 88,4,228
Mvva 92,8,228
Mvva 80,4,48
Mvva 84,8,48
Mvva 88,4,232
Mvva 92,8,232
Mvva 80,4,52
Mvva 84,8,52
Mvva 88,4,236
Mvva 92,8,236
Mvva 80,4,56
Mvva 84,8,56
Mvva 88,4,240
Mvva 92,8,240
Mvva 80,4,60
Mvva 84,8,60
Mvva 88,4,244
Mvva 92,8,244
s_nop 4
K 160,80,81
K 161,82,83
K 162,84,85
K 163,86,87
v_lshlrev_b32 v80, 16, v160
v_and_b32 v81, 0xffff0000, v160
v_lshlrev_b32 v82, 16, v161
v_and_b32 v83, 0xffff0000, v161
v_lshlrev_b32 v84, 16, v162
v_and_b32 v85, 0xffff0000, v162
v_lshlrev_b32 v86, 16, v163
v_and_b32 v87, 0xffff0000, v163
v_pk_add_f32 v[80:81], v[48:49], v[80:81]
v_pk_add_f32 v[82:83], v[50:51], v[82:83]
v_pk_add_f32 v[84:85], v[52:53], v[84:85]
v_pk_add_f32 v[86:87], v[54:55], v[86:87]
K 160,80,81
K 161,82,83
K 162,84,85
K 163,86,87
v_lshlrev_b32 v80, 16, v160
v_and_b32 v81, 0xffff0000, v160
v_lshlrev_b32 v82, 16, v161
v_and_b32 v83, 0xffff0000, v161
v_lshlrev_b32 v84, 16, v162
v_and_b32 v85, 0xffff0000, v162
v_lshlrev_b32 v86, 16, v163
v_and_b32 v87, 0xffff0000, v163
v_pk_add_f32 v[80:81], v[56:57], v[80:81]
v_pk_add_f32 v[82:83], v[58:59], v[82:83]
v_pk_add_f32 v[84:85], v[60:61], v[84:85]
v_pk_add_f32 v[86:87], v[62:63], v[86:87]
K 80,80,81
K 81,82,83
K 82,84,85
K 83,86,87
buffer_store_dwordx4 v[80:83], v20, s[20:23], s73 offen nt
K 160,88,89
K 161,90,91
K 162,92,93
K 163,94,95
v_lshlrev_b32 v88, 16, v160
v_and_b32 v89, 0xffff0000, v160
v_lshlrev_b32 v90, 16, v161
v_and_b32 v91, 0xffff0000, v161
v_lshlrev_b32 v92, 16, v162
v_and_b32 v93, 0xffff0000, v162
v_lshlrev_b32 v94, 16, v163
v_and_b32 v95, 0xffff0000, v163
v_pk_add_f32 v[88:89], v[64:65], v[88:89]
v_pk_add_f32 v[90:91], v[66:67], v[90:91]
v_pk_add_f32 v[92:93], v[68:69], v[92:93]
v_pk_add_f32 v[94:95], v[70:71], v[94:95]
K 160,88,89
K 161,90,91
K 162,92,93
K 163,94,95
v_lshlrev_b32 v88, 16, v160
v_and_b32 v89, 0xffff0000, v160
v_lshlrev_b32 v90, 16, v161
v_and_b32 v91, 0xffff0000, v161
v_lshlrev_b32 v92, 16, v162
v_and_b32 v93, 0xffff0000, v162
v_lshlrev_b32 v94, 16, v163
v_and_b32 v95, 0xffff0000, v163
v_pk_add_f32 v[88:89], v[72:73], v[88:89]
v_pk_add_f32 v[90:91], v[74:75], v[90:91]
v_pk_add_f32 v[92:93], v[76:77], v[92:93]
v_pk_add_f32 v[94:95], v[78:79], v[94:95]
K 88,88,89
K 89,90,91
K 90,92,93
K 91,94,95
buffer_store_dwordx4 v[88:91], v20, s[20:23], s74 offen nt
.LL_126%=:
s_lshl_b32 s80, s15, 2
s_add_u32 s80, s80, s14
s_cmp_eq_u32 s80, 4
s_cbranch_scc0 .LL_249%=
v_mov_b64 v[28:29], s[12:13]
s_mov_b64 exec, 1
buffer_store_dwordx2 v[28:29], off, s[8:11], 0 sc1
s_mov_b64 exec, -1
.LL_249%=:
)"
R"(s_branch .Lend_250%=
.Lup_119%=:
s_waitcnt vmcnt(3)
s_mov_b32 s84, 0
.LL_121%=:
v_min3_u32 v176, v101, v103, v109
v_max3_u32 v177, v101, v103, v109
v_min3_u32 v176, v176, v111, v117
v_max3_u32 v177, v177, v111, v117
v_min3_u32 v176, v176, v119, v125
v_max3_u32 v177, v177, v119, v125
v_min_u32 v176, v176, v127
v_max_u32 v177, v177, v127
v_cmp_eq_u32 vcc, s13, v176
v_cmp_eq_u32_e64 s[86:87], s13, v177
s_and_b64 vcc, vcc, s[86:87]
s_cmp_eq_u64 vcc, -1
s_cbranch_scc1 .LL_122%=
s_add_u32 s84, s84, 1
s_cmp_lt_u32 s84, 1048576
s_cbranch_scc1 .LL_123%=
.LL_122%=:
v_readlane_b32 s70, v12, s69
v_bfe_u32 v2, v1, 4, 4
s_nop 2
s_lshr_b32 s80, s70, 4
s_and_b32 s70, s70, 15
v_cmp_eq_u32 vcc, s70, v2
s_cmp_eq_u32 s80, s61
s_cselect_b64 s[86:87], vcc, 0
s_cselect_b64 s[88:89], 0, vcc
s_bitcmp1_b32 s14, 0
s_cselect_b64 vcc, s[88:89], s[86:87]
v_lshlrev_b32 v176, 16, v100
v_and_b32 v177, 0xffff0000, v100
v_lshlrev_b32 v178, 16, v102
v_and_b32 v179, 0xffff0000, v102
v_lshlrev_b32 v180, 16, v108
v_and_b32 v181, 0xffff0000, v108
v_lshlrev_b32 v182, 16, v110
v_and_b32 v183, 0xffff0000, v110
A 176,176,40
A 177,177,41
A 178,178,42
A 179,179,43
A 180,180,44
A 181,181,45
A 182,182,46
A 183,183,47
K 184,176,177
K 185,178,179
K 186,180,181
K 187,182,183
v_lshlrev_b32 v176, 16, v184
v_and_b32 v177, 0xffff0000, v184
v_lshlrev_b32 v178, 16, v185
v_and_b32 v179, 0xffff0000, v185
v_lshlrev_b32 v180, 16, v186
v_and_b32 v181, 0xffff0000, v186
v_lshlrev_b32 v182, 16, v187
v_and_b32 v183, 0xffff0000, v187
v_cndmask_b32_e64 v40, v40, v176, s[86:87]
v_cndmask_b32_e64 v41, v41, v177, s[86:87]
v_cndmask_b32_e64 v42, v42, v178, s[86:87]
v_cndmask_b32_e64 v43, v43, v179, s[86:87]
v_cndmask_b32_e64 v44, v44, v180, s[86:87]
v_cndmask_b32_e64 v45, v45, v181, s[86:87]
v_cndmask_b32_e64 v46, v46, v182, s[86:87]
v_cndmask_b32_e64 v47, v47, v183, s[86:87]
v_lshlrev_b32 v176, 16, v100
v_and_b32 v177, 0xffff0000, v100
v_lshlrev_b32 v178, 16, v102
v_and_b32 v179, 0xffff0000, v102
v_lshlrev_b32 v180, 16, v108
v_and_b32 v181, 0xffff0000, v108
v_lshlrev_b32 v182, 16, v110
v_and_b32 v183, 0xffff0000, v110
A 176,176,56
A 177,177,57
A 178,178,58
A 179,179,59
A 180,180,60
A 181,181,61
A 182,182,62
A 183,183,63
K 184,176,177
K 185,178,179
K 186,180,181
K 187,182,183
v_lshlrev_b32 v176, 16, v184
v_and_b32 v177, 0xffff0000, v184
v_lshlrev_b32 v178, 16, v185
v_and_b32 v179, 0xffff0000, v185
v_lshlrev_b32 v180, 16, v186
v_and_b32 v181, 0xffff0000, v186
v_lshlrev_b32 v182, 16, v187
v_and_b32 v183, 0xffff0000, v187
v_cndmask_b32_e64 v56, v56, v176, s[88:89]
v_cndmask_b32_e64 v57, v57, v177, s[88:89]
v_cndmask_b32_e64 v58, v58, v178, s[88:89]
v_cndmask_b32_e64 v59, v59, v179, s[88:89]
v_cndmask_b32_e64 v60, v60, v180, s[88:89]
v_cndmask_b32_e64 v61, v61, v181, s[88:89]
v_cndmask_b32_e64 v62, v62, v182, s[88:89]
v_cndmask_b32_e64 v63, v63, v183, s[88:89]
v_lshlrev_b32 v176, 16, v116
v_and_b32 v177, 0xffff0000, v116
v_lshlrev_b32 v178, 16, v118
v_and_b32 v179, 0xffff0000, v118
v_lshlrev_b32 v180, 16, v124
v_and_b32 v181, 0xffff0000, v124
v_lshlrev_b32 v182, 16, v126
v_and_b32 v183, 0xffff0000, v126
A 176,176,72
A 177,177,73
A 178,178,74
A 179,179,75
A 180,180,76
A 181,181,77
A 182,182,78
A 183,183,79
K 184,176,177
K 185,178,179
K 186,180,181
K 187,182,183
v_lshlrev_b32 v176, 16, v184
v_and_b32 v177, 0xffff0000, v184
v_lshlrev_b32 v178, 16, v185
v_and_b32 v179, 0xffff0000, v185
v_lshlrev_b32 v180, 16, v186
v_and_b32 v181, 0xffff0000, v186
v_lshlrev_b32 v182, 16, v187
v_and_b32 v183, 0xffff0000, v187
v_cndmask_b32_e64 v72, v72, v176, vcc
v_cndmask_b32_e64 v73, v73, v177, vcc
v_cndmask_b32_e64 v74, v74, v178, vcc
v_cndmask_b32_e64 v75, v75, v179, vcc
v_cndmask_b32_e64 v76, v76, v180, vcc
v_cndmask_b32_e64 v77, v77, v181, vcc
v_cndmask_b32_e64 v78, v78, v182, vcc
v_cndmask_b32_e64 v79, v79, v183, vcc
s_bitset0_b32 s68, s69
s_cmp_eq_u32 s68, 0
s_cbranch_scc1 .Lupr_120%=
s_ff1_i32_b32 s69, s68
.LL_123%=:
s_cmp_eq_u32 s64, 0
s_cbranch_scc0 .LL_124%=
s_waitcnt vmcnt(0)
v_lshlrev_b32 v2, 2, v1
v_mov_b64 v[28:29], s[12:13]
s_mov_b64 exec, 3
buffer_store_dwordx2 v[28:29], v2, s[8:11], s53 offen offset:512 sc1
s_mov_b64 exec, -1
s_mov_b32 s64, 1
.LL_124%=:
s_mul_i32 s66, s69, 20
s_mul_i32 s67, s58, 5
s_add_u32 s66, s66, s67
s_lshl_b32 s66, s66, 7
s_add_u32 s66, s66, 98304
s_lshl_b32 s67, s14, 7
s_add_u32 s67, s67, s66
buffer_load_dwordx4 v[100:103], v3, s[8:11], s67 offen offset:0 sc1
buffer_load_dwordx4 v[108:111], v3, s[8:11], s67 offen offset:16 sc1
buffer_load_dwordx4 v[116:119], v3, s[8:11], s66 offen offset:512 sc1
buffer_load_dwordx4 v[124:127], v3, s[8:11], s66 offen offset:528 sc1
s_waitcnt vmcnt(0)
s_branch .LL_121%=
.Lspin_52%=:
s_mov_b32 s84, 0
.Llp_54%=:
s_sleep 2
v_mov_b32 v28, s74
buffer_load_dwordx2 v[28:29], v28, s[8:11], 0 offen offset:3584 sc1
s_waitcnt vmcnt(0)
s_add_u32 s84, s84, 1
v_xor_b32 v28, s12, v28
v_xor_b32 v29, s13, v29
v_lshrrev_b32 v248, 11, v28
v_or_b32 v29, v29, v248
v_cmp_eq_u32 vcc, 0, v29
s_or_b64 vcc, vcc, s[72:73]
s_cmp_eq_u64 vcc, -1
s_cbranch_scc1 .Lok_53%=
s_cmp_lt_u32 s84, 1048576
s_cbranch_scc1 .Llp_54%=
s_branch .Lok_53%=
.Lspin_57%=:
s_mov_b32 s84, 0
.Llp_59%=:
s_sleep 2
v_bfe_u32 v28, v1, 4, 2
v_lshlrev_b32 v28, 3, v28
s_lshl_b32 s80, s56, 9
s_add_u32 s80, s80, 4096
buffer_load_dwordx2 v[28:29], v28, s[8:11], s80 offen sc1
s_waitcnt vmcnt(0)
s_add_u32 s84, s84, 1
v_cmp_eq_u64 vcc, s[12:13], v[28:29]
s_cmp_eq_u64 vcc, -1
s_cbranch_scc1 .Lok_58%=
s_cmp_lt_u32 s84, 1048576
s_cbranch_scc1 .Llp_59%=
s_branch .Lok_58%=
.Lspin_60%=:
s_mov_b32 s84, 0
.Llp_62%=:
s_sleep 2
v_bfe_u32 v28, v1, 4, 4
v_lshlrev_b32 v28, 3, v28
s_lshl_b32 s80, s56, 9
s_add_u32 s80, s80, 4096
buffer_load_dwordx2 v[28:29], v28, s[8:11], s80 offen sc1
s_waitcnt vmcnt(0)
s_add_u32 s84, s84, 1
v_cmp_eq_u64 vcc, s[12:13], v[28:29]
s_cmp_eq_u64 vcc, -1
s_cbranch_scc1 .Lok_61%=
s_cmp_lt_u32 s84, 1048576
s_cbranch_scc1 .Llp_62%=
s_branch .Lok_61%=
.Lspin_63%=:
s_mov_b32 s84, 0
.Llp_65%=:
s_sleep 2
v_bfe_u32 v28, v1, 4, 4
v_lshlrev_b32 v28, 3, v28
s_lshl_b32 s80, s56, 9
s_add_u32 s80, s80, 4224
buffer_load_dwordx2 v[28:29], v28, s[8:11], s80 offen sc1
s_waitcnt vmcnt(0)
s_add_u32 s84, s84, 1
v_cmp_eq_u64 vcc, s[12:13], v[28:29]
s_cmp_eq_u64 vcc, -1
s_cbranch_scc1 .Lok_64%=
s_cmp_lt_u32 s84, 1048576
s_cbranch_scc1 .Llp_65%=
s_branch .Lok_64%=
.Lspin_67%=:
s_mov_b32 s84, 0
.Llp_69%=:
s_sleep 2
v_bfe_u32 v28, v1, 4, 4
v_lshlrev_b32 v28, 3, v28
s_lshl_b32 s80, s56, 9
s_add_u32 s80, s80, 4352
buffer_load_dwordx2 v[28:29], v28, s[8:11], s80 offen sc1
s_waitcnt vmcnt(0)
s_add_u32 s84, s84, 1
v_cmp_eq_u64 vcc, s[12:13], v[28:29]
s_cmp_eq_u64 vcc, -1
s_cbranch_scc1 .Lok_68%=
s_cmp_lt_u32 s84, 1048576
s_cbranch_scc1 .Llp_69%=
s_branch .Lok_68%=
.Lspin_70%=:
s_mov_b32 s84, 0
.Llp_72%=:
s_sleep 2
v_bfe_u32 v28, v1, 4, 4
v_lshlrev_b32 v28, 3, v28
s_lshl_b32 s80, s56, 9
s_add_u32 s80, s80, 4480
buffer_load_dwordx2 v[28:29], v28, s[8:11], s80 offen sc1
s_waitcnt vmcnt(0)
s_add_u32 s84, s84, 1
v_cmp_eq_u64 vcc, s[12:13], v[28:29]
s_cmp_eq_u64 vcc, -1
s_cbranch_scc1 .Lok_71%=
s_cmp_lt_u32 s84, 1048576
s_cbranch_scc1 .Llp_72%=
s_branch .Lok_71%=
.Lspin_73%=:
s_mov_b32 s84, 0
.Llp_75%=:
s_sleep 2
buffer_load_dwordx2 v[8:9], v4, s[8:11], 0 offen offset:2560 sc1
buffer_load_dwordx2 v[10:11], v5, s[8:11], 0 offen offset:3072 sc1
buffer_load_dwordx2 v[12:13], v6, s[8:11], 0 offen offset:3584 sc1
s_waitcnt vmcnt(0)
s_add_u32 s84, s84, 1
v_cmp_eq_u64 vcc, s[12:13], v[8:9]
v_cmp_eq_u64_e64 s[86:87], s[12:13], v[10:11]
s_and_b64 vcc, vcc, s[86:87]
s_or_b64 vcc, vcc, s[20:21]
s_cmp_eq_u64 vcc, -1
s_cbranch_scc1 .Lok_74%=
s_cmp_lt_u32 s84, 1048576
s_cbranch_scc1 .Llp_75%=
s_branch .Lok_74%=
.Lmk_77%=:
s_nop 7
v_cndmask_b32_e64 v168, v241, v168, s[72:73]
v_cndmask_b32_e64 v169, v241, v169, s[74:75]
v_cndmask_b32_e64 v170, v241, v170, s[76:77]
v_cndmask_b32_e64 v171, v241, v171, s[78:79]
v_cndmask_b32_e64 v188, v241, v188, s[72:73]
v_cndmask_b32_e64 v189, v241, v189, s[74:75]
v_cndmask_b32_e64 v190, v241, v190, s[76:77]
v_cndmask_b32_e64 v191, v241, v191, s[78:79]
v_mov_b32 v184, v241
v_mov_b32 v185, v241
v_mov_b32 v186, v241
v_mov_b32 v187, v241
s_branch .Lmkr_78%=
.Lspin_82%=:
s_mov_b32 s84, 0
.Llp_84%=:
s_sleep 2
v_bfe_u32 v28, v1, 4, 2
v_lshlrev_b32 v28, 3, v28
buffer_load_dwordx2 v[28:29], v28, s[8:11], s55 offen sc1
s_waitcnt vmcnt(0)
s_add_u32 s84, s84, 1
v_cmp_eq_u64 vcc, s[12:13], v[28:29]
s_cmp_eq_u64 vcc, -1
s_cbranch_scc1 .Lok_83%=
s_cmp_lt_u32 s84, 1048576
s_cbranch_scc1 .Llp_84%=
s_branch .Lok_83%=
.LL_87%=:
s_mov_b32 s84, 0
.LL_89%=:
s_sleep 2
buffer_load_dwordx2 v[12:13], v27, s[8:11], 0 offen offset:3584 sc1
s_waitcnt vmcnt(0)
s_add_u32 s84, s84, 1
v_xor_b32 v12, s12, v12
v_xor_b32 v13, s13, v13
v_lshrrev_b32 v3, 11, v12
v_or_b32 v13, v13, v3
v_cmp_eq_u32 vcc, 0, v13
s_cmp_eq_u64 vcc, -1
s_cbranch_scc1 .LL_88%=
s_cmp_lt_u32 s84, 1048576
s_cbranch_scc1 .LL_89%=
s_branch .LL_88%=
.Lup_85%=:
v_lshrrev_b32 v3, 8, v1
v_lshlrev_b32 v3, 5, v3
s_ff1_i32_b32 s69, s68
s_mul_i32 s66, s69, 20
s_mul_i32 s67, s58, 5
s_add_u32 s66, s66, s67
s_lshl_b32 s66, s66, 7
s_add_u32 s66, s66, 98304
s_lshl_b32 s67, s14, 7
s_add_u32 s67, s67, s66
buffer_load_dwordx4 v[100:103], v3, s[8:11], s67 offen offset:0 sc1
buffer_load_dwordx4 v[108:111], v3, s[8:11], s67 offen offset:16 sc1
buffer_load_dwordx4 v[116:119], v3, s[8:11], s66 offen offset:512 sc1
buffer_load_dwordx4 v[124:127], v3, s[8:11], s66 offen offset:528 sc1
s_branch .Lupr_86%=
.Lspin_98%=:
s_mov_b32 s84, 0
.Llp_100%=:
s_sleep 2
v_bfe_u32 v28, v1, 4, 2
v_lshlrev_b32 v28, 3, v28
buffer_load_dwordx2 v[28:29], v28, s[8:11], s54 offen sc1
s_waitcnt vmcnt(0)
s_add_u32 s84, s84, 1
v_cmp_eq_u64 vcc, s[12:13], v[28:29]
s_cmp_eq_u64 vcc, -1
s_cbranch_scc1 .Lok_99%=
s_cmp_lt_u32 s84, 1048576
s_cbranch_scc1 .Llp_100%=
s_branch .Lok_99%=
.Lspin_101%=:
s_mov_b32 s84, 0
.Llp_103%=:
s_sleep 2
v_bfe_u32 v28, v1, 4, 2
v_lshlrev_b32 v28, 3, v28
buffer_load_dwordx2 v[28:29], v28, s[8:11], s55 offen sc1
s_waitcnt vmcnt(0)
s_add_u32 s84, s84, 1
v_cmp_eq_u64 vcc, s[12:13], v[28:29]
s_cmp_eq_u64 vcc, -1
s_cbranch_scc1 .Lok_102%=
s_cmp_lt_u32 s84, 1048576
s_cbranch_scc1 .Llp_103%=
s_branch .Lok_102%=
.LL_106%=:
s_mov_b32 s84, 0
.LL_108%=:
s_sleep 2
buffer_load_dwordx2 v[12:13], v27, s[8:11], 0 offen offset:3584 sc1
s_waitcnt vmcnt(0)
s_add_u32 s84, s84, 1
v_xor_b32 v12, s12, v12
v_xor_b32 v13, s13, v13
v_lshrrev_b32 v3, 11, v12
v_or_b32 v13, v13, v3
v_cmp_eq_u32 vcc, 0, v13
s_cmp_eq_u64 vcc, -1
s_cbranch_scc1 .LL_107%=
s_cmp_lt_u32 s84, 1048576
s_cbranch_scc1 .LL_108%=
)"
R"(s_branch .LL_107%=
.Lup_104%=:
v_lshrrev_b32 v3, 8, v1
v_lshlrev_b32 v3, 5, v3
s_ff1_i32_b32 s69, s68
s_mul_i32 s66, s69, 20
s_mul_i32 s67, s58, 5
s_add_u32 s66, s66, s67
s_lshl_b32 s66, s66, 7
s_add_u32 s66, s66, 98304
s_lshl_b32 s67, s14, 7
s_add_u32 s67, s67, s66
buffer_load_dwordx4 v[100:103], v3, s[8:11], s67 offen offset:0 sc1
buffer_load_dwordx4 v[108:111], v3, s[8:11], s67 offen offset:16 sc1
buffer_load_dwordx4 v[116:119], v3, s[8:11], s66 offen offset:512 sc1
buffer_load_dwordx4 v[124:127], v3, s[8:11], s66 offen offset:528 sc1
s_branch .Lupr_105%=
.Lend_250%=:
.Lend%=:
.purgem W
.purgem D
.purgem Ta
.purgem Ra
.purgem Zvaa
.purgem Mvaa
.purgem Zaaa
.purgem Maaa
.purgem A
.purgem U
.purgem K
.purgem Rv
.purgem Mvav
.purgem G
.purgem Zvav
.purgem Cvav
.purgem X
.purgem Cvaa
.purgem Dc
.purgem Maav
.purgem Mvvv
.purgem Zvva
.purgem Mvva
)"
    : : [ka] "s"(__builtin_amdgcn_kernarg_segment_ptr()), [bid] "s"(blockIdx.x), [tid] "v"(threadIdx.x)
    : "v1", "v2", "v3", "v4", "v5", "v6", "v7", "v8", "v9", "v10", "v11", "v12", "v13", "v14", "v15", "v16", "v17", "v18", "v19", "v20", "v21", "v22", "v23", "v24", "v25", "v26", "v27", "v28", "v29", "v30", "v31", "v32", "v33", "v34", "v35", "v36", "v37", "v38", "v39", "v40", "v41", "v42", "v43", "v44", "v45", "v46", "v47", "v48", "v49", "v50", "v51", "v52", "v53", "v54", "v55", "v56", "v57", "v58", "v59", "v60", "v61", "v62", "v63", "v64", "v65", "v66", "v67", "v68", "v69", "v70", "v71", "v72", "v73", "v74", "v75", "v76", "v77", "v78", "v79", "v80", "v81", "v82", "v83", "v84", "v85", "v86", "v87", "v88", "v89", "v90", "v91", "v92", "v93", "v94", "v95", "v96", "v97", "v98", "v99", "v100", "v101", "v102", "v103", "v104", "v105", "v106", "v107", "v108", "v109", "v110", "v111", "v112", "v113", "v114", "v115", "v116", "v117", "v118", "v119", "v120", "v121", "v122", "v123", "v124", "v125", "v126", "v127", "v128", "v129", "v130", "v131", "v132", "v133", "v134", "v135", "v136", "v137", "v138", "v139", "v140", "v141", "v142", "v143", "v144", "v145", "v146", "v147", "v148", "v149", "v150", "v151", "v152", "v153", "v154", "v155", "v156", "v157", "v158", "v159", "v160", "v161", "v162", "v163", "v164", "v165", "v166", "v167", "v168", "v169", "v170", "v171", "v172", "v173", "v174", "v175", "v176", "v177", "v178", "v179", "v180", "v181", "v182", "v183", "v184", "v185", "v186", "v187", "v188", "v189", "v190", "v191", "v192", "v193", "v194", "v195", "v196", "v197", "v198", "v199", "v200", "v201", "v202", "v203", "v204", "v205", "v206", "v207", "v208", "v209", "v210", "v211", "v212", "v213", "v214", "v215", "v216", "v217", "v218", "v219", "v220", "v221", "v222", "v223", "v224", "v225", "v226", "v227", "v228", "v229", "v230", "v231", "v232", "v233", "v234", "v235", "v236", "v237", "v238", "v239", "v240", "v241", "v242", "v243", "v244", "v245", "v246", "v247", "v248", "v249", "v250", "v251", "v252", "v253", "v254", "v255", "a0", "a1", "a2", "a3", "a4", "a5", "a6", "a7", "a8", "a9", "a10", "a11", "a12", "a13", "a14", "a15", "a16", "a17", "a18", "a19", "a20", "a21", "a22", "a23", "a24", "a25", "a26", "a27", "a28", "a29", "a30", "a31", "a32", "a33", "a34", "a35", "a36", "a37", "a38", "a39", "a40", "a41", "a42", "a43", "a44", "a45", "a46", "a47", "a48", "a49", "a50", "a51", "a52", "a53", "a54", "a55", "a56", "a57", "a58", "a59", "a60", "a61", "a62", "a63", "a64", "a65", "a66", "a67", "a68", "a69", "a70", "a71", "a72", "a73", "a74", "a75", "a76", "a77", "a78", "a79", "a80", "a81", "a82", "a83", "a84", "a85", "a86", "a87", "a88", "a89", "a90", "a91", "a92", "a93", "a94", "a95", "a96", "a97", "a98", "a99", "a100", "a101", "a102", "a103", "a104", "a105", "a106", "a107", "a108", "a109", "a110", "a111", "a112", "a113", "a114", "a115", "a116", "a117", "a118", "a119", "a120", "a121", "a122", "a123", "a124", "a125", "a126", "a127", "a128", "a129", "a130", "a131", "a132", "a133", "a134", "a135", "a136", "a137", "a138", "a139", "a140", "a141", "a142", "a143", "a144", "a145", "a146", "a147", "a148", "a149", "a150", "a151", "a152", "a153", "a154", "a155", "a156", "a157", "a158", "a159", "a160", "a161", "a162", "a163", "a164", "a165", "a166", "a167", "a168", "a169", "a170", "a171", "a172", "a173", "a174", "a175", "a176", "a177", "a178", "a179", "a180", "a181", "a182", "a183", "a184", "a185", "a186", "a187", "a188", "a189", "a190", "a191", "a192", "a193", "a194", "a195", "a196", "a197", "a198", "a199", "a200", "a201", "a202", "a203", "a204", "a205", "a206", "a207", "a208", "a209", "a210", "a211", "a212", "a213", "a214", "a215", "a216", "a217", "a218", "a219", "a220", "a221", "a222", "a223", "a224", "a225", "a226", "a227", "a228", "a229", "a230", "a231", "a232", "a233", "a234", "a235", "a236", "a237", "a238", "a239", "a240", "a241", "a242", "a243", "a244", "a245", "a246", "a247", "a248", "a249", "a250", "a251", "a252", "a253", "a254", "a255", "s4", "s5", "s6", "s7", "s8", "s9", "s10", "s11", "s12", "s13", "s14", "s15", "s16", "s17", "s18", "s19", "s20", "s21", "s22", "s23", "s24", "s25", "s26", "s27", "s28", "s29", "s30", "s31", "s32", "s33", "s34", "s35", "s36", "s37", "s38", "s39", "s40", "s41", "s42", "s43", "s44", "s45", "s46", "s47", "s48", "s49", "s50", "s51", "s52", "s53", "s54", "s55", "s56", "s57", "s58", "s59", "s60", "s61", "s62", "s63", "s64", "s65", "s66", "s67", "s68", "s69", "s70", "s71", "s72", "s73", "s74", "s75", "s76", "s77", "s78", "s79", "s80", "s81", "s82", "s83", "s84", "s85", "s86", "s87", "s88", "s89", "s90", "s91", "s92", "s93", "s94", "s95", "s96", "s97", "s98", "s99", "vcc", "scc", "memory"
  );
}
extern "C" void kernel_launch(void* const* d_in, const int* in_sizes, int n_in, void* d_out, int out_size, void* d_ws, size_t ws_size, hipStream_t stream) {
  (void)hipFuncSetAttribute((const void*)mega_kernel, hipFuncAttributeMaxDynamicSharedMemorySize, LDS_BYTES);
  mega_kernel<<<256, 256, LDS_BYTES, stream>>>((const bf16*)d_in[0], (const bf16*)d_in[1], (const bf16*)d_in[2], (const bf16*)d_in[3], (const bf16*)d_in[4], (const bf16*)d_in[5],
      (const bf16*)d_in[7], (const bf16*)d_in[8], (const bf16*)d_in[9], (const bf16*)d_in[10], (char*)d_ws + 0, (bf16*)d_out);
}
```

```cpp
#include <hip/hip_runtime.h>
#include <hip/hip_bf16.h>
typedef __hip_bfloat16 bf16;
constexpr int LDS_BYTES = 163840;
__global__ __launch_bounds__(256, 1) void mega_kernel(const bf16* x, const bf16* ckv, const bf16* wq, const bf16* wuk, const bf16* wuv, const bf16* wo,
    const bf16* w_r, const bf16* wg, const bf16* wu, const bf16* wd, char* ws, bf16* out) {
  asm volatile(
R"(.macro W n
s_waitcnt lgkmcnt(\n)
.endm
.macro D v,r,s,o
buffer_load_dwordx4 v\v, s[\r:\r+3], s\s offen offset:\o lds
.endm
.macro Ta d,a,o
ds_read_b64_tr_b16 a[\d:\d+1], v\a offset:\o
.endm
.macro Ra d,a,o
ds_read_b128 a[\d:\d+3], v\a offset:\o
.endm
.macro Zvaa d,a,b
v_mfma_f32_16x16x32_bf16 v[\d:\d+3], a[\a:\a+3], a[\b:\b+3], 0
.endm
.macro Mvaa d,a,b
v_mfma_f32_16x16x32_bf16 v[\d:\d+3], a[\a:\a+3], a[\b:\b+3], v[\d:\d+3]
.endm
.macro Zaaa d,a,b
v_mfma_f32_16x16x32_bf16 a[\d:\d+3], a[\a:\a+3], a[\b:\b+3], 0
.endm
.macro Maaa d,a,b
v_mfma_f32_16x16x32_bf16 a[\d:\d+3], a[\a:\a+3], a[\b:\b+3], a[\d:\d+3]
.endm
.macro A d,a,b
v_add_f32 v\d, v\a, v\b
.endm
.macro U d,a,b
v_mul_f32 v\d, v\a, v\b
.endm
.macro K d,a,b
v_cvt_pk_bf16_f32 v\d, v\a, v\b
.endm
.macro Rv d,a,o
ds_read_b128 v[\d:\d+3], v\a offset:\o
.endm
.macro Mvav d,a,b
v_mfma_f32_16x16x32_bf16 v[\d:\d+3], a[\a:\a+3], v[\b:\b+3], v[\d:\d+3]
.endm
.macro G d,a
v_accvgpr_read_b32 v\d, a\a
.endm
.macro Zvav d,a,b
v_mfma_f32_16x16x32_bf16 v[\d:\d+3], a[\a:\a+3], v[\b:\b+3], 0
.endm
.macro Cvav d,a,b,c
v_mfma_f32_16x16x32_bf16 v[\d:\d+3], a[\a:\a+3], v[\b:\b+3], v[\c:\c+3]
.endm
.macro X d
v_exp_f32 v\d, v\d
.endm
.macro Cvaa d,a,b,c
v_mfma_f32_16x16x32_bf16 v[\d:\d+3], a[\a:\a+3], a[\b:\b+3], v[\c:\c+3]
.endm
.macro Dc v,r,s,o
buffer_load_dwordx4 v\v, s[\r:\r+3], s\s offen offset:\o sc1 lds
.endm
.macro Maav d,a,b
v_mfma_f32_16x16x32_bf16 a[\d:\d+3], a[\a:\a+3], v[\b:\b+3], a[\d:\d+3]
.endm
.macro Mvvv d,a,b
v_mfma_f32_16x16x32_bf16 v[\d:\d+3], v[\a:\a+3], v[\b:\b+3], v[\d:\d+3]
.endm
.macro Zvva d,a,b
v_mfma_f32_16x16x32_bf16 v[\d:\d+3], v[\a:\a+3], a[\b:\b+3], 0
.endm
.macro Mvva d,a,b
v_mfma_f32_16x16x32_bf16 v[\d:\d+3], v[\a:\a+3], a[\b:\b+3], v[\d:\d+3]
.endm
s_load_dwordx16 s[16:31], %[ka], 0
s_load_dwordx8 s[32:39], %[ka], 64
s_getpc_b64 s[6:7]
.Lbeg%=:
s_mov_b32 s15, %[bid]
v_lshrrev_b32 v2, 6, %[tid]
v_and_b32 v100, 63, %[tid]
v_readfirstlane_b32 s14, v2
v_lshlrev_b32 v1, 4, v100
v_lshrrev_b32 v101, 4, v100
v_and_b32 v102, 15, v100
s_and_b32 s59, s15, 7
s_bfe_u32 s57, s15, 0x30003
s_lshr_b32 s58, s15, 6
s_lshl_b32 s56, s59, 2
s_add_u32 s56, s56, s58
s_lshr_b32 s80, s15, 3
s_lshl_b32 s80, s80, 2
s_add_u32 s80, s80, s14
s_lshl_b32 s80, s80, 11
v_min_u32 v146, 240, v1
v_lshl_add_u32 v146, v146, 3, s80
v_min_u32 v146, .Lend%=-.Lbeg%=-4, v146
global_load_dword v142, v146, s[6:7]
v_and_b32 v88, 0x1f0, v1
v_lshrrev_b32 v105, 1, v101
v_mul_u32_u24 v105, 1280, v105
v_add_u32 v28, v88, v105
s_lshl_b32 s85, s15, 2
s_add_u32 s85, s85, s14
s_mul_i32 s84, s85, 2560
v_and_b32 v105, 1, v101
v_lshl_or_b32 v29, v105, 31, v28
v_lshlrev_b32 v104, 1, v101
v_xor_b32 v104, v104, v102
v_lshlrev_b32 v104, 4, v104
v_lshl_add_u32 v16, v101, 13, v104
v_xor_b32 v105, 128, v16
v_add_u32 v17, 31744, v16
v_add_u32 v18, 63488, v105
v_add_u32 v19, 95232, v105
v_add_u32 v20, 131072, v16
v_add_u32 v21, 162816, v16
v_add_u32 v22, 194560, v105
v_add_u32 v23, 226304, v105
s_lshl_b32 s80, s14, 3
s_add_u32 s80, s80, s57
s_lshl_b32 s80, s80, 15
v_and_b32 v104, 7, v100
v_xor_b32 v104, v104, v101
v_lshlrev_b32 v104, 4, v104
v_lshrrev_b32 v105, 3, v100
v_lshl_add_u32 v104, v105, 10, v104
v_add_u32 v2, s80, v104
v_xor_b32 v105, 64, v2
v_add_u32 v3, 7168, v105
v_add_u32 v4, 14336, v2
v_add_u32 v5, 21504, v105
v_xor_b32 v6, 1835008, v2
v_xor_b32 v7, 1835008, v3
v_xor_b32 v8, 1835008, v4
v_xor_b32 v9, 1835008, v5
s_lshl_b32 s63, s14, 13
s_add_u32 s61, s63, 65536
s_and_b32 s80, s14, 1
s_lshr_b32 s81, s14, 1
s_lshl_b32 s62, s80, 14
s_lshl_b32 s82, s81, 13
s_add_u32 s62, s62, s82
s_lshl_b32 s81, s81, 18
s_lshl_b32 s83, s56, 8
s_add_u32 s81, s81, s83
s_mov_b32 s64, 0
s_cmp_eq_u32 s80, 0
W 0
s_cselect_b32 s44, s22, s24
s_cselect_b32 s45, s23, s25
s_mov_b32 s40, s18
s_and_b32 s41, s19, 0xffff
s_mov_b32 s42, 0x200000
s_mov_b32 s43, 0x20000
s_add_u32 s44, s44, s81
s_addc_u32 s45, s45, 0
s_and_b32 s45, s45, 0xffff
s_mov_b32 s46, 0x400000
s_mov_b32 s47, 0x20000
s_sub_u32 s46, s46, s81
s_load_dwordx2 s[12:13], s[36:37], 0
s_lshl_b32 s65, s64, 12
s_add_u32 m0, s62, 0
s_nop 0
D 16,44,65,0
D 17,44,65,1024
D 18,44,65,2048
D 19,44,65,3072
s_add_u32 m0, s62, 4096
s_nop 0
D 20,44,65,0
D 21,44,65,1024
D 22,44,65,2048
D 23,44,65,3072
s_add_u32 m0, s61, 0
s_nop 0
D 2,40,64,0
D 3,40,64,1024
D 4,40,64,2048
D 5,40,64,3072
s_add_u32 m0, s61, 4096
s_nop 0
D 6,40,64,0
D 7,40,64,1024
D 8,40,64,2048
D 9,40,64,3072
s_mov_b32 s52, s16
s_and_b32 s53, s17, 0xffff
s_mov_b32 s54, 2621440
s_mov_b32 s55, 0x20000
buffer_load_dwordx4 v[32:35], v28, s[52:55], s84 offen
buffer_load_dwordx4 v[36:39], v28, s[52:55], s84 offen offset:512
buffer_load_dwordx4 v[40:43], v29, s[52:55], s84 offen offset:1024
s_mov_b32 s52, s28
s_and_b32 s53, s29, 0xffff
s_mov_b32 s54, 1280
s_mov_b32 s55, 0x20000
buffer_load_dwordx4 v[44:47], v88, s[52:55], 0 offen offset:0
buffer_load_dwordx4 v[48:51], v88, s[52:55], 0 offen offset:512
buffer_load_dwordx4 v[52:55], v88, s[52:55], 0 offen offset:1024
s_mov_b32 s69, 0
s_cmp_eq_u32 s14, 2
s_cbranch_scc1 .LL_1%=
s_cmp_eq_u32 s14, 3
s_cbranch_scc1 .LL_2%=
s_lshl_b32 s80, s58, 3
s_add_u32 s80, s80, s57
s_lshl_b32 s80, s80, 15
s_lshl_b32 s82, s14, 7
s_add_u32 s80, s80, s82
s_add_u32 s80, s80, 256
v_lshl_add_u32 v104, v88, 6, s80
v_and_b32 v105, 512, v1
v_mul_u32_u24 v105, 3584, v105
v_xor_b32 v104, v104, v105
v_mov_b32 v140, s18
v_mov_b32 v141, s19
s_mov_b32 s68, 256
s_branch .LL_3%=
.LL_1%=:
v_lshrrev_b32 v104, 6, v1
v_lshlrev_b32 v104, 13, v104
v_and_b32 v105, 16, v1
v_lshl_add_u32 v104, v105, 3, v104
s_lshl_b32 s80, s57, 17
s_add_u32 s80, s80, s83
s_add_u32 s80, s80, 1048576
v_add_u32 v104, s80, v104
v_and_b32 v105, 32, v1
v_cmp_eq_u32 vcc, 0, v105
v_mov_b32 v140, s24
v_mov_b32 v141, s25
v_mov_b32 v105, s22
v_mov_b32 v106, s23
v_cndmask_b32 v140, v140, v105, vcc
v_cndmask_b32 v141, v141, v106, vcc
s_mov_b32 s68, 1048576
s_branch .LL_3%=
.LL_2%=:
v_lshrrev_b32 v104, 5, v1
v_lshlrev_b32 v104, 13, v104
v_and_b32 v105, 16, v1
v_lshl_add_u32 v104, v105, 3, v104
s_mul_i32 s80, s57, 655360
s_add_u32 s80, s80, s83
v_add_u32 v104, s80, v104
v_mov_b32 v140, s20
v_mov_b32 v141, s21
s_mov_b32 s68, 196608
v_mov_b32 v104, 0
s_mov_b32 s68, 0
.LL_3%=:
v_mov_b32 v105, 0
v_lshl_add_u64 v[140:141], v[104:105], 0, v[140:141]
s_cmp_eq_u32 s14, 3
s_cbranch_scc0 .LL_4%=
s_lshr_b32 s88, s15, 3
s_cmp_lt_u32 s88, 14
s_cbranch_scc0 .LL_4%=
v_lshlrev_b32 v104, 16, v1
s_mov_b64 s[86:87], s[36:37]
s_mov_b32 s89, 38797312
s_cmp_eq_u32 s88, 1
s_cselect_b32 s90, 39845888, 0
s_add_u32 s86, s86, s90
s_addc_u32 s87, s87, 0
s_cmp_eq_u32 s88, 2
s_cselect_b64 s[86:87], s[26:27], s[86:87]
s_cselect_b32 s89, 5242876, s89
s_cmp_eq_u32 s88, 3
s_cselect_b64 s[86:87], s[30:31], s[86:87]
s_cselect_b32 s89, 2211836, s89
s_cmp_eq_u32 s88, 4
s_cselect_b64 s[86:87], s[32:33], s[86:87]
s_cselect_b32 s89, 2211836, s89
s_cmp_eq_u32 s88, 5
s_cselect_b64 s[86:87], s[34:35], s[86:87]
s_cselect_b32 s89, 2211836, s89
s_cmp_eq_u32 s88, 6
s_cselect_b64 s[86:87], s[38:39], s[86:87]
s_cselect_b32 s89, 2621436, s89
s_cmp_eq_u32 s88, 7
s_cselect_b64 s[86:87], s[20:21], s[86:87]
s_cselect_b32 s89, 5242876, s89
s_cmp_eq_u32 s88, 8
s_cselect_b64 s[86:87], s[18:19], s[86:87]
s_cselect_b32 s89, 2097148, s89
s_cmp_eq_u32 s88, 9
s_cselect_b64 s[86:87], s[22:23], s[86:87]
s_cselect_b32 s89, 4194300, s89
s_cmp_eq_u32 s88, 10
s_cselect_b64 s[86:87], s[24:25], s[86:87]
s_cselect_b32 s89, 4194300, s89
s_cmp_eq_u32 s88, 11
s_cselect_b64 s[86:87], s[16:17], s[86:87]
s_cselect_b32 s89, 2621436, s89
s_cmp_eq_u32 s88, 12
s_cselect_b64 s[86:87], s[28:29], s[86:87]
s_cselect_b32 s89, 1276, s89
s_cmp_eq_u32 s88, 13
s_cselect_b64 s[86:87], s[6:7], s[86:87]
s_cselect_b32 s89, 0, s89
v_min_u32 v104, s89, v104
global_load_dword v142, v104, s[86:87] sc1
.LL_4%=:
v_lshrrev_b32 v104, 1, v102
v_xor_b32 v104, v104, v101
v_lshlrev_b32 v104, 4, v104
v_lshl_add_u32 v104, v102, 7, v104
s_lshr_b32 s81, s14, 1
s_lshl_b32 s81, s81, 14
s_add_u32 s81, s81, 65536
v_add_u32 v26, s81, v104
v_xor_b32 v27, 64, v26
v_and_b32 v105, 1, v101
v_lshrrev_b32 v104, 2, v102
v_lshl_or_b32 v105, v105, 2, v104
v_lshl_add_u32 v104, v101, 3, v104
v_lshlrev_b32 v104, 8, v104
v_lshl_add_u32 v104, v105, 5, v104
v_and_b32 v105, 3, v102
v_lshl_add_u32 v10, v105, 3, v104
v_xor_b32 v11, 32, v10
v_xor_b32 v12, 64, v10
v_xor_b32 v13, 96, v10
v_xor_b32 v14, 128, v10
v_xor_b32 v15, 160, v10
v_xor_b32 v24, 192, v10
v_xor_b32 v25, 224, v10
s_mov_b32 s8, s36
s_and_b32 s9, s37, 0xffff
s_mov_b32 s10, 79691776
s_mov_b32 s11, 0x20000
s_add_u32 s48, s20, s83
s_addc_u32 s49, s21, 0
s_and_b32 s49, s49, 0xffff
s_mov_b32 s50, 5242880
s_mov_b32 s51, 0x20000
s_sub_u32 s50, s50, s83
s_lshl_b32 s80, s57, 1
s_and_b32 s81, s14, 1
s_add_u32 s80, s80, s81
s_sub_u32 s81, 31, s80
s_cmp_lt_u32 s14, 2
s_cselect_b32 s60, s80, s81
s_mul_i32 s28, s60, 81920
s_add_u32 s28, s28, 131072
s_add_u32 s4, s28, 10240
s_mov_b32 s80, 0
s_lshl_b32 s81, s80, 9
s_add_u32 s28, s28, s81
s_add_u32 s5, s80, s14
s_cmp_ge_u32 s5, 20
s_cselect_b32 s81, 20, 0
s_sub_u32 s5, s5, s81
s_and_b32 s80, s14, 1
s_mul_i32 s29, s80, 17172480
s_mul_i32 s80, s56, 536576
s_add_u32 s29, s29, s80
s_lshl_b32 s80, s57, 13
s_add_u32 s29, s29, s80
s_add_u32 s29, s29, 4194304
W 0
s_add_u32 s12, s12, 0x7f4a7c15
s_addc_u32 s13, s13, 0x9e3779b9
s_mov_b64 exec, 0
s_cmp_eq_u32 s14, 0
s_cbranch_scc1 .Lh_0_0%=
s_cmp_eq_u32 s14, 1
s_cbranch_scc1 .Lh_0_1%=
s_cmp_eq_u32 s14, 2
s_cbranch_scc1 .Lh_0_2%=
s_branch .Lh_0_3%=
.Lhr_5%=:
s_mov_b64 exec, -1
s_add_u32 s64, s64, 128
s_and_b32 s64, s64, 1023
s_lshl_b32 s65, s64, 12
s_add_u32 m0, s62, 32768
s_nop 0
D 16,44,65,0
D 17,44,65,1024
D 18,44,65,2048
D 19,44,65,3072
s_add_u32 m0, s62, 36864
s_nop 0
D 20,44,65,0
D 21,44,65,1024
D 22,44,65,2048
D 23,44,65,3072
s_bitcmp1_b32 s14, 0
s_cbranch_scc1 .Lv_6%=
s_branch .LL_8%=
.p2align 12
.LL_8%=:
s_waitcnt vmcnt(14)
s_barrier
Ta 0,10,0
Ta 2,10,1024
Ra 32,26,0
Ta 4,10,8192
.Lh_0_0%=:
s_cbranch_execz .Lh_0_0_1%=
Ta 6,10,9216
Ra 128,27,0
Ra 132,27,2048
Ra 36,26,2048
Ra 40,26,4096
Ra 136,27,4096
Ra 140,27,6144
Ra 44,26,6144
Ra 48,26,8192
Ra 144,27,8192
W 6
Zvaa 160,0,32
s_add_u32 m0, s61, 32768
Ra 148,27,10240
Ra 52,26,10240
Ra 56,26,12288
Ra 152,27,12288
Ra 156,27,14336
Ra 60,26,14336
Mvaa 160,4,128
Ta 8,11,0
Ta 10,11,1024
Ta 12,11,8192
Ta 14,11,9216
Zvaa 192,4,132
Ta 16,12,0
Ta 18,12,1024
Mvaa 192,0,36
W 14
Ta 20,12,8192
Ta 22,12,9216
Zvaa 224,0,40
D 2,40,64,0
Mvaa 224,4,136
Zaaa 160,4,140
Maaa 160,0,44
W 12
Zaaa 192,0,48
D 3,40,64,1024
Maaa 192,4,144
Zaaa 224,4,148
Maaa 224,0,52
W 8
Zaaa 64,0,56
D 4,40,64,2048
Maaa 64,4,152
Zaaa 96,4,156
)"
R"(Maaa 96,0,60
W 4
Zaaa 100,8,60
D 5,40,64,3072
Maaa 100,12,156
s_add_u32 m0, s61, 36864
Zaaa 68,12,152
Ta 24,13,0
Ta 26,13,1024
Maaa 68,8,56
Ta 28,13,8192
Ta 30,13,9216
Zaaa 228,8,52
s_waitcnt vmcnt(15)
D 6,40,64,0
Maaa 228,12,148
v_lshlrev_b32 v56, 16, v32
v_and_b32 v57, 0xffff0000, v32
v_lshlrev_b32 v58, 16, v33
v_and_b32 v59, 0xffff0000, v33
v_lshlrev_b32 v60, 16, v34
Zaaa 196,12,144
v_and_b32 v61, 0xffff0000, v34
v_lshlrev_b32 v62, 16, v35
v_and_b32 v63, 0xffff0000, v35
v_lshlrev_b32 v64, 16, v36
v_and_b32 v65, 0xffff0000, v36
Maaa 196,8,48
v_lshlrev_b32 v66, 16, v37
v_and_b32 v67, 0xffff0000, v37
v_lshlrev_b32 v68, 16, v38
v_and_b32 v69, 0xffff0000, v38
v_lshlrev_b32 v70, 16, v39
Zaaa 164,8,44
D 7,40,64,1024
v_and_b32 v71, 0xffff0000, v39
v_lshlrev_b32 v72, 16, v40
v_and_b32 v73, 0xffff0000, v40
v_lshlrev_b32 v74, 16, v41
v_and_b32 v75, 0xffff0000, v41
Maaa 164,12,140
v_lshlrev_b32 v76, 16, v42
v_and_b32 v77, 0xffff0000, v42
v_lshlrev_b32 v78, 16, v43
v_and_b32 v79, 0xffff0000, v43
A 80,56,57
Zvaa 228,12,136
A 81,58,59
A 80,80,60
A 81,81,61
A 80,80,62
A 81,81,63
Mvaa 228,8,40
A 80,80,64
A 81,81,65
A 80,80,66
A 81,81,67
A 80,80,68
Zvaa 196,8,36
D 8,40,64,2048
A 81,81,69
A 80,80,70
A 81,81,71
A 80,80,72
A 81,81,73
Mvaa 196,12,132
A 80,80,74
A 81,81,75
A 80,80,76
A 81,81,77
A 80,80,78
A 81,81,79
Zvaa 164,12,128
A 80,80,81
s_nop 1
v_add_f32_dpp v80, v80, v80 quad_perm:[1,0,3,2] row_mask:0xf bank_mask:0xf
s_nop 1
v_add_f32_dpp v80, v80, v80 quad_perm:[2,3,0,1] row_mask:0xf bank_mask:0xf
s_nop 1
v_add_f32_dpp v80, v80, v80 row_half_mirror row_mask:0xf bank_mask:0xf
s_nop 1
v_add_f32_dpp v80, v80, v80 row_mirror row_mask:0xf bank_mask:0xf
Mvaa 164,8,32
W 4
v_mov_b32 v81, v80
s_nop 1
v_permlane16_swap_b32 v80, v81
A 80,80,81
v_mul_f32 v84, 0x3acccccd, v80
s_mov_b32 s86, 0xffff
Zvaa 168,16,32
s_mov_b32 s87, 0xffff
D 9,40,64,3072
v_sub_f32 v82, v56, v84
U 80,82,82
v_sub_f32 v83, v57, v84
v_fmac_f32 v80, v83, v83
Mvaa 168,20,128
v_sub_f32 v82, v58, v84
v_fmac_f32 v80, v82, v82
v_sub_f32 v83, v59, v84
v_fmac_f32 v80, v83, v83
v_sub_f32 v82, v60, v84
Zvaa 200,20,132
Ta 0,14,0
v_fmac_f32 v80, v82, v82
v_sub_f32 v83, v61, v84
v_fmac_f32 v80, v83, v83
v_sub_f32 v82, v62, v84
v_fmac_f32 v80, v82, v82
Mvaa 200,16,36
Ta 2,14,1024
v_sub_f32 v83, v63, v84
v_fmac_f32 v80, v83, v83
v_sub_f32 v82, v64, v84
v_fmac_f32 v80, v82, v82
v_sub_f32 v83, v65, v84
Zvaa 232,16,40
global_load_dword v142, v[140:141], off
v_lshl_add_u64 v[140:141], v[140:141], 0, s[68:69]
Ta 4,14,8192
Ta 6,14,9216
v_fmac_f32 v80, v83, v83
v_sub_f32 v82, v66, v84
v_fmac_f32 v80, v82, v82
Mvaa 232,20,136
v_sub_f32 v83, v67, v84
v_fmac_f32 v80, v83, v83
v_sub_f32 v82, v68, v84
v_fmac_f32 v80, v82, v82
v_sub_f32 v83, v69, v84
v_fmac_f32 v80, v83, v83
Zaaa 168,20,140
v_sub_f32 v82, v70, v84
v_fmac_f32 v80, v82, v82
v_sub_f32 v83, v71, v84
v_fmac_f32 v80, v83, v83
v_sub_f32 v82, v72, v84
U 81,82,82
Maaa 168,16,44
v_sub_f32 v83, v73, v84
v_fmac_f32 v81, v83, v83
v_sub_f32 v82, v74, v84
v_fmac_f32 v81, v82, v82
v_sub_f32 v83, v75, v84
v_fmac_f32 v81, v83, v83
Zaaa 200,16,48
v_sub_f32 v82, v76, v84
v_fmac_f32 v81, v82, v82
v_sub_f32 v83, v77, v84
v_fmac_f32 v81, v83, v83
v_sub_f32 v82, v78, v84
Maaa 200,20,144
v_fmac_f32 v81, v82, v82
v_sub_f32 v83, v79, v84
v_fmac_f32 v81, v83, v83
v_cndmask_b32_e64 v81, 0, v81, s[86:87]
A 80,80,81
Zaaa 232,20,148
Maaa 232,16,52
v_add_f32_dpp v80, v80, v80 quad_perm:[1,0,3,2] row_mask:0xf bank_mask:0xf
s_nop 1
v_add_f32_dpp v80, v80, v80 quad_perm:[2,3,0,1] row_mask:0xf bank_mask:0xf
s_nop 1
v_add_f32_dpp v80, v80, v80 row_half_mirror row_mask:0xf bank_mask:0xf
s_nop 1
v_add_f32_dpp v80, v80, v80 row_mirror row_mask:0xf bank_mask:0xf
v_mov_b32 v81, v80
s_nop 1
v_permlane16_swap_b32 v80, v81
Zaaa 72,16,56
A 80,80,81
v_mul_f32 v80, 0x3acccccd, v80
K 80,80,80
v_lshlrev_b32 v80, 16, v80
v_add_f32 v80, 0x3727c5ac, v80
K 80,80,80
Maaa 72,20,152
v_lshlrev_b32 v80, 16, v80
v_sqrt_f32 v81, v80
s_nop 0
v_rcp_f32 v82, v81
v_fma_f32 v83, -v81, v81, v80
Zaaa 104,20,156
v_mul_f32 v82, 0.5, v82
v_fma_f32 v81, v83, v82, v81
K 86,81,81
v_lshlrev_b32 v86, 16, v86
v_rcp_f32 v87, v86
Maaa 104,16,60
W 4
K 85,84,84
v_lshlrev_b32 v85, 16, v85
s_waitcnt vmcnt(17)
v_sub_f32 v56, v56, v85
v_sub_f32 v57, v57, v85
K 80,56,57
Zaaa 108,24,60
v_lshlrev_b32 v56, 16, v80
v_and_b32 v57, 0xffff0000, v80
U 80,56,87
U 81,57,87
K 32,80,81
v_lshlrev_b32 v80, 16, v32
Maaa 108,28,156
v_and_b32 v81, 0xffff0000, v32
v_lshlrev_b32 v82, 16, v44
v_and_b32 v83, 0xffff0000, v44
U 84,80,82
v_fmac_f32 v84, v81, v83
Zaaa 76,28,152
Ta 8,15,0
v_sub_f32 v58, v58, v85
v_sub_f32 v59, v59, v85
K 80,58,59
v_lshlrev_b32 v58, 16, v80
v_and_b32 v59, 0xffff0000, v80
Maaa 76,24,56
Ta 10,15,1024
U 80,58,87
U 81,59,87
K 33,80,81
v_lshlrev_b32 v80, 16, v33
v_and_b32 v81, 0xffff0000, v33
Zaaa 236,24,52
Ta 12,15,8192
Ta 14,15,9216
v_lshlrev_b32 v82, 16, v45
v_and_b32 v83, 0xffff0000, v45
v_fmac_f32 v84, v80, v82
v_fmac_f32 v84, v81, v83
Maaa 236,28,148
v_sub_f32 v60, v60, v85
v_sub_f32 v61, v61, v85
K 80,60,61
v_lshlrev_b32 v60, 16, v80
v_and_b32 v61, 0xffff0000, v80
U 80,60,87
Zaaa 204,28,144
U 81,61,87
K 34,80,81
v_lshlrev_b32 v80, 16, v34
v_and_b32 v81, 0xffff0000, v34
v_lshlrev_b32 v82, 16, v46
v_and_b32 v83, 0xffff0000, v46
Maaa 204,24,48
v_fmac_f32 v84, v80, v82
v_fmac_f32 v84, v81, v83
v_sub_f32 v62, v62, v85
v_sub_f32 v63, v63, v85
K 80,62,63
Zaaa 172,24,44
v_lshlrev_b32 v62, 16, v80
v_and_b32 v63, 0xffff0000, v80
U 80,62,87
U 81,63,87
K 35,80,81
Maaa 172,28,140
v_lshlrev_b32 v80, 16, v35
v_and_b32 v81, 0xffff0000, v35
v_lshlrev_b32 v82, 16, v47
v_and_b32 v83, 0xffff0000, v47
v_fmac_f32 v84, v80, v82
Zvaa 236,28,136
v_fmac_f32 v84, v81, v83
v_sub_f32 v64, v64, v85
v_sub_f32 v65, v65, v85
K 80,64,65
v_lshlrev_b32 v64, 16, v80
Mvaa 236,24,40
v_and_b32 v65, 0xffff0000, v80
U 80,64,87
U 81,65,87
K 36,80,81
v_lshlrev_b32 v80, 16, v36
Zvaa 204,24,36
v_and_b32 v81, 0xffff0000, v36
v_lshlrev_b32 v82, 16, v48
v_and_b32 v83, 0xffff0000, v48
v_fmac_f32 v84, v80, v82
v_fmac_f32 v84, v81, v83
Mvaa 204,28,132
v_sub_f32 v66, v66, v85
v_sub_f32 v67, v67, v85
K 80,66,67
v_lshlrev_b32 v66, 16, v80
v_and_b32 v67, 0xffff0000, v80
U 80,66,87
Zvaa 172,28,128
U 81,67,87
K 37,80,81
v_lshlrev_b32 v80, 16, v37
v_and_b32 v81, 0xffff0000, v37
v_lshlrev_b32 v82, 16, v49
Mvaa 172,24,32
W 4
v_and_b32 v83, 0xffff0000, v49
v_fmac_f32 v84, v80, v82
v_fmac_f32 v84, v81, v83
v_sub_f32 v68, v68, v85
v_sub_f32 v69, v69, v85
Zvaa 176,0,32
K 80,68,69
v_lshlrev_b32 v68, 16, v80
v_and_b32 v69, 0xffff0000, v80
U 80,68,87
U 81,69,87
Mvaa 176,4,128
K 38,80,81
v_lshlrev_b32 v80, 16, v38
v_and_b32 v81, 0xffff0000, v38
v_lshlrev_b32 v82, 16, v50
v_and_b32 v83, 0xffff0000, v50
Zvaa 208,4,132
Ta 16,24,0
v_fmac_f32 v84, v80, v82
v_fmac_f32 v84, v81, v83
v_sub_f32 v70, v70, v85
v_sub_f32 v71, v71, v85
K 80,70,71
Mvaa 208,0,36
Ta 18,24,1024
v_lshlrev_b32 v70, 16, v80
v_and_b32 v71, 0xffff0000, v80
U 80,70,87
U 81,71,87
K 39,80,81
Zvaa 240,0,40
Ta 20,24,8192
Ta 22,24,9216
v_lshlrev_b32 v80, 16, v39
v_and_b32 v81, 0xffff0000, v39
v_lshlrev_b32 v82, 16, v51
v_and_b32 v83, 0xffff0000, v51
Mvaa 240,4,136
v_fmac_f32 v84, v80, v82
v_fmac_f32 v84, v81, v83
v_sub_f32 v72, v72, v85
v_sub_f32 v73, v73, v85
K 80,72,73
v_lshlrev_b32 v72, 16, v80
Zaaa 176,4,140
v_and_b32 v73, 0xffff0000, v80
U 80,72,87
U 81,73,87
K 40,80,81
v_lshlrev_b32 v80, 16, v40
v_and_b32 v81, 0xffff0000, v40
Maaa 176,0,44
v_lshlrev_b32 v82, 16, v52
v_and_b32 v83, 0xffff0000, v52
v_fmac_f32 v84, v80, v82
v_fmac_f32 v84, v81, v83
v_sub_f32 v74, v74, v85
Zaaa 208,0,48
v_sub_f32 v75, v75, v85
K 80,74,75
v_lshlrev_b32 v74, 16, v80
v_and_b32 v75, 0xffff0000, v80
U 80,74,87
Maaa 208,4,144
U 81,75,87
K 41,80,81
v_lshlrev_b32 v80, 16, v41
v_and_b32 v81, 0xffff0000, v41
v_lshlrev_b32 v82, 16, v53
Zaaa 240,4,148
v_and_b32 v83, 0xffff0000, v53
v_fmac_f32 v84, v80, v82
v_fmac_f32 v84, v81, v83
v_sub_f32 v76, v76, v85
v_sub_f32 v77, v77, v85
Maaa 240,0,52
K 80,76,77
v_lshlrev_b32 v76, 16, v80
v_and_b32 v77, 0xffff0000, v80
U 80,76,87
U 81,77,87
Zaaa 80,0,56
K 42,80,81
v_lshlrev_b32 v80, 16, v42
v_and_b32 v81, 0xffff0000, v42
v_lshlrev_b32 v82, 16, v54
v_and_b32 v83, 0xffff0000, v54
Maaa 80,4,152
v_fmac_f32 v84, v80, v82
v_fmac_f32 v84, v81, v83
v_sub_f32 v78, v78, v85
v_sub_f32 v79, v79, v85
K 80,78,79
v_lshlrev_b32 v78, 16, v80
Zaaa 112,4,156
v_and_b32 v79, 0xffff0000, v80
U 80,78,87
U 81,79,87
K 43,80,81
v_lshlrev_b32 v80, 16, v43
Maaa 112,0,60
W 4
v_and_b32 v81, 0xffff0000, v43
)"
R"(v_lshlrev_b32 v82, 16, v55
v_and_b32 v83, 0xffff0000, v55
v_fmac_f32 v84, v80, v82
v_fmac_f32 v84, v81, v83
Zaaa 116,8,60
Ta 24,25,0
Ta 26,25,1024
v_lshrrev_b32 v82, 9, v1
s_lshl_b32 s80, s14, 1
v_add_u32 v82, s80, v82
v_mul_u32_u24 v83, 1552, v82
v_add_u32 v83, v83, v88
Maaa 116,12,156
v_add_u32 v83, 131072, v83
ds_write_b128 v83, v[32:35] offset:0
ds_write_b128 v83, v[36:39] offset:512
ds_write_b128 v83, v[40:43] offset:1024
v_add_f32_dpp v84, v84, v84 quad_perm:[1,0,3,2] row_mask:0xf bank_mask:0xf
Zaaa 84,12,152
Ta 28,25,8192
Ta 30,25,9216
Maaa 84,8,56
v_add_f32_dpp v84, v84, v84 quad_perm:[2,3,0,1] row_mask:0xf bank_mask:0xf
s_nop 1
v_add_f32_dpp v84, v84, v84 row_half_mirror row_mask:0xf bank_mask:0xf
s_nop 1
v_add_f32_dpp v84, v84, v84 row_mirror row_mask:0xf bank_mask:0xf
v_mov_b32 v81, v84
s_nop 1
v_permlane16_swap_b32 v84, v81
A 84,84,81
Zaaa 244,8,52
K 80,84,84
v_lshlrev_b32 v80, 16, v80
s_lshl_b32 s80, s15, 3
v_add_u32 v82, s80, v82
v_sub_u32 v83, 2047, v82
v_ashrrev_i32 v81, 31, v80
v_or_b32 v81, 0x80000000, v81
Maaa 244,12,148
v_xor_b32 v80, v80, v81
v_and_b32 v80, 0xffff0000, v80
v_or_b32 v80, v80, v83
v_and_b32 v82, 7, v82
v_lshlrev_b32 v82, 2, v82
v_add_u32 v82, 143488, v82
Zaaa 212,12,144
ds_write_b32 v82, v80
Maaa 212,8,48
Zaaa 180,8,44
Maaa 180,12,140
Zvaa 244,12,136
s_waitcnt vmcnt(1) lgkmcnt(0)
s_barrier
Mvaa 244,8,40
s_add_u32 s64, s64, 128
v_bfe_u32 v80, v1, 4, 3
Zvaa 212,8,36
v_mul_u32_u24 v81, 1552, v80
Mvaa 212,12,132
s_and_b32 s64, s64, 1023
v_lshrrev_b32 v82, 7, v1
Zvaa 180,12,128
v_lshl_add_u32 v81, v82, 4, v81
Mvaa 180,8,32
s_lshl_b32 s80, s14, 7
Zvaa 184,16,32
s_lshl_b32 s65, s64, 12
s_add_u32 s80, s80, 131072
Mvaa 184,20,128
v_add_u32 v81, s80, v81
Zvaa 216,20,132
ds_read_b128 v[32:35], v81
Ta 0,10,32768
Mvaa 216,16,36
s_add_u32 m0, s62, 0
Ta 2,10,33792
Rv 36,81,512
Zvaa 248,16,40
s_and_b32 s80, s14, 2
Ta 4,10,40960
Ta 6,10,41984
Mvaa 248,20,136
s_lshl_b32 s80, s80, 7
Zaaa 184,20,140
D 16,44,65,0
v_subrev_u32 v81, s80, v81
Maaa 184,16,44
Rv 40,81,1024
Zaaa 216,16,48
v_lshlrev_b32 v80, 2, v80
Maaa 216,20,144
v_add_u32 v80, 143488, v80
Zaaa 248,20,148
D 17,44,65,1024
ds_read_b32 v84, v80
Maaa 248,16,52
v_mov_b32 v85, s13
Zaaa 88,16,56
v_lshrrev_b32 v83, 1, v1
Maaa 88,20,152
s_mul_i32 s82, s15, 10240
Zaaa 120,20,156
s_add_u32 s82, s82, 131072
D 18,44,65,2048
Maaa 120,16,60
s_lshl_b32 s80, s14, 10
Zaaa 124,24,60
s_add_u32 s83, s82, s80
Ra 60,26,47104
Maaa 124,28,156
Ra 156,27,47104
Zaaa 92,28,152
D 19,44,65,3072
Ra 152,27,45056
Maaa 92,24,56
s_add_u32 m0, s62, 4096
Ta 8,11,32768
Ta 10,11,33792
Zaaa 252,24,52
Ra 56,26,45056
Ta 12,11,40960
Maaa 252,28,148
Ta 14,11,41984
Ra 52,26,43008
Zaaa 220,28,144
W 9
buffer_store_dwordx4 v[32:35], v1, s[8:11], s83 offen sc1
s_add_u32 s83, s83, 4096
Maaa 220,24,48
Ra 148,27,43008
Ra 144,27,40960
Zaaa 188,24,44
Ra 48,26,40960
Ra 44,26,38912
Maaa 188,28,140
Ra 140,27,38912
Zvaa 252,28,136
D 20,44,65,0
Ra 136,27,36864
Mvaa 252,24,40
Ra 40,26,36864
Zvaa 220,24,36
Ra 36,26,34816
Mvaa 220,28,132
Ra 132,27,34816
Zvaa 188,28,128
buffer_store_dwordx4 v[36:39], v1, s[8:11], s83 offen sc1
Ra 128,27,32768
Mvaa 188,24,32
W 13
s_and_b32 s80, s80, 1024
s_add_u32 s83, s82, s80
Maaa 96,0,60
s_add_u32 s83, s83, 8192
Ra 32,26,32768
Maaa 96,4,156
Maaa 64,4,152
D 21,44,65,1024
Ta 16,12,32768
Maaa 64,0,56
W 9
Ta 18,12,33792
Ta 20,12,40960
Maaa 224,0,52
Ta 22,12,41984
Maaa 224,4,148
Maaa 192,4,144
buffer_store_dwordx4 v[40:43], v1, s[8:11], s83 offen sc1
Maaa 192,0,48
W 8
Maaa 160,0,44
Maaa 160,4,140
Mvaa 224,4,136
D 22,44,65,2048
Mvaa 224,0,40
W 4
Mvaa 192,0,36
Mvaa 192,4,132
Mvaa 160,4,128
D 23,44,65,3072
Mvaa 160,0,32
s_add_u32 m0, s61, 0
Mvaa 164,8,32
Mvaa 164,12,128
Mvaa 196,12,132
D 2,40,64,0
Ta 24,13,32768
Mvaa 196,8,36
Ta 26,13,33792
Ta 28,13,40960
Mvaa 228,8,40
Ta 30,13,41984
Mvaa 228,12,136
.Lh_0_1%=:
s_cbranch_execz .Lh_0_1_1%=
Maaa 164,12,140
D 3,40,64,1024
Maaa 164,8,44
Maaa 196,8,48
Maaa 196,12,144
Maaa 228,12,148
D 4,40,64,2048
Maaa 228,8,52
Maaa 68,8,56
Maaa 68,12,152
Maaa 100,12,156
D 5,40,64,3072
Maaa 100,8,60
W 4
s_add_u32 m0, s61, 4096
Maaa 104,16,60
Maaa 104,20,156
Maaa 72,20,152
D 6,40,64,0
Ta 0,14,32768
Maaa 72,16,56
Ta 2,14,33792
Ta 4,14,40960
Maaa 232,16,52
Ta 6,14,41984
Maaa 232,20,148
Maaa 200,20,144
D 7,40,64,1024
Maaa 200,16,48
Maaa 168,16,44
Maaa 168,20,140
Mvaa 232,20,136
D 8,40,64,2048
Mvaa 232,16,40
Mvaa 200,16,36
Mvaa 200,20,132
Mvaa 168,20,128
D 9,40,64,3072
v_bfe_u32 v147, v1, 5, 3
Mvaa 168,16,32
W 4
v_lshlrev_b32 v147, 13, v147
v_and_b32 v148, 16, v1
Mvaa 172,24,32
v_lshl_add_u32 v147, v148, 3, v147
Mvaa 172,28,128
s_lshl_b32 s92, s57, 2
s_add_u32 s92, s92, s14
s_lshl_b32 s92, s92, 16
Mvaa 204,28,132
buffer_load_dword v142, v147, s[48:51], s92 offen
Ta 8,15,32768
Mvaa 204,24,36
Ta 10,15,33792
Ta 12,15,40960
Mvaa 236,24,40
Ta 14,15,41984
Mvaa 236,28,136
Maaa 172,28,140
Maaa 172,24,44
Maaa 204,24,48
Maaa 204,28,144
Maaa 236,28,148
Maaa 236,24,52
Maaa 76,24,56
Maaa 76,28,152
Maaa 108,28,156
Maaa 108,24,60
W 4
Maaa 112,0,60
Maaa 112,4,156
Maaa 80,4,152
Ta 16,24,32768
Ta 18,24,33792
Maaa 80,0,56
Ta 20,24,40960
Ta 22,24,41984
Maaa 240,0,52
Maaa 240,4,148
Maaa 208,4,144
Maaa 208,0,48
Maaa 176,0,44
Maaa 176,4,140
Mvaa 240,4,136
Mvaa 240,0,40
Mvaa 208,0,36
Mvaa 208,4,132
Mvaa 176,4,128
Mvaa 176,0,32
W 4
Mvaa 180,8,32
Ta 24,25,32768
Ta 26,25,33792
Mvaa 180,12,128
Mvaa 212,12,132
Ta 28,25,40960
Ta 30,25,41984
Mvaa 212,8,36
Mvaa 244,8,40
Mvaa 244,12,136
Maaa 180,12,140
Maaa 180,8,44
Maaa 212,8,48
Maaa 212,12,144
Maaa 244,12,148
s_waitcnt vmcnt(1) lgkmcnt(0)
s_barrier
s_cmp_eq_u32 s14, 0
s_cbranch_scc0 .Lpubr_10%=
s_lshl_b32 s80, s15, 6
s_add_u32 s80, s80, 40960
s_mov_b64 exec, 0xff
buffer_store_dwordx2 v[84:85], v83, s[8:11], s80 offen sc1
s_mov_b64 exec, -1
.Lpubr_10%=:
Maaa 244,8,52
s_add_u32 s64, s64, 128
Maaa 84,8,56
Maaa 84,12,152
s_and_b32 s64, s64, 1023
Maaa 116,12,156
Maaa 116,8,60
Maaa 120,16,60
s_lshl_b32 s65, s64, 12
Maaa 120,20,156
Maaa 88,20,152
s_add_u32 m0, s62, 32768
Ta 0,10,0
Ta 2,10,1024
Maaa 88,16,56
Ta 4,10,8192
Ta 6,10,9216
Maaa 248,16,52
Maaa 248,20,148
D 16,44,65,0
Maaa 216,20,144
Maaa 216,16,48
Maaa 184,16,44
Maaa 184,20,140
D 17,44,65,1024
Mvaa 248,20,136
Mvaa 248,16,40
Mvaa 216,16,36
Mvaa 216,20,132
D 18,44,65,2048
Mvaa 184,20,128
Mvaa 184,16,32
Mvaa 188,24,32
Ra 32,26,0
Mvaa 188,28,128
D 19,44,65,3072
Ra 128,27,0
Mvaa 220,28,132
s_add_u32 m0, s62, 36864
Ra 132,27,2048
Ta 8,11,0
Mvaa 220,24,36
Ta 10,11,1024
Ra 36,26,2048
Mvaa 252,24,40
Ta 12,11,8192
Ta 14,11,9216
Mvaa 252,28,136
D 20,44,65,0
Ra 40,26,4096
Maaa 188,28,140
Ra 136,27,4096
Ra 140,27,6144
Maaa 188,24,44
Ra 44,26,6144
Maaa 220,24,48
Ra 48,26,8192
Maaa 220,28,144
D 21,44,65,1024
Ra 144,27,8192
Maaa 252,28,148
Ra 148,27,10240
Maaa 252,24,52
Ra 52,26,10240
Maaa 92,24,56
Ra 56,26,12288
Maaa 92,28,152
D 22,44,65,2048
Ra 152,27,12288
Maaa 124,28,156
Ra 156,27,14336
Maaa 124,24,60
W 13
Ra 60,26,14336
Mvaa 160,0,32
Mvaa 160,4,128
D 23,44,65,3072
Mvaa 192,4,132
s_add_u32 m0, s61, 32768
Ta 16,12,0
Ta 18,12,1024
Mvaa 192,0,36
W 10
Ta 20,12,8192
Ta 22,12,9216
Mvaa 224,0,40
Mvaa 224,4,136
D 2,40,64,0
Maaa 160,4,140
)"
R"(Maaa 160,0,44
W 8
Maaa 192,0,48
Maaa 192,4,144
D 3,40,64,1024
Maaa 224,4,148
Maaa 224,0,52
W 4
Maaa 64,0,56
Maaa 64,4,152
D 4,40,64,2048
Maaa 96,4,156
Maaa 96,0,60
Maaa 100,8,60
Maaa 100,12,156
D 5,40,64,3072
Maaa 68,12,152
s_add_u32 m0, s61, 36864
Ta 24,13,0
Ta 26,13,1024
Maaa 68,8,56
Ta 28,13,8192
Ta 30,13,9216
Maaa 228,8,52
Maaa 228,12,148
D 6,40,64,0
Maaa 196,12,144
Maaa 196,8,48
Maaa 164,8,44
Maaa 164,12,140
D 7,40,64,1024
Mvaa 228,12,136
Mvaa 228,8,40
Mvaa 196,8,36
Mvaa 196,12,132
D 8,40,64,2048
Mvaa 164,12,128
Mvaa 164,8,32
W 4
Mvaa 168,16,32
Mvaa 168,20,128
D 9,40,64,3072
Mvaa 200,20,132
Ta 0,14,0
Ta 2,14,1024
Mvaa 200,16,36
Ta 4,14,8192
Ta 6,14,9216
Mvaa 232,16,40
Mvaa 232,20,136
global_load_dword v142, v[140:141], off
v_lshl_add_u64 v[140:141], v[140:141], 0, s[68:69]
Maaa 168,20,140
Maaa 168,16,44
Maaa 200,16,48
Maaa 200,20,144
Maaa 232,20,148
Maaa 232,16,52
Maaa 72,16,56
Maaa 72,20,152
Maaa 104,20,156
Maaa 104,16,60
W 4
Maaa 108,24,60
Maaa 108,28,156
Maaa 76,28,152
Ta 8,15,0
Ta 10,15,1024
Maaa 76,24,56
Ta 12,15,8192
Ta 14,15,9216
Maaa 236,24,52
Maaa 236,28,148
Maaa 204,28,144
Maaa 204,24,48
Maaa 172,24,44
Maaa 172,28,140
Mvaa 236,28,136
Mvaa 236,24,40
Mvaa 204,24,36
Mvaa 204,28,132
Mvaa 172,28,128
Mvaa 172,24,32
W 4
Mvaa 176,0,32
Mvaa 176,4,128
Mvaa 208,4,132
Ta 16,24,0
Ta 18,24,1024
Mvaa 208,0,36
Ta 20,24,8192
Ta 22,24,9216
Mvaa 240,0,40
Mvaa 240,4,136
Maaa 176,4,140
Maaa 176,0,44
Maaa 208,0,48
Maaa 208,4,144
Maaa 240,4,148
Maaa 240,0,52
Maaa 80,0,56
Maaa 80,4,152
Maaa 112,4,156
Maaa 112,0,60
W 4
Maaa 116,8,60
Ta 24,25,0
Ta 26,25,1024
Maaa 116,12,156
Maaa 84,12,152
Ta 28,25,8192
Ta 30,25,9216
Maaa 84,8,56
Maaa 244,8,52
Maaa 244,12,148
Maaa 212,12,144
Maaa 212,8,48
Maaa 180,8,44
Maaa 180,12,140
Mvaa 244,12,136
s_waitcnt vmcnt(1) lgkmcnt(0)
s_barrier
Mvaa 244,8,40
s_add_u32 s64, s64, 128
Mvaa 212,8,36
Mvaa 212,12,132
s_and_b32 s64, s64, 1023
Mvaa 180,12,128
Mvaa 180,8,32
Mvaa 184,16,32
s_lshl_b32 s65, s64, 12
Mvaa 184,20,128
Mvaa 216,20,132
s_add_u32 m0, s62, 0
Ta 0,10,32768
Ta 2,10,33792
Mvaa 216,16,36
Ta 4,10,40960
Ta 6,10,41984
Mvaa 248,16,40
Mvaa 248,20,136
D 16,44,65,0
Maaa 184,20,140
Maaa 184,16,44
Maaa 216,16,48
Maaa 216,20,144
D 17,44,65,1024
Maaa 248,20,148
Maaa 248,16,52
Maaa 88,16,56
Maaa 88,20,152
D 18,44,65,2048
Maaa 120,20,156
Maaa 120,16,60
Maaa 124,24,60
Ra 60,26,47104
Maaa 124,28,156
D 19,44,65,3072
Ra 156,27,47104
Maaa 92,28,152
s_add_u32 m0, s62, 4096
Ra 152,27,45056
Ta 8,11,32768
Maaa 92,24,56
Ta 10,11,33792
Ra 56,26,45056
Maaa 252,24,52
Ta 12,11,40960
Ta 14,11,41984
Maaa 252,28,148
D 20,44,65,0
Ra 52,26,43008
Maaa 220,28,144
Ra 148,27,43008
Ra 144,27,40960
Maaa 220,24,48
Ra 48,26,40960
Maaa 188,24,44
Ra 44,26,38912
Maaa 188,28,140
D 21,44,65,1024
Ra 140,27,38912
Mvaa 252,28,136
Ra 136,27,36864
Mvaa 252,24,40
Ra 40,26,36864
Mvaa 220,24,36
Ra 36,26,34816
Mvaa 220,28,132
D 22,44,65,2048
Ra 132,27,34816
Mvaa 188,28,128
Ra 128,27,32768
Mvaa 188,24,32
W 13
Ra 32,26,32768
Maaa 96,0,60
Maaa 96,4,156
D 23,44,65,3072
Maaa 64,4,152
s_add_u32 m0, s61, 0
Ta 16,12,32768
Ta 18,12,33792
Maaa 64,0,56
W 10
Ta 20,12,40960
Ta 22,12,41984
Maaa 224,0,52
Maaa 224,4,148
D 2,40,64,0
Maaa 192,4,144
Maaa 192,0,48
W 8
Maaa 160,0,44
Maaa 160,4,140
D 3,40,64,1024
Mvaa 224,4,136
Mvaa 224,0,40
W 4
Mvaa 192,0,36
Mvaa 192,4,132
D 4,40,64,2048
Mvaa 160,4,128
Mvaa 160,0,32
Mvaa 164,8,32
Mvaa 164,12,128
D 5,40,64,3072
Mvaa 196,12,132
s_add_u32 m0, s61, 4096
Ta 24,13,32768
Ta 26,13,33792
Mvaa 196,8,36
Ta 28,13,40960
Ta 30,13,41984
Mvaa 228,8,40
Mvaa 228,12,136
D 6,40,64,0
Maaa 164,12,140
Maaa 164,8,44
Maaa 196,8,48
Maaa 196,12,144
D 7,40,64,1024
Maaa 228,12,148
Maaa 228,8,52
Maaa 68,8,56
Maaa 68,12,152
D 8,40,64,2048
Maaa 100,12,156
Maaa 100,8,60
W 4
Maaa 104,16,60
Maaa 104,20,156
D 9,40,64,3072
Maaa 72,20,152
Ta 0,14,32768
Ta 2,14,33792
Maaa 72,16,56
Ta 4,14,40960
Ta 6,14,41984
Maaa 232,16,52
Maaa 232,20,148
global_load_dword v142, v[140:141], off
v_lshl_add_u64 v[140:141], v[140:141], 0, s[68:69]
Maaa 200,20,144
Maaa 200,16,48
Maaa 168,16,44
Maaa 168,20,140
Mvaa 232,20,136
Mvaa 232,16,40
Mvaa 200,16,36
Mvaa 200,20,132
Mvaa 168,20,128
Mvaa 168,16,32
W 4
Mvaa 172,24,32
Mvaa 172,28,128
Mvaa 204,28,132
Ta 8,15,32768
Ta 10,15,33792
Mvaa 204,24,36
Ta 12,15,40960
Ta 14,15,41984
Mvaa 236,24,40
Mvaa 236,28,136
Maaa 172,28,140
Maaa 172,24,44
Maaa 204,24,48
Maaa 204,28,144
Maaa 236,28,148
Maaa 236,24,52
Maaa 76,24,56
Maaa 76,28,152
Maaa 108,28,156
Maaa 108,24,60
W 4
Maaa 112,0,60
Maaa 112,4,156
Maaa 80,4,152
Ta 16,24,32768
Ta 18,24,33792
Maaa 80,0,56
Ta 20,24,40960
Ta 22,24,41984
Maaa 240,0,52
Maaa 240,4,148
Maaa 208,4,144
Maaa 208,0,48
Maaa 176,0,44
Maaa 176,4,140
Mvaa 240,4,136
Mvaa 240,0,40
Mvaa 208,0,36
Mvaa 208,4,132
Mvaa 176,4,128
Mvaa 176,0,32
W 4
Mvaa 180,8,32
Ta 24,25,32768
Ta 26,25,33792
Mvaa 180,12,128
Mvaa 212,12,132
Ta 28,25,40960
Ta 30,25,41984
Mvaa 212,8,36
Mvaa 244,8,40
Mvaa 244,12,136
.Lh_0_2%=:
s_cbranch_execz .Lh_0_2_1%=
Maaa 180,12,140
Maaa 180,8,44
Maaa 212,8,48
Maaa 212,12,144
Maaa 244,12,148
s_waitcnt vmcnt(1) lgkmcnt(0)
s_barrier
Maaa 244,8,52
s_add_u32 s64, s64, 128
Maaa 84,8,56
Maaa 84,12,152
s_and_b32 s64, s64, 1023
Maaa 116,12,156
Maaa 116,8,60
Maaa 120,16,60
s_lshl_b32 s65, s64, 12
Maaa 120,20,156
Maaa 88,20,152
Ta 0,10,0
Ta 2,10,1024
Maaa 88,16,56
s_add_u32 m0, s62, 32768
Ta 4,10,8192
Ta 6,10,9216
Maaa 248,16,52
Maaa 248,20,148
Maaa 216,20,144
D 16,44,65,0
Maaa 216,16,48
Maaa 184,16,44
Maaa 184,20,140
Mvaa 248,20,136
D 17,44,65,1024
Mvaa 248,16,40
Mvaa 216,16,36
Mvaa 216,20,132
Mvaa 184,20,128
D 18,44,65,2048
Mvaa 184,16,32
Mvaa 188,24,32
Ra 32,26,0
Mvaa 188,28,128
Ra 128,27,0
Mvaa 220,28,132
D 19,44,65,3072
Ra 132,27,2048
Mvaa 220,24,36
s_add_u32 m0, s62, 36864
Ta 8,11,0
Ta 10,11,1024
Mvaa 252,24,40
Ra 36,26,2048
Ta 12,11,8192
Mvaa 252,28,136
Ta 14,11,9216
Ra 40,26,4096
Maaa 188,28,140
D 20,44,65,0
Ra 136,27,4096
Maaa 188,24,44
Ra 140,27,6144
Ra 44,26,6144
Maaa 220,24,48
Ra 48,26,8192
Maaa 220,28,144
Ra 144,27,8192
Maaa 252,28,148
D 21,44,65,1024
Ra 148,27,10240
Maaa 252,24,52
Ra 52,26,10240
Maaa 92,24,56
)"
R"(Ra 56,26,12288
Maaa 92,28,152
Ra 152,27,12288
Maaa 124,28,156
D 22,44,65,2048
Ra 156,27,14336
Maaa 124,24,60
W 13
Ra 60,26,14336
Mvaa 160,0,32
Mvaa 160,4,128
Mvaa 192,4,132
D 23,44,65,3072
Ta 16,12,0
Mvaa 192,0,36
W 9
s_add_u32 m0, s61, 32768
Ta 18,12,1024
Mvaa 224,0,40
Ta 20,12,8192
Ta 22,12,9216
Mvaa 224,4,136
Maaa 160,4,140
D 2,40,64,0
Maaa 160,0,44
W 8
Maaa 192,0,48
Maaa 192,4,144
Maaa 224,4,148
D 3,40,64,1024
Maaa 224,0,52
W 4
Maaa 64,0,56
Maaa 64,4,152
Maaa 96,4,156
D 4,40,64,2048
Maaa 96,0,60
Maaa 100,8,60
Maaa 100,12,156
Maaa 68,12,152
D 5,40,64,3072
Ta 24,13,0
Maaa 68,8,56
s_add_u32 m0, s61, 36864
Ta 26,13,1024
Ta 28,13,8192
Maaa 228,8,52
Ta 30,13,9216
Maaa 228,12,148
Maaa 196,12,144
D 6,40,64,0
Maaa 196,8,48
Maaa 164,8,44
Maaa 164,12,140
Mvaa 228,12,136
D 7,40,64,1024
Mvaa 228,8,40
Mvaa 196,8,36
Mvaa 196,12,132
Mvaa 164,12,128
D 8,40,64,2048
Mvaa 164,8,32
W 4
Mvaa 168,16,32
Mvaa 168,20,128
Mvaa 200,20,132
D 9,40,64,3072
Ta 0,14,0
Mvaa 200,16,36
s_add_u32 s92, s92, 2097152
Ta 2,14,1024
Ta 4,14,8192
Mvaa 232,16,40
Ta 6,14,9216
Mvaa 232,20,136
Maaa 168,20,140
buffer_load_dword v142, v147, s[48:51], s92 offen
Maaa 168,16,44
s_add_u32 s92, s92, 2097152
Maaa 200,16,48
Maaa 200,20,144
Maaa 232,20,148
buffer_load_dword v142, v147, s[48:51], s92 offen
Maaa 232,16,52
Maaa 72,16,56
Maaa 72,20,152
Maaa 104,20,156
Maaa 104,16,60
W 4
Maaa 108,24,60
Maaa 108,28,156
Maaa 76,28,152
Ta 8,15,0
Ta 10,15,1024
Maaa 76,24,56
Ta 12,15,8192
Ta 14,15,9216
Maaa 236,24,52
Maaa 236,28,148
Maaa 204,28,144
Maaa 204,24,48
Maaa 172,24,44
Maaa 172,28,140
Mvaa 236,28,136
Mvaa 236,24,40
Mvaa 204,24,36
Mvaa 204,28,132
Mvaa 172,28,128
Mvaa 172,24,32
W 4
Mvaa 176,0,32
Mvaa 176,4,128
Mvaa 208,4,132
Ta 16,24,0
Ta 18,24,1024
Mvaa 208,0,36
Ta 20,24,8192
Ta 22,24,9216
Mvaa 240,0,40
Mvaa 240,4,136
Maaa 176,4,140
Maaa 176,0,44
Maaa 208,0,48
Maaa 208,4,144
Maaa 240,4,148
Maaa 240,0,52
Maaa 80,0,56
Maaa 80,4,152
Maaa 112,4,156
Maaa 112,0,60
W 4
Maaa 116,8,60
Ta 24,25,0
Ta 26,25,1024
Maaa 116,12,156
Maaa 84,12,152
Ta 28,25,8192
Ta 30,25,9216
Maaa 84,8,56
Maaa 244,8,52
Maaa 244,12,148
Maaa 212,12,144
Maaa 212,8,48
Maaa 180,8,44
Maaa 180,12,140
Mvaa 244,12,136
s_waitcnt vmcnt(2) lgkmcnt(0)
s_barrier
Mvaa 244,8,40
s_add_u32 s64, s64, 128
Mvaa 212,8,36
Mvaa 212,12,132
s_and_b32 s64, s64, 1023
Mvaa 180,12,128
Mvaa 180,8,32
Mvaa 184,16,32
s_lshl_b32 s65, s64, 12
Mvaa 184,20,128
Mvaa 216,20,132
Ta 0,10,32768
Ta 2,10,33792
Mvaa 216,16,36
s_add_u32 m0, s62, 0
Ta 4,10,40960
Ta 6,10,41984
Mvaa 248,16,40
Mvaa 248,20,136
Maaa 184,20,140
D 16,44,65,0
Maaa 184,16,44
Maaa 216,16,48
Maaa 216,20,144
Maaa 248,20,148
D 17,44,65,1024
Maaa 248,16,52
Maaa 88,16,56
Maaa 88,20,152
Maaa 120,20,156
D 18,44,65,2048
Maaa 120,16,60
Maaa 124,24,60
Ra 60,26,47104
Maaa 124,28,156
Ra 156,27,47104
Maaa 92,28,152
D 19,44,65,3072
Ra 152,27,45056
Maaa 92,24,56
s_add_u32 m0, s62, 4096
Ta 8,11,32768
Ta 10,11,33792
Maaa 252,24,52
Ra 56,26,45056
Ta 12,11,40960
Maaa 252,28,148
Ta 14,11,41984
Ra 52,26,43008
Maaa 220,28,144
D 20,44,65,0
v_lshrrev_b32 v28, 1, v1
Maaa 220,24,48
s_lshl_b32 s80, s60, 9
Ra 148,27,43008
Ra 144,27,40960
Maaa 188,24,44
s_add_u32 s80, s80, 40960
Ra 48,26,40960
Ra 44,26,38912
Maaa 188,28,140
Ra 140,27,38912
Mvaa 252,28,136
D 21,44,65,1024
Ra 136,27,36864
Mvaa 252,24,40
Ra 40,26,36864
Mvaa 220,24,36
Ra 36,26,34816
Mvaa 220,28,132
Ra 132,27,34816
Mvaa 188,28,128
buffer_load_dwordx2 v[28:29], v28, s[8:11], s80 offen sc1
Ra 128,27,32768
Mvaa 188,24,32
W 13
Ra 32,26,32768
Maaa 96,0,60
Maaa 96,4,156
Maaa 64,4,152
D 22,44,65,2048
Ta 16,12,32768
Maaa 64,0,56
W 9
Ta 18,12,33792
Ta 20,12,40960
Maaa 224,0,52
Ta 22,12,41984
Maaa 224,4,148
Maaa 192,4,144
D 23,44,65,3072
Maaa 192,0,48
W 8
s_add_u32 m0, s61, 0
Maaa 160,0,44
Maaa 160,4,140
Mvaa 224,4,136
D 2,40,64,0
Mvaa 224,0,40
W 4
Mvaa 192,0,36
Mvaa 192,4,132
Mvaa 160,4,128
D 3,40,64,1024
Mvaa 160,0,32
Mvaa 164,8,32
Mvaa 164,12,128
Mvaa 196,12,132
D 4,40,64,2048
Ta 24,13,32768
Mvaa 196,8,36
Ta 26,13,33792
Ta 28,13,40960
Mvaa 228,8,40
Ta 30,13,41984
Mvaa 228,12,136
Maaa 164,12,140
D 5,40,64,3072
Maaa 164,8,44
s_add_u32 m0, s61, 4096
Maaa 196,8,48
Maaa 196,12,144
Maaa 228,12,148
D 6,40,64,0
Maaa 228,8,52
Maaa 68,8,56
Maaa 68,12,152
Maaa 100,12,156
D 7,40,64,1024
Maaa 100,8,60
W 4
Maaa 104,16,60
Maaa 104,20,156
Maaa 72,20,152
D 8,40,64,2048
Ta 0,14,32768
Maaa 72,16,56
Ta 2,14,33792
Ta 4,14,40960
Maaa 232,16,52
Ta 6,14,41984
Maaa 232,20,148
Maaa 200,20,144
D 9,40,64,3072
Maaa 200,16,48
Maaa 168,16,44
Maaa 168,20,140
Mvaa 232,20,136
s_waitcnt vmcnt(10)
v_cmp_eq_u32 vcc, s13, v29
s_cmp_eq_u64 vcc, -1
s_cbranch_scc0 .Lspin_11%=
.Lok_12%=:
global_load_dword v142, v146, s[6:7]
Mvaa 232,16,40
Mvaa 200,16,36
v_bfe_u32 v144, v1, 8, 1
Mvaa 200,20,132
v_mul_u32_u24 v144, 10240, v144
Mvaa 168,20,128
v_and_b32 v145, 0xf0, v1
Mvaa 168,16,32
W 4
v_lshl_add_u32 v144, v145, 3, v144
Mvaa 172,24,32
s_mul_i32 s91, s58, 20480
Mvaa 172,28,128
s_add_u32 s91, s91, s28
Mvaa 204,28,132
buffer_load_dword v142, v144, s[8:11], s91 offen offset:0 sc1
Ta 8,15,32768
Mvaa 204,24,36
Ta 10,15,33792
Ta 12,15,40960
Mvaa 236,24,40
Ta 14,15,41984
Mvaa 236,28,136
Maaa 172,28,140
Maaa 172,24,44
Maaa 204,24,48
Maaa 204,28,144
Maaa 236,28,148
Maaa 236,24,52
Maaa 76,24,56
Maaa 76,28,152
Maaa 108,28,156
Maaa 108,24,60
W 4
Maaa 112,0,60
Maaa 112,4,156
Maaa 80,4,152
Ta 16,24,32768
Ta 18,24,33792
Maaa 80,0,56
Ta 20,24,40960
Ta 22,24,41984
Maaa 240,0,52
Maaa 240,4,148
Maaa 208,4,144
Maaa 208,0,48
Maaa 176,0,44
Maaa 176,4,140
Mvaa 240,4,136
Mvaa 240,0,40
Mvaa 208,0,36
Mvaa 208,4,132
Mvaa 176,4,128
Mvaa 176,0,32
W 4
Mvaa 180,8,32
Ta 24,25,32768
Ta 26,25,33792
Mvaa 180,12,128
Mvaa 212,12,132
Ta 28,25,40960
Ta 30,25,41984
Mvaa 212,8,36
Mvaa 244,8,40
Mvaa 244,12,136
Maaa 180,12,140
Maaa 180,8,44
Maaa 212,8,48
Maaa 212,12,144
Maaa 244,12,148
s_waitcnt vmcnt(2) lgkmcnt(0)
s_barrier
Maaa 244,8,52
s_add_u32 s64, s64, 128
Maaa 84,8,56
Maaa 84,12,152
s_and_b32 s64, s64, 1023
Maaa 116,12,156
Maaa 116,8,60
Maaa 120,16,60
s_lshl_b32 s65, s64, 12
Maaa 120,20,156
Maaa 88,20,152
Ta 0,10,0
Ta 2,10,1024
Maaa 88,16,56
s_add_u32 m0, s62, 32768
Ta 4,10,8192
Ta 6,10,9216
Maaa 248,16,52
Maaa 248,20,148
Maaa 216,20,144
D 16,44,65,0
Maaa 216,16,48
Maaa 184,16,44
Maaa 184,20,140
Mvaa 248,20,136
D 17,44,65,1024
)"
R"(Mvaa 248,16,40
Mvaa 216,16,36
Mvaa 216,20,132
Mvaa 184,20,128
D 18,44,65,2048
Mvaa 184,16,32
Mvaa 188,24,32
Ra 32,26,0
Mvaa 188,28,128
Ra 128,27,0
Mvaa 220,28,132
D 19,44,65,3072
Ra 132,27,2048
Mvaa 220,24,36
s_add_u32 m0, s62, 36864
Ta 8,11,0
Ta 10,11,1024
Mvaa 252,24,40
Ra 36,26,2048
Ta 12,11,8192
Mvaa 252,28,136
Ta 14,11,9216
Ra 40,26,4096
Maaa 188,28,140
D 20,44,65,0
Ra 136,27,4096
Maaa 188,24,44
Ra 140,27,6144
Ra 44,26,6144
Maaa 220,24,48
Ra 48,26,8192
Maaa 220,28,144
Ra 144,27,8192
Maaa 252,28,148
D 21,44,65,1024
Ra 148,27,10240
Maaa 252,24,52
Ra 52,26,10240
Maaa 92,24,56
Ra 56,26,12288
Maaa 92,28,152
Ra 152,27,12288
Maaa 124,28,156
D 22,44,65,2048
Ra 156,27,14336
Maaa 124,24,60
W 13
Ra 60,26,14336
Mvaa 160,0,32
Mvaa 160,4,128
Mvaa 192,4,132
D 23,44,65,3072
Ta 16,12,0
Mvaa 192,0,36
W 9
s_add_u32 m0, s61, 32768
Ta 18,12,1024
Mvaa 224,0,40
Ta 20,12,8192
Ta 22,12,9216
Mvaa 224,4,136
.Lh_0_3%=:
s_cbranch_execz .Lh_0_3_1%=
Maaa 160,4,140
D 2,40,64,0
Maaa 160,0,44
W 8
Maaa 192,0,48
Maaa 192,4,144
Maaa 224,4,148
D 3,40,64,1024
Maaa 224,0,52
W 4
Maaa 64,0,56
Maaa 64,4,152
Maaa 96,4,156
D 4,40,64,2048
Maaa 96,0,60
Maaa 100,8,60
Maaa 100,12,156
Maaa 68,12,152
D 5,40,64,3072
Ta 24,13,0
Maaa 68,8,56
s_add_u32 m0, s61, 36864
Ta 26,13,1024
Ta 28,13,8192
Maaa 228,8,52
Ta 30,13,9216
Maaa 228,12,148
Maaa 196,12,144
D 6,40,64,0
Maaa 196,8,48
Maaa 164,8,44
Maaa 164,12,140
Mvaa 228,12,136
D 7,40,64,1024
Mvaa 228,8,40
Mvaa 196,8,36
Mvaa 196,12,132
Mvaa 164,12,128
D 8,40,64,2048
Mvaa 164,8,32
W 4
Mvaa 168,16,32
Mvaa 168,20,128
Mvaa 200,20,132
D 9,40,64,3072
v_add_u32 v2, 131072, v10
Mvaa 200,16,36
Ta 0,14,0
v_add_u32 v3, 131072, v11
Mvaa 232,16,40
Ta 2,14,1024
v_add_u32 v4, 131072, v12
Mvaa 232,20,136
Ta 4,14,8192
Ta 6,14,9216
Maaa 168,20,140
buffer_load_dword v142, v144, s[8:11], s91 offen offset:2048 sc1
v_add_u32 v5, 131072, v13
Maaa 168,16,44
s_add_u32 s91, s91, 4096
v_add_u32 v6, 131072, v14
Maaa 200,16,48
v_add_u32 v7, 131072, v15
Maaa 200,20,144
Maaa 232,20,148
v_add_u32 v8, 131072, v24
Maaa 232,16,52
Maaa 72,16,56
v_add_u32 v9, 131072, v25
Maaa 72,20,152
s_add_u32 s65, s5, 0
s_cmp_ge_u32 s65, 20
s_cselect_b32 s90, 20, 0
s_sub_u32 s65, s65, s90
Maaa 104,20,156
s_lshl_b32 s65, s65, 18
s_add_u32 m0, s63, 131072
Maaa 104,16,60
W 4
Maaa 108,24,60
Maaa 108,28,156
Maaa 76,28,152
Ta 8,15,0
Ta 10,15,1024
Maaa 76,24,56
D 16,48,65,0
Ta 12,15,8192
Maaa 236,24,52
Ta 14,15,9216
Maaa 236,28,148
Maaa 204,28,144
Maaa 204,24,48
D 17,48,65,1024
Maaa 172,24,44
Maaa 172,28,140
Mvaa 236,28,136
Mvaa 236,24,40
Mvaa 204,24,36
D 18,48,65,2048
Mvaa 204,28,132
Mvaa 172,28,128
Mvaa 172,24,32
W 4
Mvaa 176,0,32
Mvaa 176,4,128
D 19,48,65,3072
Mvaa 208,4,132
s_add_u32 m0, s63, 135168
Ta 16,24,0
Ta 18,24,1024
Mvaa 208,0,36
Ta 20,24,8192
Ta 22,24,9216
Mvaa 240,0,40
Mvaa 240,4,136
Maaa 176,4,140
D 20,48,65,0
Maaa 176,0,44
Maaa 208,0,48
Maaa 208,4,144
Maaa 240,4,148
Maaa 240,0,52
D 21,48,65,1024
Maaa 80,0,56
Maaa 80,4,152
Maaa 112,4,156
Maaa 112,0,60
W 4
Maaa 116,8,60
D 22,48,65,2048
Ta 24,25,0
Maaa 116,12,156
Ta 26,25,1024
Maaa 84,12,152
Ta 28,25,8192
Ta 30,25,9216
Maaa 84,8,56
Maaa 244,8,52
D 23,48,65,3072
Maaa 244,12,148
Maaa 212,12,144
Maaa 212,8,48
Maaa 180,8,44
Maaa 180,12,140
Mvaa 244,12,136
s_waitcnt vmcnt(9) lgkmcnt(0)
s_barrier
Mvaa 244,8,40
s_add_u32 s65, s5, 4
Mvaa 212,8,36
Mvaa 212,12,132
s_cmp_ge_u32 s65, 20
s_cselect_b32 s90, 20, 0
Mvaa 180,12,128
Mvaa 180,8,32
s_sub_u32 s65, s65, s90
Mvaa 184,16,32
Mvaa 184,20,128
s_lshl_b32 s65, s65, 18
Mvaa 216,20,132
Ta 0,10,32768
Ta 2,10,33792
Mvaa 216,16,36
Mvaa 248,16,40
s_add_u32 m0, s63, 0
Mvaa 248,20,136
Maaa 184,20,140
D 16,48,65,0
Maaa 184,16,44
Maaa 216,16,48
Maaa 216,20,144
Maaa 248,20,148
D 17,48,65,1024
Ta 4,11,32768
Maaa 248,16,52
Ta 6,11,33792
Maaa 88,16,56
Maaa 88,20,152
Maaa 120,20,156
D 18,48,65,2048
Maaa 120,16,60
Maaa 124,24,60
Ra 60,26,47104
Maaa 124,28,156
Maaa 92,28,152
D 19,48,65,3072
Ta 8,12,32768
Maaa 92,24,56
s_add_u32 m0, s63, 4096
Ta 10,12,33792
Ra 56,26,45056
Maaa 252,24,52
Ra 52,26,43008
Maaa 252,28,148
Maaa 220,28,144
D 20,48,65,0
Maaa 220,24,48
Ra 48,26,40960
Maaa 188,24,44
Ra 44,26,38912
Maaa 188,28,140
Mvaa 252,28,136
D 21,48,65,1024
Ta 12,13,32768
Mvaa 252,24,40
Ta 14,13,33792
Ra 40,26,36864
Mvaa 220,24,36
Ra 36,26,34816
Mvaa 220,28,132
Mvaa 188,28,128
D 22,48,65,2048
Mvaa 188,24,32
Ra 32,26,32768
W 6
Maaa 96,0,60
Maaa 64,0,56
Maaa 224,0,52
D 23,48,65,3072
Ta 16,14,32768
Maaa 192,0,48
W 1
s_add_u32 s65, s5, 8
s_cmp_ge_u32 s65, 20
s_cselect_b32 s90, 20, 0
Maaa 160,0,44
s_sub_u32 s65, s65, s90
s_lshl_b32 s65, s65, 18
Ta 18,14,33792
Mvaa 224,0,40
s_add_u32 m0, s63, 65536
Mvaa 192,0,36
D 16,48,65,0
Mvaa 160,0,32
Mvaa 164,4,32
Mvaa 196,4,36
Mvaa 228,4,40
D 17,48,65,1024
Ta 20,15,32768
Maaa 164,4,44
Ta 22,15,33792
Maaa 196,4,48
Maaa 228,4,52
Maaa 68,4,56
D 18,48,65,2048
Maaa 100,4,60
Maaa 104,8,60
Maaa 72,8,56
Maaa 232,8,52
D 19,48,65,3072
Ta 24,24,32768
Maaa 200,8,48
s_add_u32 m0, s63, 69632
Ta 26,24,33792
Maaa 168,8,44
Mvaa 232,8,40
Mvaa 200,8,36
D 20,48,65,0
Mvaa 168,8,32
Mvaa 172,12,32
Mvaa 204,12,36
Mvaa 236,12,40
D 21,48,65,1024
Ta 28,25,32768
Maaa 172,12,44
Ta 30,25,33792
Maaa 204,12,48
Ra 156,27,47104
Maaa 236,12,52
Maaa 76,12,56
D 22,48,65,2048
Maaa 108,12,60
W 7
Ra 152,27,45056
Maaa 112,16,60
Maaa 80,16,56
Maaa 240,16,52
D 23,48,65,3072
Ra 148,27,43008
Maaa 208,16,48
Ta 0,10,40960
Ta 2,10,41984
Maaa 176,16,44
Mvaa 240,16,40
Ra 144,27,40960
Mvaa 208,16,36
buffer_load_dword v142, v144, s[8:11], s91 offen offset:0 sc1
Mvaa 176,16,32
W 10
Mvaa 180,20,32
Ra 140,27,38912
Mvaa 212,20,36
Mvaa 244,20,40
Ta 4,11,40960
Ta 6,11,41984
Maaa 180,20,44
Ra 136,27,36864
Maaa 212,20,48
Maaa 244,20,52
v_bfe_u32 v29, v1, 7, 1
Maaa 84,20,56
Ra 132,27,34816
Maaa 116,20,60
W 13
v_mul_u32_u24 v28, 10240, v29
Maaa 120,24,60
Maaa 88,24,56
Ra 128,27,32768
v_lshrrev_b32 v29, 8, v1
Maaa 248,24,52
Ta 8,12,40960
Ta 10,12,41984
Maaa 216,24,48
v_lshl_add_u32 v28, v29, 7, v28
Maaa 184,24,44
v_and_b32 v29, 0x70, v1
Mvaa 248,24,40
Mvaa 216,24,36
v_add_u32 v28, v28, v29
Mvaa 184,24,32
W 14
Mvaa 188,28,32
s_and_b32 s80, s14, 2
Mvaa 220,28,36
s_mul_i32 s80, s80, 20480
Mvaa 252,28,40
Ta 12,13,40960
Ta 14,13,41984
Maaa 188,28,44
s_sub_u32 s81, 40960, s80
Maaa 220,28,48
Maaa 252,28,52
s_add_u32 s93, s28, 20480
Maaa 92,28,56
Maaa 124,28,60
W 10
v_add_u32 v26, s80, v28
)"
R"(Maaa 96,0,156
Maaa 64,0,152
Maaa 224,0,148
Ta 16,14,40960
v_add_u32 v27, s81, v28
Maaa 192,0,144
W 5
Ta 18,14,41984
Maaa 160,0,140
buffer_load_dwordx4 a[32:35], v26, s[8:11], s28 offen offset:0 sc1
Mvaa 224,0,136
Mvaa 192,0,132
Mvaa 160,0,128
Mvaa 164,4,128
buffer_load_dwordx4 a[36:39], v26, s[8:11], s93 offen offset:0 sc1
Mvaa 196,4,132
Mvaa 228,4,136
Ta 20,15,40960
Ta 22,15,41984
Maaa 164,4,140
Maaa 196,4,144
buffer_load_dwordx4 a[40:43], v27, s[8:11], s28 offen offset:0 sc1
Maaa 228,4,148
Maaa 68,4,152
Maaa 100,4,156
W 6
Maaa 104,8,156
buffer_load_dwordx4 a[44:47], v27, s[8:11], s93 offen offset:0 sc1
Ta 24,24,40960
Maaa 72,8,152
s_add_u32 s28, s28, 512
s_cmp_eq_u32 s28, s4
s_cselect_b32 s93, 10240, 0
Ta 26,24,41984
Maaa 232,8,148
s_sub_u32 s28, s28, s93
Ta 28,25,40960
Ta 30,25,41984
Maaa 200,8,144
s_add_u32 s93, s28, 20480
Maaa 168,8,140
Mvaa 232,8,136
Mvaa 200,8,132
s_add_u32 s65, s5, 12
s_cmp_ge_u32 s65, 20
s_cselect_b32 s90, 20, 0
buffer_load_dwordx4 a[48:51], v26, s[8:11], s28 offen offset:0 sc1
Mvaa 168,8,128
W 8
s_sub_u32 s65, s65, s90
s_lshl_b32 s65, s65, 18
Mvaa 172,12,128
s_add_u32 m0, s63, 32768
Mvaa 204,12,132
Mvaa 236,12,136
s_waitcnt vmcnt(6) lgkmcnt(0)
s_barrier
Maaa 172,12,140
buffer_load_dwordx4 a[52:55], v26, s[8:11], s93 offen offset:0 sc1
Maaa 204,12,144
Maaa 236,12,148
Maaa 76,12,152
Maaa 108,12,156
buffer_load_dwordx4 a[56:59], v27, s[8:11], s28 offen offset:0 sc1
Maaa 112,16,156
Maaa 80,16,152
Maaa 240,16,148
Maaa 208,16,144
buffer_load_dwordx4 a[60:63], v27, s[8:11], s93 offen offset:0 sc1
Maaa 176,16,140
Mvaa 240,16,136
Mvaa 208,16,132
Mvaa 176,16,128
D 16,48,65,0
Mvaa 180,20,128
Mvaa 212,20,132
Mvaa 244,20,136
Ta 0,2,0
Ta 2,2,1024
Maaa 180,20,140
D 17,48,65,1024
Maaa 212,20,144
Maaa 244,20,148
Maaa 84,20,152
Ta 4,2,8192
Ta 6,2,9216
Maaa 116,20,156
D 18,48,65,2048
Maaa 120,24,156
Maaa 88,24,152
Maaa 248,24,148
Ta 8,3,0
Ta 10,3,1024
Maaa 216,24,144
D 19,48,65,3072
Maaa 184,24,140
s_add_u32 m0, s63, 36864
Mvaa 248,24,136
Mvaa 216,24,132
Ta 12,3,8192
Ta 14,3,9216
Mvaa 184,24,128
D 20,48,65,0
Mvaa 188,28,128
Mvaa 220,28,132
Mvaa 252,28,136
Ta 16,4,0
Ta 18,4,1024
Maaa 188,28,140
Maaa 220,28,144
D 21,48,65,1024
Maaa 252,28,148
Maaa 92,28,152
Maaa 124,28,156
s_branch .Lq_7%=
.p2align 12
.Lv_6%=:
s_waitcnt vmcnt(14)
s_barrier
Ta 0,10,16384
Ta 2,10,17408
Ra 32,26,0
Ta 4,10,24576
.Lh_0_0_1%=:
s_cbranch_execz .Lhr_5%=
Ta 6,10,25600
Ra 128,27,0
Ra 132,27,2048
Ra 36,26,2048
Ra 40,26,4096
Ra 136,27,4096
Ra 140,27,6144
Ra 44,26,6144
Ra 48,26,8192
Ra 144,27,8192
W 6
Zvaa 160,32,0
s_add_u32 m0, s61, 32768
Ra 148,27,10240
Ra 52,26,10240
Ra 56,26,12288
Ra 152,27,12288
Ra 156,27,14336
Ra 60,26,14336
Mvaa 160,128,4
Ta 8,11,16384
Ta 10,11,17408
Ta 12,11,24576
Ta 14,11,25600
Zvaa 164,132,4
Ta 16,12,16384
Ta 18,12,17408
Mvaa 164,36,0
W 14
Ta 20,12,24576
Ta 22,12,25600
Zvaa 224,40,0
D 2,40,64,0
Mvaa 224,136,4
Zvaa 228,140,4
Mvaa 228,44,0
W 12
Zaaa 192,48,0
D 3,40,64,1024
Maaa 192,144,4
Zaaa 196,148,4
Maaa 196,52,0
W 8
Zaaa 64,56,0
D 4,40,64,2048
Maaa 64,152,4
Zaaa 68,156,4
Maaa 68,60,0
W 4
Zaaa 76,60,8
D 5,40,64,3072
Maaa 76,156,12
s_add_u32 m0, s61, 36864
Zaaa 72,152,12
Ta 24,13,16384
Ta 26,13,17408
Maaa 72,56,8
Ta 28,13,24576
Ta 30,13,25600
Zaaa 204,52,8
s_waitcnt vmcnt(15)
D 6,40,64,0
Maaa 204,148,12
v_lshlrev_b32 v56, 16, v32
v_and_b32 v57, 0xffff0000, v32
v_lshlrev_b32 v58, 16, v33
v_and_b32 v59, 0xffff0000, v33
v_lshlrev_b32 v60, 16, v34
Zaaa 200,144,12
v_and_b32 v61, 0xffff0000, v34
v_lshlrev_b32 v62, 16, v35
v_and_b32 v63, 0xffff0000, v35
v_lshlrev_b32 v64, 16, v36
v_and_b32 v65, 0xffff0000, v36
Maaa 200,48,8
v_lshlrev_b32 v66, 16, v37
v_and_b32 v67, 0xffff0000, v37
v_lshlrev_b32 v68, 16, v38
v_and_b32 v69, 0xffff0000, v38
v_lshlrev_b32 v70, 16, v39
Zvaa 236,44,8
D 7,40,64,1024
v_and_b32 v71, 0xffff0000, v39
v_lshlrev_b32 v72, 16, v40
v_and_b32 v73, 0xffff0000, v40
v_lshlrev_b32 v74, 16, v41
v_and_b32 v75, 0xffff0000, v41
Mvaa 236,140,12
v_lshlrev_b32 v76, 16, v42
v_and_b32 v77, 0xffff0000, v42
v_lshlrev_b32 v78, 16, v43
v_and_b32 v79, 0xffff0000, v43
A 80,56,57
Zvaa 232,136,12
A 81,58,59
A 80,80,60
A 81,81,61
A 80,80,62
A 81,81,63
Mvaa 232,40,8
A 80,80,64
A 81,81,65
A 80,80,66
A 81,81,67
A 80,80,68
Zvaa 172,36,8
D 8,40,64,2048
A 81,81,69
A 80,80,70
A 81,81,71
A 80,80,72
A 81,81,73
Mvaa 172,132,12
A 80,80,74
A 81,81,75
A 80,80,76
A 81,81,77
A 80,80,78
A 81,81,79
Zvaa 168,128,12
A 80,80,81
s_nop 1
v_add_f32_dpp v80, v80, v80 quad_perm:[1,0,3,2] row_mask:0xf bank_mask:0xf
s_nop 1
v_add_f32_dpp v80, v80, v80 quad_perm:[2,3,0,1] row_mask:0xf bank_mask:0xf
s_nop 1
v_add_f32_dpp v80, v80, v80 row_half_mirror row_mask:0xf bank_mask:0xf
s_nop 1
v_add_f32_dpp v80, v80, v80 row_mirror row_mask:0xf bank_mask:0xf
Mvaa 168,32,8
W 4
v_mov_b32 v81, v80
s_nop 1
v_permlane16_swap_b32 v80, v81
A 80,80,81
v_mul_f32 v84, 0x3acccccd, v80
s_mov_b32 s86, 0xffff
Zvaa 176,32,16
s_mov_b32 s87, 0xffff
D 9,40,64,3072
v_sub_f32 v82, v56, v84
U 80,82,82
v_sub_f32 v83, v57, v84
v_fmac_f32 v80, v83, v83
Mvaa 176,128,20
v_sub_f32 v82, v58, v84
v_fmac_f32 v80, v82, v82
v_sub_f32 v83, v59, v84
v_fmac_f32 v80, v83, v83
v_sub_f32 v82, v60, v84
Zvaa 180,132,20
Ta 0,14,16384
v_fmac_f32 v80, v82, v82
v_sub_f32 v83, v61, v84
v_fmac_f32 v80, v83, v83
v_sub_f32 v82, v62, v84
v_fmac_f32 v80, v82, v82
Mvaa 180,36,16
Ta 2,14,17408
v_sub_f32 v83, v63, v84
v_fmac_f32 v80, v83, v83
v_sub_f32 v82, v64, v84
v_fmac_f32 v80, v82, v82
v_sub_f32 v83, v65, v84
Zvaa 240,40,16
global_load_dword v142, v[140:141], off
v_lshl_add_u64 v[140:141], v[140:141], 0, s[68:69]
Ta 4,14,24576
Ta 6,14,25600
v_fmac_f32 v80, v83, v83
v_sub_f32 v82, v66, v84
v_fmac_f32 v80, v82, v82
Mvaa 240,136,20
v_sub_f32 v83, v67, v84
v_fmac_f32 v80, v83, v83
v_sub_f32 v82, v68, v84
v_fmac_f32 v80, v82, v82
v_sub_f32 v83, v69, v84
v_fmac_f32 v80, v83, v83
Zvaa 244,140,20
v_sub_f32 v82, v70, v84
v_fmac_f32 v80, v82, v82
v_sub_f32 v83, v71, v84
v_fmac_f32 v80, v83, v83
v_sub_f32 v82, v72, v84
U 81,82,82
Mvaa 244,44,16
v_sub_f32 v83, v73, v84
v_fmac_f32 v81, v83, v83
v_sub_f32 v82, v74, v84
v_fmac_f32 v81, v82, v82
v_sub_f32 v83, v75, v84
v_fmac_f32 v81, v83, v83
Zaaa 208,48,16
v_sub_f32 v82, v76, v84
v_fmac_f32 v81, v82, v82
v_sub_f32 v83, v77, v84
v_fmac_f32 v81, v83, v83
v_sub_f32 v82, v78, v84
Maaa 208,144,20
v_fmac_f32 v81, v82, v82
v_sub_f32 v83, v79, v84
v_fmac_f32 v81, v83, v83
v_cndmask_b32_e64 v81, 0, v81, s[86:87]
A 80,80,81
Zaaa 212,148,20
Maaa 212,52,16
v_add_f32_dpp v80, v80, v80 quad_perm:[1,0,3,2] row_mask:0xf bank_mask:0xf
s_nop 1
v_add_f32_dpp v80, v80, v80 quad_perm:[2,3,0,1] row_mask:0xf bank_mask:0xf
s_nop 1
v_add_f32_dpp v80, v80, v80 row_half_mirror row_mask:0xf bank_mask:0xf
s_nop 1
v_add_f32_dpp v80, v80, v80 row_mirror row_mask:0xf bank_mask:0xf
v_mov_b32 v81, v80
s_nop 1
v_permlane16_swap_b32 v80, v81
Zaaa 80,56,16
A 80,80,81
v_mul_f32 v80, 0x3acccccd, v80
K 80,80,80
v_lshlrev_b32 v80, 16, v80
v_add_f32 v80, 0x3727c5ac, v80
K 80,80,80
Maaa 80,152,20
v_lshlrev_b32 v80, 16, v80
v_sqrt_f32 v81, v80
s_nop 0
v_rcp_f32 v82, v81
v_fma_f32 v83, -v81, v81, v80
Zaaa 84,156,20
v_mul_f32 v82, 0.5, v82
v_fma_f32 v81, v83, v82, v81
K 86,81,81
v_lshlrev_b32 v86, 16, v86
v_rcp_f32 v87, v86
Maaa 84,60,16
W 4
K 85,84,84
v_lshlrev_b32 v85, 16, v85
s_waitcnt vmcnt(17)
v_sub_f32 v56, v56, v85
v_sub_f32 v57, v57, v85
K 80,56,57
Zaaa 92,60,24
v_lshlrev_b32 v56, 16, v80
v_and_b32 v57, 0xffff0000, v80
U 80,56,87
U 81,57,87
K 32,80,81
v_lshlrev_b32 v80, 16, v32
Maaa 92,156,28
v_and_b32 v81, 0xffff0000, v32
v_lshlrev_b32 v82, 16, v44
v_and_b32 v83, 0xffff0000, v44
U 84,80,82
v_fmac_f32 v84, v81, v83
Zaaa 88,152,28
Ta 8,15,16384
v_sub_f32 v58, v58, v85
v_sub_f32 v59, v59, v85
K 80,58,59
v_lshlrev_b32 v58, 16, v80
v_and_b32 v59, 0xffff0000, v80
Maaa 88,56,24
Ta 10,15,17408
U 80,58,87
U 81,59,87
K 33,80,81
v_lshlrev_b32 v80, 16, v33
v_and_b32 v81, 0xffff0000, v33
Zaaa 220,52,24
Ta 12,15,24576
Ta 14,15,25600
v_lshlrev_b32 v82, 16, v45
)"
R"(v_and_b32 v83, 0xffff0000, v45
v_fmac_f32 v84, v80, v82
v_fmac_f32 v84, v81, v83
Maaa 220,148,28
v_sub_f32 v60, v60, v85
v_sub_f32 v61, v61, v85
K 80,60,61
v_lshlrev_b32 v60, 16, v80
v_and_b32 v61, 0xffff0000, v80
U 80,60,87
Zaaa 216,144,28
U 81,61,87
K 34,80,81
v_lshlrev_b32 v80, 16, v34
v_and_b32 v81, 0xffff0000, v34
v_lshlrev_b32 v82, 16, v46
v_and_b32 v83, 0xffff0000, v46
Maaa 216,48,24
v_fmac_f32 v84, v80, v82
v_fmac_f32 v84, v81, v83
v_sub_f32 v62, v62, v85
v_sub_f32 v63, v63, v85
K 80,62,63
Zvaa 252,44,24
v_lshlrev_b32 v62, 16, v80
v_and_b32 v63, 0xffff0000, v80
U 80,62,87
U 81,63,87
K 35,80,81
Mvaa 252,140,28
v_lshlrev_b32 v80, 16, v35
v_and_b32 v81, 0xffff0000, v35
v_lshlrev_b32 v82, 16, v47
v_and_b32 v83, 0xffff0000, v47
v_fmac_f32 v84, v80, v82
Zvaa 248,136,28
v_fmac_f32 v84, v81, v83
v_sub_f32 v64, v64, v85
v_sub_f32 v65, v65, v85
K 80,64,65
v_lshlrev_b32 v64, 16, v80
Mvaa 248,40,24
v_and_b32 v65, 0xffff0000, v80
U 80,64,87
U 81,65,87
K 36,80,81
v_lshlrev_b32 v80, 16, v36
Zvaa 188,36,24
v_and_b32 v81, 0xffff0000, v36
v_lshlrev_b32 v82, 16, v48
v_and_b32 v83, 0xffff0000, v48
v_fmac_f32 v84, v80, v82
v_fmac_f32 v84, v81, v83
Mvaa 188,132,28
v_sub_f32 v66, v66, v85
v_sub_f32 v67, v67, v85
K 80,66,67
v_lshlrev_b32 v66, 16, v80
v_and_b32 v67, 0xffff0000, v80
U 80,66,87
Zvaa 184,128,28
U 81,67,87
K 37,80,81
v_lshlrev_b32 v80, 16, v37
v_and_b32 v81, 0xffff0000, v37
v_lshlrev_b32 v82, 16, v49
Mvaa 184,32,24
W 4
v_and_b32 v83, 0xffff0000, v49
v_fmac_f32 v84, v80, v82
v_fmac_f32 v84, v81, v83
v_sub_f32 v68, v68, v85
v_sub_f32 v69, v69, v85
Zvaa 192,32,0
K 80,68,69
v_lshlrev_b32 v68, 16, v80
v_and_b32 v69, 0xffff0000, v80
U 80,68,87
U 81,69,87
Mvaa 192,128,4
K 38,80,81
v_lshlrev_b32 v80, 16, v38
v_and_b32 v81, 0xffff0000, v38
v_lshlrev_b32 v82, 16, v50
v_and_b32 v83, 0xffff0000, v50
Zvaa 196,132,4
Ta 16,24,16384
v_fmac_f32 v84, v80, v82
v_fmac_f32 v84, v81, v83
v_sub_f32 v70, v70, v85
v_sub_f32 v71, v71, v85
K 80,70,71
Mvaa 196,36,0
Ta 18,24,17408
v_lshlrev_b32 v70, 16, v80
v_and_b32 v71, 0xffff0000, v80
U 80,70,87
U 81,71,87
K 39,80,81
Zaaa 160,40,0
Ta 20,24,24576
Ta 22,24,25600
v_lshlrev_b32 v80, 16, v39
v_and_b32 v81, 0xffff0000, v39
v_lshlrev_b32 v82, 16, v51
v_and_b32 v83, 0xffff0000, v51
Maaa 160,136,4
v_fmac_f32 v84, v80, v82
v_fmac_f32 v84, v81, v83
v_sub_f32 v72, v72, v85
v_sub_f32 v73, v73, v85
K 80,72,73
v_lshlrev_b32 v72, 16, v80
Zaaa 164,140,4
v_and_b32 v73, 0xffff0000, v80
U 80,72,87
U 81,73,87
K 40,80,81
v_lshlrev_b32 v80, 16, v40
v_and_b32 v81, 0xffff0000, v40
Maaa 164,44,0
v_lshlrev_b32 v82, 16, v52
v_and_b32 v83, 0xffff0000, v52
v_fmac_f32 v84, v80, v82
v_fmac_f32 v84, v81, v83
v_sub_f32 v74, v74, v85
Zaaa 224,48,0
v_sub_f32 v75, v75, v85
K 80,74,75
v_lshlrev_b32 v74, 16, v80
v_and_b32 v75, 0xffff0000, v80
U 80,74,87
Maaa 224,144,4
U 81,75,87
K 41,80,81
v_lshlrev_b32 v80, 16, v41
v_and_b32 v81, 0xffff0000, v41
v_lshlrev_b32 v82, 16, v53
Zaaa 228,148,4
v_and_b32 v83, 0xffff0000, v53
v_fmac_f32 v84, v80, v82
v_fmac_f32 v84, v81, v83
v_sub_f32 v76, v76, v85
v_sub_f32 v77, v77, v85
Maaa 228,52,0
K 80,76,77
v_lshlrev_b32 v76, 16, v80
v_and_b32 v77, 0xffff0000, v80
U 80,76,87
U 81,77,87
Zaaa 96,56,0
K 42,80,81
v_lshlrev_b32 v80, 16, v42
v_and_b32 v81, 0xffff0000, v42
v_lshlrev_b32 v82, 16, v54
v_and_b32 v83, 0xffff0000, v54
Maaa 96,152,4
v_fmac_f32 v84, v80, v82
v_fmac_f32 v84, v81, v83
v_sub_f32 v78, v78, v85
v_sub_f32 v79, v79, v85
K 80,78,79
v_lshlrev_b32 v78, 16, v80
Zaaa 100,156,4
v_and_b32 v79, 0xffff0000, v80
U 80,78,87
U 81,79,87
K 43,80,81
v_lshlrev_b32 v80, 16, v43
Maaa 100,60,0
W 4
v_and_b32 v81, 0xffff0000, v43
v_lshlrev_b32 v82, 16, v55
v_and_b32 v83, 0xffff0000, v55
v_fmac_f32 v84, v80, v82
v_fmac_f32 v84, v81, v83
Zaaa 108,60,8
Ta 24,25,16384
Ta 26,25,17408
v_lshrrev_b32 v82, 9, v1
s_lshl_b32 s80, s14, 1
v_add_u32 v82, s80, v82
v_mul_u32_u24 v83, 1552, v82
v_add_u32 v83, v83, v88
Maaa 108,156,12
v_add_u32 v83, 131072, v83
ds_write_b128 v83, v[32:35] offset:0
ds_write_b128 v83, v[36:39] offset:512
ds_write_b128 v83, v[40:43] offset:1024
v_add_f32_dpp v84, v84, v84 quad_perm:[1,0,3,2] row_mask:0xf bank_mask:0xf
Zaaa 104,152,12
Ta 28,25,24576
Ta 30,25,25600
Maaa 104,56,8
v_add_f32_dpp v84, v84, v84 quad_perm:[2,3,0,1] row_mask:0xf bank_mask:0xf
s_nop 1
v_add_f32_dpp v84, v84, v84 row_half_mirror row_mask:0xf bank_mask:0xf
s_nop 1
v_add_f32_dpp v84, v84, v84 row_mirror row_mask:0xf bank_mask:0xf
v_mov_b32 v81, v84
s_nop 1
v_permlane16_swap_b32 v84, v81
A 84,84,81
Zaaa 236,52,8
K 80,84,84
v_lshlrev_b32 v80, 16, v80
s_lshl_b32 s80, s15, 3
v_add_u32 v82, s80, v82
v_sub_u32 v83, 2047, v82
v_ashrrev_i32 v81, 31, v80
v_or_b32 v81, 0x80000000, v81
Maaa 236,148,12
v_xor_b32 v80, v80, v81
v_and_b32 v80, 0xffff0000, v80
v_or_b32 v80, v80, v83
v_and_b32 v82, 7, v82
v_lshlrev_b32 v82, 2, v82
v_add_u32 v82, 143488, v82
Zaaa 232,144,12
ds_write_b32 v82, v80
Maaa 232,48,8
Zaaa 172,44,8
Maaa 172,140,12
Zaaa 168,136,12
s_waitcnt vmcnt(1) lgkmcnt(0)
s_barrier
Maaa 168,40,8
s_add_u32 s64, s64, 128
v_bfe_u32 v80, v1, 4, 3
Zvaa 204,36,8
v_mul_u32_u24 v81, 1552, v80
Mvaa 204,132,12
s_and_b32 s64, s64, 1023
v_lshrrev_b32 v82, 7, v1
Zvaa 200,128,12
v_lshl_add_u32 v81, v82, 4, v81
Mvaa 200,32,8
s_lshl_b32 s80, s14, 7
Zvaa 208,32,16
s_lshl_b32 s65, s64, 12
s_add_u32 s80, s80, 131072
Mvaa 208,128,20
v_add_u32 v81, s80, v81
Zvaa 212,132,20
s_add_u32 m0, s62, 0
ds_read_b128 v[32:35], v81
Ta 0,10,49152
Mvaa 212,36,16
Ta 2,10,50176
Rv 36,81,512
Zaaa 176,40,16
s_and_b32 s80, s14, 2
Ta 4,10,57344
Ta 6,10,58368
Maaa 176,136,20
s_lshl_b32 s80, s80, 7
D 16,44,65,0
Zaaa 180,140,20
v_subrev_u32 v81, s80, v81
Maaa 180,44,16
Rv 40,81,1024
Zaaa 240,48,16
v_lshlrev_b32 v80, 2, v80
Maaa 240,144,20
D 17,44,65,1024
v_add_u32 v80, 143488, v80
Zaaa 244,148,20
ds_read_b32 v84, v80
Maaa 244,52,16
v_mov_b32 v85, s13
Zaaa 112,56,16
v_lshrrev_b32 v83, 1, v1
Maaa 112,152,20
s_mul_i32 s82, s15, 10240
D 18,44,65,2048
Zaaa 116,156,20
s_add_u32 s82, s82, 131072
Maaa 116,60,16
s_lshl_b32 s80, s14, 10
Zaaa 124,60,24
s_add_u32 s83, s82, s80
Ra 60,26,47104
Maaa 124,156,28
D 19,44,65,3072
Ra 156,27,47104
Zaaa 120,152,28
s_add_u32 m0, s62, 4096
Ra 152,27,45056
Ta 8,11,49152
Maaa 120,56,24
Ta 10,11,50176
Ra 56,26,45056
Zaaa 252,52,24
Ta 12,11,57344
Ta 14,11,58368
Maaa 252,148,28
W 8
buffer_store_dwordx4 v[32:35], v1, s[8:11], s83 offen sc1
s_add_u32 s83, s83, 4096
Zaaa 248,144,28
Ra 52,26,43008
Ra 148,27,43008
Maaa 248,48,24
Ra 144,27,40960
Ra 48,26,40960
Zaaa 188,44,24
Ra 44,26,38912
Maaa 188,140,28
D 20,44,65,0
Ra 140,27,38912
Zaaa 184,136,28
Ra 136,27,36864
Maaa 184,40,24
Ra 40,26,36864
Zvaa 220,36,24
Ra 36,26,34816
Mvaa 220,132,28
buffer_store_dwordx4 v[36:39], v1, s[8:11], s83 offen sc1
Ra 132,27,34816
Zvaa 216,128,28
s_and_b32 s80, s80, 1024
s_add_u32 s83, s82, s80
s_add_u32 s83, s83, 8192
Mvaa 216,32,24
W 12
Ra 128,27,32768
Ra 32,26,32768
Maaa 68,60,0
Maaa 68,156,4
D 21,44,65,1024
Maaa 64,152,4
Ta 16,12,49152
Ta 18,12,50176
Maaa 64,56,0
W 10
Ta 20,12,57344
Ta 22,12,58368
Maaa 196,52,0
Maaa 196,148,4
D 22,44,65,2048
Maaa 192,144,4
Maaa 192,48,0
W 8
Mvaa 228,44,0
Mvaa 228,140,4
buffer_store_dwordx4 v[40:43], v1, s[8:11], s83 offen sc1
Mvaa 224,136,4
Mvaa 224,40,0
W 4
Mvaa 164,36,0
Mvaa 164,132,4
D 23,44,65,3072
Mvaa 160,128,4
s_add_u32 m0, s61, 0
Mvaa 160,32,0
Mvaa 168,32,8
Mvaa 168,128,12
D 2,40,64,0
Mvaa 172,132,12
Ta 24,13,49152
Ta 26,13,50176
Mvaa 172,36,8
Ta 28,13,57344
Ta 30,13,58368
Mvaa 232,40,8
.Lh_0_1_1%=:
s_cbranch_execz .Lhr_5%=
Mvaa 232,136,12
D 3,40,64,1024
Mvaa 236,140,12
Mvaa 236,44,8
Maaa 200,48,8
Maaa 200,144,12
D 4,40,64,2048
Maaa 204,148,12
Maaa 204,52,8
Maaa 72,56,8
Maaa 72,152,12
D 5,40,64,3072
Maaa 76,156,12
s_add_u32 m0, s61, 4096
Maaa 76,60,8
W 4
Maaa 84,60,16
Maaa 84,156,20
D 6,40,64,0
Maaa 80,152,20
Ta 0,14,49152
Ta 2,14,50176
Maaa 80,56,16
Ta 4,14,57344
Ta 6,14,58368
Maaa 212,52,16
Maaa 212,148,20
D 7,40,64,1024
Maaa 208,144,20
Maaa 208,48,16
Mvaa 244,44,16
Mvaa 244,140,20
D 8,40,64,2048
)"
R"(Mvaa 240,136,20
Mvaa 240,40,16
Mvaa 180,36,16
Mvaa 180,132,20
D 9,40,64,3072
Mvaa 176,128,20
Mvaa 176,32,16
W 4
Mvaa 184,32,24
Mvaa 184,128,28
global_load_dword v142, v[140:141], off
v_lshl_add_u64 v[140:141], v[140:141], 0, s[68:69]
v_bfe_u32 v147, v1, 5, 3
Mvaa 188,132,28
v_lshlrev_b32 v147, 13, v147
v_and_b32 v148, 16, v1
Mvaa 188,36,24
Ta 8,15,49152
v_lshl_add_u32 v147, v148, 3, v147
Mvaa 248,40,24
s_lshl_b32 s92, s57, 2
s_add_u32 s92, s92, s14
s_lshl_b32 s92, s92, 16
Mvaa 248,136,28
buffer_load_dword v142, v147, s[48:51], s92 offen
Ta 10,15,50176
Mvaa 252,140,28
Ta 12,15,57344
Ta 14,15,58368
Mvaa 252,44,24
Maaa 216,48,24
Maaa 216,144,28
Maaa 220,148,28
Maaa 220,52,24
Maaa 88,56,24
Maaa 88,152,28
Maaa 92,156,28
Maaa 92,60,24
W 4
Maaa 100,60,0
Maaa 100,156,4
Maaa 96,152,4
Ta 16,24,49152
Ta 18,24,50176
Maaa 96,56,0
Ta 20,24,57344
Ta 22,24,58368
Maaa 228,52,0
Maaa 228,148,4
Maaa 224,144,4
Maaa 224,48,0
Maaa 164,44,0
Maaa 164,140,4
Maaa 160,136,4
Maaa 160,40,0
Mvaa 196,36,0
Mvaa 196,132,4
Mvaa 192,128,4
Mvaa 192,32,0
W 4
Mvaa 200,32,8
Ta 24,25,49152
Ta 26,25,50176
Mvaa 200,128,12
Mvaa 204,132,12
Ta 28,25,57344
Ta 30,25,58368
Mvaa 204,36,8
Maaa 168,40,8
Maaa 168,136,12
Maaa 172,140,12
Maaa 172,44,8
Maaa 232,48,8
Maaa 232,144,12
Maaa 236,148,12
s_waitcnt vmcnt(2) lgkmcnt(0)
s_barrier
s_cmp_eq_u32 s14, 0
s_cbranch_scc0 .Lpubr_15%=
s_lshl_b32 s80, s15, 6
s_add_u32 s80, s80, 40960
s_mov_b64 exec, 0xff
buffer_store_dwordx2 v[84:85], v83, s[8:11], s80 offen sc1
s_mov_b64 exec, -1
.Lpubr_15%=:
Maaa 236,52,8
s_add_u32 s64, s64, 128
Maaa 104,56,8
Maaa 104,152,12
s_and_b32 s64, s64, 1023
Maaa 108,156,12
Maaa 108,60,8
Maaa 116,60,16
s_lshl_b32 s65, s64, 12
Maaa 116,156,20
Maaa 112,152,20
Ta 0,10,16384
Ta 2,10,17408
Maaa 112,56,16
s_add_u32 m0, s62, 32768
Ta 4,10,24576
Ta 6,10,25600
Maaa 244,52,16
Maaa 244,148,20
Maaa 240,144,20
D 16,44,65,0
Maaa 240,48,16
Maaa 180,44,16
Maaa 180,140,20
Maaa 176,136,20
D 17,44,65,1024
Maaa 176,40,16
Mvaa 212,36,16
Mvaa 212,132,20
Mvaa 208,128,20
D 18,44,65,2048
Mvaa 208,32,16
Mvaa 216,32,24
Ra 32,26,0
Mvaa 216,128,28
Ra 128,27,0
Mvaa 220,132,28
D 19,44,65,3072
Ra 132,27,2048
Mvaa 220,36,24
s_add_u32 m0, s62, 36864
Ta 8,11,16384
Ta 10,11,17408
Maaa 184,40,24
Ra 36,26,2048
Ta 12,11,24576
Maaa 184,136,28
Ta 14,11,25600
Ra 40,26,4096
Maaa 188,140,28
D 20,44,65,0
Ra 136,27,4096
Maaa 188,44,24
Ra 140,27,6144
Ra 44,26,6144
Maaa 248,48,24
Ra 48,26,8192
Maaa 248,144,28
Ra 144,27,8192
Maaa 252,148,28
D 21,44,65,1024
Ra 148,27,10240
Maaa 252,52,24
Ra 52,26,10240
Maaa 120,56,24
Ra 56,26,12288
Maaa 120,152,28
Ra 152,27,12288
Maaa 124,156,28
D 22,44,65,2048
Ra 156,27,14336
Maaa 124,60,24
W 13
Ra 60,26,14336
Mvaa 160,32,0
Mvaa 160,128,4
Mvaa 164,132,4
D 23,44,65,3072
Ta 16,12,16384
Mvaa 164,36,0
W 9
s_add_u32 m0, s61, 32768
Ta 18,12,17408
Mvaa 224,40,0
Ta 20,12,24576
Ta 22,12,25600
Mvaa 224,136,4
Mvaa 228,140,4
D 2,40,64,0
Mvaa 228,44,0
W 8
Maaa 192,48,0
Maaa 192,144,4
Maaa 196,148,4
D 3,40,64,1024
Maaa 196,52,0
W 4
Maaa 64,56,0
Maaa 64,152,4
Maaa 68,156,4
D 4,40,64,2048
Maaa 68,60,0
Maaa 76,60,8
Maaa 76,156,12
Maaa 72,152,12
D 5,40,64,3072
Ta 24,13,16384
Maaa 72,56,8
s_add_u32 m0, s61, 36864
Ta 26,13,17408
Ta 28,13,24576
Maaa 204,52,8
Ta 30,13,25600
Maaa 204,148,12
Maaa 200,144,12
D 6,40,64,0
Maaa 200,48,8
Mvaa 236,44,8
Mvaa 236,140,12
Mvaa 232,136,12
D 7,40,64,1024
Mvaa 232,40,8
Mvaa 172,36,8
Mvaa 172,132,12
Mvaa 168,128,12
D 8,40,64,2048
Mvaa 168,32,8
W 4
Mvaa 176,32,16
Mvaa 176,128,20
Mvaa 180,132,20
D 9,40,64,3072
Ta 0,14,16384
Mvaa 180,36,16
Ta 2,14,17408
Ta 4,14,24576
Mvaa 240,40,16
Ta 6,14,25600
Mvaa 240,136,20
Mvaa 244,140,20
Mvaa 244,44,16
Maaa 208,48,16
Maaa 208,144,20
Maaa 212,148,20
Maaa 212,52,16
Maaa 80,56,16
Maaa 80,152,20
Maaa 84,156,20
Maaa 84,60,16
W 4
Maaa 92,60,24
Maaa 92,156,28
Maaa 88,152,28
Ta 8,15,16384
Ta 10,15,17408
Maaa 88,56,24
Ta 12,15,24576
Ta 14,15,25600
Maaa 220,52,24
Maaa 220,148,28
Maaa 216,144,28
Maaa 216,48,24
Mvaa 252,44,24
Mvaa 252,140,28
Mvaa 248,136,28
Mvaa 248,40,24
Mvaa 188,36,24
Mvaa 188,132,28
Mvaa 184,128,28
Mvaa 184,32,24
W 4
Mvaa 192,32,0
Mvaa 192,128,4
Mvaa 196,132,4
Ta 16,24,16384
Ta 18,24,17408
Mvaa 196,36,0
Ta 20,24,24576
Ta 22,24,25600
Maaa 160,40,0
Maaa 160,136,4
Maaa 164,140,4
Maaa 164,44,0
Maaa 224,48,0
Maaa 224,144,4
Maaa 228,148,4
Maaa 228,52,0
Maaa 96,56,0
Maaa 96,152,4
Maaa 100,156,4
Maaa 100,60,0
W 4
Maaa 108,60,8
Ta 24,25,16384
Ta 26,25,17408
Maaa 108,156,12
Maaa 104,152,12
Ta 28,25,24576
Ta 30,25,25600
Maaa 104,56,8
Maaa 236,52,8
Maaa 236,148,12
Maaa 232,144,12
Maaa 232,48,8
Maaa 172,44,8
Maaa 172,140,12
Maaa 168,136,12
s_waitcnt vmcnt(0) lgkmcnt(0)
s_barrier
Maaa 168,40,8
s_add_u32 s64, s64, 128
Mvaa 204,36,8
Mvaa 204,132,12
s_and_b32 s64, s64, 1023
Mvaa 200,128,12
Mvaa 200,32,8
Mvaa 208,32,16
s_lshl_b32 s65, s64, 12
Mvaa 208,128,20
Mvaa 212,132,20
s_add_u32 m0, s62, 0
Ta 0,10,49152
Ta 2,10,50176
Mvaa 212,36,16
Ta 4,10,57344
Ta 6,10,58368
Maaa 176,40,16
Maaa 176,136,20
D 16,44,65,0
Maaa 180,140,20
Maaa 180,44,16
Maaa 240,48,16
Maaa 240,144,20
D 17,44,65,1024
Maaa 244,148,20
Maaa 244,52,16
Maaa 112,56,16
Maaa 112,152,20
D 18,44,65,2048
Maaa 116,156,20
Maaa 116,60,16
Maaa 124,60,24
Ra 60,26,47104
Maaa 124,156,28
D 19,44,65,3072
Ra 156,27,47104
Maaa 120,152,28
s_add_u32 m0, s62, 4096
Ra 152,27,45056
Ta 8,11,49152
Maaa 120,56,24
Ta 10,11,50176
Ra 56,26,45056
Maaa 252,52,24
Ta 12,11,57344
Ta 14,11,58368
Maaa 252,148,28
D 20,44,65,0
Ra 52,26,43008
Maaa 248,144,28
Ra 148,27,43008
Ra 144,27,40960
Maaa 248,48,24
Ra 48,26,40960
Maaa 188,44,24
Ra 44,26,38912
Maaa 188,140,28
D 21,44,65,1024
Ra 140,27,38912
Maaa 184,136,28
Ra 136,27,36864
Maaa 184,40,24
Ra 40,26,36864
Mvaa 220,36,24
Ra 36,26,34816
Mvaa 220,132,28
D 22,44,65,2048
Ra 132,27,34816
Mvaa 216,128,28
Ra 128,27,32768
Mvaa 216,32,24
W 13
Ra 32,26,32768
Maaa 68,60,0
Maaa 68,156,4
D 23,44,65,3072
Maaa 64,152,4
s_add_u32 m0, s61, 0
Ta 16,12,49152
Ta 18,12,50176
Maaa 64,56,0
W 10
Ta 20,12,57344
Ta 22,12,58368
Maaa 196,52,0
Maaa 196,148,4
D 2,40,64,0
Maaa 192,144,4
Maaa 192,48,0
W 8
Mvaa 228,44,0
Mvaa 228,140,4
D 3,40,64,1024
Mvaa 224,136,4
Mvaa 224,40,0
W 4
Mvaa 164,36,0
Mvaa 164,132,4
D 4,40,64,2048
Mvaa 160,128,4
Mvaa 160,32,0
Mvaa 168,32,8
Mvaa 168,128,12
D 5,40,64,3072
Mvaa 172,132,12
)"
R"(s_add_u32 m0, s61, 4096
Ta 24,13,49152
Ta 26,13,50176
Mvaa 172,36,8
Ta 28,13,57344
Ta 30,13,58368
Mvaa 232,40,8
Mvaa 232,136,12
D 6,40,64,0
Mvaa 236,140,12
Mvaa 236,44,8
Maaa 200,48,8
Maaa 200,144,12
D 7,40,64,1024
Maaa 204,148,12
Maaa 204,52,8
Maaa 72,56,8
Maaa 72,152,12
D 8,40,64,2048
Maaa 76,156,12
Maaa 76,60,8
W 4
Maaa 84,60,16
Maaa 84,156,20
D 9,40,64,3072
Maaa 80,152,20
Ta 0,14,49152
Ta 2,14,50176
Maaa 80,56,16
Ta 4,14,57344
Ta 6,14,58368
Maaa 212,52,16
Maaa 212,148,20
global_load_dword v142, v[140:141], off
v_lshl_add_u64 v[140:141], v[140:141], 0, s[68:69]
Maaa 208,144,20
Maaa 208,48,16
Mvaa 244,44,16
Mvaa 244,140,20
Mvaa 240,136,20
Mvaa 240,40,16
Mvaa 180,36,16
Mvaa 180,132,20
Mvaa 176,128,20
Mvaa 176,32,16
W 4
Mvaa 184,32,24
Mvaa 184,128,28
Mvaa 188,132,28
Ta 8,15,49152
Ta 10,15,50176
Mvaa 188,36,24
Ta 12,15,57344
Ta 14,15,58368
Mvaa 248,40,24
Mvaa 248,136,28
Mvaa 252,140,28
Mvaa 252,44,24
Maaa 216,48,24
Maaa 216,144,28
Maaa 220,148,28
Maaa 220,52,24
Maaa 88,56,24
Maaa 88,152,28
Maaa 92,156,28
Maaa 92,60,24
W 4
Maaa 100,60,0
Maaa 100,156,4
Maaa 96,152,4
Ta 16,24,49152
Ta 18,24,50176
Maaa 96,56,0
Ta 20,24,57344
Ta 22,24,58368
Maaa 228,52,0
Maaa 228,148,4
Maaa 224,144,4
Maaa 224,48,0
Maaa 164,44,0
Maaa 164,140,4
Maaa 160,136,4
Maaa 160,40,0
Mvaa 196,36,0
Mvaa 196,132,4
Mvaa 192,128,4
Mvaa 192,32,0
W 4
Mvaa 200,32,8
Ta 24,25,49152
Ta 26,25,50176
Mvaa 200,128,12
Mvaa 204,132,12
Ta 28,25,57344
Ta 30,25,58368
Mvaa 204,36,8
Maaa 168,40,8
Maaa 168,136,12
.Lh_0_2_1%=:
s_cbranch_execz .Lhr_5%=
Maaa 172,140,12
Maaa 172,44,8
Maaa 232,48,8
Maaa 232,144,12
Maaa 236,148,12
s_waitcnt vmcnt(1) lgkmcnt(0)
s_barrier
Maaa 236,52,8
s_add_u32 s64, s64, 128
Maaa 104,56,8
Maaa 104,152,12
s_and_b32 s64, s64, 1023
Maaa 108,156,12
Maaa 108,60,8
Maaa 116,60,16
s_lshl_b32 s65, s64, 12
Maaa 116,156,20
Maaa 112,152,20
Ta 0,10,16384
Ta 2,10,17408
Maaa 112,56,16
s_add_u32 m0, s62, 32768
Ta 4,10,24576
Ta 6,10,25600
Maaa 244,52,16
Maaa 244,148,20
Maaa 240,144,20
D 16,44,65,0
Maaa 240,48,16
Maaa 180,44,16
Maaa 180,140,20
Maaa 176,136,20
D 17,44,65,1024
Maaa 176,40,16
Mvaa 212,36,16
Mvaa 212,132,20
Mvaa 208,128,20
D 18,44,65,2048
Mvaa 208,32,16
Mvaa 216,32,24
Ra 32,26,0
Mvaa 216,128,28
Ra 128,27,0
Mvaa 220,132,28
D 19,44,65,3072
Ra 132,27,2048
Mvaa 220,36,24
s_add_u32 m0, s62, 36864
Ta 8,11,16384
Ta 10,11,17408
Maaa 184,40,24
Ra 36,26,2048
Ta 12,11,24576
Maaa 184,136,28
Ta 14,11,25600
Ra 40,26,4096
Maaa 188,140,28
D 20,44,65,0
Ra 136,27,4096
Maaa 188,44,24
Ra 140,27,6144
Ra 44,26,6144
Maaa 248,48,24
Ra 48,26,8192
Maaa 248,144,28
Ra 144,27,8192
Maaa 252,148,28
D 21,44,65,1024
Ra 148,27,10240
Maaa 252,52,24
Ra 52,26,10240
Maaa 120,56,24
Ra 56,26,12288
Maaa 120,152,28
Ra 152,27,12288
Maaa 124,156,28
D 22,44,65,2048
Ra 156,27,14336
Maaa 124,60,24
W 13
Ra 60,26,14336
Mvaa 160,32,0
Mvaa 160,128,4
Mvaa 164,132,4
D 23,44,65,3072
Ta 16,12,16384
Mvaa 164,36,0
W 9
s_add_u32 m0, s61, 32768
Ta 18,12,17408
Mvaa 224,40,0
Ta 20,12,24576
Ta 22,12,25600
Mvaa 224,136,4
Mvaa 228,140,4
D 2,40,64,0
Mvaa 228,44,0
W 8
Maaa 192,48,0
Maaa 192,144,4
Maaa 196,148,4
D 3,40,64,1024
Maaa 196,52,0
W 4
Maaa 64,56,0
Maaa 64,152,4
Maaa 68,156,4
D 4,40,64,2048
Maaa 68,60,0
Maaa 76,60,8
Maaa 76,156,12
Maaa 72,152,12
D 5,40,64,3072
Ta 24,13,16384
Maaa 72,56,8
s_add_u32 m0, s61, 36864
Ta 26,13,17408
Ta 28,13,24576
Maaa 204,52,8
Ta 30,13,25600
Maaa 204,148,12
Maaa 200,144,12
D 6,40,64,0
Maaa 200,48,8
Mvaa 236,44,8
Mvaa 236,140,12
Mvaa 232,136,12
D 7,40,64,1024
Mvaa 232,40,8
Mvaa 172,36,8
Mvaa 172,132,12
Mvaa 168,128,12
D 8,40,64,2048
Mvaa 168,32,8
W 4
Mvaa 176,32,16
Mvaa 176,128,20
Mvaa 180,132,20
D 9,40,64,3072
Ta 0,14,16384
Mvaa 180,36,16
s_add_u32 s92, s92, 2097152
Ta 2,14,17408
Ta 4,14,24576
Mvaa 240,40,16
Ta 6,14,25600
Mvaa 240,136,20
Mvaa 244,140,20
buffer_load_dword v142, v147, s[48:51], s92 offen
Mvaa 244,44,16
s_add_u32 s92, s92, 2097152
Maaa 208,48,16
Maaa 208,144,20
Maaa 212,148,20
buffer_load_dword v142, v147, s[48:51], s92 offen
Maaa 212,52,16
Maaa 80,56,16
Maaa 80,152,20
Maaa 84,156,20
Maaa 84,60,16
W 4
Maaa 92,60,24
Maaa 92,156,28
Maaa 88,152,28
Ta 8,15,16384
Ta 10,15,17408
Maaa 88,56,24
Ta 12,15,24576
Ta 14,15,25600
Maaa 220,52,24
Maaa 220,148,28
Maaa 216,144,28
Maaa 216,48,24
Mvaa 252,44,24
Mvaa 252,140,28
Mvaa 248,136,28
Mvaa 248,40,24
Mvaa 188,36,24
Mvaa 188,132,28
Mvaa 184,128,28
Mvaa 184,32,24
W 4
Mvaa 192,32,0
Mvaa 192,128,4
Mvaa 196,132,4
Ta 16,24,16384
Ta 18,24,17408
Mvaa 196,36,0
Ta 20,24,24576
Ta 22,24,25600
Maaa 160,40,0
Maaa 160,136,4
Maaa 164,140,4
Maaa 164,44,0
Maaa 224,48,0
Maaa 224,144,4
Maaa 228,148,4
Maaa 228,52,0
Maaa 96,56,0
Maaa 96,152,4
Maaa 100,156,4
Maaa 100,60,0
W 4
Maaa 108,60,8
Ta 24,25,16384
Ta 26,25,17408
Maaa 108,156,12
Maaa 104,152,12
Ta 28,25,24576
Ta 30,25,25600
Maaa 104,56,8
Maaa 236,52,8
Maaa 236,148,12
Maaa 232,144,12
Maaa 232,48,8
Maaa 172,44,8
Maaa 172,140,12
Maaa 168,136,12
s_waitcnt vmcnt(2) lgkmcnt(0)
s_barrier
Maaa 168,40,8
s_add_u32 s64, s64, 128
Mvaa 204,36,8
Mvaa 204,132,12
s_and_b32 s64, s64, 1023
Mvaa 200,128,12
Mvaa 200,32,8
Mvaa 208,32,16
s_lshl_b32 s65, s64, 12
Mvaa 208,128,20
Mvaa 212,132,20
Ta 0,10,49152
Ta 2,10,50176
Mvaa 212,36,16
s_add_u32 m0, s62, 0
Ta 4,10,57344
Ta 6,10,58368
Maaa 176,40,16
Maaa 176,136,20
Maaa 180,140,20
D 16,44,65,0
Maaa 180,44,16
Maaa 240,48,16
Maaa 240,144,20
Maaa 244,148,20
D 17,44,65,1024
Maaa 244,52,16
Maaa 112,56,16
Maaa 112,152,20
Maaa 116,156,20
D 18,44,65,2048
Maaa 116,60,16
Maaa 124,60,24
Ra 60,26,47104
Maaa 124,156,28
Ra 156,27,47104
Maaa 120,152,28
D 19,44,65,3072
Ra 152,27,45056
Maaa 120,56,24
s_add_u32 m0, s62, 4096
Ta 8,11,49152
Ta 10,11,50176
Maaa 252,52,24
Ra 56,26,45056
Ta 12,11,57344
Maaa 252,148,28
Ta 14,11,58368
Ra 52,26,43008
Maaa 248,144,28
D 20,44,65,0
v_lshrrev_b32 v28, 1, v1
Maaa 248,48,24
s_lshl_b32 s80, s60, 9
Ra 148,27,43008
Ra 144,27,40960
Maaa 188,44,24
s_add_u32 s80, s80, 40960
Ra 48,26,40960
Ra 44,26,38912
Maaa 188,140,28
Ra 140,27,38912
Maaa 184,136,28
D 21,44,65,1024
Ra 136,27,36864
Maaa 184,40,24
Ra 40,26,36864
Mvaa 220,36,24
Ra 36,26,34816
Mvaa 220,132,28
Ra 132,27,34816
Mvaa 216,128,28
buffer_load_dwordx2 v[28:29], v28, s[8:11], s80 offen sc1
Ra 128,27,32768
Mvaa 216,32,24
W 13
Ra 32,26,32768
Maaa 68,60,0
Maaa 68,156,4
Maaa 64,152,4
)"
R"(D 22,44,65,2048
Ta 16,12,49152
Maaa 64,56,0
W 9
Ta 18,12,50176
Ta 20,12,57344
Maaa 196,52,0
Ta 22,12,58368
Maaa 196,148,4
Maaa 192,144,4
D 23,44,65,3072
Maaa 192,48,0
W 8
s_add_u32 m0, s61, 0
Mvaa 228,44,0
Mvaa 228,140,4
Mvaa 224,136,4
D 2,40,64,0
Mvaa 224,40,0
W 4
Mvaa 164,36,0
Mvaa 164,132,4
Mvaa 160,128,4
D 3,40,64,1024
Mvaa 160,32,0
Mvaa 168,32,8
Mvaa 168,128,12
Mvaa 172,132,12
D 4,40,64,2048
Ta 24,13,49152
Mvaa 172,36,8
Ta 26,13,50176
Ta 28,13,57344
Mvaa 232,40,8
Ta 30,13,58368
Mvaa 232,136,12
Mvaa 236,140,12
D 5,40,64,3072
Mvaa 236,44,8
s_add_u32 m0, s61, 4096
Maaa 200,48,8
Maaa 200,144,12
Maaa 204,148,12
D 6,40,64,0
Maaa 204,52,8
Maaa 72,56,8
Maaa 72,152,12
Maaa 76,156,12
D 7,40,64,1024
Maaa 76,60,8
W 4
Maaa 84,60,16
Maaa 84,156,20
Maaa 80,152,20
D 8,40,64,2048
Ta 0,14,49152
Maaa 80,56,16
Ta 2,14,50176
Ta 4,14,57344
Maaa 212,52,16
Ta 6,14,58368
Maaa 212,148,20
Maaa 208,144,20
D 9,40,64,3072
Maaa 208,48,16
Mvaa 244,44,16
Mvaa 244,140,20
Mvaa 240,136,20
s_waitcnt vmcnt(10)
v_cmp_eq_u32 vcc, s13, v29
s_cmp_eq_u64 vcc, -1
s_cbranch_scc0 .Lspin_16%=
.Lok_17%=:
global_load_dword v142, v146, s[6:7]
Mvaa 240,40,16
Mvaa 180,36,16
v_bfe_u32 v144, v1, 8, 1
Mvaa 180,132,20
v_mul_u32_u24 v144, 10240, v144
Mvaa 176,128,20
v_and_b32 v145, 0xf0, v1
Mvaa 176,32,16
W 4
v_lshl_add_u32 v144, v145, 3, v144
Mvaa 184,32,24
s_mul_i32 s91, s58, 20480
Mvaa 184,128,28
s_add_u32 s91, s91, s28
Mvaa 188,132,28
buffer_load_dword v142, v144, s[8:11], s91 offen offset:0 sc1
Ta 8,15,49152
Mvaa 188,36,24
Ta 10,15,50176
Ta 12,15,57344
Mvaa 248,40,24
Ta 14,15,58368
Mvaa 248,136,28
Mvaa 252,140,28
Mvaa 252,44,24
Maaa 216,48,24
Maaa 216,144,28
Maaa 220,148,28
Maaa 220,52,24
Maaa 88,56,24
Maaa 88,152,28
Maaa 92,156,28
Maaa 92,60,24
W 4
Maaa 100,60,0
Maaa 100,156,4
Maaa 96,152,4
Ta 16,24,49152
Ta 18,24,50176
Maaa 96,56,0
Ta 20,24,57344
Ta 22,24,58368
Maaa 228,52,0
Maaa 228,148,4
Maaa 224,144,4
Maaa 224,48,0
Maaa 164,44,0
Maaa 164,140,4
Maaa 160,136,4
Maaa 160,40,0
Mvaa 196,36,0
Mvaa 196,132,4
Mvaa 192,128,4
Mvaa 192,32,0
W 4
Mvaa 200,32,8
Ta 24,25,49152
Ta 26,25,50176
Mvaa 200,128,12
Mvaa 204,132,12
Ta 28,25,57344
Ta 30,25,58368
Mvaa 204,36,8
Maaa 168,40,8
Maaa 168,136,12
Maaa 172,140,12
Maaa 172,44,8
Maaa 232,48,8
Maaa 232,144,12
Maaa 236,148,12
s_waitcnt vmcnt(2) lgkmcnt(0)
s_barrier
s_cmp_eq_u32 s85, 107
s_cbranch_scc1 .Lrt_19%=
.Lrtr_20%=:
Maaa 236,52,8
s_add_u32 s64, s64, 128
Maaa 104,56,8
Maaa 104,152,12
s_and_b32 s64, s64, 1023
Maaa 108,156,12
Maaa 108,60,8
Maaa 116,60,16
s_lshl_b32 s65, s64, 12
Maaa 116,156,20
Maaa 112,152,20
Ta 0,10,16384
Ta 2,10,17408
Maaa 112,56,16
s_add_u32 m0, s62, 32768
Ta 4,10,24576
Ta 6,10,25600
Maaa 244,52,16
Maaa 244,148,20
Maaa 240,144,20
D 16,44,65,0
Maaa 240,48,16
Maaa 180,44,16
Maaa 180,140,20
Maaa 176,136,20
D 17,44,65,1024
Maaa 176,40,16
Mvaa 212,36,16
Mvaa 212,132,20
Mvaa 208,128,20
D 18,44,65,2048
Mvaa 208,32,16
Mvaa 216,32,24
Ra 32,26,0
Mvaa 216,128,28
Ra 128,27,0
Mvaa 220,132,28
D 19,44,65,3072
Ra 132,27,2048
Mvaa 220,36,24
s_add_u32 m0, s62, 36864
Ta 8,11,16384
Ta 10,11,17408
Maaa 184,40,24
Ra 36,26,2048
Ta 12,11,24576
Maaa 184,136,28
Ta 14,11,25600
Ra 40,26,4096
Maaa 188,140,28
D 20,44,65,0
Ra 136,27,4096
Maaa 188,44,24
Ra 140,27,6144
Ra 44,26,6144
Maaa 248,48,24
Ra 48,26,8192
Maaa 248,144,28
Ra 144,27,8192
Maaa 252,148,28
D 21,44,65,1024
Ra 148,27,10240
Maaa 252,52,24
Ra 52,26,10240
Maaa 120,56,24
Ra 56,26,12288
Maaa 120,152,28
Ra 152,27,12288
Maaa 124,156,28
D 22,44,65,2048
Ra 156,27,14336
Maaa 124,60,24
W 13
Ra 60,26,14336
Mvaa 160,32,0
Mvaa 160,128,4
Mvaa 164,132,4
D 23,44,65,3072
Ta 16,12,16384
Mvaa 164,36,0
W 9
s_add_u32 m0, s61, 32768
Ta 18,12,17408
Mvaa 224,40,0
.Lh_0_3_1%=:
s_cbranch_execz .Lhr_5%=
Ta 20,12,24576
Ta 22,12,25600
Mvaa 224,136,4
Mvaa 228,140,4
D 2,40,64,0
Mvaa 228,44,0
W 8
Maaa 192,48,0
Maaa 192,144,4
Maaa 196,148,4
D 3,40,64,1024
Maaa 196,52,0
W 4
Maaa 64,56,0
Maaa 64,152,4
Maaa 68,156,4
D 4,40,64,2048
Maaa 68,60,0
Maaa 76,60,8
Maaa 76,156,12
Maaa 72,152,12
D 5,40,64,3072
Ta 24,13,16384
Maaa 72,56,8
s_add_u32 m0, s61, 36864
Ta 26,13,17408
Ta 28,13,24576
Maaa 204,52,8
Ta 30,13,25600
Maaa 204,148,12
Maaa 200,144,12
D 6,40,64,0
Maaa 200,48,8
Mvaa 236,44,8
Mvaa 236,140,12
Mvaa 232,136,12
D 7,40,64,1024
Mvaa 232,40,8
Mvaa 172,36,8
Mvaa 172,132,12
Mvaa 168,128,12
D 8,40,64,2048
Mvaa 168,32,8
W 4
Mvaa 176,32,16
Mvaa 176,128,20
Mvaa 180,132,20
D 9,40,64,3072
v_add_u32 v2, 131072, v10
Mvaa 180,36,16
Ta 0,14,16384
v_add_u32 v3, 131072, v11
Mvaa 240,40,16
Ta 2,14,17408
v_add_u32 v4, 131072, v12
Mvaa 240,136,20
Ta 4,14,24576
Ta 6,14,25600
Mvaa 244,140,20
buffer_load_dword v142, v144, s[8:11], s91 offen offset:2048 sc1
v_add_u32 v5, 131072, v13
Mvaa 244,44,16
s_add_u32 s91, s91, 4096
v_add_u32 v6, 131072, v14
Maaa 208,48,16
v_add_u32 v7, 131072, v15
Maaa 208,144,20
Maaa 212,148,20
v_add_u32 v8, 131072, v24
Maaa 212,52,16
Maaa 80,56,16
v_add_u32 v9, 131072, v25
Maaa 80,152,20
s_add_u32 s65, s5, 0
s_cmp_ge_u32 s65, 20
s_cselect_b32 s90, 20, 0
s_sub_u32 s65, s65, s90
Maaa 84,156,20
s_lshl_b32 s65, s65, 18
s_add_u32 m0, s63, 131072
Maaa 84,60,16
W 4
Maaa 92,60,24
Maaa 92,156,28
Maaa 88,152,28
Ta 8,15,16384
Ta 10,15,17408
Maaa 88,56,24
D 16,48,65,0
Ta 12,15,24576
Maaa 220,52,24
Ta 14,15,25600
Maaa 220,148,28
Maaa 216,144,28
Maaa 216,48,24
D 17,48,65,1024
Mvaa 252,44,24
Mvaa 252,140,28
Mvaa 248,136,28
Mvaa 248,40,24
Mvaa 188,36,24
D 18,48,65,2048
Mvaa 188,132,28
Mvaa 184,128,28
Mvaa 184,32,24
W 4
Mvaa 192,32,0
Mvaa 192,128,4
D 19,48,65,3072
Mvaa 196,132,4
s_add_u32 m0, s63, 135168
Ta 16,24,16384
Ta 18,24,17408
Mvaa 196,36,0
Ta 20,24,24576
Ta 22,24,25600
Maaa 160,40,0
Maaa 160,136,4
Maaa 164,140,4
D 20,48,65,0
Maaa 164,44,0
Maaa 224,48,0
Maaa 224,144,4
Maaa 228,148,4
Maaa 228,52,0
D 21,48,65,1024
Maaa 96,56,0
Maaa 96,152,4
Maaa 100,156,4
Maaa 100,60,0
W 4
Maaa 108,60,8
D 22,48,65,2048
Ta 24,25,16384
Maaa 108,156,12
Ta 26,25,17408
Maaa 104,152,12
Ta 28,25,24576
Ta 30,25,25600
Maaa 104,56,8
Maaa 236,52,8
D 23,48,65,3072
Maaa 236,148,12
Maaa 232,144,12
Maaa 232,48,8
Maaa 172,44,8
Maaa 172,140,12
Maaa 168,136,12
s_waitcnt vmcnt(9) lgkmcnt(0)
s_barrier
s_cmp_eq_u32 s85, 107
s_cbranch_scc1 .Lrt_21%=
.Lrtr_22%=:
Maaa 168,40,8
s_add_u32 s65, s5, 4
Mvaa 204,36,8
Mvaa 204,132,12
s_cmp_ge_u32 s65, 20
s_cselect_b32 s90, 20, 0
Mvaa 200,128,12
Mvaa 200,32,8
s_sub_u32 s65, s65, s90
Mvaa 208,32,16
Mvaa 208,128,20
s_lshl_b32 s65, s65, 18
)"
R"(Mvaa 212,132,20
Ta 0,10,49152
Ta 2,10,50176
Mvaa 212,36,16
Maaa 176,40,16
s_add_u32 m0, s63, 0
Maaa 176,136,20
Maaa 180,140,20
D 16,48,65,0
Maaa 180,44,16
Maaa 240,48,16
Maaa 240,144,20
Maaa 244,148,20
D 17,48,65,1024
Ta 4,11,49152
Maaa 244,52,16
Ta 6,11,50176
Maaa 112,56,16
Maaa 112,152,20
Maaa 116,156,20
D 18,48,65,2048
Maaa 116,60,16
Maaa 124,60,24
Ra 60,26,47104
Maaa 124,156,28
Maaa 120,152,28
D 19,48,65,3072
Ta 8,12,49152
Maaa 120,56,24
s_add_u32 m0, s63, 4096
Ta 10,12,50176
Ra 56,26,45056
Maaa 252,52,24
Ra 52,26,43008
Maaa 252,148,28
Maaa 248,144,28
D 20,48,65,0
Maaa 248,48,24
Ra 48,26,40960
Maaa 188,44,24
Ra 44,26,38912
Maaa 188,140,28
Maaa 184,136,28
D 21,48,65,1024
Ta 12,13,49152
Maaa 184,40,24
Ta 14,13,50176
Ra 40,26,36864
Mvaa 220,36,24
Ra 36,26,34816
Mvaa 220,132,28
Mvaa 216,128,28
D 22,48,65,2048
Mvaa 216,32,24
Ra 32,26,32768
W 6
Maaa 68,60,0
Maaa 64,56,0
Maaa 196,52,0
D 23,48,65,3072
Ta 16,14,49152
Maaa 192,48,0
W 1
s_add_u32 s65, s5, 8
s_cmp_ge_u32 s65, 20
s_cselect_b32 s90, 20, 0
Mvaa 228,44,0
s_sub_u32 s65, s65, s90
s_lshl_b32 s65, s65, 18
Ta 18,14,50176
Mvaa 224,40,0
s_add_u32 m0, s63, 65536
Mvaa 164,36,0
D 16,48,65,0
Mvaa 160,32,0
Mvaa 168,32,4
Mvaa 172,36,4
Mvaa 232,40,4
D 17,48,65,1024
Ta 20,15,49152
Mvaa 236,44,4
Ta 22,15,50176
Maaa 200,48,4
Maaa 204,52,4
Maaa 72,56,4
D 18,48,65,2048
Maaa 76,60,4
Maaa 84,60,8
Maaa 80,56,8
Maaa 212,52,8
D 19,48,65,3072
Ta 24,24,49152
Maaa 208,48,8
s_add_u32 m0, s63, 69632
Ta 26,24,50176
Mvaa 244,44,8
Mvaa 240,40,8
Mvaa 180,36,8
D 20,48,65,0
Mvaa 176,32,8
Mvaa 184,32,12
Mvaa 188,36,12
Mvaa 248,40,12
D 21,48,65,1024
Ta 28,25,49152
Mvaa 252,44,12
Ta 30,25,50176
Maaa 216,48,12
Ra 156,27,47104
Maaa 220,52,12
Maaa 88,56,12
D 22,48,65,2048
Maaa 92,60,12
W 7
Ra 152,27,45056
Maaa 100,60,16
Maaa 96,56,16
Maaa 228,52,16
D 23,48,65,3072
Ra 148,27,43008
Maaa 224,48,16
Ta 0,10,57344
Ta 2,10,58368
Maaa 164,44,16
Maaa 160,40,16
Ra 144,27,40960
Mvaa 196,36,16
buffer_load_dword v142, v144, s[8:11], s91 offen offset:0 sc1
Mvaa 192,32,16
W 10
Mvaa 200,32,20
Ra 140,27,38912
Mvaa 204,36,20
Maaa 168,40,20
Ta 4,11,57344
Ta 6,11,58368
Maaa 172,44,20
Ra 136,27,36864
Maaa 232,48,20
Maaa 236,52,20
v_bfe_u32 v29, v1, 7, 1
Maaa 104,56,20
Ra 132,27,34816
Maaa 108,60,20
W 13
v_mul_u32_u24 v28, 10240, v29
Maaa 116,60,24
Maaa 112,56,24
Ra 128,27,32768
v_lshrrev_b32 v29, 8, v1
Maaa 244,52,24
Ta 8,12,57344
Ta 10,12,58368
Maaa 240,48,24
v_lshl_add_u32 v28, v29, 7, v28
Maaa 180,44,24
v_and_b32 v29, 0x70, v1
Maaa 176,40,24
Mvaa 212,36,24
v_add_u32 v28, v28, v29
Mvaa 208,32,24
W 14
Mvaa 216,32,28
s_and_b32 s80, s14, 2
Mvaa 220,36,28
s_mul_i32 s80, s80, 20480
Maaa 184,40,28
Ta 12,13,57344
Ta 14,13,58368
Maaa 188,44,28
s_sub_u32 s81, 40960, s80
Maaa 248,48,28
Maaa 252,52,28
s_add_u32 s93, s28, 20480
Maaa 120,56,28
Maaa 124,60,28
W 10
v_add_u32 v26, s80, v28
Maaa 68,156,0
Maaa 64,152,0
Maaa 196,148,0
Ta 16,14,57344
v_add_u32 v27, s81, v28
Maaa 192,144,0
W 5
Ta 18,14,58368
Mvaa 228,140,0
buffer_load_dwordx4 a[32:35], v26, s[8:11], s28 offen offset:0 sc1
Mvaa 224,136,0
Mvaa 164,132,0
Mvaa 160,128,0
Mvaa 168,128,4
buffer_load_dwordx4 a[36:39], v26, s[8:11], s93 offen offset:0 sc1
Mvaa 172,132,4
Mvaa 232,136,4
Ta 20,15,57344
Ta 22,15,58368
Mvaa 236,140,4
Maaa 200,144,4
buffer_load_dwordx4 a[40:43], v27, s[8:11], s28 offen offset:0 sc1
Maaa 204,148,4
Maaa 72,152,4
Maaa 76,156,4
W 6
Maaa 84,156,8
buffer_load_dwordx4 a[44:47], v27, s[8:11], s93 offen offset:0 sc1
Ta 24,24,57344
Maaa 80,152,8
s_add_u32 s28, s28, 512
s_cmp_eq_u32 s28, s4
s_cselect_b32 s93, 10240, 0
Ta 26,24,58368
Maaa 212,148,8
s_sub_u32 s28, s28, s93
Ta 28,25,57344
Ta 30,25,58368
Maaa 208,144,8
s_add_u32 s93, s28, 20480
Mvaa 244,140,8
Mvaa 240,136,8
Mvaa 180,132,8
s_add_u32 s65, s5, 12
s_cmp_ge_u32 s65, 20
s_cselect_b32 s90, 20, 0
buffer_load_dwordx4 a[48:51], v26, s[8:11], s28 offen offset:0 sc1
Mvaa 176,128,8
W 8
s_sub_u32 s65, s65, s90
s_lshl_b32 s65, s65, 18
Mvaa 184,128,12
s_add_u32 m0, s63, 32768
Mvaa 188,132,12
Mvaa 248,136,12
s_waitcnt vmcnt(6) lgkmcnt(0)
s_barrier
Mvaa 252,140,12
buffer_load_dwordx4 a[52:55], v26, s[8:11], s93 offen offset:0 sc1
Maaa 216,144,12
Maaa 220,148,12
Maaa 88,152,12
Maaa 92,156,12
buffer_load_dwordx4 a[56:59], v27, s[8:11], s28 offen offset:0 sc1
Maaa 100,156,16
Maaa 96,152,16
Maaa 228,148,16
Maaa 224,144,16
buffer_load_dwordx4 a[60:63], v27, s[8:11], s93 offen offset:0 sc1
Maaa 164,140,16
Maaa 160,136,16
Mvaa 196,132,16
Mvaa 192,128,16
D 16,48,65,0
Mvaa 200,128,20
Mvaa 204,132,20
Maaa 168,136,20
Ta 0,2,0
Ta 2,2,1024
Maaa 172,140,20
D 17,48,65,1024
Maaa 232,144,20
Maaa 236,148,20
Maaa 104,152,20
Ta 4,2,8192
Ta 6,2,9216
Maaa 108,156,20
D 18,48,65,2048
Maaa 116,156,24
Maaa 112,152,24
Maaa 244,148,24
Ta 8,3,0
Ta 10,3,1024
Maaa 240,144,24
D 19,48,65,3072
Maaa 180,140,24
s_add_u32 m0, s63, 36864
Maaa 176,136,24
Mvaa 212,132,24
Ta 12,3,8192
Ta 14,3,9216
Mvaa 208,128,24
D 20,48,65,0
Mvaa 216,128,28
Mvaa 220,132,28
Maaa 184,136,28
Ta 16,4,0
Ta 18,4,1024
Maaa 188,140,28
Maaa 248,144,28
D 21,48,65,1024
Maaa 252,148,28
Maaa 120,152,28
Maaa 124,156,28
.Lq_7%=:
s_cmp_ge_u32 s14, 2
s_cbranch_scc1 .Lpb_27%=
s_waitcnt vmcnt(6) lgkmcnt(6)
Zvaa 32,0,32
s_add_u32 s28, s28, 512
Ta 20,4,8192
Ta 22,4,9216
Mvaa 32,4,48
s_cmp_eq_u32 s28, s4
s_cselect_b32 s93, 10240, 0
Ta 24,5,0
Ta 26,5,1024
Zvaa 64,4,52
s_sub_u32 s28, s28, s93
s_add_u32 s93, s28, 20480
buffer_load_dwordx4 a[128:131], v26, s[8:11], s28 offen offset:0 sc1
Mvaa 64,0,36
Ta 28,5,8192
Ta 30,5,9216
Zvaa 96,0,40
K 184,184,185
K 185,186,187
Mvaa 96,4,56
K 186,188,189
K 187,190,191
Zvaa 128,4,60
K 192,192,193
Mvaa 128,0,44
W 8
buffer_load_dwordx4 a[132:135], v26, s[8:11], s93 offen offset:0 sc1
K 193,194,195
Zvaa 132,8,44
Ta 0,6,0
Ta 2,6,1024
Mvaa 132,12,60
K 194,196,197
K 195,198,199
Zvaa 100,12,56
K 200,200,201
K 201,202,203
Mvaa 100,8,40
Ta 4,6,8192
Ta 6,6,9216
Zvaa 68,8,36
buffer_load_dwordx4 a[136:139], v27, s[8:11], s28 offen offset:0 sc1
K 202,204,205
Mvaa 68,12,52
K 203,206,207
K 208,208,209
Zvaa 36,12,48
K 209,210,211
K 210,212,213
Mvaa 36,8,32
W 8
Ta 8,7,0
K 211,214,215
Zvaa 40,16,32
Ta 10,7,1024
K 216,216,217
Mvaa 40,20,48
buffer_load_dwordx4 a[140:143], v27, s[8:11], s93 offen offset:0 sc1
K 217,218,219
Zvaa 72,20,52
s_add_u32 s28, s28, 512
s_cmp_eq_u32 s28, s4
s_cselect_b32 s93, 10240, 0
s_sub_u32 s28, s28, s93
Mvaa 72,16,36
s_add_u32 s93, s28, 20480
K 218,220,221
K 219,222,223
Zvaa 104,16,40
Ta 12,7,8192
Ta 14,7,9216
Mvaa 104,20,56
K 224,224,225
K 225,226,227
Zvaa 136,20,60
K 226,228,229
K 227,230,231
Mvaa 136,16,44
W 8
buffer_load_dwordx4 a[144:147], v26, s[8:11], s28 offen offset:0 sc1
K 232,232,233
Zvaa 140,24,44
K 233,234,235
K 234,236,237
Mvaa 140,28,60
Ta 16,8,0
K 235,238,239
Zvaa 108,28,56
Ta 18,8,1024
K 240,240,241
Mvaa 108,24,40
K 241,242,243
K 242,244,245
Zvaa 76,24,36
buffer_load_dwordx4 a[148:151], v26, s[8:11], s93 offen offset:0 sc1
K 243,246,247
Mvaa 76,28,52
Ta 20,8,8192
Ta 22,8,9216
Zvaa 44,28,48
K 248,248,249
K 249,250,251
Mvaa 44,24,32
K 250,252,253
)"
R"(K 251,254,255
W 8
Zvaa 48,0,32
Ta 24,9,0
Ta 26,9,1024
Mvaa 48,4,48
buffer_load_dwordx4 a[152:155], v27, s[8:11], s28 offen offset:0 sc1
K 160,160,161
Zvaa 80,4,52
Ta 28,9,8192
Ta 30,9,9216
Mvaa 80,0,36
K 161,162,163
Zvaa 112,0,40
K 162,164,165
Mvaa 112,4,56
Zvaa 144,4,60
buffer_load_dwordx4 a[156:159], v27, s[8:11], s93 offen offset:0 sc1
K 163,166,167
Mvaa 144,0,44
W 8
s_add_u32 s28, s28, 512
s_cmp_eq_u32 s28, s4
s_cselect_b32 s93, 10240, 0
Zvaa 148,8,44
s_sub_u32 s28, s28, s93
s_add_u32 s93, s28, 20480
Ta 0,2,16384
Mvaa 148,12,60
s_add_u32 s36, s29, 0
Ta 2,2,17408
Zvaa 116,12,56
Ta 4,2,24576
Ta 6,2,25600
Mvaa 116,8,40
buffer_load_dwordx4 v[188:191], v26, s[8:11], s28 offen offset:0 sc1
Zvaa 84,8,36
Mvaa 84,12,52
Zvaa 52,12,48
Mvaa 52,8,32
W 8
Ta 8,3,16384
Ta 10,3,17408
Zvaa 56,16,32
buffer_load_dwordx4 v[196:199], v26, s[8:11], s93 offen offset:0 sc1
Mvaa 56,20,48
Zvaa 88,20,52
Ta 12,3,24576
Ta 14,3,25600
Mvaa 88,16,36
Zvaa 120,16,40
buffer_store_dwordx4 v[160:163], v1, s[8:11], s36 offen offset:0 sc1
K 168,168,169
Mvaa 120,20,56
K 169,170,171
K 170,172,173
Zvaa 152,20,60
Mvaa 152,16,44
W 8
Ta 16,4,16384
K 171,174,175
Zvaa 156,24,44
Ta 18,4,17408
Mvaa 156,28,60
buffer_load_dwordx4 v[204:207], v27, s[8:11], s28 offen offset:0 sc1
Zvaa 124,28,56
Ta 20,4,24576
Ta 22,4,25600
Mvaa 124,24,40
Zvaa 92,24,36
Mvaa 92,28,52
Zvaa 60,28,48
buffer_load_dwordx4 v[212:215], v27, s[8:11], s93 offen offset:0 sc1
Mvaa 60,24,32
s_waitcnt vmcnt(5) lgkmcnt(8)
s_add_u32 s28, s28, 512
s_cmp_eq_u32 s28, s4
s_cselect_b32 s93, 10240, 0
Mvaa 32,0,128
s_sub_u32 s28, s28, s93
s_add_u32 s93, s28, 20480
Ta 24,5,16384
Mvaa 32,4,144
Ta 26,5,17408
Mvaa 64,4,148
Ta 28,5,24576
Ta 30,5,25600
Mvaa 64,0,132
buffer_store_dwordx4 v[168:171], v1, s[8:11], s36 offen offset:1024 sc1
K 176,176,177
Mvaa 96,0,136
K 177,178,179
K 178,180,181
Mvaa 96,4,152
Mvaa 128,4,156
Mvaa 128,0,140
W 8
Ta 0,6,16384
K 179,182,183
Mvaa 132,8,140
buffer_load_dwordx4 v[220:223], v26, s[8:11], s28 offen offset:0 sc1
Ta 2,6,17408
Mvaa 132,12,156
Mvaa 100,12,152
Ta 4,6,24576
Ta 6,6,25600
Mvaa 100,8,136
Mvaa 68,8,132
Mvaa 68,12,148
buffer_load_dwordx4 v[228:231], v26, s[8:11], s93 offen offset:0 sc1
Mvaa 36,12,144
Mvaa 36,8,128
W 8
Ta 8,7,16384
Ta 10,7,17408
Mvaa 40,16,128
Mvaa 40,20,144
Mvaa 72,20,148
buffer_load_dwordx4 v[236:239], v27, s[8:11], s28 offen offset:0 sc1
Ta 12,7,24576
Mvaa 72,16,132
buffer_store_dwordx4 v[176:179], v1, s[8:11], s36 offen offset:2048 sc1
Ta 14,7,25600
Mvaa 104,16,136
Mvaa 104,20,152
Mvaa 136,20,156
Mvaa 136,16,140
W 8
Ta 16,8,16384
Ta 18,8,17408
Mvaa 140,24,140
buffer_load_dwordx4 v[244:247], v27, s[8:11], s93 offen offset:0 sc1
Mvaa 140,28,156
s_add_u32 s28, s28, 512
s_cmp_eq_u32 s28, s4
s_cselect_b32 s93, 10240, 0
s_sub_u32 s28, s28, s93
Mvaa 108,28,152
s_add_u32 s93, s28, 20480
Ta 20,8,24576
Ta 22,8,25600
Mvaa 108,24,136
Mvaa 76,24,132
Mvaa 76,28,148
buffer_load_dwordx4 v[252:255], v26, s[8:11], s28 offen offset:0 sc1
Mvaa 44,28,144
Mvaa 44,24,128
W 8
Ta 24,9,16384
Ta 26,9,17408
Mvaa 48,0,128
Mvaa 48,4,144
Mvaa 80,4,148
D 22,48,65,2048
Ta 28,9,24576
Mvaa 80,0,132
buffer_store_dwordx4 v[184:187], v1, s[8:11], s36 offen offset:3072 sc1
Ta 30,9,25600
Mvaa 112,0,136
s_add_u32 s36, s29, 4096
s_lshl_b32 s80, s56, 9
Mvaa 112,4,152
Mvaa 144,4,156
s_lshl_b32 s81, s57, 4
Mvaa 144,0,140
W 8
s_add_u32 s80, s80, s81
Ta 0,10,0
Mvaa 148,8,140
buffer_load_dwordx4 a[32:35], v26, s[8:11], s93 offen offset:0 sc1
Ta 2,10,1024
Mvaa 148,12,156
s_and_b32 s81, s14, 1
Mvaa 116,12,152
s_lshl_b32 s81, s81, 3
Ta 4,10,8192
Ta 6,10,9216
Mvaa 116,8,136
Mvaa 84,8,132
s_add_u32 s80, s80, s81
Mvaa 84,12,148
s_add_u32 s71, s80, 4096
buffer_load_dwordx4 a[36:39], v27, s[8:11], s28 offen offset:0 sc1
Mvaa 52,12,144
Mvaa 52,8,128
W 8
Ta 8,11,0
Ta 10,11,1024
Mvaa 56,16,128
Mvaa 56,20,144
Mvaa 88,20,148
buffer_store_dwordx4 v[192:195], v1, s[8:11], s36 offen offset:0 sc1
Ta 12,11,8192
Mvaa 88,16,132
Ta 14,11,9216
Mvaa 120,16,136
Mvaa 120,20,152
Mvaa 152,20,156
Mvaa 152,16,140
W 8
buffer_load_dwordx4 a[40:43], v27, s[8:11], s93 offen offset:0 sc1
Ta 16,12,0
Mvaa 156,24,140
s_add_u32 s28, s28, 512
s_cmp_eq_u32 s28, s4
s_cselect_b32 s93, 10240, 0
s_sub_u32 s28, s28, s93
Mvaa 156,28,156
s_add_u32 s93, s28, 20480
Ta 18,12,1024
Mvaa 124,28,152
Ta 20,12,8192
Ta 22,12,9216
Mvaa 124,24,136
Mvaa 92,24,132
buffer_load_dwordx4 a[44:47], v26, s[8:11], s28 offen offset:0 sc1
Mvaa 92,28,148
Mvaa 60,28,144
Mvaa 60,24,128
s_waitcnt vmcnt(8) lgkmcnt(8)
Ta 24,13,0
Ta 26,13,1024
Mvav 32,0,188
Mvav 32,4,220
D 23,48,65,3072
Mvav 64,4,228
s_add_u32 s65, s5, 16
s_cmp_ge_u32 s65, 20
s_cselect_b32 s90, 20, 0
buffer_store_dwordx4 v[200:203], v1, s[8:11], s36 offen offset:1024 sc1
Mvav 64,0,196
s_sub_u32 s65, s65, s90
s_lshl_b32 s65, s65, 18
s_add_u32 m0, s63, 98304
Mvav 96,0,204
Ta 28,13,8192
Ta 30,13,9216
Mvav 96,4,236
Mvav 128,4,244
Mvav 128,0,212
W 8
buffer_load_dwordx4 a[48:51], v26, s[8:11], s93 offen offset:0 sc1
Ta 0,14,0
Mvav 132,8,212
Ta 2,14,1024
v_add_u32 v2, 65536, v10
Mvav 132,12,244
Mvav 100,12,236
Ta 4,14,8192
Ta 6,14,9216
Mvav 100,8,204
v_add_u32 v3, 65536, v11
Mvav 68,8,196
buffer_load_dwordx4 a[52:55], v27, s[8:11], s28 offen offset:0 sc1
Mvav 68,12,228
Mvav 36,12,220
Mvav 36,8,188
W 8
Ta 8,15,0
Ta 10,15,1024
Mvav 40,16,188
v_add_u32 v4, 65536, v12
Mvav 40,20,220
buffer_load_dwordx4 a[56:59], v27, s[8:11], s93 offen offset:0 sc1
Mvav 72,20,228
s_add_u32 s28, s28, 512
s_cmp_eq_u32 s28, s4
s_cselect_b32 s93, 10240, 0
buffer_store_dwordx4 v[208:211], v1, s[8:11], s36 offen offset:2048 sc1
Mvav 72,16,196
s_sub_u32 s28, s28, s93
s_add_u32 s93, s28, 20480
Ta 12,15,8192
Mvav 104,16,204
Ta 14,15,9216
v_add_u32 v5, 65536, v13
Mvav 104,20,236
Mvav 136,20,244
Mvav 136,16,212
W 8
D 16,48,65,0
Ta 16,24,0
Mvav 140,24,212
Ta 18,24,1024
v_add_u32 v6, 65536, v14
Mvav 140,28,244
Mvav 108,28,236
Ta 20,24,8192
Ta 22,24,9216
Mvav 108,24,204
v_add_u32 v7, 65536, v15
Mvav 76,24,196
buffer_load_dwordx4 a[60:63], v26, s[8:11], s28 offen offset:0 sc1
Mvav 76,28,228
Mvav 44,28,220
Mvav 44,24,188
W 8
Ta 24,25,0
Ta 26,25,1024
Mvav 48,0,188
v_add_u32 v8, 65536, v24
Mvav 48,4,220
buffer_load_dwordx4 v[184:187], v26, s[8:11], s93 offen offset:0 sc1
Mvav 80,4,228
buffer_store_dwordx4 v[216:219], v1, s[8:11], s36 offen offset:3072 sc1
Ta 28,25,8192
Mvav 80,0,196
s_add_u32 s36, s29, 458752
Ta 30,25,9216
v_add_u32 v9, 65536, v25
Mvav 112,0,204
Mvav 112,4,236
Mvav 144,4,244
Mvav 144,0,212
W 8
buffer_load_dwordx4 v[192:195], v27, s[8:11], s28 offen offset:0 sc1
Ta 0,10,16384
Mvav 148,8,212
Ta 2,10,17408
Mvav 148,12,244
Mvav 116,12,236
Ta 4,10,24576
Ta 6,10,25600
Mvav 116,8,204
Mvav 84,8,196
D 17,48,65,1024
Mvav 84,12,228
Mvav 52,12,220
Mvav 52,8,188
W 8
Ta 8,11,16384
Ta 10,11,17408
Mvav 56,16,188
Mvav 56,20,220
buffer_store_dwordx4 v[224:227], v1, s[8:11], s36 offen offset:0 sc1
Mvav 88,20,228
Ta 12,11,24576
Ta 14,11,25600
Mvav 88,16,196
Mvav 120,16,204
Mvav 120,20,236
Mvav 152,20,244
buffer_load_dwordx4 a[128:131], v27, s[8:11], s93 offen offset:0 sc1
Mvav 152,16,212
W 8
s_add_u32 s28, s28, 512
s_cmp_eq_u32 s28, s4
s_cselect_b32 s93, 10240, 0
Mvav 156,24,212
s_sub_u32 s28, s28, s93
s_add_u32 s93, s28, 20480
Ta 16,12,16384
Mvav 156,28,244
Ta 18,12,17408
Mvav 124,28,236
Ta 20,12,24576
Ta 22,12,25600
Mvav 124,24,204
buffer_load_dwordx4 a[132:135], v26, s[8:11], s28 offen offset:0 sc1
Mvav 92,24,196
Mvav 92,28,228
Mvav 60,28,220
Mvav 60,24,188
s_waitcnt vmcnt(10) lgkmcnt(8)
Ta 24,13,16384
Ta 26,13,17408
Mvav 32,0,252
buffer_load_dwordx4 a[136:139], v26, s[8:11], s93 offen offset:0 sc1
Mvaa 32,4,44
buffer_store_dwordx4 v[232:235], v1, s[8:11], s36 offen offset:1024 sc1
Mvaa 64,4,48
Ta 28,13,24576
Ta 30,13,25600
Mvaa 64,0,32
Mvaa 96,0,36
Mvaa 96,4,52
Mvaa 128,4,56
buffer_load_dwordx4 a[140:143], v27, s[8:11], s28 offen offset:0 sc1
Mvaa 128,0,40
W 8
Ta 0,14,16384
Ta 2,14,17408
Mvaa 132,8,40
Mvaa 132,12,56
Mvaa 100,12,52
Ta 4,14,24576
Ta 6,14,25600
Mvaa 100,8,36
D 18,48,65,2048
Mvaa 68,8,32
Mvaa 68,12,48
Mvaa 36,12,44
Mvav 36,8,252
W 8
Ta 8,15,16384
Ta 10,15,17408
Mvav 40,16,252
buffer_load_dwordx4 a[144:147], v27, s[8:11], s93 offen offset:0 sc1
Mvaa 40,20,44
s_add_u32 s28, s28, 512
)"
R"(s_cmp_eq_u32 s28, s4
s_cselect_b32 s93, 10240, 0
buffer_store_dwordx4 v[240:243], v1, s[8:11], s36 offen offset:2048 sc1
Mvaa 72,20,48
s_sub_u32 s28, s28, s93
s_add_u32 s93, s28, 20480
Ta 12,15,24576
Mvaa 72,16,32
Ta 14,15,25600
Mvaa 104,16,36
Mvaa 104,20,52
Mvaa 136,20,56
buffer_load_dwordx4 a[148:151], v26, s[8:11], s28 offen offset:0 sc1
Mvaa 136,16,40
W 8
Ta 16,24,16384
Ta 18,24,17408
Mvaa 140,24,40
Mvaa 140,28,56
Mvaa 108,28,52
Ta 20,24,24576
Ta 22,24,25600
Mvaa 108,24,36
buffer_load_dwordx4 a[152:155], v26, s[8:11], s93 offen offset:0 sc1
Mvaa 76,24,32
Mvaa 76,28,48
Mvaa 44,28,44
Mvav 44,24,252
W 8
Ta 24,25,16384
Ta 26,25,17408
Mvav 48,0,252
buffer_load_dwordx4 a[156:159], v27, s[8:11], s28 offen offset:0 sc1
Mvaa 48,4,44
buffer_store_dwordx4 v[248:251], v1, s[8:11], s36 offen offset:3072 sc1
G 160,160
Mvaa 80,4,48
G 161,161
G 162,162
Mvaa 80,0,32
G 163,163
G 164,164
Mvaa 112,0,36
G 165,165
G 166,166
Mvaa 112,4,52
Ta 28,25,24576
Ta 30,25,25600
Mvaa 144,4,56
D 19,48,65,3072
G 167,167
Mvaa 144,0,40
W 8
s_add_u32 m0, s63, 102400
K 160,160,161
Mvaa 148,8,40
K 161,162,163
K 162,164,165
Mvaa 148,12,56
Ta 0,2,0
K 163,166,167
Mvaa 116,12,52
s_add_u32 s36, s29, 462848
Ta 2,2,1024
Ta 4,2,8192
Mvaa 116,8,36
buffer_load_dwordx4 v[200:203], v27, s[8:11], s93 offen offset:0 sc1
Ta 6,2,9216
Mvaa 84,8,32
s_add_u32 s28, s28, 512
s_cmp_eq_u32 s28, s4
s_cselect_b32 s93, 10240, 0
s_sub_u32 s28, s28, s93
Mvaa 84,12,48
s_add_u32 s93, s28, 20480
Mvaa 52,12,44
Mvav 52,8,252
W 8
Ta 8,3,0
Ta 10,3,1024
Mvav 56,16,252
buffer_store_dwordx4 v[160:163], v1, s[8:11], s36 offen offset:0 sc1
G 168,168
Mvaa 56,20,44
G 169,169
G 170,170
Mvaa 88,20,48
G 171,171
G 172,172
Mvaa 88,16,32
G 173,173
G 174,174
Mvaa 120,16,36
Ta 12,3,8192
Ta 14,3,9216
Mvaa 120,20,52
buffer_load_dwordx4 v[208:211], v26, s[8:11], s28 offen offset:0 sc1
G 175,175
Mvaa 152,20,56
K 168,168,169
K 169,170,171
Mvaa 152,16,40
W 8
K 170,172,173
K 171,174,175
Mvaa 156,24,40
Ta 16,4,0
Ta 18,4,1024
Mvaa 156,28,56
Mvaa 124,28,52
buffer_load_dwordx4 v[216:219], v26, s[8:11], s93 offen offset:0 sc1
Ta 20,4,8192
Mvaa 124,24,36
Ta 22,4,9216
Mvaa 92,24,32
Mvaa 92,28,48
Mvaa 60,28,44
Mvav 60,24,252
s_waitcnt vmcnt(10) lgkmcnt(8)
D 20,48,65,0
Ta 24,5,0
Mvaa 32,0,60
buffer_store_dwordx4 v[168:171], v1, s[8:11], s36 offen offset:1024 sc1
G 176,176
Mvaa 32,4,132
G 177,177
G 178,178
Mvaa 64,4,136
Ta 26,5,1024
G 179,179
Mvav 64,0,184
G 180,180
G 181,181
Mvav 96,0,192
Ta 28,5,8192
G 182,182
Mvaa 96,4,140
buffer_load_dwordx4 v[224:227], v27, s[8:11], s28 offen offset:0 sc1
Ta 30,5,9216
Mvaa 128,4,144
G 183,183
K 176,176,177
Mvaa 128,0,128
W 8
K 177,178,179
K 178,180,181
Mvaa 132,8,128
Ta 0,6,0
K 179,182,183
Mvaa 132,12,144
Ta 2,6,1024
Mvaa 100,12,140
buffer_load_dwordx4 v[188:191], v27, s[8:11], s93 offen offset:0 sc1
Ta 4,6,8192
Mvav 100,8,192
s_add_u32 s28, s28, 512
s_cmp_eq_u32 s28, s4
s_cselect_b32 s93, 10240, 0
s_sub_u32 s28, s28, s93
Mvav 68,8,184
s_add_u32 s93, s28, 20480
Ta 6,6,9216
Mvaa 68,12,136
Mvaa 36,12,132
Mvaa 36,8,60
W 8
buffer_load_dwordx4 v[196:199], v26, s[8:11], s28 offen offset:0 sc1
Ta 8,7,0
Mvaa 40,16,60
buffer_store_dwordx4 v[176:179], v1, s[8:11], s36 offen offset:2048 sc1
G 160,184
Mvaa 40,20,132
G 161,185
G 162,186
Mvaa 72,20,136
Ta 10,7,1024
G 163,187
Mvav 72,16,184
G 164,188
G 165,189
Mvav 104,16,192
Ta 12,7,8192
G 166,190
Mvaa 104,20,140
buffer_load_dwordx4 v[204:207], v26, s[8:11], s93 offen offset:0 sc1
Ta 14,7,9216
Mvaa 136,20,144
G 167,191
K 160,160,161
Mvaa 136,16,128
W 8
K 161,162,163
K 162,164,165
Mvaa 140,24,128
Ta 16,8,0
K 163,166,167
Mvaa 140,28,144
Ta 18,8,1024
Mvaa 108,28,140
D 21,48,65,1024
Ta 20,8,8192
Mvav 108,24,192
Ta 22,8,9216
Mvav 76,24,184
Mvaa 76,28,136
Mvaa 44,28,132
Mvaa 44,24,60
W 8
buffer_load_dwordx4 v[212:215], v27, s[8:11], s28 offen offset:0 sc1
Ta 24,9,0
Mvaa 48,0,60
buffer_store_dwordx4 v[160:163], v1, s[8:11], s36 offen offset:3072 sc1
G 168,192
Mvaa 48,4,132
G 169,193
G 170,194
Mvaa 80,4,136
G 171,195
G 172,196
Mvav 80,0,184
Ta 26,9,1024
G 173,197
Mvav 112,0,192
G 174,198
G 175,199
Mvaa 112,4,140
buffer_load_dwordx4 v[220:223], v27, s[8:11], s93 offen offset:0 sc1
Ta 28,9,8192
Mvaa 144,4,144
s_add_u32 s28, s28, 512
s_cmp_eq_u32 s28, s4
s_cselect_b32 s93, 10240, 0
s_sub_u32 s28, s28, s93
Mvaa 144,0,128
W 7
s_add_u32 s93, s28, 20480
Ta 30,9,9216
Mvaa 148,8,128
K 168,168,169
K 169,170,171
Mvaa 148,12,144
K 170,172,173
K 171,174,175
Mvaa 116,12,140
buffer_load_dwordx4 v[228:231], v26, s[8:11], s28 offen offset:0 sc1
Ta 0,2,16384
Mvav 116,8,192
Ta 2,2,17408
v_accvgpr_write_b32 a192, v168
Mvav 84,8,184
v_accvgpr_write_b32 a193, v169
v_accvgpr_write_b32 a194, v170
Mvaa 84,12,136
Ta 4,2,24576
Ta 6,2,25600
Mvaa 52,12,132
v_accvgpr_write_b32 a195, v171
G 176,64
Mvaa 52,8,60
W 8
D 22,48,65,2048
G 177,65
Mvaa 56,16,60
D 23,48,65,3072
G 178,66
Mvaa 56,20,132
Ta 8,3,16384
Ta 10,3,17408
Mvaa 88,20,136
G 179,67
G 180,68
Mvav 88,16,184
Ta 12,3,24576
Ta 14,3,25600
Mvav 120,16,192
G 181,69
G 182,70
Mvaa 120,20,140
buffer_load_dwordx4 v[236:239], v26, s[8:11], s93 offen offset:0 sc1
G 183,71
Mvaa 152,20,144
v_lshrrev_b32 v20, 4, v1
K 176,176,177
Mvaa 152,16,128
W 8
Ta 16,4,16384
v_add_u32 v20, 0, v20
Mvaa 156,24,128
Ta 18,4,17408
K 177,178,179
Mvaa 156,28,144
v_mul_u32_u24 v22, 205, v20
K 178,180,181
Mvaa 124,28,140
buffer_load_dwordx4 v[232:235], v27, s[8:11], s28 offen offset:0 sc1
K 179,182,183
Mvav 124,24,192
Ta 20,4,24576
Ta 22,4,25600
Mvav 92,24,184
v_lshrrev_b32 v22, 12, v22
v_accvgpr_write_b32 a196, v176
Mvaa 92,28,136
v_mul_u32_u24 v23, 20, v22
v_accvgpr_write_b32 a197, v177
Mvaa 60,28,132
v_accvgpr_write_b32 a198, v178
v_sub_u32 v20, v20, v23
Mvaa 60,24,60
s_waitcnt vmcnt(12) lgkmcnt(8)
buffer_load_dwordx4 a[32:35], v27, s[8:11], s93 offen offset:0 sc1
v_accvgpr_write_b32 a199, v179
Mvaa 32,0,148
s_add_u32 s28, s28, 512
s_cmp_eq_u32 s28, s4
s_cselect_b32 s93, 10240, 0
s_sub_u32 s28, s28, s93
Mvav 32,4,208
s_add_u32 s93, s28, 20480
Ta 24,5,16384
Ta 26,5,17408
Mvav 64,4,216
v_lshrrev_b32 v23, 1, v22
G 168,200
Mvaa 64,0,152
v_and_b32 v23, 2, v23
G 169,201
Mvaa 96,0,156
buffer_load_dwordx4 a[36:39], v26, s[8:11], s28 offen offset:0 sc1
Ta 28,5,24576
Mvav 96,4,224
Ta 30,5,25600
G 170,202
Mvav 128,4,188
v_xor_b32 v20, v20, v23
G 171,203
Mvav 128,0,200
W 8
v_mul_u32_u24 v22, 80, v22
G 172,204
Mvav 132,8,200
v_add_lshl_u32 v16, v20, v22, 4
G 173,205
Mvav 132,12,188
buffer_load_dwordx4 a[40:43], v26, s[8:11], s93 offen offset:0 sc1
Ta 0,6,16384
Mvav 100,12,224
Ta 2,6,17408
G 174,206
Mvaa 100,8,156
v_lshrrev_b32 v20, 4, v1
G 175,207
Mvaa 68,8,152
Ta 4,6,24576
v_add_u32 v20, 64, v20
Mvav 68,12,216
Ta 6,6,25600
K 168,168,169
Mvav 36,12,208
buffer_load_dwordx4 a[44:47], v27, s[8:11], s28 offen offset:0 sc1
K 169,170,171
Mvaa 36,8,148
W 8
v_mul_u32_u24 v22, 205, v20
K 170,172,173
Mvaa 40,16,148
v_lshrrev_b32 v22, 12, v22
K 171,174,175
Mvav 40,20,208
Ta 8,7,16384
v_mul_u32_u24 v23, 20, v22
Mvav 72,20,216
Ta 10,7,17408
v_accvgpr_write_b32 a200, v168
Mvaa 72,16,152
buffer_load_dwordx4 a[48:51], v27, s[8:11], s93 offen offset:0 sc1
v_accvgpr_write_b32 a201, v169
Mvaa 104,16,156
s_add_u32 s28, s28, 512
s_cmp_eq_u32 s28, s4
s_cselect_b32 s93, 10240, 0
s_sub_u32 s28, s28, s93
Mvav 104,20,224
s_add_u32 s93, s28, 20480
v_sub_u32 v20, v20, v23
v_accvgpr_write_b32 a202, v170
Mvav 136,20,188
Ta 12,7,24576
Ta 14,7,25600
Mvav 136,16,200
W 8
v_lshrrev_b32 v23, 1, v22
v_accvgpr_write_b32 a203, v171
Mvav 140,24,200
buffer_load_dwordx4 a[52:55], v26, s[8:11], s28 offen offset:0 sc1
v_and_b32 v23, 2, v23
Mvav 140,28,188
G 176,72
G 177,73
Mvav 108,28,224
)"
R"(v_xor_b32 v20, v20, v23
G 178,74
Mvaa 108,24,156
Ta 16,8,16384
Ta 18,8,17408
Mvaa 76,24,152
v_mul_u32_u24 v22, 80, v22
G 179,75
Mvav 76,28,216
buffer_load_dwordx4 a[56:59], v26, s[8:11], s93 offen offset:0 sc1
G 180,76
Mvav 44,28,208
Ta 20,8,24576
v_add_lshl_u32 v17, v20, v22, 4
Mvaa 44,24,148
W 7
Ta 22,8,25600
G 181,77
Mvaa 48,0,148
v_subrev_u32 v17, 1024, v17
G 182,78
Mvav 48,4,208
v_lshrrev_b32 v20, 4, v1
G 183,79
Mvav 80,4,216
buffer_load_dwordx4 a[184:187], v27, s[8:11], s28 offen offset:0 sc1
K 176,176,177
Mvaa 80,0,152
Ta 24,9,16384
v_add_u32 v20, 128, v20
Mvaa 112,0,156
Ta 26,9,17408
K 177,178,179
Mvav 112,4,224
v_mul_u32_u24 v22, 205, v20
K 178,180,181
Mvav 144,4,188
v_lshrrev_b32 v22, 12, v22
K 179,182,183
Mvav 144,0,200
W 6
buffer_load_dwordx4 a[188:191], v27, s[8:11], s93 offen offset:0 sc1
Ta 28,9,24576
Mvav 148,8,200
s_add_u32 s28, s28, 512
s_cmp_eq_u32 s28, s4
s_cselect_b32 s93, 10240, 0
s_sub_u32 s28, s28, s93
Mvav 148,12,188
s_add_u32 s93, s28, 20480
Ta 30,9,25600
v_accvgpr_write_b32 a204, v176
Mvav 116,12,224
v_mul_u32_u24 v23, 20, v22
v_accvgpr_write_b32 a205, v177
Mvaa 116,8,156
s_waitcnt vmcnt(15)
v_mov_b64 v[28:29], s[12:13]
s_mov_b64 exec, 1
buffer_store_dwordx2 v[28:29], off, s[8:11], s71 sc1
s_mov_b64 exec, -1
v_sub_u32 v20, v20, v23
v_accvgpr_write_b32 a206, v178
Mvaa 84,8,152
s_waitcnt vmcnt(12) lgkmcnt(0)
s_barrier
Mvav 84,12,216
v_accvgpr_write_b32 a207, v179
v_lshrrev_b32 v23, 1, v22
Mvav 52,12,208
G 168,208
v_and_b32 v23, 2, v23
Mvaa 52,8,148
G 169,209
v_xor_b32 v20, v20, v23
Mvaa 56,16,148
buffer_load_dwordx4 a[60:63], v26, s[8:11], s28 offen offset:0 sc1
G 170,210
Mvav 56,20,208
Ta 0,10,32768
G 171,211
Mvav 88,20,216
Ta 2,10,33792
Ta 4,10,40960
Mvaa 88,16,152
Ta 6,10,41984
v_mul_u32_u24 v22, 80, v22
Mvaa 120,16,156
G 172,212
v_add_lshl_u32 v18, v20, v22, 4
Mvav 120,20,224
buffer_load_dwordx4 v[184:187], v26, s[8:11], s93 offen offset:0 sc1
G 173,213
Mvav 152,20,188
Ta 8,11,32768
Ta 10,11,33792
Mvav 152,16,200
G 174,214
v_subrev_u32 v18, 2048, v18
Mvav 156,24,200
G 175,215
v_lshrrev_b32 v20, 4, v1
Mvav 156,28,188
Ta 12,11,40960
Ta 14,11,41984
Mvav 124,28,224
buffer_load_dwordx4 v[192:195], v27, s[8:11], s28 offen offset:0 sc1
K 168,168,169
Mvaa 124,24,156
v_add_u32 v20, 192, v20
K 169,170,171
Mvaa 92,24,152
K 170,172,173
v_mul_u32_u24 v22, 205, v20
Mvav 92,28,216
K 171,174,175
v_lshrrev_b32 v22, 12, v22
Mvav 60,28,208
Ta 16,12,32768
Ta 18,12,33792
Mvaa 60,24,148
s_waitcnt vmcnt(12) lgkmcnt(6)
buffer_load_dwordx4 a[128:131], v27, s[8:11], s93 offen offset:0 sc1
v_accvgpr_write_b32 a208, v168
Mvav 32,0,196
s_add_u32 s28, s28, 512
v_mul_u32_u24 v23, 20, v22
v_accvgpr_write_b32 a209, v169
Mvav 32,4,228
s_cmp_eq_u32 s28, s4
s_cselect_b32 s93, 10240, 0
v_accvgpr_write_b32 a210, v170
v_sub_u32 v20, v20, v23
Mvav 64,4,236
s_sub_u32 s28, s28, s93
Ta 20,12,40960
Ta 22,12,41984
Mvav 64,0,204
s_add_u32 s93, s28, 20480
v_accvgpr_write_b32 a211, v171
v_lshrrev_b32 v23, 1, v22
Mvav 96,0,212
G 176,80
G 177,81
Mvav 96,4,232
v_and_b32 v23, 2, v23
G 178,82
Mvaa 128,4,32
Ta 24,13,32768
v_xor_b32 v20, v20, v23
Mvav 128,0,220
W 5
Ta 26,13,33792
G 179,83
Mvav 132,8,220
buffer_load_dwordx4 a[132:135], v26, s[8:11], s28 offen offset:0 sc1
v_mul_u32_u24 v22, 80, v22
Mvaa 132,12,32
Ta 28,13,40960
G 180,84
Mvav 100,12,232
Ta 30,13,41984
G 181,85
Mvav 100,8,212
v_add_lshl_u32 v19, v20, v22, 4
G 182,86
Mvav 68,8,204
v_subrev_u32 v19, 3072, v19
G 183,87
Mvav 68,12,236
v_lshrrev_b32 v20, 4, v1
K 176,176,177
Mvav 36,12,228
Ta 0,14,32768
Ta 2,14,33792
Mvav 36,8,196
W 6
K 177,178,179
v_add_u32 v20, 256, v20
Mvav 40,16,196
buffer_load_dwordx4 a[136:139], v26, s[8:11], s93 offen offset:0 sc1
K 178,180,181
Mvav 40,20,228
v_mul_u32_u24 v22, 205, v20
K 179,182,183
Mvav 72,20,236
Ta 4,14,40960
Ta 6,14,41984
Mvav 72,16,204
v_accvgpr_write_b32 a212, v176
v_lshrrev_b32 v22, 12, v22
Mvav 104,16,212
v_accvgpr_write_b32 a213, v177
v_mul_u32_u24 v23, 20, v22
Mvav 104,20,232
v_accvgpr_write_b32 a214, v178
v_sub_u32 v20, v20, v23
Mvaa 136,20,32
Ta 8,15,32768
v_accvgpr_write_b32 a215, v179
Mvav 136,16,220
W 5
Ta 10,15,33792
buffer_load_dwordx4 a[140:143], v27, s[8:11], s28 offen offset:0 sc1
Mvav 140,24,220
G 168,216
v_lshrrev_b32 v23, 1, v22
Mvaa 140,28,32
G 169,217
v_and_b32 v23, 2, v23
Mvav 108,28,232
Ta 12,15,40960
Ta 14,15,41984
Mvav 108,24,212
G 170,218
v_xor_b32 v20, v20, v23
Mvav 76,24,204
G 171,219
G 172,220
Mvav 76,28,236
v_mul_u32_u24 v22, 80, v22
G 173,221
Mvav 44,28,228
Ta 16,24,32768
Ta 18,24,33792
Mvav 44,24,196
W 6
v_add_lshl_u32 v20, v20, v22, 4
G 174,222
Mvav 48,0,196
buffer_load_dwordx4 a[144:147], v27, s[8:11], s93 offen offset:0 sc1
G 175,223
Mvav 48,4,228
s_add_u32 s28, s28, 512
Ta 20,24,40960
v_lshrrev_b32 v22, 6, v1
Mvav 80,4,236
s_cmp_eq_u32 s28, s4
s_cselect_b32 s93, 10240, 0
Ta 22,24,41984
K 168,168,169
Mvav 80,0,204
s_sub_u32 s28, s28, s93
v_mul_u32_u24 v22, 320, v22
K 169,170,171
Mvav 112,0,212
s_add_u32 s93, s28, 20480
v_bfe_u32 v23, v1, 4, 2
K 170,172,173
Mvav 112,4,232
K 171,174,175
v_lshl_add_u32 v22, v23, 3, v22
Mvaa 144,4,32
Ta 24,25,32768
Ta 26,25,33792
Mvav 144,0,220
W 6
v_accvgpr_write_b32 a216, v168
v_bfe_u32 v23, v1, 8, 1
Mvav 148,8,220
buffer_load_dwordx4 v[200:203], v26, s[8:11], s28 offen offset:0 sc1
v_accvgpr_write_b32 a217, v169
Mvaa 148,12,32
v_lshlrev_b32 v23, 5, v23
v_accvgpr_write_b32 a218, v170
Mvav 116,12,232
Ta 28,25,40960
Ta 30,25,41984
Mvav 116,8,212
v_accvgpr_write_b32 a219, v171
v_add_u32 v21, v22, v23
Mvav 84,8,204
G 176,88
v_sub_u32 v22, v22, v23
Mvav 84,12,236
s_mul_i32 s94, s59, 655360
G 177,89
G 178,90
Mvav 52,12,228
s_mul_i32 s95, s58, 320
Ta 0,10,49152
Ta 2,10,50176
Mvav 52,8,196
W 6
buffer_load_dwordx4 v[208:211], v26, s[8:11], s93 offen offset:0 sc1
G 179,91
Mvav 56,16,196
s_add_u32 s94, s94, s95
Ta 4,10,57344
G 180,92
Mvav 56,20,228
Ta 6,10,58368
G 181,93
Mvav 88,20,236
s_add_u32 s40, s26, s94
s_addc_u32 s41, s27, 0
G 182,94
Mvav 88,16,204
s_and_b32 s41, s41, 0xffff
G 183,95
K 176,176,177
Mvav 120,16,212
s_mov_b32 s42, 5242880
Ta 8,11,49152
Ta 10,11,50176
Mvav 120,20,232
s_mov_b32 s43, 0x20000
buffer_load_dwordx4 v[216:219], v27, s[8:11], s28 offen offset:0 sc1
K 177,178,179
Mvaa 152,20,32
s_sub_u32 s42, s42, s94
K 178,180,181
K 179,182,183
Mvav 152,16,220
W 6
Ta 12,11,57344
Ta 14,11,58368
Mvav 156,24,220
s_mov_b64 s[44:45], s[8:9]
v_accvgpr_write_b32 a220, v176
v_accvgpr_write_b32 a221, v177
Mvaa 156,28,32
s_mov_b32 s47, 0x20000
v_accvgpr_write_b32 a222, v178
v_accvgpr_write_b32 a223, v179
Mvav 124,28,232
s_and_b32 s48, s14, 1
Ta 16,12,49152
Ta 18,12,50176
Mvav 124,24,212
buffer_load_dwordx4 v[224:227], v27, s[8:11], s93 offen offset:0 sc1
G 168,224
Mvav 92,24,204
s_mul_i32 s48, s48, 5120
Ta 20,12,57344
G 169,225
Mvav 92,28,236
s_lshr_b32 s51, s14, 1
Ta 22,12,58368
G 170,226
Mvav 60,28,228
G 171,227
G 172,228
Mvav 60,24,196
s_waitcnt vmcnt(13) lgkmcnt(8)
s_and_b32 s94, s14, 1
G 173,229
Mvaa 32,0,36
s_add_u32 s28, s28, 512
Ta 24,13,49152
Ta 26,13,50176
Mvaa 32,4,52
s_cmp_eq_u32 s28, s4
s_cselect_b32 s93, 10240, 0
s_sub_u32 s28, s28, s93
G 174,230
Mvaa 64,4,56
s_add_u32 s93, s28, 20480
s_lshl_b32 s95, s57, 1
buffer_load_dwordx4 v[188:191], v26, s[8:11], s28 offen offset:0 sc1
Mvaa 64,0,40
s_add_u32 s94, s94, s95
Ta 28,13,57344
G 175,231
Mvaa 96,0,44
s_lshl_b32 s54, s94, 3
Ta 30,13,58368
K 168,168,169
Mvaa 96,4,184
K 169,170,171
K 170,172,173
Mvaa 128,4,188
s_add_u32 s54, s54, s59
K 171,174,175
Mvaa 128,0,48
W 8
Ta 0,14,49152
Ta 2,14,50176
Mvaa 132,8,48
s_lshl_b32 s54, s54, 6
buffer_load_dwordx4 v[196:199], v26, s[8:11], s93 offen offset:0 sc1
v_accvgpr_write_b32 a224, v168
Mvaa 132,12,188
s_xor_b32 s55, s54, 15872
v_accvgpr_write_b32 a225, v169
Mvaa 100,12,184
Ta 4,14,57344
v_accvgpr_write_b32 a226, v170
Mvaa 100,8,44
s_add_u32 s54, s54, 20480
Ta 6,14,58368
v_accvgpr_write_b32 a227, v171
Mvaa 68,8,40
s_and_b32 s81, s14, 1
G 176,96
Mvaa 68,12,56
s_add_u32 s55, s55, 20480
Mvaa 36,12,52
G 177,97
Mvaa 36,8,36
)"
R"(W 8
s_and_b32 s95, s14, 2
G 178,98
Mvaa 40,16,36
s_mul_i32 s91, s81, 17164288
Ta 8,15,49152
Ta 10,15,50176
Mvaa 40,20,52
s_lshl_b32 s94, s94, 2
buffer_load_dwordx4 v[204:207], v27, s[8:11], s28 offen offset:0 sc1
G 179,99
Mvaa 72,20,56
s_mul_i32 s82, s56, 536576
s_add_u32 s94, s94, s95
G 180,100
Mvaa 72,16,40
Ta 12,15,57344
Ta 14,15,58368
Mvaa 104,16,44
s_lshl_b32 s95, s95, 4
G 181,101
G 182,102
Mvaa 104,20,184
s_add_u32 s91, s91, s82
G 183,103
Mvaa 136,20,188
s_add_u32 s55, s55, s95
s_add_u32 s91, s91, 4194304
K 176,176,177
Mvaa 136,16,48
W 8
s_lshl_b32 s49, s94, 17
Ta 16,24,49152
Mvaa 140,24,48
Ta 18,24,50176
K 177,178,179
Mvaa 140,28,188
s_lshl_b32 s95, s59, 14
buffer_load_dwordx4 v[212:215], v27, s[8:11], s93 offen offset:0 sc1
K 178,180,181
Mvaa 108,28,184
s_lshr_b32 s82, s14, 1
Ta 20,24,57344
K 179,182,183
Mvaa 108,24,44
s_add_u32 s49, s49, s95
Ta 22,24,58368
v_accvgpr_write_b32 a228, v176
Mvaa 76,24,40
s_lshl_b32 s83, s82, 14
s_xor_b32 s50, s49, 16252928
Mvaa 76,28,56
v_accvgpr_write_b32 a229, v177
Mvaa 44,28,52
s_add_u32 s91, s91, s83
s_add_u32 s49, s49, 39845888
v_accvgpr_write_b32 a230, v178
Mvaa 44,24,36
W 8
Ta 24,25,49152
v_accvgpr_write_b32 a231, v179
Mvaa 48,0,36
s_add_u32 s50, s50, 39845888
Ta 26,25,50176
G 168,232
Mvaa 48,4,52
s_lshl_b32 s81, s81, 14
Mvaa 80,4,56
s_lshr_b32 s94, s15, 3
Ta 28,25,57344
G 169,233
Mvaa 80,0,40
s_lshl_b32 s82, s82, 16
Ta 30,25,58368
G 170,234
Mvaa 112,0,44
s_lshl_b32 s94, s94, 3
G 171,235
Mvaa 112,4,184
s_add_u32 s94, s94, s14
s_or_b32 s92, s81, s82
G 172,236
Mvaa 144,4,188
Mvaa 144,0,48
W 8
s_lshl_b32 s94, s94, 4
G 173,237
Mvaa 148,8,48
s_xor_b32 s92, s92, 0x10000
Ta 0,2,32768
Ta 2,2,33792
Mvaa 148,12,188
s_add_u32 s94, s94, s59
G 174,238
G 175,239
Mvaa 116,12,184
s_mul_i32 s52, s94, 5120
Ta 4,2,40960
Ta 6,2,41984
Mvaa 116,8,44
K 168,168,169
Mvaa 84,8,40
s_lshl_b32 s95, s59, 12
K 169,170,171
Mvaa 84,12,56
K 170,172,173
Mvaa 52,12,52
s_sub_u32 s52, s52, s95
K 171,174,175
Mvaa 52,8,36
W 8
s_add_u32 s52, s52, 56623104
Ta 8,3,32768
Mvaa 56,16,36
Ta 10,3,33792
v_accvgpr_write_b32 a232, v168
Mvaa 56,20,52
s_lshl_b32 s53, s94, 3
v_accvgpr_write_b32 a233, v169
Mvaa 88,20,56
Ta 12,3,40960
v_accvgpr_write_b32 a234, v170
Mvaa 88,16,40
s_add_u32 s53, s53, 65536
Ta 14,3,41984
v_accvgpr_write_b32 a235, v171
Mvaa 120,16,44
G 176,104
v_bfe_u32 v244, v1, 4, 1
Mvaa 120,20,184
Mvaa 152,20,188
G 177,105
v_mul_u32_u24 v244, 0xaaaa, v244
Mvaa 152,16,48
W 8
G 178,106
v_add_u32 v244, 0x4444, v244
Mvaa 156,24,48
Ta 16,4,32768
Ta 18,4,33792
Mvaa 156,28,188
G 179,107
v_lshl_or_b32 v240, v244, 16, v244
Mvaa 124,28,184
G 180,108
v_mov_b32 v241, 0xff800000
Mvaa 124,24,44
Ta 20,4,40960
Ta 22,4,41984
Mvaa 92,24,40
G 181,109
v_add_u32 v242, 0x10000, v1
Mvaa 92,28,56
G 182,110
v_bfe_u32 v244, v1, 4, 4
Mvaa 60,28,52
G 183,111
v_lshrrev_b32 v245, 8, v1
Mvaa 60,24,36
s_waitcnt vmcnt(8) lgkmcnt(8)
K 176,176,177
v_lshlrev_b32 v245, 2, v245
Mvaa 32,0,60
Ta 24,5,32768
Ta 26,5,33792
Mvaa 32,4,132
K 177,178,179
v_sub_u32 v244, v244, v245
Mvaa 64,4,136
K 178,180,181
v_cmp_le_i32_e64 s[72:73], 0, v244
Mvav 64,0,184
Ta 28,5,40960
K 179,182,183
Mvav 96,0,192
Ta 30,5,41984
v_cmp_le_i32_e64 s[74:75], 1, v244
Mvaa 96,4,140
v_accvgpr_write_b32 a236, v176
v_accvgpr_write_b32 a237, v177
Mvaa 128,4,144
v_cmp_le_i32_e64 s[76:77], 2, v244
v_accvgpr_write_b32 a238, v178
Mvaa 128,0,128
W 8
v_cmp_le_i32_e64 s[78:79], 3, v244
v_accvgpr_write_b32 a239, v179
Mvaa 132,8,128
Ta 0,6,32768
v_add_u32 v243, 4096, v1
Mvaa 132,12,144
Ta 2,6,33792
G 168,240
Mvaa 100,12,140
v_add_u32 v244, 8192, v1
G 169,241
Mvav 100,8,192
Ta 4,6,40960
v_add_u32 v245, 12288, v1
Mvav 68,8,184
s_mov_b32 s18, 0x3f803f80
Ta 6,6,41984
G 170,242
Mvaa 68,12,136
s_mov_b32 s19, 0x3f003f00
G 171,243
G 172,244
Mvaa 36,12,132
v_accvgpr_write_b32 a176, s18
Mvaa 36,8,60
W 8
Ta 8,7,32768
G 173,245
Mvaa 40,16,60
Ta 10,7,33792
v_accvgpr_write_b32 a180, s19
Mvaa 40,20,132
G 174,246
v_mov_b32 v252, v241
Mvaa 72,20,136
G 175,247
v_accvgpr_write_b32 a177, s18
Mvav 72,16,184
Ta 12,7,40960
Ta 14,7,41984
Mvav 104,16,192
K 168,168,169
v_accvgpr_write_b32 a181, s19
Mvaa 104,20,140
K 169,170,171
K 170,172,173
Mvaa 136,20,144
v_mov_b32 v253, v241
K 171,174,175
Mvaa 136,16,128
W 8
Ta 16,8,32768
v_accvgpr_write_b32 a178, s18
Mvaa 140,24,128
Ta 18,8,33792
v_accvgpr_write_b32 a240, v168
Mvaa 140,28,144
v_accvgpr_write_b32 a182, s19
v_accvgpr_write_b32 a241, v169
Mvaa 108,28,140
v_mov_b32 v254, v241
v_accvgpr_write_b32 a242, v170
Mvav 108,24,192
Ta 20,8,40960
v_accvgpr_write_b32 a179, s18
Mvav 76,24,184
Ta 22,8,41984
v_accvgpr_write_b32 a243, v171
Mvaa 76,28,136
v_accvgpr_write_b32 a183, s19
G 176,112
Mvaa 44,28,132
v_mov_b32 v255, v241
G 177,113
Mvaa 44,24,60
W 8
v_accvgpr_write_b32 a160, 0
G 178,114
Mvaa 48,0,60
Ta 24,9,32768
Ta 26,9,33792
Mvaa 48,4,132
v_accvgpr_write_b32 a161, 0
G 179,115
Mvaa 80,4,136
v_accvgpr_write_b32 a162, 0
G 180,116
Mvav 80,0,184
Ta 28,9,40960
Ta 30,9,41984
Mvav 112,0,192
v_accvgpr_write_b32 a163, 0
G 181,117
Mvaa 112,4,140
v_accvgpr_write_b32 a164, 0
G 182,118
Mvaa 144,4,144
G 183,119
v_accvgpr_write_b32 a165, 0
Mvaa 144,0,128
W 8
K 176,176,177
v_accvgpr_write_b32 a166, 0
Mvaa 148,8,128
Ta 0,2,49152
Ta 2,2,50176
Mvaa 148,12,144
K 177,178,179
v_accvgpr_write_b32 a167, 0
Mvaa 116,12,140
K 178,180,181
v_accvgpr_write_b32 a168, 0
Mvav 116,8,192
Ta 4,2,57344
K 179,182,183
Mvav 84,8,184
Ta 6,2,58368
v_accvgpr_write_b32 a169, 0
Mvaa 84,12,136
v_accvgpr_write_b32 a244, v176
v_accvgpr_write_b32 a170, 0
Mvaa 52,12,132
v_accvgpr_write_b32 a245, v177
v_accvgpr_write_b32 a171, 0
Mvaa 52,8,60
W 8
v_accvgpr_write_b32 a246, v178
v_accvgpr_write_b32 a172, 0
Mvaa 56,16,60
Ta 8,3,49152
v_accvgpr_write_b32 a247, v179
Mvaa 56,20,132
Ta 10,3,50176
v_accvgpr_write_b32 a173, 0
Mvaa 88,20,136
G 168,248
v_accvgpr_write_b32 a174, 0
Mvav 88,16,184
Ta 12,3,57344
G 169,249
Mvav 120,16,192
Ta 14,3,58368
v_accvgpr_write_b32 a175, 0
Mvaa 120,20,140
s_mov_b32 s90, 0x5d800000
G 170,250
G 171,251
Mvaa 152,20,144
s_mov_b64 s[88:89], 0
G 172,252
Mvaa 152,16,128
W 8
s_mov_b32 s64, 0
G 173,253
Mvaa 156,24,128
s_and_b32 s18, s14, 1
Ta 16,4,49152
Ta 18,4,50176
Mvaa 156,28,144
G 174,254
G 175,255
Mvaa 124,28,140
s_lshl_b32 s62, s18, 14
Ta 20,4,57344
Ta 22,4,58368
Mvav 124,24,192
s_lshl_b32 s20, s57, 15
K 168,168,169
Mvav 92,24,184
s_sub_u32 s20, 507904, s20
K 169,170,171
Mvaa 92,28,136
K 170,172,173
Mvaa 60,28,132
s_add_u32 s20, s20, s62
K 171,174,175
Mvaa 60,24,60
s_waitcnt vmcnt(0) lgkmcnt(8)
s_lshl_b32 s65, s57, 1
Ta 24,5,49152
Mvav 32,0,200
s_sub_u32 s65, s65, 1
Ta 26,5,50176
v_accvgpr_write_b32 a248, v168
Mvav 32,4,188
v_accvgpr_write_b32 a249, v169
Mvav 64,4,196
s_max_i32 s65, s65, 0
Ta 28,5,57344
v_accvgpr_write_b32 a250, v170
Mvav 64,0,208
s_sub_u32 s66, s60, 1
Ta 30,5,58368
v_accvgpr_write_b32 a251, v171
Mvav 96,0,216
G 176,120
Mvav 96,4,204
s_cmp_lt_u32 s14, 2
s_cselect_b32 s20, s20, 0
s_cselect_b32 s66, s66, 15
s_cselect_b32 s21, 32768, 0
Mvav 128,4,212
s_add_u32 s61, s91, s20
G 177,121
Mvav 128,0,224
W 8
s_add_u32 s62, s62, s21
G 178,122
Mvav 132,8,224
Ta 0,6,49152
Ta 2,6,50176
Mvav 132,12,212
s_sub_u32 s63, 16384, s21
G 179,123
G 180,124
)"
R"(Mvav 100,12,204
s_lshl_b32 s67, s60, 7
Ta 4,6,57344
Ta 6,6,58368
Mvav 100,8,216
s_add_u32 s67, s67, s56
G 181,125
Mvav 68,8,208
G 182,126
Mvav 68,12,196
s_lshl_b32 s67, s67, 12
G 183,127
Mvav 36,12,188
s_add_u32 s67, s67, 39845888
K 176,176,177
Mvav 36,8,200
W 8
s_lshl_b32 s69, s60, 3
Ta 8,7,49152
Mvav 40,16,200
Ta 10,7,50176
K 177,178,179
Mvav 40,20,188
s_add_u32 s69, s69, s59
K 178,180,181
Mvav 72,20,196
s_lshl_b32 s69, s69, 6
Ta 12,7,57344
K 179,182,183
Mvav 72,16,208
s_lshl_b32 s21, s58, 3
Ta 14,7,58368
v_accvgpr_write_b32 a252, v176
Mvav 104,16,216
Mvav 104,20,204
s_add_u32 s69, s69, s21
v_accvgpr_write_b32 a253, v177
Mvav 136,20,212
s_add_u32 s69, s69, 20480
v_accvgpr_write_b32 a254, v178
Mvav 136,16,224
W 8
Ta 16,8,49152
v_accvgpr_write_b32 a255, v179
Mvav 140,24,224
s_mov_b32 s70, 0
Ta 18,8,50176
Mvav 140,28,212
s_mov_b32 s37, 0
Mvav 108,28,204
s_sub_u32 s18, s57, 4
Ta 20,8,57344
Ta 22,8,58368
Mvav 108,24,216
Mvav 76,24,208
s_lshl_b32 s18, s18, 2
Mvav 76,28,196
s_add_u32 s18, s18, s58
Mvav 44,28,188
s_mov_b32 s85, s59
Mvav 44,24,200
W 8
Ta 24,9,49152
Ta 26,9,50176
Mvav 48,0,200
s_mul_i32 s85, s85, 7
Mvav 48,4,188
s_add_u32 s85, s85, s18
Mvav 80,4,196
s_cmp_lt_u32 s18, 7
s_cselect_b32 s85, s85, 99
Ta 28,9,57344
Ta 30,9,58368
Mvav 80,0,208
Mvav 112,0,216
s_cmp_lt_u32 s85, 54
s_cselect_b32 s65, 2, s65
Mvav 112,4,204
s_mov_b32 s68, 0
Mvav 144,4,212
s_mov_b64 s[24:25], -1
Mvav 144,0,224
W 8
Mvav 148,8,224
s_mov_b32 s22, 0x3e0293ee
Mvav 148,12,212
s_mov_b32 s23, 0x3e0293ee
Mvav 116,12,204
Mvav 116,8,216
Mvav 84,8,208
Mvav 84,12,196
Mvav 52,12,188
Mvav 52,8,200
W 4
Mvav 56,16,200
Mvav 56,20,188
Mvav 88,20,196
Mvav 88,16,208
Mvav 120,16,216
W 0
s_barrier
Mvav 120,20,204
Ra 128,242,0
Mvav 152,20,212
Ra 132,242,1024
Mvav 152,16,224
Mvav 156,24,224
Ra 136,242,2048
Mvav 156,28,212
Mvav 124,28,204
Ra 140,242,3072
Mvav 124,24,216
Mvav 92,24,208
Mvav 92,28,196
Mvav 60,28,188
Mvav 60,24,200
.Lago%=:
s_cmp_eq_u32 s60, 1
s_cbranch_scc1 .Lpe_44%=
s_cmp_eq_u32 s60, 0
s_cbranch_scc1 .Lpd_28%=
v_pk_mul_f32 v[32:33], v[32:33], s[22:23]
v_pk_mul_f32 v[34:35], v[34:35], s[22:23]
v_pk_mul_f32 v[36:37], v[36:37], s[22:23]
v_pk_mul_f32 v[38:39], v[38:39], s[22:23]
v_pk_mul_f32 v[64:65], v[64:65], s[22:23]
v_pk_mul_f32 v[66:67], v[66:67], s[22:23]
v_pk_mul_f32 v[68:69], v[68:69], s[22:23]
v_pk_mul_f32 v[70:71], v[70:71], s[22:23]
v_pk_mul_f32 v[96:97], v[96:97], s[22:23]
v_pk_mul_f32 v[98:99], v[98:99], s[22:23]
v_pk_mul_f32 v[100:101], v[100:101], s[22:23]
v_pk_mul_f32 v[102:103], v[102:103], s[22:23]
v_pk_mul_f32 v[128:129], v[128:129], s[22:23]
v_pk_mul_f32 v[130:131], v[130:131], s[22:23]
v_pk_mul_f32 v[132:133], v[132:133], s[22:23]
v_pk_mul_f32 v[134:135], v[134:135], s[22:23]
v_pk_mul_f32 v[40:41], v[40:41], s[22:23]
v_pk_mul_f32 v[42:43], v[42:43], s[22:23]
v_pk_mul_f32 v[44:45], v[44:45], s[22:23]
v_pk_mul_f32 v[46:47], v[46:47], s[22:23]
v_pk_mul_f32 v[72:73], v[72:73], s[22:23]
v_pk_mul_f32 v[74:75], v[74:75], s[22:23]
v_pk_mul_f32 v[76:77], v[76:77], s[22:23]
v_pk_mul_f32 v[78:79], v[78:79], s[22:23]
v_pk_mul_f32 v[104:105], v[104:105], s[22:23]
v_pk_mul_f32 v[106:107], v[106:107], s[22:23]
v_pk_mul_f32 v[108:109], v[108:109], s[22:23]
v_pk_mul_f32 v[110:111], v[110:111], s[22:23]
v_pk_mul_f32 v[136:137], v[136:137], s[22:23]
v_pk_mul_f32 v[138:139], v[138:139], s[22:23]
v_pk_mul_f32 v[140:141], v[140:141], s[22:23]
v_pk_mul_f32 v[142:143], v[142:143], s[22:23]
v_pk_mul_f32 v[48:49], v[48:49], s[22:23]
v_pk_mul_f32 v[50:51], v[50:51], s[22:23]
v_pk_mul_f32 v[52:53], v[52:53], s[22:23]
v_pk_mul_f32 v[54:55], v[54:55], s[22:23]
v_pk_mul_f32 v[80:81], v[80:81], s[22:23]
v_pk_mul_f32 v[82:83], v[82:83], s[22:23]
v_pk_mul_f32 v[84:85], v[84:85], s[22:23]
v_pk_mul_f32 v[86:87], v[86:87], s[22:23]
v_pk_mul_f32 v[112:113], v[112:113], s[22:23]
v_pk_mul_f32 v[114:115], v[114:115], s[22:23]
v_pk_mul_f32 v[116:117], v[116:117], s[22:23]
v_pk_mul_f32 v[118:119], v[118:119], s[22:23]
v_pk_mul_f32 v[144:145], v[144:145], s[22:23]
v_pk_mul_f32 v[146:147], v[146:147], s[22:23]
v_pk_mul_f32 v[148:149], v[148:149], s[22:23]
v_pk_mul_f32 v[150:151], v[150:151], s[22:23]
v_pk_mul_f32 v[56:57], v[56:57], s[22:23]
v_pk_mul_f32 v[58:59], v[58:59], s[22:23]
v_pk_mul_f32 v[60:61], v[60:61], s[22:23]
v_pk_mul_f32 v[62:63], v[62:63], s[22:23]
v_pk_mul_f32 v[88:89], v[88:89], s[22:23]
v_pk_mul_f32 v[90:91], v[90:91], s[22:23]
v_pk_mul_f32 v[92:93], v[92:93], s[22:23]
v_pk_mul_f32 v[94:95], v[94:95], s[22:23]
v_pk_mul_f32 v[120:121], v[120:121], s[22:23]
v_pk_mul_f32 v[122:123], v[122:123], s[22:23]
v_pk_mul_f32 v[124:125], v[124:125], s[22:23]
v_pk_mul_f32 v[126:127], v[126:127], s[22:23]
v_pk_mul_f32 v[152:153], v[152:153], s[22:23]
v_pk_mul_f32 v[154:155], v[154:155], s[22:23]
v_pk_mul_f32 v[156:157], v[156:157], s[22:23]
v_pk_mul_f32 v[158:159], v[158:159], s[22:23]
K 32,32,33
K 33,34,35
K 34,36,37
K 35,38,39
K 40,40,41
K 41,42,43
K 42,44,45
K 43,46,47
W 0
Zvav 196,128,32
s_add_u32 s36, s29, 65536
Ra 144,242,4096
Ra 148,242,5120
Ra 152,242,6144
Ra 156,242,7168
Mvav 196,132,40
K 64,64,65
K 65,66,67
K 66,68,69
K 67,70,71
K 72,72,73
K 73,74,75
K 74,76,77
K 75,78,79
s_nop 1
Zvav 204,132,72
buffer_store_dwordx4 a[192:195], v1, s[8:11], s36 offen offset:0 sc1
K 96,96,97
K 97,98,99
K 98,100,101
Mvav 204,128,64
K 99,102,103
K 104,104,105
K 105,106,107
K 106,108,109
K 107,110,111
Zvav 212,128,96
buffer_store_dwordx4 a[200:203], v1, s[8:11], s36 offen offset:1024 sc1
K 128,128,129
K 129,130,131
K 130,132,133
Mvav 212,132,104
K 131,134,135
K 136,136,137
K 137,138,139
K 138,140,141
K 139,142,143
s_nop 1
Zvav 220,132,136
K 48,48,49
K 49,50,51
K 50,52,53
K 51,54,55
Mvav 220,128,128
K 56,56,57
K 57,58,59
K 58,60,61
K 59,62,63
Mvav 196,136,48
buffer_store_dwordx4 a[208:211], v1, s[8:11], s36 offen offset:2048 sc1
Ra 128,242,8192
Ra 132,242,9216
K 80,80,81
Mvav 196,140,56
K 81,82,83
K 82,84,85
K 83,86,87
K 88,88,89
K 89,90,91
K 90,92,93
K 91,94,95
s_nop 1
Mvav 204,140,88
buffer_store_dwordx4 a[216:219], v1, s[8:11], s36 offen offset:3072 sc1
s_add_u32 s36, s29, 69632
K 112,112,113
K 113,114,115
K 114,116,117
Mvav 204,136,80
K 115,118,119
K 120,120,121
K 121,122,123
K 122,124,125
K 123,126,127
Mvav 212,136,112
K 144,144,145
K 145,146,147
K 146,148,149
K 147,150,151
Mvav 212,140,120
K 152,152,153
K 153,154,155
K 154,156,157
K 155,158,159
s_nop 1
Mvav 220,140,152
buffer_store_dwordx4 a[224:227], v1, s[8:11], s36 offen offset:0 sc1
v_accvgpr_write_b32 a0, v32
v_accvgpr_write_b32 a1, v33
v_accvgpr_write_b32 a2, v34
Mvav 220,136,144
W 2
Ra 136,242,10240
v_accvgpr_write_b32 a3, v35
v_accvgpr_write_b32 a4, v40
Zvav 36,144,32
Ra 140,242,11264
buffer_store_dwordx4 a[232:235], v1, s[8:11], s36 offen offset:1024 sc1
v_accvgpr_write_b32 a5, v41
Mvav 36,148,40
v_accvgpr_write_b32 a6, v42
v_accvgpr_write_b32 a7, v43
v_max3_f32 v4, v196, v197, v198
v_max3_f32 v4, v4, v199, v252
Zvav 68,148,72
v_max3_f32 v5, v204, v205, v206
v_max3_f32 v5, v5, v207, v253
v_max3_f32 v6, v212, v213, v214
v_max3_f32 v6, v6, v215, v254
Mvav 68,144,64
v_max3_f32 v7, v220, v221, v222
v_max3_f32 v7, v7, v223, v255
v_mov_b32 v8, v4
v_mov_b32 v9, v5
Zvav 100,144,96
v_mov_b32 v10, v6
v_mov_b32 v11, v7
v_permlane32_swap_b32 v4, v8
v_accvgpr_write_b32 a8, v48
Mvav 100,148,104
v_permlane32_swap_b32 v5, v9
v_permlane32_swap_b32 v6, v10
v_permlane32_swap_b32 v7, v11
v_max_f32 v4, v4, v8
Zvav 132,148,136
v_max_f32 v5, v5, v9
v_max_f32 v6, v6, v10
v_max_f32 v7, v7, v11
v_mov_b32 v8, v4
Mvav 132,144,128
v_mov_b32 v9, v5
v_mov_b32 v10, v6
v_mov_b32 v11, v7
v_accvgpr_write_b32 a9, v49
Mvav 36,152,48
v_permlane16_swap_b32 v4, v8
v_permlane16_swap_b32 v5, v9
v_permlane16_swap_b32 v6, v10
v_permlane16_swap_b32 v7, v11
Mvav 36,156,56
v_max_f32 v4, v4, v8
v_max_f32 v5, v5, v9
v_max_f32 v6, v6, v10
v_max_f32 v7, v7, v11
Mvav 68,156,88
v_sub_f32 v4, 0, v4
v_sub_f32 v5, 0, v5
v_sub_f32 v6, 0, v6
v_sub_f32 v7, 0, v7
Mvav 68,152,80
v_mov_b32 v224, v4
v_mov_b32 v225, v4
v_mov_b32 v226, v4
v_mov_b32 v227, v4
Mvav 100,152,112
v_mov_b32 v228, v5
v_mov_b32 v229, v5
v_mov_b32 v230, v5
v_mov_b32 v231, v5
Mvav 100,156,120
v_mov_b32 v232, v6
v_mov_b32 v233, v6
v_mov_b32 v234, v6
v_mov_b32 v235, v6
Mvav 132,156,152
v_mov_b32 v236, v7
v_mov_b32 v237, v7
v_mov_b32 v238, v7
v_mov_b32 v239, v7
Mvav 132,152,144
W 0
buffer_store_dwordx4 a[240:243], v1, s[8:11], s36 offen offset:2048 sc1
v_accvgpr_write_b32 a10, v50
v_accvgpr_write_b32 a11, v51
v_accvgpr_write_b32 a12, v56
Cvav 160,128,32,224
s_and_b32 s80, s14, 1
v_accvgpr_write_b32 a13, v57
v_accvgpr_write_b32 a14, v58
v_accvgpr_write_b32 a15, v59
v_accvgpr_write_b32 a16, v64
Mvav 160,132,40
buffer_store_dwordx4 a[248:251], v1, s[8:11], s36 offen offset:3072 sc1
s_add_u32 s36, s29, 393216
Ra 144,242,12288
Ra 148,242,13312
v_accvgpr_write_b32 a17, v65
Cvav 164,132,72,228
s_lshl_b32 s80, s80, 14
v_accvgpr_write_b32 a18, v66
v_accvgpr_write_b32 a19, v67
v_accvgpr_write_b32 a20, v72
v_accvgpr_write_b32 a21, v73
Mvav 164,128,64
s_add_u32 s80, s80, 98304
buffer_store_dwordx4 a[196:199], v1, s[8:11], s36 offen offset:0 sc1
v_accvgpr_write_b32 a22, v74
v_accvgpr_write_b32 a23, v75
v_accvgpr_write_b32 a24, v80
Cvav 168,128,96,232
v_accvgpr_write_b32 a25, v81
v_accvgpr_write_b32 a26, v82
A 196,196,224
v_accvgpr_write_b32 a27, v83
Mvav 168,132,104
)"
R"(buffer_store_dwordx4 a[204:207], v1, s[8:11], s36 offen offset:1024 sc1
A 197,197,224
A 198,198,224
v_accvgpr_write_b32 a28, v88
Cvav 172,132,136,236
Ra 152,242,14336
Ra 156,242,15360
A 199,199,224
v_accvgpr_write_b32 a29, v89
Mvav 172,128,128
buffer_store_dwordx4 a[212:215], v1, s[8:11], s36 offen offset:2048 sc1
X 196
v_accvgpr_write_b32 a30, v90
Mvav 160,136,48
X 197
v_accvgpr_write_b32 a31, v91
v_accvgpr_write_b32 a32, v96
Mvav 160,140,56
X 198
K 192,196,197
v_accvgpr_write_b32 a33, v97
Mvav 164,140,88
X 199
A 204,204,228
v_accvgpr_write_b32 a34, v98
Mvav 164,136,80
buffer_store_dwordx4 a[220:223], v1, s[8:11], s36 offen offset:3072 sc1
s_add_u32 s36, s29, 397312
K 193,198,199
v_accvgpr_write_b32 a35, v99
A 205,205,228
Mvav 168,136,112
A 206,206,228
v_accvgpr_write_b32 a36, v104
v_add_u32 v8, s80, v1
A 207,207,228
Mvav 168,140,120
X 204
v_accvgpr_write_b32 a37, v105
v_accvgpr_write_b32 a38, v106
Mvav 172,140,152
X 205
X 206
Mvav 172,136,144
W 0
buffer_store_dwordx4 a[228:231], v1, s[8:11], s36 offen offset:0 sc1
v_accvgpr_write_b32 a39, v107
v_accvgpr_write_b32 a40, v112
K 200,204,205
Cvav 176,144,32,224
Ra 128,242,24576
X 207
v_accvgpr_write_b32 a41, v113
Mvav 176,148,40
Ra 132,242,25600
A 212,212,232
v_accvgpr_write_b32 a42, v114
K 201,206,207
Cvav 180,148,72,228
A 213,213,232
v_accvgpr_write_b32 a43, v115
A 214,214,232
A 215,215,232
Mvav 180,144,64
buffer_store_dwordx4 a[236:239], v1, s[8:11], s36 offen offset:1024 sc1
v_accvgpr_write_b32 a44, v120
X 212
Cvav 184,144,96,232
v_accvgpr_write_b32 a45, v121
X 213
v_accvgpr_write_b32 a46, v122
Mvav 184,148,104
X 214
v_accvgpr_write_b32 a47, v123
K 208,212,213
Cvav 188,148,136,236
buffer_store_dwordx4 a[244:247], v1, s[8:11], s36 offen offset:2048 sc1
v_accvgpr_write_b32 a48, v128
X 215
Mvav 188,144,128
v_accvgpr_write_b32 a49, v129
A 220,220,236
v_accvgpr_write_b32 a50, v130
K 209,214,215
Mvav 176,152,48
A 221,221,236
v_accvgpr_write_b32 a51, v131
A 222,222,236
A 223,223,236
Mvav 176,156,56
Ra 136,242,26624
v_accvgpr_write_b32 a52, v136
X 220
Mvav 180,156,88
Ra 140,242,27648
v_accvgpr_write_b32 a53, v137
X 221
Mvav 180,152,80
buffer_store_dwordx4 a[252:255], v1, s[8:11], s36 offen offset:3072 sc1
v_accvgpr_write_b32 a54, v138
X 222
Mvav 184,152,112
v_accvgpr_write_b32 a55, v139
v_accvgpr_write_b32 a56, v144
v_accvgpr_write_b32 a57, v145
v_accvgpr_write_b32 a58, v146
v_accvgpr_write_b32 a59, v147
v_accvgpr_write_b32 a60, v152
v_accvgpr_write_b32 a61, v153
v_accvgpr_write_b32 a62, v154
v_accvgpr_write_b32 a63, v155
Mvav 184,156,120
s_barrier
K 216,220,221
X 223
A 36,36,224
Mvav 188,156,152
K 217,222,223
A 37,37,224
A 38,38,224
A 39,39,224
X 36
X 37
X 38
K 194,36,37
X 39
A 68,68,228
K 195,38,39
A 69,69,228
A 70,70,228
A 71,71,228
X 68
X 69
X 70
K 202,68,69
X 71
A 100,100,232
K 203,70,71
A 101,101,232
A 102,102,232
A 103,103,232
X 100
X 101
X 102
K 210,100,101
X 103
A 132,132,236
K 211,102,103
A 133,133,236
A 134,134,236
A 135,135,236
X 132
X 133
X 134
K 218,132,133
X 135
s_nop 0
K 219,134,135
Mvav 188,152,144
W 0
Ra 144,242,28672
Ra 148,242,29696
Ra 152,242,30720
Zvav 32,128,192
Ra 156,242,31744
Zvav 64,128,200
v_xor_b32 v242, 0x10000, v242
Zvav 96,128,208
s_and_b32 s80, s14, 1
s_xor_b32 s62, s62, 0x10000
Zvav 128,128,216
s_lshl_b32 s80, s80, 14
s_add_u32 s80, s80, 131072
v_add_u32 v23, 61440, v21
Zvav 132,132,216
v_add_u32 v12, s80, v1
Ra 64,12,8192
Ra 68,12,9216
v_add_u32 v24, 61440, v22
Zvav 100,132,208
Ra 72,12,10240
Ra 76,12,11264
Ra 80,12,12288
v_add_u32 v25, 122880, v21
Zvav 68,132,200
Ra 84,12,13312
Ra 88,12,14336
Ra 92,12,15360
v_add_u32 v26, 122880, v22
Zvav 36,132,192
Ra 96,12,0
Ra 100,12,1024
Ra 104,12,2048
Zvav 40,136,192
Ra 108,12,3072
Ra 112,12,4096
X 172
Zvav 72,136,200
Ra 116,12,5120
Ra 120,12,6144
X 173
Zvav 104,136,208
Ra 124,12,7168
X 174
K 220,172,173
Zvav 136,136,216
X 175
Zvav 140,140,216
X 168
K 221,174,175
Zvav 108,140,208
X 169
X 170
Zvav 76,140,200
s_waitcnt vmcnt(0) lgkmcnt(0)
s_barrier
ds_read_b128 v[248:251], v8
K 212,168,169
Zvav 44,140,192
s_add_u32 s80, s71, 128
v_mov_b64 v[28:29], s[12:13]
s_mov_b64 exec, 1
buffer_store_dwordx2 v[28:29], off, s[8:11], s80 sc1
s_mov_b64 exec, -1
X 171
X 164
Zvav 48,144,192
Ra 128,242,0
K 213,170,171
X 165
Zvav 80,144,200
X 166
Zvav 112,144,208
Ra 132,242,1024
K 204,164,165
X 167
Zvav 144,144,216
X 160
Zvav 148,148,216
K 205,166,167
X 161
Zvav 116,148,208
Ra 136,242,2048
X 162
Zvav 84,148,200
W 3
K 196,160,161
X 163
Zvav 52,148,192
X 176
K 197,162,163
Zvav 56,152,192
Ra 140,242,3072
X 177
Zvav 88,152,200
X 178
K 198,176,177
Zvav 120,152,208
X 179
X 180
Zvav 152,152,216
K 199,178,179
Zvav 156,156,216
X 181
X 182
Zvav 124,156,208
K 206,180,181
X 183
Zvav 92,156,200
K 207,182,183
Zvav 60,156,192
.Ll0t_42%=:
s_cmp_eq_u32 s64, s65
s_cbranch_scc1 .Ll1t_31%=
.Ll0_29%=:
W 0
Cvaa 160,128,0,224
Ra 144,242,4096
Ra 148,242,5120
Mvaa 160,132,4
s_xor_b32 s62, s62, 0x10000
Ra 152,242,6144
Cvaa 164,132,20,228
X 184
Mvaa 164,128,16
X 185
Cvaa 168,128,32,232
X 186
Mvaa 168,132,36
Ra 156,242,7168
K 214,184,185
Cvaa 172,132,52,236
X 187
Mvaa 172,128,48
X 188
Mvaa 160,136,8
Ra 128,242,16384
Mvaa 160,140,12
K 215,186,187
Mvaa 164,140,28
X 189
Mvaa 164,136,24
X 190
Mvaa 168,136,40
Ra 132,242,17408
Mvaa 168,140,44
K 222,188,189
Mvaa 172,140,60
X 191
Mvaa 172,136,56
K 223,190,191
v_smfmac_f32_16x16x64_bf16 a[160:163], a[176:179], v[192:199], v240
Ra 136,242,18432
v_smfmac_f32_16x16x64_bf16 a[164:167], a[176:179], v[200:207], v240
X 160
v_smfmac_f32_16x16x64_bf16 a[168:171], a[176:179], v[208:215], v240
X 161
v_smfmac_f32_16x16x64_bf16 a[172:175], a[176:179], v[216:223], v240
W 3
X 162
Cvaa 176,144,0,224
Ra 140,242,19456
Mvaa 176,148,4
K 192,160,161
Cvaa 180,148,20,228
X 163
Mvaa 180,144,16
X 164
Cvaa 184,144,32,232
Mvaa 184,148,36
K 193,162,163
Cvaa 188,148,52,236
X 165
Mvaa 188,144,48
X 166
Mvaa 176,152,8
Ra 144,242,20480
Ra 148,242,21504
Mvaa 176,156,12
K 200,164,165
Mvaa 180,156,28
X 167
Mvaa 180,152,24
X 168
Mvaa 184,152,40
Mvaa 184,156,44
K 201,166,167
Mvaa 188,156,60
X 169
Mvaa 188,152,56
W 2
Ra 152,242,22528
Mvav 32,128,196
X 170
Mvav 64,128,204
Ra 156,242,23552
K 208,168,169
Mvav 96,128,212
X 171
Mvav 128,128,220
Mvav 132,132,220
X 172
Mvav 100,132,212
s_add_u32 s64, s64, 1
Ra 128,242,8192
K 209,170,171
Mvav 68,132,204
X 173
Mvav 36,132,196
Mvav 40,136,196
X 174
Mvav 72,136,204
Ra 132,242,9216
K 216,172,173
Mvav 104,136,212
X 175
Mvav 136,136,220
Mvav 140,140,220
X 176
Mvav 108,140,212
Ra 136,242,10240
K 217,174,175
Mvav 76,140,204
X 177
Mvav 44,140,196
W 3
Mvav 48,144,196
X 178
Mvav 80,144,204
Ra 140,242,11264
Mvav 112,144,212
K 194,176,177
Mvav 144,144,220
X 179
Mvav 148,148,220
X 180
Mvav 116,148,212
Ra 144,242,12288
Mvav 84,148,204
K 195,178,179
)"
R"(Mvav 52,148,196
X 181
Mvav 56,152,196
X 182
Mvav 88,152,204
Ra 148,242,13312
Mvav 120,152,212
K 202,180,181
Mvav 152,152,220
X 183
Mvav 156,156,220
X 184
Mvav 124,156,212
Ra 152,242,14336
Mvav 92,156,204
K 203,182,183
Mvav 60,156,196
W 3
X 185
Cvaa 160,128,0,224
X 186
Mvaa 160,132,4
Ra 156,242,15360
Cvaa 164,132,20,228
K 210,184,185
Mvaa 164,128,16
X 187
Cvaa 168,128,32,232
X 188
Mvaa 168,132,36
Cvaa 172,132,52,236
K 211,186,187
Mvaa 172,128,48
X 189
Mvaa 160,136,8
Ra 128,242,24576
Ra 132,242,25600
Mvaa 160,140,12
X 190
Mvaa 164,140,28
K 218,188,189
Mvaa 164,136,24
X 191
Mvaa 168,136,40
Mvaa 168,140,44
Mvaa 172,140,60
X 160
Mvaa 172,136,56
W 2
Ra 136,242,26624
K 219,190,191
Cvaa 176,144,0,224
X 161
Mvaa 176,148,4
Ra 140,242,27648
Cvaa 180,148,20,228
X 162
Mvaa 180,144,16
K 196,160,161
Cvaa 184,144,32,232
X 163
Mvaa 184,148,36
X 164
Cvaa 188,148,52,236
K 197,162,163
Mvaa 188,144,48
X 165
Mvaa 176,152,8
Ra 144,242,28672
Ra 148,242,29696
Mvaa 176,156,12
X 166
Mvaa 180,156,28
K 204,164,165
Mvaa 180,152,24
X 167
Mvaa 184,152,40
Mvaa 184,156,44
X 168
Mvaa 188,156,60
Mvaa 188,152,56
W 2
Ra 152,242,30720
K 205,166,167
Mvav 32,128,192
X 169
Mvav 64,128,200
Ra 156,242,31744
v_xor_b32 v242, 0x10000, v242
Mvav 96,128,208
X 170
Mvav 128,128,216
K 212,168,169
Mvav 132,132,216
X 171
Mvav 100,132,208
X 172
Mvav 68,132,200
Mvav 36,132,192
K 213,170,171
Mvav 40,136,192
X 173
Mvav 72,136,200
X 174
Mvav 104,136,208
Mvav 136,136,216
K 220,172,173
Mvav 140,140,216
X 175
Mvav 108,140,208
X 176
Mvav 76,140,200
W 0
s_barrier
Mvav 44,140,192
Ra 128,242,0
K 221,174,175
Mvav 48,144,192
X 177
Mvav 80,144,200
X 178
Mvav 112,144,208
Ra 132,242,1024
Mvav 144,144,216
K 198,176,177
Mvav 148,148,216
X 179
Mvav 116,148,208
X 180
Mvav 84,148,200
Ra 136,242,2048
Mvav 52,148,192
K 199,178,179
Mvav 56,152,192
X 181
Mvav 88,152,200
Ra 140,242,3072
Mvav 120,152,208
X 182
Mvav 152,152,216
K 206,180,181
Mvav 156,156,216
X 183
Mvav 124,156,208
Mvav 92,156,200
K 207,182,183
Mvav 60,156,192
s_cmp_lg_u32 s64, s65
s_cbranch_scc1 .Ll0_29%=
s_cmp_lt_u32 s85, 54
s_cbranch_scc1 .Lmoe_40%=
.Ll1t_31%=:
s_cmp_eq_u32 s64, s66
s_cbranch_scc1 .Lsp1_32%=
.Ll1_30%=:
W 0
Cvaa 160,128,0,224
Ra 144,242,4096
Ra 148,242,5120
Mvaa 160,132,4
s_add_u32 m0, s62, 0
Ra 152,242,6144
Cvaa 164,132,20,228
X 184
Mvaa 164,128,16
X 185
Cvaa 168,128,32,232
X 186
Mvaa 168,132,36
Ra 156,242,7168
K 214,184,185
Cvaa 172,132,52,236
Dc 1,8,61,0
Mvaa 172,128,48
X 187
Mvaa 160,136,8
X 188
Mvaa 160,140,12
Ra 128,242,16384
K 215,186,187
Mvaa 164,140,28
Dc 1,8,61,1024
Mvaa 164,136,24
X 189
Mvaa 168,136,40
X 190
Mvaa 168,140,44
Ra 132,242,17408
K 222,188,189
Mvaa 172,140,60
Dc 1,8,61,2048
Mvaa 172,136,56
X 191
v_smfmac_f32_16x16x64_bf16 a[160:163], a[176:179], v[192:199], v240
Ra 136,242,18432
K 223,190,191
v_smfmac_f32_16x16x64_bf16 a[164:167], a[176:179], v[200:207], v240
X 160
v_smfmac_f32_16x16x64_bf16 a[168:171], a[176:179], v[208:215], v240
Dc 1,8,61,3072
v_smfmac_f32_16x16x64_bf16 a[172:175], a[176:179], v[216:223], v240
W 3
X 161
Cvaa 176,144,0,224
s_add_u32 m0, s62, 4096
X 162
Mvaa 176,148,4
Ra 140,242,19456
K 192,160,161
Cvaa 180,148,20,228
X 163
Mvaa 180,144,16
Dc 243,8,61,0
Cvaa 184,144,32,232
X 164
Mvaa 184,148,36
K 193,162,163
Cvaa 188,148,52,236
X 165
Mvaa 188,144,48
Dc 243,8,61,1024
Ra 144,242,20480
Mvaa 176,152,8
X 166
Mvaa 176,156,12
Ra 148,242,21504
K 200,164,165
Mvaa 180,156,28
X 167
Mvaa 180,152,24
Dc 243,8,61,2048
Mvaa 184,152,40
X 168
Mvaa 184,156,44
K 201,166,167
Mvaa 188,156,60
X 169
Mvaa 188,152,56
W 2
Dc 243,8,61,3072
Ra 152,242,22528
Mvav 32,128,196
s_add_u32 m0, s62, 8192
X 170
Mvav 64,128,204
Ra 156,242,23552
K 208,168,169
Mvav 96,128,212
X 171
Mvav 128,128,220
Dc 244,8,61,0
Mvav 132,132,220
X 172
Mvav 100,132,212
Ra 128,242,8192
K 209,170,171
Mvav 68,132,204
X 173
Mvav 36,132,196
Dc 244,8,61,1024
Mvav 40,136,196
X 174
Mvav 72,136,204
Ra 132,242,9216
K 216,172,173
Mvav 104,136,212
X 175
Mvav 136,136,220
Dc 244,8,61,2048
Mvav 140,140,220
X 176
Mvav 108,140,212
Ra 136,242,10240
K 217,174,175
Mvav 76,140,204
X 177
Mvav 44,140,196
W 3
Dc 244,8,61,3072
Mvav 48,144,196
X 178
Mvav 80,144,204
s_add_u32 m0, s62, 12288
Ra 140,242,11264
Mvav 112,144,212
K 194,176,177
Mvav 144,144,220
X 179
Mvav 148,148,220
X 180
Mvav 116,148,212
Dc 245,8,61,0
Ra 144,242,12288
Mvav 84,148,204
K 195,178,179
Mvav 52,148,196
X 181
Mvav 56,152,196
X 182
Mvav 88,152,204
Dc 245,8,61,1024
Ra 148,242,13312
Mvav 120,152,212
K 202,180,181
Mvav 152,152,220
X 183
Mvav 156,156,220
X 184
Mvav 124,156,212
Dc 245,8,61,2048
Ra 152,242,14336
Mvav 92,156,204
K 203,182,183
Mvav 60,156,196
W 3
X 185
Cvaa 160,128,0,224
X 186
Mvaa 160,132,4
Dc 245,8,61,3072
Ra 156,242,15360
Cvaa 164,132,20,228
s_add_u32 s61, s61, s63
K 210,184,185
Mvaa 164,128,16
X 187
Cvaa 168,128,32,232
X 188
Mvaa 168,132,36
Cvaa 172,132,52,236
s_xor_b32 s62, s62, 0x10000
K 211,186,187
Mvaa 172,128,48
X 189
Mvaa 160,136,8
Ra 128,242,24576
Ra 132,242,25600
Mvaa 160,140,12
s_add_u32 s64, s64, 1
X 190
Mvaa 164,140,28
K 218,188,189
Mvaa 164,136,24
X 191
Mvaa 168,136,40
Mvaa 168,140,44
X 160
Mvaa 172,140,60
K 219,190,191
Mvaa 172,136,56
W 2
X 161
Cvaa 176,144,0,224
Ra 136,242,26624
Ra 140,242,27648
Mvaa 176,148,4
X 162
Cvaa 180,148,20,228
K 196,160,161
Mvaa 180,144,16
X 163
Cvaa 184,144,32,232
Mvaa 184,148,36
X 164
Cvaa 188,148,52,236
K 197,162,163
Mvaa 188,144,48
X 165
Mvaa 176,152,8
Ra 144,242,28672
Ra 148,242,29696
Mvaa 176,156,12
X 166
Mvaa 180,156,28
K 204,164,165
Mvaa 180,152,24
X 167
Mvaa 184,152,40
Mvaa 184,156,44
X 168
Mvaa 188,156,60
Mvaa 188,152,56
W 2
Ra 152,242,30720
K 205,166,167
Mvav 32,128,192
X 169
Mvav 64,128,200
Ra 156,242,31744
v_xor_b32 v242, 0x10000, v242
Mvav 96,128,208
X 170
Mvav 128,128,216
K 212,168,169
Mvav 132,132,216
X 171
Mvav 100,132,208
X 172
Mvav 68,132,200
Mvav 36,132,192
)"
R"(K 213,170,171
Mvav 40,136,192
X 173
Mvav 72,136,200
X 174
Mvav 104,136,208
Mvav 136,136,216
K 220,172,173
Mvav 140,140,216
X 175
Mvav 108,140,208
X 176
Mvav 76,140,200
s_waitcnt vmcnt(0) lgkmcnt(0)
s_barrier
Mvav 44,140,192
Ra 128,242,0
K 221,174,175
Mvav 48,144,192
X 177
Mvav 80,144,200
X 178
Mvav 112,144,208
Ra 132,242,1024
Mvav 144,144,216
K 198,176,177
Mvav 148,148,216
X 179
Mvav 116,148,208
X 180
Mvav 84,148,200
Ra 136,242,2048
Mvav 52,148,192
K 199,178,179
Mvav 56,152,192
X 181
Mvav 88,152,200
Ra 140,242,3072
Mvav 120,152,208
X 182
Mvav 152,152,216
K 206,180,181
Mvav 156,156,216
X 183
Mvav 124,156,208
Mvav 92,156,200
K 207,182,183
Mvav 60,156,192
s_cmp_lg_u32 s64, s66
s_cbranch_scc1 .Ll1_30%=
s_cmp_ge_u32 s14, 2
s_cbranch_scc1 .Lf1r_43%=
.Lsp1_32%=:
W 0
Cvaa 160,128,0,224
Ra 144,242,4096
Ra 148,242,5120
Mvaa 160,132,4
s_add_u32 m0, s62, 0
Ra 152,242,6144
Cvaa 164,132,20,228
X 184
Mvaa 164,128,16
X 185
Cvaa 168,128,32,232
X 186
Mvaa 168,132,36
Ra 156,242,7168
K 214,184,185
Cvaa 172,132,52,236
Dc 1,8,61,0
Mvaa 172,128,48
X 187
Mvaa 160,136,8
X 188
Mvaa 160,140,12
Ra 128,242,16384
K 215,186,187
Mvaa 164,140,28
Dc 1,8,61,1024
Ra 132,242,17408
Mvaa 164,136,24
X 189
Mvaa 168,136,40
X 190
Mvaa 168,140,44
Dc 1,8,61,2048
K 222,188,189
Mvaa 172,140,60
X 191
Mvaa 172,136,56
Ra 136,242,18432
K 223,190,191
v_smfmac_f32_16x16x64_bf16 a[160:163], a[176:179], v[192:199], v240
Dc 1,8,61,3072
Ra 140,242,19456
v_smfmac_f32_16x16x64_bf16 a[164:167], a[176:179], v[200:207], v240
s_add_u32 m0, s62, 4096
v_cndmask_b32_e64 v160, v241, v160, s[72:73]
v_cndmask_b32_e64 v161, v241, v161, s[74:75]
v_smfmac_f32_16x16x64_bf16 a[168:171], a[176:179], v[208:215], v240
v_cndmask_b32_e64 v162, v241, v162, s[76:77]
v_cndmask_b32_e64 v163, v241, v163, s[78:79]
v_smfmac_f32_16x16x64_bf16 a[172:175], a[176:179], v[216:223], v240
W 4
X 160
Cvaa 180,144,16,228
X 161
Mvaa 180,148,20
Dc 243,8,61,0
Cvaa 184,148,36,232
X 162
Mvaa 184,144,32
K 192,160,161
Cvaa 188,144,48,236
X 163
Mvaa 188,148,52
Dc 243,8,61,1024
Ra 144,242,20480
Mvaa 180,152,24
X 164
Mvaa 180,156,28
Ra 148,242,21504
K 193,162,163
Mvaa 184,156,44
Dc 243,8,61,2048
Mvaa 184,152,40
X 165
Mvaa 188,152,56
X 166
Mvaa 188,156,60
W 2
Dc 243,8,61,3072
K 200,164,165
Mvav 32,128,196
s_add_u32 m0, s62, 8192
X 167
Mvav 64,128,204
Ra 152,242,22528
Ra 156,242,23552
Mvav 96,128,212
X 168
Mvav 128,128,220
Dc 244,8,61,0
K 201,166,167
Mvav 132,132,220
X 169
Mvav 100,132,212
X 170
Mvav 68,132,204
Ra 128,242,8192
K 208,168,169
Mvav 36,132,196
X 171
Mvav 40,136,196
Dc 244,8,61,1024
Ra 132,242,9216
Mvav 72,136,204
X 172
Mvav 104,136,212
K 209,170,171
Mvav 136,136,220
X 173
Mvav 140,140,220
Dc 244,8,61,2048
Ra 136,242,10240
Mvav 108,140,212
X 174
Mvav 76,140,204
K 216,172,173
Mvav 44,140,196
W 3
Dc 244,8,61,3072
Ra 140,242,11264
Mvav 48,144,196
s_add_u32 m0, s62, 12288
X 175
Mvav 80,144,204
v_mov_b32 v194, 0
K 217,174,175
Mvav 112,144,212
v_mov_b32 v195, 0
v_cndmask_b32_e64 v180, v241, v180, s[72:73]
Mvav 144,144,220
v_cndmask_b32_e64 v181, v241, v181, s[74:75]
v_cndmask_b32_e64 v182, v241, v182, s[76:77]
Mvav 148,148,220
Dc 245,8,61,0
v_cndmask_b32_e64 v183, v241, v183, s[78:79]
Mvav 116,148,212
X 180
Mvav 84,148,204
Ra 144,242,12288
Mvav 52,148,196
X 181
Mvav 56,152,196
Dc 245,8,61,1024
Ra 148,242,13312
Mvav 88,152,204
X 182
Mvav 120,152,212
K 202,180,181
Mvav 152,152,220
X 183
Mvav 156,156,220
Dc 245,8,61,2048
Ra 152,242,14336
Mvav 124,156,212
X 184
Mvav 92,156,204
K 203,182,183
Mvav 60,156,196
W 3
Dc 245,8,61,3072
Ra 156,242,15360
Cvaa 168,128,32,232
s_add_u32 s61, s61, s63
X 185
Mvaa 168,132,36
s_xor_b32 s62, s62, 0x10000
X 186
Cvaa 172,132,52,236
K 210,184,185
Mvaa 172,128,48
s_add_u32 s64, s64, 1
X 187
Mvaa 168,136,40
X 188
Mvaa 168,140,44
Ra 128,242,24576
K 211,186,187
Mvaa 172,140,60
Ra 132,242,25600
Mvaa 172,136,56
Ra 136,242,26624
Ra 140,242,27648
X 189
X 190
K 218,188,189
X 191
v_mov_b32 v196, 0
K 219,190,191
W 4
Cvaa 188,144,48,236
Ra 144,242,28672
Mvaa 188,148,52
v_mov_b32 v197, 0
v_mov_b32 v204, 0
Mvaa 188,152,56
Ra 148,242,29696
v_mov_b32 v205, 0
Mvaa 188,156,60
W 2
Ra 152,242,30720
v_cndmask_b32_e64 v168, v241, v168, s[72:73]
Mvav 32,128,192
v_cndmask_b32_e64 v169, v241, v169, s[74:75]
v_cndmask_b32_e64 v170, v241, v170, s[76:77]
Mvav 64,128,200
Ra 156,242,31744
v_cndmask_b32_e64 v171, v241, v171, s[78:79]
Mvav 96,128,208
v_xor_b32 v242, 0x10000, v242
Mvav 128,128,216
X 168
Mvav 132,132,216
X 169
Mvav 100,132,208
X 170
Mvav 68,132,200
v_mov_b32 v224, v250
K 212,168,169
Mvav 36,132,192
v_mov_b32 v225, v250
v_mov_b32 v226, v250
Mvav 40,136,192
X 171
Mvav 72,136,200
X 172
Mvav 104,136,208
v_mov_b32 v227, v250
K 213,170,171
Mvav 136,136,216
X 173
Mvav 140,140,216
v_mov_b32 v228, v251
v_mov_b32 v229, v251
Mvav 108,140,208
X 174
Mvav 76,140,200
K 220,172,173
v_mov_b32 v230, v251
Mvav 44,140,192
W 0
X 175
Mvav 48,144,192
v_mov_b32 v231, v251
v_mov_b32 v198, 0
Mvav 80,144,200
K 221,174,175
v_mov_b32 v232, v248
Mvav 112,144,208
v_mov_b32 v199, 0
v_mov_b32 v206, 0
Mvav 144,144,216
v_mov_b32 v233, v248
v_mov_b32 v207, 0
Mvav 148,148,216
v_mov_b32 v214, 0
v_mov_b32 v234, v248
Mvav 116,148,208
v_mov_b32 v215, 0
v_cndmask_b32_e64 v188, v241, v188, s[72:73]
Mvav 84,148,200
v_mov_b32 v235, v248
v_cndmask_b32_e64 v189, v241, v189, s[74:75]
Mvav 52,148,192
v_cndmask_b32_e64 v190, v241, v190, s[76:77]
v_mov_b32 v236, v249
Mvav 56,152,192
v_cndmask_b32_e64 v191, v241, v191, s[78:79]
v_mov_b32 v237, v249
Mvav 88,152,200
X 188
Mvav 120,152,208
X 189
Mvav 152,152,216
v_mov_b32 v238, v249
v_mov_b32 v239, v249
Mvav 156,156,216
X 190
Mvav 124,156,208
s_waitcnt vmcnt(0)
s_barrier
Mvav 92,156,200
Ra 128,242,40960
K 222,188,189
Mvav 60,156,192
Ra 132,242,41984
Ra 136,242,43008
Ra 140,242,44032
X 191
s_nop 0
K 223,190,191
s_cmp_eq_u32 s64, 15
s_cbranch_scc1 .Lfs_38%=
.Lsp2_33%=:
v_smfmac_f32_16x16x64_bf16 a[160:163], a[176:179], v[192:199], v240
s_add_u32 m0, s62, 0
Ra 144,242,45056
Ra 148,242,46080
Ra 152,242,47104
Ra 156,242,48128
v_smfmac_f32_16x16x64_bf16 a[164:167], a[176:179], v[200:207], v240
v_smfmac_f32_16x16x64_bf16 a[168:171], a[176:179], v[208:215], v240
v_smfmac_f32_16x16x64_bf16 a[172:175], a[176:179], v[216:223], v240
W 4
Dc 1,8,61,0
Cvaa 168,128,96,232
v_mov_b32 v192, 0
G 12,160
Mvaa 168,132,100
Dc 1,8,61,1024
G 4,161
A 12,12,4
v_cmp_nlt_f32_e64 s[86:87], v12, s90
v_mov_b32 v193, 0
Cvaa 172,132,116,236
v_rcp_f32 v12, v12
s_or_b64 s[88:89], s[88:89], s[86:87]
v_accvgpr_write_b32 a160, 0
v_mov_b32 v200, 0
v_accvgpr_write_b32 a161, 0
v_accvgpr_write_b32 a162, 0
Mvaa 172,128,112
Dc 1,8,61,2048
v_accvgpr_write_b32 a163, 0
G 13,164
v_mov_b32 v201, 0
G 5,165
A 13,13,5
Mvaa 168,136,104
Ra 128,242,16384
Ra 132,242,17408
v_cmp_nlt_f32_e64 s[86:87], v13, s90
v_rcp_f32 v13, v13
s_or_b64 s[88:89], s[88:89], s[86:87]
v_accvgpr_write_b32 a164, 0
Mvaa 168,140,108
Dc 1,8,61,3072
v_accvgpr_write_b32 a165, 0
v_accvgpr_write_b32 a166, 0
v_accvgpr_write_b32 a167, 0
U 32,32,12
U 33,33,12
Mvaa 172,140,124
U 34,34,12
U 35,35,12
U 36,36,12
U 37,37,12
)"
R"(U 38,38,12
U 39,39,12
Mvaa 172,136,120
s_add_u32 m0, s62, 4096
Ra 136,242,18432
Ra 140,242,19456
K 32,32,33
K 33,34,35
K 34,36,37
W 4
Cvaa 188,144,112,236
K 35,38,39
s_add_u32 s93, s67, 0
buffer_store_dwordx4 v[32:35], v1, s[8:11], s93 offen offset:0 sc1
U 40,40,12
U 41,41,12
U 42,42,12
U 43,43,12
Mvaa 188,148,116
Ra 144,242,20480
v_cndmask_b32_e64 v168, v241, v168, s[72:73]
v_cndmask_b32_e64 v169, v241, v169, s[74:75]
v_cndmask_b32_e64 v170, v241, v170, s[76:77]
v_cndmask_b32_e64 v171, v241, v171, s[78:79]
U 44,44,12
Mvaa 188,152,120
X 168
X 169
U 45,45,12
U 46,46,12
Mvaa 188,156,124
W 1
Ra 148,242,21504
Dc 243,8,61,0
U 47,47,12
K 40,40,41
K 41,42,43
K 42,44,45
Mvav 96,128,212
Ra 152,242,22528
Ra 156,242,23552
K 43,46,47
X 170
K 208,168,169
Mvav 128,128,220
buffer_store_dwordx4 v[40:43], v1, s[8:11], s93 offen offset:1024 sc1
U 48,48,12
U 49,49,12
U 50,50,12
U 51,51,12
U 52,52,12
Mvav 132,132,220
U 53,53,12
U 54,54,12
U 55,55,12
K 48,48,49
K 49,50,51
K 50,52,53
Mvav 100,132,212
Dc 243,8,61,1024
Ra 128,242,32768
Ra 132,242,33792
X 171
K 51,54,55
Mvav 104,136,212
X 172
K 209,170,171
X 173
Mvav 136,136,220
buffer_store_dwordx4 v[48:51], v1, s[8:11], s93 offen offset:2048 sc1
U 56,56,12
U 57,57,12
U 58,58,12
U 59,59,12
U 60,60,12
Mvav 140,140,220
U 61,61,12
U 62,62,12
U 63,63,12
K 56,56,57
K 57,58,59
K 58,60,61
Mvav 108,140,212
W 2
Dc 243,8,61,2048
Ra 136,242,34816
Ra 140,242,35840
K 59,62,63
X 174
Mvav 112,144,212
K 216,172,173
X 175
Mvav 144,144,220
buffer_store_dwordx4 v[56:59], v1, s[8:11], s93 offen offset:3072 sc1
Ra 144,242,36864
G 14,168
G 6,169
A 14,14,6
v_cmp_nlt_f32_e64 s[86:87], v14, s90
Mvav 148,148,220
v_rcp_f32 v14, v14
s_or_b64 s[88:89], s[88:89], s[86:87]
v_accvgpr_write_b32 a168, 0
v_accvgpr_write_b32 a169, 0
v_accvgpr_write_b32 a170, 0
v_accvgpr_write_b32 a171, 0
Mvav 116,148,212
Dc 243,8,61,3072
s_add_u32 m0, s62, 8192
G 15,172
G 7,173
A 15,15,7
v_cmp_nlt_f32_e64 s[86:87], v15, s90
v_mov_b32 v194, 0
Mvav 120,152,212
v_rcp_f32 v15, v15
s_or_b64 s[88:89], s[88:89], s[86:87]
v_accvgpr_write_b32 a172, 0
v_accvgpr_write_b32 a173, 0
v_accvgpr_write_b32 a174, 0
v_accvgpr_write_b32 a175, 0
Mvav 152,152,220
Dc 244,8,61,0
U 64,64,13
U 65,65,13
U 66,66,13
U 67,67,13
U 68,68,13
Mvav 156,156,220
U 69,69,13
U 70,70,13
U 71,71,13
K 64,64,65
K 65,66,67
K 66,68,69
Mvav 124,156,212
W 1
Dc 244,8,61,1024
Ra 148,242,37888
K 67,70,71
s_add_u32 s93, s67, 131072
K 217,174,175
v_mov_b32 v195, 0
Cvaa 160,128,64,224
Ra 152,242,38912
Ra 156,242,39936
v_mov_b32 v202, 0
v_mov_b32 v203, 0
v_mov_b32 v210, 0
v_mov_b32 v211, 0
Mvaa 160,132,68
buffer_store_dwordx4 v[64:67], v1, s[8:11], s93 offen offset:0 sc1
U 72,72,13
U 73,73,13
U 74,74,13
U 75,75,13
U 76,76,13
Cvaa 164,132,84,228
U 77,77,13
U 78,78,13
U 79,79,13
K 72,72,73
K 73,74,75
K 74,76,77
Mvaa 164,128,80
Dc 244,8,61,2048
K 75,78,79
v_cndmask_b32_e64 v188, v241, v188, s[72:73]
v_cndmask_b32_e64 v189, v241, v189, s[74:75]
v_cndmask_b32_e64 v190, v241, v190, s[76:77]
v_cndmask_b32_e64 v191, v241, v191, s[78:79]
Cvaa 168,128,96,232
X 188
X 189
Mvaa 168,132,100
buffer_store_dwordx4 v[72:75], v1, s[8:11], s93 offen offset:1024 sc1
U 80,80,13
U 81,81,13
U 82,82,13
U 83,83,13
U 84,84,13
Cvaa 172,132,116,236
U 85,85,13
U 86,86,13
U 87,87,13
K 80,80,81
K 81,82,83
K 82,84,85
Mvaa 172,128,112
Dc 244,8,61,3072
s_add_u32 m0, s62, 12288
Ra 128,242,49152
Ra 132,242,50176
K 83,86,87
X 190
Mvaa 160,136,72
K 218,188,189
X 191
Mvaa 160,140,76
buffer_store_dwordx4 v[80:83], v1, s[8:11], s93 offen offset:2048 sc1
U 88,88,13
U 89,89,13
U 90,90,13
U 91,91,13
U 92,92,13
Mvaa 164,140,92
U 93,93,13
U 94,94,13
U 95,95,13
K 88,88,89
K 89,90,91
K 90,92,93
Mvaa 164,136,88
K 91,94,95
buffer_store_dwordx4 v[88:91], v1, s[8:11], s93 offen offset:3072 sc1
U 96,96,14
U 97,97,14
U 98,98,14
U 99,99,14
Mvaa 168,136,104
U 100,100,14
U 101,101,14
U 102,102,14
U 103,103,14
K 96,96,97
K 97,98,99
Mvaa 168,140,108
Dc 245,8,61,0
K 98,100,101
K 99,102,103
s_add_u32 s93, s67, 262144
v_cndmask_b32_e64 v160, v241, v160, s[72:73]
K 219,190,191
v_cndmask_b32_e64 v161, v241, v161, s[74:75]
Mvaa 172,140,124
v_cndmask_b32_e64 v162, v241, v162, s[76:77]
v_cndmask_b32_e64 v163, v241, v163, s[78:79]
X 160
Mvaa 172,136,120
W 2
Dc 245,8,61,1024
Ra 136,242,51200
Ra 140,242,52224
X 161
Cvaa 180,144,80,228
Mvaa 180,148,84
buffer_store_dwordx4 v[96:99], v1, s[8:11], s93 offen offset:0 sc1
U 104,104,14
U 105,105,14
U 106,106,14
U 107,107,14
U 108,108,14
Cvaa 184,148,100,232
U 109,109,14
U 110,110,14
U 111,111,14
K 104,104,105
K 105,106,107
K 106,108,109
Mvaa 184,144,96
Dc 245,8,61,2048
K 107,110,111
X 162
K 196,160,161
Cvaa 188,144,112,236
Ra 144,242,53248
X 163
Mvaa 188,148,116
buffer_store_dwordx4 v[104:107], v1, s[8:11], s93 offen offset:1024 sc1
U 112,112,14
U 113,113,14
U 114,114,14
U 115,115,14
U 116,116,14
Mvaa 180,152,88
U 117,117,14
U 118,118,14
U 119,119,14
K 112,112,113
K 113,114,115
K 114,116,117
Mvaa 180,156,92
Dc 245,8,61,3072
s_add_u32 s61, s61, s63
s_xor_b32 s62, s62, 0x10000
s_add_u32 s64, s64, 1
K 115,118,119
X 164
Mvaa 184,156,108
Ra 148,242,54272
K 197,162,163
X 165
Mvaa 184,152,104
buffer_store_dwordx4 v[112:115], v1, s[8:11], s93 offen offset:2048 sc1
U 120,120,14
U 121,121,14
U 122,122,14
U 123,123,14
U 124,124,14
Mvaa 188,152,120
U 125,125,14
U 126,126,14
U 127,127,14
K 120,120,121
K 121,122,123
K 122,124,125
Mvaa 188,156,124
W 2
K 123,126,127
buffer_store_dwordx4 v[120:123], v1, s[8:11], s93 offen offset:3072 sc1
U 128,128,15
U 129,129,15
U 130,130,15
U 131,131,15
Zvav 32,128,192
U 132,132,15
U 133,133,15
U 134,134,15
U 135,135,15
K 128,128,129
K 129,130,131
Zvav 64,128,200
K 130,132,133
K 131,134,135
s_add_u32 s93, s67, 393216
buffer_store_dwordx4 v[128:131], v1, s[8:11], s93 offen offset:0 sc1
U 136,136,15
U 137,137,15
U 138,138,15
Zvav 96,128,208
U 139,139,15
U 140,140,15
U 141,141,15
U 142,142,15
U 143,143,15
K 136,136,137
Zvav 128,128,216
K 137,138,139
K 138,140,141
K 139,142,143
buffer_store_dwordx4 v[136:139], v1, s[8:11], s93 offen offset:1024 sc1
U 144,144,15
U 145,145,15
Zvav 132,132,216
U 146,146,15
U 147,147,15
U 148,148,15
U 149,149,15
U 150,150,15
U 151,151,15
Zvav 100,132,208
K 144,144,145
K 145,146,147
K 146,148,149
K 147,150,151
buffer_store_dwordx4 v[144:147], v1, s[8:11], s93 offen offset:2048 sc1
U 152,152,15
Zvav 68,132,200
U 153,153,15
U 154,154,15
U 155,155,15
U 156,156,15
U 157,157,15
U 158,158,15
Zvav 36,132,192
U 159,159,15
K 152,152,153
K 153,154,155
K 154,156,157
K 155,158,159
buffer_store_dwordx4 v[152:155], v1, s[8:11], s93 offen offset:3072 sc1
Zvav 40,136,192
Ra 152,242,55296
X 166
K 204,164,165
X 167
Zvav 72,136,200
Ra 156,242,56320
X 168
v_xor_b32 v242, 0x10000, v242
K 205,166,167
Zvav 104,136,208
X 169
X 170
K 212,168,169
Zvav 136,136,216
X 171
X 172
K 213,170,171
Zvav 140,140,216
X 173
Zvav 108,140,208
X 174
Zvav 76,140,200
s_waitcnt vmcnt(6) lgkmcnt(0)
s_barrier
Zvav 44,140,192
Ra 128,242,40960
K 220,172,173
X 175
)"
R"(Zvav 48,144,192
Zvav 80,144,200
v_mov_b32 v198, 0
Zvav 112,144,208
Ra 132,242,41984
K 221,174,175
Zvav 144,144,216
v_mov_b32 v199, 0
v_cndmask_b32_e64 v180, v241, v180, s[72:73]
Zvav 148,148,216
v_cndmask_b32_e64 v181, v241, v181, s[74:75]
Zvav 116,148,208
Ra 136,242,43008
v_cndmask_b32_e64 v182, v241, v182, s[76:77]
Zvav 84,148,200
v_cndmask_b32_e64 v183, v241, v183, s[78:79]
Zvav 52,148,192
X 180
Zvav 56,152,192
Ra 140,242,44032
X 181
Zvav 88,152,200
Zvav 120,152,208
X 182
Zvav 152,152,216
K 206,180,181
Zvav 156,156,216
X 183
Zvav 124,156,208
Zvav 92,156,200
s_waitcnt vmcnt(0)
s_and_b64 s[86:87], s[88:89], s[24:25]
s_cbranch_scc1 .LL_45%=
v_mov_b64 v[28:29], s[12:13]
s_mov_b64 exec, 1
buffer_store_dwordx2 v[28:29], off, s[8:11], s69 sc1
s_mov_b64 exec, -1
s_mov_b32 s70, 134217728
.LL_45%=:
K 207,182,183
Zvav 60,156,192
s_cmp_eq_u32 s64, 15
s_cbranch_scc1 .Lf2_37%=
.Ll2_34%=:
W 0
Cvaa 160,128,64,224
Ra 144,242,45056
Ra 148,242,46080
Mvaa 160,132,68
s_add_u32 m0, s62, 8192
Ra 152,242,47104
Cvaa 164,132,84,228
X 184
Mvaa 164,128,80
X 185
Cvaa 168,128,96,232
X 186
Mvaa 168,132,100
Ra 156,242,48128
K 214,184,185
Cvaa 172,132,116,236
Dc 244,8,61,0
Mvaa 172,128,112
X 187
Mvaa 160,136,72
X 188
Mvaa 160,140,76
Ra 128,242,57344
K 215,186,187
Mvaa 164,140,92
Dc 244,8,61,1024
Mvaa 164,136,88
X 189
Mvaa 168,136,104
X 190
Mvaa 168,140,108
Ra 132,242,58368
K 222,188,189
Mvaa 172,140,124
Dc 244,8,61,2048
Mvaa 172,136,120
X 191
v_smfmac_f32_16x16x64_bf16 a[160:163], a[176:179], v[192:199], v240
Ra 136,242,59392
K 223,190,191
v_smfmac_f32_16x16x64_bf16 a[164:167], a[176:179], v[200:207], v240
X 160
v_smfmac_f32_16x16x64_bf16 a[168:171], a[176:179], v[208:215], v240
Dc 244,8,61,3072
v_smfmac_f32_16x16x64_bf16 a[172:175], a[176:179], v[216:223], v240
W 3
X 161
Cvaa 176,144,64,224
s_add_u32 m0, s62, 12288
X 162
Mvaa 176,148,68
Ra 140,242,60416
K 192,160,161
Cvaa 180,148,84,228
X 163
Mvaa 180,144,80
Dc 245,8,61,0
Cvaa 184,144,96,232
X 164
Mvaa 184,148,100
K 193,162,163
Cvaa 188,148,116,236
X 165
Mvaa 188,144,112
Dc 245,8,61,1024
Ra 144,242,61440
Mvaa 176,152,72
X 166
Mvaa 176,156,76
Ra 148,242,62464
K 200,164,165
Mvaa 180,156,92
X 167
Mvaa 180,152,88
Dc 245,8,61,2048
Mvaa 184,152,104
X 168
Mvaa 184,156,108
K 201,166,167
Mvaa 188,156,124
X 169
Mvaa 188,152,120
W 2
Dc 245,8,61,3072
Ra 152,242,63488
Mvav 32,128,196
s_add_u32 m0, s62, 0
X 170
Mvav 64,128,204
Ra 156,242,64512
K 208,168,169
Mvav 96,128,212
X 171
Mvav 128,128,220
Dc 1,8,61,0
Mvav 132,132,220
X 172
Mvav 100,132,212
Ra 128,242,32768
K 209,170,171
Mvav 68,132,204
X 173
Mvav 36,132,196
Dc 1,8,61,1024
Mvav 40,136,196
X 174
Mvav 72,136,204
Ra 132,242,33792
K 216,172,173
Mvav 104,136,212
X 175
Mvav 136,136,220
Dc 1,8,61,2048
Mvav 140,140,220
X 176
Mvav 108,140,212
Ra 136,242,34816
K 217,174,175
Mvav 76,140,204
X 177
Mvav 44,140,196
W 3
Dc 1,8,61,3072
Mvav 48,144,196
X 178
Mvav 80,144,204
s_add_u32 m0, s62, 4096
Ra 140,242,35840
Mvav 112,144,212
K 194,176,177
Mvav 144,144,220
X 179
Mvav 148,148,220
X 180
Mvav 116,148,212
Dc 243,8,61,0
Ra 144,242,36864
Mvav 84,148,204
K 195,178,179
Mvav 52,148,196
X 181
Mvav 56,152,196
X 182
Mvav 88,152,204
Dc 243,8,61,1024
Ra 148,242,37888
Mvav 120,152,212
K 202,180,181
Mvav 152,152,220
X 183
Mvav 156,156,220
X 184
Mvav 124,156,212
Dc 243,8,61,2048
Ra 152,242,38912
Mvav 92,156,204
K 203,182,183
Mvav 60,156,196
W 3
X 185
Cvaa 160,128,64,224
X 186
Mvaa 160,132,68
Dc 243,8,61,3072
Ra 156,242,39936
Cvaa 164,132,84,228
s_add_u32 s61, s61, s63
K 210,184,185
Mvaa 164,128,80
X 187
Cvaa 168,128,96,232
X 188
Mvaa 168,132,100
Cvaa 172,132,116,236
s_xor_b32 s62, s62, 0x10000
K 211,186,187
Mvaa 172,128,112
X 189
Mvaa 160,136,72
Ra 128,242,49152
Ra 132,242,50176
Mvaa 160,140,76
s_add_u32 s64, s64, 1
X 190
Mvaa 164,140,92
K 218,188,189
Mvaa 164,136,88
X 191
Mvaa 168,136,104
Mvaa 168,140,108
X 160
Mvaa 172,140,124
K 219,190,191
Mvaa 172,136,120
W 2
X 161
Cvaa 176,144,64,224
Ra 136,242,51200
Ra 140,242,52224
Mvaa 176,148,68
X 162
Cvaa 180,148,84,228
K 196,160,161
Mvaa 180,144,80
X 163
Cvaa 184,144,96,232
Mvaa 184,148,100
X 164
Cvaa 188,148,116,236
K 197,162,163
Mvaa 188,144,112
X 165
Mvaa 176,152,72
Ra 144,242,53248
Ra 148,242,54272
Mvaa 176,156,76
X 166
Mvaa 180,156,92
K 204,164,165
Mvaa 180,152,88
X 167
Mvaa 184,152,104
Mvaa 184,156,108
X 168
Mvaa 188,156,124
Mvaa 188,152,120
W 2
Ra 152,242,55296
K 205,166,167
Mvav 32,128,192
X 169
Mvav 64,128,200
Ra 156,242,56320
v_xor_b32 v242, 0x10000, v242
Mvav 96,128,208
X 170
Mvav 128,128,216
K 212,168,169
Mvav 132,132,216
X 171
Mvav 100,132,208
X 172
Mvav 68,132,200
Mvav 36,132,192
K 213,170,171
Mvav 40,136,192
X 173
Mvav 72,136,200
X 174
Mvav 104,136,208
Mvav 136,136,216
K 220,172,173
Mvav 140,140,216
X 175
Mvav 108,140,208
X 176
Mvav 76,140,200
s_waitcnt vmcnt(0) lgkmcnt(0)
s_barrier
Mvav 44,140,192
Ra 128,242,40960
K 221,174,175
Mvav 48,144,192
X 177
Mvav 80,144,200
X 178
Mvav 112,144,208
Ra 132,242,41984
Mvav 144,144,216
K 198,176,177
Mvav 148,148,216
X 179
Mvav 116,148,208
X 180
Mvav 84,148,200
Ra 136,242,43008
Mvav 52,148,192
K 199,178,179
Mvav 56,152,192
X 181
Mvav 88,152,200
Ra 140,242,44032
Mvav 120,152,208
X 182
Mvav 152,152,216
K 206,180,181
Mvav 156,156,216
X 183
Mvav 124,156,208
Mvav 92,156,200
K 207,182,183
Mvav 60,156,192
s_cmp_lg_u32 s64, 15
s_cbranch_scc1 .Ll2_34%=
.Lf2_37%=:
W 0
Cvaa 160,128,64,224
.Lh_4_2%=:
s_cbranch_execz .Lh_4_2_1%=
v_bfe_u32 v28, v1, 4, 2
v_lshlrev_b32 v28, 3, v28
Mvaa 160,132,68
Ra 144,242,45056
Ra 148,242,46080
Cvaa 164,132,84,228
Ra 152,242,47104
buffer_load_dwordx2 v[28:29], v28, s[8:11], s54 offen sc1
Mvaa 164,128,80
s_lshl_b32 s92, s58, 2
s_add_u32 s91, s92, s51
s_add_u32 s95, s91, 3
Cvaa 168,128,96,232
s_and_b32 s95, s95, 15
s_mul_i32 s95, s95, 40960
s_mul_i32 s71, s51, 10240
Mvaa 168,132,100
s_add_u32 s71, s71, 0
s_add_u32 m0, s71, s48
s_lshl2_add_u32 s94, s48, s95
Cvaa 172,132,116,236
X 184
Mvaa 172,128,112
X 185
Mvaa 160,136,72
X 186
Mvaa 160,140,76
Ra 156,242,48128
K 214,184,185
Mvaa 164,140,92
X 187
Mvaa 164,136,88
D 16,40,94,0
Ra 128,242,57344
Mvaa 168,136,104
s_add_u32 s95, s91, 5
s_and_b32 s95, s95, 15
s_mul_i32 s95, s95, 40960
Mvaa 168,140,108
s_mul_i32 s71, s51, 10240
s_add_u32 s71, s71, 20480
Ra 132,242,58368
Mvaa 172,140,124
X 188
Mvaa 172,136,120
D 17,40,94,1024
K 215,186,187
v_smfmac_f32_16x16x64_bf16 a[160:163], a[176:179], v[192:199], v240
X 189
v_smfmac_f32_16x16x64_bf16 a[164:167], a[176:179], v[200:207], v240
X 190
K 222,188,189
X 191
s_nop 0
K 223,190,191
v_smfmac_f32_16x16x64_bf16 a[168:171], a[176:179], v[208:215], v240
)"
R"(D 18,40,94,2048
Ra 136,242,59392
v_smfmac_f32_16x16x64_bf16 a[172:175], a[176:179], v[216:223], v240
W 3
X 160
Cvaa 176,144,64,224
X 161
Mvaa 176,148,68
X 162
Cvaa 180,148,84,228
D 19,40,94,3072
K 192,160,161
Mvaa 180,144,80
s_add_u32 m0, m0, 4096
X 163
Cvaa 184,144,96,232
X 164
Mvaa 184,148,100
Ra 140,242,60416
K 193,162,163
Cvaa 188,148,116,236
buffer_load_dwordx4 v20, s[40:43], s94 offen lds
Mvaa 188,144,112
s_add_u32 m0, s71, s48
s_lshl2_add_u32 s94, s48, s95
Ra 144,242,61440
Mvaa 176,152,72
X 165
Mvaa 176,156,76
X 166
Mvaa 180,156,92
D 16,40,94,0
K 200,164,165
Mvaa 180,152,88
X 167
Mvaa 184,152,104
X 172
Mvaa 184,156,108
D 17,40,94,1024
K 201,166,167
Mvaa 188,156,124
X 173
Mvaa 188,152,120
W 1
Ra 148,242,62464
Ra 152,242,63488
Mvav 32,128,196
X 174
Mvav 64,128,204
D 18,40,94,2048
K 216,172,173
Mvav 96,128,212
X 175
Mvav 128,128,220
X 168
Mvav 132,132,220
s_mov_b32 s18, 40960
D 19,40,94,3072
K 217,174,175
Mvav 100,132,212
s_add_u32 m0, m0, 4096
X 169
Mvav 68,132,204
s_mov_b32 s19, 73728
X 170
Mvav 36,132,196
s_bitcmp1_b32 s14, 0
s_cselect_b32 s18, 49152, s18
s_cselect_b32 s19, 98304, s19
.Lh_4_3%=:
s_cbranch_execz .Lh_4_3_1%=
Ra 156,242,64512
K 208,168,169
Mvav 40,136,196
s_cmp_lt_u32 s14, 2
s_cselect_b32 s20, s18, s19
s_cselect_b32 s21, s19, s18
X 171
Mvav 72,136,204
X 184
Mvav 104,136,212
buffer_load_dwordx4 v20, s[40:43], s94 offen lds
K 209,170,171
Mvav 136,136,220
s_waitcnt vmcnt(10)
v_cmp_eq_u64 vcc, s[12:13], v[28:29]
s_cmp_eq_u64 vcc, -1
s_cselect_b32 s46, 79691776, 0
s_add_u32 s81, s58, 0
s_and_b32 s81, s81, 3
Mvav 140,140,220
s_lshl_b32 s81, s81, 12
s_add_u32 s81, s81, s49
Ra 128,242,49152
Mvav 108,140,212
X 185
Mvav 76,140,204
X 186
Mvav 44,140,196
W 1
buffer_load_dwordx4 a[184:187], v1, s[44:47], s81 offen offset:0 sc1
K 210,184,185
Mvav 48,144,196
s_add_u32 s82, s81, 131072
X 187
Mvav 80,144,204
v_add_u32 v2, s20, v1
v_add_u32 v3, s21, v1
Mvav 112,144,212
s_lshl_b32 s18, s14, 9
X 188
Mvav 144,144,220
Ra 132,242,50176
K 211,186,187
Mvav 148,148,220
buffer_load_dwordx4 a[200:203], v1, s[44:47], s82 offen offset:0 sc1
Ra 136,242,51200
Mvav 116,148,212
X 189
Mvav 84,148,204
v_lshrrev_b32 v232, 1, v1
v_add_u32 v232, s18, v232
Mvav 52,148,196
X 190
Mvav 56,152,196
K 218,188,189
v_bfe_u32 v12, v1, 6, 2
Mvav 88,152,204
buffer_load_dwordx4 a[188:191], v1, s[44:47], s81 offen offset:1024 sc1
Ra 140,242,52224
Mvav 120,152,212
X 191
Mvav 152,152,220
v_lshrrev_b32 v13, 8, v1
v_bfe_u32 v14, v1, 4, 1
Mvav 156,156,220
X 176
Mvav 124,156,212
K 219,190,191
v_lshlrev_b32 v14, 4, v14
Mvav 92,156,204
X 177
Mvav 60,156,196
buffer_load_dwordx4 a[204:207], v1, s[44:47], s82 offen offset:1024 sc1
v_mov_b32 v15, 0x3f80
Maav 168,180,208
X 178
K 194,176,177
X 179
X 180
K 195,178,179
Maav 172,180,216
X 181
X 182
K 202,180,181
X 183
s_nop 0
K 203,182,183
Maav 160,180,192
Ra 144,242,53248
v_lshlrev_b32 v15, v14, v15
Maav 164,180,200
W 1
buffer_load_dwordx4 a[192:195], v1, s[44:47], s81 offen offset:2048 sc1
v_bfe_u32 v14, v1, 5, 1
Mvav 96,128,208
Ra 148,242,54272
v_xor_b32 v27, v12, v13
v_lshl_or_b32 v27, v27, 1, v14
Mvav 128,128,216
v_cmp_eq_u32_e64 s[86:87], 0, v27
v_mov_b32 v226, 0
s_nop 0
v_cndmask_b32_e64 v224, 0, v15, s[86:87]
Mvav 132,132,216
Ra 152,242,55296
Ra 156,242,56320
buffer_load_dwordx4 a[208:211], v1, s[44:47], s82 offen offset:2048 sc1
Mvav 100,132,208
v_mov_b32 v228, 0
v_mov_b32 v230, v224
v_cmp_eq_u32_e64 s[86:87], 1, v27
Mvav 104,136,208
v_mov_b32 v227, 0
Mvav 136,136,216
buffer_load_dwordx4 a[196:199], v1, s[44:47], s81 offen offset:3072 sc1
v_cndmask_b32_e64 v225, 0, v15, s[86:87]
v_mov_b32 v229, 0
Mvav 140,140,216
v_mov_b32 v231, v225
G 4,160
G 8,161
Mvav 108,140,208
W 0
G 5,164
G 9,165
G 6,168
Mvav 112,144,208
buffer_load_dwordx4 a[212:215], v1, s[44:47], s82 offen offset:3072 sc1
s_add_u32 s81, s58, 1
s_and_b32 s81, s81, 3
s_lshl_b32 s81, s81, 12
Mvav 144,144,216
s_add_u32 s81, s81, s49
G 10,169
G 7,172
G 11,173
Mvav 148,148,216
A 4,4,8
A 5,5,9
A 6,6,10
Mvav 116,148,208
buffer_load_dwordx4 a[216:219], v1, s[44:47], s81 offen offset:0 sc1
s_add_u32 s82, s81, 131072
A 7,7,11
ds_write_b64 v232, v[6:7] offset:57344
Mvav 120,152,208
A 8,4,5
A 9,6,7
A 8,8,9
Mvav 152,152,216
v_cmp_ngt_f32_e64 s[86:87], s90, v8
s_lshl_b32 s19, s14, 2
v_mov_b32 v9, s19
s_or_b64 s[88:89], s[88:89], s[86:87]
Mvav 156,156,216
s_and_b64 s[88:89], s[88:89], s[24:25]
s_cselect_b32 s18, 1, 0
buffer_load_dwordx4 a[232:235], v1, s[44:47], s82 offen offset:0 sc1
v_mov_b32 v8, s18
ds_write_b32 v9, v8 offset:59392
Mvav 124,156,208
v_xor_b32 v10, 1024, v232
v_bfe_u32 v11, v1, 2, 4
K 96,96,97
Mvav 32,128,192
K 97,98,99
K 98,100,101
K 99,102,103
Mvav 64,128,200
ds_write_b128 v2, v[96:99] offset:0
K 128,128,129
K 129,130,131
Mvav 68,132,200
K 130,132,133
K 131,134,135
ds_write_b128 v2, v[128:131] offset:4096
Mvav 36,132,192
buffer_load_dwordx4 a[220:223], v1, s[44:47], s81 offen offset:1024 sc1
K 104,104,105
K 105,106,107
Mvav 40,136,192
K 106,108,109
K 107,110,111
ds_write_b128 v2, v[104:107] offset:1024
Mvav 72,136,200
K 136,136,137
K 137,138,139
K 138,140,141
Mvav 76,140,200
buffer_load_dwordx4 a[236:239], v1, s[44:47], s82 offen offset:1024 sc1
K 139,142,143
ds_write_b128 v2, v[136:139] offset:5120
Mvav 44,140,192
K 112,112,113
K 113,114,115
K 114,116,117
Mvav 48,144,192
K 115,118,119
ds_write_b128 v2, v[112:115] offset:2048
K 144,144,145
Mvav 80,144,200
K 145,146,147
K 146,148,149
K 147,150,151
Mvav 84,148,200
ds_write_b128 v2, v[144:147] offset:6144
buffer_load_dwordx4 a[224:227], v1, s[44:47], s81 offen offset:2048 sc1
K 120,120,121
Mvav 52,148,192
K 121,122,123
K 122,124,125
K 123,126,127
Mvav 56,152,192
ds_write_b128 v2, v[120:123] offset:3072
K 152,152,153
K 153,154,155
Mvav 88,152,200
buffer_load_dwordx4 a[240:243], v1, s[44:47], s82 offen offset:2048 sc1
K 154,156,157
K 155,158,159
Mvav 92,156,200
ds_write_b128 v2, v[152:155] offset:7168
Mvav 60,156,192
buffer_load_dwordx4 a[228:231], v1, s[44:47], s81 offen offset:3072 sc1
buffer_load_dwordx4 a[244:247], v1, s[44:47], s82 offen offset:3072 sc1
s_branch .Lend_39%=
.p2align 12
.Lfs_38%=:
v_smfmac_f32_16x16x64_bf16 a[160:163], a[176:179], v[192:199], v240
s_mov_b32 s46, 0
s_lshl_b32 s92, s58, 2
s_add_u32 s91, s92, s51
s_add_u32 s95, s91, 3
s_and_b32 s95, s95, 15
s_mul_i32 s95, s95, 40960
s_mul_i32 s71, s51, 10240
v_smfmac_f32_16x16x64_bf16 a[164:167], a[176:179], v[200:207], v240
s_add_u32 s71, s71, 0
s_add_u32 m0, s71, s48
s_lshl2_add_u32 s94, s48, s95
Ra 144,242,45056
Ra 148,242,46080
Ra 152,242,47104
Ra 156,242,48128
v_smfmac_f32_16x16x64_bf16 a[168:171], a[176:179], v[208:215], v240
D 16,40,94,0
s_add_u32 s95, s91, 5
s_and_b32 s95, s95, 15
s_mul_i32 s95, s95, 40960
s_mul_i32 s71, s51, 10240
s_add_u32 s71, s71, 20480
v_smfmac_f32_16x16x64_bf16 a[172:175], a[176:179], v[216:223], v240
W 4
Cvaa 168,128,96,232
v_mov_b32 v192, 0
G 12,160
Mvaa 168,132,100
D 17,40,94,1024
G 4,161
A 12,12,4
v_cmp_nlt_f32_e64 s[86:87], v12, s90
v_rcp_f32 v12, v12
s_or_b64 s[88:89], s[88:89], s[86:87]
Cvaa 172,132,116,236
v_accvgpr_write_b32 a160, 0
v_accvgpr_write_b32 a161, 0
v_accvgpr_write_b32 a162, 0
v_accvgpr_write_b32 a163, 0
G 13,164
G 5,165
Mvaa 172,128,112
v_mov_b32 v193, 0
A 13,13,5
v_cmp_nlt_f32_e64 s[86:87], v13, s90
v_rcp_f32 v13, v13
s_or_b64 s[88:89], s[88:89], s[86:87]
v_accvgpr_write_b32 a164, 0
Mvaa 168,136,104
v_accvgpr_write_b32 a165, 0
v_accvgpr_write_b32 a166, 0
v_accvgpr_write_b32 a167, 0
U 32,32,12
U 33,33,12
U 34,34,12
Mvaa 168,140,108
v_mov_b32 v200, 0
U 35,35,12
U 36,36,12
U 37,37,12
U 38,38,12
U 39,39,12
Mvaa 172,140,124
Ra 128,242,16384
Ra 132,242,17408
K 32,32,33
K 33,34,35
K 34,36,37
K 35,38,39
s_add_u32 s93, s67, 0
Mvaa 172,136,120
Ra 136,242,18432
Ra 140,242,19456
D 18,40,94,2048
v_mov_b32 v201, 0
W 4
Cvaa 188,144,112,236
Ra 144,242,20480
v_cndmask_b32_e64 v168, v241, v168, s[72:73]
v_cndmask_b32_e64 v169, v241, v169, s[74:75]
v_cndmask_b32_e64 v170, v241, v170, s[76:77]
Mvaa 188,148,116
buffer_store_dwordx4 v[32:35], v1, s[8:11], s93 offen offset:0 sc1
U 40,40,12
U 41,41,12
U 42,42,12
U 43,43,12
U 44,44,12
Mvaa 188,152,120
U 45,45,12
U 46,46,12
U 47,47,12
K 40,40,41
K 41,42,43
K 42,44,45
Mvaa 188,156,124
W 1
K 43,46,47
buffer_store_dwordx4 v[40:43], v1, s[8:11], s93 offen offset:1024 sc1
U 48,48,12
)"
R"(U 49,49,12
U 50,50,12
U 51,51,12
Mvav 96,128,212
U 52,52,12
U 53,53,12
U 54,54,12
U 55,55,12
K 48,48,49
K 49,50,51
Mvav 128,128,220
K 50,52,53
K 51,54,55
buffer_store_dwordx4 v[48:51], v1, s[8:11], s93 offen offset:2048 sc1
U 56,56,12
U 57,57,12
U 58,58,12
Mvav 132,132,220
Ra 148,242,21504
Ra 152,242,22528
Ra 156,242,23552
U 59,59,12
U 60,60,12
U 61,61,12
Mvav 100,132,212
D 19,40,94,3072
s_add_u32 m0, m0, 4096
U 62,62,12
U 63,63,12
K 56,56,57
K 57,58,59
K 58,60,61
Mvav 104,136,212
K 59,62,63
v_cndmask_b32_e64 v171, v241, v171, s[78:79]
X 168
X 169
Mvav 136,136,220
buffer_store_dwordx4 v[56:59], v1, s[8:11], s93 offen offset:3072 sc1
Ra 128,242,49152
G 14,168
G 6,169
A 14,14,6
v_cmp_nlt_f32_e64 s[86:87], v14, s90
Mvav 140,140,220
v_rcp_f32 v14, v14
s_or_b64 s[88:89], s[88:89], s[86:87]
v_accvgpr_write_b32 a168, 0
v_accvgpr_write_b32 a169, 0
v_accvgpr_write_b32 a170, 0
v_accvgpr_write_b32 a171, 0
Mvav 108,140,212
W 1
G 15,172
G 7,173
A 15,15,7
v_cmp_nlt_f32_e64 s[86:87], v15, s90
v_rcp_f32 v15, v15
Mvav 112,144,212
s_or_b64 s[88:89], s[88:89], s[86:87]
buffer_load_dwordx4 v20, s[40:43], s94 offen lds
v_accvgpr_write_b32 a172, 0
v_accvgpr_write_b32 a173, 0
v_accvgpr_write_b32 a174, 0
v_accvgpr_write_b32 a175, 0
U 64,64,13
Mvav 144,144,220
s_add_u32 m0, s71, s48
s_lshl2_add_u32 s94, s48, s95
U 65,65,13
U 66,66,13
U 67,67,13
U 68,68,13
U 69,69,13
Mvav 148,148,220
U 70,70,13
U 71,71,13
K 64,64,65
K 65,66,67
K 66,68,69
K 67,70,71
s_add_u32 s93, s67, 131072
Mvav 116,148,212
buffer_store_dwordx4 v[64:67], v1, s[8:11], s93 offen offset:0 sc1
U 72,72,13
U 73,73,13
U 74,74,13
U 75,75,13
U 76,76,13
Mvav 120,152,212
Ra 132,242,50176
Ra 136,242,51200
Ra 140,242,52224
U 77,77,13
U 78,78,13
U 79,79,13
Mvav 152,152,220
K 72,72,73
K 73,74,75
K 74,76,77
K 75,78,79
buffer_store_dwordx4 v[72:75], v1, s[8:11], s93 offen offset:1024 sc1
U 80,80,13
Mvav 156,156,220
X 170
K 208,168,169
X 171
X 172
K 209,170,171
X 173
X 174
K 216,172,173
X 175
v_mov_b32 v194, 0
K 217,174,175
v_mov_b32 v195, 0
v_mov_b32 v202, 0
v_mov_b32 v203, 0
v_mov_b32 v210, 0
v_mov_b32 v211, 0
Mvav 124,156,212
v_cndmask_b32_e64 v188, v241, v188, s[72:73]
v_cndmask_b32_e64 v189, v241, v189, s[74:75]
v_cndmask_b32_e64 v190, v241, v190, s[76:77]
v_cndmask_b32_e64 v191, v241, v191, s[78:79]
X 188
X 189
X 190
K 218,188,189
X 191
s_nop 0
K 219,190,191
Maav 168,180,208
D 16,40,94,0
U 81,81,13
U 82,82,13
U 83,83,13
U 84,84,13
U 85,85,13
Maav 172,180,216
W 0
U 86,86,13
U 87,87,13
K 80,80,81
K 81,82,83
K 82,84,85
K 83,86,87
Zvav 32,128,192
buffer_store_dwordx4 v[80:83], v1, s[8:11], s93 offen offset:2048 sc1
U 88,88,13
U 89,89,13
U 90,90,13
U 91,91,13
U 92,92,13
Zvav 64,128,200
U 93,93,13
U 94,94,13
U 95,95,13
K 88,88,89
K 89,90,91
K 90,92,93
K 91,94,95
buffer_store_dwordx4 v[88:91], v1, s[8:11], s93 offen offset:3072 sc1
U 96,96,14
U 97,97,14
U 98,98,14
U 99,99,14
U 100,100,14
U 101,101,14
U 102,102,14
U 103,103,14
K 96,96,97
K 97,98,99
K 98,100,101
K 99,102,103
s_add_u32 s93, s67, 262144
buffer_store_dwordx4 v[96:99], v1, s[8:11], s93 offen offset:0 sc1
s_nop 1
Zvav 96,128,208
U 104,104,14
U 105,105,14
U 106,106,14
U 107,107,14
U 108,108,14
U 109,109,14
U 110,110,14
U 111,111,14
K 104,104,105
K 105,106,107
K 106,108,109
K 107,110,111
buffer_store_dwordx4 v[104:107], v1, s[8:11], s93 offen offset:1024 sc1
U 112,112,14
U 113,113,14
U 114,114,14
U 115,115,14
U 116,116,14
U 117,117,14
U 118,118,14
U 119,119,14
K 112,112,113
K 113,114,115
K 114,116,117
K 115,118,119
buffer_store_dwordx4 v[112:115], v1, s[8:11], s93 offen offset:2048 sc1
U 120,120,14
U 121,121,14
U 122,122,14
U 123,123,14
U 124,124,14
U 125,125,14
U 126,126,14
U 127,127,14
K 120,120,121
K 121,122,123
K 122,124,125
K 123,126,127
buffer_store_dwordx4 v[120:123], v1, s[8:11], s93 offen offset:3072 sc1
U 128,128,15
U 129,129,15
U 130,130,15
U 131,131,15
U 132,132,15
U 133,133,15
U 134,134,15
U 135,135,15
K 128,128,129
K 129,130,131
K 130,132,133
K 131,134,135
s_add_u32 s93, s67, 393216
buffer_store_dwordx4 v[128:131], v1, s[8:11], s93 offen offset:0 sc1
s_nop 1
Zvav 128,128,216
Ra 144,242,53248
U 136,136,15
U 137,137,15
U 138,138,15
U 139,139,15
U 140,140,15
Zvav 132,132,216
D 17,40,94,1024
U 141,141,15
U 142,142,15
U 143,143,15
K 136,136,137
K 137,138,139
Zvav 100,132,208
Ra 148,242,54272
Ra 152,242,55296
Ra 156,242,56320
K 138,140,141
K 139,142,143
Zvav 68,132,200
D 18,40,94,2048
Zvav 36,132,192
Zvav 40,136,192
D 19,40,94,3072
s_add_u32 m0, m0, 4096
Zvav 72,136,200
Zvav 104,136,208
buffer_store_dwordx4 v[136:139], v1, s[8:11], s93 offen offset:1024 sc1
U 144,144,15
U 145,145,15
U 146,146,15
U 147,147,15
U 148,148,15
Zvav 136,136,216
U 149,149,15
U 150,150,15
U 151,151,15
K 144,144,145
K 145,146,147
K 146,148,149
Zvav 140,140,216
K 147,150,151
buffer_store_dwordx4 v[144:147], v1, s[8:11], s93 offen offset:2048 sc1
U 152,152,15
U 153,153,15
U 154,154,15
U 155,155,15
Zvav 108,140,208
U 156,156,15
U 157,157,15
U 158,158,15
U 159,159,15
K 152,152,153
K 153,154,155
Zvav 76,140,200
buffer_load_dwordx4 v20, s[40:43], s94 offen lds
K 154,156,157
K 155,158,159
Zvav 44,140,192
W 0
Zvav 48,144,192
buffer_store_dwordx4 v[152:155], v1, s[8:11], s93 offen offset:3072 sc1
s_mov_b32 s18, 40960
s_mov_b32 s19, 73728
s_bitcmp1_b32 s14, 0
s_cselect_b32 s18, 49152, s18
s_cselect_b32 s19, 98304, s19
s_cmp_lt_u32 s14, 2
s_cselect_b32 s20, s18, s19
s_cselect_b32 s21, s19, s18
v_add_u32 v2, s20, v1
v_add_u32 v3, s21, v1
Zvav 80,144,200
s_lshl_b32 s18, s14, 9
v_lshrrev_b32 v232, 1, v1
v_add_u32 v232, s18, v232
v_bfe_u32 v12, v1, 6, 2
v_lshrrev_b32 v13, 8, v1
v_bfe_u32 v14, v1, 4, 1
v_lshlrev_b32 v14, 4, v14
Zvav 112,144,208
v_mov_b32 v15, 0x3f80
v_lshlrev_b32 v15, v14, v15
v_bfe_u32 v14, v1, 5, 1
v_xor_b32 v27, v12, v13
v_lshl_or_b32 v27, v27, 1, v14
v_cmp_eq_u32_e64 s[86:87], 0, v27
Zvav 144,144,216
v_mov_b32 v226, 0
Zvav 148,148,216
v_cndmask_b32_e64 v224, 0, v15, s[86:87]
v_mov_b32 v228, 0
v_mov_b32 v230, v224
v_cmp_eq_u32_e64 s[86:87], 1, v27
v_mov_b32 v227, 0
s_nop 0
v_cndmask_b32_e64 v225, 0, v15, s[86:87]
Zvav 116,148,208
v_mov_b32 v229, 0
v_mov_b32 v231, v225
G 4,160
G 8,161
G 5,164
G 9,165
Zvav 84,148,200
G 6,168
G 10,169
G 7,172
G 11,173
A 4,4,8
A 5,5,9
Zvav 52,148,192
A 6,6,10
A 7,7,11
ds_write_b64 v232, v[6:7] offset:57344
A 8,4,5
A 9,6,7
A 8,8,9
Zvav 56,152,192
v_cmp_ngt_f32_e64 s[86:87], s90, v8
s_lshl_b32 s19, s14, 2
v_mov_b32 v9, s19
s_or_b64 s[88:89], s[88:89], s[86:87]
s_and_b64 s[88:89], s[88:89], s[24:25]
s_cselect_b32 s18, 1, 0
v_mov_b32 v8, s18
ds_write_b32 v9, v8 offset:59392
Zvav 88,152,200
v_xor_b32 v10, 1024, v232
v_bfe_u32 v11, v1, 2, 4
K 96,96,97
K 97,98,99
K 98,100,101
K 99,102,103
Zvav 120,152,208
ds_write_b128 v2, v[96:99] offset:0
K 128,128,129
K 129,130,131
K 130,132,133
K 131,134,135
ds_write_b128 v2, v[128:131] offset:4096
Zvav 152,152,216
K 104,104,105
K 105,106,107
K 106,108,109
K 107,110,111
ds_write_b128 v2, v[104:107] offset:1024
K 136,136,137
Zvav 156,156,216
K 137,138,139
K 138,140,141
K 139,142,143
ds_write_b128 v2, v[136:139] offset:5120
K 112,112,113
K 113,114,115
Zvav 124,156,208
K 114,116,117
K 115,118,119
ds_write_b128 v2, v[112:115] offset:2048
K 144,144,145
K 145,146,147
K 146,148,149
Zvav 92,156,200
K 147,150,151
ds_write_b128 v2, v[144:147] offset:6144
Zvav 60,156,192
)"
R"(K 120,120,121
K 121,122,123
K 122,124,125
K 123,126,127
ds_write_b128 v2, v[120:123] offset:3072
K 152,152,153
K 153,154,155
K 154,156,157
K 155,158,159
ds_write_b128 v2, v[152:155] offset:7168
s_waitcnt vmcnt(0)
s_and_b64 s[86:87], s[88:89], s[24:25]
s_cbranch_scc1 .LL_46%=
v_mov_b64 v[28:29], s[12:13]
s_mov_b64 exec, 1
buffer_store_dwordx2 v[28:29], off, s[8:11], s69 sc1
s_mov_b64 exec, -1
s_mov_b32 s70, 134217728
.LL_46%=:
s_branch .Lend_39%=
.p2align 12
.Lpd_28%=:
v_pk_mul_f32 v[32:33], v[32:33], s[22:23]
v_pk_mul_f32 v[34:35], v[34:35], s[22:23]
v_pk_mul_f32 v[36:37], v[36:37], s[22:23]
v_pk_mul_f32 v[38:39], v[38:39], s[22:23]
v_pk_mul_f32 v[64:65], v[64:65], s[22:23]
v_pk_mul_f32 v[66:67], v[66:67], s[22:23]
v_pk_mul_f32 v[68:69], v[68:69], s[22:23]
v_pk_mul_f32 v[70:71], v[70:71], s[22:23]
v_pk_mul_f32 v[96:97], v[96:97], s[22:23]
v_pk_mul_f32 v[98:99], v[98:99], s[22:23]
v_pk_mul_f32 v[100:101], v[100:101], s[22:23]
v_pk_mul_f32 v[102:103], v[102:103], s[22:23]
v_pk_mul_f32 v[128:129], v[128:129], s[22:23]
v_pk_mul_f32 v[130:131], v[130:131], s[22:23]
v_pk_mul_f32 v[132:133], v[132:133], s[22:23]
v_pk_mul_f32 v[134:135], v[134:135], s[22:23]
v_pk_mul_f32 v[40:41], v[40:41], s[22:23]
v_pk_mul_f32 v[42:43], v[42:43], s[22:23]
v_pk_mul_f32 v[44:45], v[44:45], s[22:23]
v_pk_mul_f32 v[46:47], v[46:47], s[22:23]
v_pk_mul_f32 v[72:73], v[72:73], s[22:23]
v_pk_mul_f32 v[74:75], v[74:75], s[22:23]
v_pk_mul_f32 v[76:77], v[76:77], s[22:23]
v_pk_mul_f32 v[78:79], v[78:79], s[22:23]
v_pk_mul_f32 v[104:105], v[104:105], s[22:23]
v_pk_mul_f32 v[106:107], v[106:107], s[22:23]
v_pk_mul_f32 v[108:109], v[108:109], s[22:23]
v_pk_mul_f32 v[110:111], v[110:111], s[22:23]
v_pk_mul_f32 v[136:137], v[136:137], s[22:23]
v_pk_mul_f32 v[138:139], v[138:139], s[22:23]
v_pk_mul_f32 v[140:141], v[140:141], s[22:23]
v_pk_mul_f32 v[142:143], v[142:143], s[22:23]
v_pk_mul_f32 v[48:49], v[48:49], s[22:23]
v_pk_mul_f32 v[50:51], v[50:51], s[22:23]
v_pk_mul_f32 v[52:53], v[52:53], s[22:23]
v_pk_mul_f32 v[54:55], v[54:55], s[22:23]
v_pk_mul_f32 v[80:81], v[80:81], s[22:23]
v_pk_mul_f32 v[82:83], v[82:83], s[22:23]
v_pk_mul_f32 v[84:85], v[84:85], s[22:23]
v_pk_mul_f32 v[86:87], v[86:87], s[22:23]
v_pk_mul_f32 v[112:113], v[112:113], s[22:23]
v_pk_mul_f32 v[114:115], v[114:115], s[22:23]
v_pk_mul_f32 v[116:117], v[116:117], s[22:23]
v_pk_mul_f32 v[118:119], v[118:119], s[22:23]
v_pk_mul_f32 v[144:145], v[144:145], s[22:23]
v_pk_mul_f32 v[146:147], v[146:147], s[22:23]
v_pk_mul_f32 v[148:149], v[148:149], s[22:23]
v_pk_mul_f32 v[150:151], v[150:151], s[22:23]
v_pk_mul_f32 v[56:57], v[56:57], s[22:23]
v_pk_mul_f32 v[58:59], v[58:59], s[22:23]
v_pk_mul_f32 v[60:61], v[60:61], s[22:23]
v_pk_mul_f32 v[62:63], v[62:63], s[22:23]
v_pk_mul_f32 v[88:89], v[88:89], s[22:23]
v_pk_mul_f32 v[90:91], v[90:91], s[22:23]
v_pk_mul_f32 v[92:93], v[92:93], s[22:23]
v_pk_mul_f32 v[94:95], v[94:95], s[22:23]
v_pk_mul_f32 v[120:121], v[120:121], s[22:23]
v_pk_mul_f32 v[122:123], v[122:123], s[22:23]
v_pk_mul_f32 v[124:125], v[124:125], s[22:23]
v_pk_mul_f32 v[126:127], v[126:127], s[22:23]
v_pk_mul_f32 v[152:153], v[152:153], s[22:23]
v_pk_mul_f32 v[154:155], v[154:155], s[22:23]
v_pk_mul_f32 v[156:157], v[156:157], s[22:23]
v_pk_mul_f32 v[158:159], v[158:159], s[22:23]
K 32,32,33
K 33,34,35
K 34,36,37
K 35,38,39
K 40,40,41
K 41,42,43
K 42,44,45
K 43,46,47
W 0
Zvav 196,128,32
s_add_u32 m0, s62, 0
Ra 144,242,4096
Ra 148,242,5120
Ra 152,242,6144
Ra 156,242,7168
Mvav 196,132,40
K 64,64,65
K 65,66,67
K 66,68,69
K 67,70,71
K 72,72,73
K 73,74,75
K 74,76,77
K 75,78,79
s_nop 1
Zvav 204,132,72
Dc 1,8,61,0
K 96,96,97
K 97,98,99
K 98,100,101
Mvav 204,128,64
K 99,102,103
K 104,104,105
K 105,106,107
K 106,108,109
K 107,110,111
Zvav 212,128,96
Dc 1,8,61,1024
K 128,128,129
K 129,130,131
K 130,132,133
Mvav 212,132,104
K 131,134,135
K 136,136,137
K 137,138,139
K 138,140,141
K 139,142,143
s_nop 1
Zvav 220,132,136
Dc 1,8,61,2048
K 48,48,49
K 49,50,51
K 50,52,53
Mvav 220,128,128
K 51,54,55
K 56,56,57
K 57,58,59
K 58,60,61
K 59,62,63
Mvav 196,136,48
Dc 1,8,61,3072
s_add_u32 m0, s62, 4096
Ra 128,242,8192
Ra 132,242,9216
K 80,80,81
Mvav 196,140,56
K 81,82,83
K 82,84,85
K 83,86,87
K 88,88,89
K 89,90,91
K 90,92,93
K 91,94,95
s_nop 1
Mvav 204,140,88
Dc 243,8,61,0
K 112,112,113
K 113,114,115
K 114,116,117
Mvav 204,136,80
K 115,118,119
K 120,120,121
K 121,122,123
K 122,124,125
K 123,126,127
Mvav 212,136,112
Dc 243,8,61,1024
K 144,144,145
K 145,146,147
K 146,148,149
Mvav 212,140,120
K 147,150,151
K 152,152,153
K 153,154,155
K 154,156,157
K 155,158,159
s_nop 1
Mvav 220,140,152
Dc 243,8,61,2048
v_accvgpr_write_b32 a0, v32
v_accvgpr_write_b32 a1, v33
v_accvgpr_write_b32 a2, v34
Mvav 220,136,144
W 2
Dc 243,8,61,3072
s_add_u32 m0, s62, 8192
v_accvgpr_write_b32 a3, v35
v_accvgpr_write_b32 a4, v40
Zvav 68,144,64
Dc 244,8,61,0
v_accvgpr_write_b32 a5, v41
v_accvgpr_write_b32 a6, v42
v_accvgpr_write_b32 a7, v43
Mvav 68,148,72
Dc 244,8,61,1024
v_accvgpr_write_b32 a8, v48
v_accvgpr_write_b32 a9, v49
v_accvgpr_write_b32 a10, v50
Zvav 100,148,104
Dc 244,8,61,2048
Ra 136,242,10240
Ra 140,242,11264
v_accvgpr_write_b32 a11, v51
Mvav 100,144,96
Dc 244,8,61,3072
s_add_u32 m0, s62, 12288
v_accvgpr_write_b32 a12, v56
v_accvgpr_write_b32 a13, v57
v_accvgpr_write_b32 a14, v58
Zvav 132,144,128
Dc 245,8,61,0
v_accvgpr_write_b32 a15, v59
v_accvgpr_write_b32 a16, v64
v_cndmask_b32_e64 v196, v241, v196, s[72:73]
Mvav 132,148,136
Dc 245,8,61,1024
v_cndmask_b32_e64 v197, v241, v197, s[74:75]
v_cndmask_b32_e64 v198, v241, v198, s[76:77]
v_cndmask_b32_e64 v199, v241, v199, s[78:79]
Mvav 68,152,80
Dc 245,8,61,2048
v_max3_f32 v4, v196, v197, v198
v_max3_f32 v4, v4, v199, v252
v_max3_f32 v5, v204, v205, v206
Mvav 68,156,88
v_max3_f32 v5, v5, v207, v253
v_max3_f32 v6, v212, v213, v214
v_max3_f32 v6, v6, v215, v254
v_max3_f32 v7, v220, v221, v222
Mvav 100,156,120
v_max3_f32 v7, v7, v223, v255
v_mov_b32 v8, v4
v_mov_b32 v9, v5
v_mov_b32 v10, v6
Mvav 100,152,112
v_mov_b32 v11, v7
v_permlane32_swap_b32 v4, v8
v_permlane32_swap_b32 v5, v9
v_accvgpr_write_b32 a17, v65
Mvav 132,152,144
v_permlane32_swap_b32 v6, v10
v_permlane32_swap_b32 v7, v11
v_max_f32 v4, v4, v8
v_max_f32 v5, v5, v9
v_max_f32 v6, v6, v10
v_max_f32 v7, v7, v11
v_mov_b32 v8, v4
v_mov_b32 v9, v5
v_mov_b32 v10, v6
v_mov_b32 v11, v7
v_permlane16_swap_b32 v4, v8
v_permlane16_swap_b32 v5, v9
v_permlane16_swap_b32 v6, v10
v_permlane16_swap_b32 v7, v11
v_max_f32 v4, v4, v8
v_max_f32 v5, v5, v9
v_max_f32 v6, v6, v10
v_max_f32 v7, v7, v11
v_sub_f32 v4, 0, v4
v_sub_f32 v5, 0, v5
v_sub_f32 v6, 0, v6
v_sub_f32 v7, 0, v7
v_mov_b32 v224, v4
v_mov_b32 v225, v4
v_mov_b32 v226, v4
v_mov_b32 v227, v4
v_mov_b32 v228, v5
v_mov_b32 v229, v5
v_mov_b32 v230, v5
v_mov_b32 v231, v5
v_mov_b32 v232, v6
v_mov_b32 v233, v6
v_mov_b32 v234, v6
v_mov_b32 v235, v6
v_mov_b32 v236, v7
v_mov_b32 v237, v7
v_mov_b32 v238, v7
v_mov_b32 v239, v7
Mvav 132,156,152
W 0
Dc 245,8,61,3072
v_accvgpr_write_b32 a18, v66
v_accvgpr_write_b32 a19, v67
v_accvgpr_write_b32 a20, v72
Cvav 168,128,96,232
s_add_u32 s61, s61, s63
s_add_u32 s36, s29, 65536
v_accvgpr_write_b32 a21, v73
v_accvgpr_write_b32 a22, v74
v_accvgpr_write_b32 a23, v75
Mvav 168,132,104
s_and_b32 s80, s14, 1
buffer_store_dwordx4 a[192:195], v1, s[8:11], s36 offen offset:0 sc1
v_accvgpr_write_b32 a24, v80
v_accvgpr_write_b32 a25, v81
v_accvgpr_write_b32 a26, v82
Cvav 172,132,136,236
s_lshl_b32 s80, s80, 14
Ra 144,242,12288
Ra 148,242,13312
Ra 152,242,14336
Ra 156,242,15360
Mvav 172,128,128
s_add_u32 s80, s80, 98304
v_accvgpr_write_b32 a27, v83
v_accvgpr_write_b32 a28, v88
v_accvgpr_write_b32 a29, v89
v_accvgpr_write_b32 a30, v90
Mvav 168,136,112
buffer_store_dwordx4 a[200:203], v1, s[8:11], s36 offen offset:1024 sc1
v_accvgpr_write_b32 a31, v91
v_accvgpr_write_b32 a32, v96
v_accvgpr_write_b32 a33, v97
Mvav 168,140,120
v_accvgpr_write_b32 a34, v98
v_accvgpr_write_b32 a35, v99
v_accvgpr_write_b32 a36, v104
v_accvgpr_write_b32 a37, v105
Mvav 172,140,152
Ra 128,242,24576
Ra 132,242,25600
buffer_store_dwordx4 a[208:211], v1, s[8:11], s36 offen offset:2048 sc1
v_accvgpr_write_b32 a38, v106
Mvav 172,136,144
Ra 136,242,26624
Ra 140,242,27648
v_accvgpr_write_b32 a39, v107
W 4
Cvav 188,144,128,236
v_accvgpr_write_b32 a40, v112
v_accvgpr_write_b32 a41, v113
v_accvgpr_write_b32 a42, v114
v_accvgpr_write_b32 a43, v115
v_accvgpr_write_b32 a44, v120
v_accvgpr_write_b32 a45, v121
v_accvgpr_write_b32 a46, v122
v_accvgpr_write_b32 a47, v123
v_accvgpr_write_b32 a48, v128
v_accvgpr_write_b32 a49, v129
v_accvgpr_write_b32 a50, v130
v_accvgpr_write_b32 a51, v131
v_accvgpr_write_b32 a52, v136
v_accvgpr_write_b32 a53, v137
v_accvgpr_write_b32 a54, v138
v_accvgpr_write_b32 a55, v139
v_accvgpr_write_b32 a56, v144
v_accvgpr_write_b32 a57, v145
v_accvgpr_write_b32 a58, v146
v_accvgpr_write_b32 a59, v147
v_accvgpr_write_b32 a60, v152
v_accvgpr_write_b32 a61, v153
v_accvgpr_write_b32 a62, v154
v_accvgpr_write_b32 a63, v155
Mvav 188,148,136
s_barrier
buffer_store_dwordx4 a[216:219], v1, s[8:11], s36 offen offset:3072 sc1
s_add_u32 s36, s29, 69632
v_add_u32 v8, s80, v1
A 196,196,224
Mvav 188,152,144
A 197,197,224
A 198,198,224
A 199,199,224
X 196
X 197
X 198
K 192,196,197
X 199
A 204,204,228
K 193,198,199
A 205,205,228
A 206,206,228
A 207,207,228
X 204
X 205
X 206
K 200,204,205
X 207
A 212,212,232
K 201,206,207
A 213,213,232
A 214,214,232
A 215,215,232
X 212
X 213
X 214
K 208,212,213
X 215
A 220,220,236
K 209,214,215
A 221,221,236
A 222,222,236
A 223,223,236
X 220
)"
R"(X 221
X 222
K 216,220,221
X 223
v_mov_b32 v194, 0
K 217,222,223
v_mov_b32 v195, 0
v_cndmask_b32_e64 v68, v241, v68, s[72:73]
v_cndmask_b32_e64 v69, v241, v69, s[74:75]
v_cndmask_b32_e64 v70, v241, v70, s[76:77]
v_cndmask_b32_e64 v71, v241, v71, s[78:79]
A 68,68,228
A 69,69,228
A 70,70,228
A 71,71,228
X 68
X 69
X 70
K 202,68,69
X 71
A 100,100,232
K 203,70,71
A 101,101,232
A 102,102,232
A 103,103,232
X 100
X 101
X 102
K 210,100,101
X 103
A 132,132,236
K 211,102,103
A 133,133,236
A 134,134,236
A 135,135,236
X 132
X 133
X 134
K 218,132,133
X 135
s_nop 0
K 219,134,135
Mvav 188,156,152
W 0
buffer_store_dwordx4 a[224:227], v1, s[8:11], s36 offen offset:0 sc1
Ra 144,242,28672
Ra 148,242,29696
Ra 152,242,30720
Zvav 32,128,192
buffer_store_dwordx4 a[232:235], v1, s[8:11], s36 offen offset:1024 sc1
Ra 156,242,31744
Zvav 64,128,200
buffer_store_dwordx4 a[240:243], v1, s[8:11], s36 offen offset:2048 sc1
v_xor_b32 v242, 0x10000, v242
Zvav 96,128,208
s_and_b32 s80, s14, 1
s_xor_b32 s62, s62, 0x10000
buffer_store_dwordx4 a[248:251], v1, s[8:11], s36 offen offset:3072 sc1
s_add_u32 s36, s29, 393216
Zvav 128,128,216
s_lshl_b32 s80, s80, 14
s_add_u32 s80, s80, 131072
buffer_store_dwordx4 a[196:199], v1, s[8:11], s36 offen offset:0 sc1
v_add_u32 v23, 61440, v21
Zvav 132,132,216
buffer_store_dwordx4 a[204:207], v1, s[8:11], s36 offen offset:1024 sc1
v_add_u32 v12, s80, v1
Ra 64,12,8192
v_add_u32 v24, 61440, v22
Zvav 100,132,208
buffer_store_dwordx4 a[212:215], v1, s[8:11], s36 offen offset:2048 sc1
Ra 68,12,9216
Ra 72,12,10240
v_add_u32 v25, 122880, v21
Zvav 68,132,200
buffer_store_dwordx4 a[220:223], v1, s[8:11], s36 offen offset:3072 sc1
s_add_u32 s36, s29, 397312
Ra 76,12,11264
Ra 80,12,12288
Ra 84,12,13312
Zvav 36,132,192
buffer_store_dwordx4 a[228:231], v1, s[8:11], s36 offen offset:0 sc1
Ra 88,12,14336
Ra 92,12,15360
v_add_u32 v26, 122880, v22
Zvav 40,136,192
buffer_store_dwordx4 a[236:239], v1, s[8:11], s36 offen offset:1024 sc1
Ra 96,12,0
Ra 100,12,1024
Ra 104,12,2048
Zvav 72,136,200
Ra 108,12,3072
Ra 112,12,4096
buffer_store_dwordx4 a[244:247], v1, s[8:11], s36 offen offset:2048 sc1
Ra 116,12,5120
Zvav 104,136,208
Ra 120,12,6144
Ra 124,12,7168
buffer_store_dwordx4 a[252:255], v1, s[8:11], s36 offen offset:3072 sc1
Zvav 136,136,216
Zvav 140,140,216
Zvav 108,140,208
Zvav 76,140,200
s_waitcnt vmcnt(16) lgkmcnt(0)
s_barrier
ds_read_b128 v[248:251], v8
Zvav 44,140,192
Ra 128,242,40960
Zvav 48,144,192
Zvav 80,144,200
X 172
X 173
Zvav 112,144,208
Ra 132,242,41984
X 174
K 220,172,173
Zvav 144,144,216
X 175
v_cndmask_b32_e64 v168, v241, v168, s[72:73]
K 221,174,175
Zvav 148,148,216
v_cndmask_b32_e64 v169, v241, v169, s[74:75]
v_cndmask_b32_e64 v170, v241, v170, s[76:77]
v_cndmask_b32_e64 v171, v241, v171, s[78:79]
Zvav 116,148,208
X 168
X 169
Zvav 84,148,200
W 2
Ra 136,242,43008
X 170
K 212,168,169
Zvav 52,148,192
v_mov_b32 v224, v250
v_mov_b32 v225, v250
v_mov_b32 v226, v250
v_mov_b32 v227, v250
Zvav 56,152,192
v_mov_b32 v228, v251
v_mov_b32 v229, v251
v_mov_b32 v230, v251
v_mov_b32 v231, v251
Zvav 88,152,200
v_mov_b32 v232, v248
v_mov_b32 v233, v248
v_mov_b32 v234, v248
v_mov_b32 v235, v248
Zvav 120,152,208
v_mov_b32 v236, v249
v_mov_b32 v237, v249
v_mov_b32 v238, v249
v_mov_b32 v239, v249
Zvav 152,152,216
X 171
v_mov_b32 v204, 0
K 213,170,171
Zvav 156,156,216
Ra 140,242,44032
v_mov_b32 v205, 0
v_mov_b32 v196, 0
v_mov_b32 v197, 0
Zvav 124,156,208
v_mov_b32 v198, 0
v_mov_b32 v199, 0
v_mov_b32 v206, 0
v_mov_b32 v207, 0
Zvav 92,156,200
v_mov_b32 v214, 0
v_mov_b32 v215, 0
v_cndmask_b32_e64 v188, v241, v188, s[72:73]
v_cndmask_b32_e64 v189, v241, v189, s[74:75]
Zvav 60,156,192
v_cndmask_b32_e64 v190, v241, v190, s[76:77]
v_cndmask_b32_e64 v191, v241, v191, s[78:79]
X 188
X 189
X 190
K 222,188,189
X 191
s_nop 0
K 223,190,191
s_waitcnt vmcnt(0)
s_add_u32 s80, s71, 128
v_mov_b64 v[28:29], s[12:13]
s_mov_b64 exec, 1
buffer_store_dwordx2 v[28:29], off, s[8:11], s80 sc1
s_mov_b64 exec, -1
s_nop 1
s_branch .Lsp2_33%=
.p2align 12
.Lpe_44%=:
v_pk_mul_f32 v[32:33], v[32:33], s[22:23]
v_pk_mul_f32 v[34:35], v[34:35], s[22:23]
v_pk_mul_f32 v[36:37], v[36:37], s[22:23]
v_pk_mul_f32 v[38:39], v[38:39], s[22:23]
v_pk_mul_f32 v[64:65], v[64:65], s[22:23]
v_pk_mul_f32 v[66:67], v[66:67], s[22:23]
v_pk_mul_f32 v[68:69], v[68:69], s[22:23]
v_pk_mul_f32 v[70:71], v[70:71], s[22:23]
v_pk_mul_f32 v[96:97], v[96:97], s[22:23]
v_pk_mul_f32 v[98:99], v[98:99], s[22:23]
v_pk_mul_f32 v[100:101], v[100:101], s[22:23]
v_pk_mul_f32 v[102:103], v[102:103], s[22:23]
v_pk_mul_f32 v[128:129], v[128:129], s[22:23]
v_pk_mul_f32 v[130:131], v[130:131], s[22:23]
v_pk_mul_f32 v[132:133], v[132:133], s[22:23]
v_pk_mul_f32 v[134:135], v[134:135], s[22:23]
v_pk_mul_f32 v[40:41], v[40:41], s[22:23]
v_pk_mul_f32 v[42:43], v[42:43], s[22:23]
v_pk_mul_f32 v[44:45], v[44:45], s[22:23]
v_pk_mul_f32 v[46:47], v[46:47], s[22:23]
v_pk_mul_f32 v[72:73], v[72:73], s[22:23]
v_pk_mul_f32 v[74:75], v[74:75], s[22:23]
v_pk_mul_f32 v[76:77], v[76:77], s[22:23]
v_pk_mul_f32 v[78:79], v[78:79], s[22:23]
v_pk_mul_f32 v[104:105], v[104:105], s[22:23]
v_pk_mul_f32 v[106:107], v[106:107], s[22:23]
v_pk_mul_f32 v[108:109], v[108:109], s[22:23]
v_pk_mul_f32 v[110:111], v[110:111], s[22:23]
v_pk_mul_f32 v[136:137], v[136:137], s[22:23]
v_pk_mul_f32 v[138:139], v[138:139], s[22:23]
v_pk_mul_f32 v[140:141], v[140:141], s[22:23]
v_pk_mul_f32 v[142:143], v[142:143], s[22:23]
v_pk_mul_f32 v[48:49], v[48:49], s[22:23]
v_pk_mul_f32 v[50:51], v[50:51], s[22:23]
v_pk_mul_f32 v[52:53], v[52:53], s[22:23]
v_pk_mul_f32 v[54:55], v[54:55], s[22:23]
v_pk_mul_f32 v[80:81], v[80:81], s[22:23]
v_pk_mul_f32 v[82:83], v[82:83], s[22:23]
v_pk_mul_f32 v[84:85], v[84:85], s[22:23]
v_pk_mul_f32 v[86:87], v[86:87], s[22:23]
v_pk_mul_f32 v[112:113], v[112:113], s[22:23]
v_pk_mul_f32 v[114:115], v[114:115], s[22:23]
v_pk_mul_f32 v[116:117], v[116:117], s[22:23]
v_pk_mul_f32 v[118:119], v[118:119], s[22:23]
v_pk_mul_f32 v[144:145], v[144:145], s[22:23]
v_pk_mul_f32 v[146:147], v[146:147], s[22:23]
v_pk_mul_f32 v[148:149], v[148:149], s[22:23]
v_pk_mul_f32 v[150:151], v[150:151], s[22:23]
v_pk_mul_f32 v[56:57], v[56:57], s[22:23]
v_pk_mul_f32 v[58:59], v[58:59], s[22:23]
v_pk_mul_f32 v[60:61], v[60:61], s[22:23]
v_pk_mul_f32 v[62:63], v[62:63], s[22:23]
v_pk_mul_f32 v[88:89], v[88:89], s[22:23]
v_pk_mul_f32 v[90:91], v[90:91], s[22:23]
v_pk_mul_f32 v[92:93], v[92:93], s[22:23]
v_pk_mul_f32 v[94:95], v[94:95], s[22:23]
v_pk_mul_f32 v[120:121], v[120:121], s[22:23]
v_pk_mul_f32 v[122:123], v[122:123], s[22:23]
v_pk_mul_f32 v[124:125], v[124:125], s[22:23]
v_pk_mul_f32 v[126:127], v[126:127], s[22:23]
v_pk_mul_f32 v[152:153], v[152:153], s[22:23]
v_pk_mul_f32 v[154:155], v[154:155], s[22:23]
v_pk_mul_f32 v[156:157], v[156:157], s[22:23]
v_pk_mul_f32 v[158:159], v[158:159], s[22:23]
K 32,32,33
K 33,34,35
K 34,36,37
K 35,38,39
K 40,40,41
K 41,42,43
K 42,44,45
K 43,46,47
W 0
Zvav 196,128,32
s_add_u32 m0, s62, 0
Ra 144,242,4096
Ra 148,242,5120
Ra 152,242,6144
Ra 156,242,7168
Mvav 196,132,40
K 64,64,65
K 65,66,67
K 66,68,69
K 67,70,71
K 72,72,73
K 73,74,75
K 74,76,77
K 75,78,79
s_nop 1
Zvav 204,132,72
Dc 1,8,61,0
K 96,96,97
K 97,98,99
K 98,100,101
Mvav 204,128,64
K 99,102,103
K 104,104,105
K 105,106,107
K 106,108,109
K 107,110,111
Zvav 212,128,96
K 128,128,129
K 129,130,131
K 130,132,133
K 131,134,135
Mvav 212,132,104
K 136,136,137
K 137,138,139
K 138,140,141
K 139,142,143
s_nop 1
Zvav 220,132,136
Dc 1,8,61,1024
K 48,48,49
K 49,50,51
K 50,52,53
Mvav 220,128,128
K 51,54,55
K 56,56,57
K 57,58,59
K 58,60,61
K 59,62,63
Mvav 196,136,48
Ra 128,242,8192
Ra 132,242,9216
K 80,80,81
K 81,82,83
Mvav 196,140,56
K 82,84,85
K 83,86,87
K 88,88,89
K 89,90,91
K 90,92,93
K 91,94,95
s_nop 1
Mvav 204,140,88
Dc 1,8,61,2048
K 112,112,113
K 113,114,115
K 114,116,117
Mvav 204,136,80
K 115,118,119
K 120,120,121
K 121,122,123
K 122,124,125
K 123,126,127
Mvav 212,136,112
K 144,144,145
K 145,146,147
K 146,148,149
K 147,150,151
Mvav 212,140,120
K 152,152,153
K 153,154,155
K 154,156,157
K 155,158,159
s_nop 1
Mvav 220,140,152
Dc 1,8,61,3072
s_add_u32 m0, s62, 4096
v_accvgpr_write_b32 a0, v32
v_accvgpr_write_b32 a1, v33
v_accvgpr_write_b32 a2, v34
Mvav 220,136,144
W 2
Ra 136,242,10240
v_accvgpr_write_b32 a3, v35
v_accvgpr_write_b32 a4, v40
Zvav 36,144,32
Ra 140,242,11264
v_accvgpr_write_b32 a5, v41
Mvav 36,148,40
v_accvgpr_write_b32 a6, v42
v_accvgpr_write_b32 a7, v43
v_max3_f32 v4, v196, v197, v198
v_max3_f32 v4, v4, v199, v252
Zvav 68,148,72
v_max3_f32 v5, v204, v205, v206
v_max3_f32 v5, v5, v207, v253
v_max3_f32 v6, v212, v213, v214
v_max3_f32 v6, v6, v215, v254
Mvav 68,144,64
v_max3_f32 v7, v220, v221, v222
v_max3_f32 v7, v7, v223, v255
v_mov_b32 v8, v4
v_mov_b32 v9, v5
Zvav 100,144,96
Dc 243,8,61,0
v_mov_b32 v10, v6
v_mov_b32 v11, v7
v_permlane32_swap_b32 v4, v8
Mvav 100,148,104
v_permlane32_swap_b32 v5, v9
v_permlane32_swap_b32 v6, v10
v_permlane32_swap_b32 v7, v11
v_max_f32 v4, v4, v8
Zvav 132,148,136
v_max_f32 v5, v5, v9
v_max_f32 v6, v6, v10
v_max_f32 v7, v7, v11
v_mov_b32 v8, v4
Mvav 132,144,128
v_mov_b32 v9, v5
v_mov_b32 v10, v6
v_mov_b32 v11, v7
v_accvgpr_write_b32 a8, v48
Mvav 36,152,48
v_permlane16_swap_b32 v4, v8
v_permlane16_swap_b32 v5, v9
)"
R"(v_permlane16_swap_b32 v6, v10
v_permlane16_swap_b32 v7, v11
Mvav 36,156,56
v_max_f32 v4, v4, v8
v_max_f32 v5, v5, v9
v_max_f32 v6, v6, v10
v_max_f32 v7, v7, v11
Mvav 68,156,88
v_sub_f32 v4, 0, v4
v_sub_f32 v5, 0, v5
v_sub_f32 v6, 0, v6
v_sub_f32 v7, 0, v7
Mvav 68,152,80
v_mov_b32 v224, v4
v_mov_b32 v225, v4
v_mov_b32 v226, v4
v_mov_b32 v227, v4
Mvav 100,152,112
v_mov_b32 v228, v5
v_mov_b32 v229, v5
v_mov_b32 v230, v5
v_mov_b32 v231, v5
Mvav 100,156,120
v_mov_b32 v232, v6
v_mov_b32 v233, v6
v_mov_b32 v234, v6
v_mov_b32 v235, v6
Mvav 132,156,152
v_mov_b32 v236, v7
v_mov_b32 v237, v7
v_mov_b32 v238, v7
v_mov_b32 v239, v7
Mvav 132,152,144
W 0
v_accvgpr_write_b32 a9, v49
v_accvgpr_write_b32 a10, v50
v_accvgpr_write_b32 a11, v51
v_accvgpr_write_b32 a12, v56
Cvav 160,128,32,224
s_and_b32 s80, s14, 1
Dc 243,8,61,1024
v_accvgpr_write_b32 a13, v57
v_accvgpr_write_b32 a14, v58
v_accvgpr_write_b32 a15, v59
Mvav 160,132,40
s_lshl_b32 s80, s80, 14
Ra 144,242,12288
Ra 148,242,13312
v_accvgpr_write_b32 a16, v64
v_accvgpr_write_b32 a17, v65
Cvav 164,132,72,228
Dc 243,8,61,2048
v_accvgpr_write_b32 a18, v66
v_accvgpr_write_b32 a19, v67
v_accvgpr_write_b32 a20, v72
Mvav 164,128,64
s_add_u32 s80, s80, 98304
v_accvgpr_write_b32 a21, v73
v_accvgpr_write_b32 a22, v74
v_accvgpr_write_b32 a23, v75
v_accvgpr_write_b32 a24, v80
Cvav 168,128,96,232
Dc 243,8,61,3072
s_add_u32 s61, s61, s63
v_accvgpr_write_b32 a25, v81
v_accvgpr_write_b32 a26, v82
A 196,196,224
Mvav 168,132,104
s_add_u32 s36, s29, 65536
v_accvgpr_write_b32 a27, v83
A 197,197,224
A 198,198,224
v_accvgpr_write_b32 a28, v88
Cvav 172,132,136,236
Ra 152,242,14336
buffer_store_dwordx4 a[192:195], v1, s[8:11], s36 offen offset:0 sc1
A 199,199,224
v_accvgpr_write_b32 a29, v89
Mvav 172,128,128
Ra 156,242,15360
X 196
v_accvgpr_write_b32 a30, v90
Mvav 160,136,48
X 197
v_accvgpr_write_b32 a31, v91
v_accvgpr_write_b32 a32, v96
Mvav 160,140,56
buffer_store_dwordx4 a[200:203], v1, s[8:11], s36 offen offset:1024 sc1
X 198
K 192,196,197
Mvav 164,140,88
X 199
v_accvgpr_write_b32 a33, v97
A 204,204,228
Mvav 164,136,80
v_accvgpr_write_b32 a34, v98
K 193,198,199
v_accvgpr_write_b32 a35, v99
A 205,205,228
Mvav 168,136,112
A 206,206,228
v_accvgpr_write_b32 a36, v104
v_add_u32 v8, s80, v1
A 207,207,228
Mvav 168,140,120
buffer_store_dwordx4 a[208:211], v1, s[8:11], s36 offen offset:2048 sc1
X 204
v_accvgpr_write_b32 a37, v105
Mvav 172,140,152
X 205
v_accvgpr_write_b32 a38, v106
v_accvgpr_write_b32 a39, v107
Mvav 172,136,144
W 0
X 206
v_accvgpr_write_b32 a40, v112
K 200,204,205
Cvav 176,144,32,224
Ra 128,242,24576
X 207
v_accvgpr_write_b32 a41, v113
Mvav 176,148,40
buffer_store_dwordx4 a[216:219], v1, s[8:11], s36 offen offset:3072 sc1
s_add_u32 s36, s29, 69632
Ra 132,242,25600
A 212,212,232
v_accvgpr_write_b32 a42, v114
Cvav 180,148,72,228
K 201,206,207
A 213,213,232
v_accvgpr_write_b32 a43, v115
A 214,214,232
Mvav 180,144,64
A 215,215,232
v_accvgpr_write_b32 a44, v120
X 212
Cvav 184,144,96,232
buffer_store_dwordx4 a[224:227], v1, s[8:11], s36 offen offset:0 sc1
v_accvgpr_write_b32 a45, v121
X 213
Mvav 184,148,104
v_accvgpr_write_b32 a46, v122
X 214
v_accvgpr_write_b32 a47, v123
Cvav 188,148,136,236
K 208,212,213
v_accvgpr_write_b32 a48, v128
X 215
Mvav 188,144,128
buffer_store_dwordx4 a[232:235], v1, s[8:11], s36 offen offset:1024 sc1
v_accvgpr_write_b32 a49, v129
A 220,220,236
v_accvgpr_write_b32 a50, v130
Mvav 176,152,48
K 209,214,215
A 221,221,236
v_accvgpr_write_b32 a51, v131
A 222,222,236
Mvav 176,156,56
Ra 136,242,26624
A 223,223,236
v_accvgpr_write_b32 a52, v136
v_accvgpr_write_b32 a53, v137
Mvav 180,156,88
Ra 140,242,27648
buffer_store_dwordx4 a[240:243], v1, s[8:11], s36 offen offset:2048 sc1
X 220
Mvav 180,152,80
X 221
v_accvgpr_write_b32 a54, v138
v_accvgpr_write_b32 a55, v139
Mvav 184,152,112
v_accvgpr_write_b32 a56, v144
v_accvgpr_write_b32 a57, v145
v_accvgpr_write_b32 a58, v146
v_accvgpr_write_b32 a59, v147
v_accvgpr_write_b32 a60, v152
v_accvgpr_write_b32 a61, v153
v_accvgpr_write_b32 a62, v154
v_accvgpr_write_b32 a63, v155
Mvav 184,156,120
s_barrier
buffer_store_dwordx4 a[248:251], v1, s[8:11], s36 offen offset:3072 sc1
X 222
K 216,220,221
Mvav 188,156,152
X 223
A 36,36,224
K 217,222,223
A 37,37,224
A 38,38,224
A 39,39,224
X 36
X 37
X 38
K 194,36,37
X 39
A 68,68,228
K 195,38,39
A 69,69,228
A 70,70,228
A 71,71,228
X 68
X 69
X 70
K 202,68,69
X 71
A 100,100,232
K 203,70,71
A 101,101,232
A 102,102,232
A 103,103,232
X 100
X 101
X 102
K 210,100,101
X 103
A 132,132,236
K 211,102,103
A 133,133,236
A 134,134,236
A 135,135,236
X 132
X 133
X 134
K 218,132,133
X 135
s_nop 0
K 219,134,135
Mvav 188,152,144
W 0
s_add_u32 s36, s29, 393216
buffer_store_dwordx4 a[196:199], v1, s[8:11], s36 offen offset:0 sc1
Ra 144,242,28672
Ra 148,242,29696
Zvav 32,128,192
buffer_store_dwordx4 a[204:207], v1, s[8:11], s36 offen offset:1024 sc1
Ra 152,242,30720
Ra 156,242,31744
Zvav 64,128,200
buffer_store_dwordx4 a[212:215], v1, s[8:11], s36 offen offset:2048 sc1
v_xor_b32 v242, 0x10000, v242
Zvav 96,128,208
s_and_b32 s80, s14, 1
s_xor_b32 s62, s62, 0x10000
buffer_store_dwordx4 a[220:223], v1, s[8:11], s36 offen offset:3072 sc1
s_add_u32 s36, s29, 397312
Zvav 128,128,216
s_lshl_b32 s80, s80, 14
s_add_u32 s80, s80, 131072
buffer_store_dwordx4 a[228:231], v1, s[8:11], s36 offen offset:0 sc1
v_add_u32 v23, 61440, v21
Zvav 132,132,216
v_add_u32 v12, s80, v1
Ra 64,12,8192
Ra 68,12,9216
v_add_u32 v24, 61440, v22
Zvav 100,132,208
Ra 72,12,10240
buffer_store_dwordx4 a[236:239], v1, s[8:11], s36 offen offset:1024 sc1
Ra 76,12,11264
v_add_u32 v25, 122880, v21
Zvav 68,132,200
Ra 80,12,12288
Ra 84,12,13312
Ra 88,12,14336
v_add_u32 v26, 122880, v22
Zvav 36,132,192
Ra 92,12,15360
Ra 96,12,0
Ra 100,12,1024
Ra 104,12,2048
Zvav 40,136,192
buffer_store_dwordx4 a[244:247], v1, s[8:11], s36 offen offset:2048 sc1
Ra 108,12,3072
Ra 112,12,4096
Ra 116,12,5120
Zvav 72,136,200
Ra 120,12,6144
Ra 124,12,7168
X 172
Zvav 104,136,208
X 173
X 174
Zvav 136,136,216
buffer_store_dwordx4 a[252:255], v1, s[8:11], s36 offen offset:3072 sc1
K 220,172,173
X 175
Zvav 140,140,216
X 168
K 221,174,175
Zvav 108,140,208
X 169
X 170
Zvav 76,140,200
s_waitcnt vmcnt(16) lgkmcnt(0)
s_barrier
ds_read_b128 v[248:251], v8
K 212,168,169
Zvav 44,140,192
X 171
X 164
Zvav 48,144,192
Ra 128,242,0
K 213,170,171
X 165
Zvav 80,144,200
X 166
Zvav 112,144,208
Ra 132,242,1024
K 204,164,165
X 167
Zvav 144,144,216
X 160
Zvav 148,148,216
K 205,166,167
X 161
Zvav 116,148,208
Ra 136,242,2048
X 162
Zvav 84,148,200
W 3
K 196,160,161
X 163
Zvav 52,148,192
X 176
K 197,162,163
Zvav 56,152,192
Ra 140,242,3072
X 177
Zvav 88,152,200
X 178
K 198,176,177
Zvav 120,152,208
X 179
X 180
Zvav 152,152,216
K 199,178,179
Zvav 156,156,216
X 181
X 182
Zvav 124,156,208
K 206,180,181
X 183
Zvav 92,156,200
K 207,182,183
Zvav 60,156,192
s_waitcnt vmcnt(0)
s_add_u32 s80, s71, 128
v_mov_b64 v[28:29], s[12:13]
s_mov_b64 exec, 1
buffer_store_dwordx2 v[28:29], off, s[8:11], s80 sc1
s_mov_b64 exec, -1
s_branch .Lsp1_32%=
.p2align 12
.Lmoe_40%=:
W 0
Cvaa 160,128,0,224
s_cmp_eq_u32 s14, 0
s_cselect_b32 s20, s30, s32
s_cselect_b32 s21, s31, s33
Ra 144,242,4096
Ra 148,242,5120
Mvaa 160,132,4
s_xor_b32 s62, s62, 0x10000
Ra 152,242,6144
Cvaa 164,132,20,228
X 184
Mvaa 164,128,16
s_and_b32 s21, s21, 0xffff
X 185
Cvaa 168,128,32,232
X 186
Mvaa 168,132,36
Ra 156,242,7168
K 214,184,185
Cvaa 172,132,52,236
s_mov_b32 s22, 2211840
X 187
Mvaa 172,128,48
X 188
Mvaa 160,136,8
Ra 128,242,16384
Mvaa 160,140,12
s_mov_b32 s23, 0x20000
K 215,186,187
Mvaa 164,140,28
X 189
Mvaa 164,136,24
X 190
Mvaa 168,136,40
Ra 132,242,17408
v_lshrrev_b32 v4, 6, v1
Mvaa 168,140,44
K 222,188,189
Mvaa 172,140,60
X 191
Mvaa 172,136,56
v_mul_u32_u24 v4, 3456, v4
K 223,190,191
)"
R"(v_smfmac_f32_16x16x64_bf16 a[160:163], a[176:179], v[192:199], v240
Ra 136,242,18432
v_smfmac_f32_16x16x64_bf16 a[164:167], a[176:179], v[200:207], v240
X 160
v_smfmac_f32_16x16x64_bf16 a[168:171], a[176:179], v[208:215], v240
v_lshrrev_b32 v5, 4, v1
v_smfmac_f32_16x16x64_bf16 a[172:175], a[176:179], v[216:223], v240
W 3
X 161
Cvaa 176,144,0,224
X 162
Mvaa 176,148,4
Ra 140,242,19456
v_xor_b32 v5, v5, v1
Cvaa 180,148,20,228
K 192,160,161
Mvaa 180,144,16
X 163
Cvaa 184,144,32,232
v_and_b32 v5, 32, v5
Mvaa 184,148,36
X 164
Cvaa 188,148,52,236
K 193,162,163
v_and_b32 v6, 16, v1
Mvaa 188,144,48
X 165
Mvaa 176,152,8
X 166
Mvaa 176,156,12
Ra 144,242,20480
Ra 148,242,21504
Mvaa 180,156,28
v_add3_u32 v4, v4, v5, v6
K 200,164,165
Mvaa 180,152,24
X 167
Mvaa 184,152,40
X 168
Mvaa 184,156,44
v_add_u32 v5, 54272, v4
K 201,166,167
Mvaa 188,156,60
X 169
Mvaa 188,152,56
W 2
Ra 152,242,22528
v_add_u32 v6, 108544, v4
Mvav 32,128,196
X 170
Mvav 64,128,204
Ra 156,242,23552
v_add_u32 v7, 162816, v4
Mvav 96,128,212
K 208,168,169
Mvav 128,128,220
X 171
Mvav 132,132,220
s_lshl_b32 s94, s85, 6
X 172
Mvav 100,132,212
s_add_u32 s64, s64, 1
Ra 128,242,8192
K 209,170,171
Mvav 68,132,204
X 173
Mvav 36,132,196
s_mul_i32 s95, s14, 20480
Mvav 40,136,196
X 174
Mvav 72,136,204
Ra 132,242,9216
K 216,172,173
Mvav 104,136,212
s_add_u32 s95, s95, 98304
X 175
Mvav 136,136,220
Mvav 140,140,220
s_add_u32 m0, s95, 0
X 176
Mvav 108,140,212
Ra 136,242,10240
K 217,174,175
Mvav 76,140,204
X 177
Mvav 44,140,196
W 3
D 4,20,94,0
Mvav 48,144,196
X 178
Mvav 80,144,204
Ra 140,242,11264
Mvav 112,144,212
D 5,20,94,1024
K 194,176,177
Mvav 144,144,220
X 179
Mvav 148,148,220
X 180
Mvav 116,148,212
Ra 144,242,12288
D 6,20,94,2048
Mvav 84,148,204
K 195,178,179
Mvav 52,148,196
X 181
Mvav 56,152,196
D 7,20,94,3072
Ra 148,242,13312
Mvav 88,152,204
X 182
Mvav 120,152,212
s_add_u32 s94, s94, 221184
K 202,180,181
Mvav 152,152,220
X 183
Mvav 156,156,220
X 184
Mvav 124,156,212
s_add_u32 m0, s95, 4096
Ra 152,242,14336
Mvav 92,156,204
K 203,182,183
Mvav 60,156,196
W 3
X 185
Cvaa 160,128,0,224
D 4,20,94,0
Ra 156,242,15360
Mvaa 160,132,4
X 186
Cvaa 164,132,20,228
K 210,184,185
Mvaa 164,128,16
X 187
Cvaa 168,128,32,232
D 5,20,94,1024
Mvaa 168,132,36
X 188
Cvaa 172,132,52,236
D 6,20,94,2048
K 211,186,187
Mvaa 172,128,48
X 189
Mvaa 160,136,8
Ra 128,242,24576
Ra 132,242,25600
Mvaa 160,140,12
X 190
Mvaa 164,140,28
D 7,20,94,3072
K 218,188,189
Mvaa 164,136,24
s_add_u32 s94, s94, 221184
X 191
Mvaa 168,136,40
Mvaa 168,140,44
X 160
Mvaa 172,140,60
s_add_u32 m0, s95, 8192
K 219,190,191
Mvaa 172,136,56
W 2
X 161
Cvaa 176,144,0,224
Ra 136,242,26624
Ra 140,242,27648
Mvaa 176,148,4
X 162
Cvaa 180,148,20,228
D 4,20,94,0
K 196,160,161
Mvaa 180,144,16
X 163
Cvaa 184,144,32,232
D 5,20,94,1024
Mvaa 184,148,36
X 164
Cvaa 188,148,52,236
K 197,162,163
Mvaa 188,144,48
D 6,20,94,2048
Ra 144,242,28672
Mvaa 176,152,8
X 165
Mvaa 176,156,12
Ra 148,242,29696
D 7,20,94,3072
Mvaa 180,156,28
X 166
Mvaa 180,152,24
K 204,164,165
Mvaa 184,152,40
s_add_u32 s94, s94, 221184
X 167
Mvaa 184,156,44
X 168
Mvaa 188,156,60
Mvaa 188,152,56
W 2
s_add_u32 m0, s95, 12288
K 205,166,167
Mvav 32,128,192
X 169
Mvav 64,128,200
Ra 152,242,30720
Ra 156,242,31744
Mvav 96,128,208
X 170
Mvav 128,128,216
D 4,20,94,0
v_xor_b32 v242, 0x10000, v242
Mvav 132,132,216
K 212,168,169
Mvav 100,132,208
X 171
Mvav 68,132,200
D 5,20,94,1024
Mvav 36,132,192
X 172
Mvav 40,136,192
D 6,20,94,2048
K 213,170,171
Mvav 72,136,200
X 173
Mvav 104,136,208
X 174
Mvav 136,136,216
D 7,20,94,3072
K 220,172,173
Mvav 140,140,216
X 175
Mvav 108,140,208
s_add_u32 s94, s94, 221184
X 176
Mvav 76,140,200
W 0
s_barrier
Mvav 44,140,192
Ra 128,242,0
K 221,174,175
Mvav 48,144,192
X 177
Mvav 80,144,200
v_bfe_u32 v11, v1, 4, 3
Mvav 112,144,208
X 178
Mvav 144,144,216
Ra 132,242,1024
v_lshlrev_b32 v11, 3, v11
Mvav 148,148,216
K 198,176,177
Mvav 116,148,208
X 179
Mvav 84,148,200
X 180
Mvav 52,148,192
Ra 136,242,2048
buffer_load_dwordx2 v[8:9], v11, s[8:11], 0 offen offset:3840 sc1
Mvav 56,152,192
Ra 140,242,3072
K 199,178,179
Mvav 88,152,200
X 181
Mvav 120,152,208
X 182
Mvav 152,152,216
K 206,180,181
Mvav 156,156,216
X 183
Mvav 124,156,208
Mvav 92,156,200
K 207,182,183
Mvav 60,156,192
W 0
Cvaa 160,128,0,224
s_add_u32 m0, s95, 16384
Ra 144,242,4096
Ra 148,242,5120
Mvaa 160,132,4
s_xor_b32 s62, s62, 0x10000
Ra 152,242,6144
Cvaa 164,132,20,228
X 184
Mvaa 164,128,16
X 185
Cvaa 168,128,32,232
X 186
Mvaa 168,132,36
D 4,20,94,0
K 214,184,185
Cvaa 172,132,52,236
Ra 156,242,7168
Mvaa 172,128,48
X 187
Mvaa 160,136,8
X 188
Mvaa 160,140,12
D 5,20,94,1024
Ra 128,242,16384
Mvaa 164,140,28
K 215,186,187
Mvaa 164,136,24
X 189
Mvaa 168,136,40
X 190
Mvaa 168,140,44
D 6,20,94,2048
Ra 132,242,17408
Mvaa 172,140,60
K 222,188,189
Mvaa 172,136,56
X 191
v_smfmac_f32_16x16x64_bf16 a[160:163], a[176:179], v[192:199], v240
Ra 136,242,18432
K 223,190,191
v_smfmac_f32_16x16x64_bf16 a[164:167], a[176:179], v[200:207], v240
D 7,20,94,3072
v_smfmac_f32_16x16x64_bf16 a[168:171], a[176:179], v[208:215], v240
s_add_u32 s94, s94, 221184
v_mul_u32_u24 v10, 160, v11
v_smfmac_f32_16x16x64_bf16 a[172:175], a[176:179], v[216:223], v240
W 3
X 160
Cvaa 176,144,0,224
X 161
Mvaa 176,148,4
Ra 140,242,19456
v_lshrrev_b32 v12, 7, v1
Cvaa 180,148,20,228
X 162
Mvaa 180,144,16
K 192,160,161
v_lshl_add_u32 v10, v12, 4, v10
Cvaa 184,144,32,232
s_mul_i32 s82, s14, 5120
X 163
Mvaa 184,148,36
X 164
Cvaa 188,148,52,236
K 193,162,163
Mvaa 188,144,48
s_mul_i32 s81, s14, 640
X 165
Mvaa 176,152,8
X 166
Mvaa 176,156,12
s_add_u32 s81, s81, 118784
Ra 144,242,20480
Ra 148,242,21504
Mvaa 180,156,28
K 200,164,165
Mvaa 180,152,24
X 167
Mvaa 184,152,40
s_waitcnt vmcnt(4)
v_cmp_eq_u64 vcc, s[12:13], v[8:9]
s_cmp_eq_u64 vcc, -1
s_cbranch_scc0 .Lspin_47%=
.Lok_48%=:
X 168
Mvaa 184,156,44
K 201,166,167
Mvaa 188,156,60
s_add_u32 m0, s82, 53248
X 169
Mvaa 188,152,56
W 2
Ra 152,242,22528
Mvav 32,128,196
X 170
Mvav 64,128,204
Ra 156,242,23552
Dc 10,8,81,0
Mvav 96,128,212
K 208,168,169
Mvav 128,128,220
X 171
Mvav 132,132,220
s_add_u32 m0, s82, 54144
X 172
Mvav 100,132,212
s_add_u32 s64, s64, 1
Ra 128,242,8192
K 209,170,171
Mvav 68,132,204
X 173
Mvav 36,132,196
Dc 10,8,81,128
Mvav 40,136,196
X 174
Mvav 72,136,204
s_add_u32 m0, s82, 55040
Ra 132,242,9216
K 216,172,173
Mvav 104,136,212
X 175
Mvav 136,136,220
Dc 10,8,81,256
Mvav 140,140,220
)"
R"(X 176
Mvav 108,140,212
Ra 136,242,10240
K 217,174,175
Mvav 76,140,204
s_add_u32 m0, s82, 55936
X 177
Mvav 44,140,196
W 3
Mvav 48,144,196
X 178
Mvav 80,144,204
Ra 140,242,11264
Dc 10,8,81,384
Mvav 112,144,212
K 194,176,177
Mvav 144,144,220
s_add_u32 m0, s82, 56832
X 179
Mvav 148,148,220
X 180
Mvav 116,148,212
Ra 144,242,12288
Mvav 84,148,204
Dc 10,8,81,512
K 195,178,179
Mvav 52,148,196
X 181
Mvav 56,152,196
X 182
Mvav 88,152,204
Ra 148,242,13312
Mvav 120,152,212
K 202,180,181
Mvav 152,152,220
X 183
Mvav 156,156,220
X 184
Mvav 124,156,212
Ra 152,242,14336
Mvav 92,156,204
K 203,182,183
Mvav 60,156,196
W 3
X 185
Cvaa 160,128,0,224
X 186
Mvaa 160,132,4
Ra 156,242,15360
Cvaa 164,132,20,228
K 210,184,185
Mvaa 164,128,16
X 187
Cvaa 168,128,32,232
X 188
Mvaa 168,132,36
Cvaa 172,132,52,236
K 211,186,187
Mvaa 172,128,48
X 189
Mvaa 160,136,8
Ra 128,242,24576
Ra 132,242,25600
Mvaa 160,140,12
X 190
Mvaa 164,140,28
K 218,188,189
Mvaa 164,136,24
X 191
Mvaa 168,136,40
Mvaa 168,140,44
Mvaa 172,140,60
X 160
Mvaa 172,136,56
W 2
Ra 136,242,26624
K 219,190,191
Cvaa 176,144,0,224
X 161
Mvaa 176,148,4
Ra 140,242,27648
v_lshrrev_b32 v8, 8, v1
Cvaa 180,148,20,228
X 162
Mvaa 180,144,16
K 196,160,161
Cvaa 184,144,32,232
X 163
Mvaa 184,148,36
v_bfe_u32 v9, v1, 6, 2
Cvaa 188,148,52,236
X 164
Mvaa 188,144,48
K 197,162,163
v_lshl_add_u32 v9, v8, 3, v9
Mvaa 176,152,8
X 165
Mvaa 176,156,12
Ra 144,242,28672
Ra 148,242,29696
Mvaa 180,156,28
X 166
Mvaa 180,152,24
K 204,164,165
v_lshlrev_b32 v11, 6, v9
Mvaa 184,152,40
X 167
Mvaa 184,156,44
X 168
Mvaa 188,156,60
v_xor_b32 v9, s14, v8
Mvaa 188,152,56
W 2
Ra 152,242,30720
K 205,166,167
Mvav 32,128,192
X 169
Mvav 64,128,200
Ra 156,242,31744
v_and_b32 v9, 1, v9
Mvav 96,128,208
X 170
Mvav 128,128,216
v_xor_b32 v242, 0x10000, v242
K 212,168,169
Mvav 132,132,216
X 171
Mvav 100,132,208
v_lshl_add_u32 v11, v9, 5, v11
Mvav 68,132,200
X 172
Mvav 36,132,192
K 213,170,171
Mvav 40,136,192
X 173
Mvav 72,136,200
v_bfe_u32 v9, v1, 4, 2
Mvav 104,136,208
X 174
Mvav 136,136,216
K 220,172,173
v_lshl_add_u32 v12, v9, 3, v11
Mvav 140,140,216
X 175
Mvav 108,140,208
X 176
Mvav 76,140,200
s_waitcnt vmcnt(0) lgkmcnt(0)
s_barrier
Mvav 44,140,192
v_add_u32 v11, 98304, v12
K 221,174,175
Mvav 48,144,192
Ra 128,242,0
Mvav 80,144,200
X 177
Mvav 112,144,208
X 178
Mvav 144,144,216
Ra 132,242,1024
v_and_b32 v13, 0x70, v1
Mvav 148,148,216
K 198,176,177
Mvav 116,148,208
X 179
Mvav 84,148,200
Ra 136,242,2048
v_lshl_add_u32 v13, v8, 7, v13
Mvav 52,148,192
X 180
Mvav 56,152,192
Ra 140,242,3072
K 199,178,179
Mvav 88,152,200
X 181
Mvav 120,152,208
X 182
Mvav 152,152,216
K 206,180,181
Mvav 156,156,216
X 183
Mvav 124,156,208
Mvav 92,156,200
K 207,182,183
Mvav 60,156,192
W 0
Cvaa 160,128,0,224
s_cmp_eq_u32 s14, 0
s_cselect_b32 s95, 106496, 0
Ra 144,242,4096
Ra 148,242,5120
Mvaa 160,132,4
s_xor_b32 s62, s62, 0x10000
X 184
Cvaa 164,132,20,228
Ta 184,11,0
Ta 186,11,256
Mvaa 164,128,16
s_add_u32 s95, s95, 32768
Ta 188,11,20480
Ta 190,11,20736
Cvaa 168,128,32,232
Ra 192,13,53248
Ta 196,11,2048
Mvaa 168,132,36
s_add_u32 m0, s95, 0
X 185
Cvaa 172,132,52,236
Ta 198,11,2304
Ta 200,11,22528
Mvaa 172,128,48
W 3
X 186
Zaaa 232,184,192
Ta 202,11,22784
K 214,184,185
Zaaa 236,188,192
Ra 204,13,53760
Ra 152,242,6144
Mvaa 160,136,8
X 187
Mvaa 160,140,12
Ta 208,11,4096
D 4,20,94,0
Mvaa 164,140,28
X 188
Mvaa 164,136,24
Ta 210,11,4352
D 5,20,94,1024
Mvaa 168,136,40
Ra 156,242,7168
K 215,186,187
Mvaa 168,140,44
X 189
Mvaa 172,140,60
Ta 212,11,24576
D 6,20,94,2048
Mvaa 172,136,56
X 190
v_smfmac_f32_16x16x64_bf16 a[160:163], a[176:179], v[192:199], v240
W 5
Ta 214,11,24832
D 7,20,94,3072
Maaa 232,196,204
s_add_u32 s94, s94, 221184
Ra 216,13,54272
K 222,188,189
Maaa 236,200,204
s_add_u32 m0, s95, 4096
X 191
v_smfmac_f32_16x16x64_bf16 a[164:167], a[176:179], v[200:207], v240
X 160
K 223,190,191
v_smfmac_f32_16x16x64_bf16 a[168:171], a[176:179], v[208:215], v240
Ra 128,242,16384
Ta 220,11,6144
v_smfmac_f32_16x16x64_bf16 a[172:175], a[176:179], v[216:223], v240
W 5
Ta 222,11,6400
Ra 132,242,17408
Cvaa 176,144,0,224
X 161
Mvaa 176,148,4
D 4,20,94,0
Ta 224,11,26624
Cvaa 180,148,20,228
X 162
Mvaa 180,144,16
D 5,20,94,1024
K 192,160,161
Cvaa 184,144,32,232
Ta 226,11,26880
Ra 228,13,54784
Mvaa 184,148,36
W 7
X 163
Maaa 232,208,216
D 6,20,94,2048
Ra 136,242,18432
Maaa 236,212,216
X 164
Cvaa 188,148,52,236
D 7,20,94,3072
Ta 184,11,8192
Mvaa 188,144,48
s_add_u32 s94, s94, 221184
Ra 140,242,19456
K 193,162,163
Mvaa 176,152,8
s_add_u32 m0, s95, 8192
X 165
Mvaa 176,156,12
Ta 186,11,8448
Ta 188,11,28672
Mvaa 180,156,28
X 166
Mvaa 180,152,24
Ra 144,242,20480
K 200,164,165
Mvaa 184,152,40
X 167
Mvaa 184,156,44
Ta 190,11,28928
D 4,20,94,0
Mvaa 188,156,60
W 7
X 168
Maaa 232,220,228
s_add_u32 s64, s64, 1
Ra 192,13,55296
D 5,20,94,1024
Maaa 236,224,228
Ta 196,11,10240
K 201,166,167
Mvaa 188,152,56
W 6
X 169
Mvav 32,128,196
D 6,20,94,2048
Ra 148,242,21504
Mvav 64,128,204
X 170
Mvav 96,128,212
Ta 198,11,10496
D 7,20,94,3072
Mvav 128,128,220
s_add_u32 s94, s94, 221184
Ta 200,11,30720
K 208,168,169
Mvav 132,132,220
s_add_u32 m0, s95, 12288
X 171
Mvav 100,132,212
Ra 152,242,22528
Ta 202,11,30976
Mvav 68,132,204
X 172
Mvav 36,132,196
W 6
Ra 204,13,55808
K 209,170,171
Maaa 232,184,192
X 173
Maaa 236,188,192
D 4,20,94,0
Ra 156,242,23552
Mvav 40,136,196
X 174
Mvav 72,136,204
Ta 208,11,12288
D 5,20,94,1024
Mvav 104,136,212
Ta 210,11,12544
K 216,172,173
Mvav 136,136,220
X 175
Mvav 140,140,220
D 6,20,94,2048
Ra 128,242,8192
Mvav 108,140,212
Ta 212,11,32768
Ta 214,11,33024
Mvav 76,140,204
X 176
Mvav 44,140,196
W 5
D 7,20,94,3072
K 217,174,175
Mvav 48,144,196
s_add_u32 s94, s94, 221184
X 177
Maaa 232,196,204
s_add_u32 m0, s95, 16384
Ra 132,242,9216
Ra 216,13,56320
Maaa 236,200,204
X 178
Mvav 80,144,204
Ta 220,11,14336
K 194,176,177
Mvav 112,144,212
X 179
Mvav 144,144,220
D 4,20,94,0
Ta 222,11,14592
Mvav 148,148,220
Ra 136,242,10240
Ta 224,11,34816
Mvav 116,148,212
X 180
Mvav 84,148,204
D 5,20,94,1024
K 195,178,179
Mvav 52,148,196
X 181
Mvav 56,152,196
D 6,20,94,2048
Ta 226,11,35072
Mvav 88,152,204
W 5
)"
R"(Ra 140,242,11264
Ra 228,13,56832
Maaa 232,208,216
X 182
Maaa 236,212,216
D 7,20,94,3072
K 202,180,181
Mvav 120,152,212
s_add_u32 s94, s94, 221184
X 183
Mvav 152,152,220
Ta 184,11,16384
Ra 144,242,12288
Mvav 156,156,220
X 184
Mvav 124,156,212
Ta 186,11,16640
K 203,182,183
Mvav 92,156,204
X 185
Mvav 60,156,196
W 4
Ta 188,11,36864
Ra 148,242,13312
Cvaa 160,128,0,224
X 186
Mvaa 160,132,4
Ta 190,11,37120
K 210,184,185
Cvaa 164,132,20,228
W 6
Ra 192,13,57344
Ta 196,11,18432
Maaa 232,220,228
X 187
Maaa 236,224,228
X 188
Mvaa 164,128,16
Ra 152,242,14336
K 211,186,187
Cvaa 168,128,32,232
Ta 198,11,18688
Ta 200,11,38912
Mvaa 168,132,36
X 189
Cvaa 172,132,52,236
X 190
Mvaa 172,128,48
Ra 156,242,15360
K 218,188,189
Mvaa 160,136,8
Ta 202,11,39168
Ra 204,13,57856
Mvaa 160,140,12
X 191
Mvaa 164,140,28
Ra 128,242,24576
Mvaa 164,136,24
W 8
Maaa 232,184,192
Ra 132,242,25600
Maaa 236,188,192
X 160
Mvaa 168,136,40
K 219,190,191
Mvaa 168,140,44
X 161
Mvaa 172,140,60
X 162
Mvaa 172,136,56
W 4
Ra 136,242,26624
K 196,160,161
Cvaa 176,144,0,224
X 163
Mvaa 176,148,4
X 164
Cvaa 180,148,20,228
Ra 140,242,27648
K 197,162,163
Mvaa 180,144,16
X 165
Cvaa 184,144,32,232
W 4
Maaa 232,196,204
X 166
Maaa 236,200,204
Mvaa 184,148,36
K 204,164,165
Cvaa 188,148,52,236
X 167
Mvaa 188,144,48
Ra 144,242,28672
Mvaa 176,152,8
X 168
Mvaa 176,156,12
Ra 148,242,29696
Mvaa 180,156,28
K 205,166,167
Mvaa 180,152,24
X 169
Mvaa 184,152,40
Mvaa 184,156,44
X 170
Mvaa 188,156,60
K 212,168,169
Mvaa 188,152,56
W 2
X 171
Mvav 32,128,192
Ra 152,242,30720
Ra 156,242,31744
Mvav 64,128,200
X 172
Mvav 96,128,208
v_xor_b32 v242, 0x10000, v242
Mvav 128,128,216
K 213,170,171
Mvav 132,132,216
X 173
Mvav 100,132,208
Mvav 68,132,200
X 174
Mvav 36,132,192
Mvav 40,136,192
K 220,172,173
Mvav 72,136,200
X 175
Mvav 104,136,208
Mvav 136,136,216
X 176
Mvav 140,140,216
Mvav 108,140,208
K 221,174,175
Mvav 76,140,200
s_waitcnt vmcnt(0) lgkmcnt(0)
s_barrier
Mvav 44,140,192
X 177
Mvav 48,144,192
X 178
Mvav 80,144,200
Ra 128,242,0
K 198,176,177
Mvav 112,144,208
X 179
Mvav 144,144,216
Ra 132,242,1024
Mvav 148,148,216
X 180
Mvav 116,148,208
Ra 136,242,2048
Mvav 84,148,200
K 199,178,179
Mvav 52,148,192
X 181
Mvav 56,152,192
Ra 140,242,3072
Mvav 88,152,200
X 182
Mvav 120,152,208
Mvav 152,152,216
K 206,180,181
Mvav 156,156,216
X 183
Mvav 124,156,208
Mvav 92,156,200
K 207,182,183
Mvav 60,156,192
W 0
Cvaa 160,128,0,224
s_mov_b32 s20, s34
Ra 144,242,4096
Ra 148,242,5120
Mvaa 160,132,4
s_xor_b32 s62, s62, 0x10000
X 184
Cvaa 164,132,20,228
s_and_b32 s21, s35, 0xffff
Ta 184,11,40960
Ta 186,11,41216
Mvaa 164,128,16
Ta 188,12,32768
Ta 190,12,33024
Cvaa 168,128,32,232
s_mov_b32 s22, 2211840
Ra 192,13,58368
Ta 196,11,43008
Mvaa 168,132,36
s_mov_b32 s23, 0x20000
X 185
Cvaa 172,132,52,236
Ta 198,11,43264
Ta 200,12,34816
Mvaa 172,128,48
W 3
X 186
Maaa 232,184,192
Ta 202,12,35072
K 214,184,185
Maaa 236,188,192
Ra 204,13,58880
Ra 152,242,6144
Mvaa 160,136,8
X 187
Mvaa 160,140,12
Ta 208,11,45056
v_lshrrev_b32 v4, 8, v1
Mvaa 164,140,28
X 188
Mvaa 164,136,24
Ta 210,11,45312
v_lshlrev_b32 v5, 5, v4
Mvaa 168,136,40
v_xor_b32 v5, v5, v1
K 215,186,187
Mvaa 168,140,44
Ra 156,242,7168
Ta 212,12,36864
Mvaa 172,140,60
X 189
Mvaa 172,136,56
v_and_b32 v5, 0xf0, v5
v_mul_u32_u24 v4, 1280, v4
v_smfmac_f32_16x16x64_bf16 a[160:163], a[176:179], v[192:199], v240
W 5
X 190
Maaa 232,196,204
Ta 214,12,37120
v_add_u32 v4, v4, v5
Maaa 236,200,204
Ra 216,13,59392
K 222,188,189
v_smfmac_f32_16x16x64_bf16 a[164:167], a[176:179], v[200:207], v240
X 191
X 160
K 223,190,191
v_smfmac_f32_16x16x64_bf16 a[168:171], a[176:179], v[208:215], v240
Ra 128,242,16384
v_xor_b32 v5, 128, v4
v_smfmac_f32_16x16x64_bf16 a[172:175], a[176:179], v[216:223], v240
W 4
Ta 220,11,47104
v_add_u32 v6, 8192, v4
Cvaa 176,144,0,224
Ta 222,11,47360
v_add_u32 v7, 12288, v5
Mvaa 176,148,4
Ra 132,242,17408
v_add_u32 v5, 4096, v5
Cvaa 180,148,20,228
s_mul_i32 s94, s85, 40960
X 161
Mvaa 180,144,16
s_mul_i32 s81, s14, 20480
Ta 224,12,38912
Ta 226,12,39168
Cvaa 184,144,32,232
s_add_u32 s94, s94, s81
X 162
Mvaa 184,148,36
W 6
s_lshl_b32 s95, s14, 12
K 192,160,161
Maaa 232,208,216
s_add_u32 s95, s95, 98304
X 163
Maaa 236,212,216
s_add_u32 m0, s95, 0
Ra 228,13,59904
Ra 136,242,18432
Cvaa 188,148,52,236
X 164
Mvaa 188,144,48
Ta 184,11,49152
K 193,162,163
Mvaa 176,152,8
X 165
Mvaa 176,156,12
Ra 140,242,19456
Ta 186,11,49408
Mvaa 180,156,28
D 4,20,94,0
Ta 188,12,40960
Mvaa 180,152,24
X 166
Mvaa 184,152,40
D 5,20,94,1024
K 200,164,165
Mvaa 184,156,44
X 167
Mvaa 188,156,60
W 5
D 6,20,94,2048
Ra 144,242,20480
Maaa 232,220,228
s_add_u32 s64, s64, 1
Ta 190,12,41216
Ra 192,13,60416
Maaa 236,224,228
D 7,20,94,3072
Ta 196,11,51200
Mvaa 188,152,56
W 6
X 168
Mvav 32,128,196
s_add_u32 s94, s94, 256
s_add_u32 m0, s95, 8192
K 201,166,167
Mvav 64,128,204
X 169
Mvav 96,128,212
D 4,20,94,0
Ra 148,242,21504
Mvav 128,128,220
X 170
Mvav 132,132,220
D 5,20,94,1024
Ta 198,11,51456
Mvav 100,132,212
Ta 200,12,43008
K 208,168,169
Mvav 68,132,204
D 6,20,94,2048
Ra 152,242,22528
Mvav 36,132,196
W 5
X 171
Maaa 232,184,192
D 7,20,94,3072
Ta 202,12,43264
Maaa 236,188,192
s_add_u32 s94, s94, 256
X 172
Mvav 40,136,196
s_add_u32 m0, s95, 16384
Ra 204,13,60928
K 209,170,171
Mvav 72,136,204
X 173
Mvav 104,136,212
Ra 156,242,23552
D 4,20,94,0
Mvav 136,136,220
X 174
Mvav 140,140,220
D 5,20,94,1024
Ta 208,11,53248
Mvav 108,140,212
Ta 210,11,53504
K 216,172,173
Mvav 76,140,204
D 6,20,94,2048
Ra 128,242,8192
Mvav 44,140,196
W 3
X 175
Mvav 48,144,196
D 7,20,94,3072
Ta 212,12,45056
Maaa 232,196,204
s_add_u32 s94, s94, 256
X 176
Maaa 236,200,204
s_add_u32 m0, s95, 24576
Ta 214,12,45312
K 217,174,175
Mvav 80,144,204
D 4,20,94,0
Ra 132,242,9216
Mvav 112,144,212
X 177
Mvav 144,144,220
Ra 216,13,61440
D 5,20,94,1024
Mvav 148,148,220
X 178
Mvav 116,148,212
D 6,20,94,2048
Ta 220,11,55296
Mvav 84,148,204
Ta 222,11,55552
K 194,176,177
Mvav 52,148,196
D 7,20,94,3072
Ra 136,242,10240
Mvav 56,152,196
s_add_u32 s94, s94, 256
X 179
Mvav 88,152,204
W 3
X 180
Maaa 232,208,216
s_add_u32 m0, s95, 32768
Ta 224,12,47104
K 195,178,179
Maaa 236,212,216
D 4,20,94,0
Ta 226,12,47360
Mvav 120,152,212
)"
R"(Ra 140,242,11264
Ra 228,13,61952
Mvav 152,152,220
X 181
Mvav 156,156,220
D 5,20,94,1024
Ta 184,11,57344
Mvav 124,156,212
X 182
Mvav 92,156,204
D 6,20,94,2048
K 202,180,181
Mvav 60,156,196
W 2
X 183
Cvaa 160,128,0,224
D 7,20,94,3072
Ra 144,242,12288
Mvaa 160,132,4
s_add_u32 s94, s94, 256
X 184
Cvaa 164,132,20,228
W 2
Ta 186,11,57600
K 203,182,183
Maaa 232,220,228
X 185
Maaa 236,224,228
Ta 188,12,49152
Ra 148,242,13312
Mvaa 164,128,16
X 186
Cvaa 168,128,32,232
Ta 190,12,49408
K 210,184,185
Mvaa 168,132,36
Ra 192,13,62464
Ta 196,11,59392
Cvaa 172,132,52,236
X 187
Mvaa 172,128,48
X 188
Mvaa 160,136,8
Ra 152,242,14336
K 211,186,187
Mvaa 160,140,12
Ta 198,11,59648
Ta 200,12,51200
Mvaa 164,140,28
X 189
Mvaa 164,136,24
W 4
X 190
Maaa 232,184,192
Ra 156,242,15360
K 218,188,189
Maaa 236,188,192
Ta 202,12,51456
Ra 204,13,62976
Mvaa 168,136,40
X 191
Mvaa 168,140,44
Ra 128,242,24576
Ra 132,242,25600
Mvaa 172,140,60
X 160
Mvaa 172,136,56
W 4
Ra 136,242,26624
K 219,190,191
Cvaa 176,144,0,224
X 161
Mvaa 176,148,4
X 162
Cvaa 180,148,20,228
Ra 140,242,27648
K 196,160,161
Mvaa 180,144,16
X 163
Cvaa 184,144,32,232
W 4
X 164
Maaa 232,196,204
K 197,162,163
Maaa 236,200,204
X 165
Mvaa 184,148,36
X 166
Cvaa 188,148,52,236
K 204,164,165
Mvaa 188,144,48
X 167
Mvaa 176,152,8
Ra 144,242,28672
G 252,232
Mvaa 176,156,12
G 253,233
G 254,234
Mvaa 180,156,28
X 168
Mvaa 180,152,24
G 255,235
G 8,236
Mvaa 184,152,40
Ra 148,242,29696
G 9,237
Mvaa 184,156,44
G 10,238
G 14,239
Mvaa 188,156,60
K 252,252,8
K 253,253,9
Mvaa 188,152,56
W 2
K 205,166,167
K 254,254,10
Mvav 32,128,192
K 255,255,14
v_and_b32 v8, 0xffff0000, v252
Mvav 64,128,200
v_and_b32 v9, 0xffff0000, v253
v_and_b32 v10, 0xffff0000, v254
Mvav 96,128,208
X 169
Mvav 128,128,216
v_and_b32 v14, 0xffff0000, v255
v_lshlrev_b32 v252, 16, v252
Mvav 132,132,216
v_lshlrev_b32 v253, 16, v253
v_lshlrev_b32 v254, 16, v254
Mvav 100,132,208
v_lshlrev_b32 v255, 16, v255
v_mul_f32 v15, 0xbfb8aa3b, v252
Mvav 68,132,200
v_mul_f32 v2, 0xbfb8aa3b, v253
v_mul_f32 v3, 0xbfb8aa3b, v254
Mvav 36,132,192
X 170
Mvav 40,136,192
v_mul_f32 v27, 0xbfb8aa3b, v255
K 212,168,169
Mvav 72,136,200
X 15
Mvav 104,136,208
Ra 152,242,30720
Ra 156,242,31744
Mvav 136,136,216
X 2
X 3
X 27
v_add_f32 v15, 1.0, v15
v_add_f32 v2, 1.0, v2
v_add_f32 v3, 1.0, v3
v_add_f32 v27, 1.0, v27
v_rcp_f32 v15, v15
v_rcp_f32 v2, v2
v_rcp_f32 v3, v3
v_rcp_f32 v27, v27
U 252,252,15
U 253,253,2
U 254,254,3
U 255,255,27
K 252,252,252
K 253,253,253
K 254,254,254
K 255,255,255
v_lshlrev_b32 v252, 16, v252
v_lshlrev_b32 v253, 16, v253
v_lshlrev_b32 v254, 16, v254
v_lshlrev_b32 v255, 16, v255
U 252,252,8
U 253,253,9
U 254,254,10
U 255,255,14
K 246,252,253
K 247,254,255
s_lshl_b32 s81, s14, 3
v_add_u32 v2, s81, v1
s_mov_b32 s86, 0x00ff00ff
s_mov_b32 s87, 0x00ff00ff
v_cndmask_b32_e64 v252, 0, v246, s[86:87]
v_cndmask_b32_e64 v253, 0, v247, s[86:87]
v_cndmask_b32_e64 v246, v246, 0, s[86:87]
v_cndmask_b32_e64 v247, v247, 0, s[86:87]
ds_write_b64 v2, v[252:253] offset:63488
ds_write_b64 v2, v[246:247] offset:64512
Mvav 140,140,216
X 171
Mvav 108,140,208
v_xor_b32 v242, 0x10000, v242
Mvav 76,140,200
s_waitcnt vmcnt(0) lgkmcnt(0)
s_barrier
Mvav 44,140,192
X 172
Mvav 48,144,192
Ra 128,242,0
K 213,170,171
Mvav 80,144,200
X 173
Mvav 112,144,208
X 174
Mvav 144,144,216
Ra 132,242,1024
K 220,172,173
Mvav 148,148,216
X 175
Mvav 116,148,208
X 176
Mvav 84,148,200
Ra 136,242,2048
K 221,174,175
Mvav 52,148,192
X 177
Mvav 56,152,192
X 178
Mvav 88,152,200
Ra 140,242,3072
K 198,176,177
Mvav 120,152,208
X 179
Mvav 152,152,216
X 180
Mvav 156,156,216
K 199,178,179
Mvav 124,156,208
X 181
Mvav 92,156,200
X 182
Mvav 60,156,192
K 206,180,181
X 183
s_nop 0
K 207,182,183
W 0
Cvaa 160,128,0,224
Ra 144,242,4096
Ra 148,242,5120
Mvaa 160,132,4
s_xor_b32 s62, s62, 0x10000
X 184
Cvaa 164,132,20,228
Ra 216,1,63488
Ra 220,1,64512
Mvaa 164,128,16
v_lshrrev_b32 v8, 6, v1
v_and_b32 v9, 7, v8
Cvaa 168,128,32,232
s_lshl_b32 s81, s14, 2
X 185
Mvaa 168,132,36
v_xor_b32 v9, s81, v9
v_lshlrev_b32 v9, 5, v9
Cvaa 172,132,52,236
X 186
Mvaa 172,128,48
v_lshl_add_u32 v4, v8, 8, v9
v_bfe_u32 v9, v1, 4, 2
Mvaa 160,136,8
v_lshl_add_u32 v4, v9, 3, v4
v_add_u32 v4, 98304, v4
Mvaa 160,140,12
K 214,184,185
v_xor_b32 v5, 32, v4
Mvaa 164,140,28
v_xor_b32 v6, 64, v4
v_xor_b32 v7, 96, v4
Ta 184,4,0
Ta 186,4,4096
Mvaa 164,136,24
Ta 188,5,0
Ta 190,5,4096
Mvaa 168,136,40
Ra 152,242,6144
Ta 192,6,0
Mvaa 168,140,44
W 4
X 187
Zaaa 232,184,216
Ta 194,6,4096
v_and_b32 v11, 0x70, v1
Mvaa 172,140,60
v_mul_u32_u24 v11, 160, v11
v_lshrrev_b32 v10, 8, v1
Mvaa 172,136,56
X 188
v_smfmac_f32_16x16x64_bf16 a[160:163], a[176:179], v[192:199], v240
Ra 156,242,7168
W 4
Maaa 232,188,220
K 215,186,187
v_lshl_add_u32 v11, v10, 4, v11
v_smfmac_f32_16x16x64_bf16 a[164:167], a[176:179], v[200:207], v240
X 189
X 190
K 222,188,189
X 191
X 160
K 223,190,191
v_smfmac_f32_16x16x64_bf16 a[168:171], a[176:179], v[208:215], v240
Ta 196,7,0
v_bfe_u32 v10, v1, 7, 1
v_smfmac_f32_16x16x64_bf16 a[172:175], a[176:179], v[216:223], v240
W 2
Ta 198,7,4096
v_lshl_add_u32 v10, v10, 6, v11
Zaaa 236,192,216
W 2
s_mul_i32 s94, s85, 20480
s_lshl_b32 s82, s14, 8
Cvaa 176,144,0,224
s_add_u32 s94, s94, s82
Ta 200,4,8192
Ta 202,4,12288
Mvaa 176,148,4
s_add_u32 s94, s94, 2752512
s_lshl_b32 s71, s85, 4
Ra 128,242,16384
Cvaa 180,148,20,228
W 3
s_lshl_b32 s82, s14, 3
Ta 204,5,8192
Maaa 236,196,220
s_add_u32 s71, s71, s82
s_add_u32 s71, s71, 2560
Ta 206,5,12288
Mvaa 180,144,16
X 161
Cvaa 184,144,32,232
Ra 132,242,17408
Ta 208,6,8192
Mvaa 184,148,36
W 5
X 162
Zaaa 240,200,216
Ta 210,6,12288
K 192,160,161
Cvaa 188,148,52,236
buffer_store_dwordx4 a[232:235], v10, s[8:11], s94 offen offset:0 sc1
Ta 212,7,8192
Mvaa 188,144,48
X 163
Mvaa 176,152,8
W 4
X 164
Maaa 240,204,220
Ra 136,242,18432
Ta 214,7,12288
Mvaa 176,156,12
Ta 184,4,16384
K 193,162,163
Mvaa 180,156,28
X 165
Mvaa 180,152,24
W 4
Ta 186,4,20480
Ra 140,242,19456
Zaaa 244,208,216
X 166
Mvaa 184,152,40
buffer_store_dwordx4 a[236:239], v10, s[8:11], s94 offen offset:128 sc1
K 200,164,165
Mvaa 184,156,44
X 167
Mvaa 188,156,60
W 3
Ta 188,5,16384
Ta 190,5,20480
Maaa 244,212,220
s_add_u32 s64, s64, 1
X 168
Mvaa 188,152,56
W 2
Ra 144,242,20480
K 201,166,167
Mvav 32,128,196
Ta 192,6,16384
Ta 194,6,20480
Mvav 64,128,204
X 169
Zaaa 248,184,216
buffer_store_dwordx4 a[240:243], v10, s[8:11], s94 offen offset:512 sc1
Ta 196,7,16384
Mvav 96,128,212
X 170
Mvav 128,128,220
Ra 148,242,21504
K 208,168,169
Mvav 132,132,220
W 5
Ta 198,7,20480
Ta 200,4,24576
Maaa 248,188,220
X 171
Mvav 100,132,212
X 172
Mvav 68,132,204
Ta 202,4,28672
K 209,170,171
Mvav 36,132,196
)"
R"(W 5
Ra 152,242,22528
buffer_store_dwordx4 a[244:247], v10, s[8:11], s94 offen offset:640 sc1
Zaaa 252,192,216
X 173
Mvav 40,136,196
X 174
Mvav 72,136,204
Ta 204,5,24576
Ta 206,5,28672
Mvav 104,136,212
W 5
Ra 156,242,23552
K 216,172,173
Maaa 252,196,220
X 175
Mvav 136,136,220
Ta 208,6,24576
Ta 210,6,28672
Mvav 140,140,220
X 176
Mvav 108,140,212
W 6
buffer_store_dwordx4 a[248:251], v10, s[8:11], s94 offen offset:1024 sc1
K 217,174,175
Zaaa 232,200,216
X 177
Mvav 76,140,204
Ta 212,7,24576
Ra 128,242,8192
Mvav 44,140,196
W 4
Ta 214,7,28672
Ta 184,4,32768
Mvav 48,144,196
X 178
Maaa 232,204,220
Ra 132,242,9216
K 194,176,177
Mvav 80,144,204
X 179
Mvav 112,144,212
Ta 186,4,36864
buffer_store_dwordx4 a[252:255], v10, s[8:11], s94 offen offset:1152 sc1
Mvav 144,144,220
W 6
X 180
Zaaa 236,208,216
Ta 188,5,32768
K 195,178,179
Mvav 148,148,220
X 181
Mvav 116,148,212
Ta 190,5,36864
Ra 136,242,10240
Mvav 84,148,204
W 6
X 182
Maaa 236,212,220
Ta 192,6,32768
K 202,180,181
Mvav 52,148,196
Ta 194,6,36864
Ra 140,242,11264
Mvav 56,152,196
X 183
Mvav 88,152,204
W 6
buffer_store_dwordx4 a[232:235], v10, s[8:11], s94 offen offset:1536 sc1
Ta 196,7,32768
Zaaa 240,184,216
X 184
Mvav 120,152,212
Ta 198,7,36864
K 203,182,183
Mvav 152,152,220
Ra 144,242,12288
Ra 148,242,13312
Mvav 156,156,220
W 8
X 185
Maaa 240,188,220
X 186
Mvav 124,156,212
buffer_store_dwordx4 a[236:239], v10, s[8:11], s94 offen offset:1664 sc1
K 210,184,185
Mvav 92,156,204
X 187
Mvav 60,156,196
W 5
X 188
Zaaa 244,192,216
W 4
Ra 152,242,14336
K 211,186,187
Cvaa 160,128,0,224
X 189
Mvaa 160,132,4
X 190
Cvaa 164,132,20,228
W 3
Ra 156,242,15360
buffer_store_dwordx4 a[240:243], v10, s[8:11], s94 offen offset:2048 sc1
Maaa 244,196,220
K 218,188,189
Mvaa 164,128,16
X 191
Cvaa 168,128,32,232
Mvaa 168,132,36
Cvaa 172,132,52,236
Mvaa 172,128,48
Ra 128,242,24576
Mvaa 160,136,8
Ra 132,242,25600
buffer_store_dwordx4 a[244:247], v10, s[8:11], s94 offen offset:2176 sc1
Mvaa 160,140,12
Mvaa 164,140,28
Mvaa 164,136,24
Mvaa 168,136,40
Mvaa 168,140,44
Mvaa 172,140,60
Mvaa 172,136,56
W 2
Ra 136,242,26624
Cvaa 176,144,0,224
X 160
Mvaa 176,148,4
Ra 140,242,27648
K 219,190,191
Cvaa 180,148,20,228
X 161
Mvaa 180,144,16
X 162
Cvaa 184,144,32,232
K 196,160,161
Mvaa 184,148,36
X 163
Cvaa 188,148,52,236
X 164
Mvaa 188,144,48
Ra 144,242,28672
K 197,162,163
Mvaa 176,152,8
X 165
Mvaa 176,156,12
X 166
Mvaa 180,156,28
Ra 148,242,29696
K 204,164,165
Mvaa 180,152,24
X 167
Mvaa 184,152,40
X 168
Mvaa 184,156,44
K 205,166,167
Mvaa 188,156,60
X 169
Mvaa 188,152,56
W 2
X 170
Mvav 32,128,192
Ra 152,242,30720
K 212,168,169
Mvav 64,128,200
X 171
Mvav 96,128,208
Ra 156,242,31744
v_xor_b32 v242, 0x10000, v242
Mvav 128,128,216
X 172
Mvav 132,132,216
K 213,170,171
Mvav 100,132,208
X 173
Mvav 68,132,200
X 174
Mvav 36,132,192
Mvav 40,136,192
K 220,172,173
Mvav 72,136,200
X 175
Mvav 104,136,208
Mvav 136,136,216
X 176
Mvav 140,140,216
Mvav 108,140,208
K 221,174,175
Mvav 76,140,200
W 0
s_barrier
Mvav 44,140,192
X 177
Mvav 48,144,192
X 178
Mvav 80,144,200
Ra 128,242,0
K 198,176,177
Mvav 112,144,208
X 179
Mvav 144,144,216
Ra 132,242,1024
Mvav 148,148,216
X 180
Mvav 116,148,208
Ra 136,242,2048
Mvav 84,148,200
K 199,178,179
Mvav 52,148,192
X 181
Mvav 56,152,192
Ra 140,242,3072
Mvav 88,152,200
X 182
Mvav 120,152,208
Mvav 152,152,216
K 206,180,181
Mvav 156,156,216
X 183
Mvav 124,156,208
Mvav 92,156,200
s_waitcnt vmcnt(0)
v_mov_b64 v[28:29], s[12:13]
s_mov_b64 exec, 1
buffer_store_dwordx2 v[28:29], off, s[8:11], s71 sc1
s_mov_b64 exec, -1
K 207,182,183
Mvav 60,156,192
s_mov_b32 s85, 99
s_lshl_b32 s65, s57, 1
s_sub_u32 s65, s65, 1
s_branch .Ll0t_42%=
.Lf1r_43%=:
s_lshl_b32 s92, s58, 2
s_add_u32 s91, s92, s51
s_add_u32 s95, s92, 0
s_and_b32 s95, s95, 15
s_mul_i32 s95, s95, 40960
s_mul_i32 s71, s51, 0
s_add_u32 s71, s71, 133120
s_add_u32 m0, s71, s48
s_lshl2_add_u32 s94, s48, s95
D 16,40,94,0
s_add_u32 s95, s92, 1
s_and_b32 s95, s95, 15
s_mul_i32 s95, s95, 40960
s_mul_i32 s71, s51, 0
s_add_u32 s71, s71, 143360
D 17,40,94,1024
D 18,40,94,2048
D 19,40,94,3072
s_add_u32 m0, m0, 4096
s_nop 0
buffer_load_dwordx4 v20, s[40:43], s94 offen lds
s_add_u32 m0, s71, s48
s_lshl2_add_u32 s94, s48, s95
D 16,40,94,0
s_add_u32 s95, s92, 2
s_and_b32 s95, s95, 15
s_mul_i32 s95, s95, 40960
s_mul_i32 s71, s51, 0
s_add_u32 s71, s71, 153600
D 17,40,94,1024
D 18,40,94,2048
D 19,40,94,3072
s_add_u32 m0, m0, 4096
s_nop 0
buffer_load_dwordx4 v20, s[40:43], s94 offen lds
s_add_u32 m0, s71, s48
s_lshl2_add_u32 s94, s48, s95
D 16,40,94,0
D 17,40,94,1024
D 18,40,94,2048
D 19,40,94,3072
s_add_u32 m0, m0, 4096
s_nop 0
buffer_load_dwordx4 v20, s[40:43], s94 offen lds
s_branch .Lf1_36%=
.Lspin_11%=:
s_mov_b32 s84, 0
.Llp_13%=:
s_sleep 2
v_lshrrev_b32 v28, 1, v1
s_lshl_b32 s80, s60, 9
s_add_u32 s80, s80, 40960
buffer_load_dwordx2 v[28:29], v28, s[8:11], s80 offen sc1
s_waitcnt vmcnt(0)
s_add_u32 s84, s84, 1
v_cmp_eq_u32 vcc, s13, v29
s_cmp_eq_u64 vcc, -1
s_cbranch_scc1 .Lok_12%=
s_cmp_lt_u32 s84, 1048576
s_cbranch_scc1 .Llp_13%=
s_branch .Lok_12%=
.Lspin_16%=:
s_mov_b32 s84, 0
.Llp_18%=:
s_sleep 2
v_lshrrev_b32 v28, 1, v1
s_lshl_b32 s80, s60, 9
s_add_u32 s80, s80, 40960
buffer_load_dwordx2 v[28:29], v28, s[8:11], s80 offen sc1
s_waitcnt vmcnt(0)
s_add_u32 s84, s84, 1
v_cmp_eq_u32 vcc, s13, v29
s_cmp_eq_u64 vcc, -1
s_cbranch_scc1 .Lok_17%=
s_cmp_lt_u32 s84, 1048576
s_cbranch_scc1 .Llp_18%=
s_branch .Lok_17%=
.Lrt_19%=:
s_waitcnt vmcnt(0)
s_mov_b32 s86, 40960
buffer_load_dwordx4 v[32:35], v1, s[8:11], s86 offen offset:0 sc1
buffer_load_dwordx4 v[36:39], v1, s[8:11], s86 offen offset:1024 sc1
buffer_load_dwordx4 v[40:43], v1, s[8:11], s86 offen offset:2048 sc1
buffer_load_dwordx4 v[44:47], v1, s[8:11], s86 offen offset:3072 sc1
s_mov_b32 s86, 45056
buffer_load_dwordx4 v[48:51], v1, s[8:11], s86 offen offset:0 sc1
buffer_load_dwordx4 v[52:55], v1, s[8:11], s86 offen offset:1024 sc1
buffer_load_dwordx4 v[56:59], v1, s[8:11], s86 offen offset:2048 sc1
buffer_load_dwordx4 v[60:63], v1, s[8:11], s86 offen offset:3072 sc1
s_mov_b32 s86, 49152
buffer_load_dwordx4 v[64:67], v1, s[8:11], s86 offen offset:0 sc1
buffer_load_dwordx4 v[68:71], v1, s[8:11], s86 offen offset:1024 sc1
buffer_load_dwordx4 v[72:75], v1, s[8:11], s86 offen offset:2048 sc1
buffer_load_dwordx4 v[76:79], v1, s[8:11], s86 offen offset:3072 sc1
s_mov_b32 s86, 53248
buffer_load_dwordx4 v[80:83], v1, s[8:11], s86 offen offset:0 sc1
buffer_load_dwordx4 v[84:87], v1, s[8:11], s86 offen offset:1024 sc1
buffer_load_dwordx4 v[88:91], v1, s[8:11], s86 offen offset:2048 sc1
buffer_load_dwordx4 v[92:95], v1, s[8:11], s86 offen offset:3072 sc1
s_branch .Lrtr_20%=
.Lrt_21%=:
v_lshrrev_b32 v136, 1, v1
s_mov_b32 s84, 0
.LL_23%=:
s_waitcnt vmcnt(0)
v_min3_u32 v96, v33, v35, v95
v_max3_u32 v97, v33, v35, v95
v_min3_u32 v96, v96, v37, v39
v_max3_u32 v97, v97, v37, v39
v_min3_u32 v96, v96, v41, v43
v_max3_u32 v97, v97, v41, v43
v_min3_u32 v96, v96, v45, v47
v_max3_u32 v97, v97, v45, v47
v_min3_u32 v96, v96, v49, v51
v_max3_u32 v97, v97, v49, v51
v_min3_u32 v96, v96, v53, v55
v_max3_u32 v97, v97, v53, v55
v_min3_u32 v96, v96, v57, v59
v_max3_u32 v97, v97, v57, v59
v_min3_u32 v96, v96, v61, v63
v_max3_u32 v97, v97, v61, v63
v_min3_u32 v96, v96, v65, v67
v_max3_u32 v97, v97, v65, v67
v_min3_u32 v96, v96, v69, v71
v_max3_u32 v97, v97, v69, v71
v_min3_u32 v96, v96, v73, v75
v_max3_u32 v97, v97, v73, v75
v_min3_u32 v96, v96, v77, v79
v_max3_u32 v97, v97, v77, v79
v_min3_u32 v96, v96, v81, v83
v_max3_u32 v97, v97, v81, v83
v_min3_u32 v96, v96, v85, v87
v_max3_u32 v97, v97, v85, v87
v_min3_u32 v96, v96, v89, v91
v_max3_u32 v97, v97, v89, v91
v_min_u32 v96, v96, v93
v_max_u32 v97, v97, v93
v_cmp_eq_u32 vcc, s13, v96
v_cmp_eq_u32_e64 s[86:87], s13, v97
s_and_b64 vcc, vcc, s[86:87]
s_cmp_eq_u64 vcc, -1
s_cbranch_scc1 .LL_24%=
s_sleep 2
s_mov_b32 s86, 40960
buffer_load_dwordx4 v[32:35], v1, s[8:11], s86 offen offset:0 sc1
buffer_load_dwordx4 v[36:39], v1, s[8:11], s86 offen offset:1024 sc1
buffer_load_dwordx4 v[40:43], v1, s[8:11], s86 offen offset:2048 sc1
buffer_load_dwordx4 v[44:47], v1, s[8:11], s86 offen offset:3072 sc1
s_mov_b32 s86, 45056
buffer_load_dwordx4 v[48:51], v1, s[8:11], s86 offen offset:0 sc1
buffer_load_dwordx4 v[52:55], v1, s[8:11], s86 offen offset:1024 sc1
buffer_load_dwordx4 v[56:59], v1, s[8:11], s86 offen offset:2048 sc1
buffer_load_dwordx4 v[60:63], v1, s[8:11], s86 offen offset:3072 sc1
s_mov_b32 s86, 49152
buffer_load_dwordx4 v[64:67], v1, s[8:11], s86 offen offset:0 sc1
buffer_load_dwordx4 v[68:71], v1, s[8:11], s86 offen offset:1024 sc1
buffer_load_dwordx4 v[72:75], v1, s[8:11], s86 offen offset:2048 sc1
buffer_load_dwordx4 v[76:79], v1, s[8:11], s86 offen offset:3072 sc1
s_mov_b32 s86, 53248
buffer_load_dwordx4 v[80:83], v1, s[8:11], s86 offen offset:0 sc1
buffer_load_dwordx4 v[84:87], v1, s[8:11], s86 offen offset:1024 sc1
buffer_load_dwordx4 v[88:91], v1, s[8:11], s86 offen offset:2048 sc1
buffer_load_dwordx4 v[92:95], v1, s[8:11], s86 offen offset:3072 sc1
s_add_u32 s84, s84, 1
s_cmp_lt_u32 s84, 1048576
s_cbranch_scc1 .LL_23%=
s_waitcnt vmcnt(0)
.LL_24%=:
)"
R"(v_mov_b32 v96, v32
v_mov_b32 v97, 0
v_mov_b32 v98, 0
v_mov_b32 v100, 0
v_med3_u32 v98, v97, v98, v34
v_med3_u32 v97, v96, v97, v34
v_max_u32 v96, v96, v34
v_med3_u32 v98, v97, v98, v36
v_med3_u32 v97, v96, v97, v36
v_max_u32 v96, v96, v36
v_med3_u32 v98, v97, v98, v38
v_med3_u32 v97, v96, v97, v38
v_max_u32 v96, v96, v38
v_med3_u32 v98, v97, v98, v40
v_med3_u32 v97, v96, v97, v40
v_max_u32 v96, v96, v40
v_med3_u32 v98, v97, v98, v42
v_med3_u32 v97, v96, v97, v42
v_max_u32 v96, v96, v42
v_med3_u32 v98, v97, v98, v44
v_med3_u32 v97, v96, v97, v44
v_max_u32 v96, v96, v44
v_med3_u32 v98, v97, v98, v46
v_med3_u32 v97, v96, v97, v46
v_max_u32 v96, v96, v46
v_med3_u32 v98, v97, v98, v48
v_med3_u32 v97, v96, v97, v48
v_max_u32 v96, v96, v48
v_med3_u32 v98, v97, v98, v50
v_med3_u32 v97, v96, v97, v50
v_max_u32 v96, v96, v50
v_med3_u32 v98, v97, v98, v52
v_med3_u32 v97, v96, v97, v52
v_max_u32 v96, v96, v52
v_med3_u32 v98, v97, v98, v54
v_med3_u32 v97, v96, v97, v54
v_max_u32 v96, v96, v54
v_med3_u32 v98, v97, v98, v56
v_med3_u32 v97, v96, v97, v56
v_max_u32 v96, v96, v56
v_med3_u32 v98, v97, v98, v58
v_med3_u32 v97, v96, v97, v58
v_max_u32 v96, v96, v58
v_med3_u32 v98, v97, v98, v60
v_med3_u32 v97, v96, v97, v60
v_max_u32 v96, v96, v60
v_med3_u32 v98, v97, v98, v62
v_med3_u32 v97, v96, v97, v62
v_max_u32 v96, v96, v62
v_med3_u32 v98, v97, v98, v64
v_med3_u32 v97, v96, v97, v64
v_max_u32 v96, v96, v64
v_med3_u32 v98, v97, v98, v66
v_med3_u32 v97, v96, v97, v66
v_max_u32 v96, v96, v66
v_med3_u32 v98, v97, v98, v68
v_med3_u32 v97, v96, v97, v68
v_max_u32 v96, v96, v68
v_med3_u32 v98, v97, v98, v70
v_med3_u32 v97, v96, v97, v70
v_max_u32 v96, v96, v70
v_med3_u32 v98, v97, v98, v72
v_med3_u32 v97, v96, v97, v72
v_max_u32 v96, v96, v72
v_med3_u32 v98, v97, v98, v74
v_med3_u32 v97, v96, v97, v74
v_max_u32 v96, v96, v74
v_med3_u32 v98, v97, v98, v76
v_med3_u32 v97, v96, v97, v76
v_max_u32 v96, v96, v76
v_med3_u32 v98, v97, v98, v78
v_med3_u32 v97, v96, v97, v78
v_max_u32 v96, v96, v78
v_med3_u32 v98, v97, v98, v80
v_med3_u32 v97, v96, v97, v80
v_max_u32 v96, v96, v80
v_med3_u32 v98, v97, v98, v82
v_med3_u32 v97, v96, v97, v82
v_max_u32 v96, v96, v82
v_med3_u32 v98, v97, v98, v84
v_med3_u32 v97, v96, v97, v84
v_max_u32 v96, v96, v84
v_med3_u32 v98, v97, v98, v86
v_med3_u32 v97, v96, v97, v86
v_max_u32 v96, v96, v86
v_med3_u32 v98, v97, v98, v88
v_med3_u32 v97, v96, v97, v88
v_max_u32 v96, v96, v88
v_med3_u32 v98, v97, v98, v90
v_med3_u32 v97, v96, v97, v90
v_max_u32 v96, v96, v90
v_med3_u32 v98, v97, v98, v92
v_med3_u32 v97, v96, v97, v92
v_max_u32 v96, v96, v92
v_med3_u32 v98, v97, v98, v94
v_med3_u32 v97, v96, v97, v94
v_max_u32 v96, v96, v94
v_mov_b32 v99, v96
s_nop 1
v_max_u32_dpp v99, v99, v99 row_shr:1 row_mask:0xf bank_mask:0xf
s_nop 1
v_max_u32_dpp v99, v99, v99 row_shr:2 row_mask:0xf bank_mask:0xf
s_nop 1
v_max_u32_dpp v99, v99, v99 row_shr:4 row_mask:0xf bank_mask:0xf
s_nop 1
v_max_u32_dpp v99, v99, v99 row_shr:8 row_mask:0xf bank_mask:0xf
s_nop 1
v_max_u32_dpp v99, v99, v99 row_bcast:15 row_mask:0xa bank_mask:0xf
s_nop 1
v_max_u32_dpp v99, v99, v99 row_bcast:31 row_mask:0xc bank_mask:0xf
s_nop 0
v_readlane_b32 s86, v99, 63
s_nop 1
v_cmp_eq_u32 vcc, s86, v96
s_and_b32 s87, s86, 0xffff
s_sub_u32 s87, 2047, s87
v_cndmask_b32 v96, v96, v97, vcc
v_cndmask_b32_e64 v97, v97, 0, vcc
v_writelane_b32 v100, s87, 0
v_mov_b32 v99, v96
s_nop 1
v_max_u32_dpp v99, v99, v99 row_shr:1 row_mask:0xf bank_mask:0xf
s_nop 1
v_max_u32_dpp v99, v99, v99 row_shr:2 row_mask:0xf bank_mask:0xf
s_nop 1
v_max_u32_dpp v99, v99, v99 row_shr:4 row_mask:0xf bank_mask:0xf
s_nop 1
v_max_u32_dpp v99, v99, v99 row_shr:8 row_mask:0xf bank_mask:0xf
s_nop 1
v_max_u32_dpp v99, v99, v99 row_bcast:15 row_mask:0xa bank_mask:0xf
s_nop 1
v_max_u32_dpp v99, v99, v99 row_bcast:31 row_mask:0xc bank_mask:0xf
s_nop 0
v_readlane_b32 s86, v99, 63
s_nop 1
v_cmp_eq_u32 vcc, s86, v96
s_and_b32 s87, s86, 0xffff
s_sub_u32 s87, 2047, s87
v_cndmask_b32 v96, v96, v97, vcc
v_cndmask_b32_e64 v97, v97, 0, vcc
v_writelane_b32 v100, s87, 1
v_mov_b32 v99, v96
s_nop 1
v_max_u32_dpp v99, v99, v99 row_shr:1 row_mask:0xf bank_mask:0xf
s_nop 1
v_max_u32_dpp v99, v99, v99 row_shr:2 row_mask:0xf bank_mask:0xf
s_nop 1
v_max_u32_dpp v99, v99, v99 row_shr:4 row_mask:0xf bank_mask:0xf
s_nop 1
v_max_u32_dpp v99, v99, v99 row_shr:8 row_mask:0xf bank_mask:0xf
s_nop 1
v_max_u32_dpp v99, v99, v99 row_bcast:15 row_mask:0xa bank_mask:0xf
s_nop 1
v_max_u32_dpp v99, v99, v99 row_bcast:31 row_mask:0xc bank_mask:0xf
s_nop 0
v_readlane_b32 s86, v99, 63
s_nop 1
v_cmp_eq_u32 vcc, s86, v96
s_and_b32 s87, s86, 0xffff
s_sub_u32 s87, 2047, s87
v_cndmask_b32 v96, v96, v97, vcc
v_cndmask_b32_e64 v97, v97, 0, vcc
v_writelane_b32 v100, s87, 2
v_mov_b32 v99, v96
s_nop 1
v_max_u32_dpp v99, v99, v99 row_shr:1 row_mask:0xf bank_mask:0xf
s_nop 1
v_max_u32_dpp v99, v99, v99 row_shr:2 row_mask:0xf bank_mask:0xf
s_nop 1
v_max_u32_dpp v99, v99, v99 row_shr:4 row_mask:0xf bank_mask:0xf
s_nop 1
v_max_u32_dpp v99, v99, v99 row_shr:8 row_mask:0xf bank_mask:0xf
s_nop 1
v_max_u32_dpp v99, v99, v99 row_bcast:15 row_mask:0xa bank_mask:0xf
s_nop 1
v_max_u32_dpp v99, v99, v99 row_bcast:31 row_mask:0xc bank_mask:0xf
s_nop 0
v_readlane_b32 s86, v99, 63
s_nop 1
v_cmp_eq_u32 vcc, s86, v96
s_and_b32 s87, s86, 0xffff
s_sub_u32 s87, 2047, s87
v_cndmask_b32 v96, v96, v97, vcc
v_cndmask_b32_e64 v97, v97, 0, vcc
v_writelane_b32 v100, s87, 3
v_mov_b32 v99, v96
s_nop 1
v_max_u32_dpp v99, v99, v99 row_shr:1 row_mask:0xf bank_mask:0xf
s_nop 1
v_max_u32_dpp v99, v99, v99 row_shr:2 row_mask:0xf bank_mask:0xf
s_nop 1
v_max_u32_dpp v99, v99, v99 row_shr:4 row_mask:0xf bank_mask:0xf
s_nop 1
v_max_u32_dpp v99, v99, v99 row_shr:8 row_mask:0xf bank_mask:0xf
s_nop 1
v_max_u32_dpp v99, v99, v99 row_bcast:15 row_mask:0xa bank_mask:0xf
s_nop 1
v_max_u32_dpp v99, v99, v99 row_bcast:31 row_mask:0xc bank_mask:0xf
s_nop 0
v_readlane_b32 s86, v99, 63
s_nop 1
v_cmp_eq_u32 vcc, s86, v96
s_and_b32 s87, s86, 0xffff
s_sub_u32 s87, 2047, s87
v_cndmask_b32 v96, v96, v97, vcc
v_cndmask_b32_e64 v97, v97, 0, vcc
v_writelane_b32 v100, s87, 4
v_mov_b32 v99, v96
s_nop 1
v_max_u32_dpp v99, v99, v99 row_shr:1 row_mask:0xf bank_mask:0xf
s_nop 1
v_max_u32_dpp v99, v99, v99 row_shr:2 row_mask:0xf bank_mask:0xf
s_nop 1
v_max_u32_dpp v99, v99, v99 row_shr:4 row_mask:0xf bank_mask:0xf
s_nop 1
v_max_u32_dpp v99, v99, v99 row_shr:8 row_mask:0xf bank_mask:0xf
s_nop 1
v_max_u32_dpp v99, v99, v99 row_bcast:15 row_mask:0xa bank_mask:0xf
s_nop 1
v_max_u32_dpp v99, v99, v99 row_bcast:31 row_mask:0xc bank_mask:0xf
s_nop 0
v_readlane_b32 s86, v99, 63
s_nop 1
v_cmp_eq_u32 vcc, s86, v96
s_and_b32 s87, s86, 0xffff
s_sub_u32 s87, 2047, s87
v_cndmask_b32 v96, v96, v97, vcc
v_cndmask_b32_e64 v97, v97, 0, vcc
v_writelane_b32 v100, s87, 5
v_mov_b32 v99, v96
s_nop 1
v_max_u32_dpp v99, v99, v99 row_shr:1 row_mask:0xf bank_mask:0xf
s_nop 1
v_max_u32_dpp v99, v99, v99 row_shr:2 row_mask:0xf bank_mask:0xf
s_nop 1
v_max_u32_dpp v99, v99, v99 row_shr:4 row_mask:0xf bank_mask:0xf
s_nop 1
v_max_u32_dpp v99, v99, v99 row_shr:8 row_mask:0xf bank_mask:0xf
s_nop 1
v_max_u32_dpp v99, v99, v99 row_bcast:15 row_mask:0xa bank_mask:0xf
s_nop 1
v_max_u32_dpp v99, v99, v99 row_bcast:31 row_mask:0xc bank_mask:0xf
s_nop 0
v_readlane_b32 s86, v99, 63
s_nop 1
v_cmp_eq_u32 vcc, s86, v96
s_and_b32 s87, s86, 0xffff
s_sub_u32 s87, 2047, s87
v_cndmask_b32 v96, v96, v97, vcc
v_cndmask_b32_e64 v97, v97, 0, vcc
v_writelane_b32 v100, s87, 6
v_mov_b32 v99, v96
s_nop 1
v_max_u32_dpp v99, v99, v99 row_shr:1 row_mask:0xf bank_mask:0xf
s_nop 1
v_max_u32_dpp v99, v99, v99 row_shr:2 row_mask:0xf bank_mask:0xf
s_nop 1
v_max_u32_dpp v99, v99, v99 row_shr:4 row_mask:0xf bank_mask:0xf
s_nop 1
v_max_u32_dpp v99, v99, v99 row_shr:8 row_mask:0xf bank_mask:0xf
s_nop 1
v_max_u32_dpp v99, v99, v99 row_bcast:15 row_mask:0xa bank_mask:0xf
s_nop 1
v_max_u32_dpp v99, v99, v99 row_bcast:31 row_mask:0xc bank_mask:0xf
s_nop 0
v_readlane_b32 s86, v99, 63
s_nop 1
v_cmp_eq_u32 vcc, s86, v96
s_and_b32 s87, s86, 0xffff
s_sub_u32 s87, 2047, s87
v_cndmask_b32 v96, v96, v97, vcc
v_cndmask_b32_e64 v97, v97, 0, vcc
v_writelane_b32 v100, s87, 7
v_mov_b32 v99, v98
s_nop 1
v_max_u32_dpp v99, v99, v99 row_shr:1 row_mask:0xf bank_mask:0xf
s_nop 1
v_max_u32_dpp v99, v99, v99 row_shr:2 row_mask:0xf bank_mask:0xf
s_nop 1
v_max_u32_dpp v99, v99, v99 row_shr:4 row_mask:0xf bank_mask:0xf
s_nop 1
v_max_u32_dpp v99, v99, v99 row_shr:8 row_mask:0xf bank_mask:0xf
s_nop 1
v_max_u32_dpp v99, v99, v99 row_bcast:15 row_mask:0xa bank_mask:0xf
s_nop 1
v_max_u32_dpp v99, v99, v99 row_bcast:31 row_mask:0xc bank_mask:0xf
s_nop 0
v_readlane_b32 s87, v99, 63
s_nop 0
s_cmp_gt_u32 s87, s86
s_cbranch_scc0 .LL_25%=
s_mov_b32 m0, 0
.LL_26%=:
v_max3_u32 v96, v32, v34, v36
v_max3_u32 v96, v96, v38, v40
v_max3_u32 v96, v96, v42, v44
v_max3_u32 v96, v96, v46, v48
v_max3_u32 v96, v96, v50, v52
v_max3_u32 v96, v96, v54, v56
v_max3_u32 v96, v96, v58, v60
v_max3_u32 v96, v96, v62, v64
v_max3_u32 v96, v96, v66, v68
v_max3_u32 v96, v96, v70, v72
v_max3_u32 v96, v96, v74, v76
v_max3_u32 v96, v96, v78, v80
v_max3_u32 v96, v96, v82, v84
v_max3_u32 v96, v96, v86, v88
v_max3_u32 v96, v96, v90, v92
v_max_u32 v96, v96, v94
v_mov_b32 v99, v96
s_nop 1
v_max_u32_dpp v99, v99, v99 row_shr:1 row_mask:0xf bank_mask:0xf
s_nop 1
v_max_u32_dpp v99, v99, v99 row_shr:2 row_mask:0xf bank_mask:0xf
s_nop 1
v_max_u32_dpp v99, v99, v99 row_shr:4 row_mask:0xf bank_mask:0xf
s_nop 1
v_max_u32_dpp v99, v99, v99 row_shr:8 row_mask:0xf bank_mask:0xf
s_nop 1
v_max_u32_dpp v99, v99, v99 row_bcast:15 row_mask:0xa bank_mask:0xf
s_nop 1
v_max_u32_dpp v99, v99, v99 row_bcast:31 row_mask:0xc bank_mask:0xf
s_nop 0
v_readlane_b32 s86, v99, 63
s_nop 1
v_cmp_eq_u32 vcc, s86, v32
s_nop 1
v_cndmask_b32_e64 v32, v32, 0, vcc
v_cmp_eq_u32 vcc, s86, v34
s_nop 1
v_cndmask_b32_e64 v34, v34, 0, vcc
v_cmp_eq_u32 vcc, s86, v36
s_nop 1
v_cndmask_b32_e64 v36, v36, 0, vcc
v_cmp_eq_u32 vcc, s86, v38
s_nop 1
v_cndmask_b32_e64 v38, v38, 0, vcc
v_cmp_eq_u32 vcc, s86, v40
s_nop 1
v_cndmask_b32_e64 v40, v40, 0, vcc
v_cmp_eq_u32 vcc, s86, v42
s_nop 1
v_cndmask_b32_e64 v42, v42, 0, vcc
v_cmp_eq_u32 vcc, s86, v44
s_nop 1
v_cndmask_b32_e64 v44, v44, 0, vcc
v_cmp_eq_u32 vcc, s86, v46
s_nop 1
v_cndmask_b32_e64 v46, v46, 0, vcc
v_cmp_eq_u32 vcc, s86, v48
s_nop 1
v_cndmask_b32_e64 v48, v48, 0, vcc
v_cmp_eq_u32 vcc, s86, v50
s_nop 1
v_cndmask_b32_e64 v50, v50, 0, vcc
v_cmp_eq_u32 vcc, s86, v52
s_nop 1
v_cndmask_b32_e64 v52, v52, 0, vcc
v_cmp_eq_u32 vcc, s86, v54
s_nop 1
v_cndmask_b32_e64 v54, v54, 0, vcc
v_cmp_eq_u32 vcc, s86, v56
s_nop 1
v_cndmask_b32_e64 v56, v56, 0, vcc
v_cmp_eq_u32 vcc, s86, v58
s_nop 1
v_cndmask_b32_e64 v58, v58, 0, vcc
v_cmp_eq_u32 vcc, s86, v60
s_nop 1
v_cndmask_b32_e64 v60, v60, 0, vcc
v_cmp_eq_u32 vcc, s86, v62
s_nop 1
v_cndmask_b32_e64 v62, v62, 0, vcc
v_cmp_eq_u32 vcc, s86, v64
s_nop 1
v_cndmask_b32_e64 v64, v64, 0, vcc
v_cmp_eq_u32 vcc, s86, v66
s_nop 1
v_cndmask_b32_e64 v66, v66, 0, vcc
v_cmp_eq_u32 vcc, s86, v68
s_nop 1
v_cndmask_b32_e64 v68, v68, 0, vcc
v_cmp_eq_u32 vcc, s86, v70
s_nop 1
v_cndmask_b32_e64 v70, v70, 0, vcc
v_cmp_eq_u32 vcc, s86, v72
s_nop 1
v_cndmask_b32_e64 v72, v72, 0, vcc
v_cmp_eq_u32 vcc, s86, v74
s_nop 1
v_cndmask_b32_e64 v74, v74, 0, vcc
v_cmp_eq_u32 vcc, s86, v76
s_nop 1
v_cndmask_b32_e64 v76, v76, 0, vcc
v_cmp_eq_u32 vcc, s86, v78
s_nop 1
v_cndmask_b32_e64 v78, v78, 0, vcc
v_cmp_eq_u32 vcc, s86, v80
s_nop 1
v_cndmask_b32_e64 v80, v80, 0, vcc
)"
R"(v_cmp_eq_u32 vcc, s86, v82
s_nop 1
v_cndmask_b32_e64 v82, v82, 0, vcc
v_cmp_eq_u32 vcc, s86, v84
s_nop 1
v_cndmask_b32_e64 v84, v84, 0, vcc
v_cmp_eq_u32 vcc, s86, v86
s_nop 1
v_cndmask_b32_e64 v86, v86, 0, vcc
v_cmp_eq_u32 vcc, s86, v88
s_nop 1
v_cndmask_b32_e64 v88, v88, 0, vcc
v_cmp_eq_u32 vcc, s86, v90
s_nop 1
v_cndmask_b32_e64 v90, v90, 0, vcc
v_cmp_eq_u32 vcc, s86, v92
s_nop 1
v_cndmask_b32_e64 v92, v92, 0, vcc
v_cmp_eq_u32 vcc, s86, v94
s_nop 1
v_cndmask_b32_e64 v94, v94, 0, vcc
s_and_b32 s87, s86, 0xffff
s_sub_u32 s87, 2047, s87
s_nop 0
v_writelane_b32 v100, s87, m0
s_add_u32 m0, m0, 1
s_cmp_lt_u32 m0, 8
s_cbranch_scc1 .LL_26%=
.LL_25%=:
v_xor_b32 v102, s12, v100
v_mov_b32 v103, s13
s_mov_b64 exec, 0xff
buffer_store_dwordx2 v[102:103], v136, s[8:11], 0 offen offset:3584 sc1
s_mov_b64 exec, -1
s_branch .Lrtr_22%=
.Lspin_47%=:
s_mov_b32 s84, 0
.Llp_49%=:
s_sleep 2
buffer_load_dwordx2 v[8:9], v11, s[8:11], 0 offen offset:3840 sc1
s_waitcnt vmcnt(0)
s_add_u32 s84, s84, 1
v_cmp_eq_u64 vcc, s[12:13], v[8:9]
s_cmp_eq_u64 vcc, -1
s_cbranch_scc1 .Lok_48%=
s_cmp_lt_u32 s84, 1048576
s_cbranch_scc1 .Llp_49%=
s_branch .Lok_48%=
.Lredo%=:
s_waitcnt vmcnt(0) lgkmcnt(0)
s_barrier
s_and_b32 s81, s14, 1
s_mul_i32 s91, s81, 17164288
s_mul_i32 s82, s56, 536576
s_add_u32 s91, s91, s82
s_add_u32 s91, s91, 4194304
s_lshr_b32 s82, s14, 1
s_lshl_b32 s83, s82, 14
s_add_u32 s91, s91, s83
s_lshl_b32 s81, s81, 14
s_lshl_b32 s82, s82, 16
s_or_b32 s92, s81, s82
s_xor_b32 s92, s92, 0x10000
s_add_u32 m0, s92, 0
s_add_u32 s81, s91, 0
Dc 1,8,81,0
Dc 1,8,81,1024
Dc 1,8,81,2048
Dc 1,8,81,3072
s_add_u32 m0, s92, 4096
s_add_u32 s81, s91, 4096
Dc 1,8,81,0
Dc 1,8,81,1024
Dc 1,8,81,2048
Dc 1,8,81,3072
s_add_u32 m0, s92, 8192
s_add_u32 s81, s91, 8192
Dc 1,8,81,0
Dc 1,8,81,1024
Dc 1,8,81,2048
Dc 1,8,81,3072
s_add_u32 m0, s92, 12288
s_add_u32 s81, s91, 12288
Dc 1,8,81,0
Dc 1,8,81,1024
Dc 1,8,81,2048
Dc 1,8,81,3072
s_mov_b32 s86, s70
v_bfe_u32 v244, v1, 4, 1
v_mul_u32_u24 v244, 0xaaaa, v244
v_add_u32 v244, 0x4444, v244
v_lshl_or_b32 v240, v244, 16, v244
v_mov_b32 v241, 0xff800000
v_add_u32 v242, 0x10000, v1
v_bfe_u32 v244, v1, 4, 4
v_lshrrev_b32 v245, 8, v1
v_lshlrev_b32 v245, 2, v245
v_sub_u32 v244, v244, v245
v_cmp_le_i32_e64 s[72:73], 0, v244
v_cmp_le_i32_e64 s[74:75], 1, v244
v_cmp_le_i32_e64 s[76:77], 2, v244
v_cmp_le_i32_e64 s[78:79], 3, v244
v_add_u32 v243, 4096, v1
v_add_u32 v244, 8192, v1
v_add_u32 v245, 12288, v1
s_mov_b32 s18, 0x3f803f80
s_mov_b32 s19, 0x3f003f00
v_accvgpr_write_b32 a176, s18
v_accvgpr_write_b32 a180, s19
v_mov_b32 v252, v241
v_accvgpr_write_b32 a177, s18
v_accvgpr_write_b32 a181, s19
v_mov_b32 v253, v241
v_accvgpr_write_b32 a178, s18
v_accvgpr_write_b32 a182, s19
v_mov_b32 v254, v241
v_accvgpr_write_b32 a179, s18
v_accvgpr_write_b32 a183, s19
v_mov_b32 v255, v241
v_accvgpr_write_b32 a160, 0
v_accvgpr_write_b32 a161, 0
v_accvgpr_write_b32 a162, 0
v_accvgpr_write_b32 a163, 0
v_accvgpr_write_b32 a164, 0
v_accvgpr_write_b32 a165, 0
v_accvgpr_write_b32 a166, 0
v_accvgpr_write_b32 a167, 0
v_accvgpr_write_b32 a168, 0
v_accvgpr_write_b32 a169, 0
v_accvgpr_write_b32 a170, 0
v_accvgpr_write_b32 a171, 0
v_accvgpr_write_b32 a172, 0
v_accvgpr_write_b32 a173, 0
v_accvgpr_write_b32 a174, 0
v_accvgpr_write_b32 a175, 0
s_mov_b32 s90, 0x5d800000
s_mov_b64 s[88:89], 0
s_mov_b32 s64, 0
s_and_b32 s18, s14, 1
s_lshl_b32 s62, s18, 14
s_lshl_b32 s20, s57, 15
s_sub_u32 s20, 507904, s20
s_add_u32 s20, s20, s62
s_lshl_b32 s65, s57, 1
s_sub_u32 s65, s65, 1
s_max_i32 s65, s65, 0
s_sub_u32 s66, s60, 1
s_cmp_lt_u32 s14, 2
s_cselect_b32 s20, s20, 0
s_cselect_b32 s66, s66, 15
s_cselect_b32 s21, 32768, 0
s_add_u32 s61, s91, s20
s_add_u32 s62, s62, s21
s_sub_u32 s63, 16384, s21
s_lshl_b32 s67, s60, 7
s_add_u32 s67, s67, s56
s_lshl_b32 s67, s67, 12
s_add_u32 s67, s67, 39845888
s_lshl_b32 s69, s60, 3
s_add_u32 s69, s69, s59
s_lshl_b32 s69, s69, 6
s_lshl_b32 s21, s58, 3
s_add_u32 s69, s69, s21
s_add_u32 s69, s69, 20480
s_mov_b32 s70, 0
s_mov_b32 s37, 0
s_add_u32 s67, s67, s86
v_bfe_u32 v28, v1, 4, 4
v_lshrrev_b32 v29, 8, v1
v_lshlrev_b32 v29, 2, v29
v_sub_u32 v28, v28, v29
s_mov_b64 s[24:25], 0
s_mov_b32 s68, 0
s_mov_b32 s85, 99
s_lshl_b32 s80, s60, 6
s_and_b32 s81, s14, 2
s_lshl_b32 s81, s81, 4
s_xor_b32 s82, s81, 0
s_add_u32 s82, s82, s80
v_add_u32 v4, s82, v28
s_xor_b32 s82, s81, 16
s_add_u32 s82, s82, s80
v_add_u32 v5, s82, v28
s_xor_b32 s82, s81, 32
s_add_u32 s82, s82, s80
v_add_u32 v6, s82, v28
s_xor_b32 s82, s81, 48
s_add_u32 s82, s82, s80
v_add_u32 v7, s82, v28
s_mul_i32 s82, s56, 536576
s_add_u32 s82, s82, 4194304
s_lshl_b32 s84, s60, 1
s_add_u32 s84, s84, 2
.LL_50%=:
buffer_load_dwordx4 a[128:131], v1, s[8:11], s82 offen offset:0 sc1
buffer_load_dwordx4 a[132:135], v1, s[8:11], s82 offen offset:1024 sc1
buffer_load_dwordx4 a[136:139], v1, s[8:11], s82 offen offset:2048 sc1
buffer_load_dwordx4 a[140:143], v1, s[8:11], s82 offen offset:3072 sc1
buffer_load_dwordx4 a[144:147], v243, s[8:11], s82 offen offset:0 sc1
buffer_load_dwordx4 a[148:151], v243, s[8:11], s82 offen offset:1024 sc1
buffer_load_dwordx4 a[152:155], v243, s[8:11], s82 offen offset:2048 sc1
buffer_load_dwordx4 a[156:159], v243, s[8:11], s82 offen offset:3072 sc1
s_waitcnt vmcnt(0)
Zvaa 160,128,0
Zvaa 164,128,16
Zvaa 168,128,32
Zvaa 172,128,48
Mvaa 160,132,4
Mvaa 164,132,20
Mvaa 168,132,36
Mvaa 172,132,52
Mvaa 160,136,8
Mvaa 164,136,24
Mvaa 168,136,40
Mvaa 172,136,56
Mvaa 160,140,12
Mvaa 164,140,28
Mvaa 168,140,44
Mvaa 172,140,60
Zvaa 176,144,0
Zvaa 180,144,16
Zvaa 184,144,32
Zvaa 188,144,48
Mvaa 176,148,4
Mvaa 180,148,20
Mvaa 184,148,36
Mvaa 188,148,52
Mvaa 176,152,8
Mvaa 180,152,24
Mvaa 184,152,40
Mvaa 188,152,56
Mvaa 176,156,12
Mvaa 180,156,28
Mvaa 184,156,44
Mvaa 188,156,60
v_cmp_le_i32 vcc, 0, v4
s_nop 1
v_cndmask_b32 v160, v241, v160, vcc
v_max_f32 v252, v252, v160
v_cmp_le_i32 vcc, 1, v4
s_nop 1
v_cndmask_b32 v161, v241, v161, vcc
v_max_f32 v252, v252, v161
v_cmp_le_i32 vcc, 2, v4
s_nop 1
v_cndmask_b32 v162, v241, v162, vcc
v_max_f32 v252, v252, v162
v_cmp_le_i32 vcc, 3, v4
s_nop 1
v_cndmask_b32 v163, v241, v163, vcc
v_max_f32 v252, v252, v163
v_cmp_le_i32 vcc, 0, v5
s_nop 1
v_cndmask_b32 v164, v241, v164, vcc
v_max_f32 v253, v253, v164
v_cmp_le_i32 vcc, 1, v5
s_nop 1
v_cndmask_b32 v165, v241, v165, vcc
v_max_f32 v253, v253, v165
v_cmp_le_i32 vcc, 2, v5
s_nop 1
v_cndmask_b32 v166, v241, v166, vcc
v_max_f32 v253, v253, v166
v_cmp_le_i32 vcc, 3, v5
s_nop 1
v_cndmask_b32 v167, v241, v167, vcc
v_max_f32 v253, v253, v167
v_cmp_le_i32 vcc, 0, v6
s_nop 1
v_cndmask_b32 v168, v241, v168, vcc
v_max_f32 v254, v254, v168
v_cmp_le_i32 vcc, 1, v6
s_nop 1
v_cndmask_b32 v169, v241, v169, vcc
v_max_f32 v254, v254, v169
v_cmp_le_i32 vcc, 2, v6
s_nop 1
v_cndmask_b32 v170, v241, v170, vcc
v_max_f32 v254, v254, v170
v_cmp_le_i32 vcc, 3, v6
s_nop 1
v_cndmask_b32 v171, v241, v171, vcc
v_max_f32 v254, v254, v171
v_cmp_le_i32 vcc, 0, v7
s_nop 1
v_cndmask_b32 v172, v241, v172, vcc
v_max_f32 v255, v255, v172
v_cmp_le_i32 vcc, 1, v7
s_nop 1
v_cndmask_b32 v173, v241, v173, vcc
v_max_f32 v255, v255, v173
v_cmp_le_i32 vcc, 2, v7
s_nop 1
v_cndmask_b32 v174, v241, v174, vcc
v_max_f32 v255, v255, v174
v_cmp_le_i32 vcc, 3, v7
s_nop 1
v_cndmask_b32 v175, v241, v175, vcc
v_max_f32 v255, v255, v175
v_cmp_le_i32 vcc, 16, v4
s_nop 1
v_cndmask_b32 v176, v241, v176, vcc
v_max_f32 v252, v252, v176
v_cmp_le_i32 vcc, 17, v4
s_nop 1
v_cndmask_b32 v177, v241, v177, vcc
v_max_f32 v252, v252, v177
v_cmp_le_i32 vcc, 18, v4
s_nop 1
v_cndmask_b32 v178, v241, v178, vcc
v_max_f32 v252, v252, v178
v_cmp_le_i32 vcc, 19, v4
s_nop 1
v_cndmask_b32 v179, v241, v179, vcc
v_max_f32 v252, v252, v179
v_cmp_le_i32 vcc, 16, v5
s_nop 1
v_cndmask_b32 v180, v241, v180, vcc
v_max_f32 v253, v253, v180
v_cmp_le_i32 vcc, 17, v5
s_nop 1
v_cndmask_b32 v181, v241, v181, vcc
v_max_f32 v253, v253, v181
v_cmp_le_i32 vcc, 18, v5
s_nop 1
v_cndmask_b32 v182, v241, v182, vcc
v_max_f32 v253, v253, v182
v_cmp_le_i32 vcc, 19, v5
s_nop 1
v_cndmask_b32 v183, v241, v183, vcc
v_max_f32 v253, v253, v183
v_cmp_le_i32 vcc, 16, v6
s_nop 1
v_cndmask_b32 v184, v241, v184, vcc
v_max_f32 v254, v254, v184
v_cmp_le_i32 vcc, 17, v6
s_nop 1
v_cndmask_b32 v185, v241, v185, vcc
v_max_f32 v254, v254, v185
v_cmp_le_i32 vcc, 18, v6
s_nop 1
v_cndmask_b32 v186, v241, v186, vcc
v_max_f32 v254, v254, v186
v_cmp_le_i32 vcc, 19, v6
s_nop 1
v_cndmask_b32 v187, v241, v187, vcc
v_max_f32 v254, v254, v187
v_cmp_le_i32 vcc, 16, v7
s_nop 1
v_cndmask_b32 v188, v241, v188, vcc
v_max_f32 v255, v255, v188
v_cmp_le_i32 vcc, 17, v7
s_nop 1
v_cndmask_b32 v189, v241, v189, vcc
v_max_f32 v255, v255, v189
v_cmp_le_i32 vcc, 18, v7
s_nop 1
v_cndmask_b32 v190, v241, v190, vcc
v_max_f32 v255, v255, v190
v_cmp_le_i32 vcc, 19, v7
s_nop 1
v_cndmask_b32 v191, v241, v191, vcc
v_max_f32 v255, v255, v191
v_subrev_u32 v4, 32, v4
v_subrev_u32 v5, 32, v5
v_subrev_u32 v6, 32, v6
v_subrev_u32 v7, 32, v7
s_add_u32 s82, s82, 8192
s_sub_u32 s84, s84, 1
s_cmp_lg_u32 s84, 0
s_cbranch_scc1 .LL_50%=
G 28,0
v_lshlrev_b32 v32, 16, v28
v_and_b32 v33, 0xffff0000, v28
G 28,1
v_lshlrev_b32 v34, 16, v28
v_and_b32 v35, 0xffff0000, v28
G 28,2
v_lshlrev_b32 v36, 16, v28
v_and_b32 v37, 0xffff0000, v28
G 28,3
v_lshlrev_b32 v38, 16, v28
v_and_b32 v39, 0xffff0000, v28
G 28,4
v_lshlrev_b32 v40, 16, v28
v_and_b32 v41, 0xffff0000, v28
G 28,5
v_lshlrev_b32 v42, 16, v28
v_and_b32 v43, 0xffff0000, v28
G 28,6
v_lshlrev_b32 v44, 16, v28
v_and_b32 v45, 0xffff0000, v28
G 28,7
v_lshlrev_b32 v46, 16, v28
v_and_b32 v47, 0xffff0000, v28
G 28,8
v_lshlrev_b32 v48, 16, v28
v_and_b32 v49, 0xffff0000, v28
G 28,9
v_lshlrev_b32 v50, 16, v28
)"
R"(v_and_b32 v51, 0xffff0000, v28
G 28,10
v_lshlrev_b32 v52, 16, v28
v_and_b32 v53, 0xffff0000, v28
G 28,11
v_lshlrev_b32 v54, 16, v28
v_and_b32 v55, 0xffff0000, v28
G 28,12
v_lshlrev_b32 v56, 16, v28
v_and_b32 v57, 0xffff0000, v28
G 28,13
v_lshlrev_b32 v58, 16, v28
v_and_b32 v59, 0xffff0000, v28
G 28,14
v_lshlrev_b32 v60, 16, v28
v_and_b32 v61, 0xffff0000, v28
G 28,15
v_lshlrev_b32 v62, 16, v28
v_and_b32 v63, 0xffff0000, v28
G 28,16
v_lshlrev_b32 v64, 16, v28
v_and_b32 v65, 0xffff0000, v28
G 28,17
v_lshlrev_b32 v66, 16, v28
v_and_b32 v67, 0xffff0000, v28
G 28,18
v_lshlrev_b32 v68, 16, v28
v_and_b32 v69, 0xffff0000, v28
G 28,19
v_lshlrev_b32 v70, 16, v28
v_and_b32 v71, 0xffff0000, v28
G 28,20
v_lshlrev_b32 v72, 16, v28
v_and_b32 v73, 0xffff0000, v28
G 28,21
v_lshlrev_b32 v74, 16, v28
v_and_b32 v75, 0xffff0000, v28
G 28,22
v_lshlrev_b32 v76, 16, v28
v_and_b32 v77, 0xffff0000, v28
G 28,23
v_lshlrev_b32 v78, 16, v28
v_and_b32 v79, 0xffff0000, v28
G 28,24
v_lshlrev_b32 v80, 16, v28
v_and_b32 v81, 0xffff0000, v28
G 28,25
v_lshlrev_b32 v82, 16, v28
v_and_b32 v83, 0xffff0000, v28
G 28,26
v_lshlrev_b32 v84, 16, v28
v_and_b32 v85, 0xffff0000, v28
G 28,27
v_lshlrev_b32 v86, 16, v28
v_and_b32 v87, 0xffff0000, v28
G 28,28
v_lshlrev_b32 v88, 16, v28
v_and_b32 v89, 0xffff0000, v28
G 28,29
v_lshlrev_b32 v90, 16, v28
v_and_b32 v91, 0xffff0000, v28
G 28,30
v_lshlrev_b32 v92, 16, v28
v_and_b32 v93, 0xffff0000, v28
G 28,31
v_lshlrev_b32 v94, 16, v28
v_and_b32 v95, 0xffff0000, v28
G 28,32
v_lshlrev_b32 v96, 16, v28
v_and_b32 v97, 0xffff0000, v28
G 28,33
v_lshlrev_b32 v98, 16, v28
v_and_b32 v99, 0xffff0000, v28
G 28,34
v_lshlrev_b32 v100, 16, v28
v_and_b32 v101, 0xffff0000, v28
G 28,35
v_lshlrev_b32 v102, 16, v28
v_and_b32 v103, 0xffff0000, v28
G 28,36
v_lshlrev_b32 v104, 16, v28
v_and_b32 v105, 0xffff0000, v28
G 28,37
v_lshlrev_b32 v106, 16, v28
v_and_b32 v107, 0xffff0000, v28
G 28,38
v_lshlrev_b32 v108, 16, v28
v_and_b32 v109, 0xffff0000, v28
G 28,39
v_lshlrev_b32 v110, 16, v28
v_and_b32 v111, 0xffff0000, v28
G 28,40
v_lshlrev_b32 v112, 16, v28
v_and_b32 v113, 0xffff0000, v28
G 28,41
v_lshlrev_b32 v114, 16, v28
v_and_b32 v115, 0xffff0000, v28
G 28,42
v_lshlrev_b32 v116, 16, v28
v_and_b32 v117, 0xffff0000, v28
G 28,43
v_lshlrev_b32 v118, 16, v28
v_and_b32 v119, 0xffff0000, v28
G 28,44
v_lshlrev_b32 v120, 16, v28
v_and_b32 v121, 0xffff0000, v28
G 28,45
v_lshlrev_b32 v122, 16, v28
v_and_b32 v123, 0xffff0000, v28
G 28,46
v_lshlrev_b32 v124, 16, v28
v_and_b32 v125, 0xffff0000, v28
G 28,47
v_lshlrev_b32 v126, 16, v28
v_and_b32 v127, 0xffff0000, v28
G 28,48
v_lshlrev_b32 v128, 16, v28
v_and_b32 v129, 0xffff0000, v28
G 28,49
v_lshlrev_b32 v130, 16, v28
v_and_b32 v131, 0xffff0000, v28
G 28,50
v_lshlrev_b32 v132, 16, v28
v_and_b32 v133, 0xffff0000, v28
G 28,51
v_lshlrev_b32 v134, 16, v28
v_and_b32 v135, 0xffff0000, v28
G 28,52
v_lshlrev_b32 v136, 16, v28
v_and_b32 v137, 0xffff0000, v28
G 28,53
v_lshlrev_b32 v138, 16, v28
v_and_b32 v139, 0xffff0000, v28
G 28,54
v_lshlrev_b32 v140, 16, v28
v_and_b32 v141, 0xffff0000, v28
G 28,55
v_lshlrev_b32 v142, 16, v28
v_and_b32 v143, 0xffff0000, v28
G 28,56
v_lshlrev_b32 v144, 16, v28
v_and_b32 v145, 0xffff0000, v28
G 28,57
v_lshlrev_b32 v146, 16, v28
v_and_b32 v147, 0xffff0000, v28
G 28,58
v_lshlrev_b32 v148, 16, v28
v_and_b32 v149, 0xffff0000, v28
G 28,59
v_lshlrev_b32 v150, 16, v28
v_and_b32 v151, 0xffff0000, v28
G 28,60
v_lshlrev_b32 v152, 16, v28
v_and_b32 v153, 0xffff0000, v28
G 28,61
v_lshlrev_b32 v154, 16, v28
v_and_b32 v155, 0xffff0000, v28
G 28,62
v_lshlrev_b32 v156, 16, v28
v_and_b32 v157, 0xffff0000, v28
G 28,63
v_lshlrev_b32 v158, 16, v28
v_and_b32 v159, 0xffff0000, v28
s_mov_b32 s22, 1.0
s_mov_b32 s23, 1.0
s_mov_b32 s29, 134217728
s_mov_b32 s71, 3584
s_barrier
Ra 128,242,0
Ra 132,242,1024
Ra 136,242,2048
Ra 140,242,3072
s_cmp_ge_u32 s14, 2
s_cbranch_scc1 .Lbgo%=
s_branch .Lago%=
.p2align 12
.Lpb_27%=:
s_waitcnt vmcnt(6) lgkmcnt(6)
Zvaa 32,0,32
s_add_u32 s28, s28, 512
Ta 20,4,8192
Ta 22,4,9216
Mvaa 32,4,48
s_cmp_eq_u32 s28, s4
s_cselect_b32 s93, 10240, 0
Ta 24,5,0
Ta 26,5,1024
Zvaa 64,4,52
s_sub_u32 s28, s28, s93
s_add_u32 s93, s28, 20480
buffer_load_dwordx4 a[128:131], v26, s[8:11], s28 offen offset:0 sc1
Mvaa 64,0,36
Ta 28,5,8192
Ta 30,5,9216
Zvaa 96,0,40
K 184,184,185
K 185,186,187
Mvaa 96,4,56
K 186,188,189
K 187,190,191
Zvaa 128,4,60
K 192,192,193
Mvaa 128,0,44
W 8
buffer_load_dwordx4 a[132:135], v26, s[8:11], s93 offen offset:0 sc1
K 193,194,195
Zvaa 132,8,44
Ta 0,6,0
Ta 2,6,1024
Mvaa 132,12,60
K 194,196,197
K 195,198,199
Zvaa 100,12,56
K 200,200,201
K 201,202,203
Mvaa 100,8,40
Ta 4,6,8192
Ta 6,6,9216
Zvaa 68,8,36
buffer_load_dwordx4 a[136:139], v27, s[8:11], s28 offen offset:0 sc1
K 202,204,205
Mvaa 68,12,52
K 203,206,207
K 208,208,209
Zvaa 36,12,48
K 209,210,211
K 210,212,213
Mvaa 36,8,32
W 8
Ta 8,7,0
K 211,214,215
Zvaa 40,16,32
Ta 10,7,1024
K 216,216,217
Mvaa 40,20,48
buffer_load_dwordx4 a[140:143], v27, s[8:11], s93 offen offset:0 sc1
K 217,218,219
Zvaa 72,20,52
s_add_u32 s28, s28, 512
s_cmp_eq_u32 s28, s4
s_cselect_b32 s93, 10240, 0
s_sub_u32 s28, s28, s93
Mvaa 72,16,36
s_add_u32 s93, s28, 20480
K 218,220,221
K 219,222,223
Zvaa 104,16,40
Ta 12,7,8192
Ta 14,7,9216
Mvaa 104,20,56
K 224,224,225
K 225,226,227
Zvaa 136,20,60
K 226,228,229
K 227,230,231
Mvaa 136,16,44
W 8
buffer_load_dwordx4 a[144:147], v26, s[8:11], s28 offen offset:0 sc1
K 232,232,233
Zvaa 140,24,44
K 233,234,235
K 234,236,237
Mvaa 140,28,60
Ta 16,8,0
K 235,238,239
Zvaa 108,28,56
Ta 18,8,1024
K 240,240,241
Mvaa 108,24,40
K 241,242,243
K 242,244,245
Zvaa 76,24,36
buffer_load_dwordx4 a[148:151], v26, s[8:11], s93 offen offset:0 sc1
K 243,246,247
Mvaa 76,28,52
Ta 20,8,8192
Ta 22,8,9216
Zvaa 44,28,48
K 248,248,249
K 249,250,251
Mvaa 44,24,32
K 250,252,253
K 251,254,255
W 8
Zvaa 48,0,32
Ta 24,9,0
Ta 26,9,1024
Mvaa 48,4,48
buffer_load_dwordx4 a[152:155], v27, s[8:11], s28 offen offset:0 sc1
K 160,160,161
Zvaa 80,4,52
K 161,162,163
K 162,164,165
Mvaa 80,0,36
Ta 28,9,8192
K 163,166,167
Zvaa 112,0,40
Ta 30,9,9216
K 168,168,169
Mvaa 112,4,56
K 169,170,171
K 170,172,173
Zvaa 144,4,60
buffer_load_dwordx4 a[156:159], v27, s[8:11], s93 offen offset:0 sc1
K 171,174,175
Mvaa 144,0,44
W 8
s_add_u32 s28, s28, 512
s_cmp_eq_u32 s28, s4
s_cselect_b32 s93, 10240, 0
Zvaa 148,8,44
s_sub_u32 s28, s28, s93
s_add_u32 s93, s28, 20480
G 164,184
Mvaa 148,12,60
G 165,185
G 166,186
Zvaa 116,12,56
Ta 0,2,16384
Ta 2,2,17408
Mvaa 116,8,40
buffer_load_dwordx4 v[188:191], v26, s[8:11], s28 offen offset:0 sc1
G 167,187
Zvaa 84,8,36
Ta 4,2,24576
G 172,188
Mvaa 84,12,52
Ta 6,2,25600
G 173,189
Zvaa 52,12,48
G 174,190
G 175,191
Mvaa 52,8,32
W 8
Ta 8,3,16384
K 164,164,165
Zvaa 56,16,32
buffer_load_dwordx4 v[196:199], v26, s[8:11], s93 offen offset:0 sc1
Ta 10,3,17408
Mvaa 56,20,48
K 165,166,167
K 166,172,173
Zvaa 88,20,52
Ta 12,3,24576
K 167,174,175
Mvaa 88,16,36
Ta 14,3,25600
v_accvgpr_write_b32 a184, v164
Zvaa 120,16,40
Mvaa 120,20,56
buffer_load_dwordx4 v[204:207], v27, s[8:11], s28 offen offset:0 sc1
v_accvgpr_write_b32 a185, v165
Zvaa 152,20,60
v_accvgpr_write_b32 a186, v166
Mvaa 152,16,44
W 8
Ta 16,4,16384
v_accvgpr_write_b32 a187, v167
Zvaa 156,24,44
Ta 18,4,17408
v_accvgpr_write_b32 a188, v240
Mvaa 156,28,60
Zvaa 124,28,56
buffer_load_dwordx4 v[212:215], v27, s[8:11], s93 offen offset:0 sc1
v_accvgpr_write_b32 a189, v241
Mvaa 124,24,40
s_add_u32 s28, s28, 512
s_cmp_eq_u32 s28, s4
s_cselect_b32 s93, 10240, 0
s_sub_u32 s28, s28, s93
Zvaa 92,24,36
s_add_u32 s93, s28, 20480
Ta 20,4,24576
Ta 22,4,25600
Mvaa 92,28,52
v_accvgpr_write_b32 a190, v242
v_accvgpr_write_b32 a191, v243
Zvaa 60,28,48
G 164,192
Mvaa 60,24,32
s_waitcnt vmcnt(4) lgkmcnt(8)
buffer_load_dwordx4 v[220:223], v26, s[8:11], s28 offen offset:0 sc1
G 165,193
Mvaa 32,0,128
Ta 24,5,16384
Ta 26,5,17408
Mvaa 32,4,144
G 166,194
G 167,195
Mvaa 64,4,148
Ta 28,5,24576
Ta 30,5,25600
Mvaa 64,0,132
G 172,196
Mvaa 96,0,136
buffer_load_dwordx4 v[228:231], v26, s[8:11], s93 offen offset:0 sc1
G 173,197
Mvaa 96,4,152
G 174,198
Mvaa 128,4,156
)"
R"(G 175,199
Mvaa 128,0,140
W 8
Ta 0,6,16384
Ta 2,6,17408
Mvaa 132,8,140
K 164,164,165
Mvaa 132,12,156
buffer_load_dwordx4 v[236:239], v27, s[8:11], s28 offen offset:0 sc1
K 165,166,167
Mvaa 100,12,152
Ta 4,6,24576
K 166,172,173
Mvaa 100,8,136
Ta 6,6,25600
K 167,174,175
Mvaa 68,8,132
v_accvgpr_write_b32 a192, v164
Mvaa 68,12,148
Mvaa 36,12,144
buffer_load_dwordx4 v[244:247], v27, s[8:11], s93 offen offset:0 sc1
v_accvgpr_write_b32 a193, v165
Mvaa 36,8,128
W 8
s_add_u32 s28, s28, 512
s_cmp_eq_u32 s28, s4
s_cselect_b32 s93, 10240, 0
Mvaa 40,16,128
s_sub_u32 s28, s28, s93
s_add_u32 s93, s28, 20480
v_accvgpr_write_b32 a194, v166
Mvaa 40,20,144
Ta 8,7,16384
Ta 10,7,17408
Mvaa 72,20,148
v_accvgpr_write_b32 a195, v167
v_accvgpr_write_b32 a196, v248
Mvaa 72,16,132
D 22,48,65,2048
Ta 12,7,24576
Mvaa 104,16,136
Ta 14,7,25600
v_accvgpr_write_b32 a197, v249
Mvaa 104,20,152
v_accvgpr_write_b32 a198, v250
v_accvgpr_write_b32 a199, v251
Mvaa 136,20,156
G 164,200
Mvaa 136,16,140
W 8
Ta 16,8,16384
Ta 18,8,17408
Mvaa 140,24,140
buffer_load_dwordx4 a[32:35], v26, s[8:11], s28 offen offset:0 sc1
G 165,201
Mvaa 140,28,156
G 166,202
Mvaa 108,28,152
Ta 20,8,24576
G 167,203
Mvaa 108,24,136
Ta 22,8,25600
G 172,204
Mvaa 76,24,132
Mvaa 76,28,148
buffer_load_dwordx4 a[36:39], v26, s[8:11], s93 offen offset:0 sc1
G 173,205
Mvaa 44,28,144
G 174,206
Mvaa 44,24,128
W 8
Ta 24,9,16384
G 175,207
Mvaa 48,0,128
Ta 26,9,17408
K 164,164,165
Mvaa 48,4,144
Mvaa 80,4,148
buffer_load_dwordx4 a[40:43], v27, s[8:11], s28 offen offset:0 sc1
K 165,166,167
Mvaa 80,0,132
Ta 28,9,24576
Ta 30,9,25600
Mvaa 112,0,136
s_lshl_b32 s80, s56, 9
K 166,172,173
K 167,174,175
Mvaa 112,4,152
v_accvgpr_write_b32 a200, v164
Mvaa 144,4,156
s_lshl_b32 s81, s57, 4
Mvaa 144,0,140
W 8
buffer_load_dwordx4 a[44:47], v27, s[8:11], s93 offen offset:0 sc1
v_accvgpr_write_b32 a201, v165
Mvaa 148,8,140
s_add_u32 s28, s28, 512
s_cmp_eq_u32 s28, s4
s_cselect_b32 s93, 10240, 0
s_sub_u32 s28, s28, s93
Mvaa 148,12,156
s_add_u32 s93, s28, 20480
s_add_u32 s80, s80, s81
Ta 0,10,0
Mvaa 116,12,152
s_and_b32 s81, s14, 1
Ta 2,10,1024
v_accvgpr_write_b32 a202, v166
Mvaa 116,8,136
s_lshl_b32 s81, s81, 3
Ta 4,10,8192
v_accvgpr_write_b32 a203, v167
Mvaa 84,8,132
s_add_u32 s80, s80, s81
buffer_load_dwordx4 a[48:51], v26, s[8:11], s28 offen offset:0 sc1
Ta 6,10,9216
Mvaa 84,12,148
s_add_u32 s71, s80, 4096
v_accvgpr_write_b32 a204, v184
v_accvgpr_write_b32 a205, v185
Mvaa 52,12,144
v_accvgpr_write_b32 a206, v186
v_accvgpr_write_b32 a207, v187
Mvaa 52,8,128
W 8
Ta 8,11,0
Ta 10,11,1024
Mvaa 56,16,128
G 164,208
Mvaa 56,20,144
D 23,48,65,3072
G 165,209
Mvaa 88,20,148
s_add_u32 s65, s5, 16
s_cmp_ge_u32 s65, 20
s_cselect_b32 s90, 20, 0
s_sub_u32 s65, s65, s90
Mvaa 88,16,132
s_lshl_b32 s65, s65, 18
s_add_u32 m0, s63, 98304
G 166,210
Mvaa 120,16,136
Ta 12,11,8192
Ta 14,11,9216
Mvaa 120,20,152
G 167,211
G 172,212
Mvaa 152,20,156
buffer_load_dwordx4 a[52:55], v26, s[8:11], s93 offen offset:0 sc1
G 173,213
Mvaa 152,16,140
W 8
Ta 16,12,0
G 174,214
Mvaa 156,24,140
s_lshr_b32 s80, s15, 3
Ta 18,12,1024
Mvaa 156,28,156
G 175,215
Mvaa 124,28,152
s_lshl_b32 s80, s80, 1
Ta 20,12,8192
K 164,164,165
Mvaa 124,24,136
s_add_u32 s80, s80, s14
buffer_load_dwordx4 a[56:59], v27, s[8:11], s28 offen offset:0 sc1
Ta 22,12,9216
Mvaa 92,24,132
s_sub_u32 s80, s80, 2
K 165,166,167
K 166,172,173
Mvaa 92,28,148
s_lshl_b32 s80, s80, 12
Mvaa 60,28,144
K 167,174,175
v_min_u32 v28, 496, v1
Mvaa 60,24,128
s_waitcnt vmcnt(9) lgkmcnt(8)
v_accvgpr_write_b32 a208, v164
v_lshl_add_u32 v28, v28, 3, s80
Mvav 32,0,188
buffer_load_dwordx4 a[60:63], v27, s[8:11], s93 offen offset:0 sc1
Ta 24,13,0
Mvav 32,4,220
s_add_u32 s28, s28, 512
s_cmp_eq_u32 s28, s4
s_cselect_b32 s93, 10240, 0
s_sub_u32 s28, s28, s93
Mvav 64,4,228
s_add_u32 s93, s28, 20480
Ta 26,13,1024
v_min_u32 v28, .Lend%=-.Lbeg%=-4, v28
Mvav 64,0,196
v_accvgpr_write_b32 a209, v165
v_accvgpr_write_b32 a210, v166
Mvav 96,0,204
Ta 28,13,8192
Ta 30,13,9216
Mvav 96,4,236
D 16,48,65,0
v_accvgpr_write_b32 a211, v167
Mvav 128,4,244
v_accvgpr_write_b32 a212, v192
v_accvgpr_write_b32 a213, v193
Mvav 128,0,212
W 8
Ta 0,14,0
v_accvgpr_write_b32 a214, v194
Mvav 132,8,212
Ta 2,14,1024
v_add_u32 v2, 65536, v10
Mvav 132,12,244
v_accvgpr_write_b32 a215, v195
G 164,216
Mvav 100,12,236
buffer_load_dwordx4 v[184:187], v26, s[8:11], s28 offen offset:0 sc1
Ta 4,14,8192
Mvav 100,8,204
Ta 6,14,9216
v_add_u32 v3, 65536, v11
Mvav 68,8,196
G 165,217
G 166,218
Mvav 68,12,228
G 167,219
Mvav 36,12,220
G 172,220
Mvav 36,8,188
W 8
D 17,48,65,1024
Ta 8,15,0
Mvav 40,16,188
Ta 10,15,1024
v_add_u32 v4, 65536, v12
Mvav 40,20,220
G 173,221
G 174,222
Mvav 72,20,228
Ta 12,15,8192
G 175,223
Mvav 72,16,196
Ta 14,15,9216
v_add_u32 v5, 65536, v13
Mvav 104,16,204
buffer_load_dwordx4 a[128:131], v26, s[8:11], s93 offen offset:0 sc1
K 164,164,165
Mvav 104,20,236
K 165,166,167
Mvav 136,20,244
K 166,172,173
Mvav 136,16,212
W 8
Ta 16,24,0
K 167,174,175
Mvav 140,24,212
Ta 18,24,1024
v_add_u32 v6, 65536, v14
Mvav 140,28,244
buffer_load_dwordx4 a[132:135], v27, s[8:11], s28 offen offset:0 sc1
v_accvgpr_write_b32 a216, v164
Mvav 108,28,236
Ta 20,24,8192
v_accvgpr_write_b32 a217, v165
Mvav 108,24,204
Ta 22,24,9216
v_add_u32 v7, 65536, v15
Mvav 76,24,196
v_accvgpr_write_b32 a218, v166
v_accvgpr_write_b32 a219, v167
Mvav 76,28,228
v_accvgpr_write_b32 a220, v200
Mvav 44,28,220
buffer_load_dwordx4 a[136:139], v27, s[8:11], s93 offen offset:0 sc1
Mvav 44,24,188
W 8
s_add_u32 s28, s28, 512
s_cmp_eq_u32 s28, s4
s_cselect_b32 s93, 10240, 0
Mvav 48,0,188
s_sub_u32 s28, s28, s93
s_add_u32 s93, s28, 20480
v_accvgpr_write_b32 a221, v201
Mvav 48,4,220
Ta 24,25,0
Ta 26,25,1024
Mvav 80,4,228
v_add_u32 v8, 65536, v24
v_accvgpr_write_b32 a222, v202
Mvav 80,0,196
buffer_load_dwordx4 a[140:143], v26, s[8:11], s28 offen offset:0 sc1
v_accvgpr_write_b32 a223, v203
Mvav 112,0,204
Ta 28,25,8192
Ta 30,25,9216
Mvav 112,4,236
v_add_u32 v9, 65536, v25
G 164,224
Mvav 144,4,244
G 165,225
G 166,226
Mvav 144,0,212
W 8
Ta 0,10,16384
G 167,227
Mvav 148,8,212
global_load_dword v29, v28, s[6:7]
Ta 2,10,17408
Mvav 148,12,244
s_sub_u32 s74, s85, 66
s_and_b32 s75, s74, 0xffffffe3
s_cselect_b64 s[72:73], -1, 0
s_and_b32 s74, s74, 28
Mvav 116,12,236
s_lshl_b32 s74, s74, 1
v_mov_b32 v28, s74
G 172,228
Mvav 116,8,204
G 173,229
G 174,230
Mvav 84,8,196
Ta 4,10,24576
Ta 6,10,25600
Mvav 84,12,228
buffer_load_dwordx4 a[144:147], v26, s[8:11], s93 offen offset:0 sc1
G 175,231
Mvav 52,12,220
K 164,164,165
K 165,166,167
Mvav 52,8,188
W 8
Ta 8,11,16384
K 166,172,173
Mvav 56,16,188
Ta 10,11,17408
K 167,174,175
Mvav 56,20,220
v_accvgpr_write_b32 a224, v164
Mvav 88,20,228
buffer_load_dwordx4 a[148:151], v27, s[8:11], s28 offen offset:0 sc1
Ta 12,11,24576
Mvav 88,16,196
Ta 14,11,25600
v_accvgpr_write_b32 a225, v165
Mvav 120,16,204
v_accvgpr_write_b32 a226, v166
Mvav 120,20,236
v_accvgpr_write_b32 a227, v167
Mvav 152,20,244
v_accvgpr_write_b32 a228, v208
Mvav 152,16,212
W 8
D 18,48,65,2048
Ta 16,12,16384
Mvav 156,24,212
Ta 18,12,17408
v_accvgpr_write_b32 a229, v209
Mvav 156,28,244
v_accvgpr_write_b32 a230, v210
Mvav 124,28,236
Ta 20,12,24576
v_accvgpr_write_b32 a231, v211
Mvav 124,24,204
Ta 22,12,25600
G 164,232
Mvav 92,24,196
buffer_load_dwordx4 a[152:155], v27, s[8:11], s93 offen offset:0 sc1
G 165,233
Mvav 92,28,228
s_add_u32 s28, s28, 512
s_cmp_eq_u32 s28, s4
s_cselect_b32 s93, 10240, 0
s_sub_u32 s28, s28, s93
Mvav 60,28,220
s_add_u32 s93, s28, 20480
G 166,234
G 167,235
Mvav 60,24,188
s_waitcnt vmcnt(12) lgkmcnt(8)
G 172,236
G 173,237
Mvaa 32,0,32
Ta 24,13,16384
Ta 26,13,17408
Mvaa 32,4,48
buffer_load_dwordx2 v[28:29], v28, s[8:11], 0 offen offset:3584 sc1
G 174,238
Mvaa 64,4,52
G 175,239
K 164,164,165
Mvaa 64,0,36
Ta 28,13,24576
Ta 30,13,25600
Mvaa 96,0,40
K 165,166,167
K 166,172,173
Mvaa 96,4,56
K 167,174,175
v_accvgpr_write_b32 a232, v164
Mvaa 128,4,60
buffer_load_dwordx4 a[156:159], v26, s[8:11], s28 offen offset:0 sc1
)"
R"(v_accvgpr_write_b32 a233, v165
Mvaa 128,0,44
W 8
v_accvgpr_write_b32 a234, v166
v_accvgpr_write_b32 a235, v167
Mvaa 132,8,44
Ta 0,14,16384
Ta 2,14,17408
Mvaa 132,12,60
G 164,160
G 165,161
Mvaa 100,12,56
G 166,162
G 167,163
Mvaa 100,8,40
buffer_load_dwordx4 v[192:195], v26, s[8:11], s93 offen offset:0 sc1
Ta 4,14,24576
Mvaa 68,8,36
Ta 6,14,25600
G 172,164
Mvaa 68,12,52
G 173,165
G 174,166
Mvaa 36,12,48
G 175,167
K 164,164,165
Mvaa 36,8,32
W 8
Ta 8,15,16384
K 165,166,167
Mvaa 40,16,32
buffer_load_dwordx4 v[200:203], v27, s[8:11], s28 offen offset:0 sc1
Ta 10,15,17408
Mvaa 40,20,48
K 166,172,173
K 167,174,175
Mvaa 72,20,52
s_cmp_lg_u32 s72, 0
s_cbranch_scc1 .LL_51%=
s_waitcnt vmcnt(3)
.LL_51%=:
v_xor_b32 v28, s12, v28
v_xor_b32 v29, s13, v29
v_lshrrev_b32 v248, 11, v28
v_or_b32 v29, v29, v248
v_cmp_eq_u32 vcc, 0, v29
s_or_b64 vcc, vcc, s[72:73]
s_cmp_eq_u64 vcc, -1
s_cbranch_scc0 .Lspin_52%=
.Lok_53%=:
v_accvgpr_write_b32 a236, v164
v_accvgpr_write_b32 a237, v165
Mvaa 72,16,36
Ta 12,15,24576
Ta 14,15,25600
Mvaa 104,16,40
v_accvgpr_write_b32 a238, v166
v_accvgpr_write_b32 a239, v167
Mvaa 104,20,56
D 19,48,65,3072
G 164,240
Mvaa 136,20,60
s_add_u32 m0, s63, 102400
G 165,241
G 166,242
Mvaa 136,16,44
W 8
v_bfe_u32 v248, v28, 3, 8
G 167,243
Mvaa 140,24,44
Ta 16,24,16384
Ta 18,24,17408
Mvaa 140,28,60
v_mul_u32_u24 v248, 10240, v248
G 172,244
Mvaa 108,28,56
buffer_load_dwordx4 v[188:191], v27, s[8:11], s93 offen offset:0 sc1
G 173,245
Mvaa 108,24,40
s_add_u32 s28, s28, 512
s_cmp_eq_u32 s28, s4
s_cselect_b32 s93, 10240, 0
s_sub_u32 s28, s28, s93
Mvaa 76,24,36
s_add_u32 s93, s28, 20480
v_and_b32 v28, 7, v28
G 174,246
Mvaa 76,28,52
v_lshl_add_u32 v248, v28, 4, v248
G 175,247
Mvaa 44,28,48
Ta 20,24,24576
Ta 22,24,25600
Mvaa 44,24,32
W 8
buffer_load_dwordx4 v[196:199], v26, s[8:11], s28 offen offset:0 sc1
K 164,164,165
Mvaa 48,0,32
v_lshl_add_u32 v248, v1, 3, v248
K 165,166,167
Mvaa 48,4,48
v_lshrrev_b32 v29, 8, v1
K 166,172,173
Mvaa 80,4,52
K 167,174,175
v_lshlrev_b32 v29, 28, v29
Mvaa 80,0,36
v_accvgpr_write_b32 a240, v164
v_or_b32 v249, v248, v29
Mvaa 112,0,40
s_and_b32 s75, s72, 134217728
buffer_load_dwordx4 v[204:207], v26, s[8:11], s93 offen offset:0 sc1
v_accvgpr_write_b32 a241, v165
Mvaa 112,4,56
s_add_u32 s76, s75, 131072
Ta 24,25,16384
Ta 26,25,17408
Mvaa 144,4,60
v_accvgpr_write_b32 a242, v166
v_accvgpr_write_b32 a243, v167
Mvaa 144,0,44
W 6
G 164,168
G 165,169
Mvaa 148,8,44
Ta 28,25,24576
Ta 30,25,25600
Mvaa 148,12,60
D 20,48,65,0
G 166,170
Mvaa 116,12,56
G 167,171
G 172,172
Mvaa 116,8,40
G 173,173
G 174,174
Mvaa 84,8,36
Ta 0,2,0
G 175,175
Mvaa 84,12,52
Ta 2,2,1024
K 164,164,165
Mvaa 52,12,48
buffer_load_dwordx4 v[212:215], v27, s[8:11], s28 offen offset:0 sc1
K 165,166,167
Mvaa 52,8,32
W 6
K 166,172,173
K 167,174,175
Mvaa 56,16,32
Ta 4,2,8192
Ta 6,2,9216
Mvaa 56,20,48
v_accvgpr_write_b32 a244, v164
v_accvgpr_write_b32 a245, v165
Mvaa 88,20,52
v_accvgpr_write_b32 a246, v166
v_accvgpr_write_b32 a247, v167
Mvaa 88,16,36
buffer_load_dwordx4 v[220:223], v27, s[8:11], s93 offen offset:0 sc1
G 164,64
Mvaa 120,16,40
s_add_u32 s28, s28, 512
s_cmp_eq_u32 s28, s4
s_cselect_b32 s93, 10240, 0
s_sub_u32 s28, s28, s93
Mvaa 120,20,56
s_add_u32 s93, s28, 20480
Ta 8,3,0
G 165,65
Mvaa 152,20,60
Ta 10,3,1024
G 166,66
Mvaa 152,16,44
W 6
G 167,67
G 172,68
Mvaa 156,24,44
buffer_load_dwordx4 v[228:231], v26, s[8:11], s28 offen offset:0 sc1
G 173,69
Mvaa 156,28,60
Ta 12,3,8192
Ta 14,3,9216
Mvaa 124,28,56
G 174,70
G 175,71
Mvaa 124,24,40
K 164,164,165
K 165,166,167
Mvaa 92,24,36
K 166,172,173
K 167,174,175
Mvaa 92,28,52
buffer_load_dwordx4 v[240:243], v248, s[8:11], s76 offen sc1
Ta 16,4,0
Mvaa 60,28,48
s_add_u32 s76, s76, 8192
Ta 18,4,1024
v_accvgpr_write_b32 a64, v164
Mvaa 60,24,32
s_waitcnt vmcnt(13) lgkmcnt(6)
v_accvgpr_write_b32 a65, v165
v_accvgpr_write_b32 a66, v166
Mvav 32,0,184
Ta 20,4,8192
v_accvgpr_write_b32 a67, v167
Mvaa 32,4,140
Ta 22,4,9216
G 164,176
Mvaa 64,4,144
buffer_load_dwordx4 v[244:247], v249, s[8:11], s76 offen sc1
v_or_b32 v249, v1, v29
Mvaa 64,0,128
s_mul_i32 s76, s74, 160
s_add_u32 s76, s76, s75
s_add_u32 s76, s76, 118784
Mvaa 96,0,132
G 165,177
G 166,178
Mvaa 96,4,148
G 167,179
G 172,180
Mvaa 128,4,152
Ta 24,5,0
G 173,181
Mvaa 128,0,136
W 5
buffer_load_dwordx4 v[236:239], v26, s[8:11], s93 offen offset:0 sc1
Ta 26,5,1024
Mvaa 132,8,136
G 174,182
G 175,183
Mvaa 132,12,152
K 164,164,165
K 165,166,167
Mvaa 100,12,148
Ta 28,5,8192
Ta 30,5,9216
Mvaa 100,8,132
K 166,172,173
K 167,174,175
Mvaa 68,8,128
D 21,48,65,1024
v_accvgpr_write_b32 a68, v164
Mvaa 68,12,144
v_accvgpr_write_b32 a69, v165
v_accvgpr_write_b32 a70, v166
Mvaa 36,12,140
Ta 0,6,0
v_accvgpr_write_b32 a71, v167
Mvav 36,8,184
W 5
Ta 2,6,1024
G 164,72
Mvav 40,16,184
G 165,73
G 166,74
Mvaa 40,20,140
buffer_load_dwordx4 v[208:211], v27, s[8:11], s28 offen offset:0 sc1
Ta 4,6,8192
Mvaa 72,20,144
Ta 6,6,9216
G 167,75
Mvaa 72,16,128
G 172,76
G 173,77
Mvaa 104,16,132
Ta 8,7,0
G 174,78
Mvaa 104,20,148
Ta 10,7,1024
G 175,79
Mvaa 136,20,152
buffer_load_dwordx4 a[32:35], v27, s[8:11], s93 offen offset:0 sc1
K 164,164,165
Mvaa 136,16,136
W 6
s_add_u32 s28, s28, 512
s_cmp_eq_u32 s28, s4
s_cselect_b32 s93, 10240, 0
Mvaa 140,24,136
s_sub_u32 s28, s28, s93
s_add_u32 s93, s28, 20480
Ta 12,7,8192
Mvaa 140,28,152
Ta 14,7,9216
K 165,166,167
Mvaa 108,28,148
K 166,172,173
K 167,174,175
Mvaa 108,24,132
buffer_load_dwordx4 a[36:39], v26, s[8:11], s28 offen offset:0 sc1
v_accvgpr_write_b32 a72, v164
Mvaa 76,24,128
Ta 16,8,0
Ta 18,8,1024
Mvaa 76,28,144
v_accvgpr_write_b32 a73, v165
v_accvgpr_write_b32 a74, v166
Mvaa 44,28,140
Ta 20,8,8192
v_accvgpr_write_b32 a75, v167
Mvav 44,24,184
W 7
Ta 22,8,9216
v_accvgpr_write_b32 a76, v216
Mvav 48,0,184
D 22,48,65,2048
v_accvgpr_write_b32 a77, v217
Mvaa 48,4,140
v_accvgpr_write_b32 a78, v218
v_accvgpr_write_b32 a79, v219
Mvaa 80,4,144
Ta 24,9,0
Ta 26,9,1024
Mvaa 80,0,128
G 164,80
G 165,81
Mvaa 112,0,132
Ta 28,9,8192
Ta 30,9,9216
Mvaa 112,4,148
buffer_load_dwordx4 a[40:43], v26, s[8:11], s93 offen offset:0 sc1
G 166,82
Mvaa 144,4,152
G 167,83
G 172,84
Mvaa 144,0,136
W 8
Ta 0,2,16384
G 173,85
Mvaa 148,8,136
Ta 2,2,17408
G 174,86
Mvaa 148,12,152
G 175,87
Mvaa 116,12,148
buffer_load_dwordx4 a[44:47], v27, s[8:11], s28 offen offset:0 sc1
Ta 4,2,24576
Mvaa 116,8,132
Ta 6,2,25600
K 164,164,165
Mvaa 84,8,128
K 165,166,167
Mvaa 84,12,144
K 166,172,173
Mvaa 52,12,140
K 167,174,175
Mvav 52,8,184
W 8
buffer_load_dwordx4 a[48:51], v27, s[8:11], s93 offen offset:0 sc1
Ta 8,3,16384
Mvav 56,16,184
s_add_u32 s28, s28, 512
s_cmp_eq_u32 s28, s4
s_cselect_b32 s93, 10240, 0
D 23,48,65,3072
Mvaa 56,20,140
s_sub_u32 s28, s28, s93
s_add_u32 s93, s28, 20480
Ta 10,3,17408
Mvaa 88,20,144
v_accvgpr_write_b32 a80, v164
v_accvgpr_write_b32 a81, v165
Mvaa 88,16,128
Ta 12,3,24576
v_accvgpr_write_b32 a82, v166
Mvaa 120,16,132
Ta 14,3,25600
v_accvgpr_write_b32 a83, v167
Mvaa 120,20,148
buffer_load_dwordx4 a[52:55], v26, s[8:11], s28 offen offset:0 sc1
v_accvgpr_write_b32 a84, v224
Mvaa 152,20,152
v_lshrrev_b32 v20, 4, v1
v_accvgpr_write_b32 a85, v225
Mvaa 152,16,136
W 8
v_accvgpr_write_b32 a86, v226
v_add_u32 v20, 0, v20
Mvaa 156,24,136
Ta 16,4,16384
Ta 18,4,17408
Mvaa 156,28,152
v_accvgpr_write_b32 a87, v227
v_mul_u32_u24 v22, 205, v20
Mvaa 124,28,148
buffer_load_dwordx4 a[56:59], v26, s[8:11], s93 offen offset:0 sc1
G 164,88
Mvaa 124,24,132
Ta 20,4,24576
Ta 22,4,25600
Mvaa 92,24,128
v_lshrrev_b32 v22, 12, v22
G 165,89
Mvaa 92,28,144
G 166,90
v_mul_u32_u24 v23, 20, v22
Mvaa 60,28,140
G 167,91
)"
R"(v_sub_u32 v20, v20, v23
Mvav 60,24,184
s_waitcnt vmcnt(15) lgkmcnt(8)
buffer_load_dwordx4 a[60:63], v27, s[8:11], s28 offen offset:0 sc1
G 172,92
Mvaa 32,0,156
Ta 24,5,16384
Ta 26,5,17408
Mvav 32,4,196
v_lshrrev_b32 v23, 1, v22
G 173,93
Mvav 64,4,204
G 174,94
v_and_b32 v23, 2, v23
Mvav 64,0,192
Ta 28,5,24576
Ta 30,5,25600
Mvav 96,0,200
buffer_load_dwordx4 v[216:219], v27, s[8:11], s93 offen offset:0 sc1
G 175,95
Mvav 96,4,212
s_add_u32 s28, s28, 512
s_cmp_eq_u32 s28, s4
s_cselect_b32 s93, 10240, 0
s_sub_u32 s28, s28, s93
Mvav 128,4,220
s_add_u32 s93, s28, 20480
v_xor_b32 v20, v20, v23
K 164,164,165
Mvav 128,0,188
W 8
v_mul_u32_u24 v22, 80, v22
K 165,166,167
Mvav 132,8,188
K 166,172,173
v_add_lshl_u32 v16, v20, v22, 4
Mvav 132,12,220
buffer_load_dwordx4 v[184:187], v26, s[8:11], s28 offen offset:0 sc1
Ta 0,6,16384
Mvav 100,12,212
Ta 2,6,17408
K 167,174,175
Mvav 100,8,200
v_lshrrev_b32 v20, 4, v1
v_accvgpr_write_b32 a88, v164
Mvav 68,8,192
v_add_u32 v20, 64, v20
v_accvgpr_write_b32 a89, v165
Mvav 68,12,204
Ta 4,6,24576
Ta 6,6,25600
Mvav 36,12,196
s_cmp_lg_u32 s72, 0
s_cbranch_scc1 .LL_55%=
s_waitcnt vmcnt(15)
.LL_55%=:
buffer_store_dwordx4 v[240:243], v1, s[8:11], s76 offen sc1
v_accvgpr_write_b32 a90, v166
v_mul_u32_u24 v22, 205, v20
Mvaa 36,8,156
W 8
v_accvgpr_write_b32 a91, v167
v_lshrrev_b32 v22, 12, v22
Mvaa 40,16,156
v_accvgpr_write_b32 a92, v232
v_mul_u32_u24 v23, 20, v22
Mvav 40,20,196
Ta 8,7,16384
Ta 10,7,17408
Mvav 72,20,204
v_accvgpr_write_b32 a93, v233
v_accvgpr_write_b32 a94, v234
Mvav 72,16,192
buffer_store_dwordx4 v[244:247], v249, s[8:11], s76 offen offset:1024 sc1
v_sub_u32 v20, v20, v23
Mvav 104,16,200
s_add_u32 s76, s74, s75
s_add_u32 s76, s76, 3840
v_accvgpr_write_b32 a95, v235
Mvav 104,20,212
Ta 12,7,24576
Ta 14,7,25600
Mvav 136,20,220
v_lshrrev_b32 v23, 1, v22
G 164,96
Mvav 136,16,188
W 8
G 165,97
v_and_b32 v23, 2, v23
Mvav 140,24,188
buffer_load_dwordx4 a[128:131], v26, s[8:11], s93 offen offset:0 sc1
G 166,98
Mvav 140,28,220
G 167,99
v_xor_b32 v20, v20, v23
Mvav 108,28,212
G 172,100
G 173,101
Mvav 108,24,200
Ta 16,8,16384
Ta 18,8,17408
Mvav 76,24,192
v_mul_u32_u24 v22, 80, v22
G 174,102
Mvav 76,28,204
buffer_load_dwordx4 a[132:135], v27, s[8:11], s28 offen offset:0 sc1
G 175,103
Mvav 44,28,196
v_add_lshl_u32 v17, v20, v22, 4
K 164,164,165
Mvaa 44,24,156
W 6
Ta 20,8,24576
Ta 22,8,25600
Mvaa 48,0,156
K 165,166,167
K 166,172,173
Mvav 48,4,196
v_subrev_u32 v17, 1024, v17
K 167,174,175
Mvav 80,4,204
buffer_load_dwordx4 a[136:139], v27, s[8:11], s93 offen offset:0 sc1
v_accvgpr_write_b32 a96, v164
Mvav 80,0,192
s_add_u32 s28, s28, 512
v_lshrrev_b32 v20, 4, v1
v_accvgpr_write_b32 a97, v165
Mvav 112,0,200
s_cmp_eq_u32 s28, s4
s_cselect_b32 s93, 10240, 0
v_accvgpr_write_b32 a98, v166
v_add_u32 v20, 128, v20
Mvav 112,4,212
s_sub_u32 s28, s28, s93
Ta 24,9,16384
v_accvgpr_write_b32 a99, v167
Mvav 144,4,220
s_add_u32 s93, s28, 20480
Ta 26,9,17408
G 164,248
Mvav 144,0,188
W 6
buffer_load_dwordx4 a[140:143], v26, s[8:11], s28 offen offset:0 sc1
v_mul_u32_u24 v22, 205, v20
Mvav 148,8,188
G 165,249
G 166,250
Mvav 148,12,220
Ta 28,9,24576
Ta 30,9,25600
Mvav 116,12,212
v_lshrrev_b32 v22, 12, v22
G 167,251
Mvav 116,8,200
G 172,252
G 173,253
Mvav 84,8,192
s_waitcnt vmcnt(11) lgkmcnt(0)
s_barrier
Mvav 84,12,204
v_mul_u32_u24 v23, 20, v22
G 174,254
Mvav 52,12,196
G 175,255
v_sub_u32 v20, v20, v23
Mvaa 52,8,156
K 164,164,165
K 165,166,167
Mvaa 56,16,156
v_lshrrev_b32 v23, 1, v22
K 166,172,173
Mvav 56,20,196
buffer_load_dwordx4 a[144:147], v26, s[8:11], s93 offen offset:0 sc1
K 167,174,175
Mvav 88,20,204
v_and_b32 v23, 2, v23
v_accvgpr_write_b32 a100, v164
Mvav 88,16,192
v_accvgpr_write_b32 a101, v165
v_xor_b32 v20, v20, v23
Mvav 120,16,200
v_accvgpr_write_b32 a102, v166
v_accvgpr_write_b32 a103, v167
Mvav 120,20,212
Ta 0,10,32768
Ta 2,10,33792
Mvav 152,20,220
Ta 4,10,40960
Ta 6,10,41984
Mvav 152,16,188
G 164,104
v_mul_u32_u24 v22, 80, v22
Mvav 156,24,188
G 165,105
v_add_lshl_u32 v18, v20, v22, 4
Mvav 156,28,220
Ta 8,11,32768
G 166,106
Mvav 124,28,212
Ta 10,11,33792
G 167,107
Mvav 124,24,200
buffer_load_dwordx4 a[148:151], v27, s[8:11], s28 offen offset:0 sc1
v_subrev_u32 v18, 2048, v18
Mvav 92,24,192
G 172,108
G 173,109
Mvav 92,28,204
Ta 12,11,40960
v_lshrrev_b32 v20, 4, v1
Mvav 60,28,196
Ta 14,11,41984
G 174,110
Mvaa 60,24,156
W 4
v_add_u32 v20, 192, v20
G 175,111
Mvav 32,0,228
K 164,164,165
v_mul_u32_u24 v22, 205, v20
Mvaa 32,4,36
K 165,166,167
v_lshrrev_b32 v22, 12, v22
Mvaa 64,4,40
Ta 16,12,32768
K 166,172,173
Mvav 64,0,236
Ta 18,12,33792
K 167,174,175
Mvav 96,0,208
buffer_load_dwordx4 a[152:155], v27, s[8:11], s93 offen offset:0 sc1
v_mul_u32_u24 v23, 20, v22
Mvaa 96,4,44
s_add_u32 s28, s28, 512
v_accvgpr_write_b32 a104, v164
v_accvgpr_write_b32 a105, v165
Mvaa 128,4,48
s_cmp_eq_u32 s28, s4
s_cselect_b32 s93, 10240, 0
Ta 20,12,40960
v_sub_u32 v20, v20, v23
Mvaa 128,0,32
W 3
Ta 22,12,41984
v_accvgpr_write_b32 a106, v166
Mvaa 132,8,32
s_sub_u32 s28, s28, s93
v_lshrrev_b32 v23, 1, v22
v_accvgpr_write_b32 a107, v167
Mvaa 132,12,48
s_add_u32 s93, s28, 20480
K 176,176,177
v_and_b32 v23, 2, v23
Mvaa 100,12,44
K 177,178,179
K 178,180,181
Mvav 100,8,208
Ta 24,13,32768
v_xor_b32 v20, v20, v23
Mvav 68,8,236
Ta 26,13,33792
K 179,182,183
Mvaa 68,12,40
v_accvgpr_write_b32 a108, v176
v_accvgpr_write_b32 a109, v177
v_accvgpr_write_b32 a110, v178
v_accvgpr_write_b32 a111, v179
Mvaa 36,12,36
buffer_load_dwordx4 v[224:227], v26, s[8:11], s28 offen offset:0 sc1
v_mul_u32_u24 v22, 80, v22
Mvav 36,8,228
W 2
Ta 28,13,40960
Ta 30,13,41984
Mvav 40,16,228
v_add_lshl_u32 v19, v20, v22, 4
v_subrev_u32 v19, 3072, v19
Mvaa 40,20,36
G 164,112
v_lshrrev_b32 v20, 4, v1
Mvaa 72,20,40
Ta 0,14,32768
G 165,113
Mvav 72,16,236
s_cmp_lg_u32 s72, 0
s_cbranch_scc1 .LL_56%=
s_waitcnt vmcnt(8)
.LL_56%=:
v_mov_b64 v[28:29], s[12:13]
s_mov_b64 exec, 1
buffer_store_dwordx2 v[28:29], off, s[8:11], s76 sc1
s_mov_b64 exec, -1
Ta 2,14,33792
G 166,114
Mvav 104,16,208
v_add_u32 v20, 256, v20
G 167,115
Mvaa 104,20,44
Ta 4,14,40960
v_mul_u32_u24 v22, 205, v20
Mvaa 136,20,48
Ta 6,14,41984
G 172,116
Mvaa 136,16,32
W 4
v_lshrrev_b32 v22, 12, v22
G 173,117
Mvaa 140,24,32
buffer_load_dwordx4 v[232:235], v26, s[8:11], s93 offen offset:0 sc1
G 174,118
Mvaa 140,28,48
v_mul_u32_u24 v23, 20, v22
G 175,119
Mvaa 108,28,44
Ta 8,15,32768
v_sub_u32 v20, v20, v23
Mvav 108,24,208
Ta 10,15,33792
K 164,164,165
Mvav 76,24,236
v_lshrrev_b32 v23, 1, v22
K 165,166,167
Mvaa 76,28,40
buffer_load_dwordx4 a[248:251], v27, s[8:11], s28 offen offset:0 sc1
K 166,172,173
Mvaa 44,28,36
Ta 12,15,40960
v_and_b32 v23, 2, v23
Mvav 44,24,228
W 3
Ta 14,15,41984
K 167,174,175
Mvav 48,0,228
v_xor_b32 v20, v20, v23
v_accvgpr_write_b32 a112, v164
Mvaa 48,4,36
v_accvgpr_write_b32 a113, v165
v_mul_u32_u24 v22, 80, v22
Mvaa 80,4,40
Ta 16,24,32768
v_accvgpr_write_b32 a114, v166
Mvav 80,0,236
Ta 18,24,33792
v_add_lshl_u32 v20, v20, v22, 4
Mvav 112,0,208
buffer_load_dwordx4 a[252:255], v27, s[8:11], s93 offen offset:0 sc1
v_accvgpr_write_b32 a115, v167
Mvaa 112,4,44
s_add_u32 s28, s28, 512
v_lshrrev_b32 v22, 6, v1
v_accvgpr_write_b32 a116, v168
Mvaa 144,4,48
v_accvgpr_write_b32 a117, v169
v_accvgpr_write_b32 a118, v170
v_accvgpr_write_b32 a119, v171
Mvaa 144,0,32
W 2
Ta 20,24,40960
Ta 22,24,41984
Mvaa 148,8,32
s_cmp_eq_u32 s28, s4
s_cselect_b32 s93, 10240, 0
v_mul_u32_u24 v22, 320, v22
v_bfe_u32 v23, v1, 4, 2
Mvaa 148,12,48
s_sub_u32 s28, s28, s93
v_lshl_add_u32 v22, v23, 3, v22
G 164,120
Mvaa 116,12,44
s_add_u32 s93, s28, 20480
Ta 24,25,32768
Ta 26,25,33792
Mvav 116,8,208
G 165,121
v_bfe_u32 v23, v1, 8, 1
Mvav 84,8,236
buffer_load_dwordx4 v[192:195], v26, s[8:11], s28 offen offset:0 sc1
G 166,122
Mvaa 84,12,40
v_lshlrev_b32 v23, 5, v23
G 167,123
Mvaa 52,12,36
Ta 28,25,40960
Ta 30,25,41984
Mvav 52,8,228
W 4
v_add_u32 v21, v22, v23
G 172,124
Mvav 56,16,228
G 173,125
v_sub_u32 v22, v22, v23
Mvaa 56,20,36
s_mul_i32 s94, s59, 655360
Ta 0,10,49152
G 174,126
Mvaa 88,20,40
s_mul_i32 s95, s58, 320
Ta 2,10,50176
G 175,127
Mvav 88,16,236
)"
R"(s_add_u32 s94, s94, s95
buffer_load_dwordx4 v[200:203], v26, s[8:11], s93 offen offset:0 sc1
K 164,164,165
Mvav 120,16,208
Ta 4,10,57344
K 165,166,167
Mvaa 120,20,44
Ta 6,10,58368
K 166,172,173
Mvaa 152,20,48
s_add_u32 s40, s26, s94
s_addc_u32 s41, s27, 0
K 167,174,175
Mvaa 152,16,32
W 4
v_accvgpr_write_b32 a120, v164
v_accvgpr_write_b32 a121, v165
Mvaa 156,24,32
s_and_b32 s41, s41, 0xffff
Ta 8,11,49152
Ta 10,11,50176
Mvaa 156,28,48
s_mov_b32 s42, 5242880
buffer_load_dwordx4 v[188:191], v27, s[8:11], s28 offen offset:0 sc1
v_accvgpr_write_b32 a122, v166
Mvaa 124,28,44
s_mov_b32 s43, 0x20000
Ta 12,11,57344
v_accvgpr_write_b32 a123, v167
Mvav 124,24,208
s_sub_u32 s42, s42, s94
Ta 14,11,58368
v_accvgpr_write_b32 a124, v160
Mvav 92,24,236
s_mov_b64 s[44:45], s[8:9]
v_accvgpr_write_b32 a125, v161
v_accvgpr_write_b32 a126, v162
Mvaa 92,28,40
s_mov_b32 s47, 0x20000
Ta 16,12,49152
v_accvgpr_write_b32 a127, v163
Mvaa 60,28,36
s_and_b32 s48, s14, 1
Ta 18,12,50176
buffer_load_dwordx4 v[196:199], v27, s[8:11], s93 offen offset:0 sc1
Mvav 60,24,228
s_waitcnt vmcnt(13) lgkmcnt(6)
v_bfe_u32 v28, v1, 4, 2
v_lshlrev_b32 v28, 3, v28
Mvaa 32,0,52
s_lshl_b32 s80, s56, 9
Ta 20,12,57344
Ta 22,12,58368
Mvav 32,4,184
s_mul_i32 s48, s48, 5120
s_add_u32 s80, s80, 4096
s_lshr_b32 s51, s14, 1
Mvaa 64,4,128
s_and_b32 s94, s14, 1
Ta 24,13,49152
Ta 26,13,50176
Mvaa 64,0,56
s_add_u32 s28, s28, 512
s_cmp_eq_u32 s28, s4
s_cselect_b32 s93, 10240, 0
buffer_load_dwordx2 v[28:29], v28, s[8:11], s80 offen sc1
Mvaa 96,0,60
s_sub_u32 s28, s28, s93
s_add_u32 s93, s28, 20480
s_lshl_b32 s95, s57, 1
Mvaa 96,4,132
s_add_u32 s94, s94, s95
Ta 28,13,57344
Ta 30,13,58368
Mvaa 128,4,136
s_lshl_b32 s54, s94, 3
s_add_u32 s54, s54, s59
Mvav 128,0,216
W 8
Ta 0,14,49152
Ta 2,14,50176
Mvav 132,8,216
s_lshl_b32 s54, s54, 6
buffer_load_dwordx4 v[204:207], v26, s[8:11], s28 offen offset:0 sc1
Mvaa 132,12,136
s_xor_b32 s55, s54, 15872
Mvaa 100,12,132
Ta 4,14,57344
Ta 6,14,58368
Mvaa 100,8,60
s_add_u32 s54, s54, 20480
Mvaa 68,8,56
s_and_b32 s81, s14, 1
Mvaa 68,12,128
s_add_u32 s55, s55, 20480
buffer_load_dwordx4 v[212:215], v26, s[8:11], s93 offen offset:0 sc1
Mvav 36,12,184
Mvaa 36,8,52
W 8
s_and_b32 s95, s14, 2
s_mul_i32 s91, s81, 17164288
Mvaa 40,16,52
s_lshl_b32 s94, s94, 2
Ta 8,15,49152
Ta 10,15,50176
Mvav 40,20,184
s_mul_i32 s82, s56, 536576
Mvaa 72,20,128
s_add_u32 s94, s94, s95
buffer_load_dwordx4 v[220:223], v27, s[8:11], s28 offen offset:0 sc1
Ta 12,15,57344
Mvaa 72,16,56
Ta 14,15,58368
Mvaa 104,16,60
s_lshl_b32 s95, s95, 4
s_add_u32 s91, s91, s82
Mvaa 104,20,132
Mvaa 136,20,136
s_add_u32 s55, s55, s95
s_add_u32 s91, s91, 4194304
Mvav 136,16,216
W 8
s_lshl_b32 s49, s94, 17
Ta 16,24,49152
Mvav 140,24,216
Ta 18,24,50176
buffer_load_dwordx4 v[176:179], v27, s[8:11], s93 offen offset:0 sc1
Mvaa 140,28,136
s_lshl_b32 s95, s59, 14
s_lshr_b32 s82, s14, 1
Mvaa 108,28,132
Ta 20,24,57344
Ta 22,24,58368
Mvaa 108,24,60
s_add_u32 s49, s49, s95
s_lshl_b32 s83, s82, 14
Mvaa 76,24,56
s_xor_b32 s50, s49, 16252928
Mvaa 76,28,128
Mvav 44,28,184
s_add_u32 s91, s91, s83
s_add_u32 s49, s49, 39845888
Mvaa 44,24,52
W 8
Ta 24,25,49152
Ta 26,25,50176
Mvaa 48,0,52
s_add_u32 s50, s50, 39845888
s_lshl_b32 s81, s81, 14
Mvav 48,4,184
Mvaa 80,4,128
s_lshr_b32 s94, s15, 3
Ta 28,25,57344
Ta 30,25,58368
Mvaa 80,0,56
s_lshl_b32 s82, s82, 16
s_lshl_b32 s94, s94, 3
Mvaa 112,0,60
Mvaa 112,4,132
s_add_u32 s94, s94, s14
s_or_b32 s92, s81, s82
Mvaa 144,4,136
Mvav 144,0,216
W 8
s_lshl_b32 s94, s94, 4
Ta 0,2,32768
Mvav 148,8,216
s_xor_b32 s92, s92, 0x10000
s_add_u32 s94, s94, s59
Ta 2,2,33792
Mvaa 148,12,136
Mvaa 116,12,132
s_mul_i32 s52, s94, 5120
Ta 4,2,40960
Ta 6,2,41984
Mvaa 116,8,60
Mvaa 84,8,56
s_lshl_b32 s95, s59, 12
Mvaa 84,12,128
Mvav 52,12,184
s_sub_u32 s52, s52, s95
Mvaa 52,8,52
W 8
s_add_u32 s52, s52, 56623104
Ta 8,3,32768
Mvaa 56,16,52
Ta 10,3,33792
Mvav 56,20,184
s_lshl_b32 s53, s94, 3
Mvaa 88,20,128
Ta 12,3,40960
Ta 14,3,41984
Mvaa 88,16,56
s_add_u32 s53, s53, 65536
Mvaa 120,16,60
s_waitcnt vmcnt(4)
v_cmp_eq_u64 vcc, s[12:13], v[28:29]
s_cmp_eq_u64 vcc, -1
s_cbranch_scc0 .Lspin_57%=
.Lok_58%=:
s_sub_u32 s82, s91, 16384
Mvaa 120,20,132
s_xor_b32 s83, s92, 0x10000
s_and_b32 s5, s14, 1
s_lshl_b32 s5, s5, 13
Mvaa 152,20,136
s_add_u32 s5, s82, s5
s_add_u32 m0, s83, 0
s_add_u32 s81, s5, 0
Mvav 152,16,216
W 8
v_bfe_u32 v244, v1, 4, 1
v_mul_u32_u24 v244, 0xaaaa, v244
Mvav 156,24,216
Dc 1,8,81,0
v_add_u32 v244, 0x4444, v244
Mvaa 156,28,136
Ta 16,4,32768
Ta 18,4,33792
Mvaa 124,28,132
v_lshl_or_b32 v240, v244, 16, v244
v_mov_b32 v241, 0xff800000
Mvaa 124,24,60
Ta 20,4,40960
Ta 22,4,41984
Mvaa 92,24,56
v_add_u32 v242, 0x10000, v1
Mvaa 92,28,128
Dc 1,8,81,1024
v_bfe_u32 v244, v1, 4, 4
Mvav 60,28,184
v_lshrrev_b32 v245, 8, v1
Mvaa 60,24,52
W 8
Ta 24,5,32768
v_lshlrev_b32 v245, 2, v245
Mvaa 32,0,140
Ta 26,5,33792
Mvav 32,4,224
v_sub_u32 v244, v244, v245
Mvav 64,4,232
Dc 1,8,81,2048
v_cmp_le_i32_e64 s[72:73], 0, v244
Mvaa 64,0,144
Ta 28,5,40960
Ta 30,5,41984
Mvaa 96,0,148
v_cmp_le_i32_e64 s[74:75], 1, v244
Mvaa 96,4,248
v_cmp_le_i32_e64 s[76:77], 2, v244
Mvaa 128,4,252
v_cmp_le_i32_e64 s[78:79], 3, v244
Mvaa 128,0,152
W 8
Dc 1,8,81,3072
v_add_u32 v243, 4096, v1
Mvaa 132,8,152
s_add_u32 m0, s83, 4096
s_add_u32 s81, s5, 4096
Ta 0,6,32768
Mvaa 132,12,252
Ta 2,6,33792
v_add_u32 v244, 8192, v1
Mvaa 100,12,248
Ta 4,6,40960
v_add_u32 v245, 12288, v1
Mvaa 100,8,148
Ta 6,6,41984
Mvaa 68,8,144
s_mov_b32 s18, 0x3f803f80
Dc 1,8,81,0
Mvav 68,12,232
s_mov_b32 s19, 0x3f003f00
Mvav 36,12,224
v_accvgpr_write_b32 a176, s18
Mvaa 36,8,140
W 8
Ta 8,7,32768
Ta 10,7,33792
Mvaa 40,16,140
v_accvgpr_write_b32 a180, s19
Mvav 40,20,224
Dc 1,8,81,1024
v_mov_b32 v252, v241
Mvav 72,20,232
Ta 12,7,40960
v_accvgpr_write_b32 a177, s18
Mvaa 72,16,144
Ta 14,7,41984
Mvaa 104,16,148
v_accvgpr_write_b32 a181, s19
Mvaa 104,20,248
v_mov_b32 v253, v241
Mvaa 136,20,252
Dc 1,8,81,2048
v_accvgpr_write_b32 a178, s18
Mvaa 136,16,152
W 8
Ta 16,8,32768
Ta 18,8,33792
Mvaa 140,24,152
v_accvgpr_write_b32 a182, s19
Mvaa 140,28,252
v_mov_b32 v254, v241
Mvaa 108,28,248
Ta 20,8,40960
v_accvgpr_write_b32 a179, s18
Mvaa 108,24,148
Dc 1,8,81,3072
Ta 22,8,41984
Mvaa 76,24,144
s_add_u32 m0, s83, 8192
s_add_u32 s81, s82, 8192
v_accvgpr_write_b32 a183, s19
Mvav 76,28,232
v_mov_b32 v255, v241
Mvav 44,28,224
Mvaa 44,24,140
W 8
Ta 24,9,32768
v_accvgpr_write_b32 a160, 0
Mvaa 48,0,140
Dc 1,8,81,0
Ta 26,9,33792
Mvav 48,4,224
v_accvgpr_write_b32 a161, 0
v_accvgpr_write_b32 a162, 0
Mvav 80,4,232
Ta 28,9,40960
Ta 30,9,41984
Mvaa 80,0,144
v_accvgpr_write_b32 a163, 0
Mvaa 112,0,148
v_accvgpr_write_b32 a164, 0
Mvaa 112,4,248
Dc 1,8,81,1024
v_accvgpr_write_b32 a165, 0
Mvaa 144,4,252
Mvaa 144,0,152
W 8
Ta 0,2,49152
v_accvgpr_write_b32 a166, 0
Mvaa 148,8,152
Ta 2,2,50176
v_accvgpr_write_b32 a167, 0
Mvaa 148,12,252
v_accvgpr_write_b32 a168, 0
Mvaa 116,12,248
Dc 1,8,81,2048
Ta 4,2,57344
Mvaa 116,8,148
Ta 6,2,58368
v_accvgpr_write_b32 a169, 0
Mvaa 84,8,144
v_accvgpr_write_b32 a170, 0
Mvav 84,12,232
v_accvgpr_write_b32 a171, 0
Mvav 52,12,224
Mvaa 52,8,140
W 8
Dc 1,8,81,3072
v_accvgpr_write_b32 a172, 0
Mvaa 56,16,140
s_add_u32 m0, s83, 12288
s_add_u32 s81, s82, 12288
Ta 8,3,49152
Mvav 56,20,224
Ta 10,3,50176
v_accvgpr_write_b32 a173, 0
Mvav 88,20,232
Ta 12,3,57344
v_accvgpr_write_b32 a174, 0
Mvaa 88,16,144
Ta 14,3,58368
v_accvgpr_write_b32 a175, 0
Mvaa 120,16,148
s_mov_b32 s90, 0x5d800000
Dc 1,8,81,0
Mvaa 120,20,248
Mvaa 152,20,252
s_mov_b64 s[88:89], 0
Mvaa 152,16,152
W 8
s_mov_b32 s64, 0
Ta 16,4,49152
Mvaa 156,24,152
s_and_b32 s18, s14, 1
Ta 18,4,50176
Mvaa 156,28,252
Dc 1,8,81,1024
Mvaa 124,28,248
s_lshl_b32 s62, s18, 14
Ta 20,4,57344
Ta 22,4,58368
Mvaa 124,24,148
s_lshl_b32 s20, s57, 15
Mvaa 92,24,144
s_sub_u32 s20, 507904, s20
Mvav 92,28,232
Mvav 60,28,224
)"
R"(s_add_u32 s20, s20, s62
Dc 1,8,81,2048
Mvaa 60,24,140
s_waitcnt vmcnt(15) lgkmcnt(8)
s_lshl_b32 s65, s57, 1
Ta 24,5,49152
Mvav 32,0,192
s_sub_u32 s65, s65, 1
Ta 26,5,50176
Mvav 32,4,204
Mvav 64,4,212
s_max_i32 s65, s65, 0
Ta 28,5,57344
Ta 30,5,58368
Mvav 64,0,200
s_sub_u32 s66, s60, 1
Dc 1,8,81,3072
v_bfe_u32 v28, v1, 4, 4
Mvav 96,0,188
v_lshlrev_b32 v28, 3, v28
Mvav 96,4,220
s_lshl_b32 s80, s56, 9
s_cmp_lt_u32 s14, 2
s_cselect_b32 s20, s20, 0
s_cselect_b32 s66, s66, 15
s_cselect_b32 s21, 32768, 0
s_add_u32 s80, s80, 4096
Mvav 128,4,176
s_add_u32 s61, s91, s20
Mvav 128,0,196
W 8
s_add_u32 s62, s62, s21
Ta 0,6,49152
Mvav 132,8,196
buffer_load_dwordx2 v[28:29], v28, s[8:11], s80 offen sc1
Ta 2,6,50176
Mvav 132,12,176
s_sub_u32 s63, 16384, s21
Mvav 100,12,220
s_lshl_b32 s67, s60, 7
Ta 4,6,57344
Ta 6,6,58368
Mvav 100,8,188
s_add_u32 s67, s67, s56
Mvav 68,8,200
Mvav 68,12,212
s_lshl_b32 s67, s67, 12
Mvav 36,12,204
s_add_u32 s67, s67, 39845888
Mvav 36,8,192
W 8
s_lshl_b32 s69, s60, 3
Ta 8,7,49152
Mvav 40,16,192
Ta 10,7,50176
Mvav 40,20,204
s_add_u32 s69, s69, s59
Mvav 72,20,212
s_lshl_b32 s69, s69, 6
Ta 12,7,57344
Ta 14,7,58368
Mvav 72,16,200
s_lshl_b32 s21, s58, 3
Mvav 104,16,188
Mvav 104,20,220
s_add_u32 s69, s69, s21
Mvav 136,20,176
s_add_u32 s69, s69, 20480
Mvav 136,16,196
W 8
Ta 16,8,49152
Ta 18,8,50176
Mvav 140,24,196
s_mov_b32 s70, 0
Mvav 140,28,176
s_mov_b32 s37, 0
Mvav 108,28,220
s_sub_u32 s18, s57, 4
Ta 20,8,57344
Ta 22,8,58368
Mvav 108,24,188
Mvav 76,24,200
s_lshl_b32 s18, s18, 2
Mvav 76,28,212
s_add_u32 s18, s18, s58
Mvav 44,28,204
s_mov_b32 s85, s59
Mvav 44,24,192
W 8
Ta 24,9,49152
Ta 26,9,50176
Mvav 48,0,192
s_mul_i32 s85, s85, 7
Mvav 48,4,204
s_add_u32 s85, s85, s18
Mvav 80,4,212
s_cmp_lt_u32 s18, 7
s_cselect_b32 s85, s85, 99
Ta 28,9,57344
Ta 30,9,58368
Mvav 80,0,200
Mvav 112,0,188
s_cmp_lt_u32 s85, 54
s_cselect_b32 s65, 2, s65
Mvav 112,4,220
s_mov_b32 s68, 0
Mvav 144,4,176
s_mov_b64 s[24:25], -1
Mvav 144,0,196
W 8
Mvav 148,8,196
s_mov_b32 s22, 0x3e0293ee
Mvav 148,12,176
s_mov_b32 s23, 0x3e0293ee
Mvav 116,12,220
Mvav 116,8,188
Mvav 84,8,200
Mvav 84,12,212
Mvav 52,12,204
Mvav 52,8,192
W 4
Mvav 56,16,192
Mvav 56,20,204
s_waitcnt vmcnt(0)
v_cmp_eq_u64 vcc, s[12:13], v[28:29]
s_cmp_eq_u64 vcc, -1
s_cbranch_scc0 .Lspin_60%=
.Lok_61%=:
Mvav 88,20,212
Mvav 88,16,200
Mvav 120,16,188
W 0
s_barrier
Mvav 120,20,220
Ra 128,242,0
Mvav 152,20,176
Ra 132,242,1024
Mvav 152,16,196
Mvav 156,24,196
Ra 136,242,2048
Mvav 156,28,176
Mvav 124,28,220
Ra 140,242,3072
Mvav 124,24,188
Mvav 92,24,200
Mvav 92,28,212
Mvav 60,28,204
Mvav 60,24,192
.Lbgo%=:
v_pk_mul_f32 v[32:33], v[32:33], s[22:23]
v_pk_mul_f32 v[34:35], v[34:35], s[22:23]
v_pk_mul_f32 v[36:37], v[36:37], s[22:23]
v_pk_mul_f32 v[38:39], v[38:39], s[22:23]
v_pk_mul_f32 v[64:65], v[64:65], s[22:23]
v_pk_mul_f32 v[66:67], v[66:67], s[22:23]
v_pk_mul_f32 v[68:69], v[68:69], s[22:23]
v_pk_mul_f32 v[70:71], v[70:71], s[22:23]
v_pk_mul_f32 v[96:97], v[96:97], s[22:23]
v_pk_mul_f32 v[98:99], v[98:99], s[22:23]
v_pk_mul_f32 v[100:101], v[100:101], s[22:23]
v_pk_mul_f32 v[102:103], v[102:103], s[22:23]
v_pk_mul_f32 v[128:129], v[128:129], s[22:23]
v_pk_mul_f32 v[130:131], v[130:131], s[22:23]
v_pk_mul_f32 v[132:133], v[132:133], s[22:23]
v_pk_mul_f32 v[134:135], v[134:135], s[22:23]
v_pk_mul_f32 v[40:41], v[40:41], s[22:23]
v_pk_mul_f32 v[42:43], v[42:43], s[22:23]
v_pk_mul_f32 v[44:45], v[44:45], s[22:23]
v_pk_mul_f32 v[46:47], v[46:47], s[22:23]
v_pk_mul_f32 v[72:73], v[72:73], s[22:23]
v_pk_mul_f32 v[74:75], v[74:75], s[22:23]
v_pk_mul_f32 v[76:77], v[76:77], s[22:23]
v_pk_mul_f32 v[78:79], v[78:79], s[22:23]
v_pk_mul_f32 v[104:105], v[104:105], s[22:23]
v_pk_mul_f32 v[106:107], v[106:107], s[22:23]
v_pk_mul_f32 v[108:109], v[108:109], s[22:23]
v_pk_mul_f32 v[110:111], v[110:111], s[22:23]
v_pk_mul_f32 v[136:137], v[136:137], s[22:23]
v_pk_mul_f32 v[138:139], v[138:139], s[22:23]
v_pk_mul_f32 v[140:141], v[140:141], s[22:23]
v_pk_mul_f32 v[142:143], v[142:143], s[22:23]
v_pk_mul_f32 v[48:49], v[48:49], s[22:23]
v_pk_mul_f32 v[50:51], v[50:51], s[22:23]
v_pk_mul_f32 v[52:53], v[52:53], s[22:23]
v_pk_mul_f32 v[54:55], v[54:55], s[22:23]
v_pk_mul_f32 v[80:81], v[80:81], s[22:23]
v_pk_mul_f32 v[82:83], v[82:83], s[22:23]
v_pk_mul_f32 v[84:85], v[84:85], s[22:23]
v_pk_mul_f32 v[86:87], v[86:87], s[22:23]
v_pk_mul_f32 v[112:113], v[112:113], s[22:23]
v_pk_mul_f32 v[114:115], v[114:115], s[22:23]
v_pk_mul_f32 v[116:117], v[116:117], s[22:23]
v_pk_mul_f32 v[118:119], v[118:119], s[22:23]
v_pk_mul_f32 v[144:145], v[144:145], s[22:23]
v_pk_mul_f32 v[146:147], v[146:147], s[22:23]
v_pk_mul_f32 v[148:149], v[148:149], s[22:23]
v_pk_mul_f32 v[150:151], v[150:151], s[22:23]
v_pk_mul_f32 v[56:57], v[56:57], s[22:23]
v_pk_mul_f32 v[58:59], v[58:59], s[22:23]
v_pk_mul_f32 v[60:61], v[60:61], s[22:23]
v_pk_mul_f32 v[62:63], v[62:63], s[22:23]
v_pk_mul_f32 v[88:89], v[88:89], s[22:23]
v_pk_mul_f32 v[90:91], v[90:91], s[22:23]
v_pk_mul_f32 v[92:93], v[92:93], s[22:23]
v_pk_mul_f32 v[94:95], v[94:95], s[22:23]
v_pk_mul_f32 v[120:121], v[120:121], s[22:23]
v_pk_mul_f32 v[122:123], v[122:123], s[22:23]
v_pk_mul_f32 v[124:125], v[124:125], s[22:23]
v_pk_mul_f32 v[126:127], v[126:127], s[22:23]
v_pk_mul_f32 v[152:153], v[152:153], s[22:23]
v_pk_mul_f32 v[154:155], v[154:155], s[22:23]
v_pk_mul_f32 v[156:157], v[156:157], s[22:23]
v_pk_mul_f32 v[158:159], v[158:159], s[22:23]
K 32,32,33
K 33,34,35
K 34,36,37
K 35,38,39
K 40,40,41
K 41,42,43
K 42,44,45
K 43,46,47
W 0
Zvav 196,128,32
s_add_u32 m0, s62, 0
Ra 144,242,4096
Ra 148,242,5120
Ra 152,242,6144
Ra 156,242,7168
Mvav 196,132,40
K 64,64,65
K 65,66,67
K 66,68,69
K 67,70,71
K 72,72,73
K 73,74,75
K 74,76,77
K 75,78,79
s_nop 1
Zvav 204,132,72
Dc 1,8,61,0
K 96,96,97
K 97,98,99
K 98,100,101
Mvav 204,128,64
K 99,102,103
K 104,104,105
K 105,106,107
K 106,108,109
K 107,110,111
Zvav 212,128,96
Dc 1,8,61,1024
K 128,128,129
K 129,130,131
K 130,132,133
Mvav 212,132,104
K 131,134,135
K 136,136,137
K 137,138,139
K 138,140,141
K 139,142,143
s_nop 1
Zvav 220,132,136
Dc 1,8,61,2048
K 48,48,49
K 49,50,51
K 50,52,53
Mvav 220,128,128
K 51,54,55
K 56,56,57
K 57,58,59
K 58,60,61
K 59,62,63
Mvav 196,136,48
Ra 128,242,8192
Ra 132,242,9216
K 80,80,81
K 81,82,83
Mvav 196,140,56
K 82,84,85
K 83,86,87
K 88,88,89
K 89,90,91
K 90,92,93
K 91,94,95
s_nop 1
Mvav 204,140,88
Dc 1,8,61,3072
s_add_u32 m0, s62, 4096
K 112,112,113
K 113,114,115
K 114,116,117
Mvav 204,136,80
K 115,118,119
K 120,120,121
K 121,122,123
K 122,124,125
K 123,126,127
Mvav 212,136,112
Dc 243,8,61,0
K 144,144,145
K 145,146,147
K 146,148,149
Mvav 212,140,120
K 147,150,151
K 152,152,153
K 153,154,155
K 154,156,157
K 155,158,159
s_nop 1
Mvav 220,140,152
s_add_u32 s80, s62, 131072
Dc 243,8,61,1024
v_add_u32 v12, s80, v1
Mvav 220,136,144
W 2
ds_write_b128 v12, v[32:35] offset:0
Ra 136,242,10240
Zvav 36,144,32
Ra 0,12,0
Ra 140,242,11264
Dc 243,8,61,2048
Mvav 36,148,40
v_max3_f32 v4, v196, v197, v198
v_max3_f32 v4, v4, v199, v252
v_max3_f32 v5, v204, v205, v206
v_max3_f32 v5, v5, v207, v253
Zvav 68,148,72
v_max3_f32 v6, v212, v213, v214
v_max3_f32 v6, v6, v215, v254
v_max3_f32 v7, v220, v221, v222
v_max3_f32 v7, v7, v223, v255
Mvav 68,144,64
v_mov_b32 v8, v4
v_mov_b32 v9, v5
v_mov_b32 v10, v6
v_mov_b32 v11, v7
Zvav 100,144,96
ds_write_b128 v12, v[40:43] offset:1024
v_permlane32_swap_b32 v4, v8
v_permlane32_swap_b32 v5, v9
v_permlane32_swap_b32 v6, v10
Mvav 100,148,104
v_permlane32_swap_b32 v7, v11
v_max_f32 v4, v4, v8
v_max_f32 v5, v5, v9
v_max_f32 v6, v6, v10
Zvav 132,148,136
v_max_f32 v7, v7, v11
v_mov_b32 v8, v4
v_mov_b32 v9, v5
v_mov_b32 v10, v6
Mvav 132,144,128
Ra 4,12,1024
v_mov_b32 v11, v7
v_permlane16_swap_b32 v4, v8
v_permlane16_swap_b32 v5, v9
Mvav 36,152,48
v_permlane16_swap_b32 v6, v10
v_permlane16_swap_b32 v7, v11
v_max_f32 v4, v4, v8
v_max_f32 v5, v5, v9
Mvav 36,156,56
v_max_f32 v6, v6, v10
v_max_f32 v7, v7, v11
v_sub_f32 v4, 0, v4
v_sub_f32 v5, 0, v5
Mvav 68,156,88
v_sub_f32 v6, 0, v6
v_sub_f32 v7, 0, v7
v_mov_b32 v224, v4
v_mov_b32 v225, v4
Mvav 68,152,80
v_mov_b32 v226, v4
v_mov_b32 v227, v4
v_mov_b32 v228, v5
v_mov_b32 v229, v5
Mvav 100,152,112
v_mov_b32 v230, v5
v_mov_b32 v231, v5
v_mov_b32 v232, v6
v_mov_b32 v233, v6
Mvav 100,156,120
v_mov_b32 v234, v6
v_mov_b32 v235, v6
v_mov_b32 v236, v7
v_mov_b32 v237, v7
Mvav 132,156,152
Dc 243,8,61,3072
s_add_u32 m0, s62, 8192
ds_write_b128 v12, v[48:51] offset:2048
v_mov_b32 v238, v7
v_mov_b32 v239, v7
Mvav 132,152,144
W 3
Ra 8,12,2048
ds_write_b128 v12, v[56:59] offset:3072
Ra 144,242,12288
Ra 12,12,3072
Cvav 160,128,32,224
s_and_b32 s80, s14, 1
)"
R"(Dc 244,8,61,0
Ra 148,242,13312
ds_write_b128 v12, v[64:67] offset:4096
Ra 16,12,4096
Mvav 160,132,40
s_lshl_b32 s80, s80, 14
ds_write_b128 v12, v[72:75] offset:5120
Ra 20,12,5120
ds_write_b128 v12, v[80:83] offset:6144
A 196,196,224
Cvav 164,132,72,228
s_add_u32 s80, s80, 98304
Dc 244,8,61,1024
Ra 24,12,6144
A 197,197,224
A 198,198,224
Mvav 164,128,64
Ra 152,242,14336
A 199,199,224
X 196
Cvav 168,128,96,232
Dc 244,8,61,2048
Ra 156,242,15360
X 197
Mvav 168,132,104
ds_write_b128 v12, v[88:91] offset:7168
Ra 28,12,7168
X 198
Cvav 172,132,136,236
Dc 244,8,61,3072
s_add_u32 m0, s62, 12288
K 192,196,197
X 199
Mvav 172,128,128
ds_write_b128 v12, v[96:99] offset:8192
A 204,204,228
v_add_u32 v8, s80, v1
K 193,198,199
Mvav 160,136,48
Dc 245,8,61,0
Ra 32,12,8192
A 205,205,228
A 206,206,228
Mvav 160,140,56
ds_write_b128 v8, v[4:7]
A 207,207,228
X 204
Mvav 164,140,88
Dc 245,8,61,1024
ds_write_b128 v12, v[104:107] offset:9216
X 205
Mvav 164,136,80
Ra 36,12,9216
X 206
K 200,204,205
Mvav 168,136,112
Dc 245,8,61,2048
Ra 128,242,24576
X 207
Mvav 168,140,120
ds_write_b128 v12, v[112:115] offset:10240
Ra 132,242,25600
A 212,212,232
K 201,206,207
Mvav 172,140,152
Dc 245,8,61,3072
s_add_u32 s61, s61, s63
Ra 40,12,10240
A 213,213,232
A 214,214,232
Mvav 172,136,144
W 11
ds_write_b128 v12, v[120:123] offset:11264
A 215,215,232
X 212
Cvav 176,144,32,224
X 213
X 214
Mvav 176,148,40
Ra 44,12,11264
K 208,212,213
X 215
Cvav 180,148,72,228
ds_write_b128 v12, v[128:131] offset:12288
A 220,220,236
K 209,214,215
A 221,221,236
Mvav 180,144,64
Ra 48,12,12288
Ra 136,242,26624
A 222,222,236
A 223,223,236
Cvav 184,144,96,232
ds_write_b128 v12, v[136:139] offset:13312
Ra 140,242,27648
X 220
Mvav 184,148,104
X 221
X 222
Cvav 188,148,136,236
Ra 52,12,13312
K 216,220,221
X 223
Mvav 188,144,128
ds_write_b128 v12, v[144:147] offset:14336
Ra 56,12,14336
A 36,36,224
K 217,222,223
Mvav 176,152,48
ds_write_b128 v12, v[152:155] offset:15360
A 37,37,224
A 38,38,224
A 39,39,224
Mvav 176,156,56
X 36
X 37
Mvav 180,156,88
Ra 60,12,15360
X 38
K 194,36,37
Mvav 180,152,80
Ra 144,242,28672
X 39
A 68,68,228
Mvav 184,152,112
Ra 148,242,29696
K 195,38,39
A 69,69,228
A 70,70,228
Mvav 184,156,120
W 3
s_barrier
A 71,71,228
X 68
Mvav 188,156,152
X 69
X 70
K 202,68,69
X 71
A 100,100,232
K 203,70,71
A 101,101,232
A 102,102,232
A 103,103,232
X 100
X 101
X 102
K 210,100,101
X 103
A 132,132,236
K 211,102,103
A 133,133,236
A 134,134,236
A 135,135,236
X 132
X 133
X 134
K 218,132,133
X 135
s_nop 0
K 219,134,135
Mvav 188,152,144
Ra 152,242,30720
Zvav 32,128,192
Ra 156,242,31744
Zvav 64,128,200
v_xor_b32 v242, 0x10000, v242
Zvav 96,128,208
s_xor_b32 s62, s62, 0x10000
Zvav 128,128,216
v_add_u32 v23, 61440, v21
Zvav 132,132,216
v_add_u32 v24, 61440, v22
Zvav 100,132,208
v_add_u32 v25, 122880, v21
Zvav 68,132,200
v_add_u32 v26, 122880, v22
Zvav 36,132,192
Zvav 40,136,192
X 172
Zvav 72,136,200
X 173
Zvav 104,136,208
X 174
K 220,172,173
Zvav 136,136,216
X 175
Zvav 140,140,216
X 168
K 221,174,175
Zvav 108,140,208
X 169
X 170
Zvav 76,140,200
s_waitcnt vmcnt(0) lgkmcnt(0)
s_barrier
K 212,168,169
X 171
Zvav 44,140,192
Ra 128,242,0
X 164
Zvav 48,144,192
K 213,170,171
X 165
Zvav 80,144,200
X 166
Zvav 112,144,208
Ra 132,242,1024
K 204,164,165
X 167
Zvav 144,144,216
X 160
Zvav 148,148,216
K 205,166,167
X 161
Zvav 116,148,208
Ra 136,242,2048
X 162
Zvav 84,148,200
K 196,160,161
X 163
Zvav 52,148,192
X 176
K 197,162,163
Zvav 56,152,192
Ra 140,242,3072
X 177
Zvav 88,152,200
X 178
K 198,176,177
Zvav 120,152,208
X 179
X 180
Zvav 152,152,216
K 199,178,179
Zvav 156,156,216
X 181
X 182
Zvav 124,156,208
K 206,180,181
X 183
Zvav 92,156,200
K 207,182,183
Zvav 60,156,192
s_cmp_eq_u64 s[24:25], 0
s_cbranch_scc1 .Ll1_30%=
W 0
Cvaa 160,128,0,224
Ra 144,242,4096
Ra 148,242,5120
Mvaa 160,132,4
s_add_u32 m0, s62, 0
Ra 152,242,6144
Cvaa 164,132,20,228
X 184
Mvaa 164,128,16
X 185
Cvaa 168,128,32,232
X 186
Mvaa 168,132,36
Ra 156,242,7168
K 214,184,185
Cvaa 172,132,52,236
Dc 1,8,61,0
Mvaa 172,128,48
X 187
Mvaa 160,136,8
X 188
Mvaa 160,140,12
Ra 128,242,16384
K 215,186,187
Mvaa 164,140,28
Dc 1,8,61,1024
Mvaa 164,136,24
X 189
Mvaa 168,136,40
X 190
Mvaa 168,140,44
Ra 132,242,17408
K 222,188,189
Mvaa 172,140,60
Dc 1,8,61,2048
Mvaa 172,136,56
X 191
v_smfmac_f32_16x16x64_bf16 a[160:163], a[176:179], v[192:199], v240
Ra 136,242,18432
K 223,190,191
v_smfmac_f32_16x16x64_bf16 a[164:167], a[176:179], v[200:207], v240
X 160
v_smfmac_f32_16x16x64_bf16 a[168:171], a[176:179], v[208:215], v240
Dc 1,8,61,3072
v_smfmac_f32_16x16x64_bf16 a[172:175], a[176:179], v[216:223], v240
W 3
X 161
Cvaa 176,144,0,224
s_add_u32 m0, s62, 4096
X 162
Mvaa 176,148,4
Ra 140,242,19456
K 192,160,161
Cvaa 180,148,20,228
X 163
Mvaa 180,144,16
Dc 243,8,61,0
Cvaa 184,144,32,232
X 164
Mvaa 184,148,36
K 193,162,163
Cvaa 188,148,52,236
X 165
Mvaa 188,144,48
Dc 243,8,61,1024
Ra 144,242,20480
Mvaa 176,152,8
X 166
Mvaa 176,156,12
Ra 148,242,21504
K 200,164,165
Mvaa 180,156,28
X 167
Mvaa 180,152,24
Dc 243,8,61,2048
Mvaa 184,152,40
X 168
Mvaa 184,156,44
K 201,166,167
Mvaa 188,156,60
X 169
Mvaa 188,152,56
W 2
Dc 243,8,61,3072
Ra 152,242,22528
Mvav 32,128,196
s_add_u32 m0, s62, 8192
X 170
Mvav 64,128,204
Ra 156,242,23552
K 208,168,169
Mvav 96,128,212
X 171
Mvav 128,128,220
Dc 244,8,61,0
Mvav 132,132,220
X 172
Mvav 100,132,212
Ra 128,242,8192
K 209,170,171
Mvav 68,132,204
X 173
Mvav 36,132,196
Dc 244,8,61,1024
Mvav 40,136,196
X 174
Mvav 72,136,204
Ra 132,242,9216
K 216,172,173
Mvav 104,136,212
X 175
Mvav 136,136,220
Dc 244,8,61,2048
Mvav 140,140,220
X 176
Mvav 108,140,212
Ra 136,242,10240
K 217,174,175
Mvav 76,140,204
X 177
Mvav 44,140,196
W 3
Dc 244,8,61,3072
Mvav 48,144,196
X 178
Mvav 80,144,204
s_add_u32 m0, s62, 12288
Ra 140,242,11264
Mvav 112,144,212
K 194,176,177
Mvav 144,144,220
X 179
Mvav 148,148,220
X 180
Mvav 116,148,212
Dc 245,8,61,0
Ra 144,242,12288
Mvav 84,148,204
K 195,178,179
Mvav 52,148,196
X 181
Mvav 56,152,196
s_cmp_eq_u32 s14, 2
s_cselect_b64 s[18:19], s[30:31], s[32:33]
X 182
Mvav 88,152,204
s_mul_i32 s81, s58, 6
Dc 245,8,61,1024
Ra 148,242,13312
Mvav 120,152,212
s_add_u32 s81, s81, s57
K 202,180,181
Mvav 152,152,220
)"
R"(s_sub_u32 s81, s81, 2
X 183
Mvav 156,156,220
s_cmp_ge_u32 s57, 2
s_cselect_b32 s23, 2211836, 0
X 184
Mvav 124,156,212
s_mul_i32 s82, s81, 48384
Dc 245,8,61,2048
Ra 152,242,14336
Mvav 92,156,204
s_mov_b32 s83, s59
K 203,182,183
Mvav 60,156,196
W 3
X 185
Cvaa 160,128,0,224
s_mul_i32 s83, s83, 448
X 186
Mvaa 160,132,4
s_andn2_b32 s83, s83, 127
Dc 245,8,61,3072
Ra 156,242,15360
Cvaa 164,132,20,228
s_add_u32 s82, s82, s83
s_add_u32 s61, s61, s63
K 210,184,185
Mvaa 164,128,16
v_min_u32 v246, 880, v1
v_lshrrev_b32 v247, 6, v246
Cvaa 168,128,32,232
X 187
Mvaa 168,132,36
X 188
Cvaa 172,132,52,236
s_xor_b32 s62, s62, 0x10000
v_mul_u32_u24 v247, 3456, v247
v_and_b32 v246, 48, v246
Mvaa 172,128,48
K 211,186,187
v_lshl_add_u32 v246, v246, 3, v247
Mvaa 160,136,8
X 189
Mvaa 160,140,12
s_add_u32 s64, s64, 1
Ra 128,242,24576
v_add_u32 v246, s82, v246
Mvaa 164,140,28
X 190
Mvaa 164,136,24
Ra 132,242,25600
v_min_u32 v246, s23, v246
Mvaa 168,136,40
global_load_dword v3, v246, s[18:19]
K 218,188,189
Mvaa 168,140,44
X 191
Mvaa 172,140,60
X 160
Mvaa 172,136,56
W 2
Ra 136,242,26624
K 219,190,191
Cvaa 176,144,0,224
X 161
Mvaa 176,148,4
Ra 140,242,27648
Cvaa 180,148,20,228
X 162
Mvaa 180,144,16
K 196,160,161
Cvaa 184,144,32,232
X 163
Mvaa 184,148,36
X 164
Cvaa 188,148,52,236
K 197,162,163
Mvaa 188,144,48
X 165
Mvaa 176,152,8
Ra 144,242,28672
Ra 148,242,29696
Mvaa 176,156,12
X 166
Mvaa 180,156,28
K 204,164,165
Mvaa 180,152,24
X 167
Mvaa 184,152,40
Mvaa 184,156,44
X 168
Mvaa 188,156,60
Mvaa 188,152,56
W 2
Ra 152,242,30720
K 205,166,167
Mvav 32,128,192
X 169
Mvav 64,128,200
Ra 156,242,31744
v_xor_b32 v242, 0x10000, v242
Mvav 96,128,208
X 170
Mvav 128,128,216
K 212,168,169
Mvav 132,132,216
X 171
Mvav 100,132,208
X 172
Mvav 68,132,200
Mvav 36,132,192
K 213,170,171
Mvav 40,136,192
X 173
Mvav 72,136,200
X 174
Mvav 104,136,208
Mvav 136,136,216
K 220,172,173
Mvav 140,140,216
X 175
Mvav 108,140,208
X 176
Mvav 76,140,200
s_waitcnt vmcnt(1) lgkmcnt(0)
s_barrier
Mvav 44,140,192
Ra 128,242,0
K 221,174,175
Mvav 48,144,192
X 177
Mvav 80,144,200
X 178
Mvav 112,144,208
Ra 132,242,1024
Mvav 144,144,216
K 198,176,177
Mvav 148,148,216
X 179
Mvav 116,148,208
X 180
Mvav 84,148,200
Ra 136,242,2048
Mvav 52,148,192
K 199,178,179
Mvav 56,152,192
X 181
Mvav 88,152,200
Ra 140,242,3072
Mvav 120,152,208
X 182
Mvav 152,152,216
K 206,180,181
Mvav 156,156,216
X 183
Mvav 124,156,208
Mvav 92,156,200
K 207,182,183
Mvav 60,156,192
W 0
Cvaa 160,128,0,224
Ra 144,242,4096
Ra 148,242,5120
Mvaa 160,132,4
s_add_u32 m0, s62, 0
Ra 152,242,6144
Cvaa 164,132,20,228
X 184
Mvaa 164,128,16
X 185
Cvaa 168,128,32,232
X 186
Mvaa 168,132,36
Ra 156,242,7168
K 214,184,185
Cvaa 172,132,52,236
Dc 1,8,61,0
Mvaa 172,128,48
X 187
Mvaa 160,136,8
X 188
Mvaa 160,140,12
Ra 128,242,16384
K 215,186,187
Mvaa 164,140,28
Dc 1,8,61,1024
Mvaa 164,136,24
X 189
Mvaa 168,136,40
X 190
Mvaa 168,140,44
Ra 132,242,17408
K 222,188,189
Mvaa 172,140,60
Dc 1,8,61,2048
Mvaa 172,136,56
X 191
v_smfmac_f32_16x16x64_bf16 a[160:163], a[176:179], v[192:199], v240
Ra 136,242,18432
K 223,190,191
v_smfmac_f32_16x16x64_bf16 a[164:167], a[176:179], v[200:207], v240
X 160
v_smfmac_f32_16x16x64_bf16 a[168:171], a[176:179], v[208:215], v240
Dc 1,8,61,3072
v_smfmac_f32_16x16x64_bf16 a[172:175], a[176:179], v[216:223], v240
W 3
X 161
Cvaa 176,144,0,224
s_add_u32 m0, s62, 4096
X 162
Mvaa 176,148,4
Ra 140,242,19456
K 192,160,161
Cvaa 180,148,20,228
X 163
Mvaa 180,144,16
Dc 243,8,61,0
Cvaa 184,144,32,232
X 164
Mvaa 184,148,36
K 193,162,163
Cvaa 188,148,52,236
X 165
Mvaa 188,144,48
Dc 243,8,61,1024
Ra 144,242,20480
Mvaa 176,152,8
X 166
Mvaa 176,156,12
Ra 148,242,21504
K 200,164,165
Mvaa 180,156,28
X 167
Mvaa 180,152,24
Dc 243,8,61,2048
Mvaa 184,152,40
X 168
Mvaa 184,156,44
K 201,166,167
Mvaa 188,156,60
X 169
Mvaa 188,152,56
W 2
Dc 243,8,61,3072
Ra 152,242,22528
Mvav 32,128,196
s_add_u32 m0, s62, 8192
X 170
Mvav 64,128,204
Ra 156,242,23552
K 208,168,169
Mvav 96,128,212
X 171
Mvav 128,128,220
Dc 244,8,61,0
Mvav 132,132,220
X 172
Mvav 100,132,212
Ra 128,242,8192
K 209,170,171
Mvav 68,132,204
X 173
Mvav 36,132,196
Dc 244,8,61,1024
Mvav 40,136,196
X 174
Mvav 72,136,204
Ra 132,242,9216
K 216,172,173
Mvav 104,136,212
X 175
Mvav 136,136,220
Dc 244,8,61,2048
Mvav 140,140,220
X 176
Mvav 108,140,212
Ra 136,242,10240
K 217,174,175
Mvav 76,140,204
X 177
Mvav 44,140,196
W 3
Dc 244,8,61,3072
Mvav 48,144,196
X 178
Mvav 80,144,204
s_add_u32 m0, s62, 12288
Ra 140,242,11264
Mvav 112,144,212
K 194,176,177
Mvav 144,144,220
X 179
Mvav 148,148,220
X 180
Mvav 116,148,212
Dc 245,8,61,0
Ra 144,242,12288
Mvav 84,148,204
K 195,178,179
Mvav 52,148,196
X 181
Mvav 56,152,196
Ra 148,242,13312
v_add_u32 v246, 1161216, v246
Mvav 88,152,204
Dc 245,8,61,1024
v_min_u32 v246, s23, v246
Mvav 120,152,212
X 182
Mvav 152,152,220
K 202,180,181
Mvav 156,156,220
X 183
Mvav 124,156,212
Dc 245,8,61,2048
Ra 152,242,14336
Mvav 92,156,204
X 184
Mvav 60,156,196
W 3
K 203,182,183
v_bfe_u32 v28, v1, 4, 4
Cvaa 160,128,0,224
X 185
Mvaa 160,132,4
Dc 245,8,61,3072
v_lshlrev_b32 v28, 3, v28
Cvaa 164,132,20,228
s_lshl_b32 s80, s56, 9
X 186
Mvaa 164,128,16
s_add_u32 s80, s80, 4224
Ra 156,242,15360
K 210,184,185
Cvaa 168,128,32,232
s_add_u32 s61, s61, s63
X 187
Mvaa 168,132,36
buffer_load_dwordx2 v[28:29], v28, s[8:11], s80 offen sc1
Cvaa 172,132,52,236
s_xor_b32 s62, s62, 0x10000
X 188
Mvaa 172,128,48
Ra 128,242,24576
K 211,186,187
Mvaa 160,136,8
X 189
Mvaa 160,140,12
s_add_u32 s64, s64, 1
global_load_dword v3, v246, s[18:19]
Ra 132,242,25600
Mvaa 164,140,28
s_mov_b64 s[18:19], s[34:35]
s_mul_i32 s81, s58, 6
s_add_u32 s81, s81, s57
Mvaa 164,136,24
s_sub_u32 s81, s81, 2
s_cmp_ge_u32 s57, 2
s_cselect_b32 s23, 2211836, 0
s_lshl_b32 s81, s81, 1
Mvaa 168,136,40
s_add_u32 s81, s81, s14
s_sub_u32 s81, s81, 2
s_mul_i32 s81, s81, 6016
Mvaa 168,140,44
s_mov_b32 s82, s59
s_mul_i32 s82, s82, 286720
s_add_u32 s81, s81, s82
Mvaa 172,140,60
v_min_u32 v246, 736, v1
v_lshl_add_u32 v246, v246, 3, s81
Mvaa 172,136,56
W 2
X 190
Cvaa 176,144,0,224
v_min_u32 v246, s23, v246
K 218,188,189
Mvaa 176,148,4
global_load_dword v3, v246, s[18:19]
Ra 136,242,26624
Cvaa 180,148,20,228
X 191
Mvaa 180,144,16
X 160
Cvaa 184,144,32,232
Ra 140,242,27648
K 219,190,191
Mvaa 184,148,36
X 161
Cvaa 188,148,52,236
X 162
Mvaa 188,144,48
Ra 144,242,28672
K 196,160,161
Mvaa 176,152,8
X 163
Mvaa 176,156,12
X 164
Mvaa 180,156,28
Ra 148,242,29696
)"
R"(K 197,162,163
Mvaa 180,152,24
X 165
Mvaa 184,152,40
X 166
Mvaa 184,156,44
K 204,164,165
Mvaa 188,156,60
X 167
Mvaa 188,152,56
W 2
X 168
Mvav 32,128,192
Ra 152,242,30720
K 205,166,167
Mvav 64,128,200
X 169
Mvav 96,128,208
Ra 156,242,31744
v_xor_b32 v242, 0x10000, v242
Mvav 128,128,216
X 170
Mvav 132,132,216
K 212,168,169
Mvav 100,132,208
X 171
Mvav 68,132,200
X 172
Mvav 36,132,192
K 213,170,171
Mvav 40,136,192
X 173
Mvav 72,136,200
X 174
Mvav 104,136,208
Mvav 136,136,216
s_waitcnt vmcnt(2)
v_cmp_eq_u64 vcc, s[12:13], v[28:29]
s_cmp_eq_u64 vcc, -1
s_cbranch_scc0 .Lspin_63%=
.Lok_64%=:
K 220,172,173
Mvav 140,140,216
X 175
Mvav 108,140,208
X 176
Mvav 76,140,200
W 0
s_barrier
Mvav 44,140,192
Ra 128,242,0
K 221,174,175
Mvav 48,144,192
X 177
Mvav 80,144,200
X 178
Mvav 112,144,208
Ra 132,242,1024
Mvav 144,144,216
K 198,176,177
Mvav 148,148,216
X 179
Mvav 116,148,208
X 180
Mvav 84,148,200
Ra 136,242,2048
Mvav 52,148,192
K 199,178,179
Mvav 56,152,192
X 181
Mvav 88,152,200
Ra 140,242,3072
Mvav 120,152,208
X 182
Mvav 152,152,216
K 206,180,181
Mvav 156,156,216
X 183
Mvav 124,156,208
Mvav 92,156,200
K 207,182,183
Mvav 60,156,192
W 0
Cvaa 160,128,0,224
Ra 144,242,4096
Ra 148,242,5120
Mvaa 160,132,4
s_add_u32 m0, s62, 0
Ra 152,242,6144
Cvaa 164,132,20,228
X 184
Mvaa 164,128,16
X 185
Cvaa 168,128,32,232
X 186
Mvaa 168,132,36
Ra 156,242,7168
K 214,184,185
Cvaa 172,132,52,236
Dc 1,8,61,0
Mvaa 172,128,48
X 187
Mvaa 160,136,8
X 188
Mvaa 160,140,12
Ra 128,242,16384
K 215,186,187
Mvaa 164,140,28
Dc 1,8,61,1024
Mvaa 164,136,24
X 189
Mvaa 168,136,40
X 190
Mvaa 168,140,44
Ra 132,242,17408
K 222,188,189
Mvaa 172,140,60
Dc 1,8,61,2048
Mvaa 172,136,56
X 191
v_smfmac_f32_16x16x64_bf16 a[160:163], a[176:179], v[192:199], v240
Ra 136,242,18432
K 223,190,191
v_smfmac_f32_16x16x64_bf16 a[164:167], a[176:179], v[200:207], v240
X 160
v_smfmac_f32_16x16x64_bf16 a[168:171], a[176:179], v[208:215], v240
Dc 1,8,61,3072
v_smfmac_f32_16x16x64_bf16 a[172:175], a[176:179], v[216:223], v240
W 3
X 161
Cvaa 176,144,0,224
s_add_u32 m0, s62, 4096
X 162
Mvaa 176,148,4
Ra 140,242,19456
K 192,160,161
Cvaa 180,148,20,228
X 163
Mvaa 180,144,16
Dc 243,8,61,0
Cvaa 184,144,32,232
X 164
Mvaa 184,148,36
K 193,162,163
Cvaa 188,148,52,236
X 165
Mvaa 188,144,48
Dc 243,8,61,1024
Ra 144,242,20480
Mvaa 176,152,8
X 166
Mvaa 176,156,12
Ra 148,242,21504
K 200,164,165
Mvaa 180,156,28
X 167
Mvaa 180,152,24
Dc 243,8,61,2048
Mvaa 184,152,40
X 168
Mvaa 184,156,44
K 201,166,167
Mvaa 188,156,60
X 169
Mvaa 188,152,56
W 2
Dc 243,8,61,3072
Ra 152,242,22528
Mvav 32,128,196
s_add_u32 m0, s62, 8192
X 170
Mvav 64,128,204
Ra 156,242,23552
K 208,168,169
Mvav 96,128,212
X 171
Mvav 128,128,220
Dc 244,8,61,0
Mvav 132,132,220
X 172
Mvav 100,132,212
Ra 128,242,8192
K 209,170,171
Mvav 68,132,204
X 173
Mvav 36,132,196
Dc 244,8,61,1024
Mvav 40,136,196
X 174
Mvav 72,136,204
Ra 132,242,9216
K 216,172,173
Mvav 104,136,212
X 175
Mvav 136,136,220
Dc 244,8,61,2048
Mvav 140,140,220
X 176
Mvav 108,140,212
Ra 136,242,10240
K 217,174,175
Mvav 76,140,204
X 177
Mvav 44,140,196
W 3
Dc 244,8,61,3072
Mvav 48,144,196
X 178
Mvav 80,144,204
s_add_u32 m0, s62, 12288
Ra 140,242,11264
Mvav 112,144,212
K 194,176,177
Mvav 144,144,220
X 179
Mvav 148,148,220
X 180
Mvav 116,148,212
Dc 245,8,61,0
Ra 144,242,12288
Mvav 84,148,204
K 195,178,179
Mvav 52,148,196
X 181
Mvav 56,152,196
X 182
Mvav 88,152,204
Dc 245,8,61,1024
Ra 148,242,13312
Mvav 120,152,212
K 202,180,181
Mvav 152,152,220
X 183
Mvav 156,156,220
X 184
Mvav 124,156,212
Dc 245,8,61,2048
Ra 152,242,14336
Mvav 92,156,204
K 203,182,183
Mvav 60,156,196
W 3
X 185
Cvaa 160,128,0,224
X 186
Mvaa 160,132,4
Dc 245,8,61,3072
Ra 156,242,15360
Cvaa 164,132,20,228
s_add_u32 s61, s61, s63
K 210,184,185
Mvaa 164,128,16
X 187
Cvaa 168,128,32,232
X 188
Mvaa 168,132,36
Cvaa 172,132,52,236
s_xor_b32 s62, s62, 0x10000
K 211,186,187
Mvaa 172,128,48
X 189
Mvaa 160,136,8
Ra 128,242,24576
Ra 132,242,25600
Mvaa 160,140,12
s_add_u32 s64, s64, 1
X 190
Mvaa 164,140,28
K 218,188,189
Mvaa 164,136,24
X 191
Mvaa 168,136,40
Mvaa 168,140,44
X 160
Mvaa 172,140,60
s_add_u32 s36, s29, 131072
K 219,190,191
Mvaa 172,136,56
W 2
X 161
Cvaa 176,144,0,224
Ra 136,242,26624
Ra 140,242,27648
Mvaa 176,148,4
X 162
Cvaa 180,148,20,228
K 196,160,161
Mvaa 180,144,16
buffer_store_dwordx4 a[124:127], v1, s[8:11], s36 offen offset:0 sc1
Cvaa 184,144,32,232
X 163
Mvaa 184,148,36
X 164
Cvaa 188,148,52,236
K 197,162,163
Mvaa 188,144,48
X 165
Mvaa 176,152,8
Ra 144,242,28672
buffer_store_dwordx4 a[116:119], v1, s[8:11], s36 offen offset:1024 sc1
Mvaa 176,156,12
Ra 148,242,29696
Mvaa 180,156,28
X 166
Mvaa 180,152,24
K 204,164,165
Mvaa 184,152,40
X 167
Mvaa 184,156,44
buffer_store_dwordx4 a[108:111], v1, s[8:11], s36 offen offset:2048 sc1
Mvaa 188,156,60
X 168
Mvaa 188,152,56
W 2
Ra 152,242,30720
K 205,166,167
Mvav 32,128,192
X 169
Mvav 64,128,200
Ra 156,242,31744
buffer_store_dwordx4 a[204:207], v1, s[8:11], s36 offen offset:3072 sc1
Mvav 96,128,208
X 170
Mvav 128,128,216
v_xor_b32 v242, 0x10000, v242
K 212,168,169
Mvav 132,132,216
X 171
Mvav 100,132,208
s_add_u32 s36, s29, 135168
X 172
Mvav 68,132,200
Mvav 36,132,192
K 213,170,171
Mvav 40,136,192
X 173
Mvav 72,136,200
X 174
Mvav 104,136,208
buffer_store_dwordx4 a[212:215], v1, s[8:11], s36 offen offset:0 sc1
Mvav 136,136,216
K 220,172,173
Mvav 140,140,216
X 175
Mvav 108,140,208
X 176
Mvav 76,140,200
s_waitcnt vmcnt(5) lgkmcnt(0)
s_barrier
Mvav 44,140,192
buffer_store_dwordx4 a[220:223], v1, s[8:11], s36 offen offset:1024 sc1
K 221,174,175
Mvav 48,144,192
Ra 128,242,0
Mvav 80,144,200
X 177
Mvav 112,144,208
X 178
Mvav 144,144,216
Ra 132,242,1024
K 198,176,177
Mvav 148,148,216
buffer_store_dwordx4 a[228:231], v1, s[8:11], s36 offen offset:2048 sc1
Mvav 116,148,208
X 179
Mvav 84,148,200
X 180
Mvav 52,148,192
Ra 136,242,2048
K 199,178,179
Mvav 56,152,192
buffer_store_dwordx4 a[76:79], v1, s[8:11], s36 offen offset:3072 sc1
Ra 140,242,3072
Mvav 88,152,200
X 181
Mvav 120,152,208
X 182
Mvav 152,152,216
K 206,180,181
Mvav 156,156,216
X 183
Mvav 124,156,208
Mvav 92,156,200
K 207,182,183
Mvav 60,156,192
W 0
Cvaa 160,128,0,224
Ra 144,242,4096
Ra 148,242,5120
Mvaa 160,132,4
s_add_u32 m0, s62, 0
Ra 152,242,6144
Cvaa 164,132,20,228
X 184
Mvaa 164,128,16
X 185
Cvaa 168,128,32,232
X 186
Mvaa 168,132,36
Ra 156,242,7168
)"
R"(K 214,184,185
Cvaa 172,132,52,236
Dc 1,8,61,0
Mvaa 172,128,48
X 187
Mvaa 160,136,8
X 188
Mvaa 160,140,12
Ra 128,242,16384
K 215,186,187
Mvaa 164,140,28
Dc 1,8,61,1024
Mvaa 164,136,24
X 189
Mvaa 168,136,40
X 190
Mvaa 168,140,44
Ra 132,242,17408
K 222,188,189
Mvaa 172,140,60
Dc 1,8,61,2048
Mvaa 172,136,56
X 191
v_smfmac_f32_16x16x64_bf16 a[160:163], a[176:179], v[192:199], v240
Ra 136,242,18432
K 223,190,191
v_smfmac_f32_16x16x64_bf16 a[164:167], a[176:179], v[200:207], v240
X 160
v_smfmac_f32_16x16x64_bf16 a[168:171], a[176:179], v[208:215], v240
Dc 1,8,61,3072
v_smfmac_f32_16x16x64_bf16 a[172:175], a[176:179], v[216:223], v240
W 3
X 161
Cvaa 176,144,0,224
s_add_u32 m0, s62, 4096
X 162
Mvaa 176,148,4
Ra 140,242,19456
K 192,160,161
Cvaa 180,148,20,228
X 163
Mvaa 180,144,16
Dc 243,8,61,0
Cvaa 184,144,32,232
X 164
Mvaa 184,148,36
K 193,162,163
Cvaa 188,148,52,236
X 165
Mvaa 188,144,48
Dc 243,8,61,1024
Ra 144,242,20480
Mvaa 176,152,8
X 166
Mvaa 176,156,12
Ra 148,242,21504
K 200,164,165
Mvaa 180,156,28
X 167
Mvaa 180,152,24
Dc 243,8,61,2048
Mvaa 184,152,40
X 168
Mvaa 184,156,44
K 201,166,167
Mvaa 188,156,60
X 169
Mvaa 188,152,56
W 2
Dc 243,8,61,3072
Ra 152,242,22528
Mvav 32,128,196
s_add_u32 m0, s62, 8192
X 170
Mvav 64,128,204
Ra 156,242,23552
K 208,168,169
Mvav 96,128,212
X 171
Mvav 128,128,220
Dc 244,8,61,0
Mvav 132,132,220
X 172
Mvav 100,132,212
Ra 128,242,8192
K 209,170,171
Mvav 68,132,204
X 173
Mvav 36,132,196
Dc 244,8,61,1024
Mvav 40,136,196
X 174
Mvav 72,136,204
Ra 132,242,9216
K 216,172,173
Mvav 104,136,212
X 175
Mvav 136,136,220
Dc 244,8,61,2048
Mvav 140,140,220
X 176
Mvav 108,140,212
Ra 136,242,10240
K 217,174,175
Mvav 76,140,204
X 177
Mvav 44,140,196
W 3
Dc 244,8,61,3072
Mvav 48,144,196
X 178
Mvav 80,144,204
s_add_u32 m0, s62, 12288
Ra 140,242,11264
Mvav 112,144,212
K 194,176,177
Mvav 144,144,220
X 179
Mvav 148,148,220
X 180
Mvav 116,148,212
Dc 245,8,61,0
Ra 144,242,12288
Mvav 84,148,204
K 195,178,179
Mvav 52,148,196
X 181
Mvav 56,152,196
X 182
Mvav 88,152,204
Dc 245,8,61,1024
Ra 148,242,13312
Mvav 120,152,212
K 202,180,181
Mvav 152,152,220
X 183
Mvav 156,156,220
X 184
Mvav 124,156,212
Dc 245,8,61,2048
Ra 152,242,14336
Mvav 92,156,204
K 203,182,183
Mvav 60,156,196
W 3
X 185
Cvaa 160,128,0,224
X 186
Mvaa 160,132,4
Dc 245,8,61,3072
Ra 156,242,15360
Cvaa 164,132,20,228
s_add_u32 s61, s61, s63
K 210,184,185
Mvaa 164,128,16
X 187
Cvaa 168,128,32,232
X 188
Mvaa 168,132,36
Cvaa 172,132,52,236
s_xor_b32 s62, s62, 0x10000
K 211,186,187
Mvaa 172,128,48
X 189
Mvaa 160,136,8
Ra 128,242,24576
Ra 132,242,25600
Mvaa 160,140,12
s_add_u32 s64, s64, 1
X 190
Mvaa 164,140,28
K 218,188,189
Mvaa 164,136,24
X 191
Mvaa 168,136,40
Mvaa 168,140,44
X 160
Mvaa 172,140,60
s_add_u32 s36, s29, 327680
K 219,190,191
Mvaa 172,136,56
W 2
X 161
Cvaa 176,144,0,224
Ra 136,242,26624
Ra 140,242,27648
Mvaa 176,148,4
X 162
Cvaa 180,148,20,228
K 196,160,161
Mvaa 180,144,16
buffer_store_dwordx4 a[84:87], v1, s[8:11], s36 offen offset:0 sc1
Cvaa 184,144,32,232
X 163
Mvaa 184,148,36
X 164
Cvaa 188,148,52,236
K 197,162,163
Mvaa 188,144,48
X 165
Mvaa 176,152,8
Ra 144,242,28672
buffer_store_dwordx4 a[92:95], v1, s[8:11], s36 offen offset:1024 sc1
Mvaa 176,156,12
Ra 148,242,29696
Mvaa 180,156,28
X 166
Mvaa 180,152,24
K 204,164,165
Mvaa 184,152,40
X 167
Mvaa 184,156,44
buffer_store_dwordx4 a[188:191], v1, s[8:11], s36 offen offset:2048 sc1
Mvaa 188,156,60
X 168
Mvaa 188,152,56
W 2
Ra 152,242,30720
K 205,166,167
Mvav 32,128,192
X 169
Mvav 64,128,200
Ra 156,242,31744
buffer_store_dwordx4 a[196:199], v1, s[8:11], s36 offen offset:3072 sc1
Mvav 96,128,208
X 170
Mvav 128,128,216
v_xor_b32 v242, 0x10000, v242
K 212,168,169
Mvav 132,132,216
X 171
Mvav 100,132,208
s_add_u32 s36, s29, 331776
X 172
Mvav 68,132,200
Mvav 36,132,192
K 213,170,171
Mvav 40,136,192
X 173
Mvav 72,136,200
X 174
Mvav 104,136,208
buffer_store_dwordx4 a[236:239], v1, s[8:11], s36 offen offset:0 sc1
Mvav 136,136,216
K 220,172,173
Mvav 140,140,216
X 175
Mvav 108,140,208
X 176
Mvav 76,140,200
s_waitcnt vmcnt(5) lgkmcnt(0)
s_barrier
Mvav 44,140,192
buffer_store_dwordx4 a[244:247], v1, s[8:11], s36 offen offset:1024 sc1
K 221,174,175
Mvav 48,144,192
Ra 128,242,0
Mvav 80,144,200
X 177
Mvav 112,144,208
X 178
Mvav 144,144,216
Ra 132,242,1024
K 198,176,177
Mvav 148,148,216
buffer_store_dwordx4 a[68:71], v1, s[8:11], s36 offen offset:2048 sc1
Mvav 116,148,208
X 179
Mvav 84,148,200
X 180
Mvav 52,148,192
Ra 136,242,2048
K 199,178,179
Mvav 56,152,192
buffer_store_dwordx4 a[184:187], v1, s[8:11], s36 offen offset:3072 sc1
Ra 140,242,3072
Mvav 88,152,200
X 181
Mvav 120,152,208
X 182
Mvav 152,152,216
K 206,180,181
Mvav 156,156,216
X 183
Mvav 124,156,208
Mvav 92,156,200
K 207,182,183
Mvav 60,156,192
s_mov_b64 exec, 0
s_cmp_eq_u32 s14, 2
s_cbranch_scc1 .Lh_3_2%=
s_branch .Lh_3_3%=
.Lhr_66%=:
s_mov_b64 exec, -1
W 0
Cvaa 160,128,0,224
Ra 144,242,4096
Ra 148,242,5120
Mvaa 160,132,4
s_add_u32 m0, s62, 0
Ra 152,242,6144
Cvaa 164,132,20,228
X 184
Mvaa 164,128,16
X 185
Cvaa 168,128,32,232
X 186
Mvaa 168,132,36
Ra 156,242,7168
K 214,184,185
Cvaa 172,132,52,236
Dc 1,8,61,0
Mvaa 172,128,48
X 187
Mvaa 160,136,8
X 188
Mvaa 160,140,12
Ra 128,242,16384
K 215,186,187
Mvaa 164,140,28
Dc 1,8,61,1024
Mvaa 164,136,24
X 189
Mvaa 168,136,40
X 190
Mvaa 168,140,44
Ra 132,242,17408
K 222,188,189
Mvaa 172,140,60
Dc 1,8,61,2048
Mvaa 172,136,56
X 191
v_smfmac_f32_16x16x64_bf16 a[160:163], a[176:179], v[192:199], v240
Ra 136,242,18432
K 223,190,191
v_smfmac_f32_16x16x64_bf16 a[164:167], a[176:179], v[200:207], v240
X 160
v_smfmac_f32_16x16x64_bf16 a[168:171], a[176:179], v[208:215], v240
Dc 1,8,61,3072
v_smfmac_f32_16x16x64_bf16 a[172:175], a[176:179], v[216:223], v240
W 3
X 161
Cvaa 176,144,0,224
s_add_u32 m0, s62, 4096
X 162
Mvaa 176,148,4
Ra 140,242,19456
K 192,160,161
Cvaa 180,148,20,228
X 163
Mvaa 180,144,16
Dc 243,8,61,0
Cvaa 184,144,32,232
X 164
Mvaa 184,148,36
K 193,162,163
Cvaa 188,148,52,236
X 165
Mvaa 188,144,48
Dc 243,8,61,1024
Ra 144,242,20480
Mvaa 176,152,8
X 166
Mvaa 176,156,12
Ra 148,242,21504
K 200,164,165
Mvaa 180,156,28
X 167
Mvaa 180,152,24
Dc 243,8,61,2048
Mvaa 184,152,40
X 168
Mvaa 184,156,44
K 201,166,167
Mvaa 188,156,60
X 169
Mvaa 188,152,56
W 2
Dc 243,8,61,3072
Ra 152,242,22528
Mvav 32,128,196
s_add_u32 m0, s62, 8192
X 170
Mvav 64,128,204
Ra 156,242,23552
K 208,168,169
Mvav 96,128,212
X 171
Mvav 128,128,220
Dc 244,8,61,0
Mvav 132,132,220
X 172
Mvav 100,132,212
Ra 128,242,8192
K 209,170,171
Mvav 68,132,204
X 173
Mvav 36,132,196
Dc 244,8,61,1024
Mvav 40,136,196
)"
R"(X 174
Mvav 72,136,204
Ra 132,242,9216
K 216,172,173
Mvav 104,136,212
X 175
Mvav 136,136,220
Dc 244,8,61,2048
Mvav 140,140,220
X 176
Mvav 108,140,212
Ra 136,242,10240
K 217,174,175
Mvav 76,140,204
X 177
Mvav 44,140,196
W 3
Dc 244,8,61,3072
Mvav 48,144,196
X 178
Mvav 80,144,204
s_add_u32 m0, s62, 12288
Ra 140,242,11264
Mvav 112,144,212
K 194,176,177
Mvav 144,144,220
X 179
Mvav 148,148,220
X 180
Mvav 116,148,212
Dc 245,8,61,0
Ra 144,242,12288
Mvav 84,148,204
K 195,178,179
Mvav 52,148,196
X 181
Mvav 56,152,196
X 182
Mvav 88,152,204
Dc 245,8,61,1024
Ra 148,242,13312
Mvav 120,152,212
K 202,180,181
Mvav 152,152,220
X 183
Mvav 156,156,220
s_waitcnt vmcnt(14)
s_add_u32 s80, s71, 256
v_mov_b64 v[28:29], s[12:13]
s_mov_b64 exec, 1
buffer_store_dwordx2 v[28:29], off, s[8:11], s80 sc1
s_mov_b64 exec, -1
X 184
Mvav 124,156,212
Ra 152,242,14336
Mvav 92,156,204
K 203,182,183
Mvav 60,156,196
W 3
X 185
Cvaa 160,128,0,224
Dc 245,8,61,2048
Ra 156,242,15360
Mvaa 160,132,4
X 186
Cvaa 164,132,20,228
K 210,184,185
Mvaa 164,128,16
X 187
Cvaa 168,128,32,232
Dc 245,8,61,3072
Mvaa 168,132,36
s_add_u32 s61, s61, s63
X 188
Cvaa 172,132,52,236
s_xor_b32 s62, s62, 0x10000
K 211,186,187
Mvaa 172,128,48
X 189
Mvaa 160,136,8
Ra 128,242,24576
Ra 132,242,25600
Mvaa 160,140,12
s_add_u32 s64, s64, 1
X 190
Mvaa 164,140,28
K 218,188,189
Mvaa 164,136,24
X 191
Mvaa 168,136,40
Mvaa 168,140,44
X 160
Mvaa 172,140,60
s_add_u32 s36, s29, 196608
K 219,190,191
Mvaa 172,136,56
W 2
X 161
Cvaa 176,144,0,224
Ra 136,242,26624
Ra 140,242,27648
Mvaa 176,148,4
X 162
Cvaa 180,148,20,228
K 196,160,161
Mvaa 180,144,16
buffer_store_dwordx4 a[192:195], v1, s[8:11], s36 offen offset:0 sc1
Cvaa 184,144,32,232
X 163
Mvaa 184,148,36
X 164
Cvaa 188,148,52,236
K 197,162,163
Mvaa 188,144,48
X 165
Mvaa 176,152,8
Ra 144,242,28672
buffer_store_dwordx4 a[200:203], v1, s[8:11], s36 offen offset:1024 sc1
Mvaa 176,156,12
Ra 148,242,29696
Mvaa 180,156,28
X 166
Mvaa 180,152,24
K 204,164,165
Mvaa 184,152,40
X 167
Mvaa 184,156,44
buffer_store_dwordx4 a[208:211], v1, s[8:11], s36 offen offset:2048 sc1
Mvaa 188,156,60
X 168
Mvaa 188,152,56
W 2
Ra 152,242,30720
K 205,166,167
Mvav 32,128,192
X 169
Mvav 64,128,200
Ra 156,242,31744
buffer_store_dwordx4 a[216:219], v1, s[8:11], s36 offen offset:3072 sc1
Mvav 96,128,208
X 170
Mvav 128,128,216
v_xor_b32 v242, 0x10000, v242
K 212,168,169
Mvav 132,132,216
X 171
Mvav 100,132,208
s_add_u32 s36, s29, 200704
X 172
Mvav 68,132,200
Mvav 36,132,192
K 213,170,171
Mvav 40,136,192
X 173
Mvav 72,136,200
X 174
Mvav 104,136,208
buffer_store_dwordx4 a[224:227], v1, s[8:11], s36 offen offset:0 sc1
Mvav 136,136,216
K 220,172,173
Mvav 140,140,216
X 175
Mvav 108,140,208
X 176
Mvav 76,140,200
s_waitcnt vmcnt(5) lgkmcnt(0)
s_barrier
Mvav 44,140,192
buffer_store_dwordx4 a[232:235], v1, s[8:11], s36 offen offset:1024 sc1
K 221,174,175
Mvav 48,144,192
Ra 128,242,0
Mvav 80,144,200
X 177
Mvav 112,144,208
X 178
Mvav 144,144,216
Ra 132,242,1024
K 198,176,177
Mvav 148,148,216
buffer_store_dwordx4 a[240:243], v1, s[8:11], s36 offen offset:2048 sc1
Mvav 116,148,208
X 179
Mvav 84,148,200
X 180
Mvav 52,148,192
Ra 136,242,2048
K 199,178,179
Mvav 56,152,192
buffer_store_dwordx4 a[100:103], v1, s[8:11], s36 offen offset:3072 sc1
Ra 140,242,3072
Mvav 88,152,200
X 181
Mvav 120,152,208
X 182
Mvav 152,152,216
K 206,180,181
Mvav 156,156,216
X 183
Mvav 124,156,208
Mvav 92,156,200
K 207,182,183
Mvav 60,156,192
W 0
Cvaa 160,128,0,224
Ra 144,242,4096
Ra 148,242,5120
Mvaa 160,132,4
s_add_u32 m0, s62, 0
Ra 152,242,6144
Cvaa 164,132,20,228
X 184
Mvaa 164,128,16
X 185
Cvaa 168,128,32,232
X 186
Mvaa 168,132,36
Ra 156,242,7168
K 214,184,185
Cvaa 172,132,52,236
Dc 1,8,61,0
Mvaa 172,128,48
X 187
Mvaa 160,136,8
X 188
Mvaa 160,140,12
Ra 128,242,16384
K 215,186,187
Mvaa 164,140,28
Dc 1,8,61,1024
Mvaa 164,136,24
X 189
Mvaa 168,136,40
X 190
Mvaa 168,140,44
Ra 132,242,17408
K 222,188,189
Mvaa 172,140,60
Dc 1,8,61,2048
Mvaa 172,136,56
X 191
v_smfmac_f32_16x16x64_bf16 a[160:163], a[176:179], v[192:199], v240
Ra 136,242,18432
K 223,190,191
v_smfmac_f32_16x16x64_bf16 a[164:167], a[176:179], v[200:207], v240
X 160
v_smfmac_f32_16x16x64_bf16 a[168:171], a[176:179], v[208:215], v240
Dc 1,8,61,3072
v_smfmac_f32_16x16x64_bf16 a[172:175], a[176:179], v[216:223], v240
W 3
X 161
Cvaa 176,144,0,224
s_add_u32 m0, s62, 4096
X 162
Mvaa 176,148,4
Ra 140,242,19456
K 192,160,161
Cvaa 180,148,20,228
X 163
Mvaa 180,144,16
Dc 243,8,61,0
Cvaa 184,144,32,232
X 164
Mvaa 184,148,36
K 193,162,163
Cvaa 188,148,52,236
X 165
Mvaa 188,144,48
Dc 243,8,61,1024
Ra 144,242,20480
Mvaa 176,152,8
X 166
Mvaa 176,156,12
Ra 148,242,21504
K 200,164,165
Mvaa 180,156,28
X 167
Mvaa 180,152,24
Dc 243,8,61,2048
Mvaa 184,152,40
X 168
Mvaa 184,156,44
K 201,166,167
Mvaa 188,156,60
X 169
Mvaa 188,152,56
W 2
Dc 243,8,61,3072
Ra 152,242,22528
Mvav 32,128,196
s_add_u32 m0, s62, 8192
X 170
Mvav 64,128,204
Ra 156,242,23552
K 208,168,169
Mvav 96,128,212
X 171
Mvav 128,128,220
Dc 244,8,61,0
Mvav 132,132,220
X 172
Mvav 100,132,212
Ra 128,242,8192
K 209,170,171
Mvav 68,132,204
X 173
Mvav 36,132,196
Dc 244,8,61,1024
Mvav 40,136,196
X 174
Mvav 72,136,204
Ra 132,242,9216
K 216,172,173
Mvav 104,136,212
X 175
Mvav 136,136,220
Dc 244,8,61,2048
Mvav 140,140,220
X 176
Mvav 108,140,212
Ra 136,242,10240
K 217,174,175
Mvav 76,140,204
X 177
Mvav 44,140,196
W 3
Dc 244,8,61,3072
Mvav 48,144,196
X 178
Mvav 80,144,204
s_add_u32 m0, s62, 12288
Ra 140,242,11264
Mvav 112,144,212
K 194,176,177
Mvav 144,144,220
X 179
Mvav 148,148,220
X 180
Mvav 116,148,212
Dc 245,8,61,0
Ra 144,242,12288
Mvav 84,148,204
K 195,178,179
Mvav 52,148,196
X 181
Mvav 56,152,196
X 182
Mvav 88,152,204
Dc 245,8,61,1024
Ra 148,242,13312
Mvav 120,152,212
K 202,180,181
Mvav 152,152,220
X 183
Mvav 156,156,220
X 184
Mvav 124,156,212
Dc 245,8,61,2048
Ra 152,242,14336
Mvav 92,156,204
K 203,182,183
v_bfe_u32 v28, v1, 4, 4
Mvav 60,156,196
W 3
X 185
Cvaa 160,128,0,224
Ra 156,242,15360
v_lshlrev_b32 v28, 3, v28
Mvaa 160,132,4
s_lshl_b32 s80, s56, 9
s_add_u32 s80, s80, 4352
Dc 245,8,61,3072
Cvaa 164,132,20,228
s_add_u32 s61, s61, s63
X 186
Mvaa 164,128,16
K 210,184,185
Cvaa 168,128,32,232
X 187
Mvaa 168,132,36
buffer_load_dwordx2 v[28:29], v28, s[8:11], s80 offen sc1
Cvaa 172,132,52,236
s_xor_b32 s62, s62, 0x10000
X 188
Mvaa 172,128,48
Ra 128,242,24576
K 211,186,187
Mvaa 160,136,8
X 189
Mvaa 160,140,12
s_add_u32 s64, s64, 1
X 190
Mvaa 164,140,28
Ra 132,242,25600
K 218,188,189
Mvaa 164,136,24
X 191
Mvaa 168,136,40
Mvaa 168,140,44
X 160
)"
R"(Mvaa 172,140,60
s_add_u32 s36, s29, 262144
K 219,190,191
Mvaa 172,136,56
W 2
X 161
Cvaa 176,144,0,224
Ra 136,242,26624
Ra 140,242,27648
Mvaa 176,148,4
X 162
Cvaa 180,148,20,228
K 196,160,161
Mvaa 180,144,16
buffer_store_dwordx4 a[64:67], v1, s[8:11], s36 offen offset:0 sc1
Cvaa 184,144,32,232
X 163
Mvaa 184,148,36
X 164
Cvaa 188,148,52,236
K 197,162,163
Mvaa 188,144,48
X 165
Mvaa 176,152,8
Ra 144,242,28672
buffer_store_dwordx4 a[72:75], v1, s[8:11], s36 offen offset:1024 sc1
Mvaa 176,156,12
Ra 148,242,29696
Mvaa 180,156,28
X 166
Mvaa 180,152,24
K 204,164,165
Mvaa 184,152,40
X 167
Mvaa 184,156,44
buffer_store_dwordx4 a[80:83], v1, s[8:11], s36 offen offset:2048 sc1
Mvaa 188,156,60
X 168
Mvaa 188,152,56
W 2
Ra 152,242,30720
K 205,166,167
Mvav 32,128,192
X 169
Mvav 64,128,200
Ra 156,242,31744
buffer_store_dwordx4 a[88:91], v1, s[8:11], s36 offen offset:3072 sc1
Mvav 96,128,208
X 170
Mvav 128,128,216
v_xor_b32 v242, 0x10000, v242
K 212,168,169
Mvav 132,132,216
X 171
Mvav 100,132,208
s_add_u32 s36, s29, 266240
X 172
Mvav 68,132,200
Mvav 36,132,192
K 213,170,171
Mvav 40,136,192
X 173
Mvav 72,136,200
X 174
Mvav 104,136,208
buffer_store_dwordx4 a[96:99], v1, s[8:11], s36 offen offset:0 sc1
Mvav 136,136,216
s_waitcnt vmcnt(5)
v_cmp_eq_u64 vcc, s[12:13], v[28:29]
s_cmp_eq_u64 vcc, -1
s_cbranch_scc0 .Lspin_67%=
.Lok_68%=:
K 220,172,173
Mvav 140,140,216
X 175
Mvav 108,140,208
X 176
Mvav 76,140,200
W 0
s_barrier
Mvav 44,140,192
buffer_store_dwordx4 a[104:107], v1, s[8:11], s36 offen offset:1024 sc1
K 221,174,175
Mvav 48,144,192
Ra 128,242,0
Mvav 80,144,200
X 177
Mvav 112,144,208
X 178
Mvav 144,144,216
Ra 132,242,1024
K 198,176,177
Mvav 148,148,216
buffer_store_dwordx4 a[112:115], v1, s[8:11], s36 offen offset:2048 sc1
Mvav 116,148,208
X 179
Mvav 84,148,200
X 180
Mvav 52,148,192
Ra 136,242,2048
K 199,178,179
Mvav 56,152,192
buffer_store_dwordx4 a[120:123], v1, s[8:11], s36 offen offset:3072 sc1
Ra 140,242,3072
Mvav 88,152,200
X 181
Mvav 120,152,208
X 182
Mvav 152,152,216
K 206,180,181
Mvav 156,156,216
X 183
Mvav 124,156,208
Mvav 92,156,200
K 207,182,183
Mvav 60,156,192
W 0
Cvaa 160,128,0,224
Ra 144,242,4096
Ra 148,242,5120
Mvaa 160,132,4
s_add_u32 m0, s62, 0
Ra 152,242,6144
Cvaa 164,132,20,228
X 184
Mvaa 164,128,16
X 185
Cvaa 168,128,32,232
X 186
Mvaa 168,132,36
Ra 156,242,7168
K 214,184,185
Cvaa 172,132,52,236
Dc 1,8,61,0
Mvaa 172,128,48
X 187
Mvaa 160,136,8
X 188
Mvaa 160,140,12
Ra 128,242,16384
K 215,186,187
Mvaa 164,140,28
Dc 1,8,61,1024
Mvaa 164,136,24
X 189
Mvaa 168,136,40
X 190
Mvaa 168,140,44
Ra 132,242,17408
K 222,188,189
Mvaa 172,140,60
Dc 1,8,61,2048
Mvaa 172,136,56
X 191
v_smfmac_f32_16x16x64_bf16 a[160:163], a[176:179], v[192:199], v240
Ra 136,242,18432
K 223,190,191
v_smfmac_f32_16x16x64_bf16 a[164:167], a[176:179], v[200:207], v240
X 160
v_smfmac_f32_16x16x64_bf16 a[168:171], a[176:179], v[208:215], v240
Dc 1,8,61,3072
v_smfmac_f32_16x16x64_bf16 a[172:175], a[176:179], v[216:223], v240
W 3
X 161
Cvaa 176,144,0,224
s_add_u32 m0, s62, 4096
X 162
Mvaa 176,148,4
Ra 140,242,19456
K 192,160,161
Cvaa 180,148,20,228
X 163
Mvaa 180,144,16
Dc 243,8,61,0
Cvaa 184,144,32,232
X 164
Mvaa 184,148,36
K 193,162,163
Cvaa 188,148,52,236
X 165
Mvaa 188,144,48
Dc 243,8,61,1024
Ra 144,242,20480
Mvaa 176,152,8
X 166
Mvaa 176,156,12
Ra 148,242,21504
K 200,164,165
Mvaa 180,156,28
X 167
Mvaa 180,152,24
Dc 243,8,61,2048
Mvaa 184,152,40
X 168
Mvaa 184,156,44
K 201,166,167
Mvaa 188,156,60
X 169
Mvaa 188,152,56
W 2
Dc 243,8,61,3072
Ra 152,242,22528
Mvav 32,128,196
s_add_u32 m0, s62, 8192
X 170
Mvav 64,128,204
Ra 156,242,23552
K 208,168,169
Mvav 96,128,212
X 171
Mvav 128,128,220
Dc 244,8,61,0
Mvav 132,132,220
X 172
Mvav 100,132,212
Ra 128,242,8192
K 209,170,171
Mvav 68,132,204
X 173
Mvav 36,132,196
Dc 244,8,61,1024
Mvav 40,136,196
X 174
Mvav 72,136,204
Ra 132,242,9216
K 216,172,173
Mvav 104,136,212
X 175
Mvav 136,136,220
Dc 244,8,61,2048
Mvav 140,140,220
X 176
Mvav 108,140,212
Ra 136,242,10240
K 217,174,175
Mvav 76,140,204
X 177
Mvav 44,140,196
W 3
Dc 244,8,61,3072
Mvav 48,144,196
X 178
Mvav 80,144,204
s_add_u32 m0, s62, 12288
Ra 140,242,11264
Mvav 112,144,212
K 194,176,177
Mvav 144,144,220
X 179
Mvav 148,148,220
X 180
Mvav 116,148,212
Dc 245,8,61,0
Ra 144,242,12288
Mvav 84,148,204
K 195,178,179
Mvav 52,148,196
X 181
Mvav 56,152,196
X 182
Mvav 88,152,204
Dc 245,8,61,1024
Ra 148,242,13312
Mvav 120,152,212
s_bitcmp1_b32 195, s57
s_cselect_b32 s22, -1, 0
K 202,180,181
Mvav 152,152,220
s_lshr_b32 s82, s57, 2
X 183
Mvav 156,156,220
s_waitcnt vmcnt(14)
s_add_u32 s80, s71, 384
v_mov_b64 v[28:29], s[12:13]
s_mov_b64 exec, 1
buffer_store_dwordx2 v[28:29], off, s[8:11], s80 sc1
s_mov_b64 exec, -1
X 184
Mvav 124,156,212
s_and_b32 s81, s57, 1
s_lshl1_add_u32 s81, s82, s81
Ra 152,242,14336
Mvav 92,156,204
s_lshl_b32 s82, s56, 2
K 203,182,183
Mvav 60,156,196
W 3
X 185
Cvaa 160,128,0,224
s_add_u32 s81, s81, s82
Dc 245,8,61,2048
Ra 156,242,15360
Mvaa 160,132,4
s_lshl_b32 s81, s81, 1
X 186
Cvaa 164,132,20,228
s_add_u32 s81, s81, s14
s_sub_u32 s81, s81, 2
K 210,184,185
Mvaa 164,128,16
s_mul_i32 s81, s81, 20480
X 187
Cvaa 168,128,32,232
Dc 245,8,61,3072
v_min_u32 v2, 624, v1
Mvaa 168,132,36
s_add_u32 s61, s61, s63
X 188
Cvaa 172,132,52,236
s_xor_b32 s62, s62, 0x10000
v_lshl_add_u32 v2, v2, 3, s81
K 211,186,187
Mvaa 172,128,48
Ra 128,242,24576
v_and_b32 v2, s22, v2
Mvaa 160,136,8
s_and_b32 s22, s22, 5120
X 189
Mvaa 160,140,12
s_add_u32 s64, s64, 1
global_load_dword v3, v2, s[26:27]
Ra 132,242,25600
Mvaa 164,140,28
X 190
Mvaa 164,136,24
v_add_u32 v2, s22, v2
K 218,188,189
Mvaa 168,136,40
X 191
Mvaa 168,140,44
X 160
Mvaa 172,140,60
K 219,190,191
Mvaa 172,136,56
W 2
X 161
Cvaa 176,144,0,224
Ra 136,242,26624
Ra 140,242,27648
Mvaa 176,148,4
X 162
Cvaa 180,148,20,228
K 196,160,161
Mvaa 180,144,16
X 163
Cvaa 184,144,32,232
Mvaa 184,148,36
X 164
Cvaa 188,148,52,236
K 197,162,163
Mvaa 188,144,48
X 165
Mvaa 176,152,8
Ra 144,242,28672
Ra 148,242,29696
Mvaa 176,156,12
X 166
Mvaa 180,156,28
K 204,164,165
Mvaa 180,152,24
X 167
Mvaa 184,152,40
Mvaa 184,156,44
X 168
Mvaa 188,156,60
Mvaa 188,152,56
W 2
Ra 152,242,30720
K 205,166,167
Mvav 32,128,192
X 169
Mvav 64,128,200
Ra 156,242,31744
v_xor_b32 v242, 0x10000, v242
Mvav 96,128,208
X 170
Mvav 128,128,216
K 212,168,169
Mvav 132,132,216
X 171
Mvav 100,132,208
X 172
Mvav 68,132,200
Mvav 36,132,192
K 213,170,171
Mvav 40,136,192
X 173
Mvav 72,136,200
X 174
Mvav 104,136,208
Mvav 136,136,216
K 220,172,173
Mvav 140,140,216
X 175
Mvav 108,140,208
X 176
Mvav 76,140,200
s_waitcnt vmcnt(1) lgkmcnt(0)
s_barrier
Mvav 44,140,192
)"
R"(Ra 128,242,0
K 221,174,175
Mvav 48,144,192
.Lh_3_2%=:
s_cbranch_execz .Lh_3_2_1%=
X 177
Mvav 80,144,200
X 178
Mvav 112,144,208
Ra 132,242,1024
Mvav 144,144,216
K 198,176,177
Mvav 148,148,216
X 179
Mvav 116,148,208
X 180
Mvav 84,148,200
Ra 136,242,2048
Mvav 52,148,192
K 199,178,179
Mvav 56,152,192
X 181
Mvav 88,152,200
Ra 140,242,3072
Mvav 120,152,208
X 182
Mvav 152,152,216
K 206,180,181
Mvav 156,156,216
X 183
Mvav 124,156,208
Mvav 92,156,200
K 207,182,183
Mvav 60,156,192
W 0
Cvaa 160,128,0,224
Ra 144,242,4096
Ra 148,242,5120
Mvaa 160,132,4
s_add_u32 m0, s62, 0
Ra 152,242,6144
Cvaa 164,132,20,228
X 184
Mvaa 164,128,16
X 185
Cvaa 168,128,32,232
X 186
Mvaa 168,132,36
Ra 156,242,7168
K 214,184,185
Cvaa 172,132,52,236
Dc 1,8,61,0
Mvaa 172,128,48
X 187
Mvaa 160,136,8
X 188
Mvaa 160,140,12
Ra 128,242,16384
K 215,186,187
Mvaa 164,140,28
Dc 1,8,61,1024
Mvaa 164,136,24
X 189
Mvaa 168,136,40
X 190
Mvaa 168,140,44
Ra 132,242,17408
K 222,188,189
Mvaa 172,140,60
Dc 1,8,61,2048
Mvaa 172,136,56
X 191
v_smfmac_f32_16x16x64_bf16 a[160:163], a[176:179], v[192:199], v240
Ra 136,242,18432
K 223,190,191
v_smfmac_f32_16x16x64_bf16 a[164:167], a[176:179], v[200:207], v240
X 160
v_smfmac_f32_16x16x64_bf16 a[168:171], a[176:179], v[208:215], v240
Dc 1,8,61,3072
v_smfmac_f32_16x16x64_bf16 a[172:175], a[176:179], v[216:223], v240
W 3
X 161
Cvaa 176,144,0,224
s_add_u32 m0, s62, 4096
X 162
Mvaa 176,148,4
Ra 140,242,19456
K 192,160,161
Cvaa 180,148,20,228
X 163
Mvaa 180,144,16
Dc 243,8,61,0
Cvaa 184,144,32,232
X 164
Mvaa 184,148,36
K 193,162,163
Cvaa 188,148,52,236
X 165
Mvaa 188,144,48
Dc 243,8,61,1024
Ra 144,242,20480
Mvaa 176,152,8
X 166
Mvaa 176,156,12
Ra 148,242,21504
K 200,164,165
Mvaa 180,156,28
X 167
Mvaa 180,152,24
Dc 243,8,61,2048
Mvaa 184,152,40
X 168
Mvaa 184,156,44
K 201,166,167
Mvaa 188,156,60
X 169
Mvaa 188,152,56
W 2
Dc 243,8,61,3072
Ra 152,242,22528
Mvav 32,128,196
s_add_u32 m0, s62, 8192
X 170
Mvav 64,128,204
Ra 156,242,23552
K 208,168,169
Mvav 96,128,212
X 171
Mvav 128,128,220
Dc 244,8,61,0
Mvav 132,132,220
X 172
Mvav 100,132,212
Ra 128,242,8192
K 209,170,171
Mvav 68,132,204
X 173
Mvav 36,132,196
Dc 244,8,61,1024
Mvav 40,136,196
X 174
Mvav 72,136,204
Ra 132,242,9216
K 216,172,173
Mvav 104,136,212
X 175
Mvav 136,136,220
Dc 244,8,61,2048
Mvav 140,140,220
X 176
Mvav 108,140,212
Ra 136,242,10240
K 217,174,175
Mvav 76,140,204
X 177
Mvav 44,140,196
W 3
Dc 244,8,61,3072
Mvav 48,144,196
X 178
Mvav 80,144,204
s_add_u32 m0, s62, 12288
Ra 140,242,11264
Mvav 112,144,212
K 194,176,177
Mvav 144,144,220
X 179
Mvav 148,148,220
X 180
Mvav 116,148,212
Dc 245,8,61,0
Ra 144,242,12288
Mvav 84,148,204
K 195,178,179
Mvav 52,148,196
X 181
Mvav 56,152,196
X 182
Mvav 88,152,204
Dc 245,8,61,1024
Ra 148,242,13312
Mvav 120,152,212
K 202,180,181
Mvav 152,152,220
X 183
Mvav 156,156,220
X 184
Mvav 124,156,212
Dc 245,8,61,2048
Ra 152,242,14336
Mvav 92,156,204
K 203,182,183
Mvav 60,156,196
W 3
X 185
Cvaa 160,128,0,224
X 186
Mvaa 160,132,4
Dc 245,8,61,3072
Ra 156,242,15360
Cvaa 164,132,20,228
s_add_u32 s61, s61, s63
K 210,184,185
Mvaa 164,128,16
X 187
Cvaa 168,128,32,232
X 188
Mvaa 168,132,36
Cvaa 172,132,52,236
s_xor_b32 s62, s62, 0x10000
global_load_dword v3, v2, s[26:27]
K 211,186,187
Mvaa 172,128,48
X 189
Mvaa 160,136,8
Ra 128,242,24576
Ra 132,242,25600
Mvaa 160,140,12
s_add_u32 s64, s64, 1
X 190
Mvaa 164,140,28
v_add_u32 v2, s22, v2
K 218,188,189
Mvaa 164,136,24
X 191
Mvaa 168,136,40
Mvaa 168,140,44
X 160
Mvaa 172,140,60
K 219,190,191
Mvaa 172,136,56
W 2
X 161
Cvaa 176,144,0,224
Ra 136,242,26624
Ra 140,242,27648
Mvaa 176,148,4
X 162
Cvaa 180,148,20,228
K 196,160,161
Mvaa 180,144,16
X 163
Cvaa 184,144,32,232
Mvaa 184,148,36
X 164
Cvaa 188,148,52,236
K 197,162,163
Mvaa 188,144,48
X 165
Mvaa 176,152,8
Ra 144,242,28672
Ra 148,242,29696
Mvaa 176,156,12
X 166
Mvaa 180,156,28
K 204,164,165
Mvaa 180,152,24
X 167
Mvaa 184,152,40
Mvaa 184,156,44
X 168
Mvaa 188,156,60
Mvaa 188,152,56
W 2
Ra 152,242,30720
K 205,166,167
Mvav 32,128,192
X 169
Mvav 64,128,200
Ra 156,242,31744
v_xor_b32 v242, 0x10000, v242
Mvav 96,128,208
X 170
Mvav 128,128,216
K 212,168,169
Mvav 132,132,216
X 171
Mvav 100,132,208
X 172
Mvav 68,132,200
Mvav 36,132,192
K 213,170,171
Mvav 40,136,192
X 173
Mvav 72,136,200
X 174
Mvav 104,136,208
Mvav 136,136,216
K 220,172,173
Mvav 140,140,216
X 175
Mvav 108,140,208
X 176
Mvav 76,140,200
s_waitcnt vmcnt(1) lgkmcnt(0)
s_barrier
Mvav 44,140,192
Ra 128,242,0
K 221,174,175
Mvav 48,144,192
X 177
Mvav 80,144,200
X 178
Mvav 112,144,208
Ra 132,242,1024
Mvav 144,144,216
K 198,176,177
Mvav 148,148,216
X 179
Mvav 116,148,208
X 180
Mvav 84,148,200
Ra 136,242,2048
Mvav 52,148,192
K 199,178,179
Mvav 56,152,192
X 181
Mvav 88,152,200
Ra 140,242,3072
Mvav 120,152,208
X 182
Mvav 152,152,216
K 206,180,181
Mvav 156,156,216
X 183
Mvav 124,156,208
Mvav 92,156,200
K 207,182,183
Mvav 60,156,192
W 0
Cvaa 160,128,0,224
Ra 144,242,4096
Ra 148,242,5120
Mvaa 160,132,4
s_add_u32 m0, s62, 0
Ra 152,242,6144
Cvaa 164,132,20,228
X 184
Mvaa 164,128,16
X 185
Cvaa 168,128,32,232
X 186
Mvaa 168,132,36
Ra 156,242,7168
K 214,184,185
Cvaa 172,132,52,236
Dc 1,8,61,0
Mvaa 172,128,48
X 187
Mvaa 160,136,8
X 188
Mvaa 160,140,12
Ra 128,242,16384
K 215,186,187
Mvaa 164,140,28
Dc 1,8,61,1024
Mvaa 164,136,24
X 189
Mvaa 168,136,40
X 190
Mvaa 168,140,44
Ra 132,242,17408
K 222,188,189
Mvaa 172,140,60
Dc 1,8,61,2048
Mvaa 172,136,56
X 191
v_smfmac_f32_16x16x64_bf16 a[160:163], a[176:179], v[192:199], v240
Ra 136,242,18432
K 223,190,191
v_smfmac_f32_16x16x64_bf16 a[164:167], a[176:179], v[200:207], v240
X 160
v_smfmac_f32_16x16x64_bf16 a[168:171], a[176:179], v[208:215], v240
Dc 1,8,61,3072
v_smfmac_f32_16x16x64_bf16 a[172:175], a[176:179], v[216:223], v240
W 3
X 161
Cvaa 176,144,0,224
s_add_u32 m0, s62, 4096
X 162
Mvaa 176,148,4
Ra 140,242,19456
K 192,160,161
Cvaa 180,148,20,228
X 163
Mvaa 180,144,16
Dc 243,8,61,0
Cvaa 184,144,32,232
X 164
Mvaa 184,148,36
K 193,162,163
Cvaa 188,148,52,236
X 165
Mvaa 188,144,48
Dc 243,8,61,1024
Ra 144,242,20480
Mvaa 176,152,8
X 166
Mvaa 176,156,12
Ra 148,242,21504
)"
R"(K 200,164,165
Mvaa 180,156,28
X 167
Mvaa 180,152,24
Dc 243,8,61,2048
Mvaa 184,152,40
X 168
Mvaa 184,156,44
K 201,166,167
Mvaa 188,156,60
X 169
Mvaa 188,152,56
W 2
Dc 243,8,61,3072
Ra 152,242,22528
Mvav 32,128,196
s_add_u32 m0, s62, 8192
X 170
Mvav 64,128,204
Ra 156,242,23552
K 208,168,169
Mvav 96,128,212
X 171
Mvav 128,128,220
Dc 244,8,61,0
Mvav 132,132,220
X 172
Mvav 100,132,212
Ra 128,242,8192
K 209,170,171
Mvav 68,132,204
X 173
Mvav 36,132,196
Dc 244,8,61,1024
Mvav 40,136,196
X 174
Mvav 72,136,204
Ra 132,242,9216
K 216,172,173
Mvav 104,136,212
X 175
Mvav 136,136,220
Dc 244,8,61,2048
Mvav 140,140,220
X 176
Mvav 108,140,212
Ra 136,242,10240
K 217,174,175
Mvav 76,140,204
X 177
Mvav 44,140,196
W 3
Dc 244,8,61,3072
Mvav 48,144,196
X 178
Mvav 80,144,204
s_add_u32 m0, s62, 12288
Ra 140,242,11264
Mvav 112,144,212
K 194,176,177
Mvav 144,144,220
X 179
Mvav 148,148,220
X 180
Mvav 116,148,212
Dc 245,8,61,0
Ra 144,242,12288
Mvav 84,148,204
K 195,178,179
Mvav 52,148,196
X 181
Mvav 56,152,196
X 182
Mvav 88,152,204
Dc 245,8,61,1024
Ra 148,242,13312
Mvav 120,152,212
K 202,180,181
Mvav 152,152,220
X 183
Mvav 156,156,220
X 184
Mvav 124,156,212
Dc 245,8,61,2048
Ra 152,242,14336
Mvav 92,156,204
K 203,182,183
Mvav 60,156,196
W 3
X 185
Cvaa 160,128,0,224
X 186
Mvaa 160,132,4
Dc 245,8,61,3072
Ra 156,242,15360
Cvaa 164,132,20,228
s_add_u32 s61, s61, s63
K 210,184,185
Mvaa 164,128,16
X 187
Cvaa 168,128,32,232
X 188
Mvaa 168,132,36
Cvaa 172,132,52,236
s_xor_b32 s62, s62, 0x10000
global_load_dword v3, v2, s[26:27]
K 211,186,187
Mvaa 172,128,48
Ra 128,242,24576
v_add_u32 v2, s22, v2
Mvaa 160,136,8
s_lshr_b32 s80, s15, 3
s_lshl_b32 s80, s80, 1
s_add_u32 s80, s80, s14
Mvaa 160,140,12
s_sub_u32 s80, s80, 2
s_lshl_b32 s80, s80, 12
v_min_u32 v27, 496, v1
Mvaa 164,140,28
s_add_u32 s64, s64, 1
X 189
Mvaa 164,136,24
Ra 132,242,25600
v_lshl_add_u32 v27, v27, 3, s80
Mvaa 168,136,40
X 190
Mvaa 168,140,44
v_min_u32 v27, .Lend%=-.Lbeg%=-4, v27
K 218,188,189
Mvaa 172,140,60
global_load_dword v3, v27, s[6:7]
Mvaa 172,136,56
W 2
X 191
Cvaa 176,144,0,224
X 160
Mvaa 176,148,4
Ra 136,242,26624
K 219,190,191
Cvaa 180,148,20,228
X 161
Mvaa 180,144,16
Ra 140,242,27648
Cvaa 184,144,32,232
X 162
Mvaa 184,148,36
K 196,160,161
Cvaa 188,148,52,236
X 163
Mvaa 188,144,48
X 164
Mvaa 176,152,8
Ra 144,242,28672
K 197,162,163
Mvaa 176,156,12
X 165
Mvaa 180,156,28
Ra 148,242,29696
Mvaa 180,152,24
X 166
Mvaa 184,152,40
K 204,164,165
Mvaa 184,156,44
X 167
Mvaa 188,156,60
X 168
Mvaa 188,152,56
W 2
Ra 152,242,30720
K 205,166,167
Mvav 32,128,192
X 169
Mvav 64,128,200
Ra 156,242,31744
v_xor_b32 v242, 0x10000, v242
Mvav 96,128,208
X 170
Mvav 128,128,216
K 212,168,169
Mvav 132,132,216
X 171
Mvav 100,132,208
X 172
Mvav 68,132,200
Mvav 36,132,192
K 213,170,171
Mvav 40,136,192
X 173
Mvav 72,136,200
X 174
Mvav 104,136,208
Mvav 136,136,216
K 220,172,173
Mvav 140,140,216
.Lh_3_3%=:
s_cbranch_execz .Lhr_66%=
X 175
Mvav 108,140,208
X 176
Mvav 76,140,200
s_waitcnt vmcnt(2) lgkmcnt(0)
s_barrier
Mvav 44,140,192
Ra 128,242,0
K 221,174,175
Mvav 48,144,192
X 177
Mvav 80,144,200
X 178
Mvav 112,144,208
Ra 132,242,1024
Mvav 144,144,216
K 198,176,177
Mvav 148,148,216
X 179
Mvav 116,148,208
X 180
Mvav 84,148,200
Ra 136,242,2048
Mvav 52,148,192
K 199,178,179
Mvav 56,152,192
X 181
Mvav 88,152,200
Ra 140,242,3072
Mvav 120,152,208
X 182
Mvav 152,152,216
K 206,180,181
Mvav 156,156,216
X 183
Mvav 124,156,208
Mvav 92,156,200
K 207,182,183
Mvav 60,156,192
W 0
Cvaa 160,128,0,224
Ra 144,242,4096
Ra 148,242,5120
Mvaa 160,132,4
s_add_u32 m0, s62, 0
Ra 152,242,6144
Cvaa 164,132,20,228
X 184
Mvaa 164,128,16
X 185
Cvaa 168,128,32,232
X 186
Mvaa 168,132,36
Ra 156,242,7168
K 214,184,185
Cvaa 172,132,52,236
Dc 1,8,61,0
Mvaa 172,128,48
X 187
Mvaa 160,136,8
X 188
Mvaa 160,140,12
Ra 128,242,16384
K 215,186,187
Mvaa 164,140,28
Dc 1,8,61,1024
Mvaa 164,136,24
X 189
Mvaa 168,136,40
X 190
Mvaa 168,140,44
Ra 132,242,17408
K 222,188,189
Mvaa 172,140,60
Dc 1,8,61,2048
Mvaa 172,136,56
X 191
v_smfmac_f32_16x16x64_bf16 a[160:163], a[176:179], v[192:199], v240
Ra 136,242,18432
K 223,190,191
v_smfmac_f32_16x16x64_bf16 a[164:167], a[176:179], v[200:207], v240
X 160
v_smfmac_f32_16x16x64_bf16 a[168:171], a[176:179], v[208:215], v240
Dc 1,8,61,3072
v_smfmac_f32_16x16x64_bf16 a[172:175], a[176:179], v[216:223], v240
W 3
X 161
Cvaa 176,144,0,224
s_add_u32 m0, s62, 4096
X 162
Mvaa 176,148,4
s_sub_u32 s81, s57, 1
Ra 140,242,19456
K 192,160,161
Cvaa 180,148,20,228
X 163
Mvaa 180,144,16
s_lshl_b32 s85, s81, 5
Dc 243,8,61,0
Cvaa 184,144,32,232
X 164
Mvaa 184,148,36
s_add_u32 s85, s85, s56
K 193,162,163
Cvaa 188,148,52,236
X 165
Mvaa 188,144,48
Dc 243,8,61,1024
Ra 144,242,20480
Mvaa 176,152,8
s_lshl_b32 s85, s85, 1
X 166
Mvaa 176,156,12
Ra 148,242,21504
K 200,164,165
Mvaa 180,156,28
s_add_u32 s85, s85, s14
X 167
Mvaa 180,152,24
Dc 243,8,61,2048
Mvaa 184,152,40
s_sub_u32 s85, s85, 2
X 168
Mvaa 184,156,44
K 201,166,167
Mvaa 188,156,60
X 169
Mvaa 188,152,56
W 2
s_cmp_lt_u32 s85, 160
s_cselect_b64 s[20:21], 0, -1
Dc 243,8,61,3072
Mvav 32,128,196
s_add_u32 m0, s62, 8192
Ra 152,242,22528
Ra 156,242,23552
Mvav 64,128,204
s_mul_i32 s18, s85, 205
X 170
Mvav 96,128,212
K 208,168,169
Mvav 128,128,220
s_lshr_b32 s18, s18, 12
X 171
Mvav 132,132,220
Dc 244,8,61,0
Ra 128,242,8192
Mvav 100,132,212
X 172
Mvav 68,132,204
s_and_b32 s18, s18, 7
K 209,170,171
Mvav 36,132,196
X 173
Mvav 40,136,196
Dc 244,8,61,1024
v_lshrrev_b32 v4, 1, v1
Mvav 72,136,204
X 174
Mvav 104,136,212
Ra 132,242,9216
K 216,172,173
Mvav 136,136,220
X 175
Mvav 140,140,220
Dc 244,8,61,2048
v_min_u32 v5, 344, v4
Mvav 108,140,212
s_lshl_b32 s81, s18, 3
X 176
Mvav 76,140,204
Ra 136,242,10240
K 217,174,175
Mvav 44,140,196
W 3
X 177
Mvav 48,144,196
Dc 244,8,61,3072
v_mov_b32 v6, s81
Mvav 80,144,204
s_add_u32 m0, s62, 12288
X 178
Mvav 112,144,212
Ra 140,242,11264
K 194,176,177
Mvav 144,144,220
X 179
Mvav 148,148,220
buffer_load_dwordx2 v[8:9], v4, s[8:11], 0 offen offset:2560 sc1
Ra 144,242,12288
Mvav 116,148,212
X 180
Mvav 84,148,204
K 195,178,179
Mvav 52,148,196
X 181
Mvav 56,152,196
buffer_load_dwordx2 v[10:11], v5, s[8:11], 0 offen offset:3072 sc1
Ra 148,242,13312
Mvav 88,152,204
X 182
Mvav 120,152,212
K 202,180,181
Mvav 152,152,220
)"
R"(X 183
Mvav 156,156,220
Dc 245,8,61,0
Ra 152,242,14336
Mvav 124,156,212
X 184
Mvav 92,156,204
K 203,182,183
v_bfe_u32 v28, v1, 4, 4
Mvav 60,156,196
W 3
X 185
Cvaa 160,128,0,224
buffer_load_dwordx2 v[12:13], v6, s[8:11], 0 offen offset:3584 sc1
v_lshlrev_b32 v28, 3, v28
Mvaa 160,132,4
s_lshl_b32 s80, s56, 9
X 186
Cvaa 164,132,20,228
s_add_u32 s80, s80, 4480
Ra 156,242,15360
K 210,184,185
Mvaa 164,128,16
X 187
Cvaa 168,128,32,232
Dc 245,8,61,1024
Mvaa 168,132,36
X 188
Cvaa 172,132,52,236
K 211,186,187
Mvaa 172,128,48
X 189
Mvaa 160,136,8
Dc 245,8,61,2048
Ra 128,242,24576
Mvaa 160,140,12
X 190
Mvaa 164,140,28
Ra 132,242,25600
K 218,188,189
Mvaa 164,136,24
X 191
Mvaa 168,136,40
buffer_load_dwordx2 v[28:29], v28, s[8:11], s80 offen sc1
Mvaa 168,140,44
Dc 245,8,61,3072
s_add_u32 s61, s61, s63
s_xor_b32 s62, s62, 0x10000
s_add_u32 s64, s64, 1
Mvaa 172,140,60
X 160
Mvaa 172,136,56
W 2
Ra 136,242,26624
K 219,190,191
Cvaa 176,144,0,224
X 161
Mvaa 176,148,4
global_load_dword v3, v2, s[26:27]
v_add_u32 v2, s22, v2
Cvaa 180,148,20,228
Ra 140,242,27648
Mvaa 180,144,16
X 162
Cvaa 184,144,32,232
K 196,160,161
Mvaa 184,148,36
X 163
Cvaa 188,148,52,236
X 164
Mvaa 188,144,48
Ra 144,242,28672
K 197,162,163
Mvaa 176,152,8
X 165
Mvaa 176,156,12
Ra 148,242,29696
Mvaa 180,156,28
X 166
Mvaa 180,152,24
K 204,164,165
Mvaa 184,152,40
X 167
Mvaa 184,156,44
X 168
Mvaa 188,156,60
Mvaa 188,152,56
W 2
Ra 152,242,30720
K 205,166,167
Mvav 32,128,192
X 169
Mvav 64,128,200
Ra 156,242,31744
v_xor_b32 v242, 0x10000, v242
Mvav 96,128,208
X 170
Mvav 128,128,216
K 212,168,169
Mvav 132,132,216
X 171
Mvav 100,132,208
X 172
Mvav 68,132,200
Mvav 36,132,192
K 213,170,171
Mvav 40,136,192
X 173
Mvav 72,136,200
X 174
Mvav 104,136,208
Mvav 136,136,216
s_waitcnt vmcnt(2)
v_cmp_eq_u64 vcc, s[12:13], v[28:29]
s_cmp_eq_u64 vcc, -1
s_cbranch_scc0 .Lspin_70%=
.Lok_71%=:
v_cmp_eq_u64 vcc, s[12:13], v[8:9]
v_cmp_eq_u64_e64 s[86:87], s[12:13], v[10:11]
s_and_b64 vcc, vcc, s[86:87]
s_or_b64 vcc, vcc, s[20:21]
s_cmp_eq_u64 vcc, -1
s_cbranch_scc0 .Lspin_73%=
.Lok_74%=:
K 220,172,173
Mvav 140,140,216
X 175
Mvav 108,140,208
X 176
Mvav 76,140,200
s_waitcnt vmcnt(1) lgkmcnt(0)
s_barrier
Mvav 44,140,192
Ra 128,242,0
K 221,174,175
Mvav 48,144,192
X 177
Mvav 80,144,200
X 178
Mvav 112,144,208
Ra 132,242,1024
Mvav 144,144,216
K 198,176,177
Mvav 148,148,216
X 179
Mvav 116,148,208
X 180
Mvav 84,148,200
Ra 136,242,2048
Mvav 52,148,192
K 199,178,179
Mvav 56,152,192
X 181
Mvav 88,152,200
Ra 140,242,3072
Mvav 120,152,208
X 182
Mvav 152,152,216
K 206,180,181
Mvav 156,156,216
X 183
Mvav 124,156,208
Mvav 92,156,200
K 207,182,183
Mvav 60,156,192
s_mov_b64 exec, 0
s_cmp_eq_u32 s14, 2
s_cbranch_scc1 .Lh_4_2%=
s_branch .Lh_4_3%=
.Lhr_76%=:
s_mov_b64 exec, -1
W 0
Cvaa 160,128,0,224
Ra 144,242,4096
Ra 148,242,5120
Mvaa 160,132,4
s_add_u32 m0, s62, 0
Ra 152,242,6144
Cvaa 164,132,20,228
X 184
Mvaa 164,128,16
X 185
Cvaa 168,128,32,232
X 186
Mvaa 168,132,36
Ra 156,242,7168
K 214,184,185
Cvaa 172,132,52,236
Dc 1,8,61,0
Mvaa 172,128,48
X 187
Mvaa 160,136,8
X 188
Mvaa 160,140,12
Ra 128,242,16384
K 215,186,187
Mvaa 164,140,28
Dc 1,8,61,1024
Mvaa 164,136,24
X 189
Mvaa 168,136,40
X 190
Mvaa 168,140,44
Ra 132,242,17408
K 222,188,189
Mvaa 172,140,60
Dc 1,8,61,2048
Mvaa 172,136,56
X 191
v_smfmac_f32_16x16x64_bf16 a[160:163], a[176:179], v[192:199], v240
Ra 136,242,18432
K 223,190,191
v_smfmac_f32_16x16x64_bf16 a[164:167], a[176:179], v[200:207], v240
X 160
v_smfmac_f32_16x16x64_bf16 a[168:171], a[176:179], v[208:215], v240
Dc 1,8,61,3072
v_smfmac_f32_16x16x64_bf16 a[172:175], a[176:179], v[216:223], v240
W 3
X 161
Cvaa 176,144,0,224
s_add_u32 m0, s62, 4096
X 162
Mvaa 176,148,4
Ra 140,242,19456
K 192,160,161
Cvaa 180,148,20,228
X 163
Mvaa 180,144,16
Dc 243,8,61,0
Cvaa 184,144,32,232
X 164
Mvaa 184,148,36
K 193,162,163
Cvaa 188,148,52,236
X 165
Mvaa 188,144,48
Dc 243,8,61,1024
Ra 144,242,20480
Mvaa 176,152,8
X 166
Mvaa 176,156,12
Ra 148,242,21504
K 200,164,165
Mvaa 180,156,28
X 167
Mvaa 180,152,24
Dc 243,8,61,2048
Mvaa 184,152,40
X 168
Mvaa 184,156,44
K 201,166,167
Mvaa 188,156,60
X 169
Mvaa 188,152,56
W 2
Dc 243,8,61,3072
Ra 152,242,22528
Mvav 32,128,196
s_add_u32 m0, s62, 8192
X 170
Mvav 64,128,204
Ra 156,242,23552
K 208,168,169
Mvav 96,128,212
X 171
Mvav 128,128,220
Dc 244,8,61,0
Mvav 132,132,220
X 172
Mvav 100,132,212
Ra 128,242,8192
K 209,170,171
Mvav 68,132,204
X 173
Mvav 36,132,196
Dc 244,8,61,1024
Mvav 40,136,196
X 174
Mvav 72,136,204
Ra 132,242,9216
K 216,172,173
Mvav 104,136,212
X 175
Mvav 136,136,220
Dc 244,8,61,2048
Mvav 140,140,220
X 176
Mvav 108,140,212
Ra 136,242,10240
K 217,174,175
Mvav 76,140,204
X 177
Mvav 44,140,196
W 3
Dc 244,8,61,3072
Mvav 48,144,196
X 178
Mvav 80,144,204
s_add_u32 m0, s62, 12288
Ra 140,242,11264
Mvav 112,144,212
K 194,176,177
Mvav 144,144,220
X 179
Mvav 148,148,220
X 180
Mvav 116,148,212
Dc 245,8,61,0
Ra 144,242,12288
Mvav 84,148,204
K 195,178,179
Mvav 52,148,196
X 181
Mvav 56,152,196
X 182
Mvav 88,152,204
Dc 245,8,61,1024
Ra 148,242,13312
Mvav 120,152,212
v_xor_b32 v6, s12, v12
K 202,180,181
Mvav 152,152,220
X 183
Mvav 156,156,220
Ra 152,242,14336
v_and_b32 v6, 2047, v6
Mvav 124,156,212
Dc 245,8,61,2048
v_lshlrev_b32 v6, 3, v6
Mvav 92,156,204
X 184
Mvav 60,156,196
W 3
K 203,182,183
Cvaa 160,128,0,224
s_mov_b32 s81, 40960
X 185
Mvaa 160,132,4
Dc 245,8,61,3072
Ra 156,242,15360
Cvaa 164,132,20,228
s_add_u32 s61, s61, s63
X 186
Mvaa 164,128,16
K 210,184,185
Cvaa 168,128,32,232
X 187
Mvaa 168,132,36
buffer_load_dword v7, v6, s[8:11], s81 offen sc1
v_lshrrev_b32 v5, 7, v1
Cvaa 172,132,52,236
s_xor_b32 s62, s62, 0x10000
X 188
Mvaa 172,128,48
K 211,186,187
v_mul_u32_u24 v4, 20480, v5
Mvaa 160,136,8
X 189
Mvaa 160,140,12
s_add_u32 s64, s64, 1
Ra 128,242,24576
Ra 132,242,25600
Mvaa 164,140,28
X 190
Mvaa 164,136,24
v_and_b32 v6, 0x70, v1
K 218,188,189
Mvaa 168,136,40
X 191
Mvaa 168,140,44
v_add_u32 v4, v4, v6
Mvaa 172,140,60
X 160
Mvaa 172,136,56
W 2
v_add_u32 v5, 2, v5
K 219,190,191
Cvaa 176,144,0,224
X 161
Mvaa 176,148,4
Ra 136,242,26624
v_lshrrev_b32 v5, 3, v5
Cvaa 180,148,20,228
Ra 140,242,27648
v_lshl_or_b32 v5, v5, 31, v4
Mvaa 180,144,16
X 162
Cvaa 184,144,32,232
K 196,160,161
Mvaa 184,148,36
s_lshl_b32 s19, s85, 7
X 163
Cvaa 188,148,52,236
X 164
Mvaa 188,144,48
s_add_u32 s19, s19, 2752512
Ra 144,242,28672
K 197,162,163
Mvaa 176,152,8
.Lh_3_2_1%=:
)"
R"(s_cbranch_execz .Lhr_66%=
X 165
Mvaa 176,156,12
s_and_b32 s81, s20, 134217728
Ra 148,242,29696
Mvaa 180,156,28
X 166
Mvaa 180,152,24
K 204,164,165
Mvaa 184,152,40
s_or_b32 s19, s19, s81
X 167
Mvaa 184,156,44
X 168
Mvaa 188,156,60
buffer_load_dwordx4 a[64:67], v4, s[8:11], s19 offen sc1
Mvaa 188,152,56
W 2
Ra 152,242,30720
K 205,166,167
Mvav 32,128,192
s_add_u32 s19, s19, 163840
X 169
Mvav 64,128,200
Ra 156,242,31744
v_xor_b32 v242, 0x10000, v242
Mvav 96,128,208
X 170
Mvav 128,128,216
buffer_load_dwordx4 a[68:71], v4, s[8:11], s19 offen sc1
K 212,168,169
Mvav 132,132,216
X 171
Mvav 100,132,208
s_add_u32 s19, s19, 163840
X 172
Mvav 68,132,200
Mvav 36,132,192
buffer_load_dwordx4 a[72:75], v4, s[8:11], s19 offen sc1
K 213,170,171
Mvav 40,136,192
X 173
Mvav 72,136,200
X 174
Mvav 104,136,208
s_add_u32 s19, s19, 163840
Mvav 136,136,216
K 220,172,173
Mvav 140,140,216
X 175
Mvav 108,140,208
buffer_load_dwordx4 a[76:79], v4, s[8:11], s19 offen sc1
Mvav 76,140,200
s_waitcnt vmcnt(5) lgkmcnt(0)
s_barrier
Mvav 44,140,192
s_add_u32 s19, s19, 163840
X 176
Mvav 48,144,192
Ra 128,242,0
K 221,174,175
Mvav 80,144,200
X 177
Mvav 112,144,208
buffer_load_dwordx4 a[80:83], v4, s[8:11], s19 offen sc1
Ra 132,242,1024
Mvav 144,144,216
s_add_u32 s19, s19, 163840
X 178
Mvav 148,148,216
K 198,176,177
Mvav 116,148,208
X 179
Mvav 84,148,200
buffer_load_dwordx4 a[84:87], v4, s[8:11], s19 offen sc1
Ra 136,242,2048
Mvav 52,148,192
X 180
Mvav 56,152,192
s_add_u32 s19, s19, 163840
Ra 140,242,3072
K 199,178,179
Mvav 88,152,200
X 181
Mvav 120,152,208
X 182
Mvav 152,152,216
buffer_load_dwordx4 a[88:91], v5, s[8:11], s19 offen sc1
K 206,180,181
Mvav 156,156,216
X 183
Mvav 124,156,208
Mvav 92,156,200
K 207,182,183
Mvav 60,156,192
W 0
Cvaa 160,128,0,224
Ra 144,242,4096
Ra 148,242,5120
Mvaa 160,132,4
s_add_u32 m0, s62, 0
Ra 152,242,6144
Cvaa 164,132,20,228
X 184
Mvaa 164,128,16
X 185
Cvaa 168,128,32,232
X 186
Mvaa 168,132,36
Ra 156,242,7168
K 214,184,185
Cvaa 172,132,52,236
Dc 1,8,61,0
Mvaa 172,128,48
X 187
Mvaa 160,136,8
X 188
Mvaa 160,140,12
Ra 128,242,16384
K 215,186,187
Mvaa 164,140,28
Dc 1,8,61,1024
Mvaa 164,136,24
X 189
Mvaa 168,136,40
X 190
Mvaa 168,140,44
Ra 132,242,17408
K 222,188,189
Mvaa 172,140,60
Dc 1,8,61,2048
Mvaa 172,136,56
X 191
v_smfmac_f32_16x16x64_bf16 a[160:163], a[176:179], v[192:199], v240
Ra 136,242,18432
K 223,190,191
v_smfmac_f32_16x16x64_bf16 a[164:167], a[176:179], v[200:207], v240
X 160
v_smfmac_f32_16x16x64_bf16 a[168:171], a[176:179], v[208:215], v240
Dc 1,8,61,3072
v_smfmac_f32_16x16x64_bf16 a[172:175], a[176:179], v[216:223], v240
W 3
X 161
Cvaa 176,144,0,224
s_add_u32 m0, s62, 4096
X 162
Mvaa 176,148,4
Ra 140,242,19456
K 192,160,161
Cvaa 180,148,20,228
X 163
Mvaa 180,144,16
Dc 243,8,61,0
Cvaa 184,144,32,232
X 164
Mvaa 184,148,36
K 193,162,163
Cvaa 188,148,52,236
X 165
Mvaa 188,144,48
Dc 243,8,61,1024
Ra 144,242,20480
Mvaa 176,152,8
X 166
Mvaa 176,156,12
Ra 148,242,21504
K 200,164,165
Mvaa 180,156,28
X 167
Mvaa 180,152,24
Dc 243,8,61,2048
Mvaa 184,152,40
X 168
Mvaa 184,156,44
K 201,166,167
Mvaa 188,156,60
X 169
Mvaa 188,152,56
W 2
Dc 243,8,61,3072
Ra 152,242,22528
Mvav 32,128,196
s_add_u32 m0, s62, 8192
X 170
Mvav 64,128,204
Ra 156,242,23552
K 208,168,169
Mvav 96,128,212
X 171
Mvav 128,128,220
Dc 244,8,61,0
Mvav 132,132,220
X 172
Mvav 100,132,212
Ra 128,242,8192
K 209,170,171
Mvav 68,132,204
X 173
Mvav 36,132,196
Dc 244,8,61,1024
Mvav 40,136,196
X 174
Mvav 72,136,204
Ra 132,242,9216
K 216,172,173
Mvav 104,136,212
X 175
Mvav 136,136,220
Dc 244,8,61,2048
Mvav 140,140,220
s_bfe_u32 s81, s59, 0x10001
X 176
Mvav 108,140,212
s_lshl_b32 s82, s57, 1
Ra 136,242,10240
K 217,174,175
Mvav 76,140,204
s_add_u32 s81, s81, s82
X 177
Mvav 44,140,196
W 3
s_lshl_b32 s83, s81, 2
Dc 244,8,61,3072
Mvav 48,144,196
s_lshr_b32 s81, s59, 2
X 178
Mvav 80,144,204
s_add_u32 m0, s62, 12288
s_lshl_b32 s81, s81, 1
Ra 140,242,11264
Mvav 112,144,212
s_add_u32 s83, s83, s81
K 194,176,177
Mvav 144,144,220
s_and_b32 s81, s59, 1
X 179
Mvav 148,148,220
s_add_u32 s83, s83, s81
X 180
Mvav 116,148,212
s_and_b32 s81, s14, 1
Dc 245,8,61,0
Ra 144,242,12288
Mvav 84,148,204
s_mul_i32 s81, s81, 124
K 195,178,179
Mvav 52,148,196
s_xor_b32 s83, s83, s81
X 181
Mvav 56,152,196
s_mul_i32 s83, s83, 20480
X 182
Mvav 88,152,204
s_mul_i32 s81, s58, 320
Dc 245,8,61,1024
Ra 148,242,13312
Mvav 120,152,212
s_add_u32 s82, s83, s81
K 202,180,181
Mvav 152,152,220
X 183
Mvav 156,156,220
Ra 152,242,14336
v_bfe_u32 v246, v1, 4, 4
Mvav 124,156,212
Dc 245,8,61,2048
v_mul_u32_u24 v246, 1280, v246
Mvav 92,156,204
X 184
Mvav 60,156,196
W 3
v_lshrrev_b32 v247, 8, v1
K 203,182,183
Cvaa 160,128,0,224
v_mul_u32_u24 v247, 104, v247
v_add3_u32 v246, v246, v247, s82
Mvaa 160,132,4
X 185
Cvaa 164,132,20,228
Dc 245,8,61,3072
Ra 156,242,15360
Mvaa 164,128,16
s_add_u32 s61, s61, s63
X 186
Cvaa 168,128,32,232
K 210,184,185
Mvaa 168,132,36
X 187
Cvaa 172,132,52,236
s_xor_b32 s62, s62, 0x10000
X 188
Mvaa 172,128,48
global_load_dword v3, v246, s[16:17]
K 211,186,187
Mvaa 160,136,8
s_lshl_b32 s81, s81, 3
s_add_u32 s83, s83, s81
s_add_u32 s83, s83, 131072
Mvaa 160,140,12
s_add_u32 s64, s64, 1
v_lshrrev_b32 v246, 9, v1
v_mul_u32_u24 v246, 10240, v246
Mvaa 164,140,28
Ra 128,242,24576
v_and_b32 v247, 0x1f0, v1
Mvaa 164,136,24
X 189
Mvaa 168,136,40
Ra 132,242,25600
v_min_u32 v247, 304, v247
Mvaa 168,140,44
X 190
Mvaa 172,140,60
v_lshl_add_u32 v246, v247, 3, v246
K 218,188,189
Mvaa 172,136,56
W 2
buffer_load_dword v3, v246, s[8:11], s83 offen sc1
Ra 136,242,26624
Cvaa 176,144,0,224
X 191
Mvaa 176,148,4
X 160
Cvaa 180,148,20,228
Ra 140,242,27648
K 219,190,191
Mvaa 180,144,16
X 161
Cvaa 184,144,32,232
X 162
Mvaa 184,148,36
K 196,160,161
Cvaa 188,148,52,236
X 163
Mvaa 188,144,48
X 164
Mvaa 176,152,8
Ra 144,242,28672
K 197,162,163
Mvaa 176,156,12
X 165
Mvaa 180,156,28
Ra 148,242,29696
Mvaa 180,152,24
X 166
Mvaa 184,152,40
K 204,164,165
Mvaa 184,156,44
X 167
Mvaa 188,156,60
X 168
Mvaa 188,152,56
W 2
Ra 152,242,30720
K 205,166,167
Mvav 32,128,192
X 169
Mvav 64,128,200
Ra 156,242,31744
v_xor_b32 v242, 0x10000, v242
Mvav 96,128,208
X 170
Mvav 128,128,216
K 212,168,169
Mvav 132,132,216
X 171
Mvav 100,132,208
X 172
Mvav 68,132,200
Mvav 36,132,192
K 213,170,171
Mvav 40,136,192
X 173
Mvav 72,136,200
X 174
Mvav 104,136,208
Mvav 136,136,216
K 220,172,173
Mvav 140,140,216
X 175
Mvav 108,140,208
X 176
Mvav 76,140,200
s_waitcnt vmcnt(2) lgkmcnt(0)
s_barrier
Mvav 44,140,192
Ra 128,242,0
K 221,174,175
Mvav 48,144,192
X 177
Mvav 80,144,200
X 178
Mvav 112,144,208
Ra 132,242,1024
Mvav 144,144,216
K 198,176,177
Mvav 148,148,216
X 179
Mvav 116,148,208
X 180
)"
R"(Mvav 84,148,200
Ra 136,242,2048
Mvav 52,148,192
K 199,178,179
Mvav 56,152,192
X 181
Mvav 88,152,200
Ra 140,242,3072
Mvav 120,152,208
X 182
Mvav 152,152,216
K 206,180,181
Mvav 156,156,216
X 183
Mvav 124,156,208
Mvav 92,156,200
K 207,182,183
Mvav 60,156,192
W 0
Cvaa 160,128,0,224
Ra 144,242,4096
Ra 148,242,5120
Mvaa 160,132,4
s_add_u32 m0, s62, 0
Ra 152,242,6144
G 8,64
Cvaa 164,132,20,228
X 184
Mvaa 164,128,16
X 185
Cvaa 168,128,32,232
X 186
Mvaa 168,132,36
G 9,65
K 214,184,185
Cvaa 172,132,52,236
Ra 156,242,7168
Dc 1,8,61,0
Mvaa 172,128,48
X 187
Mvaa 160,136,8
X 188
Mvaa 160,140,12
Ra 128,242,16384
G 10,66
Mvaa 164,140,28
Dc 1,8,61,1024
K 215,186,187
Mvaa 164,136,24
X 189
Mvaa 168,136,40
Ra 132,242,17408
G 11,67
Mvaa 168,140,44
X 190
Mvaa 172,140,60
Dc 1,8,61,2048
K 222,188,189
Mvaa 172,136,56
X 191
v_smfmac_f32_16x16x64_bf16 a[160:163], a[176:179], v[192:199], v240
G 12,68
K 223,190,191
v_smfmac_f32_16x16x64_bf16 a[164:167], a[176:179], v[200:207], v240
Ra 136,242,18432
v_smfmac_f32_16x16x64_bf16 a[168:171], a[176:179], v[208:215], v240
Dc 1,8,61,3072
G 13,69
v_smfmac_f32_16x16x64_bf16 a[172:175], a[176:179], v[216:223], v240
W 3
X 160
Cvaa 176,144,0,224
s_add_u32 m0, s62, 4096
X 161
Mvaa 176,148,4
X 162
Cvaa 180,148,20,228
Ra 140,242,19456
K 192,160,161
Mvaa 180,144,16
Dc 243,8,61,0
G 14,70
Cvaa 184,144,32,232
X 163
Mvaa 184,148,36
X 164
Cvaa 188,148,52,236
G 15,71
K 193,162,163
Mvaa 188,144,48
Dc 243,8,61,1024
Ra 144,242,20480
Mvaa 176,152,8
X 165
Mvaa 176,156,12
X 166
Mvaa 180,156,28
Ra 148,242,21504
A 8,8,12
Mvaa 180,152,24
Dc 243,8,61,2048
K 200,164,165
Mvaa 184,152,40
X 167
Mvaa 184,156,44
A 9,9,13
Mvaa 188,156,60
X 168
Mvaa 188,152,56
W 2
Dc 243,8,61,3072
K 201,166,167
Mvav 32,128,196
s_add_u32 m0, s62, 8192
X 169
Mvav 64,128,204
Ra 152,242,22528
A 10,10,14
Mvav 96,128,212
X 170
Mvav 128,128,220
Ra 156,242,23552
K 208,168,169
Mvav 132,132,220
Dc 244,8,61,0
A 11,11,15
Mvav 100,132,212
X 171
Mvav 68,132,204
X 172
Mvav 36,132,196
Ra 128,242,8192
K 209,170,171
Mvav 40,136,196
Dc 244,8,61,1024
G 12,72
Mvav 72,136,204
X 173
Mvav 104,136,212
X 174
Mvav 136,136,220
Ra 132,242,9216
G 13,73
Mvav 140,140,220
Dc 244,8,61,2048
K 216,172,173
Mvav 108,140,212
X 175
Mvav 76,140,204
X 176
Mvav 44,140,196
W 2
Ra 136,242,10240
G 14,74
Mvav 48,144,196
Dc 244,8,61,3072
K 217,174,175
Mvav 80,144,204
s_add_u32 m0, s62, 12288
X 177
Mvav 112,144,212
Ra 140,242,11264
G 15,75
Mvav 144,144,220
X 178
Mvav 148,148,220
K 194,176,177
A 8,8,12
Mvav 116,148,212
X 179
Mvav 84,148,204
X 180
Mvav 52,148,196
Dc 245,8,61,0
Ra 144,242,12288
Mvav 56,152,196
K 195,178,179
A 9,9,13
Mvav 88,152,204
X 181
Mvav 120,152,212
X 182
Mvav 152,152,220
Dc 245,8,61,1024
Ra 148,242,13312
Mvav 156,156,220
K 202,180,181
A 10,10,14
Mvav 124,156,212
X 183
Mvav 92,156,204
X 184
Mvav 60,156,196
W 2
Dc 245,8,61,2048
Ra 152,242,14336
Cvaa 160,128,0,224
.Lh_4_2_1%=:
s_cbranch_execz .Lh_4_2_2%=
A 11,11,15
K 203,182,183
Mvaa 160,132,4
X 185
Cvaa 164,132,20,228
X 186
Mvaa 164,128,16
Dc 245,8,61,3072
Ra 156,242,15360
Cvaa 168,128,32,232
s_add_u32 s61, s61, s63
G 12,76
K 210,184,185
Mvaa 168,132,36
X 187
Cvaa 172,132,52,236
s_xor_b32 s62, s62, 0x10000
G 13,77
Mvaa 172,128,48
X 188
Mvaa 160,136,8
K 211,186,187
G 14,78
Mvaa 160,140,12
s_add_u32 s64, s64, 1
X 189
Mvaa 164,140,28
Ra 128,242,24576
Ra 132,242,25600
Mvaa 164,136,24
X 190
Mvaa 168,136,40
K 218,188,189
G 15,79
Mvaa 168,140,44
X 191
Mvaa 172,140,60
s_lshl_b32 s92, s58, 2
X 160
Mvaa 172,136,56
W 2
K 219,190,191
A 8,8,12
Cvaa 176,144,0,224
X 161
Mvaa 176,148,4
Ra 136,242,26624
Ra 140,242,27648
Cvaa 180,148,20,228
s_add_u32 s91, s92, s51
X 162
Mvaa 180,144,16
A 9,9,13
K 196,160,161
Cvaa 184,144,32,232
X 163
Mvaa 184,148,36
s_add_u32 s95, s92, 0
X 164
Cvaa 188,148,52,236
A 10,10,14
K 197,162,163
Mvaa 188,144,48
X 165
Mvaa 176,152,8
s_and_b32 s95, s95, 15
Ra 144,242,28672
A 11,11,15
Mvaa 176,156,12
Ra 148,242,29696
Mvaa 180,156,28
X 166
Mvaa 180,152,24
s_mul_i32 s95, s95, 40960
K 204,164,165
Mvaa 184,152,40
X 167
Mvaa 184,156,44
G 12,80
Mvaa 188,156,60
s_mul_i32 s71, s51, 0
X 168
Mvaa 188,152,56
W 2
K 205,166,167
G 13,81
Mvav 32,128,192
X 169
Mvav 64,128,200
s_add_u32 s71, s71, 133120
Ra 152,242,30720
Ra 156,242,31744
Mvav 96,128,208
X 170
Mvav 128,128,216
v_xor_b32 v242, 0x10000, v242
G 14,82
Mvav 132,132,216
s_add_u32 m0, s71, s48
K 212,168,169
Mvav 100,132,208
X 171
Mvav 68,132,200
X 172
Mvav 36,132,192
s_lshl2_add_u32 s94, s48, s95
G 15,83
K 213,170,171
Mvav 40,136,192
X 173
Mvav 72,136,200
X 174
Mvav 104,136,208
D 16,40,94,0
A 8,8,12
Mvav 136,136,216
K 220,172,173
Mvav 140,140,216
X 175
Mvav 108,140,208
A 9,9,13
Mvav 76,140,200
s_waitcnt vmcnt(1) lgkmcnt(0)
s_barrier
Mvav 44,140,192
D 17,40,94,1024
Ra 128,242,0
Mvav 48,144,192
X 176
Mvav 80,144,200
K 221,174,175
A 10,10,14
Mvav 112,144,208
X 177
Mvav 144,144,216
D 18,40,94,2048
Ra 132,242,1024
Mvav 148,148,216
X 178
Mvav 116,148,208
K 198,176,177
A 11,11,15
Mvav 84,148,200
X 179
Mvav 52,148,192
D 19,40,94,3072
Ra 136,242,2048
Mvav 56,152,192
s_add_u32 m0, m0, 4096
X 180
Mvav 88,152,200
G 12,84
K 199,178,179
Mvav 120,152,208
X 181
Mvav 152,152,216
Ra 140,242,3072
buffer_load_dwordx4 v20, s[40:43], s94 offen lds
Mvav 156,156,216
X 182
Mvav 124,156,208
K 206,180,181
Mvav 92,156,200
X 183
Mvav 60,156,192
K 207,182,183
W 0
Cvaa 160,128,0,224
Ra 144,242,4096
Ra 148,242,5120
Mvaa 160,132,4
s_add_u32 m0, s62, 0
Ra 152,242,6144
G 13,85
Cvaa 164,132,20,228
X 184
Mvaa 164,128,16
X 185
Cvaa 168,128,32,232
X 186
Mvaa 168,132,36
G 14,86
K 214,184,185
Cvaa 172,132,52,236
Ra 156,242,7168
Dc 1,8,61,0
Mvaa 172,128,48
X 187
Mvaa 160,136,8
X 188
Mvaa 160,140,12
Ra 128,242,16384
G 15,87
Mvaa 164,140,28
Dc 1,8,61,1024
K 215,186,187
Mvaa 164,136,24
X 189
Mvaa 168,136,40
Ra 132,242,17408
A 8,8,12
)"
R"(Mvaa 168,140,44
X 190
Mvaa 172,140,60
Dc 1,8,61,2048
K 222,188,189
Mvaa 172,136,56
X 191
v_smfmac_f32_16x16x64_bf16 a[160:163], a[176:179], v[192:199], v240
A 9,9,13
K 223,190,191
v_smfmac_f32_16x16x64_bf16 a[164:167], a[176:179], v[200:207], v240
Ra 136,242,18432
A 10,10,14
v_smfmac_f32_16x16x64_bf16 a[168:171], a[176:179], v[208:215], v240
Dc 1,8,61,3072
v_smfmac_f32_16x16x64_bf16 a[172:175], a[176:179], v[216:223], v240
W 3
X 160
Cvaa 176,144,0,224
s_add_u32 m0, s62, 4096
X 161
Mvaa 176,148,4
X 162
Cvaa 180,148,20,228
Ra 140,242,19456
A 11,11,15
Mvaa 180,144,16
Dc 243,8,61,0
K 192,160,161
Cvaa 184,144,32,232
X 163
Mvaa 184,148,36
X 164
Cvaa 188,148,52,236
G 12,88
K 193,162,163
Mvaa 188,144,48
Dc 243,8,61,1024
G 13,89
Mvaa 176,152,8
X 165
Mvaa 176,156,12
X 166
Mvaa 180,156,28
Ra 144,242,20480
Ra 148,242,21504
Mvaa 180,152,24
Dc 243,8,61,2048
K 200,164,165
Mvaa 184,152,40
X 167
Mvaa 184,156,44
G 14,90
Mvaa 188,156,60
X 168
Mvaa 188,152,56
W 2
Dc 243,8,61,3072
K 201,166,167
Mvav 32,128,196
s_add_u32 m0, s62, 8192
Ra 152,242,22528
G 15,91
Mvav 64,128,204
X 169
Mvav 96,128,212
X 170
Mvav 128,128,220
Ra 156,242,23552
A 8,8,12
Mvav 132,132,220
Dc 244,8,61,0
K 208,168,169
Mvav 100,132,212
X 171
Mvav 68,132,204
X 172
Mvav 36,132,196
Ra 128,242,8192
A 9,9,13
Mvav 40,136,196
Dc 244,8,61,1024
K 209,170,171
Mvav 72,136,204
X 173
Mvav 104,136,212
Ra 132,242,9216
A 10,10,14
Mvav 136,136,220
X 174
Mvav 140,140,220
Dc 244,8,61,2048
K 216,172,173
Mvav 108,140,212
X 175
Mvav 76,140,204
Ra 136,242,10240
A 11,11,15
Mvav 44,140,196
W 3
X 176
Mvav 48,144,196
Dc 244,8,61,3072
K 217,174,175
Mvav 80,144,204
s_add_u32 m0, s62, 12288
Ra 140,242,11264
v_add_f32_dpp v8, v8, v8 row_ror:8 row_mask:0xf bank_mask:0xf
Mvav 112,144,212
X 177
Mvav 144,144,220
X 178
Mvav 148,148,220
K 194,176,177
v_add_f32_dpp v9, v9, v9 row_ror:8 row_mask:0xf bank_mask:0xf
Mvav 116,148,212
X 179
Mvav 84,148,204
X 180
Mvav 52,148,196
Dc 245,8,61,0
Ra 144,242,12288
Mvav 56,152,196
v_add_f32_dpp v10, v10, v10 row_ror:8 row_mask:0xf bank_mask:0xf
K 195,178,179
Mvav 88,152,204
X 181
Mvav 120,152,212
X 182
Mvav 152,152,220
Dc 245,8,61,1024
v_add_f32_dpp v11, v11, v11 row_ror:8 row_mask:0xf bank_mask:0xf
Mvav 156,156,220
Ra 148,242,13312
K 202,180,181
Mvav 124,156,212
X 183
Mvav 92,156,204
Ra 152,242,14336
v_mov_b32 v12, v8
Mvav 60,156,196
W 3
Dc 245,8,61,2048
v_mov_b32 v13, v9
Cvaa 160,128,0,224
X 184
Mvaa 160,132,4
Ra 156,242,15360
K 203,182,183
Cvaa 164,132,20,228
X 185
Mvaa 164,128,16
Dc 245,8,61,3072
v_mov_b32 v14, v10
Cvaa 168,128,32,232
s_add_u32 s61, s61, s63
X 186
Mvaa 168,132,36
K 210,184,185
v_mov_b32 v15, v11
Cvaa 172,132,52,236
s_xor_b32 s62, s62, 0x10000
X 187
Mvaa 172,128,48
X 188
Mvaa 160,136,8
Ra 128,242,24576
K 211,186,187
Mvaa 160,140,12
s_add_u32 s64, s64, 1
X 189
Mvaa 164,140,28
X 190
Mvaa 164,136,24
Ra 132,242,25600
v_permlane16_swap_b32 v8, v12
Mvaa 168,136,40
K 218,188,189
v_permlane16_swap_b32 v9, v13
Mvaa 168,140,44
X 191
Mvaa 172,140,60
s_lshl_b32 s92, s58, 2
X 160
Mvaa 172,136,56
W 2
K 219,190,191
v_permlane16_swap_b32 v10, v14
Cvaa 176,144,0,224
X 161
Mvaa 176,148,4
Ra 136,242,26624
Ra 140,242,27648
Cvaa 180,148,20,228
s_add_u32 s91, s92, s51
X 162
Mvaa 180,144,16
K 196,160,161
v_permlane16_swap_b32 v11, v15
Cvaa 184,144,32,232
X 163
Mvaa 184,148,36
s_add_u32 s95, s92, 1
X 164
Cvaa 188,148,52,236
A 8,8,12
K 197,162,163
Mvaa 188,144,48
X 165
Mvaa 176,152,8
s_and_b32 s95, s95, 15
Ra 144,242,28672
Ra 148,242,29696
Mvaa 176,156,12
X 166
Mvaa 180,156,28
A 9,9,13
K 204,164,165
Mvaa 180,152,24
s_mul_i32 s95, s95, 40960
X 167
Mvaa 184,152,40
Mvaa 184,156,44
A 10,10,14
Mvaa 188,156,60
s_mul_i32 s71, s51, 0
X 168
Mvaa 188,152,56
W 2
Ra 152,242,30720
K 205,166,167
Mvav 32,128,192
X 169
Mvav 64,128,200
s_add_u32 s71, s71, 143360
Ra 156,242,31744
A 11,11,15
Mvav 96,128,208
X 170
Mvav 128,128,216
v_xor_b32 v242, 0x10000, v242
K 212,168,169
Mvav 132,132,216
s_add_u32 m0, s71, s48
v_mov_b32 v12, v8
Mvav 100,132,208
X 171
Mvav 68,132,200
X 172
Mvav 36,132,192
s_lshl2_add_u32 s94, s48, s95
v_mov_b32 v13, v9
K 213,170,171
Mvav 40,136,192
X 173
Mvav 72,136,200
X 174
Mvav 104,136,208
D 16,40,94,0
v_mov_b32 v14, v10
Mvav 136,136,216
K 220,172,173
Mvav 140,140,216
X 175
Mvav 108,140,208
v_mov_b32 v15, v11
Mvav 76,140,200
s_waitcnt vmcnt(1) lgkmcnt(0)
s_barrier
Mvav 44,140,192
D 17,40,94,1024
Ra 128,242,0
Mvav 48,144,192
X 176
Mvav 80,144,200
K 221,174,175
v_permlane32_swap_b32 v8, v12
Mvav 112,144,208
X 177
Mvav 144,144,216
D 18,40,94,2048
Ra 132,242,1024
Mvav 148,148,216
X 178
Mvav 116,148,208
K 198,176,177
v_permlane32_swap_b32 v9, v13
Mvav 84,148,200
X 179
Mvav 52,148,192
D 19,40,94,3072
Ra 136,242,2048
Mvav 56,152,192
s_add_u32 m0, m0, 4096
X 180
Mvav 88,152,200
K 199,178,179
v_permlane32_swap_b32 v10, v14
Mvav 120,152,208
X 181
Mvav 152,152,216
Ra 140,242,3072
buffer_load_dwordx4 v20, s[40:43], s94 offen lds
Mvav 156,156,216
X 182
Mvav 124,156,208
K 206,180,181
Mvav 92,156,200
X 183
Mvav 60,156,192
K 207,182,183
W 0
Cvaa 160,128,0,224
Ra 144,242,4096
Ra 148,242,5120
Mvaa 160,132,4
s_add_u32 m0, s62, 0
Ra 152,242,6144
v_permlane32_swap_b32 v11, v15
Cvaa 164,132,20,228
X 184
Mvaa 164,128,16
X 185
Cvaa 168,128,32,232
X 186
Mvaa 168,132,36
A 8,8,12
K 214,184,185
Cvaa 172,132,52,236
Ra 156,242,7168
Dc 1,8,61,0
Mvaa 172,128,48
X 187
Mvaa 160,136,8
Ra 128,242,16384
A 9,9,13
Mvaa 160,140,12
X 188
Mvaa 164,140,28
Dc 1,8,61,1024
K 215,186,187
Mvaa 164,136,24
X 189
Mvaa 168,136,40
Ra 132,242,17408
A 10,10,14
Mvaa 168,140,44
X 190
Mvaa 172,140,60
Dc 1,8,61,2048
K 222,188,189
Mvaa 172,136,56
A 11,11,15
v_smfmac_f32_16x16x64_bf16 a[160:163], a[176:179], v[192:199], v240
X 191
v_smfmac_f32_16x16x64_bf16 a[164:167], a[176:179], v[200:207], v240
K 223,190,191
v_and_b32 v7, 0xffff0000, v7
v_smfmac_f32_16x16x64_bf16 a[168:171], a[176:179], v[208:215], v240
Dc 1,8,61,3072
Ra 136,242,18432
v_smfmac_f32_16x16x64_bf16 a[172:175], a[176:179], v[216:223], v240
W 3
X 160
Cvaa 176,144,0,224
s_add_u32 m0, s62, 4096
X 161
Mvaa 176,148,4
X 162
Cvaa 180,148,20,228
Ra 140,242,19456
v_ashrrev_i32 v6, 31, v7
Mvaa 180,144,16
Dc 243,8,61,0
K 192,160,161
Cvaa 184,144,32,232
X 163
Mvaa 184,148,36
v_not_b32 v6, v6
Cvaa 188,148,52,236
X 164
Mvaa 188,144,48
Dc 243,8,61,1024
K 193,162,163
Mvaa 176,152,8
X 165
Mvaa 176,156,12
Ra 144,242,20480
v_or_b32 v6, 0x80000000, v6
Mvaa 180,156,28
X 166
Mvaa 180,152,24
Ra 148,242,21504
v_xor_b32 v7, v7, v6
Mvaa 184,152,40
Dc 243,8,61,2048
K 200,164,165
Mvaa 184,156,44
X 167
Mvaa 188,156,60
)"
R"(X 168
Mvaa 188,152,56
W 2
v_and_b32 v7, 0xffff0000, v7
K 201,166,167
Mvav 32,128,196
.Lh_4_3_1%=:
s_cbranch_execz .Lhr_76%=
Dc 243,8,61,3072
v_mul_f32 v7, 0xbfb8aa3b, v7
Mvav 64,128,204
s_add_u32 m0, s62, 8192
X 169
Mvav 96,128,212
Ra 152,242,22528
Ra 156,242,23552
Mvav 128,128,220
X 170
Mvav 132,132,220
Dc 244,8,61,0
K 208,168,169
Mvav 100,132,212
X 171
Mvav 68,132,204
X 7
Mvav 36,132,196
X 172
Mvav 40,136,196
Ra 128,242,8192
K 209,170,171
Mvav 72,136,204
X 173
Mvav 104,136,212
Dc 244,8,61,1024
Ra 132,242,9216
Mvav 136,136,220
X 174
Mvav 140,140,220
v_add_f32 v7, 1.0, v7
K 216,172,173
Mvav 108,140,212
X 175
Mvav 76,140,204
Dc 244,8,61,2048
Ra 136,242,10240
Mvav 44,140,196
W 3
v_rcp_f32 v7, v7
Mvav 48,144,196
X 176
Mvav 80,144,204
Ra 140,242,11264
K 217,174,175
Mvav 112,144,212
Dc 244,8,61,3072
K 7,7,7
Mvav 144,144,220
s_add_u32 m0, s62, 12288
X 177
Mvav 148,148,220
X 178
Mvav 116,148,212
Ra 144,242,12288
K 194,176,177
Mvav 84,148,204
X 179
Mvav 52,148,196
X 180
Mvav 56,152,196
Dc 245,8,61,0
v_lshlrev_b32 v7, 16, v7
Mvav 88,152,204
K 195,178,179
K 12,8,9
Mvav 120,152,212
X 181
Mvav 152,152,220
X 182
Mvav 156,156,220
Dc 245,8,61,1024
Ra 148,242,13312
Mvav 124,156,212
K 202,180,181
K 13,10,11
Mvav 92,156,204
X 183
Mvav 60,156,196
W 2
X 184
Cvaa 160,128,0,224
Dc 245,8,61,2048
Ra 152,242,14336
Mvaa 160,132,4
K 203,182,183
v_lshlrev_b32 v8, 16, v12
Cvaa 164,132,20,228
X 185
Mvaa 164,128,16
X 186
Cvaa 168,128,32,232
Dc 245,8,61,3072
Ra 156,242,15360
Mvaa 168,132,36
s_add_u32 s61, s61, s63
v_and_b32 v9, 0xffff0000, v12
K 210,184,185
Cvaa 172,132,52,236
s_xor_b32 s62, s62, 0x10000
X 187
Mvaa 172,128,48
X 188
Mvaa 160,136,8
v_lshlrev_b32 v10, 16, v13
K 211,186,187
Mvaa 160,140,12
s_add_u32 s64, s64, 1
X 189
Mvaa 164,140,28
Ra 128,242,24576
v_and_b32 v11, 0xffff0000, v13
Mvaa 164,136,24
X 190
Mvaa 168,136,40
Ra 132,242,25600
K 218,188,189
Mvaa 168,140,44
X 191
Mvaa 172,140,60
s_lshl_b32 s92, s58, 2
U 8,8,7
U 9,9,7
Mvaa 172,136,56
W 2
X 160
Cvaa 176,144,0,224
Ra 136,242,26624
K 219,190,191
Mvaa 176,148,4
X 161
Cvaa 180,148,20,228
s_add_u32 s91, s92, s51
Ra 140,242,27648
U 10,10,7
Mvaa 180,144,16
X 162
Cvaa 184,144,32,232
K 196,160,161
Mvaa 184,148,36
s_add_u32 s95, s92, 2
X 163
Cvaa 188,148,52,236
U 11,11,7
Mvaa 188,144,48
X 164
Mvaa 176,152,8
s_and_b32 s95, s95, 15
Ra 144,242,28672
K 197,162,163
Mvaa 176,156,12
X 165
Mvaa 180,156,28
Ra 148,242,29696
K 12,8,9
Mvaa 180,152,24
s_mul_i32 s95, s95, 40960
X 166
Mvaa 184,152,40
K 204,164,165
K 14,10,11
Mvaa 184,156,44
X 167
Mvaa 188,156,60
s_mul_i32 s71, s51, 0
X 168
Mvaa 188,152,56
W 2
v_mov_b32 v13, s13
K 205,166,167
Mvav 32,128,192
X 169
Mvav 64,128,200
s_add_u32 s71, s71, 153600
Ra 152,242,30720
Ra 156,242,31744
Mvav 96,128,208
X 170
Mvav 128,128,216
v_mov_b32 v15, s13
v_xor_b32 v242, 0x10000, v242
Mvav 132,132,216
s_add_u32 m0, s71, s48
K 212,168,169
v_lshrrev_b32 v6, 7, v1
Mvav 100,132,208
X 171
Mvav 68,132,200
X 172
Mvav 36,132,192
s_lshl2_add_u32 s94, s48, s95
K 213,170,171
Mvav 40,136,192
X 173
Mvav 72,136,200
v_lshl_or_b32 v6, v6, 28, v1
Mvav 104,136,208
X 174
Mvav 136,136,216
s_lshl_b32 s19, s85, 7
D 16,40,94,0
K 220,172,173
Mvav 140,140,216
X 175
Mvav 108,140,208
X 176
Mvav 76,140,200
s_waitcnt vmcnt(1) lgkmcnt(0)
s_barrier
Mvav 44,140,192
s_add_u32 s19, s19, 98304
D 17,40,94,1024
K 221,174,175
Mvav 48,144,192
Ra 128,242,0
Mvav 80,144,200
s_and_b32 s21, s20, 134217728
X 177
Mvav 112,144,208
X 178
Mvav 144,144,216
D 18,40,94,2048
Ra 132,242,1024
Mvav 148,148,216
s_or_b32 s19, s19, s21
K 198,176,177
Mvav 116,148,208
X 179
Mvav 84,148,200
X 180
Mvav 52,148,192
D 19,40,94,3072
Ra 136,242,2048
Mvav 56,152,192
s_add_u32 m0, m0, 4096
Ra 140,242,3072
K 199,178,179
Mvav 88,152,200
X 181
Mvav 120,152,208
X 182
Mvav 152,152,216
buffer_store_dwordx4 v[12:15], v6, s[8:11], s19 offen sc1
K 206,180,181
Mvav 156,156,216
X 183
Mvav 124,156,208
Mvav 92,156,200
K 207,182,183
Mvav 60,156,192
buffer_load_dwordx4 v20, s[40:43], s94 offen lds
.Lf1_36%=:
W 0
Cvaa 160,128,0,224
v_bfe_u32 v28, v1, 4, 2
v_lshlrev_b32 v28, 3, v28
Mvaa 160,132,4
Ra 144,242,4096
Ra 148,242,5120
Cvaa 164,132,20,228
Ra 152,242,6144
buffer_load_dwordx2 v[28:29], v28, s[8:11], s54 offen sc1
Mvaa 164,128,16
s_lshl_b32 s92, s58, 2
s_add_u32 s91, s92, s51
s_add_u32 s95, s91, 3
Cvaa 168,128,32,232
s_and_b32 s95, s95, 15
s_mul_i32 s95, s95, 40960
s_mul_i32 s71, s51, 10240
Mvaa 168,132,36
s_add_u32 s71, s71, 0
s_add_u32 m0, s71, s48
s_lshl2_add_u32 s94, s48, s95
Cvaa 172,132,52,236
X 184
Mvaa 172,128,48
X 185
Mvaa 160,136,8
X 186
Mvaa 160,140,12
Ra 156,242,7168
K 214,184,185
Mvaa 164,140,28
X 187
Mvaa 164,136,24
D 16,40,94,0
Ra 128,242,16384
Mvaa 168,136,40
s_add_u32 s95, s91, 5
s_and_b32 s95, s95, 15
s_mul_i32 s95, s95, 40960
Mvaa 168,140,44
s_mul_i32 s71, s51, 10240
s_add_u32 s71, s71, 20480
Ra 132,242,17408
Mvaa 172,140,60
X 188
Mvaa 172,136,56
D 17,40,94,1024
K 215,186,187
v_smfmac_f32_16x16x64_bf16 a[160:163], a[176:179], v[192:199], v240
X 189
v_smfmac_f32_16x16x64_bf16 a[164:167], a[176:179], v[200:207], v240
X 190
K 222,188,189
X 191
s_nop 0
K 223,190,191
v_smfmac_f32_16x16x64_bf16 a[168:171], a[176:179], v[208:215], v240
D 18,40,94,2048
Ra 136,242,18432
v_smfmac_f32_16x16x64_bf16 a[172:175], a[176:179], v[216:223], v240
W 3
X 160
Cvaa 176,144,0,224
X 161
Mvaa 176,148,4
X 162
Cvaa 180,148,20,228
D 19,40,94,3072
K 192,160,161
Mvaa 180,144,16
s_add_u32 m0, m0, 4096
X 163
Cvaa 184,144,32,232
X 164
Mvaa 184,148,36
Ra 140,242,19456
K 193,162,163
Cvaa 188,148,52,236
buffer_load_dwordx4 v20, s[40:43], s94 offen lds
Mvaa 188,144,48
s_add_u32 m0, s71, s48
s_lshl2_add_u32 s94, s48, s95
Ra 144,242,20480
Mvaa 176,152,8
X 165
Mvaa 176,156,12
X 166
Mvaa 180,156,28
D 16,40,94,0
K 200,164,165
Mvaa 180,152,24
X 167
Mvaa 184,152,40
X 172
Mvaa 184,156,44
D 17,40,94,1024
K 201,166,167
Mvaa 188,156,60
X 173
Mvaa 188,152,56
s_cmp_eq_u32 s60, 16
s_cbranch_scc1 .Lmk_77%=
.Lmkr_78%=:
W 1
Mvav 32,128,196
Ra 148,242,21504
D 18,40,94,2048
Mvav 64,128,204
X 174
Mvav 96,128,212
Ra 152,242,22528
K 216,172,173
Mvav 128,128,220
X 175
Mvav 132,132,220
s_mov_b32 s18, 40960
X 168
Mvav 100,132,212
D 19,40,94,3072
K 217,174,175
Mvav 68,132,204
s_add_u32 m0, m0, 4096
X 169
Mvav 36,132,196
s_mov_b32 s19, 73728
X 170
Mvav 40,136,196
s_bitcmp1_b32 s14, 0
s_cselect_b32 s18, 49152, s18
s_cselect_b32 s19, 98304, s19
Ra 156,242,23552
K 208,168,169
Mvav 72,136,204
s_cmp_lt_u32 s14, 2
s_cselect_b32 s20, s18, s19
s_cselect_b32 s21, s19, s18
X 171
Mvav 104,136,212
X 184
)"
R"(Mvav 136,136,220
buffer_load_dwordx4 v20, s[40:43], s94 offen lds
K 209,170,171
Mvav 140,140,220
s_waitcnt vmcnt(10)
v_cmp_eq_u64 vcc, s[12:13], v[28:29]
s_cmp_eq_u64 vcc, -1
s_cselect_b32 s46, 79691776, 0
s_add_u32 s81, s58, 0
s_and_b32 s81, s81, 3
Mvav 108,140,212
s_lshl_b32 s81, s81, 12
s_add_u32 s81, s81, s49
Ra 128,242,24576
Mvav 76,140,204
X 185
Mvav 44,140,196
W 1
X 186
Mvav 48,144,196
buffer_load_dwordx4 a[184:187], v1, s[44:47], s81 offen offset:0 sc1
K 210,184,185
Mvav 80,144,204
s_add_u32 s82, s81, 131072
X 187
Mvav 112,144,212
v_add_u32 v2, s20, v1
v_add_u32 v3, s21, v1
Mvav 144,144,220
s_lshl_b32 s18, s14, 9
X 188
Mvav 148,148,220
Ra 132,242,25600
K 211,186,187
Mvav 116,148,212
buffer_load_dwordx4 a[200:203], v1, s[44:47], s82 offen offset:0 sc1
Ra 136,242,26624
Mvav 84,148,204
X 189
Mvav 52,148,196
v_lshrrev_b32 v232, 1, v1
v_add_u32 v232, s18, v232
Mvav 56,152,196
X 190
Mvav 88,152,204
K 218,188,189
v_bfe_u32 v12, v1, 6, 2
Mvav 120,152,212
buffer_load_dwordx4 a[188:191], v1, s[44:47], s81 offen offset:1024 sc1
Ra 140,242,27648
Mvav 152,152,220
X 191
Mvav 156,156,220
v_lshrrev_b32 v13, 8, v1
v_bfe_u32 v14, v1, 4, 1
Mvav 124,156,212
X 176
Mvav 92,156,204
K 219,190,191
v_lshlrev_b32 v14, 4, v14
Mvav 60,156,196
X 177
Maav 168,180,208
X 178
K 194,176,177
X 179
X 180
K 195,178,179
Maav 172,180,216
X 181
X 182
K 202,180,181
X 183
s_nop 0
K 203,182,183
Maav 160,180,192
buffer_load_dwordx4 a[204:207], v1, s[44:47], s82 offen offset:1024 sc1
v_mov_b32 v15, 0x3f80
Maav 164,180,200
W 0
Ra 144,242,28672
v_lshlrev_b32 v15, v14, v15
Mvav 96,128,208
Ra 148,242,29696
v_bfe_u32 v14, v1, 5, 1
v_xor_b32 v27, v12, v13
Mvav 128,128,216
buffer_load_dwordx4 a[192:195], v1, s[44:47], s81 offen offset:2048 sc1
v_lshl_or_b32 v27, v27, 1, v14
v_cmp_eq_u32_e64 s[86:87], 0, v27
Mvav 132,132,216
Ra 152,242,30720
Ra 156,242,31744
v_mov_b32 v226, 0
Mvav 100,132,208
v_cndmask_b32_e64 v224, 0, v15, s[86:87]
v_mov_b32 v228, 0
v_mov_b32 v230, v224
Mvav 104,136,208
buffer_load_dwordx4 a[208:211], v1, s[44:47], s82 offen offset:2048 sc1
v_cmp_eq_u32_e64 s[86:87], 1, v27
v_mov_b32 v227, 0
Mvav 136,136,216
v_cndmask_b32_e64 v225, 0, v15, s[86:87]
v_mov_b32 v229, 0
v_mov_b32 v231, v225
Mvav 140,140,216
G 4,160
G 8,161
G 5,164
Mvav 108,140,208
W 0
buffer_load_dwordx4 a[196:199], v1, s[44:47], s81 offen offset:3072 sc1
G 9,165
G 6,168
Mvav 112,144,208
G 10,169
G 7,172
G 11,173
Mvav 144,144,216
A 4,4,8
A 5,5,9
A 6,6,10
Mvav 148,148,216
buffer_load_dwordx4 a[212:215], v1, s[44:47], s82 offen offset:3072 sc1
s_add_u32 s81, s58, 1
s_and_b32 s81, s81, 3
s_lshl_b32 s81, s81, 12
Mvav 116,148,208
s_add_u32 s81, s81, s49
A 7,7,11
ds_write_b64 v232, v[6:7] offset:57344
A 8,4,5
Mvav 120,152,208
A 9,6,7
A 8,8,9
v_cmp_ngt_f32_e64 s[86:87], s90, v8
s_lshl_b32 s19, s14, 2
Mvav 152,152,216
buffer_load_dwordx4 a[216:219], v1, s[44:47], s81 offen offset:0 sc1
s_add_u32 s82, s81, 131072
v_mov_b32 v9, s19
s_or_b64 s[88:89], s[88:89], s[86:87]
Mvav 156,156,216
s_and_b64 s[88:89], s[88:89], s[24:25]
s_cselect_b32 s18, 1, 0
v_mov_b32 v8, s18
ds_write_b32 v9, v8 offset:59392
v_xor_b32 v10, 1024, v232
Mvav 124,156,208
v_bfe_u32 v11, v1, 2, 4
K 96,96,97
K 97,98,99
Mvav 32,128,192
buffer_load_dwordx4 a[232:235], v1, s[44:47], s82 offen offset:0 sc1
K 98,100,101
K 99,102,103
Mvav 64,128,200
ds_write_b128 v2, v[96:99] offset:0
K 128,128,129
K 129,130,131
Mvav 68,132,200
K 130,132,133
K 131,134,135
ds_write_b128 v2, v[128:131] offset:4096
Mvav 36,132,192
buffer_load_dwordx4 a[220:223], v1, s[44:47], s81 offen offset:1024 sc1
K 104,104,105
K 105,106,107
Mvav 40,136,192
K 106,108,109
K 107,110,111
ds_write_b128 v2, v[104:107] offset:1024
Mvav 72,136,200
K 136,136,137
K 137,138,139
K 138,140,141
Mvav 76,140,200
buffer_load_dwordx4 a[236:239], v1, s[44:47], s82 offen offset:1024 sc1
K 139,142,143
ds_write_b128 v2, v[136:139] offset:5120
Mvav 44,140,192
K 112,112,113
K 113,114,115
K 114,116,117
Mvav 48,144,192
K 115,118,119
ds_write_b128 v2, v[112:115] offset:2048
K 144,144,145
Mvav 80,144,200
K 145,146,147
K 146,148,149
K 147,150,151
Mvav 84,148,200
ds_write_b128 v2, v[144:147] offset:6144
buffer_load_dwordx4 a[224:227], v1, s[44:47], s81 offen offset:2048 sc1
K 120,120,121
Mvav 52,148,192
K 121,122,123
K 122,124,125
K 123,126,127
Mvav 56,152,192
ds_write_b128 v2, v[120:123] offset:3072
K 152,152,153
K 153,154,155
Mvav 88,152,200
buffer_load_dwordx4 a[240:243], v1, s[44:47], s82 offen offset:2048 sc1
K 154,156,157
K 155,158,159
Mvav 92,156,200
.Lh_4_2_2%=:
s_cbranch_execz .Lhr_76%=
ds_write_b128 v2, v[152:155] offset:7168
Mvav 60,156,192
buffer_load_dwordx4 a[228:231], v1, s[44:47], s81 offen offset:3072 sc1
buffer_load_dwordx4 a[244:247], v1, s[44:47], s82 offen offset:3072 sc1
s_branch .LL_79%=
.p2align 12
.LL_79%=:
.Lend_39%=:
s_waitcnt vmcnt(8) lgkmcnt(0)
s_barrier
s_cmp_eq_u32 s46, 0
s_cbranch_scc1 .Llate_80%=
ds_read_b32 v11, v11 offset:59392
ds_read_b64 v[8:9], v10 offset:57344
Rv 176,3,0
Rv 180,3,1024
Rv 184,3,2048
Rv 188,3,3072
Rv 192,3,4096
Rv 196,3,5120
Rv 200,3,6144
Rv 204,3,7168
W 6
Mvvv 32,224,176
s_lshl_b32 s92, s58, 2
s_add_u32 s91, s92, s51
s_add_u32 s95, s91, 7
Mvvv 36,228,176
s_and_b32 s95, s95, 15
s_mul_i32 s95, s95, 40960
s_mul_i32 s71, s51, 20480
Mvvv 40,224,180
s_add_u32 s71, s71, 61440
s_add_u32 m0, s71, s48
s_lshl2_add_u32 s94, s48, s95
Mvvv 44,228,180
W 4
v_cmp_ne_u32 vcc, 0, v11
s_cmp_lg_u64 vcc, 0
s_cbranch_scc1 .Lredo%=
A 4,4,8
Mvvv 48,224,184
v_rcp_f32 v4, v4
Mvvv 52,228,184
Ta 128,25,10240
A 5,5,9
Mvvv 56,224,188
v_rcp_f32 v5, v5
Mvvv 60,228,188
W 3
Ta 130,25,15360
D 16,40,94,0
Mvvv 64,224,192
s_add_u32 s95, s91, 9
s_and_b32 s95, s95, 15
s_mul_i32 s95, s95, 40960
Mvvv 68,228,192
s_mul_i32 s71, s51, 10240
s_add_u32 s71, s71, 112640
s_lshl_b32 s93, s58, 12
Mvvv 72,224,196
s_add_u32 s93, s93, s50
U 32,32,4
U 33,33,4
Mvvv 76,228,196
Ta 132,25,20480
Ta 134,25,25600
W 4
Mvvv 80,224,200
U 34,34,4
U 35,35,4
Mvvv 84,228,200
U 36,36,4
U 37,37,4
Mvvv 88,224,204
Ta 136,26,10272
Ta 138,26,15392
Mvvv 92,228,204
Ta 140,26,20512
Ta 142,26,25632
W 4
Zvaa 96,128,184
U 38,38,4
U 39,39,4
Mvaa 96,132,188
K 32,32,33
K 33,34,35
Zvaa 104,132,204
D 17,40,94,1024
K 34,36,37
Mvaa 104,128,200
Ta 144,25,10304
Ta 146,25,15424
Ta 148,25,20544
Ta 150,25,25664
W 4
Zvaa 108,136,200
Ta 152,26,10336
K 35,38,39
Mvaa 108,140,204
Ta 154,26,15456
Ta 156,26,20576
Zvaa 100,140,188
buffer_store_dwordx4 v[32:35], v1, s[8:11], s93 offen offset:0 sc1
U 40,40,4
Mvaa 100,136,184
Ta 158,26,25696
W 4
Zvaa 112,144,184
U 41,41,4
U 42,42,4
Mvaa 112,148,188
U 43,43,4
U 44,44,4
Zvaa 120,148,204
U 45,45,4
U 46,46,4
Mvaa 120,144,200
Ta 128,25,10368
Ta 130,25,15488
Ta 132,25,20608
Ta 134,25,25728
W 4
Zvaa 124,152,200
U 47,47,4
K 40,40,41
Mvaa 124,156,204
D 18,40,94,2048
K 41,42,43
Zvaa 116,156,188
K 42,44,45
K 43,46,47
Mvaa 116,152,184
Ta 136,26,10400
Ta 138,26,15520
Ta 140,26,20640
Ta 142,26,25760
W 4
Zvaa 128,128,184
Ta 144,25,10432
Ta 146,25,15552
Mvaa 128,132,188
buffer_store_dwordx4 v[40:43], v1, s[8:11], s93 offen offset:1024 sc1
U 48,48,4
Zvaa 136,132,204
U 49,49,4
U 50,50,4
Mvaa 136,128,200
Ta 148,25,20672
Ta 150,25,25792
W 4
Zvaa 140,136,200
U 51,51,4
U 52,52,4
Mvaa 140,140,204
U 53,53,4
U 54,54,4
Zvaa 132,140,188
U 55,55,4
K 48,48,49
Mvaa 132,136,184
Ta 152,26,10464
Ta 154,26,15584
Ta 156,26,20704
Ta 158,26,25824
W 4
Zvaa 144,144,184
D 19,40,94,3072
K 49,50,51
Mvaa 144,148,188
s_add_u32 m0, m0, 4096
K 50,52,53
K 51,54,55
Zvaa 152,148,204
Ta 128,25,10496
Ta 130,25,15616
Mvaa 152,144,200
Ta 132,25,20736
Ta 134,25,25856
W 4
Zvaa 156,152,200
buffer_store_dwordx4 v[48:51], v1, s[8:11], s93 offen offset:2048 sc1
U 56,56,4
Mvaa 156,156,204
U 57,57,4
U 58,58,4
Zvaa 148,156,188
U 59,59,4
)"
R"(U 60,60,4
Mvaa 148,152,184
Ta 136,26,10528
Ta 138,26,15648
Ta 140,26,20768
Ta 142,26,25888
W 4
Zvaa 160,128,184
U 61,61,4
U 62,62,4
Mvaa 160,132,188
U 63,63,4
K 56,56,57
Zvaa 168,132,204
buffer_load_dwordx4 v20, s[40:43], s94 offen lds
K 57,58,59
Mvaa 168,128,200
Ta 144,25,30720
Ta 146,25,35840
Ta 148,21,0
Ta 150,21,5120
W 4
Zvaa 172,136,200
s_add_u32 m0, s71, s48
s_lshl2_add_u32 s94, s48, s95
K 58,60,61
Mvaa 172,140,204
Ta 152,26,30752
K 59,62,63
Zvaa 164,140,188
buffer_store_dwordx4 v[56:59], v1, s[8:11], s93 offen offset:3072 sc1
U 64,64,5
Mvaa 164,136,184
Ta 154,26,35872
Ta 156,22,32
Ta 158,22,5152
W 4
Mvaa 96,144,192
U 65,65,5
U 66,66,5
Mvaa 96,148,196
U 67,67,5
U 68,68,5
Mvaa 104,148,212
U 69,69,5
U 70,70,5
Mvaa 104,144,208
Ta 128,25,30784
Ta 130,25,35904
Ta 132,21,64
Ta 134,21,5184
W 4
Mvaa 108,152,208
s_lshl_b32 s66, s14, 1
U 71,71,5
K 64,64,65
Mvaa 108,156,212
s_add_u32 s66, s66, 0
D 16,40,94,0
K 65,66,67
Mvaa 100,156,196
s_cmp_eq_u32 s66, s59
s_cselect_b32 s66, 134217728, 0
K 66,68,69
K 67,70,71
Mvaa 100,152,192
Ta 136,26,30816
Ta 138,26,35936
Ta 140,22,96
Ta 142,22,5216
W 4
Mvaa 112,128,192
s_add_u32 s67, s93, 131072
Ta 144,25,30848
Ta 146,25,35968
Mvaa 112,132,196
s_add_u32 s28, s52, s66
buffer_store_dwordx4 v[64:67], v1, s[8:11], s67 offen offset:0 sc1
U 72,72,5
Mvaa 120,132,212
s_lshl_b32 s66, s14, 1
U 73,73,5
U 74,74,5
Mvaa 120,128,208
Ta 148,21,128
Ta 150,21,5248
W 4
Mvaa 124,136,208
s_add_u32 s66, s66, 1
U 75,75,5
U 76,76,5
Mvaa 124,140,212
s_cmp_eq_u32 s66, s59
s_cselect_b32 s66, 134217728, 0
U 77,77,5
U 78,78,5
Mvaa 116,140,196
s_add_u32 s29, s52, s66
U 79,79,5
K 72,72,73
Mvaa 116,136,192
Ta 152,26,30880
Ta 154,26,36000
Ta 156,22,160
Ta 158,22,5280
W 4
Mvaa 128,144,192
D 17,40,94,1024
K 73,74,75
Mvaa 128,148,196
K 74,76,77
K 75,78,79
Mvaa 136,148,212
Ta 128,25,30912
Ta 130,25,36032
Mvaa 136,144,208
Ta 132,21,192
Ta 134,21,5312
W 4
Mvaa 140,152,208
D 18,40,94,2048
D 19,40,94,3072
s_add_u32 m0, m0, 4096
s_nop 0
buffer_load_dwordx4 v20, s[40:43], s94 offen lds
Mvaa 140,156,212
s_add_u32 s81, s58, 2
s_and_b32 s81, s81, 3
s_lshl_b32 s81, s81, 12
Mvaa 132,156,196
s_add_u32 s81, s81, s49
Ta 136,26,30944
Ta 138,26,36064
Mvaa 132,152,192
Ta 140,22,224
Ta 142,22,5344
W 4
Mvaa 144,128,192
buffer_store_dwordx4 v[72:75], v1, s[8:11], s67 offen offset:1024 sc1
U 80,80,5
Mvaa 144,132,196
U 81,81,5
U 82,82,5
Mvaa 152,132,212
U 83,83,5
U 84,84,5
Mvaa 152,128,208
Ta 144,25,30976
Ta 146,25,36096
Ta 148,21,256
Ta 150,21,5376
W 4
Mvaa 156,136,208
U 85,85,5
U 86,86,5
Mvaa 156,140,212
U 87,87,5
K 80,80,81
Mvaa 148,140,196
K 81,82,83
K 82,84,85
Mvaa 148,136,192
Ta 152,26,31008
Ta 154,26,36128
Ta 156,22,288
Ta 158,22,5408
W 4
Mvaa 160,144,192
buffer_load_dwordx4 a[0:3], v1, s[8:11], s81 offen offset:0 sc1
K 83,86,87
Mvaa 160,148,196
s_add_u32 s82, s81, 131072
Ta 128,21,10240
Ta 130,21,15360
Mvaa 168,148,212
Ta 132,21,20480
Ta 134,21,25600
Mvaa 168,144,208
W 4
Ta 136,22,10272
Ta 138,22,15392
Mvaa 172,152,208
buffer_store_dwordx4 v[80:83], v1, s[8:11], s67 offen offset:2048 sc1
U 88,88,5
Mvaa 172,156,212
U 89,89,5
U 90,90,5
Mvaa 164,156,196
U 91,91,5
U 92,92,5
Mvaa 164,152,192
Ta 140,22,20512
Ta 142,22,25632
s_waitcnt vmcnt(22) lgkmcnt(4)
Mvaa 96,128,216
U 93,93,5
U 94,94,5
Mvaa 96,132,220
U 95,95,5
K 88,88,89
Mvaa 104,132,236
K 89,90,91
K 90,92,93
Mvaa 104,128,232
Ta 144,21,10304
Ta 146,21,15424
Ta 148,21,20544
Ta 150,21,25664
W 4
Mvaa 108,136,232
buffer_load_dwordx4 a[16:19], v1, s[8:11], s82 offen offset:0 sc1
K 91,94,95
Mvaa 108,140,236
Ta 152,22,10336
Ta 154,22,15456
Mvaa 100,140,220
Ta 156,22,20576
Ta 158,22,25696
Mvaa 100,136,216
W 4
Ta 128,21,10368
Ta 130,21,15488
Mvaa 112,144,216
buffer_store_dwordx4 v[88:91], v1, s[8:11], s67 offen offset:3072 sc1
Ta 132,21,20608
Mvaa 112,148,220
Ta 134,21,25728
Ta 136,22,10400
Mvaa 120,148,236
Ta 138,22,15520
Ta 140,22,20640
Mvaa 120,144,232
W 7
Ta 142,22,25760
Ta 144,21,10432
Mvaa 124,152,232
buffer_load_dwordx4 a[4:7], v1, s[8:11], s81 offen offset:1024 sc1
Ta 146,21,15552
Mvaa 124,156,236
Ta 148,21,20672
Ta 150,21,25792
Mvaa 116,156,220
Mvaa 116,152,216
W 8
Ta 152,22,10464
Ta 154,22,15584
Mvaa 128,128,216
buffer_load_dwordx4 a[20:23], v1, s[8:11], s82 offen offset:1024 sc1
Mvaa 128,132,220
Ta 156,22,20704
Ta 158,22,25824
Mvaa 136,132,236
Mvaa 136,128,232
W 8
Ta 128,21,10496
Ta 130,21,15616
Mvaa 140,136,232
s_waitcnt vmcnt(7)
s_barrier
Mvaa 140,140,236
buffer_load_dwordx4 a[8:11], v1, s[8:11], s81 offen offset:2048 sc1
Ta 132,21,20736
Mvaa 132,140,220
Ta 134,21,25856
Mvaa 132,136,216
W 8
Ta 136,22,10528
Ta 138,22,15648
Mvaa 144,144,216
Mvaa 144,148,220
buffer_load_dwordx4 a[24:27], v1, s[8:11], s82 offen offset:2048 sc1
Ta 140,22,20768
Mvaa 152,148,236
Ta 142,22,25888
Mvaa 152,144,232
W 8
Ta 144,21,30720
Ta 146,21,35840
Mvaa 156,152,232
Mvaa 156,156,236
buffer_load_dwordx4 a[12:15], v1, s[8:11], s81 offen offset:3072 sc1
Ta 148,23,0
Mvaa 148,156,220
Ta 150,23,5120
Mvaa 148,152,216
W 8
Ta 152,22,30752
Ta 154,22,35872
Mvaa 160,128,216
Mvaa 160,132,220
buffer_load_dwordx4 a[28:31], v1, s[8:11], s82 offen offset:3072 sc1
Ta 156,24,32
Mvaa 168,132,236
s_lshl_b32 s92, s58, 2
s_add_u32 s91, s92, s51
s_add_u32 s95, s91, 11
Mvaa 168,128,232
W 7
s_and_b32 s95, s95, 15
s_mul_i32 s95, s95, 40960
Mvaa 172,136,232
s_mul_i32 s71, s51, 10240
s_add_u32 s71, s71, 40960
s_add_u32 m0, s71, s48
Mvaa 172,140,236
s_lshl2_add_u32 s94, s48, s95
D 16,40,94,0
Ta 158,24,5152
Mvaa 164,140,220
s_add_u32 s95, s91, 13
s_and_b32 s95, s95, 15
s_mul_i32 s95, s95, 40960
Mvaa 164,136,216
W 4
s_mul_i32 s71, s51, 20480
s_add_u32 s71, s71, 71680
Mvaa 96,144,224
s_lshl_b32 s80, s58, 3
Ta 128,21,30784
Ta 130,21,35904
Mvaa 96,148,228
s_add_u32 s80, s80, s55
D 17,40,94,1024
Ta 132,23,64
Mvaa 104,148,244
Ta 134,23,5184
Ta 136,22,30816
Mvaa 104,144,240
W 5
Ta 138,22,35936
v_and_b32 v27, 1, v12
Mvaa 108,152,240
s_waitcnt vmcnt(8)
v_mov_b64 v[28:29], s[12:13]
s_mov_b64 exec, 1
buffer_store_dwordx2 v[28:29], off, s[8:11], s80 sc1
s_mov_b64 exec, -1
Ta 140,24,96
v_lshlrev_b32 v27, 1, v27
Mvaa 108,156,244
Ta 142,24,5216
v_xor_b32 v27, v27, v13
Mvaa 100,156,228
Ta 144,21,30848
v_and_b32 v12, 2, v12
Mvaa 100,152,224
W 5
Ta 146,21,35968
v_or_b32 v12, v12, v14
Mvaa 112,128,224
D 18,40,94,2048
v_lshl_or_b32 v12, v27, 2, v12
Mvaa 112,132,228
Ta 148,23,128
v_cmp_eq_u32_e64 s[86:87], 0, v12
Mvaa 120,132,244
Ta 150,23,5248
Ta 152,22,30880
Mvaa 120,128,240
W 5
v_cndmask_b32_e64 v4, 0, v15, s[86:87]
v_cmp_eq_u32_e64 s[86:87], 1, v12
Mvaa 124,136,240
D 19,40,94,3072
Ta 154,22,36000
Mvaa 124,140,244
s_add_u32 m0, m0, 4096
v_cndmask_b32_e64 v5, 0, v15, s[86:87]
v_cmp_eq_u32_e64 s[86:87], 2, v12
Mvaa 116,140,228
Ta 156,24,160
Ta 158,24,5280
Mvaa 116,136,224
W 4
v_cndmask_b32_e64 v6, 0, v15, s[86:87]
v_cmp_eq_u32_e64 s[86:87], 3, v12
Mvaa 128,144,224
buffer_load_dwordx4 v20, s[40:43], s94 offen lds
Ta 128,21,30912
Mvaa 128,148,228
s_add_u32 m0, s71, s48
s_lshl2_add_u32 s94, s48, s95
v_cndmask_b32_e64 v7, 0, v15, s[86:87]
Mvaa 136,148,244
Ta 130,21,36032
v_cmp_eq_u32_e64 s[86:87], 4, v12
Mvaa 136,144,240
Ta 132,23,192
Ta 134,23,5312
W 4
Mvaa 140,152,240
D 16,40,94,0
v_cndmask_b32_e64 v8, 0, v15, s[86:87]
Mvaa 140,156,244
s_add_u32 s95, s92, 15
s_and_b32 s95, s95, 15
s_mul_i32 s95, s95, 40960
Mvaa 132,156,228
s_mul_i32 s71, s51, 0
s_add_u32 s71, s71, 102400
)"
R"(v_cmp_eq_u32_e64 s[86:87], 5, v12
Mvaa 132,152,224
Ta 136,22,30944
Ta 138,22,36064
Ta 140,24,224
Ta 142,24,5344
W 4
Mvaa 144,128,224
D 17,40,94,1024
v_cndmask_b32_e64 v9, 0, v15, s[86:87]
Mvaa 144,132,228
Ta 144,21,30976
v_cmp_eq_u32_e64 s[86:87], 6, v12
Mvaa 152,132,244
Ta 146,21,36096
Ta 148,23,256
Mvaa 152,128,240
Ta 150,23,5376
W 4
Mvaa 156,136,240
D 18,40,94,2048
v_cndmask_b32_e64 v10, 0, v15, s[86:87]
Mvaa 156,140,244
Ta 152,22,31008
v_cmp_eq_u32_e64 s[86:87], 7, v12
Mvaa 148,140,228
Ta 154,22,36128
Ta 156,24,288
Mvaa 148,136,224
Ta 158,24,5408
W 4
Mvaa 160,144,224
D 19,40,94,3072
v_cndmask_b32_e64 v11, 0, v15, s[86:87]
Mvaa 160,148,228
s_add_u32 m0, m0, 4096
s_mov_b32 s20, s38
s_and_b32 s21, s39, 0xffff
Mvaa 168,148,244
s_mov_b32 s22, 2621440
s_mov_b32 s23, 0x20000
s_and_b32 s17, s17, 0xffff
Mvaa 168,144,240
Ta 128,23,20480
Ta 130,23,25600
Ta 132,23,51200
Ta 134,23,56320
W 4
Mvaa 172,152,240
s_mov_b32 s18, 2621440
s_mov_b32 s19, 0x20000
buffer_load_dwordx4 v20, s[40:43], s94 offen lds
Mvaa 172,156,244
s_add_u32 m0, s71, s48
s_lshl2_add_u32 s94, s48, s95
s_lshr_b32 s66, s15, 3
Mvaa 164,156,228
s_lshl_b32 s66, s66, 4
s_add_u32 s66, s66, s59
s_lshl_b32 s63, s66, 6
Mvaa 164,152,224
Ta 136,24,20512
Ta 138,24,25632
Ta 140,24,51232
Ta 142,24,56352
s_waitcnt vmcnt(15) lgkmcnt(4)
Mvaa 96,128,0
s_add_u32 s63, s63, 65536
s_mul_i32 s62, s66, 40960
D 16,40,94,0
Mvaa 96,132,4
s_add_u32 s62, s62, 56623104
s_lshl_b32 s66, s14, 13
s_add_u32 s78, s62, s66
Mvaa 104,132,20
s_add_u32 s79, s78, 327680
s_and_b32 s66, s14, 1
s_mul_i32 s66, s66, 327680
Mvaa 104,128,16
Ta 144,23,20544
Ta 146,23,25664
Ta 148,23,51264
Ta 150,23,56384
W 4
Mvaa 108,136,16
s_add_u32 s65, s62, s66
s_add_u32 s65, s65, 32768
D 17,40,94,1024
Mvaa 108,140,20
s_mov_b32 s64, 0
s_bfe_u32 s66, s59, 0x10001
s_lshl_b32 s67, s57, 1
Mvaa 100,140,4
s_add_u32 s66, s66, s67
s_lshl_b32 s61, s66, 2
s_lshr_b32 s66, s59, 2
Mvaa 100,136,0
Ta 152,24,20576
Ta 154,24,25696
Ta 156,24,51296
Ta 158,24,56416
W 4
Mvaa 112,144,0
s_lshl_b32 s66, s66, 1
s_add_u32 s61, s61, s66
D 18,40,94,2048
Mvaa 112,148,4
s_and_b32 s66, s59, 1
s_add_u32 s61, s61, s66
s_xor_b32 s67, s61, 0
Mvaa 120,148,20
s_mul_i32 s72, s67, 20480
s_mul_i32 s66, s58, 320
Ta 128,23,20608
Mvaa 120,144,16
Ta 130,23,25728
Ta 132,23,51328
Ta 134,23,56448
W 4
Mvaa 124,152,16
s_add_u32 s72, s72, s66
s_mul_i32 s75, s67, 20480
D 19,40,94,3072
Mvaa 124,156,20
s_add_u32 m0, m0, 4096
s_mul_i32 s66, s58, 2560
s_add_u32 s75, s75, s66
Mvaa 116,156,4
s_add_u32 s75, s75, 131072
s_xor_b32 s67, s61, 124
buffer_load_dwordx4 v20, s[40:43], s94 offen lds
Mvaa 116,152,0
Ta 136,24,20640
Ta 138,24,25760
Ta 140,24,51360
Ta 142,24,56480
W 4
Mvaa 128,128,0
s_add_u32 s81, s58, 3
s_and_b32 s81, s81, 3
s_lshl_b32 s81, s81, 12
Mvaa 128,132,4
s_add_u32 s81, s81, s49
s_mul_i32 s73, s67, 20480
s_mul_i32 s66, s58, 320
Mvaa 136,132,20
s_add_u32 s73, s73, s66
s_mul_i32 s76, s67, 20480
buffer_load_dwordx4 a[32:35], v1, s[8:11], s81 offen offset:0 sc1
Mvaa 136,128,16
Ta 144,23,20672
Ta 146,23,25792
Ta 148,23,51392
Ta 150,23,56512
W 4
Mvaa 140,136,16
s_waitcnt vmcnt(11)
s_barrier
Mvaa 140,140,20
s_add_u32 s82, s81, 131072
s_mul_i32 s66, s58, 2560
s_add_u32 s76, s76, s66
Mvaa 132,140,4
s_add_u32 s76, s76, 131072
s_bitcmp1_b32 s14, 0
s_cselect_b32 s74, s73, s72
s_cselect_b32 s77, s76, s75
buffer_load_dwordx4 a[48:51], v1, s[8:11], s82 offen offset:0 sc1
Mvaa 132,136,0
Ta 152,24,20704
Ta 154,24,25824
Ta 156,24,51424
Ta 158,24,56544
W 4
Mvaa 144,144,0
s_add_u32 s74, s74, 256
s_add_u32 s77, s77, 2048
s_lshl_b32 s66, s14, 6
Mvaa 144,148,4
s_lshl_b32 s67, s14, 9
s_add_u32 s72, s72, s66
s_add_u32 s75, s75, s67
Mvaa 152,148,20
s_add_u32 s73, s73, s66
buffer_load_dwordx4 a[36:39], v1, s[8:11], s81 offen offset:1024 sc1
Ta 128,23,20736
Mvaa 152,144,16
Ta 130,23,25856
Ta 132,23,51456
Ta 134,23,56576
W 4
Mvaa 156,152,16
s_add_u32 s76, s76, s67
v_add_u32 v19, 4096, v1
v_bfe_u32 v27, v1, 4, 3
Mvaa 156,156,20
Ta 136,24,20768
Ta 138,24,25888
Mvaa 148,156,4
buffer_load_dwordx4 a[52:55], v1, s[8:11], s82 offen offset:1024 sc1
v_lshlrev_b32 v27, 3, v27
Mvaa 148,152,0
Ta 140,24,51488
Ta 142,24,56608
W 4
Mvaa 160,128,0
Ta 144,25,0
Ta 146,25,5120
Mvaa 160,132,4
Ta 148,21,40960
v_bfe_u32 v18, v1, 7, 1
Mvaa 168,132,20
buffer_load_dwordx4 a[40:43], v1, s[8:11], s81 offen offset:2048 sc1
Ta 150,21,46080
Mvaa 168,128,16
W 4
Ta 152,26,32
v_lshl_add_u32 v18, v18, 9, v27
Mvaa 172,136,16
s_mov_b32 s71, 0
Ta 154,26,5152
Ta 156,22,40992
Mvaa 172,140,20
Ta 158,22,46112
v_bfe_u32 v20, v1, 4, 4
Mvaa 164,140,4
buffer_load_dwordx4 a[56:59], v1, s[8:11], s82 offen offset:2048 sc1
Ta 128,25,64
Mvaa 164,136,0
W 5
Ta 130,25,5184
v_mul_u32_u24 v20, 1280, v20
Mvaa 96,144,8
Ta 132,21,41024
Ta 134,21,46144
Mvaa 96,148,12
Ta 136,26,96
Ta 138,26,5216
Mvaa 104,148,28
buffer_load_dwordx4 a[44:47], v1, s[8:11], s81 offen offset:3072 sc1
v_lshrrev_b32 v12, 8, v1
Mvaa 104,144,24
W 6
Ta 140,22,41056
Ta 142,22,46176
Mvaa 108,152,24
Ta 144,25,128
v_lshl_add_u32 v20, v12, 4, v20
Mvaa 108,156,28
Ta 146,25,5248
v_bfe_u32 v15, v1, 7, 1
Mvaa 100,156,12
buffer_load_dwordx4 a[60:63], v1, s[8:11], s82 offen offset:3072 sc1
Ta 148,21,41088
Mvaa 100,152,8
W 7
Ta 150,21,46208
v_mul_u32_u24 v14, 10240, v15
Mvaa 112,128,8
Ta 152,26,160
Ta 154,26,5280
Mvaa 112,132,12
Ta 156,22,41120
v_lshrrev_b32 v15, 8, v1
Mvaa 120,132,28
Ta 158,22,46240
Mvaa 120,128,24
W 8
Ta 128,25,192
v_lshl_add_u32 v14, v15, 7, v14
Mvaa 124,136,24
Ta 130,25,5312
v_bfe_u32 v28, v1, 4, 2
Mvaa 124,140,28
Ta 132,21,41152
v_lshlrev_b32 v28, 3, v28
Mvaa 116,140,12
Ta 134,21,46272
v_and_b32 v15, 0x70, v1
Mvaa 116,136,8
W 8
buffer_load_dwordx2 v[28:29], v28, s[8:11], s55 offen sc1
Ta 136,26,224
Mvaa 128,144,8
Ta 138,26,5344
v_add_u32 v14, v14, v15
Mvaa 128,148,12
Ta 140,22,41184
Ta 142,22,46304
Mvaa 136,148,28
Mvaa 136,144,24
W 8
buffer_load_dwordx4 v[36:39], v20, s[16:19], s72 offen
Ta 144,25,256
Mvaa 140,152,24
s_waitcnt vmcnt(10)
s_barrier
Mvaa 140,156,28
Ta 146,25,5376
Ta 148,21,41216
Mvaa 132,156,12
Ta 150,21,46336
Mvaa 132,152,8
W 8
Ta 152,26,288
Ta 154,26,5408
Mvaa 144,128,8
Mvaa 144,132,12
buffer_load_dwordx4 v[44:47], v14, s[8:11], s75 offen sc1
Ta 156,22,41248
Mvaa 152,132,28
Ta 158,22,46368
Mvaa 152,128,24
W 8
Ta 128,21,51200
Ta 130,21,56320
Mvaa 156,136,24
Mvaa 156,140,28
Ta 132,23,10240
Ta 134,23,15360
Mvaa 148,140,12
Mvaa 148,136,8
W 8
Ta 136,23,30720
Ta 138,23,35840
Mvaa 160,144,8
buffer_load_dwordx4 v[52:55], v20, s[16:19], s73 offen
Mvaa 160,148,12
Ta 140,23,40960
Ta 142,23,46080
Mvaa 168,148,28
Mvaa 168,144,24
W 8
Ta 144,22,51232
Ta 146,22,56352
Mvaa 172,152,24
Mvaa 172,156,28
Ta 148,24,10272
Ta 150,24,15392
Mvaa 164,156,12
buffer_load_dwordx4 v[60:63], v14, s[8:11], s76 offen sc1
Mvaa 164,152,8
s_waitcnt vmcnt(9) lgkmcnt(8)
Ta 152,24,30752
Ta 154,24,35872
Mvaa 96,128,32
Mvaa 96,132,36
Ta 156,24,40992
Ta 158,24,46112
Mvaa 104,132,52
Mvaa 104,128,48
s_waitcnt vmcnt(5) lgkmcnt(8)
Ta 128,21,51264
Ta 130,21,56384
Mvaa 104,136,56
Mvaa 104,140,60
buffer_load_dwordx4 v[68:71], v20, s[16:19], s74 offen
Ta 132,23,10304
Mvaa 96,140,44
Ta 134,23,15424
Mvaa 96,136,40
W 8
Ta 136,23,30784
Ta 138,23,35904
Mvaa 100,144,32
Mvaa 100,148,36
Ta 140,23,41024
Ta 142,23,46144
Mvaa 108,148,52
Mvaa 108,144,48
W 8
Ta 144,22,51296
Ta 146,22,56416
Mvaa 108,152,56
buffer_load_dwordx4 v[76:79], v14, s[8:11], s77 offen sc1
Mvaa 108,156,60
Ta 148,24,10336
Ta 150,24,15456
Mvaa 100,156,44
s_waitcnt vmcnt(6)
v_cmp_eq_u64 vcc, s[12:13], v[28:29]
s_cmp_eq_u64 vcc, -1
s_cbranch_scc0 .Lspin_82%=
.Lok_83%=:
s_add_u32 s81, s58, 1
s_and_b32 s81, s81, 3
Mvaa 100,152,40
W 8
s_lshl_b32 s81, s81, 12
s_add_u32 s81, s81, s50
Mvaa 112,128,32
buffer_load_dwordx4 a[64:67], v1, s[8:11], s81 offen offset:0 sc1
Ta 152,24,30816
Mvaa 112,132,36
s_add_u32 s82, s81, 131072
Ta 154,24,35936
Ta 156,24,41056
Mvaa 120,132,52
Ta 158,24,46176
K 96,96,97
)"
R"(Mvaa 120,128,48
W 8
Ta 128,21,51328
K 97,98,99
Mvaa 120,136,56
buffer_load_dwordx4 a[80:83], v1, s[8:11], s82 offen offset:0 sc1
Ta 130,21,56448
Mvaa 120,140,60
Ta 132,23,10368
K 98,100,101
Mvaa 112,140,44
Ta 134,23,15488
Mvaa 112,136,40
W 8
Ta 136,23,30848
K 99,102,103
Mvaa 116,144,32
buffer_load_dwordx2 v[12:13], v27, s[8:11], 0 offen offset:3584 sc1
Ta 138,23,35968
Mvaa 116,148,36
s_add_u32 s83, s28, 0
Ta 140,23,41088
Ta 142,23,46208
Mvaa 124,148,52
Mvaa 124,144,48
W 8
Ta 144,22,51360
Ta 146,22,56480
Mvaa 124,152,56
buffer_load_dwordx4 a[68:71], v1, s[8:11], s81 offen offset:1024 sc1
Mvaa 124,156,60
Ta 148,24,10400
Ta 150,24,15520
Mvaa 116,156,44
Mvaa 116,152,40
W 8
Ta 152,24,30880
Ta 154,24,36000
Mvaa 128,128,32
buffer_load_dwordx4 a[84:87], v1, s[8:11], s82 offen offset:1024 sc1
Mvaa 128,132,36
Ta 156,24,41120
Ta 158,24,46240
Mvaa 136,132,52
Mvaa 136,128,48
W 8
Ta 128,21,51392
Ta 130,21,56512
Mvaa 136,136,56
buffer_store_dwordx4 v[96:99], v1, s[8:11], s83 offen sc1
K 104,104,105
Mvaa 136,140,60
K 105,106,107
K 106,108,109
Mvaa 128,140,44
Ta 132,23,10432
K 107,110,111
Mvaa 128,136,40
W 7
s_add_u32 s83, s29, 40960
Ta 134,23,15552
Mvaa 132,144,32
Ta 136,23,30912
Ta 138,23,36032
Mvaa 132,148,36
Ta 140,23,41152
Ta 142,23,46272
Mvaa 140,148,52
Mvaa 140,144,48
W 8
Ta 144,22,51424
Ta 146,22,56544
Mvaa 140,152,56
Mvaa 140,156,60
Ta 148,24,10464
Ta 150,24,15584
Mvaa 132,156,44
Mvaa 132,152,40
W 8
buffer_load_dwordx4 a[72:75], v1, s[8:11], s81 offen offset:2048 sc1
Ta 152,24,30944
Mvaa 144,128,32
Ta 154,24,36064
Mvaa 144,132,36
Ta 156,24,41184
Ta 158,24,46304
Mvaa 152,132,52
Mvaa 152,128,48
W 8
buffer_load_dwordx4 a[88:91], v1, s[8:11], s82 offen offset:2048 sc1
Ta 128,21,51456
Mvaa 152,136,56
Ta 130,21,56576
Mvaa 152,140,60
Ta 132,23,10496
Ta 134,23,15616
Mvaa 144,140,44
Mvaa 144,136,40
W 8
buffer_load_dwordx4 a[76:79], v1, s[8:11], s81 offen offset:3072 sc1
Ta 136,23,30976
Mvaa 148,144,32
Ta 138,23,36096
Mvaa 148,148,36
Ta 140,23,41216
Ta 142,23,46336
Mvaa 156,148,52
Mvaa 156,144,48
W 8
buffer_store_dwordx4 v[104:107], v1, s[8:11], s83 offen sc1
K 112,112,113
Mvaa 156,152,56
K 113,114,115
K 114,116,117
Mvaa 156,156,60
Ta 144,22,51488
K 115,118,119
Mvaa 148,156,44
s_add_u32 s83, s28, 8192
Ta 146,22,56608
Ta 148,24,10528
Mvaa 148,152,40
W 7
Ta 150,24,15648
Ta 152,24,31008
Mvaa 160,128,32
Ta 154,24,36128
Mvaa 160,132,36
Ta 156,24,41248
Ta 158,24,46368
Mvaa 168,132,52
Mvaa 168,128,48
W 8
Ta 128,25,10240
Ta 130,25,15360
Mvaa 168,136,56
Mvaa 168,140,60
Ta 132,25,20480
Ta 134,25,25600
Mvaa 160,140,44
buffer_load_dwordx4 a[92:95], v1, s[8:11], s82 offen offset:3072 sc1
Mvaa 160,136,40
W 8
Ta 136,26,10272
Ta 138,26,15392
Mvaa 164,144,32
Mvaa 164,148,36
Ta 140,26,20512
Ta 142,26,25632
Mvaa 172,148,52
buffer_store_dwordx4 v[112:115], v1, s[8:11], s83 offen sc1
K 120,120,121
Mvaa 172,144,48
W 8
K 121,122,123
K 122,124,125
Mvaa 172,152,56
Ta 144,25,10304
K 123,126,127
Mvaa 172,156,60
s_add_u32 s83, s29, 49152
Ta 146,25,15424
Ta 148,25,20544
Mvaa 164,156,44
Ta 150,25,25664
Mvaa 164,152,40
W 8
Ta 152,26,10336
Ta 154,26,15456
Zvav 176,128,32
Mvav 176,132,40
Ta 156,26,20576
Ta 158,26,25696
Zvav 184,132,72
Mvav 184,128,64
W 8
Ta 128,25,10368
Ta 130,25,15488
Zvav 188,136,64
Mvav 188,140,72
buffer_store_dwordx4 v[120:123], v1, s[8:11], s83 offen sc1
K 128,128,129
Zvav 180,140,40
K 129,130,131
K 130,132,133
Mvav 180,136,32
W 6
Ta 132,25,20608
K 131,134,135
Zvav 192,144,32
s_add_u32 s83, s28, 16384
Ta 134,25,25728
Ta 136,26,10400
Mvav 192,148,40
Ta 138,26,15520
Ta 140,26,20640
Zvav 200,148,72
Ta 142,26,25760
Mvav 200,144,64
W 8
Ta 144,25,10432
Ta 146,25,15552
Zvav 204,152,64
Mvav 204,156,72
Ta 148,25,20672
Ta 150,25,25792
Zvav 196,156,40
Mvav 196,152,32
W 8
Ta 152,26,10464
Ta 154,26,15584
Zvav 208,128,32
s_add_u32 s81, s58, 2
buffer_store_dwordx4 v[128:131], v1, s[8:11], s83 offen sc1
K 136,136,137
Mvav 208,132,40
s_and_b32 s81, s81, 3
K 137,138,139
K 138,140,141
Zvav 216,132,72
s_lshl_b32 s81, s81, 12
s_add_u32 s81, s81, s50
K 139,142,143
Mvav 216,128,64
W 6
s_add_u32 s83, s29, 57344
Ta 156,26,20704
Zvav 220,136,64
Ta 158,26,25824
Ta 128,25,10496
Mvav 220,140,72
Ta 130,25,15616
Ta 132,25,20736
Zvav 212,140,40
Ta 134,25,25856
Mvav 212,136,32
W 8
Ta 136,26,10528
Ta 138,26,15648
Zvav 224,144,32
Mvav 224,148,40
Ta 140,26,20768
Ta 142,26,25888
Zvav 232,148,72
Mvav 232,144,64
W 8
buffer_store_dwordx4 v[136:139], v1, s[8:11], s83 offen sc1
K 144,144,145
Zvav 236,152,64
K 145,146,147
K 146,148,149
Mvav 236,156,72
Ta 144,25,30720
K 147,150,151
Zvav 228,156,40
s_add_u32 s83, s28, 24576
Ta 146,25,35840
Ta 148,21,0
Mvav 228,152,32
W 7
Ta 150,21,5120
Ta 152,26,30752
Zvav 240,128,32
Ta 154,26,35872
Mvav 240,132,40
Ta 156,22,32
Ta 158,22,5152
Zvav 248,132,72
Mvav 248,128,64
W 8
Ta 128,25,30784
Ta 130,25,35904
Zvav 252,136,64
Mvav 252,140,72
Ta 132,21,64
Ta 134,21,5184
Zvav 244,140,40
buffer_store_dwordx4 v[144:147], v1, s[8:11], s83 offen sc1
K 152,152,153
Mvav 244,136,32
W 8
K 153,154,155
K 154,156,157
Mvav 176,144,48
Ta 136,26,30816
K 155,158,159
Mvav 176,148,56
s_add_u32 s83, s29, 65536
Ta 138,26,35936
Ta 140,22,96
Mvav 184,148,88
Ta 142,22,5216
Mvav 184,144,80
W 8
Ta 144,25,30848
Ta 146,25,35968
Mvav 188,152,80
Mvav 188,156,88
Ta 148,21,128
Ta 150,21,5248
Mvav 180,156,56
Mvav 180,152,48
W 8
Ta 152,26,30880
Ta 154,26,36000
Mvav 192,128,48
Mvav 192,132,56
buffer_load_dwordx4 a[96:99], v1, s[8:11], s81 offen offset:0 sc1
Ta 156,22,160
Mvav 200,132,88
s_add_u32 s82, s81, 131072
Ta 158,22,5280
Mvav 200,128,80
W 8
Ta 128,25,30912
Ta 130,25,36032
Mvav 204,136,80
Mvav 204,140,88
buffer_store_dwordx4 v[152:155], v1, s[8:11], s83 offen sc1
K 160,160,161
Mvav 196,140,56
K 161,162,163
K 162,164,165
Mvav 196,136,48
W 6
Ta 132,21,192
K 163,166,167
Mvav 208,144,48
s_add_u32 s83, s28, 32768
Ta 134,21,5312
Ta 136,26,30944
Mvav 208,148,56
Ta 138,26,36064
Ta 140,22,224
Mvav 216,148,88
Ta 142,22,5344
Mvav 216,144,80
W 8
Ta 144,25,30976
Ta 146,25,36096
Mvav 220,152,80
Mvav 220,156,88
Ta 148,21,256
Ta 150,21,5376
Mvav 212,156,56
Mvav 212,152,48
W 8
Ta 152,26,31008
Ta 154,26,36128
Mvav 224,128,48
buffer_load_dwordx4 a[112:115], v1, s[8:11], s82 offen offset:0 sc1
Mvav 224,132,56
Ta 156,22,288
Ta 158,22,5408
Mvav 232,132,88
Mvav 232,128,80
W 8
Ta 128,21,10240
Ta 130,21,15360
Mvav 236,136,80
buffer_load_dwordx4 a[100:103], v1, s[8:11], s81 offen offset:1024 sc1
Mvav 236,140,88
Ta 132,21,20480
Ta 134,21,25600
Mvav 228,140,56
Mvav 228,136,48
W 8
Ta 136,22,10272
Ta 138,22,15392
Mvav 240,144,48
buffer_load_dwordx4 a[116:119], v1, s[8:11], s82 offen offset:1024 sc1
Mvav 240,148,56
Ta 140,22,20512
Ta 142,22,25632
Mvav 248,148,88
Mvav 248,144,80
W 8
Ta 144,21,10304
Ta 146,21,15424
Mvav 252,152,80
buffer_store_dwordx4 v[160:163], v1, s[8:11], s83 offen sc1
K 168,168,169
Mvav 252,156,88
K 169,170,171
K 170,172,173
Mvav 244,156,56
Ta 148,21,20544
K 171,174,175
Mvav 244,152,48
s_waitcnt vmcnt(17) lgkmcnt(7)
s_add_u32 s83, s29, 73728
Ta 150,21,25664
Mvaa 176,128,64
Ta 152,22,10336
Ta 154,22,15456
Mvaa 176,132,68
Ta 156,22,20576
Ta 158,22,25696
Mvaa 184,132,84
Mvaa 184,128,80
)"
R"(W 8
Ta 128,21,10368
Ta 130,21,15488
Mvaa 188,136,80
Mvaa 188,140,84
Ta 132,21,20608
Ta 134,21,25728
Mvaa 180,140,68
Mvaa 180,136,64
W 8
buffer_load_dwordx4 a[104:107], v1, s[8:11], s81 offen offset:2048 sc1
Ta 136,22,10400
Mvaa 192,144,64
Ta 138,22,15520
Mvaa 192,148,68
Ta 140,22,20640
Ta 142,22,25760
Mvaa 200,148,84
Mvaa 200,144,80
W 8
buffer_load_dwordx4 a[120:123], v1, s[8:11], s82 offen offset:2048 sc1
Ta 144,21,10432
Mvaa 204,152,80
Ta 146,21,15552
Mvaa 204,156,84
Ta 148,21,20672
Ta 150,21,25792
Mvaa 196,156,68
Mvaa 196,152,64
W 8
buffer_load_dwordx4 a[108:111], v1, s[8:11], s81 offen offset:3072 sc1
Ta 152,22,10464
Mvaa 208,128,64
Ta 154,22,15584
Mvaa 208,132,68
Ta 156,22,20704
Ta 158,22,25824
Mvaa 216,132,84
Mvaa 216,128,80
W 8
buffer_store_dwordx4 v[168:171], v1, s[8:11], s83 offen sc1
Ta 128,21,10496
Mvaa 220,136,80
Ta 130,21,15616
Mvaa 220,140,84
Ta 132,21,20736
Ta 134,21,25856
Mvaa 212,140,68
Mvaa 212,136,64
W 8
Ta 136,22,10528
Ta 138,22,15648
Mvaa 224,144,64
Mvaa 224,148,68
Ta 140,22,20768
Ta 142,22,25888
Mvaa 232,148,84
Mvaa 232,144,80
W 8
Ta 144,21,30720
Ta 146,21,35840
Mvaa 236,152,80
Mvaa 236,156,84
Ta 148,23,0
Ta 150,23,5120
Mvaa 228,156,68
buffer_load_dwordx4 a[124:127], v1, s[8:11], s82 offen offset:3072 sc1
Mvaa 228,152,64
W 8
s_add_u32 s81, s58, 3
s_and_b32 s81, s81, 3
Mvaa 240,128,64
s_lshl_b32 s81, s81, 12
s_add_u32 s81, s81, s50
Ta 152,22,30752
Mvaa 240,132,68
Ta 154,22,35872
Ta 156,24,32
Mvaa 248,132,84
buffer_load_dwordx4 a[184:187], v1, s[8:11], s81 offen offset:0 sc1
Ta 158,24,5152
Mvaa 248,128,80
W 8
s_add_u32 s82, s81, 131072
Ta 128,21,30784
Mvaa 252,136,80
Ta 130,21,35904
Mvaa 252,140,84
Ta 132,23,64
Ta 134,23,5184
Mvaa 244,140,68
buffer_load_dwordx4 a[200:203], v1, s[8:11], s82 offen offset:0 sc1
Mvaa 244,136,64
s_waitcnt vmcnt(18) lgkmcnt(8)
Ta 136,22,30816
Ta 138,22,35936
Mvaa 176,144,72
Mvaa 176,148,76
Ta 140,24,96
Ta 142,24,5216
Mvaa 184,148,92
buffer_load_dwordx4 a[188:191], v1, s[8:11], s81 offen offset:1024 sc1
Mvaa 184,144,88
W 8
Ta 144,21,30848
Ta 146,21,35968
Mvaa 188,152,88
Mvaa 188,156,92
Ta 148,23,128
Ta 150,23,5248
Mvaa 180,156,76
buffer_load_dwordx4 a[204:207], v1, s[8:11], s82 offen offset:1024 sc1
Mvaa 180,152,72
W 8
Ta 152,22,30880
Ta 154,22,36000
Mvaa 192,128,72
Mvaa 192,132,76
Ta 156,24,160
Ta 158,24,5280
Mvaa 200,132,92
buffer_load_dwordx4 a[192:195], v1, s[8:11], s81 offen offset:2048 sc1
Mvaa 200,128,88
W 8
Ta 128,21,30912
Ta 130,21,36032
Mvaa 204,136,88
Mvaa 204,140,92
Ta 132,23,192
Ta 134,23,5312
Mvaa 196,140,76
buffer_load_dwordx4 a[208:211], v1, s[8:11], s82 offen offset:2048 sc1
Mvaa 196,136,72
W 8
Ta 136,22,30944
Ta 138,22,36064
Mvaa 208,144,72
Mvaa 208,148,76
Ta 140,24,224
Ta 142,24,5344
Mvaa 216,148,92
buffer_load_dwordx4 a[196:199], v1, s[8:11], s81 offen offset:3072 sc1
Mvaa 216,144,88
W 8
Ta 144,21,30976
Ta 146,21,36096
Mvaa 220,152,88
Mvaa 220,156,92
Ta 148,23,256
Ta 150,23,5376
Mvaa 212,156,76
buffer_load_dwordx4 a[212:215], v1, s[8:11], s82 offen offset:3072 sc1
Mvaa 212,152,72
W 8
Ta 152,22,31008
Ta 154,22,36128
Mvaa 224,128,72
Mvaa 224,132,76
Ta 156,24,288
Ta 158,24,5408
Mvaa 232,132,92
Mvaa 232,128,88
W 8
Ta 128,23,20480
Ta 130,23,25600
Mvaa 236,136,88
Mvaa 236,140,92
Ta 132,23,51200
Ta 134,23,56320
Mvaa 228,140,76
Mvaa 228,136,72
W 8
Ta 136,25,0
Ta 138,25,5120
Mvaa 240,144,72
Mvaa 240,148,76
Ta 140,21,40960
Ta 142,21,46080
Mvaa 248,148,92
Mvaa 248,144,88
W 8
Ta 144,21,51200
Ta 146,21,56320
Mvaa 252,152,88
Mvaa 252,156,92
Ta 148,23,10240
Ta 150,23,15360
Mvaa 244,156,76
Mvaa 244,152,72
s_waitcnt vmcnt(14) lgkmcnt(8)
Ta 152,23,30720
Ta 154,23,35840
Mvaa 176,128,96
s_waitcnt vmcnt(9)
v_lshlrev_b32 v2, 2, v1
v_mov_b64 v[28:29], s[12:13]
s_mov_b64 exec, 3
buffer_store_dwordx2 v[28:29], v2, s[8:11], s53 offen offset:0 sc1
s_mov_b64 exec, -1
Mvaa 176,132,100
Ta 156,23,40960
Ta 158,23,46080
Mvaa 184,132,116
Mvaa 184,128,112
s_waitcnt vmcnt(9) lgkmcnt(8)
Ta 128,24,20512
Ta 130,24,25632
Mvaa 184,136,120
Mvaa 184,140,124
v_xor_b32 v12, s12, v12
v_xor_b32 v13, s13, v13
v_lshrrev_b32 v3, 11, v12
v_or_b32 v13, v13, v3
v_cmp_eq_u32 vcc, 0, v13
s_cmp_eq_u64 vcc, -1
s_cbranch_scc0 .LL_87%=
.LL_88%=:
v_bfe_u32 v13, v12, 4, 7
s_xor_b32 s66, s61, 124
v_cmp_eq_u32 vcc, s61, v13
v_cmp_eq_u32_e64 s[86:87], s66, v13
s_or_b32 s68, vcc_lo, s86
s_and_b32 s68, s68, 255
s_cbranch_scc1 .Lup_85%=
.Lupr_86%=:
Ta 132,24,51232
Ta 134,24,56352
Mvaa 176,140,108
Mvaa 176,136,104
s_waitcnt vmcnt(5) lgkmcnt(8)
Ta 136,26,32
v_lshlrev_b32 v32, 16, v36
Mvaa 176,144,184
Ta 138,26,5152
v_and_b32 v33, 0xffff0000, v36
Mvaa 176,148,188
Ta 140,22,40992
v_lshlrev_b32 v34, 16, v37
Mvaa 184,148,204
Ta 142,22,46112
v_and_b32 v35, 0xffff0000, v37
Mvaa 184,144,200
s_waitcnt vmcnt(1) lgkmcnt(8)
Ta 144,22,51232
v_lshlrev_b32 v36, 16, v38
Mvaa 184,152,208
Ta 146,22,56352
v_and_b32 v37, 0xffff0000, v38
Mvaa 184,156,212
Ta 148,24,10272
v_lshlrev_b32 v38, 16, v39
Mvaa 176,156,196
Ta 150,24,15392
v_and_b32 v39, 0xffff0000, v39
Mvaa 176,152,192
W 8
Ta 152,24,30752
v_lshlrev_b32 v40, 16, v44
Mvaa 180,128,96
Ta 154,24,35872
v_and_b32 v41, 0xffff0000, v44
Mvaa 180,132,100
Ta 156,24,40992
v_lshlrev_b32 v42, 16, v45
Mvaa 188,132,116
Ta 158,24,46112
v_and_b32 v43, 0xffff0000, v45
Mvaa 188,128,112
W 8
Ta 128,23,20544
v_lshlrev_b32 v44, 16, v46
Mvaa 188,136,120
Ta 130,23,25664
v_and_b32 v45, 0xffff0000, v46
Mvaa 188,140,124
buffer_load_dwordx2 v[16:17], v18, s[8:11], s63 offen sc1
v_lshlrev_b32 v46, 16, v47
Mvaa 180,140,108
Ta 132,23,51264
Ta 134,23,56384
Mvaa 180,136,104
W 8
v_and_b32 v47, 0xffff0000, v47
v_lshlrev_b32 v48, 16, v52
Mvaa 180,144,184
Ta 136,25,64
Ta 138,25,5184
Mvaa 180,148,188
v_and_b32 v49, 0xffff0000, v52
v_lshlrev_b32 v50, 16, v53
Mvaa 188,148,204
Ta 140,21,41024
Ta 142,21,46144
Mvaa 188,144,200
W 8
v_and_b32 v51, 0xffff0000, v53
v_lshlrev_b32 v52, 16, v54
Mvaa 188,152,208
Ta 144,21,51264
Ta 146,21,56384
Mvaa 188,156,212
v_and_b32 v53, 0xffff0000, v54
v_lshlrev_b32 v54, 16, v55
Mvaa 180,156,196
Ta 148,23,10304
Ta 150,23,15424
Mvaa 180,152,192
W 8
v_and_b32 v55, 0xffff0000, v55
v_lshlrev_b32 v56, 16, v60
Mvaa 192,128,96
Ta 152,23,30784
Ta 154,23,35904
Mvaa 192,132,100
v_and_b32 v57, 0xffff0000, v60
v_lshlrev_b32 v58, 16, v61
Mvaa 200,132,116
Ta 156,23,41024
Ta 158,23,46144
Mvaa 200,128,112
W 8
v_and_b32 v59, 0xffff0000, v61
K 176,176,177
Mvaa 200,136,120
Ta 128,24,20576
v_lshlrev_b32 v60, 16, v62
Mvaa 200,140,124
Ta 130,24,25696
K 177,178,179
Mvaa 192,140,108
v_and_b32 v61, 0xffff0000, v62
v_lshlrev_b32 v62, 16, v63
Mvaa 192,136,104
W 6
Ta 132,24,51296
K 178,180,181
Mvaa 192,144,184
Ta 134,24,56416
v_and_b32 v63, 0xffff0000, v63
Mvaa 192,148,188
K 179,182,183
v_lshlrev_b32 v64, 16, v68
Mvaa 200,148,204
s_add_u32 s83, s28, 327680
Ta 136,26,96
Ta 138,26,5216
Mvaa 200,144,200
W 6
buffer_store_dwordx4 v[176:179], v1, s[8:11], s83 offen sc1
v_and_b32 v65, 0xffff0000, v68
Mvaa 200,152,208
K 184,184,185
K 185,186,187
Mvaa 200,156,212
K 186,188,189
K 187,190,191
Mvaa 192,156,196
s_add_u32 s83, s29, 368640
buffer_store_dwordx4 v[184:187], v1, s[8:11], s83 offen sc1
v_lshlrev_b32 v66, 16, v69
Mvaa 192,152,192
Ta 140,22,41056
Ta 142,22,46176
W 4
Mvaa 196,128,96
v_and_b32 v67, 0xffff0000, v69
v_lshlrev_b32 v68, 16, v70
Mvaa 196,132,100
Ta 144,22,51296
Ta 146,22,56416
Mvaa 204,132,116
v_and_b32 v69, 0xffff0000, v70
v_lshlrev_b32 v70, 16, v71
Mvaa 204,128,112
Ta 148,24,10336
Ta 150,24,15456
W 4
Mvaa 204,136,120
v_and_b32 v71, 0xffff0000, v71
v_lshlrev_b32 v72, 16, v76
Mvaa 204,140,124
Ta 152,24,30816
Ta 154,24,35936
Mvaa 196,140,108
v_and_b32 v73, 0xffff0000, v76
v_lshlrev_b32 v74, 16, v77
Mvaa 196,136,104
Ta 156,24,41056
Ta 158,24,46176
W 4
Mvaa 196,144,184
v_and_b32 v75, 0xffff0000, v77
v_lshlrev_b32 v76, 16, v78
Mvaa 196,148,188
Ta 128,23,20608
Ta 130,23,25728
Mvaa 204,148,204
v_and_b32 v77, 0xffff0000, v78
v_lshlrev_b32 v78, 16, v79
Mvaa 204,144,200
)"
R"(Ta 132,23,51328
Ta 134,23,56448
W 4
Mvaa 204,152,208
Ta 136,25,128
v_and_b32 v79, 0xffff0000, v79
Mvaa 204,156,212
Ta 138,25,5248
Ta 140,21,41088
Mvaa 196,156,196
Ta 142,21,46208
Ta 144,21,51328
Mvaa 196,152,192
W 5
Ta 146,21,56448
Ta 148,23,10368
Mvaa 208,128,96
s_waitcnt vmcnt(2)
v_cmp_eq_u64 vcc, s[12:13], v[16:17]
s_and_b32 s71, vcc_lo, 255
s_bitset0_b32 s71, s59
Ta 150,23,15488
Ta 152,23,30848
Mvaa 208,132,100
s_lshl_b32 s92, s14, 3
s_lshr_b32 s92, s71, s92
s_mov_b32 s91, 134217728
Mvaa 216,132,116
s_bitcmp1_b32 s71, 0
s_cselect_b32 s94, s78, s91
Ta 154,23,35968
Mvaa 216,128,112
W 6
Ta 156,23,41088
Ta 158,23,46208
Mvaa 216,136,120
Ta 128,24,20640
K 192,192,193
Mvaa 216,140,124
Ta 130,24,25760
K 193,194,195
Mvaa 208,140,108
buffer_load_dwordx4 a[0:3], v1, s[8:11], s94 offen offset:0 sc1
K 194,196,197
Mvaa 208,136,104
W 6
Ta 132,24,51360
Ta 134,24,56480
Mvaa 208,144,184
Ta 136,26,160
K 195,198,199
Mvaa 208,148,188
s_add_u32 s83, s28, 335872
Ta 138,26,5280
Ta 140,22,41120
Mvaa 216,148,204
buffer_store_dwordx4 v[192:195], v1, s[8:11], s83 offen sc1
K 200,200,201
Mvaa 216,144,200
W 7
K 201,202,203
K 202,204,205
Mvaa 216,152,208
Ta 142,22,46240
K 203,206,207
Mvaa 216,156,212
s_add_u32 s83, s29, 376832
Ta 144,22,51360
Ta 146,22,56480
Mvaa 208,156,196
buffer_store_dwordx4 v[200:203], v1, s[8:11], s83 offen sc1
Ta 148,24,10400
Mvaa 208,152,192
W 7
Ta 150,24,15520
Ta 152,24,30880
Mvaa 212,128,96
Ta 154,24,36000
Mvaa 212,132,100
Ta 156,24,41120
Ta 158,24,46240
Mvaa 220,132,116
s_bitcmp1_b32 s92, 0
s_cbranch_scc0 .LL_90%=
buffer_load_dwordx4 a[216:219], v1, s[8:11], s65 offen offset:0 sc1
.LL_90%=:
Mvaa 220,128,112
W 8
s_bitcmp1_b32 s71, 1
s_cselect_b32 s94, s78, s91
Mvaa 220,136,120
Ta 128,23,20672
Ta 130,23,25792
Mvaa 220,140,124
Ta 132,23,51392
Ta 134,23,56512
Mvaa 212,140,108
buffer_load_dwordx4 a[4:7], v1, s[8:11], s94 offen offset:1024 sc1
Mvaa 212,136,104
W 8
Ta 136,25,192
Ta 138,25,5312
Mvaa 212,144,184
Mvaa 212,148,188
Ta 140,21,41152
Ta 142,21,46272
Mvaa 220,148,204
s_bitcmp1_b32 s92, 1
s_cbranch_scc0 .LL_91%=
buffer_load_dwordx4 a[220:223], v1, s[8:11], s65 offen offset:1024 sc1
.LL_91%=:
Mvaa 220,144,200
W 8
s_bitcmp1_b32 s71, 2
s_cselect_b32 s94, s78, s91
Mvaa 220,152,208
Ta 144,21,51392
Ta 146,21,56512
Mvaa 220,156,212
Ta 148,23,10432
Ta 150,23,15552
Mvaa 212,156,196
buffer_load_dwordx4 a[8:11], v1, s[8:11], s94 offen offset:2048 sc1
Mvaa 212,152,192
W 8
Ta 152,23,30912
Ta 154,23,36032
Mvaa 224,128,96
Mvaa 224,132,100
Ta 156,23,41152
Ta 158,23,46272
Mvaa 232,132,116
s_bitcmp1_b32 s92, 2
s_cbranch_scc0 .LL_92%=
buffer_load_dwordx4 a[224:227], v1, s[8:11], s65 offen offset:2048 sc1
.LL_92%=:
K 208,208,209
Mvaa 232,128,112
W 8
s_bitcmp1_b32 s71, 3
s_cselect_b32 s94, s78, s91
Mvaa 232,136,120
Ta 128,24,20704
Ta 130,24,25824
Mvaa 232,140,124
K 209,210,211
K 210,212,213
Mvaa 224,140,108
Ta 132,24,51424
Ta 134,24,56544
Mvaa 224,136,104
W 8
buffer_load_dwordx4 a[12:15], v1, s[8:11], s94 offen offset:3072 sc1
K 211,214,215
Mvaa 224,144,184
s_add_u32 s83, s28, 344064
Ta 136,26,224
Ta 138,26,5344
Mvaa 224,148,188
Ta 140,22,41184
Ta 142,22,46304
Mvaa 232,148,204
Mvaa 232,144,200
W 8
buffer_store_dwordx4 v[208:211], v1, s[8:11], s83 offen sc1
K 216,216,217
Mvaa 232,152,208
K 217,218,219
K 218,220,221
Mvaa 232,156,212
Ta 144,22,51424
K 219,222,223
Mvaa 224,156,196
s_add_u32 s83, s29, 385024
buffer_store_dwordx4 v[216:219], v1, s[8:11], s83 offen sc1
Ta 146,22,56544
Mvaa 224,152,192
W 6
Ta 148,24,10464
Ta 150,24,15584
Mvaa 228,128,96
Ta 152,24,30944
Ta 154,24,36064
Mvaa 228,132,100
Ta 156,24,41184
Ta 158,24,46304
Mvaa 236,132,116
s_bitcmp1_b32 s92, 3
s_cbranch_scc0 .LL_93%=
buffer_load_dwordx4 a[228:231], v1, s[8:11], s65 offen offset:3072 sc1
.LL_93%=:
Mvaa 236,128,112
W 8
s_bitcmp1_b32 s71, 4
s_cselect_b32 s94, s78, s91
Mvaa 236,136,120
Ta 128,23,20736
Ta 130,23,25856
Mvaa 236,140,124
Ta 132,23,51456
Ta 134,23,56576
Mvaa 228,140,108
buffer_load_dwordx4 a[16:19], v19, s[8:11], s94 offen offset:0 sc1
Mvaa 228,136,104
W 8
Ta 136,25,256
Ta 138,25,5376
Mvaa 228,144,184
Mvaa 228,148,188
Ta 140,21,41216
Ta 142,21,46336
Mvaa 236,148,204
s_bitcmp1_b32 s92, 4
s_cbranch_scc0 .LL_94%=
buffer_load_dwordx4 a[232:235], v19, s[8:11], s65 offen offset:0 sc1
.LL_94%=:
Mvaa 236,144,200
W 8
s_bitcmp1_b32 s71, 5
s_cselect_b32 s94, s78, s91
Mvaa 236,152,208
Ta 144,21,51456
Ta 146,21,56576
Mvaa 236,156,212
Ta 148,23,10496
Ta 150,23,15616
Mvaa 228,156,196
buffer_load_dwordx4 a[20:23], v19, s[8:11], s94 offen offset:1024 sc1
Mvaa 228,152,192
W 8
Ta 152,23,30976
Ta 154,23,36096
Mvaa 240,128,96
Mvaa 240,132,100
Ta 156,23,41216
Ta 158,23,46336
Mvaa 248,132,116
s_bitcmp1_b32 s92, 5
s_cbranch_scc0 .LL_95%=
buffer_load_dwordx4 a[236:239], v19, s[8:11], s65 offen offset:1024 sc1
.LL_95%=:
K 224,224,225
Mvaa 248,128,112
W 8
s_bitcmp1_b32 s71, 6
s_cselect_b32 s94, s78, s91
Mvaa 248,136,120
Ta 128,24,20768
Ta 130,24,25888
Mvaa 248,140,124
K 225,226,227
K 226,228,229
Mvaa 240,140,108
Ta 132,24,51488
Ta 134,24,56608
Mvaa 240,136,104
W 8
Ta 136,26,288
K 227,230,231
Mvaa 240,144,184
s_add_u32 s83, s28, 352256
Ta 138,26,5408
buffer_load_dwordx4 a[24:27], v19, s[8:11], s94 offen offset:2048 sc1
Mvaa 240,148,188
Ta 140,22,41248
Ta 142,22,46368
Mvaa 248,148,204
Mvaa 248,144,200
W 8
Ta 144,22,51488
Ta 146,22,56608
Mvaa 248,152,208
buffer_store_dwordx4 v[224:227], v1, s[8:11], s83 offen sc1
K 232,232,233
Mvaa 248,156,212
K 233,234,235
K 234,236,237
Mvaa 240,156,196
K 235,238,239
s_add_u32 s83, s29, 393216
buffer_store_dwordx4 v[232:235], v1, s[8:11], s83 offen sc1
Mvaa 240,152,192
W 6
Ta 148,24,10528
Ta 150,24,15648
Mvaa 244,128,96
Ta 152,24,31008
Ta 154,24,36128
Mvaa 244,132,100
Ta 156,24,41248
Ta 158,24,46368
Mvaa 252,132,116
s_bitcmp1_b32 s92, 6
s_cbranch_scc0 .LL_96%=
buffer_load_dwordx4 a[240:243], v19, s[8:11], s65 offen offset:2048 sc1
.LL_96%=:
Mvaa 252,128,112
W 8
s_bitcmp1_b32 s71, 7
s_cselect_b32 s94, s78, s91
Mvaa 252,136,120
Mvaa 252,140,124
Mvaa 244,140,108
buffer_load_dwordx4 a[28:31], v19, s[8:11], s94 offen offset:3072 sc1
Mvaa 244,136,104
W 4
Mvaa 244,144,184
Mvaa 244,148,188
Mvaa 252,148,204
s_bitcmp1_b32 s92, 7
s_cbranch_scc0 .LL_97%=
buffer_load_dwordx4 a[244:247], v19, s[8:11], s65 offen offset:3072 sc1
.LL_97%=:
Mvaa 252,144,200
W 0
Mvaa 252,152,208
Mvaa 252,156,212
Mvaa 244,156,196
Mvaa 244,152,192
K 240,240,241
K 241,242,243
s_nop 5
K 242,244,245
K 243,246,247
s_add_u32 s83, s28, 360448
buffer_store_dwordx4 v[240:243], v1, s[8:11], s83 offen sc1
K 248,248,249
K 249,250,251
K 250,252,253
K 251,254,255
s_add_u32 s83, s29, 401408
buffer_store_dwordx4 v[248:251], v1, s[8:11], s83 offen sc1
s_branch .Ltend_81%=
.p2align 12
.Llate_80%=:
ds_read_b32 v11, v11 offset:59392
ds_read_b64 v[8:9], v10 offset:57344
Rv 176,3,0
Rv 180,3,1024
Rv 184,3,2048
Rv 188,3,3072
Rv 192,3,4096
Rv 196,3,5120
Rv 200,3,6144
Rv 204,3,7168
W 6
Mvvv 32,224,176
s_lshl_b32 s92, s58, 2
s_add_u32 s91, s92, s51
s_add_u32 s95, s91, 7
Mvvv 36,228,176
s_and_b32 s95, s95, 15
s_mul_i32 s95, s95, 40960
s_mul_i32 s71, s51, 20480
Mvvv 40,224,180
s_add_u32 s71, s71, 61440
s_add_u32 m0, s71, s48
s_lshl2_add_u32 s94, s48, s95
Mvvv 44,228,180
W 4
v_cmp_ne_u32 vcc, 0, v11
s_cmp_lg_u64 vcc, 0
s_cbranch_scc1 .Lredo%=
A 4,4,8
Mvvv 48,224,184
v_rcp_f32 v4, v4
Mvvv 52,228,184
Ta 128,25,10240
A 5,5,9
Mvvv 56,224,188
v_rcp_f32 v5, v5
Mvvv 60,228,188
W 3
Ta 130,25,15360
D 16,40,94,0
Mvvv 64,224,192
s_add_u32 s95, s91, 9
s_and_b32 s95, s95, 15
s_mul_i32 s95, s95, 40960
Mvvv 68,228,192
s_mul_i32 s71, s51, 10240
s_add_u32 s71, s71, 112640
s_lshl_b32 s93, s58, 12
Mvvv 72,224,196
s_add_u32 s93, s93, s50
U 32,32,4
U 33,33,4
Mvvv 76,228,196
Ta 132,25,20480
Ta 134,25,25600
W 4
Mvvv 80,224,200
U 34,34,4
U 35,35,4
Mvvv 84,228,200
U 36,36,4
U 37,37,4
Mvvv 88,224,204
U 38,38,4
U 39,39,4
)"
R"(K 32,32,33
K 33,34,35
K 34,36,37
K 35,38,39
buffer_store_dwordx4 v[32:35], v1, s[8:11], s93 offen offset:0 sc1
U 40,40,4
U 41,41,4
U 42,42,4
U 43,43,4
U 44,44,4
U 45,45,4
U 46,46,4
U 47,47,4
K 40,40,41
K 41,42,43
K 42,44,45
K 43,46,47
buffer_store_dwordx4 v[40:43], v1, s[8:11], s93 offen offset:1024 sc1
U 64,64,5
U 65,65,5
U 66,66,5
U 67,67,5
U 68,68,5
U 69,69,5
U 70,70,5
U 71,71,5
K 64,64,65
K 65,66,67
K 66,68,69
K 67,70,71
s_add_u32 s67, s93, 131072
buffer_store_dwordx4 v[64:67], v1, s[8:11], s67 offen offset:0 sc1
U 72,72,5
U 73,73,5
U 74,74,5
U 75,75,5
U 76,76,5
U 77,77,5
U 78,78,5
U 79,79,5
K 72,72,73
K 73,74,75
K 74,76,77
K 75,78,79
Mvvv 92,228,204
Ta 136,26,10272
Ta 138,26,15392
Ta 140,26,20512
Ta 142,26,25632
W 4
Zvav 176,128,32
Ta 144,25,10304
Ta 146,25,15424
Mvav 176,132,40
Ta 148,25,20544
Ta 150,25,25664
Zvav 184,132,72
D 17,40,94,1024
Ta 152,26,10336
Mvav 184,128,64
W 5
Ta 154,26,15456
Ta 156,26,20576
Zvav 188,136,64
Ta 158,26,25696
Ta 128,25,10368
Mvav 188,140,72
Ta 130,25,15488
Ta 132,25,20608
Zvav 180,140,40
D 18,40,94,2048
Ta 134,25,25728
Mvav 180,136,32
W 8
Ta 136,26,10400
Ta 138,26,15520
Zvav 192,144,32
Mvav 192,148,40
Ta 140,26,20640
Ta 142,26,25760
Zvav 200,148,72
D 19,40,94,3072
Mvav 200,144,64
W 8
s_add_u32 m0, m0, 4096
Ta 144,25,10432
Zvav 204,152,64
Ta 146,25,15552
Mvav 204,156,72
Ta 148,25,20672
Ta 150,25,25792
Zvav 196,156,40
buffer_load_dwordx4 v20, s[40:43], s94 offen lds
Mvav 196,152,32
W 8
s_add_u32 m0, s71, s48
s_lshl2_add_u32 s94, s48, s95
Zvav 208,128,32
Ta 152,26,10464
Ta 154,26,15584
Mvav 208,132,40
Ta 156,26,20704
Ta 158,26,25824
Zvav 216,132,72
D 16,40,94,0
v_bfe_u32 v28, v1, 4, 2
Mvav 216,128,64
W 8
Ta 128,25,10496
v_lshlrev_b32 v28, 3, v28
Zvav 220,136,64
Ta 130,25,15616
Mvav 220,140,72
Ta 132,25,20736
Ta 134,25,25856
Zvav 212,140,40
buffer_load_dwordx2 v[28:29], v28, s[8:11], s54 offen sc1
Mvav 212,136,32
W 8
Ta 136,26,10528
Ta 138,26,15648
Zvav 224,144,32
Mvav 224,148,40
Ta 140,26,20768
Ta 142,26,25888
Zvav 232,148,72
D 17,40,94,1024
Mvav 232,144,64
W 8
Ta 144,25,30720
Ta 146,25,35840
Zvav 236,152,64
Mvav 236,156,72
Ta 148,21,0
Ta 150,21,5120
Zvav 228,156,40
buffer_store_dwordx4 v[72:75], v1, s[8:11], s67 offen offset:1024 sc1
U 48,48,4
Mvav 228,152,32
W 8
U 49,49,4
U 50,50,4
Zvav 240,128,32
U 51,51,4
U 52,52,4
Mvav 240,132,40
U 53,53,4
U 54,54,4
Zvav 248,132,72
D 18,40,94,2048
U 55,55,4
Mvav 248,128,64
W 4
K 48,48,49
K 49,50,51
Zvav 252,136,64
K 50,52,53
K 51,54,55
Mvav 252,140,72
Ta 152,26,30752
Ta 154,26,35872
Zvav 244,140,40
buffer_store_dwordx4 v[48:51], v1, s[8:11], s93 offen offset:2048 sc1
U 56,56,4
U 57,57,4
U 58,58,4
U 59,59,4
U 60,60,4
U 61,61,4
U 62,62,4
U 63,63,4
K 56,56,57
K 57,58,59
K 58,60,61
K 59,62,63
buffer_store_dwordx4 v[56:59], v1, s[8:11], s93 offen offset:3072 sc1
U 80,80,5
U 81,81,5
U 82,82,5
U 83,83,5
U 84,84,5
U 85,85,5
U 86,86,5
U 87,87,5
K 80,80,81
K 81,82,83
K 82,84,85
K 83,86,87
buffer_store_dwordx4 v[80:83], v1, s[8:11], s67 offen offset:2048 sc1
U 88,88,5
U 89,89,5
U 90,90,5
U 91,91,5
U 92,92,5
U 93,93,5
U 94,94,5
U 95,95,5
K 88,88,89
K 89,90,91
K 90,92,93
K 91,94,95
Mvav 244,136,32
Ta 156,22,32
Ta 158,22,5152
W 4
Mvav 176,144,48
Ta 128,25,30784
Ta 130,25,35904
Mvav 176,148,56
Ta 132,21,64
Ta 134,21,5184
Mvav 184,148,88
D 19,40,94,3072
Ta 136,26,30816
Mvav 184,144,80
s_add_u32 m0, m0, 4096
s_nop 0
buffer_load_dwordx4 v20, s[40:43], s94 offen lds
s_waitcnt vmcnt(8)
v_cmp_eq_u64 vcc, s[12:13], v[28:29]
s_cmp_eq_u64 vcc, -1
s_cbranch_scc0 .Lspin_98%=
.Lok_99%=:
W 5
Mvav 188,152,80
s_add_u32 s81, s58, 0
s_and_b32 s81, s81, 3
s_lshl_b32 s81, s81, 12
Mvav 188,156,88
s_add_u32 s81, s81, s49
Ta 138,26,35936
Ta 140,22,96
Mvav 180,156,56
s_lshl_b32 s66, s14, 1
Ta 142,22,5216
Ta 144,25,30848
Mvav 180,152,48
W 5
buffer_store_dwordx4 v[88:91], v1, s[8:11], s67 offen offset:3072 sc1
Ta 146,25,35968
Mvav 192,128,48
s_add_u32 s66, s66, 0
Ta 148,21,128
Ta 150,21,5248
Mvav 192,132,56
s_cmp_eq_u32 s66, s59
s_cselect_b32 s66, 134217728, 0
Ta 152,26,30880
Ta 154,26,36000
Mvav 200,132,88
s_add_u32 s28, s52, s66
Ta 156,22,160
Ta 158,22,5280
Mvav 200,128,80
W 8
s_lshl_b32 s66, s14, 1
buffer_load_dwordx4 a[184:187], v1, s[8:11], s81 offen offset:0 sc1
Mvav 204,136,80
s_add_u32 s82, s81, 131072
Ta 128,25,30912
Ta 130,25,36032
Mvav 204,140,88
s_add_u32 s66, s66, 1
s_cmp_eq_u32 s66, s59
s_cselect_b32 s66, 134217728, 0
Ta 132,21,192
Mvav 196,140,56
s_add_u32 s29, s52, s66
Ta 134,21,5312
Mvav 196,136,48
W 8
buffer_load_dwordx4 a[200:203], v1, s[8:11], s82 offen offset:0 sc1
Ta 136,26,30944
Mvav 208,144,48
Ta 138,26,36064
Mvav 208,148,56
Ta 140,22,224
Ta 142,22,5344
Mvav 216,148,88
Mvav 216,144,80
W 8
buffer_load_dwordx4 a[188:191], v1, s[8:11], s81 offen offset:1024 sc1
Ta 144,25,30976
Mvav 220,152,80
Ta 146,25,36096
Mvav 220,156,88
Ta 148,21,256
Ta 150,21,5376
Mvav 212,156,56
Mvav 212,152,48
W 8
buffer_load_dwordx4 a[204:207], v1, s[8:11], s82 offen offset:1024 sc1
Ta 152,26,31008
Mvav 224,128,48
Ta 154,26,36128
Mvav 224,132,56
Ta 156,22,288
Ta 158,22,5408
Mvav 232,132,88
Mvav 232,128,80
W 8
buffer_load_dwordx4 a[192:195], v1, s[8:11], s81 offen offset:2048 sc1
Ta 128,25,10240
Mvav 236,136,80
Ta 130,25,15360
Mvav 236,140,88
Ta 132,25,20480
Ta 134,25,25600
Mvav 228,140,56
Mvav 228,136,48
W 8
buffer_load_dwordx4 a[208:211], v1, s[8:11], s82 offen offset:2048 sc1
Ta 136,26,10272
Mvav 240,144,48
Ta 138,26,15392
Mvav 240,148,56
Ta 140,26,20512
Ta 142,26,25632
Mvav 248,148,88
Mvav 248,144,80
W 8
buffer_load_dwordx4 a[196:199], v1, s[8:11], s81 offen offset:3072 sc1
Ta 144,25,10304
Mvav 252,152,80
Ta 146,25,15424
Mvav 252,156,88
Ta 148,25,20544
Ta 150,25,25664
Mvav 244,156,56
Mvav 244,152,48
s_waitcnt vmcnt(3) lgkmcnt(8)
buffer_load_dwordx4 a[212:215], v1, s[8:11], s82 offen offset:3072 sc1
Ta 152,26,10336
Zvaa 96,128,184
Ta 154,26,15456
Mvaa 96,132,188
Ta 156,26,20576
Ta 158,26,25696
Zvaa 104,132,204
Mvaa 104,128,200
W 8
Ta 128,25,10368
Ta 130,25,15488
Zvaa 108,136,200
Mvaa 108,140,204
Ta 132,25,20608
Ta 134,25,25728
Zvaa 100,140,188
s_lshl_b32 s92, s58, 2
s_add_u32 s91, s92, s51
s_add_u32 s95, s91, 11
Mvaa 100,136,184
W 8
s_and_b32 s95, s95, 15
s_mul_i32 s95, s95, 40960
Zvaa 112,144,184
s_mul_i32 s71, s51, 10240
s_add_u32 s71, s71, 40960
s_add_u32 m0, s71, s48
Mvaa 112,148,188
s_lshl2_add_u32 s94, s48, s95
Ta 136,26,10400
Ta 138,26,15520
Zvaa 120,148,204
Ta 140,26,20640
Ta 142,26,25760
Mvaa 120,144,200
W 8
Ta 144,25,10432
Ta 146,25,15552
Zvaa 124,152,200
Mvaa 124,156,204
Ta 148,25,20672
Ta 150,25,25792
Zvaa 116,156,188
Mvaa 116,152,184
W 8
Ta 152,26,10464
Ta 154,26,15584
Zvaa 128,128,184
s_lshl_b32 s80, s58, 3
s_add_u32 s80, s80, s55
v_mov_b64 v[28:29], s[12:13]
s_mov_b64 exec, 1
buffer_store_dwordx2 v[28:29], off, s[8:11], s80 sc1
s_mov_b64 exec, -1
Mvaa 128,132,188
Ta 156,26,20704
v_and_b32 v27, 1, v12
Zvaa 136,132,204
Ta 158,26,25824
v_lshlrev_b32 v27, 1, v27
Mvaa 136,128,200
W 8
Ta 128,25,10496
v_xor_b32 v27, v27, v13
Zvaa 140,136,200
s_barrier
Mvaa 140,140,204
Ta 130,25,15616
)"
R"(v_and_b32 v12, 2, v12
Zvaa 132,140,188
D 16,40,94,0
v_or_b32 v12, v12, v14
Mvaa 132,136,184
W 6
s_add_u32 s95, s91, 13
s_and_b32 s95, s95, 15
Zvaa 144,144,184
s_mul_i32 s95, s95, 40960
s_mul_i32 s71, s51, 20480
s_add_u32 s71, s71, 71680
Mvaa 144,148,188
Ta 132,25,20736
Ta 134,25,25856
Zvaa 152,148,204
v_lshl_or_b32 v12, v27, 2, v12
v_cmp_eq_u32_e64 s[86:87], 0, v12
Mvaa 152,144,200
W 4
Ta 136,26,10528
Ta 138,26,15648
Zvaa 156,152,200
D 17,40,94,1024
v_cndmask_b32_e64 v4, 0, v15, s[86:87]
Mvaa 156,156,204
Ta 140,26,20768
v_cmp_eq_u32_e64 s[86:87], 1, v12
Zvaa 148,156,188
Ta 142,26,25888
Ta 144,25,30720
Mvaa 148,152,184
W 5
v_cndmask_b32_e64 v5, 0, v15, s[86:87]
v_cmp_eq_u32_e64 s[86:87], 2, v12
Zvaa 160,128,184
Ta 146,25,35840
D 18,40,94,2048
Mvaa 160,132,188
v_cndmask_b32_e64 v6, 0, v15, s[86:87]
v_cmp_eq_u32_e64 s[86:87], 3, v12
Zvaa 168,132,204
Ta 148,21,0
Ta 150,21,5120
Mvaa 168,128,200
W 4
Ta 152,26,30752
v_cndmask_b32_e64 v7, 0, v15, s[86:87]
Zvaa 172,136,200
Ta 154,26,35872
v_cmp_eq_u32_e64 s[86:87], 4, v12
Mvaa 172,140,204
D 19,40,94,3072
Ta 156,22,32
Zvaa 164,140,188
s_add_u32 m0, m0, 4096
Ta 158,22,5152
v_cndmask_b32_e64 v8, 0, v15, s[86:87]
Mvaa 164,136,184
s_waitcnt vmcnt(5) lgkmcnt(4)
Ta 128,25,30784
v_cmp_eq_u32_e64 s[86:87], 5, v12
Mvaa 96,144,192
Ta 130,25,35904
Ta 132,21,64
Mvaa 96,148,196
v_cndmask_b32_e64 v9, 0, v15, s[86:87]
v_cmp_eq_u32_e64 s[86:87], 6, v12
Mvaa 104,148,212
buffer_load_dwordx4 v20, s[40:43], s94 offen lds
Ta 134,21,5184
Mvaa 104,144,208
W 4
s_add_u32 s81, s58, 1
s_and_b32 s81, s81, 3
Mvaa 108,152,208
s_lshl_b32 s81, s81, 12
s_add_u32 s81, s81, s49
v_cndmask_b32_e64 v10, 0, v15, s[86:87]
Mvaa 108,156,212
Ta 136,26,30816
v_cmp_eq_u32_e64 s[86:87], 7, v12
Mvaa 100,156,196
Ta 138,26,35936
buffer_load_dwordx4 a[216:219], v1, s[8:11], s81 offen offset:0 sc1
Mvaa 100,152,192
Ta 140,22,96
Ta 142,22,5216
W 4
Mvaa 112,128,192
s_add_u32 s82, s81, 131072
Ta 144,25,30848
v_cndmask_b32_e64 v11, 0, v15, s[86:87]
Mvaa 112,132,196
s_mov_b32 s20, s38
s_and_b32 s21, s39, 0xffff
s_mov_b32 s22, 2621440
Mvaa 120,132,212
s_mov_b32 s23, 0x20000
buffer_load_dwordx4 a[232:235], v1, s[8:11], s82 offen offset:0 sc1
Ta 146,25,35968
Mvaa 120,128,208
Ta 148,21,128
Ta 150,21,5248
W 4
Mvaa 124,136,208
s_and_b32 s17, s17, 0xffff
s_mov_b32 s18, 2621440
s_mov_b32 s19, 0x20000
Mvaa 124,140,212
s_lshr_b32 s66, s15, 3
Ta 152,26,30880
Ta 154,26,36000
Mvaa 116,140,196
s_lshl_b32 s66, s66, 4
buffer_load_dwordx4 a[220:223], v1, s[8:11], s81 offen offset:1024 sc1
Ta 156,22,160
Mvaa 116,136,192
s_add_u32 s66, s66, s59
Ta 158,22,5280
W 4
Mvaa 128,144,192
s_lshl_b32 s63, s66, 6
Ta 128,25,30912
Ta 130,25,36032
Mvaa 128,148,196
s_add_u32 s63, s63, 65536
s_mul_i32 s62, s66, 40960
Ta 132,21,192
Mvaa 136,148,212
s_add_u32 s62, s62, 56623104
buffer_load_dwordx4 a[236:239], v1, s[8:11], s82 offen offset:1024 sc1
Ta 134,21,5312
Mvaa 136,144,208
W 4
s_lshl_b32 s66, s14, 13
Ta 136,26,30944
Mvaa 140,152,208
s_add_u32 s78, s62, s66
s_add_u32 s79, s78, 327680
Ta 138,26,36064
Mvaa 140,156,212
s_and_b32 s66, s14, 1
Ta 140,22,224
Ta 142,22,5344
Mvaa 132,156,196
s_mul_i32 s66, s66, 327680
buffer_load_dwordx4 a[224:227], v1, s[8:11], s81 offen offset:2048 sc1
Ta 144,25,30976
Mvaa 132,152,192
W 5
s_add_u32 s65, s62, s66
Ta 146,25,36096
Mvaa 144,128,192
s_add_u32 s65, s65, 32768
Ta 148,21,256
Ta 150,21,5376
Mvaa 144,132,196
s_mov_b32 s64, 0
Ta 152,26,31008
Ta 154,26,36128
Mvaa 152,132,212
s_bfe_u32 s66, s59, 0x10001
s_lshl_b32 s67, s57, 1
buffer_load_dwordx4 a[240:243], v1, s[8:11], s82 offen offset:2048 sc1
Mvaa 152,128,208
W 6
Ta 156,22,288
Ta 158,22,5408
Mvaa 156,136,208
s_add_u32 s66, s66, s67
s_lshl_b32 s61, s66, 2
Ta 128,21,10240
Mvaa 156,140,212
s_lshr_b32 s66, s59, 2
s_lshl_b32 s66, s66, 1
Ta 130,21,15360
Mvaa 148,140,196
s_add_u32 s61, s61, s66
buffer_load_dwordx4 a[228:231], v1, s[8:11], s81 offen offset:3072 sc1
Ta 132,21,20480
Mvaa 148,136,192
W 7
s_and_b32 s66, s59, 1
Ta 134,21,25600
Mvaa 160,144,192
s_add_u32 s61, s61, s66
Ta 136,22,10272
Ta 138,22,15392
Mvaa 160,148,196
Ta 140,22,20512
Ta 142,22,25632
Mvaa 168,148,212
s_xor_b32 s67, s61, 0
buffer_load_dwordx4 a[244:247], v1, s[8:11], s82 offen offset:3072 sc1
Mvaa 168,144,208
W 8
s_add_u32 m0, s71, s48
s_lshl2_add_u32 s94, s48, s95
Mvaa 172,152,208
s_mul_i32 s72, s67, 20480
Ta 144,21,10304
Ta 146,21,15424
Mvaa 172,156,212
s_mul_i32 s66, s58, 320
s_add_u32 s72, s72, s66
Ta 148,21,20544
Mvaa 164,156,196
s_mul_i32 s75, s67, 20480
D 16,40,94,0
Ta 150,21,25664
Mvaa 164,152,192
s_waitcnt vmcnt(5) lgkmcnt(8)
s_add_u32 s95, s92, 15
s_and_b32 s95, s95, 15
Mvaa 96,128,216
s_mul_i32 s95, s95, 40960
s_mul_i32 s71, s51, 0
s_add_u32 s71, s71, 102400
Mvaa 96,132,220
s_mul_i32 s66, s58, 2560
Ta 152,22,10336
Ta 154,22,15456
Mvaa 104,132,236
s_add_u32 s75, s75, s66
s_add_u32 s75, s75, 131072
D 17,40,94,1024
Mvaa 104,128,232
W 6
Ta 156,22,20576
Ta 158,22,25696
Mvaa 108,136,232
s_xor_b32 s67, s61, 124
s_mul_i32 s73, s67, 20480
Ta 128,21,10368
Mvaa 108,140,236
s_mul_i32 s66, s58, 320
s_add_u32 s73, s73, s66
Ta 130,21,15488
Mvaa 100,140,220
s_mul_i32 s76, s67, 20480
D 18,40,94,2048
Ta 132,21,20608
Mvaa 100,136,216
W 7
Ta 134,21,25728
Ta 136,22,10400
Mvaa 112,144,216
s_mul_i32 s66, s58, 2560
Ta 138,22,15520
Mvaa 112,148,220
s_add_u32 s76, s76, s66
Ta 140,22,20640
Ta 142,22,25760
Mvaa 120,148,236
s_add_u32 s76, s76, 131072
D 19,40,94,3072
Mvaa 120,144,232
W 8
s_add_u32 m0, m0, 4096
s_bitcmp1_b32 s14, 0
s_cselect_b32 s74, s73, s72
s_cselect_b32 s77, s76, s75
Mvaa 124,152,232
s_add_u32 s74, s74, 256
Ta 144,21,10432
Ta 146,21,15552
Mvaa 124,156,236
s_add_u32 s77, s77, 2048
Ta 148,21,20672
Ta 150,21,25792
Mvaa 116,156,220
buffer_load_dwordx4 v20, s[40:43], s94 offen lds
s_add_u32 m0, s71, s48
s_lshl2_add_u32 s94, s48, s95
D 16,40,94,0
D 17,40,94,1024
D 18,40,94,2048
D 19,40,94,3072
s_add_u32 m0, m0, 4096
s_nop 0
buffer_load_dwordx4 v20, s[40:43], s94 offen lds
Mvaa 116,152,216
W 8
s_add_u32 s81, s58, 2
s_and_b32 s81, s81, 3
Mvaa 128,128,216
s_lshl_b32 s81, s81, 12
s_add_u32 s81, s81, s49
s_lshl_b32 s66, s14, 6
Mvaa 128,132,220
s_lshl_b32 s67, s14, 9
Ta 152,22,10464
Ta 154,22,15584
Mvaa 136,132,236
s_add_u32 s72, s72, s66
s_add_u32 s75, s75, s67
Ta 156,22,20704
Mvaa 136,128,232
W 7
s_add_u32 s73, s73, s66
Ta 158,22,25824
Mvaa 140,136,232
s_barrier
Mvaa 140,140,236
s_add_u32 s76, s76, s67
Ta 128,21,10496
Ta 130,21,15616
Mvaa 132,140,220
buffer_load_dwordx4 a[0:3], v1, s[8:11], s81 offen offset:0 sc1
v_add_u32 v19, 4096, v1
Mvaa 132,136,216
W 6
s_add_u32 s82, s81, 131072
Ta 132,21,20736
Mvaa 144,144,216
Ta 134,21,25856
Ta 136,22,10528
Mvaa 144,148,220
Ta 138,22,15648
Ta 140,22,20768
Mvaa 152,148,236
buffer_load_dwordx4 a[16:19], v1, s[8:11], s82 offen offset:0 sc1
Ta 142,22,25888
Mvaa 152,144,232
W 8
Ta 144,21,30720
Ta 146,21,35840
Mvaa 156,152,232
Mvaa 156,156,236
Ta 148,23,0
Ta 150,23,5120
Mvaa 148,156,220
buffer_load_dwordx4 a[4:7], v1, s[8:11], s81 offen offset:1024 sc1
Mvaa 148,152,216
W 8
Ta 152,22,30752
Ta 154,22,35872
Mvaa 160,128,216
Mvaa 160,132,220
Ta 156,24,32
Ta 158,24,5152
Mvaa 168,132,236
buffer_load_dwordx4 a[20:23], v1, s[8:11], s82 offen offset:1024 sc1
Mvaa 168,128,232
W 8
Ta 128,21,30784
v_bfe_u32 v27, v1, 4, 3
Mvaa 172,136,232
Ta 130,21,35904
Mvaa 172,140,236
Ta 132,23,64
Ta 134,23,5184
Mvaa 164,140,220
buffer_load_dwordx4 a[8:11], v1, s[8:11], s81 offen offset:2048 sc1
Mvaa 164,136,216
s_waitcnt vmcnt(15) lgkmcnt(8)
Ta 136,22,30816
Ta 138,22,35936
Mvaa 96,144,224
Mvaa 96,148,228
Ta 140,24,96
Ta 142,24,5216
Mvaa 104,148,244
buffer_load_dwordx4 a[24:27], v1, s[8:11], s82 offen offset:2048 sc1
Mvaa 104,144,240
W 8
Ta 144,21,30848
Ta 146,21,35968
Mvaa 108,152,240
Mvaa 108,156,244
Ta 148,23,128
Ta 150,23,5248
Mvaa 100,156,228
buffer_load_dwordx4 a[12:15], v1, s[8:11], s81 offen offset:3072 sc1
Mvaa 100,152,224
W 8
Ta 152,22,30880
Ta 154,22,36000
Mvaa 112,128,224
Mvaa 112,132,228
Ta 156,24,160
v_lshlrev_b32 v27, 3, v27
Mvaa 120,132,244
buffer_load_dwordx4 a[28:31], v1, s[8:11], s82 offen offset:3072 sc1
Ta 158,24,5280
Mvaa 120,128,240
W 8
s_add_u32 s81, s58, 3
s_and_b32 s81, s81, 3
Mvaa 124,136,240
s_lshl_b32 s81, s81, 12
s_add_u32 s81, s81, s49
Ta 128,21,30912
Mvaa 124,140,244
Ta 130,21,36032
Ta 132,23,192
Mvaa 116,140,228
buffer_load_dwordx4 a[32:35], v1, s[8:11], s81 offen offset:0 sc1
Ta 134,23,5312
)"
R"(Mvaa 116,136,224
W 8
s_add_u32 s82, s81, 131072
Ta 136,22,30944
Mvaa 128,144,224
Ta 138,22,36064
Mvaa 128,148,228
Ta 140,24,224
Ta 142,24,5344
Mvaa 136,148,244
buffer_load_dwordx4 a[48:51], v1, s[8:11], s82 offen offset:0 sc1
Mvaa 136,144,240
W 8
Ta 144,21,30976
Ta 146,21,36096
Mvaa 140,152,240
s_waitcnt vmcnt(10)
s_barrier
Mvaa 140,156,244
Ta 148,23,256
Ta 150,23,5376
Mvaa 132,156,228
buffer_load_dwordx4 a[36:39], v1, s[8:11], s81 offen offset:1024 sc1
Mvaa 132,152,224
W 8
Ta 152,22,31008
Ta 154,22,36128
Mvaa 144,128,224
v_bfe_u32 v18, v1, 7, 1
Mvaa 144,132,228
Ta 156,24,288
Ta 158,24,5408
Mvaa 152,132,244
buffer_load_dwordx4 a[52:55], v1, s[8:11], s82 offen offset:1024 sc1
Mvaa 152,128,240
W 8
Ta 128,23,20480
Ta 130,23,25600
Mvaa 156,136,240
Mvaa 156,140,244
Ta 132,23,51200
Ta 134,23,56320
Mvaa 148,140,228
buffer_load_dwordx4 a[40:43], v1, s[8:11], s81 offen offset:2048 sc1
Mvaa 148,136,224
W 8
Ta 136,24,20512
Ta 138,24,25632
Mvaa 160,144,224
Mvaa 160,148,228
Ta 140,24,51232
Ta 142,24,56352
Mvaa 168,148,244
buffer_load_dwordx4 a[56:59], v1, s[8:11], s82 offen offset:2048 sc1
Mvaa 168,144,240
W 8
Ta 144,23,20544
Ta 146,23,25664
Mvaa 172,152,240
Mvaa 172,156,244
Ta 148,23,51264
Ta 150,23,56384
Mvaa 164,156,228
buffer_load_dwordx4 a[44:47], v1, s[8:11], s81 offen offset:3072 sc1
v_lshl_add_u32 v18, v18, 9, v27
Mvaa 164,152,224
s_waitcnt vmcnt(11) lgkmcnt(8)
Ta 152,24,20576
Ta 154,24,25696
Mvaa 96,128,0
Mvaa 96,132,4
Ta 156,24,51296
Ta 158,24,56416
Mvaa 104,132,20
buffer_load_dwordx4 a[60:63], v1, s[8:11], s82 offen offset:3072 sc1
Mvaa 104,128,16
W 8
Ta 128,23,20608
Ta 130,23,25728
Mvaa 108,136,16
Mvaa 108,140,20
Ta 132,23,51328
Ta 134,23,56448
Mvaa 100,140,4
Mvaa 100,136,0
W 8
Ta 136,24,20640
Ta 138,24,25760
Mvaa 112,144,0
v_bfe_u32 v28, v1, 4, 2
Mvaa 112,148,4
Ta 140,24,51360
v_lshlrev_b32 v28, 3, v28
Mvaa 120,148,20
Ta 142,24,56480
buffer_load_dwordx2 v[28:29], v28, s[8:11], s55 offen sc1
Mvaa 120,144,16
W 8
Ta 144,23,20672
Ta 146,23,25792
Mvaa 124,152,16
s_mov_b32 s71, 0
Mvaa 124,156,20
Ta 148,23,51392
Ta 150,23,56512
Mvaa 116,156,4
Mvaa 116,152,0
W 8
Ta 152,24,20704
Ta 154,24,25824
Mvaa 128,128,0
Mvaa 128,132,4
Ta 156,24,51424
Ta 158,24,56544
Mvaa 136,132,20
Mvaa 136,128,16
W 8
Ta 128,23,20736
Ta 130,23,25856
Mvaa 140,136,16
Mvaa 140,140,20
Ta 132,23,51456
Ta 134,23,56576
Mvaa 132,140,4
Mvaa 132,136,0
W 8
Ta 136,24,20768
Ta 138,24,25888
Mvaa 144,144,0
Mvaa 144,148,4
Ta 140,24,51488
Ta 142,24,56608
Mvaa 152,148,20
Mvaa 152,144,16
W 8
Ta 144,25,0
v_bfe_u32 v20, v1, 4, 4
Mvaa 156,152,16
Ta 146,25,5120
Mvaa 156,156,20
Ta 148,21,40960
Ta 150,21,46080
Mvaa 148,156,4
Mvaa 148,152,0
W 8
Ta 152,26,32
Ta 154,26,5152
Mvaa 160,128,0
Mvaa 160,132,4
Ta 156,22,40992
Ta 158,22,46112
Mvaa 168,132,20
Mvaa 168,128,16
W 8
Ta 128,25,64
Ta 130,25,5184
Mvaa 172,136,16
Mvaa 172,140,20
Ta 132,21,41024
Ta 134,21,46144
Mvaa 164,140,4
Mvaa 164,136,0
s_waitcnt vmcnt(9) lgkmcnt(8)
Ta 136,26,96
Ta 138,26,5216
Mvaa 96,144,8
Mvaa 96,148,12
Ta 140,22,41056
v_mul_u32_u24 v20, 1280, v20
Mvaa 104,148,28
Ta 142,22,46176
Mvaa 104,144,24
W 8
Ta 144,25,128
Ta 146,25,5248
Mvaa 108,152,24
Mvaa 108,156,28
Ta 148,21,41088
Ta 150,21,46208
Mvaa 100,156,12
Mvaa 100,152,8
W 8
Ta 152,26,160
Ta 154,26,5280
Mvaa 112,128,8
Mvaa 112,132,12
Ta 156,22,41120
Ta 158,22,46240
Mvaa 120,132,28
s_waitcnt vmcnt(0)
v_cmp_eq_u64 vcc, s[12:13], v[28:29]
s_cmp_eq_u64 vcc, -1
s_cbranch_scc0 .Lspin_101%=
.Lok_102%=:
s_add_u32 s81, s58, 1
s_and_b32 s81, s81, 3
Mvaa 120,128,24
W 8
s_lshl_b32 s81, s81, 12
s_add_u32 s81, s81, s50
Mvaa 124,136,24
buffer_load_dwordx4 a[64:67], v1, s[8:11], s81 offen offset:0 sc1
Ta 128,25,192
Mvaa 124,140,28
s_add_u32 s82, s81, 131072
Ta 130,25,5312
Ta 132,21,41152
Mvaa 116,140,12
Ta 134,21,46272
Mvaa 116,136,8
W 8
Ta 136,26,224
Ta 138,26,5344
Mvaa 128,144,8
buffer_load_dwordx4 a[80:83], v1, s[8:11], s82 offen offset:0 sc1
v_lshrrev_b32 v12, 8, v1
Mvaa 128,148,12
Ta 140,22,41184
Ta 142,22,46304
Mvaa 136,148,28
Mvaa 136,144,24
W 8
Ta 144,25,256
Ta 146,25,5376
Mvaa 140,152,24
Mvaa 140,156,28
buffer_load_dwordx4 a[68:71], v1, s[8:11], s81 offen offset:1024 sc1
Ta 148,21,41216
Mvaa 132,156,12
Ta 150,21,46336
Mvaa 132,152,8
W 8
Ta 152,26,288
Ta 154,26,5408
Mvaa 144,128,8
Mvaa 144,132,12
Ta 156,22,41248
Ta 158,22,46368
Mvaa 152,132,28
buffer_load_dwordx4 a[84:87], v1, s[8:11], s82 offen offset:1024 sc1
Mvaa 152,128,24
W 8
Ta 128,21,51200
Ta 130,21,56320
Mvaa 156,136,24
Mvaa 156,140,28
Ta 132,23,10240
Ta 134,23,15360
Mvaa 148,140,12
v_lshl_add_u32 v20, v12, 4, v20
Mvaa 148,136,8
W 8
buffer_load_dwordx4 a[72:75], v1, s[8:11], s81 offen offset:2048 sc1
Ta 136,23,30720
Mvaa 160,144,8
Ta 138,23,35840
Mvaa 160,148,12
Ta 140,23,40960
Ta 142,23,46080
Mvaa 168,148,28
Mvaa 168,144,24
W 8
Ta 144,22,51232
Ta 146,22,56352
Mvaa 172,152,24
buffer_load_dwordx4 a[88:91], v1, s[8:11], s82 offen offset:2048 sc1
Mvaa 172,156,28
Ta 148,24,10272
Ta 150,24,15392
Mvaa 164,156,12
Mvaa 164,152,8
W 8
Ta 152,24,30752
Ta 154,24,35872
Mvaa 96,128,32
Mvaa 96,132,36
buffer_load_dwordx4 a[76:79], v1, s[8:11], s81 offen offset:3072 sc1
Ta 156,24,40992
Mvaa 104,132,52
Ta 158,24,46112
Mvaa 104,128,48
W 8
Ta 128,21,51264
Ta 130,21,56384
Mvaa 104,136,56
v_bfe_u32 v15, v1, 7, 1
Mvaa 104,140,60
Ta 132,23,10304
Ta 134,23,15424
Mvaa 96,140,44
buffer_load_dwordx4 a[92:95], v1, s[8:11], s82 offen offset:3072 sc1
Mvaa 96,136,40
W 8
s_add_u32 s81, s58, 2
s_and_b32 s81, s81, 3
Mvaa 100,144,32
s_lshl_b32 s81, s81, 12
s_add_u32 s81, s81, s50
Ta 136,23,30784
Mvaa 100,148,36
Ta 138,23,35904
Ta 140,23,41024
Mvaa 108,148,52
Ta 142,23,46144
buffer_load_dwordx4 a[96:99], v1, s[8:11], s81 offen offset:0 sc1
Mvaa 108,144,48
W 8
s_add_u32 s82, s81, 131072
Ta 144,22,51296
Mvaa 108,152,56
Ta 146,22,56416
Mvaa 108,156,60
Ta 148,24,10336
Ta 150,24,15456
Mvaa 100,156,44
Mvaa 100,152,40
W 8
Ta 152,24,30816
Ta 154,24,35936
Mvaa 112,128,32
buffer_load_dwordx4 a[112:115], v1, s[8:11], s82 offen offset:0 sc1
Mvaa 112,132,36
Ta 156,24,41056
Ta 158,24,46176
Mvaa 120,132,52
K 96,96,97
K 97,98,99
Mvaa 120,128,48
W 8
Ta 128,21,51328
v_mul_u32_u24 v14, 10240, v15
Mvaa 120,136,56
Ta 130,21,56448
K 98,100,101
Mvaa 120,140,60
buffer_load_dwordx4 a[100:103], v1, s[8:11], s81 offen offset:1024 sc1
K 99,102,103
Mvaa 112,140,44
Ta 132,23,10368
Ta 134,23,15488
Mvaa 112,136,40
W 8
s_add_u32 s83, s28, 0
Ta 136,23,30848
Mvaa 116,144,32
Ta 138,23,35968
Mvaa 116,148,36
buffer_store_dwordx4 v[96:99], v1, s[8:11], s83 offen sc1
Ta 140,23,41088
Mvaa 124,148,52
Ta 142,23,46208
K 104,104,105
Mvaa 124,144,48
W 8
Ta 144,22,51360
K 105,106,107
Mvaa 124,152,56
Ta 146,22,56480
Mvaa 124,156,60
buffer_load_dwordx4 a[116:119], v1, s[8:11], s82 offen offset:1024 sc1
K 106,108,109
Mvaa 116,156,44
Ta 148,24,10400
Ta 150,24,15520
Mvaa 116,152,40
W 8
Ta 152,24,30880
K 107,110,111
Mvaa 128,128,32
s_add_u32 s83, s29, 40960
Ta 154,24,36000
Mvaa 128,132,36
buffer_load_dwordx4 a[104:107], v1, s[8:11], s81 offen offset:2048 sc1
v_lshrrev_b32 v15, 8, v1
Mvaa 136,132,52
Ta 156,24,41120
Ta 158,24,46240
Mvaa 136,128,48
W 8
Ta 128,21,51392
Ta 130,21,56512
Mvaa 136,136,56
Mvaa 136,140,60
buffer_store_dwordx4 v[104:107], v1, s[8:11], s83 offen sc1
K 112,112,113
Mvaa 128,140,44
Ta 132,23,10432
K 113,114,115
Mvaa 128,136,40
W 7
Ta 134,23,15552
K 114,116,117
Mvaa 132,144,32
Ta 136,23,30912
Ta 138,23,36032
Mvaa 132,148,36
buffer_load_dwordx4 a[120:123], v1, s[8:11], s82 offen offset:2048 sc1
K 115,118,119
Mvaa 140,148,52
s_add_u32 s83, s28, 8192
)"
R"(Ta 140,23,41152
Ta 142,23,46272
Mvaa 140,144,48
W 8
Ta 144,22,51424
Ta 146,22,56544
Mvaa 140,152,56
Mvaa 140,156,60
buffer_load_dwordx4 a[108:111], v1, s[8:11], s81 offen offset:3072 sc1
Ta 148,24,10464
Mvaa 132,156,44
Ta 150,24,15584
Mvaa 132,152,40
W 8
Ta 152,24,30944
v_lshl_add_u32 v14, v15, 7, v14
Mvaa 144,128,32
Ta 154,24,36064
Mvaa 144,132,36
buffer_store_dwordx4 v[112:115], v1, s[8:11], s83 offen sc1
K 120,120,121
Mvaa 152,132,52
K 121,122,123
K 122,124,125
Mvaa 152,128,48
W 6
Ta 156,24,41184
K 123,126,127
Mvaa 152,136,56
s_add_u32 s83, s29, 49152
Ta 158,24,46304
Ta 128,21,51456
Mvaa 152,140,60
buffer_load_dwordx4 a[124:127], v1, s[8:11], s82 offen offset:3072 sc1
Ta 130,21,56576
Mvaa 144,140,44
s_add_u32 s81, s58, 3
s_and_b32 s81, s81, 3
s_lshl_b32 s81, s81, 12
Mvaa 144,136,40
W 6
s_add_u32 s81, s81, s50
Ta 132,23,10496
Mvaa 148,144,32
Ta 134,23,15616
Ta 136,23,30976
Mvaa 148,148,36
buffer_load_dwordx4 a[184:187], v1, s[8:11], s81 offen offset:0 sc1
Ta 138,23,36096
Mvaa 156,148,52
s_add_u32 s82, s81, 131072
Ta 140,23,41216
Ta 142,23,46336
Mvaa 156,144,48
W 8
Ta 144,22,51488
Ta 146,22,56608
Mvaa 156,152,56
Mvaa 156,156,60
buffer_store_dwordx4 v[120:123], v1, s[8:11], s83 offen sc1
K 128,128,129
Mvaa 148,156,44
K 129,130,131
K 130,132,133
Mvaa 148,152,40
W 6
Ta 148,24,10528
K 131,134,135
Mvaa 160,128,32
s_add_u32 s83, s28, 16384
Ta 150,24,15648
v_and_b32 v15, 0x70, v1
Mvaa 160,132,36
buffer_load_dwordx4 a[200:203], v1, s[8:11], s82 offen offset:0 sc1
Ta 152,24,31008
Mvaa 168,132,52
Ta 154,24,36128
Ta 156,24,41248
Mvaa 168,128,48
W 7
Ta 158,24,46368
Ta 128,21,10240
Mvaa 168,136,56
Ta 130,21,15360
Mvaa 168,140,60
buffer_load_dwordx4 a[188:191], v1, s[8:11], s81 offen offset:1024 sc1
Ta 132,21,20480
Mvaa 160,140,44
Ta 134,21,25600
Mvaa 160,136,40
W 8
Ta 136,22,10272
Ta 138,22,15392
Mvaa 164,144,32
Mvaa 164,148,36
buffer_store_dwordx4 v[128:131], v1, s[8:11], s83 offen sc1
K 136,136,137
Mvaa 172,148,52
K 137,138,139
K 138,140,141
Mvaa 172,144,48
W 6
Ta 140,22,20512
K 139,142,143
Mvaa 172,152,56
s_add_u32 s83, s29, 57344
Ta 142,22,25632
Ta 144,21,10304
Mvaa 172,156,60
buffer_load_dwordx4 a[204:207], v1, s[8:11], s82 offen offset:1024 sc1
Ta 146,21,15424
Mvaa 164,156,44
Ta 148,21,20544
v_add_u32 v14, v14, v15
Mvaa 164,152,40
s_waitcnt vmcnt(21) lgkmcnt(7)
Ta 150,21,25664
Ta 152,22,10336
Mvaa 176,128,64
Ta 154,22,15456
Mvaa 176,132,68
buffer_load_dwordx4 a[192:195], v1, s[8:11], s81 offen offset:2048 sc1
Ta 156,22,20576
Mvaa 184,132,84
Ta 158,22,25696
Mvaa 184,128,80
W 8
Ta 128,21,10368
Ta 130,21,15488
Mvaa 188,136,80
Mvaa 188,140,84
buffer_store_dwordx4 v[136:139], v1, s[8:11], s83 offen sc1
K 144,144,145
Mvaa 180,140,68
K 145,146,147
K 146,148,149
Mvaa 180,136,64
W 6
Ta 132,21,20608
K 147,150,151
Mvaa 192,144,64
s_add_u32 s83, s28, 24576
Ta 134,21,25728
Ta 136,22,10400
Mvaa 192,148,68
buffer_load_dwordx4 a[208:211], v1, s[8:11], s82 offen offset:2048 sc1
Ta 138,22,15520
Mvaa 200,148,84
Ta 140,22,20640
Ta 142,22,25760
Mvaa 200,144,80
W 8
Ta 144,21,10432
Ta 146,21,15552
Mvaa 204,152,80
Mvaa 204,156,84
buffer_store_dwordx4 v[144:147], v1, s[8:11], s83 offen sc1
K 152,152,153
Mvaa 196,156,68
K 153,154,155
K 154,156,157
Mvaa 196,152,64
W 6
Ta 148,21,20672
K 155,158,159
Mvaa 208,128,64
s_add_u32 s83, s29, 65536
Ta 150,21,25792
Ta 152,22,10464
Mvaa 208,132,68
buffer_load_dwordx4 a[196:199], v1, s[8:11], s81 offen offset:3072 sc1
Ta 154,22,15584
Mvaa 216,132,84
Ta 156,22,20704
Ta 158,22,25824
Mvaa 216,128,80
W 8
Ta 128,21,10496
Ta 130,21,15616
Mvaa 220,136,80
Mvaa 220,140,84
buffer_load_dwordx4 a[212:215], v1, s[8:11], s82 offen offset:3072 sc1
Ta 132,21,20736
Mvaa 212,140,68
Ta 134,21,25856
Mvaa 212,136,64
W 8
Ta 136,22,10528
Ta 138,22,15648
Mvaa 224,144,64
Mvaa 224,148,68
buffer_store_dwordx4 v[152:155], v1, s[8:11], s83 offen sc1
K 160,160,161
Mvaa 232,148,84
K 161,162,163
K 162,164,165
Mvaa 232,144,80
W 6
Ta 140,22,20768
K 163,166,167
Mvaa 236,152,80
s_add_u32 s83, s28, 32768
Ta 142,22,25888
Ta 144,21,30720
Mvaa 236,156,84
buffer_load_dwordx4 v[36:39], v20, s[16:19], s72 offen
Ta 146,21,35840
Mvaa 228,156,68
Ta 148,23,0
Ta 150,23,5120
Mvaa 228,152,64
W 8
Ta 152,22,30752
Ta 154,22,35872
Mvaa 240,128,64
Mvaa 240,132,68
buffer_load_dwordx4 v[44:47], v14, s[8:11], s75 offen sc1
Ta 156,24,32
Mvaa 248,132,84
Ta 158,24,5152
Mvaa 248,128,80
W 8
Ta 128,21,30784
Ta 130,21,35904
Mvaa 252,136,80
Mvaa 252,140,84
buffer_store_dwordx4 v[160:163], v1, s[8:11], s83 offen sc1
K 168,168,169
Mvaa 244,140,68
K 169,170,171
K 170,172,173
Mvaa 244,136,64
s_waitcnt vmcnt(27) lgkmcnt(6)
Ta 132,23,64
K 171,174,175
Mvaa 176,144,72
s_add_u32 s83, s29, 73728
Ta 134,23,5184
Ta 136,22,30816
Mvaa 176,148,76
buffer_load_dwordx4 v[52:55], v20, s[16:19], s73 offen
Ta 138,22,35936
Mvaa 184,148,92
Ta 140,24,96
Ta 142,24,5216
Mvaa 184,144,88
W 8
Ta 144,21,30848
Ta 146,21,35968
Mvaa 188,152,88
Mvaa 188,156,92
buffer_store_dwordx4 v[168:171], v1, s[8:11], s83 offen sc1
Ta 148,23,128
Mvaa 180,156,76
Ta 150,23,5248
Mvaa 180,152,72
W 8
Ta 152,22,30880
Ta 154,22,36000
Mvaa 192,128,72
Mvaa 192,132,76
buffer_load_dwordx4 v[60:63], v14, s[8:11], s76 offen sc1
Ta 156,24,160
Mvaa 200,132,92
Ta 158,24,5280
Mvaa 200,128,88
W 8
Ta 128,21,30912
Ta 130,21,36032
Mvaa 204,136,88
Mvaa 204,140,92
buffer_load_dwordx4 v[68:71], v20, s[16:19], s74 offen
Ta 132,23,192
Mvaa 196,140,76
Ta 134,23,5312
Mvaa 196,136,72
W 8
Ta 136,22,30944
Ta 138,22,36064
Mvaa 208,144,72
Mvaa 208,148,76
buffer_load_dwordx4 v[76:79], v14, s[8:11], s77 offen sc1
Ta 140,24,224
Mvaa 216,148,92
Ta 142,24,5344
Mvaa 216,144,88
W 8
Ta 144,21,30976
Ta 146,21,36096
Mvaa 220,152,88
Mvaa 220,156,92
buffer_load_dwordx2 v[12:13], v27, s[8:11], 0 offen offset:3584 sc1
Ta 148,23,256
Mvaa 212,156,76
Ta 150,23,5376
Mvaa 212,152,72
W 8
Ta 152,22,31008
Ta 154,22,36128
Mvaa 224,128,72
Mvaa 224,132,76
Ta 156,24,288
Ta 158,24,5408
Mvaa 232,132,92
Mvaa 232,128,88
W 8
Ta 128,23,20480
Ta 130,23,25600
Mvaa 236,136,88
Mvaa 236,140,92
Ta 132,23,51200
Ta 134,23,56320
Mvaa 228,140,76
Mvaa 228,136,72
W 8
Ta 136,25,0
Ta 138,25,5120
Mvaa 240,144,72
Mvaa 240,148,76
Ta 140,21,40960
Ta 142,21,46080
Mvaa 248,148,92
Mvaa 248,144,88
W 8
Ta 144,21,51200
Ta 146,21,56320
Mvaa 252,152,88
Mvaa 252,156,92
Ta 148,23,10240
Ta 150,23,15360
Mvaa 244,156,76
Mvaa 244,152,72
s_waitcnt vmcnt(28) lgkmcnt(8)
Ta 152,23,30720
Ta 154,23,35840
Mvaa 176,128,96
Mvaa 176,132,100
Ta 156,23,40960
Ta 158,23,46080
Mvaa 184,132,116
Mvaa 184,128,112
s_waitcnt vmcnt(22) lgkmcnt(8)
Ta 128,24,20512
Ta 130,24,25632
Mvaa 184,136,120
Mvaa 184,140,124
Ta 132,24,51232
Ta 134,24,56352
Mvaa 176,140,108
Mvaa 176,136,104
s_waitcnt vmcnt(16) lgkmcnt(8)
Ta 136,26,32
Ta 138,26,5152
Mvaa 176,144,184
Mvaa 176,148,188
Ta 140,22,40992
Ta 142,22,46112
Mvaa 184,148,204
s_waitcnt vmcnt(4)
v_lshlrev_b32 v2, 2, v1
v_mov_b64 v[28:29], s[12:13]
s_mov_b64 exec, 3
buffer_store_dwordx2 v[28:29], v2, s[8:11], s53 offen offset:0 sc1
s_mov_b64 exec, -1
Mvaa 184,144,200
W 8
Ta 144,22,51232
Ta 146,22,56352
Mvaa 184,152,208
Mvaa 184,156,212
Ta 148,24,10272
Ta 150,24,15392
Mvaa 176,156,196
Mvaa 176,152,192
W 8
Ta 152,24,30752
Ta 154,24,35872
Mvaa 180,128,96
Mvaa 180,132,100
Ta 156,24,40992
Ta 158,24,46112
Mvaa 188,132,116
Mvaa 188,128,112
W 8
Ta 128,23,20544
Ta 130,23,25664
Mvaa 188,136,120
Mvaa 188,140,124
Ta 132,23,51264
Ta 134,23,56384
Mvaa 180,140,108
Mvaa 180,136,104
W 8
Ta 136,25,64
Ta 138,25,5184
Mvaa 180,144,184
Mvaa 180,148,188
Ta 140,21,41024
Ta 142,21,46144
Mvaa 188,148,204
)"
R"(Mvaa 188,144,200
W 8
Ta 144,21,51264
Ta 146,21,56384
Mvaa 188,152,208
Mvaa 188,156,212
Ta 148,23,10304
Ta 150,23,15424
Mvaa 180,156,196
s_waitcnt vmcnt(1)
v_xor_b32 v12, s12, v12
v_xor_b32 v13, s13, v13
v_lshrrev_b32 v3, 11, v12
v_or_b32 v13, v13, v3
v_cmp_eq_u32 vcc, 0, v13
s_cmp_eq_u64 vcc, -1
s_cbranch_scc0 .LL_106%=
.LL_107%=:
v_bfe_u32 v13, v12, 4, 7
s_xor_b32 s66, s61, 124
v_cmp_eq_u32 vcc, s61, v13
v_cmp_eq_u32_e64 s[86:87], s66, v13
s_or_b32 s68, vcc_lo, s86
s_and_b32 s68, s68, 255
s_cbranch_scc1 .Lup_104%=
.Lupr_105%=:
Mvaa 180,152,192
W 8
Ta 152,23,30784
Ta 154,23,35904
Mvaa 192,128,96
v_lshlrev_b32 v32, 16, v36
Mvaa 192,132,100
Ta 156,23,41024
v_and_b32 v33, 0xffff0000, v36
Mvaa 200,132,116
Ta 158,23,46144
v_lshlrev_b32 v34, 16, v37
Mvaa 200,128,112
W 8
K 176,176,177
v_and_b32 v35, 0xffff0000, v37
Mvaa 200,136,120
Ta 128,24,20576
Ta 130,24,25696
Mvaa 200,140,124
buffer_load_dwordx2 v[16:17], v18, s[8:11], s63 offen sc1
v_lshlrev_b32 v36, 16, v38
Mvaa 192,140,108
K 177,178,179
v_and_b32 v37, 0xffff0000, v38
Mvaa 192,136,104
W 6
Ta 132,24,51296
K 178,180,181
Mvaa 192,144,184
Ta 134,24,56416
v_lshlrev_b32 v38, 16, v39
Mvaa 192,148,188
K 179,182,183
v_and_b32 v39, 0xffff0000, v39
Mvaa 200,148,204
s_add_u32 s83, s28, 327680
Ta 136,26,96
Ta 138,26,5216
Mvaa 200,144,200
W 6
v_lshlrev_b32 v40, 16, v44
v_and_b32 v41, 0xffff0000, v44
Mvaa 200,152,208
buffer_store_dwordx4 v[176:179], v1, s[8:11], s83 offen sc1
K 184,184,185
Mvaa 200,156,212
K 185,186,187
K 186,188,189
Mvaa 192,156,196
K 187,190,191
s_add_u32 s83, s29, 368640
buffer_store_dwordx4 v[184:187], v1, s[8:11], s83 offen sc1
Mvaa 192,152,192
Ta 140,22,41056
Ta 142,22,46176
W 4
Mvaa 196,128,96
v_lshlrev_b32 v42, 16, v45
v_and_b32 v43, 0xffff0000, v45
Mvaa 196,132,100
Ta 144,22,51296
Ta 146,22,56416
Mvaa 204,132,116
v_lshlrev_b32 v44, 16, v46
v_and_b32 v45, 0xffff0000, v46
Mvaa 204,128,112
Ta 148,24,10336
Ta 150,24,15456
W 4
Mvaa 204,136,120
v_lshlrev_b32 v46, 16, v47
v_and_b32 v47, 0xffff0000, v47
Mvaa 204,140,124
Ta 152,24,30816
Ta 154,24,35936
Mvaa 196,140,108
v_lshlrev_b32 v48, 16, v52
v_and_b32 v49, 0xffff0000, v52
Mvaa 196,136,104
Ta 156,24,41056
Ta 158,24,46176
W 4
Mvaa 196,144,184
v_lshlrev_b32 v50, 16, v53
v_and_b32 v51, 0xffff0000, v53
Mvaa 196,148,188
Ta 128,23,20608
Ta 130,23,25728
Mvaa 204,148,204
v_lshlrev_b32 v52, 16, v54
v_and_b32 v53, 0xffff0000, v54
Mvaa 204,144,200
Ta 132,23,51328
Ta 134,23,56448
W 4
Mvaa 204,152,208
v_lshlrev_b32 v54, 16, v55
v_and_b32 v55, 0xffff0000, v55
Mvaa 204,156,212
Ta 136,25,128
Ta 138,25,5248
Mvaa 196,156,196
v_lshlrev_b32 v56, 16, v60
v_and_b32 v57, 0xffff0000, v60
Mvaa 196,152,192
Ta 140,21,41088
Ta 142,21,46208
W 4
Mvaa 208,128,96
v_lshlrev_b32 v58, 16, v61
v_and_b32 v59, 0xffff0000, v61
Mvaa 208,132,100
Ta 144,21,51328
Ta 146,21,56448
Mvaa 216,132,116
v_lshlrev_b32 v60, 16, v62
v_and_b32 v61, 0xffff0000, v62
Mvaa 216,128,112
Ta 148,23,10368
Ta 150,23,15488
W 4
Mvaa 216,136,120
v_lshlrev_b32 v62, 16, v63
v_and_b32 v63, 0xffff0000, v63
Mvaa 216,140,124
Ta 152,23,30848
Ta 154,23,35968
Mvaa 208,140,108
v_lshlrev_b32 v64, 16, v68
v_and_b32 v65, 0xffff0000, v68
Mvaa 208,136,104
Ta 156,23,41088
Ta 158,23,46208
W 4
Mvaa 208,144,184
v_lshlrev_b32 v66, 16, v69
K 192,192,193
Mvaa 208,148,188
Ta 128,24,20640
v_and_b32 v67, 0xffff0000, v69
Mvaa 216,148,204
Ta 130,24,25760
K 193,194,195
Mvaa 216,144,200
Ta 132,24,51360
Ta 134,24,56480
W 4
Mvaa 216,152,208
v_lshlrev_b32 v68, 16, v70
K 194,196,197
Mvaa 216,156,212
v_and_b32 v69, 0xffff0000, v70
v_lshlrev_b32 v70, 16, v71
Mvaa 208,156,196
K 195,198,199
s_add_u32 s83, s28, 335872
buffer_store_dwordx4 v[192:195], v1, s[8:11], s83 offen sc1
K 200,200,201
K 201,202,203
K 202,204,205
K 203,206,207
s_add_u32 s83, s29, 376832
buffer_store_dwordx4 v[200:203], v1, s[8:11], s83 offen sc1
Mvaa 208,152,192
Ta 136,26,160
Ta 138,26,5280
Ta 140,22,41120
Ta 142,22,46240
W 4
Mvaa 212,128,96
s_waitcnt vmcnt(4)
v_cmp_eq_u64 vcc, s[12:13], v[16:17]
s_and_b32 s71, vcc_lo, 255
s_bitset0_b32 s71, s59
v_and_b32 v71, 0xffff0000, v71
v_lshlrev_b32 v72, 16, v76
Mvaa 212,132,100
s_lshl_b32 s92, s14, 3
s_lshr_b32 s92, s71, s92
s_mov_b32 s91, 134217728
Mvaa 220,132,116
s_bitcmp1_b32 s71, 0
s_cselect_b32 s94, s78, s91
v_and_b32 v73, 0xffff0000, v76
Mvaa 220,128,112
Ta 144,22,51360
Ta 146,22,56480
Ta 148,24,10400
Ta 150,24,15520
W 4
Mvaa 220,136,120
v_lshlrev_b32 v74, 16, v77
v_and_b32 v75, 0xffff0000, v77
Mvaa 220,140,124
v_lshlrev_b32 v76, 16, v78
v_and_b32 v77, 0xffff0000, v78
Mvaa 212,140,108
v_lshlrev_b32 v78, 16, v79
v_and_b32 v79, 0xffff0000, v79
Mvaa 212,136,104
Ta 152,24,30880
Ta 154,24,36000
Ta 156,24,41120
Ta 158,24,46240
W 4
Mvaa 212,144,184
Ta 128,23,20672
Ta 130,23,25792
Mvaa 212,148,188
buffer_load_dwordx4 a[0:3], v1, s[8:11], s94 offen offset:0 sc1
Ta 132,23,51392
Mvaa 220,148,204
Ta 134,23,56512
Ta 136,25,192
Mvaa 220,144,200
W 5
Ta 138,25,5312
Ta 140,21,41152
Mvaa 220,152,208
Ta 142,21,46272
Ta 144,21,51392
Mvaa 220,156,212
s_bitcmp1_b32 s92, 0
s_cbranch_scc0 .LL_109%=
buffer_load_dwordx4 a[216:219], v1, s[8:11], s65 offen offset:0 sc1
.LL_109%=:
Ta 146,21,56512
Mvaa 212,156,196
s_bitcmp1_b32 s71, 1
s_cselect_b32 s94, s78, s91
Ta 148,23,10432
Mvaa 212,152,192
W 7
Ta 150,23,15552
Ta 152,23,30912
Mvaa 224,128,96
Ta 154,23,36032
Mvaa 224,132,100
buffer_load_dwordx4 a[4:7], v1, s[8:11], s94 offen offset:1024 sc1
Ta 156,23,41152
Mvaa 232,132,116
Ta 158,23,46272
K 208,208,209
Mvaa 232,128,112
W 8
Ta 128,24,20704
Ta 130,24,25824
Mvaa 232,136,120
K 209,210,211
Mvaa 232,140,124
s_bitcmp1_b32 s92, 1
s_cbranch_scc0 .LL_110%=
buffer_load_dwordx4 a[220:223], v1, s[8:11], s65 offen offset:1024 sc1
.LL_110%=:
K 210,212,213
Mvaa 224,140,108
s_bitcmp1_b32 s71, 2
s_cselect_b32 s94, s78, s91
Ta 132,24,51424
Mvaa 224,136,104
W 7
Ta 134,24,56544
K 211,214,215
Mvaa 224,144,184
s_add_u32 s83, s28, 344064
Ta 136,26,224
Ta 138,26,5344
Mvaa 224,148,188
buffer_load_dwordx4 a[8:11], v1, s[8:11], s94 offen offset:2048 sc1
Ta 140,22,41184
Mvaa 232,148,204
Ta 142,22,46304
Mvaa 232,144,200
W 8
Ta 144,22,51424
Ta 146,22,56544
Mvaa 232,152,208
Mvaa 232,156,212
buffer_store_dwordx4 v[208:211], v1, s[8:11], s83 offen sc1
K 216,216,217
Mvaa 224,156,196
K 217,218,219
K 218,220,221
K 219,222,223
s_add_u32 s83, s29, 385024
buffer_store_dwordx4 v[216:219], v1, s[8:11], s83 offen sc1
Mvaa 224,152,192
W 6
Ta 148,24,10464
Ta 150,24,15584
Mvaa 228,128,96
Ta 152,24,30944
Ta 154,24,36064
Mvaa 228,132,100
Ta 156,24,41184
Ta 158,24,46304
Mvaa 236,132,116
s_bitcmp1_b32 s92, 2
s_cbranch_scc0 .LL_111%=
buffer_load_dwordx4 a[224:227], v1, s[8:11], s65 offen offset:2048 sc1
.LL_111%=:
Mvaa 236,128,112
W 8
s_bitcmp1_b32 s71, 3
s_cselect_b32 s94, s78, s91
Mvaa 236,136,120
Ta 128,23,20736
Ta 130,23,25856
Mvaa 236,140,124
Ta 132,23,51456
Ta 134,23,56576
Mvaa 228,140,108
buffer_load_dwordx4 a[12:15], v1, s[8:11], s94 offen offset:3072 sc1
Mvaa 228,136,104
W 8
Ta 136,25,256
Ta 138,25,5376
Mvaa 228,144,184
Mvaa 228,148,188
Ta 140,21,41216
Ta 142,21,46336
Mvaa 236,148,204
s_bitcmp1_b32 s92, 3
s_cbranch_scc0 .LL_112%=
buffer_load_dwordx4 a[228:231], v1, s[8:11], s65 offen offset:3072 sc1
.LL_112%=:
Mvaa 236,144,200
W 8
s_bitcmp1_b32 s71, 4
s_cselect_b32 s94, s78, s91
Mvaa 236,152,208
Ta 144,21,51456
Ta 146,21,56576
Mvaa 236,156,212
Ta 148,23,10496
Ta 150,23,15616
Mvaa 228,156,196
buffer_load_dwordx4 a[16:19], v19, s[8:11], s94 offen offset:0 sc1
Mvaa 228,152,192
W 8
Ta 152,23,30976
Ta 154,23,36096
Mvaa 240,128,96
Mvaa 240,132,100
Ta 156,23,41216
Ta 158,23,46336
Mvaa 248,132,116
s_bitcmp1_b32 s92, 4
s_cbranch_scc0 .LL_113%=
buffer_load_dwordx4 a[232:235], v19, s[8:11], s65 offen offset:0 sc1
.LL_113%=:
K 224,224,225
Mvaa 248,128,112
W 8
s_bitcmp1_b32 s71, 5
s_cselect_b32 s94, s78, s91
Mvaa 248,136,120
Ta 128,24,20768
Ta 130,24,25888
Mvaa 248,140,124
K 225,226,227
K 226,228,229
Mvaa 240,140,108
buffer_load_dwordx4 a[20:23], v19, s[8:11], s94 offen offset:1024 sc1
Ta 132,24,51488
Mvaa 240,136,104
W 7
Ta 134,24,56608
K 227,230,231
Mvaa 240,144,184
s_add_u32 s83, s28, 352256
Ta 136,26,288
Ta 138,26,5408
)"
R"(Mvaa 240,148,188
Ta 140,22,41248
Ta 142,22,46368
Mvaa 248,148,204
s_bitcmp1_b32 s92, 5
s_cbranch_scc0 .LL_114%=
buffer_load_dwordx4 a[236:239], v19, s[8:11], s65 offen offset:1024 sc1
.LL_114%=:
Mvaa 248,144,200
W 8
s_bitcmp1_b32 s71, 6
s_cselect_b32 s94, s78, s91
Mvaa 248,152,208
Ta 144,22,51488
Ta 146,22,56608
Mvaa 248,156,212
Ta 148,24,10528
Ta 150,24,15648
Mvaa 240,156,196
buffer_store_dwordx4 v[224:227], v1, s[8:11], s83 offen sc1
K 232,232,233
K 233,234,235
K 234,236,237
K 235,238,239
s_add_u32 s83, s29, 393216
buffer_store_dwordx4 v[232:235], v1, s[8:11], s83 offen sc1
Mvaa 240,152,192
W 8
Ta 152,24,31008
Ta 154,24,36128
Mvaa 244,128,96
Mvaa 244,132,100
Ta 156,24,41248
Ta 158,24,46368
Mvaa 252,132,116
buffer_load_dwordx4 a[24:27], v19, s[8:11], s94 offen offset:2048 sc1
Mvaa 252,128,112
W 8
Mvaa 252,136,120
Mvaa 252,140,124
Mvaa 244,140,108
s_bitcmp1_b32 s92, 6
s_cbranch_scc0 .LL_115%=
buffer_load_dwordx4 a[240:243], v19, s[8:11], s65 offen offset:2048 sc1
.LL_115%=:
Mvaa 244,136,104
W 4
s_bitcmp1_b32 s71, 7
s_cselect_b32 s94, s78, s91
Mvaa 244,144,184
Mvaa 244,148,188
Mvaa 252,148,204
buffer_load_dwordx4 a[28:31], v19, s[8:11], s94 offen offset:3072 sc1
Mvaa 252,144,200
W 0
Mvaa 252,152,208
Mvaa 252,156,212
Mvaa 244,156,196
s_bitcmp1_b32 s92, 7
s_cbranch_scc0 .LL_116%=
buffer_load_dwordx4 a[244:247], v19, s[8:11], s65 offen offset:3072 sc1
.LL_116%=:
Mvaa 244,152,192
K 240,240,241
K 241,242,243
s_nop 5
K 242,244,245
K 243,246,247
s_add_u32 s83, s28, 360448
buffer_store_dwordx4 v[240:243], v1, s[8:11], s83 offen sc1
K 248,248,249
K 249,250,251
K 250,252,253
K 251,254,255
s_add_u32 s83, s29, 401408
buffer_store_dwordx4 v[248:251], v1, s[8:11], s83 offen sc1
.Ltend_81%=:
buffer_load_dwordx2 v[16:17], v18, s[8:11], s63 offen sc1
s_barrier
s_bitcmp1_b32 s28, 27
s_cbranch_scc0 .LL_117%=
ds_write_b128 v1, v[96:99] offset:0
ds_write_b128 v1, v[112:115] offset:1024
ds_write_b128 v1, v[128:131] offset:2048
ds_write_b128 v1, v[144:147] offset:3072
ds_write_b128 v1, v[160:163] offset:4096
ds_write_b128 v1, v[176:179] offset:5120
ds_write_b128 v1, v[192:195] offset:6144
ds_write_b128 v1, v[208:211] offset:7168
ds_write_b128 v1, v[224:227] offset:8192
ds_write_b128 v1, v[240:243] offset:9216
W 0
.LL_117%=:
s_bitcmp1_b32 s29, 27
s_cbranch_scc0 .LL_118%=
ds_write_b128 v1, v[104:107] offset:0
ds_write_b128 v1, v[120:123] offset:1024
ds_write_b128 v1, v[136:139] offset:2048
ds_write_b128 v1, v[152:155] offset:3072
ds_write_b128 v1, v[168:171] offset:4096
ds_write_b128 v1, v[184:187] offset:5120
ds_write_b128 v1, v[200:203] offset:6144
ds_write_b128 v1, v[216:219] offset:7168
ds_write_b128 v1, v[232:235] offset:8192
ds_write_b128 v1, v[248:251] offset:9216
W 0
.LL_118%=:
s_lshl_b32 s80, s14, 10
v_add_u32 v21, s80, v1
s_cmp_lg_u32 s68, 0
s_cbranch_scc1 .Lup_119%=
.Lupr_120%=:
s_lshl_b32 s66, 0x101, s59
s_waitcnt vmcnt(1)
s_cmp_eq_u32 s64, 0
s_cbranch_scc0 .LL_125%=
v_lshlrev_b32 v2, 2, v1
v_mov_b64 v[28:29], s[12:13]
s_mov_b64 exec, 3
buffer_store_dwordx2 v[28:29], v2, s[8:11], s53 offen offset:512 sc1
s_mov_b64 exec, -1
.LL_125%=:
s_barrier
s_waitcnt vmcnt(1)
v_cmp_eq_u64 vcc, s[12:13], v[16:17]
s_or_b32 s67, vcc_lo, s66
s_cmp_eq_u32 s14, 0
s_cbranch_scc1 .LL_127%=
s_cmp_eq_u32 s14, 1
s_cbranch_scc1 .LL_128%=
s_mov_b32 s84, 0
.LL_129%=:
s_or_b32 s70, s71, s66
s_andn2_b32 s69, s67, s71
s_and_b32 s69, s69, 0xffff
s_cbranch_scc0 .LL_132%=
s_and_b32 s80, s69, 255
s_cbranch_scc0 .LL_133%=
s_bitcmp1_b32 s69, 0
s_cbranch_scc0 .LL_134%=
s_cmp_eq_u32 s59, 0
s_cbranch_scc0 .LL_135%=
Ra 0,21,0
s_branch .LL_134%=
.LL_135%=:
buffer_load_dwordx4 a[0:3], v1, s[8:11], s78 offen offset:0 sc1
.LL_134%=:
s_bitcmp1_b32 s69, 1
s_cbranch_scc0 .LL_136%=
s_cmp_eq_u32 s59, 1
s_cbranch_scc0 .LL_137%=
Ra 4,21,0
s_branch .LL_136%=
.LL_137%=:
buffer_load_dwordx4 a[4:7], v1, s[8:11], s78 offen offset:1024 sc1
.LL_136%=:
s_bitcmp1_b32 s69, 2
s_cbranch_scc0 .LL_138%=
s_cmp_eq_u32 s59, 2
s_cbranch_scc0 .LL_139%=
Ra 8,21,0
s_branch .LL_138%=
.LL_139%=:
buffer_load_dwordx4 a[8:11], v1, s[8:11], s78 offen offset:2048 sc1
.LL_138%=:
s_bitcmp1_b32 s69, 3
s_cbranch_scc0 .LL_140%=
s_cmp_eq_u32 s59, 3
s_cbranch_scc0 .LL_141%=
Ra 12,21,0
s_branch .LL_140%=
.LL_141%=:
buffer_load_dwordx4 a[12:15], v1, s[8:11], s78 offen offset:3072 sc1
.LL_140%=:
s_bitcmp1_b32 s69, 4
s_cbranch_scc0 .LL_142%=
s_cmp_eq_u32 s59, 4
s_cbranch_scc0 .LL_143%=
Ra 16,21,0
s_branch .LL_142%=
.LL_143%=:
buffer_load_dwordx4 a[16:19], v19, s[8:11], s78 offen offset:0 sc1
.LL_142%=:
s_bitcmp1_b32 s69, 5
s_cbranch_scc0 .LL_144%=
s_cmp_eq_u32 s59, 5
s_cbranch_scc0 .LL_145%=
Ra 20,21,0
s_branch .LL_144%=
.LL_145%=:
buffer_load_dwordx4 a[20:23], v19, s[8:11], s78 offen offset:1024 sc1
.LL_144%=:
s_bitcmp1_b32 s69, 6
s_cbranch_scc0 .LL_146%=
s_cmp_eq_u32 s59, 6
s_cbranch_scc0 .LL_147%=
Ra 24,21,0
s_branch .LL_146%=
.LL_147%=:
buffer_load_dwordx4 a[24:27], v19, s[8:11], s78 offen offset:2048 sc1
.LL_146%=:
s_bitcmp1_b32 s69, 7
s_cbranch_scc0 .LL_148%=
s_cmp_eq_u32 s59, 7
s_cbranch_scc0 .LL_149%=
Ra 28,21,0
s_branch .LL_148%=
.LL_149%=:
buffer_load_dwordx4 a[28:31], v19, s[8:11], s78 offen offset:3072 sc1
.LL_148%=:
.LL_133%=:
s_and_b32 s80, s69, 65280
s_cbranch_scc0 .LL_150%=
s_bitcmp1_b32 s69, 8
s_cbranch_scc0 .LL_151%=
s_cmp_eq_u32 s59, 0
s_cbranch_scc0 .LL_152%=
Ra 32,21,5120
s_branch .LL_151%=
.LL_152%=:
buffer_load_dwordx4 a[32:35], v1, s[8:11], s79 offen offset:0 sc1
.LL_151%=:
s_bitcmp1_b32 s69, 9
s_cbranch_scc0 .LL_153%=
s_cmp_eq_u32 s59, 1
s_cbranch_scc0 .LL_154%=
Ra 36,21,5120
s_branch .LL_153%=
.LL_154%=:
buffer_load_dwordx4 a[36:39], v1, s[8:11], s79 offen offset:1024 sc1
.LL_153%=:
s_bitcmp1_b32 s69, 10
s_cbranch_scc0 .LL_155%=
s_cmp_eq_u32 s59, 2
s_cbranch_scc0 .LL_156%=
Ra 40,21,5120
s_branch .LL_155%=
.LL_156%=:
buffer_load_dwordx4 a[40:43], v1, s[8:11], s79 offen offset:2048 sc1
.LL_155%=:
s_bitcmp1_b32 s69, 11
s_cbranch_scc0 .LL_157%=
s_cmp_eq_u32 s59, 3
s_cbranch_scc0 .LL_158%=
Ra 44,21,5120
s_branch .LL_157%=
.LL_158%=:
buffer_load_dwordx4 a[44:47], v1, s[8:11], s79 offen offset:3072 sc1
.LL_157%=:
s_bitcmp1_b32 s69, 12
s_cbranch_scc0 .LL_159%=
s_cmp_eq_u32 s59, 4
s_cbranch_scc0 .LL_160%=
Ra 48,21,5120
s_branch .LL_159%=
.LL_160%=:
buffer_load_dwordx4 a[48:51], v19, s[8:11], s79 offen offset:0 sc1
.LL_159%=:
s_bitcmp1_b32 s69, 13
s_cbranch_scc0 .LL_161%=
s_cmp_eq_u32 s59, 5
s_cbranch_scc0 .LL_162%=
Ra 52,21,5120
s_branch .LL_161%=
.LL_162%=:
buffer_load_dwordx4 a[52:55], v19, s[8:11], s79 offen offset:1024 sc1
.LL_161%=:
s_bitcmp1_b32 s69, 14
s_cbranch_scc0 .LL_163%=
s_cmp_eq_u32 s59, 6
s_cbranch_scc0 .LL_164%=
Ra 56,21,5120
s_branch .LL_163%=
.LL_164%=:
buffer_load_dwordx4 a[56:59], v19, s[8:11], s79 offen offset:2048 sc1
.LL_163%=:
s_bitcmp1_b32 s69, 15
s_cbranch_scc0 .LL_165%=
s_cmp_eq_u32 s59, 7
s_cbranch_scc0 .LL_166%=
Ra 60,21,5120
s_branch .LL_165%=
.LL_166%=:
buffer_load_dwordx4 a[60:63], v19, s[8:11], s79 offen offset:3072 sc1
.LL_165%=:
.LL_150%=:
s_or_b32 s71, s71, s69
.LL_132%=:
s_andn2_b32 s80, 0xffff, s71
s_cbranch_scc0 .LL_131%=
s_sleep 4
buffer_load_dwordx2 v[16:17], v18, s[8:11], s63 offen sc1
s_and_b32 s80, s70, 0x100ff
s_cmp_eq_u32 s80, 0xff
s_cbranch_scc0 .LL_130%=
W 0
Zvva 80,4,0
Zvva 84,8,0
Mvva 80,4,4
Mvva 84,8,4
Mvva 80,4,8
Mvva 84,8,8
Mvva 80,4,12
Mvva 84,8,12
Mvva 80,4,16
Mvva 84,8,16
Mvva 80,4,20
Mvva 84,8,20
Mvva 80,4,24
Mvva 84,8,24
Mvva 80,4,28
Mvva 84,8,28
s_nop 6
K 160,80,81
K 161,82,83
K 162,84,85
K 163,86,87
v_lshlrev_b32 v80, 16, v160
v_and_b32 v81, 0xffff0000, v160
v_lshlrev_b32 v82, 16, v161
v_and_b32 v83, 0xffff0000, v161
v_lshlrev_b32 v84, 16, v162
v_and_b32 v85, 0xffff0000, v162
v_lshlrev_b32 v86, 16, v163
v_and_b32 v87, 0xffff0000, v163
v_pk_add_f32 v[80:81], v[32:33], v[80:81]
v_pk_add_f32 v[82:83], v[34:35], v[82:83]
v_pk_add_f32 v[84:85], v[36:37], v[84:85]
v_pk_add_f32 v[86:87], v[38:39], v[86:87]
K 160,80,81
K 161,82,83
K 162,84,85
K 163,86,87
v_lshlrev_b32 v80, 16, v160
v_and_b32 v81, 0xffff0000, v160
v_lshlrev_b32 v82, 16, v161
v_and_b32 v83, 0xffff0000, v161
v_lshlrev_b32 v84, 16, v162
v_and_b32 v85, 0xffff0000, v162
v_lshlrev_b32 v86, 16, v163
v_and_b32 v87, 0xffff0000, v163
v_pk_add_f32 v[80:81], v[40:41], v[80:81]
v_pk_add_f32 v[82:83], v[42:43], v[82:83]
v_pk_add_f32 v[84:85], v[44:45], v[84:85]
v_pk_add_f32 v[86:87], v[46:47], v[86:87]
K 80,80,81
K 81,82,83
K 82,84,85
K 83,86,87
buffer_store_dwordx4 v[80:83], v20, s[20:23], s72 offen nt
s_bitset1_b32 s71, 16
.LL_130%=:
s_add_u32 s84, s84, 1
s_waitcnt vmcnt(0)
v_cmp_eq_u64 vcc, s[12:13], v[16:17]
s_or_b32 s67, vcc_lo, s66
s_cmp_lt_u32 s84, 4194304
s_cbranch_scc1 .LL_129%=
.LL_131%=:
s_bitcmp1_b32 s71, 16
s_cbranch_scc1 .LL_167%=
s_and_b32 s80, s70, 0xff
s_cmp_eq_u32 s80, 0xff
s_cbranch_scc1 .LL_168%=
s_waitcnt vmcnt(0)
.LL_168%=:
W 0
Zvva 80,4,0
Zvva 84,8,0
Mvva 80,4,4
Mvva 84,8,4
Mvva 80,4,8
Mvva 84,8,8
Mvva 80,4,12
Mvva 84,8,12
Mvva 80,4,16
Mvva 84,8,16
Mvva 80,4,20
Mvva 84,8,20
Mvva 80,4,24
Mvva 84,8,24
Mvva 80,4,28
Mvva 84,8,28
s_nop 6
K 160,80,81
K 161,82,83
K 162,84,85
K 163,86,87
v_lshlrev_b32 v80, 16, v160
v_and_b32 v81, 0xffff0000, v160
v_lshlrev_b32 v82, 16, v161
v_and_b32 v83, 0xffff0000, v161
v_lshlrev_b32 v84, 16, v162
v_and_b32 v85, 0xffff0000, v162
v_lshlrev_b32 v86, 16, v163
v_and_b32 v87, 0xffff0000, v163
v_pk_add_f32 v[80:81], v[32:33], v[80:81]
v_pk_add_f32 v[82:83], v[34:35], v[82:83]
v_pk_add_f32 v[84:85], v[36:37], v[84:85]
v_pk_add_f32 v[86:87], v[38:39], v[86:87]
)"
R"(K 160,80,81
K 161,82,83
K 162,84,85
K 163,86,87
v_lshlrev_b32 v80, 16, v160
v_and_b32 v81, 0xffff0000, v160
v_lshlrev_b32 v82, 16, v161
v_and_b32 v83, 0xffff0000, v161
v_lshlrev_b32 v84, 16, v162
v_and_b32 v85, 0xffff0000, v162
v_lshlrev_b32 v86, 16, v163
v_and_b32 v87, 0xffff0000, v163
v_pk_add_f32 v[80:81], v[40:41], v[80:81]
v_pk_add_f32 v[82:83], v[42:43], v[82:83]
v_pk_add_f32 v[84:85], v[44:45], v[84:85]
v_pk_add_f32 v[86:87], v[46:47], v[86:87]
K 80,80,81
K 81,82,83
K 82,84,85
K 83,86,87
buffer_store_dwordx4 v[80:83], v20, s[20:23], s72 offen nt
s_waitcnt vmcnt(1)
W 0
Zvva 80,4,32
Zvva 84,8,32
Mvva 80,4,36
Mvva 84,8,36
Mvva 80,4,40
Mvva 84,8,40
Mvva 80,4,44
Mvva 84,8,44
Mvva 80,4,48
Mvva 84,8,48
Mvva 80,4,52
Mvva 84,8,52
Mvva 80,4,56
Mvva 84,8,56
Mvva 80,4,60
Mvva 84,8,60
s_nop 6
K 160,80,81
K 161,82,83
K 162,84,85
K 163,86,87
v_lshlrev_b32 v80, 16, v160
v_and_b32 v81, 0xffff0000, v160
v_lshlrev_b32 v82, 16, v161
v_and_b32 v83, 0xffff0000, v161
v_lshlrev_b32 v84, 16, v162
v_and_b32 v85, 0xffff0000, v162
v_lshlrev_b32 v86, 16, v163
v_and_b32 v87, 0xffff0000, v163
v_pk_add_f32 v[80:81], v[48:49], v[80:81]
v_pk_add_f32 v[82:83], v[50:51], v[82:83]
v_pk_add_f32 v[84:85], v[52:53], v[84:85]
v_pk_add_f32 v[86:87], v[54:55], v[86:87]
K 160,80,81
K 161,82,83
K 162,84,85
K 163,86,87
v_lshlrev_b32 v80, 16, v160
v_and_b32 v81, 0xffff0000, v160
v_lshlrev_b32 v82, 16, v161
v_and_b32 v83, 0xffff0000, v161
v_lshlrev_b32 v84, 16, v162
v_and_b32 v85, 0xffff0000, v162
v_lshlrev_b32 v86, 16, v163
v_and_b32 v87, 0xffff0000, v163
v_pk_add_f32 v[80:81], v[56:57], v[80:81]
v_pk_add_f32 v[82:83], v[58:59], v[82:83]
v_pk_add_f32 v[84:85], v[60:61], v[84:85]
v_pk_add_f32 v[86:87], v[62:63], v[86:87]
K 80,80,81
K 81,82,83
K 82,84,85
K 83,86,87
buffer_store_dwordx4 v[80:83], v20, s[20:23], s73 offen nt
s_branch .LL_126%=
.LL_167%=:
s_waitcnt vmcnt(0)
W 0
Zvva 80,4,32
Zvva 84,8,32
Mvva 80,4,36
Mvva 84,8,36
Mvva 80,4,40
Mvva 84,8,40
Mvva 80,4,44
Mvva 84,8,44
Mvva 80,4,48
Mvva 84,8,48
Mvva 80,4,52
Mvva 84,8,52
Mvva 80,4,56
Mvva 84,8,56
Mvva 80,4,60
Mvva 84,8,60
s_nop 6
K 160,80,81
K 161,82,83
K 162,84,85
K 163,86,87
v_lshlrev_b32 v80, 16, v160
v_and_b32 v81, 0xffff0000, v160
v_lshlrev_b32 v82, 16, v161
v_and_b32 v83, 0xffff0000, v161
v_lshlrev_b32 v84, 16, v162
v_and_b32 v85, 0xffff0000, v162
v_lshlrev_b32 v86, 16, v163
v_and_b32 v87, 0xffff0000, v163
v_pk_add_f32 v[80:81], v[48:49], v[80:81]
v_pk_add_f32 v[82:83], v[50:51], v[82:83]
v_pk_add_f32 v[84:85], v[52:53], v[84:85]
v_pk_add_f32 v[86:87], v[54:55], v[86:87]
K 160,80,81
K 161,82,83
K 162,84,85
K 163,86,87
v_lshlrev_b32 v80, 16, v160
v_and_b32 v81, 0xffff0000, v160
v_lshlrev_b32 v82, 16, v161
v_and_b32 v83, 0xffff0000, v161
v_lshlrev_b32 v84, 16, v162
v_and_b32 v85, 0xffff0000, v162
v_lshlrev_b32 v86, 16, v163
v_and_b32 v87, 0xffff0000, v163
v_pk_add_f32 v[80:81], v[56:57], v[80:81]
v_pk_add_f32 v[82:83], v[58:59], v[82:83]
v_pk_add_f32 v[84:85], v[60:61], v[84:85]
v_pk_add_f32 v[86:87], v[62:63], v[86:87]
K 80,80,81
K 81,82,83
K 82,84,85
K 83,86,87
buffer_store_dwordx4 v[80:83], v20, s[20:23], s73 offen nt
s_branch .LL_126%=
.LL_127%=:
s_mov_b32 s84, 0
.LL_169%=:
s_or_b32 s70, s71, s66
s_andn2_b32 s69, s67, s71
s_and_b32 s69, s69, 0xffff
s_cbranch_scc0 .LL_172%=
s_and_b32 s80, s69, 255
s_cbranch_scc0 .LL_173%=
s_bitcmp1_b32 s69, 0
s_cbranch_scc0 .LL_174%=
s_cmp_eq_u32 s59, 0
s_cbranch_scc0 .LL_175%=
Ra 0,21,0
Ra 216,1,4096
s_branch .LL_174%=
.LL_175%=:
buffer_load_dwordx4 a[0:3], v1, s[8:11], s78 offen offset:0 sc1
buffer_load_dwordx4 a[216:219], v1, s[8:11], s65 offen offset:0 sc1
.LL_174%=:
s_bitcmp1_b32 s69, 1
s_cbranch_scc0 .LL_176%=
s_cmp_eq_u32 s59, 1
s_cbranch_scc0 .LL_177%=
Ra 4,21,0
Ra 220,1,4096
s_branch .LL_176%=
.LL_177%=:
buffer_load_dwordx4 a[4:7], v1, s[8:11], s78 offen offset:1024 sc1
buffer_load_dwordx4 a[220:223], v1, s[8:11], s65 offen offset:1024 sc1
.LL_176%=:
s_bitcmp1_b32 s69, 2
s_cbranch_scc0 .LL_178%=
s_cmp_eq_u32 s59, 2
s_cbranch_scc0 .LL_179%=
Ra 8,21,0
Ra 224,1,4096
s_branch .LL_178%=
.LL_179%=:
buffer_load_dwordx4 a[8:11], v1, s[8:11], s78 offen offset:2048 sc1
buffer_load_dwordx4 a[224:227], v1, s[8:11], s65 offen offset:2048 sc1
.LL_178%=:
s_bitcmp1_b32 s69, 3
s_cbranch_scc0 .LL_180%=
s_cmp_eq_u32 s59, 3
s_cbranch_scc0 .LL_181%=
Ra 12,21,0
Ra 228,1,4096
s_branch .LL_180%=
.LL_181%=:
buffer_load_dwordx4 a[12:15], v1, s[8:11], s78 offen offset:3072 sc1
buffer_load_dwordx4 a[228:231], v1, s[8:11], s65 offen offset:3072 sc1
.LL_180%=:
s_bitcmp1_b32 s69, 4
s_cbranch_scc0 .LL_182%=
s_cmp_eq_u32 s59, 4
s_cbranch_scc0 .LL_183%=
Ra 16,21,0
Ra 232,1,4096
s_branch .LL_182%=
.LL_183%=:
buffer_load_dwordx4 a[16:19], v19, s[8:11], s78 offen offset:0 sc1
buffer_load_dwordx4 a[232:235], v19, s[8:11], s65 offen offset:0 sc1
.LL_182%=:
s_bitcmp1_b32 s69, 5
s_cbranch_scc0 .LL_184%=
s_cmp_eq_u32 s59, 5
s_cbranch_scc0 .LL_185%=
Ra 20,21,0
Ra 236,1,4096
s_branch .LL_184%=
.LL_185%=:
buffer_load_dwordx4 a[20:23], v19, s[8:11], s78 offen offset:1024 sc1
buffer_load_dwordx4 a[236:239], v19, s[8:11], s65 offen offset:1024 sc1
.LL_184%=:
s_bitcmp1_b32 s69, 6
s_cbranch_scc0 .LL_186%=
s_cmp_eq_u32 s59, 6
s_cbranch_scc0 .LL_187%=
Ra 24,21,0
Ra 240,1,4096
s_branch .LL_186%=
.LL_187%=:
buffer_load_dwordx4 a[24:27], v19, s[8:11], s78 offen offset:2048 sc1
buffer_load_dwordx4 a[240:243], v19, s[8:11], s65 offen offset:2048 sc1
.LL_186%=:
s_bitcmp1_b32 s69, 7
s_cbranch_scc0 .LL_188%=
s_cmp_eq_u32 s59, 7
s_cbranch_scc0 .LL_189%=
Ra 28,21,0
Ra 244,1,4096
s_branch .LL_188%=
.LL_189%=:
buffer_load_dwordx4 a[28:31], v19, s[8:11], s78 offen offset:3072 sc1
buffer_load_dwordx4 a[244:247], v19, s[8:11], s65 offen offset:3072 sc1
.LL_188%=:
.LL_173%=:
s_and_b32 s80, s69, 65280
s_cbranch_scc0 .LL_190%=
s_bitcmp1_b32 s69, 8
s_cbranch_scc0 .LL_191%=
s_cmp_eq_u32 s59, 0
s_cbranch_scc0 .LL_192%=
Ra 32,21,5120
s_branch .LL_191%=
.LL_192%=:
buffer_load_dwordx4 a[32:35], v1, s[8:11], s79 offen offset:0 sc1
.LL_191%=:
s_bitcmp1_b32 s69, 9
s_cbranch_scc0 .LL_193%=
s_cmp_eq_u32 s59, 1
s_cbranch_scc0 .LL_194%=
Ra 36,21,5120
s_branch .LL_193%=
.LL_194%=:
buffer_load_dwordx4 a[36:39], v1, s[8:11], s79 offen offset:1024 sc1
.LL_193%=:
s_bitcmp1_b32 s69, 10
s_cbranch_scc0 .LL_195%=
s_cmp_eq_u32 s59, 2
s_cbranch_scc0 .LL_196%=
Ra 40,21,5120
s_branch .LL_195%=
.LL_196%=:
buffer_load_dwordx4 a[40:43], v1, s[8:11], s79 offen offset:2048 sc1
.LL_195%=:
s_bitcmp1_b32 s69, 11
s_cbranch_scc0 .LL_197%=
s_cmp_eq_u32 s59, 3
s_cbranch_scc0 .LL_198%=
Ra 44,21,5120
s_branch .LL_197%=
.LL_198%=:
buffer_load_dwordx4 a[44:47], v1, s[8:11], s79 offen offset:3072 sc1
.LL_197%=:
s_bitcmp1_b32 s69, 12
s_cbranch_scc0 .LL_199%=
s_cmp_eq_u32 s59, 4
s_cbranch_scc0 .LL_200%=
Ra 48,21,5120
s_branch .LL_199%=
.LL_200%=:
buffer_load_dwordx4 a[48:51], v19, s[8:11], s79 offen offset:0 sc1
.LL_199%=:
s_bitcmp1_b32 s69, 13
s_cbranch_scc0 .LL_201%=
s_cmp_eq_u32 s59, 5
s_cbranch_scc0 .LL_202%=
Ra 52,21,5120
s_branch .LL_201%=
.LL_202%=:
buffer_load_dwordx4 a[52:55], v19, s[8:11], s79 offen offset:1024 sc1
.LL_201%=:
s_bitcmp1_b32 s69, 14
s_cbranch_scc0 .LL_203%=
s_cmp_eq_u32 s59, 6
s_cbranch_scc0 .LL_204%=
Ra 56,21,5120
s_branch .LL_203%=
.LL_204%=:
buffer_load_dwordx4 a[56:59], v19, s[8:11], s79 offen offset:2048 sc1
.LL_203%=:
s_bitcmp1_b32 s69, 15
s_cbranch_scc0 .LL_205%=
s_cmp_eq_u32 s59, 7
s_cbranch_scc0 .LL_206%=
Ra 60,21,5120
s_branch .LL_205%=
.LL_206%=:
buffer_load_dwordx4 a[60:63], v19, s[8:11], s79 offen offset:3072 sc1
.LL_205%=:
.LL_190%=:
s_or_b32 s71, s71, s69
.LL_172%=:
s_andn2_b32 s80, 0xffff, s71
s_cbranch_scc0 .LL_171%=
s_sleep 4
buffer_load_dwordx2 v[16:17], v18, s[8:11], s63 offen sc1
s_and_b32 s80, s70, 0x100ff
s_cmp_eq_u32 s80, 0xff
s_cbranch_scc0 .LL_170%=
W 0
Zvva 80,4,0
Zvva 84,8,0
Zvva 88,4,216
Zvva 92,8,216
Mvva 80,4,4
Mvva 84,8,4
Mvva 88,4,220
Mvva 92,8,220
Mvva 80,4,8
Mvva 84,8,8
Mvva 88,4,224
Mvva 92,8,224
Mvva 80,4,12
Mvva 84,8,12
Mvva 88,4,228
Mvva 92,8,228
Mvva 80,4,16
Mvva 84,8,16
Mvva 88,4,232
Mvva 92,8,232
Mvva 80,4,20
Mvva 84,8,20
Mvva 88,4,236
Mvva 92,8,236
Mvva 80,4,24
Mvva 84,8,24
Mvva 88,4,240
Mvva 92,8,240
Mvva 80,4,28
Mvva 84,8,28
Mvva 88,4,244
Mvva 92,8,244
s_nop 4
K 160,80,81
K 161,82,83
K 162,84,85
K 163,86,87
v_lshlrev_b32 v80, 16, v160
v_and_b32 v81, 0xffff0000, v160
v_lshlrev_b32 v82, 16, v161
v_and_b32 v83, 0xffff0000, v161
v_lshlrev_b32 v84, 16, v162
v_and_b32 v85, 0xffff0000, v162
v_lshlrev_b32 v86, 16, v163
v_and_b32 v87, 0xffff0000, v163
v_pk_add_f32 v[80:81], v[32:33], v[80:81]
v_pk_add_f32 v[82:83], v[34:35], v[82:83]
v_pk_add_f32 v[84:85], v[36:37], v[84:85]
v_pk_add_f32 v[86:87], v[38:39], v[86:87]
K 160,80,81
K 161,82,83
K 162,84,85
K 163,86,87
v_lshlrev_b32 v80, 16, v160
v_and_b32 v81, 0xffff0000, v160
v_lshlrev_b32 v82, 16, v161
v_and_b32 v83, 0xffff0000, v161
v_lshlrev_b32 v84, 16, v162
v_and_b32 v85, 0xffff0000, v162
v_lshlrev_b32 v86, 16, v163
v_and_b32 v87, 0xffff0000, v163
v_pk_add_f32 v[80:81], v[40:41], v[80:81]
v_pk_add_f32 v[82:83], v[42:43], v[82:83]
v_pk_add_f32 v[84:85], v[44:45], v[84:85]
v_pk_add_f32 v[86:87], v[46:47], v[86:87]
K 80,80,81
K 81,82,83
K 82,84,85
K 83,86,87
buffer_store_dwordx4 v[80:83], v20, s[20:23], s72 offen nt
K 160,88,89
K 161,90,91
K 162,92,93
K 163,94,95
v_lshlrev_b32 v88, 16, v160
v_and_b32 v89, 0xffff0000, v160
v_lshlrev_b32 v90, 16, v161
v_and_b32 v91, 0xffff0000, v161
v_lshlrev_b32 v92, 16, v162
v_and_b32 v93, 0xffff0000, v162
v_lshlrev_b32 v94, 16, v163
)"
R"(v_and_b32 v95, 0xffff0000, v163
v_pk_add_f32 v[88:89], v[64:65], v[88:89]
v_pk_add_f32 v[90:91], v[66:67], v[90:91]
v_pk_add_f32 v[92:93], v[68:69], v[92:93]
v_pk_add_f32 v[94:95], v[70:71], v[94:95]
K 160,88,89
K 161,90,91
K 162,92,93
K 163,94,95
v_lshlrev_b32 v88, 16, v160
v_and_b32 v89, 0xffff0000, v160
v_lshlrev_b32 v90, 16, v161
v_and_b32 v91, 0xffff0000, v161
v_lshlrev_b32 v92, 16, v162
v_and_b32 v93, 0xffff0000, v162
v_lshlrev_b32 v94, 16, v163
v_and_b32 v95, 0xffff0000, v163
v_pk_add_f32 v[88:89], v[72:73], v[88:89]
v_pk_add_f32 v[90:91], v[74:75], v[90:91]
v_pk_add_f32 v[92:93], v[76:77], v[92:93]
v_pk_add_f32 v[94:95], v[78:79], v[94:95]
K 88,88,89
K 89,90,91
K 90,92,93
K 91,94,95
buffer_store_dwordx4 v[88:91], v20, s[20:23], s74 offen nt
s_bitset1_b32 s71, 16
.LL_170%=:
s_add_u32 s84, s84, 1
s_waitcnt vmcnt(0)
v_cmp_eq_u64 vcc, s[12:13], v[16:17]
s_or_b32 s67, vcc_lo, s66
s_cmp_lt_u32 s84, 4194304
s_cbranch_scc1 .LL_169%=
.LL_171%=:
s_bitcmp1_b32 s71, 16
s_cbranch_scc1 .LL_207%=
s_and_b32 s80, s70, 0xff
s_cmp_eq_u32 s80, 0xff
s_cbranch_scc1 .LL_208%=
s_waitcnt vmcnt(0)
.LL_208%=:
W 0
Zvva 80,4,0
Zvva 84,8,0
Zvva 88,4,216
Zvva 92,8,216
Mvva 80,4,4
Mvva 84,8,4
Mvva 88,4,220
Mvva 92,8,220
Mvva 80,4,8
Mvva 84,8,8
Mvva 88,4,224
Mvva 92,8,224
Mvva 80,4,12
Mvva 84,8,12
Mvva 88,4,228
Mvva 92,8,228
Mvva 80,4,16
Mvva 84,8,16
Mvva 88,4,232
Mvva 92,8,232
Mvva 80,4,20
Mvva 84,8,20
Mvva 88,4,236
Mvva 92,8,236
Mvva 80,4,24
Mvva 84,8,24
Mvva 88,4,240
Mvva 92,8,240
Mvva 80,4,28
Mvva 84,8,28
Mvva 88,4,244
Mvva 92,8,244
s_nop 4
K 160,80,81
K 161,82,83
K 162,84,85
K 163,86,87
v_lshlrev_b32 v80, 16, v160
v_and_b32 v81, 0xffff0000, v160
v_lshlrev_b32 v82, 16, v161
v_and_b32 v83, 0xffff0000, v161
v_lshlrev_b32 v84, 16, v162
v_and_b32 v85, 0xffff0000, v162
v_lshlrev_b32 v86, 16, v163
v_and_b32 v87, 0xffff0000, v163
v_pk_add_f32 v[80:81], v[32:33], v[80:81]
v_pk_add_f32 v[82:83], v[34:35], v[82:83]
v_pk_add_f32 v[84:85], v[36:37], v[84:85]
v_pk_add_f32 v[86:87], v[38:39], v[86:87]
K 160,80,81
K 161,82,83
K 162,84,85
K 163,86,87
v_lshlrev_b32 v80, 16, v160
v_and_b32 v81, 0xffff0000, v160
v_lshlrev_b32 v82, 16, v161
v_and_b32 v83, 0xffff0000, v161
v_lshlrev_b32 v84, 16, v162
v_and_b32 v85, 0xffff0000, v162
v_lshlrev_b32 v86, 16, v163
v_and_b32 v87, 0xffff0000, v163
v_pk_add_f32 v[80:81], v[40:41], v[80:81]
v_pk_add_f32 v[82:83], v[42:43], v[82:83]
v_pk_add_f32 v[84:85], v[44:45], v[84:85]
v_pk_add_f32 v[86:87], v[46:47], v[86:87]
K 80,80,81
K 81,82,83
K 82,84,85
K 83,86,87
buffer_store_dwordx4 v[80:83], v20, s[20:23], s72 offen nt
K 160,88,89
K 161,90,91
K 162,92,93
K 163,94,95
v_lshlrev_b32 v88, 16, v160
v_and_b32 v89, 0xffff0000, v160
v_lshlrev_b32 v90, 16, v161
v_and_b32 v91, 0xffff0000, v161
v_lshlrev_b32 v92, 16, v162
v_and_b32 v93, 0xffff0000, v162
v_lshlrev_b32 v94, 16, v163
v_and_b32 v95, 0xffff0000, v163
v_pk_add_f32 v[88:89], v[64:65], v[88:89]
v_pk_add_f32 v[90:91], v[66:67], v[90:91]
v_pk_add_f32 v[92:93], v[68:69], v[92:93]
v_pk_add_f32 v[94:95], v[70:71], v[94:95]
K 160,88,89
K 161,90,91
K 162,92,93
K 163,94,95
v_lshlrev_b32 v88, 16, v160
v_and_b32 v89, 0xffff0000, v160
v_lshlrev_b32 v90, 16, v161
v_and_b32 v91, 0xffff0000, v161
v_lshlrev_b32 v92, 16, v162
v_and_b32 v93, 0xffff0000, v162
v_lshlrev_b32 v94, 16, v163
v_and_b32 v95, 0xffff0000, v163
v_pk_add_f32 v[88:89], v[72:73], v[88:89]
v_pk_add_f32 v[90:91], v[74:75], v[90:91]
v_pk_add_f32 v[92:93], v[76:77], v[92:93]
v_pk_add_f32 v[94:95], v[78:79], v[94:95]
K 88,88,89
K 89,90,91
K 90,92,93
K 91,94,95
buffer_store_dwordx4 v[88:91], v20, s[20:23], s74 offen nt
s_waitcnt vmcnt(2)
W 0
Zvva 80,4,32
Zvva 84,8,32
Mvva 80,4,36
Mvva 84,8,36
Mvva 80,4,40
Mvva 84,8,40
Mvva 80,4,44
Mvva 84,8,44
Mvva 80,4,48
Mvva 84,8,48
Mvva 80,4,52
Mvva 84,8,52
Mvva 80,4,56
Mvva 84,8,56
Mvva 80,4,60
Mvva 84,8,60
s_nop 6
K 160,80,81
K 161,82,83
K 162,84,85
K 163,86,87
v_lshlrev_b32 v80, 16, v160
v_and_b32 v81, 0xffff0000, v160
v_lshlrev_b32 v82, 16, v161
v_and_b32 v83, 0xffff0000, v161
v_lshlrev_b32 v84, 16, v162
v_and_b32 v85, 0xffff0000, v162
v_lshlrev_b32 v86, 16, v163
v_and_b32 v87, 0xffff0000, v163
v_pk_add_f32 v[80:81], v[48:49], v[80:81]
v_pk_add_f32 v[82:83], v[50:51], v[82:83]
v_pk_add_f32 v[84:85], v[52:53], v[84:85]
v_pk_add_f32 v[86:87], v[54:55], v[86:87]
K 160,80,81
K 161,82,83
K 162,84,85
K 163,86,87
v_lshlrev_b32 v80, 16, v160
v_and_b32 v81, 0xffff0000, v160
v_lshlrev_b32 v82, 16, v161
v_and_b32 v83, 0xffff0000, v161
v_lshlrev_b32 v84, 16, v162
v_and_b32 v85, 0xffff0000, v162
v_lshlrev_b32 v86, 16, v163
v_and_b32 v87, 0xffff0000, v163
v_pk_add_f32 v[80:81], v[56:57], v[80:81]
v_pk_add_f32 v[82:83], v[58:59], v[82:83]
v_pk_add_f32 v[84:85], v[60:61], v[84:85]
v_pk_add_f32 v[86:87], v[62:63], v[86:87]
K 80,80,81
K 81,82,83
K 82,84,85
K 83,86,87
buffer_store_dwordx4 v[80:83], v20, s[20:23], s73 offen nt
s_branch .LL_126%=
.LL_207%=:
s_waitcnt vmcnt(0)
W 0
Zvva 80,4,32
Zvva 84,8,32
Mvva 80,4,36
Mvva 84,8,36
Mvva 80,4,40
Mvva 84,8,40
Mvva 80,4,44
Mvva 84,8,44
Mvva 80,4,48
Mvva 84,8,48
Mvva 80,4,52
Mvva 84,8,52
Mvva 80,4,56
Mvva 84,8,56
Mvva 80,4,60
Mvva 84,8,60
s_nop 6
K 160,80,81
K 161,82,83
K 162,84,85
K 163,86,87
v_lshlrev_b32 v80, 16, v160
v_and_b32 v81, 0xffff0000, v160
v_lshlrev_b32 v82, 16, v161
v_and_b32 v83, 0xffff0000, v161
v_lshlrev_b32 v84, 16, v162
v_and_b32 v85, 0xffff0000, v162
v_lshlrev_b32 v86, 16, v163
v_and_b32 v87, 0xffff0000, v163
v_pk_add_f32 v[80:81], v[48:49], v[80:81]
v_pk_add_f32 v[82:83], v[50:51], v[82:83]
v_pk_add_f32 v[84:85], v[52:53], v[84:85]
v_pk_add_f32 v[86:87], v[54:55], v[86:87]
K 160,80,81
K 161,82,83
K 162,84,85
K 163,86,87
v_lshlrev_b32 v80, 16, v160
v_and_b32 v81, 0xffff0000, v160
v_lshlrev_b32 v82, 16, v161
v_and_b32 v83, 0xffff0000, v161
v_lshlrev_b32 v84, 16, v162
v_and_b32 v85, 0xffff0000, v162
v_lshlrev_b32 v86, 16, v163
v_and_b32 v87, 0xffff0000, v163
v_pk_add_f32 v[80:81], v[56:57], v[80:81]
v_pk_add_f32 v[82:83], v[58:59], v[82:83]
v_pk_add_f32 v[84:85], v[60:61], v[84:85]
v_pk_add_f32 v[86:87], v[62:63], v[86:87]
K 80,80,81
K 81,82,83
K 82,84,85
K 83,86,87
buffer_store_dwordx4 v[80:83], v20, s[20:23], s73 offen nt
s_branch .LL_126%=
.LL_128%=:
s_mov_b32 s84, 0
.LL_209%=:
s_or_b32 s70, s71, s66
s_andn2_b32 s69, s67, s71
s_and_b32 s69, s69, 0xffff
s_cbranch_scc0 .LL_212%=
s_and_b32 s80, s69, 255
s_cbranch_scc0 .LL_213%=
s_bitcmp1_b32 s69, 0
s_cbranch_scc0 .LL_214%=
s_cmp_eq_u32 s59, 0
s_cbranch_scc0 .LL_215%=
Ra 0,21,0
s_branch .LL_214%=
.LL_215%=:
buffer_load_dwordx4 a[0:3], v1, s[8:11], s78 offen offset:0 sc1
.LL_214%=:
s_bitcmp1_b32 s69, 1
s_cbranch_scc0 .LL_216%=
s_cmp_eq_u32 s59, 1
s_cbranch_scc0 .LL_217%=
Ra 4,21,0
s_branch .LL_216%=
.LL_217%=:
buffer_load_dwordx4 a[4:7], v1, s[8:11], s78 offen offset:1024 sc1
.LL_216%=:
s_bitcmp1_b32 s69, 2
s_cbranch_scc0 .LL_218%=
s_cmp_eq_u32 s59, 2
s_cbranch_scc0 .LL_219%=
Ra 8,21,0
s_branch .LL_218%=
.LL_219%=:
buffer_load_dwordx4 a[8:11], v1, s[8:11], s78 offen offset:2048 sc1
.LL_218%=:
s_bitcmp1_b32 s69, 3
s_cbranch_scc0 .LL_220%=
s_cmp_eq_u32 s59, 3
s_cbranch_scc0 .LL_221%=
Ra 12,21,0
s_branch .LL_220%=
.LL_221%=:
buffer_load_dwordx4 a[12:15], v1, s[8:11], s78 offen offset:3072 sc1
.LL_220%=:
s_bitcmp1_b32 s69, 4
s_cbranch_scc0 .LL_222%=
s_cmp_eq_u32 s59, 4
s_cbranch_scc0 .LL_223%=
Ra 16,21,0
s_branch .LL_222%=
.LL_223%=:
buffer_load_dwordx4 a[16:19], v19, s[8:11], s78 offen offset:0 sc1
.LL_222%=:
s_bitcmp1_b32 s69, 5
s_cbranch_scc0 .LL_224%=
s_cmp_eq_u32 s59, 5
s_cbranch_scc0 .LL_225%=
Ra 20,21,0
s_branch .LL_224%=
.LL_225%=:
buffer_load_dwordx4 a[20:23], v19, s[8:11], s78 offen offset:1024 sc1
.LL_224%=:
s_bitcmp1_b32 s69, 6
s_cbranch_scc0 .LL_226%=
s_cmp_eq_u32 s59, 6
s_cbranch_scc0 .LL_227%=
Ra 24,21,0
s_branch .LL_226%=
.LL_227%=:
buffer_load_dwordx4 a[24:27], v19, s[8:11], s78 offen offset:2048 sc1
.LL_226%=:
s_bitcmp1_b32 s69, 7
s_cbranch_scc0 .LL_228%=
s_cmp_eq_u32 s59, 7
s_cbranch_scc0 .LL_229%=
Ra 28,21,0
s_branch .LL_228%=
.LL_229%=:
buffer_load_dwordx4 a[28:31], v19, s[8:11], s78 offen offset:3072 sc1
.LL_228%=:
.LL_213%=:
s_and_b32 s80, s69, 65280
s_cbranch_scc0 .LL_230%=
s_bitcmp1_b32 s69, 8
s_cbranch_scc0 .LL_231%=
s_cmp_eq_u32 s59, 0
s_cbranch_scc0 .LL_232%=
Ra 32,21,5120
Ra 216,1,9216
s_branch .LL_231%=
.LL_232%=:
buffer_load_dwordx4 a[32:35], v1, s[8:11], s79 offen offset:0 sc1
buffer_load_dwordx4 a[216:219], v1, s[8:11], s65 offen offset:0 sc1
.LL_231%=:
s_bitcmp1_b32 s69, 9
s_cbranch_scc0 .LL_233%=
s_cmp_eq_u32 s59, 1
s_cbranch_scc0 .LL_234%=
Ra 36,21,5120
Ra 220,1,9216
s_branch .LL_233%=
.LL_234%=:
buffer_load_dwordx4 a[36:39], v1, s[8:11], s79 offen offset:1024 sc1
buffer_load_dwordx4 a[220:223], v1, s[8:11], s65 offen offset:1024 sc1
.LL_233%=:
s_bitcmp1_b32 s69, 10
s_cbranch_scc0 .LL_235%=
s_cmp_eq_u32 s59, 2
s_cbranch_scc0 .LL_236%=
Ra 40,21,5120
Ra 224,1,9216
s_branch .LL_235%=
.LL_236%=:
buffer_load_dwordx4 a[40:43], v1, s[8:11], s79 offen offset:2048 sc1
buffer_load_dwordx4 a[224:227], v1, s[8:11], s65 offen offset:2048 sc1
.LL_235%=:
s_bitcmp1_b32 s69, 11
s_cbranch_scc0 .LL_237%=
s_cmp_eq_u32 s59, 3
s_cbranch_scc0 .LL_238%=
Ra 44,21,5120
Ra 228,1,9216
s_branch .LL_237%=
.LL_238%=:
buffer_load_dwordx4 a[44:47], v1, s[8:11], s79 offen offset:3072 sc1
buffer_load_dwordx4 a[228:231], v1, s[8:11], s65 offen offset:3072 sc1
.LL_237%=:
s_bitcmp1_b32 s69, 12
s_cbranch_scc0 .LL_239%=
s_cmp_eq_u32 s59, 4
s_cbranch_scc0 .LL_240%=
Ra 48,21,5120
Ra 232,1,9216
s_branch .LL_239%=
)"
R"(.LL_240%=:
buffer_load_dwordx4 a[48:51], v19, s[8:11], s79 offen offset:0 sc1
buffer_load_dwordx4 a[232:235], v19, s[8:11], s65 offen offset:0 sc1
.LL_239%=:
s_bitcmp1_b32 s69, 13
s_cbranch_scc0 .LL_241%=
s_cmp_eq_u32 s59, 5
s_cbranch_scc0 .LL_242%=
Ra 52,21,5120
Ra 236,1,9216
s_branch .LL_241%=
.LL_242%=:
buffer_load_dwordx4 a[52:55], v19, s[8:11], s79 offen offset:1024 sc1
buffer_load_dwordx4 a[236:239], v19, s[8:11], s65 offen offset:1024 sc1
.LL_241%=:
s_bitcmp1_b32 s69, 14
s_cbranch_scc0 .LL_243%=
s_cmp_eq_u32 s59, 6
s_cbranch_scc0 .LL_244%=
Ra 56,21,5120
Ra 240,1,9216
s_branch .LL_243%=
.LL_244%=:
buffer_load_dwordx4 a[56:59], v19, s[8:11], s79 offen offset:2048 sc1
buffer_load_dwordx4 a[240:243], v19, s[8:11], s65 offen offset:2048 sc1
.LL_243%=:
s_bitcmp1_b32 s69, 15
s_cbranch_scc0 .LL_245%=
s_cmp_eq_u32 s59, 7
s_cbranch_scc0 .LL_246%=
Ra 60,21,5120
Ra 244,1,9216
s_branch .LL_245%=
.LL_246%=:
buffer_load_dwordx4 a[60:63], v19, s[8:11], s79 offen offset:3072 sc1
buffer_load_dwordx4 a[244:247], v19, s[8:11], s65 offen offset:3072 sc1
.LL_245%=:
.LL_230%=:
s_or_b32 s71, s71, s69
.LL_212%=:
s_andn2_b32 s80, 0xffff, s71
s_cbranch_scc0 .LL_211%=
s_sleep 4
buffer_load_dwordx2 v[16:17], v18, s[8:11], s63 offen sc1
s_and_b32 s80, s70, 0x100ff
s_cmp_eq_u32 s80, 0xff
s_cbranch_scc0 .LL_210%=
W 0
Zvva 80,4,0
Zvva 84,8,0
Mvva 80,4,4
Mvva 84,8,4
Mvva 80,4,8
Mvva 84,8,8
Mvva 80,4,12
Mvva 84,8,12
Mvva 80,4,16
Mvva 84,8,16
Mvva 80,4,20
Mvva 84,8,20
Mvva 80,4,24
Mvva 84,8,24
Mvva 80,4,28
Mvva 84,8,28
s_nop 6
K 160,80,81
K 161,82,83
K 162,84,85
K 163,86,87
v_lshlrev_b32 v80, 16, v160
v_and_b32 v81, 0xffff0000, v160
v_lshlrev_b32 v82, 16, v161
v_and_b32 v83, 0xffff0000, v161
v_lshlrev_b32 v84, 16, v162
v_and_b32 v85, 0xffff0000, v162
v_lshlrev_b32 v86, 16, v163
v_and_b32 v87, 0xffff0000, v163
v_pk_add_f32 v[80:81], v[32:33], v[80:81]
v_pk_add_f32 v[82:83], v[34:35], v[82:83]
v_pk_add_f32 v[84:85], v[36:37], v[84:85]
v_pk_add_f32 v[86:87], v[38:39], v[86:87]
K 160,80,81
K 161,82,83
K 162,84,85
K 163,86,87
v_lshlrev_b32 v80, 16, v160
v_and_b32 v81, 0xffff0000, v160
v_lshlrev_b32 v82, 16, v161
v_and_b32 v83, 0xffff0000, v161
v_lshlrev_b32 v84, 16, v162
v_and_b32 v85, 0xffff0000, v162
v_lshlrev_b32 v86, 16, v163
v_and_b32 v87, 0xffff0000, v163
v_pk_add_f32 v[80:81], v[40:41], v[80:81]
v_pk_add_f32 v[82:83], v[42:43], v[82:83]
v_pk_add_f32 v[84:85], v[44:45], v[84:85]
v_pk_add_f32 v[86:87], v[46:47], v[86:87]
K 80,80,81
K 81,82,83
K 82,84,85
K 83,86,87
buffer_store_dwordx4 v[80:83], v20, s[20:23], s72 offen nt
s_bitset1_b32 s71, 16
.LL_210%=:
s_add_u32 s84, s84, 1
s_waitcnt vmcnt(0)
v_cmp_eq_u64 vcc, s[12:13], v[16:17]
s_or_b32 s67, vcc_lo, s66
s_cmp_lt_u32 s84, 4194304
s_cbranch_scc1 .LL_209%=
.LL_211%=:
s_bitcmp1_b32 s71, 16
s_cbranch_scc1 .LL_247%=
s_and_b32 s80, s70, 0xff
s_cmp_eq_u32 s80, 0xff
s_cbranch_scc1 .LL_248%=
s_waitcnt vmcnt(0)
.LL_248%=:
W 0
Zvva 80,4,0
Zvva 84,8,0
Mvva 80,4,4
Mvva 84,8,4
Mvva 80,4,8
Mvva 84,8,8
Mvva 80,4,12
Mvva 84,8,12
Mvva 80,4,16
Mvva 84,8,16
Mvva 80,4,20
Mvva 84,8,20
Mvva 80,4,24
Mvva 84,8,24
Mvva 80,4,28
Mvva 84,8,28
s_nop 6
K 160,80,81
K 161,82,83
K 162,84,85
K 163,86,87
v_lshlrev_b32 v80, 16, v160
v_and_b32 v81, 0xffff0000, v160
v_lshlrev_b32 v82, 16, v161
v_and_b32 v83, 0xffff0000, v161
v_lshlrev_b32 v84, 16, v162
v_and_b32 v85, 0xffff0000, v162
v_lshlrev_b32 v86, 16, v163
v_and_b32 v87, 0xffff0000, v163
v_pk_add_f32 v[80:81], v[32:33], v[80:81]
v_pk_add_f32 v[82:83], v[34:35], v[82:83]
v_pk_add_f32 v[84:85], v[36:37], v[84:85]
v_pk_add_f32 v[86:87], v[38:39], v[86:87]
K 160,80,81
K 161,82,83
K 162,84,85
K 163,86,87
v_lshlrev_b32 v80, 16, v160
v_and_b32 v81, 0xffff0000, v160
v_lshlrev_b32 v82, 16, v161
v_and_b32 v83, 0xffff0000, v161
v_lshlrev_b32 v84, 16, v162
v_and_b32 v85, 0xffff0000, v162
v_lshlrev_b32 v86, 16, v163
v_and_b32 v87, 0xffff0000, v163
v_pk_add_f32 v[80:81], v[40:41], v[80:81]
v_pk_add_f32 v[82:83], v[42:43], v[82:83]
v_pk_add_f32 v[84:85], v[44:45], v[84:85]
v_pk_add_f32 v[86:87], v[46:47], v[86:87]
K 80,80,81
K 81,82,83
K 82,84,85
K 83,86,87
buffer_store_dwordx4 v[80:83], v20, s[20:23], s72 offen nt
s_waitcnt vmcnt(1)
W 0
Zvva 80,4,32
Zvva 84,8,32
Zvva 88,4,216
Zvva 92,8,216
Mvva 80,4,36
Mvva 84,8,36
Mvva 88,4,220
Mvva 92,8,220
Mvva 80,4,40
Mvva 84,8,40
Mvva 88,4,224
Mvva 92,8,224
Mvva 80,4,44
Mvva 84,8,44
Mvva 88,4,228
Mvva 92,8,228
Mvva 80,4,48
Mvva 84,8,48
Mvva 88,4,232
Mvva 92,8,232
Mvva 80,4,52
Mvva 84,8,52
Mvva 88,4,236
Mvva 92,8,236
Mvva 80,4,56
Mvva 84,8,56
Mvva 88,4,240
Mvva 92,8,240
Mvva 80,4,60
Mvva 84,8,60
Mvva 88,4,244
Mvva 92,8,244
s_nop 4
K 160,80,81
K 161,82,83
K 162,84,85
K 163,86,87
v_lshlrev_b32 v80, 16, v160
v_and_b32 v81, 0xffff0000, v160
v_lshlrev_b32 v82, 16, v161
v_and_b32 v83, 0xffff0000, v161
v_lshlrev_b32 v84, 16, v162
v_and_b32 v85, 0xffff0000, v162
v_lshlrev_b32 v86, 16, v163
v_and_b32 v87, 0xffff0000, v163
v_pk_add_f32 v[80:81], v[48:49], v[80:81]
v_pk_add_f32 v[82:83], v[50:51], v[82:83]
v_pk_add_f32 v[84:85], v[52:53], v[84:85]
v_pk_add_f32 v[86:87], v[54:55], v[86:87]
K 160,80,81
K 161,82,83
K 162,84,85
K 163,86,87
v_lshlrev_b32 v80, 16, v160
v_and_b32 v81, 0xffff0000, v160
v_lshlrev_b32 v82, 16, v161
v_and_b32 v83, 0xffff0000, v161
v_lshlrev_b32 v84, 16, v162
v_and_b32 v85, 0xffff0000, v162
v_lshlrev_b32 v86, 16, v163
v_and_b32 v87, 0xffff0000, v163
v_pk_add_f32 v[80:81], v[56:57], v[80:81]
v_pk_add_f32 v[82:83], v[58:59], v[82:83]
v_pk_add_f32 v[84:85], v[60:61], v[84:85]
v_pk_add_f32 v[86:87], v[62:63], v[86:87]
K 80,80,81
K 81,82,83
K 82,84,85
K 83,86,87
buffer_store_dwordx4 v[80:83], v20, s[20:23], s73 offen nt
K 160,88,89
K 161,90,91
K 162,92,93
K 163,94,95
v_lshlrev_b32 v88, 16, v160
v_and_b32 v89, 0xffff0000, v160
v_lshlrev_b32 v90, 16, v161
v_and_b32 v91, 0xffff0000, v161
v_lshlrev_b32 v92, 16, v162
v_and_b32 v93, 0xffff0000, v162
v_lshlrev_b32 v94, 16, v163
v_and_b32 v95, 0xffff0000, v163
v_pk_add_f32 v[88:89], v[64:65], v[88:89]
v_pk_add_f32 v[90:91], v[66:67], v[90:91]
v_pk_add_f32 v[92:93], v[68:69], v[92:93]
v_pk_add_f32 v[94:95], v[70:71], v[94:95]
K 160,88,89
K 161,90,91
K 162,92,93
K 163,94,95
v_lshlrev_b32 v88, 16, v160
v_and_b32 v89, 0xffff0000, v160
v_lshlrev_b32 v90, 16, v161
v_and_b32 v91, 0xffff0000, v161
v_lshlrev_b32 v92, 16, v162
v_and_b32 v93, 0xffff0000, v162
v_lshlrev_b32 v94, 16, v163
v_and_b32 v95, 0xffff0000, v163
v_pk_add_f32 v[88:89], v[72:73], v[88:89]
v_pk_add_f32 v[90:91], v[74:75], v[90:91]
v_pk_add_f32 v[92:93], v[76:77], v[92:93]
v_pk_add_f32 v[94:95], v[78:79], v[94:95]
K 88,88,89
K 89,90,91
K 90,92,93
K 91,94,95
buffer_store_dwordx4 v[88:91], v20, s[20:23], s74 offen nt
s_branch .LL_126%=
.LL_247%=:
s_waitcnt vmcnt(0)
W 0
Zvva 80,4,32
Zvva 84,8,32
Zvva 88,4,216
Zvva 92,8,216
Mvva 80,4,36
Mvva 84,8,36
Mvva 88,4,220
Mvva 92,8,220
Mvva 80,4,40
Mvva 84,8,40
Mvva 88,4,224
Mvva 92,8,224
Mvva 80,4,44
Mvva 84,8,44
Mvva 88,4,228
Mvva 92,8,228
Mvva 80,4,48
Mvva 84,8,48
Mvva 88,4,232
Mvva 92,8,232
Mvva 80,4,52
Mvva 84,8,52
Mvva 88,4,236
Mvva 92,8,236
Mvva 80,4,56
Mvva 84,8,56
Mvva 88,4,240
Mvva 92,8,240
Mvva 80,4,60
Mvva 84,8,60
Mvva 88,4,244
Mvva 92,8,244
s_nop 4
K 160,80,81
K 161,82,83
K 162,84,85
K 163,86,87
v_lshlrev_b32 v80, 16, v160
v_and_b32 v81, 0xffff0000, v160
v_lshlrev_b32 v82, 16, v161
v_and_b32 v83, 0xffff0000, v161
v_lshlrev_b32 v84, 16, v162
v_and_b32 v85, 0xffff0000, v162
v_lshlrev_b32 v86, 16, v163
v_and_b32 v87, 0xffff0000, v163
v_pk_add_f32 v[80:81], v[48:49], v[80:81]
v_pk_add_f32 v[82:83], v[50:51], v[82:83]
v_pk_add_f32 v[84:85], v[52:53], v[84:85]
v_pk_add_f32 v[86:87], v[54:55], v[86:87]
K 160,80,81
K 161,82,83
K 162,84,85
K 163,86,87
v_lshlrev_b32 v80, 16, v160
v_and_b32 v81, 0xffff0000, v160
v_lshlrev_b32 v82, 16, v161
v_and_b32 v83, 0xffff0000, v161
v_lshlrev_b32 v84, 16, v162
v_and_b32 v85, 0xffff0000, v162
v_lshlrev_b32 v86, 16, v163
v_and_b32 v87, 0xffff0000, v163
v_pk_add_f32 v[80:81], v[56:57], v[80:81]
v_pk_add_f32 v[82:83], v[58:59], v[82:83]
v_pk_add_f32 v[84:85], v[60:61], v[84:85]
v_pk_add_f32 v[86:87], v[62:63], v[86:87]
K 80,80,81
K 81,82,83
K 82,84,85
K 83,86,87
buffer_store_dwordx4 v[80:83], v20, s[20:23], s73 offen nt
K 160,88,89
K 161,90,91
K 162,92,93
K 163,94,95
v_lshlrev_b32 v88, 16, v160
v_and_b32 v89, 0xffff0000, v160
v_lshlrev_b32 v90, 16, v161
v_and_b32 v91, 0xffff0000, v161
v_lshlrev_b32 v92, 16, v162
v_and_b32 v93, 0xffff0000, v162
v_lshlrev_b32 v94, 16, v163
v_and_b32 v95, 0xffff0000, v163
v_pk_add_f32 v[88:89], v[64:65], v[88:89]
v_pk_add_f32 v[90:91], v[66:67], v[90:91]
v_pk_add_f32 v[92:93], v[68:69], v[92:93]
v_pk_add_f32 v[94:95], v[70:71], v[94:95]
K 160,88,89
K 161,90,91
K 162,92,93
K 163,94,95
v_lshlrev_b32 v88, 16, v160
v_and_b32 v89, 0xffff0000, v160
v_lshlrev_b32 v90, 16, v161
v_and_b32 v91, 0xffff0000, v161
v_lshlrev_b32 v92, 16, v162
v_and_b32 v93, 0xffff0000, v162
v_lshlrev_b32 v94, 16, v163
v_and_b32 v95, 0xffff0000, v163
v_pk_add_f32 v[88:89], v[72:73], v[88:89]
v_pk_add_f32 v[90:91], v[74:75], v[90:91]
v_pk_add_f32 v[92:93], v[76:77], v[92:93]
v_pk_add_f32 v[94:95], v[78:79], v[94:95]
K 88,88,89
K 89,90,91
K 90,92,93
K 91,94,95
buffer_store_dwordx4 v[88:91], v20, s[20:23], s74 offen nt
.LL_126%=:
s_lshl_b32 s80, s15, 2
s_add_u32 s80, s80, s14
s_cmp_eq_u32 s80, 4
s_cbranch_scc0 .LL_249%=
v_mov_b64 v[28:29], s[12:13]
s_mov_b64 exec, 1
)"
R"(buffer_store_dwordx2 v[28:29], off, s[8:11], 0 sc1
s_mov_b64 exec, -1
.LL_249%=:
s_branch .Lend_250%=
.Lup_119%=:
s_waitcnt vmcnt(3)
s_mov_b32 s84, 0
.LL_121%=:
v_min3_u32 v176, v101, v103, v109
v_max3_u32 v177, v101, v103, v109
v_min3_u32 v176, v176, v111, v117
v_max3_u32 v177, v177, v111, v117
v_min3_u32 v176, v176, v119, v125
v_max3_u32 v177, v177, v119, v125
v_min_u32 v176, v176, v127
v_max_u32 v177, v177, v127
v_cmp_eq_u32 vcc, s13, v176
v_cmp_eq_u32_e64 s[86:87], s13, v177
s_and_b64 vcc, vcc, s[86:87]
s_cmp_eq_u64 vcc, -1
s_cbranch_scc1 .LL_122%=
s_add_u32 s84, s84, 1
s_cmp_lt_u32 s84, 1048576
s_cbranch_scc1 .LL_123%=
.LL_122%=:
v_readlane_b32 s70, v12, s69
v_bfe_u32 v2, v1, 4, 4
s_nop 2
s_lshr_b32 s80, s70, 4
s_and_b32 s70, s70, 15
v_cmp_eq_u32 vcc, s70, v2
s_cmp_eq_u32 s80, s61
s_cselect_b64 s[86:87], vcc, 0
s_cselect_b64 s[88:89], 0, vcc
s_bitcmp1_b32 s14, 0
s_cselect_b64 vcc, s[88:89], s[86:87]
v_lshlrev_b32 v176, 16, v100
v_and_b32 v177, 0xffff0000, v100
v_lshlrev_b32 v178, 16, v102
v_and_b32 v179, 0xffff0000, v102
v_lshlrev_b32 v180, 16, v108
v_and_b32 v181, 0xffff0000, v108
v_lshlrev_b32 v182, 16, v110
v_and_b32 v183, 0xffff0000, v110
A 176,176,40
A 177,177,41
A 178,178,42
A 179,179,43
A 180,180,44
A 181,181,45
A 182,182,46
A 183,183,47
K 184,176,177
K 185,178,179
K 186,180,181
K 187,182,183
v_lshlrev_b32 v176, 16, v184
v_and_b32 v177, 0xffff0000, v184
v_lshlrev_b32 v178, 16, v185
v_and_b32 v179, 0xffff0000, v185
v_lshlrev_b32 v180, 16, v186
v_and_b32 v181, 0xffff0000, v186
v_lshlrev_b32 v182, 16, v187
v_and_b32 v183, 0xffff0000, v187
v_cndmask_b32_e64 v40, v40, v176, s[86:87]
v_cndmask_b32_e64 v41, v41, v177, s[86:87]
v_cndmask_b32_e64 v42, v42, v178, s[86:87]
v_cndmask_b32_e64 v43, v43, v179, s[86:87]
v_cndmask_b32_e64 v44, v44, v180, s[86:87]
v_cndmask_b32_e64 v45, v45, v181, s[86:87]
v_cndmask_b32_e64 v46, v46, v182, s[86:87]
v_cndmask_b32_e64 v47, v47, v183, s[86:87]
v_lshlrev_b32 v176, 16, v100
v_and_b32 v177, 0xffff0000, v100
v_lshlrev_b32 v178, 16, v102
v_and_b32 v179, 0xffff0000, v102
v_lshlrev_b32 v180, 16, v108
v_and_b32 v181, 0xffff0000, v108
v_lshlrev_b32 v182, 16, v110
v_and_b32 v183, 0xffff0000, v110
A 176,176,56
A 177,177,57
A 178,178,58
A 179,179,59
A 180,180,60
A 181,181,61
A 182,182,62
A 183,183,63
K 184,176,177
K 185,178,179
K 186,180,181
K 187,182,183
v_lshlrev_b32 v176, 16, v184
v_and_b32 v177, 0xffff0000, v184
v_lshlrev_b32 v178, 16, v185
v_and_b32 v179, 0xffff0000, v185
v_lshlrev_b32 v180, 16, v186
v_and_b32 v181, 0xffff0000, v186
v_lshlrev_b32 v182, 16, v187
v_and_b32 v183, 0xffff0000, v187
v_cndmask_b32_e64 v56, v56, v176, s[88:89]
v_cndmask_b32_e64 v57, v57, v177, s[88:89]
v_cndmask_b32_e64 v58, v58, v178, s[88:89]
v_cndmask_b32_e64 v59, v59, v179, s[88:89]
v_cndmask_b32_e64 v60, v60, v180, s[88:89]
v_cndmask_b32_e64 v61, v61, v181, s[88:89]
v_cndmask_b32_e64 v62, v62, v182, s[88:89]
v_cndmask_b32_e64 v63, v63, v183, s[88:89]
v_lshlrev_b32 v176, 16, v116
v_and_b32 v177, 0xffff0000, v116
v_lshlrev_b32 v178, 16, v118
v_and_b32 v179, 0xffff0000, v118
v_lshlrev_b32 v180, 16, v124
v_and_b32 v181, 0xffff0000, v124
v_lshlrev_b32 v182, 16, v126
v_and_b32 v183, 0xffff0000, v126
A 176,176,72
A 177,177,73
A 178,178,74
A 179,179,75
A 180,180,76
A 181,181,77
A 182,182,78
A 183,183,79
K 184,176,177
K 185,178,179
K 186,180,181
K 187,182,183
v_lshlrev_b32 v176, 16, v184
v_and_b32 v177, 0xffff0000, v184
v_lshlrev_b32 v178, 16, v185
v_and_b32 v179, 0xffff0000, v185
v_lshlrev_b32 v180, 16, v186
v_and_b32 v181, 0xffff0000, v186
v_lshlrev_b32 v182, 16, v187
v_and_b32 v183, 0xffff0000, v187
v_cndmask_b32_e64 v72, v72, v176, vcc
v_cndmask_b32_e64 v73, v73, v177, vcc
v_cndmask_b32_e64 v74, v74, v178, vcc
v_cndmask_b32_e64 v75, v75, v179, vcc
v_cndmask_b32_e64 v76, v76, v180, vcc
v_cndmask_b32_e64 v77, v77, v181, vcc
v_cndmask_b32_e64 v78, v78, v182, vcc
v_cndmask_b32_e64 v79, v79, v183, vcc
s_bitset0_b32 s68, s69
s_cmp_eq_u32 s68, 0
s_cbranch_scc1 .Lupr_120%=
s_ff1_i32_b32 s69, s68
.LL_123%=:
s_cmp_eq_u32 s64, 0
s_cbranch_scc0 .LL_124%=
s_waitcnt vmcnt(0)
v_lshlrev_b32 v2, 2, v1
v_mov_b64 v[28:29], s[12:13]
s_mov_b64 exec, 3
buffer_store_dwordx2 v[28:29], v2, s[8:11], s53 offen offset:512 sc1
s_mov_b64 exec, -1
s_mov_b32 s64, 1
.LL_124%=:
s_mul_i32 s66, s69, 20
s_mul_i32 s67, s58, 5
s_add_u32 s66, s66, s67
s_lshl_b32 s66, s66, 7
s_add_u32 s66, s66, 98304
s_lshl_b32 s67, s14, 7
s_add_u32 s67, s67, s66
buffer_load_dwordx4 v[100:103], v3, s[8:11], s67 offen offset:0 sc1
buffer_load_dwordx4 v[108:111], v3, s[8:11], s67 offen offset:16 sc1
buffer_load_dwordx4 v[116:119], v3, s[8:11], s66 offen offset:512 sc1
buffer_load_dwordx4 v[124:127], v3, s[8:11], s66 offen offset:528 sc1
s_waitcnt vmcnt(0)
s_branch .LL_121%=
.Lspin_52%=:
s_mov_b32 s84, 0
.Llp_54%=:
s_sleep 2
v_mov_b32 v28, s74
buffer_load_dwordx2 v[28:29], v28, s[8:11], 0 offen offset:3584 sc1
s_waitcnt vmcnt(0)
s_add_u32 s84, s84, 1
v_xor_b32 v28, s12, v28
v_xor_b32 v29, s13, v29
v_lshrrev_b32 v248, 11, v28
v_or_b32 v29, v29, v248
v_cmp_eq_u32 vcc, 0, v29
s_or_b64 vcc, vcc, s[72:73]
s_cmp_eq_u64 vcc, -1
s_cbranch_scc1 .Lok_53%=
s_cmp_lt_u32 s84, 1048576
s_cbranch_scc1 .Llp_54%=
s_branch .Lok_53%=
.Lspin_57%=:
s_mov_b32 s84, 0
.Llp_59%=:
s_sleep 2
v_bfe_u32 v28, v1, 4, 2
v_lshlrev_b32 v28, 3, v28
s_lshl_b32 s80, s56, 9
s_add_u32 s80, s80, 4096
buffer_load_dwordx2 v[28:29], v28, s[8:11], s80 offen sc1
s_waitcnt vmcnt(0)
s_add_u32 s84, s84, 1
v_cmp_eq_u64 vcc, s[12:13], v[28:29]
s_cmp_eq_u64 vcc, -1
s_cbranch_scc1 .Lok_58%=
s_cmp_lt_u32 s84, 1048576
s_cbranch_scc1 .Llp_59%=
s_branch .Lok_58%=
.Lspin_60%=:
s_mov_b32 s84, 0
.Llp_62%=:
s_sleep 2
v_bfe_u32 v28, v1, 4, 4
v_lshlrev_b32 v28, 3, v28
s_lshl_b32 s80, s56, 9
s_add_u32 s80, s80, 4096
buffer_load_dwordx2 v[28:29], v28, s[8:11], s80 offen sc1
s_waitcnt vmcnt(0)
s_add_u32 s84, s84, 1
v_cmp_eq_u64 vcc, s[12:13], v[28:29]
s_cmp_eq_u64 vcc, -1
s_cbranch_scc1 .Lok_61%=
s_cmp_lt_u32 s84, 1048576
s_cbranch_scc1 .Llp_62%=
s_branch .Lok_61%=
.Lspin_63%=:
s_mov_b32 s84, 0
.Llp_65%=:
s_sleep 2
v_bfe_u32 v28, v1, 4, 4
v_lshlrev_b32 v28, 3, v28
s_lshl_b32 s80, s56, 9
s_add_u32 s80, s80, 4224
buffer_load_dwordx2 v[28:29], v28, s[8:11], s80 offen sc1
s_waitcnt vmcnt(0)
s_add_u32 s84, s84, 1
v_cmp_eq_u64 vcc, s[12:13], v[28:29]
s_cmp_eq_u64 vcc, -1
s_cbranch_scc1 .Lok_64%=
s_cmp_lt_u32 s84, 1048576
s_cbranch_scc1 .Llp_65%=
s_branch .Lok_64%=
.Lspin_67%=:
s_mov_b32 s84, 0
.Llp_69%=:
s_sleep 2
v_bfe_u32 v28, v1, 4, 4
v_lshlrev_b32 v28, 3, v28
s_lshl_b32 s80, s56, 9
s_add_u32 s80, s80, 4352
buffer_load_dwordx2 v[28:29], v28, s[8:11], s80 offen sc1
s_waitcnt vmcnt(0)
s_add_u32 s84, s84, 1
v_cmp_eq_u64 vcc, s[12:13], v[28:29]
s_cmp_eq_u64 vcc, -1
s_cbranch_scc1 .Lok_68%=
s_cmp_lt_u32 s84, 1048576
s_cbranch_scc1 .Llp_69%=
s_branch .Lok_68%=
.Lspin_70%=:
s_mov_b32 s84, 0
.Llp_72%=:
s_sleep 2
v_bfe_u32 v28, v1, 4, 4
v_lshlrev_b32 v28, 3, v28
s_lshl_b32 s80, s56, 9
s_add_u32 s80, s80, 4480
buffer_load_dwordx2 v[28:29], v28, s[8:11], s80 offen sc1
s_waitcnt vmcnt(0)
s_add_u32 s84, s84, 1
v_cmp_eq_u64 vcc, s[12:13], v[28:29]
s_cmp_eq_u64 vcc, -1
s_cbranch_scc1 .Lok_71%=
s_cmp_lt_u32 s84, 1048576
s_cbranch_scc1 .Llp_72%=
s_branch .Lok_71%=
.Lspin_73%=:
s_mov_b32 s84, 0
.Llp_75%=:
s_sleep 2
buffer_load_dwordx2 v[8:9], v4, s[8:11], 0 offen offset:2560 sc1
buffer_load_dwordx2 v[10:11], v5, s[8:11], 0 offen offset:3072 sc1
buffer_load_dwordx2 v[12:13], v6, s[8:11], 0 offen offset:3584 sc1
s_waitcnt vmcnt(0)
s_add_u32 s84, s84, 1
v_cmp_eq_u64 vcc, s[12:13], v[8:9]
v_cmp_eq_u64_e64 s[86:87], s[12:13], v[10:11]
s_and_b64 vcc, vcc, s[86:87]
s_or_b64 vcc, vcc, s[20:21]
s_cmp_eq_u64 vcc, -1
s_cbranch_scc1 .Lok_74%=
s_cmp_lt_u32 s84, 1048576
s_cbranch_scc1 .Llp_75%=
s_branch .Lok_74%=
.Lmk_77%=:
s_nop 7
v_cndmask_b32_e64 v168, v241, v168, s[72:73]
v_cndmask_b32_e64 v169, v241, v169, s[74:75]
v_cndmask_b32_e64 v170, v241, v170, s[76:77]
v_cndmask_b32_e64 v171, v241, v171, s[78:79]
v_cndmask_b32_e64 v188, v241, v188, s[72:73]
v_cndmask_b32_e64 v189, v241, v189, s[74:75]
v_cndmask_b32_e64 v190, v241, v190, s[76:77]
v_cndmask_b32_e64 v191, v241, v191, s[78:79]
v_mov_b32 v184, v241
v_mov_b32 v185, v241
v_mov_b32 v186, v241
v_mov_b32 v187, v241
s_branch .Lmkr_78%=
.Lspin_82%=:
s_mov_b32 s84, 0
.Llp_84%=:
s_sleep 2
v_bfe_u32 v28, v1, 4, 2
v_lshlrev_b32 v28, 3, v28
buffer_load_dwordx2 v[28:29], v28, s[8:11], s55 offen sc1
s_waitcnt vmcnt(0)
s_add_u32 s84, s84, 1
v_cmp_eq_u64 vcc, s[12:13], v[28:29]
s_cmp_eq_u64 vcc, -1
s_cbranch_scc1 .Lok_83%=
s_cmp_lt_u32 s84, 1048576
s_cbranch_scc1 .Llp_84%=
s_branch .Lok_83%=
.LL_87%=:
s_mov_b32 s84, 0
.LL_89%=:
s_sleep 2
buffer_load_dwordx2 v[12:13], v27, s[8:11], 0 offen offset:3584 sc1
s_waitcnt vmcnt(0)
s_add_u32 s84, s84, 1
v_xor_b32 v12, s12, v12
v_xor_b32 v13, s13, v13
v_lshrrev_b32 v3, 11, v12
v_or_b32 v13, v13, v3
v_cmp_eq_u32 vcc, 0, v13
s_cmp_eq_u64 vcc, -1
s_cbranch_scc1 .LL_88%=
s_cmp_lt_u32 s84, 1048576
s_cbranch_scc1 .LL_89%=
s_branch .LL_88%=
.Lup_85%=:
v_lshrrev_b32 v3, 8, v1
v_lshlrev_b32 v3, 5, v3
s_ff1_i32_b32 s69, s68
s_mul_i32 s66, s69, 20
s_mul_i32 s67, s58, 5
s_add_u32 s66, s66, s67
s_lshl_b32 s66, s66, 7
s_add_u32 s66, s66, 98304
s_lshl_b32 s67, s14, 7
s_add_u32 s67, s67, s66
buffer_load_dwordx4 v[100:103], v3, s[8:11], s67 offen offset:0 sc1
buffer_load_dwordx4 v[108:111], v3, s[8:11], s67 offen offset:16 sc1
buffer_load_dwordx4 v[116:119], v3, s[8:11], s66 offen offset:512 sc1
buffer_load_dwordx4 v[124:127], v3, s[8:11], s66 offen offset:528 sc1
s_branch .Lupr_86%=
.Lspin_98%=:
s_mov_b32 s84, 0
.Llp_100%=:
s_sleep 2
v_bfe_u32 v28, v1, 4, 2
v_lshlrev_b32 v28, 3, v28
buffer_load_dwordx2 v[28:29], v28, s[8:11], s54 offen sc1
s_waitcnt vmcnt(0)
s_add_u32 s84, s84, 1
v_cmp_eq_u64 vcc, s[12:13], v[28:29]
s_cmp_eq_u64 vcc, -1
s_cbranch_scc1 .Lok_99%=
s_cmp_lt_u32 s84, 1048576
s_cbranch_scc1 .Llp_100%=
s_branch .Lok_99%=
.Lspin_101%=:
s_mov_b32 s84, 0
.Llp_103%=:
s_sleep 2
v_bfe_u32 v28, v1, 4, 2
v_lshlrev_b32 v28, 3, v28
buffer_load_dwordx2 v[28:29], v28, s[8:11], s55 offen sc1
s_waitcnt vmcnt(0)
s_add_u32 s84, s84, 1
v_cmp_eq_u64 vcc, s[12:13], v[28:29]
s_cmp_eq_u64 vcc, -1
s_cbranch_scc1 .Lok_102%=
s_cmp_lt_u32 s84, 1048576
s_cbranch_scc1 .Llp_103%=
s_branch .Lok_102%=
.LL_106%=:
s_mov_b32 s84, 0
.LL_108%=:
s_sleep 2
buffer_load_dwordx2 v[12:13], v27, s[8:11], 0 offen offset:3584 sc1
s_waitcnt vmcnt(0)
s_add_u32 s84, s84, 1
v_xor_b32 v12, s12, v12
v_xor_b32 v13, s13, v13
v_lshrrev_b32 v3, 11, v12
v_or_b32 v13, v13, v3
v_cmp_eq_u32 vcc, 0, v13
s_cmp_eq_u64 vcc, -1
)"
R"(s_cbranch_scc1 .LL_107%=
s_cmp_lt_u32 s84, 1048576
s_cbranch_scc1 .LL_108%=
s_branch .LL_107%=
.Lup_104%=:
v_lshrrev_b32 v3, 8, v1
v_lshlrev_b32 v3, 5, v3
s_ff1_i32_b32 s69, s68
s_mul_i32 s66, s69, 20
s_mul_i32 s67, s58, 5
s_add_u32 s66, s66, s67
s_lshl_b32 s66, s66, 7
s_add_u32 s66, s66, 98304
s_lshl_b32 s67, s14, 7
s_add_u32 s67, s67, s66
buffer_load_dwordx4 v[100:103], v3, s[8:11], s67 offen offset:0 sc1
buffer_load_dwordx4 v[108:111], v3, s[8:11], s67 offen offset:16 sc1
buffer_load_dwordx4 v[116:119], v3, s[8:11], s66 offen offset:512 sc1
buffer_load_dwordx4 v[124:127], v3, s[8:11], s66 offen offset:528 sc1
s_branch .Lupr_105%=
.Lend_250%=:
.Lend%=:
.purgem W
.purgem D
.purgem Ta
.purgem Ra
.purgem Zvaa
.purgem Mvaa
.purgem Zaaa
.purgem Maaa
.purgem A
.purgem U
.purgem K
.purgem Rv
.purgem Mvav
.purgem G
.purgem Zvav
.purgem Cvav
.purgem X
.purgem Cvaa
.purgem Dc
.purgem Maav
.purgem Mvvv
.purgem Zvva
.purgem Mvva
)"
    : : [ka] "s"(__builtin_amdgcn_kernarg_segment_ptr()), [bid] "s"(blockIdx.x), [tid] "v"(threadIdx.x)
    : "v1", "v2", "v3", "v4", "v5", "v6", "v7", "v8", "v9", "v10", "v11", "v12", "v13", "v14", "v15", "v16", "v17", "v18", "v19", "v20", "v21", "v22", "v23", "v24", "v25", "v26", "v27", "v28", "v29", "v30", "v31", "v32", "v33", "v34", "v35", "v36", "v37", "v38", "v39", "v40", "v41", "v42", "v43", "v44", "v45", "v46", "v47", "v48", "v49", "v50", "v51", "v52", "v53", "v54", "v55", "v56", "v57", "v58", "v59", "v60", "v61", "v62", "v63", "v64", "v65", "v66", "v67", "v68", "v69", "v70", "v71", "v72", "v73", "v74", "v75", "v76", "v77", "v78", "v79", "v80", "v81", "v82", "v83", "v84", "v85", "v86", "v87", "v88", "v89", "v90", "v91", "v92", "v93", "v94", "v95", "v96", "v97", "v98", "v99", "v100", "v101", "v102", "v103", "v104", "v105", "v106", "v107", "v108", "v109", "v110", "v111", "v112", "v113", "v114", "v115", "v116", "v117", "v118", "v119", "v120", "v121", "v122", "v123", "v124", "v125", "v126", "v127", "v128", "v129", "v130", "v131", "v132", "v133", "v134", "v135", "v136", "v137", "v138", "v139", "v140", "v141", "v142", "v143", "v144", "v145", "v146", "v147", "v148", "v149", "v150", "v151", "v152", "v153", "v154", "v155", "v156", "v157", "v158", "v159", "v160", "v161", "v162", "v163", "v164", "v165", "v166", "v167", "v168", "v169", "v170", "v171", "v172", "v173", "v174", "v175", "v176", "v177", "v178", "v179", "v180", "v181", "v182", "v183", "v184", "v185", "v186", "v187", "v188", "v189", "v190", "v191", "v192", "v193", "v194", "v195", "v196", "v197", "v198", "v199", "v200", "v201", "v202", "v203", "v204", "v205", "v206", "v207", "v208", "v209", "v210", "v211", "v212", "v213", "v214", "v215", "v216", "v217", "v218", "v219", "v220", "v221", "v222", "v223", "v224", "v225", "v226", "v227", "v228", "v229", "v230", "v231", "v232", "v233", "v234", "v235", "v236", "v237", "v238", "v239", "v240", "v241", "v242", "v243", "v244", "v245", "v246", "v247", "v248", "v249", "v250", "v251", "v252", "v253", "v254", "v255", "a0", "a1", "a2", "a3", "a4", "a5", "a6", "a7", "a8", "a9", "a10", "a11", "a12", "a13", "a14", "a15", "a16", "a17", "a18", "a19", "a20", "a21", "a22", "a23", "a24", "a25", "a26", "a27", "a28", "a29", "a30", "a31", "a32", "a33", "a34", "a35", "a36", "a37", "a38", "a39", "a40", "a41", "a42", "a43", "a44", "a45", "a46", "a47", "a48", "a49", "a50", "a51", "a52", "a53", "a54", "a55", "a56", "a57", "a58", "a59", "a60", "a61", "a62", "a63", "a64", "a65", "a66", "a67", "a68", "a69", "a70", "a71", "a72", "a73", "a74", "a75", "a76", "a77", "a78", "a79", "a80", "a81", "a82", "a83", "a84", "a85", "a86", "a87", "a88", "a89", "a90", "a91", "a92", "a93", "a94", "a95", "a96", "a97", "a98", "a99", "a100", "a101", "a102", "a103", "a104", "a105", "a106", "a107", "a108", "a109", "a110", "a111", "a112", "a113", "a114", "a115", "a116", "a117", "a118", "a119", "a120", "a121", "a122", "a123", "a124", "a125", "a126", "a127", "a128", "a129", "a130", "a131", "a132", "a133", "a134", "a135", "a136", "a137", "a138", "a139", "a140", "a141", "a142", "a143", "a144", "a145", "a146", "a147", "a148", "a149", "a150", "a151", "a152", "a153", "a154", "a155", "a156", "a157", "a158", "a159", "a160", "a161", "a162", "a163", "a164", "a165", "a166", "a167", "a168", "a169", "a170", "a171", "a172", "a173", "a174", "a175", "a176", "a177", "a178", "a179", "a180", "a181", "a182", "a183", "a184", "a185", "a186", "a187", "a188", "a189", "a190", "a191", "a192", "a193", "a194", "a195", "a196", "a197", "a198", "a199", "a200", "a201", "a202", "a203", "a204", "a205", "a206", "a207", "a208", "a209", "a210", "a211", "a212", "a213", "a214", "a215", "a216", "a217", "a218", "a219", "a220", "a221", "a222", "a223", "a224", "a225", "a226", "a227", "a228", "a229", "a230", "a231", "a232", "a233", "a234", "a235", "a236", "a237", "a238", "a239", "a240", "a241", "a242", "a243", "a244", "a245", "a246", "a247", "a248", "a249", "a250", "a251", "a252", "a253", "a254", "a255", "s4", "s5", "s6", "s7", "s8", "s9", "s10", "s11", "s12", "s13", "s14", "s15", "s16", "s17", "s18", "s19", "s20", "s21", "s22", "s23", "s24", "s25", "s26", "s27", "s28", "s29", "s30", "s31", "s32", "s33", "s34", "s35", "s36", "s37", "s38", "s39", "s40", "s41", "s42", "s43", "s44", "s45", "s46", "s47", "s48", "s49", "s50", "s51", "s52", "s53", "s54", "s55", "s56", "s57", "s58", "s59", "s60", "s61", "s62", "s63", "s64", "s65", "s66", "s67", "s68", "s69", "s70", "s71", "s72", "s73", "s74", "s75", "s76", "s77", "s78", "s79", "s80", "s81", "s82", "s83", "s84", "s85", "s86", "s87", "s88", "s89", "s90", "s91", "s92", "s93", "s94", "s95", "s96", "s97", "s98", "s99", "vcc", "scc", "memory"
  );
}
extern "C" void kernel_launch(void* const* d_in, const int* in_sizes, int n_in, void* d_out, int out_size, void* d_ws, size_t ws_size, hipStream_t stream) {
  (void)hipFuncSetAttribute((const void*)mega_kernel, hipFuncAttributeMaxDynamicSharedMemorySize, LDS_BYTES);
  mega_kernel<<<256, 256, LDS_BYTES, stream>>>((const bf16*)d_in[0], (const bf16*)d_in[1], (const bf16*)d_in[2], (const bf16*)d_in[3], (const bf16*)d_in[4], (const bf16*)d_in[5],
      (const bf16*)d_in[7], (const bf16*)d_in[8], (const bf16*)d_in[9], (const bf16*)d_in[10], (char*)d_ws + 0, (bf16*)d_out);
}
```
